```python
import torch

def _softmax(x, axis=-1):
    return torch.softmax(x, dim=axis)
def _gelu(x):
    return torch.nn.functional.gelu(x, approximate="tanh")
def _silu(x):
    return torch.nn.functional.silu(x)
def _sigmoid(x):
    return torch.sigmoid(x)
def _layernorm(x, axis=-1, eps=1e-5):
    m = x.mean(dim=axis, keepdim=True)
    v = x.var(dim=axis, keepdim=True, unbiased=False)
    return (x - m) / torch.sqrt(v + eps)
def _scatter_add(tgt, idx, src, axis=0):
    return tgt.clone().index_add_(axis, idx, src)
def _segment_sum(src, seg, n):
    out = torch.zeros((n,) + src.shape[1:], dtype=src.dtype)
    return out.index_add_(0, seg, src)
def _pad(x, pw):
    flat = tuple(v for pair in reversed(pw) for v in pair)
    return torch.nn.functional.pad(x, flat)

kernel_name = 'gated_mix__compressed_kv_cla__ssm_conv_h3__prompt'

def setup_inputs():

    _w = (lambda a: (lambda t: t.to(torch.bfloat16) if t.is_floating_point() else t)(torch.as_tensor(a)))
    def _A_setup():
        B, H, Sq, Sk, D, M = 1, 32, 2048, 2048, 128, 1024
        Dc = 4 * D
        return {
            "x": _w((lambda *s: torch.randn(*s, dtype=torch.bfloat16) * 0.05)(B, Sq, M)),
            "c_kv": _w((lambda *s: torch.randn(*s, dtype=torch.bfloat16) * 0.8)(B, Sk, Dc)),
            "w_q": _w((lambda *s: torch.randn(*s, dtype=torch.bfloat16) * 6.4)(M, H, D) * (M ** -0.5)),
            "w_uk": _w((lambda *s: torch.randn(*s, dtype=torch.bfloat16) * 6.4)(Dc, H, D) * (Dc ** -0.5)),
            "w_uv": _w((lambda *s: torch.randn(*s, dtype=torch.bfloat16) * 0.8)(Dc, H, D) * (Dc ** -0.5)),
            "w_o": _w((lambda *s: torch.randn(*s, dtype=torch.bfloat16) * 0.8)(H, D, M) * ((H * D) ** -0.5)),
        }
    _A_in = _A_setup()

    def _B_setup():
        B, S, D = 1, 2048, 1024
        ks = 4
        return {
            "q": _w((lambda *s: torch.randn(*s, dtype=torch.bfloat16) * 0.05)(B, S, D)),
            "k": _w((lambda *s: torch.randn(*s, dtype=torch.bfloat16) * 0.8)(B, S, D)),
            "v": _w((lambda *s: torch.randn(*s, dtype=torch.bfloat16) * 0.8)(B, S, D)),
            "shift_k": _w((lambda *s: torch.randn(*s, dtype=torch.bfloat16) * 0.8)(D, ks) * 0.5),
            "ssm_k": _w((lambda *s: torch.randn(*s, dtype=torch.bfloat16) * 1.6)(D, S) * (1.0 / S ** 0.5)),
            "ssm_D": _w((lambda *s: torch.randn(*s, dtype=torch.bfloat16) * 0.8)(D)),
        }
    _B_in = _B_setup()

    return {
        "A_x": _A_in["x"],
        "A_c_kv": _A_in["c_kv"],
        "A_w_q": _A_in["w_q"],
        "A_w_uk": _A_in["w_uk"],
        "A_w_uv": _A_in["w_uv"],
        "A_w_o": _A_in["w_o"],
        "B_q": _B_in["q"],
        "B_k": _B_in["k"],
        "B_v": _B_in["v"],
        "B_shift_k": _B_in["shift_k"],
        "B_ssm_k": _B_in["ssm_k"],
        "B_ssm_D": _B_in["ssm_D"],
        "gate_w": _w((lambda *s: torch.randn(*s, dtype=torch.bfloat16) * 0.05)(1024) * 16.0),
    }

def reference(A_x, A_c_kv, A_w_q, A_w_uk, A_w_uv, A_w_o, B_q, B_k, B_v, B_shift_k, B_ssm_k, B_ssm_D, gate_w):
    def _A_ref(x, c_kv, w_q, w_uk, w_uv, w_o):
        B, Sq, M = x.shape[0], x.shape[1], x.shape[2]
        Sk = c_kv.shape[1]
        H, D = w_q.shape[1], w_q.shape[2]
        q = torch.einsum("bqm,mhd->bhqd", x, w_q)
        k = torch.einsum("bkc,chd->bhkd", c_kv, w_uk)
        v = torch.einsum("bkc,chd->bhkd", c_kv, w_uv)
        scores = torch.einsum("bhqd,bhkd->bhqk", q, k) * (D ** -0.5)
        off = Sk - Sq
        qi = torch.arange(Sq)[:, None]
        kj = torch.arange(Sk)[None, :]
        cmask = kj <= (qi + off)
        scores = torch.where(cmask[None, None], scores, -1e9)
        p = _softmax(scores, axis=-1)
        ctx = torch.einsum("bhqk,bhkd->bhqd", p, v)
        return torch.einsum("bhqd,hdm->bqm", ctx, w_o)

    def _B_ref(q, k, v, shift_k, ssm_k, ssm_D):
        B, S, D = q.shape
        ks = shift_k.shape[-1]
        k_pad = _pad(k, ((0, 0), (ks - 1, 0), (0, 0)))
        k_shift = torch.zeros_like(k)
        for t in range(ks):
            k_shift = k_shift + k_pad[:, t:t + S, :] * shift_k[:, ks - 1 - t][None, None, :]
        kv = k_shift * v
        L = 2 * S
        kv_pad = _pad(kv, ((0, 0), (0, S), (0, 0)))
        kf_pad = _pad(ssm_k, ((0, 0), (0, S)))
        kv_f = (lambda x, axes=(-2,-1): torch.fft.rfft2(x.float(), dim=axes))(kv_pad[:, None, :, :], axes=(1, 2))
        kf_f = (lambda x, axes=(-2,-1): torch.fft.rfft2(x.float(), dim=axes))(kf_pad[:, None, :], axes=(1, 2))
        kf_f = (lambda x, *d: x.permute(*d))(kf_f, 1, 2, 0)
        y_f = kv_f * kf_f[None, :, :, :]
        y = (lambda x, s, axes=(-2,-1): torch.fft.irfft2(x, s=s, dim=axes).to(torch.bfloat16))(y_f, (1, L), axes=(1, 2))[:, 0, :S, :]
        y = y + kv * ssm_D[None, None, :]
        return q * y

    x = A_x
    g = _sigmoid((x * gate_w).sum(-1, keepdims=True))
    a = _A_ref(x, A_c_kv, A_w_q, A_w_uk, A_w_uv, A_w_o)
    b = _B_ref(x, B_k, B_v, B_shift_k, B_ssm_k, B_ssm_D)
    return g * a + (1 - g) * b
```

```cpp
#include <hip/hip_runtime.h>
#include <stdint.h>
#include <type_traits>

typedef short bf16x8 __attribute__((ext_vector_type(8)));
typedef short s16x4 __attribute__((ext_vector_type(4)));
typedef float f32x4 __attribute__((ext_vector_type(4)));
typedef float f32x2 __attribute__((ext_vector_type(2)));
typedef unsigned u32x4 __attribute__((ext_vector_type(4)));
typedef unsigned u32x2 __attribute__((ext_vector_type(2)));
typedef __bf16 bf16x2_t __attribute__((ext_vector_type(2)));
typedef unsigned short u16;
typedef unsigned long long u64;
#define LAS __attribute__((address_space(3)))
typedef LAS char* ldsp;
typedef const LAS char* ldscp;
typedef __attribute__((address_space(1))) unsigned long long gu64;
#define DI __device__ __forceinline__

constexpr int SEQ = 2048, DM = 1024, DC = 512, NH = 32, HD = 128, HDN = NH * HD;
constexpr size_t MB = 1u << 20;
constexpr size_t OFF_KF = 0, OFF_VF = 16 * MB, OFF_CTX = 32 * MB, OFF_UR = 48 * MB, OFF_Z = 52 * MB, OFF_G = 56 * MB, OFF_SYNC = 57 * MB, OFF_XB = 64 * MB;
constexpr size_t WS_USED = 72 * MB;
constexpr int SY_PREP = 512, SY_KV = 1024, SY_CTX = 1536, SY_PAIR = 2048;

#define MFMA(a, b, c) __builtin_amdgcn_mfma_f32_16x16x32_bf16((a), (b), (c), 0, 0, 0)
#define SBAR() __builtin_amdgcn_sched_barrier(0)
#define PIN(x) asm volatile("" : "+v"(x))
#define WAIT_V(n) asm volatile("s_waitcnt vmcnt(" #n ")" ::: "memory")
#define WAIT_L(n) asm volatile("s_waitcnt lgkmcnt(" #n ")" ::: "memory")
#define BAR() asm volatile("s_barrier" ::: "memory")
#define WAIT_BAR(n) asm volatile("s_waitcnt vmcnt(" #n ") lgkmcnt(0)\n\ts_barrier" ::: "memory")
#define LBAR() asm volatile("s_waitcnt lgkmcnt(0)\n\ts_barrier" ::: "memory")
#define RLX_AGENT __ATOMIC_RELAXED, __HIP_MEMORY_SCOPE_AGENT

DI unsigned cvtpk(float lo, float hi) { f32x2 v = {lo, hi}; bf16x2_t b = __builtin_convertvector(v, bf16x2_t); return __builtin_bit_cast(unsigned, b); }
DI float bf_lo(unsigned w) { return __uint_as_float(w << 16); }
DI float bf_hi(unsigned w) { return __uint_as_float(w & 0xffff0000u); }
DI float rbf(float x) { return bf_lo(cvtpk(x, x)); }
template <int CTRL, int RM> DI float dpp_add(float v) { return v + __builtin_bit_cast(float, __builtin_amdgcn_update_dpp(0, __builtin_bit_cast(int, v), CTRL, RM, 0xf, true)); }
DI float wave_sum63(float v) {
    v = dpp_add<0x111, 0xf>(v); v = dpp_add<0x112, 0xf>(v); v = dpp_add<0x114, 0xf>(v); v = dpp_add<0x118, 0xf>(v);
    v = dpp_add<0x142, 0xa>(v); v = dpp_add<0x143, 0xc>(v); return v;
}
DI bf16x8 pack8(const f32x4& a, const f32x4& b) { u32x4 w = {cvtpk(a[0], a[1]), cvtpk(a[2], a[3]), cvtpk(b[0], b[1]), cvtpk(b[2], b[3])}; return __builtin_bit_cast(bf16x8, w); }
DI float fadd_s(float a, float b) { float r; asm("v_add_f32_e32 %0, %1, %2" : "=v"(r) : "v"(a), "v"(b)); return r; }
DI void dma16(unsigned lds_dst, const void* sbase, unsigned voff) {
    asm volatile("s_mov_b32 m0, %0\n\ts_nop 0\n\tglobal_load_lds_dwordx4 %1, %2" :: "s"(lds_dst), "v"(voff), "s"(sbase) : "memory");
}
DI void dma16c(unsigned lds_dst, const void* sbase, unsigned voff) {
    asm volatile("s_mov_b32 m0, %0\n\ts_nop 0\n\tglobal_load_lds_dwordx4 %1, %2 sc1" :: "s"(lds_dst), "v"(voff), "s"(sbase) : "memory");
}
template <int Q> DI void dma4k(unsigned lds_dst, const void* sbase, unsigned voff) {
    if constexpr (Q == 0) asm volatile("s_mov_b32 m0, %0\n\ts_nop 0\n\tglobal_load_lds_dwordx4 %1, %2 sc1" :: "s"(lds_dst), "v"(voff), "s"(sbase) : "memory");
    else asm volatile("global_load_lds_dwordx4 %0, %1 offset:%2 sc1" :: "v"(voff), "s"(sbase), "i"(Q * 1024) : "memory");
}
DI u32x4 gload16(const void* sbase, unsigned voff) { u32x4 r; asm volatile("global_load_dwordx4 %0, %1, %2" : "=v"(r) : "v"(voff), "s"(sbase) : "memory"); return r; }
typedef short v4i16_t __attribute__((ext_vector_type(4)));
DI s16x4 ldtr(ldscp p) { return __builtin_bit_cast(s16x4, __builtin_amdgcn_ds_read_tr16_b64_v4i16((LAS v4i16_t*)p)); }
DI bf16x8 ldtr2(ldscp p0, ldscp p1) { s16x4 a = ldtr(p0), b = ldtr(p1); return __builtin_shufflevector(a, b, 0, 1, 2, 3, 4, 5, 6, 7); }
DI int fresh_lane() { int l; asm volatile("v_mbcnt_lo_u32_b32 %0, -1, 0\n\tv_mbcnt_hi_u32_b32 %0, -1, %0" : "=v"(l)); return l; }
DI bf16x8 ld128(ldscp p) { return *(const LAS bf16x8*)p; }
DI void wait_flags(gu64* flags, int n, u64 tag, int lane) {
    const u64 t0 = __builtin_amdgcn_s_memrealtime();
    for (;;) {
        const u64 v = (lane < n) ? __hip_atomic_load(flags + lane, RLX_AGENT) : tag;
        if (__all(v == tag)) break;
        if (__builtin_amdgcn_s_memrealtime() - t0 > 10000000ull) break;
        __builtin_amdgcn_s_sleep(2);
    }
    asm volatile("" ::: "memory");
}
DI void wait_flags2(gu64* flags, u64 tag, int lane) {
    const u64 t0 = __builtin_amdgcn_s_memrealtime();
    for (;;) {
        if (__all(__hip_atomic_load(flags + 2 * (lane & 7), RLX_AGENT) == tag)) break;
        if (__builtin_amdgcn_s_memrealtime() - t0 > 10000000ull) break;
        __builtin_amdgcn_s_sleep(2);
    }
    asm volatile("" ::: "memory");
}
DI void flags_fetch(unsigned lds_dst, const void* flags, int lane) {
    asm volatile("s_mov_b32 m0, %0\n\ts_nop 0\n\tglobal_load_lds_dword %1, %2 sc1" :: "s"(lds_dst), "v"((unsigned)(lane * 4)), "s"(flags) : "memory");
}
DI void flags_check(ldscp fetched, int first, gu64* flags, int n, u64 tag, int lane) {
    const u64 v = (lane < n) ? *(const LAS u64*)(fetched + (first + lane) * 8) : tag;
    if (!__all(v == tag)) wait_flags(flags + first, n, tag, lane);
}
DI u32x2 flags8_issue(gu64* flags, int lane) {
    const u64 a = (u64)(uintptr_t)flags;
    const u64 b = ((u64)(unsigned)__builtin_amdgcn_readfirstlane((unsigned)(a >> 32)) << 32) | (unsigned)__builtin_amdgcn_readfirstlane((unsigned)a);
    u32x2 r; asm volatile("s_nop 4\n\tglobal_load_dwordx2 %0, %1, %2 sc1" : "=v"(r) : "v"((unsigned)((lane & 7) * 16)), "s"(b) : "memory"); return r;
}
DI void store16_wt(__amdgpu_buffer_rsrc_t rs, unsigned off, bf16x8 v) { __builtin_amdgcn_raw_buffer_store_b128(__builtin_bit_cast(u32x4, v), rs, off, 0, 16); }

#define STAMP_DECL do { } while (0)
#define STAMP(i) do { } while (0)
#define STAMP_END(P, k) do { } while (0)
#define STAMP_ARG
#define STAMP_PASS

struct Params {
    const u16 *x, *ckv, *wq, *wuk, *wuv, *wo, *bk, *bv, *shk, *ssk, *ssd, *gw;
    u16* out; char* ws;
};

DI Params fresh_params() {
    typedef const __attribute__((address_space(4))) Params* kp_t;
    kp_t k = (kp_t)__builtin_amdgcn_kernarg_segment_ptr(); asm volatile("" : "+s"(k));
    Params q; q.x = k->x; q.ckv = k->ckv; q.wq = k->wq; q.wuk = k->wuk; q.wuv = k->wuv; q.wo = k->wo; q.bk = k->bk; q.bv = k->bv; q.shk = k->shk; q.ssk = k->ssk; q.ssd = k->ssd; q.gw = k->gw;
    q.out = k->out; q.ws = k->ws; return q;
}
constexpr int HT = 16384;
constexpr int L_PK = 131072, L_PO = L_PK + 18432, L_FLG = 163840 - 256, L_KVF = 147456;
constexpr int CV_E = 0, CV_O = 4160, CV_EO = 8320, CV_US = 4608;
static_assert(CV_O % 128 == 64 && CV_EO % 128 == 0, "the two h copies sit 16 banks apart");
DI int cv_eo(int ch) { return ch < 3 ? 65536 + ch * CV_EO : 0; }
DI int cv_u(int ch) { return ch < 3 ? 147456 + ch * CV_US : 90496; }
constexpr int L_CVX = 16384;
constexpr int L_K = 0, L_V = 65536, L_QB = 131072, L_QA = 98304;
constexpr int QSTG = 3 * HT;
constexpr float QSCALE = 0.08838834764831845f * 1.4426950408889634f;
constexpr bool AT_SKIP = true, AT_LEAD = false;
constexpr float L_LIMIT = 1.0e9f;

DI unsigned voff_act(int wid, int lane, int ld) { return (unsigned)(((8 * wid + (lane >> 3)) * ld + (((lane & 7) ^ ((4 * (wid & 1) + (lane >> 4)) & 7)) * 8)) * 2); }
DI unsigned voff_wgt(int wid, int lane, int ld) {
    const int kb = wid >> 1, slot = lane >> 3, row = slot ^ ((kb & 1) << 2), chunk = (lane & 7) ^ (2 * ((slot >> 1) & 3));
    return (unsigned)(((8 * kb + row) * ld + 64 * (wid & 1) + 8 * chunk) * 2);
}
DI int wgt_rd(int row, int kpar, int a, int pp) { const int slot = row ^ (kpar << 2); return slot * 128 + (2 * (a ^ ((slot >> 1) & 3)) + (pp >> 1)) * 16 + 8 * (pp & 1); }

DI void prep_issue(const Params& P, ldsp lds, int wid, int r, int idx, int hf) {
    const int lane = fresh_lane();
    const int tb = idx * 64 + 32 * hf, d0 = r * 128;
#pragma unroll
    for (int i = 0; i < 3; ++i) {
        const int j = wid + 8 * i;
        int t = (j < 9 ? tb - 3 + 4 * j : tb + 4 * (j - 9)) + (lane >> 4); t = t < 0 ? 0 : (t > SEQ - 1 ? SEQ - 1 : t);
        const unsigned vr = (unsigned)((t * DM + d0 + (lane & 15) * 8) * 2), vs = (unsigned)(d0 * 8 + lane * 16);
        const char* src = j < 9 ? (const char*)P.bk : (j < 17 ? (const char*)P.bv : (const char*)P.shk);
        dma16((unsigned)(uintptr_t)lds + L_PK + (j < 17 ? j : 17) * 1024, src, j < 17 ? vr : vs);
    }
}
DI void prep_compute(ldsp lds, int wid, int idx, int hf) {
    const int tid = wid * 64 + fresh_lane();
    const int dc = tid & 15, tr = tid >> 4, t = idx * 64 + 32 * hf + tr;
    u32x4 kr[4], sw[4];
#pragma unroll
    for (int i = 0; i < 4; ++i) kr[i] = *(const LAS u32x4*)(lds + L_PK + (tr + i) * 256 + dc * 16);
    const u32x4 vr = *(const LAS u32x4*)(lds + L_PK + 9216 + tr * 256 + dc * 16);
#pragma unroll
    for (int i = 0; i < 4; ++i) sw[i] = *(const LAS u32x4*)(lds + L_PK + 17408 + dc * 64 + i * 16);
#pragma unroll
    for (int i = 0; i < 3; ++i) if (t - 3 + i < 0) kr[i] = (u32x4){0, 0, 0, 0};
    u32x4 res;
#pragma unroll
    for (int cp = 0; cp < 4; ++cp) {
        const unsigned wl[2] = {sw[cp][0], sw[cp][1]}, wh[2] = {sw[cp][2], sw[cp][3]};
        unsigned acc = 0;
#pragma unroll
        for (int tt = 0; tt < 4; ++tt) {
            const int wi = 3 - tt;
            const float w0 = (wi & 1) ? bf_hi(wl[wi >> 1]) : bf_lo(wl[wi >> 1]), w1 = (wi & 1) ? bf_hi(wh[wi >> 1]) : bf_lo(wh[wi >> 1]);
            const unsigned pr = cvtpk(bf_lo(kr[tt][cp]) * w0, bf_hi(kr[tt][cp]) * w1);
            acc = tt == 0 ? pr : cvtpk(bf_lo(acc) + bf_lo(pr), bf_hi(acc) + bf_hi(pr));
        }
        res[cp] = cvtpk(bf_lo(acc) * bf_lo(vr[cp]), bf_hi(acc) * bf_hi(vr[cp]));
    }
    *(LAS u32x4*)(lds + L_PO + (31 - tr) * 272 + dc * 16) = res;
    LBAR();
}
DI void prep_store(const Params& P, ldsp lds, int wid, int r, int idx, int hf) {
    const int lane = fresh_lane();
    const int i16 = lane & 15, G = lane >> 4, q = i16 >> 2, p = i16 & 3;
    const __amdgpu_buffer_rsrc_t rs = __builtin_amdgcn_make_buffer_rsrc(P.ws + OFF_UR, (short)0, (int)(4 * MB), 0x00020000);
    ldscp a = lds + L_PO + (8 * G + q) * 272 + (16 * wid + 4 * p) * 2;
    store16_wt(rs, (unsigned)(((r * 128 + 16 * wid + i16) * SEQ + (SEQ - 32 - idx * 64 - 32 * hf) + 8 * G) * 2), ldtr2(a, a + 4 * 272));
}

struct KvOut { bf16x8 f[16]; __amdgpu_buffer_rsrc_t rs; unsigned ko, vo; int p, wr, wc; };
template <int I> DI void kv_store(const KvOut& o) {
    constexpr int ai = I >> 3, j = I & 7;
    if constexpr (j < 4) { const int kt = 16 * o.p + 8 * ai + 4 * o.wr + j; store16_wt(o.rs, o.ko + (unsigned)(((kt >> 1) * 8 + (kt & 1) * 4 + o.wc) * 1024), o.f[I]); }
    else { constexpr int mm = (j - 4) >> 1, n = (j - 4) & 1; const int u = 8 * o.p + 4 * ai + 2 * o.wr + mm; store16_wt(o.rs, o.vo + (unsigned)((u * 8 + 2 * o.wc + n) * 1024), o.f[I]); }
}
struct GateRegs { u32x4 xv[2], gv[2]; };
DI void phase_conv(const Params& P, ldsp lds0, int wid, int dch0, float Dv, const KvOut& kvo, GateRegs& GR, int tg STAMP_ARG) {
    const int lane = fresh_lane(), tid = wid * 64 + lane;
    const int c = lane & 15, G = lane >> 4;
    kv_store<0>(kvo); kv_store<1>(kvo);
    {
        const int ch = tid >> 7, q = (tid & 127) * 2 + 2;
        ldsp cb = lds0 + cv_eo(ch), ub = lds0 + cv_u(ch);
#pragma unroll
        for (int i = 0; i < 2; ++i) {
            const u32x4 w = *(const LAS u32x4*)(cb + CV_E + (q + i) * 16);
            const unsigned nx = *(const LAS unsigned*)(cb + CV_E + (q + i + 1) * 16);
            u32x4 o; o[0] = (w[0] >> 16) | (w[1] << 16); o[1] = (w[1] >> 16) | (w[2] << 16); o[2] = (w[2] >> 16) | (w[3] << 16); o[3] = (w[3] >> 16) | (nx << 16);
            *(LAS u32x4*)(cb + CV_O + (q + i) * 16) = o;
            if (q + i == 2) { *(LAS u32x4*)(cb + CV_O) = (u32x4){0, 0, 0, 0}; *(LAS u32x4*)(cb + CV_O + 16) = (u32x4){0, 0, 0, w[0] << 16}; }
        }
        const int z = tid & 127;
        if (z < 2) *(LAS u32x4*)(cb + CV_E + z * 16) = (u32x4){0, 0, 0, 0};
        if (z >= 96) *(LAS u32x4*)(ub + 4096 + (z - 96) * 16) = (u32x4){0, 0, 0, 0};
    }
    WAIT_BAR(8);
    STAMP(9);
    const int ch = wid >> 1, half = wid & 1;
    ldscp cb = lds0 + cv_eo(ch), ub = lds0 + cv_u(ch);
    ldscp aP = cb + ((c & 1) ? (CV_E + (c + 1) * 2) : (CV_O + c * 2)) + 16 * G;
    ldscp bP = ub + (2032 - 16 * c + 8 * G - 256 * 7) * 2;
    f32x4 acc[8];
#pragma unroll
    for (int i = 0; i < 8; ++i) acc[i] = (f32x4){0.f, 0.f, 0.f, 0.f};
#pragma unroll
    for (int bb = 0; bb < 4; ++bb) {
        const int b = 2 * bb + half;
        bf16x8 A[8], B[8];
        if (bb == 0) { kv_store<2>(kvo); kv_store<3>(kvo); kv_store<4>(kvo); } else if (bb == 1) { kv_store<5>(kvo); kv_store<6>(kvo); kv_store<7>(kvo); }
        else if (bb == 2) { kv_store<8>(kvo); kv_store<9>(kvo); kv_store<10>(kvo); } else { kv_store<11>(kvo); kv_store<12>(kvo); kv_store<13>(kvo); }
#pragma unroll
        for (int a = 0; a < 8; ++a) {
            const LAS unsigned* ap = (const LAS unsigned*)(aP + (8 * a + b) * 64);
            u32x4 w = {ap[0], ap[1], ap[2], ap[3]}; A[a] = __builtin_bit_cast(bf16x8, w);
            B[a] = ld128(bP + b * 64 + (7 - a) * 512);
        }
#pragma unroll
        for (int a = 0; a < 8; ++a)
#pragma unroll
            for (int cc = a; cc < 8; ++cc) acc[cc] = MFMA(A[a], B[cc - a], acc[cc]);
        SBAR();
    }
    STAMP(10);
    ldsp ex = lds0 + L_CVX + wid * 4096;
    if (half) {
#pragma unroll
        for (int i = 0; i < 4; ++i) { *(LAS f32x4*)(ex + i * 1024 + lane * 16) = acc[i]; acc[i] = acc[4 + i]; }
    } else {
#pragma unroll
        for (int i = 0; i < 4; ++i) *(LAS f32x4*)(ex + i * 1024 + lane * 16) = acc[4 + i];
    }
    asm volatile("s_waitcnt vmcnt(14) lgkmcnt(0)\n\ts_barrier" : "+v"(GR.xv[0]), "+v"(GR.xv[1]), "+v"(GR.gv[0]), "+v"(GR.gv[1]) :: "memory");
    kv_store<14>(kvo); kv_store<15>(kvo);
    {
        float s = 0.f;
#pragma unroll
        for (int i = 0; i < 2; ++i)
#pragma unroll
            for (int e = 0; e < 4; ++e) { const unsigned pr = cvtpk(bf_lo(GR.xv[i][e]) * bf_lo(GR.gv[i][e]), bf_hi(GR.xv[i][e]) * bf_hi(GR.gv[i][e])); s += bf_lo(pr) + bf_hi(pr); }
        s = wave_sum63(s);
        const float g = rbf(__builtin_amdgcn_rcpf(1.f + __expf(-rbf(s))));
        if (lane == 63) __hip_atomic_store((__attribute__((address_space(1))) unsigned*)(P.ws + OFF_G) + tg, __float_as_uint(g), RLX_AGENT);
    }
    ldscp exo = lds0 + L_CVX + (wid ^ 1) * 4096;
    const __amdgpu_buffer_rsrc_t zrs = __builtin_amdgcn_make_buffer_rsrc(P.ws + OFF_Z, (short)0, (int)(4 * MB), 0x00020000);
    u32x2 z[4];
#pragma unroll
    for (int i = 0; i < 4; ++i) {
        const f32x4 o = *(const LAS f32x4*)(exo + i * 1024 + lane * 16);
        const f32x4 y = acc[i] + o;
        const int t = 256 * (4 * half + i) + 16 * c + 4 * G;
        const u32x2 uw = *(const LAS u32x2*)(ub + (2044 - t) * 2);
        const float u0 = bf_hi(uw[1]), u1 = bf_lo(uw[1]), u2 = bf_hi(uw[0]), u3 = bf_lo(uw[0]);
        z[i][0] = cvtpk(rbf(y[0]) + rbf(u0 * Dv), rbf(y[1]) + rbf(u1 * Dv)); z[i][1] = cvtpk(rbf(y[2]) + rbf(u2 * Dv), rbf(y[3]) + rbf(u3 * Dv));
    }
#pragma unroll
    for (int i = 0; i < 4; i += 2) {
        const auto r0 = __builtin_amdgcn_permlane16_swap(z[i][0], z[i + 1][0], false, false), r1 = __builtin_amdgcn_permlane16_swap(z[i][1], z[i + 1][1], false, false);
        const u32x4 w = {r0[0], r1[0], r0[1], r1[1]};
        const int t0 = 256 * (4 * half + i + (G & 1)) + 16 * c + 8 * (G >> 1);
        __builtin_amdgcn_raw_buffer_store_b128(w, zrs, (unsigned)(((dch0 + ch) * SEQ + t0) * 2), 0, 16);
    }
    LBAR();
}

struct AttnRegs {
    bf16x8 qf[2][4];
    f32x4 O[8][2];
    f32x4 negm[2];
    f32x4 l[2];
};
constexpr bf16x8 BF_ONES = {0x3f80, 0x3f80, 0x3f80, 0x3f80, 0x3f80, 0x3f80, 0x3f80, 0x3f80};
DI void sm_mask(f32x4 (&S)[2][2], int dq0) {
#pragma unroll
    for (int qt = 0; qt < 2; ++qt)
#pragma unroll
        for (int k2 = 0; k2 < 2; ++k2)
#pragma unroll
            for (int e = 0; e < 4; ++e) if (16 * k2 + e > dq0 + 16 * qt) S[k2][qt][e] = -INFINITY;
}
DI float rowmax8(const f32x4& a, const f32x4& b) {
    float m = fmaxf(fmaxf(fmaxf(a[0], a[1]), fmaxf(a[2], a[3])), fmaxf(fmaxf(b[0], b[1]), fmaxf(b[2], b[3])));
    auto r16 = __builtin_amdgcn_permlane16_swap(__float_as_uint(m), __float_as_uint(m), false, false);
    m = fmaxf(__uint_as_float(r16[0]), __uint_as_float(r16[1]));
    auto r32 = __builtin_amdgcn_permlane32_swap(__float_as_uint(m), __float_as_uint(m), false, false);
    return fmaxf(__uint_as_float(r32[0]), __uint_as_float(r32[1]));
}
struct DmaJob { const char* gk; const char* gv; unsigned lk, lv, voff; };
DI void mfma_acc(f32x4& d, const bf16x8& a, const bf16x8& b) { d = MFMA(a, b, d); PIN(d); }
DI void mfma_inpl(f32x4& d, const bf16x8& a, const bf16x8& b) { asm volatile("s_nop 1\n\tv_mfma_f32_16x16x32_bf16 %0, %1, %2, %0" : "+v"(d) : "v"(a), "v"(b)); }
DI void pv_fence() { asm volatile("s_nop 7\n\ts_nop 3" ::: "memory"); }
DI void mfma_new(f32x4& d, const bf16x8& a, const bf16x8& b, const f32x4& c) { d = MFMA(a, b, c); PIN(d); }
template <bool SM, bool QK, bool PV, bool NXK, bool NXV, bool DMA, bool LEAD, bool INPL = false>
DI void attn_slot(AttnRegs& R, f32x4 (&SC)[2][2], f32x4 (&SN)[2][2], u32x4 (&PC)[2], u32x4 (&PP)[2], bf16x8 (&kf)[4], bf16x8 (&vf)[4], ldscp kp, ldscp vp, const DmaJob& J) {
    auto sm_step = [&](int j) {
        const int sq = j >> 2, w = j & 3;
        float e0 = __builtin_amdgcn_exp2f(SC[w >> 1][sq][2 * (w & 1)]); PIN(e0);
        float e1 = __builtin_amdgcn_exp2f(SC[w >> 1][sq][2 * (w & 1) + 1]); PIN(e1);
        PC[sq][w] = cvtpk(e0, e1); PIN(PC[sq]);
    };
    if (SM && LEAD) { sm_step(0); sm_step(1); SBAR(); }
#pragma unroll
    for (int j = 0; j < 8; ++j) {
        const int k2 = j >> 2, s = j & 3, js = LEAD ? j + 2 : j, sq = js >> 2, w = js & 3;
        const bool doe = SM && js < 8;
        float e0 = 0.f, e1 = 0.f;
        if (doe) { e0 = __builtin_amdgcn_exp2f(SC[w >> 1][sq][2 * (w & 1)]); PIN(e0); }
        if (QK) { if (s == 0) mfma_new(SN[k2][0], kf[j & 3], R.qf[0][s], R.negm[0]); else mfma_acc(SN[k2][0], kf[j & 3], R.qf[0][s]); }
        SBAR();
        if (doe) { e1 = __builtin_amdgcn_exp2f(SC[w >> 1][sq][2 * (w & 1) + 1]); PIN(e1); }
        if (QK) { if (s == 0) mfma_new(SN[k2][1], kf[j & 3], R.qf[1][s], R.negm[1]); else mfma_acc(SN[k2][1], kf[j & 3], R.qf[1][s]); }
        if (j < 4 ? QK : NXK) kf[j & 3] = ld128(kp + (j + 4) * 1024);
        SBAR();
        if (PV) { if (INPL) mfma_inpl(R.O[j][0], vf[j & 3], __builtin_bit_cast(bf16x8, PP[0])); else mfma_acc(R.O[j][0], vf[j & 3], __builtin_bit_cast(bf16x8, PP[0])); }
        if (DMA) { switch (j) { case 0: dma4k<0>(J.lk, J.gk, J.voff); break; case 1: dma4k<1>(J.lk, J.gk, J.voff); break; case 2: dma4k<2>(J.lk, J.gk, J.voff); break; case 3: dma4k<3>(J.lk, J.gk, J.voff); break;
            case 4: dma4k<0>(J.lv, J.gv, J.voff); break; case 5: dma4k<1>(J.lv, J.gv, J.voff); break; case 6: dma4k<2>(J.lv, J.gv, J.voff); break; default: dma4k<3>(J.lv, J.gv, J.voff); break; } }
        SBAR();
        if (doe) { PC[sq][w] = cvtpk(e0, e1); PIN(PC[sq]); }
        if (PV) { if (INPL) mfma_inpl(R.O[j][1], vf[j & 3], __builtin_bit_cast(bf16x8, PP[1])); else mfma_acc(R.O[j][1], vf[j & 3], __builtin_bit_cast(bf16x8, PP[1])); }
        if (j < 4 ? PV : NXV) vf[j & 3] = ld128(vp + (j + 4) * 1024);
        if (SM && j == (LEAD ? 3 : 5)) mfma_acc(R.l[0], BF_ONES, __builtin_bit_cast(bf16x8, PC[0]));
        SBAR();
    }
    if (SM) mfma_acc(R.l[1], BF_ONES, __builtin_bit_cast(bf16x8, PC[1]));
}

DI void attn_redo(const Params& P, AttnRegs& R, int h, int x, int g, int lane, int dqa) {
    const __amdgpu_buffer_rsrc_t rs = __builtin_amdgcn_make_buffer_rsrc(P.ws + OFF_KF, (short)0, (int)(32 * MB), 0x00020000);
    float m[2] = {-1.0e30f, -1.0e30f};
#pragma unroll
    for (int dt = 0; dt < 8; ++dt) { R.O[dt][0] = (f32x4){0.f, 0.f, 0.f, 0.f}; R.O[dt][1] = (f32x4){0.f, 0.f, 0.f, 0.f}; }
    R.l[0] = (f32x4){0.f, 0.f, 0.f, 0.f}; R.l[1] = (f32x4){0.f, 0.f, 0.f, 0.f};
#pragma unroll 1
    for (int z = 0; z < 2 * x + 2; ++z) {
        const unsigned ko = (unsigned)(h * 512 * 1024 + (4 * (z >> 1) + 2 * g + (z & 1)) * 8192 + lane * 16), vo = ko + (unsigned)(16 * MB);
        f32x4 S[2][2];
#pragma unroll
        for (int k2 = 0; k2 < 2; ++k2) {
            S[k2][0] = (f32x4){0.f, 0.f, 0.f, 0.f}; S[k2][1] = (f32x4){0.f, 0.f, 0.f, 0.f};
#pragma unroll
            for (int s = 0; s < 4; ++s) {
                const bf16x8 kf = __builtin_bit_cast(bf16x8, __builtin_amdgcn_raw_buffer_load_b128(rs, ko + (k2 * 4 + s) * 1024, 0, 16));
                S[k2][0] = MFMA(kf, R.qf[0][s], S[k2][0]); S[k2][1] = MFMA(kf, R.qf[1][s], S[k2][1]);
            }
        }
        if (z >= 2 * x) sm_mask(S, dqa - 32 * (z & 1));
        bf16x8 pf[2]; float f[2];
#pragma unroll
        for (int qt = 0; qt < 2; ++qt) {
            const float mn = fmaxf(m[qt], rowmax8(S[0][qt], S[1][qt])); f[qt] = __builtin_amdgcn_exp2f(m[qt] - mn);
            m[qt] = mn;
            f32x4 p0, p1;
#pragma unroll
            for (int e = 0; e < 4; ++e) { p0[e] = __builtin_amdgcn_exp2f(S[0][qt][e] - mn); p1[e] = __builtin_amdgcn_exp2f(S[1][qt][e] - mn); }
            pf[qt] = pack8(p0, p1);
            R.l[qt] = MFMA(BF_ONES, pf[qt], R.l[qt] * f[qt]);
        }
#pragma unroll
        for (int dt = 0; dt < 8; ++dt) {
            const bf16x8 vf = __builtin_bit_cast(bf16x8, __builtin_amdgcn_raw_buffer_load_b128(rs, vo + dt * 1024, 0, 16));
            R.O[dt][0] = MFMA(vf, pf[0], R.O[dt][0] * f[0]); R.O[dt][1] = MFMA(vf, pf[1], R.O[dt][1] * f[1]);
        }
    }
    R.negm[0] = (f32x4){-m[0], -m[0], -m[0], -m[0]}; R.negm[1] = (f32x4){-m[1], -m[1], -m[1], -m[1]};
}

DI void phase_attn(const Params& P, ldsp lds, int wid, int h, int xa, const char* warm, gu64* cflag, gu64* eflags, u64 tag STAMP_ARG) {
    const int lane = fresh_lane();
    const int g = wid >> 2, i = g ? 3 - (wid & 3) : (wid & 3);
    const unsigned ldsb = (unsigned)(uintptr_t)lds + wid * 1024;
    const char* gK = P.ws + OFF_KF + (size_t)h * 512 * 1024 + wid * 1024;
    const char* gV = P.ws + OFF_VF + (size_t)h * 512 * 1024 + wid * 1024;
    const unsigned voff = lane * 16;
#define DMA_K(T, slot) do { _Pragma("unroll") for (int q_ = 0; q_ < 4; ++q_) dma16c(ldsb + L_K + (slot) * 32768 + q_ * 8192, gK + (size_t)(T) * 32768 + q_ * 8192, voff); } while (0)
#define DMA_V(T, slot) do { _Pragma("unroll") for (int q_ = 0; q_ < 4; ++q_) dma16c(ldsb + L_V + (slot) * 32768 + q_ * 8192, gV + (size_t)(T) * 32768 + q_ * 8192, voff); } while (0)
    AttnRegs R;
    f32x4 SA[2][2], SB[2][2]; u32x4 PA[2], PB[2]; bf16x8 kf[4], vf[4];
    ldscp kbase = lds + L_K + g * 16384 + lane * 16, vbase = lds + L_V + g * 16384 + lane * 16;
    auto dq = [&]() { const int l_ = fresh_lane(); return 32 * i + (l_ & 15) - 64 * g - 4 * (l_ >> 4); };
    DmaJob J; J.voff = voff;
    int kp = 0, vp = 0;
    const int xb = 15 - xa;
    auto load_q = [&](int qoff) {
        ldscp qb = lds + qoff + 2 * i * 4096 + fresh_lane() * 16;
#pragma unroll
        for (int qt = 0; qt < 2; ++qt)
#pragma unroll
            for (int s = 0; s < 4; ++s) R.qf[qt][s] = ld128(qb + (qt * 4 + s) * 1024);
    };
    auto clear_o = [&]() {
#pragma unroll
        for (int dt = 0; dt < 8; ++dt) { R.O[dt][0] = (f32x4){0.f, 0.f, 0.f, 0.f}; R.O[dt][1] = (f32x4){0.f, 0.f, 0.f, 0.f}; }
        R.l[0] = (f32x4){0.f, 0.f, 0.f, 0.f}; R.l[1] = (f32x4){0.f, 0.f, 0.f, 0.f};
    };
    auto set_ref = [&](bool masked) {
        if (masked) sm_mask(SA, dq());
#pragma unroll
        for (int qt = 0; qt < 2; ++qt) {
            const float m = fmaxf(rowmax8(SA[0][qt], SA[1][qt]), -1.0e30f);
            R.negm[qt] = (f32x4){-m, -m, -m, -m};
            SA[0][qt] -= m; SA[1][qt] -= m;
        }
    };
    const bool a0 = 2 * g <= i, a1 = 2 * g + 1 <= i;
    bool pend = false;
    auto publish = [&]() { if (pend) { if (wid == 0 && fresh_lane() == 0) __hip_atomic_store(cflag + 32 * xa, tag, RLX_AGENT); pend = false; } };
    auto open_interval = [&](int T, int x, bool first) {
        WAIT_BAR(0);
        publish();
        kp ^= 1;
        ldscp kq = kbase + kp * 32768, vq = vbase + vp * 32768;
#pragma unroll
        for (int j = 0; j < 4; ++j) { kf[j] = ld128(kq + j * 1024); vf[j] = ld128(vq + j * 1024); }
        J.gk = gK + wid * 3072 + (size_t)(T < x ? T + 1 : 0) * 32768; J.lk = ldsb + wid * 3072 + L_K + (kp ^ 1) * 32768;
        J.gv = gV + wid * 3072 + (size_t)T * 32768; J.lv = ldsb + wid * 3072 + L_V + (vp ^ 1) * 32768;
        SBAR();
    };
    auto steps = [&](int last, int x, bool first) {
        unsigned wdump = 0;
        for (int T = 1; T <= last; ++T) {
            open_interval(T, x, first);
            if (x >= 8 && T >= x - 5 && T < x - 1) {
                const char* w_ = warm + (size_t)(T - (x - 5)) * 2 * DM * 2;
                asm volatile("global_load_dword %0, %1, %2" : "+v"(wdump) : "v"(voff), "s"(w_) : "memory");
                asm volatile("global_load_dword %0, %1, %2" : "+v"(wdump) : "v"(voff), "s"(w_ + DM * 2) : "memory");
            }
            ldscp kq = kbase + kp * 32768, vq = vbase + vp * 32768;
            attn_slot<true, true, true, true, true, true, AT_LEAD>(R, SB, SA, PB, PA, kf, vf, kq, vq, J);
            if (T == x) sm_mask(SA, dq());
            attn_slot<true, true, true, false, false, false, false>(R, SA, SB, PA, PB, kf, vf, kq + 8192, vq + 8192, J);
            vp ^= 1;
        }
        asm volatile("" :: "v"(wdump));
    };
    auto finish = [&](auto next_tag, int x, int xn) {
        constexpr bool NEXT = decltype(next_tag)::value;
        const int mode = (!AT_SKIP || x == 0 || a1) ? 2 : (a0 ? 1 : 0);
        auto last_interval = [&](auto mode_tag) {
            constexpr int M = decltype(mode_tag)::value;
            if (M < 2) {
                open_interval(x, x, false);
                ldscp kq = kbase + kp * 32768, vq = vbase + vp * 32768;
                attn_slot<true, M == 1, true, false, true, true, AT_LEAD>(R, SB, SA, PB, PA, kf, vf, kq, vq, J);
                if (M == 1) sm_mask(SA, dq());
                attn_slot<M == 1, false, true, false, false, false, false, M == 0>(R, SA, SB, PA, PB, kf, vf, kq + 8192, vq + 8192, J);
                vp ^= 1;
            }
            WAIT_BAR(0);
            publish();
            STAMP(NEXT ? 52 : 56);
            if (NEXT) DMA_K(xn > 0 ? 1 : 0, kp);
            ldscp vq = vbase + vp * 32768;
            if (M > 0) {
#pragma unroll
                for (int j = 0; j < 4; ++j) vf[j] = ld128(vq + j * 1024);
            }
            if (M == 2) {
                sm_mask(SB, dq() - 32);
                attn_slot<true, false, true, false, true, false, AT_LEAD>(R, SB, SA, PB, PA, kf, vf, kbase, vq, J);
            } else {
                if (M == 1) attn_slot<false, false, true, false, true, false, false>(R, SB, SA, PB, PA, kf, vf, kbase, vq, J);
                else {
#pragma unroll
                    for (int j = 0; j < 4; ++j) vf[j] = ld128(vq + 8192 + j * 1024);
                }
                PB[0] = (u32x4){0, 0, 0, 0}; PB[1] = (u32x4){0, 0, 0, 0};
            }
        };
        if (mode == 2) last_interval(std::integral_constant<int, 2>{});
        else if (mode == 1) last_interval(std::integral_constant<int, 1>{});
        else last_interval(std::integral_constant<int, 0>{});
        if (__builtin_expect(__any(!(R.l[0][0] <= L_LIMIT) || !(R.l[1][0] <= L_LIMIT)), 0)) {
            pv_fence();
            attn_redo(P, R, h, x, g, fresh_lane(), dq());
            PB[0] = (u32x4){0, 0, 0, 0}; PB[1] = (u32x4){0, 0, 0, 0};
        }
        u32x2 efl;
        if constexpr (!NEXT) efl = flags8_issue(eflags, fresh_lane());
        STAMP(NEXT ? 53 : 57);
        const float nm0 = R.negm[0][0], nm1 = R.negm[1][0], lr[2] = {R.l[0][0], R.l[1][0]};
        const int kn = kp ^ 1;
        ldscp k0 = kbase + kn * 32768;
        if (NEXT) {
            load_q(L_QB);
#pragma unroll
            for (int j = 0; j < 4; ++j) kf[j] = ld128(k0 + j * 1024);
            R.negm[0] = (f32x4){0.f, 0.f, 0.f, 0.f}; R.negm[1] = (f32x4){0.f, 0.f, 0.f, 0.f};
        }
        SBAR();
        attn_slot<false, NEXT, true, NEXT, false, false, false>(R, SB, SA, PA, PB, kf, vf, k0, vbase + vp * 32768 + 8192, J);
        STAMP(NEXT ? 54 : 58);
        ldsp ex = lds + L_V + (vp ^ 1) * 32768;
        const int lane = fresh_lane();
        {
            ldsp mine = ex + (g * 4 + i) * 4096 + lane * 16;
            if (g) {
#pragma unroll
                for (int sg = 0; sg < 2; ++sg)
#pragma unroll
                    for (int qt = 0; qt < 2; ++qt) *(LAS bf16x8*)(mine + (sg * 2 + qt) * 1024) = pack8(R.O[2 * sg][qt], R.O[2 * sg + 1][qt]);
#pragma unroll
                for (int d4 = 0; d4 < 4; ++d4) { R.O[d4][0] = R.O[4 + d4][0]; R.O[d4][1] = R.O[4 + d4][1]; }
            } else {
#pragma unroll
                for (int sg = 0; sg < 2; ++sg)
#pragma unroll
                    for (int qt = 0; qt < 2; ++qt) *(LAS bf16x8*)(mine + (sg * 2 + qt) * 1024) = pack8(R.O[4 + 2 * sg][qt], R.O[4 + 2 * sg + 1][qt]);
            }
        }
        if constexpr (!NEXT) {
            asm volatile("s_waitcnt vmcnt(0)" : "+v"(efl) :: "memory");
            if (!__all((((u64)efl[1] << 32) | efl[0]) == tag)) wait_flags2(eflags, tag, fresh_lane());
        }
        LBAR();
        ldsp ml = lds + L_QB;
        *(LAS f32x4*)(ml + (g * 4 + i) * 1024 + lane * 16) = (f32x4){nm0, nm1, lr[0], lr[1]};
        if (NEXT) { DMA_V(0, vp); set_ref(xn == 0); }
        LBAR();
        {
            const f32x4 oth = *(const LAS f32x4*)(ml + ((g ^ 1) * 4 + i) * 1024 + lane * 16);
            ldscp theirs = ex + ((g ^ 1) * 4 + i) * 4096 + lane * 16;
            float fo[2], ft[2];
#pragma unroll
            for (int qt = 0; qt < 2; ++qt) {
                const float own = qt ? nm1 : nm0, nm = fminf(own, oth[qt]);
                const float a = __builtin_amdgcn_exp2f(nm - own), b = __builtin_amdgcn_exp2f(nm - oth[qt]);
                const float inv = __builtin_amdgcn_rcpf(lr[qt] * a + oth[2 + qt] * b);
                fo[qt] = a * inv; ft[qt] = b * inv;
            }
            const __amdgpu_buffer_rsrc_t crs = __builtin_amdgcn_make_buffer_rsrc(P.ws + OFF_CTX, (short)0, (int)(16 * MB), 0x00020000);
            const unsigned C = (unsigned)(((x * 128 + 4 * h + 2 * g) * 8 + 2 * i) * 1024 + lane * 16);
#pragma unroll
            for (int sg = 0; sg < 2; ++sg)
#pragma unroll
                for (int qt = 0; qt < 2; ++qt) {
                    const u32x4 t = *(const LAS u32x4*)(theirs + (sg * 2 + qt) * 1024);
                    const f32x4 t0 = {bf_lo(t[0]), bf_hi(t[0]), bf_lo(t[1]), bf_hi(t[1])}, t1 = {bf_lo(t[2]), bf_hi(t[2]), bf_lo(t[3]), bf_hi(t[3])};
                    store16_wt(crs, C + (sg * 8 + qt) * 1024, pack8(R.O[2 * sg][qt] * fo[qt] + t0 * ft[qt], R.O[2 * sg + 1][qt] * fo[qt] + t1 * ft[qt]));
                }
        }
        STAMP(NEXT ? 55 : 59);
        if (NEXT) {
            clear_o();
            attn_slot<true, true, false, false, false, false, false>(R, SA, SB, PA, PB, kf, vf, k0 + 8192, vbase, J);
            kp = kn;
            pend = true;
        }
    };
    load_q(L_QA);
    clear_o();
    WAIT_BAR(0);
    STAMP(32);
    DMA_K(xa > 0 ? 1 : 0, 1);
    {
        ldscp k0 = kbase;
#pragma unroll
        for (int k2 = 0; k2 < 2; ++k2)
#pragma unroll
            for (int qt = 0; qt < 2; ++qt) {
                f32x4 a = {0.f, 0.f, 0.f, 0.f};
#pragma unroll
                for (int s = 0; s < 4; ++s) a = MFMA(ld128(k0 + (k2 * 4 + s) * 1024), R.qf[qt][s], a);
                SA[k2][qt] = a;
            }
#pragma unroll
        for (int j = 0; j < 4; ++j) kf[j] = ld128(k0 + 8192 + j * 1024);
        set_ref(xa == 0);
        attn_slot<true, true, false, false, false, false, false>(R, SA, SB, PA, PB, kf, vf, k0 + 8192, vbase, J);
    }
    const bool full = !AT_SKIP || a1;
    steps(full || xa == 0 ? xa : xa - 1, xa, true);
    finish(std::integral_constant<bool, true>{}, xa, xb);
    steps(full || xb == 0 ? xb : xb - 1, xb, false);
    finish(std::integral_constant<bool, false>{}, xb, 0);
#undef DMA_K
#undef DMA_V
}

constexpr int OP_STG = 32768, OP_NS = 4;
constexpr int OP_X = 131072, OP_Z = 147456, OP_OT = 65536;
DI void phase_oproj(const Params& P, ldsp lds, int wid, gu64* sy, u64 tag, gu64* cflag_b STAMP_ARG) {
    const int r = blockIdx.x & 7, idx = blockIdx.x >> 3;
    const int ks = r & 1, nb = 4 * (r >> 2) + (idx & 3), tb = 8 * ((r >> 1) & 1) + (idx >> 2);
    const int lane = fresh_lane(), tid = wid * 64 + lane;
    const int th = wid >> 2, nq = wid & 3, c = lane & 15, G = lane >> 4;
    const unsigned ldsb = (unsigned)(uintptr_t)lds + wid * 1024;
    const int pair = tb * 8 + nb;
    const int e = tb >= 8 ? 0 : 1;
    const char* gC0 = P.ws + OFF_CTX + (size_t)tb * 128 * 8 * 1024 + (size_t)ks * 32 * 16384 + wid * 1024;
    const char* gW0 = (const char*)P.wo + ((size_t)ks * 2048 * DM + nb * 128) * 2;
    const char* gCp[2] = {gC0 + e * 32768, gC0 + (1 - e) * 32768};
    const char* gWp[2] = {gW0 + (size_t)e * 128 * DM * 2, gW0 + (size_t)(1 - e) * 128 * DM * 2};
    const unsigned voffW = voff_wgt(wid, lane, DM);
    const unsigned voffC = lane * 16;
#define OKT(j) (4 * (((j) & 15) >> 1) + ((j) & 1))
#define OPIECE(so, j, q) do { if ((q) < 2) dma16c(ldsb + (so) + (q) * 8192, gCp[(j) >> 4] + (size_t)OKT(j) * 16384 + (q) * 8192, voffC); \
        else dma16(ldsb + (so) + 16384 + ((q) - 2) * 8192, gWp[(j) >> 4] + ((size_t)OKT(j) * 64 + ((q) - 2) * 32) * DM * 2, voffW); } while (0)
#define OST(so, t) do { OPIECE(so, t, 0); OPIECE(so, t, 1); OPIECE(so, t, 2); OPIECE(so, t, 3); } while (0)
    OPIECE(0, 0, 2); OPIECE(0, 0, 3); OPIECE(OP_STG, 1, 2); OPIECE(OP_STG, 1, 3);
    OPIECE(0, 0, 0); OPIECE(0, 0, 1); OPIECE(OP_STG, 1, 0); OPIECE(OP_STG, 1, 1);
    LBAR();
    OPIECE(2 * OP_STG, 2, 2); OPIECE(2 * OP_STG, 2, 3); OPIECE(3 * OP_STG, 3, 2); OPIECE(3 * OP_STG, 3, 3);
    OPIECE(2 * OP_STG, 2, 0); OPIECE(2 * OP_STG, 2, 1); OPIECE(3 * OP_STG, 3, 0); OPIECE(3 * OP_STG, 3, 1);
    STAMP(33);
    const int qq = (lane & 15) >> 2, pp = lane & 3;
    ldscp wP[2];
#pragma unroll
    for (int n = 0; n < 2; ++n) wP[n] = lds + 16384 + (G >> 1) * 2048 + (nq >> 1) * 1024 + wgt_rd(4 * (G & 1) + qq, (G >> 1) & 1, 2 * (nq & 1) + n, pp);
    ldscp cP = lds + th * 4096 + lane * 16;
    f32x4 acc[2][4];
#pragma unroll
    for (int n = 0; n < 2; ++n)
#pragma unroll
        for (int m = 0; m < 4; ++m) acc[n][m] = (f32x4){0.f, 0.f, 0.f, 0.f};
    constexpr int NT = 32;
    struct Frag { bf16x8 w[2][2], c[2][4]; };
#define ORD_W(F, so, s, n) F.w[s][n] = ldtr2(wP[n] + (so) + (s) * 8192, wP[n] + (so) + (s) * 8192 + 4096)
#define ORD_C(F, so, s, m) F.c[s][m] = ld128(cP + (so) + (s) * 8192 + (m) * 1024)
#define OREAD(F, so) do { _Pragma("unroll") for (int s = 0; s < 2; ++s) { ORD_W(F, so, s, 0); ORD_W(F, so, s, 1); _Pragma("unroll") for (int m = 0; m < 4; ++m) ORD_C(F, so, s, m); } } while (0)
#define OM1(F, s, n, m) do { acc[n][m] = MFMA(F.w[s][n], F.c[s][m], acc[n][m]); PIN(acc[n][m]); } while (0)
#define OBODY(FC, RD, FN, s1, FILL, so, tf) do { \
        OM1(FC, 0, 0, 0); if (RD) ORD_W(FN, s1, 0, 0); SBAR(); OM1(FC, 0, 0, 1); if (RD) ORD_C(FN, s1, 0, 0); SBAR(); OM1(FC, 0, 0, 2); if (RD) ORD_C(FN, s1, 0, 1); SBAR(); OM1(FC, 0, 0, 3); if (FILL) OPIECE(so, tf, 0); SBAR(); \
        OM1(FC, 0, 1, 0); if (RD) ORD_W(FN, s1, 0, 1); SBAR(); OM1(FC, 0, 1, 1); if (RD) ORD_C(FN, s1, 0, 2); SBAR(); OM1(FC, 0, 1, 2); if (RD) ORD_C(FN, s1, 0, 3); SBAR(); OM1(FC, 0, 1, 3); if (FILL) OPIECE(so, tf, 1); SBAR(); \
        OM1(FC, 1, 0, 0); if (RD) ORD_W(FN, s1, 1, 0); SBAR(); OM1(FC, 1, 0, 1); if (RD) ORD_C(FN, s1, 1, 0); SBAR(); OM1(FC, 1, 0, 2); if (RD) ORD_C(FN, s1, 1, 1); SBAR(); OM1(FC, 1, 0, 3); if (FILL) OPIECE(so, tf, 2); SBAR(); \
        OM1(FC, 1, 1, 0); if (RD) ORD_W(FN, s1, 1, 1); SBAR(); OM1(FC, 1, 1, 1); if (RD) ORD_C(FN, s1, 1, 2); SBAR(); OM1(FC, 1, 1, 2); if (RD) ORD_C(FN, s1, 1, 3); SBAR(); OM1(FC, 1, 1, 3); if (FILL) OPIECE(so, tf, 3); SBAR(); \
    } while (0)
    Frag fa, fb;
    WAIT_BAR(10);
    if (tid == 0) __hip_atomic_store(cflag_b, tag, RLX_AGENT);
    STAMP(35);
    OREAD(fa, 0);
#define OITER(NV, NX, FILL, t, FC, FN) do { constexpr int s0 = ((t) % OP_NS) * OP_STG, s1 = (((t) + 1) % OP_NS) * OP_STG; \
        if ((NX) != 0 && wid == 0) WAIT_BAR((NV) + (NX)); else WAIT_BAR(NV); \
        if ((t) == 11) { asm volatile("" : "+v"(lfl)); if (!__all((((u64)lfl[1] << 32) | lfl[0]) == tag)) wait_flags2(lflags, tag, lane); }        \
        if ((t) == 8) lfl = flags8_issue(lflags, lane); \
        OBODY(FC, true, FN, s1, FILL, s0, (t) + OP_NS); } while (0)
    gu64* lflags = sy + SY_CTX + 32 * tb + 16 * ks + (1 - e);
    u32x2 lfl;
    OITER(8, 1, true, 0, fa, fb);
    {
        const char* gx = (const char*)P.x + ((size_t)(tb * 128 + ks * 64) * DM + nb * 128) * 2;
        const unsigned vx = (unsigned)(((4 * wid + (lane >> 4)) * DM + (lane & 15) * 8) * 2);
        dma16(ldsb + OP_X, gx, vx); dma16(ldsb + OP_X + 8192, gx + 32 * DM * 2, vx);
        const char* gz = P.ws + OFF_Z + ((size_t)nb * 128 * SEQ + tb * 128 + ks * 64) * 2;
        const unsigned vz = (unsigned)(((8 * wid + (lane >> 3)) * SEQ + (lane & 7) * 8) * 2);
        dma16c(ldsb + OP_Z, gz, vz); dma16c(ldsb + OP_Z + 8192, gz + 64 * SEQ * 2, vz);
    }
    OITER(10, 1, true, 1, fb, fa);
    OITER(12, 1, true, 2, fa, fb);
    OITER(12, 0, true, 3, fb, fa);
    OITER(8, 0, true, 4, fa, fb);  OITER(8, 0, true, 5, fb, fa);  OITER(8, 0, true, 6, fa, fb);  OITER(8, 0, true, 7, fb, fa);
    OITER(8, 0, true, 8, fa, fb);  OITER(9, 0, true, 9, fb, fa);  OITER(9, 0, true, 10, fa, fb); OITER(8, 0, true, 11, fb, fa);
    OITER(8, 0, true, 12, fa, fb); OITER(8, 0, true, 13, fb, fa); OITER(8, 0, true, 14, fa, fb); OITER(8, 0, true, 15, fb, fa);
    OITER(8, 0, true, 16, fa, fb); OITER(8, 0, true, 17, fb, fa); OITER(8, 0, true, 18, fa, fb); OITER(8, 0, true, 19, fb, fa);
    OITER(8, 0, true, 20, fa, fb); OITER(8, 0, true, 21, fb, fa); OITER(8, 0, true, 22, fa, fb); OITER(8, 0, true, 23, fb, fa);
    OITER(8, 0, true, 24, fa, fb); OITER(8, 0, true, 25, fb, fa); OITER(8, 0, true, 26, fa, fb); OITER(8, 0, true, 27, fb, fa);
    OITER(8, 0, false, 28, fa, fb);
    OITER(4, 0, false, 29, fb, fa);
    OITER(0, 0, false, 30, fa, fb);
    OBODY(fb, false, fa, 0, false, 0, 0);
#undef OBODY
#undef OM1
#undef OREAD
#undef ORD_W
#undef ORD_C
#undef OITER
#undef OKT
    STAMP(36);
    const __amdgpu_buffer_rsrc_t xb = __builtin_amdgcn_make_buffer_rsrc(P.ws + OFF_XB, (short)0, (int)(8 * MB), 0x00020000);
    const unsigned xslot = (unsigned)(nq * 8192 + lane * 16);
    unsigned bt[4][2][2];
    float gv[4];
    if (th != ks) {
#pragma unroll
        for (int n = 0; n < 2; ++n)
#pragma unroll
            for (int m = 0; m < 4; ++m) __builtin_amdgcn_raw_buffer_store_b128(__builtin_bit_cast(u32x4, acc[n][m]), xb, (unsigned)((pair * 2 + ks) * 32768) + xslot + (n * 4 + m) * 1024, 0, 16);
    } else {
        const __attribute__((address_space(1))) unsigned* Gt = (const __attribute__((address_space(1))) unsigned*)(P.ws + OFF_G) + tb * 128 + ks * 64;
#pragma unroll
        for (int m = 0; m < 4; ++m) gv[m] = __uint_as_float(__hip_atomic_load(Gt + 16 * m + c, RLX_AGENT));
#pragma unroll
        for (int m = 0; m < 4; ++m) {
            const int trow = 16 * m + c;
            const float omg = rbf(1.f - gv[m]);
#pragma unroll
            for (int n = 0; n < 2; ++n) {
                const int nt0 = (2 * nq + n) * 16, n0 = nt0 + 4 * G;
                const u32x2 xw = *(const LAS u32x2*)(lds + OP_X + trow * 256 + n0 * 2);
                const u32x2 zw = __builtin_bit_cast(u32x2, ldtr(lds + OP_Z + (nt0 + 4 * G + qq) * 128 + (16 * m + 4 * pp) * 2));
                bt[m][n][0] = cvtpk(omg * rbf(bf_lo(xw[0]) * bf_lo(zw[0])), omg * rbf(bf_hi(xw[0]) * bf_hi(zw[0])));
                bt[m][n][1] = cvtpk(omg * rbf(bf_lo(xw[1]) * bf_lo(zw[1])), omg * rbf(bf_hi(xw[1]) * bf_hi(zw[1])));
            }
        }
    }
    WAIT_BAR(0);
    STAMP(37);
    if (tid == 0) __hip_atomic_store(sy + SY_PAIR + 2 * pair + ks, tag, RLX_AGENT);
    if (wid == 0) wait_flags(sy + SY_PAIR + 2 * pair + (ks ^ 1), 1, tag, lane);
    BAR();
    STAMP(38);
    if (th == ks) {
        u32x4 pt[2][4];
#pragma unroll
        for (int n = 0; n < 2; ++n)
#pragma unroll
            for (int m = 0; m < 4; ++m) pt[n][m] = __builtin_amdgcn_raw_buffer_load_b128(xb, (unsigned)((pair * 2 + (ks ^ 1)) * 32768) + xslot + (n * 4 + m) * 1024, 0, 16);
#pragma unroll
        for (int m = 0; m < 4; ++m) {
            const int trow = 16 * m + c;
            const float g = gv[m];
#pragma unroll
            for (int n = 0; n < 2; ++n) {
                const f32x4 a = acc[n][m] + __builtin_bit_cast(f32x4, pt[n][m]);
                const int n0 = (2 * nq + n) * 16 + 4 * G;
                u32x2 w = {cvtpk(rbf(g * rbf(a[0])) + bf_lo(bt[m][n][0]), rbf(g * rbf(a[1])) + bf_hi(bt[m][n][0])),
                           cvtpk(rbf(g * rbf(a[2])) + bf_lo(bt[m][n][1]), rbf(g * rbf(a[3])) + bf_hi(bt[m][n][1]))};
                *(LAS u32x2*)(lds + OP_OT + trow * 272 + n0 * 2) = w;
            }
        }
    }
    LBAR();
#pragma unroll
    for (int i = 0; i < 2; ++i) {
        const int n = tid + 512 * i, row = n >> 4, ch = n & 15;
        const u32x4 v = *(const LAS u32x4*)(lds + OP_OT + row * 272 + ch * 16);
        *(u32x4*)(P.out + (size_t)(tb * 128 + ks * 64 + row) * DM + nb * 128 + ch * 8) = v;
    }
    STAMP(39);
#undef OST
#undef OPIECE
}

__global__ __launch_bounds__(512, 2) void k_main(Params P) {
    extern __shared__ __attribute__((aligned(16))) char smem[];
    ldsp lds = (ldsp)smem;
    const int tid = threadIdx.x, lane = tid & 63, wid = __builtin_amdgcn_readfirstlane(tid >> 6);
    const int r = blockIdx.x & 7, idx = blockIdx.x >> 3, h = 4 * r + (idx >> 3), p = idx & 7;
    const int xa = (h & 1) ? p : 15 - p;
    const int wr = wid >> 2, wc = wid & 3, c = lane & 15, G = lane >> 4;
    const unsigned ldsb = (unsigned)(uintptr_t)lds + wid * 1024;
    STAMP_DECL; STAMP(0);
    gu64* sy = (gu64*)(P.ws + OFF_SYNC);
    ldscp aP0 = lds + wr * 4 * 2048 + c * 128 + ((G ^ (c >> 1)) & 7) * 16;
    ldscp aP1 = lds + wr * 4 * 2048 + c * 128 + (((4 + G) ^ (c >> 1)) & 7) * 16;
    const int qq = (lane & 15) >> 2, pp = lane & 3;
    ldscp bP[2][2];
#pragma unroll
    for (int n = 0; n < 2; ++n)
#pragma unroll
        for (int hf = 0; hf < 2; ++hf) bP[n][hf] = lds + G * 2048 + (wc >> 1) * 1024 + wgt_rd(qq + 4 * hf, G & 1, 2 * (wc & 1) + n, pp);
    const unsigned voffB = voff_wgt(wid, lane, HDN);
    const int dch0 = r * 128 + idx * 4;
    u64 tag;
    float Dv;
    KvOut kvo;
    GateRegs GR;
    {
        const unsigned voffA = voff_act(wid, lane, DC);
        const char* gA = (const char*)P.ckv + (size_t)(256 * p) * DC * 2;
        const char* gB0 = (const char*)P.wuk + (size_t)h * HD * 2;
        const char* gB1 = (const char*)P.wuv + (size_t)h * HD * 2;
#define SA(b, hh) (((b) * 2 + (hh)) * HT)
#define SB(b, hh) ((4 + (b) * 2 + (hh)) * HT)
#define STA(b, hh, t) do { const char* g_ = gA + (size_t)(hh) * 128 * DC * 2 + (size_t)(t) * 128; dma16(ldsb + SA(b, hh), g_, voffA); dma16(ldsb + SA(b, hh) + 8192, g_ + 64 * DC * 2, voffA); } while (0)
#define STB(b, hh, t) do { const char* g_ = ((hh) ? gB1 : gB0) + (size_t)(t) * 64 * HDN * 2; dma16(ldsb + SB(b, hh), g_, voffB); dma16(ldsb + SB(b, hh) + 8192, g_ + 32 * HDN * 2, voffB); } while (0)
#define LDA(dst, b, hh) do { _Pragma("unroll") for (int m = 0; m < 4; ++m) { dst[m][0] = ld128(aP0 + SA(b, hh) + m * 2048); dst[m][1] = ld128(aP1 + SA(b, hh) + m * 2048); } } while (0)
        ldscp bQ[2][2];
#pragma unroll
        for (int n = 0; n < 2; ++n) { bQ[n][0] = bP[n][0] + 65536; bQ[n][1] = bP[n][1] + 65536; PIN(bQ[n][0]); PIN(bQ[n][1]); }
#define LDB(dst, b, hh) do { _Pragma("unroll") for (int n = 0; n < 2; ++n) _Pragma("unroll") for (int k = 0; k < 2; ++k) dst[n][k] = ldtr2(bQ[n][0] + (SB(b, hh) - 65536) + k * 8192, bQ[n][1] + (SB(b, hh) - 65536) + k * 8192); } while (0)
#define MMA(ai, bj, At, Bt) do { __builtin_amdgcn_s_setprio(1); _Pragma("unroll") for (int m = 0; m < 4; ++m) _Pragma("unroll") for (int n = 0; n < 2; ++n) _Pragma("unroll") for (int k = 0; k < 2; ++k) \
        acc[ai][bj][m][n] = (bj) ? MFMA(At[m][k], Bt[n][k], acc[ai][bj][m][n]) : MFMA(Bt[n][k], At[m][k], acc[ai][bj][m][n]); __builtin_amdgcn_s_setprio(0); } while (0)
        u32x2 ep; unsigned dvb;
        asm volatile("global_load_dwordx2 %0, %1, %2 sc1" : "=v"(ep) : "v"(0u), "s"(sy) : "memory");
        asm volatile("global_load_ushort %0, %1, %2" : "=v"(dvb) : "v"((unsigned)((dch0 + (wid >> 1)) * 2)), "s"(P.ssd) : "memory");
        prep_issue(P, lds, wid, r, idx, 0);
        unsigned cpf = 0;
        {
            u64 pc; asm volatile("s_getpc_b64 %0" : "=s"(pc));
            unsigned o = (unsigned)(((idx * 8 + wid) * 8 + (lane & 7)) * 64); o = o < 94000u ? o : 94000u;
            asm volatile("global_load_dword %0, %1, %2" : "=v"(cpf) : "v"(o), "s"(pc & ~(u64)63) : "memory");
        }
        BAR();
        STB(0, 0, 0); STA(0, 0, 0); STB(0, 1, 0); STA(0, 1, 0);
        asm volatile("s_waitcnt vmcnt(8)\n\ts_barrier" : "+v"(ep), "+v"(dvb), "+v"(cpf) :: "memory");
        STB(1, 0, 1); STA(1, 0, 1); STB(1, 1, 1); STA(1, 1, 1);
        tag = (((u64)__builtin_amdgcn_readfirstlane(ep[1]) << 32) | __builtin_amdgcn_readfirstlane(ep[0])) + 1;
        Dv = __uint_as_float(__builtin_amdgcn_readfirstlane(dvb) << 16);
        STAMP(1);
        prep_compute(lds, wid, idx, 0);
        prep_issue(P, lds, wid, r, idx, 1);
        prep_store(P, lds, wid, r, idx, 0);
        WAIT_BAR(12);
        STAMP(2);
        f32x4 acc[2][2][4][2];
#pragma unroll
        for (int a = 0; a < 2; ++a)
#pragma unroll
            for (int b = 0; b < 2; ++b)
#pragma unroll
                for (int m = 0; m < 4; ++m)
#pragma unroll
                    for (int n = 0; n < 2; ++n) acc[a][b][m][n] = (f32x4){0.f, 0.f, 0.f, 0.f};
        bf16x8 At[4][2], B0[2][2], B1[2][2];
        if (wr == 1) BAR();
#define TRIP(t) do { constexpr bool last = (t) == 6; \
            LDB(B0, 0, 0); SBAR(); LDA(At, 0, 0); if ((t) != 0) STA(1, 1, (t) + 1); \
            if ((t) == 2) prep_store(P, lds, wid, r, idx, 1); \
            if ((t) == 4 && wid == 0 && lane == 0) __hip_atomic_store(sy + SY_PREP + 32 * r + idx, tag, RLX_AGENT); \
            WAIT_L(8); BAR(); WAIT_L(0); MMA(0, 0, At, B0); BAR(); SBAR(); \
            LDB(B1, 0, 1); \
            if (!last) STB(0, 0, (t) + 2); else { dma16(ldsb + 98304 + 2 * HT, gwq, voffB); dma16(ldsb + 98304 + 2 * HT + 8192, gwq + 32 * HDN * 2, voffB); } \
            BAR(); WAIT_L(0); MMA(0, 1, At, B1); BAR(); \
            LDA(At, 0, 1); if (!last) STA(0, 0, (t) + 2); \
            BAR(); WAIT_L(0); MMA(1, 0, At, B0); BAR(); SBAR(); \
            if (!last) STB(0, 1, (t) + 2); \
            else { const char* src = (const char*)P.ssk + (size_t)(dch0 + cch) * SEQ * 2 + cpc * 1024; const unsigned dst = (unsigned)(uintptr_t)lds + cv_eo(cch) + CV_E + 32 + cpc * 1024; \
                dma16(dst, src, lane * 16); dma16(dst + 1024, src + 1024, lane * 16); } \
            if ((t) == 0) WAIT_V(10); else if ((t) == 2) WAIT_V(7); else if ((t) == 4) { if (wid == 0) WAIT_V(7); else WAIT_V(6); } else WAIT_V(4); \
            if (last && wid == 0) { \
                flags_check(lds + L_FLG, 0, sy + SY_PREP + 32 * r, 32, tag, lane); }    \
            BAR(); MMA(1, 1, At, B1); BAR(); \
            LDB(B0, 1, 0); SBAR(); LDA(At, 1, 0); if (!last) STA(0, 1, (t) + 2); \
            WAIT_L(8); BAR(); WAIT_L(0); MMA(0, 0, At, B0); BAR(); SBAR(); \
            LDB(B1, 1, 1); \
            if (!last) STB(1, 0, (t) + 3); else { dma16(ldsb + 98304, gx0, voffX); dma16(ldsb + 98304 + 8192, gx0 + 64 * DM * 2, voffX); } \
            BAR(); WAIT_L(0); MMA(0, 1, At, B1); BAR(); \
            LDA(At, 1, 1); if (!last) STA(1, 0, (t) + 3); \
            BAR(); WAIT_L(0); MMA(1, 0, At, B0); BAR(); SBAR(); \
            if (!last) { STB(1, 1, (t) + 3); WAIT_V(6); } \
            else { const char* src = (const char*)(P.ws + OFF_UR) + (size_t)(dch0 + cch) * SEQ * 2 + cpc * 1024; const unsigned dst = (unsigned)(uintptr_t)lds + cv_u(cch) + cpc * 1024; \
                dma16c(dst, src, lane * 16); dma16c(dst + 1024, src + 1024, lane * 16); \
                dma16(ldsb + 98304 + HT, gx1, voffX); dma16(ldsb + 98304 + HT + 8192, gx1 + 64 * DM * 2, voffX); } \
            if ((t) == 4 && wid == 0) flags_fetch((unsigned)(uintptr_t)lds + L_FLG, (const void*)(sy + SY_PREP + 32 * r), lane); \
            BAR(); MMA(1, 1, At, B1); BAR(); \
            STAMP(3 + ((t) >> 1)); } while (0)
        const int cch = wid >> 1, cpc = 2 * (wid & 1);
        const char* gx0 = (const char*)P.x + (size_t)(128 * xa) * DM * 2; const char* gx1 = (const char*)P.x + (size_t)(128 * (15 - xa)) * DM * 2;
        const char* gwq = (const char*)P.wq + (size_t)h * HD * 2;
        const unsigned voffX = voff_act(wid, lane, DM);
        TRIP(0);
        if (wr == 0) BAR();
        prep_compute(lds, wid, idx, 1);
        if (wr == 1) BAR();
        TRIP(2);
        TRIP(4);
        TRIP(6);
#undef TRIP
        if (wr == 0) BAR();
        STAMP(7);
        {
            const int tg = (idx * 8 + r) * 8 + wid;
#pragma unroll
            for (int i = 0; i < 2; ++i) { GR.xv[i] = gload16(P.x, (unsigned)((tg * DM + i * 512 + lane * 8) * 2)); GR.gv[i] = gload16(P.gw, (unsigned)((i * 512 + lane * 8) * 2)); }
        }
        kvo.rs = __builtin_amdgcn_make_buffer_rsrc(P.ws + OFF_KF, (short)0, (int)(32 * MB), 0x00020000);
        kvo.ko = (unsigned)(h * 512 * 1024 + lane * 16); kvo.vo = kvo.ko + (unsigned)(16 * MB); kvo.p = p; kvo.wr = wr; kvo.wc = wc;
#pragma unroll
        for (int ai = 0; ai < 2; ++ai) {
#pragma unroll
            for (int m = 0; m < 4; ++m) kvo.f[ai * 8 + m] = pack8(acc[ai][0][m][0], acc[ai][0][m][1]);
#pragma unroll
            for (int mm = 0; mm < 2; ++mm)
#pragma unroll
                for (int n = 0; n < 2; ++n) kvo.f[ai * 8 + 4 + mm * 2 + n] = pack8(acc[ai][1][2 * mm][n], acc[ai][1][2 * mm + 1][n]);
        }
        WAIT_BAR(10);
        STAMP(8);
#undef SA
#undef SB
#undef STA
#undef STB
#undef LDA
#undef LDB
#undef MMA
    }
    phase_conv(fresh_params(), lds, wid, dch0, Dv, kvo, GR, (idx * 8 + r) * 8 + wid STAMP_PASS);
    STAMP(11);
    {
        const Params P = fresh_params();
        const int lane = fresh_lane(), tid = wid * 64 + lane, c = lane & 15, G = lane >> 4, qq = (lane & 15) >> 2, pp = lane & 3;
        ldscp aP0 = lds + wr * 4 * 2048 + c * 128 + ((G ^ (c >> 1)) & 7) * 16;
        ldscp aP1 = lds + wr * 4 * 2048 + c * 128 + (((4 + G) ^ (c >> 1)) & 7) * 16;
        ldscp bP[2][2];
#pragma unroll
        for (int n = 0; n < 2; ++n)
#pragma unroll
            for (int hf = 0; hf < 2; ++hf) bP[n][hf] = lds + G * 2048 + (wc >> 1) * 1024 + wgt_rd(qq + 4 * hf, G & 1, 2 * (wc & 1) + n, pp);
        const unsigned voffB = voff_wgt(wid, lane, HDN);
        const unsigned voffA = voff_act(wid, lane, DM);
        const char* gA0 = (const char*)P.x + (size_t)(128 * xa) * DM * 2;
        const char* gA1 = (const char*)P.x + (size_t)(128 * (15 - xa)) * DM * 2;
        const char* gB = (const char*)P.wq + (size_t)h * HD * 2;
#define STA(so, hh, t) do { const char* g_ = ((hh) ? gA1 : gA0) + (size_t)(t) * 128; dma16(ldsb + (so) + (hh) * HT, g_, voffA); dma16(ldsb + (so) + (hh) * HT + 8192, g_ + 64 * DM * 2, voffA); } while (0)
#define STB(so, t) do { const char* g_ = gB + (size_t)(t) * 64 * HDN * 2; dma16(ldsb + (so) + 2 * HT, g_, voffB); dma16(ldsb + (so) + 2 * HT + 8192, g_ + 32 * HDN * 2, voffB); } while (0)
#define LDA(dst, so, hh) do { _Pragma("unroll") for (int m = 0; m < 4; ++m) { dst[m][0] = ld128(aP0 + (so) + (hh) * HT + m * 2048); dst[m][1] = ld128(aP1 + (so) + (hh) * HT + m * 2048); } } while (0)
#define LDB(dst, so) do { _Pragma("unroll") for (int n = 0; n < 2; ++n) _Pragma("unroll") for (int k = 0; k < 2; ++k) dst[n][k] = ldtr2(bP[n][0] + 2 * HT + (so) + k * 8192, bP[n][1] + 2 * HT + (so) + k * 8192); } while (0)
#define MMA(ai, At, Bt) do { __builtin_amdgcn_s_setprio(1); _Pragma("unroll") for (int m = 0; m < 4; ++m) _Pragma("unroll") for (int n = 0; n < 2; ++n) _Pragma("unroll") for (int k = 0; k < 2; ++k) \
        acc[ai][m][n] = MFMA(Bt[n][k], At[m][k], acc[ai][m][n]); __builtin_amdgcn_s_setprio(0); } while (0)
        f32x4 acc[2][4][2];
#pragma unroll
        for (int a = 0; a < 2; ++a)
#pragma unroll
            for (int m = 0; m < 4; ++m)
#pragma unroll
                for (int n = 0; n < 2; ++n) acc[a][m][n] = (f32x4){0.f, 0.f, 0.f, 0.f};
        bf16x8 At[4][2], Bt[2][2];
        constexpr int NT = DM / 64;
        STB(0, 1); STA(0, 0, 1); STA(0, 1, 1);
        if (wr == 1) BAR();
        const char* gK0 = P.ws + OFF_KF + (size_t)h * 512 * 1024 + wid * 1024;
        const char* gV0 = P.ws + OFF_VF + (size_t)h * 512 * 1024 + wid * 1024;
#define QITER(t, NV, EXTRA) do { constexpr int s0 = (((t) + 2) % 3) * QSTG, s2 = (((t) + 4) % 3) * QSTG, t2 = (t) + 2; \
            LDB(Bt, s0); SBAR(); LDA(At, s0, 0); if ((t) < 14) STB(s2, t2); \
            BAR(); WAIT_L(0); MMA(0, At, Bt); BAR(); SBAR(); \
            LDA(At, s0, 1); \
            if ((t) < 14) { STA(s2, 0, t2); STA(s2, 1, t2); } \
            else { _Pragma("unroll") for (int q_ = 0; q_ < 4; ++q_) dma16c(ldsb + ((t) == 14 ? L_K : L_V) + q_ * 8192, ((t) == 14 ? gK0 : gV0) + q_ * 8192, lane * 16); } \
            if (((t) == 3 || (t) == 7) && wid == 0) WAIT_V(7); else if ((t) < 15) WAIT_V(NV); \
            EXTRA; \
            BAR(); WAIT_L(0); MMA(1, At, Bt); BAR(); SBAR(); STAMP(12 + (t)); } while (0)
        QITER(0, 6, (void)0); QITER(1, 6, (void)0);
        QITER(2, 6, if (tid == 0) __hip_atomic_store(sy + SY_KV + 32 * r + idx, tag, RLX_AGENT));
        QITER(3, 6, (void)0); QITER(4, 6, (void)0); QITER(5, 6, (void)0);
        QITER(6, 6, if (wid == 0) flags_fetch((unsigned)(uintptr_t)lds + L_FLG, (const void*)(sy + SY_KV + 32 * r), lane));
        QITER(7, 6, (void)0);
        QITER(8, 6, if (wid == 0) flags_check(lds + L_FLG, idx & ~7, sy + SY_KV + 32 * r, 8, tag, lane));
        QITER(9, 6, (void)0);
        QITER(10, 6, flags_fetch((unsigned)(uintptr_t)lds + L_KVF + wid * 256, (const void*)(sy + SY_KV + 32 * wid), lane));
        QITER(11, 7, (void)0);
        QITER(12, 6, flags_check(lds + L_KVF + wid * 256, 0, sy + SY_KV + 32 * wid, 32, tag, lane));
        QITER(13, 6, (void)0);
        QITER(14, 4, (void)0);
        QITER(15, 0, (void)0);
#undef QITER
        if (wr == 0) BAR();
        STAMP(30);
#pragma unroll
        for (int ai = 0; ai < 2; ++ai)
#pragma unroll
            for (int m = 0; m < 4; ++m)
                *(LAS bf16x8*)(lds + (ai ? L_QB : L_QA) + ((4 * wr + m) * 4 + wc) * 1024 + lane * 16) = pack8(acc[ai][m][0] * QSCALE, acc[ai][m][1] * QSCALE);
        LBAR();
        STAMP(31);
#undef STA
#undef STB
#undef LDA
#undef LDB
#undef MMA
    }
    {
        const Params Q = fresh_params();
        gu64* sq = (gu64*)(Q.ws + OFF_SYNC);
        const int oks = r & 1, otb = 8 * ((r >> 1) & 1) + (idx >> 2);
        phase_attn(Q, lds, wid, h, xa, (const char*)Q.wo + ((size_t)(2048 * (r & 1) + 64 * idx + 8 * wid) * DM + 512 * (r >> 2)) * 2, sq + SY_CTX + h,
                   sq + SY_CTX + 32 * otb + 16 * oks + (otb >= 8 ? 0 : 1), tag STAMP_PASS);
    }
    STAMP(60);
    {
        const Params Q = fresh_params();
        gu64* sq = (gu64*)(Q.ws + OFF_SYNC);
        phase_oproj(Q, lds, wid, sq, tag, sq + SY_CTX + 32 * (15 - xa) + h STAMP_PASS);
        if (blockIdx.x == 0 && wid == 0) {
            const int ln = fresh_lane();
            for (int j = 0; j < 4; ++j) wait_flags(sq + SY_PREP + 64 * j, 64, tag, ln);
            if (ln == 0) __hip_atomic_store(sq, tag, RLX_AGENT);
        }
    }
    STAMP_END(P, 0);
}

extern "C" void kernel_launch(void* const* d_in, const int* in_sizes, int n_in, void* d_out, int out_size, void* d_ws, size_t ws_size, hipStream_t stream) {
    (void)in_sizes; (void)n_in; (void)out_size; (void)ws_size;
    Params P;
    P.x = (const u16*)d_in[0]; P.ckv = (const u16*)d_in[1]; P.wq = (const u16*)d_in[2]; P.wuk = (const u16*)d_in[3]; P.wuv = (const u16*)d_in[4]; P.wo = (const u16*)d_in[5];
    P.bk = (const u16*)d_in[7]; P.bv = (const u16*)d_in[8]; P.shk = (const u16*)d_in[9]; P.ssk = (const u16*)d_in[10]; P.ssd = (const u16*)d_in[11]; P.gw = (const u16*)d_in[12];
    P.out = (u16*)d_out; P.ws = (char*)d_ws;
    if (ws_size > WS_USED) P.ws += (ws_size - WS_USED) & ~(size_t)0xfffff;
    constexpr int LDS_MAX = 163840;
    (void)hipFuncSetAttribute((const void*)k_main, hipFuncAttributeMaxDynamicSharedMemorySize, LDS_MAX);
    k_main<<<256, 512, LDS_MAX, stream>>>(P);
}
```

```cpp
#include <hip/hip_runtime.h>
#include <stdint.h>

struct Params {
    const void *x, *ckv, *wq, *wuk, *wuv, *wo, *bk, *bv, *shk, *ssk, *ssd, *gw;
    void* out; char* ws;
};

__global__ __launch_bounds__(256, 1) void k_main(Params P) {
    asm volatile(
        "; ---- PRO\n"
        "s_load_dwordx16 s[8:23], %0, 0\n"
        "s_load_dwordx8 s[24:31], %0, 64\n"
        "s_load_dwordx4 s[36:39], %0, 96\n"
        "s_mov_b32 s55, %1\n"
        "s_mov_b32 s40, %2\n"
        "v_mbcnt_lo_u32_b32 v96, -1, 0\n"
        "v_mbcnt_hi_u32_b32 v96, -1, v96\n"
        "s_and_b32 s41, s55, 7\n"
        "s_lshr_b32 s42, s55, 3\n"
        "s_lshr_b32 s50, s42, 3\n"
        "s_lshl2_add_u32 s43, s41, s50\n"
        "s_and_b32 s44, s42, 7\n"
        "s_sub_u32 s45, 15, s44\n"
        "s_lshl_b32 s72, s40, 13\n"
        "s_lshl_b32 s73, s40, 12\n"
        "v_lshlrev_b32 v254, 4, v96\n"
        "v_lshrrev_b32 v98, 4, v96\n"
        "v_and_b32 v97, 15, v96\n"
        "v_lshrrev_b32 v100, 3, v96\n"
        "v_and_b32 v101, 7, v96\n"
        "v_xor_b32 v102, v101, v98\n"
        "v_lshlrev_b32 v102, 4, v102\n"
        "v_xor_b32 v103, 64, v102\n"
        "v_lshl_add_u32 v234, v100, 10, v102\n"
        "v_lshl_add_u32 v104, v100, 10, v103\n"
        "v_add_u32 v235, 7168, v104\n"
        "v_add_u32 v236, 14336, v234\n"
        "v_add_u32 v237, 21504, v104\n"
        "v_and_b32 v104, 6, v100\n"
        "v_xor_b32 v104, v104, v101\n"
        "v_lshlrev_b32 v104, 4, v104\n"
        "v_lshl_add_u32 v105, v100, 13, v104\n"
        "v_xor_b32 v106, 4, v100\n"
        "v_lshl_add_u32 v106, v106, 13, v104\n"
        "v_add_u32 v238, 4096, v105\n"
        "v_add_u32 v239, 3200, v105\n"
        "v_add_u32 v240, 67584, v106\n"
        "v_add_u32 v241, 66688, v106\n"
        "s_cmp_ge_u32 s40, 2\n"
        "s_cselect_b32 s50, s45, s44\n"
        "s_and_b32 s51, s40, 1\n"
        "s_lshl1_add_u32 s50, s50, s51\n"
        "s_lshl_b32 s50, s50, 16\n"
        "s_lshl_b32 s51, s40, 17\n"
        "s_lshl_b32 s52, s43, 8\n"
        "s_add_u32 s51, s51, s52\n"
        "s_waitcnt lgkmcnt(0)\n"
        "s_add_u32 s56, s10, s50\n"
        "s_addc_u32 s57, s11, 0\n"
        "s_add_u32 s58, s56, 0x8000\n"
        "s_addc_u32 s59, s57, 0\n"
        "s_add_u32 s60, s14, s51\n"
        "s_addc_u32 s61, s15, 0\n"
        "s_sub_u32 s60, s60, 4096\n"
        "s_subb_u32 s61, s61, 0\n"
        "s_add_u32 s62, s16, s51\n"
        "s_addc_u32 s63, s17, 0\n"
        "s_sub_u32 s62, s62, 4096\n"
        "s_subb_u32 s63, s63, 0\n"
        "s_add_u32 m0, s73, 0x10000\n"
        "s_nop 0\n"
        "global_load_lds_dwordx4 v238, s[60:61] offset:0\n"
        "global_load_lds_dwordx4 v239, s[60:61] offset:1024\n"
        "global_load_lds_dwordx4 v240, s[60:61] offset:2048\n"
        "global_load_lds_dwordx4 v241, s[60:61] offset:3072\n"
        "s_add_u32 m0, s73, 0x14000\n"
        "s_nop 0\n"
        "global_load_lds_dwordx4 v238, s[62:63] offset:0\n"
        "global_load_lds_dwordx4 v239, s[62:63] offset:1024\n"
        "global_load_lds_dwordx4 v240, s[62:63] offset:2048\n"
        "global_load_lds_dwordx4 v241, s[62:63] offset:3072\n"
        "s_add_u32 s60, s60, 0x80000\n"
        "s_addc_u32 s61, s61, 0\n"
        "s_add_u32 s62, s62, 0x80000\n"
        "s_addc_u32 s63, s63, 0\n"
        "s_add_u32 m0, s72, 0x0\n"
        "s_nop 0\n"
        "global_load_lds_dwordx4 v234, s[56:57] offset:0\n"
        "global_load_lds_dwordx4 v235, s[56:57] offset:1024\n"
        "global_load_lds_dwordx4 v236, s[56:57] offset:2048\n"
        "global_load_lds_dwordx4 v237, s[56:57] offset:3072\n"
        "s_add_u32 m0, s72, 0x1000\n"
        "s_nop 0\n"
        "global_load_lds_dwordx4 v234, s[58:59] offset:0\n"
        "global_load_lds_dwordx4 v235, s[58:59] offset:1024\n"
        "global_load_lds_dwordx4 v236, s[58:59] offset:2048\n"
        "global_load_lds_dwordx4 v237, s[58:59] offset:3072\n"
        "s_add_u32 s56, s56, 0x80\n"
        "s_addc_u32 s57, s57, 0\n"
        "s_add_u32 s58, s58, 0x80\n"
        "s_addc_u32 s59, s59, 0\n"
        "s_add_u32 s68, s12, s51\n"
        "s_addc_u32 s69, s13, 0\n"
        "s_sub_u32 s68, s68, 4096\n"
        "s_subb_u32 s69, s69, 0\n"
        "s_add_u32 s48, s38, 0x3900000\n"
        "s_addc_u32 s49, s39, 0\n"
        "v_mov_b32 v107, 0\n"
        "global_load_dwordx2 v[250:251], v107, s[48:49] sc1\n"
        "s_getpc_b64 s[74:75]\n"
        "LPC_%=:\n"
        "s_lshl2_add_u32 s50, s42, s40\n"
        "s_lshl_b32 s50, s50, 10\n"
        "v_lshlrev_b32 v108, 6, v97\n"
        "v_add_u32 v108, s50, v108\n"
        "v_min_u32 v108, LEND_%=-LPC_%=-4, v108\n"
        "global_load_dword v252, v108, s[74:75]\n"
        "s_add_u32 m0, s73, 0x18000\n"
        "s_nop 0\n"
        "global_load_lds_dwordx4 v238, s[60:61] offset:0\n"
        "global_load_lds_dwordx4 v239, s[60:61] offset:1024\n"
        "global_load_lds_dwordx4 v240, s[60:61] offset:2048\n"
        "global_load_lds_dwordx4 v241, s[60:61] offset:3072\n"
        "s_add_u32 m0, s73, 0x1c000\n"
        "s_nop 0\n"
        "global_load_lds_dwordx4 v238, s[62:63] offset:0\n"
        "global_load_lds_dwordx4 v239, s[62:63] offset:1024\n"
        "global_load_lds_dwordx4 v240, s[62:63] offset:2048\n"
        "global_load_lds_dwordx4 v241, s[62:63] offset:3072\n"
        "s_add_u32 s60, s60, 0x80000\n"
        "s_addc_u32 s61, s61, 0\n"
        "s_add_u32 s62, s62, 0x80000\n"
        "s_addc_u32 s63, s63, 0\n"
        "s_add_u32 m0, s72, 0x8000\n"
        "s_nop 0\n"
        "global_load_lds_dwordx4 v234, s[56:57] offset:0\n"
        "global_load_lds_dwordx4 v235, s[56:57] offset:1024\n"
        "global_load_lds_dwordx4 v236, s[56:57] offset:2048\n"
        "global_load_lds_dwordx4 v237, s[56:57] offset:3072\n"
        "s_add_u32 m0, s72, 0x9000\n"
        "s_nop 0\n"
        "global_load_lds_dwordx4 v234, s[58:59] offset:0\n"
        "global_load_lds_dwordx4 v235, s[58:59] offset:1024\n"
        "global_load_lds_dwordx4 v236, s[58:59] offset:2048\n"
        "global_load_lds_dwordx4 v237, s[58:59] offset:3072\n"
        "s_add_u32 s56, s56, 0x80\n"
        "s_addc_u32 s57, s57, 0\n"
        "s_add_u32 s58, s58, 0x80\n"
        "s_addc_u32 s59, s59, 0\n"
        "v_lshrrev_b32 v104, 1, v97\n"
        "v_xor_b32 v104, v104, v98\n"
        "v_lshlrev_b32 v104, 4, v104\n"
        "v_lshl_add_u32 v104, v97, 7, v104\n"
        "v_xor_b32 v105, 64, v104\n"
        "s_lshr_b32 s50, s40, 1\n"
        "s_lshl_b32 s50, s50, 14\n"
        "v_add_u32 v224, s50, v104\n"
        "v_add_u32 v246, s72, v104\n"
        "v_add_u32 v248, 0x10000, v246\n"
        "v_add_u32 v225, s50, v105\n"
        "v_add_u32 v247, s72, v105\n"
        "v_add_u32 v249, 0x10000, v247\n"
        "s_and_b32 s50, s40, 1\n"
        "s_lshl_b32 s50, s50, 10\n"
        "s_add_u32 s50, s50, 0x10000\n"
        "v_lshlrev_b32 v104, 11, v98\n"
        "v_add_u32 v104, s50, v104\n"
        "v_lshrrev_b32 v105, 2, v97\n"
        "v_or_b32 v106, 4, v105\n"
        "v_and_b32 v107, 1, v98\n"
        "v_and_b32 v109, 3, v96\n"
        "v_lshlrev_b32 v110, 2, v107\n"
        "v_xor_b32 v110, v110, v105\n"
        "v_bfe_u32 v111, v110, 1, 2\n"
        "v_xor_b32 v111, 0, v111\n"
        "v_lshlrev_b32 v111, 5, v111\n"
        "v_lshl_add_u32 v110, v110, 7, v111\n"
        "v_lshl_add_u32 v110, v109, 3, v110\n"
        "v_add_u32 v226, v110, v104\n"
        "v_xor_b32 v228, 32, v226\n"
        "v_xor_b32 v230, 64, v226\n"
        "v_xor_b32 v232, 96, v226\n"
        "v_lshlrev_b32 v110, 2, v107\n"
        "v_xor_b32 v110, v110, v106\n"
        "v_bfe_u32 v111, v110, 1, 2\n"
        "v_xor_b32 v111, 0, v111\n"
        "v_lshlrev_b32 v111, 5, v111\n"
        "v_lshl_add_u32 v110, v110, 7, v111\n"
        "v_lshl_add_u32 v110, v109, 3, v110\n"
        "v_add_u32 v227, v110, v104\n"
        "v_xor_b32 v229, 32, v227\n"
        "v_xor_b32 v231, 64, v227\n"
        "v_xor_b32 v233, 96, v227\n"
        "v_add_u32 v216, 0x10000, v226\n"
        "v_add_u32 v217, 0x10000, v227\n"
        "v_add_u32 v218, 0x10000, v228\n"
        "v_add_u32 v219, 0x10000, v229\n"
        "v_add_u32 v220, 0x10000, v230\n"
        "v_add_u32 v221, 0x10000, v231\n"
        "v_add_u32 v222, 0x10000, v232\n"
        "v_add_u32 v223, 0x10000, v233\n"
        "v_lshl_add_u32 v242, v100, 11, v102\n"
        "v_lshl_add_u32 v104, v100, 11, v103\n"
        "v_add_u32 v243, 15360, v104\n"
        "v_add_u32 v244, 129024, v242\n"
        "v_add_u32 v245, 144384, v104\n"
        "s_lshl_b32 s50, s45, 18\n"
        "s_lshl_b32 s51, s40, 15\n"
        "s_add_u32 s50, s50, s51\n"
        "s_add_u32 s64, s8, s50\n"
        "s_addc_u32 s65, s9, 0\n"
        "s_lshl_b32 s50, s44, 18\n"
        "s_add_u32 s50, s50, s51\n"
        "s_add_u32 s66, s8, s50\n"
        "s_addc_u32 s67, s9, 0\n"
        "s_lshl2_add_u32 s82, s55, s40\n"
        "s_lshl_b32 s82, s82, 11\n"
        "v_and_b32 v110, 0xf0, v254\n"
        "v_lshl_add_u32 v110, v110, 3, s82\n"
        "global_load_dword v253, v110, s[10:11] offset:0\n"
        "s_lshl2_add_u32 s82, s42, s40\n"
        "s_lshl3_add_u32 s82, s82, s41\n"
        "s_lshl_b32 s82, s82, 10\n"
        "v_and_b32 v111, 0x70, v254\n"
        "v_lshl_add_u32 v173, v111, 3, s82\n"
        "; ---- FRONT\n"
        "s_waitcnt vmcnt(19)\n"
        "s_barrier\n"
        "ds_read_b64_tr_b16 v[32:33], v226 offset:0\n"
        "ds_read_b64_tr_b16 v[34:35], v227 offset:0\n"
        "ds_read_b128 v[0:3], v224 offset:0\n"
        "ds_read_b64_tr_b16 v[36:37], v228 offset:0\n"
        "ds_read_b64_tr_b16 v[38:39], v229 offset:0\n"
        "ds_read_b64_tr_b16 v[40:41], v230 offset:0\n"
        "ds_read_b64_tr_b16 v[42:43], v231 offset:0\n"
        "ds_read_b64_tr_b16 v[44:45], v232 offset:0\n"
        "ds_read_b64_tr_b16 v[46:47], v233 offset:0\n"
        "ds_read_b64_tr_b16 v[48:49], v226 offset:16384\n"
        "ds_read_b64_tr_b16 v[50:51], v227 offset:16384\n"
        "ds_read_b64_tr_b16 v[52:53], v228 offset:16384\n"
        "ds_read_b64_tr_b16 v[54:55], v229 offset:16384\n"
        "ds_read_b64_tr_b16 v[56:57], v230 offset:16384\n"
        "ds_read_b64_tr_b16 v[58:59], v231 offset:16384\n"
        "ds_read_b64_tr_b16 v[60:61], v232 offset:16384\n"
        "ds_read_b64_tr_b16 v[62:63], v233 offset:16384\n"
        "s_add_u32 m0, s73, 0x20000\n"
        "s_waitcnt lgkmcnt(14)\n"
        "v_mfma_f32_16x16x32_bf16 a[0:3], v[32:35], v[0:3], 0\n"
        "ds_read_b128 v[4:7], v224 offset:2048\n"
        "s_waitcnt lgkmcnt(13)\n"
        "v_mfma_f32_16x16x32_bf16 a[4:7], v[36:39], v[0:3], 0\n"
        "ds_read_b128 v[8:11], v224 offset:4096\n"
        "s_waitcnt lgkmcnt(12)\n"
        "v_mfma_f32_16x16x32_bf16 a[8:11], v[40:43], v[0:3], 0\n"
        "ds_read_b128 v[12:15], v224 offset:6144\n"
        "s_waitcnt lgkmcnt(11)\n"
        "v_mfma_f32_16x16x32_bf16 a[12:15], v[44:47], v[0:3], 0\n"
        "ds_read_b128 v[16:19], v224 offset:8192\n"
        "s_waitcnt lgkmcnt(10)\n"
        "v_mfma_f32_16x16x32_bf16 a[16:19], v[0:3], v[48:51], 0\n"
        "ds_read_b128 v[20:23], v224 offset:10240\n"
        "s_waitcnt lgkmcnt(9)\n"
        "v_mfma_f32_16x16x32_bf16 a[20:23], v[0:3], v[52:55], 0\n"
        "ds_read_b128 v[24:27], v224 offset:12288\n"
        "s_waitcnt lgkmcnt(8)\n"
        "v_mfma_f32_16x16x32_bf16 a[24:27], v[0:3], v[56:59], 0\n"
        "ds_read_b128 v[28:31], v224 offset:14336\n"
        "s_waitcnt lgkmcnt(7)\n"
        "v_mfma_f32_16x16x32_bf16 a[28:31], v[0:3], v[60:63], 0\n"
        "global_load_lds_dwordx4 v238, s[60:61] offset:0\n"
        "ds_read_b128 v[0:3], v225 offset:0\n"
        "s_bfe_u32 s50, s43, 0x10003\n"
        "s_waitcnt lgkmcnt(7)\n"
        "v_mfma_f32_16x16x32_bf16 a[32:35], v[32:35], v[4:7], 0\n"
        "ds_read_b64_tr_b16 v[64:65], v226 offset:8192\n"
        "s_lshl_b32 s50, s50, 6\n"
        "s_lshr_b32 s51, s43, 4\n"
        "v_mfma_f32_16x16x32_bf16 a[36:39], v[36:39], v[4:7], 0\n"
        "ds_read_b64_tr_b16 v[66:67], v227 offset:8192\n"
        "s_lshl_b32 s51, s51, 5\n"
        "s_add_u32 s50, s50, s51\n"
        "v_mfma_f32_16x16x32_bf16 a[40:43], v[40:43], v[4:7], 0\n"
        "ds_read_b64_tr_b16 v[68:69], v228 offset:8192\n"
        "s_waitcnt lgkmcnt(8)\n"
        "v_mfma_f32_16x16x32_bf16 a[44:47], v[44:47], v[4:7], 0\n"
        "global_load_lds_dwordx4 v239, s[60:61] offset:1024\n"
        "ds_read_b64_tr_b16 v[70:71], v229 offset:8192\n"
        "s_and_b32 s51, s43, 7\n"
        "v_mfma_f32_16x16x32_bf16 a[48:51], v[4:7], v[48:51], 0\n"
        "ds_read_b64_tr_b16 v[72:73], v230 offset:8192\n"
        "s_lshl2_add_u32 s50, s51, s50\n"
        "s_add_u32 s50, s50, s40\n"
        "v_mfma_f32_16x16x32_bf16 a[52:55], v[4:7], v[52:55], 0\n"
        "ds_read_b64_tr_b16 v[74:75], v231 offset:8192\n"
        "s_lshl_b32 s51, s44, 7\n"
        "s_add_u32 s80, s51, s50\n"
        "v_mfma_f32_16x16x32_bf16 a[56:59], v[4:7], v[56:59], 0\n"
        "ds_read_b64_tr_b16 v[76:77], v232 offset:8192\n"
        "s_lshl_b32 s51, s80, 11\n"
        "v_mfma_f32_16x16x32_bf16 a[60:63], v[4:7], v[60:63], 0\n"
        "global_load_lds_dwordx4 v240, s[60:61] offset:2048\n"
        "ds_read_b128 v[4:7], v225 offset:2048\n"
        "v_mfma_f32_16x16x32_bf16 a[64:67], v[32:35], v[8:11], 0\n"
        "ds_read_b64_tr_b16 v[78:79], v233 offset:8192\n"
        "s_add_u32 s74, s8, s51\n"
        "s_addc_u32 s75, s9, 0\n"
        "v_mfma_f32_16x16x32_bf16 a[68:71], v[36:39], v[8:11], 0\n"
        "ds_read_b64_tr_b16 v[80:81], v226 offset:24576\n"
        "s_lshl_b32 s51, s45, 7\n"
        "s_add_u32 s81, s51, s50\n"
        "v_mfma_f32_16x16x32_bf16 a[72:75], v[40:43], v[8:11], 0\n"
        "ds_read_b64_tr_b16 v[82:83], v227 offset:24576\n"
        "s_lshl_b32 s51, s81, 11\n"
        "s_lshl_b32 s50, s41, 7\n"
        "v_mfma_f32_16x16x32_bf16 a[76:79], v[44:47], v[8:11], 0\n"
        "global_load_lds_dwordx4 v241, s[60:61] offset:3072\n"
        "s_add_u32 m0, s73, 0x24000\n"
        "s_lshl2_add_u32 s50, s42, s50\n"
        "v_mfma_f32_16x16x32_bf16 a[80:83], v[8:11], v[48:51], 0\n"
        "ds_read_b64_tr_b16 v[84:85], v228 offset:24576\n"
        "s_add_u32 s60, s60, 0x80000\n"
        "s_addc_u32 s61, s61, 0\n"
        "v_mfma_f32_16x16x32_bf16 a[84:87], v[8:11], v[52:55], 0\n"
        "ds_read_b64_tr_b16 v[86:87], v229 offset:24576\n"
        "s_add_u32 s50, s50, s40\n"
        "s_lshl_b32 s50, s50, 1\n"
        "v_mfma_f32_16x16x32_bf16 a[88:91], v[8:11], v[56:59], 0\n"
        "ds_read_b64_tr_b16 v[88:89], v230 offset:24576\n"
        "s_lshl_b32 s52, s40, 5\n"
        "v_mov_b32 v172, s50\n"
        "v_mfma_f32_16x16x32_bf16 a[92:95], v[8:11], v[60:63], 0\n"
        "global_load_lds_dwordx4 v238, s[62:63] offset:0\n"
        "ds_read_b128 v[8:11], v225 offset:4096\n"
        "s_lshl_b32 s50, s42, 6\n"
        "v_mfma_f32_16x16x32_bf16 a[96:99], v[32:35], v[12:15], 0\n"
        "ds_read_b64_tr_b16 v[90:91], v231 offset:24576\n"
        "s_lshl_b32 s80, s80, 7\n"
        "v_lshrrev_b32 v166, 4, v254\n"
        "v_mfma_f32_16x16x32_bf16 a[100:103], v[36:39], v[12:15], 0\n"
        "ds_read_b64_tr_b16 v[92:93], v232 offset:24576\n"
        "s_lshl_b32 s81, s81, 7\n"
        "v_lshrrev_b32 v167, 3, v166\n"
        "v_mfma_f32_16x16x32_bf16 a[104:107], v[40:43], v[12:15], 0\n"
        "ds_read_b64_tr_b16 v[94:95], v233 offset:24576\n"
        "v_and_b32 v168, 7, v166\n"
        "v_mfma_f32_16x16x32_bf16 a[108:111], v[44:47], v[12:15], 0\n"
        "global_load_lds_dwordx4 v239, s[62:63] offset:1024\n"
        "v_lshl_add_u32 v167, v167, 3, s50\n"
        "v_mfma_f32_16x16x32_bf16 a[112:115], v[12:15], v[48:51], 0\n"
        "s_waitcnt lgkmcnt(6)\n"
        "v_lshlrev_b32 v169, 11, v167\n"
        "v_cmp_eq_u32 s[78:79], 0, v167\n"
        "v_mfma_f32_16x16x32_bf16 a[116:119], v[12:15], v[52:55], 0\n"
        "s_add_u32 s82, s12, 0x0\n"
        "s_addc_u32 s83, s13, 0\n"
        "v_mov_b32 v171, 6144\n"
        "v_mfma_f32_16x16x32_bf16 a[120:123], v[12:15], v[56:59], 0\n"
        "s_add_u32 s76, s38, 0x3800000\n"
        "s_addc_u32 s77, s39, 0\n"
        "v_cndmask_b32 v170, 0, v171, s[78:79]\n"
        "v_mfma_f32_16x16x32_bf16 a[124:127], v[12:15], v[60:63], 0\n"
        "global_load_dwordx4 v[142:145], v254, s[74:75] offset:0\n"
        "ds_read_b128 v[12:15], v225 offset:6144\n"
        "v_mfma_f32_16x16x32_bf16 a[128:131], v[32:35], v[16:19], 0\n"
        "v_sub_u32 v171, 2040, v167\n"
        "v_lshlrev_b32 v171, 1, v171\n"
        "v_mfma_f32_16x16x32_bf16 a[132:135], v[36:39], v[16:19], 0\n"
        "v_mfma_f32_16x16x32_bf16 a[136:139], v[40:43], v[16:19], 0\n"
        "v_mfma_f32_16x16x32_bf16 a[140:143], v[44:47], v[16:19], 0\n"
        "global_load_lds_dwordx4 v240, s[62:63] offset:2048\n"
        "v_mfma_f32_16x16x32_bf16 a[144:147], v[16:19], v[48:51], 0\n"
        "v_mfma_f32_16x16x32_bf16 a[148:151], v[16:19], v[52:55], 0\n"
        "v_mfma_f32_16x16x32_bf16 a[152:155], v[16:19], v[56:59], 0\n"
        "v_mfma_f32_16x16x32_bf16 a[156:159], v[16:19], v[60:63], 0\n"
        "global_load_lds_dwordx4 v241, s[62:63] offset:3072\n"
        "s_add_u32 s62, s62, 0x80000\n"
        "s_addc_u32 s63, s63, 0\n"
        "v_mfma_f32_16x16x32_bf16 a[160:163], v[32:35], v[20:23], 0\n"
        "ds_read_b128 v[16:19], v225 offset:8192\n"
        "s_add_u32 m0, s72, 0x0\n"
        "v_mfma_f32_16x16x32_bf16 a[164:167], v[36:39], v[20:23], 0\n"
        "v_mfma_f32_16x16x32_bf16 a[168:171], v[40:43], v[20:23], 0\n"
        "v_mfma_f32_16x16x32_bf16 a[172:175], v[44:47], v[20:23], 0\n"
        "global_load_dwordx4 v[146:149], v254, s[74:75] offset:1024\n"
        "s_add_u32 s74, s8, s51\n"
        "s_addc_u32 s75, s9, 0\n"
        "v_mfma_f32_16x16x32_bf16 a[176:179], v[20:23], v[48:51], 0\n"
        "s_lshl_b32 s51, s41, 7\n"
        "s_add_u32 s51, s51, s52\n"
        "v_lshl_add_u32 v168, v168, 2, s51\n"
        "v_mfma_f32_16x16x32_bf16 a[180:183], v[20:23], v[52:55], 0\n"
        "v_lshl_add_u32 v169, v168, 1, v169\n"
        "v_add_u32 v170, v170, v169\n"
        "v_mfma_f32_16x16x32_bf16 a[184:187], v[20:23], v[56:59], 0\n"
        "v_lshl_add_u32 v171, v168, 12, v171\n"
        "v_lshlrev_b32 v168, 3, v168\n"
        "v_mfma_f32_16x16x32_bf16 a[188:191], v[20:23], v[60:63], 0\n"
        "global_load_dwordx4 v[150:153], v254, s[74:75] offset:0\n"
        "ds_read_b128 v[20:23], v225 offset:10240\n"
        "v_mfma_f32_16x16x32_bf16 a[192:195], v[32:35], v[24:27], 0\n"
        "v_mfma_f32_16x16x32_bf16 a[196:199], v[36:39], v[24:27], 0\n"
        "v_mfma_f32_16x16x32_bf16 a[200:203], v[40:43], v[24:27], 0\n"
        "v_mfma_f32_16x16x32_bf16 a[204:207], v[44:47], v[24:27], 0\n"
        "global_load_dwordx4 v[154:157], v254, s[74:75] offset:1024\n"
        "s_sub_u32 s74, s20, 2048\n"
        "s_subb_u32 s75, s21, 0\n"
        "v_mfma_f32_16x16x32_bf16 a[208:211], v[24:27], v[48:51], 0\n"
        "v_mfma_f32_16x16x32_bf16 a[212:215], v[24:27], v[52:55], 0\n"
        "v_mfma_f32_16x16x32_bf16 a[216:219], v[24:27], v[56:59], 0\n"
        "v_mfma_f32_16x16x32_bf16 a[220:223], v[24:27], v[60:63], 0\n"
        "global_load_dwordx4 v[158:161], v254, s[30:31] offset:0\n"
        "ds_read_b128 v[24:27], v225 offset:12288\n"
        "v_mfma_f32_16x16x32_bf16 a[224:227], v[32:35], v[28:31], 0\n"
        "v_mfma_f32_16x16x32_bf16 a[228:231], v[36:39], v[28:31], 0\n"
        "v_mfma_f32_16x16x32_bf16 a[232:235], v[40:43], v[28:31], 0\n"
        "v_mfma_f32_16x16x32_bf16 a[236:239], v[44:47], v[28:31], 0\n"
        "global_load_dwordx4 v[162:165], v254, s[30:31] offset:1024\n"
        "v_mfma_f32_16x16x32_bf16 a[240:243], v[28:31], v[48:51], 0\n"
        "v_mfma_f32_16x16x32_bf16 a[244:247], v[28:31], v[52:55], 0\n"
        "v_mfma_f32_16x16x32_bf16 a[248:251], v[28:31], v[56:59], 0\n"
        "v_mfma_f32_16x16x32_bf16 a[252:255], v[28:31], v[60:63], 0\n"
        "ds_read_b128 v[28:31], v225 offset:14336\n"
        "global_load_ushort v172, v172, s[28:29]\n"
        "v_mfma_f32_16x16x32_bf16 a[0:3], v[64:67], v[0:3], a[0:3]\n"
        "s_waitcnt lgkmcnt(1)\n"
        "s_waitcnt vmcnt(33)\n"
        "v_readfirstlane_b32 s46, v250\n"
        "v_readfirstlane_b32 s47, v251\n"
        "v_mfma_f32_16x16x32_bf16 a[4:7], v[68:71], v[0:3], a[4:7]\n"
        "s_add_u32 s46, s46, 0x1\n"
        "s_addc_u32 s47, s47, 0\n"
        "v_mov_b32 v250, s46\n"
        "v_mfma_f32_16x16x32_bf16 a[8:11], v[72:75], v[0:3], a[8:11]\n"
        "v_mov_b32 v251, s47\n"
        "v_mfma_f32_16x16x32_bf16 a[12:15], v[76:79], v[0:3], a[12:15]\n"
        "v_mfma_f32_16x16x32_bf16 a[16:19], v[0:3], v[80:83], a[16:19]\n"
        "v_mfma_f32_16x16x32_bf16 a[20:23], v[0:3], v[84:87], a[20:23]\n"
        "v_mfma_f32_16x16x32_bf16 a[24:27], v[0:3], v[88:91], a[24:27]\n"
        "v_mfma_f32_16x16x32_bf16 a[28:31], v[0:3], v[92:95], a[28:31]\n"
        "s_waitcnt vmcnt(16) lgkmcnt(0)\n"
        "s_barrier\n"
        "v_mfma_f32_16x16x32_bf16 a[32:35], v[64:67], v[4:7], a[32:35]\n"
        "global_load_lds_dwordx4 v234, s[56:57] offset:0\n"
        "ds_read_b128 v[0:3], v224 offset:32768\n"
        "v_mfma_f32_16x16x32_bf16 a[36:39], v[68:71], v[4:7], a[36:39]\n"
        "ds_read_b64_tr_b16 v[32:33], v226 offset:32768\n"
        "v_mfma_f32_16x16x32_bf16 a[40:43], v[72:75], v[4:7], a[40:43]\n"
        "ds_read_b64_tr_b16 v[34:35], v227 offset:32768\n"
        "v_mfma_f32_16x16x32_bf16 a[44:47], v[76:79], v[4:7], a[44:47]\n"
        "ds_read_b64_tr_b16 v[36:37], v228 offset:32768\n"
        "v_mfma_f32_16x16x32_bf16 a[48:51], v[4:7], v[80:83], a[48:51]\n"
        "ds_read_b64_tr_b16 v[38:39], v229 offset:32768\n"
        "global_load_lds_dwordx4 v235, s[56:57] offset:1024\n"
        "v_mfma_f32_16x16x32_bf16 a[52:55], v[4:7], v[84:87], a[52:55]\n"
        "ds_read_b64_tr_b16 v[40:41], v230 offset:32768\n"
        "v_mfma_f32_16x16x32_bf16 a[56:59], v[4:7], v[88:91], a[56:59]\n"
        "ds_read_b64_tr_b16 v[42:43], v231 offset:32768\n"
        "v_mfma_f32_16x16x32_bf16 a[60:63], v[4:7], v[92:95], a[60:63]\n"
        "ds_read_b128 v[4:7], v224 offset:34816\n"
        "v_mfma_f32_16x16x32_bf16 a[64:67], v[64:67], v[8:11], a[64:67]\n"
        "ds_read_b64_tr_b16 v[44:45], v232 offset:32768\n"
        "global_load_lds_dwordx4 v236, s[56:57] offset:2048\n"
        "v_mfma_f32_16x16x32_bf16 a[68:71], v[68:71], v[8:11], a[68:71]\n"
        "ds_read_b64_tr_b16 v[46:47], v233 offset:32768\n"
        "v_mfma_f32_16x16x32_bf16 a[72:75], v[72:75], v[8:11], a[72:75]\n"
        "ds_read_b64_tr_b16 v[48:49], v226 offset:49152\n"
        "v_mfma_f32_16x16x32_bf16 a[76:79], v[76:79], v[8:11], a[76:79]\n"
        "ds_read_b64_tr_b16 v[50:51], v227 offset:49152\n"
        "v_mfma_f32_16x16x32_bf16 a[80:83], v[8:11], v[80:83], a[80:83]\n"
        "global_load_lds_dwordx4 v237, s[56:57] offset:3072\n"
        "s_add_u32 m0, s72, 0x1000\n"
        "v_mfma_f32_16x16x32_bf16 a[84:87], v[8:11], v[84:87], a[84:87]\n"
        "ds_read_b64_tr_b16 v[52:53], v228 offset:49152\n"
        "s_add_u32 s56, s56, 0x80\n"
        "s_addc_u32 s57, s57, 0\n"
        "v_mfma_f32_16x16x32_bf16 a[88:91], v[8:11], v[88:91], a[88:91]\n"
        "ds_read_b64_tr_b16 v[54:55], v229 offset:49152\n"
        "v_mfma_f32_16x16x32_bf16 a[92:95], v[8:11], v[92:95], a[92:95]\n"
        "ds_read_b128 v[8:11], v224 offset:36864\n"
        "v_mfma_f32_16x16x32_bf16 a[96:99], v[64:67], v[12:15], a[96:99]\n"
        "global_load_lds_dwordx4 v234, s[58:59] offset:0\n"
        "v_mfma_f32_16x16x32_bf16 a[100:103], v[68:71], v[12:15], a[100:103]\n"
        "ds_read_b64_tr_b16 v[56:57], v230 offset:49152\n"
        "v_mfma_f32_16x16x32_bf16 a[104:107], v[72:75], v[12:15], a[104:107]\n"
        "ds_read_b64_tr_b16 v[58:59], v231 offset:49152\n"
        "v_mfma_f32_16x16x32_bf16 a[108:111], v[76:79], v[12:15], a[108:111]\n"
        "ds_read_b64_tr_b16 v[60:61], v232 offset:49152\n"
        "v_mfma_f32_16x16x32_bf16 a[112:115], v[12:15], v[80:83], a[112:115]\n"
        "global_load_lds_dwordx4 v235, s[58:59] offset:1024\n"
        "v_mfma_f32_16x16x32_bf16 a[116:119], v[12:15], v[84:87], a[116:119]\n"
        "ds_read_b64_tr_b16 v[62:63], v233 offset:49152\n"
        "v_mfma_f32_16x16x32_bf16 a[120:123], v[12:15], v[88:91], a[120:123]\n"
        "v_mfma_f32_16x16x32_bf16 a[124:127], v[12:15], v[92:95], a[124:127]\n"
        "ds_read_b128 v[12:15], v224 offset:38912\n"
        "v_mfma_f32_16x16x32_bf16 a[128:131], v[64:67], v[16:19], a[128:131]\n"
        "global_load_lds_dwordx4 v236, s[58:59] offset:2048\n"
        "v_mfma_f32_16x16x32_bf16 a[132:135], v[68:71], v[16:19], a[132:135]\n"
        "v_mfma_f32_16x16x32_bf16 a[136:139], v[72:75], v[16:19], a[136:139]\n"
        "v_mfma_f32_16x16x32_bf16 a[140:143], v[76:79], v[16:19], a[140:143]\n"
        "v_mfma_f32_16x16x32_bf16 a[144:147], v[16:19], v[80:83], a[144:147]\n"
        "global_load_dwordx4 v[134:137], v168, s[24:25] offset:0\n"
        "v_mfma_f32_16x16x32_bf16 a[148:151], v[16:19], v[84:87], a[148:151]\n"
        "v_mfma_f32_16x16x32_bf16 a[152:155], v[16:19], v[88:91], a[152:155]\n"
        "v_mfma_f32_16x16x32_bf16 a[156:159], v[16:19], v[92:95], a[156:159]\n"
        "ds_read_b128 v[16:19], v224 offset:40960\n"
        "v_mfma_f32_16x16x32_bf16 a[160:163], v[64:67], v[20:23], a[160:163]\n"
        "global_load_lds_dwordx4 v237, s[58:59] offset:3072\n"
        "s_add_u32 m0, s73, 0x10000\n"
        "v_mfma_f32_16x16x32_bf16 a[164:167], v[68:71], v[20:23], a[164:167]\n"
        "s_add_u32 s58, s58, 0x80\n"
        "s_addc_u32 s59, s59, 0\n"
        "v_mfma_f32_16x16x32_bf16 a[168:171], v[72:75], v[20:23], a[168:171]\n"
        "v_mfma_f32_16x16x32_bf16 a[172:175], v[76:79], v[20:23], a[172:175]\n"
        "v_mfma_f32_16x16x32_bf16 a[176:179], v[20:23], v[80:83], a[176:179]\n"
        "global_load_dwordx4 v[138:141], v168, s[24:25] offset:16\n"
        "v_mfma_f32_16x16x32_bf16 a[180:183], v[20:23], v[84:87], a[180:183]\n"
        "v_mfma_f32_16x16x32_bf16 a[184:187], v[20:23], v[88:91], a[184:187]\n"
        "v_mfma_f32_16x16x32_bf16 a[188:191], v[20:23], v[92:95], a[188:191]\n"
        "ds_read_b128 v[20:23], v224 offset:43008\n"
        "v_mfma_f32_16x16x32_bf16 a[192:195], v[64:67], v[24:27], a[192:195]\n"
        "global_load_lds_dwordx4 v238, s[60:61] offset:0\n"
        "v_mfma_f32_16x16x32_bf16 a[196:199], v[68:71], v[24:27], a[196:199]\n"
        "v_mfma_f32_16x16x32_bf16 a[200:203], v[72:75], v[24:27], a[200:203]\n"
        "v_mfma_f32_16x16x32_bf16 a[204:207], v[76:79], v[24:27], a[204:207]\n"
        "v_mfma_f32_16x16x32_bf16 a[208:211], v[24:27], v[80:83], a[208:211]\n"
        "global_load_lds_dwordx4 v239, s[60:61] offset:1024\n"
        "v_mfma_f32_16x16x32_bf16 a[212:215], v[24:27], v[84:87], a[212:215]\n"
        "v_mfma_f32_16x16x32_bf16 a[216:219], v[24:27], v[88:91], a[216:219]\n"
        "v_mfma_f32_16x16x32_bf16 a[220:223], v[24:27], v[92:95], a[220:223]\n"
        "ds_read_b128 v[24:27], v224 offset:45056\n"
        "v_mfma_f32_16x16x32_bf16 a[224:227], v[64:67], v[28:31], a[224:227]\n"
        "global_load_dwordx2 v[96:97], v170, s[74:75] offset:-4096\n"
        "v_mfma_f32_16x16x32_bf16 a[228:231], v[68:71], v[28:31], a[228:231]\n"
        "global_load_lds_dwordx4 v240, s[60:61] offset:2048\n"
        "ds_read_b64_tr_b16 v[64:65], v226 offset:40960\n"
        "v_mfma_f32_16x16x32_bf16 a[232:235], v[72:75], v[28:31], a[232:235]\n"
        "ds_read_b64_tr_b16 v[66:67], v227 offset:40960\n"
        "s_waitcnt lgkmcnt(3)\n"
        "v_mfma_f32_16x16x32_bf16 a[236:239], v[76:79], v[28:31], a[236:239]\n"
        "ds_read_b64_tr_b16 v[68:69], v228 offset:40960\n"
        "v_mfma_f32_16x16x32_bf16 a[240:243], v[28:31], v[80:83], a[240:243]\n"
        "ds_read_b64_tr_b16 v[70:71], v229 offset:40960\n"
        "v_mfma_f32_16x16x32_bf16 a[244:247], v[28:31], v[84:87], a[244:247]\n"
        "global_load_lds_dwordx4 v241, s[60:61] offset:3072\n"
        "s_add_u32 m0, s73, 0x14000\n"
        "v_mfma_f32_16x16x32_bf16 a[248:251], v[28:31], v[88:91], a[248:251]\n"
        "s_add_u32 s60, s60, 0x80000\n"
        "s_addc_u32 s61, s61, 0\n"
        "v_mfma_f32_16x16x32_bf16 a[252:255], v[28:31], v[92:95], a[252:255]\n"
        "ds_read_b128 v[28:31], v224 offset:47104\n"
        "v_mfma_f32_16x16x32_bf16 a[0:3], v[32:35], v[0:3], a[0:3]\n"
        "ds_read_b64_tr_b16 v[72:73], v230 offset:40960\n"
        "v_mfma_f32_16x16x32_bf16 a[4:7], v[36:39], v[0:3], a[4:7]\n"
        "global_load_dwordx2 v[98:99], v170, s[74:75] offset:-2048\n"
        "ds_read_b64_tr_b16 v[74:75], v231 offset:40960\n"
        "v_mfma_f32_16x16x32_bf16 a[8:11], v[40:43], v[0:3], a[8:11]\n"
        "global_load_lds_dwordx4 v238, s[62:63] offset:0\n"
        "ds_read_b64_tr_b16 v[76:77], v232 offset:40960\n"
        "v_mfma_f32_16x16x32_bf16 a[12:15], v[44:47], v[0:3], a[12:15]\n"
        "ds_read_b64_tr_b16 v[78:79], v233 offset:40960\n"
        "v_mfma_f32_16x16x32_bf16 a[16:19], v[0:3], v[48:51], a[16:19]\n"
        "ds_read_b64_tr_b16 v[80:81], v226 offset:57344\n"
        "v_mfma_f32_16x16x32_bf16 a[20:23], v[0:3], v[52:55], a[20:23]\n"
        "ds_read_b64_tr_b16 v[82:83], v227 offset:57344\n"
        "v_mfma_f32_16x16x32_bf16 a[24:27], v[0:3], v[56:59], a[24:27]\n"
        "global_load_lds_dwordx4 v239, s[62:63] offset:1024\n"
        "ds_read_b64_tr_b16 v[84:85], v228 offset:57344\n"
        "v_mfma_f32_16x16x32_bf16 a[28:31], v[0:3], v[60:63], a[28:31]\n"
        "ds_read_b128 v[0:3], v225 offset:32768\n"
        "v_mfma_f32_16x16x32_bf16 a[32:35], v[32:35], v[4:7], a[32:35]\n"
        "ds_read_b64_tr_b16 v[86:87], v229 offset:57344\n"
        "v_mfma_f32_16x16x32_bf16 a[36:39], v[36:39], v[4:7], a[36:39]\n"
        "ds_read_b64_tr_b16 v[88:89], v230 offset:57344\n"
        "v_mfma_f32_16x16x32_bf16 a[40:43], v[40:43], v[4:7], a[40:43]\n"
        "global_load_dwordx2 v[100:101], v170, s[74:75] offset:0\n"
        "ds_read_b64_tr_b16 v[90:91], v231 offset:57344\n"
        "v_mfma_f32_16x16x32_bf16 a[44:47], v[44:47], v[4:7], a[44:47]\n"
        "global_load_lds_dwordx4 v240, s[62:63] offset:2048\n"
        "ds_read_b64_tr_b16 v[92:93], v232 offset:57344\n"
        "v_mfma_f32_16x16x32_bf16 a[48:51], v[4:7], v[48:51], a[48:51]\n"
        "ds_read_b64_tr_b16 v[94:95], v233 offset:57344\n"
        "v_mfma_f32_16x16x32_bf16 a[52:55], v[4:7], v[52:55], a[52:55]\n"
        "v_mfma_f32_16x16x32_bf16 a[56:59], v[4:7], v[56:59], a[56:59]\n"
        "v_mfma_f32_16x16x32_bf16 a[60:63], v[4:7], v[60:63], a[60:63]\n"
        "global_load_lds_dwordx4 v241, s[62:63] offset:3072\n"
        "s_add_u32 s62, s62, 0x80000\n"
        "s_addc_u32 s63, s63, 0\n"
        "v_mfma_f32_16x16x32_bf16 a[64:67], v[32:35], v[8:11], a[64:67]\n"
        "ds_read_b128 v[4:7], v225 offset:34816\n"
        "s_add_u32 m0, s72, 0x8000\n"
        "v_mfma_f32_16x16x32_bf16 a[68:71], v[36:39], v[8:11], a[68:71]\n"
        "v_mfma_f32_16x16x32_bf16 a[72:75], v[40:43], v[8:11], a[72:75]\n"
        "v_mfma_f32_16x16x32_bf16 a[76:79], v[44:47], v[8:11], a[76:79]\n"
        "global_load_dwordx2 v[102:103], v169, s[74:75] offset:2048\n"
        "s_add_u32 s74, s20, 0x1800\n"
        "s_addc_u32 s75, s21, 0\n"
        "v_mfma_f32_16x16x32_bf16 a[80:83], v[8:11], v[48:51], a[80:83]\n"
        "global_load_dwordx2 v[104:105], v169, s[74:75] offset:-4096\n"
        "v_mfma_f32_16x16x32_bf16 a[84:87], v[8:11], v[52:55], a[84:87]\n"
        "global_load_dwordx2 v[106:107], v169, s[74:75] offset:-2048\n"
        "v_mfma_f32_16x16x32_bf16 a[88:91], v[8:11], v[56:59], a[88:91]\n"
        "v_mfma_f32_16x16x32_bf16 a[92:95], v[8:11], v[60:63], a[92:95]\n"
        "ds_read_b128 v[8:11], v225 offset:36864\n"
        "global_load_dwordx2 v[108:109], v169, s[74:75] offset:0\n"
        "v_mfma_f32_16x16x32_bf16 a[96:99], v[32:35], v[12:15], a[96:99]\n"
        "v_mfma_f32_16x16x32_bf16 a[100:103], v[36:39], v[12:15], a[100:103]\n"
        "v_mfma_f32_16x16x32_bf16 a[104:107], v[40:43], v[12:15], a[104:107]\n"
        "v_mfma_f32_16x16x32_bf16 a[108:111], v[44:47], v[12:15], a[108:111]\n"
        "v_mfma_f32_16x16x32_bf16 a[112:115], v[12:15], v[48:51], a[112:115]\n"
        "v_mfma_f32_16x16x32_bf16 a[116:119], v[12:15], v[52:55], a[116:119]\n"
        "v_mfma_f32_16x16x32_bf16 a[120:123], v[12:15], v[56:59], a[120:123]\n"
        "v_mfma_f32_16x16x32_bf16 a[124:127], v[12:15], v[60:63], a[124:127]\n"
        "ds_read_b128 v[12:15], v225 offset:38912\n"
        "global_load_dwordx2 v[110:111], v169, s[74:75] offset:2048\n"
        "v_mfma_f32_16x16x32_bf16 a[128:131], v[32:35], v[16:19], a[128:131]\n"
        "s_add_u32 s74, s20, 0x3800\n"
        "s_addc_u32 s75, s21, 0\n"
        "v_mfma_f32_16x16x32_bf16 a[132:135], v[36:39], v[16:19], a[132:135]\n"
        "v_mfma_f32_16x16x32_bf16 a[136:139], v[40:43], v[16:19], a[136:139]\n"
        "v_mfma_f32_16x16x32_bf16 a[140:143], v[44:47], v[16:19], a[140:143]\n"
        "v_mfma_f32_16x16x32_bf16 a[144:147], v[16:19], v[48:51], a[144:147]\n"
        "v_mfma_f32_16x16x32_bf16 a[148:151], v[16:19], v[52:55], a[148:151]\n"
        "v_mfma_f32_16x16x32_bf16 a[152:155], v[16:19], v[56:59], a[152:155]\n"
        "v_mfma_f32_16x16x32_bf16 a[156:159], v[16:19], v[60:63], a[156:159]\n"
        "ds_read_b128 v[16:19], v225 offset:40960\n"
        "global_load_dwordx2 v[112:113], v169, s[74:75] offset:-4096\n"
        "v_mfma_f32_16x16x32_bf16 a[160:163], v[32:35], v[20:23], a[160:163]\n"
        "v_mfma_f32_16x16x32_bf16 a[164:167], v[36:39], v[20:23], a[164:167]\n"
        "v_mfma_f32_16x16x32_bf16 a[168:171], v[40:43], v[20:23], a[168:171]\n"
        "s_waitcnt lgkmcnt(1)\n"
        "v_mfma_f32_16x16x32_bf16 a[172:175], v[44:47], v[20:23], a[172:175]\n"
        "v_mfma_f32_16x16x32_bf16 a[176:179], v[20:23], v[48:51], a[176:179]\n"
        "v_mfma_f32_16x16x32_bf16 a[180:183], v[20:23], v[52:55], a[180:183]\n"
        "v_mfma_f32_16x16x32_bf16 a[184:187], v[20:23], v[56:59], a[184:187]\n"
        "v_mfma_f32_16x16x32_bf16 a[188:191], v[20:23], v[60:63], a[188:191]\n"
        "ds_read_b128 v[20:23], v225 offset:43008\n"
        "global_load_dwordx2 v[114:115], v169, s[74:75] offset:-2048\n"
        "v_mfma_f32_16x16x32_bf16 a[192:195], v[32:35], v[24:27], a[192:195]\n"
        "v_mfma_f32_16x16x32_bf16 a[196:199], v[36:39], v[24:27], a[196:199]\n"
        "v_mfma_f32_16x16x32_bf16 a[200:203], v[40:43], v[24:27], a[200:203]\n"
        "v_mfma_f32_16x16x32_bf16 a[204:207], v[44:47], v[24:27], a[204:207]\n"
        "v_mfma_f32_16x16x32_bf16 a[208:211], v[24:27], v[48:51], a[208:211]\n"
        "v_mfma_f32_16x16x32_bf16 a[212:215], v[24:27], v[52:55], a[212:215]\n"
        "v_mfma_f32_16x16x32_bf16 a[216:219], v[24:27], v[56:59], a[216:219]\n"
        "v_mfma_f32_16x16x32_bf16 a[220:223], v[24:27], v[60:63], a[220:223]\n"
        "ds_read_b128 v[24:27], v225 offset:45056\n"
        "global_load_dwordx2 v[116:117], v169, s[74:75] offset:0\n"
        "v_mfma_f32_16x16x32_bf16 a[224:227], v[32:35], v[28:31], a[224:227]\n"
        "s_add_u32 s74, s22, 0x1000\n"
        "s_addc_u32 s75, s23, 0\n"
        "v_mfma_f32_16x16x32_bf16 a[228:231], v[36:39], v[28:31], a[228:231]\n"
        "v_mfma_f32_16x16x32_bf16 a[232:235], v[40:43], v[28:31], a[232:235]\n"
        "v_mfma_f32_16x16x32_bf16 a[236:239], v[44:47], v[28:31], a[236:239]\n"
        "s_waitcnt vmcnt(37) lgkmcnt(1)\n"
        "v_lshlrev_b32 v192, 16, v142\n"
        "v_and_b32 v193, 0xffff0000, v142\n"
        "v_mfma_f32_16x16x32_bf16 a[240:243], v[28:31], v[48:51], a[240:243]\n"
        "v_lshlrev_b32 v194, 16, v143\n"
        "v_and_b32 v195, 0xffff0000, v143\n"
        "v_mfma_f32_16x16x32_bf16 a[244:247], v[28:31], v[52:55], a[244:247]\n"
        "v_mfma_f32_16x16x32_bf16 a[248:251], v[28:31], v[56:59], a[248:251]\n"
        "v_mfma_f32_16x16x32_bf16 a[252:255], v[28:31], v[60:63], a[252:255]\n"
        "ds_read_b128 v[28:31], v225 offset:47104\n"
        "global_load_dwordx2 v[118:119], v169, s[74:75] offset:-4096\n"
        "v_mfma_f32_16x16x32_bf16 a[0:3], v[64:67], v[0:3], a[0:3]\n"
        "v_mfma_f32_16x16x32_bf16 a[4:7], v[68:71], v[0:3], a[4:7]\n"
        "v_mfma_f32_16x16x32_bf16 a[8:11], v[72:75], v[0:3], a[8:11]\n"
        "v_mfma_f32_16x16x32_bf16 a[12:15], v[76:79], v[0:3], a[12:15]\n"
        "v_mfma_f32_16x16x32_bf16 a[16:19], v[0:3], v[80:83], a[16:19]\n"
        "v_mfma_f32_16x16x32_bf16 a[20:23], v[0:3], v[84:87], a[20:23]\n"
        "v_mfma_f32_16x16x32_bf16 a[24:27], v[0:3], v[88:91], a[24:27]\n"
        "v_mfma_f32_16x16x32_bf16 a[28:31], v[0:3], v[92:95], a[28:31]\n"
        "s_waitcnt vmcnt(21) lgkmcnt(0)\n"
        "s_barrier\n"
        "v_mfma_f32_16x16x32_bf16 a[32:35], v[64:67], v[4:7], a[32:35]\n"
        "global_load_lds_dwordx4 v234, s[56:57] offset:0\n"
        "ds_read_b128 v[0:3], v224 offset:0\n"
        "v_mfma_f32_16x16x32_bf16 a[36:39], v[68:71], v[4:7], a[36:39]\n"
        "ds_read_b64_tr_b16 v[32:33], v216 offset:0\n"
        "v_mfma_f32_16x16x32_bf16 a[40:43], v[72:75], v[4:7], a[40:43]\n"
        "ds_read_b64_tr_b16 v[34:35], v217 offset:0\n"
        "v_mfma_f32_16x16x32_bf16 a[44:47], v[76:79], v[4:7], a[44:47]\n"
        "ds_read_b64_tr_b16 v[36:37], v218 offset:0\n"
        "v_mfma_f32_16x16x32_bf16 a[48:51], v[4:7], v[80:83], a[48:51]\n"
        "ds_read_b64_tr_b16 v[38:39], v219 offset:0\n"
        "global_load_lds_dwordx4 v235, s[56:57] offset:1024\n"
        "v_mfma_f32_16x16x32_bf16 a[52:55], v[4:7], v[84:87], a[52:55]\n"
        "ds_read_b64_tr_b16 v[40:41], v220 offset:0\n"
        "v_mfma_f32_16x16x32_bf16 a[56:59], v[4:7], v[88:91], a[56:59]\n"
        "ds_read_b64_tr_b16 v[42:43], v221 offset:0\n"
        "v_mfma_f32_16x16x32_bf16 a[60:63], v[4:7], v[92:95], a[60:63]\n"
        "ds_read_b128 v[4:7], v224 offset:2048\n"
        "v_mfma_f32_16x16x32_bf16 a[64:67], v[64:67], v[8:11], a[64:67]\n"
        "global_load_dwordx2 v[120:121], v169, s[74:75] offset:-2048\n"
        "v_mfma_f32_16x16x32_bf16 a[68:71], v[68:71], v[8:11], a[68:71]\n"
        "ds_read_b64_tr_b16 v[44:45], v222 offset:0\n"
        "global_load_lds_dwordx4 v236, s[56:57] offset:2048\n"
        "v_mfma_f32_16x16x32_bf16 a[72:75], v[72:75], v[8:11], a[72:75]\n"
        "ds_read_b64_tr_b16 v[46:47], v223 offset:0\n"
        "v_mfma_f32_16x16x32_bf16 a[76:79], v[76:79], v[8:11], a[76:79]\n"
        "ds_read_b64_tr_b16 v[48:49], v216 offset:16384\n"
        "v_lshlrev_b32 v174, 16, v158\n"
        "v_mfma_f32_16x16x32_bf16 a[80:83], v[8:11], v[80:83], a[80:83]\n"
        "ds_read_b64_tr_b16 v[50:51], v217 offset:16384\n"
        "v_and_b32 v175, 0xffff0000, v158\n"
        "v_mfma_f32_16x16x32_bf16 a[84:87], v[8:11], v[84:87], a[84:87]\n"
        "global_load_lds_dwordx4 v237, s[56:57] offset:3072\n"
        "s_add_u32 m0, s72, 0x9000\n"
        "v_mfma_f32_16x16x32_bf16 a[88:91], v[8:11], v[88:91], a[88:91]\n"
        "ds_read_b64_tr_b16 v[52:53], v218 offset:16384\n"
        "s_add_u32 s56, s56, 0x80\n"
        "s_addc_u32 s57, s57, 0\n"
        "v_mfma_f32_16x16x32_bf16 a[92:95], v[8:11], v[92:95], a[92:95]\n"
        "ds_read_b128 v[8:11], v224 offset:4096\n"
        "v_lshlrev_b32 v176, 16, v159\n"
        "v_mfma_f32_16x16x32_bf16 a[96:99], v[64:67], v[12:15], a[96:99]\n"
        "ds_read_b64_tr_b16 v[54:55], v219 offset:16384\n"
        "v_and_b32 v177, 0xffff0000, v159\n"
        "v_mfma_f32_16x16x32_bf16 a[100:103], v[68:71], v[12:15], a[100:103]\n"
        "global_load_dwordx2 v[122:123], v169, s[74:75] offset:0\n"
        "v_mfma_f32_16x16x32_bf16 a[104:107], v[72:75], v[12:15], a[104:107]\n"
        "global_load_lds_dwordx4 v234, s[58:59] offset:0\n"
        "ds_read_b64_tr_b16 v[56:57], v220 offset:16384\n"
        "v_mfma_f32_16x16x32_bf16 a[108:111], v[76:79], v[12:15], a[108:111]\n"
        "ds_read_b64_tr_b16 v[58:59], v221 offset:16384\n"
        "v_readfirstlane_b32 s54, v172\n"
        "v_mfma_f32_16x16x32_bf16 a[112:115], v[12:15], v[80:83], a[112:115]\n"
        "ds_read_b64_tr_b16 v[60:61], v222 offset:16384\n"
        "s_lshl_b32 s54, s54, 16\n"
        "v_lshlrev_b32 v178, 16, v160\n"
        "v_mfma_f32_16x16x32_bf16 a[116:119], v[12:15], v[84:87], a[116:119]\n"
        "ds_read_b64_tr_b16 v[62:63], v223 offset:16384\n"
        "v_and_b32 v179, 0xffff0000, v160\n"
        "v_mfma_f32_16x16x32_bf16 a[120:123], v[12:15], v[88:91], a[120:123]\n"
        "global_load_lds_dwordx4 v235, s[58:59] offset:1024\n"
        "v_lshlrev_b32 v180, 16, v161\n"
        "v_mfma_f32_16x16x32_bf16 a[124:127], v[12:15], v[92:95], a[124:127]\n"
        "ds_read_b128 v[12:15], v224 offset:6144\n"
        "v_and_b32 v181, 0xffff0000, v161\n"
        "v_mfma_f32_16x16x32_bf16 a[128:131], v[64:67], v[16:19], a[128:131]\n"
        "v_lshlrev_b32 v182, 16, v162\n"
        "v_and_b32 v183, 0xffff0000, v162\n"
        "v_mfma_f32_16x16x32_bf16 a[132:135], v[68:71], v[16:19], a[132:135]\n"
        "v_lshlrev_b32 v184, 16, v163\n"
        "v_and_b32 v185, 0xffff0000, v163\n"
        "v_mfma_f32_16x16x32_bf16 a[136:139], v[72:75], v[16:19], a[136:139]\n"
        "global_load_dwordx2 v[124:125], v169, s[74:75] offset:2048\n"
        "s_add_u32 s74, s22, 0x3000\n"
        "s_addc_u32 s75, s23, 0\n"
        "v_mfma_f32_16x16x32_bf16 a[140:143], v[76:79], v[16:19], a[140:143]\n"
        "global_load_lds_dwordx4 v236, s[58:59] offset:2048\n"
        "v_lshlrev_b32 v186, 16, v164\n"
        "v_mfma_f32_16x16x32_bf16 a[144:147], v[16:19], v[80:83], a[144:147]\n"
        "v_and_b32 v187, 0xffff0000, v164\n"
        "v_lshlrev_b32 v188, 16, v165\n"
        "v_mfma_f32_16x16x32_bf16 a[148:151], v[16:19], v[84:87], a[148:151]\n"
        "v_and_b32 v189, 0xffff0000, v165\n"
        "v_mul_f32 v192, v192, v174\n"
        "v_mfma_f32_16x16x32_bf16 a[152:155], v[16:19], v[88:91], a[152:155]\n"
        "v_mul_f32 v193, v193, v175\n"
        "v_cvt_pk_bf16_f32 v192, 0, v192\n"
        "v_mfma_f32_16x16x32_bf16 a[156:159], v[16:19], v[92:95], a[156:159]\n"
        "global_load_lds_dwordx4 v237, s[58:59] offset:3072\n"
        "ds_read_b128 v[16:19], v224 offset:8192\n"
        "s_add_u32 m0, s73, 0x18000\n"
        "v_mfma_f32_16x16x32_bf16 a[160:163], v[64:67], v[20:23], a[160:163]\n"
        "s_add_u32 s58, s58, 0x80\n"
        "s_addc_u32 s59, s59, 0\n"
        "v_cvt_pk_bf16_f32 v193, 0, v193\n"
        "v_mfma_f32_16x16x32_bf16 a[164:167], v[68:71], v[20:23], a[164:167]\n"
        "v_add_f32 v190, v192, v193\n"
        "v_mul_f32 v194, v194, v176\n"
        "v_mfma_f32_16x16x32_bf16 a[168:171], v[72:75], v[20:23], a[168:171]\n"
        "v_mul_f32 v195, v195, v177\n"
        "v_cvt_pk_bf16_f32 v194, 0, v194\n"
        "v_mfma_f32_16x16x32_bf16 a[172:175], v[76:79], v[20:23], a[172:175]\n"
        "global_load_dwordx2 v[126:127], v169, s[74:75] offset:-4096\n"
        "v_cvt_pk_bf16_f32 v195, 0, v195\n"
        "v_mfma_f32_16x16x32_bf16 a[176:179], v[20:23], v[80:83], a[176:179]\n"
        "global_load_lds_dwordx4 v238, s[60:61] offset:0\n"
        "v_add_f32 v194, v194, v195\n"
        "v_mfma_f32_16x16x32_bf16 a[180:183], v[20:23], v[84:87], a[180:183]\n"
        "v_add_f32 v190, v190, v194\n"
        "v_lshlrev_b32 v192, 16, v144\n"
        "v_mfma_f32_16x16x32_bf16 a[184:187], v[20:23], v[88:91], a[184:187]\n"
        "v_and_b32 v193, 0xffff0000, v144\n"
        "v_mul_f32 v192, v192, v178\n"
        "v_mfma_f32_16x16x32_bf16 a[188:191], v[20:23], v[92:95], a[188:191]\n"
        "ds_read_b128 v[20:23], v224 offset:10240\n"
        "v_mul_f32 v193, v193, v179\n"
        "v_mfma_f32_16x16x32_bf16 a[192:195], v[64:67], v[24:27], a[192:195]\n"
        "global_load_lds_dwordx4 v239, s[60:61] offset:1024\n"
        "v_cvt_pk_bf16_f32 v192, 0, v192\n"
        "v_mfma_f32_16x16x32_bf16 a[196:199], v[68:71], v[24:27], a[196:199]\n"
        "v_cvt_pk_bf16_f32 v193, 0, v193\n"
        "v_add_f32 v192, v192, v193\n"
        "v_mfma_f32_16x16x32_bf16 a[200:203], v[72:75], v[24:27], a[200:203]\n"
        "v_add_f32 v190, v190, v192\n"
        "v_lshlrev_b32 v194, 16, v145\n"
        "v_mfma_f32_16x16x32_bf16 a[204:207], v[76:79], v[24:27], a[204:207]\n"
        "v_and_b32 v195, 0xffff0000, v145\n"
        "v_mul_f32 v194, v194, v180\n"
        "v_mfma_f32_16x16x32_bf16 a[208:211], v[24:27], v[80:83], a[208:211]\n"
        "global_load_dwordx2 v[128:129], v169, s[74:75] offset:-2048\n"
        "v_mul_f32 v195, v195, v181\n"
        "v_mfma_f32_16x16x32_bf16 a[212:215], v[24:27], v[84:87], a[212:215]\n"
        "global_load_lds_dwordx4 v240, s[60:61] offset:2048\n"
        "v_cvt_pk_bf16_f32 v194, 0, v194\n"
        "v_mfma_f32_16x16x32_bf16 a[216:219], v[24:27], v[88:91], a[216:219]\n"
        "v_cvt_pk_bf16_f32 v195, 0, v195\n"
        "v_add_f32 v194, v194, v195\n"
        "v_mfma_f32_16x16x32_bf16 a[220:223], v[24:27], v[92:95], a[220:223]\n"
        "ds_read_b128 v[24:27], v224 offset:12288\n"
        "v_add_f32 v190, v190, v194\n"
        "v_mfma_f32_16x16x32_bf16 a[224:227], v[64:67], v[28:31], a[224:227]\n"
        "ds_read_b64_tr_b16 v[64:65], v216 offset:8192\n"
        "v_lshlrev_b32 v192, 16, v146\n"
        "v_mfma_f32_16x16x32_bf16 a[228:231], v[68:71], v[28:31], a[228:231]\n"
        "global_load_lds_dwordx4 v241, s[60:61] offset:3072\n"
        "s_add_u32 m0, s73, 0x1c000\n"
        "v_and_b32 v193, 0xffff0000, v146\n"
        "v_mfma_f32_16x16x32_bf16 a[232:235], v[72:75], v[28:31], a[232:235]\n"
        "ds_read_b64_tr_b16 v[66:67], v217 offset:8192\n"
        "s_add_u32 s60, s60, 0x80000\n"
        "s_addc_u32 s61, s61, 0\n"
        "v_mfma_f32_16x16x32_bf16 a[236:239], v[76:79], v[28:31], a[236:239]\n"
        "ds_read_b64_tr_b16 v[68:69], v218 offset:8192\n"
        "v_mul_f32 v192, v192, v182\n"
        "v_mfma_f32_16x16x32_bf16 a[240:243], v[28:31], v[80:83], a[240:243]\n"
        "ds_read_b64_tr_b16 v[70:71], v219 offset:8192\n"
        "v_mul_f32 v193, v193, v183\n"
        "v_mfma_f32_16x16x32_bf16 a[244:247], v[28:31], v[84:87], a[244:247]\n"
        "global_load_dwordx2 v[130:131], v169, s[74:75] offset:0\n"
        "ds_read_b64_tr_b16 v[72:73], v220 offset:8192\n"
        "v_mfma_f32_16x16x32_bf16 a[248:251], v[28:31], v[88:91], a[248:251]\n"
        "global_load_lds_dwordx4 v238, s[62:63] offset:0\n"
        "ds_read_b64_tr_b16 v[74:75], v221 offset:8192\n"
        "v_mfma_f32_16x16x32_bf16 a[252:255], v[28:31], v[92:95], a[252:255]\n"
        "ds_read_b128 v[28:31], v224 offset:14336\n"
        "s_waitcnt lgkmcnt(7)\n"
        "v_mfma_f32_16x16x32_bf16 a[0:3], v[32:35], v[0:3], a[0:3]\n"
        "ds_read_b64_tr_b16 v[76:77], v222 offset:8192\n"
        "v_cvt_pk_bf16_f32 v192, 0, v192\n"
        "v_mfma_f32_16x16x32_bf16 a[4:7], v[36:39], v[0:3], a[4:7]\n"
        "ds_read_b64_tr_b16 v[78:79], v223 offset:8192\n"
        "v_cvt_pk_bf16_f32 v193, 0, v193\n"
        "v_mfma_f32_16x16x32_bf16 a[8:11], v[40:43], v[0:3], a[8:11]\n"
        "global_load_lds_dwordx4 v239, s[62:63] offset:1024\n"
        "ds_read_b64_tr_b16 v[80:81], v216 offset:24576\n"
        "v_mfma_f32_16x16x32_bf16 a[12:15], v[44:47], v[0:3], a[12:15]\n"
        "ds_read_b64_tr_b16 v[82:83], v217 offset:24576\n"
        "v_add_f32 v192, v192, v193\n"
        "v_mfma_f32_16x16x32_bf16 a[16:19], v[0:3], v[48:51], a[16:19]\n"
        "ds_read_b64_tr_b16 v[84:85], v218 offset:24576\n"
        "v_add_f32 v190, v190, v192\n"
        "v_mfma_f32_16x16x32_bf16 a[20:23], v[0:3], v[52:55], a[20:23]\n"
        "ds_read_b64_tr_b16 v[86:87], v219 offset:24576\n"
        "v_lshlrev_b32 v194, 16, v147\n"
        "v_mfma_f32_16x16x32_bf16 a[24:27], v[0:3], v[56:59], a[24:27]\n"
        "global_load_dwordx2 v[132:133], v169, s[74:75] offset:2048\n"
        "ds_read_b64_tr_b16 v[88:89], v220 offset:24576\n"
        "v_mfma_f32_16x16x32_bf16 a[28:31], v[0:3], v[60:63], a[28:31]\n"
        "global_load_lds_dwordx4 v240, s[62:63] offset:2048\n"
        "ds_read_b128 v[0:3], v225 offset:0\n"
        "v_mfma_f32_16x16x32_bf16 a[32:35], v[32:35], v[4:7], a[32:35]\n"
        "ds_read_b64_tr_b16 v[90:91], v221 offset:24576\n"
        "v_and_b32 v195, 0xffff0000, v147\n"
        "v_mfma_f32_16x16x32_bf16 a[36:39], v[36:39], v[4:7], a[36:39]\n"
        "ds_read_b64_tr_b16 v[92:93], v222 offset:24576\n"
        "v_mul_f32 v194, v194, v184\n"
        "v_mfma_f32_16x16x32_bf16 a[40:43], v[40:43], v[4:7], a[40:43]\n"
        "ds_read_b64_tr_b16 v[94:95], v223 offset:24576\n"
        "v_mul_f32 v195, v195, v185\n"
        "v_mfma_f32_16x16x32_bf16 a[44:47], v[44:47], v[4:7], a[44:47]\n"
        "global_load_lds_dwordx4 v241, s[62:63] offset:3072\n"
        "s_add_u32 s62, s62, 0x80000\n"
        "s_addc_u32 s63, s63, 0\n"
        "v_mfma_f32_16x16x32_bf16 a[48:51], v[4:7], v[48:51], a[48:51]\n"
        "s_add_u32 m0, s72, 0x0\n"
        "v_cvt_pk_bf16_f32 v194, 0, v194\n"
        "v_cvt_pk_bf16_f32 v195, 0, v195\n"
        "v_mfma_f32_16x16x32_bf16 a[52:55], v[4:7], v[52:55], a[52:55]\n"
        "v_add_f32 v194, v194, v195\n"
        "v_add_f32 v190, v190, v194\n"
        "v_mfma_f32_16x16x32_bf16 a[56:59], v[4:7], v[56:59], a[56:59]\n"
        "v_lshlrev_b32 v192, 16, v148\n"
        "v_and_b32 v193, 0xffff0000, v148\n"
        "v_mfma_f32_16x16x32_bf16 a[60:63], v[4:7], v[60:63], a[60:63]\n"
        "ds_read_b128 v[4:7], v225 offset:2048\n"
        "v_mul_f32 v192, v192, v186\n"
        "v_mfma_f32_16x16x32_bf16 a[64:67], v[32:35], v[8:11], a[64:67]\n"
        "v_mul_f32 v193, v193, v187\n"
        "v_cvt_pk_bf16_f32 v192, 0, v192\n"
        "v_mfma_f32_16x16x32_bf16 a[68:71], v[36:39], v[8:11], a[68:71]\n"
        "v_cvt_pk_bf16_f32 v193, 0, v193\n"
        "v_add_f32 v192, v192, v193\n"
        "v_mfma_f32_16x16x32_bf16 a[72:75], v[40:43], v[8:11], a[72:75]\n"
        "v_add_f32 v190, v190, v192\n"
        "v_lshlrev_b32 v194, 16, v149\n"
        "v_mfma_f32_16x16x32_bf16 a[76:79], v[44:47], v[8:11], a[76:79]\n"
        "v_and_b32 v195, 0xffff0000, v149\n"
        "v_mul_f32 v194, v194, v188\n"
        "v_mfma_f32_16x16x32_bf16 a[80:83], v[8:11], v[48:51], a[80:83]\n"
        "v_mul_f32 v195, v195, v189\n"
        "v_cvt_pk_bf16_f32 v194, 0, v194\n"
        "v_mfma_f32_16x16x32_bf16 a[84:87], v[8:11], v[52:55], a[84:87]\n"
        "s_waitcnt vmcnt(40) lgkmcnt(1)\n"
        "v_cvt_pk_bf16_f32 v195, 0, v195\n"
        "v_add_f32 v194, v194, v195\n"
        "v_mfma_f32_16x16x32_bf16 a[88:91], v[8:11], v[56:59], a[88:91]\n"
        "v_add_f32 v190, v190, v194\n"
        "v_lshlrev_b32 v192, 16, v150\n"
        "v_mfma_f32_16x16x32_bf16 a[92:95], v[8:11], v[60:63], a[92:95]\n"
        "ds_read_b128 v[8:11], v225 offset:4096\n"
        "v_and_b32 v193, 0xffff0000, v150\n"
        "v_mfma_f32_16x16x32_bf16 a[96:99], v[32:35], v[12:15], a[96:99]\n"
        "v_mul_f32 v192, v192, v174\n"
        "v_mul_f32 v193, v193, v175\n"
        "v_mfma_f32_16x16x32_bf16 a[100:103], v[36:39], v[12:15], a[100:103]\n"
        "v_cvt_pk_bf16_f32 v192, 0, v192\n"
        "v_cvt_pk_bf16_f32 v193, 0, v193\n"
        "v_mfma_f32_16x16x32_bf16 a[104:107], v[40:43], v[12:15], a[104:107]\n"
        "v_add_f32 v191, v192, v193\n"
        "v_lshlrev_b32 v194, 16, v151\n"
        "v_mfma_f32_16x16x32_bf16 a[108:111], v[44:47], v[12:15], a[108:111]\n"
        "v_and_b32 v195, 0xffff0000, v151\n"
        "v_mul_f32 v194, v194, v176\n"
        "v_mfma_f32_16x16x32_bf16 a[112:115], v[12:15], v[48:51], a[112:115]\n"
        "v_mul_f32 v195, v195, v177\n"
        "v_cvt_pk_bf16_f32 v194, 0, v194\n"
        "v_mfma_f32_16x16x32_bf16 a[116:119], v[12:15], v[52:55], a[116:119]\n"
        "s_waitcnt vmcnt(37) lgkmcnt(1)\n"
        "v_cvt_pk_bf16_f32 v195, 0, v195\n"
        "v_add_f32 v194, v194, v195\n"
        "v_mfma_f32_16x16x32_bf16 a[120:123], v[12:15], v[56:59], a[120:123]\n"
        "v_add_f32 v191, v191, v194\n"
        "v_lshlrev_b32 v192, 16, v152\n"
        "v_mfma_f32_16x16x32_bf16 a[124:127], v[12:15], v[60:63], a[124:127]\n"
        "ds_read_b128 v[12:15], v225 offset:6144\n"
        "v_and_b32 v193, 0xffff0000, v152\n"
        "v_mfma_f32_16x16x32_bf16 a[128:131], v[32:35], v[16:19], a[128:131]\n"
        "v_mul_f32 v192, v192, v178\n"
        "v_mul_f32 v193, v193, v179\n"
        "v_mfma_f32_16x16x32_bf16 a[132:135], v[36:39], v[16:19], a[132:135]\n"
        "v_cvt_pk_bf16_f32 v192, 0, v192\n"
        "v_cvt_pk_bf16_f32 v193, 0, v193\n"
        "v_mfma_f32_16x16x32_bf16 a[136:139], v[40:43], v[16:19], a[136:139]\n"
        "v_add_f32 v192, v192, v193\n"
        "v_add_f32 v191, v191, v192\n"
        "v_mfma_f32_16x16x32_bf16 a[140:143], v[44:47], v[16:19], a[140:143]\n"
        "v_lshlrev_b32 v194, 16, v153\n"
        "v_and_b32 v195, 0xffff0000, v153\n"
        "v_mfma_f32_16x16x32_bf16 a[144:147], v[16:19], v[48:51], a[144:147]\n"
        "v_mul_f32 v194, v194, v180\n"
        "v_mul_f32 v195, v195, v181\n"
        "v_mfma_f32_16x16x32_bf16 a[148:151], v[16:19], v[52:55], a[148:151]\n"
        "v_cvt_pk_bf16_f32 v194, 0, v194\n"
        "v_cvt_pk_bf16_f32 v195, 0, v195\n"
        "v_mfma_f32_16x16x32_bf16 a[152:155], v[16:19], v[56:59], a[152:155]\n"
        "s_waitcnt vmcnt(34) lgkmcnt(1)\n"
        "v_add_f32 v194, v194, v195\n"
        "v_add_f32 v191, v191, v194\n"
        "v_mfma_f32_16x16x32_bf16 a[156:159], v[16:19], v[60:63], a[156:159]\n"
        "ds_read_b128 v[16:19], v225 offset:8192\n"
        "global_load_dword v253, v173, s[82:83] offset:0\n"
        "v_mfma_f32_16x16x32_bf16 a[160:163], v[32:35], v[20:23], a[160:163]\n"
        "s_add_u32 s82, s12, 0x100000\n"
        "s_addc_u32 s83, s13, 0\n"
        "v_lshlrev_b32 v192, 16, v154\n"
        "v_mfma_f32_16x16x32_bf16 a[164:167], v[36:39], v[20:23], a[164:167]\n"
        "v_and_b32 v193, 0xffff0000, v154\n"
        "v_mul_f32 v192, v192, v182\n"
        "v_mfma_f32_16x16x32_bf16 a[168:171], v[40:43], v[20:23], a[168:171]\n"
        "v_mul_f32 v193, v193, v183\n"
        "v_cvt_pk_bf16_f32 v192, 0, v192\n"
        "v_mfma_f32_16x16x32_bf16 a[172:175], v[44:47], v[20:23], a[172:175]\n"
        "v_cvt_pk_bf16_f32 v193, 0, v193\n"
        "v_add_f32 v192, v192, v193\n"
        "v_mfma_f32_16x16x32_bf16 a[176:179], v[20:23], v[48:51], a[176:179]\n"
        "v_add_f32 v191, v191, v192\n"
        "v_lshlrev_b32 v194, 16, v155\n"
        "v_mfma_f32_16x16x32_bf16 a[180:183], v[20:23], v[52:55], a[180:183]\n"
        "v_and_b32 v195, 0xffff0000, v155\n"
        "v_mul_f32 v194, v194, v184\n"
        "v_mfma_f32_16x16x32_bf16 a[184:187], v[20:23], v[56:59], a[184:187]\n"
        "v_mul_f32 v195, v195, v185\n"
        "v_cvt_pk_bf16_f32 v194, 0, v194\n"
        "v_mfma_f32_16x16x32_bf16 a[188:191], v[20:23], v[60:63], a[188:191]\n"
        "ds_read_b128 v[20:23], v225 offset:10240\n"
        "v_cvt_pk_bf16_f32 v195, 0, v195\n"
        "v_mfma_f32_16x16x32_bf16 a[192:195], v[32:35], v[24:27], a[192:195]\n"
        "s_waitcnt vmcnt(32) lgkmcnt(1)\n"
        "v_add_f32 v194, v194, v195\n"
        "v_add_f32 v191, v191, v194\n"
        "v_mfma_f32_16x16x32_bf16 a[196:199], v[36:39], v[24:27], a[196:199]\n"
        "s_waitcnt vmcnt(31)\n"
        "v_lshlrev_b32 v192, 16, v156\n"
        "v_and_b32 v193, 0xffff0000, v156\n"
        "v_mfma_f32_16x16x32_bf16 a[200:203], v[40:43], v[24:27], a[200:203]\n"
        "s_waitcnt vmcnt(30)\n"
        "v_mul_f32 v192, v192, v186\n"
        "v_mul_f32 v193, v193, v187\n"
        "v_mfma_f32_16x16x32_bf16 a[204:207], v[44:47], v[24:27], a[204:207]\n"
        "v_cvt_pk_bf16_f32 v192, 0, v192\n"
        "v_cvt_pk_bf16_f32 v193, 0, v193\n"
        "v_mfma_f32_16x16x32_bf16 a[208:211], v[24:27], v[48:51], a[208:211]\n"
        "s_waitcnt vmcnt(29)\n"
        "v_add_f32 v192, v192, v193\n"
        "v_add_f32 v191, v191, v192\n"
        "v_mfma_f32_16x16x32_bf16 a[212:215], v[24:27], v[52:55], a[212:215]\n"
        "v_lshlrev_b32 v194, 16, v157\n"
        "v_and_b32 v195, 0xffff0000, v157\n"
        "v_mfma_f32_16x16x32_bf16 a[216:219], v[24:27], v[56:59], a[216:219]\n"
        "v_mul_f32 v194, v194, v188\n"
        "v_mul_f32 v195, v195, v189\n"
        "v_mfma_f32_16x16x32_bf16 a[220:223], v[24:27], v[60:63], a[220:223]\n"
        "ds_read_b128 v[24:27], v225 offset:12288\n"
        "v_cvt_pk_bf16_f32 v194, 0, v194\n"
        "v_mfma_f32_16x16x32_bf16 a[224:227], v[32:35], v[28:31], a[224:227]\n"
        "v_cvt_pk_bf16_f32 v195, 0, v195\n"
        "v_add_f32 v194, v194, v195\n"
        "v_mfma_f32_16x16x32_bf16 a[228:231], v[36:39], v[28:31], a[228:231]\n"
        "v_add_f32 v191, v191, v194\n"
        "v_add_f32_dpp v190, v190, v190 row_shr:1 row_mask:0xf bank_mask:0xf bound_ctrl:1\n"
        "v_mfma_f32_16x16x32_bf16 a[232:235], v[40:43], v[28:31], a[232:235]\n"
        "v_add_f32_dpp v191, v191, v191 row_shr:1 row_mask:0xf bank_mask:0xf bound_ctrl:1\n"
        "v_add_f32_dpp v190, v190, v190 row_shr:2 row_mask:0xf bank_mask:0xf bound_ctrl:1\n"
        "v_mfma_f32_16x16x32_bf16 a[236:239], v[44:47], v[28:31], a[236:239]\n"
        "v_add_f32_dpp v191, v191, v191 row_shr:2 row_mask:0xf bank_mask:0xf bound_ctrl:1\n"
        "v_add_f32_dpp v190, v190, v190 row_shr:4 row_mask:0xf bank_mask:0xf bound_ctrl:1\n"
        "v_mfma_f32_16x16x32_bf16 a[240:243], v[28:31], v[48:51], a[240:243]\n"
        "s_waitcnt vmcnt(28) lgkmcnt(1)\n"
        "v_add_f32_dpp v191, v191, v191 row_shr:4 row_mask:0xf bank_mask:0xf bound_ctrl:1\n"
        "v_add_f32_dpp v190, v190, v190 row_shr:8 row_mask:0xf bank_mask:0xf bound_ctrl:1\n"
        "v_mfma_f32_16x16x32_bf16 a[244:247], v[28:31], v[52:55], a[244:247]\n"
        "v_add_f32_dpp v191, v191, v191 row_shr:8 row_mask:0xf bank_mask:0xf bound_ctrl:1\n"
        "v_add_f32_dpp v190, v190, v190 row_bcast:15 row_mask:0xa bank_mask:0xf bound_ctrl:1\n"
        "v_mfma_f32_16x16x32_bf16 a[248:251], v[28:31], v[56:59], a[248:251]\n"
        "v_add_f32_dpp v191, v191, v191 row_bcast:15 row_mask:0xa bank_mask:0xf bound_ctrl:1\n"
        "v_add_f32_dpp v190, v190, v190 row_bcast:31 row_mask:0xc bank_mask:0xf bound_ctrl:1\n"
        "v_mfma_f32_16x16x32_bf16 a[252:255], v[28:31], v[60:63], a[252:255]\n"
        "ds_read_b128 v[28:31], v225 offset:14336\n"
        "v_add_f32_dpp v191, v191, v191 row_bcast:31 row_mask:0xc bank_mask:0xf bound_ctrl:1\n"
        "v_mfma_f32_16x16x32_bf16 a[0:3], v[64:67], v[0:3], a[0:3]\n"
        "v_cvt_pk_bf16_f32 v190, 0, v190\n"
        "v_mul_f32 v190, 0xbfb8aa3b, v190\n"
        "v_mfma_f32_16x16x32_bf16 a[4:7], v[68:71], v[0:3], a[4:7]\n"
        "v_exp_f32 v190, v190\n"
        "v_mfma_f32_16x16x32_bf16 a[8:11], v[72:75], v[0:3], a[8:11]\n"
        "v_add_f32 v190, 1.0, v190\n"
        "v_lshrrev_b32 v192, 2, v254\n"
        "v_mfma_f32_16x16x32_bf16 a[12:15], v[76:79], v[0:3], a[12:15]\n"
        "v_rcp_f32 v190, v190\n"
        "v_mfma_f32_16x16x32_bf16 a[16:19], v[0:3], v[80:83], a[16:19]\n"
        "s_waitcnt vmcnt(27) lgkmcnt(1)\n"
        "v_cvt_pk_bf16_f32 v190, 0, v190\n"
        "v_add_u32 v192, s80, v192\n"
        "v_mfma_f32_16x16x32_bf16 a[20:23], v[0:3], v[84:87], a[20:23]\n"
        "v_readlane_b32 s84, v190, 63\n"
        "v_cvt_pk_bf16_f32 v191, 0, v191\n"
        "v_mfma_f32_16x16x32_bf16 a[24:27], v[0:3], v[88:91], a[24:27]\n"
        "v_mov_b32 v190, s84\n"
        "v_mul_f32 v191, 0xbfb8aa3b, v191\n"
        "v_mfma_f32_16x16x32_bf16 a[28:31], v[0:3], v[92:95], a[28:31]\n"
        "s_waitcnt vmcnt(13) lgkmcnt(0)\n"
        "s_barrier\n"
        "v_mfma_f32_16x16x32_bf16 a[32:35], v[64:67], v[4:7], a[32:35]\n"
        "global_load_lds_dwordx4 v234, s[56:57] offset:0\n"
        "ds_read_b128 v[0:3], v224 offset:32768\n"
        "v_mfma_f32_16x16x32_bf16 a[36:39], v[68:71], v[4:7], a[36:39]\n"
        "ds_read_b64_tr_b16 v[32:33], v226 offset:0\n"
        "v_mfma_f32_16x16x32_bf16 a[40:43], v[72:75], v[4:7], a[40:43]\n"
        "ds_read_b64_tr_b16 v[34:35], v227 offset:0\n"
        "v_mfma_f32_16x16x32_bf16 a[44:47], v[76:79], v[4:7], a[44:47]\n"
        "ds_read_b64_tr_b16 v[36:37], v228 offset:0\n"
        "v_mfma_f32_16x16x32_bf16 a[48:51], v[4:7], v[80:83], a[48:51]\n"
        "ds_read_b64_tr_b16 v[38:39], v229 offset:0\n"
        "global_load_lds_dwordx4 v235, s[56:57] offset:1024\n"
        "v_mfma_f32_16x16x32_bf16 a[52:55], v[4:7], v[84:87], a[52:55]\n"
        "ds_read_b64_tr_b16 v[40:41], v230 offset:0\n"
        "v_mfma_f32_16x16x32_bf16 a[56:59], v[4:7], v[88:91], a[56:59]\n"
        "ds_read_b64_tr_b16 v[42:43], v231 offset:0\n"
        "v_mfma_f32_16x16x32_bf16 a[60:63], v[4:7], v[92:95], a[60:63]\n"
        "ds_read_b128 v[4:7], v224 offset:34816\n"
        "v_mfma_f32_16x16x32_bf16 a[64:67], v[64:67], v[8:11], a[64:67]\n"
        "ds_read_b64_tr_b16 v[44:45], v232 offset:0\n"
        "global_load_lds_dwordx4 v236, s[56:57] offset:2048\n"
        "v_mfma_f32_16x16x32_bf16 a[68:71], v[68:71], v[8:11], a[68:71]\n"
        "ds_read_b64_tr_b16 v[46:47], v233 offset:0\n"
        "v_lshrrev_b32 v193, 2, v254\n"
        "v_mfma_f32_16x16x32_bf16 a[72:75], v[72:75], v[8:11], a[72:75]\n"
        "ds_read_b64_tr_b16 v[48:49], v226 offset:16384\n"
        "v_add_u32 v193, s81, v193\n"
        "v_mfma_f32_16x16x32_bf16 a[76:79], v[76:79], v[8:11], a[76:79]\n"
        "ds_read_b64_tr_b16 v[50:51], v227 offset:16384\n"
        "v_cndmask_b32 v100, v100, 0, s[78:79]\n"
        "v_mfma_f32_16x16x32_bf16 a[80:83], v[8:11], v[80:83], a[80:83]\n"
        "global_load_lds_dwordx4 v237, s[56:57] offset:3072\n"
        "s_add_u32 m0, s72, 0x1000\n"
        "v_mfma_f32_16x16x32_bf16 a[84:87], v[8:11], v[84:87], a[84:87]\n"
        "ds_read_b64_tr_b16 v[52:53], v228 offset:16384\n"
        "s_add_u32 s56, s56, 0x80\n"
        "s_addc_u32 s57, s57, 0\n"
        "v_mfma_f32_16x16x32_bf16 a[88:91], v[8:11], v[88:91], a[88:91]\n"
        "ds_read_b64_tr_b16 v[54:55], v229 offset:16384\n"
        "v_cndmask_b32 v96, v96, 0, s[78:79]\n"
        "v_mfma_f32_16x16x32_bf16 a[92:95], v[8:11], v[92:95], a[92:95]\n"
        "ds_read_b128 v[8:11], v224 offset:36864\n"
        "v_cndmask_b32 v98, v98, 0, s[78:79]\n"
        "v_mfma_f32_16x16x32_bf16 a[96:99], v[64:67], v[12:15], a[96:99]\n"
        "global_load_lds_dwordx4 v234, s[58:59] offset:0\n"
        "v_mfma_f32_16x16x32_bf16 a[100:103], v[68:71], v[12:15], a[100:103]\n"
        "ds_read_b64_tr_b16 v[56:57], v230 offset:16384\n"
        "v_lshlrev_b32 v177, 16, v102\n"
        "v_mfma_f32_16x16x32_bf16 a[104:107], v[72:75], v[12:15], a[104:107]\n"
        "ds_read_b64_tr_b16 v[58:59], v231 offset:16384\n"
        "v_lshlrev_b32 v178, 16, v104\n"
        "v_mfma_f32_16x16x32_bf16 a[108:111], v[76:79], v[12:15], a[108:111]\n"
        "ds_read_b64_tr_b16 v[60:61], v232 offset:16384\n"
        "v_lshlrev_b32 v187, 16, v135\n"
        "v_mfma_f32_16x16x32_bf16 a[112:115], v[12:15], v[80:83], a[112:115]\n"
        "global_load_lds_dwordx4 v235, s[58:59] offset:1024\n"
        "v_mfma_f32_16x16x32_bf16 a[116:119], v[12:15], v[84:87], a[116:119]\n"
        "ds_read_b64_tr_b16 v[62:63], v233 offset:16384\n"
        "v_and_b32 v188, 0xffff0000, v135\n"
        "v_mfma_f32_16x16x32_bf16 a[120:123], v[12:15], v[88:91], a[120:123]\n"
        "v_exp_f32 v191, v191\n"
        "v_mfma_f32_16x16x32_bf16 a[124:127], v[12:15], v[92:95], a[124:127]\n"
        "ds_read_b128 v[12:15], v224 offset:38912\n"
        "v_add_f32 v191, 1.0, v191\n"
        "v_mfma_f32_16x16x32_bf16 a[128:131], v[64:67], v[16:19], a[128:131]\n"
        "global_load_lds_dwordx4 v236, s[58:59] offset:2048\n"
        "v_mfma_f32_16x16x32_bf16 a[132:135], v[68:71], v[16:19], a[132:135]\n"
        "v_rcp_f32 v191, v191\n"
        "v_mfma_f32_16x16x32_bf16 a[136:139], v[72:75], v[16:19], a[136:139]\n"
        "v_cvt_pk_bf16_f32 v191, 0, v191\n"
        "v_lshlrev_b32 v176, 16, v100\n"
        "v_mfma_f32_16x16x32_bf16 a[140:143], v[76:79], v[16:19], a[140:143]\n"
        "v_readlane_b32 s85, v191, 63\n"
        "v_lshlrev_b32 v174, 16, v96\n"
        "v_mfma_f32_16x16x32_bf16 a[144:147], v[16:19], v[80:83], a[144:147]\n"
        "global_load_lds_dwordx4 v237, s[58:59] offset:3072\n"
        "s_add_u32 m0, s73, 0x20000\n"
        "v_mfma_f32_16x16x32_bf16 a[148:151], v[16:19], v[84:87], a[148:151]\n"
        "s_add_u32 s58, s58, 0x80\n"
        "s_addc_u32 s59, s59, 0\n"
        "v_mov_b32 v191, s85\n"
        "v_mfma_f32_16x16x32_bf16 a[152:155], v[16:19], v[88:91], a[152:155]\n"
        "v_lshlrev_b32 v175, 16, v98\n"
        "v_mul_f32 v194, v177, v187\n"
        "v_mfma_f32_16x16x32_bf16 a[156:159], v[16:19], v[92:95], a[156:159]\n"
        "ds_read_b128 v[16:19], v224 offset:40960\n"
        "v_mul_f32 v196, v178, v187\n"
        "v_mfma_f32_16x16x32_bf16 a[160:163], v[64:67], v[20:23], a[160:163]\n"
        "global_load_lds_dwordx4 v238, s[60:61] offset:0\n"
        "v_mfma_f32_16x16x32_bf16 a[164:167], v[68:71], v[20:23], a[164:167]\n"
        "v_mul_f32 v195, v177, v188\n"
        "v_mul_f32 v189, v174, v188\n"
        "v_mfma_f32_16x16x32_bf16 a[168:171], v[72:75], v[20:23], a[168:171]\n"
        "v_cvt_pk_bf16_f32 v194, 0, v194\n"
        "v_cvt_pk_bf16_f32 v196, 0, v196\n"
        "v_mfma_f32_16x16x32_bf16 a[172:175], v[76:79], v[20:23], a[172:175]\n"
        "v_cvt_pk_bf16_f32 v195, 0, v195\n"
        "v_lshlrev_b32 v179, 16, v106\n"
        "v_mfma_f32_16x16x32_bf16 a[176:179], v[20:23], v[80:83], a[176:179]\n"
        "global_load_lds_dwordx4 v239, s[60:61] offset:1024\n"
        "v_mfma_f32_16x16x32_bf16 a[180:183], v[20:23], v[84:87], a[180:183]\n"
        "v_and_b32 v186, 0xffff0000, v134\n"
        "v_cvt_pk_bf16_f32 v189, 0, v189\n"
        "v_mfma_f32_16x16x32_bf16 a[184:187], v[20:23], v[88:91], a[184:187]\n"
        "v_add_f32 v195, v195, v196\n"
        "v_mul_f32 v196, v179, v186\n"
        "v_mfma_f32_16x16x32_bf16 a[188:191], v[20:23], v[92:95], a[188:191]\n"
        "ds_read_b128 v[20:23], v224 offset:43008\n"
        "v_cvt_pk_bf16_f32 v195, 0, v195\n"
        "v_mfma_f32_16x16x32_bf16 a[192:195], v[64:67], v[24:27], a[192:195]\n"
        "global_load_lds_dwordx4 v240, s[60:61] offset:2048\n"
        "v_mfma_f32_16x16x32_bf16 a[196:199], v[68:71], v[24:27], a[196:199]\n"
        "v_cvt_pk_bf16_f32 v196, 0, v196\n"
        "v_lshlrev_b32 v180, 16, v108\n"
        "v_mfma_f32_16x16x32_bf16 a[200:203], v[72:75], v[24:27], a[200:203]\n"
        "v_lshlrev_b32 v185, 16, v134\n"
        "v_add_f32 v195, v195, v196\n"
        "v_mfma_f32_16x16x32_bf16 a[204:207], v[76:79], v[24:27], a[204:207]\n"
        "v_mul_f32 v196, v180, v185\n"
        "v_cvt_pk_bf16_f32 v195, 0, v195\n"
        "v_mfma_f32_16x16x32_bf16 a[208:211], v[24:27], v[80:83], a[208:211]\n"
        "global_load_lds_dwordx4 v241, s[60:61] offset:3072\n"
        "s_add_u32 m0, s73, 0x24000\n"
        "v_mfma_f32_16x16x32_bf16 a[212:215], v[24:27], v[84:87], a[212:215]\n"
        "s_add_u32 s60, s60, 0x80000\n"
        "s_addc_u32 s61, s61, 0\n"
        "v_cvt_pk_bf16_f32 v196, 0, v196\n"
        "v_mfma_f32_16x16x32_bf16 a[216:219], v[24:27], v[88:91], a[216:219]\n"
        "v_add_f32 v195, v195, v196\n"
        "v_cvt_pk_bf16_f32 v195, 0, v195\n"
        "v_mfma_f32_16x16x32_bf16 a[220:223], v[24:27], v[92:95], a[220:223]\n"
        "ds_read_b128 v[24:27], v224 offset:45056\n"
        "v_lshlrev_b32 v181, 16, v110\n"
        "v_mfma_f32_16x16x32_bf16 a[224:227], v[64:67], v[28:31], a[224:227]\n"
        "global_load_lds_dwordx4 v238, s[62:63] offset:0\n"
        "v_mfma_f32_16x16x32_bf16 a[228:231], v[68:71], v[28:31], a[228:231]\n"
        "ds_read_b64_tr_b16 v[64:65], v226 offset:8192\n"
        "v_lshlrev_b32 v182, 16, v112\n"
        "v_mfma_f32_16x16x32_bf16 a[232:235], v[72:75], v[28:31], a[232:235]\n"
        "ds_read_b64_tr_b16 v[66:67], v227 offset:8192\n"
        "v_lshlrev_b32 v183, 16, v114\n"
        "v_mfma_f32_16x16x32_bf16 a[236:239], v[76:79], v[28:31], a[236:239]\n"
        "ds_read_b64_tr_b16 v[68:69], v228 offset:8192\n"
        "v_lshlrev_b32 v184, 16, v116\n"
        "v_mfma_f32_16x16x32_bf16 a[240:243], v[28:31], v[80:83], a[240:243]\n"
        "global_load_lds_dwordx4 v239, s[62:63] offset:1024\n"
        "v_mfma_f32_16x16x32_bf16 a[244:247], v[28:31], v[84:87], a[244:247]\n"
        "ds_read_b64_tr_b16 v[70:71], v229 offset:8192\n"
        "v_and_b32 v174, 0xffff0000, v96\n"
        "v_mfma_f32_16x16x32_bf16 a[248:251], v[28:31], v[88:91], a[248:251]\n"
        "ds_read_b64_tr_b16 v[72:73], v230 offset:8192\n"
        "v_lshlrev_b32 v196, 16, v124\n"
        "v_mfma_f32_16x16x32_bf16 a[252:255], v[28:31], v[92:95], a[252:255]\n"
        "ds_read_b128 v[28:31], v224 offset:47104\n"
        "s_waitcnt lgkmcnt(6)\n"
        "v_mfma_f32_16x16x32_bf16 a[0:3], v[32:35], v[0:3], a[0:3]\n"
        "global_load_dword v253, v173, s[82:83] offset:0\n"
        "v_mfma_f32_16x16x32_bf16 a[4:7], v[36:39], v[0:3], a[4:7]\n"
        "s_mov_b32 exec_hi, 0\n"
        "global_store_dword v192, v190, s[76:77] sc1\n"
        "s_mov_b64 exec, -1\n"
        "v_mfma_f32_16x16x32_bf16 a[8:11], v[40:43], v[0:3], a[8:11]\n"
        "s_mov_b32 exec_hi, 0\n"
        "global_store_dword v193, v191, s[76:77] sc1\n"
        "s_mov_b64 exec, -1\n"
        "v_mfma_f32_16x16x32_bf16 a[12:15], v[44:47], v[0:3], a[12:15]\n"
        "global_load_lds_dwordx4 v240, s[62:63] offset:2048\n"
        "ds_read_b64_tr_b16 v[74:75], v231 offset:8192\n"
        "v_mfma_f32_16x16x32_bf16 a[16:19], v[0:3], v[48:51], a[16:19]\n"
        "ds_read_b64_tr_b16 v[76:77], v232 offset:8192\n"
        "s_add_u32 s82, s12, 0x200000\n"
        "s_addc_u32 s83, s13, 0\n"
        "v_mfma_f32_16x16x32_bf16 a[20:23], v[0:3], v[52:55], a[20:23]\n"
        "ds_read_b64_tr_b16 v[78:79], v233 offset:8192\n"
        "v_mul_f32 v193, v176, v188\n"
        "v_mfma_f32_16x16x32_bf16 a[24:27], v[0:3], v[56:59], a[24:27]\n"
        "ds_read_b64_tr_b16 v[80:81], v226 offset:24576\n"
        "v_mul_f32 v190, v175, v187\n"
        "v_mfma_f32_16x16x32_bf16 a[28:31], v[0:3], v[60:63], a[28:31]\n"
        "global_load_lds_dwordx4 v241, s[62:63] offset:3072\n"
        "ds_read_b128 v[0:3], v225 offset:32768\n"
        "s_add_u32 m0, s72, 0x8000\n"
        "v_mfma_f32_16x16x32_bf16 a[32:35], v[32:35], v[4:7], a[32:35]\n"
        "s_add_u32 s62, s62, 0x80000\n"
        "s_addc_u32 s63, s63, 0\n"
        "v_mul_f32 v192, v176, v187\n"
        "v_mfma_f32_16x16x32_bf16 a[36:39], v[36:39], v[4:7], a[36:39]\n"
        "ds_read_b64_tr_b16 v[82:83], v227 offset:24576\n"
        "v_mul_f32 v191, v175, v188\n"
        "v_mfma_f32_16x16x32_bf16 a[40:43], v[40:43], v[4:7], a[40:43]\n"
        "ds_read_b64_tr_b16 v[84:85], v228 offset:24576\n"
        "v_cvt_pk_bf16_f32 v193, 0, v193\n"
        "v_mfma_f32_16x16x32_bf16 a[44:47], v[44:47], v[4:7], a[44:47]\n"
        "ds_read_b64_tr_b16 v[86:87], v229 offset:24576\n"
        "v_cvt_pk_bf16_f32 v190, 0, v190\n"
        "v_mfma_f32_16x16x32_bf16 a[48:51], v[4:7], v[48:51], a[48:51]\n"
        "ds_read_b64_tr_b16 v[88:89], v230 offset:24576\n"
        "v_cvt_pk_bf16_f32 v192, 0, v192\n"
        "v_mfma_f32_16x16x32_bf16 a[52:55], v[4:7], v[52:55], a[52:55]\n"
        "ds_read_b64_tr_b16 v[90:91], v231 offset:24576\n"
        "v_cvt_pk_bf16_f32 v191, 0, v191\n"
        "v_mfma_f32_16x16x32_bf16 a[56:59], v[4:7], v[56:59], a[56:59]\n"
        "ds_read_b64_tr_b16 v[92:93], v232 offset:24576\n"
        "v_add_f32 v193, v193, v194\n"
        "v_mfma_f32_16x16x32_bf16 a[60:63], v[4:7], v[60:63], a[60:63]\n"
        "ds_read_b128 v[4:7], v225 offset:34816\n"
        "v_add_f32 v189, v189, v190\n"
        "v_mfma_f32_16x16x32_bf16 a[64:67], v[32:35], v[8:11], a[64:67]\n"
        "ds_read_b64_tr_b16 v[94:95], v233 offset:24576\n"
        "v_add_f32 v191, v191, v192\n"
        "v_mfma_f32_16x16x32_bf16 a[68:71], v[36:39], v[8:11], a[68:71]\n"
        "v_mul_f32 v194, v178, v186\n"
        "v_cvt_pk_bf16_f32 v193, 0, v193\n"
        "v_mfma_f32_16x16x32_bf16 a[72:75], v[40:43], v[8:11], a[72:75]\n"
        "v_mul_f32 v190, v176, v186\n"
        "v_mul_f32 v192, v177, v186\n"
        "v_mfma_f32_16x16x32_bf16 a[76:79], v[44:47], v[8:11], a[76:79]\n"
        "v_cvt_pk_bf16_f32 v194, 0, v194\n"
        "v_cvt_pk_bf16_f32 v189, 0, v189\n"
        "v_mfma_f32_16x16x32_bf16 a[80:83], v[8:11], v[48:51], a[80:83]\n"
        "v_cvt_pk_bf16_f32 v191, 0, v191\n"
        "v_cvt_pk_bf16_f32 v190, 0, v190\n"
        "v_mfma_f32_16x16x32_bf16 a[84:87], v[8:11], v[52:55], a[84:87]\n"
        "s_add_u32 s76, s38, 0x3001000\n"
        "s_addc_u32 s77, s39, 0\n"
        "v_cvt_pk_bf16_f32 v192, 0, v192\n"
        "v_mfma_f32_16x16x32_bf16 a[88:91], v[8:11], v[56:59], a[88:91]\n"
        "v_add_f32 v193, v193, v194\n"
        "v_add_f32 v189, v189, v190\n"
        "v_mfma_f32_16x16x32_bf16 a[92:95], v[8:11], v[60:63], a[92:95]\n"
        "ds_read_b128 v[8:11], v225 offset:36864\n"
        "v_add_f32 v191, v191, v192\n"
        "v_mfma_f32_16x16x32_bf16 a[96:99], v[32:35], v[12:15], a[96:99]\n"
        "v_mul_f32 v194, v179, v185\n"
        "v_cvt_pk_bf16_f32 v193, 0, v193\n"
        "v_mfma_f32_16x16x32_bf16 a[100:103], v[36:39], v[12:15], a[100:103]\n"
        "v_mul_f32 v190, v177, v185\n"
        "v_mul_f32 v192, v178, v185\n"
        "v_mfma_f32_16x16x32_bf16 a[104:107], v[40:43], v[12:15], a[104:107]\n"
        "v_cvt_pk_bf16_f32 v194, 0, v194\n"
        "v_cvt_pk_bf16_f32 v189, 0, v189\n"
        "v_mfma_f32_16x16x32_bf16 a[108:111], v[44:47], v[12:15], a[108:111]\n"
        "v_cvt_pk_bf16_f32 v191, 0, v191\n"
        "v_cvt_pk_bf16_f32 v190, 0, v190\n"
        "v_mfma_f32_16x16x32_bf16 a[112:115], v[12:15], v[48:51], a[112:115]\n"
        "v_cvt_pk_bf16_f32 v192, 0, v192\n"
        "v_add_f32 v193, v193, v194\n"
        "v_mfma_f32_16x16x32_bf16 a[116:119], v[12:15], v[52:55], a[116:119]\n"
        "v_add_f32 v189, v189, v190\n"
        "v_add_f32 v191, v191, v192\n"
        "v_mfma_f32_16x16x32_bf16 a[120:123], v[12:15], v[56:59], a[120:123]\n"
        "v_cvt_pk_bf16_f32 v193, 0, v193\n"
        "v_lshlrev_b32 v194, 16, v122\n"
        "v_mfma_f32_16x16x32_bf16 a[124:127], v[12:15], v[60:63], a[124:127]\n"
        "ds_read_b128 v[12:15], v225 offset:38912\n"
        "v_mul_f32 v195, v195, v196\n"
        "v_mfma_f32_16x16x32_bf16 a[128:131], v[32:35], v[16:19], a[128:131]\n"
        "v_cvt_pk_bf16_f32 v189, 0, v189\n"
        "v_lshlrev_b32 v190, 16, v118\n"
        "v_mfma_f32_16x16x32_bf16 a[132:135], v[36:39], v[16:19], a[132:135]\n"
        "v_cvt_pk_bf16_f32 v191, 0, v191\n"
        "v_lshlrev_b32 v192, 16, v120\n"
        "v_mfma_f32_16x16x32_bf16 a[136:139], v[40:43], v[16:19], a[136:139]\n"
        "v_mul_f32 v193, v193, v194\n"
        "v_mul_f32 v189, v189, v190\n"
        "v_mfma_f32_16x16x32_bf16 a[140:143], v[44:47], v[16:19], a[140:143]\n"
        "v_mul_f32 v191, v191, v192\n"
        "v_cvt_pk_bf16_f32 v202, v195, v193\n"
        "v_mfma_f32_16x16x32_bf16 a[144:147], v[16:19], v[48:51], a[144:147]\n"
        "v_mul_f32 v196, v182, v187\n"
        "v_mul_f32 v194, v181, v187\n"
        "v_mfma_f32_16x16x32_bf16 a[148:151], v[16:19], v[52:55], a[148:151]\n"
        "v_cvt_pk_bf16_f32 v203, v191, v189\n"
        "v_mul_f32 v193, v180, v188\n"
        "v_mfma_f32_16x16x32_bf16 a[152:155], v[16:19], v[56:59], a[152:155]\n"
        "v_mul_f32 v195, v181, v188\n"
        "v_mul_f32 v190, v179, v187\n"
        "v_mfma_f32_16x16x32_bf16 a[156:159], v[16:19], v[60:63], a[156:159]\n"
        "ds_read_b128 v[16:19], v225 offset:40960\n"
        "v_mul_f32 v192, v180, v187\n"
        "v_mfma_f32_16x16x32_bf16 a[160:163], v[32:35], v[20:23], a[160:163]\n"
        "v_cvt_pk_bf16_f32 v196, 0, v196\n"
        "v_cvt_pk_bf16_f32 v194, 0, v194\n"
        "v_mfma_f32_16x16x32_bf16 a[164:167], v[36:39], v[20:23], a[164:167]\n"
        "v_mul_f32 v189, v178, v188\n"
        "v_mul_f32 v191, v179, v188\n"
        "v_mfma_f32_16x16x32_bf16 a[168:171], v[40:43], v[20:23], a[168:171]\n"
        "v_cvt_pk_bf16_f32 v193, 0, v193\n"
        "v_cvt_pk_bf16_f32 v195, 0, v195\n"
        "v_mfma_f32_16x16x32_bf16 a[172:175], v[44:47], v[20:23], a[172:175]\n"
        "v_cvt_pk_bf16_f32 v190, 0, v190\n"
        "v_cvt_pk_bf16_f32 v192, 0, v192\n"
        "v_mfma_f32_16x16x32_bf16 a[176:179], v[20:23], v[48:51], a[176:179]\n"
        "v_cvt_pk_bf16_f32 v189, 0, v189\n"
        "v_cvt_pk_bf16_f32 v191, 0, v191\n"
        "v_mfma_f32_16x16x32_bf16 a[180:183], v[20:23], v[52:55], a[180:183]\n"
        "v_add_f32 v193, v193, v194\n"
        "v_add_f32 v195, v195, v196\n"
        "v_mfma_f32_16x16x32_bf16 a[184:187], v[20:23], v[56:59], a[184:187]\n"
        "v_add_f32 v189, v189, v190\n"
        "v_add_f32 v191, v191, v192\n"
        "v_mfma_f32_16x16x32_bf16 a[188:191], v[20:23], v[60:63], a[188:191]\n"
        "ds_read_b128 v[20:23], v225 offset:43008\n"
        "v_mul_f32 v194, v182, v186\n"
        "v_mfma_f32_16x16x32_bf16 a[192:195], v[32:35], v[24:27], a[192:195]\n"
        "v_mul_f32 v196, v183, v186\n"
        "v_cvt_pk_bf16_f32 v193, 0, v193\n"
        "v_mfma_f32_16x16x32_bf16 a[196:199], v[36:39], v[24:27], a[196:199]\n"
        "v_cvt_pk_bf16_f32 v195, 0, v195\n"
        "v_mul_f32 v190, v180, v186\n"
        "v_mfma_f32_16x16x32_bf16 a[200:203], v[40:43], v[24:27], a[200:203]\n"
        "s_waitcnt lgkmcnt(1)\n"
        "v_mul_f32 v192, v181, v186\n"
        "v_cvt_pk_bf16_f32 v194, 0, v194\n"
        "v_mfma_f32_16x16x32_bf16 a[204:207], v[44:47], v[24:27], a[204:207]\n"
        "v_cvt_pk_bf16_f32 v196, 0, v196\n"
        "v_cvt_pk_bf16_f32 v189, 0, v189\n"
        "v_mfma_f32_16x16x32_bf16 a[208:211], v[24:27], v[48:51], a[208:211]\n"
        "v_cvt_pk_bf16_f32 v191, 0, v191\n"
        "v_cvt_pk_bf16_f32 v190, 0, v190\n"
        "v_mfma_f32_16x16x32_bf16 a[212:215], v[24:27], v[52:55], a[212:215]\n"
        "v_cvt_pk_bf16_f32 v192, 0, v192\n"
        "v_add_f32 v193, v193, v194\n"
        "v_mfma_f32_16x16x32_bf16 a[216:219], v[24:27], v[56:59], a[216:219]\n"
        "v_add_f32 v195, v195, v196\n"
        "v_add_f32 v189, v189, v190\n"
        "v_mfma_f32_16x16x32_bf16 a[220:223], v[24:27], v[60:63], a[220:223]\n"
        "ds_read_b128 v[24:27], v225 offset:45056\n"
        "v_add_f32 v191, v191, v192\n"
        "v_mfma_f32_16x16x32_bf16 a[224:227], v[32:35], v[28:31], a[224:227]\n"
        "v_mul_f32 v194, v183, v185\n"
        "v_mul_f32 v196, v184, v185\n"
        "v_mfma_f32_16x16x32_bf16 a[228:231], v[36:39], v[28:31], a[228:231]\n"
        "v_cvt_pk_bf16_f32 v193, 0, v193\n"
        "v_cvt_pk_bf16_f32 v195, 0, v195\n"
        "v_mfma_f32_16x16x32_bf16 a[232:235], v[40:43], v[28:31], a[232:235]\n"
        "v_mul_f32 v190, v181, v185\n"
        "v_mul_f32 v192, v182, v185\n"
        "v_mfma_f32_16x16x32_bf16 a[236:239], v[44:47], v[28:31], a[236:239]\n"
        "v_cvt_pk_bf16_f32 v194, 0, v194\n"
        "v_cvt_pk_bf16_f32 v196, 0, v196\n"
        "v_mfma_f32_16x16x32_bf16 a[240:243], v[28:31], v[48:51], a[240:243]\n"
        "v_cvt_pk_bf16_f32 v189, 0, v189\n"
        "v_cvt_pk_bf16_f32 v191, 0, v191\n"
        "v_mfma_f32_16x16x32_bf16 a[244:247], v[28:31], v[52:55], a[244:247]\n"
        "v_cvt_pk_bf16_f32 v190, 0, v190\n"
        "v_cvt_pk_bf16_f32 v192, 0, v192\n"
        "v_mfma_f32_16x16x32_bf16 a[248:251], v[28:31], v[56:59], a[248:251]\n"
        "v_add_f32 v193, v193, v194\n"
        "v_add_f32 v195, v195, v196\n"
        "v_mfma_f32_16x16x32_bf16 a[252:255], v[28:31], v[60:63], a[252:255]\n"
        "ds_read_b128 v[28:31], v225 offset:47104\n"
        "v_add_f32 v189, v189, v190\n"
        "v_mfma_f32_16x16x32_bf16 a[0:3], v[64:67], v[0:3], a[0:3]\n"
        "s_waitcnt vmcnt(25) lgkmcnt(1)\n"
        "v_add_f32 v191, v191, v192\n"
        "v_cvt_pk_bf16_f32 v193, 0, v193\n"
        "v_mfma_f32_16x16x32_bf16 a[4:7], v[68:71], v[0:3], a[4:7]\n"
        "s_waitcnt vmcnt(22)\n"
        "v_lshlrev_b32 v194, 16, v130\n"
        "v_cvt_pk_bf16_f32 v195, 0, v195\n"
        "v_mfma_f32_16x16x32_bf16 a[8:11], v[72:75], v[0:3], a[8:11]\n"
        "v_lshlrev_b32 v196, 16, v132\n"
        "v_cvt_pk_bf16_f32 v189, 0, v189\n"
        "v_mfma_f32_16x16x32_bf16 a[12:15], v[76:79], v[0:3], a[12:15]\n"
        "v_lshlrev_b32 v190, 16, v126\n"
        "v_cvt_pk_bf16_f32 v191, 0, v191\n"
        "v_mfma_f32_16x16x32_bf16 a[16:19], v[0:3], v[80:83], a[16:19]\n"
        "v_lshlrev_b32 v192, 16, v128\n"
        "v_mul_f32 v193, v193, v194\n"
        "v_mfma_f32_16x16x32_bf16 a[20:23], v[0:3], v[84:87], a[20:23]\n"
        "v_mul_f32 v195, v195, v196\n"
        "v_and_b32 v176, 0xffff0000, v100\n"
        "v_mfma_f32_16x16x32_bf16 a[24:27], v[0:3], v[88:91], a[24:27]\n"
        "v_and_b32 v177, 0xffff0000, v102\n"
        "v_lshlrev_b32 v187, 16, v137\n"
        "v_mfma_f32_16x16x32_bf16 a[28:31], v[0:3], v[92:95], a[28:31]\n"
        "s_waitcnt vmcnt(11) lgkmcnt(0)\n"
        "s_barrier\n"
        "v_mfma_f32_16x16x32_bf16 a[32:35], v[64:67], v[4:7], a[32:35]\n"
        "global_load_lds_dwordx4 v234, s[56:57] offset:0\n"
        "ds_read_b128 v[0:3], v224 offset:0\n"
        "v_mfma_f32_16x16x32_bf16 a[36:39], v[68:71], v[4:7], a[36:39]\n"
        "ds_read_b64_tr_b16 v[32:33], v226 offset:32768\n"
        "v_mfma_f32_16x16x32_bf16 a[40:43], v[72:75], v[4:7], a[40:43]\n"
        "ds_read_b64_tr_b16 v[34:35], v227 offset:32768\n"
        "v_mfma_f32_16x16x32_bf16 a[44:47], v[76:79], v[4:7], a[44:47]\n"
        "ds_read_b64_tr_b16 v[36:37], v228 offset:32768\n"
        "v_mfma_f32_16x16x32_bf16 a[48:51], v[4:7], v[80:83], a[48:51]\n"
        "ds_read_b64_tr_b16 v[38:39], v229 offset:32768\n"
        "global_load_lds_dwordx4 v235, s[56:57] offset:1024\n"
        "v_mfma_f32_16x16x32_bf16 a[52:55], v[4:7], v[84:87], a[52:55]\n"
        "ds_read_b64_tr_b16 v[40:41], v230 offset:32768\n"
        "v_mfma_f32_16x16x32_bf16 a[56:59], v[4:7], v[88:91], a[56:59]\n"
        "ds_read_b64_tr_b16 v[42:43], v231 offset:32768\n"
        "v_mfma_f32_16x16x32_bf16 a[60:63], v[4:7], v[92:95], a[60:63]\n"
        "ds_read_b128 v[4:7], v224 offset:2048\n"
        "v_mfma_f32_16x16x32_bf16 a[64:67], v[64:67], v[8:11], a[64:67]\n"
        "global_load_dword v253, v173, s[82:83] offset:0\n"
        "v_mfma_f32_16x16x32_bf16 a[68:71], v[68:71], v[8:11], a[68:71]\n"
        "ds_read_b64_tr_b16 v[44:45], v232 offset:32768\n"
        "global_load_lds_dwordx4 v236, s[56:57] offset:2048\n"
        "v_mfma_f32_16x16x32_bf16 a[72:75], v[72:75], v[8:11], a[72:75]\n"
        "ds_read_b64_tr_b16 v[46:47], v233 offset:32768\n"
        "s_add_u32 s82, s12, 0x300000\n"
        "s_addc_u32 s83, s13, 0\n"
        "v_mfma_f32_16x16x32_bf16 a[76:79], v[76:79], v[8:11], a[76:79]\n"
        "ds_read_b64_tr_b16 v[48:49], v226 offset:49152\n"
        "v_and_b32 v178, 0xffff0000, v104\n"
        "v_mfma_f32_16x16x32_bf16 a[80:83], v[8:11], v[80:83], a[80:83]\n"
        "ds_read_b64_tr_b16 v[50:51], v227 offset:49152\n"
        "v_and_b32 v188, 0xffff0000, v137\n"
        "v_mfma_f32_16x16x32_bf16 a[84:87], v[8:11], v[84:87], a[84:87]\n"
        "global_load_lds_dwordx4 v237, s[56:57] offset:3072\n"
        "s_add_u32 m0, s72, 0x9000\n"
        "v_mfma_f32_16x16x32_bf16 a[88:91], v[8:11], v[88:91], a[88:91]\n"
        "ds_read_b64_tr_b16 v[52:53], v228 offset:49152\n"
        "s_add_u32 s56, s56, 0x80\n"
        "s_addc_u32 s57, s57, 0\n"
        "v_mfma_f32_16x16x32_bf16 a[92:95], v[8:11], v[92:95], a[92:95]\n"
        "ds_read_b128 v[8:11], v224 offset:4096\n"
        "v_mul_f32 v189, v189, v190\n"
        "v_mfma_f32_16x16x32_bf16 a[96:99], v[64:67], v[12:15], a[96:99]\n"
        "ds_read_b64_tr_b16 v[54:55], v229 offset:49152\n"
        "v_mul_f32 v191, v191, v192\n"
        "v_mfma_f32_16x16x32_bf16 a[100:103], v[68:71], v[12:15], a[100:103]\n"
        "global_load_lds_dwordx4 v234, s[58:59] offset:0\n"
        "v_mfma_f32_16x16x32_bf16 a[104:107], v[72:75], v[12:15], a[104:107]\n"
        "ds_read_b64_tr_b16 v[56:57], v230 offset:49152\n"
        "v_cvt_pk_bf16_f32 v200, v195, v193\n"
        "v_mfma_f32_16x16x32_bf16 a[108:111], v[76:79], v[12:15], a[108:111]\n"
        "ds_read_b64_tr_b16 v[58:59], v231 offset:49152\n"
        "v_and_b32 v175, 0xffff0000, v98\n"
        "v_mfma_f32_16x16x32_bf16 a[112:115], v[12:15], v[80:83], a[112:115]\n"
        "ds_read_b64_tr_b16 v[60:61], v232 offset:49152\n"
        "v_mul_f32 v194, v177, v187\n"
        "v_mfma_f32_16x16x32_bf16 a[116:119], v[12:15], v[84:87], a[116:119]\n"
        "global_load_lds_dwordx4 v235, s[58:59] offset:1024\n"
        "v_mfma_f32_16x16x32_bf16 a[120:123], v[12:15], v[88:91], a[120:123]\n"
        "ds_read_b64_tr_b16 v[62:63], v233 offset:49152\n"
        "v_mul_f32 v196, v178, v187\n"
        "v_mfma_f32_16x16x32_bf16 a[124:127], v[12:15], v[92:95], a[124:127]\n"
        "ds_read_b128 v[12:15], v224 offset:6144\n"
        "v_cvt_pk_bf16_f32 v201, v191, v189\n"
        "v_mfma_f32_16x16x32_bf16 a[128:131], v[64:67], v[16:19], a[128:131]\n"
        "v_mul_f32 v193, v176, v188\n"
        "v_mul_f32 v195, v177, v188\n"
        "v_mfma_f32_16x16x32_bf16 a[132:135], v[68:71], v[16:19], a[132:135]\n"
        "global_load_lds_dwordx4 v236, s[58:59] offset:2048\n"
        "v_mfma_f32_16x16x32_bf16 a[136:139], v[72:75], v[16:19], a[136:139]\n"
        "v_mul_f32 v192, v176, v187\n"
        "v_mul_f32 v190, v175, v187\n"
        "v_mfma_f32_16x16x32_bf16 a[140:143], v[76:79], v[16:19], a[140:143]\n"
        "v_cvt_pk_bf16_f32 v194, 0, v194\n"
        "v_cvt_pk_bf16_f32 v196, 0, v196\n"
        "v_mfma_f32_16x16x32_bf16 a[144:147], v[16:19], v[80:83], a[144:147]\n"
        "v_mul_f32 v189, v174, v188\n"
        "v_mul_f32 v191, v175, v188\n"
        "v_mfma_f32_16x16x32_bf16 a[148:151], v[16:19], v[84:87], a[148:151]\n"
        "global_load_lds_dwordx4 v237, s[58:59] offset:3072\n"
        "s_add_u32 m0, s73, 0x10000\n"
        "v_mfma_f32_16x16x32_bf16 a[152:155], v[16:19], v[88:91], a[152:155]\n"
        "s_add_u32 s58, s58, 0x80\n"
        "s_addc_u32 s59, s59, 0\n"
        "v_cvt_pk_bf16_f32 v193, 0, v193\n"
        "v_mfma_f32_16x16x32_bf16 a[156:159], v[16:19], v[92:95], a[156:159]\n"
        "ds_read_b128 v[16:19], v224 offset:8192\n"
        "v_cvt_pk_bf16_f32 v195, 0, v195\n"
        "v_mfma_f32_16x16x32_bf16 a[160:163], v[64:67], v[20:23], a[160:163]\n"
        "v_and_b32 v179, 0xffff0000, v106\n"
        "v_and_b32 v186, 0xffff0000, v136\n"
        "v_mfma_f32_16x16x32_bf16 a[164:167], v[68:71], v[20:23], a[164:167]\n"
        "global_load_lds_dwordx4 v238, s[60:61] offset:0\n"
        "v_mfma_f32_16x16x32_bf16 a[168:171], v[72:75], v[20:23], a[168:171]\n"
        "v_cvt_pk_bf16_f32 v192, 0, v192\n"
        "v_cvt_pk_bf16_f32 v190, 0, v190\n"
        "v_mfma_f32_16x16x32_bf16 a[172:175], v[76:79], v[20:23], a[172:175]\n"
        "v_cvt_pk_bf16_f32 v189, 0, v189\n"
        "v_cvt_pk_bf16_f32 v191, 0, v191\n"
        "v_mfma_f32_16x16x32_bf16 a[176:179], v[20:23], v[80:83], a[176:179]\n"
        "v_add_f32 v193, v193, v194\n"
        "v_add_f32 v195, v195, v196\n"
        "v_mfma_f32_16x16x32_bf16 a[180:183], v[20:23], v[84:87], a[180:183]\n"
        "global_load_lds_dwordx4 v239, s[60:61] offset:1024\n"
        "v_mfma_f32_16x16x32_bf16 a[184:187], v[20:23], v[88:91], a[184:187]\n"
        "v_add_f32 v189, v189, v190\n"
        "v_add_f32 v191, v191, v192\n"
        "v_mfma_f32_16x16x32_bf16 a[188:191], v[20:23], v[92:95], a[188:191]\n"
        "ds_read_b128 v[20:23], v224 offset:10240\n"
        "v_mul_f32 v194, v178, v186\n"
        "v_mfma_f32_16x16x32_bf16 a[192:195], v[64:67], v[24:27], a[192:195]\n"
        "v_mul_f32 v196, v179, v186\n"
        "v_cvt_pk_bf16_f32 v193, 0, v193\n"
        "v_mfma_f32_16x16x32_bf16 a[196:199], v[68:71], v[24:27], a[196:199]\n"
        "global_load_lds_dwordx4 v240, s[60:61] offset:2048\n"
        "v_mfma_f32_16x16x32_bf16 a[200:203], v[72:75], v[24:27], a[200:203]\n"
        "v_cvt_pk_bf16_f32 v195, 0, v195\n"
        "v_mul_f32 v190, v176, v186\n"
        "v_mfma_f32_16x16x32_bf16 a[204:207], v[76:79], v[24:27], a[204:207]\n"
        "v_mul_f32 v192, v177, v186\n"
        "v_cvt_pk_bf16_f32 v194, 0, v194\n"
        "v_mfma_f32_16x16x32_bf16 a[208:211], v[24:27], v[80:83], a[208:211]\n"
        "v_cvt_pk_bf16_f32 v196, 0, v196\n"
        "v_and_b32 v180, 0xffff0000, v108\n"
        "v_mfma_f32_16x16x32_bf16 a[212:215], v[24:27], v[84:87], a[212:215]\n"
        "global_load_lds_dwordx4 v241, s[60:61] offset:3072\n"
        "s_add_u32 m0, s73, 0x14000\n"
        "v_mfma_f32_16x16x32_bf16 a[216:219], v[24:27], v[88:91], a[216:219]\n"
        "s_add_u32 s60, s60, 0x80000\n"
        "s_addc_u32 s61, s61, 0\n"
        "v_lshlrev_b32 v185, 16, v136\n"
        "v_mfma_f32_16x16x32_bf16 a[220:223], v[24:27], v[92:95], a[220:223]\n"
        "ds_read_b128 v[24:27], v224 offset:12288\n"
        "v_cvt_pk_bf16_f32 v189, 0, v189\n"
        "v_mfma_f32_16x16x32_bf16 a[224:227], v[64:67], v[28:31], a[224:227]\n"
        "ds_read_b64_tr_b16 v[64:65], v226 offset:40960\n"
        "v_cvt_pk_bf16_f32 v191, 0, v191\n"
        "v_mfma_f32_16x16x32_bf16 a[228:231], v[68:71], v[28:31], a[228:231]\n"
        "global_load_lds_dwordx4 v238, s[62:63] offset:0\n"
        "v_mfma_f32_16x16x32_bf16 a[232:235], v[72:75], v[28:31], a[232:235]\n"
        "ds_read_b64_tr_b16 v[66:67], v227 offset:40960\n"
        "v_cvt_pk_bf16_f32 v190, 0, v190\n"
        "v_mfma_f32_16x16x32_bf16 a[236:239], v[76:79], v[28:31], a[236:239]\n"
        "ds_read_b64_tr_b16 v[68:69], v228 offset:40960\n"
        "v_cvt_pk_bf16_f32 v192, 0, v192\n"
        "v_mfma_f32_16x16x32_bf16 a[240:243], v[28:31], v[80:83], a[240:243]\n"
        "ds_read_b64_tr_b16 v[70:71], v229 offset:40960\n"
        "v_add_f32 v193, v193, v194\n"
        "v_mfma_f32_16x16x32_bf16 a[244:247], v[28:31], v[84:87], a[244:247]\n"
        "global_load_lds_dwordx4 v239, s[62:63] offset:1024\n"
        "v_mfma_f32_16x16x32_bf16 a[248:251], v[28:31], v[88:91], a[248:251]\n"
        "ds_read_b64_tr_b16 v[72:73], v230 offset:40960\n"
        "v_add_f32 v195, v195, v196\n"
        "v_mfma_f32_16x16x32_bf16 a[252:255], v[28:31], v[92:95], a[252:255]\n"
        "ds_read_b128 v[28:31], v224 offset:14336\n"
        "s_waitcnt lgkmcnt(6)\n"
        "v_mfma_f32_16x16x32_bf16 a[0:3], v[32:35], v[0:3], a[0:3]\n"
        "ds_read_b64_tr_b16 v[74:75], v231 offset:40960\n"
        "v_mfma_f32_16x16x32_bf16 a[4:7], v[36:39], v[0:3], a[4:7]\n"
        "global_load_lds_dwordx4 v240, s[62:63] offset:2048\n"
        "v_mfma_f32_16x16x32_bf16 a[8:11], v[40:43], v[0:3], a[8:11]\n"
        "ds_read_b64_tr_b16 v[76:77], v232 offset:40960\n"
        "v_add_f32 v189, v189, v190\n"
        "v_mfma_f32_16x16x32_bf16 a[12:15], v[44:47], v[0:3], a[12:15]\n"
        "ds_read_b64_tr_b16 v[78:79], v233 offset:40960\n"
        "v_add_f32 v191, v191, v192\n"
        "v_mfma_f32_16x16x32_bf16 a[16:19], v[0:3], v[48:51], a[16:19]\n"
        "ds_read_b64_tr_b16 v[80:81], v226 offset:57344\n"
        "v_mul_f32 v194, v179, v185\n"
        "v_mfma_f32_16x16x32_bf16 a[20:23], v[0:3], v[52:55], a[20:23]\n"
        "global_load_lds_dwordx4 v241, s[62:63] offset:3072\n"
        "s_add_u32 m0, s72, 0x0\n"
        "v_mfma_f32_16x16x32_bf16 a[24:27], v[0:3], v[56:59], a[24:27]\n"
        "s_add_u32 s62, s62, 0x80000\n"
        "s_addc_u32 s63, s63, 0\n"
        "v_mul_f32 v196, v180, v185\n"
        "v_mfma_f32_16x16x32_bf16 a[28:31], v[0:3], v[60:63], a[28:31]\n"
        "ds_read_b128 v[0:3], v225 offset:0\n"
        "v_cvt_pk_bf16_f32 v193, 0, v193\n"
        "v_mfma_f32_16x16x32_bf16 a[32:35], v[32:35], v[4:7], a[32:35]\n"
        "ds_read_b64_tr_b16 v[82:83], v227 offset:57344\n"
        "v_cvt_pk_bf16_f32 v195, 0, v195\n"
        "v_mfma_f32_16x16x32_bf16 a[36:39], v[36:39], v[4:7], a[36:39]\n"
        "ds_read_b64_tr_b16 v[84:85], v228 offset:57344\n"
        "v_mul_f32 v190, v177, v185\n"
        "v_mfma_f32_16x16x32_bf16 a[40:43], v[40:43], v[4:7], a[40:43]\n"
        "ds_read_b64_tr_b16 v[86:87], v229 offset:57344\n"
        "v_mul_f32 v192, v178, v185\n"
        "v_mfma_f32_16x16x32_bf16 a[44:47], v[44:47], v[4:7], a[44:47]\n"
        "ds_read_b64_tr_b16 v[88:89], v230 offset:57344\n"
        "v_cvt_pk_bf16_f32 v194, 0, v194\n"
        "v_mfma_f32_16x16x32_bf16 a[48:51], v[4:7], v[48:51], a[48:51]\n"
        "ds_read_b64_tr_b16 v[90:91], v231 offset:57344\n"
        "v_cvt_pk_bf16_f32 v196, 0, v196\n"
        "v_mfma_f32_16x16x32_bf16 a[52:55], v[4:7], v[52:55], a[52:55]\n"
        "ds_read_b64_tr_b16 v[92:93], v232 offset:57344\n"
        "v_cvt_pk_bf16_f32 v189, 0, v189\n"
        "v_mfma_f32_16x16x32_bf16 a[56:59], v[4:7], v[56:59], a[56:59]\n"
        "ds_read_b64_tr_b16 v[94:95], v233 offset:57344\n"
        "v_cvt_pk_bf16_f32 v191, 0, v191\n"
        "v_mfma_f32_16x16x32_bf16 a[60:63], v[4:7], v[60:63], a[60:63]\n"
        "ds_read_b128 v[4:7], v225 offset:2048\n"
        "v_cvt_pk_bf16_f32 v190, 0, v190\n"
        "v_mfma_f32_16x16x32_bf16 a[64:67], v[32:35], v[8:11], a[64:67]\n"
        "v_cvt_pk_bf16_f32 v192, 0, v192\n"
        "v_add_f32 v193, v193, v194\n"
        "v_mfma_f32_16x16x32_bf16 a[68:71], v[36:39], v[8:11], a[68:71]\n"
        "v_add_f32 v195, v195, v196\n"
        "v_add_f32 v189, v189, v190\n"
        "v_mfma_f32_16x16x32_bf16 a[72:75], v[40:43], v[8:11], a[72:75]\n"
        "v_add_f32 v191, v191, v192\n"
        "v_cvt_pk_bf16_f32 v193, 0, v193\n"
        "v_mfma_f32_16x16x32_bf16 a[76:79], v[44:47], v[8:11], a[76:79]\n"
        "v_and_b32 v194, 0xffff0000, v122\n"
        "v_cvt_pk_bf16_f32 v195, 0, v195\n"
        "v_mfma_f32_16x16x32_bf16 a[80:83], v[8:11], v[48:51], a[80:83]\n"
        "v_and_b32 v196, 0xffff0000, v124\n"
        "v_cvt_pk_bf16_f32 v189, 0, v189\n"
        "v_mfma_f32_16x16x32_bf16 a[84:87], v[8:11], v[52:55], a[84:87]\n"
        "v_and_b32 v190, 0xffff0000, v118\n"
        "v_cvt_pk_bf16_f32 v191, 0, v191\n"
        "v_mfma_f32_16x16x32_bf16 a[88:91], v[8:11], v[56:59], a[88:91]\n"
        "v_and_b32 v192, 0xffff0000, v120\n"
        "v_mul_f32 v193, v193, v194\n"
        "v_mfma_f32_16x16x32_bf16 a[92:95], v[8:11], v[60:63], a[92:95]\n"
        "ds_read_b128 v[8:11], v225 offset:4096\n"
        "global_load_dword v253, v173, s[82:83] offset:0\n"
        "v_mfma_f32_16x16x32_bf16 a[96:99], v[32:35], v[12:15], a[96:99]\n"
        "s_add_u32 s82, s12, 0x400000\n"
        "s_addc_u32 s83, s13, 0\n"
        "v_mul_f32 v195, v195, v196\n"
        "v_mfma_f32_16x16x32_bf16 a[100:103], v[36:39], v[12:15], a[100:103]\n"
        "v_and_b32 v181, 0xffff0000, v110\n"
        "v_and_b32 v182, 0xffff0000, v112\n"
        "v_mfma_f32_16x16x32_bf16 a[104:107], v[40:43], v[12:15], a[104:107]\n"
        "v_mul_f32 v189, v189, v190\n"
        "v_mul_f32 v191, v191, v192\n"
        "v_mfma_f32_16x16x32_bf16 a[108:111], v[44:47], v[12:15], a[108:111]\n"
        "v_cvt_pk_bf16_f32 v210, v195, v193\n"
        "v_mul_f32 v194, v181, v187\n"
        "v_mfma_f32_16x16x32_bf16 a[112:115], v[12:15], v[48:51], a[112:115]\n"
        "v_mul_f32 v196, v182, v187\n"
        "v_cvt_pk_bf16_f32 v211, v191, v189\n"
        "v_mfma_f32_16x16x32_bf16 a[116:119], v[12:15], v[52:55], a[116:119]\n"
        "v_mul_f32 v193, v180, v188\n"
        "v_mul_f32 v195, v181, v188\n"
        "v_mfma_f32_16x16x32_bf16 a[120:123], v[12:15], v[56:59], a[120:123]\n"
        "v_mul_f32 v190, v179, v187\n"
        "v_mul_f32 v192, v180, v187\n"
        "v_mfma_f32_16x16x32_bf16 a[124:127], v[12:15], v[60:63], a[124:127]\n"
        "ds_read_b128 v[12:15], v225 offset:6144\n"
        "v_cvt_pk_bf16_f32 v194, 0, v194\n"
        "v_mfma_f32_16x16x32_bf16 a[128:131], v[32:35], v[16:19], a[128:131]\n"
        "v_cvt_pk_bf16_f32 v196, 0, v196\n"
        "v_mul_f32 v189, v178, v188\n"
        "v_mfma_f32_16x16x32_bf16 a[132:135], v[36:39], v[16:19], a[132:135]\n"
        "v_mul_f32 v191, v179, v188\n"
        "v_cvt_pk_bf16_f32 v193, 0, v193\n"
        "v_mfma_f32_16x16x32_bf16 a[136:139], v[40:43], v[16:19], a[136:139]\n"
        "v_cvt_pk_bf16_f32 v195, 0, v195\n"
        "v_and_b32 v183, 0xffff0000, v114\n"
        "v_mfma_f32_16x16x32_bf16 a[140:143], v[44:47], v[16:19], a[140:143]\n"
        "v_cvt_pk_bf16_f32 v190, 0, v190\n"
        "v_cvt_pk_bf16_f32 v192, 0, v192\n"
        "v_mfma_f32_16x16x32_bf16 a[144:147], v[16:19], v[48:51], a[144:147]\n"
        "v_cvt_pk_bf16_f32 v189, 0, v189\n"
        "v_cvt_pk_bf16_f32 v191, 0, v191\n"
        "v_mfma_f32_16x16x32_bf16 a[148:151], v[16:19], v[52:55], a[148:151]\n"
        "v_add_f32 v193, v193, v194\n"
        "v_add_f32 v195, v195, v196\n"
        "v_mfma_f32_16x16x32_bf16 a[152:155], v[16:19], v[56:59], a[152:155]\n"
        "v_add_f32 v189, v189, v190\n"
        "v_add_f32 v191, v191, v192\n"
        "v_mfma_f32_16x16x32_bf16 a[156:159], v[16:19], v[60:63], a[156:159]\n"
        "ds_read_b128 v[16:19], v225 offset:8192\n"
        "v_mul_f32 v194, v182, v186\n"
        "v_mfma_f32_16x16x32_bf16 a[160:163], v[32:35], v[20:23], a[160:163]\n"
        "v_mul_f32 v196, v183, v186\n"
        "v_cvt_pk_bf16_f32 v193, 0, v193\n"
        "v_mfma_f32_16x16x32_bf16 a[164:167], v[36:39], v[20:23], a[164:167]\n"
        "v_cvt_pk_bf16_f32 v195, 0, v195\n"
        "v_mul_f32 v190, v180, v186\n"
        "v_mfma_f32_16x16x32_bf16 a[168:171], v[40:43], v[20:23], a[168:171]\n"
        "v_mul_f32 v192, v181, v186\n"
        "v_cvt_pk_bf16_f32 v194, 0, v194\n"
        "v_mfma_f32_16x16x32_bf16 a[172:175], v[44:47], v[20:23], a[172:175]\n"
        "v_cvt_pk_bf16_f32 v196, 0, v196\n"
        "v_and_b32 v184, 0xffff0000, v116\n"
        "v_mfma_f32_16x16x32_bf16 a[176:179], v[20:23], v[48:51], a[176:179]\n"
        "v_cvt_pk_bf16_f32 v189, 0, v189\n"
        "v_cvt_pk_bf16_f32 v191, 0, v191\n"
        "v_mfma_f32_16x16x32_bf16 a[180:183], v[20:23], v[52:55], a[180:183]\n"
        "v_cvt_pk_bf16_f32 v190, 0, v190\n"
        "v_cvt_pk_bf16_f32 v192, 0, v192\n"
        "v_mfma_f32_16x16x32_bf16 a[184:187], v[20:23], v[56:59], a[184:187]\n"
        "v_add_f32 v193, v193, v194\n"
        "v_add_f32 v195, v195, v196\n"
        "v_mfma_f32_16x16x32_bf16 a[188:191], v[20:23], v[60:63], a[188:191]\n"
        "ds_read_b128 v[20:23], v225 offset:10240\n"
        "v_add_f32 v189, v189, v190\n"
        "v_mfma_f32_16x16x32_bf16 a[192:195], v[32:35], v[24:27], a[192:195]\n"
        "v_add_f32 v191, v191, v192\n"
        "v_mul_f32 v194, v183, v185\n"
        "v_mfma_f32_16x16x32_bf16 a[196:199], v[36:39], v[24:27], a[196:199]\n"
        "v_mul_f32 v196, v184, v185\n"
        "v_cvt_pk_bf16_f32 v193, 0, v193\n"
        "v_mfma_f32_16x16x32_bf16 a[200:203], v[40:43], v[24:27], a[200:203]\n"
        "s_waitcnt lgkmcnt(1)\n"
        "v_cvt_pk_bf16_f32 v195, 0, v195\n"
        "v_mul_f32 v190, v181, v185\n"
        "v_mfma_f32_16x16x32_bf16 a[204:207], v[44:47], v[24:27], a[204:207]\n"
        "v_mul_f32 v192, v182, v185\n"
        "v_cvt_pk_bf16_f32 v194, 0, v194\n"
        "v_mfma_f32_16x16x32_bf16 a[208:211], v[24:27], v[48:51], a[208:211]\n"
        "v_cvt_pk_bf16_f32 v196, 0, v196\n"
        "v_cvt_pk_bf16_f32 v189, 0, v189\n"
        "v_mfma_f32_16x16x32_bf16 a[212:215], v[24:27], v[52:55], a[212:215]\n"
        "v_cvt_pk_bf16_f32 v191, 0, v191\n"
        "v_cvt_pk_bf16_f32 v190, 0, v190\n"
        "v_mfma_f32_16x16x32_bf16 a[216:219], v[24:27], v[56:59], a[216:219]\n"
        "v_cvt_pk_bf16_f32 v192, 0, v192\n"
        "v_add_f32 v193, v193, v194\n"
        "v_mfma_f32_16x16x32_bf16 a[220:223], v[24:27], v[60:63], a[220:223]\n"
        "ds_read_b128 v[24:27], v225 offset:12288\n"
        "v_add_f32 v195, v195, v196\n"
        "v_mfma_f32_16x16x32_bf16 a[224:227], v[32:35], v[28:31], a[224:227]\n"
        "v_add_f32 v189, v189, v190\n"
        "v_add_f32 v191, v191, v192\n"
        "v_mfma_f32_16x16x32_bf16 a[228:231], v[36:39], v[28:31], a[228:231]\n"
        "v_cvt_pk_bf16_f32 v193, 0, v193\n"
        "v_and_b32 v194, 0xffff0000, v130\n"
        "v_mfma_f32_16x16x32_bf16 a[232:235], v[40:43], v[28:31], a[232:235]\n"
        "v_cvt_pk_bf16_f32 v195, 0, v195\n"
        "v_and_b32 v196, 0xffff0000, v132\n"
        "v_mfma_f32_16x16x32_bf16 a[236:239], v[44:47], v[28:31], a[236:239]\n"
        "v_cndmask_b32 v101, v101, 0, s[78:79]\n"
        "v_cvt_pk_bf16_f32 v189, 0, v189\n"
        "v_mfma_f32_16x16x32_bf16 a[240:243], v[28:31], v[48:51], a[240:243]\n"
        "v_and_b32 v190, 0xffff0000, v126\n"
        "v_cvt_pk_bf16_f32 v191, 0, v191\n"
        "v_mfma_f32_16x16x32_bf16 a[244:247], v[28:31], v[52:55], a[244:247]\n"
        "v_and_b32 v192, 0xffff0000, v128\n"
        "v_mul_f32 v193, v193, v194\n"
        "v_mfma_f32_16x16x32_bf16 a[248:251], v[28:31], v[56:59], a[248:251]\n"
        "v_mul_f32 v195, v195, v196\n"
        "v_cndmask_b32 v97, v97, 0, s[78:79]\n"
        "v_mfma_f32_16x16x32_bf16 a[252:255], v[28:31], v[60:63], a[252:255]\n"
        "ds_read_b128 v[28:31], v225 offset:14336\n"
        "v_cndmask_b32 v99, v99, 0, s[78:79]\n"
        "v_mfma_f32_16x16x32_bf16 a[0:3], v[64:67], v[0:3], a[0:3]\n"
        "v_lshlrev_b32 v177, 16, v103\n"
        "v_lshlrev_b32 v187, 16, v139\n"
        "v_mfma_f32_16x16x32_bf16 a[4:7], v[68:71], v[0:3], a[4:7]\n"
        "v_lshlrev_b32 v178, 16, v105\n"
        "v_and_b32 v188, 0xffff0000, v139\n"
        "v_mfma_f32_16x16x32_bf16 a[8:11], v[72:75], v[0:3], a[8:11]\n"
        "v_lshlrev_b32 v176, 16, v101\n"
        "v_mul_f32 v189, v189, v190\n"
        "v_mfma_f32_16x16x32_bf16 a[12:15], v[76:79], v[0:3], a[12:15]\n"
        "v_mul_f32 v191, v191, v192\n"
        "v_cvt_pk_bf16_f32 v208, v195, v193\n"
        "v_mfma_f32_16x16x32_bf16 a[16:19], v[0:3], v[80:83], a[16:19]\n"
        "v_lshlrev_b32 v174, 16, v97\n"
        "v_lshlrev_b32 v175, 16, v99\n"
        "v_mfma_f32_16x16x32_bf16 a[20:23], v[0:3], v[84:87], a[20:23]\n"
        "v_mul_f32 v194, v177, v187\n"
        "v_mul_f32 v196, v178, v187\n"
        "v_mfma_f32_16x16x32_bf16 a[24:27], v[0:3], v[88:91], a[24:27]\n"
        "v_cvt_pk_bf16_f32 v209, v191, v189\n"
        "v_mul_f32 v193, v176, v188\n"
        "v_mfma_f32_16x16x32_bf16 a[28:31], v[0:3], v[92:95], a[28:31]\n"
        "s_waitcnt vmcnt(9) lgkmcnt(0)\n"
        "s_barrier\n"
        "v_mfma_f32_16x16x32_bf16 a[32:35], v[64:67], v[4:7], a[32:35]\n"
        "global_load_lds_dwordx4 v234, s[56:57] offset:0\n"
        "ds_read_b128 v[0:3], v224 offset:32768\n"
        "v_mfma_f32_16x16x32_bf16 a[36:39], v[68:71], v[4:7], a[36:39]\n"
        "ds_read_b64_tr_b16 v[32:33], v216 offset:0\n"
        "v_mfma_f32_16x16x32_bf16 a[40:43], v[72:75], v[4:7], a[40:43]\n"
        "ds_read_b64_tr_b16 v[34:35], v217 offset:0\n"
        "v_mfma_f32_16x16x32_bf16 a[44:47], v[76:79], v[4:7], a[44:47]\n"
        "ds_read_b64_tr_b16 v[36:37], v218 offset:0\n"
        "v_mfma_f32_16x16x32_bf16 a[48:51], v[4:7], v[80:83], a[48:51]\n"
        "ds_read_b64_tr_b16 v[38:39], v219 offset:0\n"
        "global_load_lds_dwordx4 v235, s[56:57] offset:1024\n"
        "v_mfma_f32_16x16x32_bf16 a[52:55], v[4:7], v[84:87], a[52:55]\n"
        "ds_read_b64_tr_b16 v[40:41], v220 offset:0\n"
        "v_mfma_f32_16x16x32_bf16 a[56:59], v[4:7], v[88:91], a[56:59]\n"
        "ds_read_b64_tr_b16 v[42:43], v221 offset:0\n"
        "v_mfma_f32_16x16x32_bf16 a[60:63], v[4:7], v[92:95], a[60:63]\n"
        "ds_read_b128 v[4:7], v224 offset:34816\n"
        "v_mfma_f32_16x16x32_bf16 a[64:67], v[64:67], v[8:11], a[64:67]\n"
        "ds_read_b64_tr_b16 v[44:45], v222 offset:0\n"
        "global_load_lds_dwordx4 v236, s[56:57] offset:2048\n"
        "v_mfma_f32_16x16x32_bf16 a[68:71], v[68:71], v[8:11], a[68:71]\n"
        "ds_read_b64_tr_b16 v[46:47], v223 offset:0\n"
        "v_mul_f32 v195, v177, v188\n"
        "v_mfma_f32_16x16x32_bf16 a[72:75], v[72:75], v[8:11], a[72:75]\n"
        "ds_read_b64_tr_b16 v[48:49], v216 offset:16384\n"
        "v_mul_f32 v192, v176, v187\n"
        "v_mfma_f32_16x16x32_bf16 a[76:79], v[76:79], v[8:11], a[76:79]\n"
        "ds_read_b64_tr_b16 v[50:51], v217 offset:16384\n"
        "v_mul_f32 v190, v175, v187\n"
        "v_mfma_f32_16x16x32_bf16 a[80:83], v[8:11], v[80:83], a[80:83]\n"
        "global_load_lds_dwordx4 v237, s[56:57] offset:3072\n"
        "s_add_u32 m0, s72, 0x1000\n"
        "v_mfma_f32_16x16x32_bf16 a[84:87], v[8:11], v[84:87], a[84:87]\n"
        "ds_read_b64_tr_b16 v[52:53], v218 offset:16384\n"
        "s_add_u32 s56, s56, 0x80\n"
        "s_addc_u32 s57, s57, 0\n"
        "v_mfma_f32_16x16x32_bf16 a[88:91], v[8:11], v[88:91], a[88:91]\n"
        "ds_read_b64_tr_b16 v[54:55], v219 offset:16384\n"
        "v_cvt_pk_bf16_f32 v194, 0, v194\n"
        "v_mfma_f32_16x16x32_bf16 a[92:95], v[8:11], v[92:95], a[92:95]\n"
        "ds_read_b128 v[8:11], v224 offset:36864\n"
        "v_cvt_pk_bf16_f32 v196, 0, v196\n"
        "v_mfma_f32_16x16x32_bf16 a[96:99], v[64:67], v[12:15], a[96:99]\n"
        "global_load_lds_dwordx4 v234, s[58:59] offset:0\n"
        "v_mfma_f32_16x16x32_bf16 a[100:103], v[68:71], v[12:15], a[100:103]\n"
        "ds_read_b64_tr_b16 v[56:57], v220 offset:16384\n"
        "v_mul_f32 v189, v174, v188\n"
        "v_mfma_f32_16x16x32_bf16 a[104:107], v[72:75], v[12:15], a[104:107]\n"
        "ds_read_b64_tr_b16 v[58:59], v221 offset:16384\n"
        "v_mul_f32 v191, v175, v188\n"
        "v_mfma_f32_16x16x32_bf16 a[108:111], v[76:79], v[12:15], a[108:111]\n"
        "ds_read_b64_tr_b16 v[60:61], v222 offset:16384\n"
        "v_cvt_pk_bf16_f32 v193, 0, v193\n"
        "v_mfma_f32_16x16x32_bf16 a[112:115], v[12:15], v[80:83], a[112:115]\n"
        "global_load_lds_dwordx4 v235, s[58:59] offset:1024\n"
        "v_mfma_f32_16x16x32_bf16 a[116:119], v[12:15], v[84:87], a[116:119]\n"
        "ds_read_b64_tr_b16 v[62:63], v223 offset:16384\n"
        "v_cvt_pk_bf16_f32 v195, 0, v195\n"
        "v_mfma_f32_16x16x32_bf16 a[120:123], v[12:15], v[88:91], a[120:123]\n"
        "v_lshlrev_b32 v179, 16, v107\n"
        "v_and_b32 v186, 0xffff0000, v138\n"
        "v_mfma_f32_16x16x32_bf16 a[124:127], v[12:15], v[92:95], a[124:127]\n"
        "ds_read_b128 v[12:15], v224 offset:38912\n"
        "v_cvt_pk_bf16_f32 v192, 0, v192\n"
        "v_mfma_f32_16x16x32_bf16 a[128:131], v[64:67], v[16:19], a[128:131]\n"
        "global_load_lds_dwordx4 v236, s[58:59] offset:2048\n"
        "v_mfma_f32_16x16x32_bf16 a[132:135], v[68:71], v[16:19], a[132:135]\n"
        "v_cvt_pk_bf16_f32 v190, 0, v190\n"
        "v_cvt_pk_bf16_f32 v189, 0, v189\n"
        "v_mfma_f32_16x16x32_bf16 a[136:139], v[72:75], v[16:19], a[136:139]\n"
        "v_cvt_pk_bf16_f32 v191, 0, v191\n"
        "v_add_f32 v193, v193, v194\n"
        "v_mfma_f32_16x16x32_bf16 a[140:143], v[76:79], v[16:19], a[140:143]\n"
        "v_add_f32 v195, v195, v196\n"
        "v_add_f32 v189, v189, v190\n"
        "v_mfma_f32_16x16x32_bf16 a[144:147], v[16:19], v[80:83], a[144:147]\n"
        "global_load_lds_dwordx4 v237, s[58:59] offset:3072\n"
        "s_add_u32 m0, s73, 0x18000\n"
        "v_mfma_f32_16x16x32_bf16 a[148:151], v[16:19], v[84:87], a[148:151]\n"
        "s_add_u32 s58, s58, 0x80\n"
        "s_addc_u32 s59, s59, 0\n"
        "v_add_f32 v191, v191, v192\n"
        "v_mfma_f32_16x16x32_bf16 a[152:155], v[16:19], v[88:91], a[152:155]\n"
        "v_mul_f32 v194, v178, v186\n"
        "v_mul_f32 v196, v179, v186\n"
        "v_mfma_f32_16x16x32_bf16 a[156:159], v[16:19], v[92:95], a[156:159]\n"
        "ds_read_b128 v[16:19], v224 offset:40960\n"
        "v_cvt_pk_bf16_f32 v193, 0, v193\n"
        "v_mfma_f32_16x16x32_bf16 a[160:163], v[64:67], v[20:23], a[160:163]\n"
        "global_load_lds_dwordx4 v238, s[60:61] offset:0\n"
        "v_mfma_f32_16x16x32_bf16 a[164:167], v[68:71], v[20:23], a[164:167]\n"
        "v_cvt_pk_bf16_f32 v195, 0, v195\n"
        "v_mul_f32 v190, v176, v186\n"
        "v_mfma_f32_16x16x32_bf16 a[168:171], v[72:75], v[20:23], a[168:171]\n"
        "v_mul_f32 v192, v177, v186\n"
        "v_cvt_pk_bf16_f32 v194, 0, v194\n"
        "v_mfma_f32_16x16x32_bf16 a[172:175], v[76:79], v[20:23], a[172:175]\n"
        "v_cvt_pk_bf16_f32 v196, 0, v196\n"
        "v_lshlrev_b32 v180, 16, v109\n"
        "v_mfma_f32_16x16x32_bf16 a[176:179], v[20:23], v[80:83], a[176:179]\n"
        "global_load_lds_dwordx4 v239, s[60:61] offset:1024\n"
        "v_mfma_f32_16x16x32_bf16 a[180:183], v[20:23], v[84:87], a[180:183]\n"
        "v_lshlrev_b32 v185, 16, v138\n"
        "v_cvt_pk_bf16_f32 v189, 0, v189\n"
        "v_mfma_f32_16x16x32_bf16 a[184:187], v[20:23], v[88:91], a[184:187]\n"
        "v_cvt_pk_bf16_f32 v191, 0, v191\n"
        "v_cvt_pk_bf16_f32 v190, 0, v190\n"
        "v_mfma_f32_16x16x32_bf16 a[188:191], v[20:23], v[92:95], a[188:191]\n"
        "ds_read_b128 v[20:23], v224 offset:43008\n"
        "v_cvt_pk_bf16_f32 v192, 0, v192\n"
        "v_mfma_f32_16x16x32_bf16 a[192:195], v[64:67], v[24:27], a[192:195]\n"
        "global_load_dword v253, v173, s[82:83] offset:0\n"
        "v_mfma_f32_16x16x32_bf16 a[196:199], v[68:71], v[24:27], a[196:199]\n"
        "global_load_lds_dwordx4 v240, s[60:61] offset:2048\n"
        "s_add_u32 s82, s12, 0x500000\n"
        "s_addc_u32 s83, s13, 0\n"
        "v_mfma_f32_16x16x32_bf16 a[200:203], v[72:75], v[24:27], a[200:203]\n"
        "v_add_f32 v193, v193, v194\n"
        "v_add_f32 v195, v195, v196\n"
        "v_mfma_f32_16x16x32_bf16 a[204:207], v[76:79], v[24:27], a[204:207]\n"
        "v_add_f32 v189, v189, v190\n"
        "v_add_f32 v191, v191, v192\n"
        "v_mfma_f32_16x16x32_bf16 a[208:211], v[24:27], v[80:83], a[208:211]\n"
        "v_mul_f32 v194, v179, v185\n"
        "v_mul_f32 v196, v180, v185\n"
        "v_mfma_f32_16x16x32_bf16 a[212:215], v[24:27], v[84:87], a[212:215]\n"
        "global_load_lds_dwordx4 v241, s[60:61] offset:3072\n"
        "s_add_u32 m0, s73, 0x1c000\n"
        "v_cvt_pk_bf16_f32 v193, 0, v193\n"
        "v_mfma_f32_16x16x32_bf16 a[216:219], v[24:27], v[88:91], a[216:219]\n"
        "v_cvt_pk_bf16_f32 v195, 0, v195\n"
        "v_mul_f32 v190, v177, v185\n"
        "v_mfma_f32_16x16x32_bf16 a[220:223], v[24:27], v[92:95], a[220:223]\n"
        "ds_read_b128 v[24:27], v224 offset:45056\n"
        "v_mul_f32 v192, v178, v185\n"
        "v_mfma_f32_16x16x32_bf16 a[224:227], v[64:67], v[28:31], a[224:227]\n"
        "ds_read_b64_tr_b16 v[64:65], v216 offset:8192\n"
        "v_cvt_pk_bf16_f32 v194, 0, v194\n"
        "v_mfma_f32_16x16x32_bf16 a[228:231], v[68:71], v[28:31], a[228:231]\n"
        "global_load_lds_dwordx4 v238, s[62:63] offset:0\n"
        "ds_read_b64_tr_b16 v[66:67], v217 offset:8192\n"
        "v_mfma_f32_16x16x32_bf16 a[232:235], v[72:75], v[28:31], a[232:235]\n"
        "ds_read_b64_tr_b16 v[68:69], v218 offset:8192\n"
        "v_cvt_pk_bf16_f32 v196, 0, v196\n"
        "v_mfma_f32_16x16x32_bf16 a[236:239], v[76:79], v[28:31], a[236:239]\n"
        "ds_read_b64_tr_b16 v[70:71], v219 offset:8192\n"
        "v_cvt_pk_bf16_f32 v189, 0, v189\n"
        "v_mfma_f32_16x16x32_bf16 a[240:243], v[28:31], v[80:83], a[240:243]\n"
        "ds_read_b64_tr_b16 v[72:73], v220 offset:8192\n"
        "v_cvt_pk_bf16_f32 v191, 0, v191\n"
        "v_mfma_f32_16x16x32_bf16 a[244:247], v[28:31], v[84:87], a[244:247]\n"
        "global_load_lds_dwordx4 v239, s[62:63] offset:1024\n"
        "ds_read_b64_tr_b16 v[74:75], v221 offset:8192\n"
        "v_mfma_f32_16x16x32_bf16 a[248:251], v[28:31], v[88:91], a[248:251]\n"
        "ds_read_b64_tr_b16 v[76:77], v222 offset:8192\n"
        "v_cvt_pk_bf16_f32 v190, 0, v190\n"
        "v_mfma_f32_16x16x32_bf16 a[252:255], v[28:31], v[92:95], a[252:255]\n"
        "ds_read_b128 v[28:31], v224 offset:47104\n"
        "s_waitcnt lgkmcnt(8)\n"
        "v_mfma_f32_16x16x32_bf16 a[0:3], v[32:35], v[0:3], a[0:3]\n"
        "ds_read_b64_tr_b16 v[78:79], v223 offset:8192\n"
        "v_cvt_pk_bf16_f32 v192, 0, v192\n"
        "v_mfma_f32_16x16x32_bf16 a[4:7], v[36:39], v[0:3], a[4:7]\n"
        "global_load_lds_dwordx4 v240, s[62:63] offset:2048\n"
        "ds_read_b64_tr_b16 v[80:81], v216 offset:24576\n"
        "v_mfma_f32_16x16x32_bf16 a[8:11], v[40:43], v[0:3], a[8:11]\n"
        "ds_read_b64_tr_b16 v[82:83], v217 offset:24576\n"
        "v_add_f32 v193, v193, v194\n"
        "v_mfma_f32_16x16x32_bf16 a[12:15], v[44:47], v[0:3], a[12:15]\n"
        "ds_read_b64_tr_b16 v[84:85], v218 offset:24576\n"
        "v_add_f32 v195, v195, v196\n"
        "v_mfma_f32_16x16x32_bf16 a[16:19], v[0:3], v[48:51], a[16:19]\n"
        "ds_read_b64_tr_b16 v[86:87], v219 offset:24576\n"
        "v_add_f32 v189, v189, v190\n"
        "v_mfma_f32_16x16x32_bf16 a[20:23], v[0:3], v[52:55], a[20:23]\n"
        "global_load_lds_dwordx4 v241, s[62:63] offset:3072\n"
        "ds_read_b64_tr_b16 v[88:89], v220 offset:24576\n"
        "s_add_u32 m0, s72, 0x8000\n"
        "v_mfma_f32_16x16x32_bf16 a[24:27], v[0:3], v[56:59], a[24:27]\n"
        "ds_read_b64_tr_b16 v[90:91], v221 offset:24576\n"
        "v_add_f32 v191, v191, v192\n"
        "v_mfma_f32_16x16x32_bf16 a[28:31], v[0:3], v[60:63], a[28:31]\n"
        "ds_read_b128 v[0:3], v225 offset:32768\n"
        "v_cvt_pk_bf16_f32 v193, 0, v193\n"
        "v_mfma_f32_16x16x32_bf16 a[32:35], v[32:35], v[4:7], a[32:35]\n"
        "ds_read_b64_tr_b16 v[92:93], v222 offset:24576\n"
        "v_lshlrev_b32 v194, 16, v123\n"
        "v_mfma_f32_16x16x32_bf16 a[36:39], v[36:39], v[4:7], a[36:39]\n"
        "ds_read_b64_tr_b16 v[94:95], v223 offset:24576\n"
        "v_cvt_pk_bf16_f32 v195, 0, v195\n"
        "v_mfma_f32_16x16x32_bf16 a[40:43], v[40:43], v[4:7], a[40:43]\n"
        "v_lshlrev_b32 v196, 16, v125\n"
        "v_cvt_pk_bf16_f32 v189, 0, v189\n"
        "v_mfma_f32_16x16x32_bf16 a[44:47], v[44:47], v[4:7], a[44:47]\n"
        "global_store_dwordx4 v171, v[200:203], s[76:77] offset:-4096 sc1\n"
        "v_lshlrev_b32 v190, 16, v119\n"
        "v_cvt_pk_bf16_f32 v191, 0, v191\n"
        "v_lshlrev_b32 v192, 16, v121\n"
        "v_mul_f32 v193, v193, v194\n"
        "v_mfma_f32_16x16x32_bf16 a[48:51], v[4:7], v[48:51], a[48:51]\n"
        "v_mul_f32 v195, v195, v196\n"
        "v_lshlrev_b32 v181, 16, v111\n"
        "v_mfma_f32_16x16x32_bf16 a[52:55], v[4:7], v[52:55], a[52:55]\n"
        "v_lshlrev_b32 v182, 16, v113\n"
        "v_mul_f32 v189, v189, v190\n"
        "v_mfma_f32_16x16x32_bf16 a[56:59], v[4:7], v[56:59], a[56:59]\n"
        "v_mul_f32 v191, v191, v192\n"
        "v_cvt_pk_bf16_f32 v202, v195, v193\n"
        "v_mfma_f32_16x16x32_bf16 a[60:63], v[4:7], v[60:63], a[60:63]\n"
        "ds_read_b128 v[4:7], v225 offset:34816\n"
        "v_mul_f32 v194, v181, v187\n"
        "v_mfma_f32_16x16x32_bf16 a[64:67], v[32:35], v[8:11], a[64:67]\n"
        "v_mul_f32 v196, v182, v187\n"
        "v_cvt_pk_bf16_f32 v203, v191, v189\n"
        "v_mfma_f32_16x16x32_bf16 a[68:71], v[36:39], v[8:11], a[68:71]\n"
        "v_mul_f32 v193, v180, v188\n"
        "v_mul_f32 v195, v181, v188\n"
        "v_mfma_f32_16x16x32_bf16 a[72:75], v[40:43], v[8:11], a[72:75]\n"
        "v_mul_f32 v190, v179, v187\n"
        "v_mul_f32 v192, v180, v187\n"
        "v_mfma_f32_16x16x32_bf16 a[76:79], v[44:47], v[8:11], a[76:79]\n"
        "v_cvt_pk_bf16_f32 v194, 0, v194\n"
        "v_cvt_pk_bf16_f32 v196, 0, v196\n"
        "v_mfma_f32_16x16x32_bf16 a[80:83], v[8:11], v[48:51], a[80:83]\n"
        "v_mul_f32 v189, v178, v188\n"
        "v_mul_f32 v191, v179, v188\n"
        "v_mfma_f32_16x16x32_bf16 a[84:87], v[8:11], v[52:55], a[84:87]\n"
        "v_cvt_pk_bf16_f32 v193, 0, v193\n"
        "v_cvt_pk_bf16_f32 v195, 0, v195\n"
        "v_mfma_f32_16x16x32_bf16 a[88:91], v[8:11], v[56:59], a[88:91]\n"
        "v_lshlrev_b32 v183, 16, v115\n"
        "v_cvt_pk_bf16_f32 v190, 0, v190\n"
        "v_mfma_f32_16x16x32_bf16 a[92:95], v[8:11], v[60:63], a[92:95]\n"
        "ds_read_b128 v[8:11], v225 offset:36864\n"
        "v_cvt_pk_bf16_f32 v192, 0, v192\n"
        "v_mfma_f32_16x16x32_bf16 a[96:99], v[32:35], v[12:15], a[96:99]\n"
        "v_cvt_pk_bf16_f32 v189, 0, v189\n"
        "v_cvt_pk_bf16_f32 v191, 0, v191\n"
        "v_mfma_f32_16x16x32_bf16 a[100:103], v[36:39], v[12:15], a[100:103]\n"
        "v_add_f32 v193, v193, v194\n"
        "v_add_f32 v195, v195, v196\n"
        "v_mfma_f32_16x16x32_bf16 a[104:107], v[40:43], v[12:15], a[104:107]\n"
        "v_add_f32 v189, v189, v190\n"
        "v_add_f32 v191, v191, v192\n"
        "v_mfma_f32_16x16x32_bf16 a[108:111], v[44:47], v[12:15], a[108:111]\n"
        "v_mul_f32 v194, v182, v186\n"
        "v_mul_f32 v196, v183, v186\n"
        "v_mfma_f32_16x16x32_bf16 a[112:115], v[12:15], v[48:51], a[112:115]\n"
        "v_cvt_pk_bf16_f32 v193, 0, v193\n"
        "v_cvt_pk_bf16_f32 v195, 0, v195\n"
        "v_mfma_f32_16x16x32_bf16 a[116:119], v[12:15], v[52:55], a[116:119]\n"
        "v_mul_f32 v190, v180, v186\n"
        "v_mul_f32 v192, v181, v186\n"
        "v_mfma_f32_16x16x32_bf16 a[120:123], v[12:15], v[56:59], a[120:123]\n"
        "v_cvt_pk_bf16_f32 v194, 0, v194\n"
        "v_cvt_pk_bf16_f32 v196, 0, v196\n"
        "v_mfma_f32_16x16x32_bf16 a[124:127], v[12:15], v[60:63], a[124:127]\n"
        "ds_read_b128 v[12:15], v225 offset:38912\n"
        "v_lshlrev_b32 v184, 16, v117\n"
        "v_mfma_f32_16x16x32_bf16 a[128:131], v[32:35], v[16:19], a[128:131]\n"
        "v_cvt_pk_bf16_f32 v189, 0, v189\n"
        "v_cvt_pk_bf16_f32 v191, 0, v191\n"
        "v_mfma_f32_16x16x32_bf16 a[132:135], v[36:39], v[16:19], a[132:135]\n"
        "v_cvt_pk_bf16_f32 v190, 0, v190\n"
        "v_cvt_pk_bf16_f32 v192, 0, v192\n"
        "v_mfma_f32_16x16x32_bf16 a[136:139], v[40:43], v[16:19], a[136:139]\n"
        "v_add_f32 v193, v193, v194\n"
        "v_add_f32 v195, v195, v196\n"
        "v_mfma_f32_16x16x32_bf16 a[140:143], v[44:47], v[16:19], a[140:143]\n"
        "v_add_f32 v189, v189, v190\n"
        "v_add_f32 v191, v191, v192\n"
        "v_mfma_f32_16x16x32_bf16 a[144:147], v[16:19], v[48:51], a[144:147]\n"
        "v_mul_f32 v194, v183, v185\n"
        "v_mul_f32 v196, v184, v185\n"
        "v_mfma_f32_16x16x32_bf16 a[148:151], v[16:19], v[52:55], a[148:151]\n"
        "v_cvt_pk_bf16_f32 v193, 0, v193\n"
        "v_cvt_pk_bf16_f32 v195, 0, v195\n"
        "v_mfma_f32_16x16x32_bf16 a[152:155], v[16:19], v[56:59], a[152:155]\n"
        "v_mul_f32 v190, v181, v185\n"
        "v_mul_f32 v192, v182, v185\n"
        "v_mfma_f32_16x16x32_bf16 a[156:159], v[16:19], v[60:63], a[156:159]\n"
        "ds_read_b128 v[16:19], v225 offset:40960\n"
        "v_cvt_pk_bf16_f32 v194, 0, v194\n"
        "v_mfma_f32_16x16x32_bf16 a[160:163], v[32:35], v[20:23], a[160:163]\n"
        "v_cvt_pk_bf16_f32 v196, 0, v196\n"
        "v_cvt_pk_bf16_f32 v189, 0, v189\n"
        "v_mfma_f32_16x16x32_bf16 a[164:167], v[36:39], v[20:23], a[164:167]\n"
        "v_cvt_pk_bf16_f32 v191, 0, v191\n"
        "v_cvt_pk_bf16_f32 v190, 0, v190\n"
        "v_mfma_f32_16x16x32_bf16 a[168:171], v[40:43], v[20:23], a[168:171]\n"
        "v_cvt_pk_bf16_f32 v192, 0, v192\n"
        "v_add_f32 v193, v193, v194\n"
        "v_mfma_f32_16x16x32_bf16 a[172:175], v[44:47], v[20:23], a[172:175]\n"
        "v_add_f32 v195, v195, v196\n"
        "v_add_f32 v189, v189, v190\n"
        "v_mfma_f32_16x16x32_bf16 a[176:179], v[20:23], v[48:51], a[176:179]\n"
        "v_add_f32 v191, v191, v192\n"
        "v_cvt_pk_bf16_f32 v193, 0, v193\n"
        "v_mfma_f32_16x16x32_bf16 a[180:183], v[20:23], v[52:55], a[180:183]\n"
        "v_lshlrev_b32 v194, 16, v131\n"
        "v_cvt_pk_bf16_f32 v195, 0, v195\n"
        "v_mfma_f32_16x16x32_bf16 a[184:187], v[20:23], v[56:59], a[184:187]\n"
        "v_lshlrev_b32 v196, 16, v133\n"
        "v_cvt_pk_bf16_f32 v189, 0, v189\n"
        "v_mfma_f32_16x16x32_bf16 a[188:191], v[20:23], v[60:63], a[188:191]\n"
        "ds_read_b128 v[20:23], v225 offset:43008\n"
        "v_lshlrev_b32 v190, 16, v127\n"
        "v_mfma_f32_16x16x32_bf16 a[192:195], v[32:35], v[24:27], a[192:195]\n"
        "v_cvt_pk_bf16_f32 v191, 0, v191\n"
        "v_lshlrev_b32 v192, 16, v129\n"
        "v_mfma_f32_16x16x32_bf16 a[196:199], v[36:39], v[24:27], a[196:199]\n"
        "v_mul_f32 v193, v193, v194\n"
        "v_mul_f32 v195, v195, v196\n"
        "v_mfma_f32_16x16x32_bf16 a[200:203], v[40:43], v[24:27], a[200:203]\n"
        "s_waitcnt lgkmcnt(1)\n"
        "v_and_b32 v176, 0xffff0000, v101\n"
        "v_and_b32 v177, 0xffff0000, v103\n"
        "v_mfma_f32_16x16x32_bf16 a[204:207], v[44:47], v[24:27], a[204:207]\n"
        "v_lshlrev_b32 v187, 16, v141\n"
        "v_and_b32 v178, 0xffff0000, v105\n"
        "v_mfma_f32_16x16x32_bf16 a[208:211], v[24:27], v[48:51], a[208:211]\n"
        "v_and_b32 v188, 0xffff0000, v141\n"
        "v_mul_f32 v189, v189, v190\n"
        "v_mfma_f32_16x16x32_bf16 a[212:215], v[24:27], v[52:55], a[212:215]\n"
        "v_mul_f32 v191, v191, v192\n"
        "v_cvt_pk_bf16_f32 v200, v195, v193\n"
        "v_mfma_f32_16x16x32_bf16 a[216:219], v[24:27], v[56:59], a[216:219]\n"
        "v_and_b32 v174, 0xffff0000, v97\n"
        "v_and_b32 v175, 0xffff0000, v99\n"
        "v_mfma_f32_16x16x32_bf16 a[220:223], v[24:27], v[60:63], a[220:223]\n"
        "ds_read_b128 v[24:27], v225 offset:45056\n"
        "global_load_dword v253, v173, s[82:83] offset:0\n"
        "v_mfma_f32_16x16x32_bf16 a[224:227], v[32:35], v[28:31], a[224:227]\n"
        "s_add_u32 s82, s12, 0x600000\n"
        "s_addc_u32 s83, s13, 0\n"
        "v_mul_f32 v194, v177, v187\n"
        "v_mfma_f32_16x16x32_bf16 a[228:231], v[36:39], v[28:31], a[228:231]\n"
        "v_mul_f32 v196, v178, v187\n"
        "v_cvt_pk_bf16_f32 v201, v191, v189\n"
        "v_mfma_f32_16x16x32_bf16 a[232:235], v[40:43], v[28:31], a[232:235]\n"
        "v_mul_f32 v193, v176, v188\n"
        "v_mul_f32 v195, v177, v188\n"
        "v_mfma_f32_16x16x32_bf16 a[236:239], v[44:47], v[28:31], a[236:239]\n"
        "v_mul_f32 v192, v176, v187\n"
        "v_mul_f32 v190, v175, v187\n"
        "v_mfma_f32_16x16x32_bf16 a[240:243], v[28:31], v[48:51], a[240:243]\n"
        "v_cvt_pk_bf16_f32 v194, 0, v194\n"
        "v_cvt_pk_bf16_f32 v196, 0, v196\n"
        "v_mfma_f32_16x16x32_bf16 a[244:247], v[28:31], v[52:55], a[244:247]\n"
        "v_mul_f32 v189, v174, v188\n"
        "v_mul_f32 v191, v175, v188\n"
        "v_mfma_f32_16x16x32_bf16 a[248:251], v[28:31], v[56:59], a[248:251]\n"
        "v_cvt_pk_bf16_f32 v193, 0, v193\n"
        "v_cvt_pk_bf16_f32 v195, 0, v195\n"
        "v_mfma_f32_16x16x32_bf16 a[252:255], v[28:31], v[60:63], a[252:255]\n"
        "ds_read_b128 v[28:31], v225 offset:47104\n"
        "v_and_b32 v179, 0xffff0000, v107\n"
        "v_mfma_f32_16x16x32_bf16 a[0:3], v[64:67], v[0:3], a[0:3]\n"
        "v_and_b32 v186, 0xffff0000, v140\n"
        "v_cvt_pk_bf16_f32 v192, 0, v192\n"
        "v_mfma_f32_16x16x32_bf16 a[4:7], v[68:71], v[0:3], a[4:7]\n"
        "v_cvt_pk_bf16_f32 v190, 0, v190\n"
        "v_cvt_pk_bf16_f32 v189, 0, v189\n"
        "v_mfma_f32_16x16x32_bf16 a[8:11], v[72:75], v[0:3], a[8:11]\n"
        "v_cvt_pk_bf16_f32 v191, 0, v191\n"
        "v_add_f32 v193, v193, v194\n"
        "v_mfma_f32_16x16x32_bf16 a[12:15], v[76:79], v[0:3], a[12:15]\n"
        "v_add_f32 v195, v195, v196\n"
        "v_add_f32 v189, v189, v190\n"
        "v_mfma_f32_16x16x32_bf16 a[16:19], v[0:3], v[80:83], a[16:19]\n"
        "v_add_f32 v191, v191, v192\n"
        "v_mul_f32 v194, v178, v186\n"
        "v_mfma_f32_16x16x32_bf16 a[20:23], v[0:3], v[84:87], a[20:23]\n"
        "v_mul_f32 v196, v179, v186\n"
        "v_cvt_pk_bf16_f32 v193, 0, v193\n"
        "v_mfma_f32_16x16x32_bf16 a[24:27], v[0:3], v[88:91], a[24:27]\n"
        "v_cvt_pk_bf16_f32 v195, 0, v195\n"
        "v_mul_f32 v190, v176, v186\n"
        "v_mfma_f32_16x16x32_bf16 a[28:31], v[0:3], v[92:95], a[28:31]\n"
        "s_waitcnt vmcnt(11) lgkmcnt(0)\n"
        "s_barrier\n"
        "v_mfma_f32_16x16x32_bf16 a[32:35], v[64:67], v[4:7], a[32:35]\n"
        "global_load_lds_dwordx4 v234, s[56:57] offset:0\n"
        "ds_read_b128 v[0:3], v224 offset:0\n"
        "v_mfma_f32_16x16x32_bf16 a[36:39], v[68:71], v[4:7], a[36:39]\n"
        "ds_read_b64_tr_b16 v[32:33], v226 offset:0\n"
        "v_mfma_f32_16x16x32_bf16 a[40:43], v[72:75], v[4:7], a[40:43]\n"
        "ds_read_b64_tr_b16 v[34:35], v227 offset:0\n"
        "v_mfma_f32_16x16x32_bf16 a[44:47], v[76:79], v[4:7], a[44:47]\n"
        "ds_read_b64_tr_b16 v[36:37], v228 offset:0\n"
        "v_mfma_f32_16x16x32_bf16 a[48:51], v[4:7], v[80:83], a[48:51]\n"
        "ds_read_b64_tr_b16 v[38:39], v229 offset:0\n"
        "global_load_lds_dwordx4 v235, s[56:57] offset:1024\n"
        "v_mfma_f32_16x16x32_bf16 a[52:55], v[4:7], v[84:87], a[52:55]\n"
        "ds_read_b64_tr_b16 v[40:41], v230 offset:0\n"
        "v_mfma_f32_16x16x32_bf16 a[56:59], v[4:7], v[88:91], a[56:59]\n"
        "ds_read_b64_tr_b16 v[42:43], v231 offset:0\n"
        "v_mfma_f32_16x16x32_bf16 a[60:63], v[4:7], v[92:95], a[60:63]\n"
        "ds_read_b128 v[4:7], v224 offset:2048\n"
        "v_mfma_f32_16x16x32_bf16 a[64:67], v[64:67], v[8:11], a[64:67]\n"
        "ds_read_b64_tr_b16 v[44:45], v232 offset:0\n"
        "global_load_lds_dwordx4 v236, s[56:57] offset:2048\n"
        "v_mfma_f32_16x16x32_bf16 a[68:71], v[68:71], v[8:11], a[68:71]\n"
        "ds_read_b64_tr_b16 v[46:47], v233 offset:0\n"
        "v_mul_f32 v192, v177, v186\n"
        "v_mfma_f32_16x16x32_bf16 a[72:75], v[72:75], v[8:11], a[72:75]\n"
        "ds_read_b64_tr_b16 v[48:49], v226 offset:16384\n"
        "v_cvt_pk_bf16_f32 v194, 0, v194\n"
        "v_mfma_f32_16x16x32_bf16 a[76:79], v[76:79], v[8:11], a[76:79]\n"
        "ds_read_b64_tr_b16 v[50:51], v227 offset:16384\n"
        "v_cvt_pk_bf16_f32 v196, 0, v196\n"
        "v_mfma_f32_16x16x32_bf16 a[80:83], v[8:11], v[80:83], a[80:83]\n"
        "global_load_lds_dwordx4 v237, s[56:57] offset:3072\n"
        "s_add_u32 m0, s72, 0x9000\n"
        "v_mfma_f32_16x16x32_bf16 a[84:87], v[8:11], v[84:87], a[84:87]\n"
        "ds_read_b64_tr_b16 v[52:53], v228 offset:16384\n"
        "v_and_b32 v180, 0xffff0000, v109\n"
        "v_mfma_f32_16x16x32_bf16 a[88:91], v[8:11], v[88:91], a[88:91]\n"
        "ds_read_b64_tr_b16 v[54:55], v229 offset:16384\n"
        "v_lshlrev_b32 v185, 16, v140\n"
        "v_mfma_f32_16x16x32_bf16 a[92:95], v[8:11], v[92:95], a[92:95]\n"
        "ds_read_b128 v[8:11], v224 offset:4096\n"
        "v_cvt_pk_bf16_f32 v189, 0, v189\n"
        "v_mfma_f32_16x16x32_bf16 a[96:99], v[64:67], v[12:15], a[96:99]\n"
        "global_load_lds_dwordx4 v234, s[58:59] offset:0\n"
        "v_mfma_f32_16x16x32_bf16 a[100:103], v[68:71], v[12:15], a[100:103]\n"
        "ds_read_b64_tr_b16 v[56:57], v230 offset:16384\n"
        "v_cvt_pk_bf16_f32 v191, 0, v191\n"
        "v_mfma_f32_16x16x32_bf16 a[104:107], v[72:75], v[12:15], a[104:107]\n"
        "ds_read_b64_tr_b16 v[58:59], v231 offset:16384\n"
        "v_cvt_pk_bf16_f32 v190, 0, v190\n"
        "v_mfma_f32_16x16x32_bf16 a[108:111], v[76:79], v[12:15], a[108:111]\n"
        "ds_read_b64_tr_b16 v[60:61], v232 offset:16384\n"
        "v_cvt_pk_bf16_f32 v192, 0, v192\n"
        "v_mfma_f32_16x16x32_bf16 a[112:115], v[12:15], v[80:83], a[112:115]\n"
        "global_load_lds_dwordx4 v235, s[58:59] offset:1024\n"
        "v_mfma_f32_16x16x32_bf16 a[116:119], v[12:15], v[84:87], a[116:119]\n"
        "ds_read_b64_tr_b16 v[62:63], v233 offset:16384\n"
        "v_add_f32 v193, v193, v194\n"
        "v_mfma_f32_16x16x32_bf16 a[120:123], v[12:15], v[88:91], a[120:123]\n"
        "v_add_f32 v195, v195, v196\n"
        "v_add_f32 v189, v189, v190\n"
        "v_mfma_f32_16x16x32_bf16 a[124:127], v[12:15], v[92:95], a[124:127]\n"
        "ds_read_b128 v[12:15], v224 offset:6144\n"
        "v_add_f32 v191, v191, v192\n"
        "v_mfma_f32_16x16x32_bf16 a[128:131], v[64:67], v[16:19], a[128:131]\n"
        "global_load_lds_dwordx4 v236, s[58:59] offset:2048\n"
        "v_mfma_f32_16x16x32_bf16 a[132:135], v[68:71], v[16:19], a[132:135]\n"
        "v_mul_f32 v194, v179, v185\n"
        "v_mul_f32 v196, v180, v185\n"
        "v_mfma_f32_16x16x32_bf16 a[136:139], v[72:75], v[16:19], a[136:139]\n"
        "v_cvt_pk_bf16_f32 v193, 0, v193\n"
        "v_cvt_pk_bf16_f32 v195, 0, v195\n"
        "v_mfma_f32_16x16x32_bf16 a[140:143], v[76:79], v[16:19], a[140:143]\n"
        "v_mul_f32 v190, v177, v185\n"
        "v_mul_f32 v192, v178, v185\n"
        "v_mfma_f32_16x16x32_bf16 a[144:147], v[16:19], v[80:83], a[144:147]\n"
        "global_load_lds_dwordx4 v237, s[58:59] offset:3072\n"
        "s_add_u32 m0, s73, 0x20000\n"
        "v_mfma_f32_16x16x32_bf16 a[148:151], v[16:19], v[84:87], a[148:151]\n"
        "v_cvt_pk_bf16_f32 v194, 0, v194\n"
        "v_cvt_pk_bf16_f32 v196, 0, v196\n"
        "v_mfma_f32_16x16x32_bf16 a[152:155], v[16:19], v[88:91], a[152:155]\n"
        "v_cvt_pk_bf16_f32 v189, 0, v189\n"
        "v_cvt_pk_bf16_f32 v191, 0, v191\n"
        "v_mfma_f32_16x16x32_bf16 a[156:159], v[16:19], v[92:95], a[156:159]\n"
        "ds_read_b128 v[16:19], v224 offset:8192\n"
        "v_cvt_pk_bf16_f32 v190, 0, v190\n"
        "v_mfma_f32_16x16x32_bf16 a[160:163], v[64:67], v[20:23], a[160:163]\n"
        "global_load_lds_dwordx4 v238, s[68:69] offset:0\n"
        "v_mfma_f32_16x16x32_bf16 a[164:167], v[68:71], v[20:23], a[164:167]\n"
        "v_cvt_pk_bf16_f32 v192, 0, v192\n"
        "v_add_f32 v193, v193, v194\n"
        "v_mfma_f32_16x16x32_bf16 a[168:171], v[72:75], v[20:23], a[168:171]\n"
        "v_add_f32 v195, v195, v196\n"
        "v_add_f32 v189, v189, v190\n"
        "v_mfma_f32_16x16x32_bf16 a[172:175], v[76:79], v[20:23], a[172:175]\n"
        "v_add_f32 v191, v191, v192\n"
        "v_cvt_pk_bf16_f32 v193, 0, v193\n"
        "v_mfma_f32_16x16x32_bf16 a[176:179], v[20:23], v[80:83], a[176:179]\n"
        "global_load_lds_dwordx4 v239, s[68:69] offset:1024\n"
        "v_mfma_f32_16x16x32_bf16 a[180:183], v[20:23], v[84:87], a[180:183]\n"
        "v_and_b32 v194, 0xffff0000, v123\n"
        "v_cvt_pk_bf16_f32 v195, 0, v195\n"
        "v_mfma_f32_16x16x32_bf16 a[184:187], v[20:23], v[88:91], a[184:187]\n"
        "v_and_b32 v196, 0xffff0000, v125\n"
        "v_cvt_pk_bf16_f32 v189, 0, v189\n"
        "v_mfma_f32_16x16x32_bf16 a[188:191], v[20:23], v[92:95], a[188:191]\n"
        "ds_read_b128 v[20:23], v224 offset:10240\n"
        "v_and_b32 v190, 0xffff0000, v119\n"
        "v_mfma_f32_16x16x32_bf16 a[192:195], v[64:67], v[24:27], a[192:195]\n"
        "global_load_lds_dwordx4 v240, s[68:69] offset:2048\n"
        "v_mfma_f32_16x16x32_bf16 a[196:199], v[68:71], v[24:27], a[196:199]\n"
        "v_cvt_pk_bf16_f32 v191, 0, v191\n"
        "v_and_b32 v192, 0xffff0000, v121\n"
        "v_mfma_f32_16x16x32_bf16 a[200:203], v[72:75], v[24:27], a[200:203]\n"
        "v_mul_f32 v193, v193, v194\n"
        "v_mul_f32 v195, v195, v196\n"
        "v_mfma_f32_16x16x32_bf16 a[204:207], v[76:79], v[24:27], a[204:207]\n"
        "v_and_b32 v181, 0xffff0000, v111\n"
        "v_and_b32 v182, 0xffff0000, v113\n"
        "v_mfma_f32_16x16x32_bf16 a[208:211], v[24:27], v[80:83], a[208:211]\n"
        "global_load_lds_dwordx4 v241, s[68:69] offset:3072\n"
        "s_add_u32 m0, s73, 0x24000\n"
        "v_mfma_f32_16x16x32_bf16 a[212:215], v[24:27], v[84:87], a[212:215]\n"
        "s_add_u32 s68, s68, 0x80000\n"
        "s_addc_u32 s69, s69, 0\n"
        "v_mul_f32 v189, v189, v190\n"
        "v_mfma_f32_16x16x32_bf16 a[216:219], v[24:27], v[88:91], a[216:219]\n"
        "v_mul_f32 v191, v191, v192\n"
        "v_mul_f32 v194, v181, v187\n"
        "v_mfma_f32_16x16x32_bf16 a[220:223], v[24:27], v[92:95], a[220:223]\n"
        "ds_read_b128 v[24:27], v224 offset:12288\n"
        "v_mul_f32 v196, v182, v187\n"
        "v_mfma_f32_16x16x32_bf16 a[224:227], v[64:67], v[28:31], a[224:227]\n"
        "global_load_lds_dwordx4 v238, s[68:69] offset:0\n"
        "v_mfma_f32_16x16x32_bf16 a[228:231], v[68:71], v[28:31], a[228:231]\n"
        "ds_read_b64_tr_b16 v[64:65], v226 offset:8192\n"
        "v_mul_f32 v192, v180, v187\n"
        "v_mfma_f32_16x16x32_bf16 a[232:235], v[72:75], v[28:31], a[232:235]\n"
        "ds_read_b64_tr_b16 v[66:67], v227 offset:8192\n"
        "v_cvt_pk_bf16_f32 v194, 0, v194\n"
        "v_mfma_f32_16x16x32_bf16 a[236:239], v[76:79], v[28:31], a[236:239]\n"
        "ds_read_b64_tr_b16 v[68:69], v228 offset:8192\n"
        "v_cvt_pk_bf16_f32 v196, 0, v196\n"
        "v_mfma_f32_16x16x32_bf16 a[240:243], v[28:31], v[80:83], a[240:243]\n"
        "global_load_lds_dwordx4 v239, s[68:69] offset:1024\n"
        "v_mfma_f32_16x16x32_bf16 a[244:247], v[28:31], v[84:87], a[244:247]\n"
        "ds_read_b64_tr_b16 v[70:71], v229 offset:8192\n"
        "v_and_b32 v183, 0xffff0000, v115\n"
        "v_mfma_f32_16x16x32_bf16 a[248:251], v[28:31], v[88:91], a[248:251]\n"
        "ds_read_b64_tr_b16 v[72:73], v230 offset:8192\n"
        "v_cvt_pk_bf16_f32 v192, 0, v192\n"
        "v_mfma_f32_16x16x32_bf16 a[252:255], v[28:31], v[92:95], a[252:255]\n"
        "ds_read_b128 v[28:31], v224 offset:14336\n"
        "s_waitcnt lgkmcnt(6)\n"
        "v_mfma_f32_16x16x32_bf16 a[0:3], v[32:35], v[0:3], a[0:3]\n"
        "global_load_lds_dwordx4 v240, s[68:69] offset:2048\n"
        "v_mfma_f32_16x16x32_bf16 a[4:7], v[36:39], v[0:3], a[4:7]\n"
        "ds_read_b64_tr_b16 v[74:75], v231 offset:8192\n"
        "v_mfma_f32_16x16x32_bf16 a[8:11], v[40:43], v[0:3], a[8:11]\n"
        "ds_read_b64_tr_b16 v[76:77], v232 offset:8192\n"
        "s_and_b32 s53, s40, 1\n"
        "s_lshl_b32 s53, s53, 10\n"
        "v_mfma_f32_16x16x32_bf16 a[12:15], v[44:47], v[0:3], a[12:15]\n"
        "ds_read_b64_tr_b16 v[78:79], v233 offset:8192\n"
        "s_sub_u32 s53, 0x8000, s53\n"
        "v_and_b32 v184, 0xffff0000, v117\n"
        "v_mfma_f32_16x16x32_bf16 a[16:19], v[0:3], v[48:51], a[16:19]\n"
        "global_load_lds_dwordx4 v241, s[68:69] offset:3072\n"
        "s_add_u32 m0, s72, 0x0\n"
        "v_mfma_f32_16x16x32_bf16 a[20:23], v[0:3], v[52:55], a[20:23]\n"
        "s_add_u32 s68, s68, 0x80000\n"
        "s_addc_u32 s69, s69, 0\n"
        "v_mul_f32 v190, v179, v187\n"
        "v_mfma_f32_16x16x32_bf16 a[24:27], v[0:3], v[56:59], a[24:27]\n"
        "ds_read_b64_tr_b16 v[80:81], v226 offset:24576\n"
        "v_cvt_pk_bf16_f32 v190, 0, v190\n"
        "v_mfma_f32_16x16x32_bf16 a[28:31], v[0:3], v[60:63], a[28:31]\n"
        "ds_read_b128 v[0:3], v225 offset:0\n"
        "v_mfma_f32_16x16x32_bf16 a[32:35], v[32:35], v[4:7], a[32:35]\n"
        "global_load_dword v253, v173, s[82:83] offset:0\n"
        "v_mfma_f32_16x16x32_bf16 a[36:39], v[36:39], v[4:7], a[36:39]\n"
        "ds_read_b64_tr_b16 v[82:83], v227 offset:24576\n"
        "s_add_u32 s82, s12, 0x700000\n"
        "s_addc_u32 s83, s13, 0\n"
        "v_mfma_f32_16x16x32_bf16 a[40:43], v[40:43], v[4:7], a[40:43]\n"
        "ds_read_b64_tr_b16 v[84:85], v228 offset:24576\n"
        "v_mfma_f32_16x16x32_bf16 a[44:47], v[44:47], v[4:7], a[44:47]\n"
        "ds_read_b64_tr_b16 v[86:87], v229 offset:24576\n"
        "v_mfma_f32_16x16x32_bf16 a[48:51], v[4:7], v[48:51], a[48:51]\n"
        "ds_read_b64_tr_b16 v[88:89], v230 offset:24576\n"
        "v_mfma_f32_16x16x32_bf16 a[52:55], v[4:7], v[52:55], a[52:55]\n"
        "ds_read_b64_tr_b16 v[90:91], v231 offset:24576\n"
        "v_mfma_f32_16x16x32_bf16 a[56:59], v[4:7], v[56:59], a[56:59]\n"
        "ds_read_b64_tr_b16 v[92:93], v232 offset:24576\n"
        "v_mfma_f32_16x16x32_bf16 a[60:63], v[4:7], v[60:63], a[60:63]\n"
        "ds_read_b128 v[4:7], v225 offset:2048\n"
        "v_mfma_f32_16x16x32_bf16 a[64:67], v[32:35], v[8:11], a[64:67]\n"
        "ds_read_b64_tr_b16 v[94:95], v233 offset:24576\n"
        "v_mfma_f32_16x16x32_bf16 a[68:71], v[36:39], v[8:11], a[68:71]\n"
        "global_store_dwordx4 v171, v[208:211], s[76:77] offset:0 sc1\n"
        "s_add_u32 s76, s76, 0x2000\n"
        "s_addc_u32 s77, s77, 0\n"
        "v_mfma_f32_16x16x32_bf16 a[72:75], v[40:43], v[8:11], a[72:75]\n"
        "v_cvt_pk_bf16_f32 v210, v195, v193\n"
        "v_cvt_pk_bf16_f32 v211, v191, v189\n"
        "v_mfma_f32_16x16x32_bf16 a[76:79], v[44:47], v[8:11], a[76:79]\n"
        "v_mul_f32 v193, v180, v188\n"
        "v_mul_f32 v195, v181, v188\n"
        "v_mfma_f32_16x16x32_bf16 a[80:83], v[8:11], v[48:51], a[80:83]\n"
        "v_mul_f32 v191, v179, v188\n"
        "v_cvt_pk_bf16_f32 v193, 0, v193\n"
        "v_mfma_f32_16x16x32_bf16 a[84:87], v[8:11], v[52:55], a[84:87]\n"
        "v_cvt_pk_bf16_f32 v195, 0, v195\n"
        "v_cvt_pk_bf16_f32 v191, 0, v191\n"
        "v_mfma_f32_16x16x32_bf16 a[88:91], v[8:11], v[56:59], a[88:91]\n"
        "v_add_f32 v193, v193, v194\n"
        "v_add_f32 v195, v195, v196\n"
        "v_mfma_f32_16x16x32_bf16 a[92:95], v[8:11], v[60:63], a[92:95]\n"
        "ds_read_b128 v[8:11], v225 offset:4096\n"
        "v_add_f32 v191, v191, v192\n"
        "v_mfma_f32_16x16x32_bf16 a[96:99], v[32:35], v[12:15], a[96:99]\n"
        "v_mul_f32 v194, v182, v186\n"
        "v_mul_f32 v196, v183, v186\n"
        "v_mfma_f32_16x16x32_bf16 a[100:103], v[36:39], v[12:15], a[100:103]\n"
        "v_cvt_pk_bf16_f32 v193, 0, v193\n"
        "v_cvt_pk_bf16_f32 v195, 0, v195\n"
        "v_mfma_f32_16x16x32_bf16 a[104:107], v[40:43], v[12:15], a[104:107]\n"
        "v_mul_f32 v192, v181, v186\n"
        "v_cvt_pk_bf16_f32 v194, 0, v194\n"
        "v_mfma_f32_16x16x32_bf16 a[108:111], v[44:47], v[12:15], a[108:111]\n"
        "v_cvt_pk_bf16_f32 v196, 0, v196\n"
        "v_cvt_pk_bf16_f32 v191, 0, v191\n"
        "v_mfma_f32_16x16x32_bf16 a[112:115], v[12:15], v[48:51], a[112:115]\n"
        "v_cvt_pk_bf16_f32 v192, 0, v192\n"
        "v_add_f32 v193, v193, v194\n"
        "v_mfma_f32_16x16x32_bf16 a[116:119], v[12:15], v[52:55], a[116:119]\n"
        "v_add_f32 v195, v195, v196\n"
        "v_add_f32 v191, v191, v192\n"
        "v_mfma_f32_16x16x32_bf16 a[120:123], v[12:15], v[56:59], a[120:123]\n"
        "v_mul_f32 v194, v183, v185\n"
        "v_mul_f32 v196, v184, v185\n"
        "v_mfma_f32_16x16x32_bf16 a[124:127], v[12:15], v[60:63], a[124:127]\n"
        "ds_read_b128 v[12:15], v225 offset:6144\n"
        "v_cvt_pk_bf16_f32 v193, 0, v193\n"
        "v_mfma_f32_16x16x32_bf16 a[128:131], v[32:35], v[16:19], a[128:131]\n"
        "v_cvt_pk_bf16_f32 v195, 0, v195\n"
        "v_mul_f32 v192, v182, v185\n"
        "v_mfma_f32_16x16x32_bf16 a[132:135], v[36:39], v[16:19], a[132:135]\n"
        "v_cvt_pk_bf16_f32 v194, 0, v194\n"
        "v_cvt_pk_bf16_f32 v196, 0, v196\n"
        "v_mfma_f32_16x16x32_bf16 a[136:139], v[40:43], v[16:19], a[136:139]\n"
        "v_cvt_pk_bf16_f32 v191, 0, v191\n"
        "v_cvt_pk_bf16_f32 v192, 0, v192\n"
        "v_mfma_f32_16x16x32_bf16 a[140:143], v[44:47], v[16:19], a[140:143]\n"
        "v_add_f32 v193, v193, v194\n"
        "v_add_f32 v195, v195, v196\n"
        "v_mfma_f32_16x16x32_bf16 a[144:147], v[16:19], v[48:51], a[144:147]\n"
        "v_add_f32 v191, v191, v192\n"
        "v_cvt_pk_bf16_f32 v193, 0, v193\n"
        "v_mfma_f32_16x16x32_bf16 a[148:151], v[16:19], v[52:55], a[148:151]\n"
        "v_and_b32 v194, 0xffff0000, v131\n"
        "v_cvt_pk_bf16_f32 v195, 0, v195\n"
        "v_mfma_f32_16x16x32_bf16 a[152:155], v[16:19], v[56:59], a[152:155]\n"
        "v_and_b32 v196, 0xffff0000, v133\n"
        "v_cvt_pk_bf16_f32 v191, 0, v191\n"
        "v_mfma_f32_16x16x32_bf16 a[156:159], v[16:19], v[60:63], a[156:159]\n"
        "ds_read_b128 v[16:19], v225 offset:8192\n"
        "v_and_b32 v192, 0xffff0000, v129\n"
        "v_mfma_f32_16x16x32_bf16 a[160:163], v[32:35], v[20:23], a[160:163]\n"
        "v_mul_f32 v193, v193, v194\n"
        "v_mul_f32 v195, v195, v196\n"
        "v_mfma_f32_16x16x32_bf16 a[164:167], v[36:39], v[20:23], a[164:167]\n"
        "v_mul_f32 v191, v191, v192\n"
        "v_cvt_pk_bf16_f32 v208, v195, v193\n"
        "v_mfma_f32_16x16x32_bf16 a[168:171], v[40:43], v[20:23], a[168:171]\n"
        "global_store_dwordx4 v171, v[200:203], s[76:77] offset:-4096 sc1\n"
        "v_mul_f32 v189, v178, v188\n"
        "v_cvt_pk_bf16_f32 v189, 0, v189\n"
        "v_add_f32 v189, v189, v190\n"
        "v_cvt_pk_bf16_f32 v189, 0, v189\n"
        "v_mfma_f32_16x16x32_bf16 a[172:175], v[44:47], v[20:23], a[172:175]\n"
        "v_mul_f32 v190, v180, v186\n"
        "v_cvt_pk_bf16_f32 v190, 0, v190\n"
        "v_mfma_f32_16x16x32_bf16 a[176:179], v[20:23], v[48:51], a[176:179]\n"
        "v_add_f32 v189, v189, v190\n"
        "v_cvt_pk_bf16_f32 v189, 0, v189\n"
        "v_mfma_f32_16x16x32_bf16 a[180:183], v[20:23], v[52:55], a[180:183]\n"
        "v_mul_f32 v190, v181, v185\n"
        "v_cvt_pk_bf16_f32 v190, 0, v190\n"
        "v_mfma_f32_16x16x32_bf16 a[184:187], v[20:23], v[56:59], a[184:187]\n"
        "v_add_f32 v189, v189, v190\n"
        "v_cvt_pk_bf16_f32 v189, 0, v189\n"
        "v_mfma_f32_16x16x32_bf16 a[188:191], v[20:23], v[60:63], a[188:191]\n"
        "ds_read_b128 v[20:23], v225 offset:10240\n"
        "v_and_b32 v190, 0xffff0000, v127\n"
        "v_mfma_f32_16x16x32_bf16 a[192:195], v[32:35], v[24:27], a[192:195]\n"
        "v_mul_f32 v189, v189, v190\n"
        "v_cvt_pk_bf16_f32 v209, v191, v189\n"
        "v_mfma_f32_16x16x32_bf16 a[196:199], v[36:39], v[24:27], a[196:199]\n"
        "global_store_dwordx4 v171, v[208:211], s[76:77] offset:0 sc1\n"
        "v_mfma_f32_16x16x32_bf16 a[200:203], v[40:43], v[24:27], a[200:203]\n"
        "s_waitcnt lgkmcnt(1)\n"
        "v_mfma_f32_16x16x32_bf16 a[204:207], v[44:47], v[24:27], a[204:207]\n"
        "v_mfma_f32_16x16x32_bf16 a[208:211], v[24:27], v[48:51], a[208:211]\n"
        "v_mfma_f32_16x16x32_bf16 a[212:215], v[24:27], v[52:55], a[212:215]\n"
        "v_mfma_f32_16x16x32_bf16 a[216:219], v[24:27], v[56:59], a[216:219]\n"
        "v_mfma_f32_16x16x32_bf16 a[220:223], v[24:27], v[60:63], a[220:223]\n"
        "ds_read_b128 v[24:27], v225 offset:12288\n"
        "s_cmp_ge_u32 s40, 2\n"
        "s_cselect_b32 s50, s45, s44\n"
        "v_mfma_f32_16x16x32_bf16 a[224:227], v[32:35], v[28:31], a[224:227]\n"
        "s_lshl_b32 s50, s50, 15\n"
        "s_lshl_b32 s51, s43, 19\n"
        "s_add_u32 s50, s50, s51\n"
        "v_mfma_f32_16x16x32_bf16 a[228:231], v[36:39], v[28:31], a[228:231]\n"
        "s_and_b32 s51, s40, 1\n"
        "s_lshl_b32 s51, s51, 11\n"
        "s_add_u32 s52, s50, s51\n"
        "v_mfma_f32_16x16x32_bf16 a[232:235], v[40:43], v[28:31], a[232:235]\n"
        "v_mfma_f32_16x16x32_bf16 a[236:239], v[44:47], v[28:31], a[236:239]\n"
        "v_mfma_f32_16x16x32_bf16 a[240:243], v[28:31], v[48:51], a[240:243]\n"
        "v_mfma_f32_16x16x32_bf16 a[244:247], v[28:31], v[52:55], a[244:247]\n"
        "v_mfma_f32_16x16x32_bf16 a[248:251], v[28:31], v[56:59], a[248:251]\n"
        "v_mfma_f32_16x16x32_bf16 a[252:255], v[28:31], v[60:63], a[252:255]\n"
        "ds_read_b128 v[28:31], v225 offset:14336\n"
        "v_mfma_f32_16x16x32_bf16 a[0:3], v[64:67], v[0:3], a[0:3]\n"
        "v_mfma_f32_16x16x32_bf16 a[4:7], v[68:71], v[0:3], a[4:7]\n"
        "v_mfma_f32_16x16x32_bf16 a[8:11], v[72:75], v[0:3], a[8:11]\n"
        "v_mfma_f32_16x16x32_bf16 a[12:15], v[76:79], v[0:3], a[12:15]\n"
        "v_mfma_f32_16x16x32_bf16 a[16:19], v[0:3], v[80:83], a[16:19]\n"
        "v_mfma_f32_16x16x32_bf16 a[20:23], v[0:3], v[84:87], a[20:23]\n"
        "v_mfma_f32_16x16x32_bf16 a[24:27], v[0:3], v[88:91], a[24:27]\n"
        "v_mfma_f32_16x16x32_bf16 a[28:31], v[0:3], v[92:95], a[28:31]\n"
        "s_waitcnt vmcnt(12) lgkmcnt(0)\n"
        "s_barrier\n"
        "v_mfma_f32_16x16x32_bf16 a[32:35], v[64:67], v[4:7], a[32:35]\n"
        "global_load_lds_dwordx4 v242, s[64:65] offset:0\n"
        "ds_read_b128 v[0:3], v224 offset:32768\n"
        "v_mfma_f32_16x16x32_bf16 a[36:39], v[68:71], v[4:7], a[36:39]\n"
        "ds_read_b64_tr_b16 v[32:33], v226 offset:32768\n"
        "v_mfma_f32_16x16x32_bf16 a[40:43], v[72:75], v[4:7], a[40:43]\n"
        "ds_read_b64_tr_b16 v[34:35], v227 offset:32768\n"
        "v_mfma_f32_16x16x32_bf16 a[44:47], v[76:79], v[4:7], a[44:47]\n"
        "ds_read_b64_tr_b16 v[36:37], v228 offset:32768\n"
        "v_mfma_f32_16x16x32_bf16 a[48:51], v[4:7], v[80:83], a[48:51]\n"
        "ds_read_b64_tr_b16 v[38:39], v229 offset:32768\n"
        "global_load_lds_dwordx4 v243, s[64:65] offset:1024\n"
        "v_mfma_f32_16x16x32_bf16 a[52:55], v[4:7], v[84:87], a[52:55]\n"
        "ds_read_b64_tr_b16 v[40:41], v230 offset:32768\n"
        "v_mfma_f32_16x16x32_bf16 a[56:59], v[4:7], v[88:91], a[56:59]\n"
        "ds_read_b64_tr_b16 v[42:43], v231 offset:32768\n"
        "v_mfma_f32_16x16x32_bf16 a[60:63], v[4:7], v[92:95], a[60:63]\n"
        "ds_read_b128 v[4:7], v224 offset:34816\n"
        "v_mfma_f32_16x16x32_bf16 a[64:67], v[64:67], v[8:11], a[64:67]\n"
        "ds_read_b64_tr_b16 v[44:45], v232 offset:32768\n"
        "global_load_lds_dwordx4 v244, s[64:65] offset:2048\n"
        "v_mfma_f32_16x16x32_bf16 a[68:71], v[68:71], v[8:11], a[68:71]\n"
        "ds_read_b64_tr_b16 v[46:47], v233 offset:32768\n"
        "v_mfma_f32_16x16x32_bf16 a[72:75], v[72:75], v[8:11], a[72:75]\n"
        "ds_read_b64_tr_b16 v[48:49], v226 offset:49152\n"
        "v_mfma_f32_16x16x32_bf16 a[76:79], v[76:79], v[8:11], a[76:79]\n"
        "ds_read_b64_tr_b16 v[50:51], v227 offset:49152\n"
        "v_mfma_f32_16x16x32_bf16 a[80:83], v[8:11], v[80:83], a[80:83]\n"
        "global_load_lds_dwordx4 v245, s[64:65] offset:3072\n"
        "s_add_u32 m0, s72, 0x1000\n"
        "v_mfma_f32_16x16x32_bf16 a[84:87], v[8:11], v[84:87], a[84:87]\n"
        "ds_read_b64_tr_b16 v[52:53], v228 offset:49152\n"
        "s_add_u32 s64, s64, 0x80\n"
        "s_addc_u32 s65, s65, 0\n"
        "v_mfma_f32_16x16x32_bf16 a[88:91], v[8:11], v[88:91], a[88:91]\n"
        "ds_read_b64_tr_b16 v[54:55], v229 offset:49152\n"
        "v_mfma_f32_16x16x32_bf16 a[92:95], v[8:11], v[92:95], a[92:95]\n"
        "ds_read_b128 v[8:11], v224 offset:36864\n"
        "v_mfma_f32_16x16x32_bf16 a[96:99], v[64:67], v[12:15], a[96:99]\n"
        "global_load_lds_dwordx4 v242, s[66:67] offset:0\n"
        "s_add_u32 s97, s52, 0x7000\n"
        "v_mfma_f32_16x16x32_bf16 a[100:103], v[68:71], v[12:15], a[100:103]\n"
        "ds_read_b64_tr_b16 v[56:57], v230 offset:49152\n"
        "s_add_u32 s70, s38, s97\n"
        "s_addc_u32 s71, s39, 0\n"
        "v_mfma_f32_16x16x32_bf16 a[104:107], v[72:75], v[12:15], a[104:107]\n"
        "ds_read_b64_tr_b16 v[58:59], v231 offset:49152\n"
        "v_mfma_f32_16x16x32_bf16 a[108:111], v[76:79], v[12:15], a[108:111]\n"
        "ds_read_b64_tr_b16 v[60:61], v232 offset:49152\n"
        "v_mfma_f32_16x16x32_bf16 a[112:115], v[12:15], v[80:83], a[112:115]\n"
        "global_load_lds_dwordx4 v243, s[66:67] offset:1024\n"
        "v_mfma_f32_16x16x32_bf16 a[116:119], v[12:15], v[84:87], a[116:119]\n"
        "ds_read_b64_tr_b16 v[62:63], v233 offset:49152\n"
        "v_mfma_f32_16x16x32_bf16 a[120:123], v[12:15], v[88:91], a[120:123]\n"
        "v_mfma_f32_16x16x32_bf16 a[124:127], v[12:15], v[92:95], a[124:127]\n"
        "ds_read_b128 v[12:15], v224 offset:38912\n"
        "v_mfma_f32_16x16x32_bf16 a[128:131], v[64:67], v[16:19], a[128:131]\n"
        "global_load_dword v253, v173, s[82:83] offset:0\n"
        "v_mfma_f32_16x16x32_bf16 a[132:135], v[68:71], v[16:19], a[132:135]\n"
        "global_load_lds_dwordx4 v244, s[66:67] offset:2048\n"
        "v_mfma_f32_16x16x32_bf16 a[136:139], v[72:75], v[16:19], a[136:139]\n"
        "v_mfma_f32_16x16x32_bf16 a[140:143], v[76:79], v[16:19], a[140:143]\n"
        "v_mfma_f32_16x16x32_bf16 a[144:147], v[16:19], v[80:83], a[144:147]\n"
        "v_mfma_f32_16x16x32_bf16 a[148:151], v[16:19], v[84:87], a[148:151]\n"
        "global_load_lds_dwordx4 v245, s[66:67] offset:3072\n"
        "s_add_u32 s66, s66, 0x80\n"
        "s_addc_u32 s67, s67, 0\n"
        "v_mfma_f32_16x16x32_bf16 a[152:155], v[16:19], v[88:91], a[152:155]\n"
        "s_add_u32 m0, s72, 0x10000\n"
        "v_mfma_f32_16x16x32_bf16 a[156:159], v[16:19], v[92:95], a[156:159]\n"
        "ds_read_b128 v[16:19], v224 offset:40960\n"
        "v_mfma_f32_16x16x32_bf16 a[160:163], v[64:67], v[20:23], a[160:163]\n"
        "v_mfma_f32_16x16x32_bf16 a[164:167], v[68:71], v[20:23], a[164:167]\n"
        "global_load_lds_dwordx4 v242, s[64:65] offset:0\n"
        "v_mfma_f32_16x16x32_bf16 a[168:171], v[72:75], v[20:23], a[168:171]\n"
        "v_mfma_f32_16x16x32_bf16 a[172:175], v[76:79], v[20:23], a[172:175]\n"
        "v_mfma_f32_16x16x32_bf16 a[176:179], v[20:23], v[80:83], a[176:179]\n"
        "v_mfma_f32_16x16x32_bf16 a[180:183], v[20:23], v[84:87], a[180:183]\n"
        "global_load_lds_dwordx4 v243, s[64:65] offset:1024\n"
        "v_mfma_f32_16x16x32_bf16 a[184:187], v[20:23], v[88:91], a[184:187]\n"
        "v_mfma_f32_16x16x32_bf16 a[188:191], v[20:23], v[92:95], a[188:191]\n"
        "ds_read_b128 v[20:23], v224 offset:43008\n"
        "v_mfma_f32_16x16x32_bf16 a[192:195], v[64:67], v[24:27], a[192:195]\n"
        "v_mfma_f32_16x16x32_bf16 a[196:199], v[68:71], v[24:27], a[196:199]\n"
        "global_load_lds_dwordx4 v244, s[64:65] offset:2048\n"
        "v_mfma_f32_16x16x32_bf16 a[200:203], v[72:75], v[24:27], a[200:203]\n"
        "v_mfma_f32_16x16x32_bf16 a[204:207], v[76:79], v[24:27], a[204:207]\n"
        "v_mfma_f32_16x16x32_bf16 a[208:211], v[24:27], v[80:83], a[208:211]\n"
        "v_mfma_f32_16x16x32_bf16 a[212:215], v[24:27], v[84:87], a[212:215]\n"
        "global_load_lds_dwordx4 v245, s[64:65] offset:3072\n"
        "s_add_u32 m0, s72, 0x11000\n"
        "v_mfma_f32_16x16x32_bf16 a[216:219], v[24:27], v[88:91], a[216:219]\n"
        "s_add_u32 s64, s64, 0x80\n"
        "s_addc_u32 s65, s65, 0\n"
        "v_mfma_f32_16x16x32_bf16 a[220:223], v[24:27], v[92:95], a[220:223]\n"
        "ds_read_b128 v[24:27], v224 offset:45056\n"
        "v_mfma_f32_16x16x32_bf16 a[224:227], v[64:67], v[28:31], a[224:227]\n"
        "ds_read_b64_tr_b16 v[64:65], v226 offset:40960\n"
        "v_mfma_f32_16x16x32_bf16 a[228:231], v[68:71], v[28:31], a[228:231]\n"
        "global_load_lds_dwordx4 v242, s[66:67] offset:0\n"
        "ds_read_b64_tr_b16 v[66:67], v227 offset:40960\n"
        "v_mfma_f32_16x16x32_bf16 a[232:235], v[72:75], v[28:31], a[232:235]\n"
        "ds_read_b64_tr_b16 v[68:69], v228 offset:40960\n"
        "s_waitcnt lgkmcnt(4)\n"
        "v_mfma_f32_16x16x32_bf16 a[236:239], v[76:79], v[28:31], a[236:239]\n"
        "ds_read_b64_tr_b16 v[70:71], v229 offset:40960\n"
        "v_mfma_f32_16x16x32_bf16 a[240:243], v[28:31], v[80:83], a[240:243]\n"
        "ds_read_b64_tr_b16 v[72:73], v230 offset:40960\n"
        "v_mfma_f32_16x16x32_bf16 a[244:247], v[28:31], v[84:87], a[244:247]\n"
        "global_load_lds_dwordx4 v243, s[66:67] offset:1024\n"
        "ds_read_b64_tr_b16 v[74:75], v231 offset:40960\n"
        "v_mfma_f32_16x16x32_bf16 a[248:251], v[28:31], v[88:91], a[248:251]\n"
        "ds_read_b64_tr_b16 v[76:77], v232 offset:40960\n"
        "v_mfma_f32_16x16x32_bf16 a[252:255], v[28:31], v[92:95], a[252:255]\n"
        "ds_read_b128 v[28:31], v224 offset:47104\n"
        "v_mfma_f32_16x16x32_bf16 a[0:3], v[32:35], v[0:3], a[0:3]\n"
        "ds_read_b64_tr_b16 v[78:79], v233 offset:40960\n"
        "v_mfma_f32_16x16x32_bf16 a[4:7], v[36:39], v[0:3], a[4:7]\n"
        "global_load_lds_dwordx4 v244, s[66:67] offset:2048\n"
        "ds_read_b64_tr_b16 v[80:81], v226 offset:57344\n"
        "v_mfma_f32_16x16x32_bf16 a[8:11], v[40:43], v[0:3], a[8:11]\n"
        "ds_read_b64_tr_b16 v[82:83], v227 offset:57344\n"
        "v_add_u32 v226, s53, v226\n"
        "v_mfma_f32_16x16x32_bf16 a[12:15], v[44:47], v[0:3], a[12:15]\n"
        "ds_read_b64_tr_b16 v[84:85], v228 offset:57344\n"
        "v_add_u32 v227, s53, v227\n"
        "v_mfma_f32_16x16x32_bf16 a[16:19], v[0:3], v[48:51], a[16:19]\n"
        "ds_read_b64_tr_b16 v[86:87], v229 offset:57344\n"
        "v_add_u32 v228, s53, v228\n"
        "v_mfma_f32_16x16x32_bf16 a[20:23], v[0:3], v[52:55], a[20:23]\n"
        "global_load_lds_dwordx4 v245, s[66:67] offset:3072\n"
        "s_add_u32 s66, s66, 0x80\n"
        "s_addc_u32 s67, s67, 0\n"
        "v_mfma_f32_16x16x32_bf16 a[24:27], v[0:3], v[56:59], a[24:27]\n"
        "ds_read_b64_tr_b16 v[88:89], v230 offset:57344\n"
        "s_add_u32 m0, s73, 0x1c000\n"
        "v_add_u32 v229, s53, v229\n"
        "v_mfma_f32_16x16x32_bf16 a[28:31], v[0:3], v[60:63], a[28:31]\n"
        "ds_read_b128 v[0:3], v225 offset:32768\n"
        "v_add_u32 v230, s53, v230\n"
        "v_mfma_f32_16x16x32_bf16 a[32:35], v[32:35], v[4:7], a[32:35]\n"
        "ds_read_b64_tr_b16 v[90:91], v231 offset:57344\n"
        "v_add_u32 v231, s53, v231\n"
        "v_mfma_f32_16x16x32_bf16 a[36:39], v[36:39], v[4:7], a[36:39]\n"
        "ds_read_b64_tr_b16 v[92:93], v232 offset:57344\n"
        "v_add_u32 v232, s53, v232\n"
        "v_mfma_f32_16x16x32_bf16 a[40:43], v[40:43], v[4:7], a[40:43]\n"
        "ds_read_b64_tr_b16 v[94:95], v233 offset:57344\n"
        "v_add_u32 v233, s53, v233\n"
        "v_mfma_f32_16x16x32_bf16 a[44:47], v[44:47], v[4:7], a[44:47]\n"
        "s_add_u32 s53, s52, s51\n"
        "s_add_u32 s53, s53, 0x1000000\n"
        "v_mfma_f32_16x16x32_bf16 a[48:51], v[4:7], v[48:51], a[48:51]\n"
        "v_mfma_f32_16x16x32_bf16 a[52:55], v[4:7], v[52:55], a[52:55]\n"
        "v_mfma_f32_16x16x32_bf16 a[56:59], v[4:7], v[56:59], a[56:59]\n"
        "v_mfma_f32_16x16x32_bf16 a[60:63], v[4:7], v[60:63], a[60:63]\n"
        "ds_read_b128 v[4:7], v225 offset:34816\n"
        "v_mfma_f32_16x16x32_bf16 a[64:67], v[32:35], v[8:11], a[64:67]\n"
        "v_mfma_f32_16x16x32_bf16 a[68:71], v[36:39], v[8:11], a[68:71]\n"
        "v_mfma_f32_16x16x32_bf16 a[72:75], v[40:43], v[8:11], a[72:75]\n"
        "v_mfma_f32_16x16x32_bf16 a[76:79], v[44:47], v[8:11], a[76:79]\n"
        "v_mfma_f32_16x16x32_bf16 a[80:83], v[8:11], v[48:51], a[80:83]\n"
        "v_mfma_f32_16x16x32_bf16 a[84:87], v[8:11], v[52:55], a[84:87]\n"
        "v_mfma_f32_16x16x32_bf16 a[88:91], v[8:11], v[56:59], a[88:91]\n"
        "v_mfma_f32_16x16x32_bf16 a[92:95], v[8:11], v[60:63], a[92:95]\n"
        "ds_read_b128 v[8:11], v225 offset:36864\n"
        "v_mfma_f32_16x16x32_bf16 a[96:99], v[32:35], v[12:15], a[96:99]\n"
        "v_mfma_f32_16x16x32_bf16 a[100:103], v[36:39], v[12:15], a[100:103]\n"
        "v_mfma_f32_16x16x32_bf16 a[104:107], v[40:43], v[12:15], a[104:107]\n"
        "v_mfma_f32_16x16x32_bf16 a[108:111], v[44:47], v[12:15], a[108:111]\n"
        "s_add_u32 s97, s52, 0x6000\n"
        "v_mfma_f32_16x16x32_bf16 a[112:115], v[12:15], v[48:51], a[112:115]\n"
        "v_mfma_f32_16x16x32_bf16 a[116:119], v[12:15], v[52:55], a[116:119]\n"
        "v_mfma_f32_16x16x32_bf16 a[120:123], v[12:15], v[56:59], a[120:123]\n"
        "v_mfma_f32_16x16x32_bf16 a[124:127], v[12:15], v[60:63], a[124:127]\n"
        "ds_read_b128 v[12:15], v225 offset:38912\n"
        "v_mfma_f32_16x16x32_bf16 a[128:131], v[32:35], v[16:19], a[128:131]\n"
        "v_mfma_f32_16x16x32_bf16 a[132:135], v[36:39], v[16:19], a[132:135]\n"
        "v_mfma_f32_16x16x32_bf16 a[136:139], v[40:43], v[16:19], a[136:139]\n"
        "v_mfma_f32_16x16x32_bf16 a[140:143], v[44:47], v[16:19], a[140:143]\n"
        "v_mfma_f32_16x16x32_bf16 a[144:147], v[16:19], v[48:51], a[144:147]\n"
        "v_mfma_f32_16x16x32_bf16 a[148:151], v[16:19], v[52:55], a[148:151]\n"
        "v_mfma_f32_16x16x32_bf16 a[152:155], v[16:19], v[56:59], a[152:155]\n"
        "v_mfma_f32_16x16x32_bf16 a[156:159], v[16:19], v[60:63], a[156:159]\n"
        "ds_read_b128 v[16:19], v225 offset:40960\n"
        "v_mfma_f32_16x16x32_bf16 a[160:163], v[32:35], v[20:23], a[160:163]\n"
        "v_mfma_f32_16x16x32_bf16 a[164:167], v[36:39], v[20:23], a[164:167]\n"
        "v_mfma_f32_16x16x32_bf16 a[168:171], v[40:43], v[20:23], a[168:171]\n"
        "s_waitcnt lgkmcnt(1)\n"
        "v_mfma_f32_16x16x32_bf16 a[172:175], v[44:47], v[20:23], a[172:175]\n"
        "v_mfma_f32_16x16x32_bf16 a[176:179], v[20:23], v[48:51], a[176:179]\n"
        "v_mfma_f32_16x16x32_bf16 a[180:183], v[20:23], v[52:55], a[180:183]\n"
        "v_mfma_f32_16x16x32_bf16 a[184:187], v[20:23], v[56:59], a[184:187]\n"
        "v_mfma_f32_16x16x32_bf16 a[188:191], v[20:23], v[60:63], a[188:191]\n"
        "ds_read_b128 v[20:23], v225 offset:43008\n"
        "v_mfma_f32_16x16x32_bf16 a[192:195], v[32:35], v[24:27], a[192:195]\n"
        "v_mfma_f32_16x16x32_bf16 a[196:199], v[36:39], v[24:27], a[196:199]\n"
        "v_mfma_f32_16x16x32_bf16 a[200:203], v[40:43], v[24:27], a[200:203]\n"
        "v_mfma_f32_16x16x32_bf16 a[204:207], v[44:47], v[24:27], a[204:207]\n"
        "v_mfma_f32_16x16x32_bf16 a[208:211], v[24:27], v[48:51], a[208:211]\n"
        "v_mfma_f32_16x16x32_bf16 a[212:215], v[24:27], v[52:55], a[212:215]\n"
        "v_mfma_f32_16x16x32_bf16 a[216:219], v[24:27], v[56:59], a[216:219]\n"
        "v_mfma_f32_16x16x32_bf16 a[220:223], v[24:27], v[60:63], a[220:223]\n"
        "ds_read_b128 v[24:27], v225 offset:45056\n"
        "v_mfma_f32_16x16x32_bf16 a[224:227], v[32:35], v[28:31], a[224:227]\n"
        "v_mfma_f32_16x16x32_bf16 a[228:231], v[36:39], v[28:31], a[228:231]\n"
        "v_mfma_f32_16x16x32_bf16 a[232:235], v[40:43], v[28:31], a[232:235]\n"
        "v_mfma_f32_16x16x32_bf16 a[236:239], v[44:47], v[28:31], a[236:239]\n"
        "v_mfma_f32_16x16x32_bf16 a[240:243], v[28:31], v[48:51], a[240:243]\n"
        "v_mfma_f32_16x16x32_bf16 a[244:247], v[28:31], v[52:55], a[244:247]\n"
        "v_mfma_f32_16x16x32_bf16 a[248:251], v[28:31], v[56:59], a[248:251]\n"
        "v_mfma_f32_16x16x32_bf16 a[252:255], v[28:31], v[60:63], a[252:255]\n"
        "ds_read_b128 v[28:31], v225 offset:47104\n"
        "v_mfma_f32_16x16x32_bf16 a[0:3], v[64:67], v[0:3], a[0:3]\n"
        "v_mfma_f32_16x16x32_bf16 a[4:7], v[68:71], v[0:3], a[4:7]\n"
        "v_mfma_f32_16x16x32_bf16 a[8:11], v[72:75], v[0:3], a[8:11]\n"
        "v_mfma_f32_16x16x32_bf16 a[12:15], v[76:79], v[0:3], a[12:15]\n"
        "v_mfma_f32_16x16x32_bf16 a[16:19], v[0:3], v[80:83], a[16:19]\n"
        "v_mfma_f32_16x16x32_bf16 a[20:23], v[0:3], v[84:87], a[20:23]\n"
        "v_mfma_f32_16x16x32_bf16 a[24:27], v[0:3], v[88:91], a[24:27]\n"
        "v_mfma_f32_16x16x32_bf16 a[28:31], v[0:3], v[92:95], a[28:31]\n"
        "s_waitcnt vmcnt(8) lgkmcnt(0)\n"
        "s_barrier\n"
        "v_mfma_f32_16x16x32_bf16 a[32:35], v[64:67], v[4:7], a[32:35]\n"
        "global_load_lds_dwordx4 v238, s[68:69] offset:0\n"
        "ds_read_b128 v[192:195], v246 offset:2048\n"
        "v_mfma_f32_16x16x32_bf16 a[36:39], v[68:71], v[4:7], a[36:39]\n"
        "ds_read_b64_tr_b16 v[32:33], v226 offset:32768\n"
        "v_mfma_f32_16x16x32_bf16 a[40:43], v[72:75], v[4:7], a[40:43]\n"
        "ds_read_b64_tr_b16 v[34:35], v227 offset:32768\n"
        "v_mfma_f32_16x16x32_bf16 a[44:47], v[76:79], v[4:7], a[44:47]\n"
        "ds_read_b64_tr_b16 v[36:37], v228 offset:32768\n"
        "v_mfma_f32_16x16x32_bf16 a[48:51], v[4:7], v[80:83], a[48:51]\n"
        "ds_read_b64_tr_b16 v[38:39], v229 offset:32768\n"
        "global_load_lds_dwordx4 v239, s[68:69] offset:1024\n"
        "v_mfma_f32_16x16x32_bf16 a[52:55], v[4:7], v[84:87], a[52:55]\n"
        "ds_read_b128 v[196:199], v246 offset:0\n"
        "v_mfma_f32_16x16x32_bf16 a[56:59], v[4:7], v[88:91], a[56:59]\n"
        "ds_read_b64_tr_b16 v[40:41], v230 offset:32768\n"
        "v_mfma_f32_16x16x32_bf16 a[60:63], v[4:7], v[92:95], a[60:63]\n"
        "ds_read_b64_tr_b16 v[42:43], v231 offset:32768\n"
        "v_mfma_f32_16x16x32_bf16 a[64:67], v[64:67], v[8:11], a[64:67]\n"
        "ds_read_b64_tr_b16 v[44:45], v232 offset:32768\n"
        "global_load_lds_dwordx4 v240, s[68:69] offset:2048\n"
        "v_mfma_f32_16x16x32_bf16 a[68:71], v[68:71], v[8:11], a[68:71]\n"
        "ds_read_b64_tr_b16 v[46:47], v233 offset:32768\n"
        "v_mfma_f32_16x16x32_bf16 a[72:75], v[72:75], v[8:11], a[72:75]\n"
        "ds_read_b128 v[200:203], v246 offset:6144\n"
        "v_mfma_f32_16x16x32_bf16 a[76:79], v[76:79], v[8:11], a[76:79]\n"
        "ds_read_b64_tr_b16 v[48:49], v226 offset:33792\n"
        "v_mfma_f32_16x16x32_bf16 a[80:83], v[8:11], v[80:83], a[80:83]\n"
        "global_load_lds_dwordx4 v241, s[68:69] offset:3072\n"
        "s_add_u32 m0, s72, 0x8000\n"
        "v_mfma_f32_16x16x32_bf16 a[84:87], v[8:11], v[84:87], a[84:87]\n"
        "ds_read_b64_tr_b16 v[50:51], v227 offset:33792\n"
        "s_add_u32 s68, s68, 0x80000\n"
        "s_addc_u32 s69, s69, 0\n"
        "v_mfma_f32_16x16x32_bf16 a[88:91], v[8:11], v[88:91], a[88:91]\n"
        "ds_read_b64_tr_b16 v[52:53], v228 offset:33792\n"
        "v_mfma_f32_16x16x32_bf16 a[92:95], v[8:11], v[92:95], a[92:95]\n"
        "ds_read_b64_tr_b16 v[54:55], v229 offset:33792\n"
        "v_mfma_f32_16x16x32_bf16 a[96:99], v[64:67], v[12:15], a[96:99]\n"
        "global_load_lds_dwordx4 v242, s[64:65] offset:0\n"
        "v_mfma_f32_16x16x32_bf16 a[100:103], v[68:71], v[12:15], a[100:103]\n"
        "ds_read_b128 v[204:207], v246 offset:4096\n"
        "v_mfma_f32_16x16x32_bf16 a[104:107], v[72:75], v[12:15], a[104:107]\n"
        "ds_read_b64_tr_b16 v[56:57], v230 offset:33792\n"
        "v_mfma_f32_16x16x32_bf16 a[108:111], v[76:79], v[12:15], a[108:111]\n"
        "ds_read_b64_tr_b16 v[58:59], v231 offset:33792\n"
        "v_mfma_f32_16x16x32_bf16 a[112:115], v[12:15], v[80:83], a[112:115]\n"
        "global_load_lds_dwordx4 v243, s[64:65] offset:1024\n"
        "v_mfma_f32_16x16x32_bf16 a[116:119], v[12:15], v[84:87], a[116:119]\n"
        "ds_read_b64_tr_b16 v[60:61], v232 offset:33792\n"
        "v_mfma_f32_16x16x32_bf16 a[120:123], v[12:15], v[88:91], a[120:123]\n"
        "ds_read_b64_tr_b16 v[62:63], v233 offset:33792\n"
        "v_mfma_f32_16x16x32_bf16 a[124:127], v[12:15], v[92:95], a[124:127]\n"
        "ds_read_b128 v[208:211], v247 offset:2048\n"
        "v_mfma_f32_16x16x32_bf16 a[128:131], v[64:67], v[16:19], a[128:131]\n"
        "global_load_lds_dwordx4 v244, s[64:65] offset:2048\n"
        "v_mfma_f32_16x16x32_bf16 a[132:135], v[68:71], v[16:19], a[132:135]\n"
        "ds_read_b128 v[212:215], v247 offset:0\n"
        "v_mfma_f32_16x16x32_bf16 a[136:139], v[72:75], v[16:19], a[136:139]\n"
        "ds_read_b128 v[216:219], v247 offset:6144\n"
        "v_mfma_f32_16x16x32_bf16 a[140:143], v[76:79], v[16:19], a[140:143]\n"
        "ds_read_b128 v[220:223], v247 offset:4096\n"
        "v_mfma_f32_16x16x32_bf16 a[144:147], v[16:19], v[80:83], a[144:147]\n"
        "global_load_lds_dwordx4 v245, s[64:65] offset:3072\n"
        "s_add_u32 m0, s72, 0x9000\n"
        "v_mfma_f32_16x16x32_bf16 a[148:151], v[16:19], v[84:87], a[148:151]\n"
        "s_add_u32 s64, s64, 0x80\n"
        "s_addc_u32 s65, s65, 0\n"
        "v_mfma_f32_16x16x32_bf16 a[152:155], v[16:19], v[88:91], a[152:155]\n"
        "v_mfma_f32_16x16x32_bf16 a[156:159], v[16:19], v[92:95], a[156:159]\n"
        "v_mfma_f32_16x16x32_bf16 a[160:163], v[64:67], v[20:23], a[160:163]\n"
        "global_load_lds_dwordx4 v242, s[66:67] offset:0\n"
        "v_mfma_f32_16x16x32_bf16 a[164:167], v[68:71], v[20:23], a[164:167]\n"
        "v_mfma_f32_16x16x32_bf16 a[168:171], v[72:75], v[20:23], a[168:171]\n"
        "v_mfma_f32_16x16x32_bf16 a[172:175], v[76:79], v[20:23], a[172:175]\n"
        "v_mfma_f32_16x16x32_bf16 a[176:179], v[20:23], v[80:83], a[176:179]\n"
        "global_load_lds_dwordx4 v243, s[66:67] offset:1024\n"
        "v_mfma_f32_16x16x32_bf16 a[180:183], v[20:23], v[84:87], a[180:183]\n"
        "v_mfma_f32_16x16x32_bf16 a[184:187], v[20:23], v[88:91], a[184:187]\n"
        "v_mfma_f32_16x16x32_bf16 a[188:191], v[20:23], v[92:95], a[188:191]\n"
        "v_mfma_f32_16x16x32_bf16 a[192:195], v[64:67], v[24:27], a[192:195]\n"
        "global_load_lds_dwordx4 v244, s[66:67] offset:2048\n"
        "v_mfma_f32_16x16x32_bf16 a[196:199], v[68:71], v[24:27], a[196:199]\n"
        "v_mfma_f32_16x16x32_bf16 a[200:203], v[72:75], v[24:27], a[200:203]\n"
        "v_mfma_f32_16x16x32_bf16 a[204:207], v[76:79], v[24:27], a[204:207]\n"
        "v_mfma_f32_16x16x32_bf16 a[208:211], v[24:27], v[80:83], a[208:211]\n"
        "global_load_lds_dwordx4 v245, s[66:67] offset:3072\n"
        "s_add_u32 m0, s73, 0x20000\n"
        "v_mfma_f32_16x16x32_bf16 a[212:215], v[24:27], v[84:87], a[212:215]\n"
        "s_add_u32 s66, s66, 0x80\n"
        "s_addc_u32 s67, s67, 0\n"
        "v_accvgpr_read_b32 v177, a193\n"
        "v_mfma_f32_16x16x32_bf16 a[216:219], v[24:27], v[88:91], a[216:219]\n"
        "v_accvgpr_read_b32 v178, a194\n"
        "v_accvgpr_read_b32 v179, a195\n"
        "v_mfma_f32_16x16x32_bf16 a[220:223], v[24:27], v[92:95], a[220:223]\n"
        "v_accvgpr_read_b32 v180, a196\n"
        "v_accvgpr_read_b32 v181, a197\n"
        "v_mfma_f32_16x16x32_bf16 a[224:227], v[64:67], v[28:31], a[224:227]\n"
        "v_accvgpr_read_b32 v182, a198\n"
        "v_accvgpr_read_b32 v183, a199\n"
        "v_mfma_f32_16x16x32_bf16 a[228:231], v[68:71], v[28:31], a[228:231]\n"
        "v_accvgpr_read_b32 v185, a201\n"
        "v_accvgpr_read_b32 v186, a202\n"
        "v_mfma_f32_16x16x32_bf16 a[232:235], v[72:75], v[28:31], a[232:235]\n"
        "s_waitcnt lgkmcnt(0)\n"
        "v_accvgpr_read_b32 v187, a203\n"
        "v_accvgpr_read_b32 v161, a225\n"
        "v_mfma_f32_16x16x32_bf16 a[236:239], v[76:79], v[28:31], a[236:239]\n"
        "v_accvgpr_read_b32 v162, a226\n"
        "v_accvgpr_read_b32 v163, a227\n"
        "v_mfma_f32_16x16x32_bf16 a[240:243], v[28:31], v[80:83], a[240:243]\n"
        "v_accvgpr_read_b32 v164, a228\n"
        "v_accvgpr_read_b32 v165, a229\n"
        "v_mfma_f32_16x16x32_bf16 a[244:247], v[28:31], v[84:87], a[244:247]\n"
        "v_accvgpr_read_b32 v166, a230\n"
        "v_accvgpr_read_b32 v167, a231\n"
        "v_mfma_f32_16x16x32_bf16 a[248:251], v[28:31], v[88:91], a[248:251]\n"
        "v_accvgpr_read_b32 v169, a233\n"
        "v_accvgpr_read_b32 v170, a234\n"
        "v_mfma_f32_16x16x32_bf16 a[252:255], v[28:31], v[92:95], a[252:255]\n"
        "v_accvgpr_read_b32 v171, a235\n"
        "v_accvgpr_read_b32 v172, a236\n"
        "v_mfma_f32_16x16x32_bf16 v[0:3], v[32:35], v[192:195], 0\n"
        "v_accvgpr_read_b32 v173, a237\n"
        "v_accvgpr_read_b32 v174, a238\n"
        "v_mfma_f32_16x16x32_bf16 v[64:67], v[32:35], v[196:199], 0\n"
        "v_accvgpr_read_b32 v175, a239\n"
        "v_accvgpr_read_b32 v188, a204\n"
        "v_mfma_f32_16x16x32_bf16 v[96:99], v[32:35], v[200:203], 0\n"
        "v_accvgpr_read_b32 v189, a205\n"
        "v_accvgpr_read_b32 v190, a206\n"
        "v_mfma_f32_16x16x32_bf16 v[128:131], v[32:35], v[204:207], 0\n"
        "ds_read_b64_tr_b16 v[32:33], v226 offset:40960\n"
        "v_accvgpr_read_b32 v191, a207\n"
        "v_mfma_f32_16x16x32_bf16 v[4:7], v[36:39], v[192:195], 0\n"
        "ds_read_b64_tr_b16 v[34:35], v227 offset:40960\n"
        "v_mfma_f32_16x16x32_bf16 v[68:71], v[36:39], v[196:199], 0\n"
        "v_mfma_f32_16x16x32_bf16 v[100:103], v[36:39], v[200:203], 0\n"
        "v_mfma_f32_16x16x32_bf16 v[132:135], v[36:39], v[204:207], 0\n"
        "ds_read_b64_tr_b16 v[36:37], v228 offset:40960\n"
        "v_accvgpr_read_b32 v160, a224\n"
        "v_mfma_f32_16x16x32_bf16 v[8:11], v[40:43], v[192:195], 0\n"
        "ds_read_b64_tr_b16 v[38:39], v229 offset:40960\n"
        "v_cvt_pk_bf16_f32 v160, v160, v161\n"
        "v_mfma_f32_16x16x32_bf16 v[72:75], v[40:43], v[196:199], 0\n"
        "v_cvt_pk_bf16_f32 v161, v162, v163\n"
        "v_cvt_pk_bf16_f32 v162, v164, v165\n"
        "v_mfma_f32_16x16x32_bf16 v[104:107], v[40:43], v[200:203], 0\n"
        "v_cvt_pk_bf16_f32 v163, v166, v167\n"
        "v_accvgpr_write_b32 a224, v160\n"
        "v_mfma_f32_16x16x32_bf16 v[136:139], v[40:43], v[204:207], 0\n"
        "ds_read_b64_tr_b16 v[40:41], v230 offset:40960\n"
        "v_accvgpr_write_b32 a225, v161\n"
        "v_mfma_f32_16x16x32_bf16 v[12:15], v[44:47], v[192:195], 0\n"
        "ds_read_b64_tr_b16 v[42:43], v231 offset:40960\n"
        "v_accvgpr_write_b32 a226, v162\n"
        "v_mfma_f32_16x16x32_bf16 v[76:79], v[44:47], v[196:199], 0\n"
        "v_accvgpr_write_b32 a227, v163\n"
        "v_accvgpr_read_b32 v161, a161\n"
        "v_mfma_f32_16x16x32_bf16 v[108:111], v[44:47], v[200:203], 0\n"
        "v_accvgpr_read_b32 v162, a162\n"
        "v_accvgpr_read_b32 v163, a163\n"
        "v_mfma_f32_16x16x32_bf16 v[140:143], v[44:47], v[204:207], 0\n"
        "ds_read_b64_tr_b16 v[44:45], v232 offset:40960\n"
        "v_accvgpr_read_b32 v164, a164\n"
        "v_mfma_f32_16x16x32_bf16 v[16:19], v[48:51], v[192:195], 0\n"
        "ds_read_b64_tr_b16 v[46:47], v233 offset:40960\n"
        "v_accvgpr_read_b32 v165, a165\n"
        "v_mfma_f32_16x16x32_bf16 v[80:83], v[48:51], v[196:199], 0\n"
        "v_accvgpr_read_b32 v166, a166\n"
        "v_accvgpr_read_b32 v167, a167\n"
        "v_mfma_f32_16x16x32_bf16 v[112:115], v[48:51], v[200:203], 0\n"
        "v_mfma_f32_16x16x32_bf16 v[144:147], v[48:51], v[204:207], 0\n"
        "ds_read_b64_tr_b16 v[48:49], v226 offset:41984\n"
        "v_mfma_f32_16x16x32_bf16 v[20:23], v[52:55], v[192:195], 0\n"
        "ds_read_b64_tr_b16 v[50:51], v227 offset:41984\n"
        "v_mfma_f32_16x16x32_bf16 v[84:87], v[52:55], v[196:199], 0\n"
        "v_mfma_f32_16x16x32_bf16 v[116:119], v[52:55], v[200:203], 0\n"
        "v_mfma_f32_16x16x32_bf16 v[148:151], v[52:55], v[204:207], 0\n"
        "ds_read_b64_tr_b16 v[52:53], v228 offset:41984\n"
        "v_mfma_f32_16x16x32_bf16 v[24:27], v[56:59], v[192:195], 0\n"
        "ds_read_b64_tr_b16 v[54:55], v229 offset:41984\n"
        "v_mfma_f32_16x16x32_bf16 v[88:91], v[56:59], v[196:199], 0\n"
        "v_mfma_f32_16x16x32_bf16 v[120:123], v[56:59], v[200:203], 0\n"
        "s_waitcnt lgkmcnt(4)\n"
        "v_mfma_f32_16x16x32_bf16 v[152:155], v[56:59], v[204:207], 0\n"
        "ds_read_b64_tr_b16 v[56:57], v230 offset:41984\n"
        "v_mfma_f32_16x16x32_bf16 v[28:31], v[60:63], v[192:195], 0\n"
        "ds_read_b64_tr_b16 v[58:59], v231 offset:41984\n"
        "v_mfma_f32_16x16x32_bf16 v[92:95], v[60:63], v[196:199], 0\n"
        "v_mfma_f32_16x16x32_bf16 v[124:127], v[60:63], v[200:203], 0\n"
        "v_mfma_f32_16x16x32_bf16 v[156:159], v[60:63], v[204:207], 0\n"
        "ds_read_b64_tr_b16 v[60:61], v232 offset:41984\n"
        "v_accvgpr_read_b32 v168, a232\n"
        "v_mfma_f32_16x16x32_bf16 v[0:3], v[32:35], v[208:211], v[0:3]\n"
        "ds_read_b64_tr_b16 v[62:63], v233 offset:41984\n"
        "v_cvt_pk_bf16_f32 v168, v168, v169\n"
        "v_mfma_f32_16x16x32_bf16 v[64:67], v[32:35], v[212:215], v[64:67]\n"
        "v_cvt_pk_bf16_f32 v169, v170, v171\n"
        "v_cvt_pk_bf16_f32 v170, v172, v173\n"
        "v_mfma_f32_16x16x32_bf16 v[96:99], v[32:35], v[216:219], v[96:99]\n"
        "v_cvt_pk_bf16_f32 v171, v174, v175\n"
        "v_accvgpr_write_b32 a228, v168\n"
        "v_mfma_f32_16x16x32_bf16 v[128:131], v[32:35], v[220:223], v[128:131]\n"
        "v_accvgpr_write_b32 a229, v169\n"
        "v_accvgpr_write_b32 a230, v170\n"
        "v_mfma_f32_16x16x32_bf16 v[4:7], v[36:39], v[208:211], v[4:7]\n"
        "v_accvgpr_write_b32 a231, v171\n"
        "v_accvgpr_read_b32 v169, a169\n"
        "v_mfma_f32_16x16x32_bf16 v[68:71], v[36:39], v[212:215], v[68:71]\n"
        "global_store_dwordx4 v254, a[224:227], s[70:71] offset:0 sc1\n"
        "v_accvgpr_read_b32 v170, a170\n"
        "v_accvgpr_read_b32 v171, a171\n"
        "v_accvgpr_read_b32 v172, a172\n"
        "v_accvgpr_read_b32 v173, a173\n"
        "v_mfma_f32_16x16x32_bf16 v[100:103], v[36:39], v[216:219], v[100:103]\n"
        "v_accvgpr_read_b32 v174, a174\n"
        "v_accvgpr_read_b32 v175, a175\n"
        "v_mfma_f32_16x16x32_bf16 v[132:135], v[36:39], v[220:223], v[132:135]\n"
        "s_waitcnt vmcnt(13) lgkmcnt(0)\n"
        "s_barrier\n"
        "v_mfma_f32_16x16x32_bf16 v[8:11], v[40:43], v[208:211], v[8:11]\n"
        "global_load_lds_dwordx4 v238, s[68:69] offset:0\n"
        "v_mfma_f32_16x16x32_bf16 v[72:75], v[40:43], v[212:215], v[72:75]\n"
        "ds_read_b64_tr_b16 v[32:33], v226 offset:49152\n"
        "v_mfma_f32_16x16x32_bf16 v[104:107], v[40:43], v[216:219], v[104:107]\n"
        "ds_read_b64_tr_b16 v[34:35], v227 offset:49152\n"
        "v_mfma_f32_16x16x32_bf16 v[136:139], v[40:43], v[220:223], v[136:139]\n"
        "ds_read_b64_tr_b16 v[36:37], v228 offset:49152\n"
        "v_mfma_f32_16x16x32_bf16 v[12:15], v[44:47], v[208:211], v[12:15]\n"
        "ds_read_b64_tr_b16 v[38:39], v229 offset:49152\n"
        "global_load_lds_dwordx4 v239, s[68:69] offset:1024\n"
        "v_mfma_f32_16x16x32_bf16 v[76:79], v[44:47], v[212:215], v[76:79]\n"
        "ds_read_b64_tr_b16 v[40:41], v230 offset:49152\n"
        "v_mfma_f32_16x16x32_bf16 v[108:111], v[44:47], v[216:219], v[108:111]\n"
        "ds_read_b64_tr_b16 v[42:43], v231 offset:49152\n"
        "v_mfma_f32_16x16x32_bf16 v[140:143], v[44:47], v[220:223], v[140:143]\n"
        "ds_read_b128 v[192:195], v248 offset:2048\n"
        "v_mfma_f32_16x16x32_bf16 v[16:19], v[48:51], v[208:211], v[16:19]\n"
        "ds_read_b64_tr_b16 v[44:45], v232 offset:49152\n"
        "global_load_lds_dwordx4 v240, s[68:69] offset:2048\n"
        "v_mfma_f32_16x16x32_bf16 v[80:83], v[48:51], v[212:215], v[80:83]\n"
        "ds_read_b64_tr_b16 v[46:47], v233 offset:49152\n"
        "v_mfma_f32_16x16x32_bf16 v[112:115], v[48:51], v[216:219], v[112:115]\n"
        "ds_read_b128 v[196:199], v248 offset:0\n"
        "v_mfma_f32_16x16x32_bf16 v[144:147], v[48:51], v[220:223], v[144:147]\n"
        "ds_read_b64_tr_b16 v[48:49], v226 offset:50176\n"
        "v_mfma_f32_16x16x32_bf16 v[20:23], v[52:55], v[208:211], v[20:23]\n"
        "ds_read_b64_tr_b16 v[50:51], v227 offset:50176\n"
        "global_load_lds_dwordx4 v241, s[68:69] offset:3072\n"
        "s_add_u32 m0, s72, 0x0\n"
        "v_mfma_f32_16x16x32_bf16 v[84:87], v[52:55], v[212:215], v[84:87]\n"
        "ds_read_b128 v[200:203], v248 offset:6144\n"
        "s_add_u32 s68, s68, 0x80000\n"
        "s_addc_u32 s69, s69, 0\n"
        "v_mfma_f32_16x16x32_bf16 v[116:119], v[52:55], v[216:219], v[116:119]\n"
        "ds_read_b128 v[204:207], v248 offset:4096\n"
        "v_mfma_f32_16x16x32_bf16 v[148:151], v[52:55], v[220:223], v[148:151]\n"
        "ds_read_b64_tr_b16 v[52:53], v228 offset:50176\n"
        "v_accvgpr_read_b32 v176, a192\n"
        "v_mfma_f32_16x16x32_bf16 v[24:27], v[56:59], v[208:211], v[24:27]\n"
        "ds_read_b64_tr_b16 v[54:55], v229 offset:50176\n"
        "global_load_lds_dwordx4 v242, s[64:65] offset:0\n"
        "v_mfma_f32_16x16x32_bf16 v[88:91], v[56:59], v[212:215], v[88:91]\n"
        "v_cvt_pk_bf16_f32 v176, v176, v177\n"
        "v_cvt_pk_bf16_f32 v177, v178, v179\n"
        "v_mfma_f32_16x16x32_bf16 v[120:123], v[56:59], v[216:219], v[120:123]\n"
        "s_waitcnt lgkmcnt(6)\n"
        "v_cvt_pk_bf16_f32 v178, v180, v181\n"
        "v_cvt_pk_bf16_f32 v179, v182, v183\n"
        "v_mfma_f32_16x16x32_bf16 v[152:155], v[56:59], v[220:223], v[152:155]\n"
        "ds_read_b64_tr_b16 v[56:57], v230 offset:50176\n"
        "v_accvgpr_write_b32 a232, v176\n"
        "v_mfma_f32_16x16x32_bf16 v[28:31], v[60:63], v[208:211], v[28:31]\n"
        "s_waitcnt lgkmcnt(5)\n"
        "v_accvgpr_write_b32 a233, v177\n"
        "v_accvgpr_write_b32 a234, v178\n"
        "v_mfma_f32_16x16x32_bf16 v[92:95], v[60:63], v[212:215], v[92:95]\n"
        "s_waitcnt lgkmcnt(4)\n"
        "v_accvgpr_write_b32 a235, v179\n"
        "v_accvgpr_read_b32 v177, a1\n"
        "v_mfma_f32_16x16x32_bf16 v[124:127], v[60:63], v[216:219], v[124:127]\n"
        "global_store_dwordx4 v254, a[228:231], s[70:71] offset:1024 sc1\n"
        "ds_read_b64_tr_b16 v[58:59], v231 offset:50176\n"
        "ds_read_b128 v[208:211], v249 offset:2048\n"
        "s_add_u32 s70, s38, s97\n"
        "s_addc_u32 s71, s39, 0\n"
        "v_mfma_f32_16x16x32_bf16 v[156:159], v[60:63], v[220:223], v[156:159]\n"
        "ds_read_b64_tr_b16 v[60:61], v232 offset:50176\n"
        "v_accvgpr_read_b32 v178, a2\n"
        "v_mfma_f32_16x16x32_bf16 v[0:3], v[32:35], v[192:195], v[0:3]\n"
        "ds_read_b64_tr_b16 v[62:63], v233 offset:50176\n"
        "s_add_u32 s97, s52, 0x5000\n"
        "v_accvgpr_read_b32 v179, a3\n"
        "v_mfma_f32_16x16x32_bf16 v[64:67], v[32:35], v[196:199], v[64:67]\n"
        "global_load_lds_dwordx4 v243, s[64:65] offset:1024\n"
        "v_mfma_f32_16x16x32_bf16 v[96:99], v[32:35], v[200:203], v[96:99]\n"
        "ds_read_b128 v[212:215], v249 offset:0\n"
        "s_waitcnt lgkmcnt(5)\n"
        "v_mfma_f32_16x16x32_bf16 v[128:131], v[32:35], v[204:207], v[128:131]\n"
        "ds_read_b64_tr_b16 v[32:33], v226 offset:57344\n"
        "v_accvgpr_read_b32 v180, a4\n"
        "v_mfma_f32_16x16x32_bf16 v[4:7], v[36:39], v[192:195], v[4:7]\n"
        "ds_read_b64_tr_b16 v[34:35], v227 offset:57344\n"
        "v_accvgpr_read_b32 v181, a5\n"
        "v_mfma_f32_16x16x32_bf16 v[68:71], v[36:39], v[196:199], v[68:71]\n"
        "global_load_lds_dwordx4 v244, s[64:65] offset:2048\n"
        "v_mfma_f32_16x16x32_bf16 v[100:103], v[36:39], v[200:203], v[100:103]\n"
        "ds_read_b128 v[216:219], v249 offset:6144\n"
        "v_accvgpr_read_b32 v182, a6\n"
        "v_mfma_f32_16x16x32_bf16 v[132:135], v[36:39], v[204:207], v[132:135]\n"
        "ds_read_b64_tr_b16 v[36:37], v228 offset:57344\n"
        "v_accvgpr_read_b32 v183, a7\n"
        "v_mfma_f32_16x16x32_bf16 v[8:11], v[40:43], v[192:195], v[8:11]\n"
        "ds_read_b64_tr_b16 v[38:39], v229 offset:57344\n"
        "v_mfma_f32_16x16x32_bf16 v[72:75], v[40:43], v[196:199], v[72:75]\n"
        "global_load_lds_dwordx4 v245, s[64:65] offset:3072\n"
        "s_add_u32 m0, s72, 0x1000\n"
        "v_mfma_f32_16x16x32_bf16 v[104:107], v[40:43], v[200:203], v[104:107]\n"
        "ds_read_b128 v[220:223], v249 offset:4096\n"
        "s_add_u32 s64, s64, 0x80\n"
        "s_addc_u32 s65, s65, 0\n"
        "v_mfma_f32_16x16x32_bf16 v[136:139], v[40:43], v[204:207], v[136:139]\n"
        "ds_read_b64_tr_b16 v[40:41], v230 offset:57344\n"
        "v_mfma_f32_16x16x32_bf16 v[12:15], v[44:47], v[192:195], v[12:15]\n"
        "ds_read_b64_tr_b16 v[42:43], v231 offset:57344\n"
        "v_mfma_f32_16x16x32_bf16 v[76:79], v[44:47], v[196:199], v[76:79]\n"
        "global_load_lds_dwordx4 v242, s[66:67] offset:0\n"
        "v_mfma_f32_16x16x32_bf16 v[108:111], v[44:47], v[200:203], v[108:111]\n"
        "v_mfma_f32_16x16x32_bf16 v[140:143], v[44:47], v[204:207], v[140:143]\n"
        "ds_read_b64_tr_b16 v[44:45], v232 offset:57344\n"
        "v_accvgpr_read_b32 v184, a200\n"
        "v_mfma_f32_16x16x32_bf16 v[16:19], v[48:51], v[192:195], v[16:19]\n"
        "ds_read_b64_tr_b16 v[46:47], v233 offset:57344\n"
        "v_cvt_pk_bf16_f32 v184, v184, v185\n"
        "v_mfma_f32_16x16x32_bf16 v[80:83], v[48:51], v[196:199], v[80:83]\n"
        "global_load_lds_dwordx4 v243, s[66:67] offset:1024\n"
        "v_mfma_f32_16x16x32_bf16 v[112:115], v[48:51], v[200:203], v[112:115]\n"
        "s_waitcnt lgkmcnt(4)\n"
        "v_cvt_pk_bf16_f32 v185, v186, v187\n"
        "v_cvt_pk_bf16_f32 v186, v188, v189\n"
        "v_mfma_f32_16x16x32_bf16 v[144:147], v[48:51], v[204:207], v[144:147]\n"
        "ds_read_b64_tr_b16 v[48:49], v226 offset:58368\n"
        "v_cvt_pk_bf16_f32 v187, v190, v191\n"
        "v_mfma_f32_16x16x32_bf16 v[20:23], v[52:55], v[192:195], v[20:23]\n"
        "ds_read_b64_tr_b16 v[50:51], v227 offset:58368\n"
        "v_accvgpr_write_b32 a236, v184\n"
        "v_mfma_f32_16x16x32_bf16 v[84:87], v[52:55], v[196:199], v[84:87]\n"
        "global_load_lds_dwordx4 v244, s[66:67] offset:2048\n"
        "v_mfma_f32_16x16x32_bf16 v[116:119], v[52:55], v[200:203], v[116:119]\n"
        "v_accvgpr_write_b32 a237, v185\n"
        "v_accvgpr_write_b32 a238, v186\n"
        "v_mfma_f32_16x16x32_bf16 v[148:151], v[52:55], v[204:207], v[148:151]\n"
        "ds_read_b64_tr_b16 v[52:53], v228 offset:58368\n"
        "v_accvgpr_write_b32 a239, v187\n"
        "v_mfma_f32_16x16x32_bf16 v[24:27], v[56:59], v[192:195], v[24:27]\n"
        "ds_read_b64_tr_b16 v[54:55], v229 offset:58368\n"
        "v_accvgpr_read_b32 v185, a9\n"
        "v_mfma_f32_16x16x32_bf16 v[88:91], v[56:59], v[196:199], v[88:91]\n"
        "global_load_lds_dwordx4 v245, s[66:67] offset:3072\n"
        "s_add_u32 m0, s73, 0x24000\n"
        "v_mfma_f32_16x16x32_bf16 v[120:123], v[56:59], v[200:203], v[120:123]\n"
        "s_add_u32 s66, s66, 0x80\n"
        "s_addc_u32 s67, s67, 0\n"
        "v_accvgpr_read_b32 v186, a10\n"
        "v_mfma_f32_16x16x32_bf16 v[152:155], v[56:59], v[204:207], v[152:155]\n"
        "ds_read_b64_tr_b16 v[56:57], v230 offset:58368\n"
        "v_accvgpr_read_b32 v187, a11\n"
        "v_mfma_f32_16x16x32_bf16 v[28:31], v[60:63], v[192:195], v[28:31]\n"
        "ds_read_b64_tr_b16 v[58:59], v231 offset:58368\n"
        "v_accvgpr_read_b32 v188, a12\n"
        "v_mfma_f32_16x16x32_bf16 v[92:95], v[60:63], v[196:199], v[92:95]\n"
        "v_accvgpr_read_b32 v189, a13\n"
        "v_accvgpr_read_b32 v190, a14\n"
        "v_mfma_f32_16x16x32_bf16 v[124:127], v[60:63], v[200:203], v[124:127]\n"
        "v_accvgpr_read_b32 v191, a15\n"
        "v_mfma_f32_16x16x32_bf16 v[156:159], v[60:63], v[204:207], v[156:159]\n"
        "ds_read_b64_tr_b16 v[60:61], v232 offset:58368\n"
        "v_mfma_f32_16x16x32_bf16 v[0:3], v[32:35], v[208:211], v[0:3]\n"
        "ds_read_b64_tr_b16 v[62:63], v233 offset:58368\n"
        "v_mfma_f32_16x16x32_bf16 v[64:67], v[32:35], v[212:215], v[64:67]\n"
        "v_mfma_f32_16x16x32_bf16 v[96:99], v[32:35], v[216:219], v[96:99]\n"
        "s_waitcnt lgkmcnt(4)\n"
        "v_mfma_f32_16x16x32_bf16 v[128:131], v[32:35], v[220:223], v[128:131]\n"
        "global_store_dwordx4 v254, a[232:235], s[70:71] offset:0 sc1\n"
        "v_mfma_f32_16x16x32_bf16 v[4:7], v[36:39], v[208:211], v[4:7]\n"
        "v_mfma_f32_16x16x32_bf16 v[68:71], v[36:39], v[212:215], v[68:71]\n"
        "v_mfma_f32_16x16x32_bf16 v[100:103], v[36:39], v[216:219], v[100:103]\n"
        "v_mfma_f32_16x16x32_bf16 v[132:135], v[36:39], v[220:223], v[132:135]\n"
        "s_waitcnt vmcnt(15) lgkmcnt(0)\n"
        "s_barrier\n"
        "v_mfma_f32_16x16x32_bf16 v[8:11], v[40:43], v[208:211], v[8:11]\n"
        "global_load_lds_dwordx4 v238, s[68:69] offset:0\n"
        "ds_read_b64_tr_b16 v[32:33], v226 offset:16384\n"
        "v_mfma_f32_16x16x32_bf16 v[72:75], v[40:43], v[212:215], v[72:75]\n"
        "ds_read_b64_tr_b16 v[34:35], v227 offset:16384\n"
        "v_accvgpr_read_b32 v160, a160\n"
        "v_mfma_f32_16x16x32_bf16 v[104:107], v[40:43], v[216:219], v[104:107]\n"
        "ds_read_b64_tr_b16 v[36:37], v228 offset:16384\n"
        "v_cvt_pk_bf16_f32 v160, v160, v161\n"
        "v_mfma_f32_16x16x32_bf16 v[136:139], v[40:43], v[220:223], v[136:139]\n"
        "ds_read_b64_tr_b16 v[38:39], v229 offset:16384\n"
        "v_cvt_pk_bf16_f32 v161, v162, v163\n"
        "v_mfma_f32_16x16x32_bf16 v[12:15], v[44:47], v[208:211], v[12:15]\n"
        "ds_read_b64_tr_b16 v[40:41], v230 offset:16384\n"
        "v_cvt_pk_bf16_f32 v162, v164, v165\n"
        "v_mfma_f32_16x16x32_bf16 v[76:79], v[44:47], v[212:215], v[76:79]\n"
        "ds_read_b64_tr_b16 v[42:43], v231 offset:16384\n"
        "v_cvt_pk_bf16_f32 v163, v166, v167\n"
        "v_mfma_f32_16x16x32_bf16 v[108:111], v[44:47], v[216:219], v[108:111]\n"
        "ds_read_b128 v[192:195], v246 offset:34816\n"
        "v_accvgpr_write_b32 a192, v160\n"
        "v_mfma_f32_16x16x32_bf16 v[140:143], v[44:47], v[220:223], v[140:143]\n"
        "global_load_lds_dwordx4 v239, s[68:69] offset:1024\n"
        "v_accvgpr_write_b32 a193, v161\n"
        "v_mfma_f32_16x16x32_bf16 v[16:19], v[48:51], v[208:211], v[16:19]\n"
        "ds_read_b64_tr_b16 v[44:45], v232 offset:16384\n"
        "v_accvgpr_write_b32 a194, v162\n"
        "v_mfma_f32_16x16x32_bf16 v[80:83], v[48:51], v[212:215], v[80:83]\n"
        "ds_read_b64_tr_b16 v[46:47], v233 offset:16384\n"
        "v_accvgpr_write_b32 a195, v163\n"
        "v_mfma_f32_16x16x32_bf16 v[112:115], v[48:51], v[216:219], v[112:115]\n"
        "ds_read_b128 v[196:199], v246 offset:32768\n"
        "v_accvgpr_read_b32 v161, a33\n"
        "v_mfma_f32_16x16x32_bf16 v[144:147], v[48:51], v[220:223], v[144:147]\n"
        "global_load_lds_dwordx4 v240, s[68:69] offset:2048\n"
        "ds_read_b64_tr_b16 v[48:49], v226 offset:17408\n"
        "v_mfma_f32_16x16x32_bf16 v[20:23], v[52:55], v[208:211], v[20:23]\n"
        "ds_read_b64_tr_b16 v[50:51], v227 offset:17408\n"
        "v_accvgpr_read_b32 v162, a34\n"
        "v_mfma_f32_16x16x32_bf16 v[84:87], v[52:55], v[212:215], v[84:87]\n"
        "ds_read_b128 v[200:203], v246 offset:38912\n"
        "v_accvgpr_read_b32 v163, a35\n"
        "v_mfma_f32_16x16x32_bf16 v[116:119], v[52:55], v[216:219], v[116:119]\n"
        "ds_read_b128 v[204:207], v246 offset:36864\n"
        "v_accvgpr_read_b32 v164, a36\n"
        "v_mfma_f32_16x16x32_bf16 v[148:151], v[52:55], v[220:223], v[148:151]\n"
        "global_load_lds_dwordx4 v241, s[68:69] offset:3072\n"
        "ds_read_b64_tr_b16 v[52:53], v228 offset:17408\n"
        "s_add_u32 m0, s72, 0x10000\n"
        "v_mfma_f32_16x16x32_bf16 v[24:27], v[56:59], v[208:211], v[24:27]\n"
        "ds_read_b64_tr_b16 v[54:55], v229 offset:17408\n"
        "s_add_u32 s68, s68, 0x80000\n"
        "s_addc_u32 s69, s69, 0\n"
        "v_mfma_f32_16x16x32_bf16 v[88:91], v[56:59], v[212:215], v[88:91]\n"
        "v_accvgpr_read_b32 v165, a37\n"
        "v_accvgpr_read_b32 v166, a38\n"
        "v_mfma_f32_16x16x32_bf16 v[120:123], v[56:59], v[216:219], v[120:123]\n"
        "s_waitcnt lgkmcnt(6)\n"
        "v_accvgpr_read_b32 v167, a39\n"
        "v_mfma_f32_16x16x32_bf16 v[152:155], v[56:59], v[220:223], v[152:155]\n"
        "global_load_lds_dwordx4 v242, s[64:65] offset:0\n"
        "ds_read_b64_tr_b16 v[56:57], v230 offset:17408\n"
        "v_mfma_f32_16x16x32_bf16 v[28:31], v[60:63], v[208:211], v[28:31]\n"
        "ds_read_b64_tr_b16 v[58:59], v231 offset:17408\n"
        "s_waitcnt lgkmcnt(6)\n"
        "v_mfma_f32_16x16x32_bf16 v[92:95], v[60:63], v[212:215], v[92:95]\n"
        "ds_read_b128 v[208:211], v247 offset:34816\n"
        "s_waitcnt lgkmcnt(6)\n"
        "v_mfma_f32_16x16x32_bf16 v[124:127], v[60:63], v[216:219], v[124:127]\n"
        "ds_read_b128 v[212:215], v247 offset:32768\n"
        "s_waitcnt lgkmcnt(6)\n"
        "v_mfma_f32_16x16x32_bf16 v[156:159], v[60:63], v[220:223], v[156:159]\n"
        "global_load_lds_dwordx4 v243, s[64:65] offset:1024\n"
        "ds_read_b64_tr_b16 v[60:61], v232 offset:17408\n"
        "v_mfma_f32_16x16x32_bf16 v[0:3], v[32:35], v[192:195], v[0:3]\n"
        "ds_read_b64_tr_b16 v[62:63], v233 offset:17408\n"
        "v_accvgpr_read_b32 v168, a168\n"
        "v_mfma_f32_16x16x32_bf16 v[64:67], v[32:35], v[196:199], v[64:67]\n"
        "ds_read_b128 v[216:219], v247 offset:38912\n"
        "v_cvt_pk_bf16_f32 v168, v168, v169\n"
        "v_mfma_f32_16x16x32_bf16 v[96:99], v[32:35], v[200:203], v[96:99]\n"
        "ds_read_b128 v[220:223], v247 offset:36864\n"
        "v_cvt_pk_bf16_f32 v169, v170, v171\n"
        "v_mfma_f32_16x16x32_bf16 v[128:131], v[32:35], v[204:207], v[128:131]\n"
        "global_load_lds_dwordx4 v244, s[64:65] offset:2048\n"
        "v_cvt_pk_bf16_f32 v170, v172, v173\n"
        "v_mfma_f32_16x16x32_bf16 v[4:7], v[36:39], v[192:195], v[4:7]\n"
        "ds_read_b64_tr_b16 v[32:33], v226 offset:24576\n"
        "v_cvt_pk_bf16_f32 v171, v174, v175\n"
        "v_mfma_f32_16x16x32_bf16 v[68:71], v[36:39], v[196:199], v[68:71]\n"
        "ds_read_b64_tr_b16 v[34:35], v227 offset:24576\n"
        "v_accvgpr_write_b32 a196, v168\n"
        "v_mfma_f32_16x16x32_bf16 v[100:103], v[36:39], v[200:203], v[100:103]\n"
        "v_accvgpr_write_b32 a197, v169\n"
        "v_accvgpr_write_b32 a198, v170\n"
        "v_mfma_f32_16x16x32_bf16 v[132:135], v[36:39], v[204:207], v[132:135]\n"
        "global_load_lds_dwordx4 v245, s[64:65] offset:3072\n"
        "s_add_u32 m0, s72, 0x11000\n"
        "v_accvgpr_write_b32 a199, v171\n"
        "v_mfma_f32_16x16x32_bf16 v[8:11], v[40:43], v[192:195], v[8:11]\n"
        "ds_read_b64_tr_b16 v[36:37], v228 offset:24576\n"
        "s_add_u32 s64, s64, 0x80\n"
        "s_addc_u32 s65, s65, 0\n"
        "v_mfma_f32_16x16x32_bf16 v[72:75], v[40:43], v[196:199], v[72:75]\n"
        "ds_read_b64_tr_b16 v[38:39], v229 offset:24576\n"
        "v_accvgpr_read_b32 v169, a41\n"
        "v_mfma_f32_16x16x32_bf16 v[104:107], v[40:43], v[200:203], v[104:107]\n"
        "v_accvgpr_read_b32 v170, a42\n"
        "v_accvgpr_read_b32 v171, a43\n"
        "v_mfma_f32_16x16x32_bf16 v[136:139], v[40:43], v[204:207], v[136:139]\n"
        "global_load_lds_dwordx4 v242, s[66:67] offset:0\n"
        "ds_read_b64_tr_b16 v[40:41], v230 offset:24576\n"
        "v_mfma_f32_16x16x32_bf16 v[12:15], v[44:47], v[192:195], v[12:15]\n"
        "ds_read_b64_tr_b16 v[42:43], v231 offset:24576\n"
        "v_accvgpr_read_b32 v172, a44\n"
        "v_mfma_f32_16x16x32_bf16 v[76:79], v[44:47], v[196:199], v[76:79]\n"
        "v_accvgpr_read_b32 v173, a45\n"
        "v_accvgpr_read_b32 v174, a46\n"
        "v_mfma_f32_16x16x32_bf16 v[108:111], v[44:47], v[200:203], v[108:111]\n"
        "s_waitcnt lgkmcnt(4)\n"
        "v_accvgpr_read_b32 v175, a47\n"
        "v_mfma_f32_16x16x32_bf16 v[140:143], v[44:47], v[204:207], v[140:143]\n"
        "global_load_lds_dwordx4 v243, s[66:67] offset:1024\n"
        "ds_read_b64_tr_b16 v[44:45], v232 offset:24576\n"
        "v_mfma_f32_16x16x32_bf16 v[16:19], v[48:51], v[192:195], v[16:19]\n"
        "ds_read_b64_tr_b16 v[46:47], v233 offset:24576\n"
        "v_mfma_f32_16x16x32_bf16 v[80:83], v[48:51], v[196:199], v[80:83]\n"
        "v_mfma_f32_16x16x32_bf16 v[112:115], v[48:51], v[200:203], v[112:115]\n"
        "v_mfma_f32_16x16x32_bf16 v[144:147], v[48:51], v[204:207], v[144:147]\n"
        "global_store_dwordx4 v254, a[236:239], s[70:71] offset:1024 sc1\n"
        "ds_read_b64_tr_b16 v[48:49], v226 offset:25600\n"
        "ds_read_b64_tr_b16 v[50:51], v227 offset:25600\n"
        "s_add_u32 s70, s38, s97\n"
        "s_addc_u32 s71, s39, 0\n"
        "v_mfma_f32_16x16x32_bf16 v[20:23], v[52:55], v[192:195], v[20:23]\n"
        "s_add_u32 s97, s52, 0x0\n"
        "v_mfma_f32_16x16x32_bf16 v[84:87], v[52:55], v[196:199], v[84:87]\n"
        "v_mfma_f32_16x16x32_bf16 v[116:119], v[52:55], v[200:203], v[116:119]\n"
        "global_load_lds_dwordx4 v244, s[66:67] offset:2048\n"
        "v_mfma_f32_16x16x32_bf16 v[148:151], v[52:55], v[204:207], v[148:151]\n"
        "ds_read_b64_tr_b16 v[52:53], v228 offset:25600\n"
        "v_accvgpr_read_b32 v176, a0\n"
        "v_mfma_f32_16x16x32_bf16 v[24:27], v[56:59], v[192:195], v[24:27]\n"
        "ds_read_b64_tr_b16 v[54:55], v229 offset:25600\n"
        "v_cvt_pk_bf16_f32 v176, v176, v177\n"
        "v_mfma_f32_16x16x32_bf16 v[88:91], v[56:59], v[196:199], v[88:91]\n"
        "v_cvt_pk_bf16_f32 v177, v178, v179\n"
        "v_cvt_pk_bf16_f32 v178, v180, v181\n"
        "v_mfma_f32_16x16x32_bf16 v[120:123], v[56:59], v[200:203], v[120:123]\n"
        "global_load_lds_dwordx4 v245, s[66:67] offset:3072\n"
        "s_add_u32 m0, s73, 0x1c000\n"
        "v_mfma_f32_16x16x32_bf16 v[152:155], v[56:59], v[204:207], v[152:155]\n"
        "s_add_u32 s66, s66, 0x80\n"
        "s_addc_u32 s67, s67, 0\n"
        "v_cvt_pk_bf16_f32 v179, v182, v183\n"
        "v_mfma_f32_16x16x32_bf16 v[28:31], v[60:63], v[192:195], v[28:31]\n"
        "ds_read_b64_tr_b16 v[56:57], v230 offset:25600\n"
        "v_accvgpr_write_b32 a200, v176\n"
        "v_mfma_f32_16x16x32_bf16 v[92:95], v[60:63], v[196:199], v[92:95]\n"
        "ds_read_b64_tr_b16 v[58:59], v231 offset:25600\n"
        "v_accvgpr_write_b32 a201, v177\n"
        "v_mfma_f32_16x16x32_bf16 v[124:127], v[60:63], v[200:203], v[124:127]\n"
        "s_waitcnt lgkmcnt(4)\n"
        "v_accvgpr_write_b32 a202, v178\n"
        "v_accvgpr_write_b32 a203, v179\n"
        "v_mfma_f32_16x16x32_bf16 v[156:159], v[60:63], v[204:207], v[156:159]\n"
        "ds_read_b64_tr_b16 v[60:61], v232 offset:25600\n"
        "v_accvgpr_read_b32 v177, a65\n"
        "v_mfma_f32_16x16x32_bf16 v[0:3], v[32:35], v[208:211], v[0:3]\n"
        "ds_read_b64_tr_b16 v[62:63], v233 offset:25600\n"
        "v_accvgpr_read_b32 v178, a66\n"
        "v_mfma_f32_16x16x32_bf16 v[64:67], v[32:35], v[212:215], v[64:67]\n"
        "v_accvgpr_read_b32 v179, a67\n"
        "v_accvgpr_read_b32 v180, a68\n"
        "v_mfma_f32_16x16x32_bf16 v[96:99], v[32:35], v[216:219], v[96:99]\n"
        "v_accvgpr_read_b32 v181, a69\n"
        "v_accvgpr_read_b32 v182, a70\n"
        "v_mfma_f32_16x16x32_bf16 v[128:131], v[32:35], v[220:223], v[128:131]\n"
        "v_accvgpr_read_b32 v183, a71\n"
        "v_mfma_f32_16x16x32_bf16 v[4:7], v[36:39], v[208:211], v[4:7]\n"
        "v_mfma_f32_16x16x32_bf16 v[68:71], v[36:39], v[212:215], v[68:71]\n"
        "v_mfma_f32_16x16x32_bf16 v[100:103], v[36:39], v[216:219], v[100:103]\n"
        "v_mfma_f32_16x16x32_bf16 v[132:135], v[36:39], v[220:223], v[132:135]\n"
        "v_mfma_f32_16x16x32_bf16 v[8:11], v[40:43], v[208:211], v[8:11]\n"
        "s_waitcnt vmcnt(14) lgkmcnt(0)\n"
        "s_barrier\n"
        "v_mfma_f32_16x16x32_bf16 v[72:75], v[40:43], v[212:215], v[72:75]\n"
        "global_load_lds_dwordx4 v238, s[68:69] offset:0\n"
        "ds_read_b64_tr_b16 v[32:33], v226 offset:32768\n"
        "v_mfma_f32_16x16x32_bf16 v[104:107], v[40:43], v[216:219], v[104:107]\n"
        "ds_read_b64_tr_b16 v[34:35], v227 offset:32768\n"
        "v_mfma_f32_16x16x32_bf16 v[136:139], v[40:43], v[220:223], v[136:139]\n"
        "ds_read_b64_tr_b16 v[36:37], v228 offset:32768\n"
        "v_mfma_f32_16x16x32_bf16 v[12:15], v[44:47], v[208:211], v[12:15]\n"
        "ds_read_b64_tr_b16 v[38:39], v229 offset:32768\n"
        "v_mfma_f32_16x16x32_bf16 v[76:79], v[44:47], v[212:215], v[76:79]\n"
        "ds_read_b64_tr_b16 v[40:41], v230 offset:32768\n"
        "global_load_lds_dwordx4 v239, s[68:69] offset:1024\n"
        "v_mfma_f32_16x16x32_bf16 v[108:111], v[44:47], v[216:219], v[108:111]\n"
        "ds_read_b64_tr_b16 v[42:43], v231 offset:32768\n"
        "v_mfma_f32_16x16x32_bf16 v[140:143], v[44:47], v[220:223], v[140:143]\n"
        "ds_read_b128 v[192:195], v246 offset:2048\n"
        "v_accvgpr_read_b32 v184, a8\n"
        "v_mfma_f32_16x16x32_bf16 v[16:19], v[48:51], v[208:211], v[16:19]\n"
        "ds_read_b64_tr_b16 v[44:45], v232 offset:32768\n"
        "v_cvt_pk_bf16_f32 v184, v184, v185\n"
        "v_mfma_f32_16x16x32_bf16 v[80:83], v[48:51], v[212:215], v[80:83]\n"
        "ds_read_b64_tr_b16 v[46:47], v233 offset:32768\n"
        "global_load_lds_dwordx4 v240, s[68:69] offset:2048\n"
        "v_mfma_f32_16x16x32_bf16 v[112:115], v[48:51], v[216:219], v[112:115]\n"
        "ds_read_b128 v[196:199], v246 offset:0\n"
        "v_cvt_pk_bf16_f32 v185, v186, v187\n"
        "v_mfma_f32_16x16x32_bf16 v[144:147], v[48:51], v[220:223], v[144:147]\n"
        "ds_read_b64_tr_b16 v[48:49], v226 offset:33792\n"
        "v_cvt_pk_bf16_f32 v186, v188, v189\n"
        "v_mfma_f32_16x16x32_bf16 v[20:23], v[52:55], v[208:211], v[20:23]\n"
        "ds_read_b64_tr_b16 v[50:51], v227 offset:33792\n"
        "v_cvt_pk_bf16_f32 v187, v190, v191\n"
        "v_mfma_f32_16x16x32_bf16 v[84:87], v[52:55], v[212:215], v[84:87]\n"
        "ds_read_b128 v[200:203], v246 offset:6144\n"
        "v_accvgpr_write_b32 a204, v184\n"
        "v_mfma_f32_16x16x32_bf16 v[116:119], v[52:55], v[216:219], v[116:119]\n"
        "global_load_lds_dwordx4 v241, s[68:69] offset:3072\n"
        "s_add_u32 m0, s72, 0x8000\n"
        "v_accvgpr_write_b32 a205, v185\n"
        "v_mfma_f32_16x16x32_bf16 v[148:151], v[52:55], v[220:223], v[148:151]\n"
        "ds_read_b64_tr_b16 v[52:53], v228 offset:33792\n"
        "v_accvgpr_write_b32 a206, v186\n"
        "v_mfma_f32_16x16x32_bf16 v[24:27], v[56:59], v[208:211], v[24:27]\n"
        "ds_read_b64_tr_b16 v[54:55], v229 offset:33792\n"
        "v_accvgpr_write_b32 a207, v187\n"
        "v_mfma_f32_16x16x32_bf16 v[88:91], v[56:59], v[212:215], v[88:91]\n"
        "ds_read_b128 v[204:207], v246 offset:4096\n"
        "s_add_u32 s68, s68, 0x80000\n"
        "s_addc_u32 s69, s69, 0\n"
        "v_mfma_f32_16x16x32_bf16 v[120:123], v[56:59], v[216:219], v[120:123]\n"
        "global_load_lds_dwordx4 v242, s[64:65] offset:0\n"
        "v_accvgpr_read_b32 v185, a73\n"
        "v_mfma_f32_16x16x32_bf16 v[152:155], v[56:59], v[220:223], v[152:155]\n"
        "ds_read_b64_tr_b16 v[56:57], v230 offset:33792\n"
        "v_accvgpr_read_b32 v186, a74\n"
        "v_mfma_f32_16x16x32_bf16 v[28:31], v[60:63], v[208:211], v[28:31]\n"
        "ds_read_b64_tr_b16 v[58:59], v231 offset:33792\n"
        "v_accvgpr_read_b32 v187, a75\n"
        "v_mfma_f32_16x16x32_bf16 v[92:95], v[60:63], v[212:215], v[92:95]\n"
        "ds_read_b128 v[208:211], v247 offset:2048\n"
        "v_accvgpr_read_b32 v188, a76\n"
        "v_mfma_f32_16x16x32_bf16 v[124:127], v[60:63], v[216:219], v[124:127]\n"
        "global_store_dwordx4 v254, a[192:195], s[70:71] offset:0 sc1\n"
        "ds_read_b128 v[212:215], v247 offset:0\n"
        "ds_read_b128 v[216:219], v247 offset:6144\n"
        "v_accvgpr_read_b32 v189, a77\n"
        "v_mfma_f32_16x16x32_bf16 v[156:159], v[60:63], v[220:223], v[156:159]\n"
        "ds_read_b64_tr_b16 v[60:61], v232 offset:33792\n"
        "s_waitcnt lgkmcnt(7)\n"
        "v_mfma_f32_16x16x32_bf16 v[0:3], v[32:35], v[192:195], v[0:3]\n"
        "ds_read_b64_tr_b16 v[62:63], v233 offset:33792\n"
        "v_accvgpr_read_b32 v190, a78\n"
        "v_mfma_f32_16x16x32_bf16 v[64:67], v[32:35], v[196:199], v[64:67]\n"
        "global_load_lds_dwordx4 v243, s[64:65] offset:1024\n"
        "v_mfma_f32_16x16x32_bf16 v[96:99], v[32:35], v[200:203], v[96:99]\n"
        "ds_read_b128 v[220:223], v247 offset:4096\n"
        "s_waitcnt lgkmcnt(7)\n"
        "v_mfma_f32_16x16x32_bf16 v[128:131], v[32:35], v[204:207], v[128:131]\n"
        "ds_read_b64_tr_b16 v[32:33], v226 offset:40960\n"
        "v_accvgpr_read_b32 v191, a79\n"
        "v_mfma_f32_16x16x32_bf16 v[4:7], v[36:39], v[192:195], v[4:7]\n"
        "ds_read_b64_tr_b16 v[34:35], v227 offset:40960\n"
        "v_mfma_f32_16x16x32_bf16 v[68:71], v[36:39], v[196:199], v[68:71]\n"
        "global_load_lds_dwordx4 v244, s[64:65] offset:2048\n"
        "v_mfma_f32_16x16x32_bf16 v[100:103], v[36:39], v[200:203], v[100:103]\n"
        "v_mfma_f32_16x16x32_bf16 v[132:135], v[36:39], v[204:207], v[132:135]\n"
        "ds_read_b64_tr_b16 v[36:37], v228 offset:40960\n"
        "v_accvgpr_read_b32 v160, a32\n"
        "v_mfma_f32_16x16x32_bf16 v[8:11], v[40:43], v[192:195], v[8:11]\n"
        "ds_read_b64_tr_b16 v[38:39], v229 offset:40960\n"
        "v_cvt_pk_bf16_f32 v160, v160, v161\n"
        "v_mfma_f32_16x16x32_bf16 v[72:75], v[40:43], v[196:199], v[72:75]\n"
        "global_load_lds_dwordx4 v245, s[64:65] offset:3072\n"
        "s_add_u32 m0, s72, 0x9000\n"
        "v_mfma_f32_16x16x32_bf16 v[104:107], v[40:43], v[200:203], v[104:107]\n"
        "s_add_u32 s64, s64, 0x80\n"
        "s_addc_u32 s65, s65, 0\n"
        "v_cvt_pk_bf16_f32 v161, v162, v163\n"
        "v_mfma_f32_16x16x32_bf16 v[136:139], v[40:43], v[204:207], v[136:139]\n"
        "ds_read_b64_tr_b16 v[40:41], v230 offset:40960\n"
        "v_cvt_pk_bf16_f32 v162, v164, v165\n"
        "v_mfma_f32_16x16x32_bf16 v[12:15], v[44:47], v[192:195], v[12:15]\n"
        "ds_read_b64_tr_b16 v[42:43], v231 offset:40960\n"
        "v_cvt_pk_bf16_f32 v163, v166, v167\n"
        "v_mfma_f32_16x16x32_bf16 v[76:79], v[44:47], v[196:199], v[76:79]\n"
        "v_accvgpr_write_b32 a160, v160\n"
        "v_accvgpr_write_b32 a161, v161\n"
        "v_mfma_f32_16x16x32_bf16 v[108:111], v[44:47], v[200:203], v[108:111]\n"
        "global_load_lds_dwordx4 v242, s[66:67] offset:0\n"
        "v_accvgpr_write_b32 a162, v162\n"
        "v_mfma_f32_16x16x32_bf16 v[140:143], v[44:47], v[204:207], v[140:143]\n"
        "ds_read_b64_tr_b16 v[44:45], v232 offset:40960\n"
        "v_accvgpr_write_b32 a163, v163\n"
        "v_mfma_f32_16x16x32_bf16 v[16:19], v[48:51], v[192:195], v[16:19]\n"
        "ds_read_b64_tr_b16 v[46:47], v233 offset:40960\n"
        "v_accvgpr_read_b32 v161, a97\n"
        "v_mfma_f32_16x16x32_bf16 v[80:83], v[48:51], v[196:199], v[80:83]\n"
        "v_accvgpr_read_b32 v162, a98\n"
        "v_accvgpr_read_b32 v163, a99\n"
        "v_mfma_f32_16x16x32_bf16 v[112:115], v[48:51], v[200:203], v[112:115]\n"
        "global_load_lds_dwordx4 v243, s[66:67] offset:1024\n"
        "v_accvgpr_read_b32 v164, a100\n"
        "v_mfma_f32_16x16x32_bf16 v[144:147], v[48:51], v[204:207], v[144:147]\n"
        "ds_read_b64_tr_b16 v[48:49], v226 offset:41984\n"
        "v_accvgpr_read_b32 v165, a101\n"
        "v_mfma_f32_16x16x32_bf16 v[20:23], v[52:55], v[192:195], v[20:23]\n"
        "ds_read_b64_tr_b16 v[50:51], v227 offset:41984\n"
        "v_accvgpr_read_b32 v166, a102\n"
        "v_mfma_f32_16x16x32_bf16 v[84:87], v[52:55], v[196:199], v[84:87]\n"
        "s_waitcnt lgkmcnt(4)\n"
        "v_accvgpr_read_b32 v167, a103\n"
        "v_mfma_f32_16x16x32_bf16 v[116:119], v[52:55], v[200:203], v[116:119]\n"
        "global_load_lds_dwordx4 v244, s[66:67] offset:2048\n"
        "v_mfma_f32_16x16x32_bf16 v[148:151], v[52:55], v[204:207], v[148:151]\n"
        "ds_read_b64_tr_b16 v[52:53], v228 offset:41984\n"
        "v_mfma_f32_16x16x32_bf16 v[24:27], v[56:59], v[192:195], v[24:27]\n"
        "ds_read_b64_tr_b16 v[54:55], v229 offset:41984\n"
        "v_mfma_f32_16x16x32_bf16 v[88:91], v[56:59], v[196:199], v[88:91]\n"
        "v_mfma_f32_16x16x32_bf16 v[120:123], v[56:59], v[200:203], v[120:123]\n"
        "global_load_lds_dwordx4 v245, s[66:67] offset:3072\n"
        "s_add_u32 s66, s66, 0x80\n"
        "s_addc_u32 s67, s67, 0\n"
        "v_mfma_f32_16x16x32_bf16 v[152:155], v[56:59], v[204:207], v[152:155]\n"
        "ds_read_b64_tr_b16 v[56:57], v230 offset:41984\n"
        "s_add_u32 m0, s73, 0x20000\n"
        "v_mfma_f32_16x16x32_bf16 v[28:31], v[60:63], v[192:195], v[28:31]\n"
        "ds_read_b64_tr_b16 v[58:59], v231 offset:41984\n"
        "v_mfma_f32_16x16x32_bf16 v[92:95], v[60:63], v[196:199], v[92:95]\n"
        "v_mfma_f32_16x16x32_bf16 v[124:127], v[60:63], v[200:203], v[124:127]\n"
        "v_mfma_f32_16x16x32_bf16 v[156:159], v[60:63], v[204:207], v[156:159]\n"
        "ds_read_b64_tr_b16 v[60:61], v232 offset:41984\n"
        "v_accvgpr_read_b32 v168, a40\n"
        "v_mfma_f32_16x16x32_bf16 v[0:3], v[32:35], v[208:211], v[0:3]\n"
        "ds_read_b64_tr_b16 v[62:63], v233 offset:41984\n"
        "v_cvt_pk_bf16_f32 v168, v168, v169\n"
        "v_mfma_f32_16x16x32_bf16 v[64:67], v[32:35], v[212:215], v[64:67]\n"
        "v_cvt_pk_bf16_f32 v169, v170, v171\n"
        "v_cvt_pk_bf16_f32 v170, v172, v173\n"
        "v_mfma_f32_16x16x32_bf16 v[96:99], v[32:35], v[216:219], v[96:99]\n"
        "v_cvt_pk_bf16_f32 v171, v174, v175\n"
        "v_accvgpr_write_b32 a164, v168\n"
        "v_mfma_f32_16x16x32_bf16 v[128:131], v[32:35], v[220:223], v[128:131]\n"
        "v_accvgpr_write_b32 a165, v169\n"
        "v_accvgpr_write_b32 a166, v170\n"
        "v_mfma_f32_16x16x32_bf16 v[4:7], v[36:39], v[208:211], v[4:7]\n"
        "v_accvgpr_write_b32 a167, v171\n"
        "v_accvgpr_read_b32 v169, a105\n"
        "v_mfma_f32_16x16x32_bf16 v[68:71], v[36:39], v[212:215], v[68:71]\n"
        "v_accvgpr_read_b32 v170, a106\n"
        "v_accvgpr_read_b32 v171, a107\n"
        "v_mfma_f32_16x16x32_bf16 v[100:103], v[36:39], v[216:219], v[100:103]\n"
        "s_waitcnt lgkmcnt(2)\n"
        "v_accvgpr_read_b32 v172, a108\n"
        "v_accvgpr_read_b32 v173, a109\n"
        "v_mfma_f32_16x16x32_bf16 v[132:135], v[36:39], v[220:223], v[132:135]\n"
        "global_store_dwordx4 v254, a[196:199], s[70:71] offset:1024 sc1\n"
        "s_waitcnt vmcnt(14) lgkmcnt(0)\n"
        "s_barrier\n"
        "v_mfma_f32_16x16x32_bf16 v[8:11], v[40:43], v[208:211], v[8:11]\n"
        "ds_read_b64_tr_b16 v[32:33], v226 offset:49152\n"
        "s_add_u32 s70, s38, s97\n"
        "s_addc_u32 s71, s39, 0\n"
        "v_mfma_f32_16x16x32_bf16 v[72:75], v[40:43], v[212:215], v[72:75]\n"
        "ds_read_b64_tr_b16 v[34:35], v227 offset:49152\n"
        "s_add_u32 s97, s52, 0x1000\n"
        "v_accvgpr_read_b32 v174, a110\n"
        "v_mfma_f32_16x16x32_bf16 v[104:107], v[40:43], v[216:219], v[104:107]\n"
        "global_load_lds_dwordx4 v238, s[68:69] offset:0\n"
        "v_mfma_f32_16x16x32_bf16 v[136:139], v[40:43], v[220:223], v[136:139]\n"
        "ds_read_b64_tr_b16 v[36:37], v228 offset:49152\n"
        "v_accvgpr_read_b32 v175, a111\n"
        "v_mfma_f32_16x16x32_bf16 v[12:15], v[44:47], v[208:211], v[12:15]\n"
        "ds_read_b64_tr_b16 v[38:39], v229 offset:49152\n"
        "v_mfma_f32_16x16x32_bf16 v[76:79], v[44:47], v[212:215], v[76:79]\n"
        "ds_read_b64_tr_b16 v[40:41], v230 offset:49152\n"
        "v_mfma_f32_16x16x32_bf16 v[108:111], v[44:47], v[216:219], v[108:111]\n"
        "ds_read_b64_tr_b16 v[42:43], v231 offset:49152\n"
        "global_load_lds_dwordx4 v239, s[68:69] offset:1024\n"
        "v_mfma_f32_16x16x32_bf16 v[140:143], v[44:47], v[220:223], v[140:143]\n"
        "ds_read_b128 v[192:195], v248 offset:2048\n"
        "v_mfma_f32_16x16x32_bf16 v[16:19], v[48:51], v[208:211], v[16:19]\n"
        "ds_read_b64_tr_b16 v[44:45], v232 offset:49152\n"
        "v_mfma_f32_16x16x32_bf16 v[80:83], v[48:51], v[212:215], v[80:83]\n"
        "ds_read_b64_tr_b16 v[46:47], v233 offset:49152\n"
        "v_mfma_f32_16x16x32_bf16 v[112:115], v[48:51], v[216:219], v[112:115]\n"
        "ds_read_b128 v[196:199], v248 offset:0\n"
        "global_load_lds_dwordx4 v240, s[68:69] offset:2048\n"
        "v_mfma_f32_16x16x32_bf16 v[144:147], v[48:51], v[220:223], v[144:147]\n"
        "ds_read_b64_tr_b16 v[48:49], v226 offset:50176\n"
        "v_mfma_f32_16x16x32_bf16 v[20:23], v[52:55], v[208:211], v[20:23]\n"
        "ds_read_b64_tr_b16 v[50:51], v227 offset:50176\n"
        "v_mfma_f32_16x16x32_bf16 v[84:87], v[52:55], v[212:215], v[84:87]\n"
        "ds_read_b128 v[200:203], v248 offset:6144\n"
        "v_mfma_f32_16x16x32_bf16 v[116:119], v[52:55], v[216:219], v[116:119]\n"
        "global_load_lds_dwordx4 v241, s[68:69] offset:3072\n"
        "s_add_u32 m0, s72, 0x0\n"
        "v_mfma_f32_16x16x32_bf16 v[148:151], v[52:55], v[220:223], v[148:151]\n"
        "ds_read_b64_tr_b16 v[52:53], v228 offset:50176\n"
        "v_accvgpr_read_b32 v176, a64\n"
        "v_mfma_f32_16x16x32_bf16 v[24:27], v[56:59], v[208:211], v[24:27]\n"
        "ds_read_b64_tr_b16 v[54:55], v229 offset:50176\n"
        "s_add_u32 s68, s68, 0x80000\n"
        "s_addc_u32 s69, s69, 0\n"
        "v_mfma_f32_16x16x32_bf16 v[88:91], v[56:59], v[212:215], v[88:91]\n"
        "ds_read_b128 v[204:207], v248 offset:4096\n"
        "v_cvt_pk_bf16_f32 v176, v176, v177\n"
        "v_mfma_f32_16x16x32_bf16 v[120:123], v[56:59], v[216:219], v[120:123]\n"
        "global_load_lds_dwordx4 v242, s[64:65] offset:0\n"
        "v_mfma_f32_16x16x32_bf16 v[152:155], v[56:59], v[220:223], v[152:155]\n"
        "s_waitcnt lgkmcnt(5)\n"
        "v_cvt_pk_bf16_f32 v177, v178, v179\n"
        "v_cvt_pk_bf16_f32 v178, v180, v181\n"
        "v_mfma_f32_16x16x32_bf16 v[28:31], v[60:63], v[208:211], v[28:31]\n"
        "s_waitcnt lgkmcnt(4)\n"
        "v_cvt_pk_bf16_f32 v179, v182, v183\n"
        "v_accvgpr_write_b32 a168, v176\n"
        "v_mfma_f32_16x16x32_bf16 v[92:95], v[60:63], v[212:215], v[92:95]\n"
        "s_waitcnt lgkmcnt(3)\n"
        "v_accvgpr_write_b32 a169, v177\n"
        "v_accvgpr_write_b32 a170, v178\n"
        "v_mfma_f32_16x16x32_bf16 v[124:127], v[60:63], v[216:219], v[124:127]\n"
        "ds_read_b64_tr_b16 v[56:57], v230 offset:50176\n"
        "v_accvgpr_write_b32 a171, v179\n"
        "v_mfma_f32_16x16x32_bf16 v[156:159], v[60:63], v[220:223], v[156:159]\n"
        "ds_read_b64_tr_b16 v[58:59], v231 offset:50176\n"
        "global_load_lds_dwordx4 v243, s[64:65] offset:1024\n"
        "v_mfma_f32_16x16x32_bf16 v[0:3], v[32:35], v[192:195], v[0:3]\n"
        "ds_read_b64_tr_b16 v[60:61], v232 offset:50176\n"
        "v_accvgpr_read_b32 v177, a129\n"
        "v_mfma_f32_16x16x32_bf16 v[64:67], v[32:35], v[196:199], v[64:67]\n"
        "ds_read_b64_tr_b16 v[62:63], v233 offset:50176\n"
        "v_accvgpr_read_b32 v178, a130\n"
        "v_mfma_f32_16x16x32_bf16 v[96:99], v[32:35], v[200:203], v[96:99]\n"
        "ds_read_b128 v[208:211], v249 offset:2048\n"
        "s_waitcnt lgkmcnt(5)\n"
        "v_mfma_f32_16x16x32_bf16 v[128:131], v[32:35], v[204:207], v[128:131]\n"
        "global_load_lds_dwordx4 v244, s[64:65] offset:2048\n"
        "v_mfma_f32_16x16x32_bf16 v[4:7], v[36:39], v[192:195], v[4:7]\n"
        "ds_read_b64_tr_b16 v[32:33], v226 offset:57344\n"
        "v_accvgpr_read_b32 v179, a131\n"
        "v_mfma_f32_16x16x32_bf16 v[68:71], v[36:39], v[196:199], v[68:71]\n"
        "ds_read_b64_tr_b16 v[34:35], v227 offset:57344\n"
        "v_accvgpr_read_b32 v180, a132\n"
        "v_mfma_f32_16x16x32_bf16 v[100:103], v[36:39], v[200:203], v[100:103]\n"
        "ds_read_b128 v[212:215], v249 offset:0\n"
        "v_accvgpr_read_b32 v181, a133\n"
        "v_mfma_f32_16x16x32_bf16 v[132:135], v[36:39], v[204:207], v[132:135]\n"
        "global_load_lds_dwordx4 v245, s[64:65] offset:3072\n"
        "s_add_u32 m0, s72, 0x1000\n"
        "v_mfma_f32_16x16x32_bf16 v[8:11], v[40:43], v[192:195], v[8:11]\n"
        "ds_read_b64_tr_b16 v[36:37], v228 offset:57344\n"
        "s_add_u32 s64, s64, 0x80\n"
        "s_addc_u32 s65, s65, 0\n"
        "v_mfma_f32_16x16x32_bf16 v[72:75], v[40:43], v[196:199], v[72:75]\n"
        "ds_read_b64_tr_b16 v[38:39], v229 offset:57344\n"
        "v_accvgpr_read_b32 v182, a134\n"
        "v_mfma_f32_16x16x32_bf16 v[104:107], v[40:43], v[200:203], v[104:107]\n"
        "ds_read_b128 v[216:219], v249 offset:6144\n"
        "v_accvgpr_read_b32 v183, a135\n"
        "v_mfma_f32_16x16x32_bf16 v[136:139], v[40:43], v[204:207], v[136:139]\n"
        "global_load_lds_dwordx4 v242, s[66:67] offset:0\n"
        "v_mfma_f32_16x16x32_bf16 v[12:15], v[44:47], v[192:195], v[12:15]\n"
        "ds_read_b64_tr_b16 v[40:41], v230 offset:57344\n"
        "v_mfma_f32_16x16x32_bf16 v[76:79], v[44:47], v[196:199], v[76:79]\n"
        "ds_read_b64_tr_b16 v[42:43], v231 offset:57344\n"
        "v_mfma_f32_16x16x32_bf16 v[108:111], v[44:47], v[200:203], v[108:111]\n"
        "ds_read_b128 v[220:223], v249 offset:4096\n"
        "v_mfma_f32_16x16x32_bf16 v[140:143], v[44:47], v[204:207], v[140:143]\n"
        "global_load_lds_dwordx4 v243, s[66:67] offset:1024\n"
        "v_mfma_f32_16x16x32_bf16 v[16:19], v[48:51], v[192:195], v[16:19]\n"
        "ds_read_b64_tr_b16 v[44:45], v232 offset:57344\n"
        "v_accvgpr_read_b32 v184, a72\n"
        "v_mfma_f32_16x16x32_bf16 v[80:83], v[48:51], v[196:199], v[80:83]\n"
        "ds_read_b64_tr_b16 v[46:47], v233 offset:57344\n"
        "v_cvt_pk_bf16_f32 v184, v184, v185\n"
        "v_mfma_f32_16x16x32_bf16 v[112:115], v[48:51], v[200:203], v[112:115]\n"
        "s_waitcnt lgkmcnt(5)\n"
        "v_cvt_pk_bf16_f32 v185, v186, v187\n"
        "v_cvt_pk_bf16_f32 v186, v188, v189\n"
        "v_mfma_f32_16x16x32_bf16 v[144:147], v[48:51], v[204:207], v[144:147]\n"
        "ds_read_b64_tr_b16 v[48:49], v226 offset:58368\n"
        "v_cvt_pk_bf16_f32 v187, v190, v191\n"
        "v_mfma_f32_16x16x32_bf16 v[20:23], v[52:55], v[192:195], v[20:23]\n"
        "global_load_lds_dwordx4 v244, s[66:67] offset:2048\n"
        "v_accvgpr_write_b32 a172, v184\n"
        "v_mfma_f32_16x16x32_bf16 v[84:87], v[52:55], v[196:199], v[84:87]\n"
        "ds_read_b64_tr_b16 v[50:51], v227 offset:58368\n"
        "v_accvgpr_write_b32 a173, v185\n"
        "v_mfma_f32_16x16x32_bf16 v[116:119], v[52:55], v[200:203], v[116:119]\n"
        "v_accvgpr_write_b32 a174, v186\n"
        "v_accvgpr_write_b32 a175, v187\n"
        "v_mfma_f32_16x16x32_bf16 v[148:151], v[52:55], v[204:207], v[148:151]\n"
        "ds_read_b64_tr_b16 v[52:53], v228 offset:58368\n"
        "v_accvgpr_read_b32 v185, a137\n"
        "v_mfma_f32_16x16x32_bf16 v[24:27], v[56:59], v[192:195], v[24:27]\n"
        "global_store_dwordx4 v254, a[200:203], s[70:71] offset:0 sc1\n"
        "ds_read_b64_tr_b16 v[54:55], v229 offset:58368\n"
        "v_accvgpr_read_b32 v186, a138\n"
        "v_accvgpr_read_b32 v187, a139\n"
        "v_accvgpr_read_b32 v188, a140\n"
        "v_mfma_f32_16x16x32_bf16 v[88:91], v[56:59], v[196:199], v[88:91]\n"
        "v_accvgpr_read_b32 v189, a141\n"
        "v_accvgpr_read_b32 v190, a142\n"
        "v_mfma_f32_16x16x32_bf16 v[120:123], v[56:59], v[200:203], v[120:123]\n"
        "v_accvgpr_read_b32 v191, a143\n"
        "v_mfma_f32_16x16x32_bf16 v[152:155], v[56:59], v[204:207], v[152:155]\n"
        "global_load_lds_dwordx4 v245, s[66:67] offset:3072\n"
        "s_add_u32 m0, s73, 0x24000\n"
        "v_mfma_f32_16x16x32_bf16 v[28:31], v[60:63], v[192:195], v[28:31]\n"
        "s_add_u32 s66, s66, 0x80\n"
        "s_addc_u32 s67, s67, 0\n"
        "v_mfma_f32_16x16x32_bf16 v[92:95], v[60:63], v[196:199], v[92:95]\n"
        "ds_read_b64_tr_b16 v[56:57], v230 offset:58368\n"
        "s_waitcnt lgkmcnt(3)\n"
        "v_mfma_f32_16x16x32_bf16 v[124:127], v[60:63], v[200:203], v[124:127]\n"
        "ds_read_b64_tr_b16 v[58:59], v231 offset:58368\n"
        "v_mfma_f32_16x16x32_bf16 v[156:159], v[60:63], v[204:207], v[156:159]\n"
        "ds_read_b64_tr_b16 v[60:61], v232 offset:58368\n"
        "v_mfma_f32_16x16x32_bf16 v[0:3], v[32:35], v[208:211], v[0:3]\n"
        "ds_read_b64_tr_b16 v[62:63], v233 offset:58368\n"
        "v_mfma_f32_16x16x32_bf16 v[64:67], v[32:35], v[212:215], v[64:67]\n"
        "v_mfma_f32_16x16x32_bf16 v[96:99], v[32:35], v[216:219], v[96:99]\n"
        "v_mfma_f32_16x16x32_bf16 v[128:131], v[32:35], v[220:223], v[128:131]\n"
        "v_mfma_f32_16x16x32_bf16 v[4:7], v[36:39], v[208:211], v[4:7]\n"
        "v_mfma_f32_16x16x32_bf16 v[68:71], v[36:39], v[212:215], v[68:71]\n"
        "v_mfma_f32_16x16x32_bf16 v[100:103], v[36:39], v[216:219], v[100:103]\n"
        "v_mfma_f32_16x16x32_bf16 v[132:135], v[36:39], v[220:223], v[132:135]\n"
        "v_accvgpr_read_b32 v160, a96\n"
        "v_cvt_pk_bf16_f32 v160, v160, v161\n"
        "v_mfma_f32_16x16x32_bf16 v[8:11], v[40:43], v[208:211], v[8:11]\n"
        "s_waitcnt vmcnt(14) lgkmcnt(0)\n"
        "s_barrier\n"
        "v_cvt_pk_bf16_f32 v161, v162, v163\n"
        "v_mfma_f32_16x16x32_bf16 v[72:75], v[40:43], v[212:215], v[72:75]\n"
        "global_load_lds_dwordx4 v238, s[68:69] offset:0\n"
        "ds_read_b64_tr_b16 v[32:33], v226 offset:16384\n"
        "v_mfma_f32_16x16x32_bf16 v[104:107], v[40:43], v[216:219], v[104:107]\n"
        "ds_read_b64_tr_b16 v[34:35], v227 offset:16384\n"
        "v_cvt_pk_bf16_f32 v162, v164, v165\n"
        "v_mfma_f32_16x16x32_bf16 v[136:139], v[40:43], v[220:223], v[136:139]\n"
        "ds_read_b64_tr_b16 v[36:37], v228 offset:16384\n"
        "v_cvt_pk_bf16_f32 v163, v166, v167\n"
        "v_mfma_f32_16x16x32_bf16 v[12:15], v[44:47], v[208:211], v[12:15]\n"
        "ds_read_b64_tr_b16 v[38:39], v229 offset:16384\n"
        "v_accvgpr_write_b32 a0, v160\n"
        "v_mfma_f32_16x16x32_bf16 v[76:79], v[44:47], v[212:215], v[76:79]\n"
        "ds_read_b64_tr_b16 v[40:41], v230 offset:16384\n"
        "v_accvgpr_write_b32 a1, v161\n"
        "v_mfma_f32_16x16x32_bf16 v[108:111], v[44:47], v[216:219], v[108:111]\n"
        "ds_read_b64_tr_b16 v[42:43], v231 offset:16384\n"
        "v_accvgpr_write_b32 a2, v162\n"
        "v_mfma_f32_16x16x32_bf16 v[140:143], v[44:47], v[220:223], v[140:143]\n"
        "ds_read_b128 v[192:195], v246 offset:34816\n"
        "v_accvgpr_write_b32 a3, v163\n"
        "v_mfma_f32_16x16x32_bf16 v[16:19], v[48:51], v[208:211], v[16:19]\n"
        "global_load_lds_dwordx4 v239, s[68:69] offset:1024\n"
        "ds_read_b64_tr_b16 v[44:45], v232 offset:16384\n"
        "v_mfma_f32_16x16x32_bf16 v[80:83], v[48:51], v[212:215], v[80:83]\n"
        "ds_read_b64_tr_b16 v[46:47], v233 offset:16384\n"
        "v_accvgpr_read_b32 v161, a209\n"
        "v_mfma_f32_16x16x32_bf16 v[112:115], v[48:51], v[216:219], v[112:115]\n"
        "ds_read_b128 v[196:199], v246 offset:32768\n"
        "v_accvgpr_read_b32 v162, a210\n"
        "v_mfma_f32_16x16x32_bf16 v[144:147], v[48:51], v[220:223], v[144:147]\n"
        "ds_read_b64_tr_b16 v[48:49], v226 offset:17408\n"
        "v_accvgpr_read_b32 v163, a211\n"
        "v_mfma_f32_16x16x32_bf16 v[20:23], v[52:55], v[208:211], v[20:23]\n"
        "global_load_lds_dwordx4 v240, s[68:69] offset:2048\n"
        "ds_read_b64_tr_b16 v[50:51], v227 offset:17408\n"
        "v_mfma_f32_16x16x32_bf16 v[84:87], v[52:55], v[212:215], v[84:87]\n"
        "ds_read_b128 v[200:203], v246 offset:38912\n"
        "v_accvgpr_read_b32 v164, a240\n"
        "v_mfma_f32_16x16x32_bf16 v[116:119], v[52:55], v[216:219], v[116:119]\n"
        "ds_read_b128 v[204:207], v246 offset:36864\n"
        "v_accvgpr_read_b32 v165, a241\n"
        "v_mfma_f32_16x16x32_bf16 v[148:151], v[52:55], v[220:223], v[148:151]\n"
        "ds_read_b64_tr_b16 v[52:53], v228 offset:17408\n"
        "v_accvgpr_read_b32 v166, a242\n"
        "v_mfma_f32_16x16x32_bf16 v[24:27], v[56:59], v[208:211], v[24:27]\n"
        "global_load_lds_dwordx4 v241, s[68:69] offset:3072\n"
        "ds_read_b64_tr_b16 v[54:55], v229 offset:17408\n"
        "s_add_u32 m0, s72, 0x10000\n"
        "v_mfma_f32_16x16x32_bf16 v[88:91], v[56:59], v[212:215], v[88:91]\n"
        "s_add_u32 s68, s68, 0x80000\n"
        "s_addc_u32 s69, s69, 0\n"
        "v_accvgpr_read_b32 v167, a243\n"
        "v_mfma_f32_16x16x32_bf16 v[120:123], v[56:59], v[216:219], v[120:123]\n"
        "s_waitcnt lgkmcnt(6)\n"
        "v_mfma_f32_16x16x32_bf16 v[152:155], v[56:59], v[220:223], v[152:155]\n"
        "ds_read_b64_tr_b16 v[56:57], v230 offset:17408\n"
        "v_mfma_f32_16x16x32_bf16 v[28:31], v[60:63], v[208:211], v[28:31]\n"
        "global_load_lds_dwordx4 v242, s[64:65] offset:0\n"
        "ds_read_b64_tr_b16 v[58:59], v231 offset:17408\n"
        "v_mfma_f32_16x16x32_bf16 v[92:95], v[60:63], v[212:215], v[92:95]\n"
        "ds_read_b128 v[208:211], v247 offset:34816\n"
        "s_waitcnt lgkmcnt(6)\n"
        "v_mfma_f32_16x16x32_bf16 v[124:127], v[60:63], v[216:219], v[124:127]\n"
        "ds_read_b128 v[212:215], v247 offset:32768\n"
        "s_waitcnt lgkmcnt(6)\n"
        "v_mfma_f32_16x16x32_bf16 v[156:159], v[60:63], v[220:223], v[156:159]\n"
        "ds_read_b64_tr_b16 v[60:61], v232 offset:17408\n"
        "v_accvgpr_read_b32 v168, a104\n"
        "v_mfma_f32_16x16x32_bf16 v[0:3], v[32:35], v[192:195], v[0:3]\n"
        "global_load_lds_dwordx4 v243, s[64:65] offset:1024\n"
        "ds_read_b64_tr_b16 v[62:63], v233 offset:17408\n"
        "v_mfma_f32_16x16x32_bf16 v[64:67], v[32:35], v[196:199], v[64:67]\n"
        "ds_read_b128 v[216:219], v247 offset:38912\n"
        "v_cvt_pk_bf16_f32 v168, v168, v169\n"
        "v_mfma_f32_16x16x32_bf16 v[96:99], v[32:35], v[200:203], v[96:99]\n"
        "ds_read_b128 v[220:223], v247 offset:36864\n"
        "v_cvt_pk_bf16_f32 v169, v170, v171\n"
        "v_mfma_f32_16x16x32_bf16 v[128:131], v[32:35], v[204:207], v[128:131]\n"
        "ds_read_b64_tr_b16 v[32:33], v226 offset:24576\n"
        "v_cvt_pk_bf16_f32 v170, v172, v173\n"
        "v_mfma_f32_16x16x32_bf16 v[4:7], v[36:39], v[192:195], v[4:7]\n"
        "global_load_lds_dwordx4 v244, s[64:65] offset:2048\n"
        "v_cvt_pk_bf16_f32 v171, v174, v175\n"
        "v_mfma_f32_16x16x32_bf16 v[68:71], v[36:39], v[196:199], v[68:71]\n"
        "ds_read_b64_tr_b16 v[34:35], v227 offset:24576\n"
        "v_accvgpr_write_b32 a4, v168\n"
        "v_mfma_f32_16x16x32_bf16 v[100:103], v[36:39], v[200:203], v[100:103]\n"
        "v_accvgpr_write_b32 a5, v169\n"
        "v_accvgpr_write_b32 a6, v170\n"
        "v_mfma_f32_16x16x32_bf16 v[132:135], v[36:39], v[204:207], v[132:135]\n"
        "ds_read_b64_tr_b16 v[36:37], v228 offset:24576\n"
        "v_accvgpr_write_b32 a7, v171\n"
        "v_mfma_f32_16x16x32_bf16 v[8:11], v[40:43], v[192:195], v[8:11]\n"
        "global_store_dwordx4 v254, a[204:207], s[70:71] offset:1024 sc1\n"
        "ds_read_b64_tr_b16 v[38:39], v229 offset:24576\n"
        "s_add_u32 s70, s38, s97\n"
        "s_addc_u32 s71, s39, 0\n"
        "s_add_u32 s97, s52, 0x2000\n"
        "v_accvgpr_read_b32 v169, a213\n"
        "v_mfma_f32_16x16x32_bf16 v[72:75], v[40:43], v[196:199], v[72:75]\n"
        "v_accvgpr_read_b32 v170, a214\n"
        "v_accvgpr_read_b32 v171, a215\n"
        "v_mfma_f32_16x16x32_bf16 v[104:107], v[40:43], v[200:203], v[104:107]\n"
        "v_accvgpr_read_b32 v172, a244\n"
        "v_accvgpr_read_b32 v173, a245\n"
        "v_mfma_f32_16x16x32_bf16 v[136:139], v[40:43], v[204:207], v[136:139]\n"
        "global_load_lds_dwordx4 v245, s[64:65] offset:3072\n"
        "s_add_u32 m0, s72, 0x11000\n"
        "v_mfma_f32_16x16x32_bf16 v[12:15], v[44:47], v[192:195], v[12:15]\n"
        "ds_read_b64_tr_b16 v[40:41], v230 offset:24576\n"
        "s_add_u32 s64, s64, 0x80\n"
        "s_addc_u32 s65, s65, 0\n"
        "v_mfma_f32_16x16x32_bf16 v[76:79], v[44:47], v[196:199], v[76:79]\n"
        "ds_read_b64_tr_b16 v[42:43], v231 offset:24576\n"
        "v_accvgpr_read_b32 v174, a246\n"
        "v_mfma_f32_16x16x32_bf16 v[108:111], v[44:47], v[200:203], v[108:111]\n"
        "s_waitcnt lgkmcnt(4)\n"
        "v_accvgpr_read_b32 v175, a247\n"
        "v_mfma_f32_16x16x32_bf16 v[140:143], v[44:47], v[204:207], v[140:143]\n"
        "global_load_lds_dwordx4 v242, s[66:67] offset:0\n"
        "v_mfma_f32_16x16x32_bf16 v[16:19], v[48:51], v[192:195], v[16:19]\n"
        "ds_read_b64_tr_b16 v[44:45], v232 offset:24576\n"
        "v_mfma_f32_16x16x32_bf16 v[80:83], v[48:51], v[196:199], v[80:83]\n"
        "ds_read_b64_tr_b16 v[46:47], v233 offset:24576\n"
        "v_mfma_f32_16x16x32_bf16 v[112:115], v[48:51], v[200:203], v[112:115]\n"
        "v_mfma_f32_16x16x32_bf16 v[144:147], v[48:51], v[204:207], v[144:147]\n"
        "global_load_lds_dwordx4 v243, s[66:67] offset:1024\n"
        "v_mfma_f32_16x16x32_bf16 v[20:23], v[52:55], v[192:195], v[20:23]\n"
        "ds_read_b64_tr_b16 v[48:49], v226 offset:25600\n"
        "v_mfma_f32_16x16x32_bf16 v[84:87], v[52:55], v[196:199], v[84:87]\n"
        "ds_read_b64_tr_b16 v[50:51], v227 offset:25600\n"
        "v_mfma_f32_16x16x32_bf16 v[116:119], v[52:55], v[200:203], v[116:119]\n"
        "v_mfma_f32_16x16x32_bf16 v[148:151], v[52:55], v[204:207], v[148:151]\n"
        "global_load_lds_dwordx4 v244, s[66:67] offset:2048\n"
        "v_mfma_f32_16x16x32_bf16 v[24:27], v[56:59], v[192:195], v[24:27]\n"
        "ds_read_b64_tr_b16 v[52:53], v228 offset:25600\n"
        "v_accvgpr_read_b32 v176, a128\n"
        "v_mfma_f32_16x16x32_bf16 v[88:91], v[56:59], v[196:199], v[88:91]\n"
        "ds_read_b64_tr_b16 v[54:55], v229 offset:25600\n"
        "v_cvt_pk_bf16_f32 v176, v176, v177\n"
        "v_mfma_f32_16x16x32_bf16 v[120:123], v[56:59], v[200:203], v[120:123]\n"
        "v_cvt_pk_bf16_f32 v177, v178, v179\n"
        "v_cvt_pk_bf16_f32 v178, v180, v181\n"
        "v_mfma_f32_16x16x32_bf16 v[152:155], v[56:59], v[204:207], v[152:155]\n"
        "global_load_lds_dwordx4 v245, s[66:67] offset:3072\n"
        "s_add_u32 m0, s73, 0x1c000\n"
        "v_mfma_f32_16x16x32_bf16 v[28:31], v[60:63], v[192:195], v[28:31]\n"
        "s_add_u32 s66, s66, 0x80\n"
        "s_addc_u32 s67, s67, 0\n"
        "v_cvt_pk_bf16_f32 v179, v182, v183\n"
        "v_mfma_f32_16x16x32_bf16 v[92:95], v[60:63], v[196:199], v[92:95]\n"
        "ds_read_b64_tr_b16 v[56:57], v230 offset:25600\n"
        "v_accvgpr_write_b32 a8, v176\n"
        "v_mfma_f32_16x16x32_bf16 v[124:127], v[60:63], v[200:203], v[124:127]\n"
        "s_waitcnt lgkmcnt(3)\n"
        "v_accvgpr_write_b32 a9, v177\n"
        "v_accvgpr_write_b32 a10, v178\n"
        "v_mfma_f32_16x16x32_bf16 v[156:159], v[60:63], v[204:207], v[156:159]\n"
        "ds_read_b64_tr_b16 v[58:59], v231 offset:25600\n"
        "v_accvgpr_write_b32 a11, v179\n"
        "v_mfma_f32_16x16x32_bf16 v[0:3], v[32:35], v[208:211], v[0:3]\n"
        "ds_read_b64_tr_b16 v[60:61], v232 offset:25600\n"
        "v_accvgpr_read_b32 v177, a217\n"
        "v_mfma_f32_16x16x32_bf16 v[64:67], v[32:35], v[212:215], v[64:67]\n"
        "ds_read_b64_tr_b16 v[62:63], v233 offset:25600\n"
        "v_accvgpr_read_b32 v178, a218\n"
        "v_mfma_f32_16x16x32_bf16 v[96:99], v[32:35], v[216:219], v[96:99]\n"
        "v_accvgpr_read_b32 v179, a219\n"
        "v_accvgpr_read_b32 v180, a248\n"
        "v_mfma_f32_16x16x32_bf16 v[128:131], v[32:35], v[220:223], v[128:131]\n"
        "v_accvgpr_read_b32 v181, a249\n"
        "v_accvgpr_read_b32 v182, a250\n"
        "v_mfma_f32_16x16x32_bf16 v[4:7], v[36:39], v[208:211], v[4:7]\n"
        "v_accvgpr_read_b32 v183, a251\n"
        "v_mfma_f32_16x16x32_bf16 v[68:71], v[36:39], v[212:215], v[68:71]\n"
        "v_mfma_f32_16x16x32_bf16 v[100:103], v[36:39], v[216:219], v[100:103]\n"
        "v_mfma_f32_16x16x32_bf16 v[132:135], v[36:39], v[220:223], v[132:135]\n"
        "v_mfma_f32_16x16x32_bf16 v[8:11], v[40:43], v[208:211], v[8:11]\n"
        "v_mfma_f32_16x16x32_bf16 v[72:75], v[40:43], v[212:215], v[72:75]\n"
        "s_waitcnt vmcnt(13) lgkmcnt(0)\n"
        "s_barrier\n"
        "v_mfma_f32_16x16x32_bf16 v[104:107], v[40:43], v[216:219], v[104:107]\n"
        "global_load_lds_dwordx4 v238, s[68:69] offset:0\n"
        "ds_read_b64_tr_b16 v[32:33], v226 offset:32768\n"
        "v_mfma_f32_16x16x32_bf16 v[136:139], v[40:43], v[220:223], v[136:139]\n"
        "ds_read_b64_tr_b16 v[34:35], v227 offset:32768\n"
        "v_mfma_f32_16x16x32_bf16 v[12:15], v[44:47], v[208:211], v[12:15]\n"
        "ds_read_b64_tr_b16 v[36:37], v228 offset:32768\n"
        "v_mfma_f32_16x16x32_bf16 v[76:79], v[44:47], v[212:215], v[76:79]\n"
        "ds_read_b64_tr_b16 v[38:39], v229 offset:32768\n"
        "v_mfma_f32_16x16x32_bf16 v[108:111], v[44:47], v[216:219], v[108:111]\n"
        "ds_read_b64_tr_b16 v[40:41], v230 offset:32768\n"
        "global_load_lds_dwordx4 v239, s[68:69] offset:1024\n"
        "v_mfma_f32_16x16x32_bf16 v[140:143], v[44:47], v[220:223], v[140:143]\n"
        "ds_read_b64_tr_b16 v[42:43], v231 offset:32768\n"
        "v_accvgpr_read_b32 v184, a136\n"
        "v_mfma_f32_16x16x32_bf16 v[16:19], v[48:51], v[208:211], v[16:19]\n"
        "ds_read_b128 v[192:195], v246 offset:2048\n"
        "v_cvt_pk_bf16_f32 v184, v184, v185\n"
        "v_mfma_f32_16x16x32_bf16 v[80:83], v[48:51], v[212:215], v[80:83]\n"
        "ds_read_b64_tr_b16 v[44:45], v232 offset:32768\n"
        "v_cvt_pk_bf16_f32 v185, v186, v187\n"
        "v_mfma_f32_16x16x32_bf16 v[112:115], v[48:51], v[216:219], v[112:115]\n"
        "ds_read_b64_tr_b16 v[46:47], v233 offset:32768\n"
        "global_load_lds_dwordx4 v240, s[68:69] offset:2048\n"
        "v_mfma_f32_16x16x32_bf16 v[144:147], v[48:51], v[220:223], v[144:147]\n"
        "ds_read_b128 v[196:199], v246 offset:0\n"
        "v_cvt_pk_bf16_f32 v186, v188, v189\n"
        "v_mfma_f32_16x16x32_bf16 v[20:23], v[52:55], v[208:211], v[20:23]\n"
        "ds_read_b64_tr_b16 v[48:49], v226 offset:33792\n"
        "v_cvt_pk_bf16_f32 v187, v190, v191\n"
        "v_mfma_f32_16x16x32_bf16 v[84:87], v[52:55], v[212:215], v[84:87]\n"
        "ds_read_b64_tr_b16 v[50:51], v227 offset:33792\n"
        "v_accvgpr_write_b32 a12, v184\n"
        "v_mfma_f32_16x16x32_bf16 v[116:119], v[52:55], v[216:219], v[116:119]\n"
        "ds_read_b128 v[200:203], v246 offset:6144\n"
        "v_accvgpr_write_b32 a13, v185\n"
        "v_mfma_f32_16x16x32_bf16 v[148:151], v[52:55], v[220:223], v[148:151]\n"
        "global_store_dwordx4 v254, a[160:163], s[70:71] offset:0 sc1\n"
        "ds_read_b64_tr_b16 v[52:53], v228 offset:33792\n"
        "ds_read_b64_tr_b16 v[54:55], v229 offset:33792\n"
        "v_accvgpr_write_b32 a14, v186\n"
        "v_mfma_f32_16x16x32_bf16 v[24:27], v[56:59], v[208:211], v[24:27]\n"
        "ds_read_b128 v[204:207], v246 offset:4096\n"
        "v_accvgpr_write_b32 a15, v187\n"
        "v_mfma_f32_16x16x32_bf16 v[88:91], v[56:59], v[212:215], v[88:91]\n"
        "v_accvgpr_read_b32 v185, a221\n"
        "v_accvgpr_read_b32 v186, a222\n"
        "v_mfma_f32_16x16x32_bf16 v[120:123], v[56:59], v[216:219], v[120:123]\n"
        "global_load_lds_dwordx4 v241, s[68:69] offset:3072\n"
        "s_add_u32 m0, s72, 0x8000\n"
        "v_mfma_f32_16x16x32_bf16 v[152:155], v[56:59], v[220:223], v[152:155]\n"
        "s_add_u32 s68, s68, 0x80000\n"
        "s_addc_u32 s69, s69, 0\n"
        "v_accvgpr_read_b32 v187, a223\n"
        "v_mfma_f32_16x16x32_bf16 v[28:31], v[60:63], v[208:211], v[28:31]\n"
        "ds_read_b64_tr_b16 v[56:57], v230 offset:33792\n"
        "v_accvgpr_read_b32 v188, a252\n"
        "v_mfma_f32_16x16x32_bf16 v[92:95], v[60:63], v[212:215], v[92:95]\n"
        "ds_read_b64_tr_b16 v[58:59], v231 offset:33792\n"
        "v_accvgpr_read_b32 v189, a253\n"
        "v_mfma_f32_16x16x32_bf16 v[124:127], v[60:63], v[216:219], v[124:127]\n"
        "global_load_lds_dwordx4 v242, s[64:65] offset:0\n"
        "v_mfma_f32_16x16x32_bf16 v[156:159], v[60:63], v[220:223], v[156:159]\n"
        "ds_read_b64_tr_b16 v[60:61], v232 offset:33792\n"
        "s_waitcnt lgkmcnt(4)\n"
        "v_mfma_f32_16x16x32_bf16 v[0:3], v[32:35], v[192:195], v[0:3]\n"
        "ds_read_b64_tr_b16 v[62:63], v233 offset:33792\n"
        "v_accvgpr_read_b32 v190, a254\n"
        "v_mfma_f32_16x16x32_bf16 v[64:67], v[32:35], v[196:199], v[64:67]\n"
        "ds_read_b128 v[208:211], v247 offset:2048\n"
        "v_accvgpr_read_b32 v191, a255\n"
        "v_mfma_f32_16x16x32_bf16 v[96:99], v[32:35], v[200:203], v[96:99]\n"
        "s_waitcnt lgkmcnt(5)\n"
        "global_load_lds_dwordx4 v243, s[64:65] offset:1024\n"
        "v_mfma_f32_16x16x32_bf16 v[128:131], v[32:35], v[204:207], v[128:131]\n"
        "ds_read_b64_tr_b16 v[32:33], v226 offset:40960\n"
        "v_mfma_f32_16x16x32_bf16 v[4:7], v[36:39], v[192:195], v[4:7]\n"
        "ds_read_b64_tr_b16 v[34:35], v227 offset:40960\n"
        "v_mfma_f32_16x16x32_bf16 v[68:71], v[36:39], v[196:199], v[68:71]\n"
        "ds_read_b128 v[212:215], v247 offset:0\n"
        "v_mfma_f32_16x16x32_bf16 v[100:103], v[36:39], v[200:203], v[100:103]\n"
        "global_load_lds_dwordx4 v244, s[64:65] offset:2048\n"
        "v_mfma_f32_16x16x32_bf16 v[132:135], v[36:39], v[204:207], v[132:135]\n"
        "ds_read_b64_tr_b16 v[36:37], v228 offset:40960\n"
        "v_accvgpr_read_b32 v160, a208\n"
        "v_mfma_f32_16x16x32_bf16 v[8:11], v[40:43], v[192:195], v[8:11]\n"
        "ds_read_b64_tr_b16 v[38:39], v229 offset:40960\n"
        "v_cvt_pk_bf16_f32 v160, v160, v161\n"
        "v_mfma_f32_16x16x32_bf16 v[72:75], v[40:43], v[196:199], v[72:75]\n"
        "ds_read_b128 v[216:219], v247 offset:6144\n"
        "v_cvt_pk_bf16_f32 v161, v162, v163\n"
        "v_mfma_f32_16x16x32_bf16 v[104:107], v[40:43], v[200:203], v[104:107]\n"
        "global_load_lds_dwordx4 v245, s[64:65] offset:3072\n"
        "s_add_u32 m0, s72, 0x9000\n"
        "v_mfma_f32_16x16x32_bf16 v[136:139], v[40:43], v[204:207], v[136:139]\n"
        "ds_read_b64_tr_b16 v[40:41], v230 offset:40960\n"
        "v_cvt_pk_bf16_f32 v162, v164, v165\n"
        "v_mfma_f32_16x16x32_bf16 v[12:15], v[44:47], v[192:195], v[12:15]\n"
        "ds_read_b64_tr_b16 v[42:43], v231 offset:40960\n"
        "v_cvt_pk_bf16_f32 v163, v166, v167\n"
        "v_mfma_f32_16x16x32_bf16 v[76:79], v[44:47], v[196:199], v[76:79]\n"
        "ds_read_b128 v[220:223], v247 offset:4096\n"
        "v_accvgpr_write_b32 a208, v160\n"
        "v_mfma_f32_16x16x32_bf16 v[108:111], v[44:47], v[200:203], v[108:111]\n"
        "global_load_lds_dwordx4 v242, s[66:67] offset:0\n"
        "v_mfma_f32_16x16x32_bf16 v[140:143], v[44:47], v[204:207], v[140:143]\n"
        "ds_read_b64_tr_b16 v[44:45], v232 offset:40960\n"
        "v_accvgpr_write_b32 a209, v161\n"
        "v_mfma_f32_16x16x32_bf16 v[16:19], v[48:51], v[192:195], v[16:19]\n"
        "ds_read_b64_tr_b16 v[46:47], v233 offset:40960\n"
        "v_accvgpr_write_b32 a210, v162\n"
        "v_mfma_f32_16x16x32_bf16 v[80:83], v[48:51], v[196:199], v[80:83]\n"
        "s_add_u32 s64, s64, 0x80\n"
        "s_addc_u32 s65, s65, 0\n"
        "v_accvgpr_write_b32 a211, v163\n"
        "v_mfma_f32_16x16x32_bf16 v[112:115], v[48:51], v[200:203], v[112:115]\n"
        "global_load_lds_dwordx4 v243, s[66:67] offset:1024\n"
        "v_mfma_f32_16x16x32_bf16 v[144:147], v[48:51], v[204:207], v[144:147]\n"
        "ds_read_b64_tr_b16 v[48:49], v226 offset:41984\n"
        "v_accvgpr_read_b32 v161, a145\n"
        "v_mfma_f32_16x16x32_bf16 v[20:23], v[52:55], v[192:195], v[20:23]\n"
        "ds_read_b64_tr_b16 v[50:51], v227 offset:41984\n"
        "v_accvgpr_read_b32 v162, a146\n"
        "v_mfma_f32_16x16x32_bf16 v[84:87], v[52:55], v[196:199], v[84:87]\n"
        "s_waitcnt lgkmcnt(4)\n"
        "v_accvgpr_read_b32 v163, a147\n"
        "v_accvgpr_read_b32 v164, a176\n"
        "v_mfma_f32_16x16x32_bf16 v[116:119], v[52:55], v[200:203], v[116:119]\n"
        "global_load_lds_dwordx4 v244, s[66:67] offset:2048\n"
        "v_mfma_f32_16x16x32_bf16 v[148:151], v[52:55], v[204:207], v[148:151]\n"
        "ds_read_b64_tr_b16 v[52:53], v228 offset:41984\n"
        "v_accvgpr_read_b32 v165, a177\n"
        "v_mfma_f32_16x16x32_bf16 v[24:27], v[56:59], v[192:195], v[24:27]\n"
        "ds_read_b64_tr_b16 v[54:55], v229 offset:41984\n"
        "v_accvgpr_read_b32 v166, a178\n"
        "v_mfma_f32_16x16x32_bf16 v[88:91], v[56:59], v[196:199], v[88:91]\n"
        "v_accvgpr_read_b32 v167, a179\n"
        "v_mfma_f32_16x16x32_bf16 v[120:123], v[56:59], v[200:203], v[120:123]\n"
        "global_load_lds_dwordx4 v245, s[66:67] offset:3072\n"
        "s_add_u32 m0, s73, 0x20000\n"
        "v_mfma_f32_16x16x32_bf16 v[152:155], v[56:59], v[204:207], v[152:155]\n"
        "s_add_u32 s66, s66, 0x80\n"
        "s_addc_u32 s67, s67, 0\n"
        "v_mfma_f32_16x16x32_bf16 v[28:31], v[60:63], v[192:195], v[28:31]\n"
        "ds_read_b64_tr_b16 v[56:57], v230 offset:41984\n"
        "v_mfma_f32_16x16x32_bf16 v[92:95], v[60:63], v[196:199], v[92:95]\n"
        "ds_read_b64_tr_b16 v[58:59], v231 offset:41984\n"
        "v_mfma_f32_16x16x32_bf16 v[124:127], v[60:63], v[200:203], v[124:127]\n"
        "v_mfma_f32_16x16x32_bf16 v[156:159], v[60:63], v[204:207], v[156:159]\n"
        "global_store_dwordx4 v254, a[164:167], s[70:71] offset:1024 sc1\n"
        "ds_read_b64_tr_b16 v[60:61], v232 offset:41984\n"
        "ds_read_b64_tr_b16 v[62:63], v233 offset:41984\n"
        "v_accvgpr_read_b32 v168, a212\n"
        "v_mfma_f32_16x16x32_bf16 v[0:3], v[32:35], v[208:211], v[0:3]\n"
        "s_add_u32 s70, s38, s97\n"
        "s_addc_u32 s71, s39, 0\n"
        "v_cvt_pk_bf16_f32 v168, v168, v169\n"
        "v_mfma_f32_16x16x32_bf16 v[64:67], v[32:35], v[212:215], v[64:67]\n"
        "s_add_u32 s97, s52, 0x3000\n"
        "v_cvt_pk_bf16_f32 v169, v170, v171\n"
        "v_cvt_pk_bf16_f32 v170, v172, v173\n"
        "v_mfma_f32_16x16x32_bf16 v[96:99], v[32:35], v[216:219], v[96:99]\n"
        "v_cvt_pk_bf16_f32 v171, v174, v175\n"
        "v_accvgpr_write_b32 a240, v168\n"
        "v_mfma_f32_16x16x32_bf16 v[128:131], v[32:35], v[220:223], v[128:131]\n"
        "v_accvgpr_write_b32 a241, v169\n"
        "v_accvgpr_write_b32 a242, v170\n"
        "v_mfma_f32_16x16x32_bf16 v[4:7], v[36:39], v[208:211], v[4:7]\n"
        "v_accvgpr_write_b32 a243, v171\n"
        "v_accvgpr_read_b32 v169, a149\n"
        "v_mfma_f32_16x16x32_bf16 v[68:71], v[36:39], v[212:215], v[68:71]\n"
        "v_accvgpr_read_b32 v170, a150\n"
        "v_accvgpr_read_b32 v171, a151\n"
        "v_mfma_f32_16x16x32_bf16 v[100:103], v[36:39], v[216:219], v[100:103]\n"
        "s_waitcnt lgkmcnt(2)\n"
        "v_accvgpr_read_b32 v172, a180\n"
        "v_accvgpr_read_b32 v173, a181\n"
        "v_mfma_f32_16x16x32_bf16 v[132:135], v[36:39], v[220:223], v[132:135]\n"
        "s_waitcnt vmcnt(14) lgkmcnt(0)\n"
        "s_barrier\n"
        "v_mfma_f32_16x16x32_bf16 v[8:11], v[40:43], v[208:211], v[8:11]\n"
        "global_load_lds_dwordx4 v238, s[68:69] offset:0\n"
        "ds_read_b64_tr_b16 v[32:33], v226 offset:49152\n"
        "v_mfma_f32_16x16x32_bf16 v[72:75], v[40:43], v[212:215], v[72:75]\n"
        "ds_read_b64_tr_b16 v[34:35], v227 offset:49152\n"
        "v_accvgpr_read_b32 v174, a182\n"
        "v_mfma_f32_16x16x32_bf16 v[104:107], v[40:43], v[216:219], v[104:107]\n"
        "ds_read_b64_tr_b16 v[36:37], v228 offset:49152\n"
        "v_accvgpr_read_b32 v175, a183\n"
        "v_mfma_f32_16x16x32_bf16 v[136:139], v[40:43], v[220:223], v[136:139]\n"
        "ds_read_b64_tr_b16 v[38:39], v229 offset:49152\n"
        "v_mfma_f32_16x16x32_bf16 v[12:15], v[44:47], v[208:211], v[12:15]\n"
        "ds_read_b64_tr_b16 v[40:41], v230 offset:49152\n"
        "global_load_lds_dwordx4 v239, s[68:69] offset:1024\n"
        "v_mfma_f32_16x16x32_bf16 v[76:79], v[44:47], v[212:215], v[76:79]\n"
        "ds_read_b64_tr_b16 v[42:43], v231 offset:49152\n"
        "v_mfma_f32_16x16x32_bf16 v[108:111], v[44:47], v[216:219], v[108:111]\n"
        "ds_read_b128 v[192:195], v248 offset:2048\n"
        "v_mfma_f32_16x16x32_bf16 v[140:143], v[44:47], v[220:223], v[140:143]\n"
        "ds_read_b64_tr_b16 v[44:45], v232 offset:49152\n"
        "v_mfma_f32_16x16x32_bf16 v[16:19], v[48:51], v[208:211], v[16:19]\n"
        "ds_read_b64_tr_b16 v[46:47], v233 offset:49152\n"
        "global_load_lds_dwordx4 v240, s[68:69] offset:2048\n"
        "v_mfma_f32_16x16x32_bf16 v[80:83], v[48:51], v[212:215], v[80:83]\n"
        "ds_read_b128 v[196:199], v248 offset:0\n"
        "v_mfma_f32_16x16x32_bf16 v[112:115], v[48:51], v[216:219], v[112:115]\n"
        "ds_read_b128 v[200:203], v248 offset:6144\n"
        "v_mfma_f32_16x16x32_bf16 v[144:147], v[48:51], v[220:223], v[144:147]\n"
        "ds_read_b64_tr_b16 v[48:49], v226 offset:50176\n"
        "v_mfma_f32_16x16x32_bf16 v[20:23], v[52:55], v[208:211], v[20:23]\n"
        "ds_read_b64_tr_b16 v[50:51], v227 offset:50176\n"
        "global_load_lds_dwordx4 v241, s[68:69] offset:3072\n"
        "s_add_u32 m0, s72, 0x0\n"
        "v_mfma_f32_16x16x32_bf16 v[84:87], v[52:55], v[212:215], v[84:87]\n"
        "ds_read_b128 v[204:207], v248 offset:4096\n"
        "s_add_u32 s68, s68, 0x80000\n"
        "s_addc_u32 s69, s69, 0\n"
        "v_mfma_f32_16x16x32_bf16 v[116:119], v[52:55], v[216:219], v[116:119]\n"
        "v_mfma_f32_16x16x32_bf16 v[148:151], v[52:55], v[220:223], v[148:151]\n"
        "ds_read_b64_tr_b16 v[52:53], v228 offset:50176\n"
        "v_accvgpr_read_b32 v176, a216\n"
        "v_mfma_f32_16x16x32_bf16 v[24:27], v[56:59], v[208:211], v[24:27]\n"
        "ds_read_b64_tr_b16 v[54:55], v229 offset:50176\n"
        "global_load_lds_dwordx4 v242, s[64:65] offset:0\n"
        "v_mfma_f32_16x16x32_bf16 v[88:91], v[56:59], v[212:215], v[88:91]\n"
        "v_cvt_pk_bf16_f32 v176, v176, v177\n"
        "v_cvt_pk_bf16_f32 v177, v178, v179\n"
        "v_mfma_f32_16x16x32_bf16 v[120:123], v[56:59], v[216:219], v[120:123]\n"
        "s_waitcnt lgkmcnt(5)\n"
        "v_cvt_pk_bf16_f32 v178, v180, v181\n"
        "v_cvt_pk_bf16_f32 v179, v182, v183\n"
        "v_mfma_f32_16x16x32_bf16 v[152:155], v[56:59], v[220:223], v[152:155]\n"
        "ds_read_b64_tr_b16 v[56:57], v230 offset:50176\n"
        "v_accvgpr_write_b32 a212, v176\n"
        "v_mfma_f32_16x16x32_bf16 v[28:31], v[60:63], v[208:211], v[28:31]\n"
        "ds_read_b64_tr_b16 v[58:59], v231 offset:50176\n"
        "v_accvgpr_write_b32 a213, v177\n"
        "v_mfma_f32_16x16x32_bf16 v[92:95], v[60:63], v[212:215], v[92:95]\n"
        "s_waitcnt lgkmcnt(4)\n"
        "v_accvgpr_write_b32 a214, v178\n"
        "v_accvgpr_write_b32 a215, v179\n"
        "v_mfma_f32_16x16x32_bf16 v[124:127], v[60:63], v[216:219], v[124:127]\n"
        "global_load_lds_dwordx4 v243, s[64:65] offset:1024\n"
        "ds_read_b128 v[208:211], v249 offset:2048\n"
        "v_mfma_f32_16x16x32_bf16 v[156:159], v[60:63], v[220:223], v[156:159]\n"
        "ds_read_b64_tr_b16 v[60:61], v232 offset:50176\n"
        "v_accvgpr_read_b32 v177, a153\n"
        "v_mfma_f32_16x16x32_bf16 v[0:3], v[32:35], v[192:195], v[0:3]\n"
        "ds_read_b64_tr_b16 v[62:63], v233 offset:50176\n"
        "v_accvgpr_read_b32 v178, a154\n"
        "v_mfma_f32_16x16x32_bf16 v[64:67], v[32:35], v[196:199], v[64:67]\n"
        "ds_read_b128 v[212:215], v249 offset:0\n"
        "v_accvgpr_read_b32 v179, a155\n"
        "v_mfma_f32_16x16x32_bf16 v[96:99], v[32:35], v[200:203], v[96:99]\n"
        "global_load_lds_dwordx4 v244, s[64:65] offset:2048\n"
        "ds_read_b128 v[216:219], v249 offset:6144\n"
        "v_mfma_f32_16x16x32_bf16 v[128:131], v[32:35], v[204:207], v[128:131]\n"
        "ds_read_b64_tr_b16 v[32:33], v226 offset:57344\n"
        "v_accvgpr_read_b32 v180, a184\n"
        "v_mfma_f32_16x16x32_bf16 v[4:7], v[36:39], v[192:195], v[4:7]\n"
        "ds_read_b64_tr_b16 v[34:35], v227 offset:57344\n"
        "v_accvgpr_read_b32 v181, a185\n"
        "v_mfma_f32_16x16x32_bf16 v[68:71], v[36:39], v[196:199], v[68:71]\n"
        "ds_read_b128 v[220:223], v249 offset:4096\n"
        "v_accvgpr_read_b32 v182, a186\n"
        "v_mfma_f32_16x16x32_bf16 v[100:103], v[36:39], v[200:203], v[100:103]\n"
        "global_store_dwordx4 v254, a[168:171], s[70:71] offset:0 sc1\n"
        "v_accvgpr_read_b32 v183, a187\n"
        "v_mfma_f32_16x16x32_bf16 v[132:135], v[36:39], v[204:207], v[132:135]\n"
        "ds_read_b64_tr_b16 v[36:37], v228 offset:57344\n"
        "v_mfma_f32_16x16x32_bf16 v[8:11], v[40:43], v[192:195], v[8:11]\n"
        "ds_read_b64_tr_b16 v[38:39], v229 offset:57344\n"
        "v_mfma_f32_16x16x32_bf16 v[72:75], v[40:43], v[196:199], v[72:75]\n"
        "global_load_lds_dwordx4 v245, s[64:65] offset:3072\n"
        "s_add_u32 m0, s72, 0x1000\n"
        "v_mfma_f32_16x16x32_bf16 v[104:107], v[40:43], v[200:203], v[104:107]\n"
        "s_add_u32 s64, s64, 0x80\n"
        "s_addc_u32 s65, s65, 0\n"
        "v_mfma_f32_16x16x32_bf16 v[136:139], v[40:43], v[204:207], v[136:139]\n"
        "ds_read_b64_tr_b16 v[40:41], v230 offset:57344\n"
        "v_mfma_f32_16x16x32_bf16 v[12:15], v[44:47], v[192:195], v[12:15]\n"
        "ds_read_b64_tr_b16 v[42:43], v231 offset:57344\n"
        "v_mfma_f32_16x16x32_bf16 v[76:79], v[44:47], v[196:199], v[76:79]\n"
        "global_load_lds_dwordx4 v242, s[66:67] offset:0\n"
        "v_mfma_f32_16x16x32_bf16 v[108:111], v[44:47], v[200:203], v[108:111]\n"
        "s_waitcnt lgkmcnt(4)\n"
        "v_mfma_f32_16x16x32_bf16 v[140:143], v[44:47], v[204:207], v[140:143]\n"
        "ds_read_b64_tr_b16 v[44:45], v232 offset:57344\n"
        "v_accvgpr_read_b32 v184, a220\n"
        "v_mfma_f32_16x16x32_bf16 v[16:19], v[48:51], v[192:195], v[16:19]\n"
        "ds_read_b64_tr_b16 v[46:47], v233 offset:57344\n"
        "v_cvt_pk_bf16_f32 v184, v184, v185\n"
        "v_mfma_f32_16x16x32_bf16 v[80:83], v[48:51], v[196:199], v[80:83]\n"
        "global_load_lds_dwordx4 v243, s[66:67] offset:1024\n"
        "v_mfma_f32_16x16x32_bf16 v[112:115], v[48:51], v[200:203], v[112:115]\n"
        "v_cvt_pk_bf16_f32 v185, v186, v187\n"
        "v_cvt_pk_bf16_f32 v186, v188, v189\n"
        "v_mfma_f32_16x16x32_bf16 v[144:147], v[48:51], v[204:207], v[144:147]\n"
        "ds_read_b64_tr_b16 v[48:49], v226 offset:58368\n"
        "v_cvt_pk_bf16_f32 v187, v190, v191\n"
        "v_mfma_f32_16x16x32_bf16 v[20:23], v[52:55], v[192:195], v[20:23]\n"
        "ds_read_b64_tr_b16 v[50:51], v227 offset:58368\n"
        "v_accvgpr_write_b32 a244, v184\n"
        "v_mfma_f32_16x16x32_bf16 v[84:87], v[52:55], v[196:199], v[84:87]\n"
        "global_load_lds_dwordx4 v244, s[66:67] offset:2048\n"
        "v_mfma_f32_16x16x32_bf16 v[116:119], v[52:55], v[200:203], v[116:119]\n"
        "v_accvgpr_write_b32 a245, v185\n"
        "v_accvgpr_write_b32 a246, v186\n"
        "v_mfma_f32_16x16x32_bf16 v[148:151], v[52:55], v[204:207], v[148:151]\n"
        "ds_read_b64_tr_b16 v[52:53], v228 offset:58368\n"
        "v_accvgpr_write_b32 a247, v187\n"
        "v_mfma_f32_16x16x32_bf16 v[24:27], v[56:59], v[192:195], v[24:27]\n"
        "ds_read_b64_tr_b16 v[54:55], v229 offset:58368\n"
        "v_accvgpr_read_b32 v185, a157\n"
        "v_mfma_f32_16x16x32_bf16 v[88:91], v[56:59], v[196:199], v[88:91]\n"
        "global_load_lds_dwordx4 v245, s[66:67] offset:3072\n"
        "s_add_u32 m0, s73, 0x24000\n"
        "v_mfma_f32_16x16x32_bf16 v[120:123], v[56:59], v[200:203], v[120:123]\n"
        "s_add_u32 s66, s66, 0x80\n"
        "s_addc_u32 s67, s67, 0\n"
        "v_accvgpr_read_b32 v186, a158\n"
        "v_mfma_f32_16x16x32_bf16 v[152:155], v[56:59], v[204:207], v[152:155]\n"
        "ds_read_b64_tr_b16 v[56:57], v230 offset:58368\n"
        "v_accvgpr_read_b32 v187, a159\n"
        "v_mfma_f32_16x16x32_bf16 v[28:31], v[60:63], v[192:195], v[28:31]\n"
        "ds_read_b64_tr_b16 v[58:59], v231 offset:58368\n"
        "v_accvgpr_read_b32 v188, a188\n"
        "v_mfma_f32_16x16x32_bf16 v[92:95], v[60:63], v[196:199], v[92:95]\n"
        "v_accvgpr_read_b32 v189, a189\n"
        "v_accvgpr_read_b32 v190, a190\n"
        "v_mfma_f32_16x16x32_bf16 v[124:127], v[60:63], v[200:203], v[124:127]\n"
        "s_waitcnt lgkmcnt(4)\n"
        "v_accvgpr_read_b32 v191, a191\n"
        "v_mfma_f32_16x16x32_bf16 v[156:159], v[60:63], v[204:207], v[156:159]\n"
        "ds_read_b64_tr_b16 v[60:61], v232 offset:58368\n"
        "v_mfma_f32_16x16x32_bf16 v[0:3], v[32:35], v[208:211], v[0:3]\n"
        "ds_read_b64_tr_b16 v[62:63], v233 offset:58368\n"
        "v_mfma_f32_16x16x32_bf16 v[64:67], v[32:35], v[212:215], v[64:67]\n"
        "v_mfma_f32_16x16x32_bf16 v[96:99], v[32:35], v[216:219], v[96:99]\n"
        "v_mfma_f32_16x16x32_bf16 v[128:131], v[32:35], v[220:223], v[128:131]\n"
        "v_mfma_f32_16x16x32_bf16 v[4:7], v[36:39], v[208:211], v[4:7]\n"
        "v_mfma_f32_16x16x32_bf16 v[68:71], v[36:39], v[212:215], v[68:71]\n"
        "v_mfma_f32_16x16x32_bf16 v[100:103], v[36:39], v[216:219], v[100:103]\n"
        "v_mfma_f32_16x16x32_bf16 v[132:135], v[36:39], v[220:223], v[132:135]\n"
        "v_accvgpr_read_b32 v160, a144\n"
        "v_cvt_pk_bf16_f32 v160, v160, v161\n"
        "v_mfma_f32_16x16x32_bf16 v[8:11], v[40:43], v[208:211], v[8:11]\n"
        "s_waitcnt vmcnt(14) lgkmcnt(0)\n"
        "s_barrier\n"
        "v_cvt_pk_bf16_f32 v161, v162, v163\n"
        "v_mfma_f32_16x16x32_bf16 v[72:75], v[40:43], v[212:215], v[72:75]\n"
        "global_load_lds_dwordx4 v238, s[68:69] offset:0\n"
        "ds_read_b64_tr_b16 v[32:33], v226 offset:16384\n"
        "v_mfma_f32_16x16x32_bf16 v[104:107], v[40:43], v[216:219], v[104:107]\n"
        "ds_read_b64_tr_b16 v[34:35], v227 offset:16384\n"
        "v_cvt_pk_bf16_f32 v162, v164, v165\n"
        "v_mfma_f32_16x16x32_bf16 v[136:139], v[40:43], v[220:223], v[136:139]\n"
        "ds_read_b64_tr_b16 v[36:37], v228 offset:16384\n"
        "v_cvt_pk_bf16_f32 v163, v166, v167\n"
        "v_mfma_f32_16x16x32_bf16 v[12:15], v[44:47], v[208:211], v[12:15]\n"
        "ds_read_b64_tr_b16 v[38:39], v229 offset:16384\n"
        "v_accvgpr_write_b32 a216, v160\n"
        "v_mfma_f32_16x16x32_bf16 v[76:79], v[44:47], v[212:215], v[76:79]\n"
        "ds_read_b64_tr_b16 v[40:41], v230 offset:16384\n"
        "v_accvgpr_write_b32 a217, v161\n"
        "v_mfma_f32_16x16x32_bf16 v[108:111], v[44:47], v[216:219], v[108:111]\n"
        "ds_read_b64_tr_b16 v[42:43], v231 offset:16384\n"
        "v_accvgpr_write_b32 a218, v162\n"
        "v_mfma_f32_16x16x32_bf16 v[140:143], v[44:47], v[220:223], v[140:143]\n"
        "ds_read_b128 v[192:195], v246 offset:34816\n"
        "v_accvgpr_write_b32 a219, v163\n"
        "v_mfma_f32_16x16x32_bf16 v[16:19], v[48:51], v[208:211], v[16:19]\n"
        "global_load_lds_dwordx4 v239, s[68:69] offset:1024\n"
        "ds_read_b64_tr_b16 v[44:45], v232 offset:16384\n"
        "v_mfma_f32_16x16x32_bf16 v[80:83], v[48:51], v[212:215], v[80:83]\n"
        "ds_read_b64_tr_b16 v[46:47], v233 offset:16384\n"
        "v_accvgpr_read_b32 v161, a17\n"
        "v_mfma_f32_16x16x32_bf16 v[112:115], v[48:51], v[216:219], v[112:115]\n"
        "ds_read_b128 v[196:199], v246 offset:32768\n"
        "v_accvgpr_read_b32 v162, a18\n"
        "v_mfma_f32_16x16x32_bf16 v[144:147], v[48:51], v[220:223], v[144:147]\n"
        "ds_read_b64_tr_b16 v[48:49], v226 offset:17408\n"
        "v_accvgpr_read_b32 v163, a19\n"
        "v_mfma_f32_16x16x32_bf16 v[20:23], v[52:55], v[208:211], v[20:23]\n"
        "global_store_dwordx4 v254, a[172:175], s[70:71] offset:1024 sc1\n"
        "ds_read_b64_tr_b16 v[50:51], v227 offset:17408\n"
        "ds_read_b128 v[200:203], v246 offset:38912\n"
        "s_add_u32 s70, s38, s97\n"
        "s_addc_u32 s71, s39, 0\n"
        "v_mfma_f32_16x16x32_bf16 v[84:87], v[52:55], v[212:215], v[84:87]\n"
        "ds_read_b128 v[204:207], v246 offset:36864\n"
        "s_add_u32 s97, s52, 0x4000\n"
        "v_accvgpr_read_b32 v164, a48\n"
        "v_mfma_f32_16x16x32_bf16 v[116:119], v[52:55], v[216:219], v[116:119]\n"
        "s_mul_i32 s52, s40, 4224\n"
        "v_accvgpr_read_b32 v165, a49\n"
        "v_accvgpr_read_b32 v166, a50\n"
        "v_mfma_f32_16x16x32_bf16 v[148:151], v[52:55], v[220:223], v[148:151]\n"
        "global_load_lds_dwordx4 v240, s[68:69] offset:2048\n"
        "s_add_u32 s52, s52, 0x20240\n"
        "v_mfma_f32_16x16x32_bf16 v[24:27], v[56:59], v[208:211], v[24:27]\n"
        "ds_read_b64_tr_b16 v[52:53], v228 offset:17408\n"
        "v_accvgpr_read_b32 v167, a51\n"
        "v_mfma_f32_16x16x32_bf16 v[88:91], v[56:59], v[212:215], v[88:91]\n"
        "ds_read_b64_tr_b16 v[54:55], v229 offset:17408\n"
        "v_mfma_f32_16x16x32_bf16 v[120:123], v[56:59], v[216:219], v[120:123]\n"
        "s_waitcnt lgkmcnt(6)\n"
        "v_mfma_f32_16x16x32_bf16 v[152:155], v[56:59], v[220:223], v[152:155]\n"
        "global_load_lds_dwordx4 v241, s[68:69] offset:3072\n"
        "s_add_u32 m0, s72, 0x10000\n"
        "v_mfma_f32_16x16x32_bf16 v[28:31], v[60:63], v[208:211], v[28:31]\n"
        "s_add_u32 s68, s68, 0x80000\n"
        "s_addc_u32 s69, s69, 0\n"
        "s_waitcnt lgkmcnt(3)\n"
        "v_mfma_f32_16x16x32_bf16 v[92:95], v[60:63], v[212:215], v[92:95]\n"
        "ds_read_b64_tr_b16 v[56:57], v230 offset:17408\n"
        "s_waitcnt lgkmcnt(3)\n"
        "v_mfma_f32_16x16x32_bf16 v[124:127], v[60:63], v[216:219], v[124:127]\n"
        "ds_read_b64_tr_b16 v[58:59], v231 offset:17408\n"
        "v_mfma_f32_16x16x32_bf16 v[156:159], v[60:63], v[220:223], v[156:159]\n"
        "global_load_lds_dwordx4 v242, s[64:65] offset:0\n"
        "v_mfma_f32_16x16x32_bf16 v[0:3], v[32:35], v[192:195], v[0:3]\n"
        "ds_read_b64_tr_b16 v[60:61], v232 offset:17408\n"
        "v_accvgpr_read_b32 v168, a148\n"
        "v_mfma_f32_16x16x32_bf16 v[64:67], v[32:35], v[196:199], v[64:67]\n"
        "ds_read_b64_tr_b16 v[62:63], v233 offset:17408\n"
        "v_cvt_pk_bf16_f32 v168, v168, v169\n"
        "v_mfma_f32_16x16x32_bf16 v[96:99], v[32:35], v[200:203], v[96:99]\n"
        "ds_read_b128 v[208:211], v247 offset:34816\n"
        "v_cvt_pk_bf16_f32 v169, v170, v171\n"
        "v_mfma_f32_16x16x32_bf16 v[128:131], v[32:35], v[204:207], v[128:131]\n"
        "global_load_lds_dwordx4 v243, s[64:65] offset:1024\n"
        "v_mfma_f32_16x16x32_bf16 v[4:7], v[36:39], v[192:195], v[4:7]\n"
        "ds_read_b64_tr_b16 v[32:33], v226 offset:24576\n"
        "v_cvt_pk_bf16_f32 v170, v172, v173\n"
        "v_mfma_f32_16x16x32_bf16 v[68:71], v[36:39], v[196:199], v[68:71]\n"
        "ds_read_b64_tr_b16 v[34:35], v227 offset:24576\n"
        "v_cvt_pk_bf16_f32 v171, v174, v175\n"
        "v_mfma_f32_16x16x32_bf16 v[100:103], v[36:39], v[200:203], v[100:103]\n"
        "ds_read_b128 v[212:215], v247 offset:32768\n"
        "v_accvgpr_write_b32 a248, v168\n"
        "v_mfma_f32_16x16x32_bf16 v[132:135], v[36:39], v[204:207], v[132:135]\n"
        "global_load_lds_dwordx4 v244, s[64:65] offset:2048\n"
        "v_mfma_f32_16x16x32_bf16 v[8:11], v[40:43], v[192:195], v[8:11]\n"
        "ds_read_b64_tr_b16 v[36:37], v228 offset:24576\n"
        "v_accvgpr_write_b32 a249, v169\n"
        "v_mfma_f32_16x16x32_bf16 v[72:75], v[40:43], v[196:199], v[72:75]\n"
        "ds_read_b64_tr_b16 v[38:39], v229 offset:24576\n"
        "v_accvgpr_write_b32 a250, v170\n"
        "v_mfma_f32_16x16x32_bf16 v[104:107], v[40:43], v[200:203], v[104:107]\n"
        "ds_read_b128 v[216:219], v247 offset:38912\n"
        "v_accvgpr_write_b32 a251, v171\n"
        "v_mfma_f32_16x16x32_bf16 v[136:139], v[40:43], v[204:207], v[136:139]\n"
        "global_load_lds_dwordx4 v245, s[64:65] offset:3072\n"
        "s_add_u32 m0, s72, 0x11000\n"
        "v_mfma_f32_16x16x32_bf16 v[12:15], v[44:47], v[192:195], v[12:15]\n"
        "ds_read_b64_tr_b16 v[40:41], v230 offset:24576\n"
        "s_add_u32 s64, s64, 0x80\n"
        "s_addc_u32 s65, s65, 0\n"
        "v_mfma_f32_16x16x32_bf16 v[76:79], v[44:47], v[196:199], v[76:79]\n"
        "ds_read_b64_tr_b16 v[42:43], v231 offset:24576\n"
        "v_accvgpr_read_b32 v169, a21\n"
        "v_mfma_f32_16x16x32_bf16 v[108:111], v[44:47], v[200:203], v[108:111]\n"
        "ds_read_b128 v[220:223], v247 offset:36864\n"
        "v_accvgpr_read_b32 v170, a22\n"
        "v_mfma_f32_16x16x32_bf16 v[140:143], v[44:47], v[204:207], v[140:143]\n"
        "global_load_lds_dwordx4 v242, s[66:67] offset:0\n"
        "v_mfma_f32_16x16x32_bf16 v[16:19], v[48:51], v[192:195], v[16:19]\n"
        "ds_read_b64_tr_b16 v[44:45], v232 offset:24576\n"
        "v_accvgpr_read_b32 v171, a23\n"
        "v_mfma_f32_16x16x32_bf16 v[80:83], v[48:51], v[196:199], v[80:83]\n"
        "ds_read_b64_tr_b16 v[46:47], v233 offset:24576\n"
        "v_accvgpr_read_b32 v172, a52\n"
        "v_mfma_f32_16x16x32_bf16 v[112:115], v[48:51], v[200:203], v[112:115]\n"
        "s_waitcnt lgkmcnt(5)\n"
        "v_accvgpr_read_b32 v173, a53\n"
        "v_accvgpr_read_b32 v174, a54\n"
        "v_mfma_f32_16x16x32_bf16 v[144:147], v[48:51], v[204:207], v[144:147]\n"
        "global_load_lds_dwordx4 v243, s[66:67] offset:1024\n"
        "v_mfma_f32_16x16x32_bf16 v[20:23], v[52:55], v[192:195], v[20:23]\n"
        "ds_read_b64_tr_b16 v[48:49], v226 offset:25600\n"
        "v_accvgpr_read_b32 v175, a55\n"
        "v_mfma_f32_16x16x32_bf16 v[84:87], v[52:55], v[196:199], v[84:87]\n"
        "ds_read_b64_tr_b16 v[50:51], v227 offset:25600\n"
        "v_mfma_f32_16x16x32_bf16 v[116:119], v[52:55], v[200:203], v[116:119]\n"
        "v_mfma_f32_16x16x32_bf16 v[148:151], v[52:55], v[204:207], v[148:151]\n"
        "global_load_lds_dwordx4 v244, s[66:67] offset:2048\n"
        "v_mfma_f32_16x16x32_bf16 v[24:27], v[56:59], v[192:195], v[24:27]\n"
        "ds_read_b64_tr_b16 v[52:53], v228 offset:25600\n"
        "v_accvgpr_read_b32 v176, a152\n"
        "v_mfma_f32_16x16x32_bf16 v[88:91], v[56:59], v[196:199], v[88:91]\n"
        "ds_read_b64_tr_b16 v[54:55], v229 offset:25600\n"
        "v_cvt_pk_bf16_f32 v176, v176, v177\n"
        "v_mfma_f32_16x16x32_bf16 v[120:123], v[56:59], v[200:203], v[120:123]\n"
        "v_cvt_pk_bf16_f32 v177, v178, v179\n"
        "v_cvt_pk_bf16_f32 v178, v180, v181\n"
        "v_mfma_f32_16x16x32_bf16 v[152:155], v[56:59], v[204:207], v[152:155]\n"
        "global_load_lds_dwordx4 v245, s[66:67] offset:3072\n"
        "s_add_u32 m0, s73, 0x1c000\n"
        "v_mfma_f32_16x16x32_bf16 v[28:31], v[60:63], v[192:195], v[28:31]\n"
        "s_add_u32 s66, s66, 0x80\n"
        "s_addc_u32 s67, s67, 0\n"
        "v_cvt_pk_bf16_f32 v179, v182, v183\n"
        "v_mfma_f32_16x16x32_bf16 v[92:95], v[60:63], v[196:199], v[92:95]\n"
        "s_waitcnt lgkmcnt(2)\n"
        "v_accvgpr_write_b32 a220, v176\n"
        "v_accvgpr_write_b32 a221, v177\n"
        "v_mfma_f32_16x16x32_bf16 v[124:127], v[60:63], v[200:203], v[124:127]\n"
        "ds_read_b64_tr_b16 v[56:57], v230 offset:25600\n"
        "v_accvgpr_write_b32 a222, v178\n"
        "v_mfma_f32_16x16x32_bf16 v[156:159], v[60:63], v[204:207], v[156:159]\n"
        "ds_read_b64_tr_b16 v[58:59], v231 offset:25600\n"
        "v_accvgpr_write_b32 a223, v179\n"
        "v_mfma_f32_16x16x32_bf16 v[0:3], v[32:35], v[208:211], v[0:3]\n"
        "global_store_dwordx4 v254, a[0:3], s[70:71] offset:0 sc1\n"
        "ds_read_b64_tr_b16 v[60:61], v232 offset:25600\n"
        "ds_read_b64_tr_b16 v[62:63], v233 offset:25600\n"
        "v_accvgpr_read_b32 v177, a25\n"
        "v_mfma_f32_16x16x32_bf16 v[64:67], v[32:35], v[212:215], v[64:67]\n"
        "v_accvgpr_read_b32 v178, a26\n"
        "v_accvgpr_read_b32 v179, a27\n"
        "v_mfma_f32_16x16x32_bf16 v[96:99], v[32:35], v[216:219], v[96:99]\n"
        "v_accvgpr_read_b32 v180, a56\n"
        "v_accvgpr_read_b32 v181, a57\n"
        "v_mfma_f32_16x16x32_bf16 v[128:131], v[32:35], v[220:223], v[128:131]\n"
        "v_accvgpr_read_b32 v182, a58\n"
        "v_accvgpr_read_b32 v183, a59\n"
        "v_mfma_f32_16x16x32_bf16 v[4:7], v[36:39], v[208:211], v[4:7]\n"
        "v_mfma_f32_16x16x32_bf16 v[68:71], v[36:39], v[212:215], v[68:71]\n"
        "v_mfma_f32_16x16x32_bf16 v[100:103], v[36:39], v[216:219], v[100:103]\n"
        "v_mfma_f32_16x16x32_bf16 v[132:135], v[36:39], v[220:223], v[132:135]\n"
        "v_mfma_f32_16x16x32_bf16 v[8:11], v[40:43], v[208:211], v[8:11]\n"
        "s_waitcnt vmcnt(14) lgkmcnt(0)\n"
        "s_barrier\n"
        "v_mfma_f32_16x16x32_bf16 v[72:75], v[40:43], v[212:215], v[72:75]\n"
        "global_load_lds_dwordx4 v238, s[68:69] offset:0\n"
        "ds_read_b64_tr_b16 v[32:33], v226 offset:32768\n"
        "v_mfma_f32_16x16x32_bf16 v[104:107], v[40:43], v[216:219], v[104:107]\n"
        "ds_read_b64_tr_b16 v[34:35], v227 offset:32768\n"
        "v_mfma_f32_16x16x32_bf16 v[136:139], v[40:43], v[220:223], v[136:139]\n"
        "ds_read_b64_tr_b16 v[36:37], v228 offset:32768\n"
        "v_mfma_f32_16x16x32_bf16 v[12:15], v[44:47], v[208:211], v[12:15]\n"
        "ds_read_b64_tr_b16 v[38:39], v229 offset:32768\n"
        "v_mfma_f32_16x16x32_bf16 v[76:79], v[44:47], v[212:215], v[76:79]\n"
        "ds_read_b64_tr_b16 v[40:41], v230 offset:32768\n"
        "global_load_lds_dwordx4 v239, s[68:69] offset:1024\n"
        "v_mfma_f32_16x16x32_bf16 v[108:111], v[44:47], v[216:219], v[108:111]\n"
        "ds_read_b64_tr_b16 v[42:43], v231 offset:32768\n"
        "v_mfma_f32_16x16x32_bf16 v[140:143], v[44:47], v[220:223], v[140:143]\n"
        "ds_read_b128 v[192:195], v246 offset:2048\n"
        "v_accvgpr_read_b32 v184, a156\n"
        "v_mfma_f32_16x16x32_bf16 v[16:19], v[48:51], v[208:211], v[16:19]\n"
        "ds_read_b64_tr_b16 v[44:45], v232 offset:32768\n"
        "v_cvt_pk_bf16_f32 v184, v184, v185\n"
        "v_mfma_f32_16x16x32_bf16 v[80:83], v[48:51], v[212:215], v[80:83]\n"
        "ds_read_b64_tr_b16 v[46:47], v233 offset:32768\n"
        "global_load_lds_dwordx4 v240, s[68:69] offset:2048\n"
        "v_mfma_f32_16x16x32_bf16 v[112:115], v[48:51], v[216:219], v[112:115]\n"
        "ds_read_b128 v[196:199], v246 offset:0\n"
        "v_cvt_pk_bf16_f32 v185, v186, v187\n"
        "v_mfma_f32_16x16x32_bf16 v[144:147], v[48:51], v[220:223], v[144:147]\n"
        "ds_read_b64_tr_b16 v[48:49], v226 offset:33792\n"
        "v_cvt_pk_bf16_f32 v186, v188, v189\n"
        "v_mfma_f32_16x16x32_bf16 v[20:23], v[52:55], v[208:211], v[20:23]\n"
        "ds_read_b64_tr_b16 v[50:51], v227 offset:33792\n"
        "v_cvt_pk_bf16_f32 v187, v190, v191\n"
        "v_mfma_f32_16x16x32_bf16 v[84:87], v[52:55], v[212:215], v[84:87]\n"
        "ds_read_b128 v[200:203], v246 offset:6144\n"
        "v_accvgpr_write_b32 a252, v184\n"
        "v_mfma_f32_16x16x32_bf16 v[116:119], v[52:55], v[216:219], v[116:119]\n"
        "global_load_lds_dwordx4 v241, s[68:69] offset:3072\n"
        "s_add_u32 m0, s72, 0x8000\n"
        "v_accvgpr_write_b32 a253, v185\n"
        "v_mfma_f32_16x16x32_bf16 v[148:151], v[52:55], v[220:223], v[148:151]\n"
        "ds_read_b64_tr_b16 v[52:53], v228 offset:33792\n"
        "v_accvgpr_write_b32 a254, v186\n"
        "v_mfma_f32_16x16x32_bf16 v[24:27], v[56:59], v[208:211], v[24:27]\n"
        "ds_read_b64_tr_b16 v[54:55], v229 offset:33792\n"
        "v_accvgpr_write_b32 a255, v187\n"
        "v_mfma_f32_16x16x32_bf16 v[88:91], v[56:59], v[212:215], v[88:91]\n"
        "ds_read_b128 v[204:207], v246 offset:4096\n"
        "s_add_u32 s68, s68, 0x80000\n"
        "s_addc_u32 s69, s69, 0\n"
        "v_mfma_f32_16x16x32_bf16 v[120:123], v[56:59], v[216:219], v[120:123]\n"
        "global_load_lds_dwordx4 v242, s[64:65] offset:0\n"
        "v_accvgpr_read_b32 v185, a29\n"
        "v_mfma_f32_16x16x32_bf16 v[152:155], v[56:59], v[220:223], v[152:155]\n"
        "ds_read_b64_tr_b16 v[56:57], v230 offset:33792\n"
        "v_accvgpr_read_b32 v186, a30\n"
        "v_mfma_f32_16x16x32_bf16 v[28:31], v[60:63], v[208:211], v[28:31]\n"
        "ds_read_b64_tr_b16 v[58:59], v231 offset:33792\n"
        "v_accvgpr_read_b32 v187, a31\n"
        "v_mfma_f32_16x16x32_bf16 v[92:95], v[60:63], v[212:215], v[92:95]\n"
        "ds_read_b128 v[208:211], v247 offset:2048\n"
        "v_accvgpr_read_b32 v188, a60\n"
        "v_mfma_f32_16x16x32_bf16 v[124:127], v[60:63], v[216:219], v[124:127]\n"
        "global_load_lds_dwordx4 v243, s[64:65] offset:1024\n"
        "ds_read_b128 v[212:215], v247 offset:0\n"
        "v_mfma_f32_16x16x32_bf16 v[156:159], v[60:63], v[220:223], v[156:159]\n"
        "ds_read_b64_tr_b16 v[60:61], v232 offset:33792\n"
        "s_waitcnt lgkmcnt(7)\n"
        "v_mfma_f32_16x16x32_bf16 v[0:3], v[32:35], v[192:195], v[0:3]\n"
        "ds_read_b64_tr_b16 v[62:63], v233 offset:33792\n"
        "v_accvgpr_read_b32 v189, a61\n"
        "v_mfma_f32_16x16x32_bf16 v[64:67], v[32:35], v[196:199], v[64:67]\n"
        "ds_read_b128 v[216:219], v247 offset:6144\n"
        "v_accvgpr_read_b32 v190, a62\n"
        "v_mfma_f32_16x16x32_bf16 v[96:99], v[32:35], v[200:203], v[96:99]\n"
        "global_load_lds_dwordx4 v244, s[64:65] offset:2048\n"
        "s_waitcnt lgkmcnt(7)\n"
        "v_mfma_f32_16x16x32_bf16 v[128:131], v[32:35], v[204:207], v[128:131]\n"
        "ds_read_b64_tr_b16 v[32:33], v226 offset:40960\n"
        "v_accvgpr_read_b32 v191, a63\n"
        "v_mfma_f32_16x16x32_bf16 v[4:7], v[36:39], v[192:195], v[4:7]\n"
        "ds_read_b64_tr_b16 v[34:35], v227 offset:40960\n"
        "v_mfma_f32_16x16x32_bf16 v[68:71], v[36:39], v[196:199], v[68:71]\n"
        "ds_read_b128 v[220:223], v247 offset:4096\n"
        "v_mfma_f32_16x16x32_bf16 v[100:103], v[36:39], v[200:203], v[100:103]\n"
        "global_load_lds_dwordx4 v245, s[64:65] offset:3072\n"
        "s_add_u32 m0, s72, 0x9000\n"
        "v_mfma_f32_16x16x32_bf16 v[132:135], v[36:39], v[204:207], v[132:135]\n"
        "ds_read_b64_tr_b16 v[36:37], v228 offset:40960\n"
        "v_accvgpr_read_b32 v160, a16\n"
        "v_mfma_f32_16x16x32_bf16 v[8:11], v[40:43], v[192:195], v[8:11]\n"
        "ds_read_b64_tr_b16 v[38:39], v229 offset:40960\n"
        "v_cvt_pk_bf16_f32 v160, v160, v161\n"
        "v_mfma_f32_16x16x32_bf16 v[72:75], v[40:43], v[196:199], v[72:75]\n"
        "s_add_u32 s64, s64, 0x80\n"
        "s_addc_u32 s65, s65, 0\n"
        "v_cvt_pk_bf16_f32 v161, v162, v163\n"
        "v_mfma_f32_16x16x32_bf16 v[104:107], v[40:43], v[200:203], v[104:107]\n"
        "global_load_lds_dwordx4 v242, s[66:67] offset:0\n"
        "v_cvt_pk_bf16_f32 v162, v164, v165\n"
        "v_mfma_f32_16x16x32_bf16 v[136:139], v[40:43], v[204:207], v[136:139]\n"
        "ds_read_b64_tr_b16 v[40:41], v230 offset:40960\n"
        "v_cvt_pk_bf16_f32 v163, v166, v167\n"
        "v_mfma_f32_16x16x32_bf16 v[12:15], v[44:47], v[192:195], v[12:15]\n"
        "ds_read_b64_tr_b16 v[42:43], v231 offset:40960\n"
        "v_accvgpr_write_b32 a176, v160\n"
        "v_mfma_f32_16x16x32_bf16 v[76:79], v[44:47], v[196:199], v[76:79]\n"
        "v_accvgpr_write_b32 a177, v161\n"
        "v_accvgpr_write_b32 a178, v162\n"
        "v_mfma_f32_16x16x32_bf16 v[108:111], v[44:47], v[200:203], v[108:111]\n"
        "global_load_lds_dwordx4 v243, s[66:67] offset:1024\n"
        "v_accvgpr_write_b32 a179, v163\n"
        "v_mfma_f32_16x16x32_bf16 v[140:143], v[44:47], v[204:207], v[140:143]\n"
        "ds_read_b64_tr_b16 v[44:45], v232 offset:40960\n"
        "v_accvgpr_read_b32 v161, a81\n"
        "v_mfma_f32_16x16x32_bf16 v[16:19], v[48:51], v[192:195], v[16:19]\n"
        "ds_read_b64_tr_b16 v[46:47], v233 offset:40960\n"
        "v_accvgpr_read_b32 v162, a82\n"
        "v_mfma_f32_16x16x32_bf16 v[80:83], v[48:51], v[196:199], v[80:83]\n"
        "v_accvgpr_read_b32 v163, a83\n"
        "v_accvgpr_read_b32 v164, a112\n"
        "v_mfma_f32_16x16x32_bf16 v[112:115], v[48:51], v[200:203], v[112:115]\n"
        "global_store_dwordx4 v254, a[4:7], s[70:71] offset:1024 sc1\n"
        "s_add_u32 s70, s38, s97\n"
        "s_addc_u32 s71, s39, 0\n"
        "s_waitcnt lgkmcnt(4)\n"
        "v_accvgpr_read_b32 v165, a113\n"
        "v_accvgpr_read_b32 v166, a114\n"
        "v_mfma_f32_16x16x32_bf16 v[144:147], v[48:51], v[204:207], v[144:147]\n"
        "ds_read_b64_tr_b16 v[48:49], v226 offset:41984\n"
        "s_add_u32 s97, s53, 0x6000\n"
        "v_accvgpr_read_b32 v167, a115\n"
        "v_mfma_f32_16x16x32_bf16 v[20:23], v[52:55], v[192:195], v[20:23]\n"
        "ds_read_b64_tr_b16 v[50:51], v227 offset:41984\n"
        "v_mfma_f32_16x16x32_bf16 v[84:87], v[52:55], v[196:199], v[84:87]\n"
        "global_load_lds_dwordx4 v244, s[66:67] offset:2048\n"
        "v_mfma_f32_16x16x32_bf16 v[116:119], v[52:55], v[200:203], v[116:119]\n"
        "v_mfma_f32_16x16x32_bf16 v[148:151], v[52:55], v[204:207], v[148:151]\n"
        "ds_read_b64_tr_b16 v[52:53], v228 offset:41984\n"
        "v_mfma_f32_16x16x32_bf16 v[24:27], v[56:59], v[192:195], v[24:27]\n"
        "ds_read_b64_tr_b16 v[54:55], v229 offset:41984\n"
        "v_mfma_f32_16x16x32_bf16 v[88:91], v[56:59], v[196:199], v[88:91]\n"
        "global_load_lds_dwordx4 v245, s[66:67] offset:3072\n"
        "s_add_u32 m0, s73, 0x20000\n"
        "v_mfma_f32_16x16x32_bf16 v[120:123], v[56:59], v[200:203], v[120:123]\n"
        "s_add_u32 s66, s66, 0x80\n"
        "s_addc_u32 s67, s67, 0\n"
        "v_mfma_f32_16x16x32_bf16 v[152:155], v[56:59], v[204:207], v[152:155]\n"
        "ds_read_b64_tr_b16 v[56:57], v230 offset:41984\n"
        "v_mfma_f32_16x16x32_bf16 v[28:31], v[60:63], v[192:195], v[28:31]\n"
        "ds_read_b64_tr_b16 v[58:59], v231 offset:41984\n"
        "v_mfma_f32_16x16x32_bf16 v[92:95], v[60:63], v[196:199], v[92:95]\n"
        "v_mfma_f32_16x16x32_bf16 v[124:127], v[60:63], v[200:203], v[124:127]\n"
        "v_mfma_f32_16x16x32_bf16 v[156:159], v[60:63], v[204:207], v[156:159]\n"
        "ds_read_b64_tr_b16 v[60:61], v232 offset:41984\n"
        "v_accvgpr_read_b32 v168, a20\n"
        "v_mfma_f32_16x16x32_bf16 v[0:3], v[32:35], v[208:211], v[0:3]\n"
        "ds_read_b64_tr_b16 v[62:63], v233 offset:41984\n"
        "v_cvt_pk_bf16_f32 v168, v168, v169\n"
        "v_mfma_f32_16x16x32_bf16 v[64:67], v[32:35], v[212:215], v[64:67]\n"
        "v_cvt_pk_bf16_f32 v169, v170, v171\n"
        "v_cvt_pk_bf16_f32 v170, v172, v173\n"
        "v_mfma_f32_16x16x32_bf16 v[96:99], v[32:35], v[216:219], v[96:99]\n"
        "s_waitcnt lgkmcnt(4)\n"
        "v_cvt_pk_bf16_f32 v171, v174, v175\n"
        "v_accvgpr_write_b32 a180, v168\n"
        "v_mfma_f32_16x16x32_bf16 v[128:131], v[32:35], v[220:223], v[128:131]\n"
        "v_accvgpr_write_b32 a181, v169\n"
        "v_accvgpr_write_b32 a182, v170\n"
        "v_mfma_f32_16x16x32_bf16 v[4:7], v[36:39], v[208:211], v[4:7]\n"
        "v_accvgpr_write_b32 a183, v171\n"
        "v_accvgpr_read_b32 v169, a85\n"
        "v_mfma_f32_16x16x32_bf16 v[68:71], v[36:39], v[212:215], v[68:71]\n"
        "v_accvgpr_read_b32 v170, a86\n"
        "v_accvgpr_read_b32 v171, a87\n"
        "v_mfma_f32_16x16x32_bf16 v[100:103], v[36:39], v[216:219], v[100:103]\n"
        "v_accvgpr_read_b32 v172, a116\n"
        "v_accvgpr_read_b32 v173, a117\n"
        "v_mfma_f32_16x16x32_bf16 v[132:135], v[36:39], v[220:223], v[132:135]\n"
        "v_accvgpr_read_b32 v174, a118\n"
        "v_accvgpr_read_b32 v175, a119\n"
        "v_mfma_f32_16x16x32_bf16 v[8:11], v[40:43], v[208:211], v[8:11]\n"
        "s_waitcnt vmcnt(14) lgkmcnt(0)\n"
        "s_barrier\n"
        "v_mfma_f32_16x16x32_bf16 v[72:75], v[40:43], v[212:215], v[72:75]\n"
        "global_load_lds_dwordx4 v238, s[68:69] offset:0\n"
        "ds_read_b64_tr_b16 v[32:33], v226 offset:49152\n"
        "v_mfma_f32_16x16x32_bf16 v[104:107], v[40:43], v[216:219], v[104:107]\n"
        "ds_read_b64_tr_b16 v[34:35], v227 offset:49152\n"
        "v_mfma_f32_16x16x32_bf16 v[136:139], v[40:43], v[220:223], v[136:139]\n"
        "ds_read_b64_tr_b16 v[36:37], v228 offset:49152\n"
        "v_mfma_f32_16x16x32_bf16 v[12:15], v[44:47], v[208:211], v[12:15]\n"
        "ds_read_b64_tr_b16 v[38:39], v229 offset:49152\n"
        "v_mfma_f32_16x16x32_bf16 v[76:79], v[44:47], v[212:215], v[76:79]\n"
        "ds_read_b64_tr_b16 v[40:41], v230 offset:49152\n"
        "global_load_lds_dwordx4 v239, s[68:69] offset:1024\n"
        "v_mfma_f32_16x16x32_bf16 v[108:111], v[44:47], v[216:219], v[108:111]\n"
        "ds_read_b64_tr_b16 v[42:43], v231 offset:49152\n"
        "v_mfma_f32_16x16x32_bf16 v[140:143], v[44:47], v[220:223], v[140:143]\n"
        "ds_read_b128 v[192:195], v248 offset:2048\n"
        "v_mfma_f32_16x16x32_bf16 v[16:19], v[48:51], v[208:211], v[16:19]\n"
        "ds_read_b64_tr_b16 v[44:45], v232 offset:49152\n"
        "v_mfma_f32_16x16x32_bf16 v[80:83], v[48:51], v[212:215], v[80:83]\n"
        "ds_read_b64_tr_b16 v[46:47], v233 offset:49152\n"
        "global_load_lds_dwordx4 v240, s[68:69] offset:2048\n"
        "v_mfma_f32_16x16x32_bf16 v[112:115], v[48:51], v[216:219], v[112:115]\n"
        "ds_read_b128 v[196:199], v248 offset:0\n"
        "v_mfma_f32_16x16x32_bf16 v[144:147], v[48:51], v[220:223], v[144:147]\n"
        "ds_read_b64_tr_b16 v[48:49], v226 offset:50176\n"
        "v_mfma_f32_16x16x32_bf16 v[20:23], v[52:55], v[208:211], v[20:23]\n"
        "ds_read_b64_tr_b16 v[50:51], v227 offset:50176\n"
        "v_mfma_f32_16x16x32_bf16 v[84:87], v[52:55], v[212:215], v[84:87]\n"
        "ds_read_b128 v[200:203], v248 offset:6144\n"
        "global_load_lds_dwordx4 v241, s[68:69] offset:3072\n"
        "s_add_u32 m0, s72, 0x0\n"
        "v_mfma_f32_16x16x32_bf16 v[116:119], v[52:55], v[216:219], v[116:119]\n"
        "ds_read_b128 v[204:207], v248 offset:4096\n"
        "s_add_u32 s68, s68, 0x80000\n"
        "s_addc_u32 s69, s69, 0\n"
        "v_mfma_f32_16x16x32_bf16 v[148:151], v[52:55], v[220:223], v[148:151]\n"
        "ds_read_b64_tr_b16 v[52:53], v228 offset:50176\n"
        "v_accvgpr_read_b32 v176, a24\n"
        "v_mfma_f32_16x16x32_bf16 v[24:27], v[56:59], v[208:211], v[24:27]\n"
        "ds_read_b64_tr_b16 v[54:55], v229 offset:50176\n"
        "v_cvt_pk_bf16_f32 v176, v176, v177\n"
        "v_mfma_f32_16x16x32_bf16 v[88:91], v[56:59], v[212:215], v[88:91]\n"
        "global_load_lds_dwordx4 v242, s[64:65] offset:0\n"
        "v_mfma_f32_16x16x32_bf16 v[120:123], v[56:59], v[216:219], v[120:123]\n"
        "s_waitcnt lgkmcnt(6)\n"
        "v_cvt_pk_bf16_f32 v177, v178, v179\n"
        "v_cvt_pk_bf16_f32 v178, v180, v181\n"
        "v_mfma_f32_16x16x32_bf16 v[152:155], v[56:59], v[220:223], v[152:155]\n"
        "ds_read_b64_tr_b16 v[56:57], v230 offset:50176\n"
        "v_cvt_pk_bf16_f32 v179, v182, v183\n"
        "v_mfma_f32_16x16x32_bf16 v[28:31], v[60:63], v[208:211], v[28:31]\n"
        "s_waitcnt lgkmcnt(5)\n"
        "v_accvgpr_write_b32 a184, v176\n"
        "v_accvgpr_write_b32 a185, v177\n"
        "v_mfma_f32_16x16x32_bf16 v[92:95], v[60:63], v[212:215], v[92:95]\n"
        "s_waitcnt lgkmcnt(4)\n"
        "v_accvgpr_write_b32 a186, v178\n"
        "v_accvgpr_write_b32 a187, v179\n"
        "v_mfma_f32_16x16x32_bf16 v[124:127], v[60:63], v[216:219], v[124:127]\n"
        "global_store_dwordx4 v254, a[8:11], s[70:71] offset:0 sc1\n"
        "ds_read_b64_tr_b16 v[58:59], v231 offset:50176\n"
        "ds_read_b128 v[208:211], v249 offset:2048\n"
        "v_accvgpr_read_b32 v177, a89\n"
        "v_mfma_f32_16x16x32_bf16 v[156:159], v[60:63], v[220:223], v[156:159]\n"
        "ds_read_b64_tr_b16 v[60:61], v232 offset:50176\n"
        "v_accvgpr_read_b32 v178, a90\n"
        "v_mfma_f32_16x16x32_bf16 v[0:3], v[32:35], v[192:195], v[0:3]\n"
        "ds_read_b64_tr_b16 v[62:63], v233 offset:50176\n"
        "v_accvgpr_read_b32 v179, a91\n"
        "v_mfma_f32_16x16x32_bf16 v[64:67], v[32:35], v[196:199], v[64:67]\n"
        "global_load_lds_dwordx4 v243, s[64:65] offset:1024\n"
        "v_mfma_f32_16x16x32_bf16 v[96:99], v[32:35], v[200:203], v[96:99]\n"
        "ds_read_b128 v[212:215], v249 offset:0\n"
        "s_waitcnt lgkmcnt(5)\n"
        "v_mfma_f32_16x16x32_bf16 v[128:131], v[32:35], v[204:207], v[128:131]\n"
        "ds_read_b64_tr_b16 v[32:33], v226 offset:57344\n"
        "v_accvgpr_read_b32 v180, a120\n"
        "v_mfma_f32_16x16x32_bf16 v[4:7], v[36:39], v[192:195], v[4:7]\n"
        "ds_read_b64_tr_b16 v[34:35], v227 offset:57344\n"
        "v_accvgpr_read_b32 v181, a121\n"
        "v_mfma_f32_16x16x32_bf16 v[68:71], v[36:39], v[196:199], v[68:71]\n"
        "global_load_lds_dwordx4 v244, s[64:65] offset:2048\n"
        "v_mfma_f32_16x16x32_bf16 v[100:103], v[36:39], v[200:203], v[100:103]\n"
        "ds_read_b128 v[216:219], v249 offset:6144\n"
        "v_accvgpr_read_b32 v182, a122\n"
        "v_mfma_f32_16x16x32_bf16 v[132:135], v[36:39], v[204:207], v[132:135]\n"
        "ds_read_b64_tr_b16 v[36:37], v228 offset:57344\n"
        "v_accvgpr_read_b32 v183, a123\n"
        "v_mfma_f32_16x16x32_bf16 v[8:11], v[40:43], v[192:195], v[8:11]\n"
        "ds_read_b64_tr_b16 v[38:39], v229 offset:57344\n"
        "v_mfma_f32_16x16x32_bf16 v[72:75], v[40:43], v[196:199], v[72:75]\n"
        "global_load_lds_dwordx4 v245, s[64:65] offset:3072\n"
        "s_add_u32 m0, s72, 0x1000\n"
        "v_mfma_f32_16x16x32_bf16 v[104:107], v[40:43], v[200:203], v[104:107]\n"
        "ds_read_b128 v[220:223], v249 offset:4096\n"
        "s_add_u32 s64, s64, 0x80\n"
        "s_addc_u32 s65, s65, 0\n"
        "v_mfma_f32_16x16x32_bf16 v[136:139], v[40:43], v[204:207], v[136:139]\n"
        "ds_read_b64_tr_b16 v[40:41], v230 offset:57344\n"
        "v_mfma_f32_16x16x32_bf16 v[12:15], v[44:47], v[192:195], v[12:15]\n"
        "ds_read_b64_tr_b16 v[42:43], v231 offset:57344\n"
        "v_mfma_f32_16x16x32_bf16 v[76:79], v[44:47], v[196:199], v[76:79]\n"
        "global_load_lds_dwordx4 v242, s[66:67] offset:0\n"
        "v_mfma_f32_16x16x32_bf16 v[108:111], v[44:47], v[200:203], v[108:111]\n"
        "v_mfma_f32_16x16x32_bf16 v[140:143], v[44:47], v[204:207], v[140:143]\n"
        "ds_read_b64_tr_b16 v[44:45], v232 offset:57344\n"
        "v_accvgpr_read_b32 v184, a28\n"
        "v_mfma_f32_16x16x32_bf16 v[16:19], v[48:51], v[192:195], v[16:19]\n"
        "ds_read_b64_tr_b16 v[46:47], v233 offset:57344\n"
        "v_cvt_pk_bf16_f32 v184, v184, v185\n"
        "v_mfma_f32_16x16x32_bf16 v[80:83], v[48:51], v[196:199], v[80:83]\n"
        "global_load_lds_dwordx4 v243, s[66:67] offset:1024\n"
        "v_mfma_f32_16x16x32_bf16 v[112:115], v[48:51], v[200:203], v[112:115]\n"
        "s_waitcnt lgkmcnt(4)\n"
        "v_cvt_pk_bf16_f32 v185, v186, v187\n"
        "v_cvt_pk_bf16_f32 v186, v188, v189\n"
        "v_mfma_f32_16x16x32_bf16 v[144:147], v[48:51], v[204:207], v[144:147]\n"
        "ds_read_b64_tr_b16 v[48:49], v226 offset:58368\n"
        "v_cvt_pk_bf16_f32 v187, v190, v191\n"
        "v_mfma_f32_16x16x32_bf16 v[20:23], v[52:55], v[192:195], v[20:23]\n"
        "ds_read_b64_tr_b16 v[50:51], v227 offset:58368\n"
        "v_accvgpr_write_b32 a188, v184\n"
        "v_mfma_f32_16x16x32_bf16 v[84:87], v[52:55], v[196:199], v[84:87]\n"
        "global_load_lds_dwordx4 v244, s[66:67] offset:2048\n"
        "v_mfma_f32_16x16x32_bf16 v[116:119], v[52:55], v[200:203], v[116:119]\n"
        "v_accvgpr_write_b32 a189, v185\n"
        "v_accvgpr_write_b32 a190, v186\n"
        "v_mfma_f32_16x16x32_bf16 v[148:151], v[52:55], v[204:207], v[148:151]\n"
        "ds_read_b64_tr_b16 v[52:53], v228 offset:58368\n"
        "v_accvgpr_write_b32 a191, v187\n"
        "v_mfma_f32_16x16x32_bf16 v[24:27], v[56:59], v[192:195], v[24:27]\n"
        "ds_read_b64_tr_b16 v[54:55], v229 offset:58368\n"
        "v_accvgpr_read_b32 v185, a93\n"
        "v_mfma_f32_16x16x32_bf16 v[88:91], v[56:59], v[196:199], v[88:91]\n"
        "global_load_lds_dwordx4 v245, s[66:67] offset:3072\n"
        "s_add_u32 m0, s73, 0x24000\n"
        "v_mfma_f32_16x16x32_bf16 v[120:123], v[56:59], v[200:203], v[120:123]\n"
        "s_add_u32 s66, s66, 0x80\n"
        "s_addc_u32 s67, s67, 0\n"
        "v_accvgpr_read_b32 v186, a94\n"
        "v_mfma_f32_16x16x32_bf16 v[152:155], v[56:59], v[204:207], v[152:155]\n"
        "ds_read_b64_tr_b16 v[56:57], v230 offset:58368\n"
        "v_accvgpr_read_b32 v187, a95\n"
        "v_mfma_f32_16x16x32_bf16 v[28:31], v[60:63], v[192:195], v[28:31]\n"
        "ds_read_b64_tr_b16 v[58:59], v231 offset:58368\n"
        "v_accvgpr_read_b32 v188, a124\n"
        "v_mfma_f32_16x16x32_bf16 v[92:95], v[60:63], v[196:199], v[92:95]\n"
        "v_accvgpr_read_b32 v189, a125\n"
        "v_accvgpr_read_b32 v190, a126\n"
        "v_mfma_f32_16x16x32_bf16 v[124:127], v[60:63], v[200:203], v[124:127]\n"
        "v_accvgpr_read_b32 v191, a127\n"
        "v_mfma_f32_16x16x32_bf16 v[156:159], v[60:63], v[204:207], v[156:159]\n"
        "ds_read_b64_tr_b16 v[60:61], v232 offset:58368\n"
        "v_mfma_f32_16x16x32_bf16 v[0:3], v[32:35], v[208:211], v[0:3]\n"
        "ds_read_b64_tr_b16 v[62:63], v233 offset:58368\n"
        "v_mfma_f32_16x16x32_bf16 v[64:67], v[32:35], v[212:215], v[64:67]\n"
        "global_store_dwordx4 v254, a[12:15], s[70:71] offset:1024 sc1\n"
        "s_add_u32 s70, s38, s97\n"
        "s_addc_u32 s71, s39, 0\n"
        "v_mfma_f32_16x16x32_bf16 v[96:99], v[32:35], v[216:219], v[96:99]\n"
        "s_waitcnt lgkmcnt(4)\n"
        "v_mfma_f32_16x16x32_bf16 v[128:131], v[32:35], v[220:223], v[128:131]\n"
        "v_mfma_f32_16x16x32_bf16 v[4:7], v[36:39], v[208:211], v[4:7]\n"
        "v_mfma_f32_16x16x32_bf16 v[68:71], v[36:39], v[212:215], v[68:71]\n"
        "v_mfma_f32_16x16x32_bf16 v[100:103], v[36:39], v[216:219], v[100:103]\n"
        "v_mfma_f32_16x16x32_bf16 v[132:135], v[36:39], v[220:223], v[132:135]\n"
        "s_waitcnt vmcnt(14) lgkmcnt(0)\n"
        "s_barrier\n"
        "v_mfma_f32_16x16x32_bf16 v[8:11], v[40:43], v[208:211], v[8:11]\n"
        "global_load_lds_dwordx4 v238, s[68:69] offset:0\n"
        "ds_read_b64_tr_b16 v[32:33], v226 offset:16384\n"
        "v_mfma_f32_16x16x32_bf16 v[72:75], v[40:43], v[212:215], v[72:75]\n"
        "ds_read_b64_tr_b16 v[34:35], v227 offset:16384\n"
        "v_accvgpr_read_b32 v160, a80\n"
        "v_mfma_f32_16x16x32_bf16 v[104:107], v[40:43], v[216:219], v[104:107]\n"
        "ds_read_b64_tr_b16 v[36:37], v228 offset:16384\n"
        "v_cvt_pk_bf16_f32 v160, v160, v161\n"
        "v_mfma_f32_16x16x32_bf16 v[136:139], v[40:43], v[220:223], v[136:139]\n"
        "ds_read_b64_tr_b16 v[38:39], v229 offset:16384\n"
        "v_cvt_pk_bf16_f32 v161, v162, v163\n"
        "v_mfma_f32_16x16x32_bf16 v[12:15], v[44:47], v[208:211], v[12:15]\n"
        "ds_read_b64_tr_b16 v[40:41], v230 offset:16384\n"
        "v_cvt_pk_bf16_f32 v162, v164, v165\n"
        "v_mfma_f32_16x16x32_bf16 v[76:79], v[44:47], v[212:215], v[76:79]\n"
        "ds_read_b64_tr_b16 v[42:43], v231 offset:16384\n"
        "v_cvt_pk_bf16_f32 v163, v166, v167\n"
        "v_mfma_f32_16x16x32_bf16 v[108:111], v[44:47], v[216:219], v[108:111]\n"
        "ds_read_b128 v[192:195], v246 offset:34816\n"
        "v_accvgpr_write_b32 a16, v160\n"
        "v_mfma_f32_16x16x32_bf16 v[140:143], v[44:47], v[220:223], v[140:143]\n"
        "global_load_lds_dwordx4 v239, s[68:69] offset:1024\n"
        "v_accvgpr_write_b32 a17, v161\n"
        "v_mfma_f32_16x16x32_bf16 v[16:19], v[48:51], v[208:211], v[16:19]\n"
        "ds_read_b64_tr_b16 v[44:45], v232 offset:16384\n"
        "v_accvgpr_write_b32 a18, v162\n"
        "v_mfma_f32_16x16x32_bf16 v[80:83], v[48:51], v[212:215], v[80:83]\n"
        "ds_read_b64_tr_b16 v[46:47], v233 offset:16384\n"
        "v_accvgpr_write_b32 a19, v163\n"
        "v_mfma_f32_16x16x32_bf16 v[112:115], v[48:51], v[216:219], v[112:115]\n"
        "ds_read_b128 v[196:199], v246 offset:32768\n"
        "v_mfma_f32_16x16x32_bf16 v[144:147], v[48:51], v[220:223], v[144:147]\n"
        "global_load_lds_dwordx4 v240, s[68:69] offset:2048\n"
        "ds_read_b64_tr_b16 v[48:49], v226 offset:17408\n"
        "v_mfma_f32_16x16x32_bf16 v[20:23], v[52:55], v[208:211], v[20:23]\n"
        "ds_read_b64_tr_b16 v[50:51], v227 offset:17408\n"
        "s_add_u32 s97, s53, 0x4000\n"
        "v_mfma_f32_16x16x32_bf16 v[84:87], v[52:55], v[212:215], v[84:87]\n"
        "ds_read_b128 v[200:203], v246 offset:38912\n"
        "v_mfma_f32_16x16x32_bf16 v[116:119], v[52:55], v[216:219], v[116:119]\n"
        "ds_read_b128 v[204:207], v246 offset:36864\n"
        "v_mfma_f32_16x16x32_bf16 v[148:151], v[52:55], v[220:223], v[148:151]\n"
        "global_load_lds_dwordx4 v241, s[68:69] offset:3072\n"
        "ds_read_b64_tr_b16 v[52:53], v228 offset:17408\n"
        "s_add_u32 m0, s72, 0x10000\n"
        "v_mfma_f32_16x16x32_bf16 v[24:27], v[56:59], v[208:211], v[24:27]\n"
        "ds_read_b64_tr_b16 v[54:55], v229 offset:17408\n"
        "s_add_u32 s68, s68, 0x80000\n"
        "s_addc_u32 s69, s69, 0\n"
        "v_mfma_f32_16x16x32_bf16 v[88:91], v[56:59], v[212:215], v[88:91]\n"
        "v_mfma_f32_16x16x32_bf16 v[120:123], v[56:59], v[216:219], v[120:123]\n"
        "s_waitcnt lgkmcnt(6)\n"
        "v_mfma_f32_16x16x32_bf16 v[152:155], v[56:59], v[220:223], v[152:155]\n"
        "global_load_lds_dwordx4 v242, s[64:65] offset:0\n"
        "ds_read_b64_tr_b16 v[56:57], v230 offset:17408\n"
        "v_mfma_f32_16x16x32_bf16 v[28:31], v[60:63], v[208:211], v[28:31]\n"
        "ds_read_b64_tr_b16 v[58:59], v231 offset:17408\n"
        "s_waitcnt lgkmcnt(6)\n"
        "v_mfma_f32_16x16x32_bf16 v[92:95], v[60:63], v[212:215], v[92:95]\n"
        "ds_read_b128 v[208:211], v247 offset:34816\n"
        "s_waitcnt lgkmcnt(6)\n"
        "v_mfma_f32_16x16x32_bf16 v[124:127], v[60:63], v[216:219], v[124:127]\n"
        "ds_read_b128 v[212:215], v247 offset:32768\n"
        "s_waitcnt lgkmcnt(6)\n"
        "v_mfma_f32_16x16x32_bf16 v[156:159], v[60:63], v[220:223], v[156:159]\n"
        "global_load_lds_dwordx4 v243, s[64:65] offset:1024\n"
        "ds_read_b64_tr_b16 v[60:61], v232 offset:17408\n"
        "v_mfma_f32_16x16x32_bf16 v[0:3], v[32:35], v[192:195], v[0:3]\n"
        "ds_read_b64_tr_b16 v[62:63], v233 offset:17408\n"
        "v_accvgpr_read_b32 v168, a84\n"
        "v_mfma_f32_16x16x32_bf16 v[64:67], v[32:35], v[196:199], v[64:67]\n"
        "ds_read_b128 v[216:219], v247 offset:38912\n"
        "v_cvt_pk_bf16_f32 v168, v168, v169\n"
        "v_mfma_f32_16x16x32_bf16 v[96:99], v[32:35], v[200:203], v[96:99]\n"
        "ds_read_b128 v[220:223], v247 offset:36864\n"
        "v_cvt_pk_bf16_f32 v169, v170, v171\n"
        "v_mfma_f32_16x16x32_bf16 v[128:131], v[32:35], v[204:207], v[128:131]\n"
        "global_load_lds_dwordx4 v244, s[64:65] offset:2048\n"
        "v_cvt_pk_bf16_f32 v170, v172, v173\n"
        "v_mfma_f32_16x16x32_bf16 v[4:7], v[36:39], v[192:195], v[4:7]\n"
        "ds_read_b64_tr_b16 v[32:33], v226 offset:24576\n"
        "v_cvt_pk_bf16_f32 v171, v174, v175\n"
        "v_mfma_f32_16x16x32_bf16 v[68:71], v[36:39], v[196:199], v[68:71]\n"
        "ds_read_b64_tr_b16 v[34:35], v227 offset:24576\n"
        "v_accvgpr_write_b32 a20, v168\n"
        "v_mfma_f32_16x16x32_bf16 v[100:103], v[36:39], v[200:203], v[100:103]\n"
        "v_accvgpr_write_b32 a21, v169\n"
        "v_accvgpr_write_b32 a22, v170\n"
        "v_mfma_f32_16x16x32_bf16 v[132:135], v[36:39], v[204:207], v[132:135]\n"
        "global_load_lds_dwordx4 v245, s[64:65] offset:3072\n"
        "s_add_u32 m0, s72, 0x11000\n"
        "v_accvgpr_write_b32 a23, v171\n"
        "v_mfma_f32_16x16x32_bf16 v[8:11], v[40:43], v[192:195], v[8:11]\n"
        "ds_read_b64_tr_b16 v[36:37], v228 offset:24576\n"
        "s_add_u32 s64, s64, 0x80\n"
        "s_addc_u32 s65, s65, 0\n"
        "v_mfma_f32_16x16x32_bf16 v[72:75], v[40:43], v[196:199], v[72:75]\n"
        "ds_read_b64_tr_b16 v[38:39], v229 offset:24576\n"
        "v_mfma_f32_16x16x32_bf16 v[104:107], v[40:43], v[200:203], v[104:107]\n"
        "v_mfma_f32_16x16x32_bf16 v[136:139], v[40:43], v[204:207], v[136:139]\n"
        "global_load_lds_dwordx4 v242, s[66:67] offset:0\n"
        "ds_read_b64_tr_b16 v[40:41], v230 offset:24576\n"
        "v_mfma_f32_16x16x32_bf16 v[12:15], v[44:47], v[192:195], v[12:15]\n"
        "ds_read_b64_tr_b16 v[42:43], v231 offset:24576\n"
        "v_mfma_f32_16x16x32_bf16 v[76:79], v[44:47], v[196:199], v[76:79]\n"
        "v_mfma_f32_16x16x32_bf16 v[108:111], v[44:47], v[200:203], v[108:111]\n"
        "s_waitcnt lgkmcnt(4)\n"
        "v_mfma_f32_16x16x32_bf16 v[140:143], v[44:47], v[204:207], v[140:143]\n"
        "global_store_dwordx4 v254, a[208:211], s[70:71] offset:0 sc1\n"
        "ds_read_b64_tr_b16 v[44:45], v232 offset:24576\n"
        "ds_read_b64_tr_b16 v[46:47], v233 offset:24576\n"
        "v_mfma_f32_16x16x32_bf16 v[16:19], v[48:51], v[192:195], v[16:19]\n"
        "v_mfma_f32_16x16x32_bf16 v[80:83], v[48:51], v[196:199], v[80:83]\n"
        "v_mfma_f32_16x16x32_bf16 v[112:115], v[48:51], v[200:203], v[112:115]\n"
        "global_load_lds_dwordx4 v243, s[66:67] offset:1024\n"
        "v_mfma_f32_16x16x32_bf16 v[144:147], v[48:51], v[204:207], v[144:147]\n"
        "ds_read_b64_tr_b16 v[48:49], v226 offset:25600\n"
        "v_mfma_f32_16x16x32_bf16 v[20:23], v[52:55], v[192:195], v[20:23]\n"
        "ds_read_b64_tr_b16 v[50:51], v227 offset:25600\n"
        "v_mfma_f32_16x16x32_bf16 v[84:87], v[52:55], v[196:199], v[84:87]\n"
        "v_mfma_f32_16x16x32_bf16 v[116:119], v[52:55], v[200:203], v[116:119]\n"
        "global_load_lds_dwordx4 v244, s[66:67] offset:2048\n"
        "v_mfma_f32_16x16x32_bf16 v[148:151], v[52:55], v[204:207], v[148:151]\n"
        "ds_read_b64_tr_b16 v[52:53], v228 offset:25600\n"
        "v_accvgpr_read_b32 v176, a88\n"
        "v_mfma_f32_16x16x32_bf16 v[24:27], v[56:59], v[192:195], v[24:27]\n"
        "ds_read_b64_tr_b16 v[54:55], v229 offset:25600\n"
        "v_cvt_pk_bf16_f32 v176, v176, v177\n"
        "v_mfma_f32_16x16x32_bf16 v[88:91], v[56:59], v[196:199], v[88:91]\n"
        "v_cvt_pk_bf16_f32 v177, v178, v179\n"
        "v_cvt_pk_bf16_f32 v178, v180, v181\n"
        "v_mfma_f32_16x16x32_bf16 v[120:123], v[56:59], v[200:203], v[120:123]\n"
        "global_load_lds_dwordx4 v245, s[66:67] offset:3072\n"
        "s_add_u32 m0, s73, 0x1c000\n"
        "v_mfma_f32_16x16x32_bf16 v[152:155], v[56:59], v[204:207], v[152:155]\n"
        "s_add_u32 s66, s66, 0x80\n"
        "s_addc_u32 s67, s67, 0\n"
        "v_cvt_pk_bf16_f32 v179, v182, v183\n"
        "v_mfma_f32_16x16x32_bf16 v[28:31], v[60:63], v[192:195], v[28:31]\n"
        "ds_read_b64_tr_b16 v[56:57], v230 offset:25600\n"
        "v_accvgpr_write_b32 a24, v176\n"
        "v_mfma_f32_16x16x32_bf16 v[92:95], v[60:63], v[196:199], v[92:95]\n"
        "ds_read_b64_tr_b16 v[58:59], v231 offset:25600\n"
        "v_accvgpr_write_b32 a25, v177\n"
        "v_mfma_f32_16x16x32_bf16 v[124:127], v[60:63], v[200:203], v[124:127]\n"
        "s_waitcnt lgkmcnt(4)\n"
        "v_accvgpr_write_b32 a26, v178\n"
        "v_accvgpr_write_b32 a27, v179\n"
        "v_mfma_f32_16x16x32_bf16 v[156:159], v[60:63], v[204:207], v[156:159]\n"
        "ds_read_b64_tr_b16 v[60:61], v232 offset:25600\n"
        "v_mfma_f32_16x16x32_bf16 v[0:3], v[32:35], v[208:211], v[0:3]\n"
        "ds_read_b64_tr_b16 v[62:63], v233 offset:25600\n"
        "s_lshl_b32 s90, s43, 19\n"
        "s_add_u32 s90, s90, s72\n"
        "v_mfma_f32_16x16x32_bf16 v[64:67], v[32:35], v[212:215], v[64:67]\n"
        "s_and_b32 s78, s45, 1\n"
        "s_lshl_b32 s78, s78, 15\n"
        "s_add_u32 s78, s78, 0x10000\n"
        "v_mfma_f32_16x16x32_bf16 v[96:99], v[32:35], v[216:219], v[96:99]\n"
        "s_and_b32 s80, s40, 1\n"
        "s_lshl_b32 s80, s80, 11\n"
        "s_lshl_b32 s82, s40, 4\n"
        "v_mfma_f32_16x16x32_bf16 v[128:131], v[32:35], v[220:223], v[128:131]\n"
        "v_accvgpr_write_b32 a48, 0\n"
        "v_accvgpr_write_b32 a49, 0\n"
        "v_mfma_f32_16x16x32_bf16 v[4:7], v[36:39], v[208:211], v[4:7]\n"
        "v_accvgpr_write_b32 a50, 0\n"
        "v_accvgpr_write_b32 a51, 0\n"
        "v_mfma_f32_16x16x32_bf16 v[68:71], v[36:39], v[212:215], v[68:71]\n"
        "v_accvgpr_write_b32 a52, 0\n"
        "v_accvgpr_write_b32 a53, 0\n"
        "v_mfma_f32_16x16x32_bf16 v[100:103], v[36:39], v[216:219], v[100:103]\n"
        "v_accvgpr_write_b32 a54, 0\n"
        "v_accvgpr_write_b32 a55, 0\n"
        "v_mfma_f32_16x16x32_bf16 v[132:135], v[36:39], v[220:223], v[132:135]\n"
        "s_sub_u32 s83, 14, s44\n"
        "v_accvgpr_write_b32 a56, 0\n"
        "v_accvgpr_write_b32 a57, 0\n"
        "v_mfma_f32_16x16x32_bf16 v[8:11], v[40:43], v[208:211], v[8:11]\n"
        "s_waitcnt vmcnt(14) lgkmcnt(0)\n"
        "s_barrier\n"
        "v_mfma_f32_16x16x32_bf16 v[72:75], v[40:43], v[212:215], v[72:75]\n"
        "global_load_lds_dwordx4 v238, s[68:69] offset:0\n"
        "ds_read_b64_tr_b16 v[32:33], v226 offset:32768\n"
        "v_mfma_f32_16x16x32_bf16 v[104:107], v[40:43], v[216:219], v[104:107]\n"
        "ds_read_b64_tr_b16 v[34:35], v227 offset:32768\n"
        "v_accvgpr_write_b32 a58, 0\n"
        "v_mfma_f32_16x16x32_bf16 v[136:139], v[40:43], v[220:223], v[136:139]\n"
        "ds_read_b64_tr_b16 v[36:37], v228 offset:32768\n"
        "v_accvgpr_write_b32 a59, 0\n"
        "v_mfma_f32_16x16x32_bf16 v[12:15], v[44:47], v[208:211], v[12:15]\n"
        "ds_read_b64_tr_b16 v[38:39], v229 offset:32768\n"
        "v_accvgpr_write_b32 a60, 0\n"
        "v_mfma_f32_16x16x32_bf16 v[76:79], v[44:47], v[212:215], v[76:79]\n"
        "ds_read_b64_tr_b16 v[40:41], v230 offset:32768\n"
        "global_load_lds_dwordx4 v239, s[68:69] offset:1024\n"
        "s_lshl_b32 s50, s41, 7\n"
        "v_mfma_f32_16x16x32_bf16 v[108:111], v[44:47], v[216:219], v[108:111]\n"
        "ds_read_b64_tr_b16 v[42:43], v231 offset:32768\n"
        "s_lshl2_add_u32 s50, s42, s50\n"
        "v_accvgpr_write_b32 a61, 0\n"
        "v_mfma_f32_16x16x32_bf16 v[140:143], v[44:47], v[220:223], v[140:143]\n"
        "ds_read_b128 v[192:195], v246 offset:2048\n"
        "s_add_u32 s50, s50, s40\n"
        "v_accvgpr_read_b32 v184, a92\n"
        "v_mfma_f32_16x16x32_bf16 v[16:19], v[48:51], v[208:211], v[16:19]\n"
        "ds_read_b64_tr_b16 v[44:45], v232 offset:32768\n"
        "s_lshl_b32 s50, s50, 12\n"
        "v_cvt_pk_bf16_f32 v184, v184, v185\n"
        "v_mfma_f32_16x16x32_bf16 v[80:83], v[48:51], v[212:215], v[80:83]\n"
        "ds_read_b64_tr_b16 v[46:47], v233 offset:32768\n"
        "global_load_lds_dwordx4 v240, s[68:69] offset:2048\n"
        "s_mul_i32 s51, s40, 8320\n"
        "v_mfma_f32_16x16x32_bf16 v[112:115], v[48:51], v[216:219], v[112:115]\n"
        "ds_read_b128 v[196:199], v246 offset:0\n"
        "s_add_u32 s51, s51, 0x18000\n"
        "v_cvt_pk_bf16_f32 v185, v186, v187\n"
        "v_mfma_f32_16x16x32_bf16 v[144:147], v[48:51], v[220:223], v[144:147]\n"
        "ds_read_b64_tr_b16 v[48:49], v226 offset:33792\n"
        "v_cvt_pk_bf16_f32 v186, v188, v189\n"
        "v_mfma_f32_16x16x32_bf16 v[20:23], v[52:55], v[208:211], v[20:23]\n"
        "ds_read_b64_tr_b16 v[50:51], v227 offset:33792\n"
        "v_cvt_pk_bf16_f32 v187, v190, v191\n"
        "v_mfma_f32_16x16x32_bf16 v[84:87], v[52:55], v[212:215], v[84:87]\n"
        "ds_read_b128 v[200:203], v246 offset:6144\n"
        "v_accvgpr_write_b32 a28, v184\n"
        "v_mfma_f32_16x16x32_bf16 v[116:119], v[52:55], v[216:219], v[116:119]\n"
        "global_load_lds_dwordx4 v241, s[68:69] offset:3072\n"
        "s_add_u32 m0, s72, 0x8000\n"
        "v_accvgpr_write_b32 a29, v185\n"
        "v_mfma_f32_16x16x32_bf16 v[148:151], v[52:55], v[220:223], v[148:151]\n"
        "ds_read_b64_tr_b16 v[52:53], v228 offset:33792\n"
        "v_accvgpr_write_b32 a30, v186\n"
        "v_mfma_f32_16x16x32_bf16 v[24:27], v[56:59], v[208:211], v[24:27]\n"
        "ds_read_b64_tr_b16 v[54:55], v229 offset:33792\n"
        "v_accvgpr_write_b32 a31, v187\n"
        "v_mfma_f32_16x16x32_bf16 v[88:91], v[56:59], v[212:215], v[88:91]\n"
        "ds_read_b128 v[204:207], v246 offset:4096\n"
        "s_add_u32 s68, s68, 0x80000\n"
        "s_addc_u32 s69, s69, 0\n"
        "v_mfma_f32_16x16x32_bf16 v[120:123], v[56:59], v[216:219], v[120:123]\n"
        "global_store_dwordx4 v254, a[240:243], s[70:71] offset:1024 sc1\n"
        "s_waitcnt lgkmcnt(6)\n"
        "v_accvgpr_write_b32 a62, 0\n"
        "v_accvgpr_write_b32 a63, 0\n"
        "v_mfma_f32_16x16x32_bf16 v[152:155], v[56:59], v[220:223], v[152:155]\n"
        "ds_read_b64_tr_b16 v[56:57], v230 offset:33792\n"
        "s_add_u32 s92, s26, s50\n"
        "s_addc_u32 s93, s27, 0\n"
        "v_mfma_f32_16x16x32_bf16 v[28:31], v[60:63], v[208:211], v[28:31]\n"
        "ds_read_b64_tr_b16 v[58:59], v231 offset:33792\n"
        "s_add_u32 s50, s50, 0x3000000\n"
        "s_waitcnt lgkmcnt(5)\n"
        "v_mfma_f32_16x16x32_bf16 v[92:95], v[60:63], v[212:215], v[92:95]\n"
        "global_load_lds_dwordx4 v242, s[64:65] offset:0\n"
        "v_mfma_f32_16x16x32_bf16 v[124:127], v[60:63], v[216:219], v[124:127]\n"
        "ds_read_b128 v[208:211], v247 offset:2048\n"
        "s_add_u32 s94, s38, s50\n"
        "s_addc_u32 s95, s39, 0\n"
        "v_mfma_f32_16x16x32_bf16 v[156:159], v[60:63], v[220:223], v[156:159]\n"
        "ds_read_b64_tr_b16 v[60:61], v232 offset:33792\n"
        "s_add_u32 s50, s51, 4128\n"
        "s_waitcnt lgkmcnt(5)\n"
        "v_mfma_f32_16x16x32_bf16 v[0:3], v[32:35], v[192:195], v[0:3]\n"
        "ds_read_b64_tr_b16 v[62:63], v233 offset:33792\n"
        "s_waitcnt lgkmcnt(5)\n"
        "v_mfma_f32_16x16x32_bf16 v[64:67], v[32:35], v[196:199], v[64:67]\n"
        "global_load_lds_dwordx4 v243, s[64:65] offset:1024\n"
        "v_mfma_f32_16x16x32_bf16 v[96:99], v[32:35], v[200:203], v[96:99]\n"
        "ds_read_b128 v[212:215], v247 offset:0\n"
        "v_mfma_f32_16x16x32_bf16 v[128:131], v[32:35], v[204:207], v[128:131]\n"
        "ds_read_b64_tr_b16 v[32:33], v226 offset:40960\n"
        "v_mfma_f32_16x16x32_bf16 v[4:7], v[36:39], v[192:195], v[4:7]\n"
        "ds_read_b64_tr_b16 v[34:35], v227 offset:40960\n"
        "v_mfma_f32_16x16x32_bf16 v[68:71], v[36:39], v[196:199], v[68:71]\n"
        "global_load_lds_dwordx4 v244, s[64:65] offset:2048\n"
        "v_mfma_f32_16x16x32_bf16 v[100:103], v[36:39], v[200:203], v[100:103]\n"
        "ds_read_b128 v[216:219], v247 offset:6144\n"
        "v_mfma_f32_16x16x32_bf16 v[132:135], v[36:39], v[204:207], v[132:135]\n"
        "ds_read_b64_tr_b16 v[36:37], v228 offset:40960\n"
        "v_mfma_f32_16x16x32_bf16 v[8:11], v[40:43], v[192:195], v[8:11]\n"
        "ds_read_b64_tr_b16 v[38:39], v229 offset:40960\n"
        "v_mfma_f32_16x16x32_bf16 v[72:75], v[40:43], v[196:199], v[72:75]\n"
        "global_load_lds_dwordx4 v245, s[64:65] offset:3072\n"
        "s_add_u32 m0, s72, 0x9000\n"
        "v_mfma_f32_16x16x32_bf16 v[104:107], v[40:43], v[200:203], v[104:107]\n"
        "ds_read_b128 v[220:223], v247 offset:4096\n"
        "s_add_u32 s64, s64, 0x80\n"
        "s_addc_u32 s65, s65, 0\n"
        "v_mfma_f32_16x16x32_bf16 v[136:139], v[40:43], v[204:207], v[136:139]\n"
        "ds_read_b64_tr_b16 v[40:41], v230 offset:40960\n"
        "v_mfma_f32_16x16x32_bf16 v[12:15], v[44:47], v[192:195], v[12:15]\n"
        "ds_read_b64_tr_b16 v[42:43], v231 offset:40960\n"
        "v_mfma_f32_16x16x32_bf16 v[76:79], v[44:47], v[196:199], v[76:79]\n"
        "global_load_lds_dwordx4 v242, s[66:67] offset:0\n"
        "v_mfma_f32_16x16x32_bf16 v[108:111], v[44:47], v[200:203], v[108:111]\n"
        "v_mfma_f32_16x16x32_bf16 v[140:143], v[44:47], v[204:207], v[140:143]\n"
        "ds_read_b64_tr_b16 v[44:45], v232 offset:40960\n"
        "v_mfma_f32_16x16x32_bf16 v[16:19], v[48:51], v[192:195], v[16:19]\n"
        "ds_read_b64_tr_b16 v[46:47], v233 offset:40960\n"
        "v_mfma_f32_16x16x32_bf16 v[80:83], v[48:51], v[196:199], v[80:83]\n"
        "global_load_lds_dwordx4 v243, s[66:67] offset:1024\n"
        "v_mfma_f32_16x16x32_bf16 v[112:115], v[48:51], v[200:203], v[112:115]\n"
        "s_waitcnt lgkmcnt(4)\n"
        "v_mfma_f32_16x16x32_bf16 v[144:147], v[48:51], v[204:207], v[144:147]\n"
        "ds_read_b64_tr_b16 v[48:49], v226 offset:41984\n"
        "v_mfma_f32_16x16x32_bf16 v[20:23], v[52:55], v[192:195], v[20:23]\n"
        "ds_read_b64_tr_b16 v[50:51], v227 offset:41984\n"
        "v_mfma_f32_16x16x32_bf16 v[84:87], v[52:55], v[196:199], v[84:87]\n"
        "global_load_lds_dwordx4 v244, s[66:67] offset:2048\n"
        "v_mfma_f32_16x16x32_bf16 v[116:119], v[52:55], v[200:203], v[116:119]\n"
        "v_mfma_f32_16x16x32_bf16 v[148:151], v[52:55], v[204:207], v[148:151]\n"
        "ds_read_b64_tr_b16 v[52:53], v228 offset:41984\n"
        "v_mfma_f32_16x16x32_bf16 v[24:27], v[56:59], v[192:195], v[24:27]\n"
        "ds_read_b64_tr_b16 v[54:55], v229 offset:41984\n"
        "v_mfma_f32_16x16x32_bf16 v[88:91], v[56:59], v[196:199], v[88:91]\n"
        "global_load_lds_dwordx4 v245, s[66:67] offset:3072\n"
        "s_add_u32 m0, s73, 0x20000\n"
        "v_mfma_f32_16x16x32_bf16 v[120:123], v[56:59], v[200:203], v[120:123]\n"
        "s_add_u32 s66, s66, 0x80\n"
        "s_addc_u32 s67, s67, 0\n"
        "s_mov_b32 s79, s72\n"
        "v_mfma_f32_16x16x32_bf16 v[152:155], v[56:59], v[204:207], v[152:155]\n"
        "ds_read_b64_tr_b16 v[56:57], v230 offset:41984\n"
        "s_xor_b32 s79, s79, 0x8000\n"
        "v_mfma_f32_16x16x32_bf16 v[28:31], v[60:63], v[192:195], v[28:31]\n"
        "ds_read_b64_tr_b16 v[58:59], v231 offset:41984\n"
        "v_mfma_f32_16x16x32_bf16 v[92:95], v[60:63], v[196:199], v[92:95]\n"
        "v_mfma_f32_16x16x32_bf16 v[124:127], v[60:63], v[200:203], v[124:127]\n"
        "v_mfma_f32_16x16x32_bf16 v[156:159], v[60:63], v[204:207], v[156:159]\n"
        "ds_read_b64_tr_b16 v[60:61], v232 offset:41984\n"
        "v_mfma_f32_16x16x32_bf16 v[0:3], v[32:35], v[208:211], v[0:3]\n"
        "ds_read_b64_tr_b16 v[62:63], v233 offset:41984\n"
        "v_mfma_f32_16x16x32_bf16 v[64:67], v[32:35], v[212:215], v[64:67]\n"
        "v_mfma_f32_16x16x32_bf16 v[96:99], v[32:35], v[216:219], v[96:99]\n"
        "s_waitcnt lgkmcnt(4)\n"
        "v_mfma_f32_16x16x32_bf16 v[128:131], v[32:35], v[220:223], v[128:131]\n"
        "global_store_dwordx4 v254, a[212:215], s[70:71] offset:2048 sc1\n"
        "v_mfma_f32_16x16x32_bf16 v[4:7], v[36:39], v[208:211], v[4:7]\n"
        "v_mfma_f32_16x16x32_bf16 v[68:71], v[36:39], v[212:215], v[68:71]\n"
        "v_mfma_f32_16x16x32_bf16 v[100:103], v[36:39], v[216:219], v[100:103]\n"
        "v_mfma_f32_16x16x32_bf16 v[132:135], v[36:39], v[220:223], v[132:135]\n"
        "s_waitcnt vmcnt(14) lgkmcnt(0)\n"
        "s_barrier\n"
        "v_mfma_f32_16x16x32_bf16 v[8:11], v[40:43], v[208:211], v[8:11]\n"
        "global_load_lds_dwordx4 v238, s[68:69] offset:0\n"
        "ds_read_b64_tr_b16 v[32:33], v226 offset:49152\n"
        "v_mfma_f32_16x16x32_bf16 v[72:75], v[40:43], v[212:215], v[72:75]\n"
        "ds_read_b64_tr_b16 v[34:35], v227 offset:49152\n"
        "v_mfma_f32_16x16x32_bf16 v[104:107], v[40:43], v[216:219], v[104:107]\n"
        "ds_read_b64_tr_b16 v[36:37], v228 offset:49152\n"
        "v_mfma_f32_16x16x32_bf16 v[136:139], v[40:43], v[220:223], v[136:139]\n"
        "ds_read_b64_tr_b16 v[38:39], v229 offset:49152\n"
        "v_mfma_f32_16x16x32_bf16 v[12:15], v[44:47], v[208:211], v[12:15]\n"
        "ds_read_b64_tr_b16 v[40:41], v230 offset:49152\n"
        "global_load_lds_dwordx4 v239, s[68:69] offset:1024\n"
        "v_mfma_f32_16x16x32_bf16 v[76:79], v[44:47], v[212:215], v[76:79]\n"
        "ds_read_b64_tr_b16 v[42:43], v231 offset:49152\n"
        "v_mfma_f32_16x16x32_bf16 v[108:111], v[44:47], v[216:219], v[108:111]\n"
        "ds_read_b128 v[192:195], v248 offset:2048\n"
        "v_mfma_f32_16x16x32_bf16 v[140:143], v[44:47], v[220:223], v[140:143]\n"
        "ds_read_b64_tr_b16 v[44:45], v232 offset:49152\n"
        "v_mfma_f32_16x16x32_bf16 v[16:19], v[48:51], v[208:211], v[16:19]\n"
        "ds_read_b64_tr_b16 v[46:47], v233 offset:49152\n"
        "global_load_lds_dwordx4 v240, s[68:69] offset:2048\n"
        "v_mfma_f32_16x16x32_bf16 v[80:83], v[48:51], v[212:215], v[80:83]\n"
        "ds_read_b128 v[196:199], v248 offset:0\n"
        "v_add_u32 v240, s78, v254\n"
        "v_mfma_f32_16x16x32_bf16 v[112:115], v[48:51], v[216:219], v[112:115]\n"
        "ds_read_b128 v[200:203], v248 offset:6144\n"
        "s_xor_b32 s78, s78, 0x8000\n"
        "v_mfma_f32_16x16x32_bf16 v[144:147], v[48:51], v[220:223], v[144:147]\n"
        "ds_read_b64_tr_b16 v[48:49], v226 offset:50176\n"
        "v_mfma_f32_16x16x32_bf16 v[20:23], v[52:55], v[208:211], v[20:23]\n"
        "ds_read_b64_tr_b16 v[50:51], v227 offset:50176\n"
        "global_load_lds_dwordx4 v241, s[68:69] offset:3072\n"
        "s_add_u32 m0, s72, 0x0\n"
        "v_mfma_f32_16x16x32_bf16 v[84:87], v[52:55], v[212:215], v[84:87]\n"
        "ds_read_b128 v[204:207], v248 offset:4096\n"
        "v_mov_b32 v241, v254\n"
        "v_mfma_f32_16x16x32_bf16 v[116:119], v[52:55], v[216:219], v[116:119]\n"
        "v_mfma_f32_16x16x32_bf16 v[148:151], v[52:55], v[220:223], v[148:151]\n"
        "ds_read_b64_tr_b16 v[52:53], v228 offset:50176\n"
        "v_mfma_f32_16x16x32_bf16 v[24:27], v[56:59], v[208:211], v[24:27]\n"
        "ds_read_b64_tr_b16 v[54:55], v229 offset:50176\n"
        "global_load_lds_dwordx4 v242, s[64:65] offset:0\n"
        "v_mfma_f32_16x16x32_bf16 v[88:91], v[56:59], v[212:215], v[88:91]\n"
        "v_mfma_f32_16x16x32_bf16 v[120:123], v[56:59], v[216:219], v[120:123]\n"
        "s_waitcnt lgkmcnt(5)\n"
        "v_mfma_f32_16x16x32_bf16 v[152:155], v[56:59], v[220:223], v[152:155]\n"
        "ds_read_b64_tr_b16 v[56:57], v230 offset:50176\n"
        "v_mfma_f32_16x16x32_bf16 v[28:31], v[60:63], v[208:211], v[28:31]\n"
        "ds_read_b64_tr_b16 v[58:59], v231 offset:50176\n"
        "global_load_lds_dwordx4 v243, s[64:65] offset:1024\n"
        "v_mfma_f32_16x16x32_bf16 v[92:95], v[60:63], v[212:215], v[92:95]\n"
        "ds_read_b128 v[208:211], v249 offset:2048\n"
        "v_xor_b32 v241, 0x8000, v241\n"
        "v_mfma_f32_16x16x32_bf16 v[124:127], v[60:63], v[216:219], v[124:127]\n"
        "ds_read_b128 v[212:215], v249 offset:0\n"
        "s_waitcnt lgkmcnt(6)\n"
        "v_mfma_f32_16x16x32_bf16 v[156:159], v[60:63], v[220:223], v[156:159]\n"
        "ds_read_b64_tr_b16 v[60:61], v232 offset:50176\n"
        "v_mfma_f32_16x16x32_bf16 v[0:3], v[32:35], v[192:195], v[0:3]\n"
        "ds_read_b64_tr_b16 v[62:63], v233 offset:50176\n"
        "global_load_lds_dwordx4 v244, s[64:65] offset:2048\n"
        "v_mfma_f32_16x16x32_bf16 v[64:67], v[32:35], v[196:199], v[64:67]\n"
        "ds_read_b128 v[216:219], v249 offset:6144\n"
        "v_mfma_f32_16x16x32_bf16 v[96:99], v[32:35], v[200:203], v[96:99]\n"
        "ds_read_b128 v[220:223], v249 offset:4096\n"
        "v_mfma_f32_16x16x32_bf16 v[128:131], v[32:35], v[204:207], v[128:131]\n"
        "ds_read_b64_tr_b16 v[32:33], v226 offset:57344\n"
        "v_mfma_f32_16x16x32_bf16 v[4:7], v[36:39], v[192:195], v[4:7]\n"
        "ds_read_b64_tr_b16 v[34:35], v227 offset:57344\n"
        "global_load_lds_dwordx4 v245, s[64:65] offset:3072\n"
        "s_add_u32 m0, s72, 0x1000\n"
        "v_mfma_f32_16x16x32_bf16 v[68:71], v[36:39], v[196:199], v[68:71]\n"
        "s_add_u32 s64, s38, s90\n"
        "s_addc_u32 s65, s39, 0\n"
        "s_lshl_b32 s90, s40, 14\n"
        "v_mfma_f32_16x16x32_bf16 v[100:103], v[36:39], v[200:203], v[100:103]\n"
        "s_and_b32 s90, s90, 0x8000\n"
        "s_xor_b32 s91, s78, s90\n"
        "s_add_u32 s91, s91, s80\n"
        "v_mfma_f32_16x16x32_bf16 v[132:135], v[36:39], v[204:207], v[132:135]\n"
        "ds_read_b64_tr_b16 v[36:37], v228 offset:57344\n"
        "s_add_u32 s78, s78, s72\n"
        "s_xor_b32 s78, s78, 0x8000\n"
        "v_mfma_f32_16x16x32_bf16 v[8:11], v[40:43], v[192:195], v[8:11]\n"
        "ds_read_b64_tr_b16 v[38:39], v229 offset:57344\n"
        "global_load_lds_dwordx4 v242, s[66:67] offset:0\n"
        "v_mfma_f32_16x16x32_bf16 v[72:75], v[40:43], v[196:199], v[72:75]\n"
        "v_lshrrev_b32 v242, 6, v254\n"
        "v_and_b32 v242, 12, v242\n"
        "v_mfma_f32_16x16x32_bf16 v[104:107], v[40:43], v[200:203], v[104:107]\n"
        "v_mfma_f32_16x16x32_bf16 v[136:139], v[40:43], v[204:207], v[136:139]\n"
        "ds_read_b64_tr_b16 v[40:41], v230 offset:57344\n"
        "v_mfma_f32_16x16x32_bf16 v[12:15], v[44:47], v[192:195], v[12:15]\n"
        "ds_read_b64_tr_b16 v[42:43], v231 offset:57344\n"
        "global_load_lds_dwordx4 v243, s[66:67] offset:1024\n"
        "v_mfma_f32_16x16x32_bf16 v[76:79], v[44:47], v[196:199], v[76:79]\n"
        "v_bfe_u32 v243, v254, 4, 4\n"
        "v_sub_u32 v243, v243, v242\n"
        "v_mfma_f32_16x16x32_bf16 v[108:111], v[44:47], v[200:203], v[108:111]\n"
        "s_waitcnt lgkmcnt(4)\n"
        "v_add_u32 v243, s82, v243\n"
        "v_cmp_gt_i32 s[10:11], 0, v243\n"
        "v_mfma_f32_16x16x32_bf16 v[140:143], v[44:47], v[204:207], v[140:143]\n"
        "ds_read_b64_tr_b16 v[44:45], v232 offset:57344\n"
        "v_cmp_gt_i32 s[12:13], 1, v243\n"
        "v_mfma_f32_16x16x32_bf16 v[16:19], v[48:51], v[192:195], v[16:19]\n"
        "ds_read_b64_tr_b16 v[46:47], v233 offset:57344\n"
        "global_load_lds_dwordx4 v244, s[66:67] offset:2048\n"
        "v_mfma_f32_16x16x32_bf16 v[80:83], v[48:51], v[196:199], v[80:83]\n"
        "v_add_u32 v244, s91, v254\n"
        "s_lshl1_add_u32 s91, s80, s90\n"
        "v_mfma_f32_16x16x32_bf16 v[112:115], v[48:51], v[200:203], v[112:115]\n"
        "v_cmp_gt_i32 s[14:15], 2, v243\n"
        "v_cmp_gt_i32 s[16:17], 3, v243\n"
        "v_mfma_f32_16x16x32_bf16 v[144:147], v[48:51], v[204:207], v[144:147]\n"
        "ds_read_b64_tr_b16 v[48:49], v226 offset:58368\n"
        "v_cmp_gt_i32 s[20:21], 16, v243\n"
        "v_mfma_f32_16x16x32_bf16 v[20:23], v[52:55], v[192:195], v[20:23]\n"
        "ds_read_b64_tr_b16 v[50:51], v227 offset:58368\n"
        "global_load_lds_dwordx4 v245, s[66:67] offset:3072\n"
        "v_mfma_f32_16x16x32_bf16 v[84:87], v[52:55], v[196:199], v[84:87]\n"
        "s_add_u32 s66, s64, 0x1000000\n"
        "s_addc_u32 s67, s65, 0\n"
        "v_add_u32 v245, s91, v254\n"
        "v_mfma_f32_16x16x32_bf16 v[116:119], v[52:55], v[200:203], v[116:119]\n"
        "v_cmp_gt_i32 s[22:23], 17, v243\n"
        "v_cmp_gt_i32 s[24:25], 18, v243\n"
        "v_mfma_f32_16x16x32_bf16 v[148:151], v[52:55], v[204:207], v[148:151]\n"
        "ds_read_b64_tr_b16 v[52:53], v228 offset:58368\n"
        "v_cmp_gt_i32 s[28:29], 19, v243\n"
        "v_mfma_f32_16x16x32_bf16 v[24:27], v[56:59], v[192:195], v[24:27]\n"
        "ds_read_b64_tr_b16 v[54:55], v229 offset:58368\n"
        "v_cmp_gt_i32 s[30:31], 32, v243\n"
        "v_mfma_f32_16x16x32_bf16 v[88:91], v[56:59], v[196:199], v[88:91]\n"
        "global_store_dwordx4 v254, a[244:247], s[70:71] offset:3072 sc1\n"
        "s_add_u32 s70, s38, s97\n"
        "s_addc_u32 s71, s39, 0\n"
        "s_mov_b32 s82, -1\n"
        "v_cmp_gt_i32 s[56:57], 33, v243\n"
        "v_cmp_gt_i32 s[58:59], 34, v243\n"
        "v_mfma_f32_16x16x32_bf16 v[120:123], v[56:59], v[200:203], v[120:123]\n"
        "s_add_u32 s97, s53, 0x0\n"
        "v_cmp_gt_i32 s[60:61], 35, v243\n"
        "v_cmp_gt_i32 s[62:63], 48, v243\n"
        "v_mfma_f32_16x16x32_bf16 v[152:155], v[56:59], v[204:207], v[152:155]\n"
        "ds_read_b64_tr_b16 v[56:57], v230 offset:58368\n"
        "s_add_u32 s82, s82, 1\n"
        "v_cmp_gt_i32 s[84:85], 49, v243\n"
        "v_mfma_f32_16x16x32_bf16 v[28:31], v[60:63], v[192:195], v[28:31]\n"
        "ds_read_b64_tr_b16 v[58:59], v231 offset:58368\n"
        "v_cmp_gt_i32 s[86:87], 50, v243\n"
        "v_mfma_f32_16x16x32_bf16 v[92:95], v[60:63], v[196:199], v[92:95]\n"
        "s_cmp_ge_u32 s82, s44\n"
        "s_addc_u32 s90, s82, 0\n"
        "v_cmp_gt_i32 s[88:89], 51, v243\n"
        "v_mfma_f32_16x16x32_bf16 v[124:127], v[60:63], v[200:203], v[124:127]\n"
        "s_waitcnt lgkmcnt(4)\n"
        "v_mov_b32 v242, 0x3f803f80\n"
        "v_accvgpr_write_b32 a80, v242\n"
        "v_mfma_f32_16x16x32_bf16 v[156:159], v[60:63], v[204:207], v[156:159]\n"
        "ds_read_b64_tr_b16 v[60:61], v232 offset:58368\n"
        "s_min_u32 s90, s90, 15\n"
        "v_accvgpr_write_b32 a81, v242\n"
        "v_mfma_f32_16x16x32_bf16 v[0:3], v[32:35], v[208:211], v[0:3]\n"
        "ds_read_b64_tr_b16 v[62:63], v233 offset:58368\n"
        "s_lshl_b32 s90, s90, 15\n"
        "v_accvgpr_write_b32 a82, v242\n"
        "v_mfma_f32_16x16x32_bf16 v[64:67], v[32:35], v[212:215], v[64:67]\n"
        "s_add_u32 s68, s64, s90\n"
        "s_addc_u32 s69, s65, 0\n"
        "v_accvgpr_write_b32 a83, v242\n"
        "v_mfma_f32_16x16x32_bf16 v[96:99], v[32:35], v[216:219], v[96:99]\n"
        "s_add_u32 s74, s68, 0x1000\n"
        "s_addc_u32 s75, s69, 0\n"
        "v_mov_b32 v242, 0xff800000\n"
        "v_mfma_f32_16x16x32_bf16 v[128:131], v[32:35], v[220:223], v[128:131]\n"
        "s_add_u32 s76, s66, s90\n"
        "s_addc_u32 s77, s67, 0\n"
        "v_mfma_f32_16x16x32_bf16 v[4:7], v[36:39], v[208:211], v[4:7]\n"
        "v_mfma_f32_16x16x32_bf16 v[68:71], v[36:39], v[212:215], v[68:71]\n"
        "v_mfma_f32_16x16x32_bf16 v[100:103], v[36:39], v[216:219], v[100:103]\n"
        "v_mfma_f32_16x16x32_bf16 v[132:135], v[36:39], v[220:223], v[132:135]\n"
        "v_mfma_f32_16x16x32_bf16 v[8:11], v[40:43], v[208:211], v[8:11]\n"
        "s_waitcnt vmcnt(14) lgkmcnt(0)\n"
        "s_barrier\n"
        "v_mfma_f32_16x16x32_bf16 v[72:75], v[40:43], v[212:215], v[72:75]\n"
        "ds_read_b64_tr_b16 v[32:33], v226 offset:16384\n"
        "v_mfma_f32_16x16x32_bf16 v[104:107], v[40:43], v[216:219], v[104:107]\n"
        "ds_read_b64_tr_b16 v[34:35], v227 offset:16384\n"
        "v_mfma_f32_16x16x32_bf16 v[136:139], v[40:43], v[220:223], v[136:139]\n"
        "ds_read_b64_tr_b16 v[36:37], v228 offset:16384\n"
        "v_mfma_f32_16x16x32_bf16 v[12:15], v[44:47], v[208:211], v[12:15]\n"
        "ds_read_b64_tr_b16 v[38:39], v229 offset:16384\n"
        "v_mfma_f32_16x16x32_bf16 v[76:79], v[44:47], v[212:215], v[76:79]\n"
        "ds_read_b64_tr_b16 v[40:41], v230 offset:16384\n"
        "v_mfma_f32_16x16x32_bf16 v[108:111], v[44:47], v[216:219], v[108:111]\n"
        "ds_read_b64_tr_b16 v[42:43], v231 offset:16384\n"
        "v_mfma_f32_16x16x32_bf16 v[140:143], v[44:47], v[220:223], v[140:143]\n"
        "ds_read_b128 v[192:195], v246 offset:34816\n"
        "v_mfma_f32_16x16x32_bf16 v[16:19], v[48:51], v[208:211], v[16:19]\n"
        "ds_read_b64_tr_b16 v[44:45], v232 offset:16384\n"
        "v_mfma_f32_16x16x32_bf16 v[80:83], v[48:51], v[212:215], v[80:83]\n"
        "ds_read_b64_tr_b16 v[46:47], v233 offset:16384\n"
        "v_mfma_f32_16x16x32_bf16 v[112:115], v[48:51], v[216:219], v[112:115]\n"
        "ds_read_b128 v[196:199], v246 offset:32768\n"
        "v_mfma_f32_16x16x32_bf16 v[144:147], v[48:51], v[220:223], v[144:147]\n"
        "ds_read_b64_tr_b16 v[48:49], v226 offset:17408\n"
        "v_mfma_f32_16x16x32_bf16 v[20:23], v[52:55], v[208:211], v[20:23]\n"
        "ds_read_b64_tr_b16 v[50:51], v227 offset:17408\n"
        "v_mfma_f32_16x16x32_bf16 v[84:87], v[52:55], v[212:215], v[84:87]\n"
        "ds_read_b128 v[200:203], v246 offset:38912\n"
        "v_mfma_f32_16x16x32_bf16 v[116:119], v[52:55], v[216:219], v[116:119]\n"
        "ds_read_b128 v[204:207], v246 offset:36864\n"
        "v_mfma_f32_16x16x32_bf16 v[148:151], v[52:55], v[220:223], v[148:151]\n"
        "ds_read_b64_tr_b16 v[52:53], v228 offset:17408\n"
        "v_mfma_f32_16x16x32_bf16 v[24:27], v[56:59], v[208:211], v[24:27]\n"
        "ds_read_b64_tr_b16 v[54:55], v229 offset:17408\n"
        "v_mfma_f32_16x16x32_bf16 v[88:91], v[56:59], v[212:215], v[88:91]\n"
        "v_mfma_f32_16x16x32_bf16 v[120:123], v[56:59], v[216:219], v[120:123]\n"
        "s_waitcnt lgkmcnt(6)\n"
        "v_mfma_f32_16x16x32_bf16 v[152:155], v[56:59], v[220:223], v[152:155]\n"
        "ds_read_b64_tr_b16 v[56:57], v230 offset:17408\n"
        "v_mfma_f32_16x16x32_bf16 v[28:31], v[60:63], v[208:211], v[28:31]\n"
        "ds_read_b64_tr_b16 v[58:59], v231 offset:17408\n"
        "s_waitcnt lgkmcnt(6)\n"
        "v_mfma_f32_16x16x32_bf16 v[92:95], v[60:63], v[212:215], v[92:95]\n"
        "global_store_dwordx4 v254, a[216:219], s[70:71] offset:0 sc1\n"
        "ds_read_b128 v[208:211], v247 offset:34816\n"
        "ds_read_b128 v[212:215], v247 offset:32768\n"
        "s_waitcnt lgkmcnt(6)\n"
        "v_mfma_f32_16x16x32_bf16 v[124:127], v[60:63], v[216:219], v[124:127]\n"
        "ds_read_b128 v[216:219], v247 offset:38912\n"
        "v_mfma_f32_16x16x32_bf16 v[156:159], v[60:63], v[220:223], v[156:159]\n"
        "ds_read_b64_tr_b16 v[60:61], v232 offset:17408\n"
        "v_mfma_f32_16x16x32_bf16 v[0:3], v[32:35], v[192:195], v[0:3]\n"
        "ds_read_b64_tr_b16 v[62:63], v233 offset:17408\n"
        "v_mfma_f32_16x16x32_bf16 v[64:67], v[32:35], v[196:199], v[64:67]\n"
        "ds_read_b128 v[220:223], v247 offset:36864\n"
        "v_mfma_f32_16x16x32_bf16 v[96:99], v[32:35], v[200:203], v[96:99]\n"
        "v_mfma_f32_16x16x32_bf16 v[128:131], v[32:35], v[204:207], v[128:131]\n"
        "ds_read_b64_tr_b16 v[32:33], v226 offset:24576\n"
        "v_mfma_f32_16x16x32_bf16 v[4:7], v[36:39], v[192:195], v[4:7]\n"
        "ds_read_b64_tr_b16 v[34:35], v227 offset:24576\n"
        "v_mfma_f32_16x16x32_bf16 v[68:71], v[36:39], v[196:199], v[68:71]\n"
        "v_mfma_f32_16x16x32_bf16 v[100:103], v[36:39], v[200:203], v[100:103]\n"
        "v_mfma_f32_16x16x32_bf16 v[132:135], v[36:39], v[204:207], v[132:135]\n"
        "ds_read_b64_tr_b16 v[36:37], v228 offset:24576\n"
        "v_mfma_f32_16x16x32_bf16 v[8:11], v[40:43], v[192:195], v[8:11]\n"
        "ds_read_b64_tr_b16 v[38:39], v229 offset:24576\n"
        "v_mfma_f32_16x16x32_bf16 v[72:75], v[40:43], v[196:199], v[72:75]\n"
        "v_mfma_f32_16x16x32_bf16 v[104:107], v[40:43], v[200:203], v[104:107]\n"
        "v_mfma_f32_16x16x32_bf16 v[136:139], v[40:43], v[204:207], v[136:139]\n"
        "ds_read_b64_tr_b16 v[40:41], v230 offset:24576\n"
        "v_mfma_f32_16x16x32_bf16 v[12:15], v[44:47], v[192:195], v[12:15]\n"
        "ds_read_b64_tr_b16 v[42:43], v231 offset:24576\n"
        "v_mfma_f32_16x16x32_bf16 v[76:79], v[44:47], v[196:199], v[76:79]\n"
        "v_mfma_f32_16x16x32_bf16 v[108:111], v[44:47], v[200:203], v[108:111]\n"
        "s_waitcnt lgkmcnt(4)\n"
        "v_mfma_f32_16x16x32_bf16 v[140:143], v[44:47], v[204:207], v[140:143]\n"
        "ds_read_b64_tr_b16 v[44:45], v232 offset:24576\n"
        "v_mfma_f32_16x16x32_bf16 v[16:19], v[48:51], v[192:195], v[16:19]\n"
        "ds_read_b64_tr_b16 v[46:47], v233 offset:24576\n"
        "v_mfma_f32_16x16x32_bf16 v[80:83], v[48:51], v[196:199], v[80:83]\n"
        "s_lshl_b32 s80, s41, 5\n"
        "s_add_u32 s80, s80, 256\n"
        "s_add_u32 s81, s80, s42\n"
        "v_mfma_f32_16x16x32_bf16 v[112:115], v[48:51], v[200:203], v[112:115]\n"
        "s_lshl_b32 s80, s80, 7\n"
        "v_mfma_f32_16x16x32_bf16 v[144:147], v[48:51], v[204:207], v[144:147]\n"
        "ds_read_b64_tr_b16 v[48:49], v226 offset:25600\n"
        "v_mfma_f32_16x16x32_bf16 v[20:23], v[52:55], v[192:195], v[20:23]\n"
        "ds_read_b64_tr_b16 v[50:51], v227 offset:25600\n"
        "s_lshl_b32 s81, s81, 7\n"
        "v_mfma_f32_16x16x32_bf16 v[84:87], v[52:55], v[196:199], v[84:87]\n"
        "v_mfma_f32_16x16x32_bf16 v[116:119], v[52:55], v[200:203], v[116:119]\n"
        "v_mfma_f32_16x16x32_bf16 v[148:151], v[52:55], v[204:207], v[148:151]\n"
        "ds_read_b64_tr_b16 v[52:53], v228 offset:25600\n"
        "v_mfma_f32_16x16x32_bf16 v[24:27], v[56:59], v[192:195], v[24:27]\n"
        "ds_read_b64_tr_b16 v[54:55], v229 offset:25600\n"
        "v_mfma_f32_16x16x32_bf16 v[88:91], v[56:59], v[196:199], v[88:91]\n"
        "v_mfma_f32_16x16x32_bf16 v[120:123], v[56:59], v[200:203], v[120:123]\n"
        "v_mfma_f32_16x16x32_bf16 v[152:155], v[56:59], v[204:207], v[152:155]\n"
        "ds_read_b64_tr_b16 v[56:57], v230 offset:25600\n"
        "v_mfma_f32_16x16x32_bf16 v[28:31], v[60:63], v[192:195], v[28:31]\n"
        "ds_read_b64_tr_b16 v[58:59], v231 offset:25600\n"
        "v_mfma_f32_16x16x32_bf16 v[92:95], v[60:63], v[196:199], v[92:95]\n"
        "v_mfma_f32_16x16x32_bf16 v[124:127], v[60:63], v[200:203], v[124:127]\n"
        "s_waitcnt lgkmcnt(4)\n"
        "v_mfma_f32_16x16x32_bf16 v[156:159], v[60:63], v[204:207], v[156:159]\n"
        "ds_read_b64_tr_b16 v[60:61], v232 offset:25600\n"
        "v_mfma_f32_16x16x32_bf16 v[0:3], v[32:35], v[208:211], v[0:3]\n"
        "ds_read_b64_tr_b16 v[62:63], v233 offset:25600\n"
        "v_mfma_f32_16x16x32_bf16 v[64:67], v[32:35], v[212:215], v[64:67]\n"
        "v_mfma_f32_16x16x32_bf16 v[96:99], v[32:35], v[216:219], v[96:99]\n"
        "v_mfma_f32_16x16x32_bf16 v[128:131], v[32:35], v[220:223], v[128:131]\n"
        "v_mfma_f32_16x16x32_bf16 v[4:7], v[36:39], v[208:211], v[4:7]\n"
        "v_mfma_f32_16x16x32_bf16 v[68:71], v[36:39], v[212:215], v[68:71]\n"
        "v_mfma_f32_16x16x32_bf16 v[100:103], v[36:39], v[216:219], v[100:103]\n"
        "v_mfma_f32_16x16x32_bf16 v[132:135], v[36:39], v[220:223], v[132:135]\n"
        "global_store_dwordx4 v254, a[248:251], s[70:71] offset:1024 sc1\n"
        "s_waitcnt vmcnt(3) lgkmcnt(0)\n"
        "s_barrier\n"
        "v_mfma_f32_16x16x32_bf16 v[8:11], v[40:43], v[208:211], v[8:11]\n"
        "ds_write_b128 v244, a[224:227] offset:12288\n"
        "v_mfma_f32_16x16x32_bf16 v[72:75], v[40:43], v[212:215], v[72:75]\n"
        "ds_read_b64_tr_b16 v[32:33], v226 offset:32768\n"
        "v_mfma_f32_16x16x32_bf16 v[104:107], v[40:43], v[216:219], v[104:107]\n"
        "ds_read_b64_tr_b16 v[34:35], v227 offset:32768\n"
        "v_mfma_f32_16x16x32_bf16 v[136:139], v[40:43], v[220:223], v[136:139]\n"
        "ds_read_b64_tr_b16 v[36:37], v228 offset:32768\n"
        "v_mfma_f32_16x16x32_bf16 v[12:15], v[44:47], v[208:211], v[12:15]\n"
        "ds_read_b64_tr_b16 v[38:39], v229 offset:32768\n"
        "v_mfma_f32_16x16x32_bf16 v[76:79], v[44:47], v[212:215], v[76:79]\n"
        "ds_write_b128 v244, a[228:231] offset:13312\n"
        "v_mfma_f32_16x16x32_bf16 v[108:111], v[44:47], v[216:219], v[108:111]\n"
        "ds_read_b64_tr_b16 v[40:41], v230 offset:32768\n"
        "v_mfma_f32_16x16x32_bf16 v[140:143], v[44:47], v[220:223], v[140:143]\n"
        "ds_read_b64_tr_b16 v[42:43], v231 offset:32768\n"
        "v_mfma_f32_16x16x32_bf16 v[16:19], v[48:51], v[208:211], v[16:19]\n"
        "ds_read_b128 v[192:195], v246 offset:2048\n"
        "v_mfma_f32_16x16x32_bf16 v[80:83], v[48:51], v[212:215], v[80:83]\n"
        "ds_write_b128 v244, a[232:235] offset:8192\n"
        "v_mfma_f32_16x16x32_bf16 v[112:115], v[48:51], v[216:219], v[112:115]\n"
        "ds_read_b64_tr_b16 v[44:45], v232 offset:32768\n"
        "v_mfma_f32_16x16x32_bf16 v[144:147], v[48:51], v[220:223], v[144:147]\n"
        "ds_read_b64_tr_b16 v[46:47], v233 offset:32768\n"
        "v_mfma_f32_16x16x32_bf16 v[20:23], v[52:55], v[208:211], v[20:23]\n"
        "ds_read_b128 v[196:199], v246 offset:0\n"
        "v_mfma_f32_16x16x32_bf16 v[84:87], v[52:55], v[212:215], v[84:87]\n"
        "ds_write_b128 v244, a[236:239] offset:9216\n"
        "v_mfma_f32_16x16x32_bf16 v[116:119], v[52:55], v[216:219], v[116:119]\n"
        "ds_read_b64_tr_b16 v[48:49], v226 offset:33792\n"
        "v_mfma_f32_16x16x32_bf16 v[148:151], v[52:55], v[220:223], v[148:151]\n"
        "ds_read_b64_tr_b16 v[50:51], v227 offset:33792\n"
        "v_mfma_f32_16x16x32_bf16 v[24:27], v[56:59], v[208:211], v[24:27]\n"
        "ds_read_b128 v[200:203], v246 offset:6144\n"
        "v_mfma_f32_16x16x32_bf16 v[88:91], v[56:59], v[212:215], v[88:91]\n"
        "ds_write_b128 v244, a[192:195] offset:4096\n"
        "v_mfma_f32_16x16x32_bf16 v[120:123], v[56:59], v[216:219], v[120:123]\n"
        "ds_write_b128 v244, a[196:199] offset:5120\n"
        "v_mfma_f32_16x16x32_bf16 v[152:155], v[56:59], v[220:223], v[152:155]\n"
        "ds_read_b64_tr_b16 v[52:53], v228 offset:33792\n"
        "s_waitcnt lgkmcnt(6)\n"
        "v_mfma_f32_16x16x32_bf16 v[28:31], v[60:63], v[208:211], v[28:31]\n"
        "ds_read_b64_tr_b16 v[54:55], v229 offset:33792\n"
        "v_mfma_f32_16x16x32_bf16 v[92:95], v[60:63], v[212:215], v[92:95]\n"
        "ds_read_b128 v[204:207], v246 offset:4096\n"
        "s_waitcnt lgkmcnt(7)\n"
        "v_mfma_f32_16x16x32_bf16 v[124:127], v[60:63], v[216:219], v[124:127]\n"
        "ds_write_b128 v244, a[200:203] offset:16384\n"
        "v_mfma_f32_16x16x32_bf16 v[156:159], v[60:63], v[220:223], v[156:159]\n"
        "ds_read_b64_tr_b16 v[56:57], v230 offset:33792\n"
        "s_waitcnt lgkmcnt(6)\n"
        "v_mfma_f32_16x16x32_bf16 v[0:3], v[32:35], v[192:195], v[0:3]\n"
        "ds_read_b64_tr_b16 v[58:59], v231 offset:33792\n"
        "v_mfma_f32_16x16x32_bf16 v[64:67], v[32:35], v[196:199], v[64:67]\n"
        "ds_write_b128 v244, a[204:207] offset:17408\n"
        "v_mfma_f32_16x16x32_bf16 v[96:99], v[32:35], v[200:203], v[96:99]\n"
        "ds_read_b64_tr_b16 v[60:61], v232 offset:33792\n"
        "s_waitcnt lgkmcnt(7)\n"
        "v_mfma_f32_16x16x32_bf16 v[4:7], v[36:39], v[192:195], v[4:7]\n"
        "ds_read_b64_tr_b16 v[62:63], v233 offset:33792\n"
        "s_waitcnt lgkmcnt(6)\n"
        "v_mfma_f32_16x16x32_bf16 v[128:131], v[32:35], v[204:207], v[128:131]\n"
        "ds_write_b128 v244, a[160:163] offset:20480\n"
        "v_mfma_f32_16x16x32_bf16 v[68:71], v[36:39], v[196:199], v[68:71]\n"
        "ds_write_b128 v244, a[164:167] offset:21504\n"
        "v_mfma_f32_16x16x32_bf16 v[100:103], v[36:39], v[200:203], v[100:103]\n"
        "ds_read_b64_tr_b16 v[32:33], v226 offset:40960\n"
        "v_mfma_f32_16x16x32_bf16 v[132:135], v[36:39], v[204:207], v[132:135]\n"
        "ds_read_b64_tr_b16 v[34:35], v227 offset:40960\n"
        "v_mfma_f32_16x16x32_bf16 v[8:11], v[40:43], v[192:195], v[8:11]\n"
        "ds_write_b128 v244, a[168:171] offset:24576\n"
        "v_mfma_f32_16x16x32_bf16 v[72:75], v[40:43], v[196:199], v[72:75]\n"
        "ds_read_b64_tr_b16 v[36:37], v228 offset:40960\n"
        "v_mfma_f32_16x16x32_bf16 v[104:107], v[40:43], v[200:203], v[104:107]\n"
        "ds_read_b64_tr_b16 v[38:39], v229 offset:40960\n"
        "v_mfma_f32_16x16x32_bf16 v[136:139], v[40:43], v[204:207], v[136:139]\n"
        "ds_write_b128 v244, a[172:175] offset:25600\n"
        "v_mfma_f32_16x16x32_bf16 v[12:15], v[44:47], v[192:195], v[12:15]\n"
        "ds_read_b64_tr_b16 v[40:41], v230 offset:40960\n"
        "v_mfma_f32_16x16x32_bf16 v[76:79], v[44:47], v[196:199], v[76:79]\n"
        "ds_read_b64_tr_b16 v[42:43], v231 offset:40960\n"
        "v_mfma_f32_16x16x32_bf16 v[108:111], v[44:47], v[200:203], v[108:111]\n"
        "ds_read_b128 v[208:211], v247 offset:2048\n"
        "v_mfma_f32_16x16x32_bf16 v[140:143], v[44:47], v[204:207], v[140:143]\n"
        "ds_write_b128 v244, a[0:3] offset:28672\n"
        "v_mfma_f32_16x16x32_bf16 v[16:19], v[48:51], v[192:195], v[16:19]\n"
        "ds_write_b128 v244, a[4:7] offset:29696\n"
        "v_mfma_f32_16x16x32_bf16 v[80:83], v[48:51], v[196:199], v[80:83]\n"
        "ds_read_b64_tr_b16 v[44:45], v232 offset:40960\n"
        "v_mfma_f32_16x16x32_bf16 v[112:115], v[48:51], v[200:203], v[112:115]\n"
        "ds_read_b64_tr_b16 v[46:47], v233 offset:40960\n"
        "s_waitcnt lgkmcnt(7)\n"
        "v_mfma_f32_16x16x32_bf16 v[144:147], v[48:51], v[204:207], v[144:147]\n"
        "ds_read_b128 v[212:215], v247 offset:0\n"
        "v_mfma_f32_16x16x32_bf16 v[20:23], v[52:55], v[192:195], v[20:23]\n"
        "ds_write_b128 v244, a[8:11] offset:0\n"
        "v_mfma_f32_16x16x32_bf16 v[84:87], v[52:55], v[196:199], v[84:87]\n"
        "ds_read_b64_tr_b16 v[48:49], v226 offset:41984\n"
        "v_mfma_f32_16x16x32_bf16 v[116:119], v[52:55], v[200:203], v[116:119]\n"
        "ds_read_b64_tr_b16 v[50:51], v227 offset:41984\n"
        "v_mfma_f32_16x16x32_bf16 v[148:151], v[52:55], v[204:207], v[148:151]\n"
        "ds_read_b128 v[216:219], v247 offset:6144\n"
        "v_mfma_f32_16x16x32_bf16 v[24:27], v[56:59], v[192:195], v[24:27]\n"
        "global_store_dwordx4 v254, a[220:223], s[70:71] offset:2048 sc1\n"
        "ds_read_b64_tr_b16 v[52:53], v228 offset:41984\n"
        "ds_read_b64_tr_b16 v[54:55], v229 offset:41984\n"
        "v_mfma_f32_16x16x32_bf16 v[88:91], v[56:59], v[196:199], v[88:91]\n"
        "ds_write_b128 v244, a[12:15] offset:1024\n"
        "v_mfma_f32_16x16x32_bf16 v[120:123], v[56:59], v[200:203], v[120:123]\n"
        "ds_read_b128 v[220:223], v247 offset:4096\n"
        "v_lshrrev_b32 v244, 1, v254\n"
        "v_mfma_f32_16x16x32_bf16 v[152:155], v[56:59], v[204:207], v[152:155]\n"
        "ds_read_b64_tr_b16 v[56:57], v230 offset:41984\n"
        "v_add_u32 v244, s81, v244\n"
        "v_mfma_f32_16x16x32_bf16 v[28:31], v[60:63], v[192:195], v[28:31]\n"
        "ds_read_b64_tr_b16 v[58:59], v231 offset:41984\n"
        "s_add_u32 s81, s78, 4096\n"
        "s_waitcnt lgkmcnt(7)\n"
        "v_mfma_f32_16x16x32_bf16 v[92:95], v[60:63], v[196:199], v[92:95]\n"
        "s_cmp_eq_u32 s82, s83\n"
        "s_cselect_b64 s[74:75], s[94:95], s[74:75]\n"
        "s_cselect_b32 s81, s51, s81\n"
        "v_mfma_f32_16x16x32_bf16 v[124:127], v[60:63], v[200:203], v[124:127]\n"
        "v_mfma_f32_16x16x32_bf16 v[156:159], v[60:63], v[204:207], v[156:159]\n"
        "ds_read_b64_tr_b16 v[60:61], v232 offset:41984\n"
        "s_waitcnt lgkmcnt(5)\n"
        "v_mfma_f32_16x16x32_bf16 v[0:3], v[32:35], v[208:211], v[0:3]\n"
        "ds_read_b64_tr_b16 v[62:63], v233 offset:41984\n"
        "v_mfma_f32_16x16x32_bf16 v[4:7], v[36:39], v[208:211], v[4:7]\n"
        "v_mfma_f32_16x16x32_bf16 v[8:11], v[40:43], v[208:211], v[8:11]\n"
        "v_mfma_f32_16x16x32_bf16 v[12:15], v[44:47], v[208:211], v[12:15]\n"
        "s_waitcnt lgkmcnt(3)\n"
        "v_mfma_f32_16x16x32_bf16 v[16:19], v[48:51], v[208:211], v[16:19]\n"
        "s_waitcnt lgkmcnt(2)\n"
        "v_mfma_f32_16x16x32_bf16 v[20:23], v[52:55], v[208:211], v[20:23]\n"
        "v_mul_f32 v0, 0x3e0293ee, v0\n"
        "v_mul_f32 v1, 0x3e0293ee, v1\n"
        "v_mfma_f32_16x16x32_bf16 v[24:27], v[56:59], v[208:211], v[24:27]\n"
        "v_mul_f32 v2, 0x3e0293ee, v2\n"
        "v_mul_f32 v3, 0x3e0293ee, v3\n"
        "v_mfma_f32_16x16x32_bf16 v[64:67], v[32:35], v[212:215], v[64:67]\n"
        "s_waitcnt lgkmcnt(1)\n"
        "v_mul_f32 v4, 0x3e0293ee, v4\n"
        "v_mul_f32 v5, 0x3e0293ee, v5\n"
        "v_mfma_f32_16x16x32_bf16 v[68:71], v[36:39], v[212:215], v[68:71]\n"
        "s_waitcnt lgkmcnt(0)\n"
        "s_barrier\n"
        "v_mul_f32 v6, 0x3e0293ee, v6\n"
        "v_mfma_f32_16x16x32_bf16 v[28:31], v[60:63], v[208:211], v[28:31]\n"
        "v_mul_f32 v7, 0x3e0293ee, v7\n"
        "v_cvt_pk_bf16_f32 v0, v0, v1\n"
        "v_mfma_f32_16x16x32_bf16 v[72:75], v[40:43], v[212:215], v[72:75]\n"
        "v_cvt_pk_bf16_f32 v1, v2, v3\n"
        "v_cvt_pk_bf16_f32 v2, v4, v5\n"
        "v_mfma_f32_16x16x32_bf16 v[76:79], v[44:47], v[212:215], v[76:79]\n"
        "v_cvt_pk_bf16_f32 v3, v6, v7\n"
        "v_mul_f32 v8, 0x3e0293ee, v8\n"
        "v_mfma_f32_16x16x32_bf16 v[80:83], v[48:51], v[212:215], v[80:83]\n"
        "v_mul_f32 v9, 0x3e0293ee, v9\n"
        "v_mul_f32 v10, 0x3e0293ee, v10\n"
        "v_mfma_f32_16x16x32_bf16 v[84:87], v[52:55], v[212:215], v[84:87]\n"
        "v_mul_f32 v11, 0x3e0293ee, v11\n"
        "v_mul_f32 v12, 0x3e0293ee, v12\n"
        "v_mfma_f32_16x16x32_bf16 v[88:91], v[56:59], v[212:215], v[88:91]\n"
        "v_mul_f32 v13, 0x3e0293ee, v13\n"
        "v_mul_f32 v14, 0x3e0293ee, v14\n"
        "v_mfma_f32_16x16x32_bf16 v[92:95], v[60:63], v[212:215], v[92:95]\n"
        "v_mul_f32 v15, 0x3e0293ee, v15\n"
        "v_cvt_pk_bf16_f32 v4, v8, v9\n"
        "v_mfma_f32_16x16x32_bf16 v[96:99], v[32:35], v[216:219], v[96:99]\n"
        "v_cvt_pk_bf16_f32 v5, v10, v11\n"
        "v_cvt_pk_bf16_f32 v6, v12, v13\n"
        "v_mfma_f32_16x16x32_bf16 v[100:103], v[36:39], v[216:219], v[100:103]\n"
        "v_cvt_pk_bf16_f32 v7, v14, v15\n"
        "v_mul_f32 v16, 0x3e0293ee, v16\n"
        "v_mfma_f32_16x16x32_bf16 v[104:107], v[40:43], v[216:219], v[104:107]\n"
        "v_mul_f32 v17, 0x3e0293ee, v17\n"
        "v_mul_f32 v18, 0x3e0293ee, v18\n"
        "v_mfma_f32_16x16x32_bf16 v[108:111], v[44:47], v[216:219], v[108:111]\n"
        "v_mul_f32 v19, 0x3e0293ee, v19\n"
        "v_mul_f32 v20, 0x3e0293ee, v20\n"
        "v_mfma_f32_16x16x32_bf16 v[112:115], v[48:51], v[216:219], v[112:115]\n"
        "v_mul_f32 v21, 0x3e0293ee, v21\n"
        "v_mul_f32 v22, 0x3e0293ee, v22\n"
        "v_mfma_f32_16x16x32_bf16 v[116:119], v[52:55], v[216:219], v[116:119]\n"
        "v_mul_f32 v23, 0x3e0293ee, v23\n"
        "v_cvt_pk_bf16_f32 v8, v16, v17\n"
        "v_mfma_f32_16x16x32_bf16 v[120:123], v[56:59], v[216:219], v[120:123]\n"
        "v_cvt_pk_bf16_f32 v9, v18, v19\n"
        "v_cvt_pk_bf16_f32 v10, v20, v21\n"
        "v_mfma_f32_16x16x32_bf16 v[124:127], v[60:63], v[216:219], v[124:127]\n"
        "v_cvt_pk_bf16_f32 v11, v22, v23\n"
        "v_mul_f32 v24, 0x3e0293ee, v24\n"
        "v_mul_f32 v25, 0x3e0293ee, v25\n"
        "v_mul_f32 v26, 0x3e0293ee, v26\n"
        "v_mul_f32 v27, 0x3e0293ee, v27\n"
        "v_mul_f32 v28, 0x3e0293ee, v28\n"
        "v_mul_f32 v29, 0x3e0293ee, v29\n"
        "v_mul_f32 v30, 0x3e0293ee, v30\n"
        "v_mul_f32 v31, 0x3e0293ee, v31\n"
        "v_cvt_pk_bf16_f32 v12, v24, v25\n"
        "v_cvt_pk_bf16_f32 v13, v26, v27\n"
        "v_cvt_pk_bf16_f32 v14, v28, v29\n"
        "v_cvt_pk_bf16_f32 v15, v30, v31\n"
        "v_mfma_f32_16x16x32_bf16 v[128:131], v[32:35], v[220:223], v[128:131]\n"
        "s_barrier\n"
        "ds_read_b128 a[96:99], v240 offset:0\n"
        "v_mfma_f32_16x16x32_bf16 v[132:135], v[36:39], v[220:223], v[132:135]\n"
        "ds_read_b128 a[100:103], v240 offset:1024\n"
        "v_mul_f32 v64, 0x3e0293ee, v64\n"
        "v_mfma_f32_16x16x32_bf16 v[136:139], v[40:43], v[220:223], v[136:139]\n"
        "ds_read_b128 a[104:107], v240 offset:2048\n"
        "v_mul_f32 v65, 0x3e0293ee, v65\n"
        "v_mfma_f32_16x16x32_bf16 v[140:143], v[44:47], v[220:223], v[140:143]\n"
        "ds_read_b128 a[108:111], v240 offset:3072\n"
        "v_mul_f32 v66, 0x3e0293ee, v66\n"
        "v_mfma_f32_16x16x32_bf16 v[144:147], v[48:51], v[220:223], v[144:147]\n"
        "ds_read_b128 a[112:115], v240 offset:4096\n"
        "v_mul_f32 v67, 0x3e0293ee, v67\n"
        "v_mfma_f32_16x16x32_bf16 v[148:151], v[52:55], v[220:223], v[148:151]\n"
        "ds_read_b128 a[116:119], v240 offset:5120\n"
        "v_mul_f32 v68, 0x3e0293ee, v68\n"
        "v_mfma_f32_16x16x32_bf16 v[152:155], v[56:59], v[220:223], v[152:155]\n"
        "v_mul_f32 v69, 0x3e0293ee, v69\n"
        "v_mul_f32 v70, 0x3e0293ee, v70\n"
        "v_mfma_f32_16x16x32_bf16 v[156:159], v[60:63], v[220:223], v[156:159]\n"
        "ds_write_b128 v245, a[208:211] offset:8192\n"
        "v_mul_f32 v71, 0x3e0293ee, v71\n"
        "v_cvt_pk_bf16_f32 v64, v64, v65\n"
        "v_cvt_pk_bf16_f32 v65, v66, v67\n"
        "v_cvt_pk_bf16_f32 v66, v68, v69\n"
        "v_cvt_pk_bf16_f32 v67, v70, v71\n"
        "v_mul_f32 v72, 0x3e0293ee, v72\n"
        "v_mul_f32 v73, 0x3e0293ee, v73\n"
        "v_mul_f32 v74, 0x3e0293ee, v74\n"
        "v_mul_f32 v75, 0x3e0293ee, v75\n"
        "v_mul_f32 v76, 0x3e0293ee, v76\n"
        "v_mul_f32 v77, 0x3e0293ee, v77\n"
        "v_mul_f32 v78, 0x3e0293ee, v78\n"
        "v_mul_f32 v79, 0x3e0293ee, v79\n"
        "v_cvt_pk_bf16_f32 v68, v72, v73\n"
        "v_cvt_pk_bf16_f32 v69, v74, v75\n"
        "v_cvt_pk_bf16_f32 v70, v76, v77\n"
        "v_cvt_pk_bf16_f32 v71, v78, v79\n"
        "v_mul_f32 v80, 0x3e0293ee, v80\n"
        "v_mul_f32 v81, 0x3e0293ee, v81\n"
        "v_mul_f32 v82, 0x3e0293ee, v82\n"
        "v_mul_f32 v83, 0x3e0293ee, v83\n"
        "v_mul_f32 v84, 0x3e0293ee, v84\n"
        "v_mul_f32 v85, 0x3e0293ee, v85\n"
        "v_mul_f32 v86, 0x3e0293ee, v86\n"
        "v_mul_f32 v87, 0x3e0293ee, v87\n"
        "v_cvt_pk_bf16_f32 v72, v80, v81\n"
        "v_cvt_pk_bf16_f32 v73, v82, v83\n"
        "v_cvt_pk_bf16_f32 v74, v84, v85\n"
        "v_cvt_pk_bf16_f32 v75, v86, v87\n"
        "v_mul_f32 v88, 0x3e0293ee, v88\n"
        "v_mul_f32 v89, 0x3e0293ee, v89\n"
        "v_mul_f32 v90, 0x3e0293ee, v90\n"
        "v_mul_f32 v91, 0x3e0293ee, v91\n"
        "v_mul_f32 v92, 0x3e0293ee, v92\n"
        "v_mul_f32 v93, 0x3e0293ee, v93\n"
        "v_mul_f32 v94, 0x3e0293ee, v94\n"
        "v_mul_f32 v95, 0x3e0293ee, v95\n"
        "v_cvt_pk_bf16_f32 v76, v88, v89\n"
        "v_cvt_pk_bf16_f32 v77, v90, v91\n"
        "v_cvt_pk_bf16_f32 v78, v92, v93\n"
        "v_cvt_pk_bf16_f32 v79, v94, v95\n"
        "ds_write_b128 v245, a[240:243] offset:9216\n"
        "ds_write_b128 v245, a[212:215] offset:10240\n"
        "ds_write_b128 v245, a[244:247] offset:11264\n"
        "ds_write_b128 v245, a[216:219] offset:0\n"
        "ds_write_b128 v245, a[248:251] offset:1024\n"
        "ds_write_b128 v245, a[220:223] offset:2048\n"
        "ds_write_b128 v245, a[252:255] offset:3072\n"
        "ds_write_b128 v245, a[176:179] offset:16384\n"
        "ds_write_b128 v245, a[180:183] offset:17408\n"
        "ds_write_b128 v245, a[184:187] offset:18432\n"
        "ds_write_b128 v245, a[188:191] offset:19456\n"
        "ds_write_b128 v245, a[16:19] offset:24576\n"
        "ds_write_b128 v245, a[20:23] offset:25600\n"
        "ds_write_b128 v245, a[24:27] offset:26624\n"
        "ds_write_b128 v245, a[28:31] offset:27648\n"
        "ds_read_b128 a[120:123], v240 offset:6144\n"
        "ds_read_b128 a[124:127], v240 offset:7168\n"
        "global_store_dwordx4 v254, a[252:255], s[70:71] offset:3072 sc1\n"
        "s_add_u32 s70, s38, s97\n"
        "s_addc_u32 s71, s39, 0\n"
        "v_cndmask_b32 v32, 0, v242, s[10:11]\n"
        "v_cndmask_b32 v33, 0, v242, s[12:13]\n"
        "v_cndmask_b32 v34, 0, v242, s[14:15]\n"
        "v_cndmask_b32 v35, 0, v242, s[16:17]\n"
        "s_waitcnt lgkmcnt(5)\n"
        "s_nop 0\n"
        "v_mfma_f32_16x16x32_bf16 v[32:35], a[96:99], v[0:3], v[32:35]\n"
        "ds_read_b128 a[96:99], v240 offset:8192\n"
        "s_add_u32 s97, s53, 0x2000\n"
        "v_cndmask_b32 v48, 0, v242, s[20:21]\n"
        "v_mfma_f32_16x16x32_bf16 v[32:35], a[100:103], v[4:7], v[32:35]\n"
        "ds_read_b128 a[100:103], v240 offset:9216\n"
        "v_cndmask_b32 v49, 0, v242, s[22:23]\n"
        "v_mfma_f32_16x16x32_bf16 v[32:35], a[104:107], v[8:11], v[32:35]\n"
        "global_store_dwordx4 v254, a[176:179], s[70:71] offset:0 sc1\n"
        "ds_read_b128 a[104:107], v240 offset:10240\n"
        "v_cndmask_b32 v50, 0, v242, s[24:25]\n"
        "v_cndmask_b32 v51, 0, v242, s[28:29]\n"
        "s_waitcnt lgkmcnt(5)\n"
        "v_mfma_f32_16x16x32_bf16 v[32:35], a[108:111], v[12:15], v[32:35]\n"
        "ds_read_b128 a[108:111], v240 offset:11264\n"
        "s_barrier\n"
        "v_cndmask_b32 v36, 0, v242, s[10:11]\n"
        "v_mfma_f32_16x16x32_bf16 v[48:51], a[112:115], v[0:3], v[48:51]\n"
        "ds_read_b128 a[112:115], v240 offset:12288\n"
        "v_cndmask_b32 v37, 0, v242, s[12:13]\n"
        "v_mfma_f32_16x16x32_bf16 v[48:51], a[116:119], v[4:7], v[48:51]\n"
        "ds_read_b128 a[144:147], v241 offset:0\n"
        "s_waitcnt lgkmcnt(6)\n"
        "v_mfma_f32_16x16x32_bf16 v[48:51], a[120:123], v[8:11], v[48:51]\n"
        "global_store_dwordx4 v254, a[180:183], s[70:71] offset:1024 sc1\n"
        "ds_read_b128 a[116:119], v240 offset:13312\n"
        "ds_read_b128 a[148:151], v241 offset:1024\n"
        "v_max_f32 v246, v32, v33\n"
        "v_mfma_f32_16x16x32_bf16 v[48:51], a[124:127], v[12:15], v[48:51]\n"
        "v_max3_f32 v246, v34, v35, v246\n"
        "v_mov_b32 v247, v246\n"
        "ds_read_b128 a[120:123], v240 offset:14336\n"
        "ds_read_b128 a[152:155], v241 offset:2048\n"
        "v_permlane16_swap_b32 v246, v247\n"
        "v_max_f32 v246, v246, v247\n"
        "v_mov_b32 v247, v246\n"
        "global_store_dwordx4 v254, a[184:187], s[70:71] offset:2048 sc1\n"
        "ds_read_b128 a[124:127], v240 offset:15360\n"
        "v_permlane32_swap_b32 v246, v247\n"
        "v_max_f32 v246, v246, v247\n"
        "v_sub_f32 v224, 0, v246\n"
        "v_sub_f32 v225, 0, v246\n"
        "v_sub_f32 v226, 0, v246\n"
        "v_sub_f32 v227, 0, v246\n"
        "v_add_f32 v32, v32, v224\n"
        "v_exp_f32 v32, v32\n"
        "v_add_f32 v33, v33, v224\n"
        "v_exp_f32 v33, v33\n"
        "v_add_f32 v34, v34, v224\n"
        "v_cvt_pk_bf16_f32 v16, v32, v33\n"
        "v_exp_f32 v34, v34\n"
        "v_add_f32 v35, v35, v224\n"
        "v_exp_f32 v35, v35\n"
        "v_cndmask_b32 v32, v224, v242, s[30:31]\n"
        "v_cvt_pk_bf16_f32 v17, v34, v35\n"
        "v_cndmask_b32 v33, v224, v242, s[56:57]\n"
        "v_cndmask_b32 v34, v224, v242, s[58:59]\n"
        "v_cndmask_b32 v35, v224, v242, s[60:61]\n"
        "s_waitcnt lgkmcnt(1)\n"
        "s_nop 0\n"
        "v_mfma_f32_16x16x32_bf16 v[32:35], a[96:99], v[0:3], v[32:35]\n"
        "global_store_dwordx4 v254, a[188:191], s[70:71] offset:3072 sc1\n"
        "ds_read_b128 a[156:159], v241 offset:3072\n"
        "ds_read_b128 a[96:99], v240 offset:16384\n"
        "v_add_f32 v48, v48, v224\n"
        "v_mfma_f32_16x16x32_bf16 v[32:35], a[100:103], v[4:7], v[32:35]\n"
        "v_exp_f32 v48, v48\n"
        "v_mfma_f32_16x16x32_bf16 v[32:35], a[104:107], v[8:11], v[32:35]\n"
        "ds_read_b128 a[128:131], v241 offset:4096\n"
        "v_add_f32 v49, v49, v224\n"
        "v_mfma_f32_16x16x32_bf16 v[32:35], a[108:111], v[12:15], v[32:35]\n"
        "v_exp_f32 v49, v49\n"
        "ds_read_b128 a[100:103], v240 offset:17408\n"
        "v_cvt_pk_bf16_f32 v18, v48, v49\n"
        "v_add_f32 v50, v50, v224\n"
        "v_exp_f32 v50, v50\n"
        "v_add_f32 v51, v51, v224\n"
        "v_exp_f32 v51, v51\n"
        "ds_read_b128 a[132:135], v241 offset:5120\n"
        "v_cvt_pk_bf16_f32 v19, v50, v51\n"
        "s_nop 1\n"
        "v_mfma_f32_16x16x32_bf16 v[160:163], a[144:147], v[16:19], 0\n"
        "s_add_u32 s70, s38, s97\n"
        "s_addc_u32 s71, s39, 0\n"
        "v_cndmask_b32 v48, v224, v242, s[62:63]\n"
        "v_mfma_f32_16x16x32_bf16 a[64:67], a[80:83], v[16:19], 0\n"
        "global_store_dwordx4 v254, a[16:19], s[70:71] offset:0 sc1\n"
        "s_waitcnt lgkmcnt(3)\n"
        "v_cndmask_b32 v49, v224, v242, s[84:85]\n"
        "v_cndmask_b32 v50, v224, v242, s[86:87]\n"
        "v_cndmask_b32 v51, v224, v242, s[88:89]\n"
        "s_nop 1\n"
        "v_mfma_f32_16x16x32_bf16 v[48:51], a[112:115], v[0:3], v[48:51]\n"
        "ds_read_b128 a[104:107], v240 offset:18432\n"
        "v_cndmask_b32 v38, 0, v242, s[14:15]\n"
        "v_mfma_f32_16x16x32_bf16 v[48:51], a[116:119], v[4:7], v[48:51]\n"
        "ds_read_b128 a[136:139], v241 offset:6144\n"
        "v_cndmask_b32 v39, 0, v242, s[16:17]\n"
        "v_mfma_f32_16x16x32_bf16 v[164:167], a[148:151], v[16:19], 0\n"
        "ds_read_b128 a[108:111], v240 offset:19456\n"
        "v_cndmask_b32 v52, 0, v242, s[20:21]\n"
        "v_mfma_f32_16x16x32_bf16 v[48:51], a[120:123], v[8:11], v[48:51]\n"
        "s_waitcnt lgkmcnt(4)\n"
        "v_exp_f32 v32, v32\n"
        "v_mfma_f32_16x16x32_bf16 v[168:171], a[152:155], v[16:19], 0\n"
        "s_waitcnt lgkmcnt(3)\n"
        "v_exp_f32 v33, v33\n"
        "v_mfma_f32_16x16x32_bf16 v[48:51], a[124:127], v[12:15], v[48:51]\n"
        "ds_read_b128 a[140:143], v241 offset:7168\n"
        "v_cvt_pk_bf16_f32 v80, v32, v33\n"
        "v_mfma_f32_16x16x32_bf16 v[172:175], a[156:159], v[16:19], 0\n"
        "ds_read_b128 a[112:115], v240 offset:20480\n"
        "v_mfma_f32_16x16x32_bf16 v[36:39], a[96:99], v[64:67], v[36:39]\n"
        "ds_read_b128 a[144:147], v241 offset:8192\n"
        "v_mfma_f32_16x16x32_bf16 v[176:179], a[128:131], v[16:19], 0\n"
        "global_store_dwordx4 v254, a[20:23], s[70:71] offset:1024 sc1\n"
        "ds_read_b128 a[116:119], v240 offset:21504\n"
        "s_waitcnt lgkmcnt(6)\n"
        "v_exp_f32 v34, v34\n"
        "v_mfma_f32_16x16x32_bf16 v[36:39], a[100:103], v[68:71], v[36:39]\n"
        "s_waitcnt lgkmcnt(5)\n"
        "v_exp_f32 v35, v35\n"
        "v_mfma_f32_16x16x32_bf16 v[180:183], a[132:135], v[16:19], 0\n"
        "ds_read_b128 a[148:151], v241 offset:9216\n"
        "v_cvt_pk_bf16_f32 v81, v34, v35\n"
        "v_mfma_f32_16x16x32_bf16 v[32:35], a[96:99], v[0:3], v[224:227]\n"
        "ds_read_b128 a[120:123], v240 offset:22528\n"
        "s_waitcnt lgkmcnt(6)\n"
        "v_mfma_f32_16x16x32_bf16 v[32:35], a[100:103], v[4:7], v[32:35]\n"
        "ds_read_b128 a[152:155], v241 offset:10240\n"
        "s_waitcnt lgkmcnt(6)\n"
        "v_mfma_f32_16x16x32_bf16 v[32:35], a[104:107], v[8:11], v[32:35]\n"
        "ds_read_b128 a[124:127], v240 offset:23552\n"
        "v_mfma_f32_16x16x32_bf16 v[36:39], a[104:107], v[72:75], v[36:39]\n"
        "v_exp_f32 v48, v48\n"
        "v_mfma_f32_16x16x32_bf16 v[184:187], a[136:139], v[16:19], 0\n"
        "v_exp_f32 v49, v49\n"
        "v_mfma_f32_16x16x32_bf16 v[32:35], a[108:111], v[12:15], v[32:35]\n"
        "ds_read_b128 a[156:159], v241 offset:11264\n"
        "v_cvt_pk_bf16_f32 v82, v48, v49\n"
        "v_mfma_f32_16x16x32_bf16 v[36:39], a[108:111], v[76:79], v[36:39]\n"
        "global_store_dwordx4 v254, a[24:27], s[70:71] offset:2048 sc1\n"
        "ds_read_b128 a[96:99], v240 offset:24576\n"
        "ds_read_b128 a[128:131], v241 offset:12288\n"
        "v_cndmask_b32 v53, 0, v242, s[22:23]\n"
        "v_mfma_f32_16x16x32_bf16 v[188:191], a[140:143], v[16:19], 0\n"
        "ds_read_b128 a[100:103], v240 offset:25600\n"
        "v_exp_f32 v50, v50\n"
        "v_exp_f32 v51, v51\n"
        "ds_read_b128 a[132:135], v241 offset:13312\n"
        "v_cvt_pk_bf16_f32 v83, v50, v51\n"
        "s_waitcnt lgkmcnt(5)\n"
        "v_mfma_f32_16x16x32_bf16 v[48:51], a[112:115], v[0:3], v[224:227]\n"
        "ds_read_b128 a[104:107], v240 offset:26624\n"
        "v_cndmask_b32 v54, 0, v242, s[24:25]\n"
        "v_mfma_f32_16x16x32_bf16 v[160:163], a[144:147], v[80:83], v[160:163]\n"
        "ds_read_b128 a[136:139], v241 offset:14336\n"
        "v_cndmask_b32 v55, 0, v242, s[28:29]\n"
        "s_nop 1\n"
        "v_mfma_f32_16x16x32_bf16 v[52:55], a[112:115], v[64:67], v[52:55]\n"
        "s_waitcnt lgkmcnt(6)\n"
        "v_exp_f32 v32, v32\n"
        "v_mfma_f32_16x16x32_bf16 a[64:67], a[80:83], v[80:83], a[64:67]\n"
        "v_exp_f32 v33, v33\n"
        "v_mfma_f32_16x16x32_bf16 v[48:51], a[116:119], v[4:7], v[48:51]\n"
        "s_waitcnt lgkmcnt(4)\n"
        "v_cvt_pk_bf16_f32 v16, v32, v33\n"
        "v_max_f32 v248, v36, v37\n"
        "v_mfma_f32_16x16x32_bf16 v[52:55], a[116:119], v[68:71], v[52:55]\n"
        "global_store_dwordx4 v254, a[28:31], s[70:71] offset:3072 sc1\n"
        "ds_read_b128 a[108:111], v240 offset:27648\n"
        "v_exp_f32 v34, v34\n"
        "v_max3_f32 v248, v38, v39, v248\n"
        "v_mfma_f32_16x16x32_bf16 v[164:167], a[148:151], v[80:83], v[164:167]\n"
        "s_waitcnt lgkmcnt(3)\n"
        "v_exp_f32 v35, v35\n"
        "v_mfma_f32_16x16x32_bf16 v[48:51], a[120:123], v[8:11], v[48:51]\n"
        "ds_read_b128 a[140:143], v241 offset:15360\n"
        "v_cvt_pk_bf16_f32 v17, v34, v35\n"
        "v_mfma_f32_16x16x32_bf16 v[52:55], a[120:123], v[72:75], v[52:55]\n"
        "ds_read_b128 a[112:115], v240 offset:28672\n"
        "v_mov_b32 v249, v248\n"
        "v_mfma_f32_16x16x32_bf16 v[168:171], a[152:155], v[80:83], v[168:171]\n"
        "ds_read_b128 a[144:147], v241 offset:16384\n"
        "v_mul_f32 v96, 0x3e0293ee, v96\n"
        "v_mfma_f32_16x16x32_bf16 v[48:51], a[124:127], v[12:15], v[48:51]\n"
        "s_waitcnt lgkmcnt(4)\n"
        "v_permlane16_swap_b32 v248, v249\n"
        "v_mfma_f32_16x16x32_bf16 v[52:55], a[124:127], v[76:79], v[52:55]\n"
        "ds_read_b128 a[116:119], v240 offset:29696\n"
        "v_max_f32 v248, v248, v249\n"
        "v_mfma_f32_16x16x32_bf16 v[172:175], a[156:159], v[80:83], v[172:175]\n"
        "ds_read_b128 a[120:123], v240 offset:30720\n"
        "v_mov_b32 v249, v248\n"
        "v_mfma_f32_16x16x32_bf16 v[32:35], a[96:99], v[0:3], v[224:227]\n"
        "ds_read_b128 a[124:127], v240 offset:31744\n"
        "v_mul_f32 v97, 0x3e0293ee, v97\n"
        "v_mfma_f32_16x16x32_bf16 v[176:179], a[128:131], v[80:83], v[176:179]\n"
        "s_waitcnt lgkmcnt(6)\n"
        "v_permlane32_swap_b32 v248, v249\n"
        "v_mfma_f32_16x16x32_bf16 v[32:35], a[100:103], v[4:7], v[32:35]\n"
        "s_waitcnt lgkmcnt(5)\n"
        "v_max_f32 v248, v248, v249\n"
        "v_sub_f32 v228, 0, v248\n"
        "v_mfma_f32_16x16x32_bf16 v[180:183], a[132:135], v[80:83], v[180:183]\n"
        "ds_read_b128 a[148:151], v241 offset:17408\n"
        "v_sub_f32 v229, 0, v248\n"
        "v_mfma_f32_16x16x32_bf16 v[32:35], a[104:107], v[8:11], v[32:35]\n"
        "ds_read_b128 a[152:155], v241 offset:18432\n"
        "v_sub_f32 v230, 0, v248\n"
        "v_mfma_f32_16x16x32_bf16 v[184:187], a[136:139], v[80:83], v[184:187]\n"
        "ds_read_b128 a[156:159], v241 offset:19456\n"
        "v_sub_f32 v231, 0, v248\n"
        "v_mfma_f32_16x16x32_bf16 v[32:35], a[108:111], v[12:15], v[32:35]\n"
        "v_add_f32 v36, v36, v228\n"
        "v_add_f32 v37, v37, v228\n"
        "v_mfma_f32_16x16x32_bf16 v[188:191], a[140:143], v[80:83], v[188:191]\n"
        "v_exp_f32 v36, v36\n"
        "v_exp_f32 v37, v37\n"
        "v_add_f32 v38, v38, v228\n"
        "v_cvt_pk_bf16_f32 v20, v36, v37\n"
        "v_exp_f32 v38, v38\n"
        "v_add_f32 v39, v39, v228\n"
        "v_exp_f32 v39, v39\n"
        "v_cndmask_b32 v36, v228, v242, s[30:31]\n"
        "v_cvt_pk_bf16_f32 v21, v38, v39\n"
        "v_cndmask_b32 v37, v228, v242, s[56:57]\n"
        "v_cndmask_b32 v38, v228, v242, s[58:59]\n"
        "v_cndmask_b32 v39, v228, v242, s[60:61]\n"
        "s_nop 1\n"
        "v_mfma_f32_16x16x32_bf16 v[36:39], a[96:99], v[64:67], v[36:39]\n"
        "v_exp_f32 v48, v48\n"
        "v_mfma_f32_16x16x32_bf16 v[36:39], a[100:103], v[68:71], v[36:39]\n"
        "v_exp_f32 v49, v49\n"
        "v_mfma_f32_16x16x32_bf16 v[36:39], a[104:107], v[72:75], v[36:39]\n"
        "v_cvt_pk_bf16_f32 v18, v48, v49\n"
        "v_mul_f32 v98, 0x3e0293ee, v98\n"
        "v_mfma_f32_16x16x32_bf16 v[36:39], a[108:111], v[76:79], v[36:39]\n"
        "v_exp_f32 v50, v50\n"
        "v_exp_f32 v51, v51\n"
        "v_mul_f32 v99, 0x3e0293ee, v99\n"
        "v_cvt_pk_bf16_f32 v19, v50, v51\n"
        "s_nop 1\n"
        "v_mfma_f32_16x16x32_bf16 a[64:67], a[80:83], v[16:19], a[64:67]\n"
        "s_waitcnt lgkmcnt(0)\n"
        "v_mul_f32 v100, 0x3e0293ee, v100\n"
        "v_mul_f32 v101, 0x3e0293ee, v101\n"
        "v_mfma_f32_16x16x32_bf16 v[48:51], a[112:115], v[0:3], v[224:227]\n"
        "v_mul_f32 v102, 0x3e0293ee, v102\n"
        "v_mul_f32 v103, 0x3e0293ee, v103\n"
        "v_mfma_f32_16x16x32_bf16 v[160:163], a[144:147], v[16:19], v[160:163]\n"
        "v_cvt_pk_bf16_f32 v96, v96, v97\n"
        "v_cvt_pk_bf16_f32 v97, v98, v99\n"
        "v_mfma_f32_16x16x32_bf16 v[48:51], a[116:119], v[4:7], v[48:51]\n"
        "v_cvt_pk_bf16_f32 v98, v100, v101\n"
        "v_cvt_pk_bf16_f32 v99, v102, v103\n"
        "v_mfma_f32_16x16x32_bf16 v[164:167], a[148:151], v[16:19], v[164:167]\n"
        "v_mul_f32 v104, 0x3e0293ee, v104\n"
        "v_mul_f32 v105, 0x3e0293ee, v105\n"
        "v_mul_f32 v106, 0x3e0293ee, v106\n"
        "v_mul_f32 v107, 0x3e0293ee, v107\n"
        "v_mul_f32 v108, 0x3e0293ee, v108\n"
        "v_mul_f32 v109, 0x3e0293ee, v109\n"
        "v_mul_f32 v110, 0x3e0293ee, v110\n"
        "v_mul_f32 v111, 0x3e0293ee, v111\n"
        "v_cvt_pk_bf16_f32 v100, v104, v105\n"
        "v_cvt_pk_bf16_f32 v101, v106, v107\n"
        "v_cvt_pk_bf16_f32 v102, v108, v109\n"
        "v_cvt_pk_bf16_f32 v103, v110, v111\n"
        "v_mul_f32 v112, 0x3e0293ee, v112\n"
        "v_mul_f32 v113, 0x3e0293ee, v113\n"
        "v_mul_f32 v114, 0x3e0293ee, v114\n"
        "v_mul_f32 v115, 0x3e0293ee, v115\n"
        "v_mul_f32 v116, 0x3e0293ee, v116\n"
        "v_mul_f32 v117, 0x3e0293ee, v117\n"
        "v_mul_f32 v118, 0x3e0293ee, v118\n"
        "v_mul_f32 v119, 0x3e0293ee, v119\n"
        "v_cvt_pk_bf16_f32 v104, v112, v113\n"
        "v_cvt_pk_bf16_f32 v105, v114, v115\n"
        "v_cvt_pk_bf16_f32 v106, v116, v117\n"
        "v_cvt_pk_bf16_f32 v107, v118, v119\n"
        "v_mul_f32 v120, 0x3e0293ee, v120\n"
        "v_mul_f32 v121, 0x3e0293ee, v121\n"
        "v_mul_f32 v122, 0x3e0293ee, v122\n"
        "v_mul_f32 v123, 0x3e0293ee, v123\n"
        "v_mul_f32 v124, 0x3e0293ee, v124\n"
        "v_mul_f32 v125, 0x3e0293ee, v125\n"
        "v_mul_f32 v126, 0x3e0293ee, v126\n"
        "v_mul_f32 v127, 0x3e0293ee, v127\n"
        "v_cvt_pk_bf16_f32 v108, v120, v121\n"
        "v_cvt_pk_bf16_f32 v109, v122, v123\n"
        "v_cvt_pk_bf16_f32 v110, v124, v125\n"
        "v_cvt_pk_bf16_f32 v111, v126, v127\n"
        "v_mfma_f32_16x16x32_bf16 v[48:51], a[120:123], v[8:11], v[48:51]\n"
        "s_barrier\n"
        "v_xor_b32 v240, 0x8000, v240\n"
        "v_add_f32 v52, v52, v228\n"
        "v_add_f32 v53, v53, v228\n"
        "v_mfma_f32_16x16x32_bf16 v[168:171], a[152:155], v[16:19], v[168:171]\n"
        "ds_read_b128 a[96:99], v240 offset:0\n"
        "v_add_f32 v54, v54, v228\n"
        "v_mfma_f32_16x16x32_bf16 v[48:51], a[124:127], v[12:15], v[48:51]\n"
        "v_exp_f32 v52, v52\n"
        "v_add_f32 v55, v55, v228\n"
        "v_mfma_f32_16x16x32_bf16 v[172:175], a[156:159], v[16:19], v[172:175]\n"
        "v_exp_f32 v53, v53\n"
        "ds_read_b128 a[128:131], v241 offset:20480\n"
        "ds_read_b128 a[100:103], v240 offset:1024\n"
        "ds_read_b128 a[132:135], v241 offset:21504\n"
        "ds_read_b128 a[104:107], v240 offset:2048\n"
        "ds_read_b128 a[136:139], v241 offset:22528\n"
        "ds_read_b128 a[108:111], v240 offset:3072\n"
        "ds_read_b128 a[140:143], v241 offset:23552\n"
        "global_load_dwordx4 v[112:115], v254, s[92:93] offset:0\n"
        "v_cvt_pk_bf16_f32 v22, v52, v53\n"
        "v_exp_f32 v54, v54\n"
        "v_exp_f32 v55, v55\n"
        "s_waitcnt lgkmcnt(6)\n"
        "v_cvt_pk_bf16_f32 v23, v54, v55\n"
        "s_nop 1\n"
        "v_mfma_f32_16x16x32_bf16 v[192:195], a[144:147], v[20:23], 0\n"
        "v_cndmask_b32 v52, v228, v242, s[62:63]\n"
        "v_cndmask_b32 v53, v228, v242, s[84:85]\n"
        "v_mfma_f32_16x16x32_bf16 a[68:71], a[80:83], v[20:23], 0\n"
        "v_cndmask_b32 v54, v228, v242, s[86:87]\n"
        "v_cndmask_b32 v55, v228, v242, s[88:89]\n"
        "s_nop 1\n"
        "v_mfma_f32_16x16x32_bf16 v[52:55], a[112:115], v[64:67], v[52:55]\n"
        "v_exp_f32 v32, v32\n"
        "v_mfma_f32_16x16x32_bf16 v[52:55], a[116:119], v[68:71], v[52:55]\n"
        "v_exp_f32 v33, v33\n"
        "v_mfma_f32_16x16x32_bf16 v[196:199], a[148:151], v[20:23], 0\n"
        "s_waitcnt lgkmcnt(1)\n"
        "v_cvt_pk_bf16_f32 v80, v32, v33\n"
        "v_mul_f32 v128, 0x3e0293ee, v128\n"
        "v_mfma_f32_16x16x32_bf16 v[52:55], a[120:123], v[72:75], v[52:55]\n"
        "v_exp_f32 v34, v34\n"
        "v_mfma_f32_16x16x32_bf16 v[200:203], a[152:155], v[20:23], 0\n"
        "v_exp_f32 v35, v35\n"
        "v_mfma_f32_16x16x32_bf16 v[52:55], a[124:127], v[76:79], v[52:55]\n"
        "ds_read_b128 a[112:115], v240 offset:4096\n"
        "v_cvt_pk_bf16_f32 v81, v34, v35\n"
        "v_mfma_f32_16x16x32_bf16 v[204:207], a[156:159], v[20:23], 0\n"
        "v_exp_f32 v36, v36\n"
        "v_mfma_f32_16x16x32_bf16 v[32:35], a[96:99], v[0:3], v[224:227]\n"
        "v_exp_f32 v37, v37\n"
        "v_mfma_f32_16x16x32_bf16 v[176:179], a[128:131], v[16:19], v[176:179]\n"
        "s_waitcnt lgkmcnt(1)\n"
        "v_cvt_pk_bf16_f32 v84, v36, v37\n"
        "v_mul_f32 v129, 0x3e0293ee, v129\n"
        "v_mfma_f32_16x16x32_bf16 v[208:211], a[128:131], v[20:23], 0\n"
        "v_mul_f32 v130, 0x3e0293ee, v130\n"
        "v_mul_f32 v131, 0x3e0293ee, v131\n"
        "v_mfma_f32_16x16x32_bf16 v[32:35], a[100:103], v[4:7], v[32:35]\n"
        "v_cvt_pk_bf16_f32 v128, v128, v129\n"
        "v_mul_f32 v132, 0x3e0293ee, v132\n"
        "v_mfma_f32_16x16x32_bf16 v[180:183], a[132:135], v[16:19], v[180:183]\n"
        "v_mul_f32 v133, 0x3e0293ee, v133\n"
        "v_cvt_pk_bf16_f32 v129, v130, v131\n"
        "v_mfma_f32_16x16x32_bf16 v[212:215], a[132:135], v[20:23], 0\n"
        "global_load_dwordx4 v[116:119], v254, s[92:93] offset:1024\n"
        "v_mul_f32 v138, 0x3e0293ee, v138\n"
        "v_mfma_f32_16x16x32_bf16 v[32:35], a[104:107], v[8:11], v[32:35]\n"
        "v_mul_f32 v139, 0x3e0293ee, v139\n"
        "v_cvt_pk_bf16_f32 v130, v132, v133\n"
        "v_mul_f32 v136, 0x3e0293ee, v136\n"
        "v_mul_f32 v137, 0x3e0293ee, v137\n"
        "v_mul_f32 v148, 0x3e0293ee, v148\n"
        "v_mul_f32 v149, 0x3e0293ee, v149\n"
        "v_cvt_pk_bf16_f32 v133, v138, v139\n"
        "v_mul_f32 v144, 0x3e0293ee, v144\n"
        "v_mul_f32 v145, 0x3e0293ee, v145\n"
        "v_cvt_pk_bf16_f32 v132, v136, v137\n"
        "v_cvt_pk_bf16_f32 v138, v148, v149\n"
        "v_cvt_pk_bf16_f32 v136, v144, v145\n"
        "v_add_u32 v148, 16, v254\n"
        "v_mul_f32 v146, 0x3e0293ee, v146\n"
        "v_mul_f32 v147, 0x3e0293ee, v147\n"
        "v_cvt_pk_bf16_f32 v137, v146, v147\n"
        "v_min_u32 v149, 1020, v148\n"
        "v_mfma_f32_16x16x32_bf16 v[184:187], a[136:139], v[16:19], v[184:187]\n"
        "global_load_dword v144, v148, s[92:93] offset:0\n"
        "ds_read_b128 a[144:147], v241 offset:24576\n"
        "v_mfma_f32_16x16x32_bf16 v[32:35], a[108:111], v[12:15], v[32:35]\n"
        "global_load_dword v145, v148, s[92:93] offset:1024\n"
        "ds_read_b128 a[116:119], v240 offset:5120\n"
        "v_mfma_f32_16x16x32_bf16 v[188:191], a[140:143], v[16:19], v[188:191]\n"
        "global_load_dwordx4 v[120:123], v254, s[92:93] offset:2048\n"
        "ds_read_b128 a[148:151], v241 offset:25600\n"
        "v_mfma_f32_16x16x32_bf16 v[216:219], a[136:139], v[20:23], 0\n"
        "v_exp_f32 v38, v38\n"
        "v_cndmask_b32 v40, 0, v242, s[10:11]\n"
        "v_cndmask_b32 v41, 0, v242, s[12:13]\n"
        "v_cndmask_b32 v42, 0, v242, s[14:15]\n"
        "v_cndmask_b32 v43, 0, v242, s[16:17]\n"
        "s_nop 1\n"
        "v_mfma_f32_16x16x32_bf16 v[40:43], a[96:99], v[96:99], v[40:43]\n"
        "v_exp_f32 v39, v39\n"
        "v_mfma_f32_16x16x32_bf16 v[40:43], a[100:103], v[100:103], v[40:43]\n"
        "global_load_dword v146, v148, s[92:93] offset:2048\n"
        "v_cvt_pk_bf16_f32 v85, v38, v39\n"
        "v_mfma_f32_16x16x32_bf16 v[36:39], a[96:99], v[64:67], v[228:231]\n"
        "global_load_dwordx4 v[124:127], v254, s[92:93] offset:3072\n"
        "ds_read_b128 a[120:123], v240 offset:6144\n"
        "v_mfma_f32_16x16x32_bf16 v[36:39], a[100:103], v[68:71], v[36:39]\n"
        "v_exp_f32 v48, v48\n"
        "v_mfma_f32_16x16x32_bf16 v[36:39], a[104:107], v[72:75], v[36:39]\n"
        "v_exp_f32 v49, v49\n"
        "v_mfma_f32_16x16x32_bf16 v[40:43], a[104:107], v[104:107], v[40:43]\n"
        "ds_read_b128 a[152:155], v241 offset:26624\n"
        "v_cvt_pk_bf16_f32 v82, v48, v49\n"
        "v_mfma_f32_16x16x32_bf16 v[36:39], a[108:111], v[76:79], v[36:39]\n"
        "global_load_dword v147, v149, s[92:93] offset:3072\n"
        "ds_read_b128 a[124:127], v240 offset:7168\n"
        "v_mfma_f32_16x16x32_bf16 v[40:43], a[108:111], v[108:111], v[40:43]\n"
        "v_exp_f32 v50, v50\n"
        "v_mfma_f32_16x16x32_bf16 v[220:223], a[140:143], v[20:23], 0\n"
        "v_exp_f32 v51, v51\n"
        "ds_read_b128 a[156:159], v241 offset:27648\n"
        "v_cvt_pk_bf16_f32 v83, v50, v51\n"
        "s_waitcnt lgkmcnt(4)\n"
        "v_mfma_f32_16x16x32_bf16 v[48:51], a[112:115], v[0:3], v[224:227]\n"
        "ds_read_b128 a[96:99], v240 offset:8192\n"
        "v_mul_f32 v134, 0x3e0293ee, v134\n"
        "v_mfma_f32_16x16x32_bf16 v[48:51], a[116:119], v[4:7], v[48:51]\n"
        "s_waitcnt lgkmcnt(4)\n"
        "v_exp_f32 v52, v52\n"
        "v_mfma_f32_16x16x32_bf16 v[48:51], a[120:123], v[8:11], v[48:51]\n"
        "v_exp_f32 v53, v53\n"
        "v_mfma_f32_16x16x32_bf16 a[64:67], a[80:83], v[80:83], a[64:67]\n"
        "s_waitcnt lgkmcnt(3)\n"
        "v_cvt_pk_bf16_f32 v86, v52, v53\n"
        "v_mul_f32 v135, 0x3e0293ee, v135\n"
        "v_mfma_f32_16x16x32_bf16 v[160:163], a[144:147], v[80:83], v[160:163]\n"
        "s_waitcnt lgkmcnt(2)\n"
        "v_cvt_pk_bf16_f32 v131, v134, v135\n"
        "v_mul_f32 v140, 0x3e0293ee, v140\n"
        "v_mfma_f32_16x16x32_bf16 v[48:51], a[124:127], v[12:15], v[48:51]\n"
        "v_mul_f32 v141, 0x3e0293ee, v141\n"
        "v_mul_f32 v142, 0x3e0293ee, v142\n"
        "v_mul_f32 v143, 0x3e0293ee, v143\n"
        "v_cvt_pk_bf16_f32 v134, v140, v141\n"
        "v_cvt_pk_bf16_f32 v135, v142, v143\n"
        "v_mul_f32 v150, 0x3e0293ee, v150\n"
        "v_mul_f32 v151, 0x3e0293ee, v151\n"
        "v_cvt_pk_bf16_f32 v139, v150, v151\n"
        "v_mul_f32 v152, 0x3e0293ee, v152\n"
        "v_mul_f32 v153, 0x3e0293ee, v153\n"
        "v_mul_f32 v154, 0x3e0293ee, v154\n"
        "v_mul_f32 v155, 0x3e0293ee, v155\n"
        "v_mul_f32 v156, 0x3e0293ee, v156\n"
        "v_mul_f32 v157, 0x3e0293ee, v157\n"
        "v_mul_f32 v158, 0x3e0293ee, v158\n"
        "v_mul_f32 v159, 0x3e0293ee, v159\n"
        "v_cvt_pk_bf16_f32 v140, v152, v153\n"
        "v_cvt_pk_bf16_f32 v141, v154, v155\n"
        "v_cvt_pk_bf16_f32 v142, v156, v157\n"
        "v_cvt_pk_bf16_f32 v143, v158, v159\n"
        "v_bfe_u32 v245, v254, 4, 5\n"
        "v_lshl_add_u32 v245, v245, 7, s80\n"
        "v_mfma_f32_16x16x32_bf16 v[164:167], a[148:151], v[80:83], v[164:167]\n"
        "s_waitcnt lgkmcnt(0)\n"
        "v_exp_f32 v32, v32\n"
        "v_mfma_f32_16x16x32_bf16 v[168:171], a[152:155], v[80:83], v[168:171]\n"
        "s_waitcnt vmcnt(8)\n"
        "v_exp_f32 v33, v33\n"
        "v_cndmask_b32 v56, 0, v242, s[20:21]\n"
        "v_cvt_pk_bf16_f32 v16, v32, v33\n"
        "v_cndmask_b32 v57, 0, v242, s[22:23]\n"
        "v_cndmask_b32 v58, 0, v242, s[24:25]\n"
        "v_cndmask_b32 v59, 0, v242, s[28:29]\n"
        "s_nop 1\n"
        "v_mfma_f32_16x16x32_bf16 v[56:59], a[112:115], v[96:99], v[56:59]\n"
        "s_barrier\n"
        "v_exp_f32 v34, v34\n"
        "v_mfma_f32_16x16x32_bf16 v[56:59], a[116:119], v[100:103], v[56:59]\n"
        "v_exp_f32 v35, v35\n"
        "v_mfma_f32_16x16x32_bf16 v[56:59], a[120:123], v[104:107], v[56:59]\n"
        "v_exp_f32 v48, v48\n"
        "v_cvt_pk_bf16_f32 v17, v34, v35\n"
        "v_mfma_f32_16x16x32_bf16 v[56:59], a[124:127], v[108:111], v[56:59]\n"
        "v_exp_f32 v49, v49\n"
        "v_mfma_f32_16x16x32_bf16 v[172:175], a[156:159], v[80:83], v[172:175]\n"
        "v_exp_f32 v50, v50\n"
        "v_cvt_pk_bf16_f32 v18, v48, v49\n"
        "v_mfma_f32_16x16x32_bf16 v[32:35], a[96:99], v[0:3], v[224:227]\n"
        "v_exp_f32 v51, v51\n"
        "ds_read_b128 a[128:131], v241 offset:28672\n"
        "s_cmp_lg_u32 s40, 0\n"
        "s_cbranch_scc1 LW1_%=\n"
        "s_mov_b64 exec, 0xffff\n"
        "global_store_dwordx2 v244, v[250:251], s[48:49] sc1\n"
        "s_mov_b64 exec, -1\n"
        "LW1_%=:\n"
        "ds_read_b128 a[100:103], v240 offset:9216\n"
        "ds_read_b128 a[132:135], v241 offset:29696\n"
        "ds_read_b128 a[104:107], v240 offset:10240\n"
        "ds_read_b128 a[136:139], v241 offset:30720\n"
        "ds_read_b128 a[140:143], v241 offset:31744\n"
        "ds_read_b128 a[108:111], v240 offset:11264\n"
        "v_cvt_pk_bf16_f32 v19, v50, v51\n"
        "s_nop 1\n"
        "v_mfma_f32_16x16x32_bf16 a[64:67], a[80:83], v[16:19], a[64:67]\n"
        "global_load_dwordx2 v[252:253], v245, s[48:49] sc1\n"
        "v_exp_f32 v54, v54\n"
        "v_exp_f32 v55, v55\n"
        "s_waitcnt lgkmcnt(6)\n"
        "v_cvt_pk_bf16_f32 v87, v54, v55\n"
        "v_mfma_f32_16x16x32_bf16 v[52:55], a[112:115], v[64:67], v[228:231]\n"
        "v_exp_f32 v36, v36\n"
        "v_mfma_f32_16x16x32_bf16 v[192:195], a[144:147], v[84:87], v[192:195]\n"
        "v_exp_f32 v37, v37\n"
        "v_mfma_f32_16x16x32_bf16 a[68:71], a[80:83], v[84:87], a[68:71]\n"
        "ds_read_b128 a[112:115], v240 offset:12288\n"
        "v_cvt_pk_bf16_f32 v20, v36, v37\n"
        "v_mfma_f32_16x16x32_bf16 v[52:55], a[116:119], v[68:71], v[52:55]\n"
        "v_exp_f32 v38, v38\n"
        "v_mfma_f32_16x16x32_bf16 v[196:199], a[148:151], v[84:87], v[196:199]\n"
        "s_waitcnt lgkmcnt(2)\n"
        "v_exp_f32 v39, v39\n"
        "v_mfma_f32_16x16x32_bf16 v[52:55], a[120:123], v[72:75], v[52:55]\n"
        "v_cvt_pk_bf16_f32 v21, v38, v39\n"
        "s_barrier\n"
        "v_mfma_f32_16x16x32_bf16 v[200:203], a[152:155], v[84:87], v[200:203]\n"
        "ds_read_b128 a[116:119], v240 offset:13312\n"
        "v_xor_b32 v241, 0x8000, v241\n"
        "v_mfma_f32_16x16x32_bf16 v[52:55], a[124:127], v[76:79], v[52:55]\n"
        "ds_read_b128 a[144:147], v241 offset:0\n"
        "v_max_f32 v246, v40, v41\n"
        "v_mfma_f32_16x16x32_bf16 v[204:207], a[156:159], v[84:87], v[204:207]\n"
        "ds_read_b128 a[148:151], v241 offset:1024\n"
        "v_max3_f32 v246, v42, v43, v246\n"
        "v_mfma_f32_16x16x32_bf16 v[36:39], a[96:99], v[64:67], v[228:231]\n"
        "ds_read_b128 a[120:123], v240 offset:14336\n"
        "v_mov_b32 v247, v246\n"
        "v_mfma_f32_16x16x32_bf16 v[176:179], a[128:131], v[80:83], v[176:179]\n"
        "ds_read_b128 a[152:155], v241 offset:2048\n"
        "v_cndmask_b32 v44, 0, v242, s[10:11]\n"
        "v_mfma_f32_16x16x32_bf16 v[208:211], a[128:131], v[84:87], v[208:211]\n"
        "v_permlane16_swap_b32 v246, v247\n"
        "v_mfma_f32_16x16x32_bf16 v[32:35], a[100:103], v[4:7], v[32:35]\n"
        "ds_read_b128 a[124:127], v240 offset:15360\n"
        "v_max_f32 v246, v246, v247\n"
        "v_mfma_f32_16x16x32_bf16 v[36:39], a[100:103], v[68:71], v[36:39]\n"
        "ds_read_b128 a[156:159], v241 offset:3072\n"
        "v_mov_b32 v247, v246\n"
        "v_mfma_f32_16x16x32_bf16 v[180:183], a[132:135], v[80:83], v[180:183]\n"
        "v_exp_f32 v52, v52\n"
        "v_mfma_f32_16x16x32_bf16 v[212:215], a[132:135], v[84:87], v[212:215]\n"
        "v_permlane32_swap_b32 v246, v247\n"
        "v_mfma_f32_16x16x32_bf16 v[32:35], a[104:107], v[8:11], v[32:35]\n"
        "s_waitcnt lgkmcnt(4)\n"
        "v_max_f32 v246, v246, v247\n"
        "v_sub_f32 v232, 0, v246\n"
        "v_mfma_f32_16x16x32_bf16 v[36:39], a[104:107], v[72:75], v[36:39]\n"
        "v_sub_f32 v233, 0, v246\n"
        "v_sub_f32 v234, 0, v246\n"
        "v_mfma_f32_16x16x32_bf16 v[184:187], a[136:139], v[80:83], v[184:187]\n"
        "ds_read_b128 a[128:131], v241 offset:4096\n"
        "v_sub_f32 v235, 0, v246\n"
        "v_add_f32 v40, v40, v232\n"
        "v_add_f32 v41, v41, v232\n"
        "v_add_f32 v42, v42, v232\n"
        "v_add_f32 v43, v43, v232\n"
        "v_mfma_f32_16x16x32_bf16 v[216:219], a[136:139], v[84:87], v[216:219]\n"
        "v_exp_f32 v40, v40\n"
        "v_mfma_f32_16x16x32_bf16 v[32:35], a[108:111], v[12:15], v[32:35]\n"
        "v_exp_f32 v41, v41\n"
        "v_mfma_f32_16x16x32_bf16 v[36:39], a[108:111], v[76:79], v[36:39]\n"
        "s_waitcnt lgkmcnt(1)\n"
        "v_cvt_pk_bf16_f32 v24, v40, v41\n"
        "v_cndmask_b32 v40, v232, v242, s[30:31]\n"
        "v_cndmask_b32 v41, v232, v242, s[56:57]\n"
        "v_mfma_f32_16x16x32_bf16 v[188:191], a[140:143], v[80:83], v[188:191]\n"
        "v_exp_f32 v42, v42\n"
        "v_mfma_f32_16x16x32_bf16 v[220:223], a[140:143], v[84:87], v[220:223]\n"
        "v_exp_f32 v43, v43\n"
        "v_mfma_f32_16x16x32_bf16 v[48:51], a[112:115], v[0:3], v[224:227]\n"
        "ds_read_b128 a[132:135], v241 offset:5120\n"
        "v_cvt_pk_bf16_f32 v25, v42, v43\n"
        "v_cndmask_b32 v42, v232, v242, s[58:59]\n"
        "v_cndmask_b32 v43, v232, v242, s[60:61]\n"
        "s_nop 1\n"
        "v_mfma_f32_16x16x32_bf16 v[40:43], a[96:99], v[96:99], v[40:43]\n"
        "v_exp_f32 v53, v53\n"
        "v_mfma_f32_16x16x32_bf16 v[40:43], a[100:103], v[100:103], v[40:43]\n"
        "ds_read_b128 a[96:99], v240 offset:16384\n"
        "v_cvt_pk_bf16_f32 v22, v52, v53\n"
        "v_mfma_f32_16x16x32_bf16 v[40:43], a[104:107], v[104:107], v[40:43]\n"
        "v_exp_f32 v54, v54\n"
        "v_mfma_f32_16x16x32_bf16 v[40:43], a[108:111], v[108:111], v[40:43]\n"
        "s_waitcnt lgkmcnt(2)\n"
        "v_exp_f32 v55, v55\n"
        "v_mfma_f32_16x16x32_bf16 v[160:163], a[144:147], v[16:19], v[160:163]\n"
        "ds_read_b128 a[100:103], v240 offset:17408\n"
        "v_cvt_pk_bf16_f32 v23, v54, v55\n"
        "s_nop 1\n"
        "v_mfma_f32_16x16x32_bf16 a[68:71], a[80:83], v[20:23], a[68:71]\n"
        "ds_read_b128 a[104:107], v240 offset:18432\n"
        "v_add_f32 v56, v56, v232\n"
        "v_mfma_f32_16x16x32_bf16 v[52:55], a[112:115], v[64:67], v[228:231]\n"
        "v_exp_f32 v56, v56\n"
        "v_mfma_f32_16x16x32_bf16 v[192:195], a[144:147], v[20:23], v[192:195]\n"
        "ds_read_b128 a[136:139], v241 offset:6144\n"
        "v_add_f32 v57, v57, v232\n"
        "v_mfma_f32_16x16x32_bf16 v[48:51], a[116:119], v[4:7], v[48:51]\n"
        "v_exp_f32 v57, v57\n"
        "v_mfma_f32_16x16x32_bf16 v[52:55], a[116:119], v[68:71], v[52:55]\n"
        "ds_read_b128 a[108:111], v240 offset:19456\n"
        "v_cvt_pk_bf16_f32 v26, v56, v57\n"
        "v_mfma_f32_16x16x32_bf16 v[164:167], a[148:151], v[16:19], v[164:167]\n"
        "v_add_f32 v58, v58, v232\n"
        "v_add_f32 v59, v59, v232\n"
        "v_mfma_f32_16x16x32_bf16 v[196:199], a[148:151], v[20:23], v[196:199]\n"
        "v_exp_f32 v58, v58\n"
        "v_mfma_f32_16x16x32_bf16 v[48:51], a[120:123], v[8:11], v[48:51]\n"
        "v_exp_f32 v59, v59\n"
        "v_mfma_f32_16x16x32_bf16 v[52:55], a[120:123], v[72:75], v[52:55]\n"
        "ds_read_b128 a[140:143], v241 offset:7168\n"
        "v_cvt_pk_bf16_f32 v27, v58, v59\n"
        "s_nop 1\n"
        "v_mfma_f32_16x16x32_bf16 a[0:3], a[144:147], v[24:27], 0\n"
        "s_waitcnt vmcnt(0) lgkmcnt(3)\n"
        "v_cmp_ne_u64 vcc, v[252:253], v[250:251]\n"
        "v_cndmask_b32 v56, v232, v242, s[62:63]\n"
        "v_mfma_f32_16x16x32_bf16 a[72:75], a[80:83], v[24:27], 0\n"
        "s_cbranch_vccnz LPKV_%=\n"
        "LBKV_%=:\n"
        "v_cndmask_b32 v57, v232, v242, s[84:85]\n"
        "v_cndmask_b32 v58, v232, v242, s[86:87]\n"
        "v_mfma_f32_16x16x32_bf16 a[4:7], a[148:151], v[24:27], 0\n"
        "ds_read_b128 a[144:147], v241 offset:8192\n"
        "v_cndmask_b32 v59, v232, v242, s[88:89]\n"
        "s_nop 1\n"
        "v_mfma_f32_16x16x32_bf16 v[56:59], a[112:115], v[96:99], v[56:59]\n"
        "v_exp_f32 v32, v32\n"
        "v_mfma_f32_16x16x32_bf16 v[56:59], a[116:119], v[100:103], v[56:59]\n"
        "v_exp_f32 v33, v33\n"
        "v_mfma_f32_16x16x32_bf16 v[56:59], a[120:123], v[104:107], v[56:59]\n"
        "ds_read_b128 a[112:115], v240 offset:20480\n"
        "v_cvt_pk_bf16_f32 v80, v32, v33\n"
        "v_mfma_f32_16x16x32_bf16 v[168:171], a[152:155], v[16:19], v[168:171]\n"
        "v_exp_f32 v34, v34\n"
        "v_mfma_f32_16x16x32_bf16 v[200:203], a[152:155], v[20:23], v[200:203]\n"
        "v_exp_f32 v35, v35\n"
        "v_mfma_f32_16x16x32_bf16 a[8:11], a[152:155], v[24:27], 0\n"
        "ds_read_b128 a[116:119], v240 offset:21504\n"
        "v_cvt_pk_bf16_f32 v81, v34, v35\n"
        "v_mfma_f32_16x16x32_bf16 v[48:51], a[124:127], v[12:15], v[48:51]\n"
        "v_exp_f32 v36, v36\n"
        "v_mfma_f32_16x16x32_bf16 v[52:55], a[124:127], v[76:79], v[52:55]\n"
        "v_exp_f32 v37, v37\n"
        "v_mfma_f32_16x16x32_bf16 v[56:59], a[124:127], v[108:111], v[56:59]\n"
        "ds_read_b128 a[148:151], v241 offset:9216\n"
        "v_cvt_pk_bf16_f32 v84, v36, v37\n"
        "v_mfma_f32_16x16x32_bf16 v[172:175], a[156:159], v[16:19], v[172:175]\n"
        "v_exp_f32 v38, v38\n"
        "v_mfma_f32_16x16x32_bf16 v[204:207], a[156:159], v[20:23], v[204:207]\n"
        "v_exp_f32 v39, v39\n"
        "v_mfma_f32_16x16x32_bf16 a[12:15], a[156:159], v[24:27], 0\n"
        "ds_read_b128 a[120:123], v240 offset:22528\n"
        "v_cvt_pk_bf16_f32 v85, v38, v39\n"
        "v_mfma_f32_16x16x32_bf16 v[32:35], a[96:99], v[0:3], v[224:227]\n"
        "v_exp_f32 v40, v40\n"
        "v_mfma_f32_16x16x32_bf16 v[36:39], a[96:99], v[64:67], v[228:231]\n"
        "v_exp_f32 v41, v41\n"
        "v_mfma_f32_16x16x32_bf16 v[176:179], a[128:131], v[16:19], v[176:179]\n"
        "ds_read_b128 a[152:155], v241 offset:10240\n"
        "v_cvt_pk_bf16_f32 v88, v40, v41\n"
        "v_mfma_f32_16x16x32_bf16 v[208:211], a[128:131], v[20:23], v[208:211]\n"
        "v_exp_f32 v42, v42\n"
        "v_mfma_f32_16x16x32_bf16 a[16:19], a[128:131], v[24:27], 0\n"
        "s_waitcnt lgkmcnt(2)\n"
        "v_exp_f32 v43, v43\n"
        "v_mfma_f32_16x16x32_bf16 v[32:35], a[100:103], v[4:7], v[32:35]\n"
        "ds_read_b128 a[124:127], v240 offset:23552\n"
        "v_cvt_pk_bf16_f32 v89, v42, v43\n"
        "v_mfma_f32_16x16x32_bf16 v[40:43], a[96:99], v[96:99], v[232:235]\n"
        "v_cndmask_b32 v45, 0, v242, s[12:13]\n"
        "v_cndmask_b32 v46, 0, v242, s[14:15]\n"
        "v_mfma_f32_16x16x32_bf16 v[36:39], a[100:103], v[68:71], v[36:39]\n"
        "ds_read_b128 a[156:159], v241 offset:11264\n"
        "v_cndmask_b32 v47, 0, v242, s[16:17]\n"
        "s_nop 1\n"
        "v_mfma_f32_16x16x32_bf16 v[44:47], a[96:99], v[128:131], v[44:47]\n"
        "v_exp_f32 v48, v48\n"
        "v_mfma_f32_16x16x32_bf16 v[40:43], a[100:103], v[100:103], v[40:43]\n"
        "v_exp_f32 v49, v49\n"
        "v_mfma_f32_16x16x32_bf16 v[44:47], a[100:103], v[132:135], v[44:47]\n"
        "ds_read_b128 a[96:99], v240 offset:24576\n"
        "v_cvt_pk_bf16_f32 v82, v48, v49\n"
        "v_mfma_f32_16x16x32_bf16 v[180:183], a[132:135], v[16:19], v[180:183]\n"
        "v_exp_f32 v50, v50\n"
        "v_mfma_f32_16x16x32_bf16 v[212:215], a[132:135], v[20:23], v[212:215]\n"
        "v_exp_f32 v51, v51\n"
        "v_mfma_f32_16x16x32_bf16 a[20:23], a[132:135], v[24:27], 0\n"
        "ds_read_b128 a[128:131], v241 offset:12288\n"
        "v_cvt_pk_bf16_f32 v83, v50, v51\n"
        "v_mfma_f32_16x16x32_bf16 v[32:35], a[104:107], v[8:11], v[32:35]\n"
        "v_exp_f32 v52, v52\n"
        "v_mfma_f32_16x16x32_bf16 v[36:39], a[104:107], v[72:75], v[36:39]\n"
        "v_exp_f32 v53, v53\n"
        "v_mfma_f32_16x16x32_bf16 v[40:43], a[104:107], v[104:107], v[40:43]\n"
        "ds_read_b128 a[100:103], v240 offset:25600\n"
        "v_cvt_pk_bf16_f32 v86, v52, v53\n"
        "v_mfma_f32_16x16x32_bf16 v[44:47], a[104:107], v[136:139], v[44:47]\n"
        "s_cmp_eq_u32 s82, s83\n"
        "s_cselect_b64 s[68:69], s[92:93], s[68:69]\n"
        "s_cselect_b32 s80, s50, s78\n"
        "v_mfma_f32_16x16x32_bf16 v[184:187], a[136:139], v[16:19], v[184:187]\n"
        "s_add_u32 m0, s80, 0x0\n"
        "v_exp_f32 v54, v54\n"
        "v_mfma_f32_16x16x32_bf16 v[216:219], a[136:139], v[20:23], v[216:219]\n"
        "global_load_lds_dwordx4 v254, s[68:69] offset:0 sc1\n"
        "ds_read_b128 a[132:135], v241 offset:13312\n"
        "v_mfma_f32_16x16x32_bf16 a[24:27], a[136:139], v[24:27], 0\n"
        "v_exp_f32 v55, v55\n"
        "v_mfma_f32_16x16x32_bf16 a[64:67], a[80:83], v[80:83], a[64:67]\n"
        "v_cvt_pk_bf16_f32 v87, v54, v55\n"
        "v_add_u32 v150, s52, v254\n"
        "v_mfma_f32_16x16x32_bf16 v[32:35], a[108:111], v[12:15], v[32:35]\n"
        "v_exp_f32 v56, v56\n"
        "v_mfma_f32_16x16x32_bf16 v[36:39], a[108:111], v[76:79], v[36:39]\n"
        "global_load_lds_dwordx4 v254, s[68:69] offset:1024 sc1\n"
        "v_mov_b32 v152, 0\n"
        "v_mfma_f32_16x16x32_bf16 v[40:43], a[108:111], v[108:111], v[40:43]\n"
        "v_exp_f32 v57, v57\n"
        "v_mfma_f32_16x16x32_bf16 v[44:47], a[108:111], v[140:143], v[44:47]\n"
        "v_cvt_pk_bf16_f32 v90, v56, v57\n"
        "v_mov_b32 v153, 0\n"
        "v_mfma_f32_16x16x32_bf16 v[188:191], a[140:143], v[16:19], v[188:191]\n"
        "v_exp_f32 v58, v58\n"
        "v_mfma_f32_16x16x32_bf16 v[220:223], a[140:143], v[20:23], v[220:223]\n"
        "global_load_lds_dwordx4 v254, s[68:69] offset:2048 sc1\n"
        "v_mov_b32 v154, 0\n"
        "v_mfma_f32_16x16x32_bf16 a[28:31], a[140:143], v[24:27], 0\n"
        "v_exp_f32 v59, v59\n"
        "v_mfma_f32_16x16x32_bf16 a[68:71], a[80:83], v[84:87], a[68:71]\n"
        "ds_read_b128 a[104:107], v240 offset:26624\n"
        "v_cvt_pk_bf16_f32 v91, v58, v59\n"
        "v_mfma_f32_16x16x32_bf16 v[48:51], a[112:115], v[0:3], v[224:227]\n"
        "v_readfirstlane_b32 s53, v112\n"
        "v_mov_b32 v155, 0\n"
        "v_mfma_f32_16x16x32_bf16 v[52:55], a[112:115], v[64:67], v[228:231]\n"
        "global_load_lds_dwordx4 v254, s[68:69] offset:3072 sc1\n"
        "s_add_u32 m0, s81, 0x0\n"
        "s_lshl_b32 s53, s53, 16\n"
        "v_mfma_f32_16x16x32_bf16 v[56:59], a[112:115], v[96:99], v[232:235]\n"
        "v_writelane_b32 v155, s53, 1\n"
        "v_alignbyte_b32 v112, v113, v112, 2\n"
        "v_mfma_f32_16x16x32_bf16 v[160:163], a[144:147], v[80:83], v[160:163]\n"
        "s_mov_b64 exec, 3\n"
        "ds_write_b128 v150, v[152:155]\n"
        "s_mov_b64 exec, -1\n"
        "v_mfma_f32_16x16x32_bf16 v[192:195], a[144:147], v[84:87], v[192:195]\n"
        "global_load_lds_dwordx4 v254, s[74:75] offset:0 sc1\n"
        "v_alignbyte_b32 v113, v114, v113, 2\n"
        "v_mfma_f32_16x16x32_bf16 a[0:3], a[144:147], v[88:91], a[0:3]\n"
        "v_alignbyte_b32 v114, v115, v114, 2\n"
        "v_alignbyte_b32 v115, v144, v115, 2\n"
        "v_mfma_f32_16x16x32_bf16 a[72:75], a[80:83], v[88:91], a[72:75]\n"
        "ds_write_b128 v150, v[112:115] offset:32\n"
        "v_mfma_f32_16x16x32_bf16 v[48:51], a[116:119], v[4:7], v[48:51]\n"
        "global_load_lds_dwordx4 v254, s[74:75] offset:1024 sc1\n"
        "v_mfma_f32_16x16x32_bf16 v[52:55], a[116:119], v[68:71], v[52:55]\n"
        "s_waitcnt lgkmcnt(2)\n"
        "v_alignbyte_b32 v116, v117, v116, 2\n"
        "v_alignbyte_b32 v117, v118, v117, 2\n"
        "v_mfma_f32_16x16x32_bf16 v[56:59], a[116:119], v[100:103], v[56:59]\n"
        "v_alignbyte_b32 v118, v119, v118, 2\n"
        "v_alignbyte_b32 v119, v145, v119, 2\n"
        "v_mfma_f32_16x16x32_bf16 v[164:167], a[148:151], v[80:83], v[164:167]\n"
        "ds_write_b128 v150, v[116:119] offset:1056\n"
        "v_mfma_f32_16x16x32_bf16 v[196:199], a[148:151], v[84:87], v[196:199]\n"
        "global_load_lds_dwordx4 v254, s[74:75] offset:2048 sc1\n"
        "v_alignbyte_b32 v120, v121, v120, 2\n"
        "v_mfma_f32_16x16x32_bf16 a[4:7], a[148:151], v[88:91], a[4:7]\n"
        "v_alignbyte_b32 v121, v122, v121, 2\n"
        "v_alignbyte_b32 v122, v123, v122, 2\n"
        "v_mfma_f32_16x16x32_bf16 v[48:51], a[120:123], v[8:11], v[48:51]\n"
        "v_alignbyte_b32 v123, v146, v123, 2\n"
        "v_alignbyte_b32 v124, v125, v124, 2\n"
        "v_mfma_f32_16x16x32_bf16 v[52:55], a[120:123], v[72:75], v[52:55]\n"
        "ds_write_b128 v150, v[120:123] offset:2080\n"
        "v_alignbyte_b32 v125, v126, v125, 2\n"
        "v_alignbyte_b32 v126, v127, v126, 2\n"
        "v_alignbyte_b32 v127, v147, v127, 2\n"
        "v_mfma_f32_16x16x32_bf16 v[56:59], a[120:123], v[104:107], v[56:59]\n"
        "global_load_lds_dwordx4 v254, s[74:75] offset:3072 sc1\n"
        "v_cndmask_b32 v60, 0, v242, s[20:21]\n"
        "v_cndmask_b32 v61, 0, v242, s[22:23]\n"
        "v_cndmask_b32 v62, 0, v242, s[24:25]\n"
        "v_cndmask_b32 v63, 0, v242, s[28:29]\n"
        "s_nop 1\n"
        "v_mfma_f32_16x16x32_bf16 v[60:63], a[112:115], v[128:131], v[60:63]\n"
        "ds_write_b128 v150, v[124:127] offset:3104\n"
        "v_mfma_f32_16x16x32_bf16 v[60:63], a[116:119], v[132:135], v[60:63]\n"
        "v_exp_f32 v32, v32\n"
        "v_mfma_f32_16x16x32_bf16 v[60:63], a[120:123], v[136:139], v[60:63]\n"
        "v_exp_f32 v33, v33\n"
        "v_mfma_f32_16x16x32_bf16 v[168:171], a[152:155], v[80:83], v[168:171]\n"
        "ds_read_b128 a[136:139], v241 offset:14336\n"
        "s_add_u32 m0, s79, 0x0\n"
        "v_cvt_pk_bf16_f32 v16, v32, v33\n"
        "v_mfma_f32_16x16x32_bf16 v[200:203], a[152:155], v[84:87], v[200:203]\n"
        "v_exp_f32 v34, v34\n"
        "v_mfma_f32_16x16x32_bf16 a[8:11], a[152:155], v[88:91], a[8:11]\n"
        "v_exp_f32 v35, v35\n"
        "v_mfma_f32_16x16x32_bf16 v[48:51], a[124:127], v[12:15], v[48:51]\n"
        "ds_read_b128 a[108:111], v240 offset:27648\n"
        "v_cvt_pk_bf16_f32 v17, v34, v35\n"
        "v_mfma_f32_16x16x32_bf16 v[52:55], a[124:127], v[76:79], v[52:55]\n"
        "v_exp_f32 v36, v36\n"
        "v_mfma_f32_16x16x32_bf16 v[56:59], a[124:127], v[108:111], v[56:59]\n"
        "v_exp_f32 v37, v37\n"
        "v_mfma_f32_16x16x32_bf16 v[60:63], a[124:127], v[140:143], v[60:63]\n"
        "ds_read_b128 a[140:143], v241 offset:15360\n"
        "v_cvt_pk_bf16_f32 v20, v36, v37\n"
        "v_mfma_f32_16x16x32_bf16 v[172:175], a[156:159], v[80:83], v[172:175]\n"
        "v_exp_f32 v38, v38\n"
        "v_mfma_f32_16x16x32_bf16 v[204:207], a[156:159], v[84:87], v[204:207]\n"
        "v_exp_f32 v39, v39\n"
        "v_mfma_f32_16x16x32_bf16 a[12:15], a[156:159], v[88:91], a[12:15]\n"
        "global_load_lds_dwordx4 v254, s[76:77] offset:0 sc1\n"
        "v_cvt_pk_bf16_f32 v21, v38, v39\n"
        "v_mfma_f32_16x16x32_bf16 v[32:35], a[96:99], v[0:3], v[224:227]\n"
        "ds_read_b128 a[112:115], v240 offset:28672\n"
        "v_max_f32 v248, v44, v45\n"
        "v_mfma_f32_16x16x32_bf16 v[36:39], a[96:99], v[64:67], v[228:231]\n"
        "v_exp_f32 v40, v40\n"
        "v_mfma_f32_16x16x32_bf16 v[176:179], a[128:131], v[80:83], v[176:179]\n"
        "v_exp_f32 v41, v41\n"
        "v_mfma_f32_16x16x32_bf16 v[208:211], a[128:131], v[84:87], v[208:211]\n"
        "global_load_lds_dwordx4 v254, s[76:77] offset:1024 sc1\n"
        "v_cvt_pk_bf16_f32 v24, v40, v41\n"
        "v_mfma_f32_16x16x32_bf16 a[16:19], a[128:131], v[88:91], a[16:19]\n"
        "s_waitcnt lgkmcnt(1)\n"
        "v_exp_f32 v42, v42\n"
        "v_mfma_f32_16x16x32_bf16 v[32:35], a[100:103], v[4:7], v[32:35]\n"
        "v_exp_f32 v43, v43\n"
        "v_mfma_f32_16x16x32_bf16 v[36:39], a[100:103], v[68:71], v[36:39]\n"
        "ds_read_b128 a[144:147], v241 offset:16384\n"
        "v_cvt_pk_bf16_f32 v25, v42, v43\n"
        "v_mfma_f32_16x16x32_bf16 v[40:43], a[96:99], v[96:99], v[232:235]\n"
        "global_load_lds_dwordx4 v254, s[76:77] offset:2048 sc1\n"
        "ds_read_b128 a[116:119], v240 offset:29696\n"
        "v_mfma_f32_16x16x32_bf16 v[40:43], a[100:103], v[100:103], v[40:43]\n"
        "ds_read_b128 a[148:151], v241 offset:17408\n"
        "v_max3_f32 v248, v46, v47, v248\n"
        "v_mfma_f32_16x16x32_bf16 v[180:183], a[132:135], v[80:83], v[180:183]\n"
        "ds_read_b128 a[120:123], v240 offset:30720\n"
        "v_mov_b32 v249, v248\n"
        "v_mfma_f32_16x16x32_bf16 v[212:215], a[132:135], v[84:87], v[212:215]\n"
        "v_exp_f32 v48, v48\n"
        "v_mfma_f32_16x16x32_bf16 a[20:23], a[132:135], v[88:91], a[20:23]\n"
        "v_permlane16_swap_b32 v248, v249\n"
        "v_max_f32 v248, v248, v249\n"
        "v_mov_b32 v249, v248\n"
        "v_mfma_f32_16x16x32_bf16 v[32:35], a[104:107], v[8:11], v[32:35]\n"
        "global_load_lds_dwordx4 v254, s[76:77] offset:3072 sc1\n"
        "s_add_u32 s76, s76, 0x1000\n"
        "s_addc_u32 s77, s77, 0\n"
        "v_mfma_f32_16x16x32_bf16 v[36:39], a[104:107], v[72:75], v[36:39]\n"
        "s_add_u32 m0, s79, 0x1000\n"
        "v_permlane32_swap_b32 v248, v249\n"
        "v_max_f32 v248, v248, v249\n"
        "v_sub_f32 v236, 0, v248\n"
        "v_sub_f32 v237, 0, v248\n"
        "v_sub_f32 v238, 0, v248\n"
        "v_sub_f32 v239, 0, v248\n"
        "v_add_f32 v44, v44, v236\n"
        "v_add_f32 v45, v45, v236\n"
        "v_add_f32 v46, v46, v236\n"
        "v_add_f32 v47, v47, v236\n"
        "v_mfma_f32_16x16x32_bf16 v[40:43], a[104:107], v[104:107], v[40:43]\n"
        "v_exp_f32 v44, v44\n"
        "v_mfma_f32_16x16x32_bf16 v[184:187], a[136:139], v[80:83], v[184:187]\n"
        "v_exp_f32 v45, v45\n"
        "v_mfma_f32_16x16x32_bf16 v[216:219], a[136:139], v[84:87], v[216:219]\n"
        "global_load_lds_dwordx4 v254, s[76:77] offset:0 sc1\n"
        "v_cvt_pk_bf16_f32 v28, v44, v45\n"
        "v_cndmask_b32 v44, v236, v242, s[30:31]\n"
        "v_cndmask_b32 v45, v236, v242, s[56:57]\n"
        "v_mfma_f32_16x16x32_bf16 a[24:27], a[136:139], v[88:91], a[24:27]\n"
        "v_exp_f32 v46, v46\n"
        "v_mfma_f32_16x16x32_bf16 v[32:35], a[108:111], v[12:15], v[32:35]\n"
        "v_exp_f32 v47, v47\n"
        "v_mfma_f32_16x16x32_bf16 v[36:39], a[108:111], v[76:79], v[36:39]\n"
        "ds_read_b128 a[152:155], v241 offset:18432\n"
        "v_cvt_pk_bf16_f32 v29, v46, v47\n"
        "v_cndmask_b32 v46, v236, v242, s[58:59]\n"
        "v_cndmask_b32 v47, v236, v242, s[60:61]\n"
        "s_nop 1\n"
        "v_mfma_f32_16x16x32_bf16 v[44:47], a[96:99], v[128:131], v[44:47]\n"
        "v_exp_f32 v49, v49\n"
        "v_mfma_f32_16x16x32_bf16 v[44:47], a[100:103], v[132:135], v[44:47]\n"
        "global_load_lds_dwordx4 v254, s[76:77] offset:1024 sc1\n"
        "s_add_u32 s82, s82, 1\n"
        "v_cvt_pk_bf16_f32 v18, v48, v49\n"
        "v_mfma_f32_16x16x32_bf16 v[44:47], a[104:107], v[136:139], v[44:47]\n"
        "s_xor_b32 s78, s78, 0x8000\n"
        "v_exp_f32 v50, v50\n"
        "v_mfma_f32_16x16x32_bf16 v[40:43], a[108:111], v[108:111], v[40:43]\n"
        "s_add_u32 s81, s78, 4096\n"
        "v_exp_f32 v51, v51\n"
        "v_mfma_f32_16x16x32_bf16 v[44:47], a[108:111], v[140:143], v[44:47]\n"
        "ds_read_b128 a[124:127], v240 offset:31744\n"
        "v_cvt_pk_bf16_f32 v19, v50, v51\n"
        "s_nop 1\n"
        "v_mfma_f32_16x16x32_bf16 a[64:67], a[80:83], v[16:19], a[64:67]\n"
        "global_load_lds_dwordx4 v254, s[76:77] offset:2048 sc1\n"
        "ds_read_b128 a[156:159], v241 offset:19456\n"
        "v_mfma_f32_16x16x32_bf16 v[188:191], a[140:143], v[80:83], v[188:191]\n"
        "s_waitcnt lgkmcnt(2)\n"
        "v_exp_f32 v52, v52\n"
        "v_mfma_f32_16x16x32_bf16 v[220:223], a[140:143], v[84:87], v[220:223]\n"
        "v_exp_f32 v53, v53\n"
        "v_mfma_f32_16x16x32_bf16 a[28:31], a[140:143], v[88:91], a[28:31]\n"
        "s_cmp_ge_u32 s82, s44\n"
        "s_addc_u32 s90, s82, 0\n"
        "v_cvt_pk_bf16_f32 v22, v52, v53\n"
        "v_mfma_f32_16x16x32_bf16 v[48:51], a[112:115], v[0:3], v[224:227]\n"
        "s_min_u32 s90, s90, 15\n"
        "v_exp_f32 v54, v54\n"
        "v_xor_b32 v240, 0x8000, v240\n"
        "v_mfma_f32_16x16x32_bf16 v[160:163], a[144:147], v[16:19], v[160:163]\n"
        "s_lshl_b32 s90, s90, 15\n"
        "v_exp_f32 v55, v55\n"
        "v_mfma_f32_16x16x32_bf16 v[48:51], a[116:119], v[4:7], v[48:51]\n"
        "global_load_lds_dwordx4 v254, s[76:77] offset:3072 sc1\n"
        "v_cvt_pk_bf16_f32 v23, v54, v55\n"
        "s_nop 1\n"
        "v_mfma_f32_16x16x32_bf16 a[68:71], a[80:83], v[20:23], a[68:71]\n"
        "s_add_u32 s68, s64, s90\n"
        "s_addc_u32 s69, s65, 0\n"
        "v_add_f32 v60, v60, v236\n"
        "v_mfma_f32_16x16x32_bf16 v[52:55], a[112:115], v[64:67], v[228:231]\n"
        "s_add_u32 s74, s68, 0x1000\n"
        "s_addc_u32 s75, s69, 0\n"
        "v_add_f32 v61, v61, v236\n"
        "v_mfma_f32_16x16x32_bf16 v[192:195], a[144:147], v[20:23], v[192:195]\n"
        "s_cmp_eq_u32 s82, s83\n"
        "s_cselect_b64 s[68:69], s[92:93], s[68:69]\n"
        "s_cselect_b32 s80, s50, s78\n"
        "v_mfma_f32_16x16x32_bf16 v[52:55], a[116:119], v[68:71], v[52:55]\n"
        "s_waitcnt vmcnt(8) lgkmcnt(1)\n"
        "s_barrier\n"
        "s_add_u32 m0, s80, 0x0\n"
        "v_add_f32 v62, v62, v236\n"
        "v_add_f32 v63, v63, v236\n"
        "v_mfma_f32_16x16x32_bf16 v[164:167], a[148:151], v[16:19], v[164:167]\n"
        "s_cmp_eq_u32 s82, s83\n"
        "s_cselect_b64 s[74:75], s[94:95], s[74:75]\n"
        "s_cselect_b32 s81, s51, s81\n"
        "v_mfma_f32_16x16x32_bf16 v[196:199], a[148:151], v[20:23], v[196:199]\n"
        "global_load_lds_dwordx4 v254, s[68:69] offset:0 sc1\n"
        "ds_read_b128 a[96:99], v240 offset:0\n"
        "v_mfma_f32_16x16x32_bf16 v[48:51], a[120:123], v[8:11], v[48:51]\n"
        "s_waitcnt lgkmcnt(1)\n"
        "v_exp_f32 v56, v56\n"
        "v_mfma_f32_16x16x32_bf16 v[52:55], a[120:123], v[72:75], v[52:55]\n"
        "v_exp_f32 v57, v57\n"
        "v_exp_f32 v58, v58\n"
        "v_cvt_pk_bf16_f32 v26, v56, v57\n"
        "v_exp_f32 v59, v59\n"
        "v_mfma_f32_16x16x32_bf16 v[168:171], a[152:155], v[16:19], v[168:171]\n"
        "ds_read_b128 a[128:131], v241 offset:20480\n"
        "v_cvt_pk_bf16_f32 v27, v58, v59\n"
        "s_nop 1\n"
        "v_mfma_f32_16x16x32_bf16 a[72:75], a[80:83], v[24:27], a[72:75]\n"
        "global_load_lds_dwordx4 v254, s[68:69] offset:1024 sc1\n"
        "ds_read_b128 a[100:103], v240 offset:1024\n"
        "v_mfma_f32_16x16x32_bf16 v[56:59], a[112:115], v[96:99], v[232:235]\n"
        "v_exp_f32 v60, v60\n"
        "v_mfma_f32_16x16x32_bf16 a[0:3], a[144:147], v[24:27], a[0:3]\n"
        "v_exp_f32 v61, v61\n"
        "v_mfma_f32_16x16x32_bf16 v[56:59], a[116:119], v[100:103], v[56:59]\n"
        "ds_read_b128 a[132:135], v241 offset:21504\n"
        "v_cvt_pk_bf16_f32 v30, v60, v61\n"
        "v_mfma_f32_16x16x32_bf16 a[4:7], a[148:151], v[24:27], a[4:7]\n"
        "v_exp_f32 v62, v62\n"
        "v_cndmask_b32 v60, v236, v242, s[62:63]\n"
        "v_cndmask_b32 v61, v236, v242, s[84:85]\n"
        "v_mfma_f32_16x16x32_bf16 v[56:59], a[120:123], v[104:107], v[56:59]\n"
        "v_exp_f32 v63, v63\n"
        "v_mfma_f32_16x16x32_bf16 v[200:203], a[152:155], v[20:23], v[200:203]\n"
        "global_load_lds_dwordx4 v254, s[68:69] offset:2048 sc1\n"
        "v_cvt_pk_bf16_f32 v31, v62, v63\n"
        "s_nop 1\n"
        "v_mfma_f32_16x16x32_bf16 a[32:35], a[144:147], v[28:31], 0\n"
        "ds_read_b128 a[104:107], v240 offset:2048\n"
        "v_cndmask_b32 v62, v236, v242, s[86:87]\n"
        "v_mfma_f32_16x16x32_bf16 a[76:79], a[80:83], v[28:31], 0\n"
        "ds_read_b128 a[136:139], v241 offset:22528\n"
        "v_cndmask_b32 v63, v236, v242, s[88:89]\n"
        "s_nop 1\n"
        "v_mfma_f32_16x16x32_bf16 v[60:63], a[112:115], v[128:131], v[60:63]\n"
        "global_load_lds_dwordx4 v254, s[68:69] offset:3072 sc1\n"
        "ds_read_b128 a[108:111], v240 offset:3072\n"
        "v_mfma_f32_16x16x32_bf16 v[60:63], a[116:119], v[132:135], v[60:63]\n"
        "v_exp_f32 v32, v32\n"
        "v_mfma_f32_16x16x32_bf16 a[36:39], a[148:151], v[28:31], 0\n"
        "v_exp_f32 v33, v33\n"
        "v_mfma_f32_16x16x32_bf16 v[60:63], a[120:123], v[136:139], v[60:63]\n"
        "ds_read_b128 a[140:143], v241 offset:23552\n"
        "v_cvt_pk_bf16_f32 v80, v32, v33\n"
        "v_mfma_f32_16x16x32_bf16 a[8:11], a[152:155], v[24:27], a[8:11]\n"
        "v_exp_f32 v34, v34\n"
        "v_mfma_f32_16x16x32_bf16 a[40:43], a[152:155], v[28:31], 0\n"
        "v_exp_f32 v35, v35\n"
        "v_mfma_f32_16x16x32_bf16 v[48:51], a[124:127], v[12:15], v[48:51]\n"
        "ds_read_b128 a[112:115], v240 offset:4096\n"
        "v_cvt_pk_bf16_f32 v81, v34, v35\n"
        "v_mfma_f32_16x16x32_bf16 v[52:55], a[124:127], v[76:79], v[52:55]\n"
        "v_exp_f32 v36, v36\n"
        "v_mfma_f32_16x16x32_bf16 v[56:59], a[124:127], v[108:111], v[56:59]\n"
        "v_exp_f32 v37, v37\n"
        "v_mfma_f32_16x16x32_bf16 v[60:63], a[124:127], v[140:143], v[60:63]\n"
        "ds_read_b128 a[144:147], v241 offset:24576\n"
        "v_cvt_pk_bf16_f32 v84, v36, v37\n"
        "v_mfma_f32_16x16x32_bf16 v[172:175], a[156:159], v[16:19], v[172:175]\n"
        "v_exp_f32 v38, v38\n"
        "v_mfma_f32_16x16x32_bf16 v[204:207], a[156:159], v[20:23], v[204:207]\n"
        "v_exp_f32 v39, v39\n"
        "v_mfma_f32_16x16x32_bf16 a[12:15], a[156:159], v[24:27], a[12:15]\n"
        "ds_read_b128 a[116:119], v240 offset:5120\n"
        "v_cvt_pk_bf16_f32 v85, v38, v39\n"
        "v_mfma_f32_16x16x32_bf16 a[44:47], a[156:159], v[28:31], 0\n"
        "v_exp_f32 v40, v40\n"
        "v_exp_f32 v41, v41\n"
        "v_exp_f32 v42, v42\n"
        "v_cvt_pk_bf16_f32 v88, v40, v41\n"
        "v_exp_f32 v43, v43\n"
        "v_exp_f32 v44, v44\n"
        "v_cvt_pk_bf16_f32 v89, v42, v43\n"
        "v_exp_f32 v45, v45\n"
        "v_exp_f32 v46, v46\n"
        "v_cvt_pk_bf16_f32 v92, v44, v45\n"
        "v_exp_f32 v47, v47\n"
        "ds_read_b128 a[148:151], v241 offset:25600\n"
        "v_cvt_pk_bf16_f32 v93, v46, v47\n"
        "s_mov_b32 s91, s44\n"
        "s_waitcnt lgkmcnt(10)\n"
        "s_cmp_eq_u32 s91, 0\n"
        "s_cbranch_scc1 LLD_%=\n"
        "; ---- DUAL\n"
        "LDU_%=:\n"
        "v_mfma_f32_16x16x32_bf16 v[32:35], a[96:99], v[0:3], v[224:227]\n"
        "s_add_u32 m0, s81, 0x0\n"
        "ds_read_b128 a[120:123], v240 offset:6144\n"
        "v_mfma_f32_16x16x32_bf16 v[36:39], a[96:99], v[64:67], v[228:231]\n"
        "global_load_lds_dwordx4 v254, s[74:75] offset:0 sc1\n"
        "ds_read_b128 a[152:155], v241 offset:26624\n"
        "v_mfma_f32_16x16x32_bf16 v[40:43], a[96:99], v[96:99], v[232:235]\n"
        "s_waitcnt lgkmcnt(4)\n"
        "v_exp_f32 v48, v48\n"
        "v_mfma_f32_16x16x32_bf16 v[44:47], a[96:99], v[128:131], v[236:239]\n"
        "v_exp_f32 v49, v49\n"
        "v_mfma_f32_16x16x32_bf16 v[176:179], a[128:131], v[16:19], v[176:179]\n"
        "ds_read_b128 a[124:127], v240 offset:7168\n"
        "v_cvt_pk_bf16_f32 v82, v48, v49\n"
        "v_mfma_f32_16x16x32_bf16 v[208:211], a[128:131], v[20:23], v[208:211]\n"
        "global_load_lds_dwordx4 v254, s[74:75] offset:1024 sc1\n"
        "ds_read_b128 a[156:159], v241 offset:27648\n"
        "v_mfma_f32_16x16x32_bf16 a[16:19], a[128:131], v[24:27], a[16:19]\n"
        "v_exp_f32 v50, v50\n"
        "v_mfma_f32_16x16x32_bf16 a[48:51], a[128:131], v[28:31], a[48:51]\n"
        "v_exp_f32 v51, v51\n"
        "v_mfma_f32_16x16x32_bf16 v[32:35], a[100:103], v[4:7], v[32:35]\n"
        "ds_read_b128 a[96:99], v240 offset:8192\n"
        "v_cvt_pk_bf16_f32 v83, v50, v51\n"
        "v_mfma_f32_16x16x32_bf16 v[36:39], a[100:103], v[68:71], v[36:39]\n"
        "global_load_lds_dwordx4 v254, s[74:75] offset:2048 sc1\n"
        "ds_read_b128 a[128:131], v241 offset:28672\n"
        "v_mfma_f32_16x16x32_bf16 v[40:43], a[100:103], v[100:103], v[40:43]\n"
        "v_exp_f32 v52, v52\n"
        "v_mfma_f32_16x16x32_bf16 v[44:47], a[100:103], v[132:135], v[44:47]\n"
        "v_exp_f32 v53, v53\n"
        "v_mfma_f32_16x16x32_bf16 v[180:183], a[132:135], v[16:19], v[180:183]\n"
        "ds_read_b128 a[100:103], v240 offset:9216\n"
        "v_cvt_pk_bf16_f32 v86, v52, v53\n"
        "v_mfma_f32_16x16x32_bf16 v[212:215], a[132:135], v[20:23], v[212:215]\n"
        "global_load_lds_dwordx4 v254, s[74:75] offset:3072 sc1\n"
        "v_mfma_f32_16x16x32_bf16 a[20:23], a[132:135], v[24:27], a[20:23]\n"
        "v_exp_f32 v54, v54\n"
        "v_mfma_f32_16x16x32_bf16 a[52:55], a[132:135], v[28:31], a[52:55]\n"
        "v_exp_f32 v55, v55\n"
        "v_mfma_f32_16x16x32_bf16 a[64:67], a[80:83], v[80:83], a[64:67]\n"
        "ds_read_b128 a[132:135], v241 offset:29696\n"
        "v_cvt_pk_bf16_f32 v87, v54, v55\n"
        "v_mfma_f32_16x16x32_bf16 v[32:35], a[104:107], v[8:11], v[32:35]\n"
        "s_xor_b32 s79, s79, 0x8000\n"
        "v_exp_f32 v56, v56\n"
        "v_mfma_f32_16x16x32_bf16 v[36:39], a[104:107], v[72:75], v[36:39]\n"
        "s_add_u32 m0, s79, 0x0\n"
        "v_exp_f32 v57, v57\n"
        "v_mfma_f32_16x16x32_bf16 v[40:43], a[104:107], v[104:107], v[40:43]\n"
        "s_add_u32 s76, s66, s90\n"
        "s_addc_u32 s77, s67, 0\n"
        "v_cvt_pk_bf16_f32 v90, v56, v57\n"
        "v_mfma_f32_16x16x32_bf16 v[44:47], a[104:107], v[136:139], v[44:47]\n"
        "v_exp_f32 v58, v58\n"
        "v_mfma_f32_16x16x32_bf16 v[184:187], a[136:139], v[16:19], v[184:187]\n"
        "v_exp_f32 v59, v59\n"
        "v_mfma_f32_16x16x32_bf16 v[216:219], a[136:139], v[20:23], v[216:219]\n"
        "ds_read_b128 a[104:107], v240 offset:10240\n"
        "v_cvt_pk_bf16_f32 v91, v58, v59\n"
        "v_mfma_f32_16x16x32_bf16 a[24:27], a[136:139], v[24:27], a[24:27]\n"
        "v_exp_f32 v60, v60\n"
        "v_mfma_f32_16x16x32_bf16 a[56:59], a[136:139], v[28:31], a[56:59]\n"
        "s_add_u32 s82, s82, 1\n"
        "v_exp_f32 v61, v61\n"
        "v_mfma_f32_16x16x32_bf16 a[68:71], a[80:83], v[84:87], a[68:71]\n"
        "ds_read_b128 a[136:139], v241 offset:30720\n"
        "s_xor_b32 s78, s78, 0x8000\n"
        "v_cvt_pk_bf16_f32 v94, v60, v61\n"
        "v_mfma_f32_16x16x32_bf16 v[32:35], a[108:111], v[12:15], v[32:35]\n"
        "s_add_u32 s81, s78, 4096\n"
        "v_exp_f32 v62, v62\n"
        "v_mfma_f32_16x16x32_bf16 v[36:39], a[108:111], v[76:79], v[36:39]\n"
        "v_exp_f32 v63, v63\n"
        "v_mfma_f32_16x16x32_bf16 v[40:43], a[108:111], v[108:111], v[40:43]\n"
        "s_cmp_ge_u32 s82, s44\n"
        "s_addc_u32 s90, s82, 0\n"
        "v_cvt_pk_bf16_f32 v95, v62, v63\n"
        "v_mfma_f32_16x16x32_bf16 v[44:47], a[108:111], v[140:143], v[44:47]\n"
        "s_min_u32 s90, s90, 15\n"
        "v_exp_f32 v32, v32\n"
        "v_mfma_f32_16x16x32_bf16 v[188:191], a[140:143], v[16:19], v[188:191]\n"
        "v_exp_f32 v33, v33\n"
        "v_mfma_f32_16x16x32_bf16 v[220:223], a[140:143], v[20:23], v[220:223]\n"
        "ds_read_b128 a[108:111], v240 offset:11264\n"
        "v_cvt_pk_bf16_f32 v16, v32, v33\n"
        "v_mfma_f32_16x16x32_bf16 a[28:31], a[140:143], v[24:27], a[28:31]\n"
        "v_exp_f32 v34, v34\n"
        "v_mfma_f32_16x16x32_bf16 a[60:63], a[140:143], v[28:31], a[60:63]\n"
        "v_exp_f32 v35, v35\n"
        "v_mfma_f32_16x16x32_bf16 a[72:75], a[80:83], v[88:91], a[72:75]\n"
        "ds_read_b128 a[140:143], v241 offset:31744\n"
        "v_cvt_pk_bf16_f32 v17, v34, v35\n"
        "v_mfma_f32_16x16x32_bf16 v[48:51], a[112:115], v[0:3], v[224:227]\n"
        "v_exp_f32 v36, v36\n"
        "v_mfma_f32_16x16x32_bf16 v[52:55], a[112:115], v[64:67], v[228:231]\n"
        "v_exp_f32 v37, v37\n"
        "v_mfma_f32_16x16x32_bf16 v[56:59], a[112:115], v[96:99], v[232:235]\n"
        "v_cvt_pk_bf16_f32 v20, v36, v37\n"
        "v_xor_b32 v241, 0x8000, v241\n"
        "v_mfma_f32_16x16x32_bf16 v[60:63], a[112:115], v[128:131], v[236:239]\n"
        "s_waitcnt lgkmcnt(2)\n"
        "v_exp_f32 v38, v38\n"
        "v_mfma_f32_16x16x32_bf16 v[160:163], a[144:147], v[80:83], v[160:163]\n"
        "v_exp_f32 v39, v39\n"
        "v_mfma_f32_16x16x32_bf16 v[192:195], a[144:147], v[84:87], v[192:195]\n"
        "ds_read_b128 a[112:115], v240 offset:12288\n"
        "s_lshl_b32 s90, s90, 15\n"
        "v_cvt_pk_bf16_f32 v21, v38, v39\n"
        "v_mfma_f32_16x16x32_bf16 a[0:3], a[144:147], v[88:91], a[0:3]\n"
        "v_exp_f32 v40, v40\n"
        "v_mfma_f32_16x16x32_bf16 a[32:35], a[144:147], v[92:95], a[32:35]\n"
        "s_waitcnt vmcnt(8) lgkmcnt(1)\n"
        "v_exp_f32 v41, v41\n"
        "v_mfma_f32_16x16x32_bf16 a[76:79], a[80:83], v[92:95], a[76:79]\n"
        "s_barrier\n"
        "v_cvt_pk_bf16_f32 v24, v40, v41\n"
        "v_mfma_f32_16x16x32_bf16 v[48:51], a[116:119], v[4:7], v[48:51]\n"
        "v_exp_f32 v42, v42\n"
        "v_mfma_f32_16x16x32_bf16 v[52:55], a[116:119], v[68:71], v[52:55]\n"
        "v_exp_f32 v43, v43\n"
        "v_mfma_f32_16x16x32_bf16 v[56:59], a[116:119], v[100:103], v[56:59]\n"
        "global_load_lds_dwordx4 v254, s[76:77] offset:0 sc1\n"
        "v_cvt_pk_bf16_f32 v25, v42, v43\n"
        "v_mfma_f32_16x16x32_bf16 v[60:63], a[116:119], v[132:135], v[60:63]\n"
        "ds_read_b128 a[144:147], v241 offset:0\n"
        "s_add_u32 s68, s64, s90\n"
        "s_addc_u32 s69, s65, 0\n"
        "v_mfma_f32_16x16x32_bf16 v[164:167], a[148:151], v[80:83], v[164:167]\n"
        "ds_read_b128 a[116:119], v240 offset:13312\n"
        "s_add_u32 s74, s68, 0x1000\n"
        "s_addc_u32 s75, s69, 0\n"
        "v_mfma_f32_16x16x32_bf16 v[196:199], a[148:151], v[84:87], v[196:199]\n"
        "v_exp_f32 v44, v44\n"
        "v_mfma_f32_16x16x32_bf16 a[4:7], a[148:151], v[88:91], a[4:7]\n"
        "v_exp_f32 v45, v45\n"
        "v_mfma_f32_16x16x32_bf16 a[36:39], a[148:151], v[92:95], a[36:39]\n"
        "global_load_lds_dwordx4 v254, s[76:77] offset:1024 sc1\n"
        "v_cvt_pk_bf16_f32 v28, v44, v45\n"
        "v_mfma_f32_16x16x32_bf16 v[48:51], a[120:123], v[8:11], v[48:51]\n"
        "v_exp_f32 v46, v46\n"
        "v_mfma_f32_16x16x32_bf16 v[52:55], a[120:123], v[72:75], v[52:55]\n"
        "v_exp_f32 v47, v47\n"
        "v_mfma_f32_16x16x32_bf16 v[56:59], a[120:123], v[104:107], v[56:59]\n"
        "ds_read_b128 a[148:151], v241 offset:1024\n"
        "v_cvt_pk_bf16_f32 v29, v46, v47\n"
        "v_mfma_f32_16x16x32_bf16 v[60:63], a[120:123], v[136:139], v[60:63]\n"
        "global_load_lds_dwordx4 v254, s[76:77] offset:2048 sc1\n"
        "ds_read_b128 a[120:123], v240 offset:14336\n"
        "v_mfma_f32_16x16x32_bf16 v[168:171], a[152:155], v[80:83], v[168:171]\n"
        "s_cmp_eq_u32 s82, s83\n"
        "s_cselect_b64 s[68:69], s[92:93], s[68:69]\n"
        "s_cselect_b32 s80, s50, s78\n"
        "v_mfma_f32_16x16x32_bf16 v[200:203], a[152:155], v[84:87], v[200:203]\n"
        "s_cmp_eq_u32 s82, s83\n"
        "s_cselect_b64 s[74:75], s[94:95], s[74:75]\n"
        "s_cselect_b32 s81, s51, s81\n"
        "v_mfma_f32_16x16x32_bf16 a[8:11], a[152:155], v[88:91], a[8:11]\n"
        "v_mfma_f32_16x16x32_bf16 a[40:43], a[152:155], v[92:95], a[40:43]\n"
        "global_load_lds_dwordx4 v254, s[76:77] offset:3072 sc1\n"
        "ds_read_b128 a[152:155], v241 offset:2048\n"
        "s_add_u32 m0, s79, 0x1000\n"
        "v_mfma_f32_16x16x32_bf16 v[48:51], a[124:127], v[12:15], v[48:51]\n"
        "s_add_u32 s76, s76, 0x1000\n"
        "s_addc_u32 s77, s77, 0\n"
        "v_mfma_f32_16x16x32_bf16 v[52:55], a[124:127], v[76:79], v[52:55]\n"
        "v_mfma_f32_16x16x32_bf16 v[56:59], a[124:127], v[108:111], v[56:59]\n"
        "v_mfma_f32_16x16x32_bf16 v[60:63], a[124:127], v[140:143], v[60:63]\n"
        "global_load_lds_dwordx4 v254, s[76:77] offset:0 sc1\n"
        "ds_read_b128 a[124:127], v240 offset:15360\n"
        "v_mfma_f32_16x16x32_bf16 v[172:175], a[156:159], v[80:83], v[172:175]\n"
        "v_mfma_f32_16x16x32_bf16 v[204:207], a[156:159], v[84:87], v[204:207]\n"
        "v_exp_f32 v48, v48\n"
        "v_mfma_f32_16x16x32_bf16 a[12:15], a[156:159], v[88:91], a[12:15]\n"
        "v_exp_f32 v49, v49\n"
        "v_mfma_f32_16x16x32_bf16 a[44:47], a[156:159], v[92:95], a[44:47]\n"
        "global_load_lds_dwordx4 v254, s[76:77] offset:1024 sc1\n"
        "v_cvt_pk_bf16_f32 v18, v48, v49\n"
        "v_mfma_f32_16x16x32_bf16 v[32:35], a[96:99], v[0:3], v[224:227]\n"
        "v_exp_f32 v50, v50\n"
        "v_mfma_f32_16x16x32_bf16 v[36:39], a[96:99], v[64:67], v[228:231]\n"
        "v_exp_f32 v51, v51\n"
        "v_mfma_f32_16x16x32_bf16 v[40:43], a[96:99], v[96:99], v[232:235]\n"
        "ds_read_b128 a[156:159], v241 offset:3072\n"
        "v_cvt_pk_bf16_f32 v19, v50, v51\n"
        "v_mfma_f32_16x16x32_bf16 v[44:47], a[96:99], v[128:131], v[236:239]\n"
        "v_exp_f32 v52, v52\n"
        "v_mfma_f32_16x16x32_bf16 v[176:179], a[128:131], v[80:83], v[176:179]\n"
        "v_exp_f32 v53, v53\n"
        "v_mfma_f32_16x16x32_bf16 v[208:211], a[128:131], v[84:87], v[208:211]\n"
        "global_load_lds_dwordx4 v254, s[76:77] offset:2048 sc1\n"
        "v_cvt_pk_bf16_f32 v22, v52, v53\n"
        "v_mfma_f32_16x16x32_bf16 a[16:19], a[128:131], v[88:91], a[16:19]\n"
        "v_exp_f32 v54, v54\n"
        "v_mfma_f32_16x16x32_bf16 a[48:51], a[128:131], v[92:95], a[48:51]\n"
        "v_exp_f32 v55, v55\n"
        "v_mfma_f32_16x16x32_bf16 v[32:35], a[100:103], v[4:7], v[32:35]\n"
        "ds_read_b128 a[96:99], v240 offset:16384\n"
        "v_cvt_pk_bf16_f32 v23, v54, v55\n"
        "v_mfma_f32_16x16x32_bf16 v[36:39], a[100:103], v[68:71], v[36:39]\n"
        "global_load_lds_dwordx4 v254, s[76:77] offset:3072 sc1\n"
        "ds_read_b128 a[128:131], v241 offset:4096\n"
        "s_add_u32 m0, s80, 0x0\n"
        "v_mfma_f32_16x16x32_bf16 v[40:43], a[100:103], v[100:103], v[40:43]\n"
        "s_sub_u32 s91, s91, 1\n"
        "v_exp_f32 v56, v56\n"
        "v_mfma_f32_16x16x32_bf16 v[44:47], a[100:103], v[132:135], v[44:47]\n"
        "v_exp_f32 v57, v57\n"
        "v_mfma_f32_16x16x32_bf16 v[180:183], a[132:135], v[80:83], v[180:183]\n"
        "ds_read_b128 a[100:103], v240 offset:17408\n"
        "v_cvt_pk_bf16_f32 v26, v56, v57\n"
        "v_mfma_f32_16x16x32_bf16 v[212:215], a[132:135], v[84:87], v[212:215]\n"
        "v_exp_f32 v58, v58\n"
        "v_mfma_f32_16x16x32_bf16 a[20:23], a[132:135], v[88:91], a[20:23]\n"
        "v_exp_f32 v59, v59\n"
        "v_mfma_f32_16x16x32_bf16 a[52:55], a[132:135], v[92:95], a[52:55]\n"
        "ds_read_b128 a[132:135], v241 offset:5120\n"
        "v_cvt_pk_bf16_f32 v27, v58, v59\n"
        "v_mfma_f32_16x16x32_bf16 a[64:67], a[80:83], v[16:19], a[64:67]\n"
        "v_exp_f32 v60, v60\n"
        "v_mfma_f32_16x16x32_bf16 v[32:35], a[104:107], v[8:11], v[32:35]\n"
        "v_exp_f32 v61, v61\n"
        "v_mfma_f32_16x16x32_bf16 v[36:39], a[104:107], v[72:75], v[36:39]\n"
        "v_cvt_pk_bf16_f32 v30, v60, v61\n"
        "v_mfma_f32_16x16x32_bf16 v[40:43], a[104:107], v[104:107], v[40:43]\n"
        "v_exp_f32 v62, v62\n"
        "v_mfma_f32_16x16x32_bf16 v[44:47], a[104:107], v[136:139], v[44:47]\n"
        "v_exp_f32 v63, v63\n"
        "v_mfma_f32_16x16x32_bf16 v[184:187], a[136:139], v[80:83], v[184:187]\n"
        "ds_read_b128 a[104:107], v240 offset:18432\n"
        "v_cvt_pk_bf16_f32 v31, v62, v63\n"
        "v_mfma_f32_16x16x32_bf16 v[216:219], a[136:139], v[84:87], v[216:219]\n"
        "v_mfma_f32_16x16x32_bf16 a[24:27], a[136:139], v[88:91], a[24:27]\n"
        "v_mfma_f32_16x16x32_bf16 a[56:59], a[136:139], v[92:95], a[56:59]\n"
        "ds_read_b128 a[136:139], v241 offset:6144\n"
        "v_mfma_f32_16x16x32_bf16 a[68:71], a[80:83], v[20:23], a[68:71]\n"
        "v_mfma_f32_16x16x32_bf16 v[32:35], a[108:111], v[12:15], v[32:35]\n"
        "v_mfma_f32_16x16x32_bf16 v[36:39], a[108:111], v[76:79], v[36:39]\n"
        "v_mfma_f32_16x16x32_bf16 v[40:43], a[108:111], v[108:111], v[40:43]\n"
        "v_mfma_f32_16x16x32_bf16 v[44:47], a[108:111], v[140:143], v[44:47]\n"
        "ds_read_b128 a[108:111], v240 offset:19456\n"
        "s_waitcnt lgkmcnt(2)\n"
        "v_mfma_f32_16x16x32_bf16 v[188:191], a[140:143], v[80:83], v[188:191]\n"
        "v_mfma_f32_16x16x32_bf16 v[220:223], a[140:143], v[84:87], v[220:223]\n"
        "v_mfma_f32_16x16x32_bf16 a[28:31], a[140:143], v[88:91], a[28:31]\n"
        "v_exp_f32 v32, v32\n"
        "v_mfma_f32_16x16x32_bf16 a[60:63], a[140:143], v[92:95], a[60:63]\n"
        "v_exp_f32 v33, v33\n"
        "v_mfma_f32_16x16x32_bf16 a[72:75], a[80:83], v[24:27], a[72:75]\n"
        "ds_read_b128 a[140:143], v241 offset:7168\n"
        "v_cvt_pk_bf16_f32 v80, v32, v33\n"
        "v_mfma_f32_16x16x32_bf16 v[48:51], a[112:115], v[0:3], v[224:227]\n"
        "v_exp_f32 v34, v34\n"
        "v_mfma_f32_16x16x32_bf16 v[52:55], a[112:115], v[64:67], v[228:231]\n"
        "v_exp_f32 v35, v35\n"
        "v_mfma_f32_16x16x32_bf16 v[56:59], a[112:115], v[96:99], v[232:235]\n"
        "v_cvt_pk_bf16_f32 v81, v34, v35\n"
        "v_mfma_f32_16x16x32_bf16 v[60:63], a[112:115], v[128:131], v[236:239]\n"
        "v_exp_f32 v36, v36\n"
        "v_mfma_f32_16x16x32_bf16 v[160:163], a[144:147], v[16:19], v[160:163]\n"
        "v_exp_f32 v37, v37\n"
        "v_mfma_f32_16x16x32_bf16 v[192:195], a[144:147], v[20:23], v[192:195]\n"
        "ds_read_b128 a[112:115], v240 offset:20480\n"
        "v_cvt_pk_bf16_f32 v84, v36, v37\n"
        "v_mfma_f32_16x16x32_bf16 a[0:3], a[144:147], v[24:27], a[0:3]\n"
        "v_exp_f32 v38, v38\n"
        "v_mfma_f32_16x16x32_bf16 a[32:35], a[144:147], v[28:31], a[32:35]\n"
        "v_exp_f32 v39, v39\n"
        "v_mfma_f32_16x16x32_bf16 a[76:79], a[80:83], v[28:31], a[76:79]\n"
        "ds_read_b128 a[144:147], v241 offset:8192\n"
        "v_cvt_pk_bf16_f32 v85, v38, v39\n"
        "v_mfma_f32_16x16x32_bf16 v[48:51], a[116:119], v[4:7], v[48:51]\n"
        "v_exp_f32 v40, v40\n"
        "v_mfma_f32_16x16x32_bf16 v[52:55], a[116:119], v[68:71], v[52:55]\n"
        "v_exp_f32 v41, v41\n"
        "v_mfma_f32_16x16x32_bf16 v[56:59], a[116:119], v[100:103], v[56:59]\n"
        "v_cvt_pk_bf16_f32 v88, v40, v41\n"
        "v_mfma_f32_16x16x32_bf16 v[60:63], a[116:119], v[132:135], v[60:63]\n"
        "v_exp_f32 v42, v42\n"
        "v_mfma_f32_16x16x32_bf16 v[164:167], a[148:151], v[16:19], v[164:167]\n"
        "v_exp_f32 v43, v43\n"
        "v_mfma_f32_16x16x32_bf16 v[196:199], a[148:151], v[20:23], v[196:199]\n"
        "ds_read_b128 a[116:119], v240 offset:21504\n"
        "v_cvt_pk_bf16_f32 v89, v42, v43\n"
        "v_mfma_f32_16x16x32_bf16 a[4:7], a[148:151], v[24:27], a[4:7]\n"
        "v_exp_f32 v44, v44\n"
        "v_mfma_f32_16x16x32_bf16 a[36:39], a[148:151], v[28:31], a[36:39]\n"
        "v_exp_f32 v45, v45\n"
        "v_mfma_f32_16x16x32_bf16 v[48:51], a[120:123], v[8:11], v[48:51]\n"
        "ds_read_b128 a[148:151], v241 offset:9216\n"
        "v_cvt_pk_bf16_f32 v92, v44, v45\n"
        "v_mfma_f32_16x16x32_bf16 v[52:55], a[120:123], v[72:75], v[52:55]\n"
        "v_exp_f32 v46, v46\n"
        "v_mfma_f32_16x16x32_bf16 v[56:59], a[120:123], v[104:107], v[56:59]\n"
        "v_exp_f32 v47, v47\n"
        "v_mfma_f32_16x16x32_bf16 v[60:63], a[120:123], v[136:139], v[60:63]\n"
        "ds_read_b128 a[120:123], v240 offset:22528\n"
        "v_cvt_pk_bf16_f32 v93, v46, v47\n"
        "v_mfma_f32_16x16x32_bf16 v[168:171], a[152:155], v[16:19], v[168:171]\n"
        "v_mfma_f32_16x16x32_bf16 v[200:203], a[152:155], v[20:23], v[200:203]\n"
        "v_mfma_f32_16x16x32_bf16 a[8:11], a[152:155], v[24:27], a[8:11]\n"
        "v_mfma_f32_16x16x32_bf16 a[40:43], a[152:155], v[28:31], a[40:43]\n"
        "ds_read_b128 a[152:155], v241 offset:10240\n"
        "v_mfma_f32_16x16x32_bf16 v[48:51], a[124:127], v[12:15], v[48:51]\n"
        "v_mfma_f32_16x16x32_bf16 v[52:55], a[124:127], v[76:79], v[52:55]\n"
        "v_mfma_f32_16x16x32_bf16 v[56:59], a[124:127], v[108:111], v[56:59]\n"
        "v_mfma_f32_16x16x32_bf16 v[60:63], a[124:127], v[140:143], v[60:63]\n"
        "ds_read_b128 a[124:127], v240 offset:23552\n"
        "v_mfma_f32_16x16x32_bf16 v[172:175], a[156:159], v[16:19], v[172:175]\n"
        "v_mfma_f32_16x16x32_bf16 v[204:207], a[156:159], v[20:23], v[204:207]\n"
        "v_mfma_f32_16x16x32_bf16 a[12:15], a[156:159], v[24:27], a[12:15]\n"
        "v_mfma_f32_16x16x32_bf16 a[44:47], a[156:159], v[28:31], a[44:47]\n"
        "v_exp_f32 v48, v48\n"
        "v_mfma_f32_16x16x32_bf16 v[32:35], a[96:99], v[0:3], v[224:227]\n"
        "v_exp_f32 v49, v49\n"
        "v_mfma_f32_16x16x32_bf16 v[36:39], a[96:99], v[64:67], v[228:231]\n"
        "ds_read_b128 a[156:159], v241 offset:11264\n"
        "v_cvt_pk_bf16_f32 v82, v48, v49\n"
        "v_mfma_f32_16x16x32_bf16 v[40:43], a[96:99], v[96:99], v[232:235]\n"
        "v_exp_f32 v50, v50\n"
        "v_mfma_f32_16x16x32_bf16 v[44:47], a[96:99], v[128:131], v[236:239]\n"
        "v_exp_f32 v51, v51\n"
        "v_mfma_f32_16x16x32_bf16 v[176:179], a[128:131], v[16:19], v[176:179]\n"
        "ds_read_b128 a[96:99], v240 offset:24576\n"
        "v_cvt_pk_bf16_f32 v83, v50, v51\n"
        "v_mfma_f32_16x16x32_bf16 v[208:211], a[128:131], v[20:23], v[208:211]\n"
        "v_exp_f32 v52, v52\n"
        "v_mfma_f32_16x16x32_bf16 a[16:19], a[128:131], v[24:27], a[16:19]\n"
        "v_exp_f32 v53, v53\n"
        "v_mfma_f32_16x16x32_bf16 a[48:51], a[128:131], v[28:31], a[48:51]\n"
        "ds_read_b128 a[128:131], v241 offset:12288\n"
        "v_cvt_pk_bf16_f32 v86, v52, v53\n"
        "v_mfma_f32_16x16x32_bf16 v[32:35], a[100:103], v[4:7], v[32:35]\n"
        "v_exp_f32 v54, v54\n"
        "v_mfma_f32_16x16x32_bf16 v[36:39], a[100:103], v[68:71], v[36:39]\n"
        "v_exp_f32 v55, v55\n"
        "v_mfma_f32_16x16x32_bf16 v[40:43], a[100:103], v[100:103], v[40:43]\n"
        "v_cvt_pk_bf16_f32 v87, v54, v55\n"
        "v_mfma_f32_16x16x32_bf16 v[44:47], a[100:103], v[132:135], v[44:47]\n"
        "v_exp_f32 v56, v56\n"
        "v_mfma_f32_16x16x32_bf16 v[180:183], a[132:135], v[16:19], v[180:183]\n"
        "v_exp_f32 v57, v57\n"
        "v_mfma_f32_16x16x32_bf16 v[212:215], a[132:135], v[20:23], v[212:215]\n"
        "ds_read_b128 a[100:103], v240 offset:25600\n"
        "v_cvt_pk_bf16_f32 v90, v56, v57\n"
        "v_mfma_f32_16x16x32_bf16 a[20:23], a[132:135], v[24:27], a[20:23]\n"
        "v_exp_f32 v58, v58\n"
        "v_mfma_f32_16x16x32_bf16 a[52:55], a[132:135], v[28:31], a[52:55]\n"
        "s_waitcnt lgkmcnt(1)\n"
        "v_exp_f32 v59, v59\n"
        "v_mfma_f32_16x16x32_bf16 a[64:67], a[80:83], v[80:83], a[64:67]\n"
        "ds_read_b128 a[132:135], v241 offset:13312\n"
        "v_cvt_pk_bf16_f32 v91, v58, v59\n"
        "v_mfma_f32_16x16x32_bf16 v[32:35], a[104:107], v[8:11], v[32:35]\n"
        "v_exp_f32 v60, v60\n"
        "v_mfma_f32_16x16x32_bf16 v[36:39], a[104:107], v[72:75], v[36:39]\n"
        "v_exp_f32 v61, v61\n"
        "v_mfma_f32_16x16x32_bf16 v[40:43], a[104:107], v[104:107], v[40:43]\n"
        "v_cvt_pk_bf16_f32 v94, v60, v61\n"
        "v_mfma_f32_16x16x32_bf16 v[44:47], a[104:107], v[136:139], v[44:47]\n"
        "v_exp_f32 v62, v62\n"
        "v_mfma_f32_16x16x32_bf16 v[184:187], a[136:139], v[16:19], v[184:187]\n"
        "v_exp_f32 v63, v63\n"
        "v_mfma_f32_16x16x32_bf16 v[216:219], a[136:139], v[20:23], v[216:219]\n"
        "ds_read_b128 a[104:107], v240 offset:26624\n"
        "v_cvt_pk_bf16_f32 v95, v62, v63\n"
        "v_mfma_f32_16x16x32_bf16 a[24:27], a[136:139], v[24:27], a[24:27]\n"
        "v_mfma_f32_16x16x32_bf16 a[56:59], a[136:139], v[28:31], a[56:59]\n"
        "ds_read_b128 a[136:139], v241 offset:14336\n"
        "v_mfma_f32_16x16x32_bf16 a[68:71], a[80:83], v[84:87], a[68:71]\n"
        "v_mfma_f32_16x16x32_bf16 v[32:35], a[108:111], v[12:15], v[32:35]\n"
        "v_mfma_f32_16x16x32_bf16 v[36:39], a[108:111], v[76:79], v[36:39]\n"
        "v_mfma_f32_16x16x32_bf16 v[40:43], a[108:111], v[108:111], v[40:43]\n"
        "v_mfma_f32_16x16x32_bf16 v[44:47], a[108:111], v[140:143], v[44:47]\n"
        "ds_read_b128 a[108:111], v240 offset:27648\n"
        "v_mfma_f32_16x16x32_bf16 v[188:191], a[140:143], v[16:19], v[188:191]\n"
        "v_mfma_f32_16x16x32_bf16 v[220:223], a[140:143], v[20:23], v[220:223]\n"
        "v_mfma_f32_16x16x32_bf16 a[28:31], a[140:143], v[24:27], a[28:31]\n"
        "v_mfma_f32_16x16x32_bf16 a[60:63], a[140:143], v[28:31], a[60:63]\n"
        "v_exp_f32 v32, v32\n"
        "v_mfma_f32_16x16x32_bf16 a[72:75], a[80:83], v[88:91], a[72:75]\n"
        "v_exp_f32 v33, v33\n"
        "v_mfma_f32_16x16x32_bf16 v[48:51], a[112:115], v[0:3], v[224:227]\n"
        "ds_read_b128 a[140:143], v241 offset:15360\n"
        "v_cvt_pk_bf16_f32 v16, v32, v33\n"
        "v_mfma_f32_16x16x32_bf16 v[52:55], a[112:115], v[64:67], v[228:231]\n"
        "v_exp_f32 v34, v34\n"
        "v_mfma_f32_16x16x32_bf16 v[56:59], a[112:115], v[96:99], v[232:235]\n"
        "v_exp_f32 v35, v35\n"
        "v_mfma_f32_16x16x32_bf16 v[60:63], a[112:115], v[128:131], v[236:239]\n"
        "ds_read_b128 a[112:115], v240 offset:28672\n"
        "v_cvt_pk_bf16_f32 v17, v34, v35\n"
        "v_mfma_f32_16x16x32_bf16 v[160:163], a[144:147], v[80:83], v[160:163]\n"
        "v_exp_f32 v36, v36\n"
        "v_mfma_f32_16x16x32_bf16 v[192:195], a[144:147], v[84:87], v[192:195]\n"
        "v_exp_f32 v37, v37\n"
        "v_mfma_f32_16x16x32_bf16 a[0:3], a[144:147], v[88:91], a[0:3]\n"
        "v_cvt_pk_bf16_f32 v20, v36, v37\n"
        "v_mfma_f32_16x16x32_bf16 a[32:35], a[144:147], v[92:95], a[32:35]\n"
        "v_exp_f32 v38, v38\n"
        "v_mfma_f32_16x16x32_bf16 a[76:79], a[80:83], v[92:95], a[76:79]\n"
        "v_exp_f32 v39, v39\n"
        "v_mfma_f32_16x16x32_bf16 v[48:51], a[116:119], v[4:7], v[48:51]\n"
        "ds_read_b128 a[144:147], v241 offset:16384\n"
        "v_cvt_pk_bf16_f32 v21, v38, v39\n"
        "v_mfma_f32_16x16x32_bf16 v[52:55], a[116:119], v[68:71], v[52:55]\n"
        "v_exp_f32 v40, v40\n"
        "v_mfma_f32_16x16x32_bf16 v[56:59], a[116:119], v[100:103], v[56:59]\n"
        "v_exp_f32 v41, v41\n"
        "v_mfma_f32_16x16x32_bf16 v[60:63], a[116:119], v[132:135], v[60:63]\n"
        "ds_read_b128 a[116:119], v240 offset:29696\n"
        "v_cvt_pk_bf16_f32 v24, v40, v41\n"
        "v_mfma_f32_16x16x32_bf16 v[164:167], a[148:151], v[80:83], v[164:167]\n"
        "v_exp_f32 v42, v42\n"
        "v_mfma_f32_16x16x32_bf16 v[196:199], a[148:151], v[84:87], v[196:199]\n"
        "v_exp_f32 v43, v43\n"
        "v_mfma_f32_16x16x32_bf16 a[4:7], a[148:151], v[88:91], a[4:7]\n"
        "v_cvt_pk_bf16_f32 v25, v42, v43\n"
        "v_mfma_f32_16x16x32_bf16 a[36:39], a[148:151], v[92:95], a[36:39]\n"
        "v_exp_f32 v44, v44\n"
        "v_mfma_f32_16x16x32_bf16 v[48:51], a[120:123], v[8:11], v[48:51]\n"
        "v_exp_f32 v45, v45\n"
        "v_mfma_f32_16x16x32_bf16 v[52:55], a[120:123], v[72:75], v[52:55]\n"
        "ds_read_b128 a[148:151], v241 offset:17408\n"
        "v_cvt_pk_bf16_f32 v28, v44, v45\n"
        "v_mfma_f32_16x16x32_bf16 v[56:59], a[120:123], v[104:107], v[56:59]\n"
        "v_exp_f32 v46, v46\n"
        "v_mfma_f32_16x16x32_bf16 v[60:63], a[120:123], v[136:139], v[60:63]\n"
        "v_exp_f32 v47, v47\n"
        "v_mfma_f32_16x16x32_bf16 v[168:171], a[152:155], v[80:83], v[168:171]\n"
        "ds_read_b128 a[120:123], v240 offset:30720\n"
        "v_cvt_pk_bf16_f32 v29, v46, v47\n"
        "v_mfma_f32_16x16x32_bf16 v[200:203], a[152:155], v[84:87], v[200:203]\n"
        "v_mfma_f32_16x16x32_bf16 a[8:11], a[152:155], v[88:91], a[8:11]\n"
        "v_mfma_f32_16x16x32_bf16 a[40:43], a[152:155], v[92:95], a[40:43]\n"
        "ds_read_b128 a[152:155], v241 offset:18432\n"
        "v_mfma_f32_16x16x32_bf16 v[48:51], a[124:127], v[12:15], v[48:51]\n"
        "v_mfma_f32_16x16x32_bf16 v[52:55], a[124:127], v[76:79], v[52:55]\n"
        "v_mfma_f32_16x16x32_bf16 v[56:59], a[124:127], v[108:111], v[56:59]\n"
        "v_mfma_f32_16x16x32_bf16 v[60:63], a[124:127], v[140:143], v[60:63]\n"
        "ds_read_b128 a[124:127], v240 offset:31744\n"
        "v_xor_b32 v240, 0x8000, v240\n"
        "v_mfma_f32_16x16x32_bf16 v[172:175], a[156:159], v[80:83], v[172:175]\n"
        "v_mfma_f32_16x16x32_bf16 v[204:207], a[156:159], v[84:87], v[204:207]\n"
        "v_mfma_f32_16x16x32_bf16 a[12:15], a[156:159], v[88:91], a[12:15]\n"
        "v_exp_f32 v48, v48\n"
        "v_mfma_f32_16x16x32_bf16 a[44:47], a[156:159], v[92:95], a[44:47]\n"
        "v_exp_f32 v49, v49\n"
        "v_mfma_f32_16x16x32_bf16 v[32:35], a[96:99], v[0:3], v[224:227]\n"
        "ds_read_b128 a[156:159], v241 offset:19456\n"
        "v_cvt_pk_bf16_f32 v18, v48, v49\n"
        "v_mfma_f32_16x16x32_bf16 v[36:39], a[96:99], v[64:67], v[228:231]\n"
        "v_exp_f32 v50, v50\n"
        "v_mfma_f32_16x16x32_bf16 v[40:43], a[96:99], v[96:99], v[232:235]\n"
        "s_waitcnt lgkmcnt(2)\n"
        "v_exp_f32 v51, v51\n"
        "v_mfma_f32_16x16x32_bf16 v[44:47], a[96:99], v[128:131], v[236:239]\n"
        "s_waitcnt vmcnt(8) lgkmcnt(1)\n"
        "v_cvt_pk_bf16_f32 v19, v50, v51\n"
        "s_barrier\n"
        "v_mfma_f32_16x16x32_bf16 v[176:179], a[128:131], v[80:83], v[176:179]\n"
        "v_exp_f32 v52, v52\n"
        "v_mfma_f32_16x16x32_bf16 v[208:211], a[128:131], v[84:87], v[208:211]\n"
        "v_exp_f32 v53, v53\n"
        "v_mfma_f32_16x16x32_bf16 a[16:19], a[128:131], v[88:91], a[16:19]\n"
        "global_load_lds_dwordx4 v254, s[68:69] offset:0 sc1\n"
        "v_cvt_pk_bf16_f32 v22, v52, v53\n"
        "v_mfma_f32_16x16x32_bf16 a[48:51], a[128:131], v[92:95], a[48:51]\n"
        "v_exp_f32 v54, v54\n"
        "v_mfma_f32_16x16x32_bf16 v[32:35], a[100:103], v[4:7], v[32:35]\n"
        "v_exp_f32 v55, v55\n"
        "v_mfma_f32_16x16x32_bf16 v[36:39], a[100:103], v[68:71], v[36:39]\n"
        "ds_read_b128 a[96:99], v240 offset:0\n"
        "v_cvt_pk_bf16_f32 v23, v54, v55\n"
        "v_mfma_f32_16x16x32_bf16 v[40:43], a[100:103], v[100:103], v[40:43]\n"
        "v_exp_f32 v56, v56\n"
        "v_mfma_f32_16x16x32_bf16 v[44:47], a[100:103], v[132:135], v[44:47]\n"
        "v_exp_f32 v57, v57\n"
        "v_mfma_f32_16x16x32_bf16 v[180:183], a[132:135], v[80:83], v[180:183]\n"
        "global_load_lds_dwordx4 v254, s[68:69] offset:1024 sc1\n"
        "v_cvt_pk_bf16_f32 v26, v56, v57\n"
        "v_mfma_f32_16x16x32_bf16 v[212:215], a[132:135], v[84:87], v[212:215]\n"
        "v_exp_f32 v58, v58\n"
        "v_mfma_f32_16x16x32_bf16 a[20:23], a[132:135], v[88:91], a[20:23]\n"
        "v_exp_f32 v59, v59\n"
        "v_mfma_f32_16x16x32_bf16 a[52:55], a[132:135], v[92:95], a[52:55]\n"
        "ds_read_b128 a[128:131], v241 offset:20480\n"
        "v_cvt_pk_bf16_f32 v27, v58, v59\n"
        "v_mfma_f32_16x16x32_bf16 a[64:67], a[80:83], v[16:19], a[64:67]\n"
        "global_load_lds_dwordx4 v254, s[68:69] offset:2048 sc1\n"
        "ds_read_b128 a[100:103], v240 offset:1024\n"
        "v_mfma_f32_16x16x32_bf16 v[32:35], a[104:107], v[8:11], v[32:35]\n"
        "v_exp_f32 v60, v60\n"
        "v_mfma_f32_16x16x32_bf16 v[36:39], a[104:107], v[72:75], v[36:39]\n"
        "v_exp_f32 v61, v61\n"
        "v_mfma_f32_16x16x32_bf16 v[40:43], a[104:107], v[104:107], v[40:43]\n"
        "ds_read_b128 a[132:135], v241 offset:21504\n"
        "v_cvt_pk_bf16_f32 v30, v60, v61\n"
        "v_mfma_f32_16x16x32_bf16 v[44:47], a[104:107], v[136:139], v[44:47]\n"
        "v_exp_f32 v62, v62\n"
        "v_mfma_f32_16x16x32_bf16 v[184:187], a[136:139], v[80:83], v[184:187]\n"
        "v_exp_f32 v63, v63\n"
        "v_mfma_f32_16x16x32_bf16 v[216:219], a[136:139], v[84:87], v[216:219]\n"
        "global_load_lds_dwordx4 v254, s[68:69] offset:3072 sc1\n"
        "v_cvt_pk_bf16_f32 v31, v62, v63\n"
        "v_mfma_f32_16x16x32_bf16 a[24:27], a[136:139], v[88:91], a[24:27]\n"
        "ds_read_b128 a[104:107], v240 offset:2048\n"
        "s_waitcnt lgkmcnt(3)\n"
        "v_mfma_f32_16x16x32_bf16 a[56:59], a[136:139], v[92:95], a[56:59]\n"
        "ds_read_b128 a[136:139], v241 offset:22528\n"
        "v_mfma_f32_16x16x32_bf16 a[68:71], a[80:83], v[20:23], a[68:71]\n"
        "v_mfma_f32_16x16x32_bf16 v[32:35], a[108:111], v[12:15], v[32:35]\n"
        "v_mfma_f32_16x16x32_bf16 v[36:39], a[108:111], v[76:79], v[36:39]\n"
        "v_mfma_f32_16x16x32_bf16 v[40:43], a[108:111], v[108:111], v[40:43]\n"
        "v_mfma_f32_16x16x32_bf16 v[44:47], a[108:111], v[140:143], v[44:47]\n"
        "ds_read_b128 a[108:111], v240 offset:3072\n"
        "v_mfma_f32_16x16x32_bf16 v[188:191], a[140:143], v[80:83], v[188:191]\n"
        "v_mfma_f32_16x16x32_bf16 v[220:223], a[140:143], v[84:87], v[220:223]\n"
        "v_mfma_f32_16x16x32_bf16 a[28:31], a[140:143], v[88:91], a[28:31]\n"
        "v_mfma_f32_16x16x32_bf16 a[60:63], a[140:143], v[92:95], a[60:63]\n"
        "v_exp_f32 v32, v32\n"
        "v_mfma_f32_16x16x32_bf16 a[72:75], a[80:83], v[24:27], a[72:75]\n"
        "v_exp_f32 v33, v33\n"
        "v_mfma_f32_16x16x32_bf16 v[48:51], a[112:115], v[0:3], v[224:227]\n"
        "ds_read_b128 a[140:143], v241 offset:23552\n"
        "v_cvt_pk_bf16_f32 v80, v32, v33\n"
        "v_mfma_f32_16x16x32_bf16 v[52:55], a[112:115], v[64:67], v[228:231]\n"
        "v_exp_f32 v34, v34\n"
        "v_mfma_f32_16x16x32_bf16 v[56:59], a[112:115], v[96:99], v[232:235]\n"
        "v_exp_f32 v35, v35\n"
        "v_mfma_f32_16x16x32_bf16 v[60:63], a[112:115], v[128:131], v[236:239]\n"
        "ds_read_b128 a[112:115], v240 offset:4096\n"
        "v_cvt_pk_bf16_f32 v81, v34, v35\n"
        "v_mfma_f32_16x16x32_bf16 v[160:163], a[144:147], v[16:19], v[160:163]\n"
        "v_exp_f32 v36, v36\n"
        "v_mfma_f32_16x16x32_bf16 v[192:195], a[144:147], v[20:23], v[192:195]\n"
        "v_exp_f32 v37, v37\n"
        "v_mfma_f32_16x16x32_bf16 a[0:3], a[144:147], v[24:27], a[0:3]\n"
        "v_cvt_pk_bf16_f32 v84, v36, v37\n"
        "v_mfma_f32_16x16x32_bf16 a[32:35], a[144:147], v[28:31], a[32:35]\n"
        "v_exp_f32 v38, v38\n"
        "v_mfma_f32_16x16x32_bf16 a[76:79], a[80:83], v[28:31], a[76:79]\n"
        "v_exp_f32 v39, v39\n"
        "v_mfma_f32_16x16x32_bf16 v[48:51], a[116:119], v[4:7], v[48:51]\n"
        "ds_read_b128 a[144:147], v241 offset:24576\n"
        "v_cvt_pk_bf16_f32 v85, v38, v39\n"
        "v_mfma_f32_16x16x32_bf16 v[52:55], a[116:119], v[68:71], v[52:55]\n"
        "v_exp_f32 v40, v40\n"
        "v_mfma_f32_16x16x32_bf16 v[56:59], a[116:119], v[100:103], v[56:59]\n"
        "v_exp_f32 v41, v41\n"
        "v_mfma_f32_16x16x32_bf16 v[60:63], a[116:119], v[132:135], v[60:63]\n"
        "ds_read_b128 a[116:119], v240 offset:5120\n"
        "v_cvt_pk_bf16_f32 v88, v40, v41\n"
        "v_mfma_f32_16x16x32_bf16 v[164:167], a[148:151], v[16:19], v[164:167]\n"
        "v_exp_f32 v42, v42\n"
        "v_mfma_f32_16x16x32_bf16 v[196:199], a[148:151], v[20:23], v[196:199]\n"
        "v_exp_f32 v43, v43\n"
        "v_mfma_f32_16x16x32_bf16 a[4:7], a[148:151], v[24:27], a[4:7]\n"
        "v_cvt_pk_bf16_f32 v89, v42, v43\n"
        "v_mfma_f32_16x16x32_bf16 a[36:39], a[148:151], v[28:31], a[36:39]\n"
        "v_exp_f32 v44, v44\n"
        "v_mfma_f32_16x16x32_bf16 v[48:51], a[120:123], v[8:11], v[48:51]\n"
        "v_exp_f32 v45, v45\n"
        "v_mfma_f32_16x16x32_bf16 v[52:55], a[120:123], v[72:75], v[52:55]\n"
        "ds_read_b128 a[148:151], v241 offset:25600\n"
        "v_cvt_pk_bf16_f32 v92, v44, v45\n"
        "v_mfma_f32_16x16x32_bf16 v[56:59], a[120:123], v[104:107], v[56:59]\n"
        "v_exp_f32 v46, v46\n"
        "v_mfma_f32_16x16x32_bf16 v[60:63], a[120:123], v[136:139], v[60:63]\n"
        "v_exp_f32 v47, v47\n"
        "v_mfma_f32_16x16x32_bf16 v[168:171], a[152:155], v[16:19], v[168:171]\n"
        "v_cvt_pk_bf16_f32 v93, v46, v47\n"
        "v_mfma_f32_16x16x32_bf16 v[200:203], a[152:155], v[20:23], v[200:203]\n"
        "v_mfma_f32_16x16x32_bf16 a[8:11], a[152:155], v[24:27], a[8:11]\n"
        "v_mfma_f32_16x16x32_bf16 a[40:43], a[152:155], v[28:31], a[40:43]\n"
        "v_mfma_f32_16x16x32_bf16 v[48:51], a[124:127], v[12:15], v[48:51]\n"
        "v_mfma_f32_16x16x32_bf16 v[52:55], a[124:127], v[76:79], v[52:55]\n"
        "v_mfma_f32_16x16x32_bf16 v[56:59], a[124:127], v[108:111], v[56:59]\n"
        "v_mfma_f32_16x16x32_bf16 v[60:63], a[124:127], v[140:143], v[60:63]\n"
        "v_mfma_f32_16x16x32_bf16 v[172:175], a[156:159], v[16:19], v[172:175]\n"
        "v_mfma_f32_16x16x32_bf16 v[204:207], a[156:159], v[20:23], v[204:207]\n"
        "v_mfma_f32_16x16x32_bf16 a[12:15], a[156:159], v[24:27], a[12:15]\n"
        "v_mfma_f32_16x16x32_bf16 a[44:47], a[156:159], v[28:31], a[44:47]\n"
        "s_cmp_lg_u32 s91, 0\n"
        "s_cbranch_scc1 LDU_%=\n"
        "; ---- LDRAIN\n"
        "LLD_%=:\n"
        "v_mfma_f32_16x16x32_bf16 a[16:19], a[128:131], v[24:27], a[16:19]\n"
        "s_lshl_b32 s53, s44, 20\n"
        "s_lshl_b32 s96, s43, 15\n"
        "s_add_u32 s53, s53, s96\n"
        "v_mfma_f32_16x16x32_bf16 a[48:51], a[128:131], v[28:31], a[48:51]\n"
        "s_lshl_b32 s96, s40, 10\n"
        "s_add_u32 s53, s53, s96\n"
        "s_waitcnt lgkmcnt(3)\n"
        "v_mfma_f32_16x16x32_bf16 a[20:23], a[132:135], v[24:27], a[20:23]\n"
        "s_add_u32 s53, s53, 0x2001000\n"
        "s_add_u32 s70, s38, s53\n"
        "s_addc_u32 s71, s39, 0\n"
        "v_mfma_f32_16x16x32_bf16 a[52:55], a[132:135], v[28:31], a[52:55]\n"
        "ds_read_b128 a[160:163], v241 offset:24576\n"
        "v_mfma_f32_16x16x32_bf16 a[24:27], a[136:139], v[24:27], a[24:27]\n"
        "ds_read_b128 a[164:167], v241 offset:25600\n"
        "v_mfma_f32_16x16x32_bf16 a[56:59], a[136:139], v[28:31], a[56:59]\n"
        "ds_read_b128 a[168:171], v241 offset:26624\n"
        "v_mfma_f32_16x16x32_bf16 a[28:31], a[140:143], v[24:27], a[28:31]\n"
        "ds_read_b128 a[172:175], v241 offset:27648\n"
        "v_mfma_f32_16x16x32_bf16 a[60:63], a[140:143], v[28:31], a[60:63]\n"
        "ds_read_b128 a[176:179], v241 offset:28672\n"
        "ds_read_b128 a[180:183], v241 offset:29696\n"
        "ds_read_b128 a[184:187], v241 offset:30720\n"
        "ds_read_b128 a[188:191], v241 offset:31744\n"
        "v_exp_f32 v56, v56\n"
        "v_exp_f32 v57, v57\n"
        "v_exp_f32 v58, v58\n"
        "v_cvt_pk_bf16_f32 v90, v56, v57\n"
        "v_exp_f32 v59, v59\n"
        "v_exp_f32 v60, v60\n"
        "v_cvt_pk_bf16_f32 v91, v58, v59\n"
        "s_nop 1\n"
        "v_mfma_f32_16x16x32_bf16 a[72:75], a[80:83], v[88:91], a[72:75]\n"
        "s_waitcnt lgkmcnt(6)\n"
        "v_exp_f32 v61, v61\n"
        "v_mfma_f32_16x16x32_bf16 a[0:3], a[160:163], v[88:91], a[0:3]\n"
        "s_waitcnt lgkmcnt(5)\n"
        "v_cvt_pk_bf16_f32 v94, v60, v61\n"
        "s_sub_u32 s91, s83, s44\n"
        "v_mfma_f32_16x16x32_bf16 a[4:7], a[164:167], v[88:91], a[4:7]\n"
        "s_waitcnt lgkmcnt(4)\n"
        "v_accvgpr_read_b32 v248, a72\n"
        "s_waitcnt lgkmcnt(3)\n"
        "v_mfma_f32_16x16x32_bf16 a[8:11], a[168:171], v[88:91], a[8:11]\n"
        "s_waitcnt lgkmcnt(2)\n"
        "v_rcp_f32 v246, v248\n"
        "v_mfma_f32_16x16x32_bf16 a[12:15], a[172:175], v[88:91], a[12:15]\n"
        "s_waitcnt lgkmcnt(0)\n"
        "v_exp_f32 v62, v62\n"
        "v_mfma_f32_16x16x32_bf16 a[16:19], a[176:179], v[88:91], a[16:19]\n"
        "v_exp_f32 v63, v63\n"
        "v_mfma_f32_16x16x32_bf16 a[20:23], a[180:183], v[88:91], a[20:23]\n"
        "v_cvt_pk_bf16_f32 v95, v62, v63\n"
        "s_nop 1\n"
        "v_mfma_f32_16x16x32_bf16 a[32:35], a[160:163], v[92:95], a[32:35]\n"
        "v_accvgpr_read_b32 v112, a0\n"
        "v_accvgpr_read_b32 v113, a1\n"
        "v_mfma_f32_16x16x32_bf16 a[76:79], a[80:83], v[92:95], a[76:79]\n"
        "v_accvgpr_read_b32 v114, a2\n"
        "v_accvgpr_read_b32 v115, a3\n"
        "v_mfma_f32_16x16x32_bf16 a[36:39], a[164:167], v[92:95], a[36:39]\n"
        "v_accvgpr_read_b32 v116, a4\n"
        "v_accvgpr_read_b32 v117, a5\n"
        "v_mfma_f32_16x16x32_bf16 a[40:43], a[168:171], v[92:95], a[40:43]\n"
        "v_accvgpr_read_b32 v118, a6\n"
        "v_accvgpr_read_b32 v119, a7\n"
        "v_mfma_f32_16x16x32_bf16 a[44:47], a[172:175], v[92:95], a[44:47]\n"
        "v_accvgpr_read_b32 v249, a76\n"
        "v_mul_f32 v112, v112, v246\n"
        "v_mfma_f32_16x16x32_bf16 a[48:51], a[176:179], v[92:95], a[48:51]\n"
        "v_rcp_f32 v247, v249\n"
        "v_mfma_f32_16x16x32_bf16 a[52:55], a[180:183], v[92:95], a[52:55]\n"
        "v_mul_f32 v113, v113, v246\n"
        "v_mul_f32 v114, v114, v246\n"
        "v_mfma_f32_16x16x32_bf16 a[24:27], a[184:187], v[88:91], a[24:27]\n"
        "v_mul_f32 v115, v115, v246\n"
        "v_mul_f32 v116, v116, v246\n"
        "v_mfma_f32_16x16x32_bf16 a[56:59], a[184:187], v[92:95], a[56:59]\n"
        "v_mul_f32 v117, v117, v246\n"
        "v_mul_f32 v118, v118, v246\n"
        "v_mfma_f32_16x16x32_bf16 a[28:31], a[188:191], v[88:91], a[28:31]\n"
        "v_mul_f32 v119, v119, v246\n"
        "v_cvt_pk_bf16_f32 v112, v112, v113\n"
        "v_mfma_f32_16x16x32_bf16 a[60:63], a[188:191], v[92:95], a[60:63]\n"
        "v_cvt_pk_bf16_f32 v113, v114, v115\n"
        "v_cvt_pk_bf16_f32 v114, v116, v117\n"
        "v_cvt_pk_bf16_f32 v115, v118, v119\n"
        "global_store_dwordx4 v254, v[112:115], s[70:71] offset:0 sc1\n"
        "v_accvgpr_read_b32 v120, a32\n"
        "v_accvgpr_read_b32 v121, a33\n"
        "v_accvgpr_read_b32 v122, a34\n"
        "v_accvgpr_read_b32 v123, a35\n"
        "v_accvgpr_read_b32 v124, a36\n"
        "v_accvgpr_read_b32 v125, a37\n"
        "v_accvgpr_read_b32 v126, a38\n"
        "v_accvgpr_read_b32 v127, a39\n"
        "v_mul_f32 v120, v120, v247\n"
        "v_mul_f32 v121, v121, v247\n"
        "v_mul_f32 v122, v122, v247\n"
        "v_mul_f32 v123, v123, v247\n"
        "v_mul_f32 v124, v124, v247\n"
        "v_mul_f32 v125, v125, v247\n"
        "v_mul_f32 v126, v126, v247\n"
        "v_mul_f32 v127, v127, v247\n"
        "v_cvt_pk_bf16_f32 v120, v120, v121\n"
        "v_cvt_pk_bf16_f32 v121, v122, v123\n"
        "v_cvt_pk_bf16_f32 v122, v124, v125\n"
        "v_cvt_pk_bf16_f32 v123, v126, v127\n"
        "global_store_dwordx4 v254, v[120:123], s[70:71] offset:-4096 sc1\n"
        "s_add_u32 s70, s70, 0x2000\n"
        "s_addc_u32 s71, s71, 0\n"
        "v_accvgpr_read_b32 v144, a8\n"
        "v_accvgpr_read_b32 v145, a9\n"
        "v_accvgpr_read_b32 v146, a10\n"
        "v_accvgpr_read_b32 v147, a11\n"
        "v_accvgpr_read_b32 v148, a12\n"
        "v_accvgpr_read_b32 v149, a13\n"
        "v_accvgpr_read_b32 v150, a14\n"
        "v_accvgpr_read_b32 v151, a15\n"
        "v_mul_f32 v144, v144, v246\n"
        "v_mul_f32 v145, v145, v246\n"
        "v_mul_f32 v146, v146, v246\n"
        "v_mul_f32 v147, v147, v246\n"
        "v_mul_f32 v148, v148, v246\n"
        "v_mul_f32 v149, v149, v246\n"
        "v_mul_f32 v150, v150, v246\n"
        "v_mul_f32 v151, v151, v246\n"
        "v_cvt_pk_bf16_f32 v144, v144, v145\n"
        "v_cvt_pk_bf16_f32 v145, v146, v147\n"
        "v_cvt_pk_bf16_f32 v146, v148, v149\n"
        "v_cvt_pk_bf16_f32 v147, v150, v151\n"
        "global_store_dwordx4 v254, v[144:147], s[70:71] offset:0 sc1\n"
        "v_accvgpr_read_b32 v152, a40\n"
        "v_accvgpr_read_b32 v153, a41\n"
        "v_accvgpr_read_b32 v154, a42\n"
        "v_accvgpr_read_b32 v155, a43\n"
        "v_accvgpr_read_b32 v156, a44\n"
        "v_accvgpr_read_b32 v157, a45\n"
        "v_accvgpr_read_b32 v158, a46\n"
        "v_accvgpr_read_b32 v159, a47\n"
        "v_mul_f32 v152, v152, v247\n"
        "v_mul_f32 v153, v153, v247\n"
        "v_mul_f32 v154, v154, v247\n"
        "v_mul_f32 v155, v155, v247\n"
        "v_mul_f32 v156, v156, v247\n"
        "v_mul_f32 v157, v157, v247\n"
        "v_mul_f32 v158, v158, v247\n"
        "v_mul_f32 v159, v159, v247\n"
        "v_cvt_pk_bf16_f32 v152, v152, v153\n"
        "v_cvt_pk_bf16_f32 v153, v154, v155\n"
        "v_cvt_pk_bf16_f32 v154, v156, v157\n"
        "v_cvt_pk_bf16_f32 v155, v158, v159\n"
        "global_store_dwordx4 v254, v[152:155], s[70:71] offset:-4096 sc1\n"
        "s_add_u32 s70, s70, 0x2000\n"
        "s_addc_u32 s71, s71, 0\n"
        "v_accvgpr_read_b32 v112, a16\n"
        "v_accvgpr_read_b32 v113, a17\n"
        "v_accvgpr_read_b32 v114, a18\n"
        "v_accvgpr_read_b32 v115, a19\n"
        "v_accvgpr_read_b32 v116, a20\n"
        "v_accvgpr_read_b32 v117, a21\n"
        "v_accvgpr_read_b32 v118, a22\n"
        "v_accvgpr_read_b32 v119, a23\n"
        "v_mul_f32 v112, v112, v246\n"
        "v_mul_f32 v113, v113, v246\n"
        "v_mul_f32 v114, v114, v246\n"
        "v_mul_f32 v115, v115, v246\n"
        "v_mul_f32 v116, v116, v246\n"
        "v_mul_f32 v117, v117, v246\n"
        "v_mul_f32 v118, v118, v246\n"
        "v_mul_f32 v119, v119, v246\n"
        "v_cvt_pk_bf16_f32 v112, v112, v113\n"
        "v_cvt_pk_bf16_f32 v113, v114, v115\n"
        "v_cvt_pk_bf16_f32 v114, v116, v117\n"
        "v_cvt_pk_bf16_f32 v115, v118, v119\n"
        "global_store_dwordx4 v254, v[112:115], s[70:71] offset:0 sc1\n"
        "v_accvgpr_read_b32 v120, a48\n"
        "v_accvgpr_read_b32 v121, a49\n"
        "v_accvgpr_read_b32 v122, a50\n"
        "v_accvgpr_read_b32 v123, a51\n"
        "v_accvgpr_read_b32 v124, a52\n"
        "v_accvgpr_read_b32 v125, a53\n"
        "v_accvgpr_read_b32 v126, a54\n"
        "v_accvgpr_read_b32 v127, a55\n"
        "v_mul_f32 v120, v120, v247\n"
        "v_mul_f32 v121, v121, v247\n"
        "v_mul_f32 v122, v122, v247\n"
        "v_mul_f32 v123, v123, v247\n"
        "v_mul_f32 v124, v124, v247\n"
        "v_mul_f32 v125, v125, v247\n"
        "v_mul_f32 v126, v126, v247\n"
        "v_mul_f32 v127, v127, v247\n"
        "v_cvt_pk_bf16_f32 v120, v120, v121\n"
        "v_cvt_pk_bf16_f32 v121, v122, v123\n"
        "v_cvt_pk_bf16_f32 v122, v124, v125\n"
        "v_cvt_pk_bf16_f32 v123, v126, v127\n"
        "global_store_dwordx4 v254, v[120:123], s[70:71] offset:-4096 sc1\n"
        "s_add_u32 s70, s70, 0x2000\n"
        "s_addc_u32 s71, s71, 0\n"
        "v_accvgpr_read_b32 v144, a24\n"
        "v_accvgpr_read_b32 v145, a25\n"
        "v_accvgpr_read_b32 v146, a26\n"
        "v_accvgpr_read_b32 v147, a27\n"
        "v_accvgpr_read_b32 v148, a28\n"
        "v_accvgpr_read_b32 v149, a29\n"
        "v_accvgpr_read_b32 v150, a30\n"
        "v_accvgpr_read_b32 v151, a31\n"
        "v_mul_f32 v144, v144, v246\n"
        "v_mul_f32 v145, v145, v246\n"
        "v_mul_f32 v146, v146, v246\n"
        "v_mul_f32 v147, v147, v246\n"
        "v_mul_f32 v148, v148, v246\n"
        "v_mul_f32 v149, v149, v246\n"
        "v_mul_f32 v150, v150, v246\n"
        "v_mul_f32 v151, v151, v246\n"
        "v_cvt_pk_bf16_f32 v144, v144, v145\n"
        "v_cvt_pk_bf16_f32 v145, v146, v147\n"
        "v_cvt_pk_bf16_f32 v146, v148, v149\n"
        "v_cvt_pk_bf16_f32 v147, v150, v151\n"
        "global_store_dwordx4 v254, v[144:147], s[70:71] offset:0 sc1\n"
        "v_accvgpr_read_b32 v152, a56\n"
        "v_accvgpr_read_b32 v153, a57\n"
        "v_accvgpr_read_b32 v154, a58\n"
        "v_accvgpr_read_b32 v155, a59\n"
        "v_accvgpr_read_b32 v156, a60\n"
        "v_accvgpr_read_b32 v157, a61\n"
        "v_accvgpr_read_b32 v158, a62\n"
        "v_accvgpr_read_b32 v159, a63\n"
        "v_mul_f32 v152, v152, v247\n"
        "v_mul_f32 v153, v153, v247\n"
        "v_mul_f32 v154, v154, v247\n"
        "v_mul_f32 v155, v155, v247\n"
        "v_mul_f32 v156, v156, v247\n"
        "v_mul_f32 v157, v157, v247\n"
        "v_mul_f32 v158, v158, v247\n"
        "v_mul_f32 v159, v159, v247\n"
        "v_cvt_pk_bf16_f32 v152, v152, v153\n"
        "v_cvt_pk_bf16_f32 v153, v154, v155\n"
        "v_cvt_pk_bf16_f32 v154, v156, v157\n"
        "v_cvt_pk_bf16_f32 v155, v158, v159\n"
        "global_store_dwordx4 v254, v[152:155], s[70:71] offset:-4096 sc1\n"
        "v_add_f32 v247, v248, v249\n"
        "v_cmp_nge_f32 vcc, 0x4e6e6b28, v247\n"
        "s_nop 0\n"
        "s_cbranch_vccnz LRL_%=\n"
        "LSL_%=:\n"
        "s_cmp_eq_u32 s91, 0\n"
        "s_cbranch_scc1 LTL_%=\n"
        "; ---- SINGLE\n"
        "LSI_%=:\n"
        "v_mfma_f32_16x16x32_bf16 v[32:35], a[96:99], v[0:3], v[224:227]\n"
        "s_add_u32 m0, s81, 0x0\n"
        "ds_read_b128 a[120:123], v240 offset:6144\n"
        "v_mfma_f32_16x16x32_bf16 v[36:39], a[96:99], v[64:67], v[228:231]\n"
        "global_load_lds_dwordx4 v254, s[74:75] offset:0 sc1\n"
        "ds_read_b128 a[152:155], v241 offset:26624\n"
        "v_mfma_f32_16x16x32_bf16 v[176:179], a[128:131], v[16:19], v[176:179]\n"
        "s_waitcnt lgkmcnt(4)\n"
        "v_exp_f32 v48, v48\n"
        "v_mfma_f32_16x16x32_bf16 v[208:211], a[128:131], v[20:23], v[208:211]\n"
        "v_exp_f32 v49, v49\n"
        "v_mfma_f32_16x16x32_bf16 v[32:35], a[100:103], v[4:7], v[32:35]\n"
        "ds_read_b128 a[124:127], v240 offset:7168\n"
        "v_cvt_pk_bf16_f32 v82, v48, v49\n"
        "v_mfma_f32_16x16x32_bf16 v[36:39], a[100:103], v[68:71], v[36:39]\n"
        "global_load_lds_dwordx4 v254, s[74:75] offset:1024 sc1\n"
        "ds_read_b128 a[156:159], v241 offset:27648\n"
        "v_mfma_f32_16x16x32_bf16 v[180:183], a[132:135], v[16:19], v[180:183]\n"
        "v_exp_f32 v50, v50\n"
        "v_mfma_f32_16x16x32_bf16 v[212:215], a[132:135], v[20:23], v[212:215]\n"
        "v_exp_f32 v51, v51\n"
        "v_mfma_f32_16x16x32_bf16 v[32:35], a[104:107], v[8:11], v[32:35]\n"
        "ds_read_b128 a[96:99], v240 offset:8192\n"
        "s_xor_b32 s79, s79, 0x8000\n"
        "v_cvt_pk_bf16_f32 v83, v50, v51\n"
        "v_mfma_f32_16x16x32_bf16 v[36:39], a[104:107], v[72:75], v[36:39]\n"
        "global_load_lds_dwordx4 v254, s[74:75] offset:2048 sc1\n"
        "ds_read_b128 a[128:131], v241 offset:28672\n"
        "s_add_u32 s82, s82, 1\n"
        "v_mfma_f32_16x16x32_bf16 v[184:187], a[136:139], v[16:19], v[184:187]\n"
        "s_xor_b32 s78, s78, 0x8000\n"
        "v_exp_f32 v52, v52\n"
        "v_mfma_f32_16x16x32_bf16 v[216:219], a[136:139], v[20:23], v[216:219]\n"
        "s_add_u32 s81, s78, 4096\n"
        "v_exp_f32 v53, v53\n"
        "v_mfma_f32_16x16x32_bf16 a[64:67], a[80:83], v[80:83], a[64:67]\n"
        "ds_read_b128 a[100:103], v240 offset:9216\n"
        "s_sub_u32 s91, s91, 1\n"
        "v_cvt_pk_bf16_f32 v86, v52, v53\n"
        "v_mfma_f32_16x16x32_bf16 v[32:35], a[108:111], v[12:15], v[32:35]\n"
        "global_load_lds_dwordx4 v254, s[74:75] offset:3072 sc1\n"
        "ds_read_b128 a[132:135], v241 offset:29696\n"
        "s_add_u32 m0, s79, 0x0\n"
        "v_mfma_f32_16x16x32_bf16 v[36:39], a[108:111], v[76:79], v[36:39]\n"
        "s_waitcnt lgkmcnt(4)\n"
        "v_exp_f32 v54, v54\n"
        "v_mfma_f32_16x16x32_bf16 v[188:191], a[140:143], v[16:19], v[188:191]\n"
        "v_exp_f32 v55, v55\n"
        "v_mfma_f32_16x16x32_bf16 v[220:223], a[140:143], v[20:23], v[220:223]\n"
        "s_waitcnt lgkmcnt(3)\n"
        "v_cvt_pk_bf16_f32 v87, v54, v55\n"
        "v_mfma_f32_16x16x32_bf16 v[48:51], a[112:115], v[0:3], v[224:227]\n"
        "ds_read_b128 a[104:107], v240 offset:10240\n"
        "s_add_u32 s76, s66, s90\n"
        "s_addc_u32 s77, s67, 0\n"
        "v_mfma_f32_16x16x32_bf16 v[52:55], a[112:115], v[64:67], v[228:231]\n"
        "v_exp_f32 v32, v32\n"
        "v_mfma_f32_16x16x32_bf16 v[160:163], a[144:147], v[80:83], v[160:163]\n"
        "v_exp_f32 v33, v33\n"
        "v_mfma_f32_16x16x32_bf16 v[192:195], a[144:147], v[84:87], v[192:195]\n"
        "ds_read_b128 a[136:139], v241 offset:30720\n"
        "v_cvt_pk_bf16_f32 v16, v32, v33\n"
        "v_mfma_f32_16x16x32_bf16 a[68:71], a[80:83], v[84:87], a[68:71]\n"
        "ds_read_b128 a[140:143], v241 offset:31744\n"
        "v_xor_b32 v241, 0x8000, v241\n"
        "v_mfma_f32_16x16x32_bf16 v[48:51], a[116:119], v[4:7], v[48:51]\n"
        "ds_read_b128 a[108:111], v240 offset:11264\n"
        "s_cmp_ge_u32 s82, s44\n"
        "s_addc_u32 s90, s82, 0\n"
        "v_mfma_f32_16x16x32_bf16 v[52:55], a[116:119], v[68:71], v[52:55]\n"
        "s_min_u32 s90, s90, 15\n"
        "v_exp_f32 v34, v34\n"
        "v_mfma_f32_16x16x32_bf16 v[164:167], a[148:151], v[80:83], v[164:167]\n"
        "s_lshl_b32 s90, s90, 15\n"
        "v_exp_f32 v35, v35\n"
        "v_mfma_f32_16x16x32_bf16 v[196:199], a[148:151], v[84:87], v[196:199]\n"
        "ds_read_b128 a[112:115], v240 offset:12288\n"
        "v_cvt_pk_bf16_f32 v17, v34, v35\n"
        "v_mfma_f32_16x16x32_bf16 v[48:51], a[120:123], v[8:11], v[48:51]\n"
        "v_exp_f32 v36, v36\n"
        "v_mfma_f32_16x16x32_bf16 v[52:55], a[120:123], v[72:75], v[52:55]\n"
        "v_exp_f32 v37, v37\n"
        "v_mfma_f32_16x16x32_bf16 v[168:171], a[152:155], v[80:83], v[168:171]\n"
        "ds_read_b128 a[116:119], v240 offset:13312\n"
        "v_cvt_pk_bf16_f32 v20, v36, v37\n"
        "v_mfma_f32_16x16x32_bf16 v[200:203], a[152:155], v[84:87], v[200:203]\n"
        "s_waitcnt vmcnt(8) lgkmcnt(3)\n"
        "s_barrier\n"
        "v_mfma_f32_16x16x32_bf16 v[48:51], a[124:127], v[12:15], v[48:51]\n"
        "global_load_lds_dwordx4 v254, s[76:77] offset:0 sc1\n"
        "ds_read_b128 a[144:147], v241 offset:0\n"
        "v_mfma_f32_16x16x32_bf16 v[52:55], a[124:127], v[76:79], v[52:55]\n"
        "v_exp_f32 v38, v38\n"
        "v_mfma_f32_16x16x32_bf16 v[172:175], a[156:159], v[80:83], v[172:175]\n"
        "v_exp_f32 v39, v39\n"
        "v_mfma_f32_16x16x32_bf16 v[204:207], a[156:159], v[84:87], v[204:207]\n"
        "ds_read_b128 a[148:151], v241 offset:1024\n"
        "v_cvt_pk_bf16_f32 v21, v38, v39\n"
        "v_mfma_f32_16x16x32_bf16 v[32:35], a[96:99], v[0:3], v[224:227]\n"
        "global_load_lds_dwordx4 v254, s[76:77] offset:1024 sc1\n"
        "ds_read_b128 a[120:123], v240 offset:14336\n"
        "v_mfma_f32_16x16x32_bf16 v[36:39], a[96:99], v[64:67], v[228:231]\n"
        "v_exp_f32 v48, v48\n"
        "v_mfma_f32_16x16x32_bf16 v[176:179], a[128:131], v[80:83], v[176:179]\n"
        "v_exp_f32 v49, v49\n"
        "v_mfma_f32_16x16x32_bf16 v[208:211], a[128:131], v[84:87], v[208:211]\n"
        "ds_read_b128 a[152:155], v241 offset:2048\n"
        "v_cvt_pk_bf16_f32 v18, v48, v49\n"
        "v_mfma_f32_16x16x32_bf16 v[32:35], a[100:103], v[4:7], v[32:35]\n"
        "global_load_lds_dwordx4 v254, s[76:77] offset:2048 sc1\n"
        "ds_read_b128 a[124:127], v240 offset:15360\n"
        "v_mfma_f32_16x16x32_bf16 v[36:39], a[100:103], v[68:71], v[36:39]\n"
        "v_exp_f32 v50, v50\n"
        "v_mfma_f32_16x16x32_bf16 v[180:183], a[132:135], v[80:83], v[180:183]\n"
        "v_exp_f32 v51, v51\n"
        "v_mfma_f32_16x16x32_bf16 v[212:215], a[132:135], v[84:87], v[212:215]\n"
        "ds_read_b128 a[156:159], v241 offset:3072\n"
        "v_cvt_pk_bf16_f32 v19, v50, v51\n"
        "v_mfma_f32_16x16x32_bf16 v[32:35], a[104:107], v[8:11], v[32:35]\n"
        "global_load_lds_dwordx4 v254, s[76:77] offset:3072 sc1\n"
        "ds_read_b128 a[96:99], v240 offset:16384\n"
        "s_add_u32 m0, s79, 0x1000\n"
        "v_mfma_f32_16x16x32_bf16 v[36:39], a[104:107], v[72:75], v[36:39]\n"
        "s_waitcnt lgkmcnt(4)\n"
        "v_exp_f32 v52, v52\n"
        "v_mfma_f32_16x16x32_bf16 v[184:187], a[136:139], v[80:83], v[184:187]\n"
        "v_exp_f32 v53, v53\n"
        "v_mfma_f32_16x16x32_bf16 v[216:219], a[136:139], v[84:87], v[216:219]\n"
        "s_add_u32 s76, s76, 0x1000\n"
        "s_addc_u32 s77, s77, 0\n"
        "v_cvt_pk_bf16_f32 v22, v52, v53\n"
        "v_mfma_f32_16x16x32_bf16 a[64:67], a[80:83], v[16:19], a[64:67]\n"
        "global_load_lds_dwordx4 v254, s[76:77] offset:0 sc1\n"
        "ds_read_b128 a[128:131], v241 offset:4096\n"
        "v_mfma_f32_16x16x32_bf16 v[32:35], a[108:111], v[12:15], v[32:35]\n"
        "ds_read_b128 a[100:103], v240 offset:17408\n"
        "s_add_u32 s68, s64, s90\n"
        "s_addc_u32 s69, s65, 0\n"
        "v_mfma_f32_16x16x32_bf16 v[36:39], a[108:111], v[76:79], v[36:39]\n"
        "s_waitcnt lgkmcnt(4)\n"
        "v_exp_f32 v54, v54\n"
        "v_mfma_f32_16x16x32_bf16 v[188:191], a[140:143], v[80:83], v[188:191]\n"
        "s_waitcnt lgkmcnt(3)\n"
        "v_exp_f32 v55, v55\n"
        "v_mfma_f32_16x16x32_bf16 v[220:223], a[140:143], v[84:87], v[220:223]\n"
        "global_load_lds_dwordx4 v254, s[76:77] offset:1024 sc1\n"
        "v_cvt_pk_bf16_f32 v23, v54, v55\n"
        "v_mfma_f32_16x16x32_bf16 v[48:51], a[112:115], v[0:3], v[224:227]\n"
        "ds_read_b128 a[132:135], v241 offset:5120\n"
        "v_mfma_f32_16x16x32_bf16 v[52:55], a[112:115], v[64:67], v[228:231]\n"
        "ds_read_b128 a[104:107], v240 offset:18432\n"
        "v_mfma_f32_16x16x32_bf16 v[160:163], a[144:147], v[16:19], v[160:163]\n"
        "v_exp_f32 v32, v32\n"
        "v_mfma_f32_16x16x32_bf16 v[192:195], a[144:147], v[20:23], v[192:195]\n"
        "v_exp_f32 v33, v33\n"
        "v_mfma_f32_16x16x32_bf16 a[68:71], a[80:83], v[20:23], a[68:71]\n"
        "global_load_lds_dwordx4 v254, s[76:77] offset:2048 sc1\n"
        "v_cvt_pk_bf16_f32 v80, v32, v33\n"
        "v_mfma_f32_16x16x32_bf16 v[48:51], a[116:119], v[4:7], v[48:51]\n"
        "ds_read_b128 a[136:139], v241 offset:6144\n"
        "v_mfma_f32_16x16x32_bf16 v[52:55], a[116:119], v[68:71], v[52:55]\n"
        "ds_read_b128 a[108:111], v240 offset:19456\n"
        "v_mfma_f32_16x16x32_bf16 v[164:167], a[148:151], v[16:19], v[164:167]\n"
        "v_exp_f32 v34, v34\n"
        "v_mfma_f32_16x16x32_bf16 v[196:199], a[148:151], v[20:23], v[196:199]\n"
        "v_exp_f32 v35, v35\n"
        "v_mfma_f32_16x16x32_bf16 v[48:51], a[120:123], v[8:11], v[48:51]\n"
        "global_load_lds_dwordx4 v254, s[76:77] offset:3072 sc1\n"
        "v_cvt_pk_bf16_f32 v81, v34, v35\n"
        "v_mfma_f32_16x16x32_bf16 v[52:55], a[120:123], v[72:75], v[52:55]\n"
        "ds_read_b128 a[140:143], v241 offset:7168\n"
        "s_add_u32 s74, s68, 0x1000\n"
        "s_addc_u32 s75, s69, 0\n"
        "v_mfma_f32_16x16x32_bf16 v[168:171], a[152:155], v[16:19], v[168:171]\n"
        "ds_read_b128 a[112:115], v240 offset:20480\n"
        "s_waitcnt lgkmcnt(4)\n"
        "v_mfma_f32_16x16x32_bf16 v[200:203], a[152:155], v[20:23], v[200:203]\n"
        "v_exp_f32 v36, v36\n"
        "v_mfma_f32_16x16x32_bf16 v[48:51], a[124:127], v[12:15], v[48:51]\n"
        "v_exp_f32 v37, v37\n"
        "v_mfma_f32_16x16x32_bf16 v[52:55], a[124:127], v[76:79], v[52:55]\n"
        "ds_read_b128 a[144:147], v241 offset:8192\n"
        "v_cvt_pk_bf16_f32 v84, v36, v37\n"
        "v_mfma_f32_16x16x32_bf16 v[172:175], a[156:159], v[16:19], v[172:175]\n"
        "ds_read_b128 a[116:119], v240 offset:21504\n"
        "v_mfma_f32_16x16x32_bf16 v[204:207], a[156:159], v[20:23], v[204:207]\n"
        "s_waitcnt lgkmcnt(4)\n"
        "v_exp_f32 v38, v38\n"
        "v_mfma_f32_16x16x32_bf16 v[32:35], a[96:99], v[0:3], v[224:227]\n"
        "v_exp_f32 v39, v39\n"
        "v_mfma_f32_16x16x32_bf16 v[176:179], a[128:131], v[16:19], v[176:179]\n"
        "ds_read_b128 a[148:151], v241 offset:9216\n"
        "v_cvt_pk_bf16_f32 v85, v38, v39\n"
        "v_mfma_f32_16x16x32_bf16 v[36:39], a[96:99], v[64:67], v[228:231]\n"
        "ds_read_b128 a[120:123], v240 offset:22528\n"
        "v_mfma_f32_16x16x32_bf16 v[208:211], a[128:131], v[20:23], v[208:211]\n"
        "v_exp_f32 v48, v48\n"
        "v_mfma_f32_16x16x32_bf16 v[32:35], a[100:103], v[4:7], v[32:35]\n"
        "v_exp_f32 v49, v49\n"
        "v_mfma_f32_16x16x32_bf16 v[36:39], a[100:103], v[68:71], v[36:39]\n"
        "ds_read_b128 a[152:155], v241 offset:10240\n"
        "v_cvt_pk_bf16_f32 v82, v48, v49\n"
        "v_mfma_f32_16x16x32_bf16 v[180:183], a[132:135], v[16:19], v[180:183]\n"
        "ds_read_b128 a[124:127], v240 offset:23552\n"
        "s_waitcnt lgkmcnt(4)\n"
        "v_mfma_f32_16x16x32_bf16 v[212:215], a[132:135], v[20:23], v[212:215]\n"
        "v_exp_f32 v50, v50\n"
        "v_mfma_f32_16x16x32_bf16 v[32:35], a[104:107], v[8:11], v[32:35]\n"
        "v_exp_f32 v51, v51\n"
        "v_mfma_f32_16x16x32_bf16 v[36:39], a[104:107], v[72:75], v[36:39]\n"
        "ds_read_b128 a[156:159], v241 offset:11264\n"
        "v_cvt_pk_bf16_f32 v83, v50, v51\n"
        "v_mfma_f32_16x16x32_bf16 v[184:187], a[136:139], v[16:19], v[184:187]\n"
        "ds_read_b128 a[96:99], v240 offset:24576\n"
        "v_mfma_f32_16x16x32_bf16 v[216:219], a[136:139], v[20:23], v[216:219]\n"
        "v_exp_f32 v52, v52\n"
        "v_mfma_f32_16x16x32_bf16 a[64:67], a[80:83], v[80:83], a[64:67]\n"
        "v_exp_f32 v53, v53\n"
        "v_mfma_f32_16x16x32_bf16 v[32:35], a[108:111], v[12:15], v[32:35]\n"
        "s_waitcnt lgkmcnt(3)\n"
        "v_cvt_pk_bf16_f32 v86, v52, v53\n"
        "v_mfma_f32_16x16x32_bf16 v[36:39], a[108:111], v[76:79], v[36:39]\n"
        "ds_read_b128 a[128:131], v241 offset:12288\n"
        "v_mfma_f32_16x16x32_bf16 v[188:191], a[140:143], v[16:19], v[188:191]\n"
        "ds_read_b128 a[100:103], v240 offset:25600\n"
        "v_mfma_f32_16x16x32_bf16 v[220:223], a[140:143], v[20:23], v[220:223]\n"
        "s_waitcnt lgkmcnt(4)\n"
        "v_exp_f32 v54, v54\n"
        "v_mfma_f32_16x16x32_bf16 v[48:51], a[112:115], v[0:3], v[224:227]\n"
        "s_waitcnt lgkmcnt(3)\n"
        "v_exp_f32 v55, v55\n"
        "v_mfma_f32_16x16x32_bf16 v[160:163], a[144:147], v[80:83], v[160:163]\n"
        "ds_read_b128 a[132:135], v241 offset:13312\n"
        "v_cvt_pk_bf16_f32 v87, v54, v55\n"
        "v_mfma_f32_16x16x32_bf16 v[52:55], a[112:115], v[64:67], v[228:231]\n"
        "ds_read_b128 a[104:107], v240 offset:26624\n"
        "v_mfma_f32_16x16x32_bf16 v[192:195], a[144:147], v[84:87], v[192:195]\n"
        "v_exp_f32 v32, v32\n"
        "v_mfma_f32_16x16x32_bf16 a[68:71], a[80:83], v[84:87], a[68:71]\n"
        "v_exp_f32 v33, v33\n"
        "v_mfma_f32_16x16x32_bf16 v[48:51], a[116:119], v[4:7], v[48:51]\n"
        "ds_read_b128 a[136:139], v241 offset:14336\n"
        "v_cvt_pk_bf16_f32 v16, v32, v33\n"
        "v_mfma_f32_16x16x32_bf16 v[52:55], a[116:119], v[68:71], v[52:55]\n"
        "ds_read_b128 a[108:111], v240 offset:27648\n"
        "v_mfma_f32_16x16x32_bf16 v[164:167], a[148:151], v[80:83], v[164:167]\n"
        "v_exp_f32 v34, v34\n"
        "v_mfma_f32_16x16x32_bf16 v[196:199], a[148:151], v[84:87], v[196:199]\n"
        "v_exp_f32 v35, v35\n"
        "v_mfma_f32_16x16x32_bf16 v[48:51], a[120:123], v[8:11], v[48:51]\n"
        "ds_read_b128 a[140:143], v241 offset:15360\n"
        "v_cvt_pk_bf16_f32 v17, v34, v35\n"
        "v_mfma_f32_16x16x32_bf16 v[52:55], a[120:123], v[72:75], v[52:55]\n"
        "ds_read_b128 a[112:115], v240 offset:28672\n"
        "v_mfma_f32_16x16x32_bf16 v[168:171], a[152:155], v[80:83], v[168:171]\n"
        "s_waitcnt lgkmcnt(4)\n"
        "v_exp_f32 v36, v36\n"
        "v_mfma_f32_16x16x32_bf16 v[200:203], a[152:155], v[84:87], v[200:203]\n"
        "v_exp_f32 v37, v37\n"
        "v_mfma_f32_16x16x32_bf16 v[48:51], a[124:127], v[12:15], v[48:51]\n"
        "ds_read_b128 a[144:147], v241 offset:16384\n"
        "v_cvt_pk_bf16_f32 v20, v36, v37\n"
        "v_mfma_f32_16x16x32_bf16 v[52:55], a[124:127], v[76:79], v[52:55]\n"
        "ds_read_b128 a[116:119], v240 offset:29696\n"
        "v_mfma_f32_16x16x32_bf16 v[172:175], a[156:159], v[80:83], v[172:175]\n"
        "v_exp_f32 v38, v38\n"
        "v_mfma_f32_16x16x32_bf16 v[204:207], a[156:159], v[84:87], v[204:207]\n"
        "s_waitcnt lgkmcnt(4)\n"
        "v_exp_f32 v39, v39\n"
        "v_mfma_f32_16x16x32_bf16 v[32:35], a[96:99], v[0:3], v[224:227]\n"
        "ds_read_b128 a[148:151], v241 offset:17408\n"
        "v_cvt_pk_bf16_f32 v21, v38, v39\n"
        "v_mfma_f32_16x16x32_bf16 v[36:39], a[96:99], v[64:67], v[228:231]\n"
        "ds_read_b128 a[120:123], v240 offset:30720\n"
        "v_mfma_f32_16x16x32_bf16 v[176:179], a[128:131], v[80:83], v[176:179]\n"
        "ds_read_b128 a[124:127], v240 offset:31744\n"
        "v_xor_b32 v240, 0x8000, v240\n"
        "v_mfma_f32_16x16x32_bf16 v[208:211], a[128:131], v[84:87], v[208:211]\n"
        "v_exp_f32 v48, v48\n"
        "v_mfma_f32_16x16x32_bf16 v[32:35], a[100:103], v[4:7], v[32:35]\n"
        "v_exp_f32 v49, v49\n"
        "v_mfma_f32_16x16x32_bf16 v[36:39], a[100:103], v[68:71], v[36:39]\n"
        "ds_read_b128 a[152:155], v241 offset:18432\n"
        "v_cvt_pk_bf16_f32 v18, v48, v49\n"
        "v_mfma_f32_16x16x32_bf16 v[180:183], a[132:135], v[80:83], v[180:183]\n"
        "s_waitcnt lgkmcnt(4)\n"
        "v_exp_f32 v50, v50\n"
        "v_mfma_f32_16x16x32_bf16 v[212:215], a[132:135], v[84:87], v[212:215]\n"
        "v_exp_f32 v51, v51\n"
        "v_mfma_f32_16x16x32_bf16 v[32:35], a[104:107], v[8:11], v[32:35]\n"
        "ds_read_b128 a[156:159], v241 offset:19456\n"
        "v_cvt_pk_bf16_f32 v19, v50, v51\n"
        "v_mfma_f32_16x16x32_bf16 v[36:39], a[104:107], v[72:75], v[36:39]\n"
        "s_cmp_eq_u32 s82, s83\n"
        "s_cselect_b64 s[68:69], s[92:93], s[68:69]\n"
        "s_cselect_b32 s80, s50, s78\n"
        "v_mfma_f32_16x16x32_bf16 v[184:187], a[136:139], v[80:83], v[184:187]\n"
        "s_waitcnt vmcnt(8) lgkmcnt(2)\n"
        "s_barrier\n"
        "s_add_u32 m0, s80, 0x0\n"
        "v_mfma_f32_16x16x32_bf16 v[216:219], a[136:139], v[84:87], v[216:219]\n"
        "s_cmp_eq_u32 s82, s83\n"
        "s_cselect_b64 s[74:75], s[94:95], s[74:75]\n"
        "s_cselect_b32 s81, s51, s81\n"
        "v_mfma_f32_16x16x32_bf16 a[64:67], a[80:83], v[16:19], a[64:67]\n"
        "global_load_lds_dwordx4 v254, s[68:69] offset:0 sc1\n"
        "ds_read_b128 a[96:99], v240 offset:0\n"
        "v_mfma_f32_16x16x32_bf16 v[32:35], a[108:111], v[12:15], v[32:35]\n"
        "v_exp_f32 v52, v52\n"
        "v_mfma_f32_16x16x32_bf16 v[36:39], a[108:111], v[76:79], v[36:39]\n"
        "s_waitcnt lgkmcnt(2)\n"
        "v_exp_f32 v53, v53\n"
        "v_mfma_f32_16x16x32_bf16 v[188:191], a[140:143], v[80:83], v[188:191]\n"
        "ds_read_b128 a[128:131], v241 offset:20480\n"
        "v_cvt_pk_bf16_f32 v22, v52, v53\n"
        "v_mfma_f32_16x16x32_bf16 v[220:223], a[140:143], v[84:87], v[220:223]\n"
        "global_load_lds_dwordx4 v254, s[68:69] offset:1024 sc1\n"
        "ds_read_b128 a[100:103], v240 offset:1024\n"
        "v_mfma_f32_16x16x32_bf16 v[48:51], a[112:115], v[0:3], v[224:227]\n"
        "s_waitcnt lgkmcnt(3)\n"
        "v_exp_f32 v54, v54\n"
        "v_mfma_f32_16x16x32_bf16 v[160:163], a[144:147], v[16:19], v[160:163]\n"
        "v_exp_f32 v55, v55\n"
        "v_mfma_f32_16x16x32_bf16 v[48:51], a[116:119], v[4:7], v[48:51]\n"
        "ds_read_b128 a[132:135], v241 offset:21504\n"
        "v_cvt_pk_bf16_f32 v23, v54, v55\n"
        "v_mfma_f32_16x16x32_bf16 v[52:55], a[112:115], v[64:67], v[228:231]\n"
        "global_load_lds_dwordx4 v254, s[68:69] offset:2048 sc1\n"
        "ds_read_b128 a[104:107], v240 offset:2048\n"
        "v_mfma_f32_16x16x32_bf16 v[192:195], a[144:147], v[20:23], v[192:195]\n"
        "v_exp_f32 v32, v32\n"
        "v_mfma_f32_16x16x32_bf16 a[68:71], a[80:83], v[20:23], a[68:71]\n"
        "v_exp_f32 v33, v33\n"
        "v_mfma_f32_16x16x32_bf16 v[52:55], a[116:119], v[68:71], v[52:55]\n"
        "ds_read_b128 a[136:139], v241 offset:22528\n"
        "v_cvt_pk_bf16_f32 v80, v32, v33\n"
        "v_mfma_f32_16x16x32_bf16 v[164:167], a[148:151], v[16:19], v[164:167]\n"
        "global_load_lds_dwordx4 v254, s[68:69] offset:3072 sc1\n"
        "ds_read_b128 a[108:111], v240 offset:3072\n"
        "v_mfma_f32_16x16x32_bf16 v[196:199], a[148:151], v[20:23], v[196:199]\n"
        "v_exp_f32 v34, v34\n"
        "v_mfma_f32_16x16x32_bf16 v[48:51], a[120:123], v[8:11], v[48:51]\n"
        "v_exp_f32 v35, v35\n"
        "v_mfma_f32_16x16x32_bf16 v[52:55], a[120:123], v[72:75], v[52:55]\n"
        "ds_read_b128 a[140:143], v241 offset:23552\n"
        "v_cvt_pk_bf16_f32 v81, v34, v35\n"
        "v_mfma_f32_16x16x32_bf16 v[168:171], a[152:155], v[16:19], v[168:171]\n"
        "ds_read_b128 a[112:115], v240 offset:4096\n"
        "s_waitcnt lgkmcnt(7)\n"
        "v_mfma_f32_16x16x32_bf16 v[200:203], a[152:155], v[20:23], v[200:203]\n"
        "v_exp_f32 v36, v36\n"
        "v_mfma_f32_16x16x32_bf16 v[48:51], a[124:127], v[12:15], v[48:51]\n"
        "v_exp_f32 v37, v37\n"
        "v_mfma_f32_16x16x32_bf16 v[52:55], a[124:127], v[76:79], v[52:55]\n"
        "ds_read_b128 a[144:147], v241 offset:24576\n"
        "v_cvt_pk_bf16_f32 v84, v36, v37\n"
        "v_mfma_f32_16x16x32_bf16 v[172:175], a[156:159], v[16:19], v[172:175]\n"
        "ds_read_b128 a[116:119], v240 offset:5120\n"
        "v_mfma_f32_16x16x32_bf16 v[204:207], a[156:159], v[20:23], v[204:207]\n"
        "v_exp_f32 v38, v38\n"
        "v_exp_f32 v39, v39\n"
        "ds_read_b128 a[148:151], v241 offset:25600\n"
        "v_cvt_pk_bf16_f32 v85, v38, v39\n"
        "s_cmp_lg_u32 s91, 0\n"
        "s_cbranch_scc1 LSI_%=\n"
        "; ---- BACK\n"
        "LTL_%=:\n"
        "s_mul_i32 s14, s40, 4224\n"
        "v_bfe_u32 v232, v254, 4, 4\n"
        "s_add_u32 s15, s51, 4098\n"
        "s_add_u32 s14, s14, 0x20240\n"
        "v_and_b32 v224, 1, v232\n"
        "s_sub_u32 s15, s15, s14\n"
        "v_mul_lo_u32 v224, v224, s15\n"
        "v_add_u32 v224, s14, v224\n"
        "v_lshrrev_b32 v233, 8, v254\n"
        "s_add_u32 s16, s51, 4096\n"
        "v_lshlrev_b32 v228, 5, v232\n"
        "v_lshl_add_u32 v224, v232, 1, v224\n"
        "s_add_u32 s14, s51, 480\n"
        "v_mov_b32 v236, 0\n"
        "v_mov_b32 v237, 0\n"
        "v_mov_b32 v238, 0\n"
        "v_mov_b32 v239, 0\n"
        "v_add_u32 v229, s16, v254\n"
        "v_lshl_add_u32 v224, v233, 4, v224\n"
        "v_sub_u32 v228, s14, v228\n"
        "s_mov_b64 exec, 3\n"
        "ds_write_b128 v229, v[236:239]\n"
        "s_mov_b64 exec, -1\n"
        "v_add_u32 v225, 1024, v224\n"
        "v_add_u32 v226, 2048, v224\n"
        "v_add_u32 v227, 3072, v224\n"
        "v_lshl_add_u32 v228, v233, 4, v228\n"
        "ds_read2_b32 a[0:1], v224 offset0:0 offset1:1\n"
        "ds_read2_b32 a[2:3], v224 offset0:2 offset1:3\n"
        "v_add_u32 v230, 3584, v228\n"
        "v_min_u32 v230, s16, v230\n"
        "ds_read_b128 a[192:195], v230 offset:0\n"
        "ds_read2_b32 a[4:5], v224 offset0:128 offset1:129\n"
        "ds_read2_b32 a[6:7], v224 offset0:130 offset1:131\n"
        "ds_read_b128 a[196:199], v228 offset:3072\n"
        "ds_read2_b32 a[8:9], v225 offset0:0 offset1:1\n"
        "ds_read2_b32 a[10:11], v225 offset0:2 offset1:3\n"
        "ds_read_b128 a[200:203], v228 offset:2560\n"
        "ds_read2_b32 a[12:13], v225 offset0:128 offset1:129\n"
        "ds_read2_b32 a[14:15], v225 offset0:130 offset1:131\n"
        "ds_read_b128 a[204:207], v228 offset:2048\n"
        "ds_read2_b32 a[16:17], v226 offset0:0 offset1:1\n"
        "ds_read2_b32 a[18:19], v226 offset0:2 offset1:3\n"
        "ds_read_b128 a[208:211], v228 offset:1536\n"
        "s_lshl_b32 s53, s45, 20\n"
        "ds_read2_b32 a[20:21], v226 offset0:128 offset1:129\n"
        "s_lshl_b32 s96, s43, 15\n"
        "ds_read2_b32 a[22:23], v226 offset0:130 offset1:131\n"
        "s_add_u32 s53, s53, s96\n"
        "ds_read_b128 a[212:215], v228 offset:1024\n"
        "s_lshl_b32 s96, s40, 10\n"
        "ds_read2_b32 a[24:25], v227 offset0:0 offset1:1\n"
        "s_add_u32 s53, s53, s96\n"
        "ds_read2_b32 a[26:27], v227 offset0:2 offset1:3\n"
        "s_add_u32 s53, s53, 0x2001000\n"
        "ds_read_b128 a[216:219], v228 offset:512\n"
        "ds_read2_b32 a[28:29], v227 offset0:128 offset1:129\n"
        "ds_read2_b32 a[30:31], v227 offset0:130 offset1:131\n"
        "ds_read_b128 a[220:223], v228 offset:0\n"
        "v_mfma_f32_16x16x32_bf16 v[176:179], a[128:131], v[16:19], v[176:179]\n"
        "s_add_u32 s70, s38, s53\n"
        "s_addc_u32 s71, s39, 0\n"
        "s_waitcnt lgkmcnt(14)\n"
        "v_mfma_f32_16x16x32_bf16 v[208:211], a[128:131], v[20:23], v[208:211]\n"
        "v_exp_f32 v48, v48\n"
        "v_mfma_f32_16x16x32_bf16 v[180:183], a[132:135], v[16:19], v[180:183]\n"
        "v_exp_f32 v49, v49\n"
        "v_mfma_f32_16x16x32_bf16 v[212:215], a[132:135], v[20:23], v[212:215]\n"
        "ds_read_b128 a[152:155], v241 offset:26624\n"
        "v_cvt_pk_bf16_f32 v82, v48, v49\n"
        "v_mfma_f32_16x16x32_bf16 v[184:187], a[136:139], v[16:19], v[184:187]\n"
        "v_exp_f32 v50, v50\n"
        "v_mfma_f32_16x16x32_bf16 v[216:219], a[136:139], v[20:23], v[216:219]\n"
        "s_waitcnt lgkmcnt(6)\n"
        "v_exp_f32 v51, v51\n"
        "v_mfma_f32_16x16x32_bf16 v[188:191], a[140:143], v[16:19], v[188:191]\n"
        "ds_read_b128 a[156:159], v241 offset:27648\n"
        "v_cvt_pk_bf16_f32 v83, v50, v51\n"
        "s_nop 1\n"
        "v_mfma_f32_16x16x32_bf16 a[64:67], a[80:83], v[80:83], a[64:67]\n"
        "v_exp_f32 v52, v52\n"
        "v_mfma_f32_16x16x32_bf16 v[220:223], a[140:143], v[20:23], v[220:223]\n"
        "v_exp_f32 v53, v53\n"
        "v_mfma_f32_16x16x32_bf16 v[160:163], a[144:147], v[80:83], v[160:163]\n"
        "s_waitcnt lgkmcnt(2)\n"
        "v_cvt_pk_bf16_f32 v86, v52, v53\n"
        "v_add_u32 v231, 3648, v228\n"
        "v_mfma_f32_16x16x32_bf16 v[164:167], a[148:151], v[80:83], v[164:167]\n"
        "v_accvgpr_read_b32 v248, a64\n"
        "s_waitcnt lgkmcnt(1)\n"
        "v_mfma_f32_16x16x32_bf16 v[168:171], a[152:155], v[80:83], v[168:171]\n"
        "v_rcp_f32 v246, v248\n"
        "v_mfma_f32_16x16x32_bf16 v[96:99], a[0:3], a[192:195], 0\n"
        "ds_read_b128 a[128:131], v241 offset:28672\n"
        "v_mul_f32 v160, v160, v246\n"
        "v_mfma_f32_16x16x32_bf16 v[100:103], a[0:3], a[196:199], 0\n"
        "s_waitcnt lgkmcnt(1)\n"
        "v_exp_f32 v54, v54\n"
        "v_mfma_f32_16x16x32_bf16 v[172:175], a[156:159], v[80:83], v[172:175]\n"
        "v_exp_f32 v55, v55\n"
        "v_mfma_f32_16x16x32_bf16 v[104:107], a[0:3], a[200:203], 0\n"
        "ds_read_b128 a[132:135], v241 offset:29696\n"
        "v_cvt_pk_bf16_f32 v87, v54, v55\n"
        "s_nop 1\n"
        "v_mfma_f32_16x16x32_bf16 v[192:195], a[144:147], v[84:87], v[192:195]\n"
        "ds_read_b128 a[136:139], v241 offset:30720\n"
        "v_mul_f32 v161, v161, v246\n"
        "v_mfma_f32_16x16x32_bf16 a[68:71], a[80:83], v[84:87], a[68:71]\n"
        "ds_read_b128 a[140:143], v241 offset:31744\n"
        "v_mul_f32 v162, v162, v246\n"
        "v_mfma_f32_16x16x32_bf16 v[196:199], a[148:151], v[84:87], v[196:199]\n"
        "v_mul_f32 v163, v163, v246\n"
        "v_mul_f32 v164, v164, v246\n"
        "v_mfma_f32_16x16x32_bf16 v[200:203], a[152:155], v[84:87], v[200:203]\n"
        "v_mul_f32 v165, v165, v246\n"
        "v_mul_f32 v166, v166, v246\n"
        "v_mfma_f32_16x16x32_bf16 v[204:207], a[156:159], v[84:87], v[204:207]\n"
        "s_waitcnt lgkmcnt(3)\n"
        "v_accvgpr_read_b32 v249, a68\n"
        "v_mul_f32 v167, v167, v246\n"
        "v_mfma_f32_16x16x32_bf16 v[176:179], a[128:131], v[80:83], v[176:179]\n"
        "v_rcp_f32 v247, v249\n"
        "v_mfma_f32_16x16x32_bf16 v[208:211], a[128:131], v[84:87], v[208:211]\n"
        "v_cvt_pk_bf16_f32 v160, v160, v161\n"
        "s_waitcnt lgkmcnt(2)\n"
        "v_mfma_f32_16x16x32_bf16 v[180:183], a[132:135], v[80:83], v[180:183]\n"
        "v_cvt_pk_bf16_f32 v161, v162, v163\n"
        "v_cvt_pk_bf16_f32 v162, v164, v165\n"
        "v_mfma_f32_16x16x32_bf16 v[212:215], a[132:135], v[84:87], v[212:215]\n"
        "s_waitcnt lgkmcnt(1)\n"
        "v_cvt_pk_bf16_f32 v163, v166, v167\n"
        "v_mul_f32 v192, v192, v247\n"
        "v_mfma_f32_16x16x32_bf16 v[184:187], a[136:139], v[80:83], v[184:187]\n"
        "global_store_dwordx4 v254, v[160:163], s[70:71] offset:0 sc1\n"
        "s_waitcnt lgkmcnt(0)\n"
        "v_mul_f32 v193, v193, v247\n"
        "v_mul_f32 v194, v194, v247\n"
        "v_mul_f32 v195, v195, v247\n"
        "v_mfma_f32_16x16x32_bf16 v[216:219], a[136:139], v[84:87], v[216:219]\n"
        "v_mul_f32 v196, v196, v247\n"
        "v_mul_f32 v197, v197, v247\n"
        "v_mfma_f32_16x16x32_bf16 v[188:191], a[140:143], v[80:83], v[188:191]\n"
        "v_mul_f32 v198, v198, v247\n"
        "v_mul_f32 v199, v199, v247\n"
        "v_mfma_f32_16x16x32_bf16 v[220:223], a[140:143], v[84:87], v[220:223]\n"
        "v_cvt_pk_bf16_f32 v192, v192, v193\n"
        "v_cvt_pk_bf16_f32 v193, v194, v195\n"
        "v_mfma_f32_16x16x32_bf16 v[108:111], a[0:3], a[204:207], 0\n"
        "v_cvt_pk_bf16_f32 v194, v196, v197\n"
        "v_cvt_pk_bf16_f32 v195, v198, v199\n"
        "v_mfma_f32_16x16x32_bf16 v[128:131], a[0:3], a[208:211], 0\n"
        "global_store_dwordx4 v254, v[192:195], s[70:71] offset:-4096 sc1\n"
        "s_add_u32 s70, s70, 0x2000\n"
        "s_addc_u32 s71, s71, 0\n"
        "v_mul_f32 v168, v168, v246\n"
        "v_mul_f32 v169, v169, v246\n"
        "v_mul_f32 v170, v170, v246\n"
        "v_mfma_f32_16x16x32_bf16 v[132:135], a[0:3], a[212:215], 0\n"
        "v_mul_f32 v171, v171, v246\n"
        "v_mul_f32 v172, v172, v246\n"
        "v_mfma_f32_16x16x32_bf16 v[136:139], a[0:3], a[216:219], 0\n"
        "v_mul_f32 v173, v173, v246\n"
        "v_mul_f32 v174, v174, v246\n"
        "v_mul_f32 v175, v175, v246\n"
        "v_cvt_pk_bf16_f32 v168, v168, v169\n"
        "v_cvt_pk_bf16_f32 v169, v170, v171\n"
        "v_cvt_pk_bf16_f32 v170, v172, v173\n"
        "v_cvt_pk_bf16_f32 v171, v174, v175\n"
        "v_mfma_f32_16x16x32_bf16 v[140:143], a[0:3], a[220:223], 0\n"
        "global_store_dwordx4 v254, v[168:171], s[70:71] offset:0 sc1\n"
        "v_mul_f32 v200, v200, v247\n"
        "v_mul_f32 v201, v201, v247\n"
        "v_mul_f32 v202, v202, v247\n"
        "v_mul_f32 v203, v203, v247\n"
        "v_mul_f32 v204, v204, v247\n"
        "v_mul_f32 v205, v205, v247\n"
        "v_mul_f32 v206, v206, v247\n"
        "v_mul_f32 v207, v207, v247\n"
        "v_cvt_pk_bf16_f32 v200, v200, v201\n"
        "v_cvt_pk_bf16_f32 v201, v202, v203\n"
        "v_cvt_pk_bf16_f32 v202, v204, v205\n"
        "v_cvt_pk_bf16_f32 v203, v206, v207\n"
        "v_mfma_f32_16x16x32_bf16 v[100:103], a[4:7], a[192:195], v[100:103]\n"
        "v_mul_f32 v176, v176, v246\n"
        "v_mul_f32 v177, v177, v246\n"
        "v_mfma_f32_16x16x32_bf16 v[104:107], a[4:7], a[196:199], v[104:107]\n"
        "global_store_dwordx4 v254, v[200:203], s[70:71] offset:-4096 sc1\n"
        "s_add_u32 s70, s70, 0x2000\n"
        "s_addc_u32 s71, s71, 0\n"
        "v_mul_f32 v178, v178, v246\n"
        "v_mul_f32 v179, v179, v246\n"
        "v_mul_f32 v180, v180, v246\n"
        "v_mfma_f32_16x16x32_bf16 v[108:111], a[4:7], a[200:203], v[108:111]\n"
        "v_mul_f32 v181, v181, v246\n"
        "v_mul_f32 v182, v182, v246\n"
        "v_mul_f32 v183, v183, v246\n"
        "v_cvt_pk_bf16_f32 v176, v176, v177\n"
        "v_cvt_pk_bf16_f32 v177, v178, v179\n"
        "v_cvt_pk_bf16_f32 v178, v180, v181\n"
        "v_cvt_pk_bf16_f32 v179, v182, v183\n"
        "v_mfma_f32_16x16x32_bf16 v[128:131], a[4:7], a[204:207], v[128:131]\n"
        "global_store_dwordx4 v254, v[176:179], s[70:71] offset:0 sc1\n"
        "v_mul_f32 v208, v208, v247\n"
        "v_mul_f32 v209, v209, v247\n"
        "v_mul_f32 v210, v210, v247\n"
        "v_mul_f32 v211, v211, v247\n"
        "v_mul_f32 v212, v212, v247\n"
        "v_mul_f32 v213, v213, v247\n"
        "v_mul_f32 v214, v214, v247\n"
        "v_mul_f32 v215, v215, v247\n"
        "v_cvt_pk_bf16_f32 v208, v208, v209\n"
        "v_cvt_pk_bf16_f32 v209, v210, v211\n"
        "v_cvt_pk_bf16_f32 v210, v212, v213\n"
        "v_cvt_pk_bf16_f32 v211, v214, v215\n"
        "v_mfma_f32_16x16x32_bf16 v[132:135], a[4:7], a[208:211], v[132:135]\n"
        "v_mul_f32 v184, v184, v246\n"
        "v_mul_f32 v185, v185, v246\n"
        "v_mfma_f32_16x16x32_bf16 v[136:139], a[4:7], a[212:215], v[136:139]\n"
        "global_store_dwordx4 v254, v[208:211], s[70:71] offset:-4096 sc1\n"
        "s_add_u32 s70, s70, 0x2000\n"
        "s_addc_u32 s71, s71, 0\n"
        "v_mul_f32 v186, v186, v246\n"
        "v_mul_f32 v187, v187, v246\n"
        "v_mul_f32 v188, v188, v246\n"
        "v_mfma_f32_16x16x32_bf16 v[140:143], a[4:7], a[216:219], v[140:143]\n"
        "v_mul_f32 v189, v189, v246\n"
        "v_mul_f32 v190, v190, v246\n"
        "v_mul_f32 v191, v191, v246\n"
        "v_cvt_pk_bf16_f32 v184, v184, v185\n"
        "v_cvt_pk_bf16_f32 v185, v186, v187\n"
        "v_cvt_pk_bf16_f32 v186, v188, v189\n"
        "v_cvt_pk_bf16_f32 v187, v190, v191\n"
        "v_mfma_f32_16x16x32_bf16 v[104:107], a[8:11], a[192:195], v[104:107]\n"
        "global_store_dwordx4 v254, v[184:187], s[70:71] offset:0 sc1\n"
        "ds_read2_b32 a[32:33], v224 offset0:16 offset1:17\n"
        "ds_read2_b32 a[34:35], v224 offset0:18 offset1:19\n"
        "v_mul_f32 v216, v216, v247\n"
        "v_mul_f32 v217, v217, v247\n"
        "v_mul_f32 v218, v218, v247\n"
        "v_mul_f32 v219, v219, v247\n"
        "v_mul_f32 v220, v220, v247\n"
        "v_mul_f32 v221, v221, v247\n"
        "v_mul_f32 v222, v222, v247\n"
        "v_mul_f32 v223, v223, v247\n"
        "v_cvt_pk_bf16_f32 v216, v216, v217\n"
        "v_cvt_pk_bf16_f32 v217, v218, v219\n"
        "v_cvt_pk_bf16_f32 v218, v220, v221\n"
        "v_cvt_pk_bf16_f32 v219, v222, v223\n"
        "v_add_f32 v247, v248, v249\n"
        "v_cmp_nge_f32 vcc, 0x4e6e6b28, v247\n"
        "v_mfma_f32_16x16x32_bf16 v[108:111], a[8:11], a[196:199], v[108:111]\n"
        "global_store_dwordx4 v254, v[216:219], s[70:71] offset:-4096 sc1\n"
        "s_cbranch_vccnz LRH_%=\n"
        "LSH_%=:\n"
        "v_min_u32 v231, s16, v231\n"
        "ds_read_b128 a[224:227], v231 offset:0\n"
        "v_mfma_f32_16x16x32_bf16 v[128:131], a[8:11], a[200:203], v[128:131]\n"
        "ds_read2_b32 a[36:37], v224 offset0:144 offset1:145\n"
        "v_mfma_f32_16x16x32_bf16 v[132:135], a[8:11], a[204:207], v[132:135]\n"
        "ds_read2_b32 a[38:39], v224 offset0:146 offset1:147\n"
        "v_mfma_f32_16x16x32_bf16 v[136:139], a[8:11], a[208:211], v[136:139]\n"
        "ds_read_b128 a[228:231], v228 offset:3136\n"
        "v_mfma_f32_16x16x32_bf16 v[140:143], a[8:11], a[212:215], v[140:143]\n"
        "ds_read2_b32 a[40:41], v225 offset0:16 offset1:17\n"
        "v_mfma_f32_16x16x32_bf16 v[108:111], a[12:15], a[192:195], v[108:111]\n"
        "ds_read2_b32 a[42:43], v225 offset0:18 offset1:19\n"
        "v_mfma_f32_16x16x32_bf16 v[128:131], a[12:15], a[196:199], v[128:131]\n"
        "ds_read_b128 a[232:235], v228 offset:2624\n"
        "v_mfma_f32_16x16x32_bf16 v[132:135], a[12:15], a[200:203], v[132:135]\n"
        "ds_read2_b32 a[44:45], v225 offset0:144 offset1:145\n"
        "s_waitcnt lgkmcnt(8)\n"
        "v_mfma_f32_16x16x32_bf16 v[136:139], a[12:15], a[204:207], v[136:139]\n"
        "ds_read2_b32 a[46:47], v225 offset0:146 offset1:147\n"
        "s_waitcnt lgkmcnt(8)\n"
        "v_mfma_f32_16x16x32_bf16 v[140:143], a[12:15], a[208:211], v[140:143]\n"
        "ds_read_b128 a[236:239], v228 offset:2112\n"
        "v_mfma_f32_16x16x32_bf16 v[128:131], a[16:19], a[192:195], v[128:131]\n"
        "ds_read2_b32 a[48:49], v226 offset0:16 offset1:17\n"
        "s_waitcnt lgkmcnt(8)\n"
        "v_mfma_f32_16x16x32_bf16 v[132:135], a[16:19], a[196:199], v[132:135]\n"
        "ds_read2_b32 a[50:51], v226 offset0:18 offset1:19\n"
        "s_waitcnt lgkmcnt(8)\n"
        "v_mfma_f32_16x16x32_bf16 v[136:139], a[16:19], a[200:203], v[136:139]\n"
        "ds_read_b128 a[240:243], v228 offset:1600\n"
        "s_and_b32 s10, s41, 1\n"
        "s_and_b32 s11, s41, 4\n"
        "v_mfma_f32_16x16x32_bf16 v[140:143], a[16:19], a[204:207], v[140:143]\n"
        "ds_read2_b32 a[52:53], v226 offset0:144 offset1:145\n"
        "s_and_b32 s14, s42, 3\n"
        "s_add_u32 s11, s11, s14\n"
        "v_mfma_f32_16x16x32_bf16 v[132:135], a[20:23], a[192:195], v[132:135]\n"
        "ds_read2_b32 a[54:55], v226 offset0:146 offset1:147\n"
        "s_and_b32 s12, s41, 2\n"
        "s_lshr_b32 s14, s42, 2\n"
        "v_mfma_f32_16x16x32_bf16 v[136:139], a[20:23], a[196:199], v[136:139]\n"
        "ds_read_b128 a[244:247], v228 offset:1088\n"
        "s_lshl2_add_u32 s12, s12, s14\n"
        "s_lshl3_add_u32 s13, s12, s11\n"
        "v_mfma_f32_16x16x32_bf16 v[140:143], a[20:23], a[200:203], v[140:143]\n"
        "ds_read2_b32 a[56:57], v227 offset0:16 offset1:17\n"
        "s_lshl1_add_u32 s13, s13, s10\n"
        "s_lshl_b32 s14, s10, 22\n"
        "v_mfma_f32_16x16x32_bf16 v[136:139], a[24:27], a[192:195], v[136:139]\n"
        "ds_read2_b32 a[58:59], v227 offset0:18 offset1:19\n"
        "s_lshl_b32 s15, s40, 15\n"
        "s_add_u32 s14, s14, s15\n"
        "v_mfma_f32_16x16x32_bf16 v[140:143], a[24:27], a[196:199], v[140:143]\n"
        "ds_read_b128 a[248:251], v228 offset:576\n"
        "s_lshl_b32 s15, s11, 8\n"
        "s_add_u32 s14, s14, s15\n"
        "v_mfma_f32_16x16x32_bf16 v[140:143], a[28:31], a[192:195], v[140:143]\n"
        "ds_read2_b32 a[60:61], v227 offset0:144 offset1:145\n"
        "s_add_u32 s22, s18, s14\n"
        "s_addc_u32 s23, s19, 0\n"
        "v_mfma_f32_16x16x32_bf16 v[96:99], a[32:35], a[224:227], v[96:99]\n"
        "ds_read2_b32 a[62:63], v227 offset0:146 offset1:147\n"
        "s_sub_u32 s22, s22, 4096\n"
        "s_subb_u32 s23, s23, 0\n"
        "v_mfma_f32_16x16x32_bf16 v[100:103], a[32:35], a[228:231], v[100:103]\n"
        "ds_read_b128 a[252:255], v228 offset:64\n"
        "s_lshl_b32 s14, s12, 20\n"
        "s_waitcnt lgkmcnt(8)\n"
        "v_mfma_f32_16x16x32_bf16 v[104:107], a[32:35], a[232:235], v[104:107]\n"
        "s_lshl_b32 s15, s10, 19\n"
        "s_add_u32 s14, s14, s15\n"
        "s_add_u32 s14, s14, s73\n"
        "v_mfma_f32_16x16x32_bf16 v[108:111], a[32:35], a[236:239], v[108:111]\n"
        "s_add_u32 s14, s14, 0x2000000\n"
        "s_add_u32 s20, s38, s14\n"
        "s_addc_u32 s21, s39, 0\n"
        "v_mfma_f32_16x16x32_bf16 v[128:131], a[32:35], a[240:243], v[128:131]\n"
        "s_waitcnt lgkmcnt(5)\n"
        "v_lshrrev_b32 v186, 7, v254\n"
        "v_bfe_u32 v187, v254, 4, 3\n"
        "v_mfma_f32_16x16x32_bf16 v[132:135], a[32:35], a[244:247], v[132:135]\n"
        "s_waitcnt lgkmcnt(4)\n"
        "v_and_b32 v188, 6, v186\n"
        "v_xor_b32 v188, v188, v187\n"
        "v_mfma_f32_16x16x32_bf16 v[100:103], a[36:39], a[224:227], v[100:103]\n"
        "s_waitcnt lgkmcnt(3)\n"
        "v_lshlrev_b32 v188, 4, v188\n"
        "v_lshl_add_u32 v189, v186, 11, v188\n"
        "v_mfma_f32_16x16x32_bf16 v[136:139], a[32:35], a[248:251], v[136:139]\n"
        "s_waitcnt vmcnt(8)\n"
        "v_xor_b32 v186, 4, v186\n"
        "v_lshl_add_u32 v186, v186, 11, v188\n"
        "v_mfma_f32_16x16x32_bf16 v[104:107], a[36:39], a[228:231], v[104:107]\n"
        "s_waitcnt lgkmcnt(1)\n"
        "v_add_u32 v160, 4096, v189\n"
        "v_add_u32 v161, 3200, v189\n"
        "v_mfma_f32_16x16x32_bf16 v[108:111], a[36:39], a[232:235], v[108:111]\n"
        "s_waitcnt lgkmcnt(0)\n"
        "v_add_u32 v162, 18432, v186\n"
        "v_add_u32 v163, 17536, v186\n"
        "v_mfma_f32_16x16x32_bf16 v[140:143], a[32:35], a[252:255], v[140:143]\n"
        "s_barrier\n"
        "s_add_u32 m0, s73, 0x4000\n"
        "v_add_u32 v230, 3712, v228\n"
        "v_mfma_f32_16x16x32_bf16 v[128:131], a[36:39], a[236:239], v[128:131]\n"
        "global_load_lds_dwordx4 v160, s[22:23] offset:0\n"
        "ds_read2_b32 a[0:1], v224 offset0:32 offset1:33\n"
        "v_mfma_f32_16x16x32_bf16 v[132:135], a[36:39], a[240:243], v[132:135]\n"
        "ds_read2_b32 a[2:3], v224 offset0:34 offset1:35\n"
        "v_min_u32 v230, s16, v230\n"
        "v_mfma_f32_16x16x32_bf16 v[136:139], a[36:39], a[244:247], v[136:139]\n"
        "ds_read_b128 a[192:195], v230 offset:0\n"
        "v_mfma_f32_16x16x32_bf16 v[140:143], a[36:39], a[248:251], v[140:143]\n"
        "ds_read2_b32 a[4:5], v224 offset0:160 offset1:161\n"
        "v_mfma_f32_16x16x32_bf16 v[104:107], a[40:43], a[224:227], v[104:107]\n"
        "global_load_lds_dwordx4 v161, s[22:23] offset:1024\n"
        "ds_read2_b32 a[6:7], v224 offset0:162 offset1:163\n"
        "v_mfma_f32_16x16x32_bf16 v[108:111], a[40:43], a[228:231], v[108:111]\n"
        "ds_read_b128 a[196:199], v228 offset:3200\n"
        "v_mfma_f32_16x16x32_bf16 v[128:131], a[40:43], a[232:235], v[128:131]\n"
        "ds_read2_b32 a[8:9], v225 offset0:32 offset1:33\n"
        "v_mfma_f32_16x16x32_bf16 v[132:135], a[40:43], a[236:239], v[132:135]\n"
        "ds_read2_b32 a[10:11], v225 offset0:34 offset1:35\n"
        "v_mfma_f32_16x16x32_bf16 v[136:139], a[40:43], a[240:243], v[136:139]\n"
        "global_load_lds_dwordx4 v162, s[22:23] offset:2048\n"
        "ds_read_b128 a[200:203], v228 offset:2688\n"
        "v_mfma_f32_16x16x32_bf16 v[140:143], a[40:43], a[244:247], v[140:143]\n"
        "ds_read2_b32 a[12:13], v225 offset0:160 offset1:161\n"
        "s_waitcnt lgkmcnt(8)\n"
        "v_mfma_f32_16x16x32_bf16 v[108:111], a[44:47], a[224:227], v[108:111]\n"
        "ds_read2_b32 a[14:15], v225 offset0:162 offset1:163\n"
        "s_waitcnt lgkmcnt(8)\n"
        "v_mfma_f32_16x16x32_bf16 v[128:131], a[44:47], a[228:231], v[128:131]\n"
        "ds_read_b128 a[204:207], v228 offset:2176\n"
        "v_mfma_f32_16x16x32_bf16 v[132:135], a[44:47], a[232:235], v[132:135]\n"
        "global_load_lds_dwordx4 v163, s[22:23] offset:3072\n"
        "s_add_u32 s22, s22, 0x20000\n"
        "s_addc_u32 s23, s23, 0\n"
        "v_mfma_f32_16x16x32_bf16 v[136:139], a[44:47], a[236:239], v[136:139]\n"
        "s_add_u32 m0, s73, 0xc000\n"
        "ds_read2_b32 a[16:17], v226 offset0:32 offset1:33\n"
        "v_mfma_f32_16x16x32_bf16 v[140:143], a[44:47], a[240:243], v[140:143]\n"
        "ds_read2_b32 a[18:19], v226 offset0:34 offset1:35\n"
        "s_waitcnt lgkmcnt(7)\n"
        "v_mfma_f32_16x16x32_bf16 v[128:131], a[48:51], a[224:227], v[128:131]\n"
        "ds_read_b128 a[208:211], v228 offset:1664\n"
        "s_waitcnt lgkmcnt(7)\n"
        "v_mfma_f32_16x16x32_bf16 v[132:135], a[48:51], a[228:231], v[132:135]\n"
        "global_load_lds_dwordx4 v160, s[22:23] offset:0\n"
        "ds_read2_b32 a[20:21], v226 offset0:160 offset1:161\n"
        "v_mfma_f32_16x16x32_bf16 v[136:139], a[48:51], a[232:235], v[136:139]\n"
        "ds_read2_b32 a[22:23], v226 offset0:162 offset1:163\n"
        "v_add_u32 v231, 3776, v228\n"
        "v_mfma_f32_16x16x32_bf16 v[140:143], a[48:51], a[236:239], v[140:143]\n"
        "ds_read_b128 a[212:215], v228 offset:1152\n"
        "v_min_u32 v231, s16, v231\n"
        "v_mfma_f32_16x16x32_bf16 v[132:135], a[52:55], a[224:227], v[132:135]\n"
        "ds_read2_b32 a[24:25], v227 offset0:32 offset1:33\n"
        "s_waitcnt lgkmcnt(7)\n"
        "v_mfma_f32_16x16x32_bf16 v[136:139], a[52:55], a[228:231], v[136:139]\n"
        "global_load_lds_dwordx4 v161, s[22:23] offset:1024\n"
        "ds_read2_b32 a[26:27], v227 offset0:34 offset1:35\n"
        "v_mfma_f32_16x16x32_bf16 v[140:143], a[52:55], a[232:235], v[140:143]\n"
        "ds_read_b128 a[216:219], v228 offset:640\n"
        "v_mfma_f32_16x16x32_bf16 v[136:139], a[56:59], a[224:227], v[136:139]\n"
        "ds_read2_b32 a[28:29], v227 offset0:160 offset1:161\n"
        "s_waitcnt lgkmcnt(8)\n"
        "v_mfma_f32_16x16x32_bf16 v[140:143], a[56:59], a[228:231], v[140:143]\n"
        "ds_read2_b32 a[30:31], v227 offset0:162 offset1:163\n"
        "s_waitcnt lgkmcnt(8)\n"
        "v_mfma_f32_16x16x32_bf16 v[140:143], a[60:63], a[224:227], v[140:143]\n"
        "global_load_lds_dwordx4 v162, s[22:23] offset:2048\n"
        "ds_read_b128 a[220:223], v228 offset:128\n"
        "v_mfma_f32_16x16x32_bf16 v[96:99], a[0:3], a[192:195], v[96:99]\n"
        "ds_read2_b32 a[32:33], v224 offset0:48 offset1:49\n"
        "s_waitcnt lgkmcnt(8)\n"
        "v_mfma_f32_16x16x32_bf16 v[100:103], a[0:3], a[196:199], v[100:103]\n"
        "ds_read2_b32 a[34:35], v224 offset0:50 offset1:51\n"
        "s_waitcnt lgkmcnt(8)\n"
        "v_mfma_f32_16x16x32_bf16 v[104:107], a[0:3], a[200:203], v[104:107]\n"
        "ds_read_b128 a[224:227], v231 offset:0\n"
        "v_mfma_f32_16x16x32_bf16 v[108:111], a[0:3], a[204:207], v[108:111]\n"
        "global_load_lds_dwordx4 v163, s[22:23] offset:3072\n"
        "s_add_u32 s22, s22, 0x20000\n"
        "s_addc_u32 s23, s23, 0\n"
        "v_mfma_f32_16x16x32_bf16 v[128:131], a[0:3], a[208:211], v[128:131]\n"
        "s_add_u32 m0, s73, 0x14000\n"
        "ds_read2_b32 a[36:37], v224 offset0:176 offset1:177\n"
        "v_mfma_f32_16x16x32_bf16 v[132:135], a[0:3], a[212:215], v[132:135]\n"
        "ds_read2_b32 a[38:39], v224 offset0:178 offset1:179\n"
        "s_waitcnt lgkmcnt(7)\n"
        "v_mfma_f32_16x16x32_bf16 v[136:139], a[0:3], a[216:219], v[136:139]\n"
        "ds_read_b128 a[228:231], v228 offset:3264\n"
        "s_waitcnt lgkmcnt(7)\n"
        "v_mfma_f32_16x16x32_bf16 v[100:103], a[4:7], a[192:195], v[100:103]\n"
        "global_load_lds_dwordx4 v160, s[22:23] offset:0\n"
        "s_waitcnt lgkmcnt(6)\n"
        "v_mfma_f32_16x16x32_bf16 v[140:143], a[0:3], a[220:223], v[140:143]\n"
        "ds_read2_b32 a[40:41], v225 offset0:48 offset1:49\n"
        "v_mfma_f32_16x16x32_bf16 v[104:107], a[4:7], a[196:199], v[104:107]\n"
        "ds_read2_b32 a[42:43], v225 offset0:50 offset1:51\n"
        "v_mfma_f32_16x16x32_bf16 v[108:111], a[4:7], a[200:203], v[108:111]\n"
        "ds_read_b128 a[232:235], v228 offset:2752\n"
        "v_mfma_f32_16x16x32_bf16 v[128:131], a[4:7], a[204:207], v[128:131]\n"
        "global_load_lds_dwordx4 v161, s[22:23] offset:1024\n"
        "ds_read2_b32 a[44:45], v225 offset0:176 offset1:177\n"
        "v_mfma_f32_16x16x32_bf16 v[132:135], a[4:7], a[208:211], v[132:135]\n"
        "ds_read2_b32 a[46:47], v225 offset0:178 offset1:179\n"
        "v_mfma_f32_16x16x32_bf16 v[136:139], a[4:7], a[212:215], v[136:139]\n"
        "ds_read_b128 a[236:239], v228 offset:2240\n"
        "v_mfma_f32_16x16x32_bf16 v[140:143], a[4:7], a[216:219], v[140:143]\n"
        "ds_read2_b32 a[48:49], v226 offset0:48 offset1:49\n"
        "v_mfma_f32_16x16x32_bf16 v[104:107], a[8:11], a[192:195], v[104:107]\n"
        "global_load_lds_dwordx4 v162, s[22:23] offset:2048\n"
        "ds_read2_b32 a[50:51], v226 offset0:50 offset1:51\n"
        "v_mfma_f32_16x16x32_bf16 v[108:111], a[8:11], a[196:199], v[108:111]\n"
        "ds_read_b128 a[240:243], v228 offset:1728\n"
        "s_lshl_b32 s14, s44, 5\n"
        "s_add_u32 s14, s14, s43\n"
        "v_mfma_f32_16x16x32_bf16 v[128:131], a[8:11], a[200:203], v[128:131]\n"
        "ds_read2_b32 a[52:53], v226 offset0:176 offset1:177\n"
        "s_lshl2_add_u32 s14, s14, s40\n"
        "s_add_u32 s14, s14, 1024\n"
        "v_mfma_f32_16x16x32_bf16 v[132:135], a[8:11], a[204:207], v[132:135]\n"
        "ds_read2_b32 a[54:55], v226 offset0:178 offset1:179\n"
        "s_lshl_b32 s14, s14, 7\n"
        "v_lshrrev_b32 v182, 1, v254\n"
        "v_mfma_f32_16x16x32_bf16 v[136:139], a[8:11], a[208:211], v[136:139]\n"
        "global_load_lds_dwordx4 v163, s[22:23] offset:3072\n"
        "s_add_u32 s22, s22, 0x20000\n"
        "s_addc_u32 s23, s23, 0\n"
        "v_mfma_f32_16x16x32_bf16 v[140:143], a[8:11], a[212:215], v[140:143]\n"
        "ds_read_b128 a[244:247], v228 offset:1216\n"
        "v_add_u32 v182, s14, v182\n"
        "v_mfma_f32_16x16x32_bf16 v[108:111], a[12:15], a[192:195], v[108:111]\n"
        "ds_read2_b32 a[56:57], v227 offset0:48 offset1:49\n"
        "s_lshl_b32 s14, s45, 5\n"
        "s_add_u32 s14, s14, s43\n"
        "v_mfma_f32_16x16x32_bf16 v[128:131], a[12:15], a[196:199], v[128:131]\n"
        "ds_read2_b32 a[58:59], v227 offset0:50 offset1:51\n"
        "s_lshl2_add_u32 s14, s14, s40\n"
        "s_add_u32 s14, s14, 1024\n"
        "v_mfma_f32_16x16x32_bf16 v[132:135], a[12:15], a[200:203], v[132:135]\n"
        "ds_read_b128 a[248:251], v228 offset:704\n"
        "s_lshl_b32 s14, s14, 7\n"
        "v_lshrrev_b32 v186, 1, v233\n"
        "v_mfma_f32_16x16x32_bf16 v[136:139], a[12:15], a[204:207], v[136:139]\n"
        "ds_read2_b32 a[60:61], v227 offset0:176 offset1:177\n"
        "v_lshlrev_b32 v187, 11, v186\n"
        "v_mfma_f32_16x16x32_bf16 v[140:143], a[12:15], a[208:211], v[140:143]\n"
        "ds_read2_b32 a[62:63], v227 offset0:178 offset1:179\n"
        "v_bfe_u32 v188, v254, 6, 3\n"
        "v_mfma_f32_16x16x32_bf16 v[128:131], a[16:19], a[192:195], v[128:131]\n"
        "ds_read_b128 a[252:255], v228 offset:192\n"
        "v_bfe_u32 v189, v254, 4, 2\n"
        "v_mfma_f32_16x16x32_bf16 v[132:135], a[16:19], a[196:199], v[132:135]\n"
        "s_waitcnt lgkmcnt(7)\n"
        "v_lshlrev_b32 v190, 2, v186\n"
        "v_xor_b32 v190, v190, v188\n"
        "v_mfma_f32_16x16x32_bf16 v[136:139], a[16:19], a[200:203], v[136:139]\n"
        "ds_read2_b32 a[0:1], v224 offset0:64 offset1:65\n"
        "v_bfe_u32 v191, v190, 1, 2\n"
        "v_mfma_f32_16x16x32_bf16 v[140:143], a[16:19], a[204:207], v[140:143]\n"
        "ds_read2_b32 a[2:3], v224 offset0:66 offset1:67\n"
        "v_xor_b32 v191, 0, v191\n"
        "v_mfma_f32_16x16x32_bf16 v[132:135], a[20:23], a[192:195], v[132:135]\n"
        "ds_read2_b32 a[4:5], v224 offset0:192 offset1:193\n"
        "v_lshlrev_b32 v191, 5, v191\n"
        "v_mfma_f32_16x16x32_bf16 v[136:139], a[20:23], a[196:199], v[136:139]\n"
        "ds_read2_b32 a[6:7], v224 offset0:194 offset1:195\n"
        "v_lshl_add_u32 v190, v190, 7, v191\n"
        "v_mfma_f32_16x16x32_bf16 v[140:143], a[20:23], a[200:203], v[140:143]\n"
        "ds_read2_b32 a[8:9], v225 offset0:64 offset1:65\n"
        "v_lshl_add_u32 v190, v189, 3, v190\n"
        "v_mfma_f32_16x16x32_bf16 v[136:139], a[24:27], a[192:195], v[136:139]\n"
        "ds_read2_b32 a[10:11], v225 offset0:66 offset1:67\n"
        "v_add_u32 v230, 3840, v228\n"
        "v_mfma_f32_16x16x32_bf16 v[140:143], a[24:27], a[196:199], v[140:143]\n"
        "ds_read_b128 a[196:199], v228 offset:3328\n"
        "v_min_u32 v230, s16, v230\n"
        "v_mfma_f32_16x16x32_bf16 v[140:143], a[28:31], a[192:195], v[140:143]\n"
        "s_waitcnt vmcnt(12)\n"
        "ds_read_b128 a[192:195], v230 offset:0\n"
        "v_mfma_f32_16x16x32_bf16 v[96:99], a[32:35], a[224:227], v[96:99]\n"
        "s_mov_b64 exec, 0xffff\n"
        "global_store_dwordx2 v182, v[250:251], s[48:49] sc1\n"
        "s_mov_b64 exec, -1\n"
        "v_mfma_f32_16x16x32_bf16 v[100:103], a[32:35], a[228:231], v[100:103]\n"
        "s_waitcnt lgkmcnt(6)\n"
        "v_lshrrev_b32 v182, 1, v254\n"
        "v_add_u32 v182, s14, v182\n"
        "v_mfma_f32_16x16x32_bf16 v[104:107], a[32:35], a[232:235], v[104:107]\n"
        "s_mov_b64 exec, 0xffff\n"
        "global_store_dwordx2 v182, v[250:251], s[48:49] sc1\n"
        "s_mov_b64 exec, -1\n"
        "s_and_b32 s14, s40, 2\n"
        "v_mfma_f32_16x16x32_bf16 v[108:111], a[32:35], a[236:239], v[108:111]\n"
        "s_add_u32 s14, s14, s10\n"
        "s_lshl_b32 s14, s14, 11\n"
        "v_add_u32 v167, s14, v254\n"
        "v_mfma_f32_16x16x32_bf16 v[128:131], a[32:35], a[240:243], v[128:131]\n"
        "s_and_b32 s14, s40, 1\n"
        "v_xor_b32 v164, 0x800, v167\n"
        "s_lshl_b32 s14, s14, 10\n"
        "v_mfma_f32_16x16x32_bf16 v[132:135], a[32:35], a[244:247], v[132:135]\n"
        "s_add_u32 s14, s14, 16384\n"
        "v_add_u32 v187, s14, v187\n"
        "v_add_u32 v170, v190, v187\n"
        "v_mfma_f32_16x16x32_bf16 v[136:139], a[32:35], a[248:251], v[136:139]\n"
        "ds_read_b128 a[200:203], v228 offset:2816\n"
        "v_xor_b32 v171, 32, v170\n"
        "v_mfma_f32_16x16x32_bf16 v[140:143], a[32:35], a[252:255], v[140:143]\n"
        "v_xor_b32 v172, 64, v170\n"
        "v_xor_b32 v173, 96, v170\n"
        "v_mfma_f32_16x16x32_bf16 v[100:103], a[36:39], a[224:227], v[100:103]\n"
        "v_add_u32 v174, 0x10000, v170\n"
        "v_add_u32 v175, 0x10000, v171\n"
        "v_mfma_f32_16x16x32_bf16 v[104:107], a[36:39], a[228:231], v[104:107]\n"
        "v_add_u32 v176, 0x10000, v172\n"
        "v_add_u32 v177, 0x10000, v173\n"
        "v_mfma_f32_16x16x32_bf16 v[108:111], a[36:39], a[232:235], v[108:111]\n"
        "ds_read2_b32 a[12:13], v225 offset0:192 offset1:193\n"
        "v_add_u32 v165, 0x10000, v164\n"
        "v_mfma_f32_16x16x32_bf16 v[128:131], a[36:39], a[236:239], v[128:131]\n"
        "ds_read2_b32 a[14:15], v225 offset0:194 offset1:195\n"
        "v_add_u32 v168, 0x10000, v167\n"
        "v_mfma_f32_16x16x32_bf16 v[132:135], a[36:39], a[240:243], v[132:135]\n"
        "ds_read_b128 a[204:207], v228 offset:2304\n"
        "v_add_u32 v178, 0x20000, v170\n"
        "v_mfma_f32_16x16x32_bf16 v[136:139], a[36:39], a[244:247], v[136:139]\n"
        "ds_read2_b32 a[16:17], v226 offset0:64 offset1:65\n"
        "v_add_u32 v179, 0x20000, v171\n"
        "v_mfma_f32_16x16x32_bf16 v[140:143], a[36:39], a[248:251], v[140:143]\n"
        "ds_read2_b32 a[18:19], v226 offset0:66 offset1:67\n"
        "v_add_u32 v180, 0x20000, v172\n"
        "v_mfma_f32_16x16x32_bf16 v[104:107], a[40:43], a[224:227], v[104:107]\n"
        "s_waitcnt lgkmcnt(5)\n"
        "v_add_u32 v181, 0x20000, v173\n"
        "v_add_u32 v166, 0x20000, v164\n"
        "v_add_u32 v169, 0x20000, v167\n"
        "v_mfma_f32_16x16x32_bf16 v[108:111], a[40:43], a[228:231], v[108:111]\n"
        "ds_read_b128 a[208:211], v228 offset:1792\n"
        "v_mfma_f32_16x16x32_bf16 v[128:131], a[40:43], a[232:235], v[128:131]\n"
        "ds_read2_b32 a[20:21], v226 offset0:192 offset1:193\n"
        "v_mfma_f32_16x16x32_bf16 v[132:135], a[40:43], a[236:239], v[132:135]\n"
        "ds_read2_b32 a[22:23], v226 offset0:194 offset1:195\n"
        "v_add_u32 v231, 3904, v228\n"
        "v_mfma_f32_16x16x32_bf16 v[136:139], a[40:43], a[240:243], v[136:139]\n"
        "ds_read_b128 a[212:215], v228 offset:1280\n"
        "v_min_u32 v231, s16, v231\n"
        "v_mfma_f32_16x16x32_bf16 v[140:143], a[40:43], a[244:247], v[140:143]\n"
        "ds_read2_b32 a[24:25], v227 offset0:64 offset1:65\n"
        "v_mfma_f32_16x16x32_bf16 v[108:111], a[44:47], a[224:227], v[108:111]\n"
        "ds_read2_b32 a[26:27], v227 offset0:66 offset1:67\n"
        "v_mfma_f32_16x16x32_bf16 v[128:131], a[44:47], a[228:231], v[128:131]\n"
        "ds_read_b128 a[216:219], v228 offset:768\n"
        "v_mfma_f32_16x16x32_bf16 v[132:135], a[44:47], a[232:235], v[132:135]\n"
        "ds_read2_b32 a[28:29], v227 offset0:192 offset1:193\n"
        "v_mfma_f32_16x16x32_bf16 v[136:139], a[44:47], a[236:239], v[136:139]\n"
        "ds_read2_b32 a[30:31], v227 offset0:194 offset1:195\n"
        "v_mfma_f32_16x16x32_bf16 v[140:143], a[44:47], a[240:243], v[140:143]\n"
        "ds_read_b128 a[220:223], v228 offset:256\n"
        "v_mfma_f32_16x16x32_bf16 v[128:131], a[48:51], a[224:227], v[128:131]\n"
        "ds_read2_b32 a[32:33], v224 offset0:80 offset1:81\n"
        "v_mfma_f32_16x16x32_bf16 v[132:135], a[48:51], a[228:231], v[132:135]\n"
        "ds_read2_b32 a[34:35], v224 offset0:82 offset1:83\n"
        "v_mfma_f32_16x16x32_bf16 v[136:139], a[48:51], a[232:235], v[136:139]\n"
        "ds_read2_b32 a[36:37], v224 offset0:208 offset1:209\n"
        "s_lshl1_add_u32 s14, s12, s10\n"
        "s_lshl_b32 s14, s14, 6\n"
        "v_mfma_f32_16x16x32_bf16 v[140:143], a[48:51], a[236:239], v[140:143]\n"
        "ds_read2_b32 a[38:39], v224 offset0:210 offset1:211\n"
        "v_mfma_f32_16x16x32_bf16 v[132:135], a[52:55], a[224:227], v[132:135]\n"
        "ds_read2_b32 a[40:41], v225 offset0:80 offset1:81\n"
        "s_waitcnt lgkmcnt(8)\n"
        "v_mfma_f32_16x16x32_bf16 v[136:139], a[52:55], a[228:231], v[136:139]\n"
        "ds_read2_b32 a[42:43], v225 offset0:82 offset1:83\n"
        "s_add_u32 s14, s14, 1024\n"
        "s_lshl_b32 s14, s14, 7\n"
        "v_mfma_f32_16x16x32_bf16 v[140:143], a[52:55], a[232:235], v[140:143]\n"
        "ds_read_b128 a[232:235], v228 offset:2880\n"
        "v_mfma_f32_16x16x32_bf16 v[136:139], a[56:59], a[224:227], v[136:139]\n"
        "ds_read2_b32 a[44:45], v225 offset0:208 offset1:209\n"
        "v_mfma_f32_16x16x32_bf16 v[140:143], a[56:59], a[228:231], v[140:143]\n"
        "ds_read_b128 a[228:231], v228 offset:3392\n"
        "v_mfma_f32_16x16x32_bf16 v[140:143], a[60:63], a[224:227], v[140:143]\n"
        "ds_read_b128 a[224:227], v231 offset:0\n"
        "v_mfma_f32_16x16x32_bf16 v[96:99], a[0:3], a[192:195], v[96:99]\n"
        "ds_read2_b32 a[46:47], v225 offset0:210 offset1:211\n"
        "v_mfma_f32_16x16x32_bf16 v[100:103], a[0:3], a[196:199], v[100:103]\n"
        "ds_read_b128 a[236:239], v228 offset:2368\n"
        "s_waitcnt lgkmcnt(8)\n"
        "v_mfma_f32_16x16x32_bf16 v[104:107], a[0:3], a[200:203], v[104:107]\n"
        "ds_read2_b32 a[48:49], v226 offset0:80 offset1:81\n"
        "v_mfma_f32_16x16x32_bf16 v[108:111], a[0:3], a[204:207], v[108:111]\n"
        "ds_read2_b32 a[50:51], v226 offset0:82 offset1:83\n"
        "v_mfma_f32_16x16x32_bf16 v[128:131], a[0:3], a[208:211], v[128:131]\n"
        "ds_read_b128 a[240:243], v228 offset:1856\n"
        "v_lshlrev_b32 v182, 3, v254\n"
        "v_mfma_f32_16x16x32_bf16 v[132:135], a[0:3], a[212:215], v[132:135]\n"
        "ds_read2_b32 a[52:53], v226 offset0:208 offset1:209\n"
        "v_add_u32 v182, s14, v182\n"
        "v_mfma_f32_16x16x32_bf16 v[136:139], a[0:3], a[216:219], v[136:139]\n"
        "ds_read2_b32 a[54:55], v226 offset0:210 offset1:211\n"
        "s_add_u32 m0, s73, 0x0\n"
        "v_add_u32 v230, 3968, v228\n"
        "v_mfma_f32_16x16x32_bf16 v[140:143], a[0:3], a[220:223], v[140:143]\n"
        "ds_read_b128 a[244:247], v228 offset:1344\n"
        "v_min_u32 v230, s16, v230\n"
        "v_mfma_f32_16x16x32_bf16 v[100:103], a[4:7], a[192:195], v[100:103]\n"
        "ds_read2_b32 a[56:57], v227 offset0:80 offset1:81\n"
        "v_add_u32 v231, 4032, v228\n"
        "v_mfma_f32_16x16x32_bf16 v[104:107], a[4:7], a[196:199], v[104:107]\n"
        "ds_read2_b32 a[58:59], v227 offset0:82 offset1:83\n"
        "s_add_u32 s14, s51, 504\n"
        "v_min_u32 v231, s16, v231\n"
        "v_mfma_f32_16x16x32_bf16 v[108:111], a[4:7], a[200:203], v[108:111]\n"
        "ds_read_b128 a[248:251], v228 offset:832\n"
        "s_and_b32 s15, s40, 1\n"
        "v_lshlrev_b32 v229, 5, v232\n"
        "v_mfma_f32_16x16x32_bf16 v[128:131], a[4:7], a[204:207], v[128:131]\n"
        "ds_read2_b32 a[60:61], v227 offset0:208 offset1:209\n"
        "s_lshl1_add_u32 s15, s11, s15\n"
        "v_lshl_add_u32 v229, v233, 3, v229\n"
        "v_mfma_f32_16x16x32_bf16 v[132:135], a[4:7], a[208:211], v[132:135]\n"
        "ds_read2_b32 a[62:63], v227 offset0:210 offset1:211\n"
        "s_lshl_b32 s15, s15, 6\n"
        "v_sub_u32 v229, s14, v229\n"
        "v_mfma_f32_16x16x32_bf16 v[136:139], a[4:7], a[212:215], v[136:139]\n"
        "ds_read_b128 a[252:255], v228 offset:320\n"
        "v_mfma_f32_16x16x32_bf16 v[140:143], a[4:7], a[216:219], v[140:143]\n"
        "ds_read2_b32 a[0:1], v224 offset0:96 offset1:97\n"
        "v_mfma_f32_16x16x32_bf16 v[104:107], a[8:11], a[192:195], v[104:107]\n"
        "ds_read2_b32 a[2:3], v224 offset0:98 offset1:99\n"
        "s_waitcnt lgkmcnt(8)\n"
        "v_mfma_f32_16x16x32_bf16 v[108:111], a[8:11], a[196:199], v[108:111]\n"
        "ds_read2_b32 a[4:5], v224 offset0:224 offset1:225\n"
        "v_mfma_f32_16x16x32_bf16 v[128:131], a[8:11], a[200:203], v[128:131]\n"
        "ds_read2_b32 a[6:7], v224 offset0:226 offset1:227\n"
        "v_mfma_f32_16x16x32_bf16 v[132:135], a[8:11], a[204:207], v[132:135]\n"
        "ds_read_b128 a[216:219], v228 offset:896\n"
        "s_add_u32 s24, s94, 0x400000\n"
        "s_addc_u32 s25, s95, 0\n"
        "v_mfma_f32_16x16x32_bf16 v[136:139], a[8:11], a[208:211], v[136:139]\n"
        "ds_read_b128 a[220:223], v228 offset:384\n"
        "s_lshl_b32 s14, s41, 5\n"
        "s_add_u32 s14, s14, s42\n"
        "v_mfma_f32_16x16x32_bf16 v[140:143], a[8:11], a[212:215], v[140:143]\n"
        "ds_read2_b32 a[8:9], v225 offset0:96 offset1:97\n"
        "v_mfma_f32_16x16x32_bf16 v[108:111], a[12:15], a[192:195], v[108:111]\n"
        "ds_read2_b32 a[10:11], v225 offset0:98 offset1:99\n"
        "s_add_u32 s14, s14, 512\n"
        "s_lshl_b32 s14, s14, 7\n"
        "v_mfma_f32_16x16x32_bf16 v[128:131], a[12:15], a[196:199], v[128:131]\n"
        "ds_read_b128 a[212:215], v228 offset:1408\n"
        "v_lshlrev_b32 v191, 11, v232\n"
        "v_mfma_f32_16x16x32_bf16 v[132:135], a[12:15], a[200:203], v[132:135]\n"
        "ds_read_b64 v[64:65], v229 offset:3584\n"
        "v_lshl_add_u32 v186, v233, 3, v191\n"
        "v_mfma_f32_16x16x32_bf16 v[136:139], a[12:15], a[204:207], v[136:139]\n"
        "ds_read_b64 v[66:67], v229 offset:3072\n"
        "v_add_u32 v187, 0x8000, v186\n"
        "v_mfma_f32_16x16x32_bf16 v[140:143], a[12:15], a[208:211], v[140:143]\n"
        "ds_read2_b32 a[12:13], v225 offset0:224 offset1:225\n"
        "v_lshrrev_b32 v188, 6, v254\n"
        "v_mfma_f32_16x16x32_bf16 v[128:131], a[16:19], a[192:195], v[128:131]\n"
        "ds_read2_b32 a[14:15], v225 offset0:226 offset1:227\n"
        "v_lshlrev_b32 v189, 6, v188\n"
        "v_mfma_f32_16x16x32_bf16 v[132:135], a[16:19], a[196:199], v[132:135]\n"
        "ds_read_b128 a[208:211], v228 offset:1920\n"
        "v_lshlrev_b32 v188, 12, v188\n"
        "v_mfma_f32_16x16x32_bf16 v[136:139], a[16:19], a[200:203], v[136:139]\n"
        "ds_read_b64 v[68:69], v229 offset:2560\n"
        "v_mfma_f32_16x16x32_bf16 v[140:143], a[16:19], a[204:207], v[140:143]\n"
        "ds_read_b128 a[204:207], v228 offset:2432\n"
        "v_mfma_f32_16x16x32_bf16 v[132:135], a[20:23], a[192:195], v[132:135]\n"
        "ds_read2_b32 a[16:17], v226 offset0:96 offset1:97\n"
        "v_mfma_f32_16x16x32_bf16 v[136:139], a[20:23], a[196:199], v[136:139]\n"
        "ds_read2_b32 a[18:19], v226 offset0:98 offset1:99\n"
        "s_waitcnt lgkmcnt(8)\n"
        "v_mfma_f32_16x16x32_bf16 v[140:143], a[20:23], a[200:203], v[140:143]\n"
        "ds_read_b128 a[200:203], v228 offset:2944\n"
        "v_and_b32 v80, 0xffff0000, v65\n"
        "v_mfma_f32_16x16x32_bf16 v[136:139], a[24:27], a[192:195], v[136:139]\n"
        "ds_read2_b32 a[20:21], v226 offset0:224 offset1:225\n"
        "v_lshlrev_b32 v81, 16, v65\n"
        "v_mfma_f32_16x16x32_bf16 v[140:143], a[24:27], a[196:199], v[140:143]\n"
        "ds_read_b128 a[196:199], v228 offset:3456\n"
        "v_and_b32 v82, 0xffff0000, v64\n"
        "v_mfma_f32_16x16x32_bf16 v[140:143], a[28:31], a[192:195], v[140:143]\n"
        "global_load_dwordx2 v[184:185], v182, s[48:49] sc1\n"
        "ds_read_b128 a[192:195], v230 offset:0\n"
        "v_mfma_f32_16x16x32_bf16 v[96:99], a[32:35], a[224:227], v[96:99]\n"
        "ds_read2_b32 a[22:23], v226 offset0:226 offset1:227\n"
        "v_lshrrev_b32 v230, 1, v233\n"
        "v_mfma_f32_16x16x32_bf16 v[100:103], a[32:35], a[228:231], v[100:103]\n"
        "ds_read2_b32 a[24:25], v227 offset0:96 offset1:97\n"
        "v_lshlrev_b32 v83, 16, v64\n"
        "v_mfma_f32_16x16x32_bf16 v[104:107], a[32:35], a[232:235], v[104:107]\n"
        "ds_read2_b32 a[26:27], v227 offset0:98 offset1:99\n"
        "v_mul_f32 v80, s54, v80\n"
        "v_mfma_f32_16x16x32_bf16 v[108:111], a[32:35], a[236:239], v[108:111]\n"
        "ds_read2_b32 a[28:29], v227 offset0:224 offset1:225\n"
        "v_mul_f32 v81, s54, v81\n"
        "v_mfma_f32_16x16x32_bf16 v[128:131], a[32:35], a[240:243], v[128:131]\n"
        "ds_read2_b32 a[30:31], v227 offset0:226 offset1:227\n"
        "v_mul_f32 v82, s54, v82\n"
        "v_mfma_f32_16x16x32_bf16 v[132:135], a[32:35], a[244:247], v[132:135]\n"
        "ds_read_b64 v[70:71], v229 offset:2048\n"
        "v_mul_f32 v83, s54, v83\n"
        "v_mfma_f32_16x16x32_bf16 v[136:139], a[32:35], a[248:251], v[136:139]\n"
        "ds_read_b64 v[72:73], v229 offset:1536\n"
        "v_cvt_pk_bf16_f32 v80, 0, v80\n"
        "v_mfma_f32_16x16x32_bf16 v[140:143], a[32:35], a[252:255], v[140:143]\n"
        "ds_read2_b32 a[32:33], v224 offset0:112 offset1:113\n"
        "v_cvt_pk_bf16_f32 v81, 0, v81\n"
        "v_mfma_f32_16x16x32_bf16 v[100:103], a[36:39], a[224:227], v[100:103]\n"
        "ds_read2_b32 a[34:35], v224 offset0:114 offset1:115\n"
        "v_cvt_pk_bf16_f32 v82, 0, v82\n"
        "v_mfma_f32_16x16x32_bf16 v[104:107], a[36:39], a[228:231], v[104:107]\n"
        "ds_read_b64 v[74:75], v229 offset:1024\n"
        "v_cvt_pk_bf16_f32 v83, 0, v83\n"
        "v_mfma_f32_16x16x32_bf16 v[108:111], a[36:39], a[232:235], v[108:111]\n"
        "ds_read_b64 v[76:77], v229 offset:512\n"
        "s_waitcnt lgkmcnt(8)\n"
        "v_mfma_f32_16x16x32_bf16 v[128:131], a[36:39], a[236:239], v[128:131]\n"
        "ds_read_b64 v[78:79], v229 offset:0\n"
        "v_and_b32 v229, 1, v233\n"
        "v_mfma_f32_16x16x32_bf16 v[132:135], a[36:39], a[240:243], v[132:135]\n"
        "ds_read_b128 a[252:255], v228 offset:448\n"
        "v_lshlrev_b32 v229, 9, v229\n"
        "v_mfma_f32_16x16x32_bf16 v[136:139], a[36:39], a[244:247], v[136:139]\n"
        "v_lshl_add_u32 v229, v232, 5, v229\n"
        "v_lshl_add_u32 v229, v230, 4, v229\n"
        "v_mfma_f32_16x16x32_bf16 v[140:143], a[36:39], a[248:251], v[140:143]\n"
        "ds_read2_b32 a[36:37], v224 offset0:240 offset1:241\n"
        "v_and_b32 v84, 0xffff0000, v67\n"
        "v_mfma_f32_16x16x32_bf16 v[104:107], a[40:43], a[224:227], v[104:107]\n"
        "ds_read2_b32 a[38:39], v224 offset0:242 offset1:243\n"
        "v_lshlrev_b32 v85, 16, v67\n"
        "v_mfma_f32_16x16x32_bf16 v[108:111], a[40:43], a[228:231], v[108:111]\n"
        "ds_read_b128 a[248:251], v228 offset:960\n"
        "v_and_b32 v86, 0xffff0000, v66\n"
        "v_mfma_f32_16x16x32_bf16 v[128:131], a[40:43], a[232:235], v[128:131]\n"
        "v_lshlrev_b32 v87, 16, v66\n"
        "v_mul_f32 v84, s54, v84\n"
        "v_mfma_f32_16x16x32_bf16 v[132:135], a[40:43], a[236:239], v[132:135]\n"
        "v_mul_f32 v85, s54, v85\n"
        "v_mul_f32 v86, s54, v86\n"
        "v_mfma_f32_16x16x32_bf16 v[136:139], a[40:43], a[240:243], v[136:139]\n"
        "v_mul_f32 v87, s54, v87\n"
        "v_cvt_pk_bf16_f32 v84, 0, v84\n"
        "v_mfma_f32_16x16x32_bf16 v[140:143], a[40:43], a[244:247], v[140:143]\n"
        "ds_read2_b32 a[40:41], v225 offset0:112 offset1:113\n"
        "v_cvt_pk_bf16_f32 v85, 0, v85\n"
        "v_mfma_f32_16x16x32_bf16 v[108:111], a[44:47], a[224:227], v[108:111]\n"
        "ds_read2_b32 a[42:43], v225 offset0:114 offset1:115\n"
        "v_cvt_pk_bf16_f32 v86, 0, v86\n"
        "v_mfma_f32_16x16x32_bf16 v[128:131], a[44:47], a[228:231], v[128:131]\n"
        "ds_read_b128 a[244:247], v228 offset:1472\n"
        "v_cvt_pk_bf16_f32 v87, 0, v87\n"
        "v_mfma_f32_16x16x32_bf16 v[132:135], a[44:47], a[232:235], v[132:135]\n"
        "v_mfma_f32_16x16x32_bf16 v[136:139], a[44:47], a[236:239], v[136:139]\n"
        "v_mfma_f32_16x16x32_bf16 v[140:143], a[44:47], a[240:243], v[140:143]\n"
        "ds_read2_b32 a[44:45], v225 offset0:240 offset1:241\n"
        "v_mfma_f32_16x16x32_bf16 v[128:131], a[48:51], a[224:227], v[128:131]\n"
        "ds_read2_b32 a[46:47], v225 offset0:242 offset1:243\n"
        "v_mfma_f32_16x16x32_bf16 v[132:135], a[48:51], a[228:231], v[132:135]\n"
        "ds_read_b128 a[240:243], v228 offset:1984\n"
        "v_mfma_f32_16x16x32_bf16 v[136:139], a[48:51], a[232:235], v[136:139]\n"
        "v_mfma_f32_16x16x32_bf16 v[140:143], a[48:51], a[236:239], v[140:143]\n"
        "ds_read_b128 a[236:239], v228 offset:2496\n"
        "v_mfma_f32_16x16x32_bf16 v[132:135], a[52:55], a[224:227], v[132:135]\n"
        "ds_read2_b32 a[48:49], v226 offset0:112 offset1:113\n"
        "v_mfma_f32_16x16x32_bf16 v[136:139], a[52:55], a[228:231], v[136:139]\n"
        "ds_read2_b32 a[50:51], v226 offset0:114 offset1:115\n"
        "v_mfma_f32_16x16x32_bf16 v[140:143], a[52:55], a[232:235], v[140:143]\n"
        "ds_read_b128 a[232:235], v228 offset:3008\n"
        "v_mfma_f32_16x16x32_bf16 v[136:139], a[56:59], a[224:227], v[136:139]\n"
        "ds_read2_b32 a[52:53], v226 offset0:240 offset1:241\n"
        "v_mfma_f32_16x16x32_bf16 v[140:143], a[56:59], a[228:231], v[140:143]\n"
        "ds_read_b128 a[228:231], v228 offset:3520\n"
        "v_mfma_f32_16x16x32_bf16 v[140:143], a[60:63], a[224:227], v[140:143]\n"
        "ds_read_b128 a[224:227], v231 offset:0\n"
        "v_mfma_f32_16x16x32_bf16 v[96:99], a[0:3], a[192:195], v[96:99]\n"
        "ds_read2_b32 a[54:55], v226 offset0:242 offset1:243\n"
        "v_mfma_f32_16x16x32_bf16 v[100:103], a[0:3], a[196:199], v[100:103]\n"
        "ds_read2_b32 a[56:57], v227 offset0:112 offset1:113\n"
        "v_mfma_f32_16x16x32_bf16 v[104:107], a[0:3], a[200:203], v[104:107]\n"
        "ds_read2_b32 a[58:59], v227 offset0:114 offset1:115\n"
        "v_mfma_f32_16x16x32_bf16 v[108:111], a[0:3], a[204:207], v[108:111]\n"
        "ds_read2_b32 a[60:61], v227 offset0:240 offset1:241\n"
        "v_mfma_f32_16x16x32_bf16 v[128:131], a[0:3], a[208:211], v[128:131]\n"
        "ds_read2_b32 a[62:63], v227 offset0:242 offset1:243\n"
        "v_mfma_f32_16x16x32_bf16 v[132:135], a[0:3], a[212:215], v[132:135]\n"
        "v_mfma_f32_16x16x32_bf16 v[136:139], a[0:3], a[216:219], v[136:139]\n"
        "v_mfma_f32_16x16x32_bf16 v[140:143], a[0:3], a[220:223], v[140:143]\n"
        "v_mfma_f32_16x16x32_bf16 v[100:103], a[4:7], a[192:195], v[100:103]\n"
        "v_mfma_f32_16x16x32_bf16 v[104:107], a[4:7], a[196:199], v[104:107]\n"
        "v_mfma_f32_16x16x32_bf16 v[108:111], a[4:7], a[200:203], v[108:111]\n"
        "v_mfma_f32_16x16x32_bf16 v[128:131], a[4:7], a[204:207], v[128:131]\n"
        "v_mfma_f32_16x16x32_bf16 v[132:135], a[4:7], a[208:211], v[132:135]\n"
        "s_waitcnt lgkmcnt(0)\n"
        "s_barrier\n"
        "v_mfma_f32_16x16x32_bf16 v[136:139], a[4:7], a[212:215], v[136:139]\n"
        "s_waitcnt vmcnt(0)\n"
        "v_cmp_ne_u64 vcc, v[184:185], v[250:251]\n"
        "v_mfma_f32_16x16x32_bf16 v[140:143], a[4:7], a[216:219], v[140:143]\n"
        "s_cbranch_vccnz LPCX_%=\n"
        "LBCX_%=:\n"
        "global_load_lds_dwordx4 v254, s[20:21] offset:0 sc1\n"
        "v_mfma_f32_16x16x32_bf16 v[104:107], a[8:11], a[192:195], v[104:107]\n"
        "v_lshrrev_b32 v182, 1, v254\n"
        "v_add_u32 v182, s14, v182\n"
        "v_mfma_f32_16x16x32_bf16 v[108:111], a[8:11], a[196:199], v[108:111]\n"
        "s_lshl_b32 s14, s11, 5\n"
        "s_add_u32 s14, s14, 512\n"
        "s_lshl_b32 s14, s14, 7\n"
        "v_mfma_f32_16x16x32_bf16 v[128:131], a[8:11], a[200:203], v[128:131]\n"
        "v_mfma_f32_16x16x32_bf16 v[132:135], a[8:11], a[204:207], v[132:135]\n"
        "global_load_lds_dwordx4 v254, s[20:21] offset:1024 sc1\n"
        "v_mfma_f32_16x16x32_bf16 v[136:139], a[8:11], a[208:211], v[136:139]\n"
        "v_mfma_f32_16x16x32_bf16 v[140:143], a[8:11], a[212:215], v[140:143]\n"
        "v_mfma_f32_16x16x32_bf16 v[108:111], a[12:15], a[192:195], v[108:111]\n"
        "v_mfma_f32_16x16x32_bf16 v[128:131], a[12:15], a[196:199], v[128:131]\n"
        "global_load_lds_dwordx4 v254, s[20:21] offset:2048 sc1\n"
        "v_mfma_f32_16x16x32_bf16 v[132:135], a[12:15], a[200:203], v[132:135]\n"
        "v_mfma_f32_16x16x32_bf16 v[136:139], a[12:15], a[204:207], v[136:139]\n"
        "v_mfma_f32_16x16x32_bf16 v[140:143], a[12:15], a[208:211], v[140:143]\n"
        "v_mfma_f32_16x16x32_bf16 v[128:131], a[16:19], a[192:195], v[128:131]\n"
        "global_load_lds_dwordx4 v254, s[20:21] offset:3072 sc1\n"
        "s_add_u32 m0, s73, 0x8000\n"
        "v_mfma_f32_16x16x32_bf16 v[132:135], a[16:19], a[196:199], v[132:135]\n"
        "s_add_u32 s20, s20, 0x4000\n"
        "s_addc_u32 s21, s21, 0\n"
        "s_waitcnt vmcnt(0)\n"
        "v_mfma_f32_16x16x32_bf16 v[136:139], a[16:19], a[200:203], v[136:139]\n"
        "s_barrier\n"
        "ds_read_b64_tr_b16 a[64:65], v170 offset:0\n"
        "v_mfma_f32_16x16x32_bf16 v[140:143], a[16:19], a[204:207], v[140:143]\n"
        "ds_read_b64_tr_b16 a[66:67], v170 offset:4096\n"
        "v_mfma_f32_16x16x32_bf16 v[132:135], a[20:23], a[192:195], v[132:135]\n"
        "global_load_lds_dwordx4 v254, s[20:21] offset:0 sc1\n"
        "v_mfma_f32_16x16x32_bf16 v[136:139], a[20:23], a[196:199], v[136:139]\n"
        "ds_read_b128 a[80:83], v164 offset:0\n"
        "v_mfma_f32_16x16x32_bf16 v[140:143], a[20:23], a[200:203], v[140:143]\n"
        "ds_read_b128 a[84:87], v164 offset:1024\n"
        "v_mfma_f32_16x16x32_bf16 v[136:139], a[24:27], a[192:195], v[136:139]\n"
        "ds_read_b128 a[88:91], v167 offset:0\n"
        "v_mfma_f32_16x16x32_bf16 v[140:143], a[24:27], a[196:199], v[140:143]\n"
        "global_load_lds_dwordx4 v254, s[20:21] offset:1024 sc1\n"
        "v_mfma_f32_16x16x32_bf16 v[140:143], a[28:31], a[192:195], v[140:143]\n"
        "ds_read_b128 a[92:95], v167 offset:1024\n"
        "v_mfma_f32_16x16x32_bf16 v[96:99], a[32:35], a[224:227], v[96:99]\n"
        "ds_read_b64_tr_b16 a[68:69], v171 offset:0\n"
        "v_mfma_f32_16x16x32_bf16 v[100:103], a[32:35], a[228:231], v[100:103]\n"
        "ds_read_b64_tr_b16 a[70:71], v171 offset:4096\n"
        "v_mfma_f32_16x16x32_bf16 v[104:107], a[32:35], a[232:235], v[104:107]\n"
        "global_load_lds_dwordx4 v254, s[20:21] offset:2048 sc1\n"
        "v_mfma_f32_16x16x32_bf16 v[108:111], a[32:35], a[236:239], v[108:111]\n"
        "ds_read_b64_tr_b16 a[72:73], v172 offset:0\n"
        "v_mfma_f32_16x16x32_bf16 v[128:131], a[32:35], a[240:243], v[128:131]\n"
        "ds_read_b64_tr_b16 a[74:75], v172 offset:4096\n"
        "v_mfma_f32_16x16x32_bf16 v[132:135], a[32:35], a[244:247], v[132:135]\n"
        "ds_read_b64_tr_b16 a[76:77], v173 offset:0\n"
        "v_mfma_f32_16x16x32_bf16 v[136:139], a[32:35], a[248:251], v[136:139]\n"
        "global_load_lds_dwordx4 v254, s[20:21] offset:3072 sc1\n"
        "s_add_u32 m0, s73, 0x10000\n"
        "v_mfma_f32_16x16x32_bf16 v[140:143], a[32:35], a[252:255], v[140:143]\n"
        "s_add_u32 s20, s20, 0x4000\n"
        "s_addc_u32 s21, s21, 0\n"
        "v_mfma_f32_16x16x32_bf16 v[100:103], a[36:39], a[224:227], v[100:103]\n"
        "ds_read_b64_tr_b16 a[78:79], v173 offset:4096\n"
        "v_mfma_f32_16x16x32_bf16 v[104:107], a[36:39], a[228:231], v[104:107]\n"
        "ds_read_b64_tr_b16 a[96:97], v170 offset:8192\n"
        "v_mfma_f32_16x16x32_bf16 v[108:111], a[36:39], a[232:235], v[108:111]\n"
        "global_load_lds_dwordx4 v254, s[20:21] offset:0 sc1\n"
        "v_mfma_f32_16x16x32_bf16 v[128:131], a[36:39], a[236:239], v[128:131]\n"
        "ds_read_b64_tr_b16 a[98:99], v170 offset:12288\n"
        "v_mfma_f32_16x16x32_bf16 v[132:135], a[36:39], a[240:243], v[132:135]\n"
        "ds_read_b128 a[112:115], v164 offset:8192\n"
        "v_cvt_pk_bf16_f32 v96, 0, v96\n"
        "v_mfma_f32_16x16x32_bf16 v[136:139], a[36:39], a[244:247], v[136:139]\n"
        "ds_read_b128 a[116:119], v164 offset:9216\n"
        "v_cvt_pk_bf16_f32 v97, 0, v97\n"
        "v_mfma_f32_16x16x32_bf16 v[140:143], a[36:39], a[248:251], v[140:143]\n"
        "global_load_lds_dwordx4 v254, s[20:21] offset:1024 sc1\n"
        "v_mfma_f32_16x16x32_bf16 v[104:107], a[40:43], a[224:227], v[104:107]\n"
        "ds_read_b128 a[120:123], v167 offset:8192\n"
        "v_cvt_pk_bf16_f32 v98, 0, v98\n"
        "v_mfma_f32_16x16x32_bf16 v[108:111], a[40:43], a[228:231], v[108:111]\n"
        "ds_read_b128 a[124:127], v167 offset:9216\n"
        "v_cvt_pk_bf16_f32 v99, 0, v99\n"
        "v_mfma_f32_16x16x32_bf16 v[128:131], a[40:43], a[232:235], v[128:131]\n"
        "ds_read_b64_tr_b16 a[100:101], v171 offset:8192\n"
        "v_add_f32 v80, v80, v96\n"
        "v_mfma_f32_16x16x32_bf16 v[132:135], a[40:43], a[236:239], v[132:135]\n"
        "global_load_lds_dwordx4 v254, s[20:21] offset:2048 sc1\n"
        "v_mfma_f32_16x16x32_bf16 v[136:139], a[40:43], a[240:243], v[136:139]\n"
        "ds_read_b64_tr_b16 a[102:103], v171 offset:12288\n"
        "v_add_f32 v81, v81, v97\n"
        "v_mfma_f32_16x16x32_bf16 v[140:143], a[40:43], a[244:247], v[140:143]\n"
        "ds_read_b64_tr_b16 a[104:105], v172 offset:8192\n"
        "v_add_f32 v82, v82, v98\n"
        "v_mfma_f32_16x16x32_bf16 v[108:111], a[44:47], a[224:227], v[108:111]\n"
        "ds_read_b64_tr_b16 a[106:107], v172 offset:12288\n"
        "v_add_f32 v83, v83, v99\n"
        "v_mfma_f32_16x16x32_bf16 v[128:131], a[44:47], a[228:231], v[128:131]\n"
        "global_load_lds_dwordx4 v254, s[20:21] offset:3072 sc1\n"
        "s_add_u32 m0, s73, 0x1c000\n"
        "v_mfma_f32_16x16x32_bf16 v[132:135], a[44:47], a[232:235], v[132:135]\n"
        "s_add_u32 s20, s20, 0x4000\n"
        "s_addc_u32 s21, s21, 0\n"
        "v_cvt_pk_bf16_f32 v88, v80, v81\n"
        "v_mfma_f32_16x16x32_bf16 v[136:139], a[44:47], a[236:239], v[136:139]\n"
        "ds_read_b64_tr_b16 a[108:109], v173 offset:8192\n"
        "v_cvt_pk_bf16_f32 v89, v82, v83\n"
        "v_mfma_f32_16x16x32_bf16 v[140:143], a[44:47], a[240:243], v[140:143]\n"
        "ds_read_b64_tr_b16 a[110:111], v173 offset:12288\n"
        "v_cvt_pk_bf16_f32 v100, 0, v100\n"
        "v_mfma_f32_16x16x32_bf16 v[128:131], a[48:51], a[224:227], v[128:131]\n"
        "global_load_lds_dwordx4 v160, s[22:23] offset:0\n"
        "v_mfma_f32_16x16x32_bf16 v[132:135], a[48:51], a[228:231], v[132:135]\n"
        "s_waitcnt lgkmcnt(4)\n"
        "v_cvt_pk_bf16_f32 v101, 0, v101\n"
        "v_cvt_pk_bf16_f32 v102, 0, v102\n"
        "v_mfma_f32_16x16x32_bf16 v[136:139], a[48:51], a[232:235], v[136:139]\n"
        "v_cvt_pk_bf16_f32 v103, 0, v103\n"
        "v_add_f32 v84, v84, v100\n"
        "v_mfma_f32_16x16x32_bf16 v[140:143], a[48:51], a[236:239], v[140:143]\n"
        "v_add_f32 v85, v85, v101\n"
        "v_add_f32 v86, v86, v102\n"
        "v_mfma_f32_16x16x32_bf16 v[132:135], a[52:55], a[224:227], v[132:135]\n"
        "global_load_lds_dwordx4 v161, s[22:23] offset:1024\n"
        "v_mfma_f32_16x16x32_bf16 v[136:139], a[52:55], a[228:231], v[136:139]\n"
        "v_add_f32 v87, v87, v103\n"
        "v_cvt_pk_bf16_f32 v90, v84, v85\n"
        "v_mfma_f32_16x16x32_bf16 v[140:143], a[52:55], a[232:235], v[140:143]\n"
        "v_cvt_pk_bf16_f32 v91, v86, v87\n"
        "v_and_b32 v80, 0xffff0000, v69\n"
        "v_mfma_f32_16x16x32_bf16 v[136:139], a[56:59], a[224:227], v[136:139]\n"
        "s_waitcnt vmcnt(6) lgkmcnt(0)\n"
        "s_barrier\n"
        "v_mfma_f32_16x16x32_bf16 v[140:143], a[56:59], a[228:231], v[140:143]\n"
        "global_load_lds_dwordx4 v162, s[22:23] offset:2048\n"
        "v_mfma_f32_16x16x32_bf16 v[140:143], a[60:63], a[224:227], v[140:143]\n"
        "ds_read_b64_tr_b16 a[128:129], v170 offset:32768\n"
        "v_mfma_f32_16x16x32_bf16 v[0:3], a[64:67], a[80:83], 0\n"
        "ds_read_b64_tr_b16 a[130:131], v170 offset:36864\n"
        "v_mfma_f32_16x16x32_bf16 v[4:7], a[64:67], a[84:87], 0\n"
        "ds_read_b128 a[144:147], v164 offset:32768\n"
        "v_mfma_f32_16x16x32_bf16 v[8:11], a[64:67], a[88:91], 0\n"
        "global_load_lds_dwordx4 v163, s[22:23] offset:3072\n"
        "s_add_u32 m0, s73, 0x18000\n"
        "v_mfma_f32_16x16x32_bf16 v[12:15], a[64:67], a[92:95], 0\n"
        "s_add_u32 s22, s22, 0x20000\n"
        "s_addc_u32 s23, s23, 0\n"
        "v_mfma_f32_16x16x32_bf16 v[16:19], a[68:71], a[80:83], 0\n"
        "ds_read_b128 a[148:151], v164 offset:33792\n"
        "v_mfma_f32_16x16x32_bf16 v[20:23], a[68:71], a[84:87], 0\n"
        "ds_read_b128 a[152:155], v167 offset:32768\n"
        "v_mfma_f32_16x16x32_bf16 v[24:27], a[68:71], a[88:91], 0\n"
        "global_load_lds_dwordx4 v254, s[20:21] offset:0 sc1\n"
        "v_mfma_f32_16x16x32_bf16 v[28:31], a[68:71], a[92:95], 0\n"
        "ds_read_b128 a[156:159], v167 offset:33792\n"
        "v_mfma_f32_16x16x32_bf16 v[32:35], a[72:75], a[80:83], 0\n"
        "ds_read_b64_tr_b16 a[132:133], v171 offset:32768\n"
        "v_mfma_f32_16x16x32_bf16 v[36:39], a[72:75], a[84:87], 0\n"
        "ds_read_b64_tr_b16 a[134:135], v171 offset:36864\n"
        "v_mfma_f32_16x16x32_bf16 v[40:43], a[72:75], a[88:91], 0\n"
        "global_load_lds_dwordx4 v254, s[20:21] offset:1024 sc1\n"
        "v_mfma_f32_16x16x32_bf16 v[44:47], a[72:75], a[92:95], 0\n"
        "ds_read_b64_tr_b16 a[136:137], v172 offset:32768\n"
        "v_mfma_f32_16x16x32_bf16 v[48:51], a[76:79], a[80:83], 0\n"
        "ds_read_b64_tr_b16 a[138:139], v172 offset:36864\n"
        "v_mfma_f32_16x16x32_bf16 v[52:55], a[76:79], a[84:87], 0\n"
        "ds_read_b64_tr_b16 a[140:141], v173 offset:32768\n"
        "v_mfma_f32_16x16x32_bf16 v[56:59], a[76:79], a[88:91], 0\n"
        "global_load_lds_dwordx4 v254, s[20:21] offset:2048 sc1\n"
        "v_mfma_f32_16x16x32_bf16 v[60:63], a[76:79], a[92:95], 0\n"
        "ds_read_b64_tr_b16 a[142:143], v173 offset:36864\n"
        "v_mfma_f32_16x16x32_bf16 v[0:3], a[96:99], a[112:115], v[0:3]\n"
        "ds_read_b64_tr_b16 a[64:65], v170 offset:40960\n"
        "v_mfma_f32_16x16x32_bf16 v[4:7], a[96:99], a[116:119], v[4:7]\n"
        "ds_read_b64_tr_b16 a[66:67], v170 offset:45056\n"
        "v_mfma_f32_16x16x32_bf16 v[8:11], a[96:99], a[120:123], v[8:11]\n"
        "global_load_lds_dwordx4 v254, s[20:21] offset:3072 sc1\n"
        "s_add_u32 m0, s73, 0x24000\n"
        "v_mfma_f32_16x16x32_bf16 v[12:15], a[96:99], a[124:127], v[12:15]\n"
        "s_add_u32 s20, s20, 0x4000\n"
        "s_addc_u32 s21, s21, 0\n"
        "v_mfma_f32_16x16x32_bf16 v[16:19], a[100:103], a[112:115], v[16:19]\n"
        "ds_read_b128 a[80:83], v164 offset:40960\n"
        "v_mfma_f32_16x16x32_bf16 v[20:23], a[100:103], a[116:119], v[20:23]\n"
        "ds_read_b128 a[84:87], v164 offset:41984\n"
        "v_mfma_f32_16x16x32_bf16 v[24:27], a[100:103], a[120:123], v[24:27]\n"
        "global_load_lds_dwordx4 v160, s[22:23] offset:0\n"
        "v_mfma_f32_16x16x32_bf16 v[28:31], a[100:103], a[124:127], v[28:31]\n"
        "ds_read_b128 a[88:91], v167 offset:40960\n"
        "v_lshlrev_b32 v81, 16, v69\n"
        "v_mfma_f32_16x16x32_bf16 v[32:35], a[104:107], a[112:115], v[32:35]\n"
        "ds_read_b64_tr_b16 a[68:69], v171 offset:40960\n"
        "v_and_b32 v82, 0xffff0000, v68\n"
        "v_mfma_f32_16x16x32_bf16 v[36:39], a[104:107], a[116:119], v[36:39]\n"
        "ds_read_b64_tr_b16 a[70:71], v171 offset:45056\n"
        "v_lshlrev_b32 v83, 16, v68\n"
        "v_mfma_f32_16x16x32_bf16 v[40:43], a[104:107], a[120:123], v[40:43]\n"
        "global_load_lds_dwordx4 v161, s[22:23] offset:1024\n"
        "v_mfma_f32_16x16x32_bf16 v[44:47], a[104:107], a[124:127], v[44:47]\n"
        "ds_read_b64_tr_b16 a[72:73], v172 offset:40960\n"
        "v_mul_f32 v80, s54, v80\n"
        "v_mfma_f32_16x16x32_bf16 v[48:51], a[108:111], a[112:115], v[48:51]\n"
        "ds_read_b64_tr_b16 a[74:75], v172 offset:45056\n"
        "v_mul_f32 v81, s54, v81\n"
        "v_mfma_f32_16x16x32_bf16 v[52:55], a[108:111], a[116:119], v[52:55]\n"
        "ds_read_b128 a[92:95], v167 offset:41984\n"
        "v_mul_f32 v82, s54, v82\n"
        "v_mfma_f32_16x16x32_bf16 v[56:59], a[108:111], a[120:123], v[56:59]\n"
        "global_load_lds_dwordx4 v162, s[22:23] offset:2048\n"
        "v_mfma_f32_16x16x32_bf16 v[60:63], a[108:111], a[124:127], v[60:63]\n"
        "ds_read_b64_tr_b16 a[76:77], v173 offset:40960\n"
        "s_waitcnt lgkmcnt(6)\n"
        "v_mfma_f32_16x16x32_bf16 v[0:3], a[128:131], a[144:147], v[0:3]\n"
        "ds_read_b64_tr_b16 a[78:79], v173 offset:45056\n"
        "v_mul_f32 v83, s54, v83\n"
        "v_mfma_f32_16x16x32_bf16 v[4:7], a[128:131], a[148:151], v[4:7]\n"
        "v_permlane16_swap_b32 v88, v90\n"
        "v_mfma_f32_16x16x32_bf16 v[8:11], a[128:131], a[152:155], v[8:11]\n"
        "global_load_lds_dwordx4 v163, s[22:23] offset:3072\n"
        "s_add_u32 m0, s73, 0x20000\n"
        "v_mfma_f32_16x16x32_bf16 v[12:15], a[128:131], a[156:159], v[12:15]\n"
        "s_add_u32 s22, s22, 0x20000\n"
        "s_addc_u32 s23, s23, 0\n"
        "v_cvt_pk_bf16_f32 v80, 0, v80\n"
        "v_mfma_f32_16x16x32_bf16 v[16:19], a[132:135], a[144:147], v[16:19]\n"
        "v_permlane16_swap_b32 v89, v91\n"
        "v_mfma_f32_16x16x32_bf16 v[20:23], a[132:135], a[148:151], v[20:23]\n"
        "v_cvt_pk_bf16_f32 v81, 0, v81\n"
        "v_cvt_pk_bf16_f32 v82, 0, v82\n"
        "v_mfma_f32_16x16x32_bf16 v[24:27], a[132:135], a[152:155], v[24:27]\n"
        "global_load_lds_dwordx4 v254, s[20:21] offset:0 sc1\n"
        "v_mfma_f32_16x16x32_bf16 v[28:31], a[132:135], a[156:159], v[28:31]\n"
        "v_cvt_pk_bf16_f32 v83, 0, v83\n"
        "v_cvt_pk_bf16_f32 v104, 0, v104\n"
        "v_mfma_f32_16x16x32_bf16 v[32:35], a[136:139], a[144:147], v[32:35]\n"
        "s_waitcnt vmcnt(13) lgkmcnt(0)\n"
        "s_barrier\n"
        "v_mfma_f32_16x16x32_bf16 v[36:39], a[136:139], a[148:151], v[36:39]\n"
        "ds_read_b64_tr_b16 a[96:97], v174 offset:0\n"
        "v_mfma_f32_16x16x32_bf16 v[40:43], a[136:139], a[152:155], v[40:43]\n"
        "global_load_lds_dwordx4 v254, s[20:21] offset:1024 sc1\n"
        "v_mfma_f32_16x16x32_bf16 v[44:47], a[136:139], a[156:159], v[44:47]\n"
        "ds_read_b64_tr_b16 a[98:99], v174 offset:4096\n"
        "v_mfma_f32_16x16x32_bf16 v[48:51], a[140:143], a[144:147], v[48:51]\n"
        "ds_read_b128 a[112:115], v165 offset:0\n"
        "v_mfma_f32_16x16x32_bf16 v[52:55], a[140:143], a[148:151], v[52:55]\n"
        "ds_read_b128 a[116:119], v165 offset:1024\n"
        "v_mfma_f32_16x16x32_bf16 v[56:59], a[140:143], a[152:155], v[56:59]\n"
        "global_load_lds_dwordx4 v254, s[20:21] offset:2048 sc1\n"
        "v_mfma_f32_16x16x32_bf16 v[60:63], a[140:143], a[156:159], v[60:63]\n"
        "ds_read_b128 a[120:123], v168 offset:0\n"
        "v_mfma_f32_16x16x32_bf16 v[0:3], a[64:67], a[80:83], v[0:3]\n"
        "ds_read_b128 a[124:127], v168 offset:1024\n"
        "v_mfma_f32_16x16x32_bf16 v[4:7], a[64:67], a[84:87], v[4:7]\n"
        "ds_read_b64_tr_b16 a[100:101], v175 offset:0\n"
        "v_mfma_f32_16x16x32_bf16 v[8:11], a[64:67], a[88:91], v[8:11]\n"
        "global_load_lds_dwordx4 v254, s[20:21] offset:3072 sc1\n"
        "s_add_u32 m0, s73, 0x4000\n"
        "v_mfma_f32_16x16x32_bf16 v[12:15], a[64:67], a[92:95], v[12:15]\n"
        "s_add_u32 s20, s20, 0x4000\n"
        "s_addc_u32 s21, s21, 0\n"
        "v_mfma_f32_16x16x32_bf16 v[16:19], a[68:71], a[80:83], v[16:19]\n"
        "ds_read_b64_tr_b16 a[102:103], v175 offset:4096\n"
        "v_mfma_f32_16x16x32_bf16 v[20:23], a[68:71], a[84:87], v[20:23]\n"
        "ds_read_b64_tr_b16 a[104:105], v176 offset:0\n"
        "v_mfma_f32_16x16x32_bf16 v[24:27], a[68:71], a[88:91], v[24:27]\n"
        "global_load_lds_dwordx4 v160, s[22:23] offset:0\n"
        "v_mfma_f32_16x16x32_bf16 v[28:31], a[68:71], a[92:95], v[28:31]\n"
        "ds_read_b64_tr_b16 a[106:107], v176 offset:4096\n"
        "v_mfma_f32_16x16x32_bf16 v[32:35], a[72:75], a[80:83], v[32:35]\n"
        "ds_read_b64_tr_b16 a[108:109], v177 offset:0\n"
        "v_mfma_f32_16x16x32_bf16 v[36:39], a[72:75], a[84:87], v[36:39]\n"
        "ds_read_b64_tr_b16 a[110:111], v177 offset:4096\n"
        "v_mfma_f32_16x16x32_bf16 v[40:43], a[72:75], a[88:91], v[40:43]\n"
        "global_load_lds_dwordx4 v161, s[22:23] offset:1024\n"
        "v_mfma_f32_16x16x32_bf16 v[44:47], a[72:75], a[92:95], v[44:47]\n"
        "ds_read_b64_tr_b16 a[128:129], v174 offset:8192\n"
        "s_waitcnt lgkmcnt(6)\n"
        "v_mfma_f32_16x16x32_bf16 v[48:51], a[76:79], a[80:83], v[48:51]\n"
        "ds_read_b64_tr_b16 a[130:131], v174 offset:12288\n"
        "v_mfma_f32_16x16x32_bf16 v[52:55], a[76:79], a[84:87], v[52:55]\n"
        "ds_read_b128 a[144:147], v165 offset:8192\n"
        "v_cvt_pk_bf16_f32 v105, 0, v105\n"
        "v_mfma_f32_16x16x32_bf16 v[56:59], a[76:79], a[88:91], v[56:59]\n"
        "global_load_lds_dwordx4 v162, s[22:23] offset:2048\n"
        "v_mfma_f32_16x16x32_bf16 v[60:63], a[76:79], a[92:95], v[60:63]\n"
        "ds_read_b128 a[148:151], v165 offset:9216\n"
        "v_cvt_pk_bf16_f32 v106, 0, v106\n"
        "v_mfma_f32_16x16x32_bf16 v[0:3], a[96:99], a[112:115], v[0:3]\n"
        "ds_read_b128 a[152:155], v168 offset:8192\n"
        "v_cvt_pk_bf16_f32 v107, 0, v107\n"
        "v_mfma_f32_16x16x32_bf16 v[4:7], a[96:99], a[116:119], v[4:7]\n"
        "ds_read_b64_tr_b16 a[132:133], v175 offset:8192\n"
        "v_add_f32 v80, v80, v104\n"
        "v_mfma_f32_16x16x32_bf16 v[8:11], a[96:99], a[120:123], v[8:11]\n"
        "global_load_lds_dwordx4 v163, s[22:23] offset:3072\n"
        "s_add_u32 m0, s73, 0x0\n"
        "s_add_u32 s22, s22, 0x20000\n"
        "s_addc_u32 s23, s23, 0\n"
        "v_mfma_f32_16x16x32_bf16 v[12:15], a[96:99], a[124:127], v[12:15]\n"
        "ds_read_b64_tr_b16 a[134:135], v175 offset:12288\n"
        "s_waitcnt lgkmcnt(6)\n"
        "v_mfma_f32_16x16x32_bf16 v[16:19], a[100:103], a[112:115], v[16:19]\n"
        "ds_read_b64_tr_b16 a[136:137], v176 offset:8192\n"
        "v_add_f32 v81, v81, v105\n"
        "v_mfma_f32_16x16x32_bf16 v[20:23], a[100:103], a[116:119], v[20:23]\n"
        "ds_read_b64_tr_b16 a[138:139], v176 offset:12288\n"
        "v_add_f32 v82, v82, v106\n"
        "global_load_lds_dwordx4 v254, s[20:21] offset:0 sc1\n"
        "v_mfma_f32_16x16x32_bf16 v[24:27], a[100:103], a[120:123], v[24:27]\n"
        "ds_read_b128 a[156:159], v168 offset:9216\n"
        "v_add_f32 v83, v83, v107\n"
        "v_mfma_f32_16x16x32_bf16 v[28:31], a[100:103], a[124:127], v[28:31]\n"
        "ds_read_b64_tr_b16 a[140:141], v177 offset:8192\n"
        "v_cvt_pk_bf16_f32 v92, v80, v81\n"
        "v_mfma_f32_16x16x32_bf16 v[32:35], a[104:107], a[112:115], v[32:35]\n"
        "ds_read_b64_tr_b16 a[142:143], v177 offset:12288\n"
        "v_cvt_pk_bf16_f32 v93, v82, v83\n"
        "v_mfma_f32_16x16x32_bf16 v[36:39], a[104:107], a[116:119], v[36:39]\n"
        "global_load_lds_dwordx4 v254, s[20:21] offset:1024 sc1\n"
        "v_mfma_f32_16x16x32_bf16 v[40:43], a[104:107], a[120:123], v[40:43]\n"
        "s_waitcnt lgkmcnt(6)\n"
        "v_and_b32 v84, 0xffff0000, v71\n"
        "v_lshlrev_b32 v85, 16, v71\n"
        "v_mfma_f32_16x16x32_bf16 v[44:47], a[104:107], a[124:127], v[44:47]\n"
        "v_and_b32 v86, 0xffff0000, v70\n"
        "v_lshlrev_b32 v87, 16, v70\n"
        "v_mfma_f32_16x16x32_bf16 v[48:51], a[108:111], a[112:115], v[48:51]\n"
        "v_mul_f32 v84, s54, v84\n"
        "v_mul_f32 v85, s54, v85\n"
        "v_mfma_f32_16x16x32_bf16 v[52:55], a[108:111], a[116:119], v[52:55]\n"
        "global_load_lds_dwordx4 v254, s[20:21] offset:2048 sc1\n"
        "v_mfma_f32_16x16x32_bf16 v[56:59], a[108:111], a[120:123], v[56:59]\n"
        "s_waitcnt lgkmcnt(2)\n"
        "v_mul_f32 v86, s54, v86\n"
        "v_mul_f32 v87, s54, v87\n"
        "v_mfma_f32_16x16x32_bf16 v[60:63], a[108:111], a[124:127], v[60:63]\n"
        "v_cvt_pk_bf16_f32 v84, 0, v84\n"
        "v_cvt_pk_bf16_f32 v85, 0, v85\n"
        "v_mfma_f32_16x16x32_bf16 v[0:3], a[128:131], a[144:147], v[0:3]\n"
        "s_waitcnt vmcnt(15) lgkmcnt(0)\n"
        "s_barrier\n"
        "v_mfma_f32_16x16x32_bf16 v[4:7], a[128:131], a[148:151], v[4:7]\n"
        "global_load_lds_dwordx4 v254, s[20:21] offset:3072 sc1\n"
        "s_add_u32 m0, s73, 0xc000\n"
        "s_add_u32 s20, s20, 0x4000\n"
        "s_addc_u32 s21, s21, 0\n"
        "v_mfma_f32_16x16x32_bf16 v[8:11], a[128:131], a[152:155], v[8:11]\n"
        "ds_read_b64_tr_b16 a[64:65], v174 offset:32768\n"
        "v_mfma_f32_16x16x32_bf16 v[12:15], a[128:131], a[156:159], v[12:15]\n"
        "ds_read_b64_tr_b16 a[66:67], v174 offset:36864\n"
        "v_mfma_f32_16x16x32_bf16 v[16:19], a[132:135], a[144:147], v[16:19]\n"
        "ds_read_b128 a[80:83], v165 offset:32768\n"
        "v_mfma_f32_16x16x32_bf16 v[20:23], a[132:135], a[148:151], v[20:23]\n"
        "global_load_lds_dwordx4 v160, s[22:23] offset:0\n"
        "ds_read_b128 a[84:87], v165 offset:33792\n"
        "v_mfma_f32_16x16x32_bf16 v[24:27], a[132:135], a[152:155], v[24:27]\n"
        "ds_read_b128 a[88:91], v168 offset:32768\n"
        "v_mfma_f32_16x16x32_bf16 v[28:31], a[132:135], a[156:159], v[28:31]\n"
        "ds_read_b128 a[92:95], v168 offset:33792\n"
        "v_mfma_f32_16x16x32_bf16 v[32:35], a[136:139], a[144:147], v[32:35]\n"
        "ds_read_b64_tr_b16 a[68:69], v175 offset:32768\n"
        "v_mfma_f32_16x16x32_bf16 v[36:39], a[136:139], a[148:151], v[36:39]\n"
        "global_load_lds_dwordx4 v161, s[22:23] offset:1024\n"
        "ds_read_b64_tr_b16 a[70:71], v175 offset:36864\n"
        "v_mfma_f32_16x16x32_bf16 v[40:43], a[136:139], a[152:155], v[40:43]\n"
        "ds_read_b64_tr_b16 a[72:73], v176 offset:32768\n"
        "v_mfma_f32_16x16x32_bf16 v[44:47], a[136:139], a[156:159], v[44:47]\n"
        "ds_read_b64_tr_b16 a[74:75], v176 offset:36864\n"
        "s_waitcnt lgkmcnt(8)\n"
        "v_mfma_f32_16x16x32_bf16 v[48:51], a[140:143], a[144:147], v[48:51]\n"
        "ds_read_b64_tr_b16 a[76:77], v177 offset:32768\n"
        "s_waitcnt lgkmcnt(8)\n"
        "v_mfma_f32_16x16x32_bf16 v[52:55], a[140:143], a[148:151], v[52:55]\n"
        "global_load_lds_dwordx4 v162, s[22:23] offset:2048\n"
        "ds_read_b64_tr_b16 a[78:79], v177 offset:36864\n"
        "v_mfma_f32_16x16x32_bf16 v[56:59], a[140:143], a[152:155], v[56:59]\n"
        "ds_read_b64_tr_b16 a[96:97], v174 offset:40960\n"
        "s_waitcnt lgkmcnt(8)\n"
        "v_mfma_f32_16x16x32_bf16 v[60:63], a[140:143], a[156:159], v[60:63]\n"
        "ds_read_b64_tr_b16 a[98:99], v174 offset:45056\n"
        "s_waitcnt lgkmcnt(8)\n"
        "v_mfma_f32_16x16x32_bf16 v[0:3], a[64:67], a[80:83], v[0:3]\n"
        "ds_read_b128 a[112:115], v165 offset:40960\n"
        "v_cvt_pk_bf16_f32 v86, 0, v86\n"
        "global_load_lds_dwordx4 v163, s[22:23] offset:3072\n"
        "s_add_u32 m0, s73, 0x8000\n"
        "s_add_u32 s22, s22, 0x20000\n"
        "s_addc_u32 s23, s23, 0\n"
        "v_mfma_f32_16x16x32_bf16 v[4:7], a[64:67], a[84:87], v[4:7]\n"
        "ds_read_b128 a[116:119], v165 offset:41984\n"
        "v_cvt_pk_bf16_f32 v87, 0, v87\n"
        "v_mfma_f32_16x16x32_bf16 v[8:11], a[64:67], a[88:91], v[8:11]\n"
        "ds_read_b128 a[120:123], v168 offset:40960\n"
        "v_cvt_pk_bf16_f32 v108, 0, v108\n"
        "v_mfma_f32_16x16x32_bf16 v[12:15], a[64:67], a[92:95], v[12:15]\n"
        "ds_read_b128 a[124:127], v168 offset:41984\n"
        "s_waitcnt lgkmcnt(7)\n"
        "global_load_lds_dwordx4 v254, s[20:21] offset:0 sc1\n"
        "v_mfma_f32_16x16x32_bf16 v[16:19], a[68:71], a[80:83], v[16:19]\n"
        "ds_read_b64_tr_b16 a[100:101], v175 offset:40960\n"
        "v_cvt_pk_bf16_f32 v109, 0, v109\n"
        "v_mfma_f32_16x16x32_bf16 v[20:23], a[68:71], a[84:87], v[20:23]\n"
        "ds_read_b64_tr_b16 a[102:103], v175 offset:45056\n"
        "v_cvt_pk_bf16_f32 v110, 0, v110\n"
        "v_mfma_f32_16x16x32_bf16 v[24:27], a[68:71], a[88:91], v[24:27]\n"
        "ds_read_b64_tr_b16 a[104:105], v176 offset:40960\n"
        "v_cvt_pk_bf16_f32 v111, 0, v111\n"
        "v_mfma_f32_16x16x32_bf16 v[28:31], a[68:71], a[92:95], v[28:31]\n"
        "global_load_lds_dwordx4 v254, s[20:21] offset:1024 sc1\n"
        "v_mfma_f32_16x16x32_bf16 v[32:35], a[72:75], a[80:83], v[32:35]\n"
        "ds_read_b64_tr_b16 a[106:107], v176 offset:45056\n"
        "v_add_f32 v84, v84, v108\n"
        "v_mfma_f32_16x16x32_bf16 v[36:39], a[72:75], a[84:87], v[36:39]\n"
        "ds_read_b64_tr_b16 a[108:109], v177 offset:40960\n"
        "v_add_f32 v85, v85, v109\n"
        "v_mfma_f32_16x16x32_bf16 v[40:43], a[72:75], a[88:91], v[40:43]\n"
        "ds_read_b64_tr_b16 a[110:111], v177 offset:45056\n"
        "v_add_f32 v86, v86, v110\n"
        "v_mfma_f32_16x16x32_bf16 v[44:47], a[72:75], a[92:95], v[44:47]\n"
        "s_waitcnt lgkmcnt(6)\n"
        "global_load_lds_dwordx4 v254, s[20:21] offset:2048 sc1\n"
        "v_mfma_f32_16x16x32_bf16 v[48:51], a[76:79], a[80:83], v[48:51]\n"
        "v_add_f32 v87, v87, v111\n"
        "v_cvt_pk_bf16_f32 v94, v84, v85\n"
        "v_mfma_f32_16x16x32_bf16 v[52:55], a[76:79], a[84:87], v[52:55]\n"
        "v_cvt_pk_bf16_f32 v95, v86, v87\n"
        "v_and_b32 v80, 0xffff0000, v73\n"
        "v_mfma_f32_16x16x32_bf16 v[56:59], a[76:79], a[88:91], v[56:59]\n"
        "s_waitcnt lgkmcnt(3)\n"
        "v_permlane16_swap_b32 v92, v94\n"
        "v_mfma_f32_16x16x32_bf16 v[60:63], a[76:79], a[92:95], v[60:63]\n"
        "global_load_lds_dwordx4 v254, s[20:21] offset:3072 sc1\n"
        "s_add_u32 m0, s73, 0x14000\n"
        "s_add_u32 s20, s20, 0x4000\n"
        "s_addc_u32 s21, s21, 0\n"
        "v_mfma_f32_16x16x32_bf16 v[0:3], a[96:99], a[112:115], v[0:3]\n"
        "v_permlane16_swap_b32 v93, v95\n"
        "v_mfma_f32_16x16x32_bf16 v[4:7], a[96:99], a[116:119], v[4:7]\n"
        "s_waitcnt vmcnt(16) lgkmcnt(0)\n"
        "v_mfma_f32_16x16x32_bf16 v[8:11], a[96:99], a[120:123], v[8:11]\n"
        "s_barrier\n"
        "ds_read_b64_tr_b16 a[128:129], v178 offset:0\n"
        "v_mfma_f32_16x16x32_bf16 v[12:15], a[96:99], a[124:127], v[12:15]\n"
        "global_load_lds_dwordx4 v160, s[22:23] offset:0\n"
        "ds_read_b64_tr_b16 a[130:131], v178 offset:4096\n"
        "v_mfma_f32_16x16x32_bf16 v[16:19], a[100:103], a[112:115], v[16:19]\n"
        "ds_read_b128 a[144:147], v166 offset:0\n"
        "v_mfma_f32_16x16x32_bf16 v[20:23], a[100:103], a[116:119], v[20:23]\n"
        "ds_read_b128 a[148:151], v166 offset:1024\n"
        "v_mfma_f32_16x16x32_bf16 v[24:27], a[100:103], a[120:123], v[24:27]\n"
        "ds_read_b128 a[152:155], v169 offset:0\n"
        "v_mfma_f32_16x16x32_bf16 v[28:31], a[100:103], a[124:127], v[28:31]\n"
        "global_load_lds_dwordx4 v161, s[22:23] offset:1024\n"
        "ds_read_b128 a[156:159], v169 offset:1024\n"
        "v_mfma_f32_16x16x32_bf16 v[32:35], a[104:107], a[112:115], v[32:35]\n"
        "ds_read_b64_tr_b16 a[132:133], v179 offset:0\n"
        "v_mfma_f32_16x16x32_bf16 v[36:39], a[104:107], a[116:119], v[36:39]\n"
        "ds_read_b64_tr_b16 a[134:135], v179 offset:4096\n"
        "v_mfma_f32_16x16x32_bf16 v[40:43], a[104:107], a[120:123], v[40:43]\n"
        "ds_read_b64_tr_b16 a[136:137], v180 offset:0\n"
        "v_mfma_f32_16x16x32_bf16 v[44:47], a[104:107], a[124:127], v[44:47]\n"
        "global_load_lds_dwordx4 v162, s[22:23] offset:2048\n"
        "ds_read_b64_tr_b16 a[138:139], v180 offset:4096\n"
        "v_mfma_f32_16x16x32_bf16 v[48:51], a[108:111], a[112:115], v[48:51]\n"
        "ds_read_b64_tr_b16 a[140:141], v181 offset:0\n"
        "s_waitcnt lgkmcnt(8)\n"
        "v_mfma_f32_16x16x32_bf16 v[52:55], a[108:111], a[116:119], v[52:55]\n"
        "ds_read_b64_tr_b16 a[142:143], v181 offset:4096\n"
        "s_waitcnt lgkmcnt(8)\n"
        "v_mfma_f32_16x16x32_bf16 v[56:59], a[108:111], a[120:123], v[56:59]\n"
        "ds_read_b64_tr_b16 a[64:65], v178 offset:8192\n"
        "s_waitcnt lgkmcnt(8)\n"
        "v_mfma_f32_16x16x32_bf16 v[60:63], a[108:111], a[124:127], v[60:63]\n"
        "global_load_lds_dwordx4 v163, s[22:23] offset:3072\n"
        "s_add_u32 s22, s22, 0x20000\n"
        "s_addc_u32 s23, s23, 0\n"
        "s_add_u32 m0, s73, 0x10000\n"
        "v_mfma_f32_16x16x32_bf16 v[0:3], a[128:131], a[144:147], v[0:3]\n"
        "ds_read_b64_tr_b16 a[66:67], v178 offset:12288\n"
        "s_waitcnt lgkmcnt(7)\n"
        "v_mfma_f32_16x16x32_bf16 v[4:7], a[128:131], a[148:151], v[4:7]\n"
        "ds_read_b128 a[80:83], v166 offset:8192\n"
        "v_lshlrev_b32 v81, 16, v73\n"
        "v_mfma_f32_16x16x32_bf16 v[8:11], a[128:131], a[152:155], v[8:11]\n"
        "ds_read_b128 a[84:87], v166 offset:9216\n"
        "s_waitcnt lgkmcnt(7)\n"
        "global_load_lds_dwordx4 v254, s[20:21] offset:0 sc1\n"
        "v_mfma_f32_16x16x32_bf16 v[12:15], a[128:131], a[156:159], v[12:15]\n"
        "ds_read_b128 a[88:91], v169 offset:8192\n"
        "v_and_b32 v82, 0xffff0000, v72\n"
        "v_mfma_f32_16x16x32_bf16 v[16:19], a[132:135], a[144:147], v[16:19]\n"
        "ds_read_b128 a[92:95], v169 offset:9216\n"
        "v_lshlrev_b32 v83, 16, v72\n"
        "v_mfma_f32_16x16x32_bf16 v[20:23], a[132:135], a[148:151], v[20:23]\n"
        "ds_read_b64_tr_b16 a[68:69], v179 offset:8192\n"
        "v_mul_f32 v80, s54, v80\n"
        "v_mfma_f32_16x16x32_bf16 v[24:27], a[132:135], a[152:155], v[24:27]\n"
        "global_load_lds_dwordx4 v254, s[20:21] offset:1024 sc1\n"
        "v_mfma_f32_16x16x32_bf16 v[28:31], a[132:135], a[156:159], v[28:31]\n"
        "ds_read_b64_tr_b16 a[70:71], v179 offset:12288\n"
        "s_waitcnt lgkmcnt(7)\n"
        "v_mfma_f32_16x16x32_bf16 v[32:35], a[136:139], a[144:147], v[32:35]\n"
        "ds_read_b64_tr_b16 a[72:73], v180 offset:8192\n"
        "v_mul_f32 v81, s54, v81\n"
        "v_mfma_f32_16x16x32_bf16 v[36:39], a[136:139], a[148:151], v[36:39]\n"
        "ds_read_b64_tr_b16 a[74:75], v180 offset:12288\n"
        "v_mul_f32 v82, s54, v82\n"
        "v_mfma_f32_16x16x32_bf16 v[40:43], a[136:139], a[152:155], v[40:43]\n"
        "global_load_lds_dwordx4 v254, s[20:21] offset:2048 sc1\n"
        "v_mfma_f32_16x16x32_bf16 v[44:47], a[136:139], a[156:159], v[44:47]\n"
        "ds_read_b64_tr_b16 a[76:77], v181 offset:8192\n"
        "v_mul_f32 v83, s54, v83\n"
        "v_mfma_f32_16x16x32_bf16 v[48:51], a[140:143], a[144:147], v[48:51]\n"
        "ds_read_b64_tr_b16 a[78:79], v181 offset:12288\n"
        "v_cvt_pk_bf16_f32 v80, 0, v80\n"
        "v_mfma_f32_16x16x32_bf16 v[52:55], a[140:143], a[148:151], v[52:55]\n"
        "s_waitcnt lgkmcnt(5)\n"
        "v_cvt_pk_bf16_f32 v81, 0, v81\n"
        "v_cvt_pk_bf16_f32 v82, 0, v82\n"
        "v_mfma_f32_16x16x32_bf16 v[56:59], a[140:143], a[152:155], v[56:59]\n"
        "global_load_lds_dwordx4 v254, s[20:21] offset:3072 sc1\n"
        "s_add_u32 m0, s73, 0x1c000\n"
        "s_add_u32 s20, s20, 0x4000\n"
        "s_addc_u32 s21, s21, 0\n"
        "v_mfma_f32_16x16x32_bf16 v[60:63], a[140:143], a[156:159], v[60:63]\n"
        "s_waitcnt lgkmcnt(4)\n"
        "v_cvt_pk_bf16_f32 v83, 0, v83\n"
        "v_cvt_pk_bf16_f32 v128, 0, v128\n"
        "v_mfma_f32_16x16x32_bf16 v[0:3], a[64:67], a[80:83], v[0:3]\n"
        "v_cvt_pk_bf16_f32 v129, 0, v129\n"
        "v_cvt_pk_bf16_f32 v130, 0, v130\n"
        "v_mfma_f32_16x16x32_bf16 v[4:7], a[64:67], a[84:87], v[4:7]\n"
        "v_cvt_pk_bf16_f32 v131, 0, v131\n"
        "v_add_f32 v80, v80, v128\n"
        "global_load_lds_dwordx4 v160, s[22:23] offset:0\n"
        "v_mfma_f32_16x16x32_bf16 v[8:11], a[64:67], a[88:91], v[8:11]\n"
        "s_waitcnt lgkmcnt(1)\n"
        "v_add_f32 v81, v81, v129\n"
        "v_add_f32 v82, v82, v130\n"
        "v_mfma_f32_16x16x32_bf16 v[12:15], a[64:67], a[92:95], v[12:15]\n"
        "s_waitcnt vmcnt(17) lgkmcnt(0)\n"
        "s_barrier\n"
        "v_mfma_f32_16x16x32_bf16 v[16:19], a[68:71], a[80:83], v[16:19]\n"
        "ds_read_b64_tr_b16 a[96:97], v170 offset:0\n"
        "v_mfma_f32_16x16x32_bf16 v[20:23], a[68:71], a[84:87], v[20:23]\n"
        "global_load_lds_dwordx4 v161, s[22:23] offset:1024\n"
        "v_mfma_f32_16x16x32_bf16 v[24:27], a[68:71], a[88:91], v[24:27]\n"
        "ds_read_b64_tr_b16 a[98:99], v170 offset:4096\n"
        "v_mfma_f32_16x16x32_bf16 v[28:31], a[68:71], a[92:95], v[28:31]\n"
        "ds_read_b128 a[112:115], v164 offset:0\n"
        "v_mfma_f32_16x16x32_bf16 v[32:35], a[72:75], a[80:83], v[32:35]\n"
        "ds_read_b128 a[116:119], v164 offset:1024\n"
        "v_mfma_f32_16x16x32_bf16 v[36:39], a[72:75], a[84:87], v[36:39]\n"
        "global_load_lds_dwordx4 v162, s[22:23] offset:2048\n"
        "v_mfma_f32_16x16x32_bf16 v[40:43], a[72:75], a[88:91], v[40:43]\n"
        "ds_read_b128 a[120:123], v167 offset:0\n"
        "v_mfma_f32_16x16x32_bf16 v[44:47], a[72:75], a[92:95], v[44:47]\n"
        "ds_read_b128 a[124:127], v167 offset:1024\n"
        "v_mfma_f32_16x16x32_bf16 v[48:51], a[76:79], a[80:83], v[48:51]\n"
        "ds_read_b64_tr_b16 a[100:101], v171 offset:0\n"
        "v_mfma_f32_16x16x32_bf16 v[52:55], a[76:79], a[84:87], v[52:55]\n"
        "global_load_lds_dwordx4 v163, s[22:23] offset:3072\n"
        "s_add_u32 m0, s73, 0x18000\n"
        "s_add_u32 s22, s22, 0x20000\n"
        "s_addc_u32 s23, s23, 0\n"
        "v_mfma_f32_16x16x32_bf16 v[56:59], a[76:79], a[88:91], v[56:59]\n"
        "ds_read_b64_tr_b16 a[102:103], v171 offset:4096\n"
        "s_waitcnt lgkmcnt(5)\n"
        "v_mfma_f32_16x16x32_bf16 v[60:63], a[76:79], a[92:95], v[60:63]\n"
        "ds_read_b64_tr_b16 a[104:105], v172 offset:0\n"
        "s_waitcnt lgkmcnt(5)\n"
        "v_mfma_f32_16x16x32_bf16 v[0:3], a[96:99], a[112:115], v[0:3]\n"
        "ds_read_b64_tr_b16 a[106:107], v172 offset:4096\n"
        "v_mfma_f32_16x16x32_bf16 v[4:7], a[96:99], a[116:119], v[4:7]\n"
        "global_load_lds_dwordx4 v254, s[20:21] offset:0 sc1\n"
        "s_waitcnt lgkmcnt(5)\n"
        "v_mfma_f32_16x16x32_bf16 v[8:11], a[96:99], a[120:123], v[8:11]\n"
        "ds_read_b64_tr_b16 a[108:109], v173 offset:0\n"
        "s_waitcnt lgkmcnt(5)\n"
        "v_mfma_f32_16x16x32_bf16 v[12:15], a[96:99], a[124:127], v[12:15]\n"
        "global_load_lds_dwordx4 v254, s[20:21] offset:1024 sc1\n"
        "ds_read_b64_tr_b16 a[110:111], v173 offset:4096\n"
        "s_waitcnt lgkmcnt(4)\n"
        "v_mfma_f32_16x16x32_bf16 v[16:19], a[100:103], a[112:115], v[16:19]\n"
        "ds_read_b64_tr_b16 a[128:129], v170 offset:8192\n"
        "v_mfma_f32_16x16x32_bf16 v[20:23], a[100:103], a[116:119], v[20:23]\n"
        "ds_read_b64_tr_b16 a[130:131], v170 offset:12288\n"
        "s_waitcnt lgkmcnt(5)\n"
        "v_mfma_f32_16x16x32_bf16 v[24:27], a[100:103], a[120:123], v[24:27]\n"
        "ds_read_b128 a[144:147], v164 offset:8192\n"
        "s_waitcnt lgkmcnt(5)\n"
        "global_load_lds_dwordx4 v254, s[20:21] offset:2048 sc1\n"
        "v_mfma_f32_16x16x32_bf16 v[28:31], a[100:103], a[124:127], v[28:31]\n"
        "ds_read_b128 a[148:151], v164 offset:9216\n"
        "v_add_f32 v83, v83, v131\n"
        "v_mfma_f32_16x16x32_bf16 v[32:35], a[104:107], a[112:115], v[32:35]\n"
        "ds_read_b128 a[152:155], v167 offset:8192\n"
        "v_and_b32 v84, 0xffff0000, v75\n"
        "v_mfma_f32_16x16x32_bf16 v[36:39], a[104:107], a[116:119], v[36:39]\n"
        "ds_read_b128 a[156:159], v167 offset:9216\n"
        "v_lshlrev_b32 v85, 16, v75\n"
        "v_mfma_f32_16x16x32_bf16 v[40:43], a[104:107], a[120:123], v[40:43]\n"
        "global_load_lds_dwordx4 v254, s[20:21] offset:3072 sc1\n"
        "s_add_u32 m0, s73, 0x24000\n"
        "s_add_u32 s20, s20, 0x4000\n"
        "s_addc_u32 s21, s21, 0\n"
        "v_mfma_f32_16x16x32_bf16 v[44:47], a[104:107], a[124:127], v[44:47]\n"
        "ds_read_b64_tr_b16 a[132:133], v171 offset:8192\n"
        "s_waitcnt lgkmcnt(6)\n"
        "v_mfma_f32_16x16x32_bf16 v[48:51], a[108:111], a[112:115], v[48:51]\n"
        "ds_read_b64_tr_b16 a[134:135], v171 offset:12288\n"
        "v_and_b32 v86, 0xffff0000, v74\n"
        "v_mfma_f32_16x16x32_bf16 v[52:55], a[108:111], a[116:119], v[52:55]\n"
        "ds_read_b64_tr_b16 a[136:137], v172 offset:8192\n"
        "v_lshlrev_b32 v87, 16, v74\n"
        "global_load_lds_dwordx4 v160, s[22:23] offset:0\n"
        "v_mfma_f32_16x16x32_bf16 v[56:59], a[108:111], a[120:123], v[56:59]\n"
        "ds_read_b64_tr_b16 a[138:139], v172 offset:12288\n"
        "v_mul_f32 v84, s54, v84\n"
        "v_mfma_f32_16x16x32_bf16 v[60:63], a[108:111], a[124:127], v[60:63]\n"
        "ds_read_b64_tr_b16 a[140:141], v173 offset:8192\n"
        "s_waitcnt lgkmcnt(6)\n"
        "v_mfma_f32_16x16x32_bf16 v[0:3], a[128:131], a[144:147], v[0:3]\n"
        "ds_read_b64_tr_b16 a[142:143], v173 offset:12288\n"
        "v_mul_f32 v85, s54, v85\n"
        "v_mfma_f32_16x16x32_bf16 v[4:7], a[128:131], a[148:151], v[4:7]\n"
        "global_load_lds_dwordx4 v161, s[22:23] offset:1024\n"
        "v_mfma_f32_16x16x32_bf16 v[8:11], a[128:131], a[152:155], v[8:11]\n"
        "s_waitcnt lgkmcnt(6)\n"
        "v_mul_f32 v86, s54, v86\n"
        "v_mul_f32 v87, s54, v87\n"
        "v_mfma_f32_16x16x32_bf16 v[12:15], a[128:131], a[156:159], v[12:15]\n"
        "s_waitcnt lgkmcnt(4)\n"
        "v_cvt_pk_bf16_f32 v84, 0, v84\n"
        "v_cvt_pk_bf16_f32 v85, 0, v85\n"
        "v_mfma_f32_16x16x32_bf16 v[16:19], a[132:135], a[144:147], v[16:19]\n"
        "v_cvt_pk_bf16_f32 v86, 0, v86\n"
        "v_cvt_pk_bf16_f32 v87, 0, v87\n"
        "v_mfma_f32_16x16x32_bf16 v[20:23], a[132:135], a[148:151], v[20:23]\n"
        "global_load_lds_dwordx4 v162, s[22:23] offset:2048\n"
        "v_mfma_f32_16x16x32_bf16 v[24:27], a[132:135], a[152:155], v[24:27]\n"
        "s_waitcnt lgkmcnt(2)\n"
        "v_cvt_pk_bf16_f32 v132, 0, v132\n"
        "v_cvt_pk_bf16_f32 v133, 0, v133\n"
        "v_mfma_f32_16x16x32_bf16 v[28:31], a[132:135], a[156:159], v[28:31]\n"
        "s_waitcnt lgkmcnt(1)\n"
        "v_cvt_pk_bf16_f32 v134, 0, v134\n"
        "v_cvt_pk_bf16_f32 v135, 0, v135\n"
        "v_mfma_f32_16x16x32_bf16 v[32:35], a[136:139], a[144:147], v[32:35]\n"
        "s_waitcnt vmcnt(19) lgkmcnt(0)\n"
        "s_barrier\n"
        "v_mfma_f32_16x16x32_bf16 v[36:39], a[136:139], a[148:151], v[36:39]\n"
        "global_load_lds_dwordx4 v163, s[22:23] offset:3072\n"
        "s_add_u32 m0, s73, 0x20000\n"
        "s_add_u32 s22, s22, 0x20000\n"
        "s_addc_u32 s23, s23, 0\n"
        "v_mfma_f32_16x16x32_bf16 v[40:43], a[136:139], a[152:155], v[40:43]\n"
        "ds_read_b64_tr_b16 a[64:65], v170 offset:32768\n"
        "v_mfma_f32_16x16x32_bf16 v[44:47], a[136:139], a[156:159], v[44:47]\n"
        "ds_read_b64_tr_b16 a[66:67], v170 offset:36864\n"
        "v_mfma_f32_16x16x32_bf16 v[48:51], a[140:143], a[144:147], v[48:51]\n"
        "ds_read_b128 a[80:83], v164 offset:32768\n"
        "v_mfma_f32_16x16x32_bf16 v[52:55], a[140:143], a[148:151], v[52:55]\n"
        "global_load_lds_dwordx4 v254, s[20:21] offset:0 sc1\n"
        "ds_read_b128 a[84:87], v164 offset:33792\n"
        "v_mfma_f32_16x16x32_bf16 v[56:59], a[140:143], a[152:155], v[56:59]\n"
        "ds_read_b128 a[88:91], v167 offset:32768\n"
        "v_mfma_f32_16x16x32_bf16 v[60:63], a[140:143], a[156:159], v[60:63]\n"
        "global_load_lds_dwordx4 v254, s[20:21] offset:1024 sc1\n"
        "ds_read_b128 a[92:95], v167 offset:33792\n"
        "ds_read_b64_tr_b16 a[68:69], v171 offset:32768\n"
        "ds_read_b64_tr_b16 a[70:71], v171 offset:36864\n"
        "ds_read_b64_tr_b16 a[72:73], v172 offset:32768\n"
        "ds_read_b64_tr_b16 a[74:75], v172 offset:36864\n"
        "s_waitcnt lgkmcnt(7)\n"
        "v_mfma_f32_16x16x32_bf16 v[0:3], a[64:67], a[80:83], v[0:3]\n"
        "ds_read_b64_tr_b16 a[76:77], v173 offset:32768\n"
        "s_waitcnt lgkmcnt(7)\n"
        "global_load_lds_dwordx4 v254, s[20:21] offset:2048 sc1\n"
        "v_mfma_f32_16x16x32_bf16 v[4:7], a[64:67], a[84:87], v[4:7]\n"
        "ds_read_b64_tr_b16 a[78:79], v173 offset:36864\n"
        "s_waitcnt lgkmcnt(7)\n"
        "v_mfma_f32_16x16x32_bf16 v[8:11], a[64:67], a[88:91], v[8:11]\n"
        "ds_read_b64_tr_b16 a[96:97], v170 offset:40960\n"
        "ds_read_b64_tr_b16 a[98:99], v170 offset:45056\n"
        "ds_read_b128 a[112:115], v164 offset:40960\n"
        "ds_read_b128 a[116:119], v164 offset:41984\n"
        "s_waitcnt lgkmcnt(10)\n"
        "global_load_lds_dwordx4 v254, s[20:21] offset:3072 sc1\n"
        "s_add_u32 m0, s73, 0x4000\n"
        "v_mfma_f32_16x16x32_bf16 v[12:15], a[64:67], a[92:95], v[12:15]\n"
        "s_add_u32 s20, s20, 0x4000\n"
        "s_addc_u32 s21, s21, 0\n"
        "s_waitcnt lgkmcnt(8)\n"
        "v_mfma_f32_16x16x32_bf16 v[16:19], a[68:71], a[80:83], v[16:19]\n"
        "ds_read_b128 a[120:123], v167 offset:40960\n"
        "v_add_f32 v84, v84, v132\n"
        "v_mfma_f32_16x16x32_bf16 v[20:23], a[68:71], a[84:87], v[20:23]\n"
        "ds_read_b128 a[124:127], v167 offset:41984\n"
        "v_add_f32 v85, v85, v133\n"
        "v_mfma_f32_16x16x32_bf16 v[24:27], a[68:71], a[88:91], v[24:27]\n"
        "global_load_lds_dwordx4 v160, s[22:23] offset:0\n"
        "v_mfma_f32_16x16x32_bf16 v[28:31], a[68:71], a[92:95], v[28:31]\n"
        "ds_read_b64_tr_b16 a[100:101], v171 offset:40960\n"
        "s_waitcnt lgkmcnt(7)\n"
        "v_mfma_f32_16x16x32_bf16 v[32:35], a[72:75], a[80:83], v[32:35]\n"
        "ds_read_b64_tr_b16 a[102:103], v171 offset:45056\n"
        "v_add_f32 v86, v86, v134\n"
        "v_mfma_f32_16x16x32_bf16 v[36:39], a[72:75], a[84:87], v[36:39]\n"
        "ds_read_b64_tr_b16 a[104:105], v172 offset:40960\n"
        "v_add_f32 v87, v87, v135\n"
        "v_mfma_f32_16x16x32_bf16 v[40:43], a[72:75], a[88:91], v[40:43]\n"
        "global_load_lds_dwordx4 v161, s[22:23] offset:1024\n"
        "v_mfma_f32_16x16x32_bf16 v[44:47], a[72:75], a[92:95], v[44:47]\n"
        "ds_read_b64_tr_b16 a[106:107], v172 offset:45056\n"
        "v_cvt_pk_bf16_f32 v136, 0, v136\n"
        "v_mfma_f32_16x16x32_bf16 v[48:51], a[76:79], a[80:83], v[48:51]\n"
        "ds_read_b64_tr_b16 a[108:109], v173 offset:40960\n"
        "v_cvt_pk_bf16_f32 v137, 0, v137\n"
        "v_mfma_f32_16x16x32_bf16 v[52:55], a[76:79], a[84:87], v[52:55]\n"
        "ds_read_b64_tr_b16 a[110:111], v173 offset:45056\n"
        "v_cvt_pk_bf16_f32 v138, 0, v138\n"
        "v_mfma_f32_16x16x32_bf16 v[56:59], a[76:79], a[88:91], v[56:59]\n"
        "global_load_lds_dwordx4 v162, s[22:23] offset:2048\n"
        "v_mfma_f32_16x16x32_bf16 v[60:63], a[76:79], a[92:95], v[60:63]\n"
        "s_waitcnt lgkmcnt(5)\n"
        "v_cvt_pk_bf16_f32 v139, 0, v139\n"
        "v_cvt_pk_bf16_f32 v140, 0, v140\n"
        "v_mfma_f32_16x16x32_bf16 v[0:3], a[96:99], a[112:115], v[0:3]\n"
        "s_waitcnt lgkmcnt(4)\n"
        "v_cvt_pk_bf16_f32 v141, 0, v141\n"
        "v_cvt_pk_bf16_f32 v142, 0, v142\n"
        "v_mfma_f32_16x16x32_bf16 v[4:7], a[96:99], a[116:119], v[4:7]\n"
        "v_cvt_pk_bf16_f32 v143, 0, v143\n"
        "v_mfma_f32_16x16x32_bf16 v[8:11], a[96:99], a[120:123], v[8:11]\n"
        "global_load_lds_dwordx4 v163, s[22:23] offset:3072\n"
        "s_add_u32 m0, s73, 0x0\n"
        "v_mfma_f32_16x16x32_bf16 v[12:15], a[96:99], a[124:127], v[12:15]\n"
        "s_add_u32 s22, s22, 0x20000\n"
        "s_addc_u32 s23, s23, 0\n"
        "s_waitcnt lgkmcnt(2)\n"
        "v_mfma_f32_16x16x32_bf16 v[16:19], a[100:103], a[112:115], v[16:19]\n"
        "v_mfma_f32_16x16x32_bf16 v[20:23], a[100:103], a[116:119], v[20:23]\n"
        "s_waitcnt vmcnt(20) lgkmcnt(0)\n"
        "s_barrier\n"
        "v_mfma_f32_16x16x32_bf16 v[24:27], a[100:103], a[120:123], v[24:27]\n"
        "global_load_lds_dwordx4 v254, s[20:21] offset:0 sc1\n"
        "v_mfma_f32_16x16x32_bf16 v[28:31], a[100:103], a[124:127], v[28:31]\n"
        "ds_read_b64_tr_b16 a[128:129], v174 offset:0\n"
        "v_mfma_f32_16x16x32_bf16 v[32:35], a[104:107], a[112:115], v[32:35]\n"
        "ds_read_b64_tr_b16 a[130:131], v174 offset:4096\n"
        "v_mfma_f32_16x16x32_bf16 v[36:39], a[104:107], a[116:119], v[36:39]\n"
        "ds_read_b128 a[144:147], v165 offset:0\n"
        "v_mfma_f32_16x16x32_bf16 v[40:43], a[104:107], a[120:123], v[40:43]\n"
        "global_load_lds_dwordx4 v254, s[20:21] offset:1024 sc1\n"
        "v_mfma_f32_16x16x32_bf16 v[44:47], a[104:107], a[124:127], v[44:47]\n"
        "ds_read_b128 a[148:151], v165 offset:1024\n"
        "v_mfma_f32_16x16x32_bf16 v[48:51], a[108:111], a[112:115], v[48:51]\n"
        "ds_read_b128 a[152:155], v168 offset:0\n"
        "v_mfma_f32_16x16x32_bf16 v[52:55], a[108:111], a[116:119], v[52:55]\n"
        "ds_read_b128 a[156:159], v168 offset:1024\n"
        "v_mfma_f32_16x16x32_bf16 v[56:59], a[108:111], a[120:123], v[56:59]\n"
        "global_load_lds_dwordx4 v254, s[20:21] offset:2048 sc1\n"
        "v_mfma_f32_16x16x32_bf16 v[60:63], a[108:111], a[124:127], v[60:63]\n"
        "ds_read_b64_tr_b16 a[132:133], v175 offset:0\n"
        "ds_read_b64_tr_b16 a[134:135], v175 offset:4096\n"
        "ds_read_b64_tr_b16 a[136:137], v176 offset:0\n"
        "s_waitcnt lgkmcnt(6)\n"
        "v_mfma_f32_16x16x32_bf16 v[0:3], a[128:131], a[144:147], v[0:3]\n"
        "global_load_lds_dwordx4 v254, s[20:21] offset:3072 sc1\n"
        "s_add_u32 m0, s73, 0xc000\n"
        "s_waitcnt lgkmcnt(5)\n"
        "v_mfma_f32_16x16x32_bf16 v[4:7], a[128:131], a[148:151], v[4:7]\n"
        "s_add_u32 s20, s20, 0x4000\n"
        "s_addc_u32 s21, s21, 0\n"
        "s_waitcnt lgkmcnt(4)\n"
        "v_mfma_f32_16x16x32_bf16 v[8:11], a[128:131], a[152:155], v[8:11]\n"
        "ds_read_b64_tr_b16 a[138:139], v176 offset:4096\n"
        "s_waitcnt lgkmcnt(4)\n"
        "v_mfma_f32_16x16x32_bf16 v[12:15], a[128:131], a[156:159], v[12:15]\n"
        "global_load_lds_dwordx4 v160, s[22:23] offset:0\n"
        "ds_read_b64_tr_b16 a[140:141], v177 offset:0\n"
        "s_waitcnt lgkmcnt(3)\n"
        "v_mfma_f32_16x16x32_bf16 v[16:19], a[132:135], a[144:147], v[16:19]\n"
        "ds_read_b64_tr_b16 a[142:143], v177 offset:4096\n"
        "v_mfma_f32_16x16x32_bf16 v[20:23], a[132:135], a[148:151], v[20:23]\n"
        "ds_read_b64_tr_b16 a[64:65], v174 offset:8192\n"
        "v_mfma_f32_16x16x32_bf16 v[24:27], a[132:135], a[152:155], v[24:27]\n"
        "ds_read_b64_tr_b16 a[66:67], v174 offset:12288\n"
        "v_mfma_f32_16x16x32_bf16 v[28:31], a[132:135], a[156:159], v[28:31]\n"
        "global_load_lds_dwordx4 v161, s[22:23] offset:1024\n"
        "ds_read_b128 a[80:83], v165 offset:8192\n"
        "ds_read_b128 a[84:87], v165 offset:9216\n"
        "s_waitcnt lgkmcnt(6)\n"
        "v_mfma_f32_16x16x32_bf16 v[32:35], a[136:139], a[144:147], v[32:35]\n"
        "ds_read_b128 a[88:91], v168 offset:8192\n"
        "v_mfma_f32_16x16x32_bf16 v[36:39], a[136:139], a[148:151], v[36:39]\n"
        "ds_read_b128 a[92:95], v168 offset:9216\n"
        "v_mfma_f32_16x16x32_bf16 v[40:43], a[136:139], a[152:155], v[40:43]\n"
        "global_load_lds_dwordx4 v162, s[22:23] offset:2048\n"
        "v_mfma_f32_16x16x32_bf16 v[44:47], a[136:139], a[156:159], v[44:47]\n"
        "ds_read_b64_tr_b16 a[68:69], v175 offset:8192\n"
        "s_waitcnt lgkmcnt(7)\n"
        "v_mfma_f32_16x16x32_bf16 v[48:51], a[140:143], a[144:147], v[48:51]\n"
        "ds_read_b64_tr_b16 a[70:71], v175 offset:12288\n"
        "v_mfma_f32_16x16x32_bf16 v[52:55], a[140:143], a[148:151], v[52:55]\n"
        "ds_read_b64_tr_b16 a[72:73], v176 offset:8192\n"
        "s_waitcnt lgkmcnt(7)\n"
        "v_mfma_f32_16x16x32_bf16 v[56:59], a[140:143], a[152:155], v[56:59]\n"
        "global_load_lds_dwordx4 v163, s[22:23] offset:3072\n"
        "s_add_u32 m0, s73, 0x8000\n"
        "v_mfma_f32_16x16x32_bf16 v[60:63], a[140:143], a[156:159], v[60:63]\n"
        "s_add_u32 s22, s22, 0x20000\n"
        "s_addc_u32 s23, s23, 0\n"
        "s_waitcnt lgkmcnt(5)\n"
        "v_mfma_f32_16x16x32_bf16 v[0:3], a[64:67], a[80:83], v[0:3]\n"
        "ds_read_b64_tr_b16 a[74:75], v176 offset:12288\n"
        "s_waitcnt lgkmcnt(5)\n"
        "v_mfma_f32_16x16x32_bf16 v[4:7], a[64:67], a[84:87], v[4:7]\n"
        "ds_read_b64_tr_b16 a[76:77], v177 offset:8192\n"
        "s_waitcnt lgkmcnt(5)\n"
        "v_mfma_f32_16x16x32_bf16 v[8:11], a[64:67], a[88:91], v[8:11]\n"
        "global_load_lds_dwordx4 v254, s[20:21] offset:0 sc1\n"
        "v_mfma_f32_16x16x32_bf16 v[12:15], a[64:67], a[92:95], v[12:15]\n"
        "ds_read_b64_tr_b16 a[78:79], v177 offset:12288\n"
        "s_waitcnt lgkmcnt(4)\n"
        "v_mfma_f32_16x16x32_bf16 v[16:19], a[68:71], a[80:83], v[16:19]\n"
        "v_mfma_f32_16x16x32_bf16 v[20:23], a[68:71], a[84:87], v[20:23]\n"
        "v_mfma_f32_16x16x32_bf16 v[24:27], a[68:71], a[88:91], v[24:27]\n"
        "global_store_dwordx4 v229, v[88:91], s[24:25] offset:0 sc1\n"
        "s_waitcnt lgkmcnt(3)\n"
        "v_mfma_f32_16x16x32_bf16 v[28:31], a[68:71], a[92:95], v[28:31]\n"
        "s_waitcnt lgkmcnt(2)\n"
        "v_cvt_pk_bf16_f32 v88, v80, v81\n"
        "v_cvt_pk_bf16_f32 v89, v82, v83\n"
        "v_cvt_pk_bf16_f32 v90, v84, v85\n"
        "v_cvt_pk_bf16_f32 v91, v86, v87\n"
        "v_and_b32 v80, 0xffff0000, v77\n"
        "v_permlane16_swap_b32 v88, v90\n"
        "v_permlane16_swap_b32 v89, v91\n"
        "global_store_dwordx4 v229, v[92:95], s[24:25] offset:1024 sc1\n"
        "s_waitcnt lgkmcnt(0)\n"
        "v_lshlrev_b32 v81, 16, v77\n"
        "v_and_b32 v82, 0xffff0000, v76\n"
        "v_lshlrev_b32 v83, 16, v76\n"
        "v_mul_f32 v80, s54, v80\n"
        "v_mul_f32 v81, s54, v81\n"
        "v_mul_f32 v82, s54, v82\n"
        "v_mul_f32 v83, s54, v83\n"
        "v_cvt_pk_bf16_f32 v80, 0, v80\n"
        "v_cvt_pk_bf16_f32 v81, 0, v81\n"
        "v_cvt_pk_bf16_f32 v82, 0, v82\n"
        "v_cvt_pk_bf16_f32 v83, 0, v83\n"
        "v_add_f32 v80, v80, v136\n"
        "global_store_dwordx4 v229, v[88:91], s[24:25] offset:2048 sc1\n"
        "s_waitcnt vmcnt(24)\n"
        "v_add_f32 v81, v81, v137\n"
        "v_add_f32 v82, v82, v138\n"
        "v_add_f32 v83, v83, v139\n"
        "v_cvt_pk_bf16_f32 v92, v80, v81\n"
        "v_cvt_pk_bf16_f32 v93, v82, v83\n"
        "v_and_b32 v84, 0xffff0000, v79\n"
        "v_lshlrev_b32 v85, 16, v79\n"
        "v_and_b32 v86, 0xffff0000, v78\n"
        "v_lshlrev_b32 v87, 16, v78\n"
        "v_mul_f32 v84, s54, v84\n"
        "v_mul_f32 v85, s54, v85\n"
        "v_mul_f32 v86, s54, v86\n"
        "v_mul_f32 v87, s54, v87\n"
        "v_cvt_pk_bf16_f32 v84, 0, v84\n"
        "v_cvt_pk_bf16_f32 v85, 0, v85\n"
        "v_cvt_pk_bf16_f32 v86, 0, v86\n"
        "v_cvt_pk_bf16_f32 v87, 0, v87\n"
        "v_add_f32 v84, v84, v140\n"
        "v_add_f32 v85, v85, v141\n"
        "v_add_f32 v86, v86, v142\n"
        "v_add_f32 v87, v87, v143\n"
        "v_cvt_pk_bf16_f32 v94, v84, v85\n"
        "v_cvt_pk_bf16_f32 v95, v86, v87\n"
        "global_load_lds_dwordx4 v254, s[20:21] offset:1024 sc1\n"
        "s_barrier\n"
        "v_permlane16_swap_b32 v92, v94\n"
        "v_permlane16_swap_b32 v93, v95\n"
        "v_mfma_f32_16x16x32_bf16 v[32:35], a[72:75], a[80:83], v[32:35]\n"
        "ds_read_b64_tr_b16 a[96:97], v174 offset:32768\n"
        "v_mfma_f32_16x16x32_bf16 v[36:39], a[72:75], a[84:87], v[36:39]\n"
        "ds_read_b64_tr_b16 a[98:99], v174 offset:36864\n"
        "v_mfma_f32_16x16x32_bf16 v[40:43], a[72:75], a[88:91], v[40:43]\n"
        "global_load_lds_dwordx4 v254, s[20:21] offset:2048 sc1\n"
        "v_mfma_f32_16x16x32_bf16 v[44:47], a[72:75], a[92:95], v[44:47]\n"
        "ds_read_b128 a[112:115], v165 offset:32768\n"
        "v_mfma_f32_16x16x32_bf16 v[48:51], a[76:79], a[80:83], v[48:51]\n"
        "ds_read_b128 a[116:119], v165 offset:33792\n"
        "v_mfma_f32_16x16x32_bf16 v[52:55], a[76:79], a[84:87], v[52:55]\n"
        "ds_read_b128 a[120:123], v168 offset:32768\n"
        "v_mfma_f32_16x16x32_bf16 v[56:59], a[76:79], a[88:91], v[56:59]\n"
        "global_load_lds_dwordx4 v254, s[20:21] offset:3072 sc1\n"
        "s_add_u32 m0, s73, 0x14000\n"
        "v_mfma_f32_16x16x32_bf16 v[60:63], a[76:79], a[92:95], v[60:63]\n"
        "global_load_lds_dwordx4 v160, s[22:23] offset:0\n"
        "s_waitcnt lgkmcnt(2)\n"
        "v_mfma_f32_16x16x32_bf16 v[0:3], a[96:99], a[112:115], v[0:3]\n"
        "s_add_u32 s20, s20, 0x4000\n"
        "s_addc_u32 s21, s21, 0\n"
        "s_waitcnt lgkmcnt(1)\n"
        "v_mfma_f32_16x16x32_bf16 v[4:7], a[96:99], a[116:119], v[4:7]\n"
        "ds_read_b128 a[124:127], v168 offset:33792\n"
        "s_waitcnt lgkmcnt(1)\n"
        "v_mfma_f32_16x16x32_bf16 v[8:11], a[96:99], a[120:123], v[8:11]\n"
        "global_store_dwordx4 v229, v[92:95], s[24:25] offset:3072 sc1\n"
        "global_load_lds_dwordx4 v161, s[22:23] offset:1024\n"
        "ds_read_b64_tr_b16 a[100:101], v175 offset:32768\n"
        "ds_read_b64_tr_b16 a[102:103], v175 offset:36864\n"
        "ds_read_b64_tr_b16 a[104:105], v176 offset:32768\n"
        "s_waitcnt lgkmcnt(3)\n"
        "v_mfma_f32_16x16x32_bf16 v[12:15], a[96:99], a[124:127], v[12:15]\n"
        "global_load_lds_dwordx4 v162, s[22:23] offset:2048\n"
        "ds_read_b64_tr_b16 a[106:107], v176 offset:36864\n"
        "ds_read_b64_tr_b16 a[108:109], v177 offset:32768\n"
        "ds_read_b64_tr_b16 a[110:111], v177 offset:36864\n"
        "global_load_lds_dwordx4 v163, s[22:23] offset:3072\n"
        "s_add_u32 m0, s73, 0x10000\n"
        "s_waitcnt lgkmcnt(4)\n"
        "v_mfma_f32_16x16x32_bf16 v[16:19], a[100:103], a[112:115], v[16:19]\n"
        "ds_read_b64_tr_b16 a[128:129], v174 offset:40960\n"
        "s_add_u32 s22, s22, 0x20000\n"
        "s_addc_u32 s23, s23, 0\n"
        "v_mfma_f32_16x16x32_bf16 v[20:23], a[100:103], a[116:119], v[20:23]\n"
        "ds_read_b64_tr_b16 a[130:131], v174 offset:45056\n"
        "v_mfma_f32_16x16x32_bf16 v[24:27], a[100:103], a[120:123], v[24:27]\n"
        "ds_read_b128 a[144:147], v165 offset:40960\n"
        "s_waitcnt lgkmcnt(6)\n"
        "v_mfma_f32_16x16x32_bf16 v[28:31], a[100:103], a[124:127], v[28:31]\n"
        "global_load_lds_dwordx4 v254, s[20:21] offset:0 sc1\n"
        "s_waitcnt lgkmcnt(5)\n"
        "v_mfma_f32_16x16x32_bf16 v[32:35], a[104:107], a[112:115], v[32:35]\n"
        "ds_read_b128 a[148:151], v165 offset:41984\n"
        "s_waitcnt lgkmcnt(4)\n"
        "v_mfma_f32_16x16x32_bf16 v[36:39], a[104:107], a[116:119], v[36:39]\n"
        "ds_read_b128 a[152:155], v168 offset:40960\n"
        "v_mfma_f32_16x16x32_bf16 v[40:43], a[104:107], a[120:123], v[40:43]\n"
        "ds_read_b128 a[156:159], v168 offset:41984\n"
        "v_mfma_f32_16x16x32_bf16 v[44:47], a[104:107], a[124:127], v[44:47]\n"
        "global_load_lds_dwordx4 v254, s[20:21] offset:1024 sc1\n"
        "ds_read_b64_tr_b16 a[132:133], v175 offset:40960\n"
        "v_mfma_f32_16x16x32_bf16 v[48:51], a[108:111], a[112:115], v[48:51]\n"
        "ds_read_b64_tr_b16 a[134:135], v175 offset:45056\n"
        "v_mfma_f32_16x16x32_bf16 v[52:55], a[108:111], a[116:119], v[52:55]\n"
        "ds_read_b64_tr_b16 a[136:137], v176 offset:40960\n"
        "s_waitcnt lgkmcnt(7)\n"
        "v_mfma_f32_16x16x32_bf16 v[56:59], a[108:111], a[120:123], v[56:59]\n"
        "ds_read_b64_tr_b16 a[138:139], v176 offset:45056\n"
        "s_waitcnt lgkmcnt(7)\n"
        "v_mfma_f32_16x16x32_bf16 v[60:63], a[108:111], a[124:127], v[60:63]\n"
        "ds_read_b64_tr_b16 a[140:141], v177 offset:40960\n"
        "global_load_lds_dwordx4 v254, s[20:21] offset:2048 sc1\n"
        "v_mfma_f32_16x16x32_bf16 v[0:3], a[128:131], a[144:147], v[0:3]\n"
        "ds_read_b64_tr_b16 a[142:143], v177 offset:45056\n"
        "s_waitcnt lgkmcnt(8)\n"
        "v_mfma_f32_16x16x32_bf16 v[4:7], a[128:131], a[148:151], v[4:7]\n"
        "s_waitcnt lgkmcnt(7)\n"
        "v_mfma_f32_16x16x32_bf16 v[8:11], a[128:131], a[152:155], v[8:11]\n"
        "s_waitcnt lgkmcnt(6)\n"
        "v_mfma_f32_16x16x32_bf16 v[12:15], a[128:131], a[156:159], v[12:15]\n"
        "global_load_lds_dwordx4 v254, s[20:21] offset:3072 sc1\n"
        "s_add_u32 m0, s73, 0x1c000\n"
        "s_waitcnt lgkmcnt(4)\n"
        "v_mfma_f32_16x16x32_bf16 v[16:19], a[132:135], a[144:147], v[16:19]\n"
        "s_add_u32 s20, s20, 0x4000\n"
        "s_addc_u32 s21, s21, 0\n"
        "v_mfma_f32_16x16x32_bf16 v[20:23], a[132:135], a[148:151], v[20:23]\n"
        "s_waitcnt lgkmcnt(3)\n"
        "v_mfma_f32_16x16x32_bf16 v[24:27], a[132:135], a[152:155], v[24:27]\n"
        "s_waitcnt lgkmcnt(2)\n"
        "v_mfma_f32_16x16x32_bf16 v[28:31], a[132:135], a[156:159], v[28:31]\n"
        "s_waitcnt lgkmcnt(1)\n"
        "v_mfma_f32_16x16x32_bf16 v[32:35], a[136:139], a[144:147], v[32:35]\n"
        "s_waitcnt vmcnt(28) lgkmcnt(0)\n"
        "s_barrier\n"
        "v_mfma_f32_16x16x32_bf16 v[36:39], a[136:139], a[148:151], v[36:39]\n"
        "global_load_lds_dwordx4 v160, s[22:23] offset:0\n"
        "ds_read_b64_tr_b16 a[64:65], v178 offset:0\n"
        "v_mfma_f32_16x16x32_bf16 v[40:43], a[136:139], a[152:155], v[40:43]\n"
        "ds_read_b64_tr_b16 a[66:67], v178 offset:4096\n"
        "v_mfma_f32_16x16x32_bf16 v[44:47], a[136:139], a[156:159], v[44:47]\n"
        "ds_read_b128 a[80:83], v166 offset:0\n"
        "v_mfma_f32_16x16x32_bf16 v[48:51], a[140:143], a[144:147], v[48:51]\n"
        "ds_read_b128 a[84:87], v166 offset:1024\n"
        "v_mfma_f32_16x16x32_bf16 v[52:55], a[140:143], a[148:151], v[52:55]\n"
        "ds_read_b128 a[88:91], v169 offset:0\n"
        "global_load_lds_dwordx4 v161, s[22:23] offset:1024\n"
        "v_mfma_f32_16x16x32_bf16 v[56:59], a[140:143], a[152:155], v[56:59]\n"
        "ds_read_b128 a[92:95], v169 offset:1024\n"
        "v_mfma_f32_16x16x32_bf16 v[60:63], a[140:143], a[156:159], v[60:63]\n"
        "ds_read_b64_tr_b16 a[68:69], v179 offset:0\n"
        "ds_read_b64_tr_b16 a[70:71], v179 offset:4096\n"
        "global_load_lds_dwordx4 v162, s[22:23] offset:2048\n"
        "ds_read_b64_tr_b16 a[72:73], v180 offset:0\n"
        "ds_read_b64_tr_b16 a[74:75], v180 offset:4096\n"
        "s_waitcnt lgkmcnt(7)\n"
        "v_mfma_f32_16x16x32_bf16 v[0:3], a[64:67], a[80:83], v[0:3]\n"
        "ds_read_b64_tr_b16 a[76:77], v181 offset:0\n"
        "s_waitcnt lgkmcnt(7)\n"
        "v_mfma_f32_16x16x32_bf16 v[4:7], a[64:67], a[84:87], v[4:7]\n"
        "ds_read_b64_tr_b16 a[78:79], v181 offset:4096\n"
        "s_waitcnt lgkmcnt(7)\n"
        "v_mfma_f32_16x16x32_bf16 v[8:11], a[64:67], a[88:91], v[8:11]\n"
        "global_load_lds_dwordx4 v163, s[22:23] offset:3072\n"
        "s_add_u32 m0, s73, 0x18000\n"
        "s_waitcnt lgkmcnt(6)\n"
        "v_mfma_f32_16x16x32_bf16 v[12:15], a[64:67], a[92:95], v[12:15]\n"
        "ds_read_b64_tr_b16 a[96:97], v178 offset:8192\n"
        "ds_read_b64_tr_b16 a[98:99], v178 offset:12288\n"
        "s_waitcnt lgkmcnt(6)\n"
        "v_mfma_f32_16x16x32_bf16 v[16:19], a[68:71], a[80:83], v[16:19]\n"
        "ds_read_b128 a[112:115], v166 offset:8192\n"
        "s_add_u32 s22, s22, 0x20000\n"
        "s_addc_u32 s23, s23, 0\n"
        "v_mfma_f32_16x16x32_bf16 v[20:23], a[68:71], a[84:87], v[20:23]\n"
        "ds_read_b128 a[116:119], v166 offset:9216\n"
        "v_mfma_f32_16x16x32_bf16 v[24:27], a[68:71], a[88:91], v[24:27]\n"
        "global_load_lds_dwordx4 v254, s[20:21] offset:0 sc1\n"
        "ds_read_b128 a[120:123], v169 offset:8192\n"
        "v_mfma_f32_16x16x32_bf16 v[28:31], a[68:71], a[92:95], v[28:31]\n"
        "ds_read_b128 a[124:127], v169 offset:9216\n"
        "s_waitcnt lgkmcnt(8)\n"
        "v_mfma_f32_16x16x32_bf16 v[32:35], a[72:75], a[80:83], v[32:35]\n"
        "ds_read_b64_tr_b16 a[100:101], v179 offset:8192\n"
        "s_waitcnt lgkmcnt(7)\n"
        "v_mfma_f32_16x16x32_bf16 v[36:39], a[72:75], a[84:87], v[36:39]\n"
        "ds_read_b64_tr_b16 a[102:103], v179 offset:12288\n"
        "v_mfma_f32_16x16x32_bf16 v[40:43], a[72:75], a[88:91], v[40:43]\n"
        "global_load_lds_dwordx4 v254, s[20:21] offset:1024 sc1\n"
        "ds_read_b64_tr_b16 a[104:105], v180 offset:8192\n"
        "v_mfma_f32_16x16x32_bf16 v[44:47], a[72:75], a[92:95], v[44:47]\n"
        "ds_read_b64_tr_b16 a[106:107], v180 offset:12288\n"
        "s_waitcnt lgkmcnt(8)\n"
        "v_mfma_f32_16x16x32_bf16 v[48:51], a[76:79], a[80:83], v[48:51]\n"
        "ds_read_b64_tr_b16 a[108:109], v181 offset:8192\n"
        "s_waitcnt lgkmcnt(8)\n"
        "v_mfma_f32_16x16x32_bf16 v[52:55], a[76:79], a[84:87], v[52:55]\n"
        "ds_read_b64_tr_b16 a[110:111], v181 offset:12288\n"
        "s_waitcnt lgkmcnt(8)\n"
        "v_mfma_f32_16x16x32_bf16 v[56:59], a[76:79], a[88:91], v[56:59]\n"
        "global_load_lds_dwordx4 v254, s[20:21] offset:2048 sc1\n"
        "s_waitcnt lgkmcnt(7)\n"
        "v_mfma_f32_16x16x32_bf16 v[60:63], a[76:79], a[92:95], v[60:63]\n"
        "s_waitcnt lgkmcnt(6)\n"
        "v_mfma_f32_16x16x32_bf16 v[0:3], a[96:99], a[112:115], v[0:3]\n"
        "s_waitcnt lgkmcnt(5)\n"
        "v_mfma_f32_16x16x32_bf16 v[4:7], a[96:99], a[116:119], v[4:7]\n"
        "s_waitcnt lgkmcnt(4)\n"
        "v_mfma_f32_16x16x32_bf16 v[8:11], a[96:99], a[120:123], v[8:11]\n"
        "global_load_lds_dwordx4 v254, s[20:21] offset:3072 sc1\n"
        "s_add_u32 m0, s73, 0x24000\n"
        "s_waitcnt lgkmcnt(3)\n"
        "v_mfma_f32_16x16x32_bf16 v[12:15], a[96:99], a[124:127], v[12:15]\n"
        "s_add_u32 s20, s20, 0x4000\n"
        "s_addc_u32 s21, s21, 0\n"
        "s_waitcnt lgkmcnt(2)\n"
        "v_mfma_f32_16x16x32_bf16 v[16:19], a[100:103], a[112:115], v[16:19]\n"
        "v_mfma_f32_16x16x32_bf16 v[20:23], a[100:103], a[116:119], v[20:23]\n"
        "s_waitcnt vmcnt(28) lgkmcnt(0)\n"
        "s_barrier\n"
        "v_mfma_f32_16x16x32_bf16 v[24:27], a[100:103], a[120:123], v[24:27]\n"
        "global_load_lds_dwordx4 v160, s[22:23] offset:0\n"
        "ds_read_b64_tr_b16 a[128:129], v170 offset:0\n"
        "v_mfma_f32_16x16x32_bf16 v[28:31], a[100:103], a[124:127], v[28:31]\n"
        "ds_read_b64_tr_b16 a[130:131], v170 offset:4096\n"
        "v_mfma_f32_16x16x32_bf16 v[32:35], a[104:107], a[112:115], v[32:35]\n"
        "ds_read_b128 a[144:147], v164 offset:0\n"
        "v_mfma_f32_16x16x32_bf16 v[36:39], a[104:107], a[116:119], v[36:39]\n"
        "ds_read_b128 a[148:151], v164 offset:1024\n"
        "v_mfma_f32_16x16x32_bf16 v[40:43], a[104:107], a[120:123], v[40:43]\n"
        "ds_read_b128 a[152:155], v167 offset:0\n"
        "global_load_lds_dwordx4 v161, s[22:23] offset:1024\n"
        "v_mfma_f32_16x16x32_bf16 v[44:47], a[104:107], a[124:127], v[44:47]\n"
        "ds_read_b128 a[156:159], v167 offset:1024\n"
        "v_mfma_f32_16x16x32_bf16 v[48:51], a[108:111], a[112:115], v[48:51]\n"
        "ds_read_b64_tr_b16 a[132:133], v171 offset:0\n"
        "v_mfma_f32_16x16x32_bf16 v[52:55], a[108:111], a[116:119], v[52:55]\n"
        "ds_read_b64_tr_b16 a[134:135], v171 offset:4096\n"
        "v_mfma_f32_16x16x32_bf16 v[56:59], a[108:111], a[120:123], v[56:59]\n"
        "global_load_lds_dwordx4 v162, s[22:23] offset:2048\n"
        "v_mfma_f32_16x16x32_bf16 v[60:63], a[108:111], a[124:127], v[60:63]\n"
        "ds_read_b64_tr_b16 a[136:137], v172 offset:0\n"
        "s_waitcnt lgkmcnt(6)\n"
        "v_mfma_f32_16x16x32_bf16 v[0:3], a[128:131], a[144:147], v[0:3]\n"
        "ds_read_b64_tr_b16 a[138:139], v172 offset:4096\n"
        "s_waitcnt lgkmcnt(6)\n"
        "v_mfma_f32_16x16x32_bf16 v[4:7], a[128:131], a[148:151], v[4:7]\n"
        "ds_read_b64_tr_b16 a[140:141], v173 offset:0\n"
        "s_waitcnt lgkmcnt(6)\n"
        "v_mfma_f32_16x16x32_bf16 v[8:11], a[128:131], a[152:155], v[8:11]\n"
        "ds_read_b64_tr_b16 a[142:143], v173 offset:4096\n"
        "s_waitcnt lgkmcnt(6)\n"
        "v_mfma_f32_16x16x32_bf16 v[12:15], a[128:131], a[156:159], v[12:15]\n"
        "global_load_lds_dwordx4 v163, s[22:23] offset:3072\n"
        "ds_read_b64_tr_b16 a[64:65], v170 offset:8192\n"
        "ds_read_b64_tr_b16 a[66:67], v170 offset:12288\n"
        "s_add_u32 s22, s22, 0x20000\n"
        "s_addc_u32 s23, s23, 0\n"
        "s_waitcnt lgkmcnt(6)\n"
        "v_mfma_f32_16x16x32_bf16 v[16:19], a[132:135], a[144:147], v[16:19]\n"
        "ds_read_b128 a[80:83], v164 offset:8192\n"
        "s_add_u32 m0, s73, 0x20000\n"
        "v_mfma_f32_16x16x32_bf16 v[20:23], a[132:135], a[148:151], v[20:23]\n"
        "ds_read_b128 a[84:87], v164 offset:9216\n"
        "v_mfma_f32_16x16x32_bf16 v[24:27], a[132:135], a[152:155], v[24:27]\n"
        "global_load_lds_dwordx4 v254, s[20:21] offset:0 sc1\n"
        "ds_read_b128 a[88:91], v167 offset:8192\n"
        "v_mfma_f32_16x16x32_bf16 v[28:31], a[132:135], a[156:159], v[28:31]\n"
        "ds_read_b128 a[92:95], v167 offset:9216\n"
        "s_waitcnt lgkmcnt(8)\n"
        "v_mfma_f32_16x16x32_bf16 v[32:35], a[136:139], a[144:147], v[32:35]\n"
        "ds_read_b64_tr_b16 a[68:69], v171 offset:8192\n"
        "s_waitcnt lgkmcnt(8)\n"
        "v_mfma_f32_16x16x32_bf16 v[36:39], a[136:139], a[148:151], v[36:39]\n"
        "ds_read_b64_tr_b16 a[70:71], v171 offset:12288\n"
        "s_waitcnt lgkmcnt(8)\n"
        "v_mfma_f32_16x16x32_bf16 v[40:43], a[136:139], a[152:155], v[40:43]\n"
        "global_load_lds_dwordx4 v254, s[20:21] offset:1024 sc1\n"
        "ds_read_b64_tr_b16 a[72:73], v172 offset:8192\n"
        "v_mfma_f32_16x16x32_bf16 v[44:47], a[136:139], a[156:159], v[44:47]\n"
        "ds_read_b64_tr_b16 a[74:75], v172 offset:12288\n"
        "s_waitcnt lgkmcnt(8)\n"
        "v_mfma_f32_16x16x32_bf16 v[48:51], a[140:143], a[144:147], v[48:51]\n"
        "ds_read_b64_tr_b16 a[76:77], v173 offset:8192\n"
        "s_waitcnt lgkmcnt(8)\n"
        "v_mfma_f32_16x16x32_bf16 v[52:55], a[140:143], a[148:151], v[52:55]\n"
        "ds_read_b64_tr_b16 a[78:79], v173 offset:12288\n"
        "s_waitcnt lgkmcnt(8)\n"
        "v_mfma_f32_16x16x32_bf16 v[56:59], a[140:143], a[152:155], v[56:59]\n"
        "global_load_lds_dwordx4 v254, s[20:21] offset:2048 sc1\n"
        "s_waitcnt lgkmcnt(7)\n"
        "v_mfma_f32_16x16x32_bf16 v[60:63], a[140:143], a[156:159], v[60:63]\n"
        "s_waitcnt lgkmcnt(6)\n"
        "v_mfma_f32_16x16x32_bf16 v[0:3], a[64:67], a[80:83], v[0:3]\n"
        "s_waitcnt lgkmcnt(5)\n"
        "v_mfma_f32_16x16x32_bf16 v[4:7], a[64:67], a[84:87], v[4:7]\n"
        "s_waitcnt lgkmcnt(4)\n"
        "v_mfma_f32_16x16x32_bf16 v[8:11], a[64:67], a[88:91], v[8:11]\n"
        "global_load_lds_dwordx4 v254, s[20:21] offset:3072 sc1\n"
        "s_add_u32 m0, s73, 0x4000\n"
        "s_waitcnt lgkmcnt(3)\n"
        "v_mfma_f32_16x16x32_bf16 v[12:15], a[64:67], a[92:95], v[12:15]\n"
        "s_add_u32 s20, s20, 0x4000\n"
        "s_addc_u32 s21, s21, 0\n"
        "s_waitcnt lgkmcnt(2)\n"
        "v_mfma_f32_16x16x32_bf16 v[16:19], a[68:71], a[80:83], v[16:19]\n"
        "v_mfma_f32_16x16x32_bf16 v[20:23], a[68:71], a[84:87], v[20:23]\n"
        "s_waitcnt vmcnt(23) lgkmcnt(0)\n"
        "s_barrier\n"
        "v_mfma_f32_16x16x32_bf16 v[24:27], a[68:71], a[88:91], v[24:27]\n"
        "global_load_lds_dwordx4 v160, s[22:23] offset:0\n"
        "ds_read_b64_tr_b16 a[96:97], v170 offset:32768\n"
        "v_mfma_f32_16x16x32_bf16 v[28:31], a[68:71], a[92:95], v[28:31]\n"
        "ds_read_b64_tr_b16 a[98:99], v170 offset:36864\n"
        "v_mfma_f32_16x16x32_bf16 v[32:35], a[72:75], a[80:83], v[32:35]\n"
        "ds_read_b128 a[112:115], v164 offset:32768\n"
        "v_mfma_f32_16x16x32_bf16 v[36:39], a[72:75], a[84:87], v[36:39]\n"
        "ds_read_b128 a[116:119], v164 offset:33792\n"
        "v_mfma_f32_16x16x32_bf16 v[40:43], a[72:75], a[88:91], v[40:43]\n"
        "ds_read_b128 a[120:123], v167 offset:32768\n"
        "global_load_lds_dwordx4 v161, s[22:23] offset:1024\n"
        "v_mfma_f32_16x16x32_bf16 v[44:47], a[72:75], a[92:95], v[44:47]\n"
        "ds_read_b128 a[124:127], v167 offset:33792\n"
        "v_mfma_f32_16x16x32_bf16 v[48:51], a[76:79], a[80:83], v[48:51]\n"
        "ds_read_b64_tr_b16 a[100:101], v171 offset:32768\n"
        "v_mfma_f32_16x16x32_bf16 v[52:55], a[76:79], a[84:87], v[52:55]\n"
        "ds_read_b64_tr_b16 a[102:103], v171 offset:36864\n"
        "v_mfma_f32_16x16x32_bf16 v[56:59], a[76:79], a[88:91], v[56:59]\n"
        "global_load_lds_dwordx4 v162, s[22:23] offset:2048\n"
        "v_mfma_f32_16x16x32_bf16 v[60:63], a[76:79], a[92:95], v[60:63]\n"
        "ds_read_b64_tr_b16 a[104:105], v172 offset:32768\n"
        "s_waitcnt lgkmcnt(6)\n"
        "v_mfma_f32_16x16x32_bf16 v[0:3], a[96:99], a[112:115], v[0:3]\n"
        "ds_read_b64_tr_b16 a[106:107], v172 offset:36864\n"
        "s_waitcnt lgkmcnt(6)\n"
        "v_mfma_f32_16x16x32_bf16 v[4:7], a[96:99], a[116:119], v[4:7]\n"
        "ds_read_b64_tr_b16 a[108:109], v173 offset:32768\n"
        "s_waitcnt lgkmcnt(6)\n"
        "v_mfma_f32_16x16x32_bf16 v[8:11], a[96:99], a[120:123], v[8:11]\n"
        "ds_read_b64_tr_b16 a[110:111], v173 offset:36864\n"
        "s_waitcnt lgkmcnt(6)\n"
        "v_mfma_f32_16x16x32_bf16 v[12:15], a[96:99], a[124:127], v[12:15]\n"
        "global_load_lds_dwordx4 v163, s[22:23] offset:3072\n"
        "ds_read_b64_tr_b16 a[128:129], v170 offset:40960\n"
        "ds_read_b64_tr_b16 a[130:131], v170 offset:45056\n"
        "s_add_u32 s22, s22, 0x20000\n"
        "s_addc_u32 s23, s23, 0\n"
        "s_waitcnt lgkmcnt(6)\n"
        "v_mfma_f32_16x16x32_bf16 v[16:19], a[100:103], a[112:115], v[16:19]\n"
        "ds_read_b128 a[144:147], v164 offset:40960\n"
        "s_add_u32 m0, s73, 0x0\n"
        "v_mfma_f32_16x16x32_bf16 v[20:23], a[100:103], a[116:119], v[20:23]\n"
        "ds_read_b128 a[148:151], v164 offset:41984\n"
        "v_mfma_f32_16x16x32_bf16 v[24:27], a[100:103], a[120:123], v[24:27]\n"
        "global_load_lds_dwordx4 v254, s[20:21] offset:0 sc1\n"
        "ds_read_b128 a[152:155], v167 offset:40960\n"
        "v_mfma_f32_16x16x32_bf16 v[28:31], a[100:103], a[124:127], v[28:31]\n"
        "ds_read_b128 a[156:159], v167 offset:41984\n"
        "s_waitcnt lgkmcnt(8)\n"
        "v_mfma_f32_16x16x32_bf16 v[32:35], a[104:107], a[112:115], v[32:35]\n"
        "ds_read_b64_tr_b16 a[132:133], v171 offset:40960\n"
        "s_waitcnt lgkmcnt(8)\n"
        "v_mfma_f32_16x16x32_bf16 v[36:39], a[104:107], a[116:119], v[36:39]\n"
        "ds_read_b64_tr_b16 a[134:135], v171 offset:45056\n"
        "s_waitcnt lgkmcnt(8)\n"
        "v_mfma_f32_16x16x32_bf16 v[40:43], a[104:107], a[120:123], v[40:43]\n"
        "global_load_lds_dwordx4 v254, s[20:21] offset:1024 sc1\n"
        "ds_read_b64_tr_b16 a[136:137], v172 offset:40960\n"
        "v_mfma_f32_16x16x32_bf16 v[44:47], a[104:107], a[124:127], v[44:47]\n"
        "ds_read_b64_tr_b16 a[138:139], v172 offset:45056\n"
        "s_waitcnt lgkmcnt(8)\n"
        "v_mfma_f32_16x16x32_bf16 v[48:51], a[108:111], a[112:115], v[48:51]\n"
        "ds_read_b64_tr_b16 a[140:141], v173 offset:40960\n"
        "s_waitcnt lgkmcnt(8)\n"
        "v_mfma_f32_16x16x32_bf16 v[52:55], a[108:111], a[116:119], v[52:55]\n"
        "ds_read_b64_tr_b16 a[142:143], v173 offset:45056\n"
        "s_waitcnt lgkmcnt(8)\n"
        "v_mfma_f32_16x16x32_bf16 v[56:59], a[108:111], a[120:123], v[56:59]\n"
        "global_load_lds_dwordx4 v254, s[20:21] offset:2048 sc1\n"
        "s_waitcnt lgkmcnt(7)\n"
        "v_mfma_f32_16x16x32_bf16 v[60:63], a[108:111], a[124:127], v[60:63]\n"
        "s_waitcnt lgkmcnt(6)\n"
        "v_mfma_f32_16x16x32_bf16 v[0:3], a[128:131], a[144:147], v[0:3]\n"
        "s_waitcnt lgkmcnt(5)\n"
        "v_mfma_f32_16x16x32_bf16 v[4:7], a[128:131], a[148:151], v[4:7]\n"
        "s_waitcnt lgkmcnt(4)\n"
        "v_mfma_f32_16x16x32_bf16 v[8:11], a[128:131], a[152:155], v[8:11]\n"
        "global_load_lds_dwordx4 v254, s[20:21] offset:3072 sc1\n"
        "s_add_u32 m0, s73, 0xc000\n"
        "s_waitcnt lgkmcnt(3)\n"
        "v_mfma_f32_16x16x32_bf16 v[12:15], a[128:131], a[156:159], v[12:15]\n"
        "s_add_u32 s20, s20, 0x4000\n"
        "s_addc_u32 s21, s21, 0\n"
        "s_waitcnt lgkmcnt(2)\n"
        "v_mfma_f32_16x16x32_bf16 v[16:19], a[132:135], a[144:147], v[16:19]\n"
        "v_mfma_f32_16x16x32_bf16 v[20:23], a[132:135], a[148:151], v[20:23]\n"
        "s_waitcnt vmcnt(24) lgkmcnt(0)\n"
        "s_barrier\n"
        "v_mfma_f32_16x16x32_bf16 v[24:27], a[132:135], a[152:155], v[24:27]\n"
        "global_load_lds_dwordx4 v160, s[22:23] offset:0\n"
        "ds_read_b64_tr_b16 a[64:65], v174 offset:0\n"
        "v_mfma_f32_16x16x32_bf16 v[28:31], a[132:135], a[156:159], v[28:31]\n"
        "ds_read_b64_tr_b16 a[66:67], v174 offset:4096\n"
        "v_mfma_f32_16x16x32_bf16 v[32:35], a[136:139], a[144:147], v[32:35]\n"
        "ds_read_b128 a[80:83], v165 offset:0\n"
        "v_mfma_f32_16x16x32_bf16 v[36:39], a[136:139], a[148:151], v[36:39]\n"
        "ds_read_b128 a[84:87], v165 offset:1024\n"
        "v_mfma_f32_16x16x32_bf16 v[40:43], a[136:139], a[152:155], v[40:43]\n"
        "s_cmp_lg_u32 s40, 0\n"
        "s_cbranch_scc1 LW2_%=\n"
        "s_mov_b64 exec, 0xffff\n"
        "global_store_dwordx2 v182, v[250:251], s[48:49] sc1\n"
        "s_mov_b64 exec, -1\n"
        "LW2_%=:\n"
        "v_mfma_f32_16x16x32_bf16 v[44:47], a[136:139], a[156:159], v[44:47]\n"
        "ds_read_b128 a[88:91], v168 offset:0\n"
        "global_load_lds_dwordx4 v161, s[22:23] offset:1024\n"
        "v_mfma_f32_16x16x32_bf16 v[48:51], a[140:143], a[144:147], v[48:51]\n"
        "ds_read_b128 a[92:95], v168 offset:1024\n"
        "v_mfma_f32_16x16x32_bf16 v[52:55], a[140:143], a[148:151], v[52:55]\n"
        "ds_read_b64_tr_b16 a[68:69], v175 offset:0\n"
        "v_mfma_f32_16x16x32_bf16 v[56:59], a[140:143], a[152:155], v[56:59]\n"
        "ds_read_b64_tr_b16 a[70:71], v175 offset:4096\n"
        "s_waitcnt lgkmcnt(6)\n"
        "v_mfma_f32_16x16x32_bf16 v[60:63], a[140:143], a[156:159], v[60:63]\n"
        "s_waitcnt lgkmcnt(5)\n"
        "global_load_lds_dwordx4 v162, s[22:23] offset:2048\n"
        "v_mfma_f32_16x16x32_bf16 v[0:3], a[64:67], a[80:83], v[0:3]\n"
        "ds_read_b64_tr_b16 a[72:73], v176 offset:0\n"
        "s_waitcnt lgkmcnt(5)\n"
        "v_mfma_f32_16x16x32_bf16 v[4:7], a[64:67], a[84:87], v[4:7]\n"
        "ds_read_b64_tr_b16 a[74:75], v176 offset:4096\n"
        "ds_read_b64_tr_b16 a[76:77], v177 offset:0\n"
        "ds_read_b64_tr_b16 a[78:79], v177 offset:4096\n"
        "s_waitcnt lgkmcnt(7)\n"
        "v_mfma_f32_16x16x32_bf16 v[8:11], a[64:67], a[88:91], v[8:11]\n"
        "global_load_lds_dwordx4 v163, s[22:23] offset:3072\n"
        "s_add_u32 m0, s73, 0x8000\n"
        "s_waitcnt lgkmcnt(6)\n"
        "v_mfma_f32_16x16x32_bf16 v[12:15], a[64:67], a[92:95], v[12:15]\n"
        "ds_read_b64_tr_b16 a[96:97], v174 offset:8192\n"
        "ds_read_b64_tr_b16 a[98:99], v174 offset:12288\n"
        "ds_read_b128 a[112:115], v165 offset:8192\n"
        "s_waitcnt lgkmcnt(7)\n"
        "v_mfma_f32_16x16x32_bf16 v[16:19], a[68:71], a[80:83], v[16:19]\n"
        "s_add_u32 s22, s22, 0x20000\n"
        "s_addc_u32 s23, s23, 0\n"
        "v_bfe_u32 v182, v254, 4, 5\n"
        "v_mfma_f32_16x16x32_bf16 v[20:23], a[68:71], a[84:87], v[20:23]\n"
        "global_load_lds_dwordx4 v254, s[20:21] offset:0 sc1\n"
        "ds_read_b128 a[116:119], v165 offset:9216\n"
        "v_mfma_f32_16x16x32_bf16 v[24:27], a[68:71], a[88:91], v[24:27]\n"
        "ds_read_b128 a[120:123], v168 offset:8192\n"
        "v_lshl_add_u32 v182, v182, 7, s14\n"
        "v_mfma_f32_16x16x32_bf16 v[28:31], a[68:71], a[92:95], v[28:31]\n"
        "ds_read_b128 a[124:127], v168 offset:9216\n"
        "s_and_b32 s14, s40, 2\n"
        "s_waitcnt lgkmcnt(7)\n"
        "v_mfma_f32_16x16x32_bf16 v[32:35], a[72:75], a[80:83], v[32:35]\n"
        "ds_read_b64_tr_b16 a[100:101], v175 offset:8192\n"
        "s_add_u32 s14, s14, s10\n"
        "s_lshl2_add_u32 s14, s12, s14\n"
        "v_mfma_f32_16x16x32_bf16 v[36:39], a[72:75], a[84:87], v[36:39]\n"
        "global_load_lds_dwordx4 v254, s[20:21] offset:1024 sc1\n"
        "ds_read_b64_tr_b16 a[102:103], v175 offset:12288\n"
        "s_lshl_b32 s14, s14, 5\n"
        "v_mfma_f32_16x16x32_bf16 v[40:43], a[72:75], a[88:91], v[40:43]\n"
        "ds_read_b64_tr_b16 a[104:105], v176 offset:8192\n"
        "s_lshl_b32 s16, s14, 11\n"
        "s_lshl1_add_u32 s16, s15, s16\n"
        "v_mfma_f32_16x16x32_bf16 v[44:47], a[72:75], a[92:95], v[44:47]\n"
        "ds_read_b64_tr_b16 a[106:107], v176 offset:12288\n"
        "s_waitcnt lgkmcnt(7)\n"
        "v_mfma_f32_16x16x32_bf16 v[48:51], a[76:79], a[80:83], v[48:51]\n"
        "ds_read_b64_tr_b16 a[108:109], v177 offset:8192\n"
        "s_add_u32 s30, s8, s16\n"
        "s_addc_u32 s31, s9, 0\n"
        "v_mfma_f32_16x16x32_bf16 v[52:55], a[76:79], a[84:87], v[52:55]\n"
        "ds_read_b64_tr_b16 a[110:111], v177 offset:12288\n"
        "global_load_lds_dwordx4 v254, s[20:21] offset:2048 sc1\n"
        "v_mfma_f32_16x16x32_bf16 v[56:59], a[76:79], a[88:91], v[56:59]\n"
        "s_waitcnt lgkmcnt(7)\n"
        "v_add_u32 v190, s14, v232\n"
        "v_lshlrev_b32 v190, 7, v190\n"
        "v_mfma_f32_16x16x32_bf16 v[60:63], a[76:79], a[92:95], v[60:63]\n"
        "s_add_u32 s56, s36, s16\n"
        "s_addc_u32 s57, s37, 0\n"
        "s_lshl_b32 s16, s15, 12\n"
        "v_mfma_f32_16x16x32_bf16 v[0:3], a[96:99], a[112:115], v[0:3]\n"
        "s_lshl1_add_u32 s16, s14, s16\n"
        "s_add_u32 s16, s16, 0x3400000\n"
        "s_lshl2_add_u32 s14, s13, s40\n"
        "v_mfma_f32_16x16x32_bf16 v[4:7], a[96:99], a[116:119], v[4:7]\n"
        "global_load_lds_dwordx4 v254, s[20:21] offset:3072 sc1\n"
        "s_add_u32 m0, s73, 0x14000\n"
        "v_mfma_f32_16x16x32_bf16 v[8:11], a[96:99], a[120:123], v[8:11]\n"
        "s_add_u32 s20, s20, 0x4000\n"
        "s_addc_u32 s21, s21, 0\n"
        "s_waitcnt lgkmcnt(3)\n"
        "v_mfma_f32_16x16x32_bf16 v[12:15], a[96:99], a[124:127], v[12:15]\n"
        "s_add_u32 s58, s38, s16\n"
        "s_addc_u32 s59, s39, 0\n"
        "s_add_u32 s16, s73, 0x10000\n"
        "v_mfma_f32_16x16x32_bf16 v[16:19], a[100:103], a[112:115], v[16:19]\n"
        "s_add_u32 s14, s14, 3072\n"
        "s_lshl_b32 s14, s14, 7\n"
        "s_waitcnt lgkmcnt(1)\n"
        "v_mfma_f32_16x16x32_bf16 v[20:23], a[100:103], a[116:119], v[20:23]\n"
        "s_waitcnt vmcnt(24) lgkmcnt(0)\n"
        "s_barrier\n"
        "v_mfma_f32_16x16x32_bf16 v[24:27], a[100:103], a[120:123], v[24:27]\n"
        "global_load_lds_dwordx4 v160, s[22:23] offset:0\n"
        "ds_read_b64_tr_b16 a[128:129], v174 offset:32768\n"
        "v_mfma_f32_16x16x32_bf16 v[28:31], a[100:103], a[124:127], v[28:31]\n"
        "ds_read_b64_tr_b16 a[130:131], v174 offset:36864\n"
        "v_mfma_f32_16x16x32_bf16 v[32:35], a[104:107], a[112:115], v[32:35]\n"
        "ds_read_b128 a[144:147], v165 offset:32768\n"
        "v_mfma_f32_16x16x32_bf16 v[36:39], a[104:107], a[116:119], v[36:39]\n"
        "ds_read_b128 a[148:151], v165 offset:33792\n"
        "v_mfma_f32_16x16x32_bf16 v[40:43], a[104:107], a[120:123], v[40:43]\n"
        "ds_read_b128 a[152:155], v168 offset:32768\n"
        "global_load_lds_dwordx4 v161, s[22:23] offset:1024\n"
        "v_mfma_f32_16x16x32_bf16 v[44:47], a[104:107], a[124:127], v[44:47]\n"
        "ds_read_b128 a[156:159], v168 offset:33792\n"
        "v_mfma_f32_16x16x32_bf16 v[48:51], a[108:111], a[112:115], v[48:51]\n"
        "ds_read_b64_tr_b16 a[132:133], v175 offset:32768\n"
        "v_mfma_f32_16x16x32_bf16 v[52:55], a[108:111], a[116:119], v[52:55]\n"
        "ds_read_b64_tr_b16 a[134:135], v175 offset:36864\n"
        "v_mfma_f32_16x16x32_bf16 v[56:59], a[108:111], a[120:123], v[56:59]\n"
        "global_load_lds_dwordx4 v162, s[22:23] offset:2048\n"
        "v_mfma_f32_16x16x32_bf16 v[60:63], a[108:111], a[124:127], v[60:63]\n"
        "ds_read_b64_tr_b16 a[136:137], v176 offset:32768\n"
        "s_waitcnt lgkmcnt(6)\n"
        "v_mfma_f32_16x16x32_bf16 v[0:3], a[128:131], a[144:147], v[0:3]\n"
        "ds_read_b64_tr_b16 a[138:139], v176 offset:36864\n"
        "s_waitcnt lgkmcnt(6)\n"
        "v_mfma_f32_16x16x32_bf16 v[4:7], a[128:131], a[148:151], v[4:7]\n"
        "ds_read_b64_tr_b16 a[140:141], v177 offset:32768\n"
        "s_waitcnt lgkmcnt(6)\n"
        "v_mfma_f32_16x16x32_bf16 v[8:11], a[128:131], a[152:155], v[8:11]\n"
        "ds_read_b64_tr_b16 a[142:143], v177 offset:36864\n"
        "s_waitcnt lgkmcnt(6)\n"
        "v_mfma_f32_16x16x32_bf16 v[12:15], a[128:131], a[156:159], v[12:15]\n"
        "global_load_lds_dwordx4 v163, s[22:23] offset:3072\n"
        "ds_read_b64_tr_b16 a[64:65], v174 offset:40960\n"
        "ds_read_b64_tr_b16 a[66:67], v174 offset:45056\n"
        "s_add_u32 s22, s22, 0x20000\n"
        "s_addc_u32 s23, s23, 0\n"
        "s_waitcnt lgkmcnt(6)\n"
        "v_mfma_f32_16x16x32_bf16 v[16:19], a[132:135], a[144:147], v[16:19]\n"
        "ds_read_b128 a[80:83], v165 offset:40960\n"
        "s_add_u32 m0, s73, 0x10000\n"
        "v_mfma_f32_16x16x32_bf16 v[20:23], a[132:135], a[148:151], v[20:23]\n"
        "ds_read_b128 a[84:87], v165 offset:41984\n"
        "v_mfma_f32_16x16x32_bf16 v[24:27], a[132:135], a[152:155], v[24:27]\n"
        "global_load_lds_dwordx4 v254, s[20:21] offset:0 sc1\n"
        "ds_read_b128 a[88:91], v168 offset:40960\n"
        "v_mfma_f32_16x16x32_bf16 v[28:31], a[132:135], a[156:159], v[28:31]\n"
        "ds_read_b128 a[92:95], v168 offset:41984\n"
        "s_waitcnt lgkmcnt(8)\n"
        "v_mfma_f32_16x16x32_bf16 v[32:35], a[136:139], a[144:147], v[32:35]\n"
        "ds_read_b64_tr_b16 a[68:69], v175 offset:40960\n"
        "s_waitcnt lgkmcnt(8)\n"
        "v_mfma_f32_16x16x32_bf16 v[36:39], a[136:139], a[148:151], v[36:39]\n"
        "ds_read_b64_tr_b16 a[70:71], v175 offset:45056\n"
        "s_waitcnt lgkmcnt(8)\n"
        "v_mfma_f32_16x16x32_bf16 v[40:43], a[136:139], a[152:155], v[40:43]\n"
        "global_load_lds_dwordx4 v254, s[20:21] offset:1024 sc1\n"
        "ds_read_b64_tr_b16 a[72:73], v176 offset:40960\n"
        "v_mfma_f32_16x16x32_bf16 v[44:47], a[136:139], a[156:159], v[44:47]\n"
        "ds_read_b64_tr_b16 a[74:75], v176 offset:45056\n"
        "s_waitcnt lgkmcnt(8)\n"
        "v_mfma_f32_16x16x32_bf16 v[48:51], a[140:143], a[144:147], v[48:51]\n"
        "ds_read_b64_tr_b16 a[76:77], v177 offset:40960\n"
        "s_waitcnt lgkmcnt(8)\n"
        "v_mfma_f32_16x16x32_bf16 v[52:55], a[140:143], a[148:151], v[52:55]\n"
        "ds_read_b64_tr_b16 a[78:79], v177 offset:45056\n"
        "s_waitcnt lgkmcnt(8)\n"
        "v_mfma_f32_16x16x32_bf16 v[56:59], a[140:143], a[152:155], v[56:59]\n"
        "global_load_lds_dwordx4 v254, s[20:21] offset:2048 sc1\n"
        "s_waitcnt lgkmcnt(7)\n"
        "v_mfma_f32_16x16x32_bf16 v[60:63], a[140:143], a[156:159], v[60:63]\n"
        "s_waitcnt lgkmcnt(6)\n"
        "v_mfma_f32_16x16x32_bf16 v[0:3], a[64:67], a[80:83], v[0:3]\n"
        "s_waitcnt lgkmcnt(5)\n"
        "v_mfma_f32_16x16x32_bf16 v[4:7], a[64:67], a[84:87], v[4:7]\n"
        "s_waitcnt lgkmcnt(4)\n"
        "v_mfma_f32_16x16x32_bf16 v[8:11], a[64:67], a[88:91], v[8:11]\n"
        "global_load_lds_dwordx4 v254, s[20:21] offset:3072 sc1\n"
        "s_add_u32 m0, s73, 0x1c000\n"
        "s_waitcnt lgkmcnt(3)\n"
        "v_mfma_f32_16x16x32_bf16 v[12:15], a[64:67], a[92:95], v[12:15]\n"
        "s_add_u32 s20, s20, 0x4000\n"
        "s_addc_u32 s21, s21, 0\n"
        "s_waitcnt lgkmcnt(2)\n"
        "v_mfma_f32_16x16x32_bf16 v[16:19], a[68:71], a[80:83], v[16:19]\n"
        "v_mfma_f32_16x16x32_bf16 v[20:23], a[68:71], a[84:87], v[20:23]\n"
        "s_waitcnt vmcnt(24) lgkmcnt(0)\n"
        "s_barrier\n"
        "v_mfma_f32_16x16x32_bf16 v[24:27], a[68:71], a[88:91], v[24:27]\n"
        "global_load_lds_dwordx4 v160, s[22:23] offset:0\n"
        "ds_read_b64_tr_b16 a[96:97], v178 offset:0\n"
        "v_mfma_f32_16x16x32_bf16 v[28:31], a[68:71], a[92:95], v[28:31]\n"
        "ds_read_b64_tr_b16 a[98:99], v178 offset:4096\n"
        "v_mfma_f32_16x16x32_bf16 v[32:35], a[72:75], a[80:83], v[32:35]\n"
        "ds_read_b128 a[112:115], v166 offset:0\n"
        "v_mfma_f32_16x16x32_bf16 v[36:39], a[72:75], a[84:87], v[36:39]\n"
        "ds_read_b128 a[116:119], v166 offset:1024\n"
        "v_mfma_f32_16x16x32_bf16 v[40:43], a[72:75], a[88:91], v[40:43]\n"
        "ds_read_b128 a[120:123], v169 offset:0\n"
        "global_load_lds_dwordx4 v161, s[22:23] offset:1024\n"
        "v_mfma_f32_16x16x32_bf16 v[44:47], a[72:75], a[92:95], v[44:47]\n"
        "ds_read_b128 a[124:127], v169 offset:1024\n"
        "v_mfma_f32_16x16x32_bf16 v[48:51], a[76:79], a[80:83], v[48:51]\n"
        "ds_read_b64_tr_b16 a[100:101], v179 offset:0\n"
        "v_mfma_f32_16x16x32_bf16 v[52:55], a[76:79], a[84:87], v[52:55]\n"
        "ds_read_b64_tr_b16 a[102:103], v179 offset:4096\n"
        "v_mfma_f32_16x16x32_bf16 v[56:59], a[76:79], a[88:91], v[56:59]\n"
        "global_load_lds_dwordx4 v162, s[22:23] offset:2048\n"
        "v_mfma_f32_16x16x32_bf16 v[60:63], a[76:79], a[92:95], v[60:63]\n"
        "ds_read_b64_tr_b16 a[104:105], v180 offset:0\n"
        "s_waitcnt lgkmcnt(6)\n"
        "v_mfma_f32_16x16x32_bf16 v[0:3], a[96:99], a[112:115], v[0:3]\n"
        "ds_read_b64_tr_b16 a[106:107], v180 offset:4096\n"
        "s_waitcnt lgkmcnt(6)\n"
        "v_mfma_f32_16x16x32_bf16 v[4:7], a[96:99], a[116:119], v[4:7]\n"
        "ds_read_b64_tr_b16 a[108:109], v181 offset:0\n"
        "s_waitcnt lgkmcnt(6)\n"
        "v_mfma_f32_16x16x32_bf16 v[8:11], a[96:99], a[120:123], v[8:11]\n"
        "ds_read_b64_tr_b16 a[110:111], v181 offset:4096\n"
        "s_waitcnt lgkmcnt(6)\n"
        "v_mfma_f32_16x16x32_bf16 v[12:15], a[96:99], a[124:127], v[12:15]\n"
        "global_load_lds_dwordx4 v163, s[22:23] offset:3072\n"
        "ds_read_b64_tr_b16 a[128:129], v178 offset:8192\n"
        "ds_read_b64_tr_b16 a[130:131], v178 offset:12288\n"
        "s_add_u32 s22, s22, 0x20000\n"
        "s_addc_u32 s23, s23, 0\n"
        "s_waitcnt lgkmcnt(6)\n"
        "v_mfma_f32_16x16x32_bf16 v[16:19], a[100:103], a[112:115], v[16:19]\n"
        "ds_read_b128 a[144:147], v166 offset:8192\n"
        "s_add_u32 m0, s73, 0x18000\n"
        "v_mfma_f32_16x16x32_bf16 v[20:23], a[100:103], a[116:119], v[20:23]\n"
        "ds_read_b128 a[148:151], v166 offset:9216\n"
        "v_mfma_f32_16x16x32_bf16 v[24:27], a[100:103], a[120:123], v[24:27]\n"
        "global_load_lds_dwordx4 v254, s[20:21] offset:0 sc1\n"
        "ds_read_b128 a[152:155], v169 offset:8192\n"
        "v_mfma_f32_16x16x32_bf16 v[28:31], a[100:103], a[124:127], v[28:31]\n"
        "ds_read_b128 a[156:159], v169 offset:9216\n"
        "s_waitcnt lgkmcnt(8)\n"
        "v_mfma_f32_16x16x32_bf16 v[32:35], a[104:107], a[112:115], v[32:35]\n"
        "ds_read_b64_tr_b16 a[132:133], v179 offset:8192\n"
        "s_waitcnt lgkmcnt(8)\n"
        "v_mfma_f32_16x16x32_bf16 v[36:39], a[104:107], a[116:119], v[36:39]\n"
        "ds_read_b64_tr_b16 a[134:135], v179 offset:12288\n"
        "s_waitcnt lgkmcnt(8)\n"
        "v_mfma_f32_16x16x32_bf16 v[40:43], a[104:107], a[120:123], v[40:43]\n"
        "global_load_lds_dwordx4 v254, s[20:21] offset:1024 sc1\n"
        "ds_read_b64_tr_b16 a[136:137], v180 offset:8192\n"
        "v_mfma_f32_16x16x32_bf16 v[44:47], a[104:107], a[124:127], v[44:47]\n"
        "ds_read_b64_tr_b16 a[138:139], v180 offset:12288\n"
        "s_waitcnt lgkmcnt(8)\n"
        "v_mfma_f32_16x16x32_bf16 v[48:51], a[108:111], a[112:115], v[48:51]\n"
        "ds_read_b64_tr_b16 a[140:141], v181 offset:8192\n"
        "s_waitcnt lgkmcnt(8)\n"
        "v_mfma_f32_16x16x32_bf16 v[52:55], a[108:111], a[116:119], v[52:55]\n"
        "ds_read_b64_tr_b16 a[142:143], v181 offset:12288\n"
        "s_waitcnt lgkmcnt(8)\n"
        "v_mfma_f32_16x16x32_bf16 v[56:59], a[108:111], a[120:123], v[56:59]\n"
        "global_load_lds_dwordx4 v254, s[20:21] offset:2048 sc1\n"
        "s_waitcnt lgkmcnt(7)\n"
        "v_mfma_f32_16x16x32_bf16 v[60:63], a[108:111], a[124:127], v[60:63]\n"
        "s_waitcnt lgkmcnt(6)\n"
        "v_mfma_f32_16x16x32_bf16 v[0:3], a[128:131], a[144:147], v[0:3]\n"
        "s_waitcnt lgkmcnt(5)\n"
        "v_mfma_f32_16x16x32_bf16 v[4:7], a[128:131], a[148:151], v[4:7]\n"
        "s_waitcnt lgkmcnt(4)\n"
        "v_mfma_f32_16x16x32_bf16 v[8:11], a[128:131], a[152:155], v[8:11]\n"
        "global_load_lds_dwordx4 v254, s[20:21] offset:3072 sc1\n"
        "s_add_u32 m0, s73, 0x24000\n"
        "s_waitcnt lgkmcnt(3)\n"
        "v_mfma_f32_16x16x32_bf16 v[12:15], a[128:131], a[156:159], v[12:15]\n"
        "s_add_u32 s20, s20, 0x4000\n"
        "s_addc_u32 s21, s21, 0\n"
        "s_waitcnt lgkmcnt(2)\n"
        "v_mfma_f32_16x16x32_bf16 v[16:19], a[132:135], a[144:147], v[16:19]\n"
        "v_mfma_f32_16x16x32_bf16 v[20:23], a[132:135], a[148:151], v[20:23]\n"
        "s_waitcnt vmcnt(24) lgkmcnt(0)\n"
        "s_barrier\n"
        "v_mfma_f32_16x16x32_bf16 v[24:27], a[132:135], a[152:155], v[24:27]\n"
        "global_load_lds_dwordx4 v160, s[22:23] offset:0\n"
        "ds_read_b64_tr_b16 a[64:65], v170 offset:0\n"
        "v_mfma_f32_16x16x32_bf16 v[28:31], a[132:135], a[156:159], v[28:31]\n"
        "ds_read_b64_tr_b16 a[66:67], v170 offset:4096\n"
        "v_mfma_f32_16x16x32_bf16 v[32:35], a[136:139], a[144:147], v[32:35]\n"
        "ds_read_b128 a[80:83], v164 offset:0\n"
        "v_mfma_f32_16x16x32_bf16 v[36:39], a[136:139], a[148:151], v[36:39]\n"
        "ds_read_b128 a[84:87], v164 offset:1024\n"
        "v_mfma_f32_16x16x32_bf16 v[40:43], a[136:139], a[152:155], v[40:43]\n"
        "ds_read_b128 a[88:91], v167 offset:0\n"
        "global_load_lds_dwordx4 v161, s[22:23] offset:1024\n"
        "v_mfma_f32_16x16x32_bf16 v[44:47], a[136:139], a[156:159], v[44:47]\n"
        "ds_read_b128 a[92:95], v167 offset:1024\n"
        "v_mfma_f32_16x16x32_bf16 v[48:51], a[140:143], a[144:147], v[48:51]\n"
        "ds_read_b64_tr_b16 a[68:69], v171 offset:0\n"
        "v_mfma_f32_16x16x32_bf16 v[52:55], a[140:143], a[148:151], v[52:55]\n"
        "ds_read_b64_tr_b16 a[70:71], v171 offset:4096\n"
        "v_mfma_f32_16x16x32_bf16 v[56:59], a[140:143], a[152:155], v[56:59]\n"
        "global_load_lds_dwordx4 v162, s[22:23] offset:2048\n"
        "v_mfma_f32_16x16x32_bf16 v[60:63], a[140:143], a[156:159], v[60:63]\n"
        "ds_read_b64_tr_b16 a[72:73], v172 offset:0\n"
        "s_waitcnt lgkmcnt(6)\n"
        "v_mfma_f32_16x16x32_bf16 v[0:3], a[64:67], a[80:83], v[0:3]\n"
        "ds_read_b64_tr_b16 a[74:75], v172 offset:4096\n"
        "s_waitcnt lgkmcnt(6)\n"
        "v_mfma_f32_16x16x32_bf16 v[4:7], a[64:67], a[84:87], v[4:7]\n"
        "ds_read_b64_tr_b16 a[76:77], v173 offset:0\n"
        "s_waitcnt lgkmcnt(6)\n"
        "v_mfma_f32_16x16x32_bf16 v[8:11], a[64:67], a[88:91], v[8:11]\n"
        "ds_read_b64_tr_b16 a[78:79], v173 offset:4096\n"
        "s_waitcnt lgkmcnt(6)\n"
        "v_mfma_f32_16x16x32_bf16 v[12:15], a[64:67], a[92:95], v[12:15]\n"
        "global_load_lds_dwordx4 v163, s[22:23] offset:3072\n"
        "ds_read_b64_tr_b16 a[96:97], v170 offset:8192\n"
        "ds_read_b64_tr_b16 a[98:99], v170 offset:12288\n"
        "s_add_u32 s22, s22, 0x20000\n"
        "s_addc_u32 s23, s23, 0\n"
        "s_waitcnt lgkmcnt(6)\n"
        "v_mfma_f32_16x16x32_bf16 v[16:19], a[68:71], a[80:83], v[16:19]\n"
        "ds_read_b128 a[112:115], v164 offset:8192\n"
        "s_add_u32 m0, s73, 0x20000\n"
        "v_mfma_f32_16x16x32_bf16 v[20:23], a[68:71], a[84:87], v[20:23]\n"
        "ds_read_b128 a[116:119], v164 offset:9216\n"
        "v_mfma_f32_16x16x32_bf16 v[24:27], a[68:71], a[88:91], v[24:27]\n"
        "global_load_lds_dwordx4 v254, s[20:21] offset:0 sc1\n"
        "ds_read_b128 a[120:123], v167 offset:8192\n"
        "v_mfma_f32_16x16x32_bf16 v[28:31], a[68:71], a[92:95], v[28:31]\n"
        "ds_read_b128 a[124:127], v167 offset:9216\n"
        "s_waitcnt lgkmcnt(8)\n"
        "v_mfma_f32_16x16x32_bf16 v[32:35], a[72:75], a[80:83], v[32:35]\n"
        "ds_read_b64_tr_b16 a[100:101], v171 offset:8192\n"
        "s_waitcnt lgkmcnt(8)\n"
        "v_mfma_f32_16x16x32_bf16 v[36:39], a[72:75], a[84:87], v[36:39]\n"
        "ds_read_b64_tr_b16 a[102:103], v171 offset:12288\n"
        "s_waitcnt lgkmcnt(8)\n"
        "v_mfma_f32_16x16x32_bf16 v[40:43], a[72:75], a[88:91], v[40:43]\n"
        "global_load_lds_dwordx4 v254, s[20:21] offset:1024 sc1\n"
        "ds_read_b64_tr_b16 a[104:105], v172 offset:8192\n"
        "v_mfma_f32_16x16x32_bf16 v[44:47], a[72:75], a[92:95], v[44:47]\n"
        "ds_read_b64_tr_b16 a[106:107], v172 offset:12288\n"
        "s_waitcnt lgkmcnt(8)\n"
        "v_mfma_f32_16x16x32_bf16 v[48:51], a[76:79], a[80:83], v[48:51]\n"
        "ds_read_b64_tr_b16 a[108:109], v173 offset:8192\n"
        "s_waitcnt lgkmcnt(8)\n"
        "v_mfma_f32_16x16x32_bf16 v[52:55], a[76:79], a[84:87], v[52:55]\n"
        "ds_read_b64_tr_b16 a[110:111], v173 offset:12288\n"
        "s_waitcnt lgkmcnt(8)\n"
        "v_mfma_f32_16x16x32_bf16 v[56:59], a[76:79], a[88:91], v[56:59]\n"
        "global_load_lds_dwordx4 v254, s[20:21] offset:2048 sc1\n"
        "s_waitcnt lgkmcnt(7)\n"
        "v_mfma_f32_16x16x32_bf16 v[60:63], a[76:79], a[92:95], v[60:63]\n"
        "s_waitcnt lgkmcnt(6)\n"
        "v_mfma_f32_16x16x32_bf16 v[0:3], a[96:99], a[112:115], v[0:3]\n"
        "s_waitcnt lgkmcnt(5)\n"
        "v_mfma_f32_16x16x32_bf16 v[4:7], a[96:99], a[116:119], v[4:7]\n"
        "s_waitcnt lgkmcnt(4)\n"
        "v_mfma_f32_16x16x32_bf16 v[8:11], a[96:99], a[120:123], v[8:11]\n"
        "global_load_lds_dwordx4 v254, s[20:21] offset:3072 sc1\n"
        "s_add_u32 m0, s73, 0x4000\n"
        "s_waitcnt lgkmcnt(3)\n"
        "v_mfma_f32_16x16x32_bf16 v[12:15], a[96:99], a[124:127], v[12:15]\n"
        "s_add_u32 s20, s20, 0x4000\n"
        "s_addc_u32 s21, s21, 0\n"
        "s_waitcnt lgkmcnt(2)\n"
        "v_mfma_f32_16x16x32_bf16 v[16:19], a[100:103], a[112:115], v[16:19]\n"
        "v_mfma_f32_16x16x32_bf16 v[20:23], a[100:103], a[116:119], v[20:23]\n"
        "s_waitcnt vmcnt(24) lgkmcnt(0)\n"
        "s_barrier\n"
        "v_mfma_f32_16x16x32_bf16 v[24:27], a[100:103], a[120:123], v[24:27]\n"
        "global_load_lds_dwordx4 v160, s[22:23] offset:0\n"
        "ds_read_b64_tr_b16 a[128:129], v170 offset:32768\n"
        "v_mfma_f32_16x16x32_bf16 v[28:31], a[100:103], a[124:127], v[28:31]\n"
        "ds_read_b64_tr_b16 a[130:131], v170 offset:36864\n"
        "v_mfma_f32_16x16x32_bf16 v[32:35], a[104:107], a[112:115], v[32:35]\n"
        "ds_read_b128 a[144:147], v164 offset:32768\n"
        "v_mfma_f32_16x16x32_bf16 v[36:39], a[104:107], a[116:119], v[36:39]\n"
        "ds_read_b128 a[148:151], v164 offset:33792\n"
        "v_mfma_f32_16x16x32_bf16 v[40:43], a[104:107], a[120:123], v[40:43]\n"
        "ds_read_b128 a[152:155], v167 offset:32768\n"
        "global_load_lds_dwordx4 v161, s[22:23] offset:1024\n"
        "v_mfma_f32_16x16x32_bf16 v[44:47], a[104:107], a[124:127], v[44:47]\n"
        "ds_read_b128 a[156:159], v167 offset:33792\n"
        "v_mfma_f32_16x16x32_bf16 v[48:51], a[108:111], a[112:115], v[48:51]\n"
        "ds_read_b64_tr_b16 a[132:133], v171 offset:32768\n"
        "v_mfma_f32_16x16x32_bf16 v[52:55], a[108:111], a[116:119], v[52:55]\n"
        "ds_read_b64_tr_b16 a[134:135], v171 offset:36864\n"
        "v_mfma_f32_16x16x32_bf16 v[56:59], a[108:111], a[120:123], v[56:59]\n"
        "global_load_lds_dwordx4 v162, s[22:23] offset:2048\n"
        "v_mfma_f32_16x16x32_bf16 v[60:63], a[108:111], a[124:127], v[60:63]\n"
        "ds_read_b64_tr_b16 a[136:137], v172 offset:32768\n"
        "s_waitcnt lgkmcnt(6)\n"
        "v_mfma_f32_16x16x32_bf16 v[0:3], a[128:131], a[144:147], v[0:3]\n"
        "ds_read_b64_tr_b16 a[138:139], v172 offset:36864\n"
        "s_waitcnt lgkmcnt(6)\n"
        "v_mfma_f32_16x16x32_bf16 v[4:7], a[128:131], a[148:151], v[4:7]\n"
        "ds_read_b64_tr_b16 a[140:141], v173 offset:32768\n"
        "s_waitcnt lgkmcnt(6)\n"
        "v_mfma_f32_16x16x32_bf16 v[8:11], a[128:131], a[152:155], v[8:11]\n"
        "ds_read_b64_tr_b16 a[142:143], v173 offset:36864\n"
        "s_waitcnt lgkmcnt(6)\n"
        "v_mfma_f32_16x16x32_bf16 v[12:15], a[128:131], a[156:159], v[12:15]\n"
        "global_load_lds_dwordx4 v163, s[22:23] offset:3072\n"
        "ds_read_b64_tr_b16 a[64:65], v170 offset:40960\n"
        "ds_read_b64_tr_b16 a[66:67], v170 offset:45056\n"
        "s_add_u32 s22, s22, 0x20000\n"
        "s_addc_u32 s23, s23, 0\n"
        "s_waitcnt lgkmcnt(6)\n"
        "v_mfma_f32_16x16x32_bf16 v[16:19], a[132:135], a[144:147], v[16:19]\n"
        "ds_read_b128 a[80:83], v164 offset:40960\n"
        "s_add_u32 m0, s73, 0x0\n"
        "v_mfma_f32_16x16x32_bf16 v[20:23], a[132:135], a[148:151], v[20:23]\n"
        "ds_read_b128 a[84:87], v164 offset:41984\n"
        "v_mfma_f32_16x16x32_bf16 v[24:27], a[132:135], a[152:155], v[24:27]\n"
        "global_load_lds_dwordx4 v254, s[20:21] offset:0 sc1\n"
        "ds_read_b128 a[88:91], v167 offset:40960\n"
        "v_mfma_f32_16x16x32_bf16 v[28:31], a[132:135], a[156:159], v[28:31]\n"
        "ds_read_b128 a[92:95], v167 offset:41984\n"
        "s_waitcnt lgkmcnt(8)\n"
        "v_mfma_f32_16x16x32_bf16 v[32:35], a[136:139], a[144:147], v[32:35]\n"
        "ds_read_b64_tr_b16 a[68:69], v171 offset:40960\n"
        "s_waitcnt lgkmcnt(8)\n"
        "v_mfma_f32_16x16x32_bf16 v[36:39], a[136:139], a[148:151], v[36:39]\n"
        "ds_read_b64_tr_b16 a[70:71], v171 offset:45056\n"
        "s_waitcnt lgkmcnt(8)\n"
        "v_mfma_f32_16x16x32_bf16 v[40:43], a[136:139], a[152:155], v[40:43]\n"
        "global_load_lds_dwordx4 v254, s[20:21] offset:1024 sc1\n"
        "ds_read_b64_tr_b16 a[72:73], v172 offset:40960\n"
        "v_mfma_f32_16x16x32_bf16 v[44:47], a[136:139], a[156:159], v[44:47]\n"
        "ds_read_b64_tr_b16 a[74:75], v172 offset:45056\n"
        "s_waitcnt lgkmcnt(8)\n"
        "v_mfma_f32_16x16x32_bf16 v[48:51], a[140:143], a[144:147], v[48:51]\n"
        "ds_read_b64_tr_b16 a[76:77], v173 offset:40960\n"
        "s_waitcnt lgkmcnt(8)\n"
        "v_mfma_f32_16x16x32_bf16 v[52:55], a[140:143], a[148:151], v[52:55]\n"
        "ds_read_b64_tr_b16 a[78:79], v173 offset:45056\n"
        "s_waitcnt lgkmcnt(8)\n"
        "v_mfma_f32_16x16x32_bf16 v[56:59], a[140:143], a[152:155], v[56:59]\n"
        "global_load_lds_dwordx4 v254, s[20:21] offset:2048 sc1\n"
        "s_waitcnt lgkmcnt(7)\n"
        "v_mfma_f32_16x16x32_bf16 v[60:63], a[140:143], a[156:159], v[60:63]\n"
        "s_waitcnt lgkmcnt(6)\n"
        "v_mfma_f32_16x16x32_bf16 v[0:3], a[64:67], a[80:83], v[0:3]\n"
        "s_waitcnt lgkmcnt(5)\n"
        "v_mfma_f32_16x16x32_bf16 v[4:7], a[64:67], a[84:87], v[4:7]\n"
        "s_waitcnt lgkmcnt(4)\n"
        "v_mfma_f32_16x16x32_bf16 v[8:11], a[64:67], a[88:91], v[8:11]\n"
        "global_load_lds_dwordx4 v254, s[20:21] offset:3072 sc1\n"
        "s_add_u32 m0, s73, 0xc000\n"
        "s_waitcnt lgkmcnt(3)\n"
        "v_mfma_f32_16x16x32_bf16 v[12:15], a[64:67], a[92:95], v[12:15]\n"
        "s_add_u32 s20, s20, 0x4000\n"
        "s_addc_u32 s21, s21, 0\n"
        "s_waitcnt lgkmcnt(2)\n"
        "v_mfma_f32_16x16x32_bf16 v[16:19], a[68:71], a[80:83], v[16:19]\n"
        "v_mfma_f32_16x16x32_bf16 v[20:23], a[68:71], a[84:87], v[20:23]\n"
        "s_waitcnt vmcnt(24) lgkmcnt(0)\n"
        "s_barrier\n"
        "v_mfma_f32_16x16x32_bf16 v[24:27], a[68:71], a[88:91], v[24:27]\n"
        "global_load_lds_dwordx4 v160, s[22:23] offset:0\n"
        "ds_read_b64_tr_b16 a[96:97], v174 offset:0\n"
        "v_mfma_f32_16x16x32_bf16 v[28:31], a[68:71], a[92:95], v[28:31]\n"
        "ds_read_b64_tr_b16 a[98:99], v174 offset:4096\n"
        "v_mfma_f32_16x16x32_bf16 v[32:35], a[72:75], a[80:83], v[32:35]\n"
        "ds_read_b128 a[112:115], v165 offset:0\n"
        "v_mfma_f32_16x16x32_bf16 v[36:39], a[72:75], a[84:87], v[36:39]\n"
        "ds_read_b128 a[116:119], v165 offset:1024\n"
        "v_mfma_f32_16x16x32_bf16 v[40:43], a[72:75], a[88:91], v[40:43]\n"
        "ds_read_b128 a[120:123], v168 offset:0\n"
        "global_load_lds_dwordx4 v161, s[22:23] offset:1024\n"
        "v_mfma_f32_16x16x32_bf16 v[44:47], a[72:75], a[92:95], v[44:47]\n"
        "ds_read_b128 a[124:127], v168 offset:1024\n"
        "v_mfma_f32_16x16x32_bf16 v[48:51], a[76:79], a[80:83], v[48:51]\n"
        "ds_read_b64_tr_b16 a[100:101], v175 offset:0\n"
        "v_mfma_f32_16x16x32_bf16 v[52:55], a[76:79], a[84:87], v[52:55]\n"
        "ds_read_b64_tr_b16 a[102:103], v175 offset:4096\n"
        "v_mfma_f32_16x16x32_bf16 v[56:59], a[76:79], a[88:91], v[56:59]\n"
        "global_load_lds_dwordx4 v162, s[22:23] offset:2048\n"
        "v_mfma_f32_16x16x32_bf16 v[60:63], a[76:79], a[92:95], v[60:63]\n"
        "ds_read_b64_tr_b16 a[104:105], v176 offset:0\n"
        "s_waitcnt lgkmcnt(6)\n"
        "v_mfma_f32_16x16x32_bf16 v[0:3], a[96:99], a[112:115], v[0:3]\n"
        "ds_read_b64_tr_b16 a[106:107], v176 offset:4096\n"
        "s_waitcnt lgkmcnt(6)\n"
        "v_mfma_f32_16x16x32_bf16 v[4:7], a[96:99], a[116:119], v[4:7]\n"
        "ds_read_b64_tr_b16 a[108:109], v177 offset:0\n"
        "s_waitcnt lgkmcnt(6)\n"
        "v_mfma_f32_16x16x32_bf16 v[8:11], a[96:99], a[120:123], v[8:11]\n"
        "ds_read_b64_tr_b16 a[110:111], v177 offset:4096\n"
        "s_waitcnt lgkmcnt(6)\n"
        "v_mfma_f32_16x16x32_bf16 v[12:15], a[96:99], a[124:127], v[12:15]\n"
        "global_load_lds_dwordx4 v163, s[22:23] offset:3072\n"
        "ds_read_b64_tr_b16 a[128:129], v174 offset:8192\n"
        "ds_read_b64_tr_b16 a[130:131], v174 offset:12288\n"
        "s_add_u32 s22, s22, 0x20000\n"
        "s_addc_u32 s23, s23, 0\n"
        "s_waitcnt lgkmcnt(6)\n"
        "v_mfma_f32_16x16x32_bf16 v[16:19], a[100:103], a[112:115], v[16:19]\n"
        "ds_read_b128 a[144:147], v165 offset:8192\n"
        "s_add_u32 m0, s73, 0x8000\n"
        "v_mfma_f32_16x16x32_bf16 v[20:23], a[100:103], a[116:119], v[20:23]\n"
        "ds_read_b128 a[148:151], v165 offset:9216\n"
        "v_mfma_f32_16x16x32_bf16 v[24:27], a[100:103], a[120:123], v[24:27]\n"
        "global_load_lds_dwordx4 v254, s[20:21] offset:0 sc1\n"
        "ds_read_b128 a[152:155], v168 offset:8192\n"
        "v_mfma_f32_16x16x32_bf16 v[28:31], a[100:103], a[124:127], v[28:31]\n"
        "ds_read_b128 a[156:159], v168 offset:9216\n"
        "s_waitcnt lgkmcnt(8)\n"
        "v_mfma_f32_16x16x32_bf16 v[32:35], a[104:107], a[112:115], v[32:35]\n"
        "ds_read_b64_tr_b16 a[132:133], v175 offset:8192\n"
        "s_waitcnt lgkmcnt(8)\n"
        "v_mfma_f32_16x16x32_bf16 v[36:39], a[104:107], a[116:119], v[36:39]\n"
        "ds_read_b64_tr_b16 a[134:135], v175 offset:12288\n"
        "s_waitcnt lgkmcnt(8)\n"
        "v_mfma_f32_16x16x32_bf16 v[40:43], a[104:107], a[120:123], v[40:43]\n"
        "global_load_lds_dwordx4 v254, s[20:21] offset:1024 sc1\n"
        "ds_read_b64_tr_b16 a[136:137], v176 offset:8192\n"
        "v_mfma_f32_16x16x32_bf16 v[44:47], a[104:107], a[124:127], v[44:47]\n"
        "ds_read_b64_tr_b16 a[138:139], v176 offset:12288\n"
        "s_waitcnt lgkmcnt(8)\n"
        "v_mfma_f32_16x16x32_bf16 v[48:51], a[108:111], a[112:115], v[48:51]\n"
        "ds_read_b64_tr_b16 a[140:141], v177 offset:8192\n"
        "s_waitcnt lgkmcnt(8)\n"
        "v_mfma_f32_16x16x32_bf16 v[52:55], a[108:111], a[116:119], v[52:55]\n"
        "ds_read_b64_tr_b16 a[142:143], v177 offset:12288\n"
        "s_waitcnt lgkmcnt(8)\n"
        "v_mfma_f32_16x16x32_bf16 v[56:59], a[108:111], a[120:123], v[56:59]\n"
        "global_load_lds_dwordx4 v254, s[20:21] offset:2048 sc1\n"
        "s_waitcnt lgkmcnt(7)\n"
        "v_mfma_f32_16x16x32_bf16 v[60:63], a[108:111], a[124:127], v[60:63]\n"
        "s_waitcnt lgkmcnt(6)\n"
        "v_mfma_f32_16x16x32_bf16 v[0:3], a[128:131], a[144:147], v[0:3]\n"
        "s_waitcnt lgkmcnt(5)\n"
        "v_mfma_f32_16x16x32_bf16 v[4:7], a[128:131], a[148:151], v[4:7]\n"
        "s_waitcnt lgkmcnt(4)\n"
        "v_mfma_f32_16x16x32_bf16 v[8:11], a[128:131], a[152:155], v[8:11]\n"
        "global_load_lds_dwordx4 v254, s[20:21] offset:3072 sc1\n"
        "s_add_u32 m0, s73, 0x14000\n"
        "s_waitcnt lgkmcnt(3)\n"
        "v_mfma_f32_16x16x32_bf16 v[12:15], a[128:131], a[156:159], v[12:15]\n"
        "s_add_u32 s20, s20, 0x4000\n"
        "s_addc_u32 s21, s21, 0\n"
        "s_waitcnt lgkmcnt(2)\n"
        "v_mfma_f32_16x16x32_bf16 v[16:19], a[132:135], a[144:147], v[16:19]\n"
        "v_mfma_f32_16x16x32_bf16 v[20:23], a[132:135], a[148:151], v[20:23]\n"
        "s_waitcnt vmcnt(24) lgkmcnt(0)\n"
        "s_barrier\n"
        "v_mfma_f32_16x16x32_bf16 v[24:27], a[132:135], a[152:155], v[24:27]\n"
        "global_load_lds_dwordx4 v160, s[22:23] offset:0\n"
        "ds_read_b64_tr_b16 a[64:65], v174 offset:32768\n"
        "v_mfma_f32_16x16x32_bf16 v[28:31], a[132:135], a[156:159], v[28:31]\n"
        "ds_read_b64_tr_b16 a[66:67], v174 offset:36864\n"
        "v_mfma_f32_16x16x32_bf16 v[32:35], a[136:139], a[144:147], v[32:35]\n"
        "ds_read_b128 a[80:83], v165 offset:32768\n"
        "v_mfma_f32_16x16x32_bf16 v[36:39], a[136:139], a[148:151], v[36:39]\n"
        "ds_read_b128 a[84:87], v165 offset:33792\n"
        "v_mfma_f32_16x16x32_bf16 v[40:43], a[136:139], a[152:155], v[40:43]\n"
        "ds_read_b128 a[88:91], v168 offset:32768\n"
        "global_load_lds_dwordx4 v161, s[22:23] offset:1024\n"
        "v_mfma_f32_16x16x32_bf16 v[44:47], a[136:139], a[156:159], v[44:47]\n"
        "ds_read_b128 a[92:95], v168 offset:33792\n"
        "v_mfma_f32_16x16x32_bf16 v[48:51], a[140:143], a[144:147], v[48:51]\n"
        "ds_read_b64_tr_b16 a[68:69], v175 offset:32768\n"
        "v_mfma_f32_16x16x32_bf16 v[52:55], a[140:143], a[148:151], v[52:55]\n"
        "ds_read_b64_tr_b16 a[70:71], v175 offset:36864\n"
        "v_mfma_f32_16x16x32_bf16 v[56:59], a[140:143], a[152:155], v[56:59]\n"
        "global_load_lds_dwordx4 v162, s[22:23] offset:2048\n"
        "v_mfma_f32_16x16x32_bf16 v[60:63], a[140:143], a[156:159], v[60:63]\n"
        "ds_read_b64_tr_b16 a[72:73], v176 offset:32768\n"
        "s_waitcnt lgkmcnt(6)\n"
        "v_mfma_f32_16x16x32_bf16 v[0:3], a[64:67], a[80:83], v[0:3]\n"
        "ds_read_b64_tr_b16 a[74:75], v176 offset:36864\n"
        "s_waitcnt lgkmcnt(6)\n"
        "v_mfma_f32_16x16x32_bf16 v[4:7], a[64:67], a[84:87], v[4:7]\n"
        "ds_read_b64_tr_b16 a[76:77], v177 offset:32768\n"
        "s_waitcnt lgkmcnt(6)\n"
        "v_mfma_f32_16x16x32_bf16 v[8:11], a[64:67], a[88:91], v[8:11]\n"
        "ds_read_b64_tr_b16 a[78:79], v177 offset:36864\n"
        "s_waitcnt lgkmcnt(6)\n"
        "v_mfma_f32_16x16x32_bf16 v[12:15], a[64:67], a[92:95], v[12:15]\n"
        "global_load_lds_dwordx4 v163, s[22:23] offset:3072\n"
        "ds_read_b64_tr_b16 a[96:97], v174 offset:40960\n"
        "ds_read_b64_tr_b16 a[98:99], v174 offset:45056\n"
        "s_add_u32 s22, s22, 0x20000\n"
        "s_addc_u32 s23, s23, 0\n"
        "s_waitcnt lgkmcnt(6)\n"
        "v_mfma_f32_16x16x32_bf16 v[16:19], a[68:71], a[80:83], v[16:19]\n"
        "ds_read_b128 a[112:115], v165 offset:40960\n"
        "s_add_u32 m0, s73, 0x10000\n"
        "v_mfma_f32_16x16x32_bf16 v[20:23], a[68:71], a[84:87], v[20:23]\n"
        "ds_read_b128 a[116:119], v165 offset:41984\n"
        "v_mfma_f32_16x16x32_bf16 v[24:27], a[68:71], a[88:91], v[24:27]\n"
        "global_load_lds_dwordx4 v254, s[20:21] offset:0 sc1\n"
        "ds_read_b128 a[120:123], v168 offset:40960\n"
        "v_mfma_f32_16x16x32_bf16 v[28:31], a[68:71], a[92:95], v[28:31]\n"
        "ds_read_b128 a[124:127], v168 offset:41984\n"
        "s_waitcnt lgkmcnt(8)\n"
        "v_mfma_f32_16x16x32_bf16 v[32:35], a[72:75], a[80:83], v[32:35]\n"
        "ds_read_b64_tr_b16 a[100:101], v175 offset:40960\n"
        "s_waitcnt lgkmcnt(8)\n"
        "v_mfma_f32_16x16x32_bf16 v[36:39], a[72:75], a[84:87], v[36:39]\n"
        "ds_read_b64_tr_b16 a[102:103], v175 offset:45056\n"
        "s_waitcnt lgkmcnt(8)\n"
        "v_mfma_f32_16x16x32_bf16 v[40:43], a[72:75], a[88:91], v[40:43]\n"
        "global_load_lds_dwordx4 v254, s[20:21] offset:1024 sc1\n"
        "ds_read_b64_tr_b16 a[104:105], v176 offset:40960\n"
        "v_mfma_f32_16x16x32_bf16 v[44:47], a[72:75], a[92:95], v[44:47]\n"
        "ds_read_b64_tr_b16 a[106:107], v176 offset:45056\n"
        "s_waitcnt lgkmcnt(8)\n"
        "v_mfma_f32_16x16x32_bf16 v[48:51], a[76:79], a[80:83], v[48:51]\n"
        "ds_read_b64_tr_b16 a[108:109], v177 offset:40960\n"
        "s_waitcnt lgkmcnt(8)\n"
        "v_mfma_f32_16x16x32_bf16 v[52:55], a[76:79], a[84:87], v[52:55]\n"
        "ds_read_b64_tr_b16 a[110:111], v177 offset:45056\n"
        "s_waitcnt lgkmcnt(8)\n"
        "v_mfma_f32_16x16x32_bf16 v[56:59], a[76:79], a[88:91], v[56:59]\n"
        "global_load_lds_dwordx4 v254, s[20:21] offset:2048 sc1\n"
        "s_waitcnt lgkmcnt(7)\n"
        "v_mfma_f32_16x16x32_bf16 v[60:63], a[76:79], a[92:95], v[60:63]\n"
        "s_waitcnt lgkmcnt(6)\n"
        "v_mfma_f32_16x16x32_bf16 v[0:3], a[96:99], a[112:115], v[0:3]\n"
        "s_waitcnt lgkmcnt(5)\n"
        "v_mfma_f32_16x16x32_bf16 v[4:7], a[96:99], a[116:119], v[4:7]\n"
        "s_waitcnt lgkmcnt(4)\n"
        "v_mfma_f32_16x16x32_bf16 v[8:11], a[96:99], a[120:123], v[8:11]\n"
        "global_load_lds_dwordx4 v254, s[20:21] offset:3072 sc1\n"
        "s_add_u32 m0, s73, 0x1c000\n"
        "s_waitcnt lgkmcnt(3)\n"
        "v_mfma_f32_16x16x32_bf16 v[12:15], a[96:99], a[124:127], v[12:15]\n"
        "s_add_u32 s20, s20, 0x4000\n"
        "s_addc_u32 s21, s21, 0\n"
        "s_waitcnt lgkmcnt(2)\n"
        "v_mfma_f32_16x16x32_bf16 v[16:19], a[100:103], a[112:115], v[16:19]\n"
        "v_mfma_f32_16x16x32_bf16 v[20:23], a[100:103], a[116:119], v[20:23]\n"
        "s_waitcnt vmcnt(24) lgkmcnt(0)\n"
        "s_barrier\n"
        "v_mfma_f32_16x16x32_bf16 v[24:27], a[100:103], a[120:123], v[24:27]\n"
        "global_load_lds_dwordx4 v160, s[22:23] offset:0\n"
        "ds_read_b64_tr_b16 a[128:129], v178 offset:0\n"
        "v_mfma_f32_16x16x32_bf16 v[28:31], a[100:103], a[124:127], v[28:31]\n"
        "ds_read_b64_tr_b16 a[130:131], v178 offset:4096\n"
        "v_mfma_f32_16x16x32_bf16 v[32:35], a[104:107], a[112:115], v[32:35]\n"
        "ds_read_b128 a[144:147], v166 offset:0\n"
        "v_mfma_f32_16x16x32_bf16 v[36:39], a[104:107], a[116:119], v[36:39]\n"
        "ds_read_b128 a[148:151], v166 offset:1024\n"
        "v_mfma_f32_16x16x32_bf16 v[40:43], a[104:107], a[120:123], v[40:43]\n"
        "ds_read_b128 a[152:155], v169 offset:0\n"
        "global_load_lds_dwordx4 v161, s[22:23] offset:1024\n"
        "v_mfma_f32_16x16x32_bf16 v[44:47], a[104:107], a[124:127], v[44:47]\n"
        "ds_read_b128 a[156:159], v169 offset:1024\n"
        "v_mfma_f32_16x16x32_bf16 v[48:51], a[108:111], a[112:115], v[48:51]\n"
        "ds_read_b64_tr_b16 a[132:133], v179 offset:0\n"
        "v_mfma_f32_16x16x32_bf16 v[52:55], a[108:111], a[116:119], v[52:55]\n"
        "ds_read_b64_tr_b16 a[134:135], v179 offset:4096\n"
        "v_mfma_f32_16x16x32_bf16 v[56:59], a[108:111], a[120:123], v[56:59]\n"
        "global_load_lds_dwordx4 v162, s[22:23] offset:2048\n"
        "v_mfma_f32_16x16x32_bf16 v[60:63], a[108:111], a[124:127], v[60:63]\n"
        "ds_read_b64_tr_b16 a[136:137], v180 offset:0\n"
        "s_waitcnt lgkmcnt(6)\n"
        "v_mfma_f32_16x16x32_bf16 v[0:3], a[128:131], a[144:147], v[0:3]\n"
        "ds_read_b64_tr_b16 a[138:139], v180 offset:4096\n"
        "s_waitcnt lgkmcnt(6)\n"
        "v_mfma_f32_16x16x32_bf16 v[4:7], a[128:131], a[148:151], v[4:7]\n"
        "ds_read_b64_tr_b16 a[140:141], v181 offset:0\n"
        "s_waitcnt lgkmcnt(6)\n"
        "v_mfma_f32_16x16x32_bf16 v[8:11], a[128:131], a[152:155], v[8:11]\n"
        "ds_read_b64_tr_b16 a[142:143], v181 offset:4096\n"
        "s_waitcnt lgkmcnt(6)\n"
        "v_mfma_f32_16x16x32_bf16 v[12:15], a[128:131], a[156:159], v[12:15]\n"
        "global_load_lds_dwordx4 v163, s[22:23] offset:3072\n"
        "ds_read_b64_tr_b16 a[64:65], v178 offset:8192\n"
        "ds_read_b64_tr_b16 a[66:67], v178 offset:12288\n"
        "s_add_u32 s22, s22, 0x20000\n"
        "s_addc_u32 s23, s23, 0\n"
        "s_waitcnt lgkmcnt(6)\n"
        "v_mfma_f32_16x16x32_bf16 v[16:19], a[132:135], a[144:147], v[16:19]\n"
        "ds_read_b128 a[80:83], v166 offset:8192\n"
        "s_add_u32 m0, s73, 0x18000\n"
        "v_mfma_f32_16x16x32_bf16 v[20:23], a[132:135], a[148:151], v[20:23]\n"
        "ds_read_b128 a[84:87], v166 offset:9216\n"
        "v_mfma_f32_16x16x32_bf16 v[24:27], a[132:135], a[152:155], v[24:27]\n"
        "global_load_lds_dwordx4 v254, s[20:21] offset:0 sc1\n"
        "ds_read_b128 a[88:91], v169 offset:8192\n"
        "v_mfma_f32_16x16x32_bf16 v[28:31], a[132:135], a[156:159], v[28:31]\n"
        "ds_read_b128 a[92:95], v169 offset:9216\n"
        "s_waitcnt lgkmcnt(8)\n"
        "v_mfma_f32_16x16x32_bf16 v[32:35], a[136:139], a[144:147], v[32:35]\n"
        "ds_read_b64_tr_b16 a[68:69], v179 offset:8192\n"
        "s_waitcnt lgkmcnt(8)\n"
        "v_mfma_f32_16x16x32_bf16 v[36:39], a[136:139], a[148:151], v[36:39]\n"
        "ds_read_b64_tr_b16 a[70:71], v179 offset:12288\n"
        "s_waitcnt lgkmcnt(8)\n"
        "v_mfma_f32_16x16x32_bf16 v[40:43], a[136:139], a[152:155], v[40:43]\n"
        "global_load_lds_dwordx4 v254, s[20:21] offset:1024 sc1\n"
        "ds_read_b64_tr_b16 a[72:73], v180 offset:8192\n"
        "v_mfma_f32_16x16x32_bf16 v[44:47], a[136:139], a[156:159], v[44:47]\n"
        "ds_read_b64_tr_b16 a[74:75], v180 offset:12288\n"
        "s_waitcnt lgkmcnt(8)\n"
        "v_mfma_f32_16x16x32_bf16 v[48:51], a[140:143], a[144:147], v[48:51]\n"
        "ds_read_b64_tr_b16 a[76:77], v181 offset:8192\n"
        "s_waitcnt lgkmcnt(8)\n"
        "v_mfma_f32_16x16x32_bf16 v[52:55], a[140:143], a[148:151], v[52:55]\n"
        "ds_read_b64_tr_b16 a[78:79], v181 offset:12288\n"
        "s_waitcnt lgkmcnt(8)\n"
        "v_mfma_f32_16x16x32_bf16 v[56:59], a[140:143], a[152:155], v[56:59]\n"
        "global_load_lds_dwordx4 v254, s[20:21] offset:2048 sc1\n"
        "s_waitcnt lgkmcnt(7)\n"
        "v_mfma_f32_16x16x32_bf16 v[60:63], a[140:143], a[156:159], v[60:63]\n"
        "s_waitcnt lgkmcnt(6)\n"
        "v_mfma_f32_16x16x32_bf16 v[0:3], a[64:67], a[80:83], v[0:3]\n"
        "s_waitcnt lgkmcnt(5)\n"
        "v_mfma_f32_16x16x32_bf16 v[4:7], a[64:67], a[84:87], v[4:7]\n"
        "s_waitcnt lgkmcnt(4)\n"
        "v_mfma_f32_16x16x32_bf16 v[8:11], a[64:67], a[88:91], v[8:11]\n"
        "global_load_lds_dwordx4 v254, s[20:21] offset:3072 sc1\n"
        "s_add_u32 m0, s73, 0x24000\n"
        "s_waitcnt lgkmcnt(3)\n"
        "v_mfma_f32_16x16x32_bf16 v[12:15], a[64:67], a[92:95], v[12:15]\n"
        "s_add_u32 s20, s20, 0x4000\n"
        "s_addc_u32 s21, s21, 0\n"
        "s_waitcnt lgkmcnt(2)\n"
        "v_mfma_f32_16x16x32_bf16 v[16:19], a[68:71], a[80:83], v[16:19]\n"
        "v_mfma_f32_16x16x32_bf16 v[20:23], a[68:71], a[84:87], v[20:23]\n"
        "s_waitcnt vmcnt(24) lgkmcnt(0)\n"
        "s_barrier\n"
        "v_mfma_f32_16x16x32_bf16 v[24:27], a[68:71], a[88:91], v[24:27]\n"
        "global_load_lds_dwordx4 v160, s[22:23] offset:0\n"
        "ds_read_b64_tr_b16 a[96:97], v170 offset:0\n"
        "v_mfma_f32_16x16x32_bf16 v[28:31], a[68:71], a[92:95], v[28:31]\n"
        "ds_read_b64_tr_b16 a[98:99], v170 offset:4096\n"
        "v_mfma_f32_16x16x32_bf16 v[32:35], a[72:75], a[80:83], v[32:35]\n"
        "ds_read_b128 a[112:115], v164 offset:0\n"
        "v_mfma_f32_16x16x32_bf16 v[36:39], a[72:75], a[84:87], v[36:39]\n"
        "ds_read_b128 a[116:119], v164 offset:1024\n"
        "v_mfma_f32_16x16x32_bf16 v[40:43], a[72:75], a[88:91], v[40:43]\n"
        "ds_read_b128 a[120:123], v167 offset:0\n"
        "global_load_lds_dwordx4 v161, s[22:23] offset:1024\n"
        "v_mfma_f32_16x16x32_bf16 v[44:47], a[72:75], a[92:95], v[44:47]\n"
        "ds_read_b128 a[124:127], v167 offset:1024\n"
        "v_mfma_f32_16x16x32_bf16 v[48:51], a[76:79], a[80:83], v[48:51]\n"
        "ds_read_b64_tr_b16 a[100:101], v171 offset:0\n"
        "v_mfma_f32_16x16x32_bf16 v[52:55], a[76:79], a[84:87], v[52:55]\n"
        "ds_read_b64_tr_b16 a[102:103], v171 offset:4096\n"
        "v_mfma_f32_16x16x32_bf16 v[56:59], a[76:79], a[88:91], v[56:59]\n"
        "global_load_lds_dwordx4 v162, s[22:23] offset:2048\n"
        "v_mfma_f32_16x16x32_bf16 v[60:63], a[76:79], a[92:95], v[60:63]\n"
        "ds_read_b64_tr_b16 a[104:105], v172 offset:0\n"
        "s_waitcnt lgkmcnt(6)\n"
        "v_mfma_f32_16x16x32_bf16 v[0:3], a[96:99], a[112:115], v[0:3]\n"
        "ds_read_b64_tr_b16 a[106:107], v172 offset:4096\n"
        "s_waitcnt lgkmcnt(6)\n"
        "v_mfma_f32_16x16x32_bf16 v[4:7], a[96:99], a[116:119], v[4:7]\n"
        "ds_read_b64_tr_b16 a[108:109], v173 offset:0\n"
        "s_waitcnt lgkmcnt(6)\n"
        "v_mfma_f32_16x16x32_bf16 v[8:11], a[96:99], a[120:123], v[8:11]\n"
        "ds_read_b64_tr_b16 a[110:111], v173 offset:4096\n"
        "s_waitcnt lgkmcnt(6)\n"
        "v_mfma_f32_16x16x32_bf16 v[12:15], a[96:99], a[124:127], v[12:15]\n"
        "global_load_lds_dwordx4 v163, s[22:23] offset:3072\n"
        "ds_read_b64_tr_b16 a[128:129], v170 offset:8192\n"
        "ds_read_b64_tr_b16 a[130:131], v170 offset:12288\n"
        "s_add_u32 s22, s22, 0x20000\n"
        "s_addc_u32 s23, s23, 0\n"
        "s_waitcnt lgkmcnt(6)\n"
        "v_mfma_f32_16x16x32_bf16 v[16:19], a[100:103], a[112:115], v[16:19]\n"
        "ds_read_b128 a[144:147], v164 offset:8192\n"
        "s_add_u32 m0, s73, 0x20000\n"
        "v_mfma_f32_16x16x32_bf16 v[20:23], a[100:103], a[116:119], v[20:23]\n"
        "ds_read_b128 a[148:151], v164 offset:9216\n"
        "v_mfma_f32_16x16x32_bf16 v[24:27], a[100:103], a[120:123], v[24:27]\n"
        "global_load_lds_dwordx4 v254, s[20:21] offset:0 sc1\n"
        "ds_read_b128 a[152:155], v167 offset:8192\n"
        "v_mfma_f32_16x16x32_bf16 v[28:31], a[100:103], a[124:127], v[28:31]\n"
        "ds_read_b128 a[156:159], v167 offset:9216\n"
        "s_waitcnt lgkmcnt(8)\n"
        "v_mfma_f32_16x16x32_bf16 v[32:35], a[104:107], a[112:115], v[32:35]\n"
        "ds_read_b64_tr_b16 a[132:133], v171 offset:8192\n"
        "s_waitcnt lgkmcnt(8)\n"
        "v_mfma_f32_16x16x32_bf16 v[36:39], a[104:107], a[116:119], v[36:39]\n"
        "ds_read_b64_tr_b16 a[134:135], v171 offset:12288\n"
        "s_waitcnt lgkmcnt(8)\n"
        "v_mfma_f32_16x16x32_bf16 v[40:43], a[104:107], a[120:123], v[40:43]\n"
        "global_load_lds_dwordx4 v254, s[20:21] offset:1024 sc1\n"
        "ds_read_b64_tr_b16 a[136:137], v172 offset:8192\n"
        "v_mfma_f32_16x16x32_bf16 v[44:47], a[104:107], a[124:127], v[44:47]\n"
        "ds_read_b64_tr_b16 a[138:139], v172 offset:12288\n"
        "s_waitcnt lgkmcnt(8)\n"
        "v_mfma_f32_16x16x32_bf16 v[48:51], a[108:111], a[112:115], v[48:51]\n"
        "ds_read_b64_tr_b16 a[140:141], v173 offset:8192\n"
        "s_waitcnt lgkmcnt(8)\n"
        "v_mfma_f32_16x16x32_bf16 v[52:55], a[108:111], a[116:119], v[52:55]\n"
        "ds_read_b64_tr_b16 a[142:143], v173 offset:12288\n"
        "s_waitcnt lgkmcnt(8)\n"
        "v_mfma_f32_16x16x32_bf16 v[56:59], a[108:111], a[120:123], v[56:59]\n"
        "global_load_lds_dwordx4 v254, s[20:21] offset:2048 sc1\n"
        "s_waitcnt lgkmcnt(7)\n"
        "v_mfma_f32_16x16x32_bf16 v[60:63], a[108:111], a[124:127], v[60:63]\n"
        "s_waitcnt lgkmcnt(6)\n"
        "v_mfma_f32_16x16x32_bf16 v[0:3], a[128:131], a[144:147], v[0:3]\n"
        "s_waitcnt lgkmcnt(5)\n"
        "v_mfma_f32_16x16x32_bf16 v[4:7], a[128:131], a[148:151], v[4:7]\n"
        "s_waitcnt lgkmcnt(4)\n"
        "v_mfma_f32_16x16x32_bf16 v[8:11], a[128:131], a[152:155], v[8:11]\n"
        "global_load_lds_dwordx4 v254, s[20:21] offset:3072 sc1\n"
        "s_add_u32 m0, s73, 0x4000\n"
        "s_waitcnt lgkmcnt(3)\n"
        "v_mfma_f32_16x16x32_bf16 v[12:15], a[128:131], a[156:159], v[12:15]\n"
        "s_add_u32 s20, s20, 0x4000\n"
        "s_addc_u32 s21, s21, 0\n"
        "s_waitcnt lgkmcnt(2)\n"
        "v_mfma_f32_16x16x32_bf16 v[16:19], a[132:135], a[144:147], v[16:19]\n"
        "v_mfma_f32_16x16x32_bf16 v[20:23], a[132:135], a[148:151], v[20:23]\n"
        "s_waitcnt vmcnt(24) lgkmcnt(0)\n"
        "s_barrier\n"
        "v_mfma_f32_16x16x32_bf16 v[24:27], a[132:135], a[152:155], v[24:27]\n"
        "global_load_lds_dwordx4 v160, s[22:23] offset:0\n"
        "ds_read_b64_tr_b16 a[64:65], v170 offset:32768\n"
        "v_mfma_f32_16x16x32_bf16 v[28:31], a[132:135], a[156:159], v[28:31]\n"
        "ds_read_b64_tr_b16 a[66:67], v170 offset:36864\n"
        "v_mfma_f32_16x16x32_bf16 v[32:35], a[136:139], a[144:147], v[32:35]\n"
        "ds_read_b128 a[80:83], v164 offset:32768\n"
        "v_mfma_f32_16x16x32_bf16 v[36:39], a[136:139], a[148:151], v[36:39]\n"
        "ds_read_b128 a[84:87], v164 offset:33792\n"
        "v_mfma_f32_16x16x32_bf16 v[40:43], a[136:139], a[152:155], v[40:43]\n"
        "ds_read_b128 a[88:91], v167 offset:32768\n"
        "global_load_lds_dwordx4 v161, s[22:23] offset:1024\n"
        "v_mfma_f32_16x16x32_bf16 v[44:47], a[136:139], a[156:159], v[44:47]\n"
        "ds_read_b128 a[92:95], v167 offset:33792\n"
        "v_mfma_f32_16x16x32_bf16 v[48:51], a[140:143], a[144:147], v[48:51]\n"
        "ds_read_b64_tr_b16 a[68:69], v171 offset:32768\n"
        "v_mfma_f32_16x16x32_bf16 v[52:55], a[140:143], a[148:151], v[52:55]\n"
        "ds_read_b64_tr_b16 a[70:71], v171 offset:36864\n"
        "v_mfma_f32_16x16x32_bf16 v[56:59], a[140:143], a[152:155], v[56:59]\n"
        "global_load_lds_dwordx4 v162, s[22:23] offset:2048\n"
        "v_mfma_f32_16x16x32_bf16 v[60:63], a[140:143], a[156:159], v[60:63]\n"
        "ds_read_b64_tr_b16 a[72:73], v172 offset:32768\n"
        "s_waitcnt lgkmcnt(6)\n"
        "v_mfma_f32_16x16x32_bf16 v[0:3], a[64:67], a[80:83], v[0:3]\n"
        "ds_read_b64_tr_b16 a[74:75], v172 offset:36864\n"
        "s_waitcnt lgkmcnt(6)\n"
        "v_mfma_f32_16x16x32_bf16 v[4:7], a[64:67], a[84:87], v[4:7]\n"
        "ds_read_b64_tr_b16 a[76:77], v173 offset:32768\n"
        "s_waitcnt lgkmcnt(6)\n"
        "v_mfma_f32_16x16x32_bf16 v[8:11], a[64:67], a[88:91], v[8:11]\n"
        "ds_read_b64_tr_b16 a[78:79], v173 offset:36864\n"
        "s_waitcnt lgkmcnt(6)\n"
        "v_mfma_f32_16x16x32_bf16 v[12:15], a[64:67], a[92:95], v[12:15]\n"
        "global_load_lds_dwordx4 v163, s[22:23] offset:3072\n"
        "ds_read_b64_tr_b16 a[96:97], v170 offset:40960\n"
        "ds_read_b64_tr_b16 a[98:99], v170 offset:45056\n"
        "s_add_u32 s22, s22, 0x20000\n"
        "s_addc_u32 s23, s23, 0\n"
        "s_waitcnt lgkmcnt(6)\n"
        "v_mfma_f32_16x16x32_bf16 v[16:19], a[68:71], a[80:83], v[16:19]\n"
        "ds_read_b128 a[112:115], v164 offset:40960\n"
        "s_add_u32 m0, s73, 0x0\n"
        "v_mfma_f32_16x16x32_bf16 v[20:23], a[68:71], a[84:87], v[20:23]\n"
        "ds_read_b128 a[116:119], v164 offset:41984\n"
        "v_mfma_f32_16x16x32_bf16 v[24:27], a[68:71], a[88:91], v[24:27]\n"
        "global_load_lds_dwordx4 v254, s[20:21] offset:0 sc1\n"
        "ds_read_b128 a[120:123], v167 offset:40960\n"
        "v_mfma_f32_16x16x32_bf16 v[28:31], a[68:71], a[92:95], v[28:31]\n"
        "ds_read_b128 a[124:127], v167 offset:41984\n"
        "s_waitcnt lgkmcnt(8)\n"
        "v_mfma_f32_16x16x32_bf16 v[32:35], a[72:75], a[80:83], v[32:35]\n"
        "ds_read_b64_tr_b16 a[100:101], v171 offset:40960\n"
        "s_waitcnt lgkmcnt(8)\n"
        "v_mfma_f32_16x16x32_bf16 v[36:39], a[72:75], a[84:87], v[36:39]\n"
        "ds_read_b64_tr_b16 a[102:103], v171 offset:45056\n"
        "s_waitcnt lgkmcnt(8)\n"
        "v_mfma_f32_16x16x32_bf16 v[40:43], a[72:75], a[88:91], v[40:43]\n"
        "global_load_lds_dwordx4 v254, s[20:21] offset:1024 sc1\n"
        "ds_read_b64_tr_b16 a[104:105], v172 offset:40960\n"
        "v_mfma_f32_16x16x32_bf16 v[44:47], a[72:75], a[92:95], v[44:47]\n"
        "ds_read_b64_tr_b16 a[106:107], v172 offset:45056\n"
        "s_waitcnt lgkmcnt(8)\n"
        "v_mfma_f32_16x16x32_bf16 v[48:51], a[76:79], a[80:83], v[48:51]\n"
        "ds_read_b64_tr_b16 a[108:109], v173 offset:40960\n"
        "s_waitcnt lgkmcnt(8)\n"
        "v_mfma_f32_16x16x32_bf16 v[52:55], a[76:79], a[84:87], v[52:55]\n"
        "ds_read_b64_tr_b16 a[110:111], v173 offset:45056\n"
        "s_waitcnt lgkmcnt(8)\n"
        "v_mfma_f32_16x16x32_bf16 v[56:59], a[76:79], a[88:91], v[56:59]\n"
        "global_load_lds_dwordx4 v254, s[20:21] offset:2048 sc1\n"
        "s_waitcnt lgkmcnt(7)\n"
        "v_mfma_f32_16x16x32_bf16 v[60:63], a[76:79], a[92:95], v[60:63]\n"
        "s_waitcnt lgkmcnt(6)\n"
        "v_mfma_f32_16x16x32_bf16 v[0:3], a[96:99], a[112:115], v[0:3]\n"
        "s_waitcnt lgkmcnt(5)\n"
        "v_mfma_f32_16x16x32_bf16 v[4:7], a[96:99], a[116:119], v[4:7]\n"
        "s_waitcnt lgkmcnt(4)\n"
        "v_mfma_f32_16x16x32_bf16 v[8:11], a[96:99], a[120:123], v[8:11]\n"
        "global_load_lds_dwordx4 v254, s[20:21] offset:3072 sc1\n"
        "s_add_u32 m0, s73, 0xc000\n"
        "s_waitcnt lgkmcnt(3)\n"
        "v_mfma_f32_16x16x32_bf16 v[12:15], a[96:99], a[124:127], v[12:15]\n"
        "s_add_u32 s20, s20, 0x4000\n"
        "s_addc_u32 s21, s21, 0\n"
        "s_waitcnt lgkmcnt(2)\n"
        "v_mfma_f32_16x16x32_bf16 v[16:19], a[100:103], a[112:115], v[16:19]\n"
        "v_mfma_f32_16x16x32_bf16 v[20:23], a[100:103], a[116:119], v[20:23]\n"
        "s_waitcnt vmcnt(24) lgkmcnt(0)\n"
        "s_barrier\n"
        "v_mfma_f32_16x16x32_bf16 v[24:27], a[100:103], a[120:123], v[24:27]\n"
        "global_load_lds_dwordx4 v160, s[22:23] offset:0\n"
        "ds_read_b64_tr_b16 a[128:129], v174 offset:0\n"
        "v_mfma_f32_16x16x32_bf16 v[28:31], a[100:103], a[124:127], v[28:31]\n"
        "ds_read_b64_tr_b16 a[130:131], v174 offset:4096\n"
        "v_mfma_f32_16x16x32_bf16 v[32:35], a[104:107], a[112:115], v[32:35]\n"
        "ds_read_b128 a[144:147], v165 offset:0\n"
        "v_mfma_f32_16x16x32_bf16 v[36:39], a[104:107], a[116:119], v[36:39]\n"
        "ds_read_b128 a[148:151], v165 offset:1024\n"
        "v_mfma_f32_16x16x32_bf16 v[40:43], a[104:107], a[120:123], v[40:43]\n"
        "global_load_dwordx2 v[184:185], v182, s[48:49] sc1\n"
        "ds_read_b128 a[152:155], v168 offset:0\n"
        "v_mfma_f32_16x16x32_bf16 v[44:47], a[104:107], a[124:127], v[44:47]\n"
        "global_load_lds_dwordx4 v161, s[22:23] offset:1024\n"
        "ds_read_b128 a[156:159], v168 offset:1024\n"
        "v_mfma_f32_16x16x32_bf16 v[48:51], a[108:111], a[112:115], v[48:51]\n"
        "ds_read_b64_tr_b16 a[132:133], v175 offset:0\n"
        "v_mfma_f32_16x16x32_bf16 v[52:55], a[108:111], a[116:119], v[52:55]\n"
        "ds_read_b64_tr_b16 a[134:135], v175 offset:4096\n"
        "v_mfma_f32_16x16x32_bf16 v[56:59], a[108:111], a[120:123], v[56:59]\n"
        "ds_read_b64_tr_b16 a[136:137], v176 offset:0\n"
        "v_mfma_f32_16x16x32_bf16 v[60:63], a[108:111], a[124:127], v[60:63]\n"
        "global_load_lds_dwordx4 v162, s[22:23] offset:2048\n"
        "ds_read_b64_tr_b16 a[138:139], v176 offset:4096\n"
        "s_waitcnt lgkmcnt(7)\n"
        "v_mfma_f32_16x16x32_bf16 v[0:3], a[128:131], a[144:147], v[0:3]\n"
        "ds_read_b64_tr_b16 a[140:141], v177 offset:0\n"
        "s_waitcnt lgkmcnt(7)\n"
        "v_mfma_f32_16x16x32_bf16 v[4:7], a[128:131], a[148:151], v[4:7]\n"
        "ds_read_b64_tr_b16 a[142:143], v177 offset:4096\n"
        "s_waitcnt lgkmcnt(7)\n"
        "v_mfma_f32_16x16x32_bf16 v[8:11], a[128:131], a[152:155], v[8:11]\n"
        "ds_read_b64_tr_b16 a[64:65], v174 offset:8192\n"
        "s_waitcnt lgkmcnt(7)\n"
        "v_mfma_f32_16x16x32_bf16 v[12:15], a[128:131], a[156:159], v[12:15]\n"
        "global_load_lds_dwordx4 v163, s[22:23] offset:3072\n"
        "ds_read_b64_tr_b16 a[66:67], v174 offset:12288\n"
        "ds_read_b128 a[80:83], v165 offset:8192\n"
        "s_add_u32 m0, s73, 0x8000\n"
        "s_waitcnt lgkmcnt(7)\n"
        "v_mfma_f32_16x16x32_bf16 v[16:19], a[132:135], a[144:147], v[16:19]\n"
        "s_add_u32 s22, s22, 0x20000\n"
        "s_addc_u32 s23, s23, 0\n"
        "v_mfma_f32_16x16x32_bf16 v[20:23], a[132:135], a[148:151], v[20:23]\n"
        "ds_read_b128 a[84:87], v165 offset:9216\n"
        "s_waitcnt lgkmcnt(7)\n"
        "v_mfma_f32_16x16x32_bf16 v[24:27], a[132:135], a[152:155], v[24:27]\n"
        "global_load_lds_dwordx4 v254, s[20:21] offset:0 sc1\n"
        "v_mfma_f32_16x16x32_bf16 v[28:31], a[132:135], a[156:159], v[28:31]\n"
        "ds_read_b128 a[88:91], v168 offset:8192\n"
        "s_waitcnt lgkmcnt(6)\n"
        "v_mfma_f32_16x16x32_bf16 v[32:35], a[136:139], a[144:147], v[32:35]\n"
        "ds_read_b128 a[92:95], v168 offset:9216\n"
        "s_waitcnt lgkmcnt(6)\n"
        "v_mfma_f32_16x16x32_bf16 v[36:39], a[136:139], a[148:151], v[36:39]\n"
        "ds_read_b64_tr_b16 a[68:69], v175 offset:8192\n"
        "v_mfma_f32_16x16x32_bf16 v[40:43], a[136:139], a[152:155], v[40:43]\n"
        "global_load_lds_dwordx4 v254, s[20:21] offset:1024 sc1\n"
        "v_mfma_f32_16x16x32_bf16 v[44:47], a[136:139], a[156:159], v[44:47]\n"
        "ds_read_b64_tr_b16 a[70:71], v175 offset:12288\n"
        "s_waitcnt lgkmcnt(5)\n"
        "v_mfma_f32_16x16x32_bf16 v[48:51], a[140:143], a[144:147], v[48:51]\n"
        "ds_read_b64_tr_b16 a[72:73], v176 offset:8192\n"
        "v_mfma_f32_16x16x32_bf16 v[52:55], a[140:143], a[148:151], v[52:55]\n"
        "ds_read_b64_tr_b16 a[74:75], v176 offset:12288\n"
        "s_waitcnt lgkmcnt(6)\n"
        "v_mfma_f32_16x16x32_bf16 v[56:59], a[140:143], a[152:155], v[56:59]\n"
        "ds_read_b64_tr_b16 a[76:77], v177 offset:8192\n"
        "global_load_lds_dwordx4 v254, s[20:21] offset:2048 sc1\n"
        "v_mfma_f32_16x16x32_bf16 v[60:63], a[140:143], a[156:159], v[60:63]\n"
        "ds_read_b64_tr_b16 a[78:79], v177 offset:12288\n"
        "s_waitcnt lgkmcnt(7)\n"
        "v_mfma_f32_16x16x32_bf16 v[0:3], a[64:67], a[80:83], v[0:3]\n"
        "s_waitcnt lgkmcnt(6)\n"
        "v_mfma_f32_16x16x32_bf16 v[4:7], a[64:67], a[84:87], v[4:7]\n"
        "v_mfma_f32_16x16x32_bf16 v[8:11], a[64:67], a[88:91], v[8:11]\n"
        "global_load_lds_dwordx4 v254, s[20:21] offset:3072 sc1\n"
        "s_add_u32 m0, s73, 0x14000\n"
        "v_mfma_f32_16x16x32_bf16 v[12:15], a[64:67], a[92:95], v[12:15]\n"
        "s_add_u32 s20, s20, 0x4000\n"
        "s_addc_u32 s21, s21, 0\n"
        "s_waitcnt lgkmcnt(4)\n"
        "v_mfma_f32_16x16x32_bf16 v[16:19], a[68:71], a[80:83], v[16:19]\n"
        "s_waitcnt lgkmcnt(3)\n"
        "v_mfma_f32_16x16x32_bf16 v[20:23], a[68:71], a[84:87], v[20:23]\n"
        "s_waitcnt lgkmcnt(2)\n"
        "v_mfma_f32_16x16x32_bf16 v[24:27], a[68:71], a[88:91], v[24:27]\n"
        "s_waitcnt lgkmcnt(1)\n"
        "v_mfma_f32_16x16x32_bf16 v[28:31], a[68:71], a[92:95], v[28:31]\n"
        "s_waitcnt vmcnt(25) lgkmcnt(0)\n"
        "s_barrier\n"
        "v_mfma_f32_16x16x32_bf16 v[32:35], a[72:75], a[80:83], v[32:35]\n"
        "global_load_lds_dwordx4 v160, s[22:23] offset:0\n"
        "ds_read_b64_tr_b16 a[96:97], v174 offset:32768\n"
        "v_mfma_f32_16x16x32_bf16 v[36:39], a[72:75], a[84:87], v[36:39]\n"
        "ds_read_b64_tr_b16 a[98:99], v174 offset:36864\n"
        "v_mfma_f32_16x16x32_bf16 v[40:43], a[72:75], a[88:91], v[40:43]\n"
        "ds_read_b128 a[112:115], v165 offset:32768\n"
        "v_mfma_f32_16x16x32_bf16 v[44:47], a[72:75], a[92:95], v[44:47]\n"
        "ds_read_b128 a[116:119], v165 offset:33792\n"
        "v_mfma_f32_16x16x32_bf16 v[48:51], a[76:79], a[80:83], v[48:51]\n"
        "ds_read_b128 a[120:123], v168 offset:32768\n"
        "global_load_lds_dwordx4 v161, s[22:23] offset:1024\n"
        "v_mfma_f32_16x16x32_bf16 v[52:55], a[76:79], a[84:87], v[52:55]\n"
        "ds_read_b128 a[124:127], v168 offset:33792\n"
        "v_mfma_f32_16x16x32_bf16 v[56:59], a[76:79], a[88:91], v[56:59]\n"
        "ds_read_b64_tr_b16 a[100:101], v175 offset:32768\n"
        "v_mfma_f32_16x16x32_bf16 v[60:63], a[76:79], a[92:95], v[60:63]\n"
        "ds_read_b64_tr_b16 a[102:103], v175 offset:36864\n"
        "global_load_lds_dwordx4 v162, s[22:23] offset:2048\n"
        "ds_read_b64_tr_b16 a[104:105], v176 offset:32768\n"
        "ds_read_b64_tr_b16 a[106:107], v176 offset:36864\n"
        "s_waitcnt lgkmcnt(7)\n"
        "v_mfma_f32_16x16x32_bf16 v[0:3], a[96:99], a[112:115], v[0:3]\n"
        "ds_read_b64_tr_b16 a[108:109], v177 offset:32768\n"
        "s_waitcnt lgkmcnt(7)\n"
        "v_mfma_f32_16x16x32_bf16 v[4:7], a[96:99], a[116:119], v[4:7]\n"
        "ds_read_b64_tr_b16 a[110:111], v177 offset:36864\n"
        "s_waitcnt lgkmcnt(7)\n"
        "v_mfma_f32_16x16x32_bf16 v[8:11], a[96:99], a[120:123], v[8:11]\n"
        "global_load_lds_dwordx4 v163, s[22:23] offset:3072\n"
        "s_add_u32 m0, s73, 0x10000\n"
        "s_waitcnt lgkmcnt(6)\n"
        "v_mfma_f32_16x16x32_bf16 v[12:15], a[96:99], a[124:127], v[12:15]\n"
        "ds_read_b64_tr_b16 a[128:129], v174 offset:40960\n"
        "ds_read_b64_tr_b16 a[130:131], v174 offset:45056\n"
        "ds_read_b128 a[144:147], v165 offset:40960\n"
        "s_waitcnt lgkmcnt(7)\n"
        "v_mfma_f32_16x16x32_bf16 v[16:19], a[100:103], a[112:115], v[16:19]\n"
        "s_add_u32 s22, s22, 0x20000\n"
        "s_addc_u32 s23, s23, 0\n"
        "v_mfma_f32_16x16x32_bf16 v[20:23], a[100:103], a[116:119], v[20:23]\n"
        "global_load_lds_dwordx4 v254, s[20:21] offset:0 sc1\n"
        "ds_read_b128 a[148:151], v165 offset:41984\n"
        "v_mfma_f32_16x16x32_bf16 v[24:27], a[100:103], a[120:123], v[24:27]\n"
        "ds_read_b128 a[152:155], v168 offset:40960\n"
        "s_waitcnt lgkmcnt(7)\n"
        "v_mfma_f32_16x16x32_bf16 v[28:31], a[100:103], a[124:127], v[28:31]\n"
        "ds_read_b128 a[156:159], v168 offset:41984\n"
        "s_waitcnt lgkmcnt(7)\n"
        "v_mfma_f32_16x16x32_bf16 v[32:35], a[104:107], a[112:115], v[32:35]\n"
        "ds_read_b64_tr_b16 a[132:133], v175 offset:40960\n"
        "s_waitcnt lgkmcnt(7)\n"
        "v_mfma_f32_16x16x32_bf16 v[36:39], a[104:107], a[116:119], v[36:39]\n"
        "global_load_lds_dwordx4 v254, s[20:21] offset:1024 sc1\n"
        "ds_read_b64_tr_b16 a[134:135], v175 offset:45056\n"
        "v_mfma_f32_16x16x32_bf16 v[40:43], a[104:107], a[120:123], v[40:43]\n"
        "ds_read_b64_tr_b16 a[136:137], v176 offset:40960\n"
        "s_waitcnt lgkmcnt(8)\n"
        "v_mfma_f32_16x16x32_bf16 v[44:47], a[104:107], a[124:127], v[44:47]\n"
        "ds_read_b64_tr_b16 a[138:139], v176 offset:45056\n"
        "s_waitcnt lgkmcnt(7)\n"
        "v_mfma_f32_16x16x32_bf16 v[48:51], a[108:111], a[112:115], v[48:51]\n"
        "ds_read_b64_tr_b16 a[140:141], v177 offset:40960\n"
        "v_mfma_f32_16x16x32_bf16 v[52:55], a[108:111], a[116:119], v[52:55]\n"
        "ds_read_b64_tr_b16 a[142:143], v177 offset:45056\n"
        "global_load_lds_dwordx4 v254, s[20:21] offset:2048 sc1\n"
        "v_mfma_f32_16x16x32_bf16 v[56:59], a[108:111], a[120:123], v[56:59]\n"
        "s_waitcnt lgkmcnt(7)\n"
        "v_mfma_f32_16x16x32_bf16 v[60:63], a[108:111], a[124:127], v[60:63]\n"
        "s_waitcnt lgkmcnt(6)\n"
        "v_mfma_f32_16x16x32_bf16 v[0:3], a[128:131], a[144:147], v[0:3]\n"
        "s_waitcnt lgkmcnt(5)\n"
        "v_mfma_f32_16x16x32_bf16 v[4:7], a[128:131], a[148:151], v[4:7]\n"
        "global_load_lds_dwordx4 v254, s[20:21] offset:3072 sc1\n"
        "s_add_u32 m0, s73, 0x1c000\n"
        "v_mfma_f32_16x16x32_bf16 v[8:11], a[128:131], a[152:155], v[8:11]\n"
        "s_add_u32 s20, s20, 0x4000\n"
        "s_addc_u32 s21, s21, 0\n"
        "s_waitcnt lgkmcnt(3)\n"
        "v_mfma_f32_16x16x32_bf16 v[12:15], a[128:131], a[156:159], v[12:15]\n"
        "s_waitcnt lgkmcnt(2)\n"
        "v_mfma_f32_16x16x32_bf16 v[16:19], a[132:135], a[144:147], v[16:19]\n"
        "v_mfma_f32_16x16x32_bf16 v[20:23], a[132:135], a[148:151], v[20:23]\n"
        "s_waitcnt vmcnt(25) lgkmcnt(0)\n"
        "s_barrier\n"
        "v_mfma_f32_16x16x32_bf16 v[24:27], a[132:135], a[152:155], v[24:27]\n"
        "global_load_lds_dwordx4 v160, s[22:23] offset:0\n"
        "ds_read_b64_tr_b16 a[64:65], v178 offset:0\n"
        "v_mfma_f32_16x16x32_bf16 v[28:31], a[132:135], a[156:159], v[28:31]\n"
        "ds_read_b64_tr_b16 a[66:67], v178 offset:4096\n"
        "v_mfma_f32_16x16x32_bf16 v[32:35], a[136:139], a[144:147], v[32:35]\n"
        "ds_read_b128 a[80:83], v166 offset:0\n"
        "v_mfma_f32_16x16x32_bf16 v[36:39], a[136:139], a[148:151], v[36:39]\n"
        "ds_read_b128 a[84:87], v166 offset:1024\n"
        "v_mfma_f32_16x16x32_bf16 v[40:43], a[136:139], a[152:155], v[40:43]\n"
        "ds_read_b128 a[88:91], v169 offset:0\n"
        "global_load_lds_dwordx4 v161, s[22:23] offset:1024\n"
        "v_mfma_f32_16x16x32_bf16 v[44:47], a[136:139], a[156:159], v[44:47]\n"
        "ds_read_b128 a[92:95], v169 offset:1024\n"
        "v_mfma_f32_16x16x32_bf16 v[48:51], a[140:143], a[144:147], v[48:51]\n"
        "ds_read_b64_tr_b16 a[68:69], v179 offset:0\n"
        "v_mfma_f32_16x16x32_bf16 v[52:55], a[140:143], a[148:151], v[52:55]\n"
        "ds_read_b64_tr_b16 a[70:71], v179 offset:4096\n"
        "v_mfma_f32_16x16x32_bf16 v[56:59], a[140:143], a[152:155], v[56:59]\n"
        "global_load_lds_dwordx4 v162, s[22:23] offset:2048\n"
        "v_mfma_f32_16x16x32_bf16 v[60:63], a[140:143], a[156:159], v[60:63]\n"
        "ds_read_b64_tr_b16 a[72:73], v180 offset:0\n"
        "s_waitcnt lgkmcnt(6)\n"
        "v_mfma_f32_16x16x32_bf16 v[0:3], a[64:67], a[80:83], v[0:3]\n"
        "ds_read_b64_tr_b16 a[74:75], v180 offset:4096\n"
        "s_waitcnt lgkmcnt(6)\n"
        "v_mfma_f32_16x16x32_bf16 v[4:7], a[64:67], a[84:87], v[4:7]\n"
        "ds_read_b64_tr_b16 a[76:77], v181 offset:0\n"
        "s_waitcnt lgkmcnt(6)\n"
        "v_mfma_f32_16x16x32_bf16 v[8:11], a[64:67], a[88:91], v[8:11]\n"
        "ds_read_b64_tr_b16 a[78:79], v181 offset:4096\n"
        "s_waitcnt lgkmcnt(6)\n"
        "v_mfma_f32_16x16x32_bf16 v[12:15], a[64:67], a[92:95], v[12:15]\n"
        "global_load_lds_dwordx4 v163, s[22:23] offset:3072\n"
        "ds_read_b64_tr_b16 a[96:97], v178 offset:8192\n"
        "ds_read_b64_tr_b16 a[98:99], v178 offset:12288\n"
        "s_add_u32 s22, s22, 0x20000\n"
        "s_addc_u32 s23, s23, 0\n"
        "s_waitcnt lgkmcnt(6)\n"
        "v_mfma_f32_16x16x32_bf16 v[16:19], a[68:71], a[80:83], v[16:19]\n"
        "ds_read_b128 a[112:115], v166 offset:8192\n"
        "s_add_u32 m0, s73, 0x18000\n"
        "v_mfma_f32_16x16x32_bf16 v[20:23], a[68:71], a[84:87], v[20:23]\n"
        "ds_read_b128 a[116:119], v166 offset:9216\n"
        "v_mfma_f32_16x16x32_bf16 v[24:27], a[68:71], a[88:91], v[24:27]\n"
        "global_load_lds_dwordx4 v254, s[20:21] offset:0 sc1\n"
        "ds_read_b128 a[120:123], v169 offset:8192\n"
        "v_mfma_f32_16x16x32_bf16 v[28:31], a[68:71], a[92:95], v[28:31]\n"
        "ds_read_b128 a[124:127], v169 offset:9216\n"
        "s_waitcnt lgkmcnt(8)\n"
        "v_mfma_f32_16x16x32_bf16 v[32:35], a[72:75], a[80:83], v[32:35]\n"
        "ds_read_b64_tr_b16 a[100:101], v179 offset:8192\n"
        "s_waitcnt lgkmcnt(8)\n"
        "v_mfma_f32_16x16x32_bf16 v[36:39], a[72:75], a[84:87], v[36:39]\n"
        "ds_read_b64_tr_b16 a[102:103], v179 offset:12288\n"
        "s_waitcnt lgkmcnt(8)\n"
        "v_mfma_f32_16x16x32_bf16 v[40:43], a[72:75], a[88:91], v[40:43]\n"
        "global_load_lds_dwordx4 v254, s[20:21] offset:1024 sc1\n"
        "ds_read_b64_tr_b16 a[104:105], v180 offset:8192\n"
        "v_mfma_f32_16x16x32_bf16 v[44:47], a[72:75], a[92:95], v[44:47]\n"
        "ds_read_b64_tr_b16 a[106:107], v180 offset:12288\n"
        "s_waitcnt lgkmcnt(8)\n"
        "v_mfma_f32_16x16x32_bf16 v[48:51], a[76:79], a[80:83], v[48:51]\n"
        "ds_read_b64_tr_b16 a[108:109], v181 offset:8192\n"
        "s_waitcnt lgkmcnt(8)\n"
        "v_mfma_f32_16x16x32_bf16 v[52:55], a[76:79], a[84:87], v[52:55]\n"
        "ds_read_b64_tr_b16 a[110:111], v181 offset:12288\n"
        "s_waitcnt lgkmcnt(8)\n"
        "v_mfma_f32_16x16x32_bf16 v[56:59], a[76:79], a[88:91], v[56:59]\n"
        "global_load_lds_dwordx4 v254, s[20:21] offset:2048 sc1\n"
        "s_waitcnt lgkmcnt(7)\n"
        "v_mfma_f32_16x16x32_bf16 v[60:63], a[76:79], a[92:95], v[60:63]\n"
        "s_waitcnt lgkmcnt(6)\n"
        "v_mfma_f32_16x16x32_bf16 v[0:3], a[96:99], a[112:115], v[0:3]\n"
        "s_waitcnt lgkmcnt(5)\n"
        "v_mfma_f32_16x16x32_bf16 v[4:7], a[96:99], a[116:119], v[4:7]\n"
        "s_waitcnt lgkmcnt(4)\n"
        "v_mfma_f32_16x16x32_bf16 v[8:11], a[96:99], a[120:123], v[8:11]\n"
        "global_load_lds_dwordx4 v254, s[20:21] offset:3072 sc1\n"
        "s_add_u32 m0, s73, 0x24000\n"
        "s_waitcnt lgkmcnt(3)\n"
        "v_mfma_f32_16x16x32_bf16 v[12:15], a[96:99], a[124:127], v[12:15]\n"
        "s_add_u32 s20, s20, 0x4000\n"
        "s_addc_u32 s21, s21, 0\n"
        "s_waitcnt lgkmcnt(2)\n"
        "v_mfma_f32_16x16x32_bf16 v[16:19], a[100:103], a[112:115], v[16:19]\n"
        "v_mfma_f32_16x16x32_bf16 v[20:23], a[100:103], a[116:119], v[20:23]\n"
        "s_waitcnt vmcnt(25) lgkmcnt(0)\n"
        "s_barrier\n"
        "v_mfma_f32_16x16x32_bf16 v[24:27], a[100:103], a[120:123], v[24:27]\n"
        "global_load_lds_dwordx4 v160, s[22:23] offset:0\n"
        "ds_read_b64_tr_b16 a[128:129], v170 offset:0\n"
        "v_mfma_f32_16x16x32_bf16 v[28:31], a[100:103], a[124:127], v[28:31]\n"
        "ds_read_b64_tr_b16 a[130:131], v170 offset:4096\n"
        "v_mfma_f32_16x16x32_bf16 v[32:35], a[104:107], a[112:115], v[32:35]\n"
        "ds_read_b128 a[144:147], v164 offset:0\n"
        "v_mfma_f32_16x16x32_bf16 v[36:39], a[104:107], a[116:119], v[36:39]\n"
        "ds_read_b128 a[148:151], v164 offset:1024\n"
        "v_mfma_f32_16x16x32_bf16 v[40:43], a[104:107], a[120:123], v[40:43]\n"
        "ds_read_b128 a[152:155], v167 offset:0\n"
        "global_load_lds_dwordx4 v161, s[22:23] offset:1024\n"
        "v_mfma_f32_16x16x32_bf16 v[44:47], a[104:107], a[124:127], v[44:47]\n"
        "ds_read_b128 a[156:159], v167 offset:1024\n"
        "v_mfma_f32_16x16x32_bf16 v[48:51], a[108:111], a[112:115], v[48:51]\n"
        "ds_read_b64_tr_b16 a[132:133], v171 offset:0\n"
        "v_mfma_f32_16x16x32_bf16 v[52:55], a[108:111], a[116:119], v[52:55]\n"
        "ds_read_b64_tr_b16 a[134:135], v171 offset:4096\n"
        "v_mfma_f32_16x16x32_bf16 v[56:59], a[108:111], a[120:123], v[56:59]\n"
        "global_load_lds_dwordx4 v162, s[22:23] offset:2048\n"
        "v_mfma_f32_16x16x32_bf16 v[60:63], a[108:111], a[124:127], v[60:63]\n"
        "ds_read_b64_tr_b16 a[136:137], v172 offset:0\n"
        "s_waitcnt lgkmcnt(6)\n"
        "v_mfma_f32_16x16x32_bf16 v[0:3], a[128:131], a[144:147], v[0:3]\n"
        "ds_read_b64_tr_b16 a[138:139], v172 offset:4096\n"
        "s_waitcnt lgkmcnt(6)\n"
        "v_mfma_f32_16x16x32_bf16 v[4:7], a[128:131], a[148:151], v[4:7]\n"
        "ds_read_b64_tr_b16 a[140:141], v173 offset:0\n"
        "s_waitcnt lgkmcnt(6)\n"
        "v_mfma_f32_16x16x32_bf16 v[8:11], a[128:131], a[152:155], v[8:11]\n"
        "ds_read_b64_tr_b16 a[142:143], v173 offset:4096\n"
        "s_waitcnt lgkmcnt(6)\n"
        "v_mfma_f32_16x16x32_bf16 v[12:15], a[128:131], a[156:159], v[12:15]\n"
        "global_load_lds_dwordx4 v163, s[22:23] offset:3072\n"
        "ds_read_b64_tr_b16 a[64:65], v170 offset:8192\n"
        "ds_read_b64_tr_b16 a[66:67], v170 offset:12288\n"
        "s_add_u32 s22, s22, 0x20000\n"
        "s_addc_u32 s23, s23, 0\n"
        "s_waitcnt lgkmcnt(6)\n"
        "v_mfma_f32_16x16x32_bf16 v[16:19], a[132:135], a[144:147], v[16:19]\n"
        "ds_read_b128 a[80:83], v164 offset:8192\n"
        "s_add_u32 m0, s73, 0x20000\n"
        "v_mfma_f32_16x16x32_bf16 v[20:23], a[132:135], a[148:151], v[20:23]\n"
        "ds_read_b128 a[84:87], v164 offset:9216\n"
        "v_mfma_f32_16x16x32_bf16 v[24:27], a[132:135], a[152:155], v[24:27]\n"
        "global_load_lds_dwordx4 v254, s[20:21] offset:0 sc1\n"
        "ds_read_b128 a[88:91], v167 offset:8192\n"
        "v_mfma_f32_16x16x32_bf16 v[28:31], a[132:135], a[156:159], v[28:31]\n"
        "ds_read_b128 a[92:95], v167 offset:9216\n"
        "s_waitcnt lgkmcnt(8)\n"
        "v_mfma_f32_16x16x32_bf16 v[32:35], a[136:139], a[144:147], v[32:35]\n"
        "ds_read_b64_tr_b16 a[68:69], v171 offset:8192\n"
        "s_waitcnt lgkmcnt(8)\n"
        "v_mfma_f32_16x16x32_bf16 v[36:39], a[136:139], a[148:151], v[36:39]\n"
        "ds_read_b64_tr_b16 a[70:71], v171 offset:12288\n"
        "s_waitcnt lgkmcnt(8)\n"
        "v_mfma_f32_16x16x32_bf16 v[40:43], a[136:139], a[152:155], v[40:43]\n"
        "global_load_lds_dwordx4 v254, s[20:21] offset:1024 sc1\n"
        "ds_read_b64_tr_b16 a[72:73], v172 offset:8192\n"
        "v_mfma_f32_16x16x32_bf16 v[44:47], a[136:139], a[156:159], v[44:47]\n"
        "ds_read_b64_tr_b16 a[74:75], v172 offset:12288\n"
        "s_waitcnt lgkmcnt(8)\n"
        "v_mfma_f32_16x16x32_bf16 v[48:51], a[140:143], a[144:147], v[48:51]\n"
        "ds_read_b64_tr_b16 a[76:77], v173 offset:8192\n"
        "s_waitcnt lgkmcnt(8)\n"
        "v_mfma_f32_16x16x32_bf16 v[52:55], a[140:143], a[148:151], v[52:55]\n"
        "ds_read_b64_tr_b16 a[78:79], v173 offset:12288\n"
        "s_waitcnt lgkmcnt(8)\n"
        "v_mfma_f32_16x16x32_bf16 v[56:59], a[140:143], a[152:155], v[56:59]\n"
        "global_load_lds_dwordx4 v254, s[20:21] offset:2048 sc1\n"
        "s_waitcnt lgkmcnt(7)\n"
        "v_mfma_f32_16x16x32_bf16 v[60:63], a[140:143], a[156:159], v[60:63]\n"
        "s_waitcnt lgkmcnt(6)\n"
        "v_mfma_f32_16x16x32_bf16 v[0:3], a[64:67], a[80:83], v[0:3]\n"
        "s_waitcnt lgkmcnt(5)\n"
        "v_mfma_f32_16x16x32_bf16 v[4:7], a[64:67], a[84:87], v[4:7]\n"
        "s_waitcnt lgkmcnt(4)\n"
        "v_mfma_f32_16x16x32_bf16 v[8:11], a[64:67], a[88:91], v[8:11]\n"
        "global_load_lds_dwordx4 v254, s[20:21] offset:3072 sc1\n"
        "s_add_u32 m0, s73, 0x4000\n"
        "s_waitcnt lgkmcnt(3)\n"
        "v_mfma_f32_16x16x32_bf16 v[12:15], a[64:67], a[92:95], v[12:15]\n"
        "s_add_u32 s20, s20, 0x4000\n"
        "s_addc_u32 s21, s21, 0\n"
        "s_waitcnt lgkmcnt(2)\n"
        "v_mfma_f32_16x16x32_bf16 v[16:19], a[68:71], a[80:83], v[16:19]\n"
        "v_mfma_f32_16x16x32_bf16 v[20:23], a[68:71], a[84:87], v[20:23]\n"
        "s_waitcnt vmcnt(24) lgkmcnt(0)\n"
        "s_barrier\n"
        "v_mfma_f32_16x16x32_bf16 v[24:27], a[68:71], a[88:91], v[24:27]\n"
        "global_load_lds_dwordx4 v160, s[22:23] offset:0\n"
        "ds_read_b64_tr_b16 a[96:97], v170 offset:32768\n"
        "v_mfma_f32_16x16x32_bf16 v[28:31], a[68:71], a[92:95], v[28:31]\n"
        "ds_read_b64_tr_b16 a[98:99], v170 offset:36864\n"
        "v_mfma_f32_16x16x32_bf16 v[32:35], a[72:75], a[80:83], v[32:35]\n"
        "ds_read_b128 a[112:115], v164 offset:32768\n"
        "v_mfma_f32_16x16x32_bf16 v[36:39], a[72:75], a[84:87], v[36:39]\n"
        "ds_read_b128 a[116:119], v164 offset:33792\n"
        "v_mfma_f32_16x16x32_bf16 v[40:43], a[72:75], a[88:91], v[40:43]\n"
        "ds_read_b128 a[120:123], v167 offset:32768\n"
        "global_load_lds_dwordx4 v161, s[22:23] offset:1024\n"
        "v_mfma_f32_16x16x32_bf16 v[44:47], a[72:75], a[92:95], v[44:47]\n"
        "ds_read_b128 a[124:127], v167 offset:33792\n"
        "v_mfma_f32_16x16x32_bf16 v[48:51], a[76:79], a[80:83], v[48:51]\n"
        "ds_read_b64_tr_b16 a[100:101], v171 offset:32768\n"
        "v_mfma_f32_16x16x32_bf16 v[52:55], a[76:79], a[84:87], v[52:55]\n"
        "ds_read_b64_tr_b16 a[102:103], v171 offset:36864\n"
        "v_mfma_f32_16x16x32_bf16 v[56:59], a[76:79], a[88:91], v[56:59]\n"
        "global_load_lds_dwordx4 v162, s[22:23] offset:2048\n"
        "v_mfma_f32_16x16x32_bf16 v[60:63], a[76:79], a[92:95], v[60:63]\n"
        "ds_read_b64_tr_b16 a[104:105], v172 offset:32768\n"
        "s_waitcnt lgkmcnt(6)\n"
        "v_mfma_f32_16x16x32_bf16 v[0:3], a[96:99], a[112:115], v[0:3]\n"
        "ds_read_b64_tr_b16 a[106:107], v172 offset:36864\n"
        "s_waitcnt lgkmcnt(6)\n"
        "v_mfma_f32_16x16x32_bf16 v[4:7], a[96:99], a[116:119], v[4:7]\n"
        "ds_read_b64_tr_b16 a[108:109], v173 offset:32768\n"
        "s_waitcnt lgkmcnt(6)\n"
        "v_mfma_f32_16x16x32_bf16 v[8:11], a[96:99], a[120:123], v[8:11]\n"
        "ds_read_b64_tr_b16 a[110:111], v173 offset:36864\n"
        "s_waitcnt lgkmcnt(6)\n"
        "v_mfma_f32_16x16x32_bf16 v[12:15], a[96:99], a[124:127], v[12:15]\n"
        "global_load_lds_dwordx4 v163, s[22:23] offset:3072\n"
        "ds_read_b64_tr_b16 a[128:129], v170 offset:40960\n"
        "ds_read_b64_tr_b16 a[130:131], v170 offset:45056\n"
        "s_add_u32 s22, s22, 0x20000\n"
        "s_addc_u32 s23, s23, 0\n"
        "s_waitcnt lgkmcnt(6)\n"
        "v_mfma_f32_16x16x32_bf16 v[16:19], a[100:103], a[112:115], v[16:19]\n"
        "ds_read_b128 a[144:147], v164 offset:40960\n"
        "s_add_u32 m0, s73, 0x0\n"
        "v_mfma_f32_16x16x32_bf16 v[20:23], a[100:103], a[116:119], v[20:23]\n"
        "ds_read_b128 a[148:151], v164 offset:41984\n"
        "v_mfma_f32_16x16x32_bf16 v[24:27], a[100:103], a[120:123], v[24:27]\n"
        "global_load_lds_dwordx4 v254, s[20:21] offset:0 sc1\n"
        "ds_read_b128 a[152:155], v167 offset:40960\n"
        "v_mfma_f32_16x16x32_bf16 v[28:31], a[100:103], a[124:127], v[28:31]\n"
        "ds_read_b128 a[156:159], v167 offset:41984\n"
        "s_waitcnt lgkmcnt(8)\n"
        "v_mfma_f32_16x16x32_bf16 v[32:35], a[104:107], a[112:115], v[32:35]\n"
        "ds_read_b64_tr_b16 a[132:133], v171 offset:40960\n"
        "s_waitcnt lgkmcnt(8)\n"
        "v_mfma_f32_16x16x32_bf16 v[36:39], a[104:107], a[116:119], v[36:39]\n"
        "ds_read_b64_tr_b16 a[134:135], v171 offset:45056\n"
        "s_waitcnt lgkmcnt(8)\n"
        "v_mfma_f32_16x16x32_bf16 v[40:43], a[104:107], a[120:123], v[40:43]\n"
        "global_load_lds_dwordx4 v254, s[20:21] offset:1024 sc1\n"
        "ds_read_b64_tr_b16 a[136:137], v172 offset:40960\n"
        "v_mfma_f32_16x16x32_bf16 v[44:47], a[104:107], a[124:127], v[44:47]\n"
        "ds_read_b64_tr_b16 a[138:139], v172 offset:45056\n"
        "s_waitcnt lgkmcnt(8)\n"
        "v_mfma_f32_16x16x32_bf16 v[48:51], a[108:111], a[112:115], v[48:51]\n"
        "ds_read_b64_tr_b16 a[140:141], v173 offset:40960\n"
        "s_waitcnt lgkmcnt(8)\n"
        "v_mfma_f32_16x16x32_bf16 v[52:55], a[108:111], a[116:119], v[52:55]\n"
        "ds_read_b64_tr_b16 a[142:143], v173 offset:45056\n"
        "s_waitcnt lgkmcnt(8)\n"
        "v_mfma_f32_16x16x32_bf16 v[56:59], a[108:111], a[120:123], v[56:59]\n"
        "global_load_lds_dwordx4 v254, s[20:21] offset:2048 sc1\n"
        "v_cmp_ne_u64 vcc, v[184:185], v[250:251]\n"
        "v_mfma_f32_16x16x32_bf16 v[60:63], a[108:111], a[124:127], v[60:63]\n"
        "s_cbranch_vccnz LPZZ_%=\n"
        "LBZZ_%=:\n"
        "v_lshrrev_b32 v182, 1, v254\n"
        "v_add_u32 v182, s14, v182\n"
        "v_mfma_f32_16x16x32_bf16 v[0:3], a[128:131], a[144:147], v[0:3]\n"
        "s_xor_b32 s14, s14, 0x200\n"
        "v_mov_b32 v183, s14\n"
        "s_waitcnt lgkmcnt(5)\n"
        "v_mfma_f32_16x16x32_bf16 v[4:7], a[128:131], a[148:151], v[4:7]\n"
        "s_waitcnt lgkmcnt(4)\n"
        "v_mfma_f32_16x16x32_bf16 v[8:11], a[128:131], a[152:155], v[8:11]\n"
        "global_load_lds_dwordx4 v254, s[20:21] offset:3072 sc1\n"
        "s_add_u32 m0, s73, 0xc000\n"
        "s_waitcnt lgkmcnt(3)\n"
        "v_mfma_f32_16x16x32_bf16 v[12:15], a[128:131], a[156:159], v[12:15]\n"
        "s_add_u32 s20, s20, 0x4000\n"
        "s_addc_u32 s21, s21, 0\n"
        "s_waitcnt lgkmcnt(2)\n"
        "v_mfma_f32_16x16x32_bf16 v[16:19], a[132:135], a[144:147], v[16:19]\n"
        "v_mfma_f32_16x16x32_bf16 v[20:23], a[132:135], a[148:151], v[20:23]\n"
        "s_waitcnt vmcnt(24) lgkmcnt(0)\n"
        "s_barrier\n"
        "v_mfma_f32_16x16x32_bf16 v[24:27], a[132:135], a[152:155], v[24:27]\n"
        "global_load_lds_dwordx4 v160, s[22:23] offset:0\n"
        "ds_read_b64_tr_b16 a[64:65], v174 offset:0\n"
        "v_mfma_f32_16x16x32_bf16 v[28:31], a[132:135], a[156:159], v[28:31]\n"
        "ds_read_b64_tr_b16 a[66:67], v174 offset:4096\n"
        "v_mfma_f32_16x16x32_bf16 v[32:35], a[136:139], a[144:147], v[32:35]\n"
        "ds_read_b128 a[80:83], v165 offset:0\n"
        "v_mfma_f32_16x16x32_bf16 v[36:39], a[136:139], a[148:151], v[36:39]\n"
        "ds_read_b128 a[84:87], v165 offset:1024\n"
        "v_mfma_f32_16x16x32_bf16 v[40:43], a[136:139], a[152:155], v[40:43]\n"
        "ds_read_b128 a[88:91], v168 offset:0\n"
        "global_load_lds_dwordx4 v161, s[22:23] offset:1024\n"
        "v_mfma_f32_16x16x32_bf16 v[44:47], a[136:139], a[156:159], v[44:47]\n"
        "ds_read_b128 a[92:95], v168 offset:1024\n"
        "v_mfma_f32_16x16x32_bf16 v[48:51], a[140:143], a[144:147], v[48:51]\n"
        "ds_read_b64_tr_b16 a[68:69], v175 offset:0\n"
        "v_mfma_f32_16x16x32_bf16 v[52:55], a[140:143], a[148:151], v[52:55]\n"
        "ds_read_b64_tr_b16 a[70:71], v175 offset:4096\n"
        "v_mfma_f32_16x16x32_bf16 v[56:59], a[140:143], a[152:155], v[56:59]\n"
        "global_load_lds_dwordx4 v162, s[22:23] offset:2048\n"
        "v_mfma_f32_16x16x32_bf16 v[60:63], a[140:143], a[156:159], v[60:63]\n"
        "ds_read_b64_tr_b16 a[72:73], v176 offset:0\n"
        "s_waitcnt lgkmcnt(6)\n"
        "v_mfma_f32_16x16x32_bf16 v[0:3], a[64:67], a[80:83], v[0:3]\n"
        "ds_read_b64_tr_b16 a[74:75], v176 offset:4096\n"
        "s_waitcnt lgkmcnt(6)\n"
        "v_mfma_f32_16x16x32_bf16 v[4:7], a[64:67], a[84:87], v[4:7]\n"
        "ds_read_b64_tr_b16 a[76:77], v177 offset:0\n"
        "s_waitcnt lgkmcnt(6)\n"
        "v_mfma_f32_16x16x32_bf16 v[8:11], a[64:67], a[88:91], v[8:11]\n"
        "ds_read_b64_tr_b16 a[78:79], v177 offset:4096\n"
        "s_waitcnt lgkmcnt(6)\n"
        "v_mfma_f32_16x16x32_bf16 v[12:15], a[64:67], a[92:95], v[12:15]\n"
        "global_load_lds_dwordx4 v163, s[22:23] offset:3072\n"
        "ds_read_b64_tr_b16 a[96:97], v174 offset:8192\n"
        "ds_read_b64_tr_b16 a[98:99], v174 offset:12288\n"
        "s_add_u32 m0, s73, 0x8000\n"
        "s_waitcnt lgkmcnt(6)\n"
        "v_mfma_f32_16x16x32_bf16 v[16:19], a[68:71], a[80:83], v[16:19]\n"
        "ds_read_b128 a[112:115], v165 offset:8192\n"
        "v_mfma_f32_16x16x32_bf16 v[20:23], a[68:71], a[84:87], v[20:23]\n"
        "ds_read_b128 a[116:119], v165 offset:9216\n"
        "v_mfma_f32_16x16x32_bf16 v[24:27], a[68:71], a[88:91], v[24:27]\n"
        "global_load_lds_dwordx4 v254, s[20:21] offset:0 sc1\n"
        "ds_read_b128 a[120:123], v168 offset:8192\n"
        "v_mfma_f32_16x16x32_bf16 v[28:31], a[68:71], a[92:95], v[28:31]\n"
        "ds_read_b128 a[124:127], v168 offset:9216\n"
        "s_waitcnt lgkmcnt(8)\n"
        "v_mfma_f32_16x16x32_bf16 v[32:35], a[72:75], a[80:83], v[32:35]\n"
        "ds_read_b64_tr_b16 a[100:101], v175 offset:8192\n"
        "s_waitcnt lgkmcnt(8)\n"
        "v_mfma_f32_16x16x32_bf16 v[36:39], a[72:75], a[84:87], v[36:39]\n"
        "ds_read_b64_tr_b16 a[102:103], v175 offset:12288\n"
        "s_waitcnt lgkmcnt(8)\n"
        "v_mfma_f32_16x16x32_bf16 v[40:43], a[72:75], a[88:91], v[40:43]\n"
        "global_load_lds_dwordx4 v254, s[20:21] offset:1024 sc1\n"
        "ds_read_b64_tr_b16 a[104:105], v176 offset:8192\n"
        "v_mfma_f32_16x16x32_bf16 v[44:47], a[72:75], a[92:95], v[44:47]\n"
        "ds_read_b64_tr_b16 a[106:107], v176 offset:12288\n"
        "s_waitcnt lgkmcnt(8)\n"
        "v_mfma_f32_16x16x32_bf16 v[48:51], a[76:79], a[80:83], v[48:51]\n"
        "ds_read_b64_tr_b16 a[108:109], v177 offset:8192\n"
        "s_waitcnt lgkmcnt(8)\n"
        "v_mfma_f32_16x16x32_bf16 v[52:55], a[76:79], a[84:87], v[52:55]\n"
        "ds_read_b64_tr_b16 a[110:111], v177 offset:12288\n"
        "s_waitcnt lgkmcnt(8)\n"
        "v_mfma_f32_16x16x32_bf16 v[56:59], a[76:79], a[88:91], v[56:59]\n"
        "global_load_lds_dwordx4 v254, s[20:21] offset:2048 sc1\n"
        "s_waitcnt lgkmcnt(7)\n"
        "v_mfma_f32_16x16x32_bf16 v[60:63], a[76:79], a[92:95], v[60:63]\n"
        "s_waitcnt lgkmcnt(6)\n"
        "v_mfma_f32_16x16x32_bf16 v[0:3], a[96:99], a[112:115], v[0:3]\n"
        "s_waitcnt lgkmcnt(5)\n"
        "v_mfma_f32_16x16x32_bf16 v[4:7], a[96:99], a[116:119], v[4:7]\n"
        "s_waitcnt lgkmcnt(4)\n"
        "v_mfma_f32_16x16x32_bf16 v[8:11], a[96:99], a[120:123], v[8:11]\n"
        "global_load_lds_dwordx4 v254, s[20:21] offset:3072 sc1\n"
        "s_add_u32 m0, s73, 0x10000\n"
        "s_waitcnt lgkmcnt(3)\n"
        "v_mfma_f32_16x16x32_bf16 v[12:15], a[96:99], a[124:127], v[12:15]\n"
        "s_waitcnt lgkmcnt(2)\n"
        "v_mfma_f32_16x16x32_bf16 v[16:19], a[100:103], a[112:115], v[16:19]\n"
        "s_waitcnt lgkmcnt(1)\n"
        "v_mfma_f32_16x16x32_bf16 v[20:23], a[100:103], a[116:119], v[20:23]\n"
        "s_waitcnt vmcnt(24) lgkmcnt(0)\n"
        "s_barrier\n"
        "v_mfma_f32_16x16x32_bf16 v[24:27], a[100:103], a[120:123], v[24:27]\n"
        "global_load_dwordx2 v[96:97], v186, s[30:31] offset:0\n"
        "ds_read_b64_tr_b16 a[0:1], v174 offset:32768\n"
        "v_mfma_f32_16x16x32_bf16 v[28:31], a[100:103], a[124:127], v[28:31]\n"
        "global_load_dwordx2 v[98:99], v186, s[30:31] offset:32\n"
        "ds_read_b64_tr_b16 a[2:3], v174 offset:36864\n"
        "v_mfma_f32_16x16x32_bf16 v[32:35], a[104:107], a[112:115], v[32:35]\n"
        "global_load_dwordx2 v[100:101], v186, s[30:31] offset:64\n"
        "ds_read_b128 a[144:147], v165 offset:32768\n"
        "v_mfma_f32_16x16x32_bf16 v[36:39], a[104:107], a[116:119], v[36:39]\n"
        "global_load_dwordx2 v[102:103], v186, s[30:31] offset:96\n"
        "ds_read_b128 a[148:151], v165 offset:33792\n"
        "v_mfma_f32_16x16x32_bf16 v[48:51], a[108:111], a[112:115], v[48:51]\n"
        "global_load_dwordx2 v[104:105], v187, s[30:31] offset:0\n"
        "ds_read_b64_tr_b16 a[4:5], v175 offset:32768\n"
        "v_mfma_f32_16x16x32_bf16 v[52:55], a[108:111], a[116:119], v[52:55]\n"
        "global_load_dwordx2 v[106:107], v187, s[30:31] offset:32\n"
        "ds_read_b64_tr_b16 a[6:7], v175 offset:36864\n"
        "v_mfma_f32_16x16x32_bf16 v[40:43], a[104:107], a[120:123], v[40:43]\n"
        "global_load_dwordx2 v[108:109], v187, s[30:31] offset:64\n"
        "ds_read_b64_tr_b16 a[8:9], v176 offset:32768\n"
        "v_mfma_f32_16x16x32_bf16 v[44:47], a[104:107], a[124:127], v[44:47]\n"
        "global_load_dwordx2 v[110:111], v187, s[30:31] offset:96\n"
        "s_add_u32 s30, s38, 0x3800000\n"
        "s_addc_u32 s31, s39, 0\n"
        "v_mfma_f32_16x16x32_bf16 v[56:59], a[108:111], a[120:123], v[56:59]\n"
        "global_load_dword v128, v190, s[30:31] offset:0 sc1\n"
        "ds_read_b64_tr_b16 a[10:11], v176 offset:36864\n"
        "v_mfma_f32_16x16x32_bf16 v[60:63], a[108:111], a[124:127], v[60:63]\n"
        "global_load_dword v129, v190, s[30:31] offset:2048 sc1\n"
        "ds_read_b64_tr_b16 a[12:13], v177 offset:32768\n"
        "v_and_b32 v190, 1, v233\n"
        "s_waitcnt lgkmcnt(6)\n"
        "v_lshl_add_u32 v191, v190, 5, v191\n"
        "v_lshrrev_b32 v190, 1, v233\n"
        "v_lshl_add_u32 v191, v190, 4, v191\n"
        "v_and_b32 v190, 0x30, v254\n"
        "v_add_u32 v188, v188, v190\n"
        "v_mfma_f32_16x16x32_bf16 v[0:3], a[0:3], a[144:147], v[0:3]\n"
        "global_load_lds_dwordx4 v188, s[58:59] offset:0 sc1\n"
        "s_add_u32 m0, s73, 0x10400\n"
        "s_waitcnt lgkmcnt(4)\n"
        "v_mfma_f32_16x16x32_bf16 v[4:7], a[0:3], a[148:151], v[4:7]\n"
        "s_add_u32 s58, s58, 0x10000\n"
        "s_addc_u32 s59, s59, 0\n"
        "s_waitcnt lgkmcnt(3)\n"
        "v_mfma_f32_16x16x32_bf16 v[16:19], a[4:7], a[144:147], v[16:19]\n"
        "ds_read_b64_tr_b16 a[14:15], v177 offset:36864\n"
        "v_mfma_f32_16x16x32_bf16 v[20:23], a[4:7], a[148:151], v[20:23]\n"
        "s_waitcnt lgkmcnt(2)\n"
        "global_load_lds_dwordx4 v188, s[58:59] offset:0 sc1\n"
        "s_add_u32 m0, s73, 0x10800\n"
        "v_mfma_f32_16x16x32_bf16 v[32:35], a[8:11], a[144:147], v[32:35]\n"
        "s_add_u32 s58, s58, 0x10000\n"
        "s_addc_u32 s59, s59, 0\n"
        "v_mfma_f32_16x16x32_bf16 v[36:39], a[8:11], a[148:151], v[36:39]\n"
        "global_load_lds_dwordx4 v188, s[58:59] offset:0 sc1\n"
        "s_add_u32 s58, s58, 0x10000\n"
        "s_addc_u32 s59, s59, 0\n"
        "s_add_u32 m0, s73, 0x10c00\n"
        "ds_read_b64_tr_b16 a[16:17], v174 offset:40960\n"
        "s_waitcnt vmcnt(29)\n"
        "s_waitcnt lgkmcnt(1)\n"
        "v_mfma_f32_16x16x32_bf16 v[48:51], a[12:15], a[144:147], v[48:51]\n"
        "ds_read_b64_tr_b16 a[18:19], v174 offset:45056\n"
        "s_barrier\n"
        "v_mfma_f32_16x16x32_bf16 v[52:55], a[12:15], a[148:151], v[52:55]\n"
        "ds_read_b128 a[80:83], v165 offset:40960\n"
        "global_load_lds_dwordx4 v188, s[58:59] offset:0 sc1\n"
        "ds_read_b128 a[84:87], v165 offset:41984\n"
        "ds_read_b64_tr_b16 a[20:21], v175 offset:40960\n"
        "ds_read_b64_tr_b16 a[22:23], v175 offset:45056\n"
        "ds_read_b64_tr_b16 a[24:25], v176 offset:40960\n"
        "ds_read_b64_tr_b16 a[26:27], v176 offset:45056\n"
        "ds_read_b64_tr_b16 a[28:29], v177 offset:40960\n"
        "ds_read_b64_tr_b16 a[30:31], v177 offset:45056\n"
        "ds_read_b64_tr_b16 a[32:33], v178 offset:0\n"
        "ds_read_b64_tr_b16 a[34:35], v178 offset:4096\n"
        "ds_read_b128 a[112:115], v166 offset:0\n"
        "ds_read_b128 a[116:119], v166 offset:1024\n"
        "ds_read_b64_tr_b16 a[36:37], v179 offset:0\n"
        "ds_read_b64_tr_b16 a[38:39], v179 offset:4096\n"
        "s_waitcnt vmcnt(22)\n"
        "s_waitcnt lgkmcnt(13)\n"
        "v_mfma_f32_16x16x32_bf16 v[0:3], a[16:19], a[80:83], v[0:3]\n"
        "ds_read_b64_tr_b16 a[40:41], v180 offset:0\n"
        "s_waitcnt lgkmcnt(13)\n"
        "v_mfma_f32_16x16x32_bf16 v[4:7], a[16:19], a[84:87], v[4:7]\n"
        "ds_read_b64_tr_b16 a[42:43], v180 offset:4096\n"
        "s_waitcnt lgkmcnt(12)\n"
        "v_mfma_f32_16x16x32_bf16 v[16:19], a[20:23], a[80:83], v[16:19]\n"
        "ds_read_b64_tr_b16 a[44:45], v181 offset:0\n"
        "s_waitcnt lgkmcnt(12)\n"
        "v_mfma_f32_16x16x32_bf16 v[20:23], a[20:23], a[84:87], v[20:23]\n"
        "ds_read_b64_tr_b16 a[46:47], v181 offset:4096\n"
        "s_waitcnt lgkmcnt(11)\n"
        "v_mfma_f32_16x16x32_bf16 v[32:35], a[24:27], a[80:83], v[32:35]\n"
        "ds_read_b64_tr_b16 a[48:49], v178 offset:8192\n"
        "s_barrier\n"
        "s_waitcnt lgkmcnt(10)\n"
        "v_mfma_f32_16x16x32_bf16 v[36:39], a[24:27], a[84:87], v[36:39]\n"
        "ds_read_b64_tr_b16 a[50:51], v178 offset:12288\n"
        "s_waitcnt lgkmcnt(9)\n"
        "v_mfma_f32_16x16x32_bf16 v[48:51], a[28:31], a[80:83], v[48:51]\n"
        "ds_read_b128 a[144:147], v166 offset:8192\n"
        "s_waitcnt lgkmcnt(8)\n"
        "v_mfma_f32_16x16x32_bf16 v[52:55], a[28:31], a[84:87], v[52:55]\n"
        "ds_read_b64_tr_b16 a[52:53], v179 offset:8192\n"
        "s_waitcnt lgkmcnt(8)\n"
        "v_mfma_f32_16x16x32_bf16 v[0:3], a[32:35], a[112:115], v[0:3]\n"
        "ds_read_b64_tr_b16 a[54:55], v179 offset:12288\n"
        "s_waitcnt lgkmcnt(8)\n"
        "v_mfma_f32_16x16x32_bf16 v[4:7], a[32:35], a[116:119], v[4:7]\n"
        "ds_read_b64_tr_b16 a[56:57], v180 offset:8192\n"
        "s_waitcnt lgkmcnt(8)\n"
        "v_mfma_f32_16x16x32_bf16 v[16:19], a[36:39], a[112:115], v[16:19]\n"
        "ds_read_b64_tr_b16 a[58:59], v180 offset:12288\n"
        "s_waitcnt lgkmcnt(8)\n"
        "v_mfma_f32_16x16x32_bf16 v[20:23], a[36:39], a[116:119], v[20:23]\n"
        "ds_read_b64_tr_b16 a[60:61], v181 offset:8192\n"
        "s_waitcnt lgkmcnt(8)\n"
        "v_mfma_f32_16x16x32_bf16 v[32:35], a[40:43], a[112:115], v[32:35]\n"
        "ds_read_b64_tr_b16 a[62:63], v181 offset:12288\n"
        "v_mfma_f32_16x16x32_bf16 v[36:39], a[40:43], a[116:119], v[36:39]\n"
        "ds_read_b128 a[148:151], v166 offset:9216\n"
        "s_waitcnt lgkmcnt(8)\n"
        "v_mfma_f32_16x16x32_bf16 v[48:51], a[44:47], a[112:115], v[48:51]\n"
        "ds_read_b64_tr_b16 a[192:193], v170 offset:0\n"
        "s_waitcnt vmcnt(14)\n"
        "v_mfma_f32_16x16x32_bf16 v[52:55], a[44:47], a[116:119], v[52:55]\n"
        "ds_read_b64_tr_b16 a[194:195], v170 offset:4096\n"
        "s_waitcnt lgkmcnt(8)\n"
        "v_mfma_f32_16x16x32_bf16 v[0:3], a[48:51], a[144:147], v[0:3]\n"
        "ds_read_b64_tr_b16 a[196:197], v171 offset:0\n"
        "s_waitcnt lgkmcnt(8)\n"
        "v_mfma_f32_16x16x32_bf16 v[16:19], a[52:55], a[144:147], v[16:19]\n"
        "ds_read_b64_tr_b16 a[198:199], v171 offset:4096\n"
        "s_waitcnt lgkmcnt(7)\n"
        "v_mfma_f32_16x16x32_bf16 v[32:35], a[56:59], a[144:147], v[32:35]\n"
        "ds_read_b64_tr_b16 a[200:201], v172 offset:0\n"
        "ds_read_b64_tr_b16 a[202:203], v172 offset:4096\n"
        "ds_read_b64_tr_b16 a[204:205], v173 offset:0\n"
        "s_waitcnt lgkmcnt(8)\n"
        "v_mfma_f32_16x16x32_bf16 v[48:51], a[60:63], a[144:147], v[48:51]\n"
        "ds_read_b64_tr_b16 a[206:207], v173 offset:4096\n"
        "s_waitcnt lgkmcnt(8)\n"
        "v_mfma_f32_16x16x32_bf16 v[4:7], a[48:51], a[148:151], v[4:7]\n"
        "ds_read_b128 a[80:83], v164 offset:0\n"
        "v_mfma_f32_16x16x32_bf16 v[20:23], a[52:55], a[148:151], v[20:23]\n"
        "ds_read_b128 a[84:87], v164 offset:1024\n"
        "s_barrier\n"
        "v_mfma_f32_16x16x32_bf16 v[36:39], a[56:59], a[148:151], v[36:39]\n"
        "ds_read_b64_tr_b16 a[208:209], v170 offset:8192\n"
        "v_mfma_f32_16x16x32_bf16 v[52:55], a[60:63], a[148:151], v[52:55]\n"
        "ds_read_b64_tr_b16 a[210:211], v170 offset:12288\n"
        "ds_read_b64_tr_b16 a[212:213], v171 offset:8192\n"
        "ds_read_b64_tr_b16 a[214:215], v171 offset:12288\n"
        "ds_read_b64_tr_b16 a[216:217], v172 offset:8192\n"
        "ds_read_b64_tr_b16 a[218:219], v172 offset:12288\n"
        "ds_read_b64_tr_b16 a[220:221], v173 offset:8192\n"
        "ds_read_b64_tr_b16 a[222:223], v173 offset:12288\n"
        "ds_read_b128 a[112:115], v164 offset:8192\n"
        "ds_read_b128 a[116:119], v164 offset:9216\n"
        "s_waitcnt lgkmcnt(11)\n"
        "v_mfma_f32_16x16x32_bf16 v[0:3], a[192:195], a[80:83], v[0:3]\n"
        "ds_read_b64_tr_b16 a[224:225], v170 offset:32768\n"
        "v_mfma_f32_16x16x32_bf16 v[16:19], a[196:199], a[80:83], v[16:19]\n"
        "ds_read_b64_tr_b16 a[226:227], v170 offset:36864\n"
        "s_waitcnt lgkmcnt(12)\n"
        "v_mfma_f32_16x16x32_bf16 v[32:35], a[200:203], a[80:83], v[32:35]\n"
        "ds_read_b128 a[144:147], v164 offset:32768\n"
        "v_mfma_f32_16x16x32_bf16 v[48:51], a[204:207], a[80:83], v[48:51]\n"
        "ds_read_b64_tr_b16 a[228:229], v171 offset:32768\n"
        "v_mfma_f32_16x16x32_bf16 v[4:7], a[192:195], a[84:87], v[4:7]\n"
        "ds_read_b64_tr_b16 a[230:231], v171 offset:36864\n"
        "v_mfma_f32_16x16x32_bf16 v[20:23], a[196:199], a[84:87], v[20:23]\n"
        "ds_read_b64_tr_b16 a[232:233], v172 offset:32768\n"
        "s_waitcnt lgkmcnt(9)\n"
        "v_mfma_f32_16x16x32_bf16 v[36:39], a[200:203], a[84:87], v[36:39]\n"
        "ds_read_b64_tr_b16 a[234:235], v172 offset:36864\n"
        "s_waitcnt lgkmcnt(8)\n"
        "v_mfma_f32_16x16x32_bf16 v[52:55], a[204:207], a[84:87], v[52:55]\n"
        "ds_read_b64_tr_b16 a[236:237], v173 offset:32768\n"
        "s_waitcnt lgkmcnt(8)\n"
        "v_mfma_f32_16x16x32_bf16 v[0:3], a[208:211], a[112:115], v[0:3]\n"
        "ds_read_b64_tr_b16 a[238:239], v173 offset:36864\n"
        "v_mfma_f32_16x16x32_bf16 v[16:19], a[212:215], a[112:115], v[16:19]\n"
        "ds_read_b128 a[148:151], v164 offset:33792\n"
        "v_mfma_f32_16x16x32_bf16 v[32:35], a[216:219], a[112:115], v[32:35]\n"
        "ds_read_b64_tr_b16 a[240:241], v170 offset:40960\n"
        "v_lshrrev_b32 v190, 1, v190\n"
        "v_mfma_f32_16x16x32_bf16 v[48:51], a[220:223], a[112:115], v[48:51]\n"
        "ds_read_b64_tr_b16 a[242:243], v170 offset:45056\n"
        "v_add_u32 v189, v189, v190\n"
        "v_mfma_f32_16x16x32_bf16 v[4:7], a[208:211], a[116:119], v[4:7]\n"
        "ds_read_b64_tr_b16 a[244:245], v171 offset:40960\n"
        "v_add_u32 v189, s16, v189\n"
        "v_mfma_f32_16x16x32_bf16 v[20:23], a[212:215], a[116:119], v[20:23]\n"
        "ds_read_b64_tr_b16 a[246:247], v171 offset:45056\n"
        "s_lshl_b32 s16, s13, 15\n"
        "s_add_u32 s16, s16, s72\n"
        "v_mfma_f32_16x16x32_bf16 v[36:39], a[216:219], a[116:119], v[36:39]\n"
        "ds_read_b64_tr_b16 a[248:249], v172 offset:40960\n"
        "s_add_u32 s16, s16, 0x4001000\n"
        "s_waitcnt lgkmcnt(8)\n"
        "v_mfma_f32_16x16x32_bf16 v[52:55], a[220:223], a[116:119], v[52:55]\n"
        "ds_read_b64_tr_b16 a[250:251], v172 offset:45056\n"
        "s_add_u32 s28, s38, s16\n"
        "s_addc_u32 s29, s39, 0\n"
        "v_mfma_f32_16x16x32_bf16 v[0:3], a[224:227], a[144:147], v[0:3]\n"
        "ds_read_b64_tr_b16 a[252:253], v173 offset:40960\n"
        "s_xor_b32 s16, s16, 0x8000\n"
        "s_waitcnt lgkmcnt(8)\n"
        "v_mfma_f32_16x16x32_bf16 v[16:19], a[228:231], a[144:147], v[16:19]\n"
        "ds_read_b64_tr_b16 a[254:255], v173 offset:45056\n"
        "s_add_u32 s30, s38, s16\n"
        "s_addc_u32 s31, s39, 0\n"
        "v_mfma_f32_16x16x32_bf16 v[32:35], a[232:235], a[144:147], v[32:35]\n"
        "ds_read_b128 a[80:83], v164 offset:40960\n"
        "s_waitcnt lgkmcnt(8)\n"
        "v_mfma_f32_16x16x32_bf16 v[48:51], a[236:239], a[144:147], v[48:51]\n"
        "ds_read_b128 a[84:87], v164 offset:41984\n"
        "v_mfma_f32_16x16x32_bf16 v[4:7], a[224:227], a[148:151], v[4:7]\n"
        "ds_read_b128 a[164:167], v168 offset:33792\n"
        "v_mfma_f32_16x16x32_bf16 v[20:23], a[228:231], a[148:151], v[20:23]\n"
        "ds_read_b128 a[172:175], v168 offset:41984\n"
        "v_mfma_f32_16x16x32_bf16 v[36:39], a[232:235], a[148:151], v[36:39]\n"
        "ds_read_b128 a[180:183], v169 offset:1024\n"
        "v_mfma_f32_16x16x32_bf16 v[52:55], a[236:239], a[148:151], v[52:55]\n"
        "ds_read_b128 a[188:191], v169 offset:9216\n"
        "ds_read_b128 a[160:163], v168 offset:32768\n"
        "ds_read_b128 a[168:171], v168 offset:40960\n"
        "ds_read_b128 a[176:179], v169 offset:0\n"
        "ds_read_b128 a[184:187], v169 offset:8192\n"
        "s_waitcnt lgkmcnt(9)\n"
        "v_mfma_f32_16x16x32_bf16 v[0:3], a[240:243], a[80:83], v[0:3]\n"
        "v_mfma_f32_16x16x32_bf16 v[16:19], a[244:247], a[80:83], v[16:19]\n"
        "s_waitcnt lgkmcnt(8)\n"
        "v_mfma_f32_16x16x32_bf16 v[32:35], a[248:251], a[80:83], v[32:35]\n"
        "s_waitcnt lgkmcnt(7)\n"
        "v_mfma_f32_16x16x32_bf16 v[48:51], a[252:255], a[80:83], v[48:51]\n"
        "v_mfma_f32_16x16x32_bf16 v[4:7], a[240:243], a[84:87], v[4:7]\n"
        "v_mfma_f32_16x16x32_bf16 v[20:23], a[244:247], a[84:87], v[20:23]\n"
        "v_mfma_f32_16x16x32_bf16 v[36:39], a[248:251], a[84:87], v[36:39]\n"
        "global_store_dwordx4 v254, v[0:3], s[28:29] offset:-4096 sc1\n"
        "s_waitcnt lgkmcnt(1)\n"
        "s_waitcnt vmcnt(14) lgkmcnt(0)\n"
        "v_lshlrev_b32 v192, 16, v96\n"
        "v_and_b32 v193, 0xffff0000, v96\n"
        "v_mfma_f32_16x16x32_bf16 v[52:55], a[252:255], a[84:87], v[52:55]\n"
        "s_waitcnt vmcnt(13)\n"
        "v_and_b32 v199, 0xffff0000, v99\n"
        "v_lshlrev_b32 v194, 16, v97\n"
        "v_mfma_f32_16x16x32_bf16 v[12:15], a[0:3], a[164:167], v[12:15]\n"
        "s_waitcnt vmcnt(12)\n"
        "v_and_b32 v195, 0xffff0000, v97\n"
        "v_lshlrev_b32 v196, 16, v98\n"
        "v_mfma_f32_16x16x32_bf16 v[28:31], a[4:7], a[164:167], v[28:31]\n"
        "s_waitcnt vmcnt(11)\n"
        "v_and_b32 v207, 0xffff0000, v103\n"
        "v_and_b32 v197, 0xffff0000, v98\n"
        "global_store_dwordx4 v254, v[16:19], s[28:29] offset:-2048 sc1\n"
        "s_waitcnt vmcnt(2)\n"
        "ds_read_b64_tr_b16 v[114:115], v189 offset:1024\n"
        "v_mfma_f32_16x16x32_bf16 v[44:47], a[8:11], a[164:167], v[44:47]\n"
        "ds_read_b64_tr_b16 v[118:119], v189 offset:3072\n"
        "v_and_b32 v215, 0xffff0000, v107\n"
        "v_mfma_f32_16x16x32_bf16 v[60:63], a[12:15], a[164:167], v[60:63]\n"
        "ds_read_b64_tr_b16 v[122:123], v189 offset:1056\n"
        "v_and_b32 v223, 0xffff0000, v111\n"
        "v_mfma_f32_16x16x32_bf16 v[12:15], a[16:19], a[172:175], v[12:15]\n"
        "ds_read_b64_tr_b16 v[126:127], v189 offset:3104\n"
        "global_store_dwordx4 v254, v[32:35], s[28:29] offset:0 sc1\n"
        "ds_read_b128 a[164:167], v167 offset:1024\n"
        "ds_read_b64_tr_b16 v[112:113], v189 offset:0\n"
        "v_sub_f32 v131, 1.0, v129\n"
        "v_mfma_f32_16x16x32_bf16 v[28:31], a[20:23], a[172:175], v[28:31]\n"
        "ds_read_b64_tr_b16 v[116:117], v189 offset:2048\n"
        "v_cvt_pk_bf16_f32 v131, 0, v131\n"
        "v_mfma_f32_16x16x32_bf16 v[44:47], a[24:27], a[172:175], v[44:47]\n"
        "ds_read_b64_tr_b16 v[120:121], v189 offset:32\n"
        "v_sub_f32 v130, 1.0, v128\n"
        "v_mfma_f32_16x16x32_bf16 v[60:63], a[28:31], a[172:175], v[60:63]\n"
        "ds_read_b128 a[172:175], v167 offset:9216\n"
        "v_cvt_pk_bf16_f32 v130, 0, v130\n"
        "global_store_dwordx4 v254, v[48:51], s[28:29] offset:2048 sc1\n"
        "ds_read_b64_tr_b16 v[124:125], v189 offset:2080\n"
        "s_waitcnt lgkmcnt(9)\n"
        "v_and_b32 v87, 0xffff0000, v115\n"
        "v_mul_f32 v199, v199, v87\n"
        "v_mfma_f32_16x16x32_bf16 v[12:15], a[32:35], a[180:183], v[12:15]\n"
        "s_waitcnt lgkmcnt(7)\n"
        "v_and_b32 v87, 0xffff0000, v119\n"
        "v_mul_f32 v207, v207, v87\n"
        "v_mfma_f32_16x16x32_bf16 v[28:31], a[36:39], a[180:183], v[28:31]\n"
        "v_and_b32 v87, 0xffff0000, v123\n"
        "v_mul_f32 v215, v215, v87\n"
        "v_mfma_f32_16x16x32_bf16 v[44:47], a[40:43], a[180:183], v[44:47]\n"
        "s_waitcnt lgkmcnt(4)\n"
        "v_and_b32 v87, 0xffff0000, v127\n"
        "v_mul_f32 v223, v223, v87\n"
        "global_store_dwordx4 v254, v[4:7], s[28:29] offset:-3072 sc1\n"
        "v_cvt_pk_bf16_f32 v223, 0, v223\n"
        "v_mul_f32 v223, v223, v131\n"
        "v_cvt_pk_bf16_f32 v223, 0, v223\n"
        "v_lshlrev_b32 v80, 16, v112\n"
        "v_mfma_f32_16x16x32_bf16 v[60:63], a[44:47], a[180:183], v[60:63]\n"
        "ds_read_b128 a[180:183], v167 offset:33792\n"
        "v_and_b32 v81, 0xffff0000, v112\n"
        "v_mfma_f32_16x16x32_bf16 v[12:15], a[48:51], a[188:191], v[12:15]\n"
        "v_lshlrev_b32 v82, 16, v113\n"
        "v_and_b32 v83, 0xffff0000, v113\n"
        "v_mfma_f32_16x16x32_bf16 v[28:31], a[52:55], a[188:191], v[28:31]\n"
        "v_mul_f32 v192, v192, v80\n"
        "v_mul_f32 v193, v193, v81\n"
        "global_store_dwordx4 v254, v[20:23], s[28:29] offset:-1024 sc1\n"
        "s_waitcnt lgkmcnt(1)\n"
        "v_mul_f32 v194, v194, v82\n"
        "v_mul_f32 v195, v195, v83\n"
        "v_cvt_pk_bf16_f32 v192, 0, v192\n"
        "v_mfma_f32_16x16x32_bf16 v[44:47], a[56:59], a[188:191], v[44:47]\n"
        "v_cvt_pk_bf16_f32 v193, 0, v193\n"
        "v_cvt_pk_bf16_f32 v194, 0, v194\n"
        "v_mfma_f32_16x16x32_bf16 v[60:63], a[60:63], a[188:191], v[60:63]\n"
        "ds_read_b128 a[188:191], v167 offset:41984\n"
        "v_cvt_pk_bf16_f32 v195, 0, v195\n"
        "v_mfma_f32_16x16x32_bf16 v[8:11], a[0:3], a[160:163], v[8:11]\n"
        "v_mul_f32 v192, v192, v130\n"
        "v_mul_f32 v193, v193, v130\n"
        "global_store_dwordx4 v254, v[36:39], s[28:29] offset:1024 sc1\n"
        "v_mul_f32 v194, v194, v130\n"
        "v_mul_f32 v195, v195, v130\n"
        "v_cvt_pk_bf16_f32 v192, 0, v192\n"
        "v_cvt_pk_bf16_f32 v193, 0, v193\n"
        "v_mfma_f32_16x16x32_bf16 v[24:27], a[4:7], a[160:163], v[24:27]\n"
        "v_cvt_pk_bf16_f32 v194, 0, v194\n"
        "v_cvt_pk_bf16_f32 v195, 0, v195\n"
        "v_mfma_f32_16x16x32_bf16 v[40:43], a[8:11], a[160:163], v[40:43]\n"
        "v_lshlrev_b32 v198, 16, v99\n"
        "v_lshlrev_b32 v84, 16, v114\n"
        "v_mfma_f32_16x16x32_bf16 v[56:59], a[12:15], a[160:163], v[56:59]\n"
        "ds_read_b128 a[160:163], v167 offset:0\n"
        "v_and_b32 v85, 0xffff0000, v114\n"
        "global_store_dwordx4 v254, v[52:55], s[28:29] offset:3072 sc1\n"
        "v_lshlrev_b32 v86, 16, v115\n"
        "v_mul_f32 v196, v196, v84\n"
        "v_mul_f32 v197, v197, v85\n"
        "v_mul_f32 v198, v198, v86\n"
        "v_mfma_f32_16x16x32_bf16 v[60:63], a[204:207], a[164:167], v[60:63]\n"
        "v_cvt_pk_bf16_f32 v196, 0, v196\n"
        "v_cvt_pk_bf16_f32 v197, 0, v197\n"
        "v_mfma_f32_16x16x32_bf16 v[60:63], a[220:223], a[172:175], v[60:63]\n"
        "s_waitcnt lgkmcnt(1)\n"
        "v_cvt_pk_bf16_f32 v198, 0, v198\n"
        "v_cvt_pk_bf16_f32 v199, 0, v199\n"
        "v_mfma_f32_16x16x32_bf16 v[60:63], a[236:239], a[180:183], v[60:63]\n"
        "v_mul_f32 v196, v196, v130\n"
        "v_mul_f32 v197, v197, v130\n"
        "v_mfma_f32_16x16x32_bf16 v[60:63], a[252:255], a[188:191], v[60:63]\n"
        "v_mul_f32 v198, v198, v130\n"
        "v_mul_f32 v199, v199, v130\n"
        "v_mfma_f32_16x16x32_bf16 v[8:11], a[16:19], a[168:171], v[8:11]\n"
        "v_cvt_pk_bf16_f32 v196, 0, v196\n"
        "v_cvt_pk_bf16_f32 v197, 0, v197\n"
        "v_mfma_f32_16x16x32_bf16 v[24:27], a[20:23], a[168:171], v[24:27]\n"
        "v_cvt_pk_bf16_f32 v198, 0, v198\n"
        "v_cvt_pk_bf16_f32 v199, 0, v199\n"
        "v_mfma_f32_16x16x32_bf16 v[40:43], a[24:27], a[168:171], v[40:43]\n"
        "v_lshlrev_b32 v200, 16, v100\n"
        "v_and_b32 v201, 0xffff0000, v100\n"
        "v_mfma_f32_16x16x32_bf16 v[56:59], a[28:31], a[168:171], v[56:59]\n"
        "ds_read_b128 a[168:171], v167 offset:8192\n"
        "v_lshlrev_b32 v202, 16, v101\n"
        "v_mfma_f32_16x16x32_bf16 v[8:11], a[32:35], a[176:179], v[8:11]\n"
        "v_and_b32 v203, 0xffff0000, v101\n"
        "v_lshlrev_b32 v80, 16, v116\n"
        "v_mfma_f32_16x16x32_bf16 v[24:27], a[36:39], a[176:179], v[24:27]\n"
        "v_and_b32 v81, 0xffff0000, v116\n"
        "v_lshlrev_b32 v82, 16, v117\n"
        "v_mfma_f32_16x16x32_bf16 v[40:43], a[40:43], a[176:179], v[40:43]\n"
        "v_and_b32 v83, 0xffff0000, v117\n"
        "v_mul_f32 v200, v200, v80\n"
        "v_mfma_f32_16x16x32_bf16 v[56:59], a[44:47], a[176:179], v[56:59]\n"
        "ds_read_b128 a[176:179], v167 offset:32768\n"
        "v_mul_f32 v201, v201, v81\n"
        "v_mfma_f32_16x16x32_bf16 v[8:11], a[48:51], a[184:187], v[8:11]\n"
        "s_waitcnt lgkmcnt(2)\n"
        "v_mul_f32 v202, v202, v82\n"
        "v_mul_f32 v203, v203, v83\n"
        "v_mfma_f32_16x16x32_bf16 v[24:27], a[52:55], a[184:187], v[24:27]\n"
        "v_cvt_pk_bf16_f32 v200, 0, v200\n"
        "v_cvt_pk_bf16_f32 v201, 0, v201\n"
        "v_mfma_f32_16x16x32_bf16 v[40:43], a[56:59], a[184:187], v[40:43]\n"
        "v_cvt_pk_bf16_f32 v202, 0, v202\n"
        "v_cvt_pk_bf16_f32 v203, 0, v203\n"
        "v_mfma_f32_16x16x32_bf16 v[56:59], a[60:63], a[184:187], v[56:59]\n"
        "ds_read_b128 a[184:187], v167 offset:40960\n"
        "v_mul_f32 v200, v200, v130\n"
        "v_mfma_f32_16x16x32_bf16 v[8:11], a[192:195], a[160:163], v[8:11]\n"
        "v_mul_f32 v201, v201, v130\n"
        "v_mul_f32 v202, v202, v130\n"
        "v_mfma_f32_16x16x32_bf16 v[12:15], a[192:195], a[164:167], v[12:15]\n"
        "s_waitcnt lgkmcnt(2)\n"
        "v_mul_f32 v203, v203, v130\n"
        "v_cvt_pk_bf16_f32 v200, 0, v200\n"
        "v_mfma_f32_16x16x32_bf16 v[24:27], a[196:199], a[160:163], v[24:27]\n"
        "v_cvt_pk_bf16_f32 v201, 0, v201\n"
        "v_cvt_pk_bf16_f32 v202, 0, v202\n"
        "v_mfma_f32_16x16x32_bf16 v[28:31], a[196:199], a[164:167], v[28:31]\n"
        "v_cvt_pk_bf16_f32 v203, 0, v203\n"
        "v_lshlrev_b32 v204, 16, v102\n"
        "v_mfma_f32_16x16x32_bf16 v[40:43], a[200:203], a[160:163], v[40:43]\n"
        "v_and_b32 v205, 0xffff0000, v102\n"
        "v_lshlrev_b32 v206, 16, v103\n"
        "v_mfma_f32_16x16x32_bf16 v[44:47], a[200:203], a[164:167], v[44:47]\n"
        "v_lshlrev_b32 v84, 16, v118\n"
        "v_and_b32 v85, 0xffff0000, v118\n"
        "v_mfma_f32_16x16x32_bf16 v[56:59], a[204:207], a[160:163], v[56:59]\n"
        "v_lshlrev_b32 v86, 16, v119\n"
        "v_mul_f32 v204, v204, v84\n"
        "v_mfma_f32_16x16x32_bf16 v[8:11], a[208:211], a[168:171], v[8:11]\n"
        "v_mul_f32 v205, v205, v85\n"
        "v_mul_f32 v206, v206, v86\n"
        "v_mfma_f32_16x16x32_bf16 v[12:15], a[208:211], a[172:175], v[12:15]\n"
        "s_waitcnt lgkmcnt(0)\n"
        "v_cvt_pk_bf16_f32 v204, 0, v204\n"
        "v_cvt_pk_bf16_f32 v205, 0, v205\n"
        "v_mfma_f32_16x16x32_bf16 v[24:27], a[212:215], a[168:171], v[24:27]\n"
        "v_cvt_pk_bf16_f32 v206, 0, v206\n"
        "v_cvt_pk_bf16_f32 v207, 0, v207\n"
        "v_mfma_f32_16x16x32_bf16 v[28:31], a[212:215], a[172:175], v[28:31]\n"
        "v_mul_f32 v204, v204, v130\n"
        "v_mul_f32 v205, v205, v130\n"
        "v_mfma_f32_16x16x32_bf16 v[40:43], a[216:219], a[168:171], v[40:43]\n"
        "v_mul_f32 v206, v206, v130\n"
        "v_mul_f32 v207, v207, v130\n"
        "v_mfma_f32_16x16x32_bf16 v[44:47], a[216:219], a[172:175], v[44:47]\n"
        "v_cvt_pk_bf16_f32 v204, 0, v204\n"
        "v_cvt_pk_bf16_f32 v205, 0, v205\n"
        "v_mfma_f32_16x16x32_bf16 v[56:59], a[220:223], a[168:171], v[56:59]\n"
        "v_cvt_pk_bf16_f32 v206, 0, v206\n"
        "v_cvt_pk_bf16_f32 v207, 0, v207\n"
        "v_mfma_f32_16x16x32_bf16 v[8:11], a[224:227], a[176:179], v[8:11]\n"
        "v_lshlrev_b32 v208, 16, v104\n"
        "v_and_b32 v209, 0xffff0000, v104\n"
        "v_mfma_f32_16x16x32_bf16 v[12:15], a[224:227], a[180:183], v[12:15]\n"
        "v_lshlrev_b32 v210, 16, v105\n"
        "v_and_b32 v211, 0xffff0000, v105\n"
        "v_mfma_f32_16x16x32_bf16 v[24:27], a[228:231], a[176:179], v[24:27]\n"
        "v_lshlrev_b32 v80, 16, v120\n"
        "v_and_b32 v81, 0xffff0000, v120\n"
        "v_mfma_f32_16x16x32_bf16 v[28:31], a[228:231], a[180:183], v[28:31]\n"
        "v_lshlrev_b32 v82, 16, v121\n"
        "v_and_b32 v83, 0xffff0000, v121\n"
        "v_mfma_f32_16x16x32_bf16 v[40:43], a[232:235], a[176:179], v[40:43]\n"
        "v_mul_f32 v208, v208, v80\n"
        "v_mul_f32 v209, v209, v81\n"
        "v_mfma_f32_16x16x32_bf16 v[44:47], a[232:235], a[180:183], v[44:47]\n"
        "v_mul_f32 v210, v210, v82\n"
        "v_mul_f32 v211, v211, v83\n"
        "v_mfma_f32_16x16x32_bf16 v[56:59], a[236:239], a[176:179], v[56:59]\n"
        "v_cvt_pk_bf16_f32 v208, 0, v208\n"
        "v_cvt_pk_bf16_f32 v209, 0, v209\n"
        "v_mfma_f32_16x16x32_bf16 v[8:11], a[240:243], a[184:187], v[8:11]\n"
        "v_cvt_pk_bf16_f32 v210, 0, v210\n"
        "v_cvt_pk_bf16_f32 v211, 0, v211\n"
        "v_mfma_f32_16x16x32_bf16 v[12:15], a[240:243], a[188:191], v[12:15]\n"
        "v_mul_f32 v208, v208, v131\n"
        "v_mul_f32 v209, v209, v131\n"
        "v_mfma_f32_16x16x32_bf16 v[24:27], a[244:247], a[184:187], v[24:27]\n"
        "v_mul_f32 v210, v210, v131\n"
        "v_mul_f32 v211, v211, v131\n"
        "v_mfma_f32_16x16x32_bf16 v[28:31], a[244:247], a[188:191], v[28:31]\n"
        "v_cvt_pk_bf16_f32 v208, 0, v208\n"
        "v_cvt_pk_bf16_f32 v209, 0, v209\n"
        "v_mfma_f32_16x16x32_bf16 v[40:43], a[248:251], a[184:187], v[40:43]\n"
        "v_cvt_pk_bf16_f32 v210, 0, v210\n"
        "v_cvt_pk_bf16_f32 v211, 0, v211\n"
        "v_mfma_f32_16x16x32_bf16 v[44:47], a[248:251], a[188:191], v[44:47]\n"
        "v_lshlrev_b32 v212, 16, v106\n"
        "v_and_b32 v213, 0xffff0000, v106\n"
        "v_mfma_f32_16x16x32_bf16 v[56:59], a[252:255], a[184:187], v[56:59]\n"
        "v_lshlrev_b32 v214, 16, v107\n"
        "v_lshlrev_b32 v84, 16, v122\n"
        "v_and_b32 v85, 0xffff0000, v122\n"
        "v_lshlrev_b32 v86, 16, v123\n"
        "v_mul_f32 v212, v212, v84\n"
        "v_mul_f32 v213, v213, v85\n"
        "v_mul_f32 v214, v214, v86\n"
        "v_cvt_pk_bf16_f32 v212, 0, v212\n"
        "v_cvt_pk_bf16_f32 v213, 0, v213\n"
        "v_cvt_pk_bf16_f32 v214, 0, v214\n"
        "v_cvt_pk_bf16_f32 v215, 0, v215\n"
        "v_mul_f32 v212, v212, v131\n"
        "v_mul_f32 v213, v213, v131\n"
        "v_mul_f32 v214, v214, v131\n"
        "v_mul_f32 v215, v215, v131\n"
        "v_cvt_pk_bf16_f32 v212, 0, v212\n"
        "v_cvt_pk_bf16_f32 v213, 0, v213\n"
        "v_cvt_pk_bf16_f32 v214, 0, v214\n"
        "v_cvt_pk_bf16_f32 v215, 0, v215\n"
        "v_lshlrev_b32 v216, 16, v108\n"
        "v_and_b32 v217, 0xffff0000, v108\n"
        "v_lshlrev_b32 v218, 16, v109\n"
        "v_and_b32 v219, 0xffff0000, v109\n"
        "v_lshlrev_b32 v80, 16, v124\n"
        "v_and_b32 v81, 0xffff0000, v124\n"
        "v_lshlrev_b32 v82, 16, v125\n"
        "v_and_b32 v83, 0xffff0000, v125\n"
        "v_mul_f32 v216, v216, v80\n"
        "v_mul_f32 v217, v217, v81\n"
        "v_mul_f32 v218, v218, v82\n"
        "v_mul_f32 v219, v219, v83\n"
        "v_cvt_pk_bf16_f32 v216, 0, v216\n"
        "v_cvt_pk_bf16_f32 v217, 0, v217\n"
        "v_cvt_pk_bf16_f32 v218, 0, v218\n"
        "v_cvt_pk_bf16_f32 v219, 0, v219\n"
        "v_mul_f32 v216, v216, v131\n"
        "v_mul_f32 v217, v217, v131\n"
        "v_mul_f32 v218, v218, v131\n"
        "v_mul_f32 v219, v219, v131\n"
        "v_cvt_pk_bf16_f32 v216, 0, v216\n"
        "v_cvt_pk_bf16_f32 v217, 0, v217\n"
        "v_cvt_pk_bf16_f32 v218, 0, v218\n"
        "v_cvt_pk_bf16_f32 v219, 0, v219\n"
        "v_lshlrev_b32 v220, 16, v110\n"
        "v_and_b32 v221, 0xffff0000, v110\n"
        "v_lshlrev_b32 v222, 16, v111\n"
        "v_lshlrev_b32 v84, 16, v126\n"
        "v_and_b32 v85, 0xffff0000, v126\n"
        "v_lshlrev_b32 v86, 16, v127\n"
        "v_mul_f32 v220, v220, v84\n"
        "v_mul_f32 v221, v221, v85\n"
        "v_mul_f32 v222, v222, v86\n"
        "v_cvt_pk_bf16_f32 v220, 0, v220\n"
        "v_cvt_pk_bf16_f32 v221, 0, v221\n"
        "v_cvt_pk_bf16_f32 v222, 0, v222\n"
        "v_mul_f32 v220, v220, v131\n"
        "v_mul_f32 v221, v221, v131\n"
        "v_mul_f32 v222, v222, v131\n"
        "v_cvt_pk_bf16_f32 v220, 0, v220\n"
        "v_cvt_pk_bf16_f32 v221, 0, v221\n"
        "v_cvt_pk_bf16_f32 v222, 0, v222\n"
        "s_waitcnt vmcnt(0)\n"
        "s_mov_b64 exec, 0xffff\n"
        "global_store_dwordx2 v182, v[250:251], s[48:49] sc1\n"
        "s_mov_b64 exec, -1\n"
        "s_or_b32 s97, s55, s40\n"
        "s_cbranch_scc0 LEP_%=\n"
        "LEB_%=:\n"
        "global_load_dwordx2 v[184:185], v183, s[48:49] sc1\n"
        "s_waitcnt vmcnt(0)\n"
        "v_cmp_ne_u64 vcc, v[184:185], v[250:251]\n"
        "s_nop 0\n"
        "s_cbranch_vccnz LPPR_%=\n"
        "LBPR_%=:\n"
        "global_load_dwordx4 v[132:135], v254, s[30:31] offset:-4096 sc1\n"
        "global_load_dwordx4 v[136:139], v254, s[30:31] offset:-2048 sc1\n"
        "global_load_dwordx4 v[140:143], v254, s[30:31] offset:0 sc1\n"
        "global_load_dwordx4 v[144:147], v254, s[30:31] offset:2048 sc1\n"
        "global_load_dwordx4 v[148:151], v254, s[30:31] offset:-3072 sc1\n"
        "global_load_dwordx4 v[152:155], v254, s[30:31] offset:-1024 sc1\n"
        "global_load_dwordx4 v[156:159], v254, s[30:31] offset:1024 sc1\n"
        "global_load_dwordx4 v[160:163], v254, s[30:31] offset:3072 sc1\n"
        "s_waitcnt vmcnt(7)\n"
        "v_add_f32 v132, v132, v8\n"
        "v_add_f32 v133, v133, v9\n"
        "v_add_f32 v134, v134, v10\n"
        "v_add_f32 v135, v135, v11\n"
        "v_cvt_pk_bf16_f32 v132, 0, v132\n"
        "v_cvt_pk_bf16_f32 v133, 0, v133\n"
        "v_cvt_pk_bf16_f32 v134, 0, v134\n"
        "v_cvt_pk_bf16_f32 v135, 0, v135\n"
        "v_mul_f32 v132, v132, v128\n"
        "v_mul_f32 v133, v133, v128\n"
        "v_mul_f32 v134, v134, v128\n"
        "v_mul_f32 v135, v135, v128\n"
        "v_cvt_pk_bf16_f32 v132, 0, v132\n"
        "v_cvt_pk_bf16_f32 v133, 0, v133\n"
        "v_cvt_pk_bf16_f32 v134, 0, v134\n"
        "v_cvt_pk_bf16_f32 v135, 0, v135\n"
        "v_add_f32 v132, v132, v192\n"
        "v_add_f32 v133, v133, v193\n"
        "v_add_f32 v134, v134, v194\n"
        "v_add_f32 v135, v135, v195\n"
        "v_cvt_pk_bf16_f32 v80, v132, v133\n"
        "v_cvt_pk_bf16_f32 v81, v134, v135\n"
        "s_waitcnt vmcnt(6)\n"
        "v_add_f32 v136, v136, v24\n"
        "v_add_f32 v137, v137, v25\n"
        "v_add_f32 v138, v138, v26\n"
        "v_add_f32 v139, v139, v27\n"
        "v_cvt_pk_bf16_f32 v136, 0, v136\n"
        "v_cvt_pk_bf16_f32 v137, 0, v137\n"
        "v_cvt_pk_bf16_f32 v138, 0, v138\n"
        "v_cvt_pk_bf16_f32 v139, 0, v139\n"
        "v_mul_f32 v136, v136, v128\n"
        "v_mul_f32 v137, v137, v128\n"
        "v_mul_f32 v138, v138, v128\n"
        "v_mul_f32 v139, v139, v128\n"
        "v_cvt_pk_bf16_f32 v136, 0, v136\n"
        "v_cvt_pk_bf16_f32 v137, 0, v137\n"
        "v_cvt_pk_bf16_f32 v138, 0, v138\n"
        "v_cvt_pk_bf16_f32 v139, 0, v139\n"
        "v_add_f32 v136, v136, v196\n"
        "v_add_f32 v137, v137, v197\n"
        "v_add_f32 v138, v138, v198\n"
        "v_add_f32 v139, v139, v199\n"
        "v_cvt_pk_bf16_f32 v82, v136, v137\n"
        "v_cvt_pk_bf16_f32 v83, v138, v139\n"
        "s_waitcnt vmcnt(5)\n"
        "v_add_f32 v140, v140, v40\n"
        "v_permlane16_swap_b32 v80, v82\n"
        "v_permlane16_swap_b32 v81, v83\n"
        "global_store_dwordx4 v191, v[80:83], s[56:57] offset:0\n"
        "v_add_f32 v141, v141, v41\n"
        "v_add_f32 v142, v142, v42\n"
        "v_add_f32 v143, v143, v43\n"
        "v_cvt_pk_bf16_f32 v140, 0, v140\n"
        "v_cvt_pk_bf16_f32 v141, 0, v141\n"
        "v_cvt_pk_bf16_f32 v142, 0, v142\n"
        "v_cvt_pk_bf16_f32 v143, 0, v143\n"
        "v_mul_f32 v140, v140, v128\n"
        "v_mul_f32 v141, v141, v128\n"
        "v_mul_f32 v142, v142, v128\n"
        "v_mul_f32 v143, v143, v128\n"
        "v_cvt_pk_bf16_f32 v140, 0, v140\n"
        "v_cvt_pk_bf16_f32 v141, 0, v141\n"
        "v_cvt_pk_bf16_f32 v142, 0, v142\n"
        "v_cvt_pk_bf16_f32 v143, 0, v143\n"
        "v_add_f32 v140, v140, v200\n"
        "v_add_f32 v141, v141, v201\n"
        "v_add_f32 v142, v142, v202\n"
        "v_add_f32 v143, v143, v203\n"
        "v_cvt_pk_bf16_f32 v84, v140, v141\n"
        "v_cvt_pk_bf16_f32 v85, v142, v143\n"
        "s_waitcnt vmcnt(5)\n"
        "v_add_f32 v144, v144, v56\n"
        "v_add_f32 v145, v145, v57\n"
        "v_add_f32 v146, v146, v58\n"
        "v_add_f32 v147, v147, v59\n"
        "v_cvt_pk_bf16_f32 v144, 0, v144\n"
        "v_cvt_pk_bf16_f32 v145, 0, v145\n"
        "v_cvt_pk_bf16_f32 v146, 0, v146\n"
        "v_cvt_pk_bf16_f32 v147, 0, v147\n"
        "v_mul_f32 v144, v144, v128\n"
        "v_mul_f32 v145, v145, v128\n"
        "v_mul_f32 v146, v146, v128\n"
        "v_mul_f32 v147, v147, v128\n"
        "v_cvt_pk_bf16_f32 v144, 0, v144\n"
        "v_cvt_pk_bf16_f32 v145, 0, v145\n"
        "v_cvt_pk_bf16_f32 v146, 0, v146\n"
        "v_cvt_pk_bf16_f32 v147, 0, v147\n"
        "v_add_f32 v144, v144, v204\n"
        "v_add_f32 v145, v145, v205\n"
        "v_add_f32 v146, v146, v206\n"
        "v_add_f32 v147, v147, v207\n"
        "v_cvt_pk_bf16_f32 v86, v144, v145\n"
        "v_cvt_pk_bf16_f32 v87, v146, v147\n"
        "s_waitcnt vmcnt(4)\n"
        "v_add_f32 v148, v148, v12\n"
        "v_permlane16_swap_b32 v84, v86\n"
        "v_permlane16_swap_b32 v85, v87\n"
        "global_store_dwordx4 v191, v[84:87], s[56:57] offset:64\n"
        "v_add_u32 v191, 0x8000, v191\n"
        "v_add_f32 v149, v149, v13\n"
        "v_add_f32 v150, v150, v14\n"
        "v_add_f32 v151, v151, v15\n"
        "v_cvt_pk_bf16_f32 v148, 0, v148\n"
        "v_cvt_pk_bf16_f32 v149, 0, v149\n"
        "v_cvt_pk_bf16_f32 v150, 0, v150\n"
        "v_cvt_pk_bf16_f32 v151, 0, v151\n"
        "v_mul_f32 v148, v148, v129\n"
        "v_mul_f32 v149, v149, v129\n"
        "v_mul_f32 v150, v150, v129\n"
        "v_mul_f32 v151, v151, v129\n"
        "v_cvt_pk_bf16_f32 v148, 0, v148\n"
        "v_cvt_pk_bf16_f32 v149, 0, v149\n"
        "v_cvt_pk_bf16_f32 v150, 0, v150\n"
        "v_cvt_pk_bf16_f32 v151, 0, v151\n"
        "v_add_f32 v148, v148, v208\n"
        "v_add_f32 v149, v149, v209\n"
        "v_add_f32 v150, v150, v210\n"
        "v_add_f32 v151, v151, v211\n"
        "v_cvt_pk_bf16_f32 v80, v148, v149\n"
        "v_cvt_pk_bf16_f32 v81, v150, v151\n"
        "s_waitcnt vmcnt(4)\n"
        "v_add_f32 v152, v152, v28\n"
        "v_add_f32 v153, v153, v29\n"
        "v_add_f32 v154, v154, v30\n"
        "v_add_f32 v155, v155, v31\n"
        "v_cvt_pk_bf16_f32 v152, 0, v152\n"
        "v_cvt_pk_bf16_f32 v153, 0, v153\n"
        "v_cvt_pk_bf16_f32 v154, 0, v154\n"
        "v_cvt_pk_bf16_f32 v155, 0, v155\n"
        "v_mul_f32 v152, v152, v129\n"
        "v_mul_f32 v153, v153, v129\n"
        "v_mul_f32 v154, v154, v129\n"
        "v_mul_f32 v155, v155, v129\n"
        "v_cvt_pk_bf16_f32 v152, 0, v152\n"
        "v_cvt_pk_bf16_f32 v153, 0, v153\n"
        "v_cvt_pk_bf16_f32 v154, 0, v154\n"
        "v_cvt_pk_bf16_f32 v155, 0, v155\n"
        "v_add_f32 v152, v152, v212\n"
        "v_add_f32 v153, v153, v213\n"
        "v_add_f32 v154, v154, v214\n"
        "v_add_f32 v155, v155, v215\n"
        "v_cvt_pk_bf16_f32 v82, v152, v153\n"
        "v_cvt_pk_bf16_f32 v83, v154, v155\n"
        "s_waitcnt vmcnt(3)\n"
        "v_add_f32 v156, v156, v44\n"
        "v_permlane16_swap_b32 v80, v82\n"
        "v_permlane16_swap_b32 v81, v83\n"
        "global_store_dwordx4 v191, v[80:83], s[56:57] offset:0\n"
        "v_add_f32 v157, v157, v45\n"
        "v_add_f32 v158, v158, v46\n"
        "v_add_f32 v159, v159, v47\n"
        "v_cvt_pk_bf16_f32 v156, 0, v156\n"
        "v_cvt_pk_bf16_f32 v157, 0, v157\n"
        "v_cvt_pk_bf16_f32 v158, 0, v158\n"
        "v_cvt_pk_bf16_f32 v159, 0, v159\n"
        "v_mul_f32 v156, v156, v129\n"
        "v_mul_f32 v157, v157, v129\n"
        "v_mul_f32 v158, v158, v129\n"
        "v_mul_f32 v159, v159, v129\n"
        "v_cvt_pk_bf16_f32 v156, 0, v156\n"
        "v_cvt_pk_bf16_f32 v157, 0, v157\n"
        "v_cvt_pk_bf16_f32 v158, 0, v158\n"
        "v_cvt_pk_bf16_f32 v159, 0, v159\n"
        "v_add_f32 v156, v156, v216\n"
        "v_add_f32 v157, v157, v217\n"
        "v_add_f32 v158, v158, v218\n"
        "v_add_f32 v159, v159, v219\n"
        "v_cvt_pk_bf16_f32 v84, v156, v157\n"
        "v_cvt_pk_bf16_f32 v85, v158, v159\n"
        "s_waitcnt vmcnt(3)\n"
        "v_add_f32 v160, v160, v60\n"
        "v_add_f32 v161, v161, v61\n"
        "v_add_f32 v162, v162, v62\n"
        "v_add_f32 v163, v163, v63\n"
        "v_cvt_pk_bf16_f32 v160, 0, v160\n"
        "v_cvt_pk_bf16_f32 v161, 0, v161\n"
        "v_cvt_pk_bf16_f32 v162, 0, v162\n"
        "v_cvt_pk_bf16_f32 v163, 0, v163\n"
        "v_mul_f32 v160, v160, v129\n"
        "v_mul_f32 v161, v161, v129\n"
        "v_mul_f32 v162, v162, v129\n"
        "v_mul_f32 v163, v163, v129\n"
        "v_cvt_pk_bf16_f32 v160, 0, v160\n"
        "v_cvt_pk_bf16_f32 v161, 0, v161\n"
        "v_cvt_pk_bf16_f32 v162, 0, v162\n"
        "v_cvt_pk_bf16_f32 v163, 0, v163\n"
        "v_add_f32 v160, v160, v220\n"
        "v_add_f32 v161, v161, v221\n"
        "v_add_f32 v162, v162, v222\n"
        "v_add_f32 v163, v163, v223\n"
        "v_cvt_pk_bf16_f32 v86, v160, v161\n"
        "v_cvt_pk_bf16_f32 v87, v162, v163\n"
        "s_nop 0\n"
        "v_permlane16_swap_b32 v84, v86\n"
        "v_permlane16_swap_b32 v85, v87\n"
        "global_store_dwordx4 v191, v[84:87], s[56:57] offset:64\n"
        "; ---- END\n"
        "s_waitcnt vmcnt(0) lgkmcnt(0)\n"
        "s_branch LEND_%=\n"
        "LPKV_%=:\n"
        "s_mov_b32 s97, 0x200000\n"
        "LQKV_%=:\n"
        "global_load_dwordx2 v[252:253], v245, s[48:49] sc1\n"
        "s_waitcnt vmcnt(0)\n"
        "v_cmp_ne_u64 vcc, v[252:253], v[250:251]\n"
        "s_nop 1\n"
        "s_cbranch_vccz LBKV_%=\n"
        "s_sleep 2\n"
        "s_sub_u32 s97, s97, 1\n"
        "s_cmp_lg_u32 s97, 0\n"
        "s_cbranch_scc1 LQKV_%=\n"
        "s_branch LBKV_%=\n"
        "LRL_%=:\n"
        "s_waitcnt vmcnt(0) lgkmcnt(0)\n"
        "s_nop 7\n"
        "v_accvgpr_write_b32 a176, v96\n"
        "v_accvgpr_write_b32 a177, v97\n"
        "v_accvgpr_write_b32 a178, v98\n"
        "v_accvgpr_write_b32 a179, v99\n"
        "v_accvgpr_write_b32 a180, v100\n"
        "v_accvgpr_write_b32 a181, v101\n"
        "v_accvgpr_write_b32 a182, v102\n"
        "v_accvgpr_write_b32 a183, v103\n"
        "v_accvgpr_write_b32 a184, v104\n"
        "v_accvgpr_write_b32 a185, v105\n"
        "v_accvgpr_write_b32 a186, v106\n"
        "v_accvgpr_write_b32 a187, v107\n"
        "v_accvgpr_write_b32 a188, v108\n"
        "v_accvgpr_write_b32 a189, v109\n"
        "v_accvgpr_write_b32 a190, v110\n"
        "v_accvgpr_write_b32 a191, v111\n"
        "s_mov_b32 s53, s44\n"
        "s_mov_b32 s96, 64\n"
        "s_mov_b32 s52, 0\n"
        "s_branch LREDO_%=\n"
        "LRET0_%=:\n"
        "v_accvgpr_write_b32 a176, v128\n"
        "v_accvgpr_write_b32 a177, v129\n"
        "v_accvgpr_write_b32 a178, v130\n"
        "v_accvgpr_write_b32 a179, v131\n"
        "v_accvgpr_write_b32 a180, v132\n"
        "v_accvgpr_write_b32 a181, v133\n"
        "v_accvgpr_write_b32 a182, v134\n"
        "v_accvgpr_write_b32 a183, v135\n"
        "v_accvgpr_write_b32 a184, v136\n"
        "v_accvgpr_write_b32 a185, v137\n"
        "v_accvgpr_write_b32 a186, v138\n"
        "v_accvgpr_write_b32 a187, v139\n"
        "v_accvgpr_write_b32 a188, v140\n"
        "v_accvgpr_write_b32 a189, v141\n"
        "v_accvgpr_write_b32 a190, v142\n"
        "v_accvgpr_write_b32 a191, v143\n"
        "s_mov_b32 s53, s44\n"
        "s_mov_b32 s96, 0\n"
        "s_mov_b32 s52, 1\n"
        "s_branch LREDO_%=\n"
        "LRET1_%=:\n"
        "s_branch LSL_%=\n"
        "LRH_%=:\n"
        "s_waitcnt vmcnt(0) lgkmcnt(0)\n"
        "s_nop 7\n"
        "v_accvgpr_write_b32 a176, v0\n"
        "v_accvgpr_write_b32 a177, v1\n"
        "v_accvgpr_write_b32 a178, v2\n"
        "v_accvgpr_write_b32 a179, v3\n"
        "v_accvgpr_write_b32 a180, v4\n"
        "v_accvgpr_write_b32 a181, v5\n"
        "v_accvgpr_write_b32 a182, v6\n"
        "v_accvgpr_write_b32 a183, v7\n"
        "v_accvgpr_write_b32 a184, v8\n"
        "v_accvgpr_write_b32 a185, v9\n"
        "v_accvgpr_write_b32 a186, v10\n"
        "v_accvgpr_write_b32 a187, v11\n"
        "v_accvgpr_write_b32 a188, v12\n"
        "v_accvgpr_write_b32 a189, v13\n"
        "v_accvgpr_write_b32 a190, v14\n"
        "v_accvgpr_write_b32 a191, v15\n"
        "s_mov_b32 s53, s45\n"
        "s_mov_b32 s96, 64\n"
        "s_mov_b32 s52, 2\n"
        "s_branch LREDO_%=\n"
        "LRET2_%=:\n"
        "v_accvgpr_write_b32 a176, v64\n"
        "v_accvgpr_write_b32 a177, v65\n"
        "v_accvgpr_write_b32 a178, v66\n"
        "v_accvgpr_write_b32 a179, v67\n"
        "v_accvgpr_write_b32 a180, v68\n"
        "v_accvgpr_write_b32 a181, v69\n"
        "v_accvgpr_write_b32 a182, v70\n"
        "v_accvgpr_write_b32 a183, v71\n"
        "v_accvgpr_write_b32 a184, v72\n"
        "v_accvgpr_write_b32 a185, v73\n"
        "v_accvgpr_write_b32 a186, v74\n"
        "v_accvgpr_write_b32 a187, v75\n"
        "v_accvgpr_write_b32 a188, v76\n"
        "v_accvgpr_write_b32 a189, v77\n"
        "v_accvgpr_write_b32 a190, v78\n"
        "v_accvgpr_write_b32 a191, v79\n"
        "s_mov_b32 s53, s45\n"
        "s_mov_b32 s96, 0\n"
        "s_mov_b32 s52, 3\n"
        "s_branch LREDO_%=\n"
        "LRET3_%=:\n"
        "s_branch LSH_%=\n"
        "LPCX_%=:\n"
        "s_mov_b32 s97, 0x200000\n"
        "LQCX_%=:\n"
        "global_load_dwordx2 v[184:185], v182, s[48:49] sc1\n"
        "s_waitcnt vmcnt(0)\n"
        "v_cmp_ne_u64 vcc, v[184:185], v[250:251]\n"
        "s_nop 1\n"
        "s_cbranch_vccz LBCX_%=\n"
        "s_sleep 2\n"
        "s_sub_u32 s97, s97, 1\n"
        "s_cmp_lg_u32 s97, 0\n"
        "s_cbranch_scc1 LQCX_%=\n"
        "s_branch LBCX_%=\n"
        "LPZZ_%=:\n"
        "s_mov_b32 s97, 0x200000\n"
        "LQZZ_%=:\n"
        "global_load_dwordx2 v[184:185], v182, s[48:49] sc1\n"
        "s_waitcnt vmcnt(0)\n"
        "v_cmp_ne_u64 vcc, v[184:185], v[250:251]\n"
        "s_nop 1\n"
        "s_cbranch_vccz LBZZ_%=\n"
        "s_sleep 2\n"
        "s_sub_u32 s97, s97, 1\n"
        "s_cmp_lg_u32 s97, 0\n"
        "s_cbranch_scc1 LQZZ_%=\n"
        "s_branch LBZZ_%=\n"
        "LEP_%=:\n"
        "v_lshlrev_b32 v88, 3, v254\n"
        "v_add_u32 v88, 0x8000, v88\n"
        "s_mov_b32 s96, 4\n"
        "s_mov_b32 s97, 0x200000\n"
        "LEQ_%=:\n"
        "global_load_dwordx2 v[90:91], v88, s[48:49] sc1\n"
        "s_waitcnt vmcnt(0)\n"
        "v_cmp_ne_u64 vcc, v[90:91], v[250:251]\n"
        "s_nop 1\n"
        "s_cbranch_vccz LER_%=\n"
        "s_sleep 2\n"
        "s_sub_u32 s97, s97, 1\n"
        "s_cmp_lg_u32 s97, 0\n"
        "s_cbranch_scc1 LEQ_%=\n"
        "LER_%=:\n"
        "v_add_u32 v88, 0x2000, v88\n"
        "s_sub_u32 s96, s96, 1\n"
        "s_cmp_lg_u32 s96, 0\n"
        "s_cbranch_scc1 LEQ_%=\n"
        "v_mov_b32 v88, 0\n"
        "s_mov_b64 exec, 1\n"
        "global_store_dwordx2 v88, v[250:251], s[48:49] sc1\n"
        "s_mov_b64 exec, -1\n"
        "s_branch LEB_%=\n"
        "LPPR_%=:\n"
        "s_mov_b32 s97, 0x200000\n"
        "LQPR_%=:\n"
        "global_load_dwordx2 v[184:185], v183, s[48:49] sc1\n"
        "s_waitcnt vmcnt(0)\n"
        "v_cmp_ne_u64 vcc, v[184:185], v[250:251]\n"
        "s_nop 1\n"
        "s_cbranch_vccz LBPR_%=\n"
        "s_sleep 2\n"
        "s_sub_u32 s97, s97, 1\n"
        "s_cmp_lg_u32 s97, 0\n"
        "s_cbranch_scc1 LQPR_%=\n"
        "s_branch LBPR_%=\n"
        "LREDO_%=:\n"
        "v_mov_b32 v112, 0\n"
        "v_mov_b32 v113, 0\n"
        "v_mov_b32 v114, 0\n"
        "v_mov_b32 v115, 0\n"
        "v_mov_b32 v116, 0\n"
        "v_mov_b32 v117, 0\n"
        "v_mov_b32 v118, 0\n"
        "v_mov_b32 v119, 0\n"
        "v_mov_b32 v120, 0\n"
        "v_mov_b32 v121, 0\n"
        "v_mov_b32 v122, 0\n"
        "v_mov_b32 v123, 0\n"
        "v_mov_b32 v124, 0\n"
        "v_mov_b32 v125, 0\n"
        "v_mov_b32 v126, 0\n"
        "v_mov_b32 v127, 0\n"
        "v_mov_b32 v144, 0\n"
        "v_mov_b32 v145, 0\n"
        "v_mov_b32 v146, 0\n"
        "v_mov_b32 v147, 0\n"
        "v_mov_b32 v148, 0\n"
        "v_mov_b32 v149, 0\n"
        "v_mov_b32 v150, 0\n"
        "v_mov_b32 v151, 0\n"
        "v_mov_b32 v152, 0\n"
        "v_mov_b32 v153, 0\n"
        "v_mov_b32 v154, 0\n"
        "v_mov_b32 v155, 0\n"
        "v_mov_b32 v156, 0\n"
        "v_mov_b32 v157, 0\n"
        "v_mov_b32 v158, 0\n"
        "v_mov_b32 v159, 0\n"
        "v_mov_b32 v60, 0\n"
        "v_mov_b32 v61, 0\n"
        "v_mov_b32 v62, 0\n"
        "v_mov_b32 v63, 0\n"
        "v_mov_b32 v24, 0xf149f2ca\n"
        "s_lshl_b32 s97, s53, 7\n"
        "s_add_u32 s97, s97, s96\n"
        "v_add_u32 v27, s97, v243\n"
        "s_lshl_b32 s97, s43, 19\n"
        "s_add_u32 s84, s38, s97\n"
        "s_addc_u32 s85, s39, 0\n"
        "s_add_u32 s84, s84, 0x1000\n"
        "s_addc_u32 s85, s85, 0\n"
        "s_add_u32 s86, s84, 0x1000000\n"
        "s_addc_u32 s87, s85, 0\n"
        "s_lshl_b32 s97, s53, 2\n"
        "s_add_u32 s97, s97, 4\n"
        "LRLOOP_%=:\n"
        "global_load_dwordx4 a[160:163], v254, s[84:85] offset:-4096 sc1\n"
        "global_load_dwordx4 a[164:167], v254, s[84:85] offset:-3072 sc1\n"
        "global_load_dwordx4 a[168:171], v254, s[84:85] offset:-2048 sc1\n"
        "global_load_dwordx4 a[172:175], v254, s[84:85] offset:-1024 sc1\n"
        "s_waitcnt vmcnt(0)\n"
        "v_mfma_f32_16x16x32_bf16 v[40:43], a[160:163], a[176:179], 0\n"
        "v_mfma_f32_16x16x32_bf16 v[40:43], a[164:167], a[180:183], v[40:43]\n"
        "v_mfma_f32_16x16x32_bf16 v[40:43], a[168:171], a[184:187], v[40:43]\n"
        "v_mfma_f32_16x16x32_bf16 v[40:43], a[172:175], a[188:191], v[40:43]\n"
        "s_nop 7\n"
        "global_load_dwordx4 a[160:163], v254, s[84:85] offset:0 sc1\n"
        "global_load_dwordx4 a[164:167], v254, s[84:85] offset:1024 sc1\n"
        "global_load_dwordx4 a[168:171], v254, s[84:85] offset:2048 sc1\n"
        "global_load_dwordx4 a[172:175], v254, s[84:85] offset:3072 sc1\n"
        "s_waitcnt vmcnt(0)\n"
        "v_mfma_f32_16x16x32_bf16 v[44:47], a[160:163], a[176:179], 0\n"
        "v_mfma_f32_16x16x32_bf16 v[44:47], a[164:167], a[180:183], v[44:47]\n"
        "v_mfma_f32_16x16x32_bf16 v[44:47], a[168:171], a[184:187], v[44:47]\n"
        "v_mfma_f32_16x16x32_bf16 v[44:47], a[172:175], a[188:191], v[44:47]\n"
        "s_nop 7\n"
        "v_cmp_gt_i32 vcc, 0, v27\n"
        "s_nop 1\n"
        "v_cndmask_b32 v40, v40, v242, vcc\n"
        "v_cmp_gt_i32 vcc, 1, v27\n"
        "s_nop 1\n"
        "v_cndmask_b32 v41, v41, v242, vcc\n"
        "v_cmp_gt_i32 vcc, 2, v27\n"
        "s_nop 1\n"
        "v_cndmask_b32 v42, v42, v242, vcc\n"
        "v_cmp_gt_i32 vcc, 3, v27\n"
        "s_nop 1\n"
        "v_cndmask_b32 v43, v43, v242, vcc\n"
        "v_cmp_gt_i32 vcc, 16, v27\n"
        "s_nop 1\n"
        "v_cndmask_b32 v44, v44, v242, vcc\n"
        "v_cmp_gt_i32 vcc, 17, v27\n"
        "s_nop 1\n"
        "v_cndmask_b32 v45, v45, v242, vcc\n"
        "v_cmp_gt_i32 vcc, 18, v27\n"
        "s_nop 1\n"
        "v_cndmask_b32 v46, v46, v242, vcc\n"
        "v_cmp_gt_i32 vcc, 19, v27\n"
        "s_nop 1\n"
        "v_cndmask_b32 v47, v47, v242, vcc\n"
        "v_max3_f32 v25, v40, v41, v24\n"
        "v_max3_f32 v25, v42, v43, v25\n"
        "v_max3_f32 v25, v44, v45, v25\n"
        "v_max3_f32 v25, v46, v47, v25\n"
        "v_mov_b32 v28, v25\n"
        "s_nop 1\n"
        "v_permlane16_swap_b32 v25, v28\n"
        "v_max_f32 v25, v25, v28\n"
        "v_mov_b32 v28, v25\n"
        "s_nop 1\n"
        "v_permlane32_swap_b32 v25, v28\n"
        "v_max_f32 v25, v25, v28\n"
        "v_sub_f32 v26, v24, v25\n"
        "v_exp_f32 v26, v26\n"
        "v_mov_b32 v24, v25\n"
        "v_sub_f32 v40, v40, v25\n"
        "v_exp_f32 v40, v40\n"
        "v_sub_f32 v41, v41, v25\n"
        "v_exp_f32 v41, v41\n"
        "v_sub_f32 v42, v42, v25\n"
        "v_exp_f32 v42, v42\n"
        "v_sub_f32 v43, v43, v25\n"
        "v_exp_f32 v43, v43\n"
        "v_sub_f32 v44, v44, v25\n"
        "v_exp_f32 v44, v44\n"
        "v_sub_f32 v45, v45, v25\n"
        "v_exp_f32 v45, v45\n"
        "v_sub_f32 v46, v46, v25\n"
        "v_exp_f32 v46, v46\n"
        "v_sub_f32 v47, v47, v25\n"
        "v_exp_f32 v47, v47\n"
        "v_cvt_pk_bf16_f32 v56, v40, v41\n"
        "v_cvt_pk_bf16_f32 v57, v42, v43\n"
        "v_cvt_pk_bf16_f32 v58, v44, v45\n"
        "v_cvt_pk_bf16_f32 v59, v46, v47\n"
        "v_mul_f32 v112, v112, v26\n"
        "v_mul_f32 v113, v113, v26\n"
        "v_mul_f32 v114, v114, v26\n"
        "v_mul_f32 v115, v115, v26\n"
        "v_mul_f32 v116, v116, v26\n"
        "v_mul_f32 v117, v117, v26\n"
        "v_mul_f32 v118, v118, v26\n"
        "v_mul_f32 v119, v119, v26\n"
        "v_mul_f32 v120, v120, v26\n"
        "v_mul_f32 v121, v121, v26\n"
        "v_mul_f32 v122, v122, v26\n"
        "v_mul_f32 v123, v123, v26\n"
        "v_mul_f32 v124, v124, v26\n"
        "v_mul_f32 v125, v125, v26\n"
        "v_mul_f32 v126, v126, v26\n"
        "v_mul_f32 v127, v127, v26\n"
        "v_mul_f32 v144, v144, v26\n"
        "v_mul_f32 v145, v145, v26\n"
        "v_mul_f32 v146, v146, v26\n"
        "v_mul_f32 v147, v147, v26\n"
        "v_mul_f32 v148, v148, v26\n"
        "v_mul_f32 v149, v149, v26\n"
        "v_mul_f32 v150, v150, v26\n"
        "v_mul_f32 v151, v151, v26\n"
        "v_mul_f32 v152, v152, v26\n"
        "v_mul_f32 v153, v153, v26\n"
        "v_mul_f32 v154, v154, v26\n"
        "v_mul_f32 v155, v155, v26\n"
        "v_mul_f32 v156, v156, v26\n"
        "v_mul_f32 v157, v157, v26\n"
        "v_mul_f32 v158, v158, v26\n"
        "v_mul_f32 v159, v159, v26\n"
        "v_mul_f32 v60, v60, v26\n"
        "v_mul_f32 v61, v61, v26\n"
        "v_mul_f32 v62, v62, v26\n"
        "v_mul_f32 v63, v63, v26\n"
        "s_nop 1\n"
        "v_mfma_f32_16x16x32_bf16 v[60:63], a[80:83], v[56:59], v[60:63]\n"
        "global_load_dwordx4 a[160:163], v254, s[86:87] offset:-4096 sc1\n"
        "global_load_dwordx4 a[164:167], v254, s[86:87] offset:-3072 sc1\n"
        "global_load_dwordx4 a[168:171], v254, s[86:87] offset:-2048 sc1\n"
        "global_load_dwordx4 a[172:175], v254, s[86:87] offset:-1024 sc1\n"
        "s_waitcnt vmcnt(0)\n"
        "v_mfma_f32_16x16x32_bf16 v[112:115], a[160:163], v[56:59], v[112:115]\n"
        "v_mfma_f32_16x16x32_bf16 v[116:119], a[164:167], v[56:59], v[116:119]\n"
        "v_mfma_f32_16x16x32_bf16 v[120:123], a[168:171], v[56:59], v[120:123]\n"
        "v_mfma_f32_16x16x32_bf16 v[124:127], a[172:175], v[56:59], v[124:127]\n"
        "s_nop 7\n"
        "global_load_dwordx4 a[160:163], v254, s[86:87] offset:0 sc1\n"
        "global_load_dwordx4 a[164:167], v254, s[86:87] offset:1024 sc1\n"
        "global_load_dwordx4 a[168:171], v254, s[86:87] offset:2048 sc1\n"
        "global_load_dwordx4 a[172:175], v254, s[86:87] offset:3072 sc1\n"
        "s_waitcnt vmcnt(0)\n"
        "v_mfma_f32_16x16x32_bf16 v[144:147], a[160:163], v[56:59], v[144:147]\n"
        "v_mfma_f32_16x16x32_bf16 v[148:151], a[164:167], v[56:59], v[148:151]\n"
        "v_mfma_f32_16x16x32_bf16 v[152:155], a[168:171], v[56:59], v[152:155]\n"
        "v_mfma_f32_16x16x32_bf16 v[156:159], a[172:175], v[56:59], v[156:159]\n"
        "s_nop 7\n"
        "s_add_u32 s84, s84, 0x2000\n"
        "s_addc_u32 s85, s85, 0\n"
        "s_add_u32 s86, s86, 0x2000\n"
        "s_addc_u32 s87, s87, 0\n"
        "v_subrev_u32 v27, 32, v27\n"
        "s_sub_u32 s97, s97, 1\n"
        "s_cmp_lg_u32 s97, 0\n"
        "s_cbranch_scc1 LRLOOP_%=\n"
        "s_lshl_b32 s97, s53, 20\n"
        "s_lshl_b32 s88, s43, 15\n"
        "s_add_u32 s97, s97, s88\n"
        "s_lshl_b32 s88, s40, 10\n"
        "s_add_u32 s97, s97, s88\n"
        "s_lshl_b32 s88, s96, 6\n"
        "s_add_u32 s97, s97, s88\n"
        "s_add_u32 s97, s97, 0x2000000\n"
        "s_add_u32 s84, s38, s97\n"
        "s_addc_u32 s85, s39, 0\n"
        "v_rcp_f32 v26, v60\n"
        "s_nop 0\n"
        "v_mul_f32 v112, v112, v26\n"
        "v_mul_f32 v113, v113, v26\n"
        "v_mul_f32 v114, v114, v26\n"
        "v_mul_f32 v115, v115, v26\n"
        "v_mul_f32 v116, v116, v26\n"
        "v_mul_f32 v117, v117, v26\n"
        "v_mul_f32 v118, v118, v26\n"
        "v_mul_f32 v119, v119, v26\n"
        "v_cvt_pk_bf16_f32 v112, v112, v113\n"
        "v_cvt_pk_bf16_f32 v113, v114, v115\n"
        "v_cvt_pk_bf16_f32 v114, v116, v117\n"
        "v_cvt_pk_bf16_f32 v115, v118, v119\n"
        "global_store_dwordx4 v254, v[112:115], s[84:85] sc1\n"
        "s_add_u32 s84, s84, 0x2000\n"
        "s_addc_u32 s85, s85, 0\n"
        "v_mul_f32 v120, v120, v26\n"
        "v_mul_f32 v121, v121, v26\n"
        "v_mul_f32 v122, v122, v26\n"
        "v_mul_f32 v123, v123, v26\n"
        "v_mul_f32 v124, v124, v26\n"
        "v_mul_f32 v125, v125, v26\n"
        "v_mul_f32 v126, v126, v26\n"
        "v_mul_f32 v127, v127, v26\n"
        "v_cvt_pk_bf16_f32 v120, v120, v121\n"
        "v_cvt_pk_bf16_f32 v121, v122, v123\n"
        "v_cvt_pk_bf16_f32 v122, v124, v125\n"
        "v_cvt_pk_bf16_f32 v123, v126, v127\n"
        "global_store_dwordx4 v254, v[120:123], s[84:85] sc1\n"
        "s_add_u32 s84, s84, 0x2000\n"
        "s_addc_u32 s85, s85, 0\n"
        "v_mul_f32 v144, v144, v26\n"
        "v_mul_f32 v145, v145, v26\n"
        "v_mul_f32 v146, v146, v26\n"
        "v_mul_f32 v147, v147, v26\n"
        "v_mul_f32 v148, v148, v26\n"
        "v_mul_f32 v149, v149, v26\n"
        "v_mul_f32 v150, v150, v26\n"
        "v_mul_f32 v151, v151, v26\n"
        "v_cvt_pk_bf16_f32 v144, v144, v145\n"
        "v_cvt_pk_bf16_f32 v145, v146, v147\n"
        "v_cvt_pk_bf16_f32 v146, v148, v149\n"
        "v_cvt_pk_bf16_f32 v147, v150, v151\n"
        "global_store_dwordx4 v254, v[144:147], s[84:85] sc1\n"
        "s_add_u32 s84, s84, 0x2000\n"
        "s_addc_u32 s85, s85, 0\n"
        "v_mul_f32 v152, v152, v26\n"
        "v_mul_f32 v153, v153, v26\n"
        "v_mul_f32 v154, v154, v26\n"
        "v_mul_f32 v155, v155, v26\n"
        "v_mul_f32 v156, v156, v26\n"
        "v_mul_f32 v157, v157, v26\n"
        "v_mul_f32 v158, v158, v26\n"
        "v_mul_f32 v159, v159, v26\n"
        "v_cvt_pk_bf16_f32 v152, v152, v153\n"
        "v_cvt_pk_bf16_f32 v153, v154, v155\n"
        "v_cvt_pk_bf16_f32 v154, v156, v157\n"
        "v_cvt_pk_bf16_f32 v155, v158, v159\n"
        "global_store_dwordx4 v254, v[152:155], s[84:85] sc1\n"
        "s_add_u32 s84, s84, 0x2000\n"
        "s_addc_u32 s85, s85, 0\n"
        "s_waitcnt vmcnt(0)\n"
        "s_cmp_eq_u32 s52, 0\n"
        "s_cbranch_scc1 LRET0_%=\n"
        "s_cmp_eq_u32 s52, 1\n"
        "s_cbranch_scc1 LRET1_%=\n"
        "s_cmp_eq_u32 s52, 2\n"
        "s_cbranch_scc1 LRET2_%=\n"
        "s_branch LRET3_%=\n"
        "LEND_%=:\n"
        :
        : "s"(__builtin_amdgcn_kernarg_segment_ptr()), "s"(blockIdx.x), "s"(__builtin_amdgcn_readfirstlane(threadIdx.x >> 6))
        : "v0", "v1", "v2", "v3", "v4", "v5", "v6", "v7", "v8", "v9", "v10", "v11", "v12", "v13", "v14", "v15", "v16", "v17", "v18", "v19", "v20", "v21", "v22", "v23", "v24", "v25", "v26", "v27", "v28", "v29", "v30", "v31",
        "v32", "v33", "v34", "v35", "v36", "v37", "v38", "v39", "v40", "v41", "v42", "v43", "v44", "v45", "v46", "v47", "v48", "v49", "v50", "v51", "v52", "v53", "v54", "v55", "v56", "v57", "v58", "v59", "v60", "v61", "v62", "v63",
        "v64", "v65", "v66", "v67", "v68", "v69", "v70", "v71", "v72", "v73", "v74", "v75", "v76", "v77", "v78", "v79", "v80", "v81", "v82", "v83", "v84", "v85", "v86", "v87", "v88", "v89", "v90", "v91", "v92", "v93", "v94", "v95",
        "v96", "v97", "v98", "v99", "v100", "v101", "v102", "v103", "v104", "v105", "v106", "v107", "v108", "v109", "v110", "v111", "v112", "v113", "v114", "v115", "v116", "v117", "v118", "v119", "v120", "v121", "v122", "v123", "v124", "v125", "v126", "v127",
        "v128", "v129", "v130", "v131", "v132", "v133", "v134", "v135", "v136", "v137", "v138", "v139", "v140", "v141", "v142", "v143", "v144", "v145", "v146", "v147", "v148", "v149", "v150", "v151", "v152", "v153", "v154", "v155", "v156", "v157", "v158", "v159",
        "v160", "v161", "v162", "v163", "v164", "v165", "v166", "v167", "v168", "v169", "v170", "v171", "v172", "v173", "v174", "v175", "v176", "v177", "v178", "v179", "v180", "v181", "v182", "v183", "v184", "v185", "v186", "v187", "v188", "v189", "v190", "v191",
        "v192", "v193", "v194", "v195", "v196", "v197", "v198", "v199", "v200", "v201", "v202", "v203", "v204", "v205", "v206", "v207", "v208", "v209", "v210", "v211", "v212", "v213", "v214", "v215", "v216", "v217", "v218", "v219", "v220", "v221", "v222", "v223",
        "v224", "v225", "v226", "v227", "v228", "v229", "v230", "v231", "v232", "v233", "v234", "v235", "v236", "v237", "v238", "v239", "v240", "v241", "v242", "v243", "v244", "v245", "v246", "v247", "v248", "v249", "v250", "v251", "v252", "v253", "v254", "v255",
        "a0", "a1", "a2", "a3", "a4", "a5", "a6", "a7", "a8", "a9", "a10", "a11", "a12", "a13", "a14", "a15", "a16", "a17", "a18", "a19", "a20", "a21", "a22", "a23", "a24", "a25", "a26", "a27", "a28", "a29", "a30", "a31",
        "a32", "a33", "a34", "a35", "a36", "a37", "a38", "a39", "a40", "a41", "a42", "a43", "a44", "a45", "a46", "a47", "a48", "a49", "a50", "a51", "a52", "a53", "a54", "a55", "a56", "a57", "a58", "a59", "a60", "a61", "a62", "a63",
        "a64", "a65", "a66", "a67", "a68", "a69", "a70", "a71", "a72", "a73", "a74", "a75", "a76", "a77", "a78", "a79", "a80", "a81", "a82", "a83", "a84", "a85", "a86", "a87", "a88", "a89", "a90", "a91", "a92", "a93", "a94", "a95",
        "a96", "a97", "a98", "a99", "a100", "a101", "a102", "a103", "a104", "a105", "a106", "a107", "a108", "a109", "a110", "a111", "a112", "a113", "a114", "a115", "a116", "a117", "a118", "a119", "a120", "a121", "a122", "a123", "a124", "a125", "a126", "a127",
        "a128", "a129", "a130", "a131", "a132", "a133", "a134", "a135", "a136", "a137", "a138", "a139", "a140", "a141", "a142", "a143", "a144", "a145", "a146", "a147", "a148", "a149", "a150", "a151", "a152", "a153", "a154", "a155", "a156", "a157", "a158", "a159",
        "a160", "a161", "a162", "a163", "a164", "a165", "a166", "a167", "a168", "a169", "a170", "a171", "a172", "a173", "a174", "a175", "a176", "a177", "a178", "a179", "a180", "a181", "a182", "a183", "a184", "a185", "a186", "a187", "a188", "a189", "a190", "a191",
        "a192", "a193", "a194", "a195", "a196", "a197", "a198", "a199", "a200", "a201", "a202", "a203", "a204", "a205", "a206", "a207", "a208", "a209", "a210", "a211", "a212", "a213", "a214", "a215", "a216", "a217", "a218", "a219", "a220", "a221", "a222", "a223",
        "a224", "a225", "a226", "a227", "a228", "a229", "a230", "a231", "a232", "a233", "a234", "a235", "a236", "a237", "a238", "a239", "a240", "a241", "a242", "a243", "a244", "a245", "a246", "a247", "a248", "a249", "a250", "a251", "a252", "a253", "a254", "a255",
        "s8", "s9", "s10", "s11", "s12", "s13", "s14", "s15", "s16", "s17", "s18", "s19", "s20", "s21", "s22", "s23", "s24", "s25", "s26", "s27", "s28", "s29", "s30", "s31", "s36", "s37", "s38", "s39", "s40", "s41", "s42", "s43",
        "s44", "s45", "s46", "s47", "s48", "s49", "s50", "s51", "s52", "s53", "s54", "s55", "s56", "s57", "s58", "s59", "s60", "s61", "s62", "s63", "s64", "s65", "s66", "s67", "s68", "s69", "s70", "s71", "s72", "s73", "s74", "s75",
        "s76", "s77", "s78", "s79", "s80", "s81", "s82", "s83", "s84", "s85", "s86", "s87", "s88", "s89", "s90", "s91", "s92", "s93", "s94", "s95", "s96", "s97", "s98", "s99", "vcc", "scc", "memory");
}

extern "C" void kernel_launch(void* const* d_in, const int* in_sizes, int n_in, void* d_out, int out_size, void* d_ws, size_t ws_size, hipStream_t stream) {
    (void)in_sizes; (void)n_in; (void)out_size;
    constexpr size_t WS_USED = (size_t)72 << 20;
    Params P;
    P.x = d_in[0]; P.ckv = d_in[1]; P.wq = d_in[2]; P.wuk = d_in[3]; P.wuv = d_in[4]; P.wo = d_in[5];
    P.bk = d_in[7]; P.bv = d_in[8]; P.shk = d_in[9]; P.ssk = d_in[10]; P.ssd = d_in[11]; P.gw = d_in[12];
    P.out = d_out; P.ws = (char*)d_ws;
    if (ws_size > WS_USED) P.ws += (ws_size - WS_USED) & ~(size_t)0xfffff;
    constexpr int LDS_MAX = 163840;
    (void)hipFuncSetAttribute((const void*)k_main, hipFuncAttributeMaxDynamicSharedMemorySize, LDS_MAX);
    k_main<<<256, 256, LDS_MAX, stream>>>(P);
}
```

```cpp
#include <hip/hip_runtime.h>
#include <stdint.h>

struct Params {
    const void *x, *ckv, *wq, *wuk, *wuv, *wo, *bk, *bv, *shk, *ssk, *ssd, *gw;
    void* out; char* ws;
};

__global__ __launch_bounds__(256, 1) void k_main(Params P) {
    asm volatile(
        "; ---- PRO\n"
        "s_load_dwordx16 s[8:23], %0, 0\n"
        "s_load_dwordx8 s[24:31], %0, 64\n"
        "s_load_dwordx4 s[36:39], %0, 96\n"
        "s_mov_b32 s55, %1\n"
        "s_mov_b32 s40, %2\n"
        "v_mbcnt_lo_u32_b32 v96, -1, 0\n"
        "v_mbcnt_hi_u32_b32 v96, -1, v96\n"
        "s_and_b32 s41, s55, 7\n"
        "s_lshr_b32 s42, s55, 3\n"
        "s_lshr_b32 s50, s42, 3\n"
        "s_lshl2_add_u32 s43, s41, s50\n"
        "s_and_b32 s44, s42, 7\n"
        "s_sub_u32 s45, 15, s44\n"
        "s_lshl_b32 s72, s40, 13\n"
        "s_lshl_b32 s73, s40, 12\n"
        "v_lshlrev_b32 v254, 4, v96\n"
        "v_lshrrev_b32 v98, 4, v96\n"
        "v_and_b32 v97, 15, v96\n"
        "v_lshrrev_b32 v100, 3, v96\n"
        "v_and_b32 v101, 7, v96\n"
        "v_xor_b32 v102, v101, v98\n"
        "v_lshlrev_b32 v102, 4, v102\n"
        "v_xor_b32 v103, 64, v102\n"
        "v_lshl_add_u32 v234, v100, 10, v102\n"
        "v_lshl_add_u32 v104, v100, 10, v103\n"
        "v_add_u32 v235, 7168, v104\n"
        "v_add_u32 v236, 14336, v234\n"
        "v_add_u32 v237, 21504, v104\n"
        "v_and_b32 v104, 6, v100\n"
        "v_xor_b32 v104, v104, v101\n"
        "v_lshlrev_b32 v104, 4, v104\n"
        "v_lshl_add_u32 v105, v100, 13, v104\n"
        "v_xor_b32 v106, 4, v100\n"
        "v_lshl_add_u32 v106, v106, 13, v104\n"
        "v_add_u32 v238, 4096, v105\n"
        "v_add_u32 v239, 3200, v105\n"
        "v_add_u32 v240, 67584, v106\n"
        "v_add_u32 v241, 66688, v106\n"
        "s_cmp_ge_u32 s40, 2\n"
        "s_cselect_b32 s50, s45, s44\n"
        "s_and_b32 s51, s40, 1\n"
        "s_lshl1_add_u32 s50, s50, s51\n"
        "s_lshl_b32 s50, s50, 16\n"
        "s_lshl_b32 s51, s40, 17\n"
        "s_lshl_b32 s52, s43, 8\n"
        "s_add_u32 s51, s51, s52\n"
        "s_waitcnt lgkmcnt(0)\n"
        "s_add_u32 s56, s10, s50\n"
        "s_addc_u32 s57, s11, 0\n"
        "s_add_u32 s58, s56, 0x8000\n"
        "s_addc_u32 s59, s57, 0\n"
        "s_add_u32 s60, s14, s51\n"
        "s_addc_u32 s61, s15, 0\n"
        "s_sub_u32 s60, s60, 4096\n"
        "s_subb_u32 s61, s61, 0\n"
        "s_add_u32 s62, s16, s51\n"
        "s_addc_u32 s63, s17, 0\n"
        "s_sub_u32 s62, s62, 4096\n"
        "s_subb_u32 s63, s63, 0\n"
        "s_add_u32 m0, s73, 0x10000\n"
        "s_nop 0\n"
        "global_load_lds_dwordx4 v238, s[60:61] offset:0\n"
        "global_load_lds_dwordx4 v239, s[60:61] offset:1024\n"
        "global_load_lds_dwordx4 v240, s[60:61] offset:2048\n"
        "global_load_lds_dwordx4 v241, s[60:61] offset:3072\n"
        "s_add_u32 m0, s73, 0x14000\n"
        "s_nop 0\n"
        "global_load_lds_dwordx4 v238, s[62:63] offset:0\n"
        "global_load_lds_dwordx4 v239, s[62:63] offset:1024\n"
        "global_load_lds_dwordx4 v240, s[62:63] offset:2048\n"
        "global_load_lds_dwordx4 v241, s[62:63] offset:3072\n"
        "s_add_u32 s60, s60, 0x80000\n"
        "s_addc_u32 s61, s61, 0\n"
        "s_add_u32 s62, s62, 0x80000\n"
        "s_addc_u32 s63, s63, 0\n"
        "s_add_u32 m0, s72, 0x0\n"
        "s_nop 0\n"
        "global_load_lds_dwordx4 v234, s[56:57] offset:0\n"
        "global_load_lds_dwordx4 v235, s[56:57] offset:1024\n"
        "global_load_lds_dwordx4 v236, s[56:57] offset:2048\n"
        "global_load_lds_dwordx4 v237, s[56:57] offset:3072\n"
        "s_add_u32 m0, s72, 0x1000\n"
        "s_nop 0\n"
        "global_load_lds_dwordx4 v234, s[58:59] offset:0\n"
        "global_load_lds_dwordx4 v235, s[58:59] offset:1024\n"
        "global_load_lds_dwordx4 v236, s[58:59] offset:2048\n"
        "global_load_lds_dwordx4 v237, s[58:59] offset:3072\n"
        "s_add_u32 s56, s56, 0x80\n"
        "s_addc_u32 s57, s57, 0\n"
        "s_add_u32 s58, s58, 0x80\n"
        "s_addc_u32 s59, s59, 0\n"
        "s_add_u32 s68, s12, s51\n"
        "s_addc_u32 s69, s13, 0\n"
        "s_sub_u32 s68, s68, 4096\n"
        "s_subb_u32 s69, s69, 0\n"
        "s_add_u32 s48, s38, 0x3900000\n"
        "s_addc_u32 s49, s39, 0\n"
        "v_mov_b32 v107, 0\n"
        "global_load_dwordx2 v[250:251], v107, s[48:49] sc1\n"
        "s_getpc_b64 s[46:47]\n"
        "LPC_%=:\n"
        "s_lshl2_add_u32 s50, s42, s40\n"
        "s_lshl_b32 s50, s50, 10\n"
        "v_lshlrev_b32 v108, 6, v97\n"
        "v_add_u32 v108, s50, v108\n"
        "v_min_u32 v252, LEND_%=-LPC_%=-4, v108\n"
        "global_load_dword v253, v252, s[46:47] offset:0\n"
        "s_add_u32 m0, s73, 0x18000\n"
        "s_nop 0\n"
        "global_load_lds_dwordx4 v238, s[60:61] offset:0\n"
        "global_load_lds_dwordx4 v239, s[60:61] offset:1024\n"
        "global_load_lds_dwordx4 v240, s[60:61] offset:2048\n"
        "global_load_lds_dwordx4 v241, s[60:61] offset:3072\n"
        "s_add_u32 m0, s73, 0x1c000\n"
        "s_nop 0\n"
        "global_load_lds_dwordx4 v238, s[62:63] offset:0\n"
        "global_load_lds_dwordx4 v239, s[62:63] offset:1024\n"
        "global_load_lds_dwordx4 v240, s[62:63] offset:2048\n"
        "global_load_lds_dwordx4 v241, s[62:63] offset:3072\n"
        "s_add_u32 s60, s60, 0x80000\n"
        "s_addc_u32 s61, s61, 0\n"
        "s_add_u32 s62, s62, 0x80000\n"
        "s_addc_u32 s63, s63, 0\n"
        "v_lshrrev_b32 v104, 1, v97\n"
        "v_xor_b32 v104, v104, v98\n"
        "v_lshlrev_b32 v104, 4, v104\n"
        "v_lshl_add_u32 v104, v97, 7, v104\n"
        "v_xor_b32 v105, 64, v104\n"
        "s_lshr_b32 s50, s40, 1\n"
        "s_lshl_b32 s50, s50, 14\n"
        "v_add_u32 v224, s50, v104\n"
        "v_add_u32 v246, s72, v104\n"
        "v_add_u32 v248, 0x10000, v246\n"
        "v_add_u32 v225, s50, v105\n"
        "v_add_u32 v247, s72, v105\n"
        "v_add_u32 v249, 0x10000, v247\n"
        "s_and_b32 s50, s40, 1\n"
        "s_lshl_b32 s50, s50, 10\n"
        "s_add_u32 s50, s50, 0x10000\n"
        "v_lshlrev_b32 v104, 11, v98\n"
        "v_add_u32 v104, s50, v104\n"
        "v_lshrrev_b32 v105, 2, v97\n"
        "v_or_b32 v106, 4, v105\n"
        "v_and_b32 v107, 1, v98\n"
        "v_and_b32 v109, 3, v96\n"
        "v_lshlrev_b32 v110, 2, v107\n"
        "v_xor_b32 v110, v110, v105\n"
        "v_bfe_u32 v111, v110, 1, 2\n"
        "v_xor_b32 v111, 0, v111\n"
        "v_lshlrev_b32 v111, 5, v111\n"
        "v_lshl_add_u32 v110, v110, 7, v111\n"
        "v_lshl_add_u32 v110, v109, 3, v110\n"
        "v_add_u32 v226, v110, v104\n"
        "v_xor_b32 v228, 32, v226\n"
        "v_xor_b32 v230, 64, v226\n"
        "v_xor_b32 v232, 96, v226\n"
        "v_lshlrev_b32 v110, 2, v107\n"
        "v_xor_b32 v110, v110, v106\n"
        "v_bfe_u32 v111, v110, 1, 2\n"
        "v_xor_b32 v111, 0, v111\n"
        "v_lshlrev_b32 v111, 5, v111\n"
        "v_lshl_add_u32 v110, v110, 7, v111\n"
        "v_lshl_add_u32 v110, v109, 3, v110\n"
        "v_add_u32 v227, v110, v104\n"
        "v_xor_b32 v229, 32, v227\n"
        "v_xor_b32 v231, 64, v227\n"
        "v_xor_b32 v233, 96, v227\n"
        "v_add_u32 v216, 0x10000, v226\n"
        "v_add_u32 v217, 0x10000, v227\n"
        "v_add_u32 v218, 0x10000, v228\n"
        "v_add_u32 v219, 0x10000, v229\n"
        "v_add_u32 v220, 0x10000, v230\n"
        "v_add_u32 v221, 0x10000, v231\n"
        "v_add_u32 v222, 0x10000, v232\n"
        "v_add_u32 v223, 0x10000, v233\n"
        "v_lshl_add_u32 v242, v100, 11, v102\n"
        "v_lshl_add_u32 v104, v100, 11, v103\n"
        "v_add_u32 v243, 15360, v104\n"
        "v_add_u32 v244, 129024, v242\n"
        "v_add_u32 v245, 144384, v104\n"
        "s_lshl_b32 s50, s45, 18\n"
        "s_lshl_b32 s51, s40, 15\n"
        "s_add_u32 s50, s50, s51\n"
        "s_add_u32 s64, s8, s50\n"
        "s_addc_u32 s65, s9, 0\n"
        "s_lshl_b32 s50, s44, 18\n"
        "s_add_u32 s50, s50, s51\n"
        "s_add_u32 s66, s8, s50\n"
        "s_addc_u32 s67, s9, 0\n"
        "s_lshl2_add_u32 s82, s55, s40\n"
        "s_lshl_b32 s82, s82, 11\n"
        "v_and_b32 v110, 0xf0, v254\n"
        "v_lshl_add_u32 v110, v110, 3, s82\n"
        "global_load_dword v253, v110, s[10:11] offset:0\n"
        "s_lshl2_add_u32 s82, s42, s40\n"
        "s_lshl3_add_u32 s82, s82, s41\n"
        "s_lshl_b32 s82, s82, 10\n"
        "v_and_b32 v111, 0x70, v254\n"
        "v_lshl_add_u32 v173, v111, 3, s82\n"
        "; ---- FRONT\n"
        "s_add_u32 m0, s72, 0x8000\n"
        "s_waitcnt vmcnt(11)\n"
        "global_load_lds_dwordx4 v234, s[56:57] offset:0\n"
        "s_barrier\n"
        "ds_read_b64_tr_b16 v[32:33], v226 offset:0\n"
        "ds_read_b64_tr_b16 v[34:35], v227 offset:0\n"
        "ds_read_b128 v[0:3], v224 offset:0\n"
        "global_load_lds_dwordx4 v235, s[56:57] offset:1024\n"
        "global_load_lds_dwordx4 v236, s[56:57] offset:2048\n"
        "ds_read_b64_tr_b16 v[36:37], v228 offset:0\n"
        "ds_read_b64_tr_b16 v[38:39], v229 offset:0\n"
        "s_waitcnt lgkmcnt(2)\n"
        "v_mfma_f32_16x16x32_bf16 a[0:3], v[32:35], v[0:3], 0\n"
        "global_load_lds_dwordx4 v237, s[56:57] offset:3072\n"
        "ds_read_b64_tr_b16 v[40:41], v230 offset:0\n"
        "ds_read_b64_tr_b16 v[42:43], v231 offset:0\n"
        "ds_read_b64_tr_b16 v[44:45], v232 offset:0\n"
        "ds_read_b64_tr_b16 v[46:47], v233 offset:0\n"
        "s_add_u32 m0, s72, 0x9000\n"
        "s_waitcnt lgkmcnt(4)\n"
        "global_load_lds_dwordx4 v234, s[58:59] offset:0\n"
        "ds_read_b64_tr_b16 v[48:49], v226 offset:16384\n"
        "v_mfma_f32_16x16x32_bf16 a[4:7], v[36:39], v[0:3], 0\n"
        "ds_read_b64_tr_b16 v[50:51], v227 offset:16384\n"
        "ds_read_b64_tr_b16 v[52:53], v228 offset:16384\n"
        "ds_read_b64_tr_b16 v[54:55], v229 offset:16384\n"
        "ds_read_b64_tr_b16 v[56:57], v230 offset:16384\n"
        "ds_read_b64_tr_b16 v[58:59], v231 offset:16384\n"
        "ds_read_b64_tr_b16 v[60:61], v232 offset:16384\n"
        "ds_read_b64_tr_b16 v[62:63], v233 offset:16384\n"
        "s_waitcnt lgkmcnt(10)\n"
        "v_mfma_f32_16x16x32_bf16 a[8:11], v[40:43], v[0:3], 0\n"
        "ds_read_b128 v[4:7], v224 offset:2048\n"
        "s_waitcnt lgkmcnt(9)\n"
        "v_mfma_f32_16x16x32_bf16 a[12:15], v[44:47], v[0:3], 0\n"
        "ds_read_b128 v[8:11], v224 offset:4096\n"
        "ds_read_b128 v[12:15], v224 offset:6144\n"
        "ds_read_b128 v[16:19], v224 offset:8192\n"
        "ds_read_b128 v[20:23], v224 offset:10240\n"
        "ds_read_b128 v[24:27], v224 offset:12288\n"
        "global_load_lds_dwordx4 v235, s[58:59] offset:1024\n"
        "s_waitcnt lgkmcnt(12)\n"
        "v_mfma_f32_16x16x32_bf16 a[16:19], v[0:3], v[48:51], 0\n"
        "ds_read_b128 v[28:31], v224 offset:14336\n"
        "s_lshl2_add_u32 s50, s43, s40\n"
        "s_waitcnt lgkmcnt(11)\n"
        "v_mfma_f32_16x16x32_bf16 a[20:23], v[0:3], v[52:55], 0\n"
        "ds_read_b64_tr_b16 v[64:65], v226 offset:8192\n"
        "s_lshl_b32 s51, s44, 7\n"
        "s_waitcnt lgkmcnt(10)\n"
        "v_mfma_f32_16x16x32_bf16 a[24:27], v[0:3], v[56:59], 0\n"
        "ds_read_b64_tr_b16 v[66:67], v227 offset:8192\n"
        "s_add_u32 s80, s51, s50\n"
        "s_waitcnt lgkmcnt(9)\n"
        "v_mfma_f32_16x16x32_bf16 a[28:31], v[0:3], v[60:63], 0\n"
        "global_load_lds_dwordx4 v236, s[58:59] offset:2048\n"
        "s_waitcnt lgkmcnt(8)\n"
        "v_mfma_f32_16x16x32_bf16 a[32:35], v[32:35], v[4:7], 0\n"
        "ds_read_b128 v[0:3], v225 offset:0\n"
        "s_add_u32 s56, s56, 0x80\n"
        "s_addc_u32 s57, s57, 0\n"
        "v_mfma_f32_16x16x32_bf16 a[36:39], v[36:39], v[4:7], 0\n"
        "ds_read_b64_tr_b16 v[68:69], v228 offset:8192\n"
        "s_lshl_b32 s51, s80, 11\n"
        "v_mfma_f32_16x16x32_bf16 a[40:43], v[40:43], v[4:7], 0\n"
        "ds_read_b64_tr_b16 v[70:71], v229 offset:8192\n"
        "s_add_u32 s74, s8, s51\n"
        "s_addc_u32 s75, s9, 0\n"
        "v_mfma_f32_16x16x32_bf16 a[44:47], v[44:47], v[4:7], 0\n"
        "global_load_lds_dwordx4 v237, s[58:59] offset:3072\n"
        "s_add_u32 s58, s58, 0x80\n"
        "s_addc_u32 s59, s59, 0\n"
        "v_mfma_f32_16x16x32_bf16 a[48:51], v[4:7], v[48:51], 0\n"
        "s_add_u32 m0, s73, 0x20000\n"
        "ds_read_b64_tr_b16 v[72:73], v230 offset:8192\n"
        "v_mfma_f32_16x16x32_bf16 a[52:55], v[4:7], v[52:55], 0\n"
        "ds_read_b64_tr_b16 v[74:75], v231 offset:8192\n"
        "s_waitcnt lgkmcnt(6)\n"
        "v_mfma_f32_16x16x32_bf16 a[56:59], v[4:7], v[56:59], 0\n"
        "ds_read_b64_tr_b16 v[76:77], v232 offset:8192\n"
        "s_lshl_b32 s51, s45, 7\n"
        "s_add_u32 s81, s51, s50\n"
        "v_mfma_f32_16x16x32_bf16 a[60:63], v[4:7], v[60:63], 0\n"
        "global_load_lds_dwordx4 v238, s[60:61] offset:0\n"
        "ds_read_b128 v[4:7], v225 offset:2048\n"
        "s_lshl_b32 s51, s81, 11\n"
        "v_mfma_f32_16x16x32_bf16 a[64:67], v[32:35], v[8:11], 0\n"
        "ds_read_b64_tr_b16 v[78:79], v233 offset:8192\n"
        "s_lshl_b32 s50, s41, 7\n"
        "s_lshl2_add_u32 s50, s42, s50\n"
        "v_mfma_f32_16x16x32_bf16 a[68:71], v[36:39], v[8:11], 0\n"
        "ds_read_b64_tr_b16 v[80:81], v226 offset:24576\n"
        "s_add_u32 s50, s50, s40\n"
        "s_lshl_b32 s50, s50, 1\n"
        "v_mfma_f32_16x16x32_bf16 a[72:75], v[40:43], v[8:11], 0\n"
        "ds_read_b64_tr_b16 v[82:83], v227 offset:24576\n"
        "s_lshl_b32 s52, s40, 5\n"
        "v_mov_b32 v172, s50\n"
        "v_mfma_f32_16x16x32_bf16 a[76:79], v[44:47], v[8:11], 0\n"
        "global_load_lds_dwordx4 v239, s[60:61] offset:1024\n"
        "ds_read_b64_tr_b16 v[84:85], v228 offset:24576\n"
        "s_lshl_b32 s50, s42, 6\n"
        "v_mfma_f32_16x16x32_bf16 a[80:83], v[8:11], v[48:51], 0\n"
        "ds_read_b64_tr_b16 v[86:87], v229 offset:24576\n"
        "s_lshl_b32 s80, s80, 7\n"
        "v_lshrrev_b32 v166, 4, v254\n"
        "v_mfma_f32_16x16x32_bf16 a[84:87], v[8:11], v[52:55], 0\n"
        "ds_read_b64_tr_b16 v[88:89], v230 offset:24576\n"
        "s_lshl_b32 s81, s81, 7\n"
        "v_lshrrev_b32 v167, 3, v166\n"
        "v_mfma_f32_16x16x32_bf16 a[88:91], v[8:11], v[56:59], 0\n"
        "ds_read_b64_tr_b16 v[90:91], v231 offset:24576\n"
        "v_and_b32 v168, 7, v166\n"
        "v_mfma_f32_16x16x32_bf16 a[92:95], v[8:11], v[60:63], 0\n"
        "global_load_lds_dwordx4 v240, s[60:61] offset:2048\n"
        "ds_read_b128 v[8:11], v225 offset:4096\n"
        "v_mfma_f32_16x16x32_bf16 a[96:99], v[32:35], v[12:15], 0\n"
        "ds_read_b64_tr_b16 v[92:93], v232 offset:24576\n"
        "v_lshl_add_u32 v167, v167, 3, s50\n"
        "v_mfma_f32_16x16x32_bf16 a[100:103], v[36:39], v[12:15], 0\n"
        "ds_read_b64_tr_b16 v[94:95], v233 offset:24576\n"
        "v_lshlrev_b32 v169, 11, v167\n"
        "v_mfma_f32_16x16x32_bf16 a[104:107], v[40:43], v[12:15], 0\n"
        "s_add_u32 s82, s12, 0x0\n"
        "s_addc_u32 s83, s13, 0\n"
        "v_cmp_eq_u32 s[78:79], 0, v167\n"
        "v_mfma_f32_16x16x32_bf16 a[108:111], v[44:47], v[12:15], 0\n"
        "global_load_dwordx4 v[142:145], v254, s[74:75] offset:0\n"
        "v_mov_b32 v171, 6144\n"
        "v_mfma_f32_16x16x32_bf16 a[112:115], v[12:15], v[48:51], 0\n"
        "s_add_u32 s76, s38, 0x3920040\n"
        "s_addc_u32 s77, s39, 0\n"
        "v_cndmask_b32 v170, 0, v171, s[78:79]\n"
        "v_mfma_f32_16x16x32_bf16 a[116:119], v[12:15], v[52:55], 0\n"
        "v_sub_u32 v171, 2040, v167\n"
        "v_lshlrev_b32 v171, 1, v171\n"
        "v_mfma_f32_16x16x32_bf16 a[120:123], v[12:15], v[56:59], 0\n"
        "v_mfma_f32_16x16x32_bf16 a[124:127], v[12:15], v[60:63], 0\n"
        "global_load_lds_dwordx4 v241, s[60:61] offset:3072\n"
        "s_add_u32 m0, s73, 0x24000\n"
        "v_mfma_f32_16x16x32_bf16 a[128:131], v[32:35], v[16:19], 0\n"
        "ds_read_b128 v[12:15], v225 offset:6144\n"
        "s_add_u32 s60, s60, 0x80000\n"
        "s_addc_u32 s61, s61, 0\n"
        "v_mfma_f32_16x16x32_bf16 a[132:135], v[36:39], v[16:19], 0\n"
        "v_mfma_f32_16x16x32_bf16 a[136:139], v[40:43], v[16:19], 0\n"
        "v_mfma_f32_16x16x32_bf16 a[140:143], v[44:47], v[16:19], 0\n"
        "global_load_lds_dwordx4 v238, s[62:63] offset:0\n"
        "v_mfma_f32_16x16x32_bf16 a[144:147], v[16:19], v[48:51], 0\n"
        "v_mfma_f32_16x16x32_bf16 a[148:151], v[16:19], v[52:55], 0\n"
        "v_mfma_f32_16x16x32_bf16 a[152:155], v[16:19], v[56:59], 0\n"
        "v_mfma_f32_16x16x32_bf16 a[156:159], v[16:19], v[60:63], 0\n"
        "global_load_dwordx4 v[146:149], v254, s[74:75] offset:1024\n"
        "ds_read_b128 v[16:19], v225 offset:8192\n"
        "v_mfma_f32_16x16x32_bf16 a[160:163], v[32:35], v[20:23], 0\n"
        "s_add_u32 s74, s8, s51\n"
        "s_addc_u32 s75, s9, 0\n"
        "s_lshl_b32 s51, s41, 7\n"
        "v_mfma_f32_16x16x32_bf16 a[164:167], v[36:39], v[20:23], 0\n"
        "s_add_u32 s51, s51, s52\n"
        "v_lshl_add_u32 v168, v168, 2, s51\n"
        "v_lshl_add_u32 v169, v168, 1, v169\n"
        "v_mfma_f32_16x16x32_bf16 a[168:171], v[40:43], v[20:23], 0\n"
        "v_add_u32 v170, v170, v169\n"
        "v_lshl_add_u32 v171, v168, 12, v171\n"
        "v_mfma_f32_16x16x32_bf16 a[172:175], v[44:47], v[20:23], 0\n"
        "global_load_lds_dwordx4 v239, s[62:63] offset:1024\n"
        "v_lshlrev_b32 v168, 3, v168\n"
        "v_mfma_f32_16x16x32_bf16 a[176:179], v[20:23], v[48:51], 0\n"
        "v_mfma_f32_16x16x32_bf16 a[180:183], v[20:23], v[52:55], 0\n"
        "v_mfma_f32_16x16x32_bf16 a[184:187], v[20:23], v[56:59], 0\n"
        "v_mfma_f32_16x16x32_bf16 a[188:191], v[20:23], v[60:63], 0\n"
        "global_load_lds_dwordx4 v240, s[62:63] offset:2048\n"
        "ds_read_b128 v[20:23], v225 offset:10240\n"
        "v_mfma_f32_16x16x32_bf16 a[192:195], v[32:35], v[24:27], 0\n"
        "v_mfma_f32_16x16x32_bf16 a[196:199], v[36:39], v[24:27], 0\n"
        "v_mfma_f32_16x16x32_bf16 a[200:203], v[40:43], v[24:27], 0\n"
        "v_mfma_f32_16x16x32_bf16 a[204:207], v[44:47], v[24:27], 0\n"
        "global_load_dwordx4 v[150:153], v254, s[74:75] offset:0\n"
        "v_mfma_f32_16x16x32_bf16 a[208:211], v[24:27], v[48:51], 0\n"
        "v_mfma_f32_16x16x32_bf16 a[212:215], v[24:27], v[52:55], 0\n"
        "v_mfma_f32_16x16x32_bf16 a[216:219], v[24:27], v[56:59], 0\n"
        "v_mfma_f32_16x16x32_bf16 a[220:223], v[24:27], v[60:63], 0\n"
        "global_load_lds_dwordx4 v241, s[62:63] offset:3072\n"
        "s_add_u32 s62, s62, 0x80000\n"
        "s_addc_u32 s63, s63, 0\n"
        "v_mfma_f32_16x16x32_bf16 a[224:227], v[32:35], v[28:31], 0\n"
        "ds_read_b128 v[24:27], v225 offset:12288\n"
        "s_add_u32 m0, s72, 0x0\n"
        "v_mfma_f32_16x16x32_bf16 a[228:231], v[36:39], v[28:31], 0\n"
        "v_mfma_f32_16x16x32_bf16 a[232:235], v[40:43], v[28:31], 0\n"
        "s_waitcnt lgkmcnt(1)\n"
        "v_mfma_f32_16x16x32_bf16 a[236:239], v[44:47], v[28:31], 0\n"
        "global_load_dwordx4 v[154:157], v254, s[74:75] offset:1024\n"
        "s_sub_u32 s74, s20, 2048\n"
        "s_subb_u32 s75, s21, 0\n"
        "v_mfma_f32_16x16x32_bf16 a[240:243], v[28:31], v[48:51], 0\n"
        "v_mfma_f32_16x16x32_bf16 a[244:247], v[28:31], v[52:55], 0\n"
        "v_mfma_f32_16x16x32_bf16 a[248:251], v[28:31], v[56:59], 0\n"
        "v_mfma_f32_16x16x32_bf16 a[252:255], v[28:31], v[60:63], 0\n"
        "global_load_dwordx4 v[158:161], v254, s[30:31] offset:0\n"
        "ds_read_b128 v[28:31], v225 offset:14336\n"
        "v_mfma_f32_16x16x32_bf16 a[0:3], v[64:67], v[0:3], a[0:3]\n"
        "s_waitcnt vmcnt(31)\n"
        "v_readfirstlane_b32 s86, v250\n"
        "v_readfirstlane_b32 s87, v251\n"
        "v_mfma_f32_16x16x32_bf16 a[4:7], v[68:71], v[0:3], a[4:7]\n"
        "s_add_u32 s86, s86, 0x1\n"
        "s_addc_u32 s87, s87, 0\n"
        "v_mov_b32 v250, s86\n"
        "v_mfma_f32_16x16x32_bf16 a[8:11], v[72:75], v[0:3], a[8:11]\n"
        "v_mov_b32 v251, s87\n"
        "v_mfma_f32_16x16x32_bf16 a[12:15], v[76:79], v[0:3], a[12:15]\n"
        "global_load_dwordx4 v[162:165], v254, s[30:31] offset:1024\n"
        "v_mfma_f32_16x16x32_bf16 a[16:19], v[0:3], v[80:83], a[16:19]\n"
        "v_mfma_f32_16x16x32_bf16 a[20:23], v[0:3], v[84:87], a[20:23]\n"
        "v_mfma_f32_16x16x32_bf16 a[24:27], v[0:3], v[88:91], a[24:27]\n"
        "v_mfma_f32_16x16x32_bf16 a[28:31], v[0:3], v[92:95], a[28:31]\n"
        "s_waitcnt vmcnt(14) lgkmcnt(0)\n"
        "s_barrier\n"
        "v_mfma_f32_16x16x32_bf16 a[32:35], v[64:67], v[4:7], a[32:35]\n"
        "global_load_lds_dwordx4 v234, s[56:57] offset:0\n"
        "ds_read_b128 v[0:3], v224 offset:32768\n"
        "v_mfma_f32_16x16x32_bf16 a[36:39], v[68:71], v[4:7], a[36:39]\n"
        "ds_read_b64_tr_b16 v[32:33], v226 offset:32768\n"
        "v_mfma_f32_16x16x32_bf16 a[40:43], v[72:75], v[4:7], a[40:43]\n"
        "ds_read_b64_tr_b16 v[34:35], v227 offset:32768\n"
        "v_mfma_f32_16x16x32_bf16 a[44:47], v[76:79], v[4:7], a[44:47]\n"
        "ds_read_b64_tr_b16 v[36:37], v228 offset:32768\n"
        "v_mfma_f32_16x16x32_bf16 a[48:51], v[4:7], v[80:83], a[48:51]\n"
        "global_load_ushort v172, v172, s[28:29]\n"
        "ds_read_b64_tr_b16 v[38:39], v229 offset:32768\n"
        "v_mfma_f32_16x16x32_bf16 a[52:55], v[4:7], v[84:87], a[52:55]\n"
        "global_load_lds_dwordx4 v235, s[56:57] offset:1024\n"
        "ds_read_b64_tr_b16 v[40:41], v230 offset:32768\n"
        "v_mfma_f32_16x16x32_bf16 a[56:59], v[4:7], v[88:91], a[56:59]\n"
        "ds_read_b64_tr_b16 v[42:43], v231 offset:32768\n"
        "v_mfma_f32_16x16x32_bf16 a[60:63], v[4:7], v[92:95], a[60:63]\n"
        "ds_read_b128 v[4:7], v224 offset:34816\n"
        "v_mfma_f32_16x16x32_bf16 a[64:67], v[64:67], v[8:11], a[64:67]\n"
        "ds_read_b64_tr_b16 v[44:45], v232 offset:32768\n"
        "v_mfma_f32_16x16x32_bf16 a[68:71], v[68:71], v[8:11], a[68:71]\n"
        "ds_read_b64_tr_b16 v[46:47], v233 offset:32768\n"
        "global_load_lds_dwordx4 v236, s[56:57] offset:2048\n"
        "v_mfma_f32_16x16x32_bf16 a[72:75], v[72:75], v[8:11], a[72:75]\n"
        "ds_read_b64_tr_b16 v[48:49], v226 offset:49152\n"
        "v_mfma_f32_16x16x32_bf16 a[76:79], v[76:79], v[8:11], a[76:79]\n"
        "ds_read_b64_tr_b16 v[50:51], v227 offset:49152\n"
        "v_mfma_f32_16x16x32_bf16 a[80:83], v[8:11], v[80:83], a[80:83]\n"
        "ds_read_b64_tr_b16 v[52:53], v228 offset:49152\n"
        "v_mfma_f32_16x16x32_bf16 a[84:87], v[8:11], v[84:87], a[84:87]\n"
        "global_load_lds_dwordx4 v237, s[56:57] offset:3072\n"
        "s_add_u32 m0, s72, 0x1000\n"
        "v_mfma_f32_16x16x32_bf16 a[88:91], v[8:11], v[88:91], a[88:91]\n"
        "ds_read_b64_tr_b16 v[54:55], v229 offset:49152\n"
        "s_add_u32 s56, s56, 0x80\n"
        "s_addc_u32 s57, s57, 0\n"
        "v_mfma_f32_16x16x32_bf16 a[92:95], v[8:11], v[92:95], a[92:95]\n"
        "ds_read_b128 v[8:11], v224 offset:36864\n"
        "v_mfma_f32_16x16x32_bf16 a[96:99], v[64:67], v[12:15], a[96:99]\n"
        "ds_read_b64_tr_b16 v[56:57], v230 offset:49152\n"
        "v_mfma_f32_16x16x32_bf16 a[100:103], v[68:71], v[12:15], a[100:103]\n"
        "global_load_lds_dwordx4 v234, s[58:59] offset:0\n"
        "v_mfma_f32_16x16x32_bf16 a[104:107], v[72:75], v[12:15], a[104:107]\n"
        "ds_read_b64_tr_b16 v[58:59], v231 offset:49152\n"
        "v_mfma_f32_16x16x32_bf16 a[108:111], v[76:79], v[12:15], a[108:111]\n"
        "ds_read_b64_tr_b16 v[60:61], v232 offset:49152\n"
        "v_mfma_f32_16x16x32_bf16 a[112:115], v[12:15], v[80:83], a[112:115]\n"
        "ds_read_b64_tr_b16 v[62:63], v233 offset:49152\n"
        "v_mfma_f32_16x16x32_bf16 a[116:119], v[12:15], v[84:87], a[116:119]\n"
        "global_load_lds_dwordx4 v235, s[58:59] offset:1024\n"
        "v_mfma_f32_16x16x32_bf16 a[120:123], v[12:15], v[88:91], a[120:123]\n"
        "v_mfma_f32_16x16x32_bf16 a[124:127], v[12:15], v[92:95], a[124:127]\n"
        "ds_read_b128 v[12:15], v224 offset:38912\n"
        "v_mfma_f32_16x16x32_bf16 a[128:131], v[64:67], v[16:19], a[128:131]\n"
        "v_mfma_f32_16x16x32_bf16 a[132:135], v[68:71], v[16:19], a[132:135]\n"
        "global_load_lds_dwordx4 v236, s[58:59] offset:2048\n"
        "v_mfma_f32_16x16x32_bf16 a[136:139], v[72:75], v[16:19], a[136:139]\n"
        "v_mfma_f32_16x16x32_bf16 a[140:143], v[76:79], v[16:19], a[140:143]\n"
        "v_mfma_f32_16x16x32_bf16 a[144:147], v[16:19], v[80:83], a[144:147]\n"
        "v_mfma_f32_16x16x32_bf16 a[148:151], v[16:19], v[84:87], a[148:151]\n"
        "global_load_dwordx4 v[134:137], v168, s[24:25] offset:0\n"
        "v_mfma_f32_16x16x32_bf16 a[152:155], v[16:19], v[88:91], a[152:155]\n"
        "v_mfma_f32_16x16x32_bf16 a[156:159], v[16:19], v[92:95], a[156:159]\n"
        "ds_read_b128 v[16:19], v224 offset:40960\n"
        "v_mfma_f32_16x16x32_bf16 a[160:163], v[64:67], v[20:23], a[160:163]\n"
        "v_mfma_f32_16x16x32_bf16 a[164:167], v[68:71], v[20:23], a[164:167]\n"
        "global_load_lds_dwordx4 v237, s[58:59] offset:3072\n"
        "s_add_u32 m0, s73, 0x10000\n"
        "v_mfma_f32_16x16x32_bf16 a[168:171], v[72:75], v[20:23], a[168:171]\n"
        "s_add_u32 s58, s58, 0x80\n"
        "s_addc_u32 s59, s59, 0\n"
        "v_mfma_f32_16x16x32_bf16 a[172:175], v[76:79], v[20:23], a[172:175]\n"
        "v_mfma_f32_16x16x32_bf16 a[176:179], v[20:23], v[80:83], a[176:179]\n"
        "v_mfma_f32_16x16x32_bf16 a[180:183], v[20:23], v[84:87], a[180:183]\n"
        "global_load_dwordx4 v[138:141], v168, s[24:25] offset:16\n"
        "v_mfma_f32_16x16x32_bf16 a[184:187], v[20:23], v[88:91], a[184:187]\n"
        "v_mfma_f32_16x16x32_bf16 a[188:191], v[20:23], v[92:95], a[188:191]\n"
        "ds_read_b128 v[20:23], v224 offset:43008\n"
        "v_mfma_f32_16x16x32_bf16 a[192:195], v[64:67], v[24:27], a[192:195]\n"
        "v_mfma_f32_16x16x32_bf16 a[196:199], v[68:71], v[24:27], a[196:199]\n"
        "global_load_lds_dwordx4 v238, s[60:61] offset:0\n"
        "v_mfma_f32_16x16x32_bf16 a[200:203], v[72:75], v[24:27], a[200:203]\n"
        "v_mfma_f32_16x16x32_bf16 a[204:207], v[76:79], v[24:27], a[204:207]\n"
        "v_mfma_f32_16x16x32_bf16 a[208:211], v[24:27], v[80:83], a[208:211]\n"
        "v_mfma_f32_16x16x32_bf16 a[212:215], v[24:27], v[84:87], a[212:215]\n"
        "global_load_lds_dwordx4 v239, s[60:61] offset:1024\n"
        "v_mfma_f32_16x16x32_bf16 a[216:219], v[24:27], v[88:91], a[216:219]\n"
        "v_mfma_f32_16x16x32_bf16 a[220:223], v[24:27], v[92:95], a[220:223]\n"
        "ds_read_b128 v[24:27], v224 offset:45056\n"
        "v_mfma_f32_16x16x32_bf16 a[224:227], v[64:67], v[28:31], a[224:227]\n"
        "ds_read_b64_tr_b16 v[64:65], v226 offset:40960\n"
        "v_mfma_f32_16x16x32_bf16 a[228:231], v[68:71], v[28:31], a[228:231]\n"
        "global_load_dwordx2 v[96:97], v170, s[74:75] offset:-4096\n"
        "v_mfma_f32_16x16x32_bf16 a[232:235], v[72:75], v[28:31], a[232:235]\n"
        "global_load_lds_dwordx4 v240, s[60:61] offset:2048\n"
        "ds_read_b64_tr_b16 v[66:67], v227 offset:40960\n"
        "v_mfma_f32_16x16x32_bf16 a[236:239], v[76:79], v[28:31], a[236:239]\n"
        "ds_read_b64_tr_b16 v[68:69], v228 offset:40960\n"
        "s_waitcnt lgkmcnt(4)\n"
        "v_mfma_f32_16x16x32_bf16 a[240:243], v[28:31], v[80:83], a[240:243]\n"
        "ds_read_b64_tr_b16 v[70:71], v229 offset:40960\n"
        "v_mfma_f32_16x16x32_bf16 a[244:247], v[28:31], v[84:87], a[244:247]\n"
        "ds_read_b64_tr_b16 v[72:73], v230 offset:40960\n"
        "v_mfma_f32_16x16x32_bf16 a[248:251], v[28:31], v[88:91], a[248:251]\n"
        "global_load_lds_dwordx4 v241, s[60:61] offset:3072\n"
        "s_add_u32 m0, s73, 0x14000\n"
        "v_mfma_f32_16x16x32_bf16 a[252:255], v[28:31], v[92:95], a[252:255]\n"
        "ds_read_b128 v[28:31], v224 offset:47104\n"
        "s_add_u32 s60, s60, 0x80000\n"
        "s_addc_u32 s61, s61, 0\n"
        "v_mfma_f32_16x16x32_bf16 a[0:3], v[32:35], v[0:3], a[0:3]\n"
        "ds_read_b64_tr_b16 v[74:75], v231 offset:40960\n"
        "v_mfma_f32_16x16x32_bf16 a[4:7], v[36:39], v[0:3], a[4:7]\n"
        "ds_read_b64_tr_b16 v[76:77], v232 offset:40960\n"
        "v_mfma_f32_16x16x32_bf16 a[8:11], v[40:43], v[0:3], a[8:11]\n"
        "global_load_dwordx2 v[98:99], v170, s[74:75] offset:-2048\n"
        "ds_read_b64_tr_b16 v[78:79], v233 offset:40960\n"
        "v_mfma_f32_16x16x32_bf16 a[12:15], v[44:47], v[0:3], a[12:15]\n"
        "global_load_lds_dwordx4 v238, s[62:63] offset:0\n"
        "ds_read_b64_tr_b16 v[80:81], v226 offset:57344\n"
        "v_mfma_f32_16x16x32_bf16 a[16:19], v[0:3], v[48:51], a[16:19]\n"
        "ds_read_b64_tr_b16 v[82:83], v227 offset:57344\n"
        "v_mfma_f32_16x16x32_bf16 a[20:23], v[0:3], v[52:55], a[20:23]\n"
        "ds_read_b64_tr_b16 v[84:85], v228 offset:57344\n"
        "v_mfma_f32_16x16x32_bf16 a[24:27], v[0:3], v[56:59], a[24:27]\n"
        "ds_read_b64_tr_b16 v[86:87], v229 offset:57344\n"
        "v_mfma_f32_16x16x32_bf16 a[28:31], v[0:3], v[60:63], a[28:31]\n"
        "global_load_lds_dwordx4 v239, s[62:63] offset:1024\n"
        "ds_read_b128 v[0:3], v225 offset:32768\n"
        "v_mfma_f32_16x16x32_bf16 a[32:35], v[32:35], v[4:7], a[32:35]\n"
        "ds_read_b64_tr_b16 v[88:89], v230 offset:57344\n"
        "v_mfma_f32_16x16x32_bf16 a[36:39], v[36:39], v[4:7], a[36:39]\n"
        "ds_read_b64_tr_b16 v[90:91], v231 offset:57344\n"
        "v_mfma_f32_16x16x32_bf16 a[40:43], v[40:43], v[4:7], a[40:43]\n"
        "ds_read_b64_tr_b16 v[92:93], v232 offset:57344\n"
        "v_mfma_f32_16x16x32_bf16 a[44:47], v[44:47], v[4:7], a[44:47]\n"
        "global_load_dwordx2 v[100:101], v170, s[74:75] offset:0\n"
        "ds_read_b64_tr_b16 v[94:95], v233 offset:57344\n"
        "v_mfma_f32_16x16x32_bf16 a[48:51], v[4:7], v[48:51], a[48:51]\n"
        "global_load_lds_dwordx4 v240, s[62:63] offset:2048\n"
        "v_mfma_f32_16x16x32_bf16 a[52:55], v[4:7], v[52:55], a[52:55]\n"
        "v_mfma_f32_16x16x32_bf16 a[56:59], v[4:7], v[56:59], a[56:59]\n"
        "v_mfma_f32_16x16x32_bf16 a[60:63], v[4:7], v[60:63], a[60:63]\n"
        "ds_read_b128 v[4:7], v225 offset:34816\n"
        "v_mfma_f32_16x16x32_bf16 a[64:67], v[32:35], v[8:11], a[64:67]\n"
        "global_load_lds_dwordx4 v241, s[62:63] offset:3072\n"
        "s_add_u32 s62, s62, 0x80000\n"
        "s_addc_u32 s63, s63, 0\n"
        "v_mfma_f32_16x16x32_bf16 a[68:71], v[36:39], v[8:11], a[68:71]\n"
        "s_add_u32 m0, s72, 0x8000\n"
        "v_mfma_f32_16x16x32_bf16 a[72:75], v[40:43], v[8:11], a[72:75]\n"
        "v_mfma_f32_16x16x32_bf16 a[76:79], v[44:47], v[8:11], a[76:79]\n"
        "v_mfma_f32_16x16x32_bf16 a[80:83], v[8:11], v[48:51], a[80:83]\n"
        "global_load_dwordx2 v[102:103], v169, s[74:75] offset:2048\n"
        "s_add_u32 s74, s20, 0x1800\n"
        "s_addc_u32 s75, s21, 0\n"
        "v_mfma_f32_16x16x32_bf16 a[84:87], v[8:11], v[52:55], a[84:87]\n"
        "global_load_dwordx2 v[104:105], v169, s[74:75] offset:-4096\n"
        "v_mfma_f32_16x16x32_bf16 a[88:91], v[8:11], v[56:59], a[88:91]\n"
        "global_load_dwordx2 v[106:107], v169, s[74:75] offset:-2048\n"
        "v_mfma_f32_16x16x32_bf16 a[92:95], v[8:11], v[60:63], a[92:95]\n"
        "ds_read_b128 v[8:11], v225 offset:36864\n"
        "global_load_dwordx2 v[108:109], v169, s[74:75] offset:0\n"
        "v_mfma_f32_16x16x32_bf16 a[96:99], v[32:35], v[12:15], a[96:99]\n"
        "v_mfma_f32_16x16x32_bf16 a[100:103], v[36:39], v[12:15], a[100:103]\n"
        "v_mfma_f32_16x16x32_bf16 a[104:107], v[40:43], v[12:15], a[104:107]\n"
        "v_mfma_f32_16x16x32_bf16 a[108:111], v[44:47], v[12:15], a[108:111]\n"
        "v_mfma_f32_16x16x32_bf16 a[112:115], v[12:15], v[48:51], a[112:115]\n"
        "v_mfma_f32_16x16x32_bf16 a[116:119], v[12:15], v[52:55], a[116:119]\n"
        "v_mfma_f32_16x16x32_bf16 a[120:123], v[12:15], v[56:59], a[120:123]\n"
        "v_mfma_f32_16x16x32_bf16 a[124:127], v[12:15], v[60:63], a[124:127]\n"
        "ds_read_b128 v[12:15], v225 offset:38912\n"
        "global_load_dwordx2 v[110:111], v169, s[74:75] offset:2048\n"
        "v_mfma_f32_16x16x32_bf16 a[128:131], v[32:35], v[16:19], a[128:131]\n"
        "s_add_u32 s74, s20, 0x3800\n"
        "s_addc_u32 s75, s21, 0\n"
        "v_mfma_f32_16x16x32_bf16 a[132:135], v[36:39], v[16:19], a[132:135]\n"
        "v_mfma_f32_16x16x32_bf16 a[136:139], v[40:43], v[16:19], a[136:139]\n"
        "v_mfma_f32_16x16x32_bf16 a[140:143], v[44:47], v[16:19], a[140:143]\n"
        "v_mfma_f32_16x16x32_bf16 a[144:147], v[16:19], v[48:51], a[144:147]\n"
        "v_mfma_f32_16x16x32_bf16 a[148:151], v[16:19], v[52:55], a[148:151]\n"
        "v_mfma_f32_16x16x32_bf16 a[152:155], v[16:19], v[56:59], a[152:155]\n"
        "v_mfma_f32_16x16x32_bf16 a[156:159], v[16:19], v[60:63], a[156:159]\n"
        "ds_read_b128 v[16:19], v225 offset:40960\n"
        "global_load_dwordx2 v[112:113], v169, s[74:75] offset:-4096\n"
        "v_mfma_f32_16x16x32_bf16 a[160:163], v[32:35], v[20:23], a[160:163]\n"
        "v_mfma_f32_16x16x32_bf16 a[164:167], v[36:39], v[20:23], a[164:167]\n"
        "v_mfma_f32_16x16x32_bf16 a[168:171], v[40:43], v[20:23], a[168:171]\n"
        "s_waitcnt lgkmcnt(1)\n"
        "v_mfma_f32_16x16x32_bf16 a[172:175], v[44:47], v[20:23], a[172:175]\n"
        "v_mfma_f32_16x16x32_bf16 a[176:179], v[20:23], v[48:51], a[176:179]\n"
        "v_mfma_f32_16x16x32_bf16 a[180:183], v[20:23], v[52:55], a[180:183]\n"
        "v_mfma_f32_16x16x32_bf16 a[184:187], v[20:23], v[56:59], a[184:187]\n"
        "v_mfma_f32_16x16x32_bf16 a[188:191], v[20:23], v[60:63], a[188:191]\n"
        "ds_read_b128 v[20:23], v225 offset:43008\n"
        "global_load_dwordx2 v[114:115], v169, s[74:75] offset:-2048\n"
        "v_mfma_f32_16x16x32_bf16 a[192:195], v[32:35], v[24:27], a[192:195]\n"
        "v_mfma_f32_16x16x32_bf16 a[196:199], v[36:39], v[24:27], a[196:199]\n"
        "v_mfma_f32_16x16x32_bf16 a[200:203], v[40:43], v[24:27], a[200:203]\n"
        "v_mfma_f32_16x16x32_bf16 a[204:207], v[44:47], v[24:27], a[204:207]\n"
        "v_mfma_f32_16x16x32_bf16 a[208:211], v[24:27], v[48:51], a[208:211]\n"
        "v_mfma_f32_16x16x32_bf16 a[212:215], v[24:27], v[52:55], a[212:215]\n"
        "v_mfma_f32_16x16x32_bf16 a[216:219], v[24:27], v[56:59], a[216:219]\n"
        "v_mfma_f32_16x16x32_bf16 a[220:223], v[24:27], v[60:63], a[220:223]\n"
        "ds_read_b128 v[24:27], v225 offset:45056\n"
        "global_load_dwordx2 v[116:117], v169, s[74:75] offset:0\n"
        "v_mfma_f32_16x16x32_bf16 a[224:227], v[32:35], v[28:31], a[224:227]\n"
        "s_add_u32 s74, s22, 0x1000\n"
        "s_addc_u32 s75, s23, 0\n"
        "s_waitcnt vmcnt(40) lgkmcnt(1)\n"
        "v_mfma_f32_16x16x32_bf16 a[228:231], v[36:39], v[28:31], a[228:231]\n"
        "v_lshlrev_b32 v192, 16, v142\n"
        "v_and_b32 v193, 0xffff0000, v142\n"
        "v_mfma_f32_16x16x32_bf16 a[232:235], v[40:43], v[28:31], a[232:235]\n"
        "v_lshlrev_b32 v194, 16, v143\n"
        "v_and_b32 v195, 0xffff0000, v143\n"
        "v_mfma_f32_16x16x32_bf16 a[236:239], v[44:47], v[28:31], a[236:239]\n"
        "v_mfma_f32_16x16x32_bf16 a[240:243], v[28:31], v[48:51], a[240:243]\n"
        "v_mfma_f32_16x16x32_bf16 a[244:247], v[28:31], v[52:55], a[244:247]\n"
        "v_mfma_f32_16x16x32_bf16 a[248:251], v[28:31], v[56:59], a[248:251]\n"
        "v_mfma_f32_16x16x32_bf16 a[252:255], v[28:31], v[60:63], a[252:255]\n"
        "ds_read_b128 v[28:31], v225 offset:47104\n"
        "global_load_dwordx2 v[118:119], v169, s[74:75] offset:-4096\n"
        "v_mfma_f32_16x16x32_bf16 a[0:3], v[64:67], v[0:3], a[0:3]\n"
        "v_mfma_f32_16x16x32_bf16 a[4:7], v[68:71], v[0:3], a[4:7]\n"
        "v_mfma_f32_16x16x32_bf16 a[8:11], v[72:75], v[0:3], a[8:11]\n"
        "v_mfma_f32_16x16x32_bf16 a[12:15], v[76:79], v[0:3], a[12:15]\n"
        "v_mfma_f32_16x16x32_bf16 a[16:19], v[0:3], v[80:83], a[16:19]\n"
        "v_mfma_f32_16x16x32_bf16 a[20:23], v[0:3], v[84:87], a[20:23]\n"
        "v_mfma_f32_16x16x32_bf16 a[24:27], v[0:3], v[88:91], a[24:27]\n"
        "v_mfma_f32_16x16x32_bf16 a[28:31], v[0:3], v[92:95], a[28:31]\n"
        "s_waitcnt vmcnt(21) lgkmcnt(0)\n"
        "s_barrier\n"
        "v_mfma_f32_16x16x32_bf16 a[32:35], v[64:67], v[4:7], a[32:35]\n"
        "global_load_lds_dwordx4 v234, s[56:57] offset:0\n"
        "ds_read_b128 v[0:3], v224 offset:0\n"
        "v_mfma_f32_16x16x32_bf16 a[36:39], v[68:71], v[4:7], a[36:39]\n"
        "ds_read_b64_tr_b16 v[32:33], v216 offset:0\n"
        "v_mfma_f32_16x16x32_bf16 a[40:43], v[72:75], v[4:7], a[40:43]\n"
        "ds_read_b64_tr_b16 v[34:35], v217 offset:0\n"
        "v_mfma_f32_16x16x32_bf16 a[44:47], v[76:79], v[4:7], a[44:47]\n"
        "ds_read_b64_tr_b16 v[36:37], v218 offset:0\n"
        "v_mfma_f32_16x16x32_bf16 a[48:51], v[4:7], v[80:83], a[48:51]\n"
        "ds_read_b64_tr_b16 v[38:39], v219 offset:0\n"
        "global_load_lds_dwordx4 v235, s[56:57] offset:1024\n"
        "v_mfma_f32_16x16x32_bf16 a[52:55], v[4:7], v[84:87], a[52:55]\n"
        "ds_read_b64_tr_b16 v[40:41], v220 offset:0\n"
        "v_mfma_f32_16x16x32_bf16 a[56:59], v[4:7], v[88:91], a[56:59]\n"
        "ds_read_b64_tr_b16 v[42:43], v221 offset:0\n"
        "v_mfma_f32_16x16x32_bf16 a[60:63], v[4:7], v[92:95], a[60:63]\n"
        "ds_read_b128 v[4:7], v224 offset:2048\n"
        "v_mfma_f32_16x16x32_bf16 a[64:67], v[64:67], v[8:11], a[64:67]\n"
        "global_load_dwordx2 v[120:121], v169, s[74:75] offset:-2048\n"
        "v_mfma_f32_16x16x32_bf16 a[68:71], v[68:71], v[8:11], a[68:71]\n"
        "ds_read_b64_tr_b16 v[44:45], v222 offset:0\n"
        "global_load_lds_dwordx4 v236, s[56:57] offset:2048\n"
        "v_mfma_f32_16x16x32_bf16 a[72:75], v[72:75], v[8:11], a[72:75]\n"
        "ds_read_b64_tr_b16 v[46:47], v223 offset:0\n"
        "v_mfma_f32_16x16x32_bf16 a[76:79], v[76:79], v[8:11], a[76:79]\n"
        "ds_read_b64_tr_b16 v[48:49], v216 offset:16384\n"
        "v_mfma_f32_16x16x32_bf16 a[80:83], v[8:11], v[80:83], a[80:83]\n"
        "ds_read_b64_tr_b16 v[50:51], v217 offset:16384\n"
        "v_mfma_f32_16x16x32_bf16 a[84:87], v[8:11], v[84:87], a[84:87]\n"
        "global_load_lds_dwordx4 v237, s[56:57] offset:3072\n"
        "s_add_u32 m0, s72, 0x9000\n"
        "v_mfma_f32_16x16x32_bf16 a[88:91], v[8:11], v[88:91], a[88:91]\n"
        "ds_read_b64_tr_b16 v[52:53], v218 offset:16384\n"
        "s_add_u32 s56, s56, 0x80\n"
        "s_addc_u32 s57, s57, 0\n"
        "v_mfma_f32_16x16x32_bf16 a[92:95], v[8:11], v[92:95], a[92:95]\n"
        "ds_read_b128 v[8:11], v224 offset:4096\n"
        "v_mfma_f32_16x16x32_bf16 a[96:99], v[64:67], v[12:15], a[96:99]\n"
        "ds_read_b64_tr_b16 v[54:55], v219 offset:16384\n"
        "v_mfma_f32_16x16x32_bf16 a[100:103], v[68:71], v[12:15], a[100:103]\n"
        "global_load_dwordx2 v[122:123], v169, s[74:75] offset:0\n"
        "v_mfma_f32_16x16x32_bf16 a[104:107], v[72:75], v[12:15], a[104:107]\n"
        "global_load_lds_dwordx4 v234, s[58:59] offset:0\n"
        "ds_read_b64_tr_b16 v[56:57], v220 offset:16384\n"
        "v_mfma_f32_16x16x32_bf16 a[108:111], v[76:79], v[12:15], a[108:111]\n"
        "ds_read_b64_tr_b16 v[58:59], v221 offset:16384\n"
        "v_lshlrev_b32 v174, 16, v158\n"
        "v_mfma_f32_16x16x32_bf16 a[112:115], v[12:15], v[80:83], a[112:115]\n"
        "ds_read_b64_tr_b16 v[60:61], v222 offset:16384\n"
        "v_and_b32 v175, 0xffff0000, v158\n"
        "v_mfma_f32_16x16x32_bf16 a[116:119], v[12:15], v[84:87], a[116:119]\n"
        "ds_read_b64_tr_b16 v[62:63], v223 offset:16384\n"
        "v_lshlrev_b32 v176, 16, v159\n"
        "v_mfma_f32_16x16x32_bf16 a[120:123], v[12:15], v[88:91], a[120:123]\n"
        "global_load_lds_dwordx4 v235, s[58:59] offset:1024\n"
        "v_and_b32 v177, 0xffff0000, v159\n"
        "v_mfma_f32_16x16x32_bf16 a[124:127], v[12:15], v[92:95], a[124:127]\n"
        "ds_read_b128 v[12:15], v224 offset:6144\n"
        "v_lshlrev_b32 v178, 16, v160\n"
        "v_mfma_f32_16x16x32_bf16 a[128:131], v[64:67], v[16:19], a[128:131]\n"
        "v_and_b32 v179, 0xffff0000, v160\n"
        "v_lshlrev_b32 v180, 16, v161\n"
        "v_mfma_f32_16x16x32_bf16 a[132:135], v[68:71], v[16:19], a[132:135]\n"
        "v_and_b32 v181, 0xffff0000, v161\n"
        "v_lshlrev_b32 v182, 16, v162\n"
        "v_mfma_f32_16x16x32_bf16 a[136:139], v[72:75], v[16:19], a[136:139]\n"
        "global_load_dwordx2 v[124:125], v169, s[74:75] offset:2048\n"
        "s_add_u32 s74, s22, 0x3000\n"
        "s_addc_u32 s75, s23, 0\n"
        "v_mfma_f32_16x16x32_bf16 a[140:143], v[76:79], v[16:19], a[140:143]\n"
        "global_load_lds_dwordx4 v236, s[58:59] offset:2048\n"
        "v_and_b32 v183, 0xffff0000, v162\n"
        "v_mfma_f32_16x16x32_bf16 a[144:147], v[16:19], v[80:83], a[144:147]\n"
        "v_lshlrev_b32 v184, 16, v163\n"
        "v_and_b32 v185, 0xffff0000, v163\n"
        "v_mfma_f32_16x16x32_bf16 a[148:151], v[16:19], v[84:87], a[148:151]\n"
        "v_lshlrev_b32 v186, 16, v164\n"
        "v_and_b32 v187, 0xffff0000, v164\n"
        "v_mfma_f32_16x16x32_bf16 a[152:155], v[16:19], v[88:91], a[152:155]\n"
        "v_lshlrev_b32 v188, 16, v165\n"
        "v_and_b32 v189, 0xffff0000, v165\n"
        "v_mfma_f32_16x16x32_bf16 a[156:159], v[16:19], v[92:95], a[156:159]\n"
        "global_load_lds_dwordx4 v237, s[58:59] offset:3072\n"
        "ds_read_b128 v[16:19], v224 offset:8192\n"
        "s_add_u32 m0, s73, 0x18000\n"
        "v_mfma_f32_16x16x32_bf16 a[160:163], v[64:67], v[20:23], a[160:163]\n"
        "s_add_u32 s58, s58, 0x80\n"
        "s_addc_u32 s59, s59, 0\n"
        "v_readfirstlane_b32 s54, v172\n"
        "v_mfma_f32_16x16x32_bf16 a[164:167], v[68:71], v[20:23], a[164:167]\n"
        "s_lshl_b32 s54, s54, 16\n"
        "v_mul_f32 v192, v192, v174\n"
        "v_mul_f32 v193, v193, v175\n"
        "v_mfma_f32_16x16x32_bf16 a[168:171], v[72:75], v[20:23], a[168:171]\n"
        "v_cvt_pk_bf16_f32 v192, 0, v192\n"
        "v_cvt_pk_bf16_f32 v193, 0, v193\n"
        "v_mfma_f32_16x16x32_bf16 a[172:175], v[76:79], v[20:23], a[172:175]\n"
        "global_load_dwordx2 v[126:127], v169, s[74:75] offset:-4096\n"
        "v_add_f32 v190, v192, v193\n"
        "v_mfma_f32_16x16x32_bf16 a[176:179], v[20:23], v[80:83], a[176:179]\n"
        "global_load_lds_dwordx4 v238, s[60:61] offset:0\n"
        "v_mul_f32 v194, v194, v176\n"
        "v_mfma_f32_16x16x32_bf16 a[180:183], v[20:23], v[84:87], a[180:183]\n"
        "v_mul_f32 v195, v195, v177\n"
        "v_cvt_pk_bf16_f32 v194, 0, v194\n"
        "v_mfma_f32_16x16x32_bf16 a[184:187], v[20:23], v[88:91], a[184:187]\n"
        "v_cvt_pk_bf16_f32 v195, 0, v195\n"
        "v_add_f32 v194, v194, v195\n"
        "v_mfma_f32_16x16x32_bf16 a[188:191], v[20:23], v[92:95], a[188:191]\n"
        "ds_read_b128 v[20:23], v224 offset:10240\n"
        "v_add_f32 v190, v190, v194\n"
        "v_mfma_f32_16x16x32_bf16 a[192:195], v[64:67], v[24:27], a[192:195]\n"
        "global_load_lds_dwordx4 v239, s[60:61] offset:1024\n"
        "v_lshlrev_b32 v192, 16, v144\n"
        "v_mfma_f32_16x16x32_bf16 a[196:199], v[68:71], v[24:27], a[196:199]\n"
        "v_and_b32 v193, 0xffff0000, v144\n"
        "v_mul_f32 v192, v192, v178\n"
        "v_mfma_f32_16x16x32_bf16 a[200:203], v[72:75], v[24:27], a[200:203]\n"
        "v_mul_f32 v193, v193, v179\n"
        "v_cvt_pk_bf16_f32 v192, 0, v192\n"
        "v_mfma_f32_16x16x32_bf16 a[204:207], v[76:79], v[24:27], a[204:207]\n"
        "v_cvt_pk_bf16_f32 v193, 0, v193\n"
        "v_add_f32 v192, v192, v193\n"
        "v_mfma_f32_16x16x32_bf16 a[208:211], v[24:27], v[80:83], a[208:211]\n"
        "global_load_dwordx2 v[128:129], v169, s[74:75] offset:-2048\n"
        "v_add_f32 v190, v190, v192\n"
        "v_mfma_f32_16x16x32_bf16 a[212:215], v[24:27], v[84:87], a[212:215]\n"
        "global_load_lds_dwordx4 v240, s[60:61] offset:2048\n"
        "v_lshlrev_b32 v194, 16, v145\n"
        "v_mfma_f32_16x16x32_bf16 a[216:219], v[24:27], v[88:91], a[216:219]\n"
        "v_and_b32 v195, 0xffff0000, v145\n"
        "v_mul_f32 v194, v194, v180\n"
        "v_mfma_f32_16x16x32_bf16 a[220:223], v[24:27], v[92:95], a[220:223]\n"
        "ds_read_b128 v[24:27], v224 offset:12288\n"
        "v_mul_f32 v195, v195, v181\n"
        "v_mfma_f32_16x16x32_bf16 a[224:227], v[64:67], v[28:31], a[224:227]\n"
        "ds_read_b64_tr_b16 v[64:65], v216 offset:8192\n"
        "v_cvt_pk_bf16_f32 v194, 0, v194\n"
        "v_mfma_f32_16x16x32_bf16 a[228:231], v[68:71], v[28:31], a[228:231]\n"
        "global_load_lds_dwordx4 v241, s[60:61] offset:3072\n"
        "s_add_u32 m0, s73, 0x1c000\n"
        "v_cvt_pk_bf16_f32 v195, 0, v195\n"
        "v_mfma_f32_16x16x32_bf16 a[232:235], v[72:75], v[28:31], a[232:235]\n"
        "ds_read_b64_tr_b16 v[66:67], v217 offset:8192\n"
        "s_add_u32 s60, s60, 0x80000\n"
        "s_addc_u32 s61, s61, 0\n"
        "v_mfma_f32_16x16x32_bf16 a[236:239], v[76:79], v[28:31], a[236:239]\n"
        "ds_read_b64_tr_b16 v[68:69], v218 offset:8192\n"
        "v_add_f32 v194, v194, v195\n"
        "v_mfma_f32_16x16x32_bf16 a[240:243], v[28:31], v[80:83], a[240:243]\n"
        "ds_read_b64_tr_b16 v[70:71], v219 offset:8192\n"
        "v_add_f32 v190, v190, v194\n"
        "v_mfma_f32_16x16x32_bf16 a[244:247], v[28:31], v[84:87], a[244:247]\n"
        "global_load_dwordx2 v[130:131], v169, s[74:75] offset:0\n"
        "ds_read_b64_tr_b16 v[72:73], v220 offset:8192\n"
        "v_mfma_f32_16x16x32_bf16 a[248:251], v[28:31], v[88:91], a[248:251]\n"
        "global_load_lds_dwordx4 v238, s[62:63] offset:0\n"
        "ds_read_b64_tr_b16 v[74:75], v221 offset:8192\n"
        "v_mfma_f32_16x16x32_bf16 a[252:255], v[28:31], v[92:95], a[252:255]\n"
        "ds_read_b128 v[28:31], v224 offset:14336\n"
        "s_waitcnt lgkmcnt(7)\n"
        "v_mfma_f32_16x16x32_bf16 a[0:3], v[32:35], v[0:3], a[0:3]\n"
        "ds_read_b64_tr_b16 v[76:77], v222 offset:8192\n"
        "v_lshlrev_b32 v192, 16, v146\n"
        "v_mfma_f32_16x16x32_bf16 a[4:7], v[36:39], v[0:3], a[4:7]\n"
        "ds_read_b64_tr_b16 v[78:79], v223 offset:8192\n"
        "v_and_b32 v193, 0xffff0000, v146\n"
        "v_mfma_f32_16x16x32_bf16 a[8:11], v[40:43], v[0:3], a[8:11]\n"
        "global_load_lds_dwordx4 v239, s[62:63] offset:1024\n"
        "ds_read_b64_tr_b16 v[80:81], v216 offset:24576\n"
        "v_mfma_f32_16x16x32_bf16 a[12:15], v[44:47], v[0:3], a[12:15]\n"
        "ds_read_b64_tr_b16 v[82:83], v217 offset:24576\n"
        "v_mul_f32 v192, v192, v182\n"
        "v_mfma_f32_16x16x32_bf16 a[16:19], v[0:3], v[48:51], a[16:19]\n"
        "ds_read_b64_tr_b16 v[84:85], v218 offset:24576\n"
        "v_mul_f32 v193, v193, v183\n"
        "v_mfma_f32_16x16x32_bf16 a[20:23], v[0:3], v[52:55], a[20:23]\n"
        "ds_read_b64_tr_b16 v[86:87], v219 offset:24576\n"
        "v_cvt_pk_bf16_f32 v192, 0, v192\n"
        "v_mfma_f32_16x16x32_bf16 a[24:27], v[0:3], v[56:59], a[24:27]\n"
        "global_load_dwordx2 v[132:133], v169, s[74:75] offset:2048\n"
        "ds_read_b64_tr_b16 v[88:89], v220 offset:24576\n"
        "v_mfma_f32_16x16x32_bf16 a[28:31], v[0:3], v[60:63], a[28:31]\n"
        "global_load_lds_dwordx4 v240, s[62:63] offset:2048\n"
        "ds_read_b128 v[0:3], v225 offset:0\n"
        "v_mfma_f32_16x16x32_bf16 a[32:35], v[32:35], v[4:7], a[32:35]\n"
        "ds_read_b64_tr_b16 v[90:91], v221 offset:24576\n"
        "v_cvt_pk_bf16_f32 v193, 0, v193\n"
        "v_mfma_f32_16x16x32_bf16 a[36:39], v[36:39], v[4:7], a[36:39]\n"
        "ds_read_b64_tr_b16 v[92:93], v222 offset:24576\n"
        "v_add_f32 v192, v192, v193\n"
        "v_mfma_f32_16x16x32_bf16 a[40:43], v[40:43], v[4:7], a[40:43]\n"
        "ds_read_b64_tr_b16 v[94:95], v223 offset:24576\n"
        "v_add_f32 v190, v190, v192\n"
        "v_mfma_f32_16x16x32_bf16 a[44:47], v[44:47], v[4:7], a[44:47]\n"
        "global_load_lds_dwordx4 v241, s[62:63] offset:3072\n"
        "s_add_u32 s62, s62, 0x80000\n"
        "s_addc_u32 s63, s63, 0\n"
        "v_mfma_f32_16x16x32_bf16 a[48:51], v[4:7], v[48:51], a[48:51]\n"
        "s_add_u32 m0, s72, 0x0\n"
        "v_lshlrev_b32 v194, 16, v147\n"
        "v_and_b32 v195, 0xffff0000, v147\n"
        "v_mfma_f32_16x16x32_bf16 a[52:55], v[4:7], v[52:55], a[52:55]\n"
        "v_mul_f32 v194, v194, v184\n"
        "v_mul_f32 v195, v195, v185\n"
        "v_mfma_f32_16x16x32_bf16 a[56:59], v[4:7], v[56:59], a[56:59]\n"
        "v_cvt_pk_bf16_f32 v194, 0, v194\n"
        "v_cvt_pk_bf16_f32 v195, 0, v195\n"
        "v_mfma_f32_16x16x32_bf16 a[60:63], v[4:7], v[60:63], a[60:63]\n"
        "ds_read_b128 v[4:7], v225 offset:2048\n"
        "v_add_f32 v194, v194, v195\n"
        "v_mfma_f32_16x16x32_bf16 a[64:67], v[32:35], v[8:11], a[64:67]\n"
        "v_add_f32 v190, v190, v194\n"
        "v_lshlrev_b32 v192, 16, v148\n"
        "v_mfma_f32_16x16x32_bf16 a[68:71], v[36:39], v[8:11], a[68:71]\n"
        "v_and_b32 v193, 0xffff0000, v148\n"
        "v_mul_f32 v192, v192, v186\n"
        "v_mfma_f32_16x16x32_bf16 a[72:75], v[40:43], v[8:11], a[72:75]\n"
        "v_mul_f32 v193, v193, v187\n"
        "v_cvt_pk_bf16_f32 v192, 0, v192\n"
        "v_mfma_f32_16x16x32_bf16 a[76:79], v[44:47], v[8:11], a[76:79]\n"
        "v_cvt_pk_bf16_f32 v193, 0, v193\n"
        "v_add_f32 v192, v192, v193\n"
        "v_mfma_f32_16x16x32_bf16 a[80:83], v[8:11], v[48:51], a[80:83]\n"
        "v_add_f32 v190, v190, v192\n"
        "v_lshlrev_b32 v194, 16, v149\n"
        "v_mfma_f32_16x16x32_bf16 a[84:87], v[8:11], v[52:55], a[84:87]\n"
        "v_and_b32 v195, 0xffff0000, v149\n"
        "v_mul_f32 v194, v194, v188\n"
        "v_mfma_f32_16x16x32_bf16 a[88:91], v[8:11], v[56:59], a[88:91]\n"
        "s_waitcnt vmcnt(40) lgkmcnt(1)\n"
        "v_mul_f32 v195, v195, v189\n"
        "v_cvt_pk_bf16_f32 v194, 0, v194\n"
        "v_mfma_f32_16x16x32_bf16 a[92:95], v[8:11], v[60:63], a[92:95]\n"
        "ds_read_b128 v[8:11], v225 offset:4096\n"
        "v_cvt_pk_bf16_f32 v195, 0, v195\n"
        "v_mfma_f32_16x16x32_bf16 a[96:99], v[32:35], v[12:15], a[96:99]\n"
        "v_add_f32 v194, v194, v195\n"
        "v_add_f32 v190, v190, v194\n"
        "v_mfma_f32_16x16x32_bf16 a[100:103], v[36:39], v[12:15], a[100:103]\n"
        "v_lshlrev_b32 v192, 16, v150\n"
        "v_and_b32 v193, 0xffff0000, v150\n"
        "v_mfma_f32_16x16x32_bf16 a[104:107], v[40:43], v[12:15], a[104:107]\n"
        "v_mul_f32 v192, v192, v174\n"
        "v_mul_f32 v193, v193, v175\n"
        "v_mfma_f32_16x16x32_bf16 a[108:111], v[44:47], v[12:15], a[108:111]\n"
        "v_cvt_pk_bf16_f32 v192, 0, v192\n"
        "v_cvt_pk_bf16_f32 v193, 0, v193\n"
        "v_mfma_f32_16x16x32_bf16 a[112:115], v[12:15], v[48:51], a[112:115]\n"
        "v_add_f32 v191, v192, v193\n"
        "v_lshlrev_b32 v194, 16, v151\n"
        "v_mfma_f32_16x16x32_bf16 a[116:119], v[12:15], v[52:55], a[116:119]\n"
        "v_and_b32 v195, 0xffff0000, v151\n"
        "v_mul_f32 v194, v194, v176\n"
        "v_mfma_f32_16x16x32_bf16 a[120:123], v[12:15], v[56:59], a[120:123]\n"
        "s_waitcnt vmcnt(37) lgkmcnt(1)\n"
        "v_mul_f32 v195, v195, v177\n"
        "v_cvt_pk_bf16_f32 v194, 0, v194\n"
        "v_mfma_f32_16x16x32_bf16 a[124:127], v[12:15], v[60:63], a[124:127]\n"
        "ds_read_b128 v[12:15], v225 offset:6144\n"
        "v_cvt_pk_bf16_f32 v195, 0, v195\n"
        "v_mfma_f32_16x16x32_bf16 a[128:131], v[32:35], v[16:19], a[128:131]\n"
        "v_add_f32 v194, v194, v195\n"
        "v_add_f32 v191, v191, v194\n"
        "v_mfma_f32_16x16x32_bf16 a[132:135], v[36:39], v[16:19], a[132:135]\n"
        "v_lshlrev_b32 v192, 16, v152\n"
        "v_and_b32 v193, 0xffff0000, v152\n"
        "v_mfma_f32_16x16x32_bf16 a[136:139], v[40:43], v[16:19], a[136:139]\n"
        "v_mul_f32 v192, v192, v178\n"
        "v_mul_f32 v193, v193, v179\n"
        "v_mfma_f32_16x16x32_bf16 a[140:143], v[44:47], v[16:19], a[140:143]\n"
        "v_cvt_pk_bf16_f32 v192, 0, v192\n"
        "v_cvt_pk_bf16_f32 v193, 0, v193\n"
        "v_mfma_f32_16x16x32_bf16 a[144:147], v[16:19], v[48:51], a[144:147]\n"
        "v_add_f32 v192, v192, v193\n"
        "v_add_f32 v191, v191, v192\n"
        "v_mfma_f32_16x16x32_bf16 a[148:151], v[16:19], v[52:55], a[148:151]\n"
        "v_lshlrev_b32 v194, 16, v153\n"
        "v_and_b32 v195, 0xffff0000, v153\n"
        "v_mfma_f32_16x16x32_bf16 a[152:155], v[16:19], v[56:59], a[152:155]\n"
        "v_mul_f32 v194, v194, v180\n"
        "v_mul_f32 v195, v195, v181\n"
        "v_mfma_f32_16x16x32_bf16 a[156:159], v[16:19], v[60:63], a[156:159]\n"
        "ds_read_b128 v[16:19], v225 offset:8192\n"
        "global_load_dword v253, v173, s[82:83] offset:0\n"
        "v_mfma_f32_16x16x32_bf16 a[160:163], v[32:35], v[20:23], a[160:163]\n"
        "s_waitcnt vmcnt(35) lgkmcnt(1)\n"
        "v_cvt_pk_bf16_f32 v194, 0, v194\n"
        "v_cvt_pk_bf16_f32 v195, 0, v195\n"
        "v_mfma_f32_16x16x32_bf16 a[164:167], v[36:39], v[20:23], a[164:167]\n"
        "s_add_u32 s82, s12, 0x100000\n"
        "s_addc_u32 s83, s13, 0\n"
        "v_add_f32 v194, v194, v195\n"
        "v_mfma_f32_16x16x32_bf16 a[168:171], v[40:43], v[20:23], a[168:171]\n"
        "v_add_f32 v191, v191, v194\n"
        "v_lshlrev_b32 v192, 16, v154\n"
        "v_mfma_f32_16x16x32_bf16 a[172:175], v[44:47], v[20:23], a[172:175]\n"
        "v_and_b32 v193, 0xffff0000, v154\n"
        "v_mul_f32 v192, v192, v182\n"
        "v_mfma_f32_16x16x32_bf16 a[176:179], v[20:23], v[48:51], a[176:179]\n"
        "v_mul_f32 v193, v193, v183\n"
        "v_cvt_pk_bf16_f32 v192, 0, v192\n"
        "v_mfma_f32_16x16x32_bf16 a[180:183], v[20:23], v[52:55], a[180:183]\n"
        "v_cvt_pk_bf16_f32 v193, 0, v193\n"
        "v_add_f32 v192, v192, v193\n"
        "v_mfma_f32_16x16x32_bf16 a[184:187], v[20:23], v[56:59], a[184:187]\n"
        "v_add_f32 v191, v191, v192\n"
        "v_lshlrev_b32 v194, 16, v155\n"
        "v_mfma_f32_16x16x32_bf16 a[188:191], v[20:23], v[60:63], a[188:191]\n"
        "ds_read_b128 v[20:23], v225 offset:10240\n"
        "v_and_b32 v195, 0xffff0000, v155\n"
        "v_mfma_f32_16x16x32_bf16 a[192:195], v[32:35], v[24:27], a[192:195]\n"
        "s_waitcnt vmcnt(32) lgkmcnt(1)\n"
        "v_mul_f32 v194, v194, v184\n"
        "v_mul_f32 v195, v195, v185\n"
        "v_mfma_f32_16x16x32_bf16 a[196:199], v[36:39], v[24:27], a[196:199]\n"
        "s_waitcnt vmcnt(31)\n"
        "v_cvt_pk_bf16_f32 v194, 0, v194\n"
        "v_cvt_pk_bf16_f32 v195, 0, v195\n"
        "v_mfma_f32_16x16x32_bf16 a[200:203], v[40:43], v[24:27], a[200:203]\n"
        "s_waitcnt vmcnt(30)\n"
        "v_add_f32 v194, v194, v195\n"
        "v_add_f32 v191, v191, v194\n"
        "v_mfma_f32_16x16x32_bf16 a[204:207], v[44:47], v[24:27], a[204:207]\n"
        "v_lshlrev_b32 v192, 16, v156\n"
        "v_and_b32 v193, 0xffff0000, v156\n"
        "v_mfma_f32_16x16x32_bf16 a[208:211], v[24:27], v[48:51], a[208:211]\n"
        "s_waitcnt vmcnt(29)\n"
        "v_mul_f32 v192, v192, v186\n"
        "v_mul_f32 v193, v193, v187\n"
        "v_mfma_f32_16x16x32_bf16 a[212:215], v[24:27], v[52:55], a[212:215]\n"
        "v_cvt_pk_bf16_f32 v192, 0, v192\n"
        "v_cvt_pk_bf16_f32 v193, 0, v193\n"
        "v_mfma_f32_16x16x32_bf16 a[216:219], v[24:27], v[56:59], a[216:219]\n"
        "v_add_f32 v192, v192, v193\n"
        "v_add_f32 v191, v191, v192\n"
        "v_mfma_f32_16x16x32_bf16 a[220:223], v[24:27], v[60:63], a[220:223]\n"
        "ds_read_b128 v[24:27], v225 offset:12288\n"
        "v_lshlrev_b32 v194, 16, v157\n"
        "v_mfma_f32_16x16x32_bf16 a[224:227], v[32:35], v[28:31], a[224:227]\n"
        "v_and_b32 v195, 0xffff0000, v157\n"
        "v_mul_f32 v194, v194, v188\n"
        "v_mfma_f32_16x16x32_bf16 a[228:231], v[36:39], v[28:31], a[228:231]\n"
        "v_mul_f32 v195, v195, v189\n"
        "v_cvt_pk_bf16_f32 v194, 0, v194\n"
        "v_mfma_f32_16x16x32_bf16 a[232:235], v[40:43], v[28:31], a[232:235]\n"
        "v_cvt_pk_bf16_f32 v195, 0, v195\n"
        "v_add_f32 v194, v194, v195\n"
        "v_mfma_f32_16x16x32_bf16 a[236:239], v[44:47], v[28:31], a[236:239]\n"
        "v_add_f32 v191, v191, v194\n"
        "v_add_f32_dpp v190, v190, v190 row_shr:1 row_mask:0xf bank_mask:0xf bound_ctrl:1\n"
        "v_mfma_f32_16x16x32_bf16 a[240:243], v[28:31], v[48:51], a[240:243]\n"
        "s_waitcnt vmcnt(28) lgkmcnt(1)\n"
        "v_add_f32_dpp v191, v191, v191 row_shr:1 row_mask:0xf bank_mask:0xf bound_ctrl:1\n"
        "v_add_f32_dpp v190, v190, v190 row_shr:2 row_mask:0xf bank_mask:0xf bound_ctrl:1\n"
        "v_mfma_f32_16x16x32_bf16 a[244:247], v[28:31], v[52:55], a[244:247]\n"
        "v_add_f32_dpp v191, v191, v191 row_shr:2 row_mask:0xf bank_mask:0xf bound_ctrl:1\n"
        "v_add_f32_dpp v190, v190, v190 row_shr:4 row_mask:0xf bank_mask:0xf bound_ctrl:1\n"
        "v_mfma_f32_16x16x32_bf16 a[248:251], v[28:31], v[56:59], a[248:251]\n"
        "v_add_f32_dpp v191, v191, v191 row_shr:4 row_mask:0xf bank_mask:0xf bound_ctrl:1\n"
        "v_add_f32_dpp v190, v190, v190 row_shr:8 row_mask:0xf bank_mask:0xf bound_ctrl:1\n"
        "v_mfma_f32_16x16x32_bf16 a[252:255], v[28:31], v[60:63], a[252:255]\n"
        "ds_read_b128 v[28:31], v225 offset:14336\n"
        "v_add_f32_dpp v191, v191, v191 row_shr:8 row_mask:0xf bank_mask:0xf bound_ctrl:1\n"
        "v_mfma_f32_16x16x32_bf16 a[0:3], v[64:67], v[0:3], a[0:3]\n"
        "v_add_f32_dpp v190, v190, v190 row_bcast:15 row_mask:0xa bank_mask:0xf bound_ctrl:1\n"
        "v_add_f32_dpp v191, v191, v191 row_bcast:15 row_mask:0xa bank_mask:0xf bound_ctrl:1\n"
        "v_mfma_f32_16x16x32_bf16 a[4:7], v[68:71], v[0:3], a[4:7]\n"
        "v_add_f32_dpp v190, v190, v190 row_bcast:31 row_mask:0xc bank_mask:0xf bound_ctrl:1\n"
        "v_add_f32_dpp v191, v191, v191 row_bcast:31 row_mask:0xc bank_mask:0xf bound_ctrl:1\n"
        "v_mfma_f32_16x16x32_bf16 a[8:11], v[72:75], v[0:3], a[8:11]\n"
        "v_cvt_pk_bf16_f32 v190, 0, v190\n"
        "v_mul_f32 v190, 0xbfb8aa3b, v190\n"
        "v_mfma_f32_16x16x32_bf16 a[12:15], v[76:79], v[0:3], a[12:15]\n"
        "v_exp_f32 v190, v190\n"
        "v_mfma_f32_16x16x32_bf16 a[16:19], v[0:3], v[80:83], a[16:19]\n"
        "s_waitcnt vmcnt(27) lgkmcnt(1)\n"
        "v_add_f32 v190, 1.0, v190\n"
        "v_lshrrev_b32 v192, 2, v254\n"
        "v_mfma_f32_16x16x32_bf16 a[20:23], v[0:3], v[84:87], a[20:23]\n"
        "v_rcp_f32 v190, v190\n"
        "v_mfma_f32_16x16x32_bf16 a[24:27], v[0:3], v[88:91], a[24:27]\n"
        "v_cvt_pk_bf16_f32 v190, 0, v190\n"
        "v_add_u32 v192, s80, v192\n"
        "v_mfma_f32_16x16x32_bf16 a[28:31], v[0:3], v[92:95], a[28:31]\n"
        "s_waitcnt vmcnt(13) lgkmcnt(0)\n"
        "s_barrier\n"
        "v_mfma_f32_16x16x32_bf16 a[32:35], v[64:67], v[4:7], a[32:35]\n"
        "global_load_lds_dwordx4 v234, s[56:57] offset:0\n"
        "ds_read_b128 v[0:3], v224 offset:32768\n"
        "v_mfma_f32_16x16x32_bf16 a[36:39], v[68:71], v[4:7], a[36:39]\n"
        "ds_read_b64_tr_b16 v[32:33], v226 offset:0\n"
        "v_mfma_f32_16x16x32_bf16 a[40:43], v[72:75], v[4:7], a[40:43]\n"
        "ds_read_b64_tr_b16 v[34:35], v227 offset:0\n"
        "v_mfma_f32_16x16x32_bf16 a[44:47], v[76:79], v[4:7], a[44:47]\n"
        "ds_read_b64_tr_b16 v[36:37], v228 offset:0\n"
        "v_mfma_f32_16x16x32_bf16 a[48:51], v[4:7], v[80:83], a[48:51]\n"
        "ds_read_b64_tr_b16 v[38:39], v229 offset:0\n"
        "global_load_lds_dwordx4 v235, s[56:57] offset:1024\n"
        "v_mfma_f32_16x16x32_bf16 a[52:55], v[4:7], v[84:87], a[52:55]\n"
        "ds_read_b64_tr_b16 v[40:41], v230 offset:0\n"
        "v_mfma_f32_16x16x32_bf16 a[56:59], v[4:7], v[88:91], a[56:59]\n"
        "ds_read_b64_tr_b16 v[42:43], v231 offset:0\n"
        "v_mfma_f32_16x16x32_bf16 a[60:63], v[4:7], v[92:95], a[60:63]\n"
        "ds_read_b128 v[4:7], v224 offset:34816\n"
        "v_mfma_f32_16x16x32_bf16 a[64:67], v[64:67], v[8:11], a[64:67]\n"
        "ds_read_b64_tr_b16 v[44:45], v232 offset:0\n"
        "global_load_lds_dwordx4 v236, s[56:57] offset:2048\n"
        "v_mfma_f32_16x16x32_bf16 a[68:71], v[68:71], v[8:11], a[68:71]\n"
        "ds_read_b64_tr_b16 v[46:47], v233 offset:0\n"
        "v_readlane_b32 s84, v190, 63\n"
        "v_mfma_f32_16x16x32_bf16 a[72:75], v[72:75], v[8:11], a[72:75]\n"
        "ds_read_b64_tr_b16 v[48:49], v226 offset:16384\n"
        "v_mov_b32 v190, s84\n"
        "v_mfma_f32_16x16x32_bf16 a[76:79], v[76:79], v[8:11], a[76:79]\n"
        "ds_read_b64_tr_b16 v[50:51], v227 offset:16384\n"
        "v_cvt_pk_bf16_f32 v191, 0, v191\n"
        "v_mfma_f32_16x16x32_bf16 a[80:83], v[8:11], v[80:83], a[80:83]\n"
        "global_load_lds_dwordx4 v237, s[56:57] offset:3072\n"
        "s_add_u32 m0, s72, 0x1000\n"
        "v_mfma_f32_16x16x32_bf16 a[84:87], v[8:11], v[84:87], a[84:87]\n"
        "ds_read_b64_tr_b16 v[52:53], v228 offset:16384\n"
        "s_add_u32 s56, s56, 0x80\n"
        "s_addc_u32 s57, s57, 0\n"
        "v_mfma_f32_16x16x32_bf16 a[88:91], v[8:11], v[88:91], a[88:91]\n"
        "ds_read_b64_tr_b16 v[54:55], v229 offset:16384\n"
        "v_mul_f32 v191, 0xbfb8aa3b, v191\n"
        "v_mfma_f32_16x16x32_bf16 a[92:95], v[8:11], v[92:95], a[92:95]\n"
        "ds_read_b128 v[8:11], v224 offset:36864\n"
        "v_lshrrev_b32 v193, 2, v254\n"
        "v_mfma_f32_16x16x32_bf16 a[96:99], v[64:67], v[12:15], a[96:99]\n"
        "global_load_lds_dwordx4 v234, s[58:59] offset:0\n"
        "v_mfma_f32_16x16x32_bf16 a[100:103], v[68:71], v[12:15], a[100:103]\n"
        "ds_read_b64_tr_b16 v[56:57], v230 offset:16384\n"
        "v_add_u32 v193, s81, v193\n"
        "v_mfma_f32_16x16x32_bf16 a[104:107], v[72:75], v[12:15], a[104:107]\n"
        "ds_read_b64_tr_b16 v[58:59], v231 offset:16384\n"
        "v_cndmask_b32 v100, v100, 0, s[78:79]\n"
        "v_mfma_f32_16x16x32_bf16 a[108:111], v[76:79], v[12:15], a[108:111]\n"
        "ds_read_b64_tr_b16 v[60:61], v232 offset:16384\n"
        "v_cndmask_b32 v96, v96, 0, s[78:79]\n"
        "v_mfma_f32_16x16x32_bf16 a[112:115], v[12:15], v[80:83], a[112:115]\n"
        "global_load_lds_dwordx4 v235, s[58:59] offset:1024\n"
        "v_mfma_f32_16x16x32_bf16 a[116:119], v[12:15], v[84:87], a[116:119]\n"
        "ds_read_b64_tr_b16 v[62:63], v233 offset:16384\n"
        "v_cndmask_b32 v98, v98, 0, s[78:79]\n"
        "v_mfma_f32_16x16x32_bf16 a[120:123], v[12:15], v[88:91], a[120:123]\n"
        "v_exp_f32 v191, v191\n"
        "v_mfma_f32_16x16x32_bf16 a[124:127], v[12:15], v[92:95], a[124:127]\n"
        "ds_read_b128 v[12:15], v224 offset:38912\n"
        "v_add_f32 v191, 1.0, v191\n"
        "v_mfma_f32_16x16x32_bf16 a[128:131], v[64:67], v[16:19], a[128:131]\n"
        "global_load_lds_dwordx4 v236, s[58:59] offset:2048\n"
        "v_mfma_f32_16x16x32_bf16 a[132:135], v[68:71], v[16:19], a[132:135]\n"
        "v_rcp_f32 v191, v191\n"
        "v_mfma_f32_16x16x32_bf16 a[136:139], v[72:75], v[16:19], a[136:139]\n"
        "v_cvt_pk_bf16_f32 v191, 0, v191\n"
        "v_lshlrev_b32 v177, 16, v102\n"
        "v_mfma_f32_16x16x32_bf16 a[140:143], v[76:79], v[16:19], a[140:143]\n"
        "v_readlane_b32 s85, v191, 63\n"
        "v_lshlrev_b32 v178, 16, v104\n"
        "v_mfma_f32_16x16x32_bf16 a[144:147], v[16:19], v[80:83], a[144:147]\n"
        "global_load_lds_dwordx4 v237, s[58:59] offset:3072\n"
        "s_add_u32 m0, s73, 0x20000\n"
        "v_mfma_f32_16x16x32_bf16 a[148:151], v[16:19], v[84:87], a[148:151]\n"
        "s_add_u32 s58, s58, 0x80\n"
        "s_addc_u32 s59, s59, 0\n"
        "v_mov_b32 v191, s85\n"
        "v_mfma_f32_16x16x32_bf16 a[152:155], v[16:19], v[88:91], a[152:155]\n"
        "v_lshlrev_b32 v187, 16, v135\n"
        "v_and_b32 v188, 0xffff0000, v135\n"
        "v_mfma_f32_16x16x32_bf16 a[156:159], v[16:19], v[92:95], a[156:159]\n"
        "ds_read_b128 v[16:19], v224 offset:40960\n"
        "v_lshlrev_b32 v176, 16, v100\n"
        "v_mfma_f32_16x16x32_bf16 a[160:163], v[64:67], v[20:23], a[160:163]\n"
        "global_load_lds_dwordx4 v238, s[60:61] offset:0\n"
        "v_mfma_f32_16x16x32_bf16 a[164:167], v[68:71], v[20:23], a[164:167]\n"
        "v_lshlrev_b32 v174, 16, v96\n"
        "v_lshlrev_b32 v175, 16, v98\n"
        "v_mfma_f32_16x16x32_bf16 a[168:171], v[72:75], v[20:23], a[168:171]\n"
        "v_mul_f32 v194, v177, v187\n"
        "v_mul_f32 v196, v178, v187\n"
        "v_mfma_f32_16x16x32_bf16 a[172:175], v[76:79], v[20:23], a[172:175]\n"
        "v_mul_f32 v195, v177, v188\n"
        "v_mul_f32 v189, v174, v188\n"
        "v_mfma_f32_16x16x32_bf16 a[176:179], v[20:23], v[80:83], a[176:179]\n"
        "global_load_lds_dwordx4 v239, s[60:61] offset:1024\n"
        "v_mfma_f32_16x16x32_bf16 a[180:183], v[20:23], v[84:87], a[180:183]\n"
        "v_cvt_pk_bf16_f32 v194, 0, v194\n"
        "v_cvt_pk_bf16_f32 v196, 0, v196\n"
        "v_mfma_f32_16x16x32_bf16 a[184:187], v[20:23], v[88:91], a[184:187]\n"
        "v_cvt_pk_bf16_f32 v195, 0, v195\n"
        "v_lshlrev_b32 v179, 16, v106\n"
        "v_mfma_f32_16x16x32_bf16 a[188:191], v[20:23], v[92:95], a[188:191]\n"
        "ds_read_b128 v[20:23], v224 offset:43008\n"
        "v_and_b32 v186, 0xffff0000, v134\n"
        "v_mfma_f32_16x16x32_bf16 a[192:195], v[64:67], v[24:27], a[192:195]\n"
        "global_load_lds_dwordx4 v240, s[60:61] offset:2048\n"
        "v_mfma_f32_16x16x32_bf16 a[196:199], v[68:71], v[24:27], a[196:199]\n"
        "v_cvt_pk_bf16_f32 v189, 0, v189\n"
        "v_add_f32 v195, v195, v196\n"
        "v_mfma_f32_16x16x32_bf16 a[200:203], v[72:75], v[24:27], a[200:203]\n"
        "v_mul_f32 v196, v179, v186\n"
        "v_cvt_pk_bf16_f32 v195, 0, v195\n"
        "v_mfma_f32_16x16x32_bf16 a[204:207], v[76:79], v[24:27], a[204:207]\n"
        "v_cvt_pk_bf16_f32 v196, 0, v196\n"
        "v_lshlrev_b32 v180, 16, v108\n"
        "v_mfma_f32_16x16x32_bf16 a[208:211], v[24:27], v[80:83], a[208:211]\n"
        "global_load_lds_dwordx4 v241, s[60:61] offset:3072\n"
        "s_add_u32 m0, s73, 0x24000\n"
        "v_mfma_f32_16x16x32_bf16 a[212:215], v[24:27], v[84:87], a[212:215]\n"
        "s_add_u32 s60, s60, 0x80000\n"
        "s_addc_u32 s61, s61, 0\n"
        "v_lshlrev_b32 v185, 16, v134\n"
        "v_mfma_f32_16x16x32_bf16 a[216:219], v[24:27], v[88:91], a[216:219]\n"
        "v_add_f32 v195, v195, v196\n"
        "v_mul_f32 v196, v180, v185\n"
        "v_mfma_f32_16x16x32_bf16 a[220:223], v[24:27], v[92:95], a[220:223]\n"
        "ds_read_b128 v[24:27], v224 offset:45056\n"
        "v_cvt_pk_bf16_f32 v195, 0, v195\n"
        "v_mfma_f32_16x16x32_bf16 a[224:227], v[64:67], v[28:31], a[224:227]\n"
        "global_load_lds_dwordx4 v238, s[62:63] offset:0\n"
        "v_mfma_f32_16x16x32_bf16 a[228:231], v[68:71], v[28:31], a[228:231]\n"
        "ds_read_b64_tr_b16 v[64:65], v226 offset:8192\n"
        "v_cvt_pk_bf16_f32 v196, 0, v196\n"
        "v_mfma_f32_16x16x32_bf16 a[232:235], v[72:75], v[28:31], a[232:235]\n"
        "ds_read_b64_tr_b16 v[66:67], v227 offset:8192\n"
        "v_add_f32 v195, v195, v196\n"
        "v_mfma_f32_16x16x32_bf16 a[236:239], v[76:79], v[28:31], a[236:239]\n"
        "ds_read_b64_tr_b16 v[68:69], v228 offset:8192\n"
        "v_cvt_pk_bf16_f32 v195, 0, v195\n"
        "v_mfma_f32_16x16x32_bf16 a[240:243], v[28:31], v[80:83], a[240:243]\n"
        "global_load_lds_dwordx4 v239, s[62:63] offset:1024\n"
        "v_mfma_f32_16x16x32_bf16 a[244:247], v[28:31], v[84:87], a[244:247]\n"
        "ds_read_b64_tr_b16 v[70:71], v229 offset:8192\n"
        "v_lshlrev_b32 v181, 16, v110\n"
        "v_mfma_f32_16x16x32_bf16 a[248:251], v[28:31], v[88:91], a[248:251]\n"
        "ds_read_b64_tr_b16 v[72:73], v230 offset:8192\n"
        "v_lshlrev_b32 v196, 16, v124\n"
        "v_mfma_f32_16x16x32_bf16 a[252:255], v[28:31], v[92:95], a[252:255]\n"
        "ds_read_b128 v[28:31], v224 offset:47104\n"
        "s_waitcnt lgkmcnt(6)\n"
        "v_mfma_f32_16x16x32_bf16 a[0:3], v[32:35], v[0:3], a[0:3]\n"
        "global_load_dword v253, v173, s[82:83] offset:0\n"
        "v_mfma_f32_16x16x32_bf16 a[4:7], v[36:39], v[0:3], a[4:7]\n"
        "s_mov_b64 exec, 0xffff\n"
        "global_store_dword v192, v190, s[76:77] sc1\n"
        "s_mov_b64 exec, -1\n"
        "v_mfma_f32_16x16x32_bf16 a[8:11], v[40:43], v[0:3], a[8:11]\n"
        "s_mov_b64 exec, 0xffff\n"
        "global_store_dword v193, v191, s[76:77] sc1\n"
        "s_mov_b64 exec, -1\n"
        "v_mfma_f32_16x16x32_bf16 a[12:15], v[44:47], v[0:3], a[12:15]\n"
        "global_load_lds_dwordx4 v240, s[62:63] offset:2048\n"
        "ds_read_b64_tr_b16 v[74:75], v231 offset:8192\n"
        "v_mfma_f32_16x16x32_bf16 a[16:19], v[0:3], v[48:51], a[16:19]\n"
        "ds_read_b64_tr_b16 v[76:77], v232 offset:8192\n"
        "s_add_u32 s82, s12, 0x200000\n"
        "s_addc_u32 s83, s13, 0\n"
        "v_mfma_f32_16x16x32_bf16 a[20:23], v[0:3], v[52:55], a[20:23]\n"
        "ds_read_b64_tr_b16 v[78:79], v233 offset:8192\n"
        "v_mul_f32 v193, v176, v188\n"
        "v_mfma_f32_16x16x32_bf16 a[24:27], v[0:3], v[56:59], a[24:27]\n"
        "ds_read_b64_tr_b16 v[80:81], v226 offset:24576\n"
        "v_mul_f32 v190, v175, v187\n"
        "v_mfma_f32_16x16x32_bf16 a[28:31], v[0:3], v[60:63], a[28:31]\n"
        "global_load_lds_dwordx4 v241, s[62:63] offset:3072\n"
        "ds_read_b128 v[0:3], v225 offset:32768\n"
        "s_add_u32 m0, s72, 0x8000\n"
        "v_mfma_f32_16x16x32_bf16 a[32:35], v[32:35], v[4:7], a[32:35]\n"
        "s_add_u32 s62, s62, 0x80000\n"
        "s_addc_u32 s63, s63, 0\n"
        "v_mul_f32 v192, v176, v187\n"
        "v_mfma_f32_16x16x32_bf16 a[36:39], v[36:39], v[4:7], a[36:39]\n"
        "ds_read_b64_tr_b16 v[82:83], v227 offset:24576\n"
        "v_mul_f32 v191, v175, v188\n"
        "v_mfma_f32_16x16x32_bf16 a[40:43], v[40:43], v[4:7], a[40:43]\n"
        "ds_read_b64_tr_b16 v[84:85], v228 offset:24576\n"
        "v_cvt_pk_bf16_f32 v193, 0, v193\n"
        "v_mfma_f32_16x16x32_bf16 a[44:47], v[44:47], v[4:7], a[44:47]\n"
        "ds_read_b64_tr_b16 v[86:87], v229 offset:24576\n"
        "v_cvt_pk_bf16_f32 v190, 0, v190\n"
        "v_mfma_f32_16x16x32_bf16 a[48:51], v[4:7], v[48:51], a[48:51]\n"
        "ds_read_b64_tr_b16 v[88:89], v230 offset:24576\n"
        "v_cvt_pk_bf16_f32 v192, 0, v192\n"
        "v_mfma_f32_16x16x32_bf16 a[52:55], v[4:7], v[52:55], a[52:55]\n"
        "ds_read_b64_tr_b16 v[90:91], v231 offset:24576\n"
        "v_cvt_pk_bf16_f32 v191, 0, v191\n"
        "v_mfma_f32_16x16x32_bf16 a[56:59], v[4:7], v[56:59], a[56:59]\n"
        "ds_read_b64_tr_b16 v[92:93], v232 offset:24576\n"
        "v_add_f32 v193, v193, v194\n"
        "v_mfma_f32_16x16x32_bf16 a[60:63], v[4:7], v[60:63], a[60:63]\n"
        "ds_read_b128 v[4:7], v225 offset:34816\n"
        "v_add_f32 v189, v189, v190\n"
        "v_mfma_f32_16x16x32_bf16 a[64:67], v[32:35], v[8:11], a[64:67]\n"
        "ds_read_b64_tr_b16 v[94:95], v233 offset:24576\n"
        "v_add_f32 v191, v191, v192\n"
        "v_mfma_f32_16x16x32_bf16 a[68:71], v[36:39], v[8:11], a[68:71]\n"
        "v_mul_f32 v194, v178, v186\n"
        "v_cvt_pk_bf16_f32 v193, 0, v193\n"
        "v_mfma_f32_16x16x32_bf16 a[72:75], v[40:43], v[8:11], a[72:75]\n"
        "v_mul_f32 v190, v176, v186\n"
        "v_mul_f32 v192, v177, v186\n"
        "v_mfma_f32_16x16x32_bf16 a[76:79], v[44:47], v[8:11], a[76:79]\n"
        "v_cvt_pk_bf16_f32 v194, 0, v194\n"
        "v_cvt_pk_bf16_f32 v189, 0, v189\n"
        "v_mfma_f32_16x16x32_bf16 a[80:83], v[8:11], v[48:51], a[80:83]\n"
        "v_cvt_pk_bf16_f32 v191, 0, v191\n"
        "v_cvt_pk_bf16_f32 v190, 0, v190\n"
        "v_mfma_f32_16x16x32_bf16 a[84:87], v[8:11], v[52:55], a[84:87]\n"
        "v_cvt_pk_bf16_f32 v192, 0, v192\n"
        "v_add_f32 v193, v193, v194\n"
        "v_mfma_f32_16x16x32_bf16 a[88:91], v[8:11], v[56:59], a[88:91]\n"
        "v_add_f32 v189, v189, v190\n"
        "v_add_f32 v191, v191, v192\n"
        "v_mfma_f32_16x16x32_bf16 a[92:95], v[8:11], v[60:63], a[92:95]\n"
        "ds_read_b128 v[8:11], v225 offset:36864\n"
        "v_mul_f32 v194, v179, v185\n"
        "v_mfma_f32_16x16x32_bf16 a[96:99], v[32:35], v[12:15], a[96:99]\n"
        "v_cvt_pk_bf16_f32 v193, 0, v193\n"
        "v_mul_f32 v190, v177, v185\n"
        "v_mfma_f32_16x16x32_bf16 a[100:103], v[36:39], v[12:15], a[100:103]\n"
        "v_mul_f32 v192, v178, v185\n"
        "v_cvt_pk_bf16_f32 v194, 0, v194\n"
        "v_mfma_f32_16x16x32_bf16 a[104:107], v[40:43], v[12:15], a[104:107]\n"
        "v_cvt_pk_bf16_f32 v189, 0, v189\n"
        "v_cvt_pk_bf16_f32 v191, 0, v191\n"
        "v_mfma_f32_16x16x32_bf16 a[108:111], v[44:47], v[12:15], a[108:111]\n"
        "v_cvt_pk_bf16_f32 v190, 0, v190\n"
        "v_cvt_pk_bf16_f32 v192, 0, v192\n"
        "v_mfma_f32_16x16x32_bf16 a[112:115], v[12:15], v[48:51], a[112:115]\n"
        "v_add_f32 v193, v193, v194\n"
        "v_add_f32 v189, v189, v190\n"
        "v_mfma_f32_16x16x32_bf16 a[116:119], v[12:15], v[52:55], a[116:119]\n"
        "v_add_f32 v191, v191, v192\n"
        "v_cvt_pk_bf16_f32 v193, 0, v193\n"
        "v_mfma_f32_16x16x32_bf16 a[120:123], v[12:15], v[56:59], a[120:123]\n"
        "v_lshlrev_b32 v194, 16, v122\n"
        "v_mul_f32 v195, v195, v196\n"
        "v_mfma_f32_16x16x32_bf16 a[124:127], v[12:15], v[60:63], a[124:127]\n"
        "ds_read_b128 v[12:15], v225 offset:38912\n"
        "v_cvt_pk_bf16_f32 v189, 0, v189\n"
        "v_mfma_f32_16x16x32_bf16 a[128:131], v[32:35], v[16:19], a[128:131]\n"
        "v_lshlrev_b32 v190, 16, v118\n"
        "v_cvt_pk_bf16_f32 v191, 0, v191\n"
        "v_mfma_f32_16x16x32_bf16 a[132:135], v[36:39], v[16:19], a[132:135]\n"
        "v_lshlrev_b32 v192, 16, v120\n"
        "v_mul_f32 v193, v193, v194\n"
        "v_mfma_f32_16x16x32_bf16 a[136:139], v[40:43], v[16:19], a[136:139]\n"
        "v_lshlrev_b32 v182, 16, v112\n"
        "v_mul_f32 v189, v189, v190\n"
        "v_mfma_f32_16x16x32_bf16 a[140:143], v[44:47], v[16:19], a[140:143]\n"
        "v_mul_f32 v191, v191, v192\n"
        "v_cvt_pk_bf16_f32 v202, v195, v193\n"
        "v_mfma_f32_16x16x32_bf16 a[144:147], v[16:19], v[48:51], a[144:147]\n"
        "v_mul_f32 v194, v181, v187\n"
        "v_mul_f32 v196, v182, v187\n"
        "v_mfma_f32_16x16x32_bf16 a[148:151], v[16:19], v[52:55], a[148:151]\n"
        "v_cvt_pk_bf16_f32 v203, v191, v189\n"
        "v_mul_f32 v193, v180, v188\n"
        "v_mfma_f32_16x16x32_bf16 a[152:155], v[16:19], v[56:59], a[152:155]\n"
        "v_mul_f32 v195, v181, v188\n"
        "v_mul_f32 v190, v179, v187\n"
        "v_mfma_f32_16x16x32_bf16 a[156:159], v[16:19], v[60:63], a[156:159]\n"
        "ds_read_b128 v[16:19], v225 offset:40960\n"
        "v_mul_f32 v192, v180, v187\n"
        "v_mfma_f32_16x16x32_bf16 a[160:163], v[32:35], v[20:23], a[160:163]\n"
        "v_cvt_pk_bf16_f32 v194, 0, v194\n"
        "v_cvt_pk_bf16_f32 v196, 0, v196\n"
        "v_mfma_f32_16x16x32_bf16 a[164:167], v[36:39], v[20:23], a[164:167]\n"
        "v_mul_f32 v189, v178, v188\n"
        "v_mul_f32 v191, v179, v188\n"
        "v_mfma_f32_16x16x32_bf16 a[168:171], v[40:43], v[20:23], a[168:171]\n"
        "v_cvt_pk_bf16_f32 v193, 0, v193\n"
        "v_cvt_pk_bf16_f32 v195, 0, v195\n"
        "v_mfma_f32_16x16x32_bf16 a[172:175], v[44:47], v[20:23], a[172:175]\n"
        "v_lshlrev_b32 v183, 16, v114\n"
        "v_cvt_pk_bf16_f32 v190, 0, v190\n"
        "v_mfma_f32_16x16x32_bf16 a[176:179], v[20:23], v[48:51], a[176:179]\n"
        "v_cvt_pk_bf16_f32 v192, 0, v192\n"
        "v_cvt_pk_bf16_f32 v189, 0, v189\n"
        "v_mfma_f32_16x16x32_bf16 a[180:183], v[20:23], v[52:55], a[180:183]\n"
        "v_cvt_pk_bf16_f32 v191, 0, v191\n"
        "v_add_f32 v193, v193, v194\n"
        "v_mfma_f32_16x16x32_bf16 a[184:187], v[20:23], v[56:59], a[184:187]\n"
        "v_add_f32 v195, v195, v196\n"
        "v_add_f32 v189, v189, v190\n"
        "v_mfma_f32_16x16x32_bf16 a[188:191], v[20:23], v[60:63], a[188:191]\n"
        "ds_read_b128 v[20:23], v225 offset:43008\n"
        "v_add_f32 v191, v191, v192\n"
        "v_mfma_f32_16x16x32_bf16 a[192:195], v[32:35], v[24:27], a[192:195]\n"
        "v_mul_f32 v194, v182, v186\n"
        "v_mul_f32 v196, v183, v186\n"
        "v_mfma_f32_16x16x32_bf16 a[196:199], v[36:39], v[24:27], a[196:199]\n"
        "v_cvt_pk_bf16_f32 v193, 0, v193\n"
        "v_cvt_pk_bf16_f32 v195, 0, v195\n"
        "v_mfma_f32_16x16x32_bf16 a[200:203], v[40:43], v[24:27], a[200:203]\n"
        "s_waitcnt lgkmcnt(1)\n"
        "v_mul_f32 v190, v180, v186\n"
        "v_mul_f32 v192, v181, v186\n"
        "v_mfma_f32_16x16x32_bf16 a[204:207], v[44:47], v[24:27], a[204:207]\n"
        "v_cvt_pk_bf16_f32 v194, 0, v194\n"
        "v_cvt_pk_bf16_f32 v196, 0, v196\n"
        "v_mfma_f32_16x16x32_bf16 a[208:211], v[24:27], v[48:51], a[208:211]\n"
        "v_lshlrev_b32 v184, 16, v116\n"
        "v_cvt_pk_bf16_f32 v189, 0, v189\n"
        "v_mfma_f32_16x16x32_bf16 a[212:215], v[24:27], v[52:55], a[212:215]\n"
        "v_cvt_pk_bf16_f32 v191, 0, v191\n"
        "v_cvt_pk_bf16_f32 v190, 0, v190\n"
        "v_mfma_f32_16x16x32_bf16 a[216:219], v[24:27], v[56:59], a[216:219]\n"
        "v_cvt_pk_bf16_f32 v192, 0, v192\n"
        "v_add_f32 v193, v193, v194\n"
        "v_mfma_f32_16x16x32_bf16 a[220:223], v[24:27], v[60:63], a[220:223]\n"
        "ds_read_b128 v[24:27], v225 offset:45056\n"
        "v_add_f32 v195, v195, v196\n"
        "v_mfma_f32_16x16x32_bf16 a[224:227], v[32:35], v[28:31], a[224:227]\n"
        "v_add_f32 v189, v189, v190\n"
        "v_add_f32 v191, v191, v192\n"
        "v_mfma_f32_16x16x32_bf16 a[228:231], v[36:39], v[28:31], a[228:231]\n"
        "v_mul_f32 v194, v183, v185\n"
        "v_mul_f32 v196, v184, v185\n"
        "v_mfma_f32_16x16x32_bf16 a[232:235], v[40:43], v[28:31], a[232:235]\n"
        "v_cvt_pk_bf16_f32 v193, 0, v193\n"
        "v_cvt_pk_bf16_f32 v195, 0, v195\n"
        "v_mfma_f32_16x16x32_bf16 a[236:239], v[44:47], v[28:31], a[236:239]\n"
        "v_mul_f32 v190, v181, v185\n"
        "v_mul_f32 v192, v182, v185\n"
        "v_mfma_f32_16x16x32_bf16 a[240:243], v[28:31], v[48:51], a[240:243]\n"
        "v_cvt_pk_bf16_f32 v194, 0, v194\n"
        "v_cvt_pk_bf16_f32 v196, 0, v196\n"
        "v_mfma_f32_16x16x32_bf16 a[244:247], v[28:31], v[52:55], a[244:247]\n"
        "v_cvt_pk_bf16_f32 v189, 0, v189\n"
        "v_cvt_pk_bf16_f32 v191, 0, v191\n"
        "v_mfma_f32_16x16x32_bf16 a[248:251], v[28:31], v[56:59], a[248:251]\n"
        "v_cvt_pk_bf16_f32 v190, 0, v190\n"
        "v_cvt_pk_bf16_f32 v192, 0, v192\n"
        "v_mfma_f32_16x16x32_bf16 a[252:255], v[28:31], v[60:63], a[252:255]\n"
        "ds_read_b128 v[28:31], v225 offset:47104\n"
        "v_add_f32 v193, v193, v194\n"
        "v_mfma_f32_16x16x32_bf16 a[0:3], v[64:67], v[0:3], a[0:3]\n"
        "s_waitcnt vmcnt(25) lgkmcnt(1)\n"
        "v_add_f32 v195, v195, v196\n"
        "v_add_f32 v189, v189, v190\n"
        "v_mfma_f32_16x16x32_bf16 a[4:7], v[68:71], v[0:3], a[4:7]\n"
        "s_waitcnt vmcnt(22)\n"
        "v_add_f32 v191, v191, v192\n"
        "v_cvt_pk_bf16_f32 v193, 0, v193\n"
        "v_mfma_f32_16x16x32_bf16 a[8:11], v[72:75], v[0:3], a[8:11]\n"
        "v_lshlrev_b32 v194, 16, v130\n"
        "v_cvt_pk_bf16_f32 v195, 0, v195\n"
        "v_mfma_f32_16x16x32_bf16 a[12:15], v[76:79], v[0:3], a[12:15]\n"
        "v_lshlrev_b32 v196, 16, v132\n"
        "v_cvt_pk_bf16_f32 v189, 0, v189\n"
        "v_mfma_f32_16x16x32_bf16 a[16:19], v[0:3], v[80:83], a[16:19]\n"
        "v_lshlrev_b32 v190, 16, v126\n"
        "v_cvt_pk_bf16_f32 v191, 0, v191\n"
        "v_mfma_f32_16x16x32_bf16 a[20:23], v[0:3], v[84:87], a[20:23]\n"
        "v_lshlrev_b32 v192, 16, v128\n"
        "v_mul_f32 v193, v193, v194\n"
        "v_mfma_f32_16x16x32_bf16 a[24:27], v[0:3], v[88:91], a[24:27]\n"
        "v_mul_f32 v195, v195, v196\n"
        "v_and_b32 v176, 0xffff0000, v100\n"
        "v_mfma_f32_16x16x32_bf16 a[28:31], v[0:3], v[92:95], a[28:31]\n"
        "s_waitcnt vmcnt(11) lgkmcnt(0)\n"
        "s_barrier\n"
        "v_mfma_f32_16x16x32_bf16 a[32:35], v[64:67], v[4:7], a[32:35]\n"
        "global_load_lds_dwordx4 v234, s[56:57] offset:0\n"
        "ds_read_b128 v[0:3], v224 offset:0\n"
        "v_mfma_f32_16x16x32_bf16 a[36:39], v[68:71], v[4:7], a[36:39]\n"
        "ds_read_b64_tr_b16 v[32:33], v226 offset:32768\n"
        "v_mfma_f32_16x16x32_bf16 a[40:43], v[72:75], v[4:7], a[40:43]\n"
        "ds_read_b64_tr_b16 v[34:35], v227 offset:32768\n"
        "v_mfma_f32_16x16x32_bf16 a[44:47], v[76:79], v[4:7], a[44:47]\n"
        "ds_read_b64_tr_b16 v[36:37], v228 offset:32768\n"
        "v_mfma_f32_16x16x32_bf16 a[48:51], v[4:7], v[80:83], a[48:51]\n"
        "ds_read_b64_tr_b16 v[38:39], v229 offset:32768\n"
        "global_load_lds_dwordx4 v235, s[56:57] offset:1024\n"
        "v_mfma_f32_16x16x32_bf16 a[52:55], v[4:7], v[84:87], a[52:55]\n"
        "ds_read_b64_tr_b16 v[40:41], v230 offset:32768\n"
        "v_mfma_f32_16x16x32_bf16 a[56:59], v[4:7], v[88:91], a[56:59]\n"
        "ds_read_b64_tr_b16 v[42:43], v231 offset:32768\n"
        "v_mfma_f32_16x16x32_bf16 a[60:63], v[4:7], v[92:95], a[60:63]\n"
        "ds_read_b128 v[4:7], v224 offset:2048\n"
        "v_mfma_f32_16x16x32_bf16 a[64:67], v[64:67], v[8:11], a[64:67]\n"
        "global_load_dword v253, v173, s[82:83] offset:0\n"
        "v_mfma_f32_16x16x32_bf16 a[68:71], v[68:71], v[8:11], a[68:71]\n"
        "ds_read_b64_tr_b16 v[44:45], v232 offset:32768\n"
        "global_load_lds_dwordx4 v236, s[56:57] offset:2048\n"
        "v_mfma_f32_16x16x32_bf16 a[72:75], v[72:75], v[8:11], a[72:75]\n"
        "ds_read_b64_tr_b16 v[46:47], v233 offset:32768\n"
        "s_add_u32 s82, s12, 0x300000\n"
        "s_addc_u32 s83, s13, 0\n"
        "v_mfma_f32_16x16x32_bf16 a[76:79], v[76:79], v[8:11], a[76:79]\n"
        "ds_read_b64_tr_b16 v[48:49], v226 offset:49152\n"
        "v_and_b32 v177, 0xffff0000, v102\n"
        "v_mfma_f32_16x16x32_bf16 a[80:83], v[8:11], v[80:83], a[80:83]\n"
        "ds_read_b64_tr_b16 v[50:51], v227 offset:49152\n"
        "v_lshlrev_b32 v187, 16, v137\n"
        "v_mfma_f32_16x16x32_bf16 a[84:87], v[8:11], v[84:87], a[84:87]\n"
        "global_load_lds_dwordx4 v237, s[56:57] offset:3072\n"
        "s_add_u32 m0, s72, 0x9000\n"
        "v_mfma_f32_16x16x32_bf16 a[88:91], v[8:11], v[88:91], a[88:91]\n"
        "ds_read_b64_tr_b16 v[52:53], v228 offset:49152\n"
        "s_add_u32 s56, s56, 0x80\n"
        "s_addc_u32 s57, s57, 0\n"
        "v_mfma_f32_16x16x32_bf16 a[92:95], v[8:11], v[92:95], a[92:95]\n"
        "ds_read_b128 v[8:11], v224 offset:4096\n"
        "v_and_b32 v178, 0xffff0000, v104\n"
        "v_mfma_f32_16x16x32_bf16 a[96:99], v[64:67], v[12:15], a[96:99]\n"
        "ds_read_b64_tr_b16 v[54:55], v229 offset:49152\n"
        "v_and_b32 v188, 0xffff0000, v137\n"
        "v_mfma_f32_16x16x32_bf16 a[100:103], v[68:71], v[12:15], a[100:103]\n"
        "global_load_lds_dwordx4 v234, s[58:59] offset:0\n"
        "v_mfma_f32_16x16x32_bf16 a[104:107], v[72:75], v[12:15], a[104:107]\n"
        "ds_read_b64_tr_b16 v[56:57], v230 offset:49152\n"
        "v_mul_f32 v189, v189, v190\n"
        "v_mfma_f32_16x16x32_bf16 a[108:111], v[76:79], v[12:15], a[108:111]\n"
        "ds_read_b64_tr_b16 v[58:59], v231 offset:49152\n"
        "v_mul_f32 v191, v191, v192\n"
        "v_mfma_f32_16x16x32_bf16 a[112:115], v[12:15], v[80:83], a[112:115]\n"
        "ds_read_b64_tr_b16 v[60:61], v232 offset:49152\n"
        "v_cvt_pk_bf16_f32 v200, v195, v193\n"
        "v_mfma_f32_16x16x32_bf16 a[116:119], v[12:15], v[84:87], a[116:119]\n"
        "global_load_lds_dwordx4 v235, s[58:59] offset:1024\n"
        "v_mfma_f32_16x16x32_bf16 a[120:123], v[12:15], v[88:91], a[120:123]\n"
        "ds_read_b64_tr_b16 v[62:63], v233 offset:49152\n"
        "v_and_b32 v174, 0xffff0000, v96\n"
        "v_mfma_f32_16x16x32_bf16 a[124:127], v[12:15], v[92:95], a[124:127]\n"
        "ds_read_b128 v[12:15], v224 offset:6144\n"
        "v_and_b32 v175, 0xffff0000, v98\n"
        "v_mfma_f32_16x16x32_bf16 a[128:131], v[64:67], v[16:19], a[128:131]\n"
        "v_mul_f32 v194, v177, v187\n"
        "v_mul_f32 v196, v178, v187\n"
        "v_mfma_f32_16x16x32_bf16 a[132:135], v[68:71], v[16:19], a[132:135]\n"
        "global_load_lds_dwordx4 v236, s[58:59] offset:2048\n"
        "v_mfma_f32_16x16x32_bf16 a[136:139], v[72:75], v[16:19], a[136:139]\n"
        "v_cvt_pk_bf16_f32 v201, v191, v189\n"
        "v_mul_f32 v193, v176, v188\n"
        "v_mfma_f32_16x16x32_bf16 a[140:143], v[76:79], v[16:19], a[140:143]\n"
        "v_mul_f32 v195, v177, v188\n"
        "v_mul_f32 v192, v176, v187\n"
        "v_mfma_f32_16x16x32_bf16 a[144:147], v[16:19], v[80:83], a[144:147]\n"
        "v_mul_f32 v190, v175, v187\n"
        "v_cvt_pk_bf16_f32 v194, 0, v194\n"
        "v_mfma_f32_16x16x32_bf16 a[148:151], v[16:19], v[84:87], a[148:151]\n"
        "global_load_lds_dwordx4 v237, s[58:59] offset:3072\n"
        "s_add_u32 m0, s73, 0x10000\n"
        "v_mfma_f32_16x16x32_bf16 a[152:155], v[16:19], v[88:91], a[152:155]\n"
        "s_add_u32 s58, s58, 0x80\n"
        "s_addc_u32 s59, s59, 0\n"
        "v_cvt_pk_bf16_f32 v196, 0, v196\n"
        "v_mfma_f32_16x16x32_bf16 a[156:159], v[16:19], v[92:95], a[156:159]\n"
        "ds_read_b128 v[16:19], v224 offset:8192\n"
        "v_mul_f32 v189, v174, v188\n"
        "v_mfma_f32_16x16x32_bf16 a[160:163], v[64:67], v[20:23], a[160:163]\n"
        "v_mul_f32 v191, v175, v188\n"
        "v_cvt_pk_bf16_f32 v193, 0, v193\n"
        "v_mfma_f32_16x16x32_bf16 a[164:167], v[68:71], v[20:23], a[164:167]\n"
        "global_load_lds_dwordx4 v238, s[60:61] offset:0\n"
        "v_mfma_f32_16x16x32_bf16 a[168:171], v[72:75], v[20:23], a[168:171]\n"
        "v_cvt_pk_bf16_f32 v195, 0, v195\n"
        "v_and_b32 v179, 0xffff0000, v106\n"
        "v_mfma_f32_16x16x32_bf16 a[172:175], v[76:79], v[20:23], a[172:175]\n"
        "v_and_b32 v186, 0xffff0000, v136\n"
        "v_cvt_pk_bf16_f32 v192, 0, v192\n"
        "v_mfma_f32_16x16x32_bf16 a[176:179], v[20:23], v[80:83], a[176:179]\n"
        "v_cvt_pk_bf16_f32 v190, 0, v190\n"
        "v_cvt_pk_bf16_f32 v189, 0, v189\n"
        "v_mfma_f32_16x16x32_bf16 a[180:183], v[20:23], v[84:87], a[180:183]\n"
        "global_load_lds_dwordx4 v239, s[60:61] offset:1024\n"
        "v_mfma_f32_16x16x32_bf16 a[184:187], v[20:23], v[88:91], a[184:187]\n"
        "v_cvt_pk_bf16_f32 v191, 0, v191\n"
        "v_add_f32 v193, v193, v194\n"
        "v_mfma_f32_16x16x32_bf16 a[188:191], v[20:23], v[92:95], a[188:191]\n"
        "ds_read_b128 v[20:23], v224 offset:10240\n"
        "v_add_f32 v195, v195, v196\n"
        "v_mfma_f32_16x16x32_bf16 a[192:195], v[64:67], v[24:27], a[192:195]\n"
        "v_add_f32 v189, v189, v190\n"
        "v_add_f32 v191, v191, v192\n"
        "v_mfma_f32_16x16x32_bf16 a[196:199], v[68:71], v[24:27], a[196:199]\n"
        "global_load_lds_dwordx4 v240, s[60:61] offset:2048\n"
        "v_mfma_f32_16x16x32_bf16 a[200:203], v[72:75], v[24:27], a[200:203]\n"
        "v_mul_f32 v194, v178, v186\n"
        "v_mul_f32 v196, v179, v186\n"
        "v_mfma_f32_16x16x32_bf16 a[204:207], v[76:79], v[24:27], a[204:207]\n"
        "v_cvt_pk_bf16_f32 v193, 0, v193\n"
        "v_cvt_pk_bf16_f32 v195, 0, v195\n"
        "v_mfma_f32_16x16x32_bf16 a[208:211], v[24:27], v[80:83], a[208:211]\n"
        "v_mul_f32 v190, v176, v186\n"
        "v_mul_f32 v192, v177, v186\n"
        "v_mfma_f32_16x16x32_bf16 a[212:215], v[24:27], v[84:87], a[212:215]\n"
        "global_load_lds_dwordx4 v241, s[60:61] offset:3072\n"
        "s_add_u32 m0, s73, 0x14000\n"
        "v_mfma_f32_16x16x32_bf16 a[216:219], v[24:27], v[88:91], a[216:219]\n"
        "s_add_u32 s60, s60, 0x80000\n"
        "s_addc_u32 s61, s61, 0\n"
        "v_cvt_pk_bf16_f32 v194, 0, v194\n"
        "v_mfma_f32_16x16x32_bf16 a[220:223], v[24:27], v[92:95], a[220:223]\n"
        "ds_read_b128 v[24:27], v224 offset:12288\n"
        "v_cvt_pk_bf16_f32 v196, 0, v196\n"
        "v_mfma_f32_16x16x32_bf16 a[224:227], v[64:67], v[28:31], a[224:227]\n"
        "ds_read_b64_tr_b16 v[64:65], v226 offset:40960\n"
        "v_and_b32 v180, 0xffff0000, v108\n"
        "v_mfma_f32_16x16x32_bf16 a[228:231], v[68:71], v[28:31], a[228:231]\n"
        "global_load_lds_dwordx4 v238, s[62:63] offset:0\n"
        "v_mfma_f32_16x16x32_bf16 a[232:235], v[72:75], v[28:31], a[232:235]\n"
        "ds_read_b64_tr_b16 v[66:67], v227 offset:40960\n"
        "v_lshlrev_b32 v185, 16, v136\n"
        "v_mfma_f32_16x16x32_bf16 a[236:239], v[76:79], v[28:31], a[236:239]\n"
        "ds_read_b64_tr_b16 v[68:69], v228 offset:40960\n"
        "v_cvt_pk_bf16_f32 v189, 0, v189\n"
        "v_mfma_f32_16x16x32_bf16 a[240:243], v[28:31], v[80:83], a[240:243]\n"
        "ds_read_b64_tr_b16 v[70:71], v229 offset:40960\n"
        "v_cvt_pk_bf16_f32 v191, 0, v191\n"
        "v_mfma_f32_16x16x32_bf16 a[244:247], v[28:31], v[84:87], a[244:247]\n"
        "global_load_lds_dwordx4 v239, s[62:63] offset:1024\n"
        "v_mfma_f32_16x16x32_bf16 a[248:251], v[28:31], v[88:91], a[248:251]\n"
        "ds_read_b64_tr_b16 v[72:73], v230 offset:40960\n"
        "v_cvt_pk_bf16_f32 v190, 0, v190\n"
        "v_mfma_f32_16x16x32_bf16 a[252:255], v[28:31], v[92:95], a[252:255]\n"
        "ds_read_b128 v[28:31], v224 offset:14336\n"
        "s_waitcnt lgkmcnt(6)\n"
        "v_mfma_f32_16x16x32_bf16 a[0:3], v[32:35], v[0:3], a[0:3]\n"
        "ds_read_b64_tr_b16 v[74:75], v231 offset:40960\n"
        "v_mfma_f32_16x16x32_bf16 a[4:7], v[36:39], v[0:3], a[4:7]\n"
        "global_load_lds_dwordx4 v240, s[62:63] offset:2048\n"
        "v_mfma_f32_16x16x32_bf16 a[8:11], v[40:43], v[0:3], a[8:11]\n"
        "ds_read_b64_tr_b16 v[76:77], v232 offset:40960\n"
        "v_cvt_pk_bf16_f32 v192, 0, v192\n"
        "v_mfma_f32_16x16x32_bf16 a[12:15], v[44:47], v[0:3], a[12:15]\n"
        "ds_read_b64_tr_b16 v[78:79], v233 offset:40960\n"
        "v_add_f32 v193, v193, v194\n"
        "v_mfma_f32_16x16x32_bf16 a[16:19], v[0:3], v[48:51], a[16:19]\n"
        "ds_read_b64_tr_b16 v[80:81], v226 offset:57344\n"
        "v_add_f32 v195, v195, v196\n"
        "v_mfma_f32_16x16x32_bf16 a[20:23], v[0:3], v[52:55], a[20:23]\n"
        "global_load_lds_dwordx4 v241, s[62:63] offset:3072\n"
        "s_add_u32 m0, s72, 0x0\n"
        "v_mfma_f32_16x16x32_bf16 a[24:27], v[0:3], v[56:59], a[24:27]\n"
        "s_add_u32 s62, s62, 0x80000\n"
        "s_addc_u32 s63, s63, 0\n"
        "v_add_f32 v189, v189, v190\n"
        "v_mfma_f32_16x16x32_bf16 a[28:31], v[0:3], v[60:63], a[28:31]\n"
        "ds_read_b128 v[0:3], v225 offset:0\n"
        "v_add_f32 v191, v191, v192\n"
        "v_mfma_f32_16x16x32_bf16 a[32:35], v[32:35], v[4:7], a[32:35]\n"
        "ds_read_b64_tr_b16 v[82:83], v227 offset:57344\n"
        "v_mul_f32 v194, v179, v185\n"
        "v_mfma_f32_16x16x32_bf16 a[36:39], v[36:39], v[4:7], a[36:39]\n"
        "ds_read_b64_tr_b16 v[84:85], v228 offset:57344\n"
        "v_mul_f32 v196, v180, v185\n"
        "v_mfma_f32_16x16x32_bf16 a[40:43], v[40:43], v[4:7], a[40:43]\n"
        "ds_read_b64_tr_b16 v[86:87], v229 offset:57344\n"
        "v_cvt_pk_bf16_f32 v193, 0, v193\n"
        "v_mfma_f32_16x16x32_bf16 a[44:47], v[44:47], v[4:7], a[44:47]\n"
        "ds_read_b64_tr_b16 v[88:89], v230 offset:57344\n"
        "v_cvt_pk_bf16_f32 v195, 0, v195\n"
        "v_mfma_f32_16x16x32_bf16 a[48:51], v[4:7], v[48:51], a[48:51]\n"
        "ds_read_b64_tr_b16 v[90:91], v231 offset:57344\n"
        "v_mul_f32 v190, v177, v185\n"
        "v_mfma_f32_16x16x32_bf16 a[52:55], v[4:7], v[52:55], a[52:55]\n"
        "ds_read_b64_tr_b16 v[92:93], v232 offset:57344\n"
        "v_mul_f32 v192, v178, v185\n"
        "v_mfma_f32_16x16x32_bf16 a[56:59], v[4:7], v[56:59], a[56:59]\n"
        "ds_read_b64_tr_b16 v[94:95], v233 offset:57344\n"
        "v_cvt_pk_bf16_f32 v194, 0, v194\n"
        "v_mfma_f32_16x16x32_bf16 a[60:63], v[4:7], v[60:63], a[60:63]\n"
        "ds_read_b128 v[4:7], v225 offset:2048\n"
        "v_cvt_pk_bf16_f32 v196, 0, v196\n"
        "v_mfma_f32_16x16x32_bf16 a[64:67], v[32:35], v[8:11], a[64:67]\n"
        "v_cvt_pk_bf16_f32 v189, 0, v189\n"
        "v_cvt_pk_bf16_f32 v191, 0, v191\n"
        "v_mfma_f32_16x16x32_bf16 a[68:71], v[36:39], v[8:11], a[68:71]\n"
        "v_cvt_pk_bf16_f32 v190, 0, v190\n"
        "v_cvt_pk_bf16_f32 v192, 0, v192\n"
        "v_mfma_f32_16x16x32_bf16 a[72:75], v[40:43], v[8:11], a[72:75]\n"
        "v_add_f32 v193, v193, v194\n"
        "v_add_f32 v195, v195, v196\n"
        "v_mfma_f32_16x16x32_bf16 a[76:79], v[44:47], v[8:11], a[76:79]\n"
        "v_add_f32 v189, v189, v190\n"
        "v_add_f32 v191, v191, v192\n"
        "v_mfma_f32_16x16x32_bf16 a[80:83], v[8:11], v[48:51], a[80:83]\n"
        "v_cvt_pk_bf16_f32 v193, 0, v193\n"
        "v_and_b32 v194, 0xffff0000, v122\n"
        "v_mfma_f32_16x16x32_bf16 a[84:87], v[8:11], v[52:55], a[84:87]\n"
        "v_cvt_pk_bf16_f32 v195, 0, v195\n"
        "v_and_b32 v196, 0xffff0000, v124\n"
        "v_mfma_f32_16x16x32_bf16 a[88:91], v[8:11], v[56:59], a[88:91]\n"
        "v_cvt_pk_bf16_f32 v189, 0, v189\n"
        "v_and_b32 v190, 0xffff0000, v118\n"
        "v_mfma_f32_16x16x32_bf16 a[92:95], v[8:11], v[60:63], a[92:95]\n"
        "ds_read_b128 v[8:11], v225 offset:4096\n"
        "global_load_dword v253, v173, s[82:83] offset:0\n"
        "v_mfma_f32_16x16x32_bf16 a[96:99], v[32:35], v[12:15], a[96:99]\n"
        "s_add_u32 s82, s12, 0x400000\n"
        "s_addc_u32 s83, s13, 0\n"
        "v_cvt_pk_bf16_f32 v191, 0, v191\n"
        "v_mfma_f32_16x16x32_bf16 a[100:103], v[36:39], v[12:15], a[100:103]\n"
        "v_and_b32 v192, 0xffff0000, v120\n"
        "v_mul_f32 v193, v193, v194\n"
        "v_mfma_f32_16x16x32_bf16 a[104:107], v[40:43], v[12:15], a[104:107]\n"
        "v_mul_f32 v195, v195, v196\n"
        "v_and_b32 v181, 0xffff0000, v110\n"
        "v_mfma_f32_16x16x32_bf16 a[108:111], v[44:47], v[12:15], a[108:111]\n"
        "v_and_b32 v182, 0xffff0000, v112\n"
        "v_mul_f32 v189, v189, v190\n"
        "v_mfma_f32_16x16x32_bf16 a[112:115], v[12:15], v[48:51], a[112:115]\n"
        "v_mul_f32 v191, v191, v192\n"
        "v_cvt_pk_bf16_f32 v210, v195, v193\n"
        "v_mfma_f32_16x16x32_bf16 a[116:119], v[12:15], v[52:55], a[116:119]\n"
        "v_mul_f32 v194, v181, v187\n"
        "v_mul_f32 v196, v182, v187\n"
        "v_mfma_f32_16x16x32_bf16 a[120:123], v[12:15], v[56:59], a[120:123]\n"
        "v_cvt_pk_bf16_f32 v211, v191, v189\n"
        "v_mul_f32 v193, v180, v188\n"
        "v_mfma_f32_16x16x32_bf16 a[124:127], v[12:15], v[60:63], a[124:127]\n"
        "ds_read_b128 v[12:15], v225 offset:6144\n"
        "v_mul_f32 v195, v181, v188\n"
        "v_mfma_f32_16x16x32_bf16 a[128:131], v[32:35], v[16:19], a[128:131]\n"
        "v_mul_f32 v190, v179, v187\n"
        "v_mul_f32 v192, v180, v187\n"
        "v_mfma_f32_16x16x32_bf16 a[132:135], v[36:39], v[16:19], a[132:135]\n"
        "v_cvt_pk_bf16_f32 v194, 0, v194\n"
        "v_cvt_pk_bf16_f32 v196, 0, v196\n"
        "v_mfma_f32_16x16x32_bf16 a[136:139], v[40:43], v[16:19], a[136:139]\n"
        "v_mul_f32 v189, v178, v188\n"
        "v_mul_f32 v191, v179, v188\n"
        "v_mfma_f32_16x16x32_bf16 a[140:143], v[44:47], v[16:19], a[140:143]\n"
        "v_cvt_pk_bf16_f32 v193, 0, v193\n"
        "v_cvt_pk_bf16_f32 v195, 0, v195\n"
        "v_mfma_f32_16x16x32_bf16 a[144:147], v[16:19], v[48:51], a[144:147]\n"
        "v_and_b32 v183, 0xffff0000, v114\n"
        "v_cvt_pk_bf16_f32 v190, 0, v190\n"
        "v_mfma_f32_16x16x32_bf16 a[148:151], v[16:19], v[52:55], a[148:151]\n"
        "v_cvt_pk_bf16_f32 v192, 0, v192\n"
        "v_cvt_pk_bf16_f32 v189, 0, v189\n"
        "v_mfma_f32_16x16x32_bf16 a[152:155], v[16:19], v[56:59], a[152:155]\n"
        "v_cvt_pk_bf16_f32 v191, 0, v191\n"
        "v_add_f32 v193, v193, v194\n"
        "v_mfma_f32_16x16x32_bf16 a[156:159], v[16:19], v[60:63], a[156:159]\n"
        "ds_read_b128 v[16:19], v225 offset:8192\n"
        "v_add_f32 v195, v195, v196\n"
        "v_mfma_f32_16x16x32_bf16 a[160:163], v[32:35], v[20:23], a[160:163]\n"
        "v_add_f32 v189, v189, v190\n"
        "v_add_f32 v191, v191, v192\n"
        "v_mfma_f32_16x16x32_bf16 a[164:167], v[36:39], v[20:23], a[164:167]\n"
        "v_mul_f32 v194, v182, v186\n"
        "v_mul_f32 v196, v183, v186\n"
        "v_mfma_f32_16x16x32_bf16 a[168:171], v[40:43], v[20:23], a[168:171]\n"
        "v_cvt_pk_bf16_f32 v193, 0, v193\n"
        "v_cvt_pk_bf16_f32 v195, 0, v195\n"
        "v_mfma_f32_16x16x32_bf16 a[172:175], v[44:47], v[20:23], a[172:175]\n"
        "v_mul_f32 v190, v180, v186\n"
        "v_mul_f32 v192, v181, v186\n"
        "v_mfma_f32_16x16x32_bf16 a[176:179], v[20:23], v[48:51], a[176:179]\n"
        "v_cvt_pk_bf16_f32 v194, 0, v194\n"
        "v_cvt_pk_bf16_f32 v196, 0, v196\n"
        "v_mfma_f32_16x16x32_bf16 a[180:183], v[20:23], v[52:55], a[180:183]\n"
        "v_and_b32 v184, 0xffff0000, v116\n"
        "v_cvt_pk_bf16_f32 v189, 0, v189\n"
        "v_mfma_f32_16x16x32_bf16 a[184:187], v[20:23], v[56:59], a[184:187]\n"
        "v_cvt_pk_bf16_f32 v191, 0, v191\n"
        "v_cvt_pk_bf16_f32 v190, 0, v190\n"
        "v_mfma_f32_16x16x32_bf16 a[188:191], v[20:23], v[60:63], a[188:191]\n"
        "ds_read_b128 v[20:23], v225 offset:10240\n"
        "v_cvt_pk_bf16_f32 v192, 0, v192\n"
        "v_mfma_f32_16x16x32_bf16 a[192:195], v[32:35], v[24:27], a[192:195]\n"
        "v_add_f32 v193, v193, v194\n"
        "v_add_f32 v195, v195, v196\n"
        "v_mfma_f32_16x16x32_bf16 a[196:199], v[36:39], v[24:27], a[196:199]\n"
        "v_add_f32 v189, v189, v190\n"
        "v_add_f32 v191, v191, v192\n"
        "v_mfma_f32_16x16x32_bf16 a[200:203], v[40:43], v[24:27], a[200:203]\n"
        "s_waitcnt lgkmcnt(1)\n"
        "v_mul_f32 v194, v183, v185\n"
        "v_mul_f32 v196, v184, v185\n"
        "v_mfma_f32_16x16x32_bf16 a[204:207], v[44:47], v[24:27], a[204:207]\n"
        "v_cvt_pk_bf16_f32 v193, 0, v193\n"
        "v_cvt_pk_bf16_f32 v195, 0, v195\n"
        "v_mfma_f32_16x16x32_bf16 a[208:211], v[24:27], v[48:51], a[208:211]\n"
        "v_mul_f32 v190, v181, v185\n"
        "v_mul_f32 v192, v182, v185\n"
        "v_mfma_f32_16x16x32_bf16 a[212:215], v[24:27], v[52:55], a[212:215]\n"
        "v_cvt_pk_bf16_f32 v194, 0, v194\n"
        "v_cvt_pk_bf16_f32 v196, 0, v196\n"
        "v_mfma_f32_16x16x32_bf16 a[216:219], v[24:27], v[56:59], a[216:219]\n"
        "v_cvt_pk_bf16_f32 v189, 0, v189\n"
        "v_cvt_pk_bf16_f32 v191, 0, v191\n"
        "v_mfma_f32_16x16x32_bf16 a[220:223], v[24:27], v[60:63], a[220:223]\n"
        "ds_read_b128 v[24:27], v225 offset:12288\n"
        "v_cvt_pk_bf16_f32 v190, 0, v190\n"
        "v_mfma_f32_16x16x32_bf16 a[224:227], v[32:35], v[28:31], a[224:227]\n"
        "v_cvt_pk_bf16_f32 v192, 0, v192\n"
        "v_add_f32 v193, v193, v194\n"
        "v_mfma_f32_16x16x32_bf16 a[228:231], v[36:39], v[28:31], a[228:231]\n"
        "v_add_f32 v195, v195, v196\n"
        "v_add_f32 v189, v189, v190\n"
        "v_mfma_f32_16x16x32_bf16 a[232:235], v[40:43], v[28:31], a[232:235]\n"
        "v_add_f32 v191, v191, v192\n"
        "v_cvt_pk_bf16_f32 v193, 0, v193\n"
        "v_mfma_f32_16x16x32_bf16 a[236:239], v[44:47], v[28:31], a[236:239]\n"
        "v_and_b32 v194, 0xffff0000, v130\n"
        "v_cvt_pk_bf16_f32 v195, 0, v195\n"
        "v_mfma_f32_16x16x32_bf16 a[240:243], v[28:31], v[48:51], a[240:243]\n"
        "v_and_b32 v196, 0xffff0000, v132\n"
        "v_cndmask_b32 v101, v101, 0, s[78:79]\n"
        "v_mfma_f32_16x16x32_bf16 a[244:247], v[28:31], v[52:55], a[244:247]\n"
        "v_cvt_pk_bf16_f32 v189, 0, v189\n"
        "v_and_b32 v190, 0xffff0000, v126\n"
        "v_mfma_f32_16x16x32_bf16 a[248:251], v[28:31], v[56:59], a[248:251]\n"
        "v_cvt_pk_bf16_f32 v191, 0, v191\n"
        "v_and_b32 v192, 0xffff0000, v128\n"
        "v_mfma_f32_16x16x32_bf16 a[252:255], v[28:31], v[60:63], a[252:255]\n"
        "ds_read_b128 v[28:31], v225 offset:14336\n"
        "v_mul_f32 v193, v193, v194\n"
        "v_mfma_f32_16x16x32_bf16 a[0:3], v[64:67], v[0:3], a[0:3]\n"
        "v_mul_f32 v195, v195, v196\n"
        "v_cndmask_b32 v97, v97, 0, s[78:79]\n"
        "v_mfma_f32_16x16x32_bf16 a[4:7], v[68:71], v[0:3], a[4:7]\n"
        "v_cndmask_b32 v99, v99, 0, s[78:79]\n"
        "v_lshlrev_b32 v177, 16, v103\n"
        "v_mfma_f32_16x16x32_bf16 a[8:11], v[72:75], v[0:3], a[8:11]\n"
        "v_lshlrev_b32 v187, 16, v139\n"
        "v_lshlrev_b32 v178, 16, v105\n"
        "v_mfma_f32_16x16x32_bf16 a[12:15], v[76:79], v[0:3], a[12:15]\n"
        "v_and_b32 v188, 0xffff0000, v139\n"
        "v_lshlrev_b32 v176, 16, v101\n"
        "v_mfma_f32_16x16x32_bf16 a[16:19], v[0:3], v[80:83], a[16:19]\n"
        "v_mul_f32 v189, v189, v190\n"
        "v_mul_f32 v191, v191, v192\n"
        "v_mfma_f32_16x16x32_bf16 a[20:23], v[0:3], v[84:87], a[20:23]\n"
        "v_cvt_pk_bf16_f32 v208, v195, v193\n"
        "v_lshlrev_b32 v174, 16, v97\n"
        "v_mfma_f32_16x16x32_bf16 a[24:27], v[0:3], v[88:91], a[24:27]\n"
        "v_lshlrev_b32 v175, 16, v99\n"
        "v_mul_f32 v194, v177, v187\n"
        "v_mfma_f32_16x16x32_bf16 a[28:31], v[0:3], v[92:95], a[28:31]\n"
        "s_waitcnt vmcnt(9) lgkmcnt(0)\n"
        "s_barrier\n"
        "v_mfma_f32_16x16x32_bf16 a[32:35], v[64:67], v[4:7], a[32:35]\n"
        "global_load_lds_dwordx4 v234, s[56:57] offset:0\n"
        "ds_read_b128 v[0:3], v224 offset:32768\n"
        "v_mfma_f32_16x16x32_bf16 a[36:39], v[68:71], v[4:7], a[36:39]\n"
        "ds_read_b64_tr_b16 v[32:33], v216 offset:0\n"
        "v_mfma_f32_16x16x32_bf16 a[40:43], v[72:75], v[4:7], a[40:43]\n"
        "ds_read_b64_tr_b16 v[34:35], v217 offset:0\n"
        "v_mfma_f32_16x16x32_bf16 a[44:47], v[76:79], v[4:7], a[44:47]\n"
        "ds_read_b64_tr_b16 v[36:37], v218 offset:0\n"
        "v_mfma_f32_16x16x32_bf16 a[48:51], v[4:7], v[80:83], a[48:51]\n"
        "ds_read_b64_tr_b16 v[38:39], v219 offset:0\n"
        "global_load_lds_dwordx4 v235, s[56:57] offset:1024\n"
        "v_mfma_f32_16x16x32_bf16 a[52:55], v[4:7], v[84:87], a[52:55]\n"
        "ds_read_b64_tr_b16 v[40:41], v220 offset:0\n"
        "v_mfma_f32_16x16x32_bf16 a[56:59], v[4:7], v[88:91], a[56:59]\n"
        "ds_read_b64_tr_b16 v[42:43], v221 offset:0\n"
        "v_mfma_f32_16x16x32_bf16 a[60:63], v[4:7], v[92:95], a[60:63]\n"
        "ds_read_b128 v[4:7], v224 offset:34816\n"
        "v_mfma_f32_16x16x32_bf16 a[64:67], v[64:67], v[8:11], a[64:67]\n"
        "ds_read_b64_tr_b16 v[44:45], v222 offset:0\n"
        "global_load_lds_dwordx4 v236, s[56:57] offset:2048\n"
        "v_mfma_f32_16x16x32_bf16 a[68:71], v[68:71], v[8:11], a[68:71]\n"
        "ds_read_b64_tr_b16 v[46:47], v223 offset:0\n"
        "v_mul_f32 v196, v178, v187\n"
        "v_mfma_f32_16x16x32_bf16 a[72:75], v[72:75], v[8:11], a[72:75]\n"
        "ds_read_b64_tr_b16 v[48:49], v216 offset:16384\n"
        "v_cvt_pk_bf16_f32 v209, v191, v189\n"
        "v_mfma_f32_16x16x32_bf16 a[76:79], v[76:79], v[8:11], a[76:79]\n"
        "ds_read_b64_tr_b16 v[50:51], v217 offset:16384\n"
        "v_mul_f32 v193, v176, v188\n"
        "v_mfma_f32_16x16x32_bf16 a[80:83], v[8:11], v[80:83], a[80:83]\n"
        "global_load_lds_dwordx4 v237, s[56:57] offset:3072\n"
        "s_add_u32 m0, s72, 0x1000\n"
        "v_mfma_f32_16x16x32_bf16 a[84:87], v[8:11], v[84:87], a[84:87]\n"
        "ds_read_b64_tr_b16 v[52:53], v218 offset:16384\n"
        "s_add_u32 s56, s56, 0x80\n"
        "s_addc_u32 s57, s57, 0\n"
        "v_mfma_f32_16x16x32_bf16 a[88:91], v[8:11], v[88:91], a[88:91]\n"
        "ds_read_b64_tr_b16 v[54:55], v219 offset:16384\n"
        "v_mul_f32 v195, v177, v188\n"
        "v_mfma_f32_16x16x32_bf16 a[92:95], v[8:11], v[92:95], a[92:95]\n"
        "ds_read_b128 v[8:11], v224 offset:36864\n"
        "v_mul_f32 v192, v176, v187\n"
        "v_mfma_f32_16x16x32_bf16 a[96:99], v[64:67], v[12:15], a[96:99]\n"
        "global_load_lds_dwordx4 v234, s[58:59] offset:0\n"
        "v_mfma_f32_16x16x32_bf16 a[100:103], v[68:71], v[12:15], a[100:103]\n"
        "ds_read_b64_tr_b16 v[56:57], v220 offset:16384\n"
        "v_mul_f32 v190, v175, v187\n"
        "v_mfma_f32_16x16x32_bf16 a[104:107], v[72:75], v[12:15], a[104:107]\n"
        "ds_read_b64_tr_b16 v[58:59], v221 offset:16384\n"
        "v_cvt_pk_bf16_f32 v194, 0, v194\n"
        "v_mfma_f32_16x16x32_bf16 a[108:111], v[76:79], v[12:15], a[108:111]\n"
        "ds_read_b64_tr_b16 v[60:61], v222 offset:16384\n"
        "v_cvt_pk_bf16_f32 v196, 0, v196\n"
        "v_mfma_f32_16x16x32_bf16 a[112:115], v[12:15], v[80:83], a[112:115]\n"
        "global_load_lds_dwordx4 v235, s[58:59] offset:1024\n"
        "v_mfma_f32_16x16x32_bf16 a[116:119], v[12:15], v[84:87], a[116:119]\n"
        "ds_read_b64_tr_b16 v[62:63], v223 offset:16384\n"
        "v_mul_f32 v189, v174, v188\n"
        "v_mfma_f32_16x16x32_bf16 a[120:123], v[12:15], v[88:91], a[120:123]\n"
        "v_mul_f32 v191, v175, v188\n"
        "v_cvt_pk_bf16_f32 v193, 0, v193\n"
        "v_mfma_f32_16x16x32_bf16 a[124:127], v[12:15], v[92:95], a[124:127]\n"
        "ds_read_b128 v[12:15], v224 offset:38912\n"
        "v_cvt_pk_bf16_f32 v195, 0, v195\n"
        "v_mfma_f32_16x16x32_bf16 a[128:131], v[64:67], v[16:19], a[128:131]\n"
        "global_load_lds_dwordx4 v236, s[58:59] offset:2048\n"
        "v_mfma_f32_16x16x32_bf16 a[132:135], v[68:71], v[16:19], a[132:135]\n"
        "v_lshlrev_b32 v179, 16, v107\n"
        "v_and_b32 v186, 0xffff0000, v138\n"
        "v_mfma_f32_16x16x32_bf16 a[136:139], v[72:75], v[16:19], a[136:139]\n"
        "s_add_u32 s76, s38, 0x3001000\n"
        "s_addc_u32 s77, s39, 0\n"
        "v_cvt_pk_bf16_f32 v192, 0, v192\n"
        "v_mfma_f32_16x16x32_bf16 a[140:143], v[76:79], v[16:19], a[140:143]\n"
        "v_cvt_pk_bf16_f32 v190, 0, v190\n"
        "v_cvt_pk_bf16_f32 v189, 0, v189\n"
        "v_mfma_f32_16x16x32_bf16 a[144:147], v[16:19], v[80:83], a[144:147]\n"
        "global_load_lds_dwordx4 v237, s[58:59] offset:3072\n"
        "s_add_u32 m0, s73, 0x18000\n"
        "v_mfma_f32_16x16x32_bf16 a[148:151], v[16:19], v[84:87], a[148:151]\n"
        "s_add_u32 s58, s58, 0x80\n"
        "s_addc_u32 s59, s59, 0\n"
        "v_cvt_pk_bf16_f32 v191, 0, v191\n"
        "v_mfma_f32_16x16x32_bf16 a[152:155], v[16:19], v[88:91], a[152:155]\n"
        "v_add_f32 v193, v193, v194\n"
        "v_add_f32 v195, v195, v196\n"
        "v_mfma_f32_16x16x32_bf16 a[156:159], v[16:19], v[92:95], a[156:159]\n"
        "ds_read_b128 v[16:19], v224 offset:40960\n"
        "v_add_f32 v189, v189, v190\n"
        "v_mfma_f32_16x16x32_bf16 a[160:163], v[64:67], v[20:23], a[160:163]\n"
        "global_load_lds_dwordx4 v238, s[60:61] offset:0\n"
        "v_mfma_f32_16x16x32_bf16 a[164:167], v[68:71], v[20:23], a[164:167]\n"
        "v_add_f32 v191, v191, v192\n"
        "v_mul_f32 v194, v178, v186\n"
        "v_mfma_f32_16x16x32_bf16 a[168:171], v[72:75], v[20:23], a[168:171]\n"
        "v_mul_f32 v196, v179, v186\n"
        "v_cvt_pk_bf16_f32 v193, 0, v193\n"
        "v_mfma_f32_16x16x32_bf16 a[172:175], v[76:79], v[20:23], a[172:175]\n"
        "v_cvt_pk_bf16_f32 v195, 0, v195\n"
        "v_mul_f32 v190, v176, v186\n"
        "v_mfma_f32_16x16x32_bf16 a[176:179], v[20:23], v[80:83], a[176:179]\n"
        "global_load_lds_dwordx4 v239, s[60:61] offset:1024\n"
        "v_mfma_f32_16x16x32_bf16 a[180:183], v[20:23], v[84:87], a[180:183]\n"
        "v_mul_f32 v192, v177, v186\n"
        "v_cvt_pk_bf16_f32 v194, 0, v194\n"
        "v_mfma_f32_16x16x32_bf16 a[184:187], v[20:23], v[88:91], a[184:187]\n"
        "v_cvt_pk_bf16_f32 v196, 0, v196\n"
        "v_lshlrev_b32 v180, 16, v109\n"
        "v_mfma_f32_16x16x32_bf16 a[188:191], v[20:23], v[92:95], a[188:191]\n"
        "ds_read_b128 v[20:23], v224 offset:43008\n"
        "v_lshlrev_b32 v185, 16, v138\n"
        "v_mfma_f32_16x16x32_bf16 a[192:195], v[64:67], v[24:27], a[192:195]\n"
        "global_load_dword v253, v173, s[82:83] offset:0\n"
        "v_mfma_f32_16x16x32_bf16 a[196:199], v[68:71], v[24:27], a[196:199]\n"
        "global_load_lds_dwordx4 v240, s[60:61] offset:2048\n"
        "s_add_u32 s82, s12, 0x500000\n"
        "s_addc_u32 s83, s13, 0\n"
        "v_mfma_f32_16x16x32_bf16 a[200:203], v[72:75], v[24:27], a[200:203]\n"
        "v_cvt_pk_bf16_f32 v189, 0, v189\n"
        "v_cvt_pk_bf16_f32 v191, 0, v191\n"
        "v_mfma_f32_16x16x32_bf16 a[204:207], v[76:79], v[24:27], a[204:207]\n"
        "v_cvt_pk_bf16_f32 v190, 0, v190\n"
        "v_cvt_pk_bf16_f32 v192, 0, v192\n"
        "v_mfma_f32_16x16x32_bf16 a[208:211], v[24:27], v[80:83], a[208:211]\n"
        "v_add_f32 v193, v193, v194\n"
        "v_add_f32 v195, v195, v196\n"
        "v_mfma_f32_16x16x32_bf16 a[212:215], v[24:27], v[84:87], a[212:215]\n"
        "global_load_lds_dwordx4 v241, s[60:61] offset:3072\n"
        "s_add_u32 m0, s73, 0x1c000\n"
        "v_add_f32 v189, v189, v190\n"
        "v_mfma_f32_16x16x32_bf16 a[216:219], v[24:27], v[88:91], a[216:219]\n"
        "v_add_f32 v191, v191, v192\n"
        "v_mul_f32 v194, v179, v185\n"
        "v_mfma_f32_16x16x32_bf16 a[220:223], v[24:27], v[92:95], a[220:223]\n"
        "ds_read_b128 v[24:27], v224 offset:45056\n"
        "v_mul_f32 v196, v180, v185\n"
        "v_mfma_f32_16x16x32_bf16 a[224:227], v[64:67], v[28:31], a[224:227]\n"
        "ds_read_b64_tr_b16 v[64:65], v216 offset:8192\n"
        "v_cvt_pk_bf16_f32 v193, 0, v193\n"
        "v_mfma_f32_16x16x32_bf16 a[228:231], v[68:71], v[28:31], a[228:231]\n"
        "global_load_lds_dwordx4 v238, s[62:63] offset:0\n"
        "ds_read_b64_tr_b16 v[66:67], v217 offset:8192\n"
        "v_mfma_f32_16x16x32_bf16 a[232:235], v[72:75], v[28:31], a[232:235]\n"
        "ds_read_b64_tr_b16 v[68:69], v218 offset:8192\n"
        "v_cvt_pk_bf16_f32 v195, 0, v195\n"
        "v_mfma_f32_16x16x32_bf16 a[236:239], v[76:79], v[28:31], a[236:239]\n"
        "ds_read_b64_tr_b16 v[70:71], v219 offset:8192\n"
        "v_mul_f32 v190, v177, v185\n"
        "v_mfma_f32_16x16x32_bf16 a[240:243], v[28:31], v[80:83], a[240:243]\n"
        "ds_read_b64_tr_b16 v[72:73], v220 offset:8192\n"
        "v_mul_f32 v192, v178, v185\n"
        "v_mfma_f32_16x16x32_bf16 a[244:247], v[28:31], v[84:87], a[244:247]\n"
        "global_load_lds_dwordx4 v239, s[62:63] offset:1024\n"
        "ds_read_b64_tr_b16 v[74:75], v221 offset:8192\n"
        "v_mfma_f32_16x16x32_bf16 a[248:251], v[28:31], v[88:91], a[248:251]\n"
        "ds_read_b64_tr_b16 v[76:77], v222 offset:8192\n"
        "v_cvt_pk_bf16_f32 v194, 0, v194\n"
        "v_mfma_f32_16x16x32_bf16 a[252:255], v[28:31], v[92:95], a[252:255]\n"
        "ds_read_b128 v[28:31], v224 offset:47104\n"
        "s_waitcnt lgkmcnt(8)\n"
        "v_mfma_f32_16x16x32_bf16 a[0:3], v[32:35], v[0:3], a[0:3]\n"
        "ds_read_b64_tr_b16 v[78:79], v223 offset:8192\n"
        "v_cvt_pk_bf16_f32 v196, 0, v196\n"
        "v_mfma_f32_16x16x32_bf16 a[4:7], v[36:39], v[0:3], a[4:7]\n"
        "global_load_lds_dwordx4 v240, s[62:63] offset:2048\n"
        "ds_read_b64_tr_b16 v[80:81], v216 offset:24576\n"
        "v_mfma_f32_16x16x32_bf16 a[8:11], v[40:43], v[0:3], a[8:11]\n"
        "ds_read_b64_tr_b16 v[82:83], v217 offset:24576\n"
        "v_cvt_pk_bf16_f32 v189, 0, v189\n"
        "v_mfma_f32_16x16x32_bf16 a[12:15], v[44:47], v[0:3], a[12:15]\n"
        "ds_read_b64_tr_b16 v[84:85], v218 offset:24576\n"
        "v_cvt_pk_bf16_f32 v191, 0, v191\n"
        "v_mfma_f32_16x16x32_bf16 a[16:19], v[0:3], v[48:51], a[16:19]\n"
        "ds_read_b64_tr_b16 v[86:87], v219 offset:24576\n"
        "v_cvt_pk_bf16_f32 v190, 0, v190\n"
        "v_mfma_f32_16x16x32_bf16 a[20:23], v[0:3], v[52:55], a[20:23]\n"
        "global_load_lds_dwordx4 v241, s[62:63] offset:3072\n"
        "ds_read_b64_tr_b16 v[88:89], v220 offset:24576\n"
        "s_add_u32 m0, s72, 0x8000\n"
        "v_mfma_f32_16x16x32_bf16 a[24:27], v[0:3], v[56:59], a[24:27]\n"
        "ds_read_b64_tr_b16 v[90:91], v221 offset:24576\n"
        "v_cvt_pk_bf16_f32 v192, 0, v192\n"
        "v_mfma_f32_16x16x32_bf16 a[28:31], v[0:3], v[60:63], a[28:31]\n"
        "ds_read_b128 v[0:3], v225 offset:32768\n"
        "v_add_f32 v193, v193, v194\n"
        "v_mfma_f32_16x16x32_bf16 a[32:35], v[32:35], v[4:7], a[32:35]\n"
        "ds_read_b64_tr_b16 v[92:93], v222 offset:24576\n"
        "v_add_f32 v195, v195, v196\n"
        "v_mfma_f32_16x16x32_bf16 a[36:39], v[36:39], v[4:7], a[36:39]\n"
        "ds_read_b64_tr_b16 v[94:95], v223 offset:24576\n"
        "v_add_f32 v189, v189, v190\n"
        "v_mfma_f32_16x16x32_bf16 a[40:43], v[40:43], v[4:7], a[40:43]\n"
        "v_add_f32 v191, v191, v192\n"
        "v_cvt_pk_bf16_f32 v193, 0, v193\n"
        "v_mfma_f32_16x16x32_bf16 a[44:47], v[44:47], v[4:7], a[44:47]\n"
        "v_lshlrev_b32 v194, 16, v123\n"
        "v_cvt_pk_bf16_f32 v195, 0, v195\n"
        "v_mfma_f32_16x16x32_bf16 a[48:51], v[4:7], v[48:51], a[48:51]\n"
        "v_lshlrev_b32 v196, 16, v125\n"
        "v_cvt_pk_bf16_f32 v189, 0, v189\n"
        "v_mfma_f32_16x16x32_bf16 a[52:55], v[4:7], v[52:55], a[52:55]\n"
        "global_store_dwordx4 v171, v[200:203], s[76:77] offset:-4096 sc1\n"
        "v_lshlrev_b32 v190, 16, v119\n"
        "v_cvt_pk_bf16_f32 v191, 0, v191\n"
        "v_lshlrev_b32 v192, 16, v121\n"
        "v_mul_f32 v193, v193, v194\n"
        "v_mfma_f32_16x16x32_bf16 a[56:59], v[4:7], v[56:59], a[56:59]\n"
        "v_mul_f32 v195, v195, v196\n"
        "v_lshlrev_b32 v181, 16, v111\n"
        "v_mfma_f32_16x16x32_bf16 a[60:63], v[4:7], v[60:63], a[60:63]\n"
        "ds_read_b128 v[4:7], v225 offset:34816\n"
        "v_lshlrev_b32 v182, 16, v113\n"
        "v_mfma_f32_16x16x32_bf16 a[64:67], v[32:35], v[8:11], a[64:67]\n"
        "v_mul_f32 v189, v189, v190\n"
        "v_mul_f32 v191, v191, v192\n"
        "v_mfma_f32_16x16x32_bf16 a[68:71], v[36:39], v[8:11], a[68:71]\n"
        "v_cvt_pk_bf16_f32 v202, v195, v193\n"
        "v_mul_f32 v194, v181, v187\n"
        "v_mfma_f32_16x16x32_bf16 a[72:75], v[40:43], v[8:11], a[72:75]\n"
        "v_mul_f32 v196, v182, v187\n"
        "v_cvt_pk_bf16_f32 v203, v191, v189\n"
        "v_mfma_f32_16x16x32_bf16 a[76:79], v[44:47], v[8:11], a[76:79]\n"
        "v_mul_f32 v193, v180, v188\n"
        "v_mul_f32 v195, v181, v188\n"
        "v_mfma_f32_16x16x32_bf16 a[80:83], v[8:11], v[48:51], a[80:83]\n"
        "v_mul_f32 v190, v179, v187\n"
        "v_mul_f32 v192, v180, v187\n"
        "v_mfma_f32_16x16x32_bf16 a[84:87], v[8:11], v[52:55], a[84:87]\n"
        "v_cvt_pk_bf16_f32 v194, 0, v194\n"
        "v_cvt_pk_bf16_f32 v196, 0, v196\n"
        "v_mfma_f32_16x16x32_bf16 a[88:91], v[8:11], v[56:59], a[88:91]\n"
        "v_mul_f32 v189, v178, v188\n"
        "v_mul_f32 v191, v179, v188\n"
        "v_mfma_f32_16x16x32_bf16 a[92:95], v[8:11], v[60:63], a[92:95]\n"
        "ds_read_b128 v[8:11], v225 offset:36864\n"
        "v_cvt_pk_bf16_f32 v193, 0, v193\n"
        "v_mfma_f32_16x16x32_bf16 a[96:99], v[32:35], v[12:15], a[96:99]\n"
        "v_cvt_pk_bf16_f32 v195, 0, v195\n"
        "v_lshlrev_b32 v183, 16, v115\n"
        "v_mfma_f32_16x16x32_bf16 a[100:103], v[36:39], v[12:15], a[100:103]\n"
        "v_cvt_pk_bf16_f32 v190, 0, v190\n"
        "v_cvt_pk_bf16_f32 v192, 0, v192\n"
        "v_mfma_f32_16x16x32_bf16 a[104:107], v[40:43], v[12:15], a[104:107]\n"
        "v_cvt_pk_bf16_f32 v189, 0, v189\n"
        "v_cvt_pk_bf16_f32 v191, 0, v191\n"
        "v_mfma_f32_16x16x32_bf16 a[108:111], v[44:47], v[12:15], a[108:111]\n"
        "v_add_f32 v193, v193, v194\n"
        "v_add_f32 v195, v195, v196\n"
        "v_mfma_f32_16x16x32_bf16 a[112:115], v[12:15], v[48:51], a[112:115]\n"
        "v_add_f32 v189, v189, v190\n"
        "v_add_f32 v191, v191, v192\n"
        "v_mfma_f32_16x16x32_bf16 a[116:119], v[12:15], v[52:55], a[116:119]\n"
        "v_mul_f32 v194, v182, v186\n"
        "v_mul_f32 v196, v183, v186\n"
        "v_mfma_f32_16x16x32_bf16 a[120:123], v[12:15], v[56:59], a[120:123]\n"
        "v_cvt_pk_bf16_f32 v193, 0, v193\n"
        "v_cvt_pk_bf16_f32 v195, 0, v195\n"
        "v_mfma_f32_16x16x32_bf16 a[124:127], v[12:15], v[60:63], a[124:127]\n"
        "ds_read_b128 v[12:15], v225 offset:38912\n"
        "v_mul_f32 v190, v180, v186\n"
        "v_mfma_f32_16x16x32_bf16 a[128:131], v[32:35], v[16:19], a[128:131]\n"
        "v_mul_f32 v192, v181, v186\n"
        "v_cvt_pk_bf16_f32 v194, 0, v194\n"
        "v_mfma_f32_16x16x32_bf16 a[132:135], v[36:39], v[16:19], a[132:135]\n"
        "v_cvt_pk_bf16_f32 v196, 0, v196\n"
        "v_lshlrev_b32 v184, 16, v117\n"
        "v_mfma_f32_16x16x32_bf16 a[136:139], v[40:43], v[16:19], a[136:139]\n"
        "v_cvt_pk_bf16_f32 v189, 0, v189\n"
        "v_cvt_pk_bf16_f32 v191, 0, v191\n"
        "v_mfma_f32_16x16x32_bf16 a[140:143], v[44:47], v[16:19], a[140:143]\n"
        "v_cvt_pk_bf16_f32 v190, 0, v190\n"
        "v_cvt_pk_bf16_f32 v192, 0, v192\n"
        "v_mfma_f32_16x16x32_bf16 a[144:147], v[16:19], v[48:51], a[144:147]\n"
        "v_add_f32 v193, v193, v194\n"
        "v_add_f32 v195, v195, v196\n"
        "v_mfma_f32_16x16x32_bf16 a[148:151], v[16:19], v[52:55], a[148:151]\n"
        "v_add_f32 v189, v189, v190\n"
        "v_add_f32 v191, v191, v192\n"
        "v_mfma_f32_16x16x32_bf16 a[152:155], v[16:19], v[56:59], a[152:155]\n"
        "v_mul_f32 v194, v183, v185\n"
        "v_mul_f32 v196, v184, v185\n"
        "v_mfma_f32_16x16x32_bf16 a[156:159], v[16:19], v[60:63], a[156:159]\n"
        "ds_read_b128 v[16:19], v225 offset:40960\n"
        "v_cvt_pk_bf16_f32 v193, 0, v193\n"
        "v_mfma_f32_16x16x32_bf16 a[160:163], v[32:35], v[20:23], a[160:163]\n"
        "v_cvt_pk_bf16_f32 v195, 0, v195\n"
        "v_mul_f32 v190, v181, v185\n"
        "v_mfma_f32_16x16x32_bf16 a[164:167], v[36:39], v[20:23], a[164:167]\n"
        "v_mul_f32 v192, v182, v185\n"
        "v_cvt_pk_bf16_f32 v194, 0, v194\n"
        "v_mfma_f32_16x16x32_bf16 a[168:171], v[40:43], v[20:23], a[168:171]\n"
        "v_cvt_pk_bf16_f32 v196, 0, v196\n"
        "v_cvt_pk_bf16_f32 v189, 0, v189\n"
        "v_mfma_f32_16x16x32_bf16 a[172:175], v[44:47], v[20:23], a[172:175]\n"
        "v_cvt_pk_bf16_f32 v191, 0, v191\n"
        "v_cvt_pk_bf16_f32 v190, 0, v190\n"
        "v_mfma_f32_16x16x32_bf16 a[176:179], v[20:23], v[48:51], a[176:179]\n"
        "v_cvt_pk_bf16_f32 v192, 0, v192\n"
        "v_add_f32 v193, v193, v194\n"
        "v_mfma_f32_16x16x32_bf16 a[180:183], v[20:23], v[52:55], a[180:183]\n"
        "v_add_f32 v195, v195, v196\n"
        "v_add_f32 v189, v189, v190\n"
        "v_mfma_f32_16x16x32_bf16 a[184:187], v[20:23], v[56:59], a[184:187]\n"
        "v_add_f32 v191, v191, v192\n"
        "v_cvt_pk_bf16_f32 v193, 0, v193\n"
        "v_mfma_f32_16x16x32_bf16 a[188:191], v[20:23], v[60:63], a[188:191]\n"
        "ds_read_b128 v[20:23], v225 offset:43008\n"
        "v_lshlrev_b32 v194, 16, v131\n"
        "v_mfma_f32_16x16x32_bf16 a[192:195], v[32:35], v[24:27], a[192:195]\n"
        "v_cvt_pk_bf16_f32 v195, 0, v195\n"
        "v_lshlrev_b32 v196, 16, v133\n"
        "v_mfma_f32_16x16x32_bf16 a[196:199], v[36:39], v[24:27], a[196:199]\n"
        "v_cvt_pk_bf16_f32 v189, 0, v189\n"
        "v_lshlrev_b32 v190, 16, v127\n"
        "v_mfma_f32_16x16x32_bf16 a[200:203], v[40:43], v[24:27], a[200:203]\n"
        "s_waitcnt lgkmcnt(1)\n"
        "v_cvt_pk_bf16_f32 v191, 0, v191\n"
        "v_lshlrev_b32 v192, 16, v129\n"
        "v_mfma_f32_16x16x32_bf16 a[204:207], v[44:47], v[24:27], a[204:207]\n"
        "v_mul_f32 v193, v193, v194\n"
        "v_mul_f32 v195, v195, v196\n"
        "v_mfma_f32_16x16x32_bf16 a[208:211], v[24:27], v[48:51], a[208:211]\n"
        "v_and_b32 v176, 0xffff0000, v101\n"
        "v_and_b32 v177, 0xffff0000, v103\n"
        "v_mfma_f32_16x16x32_bf16 a[212:215], v[24:27], v[52:55], a[212:215]\n"
        "v_lshlrev_b32 v187, 16, v141\n"
        "v_and_b32 v178, 0xffff0000, v105\n"
        "v_mfma_f32_16x16x32_bf16 a[216:219], v[24:27], v[56:59], a[216:219]\n"
        "v_and_b32 v188, 0xffff0000, v141\n"
        "v_mul_f32 v189, v189, v190\n"
        "v_mfma_f32_16x16x32_bf16 a[220:223], v[24:27], v[60:63], a[220:223]\n"
        "ds_read_b128 v[24:27], v225 offset:45056\n"
        "global_load_dword v253, v173, s[82:83] offset:0\n"
        "v_mfma_f32_16x16x32_bf16 a[224:227], v[32:35], v[28:31], a[224:227]\n"
        "s_add_u32 s82, s12, 0x600000\n"
        "s_addc_u32 s83, s13, 0\n"
        "v_mul_f32 v191, v191, v192\n"
        "v_mfma_f32_16x16x32_bf16 a[228:231], v[36:39], v[28:31], a[228:231]\n"
        "v_cvt_pk_bf16_f32 v200, v195, v193\n"
        "v_and_b32 v174, 0xffff0000, v97\n"
        "v_mfma_f32_16x16x32_bf16 a[232:235], v[40:43], v[28:31], a[232:235]\n"
        "v_and_b32 v175, 0xffff0000, v99\n"
        "v_mul_f32 v194, v177, v187\n"
        "v_mfma_f32_16x16x32_bf16 a[236:239], v[44:47], v[28:31], a[236:239]\n"
        "v_mul_f32 v196, v178, v187\n"
        "v_cvt_pk_bf16_f32 v201, v191, v189\n"
        "v_mfma_f32_16x16x32_bf16 a[240:243], v[28:31], v[48:51], a[240:243]\n"
        "v_mul_f32 v193, v176, v188\n"
        "v_mul_f32 v195, v177, v188\n"
        "v_mfma_f32_16x16x32_bf16 a[244:247], v[28:31], v[52:55], a[244:247]\n"
        "v_mul_f32 v192, v176, v187\n"
        "v_mul_f32 v190, v175, v187\n"
        "v_mfma_f32_16x16x32_bf16 a[248:251], v[28:31], v[56:59], a[248:251]\n"
        "v_cvt_pk_bf16_f32 v194, 0, v194\n"
        "v_cvt_pk_bf16_f32 v196, 0, v196\n"
        "v_mfma_f32_16x16x32_bf16 a[252:255], v[28:31], v[60:63], a[252:255]\n"
        "ds_read_b128 v[28:31], v225 offset:47104\n"
        "v_mul_f32 v189, v174, v188\n"
        "v_mfma_f32_16x16x32_bf16 a[0:3], v[64:67], v[0:3], a[0:3]\n"
        "v_mul_f32 v191, v175, v188\n"
        "v_cvt_pk_bf16_f32 v193, 0, v193\n"
        "v_mfma_f32_16x16x32_bf16 a[4:7], v[68:71], v[0:3], a[4:7]\n"
        "v_cvt_pk_bf16_f32 v195, 0, v195\n"
        "v_and_b32 v179, 0xffff0000, v107\n"
        "v_mfma_f32_16x16x32_bf16 a[8:11], v[72:75], v[0:3], a[8:11]\n"
        "v_and_b32 v186, 0xffff0000, v140\n"
        "v_cvt_pk_bf16_f32 v192, 0, v192\n"
        "v_mfma_f32_16x16x32_bf16 a[12:15], v[76:79], v[0:3], a[12:15]\n"
        "v_cvt_pk_bf16_f32 v190, 0, v190\n"
        "v_cvt_pk_bf16_f32 v189, 0, v189\n"
        "v_mfma_f32_16x16x32_bf16 a[16:19], v[0:3], v[80:83], a[16:19]\n"
        "v_cvt_pk_bf16_f32 v191, 0, v191\n"
        "v_add_f32 v193, v193, v194\n"
        "v_mfma_f32_16x16x32_bf16 a[20:23], v[0:3], v[84:87], a[20:23]\n"
        "v_add_f32 v195, v195, v196\n"
        "v_add_f32 v189, v189, v190\n"
        "v_mfma_f32_16x16x32_bf16 a[24:27], v[0:3], v[88:91], a[24:27]\n"
        "v_add_f32 v191, v191, v192\n"
        "v_mul_f32 v194, v178, v186\n"
        "v_mfma_f32_16x16x32_bf16 a[28:31], v[0:3], v[92:95], a[28:31]\n"
        "s_waitcnt vmcnt(11) lgkmcnt(0)\n"
        "s_barrier\n"
        "v_mfma_f32_16x16x32_bf16 a[32:35], v[64:67], v[4:7], a[32:35]\n"
        "global_load_lds_dwordx4 v234, s[56:57] offset:0\n"
        "ds_read_b128 v[0:3], v224 offset:0\n"
        "v_mfma_f32_16x16x32_bf16 a[36:39], v[68:71], v[4:7], a[36:39]\n"
        "ds_read_b64_tr_b16 v[32:33], v226 offset:0\n"
        "v_mfma_f32_16x16x32_bf16 a[40:43], v[72:75], v[4:7], a[40:43]\n"
        "ds_read_b64_tr_b16 v[34:35], v227 offset:0\n"
        "v_mfma_f32_16x16x32_bf16 a[44:47], v[76:79], v[4:7], a[44:47]\n"
        "ds_read_b64_tr_b16 v[36:37], v228 offset:0\n"
        "v_mfma_f32_16x16x32_bf16 a[48:51], v[4:7], v[80:83], a[48:51]\n"
        "ds_read_b64_tr_b16 v[38:39], v229 offset:0\n"
        "global_load_lds_dwordx4 v235, s[56:57] offset:1024\n"
        "v_mfma_f32_16x16x32_bf16 a[52:55], v[4:7], v[84:87], a[52:55]\n"
        "ds_read_b64_tr_b16 v[40:41], v230 offset:0\n"
        "v_mfma_f32_16x16x32_bf16 a[56:59], v[4:7], v[88:91], a[56:59]\n"
        "ds_read_b64_tr_b16 v[42:43], v231 offset:0\n"
        "v_mfma_f32_16x16x32_bf16 a[60:63], v[4:7], v[92:95], a[60:63]\n"
        "ds_read_b128 v[4:7], v224 offset:2048\n"
        "v_mfma_f32_16x16x32_bf16 a[64:67], v[64:67], v[8:11], a[64:67]\n"
        "ds_read_b64_tr_b16 v[44:45], v232 offset:0\n"
        "global_load_lds_dwordx4 v236, s[56:57] offset:2048\n"
        "v_mfma_f32_16x16x32_bf16 a[68:71], v[68:71], v[8:11], a[68:71]\n"
        "ds_read_b64_tr_b16 v[46:47], v233 offset:0\n"
        "v_mul_f32 v196, v179, v186\n"
        "v_mfma_f32_16x16x32_bf16 a[72:75], v[72:75], v[8:11], a[72:75]\n"
        "ds_read_b64_tr_b16 v[48:49], v226 offset:16384\n"
        "v_cvt_pk_bf16_f32 v193, 0, v193\n"
        "v_mfma_f32_16x16x32_bf16 a[76:79], v[76:79], v[8:11], a[76:79]\n"
        "ds_read_b64_tr_b16 v[50:51], v227 offset:16384\n"
        "v_cvt_pk_bf16_f32 v195, 0, v195\n"
        "v_mfma_f32_16x16x32_bf16 a[80:83], v[8:11], v[80:83], a[80:83]\n"
        "global_load_lds_dwordx4 v237, s[56:57] offset:3072\n"
        "s_add_u32 m0, s72, 0x9000\n"
        "v_mfma_f32_16x16x32_bf16 a[84:87], v[8:11], v[84:87], a[84:87]\n"
        "ds_read_b64_tr_b16 v[52:53], v228 offset:16384\n"
        "v_mul_f32 v190, v176, v186\n"
        "v_mfma_f32_16x16x32_bf16 a[88:91], v[8:11], v[88:91], a[88:91]\n"
        "ds_read_b64_tr_b16 v[54:55], v229 offset:16384\n"
        "v_mul_f32 v192, v177, v186\n"
        "v_mfma_f32_16x16x32_bf16 a[92:95], v[8:11], v[92:95], a[92:95]\n"
        "ds_read_b128 v[8:11], v224 offset:4096\n"
        "v_cvt_pk_bf16_f32 v194, 0, v194\n"
        "v_mfma_f32_16x16x32_bf16 a[96:99], v[64:67], v[12:15], a[96:99]\n"
        "global_load_lds_dwordx4 v234, s[58:59] offset:0\n"
        "v_mfma_f32_16x16x32_bf16 a[100:103], v[68:71], v[12:15], a[100:103]\n"
        "ds_read_b64_tr_b16 v[56:57], v230 offset:16384\n"
        "v_cvt_pk_bf16_f32 v196, 0, v196\n"
        "v_mfma_f32_16x16x32_bf16 a[104:107], v[72:75], v[12:15], a[104:107]\n"
        "ds_read_b64_tr_b16 v[58:59], v231 offset:16384\n"
        "v_and_b32 v180, 0xffff0000, v109\n"
        "v_mfma_f32_16x16x32_bf16 a[108:111], v[76:79], v[12:15], a[108:111]\n"
        "ds_read_b64_tr_b16 v[60:61], v232 offset:16384\n"
        "v_lshlrev_b32 v185, 16, v140\n"
        "v_mfma_f32_16x16x32_bf16 a[112:115], v[12:15], v[80:83], a[112:115]\n"
        "global_load_lds_dwordx4 v235, s[58:59] offset:1024\n"
        "v_mfma_f32_16x16x32_bf16 a[116:119], v[12:15], v[84:87], a[116:119]\n"
        "ds_read_b64_tr_b16 v[62:63], v233 offset:16384\n"
        "v_cvt_pk_bf16_f32 v189, 0, v189\n"
        "v_mfma_f32_16x16x32_bf16 a[120:123], v[12:15], v[88:91], a[120:123]\n"
        "v_cvt_pk_bf16_f32 v191, 0, v191\n"
        "v_cvt_pk_bf16_f32 v190, 0, v190\n"
        "v_mfma_f32_16x16x32_bf16 a[124:127], v[12:15], v[92:95], a[124:127]\n"
        "ds_read_b128 v[12:15], v224 offset:6144\n"
        "v_cvt_pk_bf16_f32 v192, 0, v192\n"
        "v_mfma_f32_16x16x32_bf16 a[128:131], v[64:67], v[16:19], a[128:131]\n"
        "global_load_lds_dwordx4 v236, s[58:59] offset:2048\n"
        "v_mfma_f32_16x16x32_bf16 a[132:135], v[68:71], v[16:19], a[132:135]\n"
        "v_add_f32 v193, v193, v194\n"
        "v_add_f32 v195, v195, v196\n"
        "v_mfma_f32_16x16x32_bf16 a[136:139], v[72:75], v[16:19], a[136:139]\n"
        "v_add_f32 v189, v189, v190\n"
        "v_add_f32 v191, v191, v192\n"
        "v_mfma_f32_16x16x32_bf16 a[140:143], v[76:79], v[16:19], a[140:143]\n"
        "v_mul_f32 v194, v179, v185\n"
        "v_mul_f32 v196, v180, v185\n"
        "v_mfma_f32_16x16x32_bf16 a[144:147], v[16:19], v[80:83], a[144:147]\n"
        "global_load_lds_dwordx4 v237, s[58:59] offset:3072\n"
        "s_add_u32 m0, s73, 0x20000\n"
        "v_mfma_f32_16x16x32_bf16 a[148:151], v[16:19], v[84:87], a[148:151]\n"
        "v_cvt_pk_bf16_f32 v193, 0, v193\n"
        "v_cvt_pk_bf16_f32 v195, 0, v195\n"
        "v_mfma_f32_16x16x32_bf16 a[152:155], v[16:19], v[88:91], a[152:155]\n"
        "v_mul_f32 v190, v177, v185\n"
        "v_mul_f32 v192, v178, v185\n"
        "v_mfma_f32_16x16x32_bf16 a[156:159], v[16:19], v[92:95], a[156:159]\n"
        "ds_read_b128 v[16:19], v224 offset:8192\n"
        "v_cvt_pk_bf16_f32 v194, 0, v194\n"
        "v_mfma_f32_16x16x32_bf16 a[160:163], v[64:67], v[20:23], a[160:163]\n"
        "global_load_lds_dwordx4 v238, s[68:69] offset:0\n"
        "v_mfma_f32_16x16x32_bf16 a[164:167], v[68:71], v[20:23], a[164:167]\n"
        "v_cvt_pk_bf16_f32 v196, 0, v196\n"
        "v_cvt_pk_bf16_f32 v189, 0, v189\n"
        "v_mfma_f32_16x16x32_bf16 a[168:171], v[72:75], v[20:23], a[168:171]\n"
        "v_cvt_pk_bf16_f32 v191, 0, v191\n"
        "v_cvt_pk_bf16_f32 v190, 0, v190\n"
        "v_mfma_f32_16x16x32_bf16 a[172:175], v[76:79], v[20:23], a[172:175]\n"
        "v_cvt_pk_bf16_f32 v192, 0, v192\n"
        "v_add_f32 v193, v193, v194\n"
        "v_mfma_f32_16x16x32_bf16 a[176:179], v[20:23], v[80:83], a[176:179]\n"
        "global_load_lds_dwordx4 v239, s[68:69] offset:1024\n"
        "v_mfma_f32_16x16x32_bf16 a[180:183], v[20:23], v[84:87], a[180:183]\n"
        "v_add_f32 v195, v195, v196\n"
        "v_add_f32 v189, v189, v190\n"
        "v_mfma_f32_16x16x32_bf16 a[184:187], v[20:23], v[88:91], a[184:187]\n"
        "v_add_f32 v191, v191, v192\n"
        "v_cvt_pk_bf16_f32 v193, 0, v193\n"
        "v_mfma_f32_16x16x32_bf16 a[188:191], v[20:23], v[92:95], a[188:191]\n"
        "ds_read_b128 v[20:23], v224 offset:10240\n"
        "v_and_b32 v194, 0xffff0000, v123\n"
        "v_mfma_f32_16x16x32_bf16 a[192:195], v[64:67], v[24:27], a[192:195]\n"
        "global_load_lds_dwordx4 v240, s[68:69] offset:2048\n"
        "v_mfma_f32_16x16x32_bf16 a[196:199], v[68:71], v[24:27], a[196:199]\n"
        "v_cvt_pk_bf16_f32 v195, 0, v195\n"
        "v_and_b32 v196, 0xffff0000, v125\n"
        "v_mfma_f32_16x16x32_bf16 a[200:203], v[72:75], v[24:27], a[200:203]\n"
        "v_cvt_pk_bf16_f32 v189, 0, v189\n"
        "v_and_b32 v190, 0xffff0000, v119\n"
        "v_mfma_f32_16x16x32_bf16 a[204:207], v[76:79], v[24:27], a[204:207]\n"
        "v_cvt_pk_bf16_f32 v191, 0, v191\n"
        "v_and_b32 v192, 0xffff0000, v121\n"
        "v_mfma_f32_16x16x32_bf16 a[208:211], v[24:27], v[80:83], a[208:211]\n"
        "global_load_lds_dwordx4 v241, s[68:69] offset:3072\n"
        "s_add_u32 m0, s73, 0x24000\n"
        "v_mfma_f32_16x16x32_bf16 a[212:215], v[24:27], v[84:87], a[212:215]\n"
        "s_add_u32 s68, s68, 0x80000\n"
        "s_addc_u32 s69, s69, 0\n"
        "v_mul_f32 v193, v193, v194\n"
        "v_mfma_f32_16x16x32_bf16 a[216:219], v[24:27], v[88:91], a[216:219]\n"
        "v_mul_f32 v195, v195, v196\n"
        "v_and_b32 v181, 0xffff0000, v111\n"
        "v_mfma_f32_16x16x32_bf16 a[220:223], v[24:27], v[92:95], a[220:223]\n"
        "ds_read_b128 v[24:27], v224 offset:12288\n"
        "v_and_b32 v182, 0xffff0000, v113\n"
        "v_mfma_f32_16x16x32_bf16 a[224:227], v[64:67], v[28:31], a[224:227]\n"
        "global_load_lds_dwordx4 v238, s[68:69] offset:0\n"
        "v_mfma_f32_16x16x32_bf16 a[228:231], v[68:71], v[28:31], a[228:231]\n"
        "ds_read_b64_tr_b16 v[64:65], v226 offset:8192\n"
        "v_mul_f32 v189, v189, v190\n"
        "v_mfma_f32_16x16x32_bf16 a[232:235], v[72:75], v[28:31], a[232:235]\n"
        "ds_read_b64_tr_b16 v[66:67], v227 offset:8192\n"
        "v_mul_f32 v191, v191, v192\n"
        "v_mfma_f32_16x16x32_bf16 a[236:239], v[76:79], v[28:31], a[236:239]\n"
        "ds_read_b64_tr_b16 v[68:69], v228 offset:8192\n"
        "v_mul_f32 v194, v181, v187\n"
        "v_mfma_f32_16x16x32_bf16 a[240:243], v[28:31], v[80:83], a[240:243]\n"
        "global_load_lds_dwordx4 v239, s[68:69] offset:1024\n"
        "v_mfma_f32_16x16x32_bf16 a[244:247], v[28:31], v[84:87], a[244:247]\n"
        "ds_read_b64_tr_b16 v[70:71], v229 offset:8192\n"
        "v_mul_f32 v196, v182, v187\n"
        "v_mfma_f32_16x16x32_bf16 a[248:251], v[28:31], v[88:91], a[248:251]\n"
        "ds_read_b64_tr_b16 v[72:73], v230 offset:8192\n"
        "v_mul_f32 v192, v180, v187\n"
        "v_mfma_f32_16x16x32_bf16 a[252:255], v[28:31], v[92:95], a[252:255]\n"
        "ds_read_b128 v[28:31], v224 offset:14336\n"
        "s_waitcnt lgkmcnt(6)\n"
        "v_mfma_f32_16x16x32_bf16 a[0:3], v[32:35], v[0:3], a[0:3]\n"
        "global_load_lds_dwordx4 v240, s[68:69] offset:2048\n"
        "v_mfma_f32_16x16x32_bf16 a[4:7], v[36:39], v[0:3], a[4:7]\n"
        "ds_read_b64_tr_b16 v[74:75], v231 offset:8192\n"
        "v_mfma_f32_16x16x32_bf16 a[8:11], v[40:43], v[0:3], a[8:11]\n"
        "ds_read_b64_tr_b16 v[76:77], v232 offset:8192\n"
        "s_and_b32 s53, s40, 1\n"
        "s_lshl_b32 s53, s53, 10\n"
        "v_mfma_f32_16x16x32_bf16 a[12:15], v[44:47], v[0:3], a[12:15]\n"
        "ds_read_b64_tr_b16 v[78:79], v233 offset:8192\n"
        "s_sub_u32 s53, 0x8000, s53\n"
        "v_cvt_pk_bf16_f32 v194, 0, v194\n"
        "v_mfma_f32_16x16x32_bf16 a[16:19], v[0:3], v[48:51], a[16:19]\n"
        "global_load_lds_dwordx4 v241, s[68:69] offset:3072\n"
        "s_add_u32 m0, s72, 0x0\n"
        "v_mfma_f32_16x16x32_bf16 a[20:23], v[0:3], v[52:55], a[20:23]\n"
        "s_add_u32 s68, s68, 0x80000\n"
        "s_addc_u32 s69, s69, 0\n"
        "v_cvt_pk_bf16_f32 v196, 0, v196\n"
        "v_mfma_f32_16x16x32_bf16 a[24:27], v[0:3], v[56:59], a[24:27]\n"
        "ds_read_b64_tr_b16 v[80:81], v226 offset:24576\n"
        "v_and_b32 v183, 0xffff0000, v115\n"
        "v_mfma_f32_16x16x32_bf16 a[28:31], v[0:3], v[60:63], a[28:31]\n"
        "ds_read_b128 v[0:3], v225 offset:0\n"
        "v_cvt_pk_bf16_f32 v192, 0, v192\n"
        "v_mfma_f32_16x16x32_bf16 a[32:35], v[32:35], v[4:7], a[32:35]\n"
        "global_load_dword v253, v173, s[82:83] offset:0\n"
        "v_mfma_f32_16x16x32_bf16 a[36:39], v[36:39], v[4:7], a[36:39]\n"
        "ds_read_b64_tr_b16 v[82:83], v227 offset:24576\n"
        "s_add_u32 s82, s12, 0x700000\n"
        "s_addc_u32 s83, s13, 0\n"
        "v_mfma_f32_16x16x32_bf16 a[40:43], v[40:43], v[4:7], a[40:43]\n"
        "ds_read_b64_tr_b16 v[84:85], v228 offset:24576\n"
        "v_and_b32 v184, 0xffff0000, v117\n"
        "v_mfma_f32_16x16x32_bf16 a[44:47], v[44:47], v[4:7], a[44:47]\n"
        "ds_read_b64_tr_b16 v[86:87], v229 offset:24576\n"
        "v_mul_f32 v190, v179, v187\n"
        "v_mfma_f32_16x16x32_bf16 a[48:51], v[4:7], v[48:51], a[48:51]\n"
        "ds_read_b64_tr_b16 v[88:89], v230 offset:24576\n"
        "v_cvt_pk_bf16_f32 v190, 0, v190\n"
        "v_mfma_f32_16x16x32_bf16 a[52:55], v[4:7], v[52:55], a[52:55]\n"
        "ds_read_b64_tr_b16 v[90:91], v231 offset:24576\n"
        "v_mfma_f32_16x16x32_bf16 a[56:59], v[4:7], v[56:59], a[56:59]\n"
        "ds_read_b64_tr_b16 v[92:93], v232 offset:24576\n"
        "v_mfma_f32_16x16x32_bf16 a[60:63], v[4:7], v[60:63], a[60:63]\n"
        "ds_read_b128 v[4:7], v225 offset:2048\n"
        "v_mfma_f32_16x16x32_bf16 a[64:67], v[32:35], v[8:11], a[64:67]\n"
        "ds_read_b64_tr_b16 v[94:95], v233 offset:24576\n"
        "v_mfma_f32_16x16x32_bf16 a[68:71], v[36:39], v[8:11], a[68:71]\n"
        "global_store_dwordx4 v171, v[208:211], s[76:77] offset:0 sc1\n"
        "s_add_u32 s76, s76, 0x2000\n"
        "s_addc_u32 s77, s77, 0\n"
        "v_mfma_f32_16x16x32_bf16 a[72:75], v[40:43], v[8:11], a[72:75]\n"
        "v_cvt_pk_bf16_f32 v210, v195, v193\n"
        "v_cvt_pk_bf16_f32 v211, v191, v189\n"
        "v_mfma_f32_16x16x32_bf16 a[76:79], v[44:47], v[8:11], a[76:79]\n"
        "v_mul_f32 v193, v180, v188\n"
        "v_mul_f32 v195, v181, v188\n"
        "v_mfma_f32_16x16x32_bf16 a[80:83], v[8:11], v[48:51], a[80:83]\n"
        "v_mul_f32 v191, v179, v188\n"
        "v_cvt_pk_bf16_f32 v193, 0, v193\n"
        "v_mfma_f32_16x16x32_bf16 a[84:87], v[8:11], v[52:55], a[84:87]\n"
        "v_cvt_pk_bf16_f32 v195, 0, v195\n"
        "v_cvt_pk_bf16_f32 v191, 0, v191\n"
        "v_mfma_f32_16x16x32_bf16 a[88:91], v[8:11], v[56:59], a[88:91]\n"
        "v_add_f32 v193, v193, v194\n"
        "v_add_f32 v195, v195, v196\n"
        "v_mfma_f32_16x16x32_bf16 a[92:95], v[8:11], v[60:63], a[92:95]\n"
        "ds_read_b128 v[8:11], v225 offset:4096\n"
        "v_add_f32 v191, v191, v192\n"
        "v_mfma_f32_16x16x32_bf16 a[96:99], v[32:35], v[12:15], a[96:99]\n"
        "v_mul_f32 v194, v182, v186\n"
        "v_mul_f32 v196, v183, v186\n"
        "v_mfma_f32_16x16x32_bf16 a[100:103], v[36:39], v[12:15], a[100:103]\n"
        "v_cvt_pk_bf16_f32 v193, 0, v193\n"
        "v_cvt_pk_bf16_f32 v195, 0, v195\n"
        "v_mfma_f32_16x16x32_bf16 a[104:107], v[40:43], v[12:15], a[104:107]\n"
        "v_mul_f32 v192, v181, v186\n"
        "v_cvt_pk_bf16_f32 v194, 0, v194\n"
        "v_mfma_f32_16x16x32_bf16 a[108:111], v[44:47], v[12:15], a[108:111]\n"
        "v_cvt_pk_bf16_f32 v196, 0, v196\n"
        "v_cvt_pk_bf16_f32 v191, 0, v191\n"
        "v_mfma_f32_16x16x32_bf16 a[112:115], v[12:15], v[48:51], a[112:115]\n"
        "v_cvt_pk_bf16_f32 v192, 0, v192\n"
        "v_add_f32 v193, v193, v194\n"
        "v_mfma_f32_16x16x32_bf16 a[116:119], v[12:15], v[52:55], a[116:119]\n"
        "v_add_f32 v195, v195, v196\n"
        "v_add_f32 v191, v191, v192\n"
        "v_mfma_f32_16x16x32_bf16 a[120:123], v[12:15], v[56:59], a[120:123]\n"
        "v_mul_f32 v194, v183, v185\n"
        "v_mul_f32 v196, v184, v185\n"
        "v_mfma_f32_16x16x32_bf16 a[124:127], v[12:15], v[60:63], a[124:127]\n"
        "ds_read_b128 v[12:15], v225 offset:6144\n"
        "v_cvt_pk_bf16_f32 v193, 0, v193\n"
        "v_mfma_f32_16x16x32_bf16 a[128:131], v[32:35], v[16:19], a[128:131]\n"
        "v_cvt_pk_bf16_f32 v195, 0, v195\n"
        "v_mul_f32 v192, v182, v185\n"
        "v_mfma_f32_16x16x32_bf16 a[132:135], v[36:39], v[16:19], a[132:135]\n"
        "v_cvt_pk_bf16_f32 v194, 0, v194\n"
        "v_cvt_pk_bf16_f32 v196, 0, v196\n"
        "v_mfma_f32_16x16x32_bf16 a[136:139], v[40:43], v[16:19], a[136:139]\n"
        "v_cvt_pk_bf16_f32 v191, 0, v191\n"
        "v_cvt_pk_bf16_f32 v192, 0, v192\n"
        "v_mfma_f32_16x16x32_bf16 a[140:143], v[44:47], v[16:19], a[140:143]\n"
        "v_add_f32 v193, v193, v194\n"
        "v_add_f32 v195, v195, v196\n"
        "v_mfma_f32_16x16x32_bf16 a[144:147], v[16:19], v[48:51], a[144:147]\n"
        "v_add_f32 v191, v191, v192\n"
        "v_cvt_pk_bf16_f32 v193, 0, v193\n"
        "v_mfma_f32_16x16x32_bf16 a[148:151], v[16:19], v[52:55], a[148:151]\n"
        "v_and_b32 v194, 0xffff0000, v131\n"
        "v_cvt_pk_bf16_f32 v195, 0, v195\n"
        "v_mfma_f32_16x16x32_bf16 a[152:155], v[16:19], v[56:59], a[152:155]\n"
        "v_and_b32 v196, 0xffff0000, v133\n"
        "v_cvt_pk_bf16_f32 v191, 0, v191\n"
        "v_mfma_f32_16x16x32_bf16 a[156:159], v[16:19], v[60:63], a[156:159]\n"
        "ds_read_b128 v[16:19], v225 offset:8192\n"
        "v_and_b32 v192, 0xffff0000, v129\n"
        "v_mfma_f32_16x16x32_bf16 a[160:163], v[32:35], v[20:23], a[160:163]\n"
        "v_mul_f32 v193, v193, v194\n"
        "v_mul_f32 v195, v195, v196\n"
        "v_mfma_f32_16x16x32_bf16 a[164:167], v[36:39], v[20:23], a[164:167]\n"
        "v_mul_f32 v191, v191, v192\n"
        "v_cvt_pk_bf16_f32 v208, v195, v193\n"
        "v_mfma_f32_16x16x32_bf16 a[168:171], v[40:43], v[20:23], a[168:171]\n"
        "global_store_dwordx4 v171, v[200:203], s[76:77] offset:-4096 sc1\n"
        "v_mul_f32 v189, v178, v188\n"
        "v_cvt_pk_bf16_f32 v189, 0, v189\n"
        "v_add_f32 v189, v189, v190\n"
        "v_cvt_pk_bf16_f32 v189, 0, v189\n"
        "v_mfma_f32_16x16x32_bf16 a[172:175], v[44:47], v[20:23], a[172:175]\n"
        "v_mul_f32 v190, v180, v186\n"
        "v_cvt_pk_bf16_f32 v190, 0, v190\n"
        "v_mfma_f32_16x16x32_bf16 a[176:179], v[20:23], v[48:51], a[176:179]\n"
        "v_add_f32 v189, v189, v190\n"
        "v_cvt_pk_bf16_f32 v189, 0, v189\n"
        "v_mfma_f32_16x16x32_bf16 a[180:183], v[20:23], v[52:55], a[180:183]\n"
        "v_mul_f32 v190, v181, v185\n"
        "v_cvt_pk_bf16_f32 v190, 0, v190\n"
        "v_mfma_f32_16x16x32_bf16 a[184:187], v[20:23], v[56:59], a[184:187]\n"
        "v_add_f32 v189, v189, v190\n"
        "v_cvt_pk_bf16_f32 v189, 0, v189\n"
        "v_mfma_f32_16x16x32_bf16 a[188:191], v[20:23], v[60:63], a[188:191]\n"
        "ds_read_b128 v[20:23], v225 offset:10240\n"
        "v_and_b32 v190, 0xffff0000, v127\n"
        "v_mfma_f32_16x16x32_bf16 a[192:195], v[32:35], v[24:27], a[192:195]\n"
        "v_mul_f32 v189, v189, v190\n"
        "v_cvt_pk_bf16_f32 v209, v191, v189\n"
        "v_mfma_f32_16x16x32_bf16 a[196:199], v[36:39], v[24:27], a[196:199]\n"
        "global_store_dwordx4 v171, v[208:211], s[76:77] offset:0 sc1\n"
        "v_mfma_f32_16x16x32_bf16 a[200:203], v[40:43], v[24:27], a[200:203]\n"
        "s_waitcnt lgkmcnt(1)\n"
        "v_mfma_f32_16x16x32_bf16 a[204:207], v[44:47], v[24:27], a[204:207]\n"
        "v_mfma_f32_16x16x32_bf16 a[208:211], v[24:27], v[48:51], a[208:211]\n"
        "v_mfma_f32_16x16x32_bf16 a[212:215], v[24:27], v[52:55], a[212:215]\n"
        "v_mfma_f32_16x16x32_bf16 a[216:219], v[24:27], v[56:59], a[216:219]\n"
        "v_mfma_f32_16x16x32_bf16 a[220:223], v[24:27], v[60:63], a[220:223]\n"
        "ds_read_b128 v[24:27], v225 offset:12288\n"
        "s_mov_b32 s88, 0x3e0293ee\n"
        "s_lshl_b32 s51, s43, 19\n"
        "v_mfma_f32_16x16x32_bf16 a[224:227], v[32:35], v[28:31], a[224:227]\n"
        "s_cmp_ge_u32 s40, 2\n"
        "s_cselect_b32 s50, s45, s44\n"
        "s_lshl_b32 s50, s50, 15\n"
        "v_mfma_f32_16x16x32_bf16 a[228:231], v[36:39], v[28:31], a[228:231]\n"
        "s_add_u32 s50, s50, s51\n"
        "s_and_b32 s51, s40, 1\n"
        "s_lshl_b32 s51, s51, 11\n"
        "v_mfma_f32_16x16x32_bf16 a[232:235], v[40:43], v[28:31], a[232:235]\n"
        "s_add_u32 s52, s50, s51\n"
        "v_mfma_f32_16x16x32_bf16 a[236:239], v[44:47], v[28:31], a[236:239]\n"
        "v_mfma_f32_16x16x32_bf16 a[240:243], v[28:31], v[48:51], a[240:243]\n"
        "v_mfma_f32_16x16x32_bf16 a[244:247], v[28:31], v[52:55], a[244:247]\n"
        "v_mfma_f32_16x16x32_bf16 a[248:251], v[28:31], v[56:59], a[248:251]\n"
        "v_mfma_f32_16x16x32_bf16 a[252:255], v[28:31], v[60:63], a[252:255]\n"
        "ds_read_b128 v[28:31], v225 offset:14336\n"
        "v_mfma_f32_16x16x32_bf16 a[0:3], v[64:67], v[0:3], a[0:3]\n"
        "v_mfma_f32_16x16x32_bf16 a[4:7], v[68:71], v[0:3], a[4:7]\n"
        "v_mfma_f32_16x16x32_bf16 a[8:11], v[72:75], v[0:3], a[8:11]\n"
        "v_mfma_f32_16x16x32_bf16 a[12:15], v[76:79], v[0:3], a[12:15]\n"
        "v_mfma_f32_16x16x32_bf16 a[16:19], v[0:3], v[80:83], a[16:19]\n"
        "v_mfma_f32_16x16x32_bf16 a[20:23], v[0:3], v[84:87], a[20:23]\n"
        "v_mfma_f32_16x16x32_bf16 a[24:27], v[0:3], v[88:91], a[24:27]\n"
        "v_mfma_f32_16x16x32_bf16 a[28:31], v[0:3], v[92:95], a[28:31]\n"
        "s_waitcnt vmcnt(12) lgkmcnt(0)\n"
        "s_barrier\n"
        "v_mfma_f32_16x16x32_bf16 a[32:35], v[64:67], v[4:7], a[32:35]\n"
        "global_load_lds_dwordx4 v242, s[64:65] offset:0\n"
        "ds_read_b128 v[0:3], v224 offset:32768\n"
        "v_mfma_f32_16x16x32_bf16 a[36:39], v[68:71], v[4:7], a[36:39]\n"
        "ds_read_b64_tr_b16 v[32:33], v226 offset:32768\n"
        "v_mfma_f32_16x16x32_bf16 a[40:43], v[72:75], v[4:7], a[40:43]\n"
        "ds_read_b64_tr_b16 v[34:35], v227 offset:32768\n"
        "v_mfma_f32_16x16x32_bf16 a[44:47], v[76:79], v[4:7], a[44:47]\n"
        "ds_read_b64_tr_b16 v[36:37], v228 offset:32768\n"
        "v_mfma_f32_16x16x32_bf16 a[48:51], v[4:7], v[80:83], a[48:51]\n"
        "ds_read_b64_tr_b16 v[38:39], v229 offset:32768\n"
        "global_load_lds_dwordx4 v243, s[64:65] offset:1024\n"
        "v_mfma_f32_16x16x32_bf16 a[52:55], v[4:7], v[84:87], a[52:55]\n"
        "ds_read_b64_tr_b16 v[40:41], v230 offset:32768\n"
        "v_mfma_f32_16x16x32_bf16 a[56:59], v[4:7], v[88:91], a[56:59]\n"
        "ds_read_b64_tr_b16 v[42:43], v231 offset:32768\n"
        "v_mfma_f32_16x16x32_bf16 a[60:63], v[4:7], v[92:95], a[60:63]\n"
        "ds_read_b128 v[4:7], v224 offset:34816\n"
        "v_mfma_f32_16x16x32_bf16 a[64:67], v[64:67], v[8:11], a[64:67]\n"
        "ds_read_b64_tr_b16 v[44:45], v232 offset:32768\n"
        "global_load_lds_dwordx4 v244, s[64:65] offset:2048\n"
        "v_mfma_f32_16x16x32_bf16 a[68:71], v[68:71], v[8:11], a[68:71]\n"
        "ds_read_b64_tr_b16 v[46:47], v233 offset:32768\n"
        "v_mfma_f32_16x16x32_bf16 a[72:75], v[72:75], v[8:11], a[72:75]\n"
        "ds_read_b64_tr_b16 v[48:49], v226 offset:49152\n"
        "v_mfma_f32_16x16x32_bf16 a[76:79], v[76:79], v[8:11], a[76:79]\n"
        "ds_read_b64_tr_b16 v[50:51], v227 offset:49152\n"
        "v_mfma_f32_16x16x32_bf16 a[80:83], v[8:11], v[80:83], a[80:83]\n"
        "global_load_lds_dwordx4 v245, s[64:65] offset:3072\n"
        "s_add_u32 m0, s72, 0x1000\n"
        "v_mfma_f32_16x16x32_bf16 a[84:87], v[8:11], v[84:87], a[84:87]\n"
        "ds_read_b64_tr_b16 v[52:53], v228 offset:49152\n"
        "s_add_u32 s64, s64, 0x80\n"
        "s_addc_u32 s65, s65, 0\n"
        "v_mfma_f32_16x16x32_bf16 a[88:91], v[8:11], v[88:91], a[88:91]\n"
        "ds_read_b64_tr_b16 v[54:55], v229 offset:49152\n"
        "v_mfma_f32_16x16x32_bf16 a[92:95], v[8:11], v[92:95], a[92:95]\n"
        "ds_read_b128 v[8:11], v224 offset:36864\n"
        "v_mfma_f32_16x16x32_bf16 a[96:99], v[64:67], v[12:15], a[96:99]\n"
        "global_load_lds_dwordx4 v242, s[66:67] offset:0\n"
        "s_add_u32 s97, s52, 0x7000\n"
        "v_mfma_f32_16x16x32_bf16 a[100:103], v[68:71], v[12:15], a[100:103]\n"
        "ds_read_b64_tr_b16 v[56:57], v230 offset:49152\n"
        "s_add_u32 s70, s38, s97\n"
        "s_addc_u32 s71, s39, 0\n"
        "v_mfma_f32_16x16x32_bf16 a[104:107], v[72:75], v[12:15], a[104:107]\n"
        "ds_read_b64_tr_b16 v[58:59], v231 offset:49152\n"
        "v_mfma_f32_16x16x32_bf16 a[108:111], v[76:79], v[12:15], a[108:111]\n"
        "ds_read_b64_tr_b16 v[60:61], v232 offset:49152\n"
        "v_mfma_f32_16x16x32_bf16 a[112:115], v[12:15], v[80:83], a[112:115]\n"
        "global_load_lds_dwordx4 v243, s[66:67] offset:1024\n"
        "v_mfma_f32_16x16x32_bf16 a[116:119], v[12:15], v[84:87], a[116:119]\n"
        "ds_read_b64_tr_b16 v[62:63], v233 offset:49152\n"
        "v_mfma_f32_16x16x32_bf16 a[120:123], v[12:15], v[88:91], a[120:123]\n"
        "v_mfma_f32_16x16x32_bf16 a[124:127], v[12:15], v[92:95], a[124:127]\n"
        "ds_read_b128 v[12:15], v224 offset:38912\n"
        "v_mfma_f32_16x16x32_bf16 a[128:131], v[64:67], v[16:19], a[128:131]\n"
        "global_load_dword v253, v173, s[82:83] offset:0\n"
        "v_mfma_f32_16x16x32_bf16 a[132:135], v[68:71], v[16:19], a[132:135]\n"
        "global_load_lds_dwordx4 v244, s[66:67] offset:2048\n"
        "v_mfma_f32_16x16x32_bf16 a[136:139], v[72:75], v[16:19], a[136:139]\n"
        "v_mfma_f32_16x16x32_bf16 a[140:143], v[76:79], v[16:19], a[140:143]\n"
        "v_mfma_f32_16x16x32_bf16 a[144:147], v[16:19], v[80:83], a[144:147]\n"
        "v_mfma_f32_16x16x32_bf16 a[148:151], v[16:19], v[84:87], a[148:151]\n"
        "global_load_lds_dwordx4 v245, s[66:67] offset:3072\n"
        "s_add_u32 s66, s66, 0x80\n"
        "s_addc_u32 s67, s67, 0\n"
        "v_mfma_f32_16x16x32_bf16 a[152:155], v[16:19], v[88:91], a[152:155]\n"
        "s_add_u32 m0, s72, 0x10000\n"
        "v_mfma_f32_16x16x32_bf16 a[156:159], v[16:19], v[92:95], a[156:159]\n"
        "ds_read_b128 v[16:19], v224 offset:40960\n"
        "v_mfma_f32_16x16x32_bf16 a[160:163], v[64:67], v[20:23], a[160:163]\n"
        "v_mfma_f32_16x16x32_bf16 a[164:167], v[68:71], v[20:23], a[164:167]\n"
        "global_load_lds_dwordx4 v242, s[64:65] offset:0\n"
        "v_mfma_f32_16x16x32_bf16 a[168:171], v[72:75], v[20:23], a[168:171]\n"
        "v_mfma_f32_16x16x32_bf16 a[172:175], v[76:79], v[20:23], a[172:175]\n"
        "v_mfma_f32_16x16x32_bf16 a[176:179], v[20:23], v[80:83], a[176:179]\n"
        "v_mfma_f32_16x16x32_bf16 a[180:183], v[20:23], v[84:87], a[180:183]\n"
        "global_load_lds_dwordx4 v243, s[64:65] offset:1024\n"
        "v_mfma_f32_16x16x32_bf16 a[184:187], v[20:23], v[88:91], a[184:187]\n"
        "v_mfma_f32_16x16x32_bf16 a[188:191], v[20:23], v[92:95], a[188:191]\n"
        "ds_read_b128 v[20:23], v224 offset:43008\n"
        "v_mfma_f32_16x16x32_bf16 a[192:195], v[64:67], v[24:27], a[192:195]\n"
        "v_mfma_f32_16x16x32_bf16 a[196:199], v[68:71], v[24:27], a[196:199]\n"
        "global_load_lds_dwordx4 v244, s[64:65] offset:2048\n"
        "v_mfma_f32_16x16x32_bf16 a[200:203], v[72:75], v[24:27], a[200:203]\n"
        "v_mfma_f32_16x16x32_bf16 a[204:207], v[76:79], v[24:27], a[204:207]\n"
        "v_mfma_f32_16x16x32_bf16 a[208:211], v[24:27], v[80:83], a[208:211]\n"
        "v_mfma_f32_16x16x32_bf16 a[212:215], v[24:27], v[84:87], a[212:215]\n"
        "global_load_lds_dwordx4 v245, s[64:65] offset:3072\n"
        "s_add_u32 m0, s72, 0x11000\n"
        "v_mfma_f32_16x16x32_bf16 a[216:219], v[24:27], v[88:91], a[216:219]\n"
        "s_add_u32 s64, s64, 0x80\n"
        "s_addc_u32 s65, s65, 0\n"
        "v_mfma_f32_16x16x32_bf16 a[220:223], v[24:27], v[92:95], a[220:223]\n"
        "ds_read_b128 v[24:27], v224 offset:45056\n"
        "v_mfma_f32_16x16x32_bf16 a[224:227], v[64:67], v[28:31], a[224:227]\n"
        "ds_read_b64_tr_b16 v[64:65], v226 offset:40960\n"
        "v_mfma_f32_16x16x32_bf16 a[228:231], v[68:71], v[28:31], a[228:231]\n"
        "global_load_lds_dwordx4 v242, s[66:67] offset:0\n"
        "ds_read_b64_tr_b16 v[66:67], v227 offset:40960\n"
        "v_mfma_f32_16x16x32_bf16 a[232:235], v[72:75], v[28:31], a[232:235]\n"
        "ds_read_b64_tr_b16 v[68:69], v228 offset:40960\n"
        "s_waitcnt lgkmcnt(4)\n"
        "v_mfma_f32_16x16x32_bf16 a[236:239], v[76:79], v[28:31], a[236:239]\n"
        "ds_read_b64_tr_b16 v[70:71], v229 offset:40960\n"
        "v_mfma_f32_16x16x32_bf16 a[240:243], v[28:31], v[80:83], a[240:243]\n"
        "ds_read_b64_tr_b16 v[72:73], v230 offset:40960\n"
        "v_mfma_f32_16x16x32_bf16 a[244:247], v[28:31], v[84:87], a[244:247]\n"
        "global_load_lds_dwordx4 v243, s[66:67] offset:1024\n"
        "ds_read_b64_tr_b16 v[74:75], v231 offset:40960\n"
        "v_mfma_f32_16x16x32_bf16 a[248:251], v[28:31], v[88:91], a[248:251]\n"
        "ds_read_b64_tr_b16 v[76:77], v232 offset:40960\n"
        "v_mfma_f32_16x16x32_bf16 a[252:255], v[28:31], v[92:95], a[252:255]\n"
        "ds_read_b128 v[28:31], v224 offset:47104\n"
        "v_mfma_f32_16x16x32_bf16 a[0:3], v[32:35], v[0:3], a[0:3]\n"
        "ds_read_b64_tr_b16 v[78:79], v233 offset:40960\n"
        "v_mfma_f32_16x16x32_bf16 a[4:7], v[36:39], v[0:3], a[4:7]\n"
        "global_load_lds_dwordx4 v244, s[66:67] offset:2048\n"
        "ds_read_b64_tr_b16 v[80:81], v226 offset:57344\n"
        "v_mfma_f32_16x16x32_bf16 a[8:11], v[40:43], v[0:3], a[8:11]\n"
        "ds_read_b64_tr_b16 v[82:83], v227 offset:57344\n"
        "v_add_u32 v226, s53, v226\n"
        "v_mfma_f32_16x16x32_bf16 a[12:15], v[44:47], v[0:3], a[12:15]\n"
        "ds_read_b64_tr_b16 v[84:85], v228 offset:57344\n"
        "v_add_u32 v227, s53, v227\n"
        "v_mfma_f32_16x16x32_bf16 a[16:19], v[0:3], v[48:51], a[16:19]\n"
        "ds_read_b64_tr_b16 v[86:87], v229 offset:57344\n"
        "v_add_u32 v228, s53, v228\n"
        "v_mfma_f32_16x16x32_bf16 a[20:23], v[0:3], v[52:55], a[20:23]\n"
        "global_load_lds_dwordx4 v245, s[66:67] offset:3072\n"
        "s_add_u32 s66, s66, 0x80\n"
        "s_addc_u32 s67, s67, 0\n"
        "v_mfma_f32_16x16x32_bf16 a[24:27], v[0:3], v[56:59], a[24:27]\n"
        "ds_read_b64_tr_b16 v[88:89], v230 offset:57344\n"
        "s_add_u32 m0, s73, 0x1c000\n"
        "v_add_u32 v229, s53, v229\n"
        "v_mfma_f32_16x16x32_bf16 a[28:31], v[0:3], v[60:63], a[28:31]\n"
        "ds_read_b128 v[0:3], v225 offset:32768\n"
        "v_add_u32 v230, s53, v230\n"
        "v_mfma_f32_16x16x32_bf16 a[32:35], v[32:35], v[4:7], a[32:35]\n"
        "ds_read_b64_tr_b16 v[90:91], v231 offset:57344\n"
        "v_add_u32 v231, s53, v231\n"
        "v_mfma_f32_16x16x32_bf16 a[36:39], v[36:39], v[4:7], a[36:39]\n"
        "ds_read_b64_tr_b16 v[92:93], v232 offset:57344\n"
        "v_add_u32 v232, s53, v232\n"
        "v_mfma_f32_16x16x32_bf16 a[40:43], v[40:43], v[4:7], a[40:43]\n"
        "ds_read_b64_tr_b16 v[94:95], v233 offset:57344\n"
        "v_add_u32 v233, s53, v233\n"
        "v_mfma_f32_16x16x32_bf16 a[44:47], v[44:47], v[4:7], a[44:47]\n"
        "s_add_u32 s53, s52, s51\n"
        "s_add_u32 s53, s53, 0x1000000\n"
        "v_mfma_f32_16x16x32_bf16 a[48:51], v[4:7], v[48:51], a[48:51]\n"
        "v_mfma_f32_16x16x32_bf16 a[52:55], v[4:7], v[52:55], a[52:55]\n"
        "v_mfma_f32_16x16x32_bf16 a[56:59], v[4:7], v[56:59], a[56:59]\n"
        "v_mfma_f32_16x16x32_bf16 a[60:63], v[4:7], v[60:63], a[60:63]\n"
        "ds_read_b128 v[4:7], v225 offset:34816\n"
        "v_mfma_f32_16x16x32_bf16 a[64:67], v[32:35], v[8:11], a[64:67]\n"
        "v_mfma_f32_16x16x32_bf16 a[68:71], v[36:39], v[8:11], a[68:71]\n"
        "v_mfma_f32_16x16x32_bf16 a[72:75], v[40:43], v[8:11], a[72:75]\n"
        "v_mfma_f32_16x16x32_bf16 a[76:79], v[44:47], v[8:11], a[76:79]\n"
        "v_mfma_f32_16x16x32_bf16 a[80:83], v[8:11], v[48:51], a[80:83]\n"
        "v_mfma_f32_16x16x32_bf16 a[84:87], v[8:11], v[52:55], a[84:87]\n"
        "v_mfma_f32_16x16x32_bf16 a[88:91], v[8:11], v[56:59], a[88:91]\n"
        "v_mfma_f32_16x16x32_bf16 a[92:95], v[8:11], v[60:63], a[92:95]\n"
        "ds_read_b128 v[8:11], v225 offset:36864\n"
        "v_mfma_f32_16x16x32_bf16 a[96:99], v[32:35], v[12:15], a[96:99]\n"
        "v_mfma_f32_16x16x32_bf16 a[100:103], v[36:39], v[12:15], a[100:103]\n"
        "v_mfma_f32_16x16x32_bf16 a[104:107], v[40:43], v[12:15], a[104:107]\n"
        "v_mfma_f32_16x16x32_bf16 a[108:111], v[44:47], v[12:15], a[108:111]\n"
        "s_add_u32 s97, s52, 0x6000\n"
        "v_mfma_f32_16x16x32_bf16 a[112:115], v[12:15], v[48:51], a[112:115]\n"
        "v_mfma_f32_16x16x32_bf16 a[116:119], v[12:15], v[52:55], a[116:119]\n"
        "v_mfma_f32_16x16x32_bf16 a[120:123], v[12:15], v[56:59], a[120:123]\n"
        "v_mfma_f32_16x16x32_bf16 a[124:127], v[12:15], v[60:63], a[124:127]\n"
        "ds_read_b128 v[12:15], v225 offset:38912\n"
        "v_mfma_f32_16x16x32_bf16 a[128:131], v[32:35], v[16:19], a[128:131]\n"
        "v_mfma_f32_16x16x32_bf16 a[132:135], v[36:39], v[16:19], a[132:135]\n"
        "v_mfma_f32_16x16x32_bf16 a[136:139], v[40:43], v[16:19], a[136:139]\n"
        "v_mfma_f32_16x16x32_bf16 a[140:143], v[44:47], v[16:19], a[140:143]\n"
        "v_mfma_f32_16x16x32_bf16 a[144:147], v[16:19], v[48:51], a[144:147]\n"
        "v_mfma_f32_16x16x32_bf16 a[148:151], v[16:19], v[52:55], a[148:151]\n"
        "v_mfma_f32_16x16x32_bf16 a[152:155], v[16:19], v[56:59], a[152:155]\n"
        "v_mfma_f32_16x16x32_bf16 a[156:159], v[16:19], v[60:63], a[156:159]\n"
        "ds_read_b128 v[16:19], v225 offset:40960\n"
        "v_mfma_f32_16x16x32_bf16 a[160:163], v[32:35], v[20:23], a[160:163]\n"
        "v_mfma_f32_16x16x32_bf16 a[164:167], v[36:39], v[20:23], a[164:167]\n"
        "v_mfma_f32_16x16x32_bf16 a[168:171], v[40:43], v[20:23], a[168:171]\n"
        "s_waitcnt lgkmcnt(1)\n"
        "v_mfma_f32_16x16x32_bf16 a[172:175], v[44:47], v[20:23], a[172:175]\n"
        "v_mfma_f32_16x16x32_bf16 a[176:179], v[20:23], v[48:51], a[176:179]\n"
        "v_mfma_f32_16x16x32_bf16 a[180:183], v[20:23], v[52:55], a[180:183]\n"
        "v_mfma_f32_16x16x32_bf16 a[184:187], v[20:23], v[56:59], a[184:187]\n"
        "v_mfma_f32_16x16x32_bf16 a[188:191], v[20:23], v[60:63], a[188:191]\n"
        "ds_read_b128 v[20:23], v225 offset:43008\n"
        "v_mfma_f32_16x16x32_bf16 a[192:195], v[32:35], v[24:27], a[192:195]\n"
        "v_mfma_f32_16x16x32_bf16 a[196:199], v[36:39], v[24:27], a[196:199]\n"
        "v_mfma_f32_16x16x32_bf16 a[200:203], v[40:43], v[24:27], a[200:203]\n"
        "v_mfma_f32_16x16x32_bf16 a[204:207], v[44:47], v[24:27], a[204:207]\n"
        "v_mfma_f32_16x16x32_bf16 a[208:211], v[24:27], v[48:51], a[208:211]\n"
        "v_mfma_f32_16x16x32_bf16 a[212:215], v[24:27], v[52:55], a[212:215]\n"
        "v_mfma_f32_16x16x32_bf16 a[216:219], v[24:27], v[56:59], a[216:219]\n"
        "v_mfma_f32_16x16x32_bf16 a[220:223], v[24:27], v[60:63], a[220:223]\n"
        "ds_read_b128 v[24:27], v225 offset:45056\n"
        "v_mfma_f32_16x16x32_bf16 a[224:227], v[32:35], v[28:31], a[224:227]\n"
        "v_mfma_f32_16x16x32_bf16 a[228:231], v[36:39], v[28:31], a[228:231]\n"
        "v_mfma_f32_16x16x32_bf16 a[232:235], v[40:43], v[28:31], a[232:235]\n"
        "v_mfma_f32_16x16x32_bf16 a[236:239], v[44:47], v[28:31], a[236:239]\n"
        "v_mfma_f32_16x16x32_bf16 a[240:243], v[28:31], v[48:51], a[240:243]\n"
        "v_mfma_f32_16x16x32_bf16 a[244:247], v[28:31], v[52:55], a[244:247]\n"
        "v_mfma_f32_16x16x32_bf16 a[248:251], v[28:31], v[56:59], a[248:251]\n"
        "v_mfma_f32_16x16x32_bf16 a[252:255], v[28:31], v[60:63], a[252:255]\n"
        "ds_read_b128 v[28:31], v225 offset:47104\n"
        "v_mfma_f32_16x16x32_bf16 a[0:3], v[64:67], v[0:3], a[0:3]\n"
        "v_mfma_f32_16x16x32_bf16 a[4:7], v[68:71], v[0:3], a[4:7]\n"
        "v_mfma_f32_16x16x32_bf16 a[8:11], v[72:75], v[0:3], a[8:11]\n"
        "v_mfma_f32_16x16x32_bf16 a[12:15], v[76:79], v[0:3], a[12:15]\n"
        "v_mfma_f32_16x16x32_bf16 a[16:19], v[0:3], v[80:83], a[16:19]\n"
        "v_mfma_f32_16x16x32_bf16 a[20:23], v[0:3], v[84:87], a[20:23]\n"
        "v_mfma_f32_16x16x32_bf16 a[24:27], v[0:3], v[88:91], a[24:27]\n"
        "v_mfma_f32_16x16x32_bf16 a[28:31], v[0:3], v[92:95], a[28:31]\n"
        "s_waitcnt vmcnt(8) lgkmcnt(0)\n"
        "s_barrier\n"
        "v_mfma_f32_16x16x32_bf16 a[32:35], v[64:67], v[4:7], a[32:35]\n"
        "global_load_lds_dwordx4 v238, s[68:69] offset:0\n"
        "ds_read_b128 v[192:195], v246 offset:2048\n"
        "v_mfma_f32_16x16x32_bf16 a[36:39], v[68:71], v[4:7], a[36:39]\n"
        "ds_read_b64_tr_b16 v[32:33], v226 offset:32768\n"
        "v_mfma_f32_16x16x32_bf16 a[40:43], v[72:75], v[4:7], a[40:43]\n"
        "ds_read_b64_tr_b16 v[34:35], v227 offset:32768\n"
        "v_mfma_f32_16x16x32_bf16 a[44:47], v[76:79], v[4:7], a[44:47]\n"
        "ds_read_b64_tr_b16 v[36:37], v228 offset:32768\n"
        "v_mfma_f32_16x16x32_bf16 a[48:51], v[4:7], v[80:83], a[48:51]\n"
        "ds_read_b64_tr_b16 v[38:39], v229 offset:32768\n"
        "global_load_lds_dwordx4 v239, s[68:69] offset:1024\n"
        "v_mfma_f32_16x16x32_bf16 a[52:55], v[4:7], v[84:87], a[52:55]\n"
        "ds_read_b128 v[196:199], v246 offset:0\n"
        "v_mfma_f32_16x16x32_bf16 a[56:59], v[4:7], v[88:91], a[56:59]\n"
        "ds_read_b64_tr_b16 v[40:41], v230 offset:32768\n"
        "v_mfma_f32_16x16x32_bf16 a[60:63], v[4:7], v[92:95], a[60:63]\n"
        "ds_read_b64_tr_b16 v[42:43], v231 offset:32768\n"
        "v_mfma_f32_16x16x32_bf16 a[64:67], v[64:67], v[8:11], a[64:67]\n"
        "ds_read_b64_tr_b16 v[44:45], v232 offset:32768\n"
        "global_load_lds_dwordx4 v240, s[68:69] offset:2048\n"
        "v_mfma_f32_16x16x32_bf16 a[68:71], v[68:71], v[8:11], a[68:71]\n"
        "ds_read_b64_tr_b16 v[46:47], v233 offset:32768\n"
        "v_mfma_f32_16x16x32_bf16 a[72:75], v[72:75], v[8:11], a[72:75]\n"
        "ds_read_b128 v[200:203], v246 offset:6144\n"
        "v_mfma_f32_16x16x32_bf16 a[76:79], v[76:79], v[8:11], a[76:79]\n"
        "ds_read_b64_tr_b16 v[48:49], v226 offset:33792\n"
        "v_mfma_f32_16x16x32_bf16 a[80:83], v[8:11], v[80:83], a[80:83]\n"
        "global_load_lds_dwordx4 v241, s[68:69] offset:3072\n"
        "s_add_u32 m0, s72, 0x8000\n"
        "v_mfma_f32_16x16x32_bf16 a[84:87], v[8:11], v[84:87], a[84:87]\n"
        "ds_read_b64_tr_b16 v[50:51], v227 offset:33792\n"
        "s_add_u32 s68, s68, 0x80000\n"
        "s_addc_u32 s69, s69, 0\n"
        "v_mfma_f32_16x16x32_bf16 a[88:91], v[8:11], v[88:91], a[88:91]\n"
        "ds_read_b64_tr_b16 v[52:53], v228 offset:33792\n"
        "v_mfma_f32_16x16x32_bf16 a[92:95], v[8:11], v[92:95], a[92:95]\n"
        "ds_read_b64_tr_b16 v[54:55], v229 offset:33792\n"
        "v_mfma_f32_16x16x32_bf16 a[96:99], v[64:67], v[12:15], a[96:99]\n"
        "global_load_lds_dwordx4 v242, s[64:65] offset:0\n"
        "v_mfma_f32_16x16x32_bf16 a[100:103], v[68:71], v[12:15], a[100:103]\n"
        "ds_read_b128 v[204:207], v246 offset:4096\n"
        "v_mfma_f32_16x16x32_bf16 a[104:107], v[72:75], v[12:15], a[104:107]\n"
        "ds_read_b64_tr_b16 v[56:57], v230 offset:33792\n"
        "v_mfma_f32_16x16x32_bf16 a[108:111], v[76:79], v[12:15], a[108:111]\n"
        "ds_read_b64_tr_b16 v[58:59], v231 offset:33792\n"
        "v_mfma_f32_16x16x32_bf16 a[112:115], v[12:15], v[80:83], a[112:115]\n"
        "global_load_lds_dwordx4 v243, s[64:65] offset:1024\n"
        "v_mfma_f32_16x16x32_bf16 a[116:119], v[12:15], v[84:87], a[116:119]\n"
        "ds_read_b64_tr_b16 v[60:61], v232 offset:33792\n"
        "v_mfma_f32_16x16x32_bf16 a[120:123], v[12:15], v[88:91], a[120:123]\n"
        "ds_read_b64_tr_b16 v[62:63], v233 offset:33792\n"
        "v_mfma_f32_16x16x32_bf16 a[124:127], v[12:15], v[92:95], a[124:127]\n"
        "ds_read_b128 v[208:211], v247 offset:2048\n"
        "v_mfma_f32_16x16x32_bf16 a[128:131], v[64:67], v[16:19], a[128:131]\n"
        "global_load_lds_dwordx4 v244, s[64:65] offset:2048\n"
        "v_mfma_f32_16x16x32_bf16 a[132:135], v[68:71], v[16:19], a[132:135]\n"
        "ds_read_b128 v[212:215], v247 offset:0\n"
        "v_mfma_f32_16x16x32_bf16 a[136:139], v[72:75], v[16:19], a[136:139]\n"
        "ds_read_b128 v[216:219], v247 offset:6144\n"
        "v_mfma_f32_16x16x32_bf16 a[140:143], v[76:79], v[16:19], a[140:143]\n"
        "ds_read_b128 v[220:223], v247 offset:4096\n"
        "v_mfma_f32_16x16x32_bf16 a[144:147], v[16:19], v[80:83], a[144:147]\n"
        "global_load_lds_dwordx4 v245, s[64:65] offset:3072\n"
        "s_add_u32 m0, s72, 0x9000\n"
        "v_mfma_f32_16x16x32_bf16 a[148:151], v[16:19], v[84:87], a[148:151]\n"
        "s_add_u32 s64, s64, 0x80\n"
        "s_addc_u32 s65, s65, 0\n"
        "v_mfma_f32_16x16x32_bf16 a[152:155], v[16:19], v[88:91], a[152:155]\n"
        "v_mfma_f32_16x16x32_bf16 a[156:159], v[16:19], v[92:95], a[156:159]\n"
        "v_mfma_f32_16x16x32_bf16 a[160:163], v[64:67], v[20:23], a[160:163]\n"
        "global_load_lds_dwordx4 v242, s[66:67] offset:0\n"
        "v_mfma_f32_16x16x32_bf16 a[164:167], v[68:71], v[20:23], a[164:167]\n"
        "v_mfma_f32_16x16x32_bf16 a[168:171], v[72:75], v[20:23], a[168:171]\n"
        "v_mfma_f32_16x16x32_bf16 a[172:175], v[76:79], v[20:23], a[172:175]\n"
        "v_mfma_f32_16x16x32_bf16 a[176:179], v[20:23], v[80:83], a[176:179]\n"
        "global_load_lds_dwordx4 v243, s[66:67] offset:1024\n"
        "v_mfma_f32_16x16x32_bf16 a[180:183], v[20:23], v[84:87], a[180:183]\n"
        "v_mfma_f32_16x16x32_bf16 a[184:187], v[20:23], v[88:91], a[184:187]\n"
        "v_mfma_f32_16x16x32_bf16 a[188:191], v[20:23], v[92:95], a[188:191]\n"
        "v_mfma_f32_16x16x32_bf16 a[192:195], v[64:67], v[24:27], a[192:195]\n"
        "global_load_lds_dwordx4 v244, s[66:67] offset:2048\n"
        "v_mfma_f32_16x16x32_bf16 a[196:199], v[68:71], v[24:27], a[196:199]\n"
        "v_mfma_f32_16x16x32_bf16 a[200:203], v[72:75], v[24:27], a[200:203]\n"
        "v_mfma_f32_16x16x32_bf16 a[204:207], v[76:79], v[24:27], a[204:207]\n"
        "v_mfma_f32_16x16x32_bf16 a[208:211], v[24:27], v[80:83], a[208:211]\n"
        "global_load_lds_dwordx4 v245, s[66:67] offset:3072\n"
        "s_add_u32 m0, s73, 0x20000\n"
        "v_mfma_f32_16x16x32_bf16 a[212:215], v[24:27], v[84:87], a[212:215]\n"
        "s_add_u32 s66, s66, 0x80\n"
        "s_addc_u32 s67, s67, 0\n"
        "v_accvgpr_read_b32 v177, a193\n"
        "v_mfma_f32_16x16x32_bf16 a[216:219], v[24:27], v[88:91], a[216:219]\n"
        "v_accvgpr_read_b32 v178, a194\n"
        "v_accvgpr_read_b32 v179, a195\n"
        "v_mfma_f32_16x16x32_bf16 a[220:223], v[24:27], v[92:95], a[220:223]\n"
        "v_accvgpr_read_b32 v180, a196\n"
        "v_accvgpr_read_b32 v181, a197\n"
        "v_mfma_f32_16x16x32_bf16 a[224:227], v[64:67], v[28:31], a[224:227]\n"
        "v_accvgpr_read_b32 v182, a198\n"
        "v_accvgpr_read_b32 v183, a199\n"
        "v_mfma_f32_16x16x32_bf16 a[228:231], v[68:71], v[28:31], a[228:231]\n"
        "v_mul_f32 v177, s88, v177\n"
        "v_mul_f32 v178, s88, v178\n"
        "v_mfma_f32_16x16x32_bf16 a[232:235], v[72:75], v[28:31], a[232:235]\n"
        "s_waitcnt lgkmcnt(0)\n"
        "v_mul_f32 v179, s88, v179\n"
        "v_accvgpr_read_b32 v161, a225\n"
        "v_mfma_f32_16x16x32_bf16 a[236:239], v[76:79], v[28:31], a[236:239]\n"
        "v_accvgpr_read_b32 v162, a226\n"
        "v_accvgpr_read_b32 v163, a227\n"
        "v_mfma_f32_16x16x32_bf16 a[240:243], v[28:31], v[80:83], a[240:243]\n"
        "v_accvgpr_read_b32 v164, a228\n"
        "v_accvgpr_read_b32 v165, a229\n"
        "v_mfma_f32_16x16x32_bf16 a[244:247], v[28:31], v[84:87], a[244:247]\n"
        "v_accvgpr_read_b32 v166, a230\n"
        "v_accvgpr_read_b32 v167, a231\n"
        "v_mfma_f32_16x16x32_bf16 a[248:251], v[28:31], v[88:91], a[248:251]\n"
        "v_mul_f32 v161, s88, v161\n"
        "v_mul_f32 v162, s88, v162\n"
        "v_mfma_f32_16x16x32_bf16 a[252:255], v[28:31], v[92:95], a[252:255]\n"
        "v_mul_f32 v163, s88, v163\n"
        "v_mul_f32 v164, s88, v164\n"
        "v_mfma_f32_16x16x32_bf16 v[0:3], v[32:35], v[192:195], 0\n"
        "v_mul_f32 v165, s88, v165\n"
        "v_mul_f32 v166, s88, v166\n"
        "v_mfma_f32_16x16x32_bf16 v[64:67], v[32:35], v[196:199], 0\n"
        "v_mul_f32 v167, s88, v167\n"
        "v_accvgpr_read_b32 v169, a233\n"
        "v_mfma_f32_16x16x32_bf16 v[96:99], v[32:35], v[200:203], 0\n"
        "v_accvgpr_read_b32 v170, a234\n"
        "v_accvgpr_read_b32 v171, a235\n"
        "v_mfma_f32_16x16x32_bf16 v[128:131], v[32:35], v[204:207], 0\n"
        "ds_read_b64_tr_b16 v[32:33], v226 offset:40960\n"
        "v_accvgpr_read_b32 v172, a236\n"
        "v_mfma_f32_16x16x32_bf16 v[4:7], v[36:39], v[192:195], 0\n"
        "ds_read_b64_tr_b16 v[34:35], v227 offset:40960\n"
        "v_accvgpr_read_b32 v173, a237\n"
        "v_mfma_f32_16x16x32_bf16 v[68:71], v[36:39], v[196:199], 0\n"
        "v_accvgpr_read_b32 v174, a238\n"
        "v_accvgpr_read_b32 v175, a239\n"
        "v_mfma_f32_16x16x32_bf16 v[100:103], v[36:39], v[200:203], 0\n"
        "v_mul_f32 v169, s88, v169\n"
        "v_mul_f32 v170, s88, v170\n"
        "v_mfma_f32_16x16x32_bf16 v[132:135], v[36:39], v[204:207], 0\n"
        "ds_read_b64_tr_b16 v[36:37], v228 offset:40960\n"
        "v_accvgpr_read_b32 v160, a224\n"
        "v_mfma_f32_16x16x32_bf16 v[8:11], v[40:43], v[192:195], 0\n"
        "ds_read_b64_tr_b16 v[38:39], v229 offset:40960\n"
        "v_mul_f32 v160, s88, v160\n"
        "v_mfma_f32_16x16x32_bf16 v[72:75], v[40:43], v[196:199], 0\n"
        "v_cvt_pk_bf16_f32 v160, v160, v161\n"
        "v_cvt_pk_bf16_f32 v161, v162, v163\n"
        "v_mfma_f32_16x16x32_bf16 v[104:107], v[40:43], v[200:203], 0\n"
        "v_cvt_pk_bf16_f32 v162, v164, v165\n"
        "v_cvt_pk_bf16_f32 v163, v166, v167\n"
        "v_mfma_f32_16x16x32_bf16 v[136:139], v[40:43], v[204:207], 0\n"
        "ds_read_b64_tr_b16 v[40:41], v230 offset:40960\n"
        "v_accvgpr_write_b32 a224, v160\n"
        "v_mfma_f32_16x16x32_bf16 v[12:15], v[44:47], v[192:195], 0\n"
        "ds_read_b64_tr_b16 v[42:43], v231 offset:40960\n"
        "v_accvgpr_write_b32 a225, v161\n"
        "v_mfma_f32_16x16x32_bf16 v[76:79], v[44:47], v[196:199], 0\n"
        "v_accvgpr_write_b32 a226, v162\n"
        "v_accvgpr_write_b32 a227, v163\n"
        "v_mfma_f32_16x16x32_bf16 v[108:111], v[44:47], v[200:203], 0\n"
        "v_mul_f32 v171, s88, v171\n"
        "v_mul_f32 v172, s88, v172\n"
        "v_mfma_f32_16x16x32_bf16 v[140:143], v[44:47], v[204:207], 0\n"
        "ds_read_b64_tr_b16 v[44:45], v232 offset:40960\n"
        "v_mul_f32 v173, s88, v173\n"
        "v_mfma_f32_16x16x32_bf16 v[16:19], v[48:51], v[192:195], 0\n"
        "ds_read_b64_tr_b16 v[46:47], v233 offset:40960\n"
        "v_mul_f32 v174, s88, v174\n"
        "v_mfma_f32_16x16x32_bf16 v[80:83], v[48:51], v[196:199], 0\n"
        "v_mul_f32 v175, s88, v175\n"
        "v_mul_f32 v180, s88, v180\n"
        "v_mfma_f32_16x16x32_bf16 v[112:115], v[48:51], v[200:203], 0\n"
        "v_mul_f32 v181, s88, v181\n"
        "v_mul_f32 v182, s88, v182\n"
        "v_mfma_f32_16x16x32_bf16 v[144:147], v[48:51], v[204:207], 0\n"
        "ds_read_b64_tr_b16 v[48:49], v226 offset:41984\n"
        "v_mul_f32 v183, s88, v183\n"
        "v_mfma_f32_16x16x32_bf16 v[20:23], v[52:55], v[192:195], 0\n"
        "ds_read_b64_tr_b16 v[50:51], v227 offset:41984\n"
        "v_accvgpr_read_b32 v185, a201\n"
        "v_mfma_f32_16x16x32_bf16 v[84:87], v[52:55], v[196:199], 0\n"
        "v_accvgpr_read_b32 v186, a202\n"
        "v_accvgpr_read_b32 v187, a203\n"
        "v_mfma_f32_16x16x32_bf16 v[116:119], v[52:55], v[200:203], 0\n"
        "v_accvgpr_read_b32 v188, a204\n"
        "v_accvgpr_read_b32 v189, a205\n"
        "v_mfma_f32_16x16x32_bf16 v[148:151], v[52:55], v[204:207], 0\n"
        "ds_read_b64_tr_b16 v[52:53], v228 offset:41984\n"
        "v_accvgpr_read_b32 v190, a206\n"
        "v_mfma_f32_16x16x32_bf16 v[24:27], v[56:59], v[192:195], 0\n"
        "ds_read_b64_tr_b16 v[54:55], v229 offset:41984\n"
        "v_accvgpr_read_b32 v191, a207\n"
        "v_mfma_f32_16x16x32_bf16 v[88:91], v[56:59], v[196:199], 0\n"
        "v_mul_f32 v185, s88, v185\n"
        "v_mul_f32 v186, s88, v186\n"
        "v_mfma_f32_16x16x32_bf16 v[120:123], v[56:59], v[200:203], 0\n"
        "s_waitcnt lgkmcnt(4)\n"
        "v_mul_f32 v187, s88, v187\n"
        "v_mul_f32 v188, s88, v188\n"
        "v_mfma_f32_16x16x32_bf16 v[152:155], v[56:59], v[204:207], 0\n"
        "ds_read_b64_tr_b16 v[56:57], v230 offset:41984\n"
        "v_mul_f32 v189, s88, v189\n"
        "v_mfma_f32_16x16x32_bf16 v[28:31], v[60:63], v[192:195], 0\n"
        "ds_read_b64_tr_b16 v[58:59], v231 offset:41984\n"
        "v_mul_f32 v190, s88, v190\n"
        "v_mfma_f32_16x16x32_bf16 v[92:95], v[60:63], v[196:199], 0\n"
        "v_mul_f32 v191, s88, v191\n"
        "v_accvgpr_read_b32 v161, a161\n"
        "v_mfma_f32_16x16x32_bf16 v[124:127], v[60:63], v[200:203], 0\n"
        "v_accvgpr_read_b32 v162, a162\n"
        "v_accvgpr_read_b32 v163, a163\n"
        "v_mfma_f32_16x16x32_bf16 v[156:159], v[60:63], v[204:207], 0\n"
        "ds_read_b64_tr_b16 v[60:61], v232 offset:41984\n"
        "v_accvgpr_read_b32 v168, a232\n"
        "v_mfma_f32_16x16x32_bf16 v[0:3], v[32:35], v[208:211], v[0:3]\n"
        "ds_read_b64_tr_b16 v[62:63], v233 offset:41984\n"
        "v_mul_f32 v168, s88, v168\n"
        "v_mfma_f32_16x16x32_bf16 v[64:67], v[32:35], v[212:215], v[64:67]\n"
        "v_cvt_pk_bf16_f32 v168, v168, v169\n"
        "v_cvt_pk_bf16_f32 v169, v170, v171\n"
        "v_mfma_f32_16x16x32_bf16 v[96:99], v[32:35], v[216:219], v[96:99]\n"
        "v_cvt_pk_bf16_f32 v170, v172, v173\n"
        "v_cvt_pk_bf16_f32 v171, v174, v175\n"
        "v_mfma_f32_16x16x32_bf16 v[128:131], v[32:35], v[220:223], v[128:131]\n"
        "v_accvgpr_write_b32 a228, v168\n"
        "v_accvgpr_write_b32 a229, v169\n"
        "v_mfma_f32_16x16x32_bf16 v[4:7], v[36:39], v[208:211], v[4:7]\n"
        "v_accvgpr_write_b32 a230, v170\n"
        "v_accvgpr_write_b32 a231, v171\n"
        "v_mfma_f32_16x16x32_bf16 v[68:71], v[36:39], v[212:215], v[68:71]\n"
        "global_store_dwordx4 v254, a[224:227], s[70:71] offset:0 sc1\n"
        "v_accvgpr_read_b32 v164, a164\n"
        "v_accvgpr_read_b32 v165, a165\n"
        "v_accvgpr_read_b32 v166, a166\n"
        "v_accvgpr_read_b32 v167, a167\n"
        "v_mfma_f32_16x16x32_bf16 v[100:103], v[36:39], v[216:219], v[100:103]\n"
        "v_mul_f32 v161, s88, v161\n"
        "v_mul_f32 v162, s88, v162\n"
        "v_mfma_f32_16x16x32_bf16 v[132:135], v[36:39], v[220:223], v[132:135]\n"
        "s_waitcnt vmcnt(13) lgkmcnt(0)\n"
        "s_barrier\n"
        "v_mfma_f32_16x16x32_bf16 v[8:11], v[40:43], v[208:211], v[8:11]\n"
        "global_load_lds_dwordx4 v238, s[68:69] offset:0\n"
        "v_mfma_f32_16x16x32_bf16 v[72:75], v[40:43], v[212:215], v[72:75]\n"
        "ds_read_b64_tr_b16 v[32:33], v226 offset:49152\n"
        "v_mul_f32 v163, s88, v163\n"
        "v_mfma_f32_16x16x32_bf16 v[104:107], v[40:43], v[216:219], v[104:107]\n"
        "ds_read_b64_tr_b16 v[34:35], v227 offset:49152\n"
        "v_mul_f32 v164, s88, v164\n"
        "v_mfma_f32_16x16x32_bf16 v[136:139], v[40:43], v[220:223], v[136:139]\n"
        "ds_read_b64_tr_b16 v[36:37], v228 offset:49152\n"
        "v_mul_f32 v165, s88, v165\n"
        "v_mfma_f32_16x16x32_bf16 v[12:15], v[44:47], v[208:211], v[12:15]\n"
        "ds_read_b64_tr_b16 v[38:39], v229 offset:49152\n"
        "global_load_lds_dwordx4 v239, s[68:69] offset:1024\n"
        "v_mfma_f32_16x16x32_bf16 v[76:79], v[44:47], v[212:215], v[76:79]\n"
        "ds_read_b64_tr_b16 v[40:41], v230 offset:49152\n"
        "v_mul_f32 v166, s88, v166\n"
        "v_mfma_f32_16x16x32_bf16 v[108:111], v[44:47], v[216:219], v[108:111]\n"
        "ds_read_b64_tr_b16 v[42:43], v231 offset:49152\n"
        "v_mul_f32 v167, s88, v167\n"
        "v_mfma_f32_16x16x32_bf16 v[140:143], v[44:47], v[220:223], v[140:143]\n"
        "ds_read_b128 v[192:195], v248 offset:2048\n"
        "v_accvgpr_read_b32 v169, a169\n"
        "v_mfma_f32_16x16x32_bf16 v[16:19], v[48:51], v[208:211], v[16:19]\n"
        "ds_read_b64_tr_b16 v[44:45], v232 offset:49152\n"
        "global_load_lds_dwordx4 v240, s[68:69] offset:2048\n"
        "v_mfma_f32_16x16x32_bf16 v[80:83], v[48:51], v[212:215], v[80:83]\n"
        "ds_read_b64_tr_b16 v[46:47], v233 offset:49152\n"
        "v_accvgpr_read_b32 v170, a170\n"
        "v_mfma_f32_16x16x32_bf16 v[112:115], v[48:51], v[216:219], v[112:115]\n"
        "ds_read_b128 v[196:199], v248 offset:0\n"
        "v_accvgpr_read_b32 v171, a171\n"
        "v_mfma_f32_16x16x32_bf16 v[144:147], v[48:51], v[220:223], v[144:147]\n"
        "ds_read_b64_tr_b16 v[48:49], v226 offset:50176\n"
        "v_accvgpr_read_b32 v172, a172\n"
        "v_mfma_f32_16x16x32_bf16 v[20:23], v[52:55], v[208:211], v[20:23]\n"
        "ds_read_b64_tr_b16 v[50:51], v227 offset:50176\n"
        "global_load_lds_dwordx4 v241, s[68:69] offset:3072\n"
        "s_add_u32 m0, s72, 0x0\n"
        "v_mfma_f32_16x16x32_bf16 v[84:87], v[52:55], v[212:215], v[84:87]\n"
        "ds_read_b128 v[200:203], v248 offset:6144\n"
        "s_add_u32 s68, s68, 0x80000\n"
        "s_addc_u32 s69, s69, 0\n"
        "v_mfma_f32_16x16x32_bf16 v[116:119], v[52:55], v[216:219], v[116:119]\n"
        "ds_read_b128 v[204:207], v248 offset:4096\n"
        "v_accvgpr_read_b32 v173, a173\n"
        "v_mfma_f32_16x16x32_bf16 v[148:151], v[52:55], v[220:223], v[148:151]\n"
        "ds_read_b64_tr_b16 v[52:53], v228 offset:50176\n"
        "v_accvgpr_read_b32 v176, a192\n"
        "v_mfma_f32_16x16x32_bf16 v[24:27], v[56:59], v[208:211], v[24:27]\n"
        "ds_read_b64_tr_b16 v[54:55], v229 offset:50176\n"
        "global_load_lds_dwordx4 v242, s[64:65] offset:0\n"
        "v_mfma_f32_16x16x32_bf16 v[88:91], v[56:59], v[212:215], v[88:91]\n"
        "v_mul_f32 v176, s88, v176\n"
        "v_cvt_pk_bf16_f32 v176, v176, v177\n"
        "v_mfma_f32_16x16x32_bf16 v[120:123], v[56:59], v[216:219], v[120:123]\n"
        "s_waitcnt lgkmcnt(6)\n"
        "v_cvt_pk_bf16_f32 v177, v178, v179\n"
        "v_cvt_pk_bf16_f32 v178, v180, v181\n"
        "v_mfma_f32_16x16x32_bf16 v[152:155], v[56:59], v[220:223], v[152:155]\n"
        "ds_read_b64_tr_b16 v[56:57], v230 offset:50176\n"
        "v_cvt_pk_bf16_f32 v179, v182, v183\n"
        "v_mfma_f32_16x16x32_bf16 v[28:31], v[60:63], v[208:211], v[28:31]\n"
        "s_waitcnt lgkmcnt(5)\n"
        "v_accvgpr_write_b32 a232, v176\n"
        "v_accvgpr_write_b32 a233, v177\n"
        "v_mfma_f32_16x16x32_bf16 v[92:95], v[60:63], v[212:215], v[92:95]\n"
        "s_waitcnt lgkmcnt(4)\n"
        "v_accvgpr_write_b32 a234, v178\n"
        "v_accvgpr_write_b32 a235, v179\n"
        "v_mfma_f32_16x16x32_bf16 v[124:127], v[60:63], v[216:219], v[124:127]\n"
        "global_store_dwordx4 v254, a[228:231], s[70:71] offset:1024 sc1\n"
        "ds_read_b64_tr_b16 v[58:59], v231 offset:50176\n"
        "ds_read_b128 v[208:211], v249 offset:2048\n"
        "s_add_u32 s70, s38, s97\n"
        "s_addc_u32 s71, s39, 0\n"
        "v_mfma_f32_16x16x32_bf16 v[156:159], v[60:63], v[220:223], v[156:159]\n"
        "ds_read_b64_tr_b16 v[60:61], v232 offset:50176\n"
        "v_accvgpr_read_b32 v174, a174\n"
        "v_mfma_f32_16x16x32_bf16 v[0:3], v[32:35], v[192:195], v[0:3]\n"
        "ds_read_b64_tr_b16 v[62:63], v233 offset:50176\n"
        "v_accvgpr_read_b32 v175, a175\n"
        "v_mfma_f32_16x16x32_bf16 v[64:67], v[32:35], v[196:199], v[64:67]\n"
        "global_load_lds_dwordx4 v243, s[64:65] offset:1024\n"
        "v_mfma_f32_16x16x32_bf16 v[96:99], v[32:35], v[200:203], v[96:99]\n"
        "ds_read_b128 v[212:215], v249 offset:0\n"
        "s_waitcnt lgkmcnt(5)\n"
        "v_mfma_f32_16x16x32_bf16 v[128:131], v[32:35], v[204:207], v[128:131]\n"
        "ds_read_b64_tr_b16 v[32:33], v226 offset:57344\n"
        "v_mul_f32 v169, s88, v169\n"
        "v_mfma_f32_16x16x32_bf16 v[4:7], v[36:39], v[192:195], v[4:7]\n"
        "ds_read_b64_tr_b16 v[34:35], v227 offset:57344\n"
        "v_mul_f32 v170, s88, v170\n"
        "v_mfma_f32_16x16x32_bf16 v[68:71], v[36:39], v[196:199], v[68:71]\n"
        "global_load_dword v253, v252, s[46:47] offset:0\n"
        "v_mfma_f32_16x16x32_bf16 v[100:103], v[36:39], v[200:203], v[100:103]\n"
        "global_load_lds_dwordx4 v244, s[64:65] offset:2048\n"
        "ds_read_b128 v[216:219], v249 offset:6144\n"
        "v_mfma_f32_16x16x32_bf16 v[132:135], v[36:39], v[204:207], v[132:135]\n"
        "ds_read_b64_tr_b16 v[36:37], v228 offset:57344\n"
        "v_mul_f32 v171, s88, v171\n"
        "v_mfma_f32_16x16x32_bf16 v[8:11], v[40:43], v[192:195], v[8:11]\n"
        "ds_read_b64_tr_b16 v[38:39], v229 offset:57344\n"
        "v_mul_f32 v172, s88, v172\n"
        "v_mfma_f32_16x16x32_bf16 v[72:75], v[40:43], v[196:199], v[72:75]\n"
        "ds_read_b128 v[220:223], v249 offset:4096\n"
        "v_mul_f32 v173, s88, v173\n"
        "v_mfma_f32_16x16x32_bf16 v[104:107], v[40:43], v[200:203], v[104:107]\n"
        "global_load_lds_dwordx4 v245, s[64:65] offset:3072\n"
        "s_add_u32 m0, s72, 0x1000\n"
        "v_mul_f32 v174, s88, v174\n"
        "v_mfma_f32_16x16x32_bf16 v[136:139], v[40:43], v[204:207], v[136:139]\n"
        "ds_read_b64_tr_b16 v[40:41], v230 offset:57344\n"
        "s_add_u32 s64, s64, 0x80\n"
        "s_addc_u32 s65, s65, 0\n"
        "v_mfma_f32_16x16x32_bf16 v[12:15], v[44:47], v[192:195], v[12:15]\n"
        "ds_read_b64_tr_b16 v[42:43], v231 offset:57344\n"
        "s_add_u32 s97, s52, 0x5000\n"
        "v_mul_f32 v175, s88, v175\n"
        "v_mfma_f32_16x16x32_bf16 v[76:79], v[44:47], v[196:199], v[76:79]\n"
        "v_accvgpr_read_b32 v177, a1\n"
        "v_accvgpr_read_b32 v178, a2\n"
        "v_mfma_f32_16x16x32_bf16 v[108:111], v[44:47], v[200:203], v[108:111]\n"
        "global_load_lds_dwordx4 v242, s[66:67] offset:0\n"
        "v_accvgpr_read_b32 v179, a3\n"
        "v_mfma_f32_16x16x32_bf16 v[140:143], v[44:47], v[204:207], v[140:143]\n"
        "ds_read_b64_tr_b16 v[44:45], v232 offset:57344\n"
        "v_accvgpr_read_b32 v184, a200\n"
        "v_mfma_f32_16x16x32_bf16 v[16:19], v[48:51], v[192:195], v[16:19]\n"
        "ds_read_b64_tr_b16 v[46:47], v233 offset:57344\n"
        "v_mul_f32 v184, s88, v184\n"
        "v_mfma_f32_16x16x32_bf16 v[80:83], v[48:51], v[196:199], v[80:83]\n"
        "v_cvt_pk_bf16_f32 v184, v184, v185\n"
        "v_cvt_pk_bf16_f32 v185, v186, v187\n"
        "v_mfma_f32_16x16x32_bf16 v[112:115], v[48:51], v[200:203], v[112:115]\n"
        "global_load_lds_dwordx4 v243, s[66:67] offset:1024\n"
        "v_cvt_pk_bf16_f32 v186, v188, v189\n"
        "v_mfma_f32_16x16x32_bf16 v[144:147], v[48:51], v[204:207], v[144:147]\n"
        "s_waitcnt lgkmcnt(3)\n"
        "v_cvt_pk_bf16_f32 v187, v190, v191\n"
        "v_accvgpr_write_b32 a236, v184\n"
        "v_mfma_f32_16x16x32_bf16 v[20:23], v[52:55], v[192:195], v[20:23]\n"
        "ds_read_b64_tr_b16 v[48:49], v226 offset:58368\n"
        "v_accvgpr_write_b32 a237, v185\n"
        "v_mfma_f32_16x16x32_bf16 v[84:87], v[52:55], v[196:199], v[84:87]\n"
        "ds_read_b64_tr_b16 v[50:51], v227 offset:58368\n"
        "v_accvgpr_write_b32 a238, v186\n"
        "v_mfma_f32_16x16x32_bf16 v[116:119], v[52:55], v[200:203], v[116:119]\n"
        "global_load_lds_dwordx4 v244, s[66:67] offset:2048\n"
        "v_accvgpr_write_b32 a239, v187\n"
        "v_mfma_f32_16x16x32_bf16 v[148:151], v[52:55], v[204:207], v[148:151]\n"
        "ds_read_b64_tr_b16 v[52:53], v228 offset:58368\n"
        "v_accvgpr_read_b32 v180, a4\n"
        "v_mfma_f32_16x16x32_bf16 v[24:27], v[56:59], v[192:195], v[24:27]\n"
        "ds_read_b64_tr_b16 v[54:55], v229 offset:58368\n"
        "v_accvgpr_read_b32 v181, a5\n"
        "v_mfma_f32_16x16x32_bf16 v[88:91], v[56:59], v[196:199], v[88:91]\n"
        "v_accvgpr_read_b32 v182, a6\n"
        "v_accvgpr_read_b32 v183, a7\n"
        "v_mfma_f32_16x16x32_bf16 v[120:123], v[56:59], v[200:203], v[120:123]\n"
        "global_load_lds_dwordx4 v245, s[66:67] offset:3072\n"
        "s_add_u32 s66, s66, 0x80\n"
        "s_addc_u32 s67, s67, 0\n"
        "v_mfma_f32_16x16x32_bf16 v[152:155], v[56:59], v[204:207], v[152:155]\n"
        "ds_read_b64_tr_b16 v[56:57], v230 offset:58368\n"
        "s_add_u32 m0, s73, 0x24000\n"
        "v_mul_f32 v177, s88, v177\n"
        "v_mfma_f32_16x16x32_bf16 v[28:31], v[60:63], v[192:195], v[28:31]\n"
        "ds_read_b64_tr_b16 v[58:59], v231 offset:58368\n"
        "v_mul_f32 v178, s88, v178\n"
        "v_mfma_f32_16x16x32_bf16 v[92:95], v[60:63], v[196:199], v[92:95]\n"
        "v_mul_f32 v179, s88, v179\n"
        "v_mul_f32 v180, s88, v180\n"
        "v_mfma_f32_16x16x32_bf16 v[124:127], v[60:63], v[200:203], v[124:127]\n"
        "v_mul_f32 v181, s88, v181\n"
        "v_mul_f32 v182, s88, v182\n"
        "v_mfma_f32_16x16x32_bf16 v[156:159], v[60:63], v[204:207], v[156:159]\n"
        "ds_read_b64_tr_b16 v[60:61], v232 offset:58368\n"
        "v_mul_f32 v183, s88, v183\n"
        "v_mfma_f32_16x16x32_bf16 v[0:3], v[32:35], v[208:211], v[0:3]\n"
        "ds_read_b64_tr_b16 v[62:63], v233 offset:58368\n"
        "v_accvgpr_read_b32 v185, a9\n"
        "v_mfma_f32_16x16x32_bf16 v[64:67], v[32:35], v[212:215], v[64:67]\n"
        "v_accvgpr_read_b32 v186, a10\n"
        "v_accvgpr_read_b32 v187, a11\n"
        "v_mfma_f32_16x16x32_bf16 v[96:99], v[32:35], v[216:219], v[96:99]\n"
        "s_waitcnt lgkmcnt(4)\n"
        "v_accvgpr_read_b32 v188, a12\n"
        "v_accvgpr_read_b32 v189, a13\n"
        "v_mfma_f32_16x16x32_bf16 v[128:131], v[32:35], v[220:223], v[128:131]\n"
        "global_store_dwordx4 v254, a[232:235], s[70:71] offset:0 sc1\n"
        "v_accvgpr_read_b32 v190, a14\n"
        "v_accvgpr_read_b32 v191, a15\n"
        "v_mul_f32 v185, s88, v185\n"
        "v_mul_f32 v186, s88, v186\n"
        "v_mfma_f32_16x16x32_bf16 v[4:7], v[36:39], v[208:211], v[4:7]\n"
        "v_mul_f32 v187, s88, v187\n"
        "v_mul_f32 v188, s88, v188\n"
        "v_mfma_f32_16x16x32_bf16 v[68:71], v[36:39], v[212:215], v[68:71]\n"
        "v_mul_f32 v189, s88, v189\n"
        "v_mul_f32 v190, s88, v190\n"
        "v_mfma_f32_16x16x32_bf16 v[100:103], v[36:39], v[216:219], v[100:103]\n"
        "v_mul_f32 v191, s88, v191\n"
        "v_mfma_f32_16x16x32_bf16 v[132:135], v[36:39], v[220:223], v[132:135]\n"
        "s_waitcnt vmcnt(16) lgkmcnt(0)\n"
        "s_barrier\n"
        "v_mfma_f32_16x16x32_bf16 v[8:11], v[40:43], v[208:211], v[8:11]\n"
        "global_load_lds_dwordx4 v238, s[68:69] offset:0\n"
        "ds_read_b64_tr_b16 v[32:33], v226 offset:16384\n"
        "v_mfma_f32_16x16x32_bf16 v[72:75], v[40:43], v[212:215], v[72:75]\n"
        "ds_read_b64_tr_b16 v[34:35], v227 offset:16384\n"
        "v_accvgpr_read_b32 v160, a160\n"
        "v_mfma_f32_16x16x32_bf16 v[104:107], v[40:43], v[216:219], v[104:107]\n"
        "ds_read_b64_tr_b16 v[36:37], v228 offset:16384\n"
        "v_mul_f32 v160, s88, v160\n"
        "v_mfma_f32_16x16x32_bf16 v[136:139], v[40:43], v[220:223], v[136:139]\n"
        "ds_read_b64_tr_b16 v[38:39], v229 offset:16384\n"
        "v_cvt_pk_bf16_f32 v160, v160, v161\n"
        "v_mfma_f32_16x16x32_bf16 v[12:15], v[44:47], v[208:211], v[12:15]\n"
        "ds_read_b64_tr_b16 v[40:41], v230 offset:16384\n"
        "v_cvt_pk_bf16_f32 v161, v162, v163\n"
        "v_mfma_f32_16x16x32_bf16 v[76:79], v[44:47], v[212:215], v[76:79]\n"
        "ds_read_b64_tr_b16 v[42:43], v231 offset:16384\n"
        "v_cvt_pk_bf16_f32 v162, v164, v165\n"
        "v_mfma_f32_16x16x32_bf16 v[108:111], v[44:47], v[216:219], v[108:111]\n"
        "ds_read_b128 v[192:195], v246 offset:34816\n"
        "v_cvt_pk_bf16_f32 v163, v166, v167\n"
        "v_mfma_f32_16x16x32_bf16 v[140:143], v[44:47], v[220:223], v[140:143]\n"
        "global_load_lds_dwordx4 v239, s[68:69] offset:1024\n"
        "v_accvgpr_write_b32 a192, v160\n"
        "v_mfma_f32_16x16x32_bf16 v[16:19], v[48:51], v[208:211], v[16:19]\n"
        "ds_read_b64_tr_b16 v[44:45], v232 offset:16384\n"
        "v_accvgpr_write_b32 a193, v161\n"
        "v_mfma_f32_16x16x32_bf16 v[80:83], v[48:51], v[212:215], v[80:83]\n"
        "ds_read_b64_tr_b16 v[46:47], v233 offset:16384\n"
        "v_accvgpr_write_b32 a194, v162\n"
        "v_mfma_f32_16x16x32_bf16 v[112:115], v[48:51], v[216:219], v[112:115]\n"
        "ds_read_b128 v[196:199], v246 offset:32768\n"
        "v_accvgpr_write_b32 a195, v163\n"
        "v_mfma_f32_16x16x32_bf16 v[144:147], v[48:51], v[220:223], v[144:147]\n"
        "global_load_lds_dwordx4 v240, s[68:69] offset:2048\n"
        "ds_read_b64_tr_b16 v[48:49], v226 offset:17408\n"
        "v_mfma_f32_16x16x32_bf16 v[20:23], v[52:55], v[208:211], v[20:23]\n"
        "ds_read_b64_tr_b16 v[50:51], v227 offset:17408\n"
        "v_accvgpr_read_b32 v161, a33\n"
        "v_mfma_f32_16x16x32_bf16 v[84:87], v[52:55], v[212:215], v[84:87]\n"
        "ds_read_b128 v[200:203], v246 offset:38912\n"
        "v_accvgpr_read_b32 v162, a34\n"
        "v_mfma_f32_16x16x32_bf16 v[116:119], v[52:55], v[216:219], v[116:119]\n"
        "ds_read_b128 v[204:207], v246 offset:36864\n"
        "v_accvgpr_read_b32 v163, a35\n"
        "v_mfma_f32_16x16x32_bf16 v[148:151], v[52:55], v[220:223], v[148:151]\n"
        "global_load_lds_dwordx4 v241, s[68:69] offset:3072\n"
        "ds_read_b64_tr_b16 v[52:53], v228 offset:17408\n"
        "s_add_u32 m0, s72, 0x10000\n"
        "v_mfma_f32_16x16x32_bf16 v[24:27], v[56:59], v[208:211], v[24:27]\n"
        "ds_read_b64_tr_b16 v[54:55], v229 offset:17408\n"
        "s_add_u32 s68, s68, 0x80000\n"
        "s_addc_u32 s69, s69, 0\n"
        "v_mfma_f32_16x16x32_bf16 v[88:91], v[56:59], v[212:215], v[88:91]\n"
        "v_accvgpr_read_b32 v164, a36\n"
        "v_accvgpr_read_b32 v165, a37\n"
        "v_mfma_f32_16x16x32_bf16 v[120:123], v[56:59], v[216:219], v[120:123]\n"
        "s_waitcnt lgkmcnt(6)\n"
        "v_accvgpr_read_b32 v166, a38\n"
        "v_accvgpr_read_b32 v167, a39\n"
        "v_mfma_f32_16x16x32_bf16 v[152:155], v[56:59], v[220:223], v[152:155]\n"
        "global_load_lds_dwordx4 v242, s[64:65] offset:0\n"
        "ds_read_b64_tr_b16 v[56:57], v230 offset:17408\n"
        "v_mfma_f32_16x16x32_bf16 v[28:31], v[60:63], v[208:211], v[28:31]\n"
        "ds_read_b64_tr_b16 v[58:59], v231 offset:17408\n"
        "v_mul_f32 v161, s88, v161\n"
        "v_mfma_f32_16x16x32_bf16 v[92:95], v[60:63], v[212:215], v[92:95]\n"
        "ds_read_b128 v[208:211], v247 offset:34816\n"
        "v_mul_f32 v162, s88, v162\n"
        "v_mfma_f32_16x16x32_bf16 v[124:127], v[60:63], v[216:219], v[124:127]\n"
        "ds_read_b128 v[212:215], v247 offset:32768\n"
        "v_mul_f32 v163, s88, v163\n"
        "v_mfma_f32_16x16x32_bf16 v[156:159], v[60:63], v[220:223], v[156:159]\n"
        "global_load_lds_dwordx4 v243, s[64:65] offset:1024\n"
        "ds_read_b64_tr_b16 v[60:61], v232 offset:17408\n"
        "v_mfma_f32_16x16x32_bf16 v[0:3], v[32:35], v[192:195], v[0:3]\n"
        "ds_read_b64_tr_b16 v[62:63], v233 offset:17408\n"
        "v_accvgpr_read_b32 v168, a168\n"
        "v_mfma_f32_16x16x32_bf16 v[64:67], v[32:35], v[196:199], v[64:67]\n"
        "s_waitcnt lgkmcnt(6)\n"
        "v_mul_f32 v168, s88, v168\n"
        "v_cvt_pk_bf16_f32 v168, v168, v169\n"
        "v_mfma_f32_16x16x32_bf16 v[96:99], v[32:35], v[200:203], v[96:99]\n"
        "ds_read_b128 v[216:219], v247 offset:38912\n"
        "v_cvt_pk_bf16_f32 v169, v170, v171\n"
        "v_mfma_f32_16x16x32_bf16 v[128:131], v[32:35], v[204:207], v[128:131]\n"
        "global_load_lds_dwordx4 v244, s[64:65] offset:2048\n"
        "v_cvt_pk_bf16_f32 v170, v172, v173\n"
        "v_mfma_f32_16x16x32_bf16 v[4:7], v[36:39], v[192:195], v[4:7]\n"
        "ds_read_b64_tr_b16 v[32:33], v226 offset:24576\n"
        "v_cvt_pk_bf16_f32 v171, v174, v175\n"
        "v_mfma_f32_16x16x32_bf16 v[68:71], v[36:39], v[196:199], v[68:71]\n"
        "ds_read_b64_tr_b16 v[34:35], v227 offset:24576\n"
        "v_accvgpr_write_b32 a196, v168\n"
        "v_mfma_f32_16x16x32_bf16 v[100:103], v[36:39], v[200:203], v[100:103]\n"
        "ds_read_b128 v[220:223], v247 offset:36864\n"
        "v_accvgpr_write_b32 a197, v169\n"
        "v_mfma_f32_16x16x32_bf16 v[132:135], v[36:39], v[204:207], v[132:135]\n"
        "global_load_lds_dwordx4 v245, s[64:65] offset:3072\n"
        "s_add_u32 m0, s72, 0x11000\n"
        "v_accvgpr_write_b32 a198, v170\n"
        "v_mfma_f32_16x16x32_bf16 v[8:11], v[40:43], v[192:195], v[8:11]\n"
        "ds_read_b64_tr_b16 v[36:37], v228 offset:24576\n"
        "v_accvgpr_write_b32 a199, v171\n"
        "v_mfma_f32_16x16x32_bf16 v[72:75], v[40:43], v[196:199], v[72:75]\n"
        "ds_read_b64_tr_b16 v[38:39], v229 offset:24576\n"
        "s_add_u32 s64, s64, 0x80\n"
        "s_addc_u32 s65, s65, 0\n"
        "v_mfma_f32_16x16x32_bf16 v[104:107], v[40:43], v[200:203], v[104:107]\n"
        "v_mul_f32 v164, s88, v164\n"
        "v_mul_f32 v165, s88, v165\n"
        "v_mfma_f32_16x16x32_bf16 v[136:139], v[40:43], v[204:207], v[136:139]\n"
        "global_load_lds_dwordx4 v242, s[66:67] offset:0\n"
        "ds_read_b64_tr_b16 v[40:41], v230 offset:24576\n"
        "v_mfma_f32_16x16x32_bf16 v[12:15], v[44:47], v[192:195], v[12:15]\n"
        "ds_read_b64_tr_b16 v[42:43], v231 offset:24576\n"
        "v_mul_f32 v166, s88, v166\n"
        "v_mfma_f32_16x16x32_bf16 v[76:79], v[44:47], v[196:199], v[76:79]\n"
        "v_mul_f32 v167, s88, v167\n"
        "v_accvgpr_read_b32 v169, a41\n"
        "v_mfma_f32_16x16x32_bf16 v[108:111], v[44:47], v[200:203], v[108:111]\n"
        "v_accvgpr_read_b32 v170, a42\n"
        "v_accvgpr_read_b32 v171, a43\n"
        "v_mfma_f32_16x16x32_bf16 v[140:143], v[44:47], v[204:207], v[140:143]\n"
        "global_load_lds_dwordx4 v243, s[66:67] offset:1024\n"
        "ds_read_b64_tr_b16 v[44:45], v232 offset:24576\n"
        "v_mfma_f32_16x16x32_bf16 v[16:19], v[48:51], v[192:195], v[16:19]\n"
        "ds_read_b64_tr_b16 v[46:47], v233 offset:24576\n"
        "v_accvgpr_read_b32 v172, a44\n"
        "v_mfma_f32_16x16x32_bf16 v[80:83], v[48:51], v[196:199], v[80:83]\n"
        "v_accvgpr_read_b32 v173, a45\n"
        "v_accvgpr_read_b32 v174, a46\n"
        "v_mfma_f32_16x16x32_bf16 v[112:115], v[48:51], v[200:203], v[112:115]\n"
        "s_waitcnt lgkmcnt(4)\n"
        "v_accvgpr_read_b32 v175, a47\n"
        "v_mul_f32 v169, s88, v169\n"
        "v_mfma_f32_16x16x32_bf16 v[144:147], v[48:51], v[204:207], v[144:147]\n"
        "global_store_dwordx4 v254, a[236:239], s[70:71] offset:1024 sc1\n"
        "ds_read_b64_tr_b16 v[48:49], v226 offset:25600\n"
        "ds_read_b64_tr_b16 v[50:51], v227 offset:25600\n"
        "s_add_u32 s70, s38, s97\n"
        "s_addc_u32 s71, s39, 0\n"
        "v_mfma_f32_16x16x32_bf16 v[20:23], v[52:55], v[192:195], v[20:23]\n"
        "s_add_u32 s97, s52, 0x0\n"
        "v_mul_f32 v170, s88, v170\n"
        "v_mul_f32 v171, s88, v171\n"
        "v_mfma_f32_16x16x32_bf16 v[84:87], v[52:55], v[196:199], v[84:87]\n"
        "v_mul_f32 v172, s88, v172\n"
        "v_mul_f32 v173, s88, v173\n"
        "v_mfma_f32_16x16x32_bf16 v[116:119], v[52:55], v[200:203], v[116:119]\n"
        "global_load_lds_dwordx4 v244, s[66:67] offset:2048\n"
        "v_mfma_f32_16x16x32_bf16 v[148:151], v[52:55], v[204:207], v[148:151]\n"
        "ds_read_b64_tr_b16 v[52:53], v228 offset:25600\n"
        "v_accvgpr_read_b32 v176, a0\n"
        "v_mfma_f32_16x16x32_bf16 v[24:27], v[56:59], v[192:195], v[24:27]\n"
        "ds_read_b64_tr_b16 v[54:55], v229 offset:25600\n"
        "v_mul_f32 v176, s88, v176\n"
        "v_mfma_f32_16x16x32_bf16 v[88:91], v[56:59], v[196:199], v[88:91]\n"
        "v_cvt_pk_bf16_f32 v176, v176, v177\n"
        "v_cvt_pk_bf16_f32 v177, v178, v179\n"
        "v_mfma_f32_16x16x32_bf16 v[120:123], v[56:59], v[200:203], v[120:123]\n"
        "global_load_lds_dwordx4 v245, s[66:67] offset:3072\n"
        "s_add_u32 m0, s73, 0x1c000\n"
        "v_mfma_f32_16x16x32_bf16 v[152:155], v[56:59], v[204:207], v[152:155]\n"
        "s_add_u32 s66, s66, 0x80\n"
        "s_addc_u32 s67, s67, 0\n"
        "v_cvt_pk_bf16_f32 v178, v180, v181\n"
        "v_mfma_f32_16x16x32_bf16 v[28:31], v[60:63], v[192:195], v[28:31]\n"
        "ds_read_b64_tr_b16 v[56:57], v230 offset:25600\n"
        "v_cvt_pk_bf16_f32 v179, v182, v183\n"
        "v_mfma_f32_16x16x32_bf16 v[92:95], v[60:63], v[196:199], v[92:95]\n"
        "ds_read_b64_tr_b16 v[58:59], v231 offset:25600\n"
        "v_accvgpr_write_b32 a200, v176\n"
        "v_mfma_f32_16x16x32_bf16 v[124:127], v[60:63], v[200:203], v[124:127]\n"
        "v_accvgpr_write_b32 a201, v177\n"
        "v_accvgpr_write_b32 a202, v178\n"
        "v_mfma_f32_16x16x32_bf16 v[156:159], v[60:63], v[204:207], v[156:159]\n"
        "ds_read_b64_tr_b16 v[60:61], v232 offset:25600\n"
        "v_accvgpr_write_b32 a203, v179\n"
        "v_mfma_f32_16x16x32_bf16 v[0:3], v[32:35], v[208:211], v[0:3]\n"
        "ds_read_b64_tr_b16 v[62:63], v233 offset:25600\n"
        "v_mul_f32 v174, s88, v174\n"
        "v_mfma_f32_16x16x32_bf16 v[64:67], v[32:35], v[212:215], v[64:67]\n"
        "v_mul_f32 v175, s88, v175\n"
        "v_accvgpr_read_b32 v177, a65\n"
        "v_mfma_f32_16x16x32_bf16 v[96:99], v[32:35], v[216:219], v[96:99]\n"
        "s_waitcnt lgkmcnt(4)\n"
        "v_accvgpr_read_b32 v178, a66\n"
        "v_accvgpr_read_b32 v179, a67\n"
        "v_mfma_f32_16x16x32_bf16 v[128:131], v[32:35], v[220:223], v[128:131]\n"
        "v_accvgpr_read_b32 v180, a68\n"
        "v_accvgpr_read_b32 v181, a69\n"
        "v_mfma_f32_16x16x32_bf16 v[4:7], v[36:39], v[208:211], v[4:7]\n"
        "v_accvgpr_read_b32 v182, a70\n"
        "v_accvgpr_read_b32 v183, a71\n"
        "v_mfma_f32_16x16x32_bf16 v[68:71], v[36:39], v[212:215], v[68:71]\n"
        "v_mul_f32 v177, s88, v177\n"
        "v_mul_f32 v178, s88, v178\n"
        "v_mfma_f32_16x16x32_bf16 v[100:103], v[36:39], v[216:219], v[100:103]\n"
        "v_mul_f32 v179, s88, v179\n"
        "v_mul_f32 v180, s88, v180\n"
        "v_mfma_f32_16x16x32_bf16 v[132:135], v[36:39], v[220:223], v[132:135]\n"
        "v_mul_f32 v181, s88, v181\n"
        "v_mul_f32 v182, s88, v182\n"
        "v_mfma_f32_16x16x32_bf16 v[8:11], v[40:43], v[208:211], v[8:11]\n"
        "s_waitcnt vmcnt(14) lgkmcnt(0)\n"
        "s_barrier\n"
        "v_mfma_f32_16x16x32_bf16 v[72:75], v[40:43], v[212:215], v[72:75]\n"
        "global_load_lds_dwordx4 v238, s[68:69] offset:0\n"
        "ds_read_b64_tr_b16 v[32:33], v226 offset:32768\n"
        "v_mfma_f32_16x16x32_bf16 v[104:107], v[40:43], v[216:219], v[104:107]\n"
        "ds_read_b64_tr_b16 v[34:35], v227 offset:32768\n"
        "v_mul_f32 v183, s88, v183\n"
        "v_mfma_f32_16x16x32_bf16 v[136:139], v[40:43], v[220:223], v[136:139]\n"
        "ds_read_b64_tr_b16 v[36:37], v228 offset:32768\n"
        "v_mfma_f32_16x16x32_bf16 v[12:15], v[44:47], v[208:211], v[12:15]\n"
        "ds_read_b64_tr_b16 v[38:39], v229 offset:32768\n"
        "v_mfma_f32_16x16x32_bf16 v[76:79], v[44:47], v[212:215], v[76:79]\n"
        "ds_read_b64_tr_b16 v[40:41], v230 offset:32768\n"
        "global_load_lds_dwordx4 v239, s[68:69] offset:1024\n"
        "v_mfma_f32_16x16x32_bf16 v[108:111], v[44:47], v[216:219], v[108:111]\n"
        "ds_read_b64_tr_b16 v[42:43], v231 offset:32768\n"
        "v_mfma_f32_16x16x32_bf16 v[140:143], v[44:47], v[220:223], v[140:143]\n"
        "ds_read_b128 v[192:195], v246 offset:2048\n"
        "v_accvgpr_read_b32 v184, a8\n"
        "v_mfma_f32_16x16x32_bf16 v[16:19], v[48:51], v[208:211], v[16:19]\n"
        "ds_read_b64_tr_b16 v[44:45], v232 offset:32768\n"
        "v_mul_f32 v184, s88, v184\n"
        "v_mfma_f32_16x16x32_bf16 v[80:83], v[48:51], v[212:215], v[80:83]\n"
        "ds_read_b64_tr_b16 v[46:47], v233 offset:32768\n"
        "global_load_lds_dwordx4 v240, s[68:69] offset:2048\n"
        "v_mfma_f32_16x16x32_bf16 v[112:115], v[48:51], v[216:219], v[112:115]\n"
        "ds_read_b128 v[196:199], v246 offset:0\n"
        "v_cvt_pk_bf16_f32 v184, v184, v185\n"
        "v_mfma_f32_16x16x32_bf16 v[144:147], v[48:51], v[220:223], v[144:147]\n"
        "ds_read_b64_tr_b16 v[48:49], v226 offset:33792\n"
        "v_cvt_pk_bf16_f32 v185, v186, v187\n"
        "v_mfma_f32_16x16x32_bf16 v[20:23], v[52:55], v[208:211], v[20:23]\n"
        "ds_read_b64_tr_b16 v[50:51], v227 offset:33792\n"
        "v_cvt_pk_bf16_f32 v186, v188, v189\n"
        "v_mfma_f32_16x16x32_bf16 v[84:87], v[52:55], v[212:215], v[84:87]\n"
        "ds_read_b128 v[200:203], v246 offset:6144\n"
        "v_cvt_pk_bf16_f32 v187, v190, v191\n"
        "v_mfma_f32_16x16x32_bf16 v[116:119], v[52:55], v[216:219], v[116:119]\n"
        "global_load_lds_dwordx4 v241, s[68:69] offset:3072\n"
        "s_add_u32 m0, s72, 0x8000\n"
        "v_accvgpr_write_b32 a204, v184\n"
        "v_mfma_f32_16x16x32_bf16 v[148:151], v[52:55], v[220:223], v[148:151]\n"
        "ds_read_b64_tr_b16 v[52:53], v228 offset:33792\n"
        "v_accvgpr_write_b32 a205, v185\n"
        "v_mfma_f32_16x16x32_bf16 v[24:27], v[56:59], v[208:211], v[24:27]\n"
        "ds_read_b64_tr_b16 v[54:55], v229 offset:33792\n"
        "v_accvgpr_write_b32 a206, v186\n"
        "v_mfma_f32_16x16x32_bf16 v[88:91], v[56:59], v[212:215], v[88:91]\n"
        "ds_read_b128 v[204:207], v246 offset:4096\n"
        "v_accvgpr_write_b32 a207, v187\n"
        "v_mfma_f32_16x16x32_bf16 v[120:123], v[56:59], v[216:219], v[120:123]\n"
        "s_add_u32 s68, s68, 0x80000\n"
        "s_addc_u32 s69, s69, 0\n"
        "v_accvgpr_read_b32 v185, a73\n"
        "v_mfma_f32_16x16x32_bf16 v[152:155], v[56:59], v[220:223], v[152:155]\n"
        "global_load_lds_dwordx4 v242, s[64:65] offset:0\n"
        "ds_read_b64_tr_b16 v[56:57], v230 offset:33792\n"
        "v_mfma_f32_16x16x32_bf16 v[28:31], v[60:63], v[208:211], v[28:31]\n"
        "ds_read_b64_tr_b16 v[58:59], v231 offset:33792\n"
        "v_accvgpr_read_b32 v186, a74\n"
        "v_mfma_f32_16x16x32_bf16 v[92:95], v[60:63], v[212:215], v[92:95]\n"
        "ds_read_b128 v[208:211], v247 offset:2048\n"
        "v_accvgpr_read_b32 v187, a75\n"
        "v_mfma_f32_16x16x32_bf16 v[124:127], v[60:63], v[216:219], v[124:127]\n"
        "ds_read_b128 v[212:215], v247 offset:0\n"
        "v_accvgpr_read_b32 v188, a76\n"
        "v_mfma_f32_16x16x32_bf16 v[156:159], v[60:63], v[220:223], v[156:159]\n"
        "global_load_lds_dwordx4 v243, s[64:65] offset:1024\n"
        "s_waitcnt lgkmcnt(6)\n"
        "v_mfma_f32_16x16x32_bf16 v[0:3], v[32:35], v[192:195], v[0:3]\n"
        "ds_read_b64_tr_b16 v[60:61], v232 offset:33792\n"
        "v_accvgpr_read_b32 v189, a77\n"
        "v_mfma_f32_16x16x32_bf16 v[64:67], v[32:35], v[196:199], v[64:67]\n"
        "ds_read_b64_tr_b16 v[62:63], v233 offset:33792\n"
        "v_accvgpr_read_b32 v190, a78\n"
        "v_mfma_f32_16x16x32_bf16 v[96:99], v[32:35], v[200:203], v[96:99]\n"
        "ds_read_b128 v[216:219], v247 offset:6144\n"
        "s_waitcnt lgkmcnt(7)\n"
        "v_mfma_f32_16x16x32_bf16 v[128:131], v[32:35], v[204:207], v[128:131]\n"
        "global_store_dwordx4 v254, a[192:195], s[70:71] offset:0 sc1\n"
        "ds_read_b64_tr_b16 v[32:33], v226 offset:40960\n"
        "ds_read_b64_tr_b16 v[34:35], v227 offset:40960\n"
        "v_accvgpr_read_b32 v191, a79\n"
        "v_mfma_f32_16x16x32_bf16 v[4:7], v[36:39], v[192:195], v[4:7]\n"
        "ds_read_b128 v[220:223], v247 offset:4096\n"
        "v_mul_f32 v185, s88, v185\n"
        "v_mfma_f32_16x16x32_bf16 v[68:71], v[36:39], v[196:199], v[68:71]\n"
        "v_mul_f32 v186, s88, v186\n"
        "v_mul_f32 v187, s88, v187\n"
        "v_mfma_f32_16x16x32_bf16 v[100:103], v[36:39], v[200:203], v[100:103]\n"
        "global_load_lds_dwordx4 v244, s[64:65] offset:2048\n"
        "v_mfma_f32_16x16x32_bf16 v[132:135], v[36:39], v[204:207], v[132:135]\n"
        "ds_read_b64_tr_b16 v[36:37], v228 offset:40960\n"
        "v_accvgpr_read_b32 v160, a32\n"
        "v_mfma_f32_16x16x32_bf16 v[8:11], v[40:43], v[192:195], v[8:11]\n"
        "ds_read_b64_tr_b16 v[38:39], v229 offset:40960\n"
        "v_mul_f32 v160, s88, v160\n"
        "v_mfma_f32_16x16x32_bf16 v[72:75], v[40:43], v[196:199], v[72:75]\n"
        "v_cvt_pk_bf16_f32 v160, v160, v161\n"
        "v_cvt_pk_bf16_f32 v161, v162, v163\n"
        "v_mfma_f32_16x16x32_bf16 v[104:107], v[40:43], v[200:203], v[104:107]\n"
        "global_load_lds_dwordx4 v245, s[64:65] offset:3072\n"
        "s_add_u32 m0, s72, 0x9000\n"
        "v_mfma_f32_16x16x32_bf16 v[136:139], v[40:43], v[204:207], v[136:139]\n"
        "ds_read_b64_tr_b16 v[40:41], v230 offset:40960\n"
        "v_cvt_pk_bf16_f32 v162, v164, v165\n"
        "v_mfma_f32_16x16x32_bf16 v[12:15], v[44:47], v[192:195], v[12:15]\n"
        "ds_read_b64_tr_b16 v[42:43], v231 offset:40960\n"
        "v_cvt_pk_bf16_f32 v163, v166, v167\n"
        "v_mfma_f32_16x16x32_bf16 v[76:79], v[44:47], v[196:199], v[76:79]\n"
        "s_add_u32 s64, s64, 0x80\n"
        "s_addc_u32 s65, s65, 0\n"
        "v_accvgpr_write_b32 a160, v160\n"
        "v_mfma_f32_16x16x32_bf16 v[108:111], v[44:47], v[200:203], v[108:111]\n"
        "global_load_lds_dwordx4 v242, s[66:67] offset:0\n"
        "v_mfma_f32_16x16x32_bf16 v[140:143], v[44:47], v[204:207], v[140:143]\n"
        "ds_read_b64_tr_b16 v[44:45], v232 offset:40960\n"
        "v_accvgpr_write_b32 a161, v161\n"
        "v_mfma_f32_16x16x32_bf16 v[16:19], v[48:51], v[192:195], v[16:19]\n"
        "ds_read_b64_tr_b16 v[46:47], v233 offset:40960\n"
        "v_accvgpr_write_b32 a162, v162\n"
        "v_mfma_f32_16x16x32_bf16 v[80:83], v[48:51], v[196:199], v[80:83]\n"
        "v_accvgpr_write_b32 a163, v163\n"
        "v_mul_f32 v188, s88, v188\n"
        "v_mfma_f32_16x16x32_bf16 v[112:115], v[48:51], v[200:203], v[112:115]\n"
        "global_load_lds_dwordx4 v243, s[66:67] offset:1024\n"
        "v_mfma_f32_16x16x32_bf16 v[144:147], v[48:51], v[204:207], v[144:147]\n"
        "ds_read_b64_tr_b16 v[48:49], v226 offset:41984\n"
        "v_mul_f32 v189, s88, v189\n"
        "v_mfma_f32_16x16x32_bf16 v[20:23], v[52:55], v[192:195], v[20:23]\n"
        "ds_read_b64_tr_b16 v[50:51], v227 offset:41984\n"
        "v_mul_f32 v190, s88, v190\n"
        "v_mfma_f32_16x16x32_bf16 v[84:87], v[52:55], v[196:199], v[84:87]\n"
        "s_waitcnt lgkmcnt(4)\n"
        "v_mul_f32 v191, s88, v191\n"
        "v_accvgpr_read_b32 v161, a97\n"
        "v_mfma_f32_16x16x32_bf16 v[116:119], v[52:55], v[200:203], v[116:119]\n"
        "global_load_lds_dwordx4 v244, s[66:67] offset:2048\n"
        "v_mfma_f32_16x16x32_bf16 v[148:151], v[52:55], v[204:207], v[148:151]\n"
        "ds_read_b64_tr_b16 v[52:53], v228 offset:41984\n"
        "v_accvgpr_read_b32 v162, a98\n"
        "v_mfma_f32_16x16x32_bf16 v[24:27], v[56:59], v[192:195], v[24:27]\n"
        "ds_read_b64_tr_b16 v[54:55], v229 offset:41984\n"
        "v_accvgpr_read_b32 v163, a99\n"
        "v_mfma_f32_16x16x32_bf16 v[88:91], v[56:59], v[196:199], v[88:91]\n"
        "v_accvgpr_read_b32 v164, a100\n"
        "v_accvgpr_read_b32 v165, a101\n"
        "v_mfma_f32_16x16x32_bf16 v[120:123], v[56:59], v[200:203], v[120:123]\n"
        "global_load_lds_dwordx4 v245, s[66:67] offset:3072\n"
        "s_add_u32 m0, s73, 0x20000\n"
        "v_mfma_f32_16x16x32_bf16 v[152:155], v[56:59], v[204:207], v[152:155]\n"
        "s_add_u32 s66, s66, 0x80\n"
        "s_addc_u32 s67, s67, 0\n"
        "v_accvgpr_read_b32 v166, a102\n"
        "v_mfma_f32_16x16x32_bf16 v[28:31], v[60:63], v[192:195], v[28:31]\n"
        "ds_read_b64_tr_b16 v[56:57], v230 offset:41984\n"
        "v_accvgpr_read_b32 v167, a103\n"
        "v_mfma_f32_16x16x32_bf16 v[92:95], v[60:63], v[196:199], v[92:95]\n"
        "ds_read_b64_tr_b16 v[58:59], v231 offset:41984\n"
        "v_mul_f32 v161, s88, v161\n"
        "v_mfma_f32_16x16x32_bf16 v[124:127], v[60:63], v[200:203], v[124:127]\n"
        "v_mul_f32 v162, s88, v162\n"
        "v_mul_f32 v163, s88, v163\n"
        "v_mfma_f32_16x16x32_bf16 v[156:159], v[60:63], v[204:207], v[156:159]\n"
        "ds_read_b64_tr_b16 v[60:61], v232 offset:41984\n"
        "v_accvgpr_read_b32 v168, a40\n"
        "v_mfma_f32_16x16x32_bf16 v[0:3], v[32:35], v[208:211], v[0:3]\n"
        "ds_read_b64_tr_b16 v[62:63], v233 offset:41984\n"
        "v_mul_f32 v168, s88, v168\n"
        "v_mfma_f32_16x16x32_bf16 v[64:67], v[32:35], v[212:215], v[64:67]\n"
        "v_cvt_pk_bf16_f32 v168, v168, v169\n"
        "v_cvt_pk_bf16_f32 v169, v170, v171\n"
        "v_mfma_f32_16x16x32_bf16 v[96:99], v[32:35], v[216:219], v[96:99]\n"
        "v_cvt_pk_bf16_f32 v170, v172, v173\n"
        "v_cvt_pk_bf16_f32 v171, v174, v175\n"
        "v_mfma_f32_16x16x32_bf16 v[128:131], v[32:35], v[220:223], v[128:131]\n"
        "v_accvgpr_write_b32 a164, v168\n"
        "v_accvgpr_write_b32 a165, v169\n"
        "v_mfma_f32_16x16x32_bf16 v[4:7], v[36:39], v[208:211], v[4:7]\n"
        "v_accvgpr_write_b32 a166, v170\n"
        "v_accvgpr_write_b32 a167, v171\n"
        "v_mfma_f32_16x16x32_bf16 v[68:71], v[36:39], v[212:215], v[68:71]\n"
        "v_mul_f32 v164, s88, v164\n"
        "v_mul_f32 v165, s88, v165\n"
        "v_mfma_f32_16x16x32_bf16 v[100:103], v[36:39], v[216:219], v[100:103]\n"
        "s_waitcnt lgkmcnt(2)\n"
        "v_mul_f32 v166, s88, v166\n"
        "v_mul_f32 v167, s88, v167\n"
        "v_mfma_f32_16x16x32_bf16 v[132:135], v[36:39], v[220:223], v[132:135]\n"
        "global_store_dwordx4 v254, a[196:199], s[70:71] offset:1024 sc1\n"
        "s_waitcnt vmcnt(14) lgkmcnt(0)\n"
        "s_barrier\n"
        "v_mfma_f32_16x16x32_bf16 v[8:11], v[40:43], v[208:211], v[8:11]\n"
        "ds_read_b64_tr_b16 v[32:33], v226 offset:49152\n"
        "s_add_u32 s70, s38, s97\n"
        "s_addc_u32 s71, s39, 0\n"
        "v_mfma_f32_16x16x32_bf16 v[72:75], v[40:43], v[212:215], v[72:75]\n"
        "ds_read_b64_tr_b16 v[34:35], v227 offset:49152\n"
        "v_accvgpr_read_b32 v169, a105\n"
        "v_mfma_f32_16x16x32_bf16 v[104:107], v[40:43], v[216:219], v[104:107]\n"
        "global_load_lds_dwordx4 v238, s[68:69] offset:0\n"
        "v_mfma_f32_16x16x32_bf16 v[136:139], v[40:43], v[220:223], v[136:139]\n"
        "ds_read_b64_tr_b16 v[36:37], v228 offset:49152\n"
        "v_accvgpr_read_b32 v170, a106\n"
        "v_mfma_f32_16x16x32_bf16 v[12:15], v[44:47], v[208:211], v[12:15]\n"
        "ds_read_b64_tr_b16 v[38:39], v229 offset:49152\n"
        "v_accvgpr_read_b32 v171, a107\n"
        "v_mfma_f32_16x16x32_bf16 v[76:79], v[44:47], v[212:215], v[76:79]\n"
        "ds_read_b64_tr_b16 v[40:41], v230 offset:49152\n"
        "v_accvgpr_read_b32 v172, a108\n"
        "v_mfma_f32_16x16x32_bf16 v[108:111], v[44:47], v[216:219], v[108:111]\n"
        "ds_read_b64_tr_b16 v[42:43], v231 offset:49152\n"
        "global_load_lds_dwordx4 v239, s[68:69] offset:1024\n"
        "v_mfma_f32_16x16x32_bf16 v[140:143], v[44:47], v[220:223], v[140:143]\n"
        "ds_read_b128 v[192:195], v248 offset:2048\n"
        "v_accvgpr_read_b32 v173, a109\n"
        "v_mfma_f32_16x16x32_bf16 v[16:19], v[48:51], v[208:211], v[16:19]\n"
        "ds_read_b64_tr_b16 v[44:45], v232 offset:49152\n"
        "v_accvgpr_read_b32 v174, a110\n"
        "v_mfma_f32_16x16x32_bf16 v[80:83], v[48:51], v[212:215], v[80:83]\n"
        "ds_read_b64_tr_b16 v[46:47], v233 offset:49152\n"
        "v_accvgpr_read_b32 v175, a111\n"
        "v_mfma_f32_16x16x32_bf16 v[112:115], v[48:51], v[216:219], v[112:115]\n"
        "ds_read_b128 v[196:199], v248 offset:0\n"
        "global_load_lds_dwordx4 v240, s[68:69] offset:2048\n"
        "s_add_u32 s97, s52, 0x1000\n"
        "v_mfma_f32_16x16x32_bf16 v[144:147], v[48:51], v[220:223], v[144:147]\n"
        "ds_read_b64_tr_b16 v[48:49], v226 offset:50176\n"
        "v_mul_f32 v169, s88, v169\n"
        "v_mfma_f32_16x16x32_bf16 v[20:23], v[52:55], v[208:211], v[20:23]\n"
        "ds_read_b64_tr_b16 v[50:51], v227 offset:50176\n"
        "v_mul_f32 v170, s88, v170\n"
        "v_mfma_f32_16x16x32_bf16 v[84:87], v[52:55], v[212:215], v[84:87]\n"
        "ds_read_b128 v[200:203], v248 offset:6144\n"
        "v_mul_f32 v171, s88, v171\n"
        "v_mfma_f32_16x16x32_bf16 v[116:119], v[52:55], v[216:219], v[116:119]\n"
        "global_load_lds_dwordx4 v241, s[68:69] offset:3072\n"
        "s_add_u32 m0, s72, 0x0\n"
        "v_mfma_f32_16x16x32_bf16 v[148:151], v[52:55], v[220:223], v[148:151]\n"
        "ds_read_b64_tr_b16 v[52:53], v228 offset:50176\n"
        "v_accvgpr_read_b32 v176, a64\n"
        "v_mfma_f32_16x16x32_bf16 v[24:27], v[56:59], v[208:211], v[24:27]\n"
        "ds_read_b64_tr_b16 v[54:55], v229 offset:50176\n"
        "s_add_u32 s68, s68, 0x80000\n"
        "s_addc_u32 s69, s69, 0\n"
        "v_mfma_f32_16x16x32_bf16 v[88:91], v[56:59], v[212:215], v[88:91]\n"
        "ds_read_b128 v[204:207], v248 offset:4096\n"
        "v_mul_f32 v176, s88, v176\n"
        "v_mfma_f32_16x16x32_bf16 v[120:123], v[56:59], v[216:219], v[120:123]\n"
        "global_load_lds_dwordx4 v242, s[64:65] offset:0\n"
        "v_mfma_f32_16x16x32_bf16 v[152:155], v[56:59], v[220:223], v[152:155]\n"
        "s_waitcnt lgkmcnt(5)\n"
        "v_cvt_pk_bf16_f32 v176, v176, v177\n"
        "v_cvt_pk_bf16_f32 v177, v178, v179\n"
        "v_mfma_f32_16x16x32_bf16 v[28:31], v[60:63], v[208:211], v[28:31]\n"
        "s_waitcnt lgkmcnt(4)\n"
        "v_cvt_pk_bf16_f32 v178, v180, v181\n"
        "v_cvt_pk_bf16_f32 v179, v182, v183\n"
        "v_mfma_f32_16x16x32_bf16 v[92:95], v[60:63], v[212:215], v[92:95]\n"
        "s_waitcnt lgkmcnt(3)\n"
        "v_accvgpr_write_b32 a168, v176\n"
        "v_accvgpr_write_b32 a169, v177\n"
        "v_mfma_f32_16x16x32_bf16 v[124:127], v[60:63], v[216:219], v[124:127]\n"
        "ds_read_b64_tr_b16 v[56:57], v230 offset:50176\n"
        "v_accvgpr_write_b32 a170, v178\n"
        "v_mfma_f32_16x16x32_bf16 v[156:159], v[60:63], v[220:223], v[156:159]\n"
        "ds_read_b64_tr_b16 v[58:59], v231 offset:50176\n"
        "v_accvgpr_write_b32 a171, v179\n"
        "v_mfma_f32_16x16x32_bf16 v[0:3], v[32:35], v[192:195], v[0:3]\n"
        "global_load_lds_dwordx4 v243, s[64:65] offset:1024\n"
        "ds_read_b64_tr_b16 v[60:61], v232 offset:50176\n"
        "v_mfma_f32_16x16x32_bf16 v[64:67], v[32:35], v[196:199], v[64:67]\n"
        "ds_read_b64_tr_b16 v[62:63], v233 offset:50176\n"
        "v_mul_f32 v172, s88, v172\n"
        "v_mfma_f32_16x16x32_bf16 v[96:99], v[32:35], v[200:203], v[96:99]\n"
        "ds_read_b128 v[208:211], v249 offset:2048\n"
        "s_waitcnt lgkmcnt(5)\n"
        "v_mfma_f32_16x16x32_bf16 v[128:131], v[32:35], v[204:207], v[128:131]\n"
        "ds_read_b64_tr_b16 v[32:33], v226 offset:57344\n"
        "v_mul_f32 v173, s88, v173\n"
        "v_mfma_f32_16x16x32_bf16 v[4:7], v[36:39], v[192:195], v[4:7]\n"
        "global_load_lds_dwordx4 v244, s[64:65] offset:2048\n"
        "ds_read_b64_tr_b16 v[34:35], v227 offset:57344\n"
        "v_mfma_f32_16x16x32_bf16 v[68:71], v[36:39], v[196:199], v[68:71]\n"
        "ds_read_b128 v[212:215], v249 offset:0\n"
        "v_mul_f32 v174, s88, v174\n"
        "v_mfma_f32_16x16x32_bf16 v[100:103], v[36:39], v[200:203], v[100:103]\n"
        "ds_read_b128 v[216:219], v249 offset:6144\n"
        "v_mul_f32 v175, s88, v175\n"
        "v_mfma_f32_16x16x32_bf16 v[132:135], v[36:39], v[204:207], v[132:135]\n"
        "ds_read_b64_tr_b16 v[36:37], v228 offset:57344\n"
        "v_accvgpr_read_b32 v177, a129\n"
        "v_mfma_f32_16x16x32_bf16 v[8:11], v[40:43], v[192:195], v[8:11]\n"
        "global_load_lds_dwordx4 v245, s[64:65] offset:3072\n"
        "ds_read_b64_tr_b16 v[38:39], v229 offset:57344\n"
        "s_add_u32 m0, s72, 0x1000\n"
        "v_mfma_f32_16x16x32_bf16 v[72:75], v[40:43], v[196:199], v[72:75]\n"
        "ds_read_b128 v[220:223], v249 offset:4096\n"
        "s_add_u32 s64, s64, 0x80\n"
        "s_addc_u32 s65, s65, 0\n"
        "v_mfma_f32_16x16x32_bf16 v[104:107], v[40:43], v[200:203], v[104:107]\n"
        "v_accvgpr_read_b32 v178, a130\n"
        "v_accvgpr_read_b32 v179, a131\n"
        "v_mfma_f32_16x16x32_bf16 v[136:139], v[40:43], v[204:207], v[136:139]\n"
        "ds_read_b64_tr_b16 v[40:41], v230 offset:57344\n"
        "v_accvgpr_read_b32 v180, a132\n"
        "v_mfma_f32_16x16x32_bf16 v[12:15], v[44:47], v[192:195], v[12:15]\n"
        "global_load_lds_dwordx4 v242, s[66:67] offset:0\n"
        "ds_read_b64_tr_b16 v[42:43], v231 offset:57344\n"
        "v_mfma_f32_16x16x32_bf16 v[76:79], v[44:47], v[196:199], v[76:79]\n"
        "v_accvgpr_read_b32 v181, a133\n"
        "v_accvgpr_read_b32 v182, a134\n"
        "v_mfma_f32_16x16x32_bf16 v[108:111], v[44:47], v[200:203], v[108:111]\n"
        "v_accvgpr_read_b32 v183, a135\n"
        "v_mul_f32 v177, s88, v177\n"
        "v_mfma_f32_16x16x32_bf16 v[140:143], v[44:47], v[204:207], v[140:143]\n"
        "ds_read_b64_tr_b16 v[44:45], v232 offset:57344\n"
        "v_accvgpr_read_b32 v184, a72\n"
        "v_mfma_f32_16x16x32_bf16 v[16:19], v[48:51], v[192:195], v[16:19]\n"
        "global_load_lds_dwordx4 v243, s[66:67] offset:1024\n"
        "ds_read_b64_tr_b16 v[46:47], v233 offset:57344\n"
        "v_mfma_f32_16x16x32_bf16 v[80:83], v[48:51], v[196:199], v[80:83]\n"
        "v_mul_f32 v184, s88, v184\n"
        "v_cvt_pk_bf16_f32 v184, v184, v185\n"
        "v_mfma_f32_16x16x32_bf16 v[112:115], v[48:51], v[200:203], v[112:115]\n"
        "s_waitcnt lgkmcnt(4)\n"
        "v_cvt_pk_bf16_f32 v185, v186, v187\n"
        "v_cvt_pk_bf16_f32 v186, v188, v189\n"
        "v_mfma_f32_16x16x32_bf16 v[144:147], v[48:51], v[204:207], v[144:147]\n"
        "ds_read_b64_tr_b16 v[48:49], v226 offset:58368\n"
        "v_cvt_pk_bf16_f32 v187, v190, v191\n"
        "v_mfma_f32_16x16x32_bf16 v[20:23], v[52:55], v[192:195], v[20:23]\n"
        "global_load_lds_dwordx4 v244, s[66:67] offset:2048\n"
        "v_accvgpr_write_b32 a172, v184\n"
        "v_mfma_f32_16x16x32_bf16 v[84:87], v[52:55], v[196:199], v[84:87]\n"
        "ds_read_b64_tr_b16 v[50:51], v227 offset:58368\n"
        "v_accvgpr_write_b32 a173, v185\n"
        "v_mfma_f32_16x16x32_bf16 v[116:119], v[52:55], v[200:203], v[116:119]\n"
        "v_accvgpr_write_b32 a174, v186\n"
        "v_accvgpr_write_b32 a175, v187\n"
        "v_mfma_f32_16x16x32_bf16 v[148:151], v[52:55], v[204:207], v[148:151]\n"
        "ds_read_b64_tr_b16 v[52:53], v228 offset:58368\n"
        "v_mul_f32 v178, s88, v178\n"
        "v_mfma_f32_16x16x32_bf16 v[24:27], v[56:59], v[192:195], v[24:27]\n"
        "global_store_dwordx4 v254, a[200:203], s[70:71] offset:0 sc1\n"
        "ds_read_b64_tr_b16 v[54:55], v229 offset:58368\n"
        "v_mul_f32 v179, s88, v179\n"
        "v_mul_f32 v180, s88, v180\n"
        "v_mul_f32 v181, s88, v181\n"
        "v_mfma_f32_16x16x32_bf16 v[88:91], v[56:59], v[196:199], v[88:91]\n"
        "v_mul_f32 v182, s88, v182\n"
        "v_mul_f32 v183, s88, v183\n"
        "v_mfma_f32_16x16x32_bf16 v[120:123], v[56:59], v[200:203], v[120:123]\n"
        "v_accvgpr_read_b32 v185, a137\n"
        "v_accvgpr_read_b32 v186, a138\n"
        "v_mfma_f32_16x16x32_bf16 v[152:155], v[56:59], v[204:207], v[152:155]\n"
        "global_load_lds_dwordx4 v245, s[66:67] offset:3072\n"
        "s_add_u32 m0, s73, 0x24000\n"
        "v_mfma_f32_16x16x32_bf16 v[28:31], v[60:63], v[192:195], v[28:31]\n"
        "s_add_u32 s66, s66, 0x80\n"
        "s_addc_u32 s67, s67, 0\n"
        "v_accvgpr_read_b32 v187, a139\n"
        "v_mfma_f32_16x16x32_bf16 v[92:95], v[60:63], v[196:199], v[92:95]\n"
        "ds_read_b64_tr_b16 v[56:57], v230 offset:58368\n"
        "v_accvgpr_read_b32 v188, a140\n"
        "v_mfma_f32_16x16x32_bf16 v[124:127], v[60:63], v[200:203], v[124:127]\n"
        "ds_read_b64_tr_b16 v[58:59], v231 offset:58368\n"
        "v_accvgpr_read_b32 v189, a141\n"
        "v_mfma_f32_16x16x32_bf16 v[156:159], v[60:63], v[204:207], v[156:159]\n"
        "ds_read_b64_tr_b16 v[60:61], v232 offset:58368\n"
        "v_accvgpr_read_b32 v190, a142\n"
        "v_mfma_f32_16x16x32_bf16 v[0:3], v[32:35], v[208:211], v[0:3]\n"
        "ds_read_b64_tr_b16 v[62:63], v233 offset:58368\n"
        "v_accvgpr_read_b32 v191, a143\n"
        "v_mfma_f32_16x16x32_bf16 v[64:67], v[32:35], v[212:215], v[64:67]\n"
        "v_mul_f32 v185, s88, v185\n"
        "v_mul_f32 v186, s88, v186\n"
        "v_mfma_f32_16x16x32_bf16 v[96:99], v[32:35], v[216:219], v[96:99]\n"
        "s_waitcnt lgkmcnt(4)\n"
        "v_mul_f32 v187, s88, v187\n"
        "v_mul_f32 v188, s88, v188\n"
        "v_mfma_f32_16x16x32_bf16 v[128:131], v[32:35], v[220:223], v[128:131]\n"
        "v_mul_f32 v189, s88, v189\n"
        "v_mul_f32 v190, s88, v190\n"
        "v_mfma_f32_16x16x32_bf16 v[4:7], v[36:39], v[208:211], v[4:7]\n"
        "v_mul_f32 v191, s88, v191\n"
        "v_mfma_f32_16x16x32_bf16 v[68:71], v[36:39], v[212:215], v[68:71]\n"
        "v_mfma_f32_16x16x32_bf16 v[100:103], v[36:39], v[216:219], v[100:103]\n"
        "v_mfma_f32_16x16x32_bf16 v[132:135], v[36:39], v[220:223], v[132:135]\n"
        "v_accvgpr_read_b32 v160, a96\n"
        "v_mul_f32 v160, s88, v160\n"
        "v_mfma_f32_16x16x32_bf16 v[8:11], v[40:43], v[208:211], v[8:11]\n"
        "s_waitcnt vmcnt(14) lgkmcnt(0)\n"
        "s_barrier\n"
        "v_cvt_pk_bf16_f32 v160, v160, v161\n"
        "v_mfma_f32_16x16x32_bf16 v[72:75], v[40:43], v[212:215], v[72:75]\n"
        "global_load_lds_dwordx4 v238, s[68:69] offset:0\n"
        "ds_read_b64_tr_b16 v[32:33], v226 offset:16384\n"
        "v_mfma_f32_16x16x32_bf16 v[104:107], v[40:43], v[216:219], v[104:107]\n"
        "ds_read_b64_tr_b16 v[34:35], v227 offset:16384\n"
        "v_cvt_pk_bf16_f32 v161, v162, v163\n"
        "v_mfma_f32_16x16x32_bf16 v[136:139], v[40:43], v[220:223], v[136:139]\n"
        "ds_read_b64_tr_b16 v[36:37], v228 offset:16384\n"
        "v_cvt_pk_bf16_f32 v162, v164, v165\n"
        "v_mfma_f32_16x16x32_bf16 v[12:15], v[44:47], v[208:211], v[12:15]\n"
        "ds_read_b64_tr_b16 v[38:39], v229 offset:16384\n"
        "v_cvt_pk_bf16_f32 v163, v166, v167\n"
        "v_mfma_f32_16x16x32_bf16 v[76:79], v[44:47], v[212:215], v[76:79]\n"
        "ds_read_b64_tr_b16 v[40:41], v230 offset:16384\n"
        "v_accvgpr_write_b32 a0, v160\n"
        "v_mfma_f32_16x16x32_bf16 v[108:111], v[44:47], v[216:219], v[108:111]\n"
        "ds_read_b64_tr_b16 v[42:43], v231 offset:16384\n"
        "v_accvgpr_write_b32 a1, v161\n"
        "v_mfma_f32_16x16x32_bf16 v[140:143], v[44:47], v[220:223], v[140:143]\n"
        "ds_read_b128 v[192:195], v246 offset:34816\n"
        "v_accvgpr_write_b32 a2, v162\n"
        "v_mfma_f32_16x16x32_bf16 v[16:19], v[48:51], v[208:211], v[16:19]\n"
        "global_load_lds_dwordx4 v239, s[68:69] offset:1024\n"
        "v_accvgpr_write_b32 a3, v163\n"
        "v_mfma_f32_16x16x32_bf16 v[80:83], v[48:51], v[212:215], v[80:83]\n"
        "ds_read_b64_tr_b16 v[44:45], v232 offset:16384\n"
        "v_accvgpr_read_b32 v161, a209\n"
        "v_mfma_f32_16x16x32_bf16 v[112:115], v[48:51], v[216:219], v[112:115]\n"
        "ds_read_b64_tr_b16 v[46:47], v233 offset:16384\n"
        "v_accvgpr_read_b32 v162, a210\n"
        "v_mfma_f32_16x16x32_bf16 v[144:147], v[48:51], v[220:223], v[144:147]\n"
        "ds_read_b128 v[196:199], v246 offset:32768\n"
        "v_accvgpr_read_b32 v163, a211\n"
        "v_mfma_f32_16x16x32_bf16 v[20:23], v[52:55], v[208:211], v[20:23]\n"
        "global_load_lds_dwordx4 v240, s[68:69] offset:2048\n"
        "ds_read_b64_tr_b16 v[48:49], v226 offset:17408\n"
        "v_mfma_f32_16x16x32_bf16 v[84:87], v[52:55], v[212:215], v[84:87]\n"
        "ds_read_b64_tr_b16 v[50:51], v227 offset:17408\n"
        "v_accvgpr_read_b32 v164, a240\n"
        "v_mfma_f32_16x16x32_bf16 v[116:119], v[52:55], v[216:219], v[116:119]\n"
        "ds_read_b128 v[200:203], v246 offset:38912\n"
        "v_accvgpr_read_b32 v165, a241\n"
        "v_mfma_f32_16x16x32_bf16 v[148:151], v[52:55], v[220:223], v[148:151]\n"
        "ds_read_b64_tr_b16 v[52:53], v228 offset:17408\n"
        "v_accvgpr_read_b32 v166, a242\n"
        "v_mfma_f32_16x16x32_bf16 v[24:27], v[56:59], v[208:211], v[24:27]\n"
        "global_load_lds_dwordx4 v241, s[68:69] offset:3072\n"
        "ds_read_b64_tr_b16 v[54:55], v229 offset:17408\n"
        "s_add_u32 m0, s72, 0x10000\n"
        "v_mfma_f32_16x16x32_bf16 v[88:91], v[56:59], v[212:215], v[88:91]\n"
        "ds_read_b128 v[204:207], v246 offset:36864\n"
        "s_add_u32 s68, s68, 0x80000\n"
        "s_addc_u32 s69, s69, 0\n"
        "v_mfma_f32_16x16x32_bf16 v[120:123], v[56:59], v[216:219], v[120:123]\n"
        "s_waitcnt lgkmcnt(7)\n"
        "v_accvgpr_read_b32 v167, a243\n"
        "v_mfma_f32_16x16x32_bf16 v[152:155], v[56:59], v[220:223], v[152:155]\n"
        "ds_read_b64_tr_b16 v[56:57], v230 offset:17408\n"
        "s_waitcnt lgkmcnt(7)\n"
        "v_mfma_f32_16x16x32_bf16 v[28:31], v[60:63], v[208:211], v[28:31]\n"
        "global_load_lds_dwordx4 v242, s[64:65] offset:0\n"
        "ds_read_b64_tr_b16 v[58:59], v231 offset:17408\n"
        "v_mfma_f32_16x16x32_bf16 v[92:95], v[60:63], v[212:215], v[92:95]\n"
        "ds_read_b128 v[208:211], v247 offset:34816\n"
        "s_waitcnt lgkmcnt(7)\n"
        "v_mfma_f32_16x16x32_bf16 v[124:127], v[60:63], v[216:219], v[124:127]\n"
        "ds_read_b128 v[212:215], v247 offset:32768\n"
        "s_waitcnt lgkmcnt(7)\n"
        "v_mfma_f32_16x16x32_bf16 v[156:159], v[60:63], v[220:223], v[156:159]\n"
        "ds_read_b64_tr_b16 v[60:61], v232 offset:17408\n"
        "v_accvgpr_read_b32 v168, a104\n"
        "v_mfma_f32_16x16x32_bf16 v[0:3], v[32:35], v[192:195], v[0:3]\n"
        "global_load_lds_dwordx4 v243, s[64:65] offset:1024\n"
        "ds_read_b64_tr_b16 v[62:63], v233 offset:17408\n"
        "v_mfma_f32_16x16x32_bf16 v[64:67], v[32:35], v[196:199], v[64:67]\n"
        "s_waitcnt lgkmcnt(6)\n"
        "v_mul_f32 v168, s88, v168\n"
        "v_cvt_pk_bf16_f32 v168, v168, v169\n"
        "v_mfma_f32_16x16x32_bf16 v[96:99], v[32:35], v[200:203], v[96:99]\n"
        "ds_read_b128 v[216:219], v247 offset:38912\n"
        "v_cvt_pk_bf16_f32 v169, v170, v171\n"
        "v_mfma_f32_16x16x32_bf16 v[128:131], v[32:35], v[204:207], v[128:131]\n"
        "ds_read_b64_tr_b16 v[32:33], v226 offset:24576\n"
        "v_cvt_pk_bf16_f32 v170, v172, v173\n"
        "v_mfma_f32_16x16x32_bf16 v[4:7], v[36:39], v[192:195], v[4:7]\n"
        "global_load_lds_dwordx4 v244, s[64:65] offset:2048\n"
        "v_cvt_pk_bf16_f32 v171, v174, v175\n"
        "v_mfma_f32_16x16x32_bf16 v[68:71], v[36:39], v[196:199], v[68:71]\n"
        "ds_read_b64_tr_b16 v[34:35], v227 offset:24576\n"
        "v_accvgpr_write_b32 a4, v168\n"
        "v_mfma_f32_16x16x32_bf16 v[100:103], v[36:39], v[200:203], v[100:103]\n"
        "ds_read_b128 v[220:223], v247 offset:36864\n"
        "v_accvgpr_write_b32 a5, v169\n"
        "v_mfma_f32_16x16x32_bf16 v[132:135], v[36:39], v[204:207], v[132:135]\n"
        "ds_read_b64_tr_b16 v[36:37], v228 offset:24576\n"
        "v_accvgpr_write_b32 a6, v170\n"
        "v_mfma_f32_16x16x32_bf16 v[8:11], v[40:43], v[192:195], v[8:11]\n"
        "global_load_lds_dwordx4 v245, s[64:65] offset:3072\n"
        "s_add_u32 m0, s72, 0x11000\n"
        "v_accvgpr_write_b32 a7, v171\n"
        "v_mfma_f32_16x16x32_bf16 v[72:75], v[40:43], v[196:199], v[72:75]\n"
        "ds_read_b64_tr_b16 v[38:39], v229 offset:24576\n"
        "s_add_u32 s64, s64, 0x80\n"
        "s_addc_u32 s65, s65, 0\n"
        "v_mfma_f32_16x16x32_bf16 v[104:107], v[40:43], v[200:203], v[104:107]\n"
        "v_accvgpr_read_b32 v169, a213\n"
        "v_accvgpr_read_b32 v170, a214\n"
        "v_mfma_f32_16x16x32_bf16 v[136:139], v[40:43], v[204:207], v[136:139]\n"
        "ds_read_b64_tr_b16 v[40:41], v230 offset:24576\n"
        "v_accvgpr_read_b32 v171, a215\n"
        "v_mfma_f32_16x16x32_bf16 v[12:15], v[44:47], v[192:195], v[12:15]\n"
        "global_store_dwordx4 v254, a[204:207], s[70:71] offset:1024 sc1\n"
        "ds_read_b64_tr_b16 v[42:43], v231 offset:24576\n"
        "s_add_u32 s70, s38, s97\n"
        "s_addc_u32 s71, s39, 0\n"
        "s_add_u32 s97, s52, 0x2000\n"
        "v_accvgpr_read_b32 v172, a244\n"
        "v_mfma_f32_16x16x32_bf16 v[76:79], v[44:47], v[196:199], v[76:79]\n"
        "v_accvgpr_read_b32 v173, a245\n"
        "v_accvgpr_read_b32 v174, a246\n"
        "v_mfma_f32_16x16x32_bf16 v[108:111], v[44:47], v[200:203], v[108:111]\n"
        "v_accvgpr_read_b32 v175, a247\n"
        "v_mfma_f32_16x16x32_bf16 v[140:143], v[44:47], v[204:207], v[140:143]\n"
        "global_load_lds_dwordx4 v242, s[66:67] offset:0\n"
        "v_mfma_f32_16x16x32_bf16 v[16:19], v[48:51], v[192:195], v[16:19]\n"
        "ds_read_b64_tr_b16 v[44:45], v232 offset:24576\n"
        "v_mfma_f32_16x16x32_bf16 v[80:83], v[48:51], v[196:199], v[80:83]\n"
        "ds_read_b64_tr_b16 v[46:47], v233 offset:24576\n"
        "v_mfma_f32_16x16x32_bf16 v[112:115], v[48:51], v[200:203], v[112:115]\n"
        "s_waitcnt lgkmcnt(4)\n"
        "v_mfma_f32_16x16x32_bf16 v[144:147], v[48:51], v[204:207], v[144:147]\n"
        "global_load_lds_dwordx4 v243, s[66:67] offset:1024\n"
        "v_mfma_f32_16x16x32_bf16 v[20:23], v[52:55], v[192:195], v[20:23]\n"
        "ds_read_b64_tr_b16 v[48:49], v226 offset:25600\n"
        "v_mfma_f32_16x16x32_bf16 v[84:87], v[52:55], v[196:199], v[84:87]\n"
        "ds_read_b64_tr_b16 v[50:51], v227 offset:25600\n"
        "v_mfma_f32_16x16x32_bf16 v[116:119], v[52:55], v[200:203], v[116:119]\n"
        "v_mfma_f32_16x16x32_bf16 v[148:151], v[52:55], v[204:207], v[148:151]\n"
        "global_load_lds_dwordx4 v244, s[66:67] offset:2048\n"
        "v_mfma_f32_16x16x32_bf16 v[24:27], v[56:59], v[192:195], v[24:27]\n"
        "ds_read_b64_tr_b16 v[52:53], v228 offset:25600\n"
        "v_accvgpr_read_b32 v176, a128\n"
        "v_mfma_f32_16x16x32_bf16 v[88:91], v[56:59], v[196:199], v[88:91]\n"
        "ds_read_b64_tr_b16 v[54:55], v229 offset:25600\n"
        "v_mul_f32 v176, s88, v176\n"
        "v_mfma_f32_16x16x32_bf16 v[120:123], v[56:59], v[200:203], v[120:123]\n"
        "v_cvt_pk_bf16_f32 v176, v176, v177\n"
        "v_cvt_pk_bf16_f32 v177, v178, v179\n"
        "v_mfma_f32_16x16x32_bf16 v[152:155], v[56:59], v[204:207], v[152:155]\n"
        "global_load_lds_dwordx4 v245, s[66:67] offset:3072\n"
        "s_add_u32 m0, s73, 0x1c000\n"
        "v_mfma_f32_16x16x32_bf16 v[28:31], v[60:63], v[192:195], v[28:31]\n"
        "s_add_u32 s66, s66, 0x80\n"
        "s_addc_u32 s67, s67, 0\n"
        "v_cvt_pk_bf16_f32 v178, v180, v181\n"
        "v_mfma_f32_16x16x32_bf16 v[92:95], v[60:63], v[196:199], v[92:95]\n"
        "ds_read_b64_tr_b16 v[56:57], v230 offset:25600\n"
        "v_cvt_pk_bf16_f32 v179, v182, v183\n"
        "v_mfma_f32_16x16x32_bf16 v[124:127], v[60:63], v[200:203], v[124:127]\n"
        "ds_read_b64_tr_b16 v[58:59], v231 offset:25600\n"
        "v_accvgpr_write_b32 a8, v176\n"
        "v_mfma_f32_16x16x32_bf16 v[156:159], v[60:63], v[204:207], v[156:159]\n"
        "ds_read_b64_tr_b16 v[60:61], v232 offset:25600\n"
        "v_accvgpr_write_b32 a9, v177\n"
        "v_mfma_f32_16x16x32_bf16 v[0:3], v[32:35], v[208:211], v[0:3]\n"
        "ds_read_b64_tr_b16 v[62:63], v233 offset:25600\n"
        "v_accvgpr_write_b32 a10, v178\n"
        "v_mfma_f32_16x16x32_bf16 v[64:67], v[32:35], v[212:215], v[64:67]\n"
        "v_accvgpr_write_b32 a11, v179\n"
        "v_accvgpr_read_b32 v177, a217\n"
        "v_mfma_f32_16x16x32_bf16 v[96:99], v[32:35], v[216:219], v[96:99]\n"
        "s_waitcnt lgkmcnt(4)\n"
        "v_accvgpr_read_b32 v178, a218\n"
        "v_accvgpr_read_b32 v179, a219\n"
        "v_mfma_f32_16x16x32_bf16 v[128:131], v[32:35], v[220:223], v[128:131]\n"
        "v_accvgpr_read_b32 v180, a248\n"
        "v_accvgpr_read_b32 v181, a249\n"
        "v_mfma_f32_16x16x32_bf16 v[4:7], v[36:39], v[208:211], v[4:7]\n"
        "v_accvgpr_read_b32 v182, a250\n"
        "v_accvgpr_read_b32 v183, a251\n"
        "v_mfma_f32_16x16x32_bf16 v[68:71], v[36:39], v[212:215], v[68:71]\n"
        "v_mfma_f32_16x16x32_bf16 v[100:103], v[36:39], v[216:219], v[100:103]\n"
        "v_mfma_f32_16x16x32_bf16 v[132:135], v[36:39], v[220:223], v[132:135]\n"
        "v_mfma_f32_16x16x32_bf16 v[8:11], v[40:43], v[208:211], v[8:11]\n"
        "s_waitcnt vmcnt(13) lgkmcnt(0)\n"
        "s_barrier\n"
        "v_mfma_f32_16x16x32_bf16 v[72:75], v[40:43], v[212:215], v[72:75]\n"
        "global_load_lds_dwordx4 v238, s[68:69] offset:0\n"
        "ds_read_b64_tr_b16 v[32:33], v226 offset:32768\n"
        "v_mfma_f32_16x16x32_bf16 v[104:107], v[40:43], v[216:219], v[104:107]\n"
        "ds_read_b64_tr_b16 v[34:35], v227 offset:32768\n"
        "v_mfma_f32_16x16x32_bf16 v[136:139], v[40:43], v[220:223], v[136:139]\n"
        "ds_read_b64_tr_b16 v[36:37], v228 offset:32768\n"
        "v_mfma_f32_16x16x32_bf16 v[12:15], v[44:47], v[208:211], v[12:15]\n"
        "ds_read_b64_tr_b16 v[38:39], v229 offset:32768\n"
        "v_mfma_f32_16x16x32_bf16 v[76:79], v[44:47], v[212:215], v[76:79]\n"
        "ds_read_b64_tr_b16 v[40:41], v230 offset:32768\n"
        "global_load_lds_dwordx4 v239, s[68:69] offset:1024\n"
        "v_mfma_f32_16x16x32_bf16 v[108:111], v[44:47], v[216:219], v[108:111]\n"
        "ds_read_b64_tr_b16 v[42:43], v231 offset:32768\n"
        "v_mfma_f32_16x16x32_bf16 v[140:143], v[44:47], v[220:223], v[140:143]\n"
        "ds_read_b128 v[192:195], v246 offset:2048\n"
        "v_accvgpr_read_b32 v184, a136\n"
        "v_mfma_f32_16x16x32_bf16 v[16:19], v[48:51], v[208:211], v[16:19]\n"
        "ds_read_b64_tr_b16 v[44:45], v232 offset:32768\n"
        "v_mul_f32 v184, s88, v184\n"
        "v_mfma_f32_16x16x32_bf16 v[80:83], v[48:51], v[212:215], v[80:83]\n"
        "ds_read_b64_tr_b16 v[46:47], v233 offset:32768\n"
        "global_load_lds_dwordx4 v240, s[68:69] offset:2048\n"
        "v_mfma_f32_16x16x32_bf16 v[112:115], v[48:51], v[216:219], v[112:115]\n"
        "ds_read_b128 v[196:199], v246 offset:0\n"
        "v_cvt_pk_bf16_f32 v184, v184, v185\n"
        "v_mfma_f32_16x16x32_bf16 v[144:147], v[48:51], v[220:223], v[144:147]\n"
        "ds_read_b64_tr_b16 v[48:49], v226 offset:33792\n"
        "v_cvt_pk_bf16_f32 v185, v186, v187\n"
        "v_mfma_f32_16x16x32_bf16 v[20:23], v[52:55], v[208:211], v[20:23]\n"
        "ds_read_b64_tr_b16 v[50:51], v227 offset:33792\n"
        "v_cvt_pk_bf16_f32 v186, v188, v189\n"
        "v_mfma_f32_16x16x32_bf16 v[84:87], v[52:55], v[212:215], v[84:87]\n"
        "ds_read_b128 v[200:203], v246 offset:6144\n"
        "v_cvt_pk_bf16_f32 v187, v190, v191\n"
        "v_mfma_f32_16x16x32_bf16 v[116:119], v[52:55], v[216:219], v[116:119]\n"
        "global_store_dwordx4 v254, a[160:163], s[70:71] offset:0 sc1\n"
        "ds_read_b128 v[204:207], v246 offset:4096\n"
        "v_accvgpr_write_b32 a12, v184\n"
        "v_accvgpr_write_b32 a13, v185\n"
        "v_accvgpr_write_b32 a14, v186\n"
        "v_mfma_f32_16x16x32_bf16 v[148:151], v[52:55], v[220:223], v[148:151]\n"
        "ds_read_b64_tr_b16 v[52:53], v228 offset:33792\n"
        "v_accvgpr_write_b32 a15, v187\n"
        "v_mfma_f32_16x16x32_bf16 v[24:27], v[56:59], v[208:211], v[24:27]\n"
        "ds_read_b64_tr_b16 v[54:55], v229 offset:33792\n"
        "v_accvgpr_read_b32 v185, a221\n"
        "v_mfma_f32_16x16x32_bf16 v[88:91], v[56:59], v[212:215], v[88:91]\n"
        "global_load_lds_dwordx4 v241, s[68:69] offset:3072\n"
        "s_add_u32 m0, s72, 0x8000\n"
        "v_mfma_f32_16x16x32_bf16 v[120:123], v[56:59], v[216:219], v[120:123]\n"
        "s_add_u32 s68, s68, 0x80000\n"
        "s_addc_u32 s69, s69, 0\n"
        "v_accvgpr_read_b32 v186, a222\n"
        "v_mfma_f32_16x16x32_bf16 v[152:155], v[56:59], v[220:223], v[152:155]\n"
        "ds_read_b64_tr_b16 v[56:57], v230 offset:33792\n"
        "v_accvgpr_read_b32 v187, a223\n"
        "v_mfma_f32_16x16x32_bf16 v[28:31], v[60:63], v[208:211], v[28:31]\n"
        "ds_read_b64_tr_b16 v[58:59], v231 offset:33792\n"
        "v_accvgpr_read_b32 v188, a252\n"
        "v_mfma_f32_16x16x32_bf16 v[92:95], v[60:63], v[212:215], v[92:95]\n"
        "global_load_lds_dwordx4 v242, s[64:65] offset:0\n"
        "v_mfma_f32_16x16x32_bf16 v[124:127], v[60:63], v[216:219], v[124:127]\n"
        "ds_read_b128 v[208:211], v247 offset:2048\n"
        "v_accvgpr_read_b32 v189, a253\n"
        "v_mfma_f32_16x16x32_bf16 v[156:159], v[60:63], v[220:223], v[156:159]\n"
        "ds_read_b64_tr_b16 v[60:61], v232 offset:33792\n"
        "s_waitcnt lgkmcnt(5)\n"
        "v_mfma_f32_16x16x32_bf16 v[0:3], v[32:35], v[192:195], v[0:3]\n"
        "ds_read_b64_tr_b16 v[62:63], v233 offset:33792\n"
        "v_accvgpr_read_b32 v190, a254\n"
        "v_mfma_f32_16x16x32_bf16 v[64:67], v[32:35], v[196:199], v[64:67]\n"
        "global_load_lds_dwordx4 v243, s[64:65] offset:1024\n"
        "v_mfma_f32_16x16x32_bf16 v[96:99], v[32:35], v[200:203], v[96:99]\n"
        "ds_read_b128 v[212:215], v247 offset:0\n"
        "v_accvgpr_read_b32 v191, a255\n"
        "v_mfma_f32_16x16x32_bf16 v[128:131], v[32:35], v[204:207], v[128:131]\n"
        "ds_read_b64_tr_b16 v[32:33], v226 offset:40960\n"
        "v_mfma_f32_16x16x32_bf16 v[4:7], v[36:39], v[192:195], v[4:7]\n"
        "ds_read_b64_tr_b16 v[34:35], v227 offset:40960\n"
        "v_mfma_f32_16x16x32_bf16 v[68:71], v[36:39], v[196:199], v[68:71]\n"
        "global_load_lds_dwordx4 v244, s[64:65] offset:2048\n"
        "v_mfma_f32_16x16x32_bf16 v[100:103], v[36:39], v[200:203], v[100:103]\n"
        "ds_read_b128 v[216:219], v247 offset:6144\n"
        "v_mfma_f32_16x16x32_bf16 v[132:135], v[36:39], v[204:207], v[132:135]\n"
        "ds_read_b64_tr_b16 v[36:37], v228 offset:40960\n"
        "v_accvgpr_read_b32 v160, a208\n"
        "v_mfma_f32_16x16x32_bf16 v[8:11], v[40:43], v[192:195], v[8:11]\n"
        "ds_read_b64_tr_b16 v[38:39], v229 offset:40960\n"
        "v_cvt_pk_bf16_f32 v160, v160, v161\n"
        "v_mfma_f32_16x16x32_bf16 v[72:75], v[40:43], v[196:199], v[72:75]\n"
        "global_load_lds_dwordx4 v245, s[64:65] offset:3072\n"
        "s_add_u32 m0, s72, 0x9000\n"
        "v_mfma_f32_16x16x32_bf16 v[104:107], v[40:43], v[200:203], v[104:107]\n"
        "ds_read_b128 v[220:223], v247 offset:4096\n"
        "v_cvt_pk_bf16_f32 v161, v162, v163\n"
        "v_mfma_f32_16x16x32_bf16 v[136:139], v[40:43], v[204:207], v[136:139]\n"
        "ds_read_b64_tr_b16 v[40:41], v230 offset:40960\n"
        "v_cvt_pk_bf16_f32 v162, v164, v165\n"
        "v_mfma_f32_16x16x32_bf16 v[12:15], v[44:47], v[192:195], v[12:15]\n"
        "ds_read_b64_tr_b16 v[42:43], v231 offset:40960\n"
        "v_cvt_pk_bf16_f32 v163, v166, v167\n"
        "v_mfma_f32_16x16x32_bf16 v[76:79], v[44:47], v[196:199], v[76:79]\n"
        "s_add_u32 s64, s64, 0x80\n"
        "s_addc_u32 s65, s65, 0\n"
        "v_accvgpr_write_b32 a208, v160\n"
        "v_mfma_f32_16x16x32_bf16 v[108:111], v[44:47], v[200:203], v[108:111]\n"
        "global_load_lds_dwordx4 v242, s[66:67] offset:0\n"
        "v_accvgpr_write_b32 a209, v161\n"
        "v_mfma_f32_16x16x32_bf16 v[140:143], v[44:47], v[204:207], v[140:143]\n"
        "s_waitcnt lgkmcnt(4)\n"
        "v_accvgpr_write_b32 a210, v162\n"
        "v_accvgpr_write_b32 a211, v163\n"
        "v_mfma_f32_16x16x32_bf16 v[16:19], v[48:51], v[192:195], v[16:19]\n"
        "ds_read_b64_tr_b16 v[44:45], v232 offset:40960\n"
        "v_accvgpr_read_b32 v161, a145\n"
        "v_mfma_f32_16x16x32_bf16 v[80:83], v[48:51], v[196:199], v[80:83]\n"
        "ds_read_b64_tr_b16 v[46:47], v233 offset:40960\n"
        "v_accvgpr_read_b32 v162, a146\n"
        "v_mfma_f32_16x16x32_bf16 v[112:115], v[48:51], v[200:203], v[112:115]\n"
        "global_load_lds_dwordx4 v243, s[66:67] offset:1024\n"
        "v_accvgpr_read_b32 v163, a147\n"
        "v_mfma_f32_16x16x32_bf16 v[144:147], v[48:51], v[204:207], v[144:147]\n"
        "ds_read_b64_tr_b16 v[48:49], v226 offset:41984\n"
        "v_accvgpr_read_b32 v164, a176\n"
        "v_mfma_f32_16x16x32_bf16 v[20:23], v[52:55], v[192:195], v[20:23]\n"
        "ds_read_b64_tr_b16 v[50:51], v227 offset:41984\n"
        "v_accvgpr_read_b32 v165, a177\n"
        "v_mfma_f32_16x16x32_bf16 v[84:87], v[52:55], v[196:199], v[84:87]\n"
        "v_accvgpr_read_b32 v166, a178\n"
        "v_accvgpr_read_b32 v167, a179\n"
        "v_mfma_f32_16x16x32_bf16 v[116:119], v[52:55], v[200:203], v[116:119]\n"
        "global_load_lds_dwordx4 v244, s[66:67] offset:2048\n"
        "v_mfma_f32_16x16x32_bf16 v[148:151], v[52:55], v[204:207], v[148:151]\n"
        "ds_read_b64_tr_b16 v[52:53], v228 offset:41984\n"
        "v_mfma_f32_16x16x32_bf16 v[24:27], v[56:59], v[192:195], v[24:27]\n"
        "ds_read_b64_tr_b16 v[54:55], v229 offset:41984\n"
        "v_mfma_f32_16x16x32_bf16 v[88:91], v[56:59], v[196:199], v[88:91]\n"
        "v_mfma_f32_16x16x32_bf16 v[120:123], v[56:59], v[200:203], v[120:123]\n"
        "global_load_lds_dwordx4 v245, s[66:67] offset:3072\n"
        "s_add_u32 s66, s66, 0x80\n"
        "s_addc_u32 s67, s67, 0\n"
        "v_mfma_f32_16x16x32_bf16 v[152:155], v[56:59], v[204:207], v[152:155]\n"
        "ds_read_b64_tr_b16 v[56:57], v230 offset:41984\n"
        "s_add_u32 m0, s73, 0x20000\n"
        "v_mfma_f32_16x16x32_bf16 v[28:31], v[60:63], v[192:195], v[28:31]\n"
        "ds_read_b64_tr_b16 v[58:59], v231 offset:41984\n"
        "v_mfma_f32_16x16x32_bf16 v[92:95], v[60:63], v[196:199], v[92:95]\n"
        "s_waitcnt lgkmcnt(4)\n"
        "v_mfma_f32_16x16x32_bf16 v[124:127], v[60:63], v[200:203], v[124:127]\n"
        "global_store_dwordx4 v254, a[164:167], s[70:71] offset:1024 sc1\n"
        "s_add_u32 s70, s38, s97\n"
        "s_addc_u32 s71, s39, 0\n"
        "s_add_u32 s97, s52, 0x3000\n"
        "v_mfma_f32_16x16x32_bf16 v[156:159], v[60:63], v[204:207], v[156:159]\n"
        "ds_read_b64_tr_b16 v[60:61], v232 offset:41984\n"
        "v_accvgpr_read_b32 v168, a212\n"
        "v_mfma_f32_16x16x32_bf16 v[0:3], v[32:35], v[208:211], v[0:3]\n"
        "ds_read_b64_tr_b16 v[62:63], v233 offset:41984\n"
        "v_cvt_pk_bf16_f32 v168, v168, v169\n"
        "v_mfma_f32_16x16x32_bf16 v[64:67], v[32:35], v[212:215], v[64:67]\n"
        "v_cvt_pk_bf16_f32 v169, v170, v171\n"
        "v_cvt_pk_bf16_f32 v170, v172, v173\n"
        "v_mfma_f32_16x16x32_bf16 v[96:99], v[32:35], v[216:219], v[96:99]\n"
        "v_cvt_pk_bf16_f32 v171, v174, v175\n"
        "v_accvgpr_write_b32 a240, v168\n"
        "v_mfma_f32_16x16x32_bf16 v[128:131], v[32:35], v[220:223], v[128:131]\n"
        "v_accvgpr_write_b32 a241, v169\n"
        "v_accvgpr_write_b32 a242, v170\n"
        "v_mfma_f32_16x16x32_bf16 v[4:7], v[36:39], v[208:211], v[4:7]\n"
        "v_accvgpr_write_b32 a243, v171\n"
        "v_accvgpr_read_b32 v169, a149\n"
        "v_mfma_f32_16x16x32_bf16 v[68:71], v[36:39], v[212:215], v[68:71]\n"
        "v_accvgpr_read_b32 v170, a150\n"
        "v_accvgpr_read_b32 v171, a151\n"
        "v_mfma_f32_16x16x32_bf16 v[100:103], v[36:39], v[216:219], v[100:103]\n"
        "v_accvgpr_read_b32 v172, a180\n"
        "v_accvgpr_read_b32 v173, a181\n"
        "v_mfma_f32_16x16x32_bf16 v[132:135], v[36:39], v[220:223], v[132:135]\n"
        "v_accvgpr_read_b32 v174, a182\n"
        "v_accvgpr_read_b32 v175, a183\n"
        "v_mfma_f32_16x16x32_bf16 v[8:11], v[40:43], v[208:211], v[8:11]\n"
        "s_waitcnt vmcnt(14) lgkmcnt(0)\n"
        "s_barrier\n"
        "v_mfma_f32_16x16x32_bf16 v[72:75], v[40:43], v[212:215], v[72:75]\n"
        "global_load_lds_dwordx4 v238, s[68:69] offset:0\n"
        "ds_read_b64_tr_b16 v[32:33], v226 offset:49152\n"
        "v_mfma_f32_16x16x32_bf16 v[104:107], v[40:43], v[216:219], v[104:107]\n"
        "ds_read_b64_tr_b16 v[34:35], v227 offset:49152\n"
        "v_mfma_f32_16x16x32_bf16 v[136:139], v[40:43], v[220:223], v[136:139]\n"
        "ds_read_b64_tr_b16 v[36:37], v228 offset:49152\n"
        "v_mfma_f32_16x16x32_bf16 v[12:15], v[44:47], v[208:211], v[12:15]\n"
        "ds_read_b64_tr_b16 v[38:39], v229 offset:49152\n"
        "v_mfma_f32_16x16x32_bf16 v[76:79], v[44:47], v[212:215], v[76:79]\n"
        "ds_read_b64_tr_b16 v[40:41], v230 offset:49152\n"
        "global_load_lds_dwordx4 v239, s[68:69] offset:1024\n"
        "v_mfma_f32_16x16x32_bf16 v[108:111], v[44:47], v[216:219], v[108:111]\n"
        "ds_read_b64_tr_b16 v[42:43], v231 offset:49152\n"
        "v_mfma_f32_16x16x32_bf16 v[140:143], v[44:47], v[220:223], v[140:143]\n"
        "ds_read_b128 v[192:195], v248 offset:2048\n"
        "v_mfma_f32_16x16x32_bf16 v[16:19], v[48:51], v[208:211], v[16:19]\n"
        "ds_read_b64_tr_b16 v[44:45], v232 offset:49152\n"
        "v_mfma_f32_16x16x32_bf16 v[80:83], v[48:51], v[212:215], v[80:83]\n"
        "ds_read_b64_tr_b16 v[46:47], v233 offset:49152\n"
        "global_load_lds_dwordx4 v240, s[68:69] offset:2048\n"
        "v_mfma_f32_16x16x32_bf16 v[112:115], v[48:51], v[216:219], v[112:115]\n"
        "ds_read_b128 v[196:199], v248 offset:0\n"
        "v_mfma_f32_16x16x32_bf16 v[144:147], v[48:51], v[220:223], v[144:147]\n"
        "ds_read_b64_tr_b16 v[48:49], v226 offset:50176\n"
        "v_mfma_f32_16x16x32_bf16 v[20:23], v[52:55], v[208:211], v[20:23]\n"
        "ds_read_b64_tr_b16 v[50:51], v227 offset:50176\n"
        "v_mfma_f32_16x16x32_bf16 v[84:87], v[52:55], v[212:215], v[84:87]\n"
        "ds_read_b128 v[200:203], v248 offset:6144\n"
        "global_load_lds_dwordx4 v241, s[68:69] offset:3072\n"
        "s_add_u32 m0, s72, 0x0\n"
        "v_mfma_f32_16x16x32_bf16 v[116:119], v[52:55], v[216:219], v[116:119]\n"
        "ds_read_b128 v[204:207], v248 offset:4096\n"
        "s_add_u32 s68, s68, 0x80000\n"
        "s_addc_u32 s69, s69, 0\n"
        "v_mfma_f32_16x16x32_bf16 v[148:151], v[52:55], v[220:223], v[148:151]\n"
        "ds_read_b64_tr_b16 v[52:53], v228 offset:50176\n"
        "v_accvgpr_read_b32 v176, a216\n"
        "v_mfma_f32_16x16x32_bf16 v[24:27], v[56:59], v[208:211], v[24:27]\n"
        "ds_read_b64_tr_b16 v[54:55], v229 offset:50176\n"
        "v_cvt_pk_bf16_f32 v176, v176, v177\n"
        "v_mfma_f32_16x16x32_bf16 v[88:91], v[56:59], v[212:215], v[88:91]\n"
        "global_load_lds_dwordx4 v242, s[64:65] offset:0\n"
        "v_mfma_f32_16x16x32_bf16 v[120:123], v[56:59], v[216:219], v[120:123]\n"
        "s_waitcnt lgkmcnt(6)\n"
        "v_cvt_pk_bf16_f32 v177, v178, v179\n"
        "v_cvt_pk_bf16_f32 v178, v180, v181\n"
        "v_mfma_f32_16x16x32_bf16 v[152:155], v[56:59], v[220:223], v[152:155]\n"
        "ds_read_b64_tr_b16 v[56:57], v230 offset:50176\n"
        "v_cvt_pk_bf16_f32 v179, v182, v183\n"
        "v_mfma_f32_16x16x32_bf16 v[28:31], v[60:63], v[208:211], v[28:31]\n"
        "s_waitcnt lgkmcnt(5)\n"
        "v_accvgpr_write_b32 a212, v176\n"
        "v_accvgpr_write_b32 a213, v177\n"
        "v_mfma_f32_16x16x32_bf16 v[92:95], v[60:63], v[212:215], v[92:95]\n"
        "s_waitcnt lgkmcnt(4)\n"
        "v_accvgpr_write_b32 a214, v178\n"
        "v_accvgpr_write_b32 a215, v179\n"
        "v_mfma_f32_16x16x32_bf16 v[124:127], v[60:63], v[216:219], v[124:127]\n"
        "ds_read_b64_tr_b16 v[58:59], v231 offset:50176\n"
        "global_load_lds_dwordx4 v243, s[64:65] offset:1024\n"
        "v_mfma_f32_16x16x32_bf16 v[156:159], v[60:63], v[220:223], v[156:159]\n"
        "ds_read_b64_tr_b16 v[60:61], v232 offset:50176\n"
        "v_accvgpr_read_b32 v177, a153\n"
        "v_mfma_f32_16x16x32_bf16 v[0:3], v[32:35], v[192:195], v[0:3]\n"
        "ds_read_b64_tr_b16 v[62:63], v233 offset:50176\n"
        "v_accvgpr_read_b32 v178, a154\n"
        "v_mfma_f32_16x16x32_bf16 v[64:67], v[32:35], v[196:199], v[64:67]\n"
        "ds_read_b128 v[208:211], v249 offset:2048\n"
        "v_accvgpr_read_b32 v179, a155\n"
        "v_mfma_f32_16x16x32_bf16 v[96:99], v[32:35], v[200:203], v[96:99]\n"
        "s_waitcnt lgkmcnt(5)\n"
        "global_load_lds_dwordx4 v244, s[64:65] offset:2048\n"
        "v_mfma_f32_16x16x32_bf16 v[128:131], v[32:35], v[204:207], v[128:131]\n"
        "ds_read_b64_tr_b16 v[32:33], v226 offset:57344\n"
        "v_accvgpr_read_b32 v180, a184\n"
        "v_mfma_f32_16x16x32_bf16 v[4:7], v[36:39], v[192:195], v[4:7]\n"
        "ds_read_b64_tr_b16 v[34:35], v227 offset:57344\n"
        "v_accvgpr_read_b32 v181, a185\n"
        "v_mfma_f32_16x16x32_bf16 v[68:71], v[36:39], v[196:199], v[68:71]\n"
        "ds_read_b128 v[212:215], v249 offset:0\n"
        "v_accvgpr_read_b32 v182, a186\n"
        "v_mfma_f32_16x16x32_bf16 v[100:103], v[36:39], v[200:203], v[100:103]\n"
        "global_load_lds_dwordx4 v245, s[64:65] offset:3072\n"
        "s_add_u32 m0, s72, 0x1000\n"
        "v_mfma_f32_16x16x32_bf16 v[132:135], v[36:39], v[204:207], v[132:135]\n"
        "ds_read_b64_tr_b16 v[36:37], v228 offset:57344\n"
        "s_add_u32 s64, s64, 0x80\n"
        "s_addc_u32 s65, s65, 0\n"
        "v_mfma_f32_16x16x32_bf16 v[8:11], v[40:43], v[192:195], v[8:11]\n"
        "ds_read_b64_tr_b16 v[38:39], v229 offset:57344\n"
        "v_accvgpr_read_b32 v183, a187\n"
        "v_mfma_f32_16x16x32_bf16 v[72:75], v[40:43], v[196:199], v[72:75]\n"
        "ds_read_b128 v[216:219], v249 offset:6144\n"
        "v_mfma_f32_16x16x32_bf16 v[104:107], v[40:43], v[200:203], v[104:107]\n"
        "global_load_lds_dwordx4 v242, s[66:67] offset:0\n"
        "v_mfma_f32_16x16x32_bf16 v[136:139], v[40:43], v[204:207], v[136:139]\n"
        "ds_read_b64_tr_b16 v[40:41], v230 offset:57344\n"
        "v_mfma_f32_16x16x32_bf16 v[12:15], v[44:47], v[192:195], v[12:15]\n"
        "ds_read_b64_tr_b16 v[42:43], v231 offset:57344\n"
        "v_mfma_f32_16x16x32_bf16 v[76:79], v[44:47], v[196:199], v[76:79]\n"
        "ds_read_b128 v[220:223], v249 offset:4096\n"
        "v_mfma_f32_16x16x32_bf16 v[108:111], v[44:47], v[200:203], v[108:111]\n"
        "global_load_lds_dwordx4 v243, s[66:67] offset:1024\n"
        "v_mfma_f32_16x16x32_bf16 v[140:143], v[44:47], v[204:207], v[140:143]\n"
        "ds_read_b64_tr_b16 v[44:45], v232 offset:57344\n"
        "v_accvgpr_read_b32 v184, a220\n"
        "v_mfma_f32_16x16x32_bf16 v[16:19], v[48:51], v[192:195], v[16:19]\n"
        "ds_read_b64_tr_b16 v[46:47], v233 offset:57344\n"
        "v_cvt_pk_bf16_f32 v184, v184, v185\n"
        "v_mfma_f32_16x16x32_bf16 v[80:83], v[48:51], v[196:199], v[80:83]\n"
        "v_cvt_pk_bf16_f32 v185, v186, v187\n"
        "v_cvt_pk_bf16_f32 v186, v188, v189\n"
        "v_mfma_f32_16x16x32_bf16 v[112:115], v[48:51], v[200:203], v[112:115]\n"
        "s_waitcnt lgkmcnt(5)\n"
        "v_cvt_pk_bf16_f32 v187, v190, v191\n"
        "v_accvgpr_write_b32 a244, v184\n"
        "v_mfma_f32_16x16x32_bf16 v[144:147], v[48:51], v[204:207], v[144:147]\n"
        "global_store_dwordx4 v254, a[168:171], s[70:71] offset:0 sc1\n"
        "ds_read_b64_tr_b16 v[48:49], v226 offset:58368\n"
        "ds_read_b64_tr_b16 v[50:51], v227 offset:58368\n"
        "v_accvgpr_write_b32 a245, v185\n"
        "v_mfma_f32_16x16x32_bf16 v[20:23], v[52:55], v[192:195], v[20:23]\n"
        "v_accvgpr_write_b32 a246, v186\n"
        "v_accvgpr_write_b32 a247, v187\n"
        "v_mfma_f32_16x16x32_bf16 v[84:87], v[52:55], v[196:199], v[84:87]\n"
        "v_accvgpr_read_b32 v185, a157\n"
        "v_accvgpr_read_b32 v186, a158\n"
        "v_mfma_f32_16x16x32_bf16 v[116:119], v[52:55], v[200:203], v[116:119]\n"
        "global_load_lds_dwordx4 v244, s[66:67] offset:2048\n"
        "v_mfma_f32_16x16x32_bf16 v[148:151], v[52:55], v[204:207], v[148:151]\n"
        "ds_read_b64_tr_b16 v[52:53], v228 offset:58368\n"
        "v_accvgpr_read_b32 v187, a159\n"
        "v_mfma_f32_16x16x32_bf16 v[24:27], v[56:59], v[192:195], v[24:27]\n"
        "ds_read_b64_tr_b16 v[54:55], v229 offset:58368\n"
        "v_accvgpr_read_b32 v188, a188\n"
        "v_mfma_f32_16x16x32_bf16 v[88:91], v[56:59], v[196:199], v[88:91]\n"
        "v_accvgpr_read_b32 v189, a189\n"
        "v_accvgpr_read_b32 v190, a190\n"
        "v_mfma_f32_16x16x32_bf16 v[120:123], v[56:59], v[200:203], v[120:123]\n"
        "global_load_lds_dwordx4 v245, s[66:67] offset:3072\n"
        "s_add_u32 m0, s73, 0x24000\n"
        "v_mfma_f32_16x16x32_bf16 v[152:155], v[56:59], v[204:207], v[152:155]\n"
        "s_add_u32 s66, s66, 0x80\n"
        "s_addc_u32 s67, s67, 0\n"
        "v_accvgpr_read_b32 v191, a191\n"
        "v_mfma_f32_16x16x32_bf16 v[28:31], v[60:63], v[192:195], v[28:31]\n"
        "ds_read_b64_tr_b16 v[56:57], v230 offset:58368\n"
        "v_mfma_f32_16x16x32_bf16 v[92:95], v[60:63], v[196:199], v[92:95]\n"
        "ds_read_b64_tr_b16 v[58:59], v231 offset:58368\n"
        "s_waitcnt lgkmcnt(4)\n"
        "v_mfma_f32_16x16x32_bf16 v[124:127], v[60:63], v[200:203], v[124:127]\n"
        "v_mfma_f32_16x16x32_bf16 v[156:159], v[60:63], v[204:207], v[156:159]\n"
        "ds_read_b64_tr_b16 v[60:61], v232 offset:58368\n"
        "v_mfma_f32_16x16x32_bf16 v[0:3], v[32:35], v[208:211], v[0:3]\n"
        "ds_read_b64_tr_b16 v[62:63], v233 offset:58368\n"
        "v_mfma_f32_16x16x32_bf16 v[64:67], v[32:35], v[212:215], v[64:67]\n"
        "v_mfma_f32_16x16x32_bf16 v[96:99], v[32:35], v[216:219], v[96:99]\n"
        "v_mfma_f32_16x16x32_bf16 v[128:131], v[32:35], v[220:223], v[128:131]\n"
        "v_mfma_f32_16x16x32_bf16 v[4:7], v[36:39], v[208:211], v[4:7]\n"
        "v_mfma_f32_16x16x32_bf16 v[68:71], v[36:39], v[212:215], v[68:71]\n"
        "v_mfma_f32_16x16x32_bf16 v[100:103], v[36:39], v[216:219], v[100:103]\n"
        "v_mfma_f32_16x16x32_bf16 v[132:135], v[36:39], v[220:223], v[132:135]\n"
        "v_accvgpr_read_b32 v160, a144\n"
        "v_cvt_pk_bf16_f32 v160, v160, v161\n"
        "v_mfma_f32_16x16x32_bf16 v[8:11], v[40:43], v[208:211], v[8:11]\n"
        "s_waitcnt vmcnt(14) lgkmcnt(0)\n"
        "s_barrier\n"
        "v_cvt_pk_bf16_f32 v161, v162, v163\n"
        "v_mfma_f32_16x16x32_bf16 v[72:75], v[40:43], v[212:215], v[72:75]\n"
        "global_load_lds_dwordx4 v238, s[68:69] offset:0\n"
        "ds_read_b64_tr_b16 v[32:33], v226 offset:16384\n"
        "v_mfma_f32_16x16x32_bf16 v[104:107], v[40:43], v[216:219], v[104:107]\n"
        "ds_read_b64_tr_b16 v[34:35], v227 offset:16384\n"
        "v_cvt_pk_bf16_f32 v162, v164, v165\n"
        "v_mfma_f32_16x16x32_bf16 v[136:139], v[40:43], v[220:223], v[136:139]\n"
        "ds_read_b64_tr_b16 v[36:37], v228 offset:16384\n"
        "v_cvt_pk_bf16_f32 v163, v166, v167\n"
        "v_mfma_f32_16x16x32_bf16 v[12:15], v[44:47], v[208:211], v[12:15]\n"
        "ds_read_b64_tr_b16 v[38:39], v229 offset:16384\n"
        "v_accvgpr_write_b32 a216, v160\n"
        "v_mfma_f32_16x16x32_bf16 v[76:79], v[44:47], v[212:215], v[76:79]\n"
        "ds_read_b64_tr_b16 v[40:41], v230 offset:16384\n"
        "v_accvgpr_write_b32 a217, v161\n"
        "v_mfma_f32_16x16x32_bf16 v[108:111], v[44:47], v[216:219], v[108:111]\n"
        "ds_read_b64_tr_b16 v[42:43], v231 offset:16384\n"
        "v_accvgpr_write_b32 a218, v162\n"
        "v_mfma_f32_16x16x32_bf16 v[140:143], v[44:47], v[220:223], v[140:143]\n"
        "ds_read_b128 v[192:195], v246 offset:34816\n"
        "v_accvgpr_write_b32 a219, v163\n"
        "v_mfma_f32_16x16x32_bf16 v[16:19], v[48:51], v[208:211], v[16:19]\n"
        "global_load_lds_dwordx4 v239, s[68:69] offset:1024\n"
        "ds_read_b64_tr_b16 v[44:45], v232 offset:16384\n"
        "v_mfma_f32_16x16x32_bf16 v[80:83], v[48:51], v[212:215], v[80:83]\n"
        "ds_read_b64_tr_b16 v[46:47], v233 offset:16384\n"
        "v_accvgpr_read_b32 v161, a17\n"
        "v_mfma_f32_16x16x32_bf16 v[112:115], v[48:51], v[216:219], v[112:115]\n"
        "ds_read_b128 v[196:199], v246 offset:32768\n"
        "v_accvgpr_read_b32 v162, a18\n"
        "v_mfma_f32_16x16x32_bf16 v[144:147], v[48:51], v[220:223], v[144:147]\n"
        "ds_read_b64_tr_b16 v[48:49], v226 offset:17408\n"
        "v_accvgpr_read_b32 v163, a19\n"
        "v_mfma_f32_16x16x32_bf16 v[20:23], v[52:55], v[208:211], v[20:23]\n"
        "global_store_dwordx4 v254, a[172:175], s[70:71] offset:1024 sc1\n"
        "ds_read_b64_tr_b16 v[50:51], v227 offset:17408\n"
        "ds_read_b128 v[200:203], v246 offset:38912\n"
        "s_add_u32 s70, s38, s97\n"
        "s_addc_u32 s71, s39, 0\n"
        "v_mfma_f32_16x16x32_bf16 v[84:87], v[52:55], v[212:215], v[84:87]\n"
        "ds_read_b128 v[204:207], v246 offset:36864\n"
        "s_add_u32 s97, s52, 0x4000\n"
        "v_accvgpr_read_b32 v164, a48\n"
        "v_mfma_f32_16x16x32_bf16 v[116:119], v[52:55], v[216:219], v[116:119]\n"
        "s_mul_i32 s52, s40, 4224\n"
        "v_accvgpr_read_b32 v165, a49\n"
        "v_accvgpr_read_b32 v166, a50\n"
        "v_mfma_f32_16x16x32_bf16 v[148:151], v[52:55], v[220:223], v[148:151]\n"
        "global_load_lds_dwordx4 v240, s[68:69] offset:2048\n"
        "s_add_u32 s52, s52, 0x20240\n"
        "v_mfma_f32_16x16x32_bf16 v[24:27], v[56:59], v[208:211], v[24:27]\n"
        "ds_read_b64_tr_b16 v[52:53], v228 offset:17408\n"
        "v_accvgpr_read_b32 v167, a51\n"
        "v_mfma_f32_16x16x32_bf16 v[88:91], v[56:59], v[212:215], v[88:91]\n"
        "ds_read_b64_tr_b16 v[54:55], v229 offset:17408\n"
        "v_mfma_f32_16x16x32_bf16 v[120:123], v[56:59], v[216:219], v[120:123]\n"
        "s_waitcnt lgkmcnt(6)\n"
        "v_mfma_f32_16x16x32_bf16 v[152:155], v[56:59], v[220:223], v[152:155]\n"
        "global_load_lds_dwordx4 v241, s[68:69] offset:3072\n"
        "s_add_u32 m0, s72, 0x10000\n"
        "v_mfma_f32_16x16x32_bf16 v[28:31], v[60:63], v[208:211], v[28:31]\n"
        "s_add_u32 s68, s68, 0x80000\n"
        "s_addc_u32 s69, s69, 0\n"
        "s_waitcnt lgkmcnt(3)\n"
        "v_mfma_f32_16x16x32_bf16 v[92:95], v[60:63], v[212:215], v[92:95]\n"
        "ds_read_b64_tr_b16 v[56:57], v230 offset:17408\n"
        "s_waitcnt lgkmcnt(3)\n"
        "v_mfma_f32_16x16x32_bf16 v[124:127], v[60:63], v[216:219], v[124:127]\n"
        "ds_read_b64_tr_b16 v[58:59], v231 offset:17408\n"
        "v_mfma_f32_16x16x32_bf16 v[156:159], v[60:63], v[220:223], v[156:159]\n"
        "global_load_lds_dwordx4 v242, s[64:65] offset:0\n"
        "v_mfma_f32_16x16x32_bf16 v[0:3], v[32:35], v[192:195], v[0:3]\n"
        "ds_read_b64_tr_b16 v[60:61], v232 offset:17408\n"
        "v_accvgpr_read_b32 v168, a148\n"
        "v_mfma_f32_16x16x32_bf16 v[64:67], v[32:35], v[196:199], v[64:67]\n"
        "ds_read_b64_tr_b16 v[62:63], v233 offset:17408\n"
        "v_cvt_pk_bf16_f32 v168, v168, v169\n"
        "v_mfma_f32_16x16x32_bf16 v[96:99], v[32:35], v[200:203], v[96:99]\n"
        "ds_read_b128 v[208:211], v247 offset:34816\n"
        "v_cvt_pk_bf16_f32 v169, v170, v171\n"
        "v_mfma_f32_16x16x32_bf16 v[128:131], v[32:35], v[204:207], v[128:131]\n"
        "global_load_lds_dwordx4 v243, s[64:65] offset:1024\n"
        "v_mfma_f32_16x16x32_bf16 v[4:7], v[36:39], v[192:195], v[4:7]\n"
        "ds_read_b64_tr_b16 v[32:33], v226 offset:24576\n"
        "v_cvt_pk_bf16_f32 v170, v172, v173\n"
        "v_mfma_f32_16x16x32_bf16 v[68:71], v[36:39], v[196:199], v[68:71]\n"
        "ds_read_b64_tr_b16 v[34:35], v227 offset:24576\n"
        "v_cvt_pk_bf16_f32 v171, v174, v175\n"
        "v_mfma_f32_16x16x32_bf16 v[100:103], v[36:39], v[200:203], v[100:103]\n"
        "ds_read_b128 v[212:215], v247 offset:32768\n"
        "v_accvgpr_write_b32 a248, v168\n"
        "v_mfma_f32_16x16x32_bf16 v[132:135], v[36:39], v[204:207], v[132:135]\n"
        "global_load_lds_dwordx4 v244, s[64:65] offset:2048\n"
        "v_mfma_f32_16x16x32_bf16 v[8:11], v[40:43], v[192:195], v[8:11]\n"
        "ds_read_b64_tr_b16 v[36:37], v228 offset:24576\n"
        "v_accvgpr_write_b32 a249, v169\n"
        "v_mfma_f32_16x16x32_bf16 v[72:75], v[40:43], v[196:199], v[72:75]\n"
        "ds_read_b64_tr_b16 v[38:39], v229 offset:24576\n"
        "v_accvgpr_write_b32 a250, v170\n"
        "v_mfma_f32_16x16x32_bf16 v[104:107], v[40:43], v[200:203], v[104:107]\n"
        "ds_read_b128 v[216:219], v247 offset:38912\n"
        "v_accvgpr_write_b32 a251, v171\n"
        "v_mfma_f32_16x16x32_bf16 v[136:139], v[40:43], v[204:207], v[136:139]\n"
        "global_load_lds_dwordx4 v245, s[64:65] offset:3072\n"
        "s_add_u32 m0, s72, 0x11000\n"
        "v_mfma_f32_16x16x32_bf16 v[12:15], v[44:47], v[192:195], v[12:15]\n"
        "ds_read_b64_tr_b16 v[40:41], v230 offset:24576\n"
        "s_add_u32 s64, s64, 0x80\n"
        "s_addc_u32 s65, s65, 0\n"
        "v_mfma_f32_16x16x32_bf16 v[76:79], v[44:47], v[196:199], v[76:79]\n"
        "ds_read_b64_tr_b16 v[42:43], v231 offset:24576\n"
        "v_accvgpr_read_b32 v169, a21\n"
        "v_mfma_f32_16x16x32_bf16 v[108:111], v[44:47], v[200:203], v[108:111]\n"
        "ds_read_b128 v[220:223], v247 offset:36864\n"
        "v_accvgpr_read_b32 v170, a22\n"
        "v_mfma_f32_16x16x32_bf16 v[140:143], v[44:47], v[204:207], v[140:143]\n"
        "global_load_lds_dwordx4 v242, s[66:67] offset:0\n"
        "v_mfma_f32_16x16x32_bf16 v[16:19], v[48:51], v[192:195], v[16:19]\n"
        "ds_read_b64_tr_b16 v[44:45], v232 offset:24576\n"
        "v_accvgpr_read_b32 v171, a23\n"
        "v_mfma_f32_16x16x32_bf16 v[80:83], v[48:51], v[196:199], v[80:83]\n"
        "ds_read_b64_tr_b16 v[46:47], v233 offset:24576\n"
        "v_accvgpr_read_b32 v172, a52\n"
        "v_mfma_f32_16x16x32_bf16 v[112:115], v[48:51], v[200:203], v[112:115]\n"
        "s_waitcnt lgkmcnt(5)\n"
        "v_accvgpr_read_b32 v173, a53\n"
        "v_accvgpr_read_b32 v174, a54\n"
        "v_mfma_f32_16x16x32_bf16 v[144:147], v[48:51], v[204:207], v[144:147]\n"
        "global_load_lds_dwordx4 v243, s[66:67] offset:1024\n"
        "v_mfma_f32_16x16x32_bf16 v[20:23], v[52:55], v[192:195], v[20:23]\n"
        "ds_read_b64_tr_b16 v[48:49], v226 offset:25600\n"
        "v_accvgpr_read_b32 v175, a55\n"
        "v_mfma_f32_16x16x32_bf16 v[84:87], v[52:55], v[196:199], v[84:87]\n"
        "ds_read_b64_tr_b16 v[50:51], v227 offset:25600\n"
        "v_mfma_f32_16x16x32_bf16 v[116:119], v[52:55], v[200:203], v[116:119]\n"
        "v_mfma_f32_16x16x32_bf16 v[148:151], v[52:55], v[204:207], v[148:151]\n"
        "global_load_lds_dwordx4 v244, s[66:67] offset:2048\n"
        "v_mfma_f32_16x16x32_bf16 v[24:27], v[56:59], v[192:195], v[24:27]\n"
        "ds_read_b64_tr_b16 v[52:53], v228 offset:25600\n"
        "v_accvgpr_read_b32 v176, a152\n"
        "v_mfma_f32_16x16x32_bf16 v[88:91], v[56:59], v[196:199], v[88:91]\n"
        "ds_read_b64_tr_b16 v[54:55], v229 offset:25600\n"
        "v_cvt_pk_bf16_f32 v176, v176, v177\n"
        "v_mfma_f32_16x16x32_bf16 v[120:123], v[56:59], v[200:203], v[120:123]\n"
        "v_cvt_pk_bf16_f32 v177, v178, v179\n"
        "v_cvt_pk_bf16_f32 v178, v180, v181\n"
        "v_mfma_f32_16x16x32_bf16 v[152:155], v[56:59], v[204:207], v[152:155]\n"
        "global_load_lds_dwordx4 v245, s[66:67] offset:3072\n"
        "s_add_u32 m0, s73, 0x1c000\n"
        "v_mfma_f32_16x16x32_bf16 v[28:31], v[60:63], v[192:195], v[28:31]\n"
        "s_add_u32 s66, s66, 0x80\n"
        "s_addc_u32 s67, s67, 0\n"
        "v_cvt_pk_bf16_f32 v179, v182, v183\n"
        "v_mfma_f32_16x16x32_bf16 v[92:95], v[60:63], v[196:199], v[92:95]\n"
        "s_waitcnt lgkmcnt(2)\n"
        "v_accvgpr_write_b32 a220, v176\n"
        "v_accvgpr_write_b32 a221, v177\n"
        "v_mfma_f32_16x16x32_bf16 v[124:127], v[60:63], v[200:203], v[124:127]\n"
        "ds_read_b64_tr_b16 v[56:57], v230 offset:25600\n"
        "v_accvgpr_write_b32 a222, v178\n"
        "v_mfma_f32_16x16x32_bf16 v[156:159], v[60:63], v[204:207], v[156:159]\n"
        "ds_read_b64_tr_b16 v[58:59], v231 offset:25600\n"
        "v_accvgpr_write_b32 a223, v179\n"
        "v_mfma_f32_16x16x32_bf16 v[0:3], v[32:35], v[208:211], v[0:3]\n"
        "global_store_dwordx4 v254, a[0:3], s[70:71] offset:0 sc1\n"
        "ds_read_b64_tr_b16 v[60:61], v232 offset:25600\n"
        "ds_read_b64_tr_b16 v[62:63], v233 offset:25600\n"
        "v_accvgpr_read_b32 v177, a25\n"
        "v_mfma_f32_16x16x32_bf16 v[64:67], v[32:35], v[212:215], v[64:67]\n"
        "v_accvgpr_read_b32 v178, a26\n"
        "v_accvgpr_read_b32 v179, a27\n"
        "v_mfma_f32_16x16x32_bf16 v[96:99], v[32:35], v[216:219], v[96:99]\n"
        "v_accvgpr_read_b32 v180, a56\n"
        "v_accvgpr_read_b32 v181, a57\n"
        "v_mfma_f32_16x16x32_bf16 v[128:131], v[32:35], v[220:223], v[128:131]\n"
        "v_accvgpr_read_b32 v182, a58\n"
        "v_accvgpr_read_b32 v183, a59\n"
        "v_mfma_f32_16x16x32_bf16 v[4:7], v[36:39], v[208:211], v[4:7]\n"
        "v_mfma_f32_16x16x32_bf16 v[68:71], v[36:39], v[212:215], v[68:71]\n"
        "v_mfma_f32_16x16x32_bf16 v[100:103], v[36:39], v[216:219], v[100:103]\n"
        "v_mfma_f32_16x16x32_bf16 v[132:135], v[36:39], v[220:223], v[132:135]\n"
        "v_mfma_f32_16x16x32_bf16 v[8:11], v[40:43], v[208:211], v[8:11]\n"
        "s_waitcnt vmcnt(14) lgkmcnt(0)\n"
        "s_barrier\n"
        "v_mfma_f32_16x16x32_bf16 v[72:75], v[40:43], v[212:215], v[72:75]\n"
        "global_load_lds_dwordx4 v238, s[68:69] offset:0\n"
        "ds_read_b64_tr_b16 v[32:33], v226 offset:32768\n"
        "v_mfma_f32_16x16x32_bf16 v[104:107], v[40:43], v[216:219], v[104:107]\n"
        "ds_read_b64_tr_b16 v[34:35], v227 offset:32768\n"
        "v_mfma_f32_16x16x32_bf16 v[136:139], v[40:43], v[220:223], v[136:139]\n"
        "ds_read_b64_tr_b16 v[36:37], v228 offset:32768\n"
        "v_mfma_f32_16x16x32_bf16 v[12:15], v[44:47], v[208:211], v[12:15]\n"
        "ds_read_b64_tr_b16 v[38:39], v229 offset:32768\n"
        "v_mfma_f32_16x16x32_bf16 v[76:79], v[44:47], v[212:215], v[76:79]\n"
        "ds_read_b64_tr_b16 v[40:41], v230 offset:32768\n"
        "global_load_lds_dwordx4 v239, s[68:69] offset:1024\n"
        "v_mfma_f32_16x16x32_bf16 v[108:111], v[44:47], v[216:219], v[108:111]\n"
        "ds_read_b64_tr_b16 v[42:43], v231 offset:32768\n"
        "v_mfma_f32_16x16x32_bf16 v[140:143], v[44:47], v[220:223], v[140:143]\n"
        "ds_read_b128 v[192:195], v246 offset:2048\n"
        "v_accvgpr_read_b32 v184, a156\n"
        "v_mfma_f32_16x16x32_bf16 v[16:19], v[48:51], v[208:211], v[16:19]\n"
        "ds_read_b64_tr_b16 v[44:45], v232 offset:32768\n"
        "v_cvt_pk_bf16_f32 v184, v184, v185\n"
        "v_mfma_f32_16x16x32_bf16 v[80:83], v[48:51], v[212:215], v[80:83]\n"
        "ds_read_b64_tr_b16 v[46:47], v233 offset:32768\n"
        "global_load_lds_dwordx4 v240, s[68:69] offset:2048\n"
        "v_mfma_f32_16x16x32_bf16 v[112:115], v[48:51], v[216:219], v[112:115]\n"
        "ds_read_b128 v[196:199], v246 offset:0\n"
        "v_cvt_pk_bf16_f32 v185, v186, v187\n"
        "v_mfma_f32_16x16x32_bf16 v[144:147], v[48:51], v[220:223], v[144:147]\n"
        "ds_read_b64_tr_b16 v[48:49], v226 offset:33792\n"
        "v_cvt_pk_bf16_f32 v186, v188, v189\n"
        "v_mfma_f32_16x16x32_bf16 v[20:23], v[52:55], v[208:211], v[20:23]\n"
        "ds_read_b64_tr_b16 v[50:51], v227 offset:33792\n"
        "v_cvt_pk_bf16_f32 v187, v190, v191\n"
        "v_mfma_f32_16x16x32_bf16 v[84:87], v[52:55], v[212:215], v[84:87]\n"
        "ds_read_b128 v[200:203], v246 offset:6144\n"
        "v_accvgpr_write_b32 a252, v184\n"
        "v_mfma_f32_16x16x32_bf16 v[116:119], v[52:55], v[216:219], v[116:119]\n"
        "global_load_lds_dwordx4 v241, s[68:69] offset:3072\n"
        "s_add_u32 m0, s72, 0x8000\n"
        "v_accvgpr_write_b32 a253, v185\n"
        "v_mfma_f32_16x16x32_bf16 v[148:151], v[52:55], v[220:223], v[148:151]\n"
        "ds_read_b64_tr_b16 v[52:53], v228 offset:33792\n"
        "v_accvgpr_write_b32 a254, v186\n"
        "v_mfma_f32_16x16x32_bf16 v[24:27], v[56:59], v[208:211], v[24:27]\n"
        "ds_read_b64_tr_b16 v[54:55], v229 offset:33792\n"
        "v_accvgpr_write_b32 a255, v187\n"
        "v_mfma_f32_16x16x32_bf16 v[88:91], v[56:59], v[212:215], v[88:91]\n"
        "ds_read_b128 v[204:207], v246 offset:4096\n"
        "s_add_u32 s68, s68, 0x80000\n"
        "s_addc_u32 s69, s69, 0\n"
        "v_mfma_f32_16x16x32_bf16 v[120:123], v[56:59], v[216:219], v[120:123]\n"
        "global_load_lds_dwordx4 v242, s[64:65] offset:0\n"
        "v_accvgpr_read_b32 v185, a29\n"
        "v_mfma_f32_16x16x32_bf16 v[152:155], v[56:59], v[220:223], v[152:155]\n"
        "ds_read_b64_tr_b16 v[56:57], v230 offset:33792\n"
        "v_accvgpr_read_b32 v186, a30\n"
        "v_mfma_f32_16x16x32_bf16 v[28:31], v[60:63], v[208:211], v[28:31]\n"
        "ds_read_b64_tr_b16 v[58:59], v231 offset:33792\n"
        "v_accvgpr_read_b32 v187, a31\n"
        "v_mfma_f32_16x16x32_bf16 v[92:95], v[60:63], v[212:215], v[92:95]\n"
        "ds_read_b128 v[208:211], v247 offset:2048\n"
        "v_accvgpr_read_b32 v188, a60\n"
        "v_mfma_f32_16x16x32_bf16 v[124:127], v[60:63], v[216:219], v[124:127]\n"
        "global_load_lds_dwordx4 v243, s[64:65] offset:1024\n"
        "ds_read_b128 v[212:215], v247 offset:0\n"
        "v_mfma_f32_16x16x32_bf16 v[156:159], v[60:63], v[220:223], v[156:159]\n"
        "ds_read_b64_tr_b16 v[60:61], v232 offset:33792\n"
        "s_waitcnt lgkmcnt(7)\n"
        "v_mfma_f32_16x16x32_bf16 v[0:3], v[32:35], v[192:195], v[0:3]\n"
        "ds_read_b64_tr_b16 v[62:63], v233 offset:33792\n"
        "v_accvgpr_read_b32 v189, a61\n"
        "v_mfma_f32_16x16x32_bf16 v[64:67], v[32:35], v[196:199], v[64:67]\n"
        "ds_read_b128 v[216:219], v247 offset:6144\n"
        "v_accvgpr_read_b32 v190, a62\n"
        "v_mfma_f32_16x16x32_bf16 v[96:99], v[32:35], v[200:203], v[96:99]\n"
        "global_load_dword v253, v252, s[46:47] offset:0\n"
        "s_waitcnt lgkmcnt(7)\n"
        "v_mfma_f32_16x16x32_bf16 v[128:131], v[32:35], v[204:207], v[128:131]\n"
        "global_load_lds_dwordx4 v244, s[64:65] offset:2048\n"
        "ds_read_b64_tr_b16 v[32:33], v226 offset:40960\n"
        "v_mfma_f32_16x16x32_bf16 v[4:7], v[36:39], v[192:195], v[4:7]\n"
        "ds_read_b64_tr_b16 v[34:35], v227 offset:40960\n"
        "v_accvgpr_read_b32 v191, a63\n"
        "v_mfma_f32_16x16x32_bf16 v[68:71], v[36:39], v[196:199], v[68:71]\n"
        "ds_read_b128 v[220:223], v247 offset:4096\n"
        "v_mfma_f32_16x16x32_bf16 v[100:103], v[36:39], v[200:203], v[100:103]\n"
        "v_mfma_f32_16x16x32_bf16 v[132:135], v[36:39], v[204:207], v[132:135]\n"
        "global_load_lds_dwordx4 v245, s[64:65] offset:3072\n"
        "ds_read_b64_tr_b16 v[36:37], v228 offset:40960\n"
        "s_add_u32 m0, s72, 0x9000\n"
        "v_mfma_f32_16x16x32_bf16 v[8:11], v[40:43], v[192:195], v[8:11]\n"
        "ds_read_b64_tr_b16 v[38:39], v229 offset:40960\n"
        "v_accvgpr_read_b32 v160, a16\n"
        "v_mfma_f32_16x16x32_bf16 v[72:75], v[40:43], v[196:199], v[72:75]\n"
        "s_add_u32 s64, s64, 0x80\n"
        "s_addc_u32 s65, s65, 0\n"
        "v_cvt_pk_bf16_f32 v160, v160, v161\n"
        "v_mfma_f32_16x16x32_bf16 v[104:107], v[40:43], v[200:203], v[104:107]\n"
        "v_cvt_pk_bf16_f32 v161, v162, v163\n"
        "v_cvt_pk_bf16_f32 v162, v164, v165\n"
        "v_mfma_f32_16x16x32_bf16 v[136:139], v[40:43], v[204:207], v[136:139]\n"
        "global_load_lds_dwordx4 v242, s[66:67] offset:0\n"
        "v_cvt_pk_bf16_f32 v163, v166, v167\n"
        "v_mfma_f32_16x16x32_bf16 v[12:15], v[44:47], v[192:195], v[12:15]\n"
        "ds_read_b64_tr_b16 v[40:41], v230 offset:40960\n"
        "v_accvgpr_write_b32 a176, v160\n"
        "v_mfma_f32_16x16x32_bf16 v[76:79], v[44:47], v[196:199], v[76:79]\n"
        "ds_read_b64_tr_b16 v[42:43], v231 offset:40960\n"
        "v_accvgpr_write_b32 a177, v161\n"
        "v_mfma_f32_16x16x32_bf16 v[108:111], v[44:47], v[200:203], v[108:111]\n"
        "v_accvgpr_write_b32 a178, v162\n"
        "v_accvgpr_write_b32 a179, v163\n"
        "v_mfma_f32_16x16x32_bf16 v[140:143], v[44:47], v[204:207], v[140:143]\n"
        "global_store_dwordx4 v254, a[4:7], s[70:71] offset:1024 sc1\n"
        "ds_read_b64_tr_b16 v[44:45], v232 offset:40960\n"
        "ds_read_b64_tr_b16 v[46:47], v233 offset:40960\n"
        "s_add_u32 s70, s38, s97\n"
        "s_addc_u32 s71, s39, 0\n"
        "v_mfma_f32_16x16x32_bf16 v[16:19], v[48:51], v[192:195], v[16:19]\n"
        "s_add_u32 s97, s53, 0x6000\n"
        "v_accvgpr_read_b32 v161, a81\n"
        "v_accvgpr_read_b32 v162, a82\n"
        "v_mfma_f32_16x16x32_bf16 v[80:83], v[48:51], v[196:199], v[80:83]\n"
        "v_accvgpr_read_b32 v163, a83\n"
        "v_accvgpr_read_b32 v164, a112\n"
        "v_mfma_f32_16x16x32_bf16 v[112:115], v[48:51], v[200:203], v[112:115]\n"
        "global_load_lds_dwordx4 v243, s[66:67] offset:1024\n"
        "v_mfma_f32_16x16x32_bf16 v[144:147], v[48:51], v[204:207], v[144:147]\n"
        "ds_read_b64_tr_b16 v[48:49], v226 offset:41984\n"
        "v_accvgpr_read_b32 v165, a113\n"
        "v_mfma_f32_16x16x32_bf16 v[20:23], v[52:55], v[192:195], v[20:23]\n"
        "ds_read_b64_tr_b16 v[50:51], v227 offset:41984\n"
        "v_accvgpr_read_b32 v166, a114\n"
        "v_mfma_f32_16x16x32_bf16 v[84:87], v[52:55], v[196:199], v[84:87]\n"
        "s_waitcnt lgkmcnt(4)\n"
        "v_accvgpr_read_b32 v167, a115\n"
        "v_mfma_f32_16x16x32_bf16 v[116:119], v[52:55], v[200:203], v[116:119]\n"
        "global_load_lds_dwordx4 v244, s[66:67] offset:2048\n"
        "v_mfma_f32_16x16x32_bf16 v[148:151], v[52:55], v[204:207], v[148:151]\n"
        "ds_read_b64_tr_b16 v[52:53], v228 offset:41984\n"
        "v_mfma_f32_16x16x32_bf16 v[24:27], v[56:59], v[192:195], v[24:27]\n"
        "ds_read_b64_tr_b16 v[54:55], v229 offset:41984\n"
        "v_mfma_f32_16x16x32_bf16 v[88:91], v[56:59], v[196:199], v[88:91]\n"
        "v_mfma_f32_16x16x32_bf16 v[120:123], v[56:59], v[200:203], v[120:123]\n"
        "global_load_lds_dwordx4 v245, s[66:67] offset:3072\n"
        "s_add_u32 m0, s73, 0x20000\n"
        "v_mfma_f32_16x16x32_bf16 v[152:155], v[56:59], v[204:207], v[152:155]\n"
        "s_add_u32 s66, s66, 0x80\n"
        "s_addc_u32 s67, s67, 0\n"
        "v_mfma_f32_16x16x32_bf16 v[28:31], v[60:63], v[192:195], v[28:31]\n"
        "ds_read_b64_tr_b16 v[56:57], v230 offset:41984\n"
        "v_mfma_f32_16x16x32_bf16 v[92:95], v[60:63], v[196:199], v[92:95]\n"
        "ds_read_b64_tr_b16 v[58:59], v231 offset:41984\n"
        "v_mfma_f32_16x16x32_bf16 v[124:127], v[60:63], v[200:203], v[124:127]\n"
        "v_mfma_f32_16x16x32_bf16 v[156:159], v[60:63], v[204:207], v[156:159]\n"
        "ds_read_b64_tr_b16 v[60:61], v232 offset:41984\n"
        "v_accvgpr_read_b32 v168, a20\n"
        "v_mfma_f32_16x16x32_bf16 v[0:3], v[32:35], v[208:211], v[0:3]\n"
        "ds_read_b64_tr_b16 v[62:63], v233 offset:41984\n"
        "v_cvt_pk_bf16_f32 v168, v168, v169\n"
        "v_mfma_f32_16x16x32_bf16 v[64:67], v[32:35], v[212:215], v[64:67]\n"
        "v_cvt_pk_bf16_f32 v169, v170, v171\n"
        "v_cvt_pk_bf16_f32 v170, v172, v173\n"
        "v_mfma_f32_16x16x32_bf16 v[96:99], v[32:35], v[216:219], v[96:99]\n"
        "v_cvt_pk_bf16_f32 v171, v174, v175\n"
        "v_accvgpr_write_b32 a180, v168\n"
        "v_mfma_f32_16x16x32_bf16 v[128:131], v[32:35], v[220:223], v[128:131]\n"
        "v_accvgpr_write_b32 a181, v169\n"
        "v_accvgpr_write_b32 a182, v170\n"
        "v_mfma_f32_16x16x32_bf16 v[4:7], v[36:39], v[208:211], v[4:7]\n"
        "v_accvgpr_write_b32 a183, v171\n"
        "v_accvgpr_read_b32 v169, a85\n"
        "v_mfma_f32_16x16x32_bf16 v[68:71], v[36:39], v[212:215], v[68:71]\n"
        "v_accvgpr_read_b32 v170, a86\n"
        "v_accvgpr_read_b32 v171, a87\n"
        "v_mfma_f32_16x16x32_bf16 v[100:103], v[36:39], v[216:219], v[100:103]\n"
        "s_waitcnt lgkmcnt(2)\n"
        "v_accvgpr_read_b32 v172, a116\n"
        "v_accvgpr_read_b32 v173, a117\n"
        "v_mfma_f32_16x16x32_bf16 v[132:135], v[36:39], v[220:223], v[132:135]\n"
        "v_accvgpr_read_b32 v174, a118\n"
        "v_accvgpr_read_b32 v175, a119\n"
        "v_mfma_f32_16x16x32_bf16 v[8:11], v[40:43], v[208:211], v[8:11]\n"
        "s_waitcnt vmcnt(15) lgkmcnt(0)\n"
        "s_barrier\n"
        "v_mfma_f32_16x16x32_bf16 v[72:75], v[40:43], v[212:215], v[72:75]\n"
        "global_load_lds_dwordx4 v238, s[68:69] offset:0\n"
        "ds_read_b64_tr_b16 v[32:33], v226 offset:49152\n"
        "v_mfma_f32_16x16x32_bf16 v[104:107], v[40:43], v[216:219], v[104:107]\n"
        "ds_read_b64_tr_b16 v[34:35], v227 offset:49152\n"
        "v_mfma_f32_16x16x32_bf16 v[136:139], v[40:43], v[220:223], v[136:139]\n"
        "ds_read_b64_tr_b16 v[36:37], v228 offset:49152\n"
        "v_mfma_f32_16x16x32_bf16 v[12:15], v[44:47], v[208:211], v[12:15]\n"
        "ds_read_b64_tr_b16 v[38:39], v229 offset:49152\n"
        "v_mfma_f32_16x16x32_bf16 v[76:79], v[44:47], v[212:215], v[76:79]\n"
        "ds_read_b64_tr_b16 v[40:41], v230 offset:49152\n"
        "global_load_lds_dwordx4 v239, s[68:69] offset:1024\n"
        "v_mfma_f32_16x16x32_bf16 v[108:111], v[44:47], v[216:219], v[108:111]\n"
        "ds_read_b64_tr_b16 v[42:43], v231 offset:49152\n"
        "v_mfma_f32_16x16x32_bf16 v[140:143], v[44:47], v[220:223], v[140:143]\n"
        "ds_read_b128 v[192:195], v248 offset:2048\n"
        "v_mfma_f32_16x16x32_bf16 v[16:19], v[48:51], v[208:211], v[16:19]\n"
        "ds_read_b64_tr_b16 v[44:45], v232 offset:49152\n"
        "v_mfma_f32_16x16x32_bf16 v[80:83], v[48:51], v[212:215], v[80:83]\n"
        "ds_read_b64_tr_b16 v[46:47], v233 offset:49152\n"
        "global_load_lds_dwordx4 v240, s[68:69] offset:2048\n"
        "v_mfma_f32_16x16x32_bf16 v[112:115], v[48:51], v[216:219], v[112:115]\n"
        "ds_read_b128 v[196:199], v248 offset:0\n"
        "v_mfma_f32_16x16x32_bf16 v[144:147], v[48:51], v[220:223], v[144:147]\n"
        "ds_read_b64_tr_b16 v[48:49], v226 offset:50176\n"
        "v_mfma_f32_16x16x32_bf16 v[20:23], v[52:55], v[208:211], v[20:23]\n"
        "ds_read_b64_tr_b16 v[50:51], v227 offset:50176\n"
        "v_mfma_f32_16x16x32_bf16 v[84:87], v[52:55], v[212:215], v[84:87]\n"
        "ds_read_b128 v[200:203], v248 offset:6144\n"
        "global_load_lds_dwordx4 v241, s[68:69] offset:3072\n"
        "s_add_u32 m0, s72, 0x0\n"
        "v_mfma_f32_16x16x32_bf16 v[116:119], v[52:55], v[216:219], v[116:119]\n"
        "ds_read_b128 v[204:207], v248 offset:4096\n"
        "s_add_u32 s68, s68, 0x80000\n"
        "s_addc_u32 s69, s69, 0\n"
        "v_mfma_f32_16x16x32_bf16 v[148:151], v[52:55], v[220:223], v[148:151]\n"
        "ds_read_b64_tr_b16 v[52:53], v228 offset:50176\n"
        "v_accvgpr_read_b32 v176, a24\n"
        "v_mfma_f32_16x16x32_bf16 v[24:27], v[56:59], v[208:211], v[24:27]\n"
        "ds_read_b64_tr_b16 v[54:55], v229 offset:50176\n"
        "v_cvt_pk_bf16_f32 v176, v176, v177\n"
        "v_mfma_f32_16x16x32_bf16 v[88:91], v[56:59], v[212:215], v[88:91]\n"
        "global_load_lds_dwordx4 v242, s[64:65] offset:0\n"
        "v_mfma_f32_16x16x32_bf16 v[120:123], v[56:59], v[216:219], v[120:123]\n"
        "s_waitcnt lgkmcnt(6)\n"
        "v_cvt_pk_bf16_f32 v177, v178, v179\n"
        "v_cvt_pk_bf16_f32 v178, v180, v181\n"
        "v_mfma_f32_16x16x32_bf16 v[152:155], v[56:59], v[220:223], v[152:155]\n"
        "ds_read_b64_tr_b16 v[56:57], v230 offset:50176\n"
        "v_cvt_pk_bf16_f32 v179, v182, v183\n"
        "v_mfma_f32_16x16x32_bf16 v[28:31], v[60:63], v[208:211], v[28:31]\n"
        "s_waitcnt lgkmcnt(5)\n"
        "v_accvgpr_write_b32 a184, v176\n"
        "v_accvgpr_write_b32 a185, v177\n"
        "v_mfma_f32_16x16x32_bf16 v[92:95], v[60:63], v[212:215], v[92:95]\n"
        "s_waitcnt lgkmcnt(4)\n"
        "v_accvgpr_write_b32 a186, v178\n"
        "v_accvgpr_write_b32 a187, v179\n"
        "v_mfma_f32_16x16x32_bf16 v[124:127], v[60:63], v[216:219], v[124:127]\n"
        "global_store_dwordx4 v254, a[8:11], s[70:71] offset:0 sc1\n"
        "ds_read_b64_tr_b16 v[58:59], v231 offset:50176\n"
        "ds_read_b128 v[208:211], v249 offset:2048\n"
        "v_accvgpr_read_b32 v177, a89\n"
        "v_mfma_f32_16x16x32_bf16 v[156:159], v[60:63], v[220:223], v[156:159]\n"
        "ds_read_b64_tr_b16 v[60:61], v232 offset:50176\n"
        "v_accvgpr_read_b32 v178, a90\n"
        "v_mfma_f32_16x16x32_bf16 v[0:3], v[32:35], v[192:195], v[0:3]\n"
        "ds_read_b64_tr_b16 v[62:63], v233 offset:50176\n"
        "v_accvgpr_read_b32 v179, a91\n"
        "v_mfma_f32_16x16x32_bf16 v[64:67], v[32:35], v[196:199], v[64:67]\n"
        "global_load_lds_dwordx4 v243, s[64:65] offset:1024\n"
        "v_mfma_f32_16x16x32_bf16 v[96:99], v[32:35], v[200:203], v[96:99]\n"
        "ds_read_b128 v[212:215], v249 offset:0\n"
        "s_waitcnt lgkmcnt(5)\n"
        "v_mfma_f32_16x16x32_bf16 v[128:131], v[32:35], v[204:207], v[128:131]\n"
        "ds_read_b64_tr_b16 v[32:33], v226 offset:57344\n"
        "v_accvgpr_read_b32 v180, a120\n"
        "v_mfma_f32_16x16x32_bf16 v[4:7], v[36:39], v[192:195], v[4:7]\n"
        "ds_read_b64_tr_b16 v[34:35], v227 offset:57344\n"
        "v_accvgpr_read_b32 v181, a121\n"
        "v_mfma_f32_16x16x32_bf16 v[68:71], v[36:39], v[196:199], v[68:71]\n"
        "global_load_lds_dwordx4 v244, s[64:65] offset:2048\n"
        "v_mfma_f32_16x16x32_bf16 v[100:103], v[36:39], v[200:203], v[100:103]\n"
        "ds_read_b128 v[216:219], v249 offset:6144\n"
        "v_accvgpr_read_b32 v182, a122\n"
        "v_mfma_f32_16x16x32_bf16 v[132:135], v[36:39], v[204:207], v[132:135]\n"
        "ds_read_b64_tr_b16 v[36:37], v228 offset:57344\n"
        "v_accvgpr_read_b32 v183, a123\n"
        "v_mfma_f32_16x16x32_bf16 v[8:11], v[40:43], v[192:195], v[8:11]\n"
        "ds_read_b64_tr_b16 v[38:39], v229 offset:57344\n"
        "v_mfma_f32_16x16x32_bf16 v[72:75], v[40:43], v[196:199], v[72:75]\n"
        "global_load_lds_dwordx4 v245, s[64:65] offset:3072\n"
        "s_add_u32 m0, s72, 0x1000\n"
        "v_mfma_f32_16x16x32_bf16 v[104:107], v[40:43], v[200:203], v[104:107]\n"
        "ds_read_b128 v[220:223], v249 offset:4096\n"
        "s_add_u32 s64, s64, 0x80\n"
        "s_addc_u32 s65, s65, 0\n"
        "v_mfma_f32_16x16x32_bf16 v[136:139], v[40:43], v[204:207], v[136:139]\n"
        "ds_read_b64_tr_b16 v[40:41], v230 offset:57344\n"
        "v_mfma_f32_16x16x32_bf16 v[12:15], v[44:47], v[192:195], v[12:15]\n"
        "ds_read_b64_tr_b16 v[42:43], v231 offset:57344\n"
        "v_mfma_f32_16x16x32_bf16 v[76:79], v[44:47], v[196:199], v[76:79]\n"
        "global_load_lds_dwordx4 v242, s[66:67] offset:0\n"
        "v_mfma_f32_16x16x32_bf16 v[108:111], v[44:47], v[200:203], v[108:111]\n"
        "v_mfma_f32_16x16x32_bf16 v[140:143], v[44:47], v[204:207], v[140:143]\n"
        "ds_read_b64_tr_b16 v[44:45], v232 offset:57344\n"
        "v_accvgpr_read_b32 v184, a28\n"
        "v_mfma_f32_16x16x32_bf16 v[16:19], v[48:51], v[192:195], v[16:19]\n"
        "ds_read_b64_tr_b16 v[46:47], v233 offset:57344\n"
        "v_cvt_pk_bf16_f32 v184, v184, v185\n"
        "v_mfma_f32_16x16x32_bf16 v[80:83], v[48:51], v[196:199], v[80:83]\n"
        "global_load_lds_dwordx4 v243, s[66:67] offset:1024\n"
        "v_mfma_f32_16x16x32_bf16 v[112:115], v[48:51], v[200:203], v[112:115]\n"
        "s_waitcnt lgkmcnt(4)\n"
        "v_cvt_pk_bf16_f32 v185, v186, v187\n"
        "v_cvt_pk_bf16_f32 v186, v188, v189\n"
        "v_mfma_f32_16x16x32_bf16 v[144:147], v[48:51], v[204:207], v[144:147]\n"
        "ds_read_b64_tr_b16 v[48:49], v226 offset:58368\n"
        "v_cvt_pk_bf16_f32 v187, v190, v191\n"
        "v_mfma_f32_16x16x32_bf16 v[20:23], v[52:55], v[192:195], v[20:23]\n"
        "ds_read_b64_tr_b16 v[50:51], v227 offset:58368\n"
        "v_accvgpr_write_b32 a188, v184\n"
        "v_mfma_f32_16x16x32_bf16 v[84:87], v[52:55], v[196:199], v[84:87]\n"
        "global_load_lds_dwordx4 v244, s[66:67] offset:2048\n"
        "v_mfma_f32_16x16x32_bf16 v[116:119], v[52:55], v[200:203], v[116:119]\n"
        "v_accvgpr_write_b32 a189, v185\n"
        "v_accvgpr_write_b32 a190, v186\n"
        "v_mfma_f32_16x16x32_bf16 v[148:151], v[52:55], v[204:207], v[148:151]\n"
        "ds_read_b64_tr_b16 v[52:53], v228 offset:58368\n"
        "v_accvgpr_write_b32 a191, v187\n"
        "v_mfma_f32_16x16x32_bf16 v[24:27], v[56:59], v[192:195], v[24:27]\n"
        "ds_read_b64_tr_b16 v[54:55], v229 offset:58368\n"
        "v_accvgpr_read_b32 v185, a93\n"
        "v_mfma_f32_16x16x32_bf16 v[88:91], v[56:59], v[196:199], v[88:91]\n"
        "global_load_lds_dwordx4 v245, s[66:67] offset:3072\n"
        "s_add_u32 m0, s73, 0x24000\n"
        "v_mfma_f32_16x16x32_bf16 v[120:123], v[56:59], v[200:203], v[120:123]\n"
        "s_add_u32 s66, s66, 0x80\n"
        "s_addc_u32 s67, s67, 0\n"
        "v_accvgpr_read_b32 v186, a94\n"
        "v_mfma_f32_16x16x32_bf16 v[152:155], v[56:59], v[204:207], v[152:155]\n"
        "ds_read_b64_tr_b16 v[56:57], v230 offset:58368\n"
        "v_accvgpr_read_b32 v187, a95\n"
        "v_mfma_f32_16x16x32_bf16 v[28:31], v[60:63], v[192:195], v[28:31]\n"
        "ds_read_b64_tr_b16 v[58:59], v231 offset:58368\n"
        "v_accvgpr_read_b32 v188, a124\n"
        "v_mfma_f32_16x16x32_bf16 v[92:95], v[60:63], v[196:199], v[92:95]\n"
        "v_accvgpr_read_b32 v189, a125\n"
        "v_accvgpr_read_b32 v190, a126\n"
        "v_mfma_f32_16x16x32_bf16 v[124:127], v[60:63], v[200:203], v[124:127]\n"
        "v_accvgpr_read_b32 v191, a127\n"
        "v_mfma_f32_16x16x32_bf16 v[156:159], v[60:63], v[204:207], v[156:159]\n"
        "ds_read_b64_tr_b16 v[60:61], v232 offset:58368\n"
        "v_mfma_f32_16x16x32_bf16 v[0:3], v[32:35], v[208:211], v[0:3]\n"
        "ds_read_b64_tr_b16 v[62:63], v233 offset:58368\n"
        "v_mfma_f32_16x16x32_bf16 v[64:67], v[32:35], v[212:215], v[64:67]\n"
        "global_store_dwordx4 v254, a[12:15], s[70:71] offset:1024 sc1\n"
        "s_add_u32 s70, s38, s97\n"
        "s_addc_u32 s71, s39, 0\n"
        "v_mfma_f32_16x16x32_bf16 v[96:99], v[32:35], v[216:219], v[96:99]\n"
        "s_waitcnt lgkmcnt(4)\n"
        "v_mfma_f32_16x16x32_bf16 v[128:131], v[32:35], v[220:223], v[128:131]\n"
        "v_mfma_f32_16x16x32_bf16 v[4:7], v[36:39], v[208:211], v[4:7]\n"
        "v_mfma_f32_16x16x32_bf16 v[68:71], v[36:39], v[212:215], v[68:71]\n"
        "v_mfma_f32_16x16x32_bf16 v[100:103], v[36:39], v[216:219], v[100:103]\n"
        "v_mfma_f32_16x16x32_bf16 v[132:135], v[36:39], v[220:223], v[132:135]\n"
        "s_waitcnt vmcnt(14) lgkmcnt(0)\n"
        "s_barrier\n"
        "v_mfma_f32_16x16x32_bf16 v[8:11], v[40:43], v[208:211], v[8:11]\n"
        "global_load_lds_dwordx4 v238, s[68:69] offset:0\n"
        "ds_read_b64_tr_b16 v[32:33], v226 offset:16384\n"
        "v_mfma_f32_16x16x32_bf16 v[72:75], v[40:43], v[212:215], v[72:75]\n"
        "ds_read_b64_tr_b16 v[34:35], v227 offset:16384\n"
        "v_accvgpr_read_b32 v160, a80\n"
        "v_mfma_f32_16x16x32_bf16 v[104:107], v[40:43], v[216:219], v[104:107]\n"
        "ds_read_b64_tr_b16 v[36:37], v228 offset:16384\n"
        "v_cvt_pk_bf16_f32 v160, v160, v161\n"
        "v_mfma_f32_16x16x32_bf16 v[136:139], v[40:43], v[220:223], v[136:139]\n"
        "ds_read_b64_tr_b16 v[38:39], v229 offset:16384\n"
        "v_cvt_pk_bf16_f32 v161, v162, v163\n"
        "v_mfma_f32_16x16x32_bf16 v[12:15], v[44:47], v[208:211], v[12:15]\n"
        "ds_read_b64_tr_b16 v[40:41], v230 offset:16384\n"
        "v_cvt_pk_bf16_f32 v162, v164, v165\n"
        "v_mfma_f32_16x16x32_bf16 v[76:79], v[44:47], v[212:215], v[76:79]\n"
        "ds_read_b64_tr_b16 v[42:43], v231 offset:16384\n"
        "v_cvt_pk_bf16_f32 v163, v166, v167\n"
        "v_mfma_f32_16x16x32_bf16 v[108:111], v[44:47], v[216:219], v[108:111]\n"
        "ds_read_b128 v[192:195], v246 offset:34816\n"
        "v_accvgpr_write_b32 a16, v160\n"
        "v_mfma_f32_16x16x32_bf16 v[140:143], v[44:47], v[220:223], v[140:143]\n"
        "global_load_lds_dwordx4 v239, s[68:69] offset:1024\n"
        "v_accvgpr_write_b32 a17, v161\n"
        "v_mfma_f32_16x16x32_bf16 v[16:19], v[48:51], v[208:211], v[16:19]\n"
        "ds_read_b64_tr_b16 v[44:45], v232 offset:16384\n"
        "v_accvgpr_write_b32 a18, v162\n"
        "v_mfma_f32_16x16x32_bf16 v[80:83], v[48:51], v[212:215], v[80:83]\n"
        "ds_read_b64_tr_b16 v[46:47], v233 offset:16384\n"
        "v_accvgpr_write_b32 a19, v163\n"
        "v_mfma_f32_16x16x32_bf16 v[112:115], v[48:51], v[216:219], v[112:115]\n"
        "ds_read_b128 v[196:199], v246 offset:32768\n"
        "v_mfma_f32_16x16x32_bf16 v[144:147], v[48:51], v[220:223], v[144:147]\n"
        "global_load_lds_dwordx4 v240, s[68:69] offset:2048\n"
        "ds_read_b64_tr_b16 v[48:49], v226 offset:17408\n"
        "v_mfma_f32_16x16x32_bf16 v[20:23], v[52:55], v[208:211], v[20:23]\n"
        "ds_read_b64_tr_b16 v[50:51], v227 offset:17408\n"
        "s_add_u32 s97, s53, 0x4000\n"
        "v_mfma_f32_16x16x32_bf16 v[84:87], v[52:55], v[212:215], v[84:87]\n"
        "ds_read_b128 v[200:203], v246 offset:38912\n"
        "v_mfma_f32_16x16x32_bf16 v[116:119], v[52:55], v[216:219], v[116:119]\n"
        "ds_read_b128 v[204:207], v246 offset:36864\n"
        "v_mfma_f32_16x16x32_bf16 v[148:151], v[52:55], v[220:223], v[148:151]\n"
        "global_load_lds_dwordx4 v241, s[68:69] offset:3072\n"
        "ds_read_b64_tr_b16 v[52:53], v228 offset:17408\n"
        "s_add_u32 m0, s72, 0x10000\n"
        "v_mfma_f32_16x16x32_bf16 v[24:27], v[56:59], v[208:211], v[24:27]\n"
        "ds_read_b64_tr_b16 v[54:55], v229 offset:17408\n"
        "s_add_u32 s68, s68, 0x80000\n"
        "s_addc_u32 s69, s69, 0\n"
        "v_mfma_f32_16x16x32_bf16 v[88:91], v[56:59], v[212:215], v[88:91]\n"
        "v_mfma_f32_16x16x32_bf16 v[120:123], v[56:59], v[216:219], v[120:123]\n"
        "s_waitcnt lgkmcnt(6)\n"
        "v_mfma_f32_16x16x32_bf16 v[152:155], v[56:59], v[220:223], v[152:155]\n"
        "global_load_lds_dwordx4 v242, s[64:65] offset:0\n"
        "ds_read_b64_tr_b16 v[56:57], v230 offset:17408\n"
        "v_mfma_f32_16x16x32_bf16 v[28:31], v[60:63], v[208:211], v[28:31]\n"
        "ds_read_b64_tr_b16 v[58:59], v231 offset:17408\n"
        "s_waitcnt lgkmcnt(6)\n"
        "v_mfma_f32_16x16x32_bf16 v[92:95], v[60:63], v[212:215], v[92:95]\n"
        "ds_read_b128 v[208:211], v247 offset:34816\n"
        "s_waitcnt lgkmcnt(6)\n"
        "v_mfma_f32_16x16x32_bf16 v[124:127], v[60:63], v[216:219], v[124:127]\n"
        "ds_read_b128 v[212:215], v247 offset:32768\n"
        "s_waitcnt lgkmcnt(6)\n"
        "v_mfma_f32_16x16x32_bf16 v[156:159], v[60:63], v[220:223], v[156:159]\n"
        "global_load_lds_dwordx4 v243, s[64:65] offset:1024\n"
        "ds_read_b64_tr_b16 v[60:61], v232 offset:17408\n"
        "v_mfma_f32_16x16x32_bf16 v[0:3], v[32:35], v[192:195], v[0:3]\n"
        "ds_read_b64_tr_b16 v[62:63], v233 offset:17408\n"
        "v_accvgpr_read_b32 v168, a84\n"
        "v_mfma_f32_16x16x32_bf16 v[64:67], v[32:35], v[196:199], v[64:67]\n"
        "ds_read_b128 v[216:219], v247 offset:38912\n"
        "v_cvt_pk_bf16_f32 v168, v168, v169\n"
        "v_mfma_f32_16x16x32_bf16 v[96:99], v[32:35], v[200:203], v[96:99]\n"
        "ds_read_b128 v[220:223], v247 offset:36864\n"
        "v_cvt_pk_bf16_f32 v169, v170, v171\n"
        "v_mfma_f32_16x16x32_bf16 v[128:131], v[32:35], v[204:207], v[128:131]\n"
        "global_load_lds_dwordx4 v244, s[64:65] offset:2048\n"
        "v_cvt_pk_bf16_f32 v170, v172, v173\n"
        "v_mfma_f32_16x16x32_bf16 v[4:7], v[36:39], v[192:195], v[4:7]\n"
        "ds_read_b64_tr_b16 v[32:33], v226 offset:24576\n"
        "v_cvt_pk_bf16_f32 v171, v174, v175\n"
        "v_mfma_f32_16x16x32_bf16 v[68:71], v[36:39], v[196:199], v[68:71]\n"
        "ds_read_b64_tr_b16 v[34:35], v227 offset:24576\n"
        "v_accvgpr_write_b32 a20, v168\n"
        "v_mfma_f32_16x16x32_bf16 v[100:103], v[36:39], v[200:203], v[100:103]\n"
        "v_accvgpr_write_b32 a21, v169\n"
        "v_accvgpr_write_b32 a22, v170\n"
        "v_mfma_f32_16x16x32_bf16 v[132:135], v[36:39], v[204:207], v[132:135]\n"
        "global_load_lds_dwordx4 v245, s[64:65] offset:3072\n"
        "s_add_u32 m0, s72, 0x11000\n"
        "v_accvgpr_write_b32 a23, v171\n"
        "v_mfma_f32_16x16x32_bf16 v[8:11], v[40:43], v[192:195], v[8:11]\n"
        "ds_read_b64_tr_b16 v[36:37], v228 offset:24576\n"
        "s_add_u32 s64, s64, 0x80\n"
        "s_addc_u32 s65, s65, 0\n"
        "v_mfma_f32_16x16x32_bf16 v[72:75], v[40:43], v[196:199], v[72:75]\n"
        "ds_read_b64_tr_b16 v[38:39], v229 offset:24576\n"
        "v_mfma_f32_16x16x32_bf16 v[104:107], v[40:43], v[200:203], v[104:107]\n"
        "v_mfma_f32_16x16x32_bf16 v[136:139], v[40:43], v[204:207], v[136:139]\n"
        "global_load_lds_dwordx4 v242, s[66:67] offset:0\n"
        "ds_read_b64_tr_b16 v[40:41], v230 offset:24576\n"
        "v_mfma_f32_16x16x32_bf16 v[12:15], v[44:47], v[192:195], v[12:15]\n"
        "ds_read_b64_tr_b16 v[42:43], v231 offset:24576\n"
        "v_mfma_f32_16x16x32_bf16 v[76:79], v[44:47], v[196:199], v[76:79]\n"
        "v_mfma_f32_16x16x32_bf16 v[108:111], v[44:47], v[200:203], v[108:111]\n"
        "s_waitcnt lgkmcnt(4)\n"
        "v_mfma_f32_16x16x32_bf16 v[140:143], v[44:47], v[204:207], v[140:143]\n"
        "global_store_dwordx4 v254, a[208:211], s[70:71] offset:0 sc1\n"
        "ds_read_b64_tr_b16 v[44:45], v232 offset:24576\n"
        "ds_read_b64_tr_b16 v[46:47], v233 offset:24576\n"
        "v_mfma_f32_16x16x32_bf16 v[16:19], v[48:51], v[192:195], v[16:19]\n"
        "v_mfma_f32_16x16x32_bf16 v[80:83], v[48:51], v[196:199], v[80:83]\n"
        "v_mfma_f32_16x16x32_bf16 v[112:115], v[48:51], v[200:203], v[112:115]\n"
        "global_load_lds_dwordx4 v243, s[66:67] offset:1024\n"
        "v_mfma_f32_16x16x32_bf16 v[144:147], v[48:51], v[204:207], v[144:147]\n"
        "ds_read_b64_tr_b16 v[48:49], v226 offset:25600\n"
        "v_mfma_f32_16x16x32_bf16 v[20:23], v[52:55], v[192:195], v[20:23]\n"
        "ds_read_b64_tr_b16 v[50:51], v227 offset:25600\n"
        "v_mfma_f32_16x16x32_bf16 v[84:87], v[52:55], v[196:199], v[84:87]\n"
        "v_mfma_f32_16x16x32_bf16 v[116:119], v[52:55], v[200:203], v[116:119]\n"
        "global_load_lds_dwordx4 v244, s[66:67] offset:2048\n"
        "v_mfma_f32_16x16x32_bf16 v[148:151], v[52:55], v[204:207], v[148:151]\n"
        "ds_read_b64_tr_b16 v[52:53], v228 offset:25600\n"
        "v_accvgpr_read_b32 v176, a88\n"
        "v_mfma_f32_16x16x32_bf16 v[24:27], v[56:59], v[192:195], v[24:27]\n"
        "ds_read_b64_tr_b16 v[54:55], v229 offset:25600\n"
        "v_cvt_pk_bf16_f32 v176, v176, v177\n"
        "v_mfma_f32_16x16x32_bf16 v[88:91], v[56:59], v[196:199], v[88:91]\n"
        "v_cvt_pk_bf16_f32 v177, v178, v179\n"
        "v_cvt_pk_bf16_f32 v178, v180, v181\n"
        "v_mfma_f32_16x16x32_bf16 v[120:123], v[56:59], v[200:203], v[120:123]\n"
        "global_load_lds_dwordx4 v245, s[66:67] offset:3072\n"
        "s_add_u32 m0, s73, 0x1c000\n"
        "v_mfma_f32_16x16x32_bf16 v[152:155], v[56:59], v[204:207], v[152:155]\n"
        "s_add_u32 s66, s66, 0x80\n"
        "s_addc_u32 s67, s67, 0\n"
        "v_cvt_pk_bf16_f32 v179, v182, v183\n"
        "v_mfma_f32_16x16x32_bf16 v[28:31], v[60:63], v[192:195], v[28:31]\n"
        "ds_read_b64_tr_b16 v[56:57], v230 offset:25600\n"
        "v_accvgpr_write_b32 a24, v176\n"
        "v_mfma_f32_16x16x32_bf16 v[92:95], v[60:63], v[196:199], v[92:95]\n"
        "ds_read_b64_tr_b16 v[58:59], v231 offset:25600\n"
        "v_accvgpr_write_b32 a25, v177\n"
        "v_mfma_f32_16x16x32_bf16 v[124:127], v[60:63], v[200:203], v[124:127]\n"
        "s_waitcnt lgkmcnt(4)\n"
        "v_accvgpr_write_b32 a26, v178\n"
        "v_accvgpr_write_b32 a27, v179\n"
        "v_mfma_f32_16x16x32_bf16 v[156:159], v[60:63], v[204:207], v[156:159]\n"
        "ds_read_b64_tr_b16 v[60:61], v232 offset:25600\n"
        "v_mfma_f32_16x16x32_bf16 v[0:3], v[32:35], v[208:211], v[0:3]\n"
        "ds_read_b64_tr_b16 v[62:63], v233 offset:25600\n"
        "s_lshl_b32 s90, s43, 19\n"
        "s_add_u32 s90, s90, s72\n"
        "v_mfma_f32_16x16x32_bf16 v[64:67], v[32:35], v[212:215], v[64:67]\n"
        "s_and_b32 s78, s45, 1\n"
        "s_lshl_b32 s78, s78, 15\n"
        "s_add_u32 s78, s78, 0x10000\n"
        "v_mfma_f32_16x16x32_bf16 v[96:99], v[32:35], v[216:219], v[96:99]\n"
        "s_and_b32 s80, s40, 1\n"
        "s_lshl_b32 s80, s80, 11\n"
        "s_lshl_b32 s82, s40, 4\n"
        "v_mfma_f32_16x16x32_bf16 v[128:131], v[32:35], v[220:223], v[128:131]\n"
        "v_accvgpr_write_b32 a48, 0\n"
        "v_accvgpr_write_b32 a49, 0\n"
        "v_mfma_f32_16x16x32_bf16 v[4:7], v[36:39], v[208:211], v[4:7]\n"
        "v_accvgpr_write_b32 a50, 0\n"
        "v_accvgpr_write_b32 a51, 0\n"
        "v_mfma_f32_16x16x32_bf16 v[68:71], v[36:39], v[212:215], v[68:71]\n"
        "v_accvgpr_write_b32 a52, 0\n"
        "v_accvgpr_write_b32 a53, 0\n"
        "v_mfma_f32_16x16x32_bf16 v[100:103], v[36:39], v[216:219], v[100:103]\n"
        "v_accvgpr_write_b32 a54, 0\n"
        "v_accvgpr_write_b32 a55, 0\n"
        "v_mfma_f32_16x16x32_bf16 v[132:135], v[36:39], v[220:223], v[132:135]\n"
        "s_sub_u32 s83, 14, s44\n"
        "v_accvgpr_write_b32 a56, 0\n"
        "v_accvgpr_write_b32 a57, 0\n"
        "v_mfma_f32_16x16x32_bf16 v[8:11], v[40:43], v[208:211], v[8:11]\n"
        "s_waitcnt vmcnt(14) lgkmcnt(0)\n"
        "s_barrier\n"
        "v_mfma_f32_16x16x32_bf16 v[72:75], v[40:43], v[212:215], v[72:75]\n"
        "global_load_lds_dwordx4 v238, s[68:69] offset:0\n"
        "ds_read_b64_tr_b16 v[32:33], v226 offset:32768\n"
        "v_mfma_f32_16x16x32_bf16 v[104:107], v[40:43], v[216:219], v[104:107]\n"
        "ds_read_b64_tr_b16 v[34:35], v227 offset:32768\n"
        "v_accvgpr_write_b32 a58, 0\n"
        "v_mfma_f32_16x16x32_bf16 v[136:139], v[40:43], v[220:223], v[136:139]\n"
        "ds_read_b64_tr_b16 v[36:37], v228 offset:32768\n"
        "v_accvgpr_write_b32 a59, 0\n"
        "v_mfma_f32_16x16x32_bf16 v[12:15], v[44:47], v[208:211], v[12:15]\n"
        "ds_read_b64_tr_b16 v[38:39], v229 offset:32768\n"
        "v_accvgpr_write_b32 a60, 0\n"
        "v_mfma_f32_16x16x32_bf16 v[76:79], v[44:47], v[212:215], v[76:79]\n"
        "ds_read_b64_tr_b16 v[40:41], v230 offset:32768\n"
        "global_load_lds_dwordx4 v239, s[68:69] offset:1024\n"
        "s_lshl_b32 s50, s41, 7\n"
        "v_mfma_f32_16x16x32_bf16 v[108:111], v[44:47], v[216:219], v[108:111]\n"
        "ds_read_b64_tr_b16 v[42:43], v231 offset:32768\n"
        "s_lshl2_add_u32 s50, s42, s50\n"
        "v_accvgpr_write_b32 a61, 0\n"
        "v_mfma_f32_16x16x32_bf16 v[140:143], v[44:47], v[220:223], v[140:143]\n"
        "ds_read_b128 v[192:195], v246 offset:2048\n"
        "s_add_u32 s50, s50, s40\n"
        "v_accvgpr_read_b32 v184, a92\n"
        "v_mfma_f32_16x16x32_bf16 v[16:19], v[48:51], v[208:211], v[16:19]\n"
        "ds_read_b64_tr_b16 v[44:45], v232 offset:32768\n"
        "s_lshl_b32 s50, s50, 12\n"
        "v_cvt_pk_bf16_f32 v184, v184, v185\n"
        "v_mfma_f32_16x16x32_bf16 v[80:83], v[48:51], v[212:215], v[80:83]\n"
        "ds_read_b64_tr_b16 v[46:47], v233 offset:32768\n"
        "global_load_lds_dwordx4 v240, s[68:69] offset:2048\n"
        "s_mul_i32 s51, s40, 8320\n"
        "v_mfma_f32_16x16x32_bf16 v[112:115], v[48:51], v[216:219], v[112:115]\n"
        "ds_read_b128 v[196:199], v246 offset:0\n"
        "s_add_u32 s51, s51, 0x18000\n"
        "v_cvt_pk_bf16_f32 v185, v186, v187\n"
        "v_mfma_f32_16x16x32_bf16 v[144:147], v[48:51], v[220:223], v[144:147]\n"
        "ds_read_b64_tr_b16 v[48:49], v226 offset:33792\n"
        "v_cvt_pk_bf16_f32 v186, v188, v189\n"
        "v_mfma_f32_16x16x32_bf16 v[20:23], v[52:55], v[208:211], v[20:23]\n"
        "ds_read_b64_tr_b16 v[50:51], v227 offset:33792\n"
        "v_cvt_pk_bf16_f32 v187, v190, v191\n"
        "v_mfma_f32_16x16x32_bf16 v[84:87], v[52:55], v[212:215], v[84:87]\n"
        "ds_read_b128 v[200:203], v246 offset:6144\n"
        "v_accvgpr_write_b32 a28, v184\n"
        "v_mfma_f32_16x16x32_bf16 v[116:119], v[52:55], v[216:219], v[116:119]\n"
        "global_load_lds_dwordx4 v241, s[68:69] offset:3072\n"
        "s_add_u32 m0, s72, 0x8000\n"
        "v_accvgpr_write_b32 a29, v185\n"
        "v_mfma_f32_16x16x32_bf16 v[148:151], v[52:55], v[220:223], v[148:151]\n"
        "ds_read_b64_tr_b16 v[52:53], v228 offset:33792\n"
        "v_accvgpr_write_b32 a30, v186\n"
        "v_mfma_f32_16x16x32_bf16 v[24:27], v[56:59], v[208:211], v[24:27]\n"
        "ds_read_b64_tr_b16 v[54:55], v229 offset:33792\n"
        "v_accvgpr_write_b32 a31, v187\n"
        "v_mfma_f32_16x16x32_bf16 v[88:91], v[56:59], v[212:215], v[88:91]\n"
        "ds_read_b128 v[204:207], v246 offset:4096\n"
        "s_add_u32 s68, s68, 0x80000\n"
        "s_addc_u32 s69, s69, 0\n"
        "v_mfma_f32_16x16x32_bf16 v[120:123], v[56:59], v[216:219], v[120:123]\n"
        "global_store_dwordx4 v254, a[240:243], s[70:71] offset:1024 sc1\n"
        "s_waitcnt lgkmcnt(6)\n"
        "v_accvgpr_write_b32 a62, 0\n"
        "v_accvgpr_write_b32 a63, 0\n"
        "v_mfma_f32_16x16x32_bf16 v[152:155], v[56:59], v[220:223], v[152:155]\n"
        "ds_read_b64_tr_b16 v[56:57], v230 offset:33792\n"
        "s_add_u32 s92, s26, s50\n"
        "s_addc_u32 s93, s27, 0\n"
        "v_mfma_f32_16x16x32_bf16 v[28:31], v[60:63], v[208:211], v[28:31]\n"
        "ds_read_b64_tr_b16 v[58:59], v231 offset:33792\n"
        "s_add_u32 s50, s50, 0x3000000\n"
        "s_waitcnt lgkmcnt(5)\n"
        "v_mfma_f32_16x16x32_bf16 v[92:95], v[60:63], v[212:215], v[92:95]\n"
        "global_load_lds_dwordx4 v242, s[64:65] offset:0\n"
        "v_mfma_f32_16x16x32_bf16 v[124:127], v[60:63], v[216:219], v[124:127]\n"
        "ds_read_b128 v[208:211], v247 offset:2048\n"
        "s_add_u32 s94, s38, s50\n"
        "s_addc_u32 s95, s39, 0\n"
        "v_mfma_f32_16x16x32_bf16 v[156:159], v[60:63], v[220:223], v[156:159]\n"
        "ds_read_b64_tr_b16 v[60:61], v232 offset:33792\n"
        "s_add_u32 s50, s51, 4128\n"
        "s_waitcnt lgkmcnt(5)\n"
        "v_mfma_f32_16x16x32_bf16 v[0:3], v[32:35], v[192:195], v[0:3]\n"
        "ds_read_b64_tr_b16 v[62:63], v233 offset:33792\n"
        "s_waitcnt lgkmcnt(5)\n"
        "v_mfma_f32_16x16x32_bf16 v[64:67], v[32:35], v[196:199], v[64:67]\n"
        "global_load_lds_dwordx4 v243, s[64:65] offset:1024\n"
        "v_mfma_f32_16x16x32_bf16 v[96:99], v[32:35], v[200:203], v[96:99]\n"
        "ds_read_b128 v[212:215], v247 offset:0\n"
        "v_mfma_f32_16x16x32_bf16 v[128:131], v[32:35], v[204:207], v[128:131]\n"
        "ds_read_b64_tr_b16 v[32:33], v226 offset:40960\n"
        "v_mfma_f32_16x16x32_bf16 v[4:7], v[36:39], v[192:195], v[4:7]\n"
        "ds_read_b64_tr_b16 v[34:35], v227 offset:40960\n"
        "v_mfma_f32_16x16x32_bf16 v[68:71], v[36:39], v[196:199], v[68:71]\n"
        "global_load_lds_dwordx4 v244, s[64:65] offset:2048\n"
        "v_mfma_f32_16x16x32_bf16 v[100:103], v[36:39], v[200:203], v[100:103]\n"
        "ds_read_b128 v[216:219], v247 offset:6144\n"
        "v_mfma_f32_16x16x32_bf16 v[132:135], v[36:39], v[204:207], v[132:135]\n"
        "ds_read_b64_tr_b16 v[36:37], v228 offset:40960\n"
        "v_mfma_f32_16x16x32_bf16 v[8:11], v[40:43], v[192:195], v[8:11]\n"
        "ds_read_b64_tr_b16 v[38:39], v229 offset:40960\n"
        "v_mfma_f32_16x16x32_bf16 v[72:75], v[40:43], v[196:199], v[72:75]\n"
        "global_load_lds_dwordx4 v245, s[64:65] offset:3072\n"
        "s_add_u32 m0, s72, 0x9000\n"
        "v_mfma_f32_16x16x32_bf16 v[104:107], v[40:43], v[200:203], v[104:107]\n"
        "ds_read_b128 v[220:223], v247 offset:4096\n"
        "s_add_u32 s64, s64, 0x80\n"
        "s_addc_u32 s65, s65, 0\n"
        "v_mfma_f32_16x16x32_bf16 v[136:139], v[40:43], v[204:207], v[136:139]\n"
        "ds_read_b64_tr_b16 v[40:41], v230 offset:40960\n"
        "v_mfma_f32_16x16x32_bf16 v[12:15], v[44:47], v[192:195], v[12:15]\n"
        "ds_read_b64_tr_b16 v[42:43], v231 offset:40960\n"
        "v_mfma_f32_16x16x32_bf16 v[76:79], v[44:47], v[196:199], v[76:79]\n"
        "global_load_lds_dwordx4 v242, s[66:67] offset:0\n"
        "v_mfma_f32_16x16x32_bf16 v[108:111], v[44:47], v[200:203], v[108:111]\n"
        "v_mfma_f32_16x16x32_bf16 v[140:143], v[44:47], v[204:207], v[140:143]\n"
        "ds_read_b64_tr_b16 v[44:45], v232 offset:40960\n"
        "v_mfma_f32_16x16x32_bf16 v[16:19], v[48:51], v[192:195], v[16:19]\n"
        "ds_read_b64_tr_b16 v[46:47], v233 offset:40960\n"
        "v_mfma_f32_16x16x32_bf16 v[80:83], v[48:51], v[196:199], v[80:83]\n"
        "global_load_lds_dwordx4 v243, s[66:67] offset:1024\n"
        "v_mfma_f32_16x16x32_bf16 v[112:115], v[48:51], v[200:203], v[112:115]\n"
        "s_waitcnt lgkmcnt(4)\n"
        "v_mfma_f32_16x16x32_bf16 v[144:147], v[48:51], v[204:207], v[144:147]\n"
        "ds_read_b64_tr_b16 v[48:49], v226 offset:41984\n"
        "v_mfma_f32_16x16x32_bf16 v[20:23], v[52:55], v[192:195], v[20:23]\n"
        "ds_read_b64_tr_b16 v[50:51], v227 offset:41984\n"
        "v_mfma_f32_16x16x32_bf16 v[84:87], v[52:55], v[196:199], v[84:87]\n"
        "global_load_lds_dwordx4 v244, s[66:67] offset:2048\n"
        "v_mfma_f32_16x16x32_bf16 v[116:119], v[52:55], v[200:203], v[116:119]\n"
        "v_mfma_f32_16x16x32_bf16 v[148:151], v[52:55], v[204:207], v[148:151]\n"
        "ds_read_b64_tr_b16 v[52:53], v228 offset:41984\n"
        "v_mfma_f32_16x16x32_bf16 v[24:27], v[56:59], v[192:195], v[24:27]\n"
        "ds_read_b64_tr_b16 v[54:55], v229 offset:41984\n"
        "v_mfma_f32_16x16x32_bf16 v[88:91], v[56:59], v[196:199], v[88:91]\n"
        "global_load_lds_dwordx4 v245, s[66:67] offset:3072\n"
        "s_add_u32 m0, s73, 0x20000\n"
        "v_mfma_f32_16x16x32_bf16 v[120:123], v[56:59], v[200:203], v[120:123]\n"
        "s_add_u32 s66, s66, 0x80\n"
        "s_addc_u32 s67, s67, 0\n"
        "s_mov_b32 s79, s72\n"
        "v_mfma_f32_16x16x32_bf16 v[152:155], v[56:59], v[204:207], v[152:155]\n"
        "ds_read_b64_tr_b16 v[56:57], v230 offset:41984\n"
        "s_xor_b32 s79, s79, 0x8000\n"
        "v_mfma_f32_16x16x32_bf16 v[28:31], v[60:63], v[192:195], v[28:31]\n"
        "ds_read_b64_tr_b16 v[58:59], v231 offset:41984\n"
        "v_mfma_f32_16x16x32_bf16 v[92:95], v[60:63], v[196:199], v[92:95]\n"
        "v_mfma_f32_16x16x32_bf16 v[124:127], v[60:63], v[200:203], v[124:127]\n"
        "v_mfma_f32_16x16x32_bf16 v[156:159], v[60:63], v[204:207], v[156:159]\n"
        "ds_read_b64_tr_b16 v[60:61], v232 offset:41984\n"
        "v_mfma_f32_16x16x32_bf16 v[0:3], v[32:35], v[208:211], v[0:3]\n"
        "ds_read_b64_tr_b16 v[62:63], v233 offset:41984\n"
        "v_mfma_f32_16x16x32_bf16 v[64:67], v[32:35], v[212:215], v[64:67]\n"
        "v_mfma_f32_16x16x32_bf16 v[96:99], v[32:35], v[216:219], v[96:99]\n"
        "s_waitcnt lgkmcnt(4)\n"
        "v_mfma_f32_16x16x32_bf16 v[128:131], v[32:35], v[220:223], v[128:131]\n"
        "global_store_dwordx4 v254, a[212:215], s[70:71] offset:2048 sc1\n"
        "v_mfma_f32_16x16x32_bf16 v[4:7], v[36:39], v[208:211], v[4:7]\n"
        "v_mfma_f32_16x16x32_bf16 v[68:71], v[36:39], v[212:215], v[68:71]\n"
        "v_mfma_f32_16x16x32_bf16 v[100:103], v[36:39], v[216:219], v[100:103]\n"
        "v_mfma_f32_16x16x32_bf16 v[132:135], v[36:39], v[220:223], v[132:135]\n"
        "s_waitcnt vmcnt(14) lgkmcnt(0)\n"
        "s_barrier\n"
        "v_mfma_f32_16x16x32_bf16 v[8:11], v[40:43], v[208:211], v[8:11]\n"
        "global_load_lds_dwordx4 v238, s[68:69] offset:0\n"
        "ds_read_b64_tr_b16 v[32:33], v226 offset:49152\n"
        "v_mfma_f32_16x16x32_bf16 v[72:75], v[40:43], v[212:215], v[72:75]\n"
        "ds_read_b64_tr_b16 v[34:35], v227 offset:49152\n"
        "v_mfma_f32_16x16x32_bf16 v[104:107], v[40:43], v[216:219], v[104:107]\n"
        "ds_read_b64_tr_b16 v[36:37], v228 offset:49152\n"
        "v_mfma_f32_16x16x32_bf16 v[136:139], v[40:43], v[220:223], v[136:139]\n"
        "ds_read_b64_tr_b16 v[38:39], v229 offset:49152\n"
        "v_mfma_f32_16x16x32_bf16 v[12:15], v[44:47], v[208:211], v[12:15]\n"
        "ds_read_b64_tr_b16 v[40:41], v230 offset:49152\n"
        "global_load_lds_dwordx4 v239, s[68:69] offset:1024\n"
        "v_mfma_f32_16x16x32_bf16 v[76:79], v[44:47], v[212:215], v[76:79]\n"
        "ds_read_b64_tr_b16 v[42:43], v231 offset:49152\n"
        "v_mfma_f32_16x16x32_bf16 v[108:111], v[44:47], v[216:219], v[108:111]\n"
        "ds_read_b128 v[192:195], v248 offset:2048\n"
        "v_mfma_f32_16x16x32_bf16 v[140:143], v[44:47], v[220:223], v[140:143]\n"
        "ds_read_b64_tr_b16 v[44:45], v232 offset:49152\n"
        "v_mfma_f32_16x16x32_bf16 v[16:19], v[48:51], v[208:211], v[16:19]\n"
        "ds_read_b64_tr_b16 v[46:47], v233 offset:49152\n"
        "global_load_lds_dwordx4 v240, s[68:69] offset:2048\n"
        "v_mfma_f32_16x16x32_bf16 v[80:83], v[48:51], v[212:215], v[80:83]\n"
        "ds_read_b128 v[196:199], v248 offset:0\n"
        "v_add_u32 v240, s78, v254\n"
        "v_mfma_f32_16x16x32_bf16 v[112:115], v[48:51], v[216:219], v[112:115]\n"
        "ds_read_b128 v[200:203], v248 offset:6144\n"
        "s_xor_b32 s78, s78, 0x8000\n"
        "v_mfma_f32_16x16x32_bf16 v[144:147], v[48:51], v[220:223], v[144:147]\n"
        "ds_read_b64_tr_b16 v[48:49], v226 offset:50176\n"
        "v_mfma_f32_16x16x32_bf16 v[20:23], v[52:55], v[208:211], v[20:23]\n"
        "ds_read_b64_tr_b16 v[50:51], v227 offset:50176\n"
        "global_load_lds_dwordx4 v241, s[68:69] offset:3072\n"
        "s_add_u32 m0, s72, 0x0\n"
        "v_mfma_f32_16x16x32_bf16 v[84:87], v[52:55], v[212:215], v[84:87]\n"
        "ds_read_b128 v[204:207], v248 offset:4096\n"
        "v_mov_b32 v241, v254\n"
        "v_mfma_f32_16x16x32_bf16 v[116:119], v[52:55], v[216:219], v[116:119]\n"
        "v_mfma_f32_16x16x32_bf16 v[148:151], v[52:55], v[220:223], v[148:151]\n"
        "ds_read_b64_tr_b16 v[52:53], v228 offset:50176\n"
        "v_mfma_f32_16x16x32_bf16 v[24:27], v[56:59], v[208:211], v[24:27]\n"
        "ds_read_b64_tr_b16 v[54:55], v229 offset:50176\n"
        "global_load_lds_dwordx4 v242, s[64:65] offset:0\n"
        "v_mfma_f32_16x16x32_bf16 v[88:91], v[56:59], v[212:215], v[88:91]\n"
        "v_mfma_f32_16x16x32_bf16 v[120:123], v[56:59], v[216:219], v[120:123]\n"
        "s_waitcnt lgkmcnt(5)\n"
        "v_mfma_f32_16x16x32_bf16 v[152:155], v[56:59], v[220:223], v[152:155]\n"
        "ds_read_b64_tr_b16 v[56:57], v230 offset:50176\n"
        "v_mfma_f32_16x16x32_bf16 v[28:31], v[60:63], v[208:211], v[28:31]\n"
        "ds_read_b64_tr_b16 v[58:59], v231 offset:50176\n"
        "global_load_lds_dwordx4 v243, s[64:65] offset:1024\n"
        "v_mfma_f32_16x16x32_bf16 v[92:95], v[60:63], v[212:215], v[92:95]\n"
        "ds_read_b128 v[208:211], v249 offset:2048\n"
        "v_xor_b32 v241, 0x8000, v241\n"
        "v_mfma_f32_16x16x32_bf16 v[124:127], v[60:63], v[216:219], v[124:127]\n"
        "ds_read_b128 v[212:215], v249 offset:0\n"
        "s_waitcnt lgkmcnt(6)\n"
        "v_mfma_f32_16x16x32_bf16 v[156:159], v[60:63], v[220:223], v[156:159]\n"
        "ds_read_b64_tr_b16 v[60:61], v232 offset:50176\n"
        "v_mfma_f32_16x16x32_bf16 v[0:3], v[32:35], v[192:195], v[0:3]\n"
        "ds_read_b64_tr_b16 v[62:63], v233 offset:50176\n"
        "global_load_lds_dwordx4 v244, s[64:65] offset:2048\n"
        "v_mfma_f32_16x16x32_bf16 v[64:67], v[32:35], v[196:199], v[64:67]\n"
        "ds_read_b128 v[216:219], v249 offset:6144\n"
        "v_mfma_f32_16x16x32_bf16 v[96:99], v[32:35], v[200:203], v[96:99]\n"
        "ds_read_b128 v[220:223], v249 offset:4096\n"
        "v_mfma_f32_16x16x32_bf16 v[128:131], v[32:35], v[204:207], v[128:131]\n"
        "ds_read_b64_tr_b16 v[32:33], v226 offset:57344\n"
        "v_mfma_f32_16x16x32_bf16 v[4:7], v[36:39], v[192:195], v[4:7]\n"
        "ds_read_b64_tr_b16 v[34:35], v227 offset:57344\n"
        "global_load_lds_dwordx4 v245, s[64:65] offset:3072\n"
        "s_add_u32 m0, s72, 0x1000\n"
        "v_mfma_f32_16x16x32_bf16 v[68:71], v[36:39], v[196:199], v[68:71]\n"
        "s_add_u32 s64, s38, s90\n"
        "s_addc_u32 s65, s39, 0\n"
        "s_lshl_b32 s90, s40, 14\n"
        "v_mfma_f32_16x16x32_bf16 v[100:103], v[36:39], v[200:203], v[100:103]\n"
        "s_and_b32 s90, s90, 0x8000\n"
        "s_xor_b32 s91, s78, s90\n"
        "s_add_u32 s91, s91, s80\n"
        "v_mfma_f32_16x16x32_bf16 v[132:135], v[36:39], v[204:207], v[132:135]\n"
        "ds_read_b64_tr_b16 v[36:37], v228 offset:57344\n"
        "s_add_u32 s78, s78, s72\n"
        "s_xor_b32 s78, s78, 0x8000\n"
        "v_mfma_f32_16x16x32_bf16 v[8:11], v[40:43], v[192:195], v[8:11]\n"
        "ds_read_b64_tr_b16 v[38:39], v229 offset:57344\n"
        "global_load_lds_dwordx4 v242, s[66:67] offset:0\n"
        "v_mfma_f32_16x16x32_bf16 v[72:75], v[40:43], v[196:199], v[72:75]\n"
        "v_lshrrev_b32 v242, 6, v254\n"
        "v_and_b32 v242, 12, v242\n"
        "v_mfma_f32_16x16x32_bf16 v[104:107], v[40:43], v[200:203], v[104:107]\n"
        "v_mfma_f32_16x16x32_bf16 v[136:139], v[40:43], v[204:207], v[136:139]\n"
        "ds_read_b64_tr_b16 v[40:41], v230 offset:57344\n"
        "v_mfma_f32_16x16x32_bf16 v[12:15], v[44:47], v[192:195], v[12:15]\n"
        "ds_read_b64_tr_b16 v[42:43], v231 offset:57344\n"
        "global_load_lds_dwordx4 v243, s[66:67] offset:1024\n"
        "v_mfma_f32_16x16x32_bf16 v[76:79], v[44:47], v[196:199], v[76:79]\n"
        "v_bfe_u32 v243, v254, 4, 4\n"
        "v_sub_u32 v243, v243, v242\n"
        "v_mfma_f32_16x16x32_bf16 v[108:111], v[44:47], v[200:203], v[108:111]\n"
        "s_waitcnt lgkmcnt(4)\n"
        "v_add_u32 v243, s82, v243\n"
        "v_cmp_gt_i32 s[10:11], 0, v243\n"
        "v_mfma_f32_16x16x32_bf16 v[140:143], v[44:47], v[204:207], v[140:143]\n"
        "ds_read_b64_tr_b16 v[44:45], v232 offset:57344\n"
        "v_cmp_gt_i32 s[12:13], 1, v243\n"
        "v_mfma_f32_16x16x32_bf16 v[16:19], v[48:51], v[192:195], v[16:19]\n"
        "ds_read_b64_tr_b16 v[46:47], v233 offset:57344\n"
        "global_load_lds_dwordx4 v244, s[66:67] offset:2048\n"
        "v_mfma_f32_16x16x32_bf16 v[80:83], v[48:51], v[196:199], v[80:83]\n"
        "v_add_u32 v244, s91, v254\n"
        "s_lshl1_add_u32 s91, s80, s90\n"
        "v_mfma_f32_16x16x32_bf16 v[112:115], v[48:51], v[200:203], v[112:115]\n"
        "v_cmp_gt_i32 s[14:15], 2, v243\n"
        "v_cmp_gt_i32 s[16:17], 3, v243\n"
        "v_mfma_f32_16x16x32_bf16 v[144:147], v[48:51], v[204:207], v[144:147]\n"
        "ds_read_b64_tr_b16 v[48:49], v226 offset:58368\n"
        "v_cmp_gt_i32 s[20:21], 16, v243\n"
        "v_mfma_f32_16x16x32_bf16 v[20:23], v[52:55], v[192:195], v[20:23]\n"
        "ds_read_b64_tr_b16 v[50:51], v227 offset:58368\n"
        "global_load_lds_dwordx4 v245, s[66:67] offset:3072\n"
        "v_mfma_f32_16x16x32_bf16 v[84:87], v[52:55], v[196:199], v[84:87]\n"
        "s_add_u32 s66, s64, 0x1000000\n"
        "s_addc_u32 s67, s65, 0\n"
        "v_add_u32 v245, s91, v254\n"
        "v_mfma_f32_16x16x32_bf16 v[116:119], v[52:55], v[200:203], v[116:119]\n"
        "v_cmp_gt_i32 s[22:23], 17, v243\n"
        "v_cmp_gt_i32 s[24:25], 18, v243\n"
        "v_mfma_f32_16x16x32_bf16 v[148:151], v[52:55], v[204:207], v[148:151]\n"
        "ds_read_b64_tr_b16 v[52:53], v228 offset:58368\n"
        "v_cmp_gt_i32 s[28:29], 19, v243\n"
        "v_mfma_f32_16x16x32_bf16 v[24:27], v[56:59], v[192:195], v[24:27]\n"
        "ds_read_b64_tr_b16 v[54:55], v229 offset:58368\n"
        "v_cmp_gt_i32 s[30:31], 32, v243\n"
        "v_mfma_f32_16x16x32_bf16 v[88:91], v[56:59], v[196:199], v[88:91]\n"
        "global_store_dwordx4 v254, a[244:247], s[70:71] offset:3072 sc1\n"
        "s_add_u32 s70, s38, s97\n"
        "s_addc_u32 s71, s39, 0\n"
        "s_mov_b32 s82, -1\n"
        "v_cmp_gt_i32 s[56:57], 33, v243\n"
        "v_cmp_gt_i32 s[58:59], 34, v243\n"
        "v_mfma_f32_16x16x32_bf16 v[120:123], v[56:59], v[200:203], v[120:123]\n"
        "s_add_u32 s97, s53, 0x0\n"
        "v_cmp_gt_i32 s[60:61], 35, v243\n"
        "v_cmp_gt_i32 s[62:63], 48, v243\n"
        "v_mfma_f32_16x16x32_bf16 v[152:155], v[56:59], v[204:207], v[152:155]\n"
        "ds_read_b64_tr_b16 v[56:57], v230 offset:58368\n"
        "s_add_u32 s82, s82, 1\n"
        "v_cmp_gt_i32 s[84:85], 49, v243\n"
        "v_mfma_f32_16x16x32_bf16 v[28:31], v[60:63], v[192:195], v[28:31]\n"
        "ds_read_b64_tr_b16 v[58:59], v231 offset:58368\n"
        "v_cmp_gt_i32 s[86:87], 50, v243\n"
        "v_mfma_f32_16x16x32_bf16 v[92:95], v[60:63], v[196:199], v[92:95]\n"
        "s_cmp_ge_u32 s82, s44\n"
        "s_addc_u32 s90, s82, 0\n"
        "v_cmp_gt_i32 s[88:89], 51, v243\n"
        "v_mfma_f32_16x16x32_bf16 v[124:127], v[60:63], v[200:203], v[124:127]\n"
        "s_waitcnt lgkmcnt(4)\n"
        "v_mov_b32 v242, 0x3f803f80\n"
        "v_accvgpr_write_b32 a80, v242\n"
        "v_mfma_f32_16x16x32_bf16 v[156:159], v[60:63], v[204:207], v[156:159]\n"
        "ds_read_b64_tr_b16 v[60:61], v232 offset:58368\n"
        "s_min_u32 s90, s90, 15\n"
        "v_accvgpr_write_b32 a81, v242\n"
        "v_mfma_f32_16x16x32_bf16 v[0:3], v[32:35], v[208:211], v[0:3]\n"
        "ds_read_b64_tr_b16 v[62:63], v233 offset:58368\n"
        "s_lshl_b32 s90, s90, 15\n"
        "v_accvgpr_write_b32 a82, v242\n"
        "v_mfma_f32_16x16x32_bf16 v[64:67], v[32:35], v[212:215], v[64:67]\n"
        "s_add_u32 s68, s64, s90\n"
        "s_addc_u32 s69, s65, 0\n"
        "v_accvgpr_write_b32 a83, v242\n"
        "v_mfma_f32_16x16x32_bf16 v[96:99], v[32:35], v[216:219], v[96:99]\n"
        "s_add_u32 s74, s68, 0x1000\n"
        "s_addc_u32 s75, s69, 0\n"
        "v_mov_b32 v242, 0xff800000\n"
        "v_mfma_f32_16x16x32_bf16 v[128:131], v[32:35], v[220:223], v[128:131]\n"
        "s_add_u32 s76, s66, s90\n"
        "s_addc_u32 s77, s67, 0\n"
        "v_mfma_f32_16x16x32_bf16 v[4:7], v[36:39], v[208:211], v[4:7]\n"
        "v_mfma_f32_16x16x32_bf16 v[68:71], v[36:39], v[212:215], v[68:71]\n"
        "v_mfma_f32_16x16x32_bf16 v[100:103], v[36:39], v[216:219], v[100:103]\n"
        "v_mfma_f32_16x16x32_bf16 v[132:135], v[36:39], v[220:223], v[132:135]\n"
        "v_mfma_f32_16x16x32_bf16 v[8:11], v[40:43], v[208:211], v[8:11]\n"
        "s_waitcnt vmcnt(14) lgkmcnt(0)\n"
        "s_barrier\n"
        "v_mfma_f32_16x16x32_bf16 v[72:75], v[40:43], v[212:215], v[72:75]\n"
        "ds_read_b64_tr_b16 v[32:33], v226 offset:16384\n"
        "v_mfma_f32_16x16x32_bf16 v[104:107], v[40:43], v[216:219], v[104:107]\n"
        "ds_read_b64_tr_b16 v[34:35], v227 offset:16384\n"
        "v_mfma_f32_16x16x32_bf16 v[136:139], v[40:43], v[220:223], v[136:139]\n"
        "ds_read_b64_tr_b16 v[36:37], v228 offset:16384\n"
        "v_mfma_f32_16x16x32_bf16 v[12:15], v[44:47], v[208:211], v[12:15]\n"
        "ds_read_b64_tr_b16 v[38:39], v229 offset:16384\n"
        "v_mfma_f32_16x16x32_bf16 v[76:79], v[44:47], v[212:215], v[76:79]\n"
        "ds_read_b64_tr_b16 v[40:41], v230 offset:16384\n"
        "v_mfma_f32_16x16x32_bf16 v[108:111], v[44:47], v[216:219], v[108:111]\n"
        "ds_read_b64_tr_b16 v[42:43], v231 offset:16384\n"
        "v_mfma_f32_16x16x32_bf16 v[140:143], v[44:47], v[220:223], v[140:143]\n"
        "ds_read_b128 v[192:195], v246 offset:34816\n"
        "v_mfma_f32_16x16x32_bf16 v[16:19], v[48:51], v[208:211], v[16:19]\n"
        "ds_read_b64_tr_b16 v[44:45], v232 offset:16384\n"
        "v_mfma_f32_16x16x32_bf16 v[80:83], v[48:51], v[212:215], v[80:83]\n"
        "ds_read_b64_tr_b16 v[46:47], v233 offset:16384\n"
        "v_mfma_f32_16x16x32_bf16 v[112:115], v[48:51], v[216:219], v[112:115]\n"
        "ds_read_b128 v[196:199], v246 offset:32768\n"
        "v_mfma_f32_16x16x32_bf16 v[144:147], v[48:51], v[220:223], v[144:147]\n"
        "ds_read_b64_tr_b16 v[48:49], v226 offset:17408\n"
        "v_mfma_f32_16x16x32_bf16 v[20:23], v[52:55], v[208:211], v[20:23]\n"
        "ds_read_b64_tr_b16 v[50:51], v227 offset:17408\n"
        "v_mfma_f32_16x16x32_bf16 v[84:87], v[52:55], v[212:215], v[84:87]\n"
        "ds_read_b128 v[200:203], v246 offset:38912\n"
        "v_mfma_f32_16x16x32_bf16 v[116:119], v[52:55], v[216:219], v[116:119]\n"
        "ds_read_b128 v[204:207], v246 offset:36864\n"
        "v_mfma_f32_16x16x32_bf16 v[148:151], v[52:55], v[220:223], v[148:151]\n"
        "ds_read_b64_tr_b16 v[52:53], v228 offset:17408\n"
        "v_mfma_f32_16x16x32_bf16 v[24:27], v[56:59], v[208:211], v[24:27]\n"
        "ds_read_b64_tr_b16 v[54:55], v229 offset:17408\n"
        "v_mfma_f32_16x16x32_bf16 v[88:91], v[56:59], v[212:215], v[88:91]\n"
        "v_mfma_f32_16x16x32_bf16 v[120:123], v[56:59], v[216:219], v[120:123]\n"
        "s_waitcnt lgkmcnt(6)\n"
        "v_mfma_f32_16x16x32_bf16 v[152:155], v[56:59], v[220:223], v[152:155]\n"
        "ds_read_b64_tr_b16 v[56:57], v230 offset:17408\n"
        "v_mfma_f32_16x16x32_bf16 v[28:31], v[60:63], v[208:211], v[28:31]\n"
        "ds_read_b64_tr_b16 v[58:59], v231 offset:17408\n"
        "s_waitcnt lgkmcnt(6)\n"
        "v_mfma_f32_16x16x32_bf16 v[92:95], v[60:63], v[212:215], v[92:95]\n"
        "global_store_dwordx4 v254, a[216:219], s[70:71] offset:0 sc1\n"
        "ds_read_b128 v[208:211], v247 offset:34816\n"
        "ds_read_b128 v[212:215], v247 offset:32768\n"
        "s_waitcnt lgkmcnt(6)\n"
        "v_mfma_f32_16x16x32_bf16 v[124:127], v[60:63], v[216:219], v[124:127]\n"
        "ds_read_b128 v[216:219], v247 offset:38912\n"
        "v_mfma_f32_16x16x32_bf16 v[156:159], v[60:63], v[220:223], v[156:159]\n"
        "ds_read_b64_tr_b16 v[60:61], v232 offset:17408\n"
        "v_mfma_f32_16x16x32_bf16 v[0:3], v[32:35], v[192:195], v[0:3]\n"
        "ds_read_b64_tr_b16 v[62:63], v233 offset:17408\n"
        "v_mfma_f32_16x16x32_bf16 v[64:67], v[32:35], v[196:199], v[64:67]\n"
        "ds_read_b128 v[220:223], v247 offset:36864\n"
        "v_mfma_f32_16x16x32_bf16 v[96:99], v[32:35], v[200:203], v[96:99]\n"
        "v_mfma_f32_16x16x32_bf16 v[128:131], v[32:35], v[204:207], v[128:131]\n"
        "ds_read_b64_tr_b16 v[32:33], v226 offset:24576\n"
        "v_mfma_f32_16x16x32_bf16 v[4:7], v[36:39], v[192:195], v[4:7]\n"
        "ds_read_b64_tr_b16 v[34:35], v227 offset:24576\n"
        "v_mfma_f32_16x16x32_bf16 v[68:71], v[36:39], v[196:199], v[68:71]\n"
        "v_mfma_f32_16x16x32_bf16 v[100:103], v[36:39], v[200:203], v[100:103]\n"
        "v_mfma_f32_16x16x32_bf16 v[132:135], v[36:39], v[204:207], v[132:135]\n"
        "ds_read_b64_tr_b16 v[36:37], v228 offset:24576\n"
        "v_mfma_f32_16x16x32_bf16 v[8:11], v[40:43], v[192:195], v[8:11]\n"
        "ds_read_b64_tr_b16 v[38:39], v229 offset:24576\n"
        "v_mfma_f32_16x16x32_bf16 v[72:75], v[40:43], v[196:199], v[72:75]\n"
        "v_mfma_f32_16x16x32_bf16 v[104:107], v[40:43], v[200:203], v[104:107]\n"
        "v_mfma_f32_16x16x32_bf16 v[136:139], v[40:43], v[204:207], v[136:139]\n"
        "ds_read_b64_tr_b16 v[40:41], v230 offset:24576\n"
        "v_mfma_f32_16x16x32_bf16 v[12:15], v[44:47], v[192:195], v[12:15]\n"
        "ds_read_b64_tr_b16 v[42:43], v231 offset:24576\n"
        "v_mfma_f32_16x16x32_bf16 v[76:79], v[44:47], v[196:199], v[76:79]\n"
        "v_mfma_f32_16x16x32_bf16 v[108:111], v[44:47], v[200:203], v[108:111]\n"
        "s_waitcnt lgkmcnt(4)\n"
        "v_mfma_f32_16x16x32_bf16 v[140:143], v[44:47], v[204:207], v[140:143]\n"
        "ds_read_b64_tr_b16 v[44:45], v232 offset:24576\n"
        "v_mfma_f32_16x16x32_bf16 v[16:19], v[48:51], v[192:195], v[16:19]\n"
        "ds_read_b64_tr_b16 v[46:47], v233 offset:24576\n"
        "v_mfma_f32_16x16x32_bf16 v[80:83], v[48:51], v[196:199], v[80:83]\n"
        "v_mfma_f32_16x16x32_bf16 v[112:115], v[48:51], v[200:203], v[112:115]\n"
        "v_mfma_f32_16x16x32_bf16 v[144:147], v[48:51], v[204:207], v[144:147]\n"
        "ds_read_b64_tr_b16 v[48:49], v226 offset:25600\n"
        "v_mfma_f32_16x16x32_bf16 v[20:23], v[52:55], v[192:195], v[20:23]\n"
        "ds_read_b64_tr_b16 v[50:51], v227 offset:25600\n"
        "s_lshl_b32 s80, s41, 5\n"
        "s_add_u32 s80, s80, 256\n"
        "v_mfma_f32_16x16x32_bf16 v[84:87], v[52:55], v[196:199], v[84:87]\n"
        "s_add_u32 s81, s80, s42\n"
        "s_lshl_b32 s81, s81, 7\n"
        "s_lshl_b32 s80, s80, 7\n"
        "v_mfma_f32_16x16x32_bf16 v[116:119], v[52:55], v[200:203], v[116:119]\n"
        "v_mfma_f32_16x16x32_bf16 v[148:151], v[52:55], v[204:207], v[148:151]\n"
        "ds_read_b64_tr_b16 v[52:53], v228 offset:25600\n"
        "v_mfma_f32_16x16x32_bf16 v[24:27], v[56:59], v[192:195], v[24:27]\n"
        "ds_read_b64_tr_b16 v[54:55], v229 offset:25600\n"
        "v_mfma_f32_16x16x32_bf16 v[88:91], v[56:59], v[196:199], v[88:91]\n"
        "v_mfma_f32_16x16x32_bf16 v[120:123], v[56:59], v[200:203], v[120:123]\n"
        "v_mfma_f32_16x16x32_bf16 v[152:155], v[56:59], v[204:207], v[152:155]\n"
        "ds_read_b64_tr_b16 v[56:57], v230 offset:25600\n"
        "v_mfma_f32_16x16x32_bf16 v[28:31], v[60:63], v[192:195], v[28:31]\n"
        "ds_read_b64_tr_b16 v[58:59], v231 offset:25600\n"
        "v_mfma_f32_16x16x32_bf16 v[92:95], v[60:63], v[196:199], v[92:95]\n"
        "v_mfma_f32_16x16x32_bf16 v[124:127], v[60:63], v[200:203], v[124:127]\n"
        "s_waitcnt lgkmcnt(4)\n"
        "v_mfma_f32_16x16x32_bf16 v[156:159], v[60:63], v[204:207], v[156:159]\n"
        "ds_read_b64_tr_b16 v[60:61], v232 offset:25600\n"
        "v_mfma_f32_16x16x32_bf16 v[0:3], v[32:35], v[208:211], v[0:3]\n"
        "ds_read_b64_tr_b16 v[62:63], v233 offset:25600\n"
        "v_mfma_f32_16x16x32_bf16 v[64:67], v[32:35], v[212:215], v[64:67]\n"
        "v_mfma_f32_16x16x32_bf16 v[96:99], v[32:35], v[216:219], v[96:99]\n"
        "v_mfma_f32_16x16x32_bf16 v[128:131], v[32:35], v[220:223], v[128:131]\n"
        "v_mfma_f32_16x16x32_bf16 v[4:7], v[36:39], v[208:211], v[4:7]\n"
        "v_mfma_f32_16x16x32_bf16 v[68:71], v[36:39], v[212:215], v[68:71]\n"
        "v_mfma_f32_16x16x32_bf16 v[100:103], v[36:39], v[216:219], v[100:103]\n"
        "v_mfma_f32_16x16x32_bf16 v[132:135], v[36:39], v[220:223], v[132:135]\n"
        "global_store_dwordx4 v254, a[248:251], s[70:71] offset:1024 sc1\n"
        "s_waitcnt vmcnt(3) lgkmcnt(0)\n"
        "s_barrier\n"
        "v_mfma_f32_16x16x32_bf16 v[8:11], v[40:43], v[208:211], v[8:11]\n"
        "ds_write_b128 v244, a[224:227] offset:12288\n"
        "v_mfma_f32_16x16x32_bf16 v[72:75], v[40:43], v[212:215], v[72:75]\n"
        "ds_read_b64_tr_b16 v[32:33], v226 offset:32768\n"
        "v_mfma_f32_16x16x32_bf16 v[104:107], v[40:43], v[216:219], v[104:107]\n"
        "ds_read_b64_tr_b16 v[34:35], v227 offset:32768\n"
        "v_mfma_f32_16x16x32_bf16 v[136:139], v[40:43], v[220:223], v[136:139]\n"
        "ds_read_b64_tr_b16 v[36:37], v228 offset:32768\n"
        "v_mfma_f32_16x16x32_bf16 v[12:15], v[44:47], v[208:211], v[12:15]\n"
        "ds_read_b64_tr_b16 v[38:39], v229 offset:32768\n"
        "v_mfma_f32_16x16x32_bf16 v[76:79], v[44:47], v[212:215], v[76:79]\n"
        "ds_write_b128 v244, a[228:231] offset:13312\n"
        "v_mfma_f32_16x16x32_bf16 v[108:111], v[44:47], v[216:219], v[108:111]\n"
        "ds_read_b64_tr_b16 v[40:41], v230 offset:32768\n"
        "v_mfma_f32_16x16x32_bf16 v[140:143], v[44:47], v[220:223], v[140:143]\n"
        "ds_read_b64_tr_b16 v[42:43], v231 offset:32768\n"
        "v_mfma_f32_16x16x32_bf16 v[16:19], v[48:51], v[208:211], v[16:19]\n"
        "ds_read_b128 v[192:195], v246 offset:2048\n"
        "v_mfma_f32_16x16x32_bf16 v[80:83], v[48:51], v[212:215], v[80:83]\n"
        "ds_write_b128 v244, a[232:235] offset:8192\n"
        "v_mfma_f32_16x16x32_bf16 v[112:115], v[48:51], v[216:219], v[112:115]\n"
        "ds_read_b64_tr_b16 v[44:45], v232 offset:32768\n"
        "v_mfma_f32_16x16x32_bf16 v[144:147], v[48:51], v[220:223], v[144:147]\n"
        "ds_read_b64_tr_b16 v[46:47], v233 offset:32768\n"
        "v_mfma_f32_16x16x32_bf16 v[20:23], v[52:55], v[208:211], v[20:23]\n"
        "ds_read_b128 v[196:199], v246 offset:0\n"
        "v_mfma_f32_16x16x32_bf16 v[84:87], v[52:55], v[212:215], v[84:87]\n"
        "ds_write_b128 v244, a[236:239] offset:9216\n"
        "v_mfma_f32_16x16x32_bf16 v[116:119], v[52:55], v[216:219], v[116:119]\n"
        "ds_read_b64_tr_b16 v[48:49], v226 offset:33792\n"
        "v_mfma_f32_16x16x32_bf16 v[148:151], v[52:55], v[220:223], v[148:151]\n"
        "ds_read_b64_tr_b16 v[50:51], v227 offset:33792\n"
        "v_mfma_f32_16x16x32_bf16 v[24:27], v[56:59], v[208:211], v[24:27]\n"
        "ds_read_b128 v[200:203], v246 offset:6144\n"
        "v_mfma_f32_16x16x32_bf16 v[88:91], v[56:59], v[212:215], v[88:91]\n"
        "ds_write_b128 v244, a[192:195] offset:4096\n"
        "v_mfma_f32_16x16x32_bf16 v[120:123], v[56:59], v[216:219], v[120:123]\n"
        "ds_write_b128 v244, a[196:199] offset:5120\n"
        "v_mfma_f32_16x16x32_bf16 v[152:155], v[56:59], v[220:223], v[152:155]\n"
        "ds_read_b64_tr_b16 v[52:53], v228 offset:33792\n"
        "s_waitcnt lgkmcnt(6)\n"
        "v_mfma_f32_16x16x32_bf16 v[28:31], v[60:63], v[208:211], v[28:31]\n"
        "ds_read_b64_tr_b16 v[54:55], v229 offset:33792\n"
        "v_mfma_f32_16x16x32_bf16 v[92:95], v[60:63], v[212:215], v[92:95]\n"
        "ds_read_b128 v[204:207], v246 offset:4096\n"
        "s_waitcnt lgkmcnt(7)\n"
        "v_mfma_f32_16x16x32_bf16 v[124:127], v[60:63], v[216:219], v[124:127]\n"
        "ds_write_b128 v244, a[200:203] offset:16384\n"
        "v_mfma_f32_16x16x32_bf16 v[156:159], v[60:63], v[220:223], v[156:159]\n"
        "ds_read_b64_tr_b16 v[56:57], v230 offset:33792\n"
        "s_waitcnt lgkmcnt(6)\n"
        "v_mfma_f32_16x16x32_bf16 v[0:3], v[32:35], v[192:195], v[0:3]\n"
        "ds_read_b64_tr_b16 v[58:59], v231 offset:33792\n"
        "v_mfma_f32_16x16x32_bf16 v[64:67], v[32:35], v[196:199], v[64:67]\n"
        "ds_write_b128 v244, a[204:207] offset:17408\n"
        "v_mfma_f32_16x16x32_bf16 v[96:99], v[32:35], v[200:203], v[96:99]\n"
        "ds_read_b64_tr_b16 v[60:61], v232 offset:33792\n"
        "s_waitcnt lgkmcnt(7)\n"
        "v_mfma_f32_16x16x32_bf16 v[4:7], v[36:39], v[192:195], v[4:7]\n"
        "ds_read_b64_tr_b16 v[62:63], v233 offset:33792\n"
        "s_waitcnt lgkmcnt(6)\n"
        "v_mfma_f32_16x16x32_bf16 v[128:131], v[32:35], v[204:207], v[128:131]\n"
        "ds_write_b128 v244, a[160:163] offset:20480\n"
        "v_mfma_f32_16x16x32_bf16 v[68:71], v[36:39], v[196:199], v[68:71]\n"
        "ds_write_b128 v244, a[164:167] offset:21504\n"
        "v_mfma_f32_16x16x32_bf16 v[100:103], v[36:39], v[200:203], v[100:103]\n"
        "ds_read_b64_tr_b16 v[32:33], v226 offset:40960\n"
        "v_mfma_f32_16x16x32_bf16 v[132:135], v[36:39], v[204:207], v[132:135]\n"
        "ds_read_b64_tr_b16 v[34:35], v227 offset:40960\n"
        "v_mfma_f32_16x16x32_bf16 v[8:11], v[40:43], v[192:195], v[8:11]\n"
        "ds_write_b128 v244, a[168:171] offset:24576\n"
        "v_mfma_f32_16x16x32_bf16 v[72:75], v[40:43], v[196:199], v[72:75]\n"
        "ds_read_b64_tr_b16 v[36:37], v228 offset:40960\n"
        "v_mfma_f32_16x16x32_bf16 v[104:107], v[40:43], v[200:203], v[104:107]\n"
        "ds_read_b64_tr_b16 v[38:39], v229 offset:40960\n"
        "v_mfma_f32_16x16x32_bf16 v[136:139], v[40:43], v[204:207], v[136:139]\n"
        "ds_write_b128 v244, a[172:175] offset:25600\n"
        "v_mfma_f32_16x16x32_bf16 v[12:15], v[44:47], v[192:195], v[12:15]\n"
        "ds_read_b64_tr_b16 v[40:41], v230 offset:40960\n"
        "v_mfma_f32_16x16x32_bf16 v[76:79], v[44:47], v[196:199], v[76:79]\n"
        "ds_read_b64_tr_b16 v[42:43], v231 offset:40960\n"
        "v_mfma_f32_16x16x32_bf16 v[108:111], v[44:47], v[200:203], v[108:111]\n"
        "ds_read_b128 v[208:211], v247 offset:2048\n"
        "v_mfma_f32_16x16x32_bf16 v[140:143], v[44:47], v[204:207], v[140:143]\n"
        "ds_write_b128 v244, a[0:3] offset:28672\n"
        "v_mfma_f32_16x16x32_bf16 v[16:19], v[48:51], v[192:195], v[16:19]\n"
        "ds_write_b128 v244, a[4:7] offset:29696\n"
        "v_mfma_f32_16x16x32_bf16 v[80:83], v[48:51], v[196:199], v[80:83]\n"
        "ds_read_b64_tr_b16 v[44:45], v232 offset:40960\n"
        "v_mfma_f32_16x16x32_bf16 v[112:115], v[48:51], v[200:203], v[112:115]\n"
        "ds_read_b64_tr_b16 v[46:47], v233 offset:40960\n"
        "s_waitcnt lgkmcnt(7)\n"
        "v_mfma_f32_16x16x32_bf16 v[144:147], v[48:51], v[204:207], v[144:147]\n"
        "ds_read_b128 v[212:215], v247 offset:0\n"
        "v_mfma_f32_16x16x32_bf16 v[20:23], v[52:55], v[192:195], v[20:23]\n"
        "ds_write_b128 v244, a[8:11] offset:0\n"
        "v_mfma_f32_16x16x32_bf16 v[84:87], v[52:55], v[196:199], v[84:87]\n"
        "ds_read_b64_tr_b16 v[48:49], v226 offset:41984\n"
        "v_mfma_f32_16x16x32_bf16 v[116:119], v[52:55], v[200:203], v[116:119]\n"
        "ds_read_b64_tr_b16 v[50:51], v227 offset:41984\n"
        "v_mfma_f32_16x16x32_bf16 v[148:151], v[52:55], v[204:207], v[148:151]\n"
        "ds_read_b128 v[216:219], v247 offset:6144\n"
        "v_mfma_f32_16x16x32_bf16 v[24:27], v[56:59], v[192:195], v[24:27]\n"
        "global_store_dwordx4 v254, a[220:223], s[70:71] offset:2048 sc1\n"
        "ds_read_b64_tr_b16 v[52:53], v228 offset:41984\n"
        "ds_read_b64_tr_b16 v[54:55], v229 offset:41984\n"
        "v_mfma_f32_16x16x32_bf16 v[88:91], v[56:59], v[196:199], v[88:91]\n"
        "ds_write_b128 v244, a[12:15] offset:1024\n"
        "v_mfma_f32_16x16x32_bf16 v[120:123], v[56:59], v[200:203], v[120:123]\n"
        "ds_read_b128 v[220:223], v247 offset:4096\n"
        "v_lshrrev_b32 v244, 1, v254\n"
        "v_mfma_f32_16x16x32_bf16 v[152:155], v[56:59], v[204:207], v[152:155]\n"
        "ds_read_b64_tr_b16 v[56:57], v230 offset:41984\n"
        "v_add_u32 v244, s81, v244\n"
        "v_mfma_f32_16x16x32_bf16 v[28:31], v[60:63], v[192:195], v[28:31]\n"
        "ds_read_b64_tr_b16 v[58:59], v231 offset:41984\n"
        "s_add_u32 s81, s78, 4096\n"
        "s_waitcnt lgkmcnt(7)\n"
        "v_mfma_f32_16x16x32_bf16 v[92:95], v[60:63], v[196:199], v[92:95]\n"
        "s_cmp_eq_u32 s82, s83\n"
        "s_cselect_b64 s[74:75], s[94:95], s[74:75]\n"
        "s_cselect_b32 s81, s51, s81\n"
        "v_mfma_f32_16x16x32_bf16 v[124:127], v[60:63], v[200:203], v[124:127]\n"
        "v_mfma_f32_16x16x32_bf16 v[156:159], v[60:63], v[204:207], v[156:159]\n"
        "ds_read_b64_tr_b16 v[60:61], v232 offset:41984\n"
        "s_waitcnt lgkmcnt(5)\n"
        "v_mfma_f32_16x16x32_bf16 v[0:3], v[32:35], v[208:211], v[0:3]\n"
        "ds_read_b64_tr_b16 v[62:63], v233 offset:41984\n"
        "v_mfma_f32_16x16x32_bf16 v[4:7], v[36:39], v[208:211], v[4:7]\n"
        "v_mfma_f32_16x16x32_bf16 v[8:11], v[40:43], v[208:211], v[8:11]\n"
        "v_mfma_f32_16x16x32_bf16 v[12:15], v[44:47], v[208:211], v[12:15]\n"
        "s_waitcnt lgkmcnt(3)\n"
        "v_mfma_f32_16x16x32_bf16 v[16:19], v[48:51], v[208:211], v[16:19]\n"
        "s_waitcnt lgkmcnt(2)\n"
        "v_mfma_f32_16x16x32_bf16 v[20:23], v[52:55], v[208:211], v[20:23]\n"
        "v_cvt_pk_bf16_f32 v0, v0, v1\n"
        "v_cvt_pk_bf16_f32 v1, v2, v3\n"
        "v_mfma_f32_16x16x32_bf16 v[24:27], v[56:59], v[208:211], v[24:27]\n"
        "v_cvt_pk_bf16_f32 v2, v4, v5\n"
        "v_cvt_pk_bf16_f32 v3, v6, v7\n"
        "v_mfma_f32_16x16x32_bf16 v[64:67], v[32:35], v[212:215], v[64:67]\n"
        "s_waitcnt lgkmcnt(1)\n"
        "v_cvt_pk_bf16_f32 v4, v8, v9\n"
        "v_cvt_pk_bf16_f32 v5, v10, v11\n"
        "v_mfma_f32_16x16x32_bf16 v[68:71], v[36:39], v[212:215], v[68:71]\n"
        "s_waitcnt lgkmcnt(0)\n"
        "s_barrier\n"
        "v_cvt_pk_bf16_f32 v6, v12, v13\n"
        "v_mfma_f32_16x16x32_bf16 v[28:31], v[60:63], v[208:211], v[28:31]\n"
        "v_cvt_pk_bf16_f32 v7, v14, v15\n"
        "v_cvt_pk_bf16_f32 v8, v16, v17\n"
        "v_mfma_f32_16x16x32_bf16 v[72:75], v[40:43], v[212:215], v[72:75]\n"
        "v_cvt_pk_bf16_f32 v9, v18, v19\n"
        "v_cvt_pk_bf16_f32 v10, v20, v21\n"
        "v_mfma_f32_16x16x32_bf16 v[76:79], v[44:47], v[212:215], v[76:79]\n"
        "v_cvt_pk_bf16_f32 v11, v22, v23\n"
        "v_cvt_pk_bf16_f32 v12, v24, v25\n"
        "v_mfma_f32_16x16x32_bf16 v[80:83], v[48:51], v[212:215], v[80:83]\n"
        "v_cvt_pk_bf16_f32 v13, v26, v27\n"
        "v_cvt_pk_bf16_f32 v14, v28, v29\n"
        "v_mfma_f32_16x16x32_bf16 v[84:87], v[52:55], v[212:215], v[84:87]\n"
        "v_cvt_pk_bf16_f32 v15, v30, v31\n"
        "v_mfma_f32_16x16x32_bf16 v[88:91], v[56:59], v[212:215], v[88:91]\n"
        "ds_write_b128 v245, a[208:211] offset:8192\n"
        "v_mfma_f32_16x16x32_bf16 v[92:95], v[60:63], v[212:215], v[92:95]\n"
        "s_barrier\n"
        "v_mfma_f32_16x16x32_bf16 v[96:99], v[32:35], v[216:219], v[96:99]\n"
        "ds_read_b128 a[96:99], v240 offset:0\n"
        "v_mfma_f32_16x16x32_bf16 v[100:103], v[36:39], v[216:219], v[100:103]\n"
        "ds_read_b128 a[100:103], v240 offset:1024\n"
        "v_mfma_f32_16x16x32_bf16 v[104:107], v[40:43], v[216:219], v[104:107]\n"
        "ds_read_b128 a[104:107], v240 offset:2048\n"
        "v_mfma_f32_16x16x32_bf16 v[108:111], v[44:47], v[216:219], v[108:111]\n"
        "ds_read_b128 a[108:111], v240 offset:3072\n"
        "v_mfma_f32_16x16x32_bf16 v[112:115], v[48:51], v[216:219], v[112:115]\n"
        "ds_read_b128 a[112:115], v240 offset:4096\n"
        "v_mfma_f32_16x16x32_bf16 v[116:119], v[52:55], v[216:219], v[116:119]\n"
        "ds_read_b128 a[116:119], v240 offset:5120\n"
        "v_mfma_f32_16x16x32_bf16 v[120:123], v[56:59], v[216:219], v[120:123]\n"
        "ds_read_b128 a[120:123], v240 offset:6144\n"
        "v_mfma_f32_16x16x32_bf16 v[124:127], v[60:63], v[216:219], v[124:127]\n"
        "ds_write_b128 v245, a[240:243] offset:9216\n"
        "v_mfma_f32_16x16x32_bf16 v[128:131], v[32:35], v[220:223], v[128:131]\n"
        "ds_write_b128 v245, a[212:215] offset:10240\n"
        "v_mfma_f32_16x16x32_bf16 v[132:135], v[36:39], v[220:223], v[132:135]\n"
        "ds_write_b128 v245, a[244:247] offset:11264\n"
        "v_mfma_f32_16x16x32_bf16 v[136:139], v[40:43], v[220:223], v[136:139]\n"
        "ds_write_b128 v245, a[216:219] offset:0\n"
        "v_mfma_f32_16x16x32_bf16 v[140:143], v[44:47], v[220:223], v[140:143]\n"
        "ds_write_b128 v245, a[248:251] offset:1024\n"
        "v_mfma_f32_16x16x32_bf16 v[144:147], v[48:51], v[220:223], v[144:147]\n"
        "ds_write_b128 v245, a[220:223] offset:2048\n"
        "v_mfma_f32_16x16x32_bf16 v[148:151], v[52:55], v[220:223], v[148:151]\n"
        "ds_write_b128 v245, a[252:255] offset:3072\n"
        "v_mfma_f32_16x16x32_bf16 v[152:155], v[56:59], v[220:223], v[152:155]\n"
        "ds_write_b128 v245, a[176:179] offset:16384\n"
        "v_mfma_f32_16x16x32_bf16 v[156:159], v[60:63], v[220:223], v[156:159]\n"
        "ds_write_b128 v245, a[180:183] offset:17408\n"
        "ds_write_b128 v245, a[184:187] offset:18432\n"
        "ds_write_b128 v245, a[188:191] offset:19456\n"
        "ds_write_b128 v245, a[16:19] offset:24576\n"
        "ds_write_b128 v245, a[20:23] offset:25600\n"
        "ds_write_b128 v245, a[24:27] offset:26624\n"
        "ds_write_b128 v245, a[28:31] offset:27648\n"
        "ds_read_b128 a[124:127], v240 offset:7168\n"
        "global_store_dwordx4 v254, a[252:255], s[70:71] offset:3072 sc1\n"
        "s_add_u32 s70, s38, s97\n"
        "s_addc_u32 s71, s39, 0\n"
        "v_cndmask_b32 v32, 0, v242, s[10:11]\n"
        "v_cndmask_b32 v33, 0, v242, s[12:13]\n"
        "v_cndmask_b32 v34, 0, v242, s[14:15]\n"
        "v_cndmask_b32 v35, 0, v242, s[16:17]\n"
        "s_waitcnt lgkmcnt(4)\n"
        "s_nop 0\n"
        "v_mfma_f32_16x16x32_bf16 v[32:35], a[96:99], v[0:3], v[32:35]\n"
        "ds_read_b128 a[96:99], v240 offset:8192\n"
        "s_add_u32 s97, s53, 0x2000\n"
        "v_cndmask_b32 v48, 0, v242, s[20:21]\n"
        "v_mfma_f32_16x16x32_bf16 v[32:35], a[100:103], v[4:7], v[32:35]\n"
        "ds_read_b128 a[100:103], v240 offset:9216\n"
        "v_cndmask_b32 v49, 0, v242, s[22:23]\n"
        "v_mfma_f32_16x16x32_bf16 v[32:35], a[104:107], v[8:11], v[32:35]\n"
        "global_store_dwordx4 v254, a[176:179], s[70:71] offset:0 sc1\n"
        "ds_read_b128 a[104:107], v240 offset:10240\n"
        "v_cndmask_b32 v50, 0, v242, s[24:25]\n"
        "v_cndmask_b32 v51, 0, v242, s[28:29]\n"
        "v_cvt_pk_bf16_f32 v64, v64, v65\n"
        "v_mfma_f32_16x16x32_bf16 v[32:35], a[108:111], v[12:15], v[32:35]\n"
        "ds_read_b128 a[108:111], v240 offset:11264\n"
        "s_waitcnt lgkmcnt(5)\n"
        "v_mfma_f32_16x16x32_bf16 v[48:51], a[112:115], v[0:3], v[48:51]\n"
        "ds_read_b128 a[112:115], v240 offset:12288\n"
        "s_barrier\n"
        "v_cvt_pk_bf16_f32 v65, v66, v67\n"
        "v_mfma_f32_16x16x32_bf16 v[48:51], a[116:119], v[4:7], v[48:51]\n"
        "ds_read_b128 a[144:147], v241 offset:0\n"
        "v_max_f32 v246, v32, v33\n"
        "v_mfma_f32_16x16x32_bf16 v[48:51], a[120:123], v[8:11], v[48:51]\n"
        "s_waitcnt lgkmcnt(6)\n"
        "v_max3_f32 v246, v34, v35, v246\n"
        "v_mov_b32 v247, v246\n"
        "v_mfma_f32_16x16x32_bf16 v[48:51], a[124:127], v[12:15], v[48:51]\n"
        "global_store_dwordx4 v254, a[180:183], s[70:71] offset:1024 sc1\n"
        "v_permlane16_swap_b32 v246, v247\n"
        "v_max_f32 v246, v246, v247\n"
        "v_mov_b32 v247, v246\n"
        "ds_read_b128 a[116:119], v240 offset:13312\n"
        "ds_read_b128 a[148:151], v241 offset:1024\n"
        "v_permlane32_swap_b32 v246, v247\n"
        "v_max_f32 v246, v246, v247\n"
        "v_sub_f32 v224, 0, v246\n"
        "v_sub_f32 v225, 0, v246\n"
        "v_sub_f32 v226, 0, v246\n"
        "v_sub_f32 v227, 0, v246\n"
        "v_add_f32 v32, v32, v224\n"
        "v_exp_f32 v32, v32\n"
        "v_add_f32 v33, v33, v224\n"
        "v_exp_f32 v33, v33\n"
        "global_store_dwordx4 v254, a[184:187], s[70:71] offset:2048 sc1\n"
        "v_cvt_pk_bf16_f32 v16, v32, v33\n"
        "ds_read_b128 a[120:123], v240 offset:14336\n"
        "v_add_f32 v34, v34, v224\n"
        "v_exp_f32 v34, v34\n"
        "v_add_f32 v35, v35, v224\n"
        "v_exp_f32 v35, v35\n"
        "ds_read_b128 a[152:155], v241 offset:2048\n"
        "v_cvt_pk_bf16_f32 v17, v34, v35\n"
        "v_cndmask_b32 v32, v224, v242, s[30:31]\n"
        "v_cndmask_b32 v33, v224, v242, s[56:57]\n"
        "v_cndmask_b32 v34, v224, v242, s[58:59]\n"
        "v_cndmask_b32 v35, v224, v242, s[60:61]\n"
        "s_waitcnt lgkmcnt(2)\n"
        "s_nop 0\n"
        "v_mfma_f32_16x16x32_bf16 v[32:35], a[96:99], v[0:3], v[32:35]\n"
        "ds_read_b128 a[124:127], v240 offset:15360\n"
        "v_add_f32 v48, v48, v224\n"
        "v_mfma_f32_16x16x32_bf16 v[32:35], a[100:103], v[4:7], v[32:35]\n"
        "ds_read_b128 a[156:159], v241 offset:3072\n"
        "v_add_f32 v49, v49, v224\n"
        "v_mfma_f32_16x16x32_bf16 v[32:35], a[104:107], v[8:11], v[32:35]\n"
        "global_store_dwordx4 v254, a[188:191], s[70:71] offset:3072 sc1\n"
        "ds_read_b128 a[96:99], v240 offset:16384\n"
        "s_add_u32 s70, s38, s97\n"
        "s_addc_u32 s71, s39, 0\n"
        "v_exp_f32 v48, v48\n"
        "v_mfma_f32_16x16x32_bf16 v[32:35], a[108:111], v[12:15], v[32:35]\n"
        "v_exp_f32 v49, v49\n"
        "ds_read_b128 a[128:131], v241 offset:4096\n"
        "v_cvt_pk_bf16_f32 v18, v48, v49\n"
        "ds_read_b128 a[100:103], v240 offset:17408\n"
        "ds_read_b128 a[132:135], v241 offset:5120\n"
        "ds_read_b128 a[104:107], v240 offset:18432\n"
        "v_add_f32 v50, v50, v224\n"
        "v_exp_f32 v50, v50\n"
        "v_add_f32 v51, v51, v224\n"
        "v_exp_f32 v51, v51\n"
        "ds_read_b128 a[136:139], v241 offset:6144\n"
        "v_cvt_pk_bf16_f32 v19, v50, v51\n"
        "s_nop 1\n"
        "v_mfma_f32_16x16x32_bf16 v[160:163], a[144:147], v[16:19], 0\n"
        "global_store_dwordx4 v254, a[16:19], s[70:71] offset:0 sc1\n"
        "s_waitcnt lgkmcnt(5)\n"
        "v_cndmask_b32 v48, v224, v242, s[62:63]\n"
        "v_cndmask_b32 v49, v224, v242, s[84:85]\n"
        "v_cndmask_b32 v50, v224, v242, s[86:87]\n"
        "v_mfma_f32_16x16x32_bf16 a[64:67], a[80:83], v[16:19], 0\n"
        "ds_read_b128 a[108:111], v240 offset:19456\n"
        "v_cndmask_b32 v51, v224, v242, s[88:89]\n"
        "s_nop 1\n"
        "v_mfma_f32_16x16x32_bf16 v[48:51], a[112:115], v[0:3], v[48:51]\n"
        "ds_read_b128 a[140:143], v241 offset:7168\n"
        "v_cvt_pk_bf16_f32 v66, v68, v69\n"
        "v_mfma_f32_16x16x32_bf16 v[48:51], a[116:119], v[4:7], v[48:51]\n"
        "ds_read_b128 a[112:115], v240 offset:20480\n"
        "v_cvt_pk_bf16_f32 v68, v72, v73\n"
        "v_mfma_f32_16x16x32_bf16 v[164:167], a[148:151], v[16:19], 0\n"
        "s_waitcnt lgkmcnt(4)\n"
        "v_exp_f32 v32, v32\n"
        "v_mfma_f32_16x16x32_bf16 v[48:51], a[120:123], v[8:11], v[48:51]\n"
        "s_waitcnt lgkmcnt(3)\n"
        "v_exp_f32 v33, v33\n"
        "v_mfma_f32_16x16x32_bf16 v[168:171], a[152:155], v[16:19], 0\n"
        "ds_read_b128 a[144:147], v241 offset:8192\n"
        "v_cvt_pk_bf16_f32 v72, v80, v81\n"
        "v_mfma_f32_16x16x32_bf16 v[48:51], a[124:127], v[12:15], v[48:51]\n"
        "ds_read_b128 a[116:119], v240 offset:21504\n"
        "v_cvt_pk_bf16_f32 v80, v32, v33\n"
        "v_mfma_f32_16x16x32_bf16 v[172:175], a[156:159], v[16:19], 0\n"
        "ds_read_b128 a[148:151], v241 offset:9216\n"
        "v_cndmask_b32 v36, 0, v242, s[10:11]\n"
        "v_mfma_f32_16x16x32_bf16 v[176:179], a[128:131], v[16:19], 0\n"
        "global_store_dwordx4 v254, a[20:23], s[70:71] offset:1024 sc1\n"
        "ds_read_b128 a[120:123], v240 offset:22528\n"
        "s_waitcnt lgkmcnt(6)\n"
        "v_exp_f32 v34, v34\n"
        "v_mfma_f32_16x16x32_bf16 v[180:183], a[132:135], v[16:19], 0\n"
        "s_waitcnt lgkmcnt(5)\n"
        "v_exp_f32 v35, v35\n"
        "v_mfma_f32_16x16x32_bf16 v[184:187], a[136:139], v[16:19], 0\n"
        "ds_read_b128 a[152:155], v241 offset:10240\n"
        "v_cvt_pk_bf16_f32 v81, v34, v35\n"
        "v_mfma_f32_16x16x32_bf16 v[32:35], a[96:99], v[0:3], v[224:227]\n"
        "ds_read_b128 a[124:127], v240 offset:23552\n"
        "v_cndmask_b32 v37, 0, v242, s[12:13]\n"
        "v_mfma_f32_16x16x32_bf16 v[32:35], a[100:103], v[4:7], v[32:35]\n"
        "ds_read_b128 a[156:159], v241 offset:11264\n"
        "v_cndmask_b32 v38, 0, v242, s[14:15]\n"
        "v_mfma_f32_16x16x32_bf16 v[32:35], a[104:107], v[8:11], v[32:35]\n"
        "ds_read_b128 a[128:131], v241 offset:12288\n"
        "v_cndmask_b32 v39, 0, v242, s[16:17]\n"
        "v_mfma_f32_16x16x32_bf16 v[32:35], a[108:111], v[12:15], v[32:35]\n"
        "ds_read_b128 a[132:135], v241 offset:13312\n"
        "v_cvt_pk_bf16_f32 v67, v70, v71\n"
        "s_nop 1\n"
        "v_mfma_f32_16x16x32_bf16 v[36:39], a[96:99], v[64:67], v[36:39]\n"
        "ds_read_b128 a[96:99], v240 offset:24576\n"
        "v_cvt_pk_bf16_f32 v69, v74, v75\n"
        "v_mfma_f32_16x16x32_bf16 v[188:191], a[140:143], v[16:19], 0\n"
        "v_cvt_pk_bf16_f32 v70, v76, v77\n"
        "v_cvt_pk_bf16_f32 v71, v78, v79\n"
        "s_nop 1\n"
        "v_mfma_f32_16x16x32_bf16 v[36:39], a[100:103], v[68:71], v[36:39]\n"
        "v_cvt_pk_bf16_f32 v73, v82, v83\n"
        "v_cvt_pk_bf16_f32 v74, v84, v85\n"
        "v_cvt_pk_bf16_f32 v75, v86, v87\n"
        "s_nop 1\n"
        "v_mfma_f32_16x16x32_bf16 v[36:39], a[104:107], v[72:75], v[36:39]\n"
        "v_cvt_pk_bf16_f32 v76, v88, v89\n"
        "v_cvt_pk_bf16_f32 v77, v90, v91\n"
        "v_cvt_pk_bf16_f32 v78, v92, v93\n"
        "v_cvt_pk_bf16_f32 v79, v94, v95\n"
        "s_nop 1\n"
        "v_mfma_f32_16x16x32_bf16 v[36:39], a[108:111], v[76:79], v[36:39]\n"
        "v_exp_f32 v48, v48\n"
        "v_exp_f32 v49, v49\n"
        "global_store_dwordx4 v254, a[24:27], s[70:71] offset:2048 sc1\n"
        "v_cvt_pk_bf16_f32 v82, v48, v49\n"
        "ds_read_b128 a[100:103], v240 offset:25600\n"
        "ds_read_b128 a[104:107], v240 offset:26624\n"
        "v_exp_f32 v50, v50\n"
        "v_exp_f32 v51, v51\n"
        "ds_read_b128 a[136:139], v241 offset:14336\n"
        "v_cvt_pk_bf16_f32 v83, v50, v51\n"
        "s_waitcnt lgkmcnt(3)\n"
        "v_mfma_f32_16x16x32_bf16 v[48:51], a[112:115], v[0:3], v[224:227]\n"
        "ds_read_b128 a[108:111], v240 offset:27648\n"
        "v_cndmask_b32 v52, 0, v242, s[20:21]\n"
        "v_mfma_f32_16x16x32_bf16 v[160:163], a[144:147], v[80:83], v[160:163]\n"
        "ds_read_b128 a[140:143], v241 offset:15360\n"
        "v_cndmask_b32 v53, 0, v242, s[22:23]\n"
        "v_mfma_f32_16x16x32_bf16 a[64:67], a[80:83], v[80:83], a[64:67]\n"
        "ds_read_b128 a[144:147], v241 offset:16384\n"
        "v_cndmask_b32 v54, 0, v242, s[24:25]\n"
        "v_mfma_f32_16x16x32_bf16 v[48:51], a[116:119], v[4:7], v[48:51]\n"
        "v_cndmask_b32 v55, 0, v242, s[28:29]\n"
        "s_nop 1\n"
        "v_mfma_f32_16x16x32_bf16 v[52:55], a[112:115], v[64:67], v[52:55]\n"
        "v_exp_f32 v32, v32\n"
        "v_mfma_f32_16x16x32_bf16 v[52:55], a[116:119], v[68:71], v[52:55]\n"
        "v_exp_f32 v33, v33\n"
        "v_mfma_f32_16x16x32_bf16 v[164:167], a[148:151], v[80:83], v[164:167]\n"
        "ds_read_b128 a[112:115], v240 offset:28672\n"
        "v_cvt_pk_bf16_f32 v16, v32, v33\n"
        "v_mfma_f32_16x16x32_bf16 v[48:51], a[120:123], v[8:11], v[48:51]\n"
        "global_store_dwordx4 v254, a[28:31], s[70:71] offset:3072 sc1\n"
        "ds_read_b128 a[116:119], v240 offset:29696\n"
        "v_exp_f32 v34, v34\n"
        "v_max_f32 v248, v36, v37\n"
        "v_mfma_f32_16x16x32_bf16 v[52:55], a[120:123], v[72:75], v[52:55]\n"
        "v_exp_f32 v35, v35\n"
        "v_mfma_f32_16x16x32_bf16 v[168:171], a[152:155], v[80:83], v[168:171]\n"
        "s_waitcnt lgkmcnt(2)\n"
        "v_cvt_pk_bf16_f32 v17, v34, v35\n"
        "v_max3_f32 v248, v38, v39, v248\n"
        "v_mfma_f32_16x16x32_bf16 v[48:51], a[124:127], v[12:15], v[48:51]\n"
        "ds_read_b128 a[120:123], v240 offset:30720\n"
        "v_mov_b32 v249, v248\n"
        "v_mfma_f32_16x16x32_bf16 v[52:55], a[124:127], v[76:79], v[52:55]\n"
        "ds_read_b128 a[124:127], v240 offset:31744\n"
        "v_cvt_pk_bf16_f32 v96, v96, v97\n"
        "v_mfma_f32_16x16x32_bf16 v[172:175], a[156:159], v[80:83], v[172:175]\n"
        "v_permlane16_swap_b32 v248, v249\n"
        "v_mfma_f32_16x16x32_bf16 v[32:35], a[96:99], v[0:3], v[224:227]\n"
        "ds_read_b128 a[148:151], v241 offset:17408\n"
        "v_max_f32 v248, v248, v249\n"
        "v_mfma_f32_16x16x32_bf16 v[176:179], a[128:131], v[80:83], v[176:179]\n"
        "ds_read_b128 a[152:155], v241 offset:18432\n"
        "v_mov_b32 v249, v248\n"
        "v_mfma_f32_16x16x32_bf16 v[32:35], a[100:103], v[4:7], v[32:35]\n"
        "ds_read_b128 a[156:159], v241 offset:19456\n"
        "v_cvt_pk_bf16_f32 v97, v98, v99\n"
        "v_mfma_f32_16x16x32_bf16 v[180:183], a[132:135], v[80:83], v[180:183]\n"
        "v_permlane32_swap_b32 v248, v249\n"
        "v_mfma_f32_16x16x32_bf16 v[32:35], a[104:107], v[8:11], v[32:35]\n"
        "ds_read_b128 a[128:131], v241 offset:20480\n"
        "v_max_f32 v248, v248, v249\n"
        "v_mfma_f32_16x16x32_bf16 v[184:187], a[136:139], v[80:83], v[184:187]\n"
        "v_sub_f32 v228, 0, v248\n"
        "v_sub_f32 v229, 0, v248\n"
        "v_mfma_f32_16x16x32_bf16 v[32:35], a[108:111], v[12:15], v[32:35]\n"
        "s_waitcnt lgkmcnt(4)\n"
        "v_sub_f32 v230, 0, v248\n"
        "v_sub_f32 v231, 0, v248\n"
        "v_add_f32 v36, v36, v228\n"
        "v_add_f32 v37, v37, v228\n"
        "v_mfma_f32_16x16x32_bf16 v[188:191], a[140:143], v[80:83], v[188:191]\n"
        "v_exp_f32 v36, v36\n"
        "v_exp_f32 v37, v37\n"
        "v_add_f32 v38, v38, v228\n"
        "v_cvt_pk_bf16_f32 v20, v36, v37\n"
        "v_exp_f32 v38, v38\n"
        "v_add_f32 v39, v39, v228\n"
        "v_exp_f32 v39, v39\n"
        "v_cndmask_b32 v36, v228, v242, s[30:31]\n"
        "v_cvt_pk_bf16_f32 v21, v38, v39\n"
        "v_cndmask_b32 v37, v228, v242, s[56:57]\n"
        "v_cndmask_b32 v38, v228, v242, s[58:59]\n"
        "v_cndmask_b32 v39, v228, v242, s[60:61]\n"
        "s_nop 1\n"
        "v_mfma_f32_16x16x32_bf16 v[36:39], a[96:99], v[64:67], v[36:39]\n"
        "s_barrier\n"
        "v_cvt_pk_bf16_f32 v98, v100, v101\n"
        "v_cvt_pk_bf16_f32 v100, v104, v105\n"
        "v_mfma_f32_16x16x32_bf16 v[36:39], a[100:103], v[68:71], v[36:39]\n"
        "v_cvt_pk_bf16_f32 v104, v112, v113\n"
        "v_cvt_pk_bf16_f32 v105, v114, v115\n"
        "v_mfma_f32_16x16x32_bf16 v[36:39], a[104:107], v[72:75], v[36:39]\n"
        "global_load_dwordx4 v[112:115], v254, s[92:93] offset:0\n"
        "v_xor_b32 v240, 0x8000, v240\n"
        "v_mfma_f32_16x16x32_bf16 v[36:39], a[108:111], v[76:79], v[36:39]\n"
        "v_exp_f32 v48, v48\n"
        "v_exp_f32 v49, v49\n"
        "ds_read_b128 a[96:99], v240 offset:0\n"
        "v_cvt_pk_bf16_f32 v18, v48, v49\n"
        "v_exp_f32 v50, v50\n"
        "v_exp_f32 v51, v51\n"
        "v_cvt_pk_bf16_f32 v99, v102, v103\n"
        "v_cvt_pk_bf16_f32 v19, v50, v51\n"
        "s_nop 1\n"
        "v_mfma_f32_16x16x32_bf16 a[64:67], a[80:83], v[16:19], a[64:67]\n"
        "s_waitcnt lgkmcnt(1)\n"
        "v_cvt_pk_bf16_f32 v101, v106, v107\n"
        "v_cvt_pk_bf16_f32 v102, v108, v109\n"
        "v_mfma_f32_16x16x32_bf16 v[48:51], a[112:115], v[0:3], v[224:227]\n"
        "ds_read_b128 a[100:103], v240 offset:1024\n"
        "v_cvt_pk_bf16_f32 v103, v110, v111\n"
        "v_mfma_f32_16x16x32_bf16 v[160:163], a[144:147], v[16:19], v[160:163]\n"
        "ds_read_b128 a[132:135], v241 offset:21504\n"
        "v_cvt_pk_bf16_f32 v106, v116, v117\n"
        "v_mfma_f32_16x16x32_bf16 v[48:51], a[116:119], v[4:7], v[48:51]\n"
        "ds_read_b128 a[104:107], v240 offset:2048\n"
        "v_cvt_pk_bf16_f32 v107, v118, v119\n"
        "v_mfma_f32_16x16x32_bf16 v[164:167], a[148:151], v[16:19], v[164:167]\n"
        "ds_read_b128 a[136:139], v241 offset:22528\n"
        "v_cvt_pk_bf16_f32 v108, v120, v121\n"
        "v_cvt_pk_bf16_f32 v109, v122, v123\n"
        "v_cvt_pk_bf16_f32 v110, v124, v125\n"
        "v_cvt_pk_bf16_f32 v111, v126, v127\n"
        "v_mfma_f32_16x16x32_bf16 v[48:51], a[120:123], v[8:11], v[48:51]\n"
        "ds_read_b128 a[108:111], v240 offset:3072\n"
        "v_add_f32 v52, v52, v228\n"
        "v_add_f32 v53, v53, v228\n"
        "v_mfma_f32_16x16x32_bf16 v[168:171], a[152:155], v[16:19], v[168:171]\n"
        "v_exp_f32 v52, v52\n"
        "v_mfma_f32_16x16x32_bf16 v[48:51], a[124:127], v[12:15], v[48:51]\n"
        "v_exp_f32 v53, v53\n"
        "v_add_f32 v54, v54, v228\n"
        "v_cvt_pk_bf16_f32 v22, v52, v53\n"
        "v_add_f32 v55, v55, v228\n"
        "v_mfma_f32_16x16x32_bf16 v[172:175], a[156:159], v[16:19], v[172:175]\n"
        "s_waitcnt lgkmcnt(3)\n"
        "v_exp_f32 v54, v54\n"
        "v_cndmask_b32 v52, v228, v242, s[62:63]\n"
        "v_cndmask_b32 v53, v228, v242, s[84:85]\n"
        "v_mfma_f32_16x16x32_bf16 v[176:179], a[128:131], v[16:19], v[176:179]\n"
        "s_waitcnt lgkmcnt(2)\n"
        "v_exp_f32 v55, v55\n"
        "v_mfma_f32_16x16x32_bf16 v[180:183], a[132:135], v[16:19], v[180:183]\n"
        "s_waitcnt lgkmcnt(1)\n"
        "v_cvt_pk_bf16_f32 v23, v54, v55\n"
        "s_nop 1\n"
        "v_mfma_f32_16x16x32_bf16 v[192:195], a[144:147], v[20:23], 0\n"
        "ds_read_b128 a[140:143], v241 offset:23552\n"
        "v_cndmask_b32 v54, v228, v242, s[86:87]\n"
        "v_mfma_f32_16x16x32_bf16 a[68:71], a[80:83], v[20:23], 0\n"
        "ds_read_b128 a[144:147], v241 offset:24576\n"
        "v_cndmask_b32 v55, v228, v242, s[88:89]\n"
        "s_nop 1\n"
        "v_mfma_f32_16x16x32_bf16 v[52:55], a[112:115], v[64:67], v[52:55]\n"
        "v_exp_f32 v32, v32\n"
        "v_mfma_f32_16x16x32_bf16 v[52:55], a[116:119], v[68:71], v[52:55]\n"
        "v_exp_f32 v33, v33\n"
        "v_mfma_f32_16x16x32_bf16 v[196:199], a[148:151], v[20:23], 0\n"
        "ds_read_b128 a[112:115], v240 offset:4096\n"
        "v_cvt_pk_bf16_f32 v80, v32, v33\n"
        "v_mfma_f32_16x16x32_bf16 v[52:55], a[120:123], v[72:75], v[52:55]\n"
        "v_exp_f32 v34, v34\n"
        "v_mfma_f32_16x16x32_bf16 v[200:203], a[152:155], v[20:23], 0\n"
        "v_exp_f32 v35, v35\n"
        "v_mfma_f32_16x16x32_bf16 v[52:55], a[124:127], v[76:79], v[52:55]\n"
        "s_waitcnt lgkmcnt(3)\n"
        "v_cvt_pk_bf16_f32 v81, v34, v35\n"
        "v_cvt_pk_bf16_f32 v128, v128, v129\n"
        "v_mfma_f32_16x16x32_bf16 v[204:207], a[156:159], v[20:23], 0\n"
        "ds_read_b128 a[116:119], v240 offset:5120\n"
        "v_cvt_pk_bf16_f32 v129, v130, v131\n"
        "v_mfma_f32_16x16x32_bf16 v[32:35], a[96:99], v[0:3], v[224:227]\n"
        "s_waitcnt lgkmcnt(2)\n"
        "v_exp_f32 v36, v36\n"
        "v_mfma_f32_16x16x32_bf16 v[208:211], a[128:131], v[20:23], 0\n"
        "v_exp_f32 v37, v37\n"
        "v_mfma_f32_16x16x32_bf16 v[32:35], a[100:103], v[4:7], v[32:35]\n"
        "global_load_dwordx4 v[116:119], v254, s[92:93] offset:1024\n"
        "v_cvt_pk_bf16_f32 v84, v36, v37\n"
        "v_mfma_f32_16x16x32_bf16 v[212:215], a[132:135], v[20:23], 0\n"
        "ds_read_b128 a[148:151], v241 offset:25600\n"
        "v_cvt_pk_bf16_f32 v130, v132, v133\n"
        "v_mfma_f32_16x16x32_bf16 v[32:35], a[104:107], v[8:11], v[32:35]\n"
        "ds_read_b128 a[120:123], v240 offset:6144\n"
        "v_cvt_pk_bf16_f32 v133, v138, v139\n"
        "v_cvt_pk_bf16_f32 v132, v136, v137\n"
        "v_cvt_pk_bf16_f32 v138, v148, v149\n"
        "v_cvt_pk_bf16_f32 v136, v144, v145\n"
        "v_add_u32 v148, 16, v254\n"
        "v_cvt_pk_bf16_f32 v137, v146, v147\n"
        "v_min_u32 v149, 1020, v148\n"
        "v_mfma_f32_16x16x32_bf16 v[184:187], a[136:139], v[16:19], v[184:187]\n"
        "global_load_dword v144, v148, s[92:93] offset:0\n"
        "ds_read_b128 a[152:155], v241 offset:26624\n"
        "v_mfma_f32_16x16x32_bf16 v[216:219], a[136:139], v[20:23], 0\n"
        "global_load_dword v145, v148, s[92:93] offset:1024\n"
        "v_cndmask_b32 v40, 0, v242, s[10:11]\n"
        "v_mfma_f32_16x16x32_bf16 v[32:35], a[108:111], v[12:15], v[32:35]\n"
        "global_load_dwordx4 v[120:123], v254, s[92:93] offset:2048\n"
        "v_cndmask_b32 v41, 0, v242, s[12:13]\n"
        "v_cndmask_b32 v42, 0, v242, s[14:15]\n"
        "v_cndmask_b32 v43, 0, v242, s[16:17]\n"
        "s_nop 1\n"
        "v_mfma_f32_16x16x32_bf16 v[40:43], a[96:99], v[96:99], v[40:43]\n"
        "v_exp_f32 v38, v38\n"
        "v_mfma_f32_16x16x32_bf16 v[40:43], a[100:103], v[100:103], v[40:43]\n"
        "v_exp_f32 v39, v39\n"
        "v_mfma_f32_16x16x32_bf16 v[40:43], a[104:107], v[104:107], v[40:43]\n"
        "global_load_dword v146, v148, s[92:93] offset:2048\n"
        "v_cvt_pk_bf16_f32 v85, v38, v39\n"
        "v_mfma_f32_16x16x32_bf16 v[36:39], a[96:99], v[64:67], v[228:231]\n"
        "global_load_dwordx4 v[124:127], v254, s[92:93] offset:3072\n"
        "ds_read_b128 a[124:127], v240 offset:7168\n"
        "v_mfma_f32_16x16x32_bf16 v[36:39], a[100:103], v[68:71], v[36:39]\n"
        "v_exp_f32 v48, v48\n"
        "v_mfma_f32_16x16x32_bf16 v[36:39], a[104:107], v[72:75], v[36:39]\n"
        "v_exp_f32 v49, v49\n"
        "v_mfma_f32_16x16x32_bf16 v[36:39], a[108:111], v[76:79], v[36:39]\n"
        "ds_read_b128 a[156:159], v241 offset:27648\n"
        "v_cvt_pk_bf16_f32 v82, v48, v49\n"
        "v_mfma_f32_16x16x32_bf16 v[40:43], a[108:111], v[108:111], v[40:43]\n"
        "global_load_dword v147, v149, s[92:93] offset:3072\n"
        "ds_read_b128 a[96:99], v240 offset:8192\n"
        "v_mfma_f32_16x16x32_bf16 v[188:191], a[140:143], v[16:19], v[188:191]\n"
        "v_exp_f32 v50, v50\n"
        "v_mfma_f32_16x16x32_bf16 v[220:223], a[140:143], v[20:23], 0\n"
        "v_exp_f32 v51, v51\n"
        "ds_read_b128 a[128:131], v241 offset:28672\n"
        "v_cvt_pk_bf16_f32 v83, v50, v51\n"
        "s_nop 1\n"
        "v_mfma_f32_16x16x32_bf16 a[64:67], a[80:83], v[80:83], a[64:67]\n"
        "ds_read_b128 a[100:103], v240 offset:9216\n"
        "s_waitcnt lgkmcnt(4)\n"
        "v_mfma_f32_16x16x32_bf16 v[48:51], a[112:115], v[0:3], v[224:227]\n"
        "s_waitcnt vmcnt(8)\n"
        "v_exp_f32 v52, v52\n"
        "v_mfma_f32_16x16x32_bf16 v[160:163], a[144:147], v[80:83], v[160:163]\n"
        "s_barrier\n"
        "v_exp_f32 v53, v53\n"
        "v_mfma_f32_16x16x32_bf16 v[48:51], a[116:119], v[4:7], v[48:51]\n"
        "s_waitcnt lgkmcnt(3)\n"
        "v_cvt_pk_bf16_f32 v86, v52, v53\n"
        "v_cvt_pk_bf16_f32 v131, v134, v135\n"
        "v_cvt_pk_bf16_f32 v134, v140, v141\n"
        "v_cvt_pk_bf16_f32 v135, v142, v143\n"
        "v_cvt_pk_bf16_f32 v139, v150, v151\n"
        "v_cvt_pk_bf16_f32 v140, v152, v153\n"
        "v_cvt_pk_bf16_f32 v141, v154, v155\n"
        "v_cvt_pk_bf16_f32 v142, v156, v157\n"
        "v_cvt_pk_bf16_f32 v143, v158, v159\n"
        "v_mfma_f32_16x16x32_bf16 v[164:167], a[148:151], v[80:83], v[164:167]\n"
        "ds_read_b128 a[132:135], v241 offset:29696\n"
        "v_bfe_u32 v245, v254, 4, 5\n"
        "v_lshl_add_u32 v245, v245, 7, s80\n"
        "v_mfma_f32_16x16x32_bf16 v[48:51], a[120:123], v[8:11], v[48:51]\n"
        "s_cmp_lg_u32 s40, 0\n"
        "s_cbranch_scc1 LW1_%=\n"
        "s_mov_b64 exec, 0xffff\n"
        "global_store_dwordx2 v244, v[250:251], s[48:49] sc1\n"
        "s_mov_b64 exec, -1\n"
        "LW1_%=:\n"
        "v_mov_b32 v244, v252\n"
        "v_mfma_f32_16x16x32_bf16 v[168:171], a[152:155], v[80:83], v[168:171]\n"
        "ds_read_b128 a[104:107], v240 offset:10240\n"
        "v_cndmask_b32 v56, 0, v242, s[20:21]\n"
        "v_mfma_f32_16x16x32_bf16 v[48:51], a[124:127], v[12:15], v[48:51]\n"
        "s_waitcnt lgkmcnt(2)\n"
        "v_exp_f32 v54, v54\n"
        "v_cndmask_b32 v57, 0, v242, s[22:23]\n"
        "v_cndmask_b32 v58, 0, v242, s[24:25]\n"
        "v_cndmask_b32 v59, 0, v242, s[28:29]\n"
        "s_nop 1\n"
        "v_mfma_f32_16x16x32_bf16 v[56:59], a[112:115], v[96:99], v[56:59]\n"
        "v_exp_f32 v55, v55\n"
        "v_mfma_f32_16x16x32_bf16 v[56:59], a[116:119], v[100:103], v[56:59]\n"
        "ds_read_b128 a[136:139], v241 offset:30720\n"
        "v_cvt_pk_bf16_f32 v87, v54, v55\n"
        "v_mfma_f32_16x16x32_bf16 v[52:55], a[112:115], v[64:67], v[228:231]\n"
        "ds_read_b128 a[140:143], v241 offset:31744\n"
        "v_xor_b32 v241, 0x8000, v241\n"
        "v_mfma_f32_16x16x32_bf16 v[192:195], a[144:147], v[84:87], v[192:195]\n"
        "ds_read_b128 a[108:111], v240 offset:11264\n"
        "v_max_f32 v246, v40, v41\n"
        "v_mfma_f32_16x16x32_bf16 a[68:71], a[80:83], v[84:87], a[68:71]\n"
        "v_exp_f32 v32, v32\n"
        "v_mfma_f32_16x16x32_bf16 v[52:55], a[116:119], v[68:71], v[52:55]\n"
        "v_exp_f32 v33, v33\n"
        "v_mfma_f32_16x16x32_bf16 v[196:199], a[148:151], v[84:87], v[196:199]\n"
        "ds_read_b128 a[112:115], v240 offset:12288\n"
        "v_cvt_pk_bf16_f32 v16, v32, v33\n"
        "v_mfma_f32_16x16x32_bf16 v[52:55], a[120:123], v[72:75], v[52:55]\n"
        "v_exp_f32 v34, v34\n"
        "v_mfma_f32_16x16x32_bf16 v[56:59], a[120:123], v[104:107], v[56:59]\n"
        "v_exp_f32 v35, v35\n"
        "v_mfma_f32_16x16x32_bf16 v[200:203], a[152:155], v[84:87], v[200:203]\n"
        "ds_read_b128 a[116:119], v240 offset:13312\n"
        "v_cvt_pk_bf16_f32 v17, v34, v35\n"
        "v_mfma_f32_16x16x32_bf16 v[52:55], a[124:127], v[76:79], v[52:55]\n"
        "s_waitcnt lgkmcnt(3)\n"
        "v_exp_f32 v36, v36\n"
        "v_mfma_f32_16x16x32_bf16 v[56:59], a[124:127], v[108:111], v[56:59]\n"
        "s_barrier\n"
        "v_exp_f32 v37, v37\n"
        "v_mfma_f32_16x16x32_bf16 v[172:175], a[156:159], v[80:83], v[172:175]\n"
        "ds_read_b128 a[144:147], v241 offset:0\n"
        "v_cvt_pk_bf16_f32 v20, v36, v37\n"
        "v_mfma_f32_16x16x32_bf16 v[204:207], a[156:159], v[84:87], v[204:207]\n"
        "v_exp_f32 v38, v38\n"
        "v_mfma_f32_16x16x32_bf16 v[32:35], a[96:99], v[0:3], v[224:227]\n"
        "v_exp_f32 v39, v39\n"
        "v_mfma_f32_16x16x32_bf16 v[176:179], a[128:131], v[80:83], v[176:179]\n"
        "ds_read_b128 a[148:151], v241 offset:1024\n"
        "v_cvt_pk_bf16_f32 v21, v38, v39\n"
        "v_mfma_f32_16x16x32_bf16 v[36:39], a[96:99], v[64:67], v[228:231]\n"
        "ds_read_b128 a[120:123], v240 offset:14336\n"
        "v_max3_f32 v246, v42, v43, v246\n"
        "v_mfma_f32_16x16x32_bf16 v[208:211], a[128:131], v[84:87], v[208:211]\n"
        "ds_read_b128 a[152:155], v241 offset:2048\n"
        "v_mov_b32 v247, v246\n"
        "v_mfma_f32_16x16x32_bf16 v[32:35], a[100:103], v[4:7], v[32:35]\n"
        "ds_read_b128 a[128:131], v241 offset:4096\n"
        "v_mfma_f32_16x16x32_bf16 v[36:39], a[100:103], v[68:71], v[36:39]\n"
        "v_permlane16_swap_b32 v246, v247\n"
        "v_mfma_f32_16x16x32_bf16 v[180:183], a[132:135], v[80:83], v[180:183]\n"
        "ds_read_b128 a[124:127], v240 offset:15360\n"
        "v_max_f32 v246, v246, v247\n"
        "v_mfma_f32_16x16x32_bf16 v[212:215], a[132:135], v[84:87], v[212:215]\n"
        "ds_read_b128 a[156:159], v241 offset:3072\n"
        "v_mov_b32 v247, v246\n"
        "v_mfma_f32_16x16x32_bf16 v[32:35], a[104:107], v[8:11], v[32:35]\n"
        "s_waitcnt lgkmcnt(6)\n"
        "v_permlane32_swap_b32 v246, v247\n"
        "v_mfma_f32_16x16x32_bf16 v[36:39], a[104:107], v[72:75], v[36:39]\n"
        "ds_read_b128 a[132:135], v241 offset:5120\n"
        "v_max_f32 v246, v246, v247\n"
        "v_mfma_f32_16x16x32_bf16 v[184:187], a[136:139], v[80:83], v[184:187]\n"
        "v_sub_f32 v232, 0, v246\n"
        "v_sub_f32 v233, 0, v246\n"
        "v_sub_f32 v234, 0, v246\n"
        "v_sub_f32 v235, 0, v246\n"
        "v_add_f32 v40, v40, v232\n"
        "v_add_f32 v41, v41, v232\n"
        "v_add_f32 v42, v42, v232\n"
        "v_add_f32 v43, v43, v232\n"
        "v_mfma_f32_16x16x32_bf16 v[216:219], a[136:139], v[84:87], v[216:219]\n"
        "v_exp_f32 v40, v40\n"
        "v_mfma_f32_16x16x32_bf16 v[32:35], a[108:111], v[12:15], v[32:35]\n"
        "v_exp_f32 v41, v41\n"
        "v_mfma_f32_16x16x32_bf16 v[36:39], a[108:111], v[76:79], v[36:39]\n"
        "v_cvt_pk_bf16_f32 v24, v40, v41\n"
        "v_cndmask_b32 v40, v232, v242, s[30:31]\n"
        "v_cndmask_b32 v41, v232, v242, s[56:57]\n"
        "v_mfma_f32_16x16x32_bf16 v[188:191], a[140:143], v[80:83], v[188:191]\n"
        "v_exp_f32 v42, v42\n"
        "v_mfma_f32_16x16x32_bf16 v[220:223], a[140:143], v[84:87], v[220:223]\n"
        "v_exp_f32 v43, v43\n"
        "v_exp_f32 v48, v48\n"
        "v_cvt_pk_bf16_f32 v25, v42, v43\n"
        "v_cndmask_b32 v42, v232, v242, s[58:59]\n"
        "v_cndmask_b32 v43, v232, v242, s[60:61]\n"
        "s_nop 1\n"
        "v_mfma_f32_16x16x32_bf16 v[40:43], a[96:99], v[96:99], v[40:43]\n"
        "v_exp_f32 v49, v49\n"
        "v_mfma_f32_16x16x32_bf16 v[40:43], a[100:103], v[100:103], v[40:43]\n"
        "ds_read_b128 a[96:99], v240 offset:16384\n"
        "v_cvt_pk_bf16_f32 v18, v48, v49\n"
        "v_mfma_f32_16x16x32_bf16 v[40:43], a[104:107], v[104:107], v[40:43]\n"
        "v_exp_f32 v50, v50\n"
        "v_mfma_f32_16x16x32_bf16 v[40:43], a[108:111], v[108:111], v[40:43]\n"
        "v_exp_f32 v51, v51\n"
        "v_exp_f32 v52, v52\n"
        "v_cvt_pk_bf16_f32 v19, v50, v51\n"
        "s_nop 1\n"
        "v_mfma_f32_16x16x32_bf16 a[64:67], a[80:83], v[16:19], a[64:67]\n"
        "s_waitcnt lgkmcnt(1)\n"
        "v_exp_f32 v53, v53\n"
        "v_mfma_f32_16x16x32_bf16 v[48:51], a[112:115], v[0:3], v[224:227]\n"
        "ds_read_b128 a[100:103], v240 offset:17408\n"
        "v_cvt_pk_bf16_f32 v22, v52, v53\n"
        "v_mfma_f32_16x16x32_bf16 v[160:163], a[144:147], v[16:19], v[160:163]\n"
        "ds_read_b128 a[104:107], v240 offset:18432\n"
        "v_add_f32 v56, v56, v232\n"
        "v_add_f32 v57, v57, v232\n"
        "v_add_f32 v58, v58, v232\n"
        "v_add_f32 v59, v59, v232\n"
        "v_mfma_f32_16x16x32_bf16 v[48:51], a[116:119], v[4:7], v[48:51]\n"
        "v_exp_f32 v56, v56\n"
        "v_mfma_f32_16x16x32_bf16 v[164:167], a[148:151], v[16:19], v[164:167]\n"
        "v_exp_f32 v57, v57\n"
        "v_mfma_f32_16x16x32_bf16 v[48:51], a[120:123], v[8:11], v[48:51]\n"
        "v_exp_f32 v58, v58\n"
        "v_cvt_pk_bf16_f32 v26, v56, v57\n"
        "v_mfma_f32_16x16x32_bf16 v[168:171], a[152:155], v[16:19], v[168:171]\n"
        "v_exp_f32 v59, v59\n"
        "v_cndmask_b32 v56, v232, v242, s[62:63]\n"
        "v_cvt_pk_bf16_f32 v27, v58, v59\n"
        "s_nop 1\n"
        "v_mfma_f32_16x16x32_bf16 a[16:19], a[128:131], v[24:27], 0\n"
        "global_load_dwordx2 v[252:253], v245, s[48:49] sc1\n"
        "ds_read_b128 a[136:139], v241 offset:6144\n"
        "v_mfma_f32_16x16x32_bf16 a[0:3], a[144:147], v[24:27], 0\n"
        "v_exp_f32 v54, v54\n"
        "v_mfma_f32_16x16x32_bf16 a[72:75], a[80:83], v[24:27], 0\n"
        "v_exp_f32 v55, v55\n"
        "v_mfma_f32_16x16x32_bf16 a[4:7], a[148:151], v[24:27], 0\n"
        "ds_read_b128 a[108:111], v240 offset:19456\n"
        "v_cvt_pk_bf16_f32 v23, v54, v55\n"
        "s_nop 1\n"
        "v_mfma_f32_16x16x32_bf16 a[68:71], a[80:83], v[20:23], a[68:71]\n"
        "ds_read_b128 a[140:143], v241 offset:7168\n"
        "v_cndmask_b32 v57, v232, v242, s[84:85]\n"
        "v_mfma_f32_16x16x32_bf16 v[52:55], a[112:115], v[64:67], v[228:231]\n"
        "v_cndmask_b32 v58, v232, v242, s[86:87]\n"
        "v_cndmask_b32 v59, v232, v242, s[88:89]\n"
        "s_nop 1\n"
        "v_mfma_f32_16x16x32_bf16 v[56:59], a[112:115], v[96:99], v[56:59]\n"
        "v_exp_f32 v32, v32\n"
        "v_mfma_f32_16x16x32_bf16 v[192:195], a[144:147], v[20:23], v[192:195]\n"
        "v_exp_f32 v33, v33\n"
        "v_mfma_f32_16x16x32_bf16 v[52:55], a[116:119], v[68:71], v[52:55]\n"
        "ds_read_b128 a[112:115], v240 offset:20480\n"
        "v_cvt_pk_bf16_f32 v80, v32, v33\n"
        "v_mfma_f32_16x16x32_bf16 v[56:59], a[116:119], v[100:103], v[56:59]\n"
        "v_exp_f32 v34, v34\n"
        "v_mfma_f32_16x16x32_bf16 v[196:199], a[148:151], v[20:23], v[196:199]\n"
        "v_exp_f32 v35, v35\n"
        "v_mfma_f32_16x16x32_bf16 v[52:55], a[120:123], v[72:75], v[52:55]\n"
        "ds_read_b128 a[144:147], v241 offset:8192\n"
        "v_cvt_pk_bf16_f32 v81, v34, v35\n"
        "v_mfma_f32_16x16x32_bf16 v[56:59], a[120:123], v[104:107], v[56:59]\n"
        "v_exp_f32 v36, v36\n"
        "v_mfma_f32_16x16x32_bf16 v[200:203], a[152:155], v[20:23], v[200:203]\n"
        "v_exp_f32 v37, v37\n"
        "v_mfma_f32_16x16x32_bf16 a[8:11], a[152:155], v[24:27], 0\n"
        "s_waitcnt vmcnt(8) lgkmcnt(2)\n"
        "v_cvt_pk_bf16_f32 v84, v36, v37\n"
        "v_mfma_f32_16x16x32_bf16 v[48:51], a[124:127], v[12:15], v[48:51]\n"
        "ds_read_b128 a[116:119], v240 offset:21504\n"
        "v_cndmask_b32 v44, 0, v242, s[10:11]\n"
        "v_mfma_f32_16x16x32_bf16 v[52:55], a[124:127], v[76:79], v[52:55]\n"
        "v_exp_f32 v38, v38\n"
        "v_mfma_f32_16x16x32_bf16 v[56:59], a[124:127], v[108:111], v[56:59]\n"
        "v_exp_f32 v39, v39\n"
        "v_mfma_f32_16x16x32_bf16 v[172:175], a[156:159], v[16:19], v[172:175]\n"
        "ds_read_b128 a[148:151], v241 offset:9216\n"
        "v_cvt_pk_bf16_f32 v85, v38, v39\n"
        "v_mfma_f32_16x16x32_bf16 v[204:207], a[156:159], v[20:23], v[204:207]\n"
        "ds_read_b128 a[120:123], v240 offset:22528\n"
        "v_cndmask_b32 v45, 0, v242, s[12:13]\n"
        "v_mfma_f32_16x16x32_bf16 a[12:15], a[156:159], v[24:27], 0\n"
        "v_exp_f32 v40, v40\n"
        "v_mfma_f32_16x16x32_bf16 v[32:35], a[96:99], v[0:3], v[224:227]\n"
        "v_exp_f32 v41, v41\n"
        "v_mfma_f32_16x16x32_bf16 v[36:39], a[96:99], v[64:67], v[228:231]\n"
        "ds_read_b128 a[152:155], v241 offset:10240\n"
        "v_cvt_pk_bf16_f32 v88, v40, v41\n"
        "v_mfma_f32_16x16x32_bf16 v[176:179], a[128:131], v[16:19], v[176:179]\n"
        "v_exp_f32 v42, v42\n"
        "v_mfma_f32_16x16x32_bf16 v[208:211], a[128:131], v[20:23], v[208:211]\n"
        "v_exp_f32 v43, v43\n"
        "v_mfma_f32_16x16x32_bf16 v[32:35], a[100:103], v[4:7], v[32:35]\n"
        "ds_read_b128 a[124:127], v240 offset:23552\n"
        "v_cvt_pk_bf16_f32 v89, v42, v43\n"
        "v_mfma_f32_16x16x32_bf16 v[40:43], a[96:99], v[96:99], v[232:235]\n"
        "ds_read_b128 a[156:159], v241 offset:11264\n"
        "v_cndmask_b32 v46, 0, v242, s[14:15]\n"
        "v_mfma_f32_16x16x32_bf16 v[36:39], a[100:103], v[68:71], v[36:39]\n"
        "v_cndmask_b32 v47, 0, v242, s[16:17]\n"
        "s_nop 1\n"
        "v_mfma_f32_16x16x32_bf16 v[44:47], a[96:99], v[128:131], v[44:47]\n"
        "v_exp_f32 v48, v48\n"
        "v_mfma_f32_16x16x32_bf16 v[40:43], a[100:103], v[100:103], v[40:43]\n"
        "v_exp_f32 v49, v49\n"
        "v_mfma_f32_16x16x32_bf16 v[44:47], a[100:103], v[132:135], v[44:47]\n"
        "ds_read_b128 a[96:99], v240 offset:24576\n"
        "v_cvt_pk_bf16_f32 v82, v48, v49\n"
        "v_mfma_f32_16x16x32_bf16 v[180:183], a[132:135], v[16:19], v[180:183]\n"
        "ds_read_b128 a[128:131], v241 offset:12288\n"
        "v_add_u32 v150, s52, v254\n"
        "v_mfma_f32_16x16x32_bf16 v[212:215], a[132:135], v[20:23], v[212:215]\n"
        "s_and_b32 s52, s41, 1\n"
        "s_lshl_b32 s52, s52, 11\n"
        "v_readfirstlane_b32 s53, v112\n"
        "v_mfma_f32_16x16x32_bf16 a[20:23], a[132:135], v[24:27], 0\n"
        "s_lshl_b32 s53, s53, 16\n"
        "v_mov_b32 v155, 0\n"
        "s_nop 0\n"
        "v_writelane_b32 v155, s53, 1\n"
        "v_mfma_f32_16x16x32_bf16 v[32:35], a[104:107], v[8:11], v[32:35]\n"
        "s_lshl2_add_u32 s53, s42, s40\n"
        "s_add_u32 s52, s52, s53\n"
        "s_add_u32 s52, s52, 1920\n"
        "v_mfma_f32_16x16x32_bf16 v[36:39], a[104:107], v[72:75], v[36:39]\n"
        "s_lshr_b32 s53, s41, 2\n"
        "s_lshl1_add_u32 s52, s52, s53\n"
        "s_lshl_b32 s52, s52, 10\n"
        "v_mfma_f32_16x16x32_bf16 v[40:43], a[104:107], v[104:107], v[40:43]\n"
        "s_add_u32 s26, s18, s52\n"
        "s_addc_u32 s27, s19, 0\n"
        "v_mfma_f32_16x16x32_bf16 v[44:47], a[104:107], v[136:139], v[44:47]\n"
        "v_exp_f32 v50, v50\n"
        "v_mfma_f32_16x16x32_bf16 v[184:187], a[136:139], v[16:19], v[184:187]\n"
        "v_exp_f32 v51, v51\n"
        "v_mfma_f32_16x16x32_bf16 v[216:219], a[136:139], v[20:23], v[216:219]\n"
        "ds_read_b128 a[100:103], v240 offset:25600\n"
        "v_cvt_pk_bf16_f32 v83, v50, v51\n"
        "v_mfma_f32_16x16x32_bf16 a[24:27], a[136:139], v[24:27], 0\n"
        "v_exp_f32 v52, v52\n"
        "v_mfma_f32_16x16x32_bf16 a[64:67], a[80:83], v[80:83], a[64:67]\n"
        "v_exp_f32 v53, v53\n"
        "v_mfma_f32_16x16x32_bf16 v[32:35], a[108:111], v[12:15], v[32:35]\n"
        "ds_read_b128 a[132:135], v241 offset:13312\n"
        "v_cvt_pk_bf16_f32 v86, v52, v53\n"
        "s_waitcnt vmcnt(0) lgkmcnt(2)\n"
        "v_cmp_ne_u64 vcc, v[252:253], v[250:251]\n"
        "v_mfma_f32_16x16x32_bf16 v[36:39], a[108:111], v[76:79], v[36:39]\n"
        "s_cbranch_vccnz LPKV_%=\n"
        "LBKV_%=:\n"
        "v_and_b32 v245, 0x70, v254\n"
        "v_lshlrev_b32 v245, 3, v245\n"
        "v_mfma_f32_16x16x32_bf16 v[40:43], a[108:111], v[108:111], v[40:43]\n"
        "s_cmp_eq_u32 s82, s83\n"
        "s_cselect_b64 s[68:69], s[92:93], s[68:69]\n"
        "s_cselect_b32 s80, s50, s78\n"
        "v_mfma_f32_16x16x32_bf16 v[44:47], a[108:111], v[140:143], v[44:47]\n"
        "s_add_u32 m0, s80, 0x0\n"
        "v_exp_f32 v54, v54\n"
        "v_mfma_f32_16x16x32_bf16 v[188:191], a[140:143], v[16:19], v[188:191]\n"
        "global_load_lds_dwordx4 v254, s[68:69] offset:0 sc1\n"
        "ds_read_b128 a[104:107], v240 offset:26624\n"
        "v_mfma_f32_16x16x32_bf16 v[220:223], a[140:143], v[20:23], v[220:223]\n"
        "v_exp_f32 v55, v55\n"
        "v_mfma_f32_16x16x32_bf16 a[28:31], a[140:143], v[24:27], 0\n"
        "ds_read_b128 a[136:139], v241 offset:14336\n"
        "v_cvt_pk_bf16_f32 v87, v54, v55\n"
        "s_nop 1\n"
        "v_mfma_f32_16x16x32_bf16 a[68:71], a[80:83], v[84:87], a[68:71]\n"
        "v_exp_f32 v56, v56\n"
        "v_mfma_f32_16x16x32_bf16 v[48:51], a[112:115], v[0:3], v[224:227]\n"
        "global_load_lds_dwordx4 v254, s[68:69] offset:1024 sc1\n"
        "v_mov_b32 v152, 0\n"
        "v_mfma_f32_16x16x32_bf16 v[52:55], a[112:115], v[64:67], v[228:231]\n"
        "v_exp_f32 v57, v57\n"
        "v_mfma_f32_16x16x32_bf16 v[160:163], a[144:147], v[80:83], v[160:163]\n"
        "v_cvt_pk_bf16_f32 v90, v56, v57\n"
        "v_mov_b32 v153, 0\n"
        "v_mfma_f32_16x16x32_bf16 v[192:195], a[144:147], v[84:87], v[192:195]\n"
        "v_exp_f32 v58, v58\n"
        "v_mfma_f32_16x16x32_bf16 v[48:51], a[116:119], v[4:7], v[48:51]\n"
        "global_load_lds_dwordx4 v254, s[68:69] offset:2048 sc1\n"
        "v_mov_b32 v154, 0\n"
        "v_mfma_f32_16x16x32_bf16 v[52:55], a[116:119], v[68:71], v[52:55]\n"
        "v_exp_f32 v59, v59\n"
        "v_mfma_f32_16x16x32_bf16 v[164:167], a[148:151], v[80:83], v[164:167]\n"
        "v_cvt_pk_bf16_f32 v91, v58, v59\n"
        "v_alignbyte_b32 v112, v113, v112, 2\n"
        "v_mfma_f32_16x16x32_bf16 v[56:59], a[112:115], v[96:99], v[232:235]\n"
        "s_mov_b64 exec, 3\n"
        "ds_write_b128 v150, v[152:155]\n"
        "s_mov_b64 exec, -1\n"
        "v_mfma_f32_16x16x32_bf16 a[0:3], a[144:147], v[88:91], a[0:3]\n"
        "global_load_lds_dwordx4 v254, s[68:69] offset:3072 sc1\n"
        "s_add_u32 m0, s81, 0x0\n"
        "v_alignbyte_b32 v113, v114, v113, 2\n"
        "v_mfma_f32_16x16x32_bf16 a[72:75], a[80:83], v[88:91], a[72:75]\n"
        "ds_read_b128 a[108:111], v240 offset:27648\n"
        "v_alignbyte_b32 v114, v115, v114, 2\n"
        "v_mfma_f32_16x16x32_bf16 v[56:59], a[116:119], v[100:103], v[56:59]\n"
        "v_alignbyte_b32 v115, v144, v115, 2\n"
        "v_alignbyte_b32 v116, v117, v116, 2\n"
        "v_mfma_f32_16x16x32_bf16 v[196:199], a[148:151], v[84:87], v[196:199]\n"
        "ds_write_b128 v150, v[112:115] offset:32\n"
        "v_mfma_f32_16x16x32_bf16 a[4:7], a[148:151], v[88:91], a[4:7]\n"
        "global_load_lds_dwordx4 v254, s[74:75] offset:0 sc1\n"
        "v_alignbyte_b32 v117, v118, v117, 2\n"
        "v_mfma_f32_16x16x32_bf16 v[48:51], a[120:123], v[8:11], v[48:51]\n"
        "v_alignbyte_b32 v118, v119, v118, 2\n"
        "v_alignbyte_b32 v119, v145, v119, 2\n"
        "v_mfma_f32_16x16x32_bf16 v[52:55], a[120:123], v[72:75], v[52:55]\n"
        "ds_write_b128 v150, v[116:119] offset:1056\n"
        "v_alignbyte_b32 v120, v121, v120, 2\n"
        "global_load_lds_dwordx4 v254, s[74:75] offset:1024 sc1\n"
        "v_alignbyte_b32 v121, v122, v121, 2\n"
        "v_alignbyte_b32 v122, v123, v122, 2\n"
        "v_alignbyte_b32 v123, v146, v123, 2\n"
        "v_alignbyte_b32 v124, v125, v124, 2\n"
        "v_alignbyte_b32 v125, v126, v125, 2\n"
        "v_alignbyte_b32 v126, v127, v126, 2\n"
        "v_alignbyte_b32 v127, v147, v127, 2\n"
        "v_mfma_f32_16x16x32_bf16 v[56:59], a[120:123], v[104:107], v[56:59]\n"
        "ds_write_b128 v150, v[120:123] offset:2080\n"
        "global_load_lds_dwordx4 v254, s[74:75] offset:2048 sc1\n"
        "v_cndmask_b32 v60, 0, v242, s[20:21]\n"
        "v_cndmask_b32 v61, 0, v242, s[22:23]\n"
        "v_cndmask_b32 v62, 0, v242, s[24:25]\n"
        "v_cndmask_b32 v63, 0, v242, s[28:29]\n"
        "s_nop 1\n"
        "v_mfma_f32_16x16x32_bf16 v[60:63], a[112:115], v[128:131], v[60:63]\n"
        "ds_write_b128 v150, v[124:127] offset:3104\n"
        "v_mfma_f32_16x16x32_bf16 v[60:63], a[116:119], v[132:135], v[60:63]\n"
        "global_load_lds_dwordx4 v254, s[74:75] offset:3072 sc1\n"
        "ds_read_b128 a[140:143], v241 offset:15360\n"
        "v_mfma_f32_16x16x32_bf16 v[60:63], a[120:123], v[136:139], v[60:63]\n"
        "v_exp_f32 v32, v32\n"
        "v_mfma_f32_16x16x32_bf16 v[168:171], a[152:155], v[80:83], v[168:171]\n"
        "v_exp_f32 v33, v33\n"
        "v_mfma_f32_16x16x32_bf16 v[200:203], a[152:155], v[84:87], v[200:203]\n"
        "ds_read_b128 a[112:115], v240 offset:28672\n"
        "v_cvt_pk_bf16_f32 v16, v32, v33\n"
        "v_mfma_f32_16x16x32_bf16 a[8:11], a[152:155], v[88:91], a[8:11]\n"
        "v_exp_f32 v34, v34\n"
        "v_mfma_f32_16x16x32_bf16 v[48:51], a[124:127], v[12:15], v[48:51]\n"
        "v_exp_f32 v35, v35\n"
        "v_mfma_f32_16x16x32_bf16 v[52:55], a[124:127], v[76:79], v[52:55]\n"
        "ds_read_b128 a[144:147], v241 offset:16384\n"
        "v_cvt_pk_bf16_f32 v17, v34, v35\n"
        "v_mfma_f32_16x16x32_bf16 v[56:59], a[124:127], v[108:111], v[56:59]\n"
        "v_exp_f32 v36, v36\n"
        "v_mfma_f32_16x16x32_bf16 v[60:63], a[124:127], v[140:143], v[60:63]\n"
        "v_exp_f32 v37, v37\n"
        "v_mfma_f32_16x16x32_bf16 v[172:175], a[156:159], v[80:83], v[172:175]\n"
        "ds_read_b128 a[116:119], v240 offset:29696\n"
        "v_cvt_pk_bf16_f32 v20, v36, v37\n"
        "v_mfma_f32_16x16x32_bf16 v[204:207], a[156:159], v[84:87], v[204:207]\n"
        "s_waitcnt lgkmcnt(3)\n"
        "v_exp_f32 v38, v38\n"
        "v_mfma_f32_16x16x32_bf16 a[12:15], a[156:159], v[88:91], a[12:15]\n"
        "v_exp_f32 v39, v39\n"
        "v_mfma_f32_16x16x32_bf16 v[32:35], a[96:99], v[0:3], v[224:227]\n"
        "ds_read_b128 a[148:151], v241 offset:17408\n"
        "v_cvt_pk_bf16_f32 v21, v38, v39\n"
        "v_mfma_f32_16x16x32_bf16 v[36:39], a[96:99], v[64:67], v[228:231]\n"
        "v_exp_f32 v40, v40\n"
        "v_mfma_f32_16x16x32_bf16 v[176:179], a[128:131], v[80:83], v[176:179]\n"
        "v_exp_f32 v41, v41\n"
        "v_mfma_f32_16x16x32_bf16 v[208:211], a[128:131], v[84:87], v[208:211]\n"
        "ds_read_b128 a[120:123], v240 offset:30720\n"
        "v_cvt_pk_bf16_f32 v24, v40, v41\n"
        "v_mfma_f32_16x16x32_bf16 a[16:19], a[128:131], v[88:91], a[16:19]\n"
        "v_exp_f32 v42, v42\n"
        "v_mfma_f32_16x16x32_bf16 v[32:35], a[100:103], v[4:7], v[32:35]\n"
        "s_add_u32 s82, s82, 1\n"
        "v_exp_f32 v43, v43\n"
        "v_mfma_f32_16x16x32_bf16 v[36:39], a[100:103], v[68:71], v[36:39]\n"
        "ds_read_b128 a[152:155], v241 offset:18432\n"
        "s_xor_b32 s78, s78, 0x8000\n"
        "v_cvt_pk_bf16_f32 v25, v42, v43\n"
        "v_mfma_f32_16x16x32_bf16 v[40:43], a[96:99], v[96:99], v[232:235]\n"
        "ds_read_b128 a[124:127], v240 offset:31744\n"
        "s_add_u32 s81, s78, 4096\n"
        "v_max_f32 v248, v44, v45\n"
        "v_mfma_f32_16x16x32_bf16 v[40:43], a[100:103], v[100:103], v[40:43]\n"
        "ds_read_b128 a[156:159], v241 offset:19456\n"
        "s_add_u32 m0, s79, 0x0\n"
        "v_max3_f32 v248, v46, v47, v248\n"
        "v_mfma_f32_16x16x32_bf16 v[180:183], a[132:135], v[80:83], v[180:183]\n"
        "s_cmp_ge_u32 s82, s44\n"
        "s_addc_u32 s90, s82, 0\n"
        "v_mov_b32 v249, v248\n"
        "v_mfma_f32_16x16x32_bf16 v[212:215], a[132:135], v[84:87], v[212:215]\n"
        "s_min_u32 s90, s90, 15\n"
        "v_permlane16_swap_b32 v248, v249\n"
        "v_max_f32 v248, v248, v249\n"
        "v_mov_b32 v249, v248\n"
        "v_mfma_f32_16x16x32_bf16 a[20:23], a[132:135], v[88:91], a[20:23]\n"
        "s_lshl_b32 s90, s90, 15\n"
        "v_permlane32_swap_b32 v248, v249\n"
        "v_max_f32 v248, v248, v249\n"
        "v_sub_f32 v236, 0, v248\n"
        "v_sub_f32 v237, 0, v248\n"
        "v_sub_f32 v238, 0, v248\n"
        "v_sub_f32 v239, 0, v248\n"
        "v_add_f32 v44, v44, v236\n"
        "v_add_f32 v45, v45, v236\n"
        "v_add_f32 v46, v46, v236\n"
        "v_add_f32 v47, v47, v236\n"
        "v_mfma_f32_16x16x32_bf16 v[32:35], a[104:107], v[8:11], v[32:35]\n"
        "v_exp_f32 v44, v44\n"
        "v_mfma_f32_16x16x32_bf16 v[36:39], a[104:107], v[72:75], v[36:39]\n"
        "v_exp_f32 v45, v45\n"
        "v_mfma_f32_16x16x32_bf16 v[40:43], a[104:107], v[104:107], v[40:43]\n"
        "s_add_u32 s68, s64, s90\n"
        "s_addc_u32 s69, s65, 0\n"
        "v_cvt_pk_bf16_f32 v28, v44, v45\n"
        "v_cndmask_b32 v44, v236, v242, s[30:31]\n"
        "v_cndmask_b32 v45, v236, v242, s[56:57]\n"
        "v_mfma_f32_16x16x32_bf16 v[184:187], a[136:139], v[80:83], v[184:187]\n"
        "v_exp_f32 v46, v46\n"
        "v_mfma_f32_16x16x32_bf16 v[216:219], a[136:139], v[84:87], v[216:219]\n"
        "v_exp_f32 v47, v47\n"
        "v_mfma_f32_16x16x32_bf16 a[24:27], a[136:139], v[88:91], a[24:27]\n"
        "s_add_u32 s74, s68, 0x1000\n"
        "s_addc_u32 s75, s69, 0\n"
        "v_cvt_pk_bf16_f32 v29, v46, v47\n"
        "v_cndmask_b32 v46, v236, v242, s[58:59]\n"
        "v_cndmask_b32 v47, v236, v242, s[60:61]\n"
        "s_nop 1\n"
        "v_mfma_f32_16x16x32_bf16 v[44:47], a[96:99], v[128:131], v[44:47]\n"
        "v_exp_f32 v48, v48\n"
        "v_mfma_f32_16x16x32_bf16 v[44:47], a[100:103], v[132:135], v[44:47]\n"
        "v_exp_f32 v49, v49\n"
        "v_mfma_f32_16x16x32_bf16 v[44:47], a[104:107], v[136:139], v[44:47]\n"
        "global_load_lds_dwordx4 v254, s[76:77] offset:0 sc1\n"
        "v_cvt_pk_bf16_f32 v18, v48, v49\n"
        "v_mfma_f32_16x16x32_bf16 v[32:35], a[108:111], v[12:15], v[32:35]\n"
        "v_exp_f32 v50, v50\n"
        "v_mfma_f32_16x16x32_bf16 v[36:39], a[108:111], v[76:79], v[36:39]\n"
        "v_exp_f32 v51, v51\n"
        "v_mfma_f32_16x16x32_bf16 v[40:43], a[108:111], v[108:111], v[40:43]\n"
        "ds_read_b128 a[128:131], v241 offset:20480\n"
        "v_cvt_pk_bf16_f32 v19, v50, v51\n"
        "s_nop 1\n"
        "v_mfma_f32_16x16x32_bf16 a[64:67], a[80:83], v[16:19], a[64:67]\n"
        "s_waitcnt lgkmcnt(1)\n"
        "v_xor_b32 v240, 0x8000, v240\n"
        "v_mfma_f32_16x16x32_bf16 v[44:47], a[108:111], v[140:143], v[44:47]\n"
        "s_cmp_eq_u32 s82, s83\n"
        "s_cselect_b64 s[68:69], s[92:93], s[68:69]\n"
        "s_cselect_b32 s80, s50, s78\n"
        "v_mfma_f32_16x16x32_bf16 v[188:191], a[140:143], v[80:83], v[188:191]\n"
        "s_cmp_eq_u32 s82, s83\n"
        "s_cselect_b64 s[74:75], s[94:95], s[74:75]\n"
        "s_cselect_b32 s81, s51, s81\n"
        "v_mfma_f32_16x16x32_bf16 v[220:223], a[140:143], v[84:87], v[220:223]\n"
        "global_load_lds_dwordx4 v254, s[76:77] offset:1024 sc1\n"
        "ds_read_b128 a[132:135], v241 offset:21504\n"
        "v_mfma_f32_16x16x32_bf16 a[28:31], a[140:143], v[88:91], a[28:31]\n"
        "v_exp_f32 v52, v52\n"
        "v_mfma_f32_16x16x32_bf16 v[48:51], a[112:115], v[0:3], v[224:227]\n"
        "v_exp_f32 v53, v53\n"
        "v_mfma_f32_16x16x32_bf16 v[160:163], a[144:147], v[16:19], v[160:163]\n"
        "v_cvt_pk_bf16_f32 v22, v52, v53\n"
        "v_add_f32 v60, v60, v236\n"
        "v_mfma_f32_16x16x32_bf16 v[48:51], a[116:119], v[4:7], v[48:51]\n"
        "global_load_lds_dwordx4 v254, s[76:77] offset:2048 sc1\n"
        "v_add_f32 v61, v61, v236\n"
        "v_exp_f32 v54, v54\n"
        "v_exp_f32 v55, v55\n"
        "v_add_f32 v62, v62, v236\n"
        "v_cvt_pk_bf16_f32 v23, v54, v55\n"
        "s_nop 1\n"
        "v_mfma_f32_16x16x32_bf16 a[68:71], a[80:83], v[20:23], a[68:71]\n"
        "v_exp_f32 v56, v56\n"
        "v_mfma_f32_16x16x32_bf16 v[52:55], a[112:115], v[64:67], v[228:231]\n"
        "global_load_lds_dwordx4 v254, s[76:77] offset:3072 sc1\n"
        "s_add_u32 s76, s76, 0x1000\n"
        "s_addc_u32 s77, s77, 0\n"
        "v_mfma_f32_16x16x32_bf16 v[192:195], a[144:147], v[20:23], v[192:195]\n"
        "s_add_u32 m0, s79, 0x1000\n"
        "v_exp_f32 v57, v57\n"
        "v_mfma_f32_16x16x32_bf16 v[52:55], a[116:119], v[68:71], v[52:55]\n"
        "ds_read_b128 a[136:139], v241 offset:22528\n"
        "v_cvt_pk_bf16_f32 v26, v56, v57\n"
        "v_add_f32 v63, v63, v236\n"
        "v_mfma_f32_16x16x32_bf16 v[164:167], a[148:151], v[16:19], v[164:167]\n"
        "v_exp_f32 v58, v58\n"
        "global_load_lds_dwordx4 v254, s[76:77] offset:0 sc1\n"
        "s_waitcnt vmcnt(5)\n"
        "s_barrier\n"
        "ds_read_b128 a[96:99], v240 offset:0\n"
        "v_mfma_f32_16x16x32_bf16 v[196:199], a[148:151], v[20:23], v[196:199]\n"
        "ds_read_b128 a[100:103], v240 offset:1024\n"
        "v_mfma_f32_16x16x32_bf16 v[48:51], a[120:123], v[8:11], v[48:51]\n"
        "v_exp_f32 v59, v59\n"
        "v_mfma_f32_16x16x32_bf16 v[52:55], a[120:123], v[72:75], v[52:55]\n"
        "global_load_lds_dwordx4 v254, s[76:77] offset:1024 sc1\n"
        "v_cvt_pk_bf16_f32 v27, v58, v59\n"
        "s_nop 1\n"
        "v_mfma_f32_16x16x32_bf16 a[72:75], a[80:83], v[24:27], a[72:75]\n"
        "ds_read_b128 a[104:107], v240 offset:2048\n"
        "v_mfma_f32_16x16x32_bf16 v[56:59], a[112:115], v[96:99], v[232:235]\n"
        "v_exp_f32 v60, v60\n"
        "v_mfma_f32_16x16x32_bf16 a[0:3], a[144:147], v[24:27], a[0:3]\n"
        "v_exp_f32 v61, v61\n"
        "v_mfma_f32_16x16x32_bf16 v[56:59], a[116:119], v[100:103], v[56:59]\n"
        "global_load_lds_dwordx4 v254, s[76:77] offset:2048 sc1\n"
        "v_cvt_pk_bf16_f32 v30, v60, v61\n"
        "v_mfma_f32_16x16x32_bf16 a[4:7], a[148:151], v[24:27], a[4:7]\n"
        "v_exp_f32 v62, v62\n"
        "v_cndmask_b32 v60, v236, v242, s[62:63]\n"
        "v_cndmask_b32 v61, v236, v242, s[84:85]\n"
        "v_mfma_f32_16x16x32_bf16 v[56:59], a[120:123], v[104:107], v[56:59]\n"
        "v_exp_f32 v63, v63\n"
        "v_mfma_f32_16x16x32_bf16 v[168:171], a[152:155], v[16:19], v[168:171]\n"
        "ds_read_b128 a[108:111], v240 offset:3072\n"
        "v_cvt_pk_bf16_f32 v31, v62, v63\n"
        "s_nop 1\n"
        "v_mfma_f32_16x16x32_bf16 a[32:35], a[144:147], v[28:31], 0\n"
        "global_load_lds_dwordx4 v254, s[76:77] offset:3072 sc1\n"
        "s_add_u32 m0, s80, 0x0\n"
        "v_cndmask_b32 v62, v236, v242, s[86:87]\n"
        "v_mfma_f32_16x16x32_bf16 a[76:79], a[80:83], v[28:31], 0\n"
        "ds_read_b128 a[140:143], v241 offset:23552\n"
        "v_cndmask_b32 v63, v236, v242, s[88:89]\n"
        "s_nop 1\n"
        "v_mfma_f32_16x16x32_bf16 v[60:63], a[112:115], v[128:131], v[60:63]\n"
        "v_exp_f32 v32, v32\n"
        "v_mfma_f32_16x16x32_bf16 v[60:63], a[116:119], v[132:135], v[60:63]\n"
        "v_exp_f32 v33, v33\n"
        "global_load_lds_dwordx4 v254, s[68:69] offset:0 sc1\n"
        "v_mfma_f32_16x16x32_bf16 a[36:39], a[148:151], v[28:31], 0\n"
        "ds_read_b128 a[112:115], v240 offset:4096\n"
        "v_cvt_pk_bf16_f32 v80, v32, v33\n"
        "v_mfma_f32_16x16x32_bf16 v[60:63], a[120:123], v[136:139], v[60:63]\n"
        "v_exp_f32 v34, v34\n"
        "v_mfma_f32_16x16x32_bf16 v[200:203], a[152:155], v[20:23], v[200:203]\n"
        "v_exp_f32 v35, v35\n"
        "v_mfma_f32_16x16x32_bf16 a[8:11], a[152:155], v[24:27], a[8:11]\n"
        "global_load_lds_dwordx4 v254, s[68:69] offset:1024 sc1\n"
        "v_cvt_pk_bf16_f32 v81, v34, v35\n"
        "v_mfma_f32_16x16x32_bf16 a[40:43], a[152:155], v[28:31], 0\n"
        "v_exp_f32 v36, v36\n"
        "v_mfma_f32_16x16x32_bf16 v[48:51], a[124:127], v[12:15], v[48:51]\n"
        "v_exp_f32 v37, v37\n"
        "v_mfma_f32_16x16x32_bf16 v[52:55], a[124:127], v[76:79], v[52:55]\n"
        "ds_read_b128 a[144:147], v241 offset:24576\n"
        "v_cvt_pk_bf16_f32 v84, v36, v37\n"
        "global_load_lds_dwordx4 v254, s[68:69] offset:2048 sc1\n"
        "v_mfma_f32_16x16x32_bf16 v[56:59], a[124:127], v[108:111], v[56:59]\n"
        "v_exp_f32 v38, v38\n"
        "v_mfma_f32_16x16x32_bf16 v[60:63], a[124:127], v[140:143], v[60:63]\n"
        "v_exp_f32 v39, v39\n"
        "v_mfma_f32_16x16x32_bf16 v[172:175], a[156:159], v[16:19], v[172:175]\n"
        "ds_read_b128 a[116:119], v240 offset:5120\n"
        "v_cvt_pk_bf16_f32 v85, v38, v39\n"
        "v_mfma_f32_16x16x32_bf16 v[204:207], a[156:159], v[20:23], v[204:207]\n"
        "global_load_lds_dwordx4 v254, s[68:69] offset:3072 sc1\n"
        "v_mfma_f32_16x16x32_bf16 a[12:15], a[156:159], v[24:27], a[12:15]\n"
        "v_exp_f32 v40, v40\n"
        "v_mfma_f32_16x16x32_bf16 a[44:47], a[156:159], v[28:31], 0\n"
        "v_exp_f32 v41, v41\n"
        "v_exp_f32 v42, v42\n"
        "v_cvt_pk_bf16_f32 v88, v40, v41\n"
        "v_exp_f32 v43, v43\n"
        "ds_read_b128 a[148:151], v241 offset:25600\n"
        "global_load_dword v253, v245, s[26:27] offset:0\n"
        "s_sub_u32 s26, s26, 262144\n"
        "s_subb_u32 s27, s27, 0\n"
        "global_load_dword v253, v245, s[26:27] offset:0\n"
        "s_sub_u32 s26, s26, 262144\n"
        "s_subb_u32 s27, s27, 0\n"
        "v_cvt_pk_bf16_f32 v89, v42, v43\n"
        "ds_read_b128 a[120:123], v240 offset:6144\n"
        "v_exp_f32 v44, v44\n"
        "v_exp_f32 v45, v45\n"
        "v_exp_f32 v46, v46\n"
        "v_cvt_pk_bf16_f32 v92, v44, v45\n"
        "v_exp_f32 v47, v47\n"
        "ds_read_b128 a[152:155], v241 offset:26624\n"
        "v_cvt_pk_bf16_f32 v93, v46, v47\n"
        "s_mov_b32 s91, s44\n"
        "s_waitcnt lgkmcnt(10)\n"
        "s_cmp_eq_u32 s91, 0\n"
        "s_cbranch_scc1 LLD_%=\n"
        "; ---- DUAL\n"
        ".p2align 6\n"
        "LDU_%=:\n"
        "v_mfma_f32_16x16x32_bf16 v[32:35], a[96:99], v[0:3], v[224:227]\n"
        "s_add_u32 m0, s81, 0x0\n"
        "ds_read_b128 a[124:127], v240 offset:7168\n"
        "v_mfma_f32_16x16x32_bf16 v[36:39], a[96:99], v[64:67], v[228:231]\n"
        "global_load_lds_dwordx4 v254, s[74:75] offset:0 sc1\n"
        "ds_read_b128 a[156:159], v241 offset:27648\n"
        "v_mfma_f32_16x16x32_bf16 v[40:43], a[96:99], v[96:99], v[232:235]\n"
        "s_waitcnt lgkmcnt(4)\n"
        "v_exp_f32 v48, v48\n"
        "v_mfma_f32_16x16x32_bf16 v[44:47], a[96:99], v[128:131], v[236:239]\n"
        "v_exp_f32 v49, v49\n"
        "v_mfma_f32_16x16x32_bf16 v[176:179], a[128:131], v[16:19], v[176:179]\n"
        "ds_read_b128 a[96:99], v240 offset:8192\n"
        "v_cvt_pk_bf16_f32 v82, v48, v49\n"
        "v_mfma_f32_16x16x32_bf16 v[208:211], a[128:131], v[20:23], v[208:211]\n"
        "global_load_lds_dwordx4 v254, s[74:75] offset:1024 sc1\n"
        "v_mfma_f32_16x16x32_bf16 a[16:19], a[128:131], v[24:27], a[16:19]\n"
        "v_exp_f32 v50, v50\n"
        "v_mfma_f32_16x16x32_bf16 a[48:51], a[128:131], v[28:31], a[48:51]\n"
        "v_exp_f32 v51, v51\n"
        "v_mfma_f32_16x16x32_bf16 v[32:35], a[100:103], v[4:7], v[32:35]\n"
        "ds_read_b128 a[128:131], v241 offset:28672\n"
        "v_cvt_pk_bf16_f32 v83, v50, v51\n"
        "v_mfma_f32_16x16x32_bf16 v[36:39], a[100:103], v[68:71], v[36:39]\n"
        "global_load_lds_dwordx4 v254, s[74:75] offset:2048 sc1\n"
        "v_mfma_f32_16x16x32_bf16 v[40:43], a[100:103], v[100:103], v[40:43]\n"
        "v_exp_f32 v52, v52\n"
        "v_mfma_f32_16x16x32_bf16 v[44:47], a[100:103], v[132:135], v[44:47]\n"
        "v_exp_f32 v53, v53\n"
        "v_mfma_f32_16x16x32_bf16 v[180:183], a[132:135], v[16:19], v[180:183]\n"
        "ds_read_b128 a[100:103], v240 offset:9216\n"
        "v_cvt_pk_bf16_f32 v86, v52, v53\n"
        "v_mfma_f32_16x16x32_bf16 v[212:215], a[132:135], v[20:23], v[212:215]\n"
        "global_load_lds_dwordx4 v254, s[74:75] offset:3072 sc1\n"
        "v_mfma_f32_16x16x32_bf16 a[20:23], a[132:135], v[24:27], a[20:23]\n"
        "v_exp_f32 v54, v54\n"
        "v_mfma_f32_16x16x32_bf16 a[52:55], a[132:135], v[28:31], a[52:55]\n"
        "v_exp_f32 v55, v55\n"
        "v_mfma_f32_16x16x32_bf16 a[64:67], a[80:83], v[80:83], a[64:67]\n"
        "ds_read_b128 a[132:135], v241 offset:29696\n"
        "v_cvt_pk_bf16_f32 v87, v54, v55\n"
        "v_mfma_f32_16x16x32_bf16 v[32:35], a[104:107], v[8:11], v[32:35]\n"
        "s_xor_b32 s79, s79, 0x8000\n"
        "v_exp_f32 v56, v56\n"
        "v_mfma_f32_16x16x32_bf16 v[36:39], a[104:107], v[72:75], v[36:39]\n"
        "s_add_u32 m0, s79, 0x0\n"
        "v_exp_f32 v57, v57\n"
        "v_mfma_f32_16x16x32_bf16 v[40:43], a[104:107], v[104:107], v[40:43]\n"
        "s_add_u32 s76, s66, s90\n"
        "s_addc_u32 s77, s67, 0\n"
        "v_cvt_pk_bf16_f32 v90, v56, v57\n"
        "v_mfma_f32_16x16x32_bf16 v[44:47], a[104:107], v[136:139], v[44:47]\n"
        "s_add_u32 s82, s82, 1\n"
        "v_exp_f32 v58, v58\n"
        "v_mfma_f32_16x16x32_bf16 v[184:187], a[136:139], v[16:19], v[184:187]\n"
        "s_xor_b32 s78, s78, 0x8000\n"
        "v_exp_f32 v59, v59\n"
        "v_mfma_f32_16x16x32_bf16 v[216:219], a[136:139], v[20:23], v[216:219]\n"
        "ds_read_b128 a[104:107], v240 offset:10240\n"
        "s_add_u32 s81, s78, 4096\n"
        "v_cvt_pk_bf16_f32 v91, v58, v59\n"
        "v_mfma_f32_16x16x32_bf16 a[24:27], a[136:139], v[24:27], a[24:27]\n"
        "v_exp_f32 v60, v60\n"
        "v_mfma_f32_16x16x32_bf16 a[56:59], a[136:139], v[28:31], a[56:59]\n"
        "v_exp_f32 v61, v61\n"
        "v_mfma_f32_16x16x32_bf16 a[68:71], a[80:83], v[84:87], a[68:71]\n"
        "ds_read_b128 a[136:139], v241 offset:30720\n"
        "v_cvt_pk_bf16_f32 v94, v60, v61\n"
        "v_mfma_f32_16x16x32_bf16 v[32:35], a[108:111], v[12:15], v[32:35]\n"
        "v_exp_f32 v62, v62\n"
        "v_mfma_f32_16x16x32_bf16 v[36:39], a[108:111], v[76:79], v[36:39]\n"
        "v_exp_f32 v63, v63\n"
        "v_mfma_f32_16x16x32_bf16 v[40:43], a[108:111], v[108:111], v[40:43]\n"
        "s_cmp_ge_u32 s82, s44\n"
        "s_addc_u32 s90, s82, 0\n"
        "v_cvt_pk_bf16_f32 v95, v62, v63\n"
        "v_mfma_f32_16x16x32_bf16 v[44:47], a[108:111], v[140:143], v[44:47]\n"
        "ds_read_b128 a[108:111], v240 offset:11264\n"
        "s_min_u32 s90, s90, 15\n"
        "s_lshl_b32 s90, s90, 15\n"
        "v_mfma_f32_16x16x32_bf16 v[188:191], a[140:143], v[16:19], v[188:191]\n"
        "v_exp_f32 v32, v32\n"
        "v_mfma_f32_16x16x32_bf16 v[220:223], a[140:143], v[20:23], v[220:223]\n"
        "v_exp_f32 v33, v33\n"
        "v_mfma_f32_16x16x32_bf16 a[28:31], a[140:143], v[24:27], a[28:31]\n"
        "v_cvt_pk_bf16_f32 v16, v32, v33\n"
        "v_mfma_f32_16x16x32_bf16 a[60:63], a[140:143], v[28:31], a[60:63]\n"
        "v_exp_f32 v34, v34\n"
        "v_mfma_f32_16x16x32_bf16 a[72:75], a[80:83], v[88:91], a[72:75]\n"
        "v_exp_f32 v35, v35\n"
        "v_mfma_f32_16x16x32_bf16 v[48:51], a[112:115], v[0:3], v[224:227]\n"
        "ds_read_b128 a[140:143], v241 offset:31744\n"
        "v_cvt_pk_bf16_f32 v17, v34, v35\n"
        "v_mfma_f32_16x16x32_bf16 v[52:55], a[112:115], v[64:67], v[228:231]\n"
        "v_exp_f32 v36, v36\n"
        "v_mfma_f32_16x16x32_bf16 v[56:59], a[112:115], v[96:99], v[232:235]\n"
        "v_exp_f32 v37, v37\n"
        "v_mfma_f32_16x16x32_bf16 v[60:63], a[112:115], v[128:131], v[236:239]\n"
        "ds_read_b128 a[112:115], v240 offset:12288\n"
        "v_cvt_pk_bf16_f32 v20, v36, v37\n"
        "v_mfma_f32_16x16x32_bf16 v[160:163], a[144:147], v[80:83], v[160:163]\n"
        "v_exp_f32 v38, v38\n"
        "v_mfma_f32_16x16x32_bf16 v[192:195], a[144:147], v[84:87], v[192:195]\n"
        "v_exp_f32 v39, v39\n"
        "v_mfma_f32_16x16x32_bf16 a[0:3], a[144:147], v[88:91], a[0:3]\n"
        "v_cvt_pk_bf16_f32 v21, v38, v39\n"
        "v_xor_b32 v241, 0x8000, v241\n"
        "v_mfma_f32_16x16x32_bf16 a[32:35], a[144:147], v[92:95], a[32:35]\n"
        "v_exp_f32 v40, v40\n"
        "v_mfma_f32_16x16x32_bf16 a[76:79], a[80:83], v[92:95], a[76:79]\n"
        "s_waitcnt vmcnt(10) lgkmcnt(1)\n"
        "s_barrier\n"
        "v_mfma_f32_16x16x32_bf16 v[48:51], a[116:119], v[4:7], v[48:51]\n"
        "global_load_lds_dwordx4 v254, s[76:77] offset:0 sc1\n"
        "ds_read_b128 a[144:147], v241 offset:0\n"
        "v_mfma_f32_16x16x32_bf16 v[52:55], a[116:119], v[68:71], v[52:55]\n"
        "v_exp_f32 v41, v41\n"
        "v_mfma_f32_16x16x32_bf16 v[56:59], a[116:119], v[100:103], v[56:59]\n"
        "s_add_u32 s68, s64, s90\n"
        "s_addc_u32 s69, s65, 0\n"
        "v_cvt_pk_bf16_f32 v24, v40, v41\n"
        "v_mfma_f32_16x16x32_bf16 v[60:63], a[116:119], v[132:135], v[60:63]\n"
        "ds_read_b128 a[116:119], v240 offset:13312\n"
        "s_add_u32 s74, s68, 0x1000\n"
        "s_addc_u32 s75, s69, 0\n"
        "v_mfma_f32_16x16x32_bf16 v[164:167], a[148:151], v[80:83], v[164:167]\n"
        "v_exp_f32 v42, v42\n"
        "v_mfma_f32_16x16x32_bf16 v[196:199], a[148:151], v[84:87], v[196:199]\n"
        "v_exp_f32 v43, v43\n"
        "v_mfma_f32_16x16x32_bf16 a[4:7], a[148:151], v[88:91], a[4:7]\n"
        "global_load_lds_dwordx4 v254, s[76:77] offset:1024 sc1\n"
        "v_cvt_pk_bf16_f32 v25, v42, v43\n"
        "v_mfma_f32_16x16x32_bf16 a[36:39], a[148:151], v[92:95], a[36:39]\n"
        "ds_read_b128 a[148:151], v241 offset:1024\n"
        "v_mfma_f32_16x16x32_bf16 v[48:51], a[120:123], v[8:11], v[48:51]\n"
        "v_exp_f32 v44, v44\n"
        "v_mfma_f32_16x16x32_bf16 v[52:55], a[120:123], v[72:75], v[52:55]\n"
        "v_exp_f32 v45, v45\n"
        "v_mfma_f32_16x16x32_bf16 v[56:59], a[120:123], v[104:107], v[56:59]\n"
        "global_load_lds_dwordx4 v254, s[76:77] offset:2048 sc1\n"
        "v_cvt_pk_bf16_f32 v28, v44, v45\n"
        "v_mfma_f32_16x16x32_bf16 v[60:63], a[120:123], v[136:139], v[60:63]\n"
        "ds_read_b128 a[120:123], v240 offset:14336\n"
        "v_mfma_f32_16x16x32_bf16 v[168:171], a[152:155], v[80:83], v[168:171]\n"
        "v_exp_f32 v46, v46\n"
        "v_mfma_f32_16x16x32_bf16 v[200:203], a[152:155], v[84:87], v[200:203]\n"
        "v_exp_f32 v47, v47\n"
        "v_mfma_f32_16x16x32_bf16 a[8:11], a[152:155], v[88:91], a[8:11]\n"
        "global_load_lds_dwordx4 v254, s[76:77] offset:3072 sc1\n"
        "s_add_u32 m0, s79, 0x1000\n"
        "v_cvt_pk_bf16_f32 v29, v46, v47\n"
        "v_mfma_f32_16x16x32_bf16 a[40:43], a[152:155], v[92:95], a[40:43]\n"
        "ds_read_b128 a[152:155], v241 offset:2048\n"
        "s_add_u32 s76, s76, 0x1000\n"
        "s_addc_u32 s77, s77, 0\n"
        "v_mfma_f32_16x16x32_bf16 v[48:51], a[124:127], v[12:15], v[48:51]\n"
        "s_cmp_eq_u32 s82, s83\n"
        "s_cselect_b64 s[68:69], s[92:93], s[68:69]\n"
        "s_cselect_b32 s80, s50, s78\n"
        "v_mfma_f32_16x16x32_bf16 v[52:55], a[124:127], v[76:79], v[52:55]\n"
        "s_cmp_eq_u32 s82, s83\n"
        "s_cselect_b64 s[74:75], s[94:95], s[74:75]\n"
        "s_cselect_b32 s81, s51, s81\n"
        "v_mfma_f32_16x16x32_bf16 v[56:59], a[124:127], v[108:111], v[56:59]\n"
        "global_load_lds_dwordx4 v254, s[76:77] offset:0 sc1\n"
        "v_mfma_f32_16x16x32_bf16 v[60:63], a[124:127], v[140:143], v[60:63]\n"
        "ds_read_b128 a[124:127], v240 offset:15360\n"
        "v_mfma_f32_16x16x32_bf16 v[172:175], a[156:159], v[80:83], v[172:175]\n"
        "v_exp_f32 v48, v48\n"
        "v_mfma_f32_16x16x32_bf16 v[204:207], a[156:159], v[84:87], v[204:207]\n"
        "v_exp_f32 v49, v49\n"
        "v_mfma_f32_16x16x32_bf16 a[12:15], a[156:159], v[88:91], a[12:15]\n"
        "global_load_lds_dwordx4 v254, s[76:77] offset:1024 sc1\n"
        "v_cvt_pk_bf16_f32 v18, v48, v49\n"
        "v_mfma_f32_16x16x32_bf16 a[44:47], a[156:159], v[92:95], a[44:47]\n"
        "v_exp_f32 v50, v50\n"
        "v_mfma_f32_16x16x32_bf16 v[32:35], a[96:99], v[0:3], v[224:227]\n"
        "v_exp_f32 v51, v51\n"
        "v_mfma_f32_16x16x32_bf16 v[36:39], a[96:99], v[64:67], v[228:231]\n"
        "ds_read_b128 a[156:159], v241 offset:3072\n"
        "v_cvt_pk_bf16_f32 v19, v50, v51\n"
        "v_mfma_f32_16x16x32_bf16 v[40:43], a[96:99], v[96:99], v[232:235]\n"
        "global_load_lds_dwordx4 v254, s[76:77] offset:2048 sc1\n"
        "v_mfma_f32_16x16x32_bf16 v[44:47], a[96:99], v[128:131], v[236:239]\n"
        "ds_read_b128 a[96:99], v240 offset:16384\n"
        "v_mfma_f32_16x16x32_bf16 v[176:179], a[128:131], v[80:83], v[176:179]\n"
        "v_exp_f32 v52, v52\n"
        "v_mfma_f32_16x16x32_bf16 v[208:211], a[128:131], v[84:87], v[208:211]\n"
        "v_exp_f32 v53, v53\n"
        "v_mfma_f32_16x16x32_bf16 a[16:19], a[128:131], v[88:91], a[16:19]\n"
        "global_load_lds_dwordx4 v254, s[76:77] offset:3072 sc1\n"
        "v_cvt_pk_bf16_f32 v22, v52, v53\n"
        "v_mfma_f32_16x16x32_bf16 a[48:51], a[128:131], v[92:95], a[48:51]\n"
        "v_exp_f32 v54, v54\n"
        "v_mfma_f32_16x16x32_bf16 v[32:35], a[100:103], v[4:7], v[32:35]\n"
        "v_exp_f32 v55, v55\n"
        "v_mfma_f32_16x16x32_bf16 v[36:39], a[100:103], v[68:71], v[36:39]\n"
        "ds_read_b128 a[128:131], v241 offset:4096\n"
        "s_add_u32 m0, s80, 0x0\n"
        "v_cvt_pk_bf16_f32 v23, v54, v55\n"
        "v_mfma_f32_16x16x32_bf16 v[40:43], a[100:103], v[100:103], v[40:43]\n"
        "s_sub_u32 s91, s91, 1\n"
        "v_exp_f32 v56, v56\n"
        "v_mfma_f32_16x16x32_bf16 v[44:47], a[100:103], v[132:135], v[44:47]\n"
        "ds_read_b128 a[100:103], v240 offset:17408\n"
        "v_mfma_f32_16x16x32_bf16 v[180:183], a[132:135], v[80:83], v[180:183]\n"
        "v_exp_f32 v57, v57\n"
        "v_mfma_f32_16x16x32_bf16 v[212:215], a[132:135], v[84:87], v[212:215]\n"
        "v_cvt_pk_bf16_f32 v26, v56, v57\n"
        "v_mfma_f32_16x16x32_bf16 a[20:23], a[132:135], v[88:91], a[20:23]\n"
        "v_exp_f32 v58, v58\n"
        "v_mfma_f32_16x16x32_bf16 a[52:55], a[132:135], v[92:95], a[52:55]\n"
        "v_exp_f32 v59, v59\n"
        "v_mfma_f32_16x16x32_bf16 a[64:67], a[80:83], v[16:19], a[64:67]\n"
        "ds_read_b128 a[132:135], v241 offset:5120\n"
        "v_cvt_pk_bf16_f32 v27, v58, v59\n"
        "v_mfma_f32_16x16x32_bf16 v[32:35], a[104:107], v[8:11], v[32:35]\n"
        "v_exp_f32 v60, v60\n"
        "v_mfma_f32_16x16x32_bf16 v[36:39], a[104:107], v[72:75], v[36:39]\n"
        "v_exp_f32 v61, v61\n"
        "v_mfma_f32_16x16x32_bf16 v[40:43], a[104:107], v[104:107], v[40:43]\n"
        "v_cvt_pk_bf16_f32 v30, v60, v61\n"
        "v_mfma_f32_16x16x32_bf16 v[44:47], a[104:107], v[136:139], v[44:47]\n"
        "ds_read_b128 a[104:107], v240 offset:18432\n"
        "v_mfma_f32_16x16x32_bf16 v[184:187], a[136:139], v[80:83], v[184:187]\n"
        "v_exp_f32 v62, v62\n"
        "v_mfma_f32_16x16x32_bf16 v[216:219], a[136:139], v[84:87], v[216:219]\n"
        "v_exp_f32 v63, v63\n"
        "v_mfma_f32_16x16x32_bf16 a[24:27], a[136:139], v[88:91], a[24:27]\n"
        "v_cvt_pk_bf16_f32 v31, v62, v63\n"
        "v_mfma_f32_16x16x32_bf16 a[56:59], a[136:139], v[92:95], a[56:59]\n"
        "ds_read_b128 a[136:139], v241 offset:6144\n"
        "v_mfma_f32_16x16x32_bf16 a[68:71], a[80:83], v[20:23], a[68:71]\n"
        "v_mfma_f32_16x16x32_bf16 v[32:35], a[108:111], v[12:15], v[32:35]\n"
        "v_mfma_f32_16x16x32_bf16 v[36:39], a[108:111], v[76:79], v[36:39]\n"
        "v_mfma_f32_16x16x32_bf16 v[40:43], a[108:111], v[108:111], v[40:43]\n"
        "v_mfma_f32_16x16x32_bf16 v[44:47], a[108:111], v[140:143], v[44:47]\n"
        "ds_read_b128 a[108:111], v240 offset:19456\n"
        "s_waitcnt lgkmcnt(2)\n"
        "v_mfma_f32_16x16x32_bf16 v[188:191], a[140:143], v[80:83], v[188:191]\n"
        "v_mfma_f32_16x16x32_bf16 v[220:223], a[140:143], v[84:87], v[220:223]\n"
        "v_mfma_f32_16x16x32_bf16 a[28:31], a[140:143], v[88:91], a[28:31]\n"
        "v_exp_f32 v32, v32\n"
        "v_mfma_f32_16x16x32_bf16 a[60:63], a[140:143], v[92:95], a[60:63]\n"
        "v_exp_f32 v33, v33\n"
        "v_mfma_f32_16x16x32_bf16 a[72:75], a[80:83], v[24:27], a[72:75]\n"
        "global_load_dword v253, v245, s[26:27] offset:0\n"
        "s_sub_u32 s26, s26, 262144\n"
        "s_subb_u32 s27, s27, 0\n"
        "v_mfma_f32_16x16x32_bf16 v[48:51], a[112:115], v[0:3], v[224:227]\n"
        "ds_read_b128 a[140:143], v241 offset:7168\n"
        "v_cvt_pk_bf16_f32 v80, v32, v33\n"
        "v_mfma_f32_16x16x32_bf16 v[52:55], a[112:115], v[64:67], v[228:231]\n"
        "v_exp_f32 v34, v34\n"
        "v_mfma_f32_16x16x32_bf16 v[56:59], a[112:115], v[96:99], v[232:235]\n"
        "v_exp_f32 v35, v35\n"
        "v_mfma_f32_16x16x32_bf16 v[60:63], a[112:115], v[128:131], v[236:239]\n"
        "ds_read_b128 a[112:115], v240 offset:20480\n"
        "v_cvt_pk_bf16_f32 v81, v34, v35\n"
        "v_mfma_f32_16x16x32_bf16 v[160:163], a[144:147], v[16:19], v[160:163]\n"
        "v_exp_f32 v36, v36\n"
        "v_mfma_f32_16x16x32_bf16 v[192:195], a[144:147], v[20:23], v[192:195]\n"
        "v_exp_f32 v37, v37\n"
        "v_mfma_f32_16x16x32_bf16 a[0:3], a[144:147], v[24:27], a[0:3]\n"
        "v_cvt_pk_bf16_f32 v84, v36, v37\n"
        "v_mfma_f32_16x16x32_bf16 a[32:35], a[144:147], v[28:31], a[32:35]\n"
        "v_exp_f32 v38, v38\n"
        "v_mfma_f32_16x16x32_bf16 a[76:79], a[80:83], v[28:31], a[76:79]\n"
        "v_exp_f32 v39, v39\n"
        "v_mfma_f32_16x16x32_bf16 v[48:51], a[116:119], v[4:7], v[48:51]\n"
        "ds_read_b128 a[144:147], v241 offset:8192\n"
        "v_cvt_pk_bf16_f32 v85, v38, v39\n"
        "v_mfma_f32_16x16x32_bf16 v[52:55], a[116:119], v[68:71], v[52:55]\n"
        "v_exp_f32 v40, v40\n"
        "v_mfma_f32_16x16x32_bf16 v[56:59], a[116:119], v[100:103], v[56:59]\n"
        "v_exp_f32 v41, v41\n"
        "v_mfma_f32_16x16x32_bf16 v[60:63], a[116:119], v[132:135], v[60:63]\n"
        "ds_read_b128 a[116:119], v240 offset:21504\n"
        "v_cvt_pk_bf16_f32 v88, v40, v41\n"
        "v_mfma_f32_16x16x32_bf16 v[164:167], a[148:151], v[16:19], v[164:167]\n"
        "v_exp_f32 v42, v42\n"
        "v_mfma_f32_16x16x32_bf16 v[196:199], a[148:151], v[20:23], v[196:199]\n"
        "v_exp_f32 v43, v43\n"
        "v_mfma_f32_16x16x32_bf16 a[4:7], a[148:151], v[24:27], a[4:7]\n"
        "v_cvt_pk_bf16_f32 v89, v42, v43\n"
        "v_mfma_f32_16x16x32_bf16 a[36:39], a[148:151], v[28:31], a[36:39]\n"
        "ds_read_b128 a[148:151], v241 offset:9216\n"
        "v_mfma_f32_16x16x32_bf16 v[48:51], a[120:123], v[8:11], v[48:51]\n"
        "v_exp_f32 v44, v44\n"
        "v_mfma_f32_16x16x32_bf16 v[52:55], a[120:123], v[72:75], v[52:55]\n"
        "v_exp_f32 v45, v45\n"
        "v_mfma_f32_16x16x32_bf16 v[56:59], a[120:123], v[104:107], v[56:59]\n"
        "v_cvt_pk_bf16_f32 v92, v44, v45\n"
        "v_mfma_f32_16x16x32_bf16 v[60:63], a[120:123], v[136:139], v[60:63]\n"
        "ds_read_b128 a[120:123], v240 offset:22528\n"
        "v_mfma_f32_16x16x32_bf16 v[168:171], a[152:155], v[16:19], v[168:171]\n"
        "v_exp_f32 v46, v46\n"
        "v_mfma_f32_16x16x32_bf16 v[200:203], a[152:155], v[20:23], v[200:203]\n"
        "v_exp_f32 v47, v47\n"
        "v_mfma_f32_16x16x32_bf16 a[8:11], a[152:155], v[24:27], a[8:11]\n"
        "v_cvt_pk_bf16_f32 v93, v46, v47\n"
        "v_mfma_f32_16x16x32_bf16 a[40:43], a[152:155], v[28:31], a[40:43]\n"
        "ds_read_b128 a[152:155], v241 offset:10240\n"
        "v_mfma_f32_16x16x32_bf16 v[48:51], a[124:127], v[12:15], v[48:51]\n"
        "v_mfma_f32_16x16x32_bf16 v[52:55], a[124:127], v[76:79], v[52:55]\n"
        "v_mfma_f32_16x16x32_bf16 v[56:59], a[124:127], v[108:111], v[56:59]\n"
        "v_mfma_f32_16x16x32_bf16 v[60:63], a[124:127], v[140:143], v[60:63]\n"
        "ds_read_b128 a[124:127], v240 offset:23552\n"
        "v_mfma_f32_16x16x32_bf16 v[172:175], a[156:159], v[16:19], v[172:175]\n"
        "v_mfma_f32_16x16x32_bf16 v[204:207], a[156:159], v[20:23], v[204:207]\n"
        "v_mfma_f32_16x16x32_bf16 a[12:15], a[156:159], v[24:27], a[12:15]\n"
        "v_mfma_f32_16x16x32_bf16 a[44:47], a[156:159], v[28:31], a[44:47]\n"
        "global_load_dword v253, v244, s[46:47] offset:0\n"
        "ds_read_b128 a[156:159], v241 offset:11264\n"
        "v_mfma_f32_16x16x32_bf16 v[32:35], a[96:99], v[0:3], v[224:227]\n"
        "v_exp_f32 v48, v48\n"
        "v_mfma_f32_16x16x32_bf16 v[36:39], a[96:99], v[64:67], v[228:231]\n"
        "v_exp_f32 v49, v49\n"
        "v_mfma_f32_16x16x32_bf16 v[40:43], a[96:99], v[96:99], v[232:235]\n"
        "v_cvt_pk_bf16_f32 v82, v48, v49\n"
        "v_mfma_f32_16x16x32_bf16 v[44:47], a[96:99], v[128:131], v[236:239]\n"
        "v_exp_f32 v50, v50\n"
        "v_mfma_f32_16x16x32_bf16 v[176:179], a[128:131], v[16:19], v[176:179]\n"
        "v_exp_f32 v51, v51\n"
        "v_mfma_f32_16x16x32_bf16 v[208:211], a[128:131], v[20:23], v[208:211]\n"
        "ds_read_b128 a[96:99], v240 offset:24576\n"
        "v_cvt_pk_bf16_f32 v83, v50, v51\n"
        "v_mfma_f32_16x16x32_bf16 a[16:19], a[128:131], v[24:27], a[16:19]\n"
        "v_exp_f32 v52, v52\n"
        "v_mfma_f32_16x16x32_bf16 a[48:51], a[128:131], v[28:31], a[48:51]\n"
        "v_exp_f32 v53, v53\n"
        "v_mfma_f32_16x16x32_bf16 v[32:35], a[100:103], v[4:7], v[32:35]\n"
        "ds_read_b128 a[128:131], v241 offset:12288\n"
        "v_cvt_pk_bf16_f32 v86, v52, v53\n"
        "v_mfma_f32_16x16x32_bf16 v[36:39], a[100:103], v[68:71], v[36:39]\n"
        "v_exp_f32 v54, v54\n"
        "v_mfma_f32_16x16x32_bf16 v[40:43], a[100:103], v[100:103], v[40:43]\n"
        "v_exp_f32 v55, v55\n"
        "v_mfma_f32_16x16x32_bf16 v[44:47], a[100:103], v[132:135], v[44:47]\n"
        "ds_read_b128 a[100:103], v240 offset:25600\n"
        "v_cvt_pk_bf16_f32 v87, v54, v55\n"
        "v_mfma_f32_16x16x32_bf16 v[180:183], a[132:135], v[16:19], v[180:183]\n"
        "v_exp_f32 v56, v56\n"
        "v_mfma_f32_16x16x32_bf16 v[212:215], a[132:135], v[20:23], v[212:215]\n"
        "v_exp_f32 v57, v57\n"
        "v_mfma_f32_16x16x32_bf16 a[20:23], a[132:135], v[24:27], a[20:23]\n"
        "v_cvt_pk_bf16_f32 v90, v56, v57\n"
        "v_mfma_f32_16x16x32_bf16 a[52:55], a[132:135], v[28:31], a[52:55]\n"
        "s_waitcnt lgkmcnt(2)\n"
        "v_exp_f32 v58, v58\n"
        "v_mfma_f32_16x16x32_bf16 a[64:67], a[80:83], v[80:83], a[64:67]\n"
        "v_exp_f32 v59, v59\n"
        "v_mfma_f32_16x16x32_bf16 v[32:35], a[104:107], v[8:11], v[32:35]\n"
        "ds_read_b128 a[132:135], v241 offset:13312\n"
        "v_cvt_pk_bf16_f32 v91, v58, v59\n"
        "v_mfma_f32_16x16x32_bf16 v[36:39], a[104:107], v[72:75], v[36:39]\n"
        "v_exp_f32 v60, v60\n"
        "v_mfma_f32_16x16x32_bf16 v[40:43], a[104:107], v[104:107], v[40:43]\n"
        "v_exp_f32 v61, v61\n"
        "v_mfma_f32_16x16x32_bf16 v[44:47], a[104:107], v[136:139], v[44:47]\n"
        "ds_read_b128 a[104:107], v240 offset:26624\n"
        "v_cvt_pk_bf16_f32 v94, v60, v61\n"
        "v_mfma_f32_16x16x32_bf16 v[184:187], a[136:139], v[16:19], v[184:187]\n"
        "v_exp_f32 v62, v62\n"
        "v_mfma_f32_16x16x32_bf16 v[216:219], a[136:139], v[20:23], v[216:219]\n"
        "v_exp_f32 v63, v63\n"
        "v_mfma_f32_16x16x32_bf16 a[24:27], a[136:139], v[24:27], a[24:27]\n"
        "v_cvt_pk_bf16_f32 v95, v62, v63\n"
        "v_mfma_f32_16x16x32_bf16 a[56:59], a[136:139], v[28:31], a[56:59]\n"
        "ds_read_b128 a[136:139], v241 offset:14336\n"
        "v_mfma_f32_16x16x32_bf16 a[68:71], a[80:83], v[84:87], a[68:71]\n"
        "v_mfma_f32_16x16x32_bf16 v[32:35], a[108:111], v[12:15], v[32:35]\n"
        "v_mfma_f32_16x16x32_bf16 v[36:39], a[108:111], v[76:79], v[36:39]\n"
        "v_mfma_f32_16x16x32_bf16 v[40:43], a[108:111], v[108:111], v[40:43]\n"
        "v_mfma_f32_16x16x32_bf16 v[44:47], a[108:111], v[140:143], v[44:47]\n"
        "ds_read_b128 a[108:111], v240 offset:27648\n"
        "v_mfma_f32_16x16x32_bf16 v[188:191], a[140:143], v[16:19], v[188:191]\n"
        "v_mfma_f32_16x16x32_bf16 v[220:223], a[140:143], v[20:23], v[220:223]\n"
        "v_mfma_f32_16x16x32_bf16 a[28:31], a[140:143], v[24:27], a[28:31]\n"
        "v_mfma_f32_16x16x32_bf16 a[60:63], a[140:143], v[28:31], a[60:63]\n"
        "v_exp_f32 v32, v32\n"
        "v_mfma_f32_16x16x32_bf16 a[72:75], a[80:83], v[88:91], a[72:75]\n"
        "global_load_dword v253, v245, s[26:27] offset:0\n"
        "s_sub_u32 s26, s26, 262144\n"
        "s_subb_u32 s27, s27, 0\n"
        "v_mfma_f32_16x16x32_bf16 v[48:51], a[112:115], v[0:3], v[224:227]\n"
        "v_exp_f32 v33, v33\n"
        "v_mfma_f32_16x16x32_bf16 v[52:55], a[112:115], v[64:67], v[228:231]\n"
        "ds_read_b128 a[140:143], v241 offset:15360\n"
        "v_cvt_pk_bf16_f32 v16, v32, v33\n"
        "v_mfma_f32_16x16x32_bf16 v[56:59], a[112:115], v[96:99], v[232:235]\n"
        "v_exp_f32 v34, v34\n"
        "v_mfma_f32_16x16x32_bf16 v[60:63], a[112:115], v[128:131], v[236:239]\n"
        "v_exp_f32 v35, v35\n"
        "v_mfma_f32_16x16x32_bf16 v[160:163], a[144:147], v[80:83], v[160:163]\n"
        "ds_read_b128 a[112:115], v240 offset:28672\n"
        "v_cvt_pk_bf16_f32 v17, v34, v35\n"
        "v_mfma_f32_16x16x32_bf16 v[192:195], a[144:147], v[84:87], v[192:195]\n"
        "v_exp_f32 v36, v36\n"
        "v_mfma_f32_16x16x32_bf16 a[0:3], a[144:147], v[88:91], a[0:3]\n"
        "v_exp_f32 v37, v37\n"
        "v_mfma_f32_16x16x32_bf16 a[32:35], a[144:147], v[92:95], a[32:35]\n"
        "ds_read_b128 a[144:147], v241 offset:16384\n"
        "v_cvt_pk_bf16_f32 v20, v36, v37\n"
        "v_mfma_f32_16x16x32_bf16 a[76:79], a[80:83], v[92:95], a[76:79]\n"
        "v_exp_f32 v38, v38\n"
        "v_mfma_f32_16x16x32_bf16 v[48:51], a[116:119], v[4:7], v[48:51]\n"
        "v_exp_f32 v39, v39\n"
        "v_mfma_f32_16x16x32_bf16 v[52:55], a[116:119], v[68:71], v[52:55]\n"
        "v_cvt_pk_bf16_f32 v21, v38, v39\n"
        "v_mfma_f32_16x16x32_bf16 v[56:59], a[116:119], v[100:103], v[56:59]\n"
        "v_exp_f32 v40, v40\n"
        "v_mfma_f32_16x16x32_bf16 v[60:63], a[116:119], v[132:135], v[60:63]\n"
        "ds_read_b128 a[116:119], v240 offset:29696\n"
        "v_mfma_f32_16x16x32_bf16 v[164:167], a[148:151], v[80:83], v[164:167]\n"
        "v_exp_f32 v41, v41\n"
        "v_mfma_f32_16x16x32_bf16 v[196:199], a[148:151], v[84:87], v[196:199]\n"
        "v_cvt_pk_bf16_f32 v24, v40, v41\n"
        "v_mfma_f32_16x16x32_bf16 a[4:7], a[148:151], v[88:91], a[4:7]\n"
        "v_exp_f32 v42, v42\n"
        "v_mfma_f32_16x16x32_bf16 a[36:39], a[148:151], v[92:95], a[36:39]\n"
        "v_exp_f32 v43, v43\n"
        "v_mfma_f32_16x16x32_bf16 v[48:51], a[120:123], v[8:11], v[48:51]\n"
        "ds_read_b128 a[148:151], v241 offset:17408\n"
        "v_cvt_pk_bf16_f32 v25, v42, v43\n"
        "v_mfma_f32_16x16x32_bf16 v[52:55], a[120:123], v[72:75], v[52:55]\n"
        "v_exp_f32 v44, v44\n"
        "v_mfma_f32_16x16x32_bf16 v[56:59], a[120:123], v[104:107], v[56:59]\n"
        "v_exp_f32 v45, v45\n"
        "v_mfma_f32_16x16x32_bf16 v[60:63], a[120:123], v[136:139], v[60:63]\n"
        "ds_read_b128 a[120:123], v240 offset:30720\n"
        "v_cvt_pk_bf16_f32 v28, v44, v45\n"
        "v_mfma_f32_16x16x32_bf16 v[168:171], a[152:155], v[80:83], v[168:171]\n"
        "v_exp_f32 v46, v46\n"
        "v_mfma_f32_16x16x32_bf16 v[200:203], a[152:155], v[84:87], v[200:203]\n"
        "v_exp_f32 v47, v47\n"
        "v_mfma_f32_16x16x32_bf16 a[8:11], a[152:155], v[88:91], a[8:11]\n"
        "v_cvt_pk_bf16_f32 v29, v46, v47\n"
        "v_mfma_f32_16x16x32_bf16 a[40:43], a[152:155], v[92:95], a[40:43]\n"
        "ds_read_b128 a[152:155], v241 offset:18432\n"
        "v_mfma_f32_16x16x32_bf16 v[48:51], a[124:127], v[12:15], v[48:51]\n"
        "v_mfma_f32_16x16x32_bf16 v[52:55], a[124:127], v[76:79], v[52:55]\n"
        "v_mfma_f32_16x16x32_bf16 v[56:59], a[124:127], v[108:111], v[56:59]\n"
        "v_mfma_f32_16x16x32_bf16 v[60:63], a[124:127], v[140:143], v[60:63]\n"
        "ds_read_b128 a[124:127], v240 offset:31744\n"
        "v_xor_b32 v240, 0x8000, v240\n"
        "v_mfma_f32_16x16x32_bf16 v[172:175], a[156:159], v[80:83], v[172:175]\n"
        "v_mfma_f32_16x16x32_bf16 v[204:207], a[156:159], v[84:87], v[204:207]\n"
        "v_mfma_f32_16x16x32_bf16 a[12:15], a[156:159], v[88:91], a[12:15]\n"
        "s_waitcnt lgkmcnt(2)\n"
        "v_exp_f32 v48, v48\n"
        "v_mfma_f32_16x16x32_bf16 a[44:47], a[156:159], v[92:95], a[44:47]\n"
        "v_exp_f32 v49, v49\n"
        "v_mfma_f32_16x16x32_bf16 v[32:35], a[96:99], v[0:3], v[224:227]\n"
        "ds_read_b128 a[156:159], v241 offset:19456\n"
        "v_cvt_pk_bf16_f32 v18, v48, v49\n"
        "v_mfma_f32_16x16x32_bf16 v[36:39], a[96:99], v[64:67], v[228:231]\n"
        "v_exp_f32 v50, v50\n"
        "v_mfma_f32_16x16x32_bf16 v[40:43], a[96:99], v[96:99], v[232:235]\n"
        "v_exp_f32 v51, v51\n"
        "v_mfma_f32_16x16x32_bf16 v[44:47], a[96:99], v[128:131], v[236:239]\n"
        "s_waitcnt vmcnt(11) lgkmcnt(1)\n"
        "v_cvt_pk_bf16_f32 v19, v50, v51\n"
        "s_barrier\n"
        "v_mfma_f32_16x16x32_bf16 v[176:179], a[128:131], v[80:83], v[176:179]\n"
        "global_load_lds_dwordx4 v254, s[68:69] offset:0 sc1\n"
        "ds_read_b128 a[96:99], v240 offset:0\n"
        "v_mfma_f32_16x16x32_bf16 v[208:211], a[128:131], v[84:87], v[208:211]\n"
        "v_exp_f32 v52, v52\n"
        "v_mfma_f32_16x16x32_bf16 a[16:19], a[128:131], v[88:91], a[16:19]\n"
        "v_exp_f32 v53, v53\n"
        "v_mfma_f32_16x16x32_bf16 a[48:51], a[128:131], v[92:95], a[48:51]\n"
        "ds_read_b128 a[128:131], v241 offset:20480\n"
        "v_cvt_pk_bf16_f32 v22, v52, v53\n"
        "v_mfma_f32_16x16x32_bf16 v[32:35], a[100:103], v[4:7], v[32:35]\n"
        "v_exp_f32 v54, v54\n"
        "v_mfma_f32_16x16x32_bf16 v[36:39], a[100:103], v[68:71], v[36:39]\n"
        "v_exp_f32 v55, v55\n"
        "v_mfma_f32_16x16x32_bf16 v[40:43], a[100:103], v[100:103], v[40:43]\n"
        "global_load_lds_dwordx4 v254, s[68:69] offset:1024 sc1\n"
        "v_cvt_pk_bf16_f32 v23, v54, v55\n"
        "v_mfma_f32_16x16x32_bf16 v[44:47], a[100:103], v[132:135], v[44:47]\n"
        "ds_read_b128 a[100:103], v240 offset:1024\n"
        "v_mfma_f32_16x16x32_bf16 v[180:183], a[132:135], v[80:83], v[180:183]\n"
        "v_exp_f32 v56, v56\n"
        "v_mfma_f32_16x16x32_bf16 v[212:215], a[132:135], v[84:87], v[212:215]\n"
        "v_exp_f32 v57, v57\n"
        "v_mfma_f32_16x16x32_bf16 a[20:23], a[132:135], v[88:91], a[20:23]\n"
        "global_load_lds_dwordx4 v254, s[68:69] offset:2048 sc1\n"
        "v_cvt_pk_bf16_f32 v26, v56, v57\n"
        "v_mfma_f32_16x16x32_bf16 a[52:55], a[132:135], v[92:95], a[52:55]\n"
        "s_waitcnt lgkmcnt(1)\n"
        "v_exp_f32 v58, v58\n"
        "v_mfma_f32_16x16x32_bf16 a[64:67], a[80:83], v[16:19], a[64:67]\n"
        "v_exp_f32 v59, v59\n"
        "v_mfma_f32_16x16x32_bf16 v[32:35], a[104:107], v[8:11], v[32:35]\n"
        "ds_read_b128 a[132:135], v241 offset:21504\n"
        "v_cvt_pk_bf16_f32 v27, v58, v59\n"
        "v_mfma_f32_16x16x32_bf16 v[36:39], a[104:107], v[72:75], v[36:39]\n"
        "global_load_lds_dwordx4 v254, s[68:69] offset:3072 sc1\n"
        "v_mfma_f32_16x16x32_bf16 v[40:43], a[104:107], v[104:107], v[40:43]\n"
        "v_exp_f32 v60, v60\n"
        "v_mfma_f32_16x16x32_bf16 v[44:47], a[104:107], v[136:139], v[44:47]\n"
        "ds_read_b128 a[104:107], v240 offset:2048\n"
        "v_mfma_f32_16x16x32_bf16 v[184:187], a[136:139], v[80:83], v[184:187]\n"
        "v_exp_f32 v61, v61\n"
        "v_mfma_f32_16x16x32_bf16 v[216:219], a[136:139], v[84:87], v[216:219]\n"
        "v_cvt_pk_bf16_f32 v30, v60, v61\n"
        "v_mfma_f32_16x16x32_bf16 a[24:27], a[136:139], v[88:91], a[24:27]\n"
        "v_exp_f32 v62, v62\n"
        "v_mfma_f32_16x16x32_bf16 a[56:59], a[136:139], v[92:95], a[56:59]\n"
        "v_exp_f32 v63, v63\n"
        "v_mfma_f32_16x16x32_bf16 a[68:71], a[80:83], v[20:23], a[68:71]\n"
        "ds_read_b128 a[136:139], v241 offset:22528\n"
        "v_cvt_pk_bf16_f32 v31, v62, v63\n"
        "v_mfma_f32_16x16x32_bf16 v[32:35], a[108:111], v[12:15], v[32:35]\n"
        "v_mfma_f32_16x16x32_bf16 v[36:39], a[108:111], v[76:79], v[36:39]\n"
        "v_mfma_f32_16x16x32_bf16 v[40:43], a[108:111], v[108:111], v[40:43]\n"
        "v_mfma_f32_16x16x32_bf16 v[44:47], a[108:111], v[140:143], v[44:47]\n"
        "ds_read_b128 a[108:111], v240 offset:3072\n"
        "v_mfma_f32_16x16x32_bf16 v[188:191], a[140:143], v[80:83], v[188:191]\n"
        "v_mfma_f32_16x16x32_bf16 v[220:223], a[140:143], v[84:87], v[220:223]\n"
        "v_mfma_f32_16x16x32_bf16 a[28:31], a[140:143], v[88:91], a[28:31]\n"
        "v_mfma_f32_16x16x32_bf16 a[60:63], a[140:143], v[92:95], a[60:63]\n"
        "v_exp_f32 v32, v32\n"
        "v_mfma_f32_16x16x32_bf16 a[72:75], a[80:83], v[24:27], a[72:75]\n"
        "v_exp_f32 v33, v33\n"
        "v_mfma_f32_16x16x32_bf16 v[48:51], a[112:115], v[0:3], v[224:227]\n"
        "ds_read_b128 a[140:143], v241 offset:23552\n"
        "v_cvt_pk_bf16_f32 v80, v32, v33\n"
        "v_mfma_f32_16x16x32_bf16 v[52:55], a[112:115], v[64:67], v[228:231]\n"
        "v_exp_f32 v34, v34\n"
        "v_mfma_f32_16x16x32_bf16 v[56:59], a[112:115], v[96:99], v[232:235]\n"
        "v_exp_f32 v35, v35\n"
        "v_mfma_f32_16x16x32_bf16 v[60:63], a[112:115], v[128:131], v[236:239]\n"
        "ds_read_b128 a[112:115], v240 offset:4096\n"
        "v_cvt_pk_bf16_f32 v81, v34, v35\n"
        "v_mfma_f32_16x16x32_bf16 v[160:163], a[144:147], v[16:19], v[160:163]\n"
        "v_exp_f32 v36, v36\n"
        "v_mfma_f32_16x16x32_bf16 v[192:195], a[144:147], v[20:23], v[192:195]\n"
        "v_exp_f32 v37, v37\n"
        "v_mfma_f32_16x16x32_bf16 a[0:3], a[144:147], v[24:27], a[0:3]\n"
        "v_cvt_pk_bf16_f32 v84, v36, v37\n"
        "v_mfma_f32_16x16x32_bf16 a[32:35], a[144:147], v[28:31], a[32:35]\n"
        "v_exp_f32 v38, v38\n"
        "v_mfma_f32_16x16x32_bf16 a[76:79], a[80:83], v[28:31], a[76:79]\n"
        "v_exp_f32 v39, v39\n"
        "v_mfma_f32_16x16x32_bf16 v[48:51], a[116:119], v[4:7], v[48:51]\n"
        "ds_read_b128 a[144:147], v241 offset:24576\n"
        "v_cvt_pk_bf16_f32 v85, v38, v39\n"
        "v_mfma_f32_16x16x32_bf16 v[52:55], a[116:119], v[68:71], v[52:55]\n"
        "v_exp_f32 v40, v40\n"
        "v_mfma_f32_16x16x32_bf16 v[56:59], a[116:119], v[100:103], v[56:59]\n"
        "v_exp_f32 v41, v41\n"
        "v_mfma_f32_16x16x32_bf16 v[60:63], a[116:119], v[132:135], v[60:63]\n"
        "ds_read_b128 a[116:119], v240 offset:5120\n"
        "v_cvt_pk_bf16_f32 v88, v40, v41\n"
        "v_mfma_f32_16x16x32_bf16 v[164:167], a[148:151], v[16:19], v[164:167]\n"
        "v_exp_f32 v42, v42\n"
        "v_mfma_f32_16x16x32_bf16 v[196:199], a[148:151], v[20:23], v[196:199]\n"
        "v_exp_f32 v43, v43\n"
        "v_mfma_f32_16x16x32_bf16 a[4:7], a[148:151], v[24:27], a[4:7]\n"
        "v_cvt_pk_bf16_f32 v89, v42, v43\n"
        "v_mfma_f32_16x16x32_bf16 a[36:39], a[148:151], v[28:31], a[36:39]\n"
        "ds_read_b128 a[148:151], v241 offset:25600\n"
        "v_mfma_f32_16x16x32_bf16 v[48:51], a[120:123], v[8:11], v[48:51]\n"
        "v_exp_f32 v44, v44\n"
        "v_mfma_f32_16x16x32_bf16 v[52:55], a[120:123], v[72:75], v[52:55]\n"
        "v_exp_f32 v45, v45\n"
        "v_mfma_f32_16x16x32_bf16 v[56:59], a[120:123], v[104:107], v[56:59]\n"
        "v_cvt_pk_bf16_f32 v92, v44, v45\n"
        "v_mfma_f32_16x16x32_bf16 v[60:63], a[120:123], v[136:139], v[60:63]\n"
        "ds_read_b128 a[120:123], v240 offset:6144\n"
        "v_mfma_f32_16x16x32_bf16 v[168:171], a[152:155], v[16:19], v[168:171]\n"
        "v_exp_f32 v46, v46\n"
        "v_mfma_f32_16x16x32_bf16 v[200:203], a[152:155], v[20:23], v[200:203]\n"
        "v_exp_f32 v47, v47\n"
        "v_mfma_f32_16x16x32_bf16 a[8:11], a[152:155], v[24:27], a[8:11]\n"
        "v_cvt_pk_bf16_f32 v93, v46, v47\n"
        "v_mfma_f32_16x16x32_bf16 a[40:43], a[152:155], v[28:31], a[40:43]\n"
        "ds_read_b128 a[152:155], v241 offset:26624\n"
        "v_mfma_f32_16x16x32_bf16 v[48:51], a[124:127], v[12:15], v[48:51]\n"
        "v_mfma_f32_16x16x32_bf16 v[52:55], a[124:127], v[76:79], v[52:55]\n"
        "v_mfma_f32_16x16x32_bf16 v[56:59], a[124:127], v[108:111], v[56:59]\n"
        "v_mfma_f32_16x16x32_bf16 v[60:63], a[124:127], v[140:143], v[60:63]\n"
        "v_mfma_f32_16x16x32_bf16 v[172:175], a[156:159], v[16:19], v[172:175]\n"
        "v_mfma_f32_16x16x32_bf16 v[204:207], a[156:159], v[20:23], v[204:207]\n"
        "v_mfma_f32_16x16x32_bf16 a[12:15], a[156:159], v[24:27], a[12:15]\n"
        "v_mfma_f32_16x16x32_bf16 a[44:47], a[156:159], v[28:31], a[44:47]\n"
        "s_cmp_lg_u32 s91, 0\n"
        "s_cbranch_scc1 LDU_%=\n"
        "; ---- LDRAIN\n"
        "LLD_%=:\n"
        "v_mfma_f32_16x16x32_bf16 a[16:19], a[128:131], v[24:27], a[16:19]\n"
        "s_lshl_b32 s53, s44, 20\n"
        "s_lshl_b32 s96, s43, 15\n"
        "s_add_u32 s53, s53, s96\n"
        "v_mfma_f32_16x16x32_bf16 a[48:51], a[128:131], v[28:31], a[48:51]\n"
        "s_lshl_b32 s96, s40, 10\n"
        "s_add_u32 s53, s53, s96\n"
        "s_waitcnt lgkmcnt(3)\n"
        "v_mfma_f32_16x16x32_bf16 a[20:23], a[132:135], v[24:27], a[20:23]\n"
        "s_add_u32 s53, s53, 0x2001000\n"
        "s_add_u32 s70, s38, s53\n"
        "s_addc_u32 s71, s39, 0\n"
        "v_mfma_f32_16x16x32_bf16 a[52:55], a[132:135], v[28:31], a[52:55]\n"
        "ds_read_b128 a[160:163], v241 offset:24576\n"
        "v_mfma_f32_16x16x32_bf16 a[24:27], a[136:139], v[24:27], a[24:27]\n"
        "ds_read_b128 a[164:167], v241 offset:25600\n"
        "v_mfma_f32_16x16x32_bf16 a[56:59], a[136:139], v[28:31], a[56:59]\n"
        "ds_read_b128 a[168:171], v241 offset:26624\n"
        "v_mfma_f32_16x16x32_bf16 a[28:31], a[140:143], v[24:27], a[28:31]\n"
        "ds_read_b128 a[172:175], v241 offset:27648\n"
        "v_mfma_f32_16x16x32_bf16 a[60:63], a[140:143], v[28:31], a[60:63]\n"
        "ds_read_b128 a[176:179], v241 offset:28672\n"
        "ds_read_b128 a[180:183], v241 offset:29696\n"
        "ds_read_b128 a[184:187], v241 offset:30720\n"
        "ds_read_b128 a[188:191], v241 offset:31744\n"
        "v_exp_f32 v56, v56\n"
        "v_exp_f32 v57, v57\n"
        "v_exp_f32 v58, v58\n"
        "v_cvt_pk_bf16_f32 v90, v56, v57\n"
        "v_exp_f32 v59, v59\n"
        "v_exp_f32 v60, v60\n"
        "v_cvt_pk_bf16_f32 v91, v58, v59\n"
        "s_nop 1\n"
        "v_mfma_f32_16x16x32_bf16 a[72:75], a[80:83], v[88:91], a[72:75]\n"
        "s_waitcnt lgkmcnt(6)\n"
        "v_exp_f32 v61, v61\n"
        "v_mfma_f32_16x16x32_bf16 a[0:3], a[160:163], v[88:91], a[0:3]\n"
        "s_waitcnt lgkmcnt(5)\n"
        "v_cvt_pk_bf16_f32 v94, v60, v61\n"
        "s_sub_u32 s91, s83, s44\n"
        "v_mfma_f32_16x16x32_bf16 a[4:7], a[164:167], v[88:91], a[4:7]\n"
        "s_waitcnt lgkmcnt(4)\n"
        "v_accvgpr_read_b32 v248, a72\n"
        "s_waitcnt lgkmcnt(3)\n"
        "v_mfma_f32_16x16x32_bf16 a[8:11], a[168:171], v[88:91], a[8:11]\n"
        "s_waitcnt lgkmcnt(2)\n"
        "v_rcp_f32 v246, v248\n"
        "v_mfma_f32_16x16x32_bf16 a[12:15], a[172:175], v[88:91], a[12:15]\n"
        "s_waitcnt lgkmcnt(0)\n"
        "v_exp_f32 v62, v62\n"
        "v_mfma_f32_16x16x32_bf16 a[16:19], a[176:179], v[88:91], a[16:19]\n"
        "v_exp_f32 v63, v63\n"
        "v_mfma_f32_16x16x32_bf16 a[20:23], a[180:183], v[88:91], a[20:23]\n"
        "v_cvt_pk_bf16_f32 v95, v62, v63\n"
        "s_nop 1\n"
        "v_mfma_f32_16x16x32_bf16 a[32:35], a[160:163], v[92:95], a[32:35]\n"
        "v_accvgpr_read_b32 v112, a0\n"
        "v_accvgpr_read_b32 v113, a1\n"
        "v_mfma_f32_16x16x32_bf16 a[76:79], a[80:83], v[92:95], a[76:79]\n"
        "v_accvgpr_read_b32 v114, a2\n"
        "v_accvgpr_read_b32 v115, a3\n"
        "v_mfma_f32_16x16x32_bf16 a[36:39], a[164:167], v[92:95], a[36:39]\n"
        "v_accvgpr_read_b32 v116, a4\n"
        "v_accvgpr_read_b32 v117, a5\n"
        "v_mfma_f32_16x16x32_bf16 a[40:43], a[168:171], v[92:95], a[40:43]\n"
        "v_accvgpr_read_b32 v118, a6\n"
        "v_accvgpr_read_b32 v119, a7\n"
        "v_mfma_f32_16x16x32_bf16 a[44:47], a[172:175], v[92:95], a[44:47]\n"
        "v_accvgpr_read_b32 v249, a76\n"
        "v_mul_f32 v112, v112, v246\n"
        "v_mfma_f32_16x16x32_bf16 a[48:51], a[176:179], v[92:95], a[48:51]\n"
        "v_rcp_f32 v247, v249\n"
        "v_mfma_f32_16x16x32_bf16 a[52:55], a[180:183], v[92:95], a[52:55]\n"
        "v_mul_f32 v113, v113, v246\n"
        "v_mul_f32 v114, v114, v246\n"
        "v_mfma_f32_16x16x32_bf16 a[24:27], a[184:187], v[88:91], a[24:27]\n"
        "v_mul_f32 v115, v115, v246\n"
        "v_mul_f32 v116, v116, v246\n"
        "v_mfma_f32_16x16x32_bf16 a[56:59], a[184:187], v[92:95], a[56:59]\n"
        "v_mul_f32 v117, v117, v246\n"
        "v_mul_f32 v118, v118, v246\n"
        "v_mfma_f32_16x16x32_bf16 a[28:31], a[188:191], v[88:91], a[28:31]\n"
        "v_mul_f32 v119, v119, v246\n"
        "v_cvt_pk_bf16_f32 v112, v112, v113\n"
        "v_mfma_f32_16x16x32_bf16 a[60:63], a[188:191], v[92:95], a[60:63]\n"
        "v_cvt_pk_bf16_f32 v113, v114, v115\n"
        "v_cvt_pk_bf16_f32 v114, v116, v117\n"
        "v_cvt_pk_bf16_f32 v115, v118, v119\n"
        "global_store_dwordx4 v254, v[112:115], s[70:71] offset:0 sc1\n"
        "v_accvgpr_read_b32 v120, a32\n"
        "v_accvgpr_read_b32 v121, a33\n"
        "v_accvgpr_read_b32 v122, a34\n"
        "v_accvgpr_read_b32 v123, a35\n"
        "v_accvgpr_read_b32 v124, a36\n"
        "v_accvgpr_read_b32 v125, a37\n"
        "v_accvgpr_read_b32 v126, a38\n"
        "v_accvgpr_read_b32 v127, a39\n"
        "v_mul_f32 v120, v120, v247\n"
        "v_mul_f32 v121, v121, v247\n"
        "v_mul_f32 v122, v122, v247\n"
        "v_mul_f32 v123, v123, v247\n"
        "v_mul_f32 v124, v124, v247\n"
        "v_mul_f32 v125, v125, v247\n"
        "v_mul_f32 v126, v126, v247\n"
        "v_mul_f32 v127, v127, v247\n"
        "v_cvt_pk_bf16_f32 v120, v120, v121\n"
        "v_cvt_pk_bf16_f32 v121, v122, v123\n"
        "v_cvt_pk_bf16_f32 v122, v124, v125\n"
        "v_cvt_pk_bf16_f32 v123, v126, v127\n"
        "global_store_dwordx4 v254, v[120:123], s[70:71] offset:-4096 sc1\n"
        "s_add_u32 s70, s70, 0x2000\n"
        "s_addc_u32 s71, s71, 0\n"
        "v_accvgpr_read_b32 v144, a8\n"
        "v_accvgpr_read_b32 v145, a9\n"
        "v_accvgpr_read_b32 v146, a10\n"
        "v_accvgpr_read_b32 v147, a11\n"
        "v_accvgpr_read_b32 v148, a12\n"
        "v_accvgpr_read_b32 v149, a13\n"
        "v_accvgpr_read_b32 v150, a14\n"
        "v_accvgpr_read_b32 v151, a15\n"
        "v_mul_f32 v144, v144, v246\n"
        "v_mul_f32 v145, v145, v246\n"
        "v_mul_f32 v146, v146, v246\n"
        "v_mul_f32 v147, v147, v246\n"
        "v_mul_f32 v148, v148, v246\n"
        "v_mul_f32 v149, v149, v246\n"
        "v_mul_f32 v150, v150, v246\n"
        "v_mul_f32 v151, v151, v246\n"
        "v_cvt_pk_bf16_f32 v144, v144, v145\n"
        "v_cvt_pk_bf16_f32 v145, v146, v147\n"
        "v_cvt_pk_bf16_f32 v146, v148, v149\n"
        "v_cvt_pk_bf16_f32 v147, v150, v151\n"
        "global_store_dwordx4 v254, v[144:147], s[70:71] offset:0 sc1\n"
        "v_accvgpr_read_b32 v152, a40\n"
        "v_accvgpr_read_b32 v153, a41\n"
        "v_accvgpr_read_b32 v154, a42\n"
        "v_accvgpr_read_b32 v155, a43\n"
        "v_accvgpr_read_b32 v156, a44\n"
        "v_accvgpr_read_b32 v157, a45\n"
        "v_accvgpr_read_b32 v158, a46\n"
        "v_accvgpr_read_b32 v159, a47\n"
        "v_mul_f32 v152, v152, v247\n"
        "v_mul_f32 v153, v153, v247\n"
        "v_mul_f32 v154, v154, v247\n"
        "v_mul_f32 v155, v155, v247\n"
        "v_mul_f32 v156, v156, v247\n"
        "v_mul_f32 v157, v157, v247\n"
        "v_mul_f32 v158, v158, v247\n"
        "v_mul_f32 v159, v159, v247\n"
        "v_cvt_pk_bf16_f32 v152, v152, v153\n"
        "v_cvt_pk_bf16_f32 v153, v154, v155\n"
        "v_cvt_pk_bf16_f32 v154, v156, v157\n"
        "v_cvt_pk_bf16_f32 v155, v158, v159\n"
        "global_store_dwordx4 v254, v[152:155], s[70:71] offset:-4096 sc1\n"
        "s_add_u32 s70, s70, 0x2000\n"
        "s_addc_u32 s71, s71, 0\n"
        "v_accvgpr_read_b32 v112, a16\n"
        "v_accvgpr_read_b32 v113, a17\n"
        "v_accvgpr_read_b32 v114, a18\n"
        "v_accvgpr_read_b32 v115, a19\n"
        "v_accvgpr_read_b32 v116, a20\n"
        "v_accvgpr_read_b32 v117, a21\n"
        "v_accvgpr_read_b32 v118, a22\n"
        "v_accvgpr_read_b32 v119, a23\n"
        "v_mul_f32 v112, v112, v246\n"
        "v_mul_f32 v113, v113, v246\n"
        "v_mul_f32 v114, v114, v246\n"
        "v_mul_f32 v115, v115, v246\n"
        "v_mul_f32 v116, v116, v246\n"
        "v_mul_f32 v117, v117, v246\n"
        "v_mul_f32 v118, v118, v246\n"
        "v_mul_f32 v119, v119, v246\n"
        "v_cvt_pk_bf16_f32 v112, v112, v113\n"
        "v_cvt_pk_bf16_f32 v113, v114, v115\n"
        "v_cvt_pk_bf16_f32 v114, v116, v117\n"
        "v_cvt_pk_bf16_f32 v115, v118, v119\n"
        "global_store_dwordx4 v254, v[112:115], s[70:71] offset:0 sc1\n"
        "v_accvgpr_read_b32 v120, a48\n"
        "v_accvgpr_read_b32 v121, a49\n"
        "v_accvgpr_read_b32 v122, a50\n"
        "v_accvgpr_read_b32 v123, a51\n"
        "v_accvgpr_read_b32 v124, a52\n"
        "v_accvgpr_read_b32 v125, a53\n"
        "v_accvgpr_read_b32 v126, a54\n"
        "v_accvgpr_read_b32 v127, a55\n"
        "v_mul_f32 v120, v120, v247\n"
        "v_mul_f32 v121, v121, v247\n"
        "v_mul_f32 v122, v122, v247\n"
        "v_mul_f32 v123, v123, v247\n"
        "v_mul_f32 v124, v124, v247\n"
        "v_mul_f32 v125, v125, v247\n"
        "v_mul_f32 v126, v126, v247\n"
        "v_mul_f32 v127, v127, v247\n"
        "v_cvt_pk_bf16_f32 v120, v120, v121\n"
        "v_cvt_pk_bf16_f32 v121, v122, v123\n"
        "v_cvt_pk_bf16_f32 v122, v124, v125\n"
        "v_cvt_pk_bf16_f32 v123, v126, v127\n"
        "global_store_dwordx4 v254, v[120:123], s[70:71] offset:-4096 sc1\n"
        "s_add_u32 s70, s70, 0x2000\n"
        "s_addc_u32 s71, s71, 0\n"
        "v_accvgpr_read_b32 v144, a24\n"
        "v_accvgpr_read_b32 v145, a25\n"
        "v_accvgpr_read_b32 v146, a26\n"
        "v_accvgpr_read_b32 v147, a27\n"
        "v_accvgpr_read_b32 v148, a28\n"
        "v_accvgpr_read_b32 v149, a29\n"
        "v_accvgpr_read_b32 v150, a30\n"
        "v_accvgpr_read_b32 v151, a31\n"
        "v_mul_f32 v144, v144, v246\n"
        "v_mul_f32 v145, v145, v246\n"
        "v_mul_f32 v146, v146, v246\n"
        "v_mul_f32 v147, v147, v246\n"
        "v_mul_f32 v148, v148, v246\n"
        "v_mul_f32 v149, v149, v246\n"
        "v_mul_f32 v150, v150, v246\n"
        "v_mul_f32 v151, v151, v246\n"
        "v_cvt_pk_bf16_f32 v144, v144, v145\n"
        "v_cvt_pk_bf16_f32 v145, v146, v147\n"
        "v_cvt_pk_bf16_f32 v146, v148, v149\n"
        "v_cvt_pk_bf16_f32 v147, v150, v151\n"
        "global_store_dwordx4 v254, v[144:147], s[70:71] offset:0 sc1\n"
        "v_accvgpr_read_b32 v152, a56\n"
        "v_accvgpr_read_b32 v153, a57\n"
        "v_accvgpr_read_b32 v154, a58\n"
        "v_accvgpr_read_b32 v155, a59\n"
        "v_accvgpr_read_b32 v156, a60\n"
        "v_accvgpr_read_b32 v157, a61\n"
        "v_accvgpr_read_b32 v158, a62\n"
        "v_accvgpr_read_b32 v159, a63\n"
        "v_mul_f32 v152, v152, v247\n"
        "v_mul_f32 v153, v153, v247\n"
        "v_mul_f32 v154, v154, v247\n"
        "v_mul_f32 v155, v155, v247\n"
        "v_mul_f32 v156, v156, v247\n"
        "v_mul_f32 v157, v157, v247\n"
        "v_mul_f32 v158, v158, v247\n"
        "v_mul_f32 v159, v159, v247\n"
        "v_cvt_pk_bf16_f32 v152, v152, v153\n"
        "v_cvt_pk_bf16_f32 v153, v154, v155\n"
        "v_cvt_pk_bf16_f32 v154, v156, v157\n"
        "v_cvt_pk_bf16_f32 v155, v158, v159\n"
        "global_store_dwordx4 v254, v[152:155], s[70:71] offset:-4096 sc1\n"
        "v_add_f32 v247, v248, v249\n"
        "v_cmp_nge_f32 vcc, 0x4e6e6b28, v247\n"
        "s_nop 0\n"
        "s_cbranch_vccnz LRL_%=\n"
        "LSL_%=:\n"
        "s_cmp_eq_u32 s91, 0\n"
        "s_cbranch_scc1 LTL_%=\n"
        "; ---- SINGLE\n"
        ".p2align 6\n"
        "LSI_%=:\n"
        "v_mfma_f32_16x16x32_bf16 v[32:35], a[96:99], v[0:3], v[224:227]\n"
        "s_add_u32 m0, s81, 0x0\n"
        "ds_read_b128 a[124:127], v240 offset:7168\n"
        "v_mfma_f32_16x16x32_bf16 v[36:39], a[96:99], v[64:67], v[228:231]\n"
        "global_load_lds_dwordx4 v254, s[74:75] offset:0 sc1\n"
        "ds_read_b128 a[156:159], v241 offset:27648\n"
        "v_mfma_f32_16x16x32_bf16 v[176:179], a[128:131], v[16:19], v[176:179]\n"
        "s_waitcnt lgkmcnt(4)\n"
        "v_exp_f32 v48, v48\n"
        "v_mfma_f32_16x16x32_bf16 v[208:211], a[128:131], v[20:23], v[208:211]\n"
        "v_exp_f32 v49, v49\n"
        "v_mfma_f32_16x16x32_bf16 v[32:35], a[100:103], v[4:7], v[32:35]\n"
        "ds_read_b128 a[96:99], v240 offset:8192\n"
        "s_xor_b32 s79, s79, 0x8000\n"
        "v_cvt_pk_bf16_f32 v82, v48, v49\n"
        "v_mfma_f32_16x16x32_bf16 v[36:39], a[100:103], v[68:71], v[36:39]\n"
        "global_load_lds_dwordx4 v254, s[74:75] offset:1024 sc1\n"
        "ds_read_b128 a[128:131], v241 offset:28672\n"
        "s_add_u32 s82, s82, 1\n"
        "v_mfma_f32_16x16x32_bf16 v[180:183], a[132:135], v[16:19], v[180:183]\n"
        "s_xor_b32 s78, s78, 0x8000\n"
        "v_exp_f32 v50, v50\n"
        "v_mfma_f32_16x16x32_bf16 v[212:215], a[132:135], v[20:23], v[212:215]\n"
        "s_add_u32 s81, s78, 4096\n"
        "v_exp_f32 v51, v51\n"
        "v_mfma_f32_16x16x32_bf16 v[32:35], a[104:107], v[8:11], v[32:35]\n"
        "ds_read_b128 a[100:103], v240 offset:9216\n"
        "s_sub_u32 s91, s91, 1\n"
        "v_cvt_pk_bf16_f32 v83, v50, v51\n"
        "v_mfma_f32_16x16x32_bf16 v[36:39], a[104:107], v[72:75], v[36:39]\n"
        "global_load_lds_dwordx4 v254, s[74:75] offset:2048 sc1\n"
        "ds_read_b128 a[132:135], v241 offset:29696\n"
        "v_mfma_f32_16x16x32_bf16 v[184:187], a[136:139], v[16:19], v[184:187]\n"
        "v_exp_f32 v52, v52\n"
        "v_mfma_f32_16x16x32_bf16 v[216:219], a[136:139], v[20:23], v[216:219]\n"
        "v_exp_f32 v53, v53\n"
        "v_mfma_f32_16x16x32_bf16 a[64:67], a[80:83], v[80:83], a[64:67]\n"
        "ds_read_b128 a[104:107], v240 offset:10240\n"
        "v_cvt_pk_bf16_f32 v86, v52, v53\n"
        "v_mfma_f32_16x16x32_bf16 v[32:35], a[108:111], v[12:15], v[32:35]\n"
        "global_load_lds_dwordx4 v254, s[74:75] offset:3072 sc1\n"
        "ds_read_b128 a[136:139], v241 offset:30720\n"
        "s_add_u32 m0, s79, 0x0\n"
        "v_mfma_f32_16x16x32_bf16 v[36:39], a[108:111], v[76:79], v[36:39]\n"
        "s_waitcnt lgkmcnt(4)\n"
        "v_exp_f32 v54, v54\n"
        "v_mfma_f32_16x16x32_bf16 v[188:191], a[140:143], v[16:19], v[188:191]\n"
        "v_exp_f32 v55, v55\n"
        "v_mfma_f32_16x16x32_bf16 v[220:223], a[140:143], v[20:23], v[220:223]\n"
        "ds_read_b128 a[140:143], v241 offset:31744\n"
        "v_cvt_pk_bf16_f32 v87, v54, v55\n"
        "v_mfma_f32_16x16x32_bf16 v[48:51], a[112:115], v[0:3], v[224:227]\n"
        "ds_read_b128 a[108:111], v240 offset:11264\n"
        "v_xor_b32 v241, 0x8000, v241\n"
        "v_mfma_f32_16x16x32_bf16 v[52:55], a[112:115], v[64:67], v[228:231]\n"
        "v_exp_f32 v32, v32\n"
        "v_mfma_f32_16x16x32_bf16 v[160:163], a[144:147], v[80:83], v[160:163]\n"
        "v_exp_f32 v33, v33\n"
        "v_mfma_f32_16x16x32_bf16 v[192:195], a[144:147], v[84:87], v[192:195]\n"
        "ds_read_b128 a[112:115], v240 offset:12288\n"
        "v_cvt_pk_bf16_f32 v16, v32, v33\n"
        "v_mfma_f32_16x16x32_bf16 a[68:71], a[80:83], v[84:87], a[68:71]\n"
        "v_exp_f32 v34, v34\n"
        "v_mfma_f32_16x16x32_bf16 v[48:51], a[116:119], v[4:7], v[48:51]\n"
        "v_exp_f32 v35, v35\n"
        "v_mfma_f32_16x16x32_bf16 v[52:55], a[116:119], v[68:71], v[52:55]\n"
        "s_add_u32 s76, s66, s90\n"
        "s_addc_u32 s77, s67, 0\n"
        "v_cvt_pk_bf16_f32 v17, v34, v35\n"
        "v_mfma_f32_16x16x32_bf16 v[164:167], a[148:151], v[80:83], v[164:167]\n"
        "s_waitcnt vmcnt(10) lgkmcnt(2)\n"
        "s_barrier\n"
        "v_mfma_f32_16x16x32_bf16 v[196:199], a[148:151], v[84:87], v[196:199]\n"
        "global_load_lds_dwordx4 v254, s[76:77] offset:0 sc1\n"
        "ds_read_b128 a[144:147], v241 offset:0\n"
        "v_mfma_f32_16x16x32_bf16 v[48:51], a[120:123], v[8:11], v[48:51]\n"
        "ds_read_b128 a[116:119], v240 offset:13312\n"
        "s_cmp_ge_u32 s82, s44\n"
        "s_addc_u32 s90, s82, 0\n"
        "v_mfma_f32_16x16x32_bf16 v[52:55], a[120:123], v[72:75], v[52:55]\n"
        "s_min_u32 s90, s90, 15\n"
        "v_exp_f32 v36, v36\n"
        "v_mfma_f32_16x16x32_bf16 v[168:171], a[152:155], v[80:83], v[168:171]\n"
        "s_lshl_b32 s90, s90, 15\n"
        "v_exp_f32 v37, v37\n"
        "v_mfma_f32_16x16x32_bf16 v[200:203], a[152:155], v[84:87], v[200:203]\n"
        "ds_read_b128 a[148:151], v241 offset:1024\n"
        "v_cvt_pk_bf16_f32 v20, v36, v37\n"
        "v_mfma_f32_16x16x32_bf16 v[48:51], a[124:127], v[12:15], v[48:51]\n"
        "global_load_lds_dwordx4 v254, s[76:77] offset:1024 sc1\n"
        "ds_read_b128 a[120:123], v240 offset:14336\n"
        "v_mfma_f32_16x16x32_bf16 v[52:55], a[124:127], v[76:79], v[52:55]\n"
        "v_exp_f32 v38, v38\n"
        "v_mfma_f32_16x16x32_bf16 v[172:175], a[156:159], v[80:83], v[172:175]\n"
        "v_exp_f32 v39, v39\n"
        "v_mfma_f32_16x16x32_bf16 v[204:207], a[156:159], v[84:87], v[204:207]\n"
        "ds_read_b128 a[152:155], v241 offset:2048\n"
        "v_cvt_pk_bf16_f32 v21, v38, v39\n"
        "v_mfma_f32_16x16x32_bf16 v[32:35], a[96:99], v[0:3], v[224:227]\n"
        "global_load_lds_dwordx4 v254, s[76:77] offset:2048 sc1\n"
        "ds_read_b128 a[124:127], v240 offset:15360\n"
        "v_mfma_f32_16x16x32_bf16 v[36:39], a[96:99], v[64:67], v[228:231]\n"
        "v_exp_f32 v48, v48\n"
        "v_mfma_f32_16x16x32_bf16 v[176:179], a[128:131], v[80:83], v[176:179]\n"
        "v_exp_f32 v49, v49\n"
        "v_mfma_f32_16x16x32_bf16 v[208:211], a[128:131], v[84:87], v[208:211]\n"
        "ds_read_b128 a[156:159], v241 offset:3072\n"
        "v_cvt_pk_bf16_f32 v18, v48, v49\n"
        "v_mfma_f32_16x16x32_bf16 v[32:35], a[100:103], v[4:7], v[32:35]\n"
        "global_load_lds_dwordx4 v254, s[76:77] offset:3072 sc1\n"
        "ds_read_b128 a[96:99], v240 offset:16384\n"
        "s_add_u32 m0, s79, 0x1000\n"
        "v_mfma_f32_16x16x32_bf16 v[36:39], a[100:103], v[68:71], v[36:39]\n"
        "v_exp_f32 v50, v50\n"
        "v_mfma_f32_16x16x32_bf16 v[180:183], a[132:135], v[80:83], v[180:183]\n"
        "v_exp_f32 v51, v51\n"
        "v_mfma_f32_16x16x32_bf16 v[212:215], a[132:135], v[84:87], v[212:215]\n"
        "ds_read_b128 a[128:131], v241 offset:4096\n"
        "v_cvt_pk_bf16_f32 v19, v50, v51\n"
        "v_mfma_f32_16x16x32_bf16 v[32:35], a[104:107], v[8:11], v[32:35]\n"
        "s_add_u32 s76, s76, 0x1000\n"
        "s_addc_u32 s77, s77, 0\n"
        "s_waitcnt lgkmcnt(3)\n"
        "v_mfma_f32_16x16x32_bf16 v[36:39], a[104:107], v[72:75], v[36:39]\n"
        "global_load_lds_dwordx4 v254, s[76:77] offset:0 sc1\n"
        "ds_read_b128 a[100:103], v240 offset:17408\n"
        "v_mfma_f32_16x16x32_bf16 v[184:187], a[136:139], v[80:83], v[184:187]\n"
        "v_exp_f32 v52, v52\n"
        "v_mfma_f32_16x16x32_bf16 v[216:219], a[136:139], v[84:87], v[216:219]\n"
        "v_exp_f32 v53, v53\n"
        "v_mfma_f32_16x16x32_bf16 a[64:67], a[80:83], v[16:19], a[64:67]\n"
        "ds_read_b128 a[132:135], v241 offset:5120\n"
        "v_cvt_pk_bf16_f32 v22, v52, v53\n"
        "v_mfma_f32_16x16x32_bf16 v[32:35], a[108:111], v[12:15], v[32:35]\n"
        "global_load_lds_dwordx4 v254, s[76:77] offset:1024 sc1\n"
        "ds_read_b128 a[104:107], v240 offset:18432\n"
        "v_mfma_f32_16x16x32_bf16 v[36:39], a[108:111], v[76:79], v[36:39]\n"
        "v_exp_f32 v54, v54\n"
        "v_mfma_f32_16x16x32_bf16 v[188:191], a[140:143], v[80:83], v[188:191]\n"
        "s_waitcnt lgkmcnt(3)\n"
        "v_exp_f32 v55, v55\n"
        "v_mfma_f32_16x16x32_bf16 v[220:223], a[140:143], v[84:87], v[220:223]\n"
        "ds_read_b128 a[136:139], v241 offset:6144\n"
        "v_cvt_pk_bf16_f32 v23, v54, v55\n"
        "v_mfma_f32_16x16x32_bf16 v[48:51], a[112:115], v[0:3], v[224:227]\n"
        "global_load_lds_dwordx4 v254, s[76:77] offset:2048 sc1\n"
        "ds_read_b128 a[108:111], v240 offset:19456\n"
        "v_mfma_f32_16x16x32_bf16 v[52:55], a[112:115], v[64:67], v[228:231]\n"
        "v_exp_f32 v32, v32\n"
        "v_mfma_f32_16x16x32_bf16 v[160:163], a[144:147], v[16:19], v[160:163]\n"
        "v_exp_f32 v33, v33\n"
        "v_mfma_f32_16x16x32_bf16 v[192:195], a[144:147], v[20:23], v[192:195]\n"
        "ds_read_b128 a[140:143], v241 offset:7168\n"
        "v_cvt_pk_bf16_f32 v80, v32, v33\n"
        "v_mfma_f32_16x16x32_bf16 a[68:71], a[80:83], v[20:23], a[68:71]\n"
        "global_load_lds_dwordx4 v254, s[76:77] offset:3072 sc1\n"
        "ds_read_b128 a[112:115], v240 offset:20480\n"
        "v_mfma_f32_16x16x32_bf16 v[48:51], a[116:119], v[4:7], v[48:51]\n"
        "v_exp_f32 v34, v34\n"
        "v_mfma_f32_16x16x32_bf16 v[52:55], a[116:119], v[68:71], v[52:55]\n"
        "v_exp_f32 v35, v35\n"
        "v_mfma_f32_16x16x32_bf16 v[164:167], a[148:151], v[16:19], v[164:167]\n"
        "ds_read_b128 a[144:147], v241 offset:8192\n"
        "v_cvt_pk_bf16_f32 v81, v34, v35\n"
        "v_mfma_f32_16x16x32_bf16 v[196:199], a[148:151], v[20:23], v[196:199]\n"
        "ds_read_b128 a[116:119], v240 offset:21504\n"
        "s_add_u32 s68, s64, s90\n"
        "s_addc_u32 s69, s65, 0\n"
        "v_mfma_f32_16x16x32_bf16 v[48:51], a[120:123], v[8:11], v[48:51]\n"
        "v_exp_f32 v36, v36\n"
        "v_mfma_f32_16x16x32_bf16 v[52:55], a[120:123], v[72:75], v[52:55]\n"
        "v_exp_f32 v37, v37\n"
        "v_mfma_f32_16x16x32_bf16 v[168:171], a[152:155], v[16:19], v[168:171]\n"
        "ds_read_b128 a[148:151], v241 offset:9216\n"
        "v_cvt_pk_bf16_f32 v84, v36, v37\n"
        "v_mfma_f32_16x16x32_bf16 v[200:203], a[152:155], v[20:23], v[200:203]\n"
        "ds_read_b128 a[120:123], v240 offset:22528\n"
        "s_add_u32 s74, s68, 0x1000\n"
        "s_addc_u32 s75, s69, 0\n"
        "v_mfma_f32_16x16x32_bf16 v[48:51], a[124:127], v[12:15], v[48:51]\n"
        "v_exp_f32 v38, v38\n"
        "v_mfma_f32_16x16x32_bf16 v[52:55], a[124:127], v[76:79], v[52:55]\n"
        "s_waitcnt lgkmcnt(4)\n"
        "v_exp_f32 v39, v39\n"
        "v_mfma_f32_16x16x32_bf16 v[172:175], a[156:159], v[16:19], v[172:175]\n"
        "ds_read_b128 a[152:155], v241 offset:10240\n"
        "v_cvt_pk_bf16_f32 v85, v38, v39\n"
        "v_mfma_f32_16x16x32_bf16 v[204:207], a[156:159], v[20:23], v[204:207]\n"
        "global_load_dword v253, v244, s[46:47] offset:0\n"
        "ds_read_b128 a[124:127], v240 offset:23552\n"
        "v_mfma_f32_16x16x32_bf16 v[32:35], a[96:99], v[0:3], v[224:227]\n"
        "v_exp_f32 v48, v48\n"
        "v_mfma_f32_16x16x32_bf16 v[36:39], a[96:99], v[64:67], v[228:231]\n"
        "v_exp_f32 v49, v49\n"
        "v_mfma_f32_16x16x32_bf16 v[176:179], a[128:131], v[16:19], v[176:179]\n"
        "ds_read_b128 a[156:159], v241 offset:11264\n"
        "v_cvt_pk_bf16_f32 v82, v48, v49\n"
        "v_mfma_f32_16x16x32_bf16 v[208:211], a[128:131], v[20:23], v[208:211]\n"
        "ds_read_b128 a[96:99], v240 offset:24576\n"
        "v_mfma_f32_16x16x32_bf16 v[32:35], a[100:103], v[4:7], v[32:35]\n"
        "v_exp_f32 v50, v50\n"
        "v_mfma_f32_16x16x32_bf16 v[36:39], a[100:103], v[68:71], v[36:39]\n"
        "v_exp_f32 v51, v51\n"
        "v_mfma_f32_16x16x32_bf16 v[180:183], a[132:135], v[16:19], v[180:183]\n"
        "ds_read_b128 a[128:131], v241 offset:12288\n"
        "v_cvt_pk_bf16_f32 v83, v50, v51\n"
        "v_mfma_f32_16x16x32_bf16 v[212:215], a[132:135], v[20:23], v[212:215]\n"
        "ds_read_b128 a[100:103], v240 offset:25600\n"
        "v_mfma_f32_16x16x32_bf16 v[32:35], a[104:107], v[8:11], v[32:35]\n"
        "v_exp_f32 v52, v52\n"
        "v_mfma_f32_16x16x32_bf16 v[36:39], a[104:107], v[72:75], v[36:39]\n"
        "v_exp_f32 v53, v53\n"
        "v_mfma_f32_16x16x32_bf16 v[184:187], a[136:139], v[16:19], v[184:187]\n"
        "ds_read_b128 a[132:135], v241 offset:13312\n"
        "v_cvt_pk_bf16_f32 v86, v52, v53\n"
        "v_mfma_f32_16x16x32_bf16 v[216:219], a[136:139], v[20:23], v[216:219]\n"
        "ds_read_b128 a[104:107], v240 offset:26624\n"
        "v_mfma_f32_16x16x32_bf16 a[64:67], a[80:83], v[80:83], a[64:67]\n"
        "s_waitcnt lgkmcnt(4)\n"
        "v_exp_f32 v54, v54\n"
        "v_mfma_f32_16x16x32_bf16 v[32:35], a[108:111], v[12:15], v[32:35]\n"
        "v_exp_f32 v55, v55\n"
        "v_mfma_f32_16x16x32_bf16 v[36:39], a[108:111], v[76:79], v[36:39]\n"
        "ds_read_b128 a[136:139], v241 offset:14336\n"
        "v_cvt_pk_bf16_f32 v87, v54, v55\n"
        "v_mfma_f32_16x16x32_bf16 v[188:191], a[140:143], v[16:19], v[188:191]\n"
        "ds_read_b128 a[108:111], v240 offset:27648\n"
        "v_mfma_f32_16x16x32_bf16 v[220:223], a[140:143], v[20:23], v[220:223]\n"
        "global_load_dword v253, v245, s[26:27] offset:0\n"
        "s_sub_u32 s26, s26, 262144\n"
        "s_subb_u32 s27, s27, 0\n"
        "v_mfma_f32_16x16x32_bf16 v[48:51], a[112:115], v[0:3], v[224:227]\n"
        "v_exp_f32 v32, v32\n"
        "v_mfma_f32_16x16x32_bf16 v[52:55], a[112:115], v[64:67], v[228:231]\n"
        "v_exp_f32 v33, v33\n"
        "v_mfma_f32_16x16x32_bf16 v[160:163], a[144:147], v[80:83], v[160:163]\n"
        "ds_read_b128 a[140:143], v241 offset:15360\n"
        "v_cvt_pk_bf16_f32 v16, v32, v33\n"
        "v_mfma_f32_16x16x32_bf16 v[192:195], a[144:147], v[84:87], v[192:195]\n"
        "ds_read_b128 a[112:115], v240 offset:28672\n"
        "v_mfma_f32_16x16x32_bf16 a[68:71], a[80:83], v[84:87], a[68:71]\n"
        "v_exp_f32 v34, v34\n"
        "v_mfma_f32_16x16x32_bf16 v[48:51], a[116:119], v[4:7], v[48:51]\n"
        "v_exp_f32 v35, v35\n"
        "v_mfma_f32_16x16x32_bf16 v[52:55], a[116:119], v[68:71], v[52:55]\n"
        "ds_read_b128 a[144:147], v241 offset:16384\n"
        "v_cvt_pk_bf16_f32 v17, v34, v35\n"
        "v_mfma_f32_16x16x32_bf16 v[164:167], a[148:151], v[80:83], v[164:167]\n"
        "ds_read_b128 a[116:119], v240 offset:29696\n"
        "v_mfma_f32_16x16x32_bf16 v[196:199], a[148:151], v[84:87], v[196:199]\n"
        "v_exp_f32 v36, v36\n"
        "v_mfma_f32_16x16x32_bf16 v[48:51], a[120:123], v[8:11], v[48:51]\n"
        "v_exp_f32 v37, v37\n"
        "v_mfma_f32_16x16x32_bf16 v[52:55], a[120:123], v[72:75], v[52:55]\n"
        "ds_read_b128 a[148:151], v241 offset:17408\n"
        "v_cvt_pk_bf16_f32 v20, v36, v37\n"
        "v_mfma_f32_16x16x32_bf16 v[168:171], a[152:155], v[80:83], v[168:171]\n"
        "ds_read_b128 a[120:123], v240 offset:30720\n"
        "v_mfma_f32_16x16x32_bf16 v[200:203], a[152:155], v[84:87], v[200:203]\n"
        "s_waitcnt lgkmcnt(4)\n"
        "v_exp_f32 v38, v38\n"
        "v_mfma_f32_16x16x32_bf16 v[48:51], a[124:127], v[12:15], v[48:51]\n"
        "v_exp_f32 v39, v39\n"
        "v_mfma_f32_16x16x32_bf16 v[52:55], a[124:127], v[76:79], v[52:55]\n"
        "ds_read_b128 a[124:127], v240 offset:31744\n"
        "v_cvt_pk_bf16_f32 v21, v38, v39\n"
        "v_mfma_f32_16x16x32_bf16 v[172:175], a[156:159], v[80:83], v[172:175]\n"
        "ds_read_b128 a[152:155], v241 offset:18432\n"
        "v_xor_b32 v240, 0x8000, v240\n"
        "v_mfma_f32_16x16x32_bf16 v[204:207], a[156:159], v[84:87], v[204:207]\n"
        "v_exp_f32 v48, v48\n"
        "v_mfma_f32_16x16x32_bf16 v[32:35], a[96:99], v[0:3], v[224:227]\n"
        "v_exp_f32 v49, v49\n"
        "v_mfma_f32_16x16x32_bf16 v[36:39], a[96:99], v[64:67], v[228:231]\n"
        "ds_read_b128 a[156:159], v241 offset:19456\n"
        "v_cvt_pk_bf16_f32 v18, v48, v49\n"
        "v_mfma_f32_16x16x32_bf16 v[176:179], a[128:131], v[80:83], v[176:179]\n"
        "s_cmp_eq_u32 s82, s83\n"
        "s_cselect_b64 s[68:69], s[92:93], s[68:69]\n"
        "s_cselect_b32 s80, s50, s78\n"
        "v_mfma_f32_16x16x32_bf16 v[208:211], a[128:131], v[84:87], v[208:211]\n"
        "s_cmp_eq_u32 s82, s83\n"
        "s_cselect_b64 s[74:75], s[94:95], s[74:75]\n"
        "s_cselect_b32 s81, s51, s81\n"
        "v_mfma_f32_16x16x32_bf16 v[32:35], a[100:103], v[4:7], v[32:35]\n"
        "s_add_u32 m0, s80, 0x0\n"
        "v_exp_f32 v50, v50\n"
        "v_mfma_f32_16x16x32_bf16 v[36:39], a[100:103], v[68:71], v[36:39]\n"
        "s_waitcnt vmcnt(10) lgkmcnt(2)\n"
        "s_barrier\n"
        "v_mfma_f32_16x16x32_bf16 v[180:183], a[132:135], v[80:83], v[180:183]\n"
        "global_load_lds_dwordx4 v254, s[68:69] offset:0 sc1\n"
        "ds_read_b128 a[96:99], v240 offset:0\n"
        "v_mfma_f32_16x16x32_bf16 v[212:215], a[132:135], v[84:87], v[212:215]\n"
        "v_exp_f32 v51, v51\n"
        "v_mfma_f32_16x16x32_bf16 v[32:35], a[104:107], v[8:11], v[32:35]\n"
        "ds_read_b128 a[128:131], v241 offset:20480\n"
        "v_cvt_pk_bf16_f32 v19, v50, v51\n"
        "v_mfma_f32_16x16x32_bf16 v[36:39], a[104:107], v[72:75], v[36:39]\n"
        "ds_read_b128 a[100:103], v240 offset:1024\n"
        "v_mfma_f32_16x16x32_bf16 v[184:187], a[136:139], v[80:83], v[184:187]\n"
        "v_exp_f32 v52, v52\n"
        "v_mfma_f32_16x16x32_bf16 v[216:219], a[136:139], v[84:87], v[216:219]\n"
        "v_exp_f32 v53, v53\n"
        "v_mfma_f32_16x16x32_bf16 a[64:67], a[80:83], v[16:19], a[64:67]\n"
        "global_load_lds_dwordx4 v254, s[68:69] offset:1024 sc1\n"
        "v_cvt_pk_bf16_f32 v22, v52, v53\n"
        "v_mfma_f32_16x16x32_bf16 v[32:35], a[108:111], v[12:15], v[32:35]\n"
        "ds_read_b128 a[132:135], v241 offset:21504\n"
        "v_mfma_f32_16x16x32_bf16 v[36:39], a[108:111], v[76:79], v[36:39]\n"
        "ds_read_b128 a[104:107], v240 offset:2048\n"
        "s_waitcnt lgkmcnt(4)\n"
        "v_mfma_f32_16x16x32_bf16 v[188:191], a[140:143], v[80:83], v[188:191]\n"
        "v_exp_f32 v54, v54\n"
        "v_mfma_f32_16x16x32_bf16 v[220:223], a[140:143], v[84:87], v[220:223]\n"
        "global_load_lds_dwordx4 v254, s[68:69] offset:2048 sc1\n"
        "ds_read_b128 a[136:139], v241 offset:22528\n"
        "v_mfma_f32_16x16x32_bf16 v[48:51], a[112:115], v[0:3], v[224:227]\n"
        "v_exp_f32 v55, v55\n"
        "v_mfma_f32_16x16x32_bf16 v[160:163], a[144:147], v[16:19], v[160:163]\n"
        "ds_read_b128 a[108:111], v240 offset:3072\n"
        "v_cvt_pk_bf16_f32 v23, v54, v55\n"
        "v_mfma_f32_16x16x32_bf16 v[52:55], a[112:115], v[64:67], v[228:231]\n"
        "v_exp_f32 v32, v32\n"
        "v_mfma_f32_16x16x32_bf16 v[192:195], a[144:147], v[20:23], v[192:195]\n"
        "global_load_lds_dwordx4 v254, s[68:69] offset:3072 sc1\n"
        "ds_read_b128 a[140:143], v241 offset:23552\n"
        "v_mfma_f32_16x16x32_bf16 a[68:71], a[80:83], v[20:23], a[68:71]\n"
        "s_waitcnt lgkmcnt(6)\n"
        "v_exp_f32 v33, v33\n"
        "v_mfma_f32_16x16x32_bf16 v[48:51], a[116:119], v[4:7], v[48:51]\n"
        "ds_read_b128 a[112:115], v240 offset:4096\n"
        "v_cvt_pk_bf16_f32 v80, v32, v33\n"
        "v_mfma_f32_16x16x32_bf16 v[52:55], a[116:119], v[68:71], v[52:55]\n"
        "v_exp_f32 v34, v34\n"
        "v_mfma_f32_16x16x32_bf16 v[164:167], a[148:151], v[16:19], v[164:167]\n"
        "v_exp_f32 v35, v35\n"
        "v_mfma_f32_16x16x32_bf16 v[196:199], a[148:151], v[20:23], v[196:199]\n"
        "ds_read_b128 a[144:147], v241 offset:24576\n"
        "v_cvt_pk_bf16_f32 v81, v34, v35\n"
        "v_mfma_f32_16x16x32_bf16 v[48:51], a[120:123], v[8:11], v[48:51]\n"
        "ds_read_b128 a[116:119], v240 offset:5120\n"
        "v_mfma_f32_16x16x32_bf16 v[52:55], a[120:123], v[72:75], v[52:55]\n"
        "v_exp_f32 v36, v36\n"
        "v_mfma_f32_16x16x32_bf16 v[168:171], a[152:155], v[16:19], v[168:171]\n"
        "v_exp_f32 v37, v37\n"
        "v_mfma_f32_16x16x32_bf16 v[200:203], a[152:155], v[20:23], v[200:203]\n"
        "ds_read_b128 a[148:151], v241 offset:25600\n"
        "v_cvt_pk_bf16_f32 v84, v36, v37\n"
        "v_mfma_f32_16x16x32_bf16 v[48:51], a[124:127], v[12:15], v[48:51]\n"
        "ds_read_b128 a[120:123], v240 offset:6144\n"
        "v_mfma_f32_16x16x32_bf16 v[52:55], a[124:127], v[76:79], v[52:55]\n"
        "v_exp_f32 v38, v38\n"
        "v_mfma_f32_16x16x32_bf16 v[172:175], a[156:159], v[16:19], v[172:175]\n"
        "v_exp_f32 v39, v39\n"
        "v_mfma_f32_16x16x32_bf16 v[204:207], a[156:159], v[20:23], v[204:207]\n"
        "v_cvt_pk_bf16_f32 v85, v38, v39\n"
        "ds_read_b128 a[152:155], v241 offset:26624\n"
        "s_cmp_lg_u32 s91, 0\n"
        "s_cbranch_scc1 LSI_%=\n"
        "; ---- BACK\n"
        "LTL_%=:\n"
        "s_mul_i32 s14, s40, 4224\n"
        "v_bfe_u32 v232, v254, 4, 4\n"
        "s_add_u32 s15, s51, 4098\n"
        "s_add_u32 s14, s14, 0x20240\n"
        "v_and_b32 v224, 1, v232\n"
        "s_sub_u32 s15, s15, s14\n"
        "v_mul_lo_u32 v224, v224, s15\n"
        "v_add_u32 v224, s14, v224\n"
        "v_lshrrev_b32 v233, 8, v254\n"
        "s_add_u32 s16, s51, 4096\n"
        "v_lshlrev_b32 v228, 5, v232\n"
        "v_lshl_add_u32 v224, v232, 1, v224\n"
        "s_add_u32 s14, s51, 480\n"
        "v_mov_b32 v236, 0\n"
        "v_mov_b32 v237, 0\n"
        "v_mov_b32 v238, 0\n"
        "v_mov_b32 v239, 0\n"
        "v_add_u32 v229, s16, v254\n"
        "v_lshl_add_u32 v224, v233, 4, v224\n"
        "v_sub_u32 v228, s14, v228\n"
        "s_mov_b64 exec, 3\n"
        "ds_write_b128 v229, v[236:239]\n"
        "s_mov_b64 exec, -1\n"
        "v_add_u32 v225, 1024, v224\n"
        "v_add_u32 v226, 2048, v224\n"
        "v_add_u32 v227, 3072, v224\n"
        "v_lshl_add_u32 v228, v233, 4, v228\n"
        "ds_read2_b32 a[0:1], v224 offset0:0 offset1:1\n"
        "ds_read2_b32 a[2:3], v224 offset0:2 offset1:3\n"
        "v_add_u32 v230, 3584, v228\n"
        "v_min_u32 v230, s16, v230\n"
        "ds_read_b128 a[192:195], v230 offset:0\n"
        "ds_read2_b32 a[4:5], v224 offset0:128 offset1:129\n"
        "ds_read2_b32 a[6:7], v224 offset0:130 offset1:131\n"
        "ds_read_b128 a[196:199], v228 offset:3072\n"
        "ds_read2_b32 a[8:9], v225 offset0:0 offset1:1\n"
        "ds_read2_b32 a[10:11], v225 offset0:2 offset1:3\n"
        "ds_read_b128 a[200:203], v228 offset:2560\n"
        "ds_read2_b32 a[12:13], v225 offset0:128 offset1:129\n"
        "ds_read2_b32 a[14:15], v225 offset0:130 offset1:131\n"
        "ds_read_b128 a[204:207], v228 offset:2048\n"
        "ds_read2_b32 a[16:17], v226 offset0:0 offset1:1\n"
        "ds_read2_b32 a[18:19], v226 offset0:2 offset1:3\n"
        "ds_read_b128 a[208:211], v228 offset:1536\n"
        "s_lshl_b32 s53, s45, 20\n"
        "ds_read2_b32 a[20:21], v226 offset0:128 offset1:129\n"
        "s_lshl_b32 s96, s43, 15\n"
        "ds_read2_b32 a[22:23], v226 offset0:130 offset1:131\n"
        "s_add_u32 s53, s53, s96\n"
        "ds_read_b128 a[212:215], v228 offset:1024\n"
        "s_lshl_b32 s96, s40, 10\n"
        "ds_read2_b32 a[24:25], v227 offset0:0 offset1:1\n"
        "s_add_u32 s53, s53, s96\n"
        "ds_read2_b32 a[26:27], v227 offset0:2 offset1:3\n"
        "s_add_u32 s53, s53, 0x2001000\n"
        "ds_read_b128 a[216:219], v228 offset:512\n"
        "ds_read2_b32 a[28:29], v227 offset0:128 offset1:129\n"
        "ds_read2_b32 a[30:31], v227 offset0:130 offset1:131\n"
        "ds_read_b128 a[220:223], v228 offset:0\n"
        "v_mfma_f32_16x16x32_bf16 v[176:179], a[128:131], v[16:19], v[176:179]\n"
        "s_add_u32 s70, s38, s53\n"
        "s_addc_u32 s71, s39, 0\n"
        "s_waitcnt lgkmcnt(14)\n"
        "v_mfma_f32_16x16x32_bf16 v[208:211], a[128:131], v[20:23], v[208:211]\n"
        "v_exp_f32 v48, v48\n"
        "v_mfma_f32_16x16x32_bf16 v[180:183], a[132:135], v[16:19], v[180:183]\n"
        "v_exp_f32 v49, v49\n"
        "v_mfma_f32_16x16x32_bf16 v[212:215], a[132:135], v[20:23], v[212:215]\n"
        "ds_read_b128 a[156:159], v241 offset:27648\n"
        "v_cvt_pk_bf16_f32 v82, v48, v49\n"
        "v_mfma_f32_16x16x32_bf16 v[184:187], a[136:139], v[16:19], v[184:187]\n"
        "v_exp_f32 v50, v50\n"
        "v_mfma_f32_16x16x32_bf16 v[216:219], a[136:139], v[20:23], v[216:219]\n"
        "s_waitcnt lgkmcnt(6)\n"
        "v_exp_f32 v51, v51\n"
        "v_mfma_f32_16x16x32_bf16 v[188:191], a[140:143], v[16:19], v[188:191]\n"
        "ds_read_b128 a[128:131], v241 offset:28672\n"
        "v_cvt_pk_bf16_f32 v83, v50, v51\n"
        "s_nop 1\n"
        "v_mfma_f32_16x16x32_bf16 a[64:67], a[80:83], v[80:83], a[64:67]\n"
        "v_exp_f32 v52, v52\n"
        "v_mfma_f32_16x16x32_bf16 v[220:223], a[140:143], v[20:23], v[220:223]\n"
        "v_exp_f32 v53, v53\n"
        "v_mfma_f32_16x16x32_bf16 v[160:163], a[144:147], v[80:83], v[160:163]\n"
        "s_waitcnt lgkmcnt(2)\n"
        "v_cvt_pk_bf16_f32 v86, v52, v53\n"
        "v_add_u32 v231, 3648, v228\n"
        "v_mfma_f32_16x16x32_bf16 v[164:167], a[148:151], v[80:83], v[164:167]\n"
        "ds_read_b128 a[132:135], v241 offset:29696\n"
        "v_accvgpr_read_b32 v248, a64\n"
        "v_mfma_f32_16x16x32_bf16 v[168:171], a[152:155], v[80:83], v[168:171]\n"
        "s_waitcnt lgkmcnt(2)\n"
        "v_rcp_f32 v246, v248\n"
        "v_mfma_f32_16x16x32_bf16 v[172:175], a[156:159], v[80:83], v[172:175]\n"
        "v_exp_f32 v54, v54\n"
        "v_mfma_f32_16x16x32_bf16 v[96:99], a[0:3], a[192:195], 0\n"
        "s_waitcnt lgkmcnt(1)\n"
        "v_exp_f32 v55, v55\n"
        "v_mfma_f32_16x16x32_bf16 v[176:179], a[128:131], v[80:83], v[176:179]\n"
        "ds_read_b128 a[136:139], v241 offset:30720\n"
        "v_cvt_pk_bf16_f32 v87, v54, v55\n"
        "s_nop 1\n"
        "v_mfma_f32_16x16x32_bf16 v[192:195], a[144:147], v[84:87], v[192:195]\n"
        "ds_read_b128 a[140:143], v241 offset:31744\n"
        "v_mul_f32 v160, v160, v246\n"
        "v_mfma_f32_16x16x32_bf16 a[68:71], a[80:83], v[84:87], a[68:71]\n"
        "v_mul_f32 v161, v161, v246\n"
        "v_mul_f32 v162, v162, v246\n"
        "v_mfma_f32_16x16x32_bf16 v[196:199], a[148:151], v[84:87], v[196:199]\n"
        "v_mul_f32 v163, v163, v246\n"
        "v_mul_f32 v164, v164, v246\n"
        "v_mfma_f32_16x16x32_bf16 v[200:203], a[152:155], v[84:87], v[200:203]\n"
        "s_waitcnt lgkmcnt(2)\n"
        "v_mul_f32 v165, v165, v246\n"
        "v_accvgpr_read_b32 v249, a68\n"
        "v_mfma_f32_16x16x32_bf16 v[204:207], a[156:159], v[84:87], v[204:207]\n"
        "v_rcp_f32 v247, v249\n"
        "v_mfma_f32_16x16x32_bf16 v[208:211], a[128:131], v[84:87], v[208:211]\n"
        "v_mul_f32 v166, v166, v246\n"
        "v_mul_f32 v167, v167, v246\n"
        "v_mfma_f32_16x16x32_bf16 v[180:183], a[132:135], v[80:83], v[180:183]\n"
        "v_cvt_pk_bf16_f32 v160, v160, v161\n"
        "v_cvt_pk_bf16_f32 v161, v162, v163\n"
        "v_mfma_f32_16x16x32_bf16 v[212:215], a[132:135], v[84:87], v[212:215]\n"
        "s_waitcnt lgkmcnt(1)\n"
        "v_cvt_pk_bf16_f32 v162, v164, v165\n"
        "v_cvt_pk_bf16_f32 v163, v166, v167\n"
        "v_mfma_f32_16x16x32_bf16 v[184:187], a[136:139], v[80:83], v[184:187]\n"
        "global_store_dwordx4 v254, v[160:163], s[70:71] offset:0 sc1\n"
        "s_waitcnt lgkmcnt(0)\n"
        "v_mul_f32 v192, v192, v247\n"
        "v_mul_f32 v193, v193, v247\n"
        "v_mul_f32 v194, v194, v247\n"
        "v_mfma_f32_16x16x32_bf16 v[216:219], a[136:139], v[84:87], v[216:219]\n"
        "v_mul_f32 v195, v195, v247\n"
        "v_mul_f32 v196, v196, v247\n"
        "v_mfma_f32_16x16x32_bf16 v[188:191], a[140:143], v[80:83], v[188:191]\n"
        "v_mul_f32 v197, v197, v247\n"
        "v_mul_f32 v198, v198, v247\n"
        "v_mfma_f32_16x16x32_bf16 v[220:223], a[140:143], v[84:87], v[220:223]\n"
        "v_mul_f32 v199, v199, v247\n"
        "v_cvt_pk_bf16_f32 v192, v192, v193\n"
        "v_mfma_f32_16x16x32_bf16 v[100:103], a[0:3], a[196:199], 0\n"
        "v_cvt_pk_bf16_f32 v193, v194, v195\n"
        "v_cvt_pk_bf16_f32 v194, v196, v197\n"
        "v_mfma_f32_16x16x32_bf16 v[104:107], a[0:3], a[200:203], 0\n"
        "v_cvt_pk_bf16_f32 v195, v198, v199\n"
        "v_mul_f32 v168, v168, v246\n"
        "v_mfma_f32_16x16x32_bf16 v[108:111], a[0:3], a[204:207], 0\n"
        "global_store_dwordx4 v254, v[192:195], s[70:71] offset:-4096 sc1\n"
        "s_add_u32 s70, s70, 0x2000\n"
        "s_addc_u32 s71, s71, 0\n"
        "v_mul_f32 v169, v169, v246\n"
        "v_mul_f32 v170, v170, v246\n"
        "v_mul_f32 v171, v171, v246\n"
        "v_mfma_f32_16x16x32_bf16 v[128:131], a[0:3], a[208:211], 0\n"
        "v_mul_f32 v172, v172, v246\n"
        "v_mul_f32 v173, v173, v246\n"
        "v_mfma_f32_16x16x32_bf16 v[132:135], a[0:3], a[212:215], 0\n"
        "v_mul_f32 v174, v174, v246\n"
        "v_mul_f32 v175, v175, v246\n"
        "v_mfma_f32_16x16x32_bf16 v[136:139], a[0:3], a[216:219], 0\n"
        "v_cvt_pk_bf16_f32 v168, v168, v169\n"
        "v_cvt_pk_bf16_f32 v169, v170, v171\n"
        "v_cvt_pk_bf16_f32 v170, v172, v173\n"
        "v_cvt_pk_bf16_f32 v171, v174, v175\n"
        "v_mfma_f32_16x16x32_bf16 v[140:143], a[0:3], a[220:223], 0\n"
        "global_store_dwordx4 v254, v[168:171], s[70:71] offset:0 sc1\n"
        "v_mul_f32 v200, v200, v247\n"
        "v_mul_f32 v201, v201, v247\n"
        "v_mul_f32 v202, v202, v247\n"
        "v_mul_f32 v203, v203, v247\n"
        "v_mul_f32 v204, v204, v247\n"
        "v_mul_f32 v205, v205, v247\n"
        "v_mul_f32 v206, v206, v247\n"
        "v_mul_f32 v207, v207, v247\n"
        "v_cvt_pk_bf16_f32 v200, v200, v201\n"
        "v_cvt_pk_bf16_f32 v201, v202, v203\n"
        "v_cvt_pk_bf16_f32 v202, v204, v205\n"
        "v_cvt_pk_bf16_f32 v203, v206, v207\n"
        "v_mfma_f32_16x16x32_bf16 v[100:103], a[4:7], a[192:195], v[100:103]\n"
        "global_store_dwordx4 v254, v[200:203], s[70:71] offset:-4096 sc1\n"
        "s_add_u32 s70, s70, 0x2000\n"
        "s_addc_u32 s71, s71, 0\n"
        "v_mul_f32 v176, v176, v246\n"
        "v_mul_f32 v177, v177, v246\n"
        "v_mul_f32 v178, v178, v246\n"
        "v_mfma_f32_16x16x32_bf16 v[104:107], a[4:7], a[196:199], v[104:107]\n"
        "v_mul_f32 v179, v179, v246\n"
        "v_mul_f32 v180, v180, v246\n"
        "v_mfma_f32_16x16x32_bf16 v[108:111], a[4:7], a[200:203], v[108:111]\n"
        "v_mul_f32 v181, v181, v246\n"
        "v_mul_f32 v182, v182, v246\n"
        "v_mul_f32 v183, v183, v246\n"
        "v_cvt_pk_bf16_f32 v176, v176, v177\n"
        "v_cvt_pk_bf16_f32 v177, v178, v179\n"
        "v_cvt_pk_bf16_f32 v178, v180, v181\n"
        "v_cvt_pk_bf16_f32 v179, v182, v183\n"
        "v_mfma_f32_16x16x32_bf16 v[128:131], a[4:7], a[204:207], v[128:131]\n"
        "global_store_dwordx4 v254, v[176:179], s[70:71] offset:0 sc1\n"
        "v_mul_f32 v208, v208, v247\n"
        "v_mul_f32 v209, v209, v247\n"
        "v_mul_f32 v210, v210, v247\n"
        "v_mul_f32 v211, v211, v247\n"
        "v_mul_f32 v212, v212, v247\n"
        "v_mul_f32 v213, v213, v247\n"
        "v_mul_f32 v214, v214, v247\n"
        "v_mul_f32 v215, v215, v247\n"
        "v_cvt_pk_bf16_f32 v208, v208, v209\n"
        "v_cvt_pk_bf16_f32 v209, v210, v211\n"
        "v_cvt_pk_bf16_f32 v210, v212, v213\n"
        "v_cvt_pk_bf16_f32 v211, v214, v215\n"
        "v_mfma_f32_16x16x32_bf16 v[132:135], a[4:7], a[208:211], v[132:135]\n"
        "global_store_dwordx4 v254, v[208:211], s[70:71] offset:-4096 sc1\n"
        "s_add_u32 s70, s70, 0x2000\n"
        "s_addc_u32 s71, s71, 0\n"
        "v_mul_f32 v184, v184, v246\n"
        "v_mul_f32 v185, v185, v246\n"
        "v_mul_f32 v186, v186, v246\n"
        "v_mfma_f32_16x16x32_bf16 v[136:139], a[4:7], a[212:215], v[136:139]\n"
        "v_mul_f32 v187, v187, v246\n"
        "v_mul_f32 v188, v188, v246\n"
        "v_mfma_f32_16x16x32_bf16 v[140:143], a[4:7], a[216:219], v[140:143]\n"
        "v_mul_f32 v189, v189, v246\n"
        "v_mul_f32 v190, v190, v246\n"
        "v_mul_f32 v191, v191, v246\n"
        "v_cvt_pk_bf16_f32 v184, v184, v185\n"
        "v_cvt_pk_bf16_f32 v185, v186, v187\n"
        "v_cvt_pk_bf16_f32 v186, v188, v189\n"
        "v_cvt_pk_bf16_f32 v187, v190, v191\n"
        "v_mfma_f32_16x16x32_bf16 v[104:107], a[8:11], a[192:195], v[104:107]\n"
        "global_store_dwordx4 v254, v[184:187], s[70:71] offset:0 sc1\n"
        "ds_read2_b32 a[32:33], v224 offset0:16 offset1:17\n"
        "ds_read2_b32 a[34:35], v224 offset0:18 offset1:19\n"
        "v_mul_f32 v216, v216, v247\n"
        "v_mul_f32 v217, v217, v247\n"
        "v_mul_f32 v218, v218, v247\n"
        "v_mul_f32 v219, v219, v247\n"
        "v_mul_f32 v220, v220, v247\n"
        "v_mul_f32 v221, v221, v247\n"
        "v_mul_f32 v222, v222, v247\n"
        "v_mul_f32 v223, v223, v247\n"
        "v_cvt_pk_bf16_f32 v216, v216, v217\n"
        "v_cvt_pk_bf16_f32 v217, v218, v219\n"
        "v_cvt_pk_bf16_f32 v218, v220, v221\n"
        "v_cvt_pk_bf16_f32 v219, v222, v223\n"
        "v_add_f32 v247, v248, v249\n"
        "v_cmp_nge_f32 vcc, 0x4e6e6b28, v247\n"
        "v_mfma_f32_16x16x32_bf16 v[108:111], a[8:11], a[196:199], v[108:111]\n"
        "global_store_dwordx4 v254, v[216:219], s[70:71] offset:-4096 sc1\n"
        "s_cbranch_vccnz LRH_%=\n"
        "LSH_%=:\n"
        "v_min_u32 v231, s16, v231\n"
        "ds_read_b128 a[224:227], v231 offset:0\n"
        "v_mfma_f32_16x16x32_bf16 v[128:131], a[8:11], a[200:203], v[128:131]\n"
        "ds_read2_b32 a[36:37], v224 offset0:144 offset1:145\n"
        "v_mfma_f32_16x16x32_bf16 v[132:135], a[8:11], a[204:207], v[132:135]\n"
        "ds_read2_b32 a[38:39], v224 offset0:146 offset1:147\n"
        "v_mfma_f32_16x16x32_bf16 v[136:139], a[8:11], a[208:211], v[136:139]\n"
        "ds_read_b128 a[228:231], v228 offset:3136\n"
        "v_mfma_f32_16x16x32_bf16 v[140:143], a[8:11], a[212:215], v[140:143]\n"
        "ds_read2_b32 a[40:41], v225 offset0:16 offset1:17\n"
        "v_mfma_f32_16x16x32_bf16 v[108:111], a[12:15], a[192:195], v[108:111]\n"
        "ds_read2_b32 a[42:43], v225 offset0:18 offset1:19\n"
        "v_mfma_f32_16x16x32_bf16 v[128:131], a[12:15], a[196:199], v[128:131]\n"
        "ds_read_b128 a[232:235], v228 offset:2624\n"
        "v_mfma_f32_16x16x32_bf16 v[132:135], a[12:15], a[200:203], v[132:135]\n"
        "ds_read2_b32 a[44:45], v225 offset0:144 offset1:145\n"
        "s_waitcnt lgkmcnt(8)\n"
        "v_mfma_f32_16x16x32_bf16 v[136:139], a[12:15], a[204:207], v[136:139]\n"
        "ds_read2_b32 a[46:47], v225 offset0:146 offset1:147\n"
        "s_waitcnt lgkmcnt(8)\n"
        "v_mfma_f32_16x16x32_bf16 v[140:143], a[12:15], a[208:211], v[140:143]\n"
        "ds_read_b128 a[236:239], v228 offset:2112\n"
        "v_mfma_f32_16x16x32_bf16 v[128:131], a[16:19], a[192:195], v[128:131]\n"
        "ds_read2_b32 a[48:49], v226 offset0:16 offset1:17\n"
        "s_waitcnt lgkmcnt(8)\n"
        "v_mfma_f32_16x16x32_bf16 v[132:135], a[16:19], a[196:199], v[132:135]\n"
        "ds_read2_b32 a[50:51], v226 offset0:18 offset1:19\n"
        "s_waitcnt lgkmcnt(8)\n"
        "v_mfma_f32_16x16x32_bf16 v[136:139], a[16:19], a[200:203], v[136:139]\n"
        "ds_read_b128 a[240:243], v228 offset:1600\n"
        "s_and_b32 s10, s41, 1\n"
        "s_and_b32 s11, s41, 4\n"
        "v_mfma_f32_16x16x32_bf16 v[140:143], a[16:19], a[204:207], v[140:143]\n"
        "ds_read2_b32 a[52:53], v226 offset0:144 offset1:145\n"
        "s_and_b32 s14, s42, 3\n"
        "s_add_u32 s11, s11, s14\n"
        "v_mfma_f32_16x16x32_bf16 v[132:135], a[20:23], a[192:195], v[132:135]\n"
        "ds_read2_b32 a[54:55], v226 offset0:146 offset1:147\n"
        "s_and_b32 s12, s41, 2\n"
        "s_lshr_b32 s14, s42, 2\n"
        "v_mfma_f32_16x16x32_bf16 v[136:139], a[20:23], a[196:199], v[136:139]\n"
        "ds_read_b128 a[244:247], v228 offset:1088\n"
        "s_lshl2_add_u32 s12, s12, s14\n"
        "s_lshl3_add_u32 s13, s12, s11\n"
        "v_mfma_f32_16x16x32_bf16 v[140:143], a[20:23], a[200:203], v[140:143]\n"
        "ds_read2_b32 a[56:57], v227 offset0:16 offset1:17\n"
        "s_lshl1_add_u32 s13, s13, s10\n"
        "s_lshl_b32 s14, s10, 22\n"
        "v_mfma_f32_16x16x32_bf16 v[136:139], a[24:27], a[192:195], v[136:139]\n"
        "ds_read2_b32 a[58:59], v227 offset0:18 offset1:19\n"
        "s_lshl_b32 s15, s40, 15\n"
        "s_add_u32 s14, s14, s15\n"
        "v_mfma_f32_16x16x32_bf16 v[140:143], a[24:27], a[196:199], v[140:143]\n"
        "ds_read_b128 a[248:251], v228 offset:576\n"
        "s_lshl_b32 s15, s11, 8\n"
        "s_add_u32 s14, s14, s15\n"
        "v_mfma_f32_16x16x32_bf16 v[140:143], a[28:31], a[192:195], v[140:143]\n"
        "ds_read2_b32 a[60:61], v227 offset0:144 offset1:145\n"
        "s_add_u32 s22, s18, s14\n"
        "s_addc_u32 s23, s19, 0\n"
        "v_mfma_f32_16x16x32_bf16 v[96:99], a[32:35], a[224:227], v[96:99]\n"
        "ds_read2_b32 a[62:63], v227 offset0:146 offset1:147\n"
        "s_sub_u32 s22, s22, 4096\n"
        "s_subb_u32 s23, s23, 0\n"
        "v_mfma_f32_16x16x32_bf16 v[100:103], a[32:35], a[228:231], v[100:103]\n"
        "ds_read_b128 a[252:255], v228 offset:64\n"
        "s_lshl_b32 s14, s12, 20\n"
        "s_waitcnt lgkmcnt(8)\n"
        "v_mfma_f32_16x16x32_bf16 v[104:107], a[32:35], a[232:235], v[104:107]\n"
        "s_lshl_b32 s15, s10, 19\n"
        "s_add_u32 s14, s14, s15\n"
        "s_add_u32 s14, s14, s73\n"
        "v_mfma_f32_16x16x32_bf16 v[108:111], a[32:35], a[236:239], v[108:111]\n"
        "s_add_u32 s14, s14, 0x2000000\n"
        "s_add_u32 s20, s38, s14\n"
        "s_addc_u32 s21, s39, 0\n"
        "v_mfma_f32_16x16x32_bf16 v[128:131], a[32:35], a[240:243], v[128:131]\n"
        "s_waitcnt lgkmcnt(5)\n"
        "v_lshrrev_b32 v246, 7, v254\n"
        "v_bfe_u32 v247, v254, 4, 3\n"
        "v_mfma_f32_16x16x32_bf16 v[132:135], a[32:35], a[244:247], v[132:135]\n"
        "s_waitcnt lgkmcnt(4)\n"
        "v_and_b32 v248, 6, v246\n"
        "v_xor_b32 v248, v248, v247\n"
        "v_mfma_f32_16x16x32_bf16 v[100:103], a[36:39], a[224:227], v[100:103]\n"
        "s_waitcnt lgkmcnt(3)\n"
        "v_lshlrev_b32 v248, 4, v248\n"
        "v_lshl_add_u32 v249, v246, 11, v248\n"
        "v_mfma_f32_16x16x32_bf16 v[136:139], a[32:35], a[248:251], v[136:139]\n"
        "s_lshl_b32 s14, s10, 12\n"
        "v_xor_b32 v246, 4, v246\n"
        "v_lshl_add_u32 v246, v246, 11, v248\n"
        "v_mfma_f32_16x16x32_bf16 v[104:107], a[36:39], a[228:231], v[104:107]\n"
        "s_waitcnt lgkmcnt(1)\n"
        "v_add_u32 v144, 4096, v249\n"
        "v_add_u32 v145, 3200, v249\n"
        "v_mfma_f32_16x16x32_bf16 v[108:111], a[36:39], a[232:235], v[108:111]\n"
        "s_waitcnt lgkmcnt(0)\n"
        "v_add_u32 v146, 18432, v246\n"
        "v_add_u32 v147, 17536, v246\n"
        "v_mfma_f32_16x16x32_bf16 v[140:143], a[32:35], a[252:255], v[140:143]\n"
        "s_waitcnt vmcnt(8)\n"
        "v_add_u32 v151, s14, v254\n"
        "v_xor_b32 v148, 0x1000, v151\n"
        "v_mfma_f32_16x16x32_bf16 v[128:131], a[36:39], a[236:239], v[128:131]\n"
        "s_lshr_b32 s14, s40, 1\n"
        "s_lshl_b32 s14, s14, 10\n"
        "v_lshrrev_b32 v246, 1, v233\n"
        "v_mfma_f32_16x16x32_bf16 v[132:135], a[36:39], a[240:243], v[132:135]\n"
        "s_barrier\n"
        "v_lshlrev_b32 v247, 11, v246\n"
        "v_add_u32 v247, s14, v247\n"
        "v_mfma_f32_16x16x32_bf16 v[136:139], a[36:39], a[244:247], v[136:139]\n"
        "s_and_b32 s14, s40, 1\n"
        "v_bfe_u32 v248, v254, 6, 3\n"
        "v_bfe_u32 v249, v254, 4, 2\n"
        "v_mfma_f32_16x16x32_bf16 v[140:143], a[36:39], a[248:251], v[140:143]\n"
        "s_lshl_b32 s14, s14, 6\n"
        "v_lshlrev_b32 v240, 2, v246\n"
        "v_xor_b32 v240, v240, v248\n"
        "v_mfma_f32_16x16x32_bf16 v[104:107], a[40:43], a[224:227], v[104:107]\n"
        "s_add_u32 m0, s73, 0x0\n"
        "v_bfe_u32 v241, v240, 1, 2\n"
        "v_xor_b32 v241, 0, v241\n"
        "v_lshlrev_b32 v241, 5, v241\n"
        "v_lshl_add_u32 v240, v240, 7, v241\n"
        "v_lshl_add_u32 v240, v249, 3, v240\n"
        "v_add_u32 v154, v240, v247\n"
        "v_xor_b32 v154, s14, v154\n"
        "v_xor_b32 v155, 32, v154\n"
        "v_add_u32 v156, 0x10000, v154\n"
        "v_add_u32 v157, 0x10000, v155\n"
        "v_add_u32 v149, 0x10000, v148\n"
        "v_add_u32 v152, 0x10000, v151\n"
        "v_add_u32 v158, 0x20000, v154\n"
        "v_add_u32 v159, 0x20000, v155\n"
        "v_add_u32 v150, 0x20000, v148\n"
        "v_add_u32 v153, 0x20000, v151\n"
        "v_mfma_f32_16x16x32_bf16 v[108:111], a[40:43], a[228:231], v[108:111]\n"
        "s_add_u32 s60, s22, 0x0\n"
        "s_addc_u32 s61, s23, 0\n"
        "v_add_u32 v230, 3712, v228\n"
        "v_mfma_f32_16x16x32_bf16 v[128:131], a[40:43], a[232:235], v[128:131]\n"
        "global_load_lds_dwordx4 v144, s[60:61] offset:0\n"
        "ds_read2_b32 a[0:1], v224 offset0:32 offset1:33\n"
        "v_mfma_f32_16x16x32_bf16 v[132:135], a[40:43], a[236:239], v[132:135]\n"
        "ds_read2_b32 a[2:3], v224 offset0:34 offset1:35\n"
        "v_min_u32 v230, s16, v230\n"
        "v_mfma_f32_16x16x32_bf16 v[136:139], a[40:43], a[240:243], v[136:139]\n"
        "ds_read_b128 a[192:195], v230 offset:0\n"
        "v_mfma_f32_16x16x32_bf16 v[140:143], a[40:43], a[244:247], v[140:143]\n"
        "global_load_lds_dwordx4 v145, s[60:61] offset:1024\n"
        "ds_read2_b32 a[4:5], v224 offset0:160 offset1:161\n"
        "v_mfma_f32_16x16x32_bf16 v[108:111], a[44:47], a[224:227], v[108:111]\n"
        "ds_read2_b32 a[6:7], v224 offset0:162 offset1:163\n"
        "v_mfma_f32_16x16x32_bf16 v[128:131], a[44:47], a[228:231], v[128:131]\n"
        "ds_read_b128 a[196:199], v228 offset:3200\n"
        "v_mfma_f32_16x16x32_bf16 v[132:135], a[44:47], a[232:235], v[132:135]\n"
        "global_load_lds_dwordx4 v146, s[60:61] offset:2048\n"
        "ds_read2_b32 a[8:9], v225 offset0:32 offset1:33\n"
        "v_mfma_f32_16x16x32_bf16 v[136:139], a[44:47], a[236:239], v[136:139]\n"
        "ds_read2_b32 a[10:11], v225 offset0:34 offset1:35\n"
        "v_mfma_f32_16x16x32_bf16 v[140:143], a[44:47], a[240:243], v[140:143]\n"
        "ds_read_b128 a[200:203], v228 offset:2688\n"
        "v_mfma_f32_16x16x32_bf16 v[128:131], a[48:51], a[224:227], v[128:131]\n"
        "global_load_lds_dwordx4 v147, s[60:61] offset:3072\n"
        "s_add_u32 m0, s73, 0x4000\n"
        "s_waitcnt lgkmcnt(7)\n"
        "v_mfma_f32_16x16x32_bf16 v[132:135], a[48:51], a[228:231], v[132:135]\n"
        "s_add_u32 s60, s22, 0x40000\n"
        "s_addc_u32 s61, s23, 0\n"
        "s_waitcnt lgkmcnt(6)\n"
        "v_mfma_f32_16x16x32_bf16 v[136:139], a[48:51], a[232:235], v[136:139]\n"
        "ds_read2_b32 a[12:13], v225 offset0:160 offset1:161\n"
        "v_mfma_f32_16x16x32_bf16 v[140:143], a[48:51], a[236:239], v[140:143]\n"
        "global_load_lds_dwordx4 v144, s[60:61] offset:0\n"
        "ds_read2_b32 a[14:15], v225 offset0:162 offset1:163\n"
        "v_mfma_f32_16x16x32_bf16 v[132:135], a[52:55], a[224:227], v[132:135]\n"
        "ds_read_b128 a[204:207], v228 offset:2176\n"
        "s_waitcnt lgkmcnt(6)\n"
        "v_mfma_f32_16x16x32_bf16 v[136:139], a[52:55], a[228:231], v[136:139]\n"
        "ds_read2_b32 a[16:17], v226 offset0:32 offset1:33\n"
        "v_mfma_f32_16x16x32_bf16 v[140:143], a[52:55], a[232:235], v[140:143]\n"
        "global_load_lds_dwordx4 v145, s[60:61] offset:1024\n"
        "ds_read2_b32 a[18:19], v226 offset0:34 offset1:35\n"
        "v_mfma_f32_16x16x32_bf16 v[136:139], a[56:59], a[224:227], v[136:139]\n"
        "ds_read_b128 a[208:211], v228 offset:1664\n"
        "s_lshl_b32 s14, s44, 5\n"
        "s_add_u32 s14, s14, s43\n"
        "v_mfma_f32_16x16x32_bf16 v[140:143], a[56:59], a[228:231], v[140:143]\n"
        "ds_read2_b32 a[20:21], v226 offset0:160 offset1:161\n"
        "s_lshl2_add_u32 s14, s14, s40\n"
        "s_add_u32 s14, s14, 1024\n"
        "v_mfma_f32_16x16x32_bf16 v[140:143], a[60:63], a[224:227], v[140:143]\n"
        "global_load_lds_dwordx4 v146, s[60:61] offset:2048\n"
        "ds_read2_b32 a[22:23], v226 offset0:162 offset1:163\n"
        "s_lshl_b32 s14, s14, 7\n"
        "v_mfma_f32_16x16x32_bf16 v[96:99], a[0:3], a[192:195], v[96:99]\n"
        "ds_read_b128 a[212:215], v228 offset:1152\n"
        "s_waitcnt vmcnt(7)\n"
        "v_mfma_f32_16x16x32_bf16 v[100:103], a[0:3], a[196:199], v[100:103]\n"
        "ds_read2_b32 a[24:25], v227 offset0:32 offset1:33\n"
        "s_waitcnt lgkmcnt(8)\n"
        "v_mfma_f32_16x16x32_bf16 v[104:107], a[0:3], a[200:203], v[104:107]\n"
        "global_load_lds_dwordx4 v147, s[60:61] offset:3072\n"
        "s_add_u32 m0, s73, 0x8000\n"
        "s_waitcnt lgkmcnt(7)\n"
        "v_mfma_f32_16x16x32_bf16 v[108:111], a[0:3], a[204:207], v[108:111]\n"
        "ds_read2_b32 a[26:27], v227 offset0:34 offset1:35\n"
        "v_lshrrev_b32 v234, 1, v254\n"
        "v_mfma_f32_16x16x32_bf16 v[100:103], a[4:7], a[192:195], v[100:103]\n"
        "ds_read_b128 a[216:219], v228 offset:640\n"
        "v_add_u32 v234, s14, v234\n"
        "v_mfma_f32_16x16x32_bf16 v[104:107], a[4:7], a[196:199], v[104:107]\n"
        "ds_read2_b32 a[28:29], v227 offset0:160 offset1:161\n"
        "s_lshl_b32 s14, s45, 5\n"
        "s_waitcnt lgkmcnt(7)\n"
        "v_mfma_f32_16x16x32_bf16 v[128:131], a[0:3], a[208:211], v[128:131]\n"
        "ds_read2_b32 a[30:31], v227 offset0:162 offset1:163\n"
        "s_add_u32 s14, s14, s43\n"
        "s_lshl2_add_u32 s14, s14, s40\n"
        "v_mfma_f32_16x16x32_bf16 v[108:111], a[4:7], a[200:203], v[108:111]\n"
        "ds_read_b128 a[220:223], v228 offset:128\n"
        "s_add_u32 s14, s14, 1024\n"
        "s_lshl_b32 s14, s14, 7\n"
        "v_mfma_f32_16x16x32_bf16 v[128:131], a[4:7], a[204:207], v[128:131]\n"
        "s_add_u32 s60, s22, 0xa0000\n"
        "s_addc_u32 s61, s23, 0\n"
        "s_waitcnt lgkmcnt(6)\n"
        "v_mfma_f32_16x16x32_bf16 v[132:135], a[0:3], a[212:215], v[132:135]\n"
        "s_mov_b64 exec, 0xff\n"
        "global_store_dwordx2 v234, v[250:251], s[48:49] sc1\n"
        "s_mov_b64 exec, -1\n"
        "v_mfma_f32_16x16x32_bf16 v[132:135], a[4:7], a[208:211], v[132:135]\n"
        "s_waitcnt lgkmcnt(5)\n"
        "v_lshrrev_b32 v234, 1, v254\n"
        "v_add_u32 v234, s14, v234\n"
        "v_mfma_f32_16x16x32_bf16 v[104:107], a[8:11], a[192:195], v[104:107]\n"
        "global_load_lds_dwordx4 v144, s[60:61] offset:0\n"
        "s_waitcnt lgkmcnt(3)\n"
        "v_mfma_f32_16x16x32_bf16 v[136:139], a[0:3], a[216:219], v[136:139]\n"
        "ds_read2_b32 a[32:33], v224 offset0:48 offset1:49\n"
        "v_add_u32 v231, 3776, v228\n"
        "v_mfma_f32_16x16x32_bf16 v[136:139], a[4:7], a[212:215], v[136:139]\n"
        "ds_read2_b32 a[34:35], v224 offset0:50 offset1:51\n"
        "v_min_u32 v231, s16, v231\n"
        "v_mfma_f32_16x16x32_bf16 v[108:111], a[8:11], a[196:199], v[108:111]\n"
        "global_load_lds_dwordx4 v145, s[60:61] offset:1024\n"
        "s_waitcnt lgkmcnt(2)\n"
        "v_mfma_f32_16x16x32_bf16 v[140:143], a[0:3], a[220:223], v[140:143]\n"
        "ds_read_b128 a[224:227], v231 offset:0\n"
        "v_mfma_f32_16x16x32_bf16 v[140:143], a[4:7], a[216:219], v[140:143]\n"
        "ds_read2_b32 a[36:37], v224 offset0:176 offset1:177\n"
        "v_mfma_f32_16x16x32_bf16 v[128:131], a[8:11], a[200:203], v[128:131]\n"
        "s_mov_b64 exec, 0xff\n"
        "global_store_dwordx2 v234, v[250:251], s[48:49] sc1\n"
        "s_mov_b64 exec, -1\n"
        "v_mfma_f32_16x16x32_bf16 v[132:135], a[8:11], a[204:207], v[132:135]\n"
        "global_load_lds_dwordx4 v146, s[60:61] offset:2048\n"
        "ds_read2_b32 a[38:39], v224 offset0:178 offset1:179\n"
        "v_mfma_f32_16x16x32_bf16 v[136:139], a[8:11], a[208:211], v[136:139]\n"
        "ds_read_b128 a[228:231], v228 offset:3264\n"
        "v_mfma_f32_16x16x32_bf16 v[140:143], a[8:11], a[212:215], v[140:143]\n"
        "ds_read2_b32 a[40:41], v225 offset0:48 offset1:49\n"
        "v_mfma_f32_16x16x32_bf16 v[108:111], a[12:15], a[192:195], v[108:111]\n"
        "global_load_lds_dwordx4 v147, s[60:61] offset:3072\n"
        "ds_read2_b32 a[42:43], v225 offset0:50 offset1:51\n"
        "s_add_u32 m0, s73, 0xc000\n"
        "v_mfma_f32_16x16x32_bf16 v[128:131], a[12:15], a[196:199], v[128:131]\n"
        "ds_read_b128 a[232:235], v228 offset:2752\n"
        "s_add_u32 s60, s22, 0x100000\n"
        "s_addc_u32 s61, s23, 0\n"
        "v_mfma_f32_16x16x32_bf16 v[132:135], a[12:15], a[200:203], v[132:135]\n"
        "ds_read2_b32 a[44:45], v225 offset0:176 offset1:177\n"
        "s_waitcnt lgkmcnt(7)\n"
        "v_mfma_f32_16x16x32_bf16 v[136:139], a[12:15], a[204:207], v[136:139]\n"
        "global_load_lds_dwordx4 v144, s[60:61] offset:0\n"
        "ds_read2_b32 a[46:47], v225 offset0:178 offset1:179\n"
        "v_mfma_f32_16x16x32_bf16 v[140:143], a[12:15], a[208:211], v[140:143]\n"
        "ds_read_b128 a[236:239], v228 offset:2240\n"
        "v_mfma_f32_16x16x32_bf16 v[128:131], a[16:19], a[192:195], v[128:131]\n"
        "ds_read2_b32 a[48:49], v226 offset0:48 offset1:49\n"
        "s_waitcnt lgkmcnt(8)\n"
        "v_mfma_f32_16x16x32_bf16 v[132:135], a[16:19], a[196:199], v[132:135]\n"
        "global_load_lds_dwordx4 v145, s[60:61] offset:1024\n"
        "ds_read2_b32 a[50:51], v226 offset0:50 offset1:51\n"
        "v_mfma_f32_16x16x32_bf16 v[136:139], a[16:19], a[200:203], v[136:139]\n"
        "ds_read_b128 a[240:243], v228 offset:1728\n"
        "s_waitcnt lgkmcnt(8)\n"
        "v_mfma_f32_16x16x32_bf16 v[140:143], a[16:19], a[204:207], v[140:143]\n"
        "ds_read2_b32 a[52:53], v226 offset0:176 offset1:177\n"
        "s_waitcnt lgkmcnt(8)\n"
        "v_mfma_f32_16x16x32_bf16 v[132:135], a[20:23], a[192:195], v[132:135]\n"
        "global_load_lds_dwordx4 v146, s[60:61] offset:2048\n"
        "ds_read2_b32 a[54:55], v226 offset0:178 offset1:179\n"
        "v_mfma_f32_16x16x32_bf16 v[136:139], a[20:23], a[196:199], v[136:139]\n"
        "ds_read_b128 a[244:247], v228 offset:1216\n"
        "v_add_u32 v230, 3840, v228\n"
        "v_mfma_f32_16x16x32_bf16 v[140:143], a[20:23], a[200:203], v[140:143]\n"
        "ds_read2_b32 a[56:57], v227 offset0:48 offset1:49\n"
        "v_min_u32 v230, s16, v230\n"
        "v_mfma_f32_16x16x32_bf16 v[136:139], a[24:27], a[192:195], v[136:139]\n"
        "global_load_lds_dwordx4 v147, s[60:61] offset:3072\n"
        "ds_read2_b32 a[58:59], v227 offset0:50 offset1:51\n"
        "s_add_u32 m0, s73, 0x10000\n"
        "v_mfma_f32_16x16x32_bf16 v[140:143], a[24:27], a[196:199], v[140:143]\n"
        "ds_read_b128 a[248:251], v228 offset:704\n"
        "s_add_u32 s60, s22, 0x140000\n"
        "s_addc_u32 s61, s23, 0\n"
        "v_mfma_f32_16x16x32_bf16 v[140:143], a[28:31], a[192:195], v[140:143]\n"
        "ds_read2_b32 a[60:61], v227 offset0:176 offset1:177\n"
        "s_waitcnt vmcnt(10)\n"
        "v_mfma_f32_16x16x32_bf16 v[96:99], a[32:35], a[224:227], v[96:99]\n"
        "ds_read2_b32 a[62:63], v227 offset0:178 offset1:179\n"
        "s_barrier\n"
        "s_waitcnt lgkmcnt(8)\n"
        "v_mfma_f32_16x16x32_bf16 v[100:103], a[32:35], a[228:231], v[100:103]\n"
        "ds_read_b128 a[252:255], v228 offset:192\n"
        "global_load_lds_dwordx4 v144, s[60:61] offset:0\n"
        "v_mfma_f32_16x16x32_bf16 v[104:107], a[32:35], a[232:235], v[104:107]\n"
        "ds_read_b64_tr_b16 v[160:161], v154 offset:0\n"
        "s_waitcnt lgkmcnt(8)\n"
        "v_mfma_f32_16x16x32_bf16 v[108:111], a[32:35], a[236:239], v[108:111]\n"
        "ds_read_b64_tr_b16 v[162:163], v154 offset:4096\n"
        "s_waitcnt lgkmcnt(8)\n"
        "v_mfma_f32_16x16x32_bf16 v[128:131], a[32:35], a[240:243], v[128:131]\n"
        "global_load_lds_dwordx4 v145, s[60:61] offset:1024\n"
        "v_mfma_f32_16x16x32_bf16 v[132:135], a[32:35], a[244:247], v[132:135]\n"
        "ds_read2_b32 a[0:1], v224 offset0:64 offset1:65\n"
        "s_waitcnt lgkmcnt(7)\n"
        "v_mfma_f32_16x16x32_bf16 v[100:103], a[36:39], a[224:227], v[100:103]\n"
        "ds_read2_b32 a[2:3], v224 offset0:66 offset1:67\n"
        "s_waitcnt lgkmcnt(7)\n"
        "v_mfma_f32_16x16x32_bf16 v[136:139], a[32:35], a[248:251], v[136:139]\n"
        "ds_read_b128 a[192:195], v230 offset:0\n"
        "global_load_lds_dwordx4 v146, s[60:61] offset:2048\n"
        "v_mfma_f32_16x16x32_bf16 v[104:107], a[36:39], a[228:231], v[104:107]\n"
        "ds_read_b64_tr_b16 v[164:165], v155 offset:0\n"
        "s_waitcnt lgkmcnt(7)\n"
        "v_mfma_f32_16x16x32_bf16 v[108:111], a[36:39], a[232:235], v[108:111]\n"
        "ds_read_b64_tr_b16 v[166:167], v155 offset:4096\n"
        "s_waitcnt lgkmcnt(7)\n"
        "v_mfma_f32_16x16x32_bf16 v[140:143], a[32:35], a[252:255], v[140:143]\n"
        "ds_read2_b32 a[4:5], v224 offset0:192 offset1:193\n"
        "global_load_lds_dwordx4 v147, s[60:61] offset:3072\n"
        "s_add_u32 m0, s73, 0x14000\n"
        "v_mfma_f32_16x16x32_bf16 v[128:131], a[36:39], a[236:239], v[128:131]\n"
        "ds_read2_b32 a[6:7], v224 offset0:194 offset1:195\n"
        "s_add_u32 s60, s22, 0x1a0000\n"
        "s_addc_u32 s61, s23, 0\n"
        "v_mfma_f32_16x16x32_bf16 v[132:135], a[36:39], a[240:243], v[132:135]\n"
        "ds_read_b128 a[196:199], v228 offset:3328\n"
        "v_mfma_f32_16x16x32_bf16 v[136:139], a[36:39], a[244:247], v[136:139]\n"
        "ds_read_b64_tr_b16 v[168:169], v154 offset:8192\n"
        "global_load_lds_dwordx4 v144, s[60:61] offset:0\n"
        "v_mfma_f32_16x16x32_bf16 v[140:143], a[36:39], a[248:251], v[140:143]\n"
        "ds_read_b64_tr_b16 v[170:171], v154 offset:12288\n"
        "v_mfma_f32_16x16x32_bf16 v[104:107], a[40:43], a[224:227], v[104:107]\n"
        "ds_read2_b32 a[8:9], v225 offset0:64 offset1:65\n"
        "s_waitcnt lgkmcnt(8)\n"
        "v_mfma_f32_16x16x32_bf16 v[108:111], a[40:43], a[228:231], v[108:111]\n"
        "ds_read2_b32 a[10:11], v225 offset0:66 offset1:67\n"
        "global_load_lds_dwordx4 v145, s[60:61] offset:1024\n"
        "v_mfma_f32_16x16x32_bf16 v[128:131], a[40:43], a[232:235], v[128:131]\n"
        "ds_read_b128 a[200:203], v228 offset:2816\n"
        "v_mfma_f32_16x16x32_bf16 v[132:135], a[40:43], a[236:239], v[132:135]\n"
        "ds_read_b64_tr_b16 v[172:173], v155 offset:8192\n"
        "v_mfma_f32_16x16x32_bf16 v[136:139], a[40:43], a[240:243], v[136:139]\n"
        "ds_read_b64_tr_b16 v[174:175], v155 offset:12288\n"
        "global_load_lds_dwordx4 v146, s[60:61] offset:2048\n"
        "v_mfma_f32_16x16x32_bf16 v[140:143], a[40:43], a[244:247], v[140:143]\n"
        "ds_read2_b32 a[12:13], v225 offset0:192 offset1:193\n"
        "v_mfma_f32_16x16x32_bf16 v[108:111], a[44:47], a[224:227], v[108:111]\n"
        "ds_read2_b32 a[14:15], v225 offset0:194 offset1:195\n"
        "s_waitcnt lgkmcnt(8)\n"
        "v_mfma_f32_16x16x32_bf16 v[128:131], a[44:47], a[228:231], v[128:131]\n"
        "ds_read_b128 a[204:207], v228 offset:2304\n"
        "global_load_lds_dwordx4 v147, s[60:61] offset:3072\n"
        "v_mfma_f32_16x16x32_bf16 v[132:135], a[44:47], a[232:235], v[132:135]\n"
        "ds_read_b64_tr_b16 v[176:177], v154 offset:16384\n"
        "s_lshl1_add_u32 s62, s12, s10\n"
        "s_lshl_b32 s62, s62, 6\n"
        "v_mfma_f32_16x16x32_bf16 v[136:139], a[44:47], a[236:239], v[136:139]\n"
        "ds_read_b64_tr_b16 v[178:179], v154 offset:20480\n"
        "s_lshl2_add_u32 s63, s11, s40\n"
        "s_lshl_b32 s63, s63, 5\n"
        "v_mfma_f32_16x16x32_bf16 v[140:143], a[44:47], a[240:243], v[140:143]\n"
        "ds_read2_b32 a[16:17], v226 offset0:64 offset1:65\n"
        "s_lshl_b32 s14, s62, 11\n"
        "s_lshl1_add_u32 s14, s63, s14\n"
        "v_mfma_f32_16x16x32_bf16 v[128:131], a[48:51], a[224:227], v[128:131]\n"
        "ds_read2_b32 a[18:19], v226 offset0:66 offset1:67\n"
        "s_add_u32 s64, s8, s14\n"
        "s_addc_u32 s65, s9, 0\n"
        "v_mfma_f32_16x16x32_bf16 v[132:135], a[48:51], a[228:231], v[132:135]\n"
        "ds_read_b128 a[208:211], v228 offset:1792\n"
        "s_add_u32 s56, s36, s14\n"
        "s_addc_u32 s57, s37, 0\n"
        "v_mfma_f32_16x16x32_bf16 v[136:139], a[48:51], a[232:235], v[136:139]\n"
        "ds_read_b64_tr_b16 v[180:181], v155 offset:16384\n"
        "s_waitcnt lgkmcnt(8)\n"
        "v_mfma_f32_16x16x32_bf16 v[140:143], a[48:51], a[236:239], v[140:143]\n"
        "ds_read_b64_tr_b16 v[182:183], v155 offset:20480\n"
        "v_mfma_f32_16x16x32_bf16 v[132:135], a[52:55], a[224:227], v[132:135]\n"
        "ds_read2_b32 a[20:21], v226 offset0:192 offset1:193\n"
        "s_waitcnt lgkmcnt(8)\n"
        "v_mfma_f32_16x16x32_bf16 v[136:139], a[52:55], a[228:231], v[136:139]\n"
        "ds_read2_b32 a[22:23], v226 offset0:194 offset1:195\n"
        "v_mfma_f32_16x16x32_bf16 v[140:143], a[52:55], a[232:235], v[140:143]\n"
        "ds_read_b128 a[212:215], v228 offset:1280\n"
        "v_mfma_f32_16x16x32_bf16 v[136:139], a[56:59], a[224:227], v[136:139]\n"
        "ds_read_b64_tr_b16 v[184:185], v154 offset:24576\n"
        "s_add_u32 m0, s73, 0x0\n"
        "v_mfma_f32_16x16x32_bf16 v[140:143], a[56:59], a[228:231], v[140:143]\n"
        "ds_read_b64_tr_b16 v[186:187], v154 offset:28672\n"
        "s_add_u32 s60, s22, 0x200000\n"
        "s_addc_u32 s61, s23, 0\n"
        "v_mfma_f32_16x16x32_bf16 v[140:143], a[60:63], a[224:227], v[140:143]\n"
        "ds_read_b64_tr_b16 v[188:189], v155 offset:24576\n"
        "v_add_u32 v231, 3904, v228\n"
        "v_mfma_f32_16x16x32_bf16 v[96:99], a[0:3], a[192:195], v[96:99]\n"
        "ds_read_b64_tr_b16 v[190:191], v155 offset:28672\n"
        "v_min_u32 v231, s16, v231\n"
        "v_mfma_f32_16x16x32_bf16 v[100:103], a[0:3], a[196:199], v[100:103]\n"
        "ds_read2_b32 a[24:25], v227 offset0:64 offset1:65\n"
        "s_waitcnt lgkmcnt(8)\n"
        "v_mfma_f32_16x16x32_bf16 v[104:107], a[0:3], a[200:203], v[104:107]\n"
        "ds_read2_b32 a[26:27], v227 offset0:66 offset1:67\n"
        "v_mfma_f32_16x16x32_bf16 v[108:111], a[0:3], a[204:207], v[108:111]\n"
        "ds_read_b128 a[216:219], v228 offset:768\n"
        "s_waitcnt lgkmcnt(8)\n"
        "v_mfma_f32_16x16x32_bf16 v[128:131], a[0:3], a[208:211], v[128:131]\n"
        "ds_read2_b32 a[28:29], v227 offset0:192 offset1:193\n"
        "s_waitcnt lgkmcnt(8)\n"
        "v_mfma_f32_16x16x32_bf16 v[132:135], a[0:3], a[212:215], v[132:135]\n"
        "ds_read2_b32 a[30:31], v227 offset0:194 offset1:195\n"
        "v_mfma_f32_16x16x32_bf16 v[100:103], a[4:7], a[192:195], v[100:103]\n"
        "ds_read_b128 a[220:223], v228 offset:256\n"
        "v_mfma_f32_16x16x32_bf16 v[104:107], a[4:7], a[196:199], v[104:107]\n"
        "ds_read2_b32 a[32:33], v224 offset0:80 offset1:81\n"
        "v_mfma_f32_16x16x32_bf16 v[108:111], a[4:7], a[200:203], v[108:111]\n"
        "s_waitcnt vmcnt(8) lgkmcnt(7)\n"
        "s_barrier\n"
        "v_mfma_f32_16x16x32_bf16 v[128:131], a[4:7], a[204:207], v[128:131]\n"
        "global_load_lds_dwordx4 v144, s[60:61] offset:0\n"
        "ds_read_b64_tr_b16 v[192:193], v154 offset:32768\n"
        "v_mfma_f32_16x16x32_bf16 v[132:135], a[4:7], a[208:211], v[132:135]\n"
        "ds_read_b64_tr_b16 v[194:195], v154 offset:36864\n"
        "s_waitcnt lgkmcnt(7)\n"
        "v_mfma_f32_16x16x32_bf16 v[104:107], a[8:11], a[192:195], v[104:107]\n"
        "ds_read2_b32 a[34:35], v224 offset0:82 offset1:83\n"
        "s_waitcnt lgkmcnt(7)\n"
        "v_mfma_f32_16x16x32_bf16 v[136:139], a[0:3], a[216:219], v[136:139]\n"
        "global_load_lds_dwordx4 v145, s[60:61] offset:1024\n"
        "ds_read_b128 a[224:227], v231 offset:0\n"
        "v_mfma_f32_16x16x32_bf16 v[136:139], a[4:7], a[212:215], v[136:139]\n"
        "ds_read_b64_tr_b16 v[196:197], v155 offset:32768\n"
        "s_waitcnt lgkmcnt(7)\n"
        "v_mfma_f32_16x16x32_bf16 v[108:111], a[8:11], a[196:199], v[108:111]\n"
        "ds_read_b64_tr_b16 v[198:199], v155 offset:36864\n"
        "s_waitcnt lgkmcnt(7)\n"
        "v_mfma_f32_16x16x32_bf16 v[140:143], a[0:3], a[220:223], v[140:143]\n"
        "global_load_lds_dwordx4 v146, s[60:61] offset:2048\n"
        "ds_read2_b32 a[36:37], v224 offset0:208 offset1:209\n"
        "v_mfma_f32_16x16x32_bf16 v[140:143], a[4:7], a[216:219], v[140:143]\n"
        "ds_read2_b32 a[38:39], v224 offset0:210 offset1:211\n"
        "v_mfma_f32_16x16x32_bf16 v[128:131], a[8:11], a[200:203], v[128:131]\n"
        "ds_read_b128 a[228:231], v228 offset:3392\n"
        "v_mfma_f32_16x16x32_bf16 v[132:135], a[8:11], a[204:207], v[132:135]\n"
        "global_load_lds_dwordx4 v147, s[60:61] offset:3072\n"
        "ds_read_b64_tr_b16 v[200:201], v154 offset:40960\n"
        "s_add_u32 m0, s73, 0x4000\n"
        "v_mfma_f32_16x16x32_bf16 v[136:139], a[8:11], a[208:211], v[136:139]\n"
        "ds_read_b64_tr_b16 v[202:203], v154 offset:45056\n"
        "s_add_u32 s60, s22, 0x240000\n"
        "s_addc_u32 s61, s23, 0\n"
        "v_mfma_f32_16x16x32_bf16 v[140:143], a[8:11], a[212:215], v[140:143]\n"
        "ds_read2_b32 a[40:41], v225 offset0:80 offset1:81\n"
        "s_waitcnt lgkmcnt(8)\n"
        "v_mfma_f32_16x16x32_bf16 v[108:111], a[12:15], a[192:195], v[108:111]\n"
        "ds_read2_b32 a[42:43], v225 offset0:82 offset1:83\n"
        "global_load_lds_dwordx4 v144, s[60:61] offset:0\n"
        "v_mfma_f32_16x16x32_bf16 v[128:131], a[12:15], a[196:199], v[128:131]\n"
        "ds_read_b128 a[232:235], v228 offset:2880\n"
        "v_mfma_f32_16x16x32_bf16 v[132:135], a[12:15], a[200:203], v[132:135]\n"
        "ds_read_b64_tr_b16 v[204:205], v155 offset:40960\n"
        "v_mfma_f32_16x16x32_bf16 v[136:139], a[12:15], a[204:207], v[136:139]\n"
        "ds_read_b64_tr_b16 v[206:207], v155 offset:45056\n"
        "global_load_lds_dwordx4 v145, s[60:61] offset:1024\n"
        "v_mfma_f32_16x16x32_bf16 v[140:143], a[12:15], a[208:211], v[140:143]\n"
        "ds_read2_b32 a[44:45], v225 offset0:208 offset1:209\n"
        "s_waitcnt lgkmcnt(8)\n"
        "v_mfma_f32_16x16x32_bf16 v[128:131], a[16:19], a[192:195], v[128:131]\n"
        "ds_read2_b32 a[46:47], v225 offset0:210 offset1:211\n"
        "v_mfma_f32_16x16x32_bf16 v[132:135], a[16:19], a[196:199], v[132:135]\n"
        "ds_read_b128 a[236:239], v228 offset:2368\n"
        "global_load_lds_dwordx4 v146, s[60:61] offset:2048\n"
        "v_mfma_f32_16x16x32_bf16 v[136:139], a[16:19], a[200:203], v[136:139]\n"
        "ds_read_b64_tr_b16 v[208:209], v154 offset:49152\n"
        "v_mfma_f32_16x16x32_bf16 v[140:143], a[16:19], a[204:207], v[140:143]\n"
        "ds_read_b64_tr_b16 v[210:211], v154 offset:53248\n"
        "s_waitcnt lgkmcnt(8)\n"
        "v_mfma_f32_16x16x32_bf16 v[132:135], a[20:23], a[192:195], v[132:135]\n"
        "ds_read2_b32 a[48:49], v226 offset0:80 offset1:81\n"
        "global_load_lds_dwordx4 v147, s[60:61] offset:3072\n"
        "v_mfma_f32_16x16x32_bf16 v[136:139], a[20:23], a[196:199], v[136:139]\n"
        "ds_read2_b32 a[50:51], v226 offset0:82 offset1:83\n"
        "s_waitcnt lgkmcnt(8)\n"
        "v_mfma_f32_16x16x32_bf16 v[140:143], a[20:23], a[200:203], v[140:143]\n"
        "ds_read_b128 a[240:243], v228 offset:1856\n"
        "v_mfma_f32_16x16x32_bf16 v[136:139], a[24:27], a[192:195], v[136:139]\n"
        "ds_read_b64_tr_b16 v[212:213], v155 offset:49152\n"
        "v_mfma_f32_16x16x32_bf16 v[140:143], a[24:27], a[196:199], v[140:143]\n"
        "ds_read_b64_tr_b16 v[214:215], v155 offset:53248\n"
        "s_waitcnt lgkmcnt(8)\n"
        "v_mfma_f32_16x16x32_bf16 v[140:143], a[28:31], a[192:195], v[140:143]\n"
        "ds_read2_b32 a[52:53], v226 offset0:208 offset1:209\n"
        "s_waitcnt lgkmcnt(8)\n"
        "v_mfma_f32_16x16x32_bf16 v[96:99], a[32:35], a[224:227], v[96:99]\n"
        "ds_read2_b32 a[54:55], v226 offset0:210 offset1:211\n"
        "v_mfma_f32_16x16x32_bf16 v[100:103], a[32:35], a[228:231], v[100:103]\n"
        "ds_read_b128 a[244:247], v228 offset:1344\n"
        "v_mfma_f32_16x16x32_bf16 v[104:107], a[32:35], a[232:235], v[104:107]\n"
        "ds_read_b64_tr_b16 v[216:217], v154 offset:57344\n"
        "s_add_u32 m0, s73, 0x8000\n"
        "v_mfma_f32_16x16x32_bf16 v[108:111], a[32:35], a[236:239], v[108:111]\n"
        "ds_read_b64_tr_b16 v[218:219], v154 offset:61440\n"
        "s_add_u32 s60, s22, 0x2a0000\n"
        "s_addc_u32 s61, s23, 0\n"
        "v_mfma_f32_16x16x32_bf16 v[100:103], a[36:39], a[224:227], v[100:103]\n"
        "ds_read_b64_tr_b16 v[220:221], v155 offset:57344\n"
        "s_waitcnt lgkmcnt(8)\n"
        "v_mfma_f32_16x16x32_bf16 v[128:131], a[32:35], a[240:243], v[128:131]\n"
        "ds_read_b64_tr_b16 v[222:223], v155 offset:61440\n"
        "s_lshl1_add_u32 s14, s12, s10\n"
        "v_add_u32 v230, 3968, v228\n"
        "v_mfma_f32_16x16x32_bf16 v[104:107], a[36:39], a[228:231], v[104:107]\n"
        "ds_read2_b32 a[56:57], v227 offset0:80 offset1:81\n"
        "s_lshl_b32 s14, s14, 6\n"
        "v_min_u32 v230, s16, v230\n"
        "v_mfma_f32_16x16x32_bf16 v[108:111], a[36:39], a[232:235], v[108:111]\n"
        "ds_read2_b32 a[58:59], v227 offset0:82 offset1:83\n"
        "s_add_u32 s14, s14, 1024\n"
        "v_lshlrev_b32 v238, 3, v254\n"
        "v_mfma_f32_16x16x32_bf16 v[128:131], a[36:39], a[236:239], v[128:131]\n"
        "ds_read_b128 a[248:251], v228 offset:832\n"
        "s_lshl_b32 s14, s14, 7\n"
        "v_add_u32 v238, s14, v238\n"
        "v_mfma_f32_16x16x32_bf16 v[104:107], a[40:43], a[224:227], v[104:107]\n"
        "ds_read2_b32 a[60:61], v227 offset0:208 offset1:209\n"
        "s_waitcnt lgkmcnt(8)\n"
        "v_mfma_f32_16x16x32_bf16 v[132:135], a[32:35], a[244:247], v[132:135]\n"
        "ds_read2_b32 a[62:63], v227 offset0:210 offset1:211\n"
        "v_mfma_f32_16x16x32_bf16 v[132:135], a[36:39], a[240:243], v[132:135]\n"
        "ds_read_b128 a[252:255], v228 offset:320\n"
        "v_mfma_f32_16x16x32_bf16 v[108:111], a[40:43], a[228:231], v[108:111]\n"
        "ds_read2_b32 a[0:1], v224 offset0:96 offset1:97\n"
        "v_mfma_f32_16x16x32_bf16 v[128:131], a[40:43], a[232:235], v[128:131]\n"
        "s_waitcnt vmcnt(8) lgkmcnt(7)\n"
        "s_barrier\n"
        "v_mfma_f32_16x16x32_bf16 v[132:135], a[40:43], a[236:239], v[132:135]\n"
        "global_load_lds_dwordx4 v144, s[60:61] offset:0\n"
        "ds_read_b64_tr_b16 a[64:65], v156 offset:0\n"
        "v_mfma_f32_16x16x32_bf16 v[108:111], a[44:47], a[224:227], v[108:111]\n"
        "ds_read_b64_tr_b16 a[66:67], v156 offset:4096\n"
        "s_waitcnt lgkmcnt(7)\n"
        "v_mfma_f32_16x16x32_bf16 v[128:131], a[44:47], a[228:231], v[128:131]\n"
        "ds_read2_b32 a[2:3], v224 offset0:98 offset1:99\n"
        "s_waitcnt lgkmcnt(7)\n"
        "v_mfma_f32_16x16x32_bf16 v[136:139], a[32:35], a[248:251], v[136:139]\n"
        "global_load_lds_dwordx4 v145, s[60:61] offset:1024\n"
        "ds_read_b128 a[192:195], v230 offset:0\n"
        "v_mfma_f32_16x16x32_bf16 v[136:139], a[36:39], a[244:247], v[136:139]\n"
        "ds_read_b64_tr_b16 a[68:69], v157 offset:0\n"
        "s_waitcnt lgkmcnt(7)\n"
        "v_mfma_f32_16x16x32_bf16 v[136:139], a[40:43], a[240:243], v[136:139]\n"
        "ds_read_b64_tr_b16 a[70:71], v157 offset:4096\n"
        "s_waitcnt lgkmcnt(7)\n"
        "v_mfma_f32_16x16x32_bf16 v[140:143], a[32:35], a[252:255], v[140:143]\n"
        "global_load_lds_dwordx4 v146, s[60:61] offset:2048\n"
        "ds_read2_b32 a[4:5], v224 offset0:224 offset1:225\n"
        "v_mfma_f32_16x16x32_bf16 v[140:143], a[36:39], a[248:251], v[140:143]\n"
        "ds_read2_b32 a[6:7], v224 offset0:226 offset1:227\n"
        "v_mfma_f32_16x16x32_bf16 v[140:143], a[40:43], a[244:247], v[140:143]\n"
        "ds_read_b128 a[196:199], v228 offset:3456\n"
        "v_mfma_f32_16x16x32_bf16 v[140:143], a[44:47], a[240:243], v[140:143]\n"
        "global_load_lds_dwordx4 v147, s[60:61] offset:3072\n"
        "ds_read_b64_tr_b16 a[72:73], v156 offset:8192\n"
        "s_add_u32 m0, s73, 0xc000\n"
        "v_mfma_f32_16x16x32_bf16 v[140:143], a[48:51], a[236:239], v[140:143]\n"
        "ds_read_b64_tr_b16 a[74:75], v156 offset:12288\n"
        "s_add_u32 s60, s22, 0x300000\n"
        "s_addc_u32 s61, s23, 0\n"
        "v_mfma_f32_16x16x32_bf16 v[140:143], a[52:55], a[232:235], v[140:143]\n"
        "ds_read2_b32 a[8:9], v225 offset0:96 offset1:97\n"
        "s_waitcnt lgkmcnt(8)\n"
        "v_mfma_f32_16x16x32_bf16 v[140:143], a[56:59], a[228:231], v[140:143]\n"
        "ds_read2_b32 a[10:11], v225 offset0:98 offset1:99\n"
        "global_load_lds_dwordx4 v144, s[60:61] offset:0\n"
        "v_mfma_f32_16x16x32_bf16 v[140:143], a[60:63], a[224:227], v[140:143]\n"
        "ds_read_b128 a[200:203], v228 offset:2944\n"
        "v_mfma_f32_16x16x32_bf16 v[132:135], a[44:47], a[232:235], v[132:135]\n"
        "ds_read_b64_tr_b16 a[76:77], v157 offset:8192\n"
        "v_mfma_f32_16x16x32_bf16 v[136:139], a[44:47], a[236:239], v[136:139]\n"
        "global_load_dwordx2 v[236:237], v238, s[48:49] sc1\n"
        "v_mfma_f32_16x16x32_bf16 v[128:131], a[48:51], a[224:227], v[128:131]\n"
        "ds_read_b64_tr_b16 a[78:79], v157 offset:12288\n"
        "global_load_lds_dwordx4 v145, s[60:61] offset:1024\n"
        "v_mfma_f32_16x16x32_bf16 v[132:135], a[48:51], a[228:231], v[132:135]\n"
        "ds_read2_b32 a[12:13], v225 offset0:224 offset1:225\n"
        "s_waitcnt lgkmcnt(7)\n"
        "v_mfma_f32_16x16x32_bf16 v[136:139], a[48:51], a[232:235], v[136:139]\n"
        "ds_read2_b32 a[14:15], v225 offset0:226 offset1:227\n"
        "v_mfma_f32_16x16x32_bf16 v[132:135], a[52:55], a[224:227], v[132:135]\n"
        "ds_read_b128 a[204:207], v228 offset:2432\n"
        "global_load_lds_dwordx4 v146, s[60:61] offset:2048\n"
        "v_mfma_f32_16x16x32_bf16 v[136:139], a[52:55], a[228:231], v[136:139]\n"
        "ds_read_b64_tr_b16 a[80:81], v156 offset:16384\n"
        "s_waitcnt lgkmcnt(7)\n"
        "v_mfma_f32_16x16x32_bf16 v[136:139], a[56:59], a[224:227], v[136:139]\n"
        "ds_read_b64_tr_b16 a[82:83], v156 offset:20480\n"
        "s_waitcnt lgkmcnt(7)\n"
        "v_mfma_f32_16x16x32_bf16 v[96:99], a[0:3], a[192:195], v[96:99]\n"
        "ds_read2_b32 a[16:17], v226 offset0:96 offset1:97\n"
        "global_load_lds_dwordx4 v147, s[60:61] offset:3072\n"
        "v_mfma_f32_16x16x32_bf16 v[100:103], a[0:3], a[196:199], v[100:103]\n"
        "ds_read2_b32 a[18:19], v226 offset0:98 offset1:99\n"
        "v_mfma_f32_16x16x32_bf16 v[104:107], a[0:3], a[200:203], v[104:107]\n"
        "ds_read_b128 a[208:211], v228 offset:1920\n"
        "v_mfma_f32_16x16x32_bf16 v[100:103], a[4:7], a[192:195], v[100:103]\n"
        "ds_read_b64_tr_b16 a[84:85], v157 offset:16384\n"
        "v_mfma_f32_16x16x32_bf16 v[104:107], a[4:7], a[196:199], v[104:107]\n"
        "ds_read_b64_tr_b16 a[86:87], v157 offset:20480\n"
        "s_waitcnt lgkmcnt(8)\n"
        "v_mfma_f32_16x16x32_bf16 v[104:107], a[8:11], a[192:195], v[104:107]\n"
        "ds_read_b64_tr_b16 a[88:89], v156 offset:24576\n"
        "s_waitcnt lgkmcnt(8)\n"
        "v_mfma_f32_16x16x32_bf16 v[108:111], a[0:3], a[204:207], v[108:111]\n"
        "ds_read_b64_tr_b16 a[90:91], v156 offset:28672\n"
        "v_mfma_f32_16x16x32_bf16 v[108:111], a[4:7], a[200:203], v[108:111]\n"
        "ds_read_b64_tr_b16 a[92:93], v157 offset:24576\n"
        "v_mfma_f32_16x16x32_bf16 v[108:111], a[8:11], a[196:199], v[108:111]\n"
        "ds_read_b64_tr_b16 a[94:95], v157 offset:28672\n"
        "s_add_u32 m0, s73, 0x10000\n"
        "ds_read2_b32 a[20:21], v226 offset0:224 offset1:225\n"
        "ds_read2_b32 a[22:23], v226 offset0:226 offset1:227\n"
        "ds_read_b128 a[212:215], v228 offset:1408\n"
        "s_waitcnt lgkmcnt(9)\n"
        "v_mfma_f32_16x16x32_bf16 v[128:131], a[0:3], a[208:211], v[128:131]\n"
        "ds_read2_b32 a[24:25], v227 offset0:96 offset1:97\n"
        "s_add_u32 s60, s22, 0x340000\n"
        "s_addc_u32 s61, s23, 0\n"
        "v_mfma_f32_16x16x32_bf16 v[128:131], a[4:7], a[204:207], v[128:131]\n"
        "ds_read2_b32 a[26:27], v227 offset0:98 offset1:99\n"
        "v_add_u32 v231, 4032, v228\n"
        "v_mfma_f32_16x16x32_bf16 v[128:131], a[8:11], a[200:203], v[128:131]\n"
        "ds_read_b128 a[216:219], v228 offset:896\n"
        "v_min_u32 v231, s16, v231\n"
        "v_mfma_f32_16x16x32_bf16 v[108:111], a[12:15], a[192:195], v[108:111]\n"
        "ds_read_b128 a[220:223], v228 offset:384\n"
        "s_add_u32 s14, s51, 504\n"
        "v_lshlrev_b32 v229, 5, v232\n"
        "v_mfma_f32_16x16x32_bf16 v[128:131], a[12:15], a[196:199], v[128:131]\n"
        "ds_read2_b32 a[28:29], v227 offset0:224 offset1:225\n"
        "v_lshl_add_u32 v229, v233, 3, v229\n"
        "ds_read2_b32 a[30:31], v227 offset0:226 offset1:227\n"
        "s_waitcnt vmcnt(9) lgkmcnt(9)\n"
        "s_barrier\n"
        "ds_read_b64_tr_b16 a[96:97], v154 offset:0\n"
        "ds_read_b64_tr_b16 a[98:99], v154 offset:4096\n"
        "s_waitcnt lgkmcnt(8)\n"
        "v_mfma_f32_16x16x32_bf16 v[132:135], a[0:3], a[212:215], v[132:135]\n"
        "ds_read_b64_tr_b16 a[100:101], v155 offset:0\n"
        "v_mfma_f32_16x16x32_bf16 v[132:135], a[4:7], a[208:211], v[132:135]\n"
        "ds_read_b64_tr_b16 a[102:103], v155 offset:4096\n"
        "s_waitcnt lgkmcnt(9)\n"
        "v_mfma_f32_16x16x32_bf16 v[132:135], a[8:11], a[204:207], v[132:135]\n"
        "ds_read_b64_tr_b16 a[104:105], v154 offset:8192\n"
        "s_waitcnt lgkmcnt(8)\n"
        "v_mfma_f32_16x16x32_bf16 v[136:139], a[0:3], a[216:219], v[136:139]\n"
        "ds_read_b64_tr_b16 a[106:107], v154 offset:12288\n"
        "s_waitcnt lgkmcnt(8)\n"
        "v_mfma_f32_16x16x32_bf16 v[140:143], a[0:3], a[220:223], v[140:143]\n"
        "ds_read_b64_tr_b16 a[108:109], v155 offset:8192\n"
        "global_load_lds_dwordx4 v144, s[60:61] offset:0\n"
        "v_mfma_f32_16x16x32_bf16 v[140:143], a[4:7], a[216:219], v[140:143]\n"
        "ds_read_b64_tr_b16 a[110:111], v155 offset:12288\n"
        "v_mfma_f32_16x16x32_bf16 v[140:143], a[8:11], a[212:215], v[140:143]\n"
        "ds_read_b64_tr_b16 a[112:113], v154 offset:16384\n"
        "v_mfma_f32_16x16x32_bf16 v[140:143], a[12:15], a[208:211], v[140:143]\n"
        "ds_read_b64_tr_b16 a[114:115], v154 offset:20480\n"
        "global_load_lds_dwordx4 v145, s[60:61] offset:1024\n"
        "v_mfma_f32_16x16x32_bf16 v[140:143], a[16:19], a[204:207], v[140:143]\n"
        "ds_read_b64_tr_b16 a[116:117], v155 offset:16384\n"
        "v_mfma_f32_16x16x32_bf16 v[140:143], a[20:23], a[200:203], v[140:143]\n"
        "ds_read_b64_tr_b16 a[118:119], v155 offset:20480\n"
        "v_mfma_f32_16x16x32_bf16 v[140:143], a[24:27], a[196:199], v[140:143]\n"
        "ds_read_b64_tr_b16 a[120:121], v154 offset:24576\n"
        "s_waitcnt lgkmcnt(8)\n"
        "v_mfma_f32_16x16x32_bf16 v[140:143], a[28:31], a[192:195], v[140:143]\n"
        "ds_read_b64_tr_b16 a[122:123], v154 offset:28672\n"
        "global_load_lds_dwordx4 v146, s[60:61] offset:2048\n"
        "v_mfma_f32_16x16x32_bf16 v[136:139], a[4:7], a[212:215], v[136:139]\n"
        "ds_read_b64_tr_b16 a[124:125], v155 offset:24576\n"
        "v_sub_u32 v229, s14, v229\n"
        "s_waitcnt vmcnt(6) lgkmcnt(8)\n"
        "v_cmp_ne_u64 vcc, v[236:237], v[250:251]\n"
        "v_mfma_f32_16x16x32_bf16 v[136:139], a[8:11], a[208:211], v[136:139]\n"
        "ds_read_b64_tr_b16 a[126:127], v155 offset:28672\n"
        "s_cbranch_vccnz LPCX_%=\n"
        "LBCX_%=:\n"
        "v_mfma_f32_16x16x32_bf16 v[132:135], a[12:15], a[200:203], v[132:135]\n"
        "global_load_lds_dwordx4 v147, s[60:61] offset:3072\n"
        "s_add_u32 m0, s73, 0x14000\n"
        "v_mfma_f32_16x16x32_bf16 v[136:139], a[12:15], a[204:207], v[136:139]\n"
        "s_add_u32 s60, s22, 0x3a0000\n"
        "s_addc_u32 s61, s23, 0\n"
        "v_mfma_f32_16x16x32_bf16 v[128:131], a[16:19], a[192:195], v[128:131]\n"
        "ds_read2_b32 a[32:33], v224 offset0:112 offset1:113\n"
        "v_mfma_f32_16x16x32_bf16 v[132:135], a[16:19], a[196:199], v[132:135]\n"
        "global_load_lds_dwordx4 v144, s[60:61] offset:0\n"
        "ds_read2_b32 a[34:35], v224 offset0:114 offset1:115\n"
        "v_mfma_f32_16x16x32_bf16 v[136:139], a[16:19], a[200:203], v[136:139]\n"
        "ds_read_b128 a[224:227], v231 offset:0\n"
        "v_mfma_f32_16x16x32_bf16 v[132:135], a[20:23], a[192:195], v[132:135]\n"
        "ds_read2_b32 a[36:37], v224 offset0:240 offset1:241\n"
        "v_mfma_f32_16x16x32_bf16 v[136:139], a[20:23], a[196:199], v[136:139]\n"
        "global_load_lds_dwordx4 v145, s[60:61] offset:1024\n"
        "ds_read2_b32 a[38:39], v224 offset0:242 offset1:243\n"
        "v_mfma_f32_16x16x32_bf16 v[136:139], a[24:27], a[192:195], v[136:139]\n"
        "s_waitcnt vmcnt(6) lgkmcnt(5)\n"
        "global_load_lds_dwordx4 v146, s[60:61] offset:2048\n"
        "s_barrier\n"
        "ds_read_b128 a[228:231], v228 offset:3520\n"
        "ds_read_b128 a[252:255], v228 offset:448\n"
        "ds_read_b128 a[248:251], v228 offset:960\n"
        "ds_read2_b32 a[40:41], v225 offset0:112 offset1:113\n"
        "s_waitcnt lgkmcnt(6)\n"
        "global_load_lds_dwordx4 v147, s[60:61] offset:3072\n"
        "s_add_u32 m0, s73, 0x0\n"
        "v_mfma_f32_16x16x32_bf16 v[96:99], a[32:35], a[224:227], v[96:99]\n"
        "global_load_lds_dwordx4 v254, s[20:21] offset:0 sc1\n"
        "ds_read2_b32 a[42:43], v225 offset0:114 offset1:115\n"
        "ds_read_b128 a[244:247], v228 offset:1472\n"
        "ds_read_b128 a[240:243], v228 offset:1984\n"
        "ds_read2_b32 a[60:61], v227 offset0:240 offset1:241\n"
        "s_waitcnt lgkmcnt(7)\n"
        "v_mfma_f32_16x16x32_bf16 v[100:103], a[32:35], a[228:231], v[100:103]\n"
        "s_waitcnt lgkmcnt(5)\n"
        "global_load_lds_dwordx4 v254, s[20:21] offset:1024 sc1\n"
        "v_mfma_f32_16x16x32_bf16 v[136:139], a[32:35], a[248:251], v[136:139]\n"
        "ds_read2_b32 a[62:63], v227 offset0:242 offset1:243\n"
        "ds_read2_b32 a[44:45], v225 offset0:240 offset1:241\n"
        "ds_read2_b32 a[46:47], v225 offset0:242 offset1:243\n"
        "ds_read_b128 a[236:239], v228 offset:2496\n"
        "ds_read_b128 a[232:235], v228 offset:3008\n"
        "global_load_lds_dwordx4 v254, s[20:21] offset:2048 sc1\n"
        "ds_read2_b32 a[48:49], v226 offset0:112 offset1:113\n"
        "ds_read2_b32 a[50:51], v226 offset0:114 offset1:115\n"
        "ds_read2_b32 a[52:53], v226 offset0:240 offset1:241\n"
        "ds_read2_b32 a[54:55], v226 offset0:242 offset1:243\n"
        "ds_read2_b32 a[56:57], v227 offset0:112 offset1:113\n"
        "s_waitcnt lgkmcnt(12)\n"
        "global_load_lds_dwordx4 v254, s[20:21] offset:3072 sc1\n"
        "s_waitcnt lgkmcnt(11)\n"
        "v_mfma_f32_16x16x32_bf16 v[128:131], a[32:35], a[240:243], v[128:131]\n"
        "ds_read2_b32 a[58:59], v227 offset0:114 offset1:115\n"
        "s_add_u32 s60, s22, 0x20000\n"
        "s_addc_u32 s61, s23, 0\n"
        "v_mfma_f32_16x16x32_bf16 v[132:135], a[32:35], a[244:247], v[132:135]\n"
        "ds_read_b64 v[64:65], v229 offset:3584\n"
        "s_add_u32 m0, s73, 0x4000\n"
        "ds_read_b64 v[66:67], v229 offset:3072\n"
        "ds_read_b64 v[68:69], v229 offset:2560\n"
        "ds_read_b64 v[70:71], v229 offset:2048\n"
        "ds_read_b64 v[72:73], v229 offset:1536\n"
        "s_waitcnt lgkmcnt(12)\n"
        "s_waitcnt lgkmcnt(11)\n"
        "v_mfma_f32_16x16x32_bf16 v[104:107], a[32:35], a[232:235], v[104:107]\n"
        "ds_read_b64 v[74:75], v229 offset:1024\n"
        "global_load_lds_dwordx4 v144, s[60:61] offset:0\n"
        "v_mfma_f32_16x16x32_bf16 v[108:111], a[32:35], a[236:239], v[108:111]\n"
        "ds_read_b64 v[76:77], v229 offset:512\n"
        "s_add_u32 s20, s20, 0x4000\n"
        "s_addc_u32 s21, s21, 0\n"
        "ds_read_b64 v[78:79], v229 offset:0\n"
        "v_mfma_f32_16x16x32_bf16 v[140:143], a[32:35], a[252:255], v[140:143]\n"
        "ds_read_b64_tr_b16 a[128:129], v154 offset:32768\n"
        "v_mfma_f32_16x16x32_bf16 v[100:103], a[36:39], a[224:227], v[100:103]\n"
        "global_load_lds_dwordx4 v145, s[60:61] offset:1024\n"
        "ds_read_b64_tr_b16 a[130:131], v154 offset:36864\n"
        "v_mfma_f32_16x16x32_bf16 v[104:107], a[36:39], a[228:231], v[104:107]\n"
        "ds_read_b64_tr_b16 a[132:133], v155 offset:32768\n"
        "s_waitcnt lgkmcnt(10)\n"
        "v_mfma_f32_16x16x32_bf16 v[108:111], a[36:39], a[232:235], v[108:111]\n"
        "ds_read_b64_tr_b16 a[134:135], v155 offset:36864\n"
        "s_waitcnt lgkmcnt(9)\n"
        "v_mfma_f32_16x16x32_bf16 v[128:131], a[36:39], a[236:239], v[128:131]\n"
        "global_load_lds_dwordx4 v146, s[60:61] offset:2048\n"
        "ds_read_b64_tr_b16 a[136:137], v154 offset:40960\n"
        "v_mfma_f32_16x16x32_bf16 v[132:135], a[36:39], a[240:243], v[132:135]\n"
        "ds_read_b64_tr_b16 a[138:139], v154 offset:45056\n"
        "v_mfma_f32_16x16x32_bf16 v[136:139], a[36:39], a[244:247], v[136:139]\n"
        "ds_read_b64_tr_b16 a[140:141], v155 offset:40960\n"
        "s_add_u32 s24, s94, 0x400000\n"
        "s_addc_u32 s25, s95, 0\n"
        "v_mfma_f32_16x16x32_bf16 v[140:143], a[36:39], a[248:251], v[140:143]\n"
        "global_load_lds_dwordx4 v147, s[60:61] offset:3072\n"
        "s_waitcnt lgkmcnt(7)\n"
        "s_add_u32 m0, s73, 0x18000\n"
        "s_barrier\n"
        "v_mfma_f32_16x16x32_bf16 v[104:107], a[40:43], a[224:227], v[104:107]\n"
        "ds_read_b64_tr_b16 a[142:143], v155 offset:45056\n"
        "s_add_u32 s60, s22, 0x60000\n"
        "s_addc_u32 s61, s23, 0\n"
        "v_mfma_f32_16x16x32_bf16 v[108:111], a[40:43], a[228:231], v[108:111]\n"
        "ds_read_b64_tr_b16 a[144:145], v154 offset:49152\n"
        "v_mfma_f32_16x16x32_bf16 v[128:131], a[40:43], a[232:235], v[128:131]\n"
        "global_load_lds_dwordx4 v254, s[20:21] offset:0 sc1\n"
        "ds_read_b64_tr_b16 a[146:147], v154 offset:53248\n"
        "v_mfma_f32_16x16x32_bf16 v[132:135], a[40:43], a[236:239], v[132:135]\n"
        "ds_read_b64_tr_b16 a[148:149], v155 offset:49152\n"
        "v_and_b32 v229, 1, v233\n"
        "v_mfma_f32_16x16x32_bf16 v[136:139], a[40:43], a[240:243], v[136:139]\n"
        "ds_read_b64_tr_b16 a[150:151], v155 offset:53248\n"
        "v_lshlrev_b32 v229, 9, v229\n"
        "global_load_lds_dwordx4 v254, s[20:21] offset:1024 sc1\n"
        "v_mfma_f32_16x16x32_bf16 v[140:143], a[40:43], a[244:247], v[140:143]\n"
        "ds_read_b64_tr_b16 a[152:153], v154 offset:57344\n"
        "v_lshl_add_u32 v229, v232, 5, v229\n"
        "v_mfma_f32_16x16x32_bf16 v[108:111], a[44:47], a[224:227], v[108:111]\n"
        "ds_read_b64_tr_b16 a[154:155], v154 offset:61440\n"
        "v_lshrrev_b32 v230, 1, v233\n"
        "v_mfma_f32_16x16x32_bf16 v[128:131], a[44:47], a[228:231], v[128:131]\n"
        "global_load_lds_dwordx4 v254, s[20:21] offset:2048 sc1\n"
        "v_mfma_f32_16x16x32_bf16 v[132:135], a[44:47], a[232:235], v[132:135]\n"
        "ds_read_b64_tr_b16 a[156:157], v155 offset:57344\n"
        "v_lshl_add_u32 v229, v230, 4, v229\n"
        "v_mfma_f32_16x16x32_bf16 v[136:139], a[44:47], a[236:239], v[136:139]\n"
        "ds_read_b64_tr_b16 a[158:159], v155 offset:61440\n"
        "v_and_b32 v80, 0xffff0000, v65\n"
        "v_mfma_f32_16x16x32_bf16 v[140:143], a[44:47], a[240:243], v[140:143]\n"
        "global_load_lds_dwordx4 v254, s[20:21] offset:3072 sc1\n"
        "s_add_u32 m0, s73, 0x1c000\n"
        "v_mfma_f32_16x16x32_bf16 v[128:131], a[48:51], a[224:227], v[128:131]\n"
        "s_add_u32 s20, s20, 0x4000\n"
        "s_addc_u32 s21, s21, 0\n"
        "v_lshlrev_b32 v81, 16, v65\n"
        "v_mfma_f32_16x16x32_bf16 v[132:135], a[48:51], a[228:231], v[132:135]\n"
        "v_and_b32 v82, 0xffff0000, v64\n"
        "v_lshlrev_b32 v83, 16, v64\n"
        "v_mfma_f32_16x16x32_bf16 v[136:139], a[48:51], a[232:235], v[136:139]\n"
        "global_load_lds_dwordx4 v254, s[20:21] offset:0 sc1\n"
        "v_mfma_f32_16x16x32_bf16 v[140:143], a[48:51], a[236:239], v[140:143]\n"
        "v_mul_f32 v80, s54, v80\n"
        "v_mul_f32 v81, s54, v81\n"
        "v_mfma_f32_16x16x32_bf16 v[132:135], a[52:55], a[224:227], v[132:135]\n"
        "v_mul_f32 v82, s54, v82\n"
        "v_mul_f32 v83, s54, v83\n"
        "v_mfma_f32_16x16x32_bf16 v[136:139], a[52:55], a[228:231], v[136:139]\n"
        "global_load_lds_dwordx4 v254, s[20:21] offset:1024 sc1\n"
        "v_mfma_f32_16x16x32_bf16 v[140:143], a[52:55], a[232:235], v[140:143]\n"
        "v_cvt_pk_bf16_f32 v80, 0, v80\n"
        "v_cvt_pk_bf16_f32 v81, 0, v81\n"
        "v_mfma_f32_16x16x32_bf16 v[136:139], a[56:59], a[224:227], v[136:139]\n"
        "v_cvt_pk_bf16_f32 v82, 0, v82\n"
        "v_cvt_pk_bf16_f32 v83, 0, v83\n"
        "v_mfma_f32_16x16x32_bf16 v[140:143], a[56:59], a[228:231], v[140:143]\n"
        "global_load_lds_dwordx4 v254, s[20:21] offset:2048 sc1\n"
        "v_mfma_f32_16x16x32_bf16 v[140:143], a[60:63], a[224:227], v[140:143]\n"
        "v_cvt_pk_bf16_f32 v96, 0, v96\n"
        "v_cvt_pk_bf16_f32 v97, 0, v97\n"
        "v_cvt_pk_bf16_f32 v98, 0, v98\n"
        "v_cvt_pk_bf16_f32 v99, 0, v99\n"
        "v_add_f32 v80, v80, v96\n"
        "v_add_f32 v81, v81, v97\n"
        "v_add_f32 v82, v82, v98\n"
        "global_load_lds_dwordx4 v254, s[20:21] offset:3072 sc1\n"
        "s_add_u32 m0, s73, 0x20000\n"
        "s_add_u32 s20, s20, 0x4000\n"
        "s_addc_u32 s21, s21, 0\n"
        "v_add_f32 v83, v83, v99\n"
        "v_cvt_pk_bf16_f32 v88, v80, v81\n"
        "v_cvt_pk_bf16_f32 v89, v82, v83\n"
        "v_and_b32 v84, 0xffff0000, v67\n"
        "v_lshlrev_b32 v85, 16, v67\n"
        "v_and_b32 v86, 0xffff0000, v66\n"
        "v_lshlrev_b32 v87, 16, v66\n"
        "global_load_lds_dwordx4 v144, s[60:61] offset:0\n"
        "v_mul_f32 v84, s54, v84\n"
        "v_mul_f32 v85, s54, v85\n"
        "v_mul_f32 v86, s54, v86\n"
        "v_mul_f32 v87, s54, v87\n"
        "v_cvt_pk_bf16_f32 v84, 0, v84\n"
        "v_cvt_pk_bf16_f32 v85, 0, v85\n"
        "v_cvt_pk_bf16_f32 v86, 0, v86\n"
        "v_cvt_pk_bf16_f32 v87, 0, v87\n"
        "v_cvt_pk_bf16_f32 v100, 0, v100\n"
        "global_load_lds_dwordx4 v145, s[60:61] offset:1024\n"
        "v_cvt_pk_bf16_f32 v101, 0, v101\n"
        "v_cvt_pk_bf16_f32 v102, 0, v102\n"
        "v_cvt_pk_bf16_f32 v103, 0, v103\n"
        "v_add_f32 v84, v84, v100\n"
        "v_add_f32 v85, v85, v101\n"
        "v_add_f32 v86, v86, v102\n"
        "v_add_f32 v87, v87, v103\n"
        "v_cvt_pk_bf16_f32 v90, v84, v85\n"
        "v_cvt_pk_bf16_f32 v91, v86, v87\n"
        "global_load_lds_dwordx4 v146, s[60:61] offset:2048\n"
        "v_permlane16_swap_b32 v88, v90\n"
        "v_permlane16_swap_b32 v89, v91\n"
        "v_and_b32 v80, 0xffff0000, v69\n"
        "v_lshlrev_b32 v81, 16, v69\n"
        "v_and_b32 v82, 0xffff0000, v68\n"
        "v_lshlrev_b32 v83, 16, v68\n"
        "v_mul_f32 v80, s54, v80\n"
        "global_load_lds_dwordx4 v147, s[60:61] offset:3072\n"
        "s_add_u32 m0, s73, 0x24000\n"
        "s_add_u32 s60, s22, 0x80000\n"
        "s_addc_u32 s61, s23, 0\n"
        "v_mul_f32 v81, s54, v81\n"
        "v_mul_f32 v82, s54, v82\n"
        "v_mul_f32 v83, s54, v83\n"
        "v_cvt_pk_bf16_f32 v80, 0, v80\n"
        "v_cvt_pk_bf16_f32 v81, 0, v81\n"
        "v_cvt_pk_bf16_f32 v82, 0, v82\n"
        "v_cvt_pk_bf16_f32 v83, 0, v83\n"
        "global_load_lds_dwordx4 v254, s[20:21] offset:0 sc1\n"
        "v_cvt_pk_bf16_f32 v104, 0, v104\n"
        "v_cvt_pk_bf16_f32 v105, 0, v105\n"
        "v_cvt_pk_bf16_f32 v106, 0, v106\n"
        "v_cvt_pk_bf16_f32 v107, 0, v107\n"
        "v_add_f32 v80, v80, v104\n"
        "v_add_f32 v81, v81, v105\n"
        "v_add_f32 v82, v82, v106\n"
        "v_add_f32 v83, v83, v107\n"
        "v_cvt_pk_bf16_f32 v92, v80, v81\n"
        "global_load_lds_dwordx4 v254, s[20:21] offset:1024 sc1\n"
        "v_cvt_pk_bf16_f32 v93, v82, v83\n"
        "v_and_b32 v84, 0xffff0000, v71\n"
        "v_lshlrev_b32 v85, 16, v71\n"
        "v_and_b32 v86, 0xffff0000, v70\n"
        "v_lshlrev_b32 v87, 16, v70\n"
        "v_mul_f32 v84, s54, v84\n"
        "v_mul_f32 v85, s54, v85\n"
        "v_mul_f32 v86, s54, v86\n"
        "v_mul_f32 v87, s54, v87\n"
        "global_load_lds_dwordx4 v254, s[20:21] offset:2048 sc1\n"
        "v_cvt_pk_bf16_f32 v84, 0, v84\n"
        "v_cvt_pk_bf16_f32 v85, 0, v85\n"
        "v_cvt_pk_bf16_f32 v86, 0, v86\n"
        "v_cvt_pk_bf16_f32 v87, 0, v87\n"
        "v_cvt_pk_bf16_f32 v108, 0, v108\n"
        "v_cvt_pk_bf16_f32 v109, 0, v109\n"
        "v_cvt_pk_bf16_f32 v110, 0, v110\n"
        "s_waitcnt vmcnt(19) lgkmcnt(0)\n"
        "s_barrier\n"
        "ds_read_b128 a[0:3], v148 offset:0\n"
        "ds_read_b128 a[4:7], v148 offset:1024\n"
        "ds_read_b128 a[8:11], v148 offset:2048\n"
        "ds_read_b128 a[12:15], v148 offset:3072\n"
        "ds_read_b64_tr_b16 a[160:161], v156 offset:0\n"
        "ds_read_b64_tr_b16 a[162:163], v156 offset:4096\n"
        "ds_read_b64_tr_b16 a[164:165], v157 offset:0\n"
        "ds_read_b64_tr_b16 a[166:167], v157 offset:4096\n"
        "ds_read_b64_tr_b16 a[168:169], v156 offset:8192\n"
        "ds_read_b64_tr_b16 a[170:171], v156 offset:12288\n"
        "ds_read_b64_tr_b16 a[172:173], v157 offset:8192\n"
        "ds_read_b64_tr_b16 a[174:175], v157 offset:12288\n"
        "ds_read_b64_tr_b16 a[176:177], v156 offset:16384\n"
        "ds_read_b64_tr_b16 a[178:179], v156 offset:20480\n"
        "ds_read_b64_tr_b16 a[180:181], v157 offset:16384\n"
        "global_load_lds_dwordx4 v254, s[20:21] offset:3072 sc1\n"
        "s_add_u32 m0, s73, 0x8000\n"
        "s_waitcnt lgkmcnt(14)\n"
        "v_mfma_f32_16x16x32_bf16 v[0:3], v[160:163], a[0:3], 0\n"
        "ds_read_b64_tr_b16 a[182:183], v157 offset:20480\n"
        "s_waitcnt lgkmcnt(14)\n"
        "v_mfma_f32_16x16x32_bf16 v[4:7], v[164:167], a[0:3], 0\n"
        "ds_read_b64_tr_b16 a[184:185], v156 offset:24576\n"
        "s_waitcnt lgkmcnt(13)\n"
        "v_mfma_f32_16x16x32_bf16 v[8:11], v[160:163], a[4:7], 0\n"
        "ds_read_b64_tr_b16 a[186:187], v156 offset:28672\n"
        "global_load_lds_dwordx4 v144, s[60:61] offset:0\n"
        "v_mfma_f32_16x16x32_bf16 v[12:15], v[164:167], a[4:7], 0\n"
        "ds_read_b64_tr_b16 a[188:189], v157 offset:24576\n"
        "v_mfma_f32_16x16x32_bf16 v[16:19], v[160:163], a[8:11], 0\n"
        "ds_read_b64_tr_b16 a[190:191], v157 offset:28672\n"
        "v_mfma_f32_16x16x32_bf16 v[20:23], v[164:167], a[8:11], 0\n"
        "ds_read_b128 a[16:19], v151 offset:0\n"
        "global_load_lds_dwordx4 v145, s[60:61] offset:1024\n"
        "v_mfma_f32_16x16x32_bf16 v[24:27], v[160:163], a[12:15], 0\n"
        "ds_read_b128 a[20:23], v151 offset:1024\n"
        "v_mfma_f32_16x16x32_bf16 v[28:31], v[164:167], a[12:15], 0\n"
        "ds_read_b128 a[24:27], v151 offset:2048\n"
        "ds_read_b128 a[28:31], v151 offset:3072\n"
        "ds_read_b128 a[192:195], v148 offset:8192\n"
        "ds_read_b128 a[196:199], v148 offset:9216\n"
        "ds_read_b128 a[200:203], v148 offset:10240\n"
        "ds_read_b128 a[204:207], v148 offset:11264\n"
        "ds_read_b128 a[208:211], v151 offset:8192\n"
        "ds_read_b128 a[212:215], v151 offset:9216\n"
        "ds_read_b128 a[216:219], v151 offset:10240\n"
        "global_load_lds_dwordx4 v146, s[60:61] offset:2048\n"
        "ds_read_b128 a[220:223], v151 offset:11264\n"
        "s_add_u32 s20, s20, 0x4000\n"
        "s_addc_u32 s21, s21, 0\n"
        "v_cvt_pk_bf16_f32 v111, 0, v111\n"
        "v_add_f32 v84, v84, v108\n"
        "s_waitcnt lgkmcnt(11)\n"
        "v_mfma_f32_16x16x32_bf16 v[32:35], v[160:163], a[16:19], 0\n"
        "v_add_f32 v85, v85, v109\n"
        "v_add_f32 v86, v86, v110\n"
        "v_mfma_f32_16x16x32_bf16 v[36:39], v[164:167], a[16:19], 0\n"
        "global_load_lds_dwordx4 v147, s[60:61] offset:3072\n"
        "s_add_u32 m0, s73, 0xc000\n"
        "s_waitcnt lgkmcnt(10)\n"
        "v_mfma_f32_16x16x32_bf16 v[40:43], v[160:163], a[20:23], 0\n"
        "s_add_u32 s60, s22, 0xc0000\n"
        "s_addc_u32 s61, s23, 0\n"
        "v_add_f32 v87, v87, v111\n"
        "v_mfma_f32_16x16x32_bf16 v[44:47], v[164:167], a[20:23], 0\n"
        "s_waitcnt lgkmcnt(7)\n"
        "v_cvt_pk_bf16_f32 v94, v84, v85\n"
        "v_cvt_pk_bf16_f32 v95, v86, v87\n"
        "v_mfma_f32_16x16x32_bf16 v[48:51], v[160:163], a[24:27], 0\n"
        "global_load_lds_dwordx4 v254, s[20:21] offset:0 sc1\n"
        "v_and_b32 v80, 0xffff0000, v73\n"
        "v_mfma_f32_16x16x32_bf16 v[52:55], v[164:167], a[24:27], 0\n"
        "s_waitcnt vmcnt(17)\n"
        "s_barrier\n"
        "v_mfma_f32_16x16x32_bf16 v[56:59], v[160:163], a[28:31], 0\n"
        "ds_read_b64_tr_b16 a[32:33], v154 offset:16384\n"
        "s_waitcnt lgkmcnt(1)\n"
        "v_mfma_f32_16x16x32_bf16 v[60:63], v[164:167], a[28:31], 0\n"
        "global_load_lds_dwordx4 v254, s[20:21] offset:1024 sc1\n"
        "ds_read_b64_tr_b16 a[34:35], v154 offset:20480\n"
        "v_mfma_f32_16x16x32_bf16 v[0:3], v[168:171], a[192:195], v[0:3]\n"
        "ds_read_b64_tr_b16 a[36:37], v155 offset:16384\n"
        "v_mfma_f32_16x16x32_bf16 v[4:7], v[172:175], a[192:195], v[4:7]\n"
        "ds_read_b64_tr_b16 a[38:39], v155 offset:20480\n"
        "v_mfma_f32_16x16x32_bf16 v[8:11], v[168:171], a[196:199], v[8:11]\n"
        "global_load_lds_dwordx4 v254, s[20:21] offset:2048 sc1\n"
        "ds_read_b128 a[0:3], v149 offset:32768\n"
        "v_mfma_f32_16x16x32_bf16 v[12:15], v[172:175], a[196:199], v[12:15]\n"
        "ds_read_b128 a[4:7], v149 offset:33792\n"
        "v_mfma_f32_16x16x32_bf16 v[16:19], v[168:171], a[200:203], v[16:19]\n"
        "ds_read_b128 a[8:11], v149 offset:34816\n"
        "v_mfma_f32_16x16x32_bf16 v[20:23], v[172:175], a[200:203], v[20:23]\n"
        "global_load_lds_dwordx4 v254, s[20:21] offset:3072 sc1\n"
        "s_add_u32 m0, s73, 0x10000\n"
        "v_mfma_f32_16x16x32_bf16 v[24:27], v[168:171], a[204:207], v[24:27]\n"
        "s_add_u32 s20, s20, 0x4000\n"
        "s_addc_u32 s21, s21, 0\n"
        "v_mfma_f32_16x16x32_bf16 v[28:31], v[172:175], a[204:207], v[28:31]\n"
        "ds_read_b128 a[12:15], v149 offset:35840\n"
        "v_mfma_f32_16x16x32_bf16 v[32:35], v[168:171], a[208:211], v[32:35]\n"
        "global_load_lds_dwordx4 v254, s[20:21] offset:0 sc1\n"
        "ds_read_b128 a[16:19], v152 offset:32768\n"
        "v_mfma_f32_16x16x32_bf16 v[36:39], v[172:175], a[208:211], v[36:39]\n"
        "ds_read_b128 a[20:23], v152 offset:33792\n"
        "v_mfma_f32_16x16x32_bf16 v[40:43], v[168:171], a[212:215], v[40:43]\n"
        "ds_read_b128 a[24:27], v152 offset:34816\n"
        "s_waitcnt lgkmcnt(6)\n"
        "v_mfma_f32_16x16x32_bf16 v[44:47], v[172:175], a[212:215], v[44:47]\n"
        "global_load_lds_dwordx4 v254, s[20:21] offset:1024 sc1\n"
        "ds_read_b128 a[28:31], v152 offset:35840\n"
        "v_mfma_f32_16x16x32_bf16 v[48:51], v[168:171], a[216:219], v[48:51]\n"
        "ds_read_b64_tr_b16 a[40:41], v154 offset:24576\n"
        "s_waitcnt lgkmcnt(6)\n"
        "v_mfma_f32_16x16x32_bf16 v[52:55], v[172:175], a[216:219], v[52:55]\n"
        "ds_read_b64_tr_b16 a[42:43], v154 offset:28672\n"
        "v_mfma_f32_16x16x32_bf16 v[56:59], v[168:171], a[220:223], v[56:59]\n"
        "global_load_lds_dwordx4 v254, s[20:21] offset:2048 sc1\n"
        "ds_read_b64_tr_b16 a[44:45], v155 offset:24576\n"
        "v_mfma_f32_16x16x32_bf16 v[60:63], v[172:175], a[220:223], v[60:63]\n"
        "ds_read_b64_tr_b16 a[46:47], v155 offset:28672\n"
        "v_mfma_f32_16x16x32_bf16 v[0:3], a[32:35], a[0:3], v[0:3]\n"
        "ds_read_b128 a[192:195], v149 offset:40960\n"
        "v_lshlrev_b32 v81, 16, v73\n"
        "v_mfma_f32_16x16x32_bf16 v[4:7], a[36:39], a[0:3], v[4:7]\n"
        "global_load_lds_dwordx4 v254, s[20:21] offset:3072 sc1\n"
        "s_add_u32 s20, s20, 0x4000\n"
        "s_addc_u32 s21, s21, 0\n"
        "v_mfma_f32_16x16x32_bf16 v[8:11], a[32:35], a[4:7], v[8:11]\n"
        "s_add_u32 m0, s73, 0x14000\n"
        "ds_read_b128 a[196:199], v149 offset:41984\n"
        "v_mfma_f32_16x16x32_bf16 v[12:15], a[36:39], a[4:7], v[12:15]\n"
        "ds_read_b128 a[200:203], v149 offset:43008\n"
        "v_and_b32 v82, 0xffff0000, v72\n"
        "v_mfma_f32_16x16x32_bf16 v[16:19], a[32:35], a[8:11], v[16:19]\n"
        "global_load_lds_dwordx4 v144, s[60:61] offset:0\n"
        "ds_read_b128 a[204:207], v149 offset:44032\n"
        "v_mfma_f32_16x16x32_bf16 v[20:23], a[36:39], a[8:11], v[20:23]\n"
        "ds_read_b128 a[208:211], v152 offset:40960\n"
        "s_waitcnt lgkmcnt(7)\n"
        "v_mfma_f32_16x16x32_bf16 v[24:27], a[32:35], a[12:15], v[24:27]\n"
        "ds_read_b128 a[212:215], v152 offset:41984\n"
        "v_lshlrev_b32 v83, 16, v72\n"
        "v_mfma_f32_16x16x32_bf16 v[28:31], a[36:39], a[12:15], v[28:31]\n"
        "global_load_lds_dwordx4 v145, s[60:61] offset:1024\n"
        "ds_read_b128 a[216:219], v152 offset:43008\n"
        "v_mfma_f32_16x16x32_bf16 v[32:35], a[32:35], a[16:19], v[32:35]\n"
        "ds_read_b128 a[220:223], v152 offset:44032\n"
        "v_mul_f32 v80, s54, v80\n"
        "v_mfma_f32_16x16x32_bf16 v[36:39], a[36:39], a[16:19], v[36:39]\n"
        "s_waitcnt vmcnt(22)\n"
        "s_barrier\n"
        "v_mul_f32 v81, s54, v81\n"
        "v_mfma_f32_16x16x32_bf16 v[40:43], a[32:35], a[20:23], v[40:43]\n"
        "global_load_lds_dwordx4 v146, s[60:61] offset:2048\n"
        "ds_read_b128 a[0:3], v149 offset:49152\n"
        "v_mfma_f32_16x16x32_bf16 v[44:47], a[36:39], a[20:23], v[44:47]\n"
        "ds_read_b128 a[4:7], v149 offset:50176\n"
        "v_mul_f32 v82, s54, v82\n"
        "v_mfma_f32_16x16x32_bf16 v[48:51], a[32:35], a[24:27], v[48:51]\n"
        "ds_read_b128 a[8:11], v149 offset:51200\n"
        "v_mul_f32 v83, s54, v83\n"
        "v_mfma_f32_16x16x32_bf16 v[52:55], a[36:39], a[24:27], v[52:55]\n"
        "global_load_lds_dwordx4 v147, s[60:61] offset:3072\n"
        "s_add_u32 m0, s73, 0x0\n"
        "v_cvt_pk_bf16_f32 v80, 0, v80\n"
        "v_mfma_f32_16x16x32_bf16 v[56:59], a[32:35], a[28:31], v[56:59]\n"
        "ds_read_b128 a[12:15], v149 offset:52224\n"
        "v_cvt_pk_bf16_f32 v81, 0, v81\n"
        "v_mfma_f32_16x16x32_bf16 v[60:63], a[36:39], a[28:31], v[60:63]\n"
        "ds_read_b128 a[16:19], v152 offset:49152\n"
        "s_waitcnt lgkmcnt(6)\n"
        "v_mfma_f32_16x16x32_bf16 v[0:3], a[40:43], a[192:195], v[0:3]\n"
        "global_load_lds_dwordx4 v254, s[20:21] offset:0 sc1\n"
        "ds_read_b128 a[20:23], v152 offset:50176\n"
        "v_mfma_f32_16x16x32_bf16 v[4:7], a[44:47], a[192:195], v[4:7]\n"
        "ds_read_b128 a[24:27], v152 offset:51200\n"
        "v_cvt_pk_bf16_f32 v82, 0, v82\n"
        "v_mfma_f32_16x16x32_bf16 v[8:11], a[40:43], a[196:199], v[8:11]\n"
        "ds_read_b128 a[28:31], v152 offset:52224\n"
        "v_cvt_pk_bf16_f32 v83, 0, v83\n"
        "v_mfma_f32_16x16x32_bf16 v[12:15], a[44:47], a[196:199], v[12:15]\n"
        "global_load_lds_dwordx4 v254, s[20:21] offset:1024 sc1\n"
        "ds_read_b128 a[192:195], v149 offset:57344\n"
        "v_mfma_f32_16x16x32_bf16 v[16:19], a[40:43], a[200:203], v[16:19]\n"
        "ds_read_b128 a[196:199], v149 offset:58368\n"
        "v_cvt_pk_bf16_f32 v128, 0, v128\n"
        "v_mfma_f32_16x16x32_bf16 v[20:23], a[44:47], a[200:203], v[20:23]\n"
        "v_permlane16_swap_b32 v92, v94\n"
        "v_mfma_f32_16x16x32_bf16 v[24:27], a[40:43], a[204:207], v[24:27]\n"
        "global_load_lds_dwordx4 v254, s[20:21] offset:2048 sc1\n"
        "v_cvt_pk_bf16_f32 v129, 0, v129\n"
        "v_mfma_f32_16x16x32_bf16 v[28:31], a[44:47], a[204:207], v[28:31]\n"
        "v_permlane16_swap_b32 v93, v95\n"
        "v_mfma_f32_16x16x32_bf16 v[32:35], a[40:43], a[208:211], v[32:35]\n"
        "s_waitcnt lgkmcnt(5)\n"
        "v_cvt_pk_bf16_f32 v130, 0, v130\n"
        "v_cvt_pk_bf16_f32 v131, 0, v131\n"
        "v_mfma_f32_16x16x32_bf16 v[36:39], a[44:47], a[208:211], v[36:39]\n"
        "global_load_lds_dwordx4 v254, s[20:21] offset:3072 sc1\n"
        "s_add_u32 s20, s20, 0x4000\n"
        "s_addc_u32 s21, s21, 0\n"
        "v_mfma_f32_16x16x32_bf16 v[40:43], a[40:43], a[212:215], v[40:43]\n"
        "v_add_f32 v80, v80, v128\n"
        "v_add_f32 v81, v81, v129\n"
        "v_mfma_f32_16x16x32_bf16 v[44:47], a[44:47], a[212:215], v[44:47]\n"
        "v_add_f32 v82, v82, v130\n"
        "v_add_f32 v83, v83, v131\n"
        "v_mfma_f32_16x16x32_bf16 v[48:51], a[40:43], a[216:219], v[48:51]\n"
        "global_store_dwordx4 v229, v[88:91], s[24:25] offset:0 sc1\n"
        "v_and_b32 v84, 0xffff0000, v75\n"
        "v_lshlrev_b32 v85, 16, v75\n"
        "v_cvt_pk_bf16_f32 v88, v80, v81\n"
        "v_cvt_pk_bf16_f32 v89, v82, v83\n"
        "v_mfma_f32_16x16x32_bf16 v[52:55], a[44:47], a[216:219], v[52:55]\n"
        "v_and_b32 v86, 0xffff0000, v74\n"
        "v_lshlrev_b32 v87, 16, v74\n"
        "v_mfma_f32_16x16x32_bf16 v[56:59], a[40:43], a[220:223], v[56:59]\n"
        "v_mul_f32 v84, s54, v84\n"
        "v_mul_f32 v85, s54, v85\n"
        "v_mfma_f32_16x16x32_bf16 v[60:63], a[44:47], a[220:223], v[60:63]\n"
        "global_store_dwordx4 v229, v[92:95], s[24:25] offset:1024 sc1\n"
        "v_mul_f32 v86, s54, v86\n"
        "v_mul_f32 v87, s54, v87\n"
        "v_cvt_pk_bf16_f32 v84, 0, v84\n"
        "v_cvt_pk_bf16_f32 v85, 0, v85\n"
        "v_mfma_f32_16x16x32_bf16 v[0:3], v[176:179], a[0:3], v[0:3]\n"
        "v_cvt_pk_bf16_f32 v86, 0, v86\n"
        "v_cvt_pk_bf16_f32 v87, 0, v87\n"
        "v_mfma_f32_16x16x32_bf16 v[4:7], v[180:183], a[0:3], v[4:7]\n"
        "v_cvt_pk_bf16_f32 v132, 0, v132\n"
        "v_cvt_pk_bf16_f32 v133, 0, v133\n"
        "v_mfma_f32_16x16x32_bf16 v[8:11], v[176:179], a[4:7], v[8:11]\n"
        "v_cvt_pk_bf16_f32 v134, 0, v134\n"
        "v_cvt_pk_bf16_f32 v135, 0, v135\n"
        "v_mfma_f32_16x16x32_bf16 v[12:15], v[180:183], a[4:7], v[12:15]\n"
        "v_add_f32 v84, v84, v132\n"
        "v_add_f32 v85, v85, v133\n"
        "v_mfma_f32_16x16x32_bf16 v[16:19], v[176:179], a[8:11], v[16:19]\n"
        "s_waitcnt lgkmcnt(0)\n"
        "v_add_f32 v86, v86, v134\n"
        "v_add_f32 v87, v87, v135\n"
        "v_mfma_f32_16x16x32_bf16 v[20:23], v[180:183], a[8:11], v[20:23]\n"
        "v_cvt_pk_bf16_f32 v90, v84, v85\n"
        "v_cvt_pk_bf16_f32 v91, v86, v87\n"
        "v_mfma_f32_16x16x32_bf16 v[24:27], v[176:179], a[12:15], v[24:27]\n"
        "v_permlane16_swap_b32 v88, v90\n"
        "v_mfma_f32_16x16x32_bf16 v[28:31], v[180:183], a[12:15], v[28:31]\n"
        "v_permlane16_swap_b32 v89, v91\n"
        "v_mfma_f32_16x16x32_bf16 v[32:35], v[176:179], a[16:19], v[32:35]\n"
        "global_store_dwordx4 v229, v[88:91], s[24:25] offset:2048 sc1\n"
        "v_and_b32 v80, 0xffff0000, v77\n"
        "v_lshlrev_b32 v81, 16, v77\n"
        "v_and_b32 v82, 0xffff0000, v76\n"
        "v_lshlrev_b32 v83, 16, v76\n"
        "v_mfma_f32_16x16x32_bf16 v[36:39], v[180:183], a[16:19], v[36:39]\n"
        "v_mul_f32 v80, s54, v80\n"
        "v_mul_f32 v81, s54, v81\n"
        "v_mfma_f32_16x16x32_bf16 v[40:43], v[176:179], a[20:23], v[40:43]\n"
        "v_mul_f32 v82, s54, v82\n"
        "v_mul_f32 v83, s54, v83\n"
        "v_mfma_f32_16x16x32_bf16 v[44:47], v[180:183], a[20:23], v[44:47]\n"
        "v_cvt_pk_bf16_f32 v80, 0, v80\n"
        "v_cvt_pk_bf16_f32 v81, 0, v81\n"
        "v_cvt_pk_bf16_f32 v82, 0, v82\n"
        "v_cvt_pk_bf16_f32 v83, 0, v83\n"
        "v_cvt_pk_bf16_f32 v136, 0, v136\n"
        "v_cvt_pk_bf16_f32 v137, 0, v137\n"
        "v_cvt_pk_bf16_f32 v138, 0, v138\n"
        "v_cvt_pk_bf16_f32 v139, 0, v139\n"
        "v_add_f32 v80, v80, v136\n"
        "v_add_f32 v81, v81, v137\n"
        "v_add_f32 v82, v82, v138\n"
        "v_add_f32 v83, v83, v139\n"
        "v_cvt_pk_bf16_f32 v92, v80, v81\n"
        "v_cvt_pk_bf16_f32 v93, v82, v83\n"
        "v_and_b32 v84, 0xffff0000, v79\n"
        "v_lshlrev_b32 v85, 16, v79\n"
        "v_and_b32 v86, 0xffff0000, v78\n"
        "v_lshlrev_b32 v87, 16, v78\n"
        "v_mul_f32 v84, s54, v84\n"
        "v_mul_f32 v85, s54, v85\n"
        "v_mul_f32 v86, s54, v86\n"
        "v_mul_f32 v87, s54, v87\n"
        "v_cvt_pk_bf16_f32 v84, 0, v84\n"
        "v_cvt_pk_bf16_f32 v85, 0, v85\n"
        "v_cvt_pk_bf16_f32 v86, 0, v86\n"
        "v_cvt_pk_bf16_f32 v87, 0, v87\n"
        "v_cvt_pk_bf16_f32 v140, 0, v140\n"
        "v_cvt_pk_bf16_f32 v141, 0, v141\n"
        "v_cvt_pk_bf16_f32 v142, 0, v142\n"
        "v_cvt_pk_bf16_f32 v143, 0, v143\n"
        "v_add_f32 v84, v84, v140\n"
        "v_add_f32 v85, v85, v141\n"
        "v_add_f32 v86, v86, v142\n"
        "v_add_f32 v87, v87, v143\n"
        "v_cvt_pk_bf16_f32 v94, v84, v85\n"
        "v_cvt_pk_bf16_f32 v95, v86, v87\n"
        "v_mfma_f32_16x16x32_bf16 v[48:51], v[176:179], a[24:27], v[48:51]\n"
        "s_waitcnt vmcnt(23)\n"
        "v_permlane16_swap_b32 v92, v94\n"
        "v_mfma_f32_16x16x32_bf16 v[52:55], v[180:183], a[24:27], v[52:55]\n"
        "s_barrier\n"
        "v_permlane16_swap_b32 v93, v95\n"
        "v_mfma_f32_16x16x32_bf16 v[56:59], v[176:179], a[28:31], v[56:59]\n"
        "global_store_dwordx4 v229, v[92:95], s[24:25] offset:3072 sc1\n"
        "ds_read_b128 a[200:203], v149 offset:59392\n"
        "ds_read_b128 a[204:207], v149 offset:60416\n"
        "s_add_u32 m0, s73, 0x4000\n"
        "v_mfma_f32_16x16x32_bf16 v[60:63], v[180:183], a[28:31], v[60:63]\n"
        "ds_read_b128 a[208:211], v152 offset:57344\n"
        "s_add_u32 s60, s22, 0xe0000\n"
        "s_addc_u32 s61, s23, 0\n"
        "v_mfma_f32_16x16x32_bf16 v[0:3], v[184:187], a[192:195], v[0:3]\n"
        "ds_read_b128 a[212:215], v152 offset:58368\n"
        "global_load_lds_dwordx4 v144, s[60:61] offset:0\n"
        "v_mfma_f32_16x16x32_bf16 v[4:7], v[188:191], a[192:195], v[4:7]\n"
        "ds_read_b128 a[216:219], v152 offset:59392\n"
        "v_mfma_f32_16x16x32_bf16 v[8:11], v[184:187], a[196:199], v[8:11]\n"
        "ds_read_b128 a[220:223], v152 offset:60416\n"
        "v_mfma_f32_16x16x32_bf16 v[12:15], v[188:191], a[196:199], v[12:15]\n"
        "ds_read_b64_tr_b16 a[48:49], v158 offset:0\n"
        "ds_read_b64_tr_b16 a[50:51], v158 offset:4096\n"
        "ds_read_b64_tr_b16 a[52:53], v159 offset:0\n"
        "ds_read_b64_tr_b16 a[54:55], v159 offset:4096\n"
        "s_waitcnt lgkmcnt(9)\n"
        "v_mfma_f32_16x16x32_bf16 v[16:19], v[184:187], a[200:203], v[16:19]\n"
        "ds_read_b128 a[0:3], v150 offset:16384\n"
        "global_load_lds_dwordx4 v145, s[60:61] offset:1024\n"
        "v_mfma_f32_16x16x32_bf16 v[20:23], v[188:191], a[200:203], v[20:23]\n"
        "ds_read_b128 a[4:7], v150 offset:17408\n"
        "s_waitcnt lgkmcnt(10)\n"
        "v_mfma_f32_16x16x32_bf16 v[24:27], v[184:187], a[204:207], v[24:27]\n"
        "ds_read_b128 a[8:11], v150 offset:18432\n"
        "s_waitcnt lgkmcnt(10)\n"
        "v_mfma_f32_16x16x32_bf16 v[28:31], v[188:191], a[204:207], v[28:31]\n"
        "ds_read_b128 a[12:15], v150 offset:19456\n"
        "global_load_lds_dwordx4 v146, s[60:61] offset:2048\n"
        "v_mfma_f32_16x16x32_bf16 v[32:35], v[184:187], a[208:211], v[32:35]\n"
        "ds_read_b128 a[16:19], v153 offset:16384\n"
        "s_waitcnt lgkmcnt(10)\n"
        "v_mfma_f32_16x16x32_bf16 v[36:39], v[188:191], a[208:211], v[36:39]\n"
        "ds_read_b128 a[20:23], v153 offset:17408\n"
        "s_waitcnt lgkmcnt(10)\n"
        "v_mfma_f32_16x16x32_bf16 v[40:43], v[184:187], a[212:215], v[40:43]\n"
        "global_load_lds_dwordx4 v147, s[60:61] offset:3072\n"
        "s_add_u32 m0, s73, 0x18000\n"
        "v_mfma_f32_16x16x32_bf16 v[44:47], v[188:191], a[212:215], v[44:47]\n"
        "ds_read_b128 a[24:27], v153 offset:18432\n"
        "s_waitcnt lgkmcnt(7)\n"
        "v_mfma_f32_16x16x32_bf16 v[48:51], v[184:187], a[216:219], v[48:51]\n"
        "ds_read_b128 a[28:31], v153 offset:19456\n"
        "s_waitcnt lgkmcnt(7)\n"
        "v_mfma_f32_16x16x32_bf16 v[52:55], v[188:191], a[216:219], v[52:55]\n"
        "global_load_lds_dwordx4 v254, s[20:21] offset:0 sc1\n"
        "v_mfma_f32_16x16x32_bf16 v[56:59], v[184:187], a[220:223], v[56:59]\n"
        "ds_read_b64_tr_b16 a[56:57], v158 offset:8192\n"
        "s_waitcnt lgkmcnt(6)\n"
        "v_mfma_f32_16x16x32_bf16 v[60:63], v[188:191], a[220:223], v[60:63]\n"
        "ds_read_b64_tr_b16 a[58:59], v158 offset:12288\n"
        "v_mfma_f32_16x16x32_bf16 v[0:3], a[48:51], a[0:3], v[0:3]\n"
        "global_load_lds_dwordx4 v254, s[20:21] offset:1024 sc1\n"
        "v_mfma_f32_16x16x32_bf16 v[4:7], a[52:55], a[0:3], v[4:7]\n"
        "ds_read_b64_tr_b16 a[60:61], v159 offset:8192\n"
        "s_add_u32 s60, s22, 0x120000\n"
        "s_addc_u32 s61, s23, 0\n"
        "v_mfma_f32_16x16x32_bf16 v[8:11], a[48:51], a[4:7], v[8:11]\n"
        "ds_read_b64_tr_b16 a[62:63], v159 offset:12288\n"
        "s_lshl_b32 s14, s41, 5\n"
        "s_add_u32 s14, s14, s42\n"
        "v_mfma_f32_16x16x32_bf16 v[12:15], a[52:55], a[4:7], v[12:15]\n"
        "ds_read_b128 a[192:195], v150 offset:24576\n"
        "global_load_lds_dwordx4 v254, s[20:21] offset:2048 sc1\n"
        "s_add_u32 s14, s14, 512\n"
        "v_mfma_f32_16x16x32_bf16 v[16:19], a[48:51], a[8:11], v[16:19]\n"
        "ds_read_b128 a[196:199], v150 offset:25600\n"
        "s_lshl_b32 s14, s14, 7\n"
        "v_lshrrev_b32 v234, 1, v254\n"
        "v_mfma_f32_16x16x32_bf16 v[20:23], a[52:55], a[8:11], v[20:23]\n"
        "ds_read_b128 a[200:203], v150 offset:26624\n"
        "s_lshl_b32 s16, s63, 12\n"
        "s_waitcnt lgkmcnt(7)\n"
        "v_mfma_f32_16x16x32_bf16 v[24:27], a[48:51], a[12:15], v[24:27]\n"
        "global_load_lds_dwordx4 v254, s[20:21] offset:3072 sc1\n"
        "s_add_u32 m0, s73, 0x1c000\n"
        "v_mfma_f32_16x16x32_bf16 v[28:31], a[52:55], a[12:15], v[28:31]\n"
        "ds_read_b128 a[204:207], v150 offset:27648\n"
        "s_add_u32 s20, s20, 0x4000\n"
        "s_addc_u32 s21, s21, 0\n"
        "v_mfma_f32_16x16x32_bf16 v[32:35], a[48:51], a[16:19], v[32:35]\n"
        "ds_read_b128 a[208:211], v153 offset:24576\n"
        "s_lshl1_add_u32 s16, s62, s16\n"
        "v_add_u32 v234, s14, v234\n"
        "v_mfma_f32_16x16x32_bf16 v[36:39], a[52:55], a[16:19], v[36:39]\n"
        "ds_read_b128 a[212:215], v153 offset:25600\n"
        "s_waitcnt vmcnt(24)\n"
        "v_mfma_f32_16x16x32_bf16 v[40:43], a[48:51], a[20:23], v[40:43]\n"
        "ds_read_b128 a[216:219], v153 offset:26624\n"
        "s_barrier\n"
        "s_waitcnt lgkmcnt(7)\n"
        "v_mfma_f32_16x16x32_bf16 v[44:47], a[52:55], a[20:23], v[44:47]\n"
        "ds_read_b128 a[220:223], v153 offset:27648\n"
        "global_load_lds_dwordx4 v254, s[20:21] offset:0 sc1\n"
        "v_mfma_f32_16x16x32_bf16 v[48:51], a[48:51], a[24:27], v[48:51]\n"
        "ds_read_b64_tr_b16 a[32:33], v154 offset:32768\n"
        "s_waitcnt lgkmcnt(7)\n"
        "v_mfma_f32_16x16x32_bf16 v[52:55], a[52:55], a[24:27], v[52:55]\n"
        "ds_read_b64_tr_b16 a[34:35], v154 offset:36864\n"
        "v_mfma_f32_16x16x32_bf16 v[56:59], a[48:51], a[28:31], v[56:59]\n"
        "ds_read_b64_tr_b16 a[36:37], v155 offset:32768\n"
        "global_load_lds_dwordx4 v254, s[20:21] offset:1024 sc1\n"
        "v_mfma_f32_16x16x32_bf16 v[60:63], a[52:55], a[28:31], v[60:63]\n"
        "ds_read_b64_tr_b16 a[38:39], v155 offset:36864\n"
        "s_waitcnt lgkmcnt(8)\n"
        "v_mfma_f32_16x16x32_bf16 v[0:3], a[56:59], a[192:195], v[0:3]\n"
        "ds_read_b128 a[0:3], v148 offset:49152\n"
        "v_mfma_f32_16x16x32_bf16 v[4:7], a[60:63], a[192:195], v[4:7]\n"
        "ds_read_b128 a[4:7], v148 offset:50176\n"
        "global_load_lds_dwordx4 v254, s[20:21] offset:2048 sc1\n"
        "v_mfma_f32_16x16x32_bf16 v[8:11], a[56:59], a[196:199], v[8:11]\n"
        "ds_read_b128 a[8:11], v148 offset:51200\n"
        "s_waitcnt lgkmcnt(8)\n"
        "v_mfma_f32_16x16x32_bf16 v[12:15], a[60:63], a[196:199], v[12:15]\n"
        "ds_read_b128 a[12:15], v148 offset:52224\n"
        "v_mfma_f32_16x16x32_bf16 v[16:19], a[56:59], a[200:203], v[16:19]\n"
        "ds_read_b128 a[16:19], v151 offset:49152\n"
        "global_load_lds_dwordx4 v254, s[20:21] offset:3072 sc1\n"
        "v_mfma_f32_16x16x32_bf16 v[20:23], a[60:63], a[200:203], v[20:23]\n"
        "ds_read_b128 a[20:23], v151 offset:50176\n"
        "s_add_u32 s20, s20, 0x4000\n"
        "s_addc_u32 s21, s21, 0\n"
        "v_mfma_f32_16x16x32_bf16 v[24:27], a[56:59], a[204:207], v[24:27]\n"
        "ds_read_b128 a[24:27], v151 offset:51200\n"
        "s_add_u32 m0, s73, 0x20000\n"
        "s_lshr_b32 s14, s63, 2\n"
        "v_mfma_f32_16x16x32_bf16 v[28:31], a[60:63], a[204:207], v[28:31]\n"
        "ds_read_b128 a[28:31], v151 offset:52224\n"
        "s_add_u32 s14, s14, 512\n"
        "s_lshl_b32 s14, s14, 7\n"
        "v_mfma_f32_16x16x32_bf16 v[32:35], a[56:59], a[208:211], v[32:35]\n"
        "ds_read_b64_tr_b16 a[40:41], v154 offset:40960\n"
        "s_add_u32 s16, s16, 0x3400000\n"
        "v_and_b32 v226, 1, v233\n"
        "v_mfma_f32_16x16x32_bf16 v[36:39], a[60:63], a[208:211], v[36:39]\n"
        "ds_read_b64_tr_b16 a[42:43], v154 offset:45056\n"
        "v_lshlrev_b32 v227, 11, v232\n"
        "v_mfma_f32_16x16x32_bf16 v[40:43], a[56:59], a[212:215], v[40:43]\n"
        "ds_read_b64_tr_b16 a[44:45], v155 offset:40960\n"
        "v_lshl_add_u32 v227, v226, 5, v227\n"
        "v_mfma_f32_16x16x32_bf16 v[44:47], a[60:63], a[212:215], v[44:47]\n"
        "ds_read_b64_tr_b16 a[46:47], v155 offset:45056\n"
        "v_lshrrev_b32 v226, 1, v233\n"
        "v_mfma_f32_16x16x32_bf16 v[48:51], a[56:59], a[216:219], v[48:51]\n"
        "ds_read_b128 a[192:195], v148 offset:57344\n"
        "v_lshl_add_u32 v227, v226, 4, v227\n"
        "v_mfma_f32_16x16x32_bf16 v[52:55], a[60:63], a[216:219], v[52:55]\n"
        "ds_read_b128 a[196:199], v148 offset:58368\n"
        "s_waitcnt lgkmcnt(8)\n"
        "v_mfma_f32_16x16x32_bf16 v[56:59], a[56:59], a[220:223], v[56:59]\n"
        "ds_read_b128 a[200:203], v148 offset:59392\n"
        "s_add_u32 s58, s38, s16\n"
        "s_addc_u32 s59, s39, 0\n"
        "v_mfma_f32_16x16x32_bf16 v[60:63], a[60:63], a[220:223], v[60:63]\n"
        "ds_read_b128 a[204:207], v148 offset:60416\n"
        "s_lshl_b32 s16, s13, 14\n"
        "v_lshrrev_b32 v224, 7, v254\n"
        "v_mfma_f32_16x16x32_bf16 v[0:3], a[32:35], a[0:3], v[0:3]\n"
        "ds_read_b128 a[208:211], v151 offset:57344\n"
        "s_add_u32 s16, s16, s73\n"
        "v_lshlrev_b32 v224, 12, v224\n"
        "v_mfma_f32_16x16x32_bf16 v[4:7], a[36:39], a[0:3], v[4:7]\n"
        "ds_read_b128 a[212:215], v151 offset:58368\n"
        "s_add_u32 s16, s16, 0x4000000\n"
        "v_and_b32 v226, 0x70, v254\n"
        "v_mfma_f32_16x16x32_bf16 v[8:11], a[32:35], a[4:7], v[8:11]\n"
        "ds_read_b128 a[216:219], v151 offset:59392\n"
        "v_add_u32 v224, v224, v226\n"
        "v_mfma_f32_16x16x32_bf16 v[12:15], a[36:39], a[4:7], v[12:15]\n"
        "ds_read_b128 a[220:223], v151 offset:60416\n"
        "v_lshrrev_b32 v225, 6, v254\n"
        "v_mfma_f32_16x16x32_bf16 v[16:19], a[32:35], a[8:11], v[16:19]\n"
        "s_add_u32 s28, s38, s16\n"
        "s_addc_u32 s29, s39, 0\n"
        "v_lshlrev_b32 v225, 7, v225\n"
        "v_mfma_f32_16x16x32_bf16 v[20:23], a[36:39], a[8:11], v[20:23]\n"
        "s_xor_b32 s16, s16, 0x4000\n"
        "v_bfe_u32 v226, v254, 4, 2\n"
        "v_lshl_add_u32 v225, v226, 3, v225\n"
        "v_mfma_f32_16x16x32_bf16 v[24:27], a[32:35], a[12:15], v[24:27]\n"
        "s_add_u32 s30, s38, s16\n"
        "s_addc_u32 s31, s39, 0\n"
        "v_mov_b32 v136, 0\n"
        "v_mfma_f32_16x16x32_bf16 v[28:31], a[36:39], a[12:15], v[28:31]\n"
        "s_waitcnt lgkmcnt(4)\n"
        "v_mov_b32 v137, 0\n"
        "v_mov_b32 v140, 0\n"
        "v_mfma_f32_16x16x32_bf16 v[32:35], a[32:35], a[16:19], v[32:35]\n"
        "s_add_u32 s16, s73, 0x18000\n"
        "v_mov_b32 v141, 0\n"
        "v_and_b32 v246, 1, v232\n"
        "v_mfma_f32_16x16x32_bf16 v[36:39], a[36:39], a[16:19], v[36:39]\n"
        "s_waitcnt vmcnt(24)\n"
        "s_barrier\n"
        "v_lshlrev_b32 v246, 4, v246\n"
        "v_mfma_f32_16x16x32_bf16 v[40:43], a[32:35], a[20:23], v[40:43]\n"
        "global_load_lds_dwordx4 v144, s[60:61] offset:0\n"
        "ds_read_b128 a[0:3], v149 offset:0\n"
        "v_mfma_f32_16x16x32_bf16 v[44:47], a[36:39], a[20:23], v[44:47]\n"
        "ds_read_b128 a[4:7], v149 offset:1024\n"
        "v_mov_b32 v247, 0x3f80\n"
        "v_mfma_f32_16x16x32_bf16 v[48:51], a[32:35], a[24:27], v[48:51]\n"
        "ds_read_b128 a[8:11], v149 offset:2048\n"
        "v_lshlrev_b32 v246, v246, v247\n"
        "v_mfma_f32_16x16x32_bf16 v[52:55], a[36:39], a[24:27], v[52:55]\n"
        "global_load_lds_dwordx4 v145, s[60:61] offset:1024\n"
        "ds_read_b128 a[12:15], v149 offset:3072\n"
        "v_mfma_f32_16x16x32_bf16 v[56:59], a[32:35], a[28:31], v[56:59]\n"
        "ds_read_b128 a[16:19], v152 offset:0\n"
        "v_lshrrev_b32 v247, 1, v232\n"
        "v_mfma_f32_16x16x32_bf16 v[60:63], a[36:39], a[28:31], v[60:63]\n"
        "ds_read_b128 a[20:23], v152 offset:1024\n"
        "v_lshlrev_b32 v248, 1, v233\n"
        "v_mfma_f32_16x16x32_bf16 v[0:3], a[40:43], a[192:195], v[0:3]\n"
        "global_load_lds_dwordx4 v146, s[60:61] offset:2048\n"
        "ds_read_b128 a[24:27], v152 offset:2048\n"
        "v_mfma_f32_16x16x32_bf16 v[4:7], a[44:47], a[192:195], v[4:7]\n"
        "ds_read_b128 a[28:31], v152 offset:3072\n"
        "v_or_b32 v249, 1, v248\n"
        "v_mfma_f32_16x16x32_bf16 v[8:11], a[40:43], a[196:199], v[8:11]\n"
        "ds_read_b128 a[192:195], v149 offset:8192\n"
        "v_add_u32 v225, s16, v225\n"
        "v_mfma_f32_16x16x32_bf16 v[12:15], a[44:47], a[196:199], v[12:15]\n"
        "global_load_lds_dwordx4 v147, s[60:61] offset:3072\n"
        "s_add_u32 m0, s73, 0x24000\n"
        "s_waitcnt lgkmcnt(7)\n"
        "v_mfma_f32_16x16x32_bf16 v[16:19], a[40:43], a[200:203], v[16:19]\n"
        "ds_read_b128 a[196:199], v149 offset:9216\n"
        "s_add_u32 s60, s22, 0x160000\n"
        "s_addc_u32 s61, s23, 0\n"
        "v_mfma_f32_16x16x32_bf16 v[20:23], a[44:47], a[200:203], v[20:23]\n"
        "ds_read_b128 a[200:203], v149 offset:10240\n"
        "v_mfma_f32_16x16x32_bf16 v[24:27], a[40:43], a[204:207], v[24:27]\n"
        "global_load_lds_dwordx4 v254, s[20:21] offset:0 sc1\n"
        "v_mfma_f32_16x16x32_bf16 v[28:31], a[44:47], a[204:207], v[28:31]\n"
        "ds_read_b128 a[204:207], v149 offset:11264\n"
        "v_mfma_f32_16x16x32_bf16 v[32:35], a[40:43], a[208:211], v[32:35]\n"
        "v_mfma_f32_16x16x32_bf16 v[36:39], a[44:47], a[208:211], v[36:39]\n"
        "global_load_lds_dwordx4 v254, s[20:21] offset:1024 sc1\n"
        "ds_read_b128 a[208:211], v152 offset:8192\n"
        "v_mfma_f32_16x16x32_bf16 v[40:43], a[40:43], a[212:215], v[40:43]\n"
        "v_mfma_f32_16x16x32_bf16 v[44:47], a[44:47], a[212:215], v[44:47]\n"
        "ds_read_b128 a[212:215], v152 offset:9216\n"
        "v_mfma_f32_16x16x32_bf16 v[48:51], a[40:43], a[216:219], v[48:51]\n"
        "global_load_lds_dwordx4 v254, s[20:21] offset:2048 sc1\n"
        "v_mfma_f32_16x16x32_bf16 v[52:55], a[44:47], a[216:219], v[52:55]\n"
        "ds_read_b128 a[216:219], v152 offset:10240\n"
        "v_mfma_f32_16x16x32_bf16 v[56:59], a[40:43], a[220:223], v[56:59]\n"
        "s_waitcnt lgkmcnt(4)\n"
        "v_mfma_f32_16x16x32_bf16 v[60:63], a[44:47], a[220:223], v[60:63]\n"
        "global_load_lds_dwordx4 v254, s[20:21] offset:3072 sc1\n"
        "s_add_u32 s20, s20, 0x4000\n"
        "s_addc_u32 s21, s21, 0\n"
        "v_mfma_f32_16x16x32_bf16 v[0:3], v[192:195], a[0:3], v[0:3]\n"
        "ds_read_b128 a[220:223], v152 offset:11264\n"
        "s_add_u32 m0, s73, 0x8000\n"
        "v_mfma_f32_16x16x32_bf16 v[4:7], v[196:199], a[0:3], v[4:7]\n"
        "v_mfma_f32_16x16x32_bf16 v[8:11], v[192:195], a[4:7], v[8:11]\n"
        "v_mfma_f32_16x16x32_bf16 v[12:15], v[196:199], a[4:7], v[12:15]\n"
        "v_mfma_f32_16x16x32_bf16 v[16:19], v[192:195], a[8:11], v[16:19]\n"
        "v_mfma_f32_16x16x32_bf16 v[20:23], v[196:199], a[8:11], v[20:23]\n"
        "v_mfma_f32_16x16x32_bf16 v[24:27], v[192:195], a[12:15], v[24:27]\n"
        "v_mfma_f32_16x16x32_bf16 v[28:31], v[196:199], a[12:15], v[28:31]\n"
        "v_mfma_f32_16x16x32_bf16 v[32:35], v[192:195], a[16:19], v[32:35]\n"
        "v_mfma_f32_16x16x32_bf16 v[36:39], v[196:199], a[16:19], v[36:39]\n"
        "s_waitcnt vmcnt(24)\n"
        "s_barrier\n"
        "v_mfma_f32_16x16x32_bf16 v[40:43], v[192:195], a[20:23], v[40:43]\n"
        "global_load_lds_dwordx4 v254, s[20:21] offset:0 sc1\n"
        "ds_read_b64_tr_b16 a[48:49], v156 offset:16384\n"
        "v_mfma_f32_16x16x32_bf16 v[44:47], v[196:199], a[20:23], v[44:47]\n"
        "ds_read_b64_tr_b16 a[50:51], v156 offset:20480\n"
        "v_mfma_f32_16x16x32_bf16 v[48:51], v[192:195], a[24:27], v[48:51]\n"
        "ds_read_b64_tr_b16 a[52:53], v157 offset:16384\n"
        "v_mfma_f32_16x16x32_bf16 v[52:55], v[196:199], a[24:27], v[52:55]\n"
        "ds_read_b64_tr_b16 a[54:55], v157 offset:20480\n"
        "global_load_lds_dwordx4 v254, s[20:21] offset:1024 sc1\n"
        "v_mfma_f32_16x16x32_bf16 v[56:59], v[192:195], a[28:31], v[56:59]\n"
        "ds_read_b128 a[0:3], v148 offset:0\n"
        "v_mfma_f32_16x16x32_bf16 v[60:63], v[196:199], a[28:31], v[60:63]\n"
        "ds_read_b128 a[4:7], v148 offset:1024\n"
        "v_mfma_f32_16x16x32_bf16 v[0:3], v[200:203], a[192:195], v[0:3]\n"
        "ds_read_b128 a[8:11], v148 offset:2048\n"
        "global_load_lds_dwordx4 v254, s[20:21] offset:2048 sc1\n"
        "v_mfma_f32_16x16x32_bf16 v[4:7], v[204:207], a[192:195], v[4:7]\n"
        "ds_read_b128 a[12:15], v148 offset:3072\n"
        "s_waitcnt lgkmcnt(8)\n"
        "v_mfma_f32_16x16x32_bf16 v[8:11], v[200:203], a[196:199], v[8:11]\n"
        "ds_read_b128 a[16:19], v151 offset:0\n"
        "v_mfma_f32_16x16x32_bf16 v[12:15], v[204:207], a[196:199], v[12:15]\n"
        "global_load_lds_dwordx4 v254, s[20:21] offset:3072 sc1\n"
        "s_add_u32 m0, s73, 0xc000\n"
        "v_mfma_f32_16x16x32_bf16 v[16:19], v[200:203], a[200:203], v[16:19]\n"
        "ds_read_b128 a[20:23], v151 offset:1024\n"
        "s_add_u32 s20, s20, 0x4000\n"
        "s_addc_u32 s21, s21, 0\n"
        "v_mfma_f32_16x16x32_bf16 v[20:23], v[204:207], a[200:203], v[20:23]\n"
        "ds_read_b128 a[24:27], v151 offset:2048\n"
        "v_mfma_f32_16x16x32_bf16 v[24:27], v[200:203], a[204:207], v[24:27]\n"
        "global_load_lds_dwordx4 v144, s[60:61] offset:0\n"
        "v_mfma_f32_16x16x32_bf16 v[28:31], v[204:207], a[204:207], v[28:31]\n"
        "ds_read_b128 a[28:31], v151 offset:3072\n"
        "v_mfma_f32_16x16x32_bf16 v[32:35], v[200:203], a[208:211], v[32:35]\n"
        "ds_read_b64_tr_b16 a[56:57], v156 offset:24576\n"
        "v_mfma_f32_16x16x32_bf16 v[36:39], v[204:207], a[208:211], v[36:39]\n"
        "global_load_lds_dwordx4 v145, s[60:61] offset:1024\n"
        "v_mfma_f32_16x16x32_bf16 v[40:43], v[200:203], a[212:215], v[40:43]\n"
        "ds_read_b64_tr_b16 a[58:59], v156 offset:28672\n"
        "s_waitcnt lgkmcnt(5)\n"
        "v_mfma_f32_16x16x32_bf16 v[44:47], v[204:207], a[212:215], v[44:47]\n"
        "ds_read_b64_tr_b16 a[60:61], v157 offset:24576\n"
        "v_mfma_f32_16x16x32_bf16 v[48:51], v[200:203], a[216:219], v[48:51]\n"
        "ds_read_b64_tr_b16 a[62:63], v157 offset:28672\n"
        "global_load_lds_dwordx4 v146, s[60:61] offset:2048\n"
        "v_mfma_f32_16x16x32_bf16 v[52:55], v[204:207], a[216:219], v[52:55]\n"
        "ds_read_b128 a[192:195], v148 offset:8192\n"
        "v_mfma_f32_16x16x32_bf16 v[56:59], v[200:203], a[220:223], v[56:59]\n"
        "ds_read_b128 a[196:199], v148 offset:9216\n"
        "v_mfma_f32_16x16x32_bf16 v[60:63], v[204:207], a[220:223], v[60:63]\n"
        "ds_read_b128 a[200:203], v148 offset:10240\n"
        "global_load_lds_dwordx4 v147, s[60:61] offset:3072\n"
        "s_add_u32 m0, s73, 0x10000\n"
        "v_mfma_f32_16x16x32_bf16 v[0:3], a[48:51], a[0:3], v[0:3]\n"
        "ds_read_b128 a[204:207], v148 offset:11264\n"
        "s_add_u32 s60, s22, 0x180000\n"
        "s_addc_u32 s61, s23, 0\n"
        "v_mfma_f32_16x16x32_bf16 v[4:7], a[52:55], a[0:3], v[4:7]\n"
        "ds_read_b128 a[208:211], v151 offset:8192\n"
        "v_mfma_f32_16x16x32_bf16 v[8:11], a[48:51], a[4:7], v[8:11]\n"
        "ds_read_b128 a[212:215], v151 offset:9216\n"
        "v_mfma_f32_16x16x32_bf16 v[12:15], a[52:55], a[4:7], v[12:15]\n"
        "ds_read_b128 a[216:219], v151 offset:10240\n"
        "v_mfma_f32_16x16x32_bf16 v[16:19], a[48:51], a[8:11], v[16:19]\n"
        "ds_read_b128 a[220:223], v151 offset:11264\n"
        "s_waitcnt lgkmcnt(8)\n"
        "v_mfma_f32_16x16x32_bf16 v[20:23], a[52:55], a[8:11], v[20:23]\n"
        "v_mfma_f32_16x16x32_bf16 v[24:27], a[48:51], a[12:15], v[24:27]\n"
        "v_mfma_f32_16x16x32_bf16 v[28:31], a[52:55], a[12:15], v[28:31]\n"
        "v_mfma_f32_16x16x32_bf16 v[32:35], a[48:51], a[16:19], v[32:35]\n"
        "v_mfma_f32_16x16x32_bf16 v[36:39], a[52:55], a[16:19], v[36:39]\n"
        "s_waitcnt vmcnt(20)\n"
        "s_barrier\n"
        "v_mfma_f32_16x16x32_bf16 v[40:43], a[48:51], a[20:23], v[40:43]\n"
        "global_load_lds_dwordx4 v254, s[20:21] offset:0 sc1\n"
        "ds_read_b64_tr_b16 a[32:33], v154 offset:16384\n"
        "v_mfma_f32_16x16x32_bf16 v[44:47], a[52:55], a[20:23], v[44:47]\n"
        "ds_read_b64_tr_b16 a[34:35], v154 offset:20480\n"
        "s_waitcnt lgkmcnt(3)\n"
        "v_mfma_f32_16x16x32_bf16 v[48:51], a[48:51], a[24:27], v[48:51]\n"
        "ds_read_b64_tr_b16 a[36:37], v155 offset:16384\n"
        "v_mfma_f32_16x16x32_bf16 v[52:55], a[52:55], a[24:27], v[52:55]\n"
        "ds_read_b64_tr_b16 a[38:39], v155 offset:20480\n"
        "global_load_lds_dwordx4 v254, s[20:21] offset:1024 sc1\n"
        "v_mfma_f32_16x16x32_bf16 v[56:59], a[48:51], a[28:31], v[56:59]\n"
        "ds_read_b128 a[0:3], v149 offset:32768\n"
        "v_mfma_f32_16x16x32_bf16 v[60:63], a[52:55], a[28:31], v[60:63]\n"
        "ds_read_b128 a[4:7], v149 offset:33792\n"
        "v_mfma_f32_16x16x32_bf16 v[0:3], a[56:59], a[192:195], v[0:3]\n"
        "ds_read_b128 a[8:11], v149 offset:34816\n"
        "global_load_lds_dwordx4 v254, s[20:21] offset:2048 sc1\n"
        "v_mfma_f32_16x16x32_bf16 v[4:7], a[60:63], a[192:195], v[4:7]\n"
        "ds_read_b128 a[12:15], v149 offset:35840\n"
        "v_mfma_f32_16x16x32_bf16 v[8:11], a[56:59], a[196:199], v[8:11]\n"
        "ds_read_b128 a[16:19], v152 offset:32768\n"
        "v_mfma_f32_16x16x32_bf16 v[12:15], a[60:63], a[196:199], v[12:15]\n"
        "global_load_lds_dwordx4 v254, s[20:21] offset:3072 sc1\n"
        "v_mfma_f32_16x16x32_bf16 v[16:19], a[56:59], a[200:203], v[16:19]\n"
        "ds_read_b128 a[20:23], v152 offset:33792\n"
        "s_add_u32 s20, s20, 0x4000\n"
        "s_addc_u32 s21, s21, 0\n"
        "v_mfma_f32_16x16x32_bf16 v[20:23], a[60:63], a[200:203], v[20:23]\n"
        "ds_read_b128 a[24:27], v152 offset:34816\n"
        "s_add_u32 m0, s73, 0x14000\n"
        "v_mfma_f32_16x16x32_bf16 v[24:27], a[56:59], a[204:207], v[24:27]\n"
        "ds_read_b128 a[28:31], v152 offset:35840\n"
        "v_mfma_f32_16x16x32_bf16 v[28:31], a[60:63], a[204:207], v[28:31]\n"
        "ds_read_b64_tr_b16 a[40:41], v154 offset:24576\n"
        "v_mfma_f32_16x16x32_bf16 v[32:35], a[56:59], a[208:211], v[32:35]\n"
        "ds_read_b64_tr_b16 a[42:43], v154 offset:28672\n"
        "s_waitcnt lgkmcnt(7)\n"
        "v_mfma_f32_16x16x32_bf16 v[36:39], a[60:63], a[208:211], v[36:39]\n"
        "ds_read_b64_tr_b16 a[44:45], v155 offset:24576\n"
        "v_mfma_f32_16x16x32_bf16 v[40:43], a[56:59], a[212:215], v[40:43]\n"
        "ds_read_b64_tr_b16 a[46:47], v155 offset:28672\n"
        "v_mfma_f32_16x16x32_bf16 v[44:47], a[60:63], a[212:215], v[44:47]\n"
        "ds_read_b128 a[192:195], v149 offset:40960\n"
        "v_mfma_f32_16x16x32_bf16 v[48:51], a[56:59], a[216:219], v[48:51]\n"
        "ds_read_b128 a[196:199], v149 offset:41984\n"
        "v_mfma_f32_16x16x32_bf16 v[52:55], a[60:63], a[216:219], v[52:55]\n"
        "ds_read_b128 a[200:203], v149 offset:43008\n"
        "v_mfma_f32_16x16x32_bf16 v[56:59], a[56:59], a[220:223], v[56:59]\n"
        "ds_read_b128 a[204:207], v149 offset:44032\n"
        "v_mfma_f32_16x16x32_bf16 v[60:63], a[60:63], a[220:223], v[60:63]\n"
        "ds_read_b128 a[208:211], v152 offset:40960\n"
        "v_mfma_f32_16x16x32_bf16 v[0:3], a[32:35], a[0:3], v[0:3]\n"
        "ds_read_b128 a[212:215], v152 offset:41984\n"
        "s_waitcnt lgkmcnt(8)\n"
        "v_mfma_f32_16x16x32_bf16 v[4:7], a[36:39], a[0:3], v[4:7]\n"
        "ds_read_b128 a[216:219], v152 offset:43008\n"
        "v_mfma_f32_16x16x32_bf16 v[8:11], a[32:35], a[4:7], v[8:11]\n"
        "ds_read_b128 a[220:223], v152 offset:44032\n"
        "v_mfma_f32_16x16x32_bf16 v[12:15], a[36:39], a[4:7], v[12:15]\n"
        "v_mfma_f32_16x16x32_bf16 v[16:19], a[32:35], a[8:11], v[16:19]\n"
        "v_mfma_f32_16x16x32_bf16 v[20:23], a[36:39], a[8:11], v[20:23]\n"
        "v_mfma_f32_16x16x32_bf16 v[24:27], a[32:35], a[12:15], v[24:27]\n"
        "v_mfma_f32_16x16x32_bf16 v[28:31], a[36:39], a[12:15], v[28:31]\n"
        "v_mfma_f32_16x16x32_bf16 v[32:35], a[32:35], a[16:19], v[32:35]\n"
        "v_mfma_f32_16x16x32_bf16 v[36:39], a[36:39], a[16:19], v[36:39]\n"
        "s_waitcnt vmcnt(20)\n"
        "s_barrier\n"
        "v_mfma_f32_16x16x32_bf16 v[40:43], a[32:35], a[20:23], v[40:43]\n"
        "global_load_lds_dwordx4 v144, s[60:61] offset:0\n"
        "ds_read_b128 a[0:3], v149 offset:49152\n"
        "v_mfma_f32_16x16x32_bf16 v[44:47], a[36:39], a[20:23], v[44:47]\n"
        "ds_read_b128 a[4:7], v149 offset:50176\n"
        "s_waitcnt lgkmcnt(2)\n"
        "v_mfma_f32_16x16x32_bf16 v[48:51], a[32:35], a[24:27], v[48:51]\n"
        "ds_read_b128 a[8:11], v149 offset:51200\n"
        "v_mfma_f32_16x16x32_bf16 v[52:55], a[36:39], a[24:27], v[52:55]\n"
        "global_load_lds_dwordx4 v145, s[60:61] offset:1024\n"
        "ds_read_b128 a[12:15], v149 offset:52224\n"
        "v_mfma_f32_16x16x32_bf16 v[56:59], a[32:35], a[28:31], v[56:59]\n"
        "ds_read_b128 a[16:19], v152 offset:49152\n"
        "v_mfma_f32_16x16x32_bf16 v[60:63], a[36:39], a[28:31], v[60:63]\n"
        "ds_read_b128 a[20:23], v152 offset:50176\n"
        "v_mfma_f32_16x16x32_bf16 v[0:3], a[40:43], a[192:195], v[0:3]\n"
        "global_load_lds_dwordx4 v146, s[60:61] offset:2048\n"
        "ds_read_b128 a[24:27], v152 offset:51200\n"
        "v_mfma_f32_16x16x32_bf16 v[4:7], a[44:47], a[192:195], v[4:7]\n"
        "ds_read_b128 a[28:31], v152 offset:52224\n"
        "v_mfma_f32_16x16x32_bf16 v[8:11], a[40:43], a[196:199], v[8:11]\n"
        "ds_read_b128 a[192:195], v149 offset:57344\n"
        "v_mfma_f32_16x16x32_bf16 v[12:15], a[44:47], a[196:199], v[12:15]\n"
        "global_load_lds_dwordx4 v147, s[60:61] offset:3072\n"
        "s_add_u32 m0, s73, 0x0\n"
        "v_mfma_f32_16x16x32_bf16 v[16:19], a[40:43], a[200:203], v[16:19]\n"
        "ds_read_b128 a[196:199], v149 offset:58368\n"
        "s_add_u32 s60, s22, 0x1c0000\n"
        "s_addc_u32 s61, s23, 0\n"
        "v_mfma_f32_16x16x32_bf16 v[20:23], a[44:47], a[200:203], v[20:23]\n"
        "ds_read_b128 a[200:203], v149 offset:59392\n"
        "v_mfma_f32_16x16x32_bf16 v[24:27], a[40:43], a[204:207], v[24:27]\n"
        "global_load_lds_dwordx4 v254, s[20:21] offset:0 sc1\n"
        "v_mfma_f32_16x16x32_bf16 v[28:31], a[44:47], a[204:207], v[28:31]\n"
        "ds_read_b128 a[204:207], v149 offset:60416\n"
        "v_mfma_f32_16x16x32_bf16 v[32:35], a[40:43], a[208:211], v[32:35]\n"
        "v_mfma_f32_16x16x32_bf16 v[36:39], a[44:47], a[208:211], v[36:39]\n"
        "global_load_lds_dwordx4 v254, s[20:21] offset:1024 sc1\n"
        "ds_read_b128 a[208:211], v152 offset:57344\n"
        "v_mfma_f32_16x16x32_bf16 v[40:43], a[40:43], a[212:215], v[40:43]\n"
        "s_waitcnt lgkmcnt(4)\n"
        "v_mfma_f32_16x16x32_bf16 v[44:47], a[44:47], a[212:215], v[44:47]\n"
        "ds_read_b128 a[212:215], v152 offset:58368\n"
        "v_mfma_f32_16x16x32_bf16 v[48:51], a[40:43], a[216:219], v[48:51]\n"
        "global_load_lds_dwordx4 v254, s[20:21] offset:2048 sc1\n"
        "v_mfma_f32_16x16x32_bf16 v[52:55], a[44:47], a[216:219], v[52:55]\n"
        "ds_read_b128 a[216:219], v152 offset:59392\n"
        "v_mfma_f32_16x16x32_bf16 v[56:59], a[40:43], a[220:223], v[56:59]\n"
        "v_mfma_f32_16x16x32_bf16 v[60:63], a[44:47], a[220:223], v[60:63]\n"
        "global_load_lds_dwordx4 v254, s[20:21] offset:3072 sc1\n"
        "s_add_u32 s20, s20, 0x4000\n"
        "s_addc_u32 s21, s21, 0\n"
        "v_mfma_f32_16x16x32_bf16 v[0:3], v[208:211], a[0:3], v[0:3]\n"
        "ds_read_b128 a[220:223], v152 offset:60416\n"
        "s_add_u32 m0, s73, 0x4000\n"
        "v_mfma_f32_16x16x32_bf16 v[4:7], v[212:215], a[0:3], v[4:7]\n"
        "v_mfma_f32_16x16x32_bf16 v[8:11], v[208:211], a[4:7], v[8:11]\n"
        "v_mfma_f32_16x16x32_bf16 v[12:15], v[212:215], a[4:7], v[12:15]\n"
        "v_mfma_f32_16x16x32_bf16 v[16:19], v[208:211], a[8:11], v[16:19]\n"
        "v_mfma_f32_16x16x32_bf16 v[20:23], v[212:215], a[8:11], v[20:23]\n"
        "v_mfma_f32_16x16x32_bf16 v[24:27], v[208:211], a[12:15], v[24:27]\n"
        "v_mfma_f32_16x16x32_bf16 v[28:31], v[212:215], a[12:15], v[28:31]\n"
        "v_mfma_f32_16x16x32_bf16 v[32:35], v[208:211], a[16:19], v[32:35]\n"
        "v_mfma_f32_16x16x32_bf16 v[36:39], v[212:215], a[16:19], v[36:39]\n"
        "s_waitcnt vmcnt(20)\n"
        "s_barrier\n"
        "v_mfma_f32_16x16x32_bf16 v[40:43], v[208:211], a[20:23], v[40:43]\n"
        "global_load_lds_dwordx4 v254, s[20:21] offset:0 sc1\n"
        "ds_read_b64_tr_b16 a[48:49], v158 offset:0\n"
        "v_mfma_f32_16x16x32_bf16 v[44:47], v[212:215], a[20:23], v[44:47]\n"
        "ds_read_b64_tr_b16 a[50:51], v158 offset:4096\n"
        "v_mfma_f32_16x16x32_bf16 v[48:51], v[208:211], a[24:27], v[48:51]\n"
        "ds_read_b64_tr_b16 a[52:53], v159 offset:0\n"
        "s_waitcnt lgkmcnt(3)\n"
        "v_mfma_f32_16x16x32_bf16 v[52:55], v[212:215], a[24:27], v[52:55]\n"
        "ds_read_b64_tr_b16 a[54:55], v159 offset:4096\n"
        "global_load_lds_dwordx4 v254, s[20:21] offset:1024 sc1\n"
        "v_mfma_f32_16x16x32_bf16 v[56:59], v[208:211], a[28:31], v[56:59]\n"
        "ds_read_b128 a[0:3], v150 offset:16384\n"
        "v_mfma_f32_16x16x32_bf16 v[60:63], v[212:215], a[28:31], v[60:63]\n"
        "ds_read_b128 a[4:7], v150 offset:17408\n"
        "v_mfma_f32_16x16x32_bf16 v[0:3], v[216:219], a[192:195], v[0:3]\n"
        "ds_read_b128 a[8:11], v150 offset:18432\n"
        "global_load_lds_dwordx4 v254, s[20:21] offset:2048 sc1\n"
        "v_mfma_f32_16x16x32_bf16 v[4:7], v[220:223], a[192:195], v[4:7]\n"
        "ds_read_b128 a[12:15], v150 offset:19456\n"
        "v_mfma_f32_16x16x32_bf16 v[8:11], v[216:219], a[196:199], v[8:11]\n"
        "ds_read_b128 a[16:19], v153 offset:16384\n"
        "v_mfma_f32_16x16x32_bf16 v[12:15], v[220:223], a[196:199], v[12:15]\n"
        "global_load_lds_dwordx4 v254, s[20:21] offset:3072 sc1\n"
        "s_add_u32 m0, s73, 0x18000\n"
        "v_mfma_f32_16x16x32_bf16 v[16:19], v[216:219], a[200:203], v[16:19]\n"
        "ds_read_b128 a[20:23], v153 offset:17408\n"
        "s_add_u32 s20, s20, 0x4000\n"
        "s_addc_u32 s21, s21, 0\n"
        "v_mfma_f32_16x16x32_bf16 v[20:23], v[220:223], a[200:203], v[20:23]\n"
        "ds_read_b128 a[24:27], v153 offset:18432\n"
        "v_mfma_f32_16x16x32_bf16 v[24:27], v[216:219], a[204:207], v[24:27]\n"
        "global_load_lds_dwordx4 v144, s[60:61] offset:0\n"
        "v_mfma_f32_16x16x32_bf16 v[28:31], v[220:223], a[204:207], v[28:31]\n"
        "ds_read_b128 a[28:31], v153 offset:19456\n"
        "v_mfma_f32_16x16x32_bf16 v[32:35], v[216:219], a[208:211], v[32:35]\n"
        "ds_read_b64_tr_b16 a[56:57], v158 offset:8192\n"
        "v_mfma_f32_16x16x32_bf16 v[36:39], v[220:223], a[208:211], v[36:39]\n"
        "global_load_lds_dwordx4 v145, s[60:61] offset:1024\n"
        "v_mfma_f32_16x16x32_bf16 v[40:43], v[216:219], a[212:215], v[40:43]\n"
        "ds_read_b64_tr_b16 a[58:59], v158 offset:12288\n"
        "s_waitcnt lgkmcnt(5)\n"
        "v_mfma_f32_16x16x32_bf16 v[44:47], v[220:223], a[212:215], v[44:47]\n"
        "ds_read_b64_tr_b16 a[60:61], v159 offset:8192\n"
        "v_mfma_f32_16x16x32_bf16 v[48:51], v[216:219], a[216:219], v[48:51]\n"
        "ds_read_b64_tr_b16 a[62:63], v159 offset:12288\n"
        "global_load_lds_dwordx4 v146, s[60:61] offset:2048\n"
        "v_mfma_f32_16x16x32_bf16 v[52:55], v[220:223], a[216:219], v[52:55]\n"
        "ds_read_b128 a[192:195], v150 offset:24576\n"
        "v_mfma_f32_16x16x32_bf16 v[56:59], v[216:219], a[220:223], v[56:59]\n"
        "ds_read_b128 a[196:199], v150 offset:25600\n"
        "v_mfma_f32_16x16x32_bf16 v[60:63], v[220:223], a[220:223], v[60:63]\n"
        "ds_read_b128 a[200:203], v150 offset:26624\n"
        "global_load_lds_dwordx4 v147, s[60:61] offset:3072\n"
        "s_add_u32 m0, s73, 0x1c000\n"
        "v_mfma_f32_16x16x32_bf16 v[0:3], a[48:51], a[0:3], v[0:3]\n"
        "ds_read_b128 a[204:207], v150 offset:27648\n"
        "s_add_u32 s60, s22, 0x1e0000\n"
        "s_addc_u32 s61, s23, 0\n"
        "v_mfma_f32_16x16x32_bf16 v[4:7], a[52:55], a[0:3], v[4:7]\n"
        "ds_read_b128 a[208:211], v153 offset:24576\n"
        "v_mfma_f32_16x16x32_bf16 v[8:11], a[48:51], a[4:7], v[8:11]\n"
        "ds_read_b128 a[212:215], v153 offset:25600\n"
        "v_mfma_f32_16x16x32_bf16 v[12:15], a[52:55], a[4:7], v[12:15]\n"
        "ds_read_b128 a[216:219], v153 offset:26624\n"
        "v_mfma_f32_16x16x32_bf16 v[16:19], a[48:51], a[8:11], v[16:19]\n"
        "ds_read_b128 a[220:223], v153 offset:27648\n"
        "s_waitcnt lgkmcnt(8)\n"
        "v_mfma_f32_16x16x32_bf16 v[20:23], a[52:55], a[8:11], v[20:23]\n"
        "v_mfma_f32_16x16x32_bf16 v[24:27], a[48:51], a[12:15], v[24:27]\n"
        "v_mfma_f32_16x16x32_bf16 v[28:31], a[52:55], a[12:15], v[28:31]\n"
        "v_mfma_f32_16x16x32_bf16 v[32:35], a[48:51], a[16:19], v[32:35]\n"
        "v_mfma_f32_16x16x32_bf16 v[36:39], a[52:55], a[16:19], v[36:39]\n"
        "s_waitcnt vmcnt(24)\n"
        "s_barrier\n"
        "v_mfma_f32_16x16x32_bf16 v[40:43], a[48:51], a[20:23], v[40:43]\n"
        "global_load_lds_dwordx4 v254, s[20:21] offset:0 sc1\n"
        "ds_read_b128 a[0:3], v148 offset:32768\n"
        "v_mfma_f32_16x16x32_bf16 v[44:47], a[52:55], a[20:23], v[44:47]\n"
        "ds_read_b128 a[4:7], v148 offset:33792\n"
        "s_waitcnt lgkmcnt(3)\n"
        "v_mfma_f32_16x16x32_bf16 v[48:51], a[48:51], a[24:27], v[48:51]\n"
        "ds_read_b128 a[8:11], v148 offset:34816\n"
        "v_mfma_f32_16x16x32_bf16 v[52:55], a[52:55], a[24:27], v[52:55]\n"
        "global_load_lds_dwordx4 v254, s[20:21] offset:1024 sc1\n"
        "ds_read_b128 a[12:15], v148 offset:35840\n"
        "v_mfma_f32_16x16x32_bf16 v[56:59], a[48:51], a[28:31], v[56:59]\n"
        "ds_read_b128 a[16:19], v151 offset:32768\n"
        "v_mfma_f32_16x16x32_bf16 v[60:63], a[52:55], a[28:31], v[60:63]\n"
        "ds_read_b128 a[20:23], v151 offset:33792\n"
        "v_mfma_f32_16x16x32_bf16 v[0:3], a[56:59], a[192:195], v[0:3]\n"
        "global_load_lds_dwordx4 v254, s[20:21] offset:2048 sc1\n"
        "ds_read_b128 a[24:27], v151 offset:34816\n"
        "v_mfma_f32_16x16x32_bf16 v[4:7], a[60:63], a[192:195], v[4:7]\n"
        "ds_read_b128 a[28:31], v151 offset:35840\n"
        "v_mfma_f32_16x16x32_bf16 v[8:11], a[56:59], a[196:199], v[8:11]\n"
        "ds_read_b128 a[192:195], v148 offset:40960\n"
        "v_mfma_f32_16x16x32_bf16 v[12:15], a[60:63], a[196:199], v[12:15]\n"
        "global_load_lds_dwordx4 v254, s[20:21] offset:3072 sc1\n"
        "s_add_u32 s20, s20, 0x4000\n"
        "s_addc_u32 s21, s21, 0\n"
        "v_mfma_f32_16x16x32_bf16 v[16:19], a[56:59], a[200:203], v[16:19]\n"
        "ds_read_b128 a[196:199], v148 offset:41984\n"
        "s_add_u32 m0, s73, 0x20000\n"
        "v_mfma_f32_16x16x32_bf16 v[20:23], a[60:63], a[200:203], v[20:23]\n"
        "ds_read_b128 a[200:203], v148 offset:43008\n"
        "v_mfma_f32_16x16x32_bf16 v[24:27], a[56:59], a[204:207], v[24:27]\n"
        "s_cmp_lg_u32 s40, 0\n"
        "s_cbranch_scc1 LW2_%=\n"
        "s_mov_b64 exec, 0xffff\n"
        "global_store_dwordx2 v234, v[250:251], s[48:49] sc1\n"
        "s_mov_b64 exec, -1\n"
        "LW2_%=:\n"
        "v_mfma_f32_16x16x32_bf16 v[28:31], a[60:63], a[204:207], v[28:31]\n"
        "ds_read_b128 a[204:207], v148 offset:44032\n"
        "v_bfe_u32 v234, v254, 4, 3\n"
        "v_mfma_f32_16x16x32_bf16 v[32:35], a[56:59], a[208:211], v[32:35]\n"
        "s_waitcnt lgkmcnt(5)\n"
        "v_lshl_add_u32 v234, v234, 7, s14\n"
        "v_cmp_eq_u32 s[14:15], v247, v248\n"
        "v_mfma_f32_16x16x32_bf16 v[36:39], a[60:63], a[208:211], v[36:39]\n"
        "ds_read_b128 a[208:211], v151 offset:40960\n"
        "v_cndmask_b32 v138, 0, v246, s[14:15]\n"
        "v_mfma_f32_16x16x32_bf16 v[40:43], a[56:59], a[212:215], v[40:43]\n"
        "v_cmp_eq_u32 s[14:15], v247, v249\n"
        "v_mfma_f32_16x16x32_bf16 v[44:47], a[60:63], a[212:215], v[44:47]\n"
        "ds_read_b128 a[212:215], v151 offset:41984\n"
        "v_cndmask_b32 v139, 0, v246, s[14:15]\n"
        "v_mfma_f32_16x16x32_bf16 v[48:51], a[56:59], a[216:219], v[48:51]\n"
        "s_lshl2_add_u32 s14, s13, s40\n"
        "s_add_u32 s14, s14, 3072\n"
        "s_lshl_b32 s14, s14, 7\n"
        "v_mfma_f32_16x16x32_bf16 v[52:55], a[60:63], a[216:219], v[52:55]\n"
        "ds_read_b128 a[216:219], v151 offset:43008\n"
        "v_mfma_f32_16x16x32_bf16 v[56:59], a[56:59], a[220:223], v[56:59]\n"
        "v_mfma_f32_16x16x32_bf16 v[60:63], a[60:63], a[220:223], v[60:63]\n"
        "ds_read_b128 a[220:223], v151 offset:44032\n"
        "v_mfma_f32_16x16x32_bf16 v[0:3], a[64:67], a[0:3], v[0:3]\n"
        "v_mfma_f32_16x16x32_bf16 v[4:7], a[68:71], a[0:3], v[4:7]\n"
        "v_mfma_f32_16x16x32_bf16 v[8:11], a[64:67], a[4:7], v[8:11]\n"
        "v_mfma_f32_16x16x32_bf16 v[12:15], a[68:71], a[4:7], v[12:15]\n"
        "v_mfma_f32_16x16x32_bf16 v[16:19], a[64:67], a[8:11], v[16:19]\n"
        "v_mfma_f32_16x16x32_bf16 v[20:23], a[68:71], a[8:11], v[20:23]\n"
        "v_mfma_f32_16x16x32_bf16 v[24:27], a[64:67], a[12:15], v[24:27]\n"
        "v_mfma_f32_16x16x32_bf16 v[28:31], a[68:71], a[12:15], v[28:31]\n"
        "v_mfma_f32_16x16x32_bf16 v[32:35], a[64:67], a[16:19], v[32:35]\n"
        "s_waitcnt lgkmcnt(0)\n"
        "v_mfma_f32_16x16x32_bf16 v[36:39], a[68:71], a[16:19], v[36:39]\n"
        "s_waitcnt vmcnt(20)\n"
        "s_barrier\n"
        "v_mfma_f32_16x16x32_bf16 v[40:43], a[64:67], a[20:23], v[40:43]\n"
        "global_load_lds_dwordx4 v144, s[60:61] offset:0\n"
        "ds_read_b64_tr_b16 a[32:33], v154 offset:49152\n"
        "v_mfma_f32_16x16x32_bf16 v[44:47], a[68:71], a[20:23], v[44:47]\n"
        "ds_read_b64_tr_b16 a[34:35], v154 offset:53248\n"
        "v_mfma_f32_16x16x32_bf16 v[48:51], a[64:67], a[24:27], v[48:51]\n"
        "ds_read_b64_tr_b16 a[36:37], v155 offset:49152\n"
        "v_mfma_f32_16x16x32_bf16 v[52:55], a[68:71], a[24:27], v[52:55]\n"
        "ds_read_b64_tr_b16 a[38:39], v155 offset:53248\n"
        "global_load_lds_dwordx4 v145, s[60:61] offset:1024\n"
        "v_mfma_f32_16x16x32_bf16 v[56:59], a[64:67], a[28:31], v[56:59]\n"
        "ds_read_b128 a[0:3], v149 offset:0\n"
        "v_mfma_f32_16x16x32_bf16 v[60:63], a[68:71], a[28:31], v[60:63]\n"
        "ds_read_b128 a[4:7], v149 offset:1024\n"
        "v_mfma_f32_16x16x32_bf16 v[0:3], a[72:75], a[192:195], v[0:3]\n"
        "ds_read_b128 a[8:11], v149 offset:2048\n"
        "global_load_lds_dwordx4 v146, s[60:61] offset:2048\n"
        "v_mfma_f32_16x16x32_bf16 v[4:7], a[76:79], a[192:195], v[4:7]\n"
        "ds_read_b128 a[12:15], v149 offset:3072\n"
        "v_mfma_f32_16x16x32_bf16 v[8:11], a[72:75], a[196:199], v[8:11]\n"
        "ds_read_b128 a[16:19], v152 offset:0\n"
        "v_mfma_f32_16x16x32_bf16 v[12:15], a[76:79], a[196:199], v[12:15]\n"
        "global_load_lds_dwordx4 v147, s[60:61] offset:3072\n"
        "s_add_u32 m0, s73, 0x24000\n"
        "v_mfma_f32_16x16x32_bf16 v[16:19], a[72:75], a[200:203], v[16:19]\n"
        "ds_read_b128 a[20:23], v152 offset:1024\n"
        "s_add_u32 s60, s22, 0x220000\n"
        "s_addc_u32 s61, s23, 0\n"
        "v_mfma_f32_16x16x32_bf16 v[20:23], a[76:79], a[200:203], v[20:23]\n"
        "ds_read_b128 a[24:27], v152 offset:2048\n"
        "v_mfma_f32_16x16x32_bf16 v[24:27], a[72:75], a[204:207], v[24:27]\n"
        "global_load_lds_dwordx4 v254, s[20:21] offset:0 sc1\n"
        "v_mfma_f32_16x16x32_bf16 v[28:31], a[76:79], a[204:207], v[28:31]\n"
        "ds_read_b128 a[28:31], v152 offset:3072\n"
        "v_mfma_f32_16x16x32_bf16 v[32:35], a[72:75], a[208:211], v[32:35]\n"
        "ds_read_b64_tr_b16 a[40:41], v154 offset:57344\n"
        "v_mfma_f32_16x16x32_bf16 v[36:39], a[76:79], a[208:211], v[36:39]\n"
        "global_load_lds_dwordx4 v254, s[20:21] offset:1024 sc1\n"
        "v_mfma_f32_16x16x32_bf16 v[40:43], a[72:75], a[212:215], v[40:43]\n"
        "ds_read_b64_tr_b16 a[42:43], v154 offset:61440\n"
        "s_waitcnt lgkmcnt(5)\n"
        "v_mfma_f32_16x16x32_bf16 v[44:47], a[76:79], a[212:215], v[44:47]\n"
        "ds_read_b64_tr_b16 a[44:45], v155 offset:57344\n"
        "v_mfma_f32_16x16x32_bf16 v[48:51], a[72:75], a[216:219], v[48:51]\n"
        "ds_read_b64_tr_b16 a[46:47], v155 offset:61440\n"
        "global_load_lds_dwordx4 v254, s[20:21] offset:2048 sc1\n"
        "v_mfma_f32_16x16x32_bf16 v[52:55], a[76:79], a[216:219], v[52:55]\n"
        "ds_read_b128 a[192:195], v149 offset:8192\n"
        "v_mfma_f32_16x16x32_bf16 v[56:59], a[72:75], a[220:223], v[56:59]\n"
        "ds_read_b128 a[196:199], v149 offset:9216\n"
        "v_mfma_f32_16x16x32_bf16 v[60:63], a[76:79], a[220:223], v[60:63]\n"
        "ds_read_b128 a[200:203], v149 offset:10240\n"
        "global_load_lds_dwordx4 v254, s[20:21] offset:3072 sc1\n"
        "s_add_u32 m0, s73, 0x8000\n"
        "v_mfma_f32_16x16x32_bf16 v[0:3], a[32:35], a[0:3], v[0:3]\n"
        "ds_read_b128 a[204:207], v149 offset:11264\n"
        "s_add_u32 s20, s20, 0x4000\n"
        "s_addc_u32 s21, s21, 0\n"
        "v_mfma_f32_16x16x32_bf16 v[4:7], a[36:39], a[0:3], v[4:7]\n"
        "ds_read_b128 a[208:211], v152 offset:8192\n"
        "v_mfma_f32_16x16x32_bf16 v[8:11], a[32:35], a[4:7], v[8:11]\n"
        "ds_read_b128 a[212:215], v152 offset:9216\n"
        "v_mfma_f32_16x16x32_bf16 v[12:15], a[36:39], a[4:7], v[12:15]\n"
        "ds_read_b128 a[216:219], v152 offset:10240\n"
        "v_mfma_f32_16x16x32_bf16 v[16:19], a[32:35], a[8:11], v[16:19]\n"
        "ds_read_b128 a[220:223], v152 offset:11264\n"
        "s_waitcnt lgkmcnt(8)\n"
        "v_mfma_f32_16x16x32_bf16 v[20:23], a[36:39], a[8:11], v[20:23]\n"
        "v_mfma_f32_16x16x32_bf16 v[24:27], a[32:35], a[12:15], v[24:27]\n"
        "v_mfma_f32_16x16x32_bf16 v[28:31], a[36:39], a[12:15], v[28:31]\n"
        "v_mfma_f32_16x16x32_bf16 v[32:35], a[32:35], a[16:19], v[32:35]\n"
        "v_mfma_f32_16x16x32_bf16 v[36:39], a[36:39], a[16:19], v[36:39]\n"
        "s_waitcnt vmcnt(20)\n"
        "s_barrier\n"
        "v_mfma_f32_16x16x32_bf16 v[40:43], a[32:35], a[20:23], v[40:43]\n"
        "global_load_lds_dwordx4 v254, s[20:21] offset:0 sc1\n"
        "ds_read_b64_tr_b16 a[48:49], v156 offset:16384\n"
        "v_mfma_f32_16x16x32_bf16 v[44:47], a[36:39], a[20:23], v[44:47]\n"
        "ds_read_b64_tr_b16 a[50:51], v156 offset:20480\n"
        "s_waitcnt lgkmcnt(3)\n"
        "v_mfma_f32_16x16x32_bf16 v[48:51], a[32:35], a[24:27], v[48:51]\n"
        "ds_read_b64_tr_b16 a[52:53], v157 offset:16384\n"
        "v_mfma_f32_16x16x32_bf16 v[52:55], a[36:39], a[24:27], v[52:55]\n"
        "ds_read_b64_tr_b16 a[54:55], v157 offset:20480\n"
        "global_load_lds_dwordx4 v254, s[20:21] offset:1024 sc1\n"
        "v_mfma_f32_16x16x32_bf16 v[56:59], a[32:35], a[28:31], v[56:59]\n"
        "ds_read_b128 a[0:3], v148 offset:0\n"
        "v_mfma_f32_16x16x32_bf16 v[60:63], a[36:39], a[28:31], v[60:63]\n"
        "ds_read_b128 a[4:7], v148 offset:1024\n"
        "v_mfma_f32_16x16x32_bf16 v[0:3], a[40:43], a[192:195], v[0:3]\n"
        "ds_read_b128 a[8:11], v148 offset:2048\n"
        "global_load_lds_dwordx4 v254, s[20:21] offset:2048 sc1\n"
        "v_mfma_f32_16x16x32_bf16 v[4:7], a[44:47], a[192:195], v[4:7]\n"
        "ds_read_b128 a[12:15], v148 offset:3072\n"
        "v_mfma_f32_16x16x32_bf16 v[8:11], a[40:43], a[196:199], v[8:11]\n"
        "ds_read_b128 a[16:19], v151 offset:0\n"
        "v_mfma_f32_16x16x32_bf16 v[12:15], a[44:47], a[196:199], v[12:15]\n"
        "global_load_lds_dwordx4 v254, s[20:21] offset:3072 sc1\n"
        "v_mfma_f32_16x16x32_bf16 v[16:19], a[40:43], a[200:203], v[16:19]\n"
        "ds_read_b128 a[20:23], v151 offset:1024\n"
        "s_add_u32 s20, s20, 0x4000\n"
        "s_addc_u32 s21, s21, 0\n"
        "v_mfma_f32_16x16x32_bf16 v[20:23], a[44:47], a[200:203], v[20:23]\n"
        "ds_read_b128 a[24:27], v151 offset:2048\n"
        "s_add_u32 m0, s73, 0xc000\n"
        "v_mfma_f32_16x16x32_bf16 v[24:27], a[40:43], a[204:207], v[24:27]\n"
        "ds_read_b128 a[28:31], v151 offset:3072\n"
        "v_mfma_f32_16x16x32_bf16 v[28:31], a[44:47], a[204:207], v[28:31]\n"
        "ds_read_b64_tr_b16 a[56:57], v156 offset:24576\n"
        "v_mfma_f32_16x16x32_bf16 v[32:35], a[40:43], a[208:211], v[32:35]\n"
        "ds_read_b64_tr_b16 a[58:59], v156 offset:28672\n"
        "s_waitcnt lgkmcnt(7)\n"
        "v_mfma_f32_16x16x32_bf16 v[36:39], a[44:47], a[208:211], v[36:39]\n"
        "ds_read_b64_tr_b16 a[60:61], v157 offset:24576\n"
        "v_mfma_f32_16x16x32_bf16 v[40:43], a[40:43], a[212:215], v[40:43]\n"
        "ds_read_b64_tr_b16 a[62:63], v157 offset:28672\n"
        "v_mfma_f32_16x16x32_bf16 v[44:47], a[44:47], a[212:215], v[44:47]\n"
        "ds_read_b128 a[192:195], v148 offset:8192\n"
        "v_mfma_f32_16x16x32_bf16 v[48:51], a[40:43], a[216:219], v[48:51]\n"
        "ds_read_b128 a[196:199], v148 offset:9216\n"
        "v_mfma_f32_16x16x32_bf16 v[52:55], a[44:47], a[216:219], v[52:55]\n"
        "ds_read_b128 a[200:203], v148 offset:10240\n"
        "v_mfma_f32_16x16x32_bf16 v[56:59], a[40:43], a[220:223], v[56:59]\n"
        "ds_read_b128 a[204:207], v148 offset:11264\n"
        "v_mfma_f32_16x16x32_bf16 v[60:63], a[44:47], a[220:223], v[60:63]\n"
        "ds_read_b128 a[208:211], v151 offset:8192\n"
        "v_mfma_f32_16x16x32_bf16 v[0:3], a[48:51], a[0:3], v[0:3]\n"
        "ds_read_b128 a[212:215], v151 offset:9216\n"
        "s_waitcnt lgkmcnt(8)\n"
        "v_mfma_f32_16x16x32_bf16 v[4:7], a[52:55], a[0:3], v[4:7]\n"
        "ds_read_b128 a[216:219], v151 offset:10240\n"
        "v_mfma_f32_16x16x32_bf16 v[8:11], a[48:51], a[4:7], v[8:11]\n"
        "ds_read_b128 a[220:223], v151 offset:11264\n"
        "v_mfma_f32_16x16x32_bf16 v[12:15], a[52:55], a[4:7], v[12:15]\n"
        "v_mfma_f32_16x16x32_bf16 v[16:19], a[48:51], a[8:11], v[16:19]\n"
        "v_mfma_f32_16x16x32_bf16 v[20:23], a[52:55], a[8:11], v[20:23]\n"
        "v_mfma_f32_16x16x32_bf16 v[24:27], a[48:51], a[12:15], v[24:27]\n"
        "v_mfma_f32_16x16x32_bf16 v[28:31], a[52:55], a[12:15], v[28:31]\n"
        "v_mfma_f32_16x16x32_bf16 v[32:35], a[48:51], a[16:19], v[32:35]\n"
        "v_mfma_f32_16x16x32_bf16 v[36:39], a[52:55], a[16:19], v[36:39]\n"
        "s_waitcnt vmcnt(20)\n"
        "s_barrier\n"
        "v_mfma_f32_16x16x32_bf16 v[40:43], a[48:51], a[20:23], v[40:43]\n"
        "global_load_lds_dwordx4 v144, s[60:61] offset:0\n"
        "ds_read_b128 a[0:3], v148 offset:16384\n"
        "v_mfma_f32_16x16x32_bf16 v[44:47], a[52:55], a[20:23], v[44:47]\n"
        "ds_read_b128 a[4:7], v148 offset:17408\n"
        "s_waitcnt lgkmcnt(2)\n"
        "v_mfma_f32_16x16x32_bf16 v[48:51], a[48:51], a[24:27], v[48:51]\n"
        "ds_read_b128 a[8:11], v148 offset:18432\n"
        "v_mfma_f32_16x16x32_bf16 v[52:55], a[52:55], a[24:27], v[52:55]\n"
        "global_load_lds_dwordx4 v145, s[60:61] offset:1024\n"
        "ds_read_b128 a[12:15], v148 offset:19456\n"
        "v_mfma_f32_16x16x32_bf16 v[56:59], a[48:51], a[28:31], v[56:59]\n"
        "ds_read_b128 a[16:19], v151 offset:16384\n"
        "v_mfma_f32_16x16x32_bf16 v[60:63], a[52:55], a[28:31], v[60:63]\n"
        "ds_read_b128 a[20:23], v151 offset:17408\n"
        "v_mfma_f32_16x16x32_bf16 v[0:3], a[56:59], a[192:195], v[0:3]\n"
        "global_load_lds_dwordx4 v146, s[60:61] offset:2048\n"
        "ds_read_b128 a[24:27], v151 offset:18432\n"
        "v_mfma_f32_16x16x32_bf16 v[4:7], a[60:63], a[192:195], v[4:7]\n"
        "ds_read_b128 a[28:31], v151 offset:19456\n"
        "v_mfma_f32_16x16x32_bf16 v[8:11], a[56:59], a[196:199], v[8:11]\n"
        "ds_read_b128 a[192:195], v148 offset:24576\n"
        "v_mfma_f32_16x16x32_bf16 v[12:15], a[60:63], a[196:199], v[12:15]\n"
        "global_load_lds_dwordx4 v147, s[60:61] offset:3072\n"
        "s_add_u32 m0, s73, 0x10000\n"
        "v_mfma_f32_16x16x32_bf16 v[16:19], a[56:59], a[200:203], v[16:19]\n"
        "ds_read_b128 a[196:199], v148 offset:25600\n"
        "s_add_u32 s60, s22, 0x260000\n"
        "s_addc_u32 s61, s23, 0\n"
        "v_mfma_f32_16x16x32_bf16 v[20:23], a[60:63], a[200:203], v[20:23]\n"
        "ds_read_b128 a[200:203], v148 offset:26624\n"
        "v_mfma_f32_16x16x32_bf16 v[24:27], a[56:59], a[204:207], v[24:27]\n"
        "global_load_lds_dwordx4 v254, s[20:21] offset:0 sc1\n"
        "v_mfma_f32_16x16x32_bf16 v[28:31], a[60:63], a[204:207], v[28:31]\n"
        "ds_read_b128 a[204:207], v148 offset:27648\n"
        "v_mfma_f32_16x16x32_bf16 v[32:35], a[56:59], a[208:211], v[32:35]\n"
        "v_mfma_f32_16x16x32_bf16 v[36:39], a[60:63], a[208:211], v[36:39]\n"
        "global_load_lds_dwordx4 v254, s[20:21] offset:1024 sc1\n"
        "ds_read_b128 a[208:211], v151 offset:24576\n"
        "v_mfma_f32_16x16x32_bf16 v[40:43], a[56:59], a[212:215], v[40:43]\n"
        "s_waitcnt lgkmcnt(4)\n"
        "v_mfma_f32_16x16x32_bf16 v[44:47], a[60:63], a[212:215], v[44:47]\n"
        "ds_read_b128 a[212:215], v151 offset:25600\n"
        "v_mfma_f32_16x16x32_bf16 v[48:51], a[56:59], a[216:219], v[48:51]\n"
        "global_load_lds_dwordx4 v254, s[20:21] offset:2048 sc1\n"
        "v_mfma_f32_16x16x32_bf16 v[52:55], a[60:63], a[216:219], v[52:55]\n"
        "ds_read_b128 a[216:219], v151 offset:26624\n"
        "v_mfma_f32_16x16x32_bf16 v[56:59], a[56:59], a[220:223], v[56:59]\n"
        "v_mfma_f32_16x16x32_bf16 v[60:63], a[60:63], a[220:223], v[60:63]\n"
        "global_load_lds_dwordx4 v254, s[20:21] offset:3072 sc1\n"
        "s_add_u32 s20, s20, 0x4000\n"
        "s_addc_u32 s21, s21, 0\n"
        "v_mfma_f32_16x16x32_bf16 v[0:3], a[80:83], a[0:3], v[0:3]\n"
        "ds_read_b128 a[220:223], v151 offset:27648\n"
        "s_add_u32 m0, s73, 0x14000\n"
        "v_mfma_f32_16x16x32_bf16 v[4:7], a[84:87], a[0:3], v[4:7]\n"
        "v_mfma_f32_16x16x32_bf16 v[8:11], a[80:83], a[4:7], v[8:11]\n"
        "v_mfma_f32_16x16x32_bf16 v[12:15], a[84:87], a[4:7], v[12:15]\n"
        "v_mfma_f32_16x16x32_bf16 v[16:19], a[80:83], a[8:11], v[16:19]\n"
        "v_mfma_f32_16x16x32_bf16 v[20:23], a[84:87], a[8:11], v[20:23]\n"
        "v_mfma_f32_16x16x32_bf16 v[24:27], a[80:83], a[12:15], v[24:27]\n"
        "v_mfma_f32_16x16x32_bf16 v[28:31], a[84:87], a[12:15], v[28:31]\n"
        "v_mfma_f32_16x16x32_bf16 v[32:35], a[80:83], a[16:19], v[32:35]\n"
        "v_mfma_f32_16x16x32_bf16 v[36:39], a[84:87], a[16:19], v[36:39]\n"
        "s_waitcnt vmcnt(20)\n"
        "s_barrier\n"
        "v_mfma_f32_16x16x32_bf16 v[40:43], a[80:83], a[20:23], v[40:43]\n"
        "global_load_lds_dwordx4 v254, s[20:21] offset:0 sc1\n"
        "ds_read_b64_tr_b16 a[32:33], v156 offset:32768\n"
        "v_mfma_f32_16x16x32_bf16 v[44:47], a[84:87], a[20:23], v[44:47]\n"
        "ds_read_b64_tr_b16 a[34:35], v156 offset:36864\n"
        "v_mfma_f32_16x16x32_bf16 v[48:51], a[80:83], a[24:27], v[48:51]\n"
        "ds_read_b64_tr_b16 a[36:37], v157 offset:32768\n"
        "s_waitcnt lgkmcnt(3)\n"
        "v_mfma_f32_16x16x32_bf16 v[52:55], a[84:87], a[24:27], v[52:55]\n"
        "ds_read_b64_tr_b16 a[38:39], v157 offset:36864\n"
        "global_load_lds_dwordx4 v254, s[20:21] offset:1024 sc1\n"
        "v_mfma_f32_16x16x32_bf16 v[56:59], a[80:83], a[28:31], v[56:59]\n"
        "ds_read_b128 a[0:3], v149 offset:49152\n"
        "v_mfma_f32_16x16x32_bf16 v[60:63], a[84:87], a[28:31], v[60:63]\n"
        "ds_read_b128 a[4:7], v149 offset:50176\n"
        "v_mfma_f32_16x16x32_bf16 v[0:3], a[88:91], a[192:195], v[0:3]\n"
        "ds_read_b128 a[8:11], v149 offset:51200\n"
        "global_load_lds_dwordx4 v254, s[20:21] offset:2048 sc1\n"
        "v_mfma_f32_16x16x32_bf16 v[4:7], a[92:95], a[192:195], v[4:7]\n"
        "ds_read_b128 a[12:15], v149 offset:52224\n"
        "v_mfma_f32_16x16x32_bf16 v[8:11], a[88:91], a[196:199], v[8:11]\n"
        "ds_read_b128 a[16:19], v152 offset:49152\n"
        "v_mfma_f32_16x16x32_bf16 v[12:15], a[92:95], a[196:199], v[12:15]\n"
        "global_load_lds_dwordx4 v254, s[20:21] offset:3072 sc1\n"
        "s_add_u32 m0, s73, 0x0\n"
        "v_mfma_f32_16x16x32_bf16 v[16:19], a[88:91], a[200:203], v[16:19]\n"
        "ds_read_b128 a[20:23], v152 offset:50176\n"
        "s_add_u32 s20, s20, 0x4000\n"
        "s_addc_u32 s21, s21, 0\n"
        "v_mfma_f32_16x16x32_bf16 v[20:23], a[92:95], a[200:203], v[20:23]\n"
        "ds_read_b128 a[24:27], v152 offset:51200\n"
        "v_mfma_f32_16x16x32_bf16 v[24:27], a[88:91], a[204:207], v[24:27]\n"
        "global_load_lds_dwordx4 v144, s[60:61] offset:0\n"
        "v_mfma_f32_16x16x32_bf16 v[28:31], a[92:95], a[204:207], v[28:31]\n"
        "ds_read_b128 a[28:31], v152 offset:52224\n"
        "v_mfma_f32_16x16x32_bf16 v[32:35], a[88:91], a[208:211], v[32:35]\n"
        "ds_read_b64_tr_b16 a[40:41], v156 offset:40960\n"
        "v_mfma_f32_16x16x32_bf16 v[36:39], a[92:95], a[208:211], v[36:39]\n"
        "global_load_lds_dwordx4 v145, s[60:61] offset:1024\n"
        "v_mfma_f32_16x16x32_bf16 v[40:43], a[88:91], a[212:215], v[40:43]\n"
        "ds_read_b64_tr_b16 a[42:43], v156 offset:45056\n"
        "s_waitcnt lgkmcnt(5)\n"
        "v_mfma_f32_16x16x32_bf16 v[44:47], a[92:95], a[212:215], v[44:47]\n"
        "ds_read_b64_tr_b16 a[44:45], v157 offset:40960\n"
        "v_mfma_f32_16x16x32_bf16 v[48:51], a[88:91], a[216:219], v[48:51]\n"
        "ds_read_b64_tr_b16 a[46:47], v157 offset:45056\n"
        "global_load_lds_dwordx4 v146, s[60:61] offset:2048\n"
        "v_mfma_f32_16x16x32_bf16 v[52:55], a[92:95], a[216:219], v[52:55]\n"
        "ds_read_b128 a[192:195], v149 offset:57344\n"
        "v_mfma_f32_16x16x32_bf16 v[56:59], a[88:91], a[220:223], v[56:59]\n"
        "ds_read_b128 a[196:199], v149 offset:58368\n"
        "v_mfma_f32_16x16x32_bf16 v[60:63], a[92:95], a[220:223], v[60:63]\n"
        "ds_read_b128 a[200:203], v149 offset:59392\n"
        "global_load_lds_dwordx4 v147, s[60:61] offset:3072\n"
        "s_add_u32 m0, s73, 0x4000\n"
        "v_mfma_f32_16x16x32_bf16 v[0:3], a[32:35], a[0:3], v[0:3]\n"
        "ds_read_b128 a[204:207], v149 offset:60416\n"
        "s_add_u32 s60, s22, 0x280000\n"
        "s_addc_u32 s61, s23, 0\n"
        "v_mfma_f32_16x16x32_bf16 v[4:7], a[36:39], a[0:3], v[4:7]\n"
        "ds_read_b128 a[208:211], v152 offset:57344\n"
        "v_mfma_f32_16x16x32_bf16 v[8:11], a[32:35], a[4:7], v[8:11]\n"
        "ds_read_b128 a[212:215], v152 offset:58368\n"
        "v_mfma_f32_16x16x32_bf16 v[12:15], a[36:39], a[4:7], v[12:15]\n"
        "ds_read_b128 a[216:219], v152 offset:59392\n"
        "v_mfma_f32_16x16x32_bf16 v[16:19], a[32:35], a[8:11], v[16:19]\n"
        "ds_read_b128 a[220:223], v152 offset:60416\n"
        "s_waitcnt lgkmcnt(8)\n"
        "v_mfma_f32_16x16x32_bf16 v[20:23], a[36:39], a[8:11], v[20:23]\n"
        "v_mfma_f32_16x16x32_bf16 v[24:27], a[32:35], a[12:15], v[24:27]\n"
        "v_mfma_f32_16x16x32_bf16 v[28:31], a[36:39], a[12:15], v[28:31]\n"
        "v_mfma_f32_16x16x32_bf16 v[32:35], a[32:35], a[16:19], v[32:35]\n"
        "v_mfma_f32_16x16x32_bf16 v[36:39], a[36:39], a[16:19], v[36:39]\n"
        "s_waitcnt vmcnt(20)\n"
        "s_barrier\n"
        "v_mfma_f32_16x16x32_bf16 v[40:43], a[32:35], a[20:23], v[40:43]\n"
        "global_load_lds_dwordx4 v254, s[20:21] offset:0 sc1\n"
        "ds_read_b64_tr_b16 a[48:49], v158 offset:0\n"
        "v_mfma_f32_16x16x32_bf16 v[44:47], a[36:39], a[20:23], v[44:47]\n"
        "ds_read_b64_tr_b16 a[50:51], v158 offset:4096\n"
        "s_waitcnt lgkmcnt(3)\n"
        "v_mfma_f32_16x16x32_bf16 v[48:51], a[32:35], a[24:27], v[48:51]\n"
        "ds_read_b64_tr_b16 a[52:53], v159 offset:0\n"
        "v_mfma_f32_16x16x32_bf16 v[52:55], a[36:39], a[24:27], v[52:55]\n"
        "ds_read_b64_tr_b16 a[54:55], v159 offset:4096\n"
        "global_load_lds_dwordx4 v254, s[20:21] offset:1024 sc1\n"
        "v_mfma_f32_16x16x32_bf16 v[56:59], a[32:35], a[28:31], v[56:59]\n"
        "ds_read_b128 a[0:3], v150 offset:16384\n"
        "v_mfma_f32_16x16x32_bf16 v[60:63], a[36:39], a[28:31], v[60:63]\n"
        "ds_read_b128 a[4:7], v150 offset:17408\n"
        "v_mfma_f32_16x16x32_bf16 v[0:3], a[40:43], a[192:195], v[0:3]\n"
        "ds_read_b128 a[8:11], v150 offset:18432\n"
        "global_load_lds_dwordx4 v254, s[20:21] offset:2048 sc1\n"
        "v_mfma_f32_16x16x32_bf16 v[4:7], a[44:47], a[192:195], v[4:7]\n"
        "ds_read_b128 a[12:15], v150 offset:19456\n"
        "v_mfma_f32_16x16x32_bf16 v[8:11], a[40:43], a[196:199], v[8:11]\n"
        "ds_read_b128 a[16:19], v153 offset:16384\n"
        "v_mfma_f32_16x16x32_bf16 v[12:15], a[44:47], a[196:199], v[12:15]\n"
        "global_load_lds_dwordx4 v254, s[20:21] offset:3072 sc1\n"
        "v_mfma_f32_16x16x32_bf16 v[16:19], a[40:43], a[200:203], v[16:19]\n"
        "ds_read_b128 a[20:23], v153 offset:17408\n"
        "s_add_u32 s20, s20, 0x4000\n"
        "s_addc_u32 s21, s21, 0\n"
        "v_mfma_f32_16x16x32_bf16 v[20:23], a[44:47], a[200:203], v[20:23]\n"
        "ds_read_b128 a[24:27], v153 offset:18432\n"
        "s_add_u32 m0, s73, 0x18000\n"
        "v_mfma_f32_16x16x32_bf16 v[24:27], a[40:43], a[204:207], v[24:27]\n"
        "ds_read_b128 a[28:31], v153 offset:19456\n"
        "v_mfma_f32_16x16x32_bf16 v[28:31], a[44:47], a[204:207], v[28:31]\n"
        "ds_read_b64_tr_b16 a[56:57], v158 offset:8192\n"
        "v_mfma_f32_16x16x32_bf16 v[32:35], a[40:43], a[208:211], v[32:35]\n"
        "ds_read_b64_tr_b16 a[58:59], v158 offset:12288\n"
        "s_waitcnt lgkmcnt(7)\n"
        "v_mfma_f32_16x16x32_bf16 v[36:39], a[44:47], a[208:211], v[36:39]\n"
        "ds_read_b64_tr_b16 a[60:61], v159 offset:8192\n"
        "v_mfma_f32_16x16x32_bf16 v[40:43], a[40:43], a[212:215], v[40:43]\n"
        "ds_read_b64_tr_b16 a[62:63], v159 offset:12288\n"
        "v_mfma_f32_16x16x32_bf16 v[44:47], a[44:47], a[212:215], v[44:47]\n"
        "ds_read_b128 a[192:195], v150 offset:24576\n"
        "v_mfma_f32_16x16x32_bf16 v[48:51], a[40:43], a[216:219], v[48:51]\n"
        "ds_read_b128 a[196:199], v150 offset:25600\n"
        "v_mfma_f32_16x16x32_bf16 v[52:55], a[44:47], a[216:219], v[52:55]\n"
        "ds_read_b128 a[200:203], v150 offset:26624\n"
        "v_mfma_f32_16x16x32_bf16 v[56:59], a[40:43], a[220:223], v[56:59]\n"
        "ds_read_b128 a[204:207], v150 offset:27648\n"
        "v_mfma_f32_16x16x32_bf16 v[60:63], a[44:47], a[220:223], v[60:63]\n"
        "ds_read_b128 a[208:211], v153 offset:24576\n"
        "v_mfma_f32_16x16x32_bf16 v[0:3], a[48:51], a[0:3], v[0:3]\n"
        "ds_read_b128 a[212:215], v153 offset:25600\n"
        "s_waitcnt lgkmcnt(8)\n"
        "v_mfma_f32_16x16x32_bf16 v[4:7], a[52:55], a[0:3], v[4:7]\n"
        "ds_read_b128 a[216:219], v153 offset:26624\n"
        "v_mfma_f32_16x16x32_bf16 v[8:11], a[48:51], a[4:7], v[8:11]\n"
        "ds_read_b128 a[220:223], v153 offset:27648\n"
        "v_mfma_f32_16x16x32_bf16 v[12:15], a[52:55], a[4:7], v[12:15]\n"
        "v_mfma_f32_16x16x32_bf16 v[16:19], a[48:51], a[8:11], v[16:19]\n"
        "v_mfma_f32_16x16x32_bf16 v[20:23], a[52:55], a[8:11], v[20:23]\n"
        "v_mfma_f32_16x16x32_bf16 v[24:27], a[48:51], a[12:15], v[24:27]\n"
        "v_mfma_f32_16x16x32_bf16 v[28:31], a[52:55], a[12:15], v[28:31]\n"
        "v_mfma_f32_16x16x32_bf16 v[32:35], a[48:51], a[16:19], v[32:35]\n"
        "v_mfma_f32_16x16x32_bf16 v[36:39], a[52:55], a[16:19], v[36:39]\n"
        "s_waitcnt vmcnt(20)\n"
        "s_barrier\n"
        "v_mfma_f32_16x16x32_bf16 v[40:43], a[48:51], a[20:23], v[40:43]\n"
        "global_load_lds_dwordx4 v144, s[60:61] offset:0\n"
        "ds_read_b128 a[0:3], v148 offset:32768\n"
        "v_mfma_f32_16x16x32_bf16 v[44:47], a[52:55], a[20:23], v[44:47]\n"
        "ds_read_b128 a[4:7], v148 offset:33792\n"
        "s_waitcnt lgkmcnt(2)\n"
        "v_mfma_f32_16x16x32_bf16 v[48:51], a[48:51], a[24:27], v[48:51]\n"
        "ds_read_b128 a[8:11], v148 offset:34816\n"
        "v_mfma_f32_16x16x32_bf16 v[52:55], a[52:55], a[24:27], v[52:55]\n"
        "global_load_lds_dwordx4 v145, s[60:61] offset:1024\n"
        "ds_read_b128 a[12:15], v148 offset:35840\n"
        "v_mfma_f32_16x16x32_bf16 v[56:59], a[48:51], a[28:31], v[56:59]\n"
        "ds_read_b128 a[16:19], v151 offset:32768\n"
        "v_mfma_f32_16x16x32_bf16 v[60:63], a[52:55], a[28:31], v[60:63]\n"
        "ds_read_b128 a[20:23], v151 offset:33792\n"
        "v_mfma_f32_16x16x32_bf16 v[0:3], a[56:59], a[192:195], v[0:3]\n"
        "global_load_lds_dwordx4 v146, s[60:61] offset:2048\n"
        "ds_read_b128 a[24:27], v151 offset:34816\n"
        "v_mfma_f32_16x16x32_bf16 v[4:7], a[60:63], a[192:195], v[4:7]\n"
        "ds_read_b128 a[28:31], v151 offset:35840\n"
        "v_mfma_f32_16x16x32_bf16 v[8:11], a[56:59], a[196:199], v[8:11]\n"
        "ds_read_b128 a[192:195], v148 offset:40960\n"
        "v_mfma_f32_16x16x32_bf16 v[12:15], a[60:63], a[196:199], v[12:15]\n"
        "global_load_lds_dwordx4 v147, s[60:61] offset:3072\n"
        "s_add_u32 m0, s73, 0x1c000\n"
        "v_mfma_f32_16x16x32_bf16 v[16:19], a[56:59], a[200:203], v[16:19]\n"
        "ds_read_b128 a[196:199], v148 offset:41984\n"
        "s_add_u32 s60, s22, 0x2c0000\n"
        "s_addc_u32 s61, s23, 0\n"
        "v_mfma_f32_16x16x32_bf16 v[20:23], a[60:63], a[200:203], v[20:23]\n"
        "ds_read_b128 a[200:203], v148 offset:43008\n"
        "v_mfma_f32_16x16x32_bf16 v[24:27], a[56:59], a[204:207], v[24:27]\n"
        "global_load_lds_dwordx4 v254, s[20:21] offset:0 sc1\n"
        "v_mfma_f32_16x16x32_bf16 v[28:31], a[60:63], a[204:207], v[28:31]\n"
        "ds_read_b128 a[204:207], v148 offset:44032\n"
        "v_mfma_f32_16x16x32_bf16 v[32:35], a[56:59], a[208:211], v[32:35]\n"
        "v_mfma_f32_16x16x32_bf16 v[36:39], a[60:63], a[208:211], v[36:39]\n"
        "global_load_lds_dwordx4 v254, s[20:21] offset:1024 sc1\n"
        "ds_read_b128 a[208:211], v151 offset:40960\n"
        "v_mfma_f32_16x16x32_bf16 v[40:43], a[56:59], a[212:215], v[40:43]\n"
        "s_waitcnt lgkmcnt(4)\n"
        "v_mfma_f32_16x16x32_bf16 v[44:47], a[60:63], a[212:215], v[44:47]\n"
        "ds_read_b128 a[212:215], v151 offset:41984\n"
        "v_mfma_f32_16x16x32_bf16 v[48:51], a[56:59], a[216:219], v[48:51]\n"
        "global_load_lds_dwordx4 v254, s[20:21] offset:2048 sc1\n"
        "v_mfma_f32_16x16x32_bf16 v[52:55], a[60:63], a[216:219], v[52:55]\n"
        "ds_read_b128 a[216:219], v151 offset:43008\n"
        "v_mfma_f32_16x16x32_bf16 v[56:59], a[56:59], a[220:223], v[56:59]\n"
        "v_mfma_f32_16x16x32_bf16 v[60:63], a[60:63], a[220:223], v[60:63]\n"
        "global_load_lds_dwordx4 v254, s[20:21] offset:3072 sc1\n"
        "s_add_u32 s20, s20, 0x4000\n"
        "s_addc_u32 s21, s21, 0\n"
        "v_mfma_f32_16x16x32_bf16 v[0:3], a[96:99], a[0:3], v[0:3]\n"
        "ds_read_b128 a[220:223], v151 offset:44032\n"
        "s_add_u32 m0, s73, 0x20000\n"
        "v_mfma_f32_16x16x32_bf16 v[4:7], a[100:103], a[0:3], v[4:7]\n"
        "v_mfma_f32_16x16x32_bf16 v[8:11], a[96:99], a[4:7], v[8:11]\n"
        "v_mfma_f32_16x16x32_bf16 v[12:15], a[100:103], a[4:7], v[12:15]\n"
        "v_mfma_f32_16x16x32_bf16 v[16:19], a[96:99], a[8:11], v[16:19]\n"
        "v_mfma_f32_16x16x32_bf16 v[20:23], a[100:103], a[8:11], v[20:23]\n"
        "v_mfma_f32_16x16x32_bf16 v[24:27], a[96:99], a[12:15], v[24:27]\n"
        "v_mfma_f32_16x16x32_bf16 v[28:31], a[100:103], a[12:15], v[28:31]\n"
        "v_mfma_f32_16x16x32_bf16 v[32:35], a[96:99], a[16:19], v[32:35]\n"
        "v_mfma_f32_16x16x32_bf16 v[36:39], a[100:103], a[16:19], v[36:39]\n"
        "s_waitcnt vmcnt(20)\n"
        "s_barrier\n"
        "v_mfma_f32_16x16x32_bf16 v[40:43], a[96:99], a[20:23], v[40:43]\n"
        "global_load_lds_dwordx4 v254, s[20:21] offset:0 sc1\n"
        "ds_read_b64_tr_b16 a[32:33], v154 offset:49152\n"
        "v_mfma_f32_16x16x32_bf16 v[44:47], a[100:103], a[20:23], v[44:47]\n"
        "ds_read_b64_tr_b16 a[34:35], v154 offset:53248\n"
        "v_mfma_f32_16x16x32_bf16 v[48:51], a[96:99], a[24:27], v[48:51]\n"
        "ds_read_b64_tr_b16 a[36:37], v155 offset:49152\n"
        "s_waitcnt lgkmcnt(3)\n"
        "v_mfma_f32_16x16x32_bf16 v[52:55], a[100:103], a[24:27], v[52:55]\n"
        "ds_read_b64_tr_b16 a[38:39], v155 offset:53248\n"
        "global_load_lds_dwordx4 v254, s[20:21] offset:1024 sc1\n"
        "v_mfma_f32_16x16x32_bf16 v[56:59], a[96:99], a[28:31], v[56:59]\n"
        "ds_read_b128 a[0:3], v149 offset:0\n"
        "v_mfma_f32_16x16x32_bf16 v[60:63], a[100:103], a[28:31], v[60:63]\n"
        "ds_read_b128 a[4:7], v149 offset:1024\n"
        "v_mfma_f32_16x16x32_bf16 v[0:3], a[104:107], a[192:195], v[0:3]\n"
        "ds_read_b128 a[8:11], v149 offset:2048\n"
        "global_load_lds_dwordx4 v254, s[20:21] offset:2048 sc1\n"
        "v_mfma_f32_16x16x32_bf16 v[4:7], a[108:111], a[192:195], v[4:7]\n"
        "ds_read_b128 a[12:15], v149 offset:3072\n"
        "v_mfma_f32_16x16x32_bf16 v[8:11], a[104:107], a[196:199], v[8:11]\n"
        "ds_read_b128 a[16:19], v152 offset:0\n"
        "v_mfma_f32_16x16x32_bf16 v[12:15], a[108:111], a[196:199], v[12:15]\n"
        "global_load_lds_dwordx4 v254, s[20:21] offset:3072 sc1\n"
        "s_add_u32 m0, s73, 0x24000\n"
        "v_mfma_f32_16x16x32_bf16 v[16:19], a[104:107], a[200:203], v[16:19]\n"
        "ds_read_b128 a[20:23], v152 offset:1024\n"
        "s_add_u32 s20, s20, 0x4000\n"
        "s_addc_u32 s21, s21, 0\n"
        "v_mfma_f32_16x16x32_bf16 v[20:23], a[108:111], a[200:203], v[20:23]\n"
        "ds_read_b128 a[24:27], v152 offset:2048\n"
        "v_mfma_f32_16x16x32_bf16 v[24:27], a[104:107], a[204:207], v[24:27]\n"
        "global_load_lds_dwordx4 v144, s[60:61] offset:0\n"
        "v_mfma_f32_16x16x32_bf16 v[28:31], a[108:111], a[204:207], v[28:31]\n"
        "ds_read_b128 a[28:31], v152 offset:3072\n"
        "v_mfma_f32_16x16x32_bf16 v[32:35], a[104:107], a[208:211], v[32:35]\n"
        "ds_read_b64_tr_b16 a[40:41], v154 offset:57344\n"
        "v_mfma_f32_16x16x32_bf16 v[36:39], a[108:111], a[208:211], v[36:39]\n"
        "global_load_lds_dwordx4 v145, s[60:61] offset:1024\n"
        "v_mfma_f32_16x16x32_bf16 v[40:43], a[104:107], a[212:215], v[40:43]\n"
        "ds_read_b64_tr_b16 a[42:43], v154 offset:61440\n"
        "s_waitcnt lgkmcnt(5)\n"
        "v_mfma_f32_16x16x32_bf16 v[44:47], a[108:111], a[212:215], v[44:47]\n"
        "ds_read_b64_tr_b16 a[44:45], v155 offset:57344\n"
        "v_mfma_f32_16x16x32_bf16 v[48:51], a[104:107], a[216:219], v[48:51]\n"
        "ds_read_b64_tr_b16 a[46:47], v155 offset:61440\n"
        "global_load_lds_dwordx4 v146, s[60:61] offset:2048\n"
        "v_mfma_f32_16x16x32_bf16 v[52:55], a[108:111], a[216:219], v[52:55]\n"
        "ds_read_b128 a[192:195], v149 offset:8192\n"
        "v_mfma_f32_16x16x32_bf16 v[56:59], a[104:107], a[220:223], v[56:59]\n"
        "ds_read_b128 a[196:199], v149 offset:9216\n"
        "v_mfma_f32_16x16x32_bf16 v[60:63], a[108:111], a[220:223], v[60:63]\n"
        "ds_read_b128 a[200:203], v149 offset:10240\n"
        "global_load_lds_dwordx4 v147, s[60:61] offset:3072\n"
        "s_add_u32 m0, s73, 0x8000\n"
        "v_mfma_f32_16x16x32_bf16 v[0:3], a[32:35], a[0:3], v[0:3]\n"
        "ds_read_b128 a[204:207], v149 offset:11264\n"
        "s_add_u32 s60, s22, 0x2e0000\n"
        "s_addc_u32 s61, s23, 0\n"
        "v_mfma_f32_16x16x32_bf16 v[4:7], a[36:39], a[0:3], v[4:7]\n"
        "ds_read_b128 a[208:211], v152 offset:8192\n"
        "v_mfma_f32_16x16x32_bf16 v[8:11], a[32:35], a[4:7], v[8:11]\n"
        "ds_read_b128 a[212:215], v152 offset:9216\n"
        "v_mfma_f32_16x16x32_bf16 v[12:15], a[36:39], a[4:7], v[12:15]\n"
        "ds_read_b128 a[216:219], v152 offset:10240\n"
        "v_mfma_f32_16x16x32_bf16 v[16:19], a[32:35], a[8:11], v[16:19]\n"
        "ds_read_b128 a[220:223], v152 offset:11264\n"
        "s_waitcnt lgkmcnt(8)\n"
        "v_mfma_f32_16x16x32_bf16 v[20:23], a[36:39], a[8:11], v[20:23]\n"
        "v_mfma_f32_16x16x32_bf16 v[24:27], a[32:35], a[12:15], v[24:27]\n"
        "v_mfma_f32_16x16x32_bf16 v[28:31], a[36:39], a[12:15], v[28:31]\n"
        "v_mfma_f32_16x16x32_bf16 v[32:35], a[32:35], a[16:19], v[32:35]\n"
        "v_mfma_f32_16x16x32_bf16 v[36:39], a[36:39], a[16:19], v[36:39]\n"
        "s_waitcnt vmcnt(24)\n"
        "s_barrier\n"
        "v_mfma_f32_16x16x32_bf16 v[40:43], a[32:35], a[20:23], v[40:43]\n"
        "global_load_lds_dwordx4 v254, s[20:21] offset:0 sc1\n"
        "ds_read_b128 a[0:3], v149 offset:16384\n"
        "v_mfma_f32_16x16x32_bf16 v[44:47], a[36:39], a[20:23], v[44:47]\n"
        "ds_read_b128 a[4:7], v149 offset:17408\n"
        "s_waitcnt lgkmcnt(3)\n"
        "v_mfma_f32_16x16x32_bf16 v[48:51], a[32:35], a[24:27], v[48:51]\n"
        "ds_read_b128 a[8:11], v149 offset:18432\n"
        "v_mfma_f32_16x16x32_bf16 v[52:55], a[36:39], a[24:27], v[52:55]\n"
        "global_load_lds_dwordx4 v254, s[20:21] offset:1024 sc1\n"
        "ds_read_b128 a[12:15], v149 offset:19456\n"
        "v_mfma_f32_16x16x32_bf16 v[56:59], a[32:35], a[28:31], v[56:59]\n"
        "ds_read_b128 a[16:19], v152 offset:16384\n"
        "v_mfma_f32_16x16x32_bf16 v[60:63], a[36:39], a[28:31], v[60:63]\n"
        "ds_read_b128 a[20:23], v152 offset:17408\n"
        "v_mfma_f32_16x16x32_bf16 v[0:3], a[40:43], a[192:195], v[0:3]\n"
        "global_load_lds_dwordx4 v254, s[20:21] offset:2048 sc1\n"
        "ds_read_b128 a[24:27], v152 offset:18432\n"
        "v_mfma_f32_16x16x32_bf16 v[4:7], a[44:47], a[192:195], v[4:7]\n"
        "ds_read_b128 a[28:31], v152 offset:19456\n"
        "v_mfma_f32_16x16x32_bf16 v[8:11], a[40:43], a[196:199], v[8:11]\n"
        "ds_read_b128 a[192:195], v149 offset:24576\n"
        "v_mfma_f32_16x16x32_bf16 v[12:15], a[44:47], a[196:199], v[12:15]\n"
        "global_load_lds_dwordx4 v254, s[20:21] offset:3072 sc1\n"
        "s_add_u32 s20, s20, 0x4000\n"
        "s_addc_u32 s21, s21, 0\n"
        "v_mfma_f32_16x16x32_bf16 v[16:19], a[40:43], a[200:203], v[16:19]\n"
        "ds_read_b128 a[196:199], v149 offset:25600\n"
        "s_add_u32 m0, s73, 0xc000\n"
        "v_mfma_f32_16x16x32_bf16 v[20:23], a[44:47], a[200:203], v[20:23]\n"
        "ds_read_b128 a[200:203], v149 offset:26624\n"
        "v_mfma_f32_16x16x32_bf16 v[24:27], a[40:43], a[204:207], v[24:27]\n"
        "global_load_dwordx2 v[236:237], v234, s[48:49] sc1\n"
        "v_mfma_f32_16x16x32_bf16 v[28:31], a[44:47], a[204:207], v[28:31]\n"
        "ds_read_b128 a[204:207], v149 offset:27648\n"
        "v_mfma_f32_16x16x32_bf16 v[32:35], a[40:43], a[208:211], v[32:35]\n"
        "s_waitcnt lgkmcnt(6)\n"
        "v_mfma_f32_16x16x32_bf16 v[36:39], a[44:47], a[208:211], v[36:39]\n"
        "ds_read_b128 a[208:211], v152 offset:24576\n"
        "v_mfma_f32_16x16x32_bf16 v[40:43], a[40:43], a[212:215], v[40:43]\n"
        "v_mfma_f32_16x16x32_bf16 v[44:47], a[44:47], a[212:215], v[44:47]\n"
        "ds_read_b128 a[212:215], v152 offset:25600\n"
        "v_mfma_f32_16x16x32_bf16 v[48:51], a[40:43], a[216:219], v[48:51]\n"
        "v_mfma_f32_16x16x32_bf16 v[52:55], a[44:47], a[216:219], v[52:55]\n"
        "ds_read_b128 a[216:219], v152 offset:26624\n"
        "v_mfma_f32_16x16x32_bf16 v[56:59], a[40:43], a[220:223], v[56:59]\n"
        "v_mfma_f32_16x16x32_bf16 v[60:63], a[44:47], a[220:223], v[60:63]\n"
        "ds_read_b128 a[220:223], v152 offset:27648\n"
        "v_mfma_f32_16x16x32_bf16 v[0:3], a[112:115], a[0:3], v[0:3]\n"
        "v_mfma_f32_16x16x32_bf16 v[4:7], a[116:119], a[0:3], v[4:7]\n"
        "v_mfma_f32_16x16x32_bf16 v[8:11], a[112:115], a[4:7], v[8:11]\n"
        "v_mfma_f32_16x16x32_bf16 v[12:15], a[116:119], a[4:7], v[12:15]\n"
        "v_mfma_f32_16x16x32_bf16 v[16:19], a[112:115], a[8:11], v[16:19]\n"
        "v_mfma_f32_16x16x32_bf16 v[20:23], a[116:119], a[8:11], v[20:23]\n"
        "v_mfma_f32_16x16x32_bf16 v[24:27], a[112:115], a[12:15], v[24:27]\n"
        "s_waitcnt lgkmcnt(1)\n"
        "v_mfma_f32_16x16x32_bf16 v[28:31], a[116:119], a[12:15], v[28:31]\n"
        "v_mfma_f32_16x16x32_bf16 v[32:35], a[112:115], a[16:19], v[32:35]\n"
        "v_mfma_f32_16x16x32_bf16 v[36:39], a[116:119], a[16:19], v[36:39]\n"
        "s_waitcnt vmcnt(21)\n"
        "s_barrier\n"
        "v_mfma_f32_16x16x32_bf16 v[40:43], a[112:115], a[20:23], v[40:43]\n"
        "global_load_lds_dwordx4 v144, s[60:61] offset:0\n"
        "ds_read_b64_tr_b16 a[48:49], v154 offset:0\n"
        "v_mfma_f32_16x16x32_bf16 v[44:47], a[116:119], a[20:23], v[44:47]\n"
        "ds_read_b64_tr_b16 a[50:51], v154 offset:4096\n"
        "v_mfma_f32_16x16x32_bf16 v[48:51], a[112:115], a[24:27], v[48:51]\n"
        "ds_read_b64_tr_b16 a[52:53], v155 offset:0\n"
        "v_mfma_f32_16x16x32_bf16 v[52:55], a[116:119], a[24:27], v[52:55]\n"
        "ds_read_b64_tr_b16 a[54:55], v155 offset:4096\n"
        "global_load_lds_dwordx4 v145, s[60:61] offset:1024\n"
        "v_mfma_f32_16x16x32_bf16 v[56:59], a[112:115], a[28:31], v[56:59]\n"
        "ds_read_b128 a[0:3], v148 offset:16384\n"
        "v_mfma_f32_16x16x32_bf16 v[60:63], a[116:119], a[28:31], v[60:63]\n"
        "ds_read_b128 a[4:7], v148 offset:17408\n"
        "v_mfma_f32_16x16x32_bf16 v[0:3], a[120:123], a[192:195], v[0:3]\n"
        "ds_read_b128 a[8:11], v148 offset:18432\n"
        "global_load_lds_dwordx4 v146, s[60:61] offset:2048\n"
        "v_mfma_f32_16x16x32_bf16 v[4:7], a[124:127], a[192:195], v[4:7]\n"
        "ds_read_b128 a[12:15], v148 offset:19456\n"
        "v_mfma_f32_16x16x32_bf16 v[8:11], a[120:123], a[196:199], v[8:11]\n"
        "ds_read_b128 a[16:19], v151 offset:16384\n"
        "v_mfma_f32_16x16x32_bf16 v[12:15], a[124:127], a[196:199], v[12:15]\n"
        "global_load_lds_dwordx4 v147, s[60:61] offset:3072\n"
        "s_add_u32 m0, s73, 0x10000\n"
        "v_mfma_f32_16x16x32_bf16 v[16:19], a[120:123], a[200:203], v[16:19]\n"
        "ds_read_b128 a[20:23], v151 offset:17408\n"
        "s_add_u32 s60, s22, 0x320000\n"
        "s_addc_u32 s61, s23, 0\n"
        "v_mfma_f32_16x16x32_bf16 v[20:23], a[124:127], a[200:203], v[20:23]\n"
        "ds_read_b128 a[24:27], v151 offset:18432\n"
        "v_mfma_f32_16x16x32_bf16 v[24:27], a[120:123], a[204:207], v[24:27]\n"
        "global_load_lds_dwordx4 v254, s[20:21] offset:0 sc1\n"
        "v_mfma_f32_16x16x32_bf16 v[28:31], a[124:127], a[204:207], v[28:31]\n"
        "ds_read_b128 a[28:31], v151 offset:19456\n"
        "v_mfma_f32_16x16x32_bf16 v[32:35], a[120:123], a[208:211], v[32:35]\n"
        "ds_read_b64_tr_b16 a[56:57], v154 offset:8192\n"
        "s_waitcnt lgkmcnt(6)\n"
        "v_mfma_f32_16x16x32_bf16 v[36:39], a[124:127], a[208:211], v[36:39]\n"
        "global_load_lds_dwordx4 v254, s[20:21] offset:1024 sc1\n"
        "v_mfma_f32_16x16x32_bf16 v[40:43], a[120:123], a[212:215], v[40:43]\n"
        "ds_read_b64_tr_b16 a[58:59], v154 offset:12288\n"
        "v_mfma_f32_16x16x32_bf16 v[44:47], a[124:127], a[212:215], v[44:47]\n"
        "ds_read_b64_tr_b16 a[60:61], v155 offset:8192\n"
        "v_mfma_f32_16x16x32_bf16 v[48:51], a[120:123], a[216:219], v[48:51]\n"
        "ds_read_b64_tr_b16 a[62:63], v155 offset:12288\n"
        "global_load_lds_dwordx4 v254, s[20:21] offset:2048 sc1\n"
        "v_mfma_f32_16x16x32_bf16 v[52:55], a[124:127], a[216:219], v[52:55]\n"
        "ds_read_b128 a[192:195], v148 offset:24576\n"
        "v_mfma_f32_16x16x32_bf16 v[56:59], a[120:123], a[220:223], v[56:59]\n"
        "ds_read_b128 a[196:199], v148 offset:25600\n"
        "v_mfma_f32_16x16x32_bf16 v[60:63], a[124:127], a[220:223], v[60:63]\n"
        "ds_read_b128 a[200:203], v148 offset:26624\n"
        "global_load_lds_dwordx4 v254, s[20:21] offset:3072 sc1\n"
        "s_add_u32 m0, s73, 0x14000\n"
        "v_mfma_f32_16x16x32_bf16 v[0:3], a[48:51], a[0:3], v[0:3]\n"
        "ds_read_b128 a[204:207], v148 offset:27648\n"
        "s_add_u32 s20, s20, 0x4000\n"
        "s_addc_u32 s21, s21, 0\n"
        "v_mfma_f32_16x16x32_bf16 v[4:7], a[52:55], a[0:3], v[4:7]\n"
        "ds_read_b128 a[208:211], v151 offset:24576\n"
        "s_waitcnt lgkmcnt(8)\n"
        "v_mfma_f32_16x16x32_bf16 v[8:11], a[48:51], a[4:7], v[8:11]\n"
        "ds_read_b128 a[212:215], v151 offset:25600\n"
        "v_mfma_f32_16x16x32_bf16 v[12:15], a[52:55], a[4:7], v[12:15]\n"
        "ds_read_b128 a[216:219], v151 offset:26624\n"
        "v_mfma_f32_16x16x32_bf16 v[16:19], a[48:51], a[8:11], v[16:19]\n"
        "ds_read_b128 a[220:223], v151 offset:27648\n"
        "v_mfma_f32_16x16x32_bf16 v[20:23], a[52:55], a[8:11], v[20:23]\n"
        "v_mfma_f32_16x16x32_bf16 v[24:27], a[48:51], a[12:15], v[24:27]\n"
        "v_mfma_f32_16x16x32_bf16 v[28:31], a[52:55], a[12:15], v[28:31]\n"
        "v_mfma_f32_16x16x32_bf16 v[32:35], a[48:51], a[16:19], v[32:35]\n"
        "v_mfma_f32_16x16x32_bf16 v[36:39], a[52:55], a[16:19], v[36:39]\n"
        "s_waitcnt vmcnt(21)\n"
        "s_barrier\n"
        "v_mfma_f32_16x16x32_bf16 v[40:43], a[48:51], a[20:23], v[40:43]\n"
        "global_load_lds_dwordx4 v254, s[20:21] offset:0 sc1\n"
        "ds_read_b64_tr_b16 a[32:33], v156 offset:32768\n"
        "v_mfma_f32_16x16x32_bf16 v[44:47], a[52:55], a[20:23], v[44:47]\n"
        "ds_read_b64_tr_b16 a[34:35], v156 offset:36864\n"
        "s_waitcnt lgkmcnt(3)\n"
        "v_mfma_f32_16x16x32_bf16 v[48:51], a[48:51], a[24:27], v[48:51]\n"
        "ds_read_b64_tr_b16 a[36:37], v157 offset:32768\n"
        "v_mfma_f32_16x16x32_bf16 v[52:55], a[52:55], a[24:27], v[52:55]\n"
        "ds_read_b64_tr_b16 a[38:39], v157 offset:36864\n"
        "global_load_lds_dwordx4 v254, s[20:21] offset:1024 sc1\n"
        "v_mfma_f32_16x16x32_bf16 v[56:59], a[48:51], a[28:31], v[56:59]\n"
        "ds_read_b128 a[0:3], v149 offset:49152\n"
        "v_mfma_f32_16x16x32_bf16 v[60:63], a[52:55], a[28:31], v[60:63]\n"
        "ds_read_b128 a[4:7], v149 offset:50176\n"
        "v_mfma_f32_16x16x32_bf16 v[0:3], a[56:59], a[192:195], v[0:3]\n"
        "ds_read_b128 a[8:11], v149 offset:51200\n"
        "global_load_lds_dwordx4 v254, s[20:21] offset:2048 sc1\n"
        "v_mfma_f32_16x16x32_bf16 v[4:7], a[60:63], a[192:195], v[4:7]\n"
        "ds_read_b128 a[12:15], v149 offset:52224\n"
        "v_mfma_f32_16x16x32_bf16 v[8:11], a[56:59], a[196:199], v[8:11]\n"
        "ds_read_b128 a[16:19], v152 offset:49152\n"
        "v_mfma_f32_16x16x32_bf16 v[12:15], a[60:63], a[196:199], v[12:15]\n"
        "global_load_lds_dwordx4 v254, s[20:21] offset:3072 sc1\n"
        "v_mfma_f32_16x16x32_bf16 v[16:19], a[56:59], a[200:203], v[16:19]\n"
        "ds_read_b128 a[20:23], v152 offset:50176\n"
        "s_add_u32 s20, s20, 0x4000\n"
        "s_addc_u32 s21, s21, 0\n"
        "v_mfma_f32_16x16x32_bf16 v[20:23], a[60:63], a[200:203], v[20:23]\n"
        "ds_read_b128 a[24:27], v152 offset:51200\n"
        "s_add_u32 m0, s73, 0x0\n"
        "v_mfma_f32_16x16x32_bf16 v[24:27], a[56:59], a[204:207], v[24:27]\n"
        "ds_read_b128 a[28:31], v152 offset:52224\n"
        "v_mfma_f32_16x16x32_bf16 v[28:31], a[60:63], a[204:207], v[28:31]\n"
        "ds_read_b64_tr_b16 a[40:41], v156 offset:40960\n"
        "v_mfma_f32_16x16x32_bf16 v[32:35], a[56:59], a[208:211], v[32:35]\n"
        "ds_read_b64_tr_b16 a[42:43], v156 offset:45056\n"
        "s_waitcnt lgkmcnt(7)\n"
        "v_mfma_f32_16x16x32_bf16 v[36:39], a[60:63], a[208:211], v[36:39]\n"
        "ds_read_b64_tr_b16 a[44:45], v157 offset:40960\n"
        "v_mfma_f32_16x16x32_bf16 v[40:43], a[56:59], a[212:215], v[40:43]\n"
        "ds_read_b64_tr_b16 a[46:47], v157 offset:45056\n"
        "v_mfma_f32_16x16x32_bf16 v[44:47], a[60:63], a[212:215], v[44:47]\n"
        "ds_read_b128 a[192:195], v149 offset:57344\n"
        "v_mfma_f32_16x16x32_bf16 v[48:51], a[56:59], a[216:219], v[48:51]\n"
        "ds_read_b128 a[196:199], v149 offset:58368\n"
        "v_mfma_f32_16x16x32_bf16 v[52:55], a[60:63], a[216:219], v[52:55]\n"
        "ds_read_b128 a[200:203], v149 offset:59392\n"
        "v_mfma_f32_16x16x32_bf16 v[56:59], a[56:59], a[220:223], v[56:59]\n"
        "ds_read_b128 a[204:207], v149 offset:60416\n"
        "v_mfma_f32_16x16x32_bf16 v[60:63], a[60:63], a[220:223], v[60:63]\n"
        "ds_read_b128 a[208:211], v152 offset:57344\n"
        "v_mfma_f32_16x16x32_bf16 v[0:3], a[32:35], a[0:3], v[0:3]\n"
        "ds_read_b128 a[212:215], v152 offset:58368\n"
        "s_waitcnt lgkmcnt(8)\n"
        "v_mfma_f32_16x16x32_bf16 v[4:7], a[36:39], a[0:3], v[4:7]\n"
        "ds_read_b128 a[216:219], v152 offset:59392\n"
        "v_mfma_f32_16x16x32_bf16 v[8:11], a[32:35], a[4:7], v[8:11]\n"
        "ds_read_b128 a[220:223], v152 offset:60416\n"
        "v_mfma_f32_16x16x32_bf16 v[12:15], a[36:39], a[4:7], v[12:15]\n"
        "v_mfma_f32_16x16x32_bf16 v[16:19], a[32:35], a[8:11], v[16:19]\n"
        "v_mfma_f32_16x16x32_bf16 v[20:23], a[36:39], a[8:11], v[20:23]\n"
        "v_mfma_f32_16x16x32_bf16 v[24:27], a[32:35], a[12:15], v[24:27]\n"
        "v_mfma_f32_16x16x32_bf16 v[28:31], a[36:39], a[12:15], v[28:31]\n"
        "v_mfma_f32_16x16x32_bf16 v[32:35], a[32:35], a[16:19], v[32:35]\n"
        "v_mfma_f32_16x16x32_bf16 v[36:39], a[36:39], a[16:19], v[36:39]\n"
        "s_waitcnt vmcnt(21)\n"
        "s_barrier\n"
        "v_mfma_f32_16x16x32_bf16 v[40:43], a[32:35], a[20:23], v[40:43]\n"
        "global_load_lds_dwordx4 v144, s[60:61] offset:0\n"
        "ds_read_b128 a[0:3], v150 offset:0\n"
        "v_mfma_f32_16x16x32_bf16 v[44:47], a[36:39], a[20:23], v[44:47]\n"
        "ds_read_b128 a[4:7], v150 offset:1024\n"
        "s_waitcnt lgkmcnt(2)\n"
        "v_mfma_f32_16x16x32_bf16 v[48:51], a[32:35], a[24:27], v[48:51]\n"
        "ds_read_b128 a[8:11], v150 offset:2048\n"
        "v_mfma_f32_16x16x32_bf16 v[52:55], a[36:39], a[24:27], v[52:55]\n"
        "global_load_lds_dwordx4 v145, s[60:61] offset:1024\n"
        "ds_read_b128 a[12:15], v150 offset:3072\n"
        "v_mfma_f32_16x16x32_bf16 v[56:59], a[32:35], a[28:31], v[56:59]\n"
        "ds_read_b128 a[16:19], v153 offset:0\n"
        "v_mfma_f32_16x16x32_bf16 v[60:63], a[36:39], a[28:31], v[60:63]\n"
        "ds_read_b128 a[20:23], v153 offset:1024\n"
        "v_mfma_f32_16x16x32_bf16 v[0:3], a[40:43], a[192:195], v[0:3]\n"
        "global_load_lds_dwordx4 v146, s[60:61] offset:2048\n"
        "ds_read_b128 a[24:27], v153 offset:2048\n"
        "v_mfma_f32_16x16x32_bf16 v[4:7], a[44:47], a[192:195], v[4:7]\n"
        "ds_read_b128 a[28:31], v153 offset:3072\n"
        "v_mfma_f32_16x16x32_bf16 v[8:11], a[40:43], a[196:199], v[8:11]\n"
        "ds_read_b128 a[192:195], v150 offset:8192\n"
        "v_mfma_f32_16x16x32_bf16 v[12:15], a[44:47], a[196:199], v[12:15]\n"
        "global_load_lds_dwordx4 v147, s[60:61] offset:3072\n"
        "s_add_u32 m0, s73, 0x4000\n"
        "v_mfma_f32_16x16x32_bf16 v[16:19], a[40:43], a[200:203], v[16:19]\n"
        "ds_read_b128 a[196:199], v150 offset:9216\n"
        "s_add_u32 s60, s22, 0x360000\n"
        "s_addc_u32 s61, s23, 0\n"
        "v_mfma_f32_16x16x32_bf16 v[20:23], a[44:47], a[200:203], v[20:23]\n"
        "ds_read_b128 a[200:203], v150 offset:10240\n"
        "v_mfma_f32_16x16x32_bf16 v[24:27], a[40:43], a[204:207], v[24:27]\n"
        "global_load_lds_dwordx4 v254, s[20:21] offset:0 sc1\n"
        "v_mfma_f32_16x16x32_bf16 v[28:31], a[44:47], a[204:207], v[28:31]\n"
        "ds_read_b128 a[204:207], v150 offset:11264\n"
        "v_mfma_f32_16x16x32_bf16 v[32:35], a[40:43], a[208:211], v[32:35]\n"
        "v_mfma_f32_16x16x32_bf16 v[36:39], a[44:47], a[208:211], v[36:39]\n"
        "global_load_lds_dwordx4 v254, s[20:21] offset:1024 sc1\n"
        "ds_read_b128 a[208:211], v153 offset:8192\n"
        "v_mfma_f32_16x16x32_bf16 v[40:43], a[40:43], a[212:215], v[40:43]\n"
        "s_waitcnt lgkmcnt(4)\n"
        "v_mfma_f32_16x16x32_bf16 v[44:47], a[44:47], a[212:215], v[44:47]\n"
        "ds_read_b128 a[212:215], v153 offset:9216\n"
        "v_mfma_f32_16x16x32_bf16 v[48:51], a[40:43], a[216:219], v[48:51]\n"
        "global_load_lds_dwordx4 v254, s[20:21] offset:2048 sc1\n"
        "v_mfma_f32_16x16x32_bf16 v[52:55], a[44:47], a[216:219], v[52:55]\n"
        "ds_read_b128 a[216:219], v153 offset:10240\n"
        "v_mfma_f32_16x16x32_bf16 v[56:59], a[40:43], a[220:223], v[56:59]\n"
        "v_mfma_f32_16x16x32_bf16 v[60:63], a[44:47], a[220:223], v[60:63]\n"
        "global_load_lds_dwordx4 v254, s[20:21] offset:3072 sc1\n"
        "s_add_u32 s20, s20, 0x4000\n"
        "s_addc_u32 s21, s21, 0\n"
        "v_mfma_f32_16x16x32_bf16 v[0:3], a[128:131], a[0:3], v[0:3]\n"
        "ds_read_b128 a[220:223], v153 offset:11264\n"
        "s_add_u32 m0, s73, 0x18000\n"
        "v_mfma_f32_16x16x32_bf16 v[4:7], a[132:135], a[0:3], v[4:7]\n"
        "v_mfma_f32_16x16x32_bf16 v[8:11], a[128:131], a[4:7], v[8:11]\n"
        "v_mfma_f32_16x16x32_bf16 v[12:15], a[132:135], a[4:7], v[12:15]\n"
        "v_mfma_f32_16x16x32_bf16 v[16:19], a[128:131], a[8:11], v[16:19]\n"
        "v_mfma_f32_16x16x32_bf16 v[20:23], a[132:135], a[8:11], v[20:23]\n"
        "v_mfma_f32_16x16x32_bf16 v[24:27], a[128:131], a[12:15], v[24:27]\n"
        "v_mfma_f32_16x16x32_bf16 v[28:31], a[132:135], a[12:15], v[28:31]\n"
        "v_mfma_f32_16x16x32_bf16 v[32:35], a[128:131], a[16:19], v[32:35]\n"
        "v_mfma_f32_16x16x32_bf16 v[36:39], a[132:135], a[16:19], v[36:39]\n"
        "s_waitcnt vmcnt(21)\n"
        "s_barrier\n"
        "v_mfma_f32_16x16x32_bf16 v[40:43], a[128:131], a[20:23], v[40:43]\n"
        "global_load_lds_dwordx4 v254, s[20:21] offset:0 sc1\n"
        "ds_read_b64_tr_b16 a[48:49], v158 offset:16384\n"
        "v_mfma_f32_16x16x32_bf16 v[44:47], a[132:135], a[20:23], v[44:47]\n"
        "ds_read_b64_tr_b16 a[50:51], v158 offset:20480\n"
        "v_mfma_f32_16x16x32_bf16 v[48:51], a[128:131], a[24:27], v[48:51]\n"
        "ds_read_b64_tr_b16 a[52:53], v159 offset:16384\n"
        "s_waitcnt lgkmcnt(3)\n"
        "v_mfma_f32_16x16x32_bf16 v[52:55], a[132:135], a[24:27], v[52:55]\n"
        "ds_read_b64_tr_b16 a[54:55], v159 offset:20480\n"
        "global_load_lds_dwordx4 v254, s[20:21] offset:1024 sc1\n"
        "v_mfma_f32_16x16x32_bf16 v[56:59], a[128:131], a[28:31], v[56:59]\n"
        "ds_read_b128 a[0:3], v148 offset:32768\n"
        "v_mfma_f32_16x16x32_bf16 v[60:63], a[132:135], a[28:31], v[60:63]\n"
        "ds_read_b128 a[4:7], v148 offset:33792\n"
        "v_mfma_f32_16x16x32_bf16 v[0:3], a[136:139], a[192:195], v[0:3]\n"
        "ds_read_b128 a[8:11], v148 offset:34816\n"
        "global_load_lds_dwordx4 v254, s[20:21] offset:2048 sc1\n"
        "v_mfma_f32_16x16x32_bf16 v[4:7], a[140:143], a[192:195], v[4:7]\n"
        "ds_read_b128 a[12:15], v148 offset:35840\n"
        "v_mfma_f32_16x16x32_bf16 v[8:11], a[136:139], a[196:199], v[8:11]\n"
        "ds_read_b128 a[16:19], v151 offset:32768\n"
        "v_mfma_f32_16x16x32_bf16 v[12:15], a[140:143], a[196:199], v[12:15]\n"
        "global_load_lds_dwordx4 v254, s[20:21] offset:3072 sc1\n"
        "s_add_u32 m0, s73, 0x1c000\n"
        "v_mfma_f32_16x16x32_bf16 v[16:19], a[136:139], a[200:203], v[16:19]\n"
        "ds_read_b128 a[20:23], v151 offset:33792\n"
        "s_add_u32 s20, s20, 0x4000\n"
        "s_addc_u32 s21, s21, 0\n"
        "v_mfma_f32_16x16x32_bf16 v[20:23], a[140:143], a[200:203], v[20:23]\n"
        "ds_read_b128 a[24:27], v151 offset:34816\n"
        "v_mfma_f32_16x16x32_bf16 v[24:27], a[136:139], a[204:207], v[24:27]\n"
        "global_load_lds_dwordx4 v144, s[60:61] offset:0\n"
        "v_mfma_f32_16x16x32_bf16 v[28:31], a[140:143], a[204:207], v[28:31]\n"
        "ds_read_b128 a[28:31], v151 offset:35840\n"
        "v_mfma_f32_16x16x32_bf16 v[32:35], a[136:139], a[208:211], v[32:35]\n"
        "ds_read_b64_tr_b16 a[56:57], v158 offset:24576\n"
        "v_mfma_f32_16x16x32_bf16 v[36:39], a[140:143], a[208:211], v[36:39]\n"
        "global_load_lds_dwordx4 v145, s[60:61] offset:1024\n"
        "v_mfma_f32_16x16x32_bf16 v[40:43], a[136:139], a[212:215], v[40:43]\n"
        "ds_read_b64_tr_b16 a[58:59], v158 offset:28672\n"
        "s_waitcnt lgkmcnt(5)\n"
        "v_mfma_f32_16x16x32_bf16 v[44:47], a[140:143], a[212:215], v[44:47]\n"
        "ds_read_b64_tr_b16 a[60:61], v159 offset:24576\n"
        "v_mfma_f32_16x16x32_bf16 v[48:51], a[136:139], a[216:219], v[48:51]\n"
        "ds_read_b64_tr_b16 a[62:63], v159 offset:28672\n"
        "global_load_lds_dwordx4 v146, s[60:61] offset:2048\n"
        "v_mfma_f32_16x16x32_bf16 v[52:55], a[140:143], a[216:219], v[52:55]\n"
        "ds_read_b128 a[192:195], v148 offset:40960\n"
        "v_mfma_f32_16x16x32_bf16 v[56:59], a[136:139], a[220:223], v[56:59]\n"
        "ds_read_b128 a[196:199], v148 offset:41984\n"
        "v_mfma_f32_16x16x32_bf16 v[60:63], a[140:143], a[220:223], v[60:63]\n"
        "ds_read_b128 a[200:203], v148 offset:43008\n"
        "global_load_lds_dwordx4 v147, s[60:61] offset:3072\n"
        "s_add_u32 m0, s73, 0x20000\n"
        "v_mfma_f32_16x16x32_bf16 v[0:3], a[48:51], a[0:3], v[0:3]\n"
        "ds_read_b128 a[204:207], v148 offset:44032\n"
        "s_add_u32 s60, s22, 0x380000\n"
        "s_addc_u32 s61, s23, 0\n"
        "v_mfma_f32_16x16x32_bf16 v[4:7], a[52:55], a[0:3], v[4:7]\n"
        "ds_read_b128 a[208:211], v151 offset:40960\n"
        "v_mfma_f32_16x16x32_bf16 v[8:11], a[48:51], a[4:7], v[8:11]\n"
        "ds_read_b128 a[212:215], v151 offset:41984\n"
        "v_mfma_f32_16x16x32_bf16 v[12:15], a[52:55], a[4:7], v[12:15]\n"
        "ds_read_b128 a[216:219], v151 offset:43008\n"
        "v_mfma_f32_16x16x32_bf16 v[16:19], a[48:51], a[8:11], v[16:19]\n"
        "ds_read_b128 a[220:223], v151 offset:44032\n"
        "s_waitcnt lgkmcnt(8)\n"
        "v_mfma_f32_16x16x32_bf16 v[20:23], a[52:55], a[8:11], v[20:23]\n"
        "v_mfma_f32_16x16x32_bf16 v[24:27], a[48:51], a[12:15], v[24:27]\n"
        "v_mfma_f32_16x16x32_bf16 v[28:31], a[52:55], a[12:15], v[28:31]\n"
        "v_mfma_f32_16x16x32_bf16 v[32:35], a[48:51], a[16:19], v[32:35]\n"
        "v_mfma_f32_16x16x32_bf16 v[36:39], a[52:55], a[16:19], v[36:39]\n"
        "s_waitcnt vmcnt(20)\n"
        "s_barrier\n"
        "v_mfma_f32_16x16x32_bf16 v[40:43], a[48:51], a[20:23], v[40:43]\n"
        "global_load_lds_dwordx4 v254, s[20:21] offset:0 sc1\n"
        "ds_read_b64_tr_b16 a[32:33], v154 offset:49152\n"
        "v_mfma_f32_16x16x32_bf16 v[44:47], a[52:55], a[20:23], v[44:47]\n"
        "ds_read_b64_tr_b16 a[34:35], v154 offset:53248\n"
        "s_waitcnt lgkmcnt(3)\n"
        "v_mfma_f32_16x16x32_bf16 v[48:51], a[48:51], a[24:27], v[48:51]\n"
        "ds_read_b64_tr_b16 a[36:37], v155 offset:49152\n"
        "v_mfma_f32_16x16x32_bf16 v[52:55], a[52:55], a[24:27], v[52:55]\n"
        "ds_read_b64_tr_b16 a[38:39], v155 offset:53248\n"
        "global_load_lds_dwordx4 v254, s[20:21] offset:1024 sc1\n"
        "v_mfma_f32_16x16x32_bf16 v[56:59], a[48:51], a[28:31], v[56:59]\n"
        "ds_read_b128 a[0:3], v149 offset:0\n"
        "v_mfma_f32_16x16x32_bf16 v[60:63], a[52:55], a[28:31], v[60:63]\n"
        "ds_read_b128 a[4:7], v149 offset:1024\n"
        "v_mfma_f32_16x16x32_bf16 v[0:3], a[56:59], a[192:195], v[0:3]\n"
        "ds_read_b128 a[8:11], v149 offset:2048\n"
        "global_load_lds_dwordx4 v254, s[20:21] offset:2048 sc1\n"
        "v_mfma_f32_16x16x32_bf16 v[4:7], a[60:63], a[192:195], v[4:7]\n"
        "ds_read_b128 a[12:15], v149 offset:3072\n"
        "v_mfma_f32_16x16x32_bf16 v[8:11], a[56:59], a[196:199], v[8:11]\n"
        "ds_read_b128 a[16:19], v152 offset:0\n"
        "v_cmp_ne_u64 vcc, v[236:237], v[250:251]\n"
        "v_mfma_f32_16x16x32_bf16 v[12:15], a[60:63], a[196:199], v[12:15]\n"
        "global_load_lds_dwordx4 v254, s[20:21] offset:3072 sc1\n"
        "v_mfma_f32_16x16x32_bf16 v[16:19], a[56:59], a[200:203], v[16:19]\n"
        "ds_read_b128 a[20:23], v152 offset:1024\n"
        "s_add_u32 s20, s20, 0x4000\n"
        "s_addc_u32 s21, s21, 0\n"
        "v_mfma_f32_16x16x32_bf16 v[20:23], a[60:63], a[200:203], v[20:23]\n"
        "ds_read_b128 a[24:27], v152 offset:2048\n"
        "s_add_u32 m0, s73, 0x24000\n"
        "s_cbranch_vccnz LPZZ_%=\n"
        "LBZZ_%=:\n"
        "v_mfma_f32_16x16x32_bf16 v[24:27], a[56:59], a[204:207], v[24:27]\n"
        "ds_read_b128 a[28:31], v152 offset:3072\n"
        "v_lshrrev_b32 v234, 1, v254\n"
        "v_mfma_f32_16x16x32_bf16 v[28:31], a[60:63], a[204:207], v[28:31]\n"
        "ds_read_b64_tr_b16 a[40:41], v154 offset:57344\n"
        "v_add_u32 v234, s14, v234\n"
        "v_mfma_f32_16x16x32_bf16 v[32:35], a[56:59], a[208:211], v[32:35]\n"
        "ds_read_b64_tr_b16 a[42:43], v154 offset:61440\n"
        "s_xor_b32 s14, s14, 0x200\n"
        "v_mov_b32 v235, s14\n"
        "v_mfma_f32_16x16x32_bf16 v[36:39], a[60:63], a[208:211], v[36:39]\n"
        "ds_read_b64_tr_b16 a[44:45], v155 offset:57344\n"
        "s_waitcnt lgkmcnt(7)\n"
        "v_mfma_f32_16x16x32_bf16 v[40:43], a[56:59], a[212:215], v[40:43]\n"
        "ds_read_b64_tr_b16 a[46:47], v155 offset:61440\n"
        "v_mfma_f32_16x16x32_bf16 v[44:47], a[60:63], a[212:215], v[44:47]\n"
        "ds_read_b128 a[192:195], v149 offset:8192\n"
        "v_mfma_f32_16x16x32_bf16 v[48:51], a[56:59], a[216:219], v[48:51]\n"
        "ds_read_b128 a[196:199], v149 offset:9216\n"
        "v_mfma_f32_16x16x32_bf16 v[52:55], a[60:63], a[216:219], v[52:55]\n"
        "ds_read_b128 a[200:203], v149 offset:10240\n"
        "v_mfma_f32_16x16x32_bf16 v[56:59], a[56:59], a[220:223], v[56:59]\n"
        "ds_read_b128 a[204:207], v149 offset:11264\n"
        "v_mfma_f32_16x16x32_bf16 v[60:63], a[60:63], a[220:223], v[60:63]\n"
        "ds_read_b128 a[208:211], v152 offset:8192\n"
        "v_mfma_f32_16x16x32_bf16 v[0:3], a[32:35], a[0:3], v[0:3]\n"
        "ds_read_b128 a[212:215], v152 offset:9216\n"
        "v_mfma_f32_16x16x32_bf16 v[4:7], a[36:39], a[0:3], v[4:7]\n"
        "ds_read_b128 a[216:219], v152 offset:10240\n"
        "v_mfma_f32_16x16x32_bf16 v[8:11], a[32:35], a[4:7], v[8:11]\n"
        "ds_read_b128 a[220:223], v152 offset:11264\n"
        "s_waitcnt lgkmcnt(8)\n"
        "v_mfma_f32_16x16x32_bf16 v[12:15], a[36:39], a[4:7], v[12:15]\n"
        "v_mfma_f32_16x16x32_bf16 v[16:19], a[32:35], a[8:11], v[16:19]\n"
        "v_mfma_f32_16x16x32_bf16 v[20:23], a[36:39], a[8:11], v[20:23]\n"
        "v_mfma_f32_16x16x32_bf16 v[24:27], a[32:35], a[12:15], v[24:27]\n"
        "v_mfma_f32_16x16x32_bf16 v[28:31], a[36:39], a[12:15], v[28:31]\n"
        "v_mfma_f32_16x16x32_bf16 v[32:35], a[32:35], a[16:19], v[32:35]\n"
        "v_mfma_f32_16x16x32_bf16 v[36:39], a[36:39], a[16:19], v[36:39]\n"
        "s_waitcnt vmcnt(20)\n"
        "s_barrier\n"
        "v_mfma_f32_16x16x32_bf16 v[40:43], a[32:35], a[20:23], v[40:43]\n"
        "global_load_lds_dwordx4 v144, s[60:61] offset:0\n"
        "ds_read_b128 a[0:3], v149 offset:16384\n"
        "v_mfma_f32_16x16x32_bf16 v[44:47], a[36:39], a[20:23], v[44:47]\n"
        "ds_read_b128 a[4:7], v149 offset:17408\n"
        "s_waitcnt lgkmcnt(2)\n"
        "v_mfma_f32_16x16x32_bf16 v[48:51], a[32:35], a[24:27], v[48:51]\n"
        "ds_read_b128 a[8:11], v149 offset:18432\n"
        "v_mfma_f32_16x16x32_bf16 v[52:55], a[36:39], a[24:27], v[52:55]\n"
        "global_load_lds_dwordx4 v145, s[60:61] offset:1024\n"
        "ds_read_b128 a[12:15], v149 offset:19456\n"
        "v_mfma_f32_16x16x32_bf16 v[56:59], a[32:35], a[28:31], v[56:59]\n"
        "ds_read_b128 a[16:19], v152 offset:16384\n"
        "v_mfma_f32_16x16x32_bf16 v[60:63], a[36:39], a[28:31], v[60:63]\n"
        "ds_read_b128 a[20:23], v152 offset:17408\n"
        "v_mfma_f32_16x16x32_bf16 v[0:3], a[40:43], a[192:195], v[0:3]\n"
        "global_load_lds_dwordx4 v146, s[60:61] offset:2048\n"
        "ds_read_b128 a[24:27], v152 offset:18432\n"
        "v_mfma_f32_16x16x32_bf16 v[4:7], a[44:47], a[192:195], v[4:7]\n"
        "ds_read_b128 a[28:31], v152 offset:19456\n"
        "v_mfma_f32_16x16x32_bf16 v[8:11], a[40:43], a[196:199], v[8:11]\n"
        "ds_read_b128 a[192:195], v149 offset:24576\n"
        "v_mfma_f32_16x16x32_bf16 v[12:15], a[44:47], a[196:199], v[12:15]\n"
        "global_load_lds_dwordx4 v147, s[60:61] offset:3072\n"
        "s_add_u32 m0, s73, 0x8000\n"
        "v_mfma_f32_16x16x32_bf16 v[16:19], a[40:43], a[200:203], v[16:19]\n"
        "ds_read_b128 a[196:199], v149 offset:25600\n"
        "s_add_u32 s60, s22, 0x3c0000\n"
        "s_addc_u32 s61, s23, 0\n"
        "v_mfma_f32_16x16x32_bf16 v[20:23], a[44:47], a[200:203], v[20:23]\n"
        "ds_read_b128 a[200:203], v149 offset:26624\n"
        "v_mfma_f32_16x16x32_bf16 v[24:27], a[40:43], a[204:207], v[24:27]\n"
        "global_load_lds_dwordx4 v254, s[20:21] offset:0 sc1\n"
        "v_mfma_f32_16x16x32_bf16 v[28:31], a[44:47], a[204:207], v[28:31]\n"
        "ds_read_b128 a[204:207], v149 offset:27648\n"
        "v_mfma_f32_16x16x32_bf16 v[32:35], a[40:43], a[208:211], v[32:35]\n"
        "v_mfma_f32_16x16x32_bf16 v[36:39], a[44:47], a[208:211], v[36:39]\n"
        "global_load_lds_dwordx4 v254, s[20:21] offset:1024 sc1\n"
        "ds_read_b128 a[208:211], v152 offset:24576\n"
        "v_mfma_f32_16x16x32_bf16 v[40:43], a[40:43], a[212:215], v[40:43]\n"
        "s_waitcnt lgkmcnt(4)\n"
        "v_mfma_f32_16x16x32_bf16 v[44:47], a[44:47], a[212:215], v[44:47]\n"
        "ds_read_b128 a[212:215], v152 offset:25600\n"
        "v_mfma_f32_16x16x32_bf16 v[48:51], a[40:43], a[216:219], v[48:51]\n"
        "global_load_lds_dwordx4 v254, s[20:21] offset:2048 sc1\n"
        "v_mfma_f32_16x16x32_bf16 v[52:55], a[44:47], a[216:219], v[52:55]\n"
        "ds_read_b128 a[216:219], v152 offset:26624\n"
        "v_mfma_f32_16x16x32_bf16 v[56:59], a[40:43], a[220:223], v[56:59]\n"
        "v_mfma_f32_16x16x32_bf16 v[60:63], a[44:47], a[220:223], v[60:63]\n"
        "global_load_lds_dwordx4 v254, s[20:21] offset:3072 sc1\n"
        "s_add_u32 s20, s20, 0x4000\n"
        "s_addc_u32 s21, s21, 0\n"
        "v_mfma_f32_16x16x32_bf16 v[0:3], a[144:147], a[0:3], v[0:3]\n"
        "ds_read_b128 a[220:223], v152 offset:27648\n"
        "s_add_u32 m0, s73, 0xc000\n"
        "v_mfma_f32_16x16x32_bf16 v[4:7], a[148:151], a[0:3], v[4:7]\n"
        "v_mfma_f32_16x16x32_bf16 v[8:11], a[144:147], a[4:7], v[8:11]\n"
        "v_mfma_f32_16x16x32_bf16 v[12:15], a[148:151], a[4:7], v[12:15]\n"
        "v_mfma_f32_16x16x32_bf16 v[16:19], a[144:147], a[8:11], v[16:19]\n"
        "v_mfma_f32_16x16x32_bf16 v[20:23], a[148:151], a[8:11], v[20:23]\n"
        "v_mfma_f32_16x16x32_bf16 v[24:27], a[144:147], a[12:15], v[24:27]\n"
        "v_mfma_f32_16x16x32_bf16 v[28:31], a[148:151], a[12:15], v[28:31]\n"
        "v_mfma_f32_16x16x32_bf16 v[32:35], a[144:147], a[16:19], v[32:35]\n"
        "v_mfma_f32_16x16x32_bf16 v[36:39], a[148:151], a[16:19], v[36:39]\n"
        "s_waitcnt vmcnt(20)\n"
        "s_barrier\n"
        "v_mfma_f32_16x16x32_bf16 v[40:43], a[144:147], a[20:23], v[40:43]\n"
        "global_load_lds_dwordx4 v254, s[20:21] offset:0 sc1\n"
        "ds_read_b64_tr_b16 a[48:49], v154 offset:0\n"
        "v_mfma_f32_16x16x32_bf16 v[44:47], a[148:151], a[20:23], v[44:47]\n"
        "ds_read_b64_tr_b16 a[50:51], v154 offset:4096\n"
        "v_mfma_f32_16x16x32_bf16 v[48:51], a[144:147], a[24:27], v[48:51]\n"
        "ds_read_b64_tr_b16 a[52:53], v155 offset:0\n"
        "s_waitcnt lgkmcnt(3)\n"
        "v_mfma_f32_16x16x32_bf16 v[52:55], a[148:151], a[24:27], v[52:55]\n"
        "ds_read_b64_tr_b16 a[54:55], v155 offset:4096\n"
        "global_load_lds_dwordx4 v254, s[20:21] offset:1024 sc1\n"
        "v_mfma_f32_16x16x32_bf16 v[56:59], a[144:147], a[28:31], v[56:59]\n"
        "ds_read_b128 a[0:3], v148 offset:16384\n"
        "v_mfma_f32_16x16x32_bf16 v[60:63], a[148:151], a[28:31], v[60:63]\n"
        "ds_read_b128 a[4:7], v148 offset:17408\n"
        "v_mfma_f32_16x16x32_bf16 v[0:3], a[152:155], a[192:195], v[0:3]\n"
        "ds_read_b128 a[8:11], v148 offset:18432\n"
        "global_load_lds_dwordx4 v254, s[20:21] offset:2048 sc1\n"
        "v_mfma_f32_16x16x32_bf16 v[4:7], a[156:159], a[192:195], v[4:7]\n"
        "ds_read_b128 a[12:15], v148 offset:19456\n"
        "v_mfma_f32_16x16x32_bf16 v[8:11], a[152:155], a[196:199], v[8:11]\n"
        "ds_read_b128 a[16:19], v151 offset:16384\n"
        "v_mfma_f32_16x16x32_bf16 v[12:15], a[156:159], a[196:199], v[12:15]\n"
        "global_load_lds_dwordx4 v254, s[20:21] offset:3072 sc1\n"
        "s_add_u32 m0, s73, 0x10000\n"
        "v_mfma_f32_16x16x32_bf16 v[16:19], a[152:155], a[200:203], v[16:19]\n"
        "ds_read_b128 a[20:23], v151 offset:17408\n"
        "s_add_u32 s20, s20, 0x4000\n"
        "s_addc_u32 s21, s21, 0\n"
        "v_mfma_f32_16x16x32_bf16 v[20:23], a[156:159], a[200:203], v[20:23]\n"
        "ds_read_b128 a[24:27], v151 offset:18432\n"
        "v_mfma_f32_16x16x32_bf16 v[24:27], a[152:155], a[204:207], v[24:27]\n"
        "global_load_lds_dwordx4 v144, s[60:61] offset:0\n"
        "v_mfma_f32_16x16x32_bf16 v[28:31], a[156:159], a[204:207], v[28:31]\n"
        "ds_read_b128 a[28:31], v151 offset:19456\n"
        "v_mfma_f32_16x16x32_bf16 v[32:35], a[152:155], a[208:211], v[32:35]\n"
        "ds_read_b64_tr_b16 a[56:57], v154 offset:8192\n"
        "v_mfma_f32_16x16x32_bf16 v[36:39], a[156:159], a[208:211], v[36:39]\n"
        "global_load_lds_dwordx4 v145, s[60:61] offset:1024\n"
        "v_mfma_f32_16x16x32_bf16 v[40:43], a[152:155], a[212:215], v[40:43]\n"
        "ds_read_b64_tr_b16 a[58:59], v154 offset:12288\n"
        "s_waitcnt lgkmcnt(5)\n"
        "v_mfma_f32_16x16x32_bf16 v[44:47], a[156:159], a[212:215], v[44:47]\n"
        "ds_read_b64_tr_b16 a[60:61], v155 offset:8192\n"
        "v_mfma_f32_16x16x32_bf16 v[48:51], a[152:155], a[216:219], v[48:51]\n"
        "ds_read_b64_tr_b16 a[62:63], v155 offset:12288\n"
        "global_load_lds_dwordx4 v146, s[60:61] offset:2048\n"
        "v_mfma_f32_16x16x32_bf16 v[52:55], a[156:159], a[216:219], v[52:55]\n"
        "ds_read_b128 a[192:195], v148 offset:24576\n"
        "v_mfma_f32_16x16x32_bf16 v[56:59], a[152:155], a[220:223], v[56:59]\n"
        "ds_read_b128 a[196:199], v148 offset:25600\n"
        "v_mfma_f32_16x16x32_bf16 v[60:63], a[156:159], a[220:223], v[60:63]\n"
        "ds_read_b128 a[200:203], v148 offset:26624\n"
        "global_load_lds_dwordx4 v147, s[60:61] offset:3072\n"
        "s_add_u32 m0, s73, 0x14000\n"
        "v_mfma_f32_16x16x32_bf16 v[0:3], a[48:51], a[0:3], v[0:3]\n"
        "ds_read_b128 a[204:207], v148 offset:27648\n"
        "s_add_u32 s60, s22, 0x3e0000\n"
        "s_addc_u32 s61, s23, 0\n"
        "v_mfma_f32_16x16x32_bf16 v[4:7], a[52:55], a[0:3], v[4:7]\n"
        "ds_read_b128 a[208:211], v151 offset:24576\n"
        "v_mfma_f32_16x16x32_bf16 v[8:11], a[48:51], a[4:7], v[8:11]\n"
        "ds_read_b128 a[212:215], v151 offset:25600\n"
        "v_mfma_f32_16x16x32_bf16 v[12:15], a[52:55], a[4:7], v[12:15]\n"
        "ds_read_b128 a[216:219], v151 offset:26624\n"
        "v_mfma_f32_16x16x32_bf16 v[16:19], a[48:51], a[8:11], v[16:19]\n"
        "ds_read_b128 a[220:223], v151 offset:27648\n"
        "s_waitcnt lgkmcnt(8)\n"
        "v_mfma_f32_16x16x32_bf16 v[20:23], a[52:55], a[8:11], v[20:23]\n"
        "v_mfma_f32_16x16x32_bf16 v[24:27], a[48:51], a[12:15], v[24:27]\n"
        "v_mfma_f32_16x16x32_bf16 v[28:31], a[52:55], a[12:15], v[28:31]\n"
        "v_mfma_f32_16x16x32_bf16 v[32:35], a[48:51], a[16:19], v[32:35]\n"
        "v_mfma_f32_16x16x32_bf16 v[36:39], a[52:55], a[16:19], v[36:39]\n"
        "s_waitcnt vmcnt(24)\n"
        "s_barrier\n"
        "v_mfma_f32_16x16x32_bf16 v[40:43], a[48:51], a[20:23], v[40:43]\n"
        "global_load_lds_dwordx4 v254, s[20:21] offset:0 sc1\n"
        "ds_read_b128 a[0:3], v149 offset:32768\n"
        "v_mfma_f32_16x16x32_bf16 v[44:47], a[52:55], a[20:23], v[44:47]\n"
        "ds_read_b128 a[4:7], v149 offset:33792\n"
        "s_waitcnt lgkmcnt(3)\n"
        "v_mfma_f32_16x16x32_bf16 v[48:51], a[48:51], a[24:27], v[48:51]\n"
        "ds_read_b128 a[8:11], v149 offset:34816\n"
        "v_mfma_f32_16x16x32_bf16 v[52:55], a[52:55], a[24:27], v[52:55]\n"
        "global_load_lds_dwordx4 v254, s[20:21] offset:1024 sc1\n"
        "ds_read_b128 a[12:15], v149 offset:35840\n"
        "v_mfma_f32_16x16x32_bf16 v[56:59], a[48:51], a[28:31], v[56:59]\n"
        "ds_read_b128 a[16:19], v152 offset:32768\n"
        "v_mfma_f32_16x16x32_bf16 v[60:63], a[52:55], a[28:31], v[60:63]\n"
        "ds_read_b128 a[20:23], v152 offset:33792\n"
        "v_mfma_f32_16x16x32_bf16 v[0:3], a[56:59], a[192:195], v[0:3]\n"
        "global_load_lds_dwordx4 v254, s[20:21] offset:2048 sc1\n"
        "ds_read_b128 a[24:27], v152 offset:34816\n"
        "v_mfma_f32_16x16x32_bf16 v[4:7], a[60:63], a[192:195], v[4:7]\n"
        "ds_read_b128 a[28:31], v152 offset:35840\n"
        "v_mfma_f32_16x16x32_bf16 v[8:11], a[56:59], a[196:199], v[8:11]\n"
        "ds_read_b128 a[192:195], v149 offset:40960\n"
        "v_mfma_f32_16x16x32_bf16 v[12:15], a[60:63], a[196:199], v[12:15]\n"
        "global_load_lds_dwordx4 v254, s[20:21] offset:3072 sc1\n"
        "s_add_u32 s20, s20, 0x4000\n"
        "s_addc_u32 s21, s21, 0\n"
        "v_mfma_f32_16x16x32_bf16 v[16:19], a[56:59], a[200:203], v[16:19]\n"
        "ds_read_b128 a[196:199], v149 offset:41984\n"
        "s_add_u32 m0, s73, 0x0\n"
        "v_mfma_f32_16x16x32_bf16 v[20:23], a[60:63], a[200:203], v[20:23]\n"
        "ds_read_b128 a[200:203], v149 offset:43008\n"
        "v_mfma_f32_16x16x32_bf16 v[24:27], a[56:59], a[204:207], v[24:27]\n"
        "v_mfma_f32_16x16x32_bf16 v[28:31], a[60:63], a[204:207], v[28:31]\n"
        "ds_read_b128 a[204:207], v149 offset:44032\n"
        "v_mfma_f32_16x16x32_bf16 v[32:35], a[56:59], a[208:211], v[32:35]\n"
        "s_waitcnt lgkmcnt(6)\n"
        "v_mfma_f32_16x16x32_bf16 v[36:39], a[60:63], a[208:211], v[36:39]\n"
        "ds_read_b128 a[208:211], v152 offset:40960\n"
        "v_mfma_f32_16x16x32_bf16 v[40:43], a[56:59], a[212:215], v[40:43]\n"
        "v_mfma_f32_16x16x32_bf16 v[44:47], a[60:63], a[212:215], v[44:47]\n"
        "ds_read_b128 a[212:215], v152 offset:41984\n"
        "v_mfma_f32_16x16x32_bf16 v[48:51], a[56:59], a[216:219], v[48:51]\n"
        "v_mfma_f32_16x16x32_bf16 v[52:55], a[60:63], a[216:219], v[52:55]\n"
        "ds_read_b128 a[216:219], v152 offset:43008\n"
        "v_mfma_f32_16x16x32_bf16 v[56:59], a[56:59], a[220:223], v[56:59]\n"
        "v_mfma_f32_16x16x32_bf16 v[60:63], a[60:63], a[220:223], v[60:63]\n"
        "ds_read_b128 a[220:223], v152 offset:44032\n"
        "v_mfma_f32_16x16x32_bf16 v[0:3], a[160:163], a[0:3], v[0:3]\n"
        "v_mfma_f32_16x16x32_bf16 v[4:7], a[164:167], a[0:3], v[4:7]\n"
        "v_mfma_f32_16x16x32_bf16 v[8:11], a[160:163], a[4:7], v[8:11]\n"
        "v_mfma_f32_16x16x32_bf16 v[12:15], a[164:167], a[4:7], v[12:15]\n"
        "v_mfma_f32_16x16x32_bf16 v[16:19], a[160:163], a[8:11], v[16:19]\n"
        "v_mfma_f32_16x16x32_bf16 v[20:23], a[164:167], a[8:11], v[20:23]\n"
        "v_mfma_f32_16x16x32_bf16 v[24:27], a[160:163], a[12:15], v[24:27]\n"
        "s_waitcnt lgkmcnt(1)\n"
        "v_mfma_f32_16x16x32_bf16 v[28:31], a[164:167], a[12:15], v[28:31]\n"
        "v_mfma_f32_16x16x32_bf16 v[32:35], a[160:163], a[16:19], v[32:35]\n"
        "v_mfma_f32_16x16x32_bf16 v[36:39], a[164:167], a[16:19], v[36:39]\n"
        "s_waitcnt vmcnt(20)\n"
        "s_barrier\n"
        "v_mfma_f32_16x16x32_bf16 v[40:43], a[160:163], a[20:23], v[40:43]\n"
        "global_load_lds_dwordx4 v144, s[60:61] offset:0\n"
        "ds_read_b64_tr_b16 a[32:33], v156 offset:49152\n"
        "v_mfma_f32_16x16x32_bf16 v[44:47], a[164:167], a[20:23], v[44:47]\n"
        "ds_read_b64_tr_b16 a[34:35], v156 offset:53248\n"
        "v_mfma_f32_16x16x32_bf16 v[48:51], a[160:163], a[24:27], v[48:51]\n"
        "ds_read_b64_tr_b16 a[36:37], v157 offset:49152\n"
        "v_mfma_f32_16x16x32_bf16 v[52:55], a[164:167], a[24:27], v[52:55]\n"
        "global_load_lds_dwordx4 v145, s[60:61] offset:1024\n"
        "ds_read_b64_tr_b16 a[38:39], v157 offset:53248\n"
        "v_mfma_f32_16x16x32_bf16 v[56:59], a[160:163], a[28:31], v[56:59]\n"
        "ds_read_b128 a[0:3], v150 offset:0\n"
        "v_mfma_f32_16x16x32_bf16 v[60:63], a[164:167], a[28:31], v[60:63]\n"
        "ds_read_b128 a[4:7], v150 offset:1024\n"
        "v_mfma_f32_16x16x32_bf16 v[0:3], a[168:171], a[192:195], v[0:3]\n"
        "global_load_lds_dwordx4 v146, s[60:61] offset:2048\n"
        "ds_read_b128 a[8:11], v150 offset:2048\n"
        "v_mfma_f32_16x16x32_bf16 v[4:7], a[172:175], a[192:195], v[4:7]\n"
        "ds_read_b128 a[12:15], v150 offset:3072\n"
        "v_mfma_f32_16x16x32_bf16 v[8:11], a[168:171], a[196:199], v[8:11]\n"
        "ds_read_b128 a[16:19], v153 offset:0\n"
        "v_mfma_f32_16x16x32_bf16 v[12:15], a[172:175], a[196:199], v[12:15]\n"
        "global_load_lds_dwordx4 v147, s[60:61] offset:3072\n"
        "s_add_u32 m0, s73, 0x4000\n"
        "v_mfma_f32_16x16x32_bf16 v[16:19], a[168:171], a[200:203], v[16:19]\n"
        "ds_read_b128 a[20:23], v153 offset:1024\n"
        "v_mfma_f32_16x16x32_bf16 v[20:23], a[172:175], a[200:203], v[20:23]\n"
        "ds_read_b128 a[24:27], v153 offset:2048\n"
        "v_mfma_f32_16x16x32_bf16 v[24:27], a[168:171], a[204:207], v[24:27]\n"
        "global_load_lds_dwordx4 v254, s[20:21] offset:0 sc1\n"
        "ds_read_b128 a[28:31], v153 offset:3072\n"
        "v_mfma_f32_16x16x32_bf16 v[28:31], a[172:175], a[204:207], v[28:31]\n"
        "ds_read_b64_tr_b16 a[40:41], v156 offset:57344\n"
        "v_mfma_f32_16x16x32_bf16 v[32:35], a[168:171], a[208:211], v[32:35]\n"
        "ds_read_b64_tr_b16 a[42:43], v156 offset:61440\n"
        "s_waitcnt lgkmcnt(7)\n"
        "v_mfma_f32_16x16x32_bf16 v[36:39], a[172:175], a[208:211], v[36:39]\n"
        "global_load_lds_dwordx4 v254, s[20:21] offset:1024 sc1\n"
        "ds_read_b64_tr_b16 a[44:45], v157 offset:57344\n"
        "v_mfma_f32_16x16x32_bf16 v[40:43], a[168:171], a[212:215], v[40:43]\n"
        "ds_read_b64_tr_b16 a[46:47], v157 offset:61440\n"
        "v_mfma_f32_16x16x32_bf16 v[44:47], a[172:175], a[212:215], v[44:47]\n"
        "ds_read_b128 a[192:195], v150 offset:8192\n"
        "v_mfma_f32_16x16x32_bf16 v[48:51], a[168:171], a[216:219], v[48:51]\n"
        "global_load_lds_dwordx4 v254, s[20:21] offset:2048 sc1\n"
        "ds_read_b128 a[196:199], v150 offset:9216\n"
        "v_mfma_f32_16x16x32_bf16 v[52:55], a[172:175], a[216:219], v[52:55]\n"
        "ds_read_b128 a[200:203], v150 offset:10240\n"
        "v_mfma_f32_16x16x32_bf16 v[56:59], a[168:171], a[220:223], v[56:59]\n"
        "ds_read_b128 a[204:207], v150 offset:11264\n"
        "v_mfma_f32_16x16x32_bf16 v[60:63], a[172:175], a[220:223], v[60:63]\n"
        "global_load_lds_dwordx4 v254, s[20:21] offset:3072 sc1\n"
        "ds_read_b128 a[208:211], v153 offset:8192\n"
        "s_add_u32 m0, s73, 0x18000\n"
        "v_mfma_f32_16x16x32_bf16 v[0:3], a[32:35], a[0:3], v[0:3]\n"
        "ds_read_b128 a[212:215], v153 offset:9216\n"
        "s_waitcnt lgkmcnt(8)\n"
        "v_mfma_f32_16x16x32_bf16 v[4:7], a[36:39], a[0:3], v[4:7]\n"
        "ds_read_b128 a[216:219], v153 offset:10240\n"
        "v_mfma_f32_16x16x32_bf16 v[8:11], a[32:35], a[4:7], v[8:11]\n"
        "ds_read_b128 a[220:223], v153 offset:11264\n"
        "v_mfma_f32_16x16x32_bf16 v[12:15], a[36:39], a[4:7], v[12:15]\n"
        "v_mfma_f32_16x16x32_bf16 v[16:19], a[32:35], a[8:11], v[16:19]\n"
        "v_mfma_f32_16x16x32_bf16 v[20:23], a[36:39], a[8:11], v[20:23]\n"
        "v_mfma_f32_16x16x32_bf16 v[24:27], a[32:35], a[12:15], v[24:27]\n"
        "v_mfma_f32_16x16x32_bf16 v[28:31], a[36:39], a[12:15], v[28:31]\n"
        "v_mfma_f32_16x16x32_bf16 v[32:35], a[32:35], a[16:19], v[32:35]\n"
        "v_mfma_f32_16x16x32_bf16 v[36:39], a[36:39], a[16:19], v[36:39]\n"
        "s_waitcnt vmcnt(20)\n"
        "s_barrier\n"
        "v_mfma_f32_16x16x32_bf16 v[40:43], a[32:35], a[20:23], v[40:43]\n"
        "global_load_lds_dwordx4 v224, s[58:59] offset:0 sc1\n"
        "ds_read_b64_tr_b16 a[48:49], v158 offset:16384\n"
        "s_add_u32 m0, s73, 0x18400\n"
        "v_mfma_f32_16x16x32_bf16 v[44:47], a[36:39], a[20:23], v[44:47]\n"
        "ds_read_b64_tr_b16 a[50:51], v158 offset:20480\n"
        "s_add_u32 s58, s58, 0x8000\n"
        "s_addc_u32 s59, s59, 0\n"
        "v_mfma_f32_16x16x32_bf16 v[48:51], a[32:35], a[24:27], v[48:51]\n"
        "ds_read_b64_tr_b16 a[52:53], v159 offset:16384\n"
        "s_waitcnt lgkmcnt(3)\n"
        "v_mfma_f32_16x16x32_bf16 v[52:55], a[36:39], a[24:27], v[52:55]\n"
        "ds_read_b64_tr_b16 a[54:55], v159 offset:20480\n"
        "global_load_lds_dwordx4 v224, s[58:59] offset:0 sc1\n"
        "s_add_u32 m0, s73, 0x18800\n"
        "v_mfma_f32_16x16x32_bf16 v[56:59], a[32:35], a[28:31], v[56:59]\n"
        "ds_read_b128 a[0:3], v148 offset:32768\n"
        "s_add_u32 s58, s58, 0x8000\n"
        "s_addc_u32 s59, s59, 0\n"
        "v_mfma_f32_16x16x32_bf16 v[60:63], a[36:39], a[28:31], v[60:63]\n"
        "ds_read_b128 a[4:7], v148 offset:33792\n"
        "v_mfma_f32_16x16x32_bf16 v[0:3], a[40:43], a[192:195], v[0:3]\n"
        "ds_read_b128 a[8:11], v148 offset:34816\n"
        "global_load_lds_dwordx4 v224, s[58:59] offset:0 sc1\n"
        "s_add_u32 m0, s73, 0x18c00\n"
        "v_mfma_f32_16x16x32_bf16 v[4:7], a[44:47], a[192:195], v[4:7]\n"
        "ds_read_b128 a[12:15], v148 offset:35840\n"
        "s_add_u32 s58, s58, 0x8000\n"
        "s_addc_u32 s59, s59, 0\n"
        "v_mfma_f32_16x16x32_bf16 v[8:11], a[40:43], a[196:199], v[8:11]\n"
        "ds_read_b128 a[16:19], v151 offset:32768\n"
        "v_mfma_f32_16x16x32_bf16 v[12:15], a[44:47], a[196:199], v[12:15]\n"
        "global_load_lds_dwordx4 v224, s[58:59] offset:0 sc1\n"
        "v_mfma_f32_16x16x32_bf16 v[16:19], a[40:43], a[200:203], v[16:19]\n"
        "ds_read_b128 a[20:23], v151 offset:33792\n"
        "v_mfma_f32_16x16x32_bf16 v[20:23], a[44:47], a[200:203], v[20:23]\n"
        "ds_read_b128 a[24:27], v151 offset:34816\n"
        "v_mfma_f32_16x16x32_bf16 v[24:27], a[40:43], a[204:207], v[24:27]\n"
        "global_load_dwordx4 v[112:115], v227, s[64:65]\n"
        "v_mfma_f32_16x16x32_bf16 v[28:31], a[44:47], a[204:207], v[28:31]\n"
        "ds_read_b128 a[28:31], v151 offset:35840\n"
        "s_add_u32 s64, s64, 0x8000\n"
        "s_addc_u32 s65, s65, 0\n"
        "v_mfma_f32_16x16x32_bf16 v[32:35], a[40:43], a[208:211], v[32:35]\n"
        "ds_read_b64_tr_b16 a[56:57], v158 offset:24576\n"
        "v_mfma_f32_16x16x32_bf16 v[36:39], a[44:47], a[208:211], v[36:39]\n"
        "global_load_dwordx4 v[116:119], v227, s[64:65]\n"
        "v_mfma_f32_16x16x32_bf16 v[40:43], a[40:43], a[212:215], v[40:43]\n"
        "ds_read_b64_tr_b16 a[58:59], v158 offset:28672\n"
        "s_add_u32 s64, s64, 0x8000\n"
        "s_addc_u32 s65, s65, 0\n"
        "v_mfma_f32_16x16x32_bf16 v[44:47], a[44:47], a[212:215], v[44:47]\n"
        "ds_read_b64_tr_b16 a[60:61], v159 offset:24576\n"
        "s_waitcnt lgkmcnt(6)\n"
        "v_mfma_f32_16x16x32_bf16 v[48:51], a[40:43], a[216:219], v[48:51]\n"
        "ds_read_b64_tr_b16 a[62:63], v159 offset:28672\n"
        "global_load_dwordx4 v[120:123], v227, s[64:65]\n"
        "v_mfma_f32_16x16x32_bf16 v[52:55], a[44:47], a[216:219], v[52:55]\n"
        "ds_read_b128 a[192:195], v148 offset:40960\n"
        "s_add_u32 s64, s64, 0x8000\n"
        "s_addc_u32 s65, s65, 0\n"
        "v_mfma_f32_16x16x32_bf16 v[56:59], a[40:43], a[220:223], v[56:59]\n"
        "ds_read_b128 a[196:199], v148 offset:41984\n"
        "v_mfma_f32_16x16x32_bf16 v[60:63], a[44:47], a[220:223], v[60:63]\n"
        "ds_read_b128 a[200:203], v148 offset:43008\n"
        "global_load_dwordx4 v[124:127], v227, s[64:65]\n"
        "v_mfma_f32_16x16x32_bf16 v[36:39], a[52:55], a[16:19], v[36:39]\n"
        "ds_read_b128 a[204:207], v148 offset:44032\n"
        "s_waitcnt vmcnt(24)\n"
        "v_mfma_f32_16x16x32_bf16 v[0:3], a[48:51], a[0:3], v[0:3]\n"
        "ds_read_b128 a[208:211], v151 offset:40960\n"
        "s_barrier\n"
        "v_mfma_f32_16x16x32_bf16 v[4:7], a[52:55], a[0:3], v[4:7]\n"
        "ds_read_b128 a[212:215], v151 offset:41984\n"
        "global_load_dword v128, v238, s[48:49] offset:64 sc1\n"
        "v_mfma_f32_16x16x32_bf16 v[32:35], a[48:51], a[16:19], v[32:35]\n"
        "ds_read_b128 a[216:219], v151 offset:43008\n"
        "v_mfma_f32_16x16x32_bf16 v[8:11], a[48:51], a[4:7], v[8:11]\n"
        "ds_read_b128 a[220:223], v151 offset:44032\n"
        "s_waitcnt lgkmcnt(8)\n"
        "v_mfma_f32_16x16x32_bf16 v[12:15], a[52:55], a[4:7], v[12:15]\n"
        "ds_read_b128 a[0:3], v148 offset:49152\n"
        "v_mfma_f32_16x16x32_bf16 v[16:19], a[48:51], a[8:11], v[16:19]\n"
        "ds_read_b128 a[16:19], v148 offset:57344\n"
        "v_mfma_f32_16x16x32_bf16 v[20:23], a[52:55], a[8:11], v[20:23]\n"
        "ds_read_b128 a[4:7], v148 offset:50176\n"
        "v_mfma_f32_16x16x32_bf16 v[24:27], a[48:51], a[12:15], v[24:27]\n"
        "ds_read_b128 a[8:11], v148 offset:51200\n"
        "v_mfma_f32_16x16x32_bf16 v[28:31], a[52:55], a[12:15], v[28:31]\n"
        "ds_read_b128 a[12:15], v148 offset:52224\n"
        "v_mfma_f32_16x16x32_bf16 v[40:43], a[48:51], a[20:23], v[40:43]\n"
        "s_waitcnt lgkmcnt(7)\n"
        "v_mfma_f32_16x16x32_bf16 v[44:47], a[52:55], a[20:23], v[44:47]\n"
        "ds_read_b128 a[20:23], v148 offset:58368\n"
        "v_mfma_f32_16x16x32_bf16 v[48:51], a[48:51], a[24:27], v[48:51]\n"
        "v_mfma_f32_16x16x32_bf16 v[52:55], a[52:55], a[24:27], v[52:55]\n"
        "ds_read_b128 a[24:27], v148 offset:59392\n"
        "v_mfma_f32_16x16x32_bf16 v[56:59], a[48:51], a[28:31], v[56:59]\n"
        "v_mfma_f32_16x16x32_bf16 v[60:63], a[52:55], a[28:31], v[60:63]\n"
        "ds_read_b128 a[28:31], v148 offset:60416\n"
        "v_mfma_f32_16x16x32_bf16 v[36:39], a[60:63], a[208:211], v[36:39]\n"
        "s_waitcnt vmcnt(17) lgkmcnt(8)\n"
        "s_barrier\n"
        "v_mfma_f32_16x16x32_bf16 v[4:7], a[60:63], a[192:195], v[4:7]\n"
        "ds_read_b64_tr_b16 a[32:33], v156 offset:0\n"
        "s_waitcnt lgkmcnt(4)\n"
        "v_mfma_f32_16x16x32_bf16 v[4:7], a[180:183], a[0:3], v[4:7]\n"
        "ds_read_b64_tr_b16 a[34:35], v156 offset:4096\n"
        "v_mfma_f32_16x16x32_bf16 v[0:3], a[56:59], a[192:195], v[0:3]\n"
        "ds_read_b128 a[192:195], v149 offset:16384\n"
        "v_mfma_f32_16x16x32_bf16 v[8:11], a[56:59], a[196:199], v[8:11]\n"
        "ds_read_b64_tr_b16 a[40:41], v156 offset:8192\n"
        "v_mfma_f32_16x16x32_bf16 v[12:15], a[60:63], a[196:199], v[12:15]\n"
        "ds_read_b128 a[196:199], v149 offset:17408\n"
        "v_mfma_f32_16x16x32_bf16 v[16:19], a[56:59], a[200:203], v[16:19]\n"
        "ds_read_b64_tr_b16 a[42:43], v156 offset:12288\n"
        "v_mfma_f32_16x16x32_bf16 v[20:23], a[60:63], a[200:203], v[20:23]\n"
        "ds_read_b128 a[200:203], v149 offset:18432\n"
        "v_mfma_f32_16x16x32_bf16 v[24:27], a[56:59], a[204:207], v[24:27]\n"
        "ds_read_b64_tr_b16 a[36:37], v157 offset:0\n"
        "v_mfma_f32_16x16x32_bf16 v[28:31], a[60:63], a[204:207], v[28:31]\n"
        "ds_read_b128 a[204:207], v149 offset:19456\n"
        "v_mfma_f32_16x16x32_bf16 v[4:7], a[188:191], a[16:19], v[4:7]\n"
        "ds_read_b64_tr_b16 a[38:39], v157 offset:4096\n"
        "v_mfma_f32_16x16x32_bf16 v[32:35], a[56:59], a[208:211], v[32:35]\n"
        "ds_read_b128 a[208:211], v149 offset:24576\n"
        "v_mfma_f32_16x16x32_bf16 v[40:43], a[56:59], a[212:215], v[40:43]\n"
        "ds_read_b64_tr_b16 a[44:45], v157 offset:8192\n"
        "v_mfma_f32_16x16x32_bf16 v[44:47], a[60:63], a[212:215], v[44:47]\n"
        "s_waitcnt vmcnt(9)\n"
        "v_mfma_f32_16x16x32_bf16 v[48:51], a[56:59], a[216:219], v[48:51]\n"
        "ds_read_b128 a[212:215], v149 offset:25600\n"
        "s_barrier\n"
        "v_mfma_f32_16x16x32_bf16 v[52:55], a[60:63], a[216:219], v[52:55]\n"
        "ds_read_b128 a[216:219], v149 offset:26624\n"
        "v_mfma_f32_16x16x32_bf16 v[56:59], a[56:59], a[220:223], v[56:59]\n"
        "ds_read_b64_tr_b16 a[48:49], v154 offset:0\n"
        "v_mfma_f32_16x16x32_bf16 v[60:63], a[60:63], a[220:223], v[60:63]\n"
        "ds_read_b128 a[220:223], v149 offset:27648\n"
        "v_mfma_f32_16x16x32_bf16 v[0:3], a[176:179], a[0:3], v[0:3]\n"
        "ds_read_b64_tr_b16 a[50:51], v154 offset:4096\n"
        "v_mfma_f32_16x16x32_bf16 v[8:11], a[176:179], a[4:7], v[8:11]\n"
        "ds_read_b128 a[0:3], v148 offset:16384\n"
        "v_mfma_f32_16x16x32_bf16 v[12:15], a[180:183], a[4:7], v[12:15]\n"
        "ds_read_b128 a[4:7], v148 offset:17408\n"
        "s_waitcnt lgkmcnt(7)\n"
        "v_mfma_f32_16x16x32_bf16 v[16:19], a[176:179], a[8:11], v[16:19]\n"
        "ds_read_b64_tr_b16 a[46:47], v157 offset:12288\n"
        "v_mfma_f32_16x16x32_bf16 v[20:23], a[180:183], a[8:11], v[20:23]\n"
        "ds_read_b128 a[8:11], v148 offset:18432\n"
        "v_mfma_f32_16x16x32_bf16 v[24:27], a[176:179], a[12:15], v[24:27]\n"
        "ds_read_b64_tr_b16 a[56:57], v154 offset:8192\n"
        "v_mfma_f32_16x16x32_bf16 v[28:31], a[180:183], a[12:15], v[28:31]\n"
        "ds_read_b128 a[12:15], v148 offset:19456\n"
        "v_mfma_f32_16x16x32_bf16 v[0:3], a[184:187], a[16:19], v[0:3]\n"
        "ds_read_b64_tr_b16 a[58:59], v154 offset:12288\n"
        "v_mfma_f32_16x16x32_bf16 v[8:11], a[184:187], a[20:23], v[8:11]\n"
        "ds_read_b128 a[16:19], v148 offset:24576\n"
        "v_mfma_f32_16x16x32_bf16 v[12:15], a[188:191], a[20:23], v[12:15]\n"
        "ds_read_b128 a[20:23], v148 offset:25600\n"
        "v_mfma_f32_16x16x32_bf16 v[16:19], a[184:187], a[24:27], v[16:19]\n"
        "ds_read_b64_tr_b16 a[52:53], v155 offset:0\n"
        "v_mfma_f32_16x16x32_bf16 v[20:23], a[188:191], a[24:27], v[20:23]\n"
        "ds_read_b128 a[24:27], v148 offset:26624\n"
        "v_mfma_f32_16x16x32_bf16 v[24:27], a[184:187], a[28:31], v[24:27]\n"
        "ds_read_b64_tr_b16 a[54:55], v155 offset:4096\n"
        "v_mfma_f32_16x16x32_bf16 v[28:31], a[188:191], a[28:31], v[28:31]\n"
        "ds_read_b128 a[28:31], v148 offset:27648\n"
        "v_mfma_f32_16x16x32_bf16 v[0:3], a[32:35], a[192:195], v[0:3]\n"
        "ds_read_b64_tr_b16 a[60:61], v155 offset:8192\n"
        "v_mfma_f32_16x16x32_bf16 v[8:11], a[32:35], a[196:199], v[8:11]\n"
        "ds_read_b64_tr_b16 a[62:63], v155 offset:12288\n"
        "s_waitcnt lgkmcnt(8)\n"
        "v_mfma_f32_16x16x32_bf16 v[16:19], a[32:35], a[200:203], v[16:19]\n"
        "v_mfma_f32_16x16x32_bf16 v[24:27], a[32:35], a[204:207], v[24:27]\n"
        "ds_read_b64_tr_b16 a[32:33], v156 offset:0\n"
        "v_mfma_f32_16x16x32_bf16 v[0:3], a[40:43], a[208:211], v[0:3]\n"
        "ds_read_b64_tr_b16 a[34:35], v156 offset:4096\n"
        "v_mfma_f32_16x16x32_bf16 v[8:11], a[40:43], a[212:215], v[8:11]\n"
        "v_mfma_f32_16x16x32_bf16 v[16:19], a[40:43], a[216:219], v[16:19]\n"
        "v_mfma_f32_16x16x32_bf16 v[24:27], a[40:43], a[220:223], v[24:27]\n"
        "ds_read_b64_tr_b16 a[40:41], v156 offset:8192\n"
        "v_mfma_f32_16x16x32_bf16 v[4:7], a[36:39], a[192:195], v[4:7]\n"
        "ds_read_b128 a[192:195], v151 offset:49152\n"
        "v_mfma_f32_16x16x32_bf16 v[12:15], a[36:39], a[196:199], v[12:15]\n"
        "ds_read_b128 a[196:199], v151 offset:50176\n"
        "v_mfma_f32_16x16x32_bf16 v[20:23], a[36:39], a[200:203], v[20:23]\n"
        "ds_read_b128 a[200:203], v151 offset:51200\n"
        "v_mfma_f32_16x16x32_bf16 v[28:31], a[36:39], a[204:207], v[28:31]\n"
        "ds_read_b128 a[204:207], v151 offset:52224\n"
        "v_mfma_f32_16x16x32_bf16 v[0:3], a[48:51], a[0:3], v[0:3]\n"
        "ds_read_b64_tr_b16 a[36:37], v157 offset:0\n"
        "v_mfma_f32_16x16x32_bf16 v[8:11], a[48:51], a[4:7], v[8:11]\n"
        "ds_read_b64_tr_b16 a[38:39], v157 offset:4096\n"
        "v_mfma_f32_16x16x32_bf16 v[16:19], a[48:51], a[8:11], v[16:19]\n"
        "s_waitcnt vmcnt(5)\n"
        "s_waitcnt lgkmcnt(7)\n"
        "v_mfma_f32_16x16x32_bf16 v[24:27], a[48:51], a[12:15], v[24:27]\n"
        "ds_read_b64_tr_b16 v[66:67], v225 offset:2048\n"
        "v_mfma_f32_16x16x32_bf16 v[4:7], a[44:47], a[208:211], v[4:7]\n"
        "ds_read_b64_tr_b16 v[70:71], v225 offset:2080\n"
        "v_mfma_f32_16x16x32_bf16 v[12:15], a[44:47], a[212:215], v[12:15]\n"
        "ds_read_b64_tr_b16 v[74:75], v225 offset:2112\n"
        "s_waitcnt vmcnt(4) lgkmcnt(7)\n"
        "v_mfma_f32_16x16x32_bf16 v[20:23], a[44:47], a[216:219], v[20:23]\n"
        "ds_read_b128 a[208:211], v151 offset:57344\n"
        "v_mfma_f32_16x16x32_bf16 v[28:31], a[44:47], a[220:223], v[28:31]\n"
        "ds_read_b128 a[220:223], v151 offset:60416\n"
        "v_mfma_f32_16x16x32_bf16 v[0:3], a[56:59], a[16:19], v[0:3]\n"
        "ds_read_b64_tr_b16 a[44:45], v157 offset:8192\n"
        "s_waitcnt vmcnt(3) lgkmcnt(7)\n"
        "v_mfma_f32_16x16x32_bf16 v[8:11], a[56:59], a[20:23], v[8:11]\n"
        "ds_read_b64_tr_b16 a[46:47], v157 offset:12288\n"
        "v_mfma_f32_16x16x32_bf16 v[16:19], a[56:59], a[24:27], v[16:19]\n"
        "ds_read_b128 a[212:215], v151 offset:58368\n"
        "v_mfma_f32_16x16x32_bf16 v[24:27], a[56:59], a[28:31], v[24:27]\n"
        "ds_read_b128 a[216:219], v151 offset:59392\n"
        "v_cvt_pk_bf16_f32 v0, v0, v1\n"
        "v_mfma_f32_16x16x32_bf16 v[4:7], a[52:55], a[0:3], v[4:7]\n"
        "s_waitcnt lgkmcnt(7)\n"
        "v_cvt_pk_bf16_f32 v8, v8, v9\n"
        "v_cvt_pk_bf16_f32 v1, v2, v3\n"
        "v_mfma_f32_16x16x32_bf16 v[12:15], a[52:55], a[4:7], v[12:15]\n"
        "s_waitcnt vmcnt(2) lgkmcnt(6)\n"
        "v_cvt_pk_bf16_f32 v16, v16, v17\n"
        "v_cvt_pk_bf16_f32 v24, v24, v25\n"
        "v_mfma_f32_16x16x32_bf16 v[20:23], a[52:55], a[8:11], v[20:23]\n"
        "s_waitcnt vmcnt(1) lgkmcnt(5)\n"
        "v_cvt_pk_bf16_f32 v9, v10, v11\n"
        "v_cvt_pk_bf16_f32 v17, v18, v19\n"
        "v_mfma_f32_16x16x32_bf16 v[28:31], a[52:55], a[12:15], v[28:31]\n"
        "ds_read_b128 a[12:15], v152 offset:19456\n"
        "v_cvt_pk_bf16_f32 v25, v26, v27\n"
        "v_mfma_f32_16x16x32_bf16 v[4:7], a[60:63], a[16:19], v[4:7]\n"
        "ds_read_b64_tr_b16 a[52:53], v155 offset:0\n"
        "v_lshlrev_b32 v84, 16, v66\n"
        "v_mfma_f32_16x16x32_bf16 v[12:15], a[60:63], a[20:23], v[12:15]\n"
        "ds_read_b64_tr_b16 a[54:55], v155 offset:4096\n"
        "s_waitcnt vmcnt(0) lgkmcnt(5)\n"
        "v_mfma_f32_16x16x32_bf16 v[20:23], a[60:63], a[24:27], v[20:23]\n"
        "v_permlane16_swap_b32 v112, v114\n"
        "v_mfma_f32_16x16x32_bf16 v[28:31], a[60:63], a[28:31], v[28:31]\n"
        "ds_read_b128 a[28:31], v152 offset:27648\n"
        "v_cvt_pk_bf16_f32 v2, v4, v5\n"
        "v_cvt_pk_bf16_f32 v3, v6, v7\n"
        "v_cvt_pk_bf16_f32 v10, v12, v13\n"
        "v_cvt_pk_bf16_f32 v11, v14, v15\n"
        "global_store_dwordx4 v254, v[0:3], s[28:29] offset:0 sc1\n"
        "ds_read_b64_tr_b16 a[60:61], v155 offset:8192\n"
        "ds_read_b64_tr_b16 a[62:63], v155 offset:12288\n"
        "v_cvt_pk_bf16_f32 v18, v20, v21\n"
        "v_cvt_pk_bf16_f32 v19, v22, v23\n"
        "v_cvt_pk_bf16_f32 v26, v28, v29\n"
        "v_cvt_pk_bf16_f32 v27, v30, v31\n"
        "v_mfma_f32_16x16x32_bf16 v[56:59], a[176:179], a[204:207], v[56:59]\n"
        "ds_read_b128 a[0:3], v152 offset:16384\n"
        "v_lshlrev_b32 v196, 16, v114\n"
        "global_store_dwordx4 v254, v[8:11], s[28:29] offset:1024 sc1\n"
        "ds_read_b128 a[4:7], v152 offset:17408\n"
        "v_permlane16_swap_b32 v116, v118\n"
        "v_mul_f32 v196, v196, v84\n"
        "v_mfma_f32_16x16x32_bf16 v[60:63], a[180:183], a[204:207], v[60:63]\n"
        "ds_read_b128 a[204:207], v151 offset:19456\n"
        "v_lshlrev_b32 v204, 16, v118\n"
        "v_mfma_f32_16x16x32_bf16 v[56:59], a[184:187], a[220:223], v[56:59]\n"
        "ds_read_b128 a[8:11], v152 offset:18432\n"
        "v_lshlrev_b32 v84, 16, v70\n"
        "global_store_dwordx4 v254, v[16:19], s[28:29] offset:2048 sc1\n"
        "ds_read_b64_tr_b16 a[42:43], v156 offset:12288\n"
        "v_permlane16_swap_b32 v120, v122\n"
        "v_mul_f32 v204, v204, v84\n"
        "v_mfma_f32_16x16x32_bf16 v[60:63], a[188:191], a[220:223], v[60:63]\n"
        "ds_read_b128 a[220:223], v151 offset:27648\n"
        "s_waitcnt lgkmcnt(6)\n"
        "v_mfma_f32_16x16x32_bf16 v[60:63], a[36:39], a[12:15], v[60:63]\n"
        "ds_read_b128 a[16:19], v152 offset:24576\n"
        "v_lshlrev_b32 v212, 16, v122\n"
        "global_store_dwordx4 v254, v[24:27], s[28:29] offset:3072 sc1\n"
        "ds_read_b64_tr_b16 v[78:79], v225 offset:2144\n"
        "ds_read_b128 a[20:23], v152 offset:25600\n"
        "v_lshlrev_b32 v84, 16, v74\n"
        "v_mfma_f32_16x16x32_bf16 v[60:63], a[44:47], a[28:31], v[60:63]\n"
        "s_waitcnt lgkmcnt(6)\n"
        "v_mul_f32 v212, v212, v84\n"
        "v_cvt_pk_bf16_f32 v212, 0, v212\n"
        "v_mfma_f32_16x16x32_bf16 v[60:63], a[52:55], a[204:207], v[60:63]\n"
        "ds_read_b128 a[24:27], v152 offset:26624\n"
        "v_and_b32 v87, 0xffff0000, v67\n"
        "v_mfma_f32_16x16x32_bf16 v[32:35], a[176:179], a[192:195], v[32:35]\n"
        "ds_read_b64_tr_b16 a[48:49], v154 offset:0\n"
        "v_mov_b32 v129, v128\n"
        "v_mfma_f32_16x16x32_bf16 v[36:39], a[180:183], a[192:195], v[36:39]\n"
        "ds_read_b64_tr_b16 a[50:51], v154 offset:4096\n"
        "s_waitcnt lgkmcnt(7)\n"
        "v_mfma_f32_16x16x32_bf16 v[40:43], a[176:179], a[196:199], v[40:43]\n"
        "ds_read_b128 a[192:195], v151 offset:16384\n"
        "s_waitcnt lgkmcnt(7)\n"
        "v_mfma_f32_16x16x32_bf16 v[60:63], a[60:63], a[220:223], v[60:63]\n"
        "v_permlane16_swap_b32 v113, v115\n"
        "v_mfma_f32_16x16x32_bf16 v[44:47], a[180:183], a[196:199], v[44:47]\n"
        "ds_read_b128 a[196:199], v151 offset:17408\n"
        "v_and_b32 v199, 0xffff0000, v115\n"
        "v_mfma_f32_16x16x32_bf16 v[48:51], a[176:179], a[200:203], v[48:51]\n"
        "s_waitcnt lgkmcnt(5)\n"
        "v_permlane16_swap_b32 v117, v119\n"
        "v_mfma_f32_16x16x32_bf16 v[52:55], a[180:183], a[200:203], v[52:55]\n"
        "ds_read_b128 a[200:203], v151 offset:18432\n"
        "v_mul_f32 v199, v199, v87\n"
        "v_mfma_f32_16x16x32_bf16 v[32:35], a[184:187], a[208:211], v[32:35]\n"
        "ds_read_b64_tr_b16 a[56:57], v154 offset:8192\n"
        "v_and_b32 v207, 0xffff0000, v119\n"
        "v_mfma_f32_16x16x32_bf16 v[36:39], a[188:191], a[208:211], v[36:39]\n"
        "ds_read_b64_tr_b16 a[58:59], v154 offset:12288\n"
        "v_and_b32 v87, 0xffff0000, v71\n"
        "v_mfma_f32_16x16x32_bf16 v[40:43], a[184:187], a[212:215], v[40:43]\n"
        "v_permlane16_swap_b32 v121, v123\n"
        "v_mfma_f32_16x16x32_bf16 v[44:47], a[188:191], a[212:215], v[44:47]\n"
        "ds_read_b128 a[208:211], v151 offset:24576\n"
        "v_mul_f32 v207, v207, v87\n"
        "v_mfma_f32_16x16x32_bf16 v[48:51], a[184:187], a[216:219], v[48:51]\n"
        "v_permlane16_swap_b32 v128, v129\n"
        "v_mfma_f32_16x16x32_bf16 v[52:55], a[188:191], a[216:219], v[52:55]\n"
        "ds_read_b128 a[212:215], v151 offset:25600\n"
        "v_and_b32 v215, 0xffff0000, v123\n"
        "v_mfma_f32_16x16x32_bf16 v[32:35], a[32:35], a[0:3], v[32:35]\n"
        "ds_read_b128 a[216:219], v151 offset:26624\n"
        "v_and_b32 v87, 0xffff0000, v75\n"
        "v_mfma_f32_16x16x32_bf16 v[36:39], a[36:39], a[0:3], v[36:39]\n"
        "v_permlane16_swap_b32 v125, v127\n"
        "v_mfma_f32_16x16x32_bf16 v[40:43], a[32:35], a[4:7], v[40:43]\n"
        "ds_read_b64_tr_b16 v[64:65], v225 offset:0\n"
        "v_mov_b32 v131, v129\n"
        "v_mfma_f32_16x16x32_bf16 v[44:47], a[36:39], a[4:7], v[44:47]\n"
        "ds_read_b64_tr_b16 v[68:69], v225 offset:32\n"
        "v_mul_f32 v215, v215, v87\n"
        "v_mfma_f32_16x16x32_bf16 v[48:51], a[32:35], a[8:11], v[48:51]\n"
        "ds_read_b64_tr_b16 v[72:73], v225 offset:64\n"
        "v_and_b32 v223, 0xffff0000, v127\n"
        "v_mfma_f32_16x16x32_bf16 v[52:55], a[36:39], a[8:11], v[52:55]\n"
        "s_waitcnt lgkmcnt(5)\n"
        "v_permlane32_swap_b32 v129, v131\n"
        "v_mfma_f32_16x16x32_bf16 v[56:59], a[32:35], a[12:15], v[56:59]\n"
        "ds_read_b64_tr_b16 v[76:77], v225 offset:96\n"
        "v_and_b32 v87, 0xffff0000, v79\n"
        "v_mfma_f32_16x16x32_bf16 v[32:35], a[40:43], a[16:19], v[32:35]\n"
        "v_sub_f32 v135, 1.0, v131\n"
        "v_mul_f32 v223, v223, v87\n"
        "v_mfma_f32_16x16x32_bf16 v[36:39], a[44:47], a[16:19], v[36:39]\n"
        "v_cvt_pk_bf16_f32 v135, 0, v135\n"
        "v_cvt_pk_bf16_f32 v223, 0, v223\n"
        "v_mfma_f32_16x16x32_bf16 v[40:43], a[40:43], a[20:23], v[40:43]\n"
        "v_mul_f32 v223, v223, v135\n"
        "v_cvt_pk_bf16_f32 v223, 0, v223\n"
        "v_mfma_f32_16x16x32_bf16 v[44:47], a[44:47], a[20:23], v[44:47]\n"
        "s_waitcnt lgkmcnt(3)\n"
        "v_mov_b32 v130, v128\n"
        "v_sub_f32 v133, 1.0, v129\n"
        "v_mfma_f32_16x16x32_bf16 v[48:51], a[40:43], a[24:27], v[48:51]\n"
        "v_permlane32_swap_b32 v128, v130\n"
        "v_mfma_f32_16x16x32_bf16 v[52:55], a[44:47], a[24:27], v[52:55]\n"
        "v_permlane16_swap_b32 v124, v126\n"
        "v_mfma_f32_16x16x32_bf16 v[56:59], a[40:43], a[28:31], v[56:59]\n"
        "v_sub_f32 v132, 1.0, v128\n"
        "v_cvt_pk_bf16_f32 v132, 0, v132\n"
        "v_mfma_f32_16x16x32_bf16 v[32:35], a[48:51], a[192:195], v[32:35]\n"
        "v_cvt_pk_bf16_f32 v133, 0, v133\n"
        "v_sub_f32 v134, 1.0, v130\n"
        "v_mfma_f32_16x16x32_bf16 v[36:39], a[52:55], a[192:195], v[36:39]\n"
        "v_cvt_pk_bf16_f32 v134, 0, v134\n"
        "v_lshlrev_b32 v192, 16, v112\n"
        "v_mfma_f32_16x16x32_bf16 v[40:43], a[48:51], a[196:199], v[40:43]\n"
        "v_and_b32 v193, 0xffff0000, v112\n"
        "v_lshlrev_b32 v194, 16, v113\n"
        "v_mfma_f32_16x16x32_bf16 v[44:47], a[52:55], a[196:199], v[44:47]\n"
        "v_and_b32 v195, 0xffff0000, v113\n"
        "v_lshlrev_b32 v80, 16, v64\n"
        "v_mfma_f32_16x16x32_bf16 v[48:51], a[48:51], a[200:203], v[48:51]\n"
        "v_and_b32 v81, 0xffff0000, v64\n"
        "v_lshlrev_b32 v82, 16, v65\n"
        "v_mfma_f32_16x16x32_bf16 v[52:55], a[52:55], a[200:203], v[52:55]\n"
        "v_and_b32 v83, 0xffff0000, v65\n"
        "v_mul_f32 v192, v192, v80\n"
        "v_mfma_f32_16x16x32_bf16 v[56:59], a[48:51], a[204:207], v[56:59]\n"
        "s_waitcnt lgkmcnt(0)\n"
        "v_mul_f32 v193, v193, v81\n"
        "v_mul_f32 v194, v194, v82\n"
        "v_mfma_f32_16x16x32_bf16 v[32:35], a[56:59], a[208:211], v[32:35]\n"
        "v_mul_f32 v195, v195, v83\n"
        "v_cvt_pk_bf16_f32 v192, 0, v192\n"
        "v_mfma_f32_16x16x32_bf16 v[36:39], a[60:63], a[208:211], v[36:39]\n"
        "v_cvt_pk_bf16_f32 v193, 0, v193\n"
        "v_cvt_pk_bf16_f32 v194, 0, v194\n"
        "v_mfma_f32_16x16x32_bf16 v[40:43], a[56:59], a[212:215], v[40:43]\n"
        "v_cvt_pk_bf16_f32 v195, 0, v195\n"
        "v_mul_f32 v192, v192, v132\n"
        "v_mfma_f32_16x16x32_bf16 v[44:47], a[60:63], a[212:215], v[44:47]\n"
        "v_mul_f32 v193, v193, v132\n"
        "v_mul_f32 v194, v194, v132\n"
        "v_mfma_f32_16x16x32_bf16 v[48:51], a[56:59], a[216:219], v[48:51]\n"
        "v_mul_f32 v195, v195, v132\n"
        "v_cvt_pk_bf16_f32 v192, 0, v192\n"
        "v_mfma_f32_16x16x32_bf16 v[52:55], a[60:63], a[216:219], v[52:55]\n"
        "v_cvt_pk_bf16_f32 v193, 0, v193\n"
        "v_cvt_pk_bf16_f32 v194, 0, v194\n"
        "v_mfma_f32_16x16x32_bf16 v[56:59], a[56:59], a[220:223], v[56:59]\n"
        "v_cvt_pk_bf16_f32 v195, 0, v195\n"
        "v_and_b32 v197, 0xffff0000, v114\n"
        "v_lshlrev_b32 v198, 16, v115\n"
        "v_and_b32 v85, 0xffff0000, v66\n"
        "v_lshlrev_b32 v86, 16, v67\n"
        "v_mul_f32 v197, v197, v85\n"
        "v_mul_f32 v198, v198, v86\n"
        "v_cvt_pk_bf16_f32 v196, 0, v196\n"
        "v_cvt_pk_bf16_f32 v197, 0, v197\n"
        "v_cvt_pk_bf16_f32 v198, 0, v198\n"
        "v_cvt_pk_bf16_f32 v199, 0, v199\n"
        "v_mul_f32 v196, v196, v132\n"
        "v_mul_f32 v197, v197, v132\n"
        "v_mul_f32 v198, v198, v132\n"
        "v_mul_f32 v199, v199, v132\n"
        "v_cvt_pk_bf16_f32 v196, 0, v196\n"
        "v_cvt_pk_bf16_f32 v197, 0, v197\n"
        "v_cvt_pk_bf16_f32 v198, 0, v198\n"
        "v_cvt_pk_bf16_f32 v199, 0, v199\n"
        "v_lshlrev_b32 v200, 16, v116\n"
        "v_and_b32 v201, 0xffff0000, v116\n"
        "v_lshlrev_b32 v202, 16, v117\n"
        "v_and_b32 v203, 0xffff0000, v117\n"
        "v_lshlrev_b32 v80, 16, v68\n"
        "v_and_b32 v81, 0xffff0000, v68\n"
        "v_lshlrev_b32 v82, 16, v69\n"
        "v_and_b32 v83, 0xffff0000, v69\n"
        "v_mul_f32 v200, v200, v80\n"
        "v_mul_f32 v201, v201, v81\n"
        "v_mul_f32 v202, v202, v82\n"
        "v_mul_f32 v203, v203, v83\n"
        "v_cvt_pk_bf16_f32 v200, 0, v200\n"
        "v_cvt_pk_bf16_f32 v201, 0, v201\n"
        "v_cvt_pk_bf16_f32 v202, 0, v202\n"
        "v_cvt_pk_bf16_f32 v203, 0, v203\n"
        "v_mul_f32 v200, v200, v133\n"
        "v_mul_f32 v201, v201, v133\n"
        "v_mul_f32 v202, v202, v133\n"
        "v_mul_f32 v203, v203, v133\n"
        "v_cvt_pk_bf16_f32 v200, 0, v200\n"
        "v_cvt_pk_bf16_f32 v201, 0, v201\n"
        "v_cvt_pk_bf16_f32 v202, 0, v202\n"
        "v_cvt_pk_bf16_f32 v203, 0, v203\n"
        "v_and_b32 v205, 0xffff0000, v118\n"
        "v_lshlrev_b32 v206, 16, v119\n"
        "v_and_b32 v85, 0xffff0000, v70\n"
        "v_lshlrev_b32 v86, 16, v71\n"
        "v_mul_f32 v205, v205, v85\n"
        "v_mul_f32 v206, v206, v86\n"
        "s_waitcnt vmcnt(0)\n"
        "s_mov_b64 exec, 0xffff\n"
        "global_store_dwordx2 v234, v[250:251], s[48:49] sc1\n"
        "s_mov_b64 exec, -1\n"
        "s_or_b32 s97, s55, s40\n"
        "s_cbranch_scc0 LEP_%=\n"
        "LEB_%=:\n"
        "global_load_dwordx2 v[236:237], v235, s[48:49] sc1\n"
        "v_cvt_pk_bf16_f32 v204, 0, v204\n"
        "v_cvt_pk_bf16_f32 v205, 0, v205\n"
        "v_cvt_pk_bf16_f32 v206, 0, v206\n"
        "v_cvt_pk_bf16_f32 v207, 0, v207\n"
        "v_mul_f32 v204, v204, v133\n"
        "v_mul_f32 v205, v205, v133\n"
        "v_mul_f32 v206, v206, v133\n"
        "v_mul_f32 v207, v207, v133\n"
        "v_cvt_pk_bf16_f32 v204, 0, v204\n"
        "v_cvt_pk_bf16_f32 v205, 0, v205\n"
        "v_cvt_pk_bf16_f32 v206, 0, v206\n"
        "v_cvt_pk_bf16_f32 v207, 0, v207\n"
        "v_lshlrev_b32 v208, 16, v120\n"
        "v_and_b32 v209, 0xffff0000, v120\n"
        "v_lshlrev_b32 v210, 16, v121\n"
        "v_and_b32 v211, 0xffff0000, v121\n"
        "v_lshlrev_b32 v80, 16, v72\n"
        "v_and_b32 v81, 0xffff0000, v72\n"
        "v_lshlrev_b32 v82, 16, v73\n"
        "v_and_b32 v83, 0xffff0000, v73\n"
        "v_mul_f32 v208, v208, v80\n"
        "v_mul_f32 v209, v209, v81\n"
        "v_mul_f32 v210, v210, v82\n"
        "v_mul_f32 v211, v211, v83\n"
        "v_cvt_pk_bf16_f32 v208, 0, v208\n"
        "v_cvt_pk_bf16_f32 v209, 0, v209\n"
        "v_cvt_pk_bf16_f32 v210, 0, v210\n"
        "v_cvt_pk_bf16_f32 v211, 0, v211\n"
        "v_mul_f32 v208, v208, v134\n"
        "v_mul_f32 v209, v209, v134\n"
        "v_mul_f32 v210, v210, v134\n"
        "v_mul_f32 v211, v211, v134\n"
        "v_cvt_pk_bf16_f32 v208, 0, v208\n"
        "v_cvt_pk_bf16_f32 v209, 0, v209\n"
        "v_cvt_pk_bf16_f32 v210, 0, v210\n"
        "v_cvt_pk_bf16_f32 v211, 0, v211\n"
        "v_and_b32 v213, 0xffff0000, v122\n"
        "v_lshlrev_b32 v214, 16, v123\n"
        "v_and_b32 v85, 0xffff0000, v74\n"
        "v_lshlrev_b32 v86, 16, v75\n"
        "v_mul_f32 v213, v213, v85\n"
        "v_mul_f32 v214, v214, v86\n"
        "v_cvt_pk_bf16_f32 v213, 0, v213\n"
        "v_cvt_pk_bf16_f32 v214, 0, v214\n"
        "v_cvt_pk_bf16_f32 v215, 0, v215\n"
        "v_mul_f32 v212, v212, v134\n"
        "v_mul_f32 v213, v213, v134\n"
        "v_mul_f32 v214, v214, v134\n"
        "v_mul_f32 v215, v215, v134\n"
        "v_cvt_pk_bf16_f32 v212, 0, v212\n"
        "v_cvt_pk_bf16_f32 v213, 0, v213\n"
        "v_cvt_pk_bf16_f32 v214, 0, v214\n"
        "v_cvt_pk_bf16_f32 v215, 0, v215\n"
        "v_lshlrev_b32 v216, 16, v124\n"
        "v_and_b32 v217, 0xffff0000, v124\n"
        "v_lshlrev_b32 v218, 16, v125\n"
        "v_and_b32 v219, 0xffff0000, v125\n"
        "v_lshlrev_b32 v80, 16, v76\n"
        "v_and_b32 v81, 0xffff0000, v76\n"
        "v_lshlrev_b32 v82, 16, v77\n"
        "v_and_b32 v83, 0xffff0000, v77\n"
        "v_mul_f32 v216, v216, v80\n"
        "v_mul_f32 v217, v217, v81\n"
        "v_mul_f32 v218, v218, v82\n"
        "v_mul_f32 v219, v219, v83\n"
        "v_cvt_pk_bf16_f32 v216, 0, v216\n"
        "v_cvt_pk_bf16_f32 v217, 0, v217\n"
        "v_cvt_pk_bf16_f32 v218, 0, v218\n"
        "v_cvt_pk_bf16_f32 v219, 0, v219\n"
        "v_mul_f32 v216, v216, v135\n"
        "v_mul_f32 v217, v217, v135\n"
        "v_mul_f32 v218, v218, v135\n"
        "v_mul_f32 v219, v219, v135\n"
        "s_waitcnt vmcnt(0)\n"
        "v_cmp_ne_u64 vcc, v[236:237], v[250:251]\n"
        "v_cvt_pk_bf16_f32 v216, 0, v216\n"
        "s_cbranch_vccnz LPPR_%=\n"
        "LBPR_%=:\n"
        "global_load_dwordx4 v[96:99], v254, s[30:31] offset:0 sc1\n"
        "global_load_dwordx4 v[100:103], v254, s[30:31] offset:1024 sc1\n"
        "global_load_dwordx4 v[104:107], v254, s[30:31] offset:2048 sc1\n"
        "global_load_dwordx4 v[108:111], v254, s[30:31] offset:3072 sc1\n"
        "v_cvt_pk_bf16_f32 v217, 0, v217\n"
        "v_cvt_pk_bf16_f32 v218, 0, v218\n"
        "v_cvt_pk_bf16_f32 v219, 0, v219\n"
        "v_lshlrev_b32 v220, 16, v126\n"
        "v_and_b32 v221, 0xffff0000, v126\n"
        "v_lshlrev_b32 v222, 16, v127\n"
        "v_lshlrev_b32 v84, 16, v78\n"
        "v_and_b32 v85, 0xffff0000, v78\n"
        "v_lshlrev_b32 v86, 16, v79\n"
        "v_mul_f32 v220, v220, v84\n"
        "v_mul_f32 v221, v221, v85\n"
        "v_mul_f32 v222, v222, v86\n"
        "v_cvt_pk_bf16_f32 v220, 0, v220\n"
        "v_cvt_pk_bf16_f32 v221, 0, v221\n"
        "v_cvt_pk_bf16_f32 v222, 0, v222\n"
        "v_mul_f32 v220, v220, v135\n"
        "v_mul_f32 v221, v221, v135\n"
        "v_mul_f32 v222, v222, v135\n"
        "v_cvt_pk_bf16_f32 v220, 0, v220\n"
        "v_cvt_pk_bf16_f32 v221, 0, v221\n"
        "v_cvt_pk_bf16_f32 v222, 0, v222\n"
        "s_waitcnt vmcnt(3)\n"
        "v_mfma_f32_16x16x32_bf16 v[32:35], v[138:141], v[96:99], v[32:35]\n"
        "v_mfma_f32_16x16x32_bf16 v[36:39], v[136:139], v[96:99], v[36:39]\n"
        "s_waitcnt vmcnt(2)\n"
        "v_mfma_f32_16x16x32_bf16 v[40:43], v[138:141], v[100:103], v[40:43]\n"
        "v_mfma_f32_16x16x32_bf16 v[44:47], v[136:139], v[100:103], v[44:47]\n"
        "s_waitcnt vmcnt(1)\n"
        "v_mfma_f32_16x16x32_bf16 v[48:51], v[138:141], v[104:107], v[48:51]\n"
        "v_mfma_f32_16x16x32_bf16 v[52:55], v[136:139], v[104:107], v[52:55]\n"
        "s_waitcnt vmcnt(0)\n"
        "v_fmac_f32 v192, v32, v128\n"
        "v_fmac_f32 v193, v33, v128\n"
        "v_fmac_f32 v194, v34, v128\n"
        "v_fmac_f32 v195, v35, v128\n"
        "v_fmac_f32 v196, v36, v128\n"
        "v_fmac_f32 v197, v37, v128\n"
        "v_mfma_f32_16x16x32_bf16 v[56:59], v[138:141], v[108:111], v[56:59]\n"
        "v_fmac_f32 v198, v38, v128\n"
        "v_fmac_f32 v199, v39, v128\n"
        "v_mfma_f32_16x16x32_bf16 v[60:63], v[136:139], v[108:111], v[60:63]\n"
        "v_cvt_pk_bf16_f32 v192, v192, v193\n"
        "v_cvt_pk_bf16_f32 v193, v194, v195\n"
        "v_cvt_pk_bf16_f32 v194, v196, v197\n"
        "v_cvt_pk_bf16_f32 v195, v198, v199\n"
        "v_fmac_f32 v200, v40, v129\n"
        "v_permlane16_swap_b32 v192, v194\n"
        "v_permlane16_swap_b32 v193, v195\n"
        "global_store_dwordx4 v227, v[192:195], s[56:57]\n"
        "s_add_u32 s56, s56, 0x8000\n"
        "s_addc_u32 s57, s57, 0\n"
        "v_fmac_f32 v201, v41, v129\n"
        "v_fmac_f32 v202, v42, v129\n"
        "v_fmac_f32 v203, v43, v129\n"
        "v_fmac_f32 v204, v44, v129\n"
        "v_fmac_f32 v205, v45, v129\n"
        "v_fmac_f32 v206, v46, v129\n"
        "v_fmac_f32 v207, v47, v129\n"
        "v_cvt_pk_bf16_f32 v200, v200, v201\n"
        "v_cvt_pk_bf16_f32 v201, v202, v203\n"
        "v_cvt_pk_bf16_f32 v202, v204, v205\n"
        "v_cvt_pk_bf16_f32 v203, v206, v207\n"
        "v_fmac_f32 v208, v48, v130\n"
        "v_permlane16_swap_b32 v200, v202\n"
        "v_permlane16_swap_b32 v201, v203\n"
        "global_store_dwordx4 v227, v[200:203], s[56:57]\n"
        "s_add_u32 s56, s56, 0x8000\n"
        "s_addc_u32 s57, s57, 0\n"
        "v_fmac_f32 v209, v49, v130\n"
        "v_fmac_f32 v210, v50, v130\n"
        "v_fmac_f32 v211, v51, v130\n"
        "v_fmac_f32 v212, v52, v130\n"
        "v_fmac_f32 v213, v53, v130\n"
        "v_fmac_f32 v214, v54, v130\n"
        "v_fmac_f32 v215, v55, v130\n"
        "v_cvt_pk_bf16_f32 v208, v208, v209\n"
        "v_cvt_pk_bf16_f32 v209, v210, v211\n"
        "v_cvt_pk_bf16_f32 v210, v212, v213\n"
        "v_cvt_pk_bf16_f32 v211, v214, v215\n"
        "v_fmac_f32 v216, v56, v131\n"
        "v_permlane16_swap_b32 v208, v210\n"
        "v_permlane16_swap_b32 v209, v211\n"
        "global_store_dwordx4 v227, v[208:211], s[56:57]\n"
        "s_add_u32 s56, s56, 0x8000\n"
        "s_addc_u32 s57, s57, 0\n"
        "v_fmac_f32 v217, v57, v131\n"
        "v_fmac_f32 v218, v58, v131\n"
        "v_fmac_f32 v219, v59, v131\n"
        "v_fmac_f32 v220, v60, v131\n"
        "v_fmac_f32 v221, v61, v131\n"
        "v_fmac_f32 v222, v62, v131\n"
        "v_fmac_f32 v223, v63, v131\n"
        "v_cvt_pk_bf16_f32 v216, v216, v217\n"
        "v_cvt_pk_bf16_f32 v217, v218, v219\n"
        "v_cvt_pk_bf16_f32 v218, v220, v221\n"
        "v_cvt_pk_bf16_f32 v219, v222, v223\n"
        "s_nop 0\n"
        "v_permlane16_swap_b32 v216, v218\n"
        "v_permlane16_swap_b32 v217, v219\n"
        "global_store_dwordx4 v227, v[216:219], s[56:57]\n"
        "; ---- END\n"
        "s_branch LEND_%=\n"
        "LPKV_%=:\n"
        "s_mov_b32 s97, 0x200000\n"
        "LQKV_%=:\n"
        "global_load_dwordx2 v[252:253], v245, s[48:49] sc1\n"
        "s_waitcnt vmcnt(0)\n"
        "v_cmp_ne_u64 vcc, v[252:253], v[250:251]\n"
        "s_nop 1\n"
        "s_cbranch_vccz LBKV_%=\n"
        "s_sleep 2\n"
        "s_sub_u32 s97, s97, 1\n"
        "s_cmp_lg_u32 s97, 0\n"
        "s_cbranch_scc1 LQKV_%=\n"
        "s_branch LBKV_%=\n"
        "LRL_%=:\n"
        "s_waitcnt vmcnt(0) lgkmcnt(0)\n"
        "s_nop 7\n"
        "v_accvgpr_write_b32 a176, v96\n"
        "v_accvgpr_write_b32 a177, v97\n"
        "v_accvgpr_write_b32 a178, v98\n"
        "v_accvgpr_write_b32 a179, v99\n"
        "v_accvgpr_write_b32 a180, v100\n"
        "v_accvgpr_write_b32 a181, v101\n"
        "v_accvgpr_write_b32 a182, v102\n"
        "v_accvgpr_write_b32 a183, v103\n"
        "v_accvgpr_write_b32 a184, v104\n"
        "v_accvgpr_write_b32 a185, v105\n"
        "v_accvgpr_write_b32 a186, v106\n"
        "v_accvgpr_write_b32 a187, v107\n"
        "v_accvgpr_write_b32 a188, v108\n"
        "v_accvgpr_write_b32 a189, v109\n"
        "v_accvgpr_write_b32 a190, v110\n"
        "v_accvgpr_write_b32 a191, v111\n"
        "s_mov_b32 s53, s44\n"
        "s_mov_b32 s96, 64\n"
        "s_mov_b32 s52, 0\n"
        "s_branch LREDO_%=\n"
        "LRET0_%=:\n"
        "v_accvgpr_write_b32 a176, v128\n"
        "v_accvgpr_write_b32 a177, v129\n"
        "v_accvgpr_write_b32 a178, v130\n"
        "v_accvgpr_write_b32 a179, v131\n"
        "v_accvgpr_write_b32 a180, v132\n"
        "v_accvgpr_write_b32 a181, v133\n"
        "v_accvgpr_write_b32 a182, v134\n"
        "v_accvgpr_write_b32 a183, v135\n"
        "v_accvgpr_write_b32 a184, v136\n"
        "v_accvgpr_write_b32 a185, v137\n"
        "v_accvgpr_write_b32 a186, v138\n"
        "v_accvgpr_write_b32 a187, v139\n"
        "v_accvgpr_write_b32 a188, v140\n"
        "v_accvgpr_write_b32 a189, v141\n"
        "v_accvgpr_write_b32 a190, v142\n"
        "v_accvgpr_write_b32 a191, v143\n"
        "s_mov_b32 s53, s44\n"
        "s_mov_b32 s96, 0\n"
        "s_mov_b32 s52, 1\n"
        "s_branch LREDO_%=\n"
        "LRET1_%=:\n"
        "s_branch LSL_%=\n"
        "LRH_%=:\n"
        "s_waitcnt vmcnt(0) lgkmcnt(0)\n"
        "s_nop 7\n"
        "v_accvgpr_write_b32 a176, v0\n"
        "v_accvgpr_write_b32 a177, v1\n"
        "v_accvgpr_write_b32 a178, v2\n"
        "v_accvgpr_write_b32 a179, v3\n"
        "v_accvgpr_write_b32 a180, v4\n"
        "v_accvgpr_write_b32 a181, v5\n"
        "v_accvgpr_write_b32 a182, v6\n"
        "v_accvgpr_write_b32 a183, v7\n"
        "v_accvgpr_write_b32 a184, v8\n"
        "v_accvgpr_write_b32 a185, v9\n"
        "v_accvgpr_write_b32 a186, v10\n"
        "v_accvgpr_write_b32 a187, v11\n"
        "v_accvgpr_write_b32 a188, v12\n"
        "v_accvgpr_write_b32 a189, v13\n"
        "v_accvgpr_write_b32 a190, v14\n"
        "v_accvgpr_write_b32 a191, v15\n"
        "s_mov_b32 s53, s45\n"
        "s_mov_b32 s96, 64\n"
        "s_mov_b32 s52, 2\n"
        "s_branch LREDO_%=\n"
        "LRET2_%=:\n"
        "v_accvgpr_write_b32 a176, v64\n"
        "v_accvgpr_write_b32 a177, v65\n"
        "v_accvgpr_write_b32 a178, v66\n"
        "v_accvgpr_write_b32 a179, v67\n"
        "v_accvgpr_write_b32 a180, v68\n"
        "v_accvgpr_write_b32 a181, v69\n"
        "v_accvgpr_write_b32 a182, v70\n"
        "v_accvgpr_write_b32 a183, v71\n"
        "v_accvgpr_write_b32 a184, v72\n"
        "v_accvgpr_write_b32 a185, v73\n"
        "v_accvgpr_write_b32 a186, v74\n"
        "v_accvgpr_write_b32 a187, v75\n"
        "v_accvgpr_write_b32 a188, v76\n"
        "v_accvgpr_write_b32 a189, v77\n"
        "v_accvgpr_write_b32 a190, v78\n"
        "v_accvgpr_write_b32 a191, v79\n"
        "s_mov_b32 s53, s45\n"
        "s_mov_b32 s96, 0\n"
        "s_mov_b32 s52, 3\n"
        "s_branch LREDO_%=\n"
        "LRET3_%=:\n"
        "s_branch LSH_%=\n"
        "LPCX_%=:\n"
        "s_mov_b32 s97, 0x200000\n"
        "LQCX_%=:\n"
        "global_load_dwordx2 v[236:237], v238, s[48:49] sc1\n"
        "s_waitcnt vmcnt(0)\n"
        "v_cmp_ne_u64 vcc, v[236:237], v[250:251]\n"
        "s_nop 1\n"
        "s_cbranch_vccz LBCX_%=\n"
        "s_sleep 2\n"
        "s_sub_u32 s97, s97, 1\n"
        "s_cmp_lg_u32 s97, 0\n"
        "s_cbranch_scc1 LQCX_%=\n"
        "s_branch LBCX_%=\n"
        "LPZZ_%=:\n"
        "s_mov_b32 s97, 0x200000\n"
        "LQZZ_%=:\n"
        "global_load_dwordx2 v[236:237], v234, s[48:49] sc1\n"
        "s_waitcnt vmcnt(0)\n"
        "v_cmp_ne_u64 vcc, v[236:237], v[250:251]\n"
        "s_nop 1\n"
        "s_cbranch_vccz LBZZ_%=\n"
        "s_sleep 2\n"
        "s_sub_u32 s97, s97, 1\n"
        "s_cmp_lg_u32 s97, 0\n"
        "s_cbranch_scc1 LQZZ_%=\n"
        "s_branch LBZZ_%=\n"
        "LEP_%=:\n"
        "v_lshlrev_b32 v88, 3, v254\n"
        "v_add_u32 v88, 0x8000, v88\n"
        "s_mov_b32 s96, 4\n"
        "s_mov_b32 s97, 0x200000\n"
        "LEQ_%=:\n"
        "global_load_dwordx2 v[90:91], v88, s[48:49] sc1\n"
        "s_waitcnt vmcnt(0)\n"
        "v_cmp_ne_u64 vcc, v[90:91], v[250:251]\n"
        "s_nop 1\n"
        "s_cbranch_vccz LER_%=\n"
        "s_sleep 2\n"
        "s_sub_u32 s97, s97, 1\n"
        "s_cmp_lg_u32 s97, 0\n"
        "s_cbranch_scc1 LEQ_%=\n"
        "LER_%=:\n"
        "v_add_u32 v88, 0x2000, v88\n"
        "s_sub_u32 s96, s96, 1\n"
        "s_cmp_lg_u32 s96, 0\n"
        "s_cbranch_scc1 LEQ_%=\n"
        "v_mov_b32 v88, 0\n"
        "s_mov_b64 exec, 1\n"
        "global_store_dwordx2 v88, v[250:251], s[48:49] sc1\n"
        "s_mov_b64 exec, -1\n"
        "s_branch LEB_%=\n"
        "LPPR_%=:\n"
        "s_mov_b32 s97, 0x200000\n"
        "LQPR_%=:\n"
        "global_load_dwordx2 v[236:237], v235, s[48:49] sc1\n"
        "s_waitcnt vmcnt(0)\n"
        "v_cmp_ne_u64 vcc, v[236:237], v[250:251]\n"
        "s_nop 1\n"
        "s_cbranch_vccz LBPR_%=\n"
        "s_sleep 2\n"
        "s_sub_u32 s97, s97, 1\n"
        "s_cmp_lg_u32 s97, 0\n"
        "s_cbranch_scc1 LQPR_%=\n"
        "s_branch LBPR_%=\n"
        "LREDO_%=:\n"
        "v_mov_b32 v112, 0\n"
        "v_mov_b32 v113, 0\n"
        "v_mov_b32 v114, 0\n"
        "v_mov_b32 v115, 0\n"
        "v_mov_b32 v116, 0\n"
        "v_mov_b32 v117, 0\n"
        "v_mov_b32 v118, 0\n"
        "v_mov_b32 v119, 0\n"
        "v_mov_b32 v120, 0\n"
        "v_mov_b32 v121, 0\n"
        "v_mov_b32 v122, 0\n"
        "v_mov_b32 v123, 0\n"
        "v_mov_b32 v124, 0\n"
        "v_mov_b32 v125, 0\n"
        "v_mov_b32 v126, 0\n"
        "v_mov_b32 v127, 0\n"
        "v_mov_b32 v144, 0\n"
        "v_mov_b32 v145, 0\n"
        "v_mov_b32 v146, 0\n"
        "v_mov_b32 v147, 0\n"
        "v_mov_b32 v148, 0\n"
        "v_mov_b32 v149, 0\n"
        "v_mov_b32 v150, 0\n"
        "v_mov_b32 v151, 0\n"
        "v_mov_b32 v152, 0\n"
        "v_mov_b32 v153, 0\n"
        "v_mov_b32 v154, 0\n"
        "v_mov_b32 v155, 0\n"
        "v_mov_b32 v156, 0\n"
        "v_mov_b32 v157, 0\n"
        "v_mov_b32 v158, 0\n"
        "v_mov_b32 v159, 0\n"
        "v_mov_b32 v60, 0\n"
        "v_mov_b32 v61, 0\n"
        "v_mov_b32 v62, 0\n"
        "v_mov_b32 v63, 0\n"
        "v_mov_b32 v24, 0xf149f2ca\n"
        "s_lshl_b32 s97, s53, 7\n"
        "s_add_u32 s97, s97, s96\n"
        "v_add_u32 v27, s97, v243\n"
        "s_lshl_b32 s97, s43, 19\n"
        "s_add_u32 s84, s38, s97\n"
        "s_addc_u32 s85, s39, 0\n"
        "s_add_u32 s84, s84, 0x1000\n"
        "s_addc_u32 s85, s85, 0\n"
        "s_add_u32 s86, s84, 0x1000000\n"
        "s_addc_u32 s87, s85, 0\n"
        "s_lshl_b32 s97, s53, 2\n"
        "s_add_u32 s97, s97, 4\n"
        "LRLOOP_%=:\n"
        "global_load_dwordx4 a[160:163], v254, s[84:85] offset:-4096 sc1\n"
        "global_load_dwordx4 a[164:167], v254, s[84:85] offset:-3072 sc1\n"
        "global_load_dwordx4 a[168:171], v254, s[84:85] offset:-2048 sc1\n"
        "global_load_dwordx4 a[172:175], v254, s[84:85] offset:-1024 sc1\n"
        "s_waitcnt vmcnt(0)\n"
        "v_mfma_f32_16x16x32_bf16 v[40:43], a[160:163], a[176:179], 0\n"
        "v_mfma_f32_16x16x32_bf16 v[40:43], a[164:167], a[180:183], v[40:43]\n"
        "v_mfma_f32_16x16x32_bf16 v[40:43], a[168:171], a[184:187], v[40:43]\n"
        "v_mfma_f32_16x16x32_bf16 v[40:43], a[172:175], a[188:191], v[40:43]\n"
        "s_nop 7\n"
        "global_load_dwordx4 a[160:163], v254, s[84:85] offset:0 sc1\n"
        "global_load_dwordx4 a[164:167], v254, s[84:85] offset:1024 sc1\n"
        "global_load_dwordx4 a[168:171], v254, s[84:85] offset:2048 sc1\n"
        "global_load_dwordx4 a[172:175], v254, s[84:85] offset:3072 sc1\n"
        "s_waitcnt vmcnt(0)\n"
        "v_mfma_f32_16x16x32_bf16 v[44:47], a[160:163], a[176:179], 0\n"
        "v_mfma_f32_16x16x32_bf16 v[44:47], a[164:167], a[180:183], v[44:47]\n"
        "v_mfma_f32_16x16x32_bf16 v[44:47], a[168:171], a[184:187], v[44:47]\n"
        "v_mfma_f32_16x16x32_bf16 v[44:47], a[172:175], a[188:191], v[44:47]\n"
        "s_nop 7\n"
        "v_cmp_gt_i32 vcc, 0, v27\n"
        "s_nop 1\n"
        "v_cndmask_b32 v40, v40, v242, vcc\n"
        "v_cmp_gt_i32 vcc, 1, v27\n"
        "s_nop 1\n"
        "v_cndmask_b32 v41, v41, v242, vcc\n"
        "v_cmp_gt_i32 vcc, 2, v27\n"
        "s_nop 1\n"
        "v_cndmask_b32 v42, v42, v242, vcc\n"
        "v_cmp_gt_i32 vcc, 3, v27\n"
        "s_nop 1\n"
        "v_cndmask_b32 v43, v43, v242, vcc\n"
        "v_cmp_gt_i32 vcc, 16, v27\n"
        "s_nop 1\n"
        "v_cndmask_b32 v44, v44, v242, vcc\n"
        "v_cmp_gt_i32 vcc, 17, v27\n"
        "s_nop 1\n"
        "v_cndmask_b32 v45, v45, v242, vcc\n"
        "v_cmp_gt_i32 vcc, 18, v27\n"
        "s_nop 1\n"
        "v_cndmask_b32 v46, v46, v242, vcc\n"
        "v_cmp_gt_i32 vcc, 19, v27\n"
        "s_nop 1\n"
        "v_cndmask_b32 v47, v47, v242, vcc\n"
        "v_max3_f32 v25, v40, v41, v24\n"
        "v_max3_f32 v25, v42, v43, v25\n"
        "v_max3_f32 v25, v44, v45, v25\n"
        "v_max3_f32 v25, v46, v47, v25\n"
        "v_mov_b32 v28, v25\n"
        "s_nop 1\n"
        "v_permlane16_swap_b32 v25, v28\n"
        "v_max_f32 v25, v25, v28\n"
        "v_mov_b32 v28, v25\n"
        "s_nop 1\n"
        "v_permlane32_swap_b32 v25, v28\n"
        "v_max_f32 v25, v25, v28\n"
        "v_sub_f32 v26, v24, v25\n"
        "v_exp_f32 v26, v26\n"
        "v_mov_b32 v24, v25\n"
        "v_sub_f32 v40, v40, v25\n"
        "v_exp_f32 v40, v40\n"
        "v_sub_f32 v41, v41, v25\n"
        "v_exp_f32 v41, v41\n"
        "v_sub_f32 v42, v42, v25\n"
        "v_exp_f32 v42, v42\n"
        "v_sub_f32 v43, v43, v25\n"
        "v_exp_f32 v43, v43\n"
        "v_sub_f32 v44, v44, v25\n"
        "v_exp_f32 v44, v44\n"
        "v_sub_f32 v45, v45, v25\n"
        "v_exp_f32 v45, v45\n"
        "v_sub_f32 v46, v46, v25\n"
        "v_exp_f32 v46, v46\n"
        "v_sub_f32 v47, v47, v25\n"
        "v_exp_f32 v47, v47\n"
        "v_cvt_pk_bf16_f32 v56, v40, v41\n"
        "v_cvt_pk_bf16_f32 v57, v42, v43\n"
        "v_cvt_pk_bf16_f32 v58, v44, v45\n"
        "v_cvt_pk_bf16_f32 v59, v46, v47\n"
        "v_mul_f32 v112, v112, v26\n"
        "v_mul_f32 v113, v113, v26\n"
        "v_mul_f32 v114, v114, v26\n"
        "v_mul_f32 v115, v115, v26\n"
        "v_mul_f32 v116, v116, v26\n"
        "v_mul_f32 v117, v117, v26\n"
        "v_mul_f32 v118, v118, v26\n"
        "v_mul_f32 v119, v119, v26\n"
        "v_mul_f32 v120, v120, v26\n"
        "v_mul_f32 v121, v121, v26\n"
        "v_mul_f32 v122, v122, v26\n"
        "v_mul_f32 v123, v123, v26\n"
        "v_mul_f32 v124, v124, v26\n"
        "v_mul_f32 v125, v125, v26\n"
        "v_mul_f32 v126, v126, v26\n"
        "v_mul_f32 v127, v127, v26\n"
        "v_mul_f32 v144, v144, v26\n"
        "v_mul_f32 v145, v145, v26\n"
        "v_mul_f32 v146, v146, v26\n"
        "v_mul_f32 v147, v147, v26\n"
        "v_mul_f32 v148, v148, v26\n"
        "v_mul_f32 v149, v149, v26\n"
        "v_mul_f32 v150, v150, v26\n"
        "v_mul_f32 v151, v151, v26\n"
        "v_mul_f32 v152, v152, v26\n"
        "v_mul_f32 v153, v153, v26\n"
        "v_mul_f32 v154, v154, v26\n"
        "v_mul_f32 v155, v155, v26\n"
        "v_mul_f32 v156, v156, v26\n"
        "v_mul_f32 v157, v157, v26\n"
        "v_mul_f32 v158, v158, v26\n"
        "v_mul_f32 v159, v159, v26\n"
        "v_mul_f32 v60, v60, v26\n"
        "v_mul_f32 v61, v61, v26\n"
        "v_mul_f32 v62, v62, v26\n"
        "v_mul_f32 v63, v63, v26\n"
        "s_nop 1\n"
        "v_mfma_f32_16x16x32_bf16 v[60:63], a[80:83], v[56:59], v[60:63]\n"
        "global_load_dwordx4 a[160:163], v254, s[86:87] offset:-4096 sc1\n"
        "global_load_dwordx4 a[164:167], v254, s[86:87] offset:-3072 sc1\n"
        "global_load_dwordx4 a[168:171], v254, s[86:87] offset:-2048 sc1\n"
        "global_load_dwordx4 a[172:175], v254, s[86:87] offset:-1024 sc1\n"
        "s_waitcnt vmcnt(0)\n"
        "v_mfma_f32_16x16x32_bf16 v[112:115], a[160:163], v[56:59], v[112:115]\n"
        "v_mfma_f32_16x16x32_bf16 v[116:119], a[164:167], v[56:59], v[116:119]\n"
        "v_mfma_f32_16x16x32_bf16 v[120:123], a[168:171], v[56:59], v[120:123]\n"
        "v_mfma_f32_16x16x32_bf16 v[124:127], a[172:175], v[56:59], v[124:127]\n"
        "s_nop 7\n"
        "global_load_dwordx4 a[160:163], v254, s[86:87] offset:0 sc1\n"
        "global_load_dwordx4 a[164:167], v254, s[86:87] offset:1024 sc1\n"
        "global_load_dwordx4 a[168:171], v254, s[86:87] offset:2048 sc1\n"
        "global_load_dwordx4 a[172:175], v254, s[86:87] offset:3072 sc1\n"
        "s_waitcnt vmcnt(0)\n"
        "v_mfma_f32_16x16x32_bf16 v[144:147], a[160:163], v[56:59], v[144:147]\n"
        "v_mfma_f32_16x16x32_bf16 v[148:151], a[164:167], v[56:59], v[148:151]\n"
        "v_mfma_f32_16x16x32_bf16 v[152:155], a[168:171], v[56:59], v[152:155]\n"
        "v_mfma_f32_16x16x32_bf16 v[156:159], a[172:175], v[56:59], v[156:159]\n"
        "s_nop 7\n"
        "s_add_u32 s84, s84, 0x2000\n"
        "s_addc_u32 s85, s85, 0\n"
        "s_add_u32 s86, s86, 0x2000\n"
        "s_addc_u32 s87, s87, 0\n"
        "v_subrev_u32 v27, 32, v27\n"
        "s_sub_u32 s97, s97, 1\n"
        "s_cmp_lg_u32 s97, 0\n"
        "s_cbranch_scc1 LRLOOP_%=\n"
        "s_lshl_b32 s97, s53, 20\n"
        "s_lshl_b32 s88, s43, 15\n"
        "s_add_u32 s97, s97, s88\n"
        "s_lshl_b32 s88, s40, 10\n"
        "s_add_u32 s97, s97, s88\n"
        "s_lshl_b32 s88, s96, 6\n"
        "s_add_u32 s97, s97, s88\n"
        "s_add_u32 s97, s97, 0x2000000\n"
        "s_add_u32 s84, s38, s97\n"
        "s_addc_u32 s85, s39, 0\n"
        "v_rcp_f32 v26, v60\n"
        "s_nop 0\n"
        "v_mul_f32 v112, v112, v26\n"
        "v_mul_f32 v113, v113, v26\n"
        "v_mul_f32 v114, v114, v26\n"
        "v_mul_f32 v115, v115, v26\n"
        "v_mul_f32 v116, v116, v26\n"
        "v_mul_f32 v117, v117, v26\n"
        "v_mul_f32 v118, v118, v26\n"
        "v_mul_f32 v119, v119, v26\n"
        "v_cvt_pk_bf16_f32 v112, v112, v113\n"
        "v_cvt_pk_bf16_f32 v113, v114, v115\n"
        "v_cvt_pk_bf16_f32 v114, v116, v117\n"
        "v_cvt_pk_bf16_f32 v115, v118, v119\n"
        "global_store_dwordx4 v254, v[112:115], s[84:85] sc1\n"
        "s_add_u32 s84, s84, 0x2000\n"
        "s_addc_u32 s85, s85, 0\n"
        "v_mul_f32 v120, v120, v26\n"
        "v_mul_f32 v121, v121, v26\n"
        "v_mul_f32 v122, v122, v26\n"
        "v_mul_f32 v123, v123, v26\n"
        "v_mul_f32 v124, v124, v26\n"
        "v_mul_f32 v125, v125, v26\n"
        "v_mul_f32 v126, v126, v26\n"
        "v_mul_f32 v127, v127, v26\n"
        "v_cvt_pk_bf16_f32 v120, v120, v121\n"
        "v_cvt_pk_bf16_f32 v121, v122, v123\n"
        "v_cvt_pk_bf16_f32 v122, v124, v125\n"
        "v_cvt_pk_bf16_f32 v123, v126, v127\n"
        "global_store_dwordx4 v254, v[120:123], s[84:85] sc1\n"
        "s_add_u32 s84, s84, 0x2000\n"
        "s_addc_u32 s85, s85, 0\n"
        "v_mul_f32 v144, v144, v26\n"
        "v_mul_f32 v145, v145, v26\n"
        "v_mul_f32 v146, v146, v26\n"
        "v_mul_f32 v147, v147, v26\n"
        "v_mul_f32 v148, v148, v26\n"
        "v_mul_f32 v149, v149, v26\n"
        "v_mul_f32 v150, v150, v26\n"
        "v_mul_f32 v151, v151, v26\n"
        "v_cvt_pk_bf16_f32 v144, v144, v145\n"
        "v_cvt_pk_bf16_f32 v145, v146, v147\n"
        "v_cvt_pk_bf16_f32 v146, v148, v149\n"
        "v_cvt_pk_bf16_f32 v147, v150, v151\n"
        "global_store_dwordx4 v254, v[144:147], s[84:85] sc1\n"
        "s_add_u32 s84, s84, 0x2000\n"
        "s_addc_u32 s85, s85, 0\n"
        "v_mul_f32 v152, v152, v26\n"
        "v_mul_f32 v153, v153, v26\n"
        "v_mul_f32 v154, v154, v26\n"
        "v_mul_f32 v155, v155, v26\n"
        "v_mul_f32 v156, v156, v26\n"
        "v_mul_f32 v157, v157, v26\n"
        "v_mul_f32 v158, v158, v26\n"
        "v_mul_f32 v159, v159, v26\n"
        "v_cvt_pk_bf16_f32 v152, v152, v153\n"
        "v_cvt_pk_bf16_f32 v153, v154, v155\n"
        "v_cvt_pk_bf16_f32 v154, v156, v157\n"
        "v_cvt_pk_bf16_f32 v155, v158, v159\n"
        "global_store_dwordx4 v254, v[152:155], s[84:85] sc1\n"
        "s_add_u32 s84, s84, 0x2000\n"
        "s_addc_u32 s85, s85, 0\n"
        "s_waitcnt vmcnt(0)\n"
        "s_cmp_eq_u32 s52, 0\n"
        "s_cbranch_scc1 LRET0_%=\n"
        "s_cmp_eq_u32 s52, 1\n"
        "s_cbranch_scc1 LRET1_%=\n"
        "s_cmp_eq_u32 s52, 2\n"
        "s_cbranch_scc1 LRET2_%=\n"
        "s_branch LRET3_%=\n"
        "LEND_%=:\n"
        :
        : "s"(__builtin_amdgcn_kernarg_segment_ptr()), "s"(blockIdx.x), "s"(__builtin_amdgcn_readfirstlane(threadIdx.x >> 6))
        : "v0", "v1", "v2", "v3", "v4", "v5", "v6", "v7", "v8", "v9", "v10", "v11", "v12", "v13", "v14", "v15", "v16", "v17", "v18", "v19", "v20", "v21", "v22", "v23", "v24", "v25", "v26", "v27", "v28", "v29", "v30", "v31",
        "v32", "v33", "v34", "v35", "v36", "v37", "v38", "v39", "v40", "v41", "v42", "v43", "v44", "v45", "v46", "v47", "v48", "v49", "v50", "v51", "v52", "v53", "v54", "v55", "v56", "v57", "v58", "v59", "v60", "v61", "v62", "v63",
        "v64", "v65", "v66", "v67", "v68", "v69", "v70", "v71", "v72", "v73", "v74", "v75", "v76", "v77", "v78", "v79", "v80", "v81", "v82", "v83", "v84", "v85", "v86", "v87", "v88", "v89", "v90", "v91", "v92", "v93", "v94", "v95",
        "v96", "v97", "v98", "v99", "v100", "v101", "v102", "v103", "v104", "v105", "v106", "v107", "v108", "v109", "v110", "v111", "v112", "v113", "v114", "v115", "v116", "v117", "v118", "v119", "v120", "v121", "v122", "v123", "v124", "v125", "v126", "v127",
        "v128", "v129", "v130", "v131", "v132", "v133", "v134", "v135", "v136", "v137", "v138", "v139", "v140", "v141", "v142", "v143", "v144", "v145", "v146", "v147", "v148", "v149", "v150", "v151", "v152", "v153", "v154", "v155", "v156", "v157", "v158", "v159",
        "v160", "v161", "v162", "v163", "v164", "v165", "v166", "v167", "v168", "v169", "v170", "v171", "v172", "v173", "v174", "v175", "v176", "v177", "v178", "v179", "v180", "v181", "v182", "v183", "v184", "v185", "v186", "v187", "v188", "v189", "v190", "v191",
        "v192", "v193", "v194", "v195", "v196", "v197", "v198", "v199", "v200", "v201", "v202", "v203", "v204", "v205", "v206", "v207", "v208", "v209", "v210", "v211", "v212", "v213", "v214", "v215", "v216", "v217", "v218", "v219", "v220", "v221", "v222", "v223",
        "v224", "v225", "v226", "v227", "v228", "v229", "v230", "v231", "v232", "v233", "v234", "v235", "v236", "v237", "v238", "v239", "v240", "v241", "v242", "v243", "v244", "v245", "v246", "v247", "v248", "v249", "v250", "v251", "v252", "v253", "v254", "v255",
        "a0", "a1", "a2", "a3", "a4", "a5", "a6", "a7", "a8", "a9", "a10", "a11", "a12", "a13", "a14", "a15", "a16", "a17", "a18", "a19", "a20", "a21", "a22", "a23", "a24", "a25", "a26", "a27", "a28", "a29", "a30", "a31",
        "a32", "a33", "a34", "a35", "a36", "a37", "a38", "a39", "a40", "a41", "a42", "a43", "a44", "a45", "a46", "a47", "a48", "a49", "a50", "a51", "a52", "a53", "a54", "a55", "a56", "a57", "a58", "a59", "a60", "a61", "a62", "a63",
        "a64", "a65", "a66", "a67", "a68", "a69", "a70", "a71", "a72", "a73", "a74", "a75", "a76", "a77", "a78", "a79", "a80", "a81", "a82", "a83", "a84", "a85", "a86", "a87", "a88", "a89", "a90", "a91", "a92", "a93", "a94", "a95",
        "a96", "a97", "a98", "a99", "a100", "a101", "a102", "a103", "a104", "a105", "a106", "a107", "a108", "a109", "a110", "a111", "a112", "a113", "a114", "a115", "a116", "a117", "a118", "a119", "a120", "a121", "a122", "a123", "a124", "a125", "a126", "a127",
        "a128", "a129", "a130", "a131", "a132", "a133", "a134", "a135", "a136", "a137", "a138", "a139", "a140", "a141", "a142", "a143", "a144", "a145", "a146", "a147", "a148", "a149", "a150", "a151", "a152", "a153", "a154", "a155", "a156", "a157", "a158", "a159",
        "a160", "a161", "a162", "a163", "a164", "a165", "a166", "a167", "a168", "a169", "a170", "a171", "a172", "a173", "a174", "a175", "a176", "a177", "a178", "a179", "a180", "a181", "a182", "a183", "a184", "a185", "a186", "a187", "a188", "a189", "a190", "a191",
        "a192", "a193", "a194", "a195", "a196", "a197", "a198", "a199", "a200", "a201", "a202", "a203", "a204", "a205", "a206", "a207", "a208", "a209", "a210", "a211", "a212", "a213", "a214", "a215", "a216", "a217", "a218", "a219", "a220", "a221", "a222", "a223",
        "a224", "a225", "a226", "a227", "a228", "a229", "a230", "a231", "a232", "a233", "a234", "a235", "a236", "a237", "a238", "a239", "a240", "a241", "a242", "a243", "a244", "a245", "a246", "a247", "a248", "a249", "a250", "a251", "a252", "a253", "a254", "a255",
        "s8", "s9", "s10", "s11", "s12", "s13", "s14", "s15", "s16", "s17", "s18", "s19", "s20", "s21", "s22", "s23", "s24", "s25", "s26", "s27", "s28", "s29", "s30", "s31", "s36", "s37", "s38", "s39", "s40", "s41", "s42", "s43",
        "s44", "s45", "s46", "s47", "s48", "s49", "s50", "s51", "s52", "s53", "s54", "s55", "s56", "s57", "s58", "s59", "s60", "s61", "s62", "s63", "s64", "s65", "s66", "s67", "s68", "s69", "s70", "s71", "s72", "s73", "s74", "s75",
        "s76", "s77", "s78", "s79", "s80", "s81", "s82", "s83", "s84", "s85", "s86", "s87", "s88", "s89", "s90", "s91", "s92", "s93", "s94", "s95", "s96", "s97", "s98", "s99", "vcc", "scc", "memory");
}

extern "C" void kernel_launch(void* const* d_in, const int* in_sizes, int n_in, void* d_out, int out_size, void* d_ws, size_t ws_size, hipStream_t stream) {
    (void)in_sizes; (void)n_in; (void)out_size;
    constexpr size_t WS_USED = (size_t)72 << 20;
    Params P;
    P.x = d_in[0]; P.ckv = d_in[1]; P.wq = d_in[2]; P.wuk = d_in[3]; P.wuv = d_in[4]; P.wo = d_in[5];
    P.bk = d_in[7]; P.bv = d_in[8]; P.shk = d_in[9]; P.ssk = d_in[10]; P.ssd = d_in[11]; P.gw = d_in[12];
    P.out = d_out; P.ws = (char*)d_ws;
    if (ws_size > WS_USED) P.ws += (ws_size - WS_USED) & ~(size_t)0xfffff;
    constexpr int LDS_MAX = 163840;
    (void)hipFuncSetAttribute((const void*)k_main, hipFuncAttributeMaxDynamicSharedMemorySize, LDS_MAX);
    k_main<<<256, 256, LDS_MAX, stream>>>(P);
}
```

```cpp
#include <hip/hip_runtime.h>
#include <stdint.h>

struct Params {
    const void *x, *ckv, *wq, *wuk, *wuv, *wo, *bk, *bv, *shk, *ssk, *ssd, *gw;
    void* out; char* ws;
};

__global__ __launch_bounds__(256, 1) void k_main(Params P) {
    asm volatile(
        "; ---- PRO\n"
        "s_load_dwordx16 s[8:23], %0, 0\n"
        "s_load_dwordx8 s[24:31], %0, 64\n"
        "s_load_dwordx4 s[36:39], %0, 96\n"
        "s_mov_b32 s55, %1\n"
        "s_mov_b32 s40, %2\n"
        "v_mbcnt_lo_u32_b32 v0, -1, 0\n"
        "v_mbcnt_hi_u32_b32 v0, -1, v0\n"
        "s_and_b32 s41, s55, 7\n"
        "s_lshr_b32 s42, s55, 3\n"
        "s_lshr_b32 s50, s42, 3\n"
        "s_lshl2_add_u32 s43, s41, s50\n"
        "s_and_b32 s44, s42, 7\n"
        "s_sub_u32 s45, 15, s44\n"
        "s_lshl_b32 s72, s40, 13\n"
        "s_lshl_b32 s73, s40, 12\n"
        "v_lshlrev_b32 v254, 4, v0\n"
        "v_lshrrev_b32 v2, 4, v0\n"
        "v_and_b32 v1, 15, v0\n"
        "s_getpc_b64 s[46:47]\n"
        "LPC_%=:\n"
        "s_lshl2_add_u32 s50, s42, s40\n"
        "s_lshl_b32 s50, s50, 10\n"
        "v_lshlrev_b32 v12, 6, v1\n"
        "v_add_u32 v12, s50, v12\n"
        "v_min_u32 v252, LEND_%=-LPC_%=-4, v12\n"
        "global_load_dword v253, v252, s[46:47] offset:0\n"
        "v_lshrrev_b32 v4, 3, v0\n"
        "v_and_b32 v5, 7, v0\n"
        "v_xor_b32 v6, v5, v2\n"
        "v_lshlrev_b32 v6, 4, v6\n"
        "v_xor_b32 v7, 64, v6\n"
        "v_lshl_add_u32 v234, v4, 10, v6\n"
        "v_lshl_add_u32 v8, v4, 10, v7\n"
        "v_add_u32 v235, 7168, v8\n"
        "v_add_u32 v236, 14336, v234\n"
        "v_add_u32 v237, 21504, v8\n"
        "v_and_b32 v8, 6, v4\n"
        "v_xor_b32 v8, v8, v5\n"
        "v_lshlrev_b32 v8, 4, v8\n"
        "v_lshl_add_u32 v9, v4, 13, v8\n"
        "v_xor_b32 v10, 4, v4\n"
        "v_lshl_add_u32 v10, v10, 13, v8\n"
        "v_add_u32 v238, 4096, v9\n"
        "v_add_u32 v239, 3200, v9\n"
        "v_add_u32 v240, 67584, v10\n"
        "v_add_u32 v241, 66688, v10\n"
        "s_cmp_ge_u32 s40, 2\n"
        "s_cselect_b32 s50, s45, s44\n"
        "s_and_b32 s51, s40, 1\n"
        "s_lshl1_add_u32 s50, s50, s51\n"
        "s_lshl_b32 s50, s50, 16\n"
        "s_lshl_b32 s51, s40, 17\n"
        "s_lshl_b32 s52, s43, 8\n"
        "s_add_u32 s51, s51, s52\n"
        "s_waitcnt lgkmcnt(0)\n"
        "s_add_u32 s56, s10, s50\n"
        "s_addc_u32 s57, s11, 0\n"
        "s_add_u32 s58, s56, 0x8000\n"
        "s_addc_u32 s59, s57, 0\n"
        "s_add_u32 s60, s14, s51\n"
        "s_addc_u32 s61, s15, 0\n"
        "s_sub_u32 s60, s60, 4096\n"
        "s_subb_u32 s61, s61, 0\n"
        "s_add_u32 s62, s16, s51\n"
        "s_addc_u32 s63, s17, 0\n"
        "s_sub_u32 s62, s62, 4096\n"
        "s_subb_u32 s63, s63, 0\n"
        "s_add_u32 m0, s73, 0x10000\n"
        "s_nop 0\n"
        "global_load_lds_dwordx4 v238, s[60:61] offset:0\n"
        "global_load_lds_dwordx4 v239, s[60:61] offset:1024\n"
        "global_load_lds_dwordx4 v240, s[60:61] offset:2048\n"
        "global_load_lds_dwordx4 v241, s[60:61] offset:3072\n"
        "s_add_u32 m0, s72, 0x0\n"
        "s_nop 0\n"
        "global_load_lds_dwordx4 v234, s[56:57] offset:0\n"
        "global_load_lds_dwordx4 v235, s[56:57] offset:1024\n"
        "global_load_lds_dwordx4 v236, s[56:57] offset:2048\n"
        "global_load_lds_dwordx4 v237, s[56:57] offset:3072\n"
        "s_add_u32 m0, s72, 0x1000\n"
        "s_nop 0\n"
        "global_load_lds_dwordx4 v234, s[58:59] offset:0\n"
        "global_load_lds_dwordx4 v235, s[58:59] offset:1024\n"
        "global_load_lds_dwordx4 v236, s[58:59] offset:2048\n"
        "global_load_lds_dwordx4 v237, s[58:59] offset:3072\n"
        "s_add_u32 s56, s56, 0x80\n"
        "s_addc_u32 s57, s57, 0\n"
        "s_add_u32 s58, s58, 0x80\n"
        "s_addc_u32 s59, s59, 0\n"
        "s_add_u32 m0, s73, 0x14000\n"
        "s_nop 0\n"
        "global_load_lds_dwordx4 v238, s[62:63] offset:0\n"
        "global_load_lds_dwordx4 v239, s[62:63] offset:1024\n"
        "global_load_lds_dwordx4 v240, s[62:63] offset:2048\n"
        "global_load_lds_dwordx4 v241, s[62:63] offset:3072\n"
        "s_add_u32 s60, s60, 0x80000\n"
        "s_addc_u32 s61, s61, 0\n"
        "s_add_u32 s62, s62, 0x80000\n"
        "s_addc_u32 s63, s63, 0\n"
        "s_add_u32 s68, s12, s51\n"
        "s_addc_u32 s69, s13, 0\n"
        "s_sub_u32 s68, s68, 4096\n"
        "s_subb_u32 s69, s69, 0\n"
        "s_add_u32 s48, s38, 0x3900000\n"
        "s_addc_u32 s49, s39, 0\n"
        "v_mov_b32 v11, 0\n"
        "global_load_dwordx2 v[250:251], v11, s[48:49] sc1\n"
        "s_add_u32 m0, s73, 0x18000\n"
        "s_nop 0\n"
        "global_load_lds_dwordx4 v238, s[60:61] offset:0\n"
        "global_load_lds_dwordx4 v239, s[60:61] offset:1024\n"
        "global_load_lds_dwordx4 v240, s[60:61] offset:2048\n"
        "global_load_lds_dwordx4 v241, s[60:61] offset:3072\n"
        "s_add_u32 m0, s73, 0x1c000\n"
        "s_nop 0\n"
        "global_load_lds_dwordx4 v238, s[62:63] offset:0\n"
        "global_load_lds_dwordx4 v239, s[62:63] offset:1024\n"
        "global_load_lds_dwordx4 v240, s[62:63] offset:2048\n"
        "global_load_lds_dwordx4 v241, s[62:63] offset:3072\n"
        "s_add_u32 s60, s60, 0x80000\n"
        "s_addc_u32 s61, s61, 0\n"
        "s_add_u32 s62, s62, 0x80000\n"
        "s_addc_u32 s63, s63, 0\n"
        "v_lshrrev_b32 v8, 1, v1\n"
        "v_xor_b32 v8, v8, v2\n"
        "v_lshlrev_b32 v8, 4, v8\n"
        "v_lshl_add_u32 v8, v1, 7, v8\n"
        "v_xor_b32 v9, 64, v8\n"
        "s_lshr_b32 s50, s40, 1\n"
        "s_lshl_b32 s50, s50, 14\n"
        "v_add_u32 v224, s50, v8\n"
        "v_add_u32 v246, s72, v8\n"
        "v_add_u32 v248, 0x10000, v246\n"
        "v_add_u32 v225, s50, v9\n"
        "v_add_u32 v247, s72, v9\n"
        "v_add_u32 v249, 0x10000, v247\n"
        "s_and_b32 s50, s40, 1\n"
        "s_lshl_b32 s50, s50, 10\n"
        "s_add_u32 s50, s50, 0x10000\n"
        "v_lshlrev_b32 v8, 11, v2\n"
        "v_add_u32 v8, s50, v8\n"
        "v_lshrrev_b32 v9, 2, v1\n"
        "v_or_b32 v10, 4, v9\n"
        "v_and_b32 v11, 1, v2\n"
        "v_and_b32 v13, 3, v0\n"
        "v_lshlrev_b32 v14, 2, v11\n"
        "v_xor_b32 v14, v14, v9\n"
        "v_bfe_u32 v15, v14, 1, 2\n"
        "v_xor_b32 v15, 0, v15\n"
        "v_lshlrev_b32 v15, 5, v15\n"
        "v_lshl_add_u32 v14, v14, 7, v15\n"
        "v_lshl_add_u32 v14, v13, 3, v14\n"
        "v_add_u32 v226, v14, v8\n"
        "v_xor_b32 v228, 32, v226\n"
        "v_xor_b32 v230, 64, v226\n"
        "v_xor_b32 v232, 96, v226\n"
        "v_lshlrev_b32 v14, 2, v11\n"
        "v_xor_b32 v14, v14, v10\n"
        "v_bfe_u32 v15, v14, 1, 2\n"
        "v_xor_b32 v15, 0, v15\n"
        "v_lshlrev_b32 v15, 5, v15\n"
        "v_lshl_add_u32 v14, v14, 7, v15\n"
        "v_lshl_add_u32 v14, v13, 3, v14\n"
        "v_add_u32 v227, v14, v8\n"
        "v_xor_b32 v229, 32, v227\n"
        "v_xor_b32 v231, 64, v227\n"
        "v_xor_b32 v233, 96, v227\n"
        "v_add_u32 v216, 0x10000, v226\n"
        "v_add_u32 v217, 0x10000, v227\n"
        "v_add_u32 v218, 0x10000, v228\n"
        "v_add_u32 v219, 0x10000, v229\n"
        "v_add_u32 v220, 0x10000, v230\n"
        "v_add_u32 v221, 0x10000, v231\n"
        "v_add_u32 v222, 0x10000, v232\n"
        "v_add_u32 v223, 0x10000, v233\n"
        "v_lshl_add_u32 v242, v4, 11, v6\n"
        "v_lshl_add_u32 v8, v4, 11, v7\n"
        "v_add_u32 v243, 15360, v8\n"
        "v_add_u32 v244, 129024, v242\n"
        "v_add_u32 v245, 144384, v8\n"
        "s_lshl_b32 s50, s45, 18\n"
        "s_lshl_b32 s51, s40, 15\n"
        "s_add_u32 s50, s50, s51\n"
        "s_add_u32 s64, s8, s50\n"
        "s_addc_u32 s65, s9, 0\n"
        "s_lshl_b32 s50, s44, 18\n"
        "s_add_u32 s50, s50, s51\n"
        "s_add_u32 s66, s8, s50\n"
        "s_addc_u32 s67, s9, 0\n"
        "s_lshl2_add_u32 s82, s55, s40\n"
        "s_lshl_b32 s82, s82, 11\n"
        "v_and_b32 v20, 0xf0, v254\n"
        "v_lshl_add_u32 v20, v20, 3, s82\n"
        "global_load_dword v253, v20, s[10:11] offset:0\n"
        "s_lshl2_add_u32 s82, s42, s40\n"
        "s_lshl3_add_u32 s82, s82, s41\n"
        "s_lshl_b32 s82, s82, 10\n"
        "v_and_b32 v21, 0x70, v254\n"
        "v_lshl_add_u32 v173, v21, 3, s82\n"
        "s_lshl2_add_u32 s82, s55, s40\n"
        "s_lshl_b32 s82, s82, 12\n"
        "v_bfe_u32 v20, v254, 4, 1\n"
        "v_bfe_u32 v21, v254, 5, 1\n"
        "v_lshlrev_b32 v20, 7, v20\n"
        "v_lshl_add_u32 v20, v21, 11, v20\n"
        "v_add_u32 v199, s82, v20\n"
        "s_mov_b64 exec, 0\n"
        "s_cmp_eq_u32 s40, 0\n"
        "s_cbranch_scc1 LHA0_%=\n"
        "s_cmp_eq_u32 s40, 1\n"
        "s_cbranch_scc1 LHA1_%=\n"
        "s_cmp_eq_u32 s40, 2\n"
        "s_cbranch_scc1 LHA2_%=\n"
        "s_branch LHA3_%=\n"
        "LHRA_%=:\n"
        "s_mov_b64 exec, -1\n"
        "s_branch LFRONT_%=\n"
        "LREDO_%=:\n"
        "v_mov_b32 v112, 0\n"
        "v_mov_b32 v113, 0\n"
        "v_mov_b32 v114, 0\n"
        "v_mov_b32 v115, 0\n"
        "v_mov_b32 v116, 0\n"
        "v_mov_b32 v117, 0\n"
        "v_mov_b32 v118, 0\n"
        "v_mov_b32 v119, 0\n"
        "v_mov_b32 v120, 0\n"
        "v_mov_b32 v121, 0\n"
        "v_mov_b32 v122, 0\n"
        "v_mov_b32 v123, 0\n"
        "v_mov_b32 v124, 0\n"
        "v_mov_b32 v125, 0\n"
        "v_mov_b32 v126, 0\n"
        "v_mov_b32 v127, 0\n"
        "v_mov_b32 v144, 0\n"
        "v_mov_b32 v145, 0\n"
        "v_mov_b32 v146, 0\n"
        "v_mov_b32 v147, 0\n"
        "v_mov_b32 v148, 0\n"
        "v_mov_b32 v149, 0\n"
        "v_mov_b32 v150, 0\n"
        "v_mov_b32 v151, 0\n"
        "v_mov_b32 v152, 0\n"
        "v_mov_b32 v153, 0\n"
        "v_mov_b32 v154, 0\n"
        "v_mov_b32 v155, 0\n"
        "v_mov_b32 v156, 0\n"
        "v_mov_b32 v157, 0\n"
        "v_mov_b32 v158, 0\n"
        "v_mov_b32 v159, 0\n"
        "v_mov_b32 v60, 0\n"
        "v_mov_b32 v61, 0\n"
        "v_mov_b32 v62, 0\n"
        "v_mov_b32 v63, 0\n"
        "v_mov_b32 v24, 0xf149f2ca\n"
        "s_lshl_b32 s97, s53, 7\n"
        "s_add_u32 s97, s97, s96\n"
        "v_add_u32 v27, s97, v243\n"
        "s_lshl_b32 s97, s43, 19\n"
        "s_add_u32 s84, s38, s97\n"
        "s_addc_u32 s85, s39, 0\n"
        "s_add_u32 s84, s84, 0x1000\n"
        "s_addc_u32 s85, s85, 0\n"
        "s_add_u32 s86, s84, 0x1000000\n"
        "s_addc_u32 s87, s85, 0\n"
        "s_lshl_b32 s97, s53, 2\n"
        "s_add_u32 s97, s97, 4\n"
        "LRLOOP_%=:\n"
        "global_load_dwordx4 a[160:163], v254, s[84:85] offset:-4096 sc1\n"
        "global_load_dwordx4 a[164:167], v254, s[84:85] offset:-3072 sc1\n"
        "global_load_dwordx4 a[168:171], v254, s[84:85] offset:-2048 sc1\n"
        "global_load_dwordx4 a[172:175], v254, s[84:85] offset:-1024 sc1\n"
        "s_waitcnt vmcnt(0)\n"
        "v_mfma_f32_16x16x32_bf16 v[40:43], a[160:163], a[176:179], 0\n"
        "v_mfma_f32_16x16x32_bf16 v[40:43], a[164:167], a[180:183], v[40:43]\n"
        "v_mfma_f32_16x16x32_bf16 v[40:43], a[168:171], a[184:187], v[40:43]\n"
        "v_mfma_f32_16x16x32_bf16 v[40:43], a[172:175], a[188:191], v[40:43]\n"
        "s_nop 7\n"
        "global_load_dwordx4 a[160:163], v254, s[84:85] offset:0 sc1\n"
        "global_load_dwordx4 a[164:167], v254, s[84:85] offset:1024 sc1\n"
        "global_load_dwordx4 a[168:171], v254, s[84:85] offset:2048 sc1\n"
        "global_load_dwordx4 a[172:175], v254, s[84:85] offset:3072 sc1\n"
        "s_waitcnt vmcnt(0)\n"
        "v_mfma_f32_16x16x32_bf16 v[44:47], a[160:163], a[176:179], 0\n"
        "v_mfma_f32_16x16x32_bf16 v[44:47], a[164:167], a[180:183], v[44:47]\n"
        "v_mfma_f32_16x16x32_bf16 v[44:47], a[168:171], a[184:187], v[44:47]\n"
        "v_mfma_f32_16x16x32_bf16 v[44:47], a[172:175], a[188:191], v[44:47]\n"
        "s_nop 7\n"
        "v_cmp_gt_i32 vcc, 0, v27\n"
        "s_nop 1\n"
        "v_cndmask_b32 v40, v40, v242, vcc\n"
        "v_cmp_gt_i32 vcc, 1, v27\n"
        "s_nop 1\n"
        "v_cndmask_b32 v41, v41, v242, vcc\n"
        "v_cmp_gt_i32 vcc, 2, v27\n"
        "s_nop 1\n"
        "v_cndmask_b32 v42, v42, v242, vcc\n"
        "v_cmp_gt_i32 vcc, 3, v27\n"
        "s_nop 1\n"
        "v_cndmask_b32 v43, v43, v242, vcc\n"
        "v_cmp_gt_i32 vcc, 16, v27\n"
        "s_nop 1\n"
        "v_cndmask_b32 v44, v44, v242, vcc\n"
        "v_cmp_gt_i32 vcc, 17, v27\n"
        "s_nop 1\n"
        "v_cndmask_b32 v45, v45, v242, vcc\n"
        "v_cmp_gt_i32 vcc, 18, v27\n"
        "s_nop 1\n"
        "v_cndmask_b32 v46, v46, v242, vcc\n"
        "v_cmp_gt_i32 vcc, 19, v27\n"
        "s_nop 1\n"
        "v_cndmask_b32 v47, v47, v242, vcc\n"
        "v_max3_f32 v25, v40, v41, v24\n"
        "v_max3_f32 v25, v42, v43, v25\n"
        "v_max3_f32 v25, v44, v45, v25\n"
        "v_max3_f32 v25, v46, v47, v25\n"
        "v_mov_b32 v28, v25\n"
        "s_nop 1\n"
        "v_permlane16_swap_b32 v25, v28\n"
        "v_max_f32 v25, v25, v28\n"
        "v_mov_b32 v28, v25\n"
        "s_nop 1\n"
        "v_permlane32_swap_b32 v25, v28\n"
        "v_max_f32 v25, v25, v28\n"
        "v_sub_f32 v26, v24, v25\n"
        "v_exp_f32 v26, v26\n"
        "v_mov_b32 v24, v25\n"
        "v_sub_f32 v40, v40, v25\n"
        "v_exp_f32 v40, v40\n"
        "v_sub_f32 v41, v41, v25\n"
        "v_exp_f32 v41, v41\n"
        "v_sub_f32 v42, v42, v25\n"
        "v_exp_f32 v42, v42\n"
        "v_sub_f32 v43, v43, v25\n"
        "v_exp_f32 v43, v43\n"
        "v_sub_f32 v44, v44, v25\n"
        "v_exp_f32 v44, v44\n"
        "v_sub_f32 v45, v45, v25\n"
        "v_exp_f32 v45, v45\n"
        "v_sub_f32 v46, v46, v25\n"
        "v_exp_f32 v46, v46\n"
        "v_sub_f32 v47, v47, v25\n"
        "v_exp_f32 v47, v47\n"
        "v_cvt_pk_bf16_f32 v56, v40, v41\n"
        "v_cvt_pk_bf16_f32 v57, v42, v43\n"
        "v_cvt_pk_bf16_f32 v58, v44, v45\n"
        "v_cvt_pk_bf16_f32 v59, v46, v47\n"
        "v_mul_f32 v112, v112, v26\n"
        "v_mul_f32 v113, v113, v26\n"
        "v_mul_f32 v114, v114, v26\n"
        "v_mul_f32 v115, v115, v26\n"
        "v_mul_f32 v116, v116, v26\n"
        "v_mul_f32 v117, v117, v26\n"
        "v_mul_f32 v118, v118, v26\n"
        "v_mul_f32 v119, v119, v26\n"
        "v_mul_f32 v120, v120, v26\n"
        "v_mul_f32 v121, v121, v26\n"
        "v_mul_f32 v122, v122, v26\n"
        "v_mul_f32 v123, v123, v26\n"
        "v_mul_f32 v124, v124, v26\n"
        "v_mul_f32 v125, v125, v26\n"
        "v_mul_f32 v126, v126, v26\n"
        "v_mul_f32 v127, v127, v26\n"
        "v_mul_f32 v144, v144, v26\n"
        "v_mul_f32 v145, v145, v26\n"
        "v_mul_f32 v146, v146, v26\n"
        "v_mul_f32 v147, v147, v26\n"
        "v_mul_f32 v148, v148, v26\n"
        "v_mul_f32 v149, v149, v26\n"
        "v_mul_f32 v150, v150, v26\n"
        "v_mul_f32 v151, v151, v26\n"
        "v_mul_f32 v152, v152, v26\n"
        "v_mul_f32 v153, v153, v26\n"
        "v_mul_f32 v154, v154, v26\n"
        "v_mul_f32 v155, v155, v26\n"
        "v_mul_f32 v156, v156, v26\n"
        "v_mul_f32 v157, v157, v26\n"
        "v_mul_f32 v158, v158, v26\n"
        "v_mul_f32 v159, v159, v26\n"
        "v_mul_f32 v60, v60, v26\n"
        "v_mul_f32 v61, v61, v26\n"
        "v_mul_f32 v62, v62, v26\n"
        "v_mul_f32 v63, v63, v26\n"
        "s_nop 1\n"
        "v_mfma_f32_16x16x32_bf16 v[60:63], a[80:83], v[56:59], v[60:63]\n"
        "global_load_dwordx4 a[160:163], v254, s[86:87] offset:-4096 sc1\n"
        "global_load_dwordx4 a[164:167], v254, s[86:87] offset:-3072 sc1\n"
        "global_load_dwordx4 a[168:171], v254, s[86:87] offset:-2048 sc1\n"
        "global_load_dwordx4 a[172:175], v254, s[86:87] offset:-1024 sc1\n"
        "s_waitcnt vmcnt(0)\n"
        "v_mfma_f32_16x16x32_bf16 v[112:115], a[160:163], v[56:59], v[112:115]\n"
        "v_mfma_f32_16x16x32_bf16 v[116:119], a[164:167], v[56:59], v[116:119]\n"
        "v_mfma_f32_16x16x32_bf16 v[120:123], a[168:171], v[56:59], v[120:123]\n"
        "v_mfma_f32_16x16x32_bf16 v[124:127], a[172:175], v[56:59], v[124:127]\n"
        "s_nop 7\n"
        "global_load_dwordx4 a[160:163], v254, s[86:87] offset:0 sc1\n"
        "global_load_dwordx4 a[164:167], v254, s[86:87] offset:1024 sc1\n"
        "global_load_dwordx4 a[168:171], v254, s[86:87] offset:2048 sc1\n"
        "global_load_dwordx4 a[172:175], v254, s[86:87] offset:3072 sc1\n"
        "s_waitcnt vmcnt(0)\n"
        "v_mfma_f32_16x16x32_bf16 v[144:147], a[160:163], v[56:59], v[144:147]\n"
        "v_mfma_f32_16x16x32_bf16 v[148:151], a[164:167], v[56:59], v[148:151]\n"
        "v_mfma_f32_16x16x32_bf16 v[152:155], a[168:171], v[56:59], v[152:155]\n"
        "v_mfma_f32_16x16x32_bf16 v[156:159], a[172:175], v[56:59], v[156:159]\n"
        "s_nop 7\n"
        "s_add_u32 s84, s84, 0x2000\n"
        "s_addc_u32 s85, s85, 0\n"
        "s_add_u32 s86, s86, 0x2000\n"
        "s_addc_u32 s87, s87, 0\n"
        "v_subrev_u32 v27, 32, v27\n"
        "s_sub_u32 s97, s97, 1\n"
        "s_cmp_lg_u32 s97, 0\n"
        "s_cbranch_scc1 LRLOOP_%=\n"
        "s_lshl_b32 s97, s53, 20\n"
        "s_lshl_b32 s88, s43, 15\n"
        "s_add_u32 s97, s97, s88\n"
        "s_lshl_b32 s88, s40, 10\n"
        "s_add_u32 s97, s97, s88\n"
        "s_lshl_b32 s88, s96, 6\n"
        "s_add_u32 s97, s97, s88\n"
        "s_add_u32 s97, s97, 0x2000000\n"
        "s_add_u32 s84, s38, s97\n"
        "s_addc_u32 s85, s39, 0\n"
        "v_rcp_f32 v26, v60\n"
        "s_nop 0\n"
        "v_mul_f32 v112, v112, v26\n"
        "v_mul_f32 v113, v113, v26\n"
        "v_mul_f32 v114, v114, v26\n"
        "v_mul_f32 v115, v115, v26\n"
        "v_mul_f32 v116, v116, v26\n"
        "v_mul_f32 v117, v117, v26\n"
        "v_mul_f32 v118, v118, v26\n"
        "v_mul_f32 v119, v119, v26\n"
        "v_cvt_pk_bf16_f32 v112, v112, v113\n"
        "v_cvt_pk_bf16_f32 v113, v114, v115\n"
        "v_cvt_pk_bf16_f32 v114, v116, v117\n"
        "v_cvt_pk_bf16_f32 v115, v118, v119\n"
        "global_store_dwordx4 v254, v[112:115], s[84:85] sc1\n"
        "s_add_u32 s84, s84, 0x2000\n"
        "s_addc_u32 s85, s85, 0\n"
        "v_mul_f32 v120, v120, v26\n"
        "v_mul_f32 v121, v121, v26\n"
        "v_mul_f32 v122, v122, v26\n"
        "v_mul_f32 v123, v123, v26\n"
        "v_mul_f32 v124, v124, v26\n"
        "v_mul_f32 v125, v125, v26\n"
        "v_mul_f32 v126, v126, v26\n"
        "v_mul_f32 v127, v127, v26\n"
        "v_cvt_pk_bf16_f32 v120, v120, v121\n"
        "v_cvt_pk_bf16_f32 v121, v122, v123\n"
        "v_cvt_pk_bf16_f32 v122, v124, v125\n"
        "v_cvt_pk_bf16_f32 v123, v126, v127\n"
        "global_store_dwordx4 v254, v[120:123], s[84:85] sc1\n"
        "s_add_u32 s84, s84, 0x2000\n"
        "s_addc_u32 s85, s85, 0\n"
        "v_mul_f32 v144, v144, v26\n"
        "v_mul_f32 v145, v145, v26\n"
        "v_mul_f32 v146, v146, v26\n"
        "v_mul_f32 v147, v147, v26\n"
        "v_mul_f32 v148, v148, v26\n"
        "v_mul_f32 v149, v149, v26\n"
        "v_mul_f32 v150, v150, v26\n"
        "v_mul_f32 v151, v151, v26\n"
        "v_cvt_pk_bf16_f32 v144, v144, v145\n"
        "v_cvt_pk_bf16_f32 v145, v146, v147\n"
        "v_cvt_pk_bf16_f32 v146, v148, v149\n"
        "v_cvt_pk_bf16_f32 v147, v150, v151\n"
        "global_store_dwordx4 v254, v[144:147], s[84:85] sc1\n"
        "s_add_u32 s84, s84, 0x2000\n"
        "s_addc_u32 s85, s85, 0\n"
        "v_mul_f32 v152, v152, v26\n"
        "v_mul_f32 v153, v153, v26\n"
        "v_mul_f32 v154, v154, v26\n"
        "v_mul_f32 v155, v155, v26\n"
        "v_mul_f32 v156, v156, v26\n"
        "v_mul_f32 v157, v157, v26\n"
        "v_mul_f32 v158, v158, v26\n"
        "v_mul_f32 v159, v159, v26\n"
        "v_cvt_pk_bf16_f32 v152, v152, v153\n"
        "v_cvt_pk_bf16_f32 v153, v154, v155\n"
        "v_cvt_pk_bf16_f32 v154, v156, v157\n"
        "v_cvt_pk_bf16_f32 v155, v158, v159\n"
        "global_store_dwordx4 v254, v[152:155], s[84:85] sc1\n"
        "s_add_u32 s84, s84, 0x2000\n"
        "s_addc_u32 s85, s85, 0\n"
        "s_waitcnt vmcnt(0)\n"
        "s_cmp_eq_u32 s52, 0\n"
        "s_cbranch_scc1 LRET0_%=\n"
        "s_cmp_eq_u32 s52, 1\n"
        "s_cbranch_scc1 LRET1_%=\n"
        "s_cmp_eq_u32 s52, 2\n"
        "s_cbranch_scc1 LRET2_%=\n"
        "s_branch LRET3_%=\n"
        "LRL_%=:\n"
        "s_waitcnt vmcnt(0) lgkmcnt(0)\n"
        "s_nop 7\n"
        "v_accvgpr_write_b32 a176, v96\n"
        "v_accvgpr_write_b32 a177, v97\n"
        "v_accvgpr_write_b32 a178, v98\n"
        "v_accvgpr_write_b32 a179, v99\n"
        "v_accvgpr_write_b32 a180, v100\n"
        "v_accvgpr_write_b32 a181, v101\n"
        "v_accvgpr_write_b32 a182, v102\n"
        "v_accvgpr_write_b32 a183, v103\n"
        "v_accvgpr_write_b32 a184, v104\n"
        "v_accvgpr_write_b32 a185, v105\n"
        "v_accvgpr_write_b32 a186, v106\n"
        "v_accvgpr_write_b32 a187, v107\n"
        "v_accvgpr_write_b32 a188, v108\n"
        "v_accvgpr_write_b32 a189, v109\n"
        "v_accvgpr_write_b32 a190, v110\n"
        "v_accvgpr_write_b32 a191, v111\n"
        "s_mov_b32 s53, s44\n"
        "s_mov_b32 s96, 64\n"
        "s_mov_b32 s52, 0\n"
        "s_branch LREDO_%=\n"
        "LRET0_%=:\n"
        "v_accvgpr_write_b32 a176, v128\n"
        "v_accvgpr_write_b32 a177, v129\n"
        "v_accvgpr_write_b32 a178, v130\n"
        "v_accvgpr_write_b32 a179, v131\n"
        "v_accvgpr_write_b32 a180, v132\n"
        "v_accvgpr_write_b32 a181, v133\n"
        "v_accvgpr_write_b32 a182, v134\n"
        "v_accvgpr_write_b32 a183, v135\n"
        "v_accvgpr_write_b32 a184, v136\n"
        "v_accvgpr_write_b32 a185, v137\n"
        "v_accvgpr_write_b32 a186, v138\n"
        "v_accvgpr_write_b32 a187, v139\n"
        "v_accvgpr_write_b32 a188, v140\n"
        "v_accvgpr_write_b32 a189, v141\n"
        "v_accvgpr_write_b32 a190, v142\n"
        "v_accvgpr_write_b32 a191, v143\n"
        "s_mov_b32 s53, s44\n"
        "s_mov_b32 s96, 0\n"
        "s_mov_b32 s52, 1\n"
        "s_branch LREDO_%=\n"
        "LRET1_%=:\n"
        "s_branch LSL_%=\n"
        "LRH_%=:\n"
        "s_waitcnt vmcnt(0) lgkmcnt(0)\n"
        "s_nop 7\n"
        "v_accvgpr_write_b32 a176, v0\n"
        "v_accvgpr_write_b32 a177, v1\n"
        "v_accvgpr_write_b32 a178, v2\n"
        "v_accvgpr_write_b32 a179, v3\n"
        "v_accvgpr_write_b32 a180, v4\n"
        "v_accvgpr_write_b32 a181, v5\n"
        "v_accvgpr_write_b32 a182, v6\n"
        "v_accvgpr_write_b32 a183, v7\n"
        "v_accvgpr_write_b32 a184, v8\n"
        "v_accvgpr_write_b32 a185, v9\n"
        "v_accvgpr_write_b32 a186, v10\n"
        "v_accvgpr_write_b32 a187, v11\n"
        "v_accvgpr_write_b32 a188, v12\n"
        "v_accvgpr_write_b32 a189, v13\n"
        "v_accvgpr_write_b32 a190, v14\n"
        "v_accvgpr_write_b32 a191, v15\n"
        "s_mov_b32 s53, s45\n"
        "s_mov_b32 s96, 64\n"
        "s_mov_b32 s52, 2\n"
        "s_branch LREDO_%=\n"
        "LRET2_%=:\n"
        "v_accvgpr_write_b32 a176, v64\n"
        "v_accvgpr_write_b32 a177, v65\n"
        "v_accvgpr_write_b32 a178, v66\n"
        "v_accvgpr_write_b32 a179, v67\n"
        "v_accvgpr_write_b32 a180, v68\n"
        "v_accvgpr_write_b32 a181, v69\n"
        "v_accvgpr_write_b32 a182, v70\n"
        "v_accvgpr_write_b32 a183, v71\n"
        "v_accvgpr_write_b32 a184, v72\n"
        "v_accvgpr_write_b32 a185, v73\n"
        "v_accvgpr_write_b32 a186, v74\n"
        "v_accvgpr_write_b32 a187, v75\n"
        "v_accvgpr_write_b32 a188, v76\n"
        "v_accvgpr_write_b32 a189, v77\n"
        "v_accvgpr_write_b32 a190, v78\n"
        "v_accvgpr_write_b32 a191, v79\n"
        "s_mov_b32 s53, s45\n"
        "s_mov_b32 s96, 0\n"
        "s_mov_b32 s52, 3\n"
        "s_branch LREDO_%=\n"
        "LRET3_%=:\n"
        "s_branch LSH_%=\n"
        "LEP_%=:\n"
        "v_lshlrev_b32 v88, 3, v254\n"
        "v_add_u32 v88, 0x8000, v88\n"
        "s_mov_b32 s96, 4\n"
        "s_mov_b32 s97, 0x200000\n"
        "LEQ_%=:\n"
        "global_load_dwordx2 v[90:91], v88, s[48:49] sc1\n"
        "s_waitcnt vmcnt(0)\n"
        "v_cmp_ne_u64 vcc, v[90:91], v[250:251]\n"
        "s_nop 1\n"
        "s_cbranch_vccz LER_%=\n"
        "s_sleep 2\n"
        "s_sub_u32 s97, s97, 1\n"
        "s_cmp_lg_u32 s97, 0\n"
        "s_cbranch_scc1 LEQ_%=\n"
        "LER_%=:\n"
        "v_add_u32 v88, 0x2000, v88\n"
        "s_sub_u32 s96, s96, 1\n"
        "s_cmp_lg_u32 s96, 0\n"
        "s_cbranch_scc1 LEQ_%=\n"
        "v_mov_b32 v88, 0\n"
        "s_mov_b64 exec, 1\n"
        "global_store_dwordx2 v88, v[250:251], s[48:49] sc1\n"
        "s_mov_b64 exec, -1\n"
        "s_branch LEB_%=\n"
        "LPKV_%=:\n"
        "s_mov_b32 s97, 0x200000\n"
        "LQKV_%=:\n"
        "global_load_dwordx2 v[252:253], v245, s[48:49] sc1\n"
        "s_waitcnt vmcnt(0)\n"
        "v_cmp_ne_u64 vcc, v[252:253], v[250:251]\n"
        "s_nop 1\n"
        "s_cbranch_vccz LBKV_%=\n"
        "s_sleep 2\n"
        "s_sub_u32 s97, s97, 1\n"
        "s_cmp_lg_u32 s97, 0\n"
        "s_cbranch_scc1 LQKV_%=\n"
        "s_branch LBKV_%=\n"
        "LPCX_%=:\n"
        "s_mov_b32 s97, 0x200000\n"
        "LQCX_%=:\n"
        "global_load_dwordx2 v[236:237], v238, s[48:49] sc1\n"
        "s_waitcnt vmcnt(0)\n"
        "v_cmp_ne_u64 vcc, v[236:237], v[250:251]\n"
        "s_nop 1\n"
        "s_cbranch_vccz LBCX_%=\n"
        "s_sleep 2\n"
        "s_sub_u32 s97, s97, 1\n"
        "s_cmp_lg_u32 s97, 0\n"
        "s_cbranch_scc1 LQCX_%=\n"
        "s_branch LBCX_%=\n"
        "LPZZ_%=:\n"
        "s_mov_b32 s97, 0x200000\n"
        "LQZZ_%=:\n"
        "global_load_dwordx2 v[236:237], v234, s[48:49] sc1\n"
        "s_waitcnt vmcnt(0)\n"
        "v_cmp_ne_u64 vcc, v[236:237], v[250:251]\n"
        "s_nop 1\n"
        "s_cbranch_vccz LBZZ_%=\n"
        "s_sleep 2\n"
        "s_sub_u32 s97, s97, 1\n"
        "s_cmp_lg_u32 s97, 0\n"
        "s_cbranch_scc1 LQZZ_%=\n"
        "s_branch LBZZ_%=\n"
        "LPPR_%=:\n"
        "s_mov_b32 s97, 0x200000\n"
        "LQPR_%=:\n"
        "global_load_dwordx2 v[236:237], v235, s[48:49] sc1\n"
        "s_waitcnt vmcnt(0)\n"
        "v_cmp_ne_u64 vcc, v[236:237], v[250:251]\n"
        "s_nop 1\n"
        "s_cbranch_vccz LBPR_%=\n"
        "s_sleep 2\n"
        "s_sub_u32 s97, s97, 1\n"
        "s_cmp_lg_u32 s97, 0\n"
        "s_cbranch_scc1 LQPR_%=\n"
        "s_branch LBPR_%=\n"
        ".p2align 12\n"
        "LFRONT_%=:\n"
        "LHA0_%=:\n"
        "s_cbranch_execz LHA4_%=\n"
        "; ---- FRONT\n"
        "s_add_u32 m0, s72, 0x8000\n"
        "s_waitcnt vmcnt(14)\n"
        "global_load_lds_dwordx4 v234, s[56:57] offset:0\n"
        "s_barrier\n"
        "ds_read_b64_tr_b16 v[32:33], v226 offset:0\n"
        "ds_read_b64_tr_b16 v[34:35], v227 offset:0\n"
        "ds_read_b128 v[0:3], v224 offset:0\n"
        "global_load_lds_dwordx4 v235, s[56:57] offset:1024\n"
        "global_load_lds_dwordx4 v236, s[56:57] offset:2048\n"
        "ds_read_b128 v[4:7], v225 offset:0\n"
        "ds_read_b64_tr_b16 v[64:65], v226 offset:8192\n"
        "s_lshl_b32 s50, s42, 6\n"
        "s_waitcnt lgkmcnt(2)\n"
        "v_mfma_f32_16x16x32_bf16 a[0:3], v[32:35], v[0:3], 0\n"
        "ds_read_b64_tr_b16 v[66:67], v227 offset:8192\n"
        "global_load_lds_dwordx4 v237, s[56:57] offset:3072\n"
        "ds_read_b128 v[12:15], v225 offset:2048\n"
        "ds_read_b128 v[8:11], v224 offset:2048\n"
        "s_add_u32 m0, s72, 0x9000\n"
        "s_waitcnt lgkmcnt(2)\n"
        "global_load_lds_dwordx4 v234, s[58:59] offset:0\n"
        "ds_read_b64_tr_b16 v[36:37], v228 offset:0\n"
        "ds_read_b64_tr_b16 v[38:39], v229 offset:0\n"
        "ds_read_b64_tr_b16 v[68:69], v228 offset:8192\n"
        "s_lshl_b32 s51, s41, 7\n"
        "v_mfma_f32_16x16x32_bf16 a[0:3], v[64:67], v[4:7], a[0:3]\n"
        "ds_read_b64_tr_b16 v[70:71], v229 offset:8192\n"
        "ds_read_b64_tr_b16 v[40:41], v230 offset:0\n"
        "ds_read_b64_tr_b16 v[42:43], v231 offset:0\n"
        "ds_read_b64_tr_b16 v[72:73], v230 offset:8192\n"
        "s_lshl_b32 s52, s40, 5\n"
        "s_waitcnt lgkmcnt(8)\n"
        "v_mfma_f32_16x16x32_bf16 a[32:35], v[64:67], v[12:15], 0\n"
        "ds_read_b64_tr_b16 v[74:75], v231 offset:8192\n"
        "s_add_u32 s51, s51, s52\n"
        "s_waitcnt lgkmcnt(8)\n"
        "v_mfma_f32_16x16x32_bf16 a[32:35], v[32:35], v[8:11], a[32:35]\n"
        "ds_read_b64_tr_b16 v[44:45], v232 offset:0\n"
        "ds_read_b64_tr_b16 v[46:47], v233 offset:0\n"
        "ds_read_b64_tr_b16 v[76:77], v232 offset:8192\n"
        "ds_read_b64_tr_b16 v[78:79], v233 offset:8192\n"
        "s_waitcnt vmcnt(15)\n"
        "s_barrier\n"
        "ds_read_b64_tr_b16 v[48:49], v226 offset:16384\n"
        "s_lshl2_add_u32 s90, s43, s40\n"
        "s_waitcnt lgkmcnt(11)\n"
        "v_mfma_f32_16x16x32_bf16 a[36:39], v[36:39], v[8:11], 0\n"
        "ds_read_b64_tr_b16 v[50:51], v227 offset:16384\n"
        "global_load_lds_dwordx4 v235, s[58:59] offset:1024\n"
        "s_lshl_b32 s91, s44, 7\n"
        "s_waitcnt lgkmcnt(10)\n"
        "v_mfma_f32_16x16x32_bf16 a[36:39], v[68:71], v[12:15], a[36:39]\n"
        "ds_read_b64_tr_b16 v[80:81], v226 offset:24576\n"
        "s_add_u32 s56, s56, 0x80\n"
        "s_addc_u32 s57, s57, 0\n"
        "v_mfma_f32_16x16x32_bf16 a[4:7], v[68:71], v[4:7], 0\n"
        "ds_read_b64_tr_b16 v[82:83], v227 offset:24576\n"
        "s_add_u32 s80, s91, s90\n"
        "v_lshrrev_b32 v166, 4, v254\n"
        "v_mfma_f32_16x16x32_bf16 a[4:7], v[36:39], v[0:3], a[4:7]\n"
        "ds_read_b64_tr_b16 v[52:53], v228 offset:16384\n"
        "s_lshl_b32 s91, s80, 11\n"
        "s_waitcnt lgkmcnt(9)\n"
        "v_mfma_f32_16x16x32_bf16 a[8:11], v[40:43], v[0:3], 0\n"
        "ds_read_b64_tr_b16 v[54:55], v229 offset:16384\n"
        "global_load_lds_dwordx4 v236, s[58:59] offset:2048\n"
        "s_lshl_b32 s80, s80, 7\n"
        "v_mfma_f32_16x16x32_bf16 a[8:11], v[72:75], v[4:7], a[8:11]\n"
        "ds_read_b64_tr_b16 v[84:85], v228 offset:24576\n"
        "v_lshrrev_b32 v167, 3, v166\n"
        "v_mfma_f32_16x16x32_bf16 a[40:43], v[72:75], v[12:15], 0\n"
        "ds_read_b64_tr_b16 v[86:87], v229 offset:24576\n"
        "v_and_b32 v168, 7, v166\n"
        "v_mfma_f32_16x16x32_bf16 a[40:43], v[40:43], v[8:11], a[40:43]\n"
        "ds_read_b64_tr_b16 v[56:57], v230 offset:16384\n"
        "s_waitcnt lgkmcnt(8)\n"
        "v_mfma_f32_16x16x32_bf16 a[44:47], v[44:47], v[8:11], 0\n"
        "ds_read_b64_tr_b16 v[58:59], v231 offset:16384\n"
        "global_load_lds_dwordx4 v237, s[58:59] offset:3072\n"
        "s_add_u32 m0, s73, 0x20000\n"
        "v_mfma_f32_16x16x32_bf16 a[44:47], v[76:79], v[12:15], a[44:47]\n"
        "ds_read_b64_tr_b16 v[88:89], v230 offset:24576\n"
        "s_add_u32 s58, s58, 0x80\n"
        "s_addc_u32 s59, s59, 0\n"
        "v_mfma_f32_16x16x32_bf16 a[12:15], v[76:79], v[4:7], 0\n"
        "ds_read_b64_tr_b16 v[90:91], v231 offset:24576\n"
        "v_lshl_add_u32 v167, v167, 3, s50\n"
        "v_mfma_f32_16x16x32_bf16 a[12:15], v[44:47], v[0:3], a[12:15]\n"
        "ds_read_b64_tr_b16 v[60:61], v232 offset:16384\n"
        "s_waitcnt lgkmcnt(8)\n"
        "v_mfma_f32_16x16x32_bf16 a[16:19], v[0:3], v[48:51], 0\n"
        "ds_read_b64_tr_b16 v[62:63], v233 offset:16384\n"
        "global_load_lds_dwordx4 v238, s[60:61] offset:0\n"
        "v_mfma_f32_16x16x32_bf16 a[16:19], v[4:7], v[80:83], a[16:19]\n"
        "ds_read_b64_tr_b16 v[92:93], v232 offset:24576\n"
        "v_lshl_add_u32 v168, v168, 2, s51\n"
        "v_mfma_f32_16x16x32_bf16 a[48:51], v[12:15], v[80:83], 0\n"
        "ds_read_b64_tr_b16 v[94:95], v233 offset:24576\n"
        "v_lshlrev_b32 v169, 11, v167\n"
        "v_mfma_f32_16x16x32_bf16 a[48:51], v[8:11], v[48:51], a[48:51]\n"
        "ds_read_b128 v[16:19], v224 offset:4096\n"
        "s_waitcnt lgkmcnt(8)\n"
        "v_mfma_f32_16x16x32_bf16 a[52:55], v[8:11], v[52:55], 0\n"
        "ds_read_b128 v[20:23], v225 offset:4096\n"
        "global_load_lds_dwordx4 v239, s[60:61] offset:1024\n"
        "v_mfma_f32_16x16x32_bf16 a[52:55], v[12:15], v[84:87], a[52:55]\n"
        "ds_read_b128 v[24:27], v224 offset:6144\n"
        "v_lshl_add_u32 v169, v168, 1, v169\n"
        "v_mfma_f32_16x16x32_bf16 a[20:23], v[4:7], v[84:87], 0\n"
        "ds_read_b128 v[28:31], v225 offset:6144\n"
        "v_cmp_eq_u32 s[78:79], 0, v167\n"
        "v_mfma_f32_16x16x32_bf16 a[20:23], v[0:3], v[52:55], a[20:23]\n"
        "s_waitcnt lgkmcnt(7)\n"
        "v_mov_b32 v171, 6144\n"
        "v_cndmask_b32 v170, 0, v171, s[78:79]\n"
        "v_mfma_f32_16x16x32_bf16 a[24:27], v[0:3], v[56:59], 0\n"
        "global_load_lds_dwordx4 v240, s[60:61] offset:2048\n"
        "v_mfma_f32_16x16x32_bf16 a[24:27], v[4:7], v[88:91], a[24:27]\n"
        "s_waitcnt lgkmcnt(5)\n"
        "v_add_u32 v170, v170, v169\n"
        "v_sub_u32 v171, 2040, v167\n"
        "v_mfma_f32_16x16x32_bf16 a[56:59], v[12:15], v[88:91], 0\n"
        "s_waitcnt lgkmcnt(4)\n"
        "v_lshlrev_b32 v171, 1, v171\n"
        "v_lshl_add_u32 v171, v168, 12, v171\n"
        "v_mfma_f32_16x16x32_bf16 a[56:59], v[8:11], v[56:59], a[56:59]\n"
        "s_waitcnt lgkmcnt(3)\n"
        "v_lshlrev_b32 v168, 3, v168\n"
        "v_mfma_f32_16x16x32_bf16 a[60:63], v[8:11], v[60:63], 0\n"
        "global_load_lds_dwordx4 v241, s[60:61] offset:3072\n"
        "s_add_u32 m0, s73, 0x24000\n"
        "v_mfma_f32_16x16x32_bf16 a[60:63], v[12:15], v[92:95], a[60:63]\n"
        "ds_read_b128 v[8:11], v224 offset:10240\n"
        "s_add_u32 s60, s60, 0x80000\n"
        "s_addc_u32 s61, s61, 0\n"
        "v_mfma_f32_16x16x32_bf16 a[28:31], v[4:7], v[92:95], 0\n"
        "ds_read_b128 v[4:7], v225 offset:8192\n"
        "s_sub_u32 s74, s20, 2048\n"
        "s_subb_u32 s75, s21, 0\n"
        "v_mfma_f32_16x16x32_bf16 a[28:31], v[0:3], v[60:63], a[28:31]\n"
        "ds_read_b128 v[0:3], v224 offset:8192\n"
        "s_add_u32 s92, s8, s91\n"
        "s_addc_u32 s93, s9, 0\n"
        "v_mfma_f32_16x16x32_bf16 a[64:67], v[32:35], v[16:19], 0\n"
        "s_waitcnt lgkmcnt(3)\n"
        "global_load_lds_dwordx4 v238, s[62:63] offset:0\n"
        "s_lshl_b32 s91, s45, 7\n"
        "v_mfma_f32_16x16x32_bf16 a[64:67], v[64:67], v[20:23], a[64:67]\n"
        "ds_read_b128 v[12:15], v225 offset:10240\n"
        "s_add_u32 s81, s91, s90\n"
        "s_lshl_b32 s91, s81, 11\n"
        "v_mfma_f32_16x16x32_bf16 a[96:99], v[64:67], v[28:31], 0\n"
        "s_lshl_b32 s90, s41, 7\n"
        "s_lshl2_add_u32 s90, s42, s90\n"
        "s_add_u32 s90, s90, s40\n"
        "v_mfma_f32_16x16x32_bf16 a[96:99], v[32:35], v[24:27], a[96:99]\n"
        "s_lshl_b32 s90, s90, 1\n"
        "v_mov_b32 v172, s90\n"
        "s_lshl_b32 s81, s81, 7\n"
        "v_mfma_f32_16x16x32_bf16 a[100:103], v[36:39], v[24:27], 0\n"
        "global_load_lds_dwordx4 v239, s[62:63] offset:1024\n"
        "v_mfma_f32_16x16x32_bf16 a[100:103], v[68:71], v[28:31], a[100:103]\n"
        "s_add_u32 s76, s38, 0x3001000\n"
        "s_addc_u32 s77, s39, 0\n"
        "v_mfma_f32_16x16x32_bf16 a[68:71], v[68:71], v[20:23], 0\n"
        "v_mfma_f32_16x16x32_bf16 a[68:71], v[36:39], v[16:19], a[68:71]\n"
        "v_mfma_f32_16x16x32_bf16 a[72:75], v[40:43], v[16:19], 0\n"
        "global_load_lds_dwordx4 v240, s[62:63] offset:2048\n"
        "v_mfma_f32_16x16x32_bf16 a[72:75], v[72:75], v[20:23], a[72:75]\n"
        "v_mfma_f32_16x16x32_bf16 a[104:107], v[72:75], v[28:31], 0\n"
        "v_mfma_f32_16x16x32_bf16 a[104:107], v[40:43], v[24:27], a[104:107]\n"
        "v_mfma_f32_16x16x32_bf16 a[108:111], v[44:47], v[24:27], 0\n"
        "global_load_lds_dwordx4 v241, s[62:63] offset:3072\n"
        "s_add_u32 m0, s72, 0x0\n"
        "v_mfma_f32_16x16x32_bf16 a[108:111], v[76:79], v[28:31], a[108:111]\n"
        "s_add_u32 s62, s62, 0x80000\n"
        "s_addc_u32 s63, s63, 0\n"
        "v_mfma_f32_16x16x32_bf16 a[76:79], v[76:79], v[20:23], 0\n"
        "v_mfma_f32_16x16x32_bf16 a[76:79], v[44:47], v[16:19], a[76:79]\n"
        "v_mfma_f32_16x16x32_bf16 a[80:83], v[16:19], v[48:51], 0\n"
        "v_mfma_f32_16x16x32_bf16 a[80:83], v[20:23], v[80:83], a[80:83]\n"
        "v_mfma_f32_16x16x32_bf16 a[112:115], v[28:31], v[80:83], 0\n"
        "v_mfma_f32_16x16x32_bf16 a[112:115], v[24:27], v[48:51], a[112:115]\n"
        "v_mfma_f32_16x16x32_bf16 a[116:119], v[24:27], v[52:55], 0\n"
        "v_mfma_f32_16x16x32_bf16 a[116:119], v[28:31], v[84:87], a[116:119]\n"
        "s_waitcnt lgkmcnt(0)\n"
        "v_mfma_f32_16x16x32_bf16 a[84:87], v[20:23], v[84:87], 0\n"
        "v_mfma_f32_16x16x32_bf16 a[84:87], v[16:19], v[52:55], a[84:87]\n"
        "v_mfma_f32_16x16x32_bf16 a[88:91], v[16:19], v[56:59], 0\n"
        "v_mfma_f32_16x16x32_bf16 a[88:91], v[20:23], v[88:91], a[88:91]\n"
        "v_mfma_f32_16x16x32_bf16 a[120:123], v[28:31], v[88:91], 0\n"
        "v_mfma_f32_16x16x32_bf16 a[120:123], v[24:27], v[56:59], a[120:123]\n"
        "v_mfma_f32_16x16x32_bf16 a[124:127], v[24:27], v[60:63], 0\n"
        "ds_read_b128 v[24:27], v224 offset:14336\n"
        "v_mfma_f32_16x16x32_bf16 a[124:127], v[28:31], v[92:95], a[124:127]\n"
        "ds_read_b128 v[28:31], v225 offset:14336\n"
        "v_mfma_f32_16x16x32_bf16 a[92:95], v[20:23], v[92:95], 0\n"
        "ds_read_b128 v[20:23], v225 offset:12288\n"
        "v_mfma_f32_16x16x32_bf16 a[92:95], v[16:19], v[60:63], a[92:95]\n"
        "ds_read_b128 v[16:19], v224 offset:12288\n"
        "v_mfma_f32_16x16x32_bf16 a[128:131], v[32:35], v[0:3], 0\n"
        "v_mfma_f32_16x16x32_bf16 a[128:131], v[64:67], v[4:7], a[128:131]\n"
        "v_mfma_f32_16x16x32_bf16 a[160:163], v[64:67], v[12:15], 0\n"
        "v_mfma_f32_16x16x32_bf16 a[160:163], v[32:35], v[8:11], a[160:163]\n"
        "v_mfma_f32_16x16x32_bf16 a[164:167], v[36:39], v[8:11], 0\n"
        "v_mfma_f32_16x16x32_bf16 a[164:167], v[68:71], v[12:15], a[164:167]\n"
        "v_mfma_f32_16x16x32_bf16 a[132:135], v[68:71], v[4:7], 0\n"
        "v_mfma_f32_16x16x32_bf16 a[132:135], v[36:39], v[0:3], a[132:135]\n"
        "v_mfma_f32_16x16x32_bf16 a[136:139], v[40:43], v[0:3], 0\n"
        "v_mfma_f32_16x16x32_bf16 a[136:139], v[72:75], v[4:7], a[136:139]\n"
        "v_mfma_f32_16x16x32_bf16 a[168:171], v[72:75], v[12:15], 0\n"
        "v_mfma_f32_16x16x32_bf16 a[168:171], v[40:43], v[8:11], a[168:171]\n"
        "v_mfma_f32_16x16x32_bf16 a[172:175], v[44:47], v[8:11], 0\n"
        "v_mfma_f32_16x16x32_bf16 a[172:175], v[76:79], v[12:15], a[172:175]\n"
        "v_mfma_f32_16x16x32_bf16 a[140:143], v[76:79], v[4:7], 0\n"
        "v_mfma_f32_16x16x32_bf16 a[140:143], v[44:47], v[0:3], a[140:143]\n"
        "v_mfma_f32_16x16x32_bf16 a[144:147], v[0:3], v[48:51], 0\n"
        "v_mfma_f32_16x16x32_bf16 a[144:147], v[4:7], v[80:83], a[144:147]\n"
        "v_mfma_f32_16x16x32_bf16 a[176:179], v[12:15], v[80:83], 0\n"
        "v_mfma_f32_16x16x32_bf16 a[176:179], v[8:11], v[48:51], a[176:179]\n"
        "v_mfma_f32_16x16x32_bf16 a[180:183], v[8:11], v[52:55], 0\n"
        "v_mfma_f32_16x16x32_bf16 a[180:183], v[12:15], v[84:87], a[180:183]\n"
        "s_waitcnt lgkmcnt(0)\n"
        "v_mfma_f32_16x16x32_bf16 a[148:151], v[4:7], v[84:87], 0\n"
        "v_mfma_f32_16x16x32_bf16 a[148:151], v[0:3], v[52:55], a[148:151]\n"
        "global_load_dwordx4 v[134:137], v168, s[24:25] offset:0\n"
        "v_mfma_f32_16x16x32_bf16 a[152:155], v[0:3], v[56:59], 0\n"
        "v_mfma_f32_16x16x32_bf16 a[152:155], v[4:7], v[88:91], a[152:155]\n"
        "v_mfma_f32_16x16x32_bf16 a[184:187], v[12:15], v[88:91], 0\n"
        "v_mfma_f32_16x16x32_bf16 a[184:187], v[8:11], v[56:59], a[184:187]\n"
        "s_waitcnt vmcnt(9)\n"
        "s_barrier\n"
        "v_mfma_f32_16x16x32_bf16 a[188:191], v[8:11], v[60:63], 0\n"
        "global_load_lds_dwordx4 v234, s[56:57] offset:0\n"
        "ds_read_b128 v[8:11], v224 offset:34816\n"
        "v_mfma_f32_16x16x32_bf16 a[188:191], v[12:15], v[92:95], a[188:191]\n"
        "ds_read_b128 v[12:15], v225 offset:34816\n"
        "v_mfma_f32_16x16x32_bf16 a[156:159], v[4:7], v[92:95], 0\n"
        "ds_read_b128 v[4:7], v225 offset:32768\n"
        "v_mfma_f32_16x16x32_bf16 a[156:159], v[0:3], v[60:63], a[156:159]\n"
        "ds_read_b128 v[0:3], v224 offset:32768\n"
        "s_add_u32 s82, s12, 0x0\n"
        "s_addc_u32 s83, s13, 0\n"
        "v_mfma_f32_16x16x32_bf16 a[192:195], v[32:35], v[16:19], 0\n"
        "global_load_lds_dwordx4 v235, s[56:57] offset:1024\n"
        "v_mfma_f32_16x16x32_bf16 a[192:195], v[64:67], v[20:23], a[192:195]\n"
        "v_mfma_f32_16x16x32_bf16 a[224:227], v[64:67], v[28:31], 0\n"
        "ds_read_b64_tr_b16 v[64:65], v226 offset:40960\n"
        "v_mfma_f32_16x16x32_bf16 a[224:227], v[32:35], v[24:27], a[224:227]\n"
        "ds_read_b64_tr_b16 v[32:33], v226 offset:32768\n"
        "v_mfma_f32_16x16x32_bf16 a[228:231], v[36:39], v[24:27], 0\n"
        "ds_read_b64_tr_b16 v[34:35], v227 offset:32768\n"
        "global_load_dwordx4 v[138:141], v168, s[24:25] offset:16\n"
        "v_mfma_f32_16x16x32_bf16 a[228:231], v[68:71], v[28:31], a[228:231]\n"
        "ds_read_b64_tr_b16 v[66:67], v227 offset:40960\n"
        "v_mfma_f32_16x16x32_bf16 a[196:199], v[68:71], v[20:23], 0\n"
        "ds_read_b64_tr_b16 v[68:69], v228 offset:40960\n"
        "v_mfma_f32_16x16x32_bf16 a[196:199], v[36:39], v[16:19], a[196:199]\n"
        "ds_read_b64_tr_b16 v[36:37], v228 offset:32768\n"
        "v_mfma_f32_16x16x32_bf16 a[200:203], v[40:43], v[16:19], 0\n"
        "ds_read_b64_tr_b16 v[38:39], v229 offset:32768\n"
        "global_load_lds_dwordx4 v236, s[56:57] offset:2048\n"
        "v_mfma_f32_16x16x32_bf16 a[200:203], v[72:75], v[20:23], a[200:203]\n"
        "ds_read_b64_tr_b16 v[70:71], v229 offset:40960\n"
        "v_mfma_f32_16x16x32_bf16 a[232:235], v[72:75], v[28:31], 0\n"
        "ds_read_b64_tr_b16 v[72:73], v230 offset:40960\n"
        "v_mfma_f32_16x16x32_bf16 a[232:235], v[40:43], v[24:27], a[232:235]\n"
        "ds_read_b64_tr_b16 v[40:41], v230 offset:32768\n"
        "v_mfma_f32_16x16x32_bf16 a[236:239], v[44:47], v[24:27], 0\n"
        "ds_read_b64_tr_b16 v[42:43], v231 offset:32768\n"
        "global_load_lds_dwordx4 v237, s[56:57] offset:3072\n"
        "s_add_u32 m0, s72, 0x1000\n"
        "v_mfma_f32_16x16x32_bf16 a[236:239], v[76:79], v[28:31], a[236:239]\n"
        "ds_read_b64_tr_b16 v[74:75], v231 offset:40960\n"
        "s_add_u32 s56, s56, 0x80\n"
        "s_addc_u32 s57, s57, 0\n"
        "v_mfma_f32_16x16x32_bf16 a[204:207], v[76:79], v[20:23], 0\n"
        "ds_read_b64_tr_b16 v[76:77], v232 offset:40960\n"
        "v_mfma_f32_16x16x32_bf16 a[204:207], v[44:47], v[16:19], a[204:207]\n"
        "ds_read_b64_tr_b16 v[44:45], v232 offset:32768\n"
        "v_mfma_f32_16x16x32_bf16 a[208:211], v[16:19], v[48:51], 0\n"
        "ds_read_b64_tr_b16 v[46:47], v233 offset:32768\n"
        "global_load_dwordx2 v[96:97], v170, s[74:75] offset:-4096\n"
        "v_mfma_f32_16x16x32_bf16 a[208:211], v[20:23], v[80:83], a[208:211]\n"
        "ds_read_b64_tr_b16 v[78:79], v233 offset:40960\n"
        "global_load_lds_dwordx4 v234, s[58:59] offset:0\n"
        "v_mfma_f32_16x16x32_bf16 a[240:243], v[28:31], v[80:83], 0\n"
        "ds_read_b64_tr_b16 v[80:81], v226 offset:57344\n"
        "v_mfma_f32_16x16x32_bf16 a[240:243], v[24:27], v[48:51], a[240:243]\n"
        "ds_read_b64_tr_b16 v[48:49], v226 offset:49152\n"
        "v_mfma_f32_16x16x32_bf16 a[244:247], v[24:27], v[52:55], 0\n"
        "ds_read_b64_tr_b16 v[50:51], v227 offset:49152\n"
        "v_mfma_f32_16x16x32_bf16 a[244:247], v[28:31], v[84:87], a[244:247]\n"
        "ds_read_b64_tr_b16 v[82:83], v227 offset:57344\n"
        "global_load_lds_dwordx4 v235, s[58:59] offset:1024\n"
        "v_mfma_f32_16x16x32_bf16 a[212:215], v[20:23], v[84:87], 0\n"
        "ds_read_b64_tr_b16 v[84:85], v228 offset:57344\n"
        "s_waitcnt lgkmcnt(8)\n"
        "v_mfma_f32_16x16x32_bf16 a[212:215], v[16:19], v[52:55], a[212:215]\n"
        "ds_read_b64_tr_b16 v[52:53], v228 offset:49152\n"
        "v_mfma_f32_16x16x32_bf16 a[216:219], v[16:19], v[56:59], 0\n"
        "ds_read_b64_tr_b16 v[54:55], v229 offset:49152\n"
        "v_mfma_f32_16x16x32_bf16 a[216:219], v[20:23], v[88:91], a[216:219]\n"
        "ds_read_b64_tr_b16 v[86:87], v229 offset:57344\n"
        "global_load_dwordx2 v[98:99], v170, s[74:75] offset:-2048\n"
        "v_mfma_f32_16x16x32_bf16 a[248:251], v[28:31], v[88:91], 0\n"
        "global_load_lds_dwordx4 v236, s[58:59] offset:2048\n"
        "ds_read_b64_tr_b16 v[88:89], v230 offset:57344\n"
        "v_mfma_f32_16x16x32_bf16 a[248:251], v[24:27], v[56:59], a[248:251]\n"
        "ds_read_b64_tr_b16 v[56:57], v230 offset:49152\n"
        "v_mfma_f32_16x16x32_bf16 a[252:255], v[24:27], v[60:63], 0\n"
        "ds_read_b64_tr_b16 v[58:59], v231 offset:49152\n"
        "v_mfma_f32_16x16x32_bf16 a[252:255], v[28:31], v[92:95], a[252:255]\n"
        "ds_read_b64_tr_b16 v[90:91], v231 offset:57344\n"
        "v_mfma_f32_16x16x32_bf16 a[220:223], v[20:23], v[92:95], 0\n"
        "global_load_lds_dwordx4 v237, s[58:59] offset:3072\n"
        "ds_read_b64_tr_b16 v[92:93], v232 offset:57344\n"
        "s_add_u32 m0, s73, 0x10000\n"
        "v_mfma_f32_16x16x32_bf16 a[220:223], v[16:19], v[60:63], a[220:223]\n"
        "ds_read_b64_tr_b16 v[60:61], v232 offset:49152\n"
        "s_add_u32 s58, s58, 0x80\n"
        "s_addc_u32 s59, s59, 0\n"
        "v_mfma_f32_16x16x32_bf16 a[0:3], v[32:35], v[0:3], a[0:3]\n"
        "ds_read_b64_tr_b16 v[62:63], v233 offset:49152\n"
        "v_mfma_f32_16x16x32_bf16 a[0:3], v[64:67], v[4:7], a[0:3]\n"
        "ds_read_b64_tr_b16 v[94:95], v233 offset:57344\n"
        "s_waitcnt lgkmcnt(8)\n"
        "v_mfma_f32_16x16x32_bf16 a[32:35], v[64:67], v[12:15], a[32:35]\n"
        "global_load_dwordx2 v[100:101], v170, s[74:75] offset:0\n"
        "ds_read_b128 v[16:19], v224 offset:36864\n"
        "v_mfma_f32_16x16x32_bf16 a[32:35], v[32:35], v[8:11], a[32:35]\n"
        "ds_read_b128 v[20:23], v225 offset:36864\n"
        "global_load_lds_dwordx4 v238, s[60:61] offset:0\n"
        "v_mfma_f32_16x16x32_bf16 a[36:39], v[36:39], v[8:11], a[36:39]\n"
        "ds_read_b128 v[24:27], v224 offset:38912\n"
        "v_mfma_f32_16x16x32_bf16 a[36:39], v[68:71], v[12:15], a[36:39]\n"
        "ds_read_b128 v[28:31], v225 offset:38912\n"
        "v_mfma_f32_16x16x32_bf16 a[4:7], v[68:71], v[4:7], a[4:7]\n"
        "v_mfma_f32_16x16x32_bf16 a[4:7], v[36:39], v[0:3], a[4:7]\n"
        "global_load_lds_dwordx4 v239, s[60:61] offset:1024\n"
        "v_mfma_f32_16x16x32_bf16 a[8:11], v[40:43], v[0:3], a[8:11]\n"
        "v_mfma_f32_16x16x32_bf16 a[8:11], v[72:75], v[4:7], a[8:11]\n"
        "v_mfma_f32_16x16x32_bf16 a[40:43], v[72:75], v[12:15], a[40:43]\n"
        "v_mfma_f32_16x16x32_bf16 a[40:43], v[40:43], v[8:11], a[40:43]\n"
        "global_load_dwordx2 v[102:103], v169, s[74:75] offset:2048\n"
        "v_mfma_f32_16x16x32_bf16 a[44:47], v[44:47], v[8:11], a[44:47]\n"
        "global_load_lds_dwordx4 v240, s[60:61] offset:2048\n"
        "s_add_u32 s74, s20, 0x1800\n"
        "s_addc_u32 s75, s21, 0\n"
        "v_mfma_f32_16x16x32_bf16 a[44:47], v[76:79], v[12:15], a[44:47]\n"
        "s_waitcnt lgkmcnt(0)\n"
        "v_mfma_f32_16x16x32_bf16 a[12:15], v[76:79], v[4:7], a[12:15]\n"
        "v_mfma_f32_16x16x32_bf16 a[12:15], v[44:47], v[0:3], a[12:15]\n"
        "v_mfma_f32_16x16x32_bf16 a[16:19], v[0:3], v[48:51], a[16:19]\n"
        "global_load_lds_dwordx4 v241, s[60:61] offset:3072\n"
        "s_add_u32 m0, s73, 0x14000\n"
        "v_mfma_f32_16x16x32_bf16 a[16:19], v[4:7], v[80:83], a[16:19]\n"
        "s_add_u32 s60, s60, 0x80000\n"
        "s_addc_u32 s61, s61, 0\n"
        "v_mfma_f32_16x16x32_bf16 a[48:51], v[12:15], v[80:83], a[48:51]\n"
        "v_mfma_f32_16x16x32_bf16 a[48:51], v[8:11], v[48:51], a[48:51]\n"
        "v_mfma_f32_16x16x32_bf16 a[52:55], v[8:11], v[52:55], a[52:55]\n"
        "global_load_dwordx2 v[104:105], v169, s[74:75] offset:-4096\n"
        "v_mfma_f32_16x16x32_bf16 a[52:55], v[12:15], v[84:87], a[52:55]\n"
        "global_load_lds_dwordx4 v238, s[62:63] offset:0\n"
        "v_mfma_f32_16x16x32_bf16 a[20:23], v[4:7], v[84:87], a[20:23]\n"
        "v_mfma_f32_16x16x32_bf16 a[20:23], v[0:3], v[52:55], a[20:23]\n"
        "v_mfma_f32_16x16x32_bf16 a[24:27], v[0:3], v[56:59], a[24:27]\n"
        "v_mfma_f32_16x16x32_bf16 a[24:27], v[4:7], v[88:91], a[24:27]\n"
        "global_load_lds_dwordx4 v239, s[62:63] offset:1024\n"
        "v_mfma_f32_16x16x32_bf16 a[56:59], v[12:15], v[88:91], a[56:59]\n"
        "v_mfma_f32_16x16x32_bf16 a[56:59], v[8:11], v[56:59], a[56:59]\n"
        "v_mfma_f32_16x16x32_bf16 a[60:63], v[8:11], v[60:63], a[60:63]\n"
        "ds_read_b128 v[8:11], v224 offset:43008\n"
        "v_mfma_f32_16x16x32_bf16 a[60:63], v[12:15], v[92:95], a[60:63]\n"
        "global_load_dwordx2 v[106:107], v169, s[74:75] offset:-2048\n"
        "ds_read_b128 v[12:15], v225 offset:43008\n"
        "v_mfma_f32_16x16x32_bf16 a[28:31], v[4:7], v[92:95], a[28:31]\n"
        "global_load_lds_dwordx4 v240, s[62:63] offset:2048\n"
        "ds_read_b128 v[4:7], v225 offset:40960\n"
        "v_mfma_f32_16x16x32_bf16 a[28:31], v[0:3], v[60:63], a[28:31]\n"
        "ds_read_b128 v[0:3], v224 offset:40960\n"
        "v_mfma_f32_16x16x32_bf16 a[64:67], v[32:35], v[16:19], a[64:67]\n"
        "v_mfma_f32_16x16x32_bf16 a[64:67], v[64:67], v[20:23], a[64:67]\n"
        "v_mfma_f32_16x16x32_bf16 a[96:99], v[64:67], v[28:31], a[96:99]\n"
        "global_load_lds_dwordx4 v241, s[62:63] offset:3072\n"
        "s_add_u32 s62, s62, 0x80000\n"
        "s_addc_u32 s63, s63, 0\n"
        "v_mfma_f32_16x16x32_bf16 a[96:99], v[32:35], v[24:27], a[96:99]\n"
        "s_add_u32 m0, s72, 0x8000\n"
        "v_mfma_f32_16x16x32_bf16 a[100:103], v[36:39], v[24:27], a[100:103]\n"
        "v_mfma_f32_16x16x32_bf16 a[100:103], v[68:71], v[28:31], a[100:103]\n"
        "v_mfma_f32_16x16x32_bf16 a[68:71], v[68:71], v[20:23], a[68:71]\n"
        "global_load_dwordx2 v[108:109], v169, s[74:75] offset:0\n"
        "v_mfma_f32_16x16x32_bf16 a[68:71], v[36:39], v[16:19], a[68:71]\n"
        "global_load_dwordx2 v[110:111], v169, s[74:75] offset:2048\n"
        "s_add_u32 s74, s20, 0x3800\n"
        "s_addc_u32 s75, s21, 0\n"
        "v_mfma_f32_16x16x32_bf16 a[72:75], v[40:43], v[16:19], a[72:75]\n"
        "global_load_dwordx2 v[112:113], v169, s[74:75] offset:-4096\n"
        "v_mfma_f32_16x16x32_bf16 a[72:75], v[72:75], v[20:23], a[72:75]\n"
        "v_mfma_f32_16x16x32_bf16 a[104:107], v[72:75], v[28:31], a[104:107]\n"
        "v_mfma_f32_16x16x32_bf16 a[104:107], v[40:43], v[24:27], a[104:107]\n"
        "v_mfma_f32_16x16x32_bf16 a[108:111], v[44:47], v[24:27], a[108:111]\n"
        "v_mfma_f32_16x16x32_bf16 a[108:111], v[76:79], v[28:31], a[108:111]\n"
        "v_mfma_f32_16x16x32_bf16 a[76:79], v[76:79], v[20:23], a[76:79]\n"
        "v_mfma_f32_16x16x32_bf16 a[76:79], v[44:47], v[16:19], a[76:79]\n"
        "global_load_dwordx2 v[114:115], v169, s[74:75] offset:-2048\n"
        "v_mfma_f32_16x16x32_bf16 a[80:83], v[16:19], v[48:51], a[80:83]\n"
        "v_mfma_f32_16x16x32_bf16 a[80:83], v[20:23], v[80:83], a[80:83]\n"
        "v_mfma_f32_16x16x32_bf16 a[112:115], v[28:31], v[80:83], a[112:115]\n"
        "v_mfma_f32_16x16x32_bf16 a[112:115], v[24:27], v[48:51], a[112:115]\n"
        "v_mfma_f32_16x16x32_bf16 a[116:119], v[24:27], v[52:55], a[116:119]\n"
        "v_mfma_f32_16x16x32_bf16 a[116:119], v[28:31], v[84:87], a[116:119]\n"
        "s_waitcnt lgkmcnt(0)\n"
        "v_mfma_f32_16x16x32_bf16 a[84:87], v[20:23], v[84:87], a[84:87]\n"
        "v_mfma_f32_16x16x32_bf16 a[84:87], v[16:19], v[52:55], a[84:87]\n"
        "global_load_dwordx2 v[116:117], v169, s[74:75] offset:0\n"
        "s_add_u32 s74, s22, 0x1000\n"
        "s_addc_u32 s75, s23, 0\n"
        "v_mfma_f32_16x16x32_bf16 a[88:91], v[16:19], v[56:59], a[88:91]\n"
        "v_mfma_f32_16x16x32_bf16 a[88:91], v[20:23], v[88:91], a[88:91]\n"
        "v_mfma_f32_16x16x32_bf16 a[120:123], v[28:31], v[88:91], a[120:123]\n"
        "v_mfma_f32_16x16x32_bf16 a[120:123], v[24:27], v[56:59], a[120:123]\n"
        "v_mfma_f32_16x16x32_bf16 a[124:127], v[24:27], v[60:63], a[124:127]\n"
        "ds_read_b128 v[24:27], v224 offset:47104\n"
        "v_mfma_f32_16x16x32_bf16 a[124:127], v[28:31], v[92:95], a[124:127]\n"
        "ds_read_b128 v[28:31], v225 offset:47104\n"
        "v_mfma_f32_16x16x32_bf16 a[92:95], v[20:23], v[92:95], a[92:95]\n"
        "ds_read_b128 v[20:23], v225 offset:45056\n"
        "v_mfma_f32_16x16x32_bf16 a[92:95], v[16:19], v[60:63], a[92:95]\n"
        "ds_read_b128 v[16:19], v224 offset:45056\n"
        "global_load_dwordx2 v[118:119], v169, s[74:75] offset:-4096\n"
        "v_mfma_f32_16x16x32_bf16 a[128:131], v[32:35], v[0:3], a[128:131]\n"
        "v_mfma_f32_16x16x32_bf16 a[128:131], v[64:67], v[4:7], a[128:131]\n"
        "v_mfma_f32_16x16x32_bf16 a[160:163], v[64:67], v[12:15], a[160:163]\n"
        "v_mfma_f32_16x16x32_bf16 a[160:163], v[32:35], v[8:11], a[160:163]\n"
        "v_mfma_f32_16x16x32_bf16 a[164:167], v[36:39], v[8:11], a[164:167]\n"
        "v_mfma_f32_16x16x32_bf16 a[164:167], v[68:71], v[12:15], a[164:167]\n"
        "v_mfma_f32_16x16x32_bf16 a[132:135], v[68:71], v[4:7], a[132:135]\n"
        "v_mfma_f32_16x16x32_bf16 a[132:135], v[36:39], v[0:3], a[132:135]\n"
        "global_load_dwordx2 v[120:121], v169, s[74:75] offset:-2048\n"
        "v_mfma_f32_16x16x32_bf16 a[136:139], v[40:43], v[0:3], a[136:139]\n"
        "v_mfma_f32_16x16x32_bf16 a[136:139], v[72:75], v[4:7], a[136:139]\n"
        "v_mfma_f32_16x16x32_bf16 a[168:171], v[72:75], v[12:15], a[168:171]\n"
        "v_mfma_f32_16x16x32_bf16 a[168:171], v[40:43], v[8:11], a[168:171]\n"
        "v_mfma_f32_16x16x32_bf16 a[172:175], v[44:47], v[8:11], a[172:175]\n"
        "v_mfma_f32_16x16x32_bf16 a[172:175], v[76:79], v[12:15], a[172:175]\n"
        "v_mfma_f32_16x16x32_bf16 a[140:143], v[76:79], v[4:7], a[140:143]\n"
        "v_mfma_f32_16x16x32_bf16 a[140:143], v[44:47], v[0:3], a[140:143]\n"
        "global_load_dwordx2 v[122:123], v169, s[74:75] offset:0\n"
        "v_mfma_f32_16x16x32_bf16 a[144:147], v[0:3], v[48:51], a[144:147]\n"
        "v_mfma_f32_16x16x32_bf16 a[144:147], v[4:7], v[80:83], a[144:147]\n"
        "v_mfma_f32_16x16x32_bf16 a[176:179], v[12:15], v[80:83], a[176:179]\n"
        "v_mfma_f32_16x16x32_bf16 a[176:179], v[8:11], v[48:51], a[176:179]\n"
        "v_mfma_f32_16x16x32_bf16 a[180:183], v[8:11], v[52:55], a[180:183]\n"
        "v_mfma_f32_16x16x32_bf16 a[180:183], v[12:15], v[84:87], a[180:183]\n"
        "s_waitcnt lgkmcnt(0)\n"
        "v_mfma_f32_16x16x32_bf16 a[148:151], v[4:7], v[84:87], a[148:151]\n"
        "v_mfma_f32_16x16x32_bf16 a[148:151], v[0:3], v[52:55], a[148:151]\n"
        "global_load_dwordx2 v[124:125], v169, s[74:75] offset:2048\n"
        "s_add_u32 s74, s22, 0x3000\n"
        "s_addc_u32 s75, s23, 0\n"
        "v_mfma_f32_16x16x32_bf16 a[152:155], v[0:3], v[56:59], a[152:155]\n"
        "v_mfma_f32_16x16x32_bf16 a[152:155], v[4:7], v[88:91], a[152:155]\n"
        "v_mfma_f32_16x16x32_bf16 a[184:187], v[12:15], v[88:91], a[184:187]\n"
        "v_mfma_f32_16x16x32_bf16 a[184:187], v[8:11], v[56:59], a[184:187]\n"
        "s_waitcnt vmcnt(21)\n"
        "s_barrier\n"
        "v_mfma_f32_16x16x32_bf16 a[188:191], v[8:11], v[60:63], a[188:191]\n"
        "global_load_lds_dwordx4 v234, s[56:57] offset:0\n"
        "ds_read_b128 v[8:11], v224 offset:2048\n"
        "v_mfma_f32_16x16x32_bf16 a[188:191], v[12:15], v[92:95], a[188:191]\n"
        "ds_read_b128 v[12:15], v225 offset:2048\n"
        "v_mfma_f32_16x16x32_bf16 a[156:159], v[4:7], v[92:95], a[156:159]\n"
        "ds_read_b128 v[4:7], v225 offset:0\n"
        "v_mfma_f32_16x16x32_bf16 a[156:159], v[0:3], v[60:63], a[156:159]\n"
        "ds_read_b128 v[0:3], v224 offset:0\n"
        "v_mfma_f32_16x16x32_bf16 a[192:195], v[32:35], v[16:19], a[192:195]\n"
        "global_load_lds_dwordx4 v235, s[56:57] offset:1024\n"
        "v_mfma_f32_16x16x32_bf16 a[192:195], v[64:67], v[20:23], a[192:195]\n"
        "v_mfma_f32_16x16x32_bf16 a[224:227], v[64:67], v[28:31], a[224:227]\n"
        "ds_read_b64_tr_b16 v[64:65], v216 offset:8192\n"
        "v_mfma_f32_16x16x32_bf16 a[224:227], v[32:35], v[24:27], a[224:227]\n"
        "ds_read_b64_tr_b16 v[32:33], v216 offset:0\n"
        "v_mfma_f32_16x16x32_bf16 a[228:231], v[36:39], v[24:27], a[228:231]\n"
        "ds_read_b64_tr_b16 v[34:35], v217 offset:0\n"
        "global_load_dwordx2 v[126:127], v169, s[74:75] offset:-4096\n"
        "v_mfma_f32_16x16x32_bf16 a[228:231], v[68:71], v[28:31], a[228:231]\n"
        "ds_read_b64_tr_b16 v[66:67], v217 offset:8192\n"
        "global_load_lds_dwordx4 v236, s[56:57] offset:2048\n"
        "v_mfma_f32_16x16x32_bf16 a[196:199], v[68:71], v[20:23], a[196:199]\n"
        "ds_read_b64_tr_b16 v[68:69], v218 offset:8192\n"
        "v_mfma_f32_16x16x32_bf16 a[196:199], v[36:39], v[16:19], a[196:199]\n"
        "ds_read_b64_tr_b16 v[36:37], v218 offset:0\n"
        "v_mfma_f32_16x16x32_bf16 a[200:203], v[40:43], v[16:19], a[200:203]\n"
        "ds_read_b64_tr_b16 v[38:39], v219 offset:0\n"
        "v_mfma_f32_16x16x32_bf16 a[200:203], v[72:75], v[20:23], a[200:203]\n"
        "ds_read_b64_tr_b16 v[70:71], v219 offset:8192\n"
        "global_load_lds_dwordx4 v237, s[56:57] offset:3072\n"
        "s_add_u32 m0, s72, 0x9000\n"
        "v_mfma_f32_16x16x32_bf16 a[232:235], v[72:75], v[28:31], a[232:235]\n"
        "ds_read_b64_tr_b16 v[72:73], v220 offset:8192\n"
        "s_add_u32 s56, s56, 0x80\n"
        "s_addc_u32 s57, s57, 0\n"
        "v_mfma_f32_16x16x32_bf16 a[232:235], v[40:43], v[24:27], a[232:235]\n"
        "ds_read_b64_tr_b16 v[40:41], v220 offset:0\n"
        "v_mfma_f32_16x16x32_bf16 a[236:239], v[44:47], v[24:27], a[236:239]\n"
        "ds_read_b64_tr_b16 v[42:43], v221 offset:0\n"
        "v_mfma_f32_16x16x32_bf16 a[236:239], v[76:79], v[28:31], a[236:239]\n"
        "ds_read_b64_tr_b16 v[74:75], v221 offset:8192\n"
        "global_load_dwordx2 v[128:129], v169, s[74:75] offset:-2048\n"
        "v_mfma_f32_16x16x32_bf16 a[204:207], v[76:79], v[20:23], a[204:207]\n"
        "ds_read_b64_tr_b16 v[76:77], v222 offset:8192\n"
        "global_load_lds_dwordx4 v234, s[58:59] offset:0\n"
        "v_mfma_f32_16x16x32_bf16 a[204:207], v[44:47], v[16:19], a[204:207]\n"
        "ds_read_b64_tr_b16 v[44:45], v222 offset:0\n"
        "v_mfma_f32_16x16x32_bf16 a[208:211], v[16:19], v[48:51], a[208:211]\n"
        "ds_read_b64_tr_b16 v[46:47], v223 offset:0\n"
        "v_mfma_f32_16x16x32_bf16 a[208:211], v[20:23], v[80:83], a[208:211]\n"
        "ds_read_b64_tr_b16 v[78:79], v223 offset:8192\n"
        "v_mfma_f32_16x16x32_bf16 a[240:243], v[28:31], v[80:83], a[240:243]\n"
        "ds_read_b64_tr_b16 v[80:81], v216 offset:24576\n"
        "global_load_lds_dwordx4 v235, s[58:59] offset:1024\n"
        "v_mfma_f32_16x16x32_bf16 a[240:243], v[24:27], v[48:51], a[240:243]\n"
        "ds_read_b64_tr_b16 v[48:49], v216 offset:16384\n"
        "v_lshlrev_b32 v185, 16, v134\n"
        "v_mfma_f32_16x16x32_bf16 a[244:247], v[24:27], v[52:55], a[244:247]\n"
        "ds_read_b64_tr_b16 v[50:51], v217 offset:16384\n"
        "v_and_b32 v186, 0xffff0000, v134\n"
        "v_mfma_f32_16x16x32_bf16 a[244:247], v[28:31], v[84:87], a[244:247]\n"
        "ds_read_b64_tr_b16 v[82:83], v217 offset:24576\n"
        "v_lshlrev_b32 v187, 16, v135\n"
        "v_mfma_f32_16x16x32_bf16 a[212:215], v[20:23], v[84:87], a[212:215]\n"
        "ds_read_b64_tr_b16 v[84:85], v218 offset:24576\n"
        "global_load_dwordx2 v[130:131], v169, s[74:75] offset:0\n"
        "v_mfma_f32_16x16x32_bf16 a[212:215], v[16:19], v[52:55], a[212:215]\n"
        "ds_read_b64_tr_b16 v[52:53], v218 offset:16384\n"
        "global_load_lds_dwordx4 v236, s[58:59] offset:2048\n"
        "v_mfma_f32_16x16x32_bf16 a[216:219], v[16:19], v[56:59], a[216:219]\n"
        "ds_read_b64_tr_b16 v[54:55], v219 offset:16384\n"
        "v_and_b32 v188, 0xffff0000, v135\n"
        "v_mfma_f32_16x16x32_bf16 a[216:219], v[20:23], v[88:91], a[216:219]\n"
        "ds_read_b64_tr_b16 v[86:87], v219 offset:24576\n"
        "s_waitcnt lgkmcnt(8)\n"
        "v_mfma_f32_16x16x32_bf16 a[248:251], v[28:31], v[88:91], a[248:251]\n"
        "ds_read_b64_tr_b16 v[88:89], v220 offset:24576\n"
        "v_mfma_f32_16x16x32_bf16 a[248:251], v[24:27], v[56:59], a[248:251]\n"
        "ds_read_b64_tr_b16 v[56:57], v220 offset:16384\n"
        "global_load_lds_dwordx4 v237, s[58:59] offset:3072\n"
        "s_add_u32 m0, s73, 0x18000\n"
        "v_mfma_f32_16x16x32_bf16 a[252:255], v[24:27], v[60:63], a[252:255]\n"
        "ds_read_b64_tr_b16 v[58:59], v221 offset:16384\n"
        "s_add_u32 s58, s58, 0x80\n"
        "s_addc_u32 s59, s59, 0\n"
        "v_mfma_f32_16x16x32_bf16 a[252:255], v[28:31], v[92:95], a[252:255]\n"
        "LHA1_%=:\n"
        "s_cbranch_execz LHA5_%=\n"
        "ds_read_b64_tr_b16 v[90:91], v221 offset:24576\n"
        "v_mfma_f32_16x16x32_bf16 a[220:223], v[20:23], v[92:95], a[220:223]\n"
        "ds_read_b64_tr_b16 v[92:93], v222 offset:24576\n"
        "v_mfma_f32_16x16x32_bf16 a[220:223], v[16:19], v[60:63], a[220:223]\n"
        "ds_read_b64_tr_b16 v[60:61], v222 offset:16384\n"
        "global_load_dwordx2 v[132:133], v169, s[74:75] offset:2048\n"
        "v_mfma_f32_16x16x32_bf16 a[0:3], v[32:35], v[0:3], a[0:3]\n"
        "ds_read_b64_tr_b16 v[62:63], v223 offset:16384\n"
        "global_load_lds_dwordx4 v238, s[60:61] offset:0\n"
        "v_mfma_f32_16x16x32_bf16 a[0:3], v[64:67], v[4:7], a[0:3]\n"
        "ds_read_b64_tr_b16 v[94:95], v223 offset:24576\n"
        "v_mfma_f32_16x16x32_bf16 a[32:35], v[64:67], v[12:15], a[32:35]\n"
        "ds_read_b128 v[16:19], v224 offset:4096\n"
        "v_mfma_f32_16x16x32_bf16 a[32:35], v[32:35], v[8:11], a[32:35]\n"
        "ds_read_b128 v[20:23], v225 offset:4096\n"
        "v_mfma_f32_16x16x32_bf16 a[36:39], v[36:39], v[8:11], a[36:39]\n"
        "ds_read_b128 v[24:27], v224 offset:6144\n"
        "global_load_lds_dwordx4 v239, s[60:61] offset:1024\n"
        "v_mfma_f32_16x16x32_bf16 a[36:39], v[68:71], v[12:15], a[36:39]\n"
        "ds_read_b128 v[28:31], v225 offset:6144\n"
        "s_waitcnt lgkmcnt(8)\n"
        "v_mfma_f32_16x16x32_bf16 a[4:7], v[68:71], v[4:7], a[4:7]\n"
        "v_mfma_f32_16x16x32_bf16 a[4:7], v[36:39], v[0:3], a[4:7]\n"
        "v_mfma_f32_16x16x32_bf16 a[8:11], v[40:43], v[0:3], a[8:11]\n"
        "global_load_lds_dwordx4 v240, s[60:61] offset:2048\n"
        "v_mfma_f32_16x16x32_bf16 a[8:11], v[72:75], v[4:7], a[8:11]\n"
        "v_mfma_f32_16x16x32_bf16 a[40:43], v[72:75], v[12:15], a[40:43]\n"
        "v_mfma_f32_16x16x32_bf16 a[40:43], v[40:43], v[8:11], a[40:43]\n"
        "v_mfma_f32_16x16x32_bf16 a[44:47], v[44:47], v[8:11], a[44:47]\n"
        "global_load_lds_dwordx4 v241, s[60:61] offset:3072\n"
        "s_add_u32 m0, s73, 0x1c000\n"
        "v_mfma_f32_16x16x32_bf16 a[44:47], v[76:79], v[12:15], a[44:47]\n"
        "s_add_u32 s60, s60, 0x80000\n"
        "s_addc_u32 s61, s61, 0\n"
        "v_cndmask_b32 v96, v96, 0, s[78:79]\n"
        "v_mfma_f32_16x16x32_bf16 a[12:15], v[76:79], v[4:7], a[12:15]\n"
        "v_cndmask_b32 v97, v97, 0, s[78:79]\n"
        "v_lshlrev_b32 v174, 16, v96\n"
        "v_mfma_f32_16x16x32_bf16 a[12:15], v[44:47], v[0:3], a[12:15]\n"
        "v_mfma_f32_16x16x32_bf16 a[16:19], v[0:3], v[48:51], a[16:19]\n"
        "global_load_lds_dwordx4 v238, s[62:63] offset:0\n"
        "v_mfma_f32_16x16x32_bf16 a[16:19], v[4:7], v[80:83], a[16:19]\n"
        "s_waitcnt lgkmcnt(0)\n"
        "v_mfma_f32_16x16x32_bf16 a[48:51], v[12:15], v[80:83], a[48:51]\n"
        "v_mfma_f32_16x16x32_bf16 a[48:51], v[8:11], v[48:51], a[48:51]\n"
        "v_mfma_f32_16x16x32_bf16 a[52:55], v[8:11], v[52:55], a[52:55]\n"
        "global_load_lds_dwordx4 v239, s[62:63] offset:1024\n"
        "v_mfma_f32_16x16x32_bf16 a[52:55], v[12:15], v[84:87], a[52:55]\n"
        "v_cndmask_b32 v98, v98, 0, s[78:79]\n"
        "v_mfma_f32_16x16x32_bf16 a[20:23], v[4:7], v[84:87], a[20:23]\n"
        "v_cndmask_b32 v99, v99, 0, s[78:79]\n"
        "v_lshlrev_b32 v175, 16, v98\n"
        "v_mfma_f32_16x16x32_bf16 a[20:23], v[0:3], v[52:55], a[20:23]\n"
        "v_mul_f32 v189, v174, v188\n"
        "v_mul_f32 v191, v175, v188\n"
        "v_mfma_f32_16x16x32_bf16 a[24:27], v[0:3], v[56:59], a[24:27]\n"
        "global_load_lds_dwordx4 v240, s[62:63] offset:2048\n"
        "v_mfma_f32_16x16x32_bf16 a[24:27], v[4:7], v[88:91], a[24:27]\n"
        "v_cvt_pk_bf16_f32 v189, 0, v189\n"
        "v_cvt_pk_bf16_f32 v191, 0, v191\n"
        "v_mfma_f32_16x16x32_bf16 a[56:59], v[12:15], v[88:91], a[56:59]\n"
        "v_mul_f32 v190, v175, v187\n"
        "v_cvt_pk_bf16_f32 v190, 0, v190\n"
        "v_mfma_f32_16x16x32_bf16 a[56:59], v[8:11], v[56:59], a[56:59]\n"
        "v_add_f32 v189, v189, v190\n"
        "v_cvt_pk_bf16_f32 v189, 0, v189\n"
        "v_mfma_f32_16x16x32_bf16 a[60:63], v[8:11], v[60:63], a[60:63]\n"
        "global_load_lds_dwordx4 v241, s[62:63] offset:3072\n"
        "s_add_u32 m0, s72, 0x0\n"
        "v_mfma_f32_16x16x32_bf16 a[60:63], v[12:15], v[92:95], a[60:63]\n"
        "ds_read_b128 v[8:11], v224 offset:10240\n"
        "s_add_u32 s62, s62, 0x80000\n"
        "s_addc_u32 s63, s63, 0\n"
        "v_mfma_f32_16x16x32_bf16 a[28:31], v[4:7], v[92:95], a[28:31]\n"
        "ds_read_b128 v[4:7], v225 offset:8192\n"
        "v_and_b32 v174, 0xffff0000, v96\n"
        "v_mfma_f32_16x16x32_bf16 a[28:31], v[0:3], v[60:63], a[28:31]\n"
        "ds_read_b128 v[0:3], v224 offset:8192\n"
        "v_and_b32 v175, 0xffff0000, v98\n"
        "v_mfma_f32_16x16x32_bf16 a[64:67], v[32:35], v[16:19], a[64:67]\n"
        "ds_read_b128 v[12:15], v225 offset:10240\n"
        "s_waitcnt vmcnt(40)\n"
        "v_mfma_f32_16x16x32_bf16 a[64:67], v[64:67], v[20:23], a[64:67]\n"
        "v_cndmask_b32 v100, v100, 0, s[78:79]\n"
        "v_cndmask_b32 v101, v101, 0, s[78:79]\n"
        "v_mfma_f32_16x16x32_bf16 a[96:99], v[64:67], v[28:31], a[96:99]\n"
        "v_lshlrev_b32 v176, 16, v100\n"
        "v_mul_f32 v192, v176, v187\n"
        "v_mfma_f32_16x16x32_bf16 a[96:99], v[32:35], v[24:27], a[96:99]\n"
        "v_cvt_pk_bf16_f32 v192, 0, v192\n"
        "v_add_f32 v191, v191, v192\n"
        "v_mfma_f32_16x16x32_bf16 a[100:103], v[36:39], v[24:27], a[100:103]\n"
        "v_cvt_pk_bf16_f32 v191, 0, v191\n"
        "v_mul_f32 v190, v176, v186\n"
        "v_mfma_f32_16x16x32_bf16 a[100:103], v[68:71], v[28:31], a[100:103]\n"
        "v_cvt_pk_bf16_f32 v190, 0, v190\n"
        "v_add_f32 v189, v189, v190\n"
        "v_mfma_f32_16x16x32_bf16 a[68:71], v[68:71], v[20:23], a[68:71]\n"
        "v_cvt_pk_bf16_f32 v189, 0, v189\n"
        "v_mul_f32 v193, v176, v188\n"
        "v_mfma_f32_16x16x32_bf16 a[68:71], v[36:39], v[16:19], a[68:71]\n"
        "global_load_dword v253, v173, s[82:83] offset:0\n"
        "s_add_u32 s82, s12, 0x100000\n"
        "s_addc_u32 s83, s13, 0\n"
        "v_mfma_f32_16x16x32_bf16 a[72:75], v[40:43], v[16:19], a[72:75]\n"
        "s_waitcnt vmcnt(38) lgkmcnt(0)\n"
        "v_lshlrev_b32 v177, 16, v102\n"
        "v_mul_f32 v192, v177, v186\n"
        "v_mfma_f32_16x16x32_bf16 a[72:75], v[72:75], v[20:23], a[72:75]\n"
        "v_cvt_pk_bf16_f32 v192, 0, v192\n"
        "v_add_f32 v191, v191, v192\n"
        "v_mfma_f32_16x16x32_bf16 a[104:107], v[72:75], v[28:31], a[104:107]\n"
        "v_cvt_pk_bf16_f32 v191, 0, v191\n"
        "v_mul_f32 v190, v177, v185\n"
        "v_mfma_f32_16x16x32_bf16 a[104:107], v[40:43], v[24:27], a[104:107]\n"
        "v_cvt_pk_bf16_f32 v190, 0, v190\n"
        "v_add_f32 v189, v189, v190\n"
        "v_mfma_f32_16x16x32_bf16 a[108:111], v[44:47], v[24:27], a[108:111]\n"
        "v_cvt_pk_bf16_f32 v189, 0, v189\n"
        "v_mul_f32 v195, v177, v188\n"
        "v_mfma_f32_16x16x32_bf16 a[108:111], v[76:79], v[28:31], a[108:111]\n"
        "v_cvt_pk_bf16_f32 v193, 0, v193\n"
        "v_cvt_pk_bf16_f32 v195, 0, v195\n"
        "v_mfma_f32_16x16x32_bf16 a[76:79], v[76:79], v[20:23], a[76:79]\n"
        "v_mul_f32 v194, v177, v187\n"
        "v_cvt_pk_bf16_f32 v194, 0, v194\n"
        "v_mfma_f32_16x16x32_bf16 a[76:79], v[44:47], v[16:19], a[76:79]\n"
        "v_add_f32 v193, v193, v194\n"
        "v_cvt_pk_bf16_f32 v193, 0, v193\n"
        "v_mfma_f32_16x16x32_bf16 a[80:83], v[16:19], v[48:51], a[80:83]\n"
        "s_waitcnt vmcnt(35)\n"
        "v_lshlrev_b32 v178, 16, v104\n"
        "v_mul_f32 v192, v178, v185\n"
        "v_mfma_f32_16x16x32_bf16 a[80:83], v[20:23], v[80:83], a[80:83]\n"
        "v_cvt_pk_bf16_f32 v192, 0, v192\n"
        "v_add_f32 v191, v191, v192\n"
        "v_mfma_f32_16x16x32_bf16 a[112:115], v[28:31], v[80:83], a[112:115]\n"
        "v_cvt_pk_bf16_f32 v191, 0, v191\n"
        "v_mul_f32 v196, v178, v187\n"
        "v_mfma_f32_16x16x32_bf16 a[112:115], v[24:27], v[48:51], a[112:115]\n"
        "v_cvt_pk_bf16_f32 v196, 0, v196\n"
        "v_add_f32 v195, v195, v196\n"
        "v_mfma_f32_16x16x32_bf16 a[116:119], v[24:27], v[52:55], a[116:119]\n"
        "v_cvt_pk_bf16_f32 v195, 0, v195\n"
        "v_mul_f32 v194, v178, v186\n"
        "v_mfma_f32_16x16x32_bf16 a[116:119], v[28:31], v[84:87], a[116:119]\n"
        "v_cvt_pk_bf16_f32 v194, 0, v194\n"
        "v_add_f32 v193, v193, v194\n"
        "v_mfma_f32_16x16x32_bf16 a[84:87], v[20:23], v[84:87], a[84:87]\n"
        "v_cvt_pk_bf16_f32 v193, 0, v193\n"
        "v_and_b32 v176, 0xffff0000, v100\n"
        "v_mfma_f32_16x16x32_bf16 a[84:87], v[16:19], v[52:55], a[84:87]\n"
        "v_and_b32 v177, 0xffff0000, v102\n"
        "v_mfma_f32_16x16x32_bf16 a[88:91], v[16:19], v[56:59], a[88:91]\n"
        "v_mfma_f32_16x16x32_bf16 a[88:91], v[20:23], v[88:91], a[88:91]\n"
        "s_waitcnt vmcnt(32)\n"
        "v_lshlrev_b32 v179, 16, v106\n"
        "v_mul_f32 v196, v179, v186\n"
        "v_mfma_f32_16x16x32_bf16 a[120:123], v[28:31], v[88:91], a[120:123]\n"
        "v_cvt_pk_bf16_f32 v196, 0, v196\n"
        "v_add_f32 v195, v195, v196\n"
        "v_mfma_f32_16x16x32_bf16 a[120:123], v[24:27], v[56:59], a[120:123]\n"
        "v_cvt_pk_bf16_f32 v195, 0, v195\n"
        "v_mul_f32 v194, v179, v185\n"
        "v_mfma_f32_16x16x32_bf16 a[124:127], v[24:27], v[60:63], a[124:127]\n"
        "ds_read_b128 v[24:27], v224 offset:14336\n"
        "v_cvt_pk_bf16_f32 v194, 0, v194\n"
        "v_mfma_f32_16x16x32_bf16 a[124:127], v[28:31], v[92:95], a[124:127]\n"
        "ds_read_b128 v[28:31], v225 offset:14336\n"
        "v_add_f32 v193, v193, v194\n"
        "v_mfma_f32_16x16x32_bf16 a[92:95], v[20:23], v[92:95], a[92:95]\n"
        "ds_read_b128 v[20:23], v225 offset:12288\n"
        "v_cvt_pk_bf16_f32 v193, 0, v193\n"
        "v_mfma_f32_16x16x32_bf16 a[92:95], v[16:19], v[60:63], a[92:95]\n"
        "ds_read_b128 v[16:19], v224 offset:12288\n"
        "v_mfma_f32_16x16x32_bf16 a[128:131], v[32:35], v[0:3], a[128:131]\n"
        "v_mfma_f32_16x16x32_bf16 a[128:131], v[64:67], v[4:7], a[128:131]\n"
        "v_mfma_f32_16x16x32_bf16 a[160:163], v[64:67], v[12:15], a[160:163]\n"
        "s_waitcnt vmcnt(29)\n"
        "v_lshlrev_b32 v180, 16, v108\n"
        "v_mul_f32 v196, v180, v185\n"
        "v_mfma_f32_16x16x32_bf16 a[160:163], v[32:35], v[8:11], a[160:163]\n"
        "s_waitcnt vmcnt(28)\n"
        "v_lshlrev_b32 v181, 16, v110\n"
        "v_cvt_pk_bf16_f32 v196, 0, v196\n"
        "v_mfma_f32_16x16x32_bf16 a[164:167], v[36:39], v[8:11], a[164:167]\n"
        "s_waitcnt vmcnt(27) lgkmcnt(3)\n"
        "v_lshlrev_b32 v182, 16, v112\n"
        "v_add_f32 v195, v195, v196\n"
        "v_mfma_f32_16x16x32_bf16 a[164:167], v[68:71], v[12:15], a[164:167]\n"
        "v_cvt_pk_bf16_f32 v195, 0, v195\n"
        "v_mfma_f32_16x16x32_bf16 a[132:135], v[68:71], v[4:7], a[132:135]\n"
        "v_mfma_f32_16x16x32_bf16 a[132:135], v[36:39], v[0:3], a[132:135]\n"
        "v_mfma_f32_16x16x32_bf16 a[136:139], v[40:43], v[0:3], a[136:139]\n"
        "v_mfma_f32_16x16x32_bf16 a[136:139], v[72:75], v[4:7], a[136:139]\n"
        "v_mfma_f32_16x16x32_bf16 a[168:171], v[72:75], v[12:15], a[168:171]\n"
        "v_mfma_f32_16x16x32_bf16 a[168:171], v[40:43], v[8:11], a[168:171]\n"
        "s_waitcnt vmcnt(26) lgkmcnt(0)\n"
        "v_lshlrev_b32 v183, 16, v114\n"
        "v_mfma_f32_16x16x32_bf16 a[172:175], v[44:47], v[8:11], a[172:175]\n"
        "v_mfma_f32_16x16x32_bf16 a[172:175], v[76:79], v[12:15], a[172:175]\n"
        "v_mfma_f32_16x16x32_bf16 a[140:143], v[76:79], v[4:7], a[140:143]\n"
        "v_mfma_f32_16x16x32_bf16 a[140:143], v[44:47], v[0:3], a[140:143]\n"
        "v_mfma_f32_16x16x32_bf16 a[144:147], v[0:3], v[48:51], a[144:147]\n"
        "v_mfma_f32_16x16x32_bf16 a[144:147], v[4:7], v[80:83], a[144:147]\n"
        "v_mfma_f32_16x16x32_bf16 a[176:179], v[12:15], v[80:83], a[176:179]\n"
        "v_mfma_f32_16x16x32_bf16 a[176:179], v[8:11], v[48:51], a[176:179]\n"
        "s_waitcnt vmcnt(25)\n"
        "v_lshlrev_b32 v184, 16, v116\n"
        "v_mfma_f32_16x16x32_bf16 a[180:183], v[8:11], v[52:55], a[180:183]\n"
        "v_mfma_f32_16x16x32_bf16 a[180:183], v[12:15], v[84:87], a[180:183]\n"
        "v_mfma_f32_16x16x32_bf16 a[148:151], v[4:7], v[84:87], a[148:151]\n"
        "v_mfma_f32_16x16x32_bf16 a[148:151], v[0:3], v[52:55], a[148:151]\n"
        "v_mfma_f32_16x16x32_bf16 a[152:155], v[0:3], v[56:59], a[152:155]\n"
        "v_mfma_f32_16x16x32_bf16 a[152:155], v[4:7], v[88:91], a[152:155]\n"
        "v_mfma_f32_16x16x32_bf16 a[184:187], v[12:15], v[88:91], a[184:187]\n"
        "v_mfma_f32_16x16x32_bf16 a[184:187], v[8:11], v[56:59], a[184:187]\n"
        "s_waitcnt vmcnt(10)\n"
        "s_barrier\n"
        "v_lshlrev_b32 v190, 16, v118\n"
        "v_mfma_f32_16x16x32_bf16 a[188:191], v[8:11], v[60:63], a[188:191]\n"
        "global_load_lds_dwordx4 v234, s[56:57] offset:0\n"
        "ds_read_b128 v[8:11], v224 offset:34816\n"
        "v_mfma_f32_16x16x32_bf16 a[188:191], v[12:15], v[92:95], a[188:191]\n"
        "ds_read_b128 v[12:15], v225 offset:34816\n"
        "v_mul_f32 v189, v189, v190\n"
        "v_mfma_f32_16x16x32_bf16 a[156:159], v[4:7], v[92:95], a[156:159]\n"
        "ds_read_b128 v[4:7], v225 offset:32768\n"
        "v_mul_f32 v190, v179, v187\n"
        "v_mfma_f32_16x16x32_bf16 a[156:159], v[0:3], v[60:63], a[156:159]\n"
        "ds_read_b128 v[0:3], v224 offset:32768\n"
        "v_cvt_pk_bf16_f32 v190, 0, v190\n"
        "v_mfma_f32_16x16x32_bf16 a[192:195], v[32:35], v[16:19], a[192:195]\n"
        "global_load_lds_dwordx4 v235, s[56:57] offset:1024\n"
        "v_mfma_f32_16x16x32_bf16 a[192:195], v[64:67], v[20:23], a[192:195]\n"
        "v_mfma_f32_16x16x32_bf16 a[224:227], v[64:67], v[28:31], a[224:227]\n"
        "ds_read_b64_tr_b16 v[64:65], v226 offset:8192\n"
        "v_mfma_f32_16x16x32_bf16 a[224:227], v[32:35], v[24:27], a[224:227]\n"
        "ds_read_b64_tr_b16 v[32:33], v226 offset:0\n"
        "v_lshlrev_b32 v192, 16, v120\n"
        "v_mfma_f32_16x16x32_bf16 a[228:231], v[36:39], v[24:27], a[228:231]\n"
        "ds_read_b64_tr_b16 v[34:35], v227 offset:0\n"
        "global_load_lds_dwordx4 v236, s[56:57] offset:2048\n"
        "v_mfma_f32_16x16x32_bf16 a[228:231], v[68:71], v[28:31], a[228:231]\n"
        "ds_read_b64_tr_b16 v[66:67], v227 offset:8192\n"
        "v_mul_f32 v191, v191, v192\n"
        "v_mfma_f32_16x16x32_bf16 a[196:199], v[68:71], v[20:23], a[196:199]\n"
        "ds_read_b64_tr_b16 v[68:69], v228 offset:8192\n"
        "v_cvt_pk_bf16_f32 v203, v191, v189\n"
        "v_mfma_f32_16x16x32_bf16 a[196:199], v[36:39], v[16:19], a[196:199]\n"
        "ds_read_b64_tr_b16 v[36:37], v228 offset:0\n"
        "v_mul_f32 v189, v178, v188\n"
        "v_mfma_f32_16x16x32_bf16 a[200:203], v[40:43], v[16:19], a[200:203]\n"
        "ds_read_b64_tr_b16 v[38:39], v229 offset:0\n"
        "global_load_lds_dwordx4 v237, s[56:57] offset:3072\n"
        "s_add_u32 m0, s72, 0x1000\n"
        "v_mfma_f32_16x16x32_bf16 a[200:203], v[72:75], v[20:23], a[200:203]\n"
        "ds_read_b64_tr_b16 v[70:71], v229 offset:8192\n"
        "s_add_u32 s56, s56, 0x80\n"
        "s_addc_u32 s57, s57, 0\n"
        "v_mfma_f32_16x16x32_bf16 a[232:235], v[72:75], v[28:31], a[232:235]\n"
        "ds_read_b64_tr_b16 v[72:73], v230 offset:8192\n"
        "v_mul_f32 v191, v179, v188\n"
        "v_mfma_f32_16x16x32_bf16 a[232:235], v[40:43], v[24:27], a[232:235]\n"
        "ds_read_b64_tr_b16 v[40:41], v230 offset:0\n"
        "v_lshlrev_b32 v194, 16, v122\n"
        "v_mfma_f32_16x16x32_bf16 a[236:239], v[44:47], v[24:27], a[236:239]\n"
        "ds_read_b64_tr_b16 v[42:43], v231 offset:0\n"
        "global_load_lds_dwordx4 v234, s[58:59] offset:0\n"
        "v_mfma_f32_16x16x32_bf16 a[236:239], v[76:79], v[28:31], a[236:239]\n"
        "ds_read_b64_tr_b16 v[74:75], v231 offset:8192\n"
        "v_mul_f32 v193, v193, v194\n"
        "v_mfma_f32_16x16x32_bf16 a[204:207], v[76:79], v[20:23], a[204:207]\n"
        "ds_read_b64_tr_b16 v[76:77], v232 offset:8192\n"
        "v_cvt_pk_bf16_f32 v189, 0, v189\n"
        "v_mfma_f32_16x16x32_bf16 a[204:207], v[44:47], v[16:19], a[204:207]\n"
        "ds_read_b64_tr_b16 v[44:45], v232 offset:0\n"
        "v_cvt_pk_bf16_f32 v191, 0, v191\n"
        "v_mfma_f32_16x16x32_bf16 a[208:211], v[16:19], v[48:51], a[208:211]\n"
        "ds_read_b64_tr_b16 v[46:47], v233 offset:0\n"
        "global_load_lds_dwordx4 v235, s[58:59] offset:1024\n"
        "v_mfma_f32_16x16x32_bf16 a[208:211], v[20:23], v[80:83], a[208:211]\n"
        "ds_read_b64_tr_b16 v[78:79], v233 offset:8192\n"
        "v_mul_f32 v192, v180, v187\n"
        "v_mfma_f32_16x16x32_bf16 a[240:243], v[28:31], v[80:83], a[240:243]\n"
        "ds_read_b64_tr_b16 v[80:81], v226 offset:24576\n"
        "v_cvt_pk_bf16_f32 v192, 0, v192\n"
        "v_mfma_f32_16x16x32_bf16 a[240:243], v[24:27], v[48:51], a[240:243]\n"
        "ds_read_b64_tr_b16 v[48:49], v226 offset:16384\n"
        "v_lshlrev_b32 v196, 16, v124\n"
        "v_mfma_f32_16x16x32_bf16 a[244:247], v[24:27], v[52:55], a[244:247]\n"
        "ds_read_b64_tr_b16 v[50:51], v227 offset:16384\n"
        "global_load_lds_dwordx4 v236, s[58:59] offset:2048\n"
        "v_mfma_f32_16x16x32_bf16 a[244:247], v[28:31], v[84:87], a[244:247]\n"
        "ds_read_b64_tr_b16 v[82:83], v227 offset:24576\n"
        "v_mul_f32 v195, v195, v196\n"
        "v_mfma_f32_16x16x32_bf16 a[212:215], v[20:23], v[84:87], a[212:215]\n"
        "ds_read_b64_tr_b16 v[84:85], v228 offset:24576\n"
        "v_cvt_pk_bf16_f32 v202, v195, v193\n"
        "v_mfma_f32_16x16x32_bf16 a[212:215], v[16:19], v[52:55], a[212:215]\n"
        "ds_read_b64_tr_b16 v[52:53], v228 offset:16384\n"
        "v_add_f32 v189, v189, v190\n"
        "v_mfma_f32_16x16x32_bf16 a[216:219], v[16:19], v[56:59], a[216:219]\n"
        "ds_read_b64_tr_b16 v[54:55], v229 offset:16384\n"
        "global_load_dword v253, v173, s[82:83] offset:0\n"
        "v_mfma_f32_16x16x32_bf16 a[216:219], v[20:23], v[88:91], a[216:219]\n"
        "ds_read_b64_tr_b16 v[86:87], v229 offset:24576\n"
        "global_load_lds_dwordx4 v237, s[58:59] offset:3072\n"
        "s_add_u32 m0, s73, 0x20000\n"
        "v_mfma_f32_16x16x32_bf16 a[248:251], v[28:31], v[88:91], a[248:251]\n"
        "ds_read_b64_tr_b16 v[88:89], v230 offset:24576\n"
        "s_add_u32 s58, s58, 0x80\n"
        "s_addc_u32 s59, s59, 0\n"
        "v_mfma_f32_16x16x32_bf16 a[248:251], v[24:27], v[56:59], a[248:251]\n"
        "ds_read_b64_tr_b16 v[56:57], v230 offset:16384\n"
        "s_add_u32 s82, s12, 0x200000\n"
        "s_addc_u32 s83, s13, 0\n"
        "v_mfma_f32_16x16x32_bf16 a[252:255], v[24:27], v[60:63], a[252:255]\n"
        "ds_read_b64_tr_b16 v[58:59], v231 offset:16384\n"
        "v_add_f32 v191, v191, v192\n"
        "v_mfma_f32_16x16x32_bf16 a[252:255], v[28:31], v[92:95], a[252:255]\n"
        "ds_read_b64_tr_b16 v[90:91], v231 offset:24576\n"
        "global_load_lds_dwordx4 v238, s[60:61] offset:0\n"
        "v_mfma_f32_16x16x32_bf16 a[220:223], v[20:23], v[92:95], a[220:223]\n"
        "ds_read_b64_tr_b16 v[92:93], v232 offset:24576\n"
        "v_cvt_pk_bf16_f32 v189, 0, v189\n"
        "v_mfma_f32_16x16x32_bf16 a[220:223], v[16:19], v[60:63], a[220:223]\n"
        "ds_read_b64_tr_b16 v[60:61], v232 offset:16384\n"
        "s_waitcnt lgkmcnt(8)\n"
        "v_mfma_f32_16x16x32_bf16 a[0:3], v[32:35], v[0:3], a[0:3]\n"
        "ds_read_b64_tr_b16 v[62:63], v233 offset:16384\n"
        "v_cvt_pk_bf16_f32 v191, 0, v191\n"
        "v_mfma_f32_16x16x32_bf16 a[0:3], v[64:67], v[4:7], a[0:3]\n"
        "ds_read_b64_tr_b16 v[94:95], v233 offset:24576\n"
        "global_load_lds_dwordx4 v239, s[60:61] offset:1024\n"
        "v_mfma_f32_16x16x32_bf16 a[32:35], v[64:67], v[12:15], a[32:35]\n"
        "ds_read_b128 v[16:19], v224 offset:36864\n"
        "v_mul_f32 v190, v180, v186\n"
        "v_mfma_f32_16x16x32_bf16 a[32:35], v[32:35], v[8:11], a[32:35]\n"
        "ds_read_b128 v[20:23], v225 offset:36864\n"
        "v_mul_f32 v192, v181, v186\n"
        "v_mfma_f32_16x16x32_bf16 a[36:39], v[36:39], v[8:11], a[36:39]\n"
        "ds_read_b128 v[24:27], v224 offset:38912\n"
        "v_cvt_pk_bf16_f32 v190, 0, v190\n"
        "v_mfma_f32_16x16x32_bf16 a[36:39], v[68:71], v[12:15], a[36:39]\n"
        "ds_read_b128 v[28:31], v225 offset:38912\n"
        "global_load_lds_dwordx4 v240, s[60:61] offset:2048\n"
        "v_mfma_f32_16x16x32_bf16 a[4:7], v[68:71], v[4:7], a[4:7]\n"
        "v_cvt_pk_bf16_f32 v192, 0, v192\n"
        "v_add_f32 v189, v189, v190\n"
        "v_mfma_f32_16x16x32_bf16 a[4:7], v[36:39], v[0:3], a[4:7]\n"
        "v_add_f32 v191, v191, v192\n"
        "v_cvt_pk_bf16_f32 v189, 0, v189\n"
        "v_mfma_f32_16x16x32_bf16 a[8:11], v[40:43], v[0:3], a[8:11]\n"
        "v_cvt_pk_bf16_f32 v191, 0, v191\n"
        "v_mul_f32 v190, v181, v185\n"
        "v_mfma_f32_16x16x32_bf16 a[8:11], v[72:75], v[4:7], a[8:11]\n"
        "global_load_lds_dwordx4 v241, s[60:61] offset:3072\n"
        "s_add_u32 m0, s73, 0x24000\n"
        "v_mfma_f32_16x16x32_bf16 a[40:43], v[72:75], v[12:15], a[40:43]\n"
        "s_add_u32 s60, s60, 0x80000\n"
        "s_addc_u32 s61, s61, 0\n"
        "v_mul_f32 v192, v182, v185\n"
        "v_mfma_f32_16x16x32_bf16 a[40:43], v[40:43], v[8:11], a[40:43]\n"
        "s_waitcnt lgkmcnt(2)\n"
        "v_cvt_pk_bf16_f32 v190, 0, v190\n"
        "v_cvt_pk_bf16_f32 v192, 0, v192\n"
        "v_mfma_f32_16x16x32_bf16 a[44:47], v[44:47], v[8:11], a[44:47]\n"
        "v_add_f32 v189, v189, v190\n"
        "v_add_f32 v191, v191, v192\n"
        "v_mfma_f32_16x16x32_bf16 a[44:47], v[76:79], v[12:15], a[44:47]\n"
        "global_load_lds_dwordx4 v238, s[62:63] offset:0\n"
        "v_mfma_f32_16x16x32_bf16 a[12:15], v[76:79], v[4:7], a[12:15]\n"
        "v_cvt_pk_bf16_f32 v189, 0, v189\n"
        "v_cvt_pk_bf16_f32 v191, 0, v191\n"
        "v_mfma_f32_16x16x32_bf16 a[12:15], v[44:47], v[0:3], a[12:15]\n"
        "v_lshlrev_b32 v190, 16, v126\n"
        "v_lshlrev_b32 v192, 16, v128\n"
        "v_mfma_f32_16x16x32_bf16 a[16:19], v[0:3], v[48:51], a[16:19]\n"
        "v_mul_f32 v189, v189, v190\n"
        "v_mul_f32 v191, v191, v192\n"
        "v_mfma_f32_16x16x32_bf16 a[16:19], v[4:7], v[80:83], a[16:19]\n"
        "global_load_lds_dwordx4 v239, s[62:63] offset:1024\n"
        "v_mfma_f32_16x16x32_bf16 a[48:51], v[12:15], v[80:83], a[48:51]\n"
        "v_cvt_pk_bf16_f32 v201, v191, v189\n"
        "v_mul_f32 v193, v180, v188\n"
        "v_mfma_f32_16x16x32_bf16 a[48:51], v[8:11], v[48:51], a[48:51]\n"
        "v_mul_f32 v195, v181, v188\n"
        "v_cvt_pk_bf16_f32 v193, 0, v193\n"
        "v_mfma_f32_16x16x32_bf16 a[52:55], v[8:11], v[52:55], a[52:55]\n"
        "v_cvt_pk_bf16_f32 v195, 0, v195\n"
        "v_mul_f32 v194, v181, v187\n"
        "v_mfma_f32_16x16x32_bf16 a[52:55], v[12:15], v[84:87], a[52:55]\n"
        "global_load_lds_dwordx4 v240, s[62:63] offset:2048\n"
        "v_mfma_f32_16x16x32_bf16 a[20:23], v[4:7], v[84:87], a[20:23]\n"
        "v_mul_f32 v196, v182, v187\n"
        "v_cvt_pk_bf16_f32 v194, 0, v194\n"
        "v_mfma_f32_16x16x32_bf16 a[20:23], v[0:3], v[52:55], a[20:23]\n"
        "s_waitcnt lgkmcnt(0)\n"
        "v_cvt_pk_bf16_f32 v196, 0, v196\n"
        "v_add_f32 v193, v193, v194\n"
        "v_mfma_f32_16x16x32_bf16 a[24:27], v[0:3], v[56:59], a[24:27]\n"
        "v_add_f32 v195, v195, v196\n"
        "v_cvt_pk_bf16_f32 v193, 0, v193\n"
        "v_mfma_f32_16x16x32_bf16 a[24:27], v[4:7], v[88:91], a[24:27]\n"
        "global_load_lds_dwordx4 v241, s[62:63] offset:3072\n"
        "s_add_u32 m0, s72, 0x8000\n"
        "v_mfma_f32_16x16x32_bf16 a[56:59], v[12:15], v[88:91], a[56:59]\n"
        "s_add_u32 s62, s62, 0x80000\n"
        "s_addc_u32 s63, s63, 0\n"
        "v_cvt_pk_bf16_f32 v195, 0, v195\n"
        "v_mfma_f32_16x16x32_bf16 a[56:59], v[8:11], v[56:59], a[56:59]\n"
        "v_mul_f32 v194, v182, v186\n"
        "v_mul_f32 v196, v183, v186\n"
        "v_mfma_f32_16x16x32_bf16 a[60:63], v[8:11], v[60:63], a[60:63]\n"
        "ds_read_b128 v[8:11], v224 offset:43008\n"
        "v_cvt_pk_bf16_f32 v194, 0, v194\n"
        "v_mfma_f32_16x16x32_bf16 a[60:63], v[12:15], v[92:95], a[60:63]\n"
        "ds_read_b128 v[12:15], v225 offset:43008\n"
        "global_load_dwordx4 v[142:145], v254, s[92:93] offset:0\n"
        "v_mfma_f32_16x16x32_bf16 a[28:31], v[4:7], v[92:95], a[28:31]\n"
        "ds_read_b128 v[4:7], v225 offset:40960\n"
        "v_cvt_pk_bf16_f32 v196, 0, v196\n"
        "v_mfma_f32_16x16x32_bf16 a[28:31], v[0:3], v[60:63], a[28:31]\n"
        "ds_read_b128 v[0:3], v224 offset:40960\n"
        "v_add_f32 v193, v193, v194\n"
        "v_mfma_f32_16x16x32_bf16 a[64:67], v[32:35], v[16:19], a[64:67]\n"
        "v_add_f32 v195, v195, v196\n"
        "v_cvt_pk_bf16_f32 v193, 0, v193\n"
        "v_mfma_f32_16x16x32_bf16 a[64:67], v[64:67], v[20:23], a[64:67]\n"
        "v_cvt_pk_bf16_f32 v195, 0, v195\n"
        "v_mul_f32 v194, v183, v185\n"
        "v_mfma_f32_16x16x32_bf16 a[96:99], v[64:67], v[28:31], a[96:99]\n"
        "v_mul_f32 v196, v184, v185\n"
        "v_cvt_pk_bf16_f32 v194, 0, v194\n"
        "v_mfma_f32_16x16x32_bf16 a[96:99], v[32:35], v[24:27], a[96:99]\n"
        "v_cvt_pk_bf16_f32 v196, 0, v196\n"
        "v_add_f32 v193, v193, v194\n"
        "v_mfma_f32_16x16x32_bf16 a[100:103], v[36:39], v[24:27], a[100:103]\n"
        "v_add_f32 v195, v195, v196\n"
        "v_cvt_pk_bf16_f32 v193, 0, v193\n"
        "v_mfma_f32_16x16x32_bf16 a[100:103], v[68:71], v[28:31], a[100:103]\n"
        "s_waitcnt vmcnt(27) lgkmcnt(2)\n"
        "v_cvt_pk_bf16_f32 v195, 0, v195\n"
        "v_lshlrev_b32 v194, 16, v130\n"
        "v_mfma_f32_16x16x32_bf16 a[68:71], v[68:71], v[20:23], a[68:71]\n"
        "v_lshlrev_b32 v196, 16, v132\n"
        "v_mul_f32 v193, v193, v194\n"
        "v_mfma_f32_16x16x32_bf16 a[68:71], v[36:39], v[16:19], a[68:71]\n"
        "v_mul_f32 v195, v195, v196\n"
        "v_cvt_pk_bf16_f32 v200, v195, v193\n"
        "v_mfma_f32_16x16x32_bf16 a[72:75], v[40:43], v[16:19], a[72:75]\n"
        "global_store_dwordx4 v171, v[200:203], s[76:77] offset:-4096 sc1\n"
        "v_lshlrev_b32 v185, 16, v136\n"
        "v_and_b32 v186, 0xffff0000, v136\n"
        "v_lshlrev_b32 v187, 16, v137\n"
        "v_and_b32 v188, 0xffff0000, v137\n"
        "v_mfma_f32_16x16x32_bf16 a[72:75], v[72:75], v[20:23], a[72:75]\n"
        "v_and_b32 v178, 0xffff0000, v104\n"
        "v_and_b32 v179, 0xffff0000, v106\n"
        "v_mfma_f32_16x16x32_bf16 a[104:107], v[72:75], v[28:31], a[104:107]\n"
        "v_and_b32 v180, 0xffff0000, v108\n"
        "v_and_b32 v181, 0xffff0000, v110\n"
        "v_mfma_f32_16x16x32_bf16 a[104:107], v[40:43], v[24:27], a[104:107]\n"
        "v_and_b32 v182, 0xffff0000, v112\n"
        "v_and_b32 v183, 0xffff0000, v114\n"
        "v_mfma_f32_16x16x32_bf16 a[108:111], v[44:47], v[24:27], a[108:111]\n"
        "v_and_b32 v184, 0xffff0000, v116\n"
        "v_mul_f32 v189, v174, v188\n"
        "v_mfma_f32_16x16x32_bf16 a[108:111], v[76:79], v[28:31], a[108:111]\n"
        "v_mul_f32 v191, v175, v188\n"
        "v_cvt_pk_bf16_f32 v189, 0, v189\n"
        "v_mfma_f32_16x16x32_bf16 a[76:79], v[76:79], v[20:23], a[76:79]\n"
        "v_cvt_pk_bf16_f32 v191, 0, v191\n"
        "v_mul_f32 v190, v175, v187\n"
        "v_mfma_f32_16x16x32_bf16 a[76:79], v[44:47], v[16:19], a[76:79]\n"
        "global_load_dwordx4 v[146:149], v254, s[92:93] offset:1024\n"
        "s_add_u32 s92, s8, s91\n"
        "s_addc_u32 s93, s9, 0\n"
        "v_mfma_f32_16x16x32_bf16 a[80:83], v[16:19], v[48:51], a[80:83]\n"
        "v_mul_f32 v192, v176, v187\n"
        "v_cvt_pk_bf16_f32 v190, 0, v190\n"
        "v_mfma_f32_16x16x32_bf16 a[80:83], v[20:23], v[80:83], a[80:83]\n"
        "v_cvt_pk_bf16_f32 v192, 0, v192\n"
        "v_add_f32 v189, v189, v190\n"
        "v_mfma_f32_16x16x32_bf16 a[112:115], v[28:31], v[80:83], a[112:115]\n"
        "v_add_f32 v191, v191, v192\n"
        "v_cvt_pk_bf16_f32 v189, 0, v189\n"
        "v_mfma_f32_16x16x32_bf16 a[112:115], v[24:27], v[48:51], a[112:115]\n"
        "v_cvt_pk_bf16_f32 v191, 0, v191\n"
        "v_mul_f32 v190, v176, v186\n"
        "v_mfma_f32_16x16x32_bf16 a[116:119], v[24:27], v[52:55], a[116:119]\n"
        "v_mul_f32 v192, v177, v186\n"
        "v_cvt_pk_bf16_f32 v190, 0, v190\n"
        "v_mfma_f32_16x16x32_bf16 a[116:119], v[28:31], v[84:87], a[116:119]\n"
        "s_waitcnt lgkmcnt(0)\n"
        "v_cvt_pk_bf16_f32 v192, 0, v192\n"
        "v_add_f32 v189, v189, v190\n"
        "v_mfma_f32_16x16x32_bf16 a[84:87], v[20:23], v[84:87], a[84:87]\n"
        "v_add_f32 v191, v191, v192\n"
        "v_cvt_pk_bf16_f32 v189, 0, v189\n"
        "v_mfma_f32_16x16x32_bf16 a[84:87], v[16:19], v[52:55], a[84:87]\n"
        "v_cvt_pk_bf16_f32 v191, 0, v191\n"
        "v_mul_f32 v190, v177, v185\n"
        "v_mfma_f32_16x16x32_bf16 a[88:91], v[16:19], v[56:59], a[88:91]\n"
        "v_mul_f32 v192, v178, v185\n"
        "v_cvt_pk_bf16_f32 v190, 0, v190\n"
        "v_mfma_f32_16x16x32_bf16 a[88:91], v[20:23], v[88:91], a[88:91]\n"
        "v_cvt_pk_bf16_f32 v192, 0, v192\n"
        "v_add_f32 v189, v189, v190\n"
        "v_mfma_f32_16x16x32_bf16 a[120:123], v[28:31], v[88:91], a[120:123]\n"
        "v_add_f32 v191, v191, v192\n"
        "v_cvt_pk_bf16_f32 v189, 0, v189\n"
        "v_mfma_f32_16x16x32_bf16 a[120:123], v[24:27], v[56:59], a[120:123]\n"
        "v_cvt_pk_bf16_f32 v191, 0, v191\n"
        "v_and_b32 v190, 0xffff0000, v118\n"
        "v_mfma_f32_16x16x32_bf16 a[124:127], v[24:27], v[60:63], a[124:127]\n"
        "ds_read_b128 v[24:27], v224 offset:47104\n"
        "v_and_b32 v192, 0xffff0000, v120\n"
        "v_mfma_f32_16x16x32_bf16 a[124:127], v[28:31], v[92:95], a[124:127]\n"
        "ds_read_b128 v[28:31], v225 offset:47104\n"
        "v_mul_f32 v189, v189, v190\n"
        "v_mfma_f32_16x16x32_bf16 a[92:95], v[20:23], v[92:95], a[92:95]\n"
        "ds_read_b128 v[20:23], v225 offset:45056\n"
        "v_mul_f32 v191, v191, v192\n"
        "v_mfma_f32_16x16x32_bf16 a[92:95], v[16:19], v[60:63], a[92:95]\n"
        "ds_read_b128 v[16:19], v224 offset:45056\n"
        "v_cvt_pk_bf16_f32 v211, v191, v189\n"
        "v_mfma_f32_16x16x32_bf16 a[128:131], v[32:35], v[0:3], a[128:131]\n"
        "v_mul_f32 v193, v176, v188\n"
        "v_mul_f32 v195, v177, v188\n"
        "v_mfma_f32_16x16x32_bf16 a[128:131], v[64:67], v[4:7], a[128:131]\n"
        "v_cvt_pk_bf16_f32 v193, 0, v193\n"
        "v_cvt_pk_bf16_f32 v195, 0, v195\n"
        "v_mfma_f32_16x16x32_bf16 a[160:163], v[64:67], v[12:15], a[160:163]\n"
        "v_mul_f32 v194, v177, v187\n"
        "v_mul_f32 v196, v178, v187\n"
        "v_mfma_f32_16x16x32_bf16 a[160:163], v[32:35], v[8:11], a[160:163]\n"
        "v_cvt_pk_bf16_f32 v194, 0, v194\n"
        "v_cvt_pk_bf16_f32 v196, 0, v196\n"
        "v_mfma_f32_16x16x32_bf16 a[164:167], v[36:39], v[8:11], a[164:167]\n"
        "v_add_f32 v193, v193, v194\n"
        "v_add_f32 v195, v195, v196\n"
        "v_mfma_f32_16x16x32_bf16 a[164:167], v[68:71], v[12:15], a[164:167]\n"
        "v_cvt_pk_bf16_f32 v193, 0, v193\n"
        "v_cvt_pk_bf16_f32 v195, 0, v195\n"
        "v_mfma_f32_16x16x32_bf16 a[132:135], v[68:71], v[4:7], a[132:135]\n"
        "v_mul_f32 v194, v178, v186\n"
        "v_mul_f32 v196, v179, v186\n"
        "v_mfma_f32_16x16x32_bf16 a[132:135], v[36:39], v[0:3], a[132:135]\n"
        "global_load_dword v253, v173, s[82:83] offset:0\n"
        "s_add_u32 s82, s12, 0x300000\n"
        "s_addc_u32 s83, s13, 0\n"
        "v_mfma_f32_16x16x32_bf16 a[136:139], v[40:43], v[0:3], a[136:139]\n"
        "global_load_dwordx4 v[150:153], v254, s[92:93] offset:0\n"
        "v_cvt_pk_bf16_f32 v194, 0, v194\n"
        "v_mfma_f32_16x16x32_bf16 a[136:139], v[72:75], v[4:7], a[136:139]\n"
        "v_cvt_pk_bf16_f32 v196, 0, v196\n"
        "v_add_f32 v193, v193, v194\n"
        "v_mfma_f32_16x16x32_bf16 a[168:171], v[72:75], v[12:15], a[168:171]\n"
        "v_add_f32 v195, v195, v196\n"
        "v_cvt_pk_bf16_f32 v193, 0, v193\n"
        "v_mfma_f32_16x16x32_bf16 a[168:171], v[40:43], v[8:11], a[168:171]\n"
        "v_cvt_pk_bf16_f32 v195, 0, v195\n"
        "v_mul_f32 v194, v179, v185\n"
        "v_mfma_f32_16x16x32_bf16 a[172:175], v[44:47], v[8:11], a[172:175]\n"
        "v_mul_f32 v196, v180, v185\n"
        "v_cvt_pk_bf16_f32 v194, 0, v194\n"
        "v_mfma_f32_16x16x32_bf16 a[172:175], v[76:79], v[12:15], a[172:175]\n"
        "v_cvt_pk_bf16_f32 v196, 0, v196\n"
        "v_add_f32 v193, v193, v194\n"
        "v_mfma_f32_16x16x32_bf16 a[140:143], v[76:79], v[4:7], a[140:143]\n"
        "v_add_f32 v195, v195, v196\n"
        "v_cvt_pk_bf16_f32 v193, 0, v193\n"
        "v_mfma_f32_16x16x32_bf16 a[140:143], v[44:47], v[0:3], a[140:143]\n"
        "v_cvt_pk_bf16_f32 v195, 0, v195\n"
        "v_and_b32 v194, 0xffff0000, v122\n"
        "v_mfma_f32_16x16x32_bf16 a[144:147], v[0:3], v[48:51], a[144:147]\n"
        "v_and_b32 v196, 0xffff0000, v124\n"
        "v_mul_f32 v193, v193, v194\n"
        "v_mfma_f32_16x16x32_bf16 a[144:147], v[4:7], v[80:83], a[144:147]\n"
        "LHA2_%=:\n"
        "s_cbranch_execz LHA6_%=\n"
        "v_mul_f32 v195, v195, v196\n"
        "v_cvt_pk_bf16_f32 v210, v195, v193\n"
        "v_mfma_f32_16x16x32_bf16 a[176:179], v[12:15], v[80:83], a[176:179]\n"
        "v_mul_f32 v189, v178, v188\n"
        "v_mul_f32 v191, v179, v188\n"
        "v_mfma_f32_16x16x32_bf16 a[176:179], v[8:11], v[48:51], a[176:179]\n"
        "v_cvt_pk_bf16_f32 v189, 0, v189\n"
        "v_cvt_pk_bf16_f32 v191, 0, v191\n"
        "v_mfma_f32_16x16x32_bf16 a[180:183], v[8:11], v[52:55], a[180:183]\n"
        "v_mul_f32 v190, v179, v187\n"
        "v_mul_f32 v192, v180, v187\n"
        "v_mfma_f32_16x16x32_bf16 a[180:183], v[12:15], v[84:87], a[180:183]\n"
        "s_waitcnt lgkmcnt(0)\n"
        "v_cvt_pk_bf16_f32 v190, 0, v190\n"
        "v_cvt_pk_bf16_f32 v192, 0, v192\n"
        "v_mfma_f32_16x16x32_bf16 a[148:151], v[4:7], v[84:87], a[148:151]\n"
        "v_add_f32 v189, v189, v190\n"
        "v_add_f32 v191, v191, v192\n"
        "v_mfma_f32_16x16x32_bf16 a[148:151], v[0:3], v[52:55], a[148:151]\n"
        "v_cvt_pk_bf16_f32 v189, 0, v189\n"
        "v_cvt_pk_bf16_f32 v191, 0, v191\n"
        "v_mfma_f32_16x16x32_bf16 a[152:155], v[0:3], v[56:59], a[152:155]\n"
        "v_mul_f32 v190, v180, v186\n"
        "v_mul_f32 v192, v181, v186\n"
        "v_mfma_f32_16x16x32_bf16 a[152:155], v[4:7], v[88:91], a[152:155]\n"
        "v_cvt_pk_bf16_f32 v190, 0, v190\n"
        "v_cvt_pk_bf16_f32 v192, 0, v192\n"
        "v_mfma_f32_16x16x32_bf16 a[184:187], v[12:15], v[88:91], a[184:187]\n"
        "v_add_f32 v189, v189, v190\n"
        "v_add_f32 v191, v191, v192\n"
        "v_mfma_f32_16x16x32_bf16 a[184:187], v[8:11], v[56:59], a[184:187]\n"
        "s_waitcnt vmcnt(13)\n"
        "s_barrier\n"
        "v_cvt_pk_bf16_f32 v189, 0, v189\n"
        "v_mfma_f32_16x16x32_bf16 a[188:191], v[8:11], v[60:63], a[188:191]\n"
        "global_load_lds_dwordx4 v234, s[56:57] offset:0\n"
        "ds_read_b128 v[8:11], v224 offset:2048\n"
        "v_mfma_f32_16x16x32_bf16 a[188:191], v[12:15], v[92:95], a[188:191]\n"
        "ds_read_b128 v[12:15], v225 offset:2048\n"
        "v_cvt_pk_bf16_f32 v191, 0, v191\n"
        "v_mfma_f32_16x16x32_bf16 a[156:159], v[4:7], v[92:95], a[156:159]\n"
        "ds_read_b128 v[4:7], v225 offset:0\n"
        "v_mul_f32 v190, v181, v185\n"
        "v_mfma_f32_16x16x32_bf16 a[156:159], v[0:3], v[60:63], a[156:159]\n"
        "ds_read_b128 v[0:3], v224 offset:0\n"
        "v_mul_f32 v192, v182, v185\n"
        "v_mfma_f32_16x16x32_bf16 a[192:195], v[32:35], v[16:19], a[192:195]\n"
        "global_load_lds_dwordx4 v235, s[56:57] offset:1024\n"
        "v_cvt_pk_bf16_f32 v190, 0, v190\n"
        "v_mfma_f32_16x16x32_bf16 a[192:195], v[64:67], v[20:23], a[192:195]\n"
        "v_cvt_pk_bf16_f32 v192, 0, v192\n"
        "v_add_f32 v189, v189, v190\n"
        "v_mfma_f32_16x16x32_bf16 a[224:227], v[64:67], v[28:31], a[224:227]\n"
        "ds_read_b64_tr_b16 v[64:65], v226 offset:40960\n"
        "v_add_f32 v191, v191, v192\n"
        "v_mfma_f32_16x16x32_bf16 a[224:227], v[32:35], v[24:27], a[224:227]\n"
        "ds_read_b64_tr_b16 v[32:33], v226 offset:32768\n"
        "v_cvt_pk_bf16_f32 v189, 0, v189\n"
        "v_mfma_f32_16x16x32_bf16 a[228:231], v[36:39], v[24:27], a[228:231]\n"
        "ds_read_b64_tr_b16 v[34:35], v227 offset:32768\n"
        "global_load_dwordx4 v[154:157], v254, s[92:93] offset:1024\n"
        "v_mfma_f32_16x16x32_bf16 a[228:231], v[68:71], v[28:31], a[228:231]\n"
        "ds_read_b64_tr_b16 v[66:67], v227 offset:40960\n"
        "v_cvt_pk_bf16_f32 v191, 0, v191\n"
        "v_mfma_f32_16x16x32_bf16 a[196:199], v[68:71], v[20:23], a[196:199]\n"
        "ds_read_b64_tr_b16 v[68:69], v228 offset:40960\n"
        "v_and_b32 v190, 0xffff0000, v126\n"
        "v_mfma_f32_16x16x32_bf16 a[196:199], v[36:39], v[16:19], a[196:199]\n"
        "ds_read_b64_tr_b16 v[36:37], v228 offset:32768\n"
        "v_and_b32 v192, 0xffff0000, v128\n"
        "v_mfma_f32_16x16x32_bf16 a[200:203], v[40:43], v[16:19], a[200:203]\n"
        "ds_read_b64_tr_b16 v[38:39], v229 offset:32768\n"
        "global_load_lds_dwordx4 v236, s[56:57] offset:2048\n"
        "v_mfma_f32_16x16x32_bf16 a[200:203], v[72:75], v[20:23], a[200:203]\n"
        "ds_read_b64_tr_b16 v[70:71], v229 offset:40960\n"
        "v_mul_f32 v189, v189, v190\n"
        "v_mfma_f32_16x16x32_bf16 a[232:235], v[72:75], v[28:31], a[232:235]\n"
        "ds_read_b64_tr_b16 v[72:73], v230 offset:40960\n"
        "v_mul_f32 v191, v191, v192\n"
        "v_mfma_f32_16x16x32_bf16 a[232:235], v[40:43], v[24:27], a[232:235]\n"
        "ds_read_b64_tr_b16 v[40:41], v230 offset:32768\n"
        "v_cvt_pk_bf16_f32 v209, v191, v189\n"
        "v_mfma_f32_16x16x32_bf16 a[236:239], v[44:47], v[24:27], a[236:239]\n"
        "ds_read_b64_tr_b16 v[42:43], v231 offset:32768\n"
        "global_load_lds_dwordx4 v237, s[56:57] offset:3072\n"
        "s_add_u32 m0, s72, 0x9000\n"
        "v_mfma_f32_16x16x32_bf16 a[236:239], v[76:79], v[28:31], a[236:239]\n"
        "ds_read_b64_tr_b16 v[74:75], v231 offset:40960\n"
        "s_add_u32 s56, s56, 0x80\n"
        "s_addc_u32 s57, s57, 0\n"
        "v_mfma_f32_16x16x32_bf16 a[204:207], v[76:79], v[20:23], a[204:207]\n"
        "ds_read_b64_tr_b16 v[76:77], v232 offset:40960\n"
        "v_mul_f32 v193, v180, v188\n"
        "v_mfma_f32_16x16x32_bf16 a[204:207], v[44:47], v[16:19], a[204:207]\n"
        "ds_read_b64_tr_b16 v[44:45], v232 offset:32768\n"
        "v_mul_f32 v195, v181, v188\n"
        "v_mfma_f32_16x16x32_bf16 a[208:211], v[16:19], v[48:51], a[208:211]\n"
        "ds_read_b64_tr_b16 v[46:47], v233 offset:32768\n"
        "global_load_lds_dwordx4 v234, s[58:59] offset:0\n"
        "v_mfma_f32_16x16x32_bf16 a[208:211], v[20:23], v[80:83], a[208:211]\n"
        "ds_read_b64_tr_b16 v[78:79], v233 offset:40960\n"
        "v_cvt_pk_bf16_f32 v193, 0, v193\n"
        "v_mfma_f32_16x16x32_bf16 a[240:243], v[28:31], v[80:83], a[240:243]\n"
        "ds_read_b64_tr_b16 v[80:81], v226 offset:57344\n"
        "v_cvt_pk_bf16_f32 v195, 0, v195\n"
        "v_mfma_f32_16x16x32_bf16 a[240:243], v[24:27], v[48:51], a[240:243]\n"
        "ds_read_b64_tr_b16 v[48:49], v226 offset:49152\n"
        "v_mul_f32 v194, v181, v187\n"
        "v_mfma_f32_16x16x32_bf16 a[244:247], v[24:27], v[52:55], a[244:247]\n"
        "ds_read_b64_tr_b16 v[50:51], v227 offset:49152\n"
        "global_load_lds_dwordx4 v235, s[58:59] offset:1024\n"
        "v_mfma_f32_16x16x32_bf16 a[244:247], v[28:31], v[84:87], a[244:247]\n"
        "ds_read_b64_tr_b16 v[82:83], v227 offset:57344\n"
        "v_mul_f32 v196, v182, v187\n"
        "v_mfma_f32_16x16x32_bf16 a[212:215], v[20:23], v[84:87], a[212:215]\n"
        "ds_read_b64_tr_b16 v[84:85], v228 offset:57344\n"
        "v_cvt_pk_bf16_f32 v194, 0, v194\n"
        "v_mfma_f32_16x16x32_bf16 a[212:215], v[16:19], v[52:55], a[212:215]\n"
        "ds_read_b64_tr_b16 v[52:53], v228 offset:49152\n"
        "v_cvt_pk_bf16_f32 v196, 0, v196\n"
        "v_mfma_f32_16x16x32_bf16 a[216:219], v[16:19], v[56:59], a[216:219]\n"
        "ds_read_b64_tr_b16 v[54:55], v229 offset:49152\n"
        "global_load_lds_dwordx4 v236, s[58:59] offset:2048\n"
        "v_mfma_f32_16x16x32_bf16 a[216:219], v[20:23], v[88:91], a[216:219]\n"
        "ds_read_b64_tr_b16 v[86:87], v229 offset:57344\n"
        "v_add_f32 v193, v193, v194\n"
        "v_mfma_f32_16x16x32_bf16 a[248:251], v[28:31], v[88:91], a[248:251]\n"
        "ds_read_b64_tr_b16 v[88:89], v230 offset:57344\n"
        "v_add_f32 v195, v195, v196\n"
        "v_mfma_f32_16x16x32_bf16 a[248:251], v[24:27], v[56:59], a[248:251]\n"
        "ds_read_b64_tr_b16 v[56:57], v230 offset:49152\n"
        "v_cvt_pk_bf16_f32 v193, 0, v193\n"
        "v_mfma_f32_16x16x32_bf16 a[252:255], v[24:27], v[60:63], a[252:255]\n"
        "ds_read_b64_tr_b16 v[58:59], v231 offset:49152\n"
        "global_load_lds_dwordx4 v237, s[58:59] offset:3072\n"
        "s_add_u32 m0, s73, 0x10000\n"
        "v_mfma_f32_16x16x32_bf16 a[252:255], v[28:31], v[92:95], a[252:255]\n"
        "ds_read_b64_tr_b16 v[90:91], v231 offset:57344\n"
        "s_add_u32 s58, s58, 0x80\n"
        "s_addc_u32 s59, s59, 0\n"
        "v_mfma_f32_16x16x32_bf16 a[220:223], v[20:23], v[92:95], a[220:223]\n"
        "ds_read_b64_tr_b16 v[92:93], v232 offset:57344\n"
        "v_cvt_pk_bf16_f32 v195, 0, v195\n"
        "v_mfma_f32_16x16x32_bf16 a[220:223], v[16:19], v[60:63], a[220:223]\n"
        "ds_read_b64_tr_b16 v[60:61], v232 offset:49152\n"
        "s_waitcnt lgkmcnt(8)\n"
        "v_mfma_f32_16x16x32_bf16 a[0:3], v[32:35], v[0:3], a[0:3]\n"
        "ds_read_b64_tr_b16 v[62:63], v233 offset:49152\n"
        "global_load_dwordx4 v[158:161], v254, s[30:31] offset:0\n"
        "v_mfma_f32_16x16x32_bf16 a[0:3], v[64:67], v[4:7], a[0:3]\n"
        "ds_read_b64_tr_b16 v[94:95], v233 offset:57344\n"
        "v_mul_f32 v194, v182, v186\n"
        "v_mfma_f32_16x16x32_bf16 a[32:35], v[64:67], v[12:15], a[32:35]\n"
        "ds_read_b128 v[16:19], v224 offset:4096\n"
        "v_mul_f32 v196, v183, v186\n"
        "v_mfma_f32_16x16x32_bf16 a[32:35], v[32:35], v[8:11], a[32:35]\n"
        "ds_read_b128 v[20:23], v225 offset:4096\n"
        "v_cvt_pk_bf16_f32 v194, 0, v194\n"
        "v_mfma_f32_16x16x32_bf16 a[36:39], v[36:39], v[8:11], a[36:39]\n"
        "ds_read_b128 v[24:27], v224 offset:6144\n"
        "global_load_lds_dwordx4 v238, s[60:61] offset:0\n"
        "v_mfma_f32_16x16x32_bf16 a[36:39], v[68:71], v[12:15], a[36:39]\n"
        "ds_read_b128 v[28:31], v225 offset:6144\n"
        "v_cvt_pk_bf16_f32 v196, 0, v196\n"
        "v_mfma_f32_16x16x32_bf16 a[4:7], v[68:71], v[4:7], a[4:7]\n"
        "v_add_f32 v193, v193, v194\n"
        "v_add_f32 v195, v195, v196\n"
        "v_mfma_f32_16x16x32_bf16 a[4:7], v[36:39], v[0:3], a[4:7]\n"
        "v_cvt_pk_bf16_f32 v193, 0, v193\n"
        "v_cvt_pk_bf16_f32 v195, 0, v195\n"
        "v_mfma_f32_16x16x32_bf16 a[8:11], v[40:43], v[0:3], a[8:11]\n"
        "global_load_lds_dwordx4 v239, s[60:61] offset:1024\n"
        "v_mfma_f32_16x16x32_bf16 a[8:11], v[72:75], v[4:7], a[8:11]\n"
        "s_waitcnt lgkmcnt(4)\n"
        "v_mul_f32 v194, v183, v185\n"
        "v_mul_f32 v196, v184, v185\n"
        "v_mfma_f32_16x16x32_bf16 a[40:43], v[72:75], v[12:15], a[40:43]\n"
        "v_cvt_pk_bf16_f32 v194, 0, v194\n"
        "v_cvt_pk_bf16_f32 v196, 0, v196\n"
        "v_mfma_f32_16x16x32_bf16 a[40:43], v[40:43], v[8:11], a[40:43]\n"
        "v_add_f32 v193, v193, v194\n"
        "v_add_f32 v195, v195, v196\n"
        "v_mfma_f32_16x16x32_bf16 a[44:47], v[44:47], v[8:11], a[44:47]\n"
        "global_load_lds_dwordx4 v240, s[60:61] offset:2048\n"
        "v_mfma_f32_16x16x32_bf16 a[44:47], v[76:79], v[12:15], a[44:47]\n"
        "v_cvt_pk_bf16_f32 v193, 0, v193\n"
        "v_cvt_pk_bf16_f32 v195, 0, v195\n"
        "v_mfma_f32_16x16x32_bf16 a[12:15], v[76:79], v[4:7], a[12:15]\n"
        "v_and_b32 v194, 0xffff0000, v130\n"
        "v_and_b32 v196, 0xffff0000, v132\n"
        "v_mfma_f32_16x16x32_bf16 a[12:15], v[44:47], v[0:3], a[12:15]\n"
        "v_mul_f32 v193, v193, v194\n"
        "v_mul_f32 v195, v195, v196\n"
        "v_mfma_f32_16x16x32_bf16 a[16:19], v[0:3], v[48:51], a[16:19]\n"
        "global_load_lds_dwordx4 v241, s[60:61] offset:3072\n"
        "s_add_u32 m0, s73, 0x14000\n"
        "v_mfma_f32_16x16x32_bf16 a[16:19], v[4:7], v[80:83], a[16:19]\n"
        "s_add_u32 s60, s60, 0x80000\n"
        "s_addc_u32 s61, s61, 0\n"
        "v_cvt_pk_bf16_f32 v208, v195, v193\n"
        "v_mfma_f32_16x16x32_bf16 a[48:51], v[12:15], v[80:83], a[48:51]\n"
        "v_lshlrev_b32 v185, 16, v138\n"
        "v_and_b32 v186, 0xffff0000, v138\n"
        "v_mfma_f32_16x16x32_bf16 a[48:51], v[8:11], v[48:51], a[48:51]\n"
        "v_lshlrev_b32 v187, 16, v139\n"
        "v_and_b32 v188, 0xffff0000, v139\n"
        "v_mfma_f32_16x16x32_bf16 a[52:55], v[8:11], v[52:55], a[52:55]\n"
        "global_load_lds_dwordx4 v238, s[62:63] offset:0\n"
        "v_mfma_f32_16x16x32_bf16 a[52:55], v[12:15], v[84:87], a[52:55]\n"
        "s_waitcnt lgkmcnt(0)\n"
        "v_lshlrev_b32 v174, 16, v97\n"
        "v_lshlrev_b32 v175, 16, v99\n"
        "v_mfma_f32_16x16x32_bf16 a[20:23], v[4:7], v[84:87], a[20:23]\n"
        "v_lshlrev_b32 v176, 16, v101\n"
        "v_lshlrev_b32 v177, 16, v103\n"
        "v_mfma_f32_16x16x32_bf16 a[20:23], v[0:3], v[52:55], a[20:23]\n"
        "v_lshlrev_b32 v178, 16, v105\n"
        "v_lshlrev_b32 v179, 16, v107\n"
        "v_mfma_f32_16x16x32_bf16 a[24:27], v[0:3], v[56:59], a[24:27]\n"
        "global_load_lds_dwordx4 v239, s[62:63] offset:1024\n"
        "v_mfma_f32_16x16x32_bf16 a[24:27], v[4:7], v[88:91], a[24:27]\n"
        "v_lshlrev_b32 v180, 16, v109\n"
        "v_lshlrev_b32 v181, 16, v111\n"
        "v_mfma_f32_16x16x32_bf16 a[56:59], v[12:15], v[88:91], a[56:59]\n"
        "v_lshlrev_b32 v182, 16, v113\n"
        "v_lshlrev_b32 v183, 16, v115\n"
        "v_mfma_f32_16x16x32_bf16 a[56:59], v[8:11], v[56:59], a[56:59]\n"
        "v_lshlrev_b32 v184, 16, v117\n"
        "v_mul_f32 v189, v174, v188\n"
        "v_mfma_f32_16x16x32_bf16 a[60:63], v[8:11], v[60:63], a[60:63]\n"
        "global_load_dwordx4 v[162:165], v254, s[30:31] offset:1024\n"
        "v_mfma_f32_16x16x32_bf16 a[60:63], v[12:15], v[92:95], a[60:63]\n"
        "ds_read_b128 v[8:11], v224 offset:10240\n"
        "v_mul_f32 v191, v175, v188\n"
        "v_mfma_f32_16x16x32_bf16 a[28:31], v[4:7], v[92:95], a[28:31]\n"
        "ds_read_b128 v[4:7], v225 offset:8192\n"
        "v_cvt_pk_bf16_f32 v189, 0, v189\n"
        "v_mfma_f32_16x16x32_bf16 a[28:31], v[0:3], v[60:63], a[28:31]\n"
        "ds_read_b128 v[0:3], v224 offset:8192\n"
        "v_cvt_pk_bf16_f32 v191, 0, v191\n"
        "v_mfma_f32_16x16x32_bf16 a[64:67], v[32:35], v[16:19], a[64:67]\n"
        "global_load_dword v253, v173, s[82:83] offset:0\n"
        "v_mfma_f32_16x16x32_bf16 a[64:67], v[64:67], v[20:23], a[64:67]\n"
        "global_load_lds_dwordx4 v240, s[62:63] offset:2048\n"
        "ds_read_b128 v[12:15], v225 offset:10240\n"
        "v_mfma_f32_16x16x32_bf16 a[96:99], v[64:67], v[28:31], a[96:99]\n"
        "s_add_u32 s82, s12, 0x400000\n"
        "s_addc_u32 s83, s13, 0\n"
        "v_mul_f32 v190, v175, v187\n"
        "v_mfma_f32_16x16x32_bf16 a[96:99], v[32:35], v[24:27], a[96:99]\n"
        "v_mul_f32 v192, v176, v187\n"
        "v_cvt_pk_bf16_f32 v190, 0, v190\n"
        "v_mfma_f32_16x16x32_bf16 a[100:103], v[36:39], v[24:27], a[100:103]\n"
        "v_cvt_pk_bf16_f32 v192, 0, v192\n"
        "v_add_f32 v189, v189, v190\n"
        "v_mfma_f32_16x16x32_bf16 a[100:103], v[68:71], v[28:31], a[100:103]\n"
        "global_load_lds_dwordx4 v241, s[62:63] offset:3072\n"
        "s_add_u32 s62, s62, 0x80000\n"
        "s_addc_u32 s63, s63, 0\n"
        "v_mfma_f32_16x16x32_bf16 a[68:71], v[68:71], v[20:23], a[68:71]\n"
        "s_add_u32 m0, s72, 0x0\n"
        "v_add_f32 v191, v191, v192\n"
        "v_cvt_pk_bf16_f32 v189, 0, v189\n"
        "v_mfma_f32_16x16x32_bf16 a[68:71], v[36:39], v[16:19], a[68:71]\n"
        "v_cvt_pk_bf16_f32 v191, 0, v191\n"
        "v_mul_f32 v190, v176, v186\n"
        "v_mfma_f32_16x16x32_bf16 a[72:75], v[40:43], v[16:19], a[72:75]\n"
        "v_mul_f32 v192, v177, v186\n"
        "v_cvt_pk_bf16_f32 v190, 0, v190\n"
        "v_mfma_f32_16x16x32_bf16 a[72:75], v[72:75], v[20:23], a[72:75]\n"
        "global_load_ushort v172, v172, s[28:29]\n"
        "v_cvt_pk_bf16_f32 v192, 0, v192\n"
        "v_mfma_f32_16x16x32_bf16 a[104:107], v[72:75], v[28:31], a[104:107]\n"
        "global_store_dwordx4 v171, v[208:211], s[76:77] offset:0 sc1\n"
        "s_add_u32 s76, s76, 0x2000\n"
        "s_addc_u32 s77, s77, 0\n"
        "v_add_f32 v189, v189, v190\n"
        "v_add_f32 v191, v191, v192\n"
        "v_cvt_pk_bf16_f32 v189, 0, v189\n"
        "v_mfma_f32_16x16x32_bf16 a[104:107], v[40:43], v[24:27], a[104:107]\n"
        "v_cvt_pk_bf16_f32 v191, 0, v191\n"
        "v_mul_f32 v190, v177, v185\n"
        "v_mfma_f32_16x16x32_bf16 a[108:111], v[44:47], v[24:27], a[108:111]\n"
        "v_mul_f32 v192, v178, v185\n"
        "v_cvt_pk_bf16_f32 v190, 0, v190\n"
        "v_mfma_f32_16x16x32_bf16 a[108:111], v[76:79], v[28:31], a[108:111]\n"
        "v_cvt_pk_bf16_f32 v192, 0, v192\n"
        "v_add_f32 v189, v189, v190\n"
        "v_mfma_f32_16x16x32_bf16 a[76:79], v[76:79], v[20:23], a[76:79]\n"
        "v_add_f32 v191, v191, v192\n"
        "v_cvt_pk_bf16_f32 v189, 0, v189\n"
        "v_mfma_f32_16x16x32_bf16 a[76:79], v[44:47], v[16:19], a[76:79]\n"
        "v_cvt_pk_bf16_f32 v191, 0, v191\n"
        "v_lshlrev_b32 v190, 16, v119\n"
        "v_mfma_f32_16x16x32_bf16 a[80:83], v[16:19], v[48:51], a[80:83]\n"
        "v_lshlrev_b32 v192, 16, v121\n"
        "v_mul_f32 v189, v189, v190\n"
        "v_mfma_f32_16x16x32_bf16 a[80:83], v[20:23], v[80:83], a[80:83]\n"
        "v_mul_f32 v191, v191, v192\n"
        "v_cvt_pk_bf16_f32 v203, v191, v189\n"
        "v_mfma_f32_16x16x32_bf16 a[112:115], v[28:31], v[80:83], a[112:115]\n"
        "v_mul_f32 v193, v176, v188\n"
        "v_mul_f32 v195, v177, v188\n"
        "v_mfma_f32_16x16x32_bf16 a[112:115], v[24:27], v[48:51], a[112:115]\n"
        "v_cvt_pk_bf16_f32 v193, 0, v193\n"
        "v_cvt_pk_bf16_f32 v195, 0, v195\n"
        "v_mfma_f32_16x16x32_bf16 a[116:119], v[24:27], v[52:55], a[116:119]\n"
        "v_mul_f32 v194, v177, v187\n"
        "v_mul_f32 v196, v178, v187\n"
        "v_mfma_f32_16x16x32_bf16 a[116:119], v[28:31], v[84:87], a[116:119]\n"
        "s_waitcnt lgkmcnt(0)\n"
        "v_cvt_pk_bf16_f32 v194, 0, v194\n"
        "v_cvt_pk_bf16_f32 v196, 0, v196\n"
        "v_mfma_f32_16x16x32_bf16 a[84:87], v[20:23], v[84:87], a[84:87]\n"
        "v_add_f32 v193, v193, v194\n"
        "v_add_f32 v195, v195, v196\n"
        "v_mfma_f32_16x16x32_bf16 a[84:87], v[16:19], v[52:55], a[84:87]\n"
        "v_cvt_pk_bf16_f32 v193, 0, v193\n"
        "v_cvt_pk_bf16_f32 v195, 0, v195\n"
        "v_mfma_f32_16x16x32_bf16 a[88:91], v[16:19], v[56:59], a[88:91]\n"
        "v_mul_f32 v194, v178, v186\n"
        "v_mul_f32 v196, v179, v186\n"
        "v_mfma_f32_16x16x32_bf16 a[88:91], v[20:23], v[88:91], a[88:91]\n"
        "v_cvt_pk_bf16_f32 v194, 0, v194\n"
        "v_cvt_pk_bf16_f32 v196, 0, v196\n"
        "v_mfma_f32_16x16x32_bf16 a[120:123], v[28:31], v[88:91], a[120:123]\n"
        "v_add_f32 v193, v193, v194\n"
        "v_add_f32 v195, v195, v196\n"
        "v_mfma_f32_16x16x32_bf16 a[120:123], v[24:27], v[56:59], a[120:123]\n"
        "v_cvt_pk_bf16_f32 v193, 0, v193\n"
        "v_cvt_pk_bf16_f32 v195, 0, v195\n"
        "v_mfma_f32_16x16x32_bf16 a[124:127], v[24:27], v[60:63], a[124:127]\n"
        "ds_read_b128 v[24:27], v224 offset:14336\n"
        "v_mul_f32 v194, v179, v185\n"
        "v_mfma_f32_16x16x32_bf16 a[124:127], v[28:31], v[92:95], a[124:127]\n"
        "ds_read_b128 v[28:31], v225 offset:14336\n"
        "v_mul_f32 v196, v180, v185\n"
        "v_mfma_f32_16x16x32_bf16 a[92:95], v[20:23], v[92:95], a[92:95]\n"
        "ds_read_b128 v[20:23], v225 offset:12288\n"
        "v_cvt_pk_bf16_f32 v194, 0, v194\n"
        "v_mfma_f32_16x16x32_bf16 a[92:95], v[16:19], v[60:63], a[92:95]\n"
        "ds_read_b128 v[16:19], v224 offset:12288\n"
        "v_cvt_pk_bf16_f32 v196, 0, v196\n"
        "v_mfma_f32_16x16x32_bf16 a[128:131], v[32:35], v[0:3], a[128:131]\n"
        "v_add_f32 v193, v193, v194\n"
        "v_add_f32 v195, v195, v196\n"
        "v_mfma_f32_16x16x32_bf16 a[128:131], v[64:67], v[4:7], a[128:131]\n"
        "v_cvt_pk_bf16_f32 v193, 0, v193\n"
        "v_cvt_pk_bf16_f32 v195, 0, v195\n"
        "v_mfma_f32_16x16x32_bf16 a[160:163], v[64:67], v[12:15], a[160:163]\n"
        "v_lshlrev_b32 v194, 16, v123\n"
        "v_lshlrev_b32 v196, 16, v125\n"
        "v_mfma_f32_16x16x32_bf16 a[160:163], v[32:35], v[8:11], a[160:163]\n"
        "v_mul_f32 v193, v193, v194\n"
        "v_mul_f32 v195, v195, v196\n"
        "v_mfma_f32_16x16x32_bf16 a[164:167], v[36:39], v[8:11], a[164:167]\n"
        "v_cvt_pk_bf16_f32 v202, v195, v193\n"
        "v_mul_f32 v189, v178, v188\n"
        "v_mfma_f32_16x16x32_bf16 a[164:167], v[68:71], v[12:15], a[164:167]\n"
        "v_mul_f32 v191, v179, v188\n"
        "v_cvt_pk_bf16_f32 v189, 0, v189\n"
        "v_mfma_f32_16x16x32_bf16 a[132:135], v[68:71], v[4:7], a[132:135]\n"
        "v_cvt_pk_bf16_f32 v191, 0, v191\n"
        "v_mul_f32 v190, v179, v187\n"
        "v_mfma_f32_16x16x32_bf16 a[132:135], v[36:39], v[0:3], a[132:135]\n"
        "v_mul_f32 v192, v180, v187\n"
        "v_cvt_pk_bf16_f32 v190, 0, v190\n"
        "v_mfma_f32_16x16x32_bf16 a[136:139], v[40:43], v[0:3], a[136:139]\n"
        "v_cvt_pk_bf16_f32 v192, 0, v192\n"
        "v_add_f32 v189, v189, v190\n"
        "v_mfma_f32_16x16x32_bf16 a[136:139], v[72:75], v[4:7], a[136:139]\n"
        "v_add_f32 v191, v191, v192\n"
        "v_cvt_pk_bf16_f32 v189, 0, v189\n"
        "v_mfma_f32_16x16x32_bf16 a[168:171], v[72:75], v[12:15], a[168:171]\n"
        "v_cvt_pk_bf16_f32 v191, 0, v191\n"
        "v_mul_f32 v190, v180, v186\n"
        "v_mfma_f32_16x16x32_bf16 a[168:171], v[40:43], v[8:11], a[168:171]\n"
        "v_mul_f32 v192, v181, v186\n"
        "v_cvt_pk_bf16_f32 v190, 0, v190\n"
        "v_mfma_f32_16x16x32_bf16 a[172:175], v[44:47], v[8:11], a[172:175]\n"
        "v_cvt_pk_bf16_f32 v192, 0, v192\n"
        "v_add_f32 v189, v189, v190\n"
        "v_mfma_f32_16x16x32_bf16 a[172:175], v[76:79], v[12:15], a[172:175]\n"
        "v_add_f32 v191, v191, v192\n"
        "v_cvt_pk_bf16_f32 v189, 0, v189\n"
        "v_mfma_f32_16x16x32_bf16 a[140:143], v[76:79], v[4:7], a[140:143]\n"
        "v_cvt_pk_bf16_f32 v191, 0, v191\n"
        "v_mul_f32 v190, v181, v185\n"
        "v_mfma_f32_16x16x32_bf16 a[140:143], v[44:47], v[0:3], a[140:143]\n"
        "v_mul_f32 v192, v182, v185\n"
        "v_cvt_pk_bf16_f32 v190, 0, v190\n"
        "v_mfma_f32_16x16x32_bf16 a[144:147], v[0:3], v[48:51], a[144:147]\n"
        "v_cvt_pk_bf16_f32 v192, 0, v192\n"
        "v_add_f32 v189, v189, v190\n"
        "v_mfma_f32_16x16x32_bf16 a[144:147], v[4:7], v[80:83], a[144:147]\n"
        "v_add_f32 v191, v191, v192\n"
        "v_cvt_pk_bf16_f32 v189, 0, v189\n"
        "v_mfma_f32_16x16x32_bf16 a[176:179], v[12:15], v[80:83], a[176:179]\n"
        "v_cvt_pk_bf16_f32 v191, 0, v191\n"
        "v_lshlrev_b32 v190, 16, v127\n"
        "v_mfma_f32_16x16x32_bf16 a[176:179], v[8:11], v[48:51], a[176:179]\n"
        "v_lshlrev_b32 v192, 16, v129\n"
        "v_mul_f32 v189, v189, v190\n"
        "v_mfma_f32_16x16x32_bf16 a[180:183], v[8:11], v[52:55], a[180:183]\n"
        "v_mul_f32 v191, v191, v192\n"
        "v_cvt_pk_bf16_f32 v201, v191, v189\n"
        "v_mfma_f32_16x16x32_bf16 a[180:183], v[12:15], v[84:87], a[180:183]\n"
        "s_waitcnt lgkmcnt(0)\n"
        "v_mul_f32 v193, v180, v188\n"
        "v_mul_f32 v195, v181, v188\n"
        "v_mfma_f32_16x16x32_bf16 a[148:151], v[4:7], v[84:87], a[148:151]\n"
        "v_cvt_pk_bf16_f32 v193, 0, v193\n"
        "v_cvt_pk_bf16_f32 v195, 0, v195\n"
        "v_mfma_f32_16x16x32_bf16 a[148:151], v[0:3], v[52:55], a[148:151]\n"
        "v_mul_f32 v194, v181, v187\n"
        "v_mul_f32 v196, v182, v187\n"
        "v_mfma_f32_16x16x32_bf16 a[152:155], v[0:3], v[56:59], a[152:155]\n"
        "v_cvt_pk_bf16_f32 v194, 0, v194\n"
        "v_cvt_pk_bf16_f32 v196, 0, v196\n"
        "v_mfma_f32_16x16x32_bf16 a[152:155], v[4:7], v[88:91], a[152:155]\n"
        "v_add_f32 v193, v193, v194\n"
        "v_add_f32 v195, v195, v196\n"
        "v_mfma_f32_16x16x32_bf16 a[184:187], v[12:15], v[88:91], a[184:187]\n"
        "v_cvt_pk_bf16_f32 v193, 0, v193\n"
        "v_cvt_pk_bf16_f32 v195, 0, v195\n"
        "v_mfma_f32_16x16x32_bf16 a[184:187], v[8:11], v[56:59], a[184:187]\n"
        "s_waitcnt vmcnt(13)\n"
        "s_barrier\n"
        "v_mul_f32 v194, v182, v186\n"
        "v_mfma_f32_16x16x32_bf16 a[188:191], v[8:11], v[60:63], a[188:191]\n"
        "global_load_lds_dwordx4 v234, s[56:57] offset:0\n"
        "ds_read_b128 v[8:11], v224 offset:34816\n"
        "v_mfma_f32_16x16x32_bf16 a[188:191], v[12:15], v[92:95], a[188:191]\n"
        "ds_read_b128 v[12:15], v225 offset:34816\n"
        "v_mul_f32 v196, v183, v186\n"
        "v_mfma_f32_16x16x32_bf16 a[156:159], v[4:7], v[92:95], a[156:159]\n"
        "ds_read_b128 v[4:7], v225 offset:32768\n"
        "v_cvt_pk_bf16_f32 v194, 0, v194\n"
        "v_mfma_f32_16x16x32_bf16 a[156:159], v[0:3], v[60:63], a[156:159]\n"
        "ds_read_b128 v[0:3], v224 offset:32768\n"
        "v_cvt_pk_bf16_f32 v196, 0, v196\n"
        "v_mfma_f32_16x16x32_bf16 a[192:195], v[32:35], v[16:19], a[192:195]\n"
        "global_load_lds_dwordx4 v235, s[56:57] offset:1024\n"
        "v_add_f32 v193, v193, v194\n"
        "v_mfma_f32_16x16x32_bf16 a[192:195], v[64:67], v[20:23], a[192:195]\n"
        "v_add_f32 v195, v195, v196\n"
        "v_cvt_pk_bf16_f32 v193, 0, v193\n"
        "v_mfma_f32_16x16x32_bf16 a[224:227], v[64:67], v[28:31], a[224:227]\n"
        "ds_read_b64_tr_b16 v[64:65], v216 offset:8192\n"
        "v_cvt_pk_bf16_f32 v195, 0, v195\n"
        "v_mfma_f32_16x16x32_bf16 a[224:227], v[32:35], v[24:27], a[224:227]\n"
        "ds_read_b64_tr_b16 v[32:33], v216 offset:0\n"
        "v_mul_f32 v194, v183, v185\n"
        "v_mfma_f32_16x16x32_bf16 a[228:231], v[36:39], v[24:27], a[228:231]\n"
        "ds_read_b64_tr_b16 v[34:35], v217 offset:0\n"
        "global_load_lds_dwordx4 v236, s[56:57] offset:2048\n"
        "v_mfma_f32_16x16x32_bf16 a[228:231], v[68:71], v[28:31], a[228:231]\n"
        "ds_read_b64_tr_b16 v[66:67], v217 offset:8192\n"
        "v_mul_f32 v196, v184, v185\n"
        "v_mfma_f32_16x16x32_bf16 a[196:199], v[68:71], v[20:23], a[196:199]\n"
        "ds_read_b64_tr_b16 v[68:69], v218 offset:8192\n"
        "v_cvt_pk_bf16_f32 v194, 0, v194\n"
        "v_mfma_f32_16x16x32_bf16 a[196:199], v[36:39], v[16:19], a[196:199]\n"
        "ds_read_b64_tr_b16 v[36:37], v218 offset:0\n"
        "v_cvt_pk_bf16_f32 v196, 0, v196\n"
        "v_mfma_f32_16x16x32_bf16 a[200:203], v[40:43], v[16:19], a[200:203]\n"
        "ds_read_b64_tr_b16 v[38:39], v219 offset:0\n"
        "global_load_dword v253, v173, s[82:83] offset:0\n"
        "v_mfma_f32_16x16x32_bf16 a[200:203], v[72:75], v[20:23], a[200:203]\n"
        "ds_read_b64_tr_b16 v[70:71], v219 offset:8192\n"
        "global_load_lds_dwordx4 v237, s[56:57] offset:3072\n"
        "s_add_u32 m0, s72, 0x1000\n"
        "v_mfma_f32_16x16x32_bf16 a[232:235], v[72:75], v[28:31], a[232:235]\n"
        "ds_read_b64_tr_b16 v[72:73], v220 offset:8192\n"
        "s_add_u32 s56, s56, 0x80\n"
        "s_addc_u32 s57, s57, 0\n"
        "v_mfma_f32_16x16x32_bf16 a[232:235], v[40:43], v[24:27], a[232:235]\n"
        "ds_read_b64_tr_b16 v[40:41], v220 offset:0\n"
        "s_add_u32 s82, s12, 0x500000\n"
        "s_addc_u32 s83, s13, 0\n"
        "v_mfma_f32_16x16x32_bf16 a[236:239], v[44:47], v[24:27], a[236:239]\n"
        "ds_read_b64_tr_b16 v[42:43], v221 offset:0\n"
        "v_add_f32 v193, v193, v194\n"
        "v_mfma_f32_16x16x32_bf16 a[236:239], v[76:79], v[28:31], a[236:239]\n"
        "ds_read_b64_tr_b16 v[74:75], v221 offset:8192\n"
        "global_load_lds_dwordx4 v234, s[58:59] offset:0\n"
        "v_mfma_f32_16x16x32_bf16 a[204:207], v[76:79], v[20:23], a[204:207]\n"
        "ds_read_b64_tr_b16 v[76:77], v222 offset:8192\n"
        "v_add_f32 v195, v195, v196\n"
        "v_mfma_f32_16x16x32_bf16 a[204:207], v[44:47], v[16:19], a[204:207]\n"
        "ds_read_b64_tr_b16 v[44:45], v222 offset:0\n"
        "v_cvt_pk_bf16_f32 v193, 0, v193\n"
        "v_mfma_f32_16x16x32_bf16 a[208:211], v[16:19], v[48:51], a[208:211]\n"
        "ds_read_b64_tr_b16 v[46:47], v223 offset:0\n"
        "v_cvt_pk_bf16_f32 v195, 0, v195\n"
        "v_mfma_f32_16x16x32_bf16 a[208:211], v[20:23], v[80:83], a[208:211]\n"
        "ds_read_b64_tr_b16 v[78:79], v223 offset:8192\n"
        "global_load_lds_dwordx4 v235, s[58:59] offset:1024\n"
        "v_mfma_f32_16x16x32_bf16 a[240:243], v[28:31], v[80:83], a[240:243]\n"
        "ds_read_b64_tr_b16 v[80:81], v216 offset:24576\n"
        "v_lshlrev_b32 v194, 16, v131\n"
        "v_mfma_f32_16x16x32_bf16 a[240:243], v[24:27], v[48:51], a[240:243]\n"
        "ds_read_b64_tr_b16 v[48:49], v216 offset:16384\n"
        "v_lshlrev_b32 v196, 16, v133\n"
        "v_mfma_f32_16x16x32_bf16 a[244:247], v[24:27], v[52:55], a[244:247]\n"
        "ds_read_b64_tr_b16 v[50:51], v217 offset:16384\n"
        "v_mul_f32 v193, v193, v194\n"
        "v_mfma_f32_16x16x32_bf16 a[244:247], v[28:31], v[84:87], a[244:247]\n"
        "ds_read_b64_tr_b16 v[82:83], v217 offset:24576\n"
        "global_load_lds_dwordx4 v236, s[58:59] offset:2048\n"
        "v_mfma_f32_16x16x32_bf16 a[212:215], v[20:23], v[84:87], a[212:215]\n"
        "ds_read_b64_tr_b16 v[84:85], v218 offset:24576\n"
        "v_mul_f32 v195, v195, v196\n"
        "v_mfma_f32_16x16x32_bf16 a[212:215], v[16:19], v[52:55], a[212:215]\n"
        "ds_read_b64_tr_b16 v[52:53], v218 offset:16384\n"
        "v_cvt_pk_bf16_f32 v200, v195, v193\n"
        "v_mfma_f32_16x16x32_bf16 a[216:219], v[16:19], v[56:59], a[216:219]\n"
        "ds_read_b64_tr_b16 v[54:55], v219 offset:16384\n"
        "v_lshlrev_b32 v185, 16, v140\n"
        "v_mfma_f32_16x16x32_bf16 a[216:219], v[20:23], v[88:91], a[216:219]\n"
        "ds_read_b64_tr_b16 v[86:87], v219 offset:24576\n"
        "global_load_lds_dwordx4 v237, s[58:59] offset:3072\n"
        "s_add_u32 m0, s73, 0x18000\n"
        "v_mfma_f32_16x16x32_bf16 a[248:251], v[28:31], v[88:91], a[248:251]\n"
        "ds_read_b64_tr_b16 v[88:89], v220 offset:24576\n"
        "s_add_u32 s58, s58, 0x80\n"
        "s_addc_u32 s59, s59, 0\n"
        "v_mfma_f32_16x16x32_bf16 a[248:251], v[24:27], v[56:59], a[248:251]\n"
        "ds_read_b64_tr_b16 v[56:57], v220 offset:16384\n"
        "v_and_b32 v186, 0xffff0000, v140\n"
        "v_mfma_f32_16x16x32_bf16 a[252:255], v[24:27], v[60:63], a[252:255]\n"
        "ds_read_b64_tr_b16 v[58:59], v221 offset:16384\n"
        "v_lshlrev_b32 v187, 16, v141\n"
        "v_mfma_f32_16x16x32_bf16 a[252:255], v[28:31], v[92:95], a[252:255]\n"
        "ds_read_b64_tr_b16 v[90:91], v221 offset:24576\n"
        "global_load_lds_dwordx4 v238, s[60:61] offset:0\n"
        "v_mfma_f32_16x16x32_bf16 a[220:223], v[20:23], v[92:95], a[220:223]\n"
        "ds_read_b64_tr_b16 v[92:93], v222 offset:24576\n"
        "v_and_b32 v188, 0xffff0000, v141\n"
        "v_mfma_f32_16x16x32_bf16 a[220:223], v[16:19], v[60:63], a[220:223]\n"
        "ds_read_b64_tr_b16 v[60:61], v222 offset:16384\n"
        "s_waitcnt lgkmcnt(8)\n"
        "v_mfma_f32_16x16x32_bf16 a[0:3], v[32:35], v[0:3], a[0:3]\n"
        "ds_read_b64_tr_b16 v[62:63], v223 offset:16384\n"
        "v_and_b32 v174, 0xffff0000, v97\n"
        "v_mfma_f32_16x16x32_bf16 a[0:3], v[64:67], v[4:7], a[0:3]\n"
        "ds_read_b64_tr_b16 v[94:95], v223 offset:24576\n"
        "global_load_lds_dwordx4 v239, s[60:61] offset:1024\n"
        "v_mfma_f32_16x16x32_bf16 a[32:35], v[64:67], v[12:15], a[32:35]\n"
        "ds_read_b128 v[16:19], v224 offset:36864\n"
        "v_and_b32 v175, 0xffff0000, v99\n"
        "v_mfma_f32_16x16x32_bf16 a[32:35], v[32:35], v[8:11], a[32:35]\n"
        "ds_read_b128 v[20:23], v225 offset:36864\n"
        "v_and_b32 v176, 0xffff0000, v101\n"
        "v_mfma_f32_16x16x32_bf16 a[36:39], v[36:39], v[8:11], a[36:39]\n"
        "ds_read_b128 v[24:27], v224 offset:38912\n"
        "v_and_b32 v177, 0xffff0000, v103\n"
        "v_mfma_f32_16x16x32_bf16 a[36:39], v[68:71], v[12:15], a[36:39]\n"
        "ds_read_b128 v[28:31], v225 offset:38912\n"
        "global_load_lds_dwordx4 v240, s[60:61] offset:2048\n"
        "v_mfma_f32_16x16x32_bf16 a[4:7], v[68:71], v[4:7], a[4:7]\n"
        "v_and_b32 v178, 0xffff0000, v105\n"
        "v_and_b32 v179, 0xffff0000, v107\n"
        "v_mfma_f32_16x16x32_bf16 a[4:7], v[36:39], v[0:3], a[4:7]\n"
        "v_and_b32 v180, 0xffff0000, v109\n"
        "v_and_b32 v181, 0xffff0000, v111\n"
        "v_mfma_f32_16x16x32_bf16 a[8:11], v[40:43], v[0:3], a[8:11]\n"
        "v_and_b32 v182, 0xffff0000, v113\n"
        "v_and_b32 v183, 0xffff0000, v115\n"
        "v_mfma_f32_16x16x32_bf16 a[8:11], v[72:75], v[4:7], a[8:11]\n"
        "global_load_lds_dwordx4 v241, s[60:61] offset:3072\n"
        "s_add_u32 m0, s73, 0x1c000\n"
        "v_mfma_f32_16x16x32_bf16 a[40:43], v[72:75], v[12:15], a[40:43]\n"
        "s_waitcnt lgkmcnt(3)\n"
        "v_and_b32 v184, 0xffff0000, v117\n"
        "v_mul_f32 v189, v174, v188\n"
        "v_mfma_f32_16x16x32_bf16 a[40:43], v[40:43], v[8:11], a[40:43]\n"
        "v_mul_f32 v191, v175, v188\n"
        "v_cvt_pk_bf16_f32 v189, 0, v189\n"
        "v_mfma_f32_16x16x32_bf16 a[44:47], v[44:47], v[8:11], a[44:47]\n"
        "v_cvt_pk_bf16_f32 v191, 0, v191\n"
        "v_mul_f32 v190, v175, v187\n"
        "v_mfma_f32_16x16x32_bf16 a[44:47], v[76:79], v[12:15], a[44:47]\n"
        "global_load_lds_dwordx4 v238, s[62:63] offset:0\n"
        "v_mfma_f32_16x16x32_bf16 a[12:15], v[76:79], v[4:7], a[12:15]\n"
        "LHA3_%=:\n"
        "s_cbranch_execz LHA7_%=\n"
        "v_mul_f32 v192, v176, v187\n"
        "v_cvt_pk_bf16_f32 v190, 0, v190\n"
        "v_mfma_f32_16x16x32_bf16 a[12:15], v[44:47], v[0:3], a[12:15]\n"
        "v_cvt_pk_bf16_f32 v192, 0, v192\n"
        "v_add_f32 v189, v189, v190\n"
        "v_mfma_f32_16x16x32_bf16 a[16:19], v[0:3], v[48:51], a[16:19]\n"
        "v_add_f32 v191, v191, v192\n"
        "v_cvt_pk_bf16_f32 v189, 0, v189\n"
        "v_mfma_f32_16x16x32_bf16 a[16:19], v[4:7], v[80:83], a[16:19]\n"
        "global_load_lds_dwordx4 v239, s[62:63] offset:1024\n"
        "v_mfma_f32_16x16x32_bf16 a[48:51], v[12:15], v[80:83], a[48:51]\n"
        "v_cvt_pk_bf16_f32 v191, 0, v191\n"
        "v_mul_f32 v190, v176, v186\n"
        "v_mfma_f32_16x16x32_bf16 a[48:51], v[8:11], v[48:51], a[48:51]\n"
        "v_mul_f32 v192, v177, v186\n"
        "v_cvt_pk_bf16_f32 v190, 0, v190\n"
        "v_mfma_f32_16x16x32_bf16 a[52:55], v[8:11], v[52:55], a[52:55]\n"
        "v_cvt_pk_bf16_f32 v192, 0, v192\n"
        "v_add_f32 v189, v189, v190\n"
        "v_mfma_f32_16x16x32_bf16 a[52:55], v[12:15], v[84:87], a[52:55]\n"
        "global_load_lds_dwordx4 v240, s[62:63] offset:2048\n"
        "v_mfma_f32_16x16x32_bf16 a[20:23], v[4:7], v[84:87], a[20:23]\n"
        "v_add_f32 v191, v191, v192\n"
        "v_cvt_pk_bf16_f32 v189, 0, v189\n"
        "v_mfma_f32_16x16x32_bf16 a[20:23], v[0:3], v[52:55], a[20:23]\n"
        "s_waitcnt lgkmcnt(0)\n"
        "v_cvt_pk_bf16_f32 v191, 0, v191\n"
        "v_mul_f32 v190, v177, v185\n"
        "v_mfma_f32_16x16x32_bf16 a[24:27], v[0:3], v[56:59], a[24:27]\n"
        "v_mul_f32 v192, v178, v185\n"
        "v_cvt_pk_bf16_f32 v190, 0, v190\n"
        "v_mfma_f32_16x16x32_bf16 a[24:27], v[4:7], v[88:91], a[24:27]\n"
        "global_load_lds_dwordx4 v241, s[62:63] offset:3072\n"
        "s_add_u32 m0, s72, 0x8000\n"
        "v_mfma_f32_16x16x32_bf16 a[56:59], v[12:15], v[88:91], a[56:59]\n"
        "v_cvt_pk_bf16_f32 v192, 0, v192\n"
        "v_add_f32 v189, v189, v190\n"
        "v_mfma_f32_16x16x32_bf16 a[56:59], v[8:11], v[56:59], a[56:59]\n"
        "v_add_f32 v191, v191, v192\n"
        "v_cvt_pk_bf16_f32 v189, 0, v189\n"
        "v_mfma_f32_16x16x32_bf16 a[60:63], v[8:11], v[60:63], a[60:63]\n"
        "ds_read_b128 v[8:11], v224 offset:43008\n"
        "v_cvt_pk_bf16_f32 v191, 0, v191\n"
        "v_mfma_f32_16x16x32_bf16 a[60:63], v[12:15], v[92:95], a[60:63]\n"
        "ds_read_b128 v[12:15], v225 offset:43008\n"
        "v_and_b32 v190, 0xffff0000, v119\n"
        "v_mfma_f32_16x16x32_bf16 a[28:31], v[4:7], v[92:95], a[28:31]\n"
        "ds_read_b128 v[4:7], v225 offset:40960\n"
        "v_and_b32 v192, 0xffff0000, v121\n"
        "v_mfma_f32_16x16x32_bf16 a[28:31], v[0:3], v[60:63], a[28:31]\n"
        "ds_read_b128 v[0:3], v224 offset:40960\n"
        "v_mul_f32 v189, v189, v190\n"
        "v_mfma_f32_16x16x32_bf16 a[64:67], v[32:35], v[16:19], a[64:67]\n"
        "global_store_dwordx4 v171, v[200:203], s[76:77] offset:-4096 sc1\n"
        "s_waitcnt vmcnt(30)\n"
        "v_mul_f32 v191, v191, v192\n"
        "v_cvt_pk_bf16_f32 v211, v191, v189\n"
        "v_mul_f32 v193, v176, v188\n"
        "v_mfma_f32_16x16x32_bf16 a[64:67], v[64:67], v[20:23], a[64:67]\n"
        "v_mul_f32 v195, v177, v188\n"
        "v_cvt_pk_bf16_f32 v193, 0, v193\n"
        "v_mfma_f32_16x16x32_bf16 a[96:99], v[64:67], v[28:31], a[96:99]\n"
        "v_cvt_pk_bf16_f32 v195, 0, v195\n"
        "v_mul_f32 v194, v177, v187\n"
        "v_mfma_f32_16x16x32_bf16 a[96:99], v[32:35], v[24:27], a[96:99]\n"
        "v_mul_f32 v196, v178, v187\n"
        "v_cvt_pk_bf16_f32 v194, 0, v194\n"
        "v_mfma_f32_16x16x32_bf16 a[100:103], v[36:39], v[24:27], a[100:103]\n"
        "v_cvt_pk_bf16_f32 v196, 0, v196\n"
        "v_add_f32 v193, v193, v194\n"
        "v_mfma_f32_16x16x32_bf16 a[100:103], v[68:71], v[28:31], a[100:103]\n"
        "v_add_f32 v195, v195, v196\n"
        "v_cvt_pk_bf16_f32 v193, 0, v193\n"
        "v_mfma_f32_16x16x32_bf16 a[68:71], v[68:71], v[20:23], a[68:71]\n"
        "v_cvt_pk_bf16_f32 v195, 0, v195\n"
        "v_mul_f32 v194, v178, v186\n"
        "v_mfma_f32_16x16x32_bf16 a[68:71], v[36:39], v[16:19], a[68:71]\n"
        "v_mul_f32 v196, v179, v186\n"
        "v_cvt_pk_bf16_f32 v194, 0, v194\n"
        "v_mfma_f32_16x16x32_bf16 a[72:75], v[40:43], v[16:19], a[72:75]\n"
        "v_cvt_pk_bf16_f32 v196, 0, v196\n"
        "v_add_f32 v193, v193, v194\n"
        "v_mfma_f32_16x16x32_bf16 a[72:75], v[72:75], v[20:23], a[72:75]\n"
        "v_add_f32 v195, v195, v196\n"
        "v_cvt_pk_bf16_f32 v193, 0, v193\n"
        "v_mfma_f32_16x16x32_bf16 a[104:107], v[72:75], v[28:31], a[104:107]\n"
        "v_cvt_pk_bf16_f32 v195, 0, v195\n"
        "v_mul_f32 v194, v179, v185\n"
        "v_mfma_f32_16x16x32_bf16 a[104:107], v[40:43], v[24:27], a[104:107]\n"
        "v_mul_f32 v196, v180, v185\n"
        "v_cvt_pk_bf16_f32 v194, 0, v194\n"
        "v_mfma_f32_16x16x32_bf16 a[108:111], v[44:47], v[24:27], a[108:111]\n"
        "v_cvt_pk_bf16_f32 v196, 0, v196\n"
        "v_add_f32 v193, v193, v194\n"
        "v_mfma_f32_16x16x32_bf16 a[108:111], v[76:79], v[28:31], a[108:111]\n"
        "v_add_f32 v195, v195, v196\n"
        "v_cvt_pk_bf16_f32 v193, 0, v193\n"
        "v_mfma_f32_16x16x32_bf16 a[76:79], v[76:79], v[20:23], a[76:79]\n"
        "v_cvt_pk_bf16_f32 v195, 0, v195\n"
        "v_and_b32 v194, 0xffff0000, v123\n"
        "v_mfma_f32_16x16x32_bf16 a[76:79], v[44:47], v[16:19], a[76:79]\n"
        "v_and_b32 v196, 0xffff0000, v125\n"
        "v_mul_f32 v193, v193, v194\n"
        "v_mfma_f32_16x16x32_bf16 a[80:83], v[16:19], v[48:51], a[80:83]\n"
        "v_mul_f32 v195, v195, v196\n"
        "v_cvt_pk_bf16_f32 v210, v195, v193\n"
        "v_mfma_f32_16x16x32_bf16 a[80:83], v[20:23], v[80:83], a[80:83]\n"
        "v_mul_f32 v189, v178, v188\n"
        "v_mul_f32 v191, v179, v188\n"
        "v_mfma_f32_16x16x32_bf16 a[112:115], v[28:31], v[80:83], a[112:115]\n"
        "v_cvt_pk_bf16_f32 v189, 0, v189\n"
        "v_cvt_pk_bf16_f32 v191, 0, v191\n"
        "v_mfma_f32_16x16x32_bf16 a[112:115], v[24:27], v[48:51], a[112:115]\n"
        "v_mul_f32 v190, v179, v187\n"
        "v_mul_f32 v192, v180, v187\n"
        "v_mfma_f32_16x16x32_bf16 a[116:119], v[24:27], v[52:55], a[116:119]\n"
        "v_cvt_pk_bf16_f32 v190, 0, v190\n"
        "v_cvt_pk_bf16_f32 v192, 0, v192\n"
        "v_mfma_f32_16x16x32_bf16 a[116:119], v[28:31], v[84:87], a[116:119]\n"
        "s_waitcnt lgkmcnt(0)\n"
        "v_add_f32 v189, v189, v190\n"
        "v_add_f32 v191, v191, v192\n"
        "v_mfma_f32_16x16x32_bf16 a[84:87], v[20:23], v[84:87], a[84:87]\n"
        "v_cvt_pk_bf16_f32 v189, 0, v189\n"
        "v_cvt_pk_bf16_f32 v191, 0, v191\n"
        "v_mfma_f32_16x16x32_bf16 a[84:87], v[16:19], v[52:55], a[84:87]\n"
        "global_load_dword v253, v173, s[82:83] offset:0\n"
        "s_add_u32 s82, s12, 0x600000\n"
        "s_addc_u32 s83, s13, 0\n"
        "v_mfma_f32_16x16x32_bf16 a[88:91], v[16:19], v[56:59], a[88:91]\n"
        "v_mul_f32 v190, v180, v186\n"
        "v_mul_f32 v192, v181, v186\n"
        "v_mfma_f32_16x16x32_bf16 a[88:91], v[20:23], v[88:91], a[88:91]\n"
        "v_cvt_pk_bf16_f32 v190, 0, v190\n"
        "v_cvt_pk_bf16_f32 v192, 0, v192\n"
        "v_mfma_f32_16x16x32_bf16 a[120:123], v[28:31], v[88:91], a[120:123]\n"
        "v_add_f32 v189, v189, v190\n"
        "v_add_f32 v191, v191, v192\n"
        "v_mfma_f32_16x16x32_bf16 a[120:123], v[24:27], v[56:59], a[120:123]\n"
        "v_cvt_pk_bf16_f32 v189, 0, v189\n"
        "v_cvt_pk_bf16_f32 v191, 0, v191\n"
        "v_mfma_f32_16x16x32_bf16 a[124:127], v[24:27], v[60:63], a[124:127]\n"
        "ds_read_b128 v[24:27], v224 offset:47104\n"
        "v_mul_f32 v190, v181, v185\n"
        "v_mfma_f32_16x16x32_bf16 a[124:127], v[28:31], v[92:95], a[124:127]\n"
        "ds_read_b128 v[28:31], v225 offset:47104\n"
        "v_mul_f32 v192, v182, v185\n"
        "v_mfma_f32_16x16x32_bf16 a[92:95], v[20:23], v[92:95], a[92:95]\n"
        "ds_read_b128 v[20:23], v225 offset:45056\n"
        "v_cvt_pk_bf16_f32 v190, 0, v190\n"
        "v_mfma_f32_16x16x32_bf16 a[92:95], v[16:19], v[60:63], a[92:95]\n"
        "ds_read_b128 v[16:19], v224 offset:45056\n"
        "v_cvt_pk_bf16_f32 v192, 0, v192\n"
        "v_mfma_f32_16x16x32_bf16 a[128:131], v[32:35], v[0:3], a[128:131]\n"
        "v_add_f32 v189, v189, v190\n"
        "v_add_f32 v191, v191, v192\n"
        "v_mfma_f32_16x16x32_bf16 a[128:131], v[64:67], v[4:7], a[128:131]\n"
        "v_cvt_pk_bf16_f32 v189, 0, v189\n"
        "v_cvt_pk_bf16_f32 v191, 0, v191\n"
        "v_mfma_f32_16x16x32_bf16 a[160:163], v[64:67], v[12:15], a[160:163]\n"
        "v_and_b32 v190, 0xffff0000, v127\n"
        "v_and_b32 v192, 0xffff0000, v129\n"
        "v_mfma_f32_16x16x32_bf16 a[160:163], v[32:35], v[8:11], a[160:163]\n"
        "v_mul_f32 v189, v189, v190\n"
        "v_mul_f32 v191, v191, v192\n"
        "v_mfma_f32_16x16x32_bf16 a[164:167], v[36:39], v[8:11], a[164:167]\n"
        "s_waitcnt vmcnt(20) lgkmcnt(3)\n"
        "v_cvt_pk_bf16_f32 v209, v191, v189\n"
        "v_mul_f32 v193, v180, v188\n"
        "v_mfma_f32_16x16x32_bf16 a[164:167], v[68:71], v[12:15], a[164:167]\n"
        "v_mul_f32 v195, v181, v188\n"
        "v_cvt_pk_bf16_f32 v193, 0, v193\n"
        "v_mfma_f32_16x16x32_bf16 a[132:135], v[68:71], v[4:7], a[132:135]\n"
        "v_cvt_pk_bf16_f32 v195, 0, v195\n"
        "v_mul_f32 v194, v181, v187\n"
        "v_mfma_f32_16x16x32_bf16 a[132:135], v[36:39], v[0:3], a[132:135]\n"
        "v_mul_f32 v196, v182, v187\n"
        "v_cvt_pk_bf16_f32 v194, 0, v194\n"
        "v_mfma_f32_16x16x32_bf16 a[136:139], v[40:43], v[0:3], a[136:139]\n"
        "v_cvt_pk_bf16_f32 v196, 0, v196\n"
        "v_add_f32 v193, v193, v194\n"
        "v_mfma_f32_16x16x32_bf16 a[136:139], v[72:75], v[4:7], a[136:139]\n"
        "v_add_f32 v195, v195, v196\n"
        "v_cvt_pk_bf16_f32 v193, 0, v193\n"
        "v_mfma_f32_16x16x32_bf16 a[168:171], v[72:75], v[12:15], a[168:171]\n"
        "v_cvt_pk_bf16_f32 v195, 0, v195\n"
        "v_mul_f32 v194, v182, v186\n"
        "v_mfma_f32_16x16x32_bf16 a[168:171], v[40:43], v[8:11], a[168:171]\n"
        "v_mul_f32 v196, v183, v186\n"
        "v_cvt_pk_bf16_f32 v194, 0, v194\n"
        "v_mfma_f32_16x16x32_bf16 a[172:175], v[44:47], v[8:11], a[172:175]\n"
        "v_cvt_pk_bf16_f32 v196, 0, v196\n"
        "v_add_f32 v193, v193, v194\n"
        "v_mfma_f32_16x16x32_bf16 a[172:175], v[76:79], v[12:15], a[172:175]\n"
        "v_add_f32 v195, v195, v196\n"
        "v_cvt_pk_bf16_f32 v193, 0, v193\n"
        "v_mfma_f32_16x16x32_bf16 a[140:143], v[76:79], v[4:7], a[140:143]\n"
        "v_cvt_pk_bf16_f32 v195, 0, v195\n"
        "v_mul_f32 v194, v183, v185\n"
        "v_mfma_f32_16x16x32_bf16 a[140:143], v[44:47], v[0:3], a[140:143]\n"
        "v_mul_f32 v196, v184, v185\n"
        "v_cvt_pk_bf16_f32 v194, 0, v194\n"
        "v_mfma_f32_16x16x32_bf16 a[144:147], v[0:3], v[48:51], a[144:147]\n"
        "v_cvt_pk_bf16_f32 v196, 0, v196\n"
        "v_add_f32 v193, v193, v194\n"
        "v_mfma_f32_16x16x32_bf16 a[144:147], v[4:7], v[80:83], a[144:147]\n"
        "v_add_f32 v195, v195, v196\n"
        "v_cvt_pk_bf16_f32 v193, 0, v193\n"
        "v_mfma_f32_16x16x32_bf16 a[176:179], v[12:15], v[80:83], a[176:179]\n"
        "v_cvt_pk_bf16_f32 v195, 0, v195\n"
        "v_and_b32 v194, 0xffff0000, v131\n"
        "v_mfma_f32_16x16x32_bf16 a[176:179], v[8:11], v[48:51], a[176:179]\n"
        "v_and_b32 v196, 0xffff0000, v133\n"
        "v_mul_f32 v193, v193, v194\n"
        "v_mfma_f32_16x16x32_bf16 a[180:183], v[8:11], v[52:55], a[180:183]\n"
        "v_mul_f32 v195, v195, v196\n"
        "v_cvt_pk_bf16_f32 v208, v195, v193\n"
        "v_mfma_f32_16x16x32_bf16 a[180:183], v[12:15], v[84:87], a[180:183]\n"
        "global_store_dwordx4 v171, v[208:211], s[76:77] offset:0 sc1\n"
        "s_waitcnt lgkmcnt(0)\n"
        "v_readfirstlane_b32 s54, v172\n"
        "v_readfirstlane_b32 s86, v250\n"
        "s_lshl_b32 s54, s54, 16\n"
        "v_mfma_f32_16x16x32_bf16 a[148:151], v[4:7], v[84:87], a[148:151]\n"
        "s_add_u32 s76, s38, 0x3920040\n"
        "s_addc_u32 s77, s39, 0\n"
        "v_readfirstlane_b32 s87, v251\n"
        "v_mfma_f32_16x16x32_bf16 a[148:151], v[0:3], v[52:55], a[148:151]\n"
        "s_add_u32 s86, s86, 0x1\n"
        "s_addc_u32 s87, s87, 0\n"
        "v_mov_b32 v250, s86\n"
        "v_mfma_f32_16x16x32_bf16 a[152:155], v[0:3], v[56:59], a[152:155]\n"
        "v_mov_b32 v251, s87\n"
        "v_lshlrev_b32 v174, 16, v158\n"
        "v_mfma_f32_16x16x32_bf16 a[152:155], v[4:7], v[88:91], a[152:155]\n"
        "v_and_b32 v175, 0xffff0000, v158\n"
        "v_lshlrev_b32 v176, 16, v159\n"
        "v_mfma_f32_16x16x32_bf16 a[184:187], v[12:15], v[88:91], a[184:187]\n"
        "v_and_b32 v177, 0xffff0000, v159\n"
        "v_lshlrev_b32 v178, 16, v160\n"
        "v_mfma_f32_16x16x32_bf16 a[184:187], v[8:11], v[56:59], a[184:187]\n"
        "s_waitcnt vmcnt(11)\n"
        "s_barrier\n"
        "v_and_b32 v179, 0xffff0000, v160\n"
        "v_mfma_f32_16x16x32_bf16 a[188:191], v[8:11], v[60:63], a[188:191]\n"
        "global_load_lds_dwordx4 v234, s[56:57] offset:0\n"
        "ds_read_b128 v[8:11], v224 offset:2048\n"
        "v_mfma_f32_16x16x32_bf16 a[188:191], v[12:15], v[92:95], a[188:191]\n"
        "ds_read_b128 v[12:15], v225 offset:2048\n"
        "v_lshlrev_b32 v180, 16, v161\n"
        "v_mfma_f32_16x16x32_bf16 a[156:159], v[4:7], v[92:95], a[156:159]\n"
        "ds_read_b128 v[4:7], v225 offset:0\n"
        "v_and_b32 v181, 0xffff0000, v161\n"
        "v_mfma_f32_16x16x32_bf16 a[156:159], v[0:3], v[60:63], a[156:159]\n"
        "ds_read_b128 v[0:3], v224 offset:0\n"
        "v_lshlrev_b32 v182, 16, v162\n"
        "v_mfma_f32_16x16x32_bf16 a[192:195], v[32:35], v[16:19], a[192:195]\n"
        "global_load_lds_dwordx4 v235, s[56:57] offset:1024\n"
        "v_and_b32 v183, 0xffff0000, v162\n"
        "v_mfma_f32_16x16x32_bf16 a[192:195], v[64:67], v[20:23], a[192:195]\n"
        "v_lshlrev_b32 v184, 16, v163\n"
        "v_and_b32 v185, 0xffff0000, v163\n"
        "v_mfma_f32_16x16x32_bf16 a[224:227], v[64:67], v[28:31], a[224:227]\n"
        "ds_read_b64_tr_b16 v[64:65], v226 offset:8192\n"
        "v_lshlrev_b32 v186, 16, v164\n"
        "v_mfma_f32_16x16x32_bf16 a[224:227], v[32:35], v[24:27], a[224:227]\n"
        "ds_read_b64_tr_b16 v[32:33], v226 offset:0\n"
        "v_and_b32 v187, 0xffff0000, v164\n"
        "v_mfma_f32_16x16x32_bf16 a[228:231], v[36:39], v[24:27], a[228:231]\n"
        "ds_read_b64_tr_b16 v[34:35], v227 offset:0\n"
        "global_load_lds_dwordx4 v236, s[56:57] offset:2048\n"
        "v_mfma_f32_16x16x32_bf16 a[228:231], v[68:71], v[28:31], a[228:231]\n"
        "ds_read_b64_tr_b16 v[66:67], v227 offset:8192\n"
        "v_lshlrev_b32 v188, 16, v165\n"
        "v_mfma_f32_16x16x32_bf16 a[196:199], v[68:71], v[20:23], a[196:199]\n"
        "ds_read_b64_tr_b16 v[68:69], v228 offset:8192\n"
        "v_and_b32 v189, 0xffff0000, v165\n"
        "v_mfma_f32_16x16x32_bf16 a[196:199], v[36:39], v[16:19], a[196:199]\n"
        "ds_read_b64_tr_b16 v[36:37], v228 offset:0\n"
        "v_lshlrev_b32 v192, 16, v142\n"
        "v_mfma_f32_16x16x32_bf16 a[200:203], v[40:43], v[16:19], a[200:203]\n"
        "ds_read_b64_tr_b16 v[38:39], v229 offset:0\n"
        "global_load_lds_dwordx4 v237, s[56:57] offset:3072\n"
        "s_add_u32 m0, s72, 0x9000\n"
        "v_mfma_f32_16x16x32_bf16 a[200:203], v[72:75], v[20:23], a[200:203]\n"
        "ds_read_b64_tr_b16 v[70:71], v229 offset:8192\n"
        "v_and_b32 v193, 0xffff0000, v142\n"
        "v_mfma_f32_16x16x32_bf16 a[232:235], v[72:75], v[28:31], a[232:235]\n"
        "ds_read_b64_tr_b16 v[72:73], v230 offset:8192\n"
        "v_mul_f32 v192, v192, v174\n"
        "v_mfma_f32_16x16x32_bf16 a[232:235], v[40:43], v[24:27], a[232:235]\n"
        "ds_read_b64_tr_b16 v[40:41], v230 offset:0\n"
        "v_mul_f32 v193, v193, v175\n"
        "v_mfma_f32_16x16x32_bf16 a[236:239], v[44:47], v[24:27], a[236:239]\n"
        "ds_read_b64_tr_b16 v[42:43], v231 offset:0\n"
        "global_load_lds_dwordx4 v234, s[58:59] offset:0\n"
        "v_mfma_f32_16x16x32_bf16 a[236:239], v[76:79], v[28:31], a[236:239]\n"
        "ds_read_b64_tr_b16 v[74:75], v231 offset:8192\n"
        "v_cvt_pk_bf16_f32 v192, 0, v192\n"
        "v_mfma_f32_16x16x32_bf16 a[204:207], v[76:79], v[20:23], a[204:207]\n"
        "ds_read_b64_tr_b16 v[76:77], v232 offset:8192\n"
        "v_cvt_pk_bf16_f32 v193, 0, v193\n"
        "v_mfma_f32_16x16x32_bf16 a[204:207], v[44:47], v[16:19], a[204:207]\n"
        "ds_read_b64_tr_b16 v[44:45], v232 offset:0\n"
        "v_add_f32 v190, v192, v193\n"
        "v_mfma_f32_16x16x32_bf16 a[208:211], v[16:19], v[48:51], a[208:211]\n"
        "ds_read_b64_tr_b16 v[46:47], v233 offset:0\n"
        "global_load_lds_dwordx4 v235, s[58:59] offset:1024\n"
        "v_mfma_f32_16x16x32_bf16 a[208:211], v[20:23], v[80:83], a[208:211]\n"
        "ds_read_b64_tr_b16 v[78:79], v233 offset:8192\n"
        "v_lshlrev_b32 v194, 16, v143\n"
        "v_mfma_f32_16x16x32_bf16 a[240:243], v[28:31], v[80:83], a[240:243]\n"
        "ds_read_b64_tr_b16 v[80:81], v226 offset:24576\n"
        "v_and_b32 v195, 0xffff0000, v143\n"
        "v_mfma_f32_16x16x32_bf16 a[240:243], v[24:27], v[48:51], a[240:243]\n"
        "ds_read_b64_tr_b16 v[48:49], v226 offset:16384\n"
        "v_mul_f32 v194, v194, v176\n"
        "v_mfma_f32_16x16x32_bf16 a[244:247], v[24:27], v[52:55], a[244:247]\n"
        "ds_read_b64_tr_b16 v[50:51], v227 offset:16384\n"
        "global_load_lds_dwordx4 v236, s[58:59] offset:2048\n"
        "v_mfma_f32_16x16x32_bf16 a[244:247], v[28:31], v[84:87], a[244:247]\n"
        "ds_read_b64_tr_b16 v[82:83], v227 offset:24576\n"
        "v_mul_f32 v195, v195, v177\n"
        "v_mfma_f32_16x16x32_bf16 a[212:215], v[20:23], v[84:87], a[212:215]\n"
        "ds_read_b64_tr_b16 v[84:85], v228 offset:24576\n"
        "v_cvt_pk_bf16_f32 v194, 0, v194\n"
        "v_mfma_f32_16x16x32_bf16 a[212:215], v[16:19], v[52:55], a[212:215]\n"
        "ds_read_b64_tr_b16 v[52:53], v228 offset:16384\n"
        "v_cvt_pk_bf16_f32 v195, 0, v195\n"
        "v_mfma_f32_16x16x32_bf16 a[216:219], v[16:19], v[56:59], a[216:219]\n"
        "ds_read_b64_tr_b16 v[54:55], v229 offset:16384\n"
        "global_load_lds_dwordx4 v237, s[58:59] offset:3072\n"
        "s_add_u32 m0, s73, 0x20000\n"
        "v_mfma_f32_16x16x32_bf16 a[216:219], v[20:23], v[88:91], a[216:219]\n"
        "ds_read_b64_tr_b16 v[86:87], v229 offset:24576\n"
        "v_add_f32 v194, v194, v195\n"
        "v_mfma_f32_16x16x32_bf16 a[248:251], v[28:31], v[88:91], a[248:251]\n"
        "ds_read_b64_tr_b16 v[88:89], v230 offset:24576\n"
        "v_add_f32 v190, v190, v194\n"
        "v_mfma_f32_16x16x32_bf16 a[248:251], v[24:27], v[56:59], a[248:251]\n"
        "ds_read_b64_tr_b16 v[56:57], v230 offset:16384\n"
        "v_lshlrev_b32 v192, 16, v144\n"
        "v_mfma_f32_16x16x32_bf16 a[252:255], v[24:27], v[60:63], a[252:255]\n"
        "ds_read_b64_tr_b16 v[58:59], v231 offset:16384\n"
        "global_load_lds_dwordx4 v238, s[68:69] offset:0\n"
        "v_mfma_f32_16x16x32_bf16 a[252:255], v[28:31], v[92:95], a[252:255]\n"
        "ds_read_b64_tr_b16 v[90:91], v231 offset:24576\n"
        "v_and_b32 v193, 0xffff0000, v144\n"
        "v_mfma_f32_16x16x32_bf16 a[220:223], v[20:23], v[92:95], a[220:223]\n"
        "ds_read_b64_tr_b16 v[92:93], v232 offset:24576\n"
        "v_mul_f32 v192, v192, v178\n"
        "v_mfma_f32_16x16x32_bf16 a[220:223], v[16:19], v[60:63], a[220:223]\n"
        "ds_read_b64_tr_b16 v[60:61], v232 offset:16384\n"
        "s_waitcnt lgkmcnt(8)\n"
        "v_mfma_f32_16x16x32_bf16 a[0:3], v[32:35], v[0:3], a[0:3]\n"
        "ds_read_b64_tr_b16 v[62:63], v233 offset:16384\n"
        "global_load_lds_dwordx4 v239, s[68:69] offset:1024\n"
        "v_mfma_f32_16x16x32_bf16 a[0:3], v[64:67], v[4:7], a[0:3]\n"
        "ds_read_b64_tr_b16 v[94:95], v233 offset:24576\n"
        "v_mul_f32 v193, v193, v179\n"
        "v_mfma_f32_16x16x32_bf16 a[32:35], v[64:67], v[12:15], a[32:35]\n"
        "ds_read_b128 v[16:19], v224 offset:4096\n"
        "v_cvt_pk_bf16_f32 v192, 0, v192\n"
        "v_mfma_f32_16x16x32_bf16 a[32:35], v[32:35], v[8:11], a[32:35]\n"
        "ds_read_b128 v[20:23], v225 offset:4096\n"
        "v_cvt_pk_bf16_f32 v193, 0, v193\n"
        "v_mfma_f32_16x16x32_bf16 a[36:39], v[36:39], v[8:11], a[36:39]\n"
        "ds_read_b128 v[24:27], v224 offset:6144\n"
        "global_load_lds_dwordx4 v240, s[68:69] offset:2048\n"
        "v_mfma_f32_16x16x32_bf16 a[36:39], v[68:71], v[12:15], a[36:39]\n"
        "ds_read_b128 v[28:31], v225 offset:6144\n"
        "v_add_f32 v192, v192, v193\n"
        "v_mfma_f32_16x16x32_bf16 a[4:7], v[68:71], v[4:7], a[4:7]\n"
        "v_add_f32 v190, v190, v192\n"
        "v_lshlrev_b32 v194, 16, v145\n"
        "v_mfma_f32_16x16x32_bf16 a[4:7], v[36:39], v[0:3], a[4:7]\n"
        "v_and_b32 v195, 0xffff0000, v145\n"
        "v_mul_f32 v194, v194, v180\n"
        "v_mfma_f32_16x16x32_bf16 a[8:11], v[40:43], v[0:3], a[8:11]\n"
        "global_load_dword v253, v173, s[82:83] offset:0\n"
        "v_mfma_f32_16x16x32_bf16 a[8:11], v[72:75], v[4:7], a[8:11]\n"
        "global_load_lds_dwordx4 v241, s[68:69] offset:3072\n"
        "s_add_u32 s68, s68, 0x80000\n"
        "s_addc_u32 s69, s69, 0\n"
        "v_mfma_f32_16x16x32_bf16 a[40:43], v[72:75], v[12:15], a[40:43]\n"
        "s_add_u32 m0, s73, 0x24000\n"
        "s_add_u32 s82, s12, 0x700000\n"
        "s_addc_u32 s83, s13, 0\n"
        "v_mfma_f32_16x16x32_bf16 a[40:43], v[40:43], v[8:11], a[40:43]\n"
        "s_waitcnt lgkmcnt(2)\n"
        "v_mul_f32 v195, v195, v181\n"
        "v_cvt_pk_bf16_f32 v194, 0, v194\n"
        "v_mfma_f32_16x16x32_bf16 a[44:47], v[44:47], v[8:11], a[44:47]\n"
        "v_cvt_pk_bf16_f32 v195, 0, v195\n"
        "v_add_f32 v194, v194, v195\n"
        "v_mfma_f32_16x16x32_bf16 a[44:47], v[76:79], v[12:15], a[44:47]\n"
        "global_load_lds_dwordx4 v238, s[68:69] offset:0\n"
        "v_add_f32 v190, v190, v194\n"
        "v_mfma_f32_16x16x32_bf16 a[12:15], v[76:79], v[4:7], a[12:15]\n"
        "v_lshlrev_b32 v192, 16, v146\n"
        "v_and_b32 v193, 0xffff0000, v146\n"
        "v_mfma_f32_16x16x32_bf16 a[12:15], v[44:47], v[0:3], a[12:15]\n"
        "v_mul_f32 v192, v192, v182\n"
        "v_mul_f32 v193, v193, v183\n"
        "v_mfma_f32_16x16x32_bf16 a[16:19], v[0:3], v[48:51], a[16:19]\n"
        "v_cvt_pk_bf16_f32 v192, 0, v192\n"
        "v_cvt_pk_bf16_f32 v193, 0, v193\n"
        "v_mfma_f32_16x16x32_bf16 a[16:19], v[4:7], v[80:83], a[16:19]\n"
        "global_load_lds_dwordx4 v239, s[68:69] offset:1024\n"
        "v_add_f32 v192, v192, v193\n"
        "v_mfma_f32_16x16x32_bf16 a[48:51], v[12:15], v[80:83], a[48:51]\n"
        "v_add_f32 v190, v190, v192\n"
        "v_lshlrev_b32 v194, 16, v147\n"
        "v_mfma_f32_16x16x32_bf16 a[48:51], v[8:11], v[48:51], a[48:51]\n"
        "v_and_b32 v195, 0xffff0000, v147\n"
        "v_mul_f32 v194, v194, v184\n"
        "v_mfma_f32_16x16x32_bf16 a[52:55], v[8:11], v[52:55], a[52:55]\n"
        "v_mul_f32 v195, v195, v185\n"
        "v_cvt_pk_bf16_f32 v194, 0, v194\n"
        "v_mfma_f32_16x16x32_bf16 a[52:55], v[12:15], v[84:87], a[52:55]\n"
        "global_load_lds_dwordx4 v240, s[68:69] offset:2048\n"
        "v_cvt_pk_bf16_f32 v195, 0, v195\n"
        "v_mfma_f32_16x16x32_bf16 a[20:23], v[4:7], v[84:87], a[20:23]\n"
        "v_add_f32 v194, v194, v195\n"
        "v_add_f32 v190, v190, v194\n"
        "v_mfma_f32_16x16x32_bf16 a[20:23], v[0:3], v[52:55], a[20:23]\n"
        "s_waitcnt lgkmcnt(0)\n"
        "v_lshlrev_b32 v192, 16, v148\n"
        "v_and_b32 v193, 0xffff0000, v148\n"
        "v_mfma_f32_16x16x32_bf16 a[24:27], v[0:3], v[56:59], a[24:27]\n"
        "s_and_b32 s53, s40, 1\n"
        "v_mul_f32 v192, v192, v186\n"
        "v_mul_f32 v193, v193, v187\n"
        "v_mfma_f32_16x16x32_bf16 a[24:27], v[4:7], v[88:91], a[24:27]\n"
        "global_load_lds_dwordx4 v241, s[68:69] offset:3072\n"
        "s_add_u32 s68, s68, 0x80000\n"
        "s_addc_u32 s69, s69, 0\n"
        "v_mfma_f32_16x16x32_bf16 a[56:59], v[12:15], v[88:91], a[56:59]\n"
        "s_add_u32 m0, s72, 0x0\n"
        "v_cvt_pk_bf16_f32 v192, 0, v192\n"
        "v_cvt_pk_bf16_f32 v193, 0, v193\n"
        "v_mfma_f32_16x16x32_bf16 a[56:59], v[8:11], v[56:59], a[56:59]\n"
        "s_lshl_b32 s53, s53, 10\n"
        "v_add_f32 v192, v192, v193\n"
        "v_add_f32 v190, v190, v192\n"
        "v_mfma_f32_16x16x32_bf16 a[60:63], v[8:11], v[60:63], a[60:63]\n"
        "ds_read_b128 v[8:11], v224 offset:10240\n"
        "s_sub_u32 s53, 0x8000, s53\n"
        "v_lshlrev_b32 v194, 16, v149\n"
        "v_mfma_f32_16x16x32_bf16 a[60:63], v[12:15], v[92:95], a[60:63]\n"
        "ds_read_b128 v[12:15], v225 offset:10240\n"
        "v_and_b32 v195, 0xffff0000, v149\n"
        "v_mfma_f32_16x16x32_bf16 a[28:31], v[4:7], v[92:95], a[28:31]\n"
        "ds_read_b128 v[4:7], v225 offset:8192\n"
        "v_mul_f32 v194, v194, v188\n"
        "v_mfma_f32_16x16x32_bf16 a[28:31], v[0:3], v[60:63], a[28:31]\n"
        "ds_read_b128 v[0:3], v224 offset:8192\n"
        "v_mul_f32 v195, v195, v189\n"
        "v_mfma_f32_16x16x32_bf16 a[64:67], v[32:35], v[16:19], a[64:67]\n"
        "v_cvt_pk_bf16_f32 v194, 0, v194\n"
        "v_cvt_pk_bf16_f32 v195, 0, v195\n"
        "v_mfma_f32_16x16x32_bf16 a[64:67], v[64:67], v[20:23], a[64:67]\n"
        "v_add_f32 v194, v194, v195\n"
        "v_add_f32 v190, v190, v194\n"
        "v_mfma_f32_16x16x32_bf16 a[96:99], v[64:67], v[28:31], a[96:99]\n"
        "v_lshlrev_b32 v192, 16, v150\n"
        "v_and_b32 v193, 0xffff0000, v150\n"
        "v_mfma_f32_16x16x32_bf16 a[96:99], v[32:35], v[24:27], a[96:99]\n"
        "v_mul_f32 v192, v192, v174\n"
        "v_mul_f32 v193, v193, v175\n"
        "v_mfma_f32_16x16x32_bf16 a[100:103], v[36:39], v[24:27], a[100:103]\n"
        "v_cvt_pk_bf16_f32 v192, 0, v192\n"
        "v_cvt_pk_bf16_f32 v193, 0, v193\n"
        "v_mfma_f32_16x16x32_bf16 a[100:103], v[68:71], v[28:31], a[100:103]\n"
        "v_add_f32 v191, v192, v193\n"
        "v_lshlrev_b32 v194, 16, v151\n"
        "v_mfma_f32_16x16x32_bf16 a[68:71], v[68:71], v[20:23], a[68:71]\n"
        "v_and_b32 v195, 0xffff0000, v151\n"
        "v_mul_f32 v194, v194, v176\n"
        "v_mfma_f32_16x16x32_bf16 a[68:71], v[36:39], v[16:19], a[68:71]\n"
        "v_mul_f32 v195, v195, v177\n"
        "v_cvt_pk_bf16_f32 v194, 0, v194\n"
        "v_mfma_f32_16x16x32_bf16 a[72:75], v[40:43], v[16:19], a[72:75]\n"
        "v_cvt_pk_bf16_f32 v195, 0, v195\n"
        "v_add_f32 v194, v194, v195\n"
        "v_mfma_f32_16x16x32_bf16 a[72:75], v[72:75], v[20:23], a[72:75]\n"
        "v_add_f32 v191, v191, v194\n"
        "v_lshlrev_b32 v192, 16, v152\n"
        "v_mfma_f32_16x16x32_bf16 a[104:107], v[72:75], v[28:31], a[104:107]\n"
        "v_and_b32 v193, 0xffff0000, v152\n"
        "v_mul_f32 v192, v192, v178\n"
        "v_mfma_f32_16x16x32_bf16 a[104:107], v[40:43], v[24:27], a[104:107]\n"
        "v_mul_f32 v193, v193, v179\n"
        "v_cvt_pk_bf16_f32 v192, 0, v192\n"
        "v_mfma_f32_16x16x32_bf16 a[108:111], v[44:47], v[24:27], a[108:111]\n"
        "v_cvt_pk_bf16_f32 v193, 0, v193\n"
        "v_add_f32 v192, v192, v193\n"
        "v_mfma_f32_16x16x32_bf16 a[108:111], v[76:79], v[28:31], a[108:111]\n"
        "v_add_f32 v191, v191, v192\n"
        "v_lshlrev_b32 v194, 16, v153\n"
        "v_mfma_f32_16x16x32_bf16 a[76:79], v[76:79], v[20:23], a[76:79]\n"
        "v_and_b32 v195, 0xffff0000, v153\n"
        "v_mul_f32 v194, v194, v180\n"
        "v_mfma_f32_16x16x32_bf16 a[76:79], v[44:47], v[16:19], a[76:79]\n"
        "v_mul_f32 v195, v195, v181\n"
        "v_cvt_pk_bf16_f32 v194, 0, v194\n"
        "v_mfma_f32_16x16x32_bf16 a[80:83], v[16:19], v[48:51], a[80:83]\n"
        "v_cvt_pk_bf16_f32 v195, 0, v195\n"
        "v_add_f32 v194, v194, v195\n"
        "v_mfma_f32_16x16x32_bf16 a[80:83], v[20:23], v[80:83], a[80:83]\n"
        "v_add_f32 v191, v191, v194\n"
        "v_lshlrev_b32 v192, 16, v154\n"
        "v_mfma_f32_16x16x32_bf16 a[112:115], v[28:31], v[80:83], a[112:115]\n"
        "v_and_b32 v193, 0xffff0000, v154\n"
        "v_mul_f32 v192, v192, v182\n"
        "v_mfma_f32_16x16x32_bf16 a[112:115], v[24:27], v[48:51], a[112:115]\n"
        "v_mul_f32 v193, v193, v183\n"
        "v_cvt_pk_bf16_f32 v192, 0, v192\n"
        "v_mfma_f32_16x16x32_bf16 a[116:119], v[24:27], v[52:55], a[116:119]\n"
        "v_cvt_pk_bf16_f32 v193, 0, v193\n"
        "v_add_f32 v192, v192, v193\n"
        "v_mfma_f32_16x16x32_bf16 a[116:119], v[28:31], v[84:87], a[116:119]\n"
        "s_waitcnt lgkmcnt(0)\n"
        "v_add_f32 v191, v191, v192\n"
        "v_lshlrev_b32 v194, 16, v155\n"
        "v_mfma_f32_16x16x32_bf16 a[84:87], v[20:23], v[84:87], a[84:87]\n"
        "v_and_b32 v195, 0xffff0000, v155\n"
        "v_mul_f32 v194, v194, v184\n"
        "v_mfma_f32_16x16x32_bf16 a[84:87], v[16:19], v[52:55], a[84:87]\n"
        "s_mov_b32 s88, 0x3e0293ee\n"
        "v_mul_f32 v195, v195, v185\n"
        "v_cvt_pk_bf16_f32 v194, 0, v194\n"
        "v_mfma_f32_16x16x32_bf16 a[88:91], v[16:19], v[56:59], a[88:91]\n"
        "s_cmp_ge_u32 s40, 2\n"
        "s_cselect_b32 s50, s45, s44\n"
        "v_cvt_pk_bf16_f32 v195, 0, v195\n"
        "v_mfma_f32_16x16x32_bf16 a[88:91], v[20:23], v[88:91], a[88:91]\n"
        "s_lshl_b32 s50, s50, 15\n"
        "v_add_f32 v194, v194, v195\n"
        "v_add_f32 v191, v191, v194\n"
        "v_mfma_f32_16x16x32_bf16 a[120:123], v[28:31], v[88:91], a[120:123]\n"
        "s_lshl_b32 s51, s43, 19\n"
        "v_lshlrev_b32 v192, 16, v156\n"
        "v_and_b32 v193, 0xffff0000, v156\n"
        "v_mfma_f32_16x16x32_bf16 a[120:123], v[24:27], v[56:59], a[120:123]\n"
        "s_add_u32 s50, s50, s51\n"
        "v_mul_f32 v192, v192, v186\n"
        "v_mul_f32 v193, v193, v187\n"
        "v_mfma_f32_16x16x32_bf16 a[124:127], v[24:27], v[60:63], a[124:127]\n"
        "ds_read_b128 v[24:27], v224 offset:14336\n"
        "s_and_b32 s51, s40, 1\n"
        "v_cvt_pk_bf16_f32 v192, 0, v192\n"
        "v_mfma_f32_16x16x32_bf16 a[124:127], v[28:31], v[92:95], a[124:127]\n"
        "ds_read_b128 v[28:31], v225 offset:14336\n"
        "s_lshl_b32 s51, s51, 11\n"
        "v_cvt_pk_bf16_f32 v193, 0, v193\n"
        "v_mfma_f32_16x16x32_bf16 a[92:95], v[20:23], v[92:95], a[92:95]\n"
        "ds_read_b128 v[20:23], v225 offset:12288\n"
        "s_add_u32 s52, s50, s51\n"
        "v_add_f32 v192, v192, v193\n"
        "v_mfma_f32_16x16x32_bf16 a[92:95], v[16:19], v[60:63], a[92:95]\n"
        "ds_read_b128 v[16:19], v224 offset:12288\n"
        "v_add_f32 v191, v191, v192\n"
        "v_mfma_f32_16x16x32_bf16 a[128:131], v[32:35], v[0:3], a[128:131]\n"
        "v_lshlrev_b32 v194, 16, v157\n"
        "v_and_b32 v195, 0xffff0000, v157\n"
        "v_mfma_f32_16x16x32_bf16 a[128:131], v[64:67], v[4:7], a[128:131]\n"
        "v_mul_f32 v194, v194, v188\n"
        "v_mul_f32 v195, v195, v189\n"
        "v_mfma_f32_16x16x32_bf16 a[160:163], v[64:67], v[12:15], a[160:163]\n"
        "v_cvt_pk_bf16_f32 v194, 0, v194\n"
        "v_cvt_pk_bf16_f32 v195, 0, v195\n"
        "v_mfma_f32_16x16x32_bf16 a[160:163], v[32:35], v[8:11], a[160:163]\n"
        "v_add_f32 v194, v194, v195\n"
        "v_add_f32 v191, v191, v194\n"
        "v_mfma_f32_16x16x32_bf16 a[164:167], v[36:39], v[8:11], a[164:167]\n"
        "v_add_f32_dpp v190, v190, v190 row_shr:1 row_mask:0xf bank_mask:0xf bound_ctrl:1\n"
        "v_add_f32_dpp v191, v191, v191 row_shr:1 row_mask:0xf bank_mask:0xf bound_ctrl:1\n"
        "v_mfma_f32_16x16x32_bf16 a[164:167], v[68:71], v[12:15], a[164:167]\n"
        "v_add_f32_dpp v190, v190, v190 row_shr:2 row_mask:0xf bank_mask:0xf bound_ctrl:1\n"
        "v_add_f32_dpp v191, v191, v191 row_shr:2 row_mask:0xf bank_mask:0xf bound_ctrl:1\n"
        "v_mfma_f32_16x16x32_bf16 a[132:135], v[68:71], v[4:7], a[132:135]\n"
        "v_add_f32_dpp v190, v190, v190 row_shr:4 row_mask:0xf bank_mask:0xf bound_ctrl:1\n"
        "v_add_f32_dpp v191, v191, v191 row_shr:4 row_mask:0xf bank_mask:0xf bound_ctrl:1\n"
        "v_mfma_f32_16x16x32_bf16 a[132:135], v[36:39], v[0:3], a[132:135]\n"
        "v_add_f32_dpp v190, v190, v190 row_shr:8 row_mask:0xf bank_mask:0xf bound_ctrl:1\n"
        "v_add_f32_dpp v191, v191, v191 row_shr:8 row_mask:0xf bank_mask:0xf bound_ctrl:1\n"
        "v_mfma_f32_16x16x32_bf16 a[136:139], v[40:43], v[0:3], a[136:139]\n"
        "v_add_f32_dpp v190, v190, v190 row_bcast:15 row_mask:0xa bank_mask:0xf bound_ctrl:1\n"
        "v_add_f32_dpp v191, v191, v191 row_bcast:15 row_mask:0xa bank_mask:0xf bound_ctrl:1\n"
        "v_mfma_f32_16x16x32_bf16 a[136:139], v[72:75], v[4:7], a[136:139]\n"
        "v_add_f32_dpp v190, v190, v190 row_bcast:31 row_mask:0xc bank_mask:0xf bound_ctrl:1\n"
        "v_add_f32_dpp v191, v191, v191 row_bcast:31 row_mask:0xc bank_mask:0xf bound_ctrl:1\n"
        "v_mfma_f32_16x16x32_bf16 a[168:171], v[72:75], v[12:15], a[168:171]\n"
        "v_cvt_pk_bf16_f32 v190, 0, v190\n"
        "v_mul_f32 v190, 0xbfb8aa3b, v190\n"
        "v_mfma_f32_16x16x32_bf16 a[168:171], v[40:43], v[8:11], a[168:171]\n"
        "v_exp_f32 v190, v190\n"
        "v_mfma_f32_16x16x32_bf16 a[172:175], v[44:47], v[8:11], a[172:175]\n"
        "v_add_f32 v190, 1.0, v190\n"
        "v_lshrrev_b32 v192, 2, v254\n"
        "v_mfma_f32_16x16x32_bf16 a[172:175], v[76:79], v[12:15], a[172:175]\n"
        "LHA4_%=:\n"
        "s_cbranch_execz LHA8_%=\n"
        "v_rcp_f32 v190, v190\n"
        "v_mfma_f32_16x16x32_bf16 a[140:143], v[76:79], v[4:7], a[140:143]\n"
        "v_cvt_pk_bf16_f32 v190, 0, v190\n"
        "v_add_u32 v192, s80, v192\n"
        "v_mfma_f32_16x16x32_bf16 a[140:143], v[44:47], v[0:3], a[140:143]\n"
        "v_readlane_b32 s84, v190, 63\n"
        "v_cvt_pk_bf16_f32 v191, 0, v191\n"
        "v_mfma_f32_16x16x32_bf16 a[144:147], v[0:3], v[48:51], a[144:147]\n"
        "v_mov_b32 v190, s84\n"
        "v_mul_f32 v191, 0xbfb8aa3b, v191\n"
        "v_mfma_f32_16x16x32_bf16 a[144:147], v[4:7], v[80:83], a[144:147]\n"
        "s_mov_b64 exec, 0xffff\n"
        "global_store_dword v192, v190, s[76:77] sc1\n"
        "s_mov_b64 exec, -1\n"
        "v_mfma_f32_16x16x32_bf16 a[176:179], v[12:15], v[80:83], a[176:179]\n"
        "v_exp_f32 v191, v191\n"
        "v_mfma_f32_16x16x32_bf16 a[176:179], v[8:11], v[48:51], a[176:179]\n"
        "v_add_f32 v191, 1.0, v191\n"
        "v_lshrrev_b32 v193, 2, v254\n"
        "v_mfma_f32_16x16x32_bf16 a[180:183], v[8:11], v[52:55], a[180:183]\n"
        "v_rcp_f32 v191, v191\n"
        "v_mfma_f32_16x16x32_bf16 a[180:183], v[12:15], v[84:87], a[180:183]\n"
        "s_waitcnt lgkmcnt(0)\n"
        "v_cvt_pk_bf16_f32 v191, 0, v191\n"
        "v_add_u32 v193, s81, v193\n"
        "v_mfma_f32_16x16x32_bf16 a[148:151], v[4:7], v[84:87], a[148:151]\n"
        "v_readlane_b32 s85, v191, 63\n"
        "v_mfma_f32_16x16x32_bf16 a[148:151], v[0:3], v[52:55], a[148:151]\n"
        "global_load_dword v253, v173, s[82:83] offset:0\n"
        "v_mov_b32 v191, s85\n"
        "v_mfma_f32_16x16x32_bf16 a[152:155], v[0:3], v[56:59], a[152:155]\n"
        "s_mov_b64 exec, 0xffff\n"
        "global_store_dword v193, v191, s[76:77] sc1\n"
        "s_mov_b64 exec, -1\n"
        "v_mfma_f32_16x16x32_bf16 a[152:155], v[4:7], v[88:91], a[152:155]\n"
        "v_mfma_f32_16x16x32_bf16 a[184:187], v[12:15], v[88:91], a[184:187]\n"
        "v_mfma_f32_16x16x32_bf16 a[184:187], v[8:11], v[56:59], a[184:187]\n"
        "s_waitcnt vmcnt(12)\n"
        "s_barrier\n"
        "v_mfma_f32_16x16x32_bf16 a[188:191], v[8:11], v[60:63], a[188:191]\n"
        "global_load_lds_dwordx4 v242, s[64:65] offset:0\n"
        "ds_read_b128 v[8:11], v224 offset:34816\n"
        "v_mfma_f32_16x16x32_bf16 a[188:191], v[12:15], v[92:95], a[188:191]\n"
        "ds_read_b128 v[12:15], v225 offset:34816\n"
        "v_mfma_f32_16x16x32_bf16 a[156:159], v[4:7], v[92:95], a[156:159]\n"
        "ds_read_b128 v[4:7], v225 offset:32768\n"
        "v_mfma_f32_16x16x32_bf16 a[156:159], v[0:3], v[60:63], a[156:159]\n"
        "ds_read_b128 v[0:3], v224 offset:32768\n"
        "v_mfma_f32_16x16x32_bf16 a[192:195], v[32:35], v[16:19], a[192:195]\n"
        "global_load_lds_dwordx4 v243, s[64:65] offset:1024\n"
        "v_mfma_f32_16x16x32_bf16 a[192:195], v[64:67], v[20:23], a[192:195]\n"
        "v_mfma_f32_16x16x32_bf16 a[224:227], v[64:67], v[28:31], a[224:227]\n"
        "ds_read_b64_tr_b16 v[64:65], v226 offset:40960\n"
        "v_mfma_f32_16x16x32_bf16 a[224:227], v[32:35], v[24:27], a[224:227]\n"
        "ds_read_b64_tr_b16 v[32:33], v226 offset:32768\n"
        "s_add_u32 s97, s52, 0x7000\n"
        "v_mfma_f32_16x16x32_bf16 a[228:231], v[36:39], v[24:27], a[228:231]\n"
        "ds_read_b64_tr_b16 v[34:35], v227 offset:32768\n"
        "global_load_lds_dwordx4 v244, s[64:65] offset:2048\n"
        "v_mfma_f32_16x16x32_bf16 a[228:231], v[68:71], v[28:31], a[228:231]\n"
        "ds_read_b64_tr_b16 v[66:67], v227 offset:40960\n"
        "s_add_u32 s70, s38, s97\n"
        "s_addc_u32 s71, s39, 0\n"
        "v_mfma_f32_16x16x32_bf16 a[196:199], v[68:71], v[20:23], a[196:199]\n"
        "ds_read_b64_tr_b16 v[68:69], v228 offset:40960\n"
        "v_mfma_f32_16x16x32_bf16 a[196:199], v[36:39], v[16:19], a[196:199]\n"
        "ds_read_b64_tr_b16 v[36:37], v228 offset:32768\n"
        "v_mfma_f32_16x16x32_bf16 a[200:203], v[40:43], v[16:19], a[200:203]\n"
        "ds_read_b64_tr_b16 v[38:39], v229 offset:32768\n"
        "global_load_lds_dwordx4 v245, s[64:65] offset:3072\n"
        "s_add_u32 m0, s72, 0x1000\n"
        "v_mfma_f32_16x16x32_bf16 a[200:203], v[72:75], v[20:23], a[200:203]\n"
        "ds_read_b64_tr_b16 v[70:71], v229 offset:40960\n"
        "s_add_u32 s64, s64, 0x80\n"
        "s_addc_u32 s65, s65, 0\n"
        "v_mfma_f32_16x16x32_bf16 a[232:235], v[72:75], v[28:31], a[232:235]\n"
        "ds_read_b64_tr_b16 v[72:73], v230 offset:40960\n"
        "v_mfma_f32_16x16x32_bf16 a[232:235], v[40:43], v[24:27], a[232:235]\n"
        "ds_read_b64_tr_b16 v[40:41], v230 offset:32768\n"
        "v_mfma_f32_16x16x32_bf16 a[236:239], v[44:47], v[24:27], a[236:239]\n"
        "ds_read_b64_tr_b16 v[42:43], v231 offset:32768\n"
        "global_load_lds_dwordx4 v242, s[66:67] offset:0\n"
        "v_mfma_f32_16x16x32_bf16 a[236:239], v[76:79], v[28:31], a[236:239]\n"
        "ds_read_b64_tr_b16 v[74:75], v231 offset:40960\n"
        "v_mfma_f32_16x16x32_bf16 a[204:207], v[76:79], v[20:23], a[204:207]\n"
        "ds_read_b64_tr_b16 v[76:77], v232 offset:40960\n"
        "v_mfma_f32_16x16x32_bf16 a[204:207], v[44:47], v[16:19], a[204:207]\n"
        "ds_read_b64_tr_b16 v[44:45], v232 offset:32768\n"
        "v_mfma_f32_16x16x32_bf16 a[208:211], v[16:19], v[48:51], a[208:211]\n"
        "ds_read_b64_tr_b16 v[46:47], v233 offset:32768\n"
        "global_load_lds_dwordx4 v243, s[66:67] offset:1024\n"
        "v_mfma_f32_16x16x32_bf16 a[208:211], v[20:23], v[80:83], a[208:211]\n"
        "ds_read_b64_tr_b16 v[78:79], v233 offset:40960\n"
        "v_mfma_f32_16x16x32_bf16 a[240:243], v[28:31], v[80:83], a[240:243]\n"
        "ds_read_b64_tr_b16 v[80:81], v226 offset:57344\n"
        "v_mfma_f32_16x16x32_bf16 a[240:243], v[24:27], v[48:51], a[240:243]\n"
        "ds_read_b64_tr_b16 v[48:49], v226 offset:49152\n"
        "v_add_u32 v226, s53, v226\n"
        "v_mfma_f32_16x16x32_bf16 a[244:247], v[24:27], v[52:55], a[244:247]\n"
        "ds_read_b64_tr_b16 v[50:51], v227 offset:49152\n"
        "global_load_lds_dwordx4 v244, s[66:67] offset:2048\n"
        "v_mfma_f32_16x16x32_bf16 a[244:247], v[28:31], v[84:87], a[244:247]\n"
        "ds_read_b64_tr_b16 v[82:83], v227 offset:57344\n"
        "v_add_u32 v227, s53, v227\n"
        "v_mfma_f32_16x16x32_bf16 a[212:215], v[20:23], v[84:87], a[212:215]\n"
        "ds_read_b64_tr_b16 v[84:85], v228 offset:57344\n"
        "s_waitcnt lgkmcnt(8)\n"
        "v_mfma_f32_16x16x32_bf16 a[212:215], v[16:19], v[52:55], a[212:215]\n"
        "ds_read_b64_tr_b16 v[52:53], v228 offset:49152\n"
        "v_add_u32 v228, s53, v228\n"
        "v_mfma_f32_16x16x32_bf16 a[216:219], v[16:19], v[56:59], a[216:219]\n"
        "ds_read_b64_tr_b16 v[54:55], v229 offset:49152\n"
        "global_load_lds_dwordx4 v245, s[66:67] offset:3072\n"
        "s_add_u32 m0, s72, 0x10000\n"
        "v_mfma_f32_16x16x32_bf16 a[216:219], v[20:23], v[88:91], a[216:219]\n"
        "ds_read_b64_tr_b16 v[86:87], v229 offset:57344\n"
        "s_add_u32 s66, s66, 0x80\n"
        "s_addc_u32 s67, s67, 0\n"
        "v_mfma_f32_16x16x32_bf16 a[248:251], v[28:31], v[88:91], a[248:251]\n"
        "ds_read_b64_tr_b16 v[88:89], v230 offset:57344\n"
        "v_add_u32 v229, s53, v229\n"
        "v_mfma_f32_16x16x32_bf16 a[248:251], v[24:27], v[56:59], a[248:251]\n"
        "ds_read_b64_tr_b16 v[56:57], v230 offset:49152\n"
        "v_add_u32 v230, s53, v230\n"
        "v_mfma_f32_16x16x32_bf16 a[252:255], v[24:27], v[60:63], a[252:255]\n"
        "ds_read_b64_tr_b16 v[58:59], v231 offset:49152\n"
        "global_load_lds_dwordx4 v242, s[64:65] offset:0\n"
        "v_mfma_f32_16x16x32_bf16 a[252:255], v[28:31], v[92:95], a[252:255]\n"
        "ds_read_b64_tr_b16 v[90:91], v231 offset:57344\n"
        "v_add_u32 v231, s53, v231\n"
        "v_mfma_f32_16x16x32_bf16 a[220:223], v[20:23], v[92:95], a[220:223]\n"
        "ds_read_b64_tr_b16 v[92:93], v232 offset:57344\n"
        "v_mfma_f32_16x16x32_bf16 a[220:223], v[16:19], v[60:63], a[220:223]\n"
        "ds_read_b64_tr_b16 v[60:61], v232 offset:49152\n"
        "v_add_u32 v232, s53, v232\n"
        "v_mfma_f32_16x16x32_bf16 a[0:3], v[32:35], v[0:3], a[0:3]\n"
        "ds_read_b64_tr_b16 v[62:63], v233 offset:49152\n"
        "global_load_lds_dwordx4 v243, s[64:65] offset:1024\n"
        "v_mfma_f32_16x16x32_bf16 a[0:3], v[64:67], v[4:7], a[0:3]\n"
        "ds_read_b64_tr_b16 v[94:95], v233 offset:57344\n"
        "v_add_u32 v233, s53, v233\n"
        "v_mfma_f32_16x16x32_bf16 a[32:35], v[64:67], v[12:15], a[32:35]\n"
        "ds_read_b128 v[16:19], v224 offset:36864\n"
        "s_add_u32 s53, s52, s51\n"
        "s_add_u32 s53, s53, 0x1000000\n"
        "v_mfma_f32_16x16x32_bf16 a[32:35], v[32:35], v[8:11], a[32:35]\n"
        "ds_read_b128 v[20:23], v225 offset:36864\n"
        "s_waitcnt lgkmcnt(8)\n"
        "v_mfma_f32_16x16x32_bf16 a[36:39], v[36:39], v[8:11], a[36:39]\n"
        "ds_read_b128 v[24:27], v224 offset:38912\n"
        "global_load_lds_dwordx4 v244, s[64:65] offset:2048\n"
        "v_mfma_f32_16x16x32_bf16 a[36:39], v[68:71], v[12:15], a[36:39]\n"
        "ds_read_b128 v[28:31], v225 offset:38912\n"
        "v_mfma_f32_16x16x32_bf16 a[4:7], v[68:71], v[4:7], a[4:7]\n"
        "v_mfma_f32_16x16x32_bf16 a[4:7], v[36:39], v[0:3], a[4:7]\n"
        "v_mfma_f32_16x16x32_bf16 a[8:11], v[40:43], v[0:3], a[8:11]\n"
        "global_load_lds_dwordx4 v245, s[64:65] offset:3072\n"
        "s_add_u32 m0, s72, 0x11000\n"
        "v_mfma_f32_16x16x32_bf16 a[8:11], v[72:75], v[4:7], a[8:11]\n"
        "s_add_u32 s64, s64, 0x80\n"
        "s_addc_u32 s65, s65, 0\n"
        "v_mfma_f32_16x16x32_bf16 a[40:43], v[72:75], v[12:15], a[40:43]\n"
        "v_mfma_f32_16x16x32_bf16 a[40:43], v[40:43], v[8:11], a[40:43]\n"
        "v_mfma_f32_16x16x32_bf16 a[44:47], v[44:47], v[8:11], a[44:47]\n"
        "global_load_lds_dwordx4 v242, s[66:67] offset:0\n"
        "v_mfma_f32_16x16x32_bf16 a[44:47], v[76:79], v[12:15], a[44:47]\n"
        "s_waitcnt lgkmcnt(0)\n"
        "v_mfma_f32_16x16x32_bf16 a[12:15], v[76:79], v[4:7], a[12:15]\n"
        "v_mfma_f32_16x16x32_bf16 a[12:15], v[44:47], v[0:3], a[12:15]\n"
        "v_mfma_f32_16x16x32_bf16 a[16:19], v[0:3], v[48:51], a[16:19]\n"
        "global_load_lds_dwordx4 v243, s[66:67] offset:1024\n"
        "v_mfma_f32_16x16x32_bf16 a[16:19], v[4:7], v[80:83], a[16:19]\n"
        "v_mfma_f32_16x16x32_bf16 a[48:51], v[12:15], v[80:83], a[48:51]\n"
        "v_mfma_f32_16x16x32_bf16 a[48:51], v[8:11], v[48:51], a[48:51]\n"
        "v_mfma_f32_16x16x32_bf16 a[52:55], v[8:11], v[52:55], a[52:55]\n"
        "global_load_lds_dwordx4 v244, s[66:67] offset:2048\n"
        "v_mfma_f32_16x16x32_bf16 a[52:55], v[12:15], v[84:87], a[52:55]\n"
        "v_mfma_f32_16x16x32_bf16 a[20:23], v[4:7], v[84:87], a[20:23]\n"
        "v_mfma_f32_16x16x32_bf16 a[20:23], v[0:3], v[52:55], a[20:23]\n"
        "v_mfma_f32_16x16x32_bf16 a[24:27], v[0:3], v[56:59], a[24:27]\n"
        "global_load_lds_dwordx4 v245, s[66:67] offset:3072\n"
        "s_add_u32 m0, s73, 0x1c000\n"
        "v_mfma_f32_16x16x32_bf16 a[24:27], v[4:7], v[88:91], a[24:27]\n"
        "s_add_u32 s66, s66, 0x80\n"
        "s_addc_u32 s67, s67, 0\n"
        "v_mfma_f32_16x16x32_bf16 a[56:59], v[12:15], v[88:91], a[56:59]\n"
        "v_mfma_f32_16x16x32_bf16 a[56:59], v[8:11], v[56:59], a[56:59]\n"
        "v_mfma_f32_16x16x32_bf16 a[60:63], v[8:11], v[60:63], a[60:63]\n"
        "ds_read_b128 v[8:11], v224 offset:43008\n"
        "v_mfma_f32_16x16x32_bf16 a[60:63], v[12:15], v[92:95], a[60:63]\n"
        "ds_read_b128 v[12:15], v225 offset:43008\n"
        "v_mfma_f32_16x16x32_bf16 a[28:31], v[4:7], v[92:95], a[28:31]\n"
        "ds_read_b128 v[4:7], v225 offset:40960\n"
        "v_mfma_f32_16x16x32_bf16 a[28:31], v[0:3], v[60:63], a[28:31]\n"
        "ds_read_b128 v[0:3], v224 offset:40960\n"
        "v_mfma_f32_16x16x32_bf16 a[64:67], v[32:35], v[16:19], a[64:67]\n"
        "v_mfma_f32_16x16x32_bf16 a[64:67], v[64:67], v[20:23], a[64:67]\n"
        "v_mfma_f32_16x16x32_bf16 a[96:99], v[64:67], v[28:31], a[96:99]\n"
        "v_mfma_f32_16x16x32_bf16 a[96:99], v[32:35], v[24:27], a[96:99]\n"
        "v_mfma_f32_16x16x32_bf16 a[100:103], v[36:39], v[24:27], a[100:103]\n"
        "v_mfma_f32_16x16x32_bf16 a[100:103], v[68:71], v[28:31], a[100:103]\n"
        "v_mfma_f32_16x16x32_bf16 a[68:71], v[68:71], v[20:23], a[68:71]\n"
        "v_mfma_f32_16x16x32_bf16 a[68:71], v[36:39], v[16:19], a[68:71]\n"
        "v_mfma_f32_16x16x32_bf16 a[72:75], v[40:43], v[16:19], a[72:75]\n"
        "v_mfma_f32_16x16x32_bf16 a[72:75], v[72:75], v[20:23], a[72:75]\n"
        "v_mfma_f32_16x16x32_bf16 a[104:107], v[72:75], v[28:31], a[104:107]\n"
        "v_mfma_f32_16x16x32_bf16 a[104:107], v[40:43], v[24:27], a[104:107]\n"
        "v_mfma_f32_16x16x32_bf16 a[108:111], v[44:47], v[24:27], a[108:111]\n"
        "v_mfma_f32_16x16x32_bf16 a[108:111], v[76:79], v[28:31], a[108:111]\n"
        "v_mfma_f32_16x16x32_bf16 a[76:79], v[76:79], v[20:23], a[76:79]\n"
        "v_mfma_f32_16x16x32_bf16 a[76:79], v[44:47], v[16:19], a[76:79]\n"
        "v_mfma_f32_16x16x32_bf16 a[80:83], v[16:19], v[48:51], a[80:83]\n"
        "v_mfma_f32_16x16x32_bf16 a[80:83], v[20:23], v[80:83], a[80:83]\n"
        "v_mfma_f32_16x16x32_bf16 a[112:115], v[28:31], v[80:83], a[112:115]\n"
        "v_mfma_f32_16x16x32_bf16 a[112:115], v[24:27], v[48:51], a[112:115]\n"
        "v_mfma_f32_16x16x32_bf16 a[116:119], v[24:27], v[52:55], a[116:119]\n"
        "v_mfma_f32_16x16x32_bf16 a[116:119], v[28:31], v[84:87], a[116:119]\n"
        "s_waitcnt lgkmcnt(0)\n"
        "v_mfma_f32_16x16x32_bf16 a[84:87], v[20:23], v[84:87], a[84:87]\n"
        "v_mfma_f32_16x16x32_bf16 a[84:87], v[16:19], v[52:55], a[84:87]\n"
        "v_mfma_f32_16x16x32_bf16 a[88:91], v[16:19], v[56:59], a[88:91]\n"
        "v_mfma_f32_16x16x32_bf16 a[88:91], v[20:23], v[88:91], a[88:91]\n"
        "v_mfma_f32_16x16x32_bf16 a[120:123], v[28:31], v[88:91], a[120:123]\n"
        "v_mfma_f32_16x16x32_bf16 a[120:123], v[24:27], v[56:59], a[120:123]\n"
        "v_mfma_f32_16x16x32_bf16 a[124:127], v[24:27], v[60:63], a[124:127]\n"
        "ds_read_b128 v[24:27], v224 offset:47104\n"
        "v_mfma_f32_16x16x32_bf16 a[124:127], v[28:31], v[92:95], a[124:127]\n"
        "ds_read_b128 v[28:31], v225 offset:47104\n"
        "v_mfma_f32_16x16x32_bf16 a[92:95], v[20:23], v[92:95], a[92:95]\n"
        "ds_read_b128 v[20:23], v225 offset:45056\n"
        "v_mfma_f32_16x16x32_bf16 a[92:95], v[16:19], v[60:63], a[92:95]\n"
        "ds_read_b128 v[16:19], v224 offset:45056\n"
        "v_mfma_f32_16x16x32_bf16 a[128:131], v[32:35], v[0:3], a[128:131]\n"
        "v_mfma_f32_16x16x32_bf16 a[128:131], v[64:67], v[4:7], a[128:131]\n"
        "v_mfma_f32_16x16x32_bf16 a[160:163], v[64:67], v[12:15], a[160:163]\n"
        "v_mfma_f32_16x16x32_bf16 a[160:163], v[32:35], v[8:11], a[160:163]\n"
        "v_mfma_f32_16x16x32_bf16 a[164:167], v[36:39], v[8:11], a[164:167]\n"
        "v_mfma_f32_16x16x32_bf16 a[164:167], v[68:71], v[12:15], a[164:167]\n"
        "v_mfma_f32_16x16x32_bf16 a[132:135], v[68:71], v[4:7], a[132:135]\n"
        "v_mfma_f32_16x16x32_bf16 a[132:135], v[36:39], v[0:3], a[132:135]\n"
        "v_mfma_f32_16x16x32_bf16 a[136:139], v[40:43], v[0:3], a[136:139]\n"
        "v_mfma_f32_16x16x32_bf16 a[136:139], v[72:75], v[4:7], a[136:139]\n"
        "v_mfma_f32_16x16x32_bf16 a[168:171], v[72:75], v[12:15], a[168:171]\n"
        "v_mfma_f32_16x16x32_bf16 a[168:171], v[40:43], v[8:11], a[168:171]\n"
        "v_mfma_f32_16x16x32_bf16 a[172:175], v[44:47], v[8:11], a[172:175]\n"
        "v_mfma_f32_16x16x32_bf16 a[172:175], v[76:79], v[12:15], a[172:175]\n"
        "v_mfma_f32_16x16x32_bf16 a[140:143], v[76:79], v[4:7], a[140:143]\n"
        "v_mfma_f32_16x16x32_bf16 a[140:143], v[44:47], v[0:3], a[140:143]\n"
        "v_mfma_f32_16x16x32_bf16 a[144:147], v[0:3], v[48:51], a[144:147]\n"
        "v_mfma_f32_16x16x32_bf16 a[144:147], v[4:7], v[80:83], a[144:147]\n"
        "v_mfma_f32_16x16x32_bf16 a[176:179], v[12:15], v[80:83], a[176:179]\n"
        "v_mfma_f32_16x16x32_bf16 a[176:179], v[8:11], v[48:51], a[176:179]\n"
        "v_mfma_f32_16x16x32_bf16 a[180:183], v[8:11], v[52:55], a[180:183]\n"
        "v_mfma_f32_16x16x32_bf16 a[180:183], v[12:15], v[84:87], a[180:183]\n"
        "s_waitcnt lgkmcnt(0)\n"
        "v_mfma_f32_16x16x32_bf16 a[148:151], v[4:7], v[84:87], a[148:151]\n"
        "v_mfma_f32_16x16x32_bf16 a[148:151], v[0:3], v[52:55], a[148:151]\n"
        "v_mfma_f32_16x16x32_bf16 a[152:155], v[0:3], v[56:59], a[152:155]\n"
        "v_mfma_f32_16x16x32_bf16 a[152:155], v[4:7], v[88:91], a[152:155]\n"
        "v_mfma_f32_16x16x32_bf16 a[184:187], v[12:15], v[88:91], a[184:187]\n"
        "v_mfma_f32_16x16x32_bf16 a[184:187], v[8:11], v[56:59], a[184:187]\n"
        "s_waitcnt vmcnt(8)\n"
        "s_barrier\n"
        "v_mfma_f32_16x16x32_bf16 a[188:191], v[8:11], v[60:63], a[188:191]\n"
        "global_load_lds_dwordx4 v238, s[68:69] offset:0\n"
        "ds_read_b64_tr_b16 v[160:161], v226 offset:32768\n"
        "v_mfma_f32_16x16x32_bf16 a[188:191], v[12:15], v[92:95], a[188:191]\n"
        "ds_read_b64_tr_b16 v[162:163], v227 offset:32768\n"
        "v_mfma_f32_16x16x32_bf16 a[156:159], v[4:7], v[92:95], a[156:159]\n"
        "ds_read_b64_tr_b16 v[164:165], v226 offset:40960\n"
        "v_mfma_f32_16x16x32_bf16 a[156:159], v[0:3], v[60:63], a[156:159]\n"
        "ds_read_b64_tr_b16 v[166:167], v227 offset:40960\n"
        "v_mfma_f32_16x16x32_bf16 a[192:195], v[32:35], v[16:19], a[192:195]\n"
        "ds_read_b64_tr_b16 v[172:173], v228 offset:40960\n"
        "global_load_lds_dwordx4 v239, s[68:69] offset:1024\n"
        "v_mfma_f32_16x16x32_bf16 a[192:195], v[64:67], v[20:23], a[192:195]\n"
        "ds_read_b64_tr_b16 v[174:175], v229 offset:40960\n"
        "v_mfma_f32_16x16x32_bf16 a[224:227], v[64:67], v[28:31], a[224:227]\n"
        "ds_read_b64_tr_b16 v[168:169], v228 offset:32768\n"
        "v_mfma_f32_16x16x32_bf16 a[224:227], v[32:35], v[24:27], a[224:227]\n"
        "ds_read_b64_tr_b16 v[170:171], v229 offset:32768\n"
        "v_mfma_f32_16x16x32_bf16 a[228:231], v[36:39], v[24:27], a[228:231]\n"
        "ds_read_b128 v[192:195], v246 offset:2048\n"
        "global_load_lds_dwordx4 v240, s[68:69] offset:2048\n"
        "v_mfma_f32_16x16x32_bf16 a[228:231], v[68:71], v[28:31], a[228:231]\n"
        "ds_read_b128 v[208:211], v247 offset:2048\n"
        "v_mfma_f32_16x16x32_bf16 a[196:199], v[68:71], v[20:23], a[196:199]\n"
        "ds_read_b128 v[196:199], v246 offset:0\n"
        "v_accvgpr_read_b32 v177, a225\n"
        "v_mfma_f32_16x16x32_bf16 a[196:199], v[36:39], v[16:19], a[196:199]\n"
        "ds_read_b128 v[212:215], v247 offset:0\n"
        "v_accvgpr_read_b32 v178, a226\n"
        "v_mfma_f32_16x16x32_bf16 a[200:203], v[40:43], v[16:19], a[200:203]\n"
        "ds_read_b128 v[200:203], v246 offset:6144\n"
        "global_load_lds_dwordx4 v241, s[68:69] offset:3072\n"
        "s_add_u32 m0, s72, 0x8000\n"
        "v_mfma_f32_16x16x32_bf16 a[200:203], v[72:75], v[20:23], a[200:203]\n"
        "ds_read_b128 v[216:219], v247 offset:6144\n"
        "s_add_u32 s68, s68, 0x80000\n"
        "s_addc_u32 s69, s69, 0\n"
        "v_mfma_f32_16x16x32_bf16 a[232:235], v[72:75], v[28:31], a[232:235]\n"
        "ds_read_b128 v[204:207], v246 offset:4096\n"
        "v_accvgpr_read_b32 v179, a227\n"
        "v_mfma_f32_16x16x32_bf16 a[232:235], v[40:43], v[24:27], a[232:235]\n"
        "ds_read_b128 v[220:223], v247 offset:4096\n"
        "v_accvgpr_read_b32 v180, a228\n"
        "v_mfma_f32_16x16x32_bf16 a[236:239], v[44:47], v[24:27], a[236:239]\n"
        "ds_read_b64_tr_b16 v[32:33], v230 offset:32768\n"
        "global_load_lds_dwordx4 v242, s[64:65] offset:0\n"
        "v_mfma_f32_16x16x32_bf16 a[236:239], v[76:79], v[28:31], a[236:239]\n"
        "ds_read_b64_tr_b16 v[34:35], v231 offset:32768\n"
        "v_accvgpr_read_b32 v181, a229\n"
        "v_mfma_f32_16x16x32_bf16 a[204:207], v[76:79], v[20:23], a[204:207]\n"
        "ds_read_b64_tr_b16 v[36:37], v230 offset:40960\n"
        "v_accvgpr_read_b32 v182, a230\n"
        "v_mfma_f32_16x16x32_bf16 a[204:207], v[44:47], v[16:19], a[204:207]\n"
        "ds_read_b64_tr_b16 v[38:39], v231 offset:40960\n"
        "v_accvgpr_read_b32 v183, a231\n"
        "v_mfma_f32_16x16x32_bf16 a[208:211], v[16:19], v[48:51], a[208:211]\n"
        "ds_read_b64_tr_b16 v[44:45], v232 offset:40960\n"
        "global_load_lds_dwordx4 v243, s[64:65] offset:1024\n"
        "v_mfma_f32_16x16x32_bf16 a[208:211], v[20:23], v[80:83], a[208:211]\n"
        "ds_read_b64_tr_b16 v[46:47], v233 offset:40960\n"
        "v_mul_f32 v177, s88, v177\n"
        "v_mfma_f32_16x16x32_bf16 a[240:243], v[28:31], v[80:83], a[240:243]\n"
        "ds_read_b64_tr_b16 v[40:41], v232 offset:32768\n"
        "v_mul_f32 v178, s88, v178\n"
        "v_mfma_f32_16x16x32_bf16 a[240:243], v[24:27], v[48:51], a[240:243]\n"
        "ds_read_b64_tr_b16 v[42:43], v233 offset:32768\n"
        "v_mul_f32 v179, s88, v179\n"
        "v_mfma_f32_16x16x32_bf16 a[244:247], v[24:27], v[52:55], a[244:247]\n"
        "global_load_lds_dwordx4 v244, s[64:65] offset:2048\n"
        "v_mfma_f32_16x16x32_bf16 a[244:247], v[28:31], v[84:87], a[244:247]\n"
        "ds_read_b64_tr_b16 v[48:49], v226 offset:33792\n"
        "v_mul_f32 v180, s88, v180\n"
        "v_mfma_f32_16x16x32_bf16 a[212:215], v[20:23], v[84:87], a[212:215]\n"
        "ds_read_b64_tr_b16 v[50:51], v227 offset:33792\n"
        "v_mul_f32 v181, s88, v181\n"
        "v_mfma_f32_16x16x32_bf16 a[212:215], v[16:19], v[52:55], a[212:215]\n"
        "ds_read_b64_tr_b16 v[52:53], v226 offset:41984\n"
        "v_mul_f32 v182, s88, v182\n"
        "v_mfma_f32_16x16x32_bf16 a[216:219], v[16:19], v[56:59], a[216:219]\n"
        "global_load_lds_dwordx4 v245, s[64:65] offset:3072\n"
        "s_add_u32 m0, s72, 0x9000\n"
        "v_mfma_f32_16x16x32_bf16 a[216:219], v[20:23], v[88:91], a[216:219]\n"
        "ds_read_b64_tr_b16 v[54:55], v227 offset:41984\n"
        "v_mul_f32 v183, s88, v183\n"
        "v_mfma_f32_16x16x32_bf16 a[248:251], v[28:31], v[88:91], a[248:251]\n"
        "s_add_u32 s64, s64, 0x80\n"
        "s_addc_u32 s65, s65, 0\n"
        "v_accvgpr_read_b32 v185, a233\n"
        "v_mfma_f32_16x16x32_bf16 a[248:251], v[24:27], v[56:59], a[248:251]\n"
        "ds_read_b64_tr_b16 v[56:57], v228 offset:33792\n"
        "v_accvgpr_read_b32 v186, a234\n"
        "v_mfma_f32_16x16x32_bf16 a[252:255], v[24:27], v[60:63], a[252:255]\n"
        "global_load_lds_dwordx4 v242, s[66:67] offset:0\n"
        "v_mfma_f32_16x16x32_bf16 a[252:255], v[28:31], v[92:95], a[252:255]\n"
        "ds_read_b64_tr_b16 v[58:59], v229 offset:33792\n"
        "v_accvgpr_read_b32 v187, a235\n"
        "v_mfma_f32_16x16x32_bf16 a[220:223], v[20:23], v[92:95], a[220:223]\n"
        "s_waitcnt lgkmcnt(4)\n"
        "v_accvgpr_read_b32 v188, a236\n"
        "v_accvgpr_read_b32 v189, a237\n"
        "v_mfma_f32_16x16x32_bf16 a[220:223], v[16:19], v[60:63], a[220:223]\n"
        "ds_read_b64_tr_b16 v[60:61], v228 offset:41984\n"
        "v_accvgpr_read_b32 v190, a238\n"
        "v_mfma_f32_16x16x32_bf16 v[0:3], v[160:163], v[192:195], 0\n"
        "ds_read_b64_tr_b16 v[62:63], v229 offset:41984\n"
        "global_load_lds_dwordx4 v243, s[66:67] offset:1024\n"
        "v_mfma_f32_16x16x32_bf16 v[0:3], v[164:167], v[208:211], v[0:3]\n"
        "v_accvgpr_read_b32 v191, a239\n"
        "v_mul_f32 v185, s88, v185\n"
        "v_mfma_f32_16x16x32_bf16 v[4:7], v[172:175], v[208:211], 0\n"
        "v_mul_f32 v186, s88, v186\n"
        "v_mul_f32 v187, s88, v187\n"
        "v_mfma_f32_16x16x32_bf16 v[4:7], v[168:171], v[192:195], v[4:7]\n"
        "v_mul_f32 v188, s88, v188\n"
        "v_mul_f32 v189, s88, v189\n"
        "v_mfma_f32_16x16x32_bf16 v[68:71], v[168:171], v[196:199], 0\n"
        "global_load_lds_dwordx4 v244, s[66:67] offset:2048\n"
        "v_mfma_f32_16x16x32_bf16 v[68:71], v[172:175], v[212:215], v[68:71]\n"
        "v_mul_f32 v190, s88, v190\n"
        "v_mul_f32 v191, s88, v191\n"
        "v_mfma_f32_16x16x32_bf16 v[64:67], v[164:167], v[212:215], 0\n"
        "v_mfma_f32_16x16x32_bf16 v[64:67], v[160:163], v[196:199], v[64:67]\n"
        "v_accvgpr_read_b32 v176, a224\n"
        "v_mul_f32 v176, s88, v176\n"
        "v_mfma_f32_16x16x32_bf16 v[96:99], v[160:163], v[200:203], 0\n"
        "v_cvt_pk_bf16_f32 v176, v176, v177\n"
        "v_cvt_pk_bf16_f32 v177, v178, v179\n"
        "v_mfma_f32_16x16x32_bf16 v[96:99], v[164:167], v[216:219], v[96:99]\n"
        "global_load_lds_dwordx4 v245, s[66:67] offset:3072\n"
        "s_add_u32 m0, s73, 0x20000\n"
        "v_cvt_pk_bf16_f32 v178, v180, v181\n"
        "v_mfma_f32_16x16x32_bf16 v[100:103], v[172:175], v[216:219], 0\n"
        "s_add_u32 s66, s66, 0x80\n"
        "s_addc_u32 s67, s67, 0\n"
        "v_cvt_pk_bf16_f32 v179, v182, v183\n"
        "v_mfma_f32_16x16x32_bf16 v[100:103], v[168:171], v[200:203], v[100:103]\n"
        "v_accvgpr_write_b32 a224, v176\n"
        "v_accvgpr_write_b32 a225, v177\n"
        "v_mfma_f32_16x16x32_bf16 v[132:135], v[168:171], v[204:207], 0\n"
        "ds_read_b64_tr_b16 v[168:169], v232 offset:33792\n"
        "v_accvgpr_write_b32 a226, v178\n"
        "v_mfma_f32_16x16x32_bf16 v[132:135], v[172:175], v[220:223], v[132:135]\n"
        "ds_read_b64_tr_b16 v[172:173], v232 offset:41984\n"
        "v_accvgpr_write_b32 a227, v179\n"
        "v_mfma_f32_16x16x32_bf16 v[128:131], v[164:167], v[220:223], 0\n"
        "ds_read_b64_tr_b16 v[164:165], v230 offset:41984\n"
        "s_add_u32 s97, s52, 0x6000\n"
        "v_accvgpr_read_b32 v177, a193\n"
        "v_mfma_f32_16x16x32_bf16 v[128:131], v[160:163], v[204:207], v[128:131]\n"
        "ds_read_b64_tr_b16 v[160:161], v230 offset:33792\n"
        "v_accvgpr_read_b32 v178, a194\n"
        "v_mfma_f32_16x16x32_bf16 v[8:11], v[32:35], v[192:195], 0\n"
        "ds_read_b64_tr_b16 v[162:163], v231 offset:33792\n"
        "v_accvgpr_read_b32 v179, a195\n"
        "v_mfma_f32_16x16x32_bf16 v[8:11], v[36:39], v[208:211], v[8:11]\n"
        "ds_read_b64_tr_b16 v[166:167], v231 offset:41984\n"
        "v_accvgpr_read_b32 v180, a196\n"
        "v_mfma_f32_16x16x32_bf16 v[12:15], v[44:47], v[208:211], 0\n"
        "ds_read_b64_tr_b16 v[174:175], v233 offset:41984\n"
        "v_accvgpr_read_b32 v181, a197\n"
        "v_mfma_f32_16x16x32_bf16 v[12:15], v[40:43], v[192:195], v[12:15]\n"
        "ds_read_b64_tr_b16 v[170:171], v233 offset:33792\n"
        "v_accvgpr_read_b32 v182, a198\n"
        "v_mfma_f32_16x16x32_bf16 v[76:79], v[40:43], v[196:199], 0\n"
        "v_accvgpr_read_b32 v183, a199\n"
        "v_mul_f32 v177, s88, v177\n"
        "v_mfma_f32_16x16x32_bf16 v[76:79], v[44:47], v[212:215], v[76:79]\n"
        "v_mul_f32 v178, s88, v178\n"
        "v_mul_f32 v179, s88, v179\n"
        "v_mfma_f32_16x16x32_bf16 v[72:75], v[36:39], v[212:215], 0\n"
        "v_mul_f32 v180, s88, v180\n"
        "v_mul_f32 v181, s88, v181\n"
        "v_mfma_f32_16x16x32_bf16 v[72:75], v[32:35], v[196:199], v[72:75]\n"
        "s_waitcnt lgkmcnt(4)\n"
        "v_mul_f32 v182, s88, v182\n"
        "v_mul_f32 v183, s88, v183\n"
        "v_mfma_f32_16x16x32_bf16 v[104:107], v[32:35], v[200:203], 0\n"
        "v_mfma_f32_16x16x32_bf16 v[104:107], v[36:39], v[216:219], v[104:107]\n"
        "v_mfma_f32_16x16x32_bf16 v[108:111], v[44:47], v[216:219], 0\n"
        "v_mfma_f32_16x16x32_bf16 v[108:111], v[40:43], v[200:203], v[108:111]\n"
        "s_waitcnt vmcnt(12) lgkmcnt(0)\n"
        "s_barrier\n"
        "v_mfma_f32_16x16x32_bf16 v[140:143], v[40:43], v[204:207], 0\n"
        "global_load_lds_dwordx4 v238, s[68:69] offset:0\n"
        "ds_read_b64_tr_b16 v[40:41], v228 offset:49152\n"
        "v_mfma_f32_16x16x32_bf16 v[140:143], v[44:47], v[220:223], v[140:143]\n"
        "ds_read_b64_tr_b16 v[44:45], v228 offset:57344\n"
        "v_mfma_f32_16x16x32_bf16 v[136:139], v[36:39], v[220:223], 0\n"
        "ds_read_b64_tr_b16 v[36:37], v226 offset:57344\n"
        "v_mfma_f32_16x16x32_bf16 v[136:139], v[32:35], v[204:207], v[136:139]\n"
        "ds_read_b64_tr_b16 v[32:33], v226 offset:49152\n"
        "v_accvgpr_read_b32 v184, a232\n"
        "v_mfma_f32_16x16x32_bf16 v[16:19], v[48:51], v[192:195], 0\n"
        "ds_read_b64_tr_b16 v[34:35], v227 offset:49152\n"
        "v_mul_f32 v184, s88, v184\n"
        "v_mfma_f32_16x16x32_bf16 v[16:19], v[52:55], v[208:211], v[16:19]\n"
        "ds_read_b64_tr_b16 v[38:39], v227 offset:57344\n"
        "v_cvt_pk_bf16_f32 v184, v184, v185\n"
        "v_mfma_f32_16x16x32_bf16 v[20:23], v[60:63], v[208:211], 0\n"
        "ds_read_b64_tr_b16 v[46:47], v229 offset:57344\n"
        "v_cvt_pk_bf16_f32 v185, v186, v187\n"
        "v_mfma_f32_16x16x32_bf16 v[20:23], v[56:59], v[192:195], v[20:23]\n"
        "ds_read_b64_tr_b16 v[42:43], v229 offset:49152\n"
        "v_cvt_pk_bf16_f32 v186, v188, v189\n"
        "v_mfma_f32_16x16x32_bf16 v[84:87], v[56:59], v[196:199], 0\n"
        "global_load_lds_dwordx4 v239, s[68:69] offset:1024\n"
        "v_cvt_pk_bf16_f32 v187, v190, v191\n"
        "v_mfma_f32_16x16x32_bf16 v[84:87], v[60:63], v[212:215], v[84:87]\n"
        "v_accvgpr_write_b32 a228, v184\n"
        "v_accvgpr_write_b32 a229, v185\n"
        "v_mfma_f32_16x16x32_bf16 v[80:83], v[52:55], v[212:215], 0\n"
        "v_accvgpr_write_b32 a230, v186\n"
        "v_accvgpr_write_b32 a231, v187\n"
        "v_mfma_f32_16x16x32_bf16 v[80:83], v[48:51], v[196:199], v[80:83]\n"
        "v_accvgpr_read_b32 v185, a201\n"
        "v_accvgpr_read_b32 v186, a202\n"
        "v_mfma_f32_16x16x32_bf16 v[112:115], v[48:51], v[200:203], 0\n"
        "global_load_lds_dwordx4 v240, s[68:69] offset:2048\n"
        "v_accvgpr_read_b32 v187, a203\n"
        "v_mfma_f32_16x16x32_bf16 v[112:115], v[52:55], v[216:219], v[112:115]\n"
        "v_accvgpr_read_b32 v188, a204\n"
        "v_accvgpr_read_b32 v189, a205\n"
        "v_mfma_f32_16x16x32_bf16 v[116:119], v[60:63], v[216:219], 0\n"
        "v_accvgpr_read_b32 v190, a206\n"
        "v_accvgpr_read_b32 v191, a207\n"
        "v_mfma_f32_16x16x32_bf16 v[116:119], v[56:59], v[200:203], v[116:119]\n"
        "v_mul_f32 v185, s88, v185\n"
        "v_mul_f32 v186, s88, v186\n"
        "v_mfma_f32_16x16x32_bf16 v[148:151], v[56:59], v[204:207], 0\n"
        "global_store_dwordx4 v254, a[224:227], s[70:71] offset:0 sc1\n"
        "ds_read_b64_tr_b16 v[56:57], v232 offset:49152\n"
        "ds_read_b64_tr_b16 v[58:59], v233 offset:49152\n"
        "v_mul_f32 v187, s88, v187\n"
        "v_mfma_f32_16x16x32_bf16 v[148:151], v[60:63], v[220:223], v[148:151]\n"
        "ds_read_b64_tr_b16 v[60:61], v232 offset:57344\n"
        "v_mul_f32 v188, s88, v188\n"
        "v_mfma_f32_16x16x32_bf16 v[144:147], v[52:55], v[220:223], 0\n"
        "ds_read_b64_tr_b16 v[52:53], v230 offset:57344\n"
        "v_mul_f32 v189, s88, v189\n"
        "v_mfma_f32_16x16x32_bf16 v[144:147], v[48:51], v[204:207], v[144:147]\n"
        "global_load_lds_dwordx4 v241, s[68:69] offset:3072\n"
        "s_add_u32 m0, s72, 0x0\n"
        "v_mfma_f32_16x16x32_bf16 v[24:27], v[160:163], v[192:195], 0\n"
        "ds_read_b64_tr_b16 v[48:49], v230 offset:49152\n"
        "s_add_u32 s68, s68, 0x80000\n"
        "s_addc_u32 s69, s69, 0\n"
        "v_mfma_f32_16x16x32_bf16 v[24:27], v[164:167], v[208:211], v[24:27]\n"
        "ds_read_b64_tr_b16 v[50:51], v231 offset:49152\n"
        "v_mul_f32 v190, s88, v190\n"
        "v_mfma_f32_16x16x32_bf16 v[28:31], v[172:175], v[208:211], 0\n"
        "ds_read_b64_tr_b16 v[54:55], v231 offset:57344\n"
        "v_mul_f32 v191, s88, v191\n"
        "v_mfma_f32_16x16x32_bf16 v[28:31], v[168:171], v[192:195], v[28:31]\n"
        "ds_read_b64_tr_b16 v[62:63], v233 offset:57344\n"
        "global_load_lds_dwordx4 v242, s[64:65] offset:0\n"
        "v_mfma_f32_16x16x32_bf16 v[92:95], v[168:171], v[196:199], 0\n"
        "ds_read_b128 v[192:195], v248 offset:2048\n"
        "v_mfma_f32_16x16x32_bf16 v[92:95], v[172:175], v[212:215], v[92:95]\n"
        "ds_read_b128 v[208:211], v249 offset:2048\n"
        "v_mfma_f32_16x16x32_bf16 v[88:91], v[164:167], v[212:215], 0\n"
        "ds_read_b128 v[212:215], v249 offset:0\n"
        "v_mfma_f32_16x16x32_bf16 v[88:91], v[160:163], v[196:199], v[88:91]\n"
        "global_load_lds_dwordx4 v243, s[64:65] offset:1024\n"
        "v_mfma_f32_16x16x32_bf16 v[120:123], v[160:163], v[200:203], 0\n"
        "ds_read_b128 v[196:199], v248 offset:0\n"
        "v_accvgpr_read_b32 v176, a192\n"
        "v_mfma_f32_16x16x32_bf16 v[120:123], v[164:167], v[216:219], v[120:123]\n"
        "v_mul_f32 v176, s88, v176\n"
        "v_cvt_pk_bf16_f32 v176, v176, v177\n"
        "v_mfma_f32_16x16x32_bf16 v[124:127], v[172:175], v[216:219], 0\n"
        "ds_read_b128 v[216:219], v249 offset:6144\n"
        "v_cvt_pk_bf16_f32 v177, v178, v179\n"
        "v_mfma_f32_16x16x32_bf16 v[124:127], v[168:171], v[200:203], v[124:127]\n"
        "s_waitcnt lgkmcnt(5)\n"
        "v_cvt_pk_bf16_f32 v178, v180, v181\n"
        "v_cvt_pk_bf16_f32 v179, v182, v183\n"
        "v_mfma_f32_16x16x32_bf16 v[156:159], v[168:171], v[204:207], 0\n"
        "s_waitcnt lgkmcnt(4)\n"
        "v_accvgpr_write_b32 a232, v176\n"
        "v_accvgpr_write_b32 a233, v177\n"
        "v_mfma_f32_16x16x32_bf16 v[156:159], v[172:175], v[220:223], v[156:159]\n"
        "s_waitcnt lgkmcnt(3)\n"
        "v_accvgpr_write_b32 a234, v178\n"
        "v_accvgpr_write_b32 a235, v179\n"
        "v_mfma_f32_16x16x32_bf16 v[152:155], v[164:167], v[220:223], 0\n"
        "global_load_lds_dwordx4 v244, s[64:65] offset:2048\n"
        "ds_read_b128 v[200:203], v248 offset:6144\n"
        "v_mfma_f32_16x16x32_bf16 v[152:155], v[160:163], v[204:207], v[152:155]\n"
        "LHA5_%=:\n"
        "s_cbranch_execz LHA9_%=\n"
        "ds_read_b128 v[204:207], v248 offset:4096\n"
        "v_accvgpr_read_b32 v177, a161\n"
        "v_mfma_f32_16x16x32_bf16 v[0:3], v[32:35], v[192:195], v[0:3]\n"
        "ds_read_b128 v[220:223], v249 offset:4096\n"
        "v_accvgpr_read_b32 v178, a162\n"
        "v_mfma_f32_16x16x32_bf16 v[0:3], v[36:39], v[208:211], v[0:3]\n"
        "ds_read_b64_tr_b16 v[160:161], v226 offset:50176\n"
        "v_accvgpr_read_b32 v179, a163\n"
        "v_mfma_f32_16x16x32_bf16 v[4:7], v[44:47], v[208:211], v[4:7]\n"
        "global_load_lds_dwordx4 v245, s[64:65] offset:3072\n"
        "ds_read_b64_tr_b16 v[162:163], v227 offset:50176\n"
        "s_add_u32 m0, s72, 0x1000\n"
        "v_mfma_f32_16x16x32_bf16 v[4:7], v[40:43], v[192:195], v[4:7]\n"
        "ds_read_b64_tr_b16 v[164:165], v226 offset:58368\n"
        "s_waitcnt lgkmcnt(6)\n"
        "v_mfma_f32_16x16x32_bf16 v[68:71], v[40:43], v[196:199], v[68:71]\n"
        "ds_read_b64_tr_b16 v[166:167], v227 offset:58368\n"
        "s_add_u32 s64, s64, 0x80\n"
        "s_addc_u32 s65, s65, 0\n"
        "v_mfma_f32_16x16x32_bf16 v[68:71], v[44:47], v[212:215], v[68:71]\n"
        "ds_read_b64_tr_b16 v[172:173], v228 offset:58368\n"
        "v_accvgpr_read_b32 v180, a164\n"
        "v_mfma_f32_16x16x32_bf16 v[64:67], v[36:39], v[212:215], v[64:67]\n"
        "ds_read_b64_tr_b16 v[174:175], v229 offset:58368\n"
        "global_load_lds_dwordx4 v242, s[66:67] offset:0\n"
        "v_mfma_f32_16x16x32_bf16 v[64:67], v[32:35], v[196:199], v[64:67]\n"
        "ds_read_b64_tr_b16 v[168:169], v228 offset:50176\n"
        "s_waitcnt lgkmcnt(8)\n"
        "v_mfma_f32_16x16x32_bf16 v[96:99], v[32:35], v[200:203], v[96:99]\n"
        "ds_read_b64_tr_b16 v[170:171], v229 offset:50176\n"
        "v_accvgpr_read_b32 v181, a165\n"
        "v_mfma_f32_16x16x32_bf16 v[96:99], v[36:39], v[216:219], v[96:99]\n"
        "s_waitcnt lgkmcnt(7)\n"
        "v_accvgpr_read_b32 v182, a166\n"
        "v_accvgpr_read_b32 v183, a167\n"
        "v_mfma_f32_16x16x32_bf16 v[100:103], v[44:47], v[216:219], v[100:103]\n"
        "global_load_lds_dwordx4 v243, s[66:67] offset:1024\n"
        "v_mfma_f32_16x16x32_bf16 v[100:103], v[40:43], v[200:203], v[100:103]\n"
        "v_mul_f32 v177, s88, v177\n"
        "v_mul_f32 v178, s88, v178\n"
        "v_mfma_f32_16x16x32_bf16 v[132:135], v[40:43], v[204:207], v[132:135]\n"
        "ds_read_b64_tr_b16 v[40:41], v232 offset:50176\n"
        "v_mul_f32 v179, s88, v179\n"
        "v_mfma_f32_16x16x32_bf16 v[132:135], v[44:47], v[220:223], v[132:135]\n"
        "ds_read_b64_tr_b16 v[44:45], v232 offset:58368\n"
        "v_mul_f32 v180, s88, v180\n"
        "v_mfma_f32_16x16x32_bf16 v[128:131], v[36:39], v[220:223], v[128:131]\n"
        "global_load_lds_dwordx4 v244, s[66:67] offset:2048\n"
        "v_mfma_f32_16x16x32_bf16 v[128:131], v[32:35], v[204:207], v[128:131]\n"
        "ds_read_b64_tr_b16 v[32:33], v230 offset:50176\n"
        "v_accvgpr_read_b32 v184, a200\n"
        "v_mfma_f32_16x16x32_bf16 v[8:11], v[48:51], v[192:195], v[8:11]\n"
        "ds_read_b64_tr_b16 v[34:35], v231 offset:50176\n"
        "v_mul_f32 v184, s88, v184\n"
        "v_mfma_f32_16x16x32_bf16 v[8:11], v[52:55], v[208:211], v[8:11]\n"
        "ds_read_b64_tr_b16 v[36:37], v230 offset:58368\n"
        "v_cvt_pk_bf16_f32 v184, v184, v185\n"
        "v_mfma_f32_16x16x32_bf16 v[12:15], v[60:63], v[208:211], v[12:15]\n"
        "global_load_lds_dwordx4 v245, s[66:67] offset:3072\n"
        "s_add_u32 m0, s73, 0x24000\n"
        "v_mfma_f32_16x16x32_bf16 v[12:15], v[56:59], v[192:195], v[12:15]\n"
        "ds_read_b64_tr_b16 v[38:39], v231 offset:58368\n"
        "v_cvt_pk_bf16_f32 v185, v186, v187\n"
        "v_mfma_f32_16x16x32_bf16 v[76:79], v[56:59], v[196:199], v[76:79]\n"
        "ds_read_b64_tr_b16 v[46:47], v233 offset:58368\n"
        "v_cvt_pk_bf16_f32 v186, v188, v189\n"
        "v_mfma_f32_16x16x32_bf16 v[76:79], v[60:63], v[212:215], v[76:79]\n"
        "s_waitcnt lgkmcnt(5)\n"
        "v_cvt_pk_bf16_f32 v187, v190, v191\n"
        "v_accvgpr_write_b32 a236, v184\n"
        "v_mfma_f32_16x16x32_bf16 v[72:75], v[52:55], v[212:215], v[72:75]\n"
        "ds_read_b64_tr_b16 v[42:43], v233 offset:50176\n"
        "v_accvgpr_write_b32 a237, v185\n"
        "v_mfma_f32_16x16x32_bf16 v[72:75], v[48:51], v[196:199], v[72:75]\n"
        "s_add_u32 s66, s66, 0x80\n"
        "s_addc_u32 s67, s67, 0\n"
        "v_accvgpr_write_b32 a238, v186\n"
        "v_mfma_f32_16x16x32_bf16 v[104:107], v[48:51], v[200:203], v[104:107]\n"
        "v_accvgpr_write_b32 a239, v187\n"
        "v_mul_f32 v181, s88, v181\n"
        "v_mfma_f32_16x16x32_bf16 v[104:107], v[52:55], v[216:219], v[104:107]\n"
        "v_mul_f32 v182, s88, v182\n"
        "v_mul_f32 v183, s88, v183\n"
        "v_mfma_f32_16x16x32_bf16 v[108:111], v[60:63], v[216:219], v[108:111]\n"
        "v_accvgpr_read_b32 v185, a169\n"
        "v_accvgpr_read_b32 v186, a170\n"
        "v_mfma_f32_16x16x32_bf16 v[108:111], v[56:59], v[200:203], v[108:111]\n"
        "v_accvgpr_read_b32 v187, a171\n"
        "v_accvgpr_read_b32 v188, a172\n"
        "v_mfma_f32_16x16x32_bf16 v[140:143], v[56:59], v[204:207], v[140:143]\n"
        "v_accvgpr_read_b32 v189, a173\n"
        "v_accvgpr_read_b32 v190, a174\n"
        "v_mfma_f32_16x16x32_bf16 v[140:143], v[60:63], v[220:223], v[140:143]\n"
        "v_accvgpr_read_b32 v191, a175\n"
        "v_mul_f32 v185, s88, v185\n"
        "v_mfma_f32_16x16x32_bf16 v[136:139], v[52:55], v[220:223], v[136:139]\n"
        "s_waitcnt vmcnt(13) lgkmcnt(0)\n"
        "s_barrier\n"
        "v_mul_f32 v186, s88, v186\n"
        "v_mfma_f32_16x16x32_bf16 v[136:139], v[48:51], v[204:207], v[136:139]\n"
        "global_load_lds_dwordx4 v238, s[68:69] offset:0\n"
        "ds_read_b64_tr_b16 v[48:49], v226 offset:16384\n"
        "v_mfma_f32_16x16x32_bf16 v[16:19], v[160:163], v[192:195], v[16:19]\n"
        "ds_read_b64_tr_b16 v[50:51], v227 offset:16384\n"
        "v_mul_f32 v187, s88, v187\n"
        "v_mfma_f32_16x16x32_bf16 v[16:19], v[164:167], v[208:211], v[16:19]\n"
        "ds_read_b64_tr_b16 v[52:53], v226 offset:24576\n"
        "v_mul_f32 v188, s88, v188\n"
        "v_mfma_f32_16x16x32_bf16 v[20:23], v[172:175], v[208:211], v[20:23]\n"
        "ds_read_b64_tr_b16 v[54:55], v227 offset:24576\n"
        "v_mul_f32 v189, s88, v189\n"
        "v_mfma_f32_16x16x32_bf16 v[20:23], v[168:171], v[192:195], v[20:23]\n"
        "ds_read_b64_tr_b16 v[60:61], v228 offset:24576\n"
        "global_load_lds_dwordx4 v239, s[68:69] offset:1024\n"
        "v_mfma_f32_16x16x32_bf16 v[84:87], v[168:171], v[196:199], v[84:87]\n"
        "ds_read_b64_tr_b16 v[62:63], v229 offset:24576\n"
        "v_mul_f32 v190, s88, v190\n"
        "v_mfma_f32_16x16x32_bf16 v[84:87], v[172:175], v[212:215], v[84:87]\n"
        "ds_read_b64_tr_b16 v[56:57], v228 offset:16384\n"
        "v_mul_f32 v191, s88, v191\n"
        "v_mfma_f32_16x16x32_bf16 v[80:83], v[164:167], v[212:215], v[80:83]\n"
        "ds_read_b64_tr_b16 v[58:59], v229 offset:16384\n"
        "v_mfma_f32_16x16x32_bf16 v[80:83], v[160:163], v[196:199], v[80:83]\n"
        "v_accvgpr_read_b32 v176, a160\n"
        "v_mul_f32 v176, s88, v176\n"
        "v_mfma_f32_16x16x32_bf16 v[112:115], v[160:163], v[200:203], v[112:115]\n"
        "global_load_lds_dwordx4 v240, s[68:69] offset:2048\n"
        "v_cvt_pk_bf16_f32 v176, v176, v177\n"
        "v_mfma_f32_16x16x32_bf16 v[112:115], v[164:167], v[216:219], v[112:115]\n"
        "v_cvt_pk_bf16_f32 v177, v178, v179\n"
        "v_cvt_pk_bf16_f32 v178, v180, v181\n"
        "v_mfma_f32_16x16x32_bf16 v[116:119], v[172:175], v[216:219], v[116:119]\n"
        "v_cvt_pk_bf16_f32 v179, v182, v183\n"
        "v_accvgpr_write_b32 a192, v176\n"
        "v_mfma_f32_16x16x32_bf16 v[116:119], v[168:171], v[200:203], v[116:119]\n"
        "v_accvgpr_write_b32 a193, v177\n"
        "v_accvgpr_write_b32 a194, v178\n"
        "v_mfma_f32_16x16x32_bf16 v[148:151], v[168:171], v[204:207], v[148:151]\n"
        "global_load_lds_dwordx4 v241, s[68:69] offset:3072\n"
        "s_add_u32 m0, s72, 0x10000\n"
        "v_accvgpr_write_b32 a195, v179\n"
        "v_mfma_f32_16x16x32_bf16 v[148:151], v[172:175], v[220:223], v[148:151]\n"
        "ds_read_b64_tr_b16 v[172:173], v232 offset:24576\n"
        "s_add_u32 s68, s68, 0x80000\n"
        "s_addc_u32 s69, s69, 0\n"
        "v_mfma_f32_16x16x32_bf16 v[144:147], v[164:167], v[220:223], v[144:147]\n"
        "ds_read_b64_tr_b16 v[164:165], v230 offset:24576\n"
        "v_accvgpr_read_b32 v177, a1\n"
        "v_mfma_f32_16x16x32_bf16 v[144:147], v[160:163], v[204:207], v[144:147]\n"
        "ds_read_b64_tr_b16 v[160:161], v230 offset:16384\n"
        "v_accvgpr_read_b32 v178, a2\n"
        "v_mfma_f32_16x16x32_bf16 v[24:27], v[32:35], v[192:195], v[24:27]\n"
        "ds_read_b64_tr_b16 v[162:163], v231 offset:16384\n"
        "global_load_lds_dwordx4 v242, s[64:65] offset:0\n"
        "v_mfma_f32_16x16x32_bf16 v[24:27], v[36:39], v[208:211], v[24:27]\n"
        "ds_read_b64_tr_b16 v[166:167], v231 offset:24576\n"
        "v_accvgpr_read_b32 v179, a3\n"
        "v_mfma_f32_16x16x32_bf16 v[28:31], v[44:47], v[208:211], v[28:31]\n"
        "ds_read_b64_tr_b16 v[174:175], v233 offset:24576\n"
        "v_accvgpr_read_b32 v180, a4\n"
        "v_mfma_f32_16x16x32_bf16 v[28:31], v[40:43], v[192:195], v[28:31]\n"
        "ds_read_b64_tr_b16 v[168:169], v232 offset:16384\n"
        "v_accvgpr_read_b32 v181, a5\n"
        "v_mfma_f32_16x16x32_bf16 v[92:95], v[40:43], v[196:199], v[92:95]\n"
        "ds_read_b64_tr_b16 v[170:171], v233 offset:16384\n"
        "global_load_lds_dwordx4 v243, s[64:65] offset:1024\n"
        "v_mfma_f32_16x16x32_bf16 v[92:95], v[44:47], v[212:215], v[92:95]\n"
        "ds_read_b128 v[192:195], v246 offset:34816\n"
        "v_accvgpr_read_b32 v182, a6\n"
        "v_mfma_f32_16x16x32_bf16 v[88:91], v[36:39], v[212:215], v[88:91]\n"
        "ds_read_b128 v[208:211], v247 offset:34816\n"
        "v_accvgpr_read_b32 v183, a7\n"
        "v_mfma_f32_16x16x32_bf16 v[88:91], v[32:35], v[196:199], v[88:91]\n"
        "ds_read_b128 v[196:199], v246 offset:32768\n"
        "v_mul_f32 v177, s88, v177\n"
        "v_mfma_f32_16x16x32_bf16 v[120:123], v[32:35], v[200:203], v[120:123]\n"
        "ds_read_b128 v[212:215], v247 offset:32768\n"
        "global_load_lds_dwordx4 v244, s[64:65] offset:2048\n"
        "v_mfma_f32_16x16x32_bf16 v[120:123], v[36:39], v[216:219], v[120:123]\n"
        "v_mul_f32 v178, s88, v178\n"
        "v_mul_f32 v179, s88, v179\n"
        "v_mfma_f32_16x16x32_bf16 v[124:127], v[44:47], v[216:219], v[124:127]\n"
        "ds_read_b128 v[216:219], v247 offset:38912\n"
        "v_mul_f32 v180, s88, v180\n"
        "v_mfma_f32_16x16x32_bf16 v[124:127], v[40:43], v[200:203], v[124:127]\n"
        "ds_read_b128 v[200:203], v246 offset:38912\n"
        "v_mul_f32 v181, s88, v181\n"
        "v_mfma_f32_16x16x32_bf16 v[156:159], v[40:43], v[204:207], v[156:159]\n"
        "global_load_lds_dwordx4 v245, s[64:65] offset:3072\n"
        "s_add_u32 m0, s72, 0x11000\n"
        "v_mfma_f32_16x16x32_bf16 v[156:159], v[44:47], v[220:223], v[156:159]\n"
        "ds_read_b64_tr_b16 v[44:45], v228 offset:25600\n"
        "s_add_u32 s64, s64, 0x80\n"
        "s_addc_u32 s65, s65, 0\n"
        "v_mfma_f32_16x16x32_bf16 v[152:155], v[36:39], v[220:223], v[152:155]\n"
        "ds_read_b128 v[220:223], v247 offset:36864\n"
        "v_mul_f32 v182, s88, v182\n"
        "v_mfma_f32_16x16x32_bf16 v[152:155], v[32:35], v[204:207], v[152:155]\n"
        "ds_read_b128 v[204:207], v246 offset:36864\n"
        "s_waitcnt lgkmcnt(6)\n"
        "v_mfma_f32_16x16x32_bf16 v[0:3], v[48:51], v[192:195], v[0:3]\n"
        "s_waitcnt lgkmcnt(5)\n"
        "v_accvgpr_read_b32 v184, a168\n"
        "v_mul_f32 v184, s88, v184\n"
        "v_mfma_f32_16x16x32_bf16 v[0:3], v[52:55], v[208:211], v[0:3]\n"
        "global_load_lds_dwordx4 v242, s[66:67] offset:0\n"
        "v_cvt_pk_bf16_f32 v184, v184, v185\n"
        "v_mfma_f32_16x16x32_bf16 v[4:7], v[60:63], v[208:211], v[4:7]\n"
        "s_waitcnt lgkmcnt(4)\n"
        "v_cvt_pk_bf16_f32 v185, v186, v187\n"
        "v_cvt_pk_bf16_f32 v186, v188, v189\n"
        "v_mfma_f32_16x16x32_bf16 v[4:7], v[56:59], v[192:195], v[4:7]\n"
        "s_waitcnt lgkmcnt(3)\n"
        "v_cvt_pk_bf16_f32 v187, v190, v191\n"
        "v_accvgpr_write_b32 a196, v184\n"
        "v_mfma_f32_16x16x32_bf16 v[68:71], v[56:59], v[196:199], v[68:71]\n"
        "ds_read_b64_tr_b16 v[32:33], v226 offset:17408\n"
        "v_accvgpr_write_b32 a197, v185\n"
        "v_mfma_f32_16x16x32_bf16 v[68:71], v[60:63], v[212:215], v[68:71]\n"
        "s_waitcnt lgkmcnt(3)\n"
        "v_accvgpr_write_b32 a198, v186\n"
        "v_accvgpr_write_b32 a199, v187\n"
        "v_mfma_f32_16x16x32_bf16 v[64:67], v[52:55], v[212:215], v[64:67]\n"
        "global_store_dwordx4 v254, a[228:231], s[70:71] offset:1024 sc1\n"
        "ds_read_b64_tr_b16 v[34:35], v227 offset:17408\n"
        "ds_read_b64_tr_b16 v[36:37], v226 offset:25600\n"
        "v_mul_f32 v183, s88, v183\n"
        "v_mfma_f32_16x16x32_bf16 v[64:67], v[48:51], v[196:199], v[64:67]\n"
        "ds_read_b64_tr_b16 v[38:39], v227 offset:25600\n"
        "v_accvgpr_read_b32 v185, a9\n"
        "v_mfma_f32_16x16x32_bf16 v[96:99], v[48:51], v[200:203], v[96:99]\n"
        "ds_read_b64_tr_b16 v[46:47], v229 offset:25600\n"
        "v_accvgpr_read_b32 v186, a10\n"
        "v_mfma_f32_16x16x32_bf16 v[96:99], v[52:55], v[216:219], v[96:99]\n"
        "ds_read_b64_tr_b16 v[40:41], v228 offset:17408\n"
        "global_load_lds_dwordx4 v243, s[66:67] offset:1024\n"
        "v_mfma_f32_16x16x32_bf16 v[100:103], v[60:63], v[216:219], v[100:103]\n"
        "ds_read_b64_tr_b16 v[42:43], v229 offset:17408\n"
        "v_accvgpr_read_b32 v187, a11\n"
        "v_mfma_f32_16x16x32_bf16 v[100:103], v[56:59], v[200:203], v[100:103]\n"
        "s_waitcnt lgkmcnt(7)\n"
        "v_accvgpr_read_b32 v188, a12\n"
        "v_accvgpr_read_b32 v189, a13\n"
        "v_mfma_f32_16x16x32_bf16 v[132:135], v[56:59], v[204:207], v[132:135]\n"
        "ds_read_b64_tr_b16 v[56:57], v232 offset:17408\n"
        "v_accvgpr_read_b32 v190, a14\n"
        "v_mfma_f32_16x16x32_bf16 v[132:135], v[60:63], v[220:223], v[132:135]\n"
        "global_load_lds_dwordx4 v244, s[66:67] offset:2048\n"
        "v_mfma_f32_16x16x32_bf16 v[128:131], v[52:55], v[220:223], v[128:131]\n"
        "ds_read_b64_tr_b16 v[52:53], v230 offset:25600\n"
        "v_accvgpr_read_b32 v191, a15\n"
        "v_mfma_f32_16x16x32_bf16 v[128:131], v[48:51], v[204:207], v[128:131]\n"
        "ds_read_b64_tr_b16 v[48:49], v230 offset:17408\n"
        "v_mul_f32 v185, s88, v185\n"
        "v_mfma_f32_16x16x32_bf16 v[8:11], v[160:163], v[192:195], v[8:11]\n"
        "ds_read_b64_tr_b16 v[50:51], v231 offset:17408\n"
        "v_mul_f32 v186, s88, v186\n"
        "v_mfma_f32_16x16x32_bf16 v[8:11], v[164:167], v[208:211], v[8:11]\n"
        "global_load_lds_dwordx4 v245, s[66:67] offset:3072\n"
        "s_add_u32 m0, s73, 0x1c000\n"
        "v_mfma_f32_16x16x32_bf16 v[12:15], v[172:175], v[208:211], v[12:15]\n"
        "ds_read_b64_tr_b16 v[54:55], v231 offset:25600\n"
        "s_add_u32 s66, s66, 0x80\n"
        "s_addc_u32 s67, s67, 0\n"
        "v_mfma_f32_16x16x32_bf16 v[12:15], v[168:171], v[192:195], v[12:15]\n"
        "ds_read_b64_tr_b16 v[60:61], v232 offset:25600\n"
        "v_mul_f32 v187, s88, v187\n"
        "v_mfma_f32_16x16x32_bf16 v[76:79], v[168:171], v[196:199], v[76:79]\n"
        "ds_read_b64_tr_b16 v[62:63], v233 offset:25600\n"
        "v_mul_f32 v188, s88, v188\n"
        "v_mfma_f32_16x16x32_bf16 v[76:79], v[172:175], v[212:215], v[76:79]\n"
        "ds_read_b64_tr_b16 v[58:59], v233 offset:17408\n"
        "v_mul_f32 v189, s88, v189\n"
        "v_mfma_f32_16x16x32_bf16 v[72:75], v[164:167], v[212:215], v[72:75]\n"
        "s_waitcnt lgkmcnt(6)\n"
        "v_mul_f32 v190, s88, v190\n"
        "v_mul_f32 v191, s88, v191\n"
        "v_mfma_f32_16x16x32_bf16 v[72:75], v[160:163], v[196:199], v[72:75]\n"
        "s_add_u32 s70, s38, s97\n"
        "s_addc_u32 s71, s39, 0\n"
        "v_accvgpr_read_b32 v176, a0\n"
        "v_mfma_f32_16x16x32_bf16 v[104:107], v[160:163], v[200:203], v[104:107]\n"
        "v_mul_f32 v176, s88, v176\n"
        "v_cvt_pk_bf16_f32 v176, v176, v177\n"
        "v_mfma_f32_16x16x32_bf16 v[104:107], v[164:167], v[216:219], v[104:107]\n"
        "v_cvt_pk_bf16_f32 v177, v178, v179\n"
        "v_cvt_pk_bf16_f32 v178, v180, v181\n"
        "v_mfma_f32_16x16x32_bf16 v[108:111], v[172:175], v[216:219], v[108:111]\n"
        "v_cvt_pk_bf16_f32 v179, v182, v183\n"
        "v_accvgpr_write_b32 a200, v176\n"
        "v_mfma_f32_16x16x32_bf16 v[108:111], v[168:171], v[200:203], v[108:111]\n"
        "v_accvgpr_write_b32 a201, v177\n"
        "v_accvgpr_write_b32 a202, v178\n"
        "v_mfma_f32_16x16x32_bf16 v[140:143], v[168:171], v[204:207], v[140:143]\n"
        "v_accvgpr_write_b32 a203, v179\n"
        "v_accvgpr_read_b32 v177, a33\n"
        "v_mfma_f32_16x16x32_bf16 v[140:143], v[172:175], v[220:223], v[140:143]\n"
        "s_waitcnt vmcnt(13) lgkmcnt(0)\n"
        "s_barrier\n"
        "v_accvgpr_read_b32 v178, a34\n"
        "v_mfma_f32_16x16x32_bf16 v[136:139], v[164:167], v[220:223], v[136:139]\n"
        "global_load_lds_dwordx4 v238, s[68:69] offset:0\n"
        "ds_read_b64_tr_b16 v[164:165], v226 offset:40960\n"
        "v_mfma_f32_16x16x32_bf16 v[136:139], v[160:163], v[204:207], v[136:139]\n"
        "ds_read_b64_tr_b16 v[160:161], v226 offset:32768\n"
        "v_accvgpr_read_b32 v179, a35\n"
        "v_mfma_f32_16x16x32_bf16 v[16:19], v[32:35], v[192:195], v[16:19]\n"
        "ds_read_b64_tr_b16 v[162:163], v227 offset:32768\n"
        "v_accvgpr_read_b32 v180, a36\n"
        "v_mfma_f32_16x16x32_bf16 v[16:19], v[36:39], v[208:211], v[16:19]\n"
        "ds_read_b64_tr_b16 v[166:167], v227 offset:40960\n"
        "v_accvgpr_read_b32 v181, a37\n"
        "v_mfma_f32_16x16x32_bf16 v[20:23], v[44:47], v[208:211], v[20:23]\n"
        "ds_read_b64_tr_b16 v[172:173], v228 offset:40960\n"
        "global_load_lds_dwordx4 v239, s[68:69] offset:1024\n"
        "v_mfma_f32_16x16x32_bf16 v[20:23], v[40:43], v[192:195], v[20:23]\n"
        "ds_read_b64_tr_b16 v[174:175], v229 offset:40960\n"
        "v_accvgpr_read_b32 v182, a38\n"
        "v_mfma_f32_16x16x32_bf16 v[84:87], v[40:43], v[196:199], v[84:87]\n"
        "ds_read_b64_tr_b16 v[168:169], v228 offset:32768\n"
        "v_accvgpr_read_b32 v183, a39\n"
        "v_mfma_f32_16x16x32_bf16 v[84:87], v[44:47], v[212:215], v[84:87]\n"
        "ds_read_b64_tr_b16 v[170:171], v229 offset:32768\n"
        "v_mul_f32 v177, s88, v177\n"
        "v_mfma_f32_16x16x32_bf16 v[80:83], v[36:39], v[212:215], v[80:83]\n"
        "global_load_lds_dwordx4 v240, s[68:69] offset:2048\n"
        "v_mfma_f32_16x16x32_bf16 v[80:83], v[32:35], v[196:199], v[80:83]\n"
        "v_mul_f32 v178, s88, v178\n"
        "v_mul_f32 v179, s88, v179\n"
        "v_mfma_f32_16x16x32_bf16 v[112:115], v[32:35], v[200:203], v[112:115]\n"
        "v_mul_f32 v180, s88, v180\n"
        "v_mul_f32 v181, s88, v181\n"
        "v_mfma_f32_16x16x32_bf16 v[112:115], v[36:39], v[216:219], v[112:115]\n"
        "v_mul_f32 v182, s88, v182\n"
        "v_mul_f32 v183, s88, v183\n"
        "v_mfma_f32_16x16x32_bf16 v[116:119], v[44:47], v[216:219], v[116:119]\n"
        "global_load_lds_dwordx4 v241, s[68:69] offset:3072\n"
        "s_add_u32 m0, s72, 0x8000\n"
        "v_mfma_f32_16x16x32_bf16 v[116:119], v[40:43], v[200:203], v[116:119]\n"
        "s_add_u32 s68, s68, 0x80000\n"
        "s_addc_u32 s69, s69, 0\n"
        "v_mfma_f32_16x16x32_bf16 v[148:151], v[40:43], v[204:207], v[148:151]\n"
        "ds_read_b64_tr_b16 v[40:41], v232 offset:32768\n"
        "v_mfma_f32_16x16x32_bf16 v[148:151], v[44:47], v[220:223], v[148:151]\n"
        "ds_read_b64_tr_b16 v[44:45], v232 offset:40960\n"
        "v_mfma_f32_16x16x32_bf16 v[144:147], v[36:39], v[220:223], v[144:147]\n"
        "global_load_lds_dwordx4 v242, s[64:65] offset:0\n"
        "v_mfma_f32_16x16x32_bf16 v[144:147], v[32:35], v[204:207], v[144:147]\n"
        "ds_read_b64_tr_b16 v[32:33], v230 offset:32768\n"
        "v_accvgpr_read_b32 v184, a8\n"
        "v_mfma_f32_16x16x32_bf16 v[24:27], v[48:51], v[192:195], v[24:27]\n"
        "ds_read_b64_tr_b16 v[34:35], v231 offset:32768\n"
        "v_mul_f32 v184, s88, v184\n"
        "v_mfma_f32_16x16x32_bf16 v[24:27], v[52:55], v[208:211], v[24:27]\n"
        "ds_read_b64_tr_b16 v[36:37], v230 offset:40960\n"
        "v_cvt_pk_bf16_f32 v184, v184, v185\n"
        "v_mfma_f32_16x16x32_bf16 v[28:31], v[60:63], v[208:211], v[28:31]\n"
        "ds_read_b64_tr_b16 v[38:39], v231 offset:40960\n"
        "v_cvt_pk_bf16_f32 v185, v186, v187\n"
        "v_mfma_f32_16x16x32_bf16 v[28:31], v[56:59], v[192:195], v[28:31]\n"
        "ds_read_b64_tr_b16 v[46:47], v233 offset:40960\n"
        "v_cvt_pk_bf16_f32 v186, v188, v189\n"
        "v_mfma_f32_16x16x32_bf16 v[92:95], v[56:59], v[196:199], v[92:95]\n"
        "ds_read_b64_tr_b16 v[42:43], v233 offset:32768\n"
        "v_cvt_pk_bf16_f32 v187, v190, v191\n"
        "v_mfma_f32_16x16x32_bf16 v[92:95], v[60:63], v[212:215], v[92:95]\n"
        "ds_read_b128 v[192:195], v246 offset:2048\n"
        "v_accvgpr_write_b32 a204, v184\n"
        "v_mfma_f32_16x16x32_bf16 v[88:91], v[52:55], v[212:215], v[88:91]\n"
        "ds_read_b128 v[208:211], v247 offset:2048\n"
        "v_accvgpr_write_b32 a205, v185\n"
        "v_mfma_f32_16x16x32_bf16 v[88:91], v[48:51], v[196:199], v[88:91]\n"
        "global_load_lds_dwordx4 v243, s[64:65] offset:1024\n"
        "v_accvgpr_write_b32 a206, v186\n"
        "v_mfma_f32_16x16x32_bf16 v[120:123], v[48:51], v[200:203], v[120:123]\n"
        "ds_read_b128 v[196:199], v246 offset:0\n"
        "v_accvgpr_write_b32 a207, v187\n"
        "v_mfma_f32_16x16x32_bf16 v[120:123], v[52:55], v[216:219], v[120:123]\n"
        "ds_read_b128 v[212:215], v247 offset:0\n"
        "s_add_u32 s97, s52, 0x5000\n"
        "v_accvgpr_read_b32 v185, a41\n"
        "v_mfma_f32_16x16x32_bf16 v[124:127], v[60:63], v[216:219], v[124:127]\n"
        "ds_read_b128 v[216:219], v247 offset:6144\n"
        "v_accvgpr_read_b32 v186, a42\n"
        "v_mfma_f32_16x16x32_bf16 v[124:127], v[56:59], v[200:203], v[124:127]\n"
        "global_load_lds_dwordx4 v244, s[64:65] offset:2048\n"
        "ds_read_b128 v[200:203], v246 offset:6144\n"
        "v_mfma_f32_16x16x32_bf16 v[156:159], v[56:59], v[204:207], v[156:159]\n"
        "ds_read_b64_tr_b16 v[56:57], v228 offset:33792\n"
        "v_accvgpr_read_b32 v187, a43\n"
        "v_mfma_f32_16x16x32_bf16 v[156:159], v[60:63], v[220:223], v[156:159]\n"
        "ds_read_b64_tr_b16 v[60:61], v228 offset:41984\n"
        "v_accvgpr_read_b32 v188, a44\n"
        "v_mfma_f32_16x16x32_bf16 v[152:155], v[52:55], v[220:223], v[152:155]\n"
        "ds_read_b128 v[220:223], v247 offset:4096\n"
        "v_accvgpr_read_b32 v189, a45\n"
        "v_mfma_f32_16x16x32_bf16 v[152:155], v[48:51], v[204:207], v[152:155]\n"
        "global_load_lds_dwordx4 v245, s[64:65] offset:3072\n"
        "s_add_u32 m0, s72, 0x9000\n"
        "s_waitcnt lgkmcnt(7)\n"
        "v_mfma_f32_16x16x32_bf16 v[0:3], v[160:163], v[192:195], v[0:3]\n"
        "ds_read_b128 v[204:207], v246 offset:4096\n"
        "s_add_u32 s64, s64, 0x80\n"
        "s_addc_u32 s65, s65, 0\n"
        "v_mfma_f32_16x16x32_bf16 v[0:3], v[164:167], v[208:211], v[0:3]\n"
        "ds_read_b64_tr_b16 v[48:49], v226 offset:33792\n"
        "v_accvgpr_read_b32 v190, a46\n"
        "v_mfma_f32_16x16x32_bf16 v[4:7], v[172:175], v[208:211], v[4:7]\n"
        "ds_read_b64_tr_b16 v[50:51], v227 offset:33792\n"
        "v_accvgpr_read_b32 v191, a47\n"
        "v_mfma_f32_16x16x32_bf16 v[4:7], v[168:171], v[192:195], v[4:7]\n"
        "ds_read_b64_tr_b16 v[52:53], v226 offset:41984\n"
        "s_waitcnt lgkmcnt(7)\n"
        "v_mfma_f32_16x16x32_bf16 v[68:71], v[168:171], v[196:199], v[68:71]\n"
        "ds_read_b64_tr_b16 v[54:55], v227 offset:41984\n"
        "global_load_lds_dwordx4 v242, s[66:67] offset:0\n"
        "v_mfma_f32_16x16x32_bf16 v[68:71], v[172:175], v[212:215], v[68:71]\n"
        "ds_read_b64_tr_b16 v[62:63], v229 offset:41984\n"
        "v_mul_f32 v185, s88, v185\n"
        "v_mfma_f32_16x16x32_bf16 v[64:67], v[164:167], v[212:215], v[64:67]\n"
        "ds_read_b64_tr_b16 v[58:59], v229 offset:33792\n"
        "v_mul_f32 v186, s88, v186\n"
        "v_mfma_f32_16x16x32_bf16 v[64:67], v[160:163], v[196:199], v[64:67]\n"
        "s_waitcnt lgkmcnt(7)\n"
        "v_accvgpr_read_b32 v176, a32\n"
        "v_mul_f32 v176, s88, v176\n"
        "v_mfma_f32_16x16x32_bf16 v[96:99], v[160:163], v[200:203], v[96:99]\n"
        "global_load_lds_dwordx4 v243, s[66:67] offset:1024\n"
        "v_mfma_f32_16x16x32_bf16 v[96:99], v[164:167], v[216:219], v[96:99]\n"
        "s_waitcnt lgkmcnt(5)\n"
        "v_cvt_pk_bf16_f32 v176, v176, v177\n"
        "v_cvt_pk_bf16_f32 v177, v178, v179\n"
        "v_mfma_f32_16x16x32_bf16 v[100:103], v[172:175], v[216:219], v[100:103]\n"
        "v_cvt_pk_bf16_f32 v178, v180, v181\n"
        "v_cvt_pk_bf16_f32 v179, v182, v183\n"
        "v_mfma_f32_16x16x32_bf16 v[100:103], v[168:171], v[200:203], v[100:103]\n"
        "v_accvgpr_write_b32 a160, v176\n"
        "v_accvgpr_write_b32 a161, v177\n"
        "v_mfma_f32_16x16x32_bf16 v[132:135], v[168:171], v[204:207], v[132:135]\n"
        "global_load_lds_dwordx4 v244, s[66:67] offset:2048\n"
        "v_mfma_f32_16x16x32_bf16 v[132:135], v[172:175], v[220:223], v[132:135]\n"
        "ds_read_b64_tr_b16 v[172:173], v232 offset:41984\n"
        "v_accvgpr_write_b32 a162, v178\n"
        "v_mfma_f32_16x16x32_bf16 v[128:131], v[164:167], v[220:223], v[128:131]\n"
        "ds_read_b64_tr_b16 v[164:165], v230 offset:41984\n"
        "v_accvgpr_write_b32 a163, v179\n"
        "v_mfma_f32_16x16x32_bf16 v[128:131], v[160:163], v[204:207], v[128:131]\n"
        "ds_read_b64_tr_b16 v[160:161], v230 offset:33792\n"
        "v_mul_f32 v187, s88, v187\n"
        "v_mfma_f32_16x16x32_bf16 v[8:11], v[32:35], v[192:195], v[8:11]\n"
        "global_load_lds_dwordx4 v245, s[66:67] offset:3072\n"
        "s_add_u32 m0, s73, 0x20000\n"
        "v_mfma_f32_16x16x32_bf16 v[8:11], v[36:39], v[208:211], v[8:11]\n"
        "ds_read_b64_tr_b16 v[162:163], v231 offset:33792\n"
        "s_add_u32 s66, s66, 0x80\n"
        "s_addc_u32 s67, s67, 0\n"
        "v_mfma_f32_16x16x32_bf16 v[12:15], v[44:47], v[208:211], v[12:15]\n"
        "ds_read_b64_tr_b16 v[166:167], v231 offset:41984\n"
        "v_mul_f32 v188, s88, v188\n"
        "v_mfma_f32_16x16x32_bf16 v[12:15], v[40:43], v[192:195], v[12:15]\n"
        "ds_read_b64_tr_b16 v[174:175], v233 offset:41984\n"
        "v_mul_f32 v189, s88, v189\n"
        "v_mfma_f32_16x16x32_bf16 v[76:79], v[40:43], v[196:199], v[76:79]\n"
        "ds_read_b64_tr_b16 v[168:169], v232 offset:33792\n"
        "v_mul_f32 v190, s88, v190\n"
        "v_mfma_f32_16x16x32_bf16 v[76:79], v[44:47], v[212:215], v[76:79]\n"
        "ds_read_b64_tr_b16 v[170:171], v233 offset:33792\n"
        "v_mul_f32 v191, s88, v191\n"
        "v_mfma_f32_16x16x32_bf16 v[72:75], v[36:39], v[212:215], v[72:75]\n"
        "global_store_dwordx4 v254, a[232:235], s[70:71] offset:0 sc1\n"
        "s_waitcnt lgkmcnt(6)\n"
        "v_accvgpr_read_b32 v177, a65\n"
        "v_accvgpr_read_b32 v178, a66\n"
        "v_accvgpr_read_b32 v179, a67\n"
        "v_mfma_f32_16x16x32_bf16 v[72:75], v[32:35], v[196:199], v[72:75]\n"
        "v_accvgpr_read_b32 v180, a68\n"
        "v_accvgpr_read_b32 v181, a69\n"
        "v_mfma_f32_16x16x32_bf16 v[104:107], v[32:35], v[200:203], v[104:107]\n"
        "v_accvgpr_read_b32 v182, a70\n"
        "v_accvgpr_read_b32 v183, a71\n"
        "v_mfma_f32_16x16x32_bf16 v[104:107], v[36:39], v[216:219], v[104:107]\n"
        "v_mul_f32 v177, s88, v177\n"
        "v_mul_f32 v178, s88, v178\n"
        "v_mfma_f32_16x16x32_bf16 v[108:111], v[44:47], v[216:219], v[108:111]\n"
        "v_mul_f32 v179, s88, v179\n"
        "v_mul_f32 v180, s88, v180\n"
        "v_mfma_f32_16x16x32_bf16 v[108:111], v[40:43], v[200:203], v[108:111]\n"
        "v_mul_f32 v181, s88, v181\n"
        "v_mul_f32 v182, s88, v182\n"
        "v_mfma_f32_16x16x32_bf16 v[140:143], v[40:43], v[204:207], v[140:143]\n"
        "s_waitcnt vmcnt(13) lgkmcnt(0)\n"
        "s_barrier\n"
        "v_mfma_f32_16x16x32_bf16 v[140:143], v[44:47], v[220:223], v[140:143]\n"
        "global_load_lds_dwordx4 v238, s[68:69] offset:0\n"
        "ds_read_b64_tr_b16 v[44:45], v228 offset:57344\n"
        "v_mfma_f32_16x16x32_bf16 v[136:139], v[36:39], v[220:223], v[136:139]\n"
        "ds_read_b64_tr_b16 v[36:37], v226 offset:57344\n"
        "v_mul_f32 v183, s88, v183\n"
        "v_mfma_f32_16x16x32_bf16 v[136:139], v[32:35], v[204:207], v[136:139]\n"
        "ds_read_b64_tr_b16 v[32:33], v226 offset:49152\n"
        "v_accvgpr_read_b32 v184, a40\n"
        "v_mfma_f32_16x16x32_bf16 v[16:19], v[48:51], v[192:195], v[16:19]\n"
        "ds_read_b64_tr_b16 v[34:35], v227 offset:49152\n"
        "v_mul_f32 v184, s88, v184\n"
        "v_mfma_f32_16x16x32_bf16 v[16:19], v[52:55], v[208:211], v[16:19]\n"
        "ds_read_b64_tr_b16 v[38:39], v227 offset:57344\n"
        "v_cvt_pk_bf16_f32 v184, v184, v185\n"
        "v_mfma_f32_16x16x32_bf16 v[20:23], v[60:63], v[208:211], v[20:23]\n"
        "ds_read_b64_tr_b16 v[46:47], v229 offset:57344\n"
        "v_cvt_pk_bf16_f32 v185, v186, v187\n"
        "v_mfma_f32_16x16x32_bf16 v[20:23], v[56:59], v[192:195], v[20:23]\n"
        "ds_read_b64_tr_b16 v[40:41], v228 offset:49152\n"
        "v_cvt_pk_bf16_f32 v186, v188, v189\n"
        "v_mfma_f32_16x16x32_bf16 v[84:87], v[56:59], v[196:199], v[84:87]\n"
        "ds_read_b64_tr_b16 v[42:43], v229 offset:49152\n"
        "v_cvt_pk_bf16_f32 v187, v190, v191\n"
        "v_mfma_f32_16x16x32_bf16 v[84:87], v[60:63], v[212:215], v[84:87]\n"
        "global_load_lds_dwordx4 v239, s[68:69] offset:1024\n"
        "v_accvgpr_write_b32 a164, v184\n"
        "v_mfma_f32_16x16x32_bf16 v[80:83], v[52:55], v[212:215], v[80:83]\n"
        "v_accvgpr_write_b32 a165, v185\n"
        "v_accvgpr_write_b32 a166, v186\n"
        "v_mfma_f32_16x16x32_bf16 v[80:83], v[48:51], v[196:199], v[80:83]\n"
        "v_accvgpr_write_b32 a167, v187\n"
        "v_accvgpr_read_b32 v185, a73\n"
        "v_mfma_f32_16x16x32_bf16 v[112:115], v[48:51], v[200:203], v[112:115]\n"
        "v_accvgpr_read_b32 v186, a74\n"
        "v_accvgpr_read_b32 v187, a75\n"
        "v_mfma_f32_16x16x32_bf16 v[112:115], v[52:55], v[216:219], v[112:115]\n"
        "global_load_lds_dwordx4 v240, s[68:69] offset:2048\n"
        "v_accvgpr_read_b32 v188, a76\n"
        "v_mfma_f32_16x16x32_bf16 v[116:119], v[60:63], v[216:219], v[116:119]\n"
        "v_accvgpr_read_b32 v189, a77\n"
        "v_accvgpr_read_b32 v190, a78\n"
        "v_mfma_f32_16x16x32_bf16 v[116:119], v[56:59], v[200:203], v[116:119]\n"
        "v_accvgpr_read_b32 v191, a79\n"
        "v_mul_f32 v185, s88, v185\n"
        "v_mfma_f32_16x16x32_bf16 v[148:151], v[56:59], v[204:207], v[148:151]\n"
        "ds_read_b64_tr_b16 v[56:57], v232 offset:49152\n"
        "v_mul_f32 v186, s88, v186\n"
        "v_mfma_f32_16x16x32_bf16 v[148:151], v[60:63], v[220:223], v[148:151]\n"
        "global_load_lds_dwordx4 v241, s[68:69] offset:3072\n"
        "ds_read_b64_tr_b16 v[60:61], v232 offset:57344\n"
        "s_add_u32 m0, s72, 0x0\n"
        "v_mfma_f32_16x16x32_bf16 v[144:147], v[52:55], v[220:223], v[144:147]\n"
        "ds_read_b64_tr_b16 v[52:53], v230 offset:57344\n"
        "s_add_u32 s68, s68, 0x80000\n"
        "s_addc_u32 s69, s69, 0\n"
        "v_mfma_f32_16x16x32_bf16 v[144:147], v[48:51], v[204:207], v[144:147]\n"
        "ds_read_b64_tr_b16 v[48:49], v230 offset:49152\n"
        "v_mul_f32 v187, s88, v187\n"
        "v_mfma_f32_16x16x32_bf16 v[24:27], v[160:163], v[192:195], v[24:27]\n"
        "ds_read_b64_tr_b16 v[50:51], v231 offset:49152\n"
        "v_mul_f32 v188, s88, v188\n"
        "v_mfma_f32_16x16x32_bf16 v[24:27], v[164:167], v[208:211], v[24:27]\n"
        "ds_read_b64_tr_b16 v[54:55], v231 offset:57344\n"
        "global_load_lds_dwordx4 v242, s[64:65] offset:0\n"
        "v_mfma_f32_16x16x32_bf16 v[28:31], v[172:175], v[208:211], v[28:31]\n"
        "LHA6_%=:\n"
        "s_cbranch_execz LHA10_%=\n"
        "ds_read_b64_tr_b16 v[62:63], v233 offset:57344\n"
        "v_mul_f32 v189, s88, v189\n"
        "v_mfma_f32_16x16x32_bf16 v[28:31], v[168:171], v[192:195], v[28:31]\n"
        "ds_read_b64_tr_b16 v[58:59], v233 offset:49152\n"
        "v_mul_f32 v190, s88, v190\n"
        "v_mfma_f32_16x16x32_bf16 v[92:95], v[168:171], v[196:199], v[92:95]\n"
        "ds_read_b128 v[192:195], v248 offset:2048\n"
        "v_mul_f32 v191, s88, v191\n"
        "v_mfma_f32_16x16x32_bf16 v[92:95], v[172:175], v[212:215], v[92:95]\n"
        "ds_read_b128 v[208:211], v249 offset:2048\n"
        "global_load_lds_dwordx4 v243, s[64:65] offset:1024\n"
        "v_mfma_f32_16x16x32_bf16 v[88:91], v[164:167], v[212:215], v[88:91]\n"
        "ds_read_b128 v[212:215], v249 offset:0\n"
        "v_mfma_f32_16x16x32_bf16 v[88:91], v[160:163], v[196:199], v[88:91]\n"
        "ds_read_b128 v[196:199], v248 offset:0\n"
        "v_accvgpr_read_b32 v176, a64\n"
        "v_mfma_f32_16x16x32_bf16 v[120:123], v[160:163], v[200:203], v[120:123]\n"
        "v_mul_f32 v176, s88, v176\n"
        "v_cvt_pk_bf16_f32 v176, v176, v177\n"
        "v_mfma_f32_16x16x32_bf16 v[120:123], v[164:167], v[216:219], v[120:123]\n"
        "v_cvt_pk_bf16_f32 v177, v178, v179\n"
        "v_cvt_pk_bf16_f32 v178, v180, v181\n"
        "v_mfma_f32_16x16x32_bf16 v[124:127], v[172:175], v[216:219], v[124:127]\n"
        "global_load_lds_dwordx4 v244, s[64:65] offset:2048\n"
        "v_cvt_pk_bf16_f32 v179, v182, v183\n"
        "v_mfma_f32_16x16x32_bf16 v[124:127], v[168:171], v[200:203], v[124:127]\n"
        "s_waitcnt lgkmcnt(4)\n"
        "v_accvgpr_write_b32 a168, v176\n"
        "v_accvgpr_write_b32 a169, v177\n"
        "v_mfma_f32_16x16x32_bf16 v[156:159], v[168:171], v[204:207], v[156:159]\n"
        "s_waitcnt lgkmcnt(3)\n"
        "v_accvgpr_write_b32 a170, v178\n"
        "v_accvgpr_write_b32 a171, v179\n"
        "v_mfma_f32_16x16x32_bf16 v[156:159], v[172:175], v[220:223], v[156:159]\n"
        "ds_read_b128 v[200:203], v248 offset:6144\n"
        "v_accvgpr_read_b32 v177, a97\n"
        "v_mfma_f32_16x16x32_bf16 v[152:155], v[164:167], v[220:223], v[152:155]\n"
        "ds_read_b128 v[216:219], v249 offset:6144\n"
        "global_load_lds_dwordx4 v245, s[64:65] offset:3072\n"
        "s_add_u32 m0, s72, 0x1000\n"
        "v_mfma_f32_16x16x32_bf16 v[152:155], v[160:163], v[204:207], v[152:155]\n"
        "ds_read_b128 v[204:207], v248 offset:4096\n"
        "s_add_u32 s64, s64, 0x80\n"
        "s_addc_u32 s65, s65, 0\n"
        "v_mfma_f32_16x16x32_bf16 v[0:3], v[32:35], v[192:195], v[0:3]\n"
        "ds_read_b128 v[220:223], v249 offset:4096\n"
        "s_waitcnt lgkmcnt(4)\n"
        "v_mfma_f32_16x16x32_bf16 v[0:3], v[36:39], v[208:211], v[0:3]\n"
        "ds_read_b64_tr_b16 v[160:161], v226 offset:50176\n"
        "v_accvgpr_read_b32 v178, a98\n"
        "v_mfma_f32_16x16x32_bf16 v[4:7], v[44:47], v[208:211], v[4:7]\n"
        "ds_read_b64_tr_b16 v[162:163], v227 offset:50176\n"
        "global_load_lds_dwordx4 v242, s[66:67] offset:0\n"
        "v_mfma_f32_16x16x32_bf16 v[4:7], v[40:43], v[192:195], v[4:7]\n"
        "ds_read_b64_tr_b16 v[164:165], v226 offset:58368\n"
        "v_accvgpr_read_b32 v179, a99\n"
        "v_mfma_f32_16x16x32_bf16 v[68:71], v[40:43], v[196:199], v[68:71]\n"
        "ds_read_b64_tr_b16 v[166:167], v227 offset:58368\n"
        "v_accvgpr_read_b32 v180, a100\n"
        "v_mfma_f32_16x16x32_bf16 v[68:71], v[44:47], v[212:215], v[68:71]\n"
        "ds_read_b64_tr_b16 v[172:173], v228 offset:58368\n"
        "v_accvgpr_read_b32 v181, a101\n"
        "v_mfma_f32_16x16x32_bf16 v[64:67], v[36:39], v[212:215], v[64:67]\n"
        "ds_read_b64_tr_b16 v[174:175], v229 offset:58368\n"
        "global_load_lds_dwordx4 v243, s[66:67] offset:1024\n"
        "v_mfma_f32_16x16x32_bf16 v[64:67], v[32:35], v[196:199], v[64:67]\n"
        "ds_read_b64_tr_b16 v[168:169], v228 offset:50176\n"
        "s_waitcnt lgkmcnt(8)\n"
        "v_mfma_f32_16x16x32_bf16 v[96:99], v[32:35], v[200:203], v[96:99]\n"
        "ds_read_b64_tr_b16 v[170:171], v229 offset:50176\n"
        "v_accvgpr_read_b32 v182, a102\n"
        "v_mfma_f32_16x16x32_bf16 v[96:99], v[36:39], v[216:219], v[96:99]\n"
        "s_waitcnt lgkmcnt(7)\n"
        "v_accvgpr_read_b32 v183, a103\n"
        "v_mul_f32 v177, s88, v177\n"
        "v_mfma_f32_16x16x32_bf16 v[100:103], v[44:47], v[216:219], v[100:103]\n"
        "global_load_lds_dwordx4 v244, s[66:67] offset:2048\n"
        "v_mfma_f32_16x16x32_bf16 v[100:103], v[40:43], v[200:203], v[100:103]\n"
        "v_mul_f32 v178, s88, v178\n"
        "v_mul_f32 v179, s88, v179\n"
        "v_mfma_f32_16x16x32_bf16 v[132:135], v[40:43], v[204:207], v[132:135]\n"
        "ds_read_b64_tr_b16 v[40:41], v232 offset:50176\n"
        "v_mul_f32 v180, s88, v180\n"
        "v_mfma_f32_16x16x32_bf16 v[132:135], v[44:47], v[220:223], v[132:135]\n"
        "ds_read_b64_tr_b16 v[44:45], v232 offset:58368\n"
        "v_mul_f32 v181, s88, v181\n"
        "v_mfma_f32_16x16x32_bf16 v[128:131], v[36:39], v[220:223], v[128:131]\n"
        "global_load_lds_dwordx4 v245, s[66:67] offset:3072\n"
        "s_add_u32 m0, s73, 0x24000\n"
        "v_mfma_f32_16x16x32_bf16 v[128:131], v[32:35], v[204:207], v[128:131]\n"
        "ds_read_b64_tr_b16 v[32:33], v230 offset:50176\n"
        "v_accvgpr_read_b32 v184, a72\n"
        "v_mfma_f32_16x16x32_bf16 v[8:11], v[48:51], v[192:195], v[8:11]\n"
        "ds_read_b64_tr_b16 v[34:35], v231 offset:50176\n"
        "v_mul_f32 v184, s88, v184\n"
        "v_mfma_f32_16x16x32_bf16 v[8:11], v[52:55], v[208:211], v[8:11]\n"
        "ds_read_b64_tr_b16 v[36:37], v230 offset:58368\n"
        "v_cvt_pk_bf16_f32 v184, v184, v185\n"
        "v_mfma_f32_16x16x32_bf16 v[12:15], v[60:63], v[208:211], v[12:15]\n"
        "ds_read_b64_tr_b16 v[38:39], v231 offset:58368\n"
        "v_cvt_pk_bf16_f32 v185, v186, v187\n"
        "v_mfma_f32_16x16x32_bf16 v[12:15], v[56:59], v[192:195], v[12:15]\n"
        "ds_read_b64_tr_b16 v[46:47], v233 offset:58368\n"
        "v_cvt_pk_bf16_f32 v186, v188, v189\n"
        "v_mfma_f32_16x16x32_bf16 v[76:79], v[56:59], v[196:199], v[76:79]\n"
        "ds_read_b64_tr_b16 v[42:43], v233 offset:50176\n"
        "v_cvt_pk_bf16_f32 v187, v190, v191\n"
        "v_mfma_f32_16x16x32_bf16 v[76:79], v[60:63], v[212:215], v[76:79]\n"
        "s_waitcnt lgkmcnt(6)\n"
        "v_accvgpr_write_b32 a172, v184\n"
        "v_accvgpr_write_b32 a173, v185\n"
        "v_mfma_f32_16x16x32_bf16 v[72:75], v[52:55], v[212:215], v[72:75]\n"
        "s_add_u32 s66, s66, 0x80\n"
        "s_addc_u32 s67, s67, 0\n"
        "v_accvgpr_write_b32 a174, v186\n"
        "v_mfma_f32_16x16x32_bf16 v[72:75], v[48:51], v[196:199], v[72:75]\n"
        "v_accvgpr_write_b32 a175, v187\n"
        "v_mul_f32 v182, s88, v182\n"
        "v_mfma_f32_16x16x32_bf16 v[104:107], v[48:51], v[200:203], v[104:107]\n"
        "v_mul_f32 v183, s88, v183\n"
        "v_accvgpr_read_b32 v185, a105\n"
        "v_mfma_f32_16x16x32_bf16 v[104:107], v[52:55], v[216:219], v[104:107]\n"
        "v_accvgpr_read_b32 v186, a106\n"
        "v_accvgpr_read_b32 v187, a107\n"
        "v_mfma_f32_16x16x32_bf16 v[108:111], v[60:63], v[216:219], v[108:111]\n"
        "v_accvgpr_read_b32 v188, a108\n"
        "v_accvgpr_read_b32 v189, a109\n"
        "v_mfma_f32_16x16x32_bf16 v[108:111], v[56:59], v[200:203], v[108:111]\n"
        "v_accvgpr_read_b32 v190, a110\n"
        "v_accvgpr_read_b32 v191, a111\n"
        "v_mfma_f32_16x16x32_bf16 v[140:143], v[56:59], v[204:207], v[140:143]\n"
        "s_waitcnt vmcnt(13) lgkmcnt(0)\n"
        "s_barrier\n"
        "v_mul_f32 v185, s88, v185\n"
        "v_mfma_f32_16x16x32_bf16 v[140:143], v[60:63], v[220:223], v[140:143]\n"
        "global_load_lds_dwordx4 v238, s[68:69] offset:0\n"
        "ds_read_b64_tr_b16 v[60:61], v228 offset:24576\n"
        "v_mfma_f32_16x16x32_bf16 v[136:139], v[52:55], v[220:223], v[136:139]\n"
        "ds_read_b64_tr_b16 v[52:53], v226 offset:24576\n"
        "v_mul_f32 v186, s88, v186\n"
        "v_mfma_f32_16x16x32_bf16 v[136:139], v[48:51], v[204:207], v[136:139]\n"
        "ds_read_b64_tr_b16 v[48:49], v226 offset:16384\n"
        "v_mul_f32 v187, s88, v187\n"
        "v_mfma_f32_16x16x32_bf16 v[16:19], v[160:163], v[192:195], v[16:19]\n"
        "ds_read_b64_tr_b16 v[50:51], v227 offset:16384\n"
        "v_mul_f32 v188, s88, v188\n"
        "v_mfma_f32_16x16x32_bf16 v[16:19], v[164:167], v[208:211], v[16:19]\n"
        "ds_read_b64_tr_b16 v[54:55], v227 offset:24576\n"
        "global_load_lds_dwordx4 v239, s[68:69] offset:1024\n"
        "v_mfma_f32_16x16x32_bf16 v[20:23], v[172:175], v[208:211], v[20:23]\n"
        "ds_read_b64_tr_b16 v[62:63], v229 offset:24576\n"
        "v_mul_f32 v189, s88, v189\n"
        "v_mfma_f32_16x16x32_bf16 v[20:23], v[168:171], v[192:195], v[20:23]\n"
        "ds_read_b64_tr_b16 v[56:57], v228 offset:16384\n"
        "v_mul_f32 v190, s88, v190\n"
        "v_mfma_f32_16x16x32_bf16 v[84:87], v[168:171], v[196:199], v[84:87]\n"
        "ds_read_b64_tr_b16 v[58:59], v229 offset:16384\n"
        "v_mul_f32 v191, s88, v191\n"
        "v_mfma_f32_16x16x32_bf16 v[84:87], v[172:175], v[212:215], v[84:87]\n"
        "global_load_lds_dwordx4 v240, s[68:69] offset:2048\n"
        "v_mfma_f32_16x16x32_bf16 v[80:83], v[164:167], v[212:215], v[80:83]\n"
        "v_mfma_f32_16x16x32_bf16 v[80:83], v[160:163], v[196:199], v[80:83]\n"
        "v_accvgpr_read_b32 v176, a96\n"
        "v_mul_f32 v176, s88, v176\n"
        "v_mfma_f32_16x16x32_bf16 v[112:115], v[160:163], v[200:203], v[112:115]\n"
        "v_cvt_pk_bf16_f32 v176, v176, v177\n"
        "v_cvt_pk_bf16_f32 v177, v178, v179\n"
        "v_mfma_f32_16x16x32_bf16 v[112:115], v[164:167], v[216:219], v[112:115]\n"
        "v_cvt_pk_bf16_f32 v178, v180, v181\n"
        "v_cvt_pk_bf16_f32 v179, v182, v183\n"
        "v_mfma_f32_16x16x32_bf16 v[116:119], v[172:175], v[216:219], v[116:119]\n"
        "global_load_lds_dwordx4 v241, s[68:69] offset:3072\n"
        "s_add_u32 m0, s72, 0x10000\n"
        "v_accvgpr_write_b32 a0, v176\n"
        "v_mfma_f32_16x16x32_bf16 v[116:119], v[168:171], v[200:203], v[116:119]\n"
        "s_add_u32 s68, s68, 0x80000\n"
        "s_addc_u32 s69, s69, 0\n"
        "v_accvgpr_write_b32 a1, v177\n"
        "v_mfma_f32_16x16x32_bf16 v[148:151], v[168:171], v[204:207], v[148:151]\n"
        "ds_read_b64_tr_b16 v[168:169], v232 offset:16384\n"
        "v_accvgpr_write_b32 a2, v178\n"
        "v_mfma_f32_16x16x32_bf16 v[148:151], v[172:175], v[220:223], v[148:151]\n"
        "ds_read_b64_tr_b16 v[172:173], v232 offset:24576\n"
        "v_accvgpr_write_b32 a3, v179\n"
        "v_mfma_f32_16x16x32_bf16 v[144:147], v[164:167], v[220:223], v[144:147]\n"
        "global_store_dwordx4 v254, a[236:239], s[70:71] offset:1024 sc1\n"
        "ds_read_b64_tr_b16 v[164:165], v230 offset:24576\n"
        "ds_read_b64_tr_b16 v[166:167], v231 offset:24576\n"
        "v_accvgpr_read_b32 v177, a129\n"
        "v_mfma_f32_16x16x32_bf16 v[144:147], v[160:163], v[204:207], v[144:147]\n"
        "ds_read_b64_tr_b16 v[160:161], v230 offset:16384\n"
        "v_accvgpr_read_b32 v178, a130\n"
        "v_mfma_f32_16x16x32_bf16 v[24:27], v[32:35], v[192:195], v[24:27]\n"
        "ds_read_b64_tr_b16 v[162:163], v231 offset:16384\n"
        "v_accvgpr_read_b32 v179, a131\n"
        "v_mfma_f32_16x16x32_bf16 v[24:27], v[36:39], v[208:211], v[24:27]\n"
        "ds_read_b64_tr_b16 v[174:175], v233 offset:24576\n"
        "global_load_lds_dwordx4 v242, s[64:65] offset:0\n"
        "v_mfma_f32_16x16x32_bf16 v[28:31], v[44:47], v[208:211], v[28:31]\n"
        "ds_read_b64_tr_b16 v[170:171], v233 offset:16384\n"
        "v_accvgpr_read_b32 v180, a132\n"
        "v_mfma_f32_16x16x32_bf16 v[28:31], v[40:43], v[192:195], v[28:31]\n"
        "ds_read_b128 v[192:195], v246 offset:34816\n"
        "v_accvgpr_read_b32 v181, a133\n"
        "v_mfma_f32_16x16x32_bf16 v[92:95], v[40:43], v[196:199], v[92:95]\n"
        "ds_read_b128 v[208:211], v247 offset:34816\n"
        "v_accvgpr_read_b32 v182, a134\n"
        "v_mfma_f32_16x16x32_bf16 v[92:95], v[44:47], v[212:215], v[92:95]\n"
        "global_load_lds_dwordx4 v243, s[64:65] offset:1024\n"
        "v_mfma_f32_16x16x32_bf16 v[88:91], v[36:39], v[212:215], v[88:91]\n"
        "ds_read_b128 v[212:215], v247 offset:32768\n"
        "v_accvgpr_read_b32 v183, a135\n"
        "v_mfma_f32_16x16x32_bf16 v[88:91], v[32:35], v[196:199], v[88:91]\n"
        "ds_read_b128 v[196:199], v246 offset:32768\n"
        "v_mul_f32 v177, s88, v177\n"
        "v_mfma_f32_16x16x32_bf16 v[120:123], v[32:35], v[200:203], v[120:123]\n"
        "s_add_u32 s70, s38, s97\n"
        "s_addc_u32 s71, s39, 0\n"
        "v_mul_f32 v178, s88, v178\n"
        "v_mfma_f32_16x16x32_bf16 v[120:123], v[36:39], v[216:219], v[120:123]\n"
        "global_load_lds_dwordx4 v244, s[64:65] offset:2048\n"
        "v_mfma_f32_16x16x32_bf16 v[124:127], v[44:47], v[216:219], v[124:127]\n"
        "ds_read_b128 v[216:219], v247 offset:38912\n"
        "v_mul_f32 v179, s88, v179\n"
        "v_mfma_f32_16x16x32_bf16 v[124:127], v[40:43], v[200:203], v[124:127]\n"
        "ds_read_b128 v[200:203], v246 offset:38912\n"
        "v_mul_f32 v180, s88, v180\n"
        "v_mfma_f32_16x16x32_bf16 v[156:159], v[40:43], v[204:207], v[156:159]\n"
        "ds_read_b64_tr_b16 v[40:41], v228 offset:17408\n"
        "v_mul_f32 v181, s88, v181\n"
        "v_mfma_f32_16x16x32_bf16 v[156:159], v[44:47], v[220:223], v[156:159]\n"
        "global_load_lds_dwordx4 v245, s[64:65] offset:3072\n"
        "s_add_u32 m0, s72, 0x11000\n"
        "v_mfma_f32_16x16x32_bf16 v[152:155], v[36:39], v[220:223], v[152:155]\n"
        "ds_read_b128 v[220:223], v247 offset:36864\n"
        "s_add_u32 s64, s64, 0x80\n"
        "s_addc_u32 s65, s65, 0\n"
        "v_mfma_f32_16x16x32_bf16 v[152:155], v[32:35], v[204:207], v[152:155]\n"
        "ds_read_b128 v[204:207], v246 offset:36864\n"
        "s_waitcnt lgkmcnt(5)\n"
        "v_mfma_f32_16x16x32_bf16 v[0:3], v[48:51], v[192:195], v[0:3]\n"
        "ds_read_b64_tr_b16 v[32:33], v226 offset:17408\n"
        "v_accvgpr_read_b32 v184, a104\n"
        "v_mfma_f32_16x16x32_bf16 v[0:3], v[52:55], v[208:211], v[0:3]\n"
        "ds_read_b64_tr_b16 v[34:35], v227 offset:17408\n"
        "v_mul_f32 v184, s88, v184\n"
        "v_mfma_f32_16x16x32_bf16 v[4:7], v[60:63], v[208:211], v[4:7]\n"
        "global_load_lds_dwordx4 v242, s[66:67] offset:0\n"
        "v_cvt_pk_bf16_f32 v184, v184, v185\n"
        "v_mfma_f32_16x16x32_bf16 v[4:7], v[56:59], v[192:195], v[4:7]\n"
        "s_waitcnt lgkmcnt(5)\n"
        "v_cvt_pk_bf16_f32 v185, v186, v187\n"
        "v_cvt_pk_bf16_f32 v186, v188, v189\n"
        "v_mfma_f32_16x16x32_bf16 v[68:71], v[56:59], v[196:199], v[68:71]\n"
        "ds_read_b64_tr_b16 v[36:37], v226 offset:25600\n"
        "v_cvt_pk_bf16_f32 v187, v190, v191\n"
        "v_mfma_f32_16x16x32_bf16 v[68:71], v[60:63], v[212:215], v[68:71]\n"
        "s_waitcnt lgkmcnt(5)\n"
        "v_accvgpr_write_b32 a4, v184\n"
        "v_accvgpr_write_b32 a5, v185\n"
        "v_mfma_f32_16x16x32_bf16 v[64:67], v[52:55], v[212:215], v[64:67]\n"
        "s_waitcnt lgkmcnt(4)\n"
        "v_accvgpr_write_b32 a6, v186\n"
        "v_accvgpr_write_b32 a7, v187\n"
        "v_mfma_f32_16x16x32_bf16 v[64:67], v[48:51], v[196:199], v[64:67]\n"
        "ds_read_b64_tr_b16 v[38:39], v227 offset:25600\n"
        "global_load_lds_dwordx4 v243, s[66:67] offset:1024\n"
        "v_mfma_f32_16x16x32_bf16 v[96:99], v[48:51], v[200:203], v[96:99]\n"
        "ds_read_b64_tr_b16 v[44:45], v228 offset:25600\n"
        "v_mul_f32 v182, s88, v182\n"
        "v_mfma_f32_16x16x32_bf16 v[96:99], v[52:55], v[216:219], v[96:99]\n"
        "ds_read_b64_tr_b16 v[46:47], v229 offset:25600\n"
        "v_mul_f32 v183, s88, v183\n"
        "v_mfma_f32_16x16x32_bf16 v[100:103], v[60:63], v[216:219], v[100:103]\n"
        "ds_read_b64_tr_b16 v[42:43], v229 offset:17408\n"
        "v_accvgpr_read_b32 v185, a137\n"
        "v_mfma_f32_16x16x32_bf16 v[100:103], v[56:59], v[200:203], v[100:103]\n"
        "s_waitcnt lgkmcnt(5)\n"
        "global_load_lds_dwordx4 v244, s[66:67] offset:2048\n"
        "v_mfma_f32_16x16x32_bf16 v[132:135], v[56:59], v[204:207], v[132:135]\n"
        "ds_read_b64_tr_b16 v[56:57], v232 offset:17408\n"
        "v_accvgpr_read_b32 v186, a138\n"
        "v_mfma_f32_16x16x32_bf16 v[132:135], v[60:63], v[220:223], v[132:135]\n"
        "ds_read_b64_tr_b16 v[60:61], v232 offset:25600\n"
        "v_accvgpr_read_b32 v187, a139\n"
        "v_mfma_f32_16x16x32_bf16 v[128:131], v[52:55], v[220:223], v[128:131]\n"
        "ds_read_b64_tr_b16 v[52:53], v230 offset:25600\n"
        "v_accvgpr_read_b32 v188, a140\n"
        "v_mfma_f32_16x16x32_bf16 v[128:131], v[48:51], v[204:207], v[128:131]\n"
        "global_load_lds_dwordx4 v245, s[66:67] offset:3072\n"
        "s_add_u32 m0, s73, 0x1c000\n"
        "v_mfma_f32_16x16x32_bf16 v[8:11], v[160:163], v[192:195], v[8:11]\n"
        "ds_read_b64_tr_b16 v[48:49], v230 offset:17408\n"
        "s_add_u32 s66, s66, 0x80\n"
        "s_addc_u32 s67, s67, 0\n"
        "v_mfma_f32_16x16x32_bf16 v[8:11], v[164:167], v[208:211], v[8:11]\n"
        "ds_read_b64_tr_b16 v[50:51], v231 offset:17408\n"
        "v_accvgpr_read_b32 v189, a141\n"
        "v_mfma_f32_16x16x32_bf16 v[12:15], v[172:175], v[208:211], v[12:15]\n"
        "ds_read_b64_tr_b16 v[54:55], v231 offset:25600\n"
        "v_accvgpr_read_b32 v190, a142\n"
        "v_mfma_f32_16x16x32_bf16 v[12:15], v[168:171], v[192:195], v[12:15]\n"
        "ds_read_b64_tr_b16 v[62:63], v233 offset:25600\n"
        "v_accvgpr_read_b32 v191, a143\n"
        "v_mfma_f32_16x16x32_bf16 v[76:79], v[168:171], v[196:199], v[76:79]\n"
        "ds_read_b64_tr_b16 v[58:59], v233 offset:17408\n"
        "v_mul_f32 v185, s88, v185\n"
        "v_mfma_f32_16x16x32_bf16 v[76:79], v[172:175], v[212:215], v[76:79]\n"
        "v_mul_f32 v186, s88, v186\n"
        "v_mul_f32 v187, s88, v187\n"
        "v_mfma_f32_16x16x32_bf16 v[72:75], v[164:167], v[212:215], v[72:75]\n"
        "v_mul_f32 v188, s88, v188\n"
        "v_mul_f32 v189, s88, v189\n"
        "v_mfma_f32_16x16x32_bf16 v[72:75], v[160:163], v[196:199], v[72:75]\n"
        "s_waitcnt lgkmcnt(5)\n"
        "v_accvgpr_read_b32 v176, a128\n"
        "v_mul_f32 v176, s88, v176\n"
        "v_mfma_f32_16x16x32_bf16 v[104:107], v[160:163], v[200:203], v[104:107]\n"
        "v_cvt_pk_bf16_f32 v176, v176, v177\n"
        "v_cvt_pk_bf16_f32 v177, v178, v179\n"
        "v_mfma_f32_16x16x32_bf16 v[104:107], v[164:167], v[216:219], v[104:107]\n"
        "v_cvt_pk_bf16_f32 v178, v180, v181\n"
        "v_cvt_pk_bf16_f32 v179, v182, v183\n"
        "v_mfma_f32_16x16x32_bf16 v[108:111], v[172:175], v[216:219], v[108:111]\n"
        "v_accvgpr_write_b32 a8, v176\n"
        "v_accvgpr_write_b32 a9, v177\n"
        "v_mfma_f32_16x16x32_bf16 v[108:111], v[168:171], v[200:203], v[108:111]\n"
        "v_accvgpr_write_b32 a10, v178\n"
        "v_accvgpr_write_b32 a11, v179\n"
        "v_mfma_f32_16x16x32_bf16 v[140:143], v[168:171], v[204:207], v[140:143]\n"
        "s_waitcnt vmcnt(13) lgkmcnt(0)\n"
        "s_barrier\n"
        "v_mul_f32 v190, s88, v190\n"
        "v_mfma_f32_16x16x32_bf16 v[140:143], v[172:175], v[220:223], v[140:143]\n"
        "global_load_lds_dwordx4 v238, s[68:69] offset:0\n"
        "ds_read_b64_tr_b16 v[172:173], v228 offset:40960\n"
        "s_add_u32 s97, s52, 0x0\n"
        "v_mfma_f32_16x16x32_bf16 v[136:139], v[164:167], v[220:223], v[136:139]\n"
        "ds_read_b64_tr_b16 v[164:165], v226 offset:40960\n"
        "v_mul_f32 v191, s88, v191\n"
        "v_mfma_f32_16x16x32_bf16 v[136:139], v[160:163], v[204:207], v[136:139]\n"
        "ds_read_b64_tr_b16 v[160:161], v226 offset:32768\n"
        "v_accvgpr_read_b32 v177, a209\n"
        "v_mfma_f32_16x16x32_bf16 v[16:19], v[32:35], v[192:195], v[16:19]\n"
        "ds_read_b64_tr_b16 v[162:163], v227 offset:32768\n"
        "v_accvgpr_read_b32 v178, a210\n"
        "v_mfma_f32_16x16x32_bf16 v[16:19], v[36:39], v[208:211], v[16:19]\n"
        "ds_read_b64_tr_b16 v[166:167], v227 offset:40960\n"
        "global_load_lds_dwordx4 v239, s[68:69] offset:1024\n"
        "v_mfma_f32_16x16x32_bf16 v[20:23], v[44:47], v[208:211], v[20:23]\n"
        "ds_read_b64_tr_b16 v[174:175], v229 offset:40960\n"
        "v_accvgpr_read_b32 v179, a211\n"
        "v_mfma_f32_16x16x32_bf16 v[20:23], v[40:43], v[192:195], v[20:23]\n"
        "ds_read_b64_tr_b16 v[168:169], v228 offset:32768\n"
        "v_accvgpr_read_b32 v180, a240\n"
        "v_mfma_f32_16x16x32_bf16 v[84:87], v[40:43], v[196:199], v[84:87]\n"
        "ds_read_b64_tr_b16 v[170:171], v229 offset:32768\n"
        "v_accvgpr_read_b32 v181, a241\n"
        "v_mfma_f32_16x16x32_bf16 v[84:87], v[44:47], v[212:215], v[84:87]\n"
        "global_load_lds_dwordx4 v240, s[68:69] offset:2048\n"
        "v_mfma_f32_16x16x32_bf16 v[80:83], v[36:39], v[212:215], v[80:83]\n"
        "v_accvgpr_read_b32 v182, a242\n"
        "v_accvgpr_read_b32 v183, a243\n"
        "v_mfma_f32_16x16x32_bf16 v[80:83], v[32:35], v[196:199], v[80:83]\n"
        "v_mfma_f32_16x16x32_bf16 v[112:115], v[32:35], v[200:203], v[112:115]\n"
        "v_mfma_f32_16x16x32_bf16 v[112:115], v[36:39], v[216:219], v[112:115]\n"
        "global_load_lds_dwordx4 v241, s[68:69] offset:3072\n"
        "s_add_u32 m0, s72, 0x8000\n"
        "v_mfma_f32_16x16x32_bf16 v[116:119], v[44:47], v[216:219], v[116:119]\n"
        "s_add_u32 s68, s68, 0x80000\n"
        "s_addc_u32 s69, s69, 0\n"
        "v_mfma_f32_16x16x32_bf16 v[116:119], v[40:43], v[200:203], v[116:119]\n"
        "v_mfma_f32_16x16x32_bf16 v[148:151], v[40:43], v[204:207], v[148:151]\n"
        "ds_read_b64_tr_b16 v[40:41], v232 offset:32768\n"
        "v_mfma_f32_16x16x32_bf16 v[148:151], v[44:47], v[220:223], v[148:151]\n"
        "global_load_lds_dwordx4 v242, s[64:65] offset:0\n"
        "v_mfma_f32_16x16x32_bf16 v[144:147], v[36:39], v[220:223], v[144:147]\n"
        "ds_read_b64_tr_b16 v[36:37], v230 offset:40960\n"
        "v_mfma_f32_16x16x32_bf16 v[144:147], v[32:35], v[204:207], v[144:147]\n"
        "ds_read_b64_tr_b16 v[32:33], v230 offset:32768\n"
        "v_accvgpr_read_b32 v184, a136\n"
        "v_mfma_f32_16x16x32_bf16 v[24:27], v[48:51], v[192:195], v[24:27]\n"
        "ds_read_b64_tr_b16 v[34:35], v231 offset:32768\n"
        "v_mul_f32 v184, s88, v184\n"
        "v_mfma_f32_16x16x32_bf16 v[24:27], v[52:55], v[208:211], v[24:27]\n"
        "ds_read_b64_tr_b16 v[38:39], v231 offset:40960\n"
        "v_cvt_pk_bf16_f32 v184, v184, v185\n"
        "v_mfma_f32_16x16x32_bf16 v[28:31], v[60:63], v[208:211], v[28:31]\n"
        "ds_read_b64_tr_b16 v[44:45], v232 offset:40960\n"
        "v_cvt_pk_bf16_f32 v185, v186, v187\n"
        "v_mfma_f32_16x16x32_bf16 v[28:31], v[56:59], v[192:195], v[28:31]\n"
        "ds_read_b64_tr_b16 v[46:47], v233 offset:40960\n"
        "v_cvt_pk_bf16_f32 v186, v188, v189\n"
        "v_mfma_f32_16x16x32_bf16 v[92:95], v[56:59], v[196:199], v[92:95]\n"
        "ds_read_b64_tr_b16 v[42:43], v233 offset:32768\n"
        "v_cvt_pk_bf16_f32 v187, v190, v191\n"
        "v_mfma_f32_16x16x32_bf16 v[92:95], v[60:63], v[212:215], v[92:95]\n"
        "ds_read_b128 v[192:195], v246 offset:2048\n"
        "v_accvgpr_write_b32 a12, v184\n"
        "v_mfma_f32_16x16x32_bf16 v[88:91], v[52:55], v[212:215], v[88:91]\n"
        "ds_read_b128 v[208:211], v247 offset:2048\n"
        "v_accvgpr_write_b32 a13, v185\n"
        "v_mfma_f32_16x16x32_bf16 v[88:91], v[48:51], v[196:199], v[88:91]\n"
        "global_load_lds_dwordx4 v243, s[64:65] offset:1024\n"
        "v_accvgpr_write_b32 a14, v186\n"
        "v_mfma_f32_16x16x32_bf16 v[120:123], v[48:51], v[200:203], v[120:123]\n"
        "ds_read_b128 v[196:199], v246 offset:0\n"
        "v_accvgpr_write_b32 a15, v187\n"
        "v_mfma_f32_16x16x32_bf16 v[120:123], v[52:55], v[216:219], v[120:123]\n"
        "ds_read_b128 v[212:215], v247 offset:0\n"
        "v_accvgpr_read_b32 v185, a213\n"
        "v_mfma_f32_16x16x32_bf16 v[124:127], v[60:63], v[216:219], v[124:127]\n"
        "ds_read_b128 v[216:219], v247 offset:6144\n"
        "v_accvgpr_read_b32 v186, a214\n"
        "v_mfma_f32_16x16x32_bf16 v[124:127], v[56:59], v[200:203], v[124:127]\n"
        "global_load_lds_dwordx4 v244, s[64:65] offset:2048\n"
        "ds_read_b128 v[200:203], v246 offset:6144\n"
        "v_mfma_f32_16x16x32_bf16 v[156:159], v[56:59], v[204:207], v[156:159]\n"
        "ds_read_b64_tr_b16 v[56:57], v228 offset:33792\n"
        "v_accvgpr_read_b32 v187, a215\n"
        "v_mfma_f32_16x16x32_bf16 v[156:159], v[60:63], v[220:223], v[156:159]\n"
        "ds_read_b64_tr_b16 v[60:61], v228 offset:41984\n"
        "v_accvgpr_read_b32 v188, a244\n"
        "v_mfma_f32_16x16x32_bf16 v[152:155], v[52:55], v[220:223], v[152:155]\n"
        "ds_read_b128 v[220:223], v247 offset:4096\n"
        "v_accvgpr_read_b32 v189, a245\n"
        "v_mfma_f32_16x16x32_bf16 v[152:155], v[48:51], v[204:207], v[152:155]\n"
        "global_load_lds_dwordx4 v245, s[64:65] offset:3072\n"
        "s_add_u32 m0, s72, 0x9000\n"
        "s_waitcnt lgkmcnt(7)\n"
        "v_mfma_f32_16x16x32_bf16 v[0:3], v[160:163], v[192:195], v[0:3]\n"
        "ds_read_b128 v[204:207], v246 offset:4096\n"
        "s_add_u32 s64, s64, 0x80\n"
        "s_addc_u32 s65, s65, 0\n"
        "v_mfma_f32_16x16x32_bf16 v[0:3], v[164:167], v[208:211], v[0:3]\n"
        "ds_read_b64_tr_b16 v[48:49], v226 offset:33792\n"
        "v_accvgpr_read_b32 v190, a246\n"
        "v_mfma_f32_16x16x32_bf16 v[4:7], v[172:175], v[208:211], v[4:7]\n"
        "ds_read_b64_tr_b16 v[50:51], v227 offset:33792\n"
        "v_accvgpr_read_b32 v191, a247\n"
        "v_mfma_f32_16x16x32_bf16 v[4:7], v[168:171], v[192:195], v[4:7]\n"
        "ds_read_b64_tr_b16 v[52:53], v226 offset:41984\n"
        "s_waitcnt lgkmcnt(7)\n"
        "v_mfma_f32_16x16x32_bf16 v[68:71], v[168:171], v[196:199], v[68:71]\n"
        "global_store_dwordx4 v254, a[192:195], s[70:71] offset:0 sc1\n"
        "ds_read_b64_tr_b16 v[54:55], v227 offset:41984\n"
        "ds_read_b64_tr_b16 v[62:63], v229 offset:41984\n"
        "s_waitcnt lgkmcnt(7)\n"
        "v_mfma_f32_16x16x32_bf16 v[68:71], v[172:175], v[212:215], v[68:71]\n"
        "ds_read_b64_tr_b16 v[58:59], v229 offset:33792\n"
        "s_waitcnt lgkmcnt(7)\n"
        "v_mfma_f32_16x16x32_bf16 v[64:67], v[164:167], v[212:215], v[64:67]\n"
        "v_mfma_f32_16x16x32_bf16 v[64:67], v[160:163], v[196:199], v[64:67]\n"
        "global_load_lds_dwordx4 v242, s[66:67] offset:0\n"
        "v_mfma_f32_16x16x32_bf16 v[96:99], v[160:163], v[200:203], v[96:99]\n"
        "s_waitcnt lgkmcnt(6)\n"
        "v_accvgpr_read_b32 v176, a208\n"
        "v_cvt_pk_bf16_f32 v176, v176, v177\n"
        "v_mfma_f32_16x16x32_bf16 v[96:99], v[164:167], v[216:219], v[96:99]\n"
        "v_cvt_pk_bf16_f32 v177, v178, v179\n"
        "v_cvt_pk_bf16_f32 v178, v180, v181\n"
        "v_mfma_f32_16x16x32_bf16 v[100:103], v[172:175], v[216:219], v[100:103]\n"
        "v_cvt_pk_bf16_f32 v179, v182, v183\n"
        "v_accvgpr_write_b32 a208, v176\n"
        "v_mfma_f32_16x16x32_bf16 v[100:103], v[168:171], v[200:203], v[100:103]\n"
        "global_load_lds_dwordx4 v243, s[66:67] offset:1024\n"
        "v_mfma_f32_16x16x32_bf16 v[132:135], v[168:171], v[204:207], v[132:135]\n"
        "ds_read_b64_tr_b16 v[168:169], v232 offset:33792\n"
        "v_accvgpr_write_b32 a209, v177\n"
        "v_mfma_f32_16x16x32_bf16 v[132:135], v[172:175], v[220:223], v[132:135]\n"
        "ds_read_b64_tr_b16 v[172:173], v232 offset:41984\n"
        "v_accvgpr_write_b32 a210, v178\n"
        "v_mfma_f32_16x16x32_bf16 v[128:131], v[164:167], v[220:223], v[128:131]\n"
        "ds_read_b64_tr_b16 v[164:165], v230 offset:41984\n"
        "v_accvgpr_write_b32 a211, v179\n"
        "v_mfma_f32_16x16x32_bf16 v[128:131], v[160:163], v[204:207], v[128:131]\n"
        "global_load_lds_dwordx4 v244, s[66:67] offset:2048\n"
        "v_mfma_f32_16x16x32_bf16 v[8:11], v[32:35], v[192:195], v[8:11]\n"
        "ds_read_b64_tr_b16 v[160:161], v230 offset:33792\n"
        "v_accvgpr_read_b32 v177, a217\n"
        "v_mfma_f32_16x16x32_bf16 v[8:11], v[36:39], v[208:211], v[8:11]\n"
        "ds_read_b64_tr_b16 v[162:163], v231 offset:33792\n"
        "v_accvgpr_read_b32 v178, a218\n"
        "v_mfma_f32_16x16x32_bf16 v[12:15], v[44:47], v[208:211], v[12:15]\n"
        "ds_read_b64_tr_b16 v[166:167], v231 offset:41984\n"
        "v_accvgpr_read_b32 v179, a219\n"
        "v_mfma_f32_16x16x32_bf16 v[12:15], v[40:43], v[192:195], v[12:15]\n"
        "global_load_lds_dwordx4 v245, s[66:67] offset:3072\n"
        "s_add_u32 m0, s73, 0x20000\n"
        "v_mfma_f32_16x16x32_bf16 v[76:79], v[40:43], v[196:199], v[76:79]\n"
        "ds_read_b64_tr_b16 v[174:175], v233 offset:41984\n"
        "s_add_u32 s66, s66, 0x80\n"
        "s_addc_u32 s67, s67, 0\n"
        "v_mfma_f32_16x16x32_bf16 v[76:79], v[44:47], v[212:215], v[76:79]\n"
        "ds_read_b64_tr_b16 v[170:171], v233 offset:33792\n"
        "v_accvgpr_read_b32 v180, a248\n"
        "v_mfma_f32_16x16x32_bf16 v[72:75], v[36:39], v[212:215], v[72:75]\n"
        "s_waitcnt lgkmcnt(5)\n"
        "v_accvgpr_read_b32 v181, a249\n"
        "v_accvgpr_read_b32 v182, a250\n"
        "v_mfma_f32_16x16x32_bf16 v[72:75], v[32:35], v[196:199], v[72:75]\n"
        "v_accvgpr_read_b32 v183, a251\n"
        "v_mfma_f32_16x16x32_bf16 v[104:107], v[32:35], v[200:203], v[104:107]\n"
        "v_mfma_f32_16x16x32_bf16 v[104:107], v[36:39], v[216:219], v[104:107]\n"
        "v_mfma_f32_16x16x32_bf16 v[108:111], v[44:47], v[216:219], v[108:111]\n"
        "v_mfma_f32_16x16x32_bf16 v[108:111], v[40:43], v[200:203], v[108:111]\n"
        "v_mfma_f32_16x16x32_bf16 v[140:143], v[40:43], v[204:207], v[140:143]\n"
        "v_mfma_f32_16x16x32_bf16 v[140:143], v[44:47], v[220:223], v[140:143]\n"
        "s_waitcnt vmcnt(13) lgkmcnt(0)\n"
        "s_barrier\n"
        "v_mfma_f32_16x16x32_bf16 v[136:139], v[36:39], v[220:223], v[136:139]\n"
        "global_load_lds_dwordx4 v238, s[68:69] offset:0\n"
        "ds_read_b64_tr_b16 v[36:37], v226 offset:57344\n"
        "v_mfma_f32_16x16x32_bf16 v[136:139], v[32:35], v[204:207], v[136:139]\n"
        "ds_read_b64_tr_b16 v[32:33], v226 offset:49152\n"
        "v_accvgpr_read_b32 v184, a212\n"
        "v_mfma_f32_16x16x32_bf16 v[16:19], v[48:51], v[192:195], v[16:19]\n"
        "ds_read_b64_tr_b16 v[34:35], v227 offset:49152\n"
        "v_cvt_pk_bf16_f32 v184, v184, v185\n"
        "v_mfma_f32_16x16x32_bf16 v[16:19], v[52:55], v[208:211], v[16:19]\n"
        "ds_read_b64_tr_b16 v[38:39], v227 offset:57344\n"
        "v_cvt_pk_bf16_f32 v185, v186, v187\n"
        "v_mfma_f32_16x16x32_bf16 v[20:23], v[60:63], v[208:211], v[20:23]\n"
        "ds_read_b64_tr_b16 v[44:45], v228 offset:57344\n"
        "v_cvt_pk_bf16_f32 v186, v188, v189\n"
        "v_mfma_f32_16x16x32_bf16 v[20:23], v[56:59], v[192:195], v[20:23]\n"
        "ds_read_b64_tr_b16 v[46:47], v229 offset:57344\n"
        "v_cvt_pk_bf16_f32 v187, v190, v191\n"
        "v_mfma_f32_16x16x32_bf16 v[84:87], v[56:59], v[196:199], v[84:87]\n"
        "ds_read_b64_tr_b16 v[40:41], v228 offset:49152\n"
        "v_accvgpr_write_b32 a240, v184\n"
        "v_mfma_f32_16x16x32_bf16 v[84:87], v[60:63], v[212:215], v[84:87]\n"
        "ds_read_b64_tr_b16 v[42:43], v229 offset:49152\n"
        "v_accvgpr_write_b32 a241, v185\n"
        "v_mfma_f32_16x16x32_bf16 v[80:83], v[52:55], v[212:215], v[80:83]\n"
        "global_load_lds_dwordx4 v239, s[68:69] offset:1024\n"
        "v_accvgpr_write_b32 a242, v186\n"
        "v_mfma_f32_16x16x32_bf16 v[80:83], v[48:51], v[196:199], v[80:83]\n"
        "v_accvgpr_write_b32 a243, v187\n"
        "v_accvgpr_read_b32 v185, a221\n"
        "v_mfma_f32_16x16x32_bf16 v[112:115], v[48:51], v[200:203], v[112:115]\n"
        "v_accvgpr_read_b32 v186, a222\n"
        "v_accvgpr_read_b32 v187, a223\n"
        "v_mfma_f32_16x16x32_bf16 v[112:115], v[52:55], v[216:219], v[112:115]\n"
        "v_accvgpr_read_b32 v188, a252\n"
        "v_accvgpr_read_b32 v189, a253\n"
        "v_mfma_f32_16x16x32_bf16 v[116:119], v[60:63], v[216:219], v[116:119]\n"
        "global_load_lds_dwordx4 v240, s[68:69] offset:2048\n"
        "v_accvgpr_read_b32 v190, a254\n"
        "v_mfma_f32_16x16x32_bf16 v[116:119], v[56:59], v[200:203], v[116:119]\n"
        "LHA7_%=:\n"
        "s_cbranch_execz LHA11_%=\n"
        "v_accvgpr_read_b32 v191, a255\n"
        "v_mfma_f32_16x16x32_bf16 v[148:151], v[56:59], v[204:207], v[148:151]\n"
        "ds_read_b64_tr_b16 v[56:57], v232 offset:49152\n"
        "v_mfma_f32_16x16x32_bf16 v[148:151], v[60:63], v[220:223], v[148:151]\n"
        "ds_read_b64_tr_b16 v[60:61], v232 offset:57344\n"
        "v_mfma_f32_16x16x32_bf16 v[144:147], v[52:55], v[220:223], v[144:147]\n"
        "global_load_lds_dwordx4 v241, s[68:69] offset:3072\n"
        "ds_read_b64_tr_b16 v[52:53], v230 offset:57344\n"
        "s_add_u32 m0, s72, 0x0\n"
        "v_mfma_f32_16x16x32_bf16 v[144:147], v[48:51], v[204:207], v[144:147]\n"
        "ds_read_b64_tr_b16 v[48:49], v230 offset:49152\n"
        "s_add_u32 s68, s68, 0x80000\n"
        "s_addc_u32 s69, s69, 0\n"
        "v_mfma_f32_16x16x32_bf16 v[24:27], v[160:163], v[192:195], v[24:27]\n"
        "ds_read_b64_tr_b16 v[50:51], v231 offset:49152\n"
        "v_mfma_f32_16x16x32_bf16 v[24:27], v[164:167], v[208:211], v[24:27]\n"
        "ds_read_b64_tr_b16 v[54:55], v231 offset:57344\n"
        "v_mfma_f32_16x16x32_bf16 v[28:31], v[172:175], v[208:211], v[28:31]\n"
        "ds_read_b64_tr_b16 v[62:63], v233 offset:57344\n"
        "global_load_lds_dwordx4 v242, s[64:65] offset:0\n"
        "v_mfma_f32_16x16x32_bf16 v[28:31], v[168:171], v[192:195], v[28:31]\n"
        "ds_read_b64_tr_b16 v[58:59], v233 offset:49152\n"
        "v_mfma_f32_16x16x32_bf16 v[92:95], v[168:171], v[196:199], v[92:95]\n"
        "ds_read_b128 v[192:195], v248 offset:2048\n"
        "v_mfma_f32_16x16x32_bf16 v[92:95], v[172:175], v[212:215], v[92:95]\n"
        "ds_read_b128 v[208:211], v249 offset:2048\n"
        "v_mfma_f32_16x16x32_bf16 v[88:91], v[164:167], v[212:215], v[88:91]\n"
        "global_load_lds_dwordx4 v243, s[64:65] offset:1024\n"
        "v_mfma_f32_16x16x32_bf16 v[88:91], v[160:163], v[196:199], v[88:91]\n"
        "ds_read_b128 v[196:199], v248 offset:0\n"
        "v_accvgpr_read_b32 v176, a216\n"
        "v_mfma_f32_16x16x32_bf16 v[120:123], v[160:163], v[200:203], v[120:123]\n"
        "ds_read_b128 v[212:215], v249 offset:0\n"
        "v_cvt_pk_bf16_f32 v176, v176, v177\n"
        "v_mfma_f32_16x16x32_bf16 v[120:123], v[164:167], v[216:219], v[120:123]\n"
        "v_cvt_pk_bf16_f32 v177, v178, v179\n"
        "v_cvt_pk_bf16_f32 v178, v180, v181\n"
        "v_mfma_f32_16x16x32_bf16 v[124:127], v[172:175], v[216:219], v[124:127]\n"
        "ds_read_b128 v[216:219], v249 offset:6144\n"
        "v_cvt_pk_bf16_f32 v179, v182, v183\n"
        "v_mfma_f32_16x16x32_bf16 v[124:127], v[168:171], v[200:203], v[124:127]\n"
        "s_waitcnt lgkmcnt(5)\n"
        "v_accvgpr_write_b32 a212, v176\n"
        "v_accvgpr_write_b32 a213, v177\n"
        "v_mfma_f32_16x16x32_bf16 v[156:159], v[168:171], v[204:207], v[156:159]\n"
        "s_waitcnt lgkmcnt(4)\n"
        "v_accvgpr_write_b32 a214, v178\n"
        "v_accvgpr_write_b32 a215, v179\n"
        "v_mfma_f32_16x16x32_bf16 v[156:159], v[172:175], v[220:223], v[156:159]\n"
        "global_load_lds_dwordx4 v244, s[64:65] offset:2048\n"
        "ds_read_b128 v[200:203], v248 offset:6144\n"
        "v_mfma_f32_16x16x32_bf16 v[152:155], v[164:167], v[220:223], v[152:155]\n"
        "ds_read_b128 v[220:223], v249 offset:4096\n"
        "v_accvgpr_read_b32 v177, a145\n"
        "v_mfma_f32_16x16x32_bf16 v[152:155], v[160:163], v[204:207], v[152:155]\n"
        "ds_read_b128 v[204:207], v248 offset:4096\n"
        "v_accvgpr_read_b32 v178, a146\n"
        "v_mfma_f32_16x16x32_bf16 v[0:3], v[32:35], v[192:195], v[0:3]\n"
        "ds_read_b64_tr_b16 v[160:161], v226 offset:50176\n"
        "s_waitcnt lgkmcnt(5)\n"
        "v_mfma_f32_16x16x32_bf16 v[0:3], v[36:39], v[208:211], v[0:3]\n"
        "global_load_lds_dwordx4 v245, s[64:65] offset:3072\n"
        "ds_read_b64_tr_b16 v[162:163], v227 offset:50176\n"
        "s_add_u32 m0, s72, 0x1000\n"
        "v_mfma_f32_16x16x32_bf16 v[4:7], v[44:47], v[208:211], v[4:7]\n"
        "ds_read_b64_tr_b16 v[164:165], v226 offset:58368\n"
        "s_add_u32 s64, s64, 0x80\n"
        "s_addc_u32 s65, s65, 0\n"
        "v_mfma_f32_16x16x32_bf16 v[4:7], v[40:43], v[192:195], v[4:7]\n"
        "ds_read_b64_tr_b16 v[166:167], v227 offset:58368\n"
        "v_accvgpr_read_b32 v179, a147\n"
        "v_mfma_f32_16x16x32_bf16 v[68:71], v[40:43], v[196:199], v[68:71]\n"
        "ds_read_b64_tr_b16 v[172:173], v228 offset:58368\n"
        "v_accvgpr_read_b32 v180, a176\n"
        "v_mfma_f32_16x16x32_bf16 v[68:71], v[44:47], v[212:215], v[68:71]\n"
        "ds_read_b64_tr_b16 v[174:175], v229 offset:58368\n"
        "global_load_lds_dwordx4 v242, s[66:67] offset:0\n"
        "v_mfma_f32_16x16x32_bf16 v[64:67], v[36:39], v[212:215], v[64:67]\n"
        "ds_read_b64_tr_b16 v[168:169], v228 offset:50176\n"
        "v_accvgpr_read_b32 v181, a177\n"
        "v_mfma_f32_16x16x32_bf16 v[64:67], v[32:35], v[196:199], v[64:67]\n"
        "ds_read_b64_tr_b16 v[170:171], v229 offset:50176\n"
        "s_waitcnt lgkmcnt(8)\n"
        "v_mfma_f32_16x16x32_bf16 v[96:99], v[32:35], v[200:203], v[96:99]\n"
        "v_accvgpr_read_b32 v182, a178\n"
        "v_accvgpr_read_b32 v183, a179\n"
        "v_mfma_f32_16x16x32_bf16 v[96:99], v[36:39], v[216:219], v[96:99]\n"
        "global_load_lds_dwordx4 v243, s[66:67] offset:1024\n"
        "v_mfma_f32_16x16x32_bf16 v[100:103], v[44:47], v[216:219], v[100:103]\n"
        "v_mfma_f32_16x16x32_bf16 v[100:103], v[40:43], v[200:203], v[100:103]\n"
        "v_mfma_f32_16x16x32_bf16 v[132:135], v[40:43], v[204:207], v[132:135]\n"
        "ds_read_b64_tr_b16 v[40:41], v232 offset:50176\n"
        "v_mfma_f32_16x16x32_bf16 v[132:135], v[44:47], v[220:223], v[132:135]\n"
        "global_load_lds_dwordx4 v244, s[66:67] offset:2048\n"
        "v_mfma_f32_16x16x32_bf16 v[128:131], v[36:39], v[220:223], v[128:131]\n"
        "ds_read_b64_tr_b16 v[36:37], v230 offset:58368\n"
        "v_mfma_f32_16x16x32_bf16 v[128:131], v[32:35], v[204:207], v[128:131]\n"
        "ds_read_b64_tr_b16 v[32:33], v230 offset:50176\n"
        "v_accvgpr_read_b32 v184, a220\n"
        "v_mfma_f32_16x16x32_bf16 v[8:11], v[48:51], v[192:195], v[8:11]\n"
        "ds_read_b64_tr_b16 v[34:35], v231 offset:50176\n"
        "v_cvt_pk_bf16_f32 v184, v184, v185\n"
        "v_mfma_f32_16x16x32_bf16 v[8:11], v[52:55], v[208:211], v[8:11]\n"
        "global_load_lds_dwordx4 v245, s[66:67] offset:3072\n"
        "s_add_u32 m0, s73, 0x24000\n"
        "v_mfma_f32_16x16x32_bf16 v[12:15], v[60:63], v[208:211], v[12:15]\n"
        "ds_read_b64_tr_b16 v[38:39], v231 offset:58368\n"
        "v_cvt_pk_bf16_f32 v185, v186, v187\n"
        "v_mfma_f32_16x16x32_bf16 v[12:15], v[56:59], v[192:195], v[12:15]\n"
        "ds_read_b64_tr_b16 v[44:45], v232 offset:58368\n"
        "v_cvt_pk_bf16_f32 v186, v188, v189\n"
        "v_mfma_f32_16x16x32_bf16 v[76:79], v[56:59], v[196:199], v[76:79]\n"
        "ds_read_b64_tr_b16 v[46:47], v233 offset:58368\n"
        "v_cvt_pk_bf16_f32 v187, v190, v191\n"
        "v_mfma_f32_16x16x32_bf16 v[76:79], v[60:63], v[212:215], v[76:79]\n"
        "s_waitcnt lgkmcnt(6)\n"
        "v_accvgpr_write_b32 a244, v184\n"
        "v_accvgpr_write_b32 a245, v185\n"
        "v_mfma_f32_16x16x32_bf16 v[72:75], v[52:55], v[212:215], v[72:75]\n"
        "ds_read_b64_tr_b16 v[42:43], v233 offset:50176\n"
        "v_accvgpr_write_b32 a246, v186\n"
        "v_mfma_f32_16x16x32_bf16 v[72:75], v[48:51], v[196:199], v[72:75]\n"
        "s_add_u32 s66, s66, 0x80\n"
        "s_addc_u32 s67, s67, 0\n"
        "v_accvgpr_write_b32 a247, v187\n"
        "v_mfma_f32_16x16x32_bf16 v[104:107], v[48:51], v[200:203], v[104:107]\n"
        "global_store_dwordx4 v254, a[196:199], s[70:71] offset:1024 sc1\n"
        "s_add_u32 s70, s38, s97\n"
        "s_addc_u32 s71, s39, 0\n"
        "v_accvgpr_read_b32 v185, a149\n"
        "v_accvgpr_read_b32 v186, a150\n"
        "v_accvgpr_read_b32 v187, a151\n"
        "v_mfma_f32_16x16x32_bf16 v[104:107], v[52:55], v[216:219], v[104:107]\n"
        "v_accvgpr_read_b32 v188, a180\n"
        "v_accvgpr_read_b32 v189, a181\n"
        "v_mfma_f32_16x16x32_bf16 v[108:111], v[60:63], v[216:219], v[108:111]\n"
        "v_accvgpr_read_b32 v190, a182\n"
        "v_accvgpr_read_b32 v191, a183\n"
        "v_mfma_f32_16x16x32_bf16 v[108:111], v[56:59], v[200:203], v[108:111]\n"
        "v_mfma_f32_16x16x32_bf16 v[140:143], v[56:59], v[204:207], v[140:143]\n"
        "v_mfma_f32_16x16x32_bf16 v[140:143], v[60:63], v[220:223], v[140:143]\n"
        "s_waitcnt vmcnt(13) lgkmcnt(0)\n"
        "s_barrier\n"
        "v_mfma_f32_16x16x32_bf16 v[136:139], v[52:55], v[220:223], v[136:139]\n"
        "global_load_lds_dwordx4 v238, s[68:69] offset:0\n"
        "ds_read_b64_tr_b16 v[52:53], v226 offset:24576\n"
        "v_mfma_f32_16x16x32_bf16 v[136:139], v[48:51], v[204:207], v[136:139]\n"
        "ds_read_b64_tr_b16 v[48:49], v226 offset:16384\n"
        "v_mfma_f32_16x16x32_bf16 v[16:19], v[160:163], v[192:195], v[16:19]\n"
        "ds_read_b64_tr_b16 v[50:51], v227 offset:16384\n"
        "v_mfma_f32_16x16x32_bf16 v[16:19], v[164:167], v[208:211], v[16:19]\n"
        "ds_read_b64_tr_b16 v[54:55], v227 offset:24576\n"
        "v_mfma_f32_16x16x32_bf16 v[20:23], v[172:175], v[208:211], v[20:23]\n"
        "ds_read_b64_tr_b16 v[60:61], v228 offset:24576\n"
        "global_load_lds_dwordx4 v239, s[68:69] offset:1024\n"
        "v_mfma_f32_16x16x32_bf16 v[20:23], v[168:171], v[192:195], v[20:23]\n"
        "ds_read_b64_tr_b16 v[62:63], v229 offset:24576\n"
        "v_mfma_f32_16x16x32_bf16 v[84:87], v[168:171], v[196:199], v[84:87]\n"
        "ds_read_b64_tr_b16 v[56:57], v228 offset:16384\n"
        "v_mfma_f32_16x16x32_bf16 v[84:87], v[172:175], v[212:215], v[84:87]\n"
        "ds_read_b64_tr_b16 v[58:59], v229 offset:16384\n"
        "v_mfma_f32_16x16x32_bf16 v[80:83], v[164:167], v[212:215], v[80:83]\n"
        "global_load_lds_dwordx4 v240, s[68:69] offset:2048\n"
        "v_mfma_f32_16x16x32_bf16 v[80:83], v[160:163], v[196:199], v[80:83]\n"
        "v_accvgpr_read_b32 v176, a144\n"
        "v_cvt_pk_bf16_f32 v176, v176, v177\n"
        "v_mfma_f32_16x16x32_bf16 v[112:115], v[160:163], v[200:203], v[112:115]\n"
        "v_cvt_pk_bf16_f32 v177, v178, v179\n"
        "v_cvt_pk_bf16_f32 v178, v180, v181\n"
        "v_mfma_f32_16x16x32_bf16 v[112:115], v[164:167], v[216:219], v[112:115]\n"
        "v_cvt_pk_bf16_f32 v179, v182, v183\n"
        "v_accvgpr_write_b32 a216, v176\n"
        "v_mfma_f32_16x16x32_bf16 v[116:119], v[172:175], v[216:219], v[116:119]\n"
        "v_accvgpr_write_b32 a217, v177\n"
        "v_accvgpr_write_b32 a218, v178\n"
        "v_mfma_f32_16x16x32_bf16 v[116:119], v[168:171], v[200:203], v[116:119]\n"
        "global_load_lds_dwordx4 v241, s[68:69] offset:3072\n"
        "s_add_u32 m0, s72, 0x10000\n"
        "v_accvgpr_write_b32 a219, v179\n"
        "v_mfma_f32_16x16x32_bf16 v[148:151], v[168:171], v[204:207], v[148:151]\n"
        "ds_read_b64_tr_b16 v[168:169], v232 offset:16384\n"
        "s_add_u32 s68, s68, 0x80000\n"
        "s_addc_u32 s69, s69, 0\n"
        "v_mfma_f32_16x16x32_bf16 v[148:151], v[172:175], v[220:223], v[148:151]\n"
        "ds_read_b64_tr_b16 v[172:173], v232 offset:24576\n"
        "v_accvgpr_read_b32 v177, a153\n"
        "v_mfma_f32_16x16x32_bf16 v[144:147], v[164:167], v[220:223], v[144:147]\n"
        "ds_read_b64_tr_b16 v[164:165], v230 offset:24576\n"
        "v_accvgpr_read_b32 v178, a154\n"
        "v_mfma_f32_16x16x32_bf16 v[144:147], v[160:163], v[204:207], v[144:147]\n"
        "ds_read_b64_tr_b16 v[160:161], v230 offset:16384\n"
        "global_load_lds_dwordx4 v242, s[64:65] offset:0\n"
        "v_mfma_f32_16x16x32_bf16 v[24:27], v[32:35], v[192:195], v[24:27]\n"
        "ds_read_b64_tr_b16 v[162:163], v231 offset:16384\n"
        "v_accvgpr_read_b32 v179, a155\n"
        "v_mfma_f32_16x16x32_bf16 v[24:27], v[36:39], v[208:211], v[24:27]\n"
        "ds_read_b64_tr_b16 v[166:167], v231 offset:24576\n"
        "v_accvgpr_read_b32 v180, a184\n"
        "v_mfma_f32_16x16x32_bf16 v[28:31], v[44:47], v[208:211], v[28:31]\n"
        "ds_read_b64_tr_b16 v[174:175], v233 offset:24576\n"
        "v_accvgpr_read_b32 v181, a185\n"
        "v_mfma_f32_16x16x32_bf16 v[28:31], v[40:43], v[192:195], v[28:31]\n"
        "ds_read_b64_tr_b16 v[170:171], v233 offset:16384\n"
        "global_load_lds_dwordx4 v243, s[64:65] offset:1024\n"
        "v_mfma_f32_16x16x32_bf16 v[92:95], v[40:43], v[196:199], v[92:95]\n"
        "ds_read_b128 v[192:195], v246 offset:34816\n"
        "v_accvgpr_read_b32 v182, a186\n"
        "v_mfma_f32_16x16x32_bf16 v[92:95], v[44:47], v[212:215], v[92:95]\n"
        "ds_read_b128 v[208:211], v247 offset:34816\n"
        "v_accvgpr_read_b32 v183, a187\n"
        "v_mfma_f32_16x16x32_bf16 v[88:91], v[36:39], v[212:215], v[88:91]\n"
        "ds_read_b128 v[212:215], v247 offset:32768\n"
        "v_mfma_f32_16x16x32_bf16 v[88:91], v[32:35], v[196:199], v[88:91]\n"
        "ds_read_b128 v[196:199], v246 offset:32768\n"
        "global_load_lds_dwordx4 v244, s[64:65] offset:2048\n"
        "v_mfma_f32_16x16x32_bf16 v[120:123], v[32:35], v[200:203], v[120:123]\n"
        "v_mfma_f32_16x16x32_bf16 v[120:123], v[36:39], v[216:219], v[120:123]\n"
        "v_mfma_f32_16x16x32_bf16 v[124:127], v[44:47], v[216:219], v[124:127]\n"
        "ds_read_b128 v[216:219], v247 offset:38912\n"
        "v_mfma_f32_16x16x32_bf16 v[124:127], v[40:43], v[200:203], v[124:127]\n"
        "ds_read_b128 v[200:203], v246 offset:38912\n"
        "global_load_lds_dwordx4 v245, s[64:65] offset:3072\n"
        "s_add_u32 m0, s72, 0x11000\n"
        "v_mfma_f32_16x16x32_bf16 v[156:159], v[40:43], v[204:207], v[156:159]\n"
        "ds_read_b64_tr_b16 v[40:41], v228 offset:17408\n"
        "s_add_u32 s64, s64, 0x80\n"
        "s_addc_u32 s65, s65, 0\n"
        "v_mfma_f32_16x16x32_bf16 v[156:159], v[44:47], v[220:223], v[156:159]\n"
        "ds_read_b64_tr_b16 v[44:45], v228 offset:25600\n"
        "s_waitcnt lgkmcnt(6)\n"
        "v_mfma_f32_16x16x32_bf16 v[152:155], v[36:39], v[220:223], v[152:155]\n"
        "ds_read_b128 v[220:223], v247 offset:36864\n"
        "s_waitcnt lgkmcnt(6)\n"
        "v_mfma_f32_16x16x32_bf16 v[152:155], v[32:35], v[204:207], v[152:155]\n"
        "ds_read_b128 v[204:207], v246 offset:36864\n"
        "v_accvgpr_read_b32 v184, a148\n"
        "v_mfma_f32_16x16x32_bf16 v[0:3], v[48:51], v[192:195], v[0:3]\n"
        "s_waitcnt lgkmcnt(6)\n"
        "v_cvt_pk_bf16_f32 v184, v184, v185\n"
        "v_cvt_pk_bf16_f32 v185, v186, v187\n"
        "v_mfma_f32_16x16x32_bf16 v[0:3], v[52:55], v[208:211], v[0:3]\n"
        "global_load_lds_dwordx4 v242, s[66:67] offset:0\n"
        "v_cvt_pk_bf16_f32 v186, v188, v189\n"
        "v_mfma_f32_16x16x32_bf16 v[4:7], v[60:63], v[208:211], v[4:7]\n"
        "s_waitcnt lgkmcnt(5)\n"
        "v_cvt_pk_bf16_f32 v187, v190, v191\n"
        "v_accvgpr_write_b32 a248, v184\n"
        "v_mfma_f32_16x16x32_bf16 v[4:7], v[56:59], v[192:195], v[4:7]\n"
        "s_waitcnt lgkmcnt(4)\n"
        "v_accvgpr_write_b32 a249, v185\n"
        "v_accvgpr_write_b32 a250, v186\n"
        "v_mfma_f32_16x16x32_bf16 v[68:71], v[56:59], v[196:199], v[68:71]\n"
        "ds_read_b64_tr_b16 v[32:33], v226 offset:17408\n"
        "v_accvgpr_write_b32 a251, v187\n"
        "v_mfma_f32_16x16x32_bf16 v[68:71], v[60:63], v[212:215], v[68:71]\n"
        "ds_read_b64_tr_b16 v[34:35], v227 offset:17408\n"
        "global_load_lds_dwordx4 v243, s[66:67] offset:1024\n"
        "v_mfma_f32_16x16x32_bf16 v[64:67], v[52:55], v[212:215], v[64:67]\n"
        "ds_read_b64_tr_b16 v[36:37], v226 offset:25600\n"
        "v_accvgpr_read_b32 v185, a157\n"
        "v_mfma_f32_16x16x32_bf16 v[64:67], v[48:51], v[196:199], v[64:67]\n"
        "ds_read_b64_tr_b16 v[38:39], v227 offset:25600\n"
        "v_accvgpr_read_b32 v186, a158\n"
        "v_mfma_f32_16x16x32_bf16 v[96:99], v[48:51], v[200:203], v[96:99]\n"
        "ds_read_b64_tr_b16 v[46:47], v229 offset:25600\n"
        "v_accvgpr_read_b32 v187, a159\n"
        "v_mfma_f32_16x16x32_bf16 v[96:99], v[52:55], v[216:219], v[96:99]\n"
        "ds_read_b64_tr_b16 v[42:43], v229 offset:17408\n"
        "global_load_lds_dwordx4 v244, s[66:67] offset:2048\n"
        "s_add_u32 s97, s52, 0x1000\n"
        "v_mfma_f32_16x16x32_bf16 v[100:103], v[60:63], v[216:219], v[100:103]\n"
        "s_waitcnt lgkmcnt(6)\n"
        "v_accvgpr_read_b32 v188, a188\n"
        "v_accvgpr_read_b32 v189, a189\n"
        "v_mfma_f32_16x16x32_bf16 v[100:103], v[56:59], v[200:203], v[100:103]\n"
        "v_accvgpr_read_b32 v190, a190\n"
        "v_accvgpr_read_b32 v191, a191\n"
        "v_mfma_f32_16x16x32_bf16 v[132:135], v[56:59], v[204:207], v[132:135]\n"
        "ds_read_b64_tr_b16 v[56:57], v232 offset:17408\n"
        "v_mfma_f32_16x16x32_bf16 v[132:135], v[60:63], v[220:223], v[132:135]\n"
        "global_load_lds_dwordx4 v245, s[66:67] offset:3072\n"
        "s_add_u32 m0, s73, 0x1c000\n"
        "v_mfma_f32_16x16x32_bf16 v[128:131], v[52:55], v[220:223], v[128:131]\n"
        "ds_read_b64_tr_b16 v[52:53], v230 offset:25600\n"
        "s_add_u32 s66, s66, 0x80\n"
        "s_addc_u32 s67, s67, 0\n"
        "v_mfma_f32_16x16x32_bf16 v[128:131], v[48:51], v[204:207], v[128:131]\n"
        "ds_read_b64_tr_b16 v[48:49], v230 offset:17408\n"
        "v_mfma_f32_16x16x32_bf16 v[8:11], v[160:163], v[192:195], v[8:11]\n"
        "ds_read_b64_tr_b16 v[50:51], v231 offset:17408\n"
        "v_mfma_f32_16x16x32_bf16 v[8:11], v[164:167], v[208:211], v[8:11]\n"
        "ds_read_b64_tr_b16 v[54:55], v231 offset:25600\n"
        "v_mfma_f32_16x16x32_bf16 v[12:15], v[172:175], v[208:211], v[12:15]\n"
        "ds_read_b64_tr_b16 v[60:61], v232 offset:25600\n"
        "v_mfma_f32_16x16x32_bf16 v[12:15], v[168:171], v[192:195], v[12:15]\n"
        "ds_read_b64_tr_b16 v[62:63], v233 offset:25600\n"
        "v_mfma_f32_16x16x32_bf16 v[76:79], v[168:171], v[196:199], v[76:79]\n"
        "ds_read_b64_tr_b16 v[58:59], v233 offset:17408\n"
        "v_mfma_f32_16x16x32_bf16 v[76:79], v[172:175], v[212:215], v[76:79]\n"
        "s_waitcnt lgkmcnt(7)\n"
        "v_mfma_f32_16x16x32_bf16 v[72:75], v[164:167], v[212:215], v[72:75]\n"
        "v_mfma_f32_16x16x32_bf16 v[72:75], v[160:163], v[196:199], v[72:75]\n"
        "v_accvgpr_read_b32 v176, a152\n"
        "v_cvt_pk_bf16_f32 v176, v176, v177\n"
        "v_mfma_f32_16x16x32_bf16 v[104:107], v[160:163], v[200:203], v[104:107]\n"
        "v_cvt_pk_bf16_f32 v177, v178, v179\n"
        "v_cvt_pk_bf16_f32 v178, v180, v181\n"
        "v_mfma_f32_16x16x32_bf16 v[104:107], v[164:167], v[216:219], v[104:107]\n"
        "v_cvt_pk_bf16_f32 v179, v182, v183\n"
        "v_accvgpr_write_b32 a220, v176\n"
        "v_mfma_f32_16x16x32_bf16 v[108:111], v[172:175], v[216:219], v[108:111]\n"
        "v_accvgpr_write_b32 a221, v177\n"
        "v_accvgpr_write_b32 a222, v178\n"
        "v_mfma_f32_16x16x32_bf16 v[108:111], v[168:171], v[200:203], v[108:111]\n"
        "v_accvgpr_write_b32 a223, v179\n"
        "v_accvgpr_read_b32 v177, a17\n"
        "v_mfma_f32_16x16x32_bf16 v[140:143], v[168:171], v[204:207], v[140:143]\n"
        "s_waitcnt vmcnt(13) lgkmcnt(0)\n"
        "s_barrier\n"
        "v_accvgpr_read_b32 v178, a18\n"
        "v_mfma_f32_16x16x32_bf16 v[140:143], v[172:175], v[220:223], v[140:143]\n"
        "global_load_lds_dwordx4 v238, s[68:69] offset:0\n"
        "ds_read_b64_tr_b16 v[172:173], v228 offset:40960\n"
        "v_mfma_f32_16x16x32_bf16 v[136:139], v[164:167], v[220:223], v[136:139]\n"
        "ds_read_b64_tr_b16 v[164:165], v226 offset:40960\n"
        "v_accvgpr_read_b32 v179, a19\n"
        "v_mfma_f32_16x16x32_bf16 v[136:139], v[160:163], v[204:207], v[136:139]\n"
        "ds_read_b64_tr_b16 v[160:161], v226 offset:32768\n"
        "v_accvgpr_read_b32 v180, a48\n"
        "v_mfma_f32_16x16x32_bf16 v[16:19], v[32:35], v[192:195], v[16:19]\n"
        "ds_read_b64_tr_b16 v[162:163], v227 offset:32768\n"
        "v_accvgpr_read_b32 v181, a49\n"
        "v_mfma_f32_16x16x32_bf16 v[16:19], v[36:39], v[208:211], v[16:19]\n"
        "ds_read_b64_tr_b16 v[166:167], v227 offset:40960\n"
        "global_load_lds_dwordx4 v239, s[68:69] offset:1024\n"
        "v_mfma_f32_16x16x32_bf16 v[20:23], v[44:47], v[208:211], v[20:23]\n"
        "ds_read_b64_tr_b16 v[174:175], v229 offset:40960\n"
        "v_accvgpr_read_b32 v182, a50\n"
        "v_mfma_f32_16x16x32_bf16 v[20:23], v[40:43], v[192:195], v[20:23]\n"
        "ds_read_b64_tr_b16 v[168:169], v228 offset:32768\n"
        "v_accvgpr_read_b32 v183, a51\n"
        "v_mfma_f32_16x16x32_bf16 v[84:87], v[40:43], v[196:199], v[84:87]\n"
        "ds_read_b64_tr_b16 v[170:171], v229 offset:32768\n"
        "v_mfma_f32_16x16x32_bf16 v[84:87], v[44:47], v[212:215], v[84:87]\n"
        "global_load_lds_dwordx4 v240, s[68:69] offset:2048\n"
        "v_mfma_f32_16x16x32_bf16 v[80:83], v[36:39], v[212:215], v[80:83]\n"
        "v_mfma_f32_16x16x32_bf16 v[80:83], v[32:35], v[196:199], v[80:83]\n"
        "v_mfma_f32_16x16x32_bf16 v[112:115], v[32:35], v[200:203], v[112:115]\n"
        "v_mfma_f32_16x16x32_bf16 v[112:115], v[36:39], v[216:219], v[112:115]\n"
        "global_load_lds_dwordx4 v241, s[68:69] offset:3072\n"
        "s_add_u32 m0, s72, 0x8000\n"
        "v_mfma_f32_16x16x32_bf16 v[116:119], v[44:47], v[216:219], v[116:119]\n"
        "s_add_u32 s68, s68, 0x80000\n"
        "s_addc_u32 s69, s69, 0\n"
        "v_mfma_f32_16x16x32_bf16 v[116:119], v[40:43], v[200:203], v[116:119]\n"
        "v_mfma_f32_16x16x32_bf16 v[148:151], v[40:43], v[204:207], v[148:151]\n"
        "ds_read_b64_tr_b16 v[40:41], v232 offset:32768\n"
        "v_mfma_f32_16x16x32_bf16 v[148:151], v[44:47], v[220:223], v[148:151]\n"
        "global_load_lds_dwordx4 v242, s[64:65] offset:0\n"
        "v_mfma_f32_16x16x32_bf16 v[144:147], v[36:39], v[220:223], v[144:147]\n"
        "ds_read_b64_tr_b16 v[36:37], v230 offset:40960\n"
        "v_mfma_f32_16x16x32_bf16 v[144:147], v[32:35], v[204:207], v[144:147]\n"
        "ds_read_b64_tr_b16 v[32:33], v230 offset:32768\n"
        "v_accvgpr_read_b32 v184, a156\n"
        "v_mfma_f32_16x16x32_bf16 v[24:27], v[48:51], v[192:195], v[24:27]\n"
        "ds_read_b64_tr_b16 v[34:35], v231 offset:32768\n"
        "v_cvt_pk_bf16_f32 v184, v184, v185\n"
        "v_mfma_f32_16x16x32_bf16 v[24:27], v[52:55], v[208:211], v[24:27]\n"
        "ds_read_b64_tr_b16 v[38:39], v231 offset:40960\n"
        "v_cvt_pk_bf16_f32 v185, v186, v187\n"
        "v_mfma_f32_16x16x32_bf16 v[28:31], v[60:63], v[208:211], v[28:31]\n"
        "global_store_dwordx4 v254, a[200:203], s[70:71] offset:0 sc1\n"
        "ds_read_b64_tr_b16 v[44:45], v232 offset:40960\n"
        "ds_read_b64_tr_b16 v[46:47], v233 offset:40960\n"
        "v_cvt_pk_bf16_f32 v186, v188, v189\n"
        "v_mfma_f32_16x16x32_bf16 v[28:31], v[56:59], v[192:195], v[28:31]\n"
        "ds_read_b64_tr_b16 v[42:43], v233 offset:32768\n"
        "v_cvt_pk_bf16_f32 v187, v190, v191\n"
        "v_mfma_f32_16x16x32_bf16 v[92:95], v[56:59], v[196:199], v[92:95]\n"
        "ds_read_b128 v[192:195], v246 offset:2048\n"
        "v_accvgpr_write_b32 a252, v184\n"
        "v_mfma_f32_16x16x32_bf16 v[92:95], v[60:63], v[212:215], v[92:95]\n"
        "ds_read_b128 v[208:211], v247 offset:2048\n"
        "v_accvgpr_write_b32 a253, v185\n"
        "v_mfma_f32_16x16x32_bf16 v[88:91], v[52:55], v[212:215], v[88:91]\n"
        "global_load_lds_dwordx4 v243, s[64:65] offset:1024\n"
        "v_accvgpr_write_b32 a254, v186\n"
        "v_mfma_f32_16x16x32_bf16 v[88:91], v[48:51], v[196:199], v[88:91]\n"
        "ds_read_b128 v[196:199], v246 offset:0\n"
        "v_accvgpr_write_b32 a255, v187\n"
        "v_mfma_f32_16x16x32_bf16 v[120:123], v[48:51], v[200:203], v[120:123]\n"
        "ds_read_b128 v[212:215], v247 offset:0\n"
        "v_accvgpr_read_b32 v185, a21\n"
        "v_mfma_f32_16x16x32_bf16 v[120:123], v[52:55], v[216:219], v[120:123]\n"
        "v_accvgpr_read_b32 v186, a22\n"
        "v_accvgpr_read_b32 v187, a23\n"
        "v_mfma_f32_16x16x32_bf16 v[124:127], v[60:63], v[216:219], v[124:127]\n"
        "global_load_lds_dwordx4 v244, s[64:65] offset:2048\n"
        "ds_read_b128 v[216:219], v247 offset:6144\n"
        "v_mfma_f32_16x16x32_bf16 v[124:127], v[56:59], v[200:203], v[124:127]\n"
        "ds_read_b128 v[200:203], v246 offset:6144\n"
        "v_accvgpr_read_b32 v188, a52\n"
        "v_mfma_f32_16x16x32_bf16 v[156:159], v[56:59], v[204:207], v[156:159]\n"
        "ds_read_b64_tr_b16 v[56:57], v228 offset:33792\n"
        "v_accvgpr_read_b32 v189, a53\n"
        "v_mfma_f32_16x16x32_bf16 v[156:159], v[60:63], v[220:223], v[156:159]\n"
        "ds_read_b64_tr_b16 v[60:61], v228 offset:41984\n"
        "v_accvgpr_read_b32 v190, a54\n"
        "v_mfma_f32_16x16x32_bf16 v[152:155], v[52:55], v[220:223], v[152:155]\n"
        "global_load_lds_dwordx4 v245, s[64:65] offset:3072\n"
        "ds_read_b128 v[220:223], v247 offset:4096\n"
        "s_add_u32 m0, s72, 0x9000\n"
        "v_mfma_f32_16x16x32_bf16 v[152:155], v[48:51], v[204:207], v[152:155]\n"
        "ds_read_b128 v[204:207], v246 offset:4096\n"
        "s_waitcnt lgkmcnt(7)\n"
        "v_mfma_f32_16x16x32_bf16 v[0:3], v[160:163], v[192:195], v[0:3]\n"
        "ds_read_b64_tr_b16 v[48:49], v226 offset:33792\n"
        "s_add_u32 s64, s64, 0x80\n"
        "s_addc_u32 s65, s65, 0\n"
        "v_mfma_f32_16x16x32_bf16 v[0:3], v[164:167], v[208:211], v[0:3]\n"
        "ds_read_b64_tr_b16 v[50:51], v227 offset:33792\n"
        "v_accvgpr_read_b32 v191, a55\n"
        "v_mfma_f32_16x16x32_bf16 v[4:7], v[172:175], v[208:211], v[4:7]\n"
        "ds_read_b64_tr_b16 v[52:53], v226 offset:41984\n"
        "global_load_lds_dwordx4 v242, s[66:67] offset:0\n"
        "v_mfma_f32_16x16x32_bf16 v[4:7], v[168:171], v[192:195], v[4:7]\n"
        "ds_read_b64_tr_b16 v[54:55], v227 offset:41984\n"
        "s_waitcnt lgkmcnt(8)\n"
        "v_mfma_f32_16x16x32_bf16 v[68:71], v[168:171], v[196:199], v[68:71]\n"
        "ds_read_b64_tr_b16 v[62:63], v229 offset:41984\n"
        "v_mfma_f32_16x16x32_bf16 v[68:71], v[172:175], v[212:215], v[68:71]\n"
        "ds_read_b64_tr_b16 v[58:59], v229 offset:33792\n"
        "s_waitcnt lgkmcnt(8)\n"
        "v_mfma_f32_16x16x32_bf16 v[64:67], v[164:167], v[212:215], v[64:67]\n"
        "global_load_lds_dwordx4 v243, s[66:67] offset:1024\n"
        "v_mfma_f32_16x16x32_bf16 v[64:67], v[160:163], v[196:199], v[64:67]\n"
        "s_waitcnt lgkmcnt(6)\n"
        "v_accvgpr_read_b32 v176, a16\n"
        "v_cvt_pk_bf16_f32 v176, v176, v177\n"
        "v_mfma_f32_16x16x32_bf16 v[96:99], v[160:163], v[200:203], v[96:99]\n"
        "v_cvt_pk_bf16_f32 v177, v178, v179\n"
        "v_cvt_pk_bf16_f32 v178, v180, v181\n"
        "v_mfma_f32_16x16x32_bf16 v[96:99], v[164:167], v[216:219], v[96:99]\n"
        "v_cvt_pk_bf16_f32 v179, v182, v183\n"
        "v_accvgpr_write_b32 a176, v176\n"
        "v_mfma_f32_16x16x32_bf16 v[100:103], v[172:175], v[216:219], v[100:103]\n"
        "v_accvgpr_write_b32 a177, v177\n"
        "v_accvgpr_write_b32 a178, v178\n"
        "v_mfma_f32_16x16x32_bf16 v[100:103], v[168:171], v[200:203], v[100:103]\n"
        "global_load_lds_dwordx4 v244, s[66:67] offset:2048\n"
        "v_accvgpr_write_b32 a179, v179\n"
        "v_mfma_f32_16x16x32_bf16 v[132:135], v[168:171], v[204:207], v[132:135]\n"
        "ds_read_b64_tr_b16 v[168:169], v232 offset:33792\n"
        "v_accvgpr_read_b32 v177, a25\n"
        "v_mfma_f32_16x16x32_bf16 v[132:135], v[172:175], v[220:223], v[132:135]\n"
        "ds_read_b64_tr_b16 v[172:173], v232 offset:41984\n"
        "v_accvgpr_read_b32 v178, a26\n"
        "v_mfma_f32_16x16x32_bf16 v[128:131], v[164:167], v[220:223], v[128:131]\n"
        "ds_read_b64_tr_b16 v[164:165], v230 offset:41984\n"
        "v_accvgpr_read_b32 v179, a27\n"
        "v_mfma_f32_16x16x32_bf16 v[128:131], v[160:163], v[204:207], v[128:131]\n"
        "global_load_lds_dwordx4 v245, s[66:67] offset:3072\n"
        "ds_read_b64_tr_b16 v[160:161], v230 offset:33792\n"
        "s_add_u32 m0, s73, 0x20000\n"
        "v_mfma_f32_16x16x32_bf16 v[8:11], v[32:35], v[192:195], v[8:11]\n"
        "ds_read_b64_tr_b16 v[162:163], v231 offset:33792\n"
        "s_add_u32 s66, s66, 0x80\n"
        "s_addc_u32 s67, s67, 0\n"
        "v_mfma_f32_16x16x32_bf16 v[8:11], v[36:39], v[208:211], v[8:11]\n"
        "ds_read_b64_tr_b16 v[166:167], v231 offset:41984\n"
        "v_accvgpr_read_b32 v180, a56\n"
        "v_mfma_f32_16x16x32_bf16 v[12:15], v[44:47], v[208:211], v[12:15]\n"
        "ds_read_b64_tr_b16 v[174:175], v233 offset:41984\n"
        "v_accvgpr_read_b32 v181, a57\n"
        "v_mfma_f32_16x16x32_bf16 v[12:15], v[40:43], v[192:195], v[12:15]\n"
        "ds_read_b64_tr_b16 v[170:171], v233 offset:33792\n"
        "v_accvgpr_read_b32 v182, a58\n"
        "v_mfma_f32_16x16x32_bf16 v[76:79], v[40:43], v[196:199], v[76:79]\n"
        "v_accvgpr_read_b32 v183, a59\n"
        "v_mfma_f32_16x16x32_bf16 v[76:79], v[44:47], v[212:215], v[76:79]\n"
        "s_waitcnt lgkmcnt(6)\n"
        "v_mfma_f32_16x16x32_bf16 v[72:75], v[36:39], v[212:215], v[72:75]\n"
        "v_mfma_f32_16x16x32_bf16 v[72:75], v[32:35], v[196:199], v[72:75]\n"
        "v_mfma_f32_16x16x32_bf16 v[104:107], v[32:35], v[200:203], v[104:107]\n"
        "v_mfma_f32_16x16x32_bf16 v[104:107], v[36:39], v[216:219], v[104:107]\n"
        "v_mfma_f32_16x16x32_bf16 v[108:111], v[44:47], v[216:219], v[108:111]\n"
        "v_mfma_f32_16x16x32_bf16 v[108:111], v[40:43], v[200:203], v[108:111]\n"
        "s_waitcnt vmcnt(13) lgkmcnt(0)\n"
        "s_barrier\n"
        "v_mfma_f32_16x16x32_bf16 v[140:143], v[40:43], v[204:207], v[140:143]\n"
        "global_load_lds_dwordx4 v238, s[68:69] offset:0\n"
        "ds_read_b64_tr_b16 v[40:41], v228 offset:49152\n"
        "v_mfma_f32_16x16x32_bf16 v[140:143], v[44:47], v[220:223], v[140:143]\n"
        "ds_read_b64_tr_b16 v[44:45], v228 offset:57344\n"
        "v_mfma_f32_16x16x32_bf16 v[136:139], v[36:39], v[220:223], v[136:139]\n"
        "ds_read_b64_tr_b16 v[36:37], v226 offset:57344\n"
        "v_mfma_f32_16x16x32_bf16 v[136:139], v[32:35], v[204:207], v[136:139]\n"
        "ds_read_b64_tr_b16 v[32:33], v226 offset:49152\n"
        "v_accvgpr_read_b32 v184, a20\n"
        "v_mfma_f32_16x16x32_bf16 v[16:19], v[48:51], v[192:195], v[16:19]\n"
        "ds_read_b64_tr_b16 v[34:35], v227 offset:49152\n"
        "v_cvt_pk_bf16_f32 v184, v184, v185\n"
        "v_mfma_f32_16x16x32_bf16 v[16:19], v[52:55], v[208:211], v[16:19]\n"
        "ds_read_b64_tr_b16 v[38:39], v227 offset:57344\n"
        "v_cvt_pk_bf16_f32 v185, v186, v187\n"
        "v_mfma_f32_16x16x32_bf16 v[20:23], v[60:63], v[208:211], v[20:23]\n"
        "ds_read_b64_tr_b16 v[46:47], v229 offset:57344\n"
        "v_cvt_pk_bf16_f32 v186, v188, v189\n"
        "v_mfma_f32_16x16x32_bf16 v[20:23], v[56:59], v[192:195], v[20:23]\n"
        "ds_read_b64_tr_b16 v[42:43], v229 offset:49152\n"
        "v_cvt_pk_bf16_f32 v187, v190, v191\n"
        "v_mfma_f32_16x16x32_bf16 v[84:87], v[56:59], v[196:199], v[84:87]\n"
        "global_load_lds_dwordx4 v239, s[68:69] offset:1024\n"
        "v_accvgpr_write_b32 a180, v184\n"
        "v_mfma_f32_16x16x32_bf16 v[84:87], v[60:63], v[212:215], v[84:87]\n"
        "v_accvgpr_write_b32 a181, v185\n"
        "v_accvgpr_write_b32 a182, v186\n"
        "v_mfma_f32_16x16x32_bf16 v[80:83], v[52:55], v[212:215], v[80:83]\n"
        "v_accvgpr_write_b32 a183, v187\n"
        "v_accvgpr_read_b32 v185, a29\n"
        "v_mfma_f32_16x16x32_bf16 v[80:83], v[48:51], v[196:199], v[80:83]\n"
        "v_accvgpr_read_b32 v186, a30\n"
        "v_accvgpr_read_b32 v187, a31\n"
        "v_mfma_f32_16x16x32_bf16 v[112:115], v[48:51], v[200:203], v[112:115]\n"
        "global_load_lds_dwordx4 v240, s[68:69] offset:2048\n"
        "v_accvgpr_read_b32 v188, a60\n"
        "v_mfma_f32_16x16x32_bf16 v[112:115], v[52:55], v[216:219], v[112:115]\n"
        "LHA8_%=:\n"
        "s_cbranch_execz LHA12_%=\n"
        "v_accvgpr_read_b32 v189, a61\n"
        "v_accvgpr_read_b32 v190, a62\n"
        "v_mfma_f32_16x16x32_bf16 v[116:119], v[60:63], v[216:219], v[116:119]\n"
        "v_accvgpr_read_b32 v191, a63\n"
        "v_mfma_f32_16x16x32_bf16 v[116:119], v[56:59], v[200:203], v[116:119]\n"
        "v_mfma_f32_16x16x32_bf16 v[148:151], v[56:59], v[204:207], v[148:151]\n"
        "global_load_lds_dwordx4 v241, s[68:69] offset:3072\n"
        "ds_read_b64_tr_b16 v[56:57], v232 offset:49152\n"
        "s_add_u32 m0, s72, 0x0\n"
        "v_mfma_f32_16x16x32_bf16 v[148:151], v[60:63], v[220:223], v[148:151]\n"
        "ds_read_b64_tr_b16 v[60:61], v232 offset:57344\n"
        "s_add_u32 s68, s68, 0x80000\n"
        "s_addc_u32 s69, s69, 0\n"
        "v_mfma_f32_16x16x32_bf16 v[144:147], v[52:55], v[220:223], v[144:147]\n"
        "ds_read_b64_tr_b16 v[52:53], v230 offset:57344\n"
        "v_mfma_f32_16x16x32_bf16 v[144:147], v[48:51], v[204:207], v[144:147]\n"
        "ds_read_b64_tr_b16 v[48:49], v230 offset:49152\n"
        "v_mfma_f32_16x16x32_bf16 v[24:27], v[160:163], v[192:195], v[24:27]\n"
        "ds_read_b64_tr_b16 v[50:51], v231 offset:49152\n"
        "global_load_lds_dwordx4 v242, s[64:65] offset:0\n"
        "v_mfma_f32_16x16x32_bf16 v[24:27], v[164:167], v[208:211], v[24:27]\n"
        "ds_read_b64_tr_b16 v[54:55], v231 offset:57344\n"
        "v_mfma_f32_16x16x32_bf16 v[28:31], v[172:175], v[208:211], v[28:31]\n"
        "ds_read_b64_tr_b16 v[62:63], v233 offset:57344\n"
        "v_mfma_f32_16x16x32_bf16 v[28:31], v[168:171], v[192:195], v[28:31]\n"
        "ds_read_b64_tr_b16 v[58:59], v233 offset:49152\n"
        "v_mfma_f32_16x16x32_bf16 v[92:95], v[168:171], v[196:199], v[92:95]\n"
        "ds_read_b128 v[192:195], v248 offset:2048\n"
        "global_load_lds_dwordx4 v243, s[64:65] offset:1024\n"
        "v_mfma_f32_16x16x32_bf16 v[92:95], v[172:175], v[212:215], v[92:95]\n"
        "ds_read_b128 v[208:211], v249 offset:2048\n"
        "v_mfma_f32_16x16x32_bf16 v[88:91], v[164:167], v[212:215], v[88:91]\n"
        "ds_read_b128 v[212:215], v249 offset:0\n"
        "v_mfma_f32_16x16x32_bf16 v[88:91], v[160:163], v[196:199], v[88:91]\n"
        "ds_read_b128 v[196:199], v248 offset:0\n"
        "v_accvgpr_read_b32 v176, a24\n"
        "v_mfma_f32_16x16x32_bf16 v[120:123], v[160:163], v[200:203], v[120:123]\n"
        "v_cvt_pk_bf16_f32 v176, v176, v177\n"
        "v_cvt_pk_bf16_f32 v177, v178, v179\n"
        "v_mfma_f32_16x16x32_bf16 v[120:123], v[164:167], v[216:219], v[120:123]\n"
        "global_load_lds_dwordx4 v244, s[64:65] offset:2048\n"
        "v_cvt_pk_bf16_f32 v178, v180, v181\n"
        "v_mfma_f32_16x16x32_bf16 v[124:127], v[172:175], v[216:219], v[124:127]\n"
        "ds_read_b128 v[216:219], v249 offset:6144\n"
        "v_cvt_pk_bf16_f32 v179, v182, v183\n"
        "v_mfma_f32_16x16x32_bf16 v[124:127], v[168:171], v[200:203], v[124:127]\n"
        "s_waitcnt lgkmcnt(5)\n"
        "v_accvgpr_write_b32 a184, v176\n"
        "v_accvgpr_write_b32 a185, v177\n"
        "v_mfma_f32_16x16x32_bf16 v[156:159], v[168:171], v[204:207], v[156:159]\n"
        "s_waitcnt lgkmcnt(4)\n"
        "v_accvgpr_write_b32 a186, v178\n"
        "v_accvgpr_write_b32 a187, v179\n"
        "v_mfma_f32_16x16x32_bf16 v[156:159], v[172:175], v[220:223], v[156:159]\n"
        "global_store_dwordx4 v254, a[204:207], s[70:71] offset:1024 sc1\n"
        "ds_read_b128 v[200:203], v248 offset:6144\n"
        "ds_read_b64_tr_b16 v[172:173], v228 offset:58368\n"
        "v_accvgpr_read_b32 v177, a81\n"
        "v_mfma_f32_16x16x32_bf16 v[152:155], v[164:167], v[220:223], v[152:155]\n"
        "ds_read_b128 v[220:223], v249 offset:4096\n"
        "v_accvgpr_read_b32 v178, a82\n"
        "v_mfma_f32_16x16x32_bf16 v[152:155], v[160:163], v[204:207], v[152:155]\n"
        "ds_read_b128 v[204:207], v248 offset:4096\n"
        "v_accvgpr_read_b32 v179, a83\n"
        "v_mfma_f32_16x16x32_bf16 v[0:3], v[32:35], v[192:195], v[0:3]\n"
        "s_waitcnt lgkmcnt(5)\n"
        "global_load_lds_dwordx4 v245, s[64:65] offset:3072\n"
        "s_add_u32 m0, s72, 0x1000\n"
        "v_mfma_f32_16x16x32_bf16 v[0:3], v[36:39], v[208:211], v[0:3]\n"
        "ds_read_b64_tr_b16 v[160:161], v226 offset:50176\n"
        "s_add_u32 s64, s64, 0x80\n"
        "s_addc_u32 s65, s65, 0\n"
        "v_mfma_f32_16x16x32_bf16 v[4:7], v[44:47], v[208:211], v[4:7]\n"
        "ds_read_b64_tr_b16 v[162:163], v227 offset:50176\n"
        "v_accvgpr_read_b32 v180, a112\n"
        "v_mfma_f32_16x16x32_bf16 v[4:7], v[40:43], v[192:195], v[4:7]\n"
        "ds_read_b64_tr_b16 v[164:165], v226 offset:58368\n"
        "v_accvgpr_read_b32 v181, a113\n"
        "v_mfma_f32_16x16x32_bf16 v[68:71], v[40:43], v[196:199], v[68:71]\n"
        "ds_read_b64_tr_b16 v[166:167], v227 offset:58368\n"
        "global_load_lds_dwordx4 v242, s[66:67] offset:0\n"
        "v_mfma_f32_16x16x32_bf16 v[68:71], v[44:47], v[212:215], v[68:71]\n"
        "ds_read_b64_tr_b16 v[174:175], v229 offset:58368\n"
        "v_accvgpr_read_b32 v182, a114\n"
        "v_mfma_f32_16x16x32_bf16 v[64:67], v[36:39], v[212:215], v[64:67]\n"
        "ds_read_b64_tr_b16 v[168:169], v228 offset:50176\n"
        "v_accvgpr_read_b32 v183, a115\n"
        "v_mfma_f32_16x16x32_bf16 v[64:67], v[32:35], v[196:199], v[64:67]\n"
        "ds_read_b64_tr_b16 v[170:171], v229 offset:50176\n"
        "s_waitcnt lgkmcnt(7)\n"
        "v_mfma_f32_16x16x32_bf16 v[96:99], v[32:35], v[200:203], v[96:99]\n"
        "global_load_lds_dwordx4 v243, s[66:67] offset:1024\n"
        "v_mfma_f32_16x16x32_bf16 v[96:99], v[36:39], v[216:219], v[96:99]\n"
        "s_add_u32 s70, s38, s97\n"
        "s_addc_u32 s71, s39, 0\n"
        "v_mfma_f32_16x16x32_bf16 v[100:103], v[44:47], v[216:219], v[100:103]\n"
        "v_mfma_f32_16x16x32_bf16 v[100:103], v[40:43], v[200:203], v[100:103]\n"
        "v_mfma_f32_16x16x32_bf16 v[132:135], v[40:43], v[204:207], v[132:135]\n"
        "global_load_lds_dwordx4 v244, s[66:67] offset:2048\n"
        "s_add_u32 s97, s52, 0x2000\n"
        "v_mfma_f32_16x16x32_bf16 v[132:135], v[44:47], v[220:223], v[132:135]\n"
        "ds_read_b64_tr_b16 v[44:45], v232 offset:58368\n"
        "v_mfma_f32_16x16x32_bf16 v[128:131], v[36:39], v[220:223], v[128:131]\n"
        "ds_read_b64_tr_b16 v[36:37], v230 offset:58368\n"
        "v_mfma_f32_16x16x32_bf16 v[128:131], v[32:35], v[204:207], v[128:131]\n"
        "ds_read_b64_tr_b16 v[32:33], v230 offset:50176\n"
        "v_accvgpr_read_b32 v184, a28\n"
        "v_mfma_f32_16x16x32_bf16 v[8:11], v[48:51], v[192:195], v[8:11]\n"
        "global_load_lds_dwordx4 v245, s[66:67] offset:3072\n"
        "s_add_u32 m0, s73, 0x24000\n"
        "v_mfma_f32_16x16x32_bf16 v[8:11], v[52:55], v[208:211], v[8:11]\n"
        "ds_read_b64_tr_b16 v[34:35], v231 offset:50176\n"
        "v_cvt_pk_bf16_f32 v184, v184, v185\n"
        "v_mfma_f32_16x16x32_bf16 v[12:15], v[60:63], v[208:211], v[12:15]\n"
        "ds_read_b64_tr_b16 v[38:39], v231 offset:58368\n"
        "v_cvt_pk_bf16_f32 v185, v186, v187\n"
        "v_mfma_f32_16x16x32_bf16 v[12:15], v[56:59], v[192:195], v[12:15]\n"
        "ds_read_b64_tr_b16 v[46:47], v233 offset:58368\n"
        "v_cvt_pk_bf16_f32 v186, v188, v189\n"
        "v_mfma_f32_16x16x32_bf16 v[76:79], v[56:59], v[196:199], v[76:79]\n"
        "ds_read_b64_tr_b16 v[40:41], v232 offset:50176\n"
        "v_cvt_pk_bf16_f32 v187, v190, v191\n"
        "v_mfma_f32_16x16x32_bf16 v[76:79], v[60:63], v[212:215], v[76:79]\n"
        "s_waitcnt lgkmcnt(6)\n"
        "v_accvgpr_write_b32 a188, v184\n"
        "v_accvgpr_write_b32 a189, v185\n"
        "v_mfma_f32_16x16x32_bf16 v[72:75], v[52:55], v[212:215], v[72:75]\n"
        "ds_read_b64_tr_b16 v[42:43], v233 offset:50176\n"
        "v_accvgpr_write_b32 a190, v186\n"
        "v_mfma_f32_16x16x32_bf16 v[72:75], v[48:51], v[196:199], v[72:75]\n"
        "s_add_u32 s66, s66, 0x80\n"
        "s_addc_u32 s67, s67, 0\n"
        "v_accvgpr_write_b32 a191, v187\n"
        "v_mfma_f32_16x16x32_bf16 v[104:107], v[48:51], v[200:203], v[104:107]\n"
        "v_accvgpr_read_b32 v185, a85\n"
        "v_accvgpr_read_b32 v186, a86\n"
        "v_mfma_f32_16x16x32_bf16 v[104:107], v[52:55], v[216:219], v[104:107]\n"
        "v_accvgpr_read_b32 v187, a87\n"
        "v_accvgpr_read_b32 v188, a116\n"
        "v_mfma_f32_16x16x32_bf16 v[108:111], v[60:63], v[216:219], v[108:111]\n"
        "v_accvgpr_read_b32 v189, a117\n"
        "v_accvgpr_read_b32 v190, a118\n"
        "v_mfma_f32_16x16x32_bf16 v[108:111], v[56:59], v[200:203], v[108:111]\n"
        "v_accvgpr_read_b32 v191, a119\n"
        "v_mfma_f32_16x16x32_bf16 v[140:143], v[56:59], v[204:207], v[140:143]\n"
        "v_mfma_f32_16x16x32_bf16 v[140:143], v[60:63], v[220:223], v[140:143]\n"
        "v_mfma_f32_16x16x32_bf16 v[136:139], v[52:55], v[220:223], v[136:139]\n"
        "s_waitcnt vmcnt(13) lgkmcnt(0)\n"
        "s_barrier\n"
        "v_mfma_f32_16x16x32_bf16 v[136:139], v[48:51], v[204:207], v[136:139]\n"
        "global_load_lds_dwordx4 v238, s[68:69] offset:0\n"
        "ds_read_b64_tr_b16 v[48:49], v226 offset:16384\n"
        "v_mfma_f32_16x16x32_bf16 v[16:19], v[160:163], v[192:195], v[16:19]\n"
        "ds_read_b64_tr_b16 v[50:51], v227 offset:16384\n"
        "v_mfma_f32_16x16x32_bf16 v[16:19], v[164:167], v[208:211], v[16:19]\n"
        "ds_read_b64_tr_b16 v[52:53], v226 offset:24576\n"
        "v_mfma_f32_16x16x32_bf16 v[20:23], v[172:175], v[208:211], v[20:23]\n"
        "ds_read_b64_tr_b16 v[54:55], v227 offset:24576\n"
        "v_mfma_f32_16x16x32_bf16 v[20:23], v[168:171], v[192:195], v[20:23]\n"
        "ds_read_b64_tr_b16 v[60:61], v228 offset:24576\n"
        "global_load_lds_dwordx4 v239, s[68:69] offset:1024\n"
        "v_mfma_f32_16x16x32_bf16 v[84:87], v[168:171], v[196:199], v[84:87]\n"
        "ds_read_b64_tr_b16 v[62:63], v229 offset:24576\n"
        "v_mfma_f32_16x16x32_bf16 v[84:87], v[172:175], v[212:215], v[84:87]\n"
        "ds_read_b64_tr_b16 v[56:57], v228 offset:16384\n"
        "v_mfma_f32_16x16x32_bf16 v[80:83], v[164:167], v[212:215], v[80:83]\n"
        "ds_read_b64_tr_b16 v[58:59], v229 offset:16384\n"
        "v_mfma_f32_16x16x32_bf16 v[80:83], v[160:163], v[196:199], v[80:83]\n"
        "v_accvgpr_read_b32 v176, a80\n"
        "v_cvt_pk_bf16_f32 v176, v176, v177\n"
        "v_mfma_f32_16x16x32_bf16 v[112:115], v[160:163], v[200:203], v[112:115]\n"
        "global_load_lds_dwordx4 v240, s[68:69] offset:2048\n"
        "v_cvt_pk_bf16_f32 v177, v178, v179\n"
        "v_mfma_f32_16x16x32_bf16 v[112:115], v[164:167], v[216:219], v[112:115]\n"
        "v_cvt_pk_bf16_f32 v178, v180, v181\n"
        "v_cvt_pk_bf16_f32 v179, v182, v183\n"
        "v_mfma_f32_16x16x32_bf16 v[116:119], v[172:175], v[216:219], v[116:119]\n"
        "v_accvgpr_write_b32 a16, v176\n"
        "v_accvgpr_write_b32 a17, v177\n"
        "v_mfma_f32_16x16x32_bf16 v[116:119], v[168:171], v[200:203], v[116:119]\n"
        "v_accvgpr_write_b32 a18, v178\n"
        "v_accvgpr_write_b32 a19, v179\n"
        "v_mfma_f32_16x16x32_bf16 v[148:151], v[168:171], v[204:207], v[148:151]\n"
        "global_load_lds_dwordx4 v241, s[68:69] offset:3072\n"
        "ds_read_b64_tr_b16 v[168:169], v232 offset:16384\n"
        "s_add_u32 m0, s72, 0x10000\n"
        "v_mfma_f32_16x16x32_bf16 v[148:151], v[172:175], v[220:223], v[148:151]\n"
        "ds_read_b64_tr_b16 v[172:173], v232 offset:24576\n"
        "s_add_u32 s68, s68, 0x80000\n"
        "s_addc_u32 s69, s69, 0\n"
        "v_mfma_f32_16x16x32_bf16 v[144:147], v[164:167], v[220:223], v[144:147]\n"
        "ds_read_b64_tr_b16 v[164:165], v230 offset:24576\n"
        "v_accvgpr_read_b32 v177, a89\n"
        "v_mfma_f32_16x16x32_bf16 v[144:147], v[160:163], v[204:207], v[144:147]\n"
        "ds_read_b64_tr_b16 v[160:161], v230 offset:16384\n"
        "v_accvgpr_read_b32 v178, a90\n"
        "v_mfma_f32_16x16x32_bf16 v[24:27], v[32:35], v[192:195], v[24:27]\n"
        "ds_read_b64_tr_b16 v[162:163], v231 offset:16384\n"
        "global_load_lds_dwordx4 v242, s[64:65] offset:0\n"
        "v_mfma_f32_16x16x32_bf16 v[24:27], v[36:39], v[208:211], v[24:27]\n"
        "ds_read_b64_tr_b16 v[166:167], v231 offset:24576\n"
        "v_accvgpr_read_b32 v179, a91\n"
        "v_mfma_f32_16x16x32_bf16 v[28:31], v[44:47], v[208:211], v[28:31]\n"
        "ds_read_b64_tr_b16 v[174:175], v233 offset:24576\n"
        "v_accvgpr_read_b32 v180, a120\n"
        "v_mfma_f32_16x16x32_bf16 v[28:31], v[40:43], v[192:195], v[28:31]\n"
        "ds_read_b64_tr_b16 v[170:171], v233 offset:16384\n"
        "v_accvgpr_read_b32 v181, a121\n"
        "v_mfma_f32_16x16x32_bf16 v[92:95], v[40:43], v[196:199], v[92:95]\n"
        "ds_read_b128 v[192:195], v246 offset:34816\n"
        "global_load_lds_dwordx4 v243, s[64:65] offset:1024\n"
        "v_mfma_f32_16x16x32_bf16 v[92:95], v[44:47], v[212:215], v[92:95]\n"
        "ds_read_b128 v[208:211], v247 offset:34816\n"
        "v_accvgpr_read_b32 v182, a122\n"
        "v_mfma_f32_16x16x32_bf16 v[88:91], v[36:39], v[212:215], v[88:91]\n"
        "ds_read_b128 v[212:215], v247 offset:32768\n"
        "v_accvgpr_read_b32 v183, a123\n"
        "v_mfma_f32_16x16x32_bf16 v[88:91], v[32:35], v[196:199], v[88:91]\n"
        "ds_read_b128 v[196:199], v246 offset:32768\n"
        "v_mfma_f32_16x16x32_bf16 v[120:123], v[32:35], v[200:203], v[120:123]\n"
        "global_load_lds_dwordx4 v244, s[64:65] offset:2048\n"
        "v_mfma_f32_16x16x32_bf16 v[120:123], v[36:39], v[216:219], v[120:123]\n"
        "v_mfma_f32_16x16x32_bf16 v[124:127], v[44:47], v[216:219], v[124:127]\n"
        "ds_read_b128 v[216:219], v247 offset:38912\n"
        "v_mfma_f32_16x16x32_bf16 v[124:127], v[40:43], v[200:203], v[124:127]\n"
        "ds_read_b128 v[200:203], v246 offset:38912\n"
        "s_waitcnt lgkmcnt(6)\n"
        "v_mfma_f32_16x16x32_bf16 v[156:159], v[40:43], v[204:207], v[156:159]\n"
        "global_load_lds_dwordx4 v245, s[64:65] offset:3072\n"
        "s_add_u32 m0, s72, 0x11000\n"
        "v_mfma_f32_16x16x32_bf16 v[156:159], v[44:47], v[220:223], v[156:159]\n"
        "ds_read_b64_tr_b16 v[44:45], v228 offset:25600\n"
        "s_add_u32 s64, s64, 0x80\n"
        "s_addc_u32 s65, s65, 0\n"
        "v_mfma_f32_16x16x32_bf16 v[152:155], v[36:39], v[220:223], v[152:155]\n"
        "ds_read_b128 v[220:223], v247 offset:36864\n"
        "s_waitcnt lgkmcnt(5)\n"
        "v_mfma_f32_16x16x32_bf16 v[152:155], v[32:35], v[204:207], v[152:155]\n"
        "ds_read_b128 v[204:207], v246 offset:36864\n"
        "v_accvgpr_read_b32 v184, a84\n"
        "v_mfma_f32_16x16x32_bf16 v[0:3], v[48:51], v[192:195], v[0:3]\n"
        "s_waitcnt lgkmcnt(5)\n"
        "v_cvt_pk_bf16_f32 v184, v184, v185\n"
        "v_cvt_pk_bf16_f32 v185, v186, v187\n"
        "v_mfma_f32_16x16x32_bf16 v[0:3], v[52:55], v[208:211], v[0:3]\n"
        "global_load_lds_dwordx4 v242, s[66:67] offset:0\n"
        "v_cvt_pk_bf16_f32 v186, v188, v189\n"
        "v_mfma_f32_16x16x32_bf16 v[4:7], v[60:63], v[208:211], v[4:7]\n"
        "s_waitcnt lgkmcnt(4)\n"
        "v_cvt_pk_bf16_f32 v187, v190, v191\n"
        "v_accvgpr_write_b32 a20, v184\n"
        "v_mfma_f32_16x16x32_bf16 v[4:7], v[56:59], v[192:195], v[4:7]\n"
        "s_waitcnt lgkmcnt(3)\n"
        "v_accvgpr_write_b32 a21, v185\n"
        "v_accvgpr_write_b32 a22, v186\n"
        "v_mfma_f32_16x16x32_bf16 v[68:71], v[56:59], v[196:199], v[68:71]\n"
        "ds_read_b64_tr_b16 v[32:33], v226 offset:17408\n"
        "v_accvgpr_write_b32 a23, v187\n"
        "v_mfma_f32_16x16x32_bf16 v[68:71], v[60:63], v[212:215], v[68:71]\n"
        "ds_read_b64_tr_b16 v[34:35], v227 offset:17408\n"
        "global_load_lds_dwordx4 v243, s[66:67] offset:1024\n"
        "v_mfma_f32_16x16x32_bf16 v[64:67], v[52:55], v[212:215], v[64:67]\n"
        "ds_read_b64_tr_b16 v[36:37], v226 offset:25600\n"
        "v_accvgpr_read_b32 v185, a93\n"
        "v_mfma_f32_16x16x32_bf16 v[64:67], v[48:51], v[196:199], v[64:67]\n"
        "ds_read_b64_tr_b16 v[38:39], v227 offset:25600\n"
        "v_accvgpr_read_b32 v186, a94\n"
        "v_mfma_f32_16x16x32_bf16 v[96:99], v[48:51], v[200:203], v[96:99]\n"
        "ds_read_b64_tr_b16 v[46:47], v229 offset:25600\n"
        "v_accvgpr_read_b32 v187, a95\n"
        "v_mfma_f32_16x16x32_bf16 v[96:99], v[52:55], v[216:219], v[96:99]\n"
        "ds_read_b64_tr_b16 v[40:41], v228 offset:17408\n"
        "global_load_lds_dwordx4 v244, s[66:67] offset:2048\n"
        "v_mfma_f32_16x16x32_bf16 v[100:103], v[60:63], v[216:219], v[100:103]\n"
        "ds_read_b64_tr_b16 v[42:43], v229 offset:17408\n"
        "v_accvgpr_read_b32 v188, a124\n"
        "v_mfma_f32_16x16x32_bf16 v[100:103], v[56:59], v[200:203], v[100:103]\n"
        "s_waitcnt lgkmcnt(7)\n"
        "v_accvgpr_read_b32 v189, a125\n"
        "v_accvgpr_read_b32 v190, a126\n"
        "v_mfma_f32_16x16x32_bf16 v[132:135], v[56:59], v[204:207], v[132:135]\n"
        "ds_read_b64_tr_b16 v[56:57], v232 offset:17408\n"
        "v_accvgpr_read_b32 v191, a127\n"
        "v_mfma_f32_16x16x32_bf16 v[132:135], v[60:63], v[220:223], v[132:135]\n"
        "global_load_lds_dwordx4 v245, s[66:67] offset:3072\n"
        "s_add_u32 m0, s73, 0x1c000\n"
        "v_mfma_f32_16x16x32_bf16 v[128:131], v[52:55], v[220:223], v[128:131]\n"
        "ds_read_b64_tr_b16 v[52:53], v230 offset:25600\n"
        "s_add_u32 s66, s66, 0x80\n"
        "s_addc_u32 s67, s67, 0\n"
        "v_mfma_f32_16x16x32_bf16 v[128:131], v[48:51], v[204:207], v[128:131]\n"
        "ds_read_b64_tr_b16 v[48:49], v230 offset:17408\n"
        "v_mfma_f32_16x16x32_bf16 v[8:11], v[160:163], v[192:195], v[8:11]\n"
        "ds_read_b64_tr_b16 v[50:51], v231 offset:17408\n"
        "v_mfma_f32_16x16x32_bf16 v[8:11], v[164:167], v[208:211], v[8:11]\n"
        "ds_read_b64_tr_b16 v[54:55], v231 offset:25600\n"
        "v_mfma_f32_16x16x32_bf16 v[12:15], v[172:175], v[208:211], v[12:15]\n"
        "global_store_dwordx4 v254, a[160:163], s[70:71] offset:0 sc1\n"
        "ds_read_b64_tr_b16 v[60:61], v232 offset:25600\n"
        "ds_read_b64_tr_b16 v[62:63], v233 offset:25600\n"
        "v_mfma_f32_16x16x32_bf16 v[12:15], v[168:171], v[192:195], v[12:15]\n"
        "ds_read_b64_tr_b16 v[58:59], v233 offset:17408\n"
        "v_mfma_f32_16x16x32_bf16 v[76:79], v[168:171], v[196:199], v[76:79]\n"
        "v_mfma_f32_16x16x32_bf16 v[76:79], v[172:175], v[212:215], v[76:79]\n"
        "s_waitcnt lgkmcnt(7)\n"
        "v_mfma_f32_16x16x32_bf16 v[72:75], v[164:167], v[212:215], v[72:75]\n"
        "v_mfma_f32_16x16x32_bf16 v[72:75], v[160:163], v[196:199], v[72:75]\n"
        "v_accvgpr_read_b32 v176, a88\n"
        "v_cvt_pk_bf16_f32 v176, v176, v177\n"
        "v_mfma_f32_16x16x32_bf16 v[104:107], v[160:163], v[200:203], v[104:107]\n"
        "s_lshl_b32 s90, s43, 19\n"
        "v_cvt_pk_bf16_f32 v177, v178, v179\n"
        "v_cvt_pk_bf16_f32 v178, v180, v181\n"
        "v_mfma_f32_16x16x32_bf16 v[104:107], v[164:167], v[216:219], v[104:107]\n"
        "s_add_u32 s90, s90, s72\n"
        "v_cvt_pk_bf16_f32 v179, v182, v183\n"
        "v_accvgpr_write_b32 a24, v176\n"
        "v_mfma_f32_16x16x32_bf16 v[108:111], v[172:175], v[216:219], v[108:111]\n"
        "s_and_b32 s78, s45, 1\n"
        "v_accvgpr_write_b32 a25, v177\n"
        "v_accvgpr_write_b32 a26, v178\n"
        "v_mfma_f32_16x16x32_bf16 v[108:111], v[168:171], v[200:203], v[108:111]\n"
        "s_waitcnt vmcnt(13) lgkmcnt(0)\n"
        "v_accvgpr_write_b32 a27, v179\n"
        "s_barrier\n"
        "v_mfma_f32_16x16x32_bf16 v[140:143], v[168:171], v[204:207], v[140:143]\n"
        "global_load_lds_dwordx4 v238, s[68:69] offset:0\n"
        "ds_read_b64_tr_b16 v[168:169], v228 offset:32768\n"
        "s_lshl_b32 s78, s78, 15\n"
        "v_mfma_f32_16x16x32_bf16 v[140:143], v[172:175], v[220:223], v[140:143]\n"
        "ds_read_b64_tr_b16 v[172:173], v228 offset:40960\n"
        "s_add_u32 s78, s78, 0x10000\n"
        "s_and_b32 s80, s40, 1\n"
        "v_mfma_f32_16x16x32_bf16 v[136:139], v[164:167], v[220:223], v[136:139]\n"
        "ds_read_b64_tr_b16 v[164:165], v226 offset:40960\n"
        "s_lshl_b32 s80, s80, 11\n"
        "s_lshl_b32 s82, s40, 4\n"
        "v_mfma_f32_16x16x32_bf16 v[136:139], v[160:163], v[204:207], v[136:139]\n"
        "ds_read_b64_tr_b16 v[160:161], v226 offset:32768\n"
        "s_sub_u32 s83, 14, s44\n"
        "v_accvgpr_write_b32 a48, 0\n"
        "v_mfma_f32_16x16x32_bf16 v[16:19], v[32:35], v[192:195], v[16:19]\n"
        "ds_read_b64_tr_b16 v[162:163], v227 offset:32768\n"
        "global_load_lds_dwordx4 v239, s[68:69] offset:1024\n"
        "s_lshl_b32 s50, s41, 7\n"
        "v_mfma_f32_16x16x32_bf16 v[16:19], v[36:39], v[208:211], v[16:19]\n"
        "ds_read_b64_tr_b16 v[166:167], v227 offset:40960\n"
        "s_lshl2_add_u32 s50, s42, s50\n"
        "v_accvgpr_write_b32 a49, 0\n"
        "v_mfma_f32_16x16x32_bf16 v[20:23], v[44:47], v[208:211], v[20:23]\n"
        "ds_read_b64_tr_b16 v[174:175], v229 offset:40960\n"
        "s_add_u32 s50, s50, s40\n"
        "v_accvgpr_write_b32 a50, 0\n"
        "v_mfma_f32_16x16x32_bf16 v[20:23], v[40:43], v[192:195], v[20:23]\n"
        "ds_read_b64_tr_b16 v[170:171], v229 offset:32768\n"
        "s_lshl_b32 s50, s50, 12\n"
        "v_accvgpr_write_b32 a51, 0\n"
        "v_mfma_f32_16x16x32_bf16 v[84:87], v[40:43], v[196:199], v[84:87]\n"
        "global_load_lds_dwordx4 v240, s[68:69] offset:2048\n"
        "s_mul_i32 s51, s40, 8320\n"
        "v_mfma_f32_16x16x32_bf16 v[84:87], v[44:47], v[212:215], v[84:87]\n"
        "s_add_u32 s92, s26, s50\n"
        "s_addc_u32 s93, s27, 0\n"
        "v_accvgpr_write_b32 a52, 0\n"
        "v_mfma_f32_16x16x32_bf16 v[80:83], v[36:39], v[212:215], v[80:83]\n"
        "s_add_u32 s50, s50, 0x3000000\n"
        "v_accvgpr_write_b32 a53, 0\n"
        "v_accvgpr_write_b32 a54, 0\n"
        "v_mfma_f32_16x16x32_bf16 v[80:83], v[32:35], v[196:199], v[80:83]\n"
        "s_add_u32 s94, s38, s50\n"
        "s_addc_u32 s95, s39, 0\n"
        "v_accvgpr_write_b32 a55, 0\n"
        "v_mfma_f32_16x16x32_bf16 v[112:115], v[32:35], v[200:203], v[112:115]\n"
        "global_load_lds_dwordx4 v241, s[68:69] offset:3072\n"
        "s_add_u32 m0, s72, 0x8000\n"
        "v_mfma_f32_16x16x32_bf16 v[112:115], v[36:39], v[216:219], v[112:115]\n"
        "s_add_u32 s68, s68, 0x80000\n"
        "s_addc_u32 s69, s69, 0\n"
        "v_accvgpr_write_b32 a56, 0\n"
        "v_mfma_f32_16x16x32_bf16 v[116:119], v[44:47], v[216:219], v[116:119]\n"
        "s_add_u32 s51, s51, 0x18000\n"
        "v_accvgpr_write_b32 a57, 0\n"
        "v_accvgpr_write_b32 a58, 0\n"
        "v_mfma_f32_16x16x32_bf16 v[116:119], v[40:43], v[200:203], v[116:119]\n"
        "s_add_u32 s50, s51, 4128\n"
        "v_accvgpr_write_b32 a59, 0\n"
        "v_accvgpr_write_b32 a60, 0\n"
        "v_mfma_f32_16x16x32_bf16 v[148:151], v[40:43], v[204:207], v[148:151]\n"
        "global_load_lds_dwordx4 v242, s[64:65] offset:0\n"
        "v_mfma_f32_16x16x32_bf16 v[148:151], v[44:47], v[220:223], v[148:151]\n"
        "ds_read_b64_tr_b16 v[44:45], v232 offset:40960\n"
        "v_accvgpr_write_b32 a61, 0\n"
        "v_mfma_f32_16x16x32_bf16 v[144:147], v[36:39], v[220:223], v[144:147]\n"
        "ds_read_b64_tr_b16 v[36:37], v230 offset:40960\n"
        "v_accvgpr_write_b32 a62, 0\n"
        "v_mfma_f32_16x16x32_bf16 v[144:147], v[32:35], v[204:207], v[144:147]\n"
        "ds_read_b64_tr_b16 v[32:33], v230 offset:32768\n"
        "v_accvgpr_read_b32 v184, a92\n"
        "v_mfma_f32_16x16x32_bf16 v[24:27], v[48:51], v[192:195], v[24:27]\n"
        "ds_read_b64_tr_b16 v[34:35], v231 offset:32768\n"
        "v_cvt_pk_bf16_f32 v184, v184, v185\n"
        "v_mfma_f32_16x16x32_bf16 v[24:27], v[52:55], v[208:211], v[24:27]\n"
        "ds_read_b64_tr_b16 v[38:39], v231 offset:40960\n"
        "v_cvt_pk_bf16_f32 v185, v186, v187\n"
        "v_mfma_f32_16x16x32_bf16 v[28:31], v[60:63], v[208:211], v[28:31]\n"
        "ds_read_b64_tr_b16 v[46:47], v233 offset:40960\n"
        "v_cvt_pk_bf16_f32 v186, v188, v189\n"
        "v_mfma_f32_16x16x32_bf16 v[28:31], v[56:59], v[192:195], v[28:31]\n"
        "ds_read_b64_tr_b16 v[40:41], v232 offset:32768\n"
        "v_cvt_pk_bf16_f32 v187, v190, v191\n"
        "v_mfma_f32_16x16x32_bf16 v[92:95], v[56:59], v[196:199], v[92:95]\n"
        "ds_read_b64_tr_b16 v[42:43], v233 offset:32768\n"
        "v_accvgpr_write_b32 a28, v184\n"
        "v_mfma_f32_16x16x32_bf16 v[92:95], v[60:63], v[212:215], v[92:95]\n"
        "ds_read_b128 v[192:195], v246 offset:2048\n"
        "v_accvgpr_write_b32 a29, v185\n"
        "v_mfma_f32_16x16x32_bf16 v[88:91], v[52:55], v[212:215], v[88:91]\n"
        "ds_read_b128 v[208:211], v247 offset:2048\n"
        "v_accvgpr_write_b32 a30, v186\n"
        "v_mfma_f32_16x16x32_bf16 v[88:91], v[48:51], v[196:199], v[88:91]\n"
        "global_load_lds_dwordx4 v243, s[64:65] offset:1024\n"
        "v_accvgpr_write_b32 a31, v187\n"
        "v_mfma_f32_16x16x32_bf16 v[120:123], v[48:51], v[200:203], v[120:123]\n"
        "ds_read_b128 v[196:199], v246 offset:0\n"
        "v_accvgpr_write_b32 a63, 0\n"
        "v_mfma_f32_16x16x32_bf16 v[120:123], v[52:55], v[216:219], v[120:123]\n"
        "ds_read_b128 v[212:215], v247 offset:0\n"
        "v_mfma_f32_16x16x32_bf16 v[124:127], v[60:63], v[216:219], v[124:127]\n"
        "ds_read_b128 v[216:219], v247 offset:6144\n"
        "v_mfma_f32_16x16x32_bf16 v[124:127], v[56:59], v[200:203], v[124:127]\n"
        "global_load_lds_dwordx4 v244, s[64:65] offset:2048\n"
        "ds_read_b128 v[200:203], v246 offset:6144\n"
        "v_mfma_f32_16x16x32_bf16 v[156:159], v[56:59], v[204:207], v[156:159]\n"
        "ds_read_b64_tr_b16 v[56:57], v228 offset:33792\n"
        "s_waitcnt lgkmcnt(7)\n"
        "v_mfma_f32_16x16x32_bf16 v[156:159], v[60:63], v[220:223], v[156:159]\n"
        "ds_read_b64_tr_b16 v[60:61], v228 offset:41984\n"
        "s_waitcnt lgkmcnt(7)\n"
        "v_mfma_f32_16x16x32_bf16 v[152:155], v[52:55], v[220:223], v[152:155]\n"
        "ds_read_b128 v[220:223], v247 offset:4096\n"
        "s_waitcnt lgkmcnt(7)\n"
        "v_mfma_f32_16x16x32_bf16 v[152:155], v[48:51], v[204:207], v[152:155]\n"
        "global_load_lds_dwordx4 v245, s[64:65] offset:3072\n"
        "ds_read_b128 v[204:207], v246 offset:4096\n"
        "s_add_u32 m0, s72, 0x9000\n"
        "v_mfma_f32_16x16x32_bf16 v[0:3], v[160:163], v[192:195], v[0:3]\n"
        "ds_read_b64_tr_b16 v[48:49], v226 offset:33792\n"
        "s_add_u32 s64, s64, 0x80\n"
        "s_addc_u32 s65, s65, 0\n"
        "v_mfma_f32_16x16x32_bf16 v[0:3], v[164:167], v[208:211], v[0:3]\n"
        "ds_read_b64_tr_b16 v[50:51], v227 offset:33792\n"
        "s_waitcnt lgkmcnt(8)\n"
        "v_mfma_f32_16x16x32_bf16 v[4:7], v[172:175], v[208:211], v[4:7]\n"
        "ds_read_b64_tr_b16 v[52:53], v226 offset:41984\n"
        "s_waitcnt lgkmcnt(8)\n"
        "v_mfma_f32_16x16x32_bf16 v[4:7], v[168:171], v[192:195], v[4:7]\n"
        "ds_read_b64_tr_b16 v[54:55], v227 offset:41984\n"
        "global_load_lds_dwordx4 v242, s[66:67] offset:0\n"
        "v_mfma_f32_16x16x32_bf16 v[68:71], v[168:171], v[196:199], v[68:71]\n"
        "ds_read_b64_tr_b16 v[62:63], v229 offset:41984\n"
        "s_waitcnt lgkmcnt(8)\n"
        "v_mfma_f32_16x16x32_bf16 v[68:71], v[172:175], v[212:215], v[68:71]\n"
        "ds_read_b64_tr_b16 v[58:59], v229 offset:33792\n"
        "s_waitcnt lgkmcnt(8)\n"
        "v_mfma_f32_16x16x32_bf16 v[64:67], v[164:167], v[212:215], v[64:67]\n"
        "s_waitcnt lgkmcnt(7)\n"
        "v_mfma_f32_16x16x32_bf16 v[64:67], v[160:163], v[196:199], v[64:67]\n"
        "global_load_lds_dwordx4 v243, s[66:67] offset:1024\n"
        "v_mfma_f32_16x16x32_bf16 v[96:99], v[160:163], v[200:203], v[96:99]\n"
        "s_waitcnt lgkmcnt(5)\n"
        "v_mfma_f32_16x16x32_bf16 v[96:99], v[164:167], v[216:219], v[96:99]\n"
        "v_mfma_f32_16x16x32_bf16 v[100:103], v[172:175], v[216:219], v[100:103]\n"
        "v_mfma_f32_16x16x32_bf16 v[100:103], v[168:171], v[200:203], v[100:103]\n"
        "global_load_lds_dwordx4 v244, s[66:67] offset:2048\n"
        "v_mfma_f32_16x16x32_bf16 v[132:135], v[168:171], v[204:207], v[132:135]\n"
        "ds_read_b64_tr_b16 v[168:169], v232 offset:33792\n"
        "v_mfma_f32_16x16x32_bf16 v[132:135], v[172:175], v[220:223], v[132:135]\n"
        "ds_read_b64_tr_b16 v[172:173], v232 offset:41984\n"
        "v_mfma_f32_16x16x32_bf16 v[128:131], v[164:167], v[220:223], v[128:131]\n"
        "ds_read_b64_tr_b16 v[164:165], v230 offset:41984\n"
        "v_mfma_f32_16x16x32_bf16 v[128:131], v[160:163], v[204:207], v[128:131]\n"
        "global_load_lds_dwordx4 v245, s[66:67] offset:3072\n"
        "s_add_u32 m0, s73, 0x20000\n"
        "v_mfma_f32_16x16x32_bf16 v[8:11], v[32:35], v[192:195], v[8:11]\n"
        "ds_read_b64_tr_b16 v[160:161], v230 offset:33792\n"
        "s_add_u32 s66, s66, 0x80\n"
        "s_addc_u32 s67, s67, 0\n"
        "v_mfma_f32_16x16x32_bf16 v[8:11], v[36:39], v[208:211], v[8:11]\n"
        "ds_read_b64_tr_b16 v[162:163], v231 offset:33792\n"
        "v_mfma_f32_16x16x32_bf16 v[12:15], v[44:47], v[208:211], v[12:15]\n"
        "ds_read_b64_tr_b16 v[166:167], v231 offset:41984\n"
        "v_mfma_f32_16x16x32_bf16 v[12:15], v[40:43], v[192:195], v[12:15]\n"
        "ds_read_b64_tr_b16 v[174:175], v233 offset:41984\n"
        "v_mfma_f32_16x16x32_bf16 v[76:79], v[40:43], v[196:199], v[76:79]\n"
        "ds_read_b64_tr_b16 v[170:171], v233 offset:33792\n"
        "v_mfma_f32_16x16x32_bf16 v[76:79], v[44:47], v[212:215], v[76:79]\n"
        "s_waitcnt lgkmcnt(6)\n"
        "v_mfma_f32_16x16x32_bf16 v[72:75], v[36:39], v[212:215], v[72:75]\n"
        "v_mfma_f32_16x16x32_bf16 v[72:75], v[32:35], v[196:199], v[72:75]\n"
        "s_mov_b32 s79, s72\n"
        "s_xor_b32 s79, s79, 0x8000\n"
        "v_mfma_f32_16x16x32_bf16 v[104:107], v[32:35], v[200:203], v[104:107]\n"
        "v_mfma_f32_16x16x32_bf16 v[104:107], v[36:39], v[216:219], v[104:107]\n"
        "v_mfma_f32_16x16x32_bf16 v[108:111], v[44:47], v[216:219], v[108:111]\n"
        "v_mfma_f32_16x16x32_bf16 v[108:111], v[40:43], v[200:203], v[108:111]\n"
        "v_mfma_f32_16x16x32_bf16 v[140:143], v[40:43], v[204:207], v[140:143]\n"
        "s_waitcnt vmcnt(13) lgkmcnt(0)\n"
        "s_barrier\n"
        "v_mfma_f32_16x16x32_bf16 v[140:143], v[44:47], v[220:223], v[140:143]\n"
        "global_load_lds_dwordx4 v238, s[68:69] offset:0\n"
        "ds_read_b64_tr_b16 v[44:45], v228 offset:57344\n"
        "v_mfma_f32_16x16x32_bf16 v[136:139], v[36:39], v[220:223], v[136:139]\n"
        "ds_read_b64_tr_b16 v[36:37], v226 offset:57344\n"
        "v_mfma_f32_16x16x32_bf16 v[136:139], v[32:35], v[204:207], v[136:139]\n"
        "ds_read_b64_tr_b16 v[32:33], v226 offset:49152\n"
        "v_mfma_f32_16x16x32_bf16 v[16:19], v[48:51], v[192:195], v[16:19]\n"
        "ds_read_b64_tr_b16 v[34:35], v227 offset:49152\n"
        "v_mfma_f32_16x16x32_bf16 v[16:19], v[52:55], v[208:211], v[16:19]\n"
        "global_store_dwordx4 v254, a[164:167], s[70:71] offset:1024 sc1\n"
        "ds_read_b64_tr_b16 v[38:39], v227 offset:57344\n"
        "ds_read_b64_tr_b16 v[46:47], v229 offset:57344\n"
        "s_add_u32 s70, s38, s97\n"
        "s_addc_u32 s71, s39, 0\n"
        "v_mfma_f32_16x16x32_bf16 v[20:23], v[60:63], v[208:211], v[20:23]\n"
        "ds_read_b64_tr_b16 v[40:41], v228 offset:49152\n"
        "s_add_u32 s97, s52, 0x3000\n"
        "v_mfma_f32_16x16x32_bf16 v[20:23], v[56:59], v[192:195], v[20:23]\n"
        "ds_read_b64_tr_b16 v[42:43], v229 offset:49152\n"
        "v_mfma_f32_16x16x32_bf16 v[84:87], v[56:59], v[196:199], v[84:87]\n"
        "global_load_lds_dwordx4 v239, s[68:69] offset:1024\n"
        "v_mfma_f32_16x16x32_bf16 v[84:87], v[60:63], v[212:215], v[84:87]\n"
        "v_mfma_f32_16x16x32_bf16 v[80:83], v[52:55], v[212:215], v[80:83]\n"
        "v_mfma_f32_16x16x32_bf16 v[80:83], v[48:51], v[196:199], v[80:83]\n"
        "v_mfma_f32_16x16x32_bf16 v[112:115], v[48:51], v[200:203], v[112:115]\n"
        "global_load_lds_dwordx4 v240, s[68:69] offset:2048\n"
        "v_mfma_f32_16x16x32_bf16 v[112:115], v[52:55], v[216:219], v[112:115]\n"
        "v_add_u32 v240, s78, v254\n"
        "s_xor_b32 s78, s78, 0x8000\n"
        "v_mfma_f32_16x16x32_bf16 v[116:119], v[60:63], v[216:219], v[116:119]\n"
        "v_mfma_f32_16x16x32_bf16 v[116:119], v[56:59], v[200:203], v[116:119]\n"
        "v_mfma_f32_16x16x32_bf16 v[148:151], v[56:59], v[204:207], v[148:151]\n"
        "global_load_lds_dwordx4 v241, s[68:69] offset:3072\n"
        "s_add_u32 m0, s72, 0x0\n"
        "v_mfma_f32_16x16x32_bf16 v[148:151], v[60:63], v[220:223], v[148:151]\n"
        "ds_read_b64_tr_b16 v[60:61], v232 offset:57344\n"
        "v_mov_b32 v241, v254\n"
        "v_mfma_f32_16x16x32_bf16 v[144:147], v[52:55], v[220:223], v[144:147]\n"
        "LHA9_%=:\n"
        "s_cbranch_execz LHA13_%=\n"
        "ds_read_b64_tr_b16 v[52:53], v230 offset:57344\n"
        "v_mfma_f32_16x16x32_bf16 v[144:147], v[48:51], v[204:207], v[144:147]\n"
        "ds_read_b64_tr_b16 v[48:49], v230 offset:49152\n"
        "v_mfma_f32_16x16x32_bf16 v[24:27], v[160:163], v[192:195], v[24:27]\n"
        "ds_read_b64_tr_b16 v[50:51], v231 offset:49152\n"
        "global_load_lds_dwordx4 v242, s[64:65] offset:0\n"
        "v_mfma_f32_16x16x32_bf16 v[24:27], v[164:167], v[208:211], v[24:27]\n"
        "ds_read_b64_tr_b16 v[54:55], v231 offset:57344\n"
        "v_mfma_f32_16x16x32_bf16 v[28:31], v[172:175], v[208:211], v[28:31]\n"
        "ds_read_b64_tr_b16 v[62:63], v233 offset:57344\n"
        "v_mfma_f32_16x16x32_bf16 v[28:31], v[168:171], v[192:195], v[28:31]\n"
        "ds_read_b64_tr_b16 v[56:57], v232 offset:49152\n"
        "v_mfma_f32_16x16x32_bf16 v[92:95], v[168:171], v[196:199], v[92:95]\n"
        "ds_read_b64_tr_b16 v[58:59], v233 offset:49152\n"
        "global_load_lds_dwordx4 v243, s[64:65] offset:1024\n"
        "v_mfma_f32_16x16x32_bf16 v[92:95], v[172:175], v[212:215], v[92:95]\n"
        "ds_read_b128 v[192:195], v248 offset:2048\n"
        "v_mfma_f32_16x16x32_bf16 v[88:91], v[164:167], v[212:215], v[88:91]\n"
        "ds_read_b128 v[208:211], v249 offset:2048\n"
        "v_mfma_f32_16x16x32_bf16 v[88:91], v[160:163], v[196:199], v[88:91]\n"
        "ds_read_b128 v[196:199], v248 offset:0\n"
        "v_mfma_f32_16x16x32_bf16 v[120:123], v[160:163], v[200:203], v[120:123]\n"
        "ds_read_b128 v[212:215], v249 offset:0\n"
        "global_load_lds_dwordx4 v244, s[64:65] offset:2048\n"
        "v_mfma_f32_16x16x32_bf16 v[120:123], v[164:167], v[216:219], v[120:123]\n"
        "v_mfma_f32_16x16x32_bf16 v[124:127], v[172:175], v[216:219], v[124:127]\n"
        "ds_read_b128 v[216:219], v249 offset:6144\n"
        "v_mfma_f32_16x16x32_bf16 v[124:127], v[168:171], v[200:203], v[124:127]\n"
        "ds_read_b128 v[200:203], v248 offset:6144\n"
        "v_mfma_f32_16x16x32_bf16 v[156:159], v[168:171], v[204:207], v[156:159]\n"
        "global_load_lds_dwordx4 v245, s[64:65] offset:3072\n"
        "s_add_u32 m0, s72, 0x1000\n"
        "v_mfma_f32_16x16x32_bf16 v[156:159], v[172:175], v[220:223], v[156:159]\n"
        "ds_read_b64_tr_b16 v[172:173], v228 offset:58368\n"
        "s_add_u32 s64, s38, s90\n"
        "s_addc_u32 s65, s39, 0\n"
        "v_mfma_f32_16x16x32_bf16 v[152:155], v[164:167], v[220:223], v[152:155]\n"
        "ds_read_b128 v[220:223], v249 offset:4096\n"
        "s_lshl_b32 s90, s40, 14\n"
        "s_and_b32 s90, s90, 0x8000\n"
        "v_mfma_f32_16x16x32_bf16 v[152:155], v[160:163], v[204:207], v[152:155]\n"
        "ds_read_b128 v[204:207], v248 offset:4096\n"
        "s_xor_b32 s91, s78, s90\n"
        "s_waitcnt lgkmcnt(6)\n"
        "v_mfma_f32_16x16x32_bf16 v[0:3], v[32:35], v[192:195], v[0:3]\n"
        "ds_read_b64_tr_b16 v[160:161], v226 offset:50176\n"
        "global_load_lds_dwordx4 v242, s[66:67] offset:0\n"
        "s_add_u32 s91, s91, s80\n"
        "v_mfma_f32_16x16x32_bf16 v[0:3], v[36:39], v[208:211], v[0:3]\n"
        "ds_read_b64_tr_b16 v[162:163], v227 offset:50176\n"
        "s_add_u32 s78, s78, s72\n"
        "v_lshrrev_b32 v242, 6, v254\n"
        "v_mfma_f32_16x16x32_bf16 v[4:7], v[44:47], v[208:211], v[4:7]\n"
        "ds_read_b64_tr_b16 v[164:165], v226 offset:58368\n"
        "s_xor_b32 s78, s78, 0x8000\n"
        "v_and_b32 v242, 12, v242\n"
        "v_mfma_f32_16x16x32_bf16 v[4:7], v[40:43], v[192:195], v[4:7]\n"
        "ds_read_b64_tr_b16 v[166:167], v227 offset:58368\n"
        "v_xor_b32 v241, 0x8000, v241\n"
        "v_mfma_f32_16x16x32_bf16 v[68:71], v[40:43], v[196:199], v[68:71]\n"
        "ds_read_b64_tr_b16 v[174:175], v229 offset:58368\n"
        "s_waitcnt lgkmcnt(8)\n"
        "v_mfma_f32_16x16x32_bf16 v[68:71], v[44:47], v[212:215], v[68:71]\n"
        "ds_read_b64_tr_b16 v[168:169], v228 offset:50176\n"
        "global_load_lds_dwordx4 v243, s[66:67] offset:1024\n"
        "v_mfma_f32_16x16x32_bf16 v[64:67], v[36:39], v[212:215], v[64:67]\n"
        "ds_read_b64_tr_b16 v[170:171], v229 offset:50176\n"
        "v_bfe_u32 v243, v254, 4, 4\n"
        "v_mfma_f32_16x16x32_bf16 v[64:67], v[32:35], v[196:199], v[64:67]\n"
        "s_waitcnt lgkmcnt(7)\n"
        "v_sub_u32 v243, v243, v242\n"
        "v_add_u32 v243, s82, v243\n"
        "v_mfma_f32_16x16x32_bf16 v[96:99], v[32:35], v[200:203], v[96:99]\n"
        "s_mov_b32 s82, -1\n"
        "v_cmp_gt_i32 s[10:11], 0, v243\n"
        "v_cmp_gt_i32 s[12:13], 1, v243\n"
        "v_mfma_f32_16x16x32_bf16 v[96:99], v[36:39], v[216:219], v[96:99]\n"
        "global_load_lds_dwordx4 v244, s[66:67] offset:2048\n"
        "s_add_u32 s82, s82, 1\n"
        "v_mfma_f32_16x16x32_bf16 v[100:103], v[44:47], v[216:219], v[100:103]\n"
        "v_add_u32 v244, s91, v254\n"
        "s_lshl1_add_u32 s91, s80, s90\n"
        "v_mfma_f32_16x16x32_bf16 v[100:103], v[40:43], v[200:203], v[100:103]\n"
        "s_cmp_ge_u32 s82, s44\n"
        "s_addc_u32 s90, s82, 0\n"
        "v_cmp_gt_i32 s[14:15], 2, v243\n"
        "v_mfma_f32_16x16x32_bf16 v[132:135], v[40:43], v[204:207], v[132:135]\n"
        "ds_read_b64_tr_b16 v[40:41], v232 offset:50176\n"
        "s_min_u32 s90, s90, 15\n"
        "v_cmp_gt_i32 s[16:17], 3, v243\n"
        "v_mfma_f32_16x16x32_bf16 v[132:135], v[44:47], v[220:223], v[132:135]\n"
        "global_load_lds_dwordx4 v245, s[66:67] offset:3072\n"
        "s_lshl_b32 s90, s90, 15\n"
        "v_mfma_f32_16x16x32_bf16 v[128:131], v[36:39], v[220:223], v[128:131]\n"
        "ds_read_b64_tr_b16 v[36:37], v230 offset:58368\n"
        "s_add_u32 s66, s64, 0x1000000\n"
        "s_addc_u32 s67, s65, 0\n"
        "v_mfma_f32_16x16x32_bf16 v[128:131], v[32:35], v[204:207], v[128:131]\n"
        "ds_read_b64_tr_b16 v[32:33], v230 offset:50176\n"
        "v_add_u32 v245, s91, v254\n"
        "v_mfma_f32_16x16x32_bf16 v[8:11], v[48:51], v[192:195], v[8:11]\n"
        "ds_read_b64_tr_b16 v[34:35], v231 offset:50176\n"
        "v_cmp_gt_i32 s[20:21], 16, v243\n"
        "v_mfma_f32_16x16x32_bf16 v[8:11], v[52:55], v[208:211], v[8:11]\n"
        "ds_read_b64_tr_b16 v[38:39], v231 offset:58368\n"
        "v_cmp_gt_i32 s[22:23], 17, v243\n"
        "v_mfma_f32_16x16x32_bf16 v[12:15], v[60:63], v[208:211], v[12:15]\n"
        "ds_read_b64_tr_b16 v[44:45], v232 offset:58368\n"
        "v_cmp_gt_i32 s[24:25], 18, v243\n"
        "v_mfma_f32_16x16x32_bf16 v[12:15], v[56:59], v[192:195], v[12:15]\n"
        "ds_read_b64_tr_b16 v[46:47], v233 offset:58368\n"
        "v_cmp_gt_i32 s[28:29], 19, v243\n"
        "v_mfma_f32_16x16x32_bf16 v[76:79], v[56:59], v[196:199], v[76:79]\n"
        "ds_read_b64_tr_b16 v[42:43], v233 offset:50176\n"
        "v_cmp_gt_i32 s[30:31], 32, v243\n"
        "v_mfma_f32_16x16x32_bf16 v[76:79], v[60:63], v[212:215], v[76:79]\n"
        "global_store_dwordx4 v254, a[168:171], s[70:71] offset:0 sc1\n"
        "s_waitcnt lgkmcnt(7)\n"
        "v_cmp_gt_i32 s[56:57], 33, v243\n"
        "v_cmp_gt_i32 s[58:59], 34, v243\n"
        "v_cmp_gt_i32 s[60:61], 35, v243\n"
        "v_mfma_f32_16x16x32_bf16 v[72:75], v[52:55], v[212:215], v[72:75]\n"
        "s_add_u32 s68, s64, s90\n"
        "s_addc_u32 s69, s65, 0\n"
        "v_cmp_gt_i32 s[62:63], 48, v243\n"
        "v_mfma_f32_16x16x32_bf16 v[72:75], v[48:51], v[196:199], v[72:75]\n"
        "s_add_u32 s74, s68, 0x1000\n"
        "s_addc_u32 s75, s69, 0\n"
        "v_cmp_gt_i32 s[84:85], 49, v243\n"
        "v_mfma_f32_16x16x32_bf16 v[104:107], v[48:51], v[200:203], v[104:107]\n"
        "s_add_u32 s76, s66, s90\n"
        "s_addc_u32 s77, s67, 0\n"
        "v_cmp_gt_i32 s[86:87], 50, v243\n"
        "v_mfma_f32_16x16x32_bf16 v[104:107], v[52:55], v[216:219], v[104:107]\n"
        "v_cmp_gt_i32 s[88:89], 51, v243\n"
        "v_mov_b32 v242, 0x3f803f80\n"
        "v_mfma_f32_16x16x32_bf16 v[108:111], v[60:63], v[216:219], v[108:111]\n"
        "v_accvgpr_write_b32 a80, v242\n"
        "v_accvgpr_write_b32 a81, v242\n"
        "v_mfma_f32_16x16x32_bf16 v[108:111], v[56:59], v[200:203], v[108:111]\n"
        "s_waitcnt vmcnt(14) lgkmcnt(0)\n"
        "s_barrier\n"
        "v_mfma_f32_16x16x32_bf16 v[140:143], v[56:59], v[204:207], v[140:143]\n"
        "ds_read_b64_tr_b16 v[56:57], v228 offset:16384\n"
        "v_accvgpr_write_b32 a82, v242\n"
        "v_mfma_f32_16x16x32_bf16 v[140:143], v[60:63], v[220:223], v[140:143]\n"
        "ds_read_b64_tr_b16 v[60:61], v228 offset:24576\n"
        "v_accvgpr_write_b32 a83, v242\n"
        "v_mfma_f32_16x16x32_bf16 v[136:139], v[52:55], v[220:223], v[136:139]\n"
        "ds_read_b64_tr_b16 v[52:53], v226 offset:24576\n"
        "v_mov_b32 v242, 0xff800000\n"
        "v_mfma_f32_16x16x32_bf16 v[136:139], v[48:51], v[204:207], v[136:139]\n"
        "global_store_dwordx4 v254, a[172:175], s[70:71] offset:1024 sc1\n"
        "ds_read_b64_tr_b16 v[48:49], v226 offset:16384\n"
        "ds_read_b64_tr_b16 v[50:51], v227 offset:16384\n"
        "s_add_u32 s70, s38, s97\n"
        "s_addc_u32 s71, s39, 0\n"
        "v_mfma_f32_16x16x32_bf16 v[16:19], v[160:163], v[192:195], v[16:19]\n"
        "ds_read_b64_tr_b16 v[54:55], v227 offset:24576\n"
        "s_add_u32 s97, s52, 0x4000\n"
        "s_mul_i32 s52, s40, 4224\n"
        "v_mfma_f32_16x16x32_bf16 v[16:19], v[164:167], v[208:211], v[16:19]\n"
        "ds_read_b64_tr_b16 v[62:63], v229 offset:24576\n"
        "s_add_u32 s52, s52, 0x20240\n"
        "v_mfma_f32_16x16x32_bf16 v[20:23], v[172:175], v[208:211], v[20:23]\n"
        "ds_read_b64_tr_b16 v[58:59], v229 offset:16384\n"
        "v_mfma_f32_16x16x32_bf16 v[20:23], v[168:171], v[192:195], v[20:23]\n"
        "v_mfma_f32_16x16x32_bf16 v[84:87], v[168:171], v[196:199], v[84:87]\n"
        "v_mfma_f32_16x16x32_bf16 v[84:87], v[172:175], v[212:215], v[84:87]\n"
        "v_mfma_f32_16x16x32_bf16 v[80:83], v[164:167], v[212:215], v[80:83]\n"
        "v_mfma_f32_16x16x32_bf16 v[80:83], v[160:163], v[196:199], v[80:83]\n"
        "v_mfma_f32_16x16x32_bf16 v[112:115], v[160:163], v[200:203], v[112:115]\n"
        "v_mfma_f32_16x16x32_bf16 v[112:115], v[164:167], v[216:219], v[112:115]\n"
        "v_mfma_f32_16x16x32_bf16 v[116:119], v[172:175], v[216:219], v[116:119]\n"
        "v_mfma_f32_16x16x32_bf16 v[116:119], v[168:171], v[200:203], v[116:119]\n"
        "global_store_dwordx4 v254, a[0:3], s[70:71] offset:0 sc1\n"
        "v_mfma_f32_16x16x32_bf16 v[148:151], v[168:171], v[204:207], v[148:151]\n"
        "ds_read_b64_tr_b16 v[168:169], v232 offset:16384\n"
        "v_mfma_f32_16x16x32_bf16 v[148:151], v[172:175], v[220:223], v[148:151]\n"
        "ds_read_b64_tr_b16 v[172:173], v232 offset:24576\n"
        "v_mfma_f32_16x16x32_bf16 v[144:147], v[164:167], v[220:223], v[144:147]\n"
        "ds_read_b64_tr_b16 v[164:165], v230 offset:24576\n"
        "v_mfma_f32_16x16x32_bf16 v[144:147], v[160:163], v[204:207], v[144:147]\n"
        "ds_read_b64_tr_b16 v[160:161], v230 offset:16384\n"
        "v_mfma_f32_16x16x32_bf16 v[24:27], v[32:35], v[192:195], v[24:27]\n"
        "ds_read_b64_tr_b16 v[162:163], v231 offset:16384\n"
        "v_mfma_f32_16x16x32_bf16 v[24:27], v[36:39], v[208:211], v[24:27]\n"
        "ds_read_b64_tr_b16 v[166:167], v231 offset:24576\n"
        "v_mfma_f32_16x16x32_bf16 v[28:31], v[44:47], v[208:211], v[28:31]\n"
        "ds_read_b64_tr_b16 v[174:175], v233 offset:24576\n"
        "v_mfma_f32_16x16x32_bf16 v[28:31], v[40:43], v[192:195], v[28:31]\n"
        "ds_read_b64_tr_b16 v[170:171], v233 offset:16384\n"
        "v_mfma_f32_16x16x32_bf16 v[92:95], v[40:43], v[196:199], v[92:95]\n"
        "ds_read_b128 v[192:195], v246 offset:34816\n"
        "v_mfma_f32_16x16x32_bf16 v[92:95], v[44:47], v[212:215], v[92:95]\n"
        "ds_read_b128 v[208:211], v247 offset:34816\n"
        "v_mfma_f32_16x16x32_bf16 v[88:91], v[36:39], v[212:215], v[88:91]\n"
        "ds_read_b128 v[212:215], v247 offset:32768\n"
        "v_mfma_f32_16x16x32_bf16 v[88:91], v[32:35], v[196:199], v[88:91]\n"
        "ds_read_b128 v[196:199], v246 offset:32768\n"
        "v_mfma_f32_16x16x32_bf16 v[120:123], v[32:35], v[200:203], v[120:123]\n"
        "global_store_dwordx4 v254, a[4:7], s[70:71] offset:1024 sc1\n"
        "s_add_u32 s70, s38, s97\n"
        "s_addc_u32 s71, s39, 0\n"
        "s_add_u32 s97, s53, 0x6000\n"
        "v_mfma_f32_16x16x32_bf16 v[120:123], v[36:39], v[216:219], v[120:123]\n"
        "v_mfma_f32_16x16x32_bf16 v[124:127], v[44:47], v[216:219], v[124:127]\n"
        "ds_read_b128 v[216:219], v247 offset:38912\n"
        "s_waitcnt lgkmcnt(5)\n"
        "v_mfma_f32_16x16x32_bf16 v[124:127], v[40:43], v[200:203], v[124:127]\n"
        "ds_read_b128 v[200:203], v246 offset:38912\n"
        "s_waitcnt lgkmcnt(5)\n"
        "v_mfma_f32_16x16x32_bf16 v[156:159], v[40:43], v[204:207], v[156:159]\n"
        "ds_read_b64_tr_b16 v[40:41], v228 offset:17408\n"
        "s_waitcnt lgkmcnt(5)\n"
        "v_mfma_f32_16x16x32_bf16 v[156:159], v[44:47], v[220:223], v[156:159]\n"
        "ds_read_b64_tr_b16 v[44:45], v228 offset:25600\n"
        "s_waitcnt lgkmcnt(5)\n"
        "v_mfma_f32_16x16x32_bf16 v[152:155], v[36:39], v[220:223], v[152:155]\n"
        "ds_read_b128 v[220:223], v247 offset:36864\n"
        "s_waitcnt lgkmcnt(5)\n"
        "v_mfma_f32_16x16x32_bf16 v[152:155], v[32:35], v[204:207], v[152:155]\n"
        "ds_read_b128 v[204:207], v246 offset:36864\n"
        "v_mfma_f32_16x16x32_bf16 v[0:3], v[48:51], v[192:195], v[0:3]\n"
        "ds_read_b64_tr_b16 v[32:33], v226 offset:17408\n"
        "v_mfma_f32_16x16x32_bf16 v[0:3], v[52:55], v[208:211], v[0:3]\n"
        "ds_read_b64_tr_b16 v[34:35], v227 offset:17408\n"
        "v_mfma_f32_16x16x32_bf16 v[4:7], v[60:63], v[208:211], v[4:7]\n"
        "ds_read_b64_tr_b16 v[36:37], v226 offset:25600\n"
        "s_waitcnt lgkmcnt(8)\n"
        "v_mfma_f32_16x16x32_bf16 v[4:7], v[56:59], v[192:195], v[4:7]\n"
        "ds_read_b64_tr_b16 v[38:39], v227 offset:25600\n"
        "s_waitcnt lgkmcnt(8)\n"
        "v_mfma_f32_16x16x32_bf16 v[68:71], v[56:59], v[196:199], v[68:71]\n"
        "ds_read_b64_tr_b16 v[46:47], v229 offset:25600\n"
        "v_mfma_f32_16x16x32_bf16 v[68:71], v[60:63], v[212:215], v[68:71]\n"
        "ds_read_b64_tr_b16 v[42:43], v229 offset:17408\n"
        "s_waitcnt lgkmcnt(8)\n"
        "v_mfma_f32_16x16x32_bf16 v[64:67], v[52:55], v[212:215], v[64:67]\n"
        "global_store_dwordx4 v254, a[8:11], s[70:71] offset:0 sc1\n"
        "s_waitcnt lgkmcnt(7)\n"
        "s_waitcnt lgkmcnt(6)\n"
        "v_mfma_f32_16x16x32_bf16 v[64:67], v[48:51], v[196:199], v[64:67]\n"
        "v_mfma_f32_16x16x32_bf16 v[96:99], v[48:51], v[200:203], v[96:99]\n"
        "v_mfma_f32_16x16x32_bf16 v[96:99], v[52:55], v[216:219], v[96:99]\n"
        "v_mfma_f32_16x16x32_bf16 v[100:103], v[60:63], v[216:219], v[100:103]\n"
        "v_mfma_f32_16x16x32_bf16 v[100:103], v[56:59], v[200:203], v[100:103]\n"
        "v_mfma_f32_16x16x32_bf16 v[132:135], v[56:59], v[204:207], v[132:135]\n"
        "ds_read_b64_tr_b16 v[56:57], v232 offset:17408\n"
        "v_mfma_f32_16x16x32_bf16 v[132:135], v[60:63], v[220:223], v[132:135]\n"
        "ds_read_b64_tr_b16 v[60:61], v232 offset:25600\n"
        "v_mfma_f32_16x16x32_bf16 v[128:131], v[52:55], v[220:223], v[128:131]\n"
        "ds_read_b64_tr_b16 v[52:53], v230 offset:25600\n"
        "v_mfma_f32_16x16x32_bf16 v[128:131], v[48:51], v[204:207], v[128:131]\n"
        "global_store_dwordx4 v254, a[12:15], s[70:71] offset:1024 sc1\n"
        "ds_read_b64_tr_b16 v[48:49], v230 offset:17408\n"
        "ds_read_b64_tr_b16 v[50:51], v231 offset:17408\n"
        "s_add_u32 s70, s38, s97\n"
        "s_addc_u32 s71, s39, 0\n"
        "v_mfma_f32_16x16x32_bf16 v[8:11], v[160:163], v[192:195], v[8:11]\n"
        "ds_read_b64_tr_b16 v[54:55], v231 offset:25600\n"
        "s_add_u32 s97, s53, 0x4000\n"
        "v_mfma_f32_16x16x32_bf16 v[8:11], v[164:167], v[208:211], v[8:11]\n"
        "ds_read_b64_tr_b16 v[62:63], v233 offset:25600\n"
        "v_mfma_f32_16x16x32_bf16 v[12:15], v[172:175], v[208:211], v[12:15]\n"
        "ds_read_b64_tr_b16 v[58:59], v233 offset:17408\n"
        "s_lshl_b32 s80, s41, 5\n"
        "s_add_u32 s80, s80, 256\n"
        "v_mfma_f32_16x16x32_bf16 v[12:15], v[168:171], v[192:195], v[12:15]\n"
        "s_add_u32 s81, s80, s42\n"
        "s_lshl_b32 s81, s81, 7\n"
        "s_lshl_b32 s80, s80, 7\n"
        "v_mfma_f32_16x16x32_bf16 v[76:79], v[168:171], v[196:199], v[76:79]\n"
        "v_mfma_f32_16x16x32_bf16 v[76:79], v[172:175], v[212:215], v[76:79]\n"
        "s_waitcnt lgkmcnt(6)\n"
        "v_mfma_f32_16x16x32_bf16 v[72:75], v[164:167], v[212:215], v[72:75]\n"
        "v_mfma_f32_16x16x32_bf16 v[72:75], v[160:163], v[196:199], v[72:75]\n"
        "v_mfma_f32_16x16x32_bf16 v[104:107], v[160:163], v[200:203], v[104:107]\n"
        "v_mfma_f32_16x16x32_bf16 v[104:107], v[164:167], v[216:219], v[104:107]\n"
        "v_mfma_f32_16x16x32_bf16 v[108:111], v[172:175], v[216:219], v[108:111]\n"
        "s_waitcnt vmcnt(6) lgkmcnt(0)\n"
        "s_barrier\n"
        "v_mfma_f32_16x16x32_bf16 v[108:111], v[168:171], v[200:203], v[108:111]\n"
        "global_store_dwordx4 v254, a[208:211], s[70:71] offset:0 sc1\n"
        "v_mfma_f32_16x16x32_bf16 v[140:143], v[168:171], v[204:207], v[140:143]\n"
        "ds_write_b128 v244, a[224:227] offset:12288\n"
        "v_mfma_f32_16x16x32_bf16 v[140:143], v[172:175], v[220:223], v[140:143]\n"
        "ds_read_b64_tr_b16 v[172:173], v228 offset:40960\n"
        "v_mfma_f32_16x16x32_bf16 v[136:139], v[164:167], v[220:223], v[136:139]\n"
        "ds_read_b64_tr_b16 v[164:165], v226 offset:40960\n"
        "v_mfma_f32_16x16x32_bf16 v[136:139], v[160:163], v[204:207], v[136:139]\n"
        "ds_read_b64_tr_b16 v[160:161], v226 offset:32768\n"
        "v_mfma_f32_16x16x32_bf16 v[16:19], v[32:35], v[192:195], v[16:19]\n"
        "ds_read_b64_tr_b16 v[162:163], v227 offset:32768\n"
        "v_mfma_f32_16x16x32_bf16 v[16:19], v[36:39], v[208:211], v[16:19]\n"
        "ds_read_b64_tr_b16 v[166:167], v227 offset:40960\n"
        "v_mfma_f32_16x16x32_bf16 v[20:23], v[44:47], v[208:211], v[20:23]\n"
        "ds_read_b64_tr_b16 v[174:175], v229 offset:40960\n"
        "v_mfma_f32_16x16x32_bf16 v[20:23], v[40:43], v[192:195], v[20:23]\n"
        "ds_read_b64_tr_b16 v[168:169], v228 offset:32768\n"
        "v_mfma_f32_16x16x32_bf16 v[84:87], v[40:43], v[196:199], v[84:87]\n"
        "ds_read_b64_tr_b16 v[170:171], v229 offset:32768\n"
        "v_mfma_f32_16x16x32_bf16 v[84:87], v[44:47], v[212:215], v[84:87]\n"
        "ds_write_b128 v244, a[228:231] offset:13312\n"
        "v_mfma_f32_16x16x32_bf16 v[80:83], v[36:39], v[212:215], v[80:83]\n"
        "ds_write_b128 v244, a[232:235] offset:8192\n"
        "v_mfma_f32_16x16x32_bf16 v[80:83], v[32:35], v[196:199], v[80:83]\n"
        "global_store_dwordx4 v254, a[240:243], s[70:71] offset:1024 sc1\n"
        "v_mfma_f32_16x16x32_bf16 v[112:115], v[32:35], v[200:203], v[112:115]\n"
        "ds_write_b128 v244, a[236:239] offset:9216\n"
        "v_mfma_f32_16x16x32_bf16 v[112:115], v[36:39], v[216:219], v[112:115]\n"
        "ds_write_b128 v244, a[192:195] offset:4096\n"
        "v_mfma_f32_16x16x32_bf16 v[116:119], v[44:47], v[216:219], v[116:119]\n"
        "ds_write_b128 v244, a[196:199] offset:5120\n"
        "v_mfma_f32_16x16x32_bf16 v[116:119], v[40:43], v[200:203], v[116:119]\n"
        "ds_write_b128 v244, a[200:203] offset:16384\n"
        "v_mfma_f32_16x16x32_bf16 v[148:151], v[40:43], v[204:207], v[148:151]\n"
        "ds_read_b64_tr_b16 v[40:41], v232 offset:32768\n"
        "v_mfma_f32_16x16x32_bf16 v[148:151], v[44:47], v[220:223], v[148:151]\n"
        "ds_read_b64_tr_b16 v[44:45], v232 offset:40960\n"
        "v_mfma_f32_16x16x32_bf16 v[144:147], v[36:39], v[220:223], v[144:147]\n"
        "ds_read_b64_tr_b16 v[36:37], v230 offset:40960\n"
        "v_mfma_f32_16x16x32_bf16 v[144:147], v[32:35], v[204:207], v[144:147]\n"
        "ds_read_b64_tr_b16 v[32:33], v230 offset:32768\n"
        "v_mfma_f32_16x16x32_bf16 v[24:27], v[48:51], v[192:195], v[24:27]\n"
        "ds_read_b64_tr_b16 v[34:35], v231 offset:32768\n"
        "v_mfma_f32_16x16x32_bf16 v[24:27], v[52:55], v[208:211], v[24:27]\n"
        "ds_read_b64_tr_b16 v[38:39], v231 offset:40960\n"
        "v_mfma_f32_16x16x32_bf16 v[28:31], v[60:63], v[208:211], v[28:31]\n"
        "ds_read_b64_tr_b16 v[46:47], v233 offset:40960\n"
        "v_mfma_f32_16x16x32_bf16 v[28:31], v[56:59], v[192:195], v[28:31]\n"
        "ds_read_b64_tr_b16 v[42:43], v233 offset:32768\n"
        "v_mfma_f32_16x16x32_bf16 v[92:95], v[56:59], v[196:199], v[92:95]\n"
        "ds_read_b128 v[192:195], v246 offset:2048\n"
        "v_mfma_f32_16x16x32_bf16 v[92:95], v[60:63], v[212:215], v[92:95]\n"
        "ds_read_b128 v[208:211], v247 offset:2048\n"
        "v_mfma_f32_16x16x32_bf16 v[88:91], v[52:55], v[212:215], v[88:91]\n"
        "global_store_dwordx4 v254, a[212:215], s[70:71] offset:2048 sc1\n"
        "ds_read_b128 v[212:215], v247 offset:0\n"
        "v_mfma_f32_16x16x32_bf16 v[88:91], v[48:51], v[196:199], v[88:91]\n"
        "ds_write_b128 v244, a[204:207] offset:17408\n"
        "v_mfma_f32_16x16x32_bf16 v[120:123], v[48:51], v[200:203], v[120:123]\n"
        "ds_read_b128 v[196:199], v246 offset:0\n"
        "v_mfma_f32_16x16x32_bf16 v[120:123], v[52:55], v[216:219], v[120:123]\n"
        "ds_write_b128 v244, a[160:163] offset:20480\n"
        "v_mfma_f32_16x16x32_bf16 v[124:127], v[60:63], v[216:219], v[124:127]\n"
        "ds_write_b128 v244, a[164:167] offset:21504\n"
        "v_mfma_f32_16x16x32_bf16 v[124:127], v[56:59], v[200:203], v[124:127]\n"
        "ds_read_b128 v[200:203], v246 offset:6144\n"
        "s_waitcnt lgkmcnt(5)\n"
        "v_mfma_f32_16x16x32_bf16 v[156:159], v[56:59], v[204:207], v[156:159]\n"
        "ds_read_b128 v[216:219], v247 offset:6144\n"
        "v_mfma_f32_16x16x32_bf16 v[156:159], v[60:63], v[220:223], v[156:159]\n"
        "ds_write_b128 v244, a[168:171] offset:24576\n"
        "v_mfma_f32_16x16x32_bf16 v[152:155], v[52:55], v[220:223], v[152:155]\n"
        "ds_read_b128 v[220:223], v247 offset:4096\n"
        "s_waitcnt lgkmcnt(6)\n"
        "v_mfma_f32_16x16x32_bf16 v[152:155], v[48:51], v[204:207], v[152:155]\n"
        "ds_read_b128 v[204:207], v246 offset:4096\n"
        "v_mfma_f32_16x16x32_bf16 v[0:3], v[160:163], v[192:195], v[0:3]\n"
        "global_store_dwordx4 v254, a[244:247], s[70:71] offset:3072 sc1\n"
        "ds_read_b64_tr_b16 v[48:49], v226 offset:33792\n"
        "ds_read_b64_tr_b16 v[50:51], v227 offset:33792\n"
        "s_add_u32 s70, s38, s97\n"
        "s_addc_u32 s71, s39, 0\n"
        "v_mfma_f32_16x16x32_bf16 v[0:3], v[164:167], v[208:211], v[0:3]\n"
        "ds_read_b64_tr_b16 v[52:53], v226 offset:41984\n"
        "s_add_u32 s97, s53, 0x0\n"
        "s_waitcnt lgkmcnt(7)\n"
        "v_mfma_f32_16x16x32_bf16 v[4:7], v[172:175], v[208:211], v[4:7]\n"
        "ds_read_b64_tr_b16 v[54:55], v227 offset:41984\n"
        "s_waitcnt lgkmcnt(7)\n"
        "v_mfma_f32_16x16x32_bf16 v[4:7], v[168:171], v[192:195], v[4:7]\n"
        "ds_read_b64_tr_b16 v[60:61], v228 offset:41984\n"
        "v_cvt_pk_bf16_f32 v0, v0, v1\n"
        "v_mfma_f32_16x16x32_bf16 v[68:71], v[168:171], v[196:199], v[68:71]\n"
        "ds_read_b64_tr_b16 v[62:63], v229 offset:41984\n"
        "v_cvt_pk_bf16_f32 v1, v2, v3\n"
        "v_mfma_f32_16x16x32_bf16 v[68:71], v[172:175], v[212:215], v[68:71]\n"
        "ds_read_b64_tr_b16 v[56:57], v228 offset:33792\n"
        "s_waitcnt lgkmcnt(8)\n"
        "v_mfma_f32_16x16x32_bf16 v[64:67], v[164:167], v[212:215], v[64:67]\n"
        "ds_read_b64_tr_b16 v[58:59], v229 offset:33792\n"
        "v_cvt_pk_bf16_f32 v2, v4, v5\n"
        "v_mfma_f32_16x16x32_bf16 v[64:67], v[160:163], v[196:199], v[64:67]\n"
        "ds_write_b128 v244, a[172:175] offset:25600\n"
        "v_mfma_f32_16x16x32_bf16 v[96:99], v[160:163], v[200:203], v[96:99]\n"
        "ds_write_b128 v244, a[0:3] offset:28672\n"
        "v_mfma_f32_16x16x32_bf16 v[96:99], v[164:167], v[216:219], v[96:99]\n"
        "ds_write_b128 v244, a[4:7] offset:29696\n"
        "v_mfma_f32_16x16x32_bf16 v[100:103], v[172:175], v[216:219], v[100:103]\n"
        "s_waitcnt lgkmcnt(5)\n"
        "v_cvt_pk_bf16_f32 v3, v6, v7\n"
        "v_cvt_pk_bf16_f32 v64, v64, v65\n"
        "v_mfma_f32_16x16x32_bf16 v[100:103], v[168:171], v[200:203], v[100:103]\n"
        "ds_write_b128 v244, a[8:11] offset:0\n"
        "v_mfma_f32_16x16x32_bf16 v[132:135], v[168:171], v[204:207], v[132:135]\n"
        "global_store_dwordx4 v254, a[216:219], s[70:71] offset:0 sc1\n"
        "ds_read_b64_tr_b16 v[168:169], v232 offset:33792\n"
        "ds_read_b64_tr_b16 v[170:171], v233 offset:33792\n"
        "v_cvt_pk_bf16_f32 v65, v66, v67\n"
        "v_mfma_f32_16x16x32_bf16 v[132:135], v[172:175], v[220:223], v[132:135]\n"
        "ds_write_b128 v244, a[12:15] offset:1024\n"
        "v_mfma_f32_16x16x32_bf16 v[128:131], v[164:167], v[220:223], v[128:131]\n"
        "ds_read_b64_tr_b16 v[164:165], v230 offset:41984\n"
        "v_cvt_pk_bf16_f32 v66, v68, v69\n"
        "v_mfma_f32_16x16x32_bf16 v[128:131], v[160:163], v[204:207], v[128:131]\n"
        "ds_read_b64_tr_b16 v[160:161], v230 offset:33792\n"
        "v_cvt_pk_bf16_f32 v67, v70, v71\n"
        "v_mfma_f32_16x16x32_bf16 v[8:11], v[32:35], v[192:195], v[8:11]\n"
        "ds_read_b64_tr_b16 v[162:163], v231 offset:33792\n"
        "v_cvt_pk_bf16_f32 v96, v96, v97\n"
        "v_mfma_f32_16x16x32_bf16 v[8:11], v[36:39], v[208:211], v[8:11]\n"
        "ds_read_b64_tr_b16 v[166:167], v231 offset:41984\n"
        "v_cvt_pk_bf16_f32 v97, v98, v99\n"
        "v_mfma_f32_16x16x32_bf16 v[12:15], v[44:47], v[208:211], v[12:15]\n"
        "ds_read_b64_tr_b16 v[172:173], v232 offset:41984\n"
        "v_cvt_pk_bf16_f32 v98, v100, v101\n"
        "v_mfma_f32_16x16x32_bf16 v[12:15], v[40:43], v[192:195], v[12:15]\n"
        "ds_read_b64_tr_b16 v[174:175], v233 offset:41984\n"
        "v_cvt_pk_bf16_f32 v99, v102, v103\n"
        "v_mfma_f32_16x16x32_bf16 v[76:79], v[40:43], v[196:199], v[76:79]\n"
        "v_cvt_pk_bf16_f32 v4, v8, v9\n"
        "v_cvt_pk_bf16_f32 v5, v10, v11\n"
        "v_mfma_f32_16x16x32_bf16 v[76:79], v[44:47], v[212:215], v[76:79]\n"
        "v_cvt_pk_bf16_f32 v128, v128, v129\n"
        "v_cvt_pk_bf16_f32 v129, v130, v131\n"
        "v_mfma_f32_16x16x32_bf16 v[72:75], v[36:39], v[212:215], v[72:75]\n"
        "v_cvt_pk_bf16_f32 v6, v12, v13\n"
        "v_cvt_pk_bf16_f32 v7, v14, v15\n"
        "v_mfma_f32_16x16x32_bf16 v[72:75], v[32:35], v[196:199], v[72:75]\n"
        "global_store_dwordx4 v254, a[248:251], s[70:71] offset:1024 sc1\n"
        "v_cvt_pk_bf16_f32 v130, v132, v133\n"
        "v_cvt_pk_bf16_f32 v70, v76, v77\n"
        "v_cvt_pk_bf16_f32 v71, v78, v79\n"
        "v_cvt_pk_bf16_f32 v131, v134, v135\n"
        "v_mfma_f32_16x16x32_bf16 v[104:107], v[32:35], v[200:203], v[104:107]\n"
        "v_lshrrev_b32 v244, 1, v254\n"
        "v_add_u32 v244, s81, v244\n"
        "v_mfma_f32_16x16x32_bf16 v[104:107], v[36:39], v[216:219], v[104:107]\n"
        "s_waitcnt lgkmcnt(2)\n"
        "v_cvt_pk_bf16_f32 v68, v72, v73\n"
        "v_cvt_pk_bf16_f32 v69, v74, v75\n"
        "v_mfma_f32_16x16x32_bf16 v[108:111], v[44:47], v[216:219], v[108:111]\n"
        "s_waitcnt lgkmcnt(0)\n"
        "s_barrier\n"
        "v_mfma_f32_16x16x32_bf16 v[108:111], v[40:43], v[200:203], v[108:111]\n"
        "ds_write_b128 v245, a[208:211] offset:8192\n"
        "v_mfma_f32_16x16x32_bf16 v[140:143], v[40:43], v[204:207], v[140:143]\n"
        "ds_write_b128 v245, a[240:243] offset:9216\n"
        "v_mfma_f32_16x16x32_bf16 v[140:143], v[44:47], v[220:223], v[140:143]\n"
        "ds_write_b128 v245, a[212:215] offset:10240\n"
        "v_mfma_f32_16x16x32_bf16 v[136:139], v[36:39], v[220:223], v[136:139]\n"
        "ds_write_b128 v245, a[244:247] offset:11264\n"
        "v_mfma_f32_16x16x32_bf16 v[136:139], v[32:35], v[204:207], v[136:139]\n"
        "ds_write_b128 v245, a[216:219] offset:0\n"
        "v_mfma_f32_16x16x32_bf16 v[16:19], v[48:51], v[192:195], v[16:19]\n"
        "ds_write_b128 v245, a[248:251] offset:1024\n"
        "v_mfma_f32_16x16x32_bf16 v[16:19], v[52:55], v[208:211], v[16:19]\n"
        "ds_write_b128 v245, a[220:223] offset:2048\n"
        "v_mfma_f32_16x16x32_bf16 v[20:23], v[60:63], v[208:211], v[20:23]\n"
        "ds_write_b128 v245, a[252:255] offset:3072\n"
        "v_mfma_f32_16x16x32_bf16 v[20:23], v[56:59], v[192:195], v[20:23]\n"
        "global_store_dwordx4 v254, a[220:223], s[70:71] offset:2048 sc1\n"
        "s_barrier\n"
        "ds_read_b128 a[96:99], v240 offset:0\n"
        "s_add_u32 s81, s78, 4096\n"
        "v_mfma_f32_16x16x32_bf16 v[84:87], v[56:59], v[196:199], v[84:87]\n"
        "ds_read_b128 a[100:103], v240 offset:1024\n"
        "v_cvt_pk_bf16_f32 v8, v16, v17\n"
        "v_mfma_f32_16x16x32_bf16 v[84:87], v[60:63], v[212:215], v[84:87]\n"
        "ds_read_b128 a[104:107], v240 offset:2048\n"
        "v_cvt_pk_bf16_f32 v9, v18, v19\n"
        "v_mfma_f32_16x16x32_bf16 v[80:83], v[52:55], v[212:215], v[80:83]\n"
        "ds_read_b128 a[108:111], v240 offset:3072\n"
        "v_cvt_pk_bf16_f32 v10, v20, v21\n"
        "v_mfma_f32_16x16x32_bf16 v[80:83], v[48:51], v[196:199], v[80:83]\n"
        "ds_read_b128 a[112:115], v240 offset:4096\n"
        "v_cvt_pk_bf16_f32 v11, v22, v23\n"
        "v_mfma_f32_16x16x32_bf16 v[112:115], v[48:51], v[200:203], v[112:115]\n"
        "ds_read_b128 a[116:119], v240 offset:5120\n"
        "v_cndmask_b32 v32, 0, v242, s[10:11]\n"
        "v_mfma_f32_16x16x32_bf16 v[112:115], v[52:55], v[216:219], v[112:115]\n"
        "ds_read_b128 a[120:123], v240 offset:6144\n"
        "v_cndmask_b32 v33, 0, v242, s[12:13]\n"
        "v_mfma_f32_16x16x32_bf16 v[116:119], v[60:63], v[216:219], v[116:119]\n"
        "ds_write_b128 v245, a[176:179] offset:16384\n"
        "v_mfma_f32_16x16x32_bf16 v[116:119], v[56:59], v[200:203], v[116:119]\n"
        "ds_write_b128 v245, a[180:183] offset:17408\n"
        "v_mfma_f32_16x16x32_bf16 v[148:151], v[56:59], v[204:207], v[148:151]\n"
        "ds_write_b128 v245, a[184:187] offset:18432\n"
        "v_mfma_f32_16x16x32_bf16 v[148:151], v[60:63], v[220:223], v[148:151]\n"
        "ds_write_b128 v245, a[188:191] offset:19456\n"
        "v_mfma_f32_16x16x32_bf16 v[144:147], v[52:55], v[220:223], v[144:147]\n"
        "ds_write_b128 v245, a[16:19] offset:24576\n"
        "v_mfma_f32_16x16x32_bf16 v[144:147], v[48:51], v[204:207], v[144:147]\n"
        "global_store_dwordx4 v254, a[252:255], s[70:71] offset:3072 sc1\n"
        "s_add_u32 s70, s38, s97\n"
        "s_addc_u32 s71, s39, 0\n"
        "ds_read_b128 a[124:127], v240 offset:7168\n"
        "s_add_u32 s97, s53, 0x2000\n"
        "v_cndmask_b32 v34, 0, v242, s[14:15]\n"
        "v_mfma_f32_16x16x32_bf16 v[24:27], v[160:163], v[192:195], v[24:27]\n"
        "ds_write_b128 v245, a[20:23] offset:25600\n"
        "v_mfma_f32_16x16x32_bf16 v[24:27], v[164:167], v[208:211], v[24:27]\n"
        "ds_write_b128 v245, a[24:27] offset:26624\n"
        "v_mfma_f32_16x16x32_bf16 v[28:31], v[172:175], v[208:211], v[28:31]\n"
        "ds_write_b128 v245, a[28:31] offset:27648\n"
        "v_mfma_f32_16x16x32_bf16 v[28:31], v[168:171], v[192:195], v[28:31]\n"
        "global_store_dwordx4 v254, a[180:183], s[70:71] offset:1024 sc1\n"
        "s_cmp_eq_u32 s82, s83\n"
        "s_cselect_b64 s[74:75], s[94:95], s[74:75]\n"
        "s_cselect_b32 s81, s51, s81\n"
        "v_cndmask_b32 v35, 0, v242, s[16:17]\n"
        "v_cndmask_b32 v48, 0, v242, s[20:21]\n"
        "v_mfma_f32_16x16x32_bf16 v[92:95], v[168:171], v[196:199], v[92:95]\n"
        "v_cvt_pk_bf16_f32 v12, v24, v25\n"
        "v_cvt_pk_bf16_f32 v13, v26, v27\n"
        "v_mfma_f32_16x16x32_bf16 v[92:95], v[172:175], v[212:215], v[92:95]\n"
        "s_waitcnt lgkmcnt(2)\n"
        "v_cvt_pk_bf16_f32 v14, v28, v29\n"
        "v_cvt_pk_bf16_f32 v15, v30, v31\n"
        "v_mfma_f32_16x16x32_bf16 v[88:91], v[164:167], v[212:215], v[88:91]\n"
        "v_cndmask_b32 v49, 0, v242, s[22:23]\n"
        "v_cndmask_b32 v50, 0, v242, s[24:25]\n"
        "v_mfma_f32_16x16x32_bf16 v[88:91], v[160:163], v[196:199], v[88:91]\n"
        "v_cndmask_b32 v51, 0, v242, s[28:29]\n"
        "v_cvt_pk_bf16_f32 v72, v80, v81\n"
        "v_mfma_f32_16x16x32_bf16 v[120:123], v[160:163], v[200:203], v[120:123]\n"
        "s_waitcnt lgkmcnt(0)\n"
        "v_mfma_f32_16x16x32_bf16 v[120:123], v[164:167], v[216:219], v[120:123]\n"
        "s_barrier\n"
        "ds_read_b128 a[144:147], v241 offset:0\n"
        "v_mfma_f32_16x16x32_bf16 v[124:127], v[172:175], v[216:219], v[124:127]\n"
        "ds_read_b128 a[148:151], v241 offset:1024\n"
        "v_cvt_pk_bf16_f32 v73, v82, v83\n"
        "v_mfma_f32_16x16x32_bf16 v[124:127], v[168:171], v[200:203], v[124:127]\n"
        "global_store_dwordx4 v254, a[176:179], s[70:71] offset:0 sc1\n"
        "ds_read_b128 a[152:155], v241 offset:2048\n"
        "ds_read_b128 a[156:159], v241 offset:3072\n"
        "v_cvt_pk_bf16_f32 v74, v84, v85\n"
        "v_mfma_f32_16x16x32_bf16 v[156:159], v[168:171], v[204:207], v[156:159]\n"
        "ds_read_b128 a[128:131], v241 offset:4096\n"
        "v_cvt_pk_bf16_f32 v75, v86, v87\n"
        "v_mfma_f32_16x16x32_bf16 v[156:159], v[172:175], v[220:223], v[156:159]\n"
        "LHA10_%=:\n"
        "s_cbranch_execz LHRA_%=\n"
        "ds_read_b128 a[132:135], v241 offset:5120\n"
        "v_cvt_pk_bf16_f32 v76, v88, v89\n"
        "v_mfma_f32_16x16x32_bf16 v[152:155], v[164:167], v[220:223], v[152:155]\n"
        "ds_read_b128 a[136:139], v241 offset:6144\n"
        "v_cvt_pk_bf16_f32 v77, v90, v91\n"
        "v_mfma_f32_16x16x32_bf16 v[152:155], v[160:163], v[204:207], v[152:155]\n"
        "ds_read_b128 a[140:143], v241 offset:7168\n"
        "v_cvt_pk_bf16_f32 v78, v92, v93\n"
        "v_mfma_f32_16x16x32_bf16 v[32:35], a[96:99], v[0:3], v[32:35]\n"
        "ds_read_b128 a[96:99], v240 offset:8192\n"
        "v_cvt_pk_bf16_f32 v79, v94, v95\n"
        "v_mfma_f32_16x16x32_bf16 v[32:35], a[100:103], v[4:7], v[32:35]\n"
        "ds_read_b128 a[100:103], v240 offset:9216\n"
        "v_cvt_pk_bf16_f32 v100, v104, v105\n"
        "v_mfma_f32_16x16x32_bf16 v[32:35], a[104:107], v[8:11], v[32:35]\n"
        "ds_read_b128 a[104:107], v240 offset:10240\n"
        "v_cvt_pk_bf16_f32 v101, v106, v107\n"
        "v_mfma_f32_16x16x32_bf16 v[32:35], a[108:111], v[12:15], v[32:35]\n"
        "global_store_dwordx4 v254, a[184:187], s[70:71] offset:2048 sc1\n"
        "ds_read_b128 a[108:111], v240 offset:11264\n"
        "v_cvt_pk_bf16_f32 v102, v108, v109\n"
        "v_cvt_pk_bf16_f32 v103, v110, v111\n"
        "v_cndmask_b32 v36, 0, v242, s[10:11]\n"
        "v_mfma_f32_16x16x32_bf16 v[48:51], a[112:115], v[0:3], v[48:51]\n"
        "ds_read_b128 a[112:115], v240 offset:12288\n"
        "v_cndmask_b32 v37, 0, v242, s[12:13]\n"
        "v_mfma_f32_16x16x32_bf16 v[48:51], a[116:119], v[4:7], v[48:51]\n"
        "ds_read_b128 a[116:119], v240 offset:13312\n"
        "v_max_f32 v246, v32, v33\n"
        "v_mfma_f32_16x16x32_bf16 v[48:51], a[120:123], v[8:11], v[48:51]\n"
        "ds_read_b128 a[120:123], v240 offset:14336\n"
        "v_max3_f32 v246, v34, v35, v246\n"
        "v_mfma_f32_16x16x32_bf16 v[48:51], a[124:127], v[12:15], v[48:51]\n"
        "v_mov_b32 v247, v246\n"
        "global_store_dwordx4 v254, a[188:191], s[70:71] offset:3072 sc1\n"
        "ds_read_b128 a[124:127], v240 offset:15360\n"
        "v_permlane16_swap_b32 v246, v247\n"
        "v_max_f32 v246, v246, v247\n"
        "v_mov_b32 v247, v246\n"
        "s_add_u32 s70, s38, s97\n"
        "s_addc_u32 s71, s39, 0\n"
        "v_cndmask_b32 v38, 0, v242, s[14:15]\n"
        "v_permlane32_swap_b32 v246, v247\n"
        "v_max_f32 v246, v246, v247\n"
        "v_sub_f32 v224, 0, v246\n"
        "v_sub_f32 v225, 0, v246\n"
        "v_sub_f32 v226, 0, v246\n"
        "v_sub_f32 v227, 0, v246\n"
        "v_add_f32 v32, v32, v224\n"
        "v_exp_f32 v32, v32\n"
        "v_add_f32 v33, v33, v224\n"
        "v_exp_f32 v33, v33\n"
        "v_add_f32 v34, v34, v224\n"
        "v_cvt_pk_bf16_f32 v16, v32, v33\n"
        "v_exp_f32 v34, v34\n"
        "v_add_f32 v35, v35, v224\n"
        "v_exp_f32 v35, v35\n"
        "v_cndmask_b32 v32, v224, v242, s[30:31]\n"
        "v_cvt_pk_bf16_f32 v17, v34, v35\n"
        "v_cndmask_b32 v33, v224, v242, s[56:57]\n"
        "v_cndmask_b32 v34, v224, v242, s[58:59]\n"
        "v_cndmask_b32 v35, v224, v242, s[60:61]\n"
        "s_waitcnt lgkmcnt(0)\n"
        "s_nop 0\n"
        "v_mfma_f32_16x16x32_bf16 v[32:35], a[96:99], v[0:3], v[32:35]\n"
        "ds_read_b128 a[96:99], v240 offset:16384\n"
        "v_add_f32 v48, v48, v224\n"
        "v_mfma_f32_16x16x32_bf16 v[32:35], a[100:103], v[4:7], v[32:35]\n"
        "v_exp_f32 v48, v48\n"
        "v_mfma_f32_16x16x32_bf16 v[32:35], a[104:107], v[8:11], v[32:35]\n"
        "ds_read_b128 a[100:103], v240 offset:17408\n"
        "v_add_f32 v49, v49, v224\n"
        "v_mfma_f32_16x16x32_bf16 v[32:35], a[108:111], v[12:15], v[32:35]\n"
        "v_exp_f32 v49, v49\n"
        "global_store_dwordx4 v254, a[16:19], s[70:71] offset:0 sc1\n"
        "v_cvt_pk_bf16_f32 v18, v48, v49\n"
        "v_add_f32 v50, v50, v224\n"
        "v_exp_f32 v50, v50\n"
        "v_add_f32 v51, v51, v224\n"
        "v_exp_f32 v51, v51\n"
        "ds_read_b128 a[104:107], v240 offset:18432\n"
        "v_cvt_pk_bf16_f32 v19, v50, v51\n"
        "s_nop 1\n"
        "v_mfma_f32_16x16x32_bf16 a[64:67], a[80:83], v[16:19], 0\n"
        "ds_read_b128 a[108:111], v240 offset:19456\n"
        "v_cndmask_b32 v48, v224, v242, s[62:63]\n"
        "v_mfma_f32_16x16x32_bf16 v[160:163], a[144:147], v[16:19], 0\n"
        "global_store_dwordx4 v254, a[20:23], s[70:71] offset:1024 sc1\n"
        "ds_read_b128 a[144:147], v241 offset:8192\n"
        "v_cndmask_b32 v49, v224, v242, s[84:85]\n"
        "v_cndmask_b32 v50, v224, v242, s[86:87]\n"
        "v_cndmask_b32 v51, v224, v242, s[88:89]\n"
        "s_nop 1\n"
        "v_mfma_f32_16x16x32_bf16 v[48:51], a[112:115], v[0:3], v[48:51]\n"
        "v_exp_f32 v32, v32\n"
        "v_mfma_f32_16x16x32_bf16 v[48:51], a[116:119], v[4:7], v[48:51]\n"
        "v_exp_f32 v33, v33\n"
        "v_mfma_f32_16x16x32_bf16 v[48:51], a[120:123], v[8:11], v[48:51]\n"
        "ds_read_b128 a[112:115], v240 offset:20480\n"
        "v_cvt_pk_bf16_f32 v80, v32, v33\n"
        "v_mfma_f32_16x16x32_bf16 v[48:51], a[124:127], v[12:15], v[48:51]\n"
        "v_exp_f32 v34, v34\n"
        "v_mfma_f32_16x16x32_bf16 v[164:167], a[148:151], v[16:19], 0\n"
        "v_exp_f32 v35, v35\n"
        "v_mfma_f32_16x16x32_bf16 v[168:171], a[152:155], v[16:19], 0\n"
        "ds_read_b128 a[116:119], v240 offset:21504\n"
        "v_cvt_pk_bf16_f32 v81, v34, v35\n"
        "v_mfma_f32_16x16x32_bf16 v[172:175], a[156:159], v[16:19], 0\n"
        "ds_read_b128 a[148:151], v241 offset:9216\n"
        "s_waitcnt lgkmcnt(3)\n"
        "v_mfma_f32_16x16x32_bf16 v[32:35], a[96:99], v[0:3], v[224:227]\n"
        "global_store_dwordx4 v254, a[24:27], s[70:71] offset:2048 sc1\n"
        "ds_read_b128 a[152:155], v241 offset:10240\n"
        "ds_read_b128 a[120:123], v240 offset:22528\n"
        "v_cndmask_b32 v39, 0, v242, s[16:17]\n"
        "v_mfma_f32_16x16x32_bf16 v[32:35], a[100:103], v[4:7], v[32:35]\n"
        "ds_read_b128 a[156:159], v241 offset:11264\n"
        "v_cndmask_b32 v52, 0, v242, s[20:21]\n"
        "v_mfma_f32_16x16x32_bf16 v[32:35], a[104:107], v[8:11], v[32:35]\n"
        "v_exp_f32 v48, v48\n"
        "v_mfma_f32_16x16x32_bf16 v[32:35], a[108:111], v[12:15], v[32:35]\n"
        "v_exp_f32 v49, v49\n"
        "v_mfma_f32_16x16x32_bf16 v[36:39], a[108:111], v[76:79], v[36:39]\n"
        "ds_read_b128 a[124:127], v240 offset:23552\n"
        "v_cvt_pk_bf16_f32 v82, v48, v49\n"
        "v_mfma_f32_16x16x32_bf16 v[36:39], a[104:107], v[72:75], v[36:39]\n"
        "global_store_dwordx4 v254, a[28:31], s[70:71] offset:3072 sc1\n"
        "ds_read_b128 a[104:107], v240 offset:26624\n"
        "v_exp_f32 v50, v50\n"
        "v_cndmask_b32 v53, 0, v242, s[22:23]\n"
        "v_mfma_f32_16x16x32_bf16 v[36:39], a[100:103], v[68:71], v[36:39]\n"
        "v_exp_f32 v51, v51\n"
        "v_mfma_f32_16x16x32_bf16 v[36:39], a[96:99], v[64:67], v[36:39]\n"
        "ds_read_b128 a[96:99], v240 offset:24576\n"
        "v_cvt_pk_bf16_f32 v83, v50, v51\n"
        "v_mfma_f32_16x16x32_bf16 v[176:179], a[128:131], v[16:19], 0\n"
        "ds_read_b128 a[100:103], v240 offset:25600\n"
        "v_cndmask_b32 v54, 0, v242, s[24:25]\n"
        "v_mfma_f32_16x16x32_bf16 v[180:183], a[132:135], v[16:19], 0\n"
        "ds_read_b128 a[128:131], v241 offset:12288\n"
        "v_cndmask_b32 v55, 0, v242, s[28:29]\n"
        "v_mfma_f32_16x16x32_bf16 v[184:187], a[136:139], v[16:19], 0\n"
        "ds_read_b128 a[132:135], v241 offset:13312\n"
        "v_cvt_pk_bf16_f32 v104, v112, v113\n"
        "v_mfma_f32_16x16x32_bf16 v[188:191], a[140:143], v[16:19], 0\n"
        "ds_read_b128 a[136:139], v241 offset:14336\n"
        "s_waitcnt lgkmcnt(6)\n"
        "v_mfma_f32_16x16x32_bf16 v[48:51], a[112:115], v[0:3], v[224:227]\n"
        "ds_read_b128 a[140:143], v241 offset:15360\n"
        "v_max_f32 v248, v36, v37\n"
        "v_mfma_f32_16x16x32_bf16 v[48:51], a[116:119], v[4:7], v[48:51]\n"
        "v_exp_f32 v32, v32\n"
        "v_mfma_f32_16x16x32_bf16 v[48:51], a[120:123], v[8:11], v[48:51]\n"
        "v_exp_f32 v33, v33\n"
        "v_mfma_f32_16x16x32_bf16 v[48:51], a[124:127], v[12:15], v[48:51]\n"
        "ds_read_b128 a[108:111], v240 offset:27648\n"
        "v_cvt_pk_bf16_f32 v16, v32, v33\n"
        "v_mfma_f32_16x16x32_bf16 v[52:55], a[124:127], v[76:79], v[52:55]\n"
        "v_exp_f32 v34, v34\n"
        "v_mfma_f32_16x16x32_bf16 v[52:55], a[120:123], v[72:75], v[52:55]\n"
        "v_exp_f32 v35, v35\n"
        "v_mfma_f32_16x16x32_bf16 v[52:55], a[116:119], v[68:71], v[52:55]\n"
        "ds_read_b128 a[116:119], v240 offset:29696\n"
        "v_cvt_pk_bf16_f32 v17, v34, v35\n"
        "v_mfma_f32_16x16x32_bf16 v[52:55], a[112:115], v[64:67], v[52:55]\n"
        "s_waitcnt lgkmcnt(3)\n"
        "v_cvt_pk_bf16_f32 v105, v114, v115\n"
        "v_max3_f32 v248, v38, v39, v248\n"
        "v_mfma_f32_16x16x32_bf16 a[64:67], a[80:83], v[80:83], a[64:67]\n"
        "ds_read_b128 a[112:115], v240 offset:28672\n"
        "v_cvt_pk_bf16_f32 v106, v116, v117\n"
        "v_mfma_f32_16x16x32_bf16 v[160:163], a[144:147], v[80:83], v[160:163]\n"
        "ds_read_b128 a[144:147], v241 offset:16384\n"
        "v_mov_b32 v249, v248\n"
        "v_mfma_f32_16x16x32_bf16 v[164:167], a[148:151], v[80:83], v[164:167]\n"
        "ds_read_b128 a[148:151], v241 offset:17408\n"
        "v_cvt_pk_bf16_f32 v107, v118, v119\n"
        "v_mfma_f32_16x16x32_bf16 v[168:171], a[152:155], v[80:83], v[168:171]\n"
        "s_waitcnt lgkmcnt(4)\n"
        "v_permlane16_swap_b32 v248, v249\n"
        "v_mfma_f32_16x16x32_bf16 v[172:175], a[156:159], v[80:83], v[172:175]\n"
        "ds_read_b128 a[152:155], v241 offset:18432\n"
        "v_max_f32 v248, v248, v249\n"
        "v_mfma_f32_16x16x32_bf16 v[32:35], a[96:99], v[0:3], v[224:227]\n"
        "ds_read_b128 a[124:127], v240 offset:31744\n"
        "v_mov_b32 v249, v248\n"
        "v_mfma_f32_16x16x32_bf16 v[32:35], a[100:103], v[4:7], v[32:35]\n"
        "ds_read_b128 a[120:123], v240 offset:30720\n"
        "global_load_dwordx4 v[112:115], v254, s[92:93] offset:0\n"
        "v_mfma_f32_16x16x32_bf16 v[32:35], a[104:107], v[8:11], v[32:35]\n"
        "v_permlane32_swap_b32 v248, v249\n"
        "v_mfma_f32_16x16x32_bf16 v[32:35], a[108:111], v[12:15], v[32:35]\n"
        "ds_read_b128 a[156:159], v241 offset:19456\n"
        "v_max_f32 v248, v248, v249\n"
        "v_mfma_f32_16x16x32_bf16 v[176:179], a[128:131], v[80:83], v[176:179]\n"
        "ds_read_b128 a[128:131], v241 offset:20480\n"
        "v_sub_f32 v228, 0, v248\n"
        "v_mfma_f32_16x16x32_bf16 v[180:183], a[132:135], v[80:83], v[180:183]\n"
        "ds_read_b128 a[132:135], v241 offset:21504\n"
        "v_sub_f32 v229, 0, v248\n"
        "v_mfma_f32_16x16x32_bf16 v[184:187], a[136:139], v[80:83], v[184:187]\n"
        "ds_read_b128 a[136:139], v241 offset:22528\n"
        "v_sub_f32 v230, 0, v248\n"
        "v_mfma_f32_16x16x32_bf16 v[188:191], a[140:143], v[80:83], v[188:191]\n"
        "v_sub_f32 v231, 0, v248\n"
        "v_add_f32 v36, v36, v228\n"
        "v_exp_f32 v36, v36\n"
        "v_add_f32 v37, v37, v228\n"
        "v_exp_f32 v37, v37\n"
        "v_add_f32 v38, v38, v228\n"
        "v_cvt_pk_bf16_f32 v20, v36, v37\n"
        "v_exp_f32 v38, v38\n"
        "v_add_f32 v39, v39, v228\n"
        "v_exp_f32 v39, v39\n"
        "v_exp_f32 v48, v48\n"
        "v_cvt_pk_bf16_f32 v21, v38, v39\n"
        "v_exp_f32 v49, v49\n"
        "v_exp_f32 v50, v50\n"
        "v_cvt_pk_bf16_f32 v18, v48, v49\n"
        "v_exp_f32 v51, v51\n"
        "v_cndmask_b32 v36, v228, v242, s[30:31]\n"
        "v_cvt_pk_bf16_f32 v19, v50, v51\n"
        "s_waitcnt lgkmcnt(1)\n"
        "v_mfma_f32_16x16x32_bf16 v[48:51], a[112:115], v[0:3], v[224:227]\n"
        "ds_read_b128 a[140:143], v241 offset:23552\n"
        "v_cndmask_b32 v37, v228, v242, s[56:57]\n"
        "v_mfma_f32_16x16x32_bf16 v[48:51], a[116:119], v[4:7], v[48:51]\n"
        "global_load_dwordx4 v[116:119], v254, s[92:93] offset:1024\n"
        "v_cndmask_b32 v38, v228, v242, s[58:59]\n"
        "v_mfma_f32_16x16x32_bf16 v[48:51], a[120:123], v[8:11], v[48:51]\n"
        "v_cndmask_b32 v39, v228, v242, s[60:61]\n"
        "s_nop 1\n"
        "v_mfma_f32_16x16x32_bf16 v[36:39], a[108:111], v[76:79], v[36:39]\n"
        "s_barrier\n"
        "v_add_f32 v52, v52, v228\n"
        "v_add_f32 v53, v53, v228\n"
        "v_mfma_f32_16x16x32_bf16 v[36:39], a[104:107], v[72:75], v[36:39]\n"
        "v_exp_f32 v52, v52\n"
        "v_mfma_f32_16x16x32_bf16 v[36:39], a[100:103], v[68:71], v[36:39]\n"
        "v_exp_f32 v53, v53\n"
        "v_mfma_f32_16x16x32_bf16 v[36:39], a[96:99], v[64:67], v[36:39]\n"
        "v_cvt_pk_bf16_f32 v22, v52, v53\n"
        "v_xor_b32 v240, 0x8000, v240\n"
        "v_mfma_f32_16x16x32_bf16 v[48:51], a[124:127], v[12:15], v[48:51]\n"
        "ds_read_b128 a[96:99], v240 offset:0\n"
        "v_add_f32 v54, v54, v228\n"
        "v_mfma_f32_16x16x32_bf16 a[64:67], a[80:83], v[16:19], a[64:67]\n"
        "ds_read_b128 a[100:103], v240 offset:1024\n"
        "v_cvt_pk_bf16_f32 v108, v120, v121\n"
        "v_mfma_f32_16x16x32_bf16 v[160:163], a[144:147], v[16:19], v[160:163]\n"
        "s_waitcnt lgkmcnt(2)\n"
        "v_exp_f32 v54, v54\n"
        "v_mfma_f32_16x16x32_bf16 v[164:167], a[148:151], v[16:19], v[164:167]\n"
        "ds_read_b128 a[104:107], v240 offset:2048\n"
        "v_add_f32 v55, v55, v228\n"
        "v_mfma_f32_16x16x32_bf16 v[168:171], a[152:155], v[16:19], v[168:171]\n"
        "v_exp_f32 v55, v55\n"
        "v_mfma_f32_16x16x32_bf16 v[172:175], a[156:159], v[16:19], v[172:175]\n"
        "ds_read_b128 a[108:111], v240 offset:3072\n"
        "v_cvt_pk_bf16_f32 v109, v122, v123\n"
        "v_mfma_f32_16x16x32_bf16 v[176:179], a[128:131], v[16:19], v[176:179]\n"
        "global_load_dwordx4 v[120:123], v254, s[92:93] offset:2048\n"
        "v_cvt_pk_bf16_f32 v110, v124, v125\n"
        "v_mfma_f32_16x16x32_bf16 v[180:183], a[132:135], v[16:19], v[180:183]\n"
        "v_cvt_pk_bf16_f32 v23, v54, v55\n"
        "s_nop 1\n"
        "v_mfma_f32_16x16x32_bf16 a[68:71], a[80:83], v[20:23], 0\n"
        "v_cvt_pk_bf16_f32 v111, v126, v127\n"
        "v_cvt_pk_bf16_f32 v132, v136, v137\n"
        "v_mfma_f32_16x16x32_bf16 v[192:195], a[144:147], v[20:23], 0\n"
        "ds_read_b128 a[144:147], v241 offset:24576\n"
        "v_cvt_pk_bf16_f32 v133, v138, v139\n"
        "v_mfma_f32_16x16x32_bf16 v[196:199], a[148:151], v[20:23], 0\n"
        "global_load_dwordx4 v[124:127], v254, s[92:93] offset:3072\n"
        "v_cvt_pk_bf16_f32 v134, v140, v141\n"
        "v_mfma_f32_16x16x32_bf16 v[200:203], a[152:155], v[20:23], 0\n"
        "ds_read_b128 a[148:151], v241 offset:25600\n"
        "v_cvt_pk_bf16_f32 v135, v142, v143\n"
        "v_mfma_f32_16x16x32_bf16 v[204:207], a[156:159], v[20:23], 0\n"
        "v_exp_f32 v32, v32\n"
        "v_mfma_f32_16x16x32_bf16 v[208:211], a[128:131], v[20:23], 0\n"
        "v_exp_f32 v33, v33\n"
        "v_mfma_f32_16x16x32_bf16 v[212:215], a[132:135], v[20:23], 0\n"
        "ds_read_b128 a[152:155], v241 offset:26624\n"
        "v_cvt_pk_bf16_f32 v80, v32, v33\n"
        "v_mfma_f32_16x16x32_bf16 v[184:187], a[136:139], v[16:19], v[184:187]\n"
        "ds_read_b128 a[156:159], v241 offset:27648\n"
        "v_cndmask_b32 v52, v228, v242, s[62:63]\n"
        "v_mfma_f32_16x16x32_bf16 v[216:219], a[136:139], v[20:23], 0\n"
        "ds_read_b128 a[128:131], v241 offset:28672\n"
        "v_cndmask_b32 v53, v228, v242, s[84:85]\n"
        "v_mfma_f32_16x16x32_bf16 v[220:223], a[140:143], v[20:23], 0\n"
        "ds_read_b128 a[132:135], v241 offset:29696\n"
        "v_cndmask_b32 v54, v228, v242, s[86:87]\n"
        "v_mfma_f32_16x16x32_bf16 v[188:191], a[140:143], v[16:19], v[188:191]\n"
        "v_cndmask_b32 v55, v228, v242, s[88:89]\n"
        "s_nop 1\n"
        "v_mfma_f32_16x16x32_bf16 v[52:55], a[124:127], v[76:79], v[52:55]\n"
        "s_waitcnt vmcnt(4)\n"
        "v_exp_f32 v34, v34\n"
        "v_mfma_f32_16x16x32_bf16 v[52:55], a[120:123], v[72:75], v[52:55]\n"
        "s_barrier\n"
        "v_exp_f32 v35, v35\n"
        "v_mfma_f32_16x16x32_bf16 v[52:55], a[116:119], v[68:71], v[52:55]\n"
        "ds_read_b128 a[116:119], v240 offset:5120\n"
        "v_cvt_pk_bf16_f32 v81, v34, v35\n"
        "v_mfma_f32_16x16x32_bf16 v[52:55], a[112:115], v[64:67], v[52:55]\n"
        "ds_read_b128 a[112:115], v240 offset:4096\n"
        "s_waitcnt lgkmcnt(5)\n"
        "v_mfma_f32_16x16x32_bf16 v[32:35], a[96:99], v[0:3], v[224:227]\n"
        "v_exp_f32 v36, v36\n"
        "v_mfma_f32_16x16x32_bf16 v[32:35], a[100:103], v[4:7], v[32:35]\n"
        "v_exp_f32 v37, v37\n"
        "v_mfma_f32_16x16x32_bf16 v[32:35], a[104:107], v[8:11], v[32:35]\n"
        "ds_read_b128 a[120:123], v240 offset:6144\n"
        "v_cvt_pk_bf16_f32 v84, v36, v37\n"
        "v_mfma_f32_16x16x32_bf16 v[32:35], a[108:111], v[12:15], v[32:35]\n"
        "v_cvt_pk_bf16_f32 v138, v148, v149\n"
        "v_cvt_pk_bf16_f32 v136, v144, v145\n"
        "v_add_u32 v148, 16, v254\n"
        "global_load_dword v144, v148, s[92:93] offset:0\n"
        "global_load_dword v145, v148, s[92:93] offset:1024\n"
        "v_cvt_pk_bf16_f32 v137, v146, v147\n"
        "global_load_dword v146, v148, s[92:93] offset:2048\n"
        "v_min_u32 v149, 1020, v148\n"
        "global_load_dword v147, v149, s[92:93] offset:3072\n"
        "v_exp_f32 v38, v38\n"
        "v_exp_f32 v39, v39\n"
        "v_cvt_pk_bf16_f32 v139, v150, v151\n"
        "v_cvt_pk_bf16_f32 v85, v38, v39\n"
        "v_mfma_f32_16x16x32_bf16 v[36:39], a[108:111], v[76:79], v[228:231]\n"
        "v_exp_f32 v48, v48\n"
        "v_mfma_f32_16x16x32_bf16 v[36:39], a[104:107], v[72:75], v[36:39]\n"
        "v_exp_f32 v49, v49\n"
        "v_mfma_f32_16x16x32_bf16 v[36:39], a[100:103], v[68:71], v[36:39]\n"
        "ds_read_b128 a[124:127], v240 offset:7168\n"
        "v_cvt_pk_bf16_f32 v82, v48, v49\n"
        "v_mfma_f32_16x16x32_bf16 v[36:39], a[96:99], v[64:67], v[36:39]\n"
        "v_exp_f32 v50, v50\n"
        "v_exp_f32 v51, v51\n"
        "v_cndmask_b32 v40, 0, v242, s[10:11]\n"
        "v_cvt_pk_bf16_f32 v83, v50, v51\n"
        "s_waitcnt lgkmcnt(1)\n"
        "v_mfma_f32_16x16x32_bf16 v[48:51], a[112:115], v[0:3], v[224:227]\n"
        "ds_read_b128 a[136:139], v241 offset:30720\n"
        "v_cndmask_b32 v41, 0, v242, s[12:13]\n"
        "v_mfma_f32_16x16x32_bf16 v[48:51], a[116:119], v[4:7], v[48:51]\n"
        "ds_read_b128 a[140:143], v241 offset:31744\n"
        "v_cndmask_b32 v42, 0, v242, s[14:15]\n"
        "v_mfma_f32_16x16x32_bf16 v[48:51], a[120:123], v[8:11], v[48:51]\n"
        "v_cndmask_b32 v43, 0, v242, s[16:17]\n"
        "s_nop 1\n"
        "v_mfma_f32_16x16x32_bf16 v[40:43], a[96:99], v[96:99], v[40:43]\n"
        "ds_read_b128 a[96:99], v240 offset:8192\n"
        "v_cvt_pk_bf16_f32 v140, v152, v153\n"
        "v_mfma_f32_16x16x32_bf16 v[40:43], a[100:103], v[100:103], v[40:43]\n"
        "v_exp_f32 v52, v52\n"
        "v_mfma_f32_16x16x32_bf16 v[40:43], a[104:107], v[104:107], v[40:43]\n"
        "s_waitcnt lgkmcnt(3)\n"
        "v_exp_f32 v53, v53\n"
        "v_mfma_f32_16x16x32_bf16 v[40:43], a[108:111], v[108:111], v[40:43]\n"
        "ds_read_b128 a[100:103], v240 offset:9216\n"
        "v_cvt_pk_bf16_f32 v86, v52, v53\n"
        "v_mfma_f32_16x16x32_bf16 v[48:51], a[124:127], v[12:15], v[48:51]\n"
        "ds_read_b128 a[104:107], v240 offset:10240\n"
        "s_mov_b32 s91, s44\n"
        "v_cvt_pk_bf16_f32 v141, v154, v155\n"
        "v_mfma_f32_16x16x32_bf16 a[64:67], a[80:83], v[80:83], a[64:67]\n"
        "ds_read_b128 a[108:111], v240 offset:11264\n"
        "v_cvt_pk_bf16_f32 v142, v156, v157\n"
        "v_mfma_f32_16x16x32_bf16 v[160:163], a[144:147], v[80:83], v[160:163]\n"
        "s_waitcnt lgkmcnt(4)\n"
        "v_cvt_pk_bf16_f32 v143, v158, v159\n"
        "v_bfe_u32 v245, v254, 4, 5\n"
        "v_mfma_f32_16x16x32_bf16 v[164:167], a[148:151], v[80:83], v[164:167]\n"
        "s_cmp_lg_u32 s40, 0\n"
        "s_cbranch_scc1 LW1_%=\n"
        "s_mov_b64 exec, 0xffff\n"
        "global_store_dwordx2 v244, v[250:251], s[48:49] sc1\n"
        "s_mov_b64 exec, -1\n"
        "LW1_%=:\n"
        "v_mfma_f32_16x16x32_bf16 v[168:171], a[152:155], v[80:83], v[168:171]\n"
        "v_lshl_add_u32 v245, v245, 7, s80\n"
        "v_mov_b32 v244, v252\n"
        "v_mfma_f32_16x16x32_bf16 v[172:175], a[156:159], v[80:83], v[172:175]\n"
        "s_barrier\n"
        "v_exp_f32 v54, v54\n"
        "v_mfma_f32_16x16x32_bf16 v[176:179], a[128:131], v[80:83], v[176:179]\n"
        "v_exp_f32 v55, v55\n"
        "v_mfma_f32_16x16x32_bf16 v[180:183], a[132:135], v[80:83], v[180:183]\n"
        "v_cvt_pk_bf16_f32 v87, v54, v55\n"
        "v_cndmask_b32 v56, 0, v242, s[20:21]\n"
        "v_mfma_f32_16x16x32_bf16 v[52:55], a[124:127], v[76:79], v[228:231]\n"
        "v_exp_f32 v32, v32\n"
        "v_mfma_f32_16x16x32_bf16 v[52:55], a[120:123], v[72:75], v[52:55]\n"
        "v_exp_f32 v33, v33\n"
        "v_mfma_f32_16x16x32_bf16 v[52:55], a[116:119], v[68:71], v[52:55]\n"
        "v_cvt_pk_bf16_f32 v16, v32, v33\n"
        "v_cndmask_b32 v57, 0, v242, s[22:23]\n"
        "v_mfma_f32_16x16x32_bf16 v[52:55], a[112:115], v[64:67], v[52:55]\n"
        "v_exp_f32 v34, v34\n"
        "v_mfma_f32_16x16x32_bf16 a[68:71], a[80:83], v[84:87], a[68:71]\n"
        "v_exp_f32 v35, v35\n"
        "v_mfma_f32_16x16x32_bf16 v[192:195], a[144:147], v[84:87], v[192:195]\n"
        "v_cvt_pk_bf16_f32 v17, v34, v35\n"
        "v_cndmask_b32 v58, 0, v242, s[24:25]\n"
        "v_mfma_f32_16x16x32_bf16 v[196:199], a[148:151], v[84:87], v[196:199]\n"
        "s_waitcnt lgkmcnt(0)\n"
        "v_exp_f32 v36, v36\n"
        "v_mfma_f32_16x16x32_bf16 v[200:203], a[152:155], v[84:87], v[200:203]\n"
        "v_exp_f32 v37, v37\n"
        "v_mfma_f32_16x16x32_bf16 v[204:207], a[156:159], v[84:87], v[204:207]\n"
        "v_cvt_pk_bf16_f32 v20, v36, v37\n"
        "v_cndmask_b32 v59, 0, v242, s[28:29]\n"
        "s_nop 1\n"
        "v_mfma_f32_16x16x32_bf16 v[56:59], a[112:115], v[96:99], v[56:59]\n"
        "ds_read_b128 a[112:115], v240 offset:12288\n"
        "v_xor_b32 v241, 0x8000, v241\n"
        "v_mfma_f32_16x16x32_bf16 v[56:59], a[116:119], v[100:103], v[56:59]\n"
        "v_exp_f32 v38, v38\n"
        "v_mfma_f32_16x16x32_bf16 v[56:59], a[120:123], v[104:107], v[56:59]\n"
        "v_exp_f32 v39, v39\n"
        "v_mfma_f32_16x16x32_bf16 v[56:59], a[124:127], v[108:111], v[56:59]\n"
        "ds_read_b128 a[144:147], v241 offset:0\n"
        "v_cvt_pk_bf16_f32 v21, v38, v39\n"
        "v_mfma_f32_16x16x32_bf16 v[32:35], a[96:99], v[0:3], v[224:227]\n"
        "ds_read_b128 a[116:119], v240 offset:13312\n"
        "v_max_f32 v246, v40, v41\n"
        "v_mfma_f32_16x16x32_bf16 v[32:35], a[100:103], v[4:7], v[32:35]\n"
        "ds_read_b128 a[148:151], v241 offset:1024\n"
        "v_max3_f32 v246, v42, v43, v246\n"
        "v_mfma_f32_16x16x32_bf16 v[32:35], a[104:107], v[8:11], v[32:35]\n"
        "ds_read_b128 a[152:155], v241 offset:2048\n"
        "v_mov_b32 v247, v246\n"
        "v_mfma_f32_16x16x32_bf16 v[36:39], a[108:111], v[76:79], v[228:231]\n"
        "ds_read_b128 a[120:123], v240 offset:14336\n"
        "v_add_u32 v150, s52, v254\n"
        "v_mfma_f32_16x16x32_bf16 v[32:35], a[108:111], v[12:15], v[32:35]\n"
        "s_and_b32 s52, s41, 1\n"
        "v_permlane16_swap_b32 v246, v247\n"
        "v_mfma_f32_16x16x32_bf16 v[36:39], a[104:107], v[72:75], v[36:39]\n"
        "ds_read_b128 a[156:159], v241 offset:3072\n"
        "s_lshl_b32 s52, s52, 11\n"
        "v_max_f32 v246, v246, v247\n"
        "v_mfma_f32_16x16x32_bf16 v[36:39], a[100:103], v[68:71], v[36:39]\n"
        "ds_read_b128 a[124:127], v240 offset:15360\n"
        "v_mov_b32 v247, v246\n"
        "v_mfma_f32_16x16x32_bf16 v[36:39], a[96:99], v[64:67], v[36:39]\n"
        "v_exp_f32 v48, v48\n"
        "v_mfma_f32_16x16x32_bf16 v[208:211], a[128:131], v[84:87], v[208:211]\n"
        "v_permlane32_swap_b32 v246, v247\n"
        "v_mfma_f32_16x16x32_bf16 v[212:215], a[132:135], v[84:87], v[212:215]\n"
        "ds_read_b128 a[128:131], v241 offset:4096\n"
        "v_max_f32 v246, v246, v247\n"
        "v_mfma_f32_16x16x32_bf16 v[216:219], a[136:139], v[84:87], v[216:219]\n"
        "ds_read_b128 a[132:135], v241 offset:5120\n"
        "v_sub_f32 v232, 0, v246\n"
        "v_mfma_f32_16x16x32_bf16 v[220:223], a[140:143], v[84:87], v[220:223]\n"
        "v_sub_f32 v233, 0, v246\n"
        "v_sub_f32 v234, 0, v246\n"
        "v_mfma_f32_16x16x32_bf16 v[184:187], a[136:139], v[80:83], v[184:187]\n"
        "ds_read_b128 a[136:139], v241 offset:6144\n"
        "v_sub_f32 v235, 0, v246\n"
        "v_mfma_f32_16x16x32_bf16 v[188:191], a[140:143], v[80:83], v[188:191]\n"
        "v_add_f32 v40, v40, v232\n"
        "v_exp_f32 v40, v40\n"
        "v_add_f32 v41, v41, v232\n"
        "v_exp_f32 v41, v41\n"
        "v_add_f32 v42, v42, v232\n"
        "v_cvt_pk_bf16_f32 v24, v40, v41\n"
        "v_exp_f32 v42, v42\n"
        "v_add_f32 v43, v43, v232\n"
        "v_exp_f32 v43, v43\n"
        "v_exp_f32 v49, v49\n"
        "v_cvt_pk_bf16_f32 v25, v42, v43\n"
        "v_cvt_pk_bf16_f32 v18, v48, v49\n"
        "v_cndmask_b32 v40, v232, v242, s[30:31]\n"
        "v_cndmask_b32 v41, v232, v242, s[56:57]\n"
        "v_cndmask_b32 v42, v232, v242, s[58:59]\n"
        "v_cndmask_b32 v43, v232, v242, s[60:61]\n"
        "s_nop 1\n"
        "v_mfma_f32_16x16x32_bf16 v[40:43], a[96:99], v[96:99], v[40:43]\n"
        "v_exp_f32 v50, v50\n"
        "v_mfma_f32_16x16x32_bf16 v[40:43], a[100:103], v[100:103], v[40:43]\n"
        "ds_read_b128 a[100:103], v240 offset:17408\n"
        "v_add_f32 v56, v56, v232\n"
        "v_mfma_f32_16x16x32_bf16 v[40:43], a[104:107], v[104:107], v[40:43]\n"
        "ds_read_b128 a[104:107], v240 offset:18432\n"
        "v_add_f32 v57, v57, v232\n"
        "v_mfma_f32_16x16x32_bf16 v[40:43], a[108:111], v[108:111], v[40:43]\n"
        "ds_read_b128 a[108:111], v240 offset:19456\n"
        "v_exp_f32 v51, v51\n"
        "v_exp_f32 v52, v52\n"
        "v_cvt_pk_bf16_f32 v19, v50, v51\n"
        "s_waitcnt lgkmcnt(3)\n"
        "v_mfma_f32_16x16x32_bf16 v[48:51], a[112:115], v[0:3], v[224:227]\n"
        "v_exp_f32 v53, v53\n"
        "v_mfma_f32_16x16x32_bf16 v[48:51], a[116:119], v[4:7], v[48:51]\n"
        "ds_read_b128 a[96:99], v240 offset:16384\n"
        "v_cvt_pk_bf16_f32 v22, v52, v53\n"
        "v_mfma_f32_16x16x32_bf16 v[48:51], a[120:123], v[8:11], v[48:51]\n"
        "v_exp_f32 v36, v36\n"
        "v_mfma_f32_16x16x32_bf16 v[48:51], a[124:127], v[12:15], v[48:51]\n"
        "v_exp_f32 v37, v37\n"
        "v_mfma_f32_16x16x32_bf16 a[64:67], a[80:83], v[16:19], a[64:67]\n"
        "v_exp_f32 v38, v38\n"
        "v_mfma_f32_16x16x32_bf16 v[160:163], a[144:147], v[16:19], v[160:163]\n"
        "v_exp_f32 v39, v39\n"
        "v_mfma_f32_16x16x32_bf16 v[164:167], a[148:151], v[16:19], v[164:167]\n"
        "ds_read_b128 a[140:143], v241 offset:7168\n"
        "v_cvt_pk_bf16_f32 v84, v36, v37\n"
        "v_mfma_f32_16x16x32_bf16 v[168:171], a[152:155], v[16:19], v[168:171]\n"
        "v_cvt_pk_bf16_f32 v85, v38, v39\n"
        "s_waitcnt lgkmcnt(2)\n"
        "v_mfma_f32_16x16x32_bf16 v[36:39], a[108:111], v[76:79], v[228:231]\n"
        "v_exp_f32 v54, v54\n"
        "v_mfma_f32_16x16x32_bf16 v[36:39], a[104:107], v[72:75], v[36:39]\n"
        "v_exp_f32 v55, v55\n"
        "v_mfma_f32_16x16x32_bf16 v[36:39], a[100:103], v[68:71], v[36:39]\n"
        "global_load_dwordx2 v[252:253], v245, s[48:49] sc1\n"
        "v_cvt_pk_bf16_f32 v23, v54, v55\n"
        "v_mfma_f32_16x16x32_bf16 v[52:55], a[124:127], v[76:79], v[228:231]\n"
        "v_exp_f32 v56, v56\n"
        "v_mfma_f32_16x16x32_bf16 v[52:55], a[120:123], v[72:75], v[52:55]\n"
        "v_exp_f32 v57, v57\n"
        "v_mfma_f32_16x16x32_bf16 v[52:55], a[116:119], v[68:71], v[52:55]\n"
        "s_waitcnt vmcnt(8) lgkmcnt(1)\n"
        "v_cvt_pk_bf16_f32 v26, v56, v57\n"
        "v_add_f32 v58, v58, v232\n"
        "v_mfma_f32_16x16x32_bf16 v[52:55], a[112:115], v[64:67], v[52:55]\n"
        "v_exp_f32 v58, v58\n"
        "v_mfma_f32_16x16x32_bf16 a[68:71], a[80:83], v[20:23], a[68:71]\n"
        "v_add_f32 v59, v59, v232\n"
        "v_cndmask_b32 v56, v232, v242, s[62:63]\n"
        "v_mfma_f32_16x16x32_bf16 v[192:195], a[144:147], v[20:23], v[192:195]\n"
        "v_exp_f32 v59, v59\n"
        "v_mfma_f32_16x16x32_bf16 v[196:199], a[148:151], v[20:23], v[196:199]\n"
        "v_cvt_pk_bf16_f32 v27, v58, v59\n"
        "s_nop 1\n"
        "v_mfma_f32_16x16x32_bf16 a[72:75], a[80:83], v[24:27], 0\n"
        "v_exp_f32 v32, v32\n"
        "v_mfma_f32_16x16x32_bf16 a[0:3], a[144:147], v[24:27], 0\n"
        "v_exp_f32 v33, v33\n"
        "v_mfma_f32_16x16x32_bf16 a[4:7], a[148:151], v[24:27], 0\n"
        "ds_read_b128 a[144:147], v241 offset:8192\n"
        "v_cvt_pk_bf16_f32 v80, v32, v33\n"
        "v_mfma_f32_16x16x32_bf16 v[200:203], a[152:155], v[20:23], v[200:203]\n"
        "v_exp_f32 v34, v34\n"
        "v_mfma_f32_16x16x32_bf16 a[8:11], a[152:155], v[24:27], 0\n"
        "v_exp_f32 v35, v35\n"
        "v_mfma_f32_16x16x32_bf16 a[12:15], a[156:159], v[24:27], 0\n"
        "ds_read_b128 a[148:151], v241 offset:9216\n"
        "v_cvt_pk_bf16_f32 v81, v34, v35\n"
        "v_mfma_f32_16x16x32_bf16 v[204:207], a[156:159], v[20:23], v[204:207]\n"
        "ds_read_b128 a[152:155], v241 offset:10240\n"
        "v_cndmask_b32 v57, v232, v242, s[84:85]\n"
        "v_mfma_f32_16x16x32_bf16 v[172:175], a[156:159], v[16:19], v[172:175]\n"
        "ds_read_b128 a[156:159], v241 offset:11264\n"
        "v_cndmask_b32 v58, v232, v242, s[86:87]\n"
        "v_mfma_f32_16x16x32_bf16 v[32:35], a[96:99], v[0:3], v[224:227]\n"
        "v_cndmask_b32 v59, v232, v242, s[88:89]\n"
        "s_nop 1\n"
        "v_mfma_f32_16x16x32_bf16 v[56:59], a[112:115], v[96:99], v[56:59]\n"
        "ds_read_b128 a[112:115], v240 offset:20480\n"
        "v_readfirstlane_b32 s53, v112\n"
        "v_mfma_f32_16x16x32_bf16 v[56:59], a[116:119], v[100:103], v[56:59]\n"
        "s_waitcnt vmcnt(7) lgkmcnt(4)\n"
        "v_exp_f32 v40, v40\n"
        "v_mfma_f32_16x16x32_bf16 v[56:59], a[120:123], v[104:107], v[56:59]\n"
        "s_lshl_b32 s53, s53, 16\n"
        "v_exp_f32 v41, v41\n"
        "v_mfma_f32_16x16x32_bf16 v[56:59], a[124:127], v[108:111], v[56:59]\n"
        "ds_read_b128 a[116:119], v240 offset:21504\n"
        "v_cvt_pk_bf16_f32 v88, v40, v41\n"
        "v_mfma_f32_16x16x32_bf16 v[32:35], a[100:103], v[4:7], v[32:35]\n"
        "v_exp_f32 v42, v42\n"
        "v_mfma_f32_16x16x32_bf16 v[32:35], a[104:107], v[8:11], v[32:35]\n"
        "v_exp_f32 v43, v43\n"
        "v_mfma_f32_16x16x32_bf16 v[32:35], a[108:111], v[12:15], v[32:35]\n"
        "ds_read_b128 a[120:123], v240 offset:22528\n"
        "v_cvt_pk_bf16_f32 v89, v42, v43\n"
        "v_mfma_f32_16x16x32_bf16 v[36:39], a[96:99], v[64:67], v[36:39]\n"
        "v_exp_f32 v48, v48\n"
        "v_mfma_f32_16x16x32_bf16 v[40:43], a[96:99], v[96:99], v[232:235]\n"
        "v_exp_f32 v49, v49\n"
        "v_mfma_f32_16x16x32_bf16 v[40:43], a[100:103], v[100:103], v[40:43]\n"
        "ds_read_b128 a[124:127], v240 offset:23552\n"
        "v_cvt_pk_bf16_f32 v82, v48, v49\n"
        "v_mfma_f32_16x16x32_bf16 v[40:43], a[104:107], v[104:107], v[40:43]\n"
        "v_exp_f32 v50, v50\n"
        "v_mfma_f32_16x16x32_bf16 v[40:43], a[108:111], v[108:111], v[40:43]\n"
        "v_exp_f32 v51, v51\n"
        "v_mfma_f32_16x16x32_bf16 v[176:179], a[128:131], v[16:19], v[176:179]\n"
        "v_cvt_pk_bf16_f32 v83, v50, v51\n"
        "v_mov_b32 v155, 0\n"
        "v_mfma_f32_16x16x32_bf16 v[208:211], a[128:131], v[20:23], v[208:211]\n"
        "s_waitcnt vmcnt(6) lgkmcnt(2)\n"
        "v_writelane_b32 v155, s53, 1\n"
        "s_lshl2_add_u32 s53, s42, s40\n"
        "v_mfma_f32_16x16x32_bf16 a[16:19], a[128:131], v[24:27], 0\n"
        "s_add_u32 s52, s52, s53\n"
        "s_add_u32 s52, s52, 1920\n"
        "s_lshr_b32 s53, s41, 2\n"
        "v_mfma_f32_16x16x32_bf16 a[20:23], a[132:135], v[24:27], 0\n"
        "s_lshl1_add_u32 s52, s52, s53\n"
        "s_lshl_b32 s52, s52, 10\n"
        "v_cndmask_b32 v44, 0, v242, s[10:11]\n"
        "v_mfma_f32_16x16x32_bf16 v[212:215], a[132:135], v[20:23], v[212:215]\n"
        "s_add_u32 s26, s18, s52\n"
        "s_addc_u32 s27, s19, 0\n"
        "v_cndmask_b32 v45, 0, v242, s[12:13]\n"
        "v_mfma_f32_16x16x32_bf16 v[180:183], a[132:135], v[16:19], v[180:183]\n"
        "s_waitcnt vmcnt(5) lgkmcnt(0)\n"
        "v_exp_f32 v52, v52\n"
        "v_mfma_f32_16x16x32_bf16 v[184:187], a[136:139], v[16:19], v[184:187]\n"
        "LHA11_%=:\n"
        "s_cbranch_execz LHRA_%=\n"
        "v_exp_f32 v53, v53\n"
        "v_mfma_f32_16x16x32_bf16 v[216:219], a[136:139], v[20:23], v[216:219]\n"
        "ds_read_b128 a[128:131], v241 offset:12288\n"
        "v_cvt_pk_bf16_f32 v86, v52, v53\n"
        "v_mfma_f32_16x16x32_bf16 a[24:27], a[136:139], v[24:27], 0\n"
        "ds_read_b128 a[132:135], v241 offset:13312\n"
        "v_cndmask_b32 v46, 0, v242, s[14:15]\n"
        "v_mfma_f32_16x16x32_bf16 a[28:31], a[140:143], v[24:27], 0\n"
        "ds_read_b128 a[136:139], v241 offset:14336\n"
        "v_cndmask_b32 v47, 0, v242, s[16:17]\n"
        "s_nop 1\n"
        "v_mfma_f32_16x16x32_bf16 v[44:47], a[108:111], v[140:143], v[44:47]\n"
        "v_exp_f32 v54, v54\n"
        "v_mfma_f32_16x16x32_bf16 v[44:47], a[104:107], v[136:139], v[44:47]\n"
        "v_exp_f32 v55, v55\n"
        "v_mfma_f32_16x16x32_bf16 v[44:47], a[100:103], v[132:135], v[44:47]\n"
        "ds_read_b128 a[100:103], v240 offset:25600\n"
        "v_cvt_pk_bf16_f32 v87, v54, v55\n"
        "v_mfma_f32_16x16x32_bf16 v[44:47], a[96:99], v[128:131], v[44:47]\n"
        "ds_read_b128 a[96:99], v240 offset:24576\n"
        "v_mov_b32 v152, 0\n"
        "s_waitcnt vmcnt(0)\n"
        "v_mfma_f32_16x16x32_bf16 v[220:223], a[140:143], v[20:23], v[220:223]\n"
        "ds_read_b128 a[104:107], v240 offset:26624\n"
        "v_cmp_ne_u64 vcc, v[252:253], v[250:251]\n"
        "v_mfma_f32_16x16x32_bf16 v[188:191], a[140:143], v[16:19], v[188:191]\n"
        "s_cbranch_vccnz LPKV_%=\n"
        "LBKV_%=:\n"
        "v_and_b32 v245, 0x70, v254\n"
        "v_lshlrev_b32 v245, 3, v245\n"
        "v_mfma_f32_16x16x32_bf16 v[48:51], a[112:115], v[0:3], v[224:227]\n"
        "s_cmp_eq_u32 s82, s83\n"
        "s_cselect_b64 s[68:69], s[92:93], s[68:69]\n"
        "s_cselect_b32 s80, s50, s78\n"
        "v_mfma_f32_16x16x32_bf16 v[48:51], a[116:119], v[4:7], v[48:51]\n"
        "s_add_u32 m0, s80, 0x0\n"
        "v_exp_f32 v56, v56\n"
        "v_mfma_f32_16x16x32_bf16 v[48:51], a[120:123], v[8:11], v[48:51]\n"
        "global_load_lds_dwordx4 v254, s[68:69] offset:0 sc1\n"
        "ds_read_b128 a[140:143], v241 offset:15360\n"
        "s_add_u32 s82, s82, 1\n"
        "v_mfma_f32_16x16x32_bf16 v[48:51], a[124:127], v[12:15], v[48:51]\n"
        "s_xor_b32 s78, s78, 0x8000\n"
        "v_exp_f32 v57, v57\n"
        "v_mfma_f32_16x16x32_bf16 v[52:55], a[124:127], v[76:79], v[228:231]\n"
        "ds_read_b128 a[108:111], v240 offset:27648\n"
        "v_cvt_pk_bf16_f32 v90, v56, v57\n"
        "v_mfma_f32_16x16x32_bf16 v[52:55], a[120:123], v[72:75], v[52:55]\n"
        "v_exp_f32 v58, v58\n"
        "v_mfma_f32_16x16x32_bf16 v[52:55], a[116:119], v[68:71], v[52:55]\n"
        "global_load_lds_dwordx4 v254, s[68:69] offset:1024 sc1\n"
        "v_mov_b32 v153, 0\n"
        "v_mfma_f32_16x16x32_bf16 v[52:55], a[112:115], v[64:67], v[52:55]\n"
        "v_exp_f32 v59, v59\n"
        "v_mfma_f32_16x16x32_bf16 a[64:67], a[80:83], v[80:83], a[64:67]\n"
        "s_cmp_ge_u32 s82, s44\n"
        "s_addc_u32 s90, s82, 0\n"
        "v_cvt_pk_bf16_f32 v91, v58, v59\n"
        "v_mfma_f32_16x16x32_bf16 v[56:59], a[112:115], v[96:99], v[232:235]\n"
        "s_min_u32 s90, s90, 15\n"
        "v_exp_f32 v32, v32\n"
        "v_mfma_f32_16x16x32_bf16 v[56:59], a[116:119], v[100:103], v[56:59]\n"
        "global_load_lds_dwordx4 v254, s[68:69] offset:2048 sc1\n"
        "s_lshl_b32 s90, s90, 15\n"
        "v_mov_b32 v154, 0\n"
        "v_mfma_f32_16x16x32_bf16 v[56:59], a[120:123], v[104:107], v[56:59]\n"
        "v_exp_f32 v33, v33\n"
        "v_mfma_f32_16x16x32_bf16 v[56:59], a[124:127], v[108:111], v[56:59]\n"
        "v_cvt_pk_bf16_f32 v16, v32, v33\n"
        "v_alignbyte_b32 v112, v113, v112, 2\n"
        "v_mfma_f32_16x16x32_bf16 a[68:71], a[80:83], v[84:87], a[68:71]\n"
        "s_mov_b64 exec, 3\n"
        "ds_write_b128 v150, v[152:155]\n"
        "s_mov_b64 exec, -1\n"
        "v_mfma_f32_16x16x32_bf16 a[72:75], a[80:83], v[88:91], a[72:75]\n"
        "global_load_lds_dwordx4 v254, s[68:69] offset:3072 sc1\n"
        "s_add_u32 m0, s81, 0x0\n"
        "v_alignbyte_b32 v113, v114, v113, 2\n"
        "v_mfma_f32_16x16x32_bf16 v[160:163], a[144:147], v[80:83], v[160:163]\n"
        "s_add_u32 s68, s64, s90\n"
        "s_addc_u32 s69, s65, 0\n"
        "v_alignbyte_b32 v114, v115, v114, 2\n"
        "v_mfma_f32_16x16x32_bf16 v[192:195], a[144:147], v[84:87], v[192:195]\n"
        "s_add_u32 s81, s78, 4096\n"
        "v_alignbyte_b32 v115, v144, v115, 2\n"
        "v_alignbyte_b32 v116, v117, v116, 2\n"
        "v_mfma_f32_16x16x32_bf16 a[0:3], a[144:147], v[88:91], a[0:3]\n"
        "ds_write_b128 v150, v[112:115] offset:32\n"
        "v_mfma_f32_16x16x32_bf16 a[4:7], a[148:151], v[88:91], a[4:7]\n"
        "global_load_lds_dwordx4 v254, s[74:75] offset:0 sc1\n"
        "v_alignbyte_b32 v117, v118, v117, 2\n"
        "v_mfma_f32_16x16x32_bf16 v[196:199], a[148:151], v[84:87], v[196:199]\n"
        "ds_read_b128 a[144:147], v241 offset:16384\n"
        "v_alignbyte_b32 v118, v119, v118, 2\n"
        "v_mfma_f32_16x16x32_bf16 v[164:167], a[148:151], v[80:83], v[164:167]\n"
        "ds_read_b128 a[148:151], v241 offset:17408\n"
        "v_alignbyte_b32 v119, v145, v119, 2\n"
        "v_mfma_f32_16x16x32_bf16 v[168:171], a[152:155], v[80:83], v[168:171]\n"
        "ds_write_b128 v150, v[116:119] offset:1056\n"
        "v_mfma_f32_16x16x32_bf16 v[200:203], a[152:155], v[84:87], v[200:203]\n"
        "global_load_lds_dwordx4 v254, s[74:75] offset:1024 sc1\n"
        "v_alignbyte_b32 v120, v121, v120, 2\n"
        "v_mfma_f32_16x16x32_bf16 a[8:11], a[152:155], v[88:91], a[8:11]\n"
        "s_waitcnt lgkmcnt(4)\n"
        "v_alignbyte_b32 v121, v122, v121, 2\n"
        "v_alignbyte_b32 v122, v123, v122, 2\n"
        "v_mfma_f32_16x16x32_bf16 a[12:15], a[156:159], v[88:91], a[12:15]\n"
        "ds_read_b128 a[152:155], v241 offset:18432\n"
        "v_alignbyte_b32 v123, v146, v123, 2\n"
        "v_mfma_f32_16x16x32_bf16 v[204:207], a[156:159], v[84:87], v[204:207]\n"
        "ds_write_b128 v150, v[120:123] offset:2080\n"
        "v_mfma_f32_16x16x32_bf16 v[172:175], a[156:159], v[80:83], v[172:175]\n"
        "global_load_lds_dwordx4 v254, s[74:75] offset:2048 sc1\n"
        "v_alignbyte_b32 v124, v125, v124, 2\n"
        "v_mfma_f32_16x16x32_bf16 v[176:179], a[128:131], v[80:83], v[176:179]\n"
        "ds_read_b128 a[156:159], v241 offset:19456\n"
        "v_alignbyte_b32 v125, v126, v125, 2\n"
        "v_mfma_f32_16x16x32_bf16 v[208:211], a[128:131], v[84:87], v[208:211]\n"
        "v_alignbyte_b32 v126, v127, v126, 2\n"
        "v_alignbyte_b32 v127, v147, v127, 2\n"
        "v_mfma_f32_16x16x32_bf16 a[16:19], a[128:131], v[88:91], a[16:19]\n"
        "ds_write_b128 v150, v[124:127] offset:3104\n"
        "v_mfma_f32_16x16x32_bf16 a[20:23], a[132:135], v[88:91], a[20:23]\n"
        "global_load_lds_dwordx4 v254, s[74:75] offset:3072 sc1\n"
        "s_add_u32 m0, s79, 0x0\n"
        "v_cndmask_b32 v60, 0, v242, s[20:21]\n"
        "v_mfma_f32_16x16x32_bf16 v[212:215], a[132:135], v[84:87], v[212:215]\n"
        "v_exp_f32 v34, v34\n"
        "v_mfma_f32_16x16x32_bf16 v[180:183], a[132:135], v[80:83], v[180:183]\n"
        "v_exp_f32 v35, v35\n"
        "v_mfma_f32_16x16x32_bf16 v[184:187], a[136:139], v[80:83], v[184:187]\n"
        "ds_read_b128 a[128:131], v241 offset:20480\n"
        "v_cvt_pk_bf16_f32 v17, v34, v35\n"
        "v_mfma_f32_16x16x32_bf16 v[32:35], a[96:99], v[0:3], v[224:227]\n"
        "v_exp_f32 v36, v36\n"
        "v_mfma_f32_16x16x32_bf16 v[32:35], a[100:103], v[4:7], v[32:35]\n"
        "v_exp_f32 v37, v37\n"
        "v_mfma_f32_16x16x32_bf16 v[32:35], a[104:107], v[8:11], v[32:35]\n"
        "global_load_lds_dwordx4 v254, s[76:77] offset:0 sc1\n"
        "v_cvt_pk_bf16_f32 v20, v36, v37\n"
        "v_mfma_f32_16x16x32_bf16 v[32:35], a[108:111], v[12:15], v[32:35]\n"
        "v_exp_f32 v38, v38\n"
        "v_mfma_f32_16x16x32_bf16 v[216:219], a[136:139], v[84:87], v[216:219]\n"
        "v_exp_f32 v39, v39\n"
        "v_mfma_f32_16x16x32_bf16 a[24:27], a[136:139], v[88:91], a[24:27]\n"
        "ds_read_b128 a[132:135], v241 offset:21504\n"
        "v_cvt_pk_bf16_f32 v21, v38, v39\n"
        "v_mfma_f32_16x16x32_bf16 v[36:39], a[108:111], v[76:79], v[228:231]\n"
        "global_load_lds_dwordx4 v254, s[76:77] offset:1024 sc1\n"
        "v_cndmask_b32 v61, 0, v242, s[22:23]\n"
        "v_mfma_f32_16x16x32_bf16 v[36:39], a[104:107], v[72:75], v[36:39]\n"
        "ds_read_b128 a[136:139], v241 offset:22528\n"
        "v_cndmask_b32 v62, 0, v242, s[24:25]\n"
        "v_mfma_f32_16x16x32_bf16 v[36:39], a[100:103], v[68:71], v[36:39]\n"
        "s_add_u32 s74, s68, 0x1000\n"
        "s_addc_u32 s75, s69, 0\n"
        "v_cndmask_b32 v63, 0, v242, s[28:29]\n"
        "s_nop 1\n"
        "v_mfma_f32_16x16x32_bf16 v[60:63], a[124:127], v[140:143], v[60:63]\n"
        "v_exp_f32 v40, v40\n"
        "v_mfma_f32_16x16x32_bf16 v[60:63], a[120:123], v[136:139], v[60:63]\n"
        "v_exp_f32 v41, v41\n"
        "v_mfma_f32_16x16x32_bf16 v[60:63], a[116:119], v[132:135], v[60:63]\n"
        "ds_read_b128 a[116:119], v240 offset:29696\n"
        "v_cvt_pk_bf16_f32 v24, v40, v41\n"
        "v_mfma_f32_16x16x32_bf16 v[60:63], a[112:115], v[128:131], v[60:63]\n"
        "ds_read_b128 a[112:115], v240 offset:28672\n"
        "v_max_f32 v248, v44, v45\n"
        "v_mfma_f32_16x16x32_bf16 v[36:39], a[96:99], v[64:67], v[36:39]\n"
        "v_exp_f32 v42, v42\n"
        "v_mfma_f32_16x16x32_bf16 a[28:31], a[140:143], v[88:91], a[28:31]\n"
        "v_exp_f32 v43, v43\n"
        "v_mfma_f32_16x16x32_bf16 v[220:223], a[140:143], v[84:87], v[220:223]\n"
        "ds_read_b128 a[120:123], v240 offset:30720\n"
        "v_cvt_pk_bf16_f32 v25, v42, v43\n"
        "v_mfma_f32_16x16x32_bf16 v[40:43], a[96:99], v[96:99], v[232:235]\n"
        "ds_read_b128 a[124:127], v240 offset:31744\n"
        "v_max3_f32 v248, v46, v47, v248\n"
        "v_mfma_f32_16x16x32_bf16 v[40:43], a[100:103], v[100:103], v[40:43]\n"
        "global_load_lds_dwordx4 v254, s[76:77] offset:2048 sc1\n"
        "v_mov_b32 v249, v248\n"
        "v_mfma_f32_16x16x32_bf16 v[40:43], a[104:107], v[104:107], v[40:43]\n"
        "v_exp_f32 v48, v48\n"
        "v_mfma_f32_16x16x32_bf16 v[40:43], a[108:111], v[108:111], v[40:43]\n"
        "v_permlane16_swap_b32 v248, v249\n"
        "v_mfma_f32_16x16x32_bf16 v[188:191], a[140:143], v[80:83], v[188:191]\n"
        "v_max_f32 v248, v248, v249\n"
        "v_mov_b32 v249, v248\n"
        "v_exp_f32 v49, v49\n"
        "v_exp_f32 v50, v50\n"
        "v_permlane32_swap_b32 v248, v249\n"
        "v_max_f32 v248, v248, v249\n"
        "v_sub_f32 v236, 0, v248\n"
        "v_sub_f32 v237, 0, v248\n"
        "v_sub_f32 v238, 0, v248\n"
        "v_sub_f32 v239, 0, v248\n"
        "v_add_f32 v44, v44, v236\n"
        "v_exp_f32 v44, v44\n"
        "v_add_f32 v45, v45, v236\n"
        "v_exp_f32 v45, v45\n"
        "v_add_f32 v46, v46, v236\n"
        "v_cvt_pk_bf16_f32 v28, v44, v45\n"
        "v_exp_f32 v46, v46\n"
        "v_add_f32 v47, v47, v236\n"
        "v_exp_f32 v47, v47\n"
        "v_cvt_pk_bf16_f32 v18, v48, v49\n"
        "v_cvt_pk_bf16_f32 v29, v46, v47\n"
        "v_exp_f32 v51, v51\n"
        "v_exp_f32 v52, v52\n"
        "v_cvt_pk_bf16_f32 v19, v50, v51\n"
        "s_waitcnt lgkmcnt(0)\n"
        "v_mfma_f32_16x16x32_bf16 v[48:51], a[112:115], v[0:3], v[224:227]\n"
        "v_exp_f32 v53, v53\n"
        "v_mfma_f32_16x16x32_bf16 v[48:51], a[116:119], v[4:7], v[48:51]\n"
        "global_load_lds_dwordx4 v254, s[76:77] offset:3072 sc1\n"
        "s_add_u32 m0, s79, 0x1000\n"
        "v_cvt_pk_bf16_f32 v22, v52, v53\n"
        "v_mfma_f32_16x16x32_bf16 v[48:51], a[120:123], v[8:11], v[48:51]\n"
        "v_exp_f32 v54, v54\n"
        "v_mfma_f32_16x16x32_bf16 v[48:51], a[124:127], v[12:15], v[48:51]\n"
        "v_exp_f32 v55, v55\n"
        "v_mfma_f32_16x16x32_bf16 a[64:67], a[80:83], v[16:19], a[64:67]\n"
        "ds_read_b128 a[140:143], v241 offset:23552\n"
        "v_cvt_pk_bf16_f32 v23, v54, v55\n"
        "v_mfma_f32_16x16x32_bf16 v[52:55], a[124:127], v[76:79], v[228:231]\n"
        "global_load_dword v253, v245, s[26:27] offset:0\n"
        "v_cndmask_b32 v44, v236, v242, s[30:31]\n"
        "v_mfma_f32_16x16x32_bf16 v[52:55], a[120:123], v[72:75], v[52:55]\n"
        "s_add_u32 s76, s76, 0x1000\n"
        "s_addc_u32 s77, s77, 0\n"
        "v_cndmask_b32 v45, v236, v242, s[56:57]\n"
        "v_mfma_f32_16x16x32_bf16 v[52:55], a[116:119], v[68:71], v[52:55]\n"
        "global_load_lds_dwordx4 v254, s[76:77] offset:0 sc1\n"
        "v_cndmask_b32 v46, v236, v242, s[58:59]\n"
        "v_mfma_f32_16x16x32_bf16 v[52:55], a[112:115], v[64:67], v[52:55]\n"
        "s_sub_u32 s26, s26, 262144\n"
        "s_subb_u32 s27, s27, 0\n"
        "v_cndmask_b32 v47, v236, v242, s[60:61]\n"
        "s_nop 1\n"
        "v_mfma_f32_16x16x32_bf16 v[44:47], a[108:111], v[140:143], v[44:47]\n"
        "v_exp_f32 v56, v56\n"
        "v_mfma_f32_16x16x32_bf16 v[44:47], a[104:107], v[136:139], v[44:47]\n"
        "v_exp_f32 v57, v57\n"
        "v_mfma_f32_16x16x32_bf16 v[44:47], a[100:103], v[132:135], v[44:47]\n"
        "global_load_lds_dwordx4 v254, s[76:77] offset:1024 sc1\n"
        "v_cvt_pk_bf16_f32 v26, v56, v57\n"
        "v_mfma_f32_16x16x32_bf16 v[44:47], a[96:99], v[128:131], v[44:47]\n"
        "v_xor_b32 v240, 0x8000, v240\n"
        "v_add_f32 v60, v60, v236\n"
        "v_mfma_f32_16x16x32_bf16 a[68:71], a[80:83], v[20:23], a[68:71]\n"
        "s_cmp_eq_u32 s82, s83\n"
        "s_cselect_b64 s[68:69], s[92:93], s[68:69]\n"
        "s_cselect_b32 s80, s50, s78\n"
        "v_mfma_f32_16x16x32_bf16 v[160:163], a[144:147], v[16:19], v[160:163]\n"
        "s_cmp_eq_u32 s82, s83\n"
        "s_cselect_b64 s[74:75], s[94:95], s[74:75]\n"
        "s_cselect_b32 s81, s51, s81\n"
        "v_mfma_f32_16x16x32_bf16 v[192:195], a[144:147], v[20:23], v[192:195]\n"
        "global_load_lds_dwordx4 v254, s[76:77] offset:2048 sc1\n"
        "v_add_f32 v61, v61, v236\n"
        "v_mfma_f32_16x16x32_bf16 v[196:199], a[148:151], v[20:23], v[196:199]\n"
        "v_exp_f32 v58, v58\n"
        "v_mfma_f32_16x16x32_bf16 v[164:167], a[148:151], v[16:19], v[164:167]\n"
        "v_exp_f32 v59, v59\n"
        "v_mfma_f32_16x16x32_bf16 v[168:171], a[152:155], v[16:19], v[168:171]\n"
        "v_cvt_pk_bf16_f32 v27, v58, v59\n"
        "v_add_f32 v62, v62, v236\n"
        "v_mfma_f32_16x16x32_bf16 v[56:59], a[112:115], v[96:99], v[232:235]\n"
        "global_load_lds_dwordx4 v254, s[76:77] offset:3072 sc1\n"
        "s_add_u32 m0, s80, 0x0\n"
        "v_add_f32 v63, v63, v236\n"
        "v_mfma_f32_16x16x32_bf16 v[56:59], a[116:119], v[100:103], v[56:59]\n"
        "v_exp_f32 v60, v60\n"
        "v_mfma_f32_16x16x32_bf16 v[56:59], a[120:123], v[104:107], v[56:59]\n"
        "v_exp_f32 v61, v61\n"
        "v_mfma_f32_16x16x32_bf16 v[56:59], a[124:127], v[108:111], v[56:59]\n"
        "s_waitcnt vmcnt(9)\n"
        "s_barrier\n"
        "v_mfma_f32_16x16x32_bf16 a[72:75], a[80:83], v[24:27], a[72:75]\n"
        "global_load_lds_dwordx4 v254, s[68:69] offset:0 sc1\n"
        "ds_read_b128 a[96:99], v240 offset:0\n"
        "v_mfma_f32_16x16x32_bf16 a[0:3], a[144:147], v[24:27], a[0:3]\n"
        "ds_read_b128 a[100:103], v240 offset:1024\n"
        "v_cvt_pk_bf16_f32 v30, v60, v61\n"
        "v_mfma_f32_16x16x32_bf16 a[4:7], a[148:151], v[24:27], a[4:7]\n"
        "v_exp_f32 v62, v62\n"
        "v_mfma_f32_16x16x32_bf16 v[200:203], a[152:155], v[20:23], v[200:203]\n"
        "v_exp_f32 v63, v63\n"
        "v_mfma_f32_16x16x32_bf16 a[8:11], a[152:155], v[24:27], a[8:11]\n"
        "global_load_lds_dwordx4 v254, s[68:69] offset:1024 sc1\n"
        "v_cvt_pk_bf16_f32 v31, v62, v63\n"
        "s_nop 1\n"
        "v_mfma_f32_16x16x32_bf16 a[76:79], a[80:83], v[28:31], 0\n"
        "ds_read_b128 a[104:107], v240 offset:2048\n"
        "v_cndmask_b32 v60, v236, v242, s[62:63]\n"
        "v_mfma_f32_16x16x32_bf16 a[32:35], a[144:147], v[28:31], 0\n"
        "v_exp_f32 v32, v32\n"
        "v_mfma_f32_16x16x32_bf16 a[36:39], a[148:151], v[28:31], 0\n"
        "v_exp_f32 v33, v33\n"
        "v_mfma_f32_16x16x32_bf16 a[40:43], a[152:155], v[28:31], 0\n"
        "global_load_lds_dwordx4 v254, s[68:69] offset:2048 sc1\n"
        "v_cvt_pk_bf16_f32 v80, v32, v33\n"
        "v_mfma_f32_16x16x32_bf16 a[44:47], a[156:159], v[28:31], 0\n"
        "s_waitcnt lgkmcnt(2)\n"
        "v_exp_f32 v34, v34\n"
        "v_mfma_f32_16x16x32_bf16 a[12:15], a[156:159], v[24:27], a[12:15]\n"
        "v_exp_f32 v35, v35\n"
        "v_mfma_f32_16x16x32_bf16 v[204:207], a[156:159], v[20:23], v[204:207]\n"
        "ds_read_b128 a[108:111], v240 offset:3072\n"
        "v_cvt_pk_bf16_f32 v81, v34, v35\n"
        "v_mfma_f32_16x16x32_bf16 v[172:175], a[156:159], v[16:19], v[172:175]\n"
        "global_load_dword v253, v245, s[26:27] offset:0\n"
        "s_sub_u32 s26, s26, 262144\n"
        "s_subb_u32 s27, s27, 0\n"
        "global_load_lds_dwordx4 v254, s[68:69] offset:3072 sc1\n"
        "v_exp_f32 v36, v36\n"
        "v_exp_f32 v37, v37\n"
        "v_cndmask_b32 v61, v236, v242, s[84:85]\n"
        "v_cvt_pk_bf16_f32 v84, v36, v37\n"
        "v_cndmask_b32 v62, v236, v242, s[86:87]\n"
        "v_cndmask_b32 v63, v236, v242, s[88:89]\n"
        "s_nop 1\n"
        "v_mfma_f32_16x16x32_bf16 v[60:63], a[124:127], v[140:143], v[60:63]\n"
        "v_exp_f32 v38, v38\n"
        "v_mfma_f32_16x16x32_bf16 v[60:63], a[120:123], v[136:139], v[60:63]\n"
        "v_exp_f32 v39, v39\n"
        "v_mfma_f32_16x16x32_bf16 v[60:63], a[116:119], v[132:135], v[60:63]\n"
        "ds_read_b128 a[144:147], v241 offset:24576\n"
        "v_cvt_pk_bf16_f32 v85, v38, v39\n"
        "v_mfma_f32_16x16x32_bf16 v[60:63], a[112:115], v[128:131], v[60:63]\n"
        "ds_read_b128 a[112:115], v240 offset:4096\n"
        "v_exp_f32 v40, v40\n"
        "v_exp_f32 v41, v41\n"
        "v_exp_f32 v42, v42\n"
        "v_cvt_pk_bf16_f32 v88, v40, v41\n"
        "v_exp_f32 v43, v43\n"
        "ds_read_b128 a[116:119], v240 offset:5120\n"
        "v_cvt_pk_bf16_f32 v89, v42, v43\n"
        "ds_read_b128 a[148:151], v241 offset:25600\n"
        "v_exp_f32 v44, v44\n"
        "v_exp_f32 v45, v45\n"
        "ds_read_b128 a[152:155], v241 offset:26624\n"
        "v_cvt_pk_bf16_f32 v92, v44, v45\n"
        "v_exp_f32 v46, v46\n"
        "v_exp_f32 v47, v47\n"
        "ds_read_b128 a[120:123], v240 offset:6144\n"
        "v_cvt_pk_bf16_f32 v93, v46, v47\n"
        "s_cmp_eq_u32 s91, 0\n"
        "s_cbranch_scc1 LLD_%=\n"
        "; ---- DUAL\n"
        "s_nop 0\n"
        "s_nop 0\n"
        "s_nop 0\n"
        "s_nop 0\n"
        "s_nop 0\n"
        "s_nop 0\n"
        "s_nop 0\n"
        "s_nop 0\n"
        "s_nop 0\n"
        "LDU_%=:\n"
        "v_mfma_f32_16x16x32_bf16 v[32:35], a[96:99], v[0:3], v[224:227]\n"
        "s_add_u32 m0, s81, 0x0\n"
        "s_xor_b32 s79, s79, 0x8000\n"
        "s_waitcnt lgkmcnt(3)\n"
        "v_mfma_f32_16x16x32_bf16 v[32:35], a[100:103], v[4:7], v[32:35]\n"
        "global_load_lds_dwordx4 v254, s[74:75] offset:0 sc1\n"
        "ds_read_b128 a[156:159], v241 offset:27648\n"
        "v_mfma_f32_16x16x32_bf16 v[32:35], a[104:107], v[8:11], v[32:35]\n"
        "v_exp_f32 v48, v48\n"
        "v_mfma_f32_16x16x32_bf16 v[32:35], a[108:111], v[12:15], v[32:35]\n"
        "v_exp_f32 v49, v49\n"
        "v_mfma_f32_16x16x32_bf16 v[36:39], a[108:111], v[76:79], v[228:231]\n"
        "ds_read_b128 a[124:127], v240 offset:7168\n"
        "v_cvt_pk_bf16_f32 v82, v48, v49\n"
        "v_mfma_f32_16x16x32_bf16 v[36:39], a[104:107], v[72:75], v[36:39]\n"
        "global_load_lds_dwordx4 v254, s[74:75] offset:1024 sc1\n"
        "s_add_u32 s76, s66, s90\n"
        "s_addc_u32 s77, s67, 0\n"
        "v_mfma_f32_16x16x32_bf16 v[36:39], a[100:103], v[68:71], v[36:39]\n"
        "v_exp_f32 v50, v50\n"
        "v_mfma_f32_16x16x32_bf16 v[36:39], a[96:99], v[64:67], v[36:39]\n"
        "v_exp_f32 v51, v51\n"
        "v_mfma_f32_16x16x32_bf16 v[40:43], a[96:99], v[96:99], v[232:235]\n"
        "v_cvt_pk_bf16_f32 v83, v50, v51\n"
        "v_mfma_f32_16x16x32_bf16 v[40:43], a[100:103], v[100:103], v[40:43]\n"
        "global_load_lds_dwordx4 v254, s[74:75] offset:2048 sc1\n"
        "v_mfma_f32_16x16x32_bf16 v[40:43], a[104:107], v[104:107], v[40:43]\n"
        "v_exp_f32 v52, v52\n"
        "v_mfma_f32_16x16x32_bf16 v[40:43], a[108:111], v[108:111], v[40:43]\n"
        "v_exp_f32 v53, v53\n"
        "v_mfma_f32_16x16x32_bf16 v[44:47], a[108:111], v[140:143], v[236:239]\n"
        "ds_read_b128 a[108:111], v240 offset:11264\n"
        "v_cvt_pk_bf16_f32 v86, v52, v53\n"
        "v_mfma_f32_16x16x32_bf16 v[44:47], a[104:107], v[136:139], v[44:47]\n"
        "global_load_lds_dwordx4 v254, s[74:75] offset:3072 sc1\n"
        "ds_read_b128 a[104:107], v240 offset:10240\n"
        "s_add_u32 m0, s79, 0x0\n"
        "v_mfma_f32_16x16x32_bf16 v[44:47], a[100:103], v[132:135], v[44:47]\n"
        "v_exp_f32 v54, v54\n"
        "v_mfma_f32_16x16x32_bf16 v[44:47], a[96:99], v[128:131], v[44:47]\n"
        "v_exp_f32 v55, v55\n"
        "v_mfma_f32_16x16x32_bf16 v[176:179], a[128:131], v[16:19], v[176:179]\n"
        "ds_read_b128 a[96:99], v240 offset:8192\n"
        "v_cvt_pk_bf16_f32 v87, v54, v55\n"
        "v_mfma_f32_16x16x32_bf16 v[208:211], a[128:131], v[20:23], v[208:211]\n"
        "v_exp_f32 v56, v56\n"
        "v_mfma_f32_16x16x32_bf16 a[16:19], a[128:131], v[24:27], a[16:19]\n"
        "v_exp_f32 v57, v57\n"
        "v_mfma_f32_16x16x32_bf16 a[48:51], a[128:131], v[28:31], a[48:51]\n"
        "ds_read_b128 a[128:131], v241 offset:28672\n"
        "v_cvt_pk_bf16_f32 v90, v56, v57\n"
        "v_mfma_f32_16x16x32_bf16 a[52:55], a[132:135], v[28:31], a[52:55]\n"
        "s_add_u32 s82, s82, 1\n"
        "v_exp_f32 v58, v58\n"
        "v_mfma_f32_16x16x32_bf16 a[20:23], a[132:135], v[24:27], a[20:23]\n"
        "s_xor_b32 s78, s78, 0x8000\n"
        "v_exp_f32 v59, v59\n"
        "v_mfma_f32_16x16x32_bf16 v[212:215], a[132:135], v[20:23], v[212:215]\n"
        "ds_read_b128 a[100:103], v240 offset:9216\n"
        "s_add_u32 s81, s78, 4096\n"
        "v_cvt_pk_bf16_f32 v91, v58, v59\n"
        "v_mfma_f32_16x16x32_bf16 v[180:183], a[132:135], v[16:19], v[180:183]\n"
        "ds_read_b128 a[132:135], v241 offset:29696\n"
        "s_cmp_ge_u32 s82, s44\n"
        "s_addc_u32 s90, s82, 0\n"
        "v_mfma_f32_16x16x32_bf16 v[184:187], a[136:139], v[16:19], v[184:187]\n"
        "s_min_u32 s90, s90, 15\n"
        "v_exp_f32 v60, v60\n"
        "v_mfma_f32_16x16x32_bf16 v[216:219], a[136:139], v[20:23], v[216:219]\n"
        "s_lshl_b32 s90, s90, 15\n"
        "v_exp_f32 v61, v61\n"
        "v_mfma_f32_16x16x32_bf16 a[24:27], a[136:139], v[24:27], a[24:27]\n"
        "s_add_u32 s68, s64, s90\n"
        "s_addc_u32 s69, s65, 0\n"
        "v_cvt_pk_bf16_f32 v94, v60, v61\n"
        "v_mfma_f32_16x16x32_bf16 a[56:59], a[136:139], v[28:31], a[56:59]\n"
        "ds_read_b128 a[136:139], v241 offset:30720\n"
        "s_add_u32 s74, s68, 0x1000\n"
        "s_addc_u32 s75, s69, 0\n"
        "v_mfma_f32_16x16x32_bf16 a[60:63], a[140:143], v[28:31], a[60:63]\n"
        "s_waitcnt lgkmcnt(3)\n"
        "v_exp_f32 v62, v62\n"
        "v_mfma_f32_16x16x32_bf16 a[28:31], a[140:143], v[24:27], a[28:31]\n"
        "v_exp_f32 v63, v63\n"
        "v_mfma_f32_16x16x32_bf16 v[220:223], a[140:143], v[20:23], v[220:223]\n"
        "v_cvt_pk_bf16_f32 v95, v62, v63\n"
        "v_mfma_f32_16x16x32_bf16 v[188:191], a[140:143], v[16:19], v[188:191]\n"
        "ds_read_b128 a[140:143], v241 offset:31744\n"
        "v_xor_b32 v241, 0x8000, v241\n"
        "v_mfma_f32_16x16x32_bf16 v[48:51], a[112:115], v[0:3], v[224:227]\n"
        "v_exp_f32 v32, v32\n"
        "v_mfma_f32_16x16x32_bf16 v[48:51], a[116:119], v[4:7], v[48:51]\n"
        "v_exp_f32 v33, v33\n"
        "v_mfma_f32_16x16x32_bf16 v[48:51], a[120:123], v[8:11], v[48:51]\n"
        "v_cvt_pk_bf16_f32 v16, v32, v33\n"
        "v_mfma_f32_16x16x32_bf16 v[48:51], a[124:127], v[12:15], v[48:51]\n"
        "v_exp_f32 v34, v34\n"
        "v_mfma_f32_16x16x32_bf16 v[52:55], a[124:127], v[76:79], v[228:231]\n"
        "v_exp_f32 v35, v35\n"
        "v_mfma_f32_16x16x32_bf16 v[52:55], a[120:123], v[72:75], v[52:55]\n"
        "v_cvt_pk_bf16_f32 v17, v34, v35\n"
        "v_mfma_f32_16x16x32_bf16 v[52:55], a[116:119], v[68:71], v[52:55]\n"
        "v_exp_f32 v36, v36\n"
        "v_mfma_f32_16x16x32_bf16 v[52:55], a[112:115], v[64:67], v[52:55]\n"
        "s_waitcnt vmcnt(9) lgkmcnt(0)\n"
        "v_exp_f32 v37, v37\n"
        "v_mfma_f32_16x16x32_bf16 v[56:59], a[112:115], v[96:99], v[232:235]\n"
        "s_barrier\n"
        "v_cvt_pk_bf16_f32 v20, v36, v37\n"
        "v_mfma_f32_16x16x32_bf16 v[56:59], a[116:119], v[100:103], v[56:59]\n"
        "v_exp_f32 v38, v38\n"
        "v_mfma_f32_16x16x32_bf16 v[56:59], a[120:123], v[104:107], v[56:59]\n"
        "v_exp_f32 v39, v39\n"
        "v_mfma_f32_16x16x32_bf16 v[56:59], a[124:127], v[108:111], v[56:59]\n"
        "global_load_lds_dwordx4 v254, s[76:77] offset:0 sc1\n"
        "v_cvt_pk_bf16_f32 v21, v38, v39\n"
        "v_mfma_f32_16x16x32_bf16 v[60:63], a[124:127], v[140:143], v[236:239]\n"
        "v_exp_f32 v40, v40\n"
        "v_mfma_f32_16x16x32_bf16 v[60:63], a[120:123], v[136:139], v[60:63]\n"
        "v_exp_f32 v41, v41\n"
        "v_mfma_f32_16x16x32_bf16 v[60:63], a[116:119], v[132:135], v[60:63]\n"
        "ds_read_b128 a[116:119], v240 offset:13312\n"
        "v_cvt_pk_bf16_f32 v24, v40, v41\n"
        "v_mfma_f32_16x16x32_bf16 v[60:63], a[112:115], v[128:131], v[60:63]\n"
        "ds_read_b128 a[112:115], v240 offset:12288\n"
        "global_load_lds_dwordx4 v254, s[76:77] offset:1024 sc1\n"
        "v_mfma_f32_16x16x32_bf16 a[64:67], a[80:83], v[80:83], a[64:67]\n"
        "v_exp_f32 v42, v42\n"
        "v_mfma_f32_16x16x32_bf16 a[68:71], a[80:83], v[84:87], a[68:71]\n"
        "v_exp_f32 v43, v43\n"
        "v_mfma_f32_16x16x32_bf16 a[72:75], a[80:83], v[88:91], a[72:75]\n"
        "ds_read_b128 a[120:123], v240 offset:14336\n"
        "v_cvt_pk_bf16_f32 v25, v42, v43\n"
        "v_mfma_f32_16x16x32_bf16 a[76:79], a[80:83], v[92:95], a[76:79]\n"
        "v_exp_f32 v44, v44\n"
        "v_mfma_f32_16x16x32_bf16 v[160:163], a[144:147], v[80:83], v[160:163]\n"
        "v_exp_f32 v45, v45\n"
        "v_mfma_f32_16x16x32_bf16 v[192:195], a[144:147], v[84:87], v[192:195]\n"
        "global_load_lds_dwordx4 v254, s[76:77] offset:2048 sc1\n"
        "v_cvt_pk_bf16_f32 v28, v44, v45\n"
        "v_mfma_f32_16x16x32_bf16 a[0:3], a[144:147], v[88:91], a[0:3]\n"
        "v_exp_f32 v46, v46\n"
        "v_mfma_f32_16x16x32_bf16 a[32:35], a[144:147], v[92:95], a[32:35]\n"
        "ds_read_b128 a[144:147], v241 offset:0\n"
        "v_mfma_f32_16x16x32_bf16 a[36:39], a[148:151], v[92:95], a[36:39]\n"
        "v_exp_f32 v47, v47\n"
        "v_mfma_f32_16x16x32_bf16 a[4:7], a[148:151], v[88:91], a[4:7]\n"
        "global_load_lds_dwordx4 v254, s[76:77] offset:3072 sc1\n"
        "s_add_u32 m0, s79, 0x1000\n"
        "v_cvt_pk_bf16_f32 v29, v46, v47\n"
        "v_mfma_f32_16x16x32_bf16 v[196:199], a[148:151], v[84:87], v[196:199]\n"
        "v_exp_f32 v48, v48\n"
        "v_mfma_f32_16x16x32_bf16 v[164:167], a[148:151], v[80:83], v[164:167]\n"
        "ds_read_b128 a[148:151], v241 offset:1024\n"
        "s_add_u32 s76, s76, 0x1000\n"
        "s_addc_u32 s77, s77, 0\n"
        "v_mfma_f32_16x16x32_bf16 v[168:171], a[152:155], v[80:83], v[168:171]\n"
        "v_exp_f32 v49, v49\n"
        "v_mfma_f32_16x16x32_bf16 v[200:203], a[152:155], v[84:87], v[200:203]\n"
        "global_load_lds_dwordx4 v254, s[76:77] offset:0 sc1\n"
        "v_cvt_pk_bf16_f32 v18, v48, v49\n"
        "v_mfma_f32_16x16x32_bf16 a[8:11], a[152:155], v[88:91], a[8:11]\n"
        "v_exp_f32 v50, v50\n"
        "v_mfma_f32_16x16x32_bf16 a[40:43], a[152:155], v[92:95], a[40:43]\n"
        "ds_read_b128 a[152:155], v241 offset:2048\n"
        "v_mfma_f32_16x16x32_bf16 a[44:47], a[156:159], v[92:95], a[44:47]\n"
        "v_exp_f32 v51, v51\n"
        "v_mfma_f32_16x16x32_bf16 a[12:15], a[156:159], v[88:91], a[12:15]\n"
        "ds_read_b128 a[124:127], v240 offset:15360\n"
        "v_cvt_pk_bf16_f32 v19, v50, v51\n"
        "v_mfma_f32_16x16x32_bf16 v[204:207], a[156:159], v[84:87], v[204:207]\n"
        "global_load_lds_dwordx4 v254, s[76:77] offset:1024 sc1\n"
        "v_mfma_f32_16x16x32_bf16 v[172:175], a[156:159], v[80:83], v[172:175]\n"
        "ds_read_b128 a[156:159], v241 offset:3072\n"
        "v_mfma_f32_16x16x32_bf16 v[32:35], a[96:99], v[0:3], v[224:227]\n"
        "v_exp_f32 v52, v52\n"
        "v_mfma_f32_16x16x32_bf16 v[32:35], a[100:103], v[4:7], v[32:35]\n"
        "v_exp_f32 v53, v53\n"
        "v_mfma_f32_16x16x32_bf16 v[32:35], a[104:107], v[8:11], v[32:35]\n"
        "global_load_lds_dwordx4 v254, s[76:77] offset:2048 sc1\n"
        "v_cvt_pk_bf16_f32 v22, v52, v53\n"
        "v_mfma_f32_16x16x32_bf16 v[32:35], a[108:111], v[12:15], v[32:35]\n"
        "s_sub_u32 s91, s91, 1\n"
        "v_exp_f32 v54, v54\n"
        "v_mfma_f32_16x16x32_bf16 v[36:39], a[108:111], v[76:79], v[228:231]\n"
        "v_exp_f32 v55, v55\n"
        "v_mfma_f32_16x16x32_bf16 v[36:39], a[104:107], v[72:75], v[36:39]\n"
        "v_cvt_pk_bf16_f32 v23, v54, v55\n"
        "v_mfma_f32_16x16x32_bf16 v[36:39], a[100:103], v[68:71], v[36:39]\n"
        "global_load_lds_dwordx4 v254, s[76:77] offset:3072 sc1\n"
        "v_mfma_f32_16x16x32_bf16 v[36:39], a[96:99], v[64:67], v[36:39]\n"
        "v_exp_f32 v56, v56\n"
        "v_mfma_f32_16x16x32_bf16 v[40:43], a[96:99], v[96:99], v[232:235]\n"
        "v_exp_f32 v57, v57\n"
        "v_mfma_f32_16x16x32_bf16 v[40:43], a[100:103], v[100:103], v[40:43]\n"
        "v_cvt_pk_bf16_f32 v26, v56, v57\n"
        "v_mfma_f32_16x16x32_bf16 v[40:43], a[104:107], v[104:107], v[40:43]\n"
        "v_exp_f32 v58, v58\n"
        "v_mfma_f32_16x16x32_bf16 v[40:43], a[108:111], v[108:111], v[40:43]\n"
        "v_exp_f32 v59, v59\n"
        "v_mfma_f32_16x16x32_bf16 v[44:47], a[108:111], v[140:143], v[236:239]\n"
        "ds_read_b128 a[108:111], v240 offset:19456\n"
        "v_cvt_pk_bf16_f32 v27, v58, v59\n"
        "v_mfma_f32_16x16x32_bf16 v[44:47], a[104:107], v[136:139], v[44:47]\n"
        "v_exp_f32 v60, v60\n"
        "v_mfma_f32_16x16x32_bf16 v[44:47], a[100:103], v[132:135], v[44:47]\n"
        "ds_read_b128 a[100:103], v240 offset:17408\n"
        "v_mfma_f32_16x16x32_bf16 v[44:47], a[96:99], v[128:131], v[44:47]\n"
        "ds_read_b128 a[96:99], v240 offset:16384\n"
        "v_mfma_f32_16x16x32_bf16 v[176:179], a[128:131], v[80:83], v[176:179]\n"
        "v_exp_f32 v61, v61\n"
        "v_mfma_f32_16x16x32_bf16 v[208:211], a[128:131], v[84:87], v[208:211]\n"
        "ds_read_b128 a[104:107], v240 offset:18432\n"
        "v_cvt_pk_bf16_f32 v30, v60, v61\n"
        "v_mfma_f32_16x16x32_bf16 a[16:19], a[128:131], v[88:91], a[16:19]\n"
        "v_exp_f32 v62, v62\n"
        "v_mfma_f32_16x16x32_bf16 a[48:51], a[128:131], v[92:95], a[48:51]\n"
        "ds_read_b128 a[128:131], v241 offset:4096\n"
        "v_mfma_f32_16x16x32_bf16 a[52:55], a[132:135], v[92:95], a[52:55]\n"
        "v_exp_f32 v63, v63\n"
        "v_mfma_f32_16x16x32_bf16 a[20:23], a[132:135], v[88:91], a[20:23]\n"
        "v_cvt_pk_bf16_f32 v31, v62, v63\n"
        "v_mfma_f32_16x16x32_bf16 v[212:215], a[132:135], v[84:87], v[212:215]\n"
        "v_exp_f32 v32, v32\n"
        "v_mfma_f32_16x16x32_bf16 v[180:183], a[132:135], v[80:83], v[180:183]\n"
        "ds_read_b128 a[132:135], v241 offset:5120\n"
        "v_mfma_f32_16x16x32_bf16 v[184:187], a[136:139], v[80:83], v[184:187]\n"
        "v_exp_f32 v33, v33\n"
        "v_mfma_f32_16x16x32_bf16 v[216:219], a[136:139], v[84:87], v[216:219]\n"
        "v_exp_f32 v34, v34\n"
        "v_mfma_f32_16x16x32_bf16 a[24:27], a[136:139], v[88:91], a[24:27]\n"
        "s_waitcnt lgkmcnt(2)\n"
        "v_exp_f32 v35, v35\n"
        "v_mfma_f32_16x16x32_bf16 a[56:59], a[136:139], v[92:95], a[56:59]\n"
        "ds_read_b128 a[136:139], v241 offset:6144\n"
        "v_mfma_f32_16x16x32_bf16 a[60:63], a[140:143], v[92:95], a[60:63]\n"
        "v_exp_f32 v36, v36\n"
        "v_mfma_f32_16x16x32_bf16 a[28:31], a[140:143], v[88:91], a[28:31]\n"
        "v_exp_f32 v37, v37\n"
        "v_mfma_f32_16x16x32_bf16 v[220:223], a[140:143], v[84:87], v[220:223]\n"
        "v_cvt_pk_bf16_f32 v84, v36, v37\n"
        "v_mfma_f32_16x16x32_bf16 v[188:191], a[140:143], v[80:83], v[188:191]\n"
        "global_load_dword v253, v245, s[26:27] offset:0\n"
        "s_sub_u32 s26, s26, 262144\n"
        "s_subb_u32 s27, s27, 0\n"
        "v_mfma_f32_16x16x32_bf16 v[48:51], a[112:115], v[0:3], v[224:227]\n"
        "ds_read_b128 a[140:143], v241 offset:7168\n"
        "v_cvt_pk_bf16_f32 v80, v32, v33\n"
        "v_mfma_f32_16x16x32_bf16 v[48:51], a[116:119], v[4:7], v[48:51]\n"
        "v_cvt_pk_bf16_f32 v81, v34, v35\n"
        "v_mfma_f32_16x16x32_bf16 v[48:51], a[120:123], v[8:11], v[48:51]\n"
        "v_exp_f32 v38, v38\n"
        "v_mfma_f32_16x16x32_bf16 v[48:51], a[124:127], v[12:15], v[48:51]\n"
        "v_exp_f32 v39, v39\n"
        "v_mfma_f32_16x16x32_bf16 v[52:55], a[124:127], v[76:79], v[228:231]\n"
        "LHA12_%=:\n"
        "s_cbranch_execz LHRA_%=\n"
        "v_cvt_pk_bf16_f32 v85, v38, v39\n"
        "v_mfma_f32_16x16x32_bf16 v[52:55], a[120:123], v[72:75], v[52:55]\n"
        "v_exp_f32 v40, v40\n"
        "v_mfma_f32_16x16x32_bf16 v[52:55], a[116:119], v[68:71], v[52:55]\n"
        "v_exp_f32 v41, v41\n"
        "v_mfma_f32_16x16x32_bf16 v[52:55], a[112:115], v[64:67], v[52:55]\n"
        "v_cvt_pk_bf16_f32 v88, v40, v41\n"
        "v_mfma_f32_16x16x32_bf16 v[56:59], a[112:115], v[96:99], v[232:235]\n"
        "v_exp_f32 v42, v42\n"
        "v_mfma_f32_16x16x32_bf16 v[56:59], a[116:119], v[100:103], v[56:59]\n"
        "v_exp_f32 v43, v43\n"
        "v_mfma_f32_16x16x32_bf16 v[56:59], a[120:123], v[104:107], v[56:59]\n"
        "v_cvt_pk_bf16_f32 v89, v42, v43\n"
        "v_mfma_f32_16x16x32_bf16 v[56:59], a[124:127], v[108:111], v[56:59]\n"
        "v_exp_f32 v44, v44\n"
        "v_mfma_f32_16x16x32_bf16 v[60:63], a[124:127], v[140:143], v[236:239]\n"
        "v_exp_f32 v45, v45\n"
        "v_mfma_f32_16x16x32_bf16 v[60:63], a[120:123], v[136:139], v[60:63]\n"
        "ds_read_b128 a[120:123], v240 offset:22528\n"
        "v_cvt_pk_bf16_f32 v92, v44, v45\n"
        "v_mfma_f32_16x16x32_bf16 v[60:63], a[116:119], v[132:135], v[60:63]\n"
        "ds_read_b128 a[116:119], v240 offset:21504\n"
        "v_mfma_f32_16x16x32_bf16 v[60:63], a[112:115], v[128:131], v[60:63]\n"
        "ds_read_b128 a[112:115], v240 offset:20480\n"
        "v_mfma_f32_16x16x32_bf16 a[64:67], a[80:83], v[16:19], a[64:67]\n"
        "v_exp_f32 v46, v46\n"
        "v_mfma_f32_16x16x32_bf16 a[68:71], a[80:83], v[20:23], a[68:71]\n"
        "v_exp_f32 v47, v47\n"
        "v_mfma_f32_16x16x32_bf16 a[72:75], a[80:83], v[24:27], a[72:75]\n"
        "ds_read_b128 a[124:127], v240 offset:23552\n"
        "v_cvt_pk_bf16_f32 v93, v46, v47\n"
        "v_mfma_f32_16x16x32_bf16 a[76:79], a[80:83], v[28:31], a[76:79]\n"
        "v_exp_f32 v48, v48\n"
        "v_mfma_f32_16x16x32_bf16 v[160:163], a[144:147], v[16:19], v[160:163]\n"
        "v_exp_f32 v49, v49\n"
        "v_mfma_f32_16x16x32_bf16 v[192:195], a[144:147], v[20:23], v[192:195]\n"
        "v_cvt_pk_bf16_f32 v82, v48, v49\n"
        "v_mfma_f32_16x16x32_bf16 a[0:3], a[144:147], v[24:27], a[0:3]\n"
        "v_exp_f32 v50, v50\n"
        "v_mfma_f32_16x16x32_bf16 a[32:35], a[144:147], v[28:31], a[32:35]\n"
        "ds_read_b128 a[144:147], v241 offset:8192\n"
        "v_mfma_f32_16x16x32_bf16 a[36:39], a[148:151], v[28:31], a[36:39]\n"
        "v_exp_f32 v51, v51\n"
        "v_mfma_f32_16x16x32_bf16 a[4:7], a[148:151], v[24:27], a[4:7]\n"
        "v_cvt_pk_bf16_f32 v83, v50, v51\n"
        "v_mfma_f32_16x16x32_bf16 v[196:199], a[148:151], v[20:23], v[196:199]\n"
        "v_exp_f32 v52, v52\n"
        "v_mfma_f32_16x16x32_bf16 v[164:167], a[148:151], v[16:19], v[164:167]\n"
        "ds_read_b128 a[148:151], v241 offset:9216\n"
        "v_mfma_f32_16x16x32_bf16 v[168:171], a[152:155], v[16:19], v[168:171]\n"
        "v_exp_f32 v53, v53\n"
        "v_mfma_f32_16x16x32_bf16 v[200:203], a[152:155], v[20:23], v[200:203]\n"
        "v_cvt_pk_bf16_f32 v86, v52, v53\n"
        "v_mfma_f32_16x16x32_bf16 a[8:11], a[152:155], v[24:27], a[8:11]\n"
        "v_exp_f32 v54, v54\n"
        "v_mfma_f32_16x16x32_bf16 a[40:43], a[152:155], v[28:31], a[40:43]\n"
        "ds_read_b128 a[152:155], v241 offset:10240\n"
        "v_mfma_f32_16x16x32_bf16 a[44:47], a[156:159], v[28:31], a[44:47]\n"
        "v_exp_f32 v55, v55\n"
        "v_mfma_f32_16x16x32_bf16 a[12:15], a[156:159], v[24:27], a[12:15]\n"
        "v_cvt_pk_bf16_f32 v87, v54, v55\n"
        "v_mfma_f32_16x16x32_bf16 v[204:207], a[156:159], v[20:23], v[204:207]\n"
        "s_waitcnt lgkmcnt(2)\n"
        "v_exp_f32 v56, v56\n"
        "v_mfma_f32_16x16x32_bf16 v[172:175], a[156:159], v[16:19], v[172:175]\n"
        "ds_read_b128 a[156:159], v241 offset:11264\n"
        "v_mfma_f32_16x16x32_bf16 v[32:35], a[96:99], v[0:3], v[224:227]\n"
        "v_exp_f32 v57, v57\n"
        "v_mfma_f32_16x16x32_bf16 v[32:35], a[100:103], v[4:7], v[32:35]\n"
        "v_cvt_pk_bf16_f32 v90, v56, v57\n"
        "v_mfma_f32_16x16x32_bf16 v[32:35], a[104:107], v[8:11], v[32:35]\n"
        "v_exp_f32 v58, v58\n"
        "v_mfma_f32_16x16x32_bf16 v[32:35], a[108:111], v[12:15], v[32:35]\n"
        "v_exp_f32 v59, v59\n"
        "v_mfma_f32_16x16x32_bf16 v[36:39], a[108:111], v[76:79], v[228:231]\n"
        "v_cvt_pk_bf16_f32 v91, v58, v59\n"
        "v_mfma_f32_16x16x32_bf16 v[36:39], a[104:107], v[72:75], v[36:39]\n"
        "v_exp_f32 v60, v60\n"
        "v_mfma_f32_16x16x32_bf16 v[36:39], a[100:103], v[68:71], v[36:39]\n"
        "v_exp_f32 v61, v61\n"
        "v_mfma_f32_16x16x32_bf16 v[36:39], a[96:99], v[64:67], v[36:39]\n"
        "v_cvt_pk_bf16_f32 v94, v60, v61\n"
        "v_mfma_f32_16x16x32_bf16 v[40:43], a[96:99], v[96:99], v[232:235]\n"
        "v_exp_f32 v62, v62\n"
        "v_mfma_f32_16x16x32_bf16 v[40:43], a[100:103], v[100:103], v[40:43]\n"
        "v_exp_f32 v63, v63\n"
        "v_mfma_f32_16x16x32_bf16 v[40:43], a[104:107], v[104:107], v[40:43]\n"
        "v_cvt_pk_bf16_f32 v95, v62, v63\n"
        "v_mfma_f32_16x16x32_bf16 v[40:43], a[108:111], v[108:111], v[40:43]\n"
        "v_exp_f32 v32, v32\n"
        "v_mfma_f32_16x16x32_bf16 v[44:47], a[108:111], v[140:143], v[236:239]\n"
        "v_exp_f32 v33, v33\n"
        "v_mfma_f32_16x16x32_bf16 v[44:47], a[104:107], v[136:139], v[44:47]\n"
        "ds_read_b128 a[104:107], v240 offset:26624\n"
        "v_mfma_f32_16x16x32_bf16 v[44:47], a[100:103], v[132:135], v[44:47]\n"
        "ds_read_b128 a[100:103], v240 offset:25600\n"
        "v_mfma_f32_16x16x32_bf16 v[44:47], a[96:99], v[128:131], v[44:47]\n"
        "ds_read_b128 a[96:99], v240 offset:24576\n"
        "v_mfma_f32_16x16x32_bf16 v[176:179], a[128:131], v[16:19], v[176:179]\n"
        "v_exp_f32 v34, v34\n"
        "v_mfma_f32_16x16x32_bf16 v[208:211], a[128:131], v[20:23], v[208:211]\n"
        "v_exp_f32 v35, v35\n"
        "v_mfma_f32_16x16x32_bf16 a[16:19], a[128:131], v[24:27], a[16:19]\n"
        "ds_read_b128 a[108:111], v240 offset:27648\n"
        "v_mfma_f32_16x16x32_bf16 a[48:51], a[128:131], v[28:31], a[48:51]\n"
        "ds_read_b128 a[128:131], v241 offset:12288\n"
        "v_mfma_f32_16x16x32_bf16 a[52:55], a[132:135], v[28:31], a[52:55]\n"
        "v_exp_f32 v36, v36\n"
        "v_mfma_f32_16x16x32_bf16 a[20:23], a[132:135], v[24:27], a[20:23]\n"
        "v_exp_f32 v37, v37\n"
        "v_mfma_f32_16x16x32_bf16 v[212:215], a[132:135], v[20:23], v[212:215]\n"
        "v_exp_f32 v38, v38\n"
        "v_mfma_f32_16x16x32_bf16 v[180:183], a[132:135], v[16:19], v[180:183]\n"
        "ds_read_b128 a[132:135], v241 offset:13312\n"
        "v_mfma_f32_16x16x32_bf16 v[184:187], a[136:139], v[16:19], v[184:187]\n"
        "v_exp_f32 v39, v39\n"
        "v_mfma_f32_16x16x32_bf16 v[216:219], a[136:139], v[20:23], v[216:219]\n"
        "v_exp_f32 v40, v40\n"
        "v_mfma_f32_16x16x32_bf16 a[24:27], a[136:139], v[24:27], a[24:27]\n"
        "v_exp_f32 v41, v41\n"
        "v_mfma_f32_16x16x32_bf16 a[56:59], a[136:139], v[28:31], a[56:59]\n"
        "ds_read_b128 a[136:139], v241 offset:14336\n"
        "v_mfma_f32_16x16x32_bf16 a[60:63], a[140:143], v[28:31], a[60:63]\n"
        "v_exp_f32 v42, v42\n"
        "v_mfma_f32_16x16x32_bf16 a[28:31], a[140:143], v[24:27], a[28:31]\n"
        "v_cvt_pk_bf16_f32 v24, v40, v41\n"
        "v_mfma_f32_16x16x32_bf16 v[220:223], a[140:143], v[20:23], v[220:223]\n"
        "v_cvt_pk_bf16_f32 v20, v36, v37\n"
        "v_cvt_pk_bf16_f32 v21, v38, v39\n"
        "v_mfma_f32_16x16x32_bf16 v[188:191], a[140:143], v[16:19], v[188:191]\n"
        "global_load_dword v253, v245, s[26:27] offset:0\n"
        "s_sub_u32 s26, s26, 262144\n"
        "s_subb_u32 s27, s27, 0\n"
        "v_mfma_f32_16x16x32_bf16 v[48:51], a[112:115], v[0:3], v[224:227]\n"
        "ds_read_b128 a[140:143], v241 offset:15360\n"
        "v_cvt_pk_bf16_f32 v16, v32, v33\n"
        "v_mfma_f32_16x16x32_bf16 v[48:51], a[116:119], v[4:7], v[48:51]\n"
        "v_cvt_pk_bf16_f32 v17, v34, v35\n"
        "v_mfma_f32_16x16x32_bf16 v[48:51], a[120:123], v[8:11], v[48:51]\n"
        "v_exp_f32 v43, v43\n"
        "v_mfma_f32_16x16x32_bf16 v[48:51], a[124:127], v[12:15], v[48:51]\n"
        "v_cvt_pk_bf16_f32 v25, v42, v43\n"
        "v_mfma_f32_16x16x32_bf16 v[52:55], a[124:127], v[76:79], v[228:231]\n"
        "v_exp_f32 v44, v44\n"
        "v_mfma_f32_16x16x32_bf16 v[52:55], a[120:123], v[72:75], v[52:55]\n"
        "v_exp_f32 v45, v45\n"
        "v_mfma_f32_16x16x32_bf16 v[52:55], a[116:119], v[68:71], v[52:55]\n"
        "v_cvt_pk_bf16_f32 v28, v44, v45\n"
        "v_mfma_f32_16x16x32_bf16 v[52:55], a[112:115], v[64:67], v[52:55]\n"
        "v_exp_f32 v46, v46\n"
        "v_mfma_f32_16x16x32_bf16 v[56:59], a[112:115], v[96:99], v[232:235]\n"
        "v_exp_f32 v47, v47\n"
        "v_mfma_f32_16x16x32_bf16 v[56:59], a[116:119], v[100:103], v[56:59]\n"
        "v_cvt_pk_bf16_f32 v29, v46, v47\n"
        "v_mfma_f32_16x16x32_bf16 v[56:59], a[120:123], v[104:107], v[56:59]\n"
        "v_exp_f32 v48, v48\n"
        "v_mfma_f32_16x16x32_bf16 v[56:59], a[124:127], v[108:111], v[56:59]\n"
        "v_exp_f32 v49, v49\n"
        "v_mfma_f32_16x16x32_bf16 v[60:63], a[124:127], v[140:143], v[236:239]\n"
        "ds_read_b128 a[124:127], v240 offset:31744\n"
        "v_cvt_pk_bf16_f32 v18, v48, v49\n"
        "v_mfma_f32_16x16x32_bf16 v[60:63], a[120:123], v[136:139], v[60:63]\n"
        "ds_read_b128 a[120:123], v240 offset:30720\n"
        "v_mfma_f32_16x16x32_bf16 v[60:63], a[116:119], v[132:135], v[60:63]\n"
        "ds_read_b128 a[116:119], v240 offset:29696\n"
        "v_mfma_f32_16x16x32_bf16 v[60:63], a[112:115], v[128:131], v[60:63]\n"
        "ds_read_b128 a[112:115], v240 offset:28672\n"
        "v_xor_b32 v240, 0x8000, v240\n"
        "v_mfma_f32_16x16x32_bf16 a[64:67], a[80:83], v[80:83], a[64:67]\n"
        "v_exp_f32 v50, v50\n"
        "v_mfma_f32_16x16x32_bf16 a[68:71], a[80:83], v[84:87], a[68:71]\n"
        "v_exp_f32 v51, v51\n"
        "v_mfma_f32_16x16x32_bf16 a[72:75], a[80:83], v[88:91], a[72:75]\n"
        "s_waitcnt lgkmcnt(4)\n"
        "v_cvt_pk_bf16_f32 v19, v50, v51\n"
        "v_mfma_f32_16x16x32_bf16 a[76:79], a[80:83], v[92:95], a[76:79]\n"
        "v_exp_f32 v52, v52\n"
        "v_mfma_f32_16x16x32_bf16 v[160:163], a[144:147], v[80:83], v[160:163]\n"
        "v_exp_f32 v53, v53\n"
        "v_mfma_f32_16x16x32_bf16 v[192:195], a[144:147], v[84:87], v[192:195]\n"
        "v_cvt_pk_bf16_f32 v22, v52, v53\n"
        "v_mfma_f32_16x16x32_bf16 a[0:3], a[144:147], v[88:91], a[0:3]\n"
        "v_exp_f32 v54, v54\n"
        "v_mfma_f32_16x16x32_bf16 a[32:35], a[144:147], v[92:95], a[32:35]\n"
        "ds_read_b128 a[144:147], v241 offset:16384\n"
        "s_waitcnt vmcnt(10) lgkmcnt(1)\n"
        "v_mfma_f32_16x16x32_bf16 a[36:39], a[148:151], v[92:95], a[36:39]\n"
        "s_barrier\n"
        "v_exp_f32 v55, v55\n"
        "v_mfma_f32_16x16x32_bf16 a[4:7], a[148:151], v[88:91], a[4:7]\n"
        "v_cvt_pk_bf16_f32 v23, v54, v55\n"
        "v_mfma_f32_16x16x32_bf16 v[196:199], a[148:151], v[84:87], v[196:199]\n"
        "s_cmp_eq_u32 s82, s83\n"
        "s_cselect_b64 s[68:69], s[92:93], s[68:69]\n"
        "s_cselect_b32 s80, s50, s78\n"
        "v_mfma_f32_16x16x32_bf16 v[164:167], a[148:151], v[80:83], v[164:167]\n"
        "ds_read_b128 a[148:151], v241 offset:17408\n"
        "s_add_u32 m0, s80, 0x0\n"
        "v_mfma_f32_16x16x32_bf16 v[168:171], a[152:155], v[80:83], v[168:171]\n"
        "s_cmp_eq_u32 s82, s83\n"
        "s_cselect_b64 s[74:75], s[94:95], s[74:75]\n"
        "s_cselect_b32 s81, s51, s81\n"
        "v_mfma_f32_16x16x32_bf16 v[200:203], a[152:155], v[84:87], v[200:203]\n"
        "global_load_lds_dwordx4 v254, s[68:69] offset:0 sc1\n"
        "v_mfma_f32_16x16x32_bf16 a[8:11], a[152:155], v[88:91], a[8:11]\n"
        "v_exp_f32 v56, v56\n"
        "v_mfma_f32_16x16x32_bf16 a[40:43], a[152:155], v[92:95], a[40:43]\n"
        "ds_read_b128 a[152:155], v241 offset:18432\n"
        "v_mfma_f32_16x16x32_bf16 a[44:47], a[156:159], v[92:95], a[44:47]\n"
        "v_exp_f32 v57, v57\n"
        "v_mfma_f32_16x16x32_bf16 a[12:15], a[156:159], v[88:91], a[12:15]\n"
        "global_load_lds_dwordx4 v254, s[68:69] offset:1024 sc1\n"
        "v_cvt_pk_bf16_f32 v26, v56, v57\n"
        "v_mfma_f32_16x16x32_bf16 v[204:207], a[156:159], v[84:87], v[204:207]\n"
        "v_exp_f32 v58, v58\n"
        "v_mfma_f32_16x16x32_bf16 v[172:175], a[156:159], v[80:83], v[172:175]\n"
        "ds_read_b128 a[156:159], v241 offset:19456\n"
        "v_mfma_f32_16x16x32_bf16 v[32:35], a[96:99], v[0:3], v[224:227]\n"
        "v_exp_f32 v59, v59\n"
        "v_mfma_f32_16x16x32_bf16 v[32:35], a[100:103], v[4:7], v[32:35]\n"
        "global_load_lds_dwordx4 v254, s[68:69] offset:2048 sc1\n"
        "v_cvt_pk_bf16_f32 v27, v58, v59\n"
        "v_mfma_f32_16x16x32_bf16 v[32:35], a[104:107], v[8:11], v[32:35]\n"
        "v_exp_f32 v60, v60\n"
        "v_mfma_f32_16x16x32_bf16 v[32:35], a[108:111], v[12:15], v[32:35]\n"
        "v_exp_f32 v61, v61\n"
        "v_mfma_f32_16x16x32_bf16 v[36:39], a[108:111], v[76:79], v[228:231]\n"
        "v_cvt_pk_bf16_f32 v30, v60, v61\n"
        "v_mfma_f32_16x16x32_bf16 v[36:39], a[104:107], v[72:75], v[36:39]\n"
        "global_load_lds_dwordx4 v254, s[68:69] offset:3072 sc1\n"
        "v_mfma_f32_16x16x32_bf16 v[36:39], a[100:103], v[68:71], v[36:39]\n"
        "v_exp_f32 v62, v62\n"
        "v_mfma_f32_16x16x32_bf16 v[36:39], a[96:99], v[64:67], v[36:39]\n"
        "v_exp_f32 v63, v63\n"
        "v_mfma_f32_16x16x32_bf16 v[40:43], a[96:99], v[96:99], v[232:235]\n"
        "v_cvt_pk_bf16_f32 v31, v62, v63\n"
        "v_mfma_f32_16x16x32_bf16 v[40:43], a[100:103], v[100:103], v[40:43]\n"
        "v_exp_f32 v32, v32\n"
        "v_mfma_f32_16x16x32_bf16 v[40:43], a[104:107], v[104:107], v[40:43]\n"
        "v_exp_f32 v33, v33\n"
        "v_mfma_f32_16x16x32_bf16 v[40:43], a[108:111], v[108:111], v[40:43]\n"
        "v_exp_f32 v34, v34\n"
        "v_mfma_f32_16x16x32_bf16 v[44:47], a[108:111], v[140:143], v[236:239]\n"
        "v_exp_f32 v35, v35\n"
        "v_mfma_f32_16x16x32_bf16 v[44:47], a[104:107], v[136:139], v[44:47]\n"
        "ds_read_b128 a[104:107], v240 offset:2048\n"
        "v_mfma_f32_16x16x32_bf16 v[44:47], a[100:103], v[132:135], v[44:47]\n"
        "ds_read_b128 a[100:103], v240 offset:1024\n"
        "v_mfma_f32_16x16x32_bf16 v[44:47], a[96:99], v[128:131], v[44:47]\n"
        "ds_read_b128 a[96:99], v240 offset:0\n"
        "v_mfma_f32_16x16x32_bf16 v[176:179], a[128:131], v[80:83], v[176:179]\n"
        "ds_read_b128 a[108:111], v240 offset:3072\n"
        "v_mfma_f32_16x16x32_bf16 v[208:211], a[128:131], v[84:87], v[208:211]\n"
        "v_exp_f32 v36, v36\n"
        "v_mfma_f32_16x16x32_bf16 a[16:19], a[128:131], v[88:91], a[16:19]\n"
        "v_exp_f32 v37, v37\n"
        "v_mfma_f32_16x16x32_bf16 a[48:51], a[128:131], v[92:95], a[48:51]\n"
        "ds_read_b128 a[128:131], v241 offset:20480\n"
        "v_mfma_f32_16x16x32_bf16 a[52:55], a[132:135], v[92:95], a[52:55]\n"
        "v_exp_f32 v38, v38\n"
        "v_mfma_f32_16x16x32_bf16 a[20:23], a[132:135], v[88:91], a[20:23]\n"
        "v_exp_f32 v39, v39\n"
        "v_mfma_f32_16x16x32_bf16 v[212:215], a[132:135], v[84:87], v[212:215]\n"
        "v_exp_f32 v40, v40\n"
        "v_mfma_f32_16x16x32_bf16 v[180:183], a[132:135], v[80:83], v[180:183]\n"
        "ds_read_b128 a[132:135], v241 offset:21504\n"
        "v_mfma_f32_16x16x32_bf16 v[184:187], a[136:139], v[80:83], v[184:187]\n"
        "v_exp_f32 v41, v41\n"
        "v_mfma_f32_16x16x32_bf16 v[216:219], a[136:139], v[84:87], v[216:219]\n"
        "v_exp_f32 v42, v42\n"
        "v_mfma_f32_16x16x32_bf16 a[24:27], a[136:139], v[88:91], a[24:27]\n"
        "v_exp_f32 v43, v43\n"
        "v_mfma_f32_16x16x32_bf16 a[56:59], a[136:139], v[92:95], a[56:59]\n"
        "ds_read_b128 a[136:139], v241 offset:22528\n"
        "s_waitcnt lgkmcnt(2)\n"
        "v_mfma_f32_16x16x32_bf16 a[60:63], a[140:143], v[92:95], a[60:63]\n"
        "v_exp_f32 v44, v44\n"
        "v_mfma_f32_16x16x32_bf16 a[28:31], a[140:143], v[88:91], a[28:31]\n"
        "v_cvt_pk_bf16_f32 v88, v40, v41\n"
        "v_cvt_pk_bf16_f32 v89, v42, v43\n"
        "v_mfma_f32_16x16x32_bf16 v[220:223], a[140:143], v[84:87], v[220:223]\n"
        "v_cvt_pk_bf16_f32 v84, v36, v37\n"
        "v_cvt_pk_bf16_f32 v85, v38, v39\n"
        "v_mfma_f32_16x16x32_bf16 v[188:191], a[140:143], v[80:83], v[188:191]\n"
        "ds_read_b128 a[140:143], v241 offset:23552\n"
        "v_cvt_pk_bf16_f32 v80, v32, v33\n"
        "v_mfma_f32_16x16x32_bf16 v[48:51], a[112:115], v[0:3], v[224:227]\n"
        "v_cvt_pk_bf16_f32 v81, v34, v35\n"
        "v_mfma_f32_16x16x32_bf16 v[48:51], a[116:119], v[4:7], v[48:51]\n"
        "v_exp_f32 v45, v45\n"
        "v_mfma_f32_16x16x32_bf16 v[48:51], a[120:123], v[8:11], v[48:51]\n"
        "v_cvt_pk_bf16_f32 v92, v44, v45\n"
        "v_mfma_f32_16x16x32_bf16 v[48:51], a[124:127], v[12:15], v[48:51]\n"
        "v_exp_f32 v46, v46\n"
        "v_mfma_f32_16x16x32_bf16 v[52:55], a[124:127], v[76:79], v[228:231]\n"
        "v_exp_f32 v47, v47\n"
        "v_mfma_f32_16x16x32_bf16 v[52:55], a[120:123], v[72:75], v[52:55]\n"
        "v_cvt_pk_bf16_f32 v93, v46, v47\n"
        "v_mfma_f32_16x16x32_bf16 v[52:55], a[116:119], v[68:71], v[52:55]\n"
        "v_mfma_f32_16x16x32_bf16 v[52:55], a[112:115], v[64:67], v[52:55]\n"
        "v_mfma_f32_16x16x32_bf16 v[56:59], a[112:115], v[96:99], v[232:235]\n"
        "v_mfma_f32_16x16x32_bf16 v[56:59], a[116:119], v[100:103], v[56:59]\n"
        "v_mfma_f32_16x16x32_bf16 v[56:59], a[120:123], v[104:107], v[56:59]\n"
        "v_mfma_f32_16x16x32_bf16 v[56:59], a[124:127], v[108:111], v[56:59]\n"
        "v_mfma_f32_16x16x32_bf16 v[60:63], a[124:127], v[140:143], v[236:239]\n"
        "v_mfma_f32_16x16x32_bf16 v[60:63], a[120:123], v[136:139], v[60:63]\n"
        "ds_read_b128 a[120:123], v240 offset:6144\n"
        "v_mfma_f32_16x16x32_bf16 v[60:63], a[116:119], v[132:135], v[60:63]\n"
        "ds_read_b128 a[116:119], v240 offset:5120\n"
        "v_mfma_f32_16x16x32_bf16 v[60:63], a[112:115], v[128:131], v[60:63]\n"
        "ds_read_b128 a[112:115], v240 offset:4096\n"
        "v_mfma_f32_16x16x32_bf16 a[64:67], a[80:83], v[16:19], a[64:67]\n"
        "v_mfma_f32_16x16x32_bf16 a[68:71], a[80:83], v[20:23], a[68:71]\n"
        "v_mfma_f32_16x16x32_bf16 a[72:75], a[80:83], v[24:27], a[72:75]\n"
        "v_mfma_f32_16x16x32_bf16 a[76:79], a[80:83], v[28:31], a[76:79]\n"
        "v_mfma_f32_16x16x32_bf16 v[160:163], a[144:147], v[16:19], v[160:163]\n"
        "v_mfma_f32_16x16x32_bf16 v[192:195], a[144:147], v[20:23], v[192:195]\n"
        "v_mfma_f32_16x16x32_bf16 a[0:3], a[144:147], v[24:27], a[0:3]\n"
        "v_mfma_f32_16x16x32_bf16 a[32:35], a[144:147], v[28:31], a[32:35]\n"
        "ds_read_b128 a[144:147], v241 offset:24576\n"
        "v_mfma_f32_16x16x32_bf16 a[36:39], a[148:151], v[28:31], a[36:39]\n"
        "v_mfma_f32_16x16x32_bf16 a[4:7], a[148:151], v[24:27], a[4:7]\n"
        "v_mfma_f32_16x16x32_bf16 v[196:199], a[148:151], v[20:23], v[196:199]\n"
        "v_mfma_f32_16x16x32_bf16 v[164:167], a[148:151], v[16:19], v[164:167]\n"
        "ds_read_b128 a[148:151], v241 offset:25600\n"
        "v_mfma_f32_16x16x32_bf16 v[168:171], a[152:155], v[16:19], v[168:171]\n"
        "v_mfma_f32_16x16x32_bf16 v[200:203], a[152:155], v[20:23], v[200:203]\n"
        "v_mfma_f32_16x16x32_bf16 a[8:11], a[152:155], v[24:27], a[8:11]\n"
        "v_mfma_f32_16x16x32_bf16 a[40:43], a[152:155], v[28:31], a[40:43]\n"
        "ds_read_b128 a[152:155], v241 offset:26624\n"
        "v_mfma_f32_16x16x32_bf16 a[44:47], a[156:159], v[28:31], a[44:47]\n"
        "v_mfma_f32_16x16x32_bf16 a[12:15], a[156:159], v[24:27], a[12:15]\n"
        "v_mfma_f32_16x16x32_bf16 v[204:207], a[156:159], v[20:23], v[204:207]\n"
        "v_mfma_f32_16x16x32_bf16 v[172:175], a[156:159], v[16:19], v[172:175]\n"
        "s_cmp_lg_u32 s91, 0\n"
        "s_cbranch_scc1 LDU_%=\n"
        "; ---- LDRAIN\n"
        "LLD_%=:\n"
        "v_mfma_f32_16x16x32_bf16 a[16:19], a[128:131], v[24:27], a[16:19]\n"
        "s_lshl_b32 s53, s44, 20\n"
        "s_lshl_b32 s96, s43, 15\n"
        "s_add_u32 s53, s53, s96\n"
        "v_mfma_f32_16x16x32_bf16 a[48:51], a[128:131], v[28:31], a[48:51]\n"
        "s_lshl_b32 s96, s40, 10\n"
        "s_add_u32 s53, s53, s96\n"
        "s_waitcnt lgkmcnt(3)\n"
        "v_mfma_f32_16x16x32_bf16 a[52:55], a[132:135], v[28:31], a[52:55]\n"
        "s_add_u32 s53, s53, 0x2001000\n"
        "s_add_u32 s70, s38, s53\n"
        "s_addc_u32 s71, s39, 0\n"
        "v_mfma_f32_16x16x32_bf16 a[20:23], a[132:135], v[24:27], a[20:23]\n"
        "ds_read_b128 a[160:163], v241 offset:24576\n"
        "s_sub_u32 s91, s83, s44\n"
        "v_mfma_f32_16x16x32_bf16 a[24:27], a[136:139], v[24:27], a[24:27]\n"
        "ds_read_b128 a[164:167], v241 offset:25600\n"
        "v_mfma_f32_16x16x32_bf16 a[56:59], a[136:139], v[28:31], a[56:59]\n"
        "ds_read_b128 a[168:171], v241 offset:26624\n"
        "v_mfma_f32_16x16x32_bf16 a[60:63], a[140:143], v[28:31], a[60:63]\n"
        "ds_read_b128 a[172:175], v241 offset:27648\n"
        "v_mfma_f32_16x16x32_bf16 a[28:31], a[140:143], v[24:27], a[28:31]\n"
        "ds_read_b128 a[176:179], v241 offset:28672\n"
        "ds_read_b128 a[180:183], v241 offset:29696\n"
        "ds_read_b128 a[184:187], v241 offset:30720\n"
        "ds_read_b128 a[188:191], v241 offset:31744\n"
        "v_exp_f32 v56, v56\n"
        "v_exp_f32 v57, v57\n"
        "v_exp_f32 v58, v58\n"
        "v_cvt_pk_bf16_f32 v90, v56, v57\n"
        "v_exp_f32 v59, v59\n"
        "v_exp_f32 v60, v60\n"
        "v_cvt_pk_bf16_f32 v91, v58, v59\n"
        "s_waitcnt lgkmcnt(6)\n"
        "s_nop 0\n"
        "v_mfma_f32_16x16x32_bf16 a[0:3], a[160:163], v[88:91], a[0:3]\n"
        "s_waitcnt lgkmcnt(5)\n"
        "v_exp_f32 v61, v61\n"
        "v_mfma_f32_16x16x32_bf16 a[72:75], a[80:83], v[88:91], a[72:75]\n"
        "s_waitcnt lgkmcnt(4)\n"
        "v_cvt_pk_bf16_f32 v94, v60, v61\n"
        "v_mfma_f32_16x16x32_bf16 a[4:7], a[164:167], v[88:91], a[4:7]\n"
        "s_waitcnt lgkmcnt(3)\n"
        "v_exp_f32 v62, v62\n"
        "v_mfma_f32_16x16x32_bf16 a[8:11], a[168:171], v[88:91], a[8:11]\n"
        "s_waitcnt lgkmcnt(2)\n"
        "v_exp_f32 v63, v63\n"
        "v_mfma_f32_16x16x32_bf16 a[12:15], a[172:175], v[88:91], a[12:15]\n"
        "s_waitcnt lgkmcnt(0)\n"
        "v_cvt_pk_bf16_f32 v95, v62, v63\n"
        "s_nop 1\n"
        "v_mfma_f32_16x16x32_bf16 a[76:79], a[80:83], v[92:95], a[76:79]\n"
        "v_accvgpr_read_b32 v248, a72\n"
        "v_accvgpr_read_b32 v120, a0\n"
        "v_mfma_f32_16x16x32_bf16 a[32:35], a[160:163], v[92:95], a[32:35]\n"
        "v_rcp_f32 v246, v248\n"
        "v_mfma_f32_16x16x32_bf16 a[36:39], a[164:167], v[92:95], a[36:39]\n"
        "v_accvgpr_read_b32 v121, a1\n"
        "v_accvgpr_read_b32 v122, a2\n"
        "v_mfma_f32_16x16x32_bf16 a[40:43], a[168:171], v[92:95], a[40:43]\n"
        "v_accvgpr_read_b32 v249, a76\n"
        "v_accvgpr_read_b32 v123, a3\n"
        "v_mfma_f32_16x16x32_bf16 a[44:47], a[172:175], v[92:95], a[44:47]\n"
        "v_rcp_f32 v247, v249\n"
        "v_mfma_f32_16x16x32_bf16 a[16:19], a[176:179], v[88:91], a[16:19]\n"
        "v_accvgpr_read_b32 v112, a32\n"
        "v_accvgpr_read_b32 v113, a33\n"
        "v_mfma_f32_16x16x32_bf16 a[48:51], a[176:179], v[92:95], a[48:51]\n"
        "v_accvgpr_read_b32 v114, a34\n"
        "v_accvgpr_read_b32 v115, a35\n"
        "v_mfma_f32_16x16x32_bf16 a[52:55], a[180:183], v[92:95], a[52:55]\n"
        "v_accvgpr_read_b32 v116, a36\n"
        "v_accvgpr_read_b32 v117, a37\n"
        "v_mfma_f32_16x16x32_bf16 a[20:23], a[180:183], v[88:91], a[20:23]\n"
        "v_accvgpr_read_b32 v118, a38\n"
        "v_accvgpr_read_b32 v119, a39\n"
        "v_mfma_f32_16x16x32_bf16 a[24:27], a[184:187], v[88:91], a[24:27]\n"
        "v_mul_f32 v112, v112, v247\n"
        "v_mul_f32 v113, v113, v247\n"
        "v_mfma_f32_16x16x32_bf16 a[56:59], a[184:187], v[92:95], a[56:59]\n"
        "v_mul_f32 v114, v114, v247\n"
        "v_mul_f32 v115, v115, v247\n"
        "v_mfma_f32_16x16x32_bf16 a[60:63], a[188:191], v[92:95], a[60:63]\n"
        "v_mul_f32 v116, v116, v247\n"
        "v_mul_f32 v117, v117, v247\n"
        "v_mfma_f32_16x16x32_bf16 a[28:31], a[188:191], v[88:91], a[28:31]\n"
        "v_mul_f32 v118, v118, v247\n"
        "v_mul_f32 v119, v119, v247\n"
        "v_cvt_pk_bf16_f32 v112, v112, v113\n"
        "v_cvt_pk_bf16_f32 v113, v114, v115\n"
        "v_cvt_pk_bf16_f32 v114, v116, v117\n"
        "v_cvt_pk_bf16_f32 v115, v118, v119\n"
        "global_store_dwordx4 v254, v[112:115], s[70:71] offset:-4096 sc1\n"
        "v_accvgpr_read_b32 v124, a4\n"
        "v_accvgpr_read_b32 v125, a5\n"
        "v_accvgpr_read_b32 v126, a6\n"
        "v_accvgpr_read_b32 v127, a7\n"
        "v_mul_f32 v120, v120, v246\n"
        "v_mul_f32 v121, v121, v246\n"
        "v_mul_f32 v122, v122, v246\n"
        "v_mul_f32 v123, v123, v246\n"
        "v_mul_f32 v124, v124, v246\n"
        "v_mul_f32 v125, v125, v246\n"
        "v_mul_f32 v126, v126, v246\n"
        "v_mul_f32 v127, v127, v246\n"
        "v_cvt_pk_bf16_f32 v120, v120, v121\n"
        "v_cvt_pk_bf16_f32 v121, v122, v123\n"
        "v_cvt_pk_bf16_f32 v122, v124, v125\n"
        "v_cvt_pk_bf16_f32 v123, v126, v127\n"
        "global_store_dwordx4 v254, v[120:123], s[70:71] offset:0 sc1\n"
        "s_add_u32 s70, s70, 0x2000\n"
        "s_addc_u32 s71, s71, 0\n"
        "v_accvgpr_read_b32 v144, a40\n"
        "v_accvgpr_read_b32 v145, a41\n"
        "v_accvgpr_read_b32 v146, a42\n"
        "v_accvgpr_read_b32 v147, a43\n"
        "v_accvgpr_read_b32 v148, a44\n"
        "v_accvgpr_read_b32 v149, a45\n"
        "v_accvgpr_read_b32 v150, a46\n"
        "v_accvgpr_read_b32 v151, a47\n"
        "v_mul_f32 v144, v144, v247\n"
        "v_mul_f32 v145, v145, v247\n"
        "v_mul_f32 v146, v146, v247\n"
        "v_mul_f32 v147, v147, v247\n"
        "v_mul_f32 v148, v148, v247\n"
        "v_mul_f32 v149, v149, v247\n"
        "v_mul_f32 v150, v150, v247\n"
        "v_mul_f32 v151, v151, v247\n"
        "v_cvt_pk_bf16_f32 v144, v144, v145\n"
        "v_cvt_pk_bf16_f32 v145, v146, v147\n"
        "v_cvt_pk_bf16_f32 v146, v148, v149\n"
        "v_cvt_pk_bf16_f32 v147, v150, v151\n"
        "global_store_dwordx4 v254, v[144:147], s[70:71] offset:-4096 sc1\n"
        "v_accvgpr_read_b32 v152, a8\n"
        "v_accvgpr_read_b32 v153, a9\n"
        "v_accvgpr_read_b32 v154, a10\n"
        "v_accvgpr_read_b32 v155, a11\n"
        "v_accvgpr_read_b32 v156, a12\n"
        "v_accvgpr_read_b32 v157, a13\n"
        "v_accvgpr_read_b32 v158, a14\n"
        "v_accvgpr_read_b32 v159, a15\n"
        "v_mul_f32 v152, v152, v246\n"
        "v_mul_f32 v153, v153, v246\n"
        "v_mul_f32 v154, v154, v246\n"
        "v_mul_f32 v155, v155, v246\n"
        "v_mul_f32 v156, v156, v246\n"
        "v_mul_f32 v157, v157, v246\n"
        "v_mul_f32 v158, v158, v246\n"
        "v_mul_f32 v159, v159, v246\n"
        "v_cvt_pk_bf16_f32 v152, v152, v153\n"
        "v_cvt_pk_bf16_f32 v153, v154, v155\n"
        "v_cvt_pk_bf16_f32 v154, v156, v157\n"
        "v_cvt_pk_bf16_f32 v155, v158, v159\n"
        "global_store_dwordx4 v254, v[152:155], s[70:71] offset:0 sc1\n"
        "s_add_u32 s70, s70, 0x2000\n"
        "s_addc_u32 s71, s71, 0\n"
        "v_accvgpr_read_b32 v112, a48\n"
        "v_accvgpr_read_b32 v113, a49\n"
        "v_accvgpr_read_b32 v114, a50\n"
        "v_accvgpr_read_b32 v115, a51\n"
        "v_accvgpr_read_b32 v116, a52\n"
        "v_accvgpr_read_b32 v117, a53\n"
        "v_accvgpr_read_b32 v118, a54\n"
        "v_accvgpr_read_b32 v119, a55\n"
        "v_mul_f32 v112, v112, v247\n"
        "v_mul_f32 v113, v113, v247\n"
        "v_mul_f32 v114, v114, v247\n"
        "v_mul_f32 v115, v115, v247\n"
        "v_mul_f32 v116, v116, v247\n"
        "v_mul_f32 v117, v117, v247\n"
        "v_mul_f32 v118, v118, v247\n"
        "v_mul_f32 v119, v119, v247\n"
        "v_cvt_pk_bf16_f32 v112, v112, v113\n"
        "v_cvt_pk_bf16_f32 v113, v114, v115\n"
        "v_cvt_pk_bf16_f32 v114, v116, v117\n"
        "v_cvt_pk_bf16_f32 v115, v118, v119\n"
        "global_store_dwordx4 v254, v[112:115], s[70:71] offset:-4096 sc1\n"
        "v_accvgpr_read_b32 v120, a16\n"
        "v_accvgpr_read_b32 v121, a17\n"
        "v_accvgpr_read_b32 v122, a18\n"
        "v_accvgpr_read_b32 v123, a19\n"
        "v_accvgpr_read_b32 v124, a20\n"
        "v_accvgpr_read_b32 v125, a21\n"
        "v_accvgpr_read_b32 v126, a22\n"
        "v_accvgpr_read_b32 v127, a23\n"
        "v_mul_f32 v120, v120, v246\n"
        "v_mul_f32 v121, v121, v246\n"
        "v_mul_f32 v122, v122, v246\n"
        "v_mul_f32 v123, v123, v246\n"
        "v_mul_f32 v124, v124, v246\n"
        "v_mul_f32 v125, v125, v246\n"
        "v_mul_f32 v126, v126, v246\n"
        "v_mul_f32 v127, v127, v246\n"
        "v_cvt_pk_bf16_f32 v120, v120, v121\n"
        "v_cvt_pk_bf16_f32 v121, v122, v123\n"
        "v_cvt_pk_bf16_f32 v122, v124, v125\n"
        "v_cvt_pk_bf16_f32 v123, v126, v127\n"
        "global_store_dwordx4 v254, v[120:123], s[70:71] offset:0 sc1\n"
        "s_add_u32 s70, s70, 0x2000\n"
        "s_addc_u32 s71, s71, 0\n"
        "v_accvgpr_read_b32 v144, a56\n"
        "v_accvgpr_read_b32 v145, a57\n"
        "v_accvgpr_read_b32 v146, a58\n"
        "v_accvgpr_read_b32 v147, a59\n"
        "v_accvgpr_read_b32 v148, a60\n"
        "v_accvgpr_read_b32 v149, a61\n"
        "v_accvgpr_read_b32 v150, a62\n"
        "v_accvgpr_read_b32 v151, a63\n"
        "v_mul_f32 v144, v144, v247\n"
        "v_mul_f32 v145, v145, v247\n"
        "v_mul_f32 v146, v146, v247\n"
        "v_mul_f32 v147, v147, v247\n"
        "v_mul_f32 v148, v148, v247\n"
        "v_mul_f32 v149, v149, v247\n"
        "v_mul_f32 v150, v150, v247\n"
        "v_mul_f32 v151, v151, v247\n"
        "v_cvt_pk_bf16_f32 v144, v144, v145\n"
        "v_cvt_pk_bf16_f32 v145, v146, v147\n"
        "v_cvt_pk_bf16_f32 v146, v148, v149\n"
        "v_cvt_pk_bf16_f32 v147, v150, v151\n"
        "global_store_dwordx4 v254, v[144:147], s[70:71] offset:-4096 sc1\n"
        "v_accvgpr_read_b32 v152, a24\n"
        "v_accvgpr_read_b32 v153, a25\n"
        "v_accvgpr_read_b32 v154, a26\n"
        "v_accvgpr_read_b32 v155, a27\n"
        "v_accvgpr_read_b32 v156, a28\n"
        "v_accvgpr_read_b32 v157, a29\n"
        "v_accvgpr_read_b32 v158, a30\n"
        "v_accvgpr_read_b32 v159, a31\n"
        "v_mul_f32 v152, v152, v246\n"
        "v_mul_f32 v153, v153, v246\n"
        "v_mul_f32 v154, v154, v246\n"
        "v_mul_f32 v155, v155, v246\n"
        "v_mul_f32 v156, v156, v246\n"
        "v_mul_f32 v157, v157, v246\n"
        "v_mul_f32 v158, v158, v246\n"
        "v_mul_f32 v159, v159, v246\n"
        "v_cvt_pk_bf16_f32 v152, v152, v153\n"
        "v_cvt_pk_bf16_f32 v153, v154, v155\n"
        "v_cvt_pk_bf16_f32 v154, v156, v157\n"
        "v_cvt_pk_bf16_f32 v155, v158, v159\n"
        "global_store_dwordx4 v254, v[152:155], s[70:71] offset:0 sc1\n"
        "v_add_f32 v247, v248, v249\n"
        "v_cmp_nge_f32 vcc, 0x4e6e6b28, v247\n"
        "s_nop 0\n"
        "s_cbranch_vccnz LRL_%=\n"
        "LSL_%=:\n"
        "s_mov_b64 exec, 0\n"
        "s_cmp_eq_u32 s40, 0\n"
        "s_cbranch_scc1 LHB0_%=\n"
        "s_cmp_eq_u32 s40, 1\n"
        "s_cbranch_scc1 LHB1_%=\n"
        "s_cmp_eq_u32 s40, 2\n"
        "s_cbranch_scc1 LHB2_%=\n"
        "s_branch LHB3_%=\n"
        "LHRB_%=:\n"
        "s_mov_b64 exec, -1\n"
        "s_cmp_eq_u32 s91, 0\n"
        "s_cbranch_scc1 LTL_%=\n"
        "; ---- SINGLE\n"
        "s_nop 0\n"
        "s_nop 0\n"
        "s_nop 0\n"
        "s_nop 0\n"
        "s_nop 0\n"
        "s_nop 0\n"
        "s_nop 0\n"
        "s_nop 0\n"
        "s_nop 0\n"
        "s_nop 0\n"
        "s_nop 0\n"
        "s_nop 0\n"
        "s_nop 0\n"
        "s_nop 0\n"
        "LSI_%=:\n"
        "v_mfma_f32_16x16x32_bf16 v[32:35], a[96:99], v[0:3], v[224:227]\n"
        "s_add_u32 m0, s81, 0x0\n"
        "s_xor_b32 s79, s79, 0x8000\n"
        "s_waitcnt lgkmcnt(3)\n"
        "v_mfma_f32_16x16x32_bf16 v[32:35], a[100:103], v[4:7], v[32:35]\n"
        "global_load_lds_dwordx4 v254, s[74:75] offset:0 sc1\n"
        "ds_read_b128 a[156:159], v241 offset:27648\n"
        "s_add_u32 s82, s82, 1\n"
        "v_mfma_f32_16x16x32_bf16 v[32:35], a[104:107], v[8:11], v[32:35]\n"
        "s_xor_b32 s78, s78, 0x8000\n"
        "LHA13_%=:\n"
        "s_cbranch_execz LHRA_%=\n"
        "v_exp_f32 v48, v48\n"
        "v_mfma_f32_16x16x32_bf16 v[32:35], a[108:111], v[12:15], v[32:35]\n"
        "s_add_u32 s81, s78, 4096\n"
        "v_exp_f32 v49, v49\n"
        "v_mfma_f32_16x16x32_bf16 v[36:39], a[108:111], v[76:79], v[228:231]\n"
        "ds_read_b128 a[124:127], v240 offset:7168\n"
        "s_sub_u32 s91, s91, 1\n"
        "v_cvt_pk_bf16_f32 v82, v48, v49\n"
        "v_mfma_f32_16x16x32_bf16 v[36:39], a[104:107], v[72:75], v[36:39]\n"
        "global_load_lds_dwordx4 v254, s[74:75] offset:1024 sc1\n"
        "ds_read_b128 a[104:107], v240 offset:10240\n"
        "v_mfma_f32_16x16x32_bf16 v[36:39], a[100:103], v[68:71], v[36:39]\n"
        "v_exp_f32 v50, v50\n"
        "v_mfma_f32_16x16x32_bf16 v[36:39], a[96:99], v[64:67], v[36:39]\n"
        "v_exp_f32 v51, v51\n"
        "v_mfma_f32_16x16x32_bf16 v[176:179], a[128:131], v[16:19], v[176:179]\n"
        "ds_read_b128 a[96:99], v240 offset:8192\n"
        "v_cvt_pk_bf16_f32 v83, v50, v51\n"
        "v_mfma_f32_16x16x32_bf16 v[208:211], a[128:131], v[20:23], v[208:211]\n"
        "global_load_lds_dwordx4 v254, s[74:75] offset:2048 sc1\n"
        "ds_read_b128 a[128:131], v241 offset:28672\n"
        "v_mfma_f32_16x16x32_bf16 v[212:215], a[132:135], v[20:23], v[212:215]\n"
        "v_exp_f32 v52, v52\n"
        "v_mfma_f32_16x16x32_bf16 v[180:183], a[132:135], v[16:19], v[180:183]\n"
        "v_exp_f32 v53, v53\n"
        "v_mfma_f32_16x16x32_bf16 v[184:187], a[136:139], v[16:19], v[184:187]\n"
        "ds_read_b128 a[100:103], v240 offset:9216\n"
        "v_cvt_pk_bf16_f32 v86, v52, v53\n"
        "v_mfma_f32_16x16x32_bf16 v[216:219], a[136:139], v[20:23], v[216:219]\n"
        "ds_read_b128 a[132:135], v241 offset:29696\n"
        "global_load_lds_dwordx4 v254, s[74:75] offset:3072 sc1\n"
        "s_add_u32 m0, s79, 0x0\n"
        "v_mfma_f32_16x16x32_bf16 v[220:223], a[140:143], v[20:23], v[220:223]\n"
        "s_waitcnt lgkmcnt(4)\n"
        "v_exp_f32 v54, v54\n"
        "v_mfma_f32_16x16x32_bf16 v[188:191], a[140:143], v[16:19], v[188:191]\n"
        "v_exp_f32 v55, v55\n"
        "v_mfma_f32_16x16x32_bf16 v[48:51], a[112:115], v[0:3], v[224:227]\n"
        "ds_read_b128 a[136:139], v241 offset:30720\n"
        "v_cvt_pk_bf16_f32 v87, v54, v55\n"
        "v_mfma_f32_16x16x32_bf16 v[48:51], a[116:119], v[4:7], v[48:51]\n"
        "ds_read_b128 a[140:143], v241 offset:31744\n"
        "v_xor_b32 v241, 0x8000, v241\n"
        "v_mfma_f32_16x16x32_bf16 v[48:51], a[120:123], v[8:11], v[48:51]\n"
        "v_exp_f32 v32, v32\n"
        "v_mfma_f32_16x16x32_bf16 v[48:51], a[124:127], v[12:15], v[48:51]\n"
        "v_exp_f32 v33, v33\n"
        "v_mfma_f32_16x16x32_bf16 v[52:55], a[124:127], v[76:79], v[228:231]\n"
        "ds_read_b128 a[108:111], v240 offset:11264\n"
        "v_cvt_pk_bf16_f32 v16, v32, v33\n"
        "v_mfma_f32_16x16x32_bf16 v[52:55], a[120:123], v[72:75], v[52:55]\n"
        "v_exp_f32 v34, v34\n"
        "v_mfma_f32_16x16x32_bf16 v[52:55], a[116:119], v[68:71], v[52:55]\n"
        "v_exp_f32 v35, v35\n"
        "v_mfma_f32_16x16x32_bf16 v[52:55], a[112:115], v[64:67], v[52:55]\n"
        "ds_read_b128 a[112:115], v240 offset:12288\n"
        "v_cvt_pk_bf16_f32 v17, v34, v35\n"
        "v_mfma_f32_16x16x32_bf16 a[64:67], a[80:83], v[80:83], a[64:67]\n"
        "v_exp_f32 v36, v36\n"
        "v_mfma_f32_16x16x32_bf16 a[68:71], a[80:83], v[84:87], a[68:71]\n"
        "s_waitcnt vmcnt(9) lgkmcnt(2)\n"
        "v_exp_f32 v37, v37\n"
        "v_mfma_f32_16x16x32_bf16 v[160:163], a[144:147], v[80:83], v[160:163]\n"
        "ds_read_b128 a[116:119], v240 offset:13312\n"
        "s_barrier\n"
        "v_cvt_pk_bf16_f32 v20, v36, v37\n"
        "v_mfma_f32_16x16x32_bf16 v[192:195], a[144:147], v[84:87], v[192:195]\n"
        "s_add_u32 s76, s66, s90\n"
        "s_addc_u32 s77, s67, 0\n"
        "v_mfma_f32_16x16x32_bf16 v[196:199], a[148:151], v[84:87], v[196:199]\n"
        "global_load_lds_dwordx4 v254, s[76:77] offset:0 sc1\n"
        "ds_read_b128 a[144:147], v241 offset:0\n"
        "v_mfma_f32_16x16x32_bf16 v[164:167], a[148:151], v[80:83], v[164:167]\n"
        "ds_read_b128 a[148:151], v241 offset:1024\n"
        "s_cmp_ge_u32 s82, s44\n"
        "s_addc_u32 s90, s82, 0\n"
        "v_mfma_f32_16x16x32_bf16 v[168:171], a[152:155], v[80:83], v[168:171]\n"
        "s_min_u32 s90, s90, 15\n"
        "v_exp_f32 v38, v38\n"
        "v_mfma_f32_16x16x32_bf16 v[200:203], a[152:155], v[84:87], v[200:203]\n"
        "s_lshl_b32 s90, s90, 15\n"
        "v_exp_f32 v39, v39\n"
        "v_mfma_f32_16x16x32_bf16 v[204:207], a[156:159], v[84:87], v[204:207]\n"
        "ds_read_b128 a[152:155], v241 offset:2048\n"
        "v_cvt_pk_bf16_f32 v21, v38, v39\n"
        "v_mfma_f32_16x16x32_bf16 v[172:175], a[156:159], v[80:83], v[172:175]\n"
        "global_load_lds_dwordx4 v254, s[76:77] offset:1024 sc1\n"
        "ds_read_b128 a[120:123], v240 offset:14336\n"
        "v_mfma_f32_16x16x32_bf16 v[32:35], a[96:99], v[0:3], v[224:227]\n"
        "ds_read_b128 a[156:159], v241 offset:3072\n"
        "s_add_u32 s68, s64, s90\n"
        "s_addc_u32 s69, s65, 0\n"
        "v_mfma_f32_16x16x32_bf16 v[32:35], a[100:103], v[4:7], v[32:35]\n"
        "s_waitcnt lgkmcnt(5)\n"
        "v_exp_f32 v48, v48\n"
        "v_mfma_f32_16x16x32_bf16 v[32:35], a[104:107], v[8:11], v[32:35]\n"
        "v_exp_f32 v49, v49\n"
        "v_mfma_f32_16x16x32_bf16 v[32:35], a[108:111], v[12:15], v[32:35]\n"
        "global_load_lds_dwordx4 v254, s[76:77] offset:2048 sc1\n"
        "v_cvt_pk_bf16_f32 v18, v48, v49\n"
        "v_mfma_f32_16x16x32_bf16 v[36:39], a[108:111], v[76:79], v[228:231]\n"
        "ds_read_b128 a[124:127], v240 offset:15360\n"
        "s_add_u32 s74, s68, 0x1000\n"
        "s_addc_u32 s75, s69, 0\n"
        "v_mfma_f32_16x16x32_bf16 v[36:39], a[104:107], v[72:75], v[36:39]\n"
        "v_exp_f32 v50, v50\n"
        "v_mfma_f32_16x16x32_bf16 v[36:39], a[100:103], v[68:71], v[36:39]\n"
        "v_exp_f32 v51, v51\n"
        "v_mfma_f32_16x16x32_bf16 v[36:39], a[96:99], v[64:67], v[36:39]\n"
        "global_load_lds_dwordx4 v254, s[76:77] offset:3072 sc1\n"
        "s_add_u32 m0, s79, 0x1000\n"
        "v_cvt_pk_bf16_f32 v19, v50, v51\n"
        "v_mfma_f32_16x16x32_bf16 v[176:179], a[128:131], v[80:83], v[176:179]\n"
        "ds_read_b128 a[96:99], v240 offset:16384\n"
        "s_add_u32 s76, s76, 0x1000\n"
        "s_addc_u32 s77, s77, 0\n"
        "v_mfma_f32_16x16x32_bf16 v[208:211], a[128:131], v[84:87], v[208:211]\n"
        "v_exp_f32 v52, v52\n"
        "v_mfma_f32_16x16x32_bf16 v[212:215], a[132:135], v[84:87], v[212:215]\n"
        "v_exp_f32 v53, v53\n"
        "v_mfma_f32_16x16x32_bf16 v[180:183], a[132:135], v[80:83], v[180:183]\n"
        "ds_read_b128 a[128:131], v241 offset:4096\n"
        "v_cvt_pk_bf16_f32 v22, v52, v53\n"
        "v_mfma_f32_16x16x32_bf16 v[184:187], a[136:139], v[80:83], v[184:187]\n"
        "global_load_lds_dwordx4 v254, s[76:77] offset:0 sc1\n"
        "ds_read_b128 a[100:103], v240 offset:17408\n"
        "v_mfma_f32_16x16x32_bf16 v[216:219], a[136:139], v[84:87], v[216:219]\n"
        "ds_read_b128 a[132:135], v241 offset:5120\n"
        "s_waitcnt lgkmcnt(4)\n"
        "v_mfma_f32_16x16x32_bf16 v[220:223], a[140:143], v[84:87], v[220:223]\n"
        "v_exp_f32 v54, v54\n"
        "v_mfma_f32_16x16x32_bf16 v[188:191], a[140:143], v[80:83], v[188:191]\n"
        "v_exp_f32 v55, v55\n"
        "v_mfma_f32_16x16x32_bf16 v[48:51], a[112:115], v[0:3], v[224:227]\n"
        "ds_read_b128 a[136:139], v241 offset:6144\n"
        "v_cvt_pk_bf16_f32 v23, v54, v55\n"
        "v_mfma_f32_16x16x32_bf16 v[48:51], a[116:119], v[4:7], v[48:51]\n"
        "global_load_lds_dwordx4 v254, s[76:77] offset:1024 sc1\n"
        "ds_read_b128 a[104:107], v240 offset:18432\n"
        "v_mfma_f32_16x16x32_bf16 v[48:51], a[120:123], v[8:11], v[48:51]\n"
        "ds_read_b128 a[140:143], v241 offset:7168\n"
        "v_mfma_f32_16x16x32_bf16 v[48:51], a[124:127], v[12:15], v[48:51]\n"
        "v_exp_f32 v32, v32\n"
        "v_mfma_f32_16x16x32_bf16 v[52:55], a[124:127], v[76:79], v[228:231]\n"
        "v_exp_f32 v33, v33\n"
        "v_mfma_f32_16x16x32_bf16 v[52:55], a[120:123], v[72:75], v[52:55]\n"
        "global_load_lds_dwordx4 v254, s[76:77] offset:2048 sc1\n"
        "v_cvt_pk_bf16_f32 v80, v32, v33\n"
        "v_mfma_f32_16x16x32_bf16 v[52:55], a[116:119], v[68:71], v[52:55]\n"
        "ds_read_b128 a[108:111], v240 offset:19456\n"
        "v_mfma_f32_16x16x32_bf16 v[52:55], a[112:115], v[64:67], v[52:55]\n"
        "v_exp_f32 v34, v34\n"
        "v_mfma_f32_16x16x32_bf16 a[64:67], a[80:83], v[16:19], a[64:67]\n"
        "v_exp_f32 v35, v35\n"
        "v_mfma_f32_16x16x32_bf16 a[68:71], a[80:83], v[20:23], a[68:71]\n"
        "global_load_lds_dwordx4 v254, s[76:77] offset:3072 sc1\n"
        "v_cvt_pk_bf16_f32 v81, v34, v35\n"
        "v_mfma_f32_16x16x32_bf16 v[160:163], a[144:147], v[16:19], v[160:163]\n"
        "ds_read_b128 a[112:115], v240 offset:20480\n"
        "v_mfma_f32_16x16x32_bf16 v[192:195], a[144:147], v[20:23], v[192:195]\n"
        "v_exp_f32 v36, v36\n"
        "v_mfma_f32_16x16x32_bf16 v[196:199], a[148:151], v[20:23], v[196:199]\n"
        "s_waitcnt lgkmcnt(2)\n"
        "v_exp_f32 v37, v37\n"
        "v_mfma_f32_16x16x32_bf16 v[164:167], a[148:151], v[16:19], v[164:167]\n"
        "ds_read_b128 a[144:147], v241 offset:8192\n"
        "v_cvt_pk_bf16_f32 v84, v36, v37\n"
        "v_mfma_f32_16x16x32_bf16 v[168:171], a[152:155], v[16:19], v[168:171]\n"
        "ds_read_b128 a[116:119], v240 offset:21504\n"
        "v_mfma_f32_16x16x32_bf16 v[200:203], a[152:155], v[20:23], v[200:203]\n"
        "ds_read_b128 a[148:151], v241 offset:9216\n"
        "v_mfma_f32_16x16x32_bf16 v[204:207], a[156:159], v[20:23], v[204:207]\n"
        "v_exp_f32 v38, v38\n"
        "v_mfma_f32_16x16x32_bf16 v[172:175], a[156:159], v[16:19], v[172:175]\n"
        "v_exp_f32 v39, v39\n"
        "v_mfma_f32_16x16x32_bf16 v[32:35], a[96:99], v[0:3], v[224:227]\n"
        "ds_read_b128 a[152:155], v241 offset:10240\n"
        "v_cvt_pk_bf16_f32 v85, v38, v39\n"
        "v_mfma_f32_16x16x32_bf16 v[32:35], a[100:103], v[4:7], v[32:35]\n"
        "ds_read_b128 a[120:123], v240 offset:22528\n"
        "s_waitcnt lgkmcnt(5)\n"
        "v_mfma_f32_16x16x32_bf16 v[32:35], a[104:107], v[8:11], v[32:35]\n"
        "ds_read_b128 a[156:159], v241 offset:11264\n"
        "v_mfma_f32_16x16x32_bf16 v[32:35], a[108:111], v[12:15], v[32:35]\n"
        "v_exp_f32 v48, v48\n"
        "v_mfma_f32_16x16x32_bf16 v[36:39], a[108:111], v[76:79], v[228:231]\n"
        "v_exp_f32 v49, v49\n"
        "v_mfma_f32_16x16x32_bf16 v[36:39], a[104:107], v[72:75], v[36:39]\n"
        "ds_read_b128 a[124:127], v240 offset:23552\n"
        "v_cvt_pk_bf16_f32 v82, v48, v49\n"
        "v_mfma_f32_16x16x32_bf16 v[36:39], a[100:103], v[68:71], v[36:39]\n"
        "v_exp_f32 v50, v50\n"
        "v_mfma_f32_16x16x32_bf16 v[36:39], a[96:99], v[64:67], v[36:39]\n"
        "v_exp_f32 v51, v51\n"
        "v_mfma_f32_16x16x32_bf16 v[176:179], a[128:131], v[16:19], v[176:179]\n"
        "ds_read_b128 a[96:99], v240 offset:24576\n"
        "v_cvt_pk_bf16_f32 v83, v50, v51\n"
        "v_mfma_f32_16x16x32_bf16 v[208:211], a[128:131], v[20:23], v[208:211]\n"
        "v_exp_f32 v52, v52\n"
        "v_mfma_f32_16x16x32_bf16 v[212:215], a[132:135], v[20:23], v[212:215]\n"
        "v_exp_f32 v53, v53\n"
        "v_mfma_f32_16x16x32_bf16 v[180:183], a[132:135], v[16:19], v[180:183]\n"
        "ds_read_b128 a[128:131], v241 offset:12288\n"
        "v_cvt_pk_bf16_f32 v86, v52, v53\n"
        "v_mfma_f32_16x16x32_bf16 v[184:187], a[136:139], v[16:19], v[184:187]\n"
        "ds_read_b128 a[100:103], v240 offset:25600\n"
        "s_waitcnt lgkmcnt(4)\n"
        "v_mfma_f32_16x16x32_bf16 v[216:219], a[136:139], v[20:23], v[216:219]\n"
        "ds_read_b128 a[132:135], v241 offset:13312\n"
        "v_mfma_f32_16x16x32_bf16 v[220:223], a[140:143], v[20:23], v[220:223]\n"
        "v_exp_f32 v54, v54\n"
        "v_mfma_f32_16x16x32_bf16 v[188:191], a[140:143], v[16:19], v[188:191]\n"
        "v_exp_f32 v55, v55\n"
        "v_mfma_f32_16x16x32_bf16 v[48:51], a[112:115], v[0:3], v[224:227]\n"
        "ds_read_b128 a[136:139], v241 offset:14336\n"
        "v_cvt_pk_bf16_f32 v87, v54, v55\n"
        "v_mfma_f32_16x16x32_bf16 v[48:51], a[116:119], v[4:7], v[48:51]\n"
        "ds_read_b128 a[104:107], v240 offset:26624\n"
        "global_load_dword v253, v245, s[26:27] offset:0\n"
        "v_mfma_f32_16x16x32_bf16 v[48:51], a[120:123], v[8:11], v[48:51]\n"
        "s_sub_u32 s26, s26, 262144\n"
        "s_subb_u32 s27, s27, 0\n"
        "s_waitcnt lgkmcnt(5)\n"
        "v_mfma_f32_16x16x32_bf16 v[48:51], a[124:127], v[12:15], v[48:51]\n"
        "ds_read_b128 a[140:143], v241 offset:15360\n"
        "v_mfma_f32_16x16x32_bf16 v[52:55], a[124:127], v[76:79], v[228:231]\n"
        "v_exp_f32 v32, v32\n"
        "v_mfma_f32_16x16x32_bf16 v[52:55], a[120:123], v[72:75], v[52:55]\n"
        "v_exp_f32 v33, v33\n"
        "v_mfma_f32_16x16x32_bf16 v[52:55], a[116:119], v[68:71], v[52:55]\n"
        "ds_read_b128 a[108:111], v240 offset:27648\n"
        "v_cvt_pk_bf16_f32 v16, v32, v33\n"
        "v_mfma_f32_16x16x32_bf16 v[52:55], a[112:115], v[64:67], v[52:55]\n"
        "v_exp_f32 v34, v34\n"
        "v_mfma_f32_16x16x32_bf16 a[64:67], a[80:83], v[80:83], a[64:67]\n"
        "v_exp_f32 v35, v35\n"
        "v_mfma_f32_16x16x32_bf16 a[68:71], a[80:83], v[84:87], a[68:71]\n"
        "ds_read_b128 a[112:115], v240 offset:28672\n"
        "v_cvt_pk_bf16_f32 v17, v34, v35\n"
        "v_mfma_f32_16x16x32_bf16 v[160:163], a[144:147], v[80:83], v[160:163]\n"
        "v_exp_f32 v36, v36\n"
        "v_mfma_f32_16x16x32_bf16 v[192:195], a[144:147], v[84:87], v[192:195]\n"
        "v_exp_f32 v37, v37\n"
        "v_mfma_f32_16x16x32_bf16 v[196:199], a[148:151], v[84:87], v[196:199]\n"
        "ds_read_b128 a[144:147], v241 offset:16384\n"
        "v_cvt_pk_bf16_f32 v20, v36, v37\n"
        "v_mfma_f32_16x16x32_bf16 v[164:167], a[148:151], v[80:83], v[164:167]\n"
        "ds_read_b128 a[116:119], v240 offset:29696\n"
        "s_waitcnt lgkmcnt(4)\n"
        "v_mfma_f32_16x16x32_bf16 v[168:171], a[152:155], v[80:83], v[168:171]\n"
        "ds_read_b128 a[148:151], v241 offset:17408\n"
        "v_mfma_f32_16x16x32_bf16 v[200:203], a[152:155], v[84:87], v[200:203]\n"
        "v_exp_f32 v38, v38\n"
        "v_mfma_f32_16x16x32_bf16 v[204:207], a[156:159], v[84:87], v[204:207]\n"
        "v_exp_f32 v39, v39\n"
        "v_mfma_f32_16x16x32_bf16 v[172:175], a[156:159], v[80:83], v[172:175]\n"
        "ds_read_b128 a[152:155], v241 offset:18432\n"
        "v_cvt_pk_bf16_f32 v21, v38, v39\n"
        "v_mfma_f32_16x16x32_bf16 v[32:35], a[96:99], v[0:3], v[224:227]\n"
        "ds_read_b128 a[124:127], v240 offset:31744\n"
        "s_waitcnt lgkmcnt(5)\n"
        "v_mfma_f32_16x16x32_bf16 v[32:35], a[100:103], v[4:7], v[32:35]\n"
        "ds_read_b128 a[120:123], v240 offset:30720\n"
        "v_xor_b32 v240, 0x8000, v240\n"
        "v_mfma_f32_16x16x32_bf16 v[32:35], a[104:107], v[8:11], v[32:35]\n"
        "ds_read_b128 a[156:159], v241 offset:19456\n"
        "v_mfma_f32_16x16x32_bf16 v[32:35], a[108:111], v[12:15], v[32:35]\n"
        "v_exp_f32 v48, v48\n"
        "v_mfma_f32_16x16x32_bf16 v[36:39], a[108:111], v[76:79], v[228:231]\n"
        "v_exp_f32 v49, v49\n"
        "v_mfma_f32_16x16x32_bf16 v[36:39], a[104:107], v[72:75], v[36:39]\n"
        "v_cvt_pk_bf16_f32 v18, v48, v49\n"
        "v_mfma_f32_16x16x32_bf16 v[36:39], a[100:103], v[68:71], v[36:39]\n"
        "v_exp_f32 v50, v50\n"
        "v_mfma_f32_16x16x32_bf16 v[36:39], a[96:99], v[64:67], v[36:39]\n"
        "v_exp_f32 v51, v51\n"
        "v_mfma_f32_16x16x32_bf16 v[176:179], a[128:131], v[80:83], v[176:179]\n"
        "v_cvt_pk_bf16_f32 v19, v50, v51\n"
        "v_mfma_f32_16x16x32_bf16 v[208:211], a[128:131], v[84:87], v[208:211]\n"
        "s_waitcnt vmcnt(9) lgkmcnt(1)\n"
        "s_barrier\n"
        "v_mfma_f32_16x16x32_bf16 v[212:215], a[132:135], v[84:87], v[212:215]\n"
        "s_cmp_eq_u32 s82, s83\n"
        "s_cselect_b64 s[68:69], s[92:93], s[68:69]\n"
        "s_cselect_b32 s80, s50, s78\n"
        "v_mfma_f32_16x16x32_bf16 v[180:183], a[132:135], v[80:83], v[180:183]\n"
        "s_cmp_eq_u32 s82, s83\n"
        "s_cselect_b64 s[74:75], s[94:95], s[74:75]\n"
        "s_cselect_b32 s81, s51, s81\n"
        "v_mfma_f32_16x16x32_bf16 v[184:187], a[136:139], v[80:83], v[184:187]\n"
        "s_add_u32 m0, s80, 0x0\n"
        "ds_read_b128 a[96:99], v240 offset:0\n"
        "v_mfma_f32_16x16x32_bf16 v[216:219], a[136:139], v[84:87], v[216:219]\n"
        "global_load_lds_dwordx4 v254, s[68:69] offset:0 sc1\n"
        "ds_read_b128 a[128:131], v241 offset:20480\n"
        "v_mfma_f32_16x16x32_bf16 v[220:223], a[140:143], v[84:87], v[220:223]\n"
        "v_exp_f32 v52, v52\n"
        "v_mfma_f32_16x16x32_bf16 v[188:191], a[140:143], v[80:83], v[188:191]\n"
        "s_waitcnt lgkmcnt(2)\n"
        "v_exp_f32 v53, v53\n"
        "v_mfma_f32_16x16x32_bf16 v[48:51], a[112:115], v[0:3], v[224:227]\n"
        "ds_read_b128 a[100:103], v240 offset:1024\n"
        "v_cvt_pk_bf16_f32 v22, v52, v53\n"
        "v_mfma_f32_16x16x32_bf16 v[48:51], a[116:119], v[4:7], v[48:51]\n"
        "ds_read_b128 a[132:135], v241 offset:21504\n"
        "global_load_lds_dwordx4 v254, s[68:69] offset:1024 sc1\n"
        "v_mfma_f32_16x16x32_bf16 v[48:51], a[120:123], v[8:11], v[48:51]\n"
        "v_exp_f32 v54, v54\n"
        "v_mfma_f32_16x16x32_bf16 v[48:51], a[124:127], v[12:15], v[48:51]\n"
        "v_exp_f32 v55, v55\n"
        "v_mfma_f32_16x16x32_bf16 a[64:67], a[80:83], v[16:19], a[64:67]\n"
        "ds_read_b128 a[136:139], v241 offset:22528\n"
        "v_cvt_pk_bf16_f32 v23, v54, v55\n"
        "v_mfma_f32_16x16x32_bf16 v[52:55], a[124:127], v[76:79], v[228:231]\n"
        "ds_read_b128 a[104:107], v240 offset:2048\n"
        "global_load_lds_dwordx4 v254, s[68:69] offset:2048 sc1\n"
        "v_mfma_f32_16x16x32_bf16 v[52:55], a[120:123], v[72:75], v[52:55]\n"
        "ds_read_b128 a[140:143], v241 offset:23552\n"
        "s_waitcnt lgkmcnt(5)\n"
        "v_mfma_f32_16x16x32_bf16 v[52:55], a[116:119], v[68:71], v[52:55]\n"
        "v_exp_f32 v32, v32\n"
        "v_mfma_f32_16x16x32_bf16 v[52:55], a[112:115], v[64:67], v[52:55]\n"
        "v_exp_f32 v33, v33\n"
        "v_mfma_f32_16x16x32_bf16 a[68:71], a[80:83], v[20:23], a[68:71]\n"
        "ds_read_b128 a[108:111], v240 offset:3072\n"
        "v_cvt_pk_bf16_f32 v80, v32, v33\n"
        "v_mfma_f32_16x16x32_bf16 v[160:163], a[144:147], v[16:19], v[160:163]\n"
        "global_load_lds_dwordx4 v254, s[68:69] offset:3072 sc1\n"
        "ds_read_b128 a[112:115], v240 offset:4096\n"
        "v_mfma_f32_16x16x32_bf16 v[192:195], a[144:147], v[20:23], v[192:195]\n"
        "v_exp_f32 v34, v34\n"
        "v_mfma_f32_16x16x32_bf16 v[196:199], a[148:151], v[20:23], v[196:199]\n"
        "v_exp_f32 v35, v35\n"
        "v_mfma_f32_16x16x32_bf16 v[164:167], a[148:151], v[16:19], v[164:167]\n"
        "ds_read_b128 a[144:147], v241 offset:24576\n"
        "v_cvt_pk_bf16_f32 v81, v34, v35\n"
        "v_mfma_f32_16x16x32_bf16 v[168:171], a[152:155], v[16:19], v[168:171]\n"
        "v_exp_f32 v36, v36\n"
        "v_mfma_f32_16x16x32_bf16 v[200:203], a[152:155], v[20:23], v[200:203]\n"
        "v_exp_f32 v37, v37\n"
        "v_mfma_f32_16x16x32_bf16 v[204:207], a[156:159], v[20:23], v[204:207]\n"
        "ds_read_b128 a[116:119], v240 offset:5120\n"
        "v_cvt_pk_bf16_f32 v84, v36, v37\n"
        "v_mfma_f32_16x16x32_bf16 v[172:175], a[156:159], v[16:19], v[172:175]\n"
        "ds_read_b128 a[148:151], v241 offset:25600\n"
        "v_exp_f32 v38, v38\n"
        "v_exp_f32 v39, v39\n"
        "ds_read_b128 a[152:155], v241 offset:26624\n"
        "v_cvt_pk_bf16_f32 v85, v38, v39\n"
        "ds_read_b128 a[120:123], v240 offset:6144\n"
        "s_cmp_lg_u32 s91, 0\n"
        "s_cbranch_scc1 LSI_%=\n"
        "; ---- BACK\n"
        "LTL_%=:\n"
        "v_mfma_f32_16x16x32_bf16 v[176:179], a[128:131], v[16:19], v[176:179]\n"
        "s_lshl_b32 s53, s45, 20\n"
        "s_lshl_b32 s96, s43, 15\n"
        "s_add_u32 s53, s53, s96\n"
        "v_mfma_f32_16x16x32_bf16 v[208:211], a[128:131], v[20:23], v[208:211]\n"
        "s_lshl_b32 s96, s40, 10\n"
        "s_add_u32 s53, s53, s96\n"
        "s_waitcnt lgkmcnt(3)\n"
        "v_mfma_f32_16x16x32_bf16 v[212:215], a[132:135], v[20:23], v[212:215]\n"
        "s_add_u32 s53, s53, 0x2001000\n"
        "s_add_u32 s70, s38, s53\n"
        "s_addc_u32 s71, s39, 0\n"
        "v_mfma_f32_16x16x32_bf16 v[180:183], a[132:135], v[16:19], v[180:183]\n"
        "ds_read_b128 a[156:159], v241 offset:27648\n"
        "s_mul_i32 s14, s40, 4224\n"
        "v_bfe_u32 v232, v254, 4, 4\n"
        "v_mfma_f32_16x16x32_bf16 v[184:187], a[136:139], v[16:19], v[184:187]\n"
        "s_add_u32 s15, s51, 4098\n"
        "v_exp_f32 v48, v48\n"
        "v_mfma_f32_16x16x32_bf16 v[216:219], a[136:139], v[20:23], v[216:219]\n"
        "s_add_u32 s14, s14, 0x20240\n"
        "v_exp_f32 v49, v49\n"
        "v_mfma_f32_16x16x32_bf16 v[220:223], a[140:143], v[20:23], v[220:223]\n"
        "ds_read_b128 a[128:131], v241 offset:28672\n"
        "s_sub_u32 s15, s15, s14\n"
        "v_cvt_pk_bf16_f32 v82, v48, v49\n"
        "v_mfma_f32_16x16x32_bf16 v[188:191], a[140:143], v[16:19], v[188:191]\n"
        "v_exp_f32 v50, v50\n"
        "v_exp_f32 v51, v51\n"
        "ds_read_b128 a[132:135], v241 offset:29696\n"
        "v_cvt_pk_bf16_f32 v83, v50, v51\n"
        "s_nop 1\n"
        "v_mfma_f32_16x16x32_bf16 v[160:163], a[144:147], v[80:83], v[160:163]\n"
        "s_waitcnt lgkmcnt(4)\n"
        "v_exp_f32 v52, v52\n"
        "v_mfma_f32_16x16x32_bf16 a[64:67], a[80:83], v[80:83], a[64:67]\n"
        "s_waitcnt lgkmcnt(2)\n"
        "v_exp_f32 v53, v53\n"
        "v_mfma_f32_16x16x32_bf16 v[164:167], a[148:151], v[80:83], v[164:167]\n"
        "ds_read_b128 a[136:139], v241 offset:30720\n"
        "s_add_u32 s16, s51, 4096\n"
        "v_cvt_pk_bf16_f32 v86, v52, v53\n"
        "v_mfma_f32_16x16x32_bf16 v[168:171], a[152:155], v[80:83], v[168:171]\n"
        "ds_read_b128 a[140:143], v241 offset:31744\n"
        "s_and_b32 s10, s41, 1\n"
        "v_and_b32 v224, 1, v232\n"
        "v_mfma_f32_16x16x32_bf16 v[172:175], a[156:159], v[80:83], v[172:175]\n"
        "s_waitcnt lgkmcnt(3)\n"
        "v_mul_lo_u32 v224, v224, s15\n"
        "v_add_u32 v224, s14, v224\n"
        "v_mfma_f32_16x16x32_bf16 v[176:179], a[128:131], v[80:83], v[176:179]\n"
        "s_add_u32 s14, s51, 480\n"
        "v_lshrrev_b32 v233, 8, v254\n"
        "v_lshlrev_b32 v228, 5, v232\n"
        "v_lshl_add_u32 v224, v232, 1, v224\n"
        "v_mov_b32 v236, 0\n"
        "s_waitcnt lgkmcnt(2)\n"
        "v_mfma_f32_16x16x32_bf16 v[180:183], a[132:135], v[80:83], v[180:183]\n"
        "s_and_b32 s11, s41, 4\n"
        "v_mov_b32 v237, 0\n"
        "v_mov_b32 v238, 0\n"
        "v_mov_b32 v239, 0\n"
        "v_add_u32 v229, s16, v254\n"
        "v_sub_u32 v228, s14, v228\n"
        "v_lshl_add_u32 v224, v233, 4, v224\n"
        "v_lshl_add_u32 v228, v233, 4, v228\n"
        "v_add_u32 v225, 1024, v224\n"
        "v_add_u32 v226, 2048, v224\n"
        "v_add_u32 v227, 3072, v224\n"
        "v_accvgpr_read_b32 v248, a64\n"
        "s_waitcnt lgkmcnt(1)\n"
        "v_mfma_f32_16x16x32_bf16 v[184:187], a[136:139], v[80:83], v[184:187]\n"
        "s_waitcnt lgkmcnt(0)\n"
        "v_exp_f32 v54, v54\n"
        "v_mfma_f32_16x16x32_bf16 v[188:191], a[140:143], v[80:83], v[188:191]\n"
        "s_mov_b64 exec, 3\n"
        "ds_write_b128 v229, v[236:239]\n"
        "s_mov_b64 exec, -1\n"
        "v_exp_f32 v55, v55\n"
        "ds_read2_b32 a[0:1], v224 offset0:0 offset1:1\n"
        "v_cvt_pk_bf16_f32 v87, v54, v55\n"
        "s_nop 1\n"
        "v_mfma_f32_16x16x32_bf16 a[68:71], a[80:83], v[84:87], a[68:71]\n"
        "s_and_b32 s14, s42, 3\n"
        "v_rcp_f32 v246, v248\n"
        "v_mfma_f32_16x16x32_bf16 v[192:195], a[144:147], v[84:87], v[192:195]\n"
        "ds_read2_b32 a[2:3], v224 offset0:2 offset1:3\n"
        "s_add_u32 s11, s11, s14\n"
        "v_add_u32 v230, 3584, v228\n"
        "v_mfma_f32_16x16x32_bf16 v[196:199], a[148:151], v[84:87], v[196:199]\n"
        "ds_read2_b32 a[4:5], v224 offset0:128 offset1:129\n"
        "s_and_b32 s12, s41, 2\n"
        "v_accvgpr_read_b32 v249, a68\n"
        "v_mfma_f32_16x16x32_bf16 v[200:203], a[152:155], v[84:87], v[200:203]\n"
        "s_lshr_b32 s14, s42, 2\n"
        "v_rcp_f32 v247, v249\n"
        "v_mfma_f32_16x16x32_bf16 v[204:207], a[156:159], v[84:87], v[204:207]\n"
        "ds_read2_b32 a[6:7], v224 offset0:130 offset1:131\n"
        "s_lshl2_add_u32 s12, s12, s14\n"
        "v_min_u32 v230, s16, v230\n"
        "v_mfma_f32_16x16x32_bf16 v[208:211], a[128:131], v[84:87], v[208:211]\n"
        "ds_read_b128 a[192:195], v230 offset:0\n"
        "s_lshl3_add_u32 s13, s12, s11\n"
        "v_mul_f32 v192, v192, v247\n"
        "v_mfma_f32_16x16x32_bf16 v[212:215], a[132:135], v[84:87], v[212:215]\n"
        "ds_read_b128 a[196:199], v228 offset:3072\n"
        "s_lshl1_add_u32 s13, s13, s10\n"
        "v_mul_f32 v193, v193, v247\n"
        "v_mfma_f32_16x16x32_bf16 v[216:219], a[136:139], v[84:87], v[216:219]\n"
        "ds_read2_b32 a[8:9], v225 offset0:0 offset1:1\n"
        "s_lshl_b32 s14, s10, 22\n"
        "v_mul_f32 v194, v194, v247\n"
        "v_mfma_f32_16x16x32_bf16 v[220:223], a[140:143], v[84:87], v[220:223]\n"
        "ds_read2_b32 a[10:11], v225 offset0:2 offset1:3\n"
        "ds_read_b128 a[200:203], v228 offset:2560\n"
        "ds_read2_b32 a[12:13], v225 offset0:128 offset1:129\n"
        "ds_read2_b32 a[14:15], v225 offset0:130 offset1:131\n"
        "ds_read_b128 a[204:207], v228 offset:2048\n"
        "ds_read2_b32 a[16:17], v226 offset0:0 offset1:1\n"
        "ds_read2_b32 a[18:19], v226 offset0:2 offset1:3\n"
        "ds_read_b128 a[208:211], v228 offset:1536\n"
        "ds_read2_b32 a[20:21], v226 offset0:128 offset1:129\n"
        "s_lshl_b32 s15, s40, 15\n"
        "v_mul_f32 v195, v195, v247\n"
        "s_waitcnt lgkmcnt(11)\n"
        "v_mfma_f32_16x16x32_bf16 v[96:99], a[0:3], a[192:195], 0\n"
        "s_add_u32 s14, s14, s15\n"
        "v_mul_f32 v196, v196, v247\n"
        "s_waitcnt lgkmcnt(10)\n"
        "v_mfma_f32_16x16x32_bf16 v[100:103], a[0:3], a[196:199], 0\n"
        "ds_read2_b32 a[22:23], v226 offset0:130 offset1:131\n"
        "s_lshl_b32 s15, s11, 8\n"
        "v_mul_f32 v197, v197, v247\n"
        "v_mfma_f32_16x16x32_bf16 v[100:103], a[4:7], a[192:195], v[100:103]\n"
        "s_add_u32 s14, s14, s15\n"
        "v_mul_f32 v198, v198, v247\n"
        "s_waitcnt lgkmcnt(9)\n"
        "v_mfma_f32_16x16x32_bf16 v[104:107], a[8:11], a[192:195], 0\n"
        "ds_read_b128 a[212:215], v228 offset:1024\n"
        "s_lshl_b32 s15, s10, 19\n"
        "v_mul_f32 v199, v199, v247\n"
        "v_mfma_f32_16x16x32_bf16 v[104:107], a[4:7], a[196:199], v[104:107]\n"
        "s_waitcnt lgkmcnt(8)\n"
        "v_cvt_pk_bf16_f32 v192, v192, v193\n"
        "v_cvt_pk_bf16_f32 v193, v194, v195\n"
        "v_mfma_f32_16x16x32_bf16 v[104:107], a[0:3], a[200:203], v[104:107]\n"
        "s_waitcnt lgkmcnt(6)\n"
        "v_cvt_pk_bf16_f32 v194, v196, v197\n"
        "v_cvt_pk_bf16_f32 v195, v198, v199\n"
        "v_mfma_f32_16x16x32_bf16 v[108:111], a[0:3], a[204:207], 0\n"
        "global_store_dwordx4 v254, v[192:195], s[70:71] offset:-4096 sc1\n"
        "s_waitcnt lgkmcnt(3)\n"
        "v_mul_f32 v160, v160, v246\n"
        "v_mul_f32 v161, v161, v246\n"
        "v_mul_f32 v162, v162, v246\n"
        "v_mfma_f32_16x16x32_bf16 v[108:111], a[4:7], a[200:203], v[108:111]\n"
        "ds_read2_b32 a[24:25], v227 offset0:0 offset1:1\n"
        "s_add_u32 m0, s73, 0x0\n"
        "v_mul_f32 v163, v163, v246\n"
        "v_mfma_f32_16x16x32_bf16 v[108:111], a[8:11], a[196:199], v[108:111]\n"
        "ds_read2_b32 a[26:27], v227 offset0:2 offset1:3\n"
        "s_lshl1_add_u32 s62, s12, s10\n"
        "v_mul_f32 v164, v164, v246\n"
        "v_mfma_f32_16x16x32_bf16 v[108:111], a[12:15], a[192:195], v[108:111]\n"
        "s_waitcnt lgkmcnt(3)\n"
        "v_mul_f32 v165, v165, v246\n"
        "v_mul_f32 v166, v166, v246\n"
        "v_mfma_f32_16x16x32_bf16 v[128:131], a[16:19], a[192:195], 0\n"
        "s_waitcnt lgkmcnt(2)\n"
        "v_mul_f32 v167, v167, v246\n"
        "v_cvt_pk_bf16_f32 v160, v160, v161\n"
        "v_mfma_f32_16x16x32_bf16 v[128:131], a[12:15], a[196:199], v[128:131]\n"
        "ds_read_b128 a[216:219], v228 offset:512\n"
        "s_lshl_b32 s62, s62, 6\n"
        "v_cvt_pk_bf16_f32 v161, v162, v163\n"
        "v_mfma_f32_16x16x32_bf16 v[128:131], a[8:11], a[200:203], v[128:131]\n"
        "ds_read2_b32 a[28:29], v227 offset0:128 offset1:129\n"
        "s_lshl2_add_u32 s63, s11, s40\n"
        "v_cvt_pk_bf16_f32 v162, v164, v165\n"
        "v_mfma_f32_16x16x32_bf16 v[128:131], a[4:7], a[204:207], v[128:131]\n"
        "ds_read2_b32 a[30:31], v227 offset0:130 offset1:131\n"
        "s_lshl_b32 s63, s63, 5\n"
        "v_cvt_pk_bf16_f32 v163, v166, v167\n"
        "v_mfma_f32_16x16x32_bf16 v[128:131], a[0:3], a[208:211], v[128:131]\n"
        "global_store_dwordx4 v254, v[160:163], s[70:71] offset:0 sc1\n"
        "s_add_u32 s70, s70, 0x2000\n"
        "s_addc_u32 s71, s71, 0\n"
        "ds_read_b128 a[220:223], v228 offset:0\n"
        "v_mul_f32 v200, v200, v247\n"
        "v_mfma_f32_16x16x32_bf16 v[132:135], a[0:3], a[212:215], 0\n"
        "s_waitcnt lgkmcnt(4)\n"
        "v_mul_f32 v201, v201, v247\n"
        "v_mul_f32 v202, v202, v247\n"
        "v_mfma_f32_16x16x32_bf16 v[132:135], a[4:7], a[208:211], v[132:135]\n"
        "ds_read2_b32 a[32:33], v224 offset0:16 offset1:17\n"
        "v_mul_f32 v203, v203, v247\n"
        "v_mfma_f32_16x16x32_bf16 v[132:135], a[8:11], a[204:207], v[132:135]\n"
        "ds_read2_b32 a[34:35], v224 offset0:18 offset1:19\n"
        "v_mul_f32 v204, v204, v247\n"
        "v_mfma_f32_16x16x32_bf16 v[132:135], a[12:15], a[200:203], v[132:135]\n"
        "ds_read2_b32 a[36:37], v224 offset0:144 offset1:145\n"
        "v_mul_f32 v205, v205, v247\n"
        "v_mfma_f32_16x16x32_bf16 v[132:135], a[16:19], a[196:199], v[132:135]\n"
        "ds_read2_b32 a[38:39], v224 offset0:146 offset1:147\n"
        "v_mul_f32 v206, v206, v247\n"
        "v_mfma_f32_16x16x32_bf16 v[132:135], a[20:23], a[192:195], v[132:135]\n"
        "ds_read_b128 a[228:231], v228 offset:3136\n"
        "v_mul_f32 v207, v207, v247\n"
        "v_mfma_f32_16x16x32_bf16 v[136:139], a[24:27], a[192:195], 0\n"
        "s_waitcnt lgkmcnt(6)\n"
        "v_cvt_pk_bf16_f32 v200, v200, v201\n"
        "v_cvt_pk_bf16_f32 v201, v202, v203\n"
        "v_mfma_f32_16x16x32_bf16 v[136:139], a[20:23], a[196:199], v[136:139]\n"
        "s_waitcnt lgkmcnt(5)\n"
        "v_cvt_pk_bf16_f32 v202, v204, v205\n"
        "v_cvt_pk_bf16_f32 v203, v206, v207\n"
        "v_mfma_f32_16x16x32_bf16 v[136:139], a[16:19], a[200:203], v[136:139]\n"
        "global_store_dwordx4 v254, v[200:203], s[70:71] offset:-4096 sc1\n"
        "ds_read2_b32 a[40:41], v225 offset0:16 offset1:17\n"
        "ds_read2_b32 a[42:43], v225 offset0:18 offset1:19\n"
        "v_mul_f32 v168, v168, v246\n"
        "v_mfma_f32_16x16x32_bf16 v[136:139], a[12:15], a[204:207], v[136:139]\n"
        "ds_read_b128 a[232:235], v228 offset:2624\n"
        "v_mul_f32 v169, v169, v246\n"
        "v_mfma_f32_16x16x32_bf16 v[136:139], a[8:11], a[208:211], v[136:139]\n"
        "ds_read2_b32 a[44:45], v225 offset0:144 offset1:145\n"
        "v_mul_f32 v170, v170, v246\n"
        "v_mfma_f32_16x16x32_bf16 v[136:139], a[4:7], a[212:215], v[136:139]\n"
        "s_waitcnt lgkmcnt(6)\n"
        "v_mul_f32 v171, v171, v246\n"
        "v_mul_f32 v172, v172, v246\n"
        "v_mfma_f32_16x16x32_bf16 v[136:139], a[0:3], a[216:219], v[136:139]\n"
        "s_waitcnt lgkmcnt(4)\n"
        "v_mul_f32 v173, v173, v246\n"
        "v_mul_f32 v174, v174, v246\n"
        "v_mfma_f32_16x16x32_bf16 v[140:143], a[0:3], a[220:223], 0\n"
        "ds_read2_b32 a[46:47], v225 offset0:146 offset1:147\n"
        "v_mul_f32 v175, v175, v246\n"
        "v_mfma_f32_16x16x32_bf16 v[140:143], a[4:7], a[216:219], v[140:143]\n"
        "ds_read_b128 a[236:239], v228 offset:2112\n"
        "v_cvt_pk_bf16_f32 v168, v168, v169\n"
        "v_mfma_f32_16x16x32_bf16 v[140:143], a[8:11], a[212:215], v[140:143]\n"
        "ds_read2_b32 a[48:49], v226 offset0:16 offset1:17\n"
        "v_cvt_pk_bf16_f32 v169, v170, v171\n"
        "v_mfma_f32_16x16x32_bf16 v[140:143], a[12:15], a[208:211], v[140:143]\n"
        "ds_read2_b32 a[50:51], v226 offset0:18 offset1:19\n"
        "v_cvt_pk_bf16_f32 v170, v172, v173\n"
        "v_mfma_f32_16x16x32_bf16 v[140:143], a[16:19], a[204:207], v[140:143]\n"
        "ds_read_b128 a[240:243], v228 offset:1600\n"
        "v_cvt_pk_bf16_f32 v171, v174, v175\n"
        "v_mfma_f32_16x16x32_bf16 v[140:143], a[20:23], a[200:203], v[140:143]\n"
        "global_store_dwordx4 v254, v[168:171], s[70:71] offset:0 sc1\n"
        "s_add_u32 s70, s70, 0x2000\n"
        "s_addc_u32 s71, s71, 0\n"
        "ds_read2_b32 a[52:53], v226 offset0:144 offset1:145\n"
        "v_mul_f32 v208, v208, v247\n"
        "v_mfma_f32_16x16x32_bf16 v[140:143], a[24:27], a[196:199], v[140:143]\n"
        "ds_read2_b32 a[54:55], v226 offset0:146 offset1:147\n"
        "v_mul_f32 v209, v209, v247\n"
        "v_mfma_f32_16x16x32_bf16 v[140:143], a[28:31], a[192:195], v[140:143]\n"
        "s_waitcnt lgkmcnt(7)\n"
        "v_mul_f32 v210, v210, v247\n"
        "v_mul_f32 v211, v211, v247\n"
        "v_mfma_f32_16x16x32_bf16 v[100:103], a[32:35], a[228:231], v[100:103]\n"
        "s_waitcnt lgkmcnt(5)\n"
        "v_mul_f32 v212, v212, v247\n"
        "v_mul_f32 v213, v213, v247\n"
        "LHB0_%=:\n"
        "s_cbranch_execz LHB4_%=\n"
        "v_mfma_f32_16x16x32_bf16 v[108:111], a[32:35], a[236:239], v[108:111]\n"
        "ds_read_b128 a[244:247], v228 offset:1088\n"
        "v_mul_f32 v214, v214, v247\n"
        "v_mfma_f32_16x16x32_bf16 v[108:111], a[36:39], a[232:235], v[108:111]\n"
        "ds_read2_b32 a[56:57], v227 offset0:16 offset1:17\n"
        "v_mul_f32 v215, v215, v247\n"
        "v_mfma_f32_16x16x32_bf16 v[108:111], a[40:43], a[228:231], v[108:111]\n"
        "ds_read2_b32 a[58:59], v227 offset0:18 offset1:19\n"
        "ds_read_b128 a[248:251], v228 offset:576\n"
        "ds_read2_b32 a[60:61], v227 offset0:144 offset1:145\n"
        "ds_read2_b32 a[62:63], v227 offset0:146 offset1:147\n"
        "ds_read_b128 a[252:255], v228 offset:64\n"
        "ds_read2_b32 a[0:1], v224 offset0:32 offset1:33\n"
        "ds_read2_b32 a[2:3], v224 offset0:34 offset1:35\n"
        "ds_read2_b32 a[4:5], v224 offset0:160 offset1:161\n"
        "ds_read2_b32 a[6:7], v224 offset0:162 offset1:163\n"
        "ds_read_b128 a[196:199], v228 offset:3200\n"
        "ds_read2_b32 a[8:9], v225 offset0:32 offset1:33\n"
        "v_cvt_pk_bf16_f32 v208, v208, v209\n"
        "s_waitcnt lgkmcnt(12)\n"
        "v_mfma_f32_16x16x32_bf16 v[132:135], a[32:35], a[244:247], v[132:135]\n"
        "ds_read2_b32 a[10:11], v225 offset0:34 offset1:35\n"
        "v_cvt_pk_bf16_f32 v209, v210, v211\n"
        "v_mfma_f32_16x16x32_bf16 v[132:135], a[36:39], a[240:243], v[132:135]\n"
        "ds_read_b128 a[200:203], v228 offset:2688\n"
        "v_cvt_pk_bf16_f32 v210, v212, v213\n"
        "v_mfma_f32_16x16x32_bf16 v[132:135], a[40:43], a[236:239], v[132:135]\n"
        "s_waitcnt lgkmcnt(12)\n"
        "v_cvt_pk_bf16_f32 v211, v214, v215\n"
        "v_mul_f32 v176, v176, v246\n"
        "v_mfma_f32_16x16x32_bf16 v[132:135], a[44:47], a[232:235], v[132:135]\n"
        "global_store_dwordx4 v254, v[208:211], s[70:71] offset:-4096 sc1\n"
        "s_waitcnt lgkmcnt(9)\n"
        "v_mul_f32 v177, v177, v246\n"
        "v_mul_f32 v178, v178, v246\n"
        "v_mul_f32 v179, v179, v246\n"
        "v_mfma_f32_16x16x32_bf16 v[132:135], a[48:51], a[228:231], v[132:135]\n"
        "s_waitcnt lgkmcnt(6)\n"
        "v_mul_f32 v180, v180, v246\n"
        "v_mul_f32 v181, v181, v246\n"
        "v_mul_f32 v182, v182, v246\n"
        "v_mul_f32 v183, v183, v246\n"
        "v_cvt_pk_bf16_f32 v176, v176, v177\n"
        "v_cvt_pk_bf16_f32 v177, v178, v179\n"
        "v_cvt_pk_bf16_f32 v178, v180, v181\n"
        "v_cvt_pk_bf16_f32 v179, v182, v183\n"
        "global_store_dwordx4 v254, v[176:179], s[70:71] offset:0 sc1\n"
        "s_add_u32 s70, s70, 0x2000\n"
        "s_addc_u32 s71, s71, 0\n"
        "ds_read2_b32 a[12:13], v225 offset0:160 offset1:161\n"
        "v_mul_f32 v216, v216, v247\n"
        "v_mul_f32 v217, v217, v247\n"
        "v_mul_f32 v218, v218, v247\n"
        "v_mul_f32 v219, v219, v247\n"
        "v_mul_f32 v220, v220, v247\n"
        "v_mul_f32 v221, v221, v247\n"
        "v_mul_f32 v222, v222, v247\n"
        "v_mul_f32 v223, v223, v247\n"
        "v_cvt_pk_bf16_f32 v216, v216, v217\n"
        "v_cvt_pk_bf16_f32 v217, v218, v219\n"
        "v_cvt_pk_bf16_f32 v218, v220, v221\n"
        "v_cvt_pk_bf16_f32 v219, v222, v223\n"
        "global_store_dwordx4 v254, v[216:219], s[70:71] offset:-4096 sc1\n"
        "ds_read2_b32 a[14:15], v225 offset0:162 offset1:163\n"
        "ds_read_b128 a[204:207], v228 offset:2176\n"
        "v_mul_f32 v184, v184, v246\n"
        "v_mul_f32 v185, v185, v246\n"
        "v_mul_f32 v186, v186, v246\n"
        "v_mul_f32 v187, v187, v246\n"
        "v_mul_f32 v188, v188, v246\n"
        "v_mul_f32 v189, v189, v246\n"
        "v_mul_f32 v190, v190, v246\n"
        "v_mul_f32 v191, v191, v246\n"
        "v_cvt_pk_bf16_f32 v184, v184, v185\n"
        "v_cvt_pk_bf16_f32 v185, v186, v187\n"
        "v_cvt_pk_bf16_f32 v186, v188, v189\n"
        "v_cvt_pk_bf16_f32 v187, v190, v191\n"
        "global_store_dwordx4 v254, v[184:187], s[70:71] offset:0 sc1\n"
        "ds_read2_b32 a[16:17], v226 offset0:32 offset1:33\n"
        "ds_read2_b32 a[18:19], v226 offset0:34 offset1:35\n"
        "v_add_f32 v247, v248, v249\n"
        "v_cmp_nge_f32 vcc, 0x4e6e6b28, v247\n"
        "v_mfma_f32_16x16x32_bf16 v[140:143], a[32:35], a[252:255], v[140:143]\n"
        "s_cbranch_vccnz LRH_%=\n"
        "LSH_%=:\n"
        "v_add_u32 v231, 3648, v228\n"
        "v_min_u32 v231, s16, v231\n"
        "v_mfma_f32_16x16x32_bf16 v[140:143], a[36:39], a[248:251], v[140:143]\n"
        "ds_read_b128 a[224:227], v231 offset:0\n"
        "s_add_u32 s22, s18, s14\n"
        "s_addc_u32 s23, s19, 0\n"
        "v_mfma_f32_16x16x32_bf16 v[140:143], a[40:43], a[244:247], v[140:143]\n"
        "s_sub_u32 s22, s22, 4096\n"
        "s_subb_u32 s23, s23, 0\n"
        "s_lshl_b32 s14, s12, 20\n"
        "v_mfma_f32_16x16x32_bf16 v[140:143], a[44:47], a[240:243], v[140:143]\n"
        "s_add_u32 s14, s14, s15\n"
        "s_add_u32 s14, s14, s73\n"
        "s_add_u32 s14, s14, 0x2000000\n"
        "v_mfma_f32_16x16x32_bf16 v[140:143], a[48:51], a[236:239], v[140:143]\n"
        "s_add_u32 s20, s38, s14\n"
        "s_addc_u32 s21, s39, 0\n"
        "v_lshrrev_b32 v246, 7, v254\n"
        "v_mfma_f32_16x16x32_bf16 v[140:143], a[52:55], a[232:235], v[140:143]\n"
        "ds_read_b128 a[208:211], v228 offset:1664\n"
        "s_lshl_b32 s14, s10, 12\n"
        "v_bfe_u32 v247, v254, 4, 3\n"
        "v_mfma_f32_16x16x32_bf16 v[140:143], a[56:59], a[228:231], v[140:143]\n"
        "ds_read2_b32 a[20:21], v226 offset0:160 offset1:161\n"
        "ds_read2_b32 a[22:23], v226 offset0:162 offset1:163\n"
        "ds_read_b128 a[212:215], v228 offset:1152\n"
        "ds_read2_b32 a[24:25], v227 offset0:32 offset1:33\n"
        "ds_read2_b32 a[26:27], v227 offset0:34 offset1:35\n"
        "v_and_b32 v248, 6, v246\n"
        "s_waitcnt lgkmcnt(6)\n"
        "v_mfma_f32_16x16x32_bf16 v[96:99], a[32:35], a[224:227], v[96:99]\n"
        "ds_read_b128 a[216:219], v228 offset:640\n"
        "v_xor_b32 v248, v248, v247\n"
        "v_mfma_f32_16x16x32_bf16 v[100:103], a[36:39], a[224:227], v[100:103]\n"
        "ds_read2_b32 a[28:29], v227 offset0:160 offset1:161\n"
        "v_lshlrev_b32 v248, 4, v248\n"
        "v_mfma_f32_16x16x32_bf16 v[104:107], a[40:43], a[224:227], v[104:107]\n"
        "ds_read2_b32 a[30:31], v227 offset0:162 offset1:163\n"
        "v_lshl_add_u32 v249, v246, 11, v248\n"
        "v_mfma_f32_16x16x32_bf16 v[104:107], a[36:39], a[228:231], v[104:107]\n"
        "ds_read_b128 a[220:223], v228 offset:128\n"
        "v_xor_b32 v246, 4, v246\n"
        "v_mfma_f32_16x16x32_bf16 v[104:107], a[32:35], a[232:235], v[104:107]\n"
        "s_waitcnt vmcnt(8)\n"
        "v_lshl_add_u32 v246, v246, 11, v248\n"
        "v_add_u32 v144, 4096, v249\n"
        "v_mfma_f32_16x16x32_bf16 v[108:111], a[44:47], a[224:227], v[108:111]\n"
        "s_barrier\n"
        "v_add_u32 v145, 3200, v249\n"
        "v_add_u32 v146, 18432, v246\n"
        "v_mfma_f32_16x16x32_bf16 v[128:131], a[48:51], a[224:227], v[128:131]\n"
        "s_add_u32 s60, s22, 0x0\n"
        "s_addc_u32 s61, s23, 0\n"
        "v_add_u32 v147, 17536, v246\n"
        "v_mfma_f32_16x16x32_bf16 v[128:131], a[44:47], a[228:231], v[128:131]\n"
        "global_load_lds_dwordx4 v144, s[60:61] offset:0\n"
        "v_add_u32 v151, s14, v254\n"
        "v_mfma_f32_16x16x32_bf16 v[128:131], a[40:43], a[232:235], v[128:131]\n"
        "s_lshr_b32 s14, s40, 1\n"
        "v_xor_b32 v148, 0x1000, v151\n"
        "s_lshl_b32 s14, s14, 10\n"
        "v_mfma_f32_16x16x32_bf16 v[128:131], a[36:39], a[236:239], v[128:131]\n"
        "ds_read_b128 a[252:255], v228 offset:192\n"
        "v_lshrrev_b32 v246, 1, v233\n"
        "v_mfma_f32_16x16x32_bf16 v[128:131], a[32:35], a[240:243], v[128:131]\n"
        "global_load_lds_dwordx4 v145, s[60:61] offset:1024\n"
        "v_lshlrev_b32 v247, 11, v246\n"
        "v_mfma_f32_16x16x32_bf16 v[132:135], a[52:55], a[224:227], v[132:135]\n"
        "s_waitcnt lgkmcnt(1)\n"
        "v_add_u32 v247, s14, v247\n"
        "v_bfe_u32 v248, v254, 6, 3\n"
        "v_mfma_f32_16x16x32_bf16 v[136:139], a[56:59], a[224:227], v[136:139]\n"
        "s_and_b32 s14, s40, 1\n"
        "v_bfe_u32 v249, v254, 4, 2\n"
        "v_lshlrev_b32 v240, 2, v246\n"
        "v_mfma_f32_16x16x32_bf16 v[136:139], a[52:55], a[228:231], v[136:139]\n"
        "global_load_lds_dwordx4 v146, s[60:61] offset:2048\n"
        "s_lshl_b32 s14, s14, 6\n"
        "v_xor_b32 v240, v240, v248\n"
        "v_mfma_f32_16x16x32_bf16 v[136:139], a[48:51], a[232:235], v[136:139]\n"
        "ds_read_b128 a[228:231], v228 offset:3264\n"
        "v_bfe_u32 v241, v240, 1, 2\n"
        "v_mfma_f32_16x16x32_bf16 v[136:139], a[44:47], a[236:239], v[136:139]\n"
        "ds_read_b128 a[232:235], v228 offset:2752\n"
        "v_xor_b32 v241, 0, v241\n"
        "v_mfma_f32_16x16x32_bf16 v[136:139], a[40:43], a[240:243], v[136:139]\n"
        "global_load_lds_dwordx4 v147, s[60:61] offset:3072\n"
        "s_add_u32 m0, s73, 0x4000\n"
        "v_lshlrev_b32 v241, 5, v241\n"
        "v_mfma_f32_16x16x32_bf16 v[136:139], a[36:39], a[244:247], v[136:139]\n"
        "ds_read2_b32 a[36:37], v224 offset0:176 offset1:177\n"
        "v_lshl_add_u32 v240, v240, 7, v241\n"
        "v_mfma_f32_16x16x32_bf16 v[136:139], a[32:35], a[248:251], v[136:139]\n"
        "ds_read2_b32 a[32:33], v224 offset0:48 offset1:49\n"
        "v_lshl_add_u32 v240, v249, 3, v240\n"
        "v_mfma_f32_16x16x32_bf16 v[140:143], a[60:63], a[224:227], v[140:143]\n"
        "ds_read2_b32 a[34:35], v224 offset0:50 offset1:51\n"
        "v_add_u32 v154, v240, v247\n"
        "v_mfma_f32_16x16x32_bf16 v[100:103], a[0:3], a[196:199], v[100:103]\n"
        "ds_read2_b32 a[38:39], v224 offset0:178 offset1:179\n"
        "v_xor_b32 v154, s14, v154\n"
        "v_mfma_f32_16x16x32_bf16 v[108:111], a[0:3], a[204:207], v[108:111]\n"
        "ds_read2_b32 a[40:41], v225 offset0:48 offset1:49\n"
        "s_lshl_b32 s14, s44, 5\n"
        "v_xor_b32 v155, 32, v154\n"
        "v_mfma_f32_16x16x32_bf16 v[108:111], a[4:7], a[200:203], v[108:111]\n"
        "ds_read2_b32 a[42:43], v225 offset0:50 offset1:51\n"
        "s_add_u32 s14, s14, s43\n"
        "v_add_u32 v156, 0x10000, v154\n"
        "v_mfma_f32_16x16x32_bf16 v[108:111], a[8:11], a[196:199], v[108:111]\n"
        "ds_read2_b32 a[44:45], v225 offset0:176 offset1:177\n"
        "s_lshl2_add_u32 s14, s14, s40\n"
        "v_add_u32 v157, 0x10000, v155\n"
        "v_mfma_f32_16x16x32_bf16 v[132:135], a[0:3], a[212:215], v[132:135]\n"
        "ds_read2_b32 a[46:47], v225 offset0:178 offset1:179\n"
        "s_add_u32 s14, s14, 1024\n"
        "v_add_u32 v149, 0x10000, v148\n"
        "v_mfma_f32_16x16x32_bf16 v[132:135], a[4:7], a[208:211], v[132:135]\n"
        "ds_read_b128 a[236:239], v228 offset:2240\n"
        "s_lshl_b32 s14, s14, 7\n"
        "v_add_u32 v152, 0x10000, v151\n"
        "v_mfma_f32_16x16x32_bf16 v[132:135], a[8:11], a[204:207], v[132:135]\n"
        "ds_read2_b32 a[48:49], v226 offset0:48 offset1:49\n"
        "v_add_u32 v158, 0x20000, v154\n"
        "v_mfma_f32_16x16x32_bf16 v[132:135], a[12:15], a[200:203], v[132:135]\n"
        "ds_read2_b32 a[50:51], v226 offset0:50 offset1:51\n"
        "v_add_u32 v159, 0x20000, v155\n"
        "v_mfma_f32_16x16x32_bf16 v[132:135], a[16:19], a[196:199], v[132:135]\n"
        "ds_read_b128 a[240:243], v228 offset:1728\n"
        "v_add_u32 v150, 0x20000, v148\n"
        "v_add_u32 v153, 0x20000, v151\n"
        "v_mfma_f32_16x16x32_bf16 v[140:143], a[0:3], a[220:223], v[140:143]\n"
        "ds_read2_b32 a[52:53], v226 offset0:176 offset1:177\n"
        "v_add_u32 v230, 3712, v228\n"
        "v_mfma_f32_16x16x32_bf16 v[140:143], a[4:7], a[216:219], v[140:143]\n"
        "ds_read2_b32 a[54:55], v226 offset0:178 offset1:179\n"
        "v_min_u32 v230, s16, v230\n"
        "v_mfma_f32_16x16x32_bf16 v[140:143], a[8:11], a[212:215], v[140:143]\n"
        "ds_read_b128 a[192:195], v230 offset:0\n"
        "s_add_u32 s60, s22, 0x40000\n"
        "s_addc_u32 s61, s23, 0\n"
        "v_mfma_f32_16x16x32_bf16 v[140:143], a[12:15], a[208:211], v[140:143]\n"
        "global_load_lds_dwordx4 v144, s[60:61] offset:0\n"
        "s_waitcnt vmcnt(5)\n"
        "v_mfma_f32_16x16x32_bf16 v[140:143], a[16:19], a[204:207], v[140:143]\n"
        "ds_read_b128 a[244:247], v228 offset:1216\n"
        "v_lshrrev_b32 v234, 1, v254\n"
        "v_mfma_f32_16x16x32_bf16 v[140:143], a[20:23], a[200:203], v[140:143]\n"
        "ds_read2_b32 a[56:57], v227 offset0:48 offset1:49\n"
        "v_add_u32 v234, s14, v234\n"
        "v_mfma_f32_16x16x32_bf16 v[140:143], a[24:27], a[196:199], v[140:143]\n"
        "global_load_lds_dwordx4 v145, s[60:61] offset:1024\n"
        "global_load_lds_dwordx4 v146, s[60:61] offset:2048\n"
        "s_lshl_b32 s14, s45, 5\n"
        "s_add_u32 s14, s14, s43\n"
        "s_waitcnt lgkmcnt(2)\n"
        "v_mfma_f32_16x16x32_bf16 v[96:99], a[0:3], a[192:195], v[96:99]\n"
        "s_lshl2_add_u32 s14, s14, s40\n"
        "s_add_u32 s14, s14, 1024\n"
        "s_lshl_b32 s14, s14, 7\n"
        "v_mfma_f32_16x16x32_bf16 v[100:103], a[4:7], a[192:195], v[100:103]\n"
        "ds_read2_b32 a[58:59], v227 offset0:50 offset1:51\n"
        "v_add_u32 v231, 3776, v228\n"
        "v_mfma_f32_16x16x32_bf16 v[104:107], a[8:11], a[192:195], v[104:107]\n"
        "global_load_lds_dwordx4 v147, s[60:61] offset:3072\n"
        "s_add_u32 m0, s73, 0x8000\n"
        "v_min_u32 v231, s16, v231\n"
        "v_mfma_f32_16x16x32_bf16 v[104:107], a[4:7], a[196:199], v[104:107]\n"
        "s_add_u32 s60, s22, 0xa0000\n"
        "s_addc_u32 s61, s23, 0\n"
        "v_add_u32 v230, 3840, v228\n"
        "v_mfma_f32_16x16x32_bf16 v[104:107], a[0:3], a[200:203], v[104:107]\n"
        "ds_read_b128 a[224:227], v231 offset:0\n"
        "v_min_u32 v230, s16, v230\n"
        "v_mfma_f32_16x16x32_bf16 v[108:111], a[12:15], a[192:195], v[108:111]\n"
        "s_mov_b64 exec, 0xff\n"
        "global_store_dwordx2 v234, v[250:251], s[48:49] sc1\n"
        "s_mov_b64 exec, -1\n"
        "v_mfma_f32_16x16x32_bf16 v[128:131], a[16:19], a[192:195], v[128:131]\n"
        "global_load_lds_dwordx4 v144, s[60:61] offset:0\n"
        "v_lshrrev_b32 v234, 1, v254\n"
        "v_mfma_f32_16x16x32_bf16 v[128:131], a[12:15], a[196:199], v[128:131]\n"
        "ds_read_b128 a[248:251], v228 offset:704\n"
        "v_add_u32 v234, s14, v234\n"
        "v_mfma_f32_16x16x32_bf16 v[128:131], a[8:11], a[200:203], v[128:131]\n"
        "ds_read2_b32 a[60:61], v227 offset0:176 offset1:177\n"
        "s_lshl_b32 s14, s62, 11\n"
        "s_lshl1_add_u32 s14, s63, s14\n"
        "v_mfma_f32_16x16x32_bf16 v[128:131], a[4:7], a[204:207], v[128:131]\n"
        "s_mov_b64 exec, 0xff\n"
        "global_store_dwordx2 v234, v[250:251], s[48:49] sc1\n"
        "s_mov_b64 exec, -1\n"
        "v_mfma_f32_16x16x32_bf16 v[128:131], a[0:3], a[208:211], v[128:131]\n"
        "global_load_lds_dwordx4 v145, s[60:61] offset:1024\n"
        "ds_read2_b32 a[62:63], v227 offset0:178 offset1:179\n"
        "v_mfma_f32_16x16x32_bf16 v[132:135], a[20:23], a[192:195], v[132:135]\n"
        "s_add_u32 s64, s8, s14\n"
        "s_addc_u32 s65, s9, 0\n"
        "v_add_u32 v231, 3904, v228\n"
        "v_mfma_f32_16x16x32_bf16 v[136:139], a[24:27], a[192:195], v[136:139]\n"
        "s_add_u32 s56, s36, s14\n"
        "s_addc_u32 s57, s37, 0\n"
        "v_min_u32 v231, s16, v231\n"
        "v_mfma_f32_16x16x32_bf16 v[136:139], a[20:23], a[196:199], v[136:139]\n"
        "global_load_lds_dwordx4 v146, s[60:61] offset:2048\n"
        "ds_read_b128 a[196:199], v228 offset:3328\n"
        "s_lshl1_add_u32 s14, s12, s10\n"
        "v_mfma_f32_16x16x32_bf16 v[136:139], a[16:19], a[200:203], v[136:139]\n"
        "ds_read_b128 a[200:203], v228 offset:2816\n"
        "s_lshl_b32 s14, s14, 6\n"
        "s_add_u32 s14, s14, 1024\n"
        "v_mfma_f32_16x16x32_bf16 v[136:139], a[12:15], a[204:207], v[136:139]\n"
        "ds_read2_b32 a[12:13], v225 offset0:192 offset1:193\n"
        "s_lshl_b32 s14, s14, 7\n"
        "v_lshlrev_b32 v238, 3, v254\n"
        "v_mfma_f32_16x16x32_bf16 v[136:139], a[8:11], a[208:211], v[136:139]\n"
        "global_load_lds_dwordx4 v147, s[60:61] offset:3072\n"
        "s_add_u32 m0, s73, 0xc000\n"
        "v_add_u32 v238, s14, v238\n"
        "v_mfma_f32_16x16x32_bf16 v[136:139], a[4:7], a[212:215], v[136:139]\n"
        "s_add_u32 s60, s22, 0x100000\n"
        "s_addc_u32 s61, s23, 0\n"
        "v_lshlrev_b32 v229, 5, v232\n"
        "v_mfma_f32_16x16x32_bf16 v[136:139], a[0:3], a[216:219], v[136:139]\n"
        "ds_read2_b32 a[0:1], v224 offset0:64 offset1:65\n"
        "s_add_u32 s14, s51, 504\n"
        "v_lshl_add_u32 v229, v233, 3, v229\n"
        "v_mfma_f32_16x16x32_bf16 v[140:143], a[28:31], a[192:195], v[140:143]\n"
        "global_load_lds_dwordx4 v144, s[60:61] offset:0\n"
        "s_waitcnt lgkmcnt(4)\n"
        "v_mfma_f32_16x16x32_bf16 v[96:99], a[32:35], a[224:227], v[96:99]\n"
        "s_waitcnt vmcnt(7)\n"
        "s_barrier\n"
        "v_sub_u32 v229, s14, v229\n"
        "v_mfma_f32_16x16x32_bf16 v[100:103], a[32:35], a[228:231], v[100:103]\n"
        "ds_read_b64_tr_b16 v[160:161], v154 offset:0\n"
        "s_add_u32 s24, s94, 0x400000\n"
        "s_addc_u32 s25, s95, 0\n"
        "v_mfma_f32_16x16x32_bf16 v[100:103], a[36:39], a[224:227], v[100:103]\n"
        "global_load_lds_dwordx4 v145, s[60:61] offset:1024\n"
        "ds_read_b64_tr_b16 v[162:163], v154 offset:4096\n"
        "s_lshl_b32 s14, s41, 5\n"
        "v_mfma_f32_16x16x32_bf16 v[104:107], a[40:43], a[224:227], v[104:107]\n"
        "ds_read2_b32 a[2:3], v224 offset0:66 offset1:67\n"
        "s_add_u32 s14, s14, s42\n"
        "s_add_u32 s14, s14, 512\n"
        "v_mfma_f32_16x16x32_bf16 v[104:107], a[36:39], a[228:231], v[104:107]\n"
        "ds_read_b128 a[192:195], v230 offset:0\n"
        "s_lshl_b32 s14, s14, 7\n"
        "v_add_u32 v230, 3968, v228\n"
        "v_mfma_f32_16x16x32_bf16 v[104:107], a[32:35], a[232:235], v[104:107]\n"
        "global_load_lds_dwordx4 v146, s[60:61] offset:2048\n"
        "ds_read_b64_tr_b16 v[164:165], v155 offset:0\n"
        "v_mfma_f32_16x16x32_bf16 v[108:111], a[32:35], a[236:239], v[108:111]\n"
        "ds_read_b64_tr_b16 v[166:167], v155 offset:4096\n"
        "v_min_u32 v230, s16, v230\n"
        "v_mfma_f32_16x16x32_bf16 v[108:111], a[36:39], a[232:235], v[108:111]\n"
        "ds_read2_b32 a[4:5], v224 offset0:192 offset1:193\n"
        "v_lshrrev_b32 v234, 1, v254\n"
        "v_mfma_f32_16x16x32_bf16 v[108:111], a[40:43], a[228:231], v[108:111]\n"
        "global_load_lds_dwordx4 v147, s[60:61] offset:3072\n"
        "s_add_u32 m0, s73, 0x10000\n"
        "v_add_u32 v234, s14, v234\n"
        "v_mfma_f32_16x16x32_bf16 v[108:111], a[44:47], a[224:227], v[108:111]\n"
        "s_add_u32 s60, s22, 0x140000\n"
        "s_addc_u32 s61, s23, 0\n"
        "s_lshr_b32 s14, s63, 2\n"
        "v_mfma_f32_16x16x32_bf16 v[128:131], a[48:51], a[224:227], v[128:131]\n"
        "ds_read2_b32 a[6:7], v224 offset0:194 offset1:195\n"
        "s_add_u32 s14, s14, 512\n"
        "s_lshl_b32 s14, s14, 7\n"
        "v_mfma_f32_16x16x32_bf16 v[128:131], a[44:47], a[228:231], v[128:131]\n"
        "global_load_lds_dwordx4 v144, s[60:61] offset:0\n"
        "ds_read_b64_tr_b16 v[168:169], v154 offset:8192\n"
        "v_mfma_f32_16x16x32_bf16 v[128:131], a[40:43], a[232:235], v[128:131]\n"
        "ds_read_b64_tr_b16 v[170:171], v154 offset:12288\n"
        "v_and_b32 v246, 1, v232\n"
        "v_mfma_f32_16x16x32_bf16 v[128:131], a[36:39], a[236:239], v[128:131]\n"
        "ds_read2_b32 a[8:9], v225 offset0:64 offset1:65\n"
        "v_lshlrev_b32 v246, 4, v246\n"
        "v_mfma_f32_16x16x32_bf16 v[128:131], a[32:35], a[240:243], v[128:131]\n"
        "global_load_lds_dwordx4 v145, s[60:61] offset:1024\n"
        "ds_read2_b32 a[10:11], v225 offset0:66 offset1:67\n"
        "v_mfma_f32_16x16x32_bf16 v[132:135], a[32:35], a[244:247], v[132:135]\n"
        "ds_read_b64_tr_b16 v[172:173], v155 offset:8192\n"
        "v_mov_b32 v247, 0x3f80\n"
        "v_mfma_f32_16x16x32_bf16 v[132:135], a[36:39], a[240:243], v[132:135]\n"
        "ds_read_b64_tr_b16 v[174:175], v155 offset:12288\n"
        "v_lshlrev_b32 v246, v246, v247\n"
        "v_mfma_f32_16x16x32_bf16 v[132:135], a[40:43], a[236:239], v[132:135]\n"
        "global_load_lds_dwordx4 v146, s[60:61] offset:2048\n"
        "ds_read2_b32 a[14:15], v225 offset0:194 offset1:195\n"
        "v_mfma_f32_16x16x32_bf16 v[132:135], a[44:47], a[232:235], v[132:135]\n"
        "ds_read_b128 a[204:207], v228 offset:2304\n"
        "v_lshrrev_b32 v247, 1, v232\n"
        "v_mfma_f32_16x16x32_bf16 v[132:135], a[48:51], a[228:231], v[132:135]\n"
        "ds_read_b64_tr_b16 v[176:177], v154 offset:16384\n"
        "v_lshlrev_b32 v248, 1, v233\n"
        "v_mfma_f32_16x16x32_bf16 v[132:135], a[52:55], a[224:227], v[132:135]\n"
        "global_load_lds_dwordx4 v147, s[60:61] offset:3072\n"
        "s_add_u32 m0, s73, 0x14000\n"
        "v_or_b32 v249, 1, v248\n"
        "v_mfma_f32_16x16x32_bf16 v[136:139], a[56:59], a[224:227], v[136:139]\n"
        "s_add_u32 s60, s22, 0x1a0000\n"
        "s_addc_u32 s61, s23, 0\n"
        "s_waitcnt lgkmcnt(6)\n"
        "v_mfma_f32_16x16x32_bf16 v[136:139], a[52:55], a[228:231], v[136:139]\n"
        "ds_read_b64_tr_b16 v[178:179], v154 offset:20480\n"
        "s_waitcnt lgkmcnt(6)\n"
        "v_mfma_f32_16x16x32_bf16 v[136:139], a[48:51], a[232:235], v[136:139]\n"
        "global_load_lds_dwordx4 v144, s[60:61] offset:0\n"
        "ds_read2_b32 a[16:17], v226 offset0:64 offset1:65\n"
        "v_mfma_f32_16x16x32_bf16 v[136:139], a[44:47], a[236:239], v[136:139]\n"
        "ds_read2_b32 a[18:19], v226 offset0:66 offset1:67\n"
        "v_mfma_f32_16x16x32_bf16 v[136:139], a[40:43], a[240:243], v[136:139]\n"
        "ds_read_b128 a[208:211], v228 offset:1792\n"
        "s_waitcnt lgkmcnt(6)\n"
        "v_mfma_f32_16x16x32_bf16 v[136:139], a[36:39], a[244:247], v[136:139]\n"
        "global_load_lds_dwordx4 v145, s[60:61] offset:1024\n"
        "ds_read_b64_tr_b16 v[180:181], v155 offset:16384\n"
        "v_mfma_f32_16x16x32_bf16 v[136:139], a[32:35], a[248:251], v[136:139]\n"
        "ds_read_b64_tr_b16 v[182:183], v155 offset:20480\n"
        "s_waitcnt lgkmcnt(6)\n"
        "v_mfma_f32_16x16x32_bf16 v[140:143], a[32:35], a[252:255], v[140:143]\n"
        "ds_read2_b32 a[20:21], v226 offset0:192 offset1:193\n"
        "v_mfma_f32_16x16x32_bf16 v[140:143], a[36:39], a[248:251], v[140:143]\n"
        "global_load_lds_dwordx4 v146, s[60:61] offset:2048\n"
        "ds_read2_b32 a[22:23], v226 offset0:194 offset1:195\n"
        "v_mfma_f32_16x16x32_bf16 v[140:143], a[40:43], a[244:247], v[140:143]\n"
        "ds_read_b128 a[212:215], v228 offset:1280\n"
        "v_mfma_f32_16x16x32_bf16 v[140:143], a[44:47], a[240:243], v[140:143]\n"
        "ds_read_b64_tr_b16 v[184:185], v154 offset:24576\n"
        "s_waitcnt lgkmcnt(8)\n"
        "v_mfma_f32_16x16x32_bf16 v[140:143], a[48:51], a[236:239], v[140:143]\n"
        "global_load_lds_dwordx4 v147, s[60:61] offset:3072\n"
        "ds_read_b64_tr_b16 v[186:187], v154 offset:28672\n"
        "s_add_u32 m0, s73, 0x0\n"
        "v_mfma_f32_16x16x32_bf16 v[140:143], a[52:55], a[232:235], v[140:143]\n"
        "ds_read_b64_tr_b16 v[188:189], v155 offset:24576\n"
        "s_add_u32 s60, s22, 0x200000\n"
        "s_addc_u32 s61, s23, 0\n"
        "v_mfma_f32_16x16x32_bf16 v[140:143], a[56:59], a[228:231], v[140:143]\n"
        "ds_read_b64_tr_b16 v[190:191], v155 offset:28672\n"
        "s_waitcnt lgkmcnt(8)\n"
        "v_mfma_f32_16x16x32_bf16 v[140:143], a[60:63], a[224:227], v[140:143]\n"
        "ds_read2_b32 a[24:25], v227 offset0:64 offset1:65\n"
        "v_mfma_f32_16x16x32_bf16 v[96:99], a[0:3], a[192:195], v[96:99]\n"
        "ds_read2_b32 a[26:27], v227 offset0:66 offset1:67\n"
        "v_mfma_f32_16x16x32_bf16 v[100:103], a[0:3], a[196:199], v[100:103]\n"
        "ds_read_b128 a[216:219], v228 offset:768\n"
        "s_waitcnt lgkmcnt(8)\n"
        "v_mfma_f32_16x16x32_bf16 v[100:103], a[4:7], a[192:195], v[100:103]\n"
        "ds_read2_b32 a[28:29], v227 offset0:192 offset1:193\n"
        "s_waitcnt lgkmcnt(8)\n"
        "v_mfma_f32_16x16x32_bf16 v[104:107], a[8:11], a[192:195], v[104:107]\n"
        "ds_read2_b32 a[30:31], v227 offset0:194 offset1:195\n"
        "v_mfma_f32_16x16x32_bf16 v[104:107], a[4:7], a[196:199], v[104:107]\n"
        "ds_read_b128 a[220:223], v228 offset:256\n"
        "v_mfma_f32_16x16x32_bf16 v[104:107], a[0:3], a[200:203], v[104:107]\n"
        "ds_read2_b32 a[32:33], v224 offset0:80 offset1:81\n"
        "v_mfma_f32_16x16x32_bf16 v[108:111], a[0:3], a[204:207], v[108:111]\n"
        "s_waitcnt vmcnt(8) lgkmcnt(7)\n"
        "s_barrier\n"
        "v_mfma_f32_16x16x32_bf16 v[108:111], a[4:7], a[200:203], v[108:111]\n"
        "global_load_lds_dwordx4 v144, s[60:61] offset:0\n"
        "ds_read_b64_tr_b16 v[192:193], v154 offset:32768\n"
        "v_mfma_f32_16x16x32_bf16 v[108:111], a[8:11], a[196:199], v[108:111]\n"
        "ds_read_b64_tr_b16 v[194:195], v154 offset:36864\n"
        "s_waitcnt lgkmcnt(7)\n"
        "v_mfma_f32_16x16x32_bf16 v[108:111], a[12:15], a[192:195], v[108:111]\n"
        "ds_read2_b32 a[34:35], v224 offset0:82 offset1:83\n"
        "v_mfma_f32_16x16x32_bf16 v[128:131], a[16:19], a[192:195], v[128:131]\n"
        "global_load_lds_dwordx4 v145, s[60:61] offset:1024\n"
        "ds_read_b128 a[224:227], v231 offset:0\n"
        "v_mfma_f32_16x16x32_bf16 v[128:131], a[12:15], a[196:199], v[128:131]\n"
        "ds_read_b64_tr_b16 v[196:197], v155 offset:32768\n"
        "v_add_u32 v231, 4032, v228\n"
        "v_mfma_f32_16x16x32_bf16 v[128:131], a[8:11], a[200:203], v[128:131]\n"
        "ds_read_b64_tr_b16 v[198:199], v155 offset:36864\n"
        "v_min_u32 v231, s16, v231\n"
        "v_mfma_f32_16x16x32_bf16 v[128:131], a[4:7], a[204:207], v[128:131]\n"
        "global_load_lds_dwordx4 v146, s[60:61] offset:2048\n"
        "ds_read2_b32 a[36:37], v224 offset0:208 offset1:209\n"
        "s_lshl_b32 s16, s63, 12\n"
        "v_mfma_f32_16x16x32_bf16 v[128:131], a[0:3], a[208:211], v[128:131]\n"
        "ds_read2_b32 a[38:39], v224 offset0:210 offset1:211\n"
        "s_lshl1_add_u32 s16, s62, s16\n"
        "s_add_u32 s16, s16, 0x3400000\n"
        "v_mfma_f32_16x16x32_bf16 v[132:135], a[0:3], a[212:215], v[132:135]\n"
        "ds_read_b128 a[228:231], v228 offset:3392\n"
        "s_add_u32 s58, s38, s16\n"
        "s_addc_u32 s59, s39, 0\n"
        "v_mfma_f32_16x16x32_bf16 v[132:135], a[4:7], a[208:211], v[132:135]\n"
        "global_load_lds_dwordx4 v147, s[60:61] offset:3072\n"
        "ds_read_b64_tr_b16 v[200:201], v154 offset:40960\n"
        "s_add_u32 m0, s73, 0x4000\n"
        "v_mfma_f32_16x16x32_bf16 v[132:135], a[8:11], a[204:207], v[132:135]\n"
        "ds_read_b64_tr_b16 v[202:203], v154 offset:45056\n"
        "s_add_u32 s60, s22, 0x240000\n"
        "s_addc_u32 s61, s23, 0\n"
        "v_mfma_f32_16x16x32_bf16 v[132:135], a[12:15], a[200:203], v[132:135]\n"
        "ds_read2_b32 a[40:41], v225 offset0:80 offset1:81\n"
        "s_lshl_b32 s16, s13, 14\n"
        "s_add_u32 s16, s16, s73\n"
        "v_mfma_f32_16x16x32_bf16 v[132:135], a[16:19], a[196:199], v[132:135]\n"
        "ds_read2_b32 a[42:43], v225 offset0:82 offset1:83\n"
        "global_load_lds_dwordx4 v144, s[60:61] offset:0\n"
        "s_add_u32 s16, s16, 0x4000000\n"
        "v_mfma_f32_16x16x32_bf16 v[132:135], a[20:23], a[192:195], v[132:135]\n"
        "ds_read_b128 a[232:235], v228 offset:2880\n"
        "s_add_u32 s28, s38, s16\n"
        "s_addc_u32 s29, s39, 0\n"
        "v_mfma_f32_16x16x32_bf16 v[136:139], a[24:27], a[192:195], v[136:139]\n"
        "ds_read_b64_tr_b16 v[204:205], v155 offset:40960\n"
        "s_xor_b32 s16, s16, 0x4000\n"
        "s_waitcnt lgkmcnt(8)\n"
        "v_mfma_f32_16x16x32_bf16 v[136:139], a[20:23], a[196:199], v[136:139]\n"
        "ds_read_b64_tr_b16 v[206:207], v155 offset:45056\n"
        "global_load_lds_dwordx4 v145, s[60:61] offset:1024\n"
        "v_mfma_f32_16x16x32_bf16 v[136:139], a[16:19], a[200:203], v[136:139]\n"
        "ds_read2_b32 a[44:45], v225 offset0:208 offset1:209\n"
        "s_add_u32 s30, s38, s16\n"
        "s_addc_u32 s31, s39, 0\n"
        "v_mfma_f32_16x16x32_bf16 v[136:139], a[12:15], a[204:207], v[136:139]\n"
        "ds_read2_b32 a[46:47], v225 offset0:210 offset1:211\n"
        "s_add_u32 s16, s73, 0x18000\n"
        "s_waitcnt lgkmcnt(8)\n"
        "v_mfma_f32_16x16x32_bf16 v[136:139], a[8:11], a[208:211], v[136:139]\n"
        "ds_read_b128 a[236:239], v228 offset:2368\n"
        "global_load_lds_dwordx4 v146, s[60:61] offset:2048\n"
        "v_mfma_f32_16x16x32_bf16 v[136:139], a[4:7], a[212:215], v[136:139]\n"
        "ds_read_b64_tr_b16 v[208:209], v154 offset:49152\n"
        "s_waitcnt lgkmcnt(8)\n"
        "v_mfma_f32_16x16x32_bf16 v[136:139], a[0:3], a[216:219], v[136:139]\n"
        "ds_read_b64_tr_b16 v[210:211], v154 offset:53248\n"
        "s_waitcnt lgkmcnt(8)\n"
        "v_mfma_f32_16x16x32_bf16 v[140:143], a[0:3], a[220:223], v[140:143]\n"
        "ds_read2_b32 a[48:49], v226 offset0:80 offset1:81\n"
        "global_load_lds_dwordx4 v147, s[60:61] offset:3072\n"
        "s_add_u32 m0, s73, 0x8000\n"
        "v_mfma_f32_16x16x32_bf16 v[140:143], a[4:7], a[216:219], v[140:143]\n"
        "ds_read2_b32 a[50:51], v226 offset0:82 offset1:83\n"
        "s_add_u32 s60, s22, 0x2a0000\n"
        "s_addc_u32 s61, s23, 0\n"
        "v_mfma_f32_16x16x32_bf16 v[140:143], a[8:11], a[212:215], v[140:143]\n"
        "ds_read_b128 a[240:243], v228 offset:1856\n"
        "v_mfma_f32_16x16x32_bf16 v[140:143], a[12:15], a[208:211], v[140:143]\n"
        "ds_read_b64_tr_b16 v[212:213], v155 offset:49152\n"
        "v_mfma_f32_16x16x32_bf16 v[140:143], a[16:19], a[204:207], v[140:143]\n"
        "ds_read_b64_tr_b16 v[214:215], v155 offset:53248\n"
        "s_waitcnt lgkmcnt(8)\n"
        "v_mfma_f32_16x16x32_bf16 v[140:143], a[20:23], a[200:203], v[140:143]\n"
        "ds_read2_b32 a[52:53], v226 offset0:208 offset1:209\n"
        "s_waitcnt lgkmcnt(8)\n"
        "v_mfma_f32_16x16x32_bf16 v[140:143], a[24:27], a[196:199], v[140:143]\n"
        "ds_read2_b32 a[54:55], v226 offset0:210 offset1:211\n"
        "v_mfma_f32_16x16x32_bf16 v[140:143], a[28:31], a[192:195], v[140:143]\n"
        "ds_read_b128 a[244:247], v228 offset:1344\n"
        "v_mfma_f32_16x16x32_bf16 v[96:99], a[32:35], a[224:227], v[96:99]\n"
        "ds_read_b64_tr_b16 v[216:217], v154 offset:57344\n"
        "s_waitcnt lgkmcnt(8)\n"
        "v_mfma_f32_16x16x32_bf16 v[100:103], a[32:35], a[228:231], v[100:103]\n"
        "ds_read_b64_tr_b16 v[218:219], v154 offset:61440\n"
        "s_waitcnt lgkmcnt(8)\n"
        "v_mfma_f32_16x16x32_bf16 v[100:103], a[36:39], a[224:227], v[100:103]\n"
        "ds_read_b64_tr_b16 v[220:221], v155 offset:57344\n"
        "v_mfma_f32_16x16x32_bf16 v[104:107], a[40:43], a[224:227], v[104:107]\n"
        "ds_read_b64_tr_b16 v[222:223], v155 offset:61440\n"
        "v_mfma_f32_16x16x32_bf16 v[104:107], a[36:39], a[228:231], v[104:107]\n"
        "ds_read2_b32 a[56:57], v227 offset0:80 offset1:81\n"
        "v_mfma_f32_16x16x32_bf16 v[104:107], a[32:35], a[232:235], v[104:107]\n"
        "ds_read2_b32 a[58:59], v227 offset0:82 offset1:83\n"
        "v_mfma_f32_16x16x32_bf16 v[108:111], a[32:35], a[236:239], v[108:111]\n"
        "ds_read_b128 a[248:251], v228 offset:832\n"
        "s_waitcnt lgkmcnt(8)\n"
        "v_mfma_f32_16x16x32_bf16 v[108:111], a[36:39], a[232:235], v[108:111]\n"
        "ds_read2_b32 a[60:61], v227 offset0:208 offset1:209\n"
        "s_waitcnt lgkmcnt(8)\n"
        "v_mfma_f32_16x16x32_bf16 v[108:111], a[40:43], a[228:231], v[108:111]\n"
        "ds_read2_b32 a[62:63], v227 offset0:210 offset1:211\n"
        "v_mfma_f32_16x16x32_bf16 v[108:111], a[44:47], a[224:227], v[108:111]\n"
        "ds_read_b128 a[252:255], v228 offset:320\n"
        "v_mfma_f32_16x16x32_bf16 v[128:131], a[48:51], a[224:227], v[128:131]\n"
        "ds_read2_b32 a[0:1], v224 offset0:96 offset1:97\n"
        "v_mfma_f32_16x16x32_bf16 v[128:131], a[44:47], a[228:231], v[128:131]\n"
        "s_waitcnt vmcnt(8) lgkmcnt(7)\n"
        "s_barrier\n"
        "v_mfma_f32_16x16x32_bf16 v[128:131], a[40:43], a[232:235], v[128:131]\n"
        "global_load_lds_dwordx4 v144, s[60:61] offset:0\n"
        "ds_read_b64_tr_b16 a[64:65], v156 offset:0\n"
        "v_mfma_f32_16x16x32_bf16 v[128:131], a[36:39], a[236:239], v[128:131]\n"
        "ds_read_b64_tr_b16 a[66:67], v156 offset:4096\n"
        "s_waitcnt lgkmcnt(7)\n"
        "v_mfma_f32_16x16x32_bf16 v[128:131], a[32:35], a[240:243], v[128:131]\n"
        "ds_read2_b32 a[2:3], v224 offset0:98 offset1:99\n"
        "v_mfma_f32_16x16x32_bf16 v[132:135], a[32:35], a[244:247], v[132:135]\n"
        "global_load_lds_dwordx4 v145, s[60:61] offset:1024\n"
        "ds_read_b128 a[192:195], v230 offset:0\n"
        "v_mfma_f32_16x16x32_bf16 v[132:135], a[36:39], a[240:243], v[132:135]\n"
        "ds_read_b64_tr_b16 a[68:69], v157 offset:0\n"
        "v_lshrrev_b32 v230, 1, v233\n"
        "v_mfma_f32_16x16x32_bf16 v[132:135], a[40:43], a[236:239], v[132:135]\n"
        "ds_read_b64_tr_b16 a[70:71], v157 offset:4096\n"
        "s_waitcnt lgkmcnt(7)\n"
        "v_mfma_f32_16x16x32_bf16 v[140:143], a[32:35], a[252:255], v[140:143]\n"
        "global_load_lds_dwordx4 v146, s[60:61] offset:2048\n"
        "ds_read2_b32 a[4:5], v224 offset0:224 offset1:225\n"
        "v_mfma_f32_16x16x32_bf16 v[140:143], a[36:39], a[248:251], v[140:143]\n"
        "ds_read2_b32 a[6:7], v224 offset0:226 offset1:227\n"
        "v_mfma_f32_16x16x32_bf16 v[140:143], a[40:43], a[244:247], v[140:143]\n"
        "ds_read_b128 a[196:199], v228 offset:3456\n"
        "LHB1_%=:\n"
        "s_cbranch_execz LHB5_%=\n"
        "v_mfma_f32_16x16x32_bf16 v[140:143], a[44:47], a[240:243], v[140:143]\n"
        "global_load_lds_dwordx4 v147, s[60:61] offset:3072\n"
        "ds_read_b64_tr_b16 a[72:73], v156 offset:8192\n"
        "s_add_u32 m0, s73, 0xc000\n"
        "v_mfma_f32_16x16x32_bf16 v[140:143], a[48:51], a[236:239], v[140:143]\n"
        "ds_read_b64_tr_b16 a[74:75], v156 offset:12288\n"
        "s_add_u32 s60, s22, 0x300000\n"
        "s_addc_u32 s61, s23, 0\n"
        "v_mfma_f32_16x16x32_bf16 v[140:143], a[52:55], a[232:235], v[140:143]\n"
        "ds_read2_b32 a[8:9], v225 offset0:96 offset1:97\n"
        "s_waitcnt lgkmcnt(8)\n"
        "v_mfma_f32_16x16x32_bf16 v[140:143], a[56:59], a[228:231], v[140:143]\n"
        "ds_read2_b32 a[10:11], v225 offset0:98 offset1:99\n"
        "global_load_lds_dwordx4 v144, s[60:61] offset:0\n"
        "v_mfma_f32_16x16x32_bf16 v[140:143], a[60:63], a[224:227], v[140:143]\n"
        "ds_read_b128 a[200:203], v228 offset:2944\n"
        "v_mfma_f32_16x16x32_bf16 v[132:135], a[44:47], a[232:235], v[132:135]\n"
        "ds_read_b64_tr_b16 a[76:77], v157 offset:8192\n"
        "v_mfma_f32_16x16x32_bf16 v[132:135], a[48:51], a[228:231], v[132:135]\n"
        "global_load_dwordx2 v[236:237], v238, s[48:49] sc1\n"
        "v_mfma_f32_16x16x32_bf16 v[132:135], a[52:55], a[224:227], v[132:135]\n"
        "ds_read_b64_tr_b16 a[78:79], v157 offset:12288\n"
        "global_load_lds_dwordx4 v145, s[60:61] offset:1024\n"
        "v_mfma_f32_16x16x32_bf16 v[136:139], a[56:59], a[224:227], v[136:139]\n"
        "ds_read2_b32 a[12:13], v225 offset0:224 offset1:225\n"
        "s_waitcnt lgkmcnt(7)\n"
        "v_mfma_f32_16x16x32_bf16 v[136:139], a[52:55], a[228:231], v[136:139]\n"
        "ds_read2_b32 a[14:15], v225 offset0:226 offset1:227\n"
        "v_mfma_f32_16x16x32_bf16 v[136:139], a[48:51], a[232:235], v[136:139]\n"
        "ds_read_b128 a[204:207], v228 offset:2432\n"
        "global_load_lds_dwordx4 v146, s[60:61] offset:2048\n"
        "v_mfma_f32_16x16x32_bf16 v[136:139], a[44:47], a[236:239], v[136:139]\n"
        "ds_read_b64_tr_b16 a[80:81], v156 offset:16384\n"
        "s_waitcnt lgkmcnt(7)\n"
        "v_mfma_f32_16x16x32_bf16 v[136:139], a[40:43], a[240:243], v[136:139]\n"
        "ds_read_b64_tr_b16 a[82:83], v156 offset:20480\n"
        "v_mfma_f32_16x16x32_bf16 v[136:139], a[36:39], a[244:247], v[136:139]\n"
        "ds_read2_b32 a[16:17], v226 offset0:96 offset1:97\n"
        "global_load_lds_dwordx4 v147, s[60:61] offset:3072\n"
        "s_add_u32 m0, s73, 0x10000\n"
        "v_mfma_f32_16x16x32_bf16 v[136:139], a[32:35], a[248:251], v[136:139]\n"
        "ds_read2_b32 a[18:19], v226 offset0:98 offset1:99\n"
        "s_add_u32 s60, s22, 0x340000\n"
        "s_addc_u32 s61, s23, 0\n"
        "v_mfma_f32_16x16x32_bf16 v[96:99], a[0:3], a[192:195], v[96:99]\n"
        "ds_read_b128 a[208:211], v228 offset:1920\n"
        "v_mfma_f32_16x16x32_bf16 v[100:103], a[0:3], a[196:199], v[100:103]\n"
        "ds_read_b64_tr_b16 a[84:85], v157 offset:16384\n"
        "v_mfma_f32_16x16x32_bf16 v[100:103], a[4:7], a[192:195], v[100:103]\n"
        "ds_read_b64_tr_b16 a[86:87], v157 offset:20480\n"
        "s_waitcnt lgkmcnt(8)\n"
        "v_mfma_f32_16x16x32_bf16 v[104:107], a[8:11], a[192:195], v[104:107]\n"
        "ds_read_b64_tr_b16 a[88:89], v156 offset:24576\n"
        "s_waitcnt lgkmcnt(8)\n"
        "v_mfma_f32_16x16x32_bf16 v[104:107], a[4:7], a[196:199], v[104:107]\n"
        "ds_read_b64_tr_b16 a[90:91], v156 offset:28672\n"
        "v_mfma_f32_16x16x32_bf16 v[104:107], a[0:3], a[200:203], v[104:107]\n"
        "ds_read_b64_tr_b16 a[92:93], v157 offset:24576\n"
        "v_mfma_f32_16x16x32_bf16 v[108:111], a[0:3], a[204:207], v[108:111]\n"
        "ds_read_b64_tr_b16 a[94:95], v157 offset:28672\n"
        "s_waitcnt lgkmcnt(8)\n"
        "v_mfma_f32_16x16x32_bf16 v[108:111], a[4:7], a[200:203], v[108:111]\n"
        "ds_read2_b32 a[20:21], v226 offset0:224 offset1:225\n"
        "s_waitcnt lgkmcnt(8)\n"
        "v_mfma_f32_16x16x32_bf16 v[108:111], a[8:11], a[196:199], v[108:111]\n"
        "ds_read2_b32 a[22:23], v226 offset0:226 offset1:227\n"
        "v_mfma_f32_16x16x32_bf16 v[108:111], a[12:15], a[192:195], v[108:111]\n"
        "ds_read_b128 a[212:215], v228 offset:1408\n"
        "v_mfma_f32_16x16x32_bf16 v[128:131], a[16:19], a[192:195], v[128:131]\n"
        "ds_read2_b32 a[24:25], v227 offset0:96 offset1:97\n"
        "v_mfma_f32_16x16x32_bf16 v[128:131], a[12:15], a[196:199], v[128:131]\n"
        "ds_read2_b32 a[26:27], v227 offset0:98 offset1:99\n"
        "v_mfma_f32_16x16x32_bf16 v[128:131], a[8:11], a[200:203], v[128:131]\n"
        "ds_read_b128 a[216:219], v228 offset:896\n"
        "v_mfma_f32_16x16x32_bf16 v[128:131], a[4:7], a[204:207], v[128:131]\n"
        "ds_read_b128 a[220:223], v228 offset:384\n"
        "s_waitcnt lgkmcnt(8)\n"
        "v_mfma_f32_16x16x32_bf16 v[128:131], a[0:3], a[208:211], v[128:131]\n"
        "s_waitcnt vmcnt(9) lgkmcnt(7)\n"
        "ds_read2_b32 a[28:29], v227 offset0:224 offset1:225\n"
        "ds_read2_b32 a[30:31], v227 offset0:226 offset1:227\n"
        "s_barrier\n"
        "ds_read_b64_tr_b16 a[96:97], v154 offset:0\n"
        "ds_read_b64_tr_b16 a[98:99], v154 offset:4096\n"
        "ds_read_b64_tr_b16 a[100:101], v155 offset:0\n"
        "s_waitcnt lgkmcnt(9)\n"
        "v_mfma_f32_16x16x32_bf16 v[132:135], a[0:3], a[212:215], v[132:135]\n"
        "ds_read_b64_tr_b16 a[102:103], v155 offset:4096\n"
        "global_load_lds_dwordx4 v144, s[60:61] offset:0\n"
        "v_mfma_f32_16x16x32_bf16 v[132:135], a[4:7], a[208:211], v[132:135]\n"
        "ds_read_b64_tr_b16 a[104:105], v154 offset:8192\n"
        "s_waitcnt lgkmcnt(10)\n"
        "v_mfma_f32_16x16x32_bf16 v[132:135], a[8:11], a[204:207], v[132:135]\n"
        "ds_read_b64_tr_b16 a[106:107], v154 offset:12288\n"
        "s_waitcnt lgkmcnt(10)\n"
        "v_mfma_f32_16x16x32_bf16 v[132:135], a[12:15], a[200:203], v[132:135]\n"
        "ds_read_b64_tr_b16 a[108:109], v155 offset:8192\n"
        "s_waitcnt lgkmcnt(9)\n"
        "v_mfma_f32_16x16x32_bf16 v[140:143], a[0:3], a[220:223], v[140:143]\n"
        "ds_read_b64_tr_b16 a[110:111], v155 offset:12288\n"
        "global_load_lds_dwordx4 v145, s[60:61] offset:1024\n"
        "v_mfma_f32_16x16x32_bf16 v[140:143], a[4:7], a[216:219], v[140:143]\n"
        "ds_read_b64_tr_b16 a[112:113], v154 offset:16384\n"
        "v_mfma_f32_16x16x32_bf16 v[140:143], a[8:11], a[212:215], v[140:143]\n"
        "ds_read_b64_tr_b16 a[114:115], v154 offset:20480\n"
        "v_mfma_f32_16x16x32_bf16 v[140:143], a[12:15], a[208:211], v[140:143]\n"
        "ds_read_b64_tr_b16 a[116:117], v155 offset:16384\n"
        "global_load_lds_dwordx4 v146, s[60:61] offset:2048\n"
        "v_mfma_f32_16x16x32_bf16 v[140:143], a[16:19], a[204:207], v[140:143]\n"
        "ds_read_b64_tr_b16 a[118:119], v155 offset:20480\n"
        "v_mfma_f32_16x16x32_bf16 v[140:143], a[20:23], a[200:203], v[140:143]\n"
        "ds_read_b64_tr_b16 a[120:121], v154 offset:24576\n"
        "v_mfma_f32_16x16x32_bf16 v[140:143], a[24:27], a[196:199], v[140:143]\n"
        "ds_read_b64_tr_b16 a[122:123], v154 offset:28672\n"
        "s_waitcnt lgkmcnt(8)\n"
        "v_mfma_f32_16x16x32_bf16 v[140:143], a[28:31], a[192:195], v[140:143]\n"
        "ds_read_b64_tr_b16 a[124:125], v155 offset:24576\n"
        "global_load_lds_dwordx4 v147, s[60:61] offset:3072\n"
        "s_add_u32 m0, s73, 0x14000\n"
        "s_waitcnt vmcnt(7) lgkmcnt(8)\n"
        "v_mfma_f32_16x16x32_bf16 v[132:135], a[16:19], a[196:199], v[132:135]\n"
        "ds_read_b64_tr_b16 a[126:127], v155 offset:28672\n"
        "s_add_u32 s60, s22, 0x3a0000\n"
        "s_addc_u32 s61, s23, 0\n"
        "v_mfma_f32_16x16x32_bf16 v[132:135], a[20:23], a[192:195], v[132:135]\n"
        "ds_read2_b32 a[32:33], v224 offset0:112 offset1:113\n"
        "v_cmp_ne_u64 vcc, v[236:237], v[250:251]\n"
        "v_mfma_f32_16x16x32_bf16 v[136:139], a[24:27], a[192:195], v[136:139]\n"
        "global_load_lds_dwordx4 v144, s[60:61] offset:0\n"
        "s_cbranch_vccnz LPCX_%=\n"
        "LBCX_%=:\n"
        "v_mfma_f32_16x16x32_bf16 v[136:139], a[20:23], a[196:199], v[136:139]\n"
        "ds_read2_b32 a[34:35], v224 offset0:114 offset1:115\n"
        "v_mfma_f32_16x16x32_bf16 v[136:139], a[16:19], a[200:203], v[136:139]\n"
        "ds_read_b128 a[224:227], v231 offset:0\n"
        "v_mfma_f32_16x16x32_bf16 v[136:139], a[12:15], a[204:207], v[136:139]\n"
        "global_load_lds_dwordx4 v145, s[60:61] offset:1024\n"
        "ds_read2_b32 a[36:37], v224 offset0:240 offset1:241\n"
        "v_mfma_f32_16x16x32_bf16 v[136:139], a[8:11], a[208:211], v[136:139]\n"
        "ds_read2_b32 a[38:39], v224 offset0:242 offset1:243\n"
        "v_lshrrev_b32 v224, 7, v254\n"
        "v_mfma_f32_16x16x32_bf16 v[136:139], a[4:7], a[212:215], v[136:139]\n"
        "ds_read_b128 a[228:231], v228 offset:3520\n"
        "v_lshlrev_b32 v224, 12, v224\n"
        "v_mfma_f32_16x16x32_bf16 v[136:139], a[0:3], a[216:219], v[136:139]\n"
        "s_waitcnt vmcnt(6) lgkmcnt(6)\n"
        "global_load_lds_dwordx4 v146, s[60:61] offset:2048\n"
        "global_load_lds_dwordx4 v147, s[60:61] offset:3072\n"
        "s_barrier\n"
        "s_waitcnt lgkmcnt(3)\n"
        "v_mfma_f32_16x16x32_bf16 v[96:99], a[32:35], a[224:227], v[96:99]\n"
        "s_add_u32 m0, s73, 0x0\n"
        "s_waitcnt lgkmcnt(0)\n"
        "global_load_lds_dwordx4 v254, s[20:21] offset:0 sc1\n"
        "ds_read2_b32 a[40:41], v225 offset0:112 offset1:113\n"
        "v_mfma_f32_16x16x32_bf16 v[100:103], a[32:35], a[228:231], v[100:103]\n"
        "ds_read2_b32 a[42:43], v225 offset0:114 offset1:115\n"
        "s_add_u32 s60, s22, 0x20000\n"
        "s_addc_u32 s61, s23, 0\n"
        "v_mfma_f32_16x16x32_bf16 v[100:103], a[36:39], a[224:227], v[100:103]\n"
        "global_load_lds_dwordx4 v254, s[20:21] offset:1024 sc1\n"
        "global_load_lds_dwordx4 v254, s[20:21] offset:2048 sc1\n"
        "ds_read_b128 a[232:235], v228 offset:3008\n"
        "ds_read_b64 v[64:65], v229 offset:3584\n"
        "ds_read_b64 v[66:67], v229 offset:3072\n"
        "ds_read_b64 v[68:69], v229 offset:2560\n"
        "ds_read_b64 v[70:71], v229 offset:2048\n"
        "s_waitcnt lgkmcnt(5)\n"
        "v_mfma_f32_16x16x32_bf16 v[104:107], a[40:43], a[224:227], v[104:107]\n"
        "global_load_lds_dwordx4 v254, s[20:21] offset:3072 sc1\n"
        "ds_read_b64 v[72:73], v229 offset:1536\n"
        "s_add_u32 m0, s73, 0x4000\n"
        "v_mfma_f32_16x16x32_bf16 v[104:107], a[36:39], a[228:231], v[104:107]\n"
        "ds_read_b64 v[74:75], v229 offset:1024\n"
        "ds_read_b64 v[76:77], v229 offset:512\n"
        "ds_read_b64 v[78:79], v229 offset:0\n"
        "ds_read_b128 a[252:255], v228 offset:448\n"
        "ds_read2_b32 a[60:61], v227 offset0:240 offset1:241\n"
        "ds_read2_b32 a[62:63], v227 offset0:242 offset1:243\n"
        "ds_read2_b32 a[56:57], v227 offset0:112 offset1:113\n"
        "global_load_lds_dwordx4 v144, s[60:61] offset:0\n"
        "s_waitcnt lgkmcnt(12)\n"
        "v_mfma_f32_16x16x32_bf16 v[104:107], a[32:35], a[232:235], v[104:107]\n"
        "ds_read2_b32 a[58:59], v227 offset0:114 offset1:115\n"
        "ds_read2_b32 a[52:53], v226 offset0:240 offset1:241\n"
        "ds_read2_b32 a[54:55], v226 offset0:242 offset1:243\n"
        "ds_read2_b32 a[48:49], v226 offset0:112 offset1:113\n"
        "ds_read2_b32 a[50:51], v226 offset0:114 offset1:115\n"
        "ds_read2_b32 a[44:45], v225 offset0:240 offset1:241\n"
        "ds_read2_b32 a[46:47], v225 offset0:242 offset1:243\n"
        "ds_read_b128 a[236:239], v228 offset:2496\n"
        "ds_read_b128 a[240:243], v228 offset:1984\n"
        "ds_read_b128 a[244:247], v228 offset:1472\n"
        "s_waitcnt lgkmcnt(13)\n"
        "v_mfma_f32_16x16x32_bf16 v[140:143], a[32:35], a[252:255], v[140:143]\n"
        "ds_read_b128 a[248:251], v228 offset:960\n"
        "global_load_lds_dwordx4 v145, s[60:61] offset:1024\n"
        "s_add_u32 s20, s20, 0x4000\n"
        "s_addc_u32 s21, s21, 0\n"
        "ds_read_b64_tr_b16 a[128:129], v154 offset:32768\n"
        "ds_read_b64_tr_b16 a[130:131], v154 offset:36864\n"
        "v_and_b32 v229, 1, v233\n"
        "s_waitcnt lgkmcnt(12)\n"
        "v_mfma_f32_16x16x32_bf16 v[136:139], a[56:59], a[224:227], v[136:139]\n"
        "ds_read_b64_tr_b16 a[132:133], v155 offset:32768\n"
        "s_waitcnt lgkmcnt(11)\n"
        "v_mfma_f32_16x16x32_bf16 v[136:139], a[52:55], a[228:231], v[136:139]\n"
        "global_load_lds_dwordx4 v146, s[60:61] offset:2048\n"
        "s_waitcnt lgkmcnt(9)\n"
        "v_mfma_f32_16x16x32_bf16 v[128:131], a[48:51], a[224:227], v[128:131]\n"
        "ds_read_b64_tr_b16 a[134:135], v155 offset:36864\n"
        "s_waitcnt lgkmcnt(8)\n"
        "v_mfma_f32_16x16x32_bf16 v[128:131], a[44:47], a[228:231], v[128:131]\n"
        "ds_read_b64_tr_b16 a[136:137], v154 offset:40960\n"
        "s_waitcnt lgkmcnt(8)\n"
        "v_mfma_f32_16x16x32_bf16 v[108:111], a[32:35], a[236:239], v[108:111]\n"
        "global_load_lds_dwordx4 v147, s[60:61] offset:3072\n"
        "s_add_u32 m0, s73, 0x18000\n"
        "v_lshlrev_b32 v229, 9, v229\n"
        "v_mfma_f32_16x16x32_bf16 v[108:111], a[36:39], a[232:235], v[108:111]\n"
        "s_waitcnt lgkmcnt(5)\n"
        "s_barrier\n"
        "v_mfma_f32_16x16x32_bf16 v[108:111], a[40:43], a[228:231], v[108:111]\n"
        "ds_read_b64_tr_b16 a[138:139], v154 offset:45056\n"
        "s_add_u32 s60, s22, 0x60000\n"
        "s_addc_u32 s61, s23, 0\n"
        "v_mfma_f32_16x16x32_bf16 v[108:111], a[44:47], a[224:227], v[108:111]\n"
        "global_load_lds_dwordx4 v254, s[20:21] offset:0 sc1\n"
        "ds_read_b64_tr_b16 a[140:141], v155 offset:40960\n"
        "v_mfma_f32_16x16x32_bf16 v[128:131], a[40:43], a[232:235], v[128:131]\n"
        "ds_read_b64_tr_b16 a[142:143], v155 offset:45056\n"
        "v_lshl_add_u32 v229, v232, 5, v229\n"
        "v_mfma_f32_16x16x32_bf16 v[128:131], a[36:39], a[236:239], v[128:131]\n"
        "ds_read_b64_tr_b16 a[144:145], v154 offset:49152\n"
        "v_lshl_add_u32 v229, v230, 4, v229\n"
        "v_mfma_f32_16x16x32_bf16 v[128:131], a[32:35], a[240:243], v[128:131]\n"
        "global_load_lds_dwordx4 v254, s[20:21] offset:1024 sc1\n"
        "ds_read_b64_tr_b16 a[146:147], v154 offset:53248\n"
        "v_mfma_f32_16x16x32_bf16 v[132:135], a[32:35], a[244:247], v[132:135]\n"
        "ds_read_b64_tr_b16 a[148:149], v155 offset:49152\n"
        "v_and_b32 v80, 0xffff0000, v65\n"
        "v_mfma_f32_16x16x32_bf16 v[132:135], a[36:39], a[240:243], v[132:135]\n"
        "ds_read_b64_tr_b16 a[150:151], v155 offset:53248\n"
        "v_lshlrev_b32 v81, 16, v65\n"
        "v_mfma_f32_16x16x32_bf16 v[132:135], a[40:43], a[236:239], v[132:135]\n"
        "global_load_lds_dwordx4 v254, s[20:21] offset:2048 sc1\n"
        "ds_read_b64_tr_b16 a[152:153], v154 offset:57344\n"
        "v_mfma_f32_16x16x32_bf16 v[132:135], a[44:47], a[232:235], v[132:135]\n"
        "ds_read_b64_tr_b16 a[154:155], v154 offset:61440\n"
        "v_and_b32 v82, 0xffff0000, v64\n"
        "v_mfma_f32_16x16x32_bf16 v[132:135], a[48:51], a[228:231], v[132:135]\n"
        "ds_read_b64_tr_b16 a[156:157], v155 offset:57344\n"
        "v_lshlrev_b32 v83, 16, v64\n"
        "v_mfma_f32_16x16x32_bf16 v[132:135], a[52:55], a[224:227], v[132:135]\n"
        "global_load_lds_dwordx4 v254, s[20:21] offset:3072 sc1\n"
        "s_add_u32 s20, s20, 0x4000\n"
        "s_addc_u32 s21, s21, 0\n"
        "v_mfma_f32_16x16x32_bf16 v[136:139], a[48:51], a[232:235], v[136:139]\n"
        "s_add_u32 m0, s73, 0x1c000\n"
        "ds_read_b64_tr_b16 a[158:159], v155 offset:61440\n"
        "v_mfma_f32_16x16x32_bf16 v[136:139], a[44:47], a[236:239], v[136:139]\n"
        "v_mul_f32 v80, s54, v80\n"
        "v_mul_f32 v81, s54, v81\n"
        "v_mfma_f32_16x16x32_bf16 v[136:139], a[40:43], a[240:243], v[136:139]\n"
        "global_load_lds_dwordx4 v254, s[20:21] offset:0 sc1\n"
        "v_mul_f32 v82, s54, v82\n"
        "v_mfma_f32_16x16x32_bf16 v[136:139], a[36:39], a[244:247], v[136:139]\n"
        "v_mul_f32 v83, s54, v83\n"
        "v_cvt_pk_bf16_f32 v80, 0, v80\n"
        "v_mfma_f32_16x16x32_bf16 v[136:139], a[32:35], a[248:251], v[136:139]\n"
        "v_cvt_pk_bf16_f32 v81, 0, v81\n"
        "v_cvt_pk_bf16_f32 v82, 0, v82\n"
        "v_mfma_f32_16x16x32_bf16 v[140:143], a[36:39], a[248:251], v[140:143]\n"
        "global_load_lds_dwordx4 v254, s[20:21] offset:1024 sc1\n"
        "v_cvt_pk_bf16_f32 v83, 0, v83\n"
        "v_mfma_f32_16x16x32_bf16 v[140:143], a[40:43], a[244:247], v[140:143]\n"
        "v_cvt_pk_bf16_f32 v96, 0, v96\n"
        "v_cvt_pk_bf16_f32 v97, 0, v97\n"
        "v_mfma_f32_16x16x32_bf16 v[140:143], a[44:47], a[240:243], v[140:143]\n"
        "v_cvt_pk_bf16_f32 v98, 0, v98\n"
        "v_cvt_pk_bf16_f32 v99, 0, v99\n"
        "v_mfma_f32_16x16x32_bf16 v[140:143], a[48:51], a[236:239], v[140:143]\n"
        "global_load_lds_dwordx4 v254, s[20:21] offset:2048 sc1\n"
        "v_add_f32 v80, v80, v96\n"
        "v_mfma_f32_16x16x32_bf16 v[140:143], a[52:55], a[232:235], v[140:143]\n"
        "v_add_f32 v81, v81, v97\n"
        "v_add_f32 v82, v82, v98\n"
        "v_mfma_f32_16x16x32_bf16 v[140:143], a[56:59], a[228:231], v[140:143]\n"
        "v_add_f32 v83, v83, v99\n"
        "v_cvt_pk_bf16_f32 v88, v80, v81\n"
        "v_mfma_f32_16x16x32_bf16 v[140:143], a[60:63], a[224:227], v[140:143]\n"
        "global_load_lds_dwordx4 v254, s[20:21] offset:3072 sc1\n"
        "s_add_u32 m0, s73, 0x20000\n"
        "s_add_u32 s20, s20, 0x4000\n"
        "s_addc_u32 s21, s21, 0\n"
        "v_cvt_pk_bf16_f32 v89, v82, v83\n"
        "v_and_b32 v84, 0xffff0000, v67\n"
        "v_lshlrev_b32 v85, 16, v67\n"
        "v_and_b32 v86, 0xffff0000, v66\n"
        "v_lshlrev_b32 v87, 16, v66\n"
        "v_mul_f32 v84, s54, v84\n"
        "v_mul_f32 v85, s54, v85\n"
        "global_load_lds_dwordx4 v144, s[60:61] offset:0\n"
        "v_mul_f32 v86, s54, v86\n"
        "v_mul_f32 v87, s54, v87\n"
        "v_cvt_pk_bf16_f32 v84, 0, v84\n"
        "v_cvt_pk_bf16_f32 v85, 0, v85\n"
        "v_cvt_pk_bf16_f32 v86, 0, v86\n"
        "v_cvt_pk_bf16_f32 v87, 0, v87\n"
        "v_cvt_pk_bf16_f32 v100, 0, v100\n"
        "v_cvt_pk_bf16_f32 v101, 0, v101\n"
        "v_cvt_pk_bf16_f32 v102, 0, v102\n"
        "global_load_lds_dwordx4 v145, s[60:61] offset:1024\n"
        "v_cvt_pk_bf16_f32 v103, 0, v103\n"
        "v_add_f32 v84, v84, v100\n"
        "v_add_f32 v85, v85, v101\n"
        "v_add_f32 v86, v86, v102\n"
        "v_add_f32 v87, v87, v103\n"
        "v_cvt_pk_bf16_f32 v90, v84, v85\n"
        "v_cvt_pk_bf16_f32 v91, v86, v87\n"
        "v_and_b32 v80, 0xffff0000, v69\n"
        "v_permlane16_swap_b32 v88, v90\n"
        "global_load_lds_dwordx4 v146, s[60:61] offset:2048\n"
        "v_permlane16_swap_b32 v89, v91\n"
        "v_lshlrev_b32 v81, 16, v69\n"
        "v_and_b32 v82, 0xffff0000, v68\n"
        "v_lshlrev_b32 v83, 16, v68\n"
        "v_mul_f32 v80, s54, v80\n"
        "v_mul_f32 v81, s54, v81\n"
        "v_mul_f32 v82, s54, v82\n"
        "v_mul_f32 v83, s54, v83\n"
        "global_load_lds_dwordx4 v147, s[60:61] offset:3072\n"
        "s_add_u32 m0, s73, 0x24000\n"
        "s_add_u32 s60, s22, 0x80000\n"
        "s_addc_u32 s61, s23, 0\n"
        "v_cvt_pk_bf16_f32 v80, 0, v80\n"
        "v_cvt_pk_bf16_f32 v81, 0, v81\n"
        "v_cvt_pk_bf16_f32 v82, 0, v82\n"
        "v_cvt_pk_bf16_f32 v83, 0, v83\n"
        "v_cvt_pk_bf16_f32 v104, 0, v104\n"
        "v_cvt_pk_bf16_f32 v105, 0, v105\n"
        "v_cvt_pk_bf16_f32 v106, 0, v106\n"
        "global_load_lds_dwordx4 v254, s[20:21] offset:0 sc1\n"
        "v_cvt_pk_bf16_f32 v107, 0, v107\n"
        "v_add_f32 v80, v80, v104\n"
        "v_add_f32 v81, v81, v105\n"
        "v_add_f32 v82, v82, v106\n"
        "v_add_f32 v83, v83, v107\n"
        "v_cvt_pk_bf16_f32 v92, v80, v81\n"
        "v_cvt_pk_bf16_f32 v93, v82, v83\n"
        "v_and_b32 v84, 0xffff0000, v71\n"
        "v_lshlrev_b32 v85, 16, v71\n"
        "global_load_lds_dwordx4 v254, s[20:21] offset:1024 sc1\n"
        "v_and_b32 v86, 0xffff0000, v70\n"
        "v_lshlrev_b32 v87, 16, v70\n"
        "v_mul_f32 v84, s54, v84\n"
        "v_mul_f32 v85, s54, v85\n"
        "v_mul_f32 v86, s54, v86\n"
        "v_mul_f32 v87, s54, v87\n"
        "v_cvt_pk_bf16_f32 v84, 0, v84\n"
        "v_cvt_pk_bf16_f32 v85, 0, v85\n"
        "v_cvt_pk_bf16_f32 v86, 0, v86\n"
        "global_load_lds_dwordx4 v254, s[20:21] offset:2048 sc1\n"
        "s_waitcnt vmcnt(19) lgkmcnt(0)\n"
        "s_barrier\n"
        "ds_read_b128 a[0:3], v148 offset:0\n"
        "ds_read_b128 a[4:7], v148 offset:8192\n"
        "ds_read_b128 a[8:11], v148 offset:1024\n"
        "ds_read_b128 a[12:15], v148 offset:9216\n"
        "ds_read_b64_tr_b16 a[160:161], v156 offset:0\n"
        "ds_read_b64_tr_b16 a[162:163], v156 offset:4096\n"
        "ds_read_b64_tr_b16 a[164:165], v157 offset:0\n"
        "ds_read_b64_tr_b16 a[166:167], v157 offset:4096\n"
        "ds_read_b64_tr_b16 a[168:169], v156 offset:8192\n"
        "ds_read_b64_tr_b16 a[170:171], v156 offset:12288\n"
        "ds_read_b64_tr_b16 a[172:173], v157 offset:8192\n"
        "ds_read_b64_tr_b16 a[174:175], v157 offset:12288\n"
        "ds_read_b64_tr_b16 a[176:177], v156 offset:16384\n"
        "ds_read_b64_tr_b16 a[178:179], v156 offset:20480\n"
        "ds_read_b64_tr_b16 a[180:181], v157 offset:16384\n"
        "global_load_lds_dwordx4 v254, s[20:21] offset:3072 sc1\n"
        "s_add_u32 m0, s73, 0x8000\n"
        "s_waitcnt lgkmcnt(14)\n"
        "v_mfma_f32_16x16x32_bf16 v[0:3], v[160:163], a[0:3], 0\n"
        "ds_read_b64_tr_b16 a[182:183], v157 offset:20480\n"
        "s_waitcnt lgkmcnt(14)\n"
        "v_mfma_f32_16x16x32_bf16 v[0:3], v[168:171], a[4:7], v[0:3]\n"
        "ds_read_b64_tr_b16 a[184:185], v156 offset:24576\n"
        "s_add_u32 s20, s20, 0x4000\n"
        "s_addc_u32 s21, s21, 0\n"
        "v_mfma_f32_16x16x32_bf16 v[4:7], v[172:175], a[4:7], 0\n"
        "ds_read_b64_tr_b16 a[186:187], v156 offset:28672\n"
        "global_load_lds_dwordx4 v144, s[60:61] offset:0\n"
        "v_mfma_f32_16x16x32_bf16 v[4:7], v[164:167], a[0:3], v[4:7]\n"
        "ds_read_b64_tr_b16 a[188:189], v157 offset:24576\n"
        "s_waitcnt lgkmcnt(11)\n"
        "v_mfma_f32_16x16x32_bf16 v[12:15], v[164:167], a[8:11], 0\n"
        "ds_read_b64_tr_b16 a[190:191], v157 offset:28672\n"
        "v_cvt_pk_bf16_f32 v87, 0, v87\n"
        "v_mfma_f32_16x16x32_bf16 v[12:15], v[172:175], a[12:15], v[12:15]\n"
        "ds_read_b128 a[16:19], v148 offset:2048\n"
        "global_load_lds_dwordx4 v145, s[60:61] offset:1024\n"
        "v_mfma_f32_16x16x32_bf16 v[8:11], v[168:171], a[12:15], 0\n"
        "ds_read_b128 a[20:23], v148 offset:10240\n"
        "v_cvt_pk_bf16_f32 v108, 0, v108\n"
        "v_mfma_f32_16x16x32_bf16 v[8:11], v[160:163], a[8:11], v[8:11]\n"
        "ds_read_b128 a[24:27], v148 offset:3072\n"
        "ds_read_b128 a[28:31], v148 offset:11264\n"
        "ds_read_b128 a[192:195], v151 offset:0\n"
        "ds_read_b128 a[196:199], v151 offset:8192\n"
        "ds_read_b128 a[200:203], v151 offset:1024\n"
        "ds_read_b128 a[204:207], v151 offset:9216\n"
        "ds_read_b128 a[208:211], v151 offset:2048\n"
        "ds_read_b128 a[212:215], v151 offset:10240\n"
        "global_load_lds_dwordx4 v146, s[60:61] offset:2048\n"
        "ds_read_b128 a[216:219], v151 offset:3072\n"
        "ds_read_b128 a[220:223], v151 offset:11264\n"
        "v_cvt_pk_bf16_f32 v109, 0, v109\n"
        "v_cvt_pk_bf16_f32 v110, 0, v110\n"
        "v_cvt_pk_bf16_f32 v111, 0, v111\n"
        "s_waitcnt lgkmcnt(11)\n"
        "v_mfma_f32_16x16x32_bf16 v[16:19], v[160:163], a[16:19], 0\n"
        "v_add_f32 v84, v84, v108\n"
        "s_waitcnt lgkmcnt(10)\n"
        "v_mfma_f32_16x16x32_bf16 v[16:19], v[168:171], a[20:23], v[16:19]\n"
        "global_load_lds_dwordx4 v147, s[60:61] offset:3072\n"
        "s_add_u32 m0, s73, 0xc000\n"
        "v_add_f32 v85, v85, v109\n"
        "v_mfma_f32_16x16x32_bf16 v[20:23], v[172:175], a[20:23], 0\n"
        "s_add_u32 s60, s22, 0xc0000\n"
        "s_addc_u32 s61, s23, 0\n"
        "v_add_f32 v86, v86, v110\n"
        "v_mfma_f32_16x16x32_bf16 v[20:23], v[164:167], a[16:19], v[20:23]\n"
        "s_waitcnt lgkmcnt(7)\n"
        "v_add_f32 v87, v87, v111\n"
        "v_cvt_pk_bf16_f32 v94, v84, v85\n"
        "v_mfma_f32_16x16x32_bf16 v[28:31], v[164:167], a[24:27], 0\n"
        "global_load_lds_dwordx4 v254, s[20:21] offset:0 sc1\n"
        "v_cvt_pk_bf16_f32 v95, v86, v87\n"
        "v_mfma_f32_16x16x32_bf16 v[28:31], v[172:175], a[28:31], v[28:31]\n"
        "s_waitcnt lgkmcnt(3)\n"
        "v_permlane16_swap_b32 v92, v94\n"
        "v_mfma_f32_16x16x32_bf16 v[24:27], v[168:171], a[28:31], 0\n"
        "v_permlane16_swap_b32 v93, v95\n"
        "v_mfma_f32_16x16x32_bf16 v[24:27], v[160:163], a[24:27], v[24:27]\n"
        "s_waitcnt vmcnt(17)\n"
        "s_barrier\n"
        "v_and_b32 v80, 0xffff0000, v73\n"
        "v_mfma_f32_16x16x32_bf16 v[32:35], v[160:163], a[192:195], 0\n"
        "global_load_lds_dwordx4 v254, s[20:21] offset:1024 sc1\n"
        "ds_read_b64_tr_b16 a[32:33], v154 offset:16384\n"
        "v_mfma_f32_16x16x32_bf16 v[32:35], v[168:171], a[196:199], v[32:35]\n"
        "ds_read_b64_tr_b16 a[34:35], v154 offset:20480\n"
        "v_lshlrev_b32 v81, 16, v73\n"
        "v_mfma_f32_16x16x32_bf16 v[36:39], v[172:175], a[196:199], 0\n"
        "ds_read_b64_tr_b16 a[40:41], v154 offset:24576\n"
        "v_and_b32 v82, 0xffff0000, v72\n"
        "v_mfma_f32_16x16x32_bf16 v[36:39], v[164:167], a[192:195], v[36:39]\n"
        "global_load_lds_dwordx4 v254, s[20:21] offset:2048 sc1\n"
        "ds_read_b64_tr_b16 a[42:43], v154 offset:28672\n"
        "v_mfma_f32_16x16x32_bf16 v[44:47], v[164:167], a[200:203], 0\n"
        "ds_read_b64_tr_b16 a[36:37], v155 offset:16384\n"
        "v_lshlrev_b32 v83, 16, v72\n"
        "v_mfma_f32_16x16x32_bf16 v[44:47], v[172:175], a[204:207], v[44:47]\n"
        "ds_read_b64_tr_b16 a[38:39], v155 offset:20480\n"
        "v_mul_f32 v80, s54, v80\n"
        "v_mfma_f32_16x16x32_bf16 v[40:43], v[168:171], a[204:207], 0\n"
        "global_load_lds_dwordx4 v254, s[20:21] offset:3072 sc1\n"
        "s_add_u32 m0, s73, 0x10000\n"
        "v_mul_f32 v81, s54, v81\n"
        "v_mfma_f32_16x16x32_bf16 v[40:43], v[160:163], a[200:203], v[40:43]\n"
        "s_add_u32 s20, s20, 0x4000\n"
        "s_addc_u32 s21, s21, 0\n"
        "v_mul_f32 v82, s54, v82\n"
        "v_mfma_f32_16x16x32_bf16 v[48:51], v[160:163], a[208:211], 0\n"
        "ds_read_b64_tr_b16 a[44:45], v155 offset:24576\n"
        "s_waitcnt lgkmcnt(6)\n"
        "v_mfma_f32_16x16x32_bf16 v[48:51], v[168:171], a[212:215], v[48:51]\n"
        "global_load_lds_dwordx4 v254, s[20:21] offset:0 sc1\n"
        "ds_read_b64_tr_b16 a[46:47], v155 offset:28672\n"
        "v_mfma_f32_16x16x32_bf16 v[52:55], v[172:175], a[212:215], 0\n"
        "ds_read_b128 a[0:3], v149 offset:32768\n"
        "v_mul_f32 v83, s54, v83\n"
        "v_mfma_f32_16x16x32_bf16 v[52:55], v[164:167], a[208:211], v[52:55]\n"
        "ds_read_b128 a[4:7], v149 offset:40960\n"
        "v_cvt_pk_bf16_f32 v80, 0, v80\n"
        "v_mfma_f32_16x16x32_bf16 v[60:63], v[164:167], a[216:219], 0\n"
        "global_load_lds_dwordx4 v254, s[20:21] offset:1024 sc1\n"
        "ds_read_b128 a[8:11], v149 offset:33792\n"
        "v_mfma_f32_16x16x32_bf16 v[60:63], v[172:175], a[220:223], v[60:63]\n"
        "ds_read_b128 a[12:15], v149 offset:41984\n"
        "v_cvt_pk_bf16_f32 v81, 0, v81\n"
        "v_mfma_f32_16x16x32_bf16 v[56:59], v[168:171], a[220:223], 0\n"
        "ds_read_b128 a[16:19], v149 offset:34816\n"
        "v_cvt_pk_bf16_f32 v82, 0, v82\n"
        "v_mfma_f32_16x16x32_bf16 v[56:59], v[160:163], a[216:219], v[56:59]\n"
        "global_load_lds_dwordx4 v254, s[20:21] offset:2048 sc1\n"
        "ds_read_b128 a[20:23], v149 offset:43008\n"
        "ds_read_b128 a[24:27], v149 offset:35840\n"
        "ds_read_b128 a[28:31], v149 offset:44032\n"
        "ds_read_b128 a[192:195], v152 offset:32768\n"
        "ds_read_b128 a[196:199], v152 offset:40960\n"
        "s_waitcnt lgkmcnt(9)\n"
        "v_mfma_f32_16x16x32_bf16 v[0:3], a[32:35], a[0:3], v[0:3]\n"
        "global_load_lds_dwordx4 v254, s[20:21] offset:3072 sc1\n"
        "s_add_u32 m0, s73, 0x14000\n"
        "s_waitcnt lgkmcnt(8)\n"
        "v_mfma_f32_16x16x32_bf16 v[0:3], a[40:43], a[4:7], v[0:3]\n"
        "s_add_u32 s20, s20, 0x4000\n"
        "s_addc_u32 s21, s21, 0\n"
        "v_cvt_pk_bf16_f32 v83, 0, v83\n"
        "v_mfma_f32_16x16x32_bf16 v[4:7], a[44:47], a[4:7], v[4:7]\n"
        "ds_read_b128 a[200:203], v152 offset:33792\n"
        "v_cvt_pk_bf16_f32 v128, 0, v128\n"
        "v_mfma_f32_16x16x32_bf16 v[4:7], a[36:39], a[0:3], v[4:7]\n"
        "global_load_lds_dwordx4 v144, s[60:61] offset:0\n"
        "s_waitcnt lgkmcnt(7)\n"
        "v_mfma_f32_16x16x32_bf16 v[12:15], a[36:39], a[8:11], v[12:15]\n"
        "ds_read_b128 a[204:207], v152 offset:41984\n"
        "v_cvt_pk_bf16_f32 v129, 0, v129\n"
        "v_mfma_f32_16x16x32_bf16 v[12:15], a[44:47], a[12:15], v[12:15]\n"
        "ds_read_b128 a[208:211], v152 offset:34816\n"
        "v_cvt_pk_bf16_f32 v130, 0, v130\n"
        "v_mfma_f32_16x16x32_bf16 v[8:11], a[40:43], a[12:15], v[8:11]\n"
        "global_load_lds_dwordx4 v145, s[60:61] offset:1024\n"
        "ds_read_b128 a[212:215], v152 offset:43008\n"
        "v_mfma_f32_16x16x32_bf16 v[8:11], a[32:35], a[8:11], v[8:11]\n"
        "ds_read_b128 a[216:219], v152 offset:35840\n"
        "s_waitcnt lgkmcnt(5)\n"
        "v_mfma_f32_16x16x32_bf16 v[16:19], a[32:35], a[16:19], v[16:19]\n"
        "ds_read_b128 a[220:223], v152 offset:44032\n"
        "v_cvt_pk_bf16_f32 v131, 0, v131\n"
        "v_mfma_f32_16x16x32_bf16 v[16:19], a[40:43], a[20:23], v[16:19]\n"
        "global_load_lds_dwordx4 v146, s[60:61] offset:2048\n"
        "v_add_f32 v80, v80, v128\n"
        "v_mfma_f32_16x16x32_bf16 v[20:23], a[44:47], a[20:23], v[20:23]\n"
        "s_waitcnt lgkmcnt(5)\n"
        "v_add_f32 v81, v81, v129\n"
        "v_add_f32 v82, v82, v130\n"
        "v_mfma_f32_16x16x32_bf16 v[20:23], a[36:39], a[16:19], v[20:23]\n"
        "s_waitcnt vmcnt(23)\n"
        "s_barrier\n"
        "v_add_f32 v83, v83, v131\n"
        "v_mfma_f32_16x16x32_bf16 v[28:31], a[36:39], a[24:27], v[28:31]\n"
        "global_load_lds_dwordx4 v147, s[60:61] offset:3072\n"
        "s_add_u32 m0, s73, 0x0\n"
        "v_and_b32 v84, 0xffff0000, v75\n"
        "v_mfma_f32_16x16x32_bf16 v[28:31], a[44:47], a[28:31], v[28:31]\n"
        "ds_read_b128 a[0:3], v149 offset:49152\n"
        "v_lshlrev_b32 v85, 16, v75\n"
        "v_mfma_f32_16x16x32_bf16 v[24:27], a[40:43], a[28:31], v[24:27]\n"
        "ds_read_b128 a[4:7], v149 offset:57344\n"
        "v_and_b32 v86, 0xffff0000, v74\n"
        "v_mfma_f32_16x16x32_bf16 v[24:27], a[32:35], a[24:27], v[24:27]\n"
        "global_load_lds_dwordx4 v254, s[20:21] offset:0 sc1\n"
        "ds_read_b128 a[8:11], v149 offset:50176\n"
        "v_mfma_f32_16x16x32_bf16 v[32:35], a[32:35], a[192:195], v[32:35]\n"
        "ds_read_b128 a[12:15], v149 offset:58368\n"
        "v_lshlrev_b32 v87, 16, v74\n"
        "v_mfma_f32_16x16x32_bf16 v[32:35], a[40:43], a[196:199], v[32:35]\n"
        "ds_read_b128 a[16:19], v149 offset:51200\n"
        "v_mul_f32 v84, s54, v84\n"
        "v_mfma_f32_16x16x32_bf16 v[36:39], a[44:47], a[196:199], v[36:39]\n"
        "global_load_lds_dwordx4 v254, s[20:21] offset:1024 sc1\n"
        "ds_read_b128 a[20:23], v149 offset:59392\n"
        "v_mfma_f32_16x16x32_bf16 v[36:39], a[36:39], a[192:195], v[36:39]\n"
        "ds_read_b128 a[24:27], v149 offset:52224\n"
        "v_mul_f32 v85, s54, v85\n"
        "v_mfma_f32_16x16x32_bf16 v[44:47], a[36:39], a[200:203], v[44:47]\n"
        "ds_read_b128 a[28:31], v149 offset:60416\n"
        "s_waitcnt lgkmcnt(8)\n"
        "v_mfma_f32_16x16x32_bf16 v[44:47], a[44:47], a[204:207], v[44:47]\n"
        "global_load_lds_dwordx4 v254, s[20:21] offset:2048 sc1\n"
        "ds_read_b128 a[192:195], v152 offset:49152\n"
        "v_mfma_f32_16x16x32_bf16 v[40:43], a[40:43], a[204:207], v[40:43]\n"
        "ds_read_b128 a[196:199], v152 offset:57344\n"
        "v_mul_f32 v86, s54, v86\n"
        "LHB2_%=:\n"
        "s_cbranch_execz LHB6_%=\n"
        "v_mfma_f32_16x16x32_bf16 v[40:43], a[32:35], a[200:203], v[40:43]\n"
        "s_waitcnt lgkmcnt(8)\n"
        "v_mul_f32 v87, s54, v87\n"
        "v_cvt_pk_bf16_f32 v84, 0, v84\n"
        "v_mfma_f32_16x16x32_bf16 v[48:51], a[32:35], a[208:211], v[48:51]\n"
        "global_load_lds_dwordx4 v254, s[20:21] offset:3072 sc1\n"
        "s_add_u32 s20, s20, 0x4000\n"
        "s_addc_u32 s21, s21, 0\n"
        "v_mfma_f32_16x16x32_bf16 v[48:51], a[40:43], a[212:215], v[48:51]\n"
        "s_waitcnt lgkmcnt(5)\n"
        "v_cvt_pk_bf16_f32 v85, 0, v85\n"
        "v_cvt_pk_bf16_f32 v86, 0, v86\n"
        "v_mfma_f32_16x16x32_bf16 v[52:55], a[44:47], a[212:215], v[52:55]\n"
        "ds_read_b128 a[200:203], v152 offset:50176\n"
        "s_add_u32 m0, s73, 0x4000\n"
        "v_cvt_pk_bf16_f32 v87, 0, v87\n"
        "v_mfma_f32_16x16x32_bf16 v[52:55], a[36:39], a[208:211], v[52:55]\n"
        "global_store_dwordx4 v229, v[88:91], s[24:25] offset:0 sc1\n"
        "ds_read_b128 a[204:207], v152 offset:58368\n"
        "ds_read_b128 a[208:211], v152 offset:51200\n"
        "v_cvt_pk_bf16_f32 v88, v80, v81\n"
        "v_mfma_f32_16x16x32_bf16 v[60:63], a[36:39], a[216:219], v[60:63]\n"
        "ds_read_b128 a[212:215], v152 offset:59392\n"
        "v_cvt_pk_bf16_f32 v89, v82, v83\n"
        "v_mfma_f32_16x16x32_bf16 v[60:63], a[44:47], a[220:223], v[60:63]\n"
        "s_add_u32 s60, s22, 0xe0000\n"
        "s_addc_u32 s61, s23, 0\n"
        "v_cvt_pk_bf16_f32 v132, 0, v132\n"
        "v_mfma_f32_16x16x32_bf16 v[56:59], a[40:43], a[220:223], v[56:59]\n"
        "global_store_dwordx4 v229, v[92:95], s[24:25] offset:1024 sc1\n"
        "ds_read_b128 a[220:223], v152 offset:60416\n"
        "v_cvt_pk_bf16_f32 v133, 0, v133\n"
        "v_cvt_pk_bf16_f32 v134, 0, v134\n"
        "v_cvt_pk_bf16_f32 v135, 0, v135\n"
        "v_mfma_f32_16x16x32_bf16 v[56:59], a[32:35], a[216:219], v[56:59]\n"
        "s_waitcnt lgkmcnt(5)\n"
        "v_add_f32 v84, v84, v132\n"
        "v_add_f32 v85, v85, v133\n"
        "v_mfma_f32_16x16x32_bf16 v[0:3], v[176:179], a[0:3], v[0:3]\n"
        "ds_read_b128 a[216:219], v152 offset:52224\n"
        "v_add_f32 v86, v86, v134\n"
        "v_mfma_f32_16x16x32_bf16 v[0:3], v[184:187], a[4:7], v[0:3]\n"
        "v_add_f32 v87, v87, v135\n"
        "v_cvt_pk_bf16_f32 v90, v84, v85\n"
        "v_mfma_f32_16x16x32_bf16 v[4:7], v[188:191], a[4:7], v[4:7]\n"
        "v_cvt_pk_bf16_f32 v91, v86, v87\n"
        "v_and_b32 v80, 0xffff0000, v77\n"
        "v_mfma_f32_16x16x32_bf16 v[4:7], v[180:183], a[0:3], v[4:7]\n"
        "v_permlane16_swap_b32 v88, v90\n"
        "v_mfma_f32_16x16x32_bf16 v[12:15], v[180:183], a[8:11], v[12:15]\n"
        "v_permlane16_swap_b32 v89, v91\n"
        "v_mfma_f32_16x16x32_bf16 v[12:15], v[188:191], a[12:15], v[12:15]\n"
        "global_store_dwordx4 v229, v[88:91], s[24:25] offset:2048 sc1\n"
        "v_lshlrev_b32 v81, 16, v77\n"
        "v_and_b32 v82, 0xffff0000, v76\n"
        "v_lshlrev_b32 v83, 16, v76\n"
        "v_mul_f32 v80, s54, v80\n"
        "v_mfma_f32_16x16x32_bf16 v[8:11], v[184:187], a[12:15], v[8:11]\n"
        "v_mul_f32 v81, s54, v81\n"
        "v_mul_f32 v82, s54, v82\n"
        "v_mfma_f32_16x16x32_bf16 v[8:11], v[176:179], a[8:11], v[8:11]\n"
        "v_mul_f32 v83, s54, v83\n"
        "v_cvt_pk_bf16_f32 v80, 0, v80\n"
        "v_mfma_f32_16x16x32_bf16 v[16:19], v[176:179], a[16:19], v[16:19]\n"
        "v_cvt_pk_bf16_f32 v81, 0, v81\n"
        "v_cvt_pk_bf16_f32 v82, 0, v82\n"
        "v_mfma_f32_16x16x32_bf16 v[16:19], v[184:187], a[20:23], v[16:19]\n"
        "s_waitcnt lgkmcnt(0)\n"
        "v_cvt_pk_bf16_f32 v83, 0, v83\n"
        "v_cvt_pk_bf16_f32 v136, 0, v136\n"
        "v_mfma_f32_16x16x32_bf16 v[20:23], v[188:191], a[20:23], v[20:23]\n"
        "v_cvt_pk_bf16_f32 v137, 0, v137\n"
        "v_cvt_pk_bf16_f32 v138, 0, v138\n"
        "v_mfma_f32_16x16x32_bf16 v[20:23], v[180:183], a[16:19], v[20:23]\n"
        "s_waitcnt vmcnt(23)\n"
        "v_cvt_pk_bf16_f32 v139, 0, v139\n"
        "v_add_f32 v80, v80, v136\n"
        "v_mfma_f32_16x16x32_bf16 v[28:31], v[180:183], a[24:27], v[28:31]\n"
        "s_barrier\n"
        "v_add_f32 v81, v81, v137\n"
        "v_add_f32 v82, v82, v138\n"
        "v_mfma_f32_16x16x32_bf16 v[28:31], v[188:191], a[28:31], v[28:31]\n"
        "global_load_lds_dwordx4 v144, s[60:61] offset:0\n"
        "v_add_f32 v83, v83, v139\n"
        "v_mfma_f32_16x16x32_bf16 v[24:27], v[184:187], a[28:31], v[24:27]\n"
        "ds_read_b64_tr_b16 a[48:49], v158 offset:0\n"
        "v_cvt_pk_bf16_f32 v92, v80, v81\n"
        "v_mfma_f32_16x16x32_bf16 v[24:27], v[176:179], a[24:27], v[24:27]\n"
        "ds_read_b64_tr_b16 a[50:51], v158 offset:4096\n"
        "v_cvt_pk_bf16_f32 v93, v82, v83\n"
        "v_mfma_f32_16x16x32_bf16 v[32:35], v[176:179], a[192:195], v[32:35]\n"
        "ds_read_b64_tr_b16 a[56:57], v158 offset:8192\n"
        "v_and_b32 v84, 0xffff0000, v79\n"
        "v_mfma_f32_16x16x32_bf16 v[32:35], v[184:187], a[196:199], v[32:35]\n"
        "ds_read_b64_tr_b16 a[58:59], v158 offset:12288\n"
        "v_lshlrev_b32 v85, 16, v79\n"
        "v_mfma_f32_16x16x32_bf16 v[36:39], v[188:191], a[196:199], v[36:39]\n"
        "ds_read_b64_tr_b16 a[52:53], v159 offset:0\n"
        "v_and_b32 v86, 0xffff0000, v78\n"
        "v_mfma_f32_16x16x32_bf16 v[36:39], v[180:183], a[192:195], v[36:39]\n"
        "ds_read_b64_tr_b16 a[54:55], v159 offset:4096\n"
        "v_lshlrev_b32 v87, 16, v78\n"
        "v_mfma_f32_16x16x32_bf16 v[44:47], v[180:183], a[200:203], v[44:47]\n"
        "ds_read_b64_tr_b16 a[60:61], v159 offset:8192\n"
        "v_mul_f32 v84, s54, v84\n"
        "v_mfma_f32_16x16x32_bf16 v[44:47], v[188:191], a[204:207], v[44:47]\n"
        "ds_read_b64_tr_b16 a[62:63], v159 offset:12288\n"
        "v_mul_f32 v85, s54, v85\n"
        "v_mfma_f32_16x16x32_bf16 v[40:43], v[184:187], a[204:207], v[40:43]\n"
        "ds_read_b128 a[0:3], v150 offset:16384\n"
        "v_mul_f32 v86, s54, v86\n"
        "v_mfma_f32_16x16x32_bf16 v[40:43], v[176:179], a[200:203], v[40:43]\n"
        "ds_read_b128 a[4:7], v150 offset:24576\n"
        "v_mul_f32 v87, s54, v87\n"
        "v_mfma_f32_16x16x32_bf16 v[48:51], v[176:179], a[208:211], v[48:51]\n"
        "global_load_lds_dwordx4 v145, s[60:61] offset:1024\n"
        "v_cvt_pk_bf16_f32 v84, 0, v84\n"
        "v_mfma_f32_16x16x32_bf16 v[48:51], v[184:187], a[212:215], v[48:51]\n"
        "ds_read_b128 a[8:11], v150 offset:17408\n"
        "v_cvt_pk_bf16_f32 v85, 0, v85\n"
        "v_mfma_f32_16x16x32_bf16 v[52:55], v[188:191], a[212:215], v[52:55]\n"
        "ds_read_b128 a[12:15], v150 offset:25600\n"
        "v_cvt_pk_bf16_f32 v86, 0, v86\n"
        "v_mfma_f32_16x16x32_bf16 v[52:55], v[180:183], a[208:211], v[52:55]\n"
        "global_load_lds_dwordx4 v146, s[60:61] offset:2048\n"
        "v_cvt_pk_bf16_f32 v87, 0, v87\n"
        "v_mfma_f32_16x16x32_bf16 v[60:63], v[180:183], a[216:219], v[60:63]\n"
        "ds_read_b128 a[16:19], v150 offset:18432\n"
        "v_cvt_pk_bf16_f32 v140, 0, v140\n"
        "v_mfma_f32_16x16x32_bf16 v[60:63], v[188:191], a[220:223], v[60:63]\n"
        "s_waitcnt lgkmcnt(5)\n"
        "v_cvt_pk_bf16_f32 v141, 0, v141\n"
        "v_cvt_pk_bf16_f32 v142, 0, v142\n"
        "v_mfma_f32_16x16x32_bf16 v[56:59], v[184:187], a[220:223], v[56:59]\n"
        "s_waitcnt lgkmcnt(4)\n"
        "v_cvt_pk_bf16_f32 v143, 0, v143\n"
        "v_add_f32 v84, v84, v140\n"
        "v_mfma_f32_16x16x32_bf16 v[56:59], v[176:179], a[216:219], v[56:59]\n"
        "s_waitcnt lgkmcnt(3)\n"
        "v_add_f32 v85, v85, v141\n"
        "v_add_f32 v86, v86, v142\n"
        "v_mfma_f32_16x16x32_bf16 v[0:3], a[48:51], a[0:3], v[0:3]\n"
        "ds_read_b128 a[20:23], v150 offset:26624\n"
        "v_add_f32 v87, v87, v143\n"
        "v_mfma_f32_16x16x32_bf16 v[0:3], a[56:59], a[4:7], v[0:3]\n"
        "s_waitcnt lgkmcnt(3)\n"
        "v_cvt_pk_bf16_f32 v94, v84, v85\n"
        "v_cvt_pk_bf16_f32 v95, v86, v87\n"
        "v_mfma_f32_16x16x32_bf16 v[4:7], a[60:63], a[4:7], v[4:7]\n"
        "s_waitcnt lgkmcnt(2)\n"
        "v_permlane16_swap_b32 v92, v94\n"
        "v_mfma_f32_16x16x32_bf16 v[4:7], a[52:55], a[0:3], v[4:7]\n"
        "v_permlane16_swap_b32 v93, v95\n"
        "v_mfma_f32_16x16x32_bf16 v[12:15], a[52:55], a[8:11], v[12:15]\n"
        "global_store_dwordx4 v229, v[92:95], s[24:25] offset:3072 sc1\n"
        "ds_read_b128 a[24:27], v150 offset:19456\n"
        "ds_read_b128 a[28:31], v150 offset:27648\n"
        "v_and_b32 v226, 1, v233\n"
        "v_mfma_f32_16x16x32_bf16 v[12:15], a[60:63], a[12:15], v[12:15]\n"
        "ds_read_b128 a[192:195], v153 offset:16384\n"
        "v_lshlrev_b32 v227, 11, v232\n"
        "v_mfma_f32_16x16x32_bf16 v[8:11], a[56:59], a[12:15], v[8:11]\n"
        "global_load_lds_dwordx4 v147, s[60:61] offset:3072\n"
        "s_add_u32 m0, s73, 0x18000\n"
        "v_mfma_f32_16x16x32_bf16 v[8:11], a[48:51], a[8:11], v[8:11]\n"
        "ds_read_b128 a[196:199], v153 offset:24576\n"
        "s_waitcnt lgkmcnt(4)\n"
        "v_mfma_f32_16x16x32_bf16 v[16:19], a[48:51], a[16:19], v[16:19]\n"
        "ds_read_b128 a[200:203], v153 offset:17408\n"
        "s_add_u32 s60, s22, 0x120000\n"
        "s_addc_u32 s61, s23, 0\n"
        "v_mfma_f32_16x16x32_bf16 v[16:19], a[56:59], a[20:23], v[16:19]\n"
        "global_load_lds_dwordx4 v254, s[20:21] offset:0 sc1\n"
        "v_mfma_f32_16x16x32_bf16 v[20:23], a[60:63], a[20:23], v[20:23]\n"
        "ds_read_b128 a[204:207], v153 offset:25600\n"
        "v_lshl_add_u32 v227, v226, 5, v227\n"
        "v_mfma_f32_16x16x32_bf16 v[20:23], a[52:55], a[16:19], v[20:23]\n"
        "ds_read_b128 a[208:211], v153 offset:18432\n"
        "s_waitcnt lgkmcnt(6)\n"
        "v_mfma_f32_16x16x32_bf16 v[28:31], a[52:55], a[24:27], v[28:31]\n"
        "s_waitcnt lgkmcnt(5)\n"
        "global_load_lds_dwordx4 v254, s[20:21] offset:1024 sc1\n"
        "v_mfma_f32_16x16x32_bf16 v[28:31], a[60:63], a[28:31], v[28:31]\n"
        "ds_read_b128 a[212:215], v153 offset:26624\n"
        "s_waitcnt vmcnt(22)\n"
        "v_mfma_f32_16x16x32_bf16 v[24:27], a[56:59], a[28:31], v[24:27]\n"
        "ds_read_b128 a[216:219], v153 offset:19456\n"
        "s_barrier\n"
        "v_lshrrev_b32 v226, 1, v233\n"
        "v_mfma_f32_16x16x32_bf16 v[24:27], a[48:51], a[24:27], v[24:27]\n"
        "s_waitcnt lgkmcnt(5)\n"
        "global_load_lds_dwordx4 v254, s[20:21] offset:2048 sc1\n"
        "v_mfma_f32_16x16x32_bf16 v[32:35], a[48:51], a[192:195], v[32:35]\n"
        "ds_read_b128 a[220:223], v153 offset:27648\n"
        "v_lshl_add_u32 v227, v226, 4, v227\n"
        "v_mfma_f32_16x16x32_bf16 v[32:35], a[56:59], a[196:199], v[32:35]\n"
        "ds_read_b64_tr_b16 a[224:225], v154 offset:32768\n"
        "v_and_b32 v226, 0x70, v254\n"
        "v_mfma_f32_16x16x32_bf16 v[36:39], a[60:63], a[196:199], v[36:39]\n"
        "global_load_lds_dwordx4 v254, s[20:21] offset:3072 sc1\n"
        "s_add_u32 m0, s73, 0x1c000\n"
        "v_mfma_f32_16x16x32_bf16 v[36:39], a[52:55], a[192:195], v[36:39]\n"
        "s_add_u32 s20, s20, 0x4000\n"
        "s_addc_u32 s21, s21, 0\n"
        "s_waitcnt lgkmcnt(4)\n"
        "v_mfma_f32_16x16x32_bf16 v[44:47], a[52:55], a[200:203], v[44:47]\n"
        "ds_read_b64_tr_b16 a[226:227], v154 offset:36864\n"
        "v_add_u32 v224, v224, v226\n"
        "v_mfma_f32_16x16x32_bf16 v[44:47], a[60:63], a[204:207], v[44:47]\n"
        "global_load_lds_dwordx4 v254, s[20:21] offset:0 sc1\n"
        "v_mfma_f32_16x16x32_bf16 v[40:43], a[56:59], a[204:207], v[40:43]\n"
        "ds_read_b64_tr_b16 a[232:233], v154 offset:40960\n"
        "v_lshrrev_b32 v225, 6, v254\n"
        "v_mfma_f32_16x16x32_bf16 v[40:43], a[48:51], a[200:203], v[40:43]\n"
        "ds_read_b64_tr_b16 a[234:235], v154 offset:45056\n"
        "v_lshlrev_b32 v225, 7, v225\n"
        "v_mfma_f32_16x16x32_bf16 v[48:51], a[48:51], a[208:211], v[48:51]\n"
        "ds_read_b64_tr_b16 a[228:229], v155 offset:32768\n"
        "s_waitcnt lgkmcnt(5)\n"
        "v_mfma_f32_16x16x32_bf16 v[48:51], a[56:59], a[212:215], v[48:51]\n"
        "ds_read_b64_tr_b16 a[230:231], v155 offset:36864\n"
        "global_load_lds_dwordx4 v254, s[20:21] offset:1024 sc1\n"
        "v_mfma_f32_16x16x32_bf16 v[52:55], a[60:63], a[212:215], v[52:55]\n"
        "ds_read_b64_tr_b16 a[236:237], v155 offset:40960\n"
        "v_bfe_u32 v226, v254, 4, 2\n"
        "v_mfma_f32_16x16x32_bf16 v[52:55], a[52:55], a[208:211], v[52:55]\n"
        "ds_read_b64_tr_b16 a[238:239], v155 offset:45056\n"
        "v_lshl_add_u32 v225, v226, 3, v225\n"
        "v_mfma_f32_16x16x32_bf16 v[60:63], a[52:55], a[216:219], v[60:63]\n"
        "ds_read_b128 a[0:3], v148 offset:49152\n"
        "global_load_lds_dwordx4 v254, s[20:21] offset:2048 sc1\n"
        "v_mfma_f32_16x16x32_bf16 v[60:63], a[60:63], a[220:223], v[60:63]\n"
        "ds_read_b128 a[4:7], v148 offset:57344\n"
        "v_mov_b32 v136, 0\n"
        "v_mfma_f32_16x16x32_bf16 v[56:59], a[56:59], a[220:223], v[56:59]\n"
        "ds_read_b128 a[8:11], v148 offset:50176\n"
        "v_mov_b32 v137, 0\n"
        "v_mfma_f32_16x16x32_bf16 v[56:59], a[48:51], a[216:219], v[56:59]\n"
        "ds_read_b128 a[12:15], v148 offset:58368\n"
        "ds_read_b128 a[16:19], v148 offset:51200\n"
        "ds_read_b128 a[20:23], v148 offset:59392\n"
        "global_load_lds_dwordx4 v254, s[20:21] offset:3072 sc1\n"
        "ds_read_b128 a[24:27], v148 offset:52224\n"
        "ds_read_b128 a[28:31], v148 offset:60416\n"
        "s_add_u32 s20, s20, 0x4000\n"
        "s_addc_u32 s21, s21, 0\n"
        "ds_read_b128 a[192:195], v151 offset:49152\n"
        "ds_read_b128 a[196:199], v151 offset:57344\n"
        "ds_read_b128 a[200:203], v151 offset:50176\n"
        "ds_read_b128 a[204:207], v151 offset:58368\n"
        "s_add_u32 m0, s73, 0x20000\n"
        "s_waitcnt lgkmcnt(11)\n"
        "v_mfma_f32_16x16x32_bf16 v[0:3], a[224:227], a[0:3], v[0:3]\n"
        "ds_read_b128 a[208:211], v151 offset:51200\n"
        "s_waitcnt lgkmcnt(11)\n"
        "v_mfma_f32_16x16x32_bf16 v[0:3], a[232:235], a[4:7], v[0:3]\n"
        "ds_read_b128 a[212:215], v151 offset:59392\n"
        "v_mov_b32 v140, 0\n"
        "v_mfma_f32_16x16x32_bf16 v[4:7], a[236:239], a[4:7], v[4:7]\n"
        "ds_read_b128 a[216:219], v151 offset:52224\n"
        "v_mov_b32 v141, 0\n"
        "v_mfma_f32_16x16x32_bf16 v[4:7], a[228:231], a[0:3], v[4:7]\n"
        "ds_read_b128 a[220:223], v151 offset:60416\n"
        "s_waitcnt lgkmcnt(11)\n"
        "v_mfma_f32_16x16x32_bf16 v[12:15], a[228:231], a[8:11], v[12:15]\n"
        "s_waitcnt lgkmcnt(10)\n"
        "v_add_u32 v225, s16, v225\n"
        "s_waitcnt lgkmcnt(9)\n"
        "v_mfma_f32_16x16x32_bf16 v[12:15], a[236:239], a[12:15], v[12:15]\n"
        "s_waitcnt lgkmcnt(8)\n"
        "s_waitcnt lgkmcnt(7)\n"
        "v_mfma_f32_16x16x32_bf16 v[8:11], a[232:235], a[12:15], v[8:11]\n"
        "v_mfma_f32_16x16x32_bf16 v[8:11], a[224:227], a[8:11], v[8:11]\n"
        "s_waitcnt lgkmcnt(4)\n"
        "v_mfma_f32_16x16x32_bf16 v[16:19], a[224:227], a[16:19], v[16:19]\n"
        "v_mfma_f32_16x16x32_bf16 v[16:19], a[232:235], a[20:23], v[16:19]\n"
        "v_mfma_f32_16x16x32_bf16 v[20:23], a[236:239], a[20:23], v[20:23]\n"
        "v_mfma_f32_16x16x32_bf16 v[20:23], a[228:231], a[16:19], v[20:23]\n"
        "s_waitcnt vmcnt(24)\n"
        "s_barrier\n"
        "v_mfma_f32_16x16x32_bf16 v[28:31], a[228:231], a[24:27], v[28:31]\n"
        "global_load_lds_dwordx4 v144, s[60:61] offset:0\n"
        "ds_read_b128 a[0:3], v149 offset:0\n"
        "v_mfma_f32_16x16x32_bf16 v[28:31], a[236:239], a[28:31], v[28:31]\n"
        "ds_read_b128 a[4:7], v149 offset:8192\n"
        "s_waitcnt lgkmcnt(2)\n"
        "v_mfma_f32_16x16x32_bf16 v[24:27], a[232:235], a[28:31], v[24:27]\n"
        "ds_read_b128 a[8:11], v149 offset:1024\n"
        "v_mfma_f32_16x16x32_bf16 v[24:27], a[224:227], a[24:27], v[24:27]\n"
        "global_load_lds_dwordx4 v145, s[60:61] offset:1024\n"
        "ds_read_b128 a[12:15], v149 offset:9216\n"
        "v_mfma_f32_16x16x32_bf16 v[32:35], a[224:227], a[192:195], v[32:35]\n"
        "ds_read_b128 a[16:19], v149 offset:2048\n"
        "v_mfma_f32_16x16x32_bf16 v[32:35], a[232:235], a[196:199], v[32:35]\n"
        "ds_read_b128 a[20:23], v149 offset:10240\n"
        "v_mfma_f32_16x16x32_bf16 v[36:39], a[236:239], a[196:199], v[36:39]\n"
        "global_load_lds_dwordx4 v146, s[60:61] offset:2048\n"
        "ds_read_b128 a[24:27], v149 offset:3072\n"
        "v_mfma_f32_16x16x32_bf16 v[36:39], a[228:231], a[192:195], v[36:39]\n"
        "ds_read_b128 a[28:31], v149 offset:11264\n"
        "v_mfma_f32_16x16x32_bf16 v[44:47], a[228:231], a[200:203], v[44:47]\n"
        "ds_read_b128 a[192:195], v152 offset:0\n"
        "v_mfma_f32_16x16x32_bf16 v[44:47], a[236:239], a[204:207], v[44:47]\n"
        "global_load_lds_dwordx4 v147, s[60:61] offset:3072\n"
        "s_add_u32 m0, s73, 0x24000\n"
        "s_waitcnt lgkmcnt(7)\n"
        "v_mfma_f32_16x16x32_bf16 v[40:43], a[232:235], a[204:207], v[40:43]\n"
        "ds_read_b128 a[196:199], v152 offset:8192\n"
        "s_add_u32 s60, s22, 0x160000\n"
        "s_addc_u32 s61, s23, 0\n"
        "v_mfma_f32_16x16x32_bf16 v[40:43], a[224:227], a[200:203], v[40:43]\n"
        "ds_read_b128 a[200:203], v152 offset:1024\n"
        "v_mfma_f32_16x16x32_bf16 v[48:51], a[224:227], a[208:211], v[48:51]\n"
        "global_load_lds_dwordx4 v254, s[20:21] offset:0 sc1\n"
        "ds_read_b128 a[204:207], v152 offset:9216\n"
        "v_mfma_f32_16x16x32_bf16 v[48:51], a[232:235], a[212:215], v[48:51]\n"
        "s_waitcnt lgkmcnt(6)\n"
        "v_mfma_f32_16x16x32_bf16 v[52:55], a[236:239], a[212:215], v[52:55]\n"
        "ds_read_b128 a[212:215], v152 offset:10240\n"
        "v_mfma_f32_16x16x32_bf16 v[52:55], a[228:231], a[208:211], v[52:55]\n"
        "global_load_lds_dwordx4 v254, s[20:21] offset:1024 sc1\n"
        "ds_read_b128 a[208:211], v152 offset:2048\n"
        "v_mfma_f32_16x16x32_bf16 v[60:63], a[228:231], a[216:219], v[60:63]\n"
        "v_mfma_f32_16x16x32_bf16 v[60:63], a[236:239], a[220:223], v[60:63]\n"
        "v_mfma_f32_16x16x32_bf16 v[56:59], a[232:235], a[220:223], v[56:59]\n"
        "global_load_lds_dwordx4 v254, s[20:21] offset:2048 sc1\n"
        "ds_read_b128 a[220:223], v152 offset:11264\n"
        "v_mfma_f32_16x16x32_bf16 v[56:59], a[224:227], a[216:219], v[56:59]\n"
        "ds_read_b128 a[216:219], v152 offset:3072\n"
        "v_mfma_f32_16x16x32_bf16 v[0:3], v[192:195], a[0:3], v[0:3]\n"
        "v_mfma_f32_16x16x32_bf16 v[0:3], v[200:203], a[4:7], v[0:3]\n"
        "global_load_lds_dwordx4 v254, s[20:21] offset:3072 sc1\n"
        "s_add_u32 s20, s20, 0x4000\n"
        "s_addc_u32 s21, s21, 0\n"
        "v_mfma_f32_16x16x32_bf16 v[4:7], v[204:207], a[4:7], v[4:7]\n"
        "s_add_u32 m0, s73, 0x8000\n"
        "s_waitcnt lgkmcnt(3)\n"
        "v_mfma_f32_16x16x32_bf16 v[4:7], v[196:199], a[0:3], v[4:7]\n"
        "v_mfma_f32_16x16x32_bf16 v[12:15], v[196:199], a[8:11], v[12:15]\n"
        "v_mfma_f32_16x16x32_bf16 v[12:15], v[204:207], a[12:15], v[12:15]\n"
        "v_mfma_f32_16x16x32_bf16 v[8:11], v[200:203], a[12:15], v[8:11]\n"
        "v_mfma_f32_16x16x32_bf16 v[8:11], v[192:195], a[8:11], v[8:11]\n"
        "v_mfma_f32_16x16x32_bf16 v[16:19], v[192:195], a[16:19], v[16:19]\n"
        "v_mfma_f32_16x16x32_bf16 v[16:19], v[200:203], a[20:23], v[16:19]\n"
        "v_mfma_f32_16x16x32_bf16 v[20:23], v[204:207], a[20:23], v[20:23]\n"
        "v_mfma_f32_16x16x32_bf16 v[20:23], v[196:199], a[16:19], v[20:23]\n"
        "s_waitcnt vmcnt(24)\n"
        "s_barrier\n"
        "v_mfma_f32_16x16x32_bf16 v[28:31], v[196:199], a[24:27], v[28:31]\n"
        "global_load_lds_dwordx4 v254, s[20:21] offset:0 sc1\n"
        "ds_read_b64_tr_b16 a[240:241], v156 offset:16384\n"
        "v_mfma_f32_16x16x32_bf16 v[28:31], v[204:207], a[28:31], v[28:31]\n"
        "ds_read_b64_tr_b16 a[242:243], v156 offset:20480\n"
        "s_waitcnt lgkmcnt(2)\n"
        "v_mfma_f32_16x16x32_bf16 v[24:27], v[200:203], a[28:31], v[24:27]\n"
        "ds_read_b64_tr_b16 a[248:249], v156 offset:24576\n"
        "v_mfma_f32_16x16x32_bf16 v[24:27], v[192:195], a[24:27], v[24:27]\n"
        "ds_read_b64_tr_b16 a[250:251], v156 offset:28672\n"
        "global_load_lds_dwordx4 v254, s[20:21] offset:1024 sc1\n"
        "v_mfma_f32_16x16x32_bf16 v[32:35], v[192:195], a[192:195], v[32:35]\n"
        "ds_read_b64_tr_b16 a[244:245], v157 offset:16384\n"
        "v_mfma_f32_16x16x32_bf16 v[32:35], v[200:203], a[196:199], v[32:35]\n"
        "ds_read_b64_tr_b16 a[246:247], v157 offset:20480\n"
        "v_mfma_f32_16x16x32_bf16 v[36:39], v[204:207], a[196:199], v[36:39]\n"
        "ds_read_b64_tr_b16 a[252:253], v157 offset:24576\n"
        "global_load_lds_dwordx4 v254, s[20:21] offset:2048 sc1\n"
        "v_mfma_f32_16x16x32_bf16 v[36:39], v[196:199], a[192:195], v[36:39]\n"
        "ds_read_b64_tr_b16 a[254:255], v157 offset:28672\n"
        "v_mfma_f32_16x16x32_bf16 v[44:47], v[196:199], a[200:203], v[44:47]\n"
        "ds_read_b128 a[0:3], v148 offset:0\n"
        "v_mfma_f32_16x16x32_bf16 v[44:47], v[204:207], a[204:207], v[44:47]\n"
        "ds_read_b128 a[4:7], v148 offset:8192\n"
        "global_load_lds_dwordx4 v254, s[20:21] offset:3072 sc1\n"
        "s_add_u32 m0, s73, 0xc000\n"
        "v_mfma_f32_16x16x32_bf16 v[40:43], v[200:203], a[204:207], v[40:43]\n"
        "ds_read_b128 a[8:11], v148 offset:1024\n"
        "s_add_u32 s20, s20, 0x4000\n"
        "s_addc_u32 s21, s21, 0\n"
        "v_mfma_f32_16x16x32_bf16 v[40:43], v[192:195], a[200:203], v[40:43]\n"
        "ds_read_b128 a[12:15], v148 offset:9216\n"
        "v_mfma_f32_16x16x32_bf16 v[48:51], v[192:195], a[208:211], v[48:51]\n"
        "ds_read_b128 a[16:19], v148 offset:2048\n"
        "global_load_lds_dwordx4 v144, s[60:61] offset:0\n"
        "v_mfma_f32_16x16x32_bf16 v[48:51], v[200:203], a[212:215], v[48:51]\n"
        "ds_read_b128 a[20:23], v148 offset:10240\n"
        "v_mfma_f32_16x16x32_bf16 v[52:55], v[204:207], a[212:215], v[52:55]\n"
        "ds_read_b128 a[24:27], v148 offset:3072\n"
        "v_mfma_f32_16x16x32_bf16 v[52:55], v[196:199], a[208:211], v[52:55]\n"
        "ds_read_b128 a[28:31], v148 offset:11264\n"
        "global_load_lds_dwordx4 v145, s[60:61] offset:1024\n"
        "v_mfma_f32_16x16x32_bf16 v[60:63], v[196:199], a[216:219], v[60:63]\n"
        "ds_read_b128 a[192:195], v151 offset:0\n"
        "s_waitcnt lgkmcnt(8)\n"
        "v_mfma_f32_16x16x32_bf16 v[60:63], v[204:207], a[220:223], v[60:63]\n"
        "ds_read_b128 a[196:199], v151 offset:8192\n"
        "s_waitcnt lgkmcnt(8)\n"
        "v_mfma_f32_16x16x32_bf16 v[56:59], v[200:203], a[220:223], v[56:59]\n"
        "global_load_lds_dwordx4 v146, s[60:61] offset:2048\n"
        "v_mfma_f32_16x16x32_bf16 v[56:59], v[192:195], a[216:219], v[56:59]\n"
        "ds_read_b128 a[200:203], v151 offset:1024\n"
        "s_waitcnt lgkmcnt(7)\n"
        "v_mfma_f32_16x16x32_bf16 v[0:3], a[240:243], a[0:3], v[0:3]\n"
        "ds_read_b128 a[204:207], v151 offset:9216\n"
        "s_waitcnt lgkmcnt(7)\n"
        "v_mfma_f32_16x16x32_bf16 v[0:3], a[248:251], a[4:7], v[0:3]\n"
        "global_load_lds_dwordx4 v147, s[60:61] offset:3072\n"
        "s_add_u32 m0, s73, 0x10000\n"
        "v_mfma_f32_16x16x32_bf16 v[4:7], a[252:255], a[4:7], v[4:7]\n"
        "ds_read_b128 a[208:211], v151 offset:2048\n"
        "s_add_u32 s60, s22, 0x180000\n"
        "s_addc_u32 s61, s23, 0\n"
        "v_mfma_f32_16x16x32_bf16 v[4:7], a[244:247], a[0:3], v[4:7]\n"
        "ds_read_b128 a[212:215], v151 offset:10240\n"
        "s_waitcnt lgkmcnt(6)\n"
        "v_mfma_f32_16x16x32_bf16 v[12:15], a[244:247], a[8:11], v[12:15]\n"
        "ds_read_b128 a[216:219], v151 offset:3072\n"
        "v_mfma_f32_16x16x32_bf16 v[12:15], a[252:255], a[12:15], v[12:15]\n"
        "ds_read_b128 a[220:223], v151 offset:11264\n"
        "s_waitcnt lgkmcnt(6)\n"
        "v_mfma_f32_16x16x32_bf16 v[8:11], a[248:251], a[12:15], v[8:11]\n"
        "v_mfma_f32_16x16x32_bf16 v[8:11], a[240:243], a[8:11], v[8:11]\n"
        "v_mfma_f32_16x16x32_bf16 v[16:19], a[240:243], a[16:19], v[16:19]\n"
        "v_mfma_f32_16x16x32_bf16 v[16:19], a[248:251], a[20:23], v[16:19]\n"
        "s_waitcnt lgkmcnt(4)\n"
        "v_mfma_f32_16x16x32_bf16 v[20:23], a[252:255], a[20:23], v[20:23]\n"
        "v_mfma_f32_16x16x32_bf16 v[20:23], a[244:247], a[16:19], v[20:23]\n"
        "s_waitcnt vmcnt(20)\n"
        "s_barrier\n"
        "v_mfma_f32_16x16x32_bf16 v[28:31], a[244:247], a[24:27], v[28:31]\n"
        "global_load_lds_dwordx4 v254, s[20:21] offset:0 sc1\n"
        "ds_read_b64_tr_b16 a[32:33], v154 offset:16384\n"
        "v_mfma_f32_16x16x32_bf16 v[28:31], a[252:255], a[28:31], v[28:31]\n"
        "ds_read_b64_tr_b16 a[34:35], v154 offset:20480\n"
        "s_waitcnt lgkmcnt(2)\n"
        "v_mfma_f32_16x16x32_bf16 v[24:27], a[248:251], a[28:31], v[24:27]\n"
        "ds_read_b64_tr_b16 a[40:41], v154 offset:24576\n"
        "v_mfma_f32_16x16x32_bf16 v[24:27], a[240:243], a[24:27], v[24:27]\n"
        "ds_read_b64_tr_b16 a[42:43], v154 offset:28672\n"
        "global_load_lds_dwordx4 v254, s[20:21] offset:1024 sc1\n"
        "v_mfma_f32_16x16x32_bf16 v[32:35], a[240:243], a[192:195], v[32:35]\n"
        "ds_read_b64_tr_b16 a[36:37], v155 offset:16384\n"
        "v_mfma_f32_16x16x32_bf16 v[32:35], a[248:251], a[196:199], v[32:35]\n"
        "ds_read_b64_tr_b16 a[38:39], v155 offset:20480\n"
        "v_mfma_f32_16x16x32_bf16 v[36:39], a[252:255], a[196:199], v[36:39]\n"
        "ds_read_b64_tr_b16 a[44:45], v155 offset:24576\n"
        "global_load_lds_dwordx4 v254, s[20:21] offset:2048 sc1\n"
        "v_mfma_f32_16x16x32_bf16 v[36:39], a[244:247], a[192:195], v[36:39]\n"
        "ds_read_b64_tr_b16 a[46:47], v155 offset:28672\n"
        "v_mfma_f32_16x16x32_bf16 v[44:47], a[244:247], a[200:203], v[44:47]\n"
        "ds_read_b128 a[0:3], v149 offset:32768\n"
        "v_mfma_f32_16x16x32_bf16 v[44:47], a[252:255], a[204:207], v[44:47]\n"
        "ds_read_b128 a[4:7], v149 offset:40960\n"
        "global_load_lds_dwordx4 v254, s[20:21] offset:3072 sc1\n"
        "s_add_u32 m0, s73, 0x14000\n"
        "v_mfma_f32_16x16x32_bf16 v[40:43], a[248:251], a[204:207], v[40:43]\n"
        "ds_read_b128 a[8:11], v149 offset:33792\n"
        "s_add_u32 s20, s20, 0x4000\n"
        "s_addc_u32 s21, s21, 0\n"
        "v_mfma_f32_16x16x32_bf16 v[40:43], a[240:243], a[200:203], v[40:43]\n"
        "ds_read_b128 a[12:15], v149 offset:41984\n"
        "v_mfma_f32_16x16x32_bf16 v[48:51], a[240:243], a[208:211], v[48:51]\n"
        "ds_read_b128 a[16:19], v149 offset:34816\n"
        "v_mfma_f32_16x16x32_bf16 v[48:51], a[248:251], a[212:215], v[48:51]\n"
        "ds_read_b128 a[20:23], v149 offset:43008\n"
        "v_mfma_f32_16x16x32_bf16 v[52:55], a[252:255], a[212:215], v[52:55]\n"
        "ds_read_b128 a[24:27], v149 offset:35840\n"
        "v_mfma_f32_16x16x32_bf16 v[52:55], a[244:247], a[208:211], v[52:55]\n"
        "ds_read_b128 a[28:31], v149 offset:44032\n"
        "s_waitcnt lgkmcnt(8)\n"
        "v_mfma_f32_16x16x32_bf16 v[60:63], a[244:247], a[216:219], v[60:63]\n"
        "ds_read_b128 a[192:195], v152 offset:32768\n"
        "s_waitcnt lgkmcnt(8)\n"
        "v_mfma_f32_16x16x32_bf16 v[60:63], a[252:255], a[220:223], v[60:63]\n"
        "ds_read_b128 a[196:199], v152 offset:40960\n"
        "s_waitcnt lgkmcnt(8)\n"
        "v_mfma_f32_16x16x32_bf16 v[56:59], a[248:251], a[220:223], v[56:59]\n"
        "ds_read_b128 a[200:203], v152 offset:33792\n"
        "s_waitcnt lgkmcnt(8)\n"
        "v_mfma_f32_16x16x32_bf16 v[56:59], a[240:243], a[216:219], v[56:59]\n"
        "ds_read_b128 a[204:207], v152 offset:41984\n"
        "s_waitcnt lgkmcnt(8)\n"
        "v_mfma_f32_16x16x32_bf16 v[0:3], a[32:35], a[0:3], v[0:3]\n"
        "ds_read_b128 a[208:211], v152 offset:34816\n"
        "s_waitcnt lgkmcnt(8)\n"
        "v_mfma_f32_16x16x32_bf16 v[0:3], a[40:43], a[4:7], v[0:3]\n"
        "ds_read_b128 a[212:215], v152 offset:43008\n"
        "s_waitcnt lgkmcnt(8)\n"
        "v_mfma_f32_16x16x32_bf16 v[4:7], a[44:47], a[4:7], v[4:7]\n"
        "ds_read_b128 a[216:219], v152 offset:35840\n"
        "s_waitcnt lgkmcnt(8)\n"
        "v_mfma_f32_16x16x32_bf16 v[4:7], a[36:39], a[0:3], v[4:7]\n"
        "ds_read_b128 a[220:223], v152 offset:44032\n"
        "s_waitcnt lgkmcnt(8)\n"
        "v_mfma_f32_16x16x32_bf16 v[12:15], a[36:39], a[8:11], v[12:15]\n"
        "v_mfma_f32_16x16x32_bf16 v[12:15], a[44:47], a[12:15], v[12:15]\n"
        "s_waitcnt lgkmcnt(6)\n"
        "v_mfma_f32_16x16x32_bf16 v[8:11], a[40:43], a[12:15], v[8:11]\n"
        "v_mfma_f32_16x16x32_bf16 v[8:11], a[32:35], a[8:11], v[8:11]\n"
        "v_mfma_f32_16x16x32_bf16 v[16:19], a[32:35], a[16:19], v[16:19]\n"
        "v_mfma_f32_16x16x32_bf16 v[16:19], a[40:43], a[20:23], v[16:19]\n"
        "s_waitcnt lgkmcnt(2)\n"
        "v_mfma_f32_16x16x32_bf16 v[20:23], a[44:47], a[20:23], v[20:23]\n"
        "v_mfma_f32_16x16x32_bf16 v[20:23], a[36:39], a[16:19], v[20:23]\n"
        "s_waitcnt vmcnt(20)\n"
        "s_barrier\n"
        "v_mfma_f32_16x16x32_bf16 v[28:31], a[36:39], a[24:27], v[28:31]\n"
        "global_load_lds_dwordx4 v144, s[60:61] offset:0\n"
        "ds_read_b128 a[0:3], v149 offset:49152\n"
        "v_mfma_f32_16x16x32_bf16 v[28:31], a[44:47], a[28:31], v[28:31]\n"
        "ds_read_b128 a[4:7], v149 offset:57344\n"
        "v_mfma_f32_16x16x32_bf16 v[24:27], a[40:43], a[28:31], v[24:27]\n"
        "ds_read_b128 a[8:11], v149 offset:50176\n"
        "v_mfma_f32_16x16x32_bf16 v[24:27], a[32:35], a[24:27], v[24:27]\n"
        "global_load_lds_dwordx4 v145, s[60:61] offset:1024\n"
        "ds_read_b128 a[12:15], v149 offset:58368\n"
        "v_mfma_f32_16x16x32_bf16 v[32:35], a[32:35], a[192:195], v[32:35]\n"
        "ds_read_b128 a[16:19], v149 offset:51200\n"
        "v_mfma_f32_16x16x32_bf16 v[32:35], a[40:43], a[196:199], v[32:35]\n"
        "ds_read_b128 a[20:23], v149 offset:59392\n"
        "s_waitcnt lgkmcnt(6)\n"
        "v_mfma_f32_16x16x32_bf16 v[36:39], a[44:47], a[196:199], v[36:39]\n"
        "global_load_lds_dwordx4 v146, s[60:61] offset:2048\n"
        "ds_read_b128 a[24:27], v149 offset:52224\n"
        "v_mfma_f32_16x16x32_bf16 v[36:39], a[36:39], a[192:195], v[36:39]\n"
        "ds_read_b128 a[28:31], v149 offset:60416\n"
        "v_mfma_f32_16x16x32_bf16 v[44:47], a[36:39], a[200:203], v[44:47]\n"
        "ds_read_b128 a[192:195], v152 offset:49152\n"
        "v_mfma_f32_16x16x32_bf16 v[44:47], a[44:47], a[204:207], v[44:47]\n"
        "global_load_lds_dwordx4 v147, s[60:61] offset:3072\n"
        "s_add_u32 m0, s73, 0x0\n"
        "s_waitcnt lgkmcnt(7)\n"
        "v_mfma_f32_16x16x32_bf16 v[40:43], a[40:43], a[204:207], v[40:43]\n"
        "ds_read_b128 a[196:199], v152 offset:57344\n"
        "s_add_u32 s60, s22, 0x1c0000\n"
        "s_addc_u32 s61, s23, 0\n"
        "v_mfma_f32_16x16x32_bf16 v[40:43], a[32:35], a[200:203], v[40:43]\n"
        "ds_read_b128 a[200:203], v152 offset:50176\n"
        "v_mfma_f32_16x16x32_bf16 v[48:51], a[32:35], a[208:211], v[48:51]\n"
        "global_load_lds_dwordx4 v254, s[20:21] offset:0 sc1\n"
        "ds_read_b128 a[204:207], v152 offset:58368\n"
        "v_mfma_f32_16x16x32_bf16 v[48:51], a[40:43], a[212:215], v[48:51]\n"
        "s_waitcnt lgkmcnt(6)\n"
        "v_mfma_f32_16x16x32_bf16 v[52:55], a[44:47], a[212:215], v[52:55]\n"
        "ds_read_b128 a[212:215], v152 offset:59392\n"
        "v_mfma_f32_16x16x32_bf16 v[52:55], a[36:39], a[208:211], v[52:55]\n"
        "global_load_lds_dwordx4 v254, s[20:21] offset:1024 sc1\n"
        "ds_read_b128 a[208:211], v152 offset:51200\n"
        "v_mfma_f32_16x16x32_bf16 v[60:63], a[36:39], a[216:219], v[60:63]\n"
        "v_mfma_f32_16x16x32_bf16 v[60:63], a[44:47], a[220:223], v[60:63]\n"
        "v_mfma_f32_16x16x32_bf16 v[56:59], a[40:43], a[220:223], v[56:59]\n"
        "global_load_lds_dwordx4 v254, s[20:21] offset:2048 sc1\n"
        "ds_read_b128 a[220:223], v152 offset:60416\n"
        "v_mfma_f32_16x16x32_bf16 v[56:59], a[32:35], a[216:219], v[56:59]\n"
        "ds_read_b128 a[216:219], v152 offset:52224\n"
        "v_mfma_f32_16x16x32_bf16 v[0:3], v[208:211], a[0:3], v[0:3]\n"
        "v_mfma_f32_16x16x32_bf16 v[0:3], v[216:219], a[4:7], v[0:3]\n"
        "global_load_lds_dwordx4 v254, s[20:21] offset:3072 sc1\n"
        "s_add_u32 s20, s20, 0x4000\n"
        "s_addc_u32 s21, s21, 0\n"
        "v_mfma_f32_16x16x32_bf16 v[4:7], v[220:223], a[4:7], v[4:7]\n"
        "s_add_u32 m0, s73, 0x4000\n"
        "s_waitcnt lgkmcnt(3)\n"
        "v_mfma_f32_16x16x32_bf16 v[4:7], v[212:215], a[0:3], v[4:7]\n"
        "v_mfma_f32_16x16x32_bf16 v[12:15], v[212:215], a[8:11], v[12:15]\n"
        "v_mfma_f32_16x16x32_bf16 v[12:15], v[220:223], a[12:15], v[12:15]\n"
        "v_mfma_f32_16x16x32_bf16 v[8:11], v[216:219], a[12:15], v[8:11]\n"
        "v_mfma_f32_16x16x32_bf16 v[8:11], v[208:211], a[8:11], v[8:11]\n"
        "v_mfma_f32_16x16x32_bf16 v[16:19], v[208:211], a[16:19], v[16:19]\n"
        "v_mfma_f32_16x16x32_bf16 v[16:19], v[216:219], a[20:23], v[16:19]\n"
        "v_mfma_f32_16x16x32_bf16 v[20:23], v[220:223], a[20:23], v[20:23]\n"
        "LHB3_%=:\n"
        "s_cbranch_execz LHRB_%=\n"
        "v_mfma_f32_16x16x32_bf16 v[20:23], v[212:215], a[16:19], v[20:23]\n"
        "s_waitcnt vmcnt(20)\n"
        "s_barrier\n"
        "v_mfma_f32_16x16x32_bf16 v[28:31], v[212:215], a[24:27], v[28:31]\n"
        "global_load_lds_dwordx4 v254, s[20:21] offset:0 sc1\n"
        "ds_read_b64_tr_b16 a[48:49], v158 offset:0\n"
        "v_mfma_f32_16x16x32_bf16 v[28:31], v[220:223], a[28:31], v[28:31]\n"
        "ds_read_b64_tr_b16 a[50:51], v158 offset:4096\n"
        "s_waitcnt lgkmcnt(2)\n"
        "v_mfma_f32_16x16x32_bf16 v[24:27], v[216:219], a[28:31], v[24:27]\n"
        "ds_read_b64_tr_b16 a[56:57], v158 offset:8192\n"
        "v_mfma_f32_16x16x32_bf16 v[24:27], v[208:211], a[24:27], v[24:27]\n"
        "ds_read_b64_tr_b16 a[58:59], v158 offset:12288\n"
        "global_load_lds_dwordx4 v254, s[20:21] offset:1024 sc1\n"
        "v_mfma_f32_16x16x32_bf16 v[32:35], v[208:211], a[192:195], v[32:35]\n"
        "ds_read_b64_tr_b16 a[52:53], v159 offset:0\n"
        "v_mfma_f32_16x16x32_bf16 v[32:35], v[216:219], a[196:199], v[32:35]\n"
        "ds_read_b64_tr_b16 a[54:55], v159 offset:4096\n"
        "v_mfma_f32_16x16x32_bf16 v[36:39], v[220:223], a[196:199], v[36:39]\n"
        "ds_read_b64_tr_b16 a[60:61], v159 offset:8192\n"
        "global_load_lds_dwordx4 v254, s[20:21] offset:2048 sc1\n"
        "v_mfma_f32_16x16x32_bf16 v[36:39], v[212:215], a[192:195], v[36:39]\n"
        "ds_read_b64_tr_b16 a[62:63], v159 offset:12288\n"
        "v_mfma_f32_16x16x32_bf16 v[44:47], v[212:215], a[200:203], v[44:47]\n"
        "ds_read_b128 a[0:3], v150 offset:16384\n"
        "v_mfma_f32_16x16x32_bf16 v[44:47], v[220:223], a[204:207], v[44:47]\n"
        "ds_read_b128 a[4:7], v150 offset:24576\n"
        "global_load_lds_dwordx4 v254, s[20:21] offset:3072 sc1\n"
        "s_add_u32 m0, s73, 0x18000\n"
        "v_mfma_f32_16x16x32_bf16 v[40:43], v[216:219], a[204:207], v[40:43]\n"
        "ds_read_b128 a[8:11], v150 offset:17408\n"
        "s_add_u32 s20, s20, 0x4000\n"
        "s_addc_u32 s21, s21, 0\n"
        "v_mfma_f32_16x16x32_bf16 v[40:43], v[208:211], a[200:203], v[40:43]\n"
        "ds_read_b128 a[12:15], v150 offset:25600\n"
        "v_mfma_f32_16x16x32_bf16 v[48:51], v[208:211], a[208:211], v[48:51]\n"
        "ds_read_b128 a[16:19], v150 offset:18432\n"
        "global_load_lds_dwordx4 v144, s[60:61] offset:0\n"
        "v_mfma_f32_16x16x32_bf16 v[48:51], v[216:219], a[212:215], v[48:51]\n"
        "ds_read_b128 a[20:23], v150 offset:26624\n"
        "v_mfma_f32_16x16x32_bf16 v[52:55], v[220:223], a[212:215], v[52:55]\n"
        "ds_read_b128 a[24:27], v150 offset:19456\n"
        "v_mfma_f32_16x16x32_bf16 v[52:55], v[212:215], a[208:211], v[52:55]\n"
        "ds_read_b128 a[28:31], v150 offset:27648\n"
        "global_load_lds_dwordx4 v145, s[60:61] offset:1024\n"
        "v_mfma_f32_16x16x32_bf16 v[60:63], v[212:215], a[216:219], v[60:63]\n"
        "ds_read_b128 a[192:195], v153 offset:16384\n"
        "s_waitcnt lgkmcnt(8)\n"
        "v_mfma_f32_16x16x32_bf16 v[60:63], v[220:223], a[220:223], v[60:63]\n"
        "ds_read_b128 a[196:199], v153 offset:24576\n"
        "s_waitcnt lgkmcnt(8)\n"
        "v_mfma_f32_16x16x32_bf16 v[56:59], v[216:219], a[220:223], v[56:59]\n"
        "global_load_lds_dwordx4 v146, s[60:61] offset:2048\n"
        "v_mfma_f32_16x16x32_bf16 v[56:59], v[208:211], a[216:219], v[56:59]\n"
        "ds_read_b128 a[200:203], v153 offset:17408\n"
        "s_waitcnt lgkmcnt(7)\n"
        "v_mfma_f32_16x16x32_bf16 v[0:3], a[48:51], a[0:3], v[0:3]\n"
        "ds_read_b128 a[204:207], v153 offset:25600\n"
        "s_waitcnt lgkmcnt(7)\n"
        "v_mfma_f32_16x16x32_bf16 v[0:3], a[56:59], a[4:7], v[0:3]\n"
        "global_load_lds_dwordx4 v147, s[60:61] offset:3072\n"
        "s_add_u32 m0, s73, 0x1c000\n"
        "v_mfma_f32_16x16x32_bf16 v[4:7], a[60:63], a[4:7], v[4:7]\n"
        "ds_read_b128 a[208:211], v153 offset:18432\n"
        "s_add_u32 s60, s22, 0x1e0000\n"
        "s_addc_u32 s61, s23, 0\n"
        "v_mfma_f32_16x16x32_bf16 v[4:7], a[52:55], a[0:3], v[4:7]\n"
        "ds_read_b128 a[212:215], v153 offset:26624\n"
        "s_waitcnt lgkmcnt(6)\n"
        "v_mfma_f32_16x16x32_bf16 v[12:15], a[52:55], a[8:11], v[12:15]\n"
        "ds_read_b128 a[216:219], v153 offset:19456\n"
        "v_mfma_f32_16x16x32_bf16 v[12:15], a[60:63], a[12:15], v[12:15]\n"
        "ds_read_b128 a[220:223], v153 offset:27648\n"
        "s_waitcnt lgkmcnt(6)\n"
        "v_mfma_f32_16x16x32_bf16 v[8:11], a[56:59], a[12:15], v[8:11]\n"
        "v_mfma_f32_16x16x32_bf16 v[8:11], a[48:51], a[8:11], v[8:11]\n"
        "v_mfma_f32_16x16x32_bf16 v[16:19], a[48:51], a[16:19], v[16:19]\n"
        "v_mfma_f32_16x16x32_bf16 v[16:19], a[56:59], a[20:23], v[16:19]\n"
        "s_waitcnt lgkmcnt(4)\n"
        "v_mfma_f32_16x16x32_bf16 v[20:23], a[60:63], a[20:23], v[20:23]\n"
        "v_mfma_f32_16x16x32_bf16 v[20:23], a[52:55], a[16:19], v[20:23]\n"
        "s_waitcnt vmcnt(24)\n"
        "s_barrier\n"
        "v_mfma_f32_16x16x32_bf16 v[28:31], a[52:55], a[24:27], v[28:31]\n"
        "global_load_lds_dwordx4 v254, s[20:21] offset:0 sc1\n"
        "ds_read_b128 a[0:3], v148 offset:32768\n"
        "v_mfma_f32_16x16x32_bf16 v[28:31], a[60:63], a[28:31], v[28:31]\n"
        "ds_read_b128 a[4:7], v148 offset:40960\n"
        "s_waitcnt lgkmcnt(2)\n"
        "v_mfma_f32_16x16x32_bf16 v[24:27], a[56:59], a[28:31], v[24:27]\n"
        "ds_read_b128 a[8:11], v148 offset:33792\n"
        "v_mfma_f32_16x16x32_bf16 v[24:27], a[48:51], a[24:27], v[24:27]\n"
        "global_load_lds_dwordx4 v254, s[20:21] offset:1024 sc1\n"
        "ds_read_b128 a[12:15], v148 offset:41984\n"
        "v_mfma_f32_16x16x32_bf16 v[32:35], a[48:51], a[192:195], v[32:35]\n"
        "ds_read_b128 a[16:19], v148 offset:34816\n"
        "v_mfma_f32_16x16x32_bf16 v[32:35], a[56:59], a[196:199], v[32:35]\n"
        "ds_read_b128 a[20:23], v148 offset:43008\n"
        "v_mfma_f32_16x16x32_bf16 v[36:39], a[60:63], a[196:199], v[36:39]\n"
        "global_load_lds_dwordx4 v254, s[20:21] offset:2048 sc1\n"
        "ds_read_b128 a[24:27], v148 offset:35840\n"
        "v_mfma_f32_16x16x32_bf16 v[36:39], a[52:55], a[192:195], v[36:39]\n"
        "ds_read_b128 a[28:31], v148 offset:44032\n"
        "v_mfma_f32_16x16x32_bf16 v[44:47], a[52:55], a[200:203], v[44:47]\n"
        "ds_read_b128 a[192:195], v151 offset:32768\n"
        "v_mfma_f32_16x16x32_bf16 v[44:47], a[60:63], a[204:207], v[44:47]\n"
        "global_load_lds_dwordx4 v254, s[20:21] offset:3072 sc1\n"
        "s_add_u32 s20, s20, 0x4000\n"
        "s_addc_u32 s21, s21, 0\n"
        "v_mfma_f32_16x16x32_bf16 v[40:43], a[56:59], a[204:207], v[40:43]\n"
        "ds_read_b128 a[196:199], v151 offset:40960\n"
        "s_add_u32 m0, s73, 0x20000\n"
        "s_waitcnt lgkmcnt(7)\n"
        "v_mfma_f32_16x16x32_bf16 v[40:43], a[48:51], a[200:203], v[40:43]\n"
        "ds_read_b128 a[200:203], v151 offset:33792\n"
        "v_mfma_f32_16x16x32_bf16 v[48:51], a[48:51], a[208:211], v[48:51]\n"
        "s_cmp_lg_u32 s40, 0\n"
        "s_cbranch_scc1 LW2_%=\n"
        "s_mov_b64 exec, 0xffff\n"
        "global_store_dwordx2 v234, v[250:251], s[48:49] sc1\n"
        "s_mov_b64 exec, -1\n"
        "LW2_%=:\n"
        "v_mfma_f32_16x16x32_bf16 v[48:51], a[56:59], a[212:215], v[48:51]\n"
        "ds_read_b128 a[204:207], v151 offset:41984\n"
        "v_bfe_u32 v234, v254, 4, 3\n"
        "v_mfma_f32_16x16x32_bf16 v[52:55], a[60:63], a[212:215], v[52:55]\n"
        "ds_read_b128 a[212:215], v151 offset:43008\n"
        "v_lshl_add_u32 v234, v234, 7, s14\n"
        "v_mfma_f32_16x16x32_bf16 v[52:55], a[52:55], a[208:211], v[52:55]\n"
        "ds_read_b128 a[208:211], v151 offset:34816\n"
        "v_cmp_eq_u32 s[14:15], v247, v248\n"
        "v_mfma_f32_16x16x32_bf16 v[60:63], a[52:55], a[216:219], v[60:63]\n"
        "s_waitcnt lgkmcnt(5)\n"
        "v_cndmask_b32 v138, 0, v246, s[14:15]\n"
        "v_cmp_eq_u32 s[14:15], v247, v249\n"
        "v_mfma_f32_16x16x32_bf16 v[60:63], a[60:63], a[220:223], v[60:63]\n"
        "v_mfma_f32_16x16x32_bf16 v[56:59], a[56:59], a[220:223], v[56:59]\n"
        "ds_read_b128 a[220:223], v151 offset:44032\n"
        "v_cndmask_b32 v139, 0, v246, s[14:15]\n"
        "v_mfma_f32_16x16x32_bf16 v[56:59], a[48:51], a[216:219], v[56:59]\n"
        "ds_read_b128 a[216:219], v151 offset:35840\n"
        "s_lshl2_add_u32 s14, s13, s40\n"
        "s_add_u32 s14, s14, 3072\n"
        "v_mfma_f32_16x16x32_bf16 v[0:3], a[64:67], a[0:3], v[0:3]\n"
        "s_lshl_b32 s14, s14, 7\n"
        "v_mfma_f32_16x16x32_bf16 v[0:3], a[72:75], a[4:7], v[0:3]\n"
        "v_mfma_f32_16x16x32_bf16 v[4:7], a[76:79], a[4:7], v[4:7]\n"
        "v_mfma_f32_16x16x32_bf16 v[4:7], a[68:71], a[0:3], v[4:7]\n"
        "v_mfma_f32_16x16x32_bf16 v[12:15], a[68:71], a[8:11], v[12:15]\n"
        "v_mfma_f32_16x16x32_bf16 v[12:15], a[76:79], a[12:15], v[12:15]\n"
        "v_mfma_f32_16x16x32_bf16 v[8:11], a[72:75], a[12:15], v[8:11]\n"
        "v_mfma_f32_16x16x32_bf16 v[8:11], a[64:67], a[8:11], v[8:11]\n"
        "s_waitcnt lgkmcnt(0)\n"
        "v_mfma_f32_16x16x32_bf16 v[16:19], a[64:67], a[16:19], v[16:19]\n"
        "v_mfma_f32_16x16x32_bf16 v[16:19], a[72:75], a[20:23], v[16:19]\n"
        "v_mfma_f32_16x16x32_bf16 v[20:23], a[76:79], a[20:23], v[20:23]\n"
        "v_mfma_f32_16x16x32_bf16 v[20:23], a[68:71], a[16:19], v[20:23]\n"
        "s_waitcnt vmcnt(20)\n"
        "s_barrier\n"
        "v_mfma_f32_16x16x32_bf16 v[28:31], a[68:71], a[24:27], v[28:31]\n"
        "global_load_lds_dwordx4 v144, s[60:61] offset:0\n"
        "ds_read_b64_tr_b16 a[224:225], v154 offset:49152\n"
        "v_mfma_f32_16x16x32_bf16 v[28:31], a[76:79], a[28:31], v[28:31]\n"
        "ds_read_b64_tr_b16 a[226:227], v154 offset:53248\n"
        "v_mfma_f32_16x16x32_bf16 v[24:27], a[72:75], a[28:31], v[24:27]\n"
        "ds_read_b64_tr_b16 a[232:233], v154 offset:57344\n"
        "v_mfma_f32_16x16x32_bf16 v[24:27], a[64:67], a[24:27], v[24:27]\n"
        "ds_read_b64_tr_b16 a[234:235], v154 offset:61440\n"
        "global_load_lds_dwordx4 v145, s[60:61] offset:1024\n"
        "v_mfma_f32_16x16x32_bf16 v[32:35], a[64:67], a[192:195], v[32:35]\n"
        "ds_read_b64_tr_b16 a[228:229], v155 offset:49152\n"
        "v_mfma_f32_16x16x32_bf16 v[32:35], a[72:75], a[196:199], v[32:35]\n"
        "ds_read_b64_tr_b16 a[230:231], v155 offset:53248\n"
        "v_mfma_f32_16x16x32_bf16 v[36:39], a[76:79], a[196:199], v[36:39]\n"
        "ds_read_b64_tr_b16 a[236:237], v155 offset:57344\n"
        "global_load_lds_dwordx4 v146, s[60:61] offset:2048\n"
        "v_mfma_f32_16x16x32_bf16 v[36:39], a[68:71], a[192:195], v[36:39]\n"
        "ds_read_b64_tr_b16 a[238:239], v155 offset:61440\n"
        "v_mfma_f32_16x16x32_bf16 v[44:47], a[68:71], a[200:203], v[44:47]\n"
        "ds_read_b128 a[0:3], v149 offset:0\n"
        "v_mfma_f32_16x16x32_bf16 v[44:47], a[76:79], a[204:207], v[44:47]\n"
        "ds_read_b128 a[4:7], v149 offset:8192\n"
        "global_load_lds_dwordx4 v147, s[60:61] offset:3072\n"
        "s_add_u32 m0, s73, 0x24000\n"
        "v_mfma_f32_16x16x32_bf16 v[40:43], a[72:75], a[204:207], v[40:43]\n"
        "ds_read_b128 a[8:11], v149 offset:1024\n"
        "s_add_u32 s60, s22, 0x220000\n"
        "s_addc_u32 s61, s23, 0\n"
        "v_mfma_f32_16x16x32_bf16 v[40:43], a[64:67], a[200:203], v[40:43]\n"
        "ds_read_b128 a[12:15], v149 offset:9216\n"
        "v_mfma_f32_16x16x32_bf16 v[48:51], a[64:67], a[208:211], v[48:51]\n"
        "ds_read_b128 a[16:19], v149 offset:2048\n"
        "global_load_lds_dwordx4 v254, s[20:21] offset:0 sc1\n"
        "v_mfma_f32_16x16x32_bf16 v[48:51], a[72:75], a[212:215], v[48:51]\n"
        "ds_read_b128 a[20:23], v149 offset:10240\n"
        "v_mfma_f32_16x16x32_bf16 v[52:55], a[76:79], a[212:215], v[52:55]\n"
        "ds_read_b128 a[24:27], v149 offset:3072\n"
        "v_mfma_f32_16x16x32_bf16 v[52:55], a[68:71], a[208:211], v[52:55]\n"
        "ds_read_b128 a[28:31], v149 offset:11264\n"
        "global_load_lds_dwordx4 v254, s[20:21] offset:1024 sc1\n"
        "v_mfma_f32_16x16x32_bf16 v[60:63], a[68:71], a[216:219], v[60:63]\n"
        "ds_read_b128 a[192:195], v152 offset:0\n"
        "s_waitcnt lgkmcnt(8)\n"
        "v_mfma_f32_16x16x32_bf16 v[60:63], a[76:79], a[220:223], v[60:63]\n"
        "ds_read_b128 a[196:199], v152 offset:8192\n"
        "s_waitcnt lgkmcnt(8)\n"
        "v_mfma_f32_16x16x32_bf16 v[56:59], a[72:75], a[220:223], v[56:59]\n"
        "global_load_lds_dwordx4 v254, s[20:21] offset:2048 sc1\n"
        "v_mfma_f32_16x16x32_bf16 v[56:59], a[64:67], a[216:219], v[56:59]\n"
        "ds_read_b128 a[200:203], v152 offset:1024\n"
        "s_waitcnt lgkmcnt(7)\n"
        "v_mfma_f32_16x16x32_bf16 v[0:3], a[224:227], a[0:3], v[0:3]\n"
        "ds_read_b128 a[204:207], v152 offset:9216\n"
        "s_waitcnt lgkmcnt(7)\n"
        "v_mfma_f32_16x16x32_bf16 v[0:3], a[232:235], a[4:7], v[0:3]\n"
        "global_load_lds_dwordx4 v254, s[20:21] offset:3072 sc1\n"
        "s_add_u32 m0, s73, 0x8000\n"
        "v_mfma_f32_16x16x32_bf16 v[4:7], a[236:239], a[4:7], v[4:7]\n"
        "ds_read_b128 a[208:211], v152 offset:2048\n"
        "s_add_u32 s20, s20, 0x4000\n"
        "s_addc_u32 s21, s21, 0\n"
        "v_mfma_f32_16x16x32_bf16 v[4:7], a[228:231], a[0:3], v[4:7]\n"
        "ds_read_b128 a[212:215], v152 offset:10240\n"
        "s_waitcnt lgkmcnt(6)\n"
        "v_mfma_f32_16x16x32_bf16 v[12:15], a[228:231], a[8:11], v[12:15]\n"
        "ds_read_b128 a[216:219], v152 offset:3072\n"
        "v_mfma_f32_16x16x32_bf16 v[12:15], a[236:239], a[12:15], v[12:15]\n"
        "ds_read_b128 a[220:223], v152 offset:11264\n"
        "s_waitcnt lgkmcnt(6)\n"
        "v_mfma_f32_16x16x32_bf16 v[8:11], a[232:235], a[12:15], v[8:11]\n"
        "v_mfma_f32_16x16x32_bf16 v[8:11], a[224:227], a[8:11], v[8:11]\n"
        "v_mfma_f32_16x16x32_bf16 v[16:19], a[224:227], a[16:19], v[16:19]\n"
        "v_mfma_f32_16x16x32_bf16 v[16:19], a[232:235], a[20:23], v[16:19]\n"
        "s_waitcnt lgkmcnt(4)\n"
        "v_mfma_f32_16x16x32_bf16 v[20:23], a[236:239], a[20:23], v[20:23]\n"
        "v_mfma_f32_16x16x32_bf16 v[20:23], a[228:231], a[16:19], v[20:23]\n"
        "s_waitcnt vmcnt(20)\n"
        "s_barrier\n"
        "v_mfma_f32_16x16x32_bf16 v[28:31], a[228:231], a[24:27], v[28:31]\n"
        "global_load_lds_dwordx4 v254, s[20:21] offset:0 sc1\n"
        "ds_read_b64_tr_b16 a[240:241], v156 offset:16384\n"
        "v_mfma_f32_16x16x32_bf16 v[28:31], a[236:239], a[28:31], v[28:31]\n"
        "ds_read_b64_tr_b16 a[242:243], v156 offset:20480\n"
        "s_waitcnt lgkmcnt(2)\n"
        "v_mfma_f32_16x16x32_bf16 v[24:27], a[232:235], a[28:31], v[24:27]\n"
        "ds_read_b64_tr_b16 a[248:249], v156 offset:24576\n"
        "v_mfma_f32_16x16x32_bf16 v[24:27], a[224:227], a[24:27], v[24:27]\n"
        "ds_read_b64_tr_b16 a[250:251], v156 offset:28672\n"
        "global_load_lds_dwordx4 v254, s[20:21] offset:1024 sc1\n"
        "v_mfma_f32_16x16x32_bf16 v[32:35], a[224:227], a[192:195], v[32:35]\n"
        "ds_read_b64_tr_b16 a[244:245], v157 offset:16384\n"
        "v_mfma_f32_16x16x32_bf16 v[32:35], a[232:235], a[196:199], v[32:35]\n"
        "ds_read_b64_tr_b16 a[246:247], v157 offset:20480\n"
        "v_mfma_f32_16x16x32_bf16 v[36:39], a[236:239], a[196:199], v[36:39]\n"
        "ds_read_b64_tr_b16 a[252:253], v157 offset:24576\n"
        "global_load_lds_dwordx4 v254, s[20:21] offset:2048 sc1\n"
        "v_mfma_f32_16x16x32_bf16 v[36:39], a[228:231], a[192:195], v[36:39]\n"
        "ds_read_b64_tr_b16 a[254:255], v157 offset:28672\n"
        "v_mfma_f32_16x16x32_bf16 v[44:47], a[228:231], a[200:203], v[44:47]\n"
        "ds_read_b128 a[0:3], v148 offset:0\n"
        "v_mfma_f32_16x16x32_bf16 v[44:47], a[236:239], a[204:207], v[44:47]\n"
        "ds_read_b128 a[4:7], v148 offset:8192\n"
        "global_load_lds_dwordx4 v254, s[20:21] offset:3072 sc1\n"
        "s_add_u32 m0, s73, 0xc000\n"
        "v_mfma_f32_16x16x32_bf16 v[40:43], a[232:235], a[204:207], v[40:43]\n"
        "ds_read_b128 a[8:11], v148 offset:1024\n"
        "s_add_u32 s20, s20, 0x4000\n"
        "s_addc_u32 s21, s21, 0\n"
        "v_mfma_f32_16x16x32_bf16 v[40:43], a[224:227], a[200:203], v[40:43]\n"
        "ds_read_b128 a[12:15], v148 offset:9216\n"
        "v_mfma_f32_16x16x32_bf16 v[48:51], a[224:227], a[208:211], v[48:51]\n"
        "ds_read_b128 a[16:19], v148 offset:2048\n"
        "v_mfma_f32_16x16x32_bf16 v[48:51], a[232:235], a[212:215], v[48:51]\n"
        "ds_read_b128 a[20:23], v148 offset:10240\n"
        "v_mfma_f32_16x16x32_bf16 v[52:55], a[236:239], a[212:215], v[52:55]\n"
        "ds_read_b128 a[24:27], v148 offset:3072\n"
        "v_mfma_f32_16x16x32_bf16 v[52:55], a[228:231], a[208:211], v[52:55]\n"
        "ds_read_b128 a[28:31], v148 offset:11264\n"
        "s_waitcnt lgkmcnt(8)\n"
        "v_mfma_f32_16x16x32_bf16 v[60:63], a[228:231], a[216:219], v[60:63]\n"
        "ds_read_b128 a[192:195], v151 offset:0\n"
        "s_waitcnt lgkmcnt(8)\n"
        "v_mfma_f32_16x16x32_bf16 v[60:63], a[236:239], a[220:223], v[60:63]\n"
        "ds_read_b128 a[196:199], v151 offset:8192\n"
        "s_waitcnt lgkmcnt(8)\n"
        "v_mfma_f32_16x16x32_bf16 v[56:59], a[232:235], a[220:223], v[56:59]\n"
        "ds_read_b128 a[200:203], v151 offset:1024\n"
        "s_waitcnt lgkmcnt(8)\n"
        "v_mfma_f32_16x16x32_bf16 v[56:59], a[224:227], a[216:219], v[56:59]\n"
        "ds_read_b128 a[204:207], v151 offset:9216\n"
        "s_waitcnt lgkmcnt(8)\n"
        "v_mfma_f32_16x16x32_bf16 v[0:3], a[240:243], a[0:3], v[0:3]\n"
        "ds_read_b128 a[208:211], v151 offset:2048\n"
        "s_waitcnt lgkmcnt(8)\n"
        "v_mfma_f32_16x16x32_bf16 v[0:3], a[248:251], a[4:7], v[0:3]\n"
        "ds_read_b128 a[212:215], v151 offset:10240\n"
        "s_waitcnt lgkmcnt(8)\n"
        "v_mfma_f32_16x16x32_bf16 v[4:7], a[252:255], a[4:7], v[4:7]\n"
        "ds_read_b128 a[216:219], v151 offset:3072\n"
        "s_waitcnt lgkmcnt(8)\n"
        "v_mfma_f32_16x16x32_bf16 v[4:7], a[244:247], a[0:3], v[4:7]\n"
        "ds_read_b128 a[220:223], v151 offset:11264\n"
        "s_waitcnt lgkmcnt(8)\n"
        "v_mfma_f32_16x16x32_bf16 v[12:15], a[244:247], a[8:11], v[12:15]\n"
        "v_mfma_f32_16x16x32_bf16 v[12:15], a[252:255], a[12:15], v[12:15]\n"
        "s_waitcnt lgkmcnt(6)\n"
        "v_mfma_f32_16x16x32_bf16 v[8:11], a[248:251], a[12:15], v[8:11]\n"
        "v_mfma_f32_16x16x32_bf16 v[8:11], a[240:243], a[8:11], v[8:11]\n"
        "v_mfma_f32_16x16x32_bf16 v[16:19], a[240:243], a[16:19], v[16:19]\n"
        "v_mfma_f32_16x16x32_bf16 v[16:19], a[248:251], a[20:23], v[16:19]\n"
        "s_waitcnt lgkmcnt(2)\n"
        "v_mfma_f32_16x16x32_bf16 v[20:23], a[252:255], a[20:23], v[20:23]\n"
        "v_mfma_f32_16x16x32_bf16 v[20:23], a[244:247], a[16:19], v[20:23]\n"
        "s_waitcnt vmcnt(20)\n"
        "s_barrier\n"
        "v_mfma_f32_16x16x32_bf16 v[28:31], a[244:247], a[24:27], v[28:31]\n"
        "global_load_lds_dwordx4 v144, s[60:61] offset:0\n"
        "ds_read_b128 a[0:3], v148 offset:16384\n"
        "v_mfma_f32_16x16x32_bf16 v[28:31], a[252:255], a[28:31], v[28:31]\n"
        "ds_read_b128 a[4:7], v148 offset:24576\n"
        "v_mfma_f32_16x16x32_bf16 v[24:27], a[248:251], a[28:31], v[24:27]\n"
        "ds_read_b128 a[8:11], v148 offset:17408\n"
        "v_mfma_f32_16x16x32_bf16 v[24:27], a[240:243], a[24:27], v[24:27]\n"
        "global_load_lds_dwordx4 v145, s[60:61] offset:1024\n"
        "ds_read_b128 a[12:15], v148 offset:25600\n"
        "v_mfma_f32_16x16x32_bf16 v[32:35], a[240:243], a[192:195], v[32:35]\n"
        "ds_read_b128 a[16:19], v148 offset:18432\n"
        "v_mfma_f32_16x16x32_bf16 v[32:35], a[248:251], a[196:199], v[32:35]\n"
        "ds_read_b128 a[20:23], v148 offset:26624\n"
        "s_waitcnt lgkmcnt(6)\n"
        "v_mfma_f32_16x16x32_bf16 v[36:39], a[252:255], a[196:199], v[36:39]\n"
        "global_load_lds_dwordx4 v146, s[60:61] offset:2048\n"
        "ds_read_b128 a[24:27], v148 offset:19456\n"
        "v_mfma_f32_16x16x32_bf16 v[36:39], a[244:247], a[192:195], v[36:39]\n"
        "ds_read_b128 a[28:31], v148 offset:27648\n"
        "v_mfma_f32_16x16x32_bf16 v[44:47], a[244:247], a[200:203], v[44:47]\n"
        "ds_read_b128 a[192:195], v151 offset:16384\n"
        "v_mfma_f32_16x16x32_bf16 v[44:47], a[252:255], a[204:207], v[44:47]\n"
        "global_load_lds_dwordx4 v147, s[60:61] offset:3072\n"
        "s_add_u32 m0, s73, 0x10000\n"
        "s_waitcnt lgkmcnt(7)\n"
        "v_mfma_f32_16x16x32_bf16 v[40:43], a[248:251], a[204:207], v[40:43]\n"
        "ds_read_b128 a[196:199], v151 offset:24576\n"
        "s_add_u32 s60, s22, 0x260000\n"
        "s_addc_u32 s61, s23, 0\n"
        "v_mfma_f32_16x16x32_bf16 v[40:43], a[240:243], a[200:203], v[40:43]\n"
        "ds_read_b128 a[200:203], v151 offset:17408\n"
        "v_mfma_f32_16x16x32_bf16 v[48:51], a[240:243], a[208:211], v[48:51]\n"
        "global_load_lds_dwordx4 v254, s[20:21] offset:0 sc1\n"
        "ds_read_b128 a[204:207], v151 offset:25600\n"
        "v_mfma_f32_16x16x32_bf16 v[48:51], a[248:251], a[212:215], v[48:51]\n"
        "s_waitcnt lgkmcnt(6)\n"
        "v_mfma_f32_16x16x32_bf16 v[52:55], a[252:255], a[212:215], v[52:55]\n"
        "ds_read_b128 a[212:215], v151 offset:26624\n"
        "v_mfma_f32_16x16x32_bf16 v[52:55], a[244:247], a[208:211], v[52:55]\n"
        "global_load_lds_dwordx4 v254, s[20:21] offset:1024 sc1\n"
        "ds_read_b128 a[208:211], v151 offset:18432\n"
        "v_mfma_f32_16x16x32_bf16 v[60:63], a[244:247], a[216:219], v[60:63]\n"
        "v_mfma_f32_16x16x32_bf16 v[60:63], a[252:255], a[220:223], v[60:63]\n"
        "v_mfma_f32_16x16x32_bf16 v[56:59], a[248:251], a[220:223], v[56:59]\n"
        "global_load_lds_dwordx4 v254, s[20:21] offset:2048 sc1\n"
        "ds_read_b128 a[220:223], v151 offset:27648\n"
        "v_mfma_f32_16x16x32_bf16 v[56:59], a[240:243], a[216:219], v[56:59]\n"
        "ds_read_b128 a[216:219], v151 offset:19456\n"
        "v_mfma_f32_16x16x32_bf16 v[0:3], a[80:83], a[0:3], v[0:3]\n"
        "v_mfma_f32_16x16x32_bf16 v[0:3], a[88:91], a[4:7], v[0:3]\n"
        "global_load_lds_dwordx4 v254, s[20:21] offset:3072 sc1\n"
        "s_add_u32 s20, s20, 0x4000\n"
        "s_addc_u32 s21, s21, 0\n"
        "v_mfma_f32_16x16x32_bf16 v[4:7], a[92:95], a[4:7], v[4:7]\n"
        "s_add_u32 m0, s73, 0x14000\n"
        "s_waitcnt lgkmcnt(3)\n"
        "v_mfma_f32_16x16x32_bf16 v[4:7], a[84:87], a[0:3], v[4:7]\n"
        "v_mfma_f32_16x16x32_bf16 v[12:15], a[84:87], a[8:11], v[12:15]\n"
        "v_mfma_f32_16x16x32_bf16 v[12:15], a[92:95], a[12:15], v[12:15]\n"
        "v_mfma_f32_16x16x32_bf16 v[8:11], a[88:91], a[12:15], v[8:11]\n"
        "v_mfma_f32_16x16x32_bf16 v[8:11], a[80:83], a[8:11], v[8:11]\n"
        "v_mfma_f32_16x16x32_bf16 v[16:19], a[80:83], a[16:19], v[16:19]\n"
        "v_mfma_f32_16x16x32_bf16 v[16:19], a[88:91], a[20:23], v[16:19]\n"
        "v_mfma_f32_16x16x32_bf16 v[20:23], a[92:95], a[20:23], v[20:23]\n"
        "v_mfma_f32_16x16x32_bf16 v[20:23], a[84:87], a[16:19], v[20:23]\n"
        "s_waitcnt vmcnt(20)\n"
        "s_barrier\n"
        "v_mfma_f32_16x16x32_bf16 v[28:31], a[84:87], a[24:27], v[28:31]\n"
        "global_load_lds_dwordx4 v254, s[20:21] offset:0 sc1\n"
        "ds_read_b64_tr_b16 a[32:33], v156 offset:32768\n"
        "v_mfma_f32_16x16x32_bf16 v[28:31], a[92:95], a[28:31], v[28:31]\n"
        "ds_read_b64_tr_b16 a[34:35], v156 offset:36864\n"
        "s_waitcnt lgkmcnt(2)\n"
        "v_mfma_f32_16x16x32_bf16 v[24:27], a[88:91], a[28:31], v[24:27]\n"
        "ds_read_b64_tr_b16 a[40:41], v156 offset:40960\n"
        "v_mfma_f32_16x16x32_bf16 v[24:27], a[80:83], a[24:27], v[24:27]\n"
        "ds_read_b64_tr_b16 a[42:43], v156 offset:45056\n"
        "global_load_lds_dwordx4 v254, s[20:21] offset:1024 sc1\n"
        "v_mfma_f32_16x16x32_bf16 v[32:35], a[80:83], a[192:195], v[32:35]\n"
        "ds_read_b64_tr_b16 a[36:37], v157 offset:32768\n"
        "v_mfma_f32_16x16x32_bf16 v[32:35], a[88:91], a[196:199], v[32:35]\n"
        "ds_read_b64_tr_b16 a[38:39], v157 offset:36864\n"
        "v_mfma_f32_16x16x32_bf16 v[36:39], a[92:95], a[196:199], v[36:39]\n"
        "ds_read_b64_tr_b16 a[44:45], v157 offset:40960\n"
        "global_load_lds_dwordx4 v254, s[20:21] offset:2048 sc1\n"
        "v_mfma_f32_16x16x32_bf16 v[36:39], a[84:87], a[192:195], v[36:39]\n"
        "ds_read_b64_tr_b16 a[46:47], v157 offset:45056\n"
        "v_mfma_f32_16x16x32_bf16 v[44:47], a[84:87], a[200:203], v[44:47]\n"
        "ds_read_b128 a[0:3], v149 offset:49152\n"
        "v_mfma_f32_16x16x32_bf16 v[44:47], a[92:95], a[204:207], v[44:47]\n"
        "ds_read_b128 a[4:7], v149 offset:57344\n"
        "global_load_lds_dwordx4 v254, s[20:21] offset:3072 sc1\n"
        "s_add_u32 m0, s73, 0x0\n"
        "v_mfma_f32_16x16x32_bf16 v[40:43], a[88:91], a[204:207], v[40:43]\n"
        "ds_read_b128 a[8:11], v149 offset:50176\n"
        "s_add_u32 s20, s20, 0x4000\n"
        "s_addc_u32 s21, s21, 0\n"
        "v_mfma_f32_16x16x32_bf16 v[40:43], a[80:83], a[200:203], v[40:43]\n"
        "ds_read_b128 a[12:15], v149 offset:58368\n"
        "v_mfma_f32_16x16x32_bf16 v[48:51], a[80:83], a[208:211], v[48:51]\n"
        "ds_read_b128 a[16:19], v149 offset:51200\n"
        "global_load_lds_dwordx4 v144, s[60:61] offset:0\n"
        "v_mfma_f32_16x16x32_bf16 v[48:51], a[88:91], a[212:215], v[48:51]\n"
        "ds_read_b128 a[20:23], v149 offset:59392\n"
        "v_mfma_f32_16x16x32_bf16 v[52:55], a[92:95], a[212:215], v[52:55]\n"
        "ds_read_b128 a[24:27], v149 offset:52224\n"
        "v_mfma_f32_16x16x32_bf16 v[52:55], a[84:87], a[208:211], v[52:55]\n"
        "ds_read_b128 a[28:31], v149 offset:60416\n"
        "global_load_lds_dwordx4 v145, s[60:61] offset:1024\n"
        "v_mfma_f32_16x16x32_bf16 v[60:63], a[84:87], a[216:219], v[60:63]\n"
        "ds_read_b128 a[192:195], v152 offset:49152\n"
        "s_waitcnt lgkmcnt(8)\n"
        "v_mfma_f32_16x16x32_bf16 v[60:63], a[92:95], a[220:223], v[60:63]\n"
        "ds_read_b128 a[196:199], v152 offset:57344\n"
        "s_waitcnt lgkmcnt(8)\n"
        "v_mfma_f32_16x16x32_bf16 v[56:59], a[88:91], a[220:223], v[56:59]\n"
        "global_load_lds_dwordx4 v146, s[60:61] offset:2048\n"
        "v_mfma_f32_16x16x32_bf16 v[56:59], a[80:83], a[216:219], v[56:59]\n"
        "ds_read_b128 a[200:203], v152 offset:50176\n"
        "s_waitcnt lgkmcnt(7)\n"
        "v_mfma_f32_16x16x32_bf16 v[0:3], a[32:35], a[0:3], v[0:3]\n"
        "ds_read_b128 a[204:207], v152 offset:58368\n"
        "s_waitcnt lgkmcnt(7)\n"
        "v_mfma_f32_16x16x32_bf16 v[0:3], a[40:43], a[4:7], v[0:3]\n"
        "global_load_lds_dwordx4 v147, s[60:61] offset:3072\n"
        "s_add_u32 m0, s73, 0x4000\n"
        "v_mfma_f32_16x16x32_bf16 v[4:7], a[44:47], a[4:7], v[4:7]\n"
        "ds_read_b128 a[208:211], v152 offset:51200\n"
        "s_add_u32 s60, s22, 0x280000\n"
        "s_addc_u32 s61, s23, 0\n"
        "v_mfma_f32_16x16x32_bf16 v[4:7], a[36:39], a[0:3], v[4:7]\n"
        "ds_read_b128 a[212:215], v152 offset:59392\n"
        "s_waitcnt lgkmcnt(6)\n"
        "v_mfma_f32_16x16x32_bf16 v[12:15], a[36:39], a[8:11], v[12:15]\n"
        "ds_read_b128 a[216:219], v152 offset:52224\n"
        "v_mfma_f32_16x16x32_bf16 v[12:15], a[44:47], a[12:15], v[12:15]\n"
        "ds_read_b128 a[220:223], v152 offset:60416\n"
        "s_waitcnt lgkmcnt(6)\n"
        "v_mfma_f32_16x16x32_bf16 v[8:11], a[40:43], a[12:15], v[8:11]\n"
        "v_mfma_f32_16x16x32_bf16 v[8:11], a[32:35], a[8:11], v[8:11]\n"
        "v_mfma_f32_16x16x32_bf16 v[16:19], a[32:35], a[16:19], v[16:19]\n"
        "v_mfma_f32_16x16x32_bf16 v[16:19], a[40:43], a[20:23], v[16:19]\n"
        "s_waitcnt lgkmcnt(4)\n"
        "v_mfma_f32_16x16x32_bf16 v[20:23], a[44:47], a[20:23], v[20:23]\n"
        "v_mfma_f32_16x16x32_bf16 v[20:23], a[36:39], a[16:19], v[20:23]\n"
        "s_waitcnt vmcnt(20)\n"
        "s_barrier\n"
        "v_mfma_f32_16x16x32_bf16 v[28:31], a[36:39], a[24:27], v[28:31]\n"
        "global_load_lds_dwordx4 v254, s[20:21] offset:0 sc1\n"
        "ds_read_b64_tr_b16 a[48:49], v158 offset:0\n"
        "v_mfma_f32_16x16x32_bf16 v[28:31], a[44:47], a[28:31], v[28:31]\n"
        "ds_read_b64_tr_b16 a[50:51], v158 offset:4096\n"
        "s_waitcnt lgkmcnt(2)\n"
        "v_mfma_f32_16x16x32_bf16 v[24:27], a[40:43], a[28:31], v[24:27]\n"
        "ds_read_b64_tr_b16 a[56:57], v158 offset:8192\n"
        "v_mfma_f32_16x16x32_bf16 v[24:27], a[32:35], a[24:27], v[24:27]\n"
        "ds_read_b64_tr_b16 a[58:59], v158 offset:12288\n"
        "global_load_lds_dwordx4 v254, s[20:21] offset:1024 sc1\n"
        "v_mfma_f32_16x16x32_bf16 v[32:35], a[32:35], a[192:195], v[32:35]\n"
        "ds_read_b64_tr_b16 a[52:53], v159 offset:0\n"
        "v_mfma_f32_16x16x32_bf16 v[32:35], a[40:43], a[196:199], v[32:35]\n"
        "ds_read_b64_tr_b16 a[54:55], v159 offset:4096\n"
        "v_mfma_f32_16x16x32_bf16 v[36:39], a[44:47], a[196:199], v[36:39]\n"
        "ds_read_b64_tr_b16 a[60:61], v159 offset:8192\n"
        "global_load_lds_dwordx4 v254, s[20:21] offset:2048 sc1\n"
        "v_mfma_f32_16x16x32_bf16 v[36:39], a[36:39], a[192:195], v[36:39]\n"
        "ds_read_b64_tr_b16 a[62:63], v159 offset:12288\n"
        "v_mfma_f32_16x16x32_bf16 v[44:47], a[36:39], a[200:203], v[44:47]\n"
        "ds_read_b128 a[0:3], v150 offset:16384\n"
        "v_mfma_f32_16x16x32_bf16 v[44:47], a[44:47], a[204:207], v[44:47]\n"
        "ds_read_b128 a[4:7], v150 offset:24576\n"
        "global_load_lds_dwordx4 v254, s[20:21] offset:3072 sc1\n"
        "s_add_u32 m0, s73, 0x18000\n"
        "v_mfma_f32_16x16x32_bf16 v[40:43], a[40:43], a[204:207], v[40:43]\n"
        "ds_read_b128 a[8:11], v150 offset:17408\n"
        "s_add_u32 s20, s20, 0x4000\n"
        "s_addc_u32 s21, s21, 0\n"
        "v_mfma_f32_16x16x32_bf16 v[40:43], a[32:35], a[200:203], v[40:43]\n"
        "ds_read_b128 a[12:15], v150 offset:25600\n"
        "v_mfma_f32_16x16x32_bf16 v[48:51], a[32:35], a[208:211], v[48:51]\n"
        "ds_read_b128 a[16:19], v150 offset:18432\n"
        "v_mfma_f32_16x16x32_bf16 v[48:51], a[40:43], a[212:215], v[48:51]\n"
        "ds_read_b128 a[20:23], v150 offset:26624\n"
        "v_mfma_f32_16x16x32_bf16 v[52:55], a[44:47], a[212:215], v[52:55]\n"
        "ds_read_b128 a[24:27], v150 offset:19456\n"
        "v_mfma_f32_16x16x32_bf16 v[52:55], a[36:39], a[208:211], v[52:55]\n"
        "ds_read_b128 a[28:31], v150 offset:27648\n"
        "s_waitcnt lgkmcnt(8)\n"
        "v_mfma_f32_16x16x32_bf16 v[60:63], a[36:39], a[216:219], v[60:63]\n"
        "ds_read_b128 a[192:195], v153 offset:16384\n"
        "s_waitcnt lgkmcnt(8)\n"
        "v_mfma_f32_16x16x32_bf16 v[60:63], a[44:47], a[220:223], v[60:63]\n"
        "ds_read_b128 a[196:199], v153 offset:24576\n"
        "s_waitcnt lgkmcnt(8)\n"
        "v_mfma_f32_16x16x32_bf16 v[56:59], a[40:43], a[220:223], v[56:59]\n"
        "ds_read_b128 a[200:203], v153 offset:17408\n"
        "s_waitcnt lgkmcnt(8)\n"
        "v_mfma_f32_16x16x32_bf16 v[56:59], a[32:35], a[216:219], v[56:59]\n"
        "ds_read_b128 a[204:207], v153 offset:25600\n"
        "s_waitcnt lgkmcnt(8)\n"
        "v_mfma_f32_16x16x32_bf16 v[0:3], a[48:51], a[0:3], v[0:3]\n"
        "ds_read_b128 a[208:211], v153 offset:18432\n"
        "s_waitcnt lgkmcnt(8)\n"
        "v_mfma_f32_16x16x32_bf16 v[0:3], a[56:59], a[4:7], v[0:3]\n"
        "ds_read_b128 a[212:215], v153 offset:26624\n"
        "s_waitcnt lgkmcnt(8)\n"
        "v_mfma_f32_16x16x32_bf16 v[4:7], a[60:63], a[4:7], v[4:7]\n"
        "ds_read_b128 a[216:219], v153 offset:19456\n"
        "s_waitcnt lgkmcnt(8)\n"
        "v_mfma_f32_16x16x32_bf16 v[4:7], a[52:55], a[0:3], v[4:7]\n"
        "ds_read_b128 a[220:223], v153 offset:27648\n"
        "s_waitcnt lgkmcnt(8)\n"
        "v_mfma_f32_16x16x32_bf16 v[12:15], a[52:55], a[8:11], v[12:15]\n"
        "v_mfma_f32_16x16x32_bf16 v[12:15], a[60:63], a[12:15], v[12:15]\n"
        "s_waitcnt lgkmcnt(6)\n"
        "v_mfma_f32_16x16x32_bf16 v[8:11], a[56:59], a[12:15], v[8:11]\n"
        "v_mfma_f32_16x16x32_bf16 v[8:11], a[48:51], a[8:11], v[8:11]\n"
        "v_mfma_f32_16x16x32_bf16 v[16:19], a[48:51], a[16:19], v[16:19]\n"
        "v_mfma_f32_16x16x32_bf16 v[16:19], a[56:59], a[20:23], v[16:19]\n"
        "s_waitcnt lgkmcnt(2)\n"
        "v_mfma_f32_16x16x32_bf16 v[20:23], a[60:63], a[20:23], v[20:23]\n"
        "v_mfma_f32_16x16x32_bf16 v[20:23], a[52:55], a[16:19], v[20:23]\n"
        "s_waitcnt vmcnt(20)\n"
        "s_barrier\n"
        "v_mfma_f32_16x16x32_bf16 v[28:31], a[52:55], a[24:27], v[28:31]\n"
        "global_load_lds_dwordx4 v144, s[60:61] offset:0\n"
        "ds_read_b128 a[0:3], v148 offset:32768\n"
        "v_mfma_f32_16x16x32_bf16 v[28:31], a[60:63], a[28:31], v[28:31]\n"
        "ds_read_b128 a[4:7], v148 offset:40960\n"
        "v_mfma_f32_16x16x32_bf16 v[24:27], a[56:59], a[28:31], v[24:27]\n"
        "ds_read_b128 a[8:11], v148 offset:33792\n"
        "v_mfma_f32_16x16x32_bf16 v[24:27], a[48:51], a[24:27], v[24:27]\n"
        "global_load_lds_dwordx4 v145, s[60:61] offset:1024\n"
        "ds_read_b128 a[12:15], v148 offset:41984\n"
        "v_mfma_f32_16x16x32_bf16 v[32:35], a[48:51], a[192:195], v[32:35]\n"
        "ds_read_b128 a[16:19], v148 offset:34816\n"
        "v_mfma_f32_16x16x32_bf16 v[32:35], a[56:59], a[196:199], v[32:35]\n"
        "ds_read_b128 a[20:23], v148 offset:43008\n"
        "s_waitcnt lgkmcnt(6)\n"
        "v_mfma_f32_16x16x32_bf16 v[36:39], a[60:63], a[196:199], v[36:39]\n"
        "global_load_lds_dwordx4 v146, s[60:61] offset:2048\n"
        "ds_read_b128 a[24:27], v148 offset:35840\n"
        "v_mfma_f32_16x16x32_bf16 v[36:39], a[52:55], a[192:195], v[36:39]\n"
        "LHB4_%=:\n"
        "s_cbranch_execz LHRB_%=\n"
        "ds_read_b128 a[28:31], v148 offset:44032\n"
        "v_mfma_f32_16x16x32_bf16 v[44:47], a[52:55], a[200:203], v[44:47]\n"
        "ds_read_b128 a[192:195], v151 offset:32768\n"
        "v_mfma_f32_16x16x32_bf16 v[44:47], a[60:63], a[204:207], v[44:47]\n"
        "global_load_lds_dwordx4 v147, s[60:61] offset:3072\n"
        "s_add_u32 m0, s73, 0x1c000\n"
        "s_waitcnt lgkmcnt(7)\n"
        "v_mfma_f32_16x16x32_bf16 v[40:43], a[56:59], a[204:207], v[40:43]\n"
        "ds_read_b128 a[196:199], v151 offset:40960\n"
        "s_add_u32 s60, s22, 0x2c0000\n"
        "s_addc_u32 s61, s23, 0\n"
        "v_mfma_f32_16x16x32_bf16 v[40:43], a[48:51], a[200:203], v[40:43]\n"
        "ds_read_b128 a[200:203], v151 offset:33792\n"
        "v_mfma_f32_16x16x32_bf16 v[48:51], a[48:51], a[208:211], v[48:51]\n"
        "global_load_lds_dwordx4 v254, s[20:21] offset:0 sc1\n"
        "ds_read_b128 a[204:207], v151 offset:41984\n"
        "v_mfma_f32_16x16x32_bf16 v[48:51], a[56:59], a[212:215], v[48:51]\n"
        "s_waitcnt lgkmcnt(6)\n"
        "v_mfma_f32_16x16x32_bf16 v[52:55], a[60:63], a[212:215], v[52:55]\n"
        "ds_read_b128 a[212:215], v151 offset:43008\n"
        "v_mfma_f32_16x16x32_bf16 v[52:55], a[52:55], a[208:211], v[52:55]\n"
        "global_load_lds_dwordx4 v254, s[20:21] offset:1024 sc1\n"
        "ds_read_b128 a[208:211], v151 offset:34816\n"
        "v_mfma_f32_16x16x32_bf16 v[60:63], a[52:55], a[216:219], v[60:63]\n"
        "v_mfma_f32_16x16x32_bf16 v[60:63], a[60:63], a[220:223], v[60:63]\n"
        "v_mfma_f32_16x16x32_bf16 v[56:59], a[56:59], a[220:223], v[56:59]\n"
        "global_load_lds_dwordx4 v254, s[20:21] offset:2048 sc1\n"
        "ds_read_b128 a[220:223], v151 offset:44032\n"
        "v_mfma_f32_16x16x32_bf16 v[56:59], a[48:51], a[216:219], v[56:59]\n"
        "ds_read_b128 a[216:219], v151 offset:35840\n"
        "v_mfma_f32_16x16x32_bf16 v[0:3], a[96:99], a[0:3], v[0:3]\n"
        "v_mfma_f32_16x16x32_bf16 v[0:3], a[104:107], a[4:7], v[0:3]\n"
        "global_load_lds_dwordx4 v254, s[20:21] offset:3072 sc1\n"
        "s_add_u32 s20, s20, 0x4000\n"
        "s_addc_u32 s21, s21, 0\n"
        "v_mfma_f32_16x16x32_bf16 v[4:7], a[108:111], a[4:7], v[4:7]\n"
        "s_add_u32 m0, s73, 0x20000\n"
        "s_waitcnt lgkmcnt(3)\n"
        "v_mfma_f32_16x16x32_bf16 v[4:7], a[100:103], a[0:3], v[4:7]\n"
        "v_mfma_f32_16x16x32_bf16 v[12:15], a[100:103], a[8:11], v[12:15]\n"
        "v_mfma_f32_16x16x32_bf16 v[12:15], a[108:111], a[12:15], v[12:15]\n"
        "v_mfma_f32_16x16x32_bf16 v[8:11], a[104:107], a[12:15], v[8:11]\n"
        "v_mfma_f32_16x16x32_bf16 v[8:11], a[96:99], a[8:11], v[8:11]\n"
        "v_mfma_f32_16x16x32_bf16 v[16:19], a[96:99], a[16:19], v[16:19]\n"
        "v_mfma_f32_16x16x32_bf16 v[16:19], a[104:107], a[20:23], v[16:19]\n"
        "v_mfma_f32_16x16x32_bf16 v[20:23], a[108:111], a[20:23], v[20:23]\n"
        "v_mfma_f32_16x16x32_bf16 v[20:23], a[100:103], a[16:19], v[20:23]\n"
        "s_waitcnt vmcnt(20)\n"
        "s_barrier\n"
        "v_mfma_f32_16x16x32_bf16 v[28:31], a[100:103], a[24:27], v[28:31]\n"
        "global_load_lds_dwordx4 v254, s[20:21] offset:0 sc1\n"
        "ds_read_b64_tr_b16 a[224:225], v154 offset:49152\n"
        "v_mfma_f32_16x16x32_bf16 v[28:31], a[108:111], a[28:31], v[28:31]\n"
        "ds_read_b64_tr_b16 a[226:227], v154 offset:53248\n"
        "s_waitcnt lgkmcnt(2)\n"
        "v_mfma_f32_16x16x32_bf16 v[24:27], a[104:107], a[28:31], v[24:27]\n"
        "ds_read_b64_tr_b16 a[232:233], v154 offset:57344\n"
        "v_mfma_f32_16x16x32_bf16 v[24:27], a[96:99], a[24:27], v[24:27]\n"
        "ds_read_b64_tr_b16 a[234:235], v154 offset:61440\n"
        "global_load_lds_dwordx4 v254, s[20:21] offset:1024 sc1\n"
        "v_mfma_f32_16x16x32_bf16 v[32:35], a[96:99], a[192:195], v[32:35]\n"
        "ds_read_b64_tr_b16 a[228:229], v155 offset:49152\n"
        "v_mfma_f32_16x16x32_bf16 v[32:35], a[104:107], a[196:199], v[32:35]\n"
        "ds_read_b64_tr_b16 a[230:231], v155 offset:53248\n"
        "v_mfma_f32_16x16x32_bf16 v[36:39], a[108:111], a[196:199], v[36:39]\n"
        "ds_read_b64_tr_b16 a[236:237], v155 offset:57344\n"
        "global_load_lds_dwordx4 v254, s[20:21] offset:2048 sc1\n"
        "v_mfma_f32_16x16x32_bf16 v[36:39], a[100:103], a[192:195], v[36:39]\n"
        "ds_read_b64_tr_b16 a[238:239], v155 offset:61440\n"
        "v_mfma_f32_16x16x32_bf16 v[44:47], a[100:103], a[200:203], v[44:47]\n"
        "ds_read_b128 a[0:3], v149 offset:0\n"
        "v_mfma_f32_16x16x32_bf16 v[44:47], a[108:111], a[204:207], v[44:47]\n"
        "ds_read_b128 a[4:7], v149 offset:8192\n"
        "global_load_lds_dwordx4 v254, s[20:21] offset:3072 sc1\n"
        "s_add_u32 m0, s73, 0x24000\n"
        "v_mfma_f32_16x16x32_bf16 v[40:43], a[104:107], a[204:207], v[40:43]\n"
        "ds_read_b128 a[8:11], v149 offset:1024\n"
        "s_add_u32 s20, s20, 0x4000\n"
        "s_addc_u32 s21, s21, 0\n"
        "v_mfma_f32_16x16x32_bf16 v[40:43], a[96:99], a[200:203], v[40:43]\n"
        "ds_read_b128 a[12:15], v149 offset:9216\n"
        "v_mfma_f32_16x16x32_bf16 v[48:51], a[96:99], a[208:211], v[48:51]\n"
        "ds_read_b128 a[16:19], v149 offset:2048\n"
        "global_load_lds_dwordx4 v144, s[60:61] offset:0\n"
        "v_mfma_f32_16x16x32_bf16 v[48:51], a[104:107], a[212:215], v[48:51]\n"
        "ds_read_b128 a[20:23], v149 offset:10240\n"
        "v_mfma_f32_16x16x32_bf16 v[52:55], a[108:111], a[212:215], v[52:55]\n"
        "ds_read_b128 a[24:27], v149 offset:3072\n"
        "v_mfma_f32_16x16x32_bf16 v[52:55], a[100:103], a[208:211], v[52:55]\n"
        "ds_read_b128 a[28:31], v149 offset:11264\n"
        "global_load_lds_dwordx4 v145, s[60:61] offset:1024\n"
        "v_mfma_f32_16x16x32_bf16 v[60:63], a[100:103], a[216:219], v[60:63]\n"
        "ds_read_b128 a[192:195], v152 offset:0\n"
        "s_waitcnt lgkmcnt(8)\n"
        "v_mfma_f32_16x16x32_bf16 v[60:63], a[108:111], a[220:223], v[60:63]\n"
        "ds_read_b128 a[196:199], v152 offset:8192\n"
        "s_waitcnt lgkmcnt(8)\n"
        "v_mfma_f32_16x16x32_bf16 v[56:59], a[104:107], a[220:223], v[56:59]\n"
        "global_load_lds_dwordx4 v146, s[60:61] offset:2048\n"
        "v_mfma_f32_16x16x32_bf16 v[56:59], a[96:99], a[216:219], v[56:59]\n"
        "ds_read_b128 a[200:203], v152 offset:1024\n"
        "s_waitcnt lgkmcnt(7)\n"
        "v_mfma_f32_16x16x32_bf16 v[0:3], a[224:227], a[0:3], v[0:3]\n"
        "ds_read_b128 a[204:207], v152 offset:9216\n"
        "s_waitcnt lgkmcnt(7)\n"
        "v_mfma_f32_16x16x32_bf16 v[0:3], a[232:235], a[4:7], v[0:3]\n"
        "global_load_lds_dwordx4 v147, s[60:61] offset:3072\n"
        "s_add_u32 m0, s73, 0x8000\n"
        "v_mfma_f32_16x16x32_bf16 v[4:7], a[236:239], a[4:7], v[4:7]\n"
        "ds_read_b128 a[208:211], v152 offset:2048\n"
        "s_add_u32 s60, s22, 0x2e0000\n"
        "s_addc_u32 s61, s23, 0\n"
        "v_mfma_f32_16x16x32_bf16 v[4:7], a[228:231], a[0:3], v[4:7]\n"
        "ds_read_b128 a[212:215], v152 offset:10240\n"
        "s_waitcnt lgkmcnt(6)\n"
        "v_mfma_f32_16x16x32_bf16 v[12:15], a[228:231], a[8:11], v[12:15]\n"
        "ds_read_b128 a[216:219], v152 offset:3072\n"
        "v_mfma_f32_16x16x32_bf16 v[12:15], a[236:239], a[12:15], v[12:15]\n"
        "ds_read_b128 a[220:223], v152 offset:11264\n"
        "s_waitcnt lgkmcnt(6)\n"
        "v_mfma_f32_16x16x32_bf16 v[8:11], a[232:235], a[12:15], v[8:11]\n"
        "v_mfma_f32_16x16x32_bf16 v[8:11], a[224:227], a[8:11], v[8:11]\n"
        "v_mfma_f32_16x16x32_bf16 v[16:19], a[224:227], a[16:19], v[16:19]\n"
        "v_mfma_f32_16x16x32_bf16 v[16:19], a[232:235], a[20:23], v[16:19]\n"
        "s_waitcnt lgkmcnt(4)\n"
        "v_mfma_f32_16x16x32_bf16 v[20:23], a[236:239], a[20:23], v[20:23]\n"
        "v_mfma_f32_16x16x32_bf16 v[20:23], a[228:231], a[16:19], v[20:23]\n"
        "s_waitcnt vmcnt(24)\n"
        "s_barrier\n"
        "v_mfma_f32_16x16x32_bf16 v[28:31], a[228:231], a[24:27], v[28:31]\n"
        "global_load_lds_dwordx4 v254, s[20:21] offset:0 sc1\n"
        "ds_read_b128 a[0:3], v149 offset:16384\n"
        "v_mfma_f32_16x16x32_bf16 v[28:31], a[236:239], a[28:31], v[28:31]\n"
        "ds_read_b128 a[4:7], v149 offset:24576\n"
        "s_waitcnt lgkmcnt(2)\n"
        "v_mfma_f32_16x16x32_bf16 v[24:27], a[232:235], a[28:31], v[24:27]\n"
        "ds_read_b128 a[8:11], v149 offset:17408\n"
        "v_mfma_f32_16x16x32_bf16 v[24:27], a[224:227], a[24:27], v[24:27]\n"
        "global_load_lds_dwordx4 v254, s[20:21] offset:1024 sc1\n"
        "ds_read_b128 a[12:15], v149 offset:25600\n"
        "v_mfma_f32_16x16x32_bf16 v[32:35], a[224:227], a[192:195], v[32:35]\n"
        "ds_read_b128 a[16:19], v149 offset:18432\n"
        "v_mfma_f32_16x16x32_bf16 v[32:35], a[232:235], a[196:199], v[32:35]\n"
        "ds_read_b128 a[20:23], v149 offset:26624\n"
        "v_mfma_f32_16x16x32_bf16 v[36:39], a[236:239], a[196:199], v[36:39]\n"
        "global_load_lds_dwordx4 v254, s[20:21] offset:2048 sc1\n"
        "ds_read_b128 a[24:27], v149 offset:19456\n"
        "v_mfma_f32_16x16x32_bf16 v[36:39], a[228:231], a[192:195], v[36:39]\n"
        "ds_read_b128 a[28:31], v149 offset:27648\n"
        "v_mfma_f32_16x16x32_bf16 v[44:47], a[228:231], a[200:203], v[44:47]\n"
        "ds_read_b128 a[192:195], v152 offset:16384\n"
        "v_mfma_f32_16x16x32_bf16 v[44:47], a[236:239], a[204:207], v[44:47]\n"
        "global_load_lds_dwordx4 v254, s[20:21] offset:3072 sc1\n"
        "s_add_u32 s20, s20, 0x4000\n"
        "s_addc_u32 s21, s21, 0\n"
        "v_mfma_f32_16x16x32_bf16 v[40:43], a[232:235], a[204:207], v[40:43]\n"
        "ds_read_b128 a[196:199], v152 offset:24576\n"
        "s_add_u32 m0, s73, 0xc000\n"
        "s_waitcnt lgkmcnt(7)\n"
        "v_mfma_f32_16x16x32_bf16 v[40:43], a[224:227], a[200:203], v[40:43]\n"
        "ds_read_b128 a[200:203], v152 offset:17408\n"
        "v_mfma_f32_16x16x32_bf16 v[48:51], a[224:227], a[208:211], v[48:51]\n"
        "global_load_dwordx2 v[236:237], v234, s[48:49] sc1\n"
        "ds_read_b128 a[204:207], v152 offset:25600\n"
        "v_mfma_f32_16x16x32_bf16 v[48:51], a[232:235], a[212:215], v[48:51]\n"
        "v_mfma_f32_16x16x32_bf16 v[52:55], a[236:239], a[212:215], v[52:55]\n"
        "ds_read_b128 a[212:215], v152 offset:26624\n"
        "v_mfma_f32_16x16x32_bf16 v[52:55], a[228:231], a[208:211], v[52:55]\n"
        "ds_read_b128 a[208:211], v152 offset:18432\n"
        "s_waitcnt lgkmcnt(6)\n"
        "v_mfma_f32_16x16x32_bf16 v[60:63], a[228:231], a[216:219], v[60:63]\n"
        "v_mfma_f32_16x16x32_bf16 v[60:63], a[236:239], a[220:223], v[60:63]\n"
        "v_mfma_f32_16x16x32_bf16 v[56:59], a[232:235], a[220:223], v[56:59]\n"
        "ds_read_b128 a[220:223], v152 offset:27648\n"
        "v_mfma_f32_16x16x32_bf16 v[56:59], a[224:227], a[216:219], v[56:59]\n"
        "ds_read_b128 a[216:219], v152 offset:19456\n"
        "v_mfma_f32_16x16x32_bf16 v[0:3], a[112:115], a[0:3], v[0:3]\n"
        "v_mfma_f32_16x16x32_bf16 v[0:3], a[120:123], a[4:7], v[0:3]\n"
        "v_mfma_f32_16x16x32_bf16 v[4:7], a[124:127], a[4:7], v[4:7]\n"
        "v_mfma_f32_16x16x32_bf16 v[4:7], a[116:119], a[0:3], v[4:7]\n"
        "v_mfma_f32_16x16x32_bf16 v[12:15], a[116:119], a[8:11], v[12:15]\n"
        "v_mfma_f32_16x16x32_bf16 v[12:15], a[124:127], a[12:15], v[12:15]\n"
        "s_waitcnt lgkmcnt(2)\n"
        "v_mfma_f32_16x16x32_bf16 v[8:11], a[120:123], a[12:15], v[8:11]\n"
        "v_mfma_f32_16x16x32_bf16 v[8:11], a[112:115], a[8:11], v[8:11]\n"
        "v_mfma_f32_16x16x32_bf16 v[16:19], a[112:115], a[16:19], v[16:19]\n"
        "v_mfma_f32_16x16x32_bf16 v[16:19], a[120:123], a[20:23], v[16:19]\n"
        "v_mfma_f32_16x16x32_bf16 v[20:23], a[124:127], a[20:23], v[20:23]\n"
        "v_mfma_f32_16x16x32_bf16 v[20:23], a[116:119], a[16:19], v[20:23]\n"
        "s_waitcnt vmcnt(21)\n"
        "s_barrier\n"
        "v_mfma_f32_16x16x32_bf16 v[28:31], a[116:119], a[24:27], v[28:31]\n"
        "global_load_lds_dwordx4 v144, s[60:61] offset:0\n"
        "ds_read_b64_tr_b16 a[240:241], v154 offset:0\n"
        "v_mfma_f32_16x16x32_bf16 v[28:31], a[124:127], a[28:31], v[28:31]\n"
        "ds_read_b64_tr_b16 a[242:243], v154 offset:4096\n"
        "v_mfma_f32_16x16x32_bf16 v[24:27], a[120:123], a[28:31], v[24:27]\n"
        "ds_read_b64_tr_b16 a[248:249], v154 offset:8192\n"
        "v_mfma_f32_16x16x32_bf16 v[24:27], a[112:115], a[24:27], v[24:27]\n"
        "ds_read_b64_tr_b16 a[250:251], v154 offset:12288\n"
        "global_load_lds_dwordx4 v145, s[60:61] offset:1024\n"
        "v_mfma_f32_16x16x32_bf16 v[32:35], a[112:115], a[192:195], v[32:35]\n"
        "ds_read_b64_tr_b16 a[244:245], v155 offset:0\n"
        "v_mfma_f32_16x16x32_bf16 v[32:35], a[120:123], a[196:199], v[32:35]\n"
        "ds_read_b64_tr_b16 a[246:247], v155 offset:4096\n"
        "s_waitcnt lgkmcnt(6)\n"
        "v_mfma_f32_16x16x32_bf16 v[36:39], a[124:127], a[196:199], v[36:39]\n"
        "ds_read_b64_tr_b16 a[252:253], v155 offset:8192\n"
        "global_load_lds_dwordx4 v146, s[60:61] offset:2048\n"
        "v_mfma_f32_16x16x32_bf16 v[36:39], a[116:119], a[192:195], v[36:39]\n"
        "ds_read_b64_tr_b16 a[254:255], v155 offset:12288\n"
        "v_mfma_f32_16x16x32_bf16 v[44:47], a[116:119], a[200:203], v[44:47]\n"
        "ds_read_b128 a[0:3], v148 offset:16384\n"
        "v_mfma_f32_16x16x32_bf16 v[44:47], a[124:127], a[204:207], v[44:47]\n"
        "ds_read_b128 a[4:7], v148 offset:24576\n"
        "global_load_lds_dwordx4 v147, s[60:61] offset:3072\n"
        "s_add_u32 m0, s73, 0x10000\n"
        "v_mfma_f32_16x16x32_bf16 v[40:43], a[120:123], a[204:207], v[40:43]\n"
        "ds_read_b128 a[8:11], v148 offset:17408\n"
        "s_add_u32 s60, s22, 0x320000\n"
        "s_addc_u32 s61, s23, 0\n"
        "v_mfma_f32_16x16x32_bf16 v[40:43], a[112:115], a[200:203], v[40:43]\n"
        "ds_read_b128 a[12:15], v148 offset:25600\n"
        "v_mfma_f32_16x16x32_bf16 v[48:51], a[112:115], a[208:211], v[48:51]\n"
        "ds_read_b128 a[16:19], v148 offset:18432\n"
        "global_load_lds_dwordx4 v254, s[20:21] offset:0 sc1\n"
        "v_mfma_f32_16x16x32_bf16 v[48:51], a[120:123], a[212:215], v[48:51]\n"
        "ds_read_b128 a[20:23], v148 offset:26624\n"
        "v_mfma_f32_16x16x32_bf16 v[52:55], a[124:127], a[212:215], v[52:55]\n"
        "ds_read_b128 a[24:27], v148 offset:19456\n"
        "v_mfma_f32_16x16x32_bf16 v[52:55], a[116:119], a[208:211], v[52:55]\n"
        "ds_read_b128 a[28:31], v148 offset:27648\n"
        "global_load_lds_dwordx4 v254, s[20:21] offset:1024 sc1\n"
        "v_mfma_f32_16x16x32_bf16 v[60:63], a[116:119], a[216:219], v[60:63]\n"
        "ds_read_b128 a[192:195], v151 offset:16384\n"
        "s_waitcnt lgkmcnt(8)\n"
        "v_mfma_f32_16x16x32_bf16 v[60:63], a[124:127], a[220:223], v[60:63]\n"
        "ds_read_b128 a[196:199], v151 offset:24576\n"
        "s_waitcnt lgkmcnt(8)\n"
        "v_mfma_f32_16x16x32_bf16 v[56:59], a[120:123], a[220:223], v[56:59]\n"
        "global_load_lds_dwordx4 v254, s[20:21] offset:2048 sc1\n"
        "v_mfma_f32_16x16x32_bf16 v[56:59], a[112:115], a[216:219], v[56:59]\n"
        "ds_read_b128 a[200:203], v151 offset:17408\n"
        "s_waitcnt lgkmcnt(7)\n"
        "v_mfma_f32_16x16x32_bf16 v[0:3], a[240:243], a[0:3], v[0:3]\n"
        "ds_read_b128 a[204:207], v151 offset:25600\n"
        "s_waitcnt lgkmcnt(7)\n"
        "v_mfma_f32_16x16x32_bf16 v[0:3], a[248:251], a[4:7], v[0:3]\n"
        "global_load_lds_dwordx4 v254, s[20:21] offset:3072 sc1\n"
        "s_add_u32 m0, s73, 0x14000\n"
        "v_mfma_f32_16x16x32_bf16 v[4:7], a[252:255], a[4:7], v[4:7]\n"
        "ds_read_b128 a[208:211], v151 offset:18432\n"
        "s_add_u32 s20, s20, 0x4000\n"
        "s_addc_u32 s21, s21, 0\n"
        "v_mfma_f32_16x16x32_bf16 v[4:7], a[244:247], a[0:3], v[4:7]\n"
        "ds_read_b128 a[212:215], v151 offset:26624\n"
        "s_waitcnt lgkmcnt(6)\n"
        "v_mfma_f32_16x16x32_bf16 v[12:15], a[244:247], a[8:11], v[12:15]\n"
        "ds_read_b128 a[216:219], v151 offset:19456\n"
        "v_mfma_f32_16x16x32_bf16 v[12:15], a[252:255], a[12:15], v[12:15]\n"
        "ds_read_b128 a[220:223], v151 offset:27648\n"
        "s_waitcnt lgkmcnt(6)\n"
        "v_mfma_f32_16x16x32_bf16 v[8:11], a[248:251], a[12:15], v[8:11]\n"
        "v_mfma_f32_16x16x32_bf16 v[8:11], a[240:243], a[8:11], v[8:11]\n"
        "v_mfma_f32_16x16x32_bf16 v[16:19], a[240:243], a[16:19], v[16:19]\n"
        "v_mfma_f32_16x16x32_bf16 v[16:19], a[248:251], a[20:23], v[16:19]\n"
        "s_waitcnt lgkmcnt(4)\n"
        "v_mfma_f32_16x16x32_bf16 v[20:23], a[252:255], a[20:23], v[20:23]\n"
        "v_mfma_f32_16x16x32_bf16 v[20:23], a[244:247], a[16:19], v[20:23]\n"
        "s_waitcnt vmcnt(21)\n"
        "s_barrier\n"
        "v_mfma_f32_16x16x32_bf16 v[28:31], a[244:247], a[24:27], v[28:31]\n"
        "global_load_lds_dwordx4 v254, s[20:21] offset:0 sc1\n"
        "ds_read_b64_tr_b16 a[32:33], v156 offset:32768\n"
        "v_mfma_f32_16x16x32_bf16 v[28:31], a[252:255], a[28:31], v[28:31]\n"
        "ds_read_b64_tr_b16 a[34:35], v156 offset:36864\n"
        "s_waitcnt lgkmcnt(2)\n"
        "v_mfma_f32_16x16x32_bf16 v[24:27], a[248:251], a[28:31], v[24:27]\n"
        "ds_read_b64_tr_b16 a[40:41], v156 offset:40960\n"
        "v_mfma_f32_16x16x32_bf16 v[24:27], a[240:243], a[24:27], v[24:27]\n"
        "ds_read_b64_tr_b16 a[42:43], v156 offset:45056\n"
        "global_load_lds_dwordx4 v254, s[20:21] offset:1024 sc1\n"
        "v_mfma_f32_16x16x32_bf16 v[32:35], a[240:243], a[192:195], v[32:35]\n"
        "ds_read_b64_tr_b16 a[36:37], v157 offset:32768\n"
        "v_mfma_f32_16x16x32_bf16 v[32:35], a[248:251], a[196:199], v[32:35]\n"
        "ds_read_b64_tr_b16 a[38:39], v157 offset:36864\n"
        "v_mfma_f32_16x16x32_bf16 v[36:39], a[252:255], a[196:199], v[36:39]\n"
        "ds_read_b64_tr_b16 a[44:45], v157 offset:40960\n"
        "global_load_lds_dwordx4 v254, s[20:21] offset:2048 sc1\n"
        "v_mfma_f32_16x16x32_bf16 v[36:39], a[244:247], a[192:195], v[36:39]\n"
        "ds_read_b64_tr_b16 a[46:47], v157 offset:45056\n"
        "v_mfma_f32_16x16x32_bf16 v[44:47], a[244:247], a[200:203], v[44:47]\n"
        "ds_read_b128 a[0:3], v149 offset:49152\n"
        "v_mfma_f32_16x16x32_bf16 v[44:47], a[252:255], a[204:207], v[44:47]\n"
        "ds_read_b128 a[4:7], v149 offset:57344\n"
        "global_load_lds_dwordx4 v254, s[20:21] offset:3072 sc1\n"
        "s_add_u32 m0, s73, 0x0\n"
        "v_mfma_f32_16x16x32_bf16 v[40:43], a[248:251], a[204:207], v[40:43]\n"
        "ds_read_b128 a[8:11], v149 offset:50176\n"
        "s_add_u32 s20, s20, 0x4000\n"
        "s_addc_u32 s21, s21, 0\n"
        "v_mfma_f32_16x16x32_bf16 v[40:43], a[240:243], a[200:203], v[40:43]\n"
        "ds_read_b128 a[12:15], v149 offset:58368\n"
        "v_mfma_f32_16x16x32_bf16 v[48:51], a[240:243], a[208:211], v[48:51]\n"
        "ds_read_b128 a[16:19], v149 offset:51200\n"
        "v_mfma_f32_16x16x32_bf16 v[48:51], a[248:251], a[212:215], v[48:51]\n"
        "ds_read_b128 a[20:23], v149 offset:59392\n"
        "v_mfma_f32_16x16x32_bf16 v[52:55], a[252:255], a[212:215], v[52:55]\n"
        "ds_read_b128 a[24:27], v149 offset:52224\n"
        "v_mfma_f32_16x16x32_bf16 v[52:55], a[244:247], a[208:211], v[52:55]\n"
        "ds_read_b128 a[28:31], v149 offset:60416\n"
        "s_waitcnt lgkmcnt(8)\n"
        "v_mfma_f32_16x16x32_bf16 v[60:63], a[244:247], a[216:219], v[60:63]\n"
        "ds_read_b128 a[192:195], v152 offset:49152\n"
        "s_waitcnt lgkmcnt(8)\n"
        "v_mfma_f32_16x16x32_bf16 v[60:63], a[252:255], a[220:223], v[60:63]\n"
        "ds_read_b128 a[196:199], v152 offset:57344\n"
        "s_waitcnt lgkmcnt(8)\n"
        "v_mfma_f32_16x16x32_bf16 v[56:59], a[248:251], a[220:223], v[56:59]\n"
        "ds_read_b128 a[200:203], v152 offset:50176\n"
        "s_waitcnt lgkmcnt(8)\n"
        "v_mfma_f32_16x16x32_bf16 v[56:59], a[240:243], a[216:219], v[56:59]\n"
        "ds_read_b128 a[204:207], v152 offset:58368\n"
        "s_waitcnt lgkmcnt(8)\n"
        "v_mfma_f32_16x16x32_bf16 v[0:3], a[32:35], a[0:3], v[0:3]\n"
        "ds_read_b128 a[208:211], v152 offset:51200\n"
        "s_waitcnt lgkmcnt(8)\n"
        "v_mfma_f32_16x16x32_bf16 v[0:3], a[40:43], a[4:7], v[0:3]\n"
        "ds_read_b128 a[212:215], v152 offset:59392\n"
        "s_waitcnt lgkmcnt(8)\n"
        "v_mfma_f32_16x16x32_bf16 v[4:7], a[44:47], a[4:7], v[4:7]\n"
        "ds_read_b128 a[216:219], v152 offset:52224\n"
        "s_waitcnt lgkmcnt(8)\n"
        "v_mfma_f32_16x16x32_bf16 v[4:7], a[36:39], a[0:3], v[4:7]\n"
        "ds_read_b128 a[220:223], v152 offset:60416\n"
        "s_waitcnt lgkmcnt(8)\n"
        "v_mfma_f32_16x16x32_bf16 v[12:15], a[36:39], a[8:11], v[12:15]\n"
        "v_mfma_f32_16x16x32_bf16 v[12:15], a[44:47], a[12:15], v[12:15]\n"
        "s_waitcnt lgkmcnt(6)\n"
        "v_mfma_f32_16x16x32_bf16 v[8:11], a[40:43], a[12:15], v[8:11]\n"
        "v_mfma_f32_16x16x32_bf16 v[8:11], a[32:35], a[8:11], v[8:11]\n"
        "v_mfma_f32_16x16x32_bf16 v[16:19], a[32:35], a[16:19], v[16:19]\n"
        "v_mfma_f32_16x16x32_bf16 v[16:19], a[40:43], a[20:23], v[16:19]\n"
        "s_waitcnt lgkmcnt(2)\n"
        "v_mfma_f32_16x16x32_bf16 v[20:23], a[44:47], a[20:23], v[20:23]\n"
        "v_mfma_f32_16x16x32_bf16 v[20:23], a[36:39], a[16:19], v[20:23]\n"
        "s_waitcnt vmcnt(21)\n"
        "s_barrier\n"
        "v_mfma_f32_16x16x32_bf16 v[28:31], a[36:39], a[24:27], v[28:31]\n"
        "global_load_lds_dwordx4 v144, s[60:61] offset:0\n"
        "ds_read_b128 a[0:3], v150 offset:0\n"
        "v_mfma_f32_16x16x32_bf16 v[28:31], a[44:47], a[28:31], v[28:31]\n"
        "ds_read_b128 a[4:7], v150 offset:8192\n"
        "v_mfma_f32_16x16x32_bf16 v[24:27], a[40:43], a[28:31], v[24:27]\n"
        "ds_read_b128 a[8:11], v150 offset:1024\n"
        "v_mfma_f32_16x16x32_bf16 v[24:27], a[32:35], a[24:27], v[24:27]\n"
        "global_load_lds_dwordx4 v145, s[60:61] offset:1024\n"
        "ds_read_b128 a[12:15], v150 offset:9216\n"
        "v_mfma_f32_16x16x32_bf16 v[32:35], a[32:35], a[192:195], v[32:35]\n"
        "ds_read_b128 a[16:19], v150 offset:2048\n"
        "v_mfma_f32_16x16x32_bf16 v[32:35], a[40:43], a[196:199], v[32:35]\n"
        "ds_read_b128 a[20:23], v150 offset:10240\n"
        "s_waitcnt lgkmcnt(6)\n"
        "v_mfma_f32_16x16x32_bf16 v[36:39], a[44:47], a[196:199], v[36:39]\n"
        "global_load_lds_dwordx4 v146, s[60:61] offset:2048\n"
        "ds_read_b128 a[24:27], v150 offset:3072\n"
        "v_mfma_f32_16x16x32_bf16 v[36:39], a[36:39], a[192:195], v[36:39]\n"
        "ds_read_b128 a[28:31], v150 offset:11264\n"
        "v_mfma_f32_16x16x32_bf16 v[44:47], a[36:39], a[200:203], v[44:47]\n"
        "ds_read_b128 a[192:195], v153 offset:0\n"
        "v_mfma_f32_16x16x32_bf16 v[44:47], a[44:47], a[204:207], v[44:47]\n"
        "global_load_lds_dwordx4 v147, s[60:61] offset:3072\n"
        "s_add_u32 m0, s73, 0x4000\n"
        "s_waitcnt lgkmcnt(7)\n"
        "v_mfma_f32_16x16x32_bf16 v[40:43], a[40:43], a[204:207], v[40:43]\n"
        "ds_read_b128 a[196:199], v153 offset:8192\n"
        "s_add_u32 s60, s22, 0x360000\n"
        "s_addc_u32 s61, s23, 0\n"
        "v_mfma_f32_16x16x32_bf16 v[40:43], a[32:35], a[200:203], v[40:43]\n"
        "ds_read_b128 a[200:203], v153 offset:1024\n"
        "v_mfma_f32_16x16x32_bf16 v[48:51], a[32:35], a[208:211], v[48:51]\n"
        "global_load_lds_dwordx4 v254, s[20:21] offset:0 sc1\n"
        "ds_read_b128 a[204:207], v153 offset:9216\n"
        "v_mfma_f32_16x16x32_bf16 v[48:51], a[40:43], a[212:215], v[48:51]\n"
        "s_waitcnt lgkmcnt(6)\n"
        "v_mfma_f32_16x16x32_bf16 v[52:55], a[44:47], a[212:215], v[52:55]\n"
        "ds_read_b128 a[212:215], v153 offset:10240\n"
        "v_mfma_f32_16x16x32_bf16 v[52:55], a[36:39], a[208:211], v[52:55]\n"
        "global_load_lds_dwordx4 v254, s[20:21] offset:1024 sc1\n"
        "ds_read_b128 a[208:211], v153 offset:2048\n"
        "v_mfma_f32_16x16x32_bf16 v[60:63], a[36:39], a[216:219], v[60:63]\n"
        "v_mfma_f32_16x16x32_bf16 v[60:63], a[44:47], a[220:223], v[60:63]\n"
        "v_mfma_f32_16x16x32_bf16 v[56:59], a[40:43], a[220:223], v[56:59]\n"
        "global_load_lds_dwordx4 v254, s[20:21] offset:2048 sc1\n"
        "ds_read_b128 a[220:223], v153 offset:11264\n"
        "v_mfma_f32_16x16x32_bf16 v[56:59], a[32:35], a[216:219], v[56:59]\n"
        "ds_read_b128 a[216:219], v153 offset:3072\n"
        "v_mfma_f32_16x16x32_bf16 v[0:3], a[128:131], a[0:3], v[0:3]\n"
        "v_mfma_f32_16x16x32_bf16 v[0:3], a[136:139], a[4:7], v[0:3]\n"
        "global_load_lds_dwordx4 v254, s[20:21] offset:3072 sc1\n"
        "s_add_u32 s20, s20, 0x4000\n"
        "s_addc_u32 s21, s21, 0\n"
        "v_mfma_f32_16x16x32_bf16 v[4:7], a[140:143], a[4:7], v[4:7]\n"
        "s_add_u32 m0, s73, 0x18000\n"
        "s_waitcnt lgkmcnt(3)\n"
        "v_mfma_f32_16x16x32_bf16 v[4:7], a[132:135], a[0:3], v[4:7]\n"
        "v_mfma_f32_16x16x32_bf16 v[12:15], a[132:135], a[8:11], v[12:15]\n"
        "v_mfma_f32_16x16x32_bf16 v[12:15], a[140:143], a[12:15], v[12:15]\n"
        "v_mfma_f32_16x16x32_bf16 v[8:11], a[136:139], a[12:15], v[8:11]\n"
        "v_mfma_f32_16x16x32_bf16 v[8:11], a[128:131], a[8:11], v[8:11]\n"
        "v_mfma_f32_16x16x32_bf16 v[16:19], a[128:131], a[16:19], v[16:19]\n"
        "v_mfma_f32_16x16x32_bf16 v[16:19], a[136:139], a[20:23], v[16:19]\n"
        "v_mfma_f32_16x16x32_bf16 v[20:23], a[140:143], a[20:23], v[20:23]\n"
        "v_mfma_f32_16x16x32_bf16 v[20:23], a[132:135], a[16:19], v[20:23]\n"
        "s_waitcnt vmcnt(21)\n"
        "s_barrier\n"
        "v_mfma_f32_16x16x32_bf16 v[28:31], a[132:135], a[24:27], v[28:31]\n"
        "global_load_lds_dwordx4 v254, s[20:21] offset:0 sc1\n"
        "ds_read_b64_tr_b16 a[48:49], v158 offset:16384\n"
        "v_mfma_f32_16x16x32_bf16 v[28:31], a[140:143], a[28:31], v[28:31]\n"
        "ds_read_b64_tr_b16 a[50:51], v158 offset:20480\n"
        "s_waitcnt lgkmcnt(2)\n"
        "v_mfma_f32_16x16x32_bf16 v[24:27], a[136:139], a[28:31], v[24:27]\n"
        "ds_read_b64_tr_b16 a[56:57], v158 offset:24576\n"
        "v_mfma_f32_16x16x32_bf16 v[24:27], a[128:131], a[24:27], v[24:27]\n"
        "ds_read_b64_tr_b16 a[58:59], v158 offset:28672\n"
        "global_load_lds_dwordx4 v254, s[20:21] offset:1024 sc1\n"
        "v_mfma_f32_16x16x32_bf16 v[32:35], a[128:131], a[192:195], v[32:35]\n"
        "ds_read_b64_tr_b16 a[52:53], v159 offset:16384\n"
        "v_mfma_f32_16x16x32_bf16 v[32:35], a[136:139], a[196:199], v[32:35]\n"
        "ds_read_b64_tr_b16 a[54:55], v159 offset:20480\n"
        "v_mfma_f32_16x16x32_bf16 v[36:39], a[140:143], a[196:199], v[36:39]\n"
        "ds_read_b64_tr_b16 a[60:61], v159 offset:24576\n"
        "global_load_lds_dwordx4 v254, s[20:21] offset:2048 sc1\n"
        "v_mfma_f32_16x16x32_bf16 v[36:39], a[132:135], a[192:195], v[36:39]\n"
        "ds_read_b64_tr_b16 a[62:63], v159 offset:28672\n"
        "v_mfma_f32_16x16x32_bf16 v[44:47], a[132:135], a[200:203], v[44:47]\n"
        "ds_read_b128 a[0:3], v148 offset:32768\n"
        "v_mfma_f32_16x16x32_bf16 v[44:47], a[140:143], a[204:207], v[44:47]\n"
        "ds_read_b128 a[4:7], v148 offset:40960\n"
        "global_load_lds_dwordx4 v254, s[20:21] offset:3072 sc1\n"
        "s_add_u32 m0, s73, 0x1c000\n"
        "v_mfma_f32_16x16x32_bf16 v[40:43], a[136:139], a[204:207], v[40:43]\n"
        "ds_read_b128 a[8:11], v148 offset:33792\n"
        "s_add_u32 s20, s20, 0x4000\n"
        "s_addc_u32 s21, s21, 0\n"
        "v_mfma_f32_16x16x32_bf16 v[40:43], a[128:131], a[200:203], v[40:43]\n"
        "ds_read_b128 a[12:15], v148 offset:41984\n"
        "v_mfma_f32_16x16x32_bf16 v[48:51], a[128:131], a[208:211], v[48:51]\n"
        "ds_read_b128 a[16:19], v148 offset:34816\n"
        "global_load_lds_dwordx4 v144, s[60:61] offset:0\n"
        "v_mfma_f32_16x16x32_bf16 v[48:51], a[136:139], a[212:215], v[48:51]\n"
        "ds_read_b128 a[20:23], v148 offset:43008\n"
        "v_mfma_f32_16x16x32_bf16 v[52:55], a[140:143], a[212:215], v[52:55]\n"
        "ds_read_b128 a[24:27], v148 offset:35840\n"
        "v_mfma_f32_16x16x32_bf16 v[52:55], a[132:135], a[208:211], v[52:55]\n"
        "ds_read_b128 a[28:31], v148 offset:44032\n"
        "global_load_lds_dwordx4 v145, s[60:61] offset:1024\n"
        "v_mfma_f32_16x16x32_bf16 v[60:63], a[132:135], a[216:219], v[60:63]\n"
        "ds_read_b128 a[192:195], v151 offset:32768\n"
        "s_waitcnt lgkmcnt(8)\n"
        "v_mfma_f32_16x16x32_bf16 v[60:63], a[140:143], a[220:223], v[60:63]\n"
        "ds_read_b128 a[196:199], v151 offset:40960\n"
        "s_waitcnt lgkmcnt(8)\n"
        "v_mfma_f32_16x16x32_bf16 v[56:59], a[136:139], a[220:223], v[56:59]\n"
        "global_load_lds_dwordx4 v146, s[60:61] offset:2048\n"
        "v_mfma_f32_16x16x32_bf16 v[56:59], a[128:131], a[216:219], v[56:59]\n"
        "ds_read_b128 a[200:203], v151 offset:33792\n"
        "s_waitcnt lgkmcnt(7)\n"
        "v_mfma_f32_16x16x32_bf16 v[0:3], a[48:51], a[0:3], v[0:3]\n"
        "ds_read_b128 a[204:207], v151 offset:41984\n"
        "s_waitcnt lgkmcnt(7)\n"
        "v_mfma_f32_16x16x32_bf16 v[0:3], a[56:59], a[4:7], v[0:3]\n"
        "global_load_lds_dwordx4 v147, s[60:61] offset:3072\n"
        "s_add_u32 m0, s73, 0x20000\n"
        "v_mfma_f32_16x16x32_bf16 v[4:7], a[60:63], a[4:7], v[4:7]\n"
        "ds_read_b128 a[208:211], v151 offset:34816\n"
        "s_add_u32 s60, s22, 0x380000\n"
        "s_addc_u32 s61, s23, 0\n"
        "v_mfma_f32_16x16x32_bf16 v[4:7], a[52:55], a[0:3], v[4:7]\n"
        "ds_read_b128 a[212:215], v151 offset:43008\n"
        "s_waitcnt lgkmcnt(6)\n"
        "v_mfma_f32_16x16x32_bf16 v[12:15], a[52:55], a[8:11], v[12:15]\n"
        "ds_read_b128 a[216:219], v151 offset:35840\n"
        "v_mfma_f32_16x16x32_bf16 v[12:15], a[60:63], a[12:15], v[12:15]\n"
        "ds_read_b128 a[220:223], v151 offset:44032\n"
        "s_waitcnt lgkmcnt(6)\n"
        "v_mfma_f32_16x16x32_bf16 v[8:11], a[56:59], a[12:15], v[8:11]\n"
        "v_mfma_f32_16x16x32_bf16 v[8:11], a[48:51], a[8:11], v[8:11]\n"
        "v_mfma_f32_16x16x32_bf16 v[16:19], a[48:51], a[16:19], v[16:19]\n"
        "v_mfma_f32_16x16x32_bf16 v[16:19], a[56:59], a[20:23], v[16:19]\n"
        "s_waitcnt lgkmcnt(4)\n"
        "v_mfma_f32_16x16x32_bf16 v[20:23], a[60:63], a[20:23], v[20:23]\n"
        "v_mfma_f32_16x16x32_bf16 v[20:23], a[52:55], a[16:19], v[20:23]\n"
        "s_waitcnt vmcnt(20)\n"
        "s_barrier\n"
        "v_mfma_f32_16x16x32_bf16 v[28:31], a[52:55], a[24:27], v[28:31]\n"
        "global_load_lds_dwordx4 v254, s[20:21] offset:0 sc1\n"
        "ds_read_b64_tr_b16 a[224:225], v154 offset:49152\n"
        "v_mfma_f32_16x16x32_bf16 v[28:31], a[60:63], a[28:31], v[28:31]\n"
        "ds_read_b64_tr_b16 a[226:227], v154 offset:53248\n"
        "s_waitcnt lgkmcnt(2)\n"
        "v_mfma_f32_16x16x32_bf16 v[24:27], a[56:59], a[28:31], v[24:27]\n"
        "ds_read_b64_tr_b16 a[232:233], v154 offset:57344\n"
        "v_mfma_f32_16x16x32_bf16 v[24:27], a[48:51], a[24:27], v[24:27]\n"
        "ds_read_b64_tr_b16 a[234:235], v154 offset:61440\n"
        "global_load_lds_dwordx4 v254, s[20:21] offset:1024 sc1\n"
        "v_mfma_f32_16x16x32_bf16 v[32:35], a[48:51], a[192:195], v[32:35]\n"
        "ds_read_b64_tr_b16 a[228:229], v155 offset:49152\n"
        "v_mfma_f32_16x16x32_bf16 v[32:35], a[56:59], a[196:199], v[32:35]\n"
        "ds_read_b64_tr_b16 a[230:231], v155 offset:53248\n"
        "v_mfma_f32_16x16x32_bf16 v[36:39], a[60:63], a[196:199], v[36:39]\n"
        "ds_read_b64_tr_b16 a[236:237], v155 offset:57344\n"
        "global_load_lds_dwordx4 v254, s[20:21] offset:2048 sc1\n"
        "v_mfma_f32_16x16x32_bf16 v[36:39], a[52:55], a[192:195], v[36:39]\n"
        "ds_read_b64_tr_b16 a[238:239], v155 offset:61440\n"
        "v_mfma_f32_16x16x32_bf16 v[44:47], a[52:55], a[200:203], v[44:47]\n"
        "ds_read_b128 a[0:3], v149 offset:0\n"
        "v_cmp_ne_u64 vcc, v[236:237], v[250:251]\n"
        "v_mfma_f32_16x16x32_bf16 v[44:47], a[60:63], a[204:207], v[44:47]\n"
        "ds_read_b128 a[4:7], v149 offset:8192\n"
        "global_load_lds_dwordx4 v254, s[20:21] offset:3072 sc1\n"
        "s_add_u32 m0, s73, 0x24000\n"
        "v_mfma_f32_16x16x32_bf16 v[40:43], a[56:59], a[204:207], v[40:43]\n"
        "ds_read_b128 a[8:11], v149 offset:1024\n"
        "s_add_u32 s20, s20, 0x4000\n"
        "s_addc_u32 s21, s21, 0\n"
        "v_mfma_f32_16x16x32_bf16 v[40:43], a[48:51], a[200:203], v[40:43]\n"
        "ds_read_b128 a[12:15], v149 offset:9216\n"
        "s_cbranch_vccnz LPZZ_%=\n"
        "LBZZ_%=:\n"
        "v_lshrrev_b32 v234, 1, v254\n"
        "v_mfma_f32_16x16x32_bf16 v[48:51], a[48:51], a[208:211], v[48:51]\n"
        "ds_read_b128 a[16:19], v149 offset:2048\n"
        "v_add_u32 v234, s14, v234\n"
        "v_mfma_f32_16x16x32_bf16 v[48:51], a[56:59], a[212:215], v[48:51]\n"
        "ds_read_b128 a[20:23], v149 offset:10240\n"
        "s_xor_b32 s14, s14, 0x200\n"
        "v_mov_b32 v235, s14\n"
        "v_mfma_f32_16x16x32_bf16 v[52:55], a[60:63], a[212:215], v[52:55]\n"
        "ds_read_b128 a[24:27], v149 offset:3072\n"
        "v_mfma_f32_16x16x32_bf16 v[52:55], a[52:55], a[208:211], v[52:55]\n"
        "ds_read_b128 a[28:31], v149 offset:11264\n"
        "s_waitcnt lgkmcnt(8)\n"
        "v_mfma_f32_16x16x32_bf16 v[60:63], a[52:55], a[216:219], v[60:63]\n"
        "ds_read_b128 a[192:195], v152 offset:0\n"
        "s_waitcnt lgkmcnt(8)\n"
        "v_mfma_f32_16x16x32_bf16 v[60:63], a[60:63], a[220:223], v[60:63]\n"
        "ds_read_b128 a[196:199], v152 offset:8192\n"
        "s_waitcnt lgkmcnt(8)\n"
        "v_mfma_f32_16x16x32_bf16 v[56:59], a[56:59], a[220:223], v[56:59]\n"
        "ds_read_b128 a[200:203], v152 offset:1024\n"
        "LHB5_%=:\n"
        "s_cbranch_execz LHRB_%=\n"
        "s_waitcnt lgkmcnt(8)\n"
        "v_mfma_f32_16x16x32_bf16 v[56:59], a[48:51], a[216:219], v[56:59]\n"
        "ds_read_b128 a[204:207], v152 offset:9216\n"
        "s_waitcnt lgkmcnt(8)\n"
        "v_mfma_f32_16x16x32_bf16 v[0:3], a[224:227], a[0:3], v[0:3]\n"
        "ds_read_b128 a[208:211], v152 offset:2048\n"
        "s_waitcnt lgkmcnt(8)\n"
        "v_mfma_f32_16x16x32_bf16 v[0:3], a[232:235], a[4:7], v[0:3]\n"
        "ds_read_b128 a[212:215], v152 offset:10240\n"
        "s_waitcnt lgkmcnt(8)\n"
        "v_mfma_f32_16x16x32_bf16 v[4:7], a[236:239], a[4:7], v[4:7]\n"
        "ds_read_b128 a[216:219], v152 offset:3072\n"
        "s_waitcnt lgkmcnt(8)\n"
        "v_mfma_f32_16x16x32_bf16 v[4:7], a[228:231], a[0:3], v[4:7]\n"
        "ds_read_b128 a[220:223], v152 offset:11264\n"
        "s_waitcnt lgkmcnt(8)\n"
        "v_mfma_f32_16x16x32_bf16 v[12:15], a[228:231], a[8:11], v[12:15]\n"
        "v_mfma_f32_16x16x32_bf16 v[12:15], a[236:239], a[12:15], v[12:15]\n"
        "s_waitcnt lgkmcnt(6)\n"
        "v_mfma_f32_16x16x32_bf16 v[8:11], a[232:235], a[12:15], v[8:11]\n"
        "v_mfma_f32_16x16x32_bf16 v[8:11], a[224:227], a[8:11], v[8:11]\n"
        "v_mfma_f32_16x16x32_bf16 v[16:19], a[224:227], a[16:19], v[16:19]\n"
        "v_mfma_f32_16x16x32_bf16 v[16:19], a[232:235], a[20:23], v[16:19]\n"
        "s_waitcnt lgkmcnt(2)\n"
        "v_mfma_f32_16x16x32_bf16 v[20:23], a[236:239], a[20:23], v[20:23]\n"
        "v_mfma_f32_16x16x32_bf16 v[20:23], a[228:231], a[16:19], v[20:23]\n"
        "s_waitcnt vmcnt(20)\n"
        "s_barrier\n"
        "v_mfma_f32_16x16x32_bf16 v[28:31], a[228:231], a[24:27], v[28:31]\n"
        "global_load_lds_dwordx4 v144, s[60:61] offset:0\n"
        "ds_read_b128 a[0:3], v149 offset:16384\n"
        "v_mfma_f32_16x16x32_bf16 v[28:31], a[236:239], a[28:31], v[28:31]\n"
        "ds_read_b128 a[4:7], v149 offset:24576\n"
        "v_mfma_f32_16x16x32_bf16 v[24:27], a[232:235], a[28:31], v[24:27]\n"
        "ds_read_b128 a[8:11], v149 offset:17408\n"
        "v_mfma_f32_16x16x32_bf16 v[24:27], a[224:227], a[24:27], v[24:27]\n"
        "global_load_lds_dwordx4 v145, s[60:61] offset:1024\n"
        "ds_read_b128 a[12:15], v149 offset:25600\n"
        "v_mfma_f32_16x16x32_bf16 v[32:35], a[224:227], a[192:195], v[32:35]\n"
        "ds_read_b128 a[16:19], v149 offset:18432\n"
        "v_mfma_f32_16x16x32_bf16 v[32:35], a[232:235], a[196:199], v[32:35]\n"
        "ds_read_b128 a[20:23], v149 offset:26624\n"
        "s_waitcnt lgkmcnt(6)\n"
        "v_mfma_f32_16x16x32_bf16 v[36:39], a[236:239], a[196:199], v[36:39]\n"
        "global_load_lds_dwordx4 v146, s[60:61] offset:2048\n"
        "ds_read_b128 a[24:27], v149 offset:19456\n"
        "v_mfma_f32_16x16x32_bf16 v[36:39], a[228:231], a[192:195], v[36:39]\n"
        "ds_read_b128 a[28:31], v149 offset:27648\n"
        "v_mfma_f32_16x16x32_bf16 v[44:47], a[228:231], a[200:203], v[44:47]\n"
        "ds_read_b128 a[192:195], v152 offset:16384\n"
        "v_mfma_f32_16x16x32_bf16 v[44:47], a[236:239], a[204:207], v[44:47]\n"
        "global_load_lds_dwordx4 v147, s[60:61] offset:3072\n"
        "s_add_u32 m0, s73, 0x8000\n"
        "s_waitcnt lgkmcnt(7)\n"
        "v_mfma_f32_16x16x32_bf16 v[40:43], a[232:235], a[204:207], v[40:43]\n"
        "ds_read_b128 a[196:199], v152 offset:24576\n"
        "s_add_u32 s60, s22, 0x3c0000\n"
        "s_addc_u32 s61, s23, 0\n"
        "v_mfma_f32_16x16x32_bf16 v[40:43], a[224:227], a[200:203], v[40:43]\n"
        "ds_read_b128 a[200:203], v152 offset:17408\n"
        "v_mfma_f32_16x16x32_bf16 v[48:51], a[224:227], a[208:211], v[48:51]\n"
        "global_load_lds_dwordx4 v254, s[20:21] offset:0 sc1\n"
        "ds_read_b128 a[204:207], v152 offset:25600\n"
        "v_mfma_f32_16x16x32_bf16 v[48:51], a[232:235], a[212:215], v[48:51]\n"
        "s_waitcnt lgkmcnt(6)\n"
        "v_mfma_f32_16x16x32_bf16 v[52:55], a[236:239], a[212:215], v[52:55]\n"
        "ds_read_b128 a[212:215], v152 offset:26624\n"
        "v_mfma_f32_16x16x32_bf16 v[52:55], a[228:231], a[208:211], v[52:55]\n"
        "global_load_lds_dwordx4 v254, s[20:21] offset:1024 sc1\n"
        "ds_read_b128 a[208:211], v152 offset:18432\n"
        "v_mfma_f32_16x16x32_bf16 v[60:63], a[228:231], a[216:219], v[60:63]\n"
        "v_mfma_f32_16x16x32_bf16 v[60:63], a[236:239], a[220:223], v[60:63]\n"
        "v_mfma_f32_16x16x32_bf16 v[56:59], a[232:235], a[220:223], v[56:59]\n"
        "global_load_lds_dwordx4 v254, s[20:21] offset:2048 sc1\n"
        "ds_read_b128 a[220:223], v152 offset:27648\n"
        "v_mfma_f32_16x16x32_bf16 v[56:59], a[224:227], a[216:219], v[56:59]\n"
        "ds_read_b128 a[216:219], v152 offset:19456\n"
        "v_mfma_f32_16x16x32_bf16 v[0:3], a[144:147], a[0:3], v[0:3]\n"
        "v_mfma_f32_16x16x32_bf16 v[0:3], a[152:155], a[4:7], v[0:3]\n"
        "global_load_lds_dwordx4 v254, s[20:21] offset:3072 sc1\n"
        "s_add_u32 s20, s20, 0x4000\n"
        "s_addc_u32 s21, s21, 0\n"
        "v_mfma_f32_16x16x32_bf16 v[4:7], a[156:159], a[4:7], v[4:7]\n"
        "s_add_u32 m0, s73, 0xc000\n"
        "s_waitcnt lgkmcnt(3)\n"
        "v_mfma_f32_16x16x32_bf16 v[4:7], a[148:151], a[0:3], v[4:7]\n"
        "v_mfma_f32_16x16x32_bf16 v[12:15], a[148:151], a[8:11], v[12:15]\n"
        "v_mfma_f32_16x16x32_bf16 v[12:15], a[156:159], a[12:15], v[12:15]\n"
        "v_mfma_f32_16x16x32_bf16 v[8:11], a[152:155], a[12:15], v[8:11]\n"
        "v_mfma_f32_16x16x32_bf16 v[8:11], a[144:147], a[8:11], v[8:11]\n"
        "v_mfma_f32_16x16x32_bf16 v[16:19], a[144:147], a[16:19], v[16:19]\n"
        "v_mfma_f32_16x16x32_bf16 v[16:19], a[152:155], a[20:23], v[16:19]\n"
        "v_mfma_f32_16x16x32_bf16 v[20:23], a[156:159], a[20:23], v[20:23]\n"
        "v_mfma_f32_16x16x32_bf16 v[20:23], a[148:151], a[16:19], v[20:23]\n"
        "s_waitcnt vmcnt(20)\n"
        "s_barrier\n"
        "v_mfma_f32_16x16x32_bf16 v[28:31], a[148:151], a[24:27], v[28:31]\n"
        "global_load_lds_dwordx4 v254, s[20:21] offset:0 sc1\n"
        "ds_read_b64_tr_b16 a[240:241], v154 offset:0\n"
        "v_mfma_f32_16x16x32_bf16 v[28:31], a[156:159], a[28:31], v[28:31]\n"
        "ds_read_b64_tr_b16 a[242:243], v154 offset:4096\n"
        "s_waitcnt lgkmcnt(2)\n"
        "v_mfma_f32_16x16x32_bf16 v[24:27], a[152:155], a[28:31], v[24:27]\n"
        "ds_read_b64_tr_b16 a[248:249], v154 offset:8192\n"
        "v_mfma_f32_16x16x32_bf16 v[24:27], a[144:147], a[24:27], v[24:27]\n"
        "ds_read_b64_tr_b16 a[250:251], v154 offset:12288\n"
        "global_load_lds_dwordx4 v254, s[20:21] offset:1024 sc1\n"
        "v_mfma_f32_16x16x32_bf16 v[32:35], a[144:147], a[192:195], v[32:35]\n"
        "ds_read_b64_tr_b16 a[244:245], v155 offset:0\n"
        "v_mfma_f32_16x16x32_bf16 v[32:35], a[152:155], a[196:199], v[32:35]\n"
        "ds_read_b64_tr_b16 a[246:247], v155 offset:4096\n"
        "v_mfma_f32_16x16x32_bf16 v[36:39], a[156:159], a[196:199], v[36:39]\n"
        "ds_read_b64_tr_b16 a[252:253], v155 offset:8192\n"
        "global_load_lds_dwordx4 v254, s[20:21] offset:2048 sc1\n"
        "v_mfma_f32_16x16x32_bf16 v[36:39], a[148:151], a[192:195], v[36:39]\n"
        "ds_read_b64_tr_b16 a[254:255], v155 offset:12288\n"
        "v_mfma_f32_16x16x32_bf16 v[44:47], a[148:151], a[200:203], v[44:47]\n"
        "ds_read_b128 a[0:3], v148 offset:16384\n"
        "v_mfma_f32_16x16x32_bf16 v[44:47], a[156:159], a[204:207], v[44:47]\n"
        "ds_read_b128 a[4:7], v148 offset:24576\n"
        "global_load_lds_dwordx4 v254, s[20:21] offset:3072 sc1\n"
        "s_add_u32 m0, s73, 0x10000\n"
        "v_mfma_f32_16x16x32_bf16 v[40:43], a[152:155], a[204:207], v[40:43]\n"
        "ds_read_b128 a[8:11], v148 offset:17408\n"
        "s_add_u32 s20, s20, 0x4000\n"
        "s_addc_u32 s21, s21, 0\n"
        "v_mfma_f32_16x16x32_bf16 v[40:43], a[144:147], a[200:203], v[40:43]\n"
        "ds_read_b128 a[12:15], v148 offset:25600\n"
        "v_mfma_f32_16x16x32_bf16 v[48:51], a[144:147], a[208:211], v[48:51]\n"
        "ds_read_b128 a[16:19], v148 offset:18432\n"
        "global_load_lds_dwordx4 v144, s[60:61] offset:0\n"
        "v_mfma_f32_16x16x32_bf16 v[48:51], a[152:155], a[212:215], v[48:51]\n"
        "ds_read_b128 a[20:23], v148 offset:26624\n"
        "v_mfma_f32_16x16x32_bf16 v[52:55], a[156:159], a[212:215], v[52:55]\n"
        "ds_read_b128 a[24:27], v148 offset:19456\n"
        "v_mfma_f32_16x16x32_bf16 v[52:55], a[148:151], a[208:211], v[52:55]\n"
        "ds_read_b128 a[28:31], v148 offset:27648\n"
        "global_load_lds_dwordx4 v145, s[60:61] offset:1024\n"
        "v_mfma_f32_16x16x32_bf16 v[60:63], a[148:151], a[216:219], v[60:63]\n"
        "ds_read_b128 a[192:195], v151 offset:16384\n"
        "s_waitcnt lgkmcnt(8)\n"
        "v_mfma_f32_16x16x32_bf16 v[60:63], a[156:159], a[220:223], v[60:63]\n"
        "ds_read_b128 a[196:199], v151 offset:24576\n"
        "s_waitcnt lgkmcnt(8)\n"
        "v_mfma_f32_16x16x32_bf16 v[56:59], a[152:155], a[220:223], v[56:59]\n"
        "global_load_lds_dwordx4 v146, s[60:61] offset:2048\n"
        "v_mfma_f32_16x16x32_bf16 v[56:59], a[144:147], a[216:219], v[56:59]\n"
        "ds_read_b128 a[200:203], v151 offset:17408\n"
        "s_waitcnt lgkmcnt(7)\n"
        "v_mfma_f32_16x16x32_bf16 v[0:3], a[240:243], a[0:3], v[0:3]\n"
        "ds_read_b128 a[204:207], v151 offset:25600\n"
        "s_waitcnt lgkmcnt(7)\n"
        "v_mfma_f32_16x16x32_bf16 v[0:3], a[248:251], a[4:7], v[0:3]\n"
        "global_load_lds_dwordx4 v147, s[60:61] offset:3072\n"
        "s_add_u32 m0, s73, 0x14000\n"
        "v_mfma_f32_16x16x32_bf16 v[4:7], a[252:255], a[4:7], v[4:7]\n"
        "ds_read_b128 a[208:211], v151 offset:18432\n"
        "s_add_u32 s60, s22, 0x3e0000\n"
        "s_addc_u32 s61, s23, 0\n"
        "v_mfma_f32_16x16x32_bf16 v[4:7], a[244:247], a[0:3], v[4:7]\n"
        "ds_read_b128 a[212:215], v151 offset:26624\n"
        "s_waitcnt lgkmcnt(6)\n"
        "v_mfma_f32_16x16x32_bf16 v[12:15], a[244:247], a[8:11], v[12:15]\n"
        "ds_read_b128 a[216:219], v151 offset:19456\n"
        "v_mfma_f32_16x16x32_bf16 v[12:15], a[252:255], a[12:15], v[12:15]\n"
        "ds_read_b128 a[220:223], v151 offset:27648\n"
        "s_waitcnt lgkmcnt(6)\n"
        "v_mfma_f32_16x16x32_bf16 v[8:11], a[248:251], a[12:15], v[8:11]\n"
        "v_mfma_f32_16x16x32_bf16 v[8:11], a[240:243], a[8:11], v[8:11]\n"
        "v_mfma_f32_16x16x32_bf16 v[16:19], a[240:243], a[16:19], v[16:19]\n"
        "v_mfma_f32_16x16x32_bf16 v[16:19], a[248:251], a[20:23], v[16:19]\n"
        "s_waitcnt lgkmcnt(4)\n"
        "v_mfma_f32_16x16x32_bf16 v[20:23], a[252:255], a[20:23], v[20:23]\n"
        "v_mfma_f32_16x16x32_bf16 v[20:23], a[244:247], a[16:19], v[20:23]\n"
        "s_waitcnt vmcnt(24)\n"
        "s_barrier\n"
        "v_mfma_f32_16x16x32_bf16 v[28:31], a[244:247], a[24:27], v[28:31]\n"
        "global_load_lds_dwordx4 v254, s[20:21] offset:0 sc1\n"
        "ds_read_b128 a[0:3], v149 offset:32768\n"
        "v_mfma_f32_16x16x32_bf16 v[28:31], a[252:255], a[28:31], v[28:31]\n"
        "ds_read_b128 a[4:7], v149 offset:40960\n"
        "s_waitcnt lgkmcnt(2)\n"
        "v_mfma_f32_16x16x32_bf16 v[24:27], a[248:251], a[28:31], v[24:27]\n"
        "ds_read_b128 a[8:11], v149 offset:33792\n"
        "v_mfma_f32_16x16x32_bf16 v[24:27], a[240:243], a[24:27], v[24:27]\n"
        "global_load_lds_dwordx4 v254, s[20:21] offset:1024 sc1\n"
        "ds_read_b128 a[12:15], v149 offset:41984\n"
        "v_mfma_f32_16x16x32_bf16 v[32:35], a[240:243], a[192:195], v[32:35]\n"
        "ds_read_b128 a[16:19], v149 offset:34816\n"
        "v_mfma_f32_16x16x32_bf16 v[32:35], a[248:251], a[196:199], v[32:35]\n"
        "ds_read_b128 a[20:23], v149 offset:43008\n"
        "v_mfma_f32_16x16x32_bf16 v[36:39], a[252:255], a[196:199], v[36:39]\n"
        "global_load_lds_dwordx4 v254, s[20:21] offset:2048 sc1\n"
        "ds_read_b128 a[24:27], v149 offset:35840\n"
        "v_mfma_f32_16x16x32_bf16 v[36:39], a[244:247], a[192:195], v[36:39]\n"
        "ds_read_b128 a[28:31], v149 offset:44032\n"
        "v_mfma_f32_16x16x32_bf16 v[44:47], a[244:247], a[200:203], v[44:47]\n"
        "ds_read_b128 a[192:195], v152 offset:32768\n"
        "v_mfma_f32_16x16x32_bf16 v[44:47], a[252:255], a[204:207], v[44:47]\n"
        "global_load_lds_dwordx4 v254, s[20:21] offset:3072 sc1\n"
        "s_add_u32 s20, s20, 0x4000\n"
        "s_addc_u32 s21, s21, 0\n"
        "v_mfma_f32_16x16x32_bf16 v[40:43], a[248:251], a[204:207], v[40:43]\n"
        "ds_read_b128 a[196:199], v152 offset:40960\n"
        "s_add_u32 m0, s73, 0x0\n"
        "s_waitcnt lgkmcnt(7)\n"
        "v_mfma_f32_16x16x32_bf16 v[40:43], a[240:243], a[200:203], v[40:43]\n"
        "ds_read_b128 a[200:203], v152 offset:33792\n"
        "v_mfma_f32_16x16x32_bf16 v[48:51], a[240:243], a[208:211], v[48:51]\n"
        "ds_read_b128 a[204:207], v152 offset:41984\n"
        "v_mfma_f32_16x16x32_bf16 v[48:51], a[248:251], a[212:215], v[48:51]\n"
        "v_mfma_f32_16x16x32_bf16 v[52:55], a[252:255], a[212:215], v[52:55]\n"
        "ds_read_b128 a[212:215], v152 offset:43008\n"
        "v_mfma_f32_16x16x32_bf16 v[52:55], a[244:247], a[208:211], v[52:55]\n"
        "ds_read_b128 a[208:211], v152 offset:34816\n"
        "s_waitcnt lgkmcnt(6)\n"
        "v_mfma_f32_16x16x32_bf16 v[60:63], a[244:247], a[216:219], v[60:63]\n"
        "v_mfma_f32_16x16x32_bf16 v[60:63], a[252:255], a[220:223], v[60:63]\n"
        "v_mfma_f32_16x16x32_bf16 v[56:59], a[248:251], a[220:223], v[56:59]\n"
        "ds_read_b128 a[220:223], v152 offset:44032\n"
        "v_mfma_f32_16x16x32_bf16 v[56:59], a[240:243], a[216:219], v[56:59]\n"
        "ds_read_b128 a[216:219], v152 offset:35840\n"
        "v_mfma_f32_16x16x32_bf16 v[0:3], a[160:163], a[0:3], v[0:3]\n"
        "v_mfma_f32_16x16x32_bf16 v[0:3], a[168:171], a[4:7], v[0:3]\n"
        "v_mfma_f32_16x16x32_bf16 v[4:7], a[172:175], a[4:7], v[4:7]\n"
        "v_mfma_f32_16x16x32_bf16 v[4:7], a[164:167], a[0:3], v[4:7]\n"
        "v_mfma_f32_16x16x32_bf16 v[12:15], a[164:167], a[8:11], v[12:15]\n"
        "v_mfma_f32_16x16x32_bf16 v[12:15], a[172:175], a[12:15], v[12:15]\n"
        "s_waitcnt lgkmcnt(2)\n"
        "v_mfma_f32_16x16x32_bf16 v[8:11], a[168:171], a[12:15], v[8:11]\n"
        "v_mfma_f32_16x16x32_bf16 v[8:11], a[160:163], a[8:11], v[8:11]\n"
        "v_mfma_f32_16x16x32_bf16 v[16:19], a[160:163], a[16:19], v[16:19]\n"
        "v_mfma_f32_16x16x32_bf16 v[20:23], a[172:175], a[20:23], v[20:23]\n"
        "v_mfma_f32_16x16x32_bf16 v[20:23], a[164:167], a[16:19], v[20:23]\n"
        "s_waitcnt vmcnt(20)\n"
        "s_barrier\n"
        "v_mfma_f32_16x16x32_bf16 v[16:19], a[168:171], a[20:23], v[16:19]\n"
        "global_load_lds_dwordx4 v144, s[60:61] offset:0\n"
        "ds_read_b64_tr_b16 a[32:33], v156 offset:49152\n"
        "v_mfma_f32_16x16x32_bf16 v[28:31], a[164:167], a[24:27], v[28:31]\n"
        "ds_read_b64_tr_b16 a[34:35], v156 offset:53248\n"
        "v_mfma_f32_16x16x32_bf16 v[28:31], a[172:175], a[28:31], v[28:31]\n"
        "ds_read_b64_tr_b16 a[40:41], v156 offset:57344\n"
        "v_mfma_f32_16x16x32_bf16 v[24:27], a[168:171], a[28:31], v[24:27]\n"
        "global_load_lds_dwordx4 v145, s[60:61] offset:1024\n"
        "ds_read_b64_tr_b16 a[42:43], v156 offset:61440\n"
        "v_mfma_f32_16x16x32_bf16 v[24:27], a[160:163], a[24:27], v[24:27]\n"
        "ds_read_b64_tr_b16 a[36:37], v157 offset:49152\n"
        "v_mfma_f32_16x16x32_bf16 v[32:35], a[160:163], a[192:195], v[32:35]\n"
        "ds_read_b64_tr_b16 a[38:39], v157 offset:53248\n"
        "v_mfma_f32_16x16x32_bf16 v[32:35], a[168:171], a[196:199], v[32:35]\n"
        "global_load_lds_dwordx4 v146, s[60:61] offset:2048\n"
        "ds_read_b64_tr_b16 a[44:45], v157 offset:57344\n"
        "v_mfma_f32_16x16x32_bf16 v[36:39], a[172:175], a[196:199], v[36:39]\n"
        "ds_read_b64_tr_b16 a[46:47], v157 offset:61440\n"
        "s_waitcnt lgkmcnt(8)\n"
        "v_mfma_f32_16x16x32_bf16 v[36:39], a[164:167], a[192:195], v[36:39]\n"
        "ds_read_b128 a[0:3], v150 offset:0\n"
        "v_mfma_f32_16x16x32_bf16 v[44:47], a[164:167], a[200:203], v[44:47]\n"
        "global_load_lds_dwordx4 v147, s[60:61] offset:3072\n"
        "s_add_u32 m0, s73, 0x4000\n"
        "v_mfma_f32_16x16x32_bf16 v[44:47], a[172:175], a[204:207], v[44:47]\n"
        "ds_read_b128 a[4:7], v150 offset:8192\n"
        "v_mfma_f32_16x16x32_bf16 v[40:43], a[168:171], a[204:207], v[40:43]\n"
        "ds_read_b128 a[8:11], v150 offset:1024\n"
        "v_mfma_f32_16x16x32_bf16 v[40:43], a[160:163], a[200:203], v[40:43]\n"
        "global_load_lds_dwordx4 v254, s[20:21] offset:0 sc1\n"
        "ds_read_b128 a[12:15], v150 offset:9216\n"
        "v_mfma_f32_16x16x32_bf16 v[48:51], a[160:163], a[208:211], v[48:51]\n"
        "ds_read_b128 a[16:19], v150 offset:2048\n"
        "v_mfma_f32_16x16x32_bf16 v[48:51], a[168:171], a[212:215], v[48:51]\n"
        "ds_read_b128 a[20:23], v150 offset:10240\n"
        "v_mfma_f32_16x16x32_bf16 v[52:55], a[172:175], a[212:215], v[52:55]\n"
        "global_load_lds_dwordx4 v254, s[20:21] offset:1024 sc1\n"
        "ds_read_b128 a[24:27], v150 offset:3072\n"
        "v_mfma_f32_16x16x32_bf16 v[52:55], a[164:167], a[208:211], v[52:55]\n"
        "ds_read_b128 a[28:31], v150 offset:11264\n"
        "s_waitcnt lgkmcnt(7)\n"
        "v_mfma_f32_16x16x32_bf16 v[60:63], a[164:167], a[216:219], v[60:63]\n"
        "ds_read_b128 a[192:195], v153 offset:0\n"
        "v_mfma_f32_16x16x32_bf16 v[60:63], a[172:175], a[220:223], v[60:63]\n"
        "global_load_lds_dwordx4 v254, s[20:21] offset:2048 sc1\n"
        "ds_read_b128 a[196:199], v153 offset:8192\n"
        "v_mfma_f32_16x16x32_bf16 v[56:59], a[168:171], a[220:223], v[56:59]\n"
        "ds_read_b128 a[200:203], v153 offset:1024\n"
        "s_waitcnt lgkmcnt(8)\n"
        "v_mfma_f32_16x16x32_bf16 v[56:59], a[160:163], a[216:219], v[56:59]\n"
        "ds_read_b128 a[204:207], v153 offset:9216\n"
        "s_waitcnt lgkmcnt(8)\n"
        "v_mfma_f32_16x16x32_bf16 v[0:3], a[32:35], a[0:3], v[0:3]\n"
        "global_load_lds_dwordx4 v254, s[20:21] offset:3072 sc1\n"
        "ds_read_b128 a[208:211], v153 offset:2048\n"
        "s_add_u32 m0, s73, 0x18000\n"
        "v_mfma_f32_16x16x32_bf16 v[0:3], a[40:43], a[4:7], v[0:3]\n"
        "ds_read_b128 a[212:215], v153 offset:10240\n"
        "s_waitcnt lgkmcnt(8)\n"
        "v_mfma_f32_16x16x32_bf16 v[4:7], a[44:47], a[4:7], v[4:7]\n"
        "ds_read_b128 a[216:219], v153 offset:3072\n"
        "s_waitcnt lgkmcnt(8)\n"
        "v_mfma_f32_16x16x32_bf16 v[4:7], a[36:39], a[0:3], v[4:7]\n"
        "ds_read_b128 a[220:223], v153 offset:11264\n"
        "s_waitcnt lgkmcnt(8)\n"
        "v_mfma_f32_16x16x32_bf16 v[12:15], a[36:39], a[8:11], v[12:15]\n"
        "v_mfma_f32_16x16x32_bf16 v[12:15], a[44:47], a[12:15], v[12:15]\n"
        "s_waitcnt lgkmcnt(7)\n"
        "v_mfma_f32_16x16x32_bf16 v[8:11], a[40:43], a[12:15], v[8:11]\n"
        "v_mfma_f32_16x16x32_bf16 v[8:11], a[32:35], a[8:11], v[8:11]\n"
        "s_waitcnt lgkmcnt(4)\n"
        "v_mfma_f32_16x16x32_bf16 v[16:19], a[32:35], a[16:19], v[16:19]\n"
        "v_mfma_f32_16x16x32_bf16 v[16:19], a[40:43], a[20:23], v[16:19]\n"
        "v_mfma_f32_16x16x32_bf16 v[20:23], a[44:47], a[20:23], v[20:23]\n"
        "v_mfma_f32_16x16x32_bf16 v[20:23], a[36:39], a[16:19], v[20:23]\n"
        "s_waitcnt vmcnt(20)\n"
        "s_barrier\n"
        "v_mfma_f32_16x16x32_bf16 v[28:31], a[36:39], a[24:27], v[28:31]\n"
        "global_load_lds_dwordx4 v224, s[58:59] offset:0 sc1\n"
        "ds_read_b64_tr_b16 a[48:49], v158 offset:16384\n"
        "s_add_u32 m0, s73, 0x18400\n"
        "v_mfma_f32_16x16x32_bf16 v[28:31], a[44:47], a[28:31], v[28:31]\n"
        "ds_read_b64_tr_b16 a[50:51], v158 offset:20480\n"
        "s_add_u32 s58, s58, 0x8000\n"
        "s_addc_u32 s59, s59, 0\n"
        "v_mfma_f32_16x16x32_bf16 v[24:27], a[40:43], a[28:31], v[24:27]\n"
        "ds_read_b64_tr_b16 a[56:57], v158 offset:24576\n"
        "s_waitcnt lgkmcnt(3)\n"
        "v_mfma_f32_16x16x32_bf16 v[24:27], a[32:35], a[24:27], v[24:27]\n"
        "ds_read_b64_tr_b16 a[58:59], v158 offset:28672\n"
        "global_load_lds_dwordx4 v224, s[58:59] offset:0 sc1\n"
        "s_add_u32 m0, s73, 0x18800\n"
        "v_mfma_f32_16x16x32_bf16 v[32:35], a[32:35], a[192:195], v[32:35]\n"
        "ds_read_b64_tr_b16 a[52:53], v159 offset:16384\n"
        "s_add_u32 s58, s58, 0x8000\n"
        "s_addc_u32 s59, s59, 0\n"
        "v_mfma_f32_16x16x32_bf16 v[32:35], a[40:43], a[196:199], v[32:35]\n"
        "ds_read_b64_tr_b16 a[54:55], v159 offset:20480\n"
        "v_mfma_f32_16x16x32_bf16 v[36:39], a[44:47], a[196:199], v[36:39]\n"
        "ds_read_b64_tr_b16 a[60:61], v159 offset:24576\n"
        "global_load_lds_dwordx4 v224, s[58:59] offset:0 sc1\n"
        "s_add_u32 m0, s73, 0x18c00\n"
        "v_mfma_f32_16x16x32_bf16 v[36:39], a[36:39], a[192:195], v[36:39]\n"
        "ds_read_b64_tr_b16 a[62:63], v159 offset:28672\n"
        "s_add_u32 s58, s58, 0x8000\n"
        "s_addc_u32 s59, s59, 0\n"
        "v_mfma_f32_16x16x32_bf16 v[44:47], a[36:39], a[200:203], v[44:47]\n"
        "ds_read_b128 a[0:3], v148 offset:32768\n"
        "v_mfma_f32_16x16x32_bf16 v[44:47], a[44:47], a[204:207], v[44:47]\n"
        "ds_read_b128 a[4:7], v148 offset:40960\n"
        "global_load_lds_dwordx4 v224, s[58:59] offset:0 sc1\n"
        "v_mfma_f32_16x16x32_bf16 v[40:43], a[40:43], a[204:207], v[40:43]\n"
        "ds_read_b128 a[8:11], v148 offset:33792\n"
        "v_mfma_f32_16x16x32_bf16 v[40:43], a[32:35], a[200:203], v[40:43]\n"
        "ds_read_b128 a[12:15], v148 offset:41984\n"
        "v_mfma_f32_16x16x32_bf16 v[48:51], a[32:35], a[208:211], v[48:51]\n"
        "ds_read_b128 a[16:19], v148 offset:34816\n"
        "global_load_dwordx4 v[112:115], v227, s[64:65]\n"
        "v_mfma_f32_16x16x32_bf16 v[48:51], a[40:43], a[212:215], v[48:51]\n"
        "ds_read_b128 a[20:23], v148 offset:43008\n"
        "s_add_u32 s64, s64, 0x8000\n"
        "s_addc_u32 s65, s65, 0\n"
        "v_mfma_f32_16x16x32_bf16 v[52:55], a[44:47], a[212:215], v[52:55]\n"
        "ds_read_b128 a[24:27], v148 offset:35840\n"
        "v_mfma_f32_16x16x32_bf16 v[52:55], a[36:39], a[208:211], v[52:55]\n"
        "ds_read_b128 a[28:31], v148 offset:44032\n"
        "global_load_dwordx4 v[116:119], v227, s[64:65]\n"
        "v_mfma_f32_16x16x32_bf16 v[60:63], a[36:39], a[216:219], v[60:63]\n"
        "ds_read_b128 a[192:195], v151 offset:32768\n"
        "s_add_u32 s64, s64, 0x8000\n"
        "s_addc_u32 s65, s65, 0\n"
        "v_mfma_f32_16x16x32_bf16 v[60:63], a[44:47], a[220:223], v[60:63]\n"
        "ds_read_b128 a[196:199], v151 offset:40960\n"
        "s_waitcnt lgkmcnt(8)\n"
        "v_mfma_f32_16x16x32_bf16 v[56:59], a[40:43], a[220:223], v[56:59]\n"
        "global_load_dwordx4 v[120:123], v227, s[64:65]\n"
        "v_mfma_f32_16x16x32_bf16 v[56:59], a[32:35], a[216:219], v[56:59]\n"
        "ds_read_b128 a[200:203], v151 offset:33792\n"
        "s_add_u32 s64, s64, 0x8000\n"
        "s_addc_u32 s65, s65, 0\n"
        "v_mfma_f32_16x16x32_bf16 v[0:3], a[48:51], a[0:3], v[0:3]\n"
        "ds_read_b128 a[204:207], v151 offset:41984\n"
        "s_waitcnt lgkmcnt(7)\n"
        "v_mfma_f32_16x16x32_bf16 v[16:19], a[48:51], a[16:19], v[16:19]\n"
        "ds_read_b128 a[212:215], v151 offset:43008\n"
        "s_waitcnt lgkmcnt(7)\n"
        "v_mfma_f32_16x16x32_bf16 v[20:23], a[60:63], a[20:23], v[20:23]\n"
        "ds_read_b128 a[208:211], v151 offset:34816\n"
        "global_load_dwordx4 v[124:127], v227, s[64:65]\n"
        "v_mfma_f32_16x16x32_bf16 v[20:23], a[52:55], a[16:19], v[20:23]\n"
        "s_waitcnt vmcnt(24)\n"
        "s_barrier\n"
        "s_waitcnt lgkmcnt(6)\n"
        "v_mfma_f32_16x16x32_bf16 v[16:19], a[56:59], a[20:23], v[16:19]\n"
        "ds_read_b128 a[220:223], v151 offset:44032\n"
        "v_mfma_f32_16x16x32_bf16 v[8:11], a[56:59], a[12:15], v[8:11]\n"
        "ds_read_b128 a[216:219], v151 offset:35840\n"
        "global_load_dword v128, v238, s[48:49] offset:64 sc1\n"
        "v_mfma_f32_16x16x32_bf16 v[24:27], a[56:59], a[28:31], v[24:27]\n"
        "ds_read_b128 a[20:23], v148 offset:59392\n"
        "v_mfma_f32_16x16x32_bf16 v[4:7], a[60:63], a[4:7], v[4:7]\n"
        "ds_read_b128 a[16:19], v148 offset:51200\n"
        "v_mfma_f32_16x16x32_bf16 v[12:15], a[52:55], a[8:11], v[12:15]\n"
        "v_mfma_f32_16x16x32_bf16 v[28:31], a[52:55], a[24:27], v[28:31]\n"
        "v_mfma_f32_16x16x32_bf16 v[0:3], a[56:59], a[4:7], v[0:3]\n"
        "ds_read_b128 a[4:7], v148 offset:57344\n"
        "s_waitcnt lgkmcnt(5)\n"
        "v_mfma_f32_16x16x32_bf16 v[8:11], a[48:51], a[8:11], v[8:11]\n"
        "ds_read_b128 a[8:11], v148 offset:50176\n"
        "v_mfma_f32_16x16x32_bf16 v[24:27], a[48:51], a[24:27], v[24:27]\n"
        "ds_read_b128 a[24:27], v148 offset:52224\n"
        "v_mfma_f32_16x16x32_bf16 v[4:7], a[52:55], a[0:3], v[4:7]\n"
        "ds_read_b128 a[0:3], v148 offset:49152\n"
        "s_waitcnt lgkmcnt(6)\n"
        "v_mfma_f32_16x16x32_bf16 v[12:15], a[60:63], a[12:15], v[12:15]\n"
        "ds_read_b128 a[12:15], v148 offset:58368\n"
        "s_waitcnt lgkmcnt(6)\n"
        "v_mfma_f32_16x16x32_bf16 v[28:31], a[60:63], a[28:31], v[28:31]\n"
        "ds_read_b128 a[28:31], v148 offset:60416\n"
        "v_mfma_f32_16x16x32_bf16 v[52:55], a[60:63], a[212:215], v[52:55]\n"
        "v_mfma_f32_16x16x32_bf16 v[52:55], a[52:55], a[208:211], v[52:55]\n"
        "s_waitcnt vmcnt(17)\n"
        "s_barrier\n"
        "v_mfma_f32_16x16x32_bf16 v[36:39], a[60:63], a[196:199], v[36:39]\n"
        "ds_read_b64_tr_b16 a[224:225], v156 offset:0\n"
        "v_mfma_f32_16x16x32_bf16 v[44:47], a[52:55], a[200:203], v[44:47]\n"
        "ds_read_b64_tr_b16 a[226:227], v156 offset:4096\n"
        "v_mfma_f32_16x16x32_bf16 v[60:63], a[52:55], a[216:219], v[60:63]\n"
        "ds_read_b64_tr_b16 a[232:233], v156 offset:8192\n"
        "v_mfma_f32_16x16x32_bf16 v[20:23], a[188:191], a[20:23], v[20:23]\n"
        "ds_read_b64_tr_b16 a[234:235], v156 offset:12288\n"
        "v_mfma_f32_16x16x32_bf16 v[32:35], a[48:51], a[192:195], v[32:35]\n"
        "ds_read_b64_tr_b16 a[228:229], v157 offset:0\n"
        "v_mfma_f32_16x16x32_bf16 v[40:43], a[56:59], a[204:207], v[40:43]\n"
        "ds_read_b64_tr_b16 a[230:231], v157 offset:4096\n"
        "s_waitcnt lgkmcnt(6)\n"
        "v_mfma_f32_16x16x32_bf16 v[48:51], a[48:51], a[208:211], v[48:51]\n"
        "ds_read_b128 a[208:211], v149 offset:18432\n"
        "v_mfma_f32_16x16x32_bf16 v[56:59], a[56:59], a[220:223], v[56:59]\n"
        "ds_read_b64_tr_b16 a[236:237], v157 offset:8192\n"
        "v_mfma_f32_16x16x32_bf16 v[36:39], a[52:55], a[192:195], v[36:39]\n"
        "ds_read_b128 a[192:195], v149 offset:16384\n"
        "v_mfma_f32_16x16x32_bf16 v[44:47], a[60:63], a[204:207], v[44:47]\n"
        "ds_read_b128 a[204:207], v149 offset:25600\n"
        "v_mfma_f32_16x16x32_bf16 v[60:63], a[60:63], a[220:223], v[60:63]\n"
        "ds_read_b128 a[220:223], v149 offset:27648\n"
        "v_mfma_f32_16x16x32_bf16 v[20:23], a[180:183], a[16:19], v[20:23]\n"
        "ds_read_b64_tr_b16 a[238:239], v157 offset:12288\n"
        "v_mfma_f32_16x16x32_bf16 v[4:7], a[188:191], a[4:7], v[4:7]\n"
        "s_waitcnt vmcnt(9)\n"
        "v_mfma_f32_16x16x32_bf16 v[12:15], a[180:183], a[8:11], v[12:15]\n"
        "s_barrier\n"
        "ds_read_b64_tr_b16 a[240:241], v154 offset:0\n"
        "v_mfma_f32_16x16x32_bf16 v[28:31], a[180:183], a[24:27], v[28:31]\n"
        "ds_read_b64_tr_b16 a[242:243], v154 offset:4096\n"
        "v_mfma_f32_16x16x32_bf16 v[32:35], a[56:59], a[196:199], v[32:35]\n"
        "ds_read_b128 a[196:199], v149 offset:24576\n"
        "v_mfma_f32_16x16x32_bf16 v[40:43], a[48:51], a[200:203], v[40:43]\n"
        "ds_read_b128 a[200:203], v149 offset:17408\n"
        "v_mfma_f32_16x16x32_bf16 v[48:51], a[56:59], a[212:215], v[48:51]\n"
        "ds_read_b128 a[212:215], v149 offset:26624\n"
        "v_mfma_f32_16x16x32_bf16 v[56:59], a[48:51], a[216:219], v[56:59]\n"
        "ds_read_b128 a[216:219], v149 offset:19456\n"
        "v_mfma_f32_16x16x32_bf16 v[16:19], a[176:179], a[16:19], v[16:19]\n"
        "ds_read_b64_tr_b16 a[248:249], v154 offset:8192\n"
        "v_mfma_f32_16x16x32_bf16 v[0:3], a[176:179], a[0:3], v[0:3]\n"
        "ds_read_b64_tr_b16 a[250:251], v154 offset:12288\n"
        "v_mfma_f32_16x16x32_bf16 v[8:11], a[184:187], a[12:15], v[8:11]\n"
        "ds_read_b128 a[16:19], v148 offset:18432\n"
        "v_mfma_f32_16x16x32_bf16 v[24:27], a[184:187], a[28:31], v[24:27]\n"
        "ds_read_b64_tr_b16 a[244:245], v155 offset:0\n"
        "v_mfma_f32_16x16x32_bf16 v[4:7], a[180:183], a[0:3], v[4:7]\n"
        "ds_read_b128 a[0:3], v148 offset:16384\n"
        "v_mfma_f32_16x16x32_bf16 v[12:15], a[188:191], a[12:15], v[12:15]\n"
        "ds_read_b128 a[12:15], v148 offset:25600\n"
        "s_waitcnt lgkmcnt(8)\n"
        "v_mfma_f32_16x16x32_bf16 v[28:31], a[188:191], a[28:31], v[28:31]\n"
        "ds_read_b128 a[28:31], v148 offset:27648\n"
        "s_waitcnt lgkmcnt(8)\n"
        "v_mfma_f32_16x16x32_bf16 v[16:19], a[184:187], a[20:23], v[16:19]\n"
        "ds_read_b128 a[20:23], v148 offset:26624\n"
        "v_mfma_f32_16x16x32_bf16 v[0:3], a[184:187], a[4:7], v[0:3]\n"
        "ds_read_b128 a[4:7], v148 offset:24576\n"
        "v_mfma_f32_16x16x32_bf16 v[8:11], a[176:179], a[8:11], v[8:11]\n"
        "ds_read_b128 a[8:11], v148 offset:17408\n"
        "v_mfma_f32_16x16x32_bf16 v[24:27], a[176:179], a[24:27], v[24:27]\n"
        "ds_read_b128 a[24:27], v148 offset:19456\n"
        "v_mfma_f32_16x16x32_bf16 v[16:19], a[224:227], a[208:211], v[16:19]\n"
        "ds_read_b64_tr_b16 a[246:247], v155 offset:4096\n"
        "v_mfma_f32_16x16x32_bf16 v[0:3], a[224:227], a[192:195], v[0:3]\n"
        "ds_read_b64_tr_b16 a[252:253], v155 offset:8192\n"
        "v_mfma_f32_16x16x32_bf16 v[8:11], a[232:235], a[204:207], v[8:11]\n"
        "ds_read_b64_tr_b16 a[254:255], v155 offset:12288\n"
        "v_mfma_f32_16x16x32_bf16 v[24:27], a[232:235], a[220:223], v[24:27]\n"
        "ds_read_b64_tr_b16 a[40:41], v156 offset:8192\n"
        "s_waitcnt lgkmcnt(8)\n"
        "v_mfma_f32_16x16x32_bf16 v[16:19], a[232:235], a[212:215], v[16:19]\n"
        "ds_read_b64_tr_b16 a[42:43], v156 offset:12288\n"
        "v_mfma_f32_16x16x32_bf16 v[0:3], a[232:235], a[196:199], v[0:3]\n"
        "ds_read_b64_tr_b16 a[32:33], v156 offset:0\n"
        "v_mfma_f32_16x16x32_bf16 v[8:11], a[224:227], a[200:203], v[8:11]\n"
        "ds_read_b64_tr_b16 a[34:35], v156 offset:4096\n"
        "v_mfma_f32_16x16x32_bf16 v[24:27], a[224:227], a[216:219], v[24:27]\n"
        "ds_read_b64_tr_b16 a[56:57], v154 offset:8192\n"
        "v_mfma_f32_16x16x32_bf16 v[20:23], a[236:239], a[212:215], v[20:23]\n"
        "ds_read_b64_tr_b16 a[58:59], v154 offset:12288\n"
        "v_mfma_f32_16x16x32_bf16 v[4:7], a[236:239], a[196:199], v[4:7]\n"
        "ds_read_b128 a[196:199], v151 offset:57344\n"
        "v_mfma_f32_16x16x32_bf16 v[12:15], a[228:231], a[200:203], v[12:15]\n"
        "ds_read_b128 a[200:203], v151 offset:50176\n"
        "v_mfma_f32_16x16x32_bf16 v[28:31], a[228:231], a[216:219], v[28:31]\n"
        "ds_read_b128 a[216:219], v151 offset:52224\n"
        "v_mfma_f32_16x16x32_bf16 v[16:19], a[240:243], a[16:19], v[16:19]\n"
        "ds_read_b64_tr_b16 a[48:49], v154 offset:0\n"
        "v_mfma_f32_16x16x32_bf16 v[0:3], a[240:243], a[0:3], v[0:3]\n"
        "ds_read_b64_tr_b16 a[50:51], v154 offset:4096\n"
        "v_mfma_f32_16x16x32_bf16 v[8:11], a[248:251], a[12:15], v[8:11]\n"
        "ds_read_b128 a[212:215], v151 offset:59392\n"
        "s_waitcnt lgkmcnt(8)\n"
        "v_mfma_f32_16x16x32_bf16 v[24:27], a[248:251], a[28:31], v[24:27]\n"
        "ds_read_b64_tr_b16 a[36:37], v157 offset:0\n"
        "v_mfma_f32_16x16x32_bf16 v[20:23], a[228:231], a[208:211], v[20:23]\n"
        "s_waitcnt vmcnt(5)\n"
        "v_mfma_f32_16x16x32_bf16 v[4:7], a[228:231], a[192:195], v[4:7]\n"
        "ds_read_b128 a[192:195], v151 offset:49152\n"
        "v_mfma_f32_16x16x32_bf16 v[12:15], a[236:239], a[204:207], v[12:15]\n"
        "ds_read_b128 a[204:207], v151 offset:58368\n"
        "v_mfma_f32_16x16x32_bf16 v[28:31], a[236:239], a[220:223], v[28:31]\n"
        "ds_read_b128 a[220:223], v151 offset:60416\n"
        "s_waitcnt vmcnt(4) lgkmcnt(7)\n"
        "v_mfma_f32_16x16x32_bf16 v[16:19], a[248:251], a[20:23], v[16:19]\n"
        "ds_read_b64_tr_b16 v[66:67], v225 offset:2048\n"
        "v_mfma_f32_16x16x32_bf16 v[0:3], a[248:251], a[4:7], v[0:3]\n"
        "ds_read_b64_tr_b16 v[70:71], v225 offset:2080\n"
        "v_mfma_f32_16x16x32_bf16 v[8:11], a[240:243], a[8:11], v[8:11]\n"
        "ds_read_b64_tr_b16 v[74:75], v225 offset:2112\n"
        "s_waitcnt vmcnt(3) lgkmcnt(7)\n"
        "v_mfma_f32_16x16x32_bf16 v[24:27], a[240:243], a[24:27], v[24:27]\n"
        "ds_read_b128 a[208:211], v151 offset:51200\n"
        "v_cvt_pk_bf16_f32 v16, v16, v17\n"
        "v_mfma_f32_16x16x32_bf16 v[20:23], a[252:255], a[20:23], v[20:23]\n"
        "ds_read_b64_tr_b16 a[38:39], v157 offset:4096\n"
        "v_cvt_pk_bf16_f32 v0, v0, v1\n"
        "v_mfma_f32_16x16x32_bf16 v[4:7], a[252:255], a[4:7], v[4:7]\n"
        "s_waitcnt vmcnt(2) lgkmcnt(7)\n"
        "v_cvt_pk_bf16_f32 v8, v8, v9\n"
        "v_cvt_pk_bf16_f32 v24, v24, v25\n"
        "v_mfma_f32_16x16x32_bf16 v[12:15], a[244:247], a[8:11], v[12:15]\n"
        "s_waitcnt lgkmcnt(6)\n"
        "v_cvt_pk_bf16_f32 v17, v18, v19\n"
        "v_cvt_pk_bf16_f32 v1, v2, v3\n"
        "v_mfma_f32_16x16x32_bf16 v[28:31], a[244:247], a[24:27], v[28:31]\n"
        "s_waitcnt lgkmcnt(5)\n"
        "v_cvt_pk_bf16_f32 v9, v10, v11\n"
        "v_cvt_pk_bf16_f32 v25, v26, v27\n"
        "LHB6_%=:\n"
        "s_cbranch_execz LHRB_%=\n"
        "v_mfma_f32_16x16x32_bf16 v[20:23], a[244:247], a[16:19], v[20:23]\n"
        "ds_read_b128 a[24:27], v152 offset:19456\n"
        "s_waitcnt lgkmcnt(5)\n"
        "v_mfma_f32_16x16x32_bf16 v[4:7], a[244:247], a[0:3], v[4:7]\n"
        "s_waitcnt vmcnt(1) lgkmcnt(4)\n"
        "v_permlane16_swap_b32 v112, v114\n"
        "v_mfma_f32_16x16x32_bf16 v[12:15], a[252:255], a[12:15], v[12:15]\n"
        "ds_read_b64_tr_b16 a[44:45], v157 offset:8192\n"
        "v_lshlrev_b32 v84, 16, v66\n"
        "v_mfma_f32_16x16x32_bf16 v[28:31], a[252:255], a[28:31], v[28:31]\n"
        "ds_read_b128 a[28:31], v152 offset:27648\n"
        "v_cvt_pk_bf16_f32 v18, v20, v21\n"
        "v_mfma_f32_16x16x32_bf16 v[60:63], a[180:183], a[216:219], v[60:63]\n"
        "s_waitcnt vmcnt(0) lgkmcnt(3)\n"
        "v_cvt_pk_bf16_f32 v19, v22, v23\n"
        "v_cvt_pk_bf16_f32 v2, v4, v5\n"
        "v_mfma_f32_16x16x32_bf16 v[56:59], a[184:187], a[220:223], v[56:59]\n"
        "ds_read_b64_tr_b16 a[46:47], v157 offset:12288\n"
        "v_cvt_pk_bf16_f32 v3, v6, v7\n"
        "v_mfma_f32_16x16x32_bf16 v[60:63], a[188:191], a[220:223], v[60:63]\n"
        "ds_read_b128 a[220:223], v151 offset:27648\n"
        "v_cvt_pk_bf16_f32 v10, v12, v13\n"
        "v_mfma_f32_16x16x32_bf16 v[56:59], a[176:179], a[216:219], v[56:59]\n"
        "ds_read_b128 a[216:219], v151 offset:19456\n"
        "v_cvt_pk_bf16_f32 v11, v14, v15\n"
        "v_cvt_pk_bf16_f32 v26, v28, v29\n"
        "v_cvt_pk_bf16_f32 v27, v30, v31\n"
        "global_store_dwordx4 v254, v[16:19], s[28:29] offset:2048 sc1\n"
        "s_waitcnt lgkmcnt(5)\n"
        "v_lshlrev_b32 v196, 16, v114\n"
        "v_permlane16_swap_b32 v116, v118\n"
        "v_mfma_f32_16x16x32_bf16 v[32:35], a[176:179], a[192:195], v[32:35]\n"
        "v_mul_f32 v196, v196, v84\n"
        "s_waitcnt lgkmcnt(3)\n"
        "v_mfma_f32_16x16x32_bf16 v[56:59], a[40:43], a[28:31], v[56:59]\n"
        "ds_read_b128 a[0:3], v152 offset:16384\n"
        "v_lshlrev_b32 v204, 16, v118\n"
        "v_mfma_f32_16x16x32_bf16 v[56:59], a[32:35], a[24:27], v[56:59]\n"
        "global_store_dwordx4 v254, v[0:3], s[28:29] offset:0 sc1\n"
        "ds_read_b128 a[4:7], v152 offset:24576\n"
        "v_lshlrev_b32 v84, 16, v70\n"
        "v_mul_f32 v204, v204, v84\n"
        "s_waitcnt lgkmcnt(3)\n"
        "v_mfma_f32_16x16x32_bf16 v[56:59], a[56:59], a[220:223], v[56:59]\n"
        "s_waitcnt lgkmcnt(2)\n"
        "v_permlane16_swap_b32 v120, v122\n"
        "v_mfma_f32_16x16x32_bf16 v[56:59], a[48:51], a[216:219], v[56:59]\n"
        "ds_read_b128 a[8:11], v152 offset:17408\n"
        "v_lshlrev_b32 v84, 16, v74\n"
        "global_store_dwordx4 v254, v[8:11], s[28:29] offset:1024 sc1\n"
        "ds_read_b128 a[12:15], v152 offset:25600\n"
        "ds_read_b64_tr_b16 v[78:79], v225 offset:2144\n"
        "v_lshlrev_b32 v212, 16, v122\n"
        "v_mfma_f32_16x16x32_bf16 v[32:35], a[184:187], a[196:199], v[32:35]\n"
        "ds_read_b128 a[16:19], v152 offset:18432\n"
        "v_mul_f32 v212, v212, v84\n"
        "v_mfma_f32_16x16x32_bf16 v[36:39], a[188:191], a[196:199], v[36:39]\n"
        "s_waitcnt lgkmcnt(5)\n"
        "v_cvt_pk_bf16_f32 v212, 0, v212\n"
        "v_and_b32 v87, 0xffff0000, v67\n"
        "global_store_dwordx4 v254, v[24:27], s[28:29] offset:3072 sc1\n"
        "ds_read_b128 a[20:23], v152 offset:26624\n"
        "v_permlane16_swap_b32 v113, v115\n"
        "v_and_b32 v199, 0xffff0000, v115\n"
        "v_mfma_f32_16x16x32_bf16 v[36:39], a[180:183], a[192:195], v[36:39]\n"
        "s_waitcnt lgkmcnt(5)\n"
        "v_permlane16_swap_b32 v117, v119\n"
        "v_mfma_f32_16x16x32_bf16 v[44:47], a[180:183], a[200:203], v[44:47]\n"
        "ds_read_b64_tr_b16 a[52:53], v155 offset:0\n"
        "v_mul_f32 v199, v199, v87\n"
        "v_mfma_f32_16x16x32_bf16 v[44:47], a[188:191], a[204:207], v[44:47]\n"
        "s_waitcnt lgkmcnt(5)\n"
        "v_and_b32 v207, 0xffff0000, v119\n"
        "v_and_b32 v87, 0xffff0000, v71\n"
        "v_mfma_f32_16x16x32_bf16 v[40:43], a[184:187], a[204:207], v[40:43]\n"
        "v_permlane16_swap_b32 v121, v123\n"
        "v_mfma_f32_16x16x32_bf16 v[40:43], a[176:179], a[200:203], v[40:43]\n"
        "s_waitcnt lgkmcnt(3)\n"
        "v_mov_b32 v129, v128\n"
        "v_mul_f32 v207, v207, v87\n"
        "v_mfma_f32_16x16x32_bf16 v[48:51], a[176:179], a[208:211], v[48:51]\n"
        "ds_read_b64_tr_b16 a[54:55], v155 offset:4096\n"
        "v_and_b32 v215, 0xffff0000, v123\n"
        "v_mfma_f32_16x16x32_bf16 v[48:51], a[184:187], a[212:215], v[48:51]\n"
        "s_waitcnt lgkmcnt(3)\n"
        "v_permlane16_swap_b32 v128, v129\n"
        "v_mfma_f32_16x16x32_bf16 v[52:55], a[188:191], a[212:215], v[52:55]\n"
        "ds_read_b64_tr_b16 a[60:61], v155 offset:8192\n"
        "v_and_b32 v87, 0xffff0000, v75\n"
        "v_mfma_f32_16x16x32_bf16 v[52:55], a[180:183], a[208:211], v[52:55]\n"
        "s_waitcnt lgkmcnt(3)\n"
        "v_permlane16_swap_b32 v125, v127\n"
        "v_mfma_f32_16x16x32_bf16 v[32:35], a[32:35], a[0:3], v[32:35]\n"
        "ds_read_b64_tr_b16 a[62:63], v155 offset:12288\n"
        "v_mov_b32 v131, v129\n"
        "v_mfma_f32_16x16x32_bf16 v[32:35], a[40:43], a[4:7], v[32:35]\n"
        "ds_read_b128 a[192:195], v151 offset:16384\n"
        "v_mul_f32 v215, v215, v87\n"
        "v_mfma_f32_16x16x32_bf16 v[36:39], a[44:47], a[4:7], v[36:39]\n"
        "ds_read_b128 a[196:199], v151 offset:24576\n"
        "v_and_b32 v223, 0xffff0000, v127\n"
        "v_mfma_f32_16x16x32_bf16 v[36:39], a[36:39], a[0:3], v[36:39]\n"
        "v_permlane32_swap_b32 v129, v131\n"
        "v_mfma_f32_16x16x32_bf16 v[44:47], a[36:39], a[8:11], v[44:47]\n"
        "ds_read_b128 a[200:203], v151 offset:17408\n"
        "v_and_b32 v87, 0xffff0000, v79\n"
        "v_mfma_f32_16x16x32_bf16 v[44:47], a[44:47], a[12:15], v[44:47]\n"
        "ds_read_b128 a[204:207], v151 offset:25600\n"
        "v_sub_f32 v135, 1.0, v131\n"
        "v_mfma_f32_16x16x32_bf16 v[40:43], a[40:43], a[12:15], v[40:43]\n"
        "ds_read_b128 a[208:211], v151 offset:18432\n"
        "v_mul_f32 v223, v223, v87\n"
        "v_mfma_f32_16x16x32_bf16 v[40:43], a[32:35], a[8:11], v[40:43]\n"
        "ds_read_b128 a[212:215], v151 offset:26624\n"
        "v_cvt_pk_bf16_f32 v135, 0, v135\n"
        "v_mfma_f32_16x16x32_bf16 v[48:51], a[32:35], a[16:19], v[48:51]\n"
        "s_waitcnt lgkmcnt(6)\n"
        "v_cvt_pk_bf16_f32 v223, 0, v223\n"
        "v_mul_f32 v223, v223, v135\n"
        "v_mfma_f32_16x16x32_bf16 v[48:51], a[40:43], a[20:23], v[48:51]\n"
        "s_waitcnt lgkmcnt(5)\n"
        "v_cvt_pk_bf16_f32 v223, 0, v223\n"
        "v_mov_b32 v130, v128\n"
        "v_mfma_f32_16x16x32_bf16 v[52:55], a[44:47], a[20:23], v[52:55]\n"
        "ds_read_b64_tr_b16 v[64:65], v225 offset:0\n"
        "v_sub_f32 v133, 1.0, v129\n"
        "v_mfma_f32_16x16x32_bf16 v[52:55], a[36:39], a[16:19], v[52:55]\n"
        "ds_read_b64_tr_b16 v[68:69], v225 offset:32\n"
        "v_cvt_pk_bf16_f32 v133, 0, v133\n"
        "v_mfma_f32_16x16x32_bf16 v[60:63], a[36:39], a[24:27], v[60:63]\n"
        "ds_read_b64_tr_b16 v[72:73], v225 offset:64\n"
        "v_lshlrev_b32 v192, 16, v112\n"
        "v_mfma_f32_16x16x32_bf16 v[60:63], a[44:47], a[28:31], v[60:63]\n"
        "ds_read_b64_tr_b16 v[76:77], v225 offset:96\n"
        "v_and_b32 v193, 0xffff0000, v112\n"
        "v_mfma_f32_16x16x32_bf16 v[32:35], a[48:51], a[192:195], v[32:35]\n"
        "s_waitcnt lgkmcnt(5)\n"
        "v_permlane32_swap_b32 v128, v130\n"
        "v_mfma_f32_16x16x32_bf16 v[32:35], a[56:59], a[196:199], v[32:35]\n"
        "s_waitcnt lgkmcnt(4)\n"
        "v_permlane16_swap_b32 v124, v126\n"
        "v_mfma_f32_16x16x32_bf16 v[36:39], a[60:63], a[196:199], v[36:39]\n"
        "v_sub_f32 v132, 1.0, v128\n"
        "v_cvt_pk_bf16_f32 v132, 0, v132\n"
        "v_mfma_f32_16x16x32_bf16 v[36:39], a[52:55], a[192:195], v[36:39]\n"
        "v_sub_f32 v134, 1.0, v130\n"
        "v_cvt_pk_bf16_f32 v134, 0, v134\n"
        "v_mfma_f32_16x16x32_bf16 v[44:47], a[52:55], a[200:203], v[44:47]\n"
        "s_waitcnt lgkmcnt(3)\n"
        "v_lshlrev_b32 v194, 16, v113\n"
        "v_and_b32 v195, 0xffff0000, v113\n"
        "v_mfma_f32_16x16x32_bf16 v[44:47], a[60:63], a[204:207], v[44:47]\n"
        "v_lshlrev_b32 v80, 16, v64\n"
        "v_and_b32 v81, 0xffff0000, v64\n"
        "v_mfma_f32_16x16x32_bf16 v[40:43], a[56:59], a[204:207], v[40:43]\n"
        "v_lshlrev_b32 v82, 16, v65\n"
        "v_and_b32 v83, 0xffff0000, v65\n"
        "v_mfma_f32_16x16x32_bf16 v[40:43], a[48:51], a[200:203], v[40:43]\n"
        "v_mul_f32 v192, v192, v80\n"
        "v_mul_f32 v193, v193, v81\n"
        "v_mfma_f32_16x16x32_bf16 v[48:51], a[48:51], a[208:211], v[48:51]\n"
        "v_mul_f32 v194, v194, v82\n"
        "v_mul_f32 v195, v195, v83\n"
        "v_mfma_f32_16x16x32_bf16 v[48:51], a[56:59], a[212:215], v[48:51]\n"
        "v_cvt_pk_bf16_f32 v192, 0, v192\n"
        "v_cvt_pk_bf16_f32 v193, 0, v193\n"
        "v_mfma_f32_16x16x32_bf16 v[52:55], a[60:63], a[212:215], v[52:55]\n"
        "v_cvt_pk_bf16_f32 v194, 0, v194\n"
        "v_cvt_pk_bf16_f32 v195, 0, v195\n"
        "v_mfma_f32_16x16x32_bf16 v[52:55], a[52:55], a[208:211], v[52:55]\n"
        "v_mul_f32 v192, v192, v132\n"
        "v_mul_f32 v193, v193, v132\n"
        "v_mfma_f32_16x16x32_bf16 v[60:63], a[52:55], a[216:219], v[60:63]\n"
        "v_mul_f32 v194, v194, v132\n"
        "v_mul_f32 v195, v195, v132\n"
        "v_mfma_f32_16x16x32_bf16 v[60:63], a[60:63], a[220:223], v[60:63]\n"
        "v_cvt_pk_bf16_f32 v192, 0, v192\n"
        "v_cvt_pk_bf16_f32 v193, 0, v193\n"
        "v_cvt_pk_bf16_f32 v194, 0, v194\n"
        "v_cvt_pk_bf16_f32 v195, 0, v195\n"
        "v_and_b32 v197, 0xffff0000, v114\n"
        "v_lshlrev_b32 v198, 16, v115\n"
        "v_and_b32 v85, 0xffff0000, v66\n"
        "v_lshlrev_b32 v86, 16, v67\n"
        "v_mul_f32 v197, v197, v85\n"
        "v_mul_f32 v198, v198, v86\n"
        "v_cvt_pk_bf16_f32 v196, 0, v196\n"
        "v_cvt_pk_bf16_f32 v197, 0, v197\n"
        "v_cvt_pk_bf16_f32 v198, 0, v198\n"
        "v_cvt_pk_bf16_f32 v199, 0, v199\n"
        "v_mul_f32 v196, v196, v132\n"
        "v_mul_f32 v197, v197, v132\n"
        "v_mul_f32 v198, v198, v132\n"
        "v_mul_f32 v199, v199, v132\n"
        "v_cvt_pk_bf16_f32 v196, 0, v196\n"
        "v_cvt_pk_bf16_f32 v197, 0, v197\n"
        "v_cvt_pk_bf16_f32 v198, 0, v198\n"
        "v_cvt_pk_bf16_f32 v199, 0, v199\n"
        "v_lshlrev_b32 v200, 16, v116\n"
        "v_and_b32 v201, 0xffff0000, v116\n"
        "v_lshlrev_b32 v202, 16, v117\n"
        "v_and_b32 v203, 0xffff0000, v117\n"
        "s_waitcnt lgkmcnt(0)\n"
        "v_lshlrev_b32 v80, 16, v68\n"
        "v_and_b32 v81, 0xffff0000, v68\n"
        "v_lshlrev_b32 v82, 16, v69\n"
        "v_and_b32 v83, 0xffff0000, v69\n"
        "v_mul_f32 v200, v200, v80\n"
        "v_mul_f32 v201, v201, v81\n"
        "v_mul_f32 v202, v202, v82\n"
        "v_mul_f32 v203, v203, v83\n"
        "v_cvt_pk_bf16_f32 v200, 0, v200\n"
        "v_cvt_pk_bf16_f32 v201, 0, v201\n"
        "v_cvt_pk_bf16_f32 v202, 0, v202\n"
        "v_cvt_pk_bf16_f32 v203, 0, v203\n"
        "v_mul_f32 v200, v200, v133\n"
        "v_mul_f32 v201, v201, v133\n"
        "v_mul_f32 v202, v202, v133\n"
        "v_mul_f32 v203, v203, v133\n"
        "v_cvt_pk_bf16_f32 v200, 0, v200\n"
        "v_cvt_pk_bf16_f32 v201, 0, v201\n"
        "v_cvt_pk_bf16_f32 v202, 0, v202\n"
        "v_cvt_pk_bf16_f32 v203, 0, v203\n"
        "v_and_b32 v205, 0xffff0000, v118\n"
        "v_lshlrev_b32 v206, 16, v119\n"
        "v_and_b32 v85, 0xffff0000, v70\n"
        "v_lshlrev_b32 v86, 16, v71\n"
        "v_mul_f32 v205, v205, v85\n"
        "v_mul_f32 v206, v206, v86\n"
        "v_cvt_pk_bf16_f32 v204, 0, v204\n"
        "v_cvt_pk_bf16_f32 v205, 0, v205\n"
        "v_cvt_pk_bf16_f32 v206, 0, v206\n"
        "v_cvt_pk_bf16_f32 v207, 0, v207\n"
        "v_mul_f32 v204, v204, v133\n"
        "v_mul_f32 v205, v205, v133\n"
        "v_mul_f32 v206, v206, v133\n"
        "v_mul_f32 v207, v207, v133\n"
        "v_cvt_pk_bf16_f32 v204, 0, v204\n"
        "v_cvt_pk_bf16_f32 v205, 0, v205\n"
        "v_cvt_pk_bf16_f32 v206, 0, v206\n"
        "v_cvt_pk_bf16_f32 v207, 0, v207\n"
        "v_lshlrev_b32 v208, 16, v120\n"
        "v_and_b32 v209, 0xffff0000, v120\n"
        "v_lshlrev_b32 v210, 16, v121\n"
        "v_and_b32 v211, 0xffff0000, v121\n"
        "v_lshlrev_b32 v80, 16, v72\n"
        "v_and_b32 v81, 0xffff0000, v72\n"
        "v_lshlrev_b32 v82, 16, v73\n"
        "v_and_b32 v83, 0xffff0000, v73\n"
        "s_waitcnt vmcnt(0)\n"
        "s_mov_b64 exec, 0xffff\n"
        "global_store_dwordx2 v234, v[250:251], s[48:49] sc1\n"
        "s_mov_b64 exec, -1\n"
        "s_or_b32 s97, s55, s40\n"
        "s_cbranch_scc0 LEP_%=\n"
        "LEB_%=:\n"
        "global_load_dwordx2 v[236:237], v235, s[48:49] sc1\n"
        "v_mul_f32 v208, v208, v80\n"
        "v_mul_f32 v209, v209, v81\n"
        "v_mul_f32 v210, v210, v82\n"
        "v_mul_f32 v211, v211, v83\n"
        "v_cvt_pk_bf16_f32 v208, 0, v208\n"
        "v_cvt_pk_bf16_f32 v209, 0, v209\n"
        "v_cvt_pk_bf16_f32 v210, 0, v210\n"
        "v_cvt_pk_bf16_f32 v211, 0, v211\n"
        "v_mul_f32 v208, v208, v134\n"
        "v_mul_f32 v209, v209, v134\n"
        "v_mul_f32 v210, v210, v134\n"
        "v_mul_f32 v211, v211, v134\n"
        "v_cvt_pk_bf16_f32 v208, 0, v208\n"
        "v_cvt_pk_bf16_f32 v209, 0, v209\n"
        "v_cvt_pk_bf16_f32 v210, 0, v210\n"
        "v_cvt_pk_bf16_f32 v211, 0, v211\n"
        "v_and_b32 v213, 0xffff0000, v122\n"
        "v_lshlrev_b32 v214, 16, v123\n"
        "v_and_b32 v85, 0xffff0000, v74\n"
        "v_lshlrev_b32 v86, 16, v75\n"
        "v_mul_f32 v213, v213, v85\n"
        "v_mul_f32 v214, v214, v86\n"
        "v_cvt_pk_bf16_f32 v213, 0, v213\n"
        "v_cvt_pk_bf16_f32 v214, 0, v214\n"
        "v_cvt_pk_bf16_f32 v215, 0, v215\n"
        "v_mul_f32 v212, v212, v134\n"
        "v_mul_f32 v213, v213, v134\n"
        "v_mul_f32 v214, v214, v134\n"
        "v_mul_f32 v215, v215, v134\n"
        "v_cvt_pk_bf16_f32 v212, 0, v212\n"
        "v_cvt_pk_bf16_f32 v213, 0, v213\n"
        "v_cvt_pk_bf16_f32 v214, 0, v214\n"
        "v_cvt_pk_bf16_f32 v215, 0, v215\n"
        "v_lshlrev_b32 v216, 16, v124\n"
        "v_and_b32 v217, 0xffff0000, v124\n"
        "v_lshlrev_b32 v218, 16, v125\n"
        "v_and_b32 v219, 0xffff0000, v125\n"
        "v_lshlrev_b32 v80, 16, v76\n"
        "v_and_b32 v81, 0xffff0000, v76\n"
        "v_lshlrev_b32 v82, 16, v77\n"
        "v_and_b32 v83, 0xffff0000, v77\n"
        "v_mul_f32 v216, v216, v80\n"
        "v_mul_f32 v217, v217, v81\n"
        "v_mul_f32 v218, v218, v82\n"
        "v_mul_f32 v219, v219, v83\n"
        "v_cvt_pk_bf16_f32 v216, 0, v216\n"
        "v_cvt_pk_bf16_f32 v217, 0, v217\n"
        "v_cvt_pk_bf16_f32 v218, 0, v218\n"
        "v_cvt_pk_bf16_f32 v219, 0, v219\n"
        "v_mul_f32 v216, v216, v135\n"
        "v_mul_f32 v217, v217, v135\n"
        "v_mul_f32 v218, v218, v135\n"
        "v_mul_f32 v219, v219, v135\n"
        "v_cvt_pk_bf16_f32 v216, 0, v216\n"
        "v_cvt_pk_bf16_f32 v217, 0, v217\n"
        "v_cvt_pk_bf16_f32 v218, 0, v218\n"
        "v_cvt_pk_bf16_f32 v219, 0, v219\n"
        "v_lshlrev_b32 v220, 16, v126\n"
        "v_and_b32 v221, 0xffff0000, v126\n"
        "v_lshlrev_b32 v222, 16, v127\n"
        "v_lshlrev_b32 v84, 16, v78\n"
        "v_and_b32 v85, 0xffff0000, v78\n"
        "v_lshlrev_b32 v86, 16, v79\n"
        "v_mul_f32 v220, v220, v84\n"
        "v_mul_f32 v221, v221, v85\n"
        "v_mul_f32 v222, v222, v86\n"
        "v_cvt_pk_bf16_f32 v220, 0, v220\n"
        "v_cvt_pk_bf16_f32 v221, 0, v221\n"
        "v_cvt_pk_bf16_f32 v222, 0, v222\n"
        "v_mul_f32 v220, v220, v135\n"
        "v_mul_f32 v221, v221, v135\n"
        "v_mul_f32 v222, v222, v135\n"
        "v_cvt_pk_bf16_f32 v220, 0, v220\n"
        "s_waitcnt vmcnt(0)\n"
        "v_cmp_ne_u64 vcc, v[236:237], v[250:251]\n"
        "v_cvt_pk_bf16_f32 v221, 0, v221\n"
        "s_cbranch_vccnz LPPR_%=\n"
        "LBPR_%=:\n"
        "global_load_dwordx4 v[96:99], v254, s[30:31] offset:0 sc1\n"
        "global_load_dwordx4 v[100:103], v254, s[30:31] offset:1024 sc1\n"
        "global_load_dwordx4 v[104:107], v254, s[30:31] offset:2048 sc1\n"
        "global_load_dwordx4 v[108:111], v254, s[30:31] offset:3072 sc1\n"
        "v_cvt_pk_bf16_f32 v222, 0, v222\n"
        "s_waitcnt vmcnt(3)\n"
        "v_mfma_f32_16x16x32_bf16 v[32:35], v[138:141], v[96:99], v[32:35]\n"
        "v_mfma_f32_16x16x32_bf16 v[36:39], v[136:139], v[96:99], v[36:39]\n"
        "s_waitcnt vmcnt(2)\n"
        "v_mfma_f32_16x16x32_bf16 v[40:43], v[138:141], v[100:103], v[40:43]\n"
        "v_mfma_f32_16x16x32_bf16 v[44:47], v[136:139], v[100:103], v[44:47]\n"
        "s_waitcnt vmcnt(1)\n"
        "v_mfma_f32_16x16x32_bf16 v[48:51], v[138:141], v[104:107], v[48:51]\n"
        "v_mfma_f32_16x16x32_bf16 v[52:55], v[136:139], v[104:107], v[52:55]\n"
        "s_waitcnt vmcnt(0)\n"
        "v_fmac_f32 v192, v32, v128\n"
        "v_fmac_f32 v193, v33, v128\n"
        "v_fmac_f32 v194, v34, v128\n"
        "v_fmac_f32 v195, v35, v128\n"
        "v_fmac_f32 v196, v36, v128\n"
        "v_fmac_f32 v197, v37, v128\n"
        "v_mfma_f32_16x16x32_bf16 v[56:59], v[138:141], v[108:111], v[56:59]\n"
        "v_fmac_f32 v198, v38, v128\n"
        "v_fmac_f32 v199, v39, v128\n"
        "v_mfma_f32_16x16x32_bf16 v[60:63], v[136:139], v[108:111], v[60:63]\n"
        "v_cvt_pk_bf16_f32 v192, v192, v193\n"
        "v_cvt_pk_bf16_f32 v193, v194, v195\n"
        "v_cvt_pk_bf16_f32 v194, v196, v197\n"
        "v_cvt_pk_bf16_f32 v195, v198, v199\n"
        "v_fmac_f32 v200, v40, v129\n"
        "v_permlane16_swap_b32 v192, v194\n"
        "v_permlane16_swap_b32 v193, v195\n"
        "global_store_dwordx4 v227, v[192:195], s[56:57]\n"
        "s_add_u32 s56, s56, 0x8000\n"
        "s_addc_u32 s57, s57, 0\n"
        "v_fmac_f32 v201, v41, v129\n"
        "v_fmac_f32 v202, v42, v129\n"
        "v_fmac_f32 v203, v43, v129\n"
        "v_fmac_f32 v204, v44, v129\n"
        "v_fmac_f32 v205, v45, v129\n"
        "v_fmac_f32 v206, v46, v129\n"
        "v_fmac_f32 v207, v47, v129\n"
        "v_cvt_pk_bf16_f32 v200, v200, v201\n"
        "v_cvt_pk_bf16_f32 v201, v202, v203\n"
        "v_cvt_pk_bf16_f32 v202, v204, v205\n"
        "v_cvt_pk_bf16_f32 v203, v206, v207\n"
        "v_fmac_f32 v208, v48, v130\n"
        "v_permlane16_swap_b32 v200, v202\n"
        "v_permlane16_swap_b32 v201, v203\n"
        "global_store_dwordx4 v227, v[200:203], s[56:57]\n"
        "s_add_u32 s56, s56, 0x8000\n"
        "s_addc_u32 s57, s57, 0\n"
        "v_fmac_f32 v209, v49, v130\n"
        "v_fmac_f32 v210, v50, v130\n"
        "v_fmac_f32 v211, v51, v130\n"
        "v_fmac_f32 v212, v52, v130\n"
        "v_fmac_f32 v213, v53, v130\n"
        "v_fmac_f32 v214, v54, v130\n"
        "v_fmac_f32 v215, v55, v130\n"
        "v_cvt_pk_bf16_f32 v208, v208, v209\n"
        "v_cvt_pk_bf16_f32 v209, v210, v211\n"
        "v_cvt_pk_bf16_f32 v210, v212, v213\n"
        "v_cvt_pk_bf16_f32 v211, v214, v215\n"
        "v_fmac_f32 v216, v56, v131\n"
        "v_permlane16_swap_b32 v208, v210\n"
        "v_permlane16_swap_b32 v209, v211\n"
        "global_store_dwordx4 v227, v[208:211], s[56:57]\n"
        "s_add_u32 s56, s56, 0x8000\n"
        "s_addc_u32 s57, s57, 0\n"
        "v_fmac_f32 v217, v57, v131\n"
        "v_fmac_f32 v218, v58, v131\n"
        "v_fmac_f32 v219, v59, v131\n"
        "v_fmac_f32 v220, v60, v131\n"
        "v_fmac_f32 v221, v61, v131\n"
        "v_fmac_f32 v222, v62, v131\n"
        "v_fmac_f32 v223, v63, v131\n"
        "v_cvt_pk_bf16_f32 v216, v216, v217\n"
        "v_cvt_pk_bf16_f32 v217, v218, v219\n"
        "v_cvt_pk_bf16_f32 v218, v220, v221\n"
        "v_cvt_pk_bf16_f32 v219, v222, v223\n"
        "s_nop 0\n"
        "v_permlane16_swap_b32 v216, v218\n"
        "v_permlane16_swap_b32 v217, v219\n"
        "global_store_dwordx4 v227, v[216:219], s[56:57]\n"
        "; ---- END\n"
        "s_branch LEND_%=\n"
        "LEND_%=:\n"
        :
        : "s"(__builtin_amdgcn_kernarg_segment_ptr()), "s"(blockIdx.x), "s"(__builtin_amdgcn_readfirstlane(threadIdx.x >> 6))
        : "v0", "v1", "v2", "v3", "v4", "v5", "v6", "v7", "v8", "v9", "v10", "v11", "v12", "v13", "v14", "v15", "v16", "v17", "v18", "v19", "v20", "v21", "v22", "v23", "v24", "v25", "v26", "v27", "v28", "v29", "v30", "v31",
        "v32", "v33", "v34", "v35", "v36", "v37", "v38", "v39", "v40", "v41", "v42", "v43", "v44", "v45", "v46", "v47", "v48", "v49", "v50", "v51", "v52", "v53", "v54", "v55", "v56", "v57", "v58", "v59", "v60", "v61", "v62", "v63",
        "v64", "v65", "v66", "v67", "v68", "v69", "v70", "v71", "v72", "v73", "v74", "v75", "v76", "v77", "v78", "v79", "v80", "v81", "v82", "v83", "v84", "v85", "v86", "v87", "v88", "v89", "v90", "v91", "v92", "v93", "v94", "v95",
        "v96", "v97", "v98", "v99", "v100", "v101", "v102", "v103", "v104", "v105", "v106", "v107", "v108", "v109", "v110", "v111", "v112", "v113", "v114", "v115", "v116", "v117", "v118", "v119", "v120", "v121", "v122", "v123", "v124", "v125", "v126", "v127",
        "v128", "v129", "v130", "v131", "v132", "v133", "v134", "v135", "v136", "v137", "v138", "v139", "v140", "v141", "v142", "v143", "v144", "v145", "v146", "v147", "v148", "v149", "v150", "v151", "v152", "v153", "v154", "v155", "v156", "v157", "v158", "v159",
        "v160", "v161", "v162", "v163", "v164", "v165", "v166", "v167", "v168", "v169", "v170", "v171", "v172", "v173", "v174", "v175", "v176", "v177", "v178", "v179", "v180", "v181", "v182", "v183", "v184", "v185", "v186", "v187", "v188", "v189", "v190", "v191",
        "v192", "v193", "v194", "v195", "v196", "v197", "v198", "v199", "v200", "v201", "v202", "v203", "v204", "v205", "v206", "v207", "v208", "v209", "v210", "v211", "v212", "v213", "v214", "v215", "v216", "v217", "v218", "v219", "v220", "v221", "v222", "v223",
        "v224", "v225", "v226", "v227", "v228", "v229", "v230", "v231", "v232", "v233", "v234", "v235", "v236", "v237", "v238", "v239", "v240", "v241", "v242", "v243", "v244", "v245", "v246", "v247", "v248", "v249", "v250", "v251", "v252", "v253", "v254", "v255",
        "a0", "a1", "a2", "a3", "a4", "a5", "a6", "a7", "a8", "a9", "a10", "a11", "a12", "a13", "a14", "a15", "a16", "a17", "a18", "a19", "a20", "a21", "a22", "a23", "a24", "a25", "a26", "a27", "a28", "a29", "a30", "a31",
        "a32", "a33", "a34", "a35", "a36", "a37", "a38", "a39", "a40", "a41", "a42", "a43", "a44", "a45", "a46", "a47", "a48", "a49", "a50", "a51", "a52", "a53", "a54", "a55", "a56", "a57", "a58", "a59", "a60", "a61", "a62", "a63",
        "a64", "a65", "a66", "a67", "a68", "a69", "a70", "a71", "a72", "a73", "a74", "a75", "a76", "a77", "a78", "a79", "a80", "a81", "a82", "a83", "a84", "a85", "a86", "a87", "a88", "a89", "a90", "a91", "a92", "a93", "a94", "a95",
        "a96", "a97", "a98", "a99", "a100", "a101", "a102", "a103", "a104", "a105", "a106", "a107", "a108", "a109", "a110", "a111", "a112", "a113", "a114", "a115", "a116", "a117", "a118", "a119", "a120", "a121", "a122", "a123", "a124", "a125", "a126", "a127",
        "a128", "a129", "a130", "a131", "a132", "a133", "a134", "a135", "a136", "a137", "a138", "a139", "a140", "a141", "a142", "a143", "a144", "a145", "a146", "a147", "a148", "a149", "a150", "a151", "a152", "a153", "a154", "a155", "a156", "a157", "a158", "a159",
        "a160", "a161", "a162", "a163", "a164", "a165", "a166", "a167", "a168", "a169", "a170", "a171", "a172", "a173", "a174", "a175", "a176", "a177", "a178", "a179", "a180", "a181", "a182", "a183", "a184", "a185", "a186", "a187", "a188", "a189", "a190", "a191",
        "a192", "a193", "a194", "a195", "a196", "a197", "a198", "a199", "a200", "a201", "a202", "a203", "a204", "a205", "a206", "a207", "a208", "a209", "a210", "a211", "a212", "a213", "a214", "a215", "a216", "a217", "a218", "a219", "a220", "a221", "a222", "a223",
        "a224", "a225", "a226", "a227", "a228", "a229", "a230", "a231", "a232", "a233", "a234", "a235", "a236", "a237", "a238", "a239", "a240", "a241", "a242", "a243", "a244", "a245", "a246", "a247", "a248", "a249", "a250", "a251", "a252", "a253", "a254", "a255",
        "s8", "s9", "s10", "s11", "s12", "s13", "s14", "s15", "s16", "s17", "s18", "s19", "s20", "s21", "s22", "s23", "s24", "s25", "s26", "s27", "s28", "s29", "s30", "s31", "s36", "s37", "s38", "s39", "s40", "s41", "s42", "s43",
        "s44", "s45", "s46", "s47", "s48", "s49", "s50", "s51", "s52", "s53", "s54", "s55", "s56", "s57", "s58", "s59", "s60", "s61", "s62", "s63", "s64", "s65", "s66", "s67", "s68", "s69", "s70", "s71", "s72", "s73", "s74", "s75",
        "s76", "s77", "s78", "s79", "s80", "s81", "s82", "s83", "s84", "s85", "s86", "s87", "s88", "s89", "s90", "s91", "s92", "s93", "s94", "s95", "s96", "s97", "s98", "s99", "vcc", "scc", "memory");
}

extern "C" void kernel_launch(void* const* d_in, const int* in_sizes, int n_in, void* d_out, int out_size, void* d_ws, size_t ws_size, hipStream_t stream) {
    (void)in_sizes; (void)n_in; (void)out_size;
    constexpr size_t WS_USED = (size_t)72 << 20;
    Params P;
    P.x = d_in[0]; P.ckv = d_in[1]; P.wq = d_in[2]; P.wuk = d_in[3]; P.wuv = d_in[4]; P.wo = d_in[5];
    P.bk = d_in[7]; P.bv = d_in[8]; P.shk = d_in[9]; P.ssk = d_in[10]; P.ssd = d_in[11]; P.gw = d_in[12];
    P.out = d_out; P.ws = (char*)d_ws;
    if (ws_size > WS_USED) P.ws += (ws_size - WS_USED) & ~(size_t)0xfffff;
    constexpr int LDS_MAX = 163840;
    (void)hipFuncSetAttribute((const void*)k_main, hipFuncAttributeMaxDynamicSharedMemorySize, LDS_MAX);
    k_main<<<256, 256, LDS_MAX, stream>>>(P);
}
```

```cpp
#include <hip/hip_runtime.h>
#include <stdint.h>

struct Params {
    const void *x, *ckv, *wq, *wuk, *wuv, *wo, *bk, *bv, *shk, *ssk, *ssd, *gw;
    void* out; char* ws;
};

__global__ __launch_bounds__(256, 1) void k_main(Params P) {
    asm volatile(
        "; ---- PRO\n"
        "s_load_dwordx16 s[8:23], %0, 0\n"
        "s_load_dwordx8 s[24:31], %0, 64\n"
        "s_load_dwordx4 s[36:39], %0, 96\n"
        "s_mov_b32 s55, %1\n"
        "s_mov_b32 s40, %2\n"
        "v_mbcnt_lo_u32_b32 v0, -1, 0\n"
        "v_mbcnt_hi_u32_b32 v0, -1, v0\n"
        "s_and_b32 s41, s55, 7\n"
        "s_lshr_b32 s42, s55, 3\n"
        "s_lshr_b32 s50, s42, 3\n"
        "s_lshl2_add_u32 s43, s41, s50\n"
        "s_and_b32 s44, s42, 7\n"
        "s_sub_u32 s45, 15, s44\n"
        "s_lshl_b32 s72, s40, 13\n"
        "s_lshl_b32 s73, s40, 12\n"
        "v_lshlrev_b32 v254, 4, v0\n"
        "v_lshrrev_b32 v2, 4, v0\n"
        "v_and_b32 v1, 15, v0\n"
        "s_getpc_b64 s[46:47]\n"
        "LPC_%=:\n"
        "s_lshl2_add_u32 s50, s42, s40\n"
        "s_lshl_b32 s50, s50, 10\n"
        "v_lshlrev_b32 v12, 6, v1\n"
        "v_add_u32 v12, s50, v12\n"
        "v_min_u32 v252, LEND_%=-LPC_%=-4, v12\n"
        "global_load_dword v253, v252, s[46:47] offset:0\n"
        "v_lshrrev_b32 v4, 3, v0\n"
        "v_and_b32 v5, 7, v0\n"
        "v_xor_b32 v6, v5, v2\n"
        "v_lshlrev_b32 v6, 4, v6\n"
        "v_xor_b32 v7, 64, v6\n"
        "v_lshl_add_u32 v234, v4, 10, v6\n"
        "v_lshl_add_u32 v8, v4, 10, v7\n"
        "v_add_u32 v235, 7168, v8\n"
        "v_add_u32 v236, 14336, v234\n"
        "v_add_u32 v237, 21504, v8\n"
        "v_and_b32 v8, 6, v4\n"
        "v_xor_b32 v8, v8, v5\n"
        "v_lshlrev_b32 v8, 4, v8\n"
        "v_lshl_add_u32 v9, v4, 13, v8\n"
        "v_xor_b32 v10, 4, v4\n"
        "v_lshl_add_u32 v10, v10, 13, v8\n"
        "v_add_u32 v238, 4096, v9\n"
        "v_add_u32 v239, 3200, v9\n"
        "v_add_u32 v240, 67584, v10\n"
        "v_add_u32 v241, 66688, v10\n"
        "s_cmp_ge_u32 s40, 2\n"
        "s_cselect_b32 s50, s45, s44\n"
        "s_and_b32 s51, s40, 1\n"
        "s_lshl1_add_u32 s50, s50, s51\n"
        "s_lshl_b32 s50, s50, 16\n"
        "s_lshl_b32 s51, s40, 17\n"
        "s_lshl_b32 s52, s43, 8\n"
        "s_add_u32 s51, s51, s52\n"
        "s_waitcnt lgkmcnt(0)\n"
        "s_add_u32 s56, s10, s50\n"
        "s_addc_u32 s57, s11, 0\n"
        "s_add_u32 s58, s56, 0x8000\n"
        "s_addc_u32 s59, s57, 0\n"
        "s_add_u32 s60, s14, s51\n"
        "s_addc_u32 s61, s15, 0\n"
        "s_sub_u32 s60, s60, 4096\n"
        "s_subb_u32 s61, s61, 0\n"
        "s_add_u32 s62, s16, s51\n"
        "s_addc_u32 s63, s17, 0\n"
        "s_sub_u32 s62, s62, 4096\n"
        "s_subb_u32 s63, s63, 0\n"
        "s_add_u32 m0, s73, 0x10000\n"
        "s_nop 0\n"
        "global_load_lds_dwordx4 v238, s[60:61] offset:0\n"
        "global_load_lds_dwordx4 v239, s[60:61] offset:1024\n"
        "global_load_lds_dwordx4 v240, s[60:61] offset:2048\n"
        "global_load_lds_dwordx4 v241, s[60:61] offset:3072\n"
        "s_add_u32 m0, s72, 0x0\n"
        "s_nop 0\n"
        "global_load_lds_dwordx4 v234, s[56:57] offset:0\n"
        "global_load_lds_dwordx4 v235, s[56:57] offset:1024\n"
        "global_load_lds_dwordx4 v236, s[56:57] offset:2048\n"
        "global_load_lds_dwordx4 v237, s[56:57] offset:3072\n"
        "s_add_u32 m0, s72, 0x1000\n"
        "s_nop 0\n"
        "global_load_lds_dwordx4 v234, s[58:59] offset:0\n"
        "global_load_lds_dwordx4 v235, s[58:59] offset:1024\n"
        "global_load_lds_dwordx4 v236, s[58:59] offset:2048\n"
        "global_load_lds_dwordx4 v237, s[58:59] offset:3072\n"
        "s_add_u32 s56, s56, 0x80\n"
        "s_addc_u32 s57, s57, 0\n"
        "s_add_u32 s58, s58, 0x80\n"
        "s_addc_u32 s59, s59, 0\n"
        "s_add_u32 m0, s73, 0x14000\n"
        "s_nop 0\n"
        "global_load_lds_dwordx4 v238, s[62:63] offset:0\n"
        "global_load_lds_dwordx4 v239, s[62:63] offset:1024\n"
        "global_load_lds_dwordx4 v240, s[62:63] offset:2048\n"
        "global_load_lds_dwordx4 v241, s[62:63] offset:3072\n"
        "s_add_u32 s60, s60, 0x80000\n"
        "s_addc_u32 s61, s61, 0\n"
        "s_add_u32 s62, s62, 0x80000\n"
        "s_addc_u32 s63, s63, 0\n"
        "s_add_u32 s68, s12, s51\n"
        "s_addc_u32 s69, s13, 0\n"
        "s_sub_u32 s68, s68, 4096\n"
        "s_subb_u32 s69, s69, 0\n"
        "s_add_u32 s48, s38, 0x3900000\n"
        "s_addc_u32 s49, s39, 0\n"
        "v_mov_b32 v11, 0\n"
        "global_load_dwordx2 v[250:251], v11, s[48:49] sc1\n"
        "s_add_u32 m0, s73, 0x18000\n"
        "s_nop 0\n"
        "global_load_lds_dwordx4 v238, s[60:61] offset:0\n"
        "global_load_lds_dwordx4 v239, s[60:61] offset:1024\n"
        "global_load_lds_dwordx4 v240, s[60:61] offset:2048\n"
        "global_load_lds_dwordx4 v241, s[60:61] offset:3072\n"
        "s_add_u32 m0, s73, 0x1c000\n"
        "s_nop 0\n"
        "global_load_lds_dwordx4 v238, s[62:63] offset:0\n"
        "global_load_lds_dwordx4 v239, s[62:63] offset:1024\n"
        "global_load_lds_dwordx4 v240, s[62:63] offset:2048\n"
        "global_load_lds_dwordx4 v241, s[62:63] offset:3072\n"
        "s_add_u32 s60, s60, 0x80000\n"
        "s_addc_u32 s61, s61, 0\n"
        "s_add_u32 s62, s62, 0x80000\n"
        "s_addc_u32 s63, s63, 0\n"
        "v_lshrrev_b32 v8, 1, v1\n"
        "v_xor_b32 v8, v8, v2\n"
        "v_lshlrev_b32 v8, 4, v8\n"
        "v_lshl_add_u32 v8, v1, 7, v8\n"
        "v_xor_b32 v9, 64, v8\n"
        "s_lshr_b32 s50, s40, 1\n"
        "s_lshl_b32 s50, s50, 14\n"
        "v_add_u32 v224, s50, v8\n"
        "v_add_u32 v246, s72, v8\n"
        "v_add_u32 v248, 0x10000, v246\n"
        "v_add_u32 v225, s50, v9\n"
        "v_add_u32 v247, s72, v9\n"
        "v_add_u32 v249, 0x10000, v247\n"
        "s_and_b32 s50, s40, 1\n"
        "s_lshl_b32 s50, s50, 10\n"
        "s_add_u32 s50, s50, 0x10000\n"
        "v_lshlrev_b32 v8, 11, v2\n"
        "v_add_u32 v8, s50, v8\n"
        "v_lshrrev_b32 v9, 2, v1\n"
        "v_or_b32 v10, 4, v9\n"
        "v_and_b32 v11, 1, v2\n"
        "v_and_b32 v13, 3, v0\n"
        "v_lshlrev_b32 v14, 2, v11\n"
        "v_xor_b32 v14, v14, v9\n"
        "v_bfe_u32 v15, v14, 1, 2\n"
        "v_xor_b32 v15, 0, v15\n"
        "v_lshlrev_b32 v15, 5, v15\n"
        "v_lshl_add_u32 v14, v14, 7, v15\n"
        "v_lshl_add_u32 v14, v13, 3, v14\n"
        "v_add_u32 v226, v14, v8\n"
        "v_xor_b32 v228, 32, v226\n"
        "v_xor_b32 v230, 64, v226\n"
        "v_xor_b32 v232, 96, v226\n"
        "v_lshlrev_b32 v14, 2, v11\n"
        "v_xor_b32 v14, v14, v10\n"
        "v_bfe_u32 v15, v14, 1, 2\n"
        "v_xor_b32 v15, 0, v15\n"
        "v_lshlrev_b32 v15, 5, v15\n"
        "v_lshl_add_u32 v14, v14, 7, v15\n"
        "v_lshl_add_u32 v14, v13, 3, v14\n"
        "v_add_u32 v227, v14, v8\n"
        "v_xor_b32 v229, 32, v227\n"
        "v_xor_b32 v231, 64, v227\n"
        "v_xor_b32 v233, 96, v227\n"
        "v_add_u32 v216, 0x10000, v226\n"
        "v_add_u32 v217, 0x10000, v227\n"
        "v_add_u32 v218, 0x10000, v228\n"
        "v_add_u32 v219, 0x10000, v229\n"
        "v_add_u32 v220, 0x10000, v230\n"
        "v_add_u32 v221, 0x10000, v231\n"
        "v_add_u32 v222, 0x10000, v232\n"
        "v_add_u32 v223, 0x10000, v233\n"
        "v_lshl_add_u32 v242, v4, 11, v6\n"
        "v_lshl_add_u32 v8, v4, 11, v7\n"
        "v_add_u32 v243, 15360, v8\n"
        "v_add_u32 v244, 129024, v242\n"
        "v_add_u32 v245, 144384, v8\n"
        "s_lshl_b32 s50, s45, 18\n"
        "s_lshl_b32 s51, s40, 15\n"
        "s_add_u32 s50, s50, s51\n"
        "s_add_u32 s64, s8, s50\n"
        "s_addc_u32 s65, s9, 0\n"
        "s_lshl_b32 s50, s44, 18\n"
        "s_add_u32 s50, s50, s51\n"
        "s_add_u32 s66, s8, s50\n"
        "s_addc_u32 s67, s9, 0\n"
        "s_lshl2_add_u32 s82, s55, s40\n"
        "s_lshl_b32 s82, s82, 11\n"
        "v_and_b32 v20, 0xf0, v254\n"
        "v_lshl_add_u32 v20, v20, 3, s82\n"
        "global_load_dword v253, v20, s[10:11] offset:0\n"
        "s_lshl2_add_u32 s82, s42, s40\n"
        "s_lshl3_add_u32 s82, s82, s41\n"
        "s_lshl_b32 s82, s82, 10\n"
        "v_and_b32 v21, 0x70, v254\n"
        "v_lshl_add_u32 v173, v21, 3, s82\n"
        "s_lshl2_add_u32 s82, s55, s40\n"
        "s_lshl_b32 s82, s82, 12\n"
        "v_bfe_u32 v20, v254, 4, 1\n"
        "v_bfe_u32 v21, v254, 5, 1\n"
        "v_lshlrev_b32 v20, 7, v20\n"
        "v_lshl_add_u32 v20, v21, 11, v20\n"
        "v_add_u32 v199, s82, v20\n"
        "s_mov_b64 exec, 0\n"
        "s_cmp_eq_u32 s40, 0\n"
        "s_cbranch_scc1 LHA0_%=\n"
        "s_cmp_eq_u32 s40, 1\n"
        "s_cbranch_scc1 LHA1_%=\n"
        "s_cmp_eq_u32 s40, 2\n"
        "s_cbranch_scc1 LHA2_%=\n"
        "s_branch LHA3_%=\n"
        "LHRA_%=:\n"
        "s_mov_b64 exec, -1\n"
        "s_branch LFRONT_%=\n"
        "LREDO_%=:\n"
        "v_mov_b32 v112, 0\n"
        "v_mov_b32 v113, 0\n"
        "v_mov_b32 v114, 0\n"
        "v_mov_b32 v115, 0\n"
        "v_mov_b32 v116, 0\n"
        "v_mov_b32 v117, 0\n"
        "v_mov_b32 v118, 0\n"
        "v_mov_b32 v119, 0\n"
        "v_mov_b32 v120, 0\n"
        "v_mov_b32 v121, 0\n"
        "v_mov_b32 v122, 0\n"
        "v_mov_b32 v123, 0\n"
        "v_mov_b32 v124, 0\n"
        "v_mov_b32 v125, 0\n"
        "v_mov_b32 v126, 0\n"
        "v_mov_b32 v127, 0\n"
        "v_mov_b32 v144, 0\n"
        "v_mov_b32 v145, 0\n"
        "v_mov_b32 v146, 0\n"
        "v_mov_b32 v147, 0\n"
        "v_mov_b32 v148, 0\n"
        "v_mov_b32 v149, 0\n"
        "v_mov_b32 v150, 0\n"
        "v_mov_b32 v151, 0\n"
        "v_mov_b32 v152, 0\n"
        "v_mov_b32 v153, 0\n"
        "v_mov_b32 v154, 0\n"
        "v_mov_b32 v155, 0\n"
        "v_mov_b32 v156, 0\n"
        "v_mov_b32 v157, 0\n"
        "v_mov_b32 v158, 0\n"
        "v_mov_b32 v159, 0\n"
        "v_mov_b32 v60, 0\n"
        "v_mov_b32 v61, 0\n"
        "v_mov_b32 v62, 0\n"
        "v_mov_b32 v63, 0\n"
        "v_mov_b32 v24, 0xf149f2ca\n"
        "s_lshl_b32 s97, s53, 7\n"
        "s_add_u32 s97, s97, s96\n"
        "v_add_u32 v27, s97, v243\n"
        "s_lshl_b32 s97, s43, 19\n"
        "s_add_u32 s84, s38, s97\n"
        "s_addc_u32 s85, s39, 0\n"
        "s_add_u32 s84, s84, 0x1000\n"
        "s_addc_u32 s85, s85, 0\n"
        "s_add_u32 s86, s84, 0x1000000\n"
        "s_addc_u32 s87, s85, 0\n"
        "s_lshl_b32 s97, s53, 2\n"
        "s_add_u32 s97, s97, 4\n"
        "LRLOOP_%=:\n"
        "global_load_dwordx4 a[160:163], v254, s[84:85] offset:-4096 sc1\n"
        "global_load_dwordx4 a[164:167], v254, s[84:85] offset:-3072 sc1\n"
        "global_load_dwordx4 a[168:171], v254, s[84:85] offset:-2048 sc1\n"
        "global_load_dwordx4 a[172:175], v254, s[84:85] offset:-1024 sc1\n"
        "s_waitcnt vmcnt(0)\n"
        "v_mfma_f32_16x16x32_bf16 v[40:43], a[160:163], a[176:179], 0\n"
        "v_mfma_f32_16x16x32_bf16 v[40:43], a[164:167], a[180:183], v[40:43]\n"
        "v_mfma_f32_16x16x32_bf16 v[40:43], a[168:171], a[184:187], v[40:43]\n"
        "v_mfma_f32_16x16x32_bf16 v[40:43], a[172:175], a[188:191], v[40:43]\n"
        "s_nop 7\n"
        "global_load_dwordx4 a[160:163], v254, s[84:85] offset:0 sc1\n"
        "global_load_dwordx4 a[164:167], v254, s[84:85] offset:1024 sc1\n"
        "global_load_dwordx4 a[168:171], v254, s[84:85] offset:2048 sc1\n"
        "global_load_dwordx4 a[172:175], v254, s[84:85] offset:3072 sc1\n"
        "s_waitcnt vmcnt(0)\n"
        "v_mfma_f32_16x16x32_bf16 v[44:47], a[160:163], a[176:179], 0\n"
        "v_mfma_f32_16x16x32_bf16 v[44:47], a[164:167], a[180:183], v[44:47]\n"
        "v_mfma_f32_16x16x32_bf16 v[44:47], a[168:171], a[184:187], v[44:47]\n"
        "v_mfma_f32_16x16x32_bf16 v[44:47], a[172:175], a[188:191], v[44:47]\n"
        "s_nop 7\n"
        "v_cmp_gt_i32 vcc, 0, v27\n"
        "s_nop 1\n"
        "v_cndmask_b32 v40, v40, v242, vcc\n"
        "v_cmp_gt_i32 vcc, 1, v27\n"
        "s_nop 1\n"
        "v_cndmask_b32 v41, v41, v242, vcc\n"
        "v_cmp_gt_i32 vcc, 2, v27\n"
        "s_nop 1\n"
        "v_cndmask_b32 v42, v42, v242, vcc\n"
        "v_cmp_gt_i32 vcc, 3, v27\n"
        "s_nop 1\n"
        "v_cndmask_b32 v43, v43, v242, vcc\n"
        "v_cmp_gt_i32 vcc, 16, v27\n"
        "s_nop 1\n"
        "v_cndmask_b32 v44, v44, v242, vcc\n"
        "v_cmp_gt_i32 vcc, 17, v27\n"
        "s_nop 1\n"
        "v_cndmask_b32 v45, v45, v242, vcc\n"
        "v_cmp_gt_i32 vcc, 18, v27\n"
        "s_nop 1\n"
        "v_cndmask_b32 v46, v46, v242, vcc\n"
        "v_cmp_gt_i32 vcc, 19, v27\n"
        "s_nop 1\n"
        "v_cndmask_b32 v47, v47, v242, vcc\n"
        "v_max3_f32 v25, v40, v41, v24\n"
        "v_max3_f32 v25, v42, v43, v25\n"
        "v_max3_f32 v25, v44, v45, v25\n"
        "v_max3_f32 v25, v46, v47, v25\n"
        "v_mov_b32 v28, v25\n"
        "s_nop 1\n"
        "v_permlane16_swap_b32 v25, v28\n"
        "v_max_f32 v25, v25, v28\n"
        "v_mov_b32 v28, v25\n"
        "s_nop 1\n"
        "v_permlane32_swap_b32 v25, v28\n"
        "v_max_f32 v25, v25, v28\n"
        "v_sub_f32 v26, v24, v25\n"
        "v_exp_f32 v26, v26\n"
        "v_mov_b32 v24, v25\n"
        "v_sub_f32 v40, v40, v25\n"
        "v_exp_f32 v40, v40\n"
        "v_sub_f32 v41, v41, v25\n"
        "v_exp_f32 v41, v41\n"
        "v_sub_f32 v42, v42, v25\n"
        "v_exp_f32 v42, v42\n"
        "v_sub_f32 v43, v43, v25\n"
        "v_exp_f32 v43, v43\n"
        "v_sub_f32 v44, v44, v25\n"
        "v_exp_f32 v44, v44\n"
        "v_sub_f32 v45, v45, v25\n"
        "v_exp_f32 v45, v45\n"
        "v_sub_f32 v46, v46, v25\n"
        "v_exp_f32 v46, v46\n"
        "v_sub_f32 v47, v47, v25\n"
        "v_exp_f32 v47, v47\n"
        "v_cvt_pk_bf16_f32 v56, v40, v41\n"
        "v_cvt_pk_bf16_f32 v57, v42, v43\n"
        "v_cvt_pk_bf16_f32 v58, v44, v45\n"
        "v_cvt_pk_bf16_f32 v59, v46, v47\n"
        "v_mul_f32 v112, v112, v26\n"
        "v_mul_f32 v113, v113, v26\n"
        "v_mul_f32 v114, v114, v26\n"
        "v_mul_f32 v115, v115, v26\n"
        "v_mul_f32 v116, v116, v26\n"
        "v_mul_f32 v117, v117, v26\n"
        "v_mul_f32 v118, v118, v26\n"
        "v_mul_f32 v119, v119, v26\n"
        "v_mul_f32 v120, v120, v26\n"
        "v_mul_f32 v121, v121, v26\n"
        "v_mul_f32 v122, v122, v26\n"
        "v_mul_f32 v123, v123, v26\n"
        "v_mul_f32 v124, v124, v26\n"
        "v_mul_f32 v125, v125, v26\n"
        "v_mul_f32 v126, v126, v26\n"
        "v_mul_f32 v127, v127, v26\n"
        "v_mul_f32 v144, v144, v26\n"
        "v_mul_f32 v145, v145, v26\n"
        "v_mul_f32 v146, v146, v26\n"
        "v_mul_f32 v147, v147, v26\n"
        "v_mul_f32 v148, v148, v26\n"
        "v_mul_f32 v149, v149, v26\n"
        "v_mul_f32 v150, v150, v26\n"
        "v_mul_f32 v151, v151, v26\n"
        "v_mul_f32 v152, v152, v26\n"
        "v_mul_f32 v153, v153, v26\n"
        "v_mul_f32 v154, v154, v26\n"
        "v_mul_f32 v155, v155, v26\n"
        "v_mul_f32 v156, v156, v26\n"
        "v_mul_f32 v157, v157, v26\n"
        "v_mul_f32 v158, v158, v26\n"
        "v_mul_f32 v159, v159, v26\n"
        "v_mul_f32 v60, v60, v26\n"
        "v_mul_f32 v61, v61, v26\n"
        "v_mul_f32 v62, v62, v26\n"
        "v_mul_f32 v63, v63, v26\n"
        "s_nop 1\n"
        "v_mfma_f32_16x16x32_bf16 v[60:63], a[80:83], v[56:59], v[60:63]\n"
        "global_load_dwordx4 a[160:163], v254, s[86:87] offset:-4096 sc1\n"
        "global_load_dwordx4 a[164:167], v254, s[86:87] offset:-3072 sc1\n"
        "global_load_dwordx4 a[168:171], v254, s[86:87] offset:-2048 sc1\n"
        "global_load_dwordx4 a[172:175], v254, s[86:87] offset:-1024 sc1\n"
        "s_waitcnt vmcnt(0)\n"
        "v_mfma_f32_16x16x32_bf16 v[112:115], a[160:163], v[56:59], v[112:115]\n"
        "v_mfma_f32_16x16x32_bf16 v[116:119], a[164:167], v[56:59], v[116:119]\n"
        "v_mfma_f32_16x16x32_bf16 v[120:123], a[168:171], v[56:59], v[120:123]\n"
        "v_mfma_f32_16x16x32_bf16 v[124:127], a[172:175], v[56:59], v[124:127]\n"
        "s_nop 7\n"
        "global_load_dwordx4 a[160:163], v254, s[86:87] offset:0 sc1\n"
        "global_load_dwordx4 a[164:167], v254, s[86:87] offset:1024 sc1\n"
        "global_load_dwordx4 a[168:171], v254, s[86:87] offset:2048 sc1\n"
        "global_load_dwordx4 a[172:175], v254, s[86:87] offset:3072 sc1\n"
        "s_waitcnt vmcnt(0)\n"
        "v_mfma_f32_16x16x32_bf16 v[144:147], a[160:163], v[56:59], v[144:147]\n"
        "v_mfma_f32_16x16x32_bf16 v[148:151], a[164:167], v[56:59], v[148:151]\n"
        "v_mfma_f32_16x16x32_bf16 v[152:155], a[168:171], v[56:59], v[152:155]\n"
        "v_mfma_f32_16x16x32_bf16 v[156:159], a[172:175], v[56:59], v[156:159]\n"
        "s_nop 7\n"
        "s_add_u32 s84, s84, 0x2000\n"
        "s_addc_u32 s85, s85, 0\n"
        "s_add_u32 s86, s86, 0x2000\n"
        "s_addc_u32 s87, s87, 0\n"
        "v_subrev_u32 v27, 32, v27\n"
        "s_sub_u32 s97, s97, 1\n"
        "s_cmp_lg_u32 s97, 0\n"
        "s_cbranch_scc1 LRLOOP_%=\n"
        "s_lshl_b32 s97, s53, 20\n"
        "s_lshl_b32 s88, s43, 15\n"
        "s_add_u32 s97, s97, s88\n"
        "s_lshl_b32 s88, s40, 10\n"
        "s_add_u32 s97, s97, s88\n"
        "s_lshl_b32 s88, s96, 6\n"
        "s_add_u32 s97, s97, s88\n"
        "s_add_u32 s97, s97, 0x2000000\n"
        "s_add_u32 s84, s38, s97\n"
        "s_addc_u32 s85, s39, 0\n"
        "v_rcp_f32 v26, v60\n"
        "s_nop 0\n"
        "v_mul_f32 v112, v112, v26\n"
        "v_mul_f32 v113, v113, v26\n"
        "v_mul_f32 v114, v114, v26\n"
        "v_mul_f32 v115, v115, v26\n"
        "v_mul_f32 v116, v116, v26\n"
        "v_mul_f32 v117, v117, v26\n"
        "v_mul_f32 v118, v118, v26\n"
        "v_mul_f32 v119, v119, v26\n"
        "v_cvt_pk_bf16_f32 v112, v112, v113\n"
        "v_cvt_pk_bf16_f32 v113, v114, v115\n"
        "v_cvt_pk_bf16_f32 v114, v116, v117\n"
        "v_cvt_pk_bf16_f32 v115, v118, v119\n"
        "global_store_dwordx4 v254, v[112:115], s[84:85] sc1\n"
        "s_add_u32 s84, s84, 0x2000\n"
        "s_addc_u32 s85, s85, 0\n"
        "v_mul_f32 v120, v120, v26\n"
        "v_mul_f32 v121, v121, v26\n"
        "v_mul_f32 v122, v122, v26\n"
        "v_mul_f32 v123, v123, v26\n"
        "v_mul_f32 v124, v124, v26\n"
        "v_mul_f32 v125, v125, v26\n"
        "v_mul_f32 v126, v126, v26\n"
        "v_mul_f32 v127, v127, v26\n"
        "v_cvt_pk_bf16_f32 v120, v120, v121\n"
        "v_cvt_pk_bf16_f32 v121, v122, v123\n"
        "v_cvt_pk_bf16_f32 v122, v124, v125\n"
        "v_cvt_pk_bf16_f32 v123, v126, v127\n"
        "global_store_dwordx4 v254, v[120:123], s[84:85] sc1\n"
        "s_add_u32 s84, s84, 0x2000\n"
        "s_addc_u32 s85, s85, 0\n"
        "v_mul_f32 v144, v144, v26\n"
        "v_mul_f32 v145, v145, v26\n"
        "v_mul_f32 v146, v146, v26\n"
        "v_mul_f32 v147, v147, v26\n"
        "v_mul_f32 v148, v148, v26\n"
        "v_mul_f32 v149, v149, v26\n"
        "v_mul_f32 v150, v150, v26\n"
        "v_mul_f32 v151, v151, v26\n"
        "v_cvt_pk_bf16_f32 v144, v144, v145\n"
        "v_cvt_pk_bf16_f32 v145, v146, v147\n"
        "v_cvt_pk_bf16_f32 v146, v148, v149\n"
        "v_cvt_pk_bf16_f32 v147, v150, v151\n"
        "global_store_dwordx4 v254, v[144:147], s[84:85] sc1\n"
        "s_add_u32 s84, s84, 0x2000\n"
        "s_addc_u32 s85, s85, 0\n"
        "v_mul_f32 v152, v152, v26\n"
        "v_mul_f32 v153, v153, v26\n"
        "v_mul_f32 v154, v154, v26\n"
        "v_mul_f32 v155, v155, v26\n"
        "v_mul_f32 v156, v156, v26\n"
        "v_mul_f32 v157, v157, v26\n"
        "v_mul_f32 v158, v158, v26\n"
        "v_mul_f32 v159, v159, v26\n"
        "v_cvt_pk_bf16_f32 v152, v152, v153\n"
        "v_cvt_pk_bf16_f32 v153, v154, v155\n"
        "v_cvt_pk_bf16_f32 v154, v156, v157\n"
        "v_cvt_pk_bf16_f32 v155, v158, v159\n"
        "global_store_dwordx4 v254, v[152:155], s[84:85] sc1\n"
        "s_add_u32 s84, s84, 0x2000\n"
        "s_addc_u32 s85, s85, 0\n"
        "s_waitcnt vmcnt(0)\n"
        "s_cmp_eq_u32 s52, 0\n"
        "s_cbranch_scc1 LRET0_%=\n"
        "s_cmp_eq_u32 s52, 1\n"
        "s_cbranch_scc1 LRET1_%=\n"
        "s_cmp_eq_u32 s52, 2\n"
        "s_cbranch_scc1 LRET2_%=\n"
        "s_branch LRET3_%=\n"
        "LRL_%=:\n"
        "s_waitcnt vmcnt(0) lgkmcnt(0)\n"
        "s_nop 7\n"
        "v_accvgpr_write_b32 a176, v96\n"
        "v_accvgpr_write_b32 a177, v97\n"
        "v_accvgpr_write_b32 a178, v98\n"
        "v_accvgpr_write_b32 a179, v99\n"
        "v_accvgpr_write_b32 a180, v100\n"
        "v_accvgpr_write_b32 a181, v101\n"
        "v_accvgpr_write_b32 a182, v102\n"
        "v_accvgpr_write_b32 a183, v103\n"
        "v_accvgpr_write_b32 a184, v104\n"
        "v_accvgpr_write_b32 a185, v105\n"
        "v_accvgpr_write_b32 a186, v106\n"
        "v_accvgpr_write_b32 a187, v107\n"
        "v_accvgpr_write_b32 a188, v108\n"
        "v_accvgpr_write_b32 a189, v109\n"
        "v_accvgpr_write_b32 a190, v110\n"
        "v_accvgpr_write_b32 a191, v111\n"
        "s_mov_b32 s53, s44\n"
        "s_mov_b32 s96, 64\n"
        "s_mov_b32 s52, 0\n"
        "s_branch LREDO_%=\n"
        "LRET0_%=:\n"
        "v_accvgpr_write_b32 a176, v128\n"
        "v_accvgpr_write_b32 a177, v129\n"
        "v_accvgpr_write_b32 a178, v130\n"
        "v_accvgpr_write_b32 a179, v131\n"
        "v_accvgpr_write_b32 a180, v132\n"
        "v_accvgpr_write_b32 a181, v133\n"
        "v_accvgpr_write_b32 a182, v134\n"
        "v_accvgpr_write_b32 a183, v135\n"
        "v_accvgpr_write_b32 a184, v136\n"
        "v_accvgpr_write_b32 a185, v137\n"
        "v_accvgpr_write_b32 a186, v138\n"
        "v_accvgpr_write_b32 a187, v139\n"
        "v_accvgpr_write_b32 a188, v140\n"
        "v_accvgpr_write_b32 a189, v141\n"
        "v_accvgpr_write_b32 a190, v142\n"
        "v_accvgpr_write_b32 a191, v143\n"
        "s_mov_b32 s53, s44\n"
        "s_mov_b32 s96, 0\n"
        "s_mov_b32 s52, 1\n"
        "s_branch LREDO_%=\n"
        "LRET1_%=:\n"
        "s_branch LSL_%=\n"
        "LRH_%=:\n"
        "s_waitcnt vmcnt(0) lgkmcnt(0)\n"
        "s_nop 7\n"
        "v_accvgpr_write_b32 a176, v0\n"
        "v_accvgpr_write_b32 a177, v1\n"
        "v_accvgpr_write_b32 a178, v2\n"
        "v_accvgpr_write_b32 a179, v3\n"
        "v_accvgpr_write_b32 a180, v4\n"
        "v_accvgpr_write_b32 a181, v5\n"
        "v_accvgpr_write_b32 a182, v6\n"
        "v_accvgpr_write_b32 a183, v7\n"
        "v_accvgpr_write_b32 a184, v8\n"
        "v_accvgpr_write_b32 a185, v9\n"
        "v_accvgpr_write_b32 a186, v10\n"
        "v_accvgpr_write_b32 a187, v11\n"
        "v_accvgpr_write_b32 a188, v12\n"
        "v_accvgpr_write_b32 a189, v13\n"
        "v_accvgpr_write_b32 a190, v14\n"
        "v_accvgpr_write_b32 a191, v15\n"
        "s_mov_b32 s53, s45\n"
        "s_mov_b32 s96, 64\n"
        "s_mov_b32 s52, 2\n"
        "s_branch LREDO_%=\n"
        "LRET2_%=:\n"
        "v_accvgpr_write_b32 a176, v64\n"
        "v_accvgpr_write_b32 a177, v65\n"
        "v_accvgpr_write_b32 a178, v66\n"
        "v_accvgpr_write_b32 a179, v67\n"
        "v_accvgpr_write_b32 a180, v68\n"
        "v_accvgpr_write_b32 a181, v69\n"
        "v_accvgpr_write_b32 a182, v70\n"
        "v_accvgpr_write_b32 a183, v71\n"
        "v_accvgpr_write_b32 a184, v72\n"
        "v_accvgpr_write_b32 a185, v73\n"
        "v_accvgpr_write_b32 a186, v74\n"
        "v_accvgpr_write_b32 a187, v75\n"
        "v_accvgpr_write_b32 a188, v76\n"
        "v_accvgpr_write_b32 a189, v77\n"
        "v_accvgpr_write_b32 a190, v78\n"
        "v_accvgpr_write_b32 a191, v79\n"
        "s_mov_b32 s53, s45\n"
        "s_mov_b32 s96, 0\n"
        "s_mov_b32 s52, 3\n"
        "s_branch LREDO_%=\n"
        "LRET3_%=:\n"
        "s_branch LSH_%=\n"
        "LEP_%=:\n"
        "v_lshlrev_b32 v88, 3, v254\n"
        "v_add_u32 v88, 0x8000, v88\n"
        "s_mov_b32 s96, 4\n"
        "s_mov_b32 s97, 0x200000\n"
        "LEQ_%=:\n"
        "global_load_dwordx2 v[90:91], v88, s[48:49] sc1\n"
        "s_waitcnt vmcnt(0)\n"
        "v_cmp_ne_u64 vcc, v[90:91], v[250:251]\n"
        "s_nop 1\n"
        "s_cbranch_vccz LER_%=\n"
        "s_sleep 2\n"
        "s_sub_u32 s97, s97, 1\n"
        "s_cmp_lg_u32 s97, 0\n"
        "s_cbranch_scc1 LEQ_%=\n"
        "LER_%=:\n"
        "v_add_u32 v88, 0x2000, v88\n"
        "s_sub_u32 s96, s96, 1\n"
        "s_cmp_lg_u32 s96, 0\n"
        "s_cbranch_scc1 LEQ_%=\n"
        "v_mov_b32 v88, 0\n"
        "s_mov_b64 exec, 1\n"
        "global_store_dwordx2 v88, v[250:251], s[48:49] sc1\n"
        "s_mov_b64 exec, -1\n"
        "s_branch LEB_%=\n"
        "LPKV_%=:\n"
        "s_mov_b32 s97, 0x200000\n"
        "LQKV_%=:\n"
        "global_load_dwordx2 v[252:253], v245, s[48:49] sc1\n"
        "s_waitcnt vmcnt(0)\n"
        "v_cmp_ne_u64 vcc, v[252:253], v[250:251]\n"
        "s_nop 1\n"
        "s_cbranch_vccz LBKV_%=\n"
        "s_sleep 2\n"
        "s_sub_u32 s97, s97, 1\n"
        "s_cmp_lg_u32 s97, 0\n"
        "s_cbranch_scc1 LQKV_%=\n"
        "s_branch LBKV_%=\n"
        "LPCX_%=:\n"
        "s_mov_b32 s97, 0x200000\n"
        "LQCX_%=:\n"
        "global_load_dwordx2 v[236:237], v238, s[48:49] sc1\n"
        "s_waitcnt vmcnt(0)\n"
        "v_cmp_ne_u64 vcc, v[236:237], v[250:251]\n"
        "s_nop 1\n"
        "s_cbranch_vccz LBCX_%=\n"
        "s_sleep 2\n"
        "s_sub_u32 s97, s97, 1\n"
        "s_cmp_lg_u32 s97, 0\n"
        "s_cbranch_scc1 LQCX_%=\n"
        "s_branch LBCX_%=\n"
        "LPZZ_%=:\n"
        "s_mov_b32 s97, 0x200000\n"
        "LQZZ_%=:\n"
        "global_load_dwordx2 v[236:237], v234, s[48:49] sc1\n"
        "s_waitcnt vmcnt(0)\n"
        "v_cmp_ne_u64 vcc, v[236:237], v[250:251]\n"
        "s_nop 1\n"
        "s_cbranch_vccz LBZZ_%=\n"
        "s_sleep 2\n"
        "s_sub_u32 s97, s97, 1\n"
        "s_cmp_lg_u32 s97, 0\n"
        "s_cbranch_scc1 LQZZ_%=\n"
        "s_branch LBZZ_%=\n"
        "LPPR_%=:\n"
        "s_mov_b32 s97, 0x200000\n"
        "LQPR_%=:\n"
        "global_load_dwordx2 v[236:237], v235, s[48:49] sc1\n"
        "s_waitcnt vmcnt(0)\n"
        "v_cmp_ne_u64 vcc, v[236:237], v[250:251]\n"
        "s_nop 1\n"
        "s_cbranch_vccz LBPR_%=\n"
        "s_sleep 2\n"
        "s_sub_u32 s97, s97, 1\n"
        "s_cmp_lg_u32 s97, 0\n"
        "s_cbranch_scc1 LQPR_%=\n"
        "s_branch LBPR_%=\n"
        ".p2align 12\n"
        "LFRONT_%=:\n"
        "LHA0_%=:\n"
        "s_cbranch_execz LHA4_%=\n"
        "; ---- FRONT\n"
        "s_add_u32 m0, s72, 0x8000\n"
        "s_waitcnt vmcnt(14)\n"
        "global_load_lds_dwordx4 v234, s[56:57] offset:0\n"
        "s_barrier\n"
        "ds_read_b64_tr_b16 v[32:33], v226 offset:0\n"
        "ds_read_b64_tr_b16 v[34:35], v227 offset:0\n"
        "ds_read_b128 v[0:3], v224 offset:0\n"
        "global_load_lds_dwordx4 v235, s[56:57] offset:1024\n"
        "global_load_lds_dwordx4 v236, s[56:57] offset:2048\n"
        "ds_read_b128 v[4:7], v225 offset:0\n"
        "ds_read_b64_tr_b16 v[64:65], v226 offset:8192\n"
        "s_lshl_b32 s50, s42, 6\n"
        "s_waitcnt lgkmcnt(2)\n"
        "v_mfma_f32_16x16x32_bf16 a[0:3], v[32:35], v[0:3], 0\n"
        "ds_read_b64_tr_b16 v[66:67], v227 offset:8192\n"
        "global_load_lds_dwordx4 v237, s[56:57] offset:3072\n"
        "ds_read_b128 v[12:15], v225 offset:2048\n"
        "ds_read_b128 v[8:11], v224 offset:2048\n"
        "s_add_u32 m0, s72, 0x9000\n"
        "s_waitcnt lgkmcnt(2)\n"
        "global_load_lds_dwordx4 v234, s[58:59] offset:0\n"
        "ds_read_b64_tr_b16 v[36:37], v228 offset:0\n"
        "ds_read_b64_tr_b16 v[38:39], v229 offset:0\n"
        "ds_read_b64_tr_b16 v[68:69], v228 offset:8192\n"
        "s_lshl_b32 s51, s41, 7\n"
        "v_mfma_f32_16x16x32_bf16 a[0:3], v[64:67], v[4:7], a[0:3]\n"
        "ds_read_b64_tr_b16 v[70:71], v229 offset:8192\n"
        "ds_read_b64_tr_b16 v[40:41], v230 offset:0\n"
        "ds_read_b64_tr_b16 v[42:43], v231 offset:0\n"
        "ds_read_b64_tr_b16 v[72:73], v230 offset:8192\n"
        "s_lshl_b32 s52, s40, 5\n"
        "s_waitcnt lgkmcnt(8)\n"
        "v_mfma_f32_16x16x32_bf16 a[32:35], v[64:67], v[12:15], 0\n"
        "ds_read_b64_tr_b16 v[74:75], v231 offset:8192\n"
        "s_add_u32 s51, s51, s52\n"
        "s_waitcnt lgkmcnt(8)\n"
        "v_mfma_f32_16x16x32_bf16 a[32:35], v[32:35], v[8:11], a[32:35]\n"
        "ds_read_b64_tr_b16 v[44:45], v232 offset:0\n"
        "ds_read_b64_tr_b16 v[46:47], v233 offset:0\n"
        "ds_read_b64_tr_b16 v[76:77], v232 offset:8192\n"
        "ds_read_b64_tr_b16 v[78:79], v233 offset:8192\n"
        "s_waitcnt vmcnt(15)\n"
        "s_barrier\n"
        "ds_read_b64_tr_b16 v[48:49], v226 offset:16384\n"
        "s_lshl2_add_u32 s90, s43, s40\n"
        "s_waitcnt lgkmcnt(11)\n"
        "v_mfma_f32_16x16x32_bf16 a[36:39], v[36:39], v[8:11], 0\n"
        "ds_read_b64_tr_b16 v[50:51], v227 offset:16384\n"
        "global_load_lds_dwordx4 v235, s[58:59] offset:1024\n"
        "s_lshl_b32 s91, s44, 7\n"
        "s_waitcnt lgkmcnt(10)\n"
        "v_mfma_f32_16x16x32_bf16 a[36:39], v[68:71], v[12:15], a[36:39]\n"
        "ds_read_b64_tr_b16 v[80:81], v226 offset:24576\n"
        "s_add_u32 s56, s56, 0x80\n"
        "s_addc_u32 s57, s57, 0\n"
        "v_mfma_f32_16x16x32_bf16 a[4:7], v[68:71], v[4:7], 0\n"
        "ds_read_b64_tr_b16 v[82:83], v227 offset:24576\n"
        "s_add_u32 s80, s91, s90\n"
        "v_lshrrev_b32 v166, 4, v254\n"
        "v_mfma_f32_16x16x32_bf16 a[4:7], v[36:39], v[0:3], a[4:7]\n"
        "ds_read_b64_tr_b16 v[52:53], v228 offset:16384\n"
        "s_lshl_b32 s91, s80, 11\n"
        "s_waitcnt lgkmcnt(9)\n"
        "v_mfma_f32_16x16x32_bf16 a[8:11], v[40:43], v[0:3], 0\n"
        "ds_read_b64_tr_b16 v[54:55], v229 offset:16384\n"
        "global_load_lds_dwordx4 v236, s[58:59] offset:2048\n"
        "s_lshl_b32 s80, s80, 7\n"
        "v_mfma_f32_16x16x32_bf16 a[8:11], v[72:75], v[4:7], a[8:11]\n"
        "ds_read_b64_tr_b16 v[84:85], v228 offset:24576\n"
        "v_lshrrev_b32 v167, 3, v166\n"
        "v_mfma_f32_16x16x32_bf16 a[40:43], v[72:75], v[12:15], 0\n"
        "ds_read_b64_tr_b16 v[86:87], v229 offset:24576\n"
        "v_and_b32 v168, 7, v166\n"
        "v_mfma_f32_16x16x32_bf16 a[40:43], v[40:43], v[8:11], a[40:43]\n"
        "ds_read_b64_tr_b16 v[56:57], v230 offset:16384\n"
        "s_waitcnt lgkmcnt(8)\n"
        "v_mfma_f32_16x16x32_bf16 a[44:47], v[44:47], v[8:11], 0\n"
        "ds_read_b64_tr_b16 v[58:59], v231 offset:16384\n"
        "global_load_lds_dwordx4 v237, s[58:59] offset:3072\n"
        "s_add_u32 m0, s73, 0x20000\n"
        "v_mfma_f32_16x16x32_bf16 a[44:47], v[76:79], v[12:15], a[44:47]\n"
        "ds_read_b64_tr_b16 v[88:89], v230 offset:24576\n"
        "s_add_u32 s58, s58, 0x80\n"
        "s_addc_u32 s59, s59, 0\n"
        "v_mfma_f32_16x16x32_bf16 a[12:15], v[76:79], v[4:7], 0\n"
        "ds_read_b64_tr_b16 v[90:91], v231 offset:24576\n"
        "v_lshl_add_u32 v167, v167, 3, s50\n"
        "v_mfma_f32_16x16x32_bf16 a[12:15], v[44:47], v[0:3], a[12:15]\n"
        "ds_read_b64_tr_b16 v[60:61], v232 offset:16384\n"
        "s_waitcnt lgkmcnt(8)\n"
        "v_mfma_f32_16x16x32_bf16 a[16:19], v[0:3], v[48:51], 0\n"
        "ds_read_b64_tr_b16 v[62:63], v233 offset:16384\n"
        "global_load_lds_dwordx4 v238, s[60:61] offset:0\n"
        "v_mfma_f32_16x16x32_bf16 a[16:19], v[4:7], v[80:83], a[16:19]\n"
        "ds_read_b64_tr_b16 v[92:93], v232 offset:24576\n"
        "v_lshl_add_u32 v168, v168, 2, s51\n"
        "v_mfma_f32_16x16x32_bf16 a[48:51], v[12:15], v[80:83], 0\n"
        "ds_read_b64_tr_b16 v[94:95], v233 offset:24576\n"
        "v_lshlrev_b32 v169, 11, v167\n"
        "v_mfma_f32_16x16x32_bf16 a[48:51], v[8:11], v[48:51], a[48:51]\n"
        "ds_read_b128 v[16:19], v224 offset:4096\n"
        "s_waitcnt lgkmcnt(8)\n"
        "v_mfma_f32_16x16x32_bf16 a[52:55], v[8:11], v[52:55], 0\n"
        "ds_read_b128 v[20:23], v225 offset:4096\n"
        "global_load_lds_dwordx4 v239, s[60:61] offset:1024\n"
        "v_mfma_f32_16x16x32_bf16 a[52:55], v[12:15], v[84:87], a[52:55]\n"
        "ds_read_b128 v[24:27], v224 offset:6144\n"
        "v_lshl_add_u32 v169, v168, 1, v169\n"
        "v_mfma_f32_16x16x32_bf16 a[20:23], v[4:7], v[84:87], 0\n"
        "ds_read_b128 v[28:31], v225 offset:6144\n"
        "v_cmp_eq_u32 s[78:79], 0, v167\n"
        "v_mfma_f32_16x16x32_bf16 a[20:23], v[0:3], v[52:55], a[20:23]\n"
        "s_waitcnt lgkmcnt(7)\n"
        "v_mov_b32 v171, 6144\n"
        "v_cndmask_b32 v170, 0, v171, s[78:79]\n"
        "v_mfma_f32_16x16x32_bf16 a[24:27], v[0:3], v[56:59], 0\n"
        "global_load_lds_dwordx4 v240, s[60:61] offset:2048\n"
        "v_mfma_f32_16x16x32_bf16 a[24:27], v[4:7], v[88:91], a[24:27]\n"
        "s_waitcnt lgkmcnt(5)\n"
        "v_add_u32 v170, v170, v169\n"
        "v_sub_u32 v171, 2040, v167\n"
        "v_mfma_f32_16x16x32_bf16 a[56:59], v[12:15], v[88:91], 0\n"
        "s_waitcnt lgkmcnt(4)\n"
        "v_lshlrev_b32 v171, 1, v171\n"
        "v_lshl_add_u32 v171, v168, 12, v171\n"
        "v_mfma_f32_16x16x32_bf16 a[56:59], v[8:11], v[56:59], a[56:59]\n"
        "s_waitcnt lgkmcnt(3)\n"
        "v_lshlrev_b32 v168, 3, v168\n"
        "v_lshrrev_b32 v252, 2, v254\n"
        "v_mfma_f32_16x16x32_bf16 a[60:63], v[8:11], v[60:63], 0\n"
        "global_load_lds_dwordx4 v241, s[60:61] offset:3072\n"
        "s_add_u32 m0, s73, 0x24000\n"
        "v_mfma_f32_16x16x32_bf16 a[60:63], v[12:15], v[92:95], a[60:63]\n"
        "ds_read_b128 v[8:11], v224 offset:10240\n"
        "s_add_u32 s60, s60, 0x80000\n"
        "s_addc_u32 s61, s61, 0\n"
        "v_mfma_f32_16x16x32_bf16 a[28:31], v[4:7], v[92:95], 0\n"
        "ds_read_b128 v[4:7], v225 offset:8192\n"
        "s_sub_u32 s74, s20, 2048\n"
        "s_subb_u32 s75, s21, 0\n"
        "v_mfma_f32_16x16x32_bf16 a[28:31], v[0:3], v[60:63], a[28:31]\n"
        "ds_read_b128 v[0:3], v224 offset:8192\n"
        "s_add_u32 s92, s8, s91\n"
        "s_addc_u32 s93, s9, 0\n"
        "v_mfma_f32_16x16x32_bf16 a[64:67], v[32:35], v[16:19], 0\n"
        "s_waitcnt lgkmcnt(3)\n"
        "global_load_lds_dwordx4 v238, s[62:63] offset:0\n"
        "s_lshl_b32 s91, s45, 7\n"
        "v_mfma_f32_16x16x32_bf16 a[64:67], v[64:67], v[20:23], a[64:67]\n"
        "ds_read_b128 v[12:15], v225 offset:10240\n"
        "s_add_u32 s81, s91, s90\n"
        "s_lshl_b32 s91, s81, 11\n"
        "v_mfma_f32_16x16x32_bf16 a[96:99], v[64:67], v[28:31], 0\n"
        "s_lshl_b32 s90, s41, 7\n"
        "s_lshl2_add_u32 s90, s42, s90\n"
        "s_add_u32 s90, s90, s40\n"
        "v_mfma_f32_16x16x32_bf16 a[96:99], v[32:35], v[24:27], a[96:99]\n"
        "s_lshl_b32 s90, s90, 1\n"
        "v_mov_b32 v172, s90\n"
        "v_lshlrev_b32 v198, 11, v172\n"
        "v_mfma_f32_16x16x32_bf16 a[100:103], v[36:39], v[24:27], 0\n"
        "global_load_lds_dwordx4 v239, s[62:63] offset:1024\n"
        "s_lshl_b32 s81, s81, 7\n"
        "v_mfma_f32_16x16x32_bf16 a[100:103], v[68:71], v[28:31], a[100:103]\n"
        "s_add_u32 s76, s38, 0x3001000\n"
        "s_addc_u32 s77, s39, 0\n"
        "v_mfma_f32_16x16x32_bf16 a[68:71], v[68:71], v[20:23], 0\n"
        "v_mfma_f32_16x16x32_bf16 a[68:71], v[36:39], v[16:19], a[68:71]\n"
        "v_mfma_f32_16x16x32_bf16 a[72:75], v[40:43], v[16:19], 0\n"
        "global_load_lds_dwordx4 v240, s[62:63] offset:2048\n"
        "v_mfma_f32_16x16x32_bf16 a[72:75], v[72:75], v[20:23], a[72:75]\n"
        "v_mfma_f32_16x16x32_bf16 a[104:107], v[72:75], v[28:31], 0\n"
        "v_mfma_f32_16x16x32_bf16 a[104:107], v[40:43], v[24:27], a[104:107]\n"
        "v_mfma_f32_16x16x32_bf16 a[108:111], v[44:47], v[24:27], 0\n"
        "global_load_lds_dwordx4 v241, s[62:63] offset:3072\n"
        "s_add_u32 m0, s72, 0x0\n"
        "v_mfma_f32_16x16x32_bf16 a[108:111], v[76:79], v[28:31], a[108:111]\n"
        "s_add_u32 s62, s62, 0x80000\n"
        "s_addc_u32 s63, s63, 0\n"
        "v_mfma_f32_16x16x32_bf16 a[76:79], v[76:79], v[20:23], 0\n"
        "v_mfma_f32_16x16x32_bf16 a[76:79], v[44:47], v[16:19], a[76:79]\n"
        "v_mfma_f32_16x16x32_bf16 a[80:83], v[16:19], v[48:51], 0\n"
        "v_mfma_f32_16x16x32_bf16 a[80:83], v[20:23], v[80:83], a[80:83]\n"
        "v_mfma_f32_16x16x32_bf16 a[112:115], v[28:31], v[80:83], 0\n"
        "v_mfma_f32_16x16x32_bf16 a[112:115], v[24:27], v[48:51], a[112:115]\n"
        "v_mfma_f32_16x16x32_bf16 a[116:119], v[24:27], v[52:55], 0\n"
        "v_mfma_f32_16x16x32_bf16 a[116:119], v[28:31], v[84:87], a[116:119]\n"
        "s_waitcnt lgkmcnt(0)\n"
        "v_mfma_f32_16x16x32_bf16 a[84:87], v[20:23], v[84:87], 0\n"
        "v_mfma_f32_16x16x32_bf16 a[84:87], v[16:19], v[52:55], a[84:87]\n"
        "v_mfma_f32_16x16x32_bf16 a[88:91], v[16:19], v[56:59], 0\n"
        "v_mfma_f32_16x16x32_bf16 a[88:91], v[20:23], v[88:91], a[88:91]\n"
        "v_mfma_f32_16x16x32_bf16 a[120:123], v[28:31], v[88:91], 0\n"
        "v_mfma_f32_16x16x32_bf16 a[120:123], v[24:27], v[56:59], a[120:123]\n"
        "v_mfma_f32_16x16x32_bf16 a[124:127], v[24:27], v[60:63], 0\n"
        "ds_read_b128 v[24:27], v224 offset:14336\n"
        "v_mfma_f32_16x16x32_bf16 a[124:127], v[28:31], v[92:95], a[124:127]\n"
        "ds_read_b128 v[28:31], v225 offset:14336\n"
        "v_mfma_f32_16x16x32_bf16 a[92:95], v[20:23], v[92:95], 0\n"
        "ds_read_b128 v[20:23], v225 offset:12288\n"
        "v_mfma_f32_16x16x32_bf16 a[92:95], v[16:19], v[60:63], a[92:95]\n"
        "ds_read_b128 v[16:19], v224 offset:12288\n"
        "v_mfma_f32_16x16x32_bf16 a[128:131], v[32:35], v[0:3], 0\n"
        "v_mfma_f32_16x16x32_bf16 a[128:131], v[64:67], v[4:7], a[128:131]\n"
        "v_mfma_f32_16x16x32_bf16 a[160:163], v[64:67], v[12:15], 0\n"
        "v_mfma_f32_16x16x32_bf16 a[160:163], v[32:35], v[8:11], a[160:163]\n"
        "v_mfma_f32_16x16x32_bf16 a[164:167], v[36:39], v[8:11], 0\n"
        "v_mfma_f32_16x16x32_bf16 a[164:167], v[68:71], v[12:15], a[164:167]\n"
        "v_mfma_f32_16x16x32_bf16 a[132:135], v[68:71], v[4:7], 0\n"
        "v_mfma_f32_16x16x32_bf16 a[132:135], v[36:39], v[0:3], a[132:135]\n"
        "v_mfma_f32_16x16x32_bf16 a[136:139], v[40:43], v[0:3], 0\n"
        "v_mfma_f32_16x16x32_bf16 a[136:139], v[72:75], v[4:7], a[136:139]\n"
        "v_mfma_f32_16x16x32_bf16 a[168:171], v[72:75], v[12:15], 0\n"
        "v_mfma_f32_16x16x32_bf16 a[168:171], v[40:43], v[8:11], a[168:171]\n"
        "v_mfma_f32_16x16x32_bf16 a[172:175], v[44:47], v[8:11], 0\n"
        "v_mfma_f32_16x16x32_bf16 a[172:175], v[76:79], v[12:15], a[172:175]\n"
        "v_mfma_f32_16x16x32_bf16 a[140:143], v[76:79], v[4:7], 0\n"
        "v_mfma_f32_16x16x32_bf16 a[140:143], v[44:47], v[0:3], a[140:143]\n"
        "v_mfma_f32_16x16x32_bf16 a[144:147], v[0:3], v[48:51], 0\n"
        "v_mfma_f32_16x16x32_bf16 a[144:147], v[4:7], v[80:83], a[144:147]\n"
        "v_mfma_f32_16x16x32_bf16 a[176:179], v[12:15], v[80:83], 0\n"
        "v_mfma_f32_16x16x32_bf16 a[176:179], v[8:11], v[48:51], a[176:179]\n"
        "v_mfma_f32_16x16x32_bf16 a[180:183], v[8:11], v[52:55], 0\n"
        "v_mfma_f32_16x16x32_bf16 a[180:183], v[12:15], v[84:87], a[180:183]\n"
        "s_waitcnt lgkmcnt(0)\n"
        "v_mfma_f32_16x16x32_bf16 a[148:151], v[4:7], v[84:87], 0\n"
        "v_mfma_f32_16x16x32_bf16 a[148:151], v[0:3], v[52:55], a[148:151]\n"
        "global_load_dwordx4 v[134:137], v168, s[24:25] offset:0\n"
        "v_mfma_f32_16x16x32_bf16 a[152:155], v[0:3], v[56:59], 0\n"
        "v_mfma_f32_16x16x32_bf16 a[152:155], v[4:7], v[88:91], a[152:155]\n"
        "v_mfma_f32_16x16x32_bf16 a[184:187], v[12:15], v[88:91], 0\n"
        "v_mfma_f32_16x16x32_bf16 a[184:187], v[8:11], v[56:59], a[184:187]\n"
        "s_waitcnt vmcnt(9)\n"
        "s_barrier\n"
        "v_mfma_f32_16x16x32_bf16 a[188:191], v[8:11], v[60:63], 0\n"
        "global_load_lds_dwordx4 v234, s[56:57] offset:0\n"
        "ds_read_b128 v[8:11], v224 offset:34816\n"
        "v_mfma_f32_16x16x32_bf16 a[188:191], v[12:15], v[92:95], a[188:191]\n"
        "ds_read_b128 v[12:15], v225 offset:34816\n"
        "v_mfma_f32_16x16x32_bf16 a[156:159], v[4:7], v[92:95], 0\n"
        "ds_read_b128 v[4:7], v225 offset:32768\n"
        "v_mfma_f32_16x16x32_bf16 a[156:159], v[0:3], v[60:63], a[156:159]\n"
        "ds_read_b128 v[0:3], v224 offset:32768\n"
        "s_add_u32 s82, s12, 0x0\n"
        "s_addc_u32 s83, s13, 0\n"
        "v_mfma_f32_16x16x32_bf16 a[192:195], v[32:35], v[16:19], 0\n"
        "global_load_lds_dwordx4 v235, s[56:57] offset:1024\n"
        "v_mfma_f32_16x16x32_bf16 a[192:195], v[64:67], v[20:23], a[192:195]\n"
        "v_mfma_f32_16x16x32_bf16 a[224:227], v[64:67], v[28:31], 0\n"
        "ds_read_b64_tr_b16 v[64:65], v226 offset:40960\n"
        "v_mfma_f32_16x16x32_bf16 a[224:227], v[32:35], v[24:27], a[224:227]\n"
        "ds_read_b64_tr_b16 v[32:33], v226 offset:32768\n"
        "v_mfma_f32_16x16x32_bf16 a[228:231], v[36:39], v[24:27], 0\n"
        "ds_read_b64_tr_b16 v[34:35], v227 offset:32768\n"
        "global_load_dwordx4 v[138:141], v168, s[24:25] offset:16\n"
        "v_mfma_f32_16x16x32_bf16 a[228:231], v[68:71], v[28:31], a[228:231]\n"
        "ds_read_b64_tr_b16 v[66:67], v227 offset:40960\n"
        "v_mfma_f32_16x16x32_bf16 a[196:199], v[68:71], v[20:23], 0\n"
        "ds_read_b64_tr_b16 v[68:69], v228 offset:40960\n"
        "v_mfma_f32_16x16x32_bf16 a[196:199], v[36:39], v[16:19], a[196:199]\n"
        "ds_read_b64_tr_b16 v[36:37], v228 offset:32768\n"
        "v_mfma_f32_16x16x32_bf16 a[200:203], v[40:43], v[16:19], 0\n"
        "ds_read_b64_tr_b16 v[38:39], v229 offset:32768\n"
        "global_load_lds_dwordx4 v236, s[56:57] offset:2048\n"
        "v_mfma_f32_16x16x32_bf16 a[200:203], v[72:75], v[20:23], a[200:203]\n"
        "ds_read_b64_tr_b16 v[70:71], v229 offset:40960\n"
        "v_mfma_f32_16x16x32_bf16 a[232:235], v[72:75], v[28:31], 0\n"
        "ds_read_b64_tr_b16 v[72:73], v230 offset:40960\n"
        "v_mfma_f32_16x16x32_bf16 a[232:235], v[40:43], v[24:27], a[232:235]\n"
        "ds_read_b64_tr_b16 v[40:41], v230 offset:32768\n"
        "v_mfma_f32_16x16x32_bf16 a[236:239], v[44:47], v[24:27], 0\n"
        "ds_read_b64_tr_b16 v[42:43], v231 offset:32768\n"
        "global_load_lds_dwordx4 v237, s[56:57] offset:3072\n"
        "s_add_u32 m0, s72, 0x1000\n"
        "v_mfma_f32_16x16x32_bf16 a[236:239], v[76:79], v[28:31], a[236:239]\n"
        "ds_read_b64_tr_b16 v[74:75], v231 offset:40960\n"
        "s_add_u32 s56, s56, 0x80\n"
        "s_addc_u32 s57, s57, 0\n"
        "v_mfma_f32_16x16x32_bf16 a[204:207], v[76:79], v[20:23], 0\n"
        "ds_read_b64_tr_b16 v[76:77], v232 offset:40960\n"
        "v_mfma_f32_16x16x32_bf16 a[204:207], v[44:47], v[16:19], a[204:207]\n"
        "ds_read_b64_tr_b16 v[44:45], v232 offset:32768\n"
        "v_mfma_f32_16x16x32_bf16 a[208:211], v[16:19], v[48:51], 0\n"
        "ds_read_b64_tr_b16 v[46:47], v233 offset:32768\n"
        "global_load_dwordx2 v[96:97], v170, s[74:75] offset:-4096\n"
        "v_mfma_f32_16x16x32_bf16 a[208:211], v[20:23], v[80:83], a[208:211]\n"
        "ds_read_b64_tr_b16 v[78:79], v233 offset:40960\n"
        "global_load_lds_dwordx4 v234, s[58:59] offset:0\n"
        "v_mfma_f32_16x16x32_bf16 a[240:243], v[28:31], v[80:83], 0\n"
        "ds_read_b64_tr_b16 v[80:81], v226 offset:57344\n"
        "v_mfma_f32_16x16x32_bf16 a[240:243], v[24:27], v[48:51], a[240:243]\n"
        "ds_read_b64_tr_b16 v[48:49], v226 offset:49152\n"
        "v_mfma_f32_16x16x32_bf16 a[244:247], v[24:27], v[52:55], 0\n"
        "ds_read_b64_tr_b16 v[50:51], v227 offset:49152\n"
        "v_mfma_f32_16x16x32_bf16 a[244:247], v[28:31], v[84:87], a[244:247]\n"
        "ds_read_b64_tr_b16 v[82:83], v227 offset:57344\n"
        "global_load_lds_dwordx4 v235, s[58:59] offset:1024\n"
        "v_mfma_f32_16x16x32_bf16 a[212:215], v[20:23], v[84:87], 0\n"
        "ds_read_b64_tr_b16 v[84:85], v228 offset:57344\n"
        "s_waitcnt lgkmcnt(8)\n"
        "v_mfma_f32_16x16x32_bf16 a[212:215], v[16:19], v[52:55], a[212:215]\n"
        "ds_read_b64_tr_b16 v[52:53], v228 offset:49152\n"
        "v_mfma_f32_16x16x32_bf16 a[216:219], v[16:19], v[56:59], 0\n"
        "ds_read_b64_tr_b16 v[54:55], v229 offset:49152\n"
        "v_mfma_f32_16x16x32_bf16 a[216:219], v[20:23], v[88:91], a[216:219]\n"
        "ds_read_b64_tr_b16 v[86:87], v229 offset:57344\n"
        "global_load_dwordx2 v[98:99], v170, s[74:75] offset:-2048\n"
        "v_mfma_f32_16x16x32_bf16 a[248:251], v[28:31], v[88:91], 0\n"
        "global_load_lds_dwordx4 v236, s[58:59] offset:2048\n"
        "ds_read_b64_tr_b16 v[88:89], v230 offset:57344\n"
        "v_mfma_f32_16x16x32_bf16 a[248:251], v[24:27], v[56:59], a[248:251]\n"
        "ds_read_b64_tr_b16 v[56:57], v230 offset:49152\n"
        "v_mfma_f32_16x16x32_bf16 a[252:255], v[24:27], v[60:63], 0\n"
        "ds_read_b64_tr_b16 v[58:59], v231 offset:49152\n"
        "v_mfma_f32_16x16x32_bf16 a[252:255], v[28:31], v[92:95], a[252:255]\n"
        "ds_read_b64_tr_b16 v[90:91], v231 offset:57344\n"
        "v_mfma_f32_16x16x32_bf16 a[220:223], v[20:23], v[92:95], 0\n"
        "global_load_lds_dwordx4 v237, s[58:59] offset:3072\n"
        "ds_read_b64_tr_b16 v[92:93], v232 offset:57344\n"
        "s_add_u32 m0, s73, 0x10000\n"
        "v_mfma_f32_16x16x32_bf16 a[220:223], v[16:19], v[60:63], a[220:223]\n"
        "ds_read_b64_tr_b16 v[60:61], v232 offset:49152\n"
        "s_add_u32 s58, s58, 0x80\n"
        "s_addc_u32 s59, s59, 0\n"
        "v_mfma_f32_16x16x32_bf16 a[0:3], v[32:35], v[0:3], a[0:3]\n"
        "ds_read_b64_tr_b16 v[62:63], v233 offset:49152\n"
        "v_mfma_f32_16x16x32_bf16 a[0:3], v[64:67], v[4:7], a[0:3]\n"
        "ds_read_b64_tr_b16 v[94:95], v233 offset:57344\n"
        "s_waitcnt lgkmcnt(8)\n"
        "v_mfma_f32_16x16x32_bf16 a[32:35], v[64:67], v[12:15], a[32:35]\n"
        "global_load_dwordx2 v[100:101], v170, s[74:75] offset:0\n"
        "ds_read_b128 v[16:19], v224 offset:36864\n"
        "v_mfma_f32_16x16x32_bf16 a[32:35], v[32:35], v[8:11], a[32:35]\n"
        "ds_read_b128 v[20:23], v225 offset:36864\n"
        "global_load_lds_dwordx4 v238, s[60:61] offset:0\n"
        "v_mfma_f32_16x16x32_bf16 a[36:39], v[36:39], v[8:11], a[36:39]\n"
        "ds_read_b128 v[24:27], v224 offset:38912\n"
        "v_mfma_f32_16x16x32_bf16 a[36:39], v[68:71], v[12:15], a[36:39]\n"
        "ds_read_b128 v[28:31], v225 offset:38912\n"
        "v_mfma_f32_16x16x32_bf16 a[4:7], v[68:71], v[4:7], a[4:7]\n"
        "v_mfma_f32_16x16x32_bf16 a[4:7], v[36:39], v[0:3], a[4:7]\n"
        "global_load_lds_dwordx4 v239, s[60:61] offset:1024\n"
        "v_mfma_f32_16x16x32_bf16 a[8:11], v[40:43], v[0:3], a[8:11]\n"
        "v_mfma_f32_16x16x32_bf16 a[8:11], v[72:75], v[4:7], a[8:11]\n"
        "v_mfma_f32_16x16x32_bf16 a[40:43], v[72:75], v[12:15], a[40:43]\n"
        "v_mfma_f32_16x16x32_bf16 a[40:43], v[40:43], v[8:11], a[40:43]\n"
        "global_load_dwordx2 v[102:103], v169, s[74:75] offset:2048\n"
        "v_mfma_f32_16x16x32_bf16 a[44:47], v[44:47], v[8:11], a[44:47]\n"
        "global_load_lds_dwordx4 v240, s[60:61] offset:2048\n"
        "s_add_u32 s74, s20, 0x1800\n"
        "s_addc_u32 s75, s21, 0\n"
        "v_mfma_f32_16x16x32_bf16 a[44:47], v[76:79], v[12:15], a[44:47]\n"
        "s_waitcnt lgkmcnt(0)\n"
        "v_mfma_f32_16x16x32_bf16 a[12:15], v[76:79], v[4:7], a[12:15]\n"
        "v_mfma_f32_16x16x32_bf16 a[12:15], v[44:47], v[0:3], a[12:15]\n"
        "v_mfma_f32_16x16x32_bf16 a[16:19], v[0:3], v[48:51], a[16:19]\n"
        "global_load_lds_dwordx4 v241, s[60:61] offset:3072\n"
        "s_add_u32 m0, s73, 0x14000\n"
        "v_mfma_f32_16x16x32_bf16 a[16:19], v[4:7], v[80:83], a[16:19]\n"
        "s_add_u32 s60, s60, 0x80000\n"
        "s_addc_u32 s61, s61, 0\n"
        "v_mfma_f32_16x16x32_bf16 a[48:51], v[12:15], v[80:83], a[48:51]\n"
        "v_mfma_f32_16x16x32_bf16 a[48:51], v[8:11], v[48:51], a[48:51]\n"
        "v_mfma_f32_16x16x32_bf16 a[52:55], v[8:11], v[52:55], a[52:55]\n"
        "global_load_dwordx2 v[104:105], v169, s[74:75] offset:-4096\n"
        "v_mfma_f32_16x16x32_bf16 a[52:55], v[12:15], v[84:87], a[52:55]\n"
        "global_load_lds_dwordx4 v238, s[62:63] offset:0\n"
        "v_mfma_f32_16x16x32_bf16 a[20:23], v[4:7], v[84:87], a[20:23]\n"
        "v_mfma_f32_16x16x32_bf16 a[20:23], v[0:3], v[52:55], a[20:23]\n"
        "v_mfma_f32_16x16x32_bf16 a[24:27], v[0:3], v[56:59], a[24:27]\n"
        "v_mfma_f32_16x16x32_bf16 a[24:27], v[4:7], v[88:91], a[24:27]\n"
        "global_load_lds_dwordx4 v239, s[62:63] offset:1024\n"
        "v_mfma_f32_16x16x32_bf16 a[56:59], v[12:15], v[88:91], a[56:59]\n"
        "v_mfma_f32_16x16x32_bf16 a[56:59], v[8:11], v[56:59], a[56:59]\n"
        "v_mfma_f32_16x16x32_bf16 a[60:63], v[8:11], v[60:63], a[60:63]\n"
        "ds_read_b128 v[8:11], v224 offset:43008\n"
        "v_mfma_f32_16x16x32_bf16 a[60:63], v[12:15], v[92:95], a[60:63]\n"
        "global_load_dwordx2 v[106:107], v169, s[74:75] offset:-2048\n"
        "ds_read_b128 v[12:15], v225 offset:43008\n"
        "v_mfma_f32_16x16x32_bf16 a[28:31], v[4:7], v[92:95], a[28:31]\n"
        "global_load_lds_dwordx4 v240, s[62:63] offset:2048\n"
        "ds_read_b128 v[4:7], v225 offset:40960\n"
        "v_mfma_f32_16x16x32_bf16 a[28:31], v[0:3], v[60:63], a[28:31]\n"
        "ds_read_b128 v[0:3], v224 offset:40960\n"
        "v_mfma_f32_16x16x32_bf16 a[64:67], v[32:35], v[16:19], a[64:67]\n"
        "v_mfma_f32_16x16x32_bf16 a[64:67], v[64:67], v[20:23], a[64:67]\n"
        "v_mfma_f32_16x16x32_bf16 a[96:99], v[64:67], v[28:31], a[96:99]\n"
        "global_load_lds_dwordx4 v241, s[62:63] offset:3072\n"
        "s_add_u32 s62, s62, 0x80000\n"
        "s_addc_u32 s63, s63, 0\n"
        "v_mfma_f32_16x16x32_bf16 a[96:99], v[32:35], v[24:27], a[96:99]\n"
        "s_add_u32 m0, s72, 0x8000\n"
        "v_mfma_f32_16x16x32_bf16 a[100:103], v[36:39], v[24:27], a[100:103]\n"
        "v_mfma_f32_16x16x32_bf16 a[100:103], v[68:71], v[28:31], a[100:103]\n"
        "v_mfma_f32_16x16x32_bf16 a[68:71], v[68:71], v[20:23], a[68:71]\n"
        "global_load_dwordx2 v[108:109], v169, s[74:75] offset:0\n"
        "v_mfma_f32_16x16x32_bf16 a[68:71], v[36:39], v[16:19], a[68:71]\n"
        "global_load_dwordx2 v[110:111], v169, s[74:75] offset:2048\n"
        "s_add_u32 s74, s20, 0x3800\n"
        "s_addc_u32 s75, s21, 0\n"
        "v_mfma_f32_16x16x32_bf16 a[72:75], v[40:43], v[16:19], a[72:75]\n"
        "global_load_dwordx2 v[112:113], v169, s[74:75] offset:-4096\n"
        "v_mfma_f32_16x16x32_bf16 a[72:75], v[72:75], v[20:23], a[72:75]\n"
        "v_mfma_f32_16x16x32_bf16 a[104:107], v[72:75], v[28:31], a[104:107]\n"
        "v_mfma_f32_16x16x32_bf16 a[104:107], v[40:43], v[24:27], a[104:107]\n"
        "v_mfma_f32_16x16x32_bf16 a[108:111], v[44:47], v[24:27], a[108:111]\n"
        "v_mfma_f32_16x16x32_bf16 a[108:111], v[76:79], v[28:31], a[108:111]\n"
        "v_mfma_f32_16x16x32_bf16 a[76:79], v[76:79], v[20:23], a[76:79]\n"
        "v_mfma_f32_16x16x32_bf16 a[76:79], v[44:47], v[16:19], a[76:79]\n"
        "global_load_dwordx2 v[114:115], v169, s[74:75] offset:-2048\n"
        "v_mfma_f32_16x16x32_bf16 a[80:83], v[16:19], v[48:51], a[80:83]\n"
        "v_mfma_f32_16x16x32_bf16 a[80:83], v[20:23], v[80:83], a[80:83]\n"
        "v_mfma_f32_16x16x32_bf16 a[112:115], v[28:31], v[80:83], a[112:115]\n"
        "v_mfma_f32_16x16x32_bf16 a[112:115], v[24:27], v[48:51], a[112:115]\n"
        "v_mfma_f32_16x16x32_bf16 a[116:119], v[24:27], v[52:55], a[116:119]\n"
        "v_mfma_f32_16x16x32_bf16 a[116:119], v[28:31], v[84:87], a[116:119]\n"
        "s_waitcnt lgkmcnt(0)\n"
        "v_mfma_f32_16x16x32_bf16 a[84:87], v[20:23], v[84:87], a[84:87]\n"
        "v_mfma_f32_16x16x32_bf16 a[84:87], v[16:19], v[52:55], a[84:87]\n"
        "global_load_dwordx2 v[116:117], v169, s[74:75] offset:0\n"
        "s_add_u32 s74, s22, 0x1000\n"
        "s_addc_u32 s75, s23, 0\n"
        "v_mfma_f32_16x16x32_bf16 a[88:91], v[16:19], v[56:59], a[88:91]\n"
        "v_mfma_f32_16x16x32_bf16 a[88:91], v[20:23], v[88:91], a[88:91]\n"
        "v_mfma_f32_16x16x32_bf16 a[120:123], v[28:31], v[88:91], a[120:123]\n"
        "v_mfma_f32_16x16x32_bf16 a[120:123], v[24:27], v[56:59], a[120:123]\n"
        "v_mfma_f32_16x16x32_bf16 a[124:127], v[24:27], v[60:63], a[124:127]\n"
        "ds_read_b128 v[24:27], v224 offset:47104\n"
        "v_mfma_f32_16x16x32_bf16 a[124:127], v[28:31], v[92:95], a[124:127]\n"
        "ds_read_b128 v[28:31], v225 offset:47104\n"
        "v_mfma_f32_16x16x32_bf16 a[92:95], v[20:23], v[92:95], a[92:95]\n"
        "ds_read_b128 v[20:23], v225 offset:45056\n"
        "v_mfma_f32_16x16x32_bf16 a[92:95], v[16:19], v[60:63], a[92:95]\n"
        "ds_read_b128 v[16:19], v224 offset:45056\n"
        "global_load_dwordx2 v[118:119], v169, s[74:75] offset:-4096\n"
        "v_mfma_f32_16x16x32_bf16 a[128:131], v[32:35], v[0:3], a[128:131]\n"
        "v_mfma_f32_16x16x32_bf16 a[128:131], v[64:67], v[4:7], a[128:131]\n"
        "v_mfma_f32_16x16x32_bf16 a[160:163], v[64:67], v[12:15], a[160:163]\n"
        "v_mfma_f32_16x16x32_bf16 a[160:163], v[32:35], v[8:11], a[160:163]\n"
        "v_mfma_f32_16x16x32_bf16 a[164:167], v[36:39], v[8:11], a[164:167]\n"
        "v_mfma_f32_16x16x32_bf16 a[164:167], v[68:71], v[12:15], a[164:167]\n"
        "v_mfma_f32_16x16x32_bf16 a[132:135], v[68:71], v[4:7], a[132:135]\n"
        "v_mfma_f32_16x16x32_bf16 a[132:135], v[36:39], v[0:3], a[132:135]\n"
        "global_load_dwordx2 v[120:121], v169, s[74:75] offset:-2048\n"
        "v_mfma_f32_16x16x32_bf16 a[136:139], v[40:43], v[0:3], a[136:139]\n"
        "v_mfma_f32_16x16x32_bf16 a[136:139], v[72:75], v[4:7], a[136:139]\n"
        "v_mfma_f32_16x16x32_bf16 a[168:171], v[72:75], v[12:15], a[168:171]\n"
        "v_mfma_f32_16x16x32_bf16 a[168:171], v[40:43], v[8:11], a[168:171]\n"
        "v_mfma_f32_16x16x32_bf16 a[172:175], v[44:47], v[8:11], a[172:175]\n"
        "v_mfma_f32_16x16x32_bf16 a[172:175], v[76:79], v[12:15], a[172:175]\n"
        "v_mfma_f32_16x16x32_bf16 a[140:143], v[76:79], v[4:7], a[140:143]\n"
        "v_mfma_f32_16x16x32_bf16 a[140:143], v[44:47], v[0:3], a[140:143]\n"
        "global_load_dwordx2 v[122:123], v169, s[74:75] offset:0\n"
        "v_mfma_f32_16x16x32_bf16 a[144:147], v[0:3], v[48:51], a[144:147]\n"
        "v_mfma_f32_16x16x32_bf16 a[144:147], v[4:7], v[80:83], a[144:147]\n"
        "v_mfma_f32_16x16x32_bf16 a[176:179], v[12:15], v[80:83], a[176:179]\n"
        "v_mfma_f32_16x16x32_bf16 a[176:179], v[8:11], v[48:51], a[176:179]\n"
        "v_mfma_f32_16x16x32_bf16 a[180:183], v[8:11], v[52:55], a[180:183]\n"
        "v_mfma_f32_16x16x32_bf16 a[180:183], v[12:15], v[84:87], a[180:183]\n"
        "s_waitcnt lgkmcnt(0)\n"
        "v_mfma_f32_16x16x32_bf16 a[148:151], v[4:7], v[84:87], a[148:151]\n"
        "v_mfma_f32_16x16x32_bf16 a[148:151], v[0:3], v[52:55], a[148:151]\n"
        "global_load_dwordx2 v[124:125], v169, s[74:75] offset:2048\n"
        "s_add_u32 s74, s22, 0x3000\n"
        "s_addc_u32 s75, s23, 0\n"
        "v_mfma_f32_16x16x32_bf16 a[152:155], v[0:3], v[56:59], a[152:155]\n"
        "v_mfma_f32_16x16x32_bf16 a[152:155], v[4:7], v[88:91], a[152:155]\n"
        "v_mfma_f32_16x16x32_bf16 a[184:187], v[12:15], v[88:91], a[184:187]\n"
        "v_mfma_f32_16x16x32_bf16 a[184:187], v[8:11], v[56:59], a[184:187]\n"
        "s_waitcnt vmcnt(21)\n"
        "s_barrier\n"
        "v_mfma_f32_16x16x32_bf16 a[188:191], v[8:11], v[60:63], a[188:191]\n"
        "global_load_lds_dwordx4 v234, s[56:57] offset:0\n"
        "ds_read_b128 v[8:11], v224 offset:2048\n"
        "v_mfma_f32_16x16x32_bf16 a[188:191], v[12:15], v[92:95], a[188:191]\n"
        "ds_read_b128 v[12:15], v225 offset:2048\n"
        "v_mfma_f32_16x16x32_bf16 a[156:159], v[4:7], v[92:95], a[156:159]\n"
        "ds_read_b128 v[4:7], v225 offset:0\n"
        "v_mfma_f32_16x16x32_bf16 a[156:159], v[0:3], v[60:63], a[156:159]\n"
        "ds_read_b128 v[0:3], v224 offset:0\n"
        "v_mfma_f32_16x16x32_bf16 a[192:195], v[32:35], v[16:19], a[192:195]\n"
        "global_load_lds_dwordx4 v235, s[56:57] offset:1024\n"
        "v_mfma_f32_16x16x32_bf16 a[192:195], v[64:67], v[20:23], a[192:195]\n"
        "v_mfma_f32_16x16x32_bf16 a[224:227], v[64:67], v[28:31], a[224:227]\n"
        "ds_read_b64_tr_b16 v[64:65], v216 offset:8192\n"
        "v_mfma_f32_16x16x32_bf16 a[224:227], v[32:35], v[24:27], a[224:227]\n"
        "ds_read_b64_tr_b16 v[32:33], v216 offset:0\n"
        "v_mfma_f32_16x16x32_bf16 a[228:231], v[36:39], v[24:27], a[228:231]\n"
        "ds_read_b64_tr_b16 v[34:35], v217 offset:0\n"
        "global_load_dwordx2 v[126:127], v169, s[74:75] offset:-4096\n"
        "v_mfma_f32_16x16x32_bf16 a[228:231], v[68:71], v[28:31], a[228:231]\n"
        "ds_read_b64_tr_b16 v[66:67], v217 offset:8192\n"
        "global_load_lds_dwordx4 v236, s[56:57] offset:2048\n"
        "v_mfma_f32_16x16x32_bf16 a[196:199], v[68:71], v[20:23], a[196:199]\n"
        "ds_read_b64_tr_b16 v[68:69], v218 offset:8192\n"
        "v_mfma_f32_16x16x32_bf16 a[196:199], v[36:39], v[16:19], a[196:199]\n"
        "ds_read_b64_tr_b16 v[36:37], v218 offset:0\n"
        "v_mfma_f32_16x16x32_bf16 a[200:203], v[40:43], v[16:19], a[200:203]\n"
        "ds_read_b64_tr_b16 v[38:39], v219 offset:0\n"
        "v_mfma_f32_16x16x32_bf16 a[200:203], v[72:75], v[20:23], a[200:203]\n"
        "ds_read_b64_tr_b16 v[70:71], v219 offset:8192\n"
        "global_load_lds_dwordx4 v237, s[56:57] offset:3072\n"
        "s_add_u32 m0, s72, 0x9000\n"
        "v_mfma_f32_16x16x32_bf16 a[232:235], v[72:75], v[28:31], a[232:235]\n"
        "ds_read_b64_tr_b16 v[72:73], v220 offset:8192\n"
        "s_add_u32 s56, s56, 0x80\n"
        "s_addc_u32 s57, s57, 0\n"
        "v_mfma_f32_16x16x32_bf16 a[232:235], v[40:43], v[24:27], a[232:235]\n"
        "ds_read_b64_tr_b16 v[40:41], v220 offset:0\n"
        "v_mfma_f32_16x16x32_bf16 a[236:239], v[44:47], v[24:27], a[236:239]\n"
        "ds_read_b64_tr_b16 v[42:43], v221 offset:0\n"
        "v_mfma_f32_16x16x32_bf16 a[236:239], v[76:79], v[28:31], a[236:239]\n"
        "ds_read_b64_tr_b16 v[74:75], v221 offset:8192\n"
        "global_load_dwordx2 v[128:129], v169, s[74:75] offset:-2048\n"
        "v_mfma_f32_16x16x32_bf16 a[204:207], v[76:79], v[20:23], a[204:207]\n"
        "ds_read_b64_tr_b16 v[76:77], v222 offset:8192\n"
        "global_load_lds_dwordx4 v234, s[58:59] offset:0\n"
        "v_mfma_f32_16x16x32_bf16 a[204:207], v[44:47], v[16:19], a[204:207]\n"
        "ds_read_b64_tr_b16 v[44:45], v222 offset:0\n"
        "v_mfma_f32_16x16x32_bf16 a[208:211], v[16:19], v[48:51], a[208:211]\n"
        "ds_read_b64_tr_b16 v[46:47], v223 offset:0\n"
        "v_mfma_f32_16x16x32_bf16 a[208:211], v[20:23], v[80:83], a[208:211]\n"
        "ds_read_b64_tr_b16 v[78:79], v223 offset:8192\n"
        "v_mfma_f32_16x16x32_bf16 a[240:243], v[28:31], v[80:83], a[240:243]\n"
        "ds_read_b64_tr_b16 v[80:81], v216 offset:24576\n"
        "global_load_lds_dwordx4 v235, s[58:59] offset:1024\n"
        "v_mfma_f32_16x16x32_bf16 a[240:243], v[24:27], v[48:51], a[240:243]\n"
        "ds_read_b64_tr_b16 v[48:49], v216 offset:16384\n"
        "v_lshlrev_b32 v185, 16, v134\n"
        "v_mfma_f32_16x16x32_bf16 a[244:247], v[24:27], v[52:55], a[244:247]\n"
        "ds_read_b64_tr_b16 v[50:51], v217 offset:16384\n"
        "v_and_b32 v186, 0xffff0000, v134\n"
        "v_mfma_f32_16x16x32_bf16 a[244:247], v[28:31], v[84:87], a[244:247]\n"
        "ds_read_b64_tr_b16 v[82:83], v217 offset:24576\n"
        "v_lshlrev_b32 v187, 16, v135\n"
        "v_mfma_f32_16x16x32_bf16 a[212:215], v[20:23], v[84:87], a[212:215]\n"
        "ds_read_b64_tr_b16 v[84:85], v218 offset:24576\n"
        "global_load_dwordx2 v[130:131], v169, s[74:75] offset:0\n"
        "v_mfma_f32_16x16x32_bf16 a[212:215], v[16:19], v[52:55], a[212:215]\n"
        "ds_read_b64_tr_b16 v[52:53], v218 offset:16384\n"
        "global_load_lds_dwordx4 v236, s[58:59] offset:2048\n"
        "v_mfma_f32_16x16x32_bf16 a[216:219], v[16:19], v[56:59], a[216:219]\n"
        "ds_read_b64_tr_b16 v[54:55], v219 offset:16384\n"
        "v_and_b32 v188, 0xffff0000, v135\n"
        "v_mfma_f32_16x16x32_bf16 a[216:219], v[20:23], v[88:91], a[216:219]\n"
        "ds_read_b64_tr_b16 v[86:87], v219 offset:24576\n"
        "s_waitcnt lgkmcnt(8)\n"
        "v_mfma_f32_16x16x32_bf16 a[248:251], v[28:31], v[88:91], a[248:251]\n"
        "ds_read_b64_tr_b16 v[88:89], v220 offset:24576\n"
        "v_mfma_f32_16x16x32_bf16 a[248:251], v[24:27], v[56:59], a[248:251]\n"
        "ds_read_b64_tr_b16 v[56:57], v220 offset:16384\n"
        "global_load_lds_dwordx4 v237, s[58:59] offset:3072\n"
        "s_add_u32 m0, s73, 0x18000\n"
        "v_mfma_f32_16x16x32_bf16 a[252:255], v[24:27], v[60:63], a[252:255]\n"
        "ds_read_b64_tr_b16 v[58:59], v221 offset:16384\n"
        "s_add_u32 s58, s58, 0x80\n"
        "s_addc_u32 s59, s59, 0\n"
        "v_mfma_f32_16x16x32_bf16 a[252:255], v[28:31], v[92:95], a[252:255]\n"
        "LHA1_%=:\n"
        "s_cbranch_execz LHA5_%=\n"
        "ds_read_b64_tr_b16 v[90:91], v221 offset:24576\n"
        "v_mfma_f32_16x16x32_bf16 a[220:223], v[20:23], v[92:95], a[220:223]\n"
        "ds_read_b64_tr_b16 v[92:93], v222 offset:24576\n"
        "v_mfma_f32_16x16x32_bf16 a[220:223], v[16:19], v[60:63], a[220:223]\n"
        "ds_read_b64_tr_b16 v[60:61], v222 offset:16384\n"
        "global_load_dwordx2 v[132:133], v169, s[74:75] offset:2048\n"
        "v_mfma_f32_16x16x32_bf16 a[0:3], v[32:35], v[0:3], a[0:3]\n"
        "ds_read_b64_tr_b16 v[62:63], v223 offset:16384\n"
        "global_load_lds_dwordx4 v238, s[60:61] offset:0\n"
        "v_mfma_f32_16x16x32_bf16 a[0:3], v[64:67], v[4:7], a[0:3]\n"
        "ds_read_b64_tr_b16 v[94:95], v223 offset:24576\n"
        "v_mfma_f32_16x16x32_bf16 a[32:35], v[64:67], v[12:15], a[32:35]\n"
        "ds_read_b128 v[16:19], v224 offset:4096\n"
        "v_mfma_f32_16x16x32_bf16 a[32:35], v[32:35], v[8:11], a[32:35]\n"
        "ds_read_b128 v[20:23], v225 offset:4096\n"
        "v_mfma_f32_16x16x32_bf16 a[36:39], v[36:39], v[8:11], a[36:39]\n"
        "ds_read_b128 v[24:27], v224 offset:6144\n"
        "global_load_lds_dwordx4 v239, s[60:61] offset:1024\n"
        "v_mfma_f32_16x16x32_bf16 a[36:39], v[68:71], v[12:15], a[36:39]\n"
        "ds_read_b128 v[28:31], v225 offset:6144\n"
        "s_waitcnt lgkmcnt(8)\n"
        "v_mfma_f32_16x16x32_bf16 a[4:7], v[68:71], v[4:7], a[4:7]\n"
        "v_mfma_f32_16x16x32_bf16 a[4:7], v[36:39], v[0:3], a[4:7]\n"
        "v_mfma_f32_16x16x32_bf16 a[8:11], v[40:43], v[0:3], a[8:11]\n"
        "global_load_lds_dwordx4 v240, s[60:61] offset:2048\n"
        "v_mfma_f32_16x16x32_bf16 a[8:11], v[72:75], v[4:7], a[8:11]\n"
        "v_mfma_f32_16x16x32_bf16 a[40:43], v[72:75], v[12:15], a[40:43]\n"
        "v_mfma_f32_16x16x32_bf16 a[40:43], v[40:43], v[8:11], a[40:43]\n"
        "v_mfma_f32_16x16x32_bf16 a[44:47], v[44:47], v[8:11], a[44:47]\n"
        "global_load_lds_dwordx4 v241, s[60:61] offset:3072\n"
        "s_add_u32 m0, s73, 0x1c000\n"
        "v_mfma_f32_16x16x32_bf16 a[44:47], v[76:79], v[12:15], a[44:47]\n"
        "s_add_u32 s60, s60, 0x80000\n"
        "s_addc_u32 s61, s61, 0\n"
        "v_cndmask_b32 v96, v96, 0, s[78:79]\n"
        "v_mfma_f32_16x16x32_bf16 a[12:15], v[76:79], v[4:7], a[12:15]\n"
        "v_cndmask_b32 v97, v97, 0, s[78:79]\n"
        "v_lshlrev_b32 v174, 16, v96\n"
        "v_mfma_f32_16x16x32_bf16 a[12:15], v[44:47], v[0:3], a[12:15]\n"
        "v_mfma_f32_16x16x32_bf16 a[16:19], v[0:3], v[48:51], a[16:19]\n"
        "global_load_lds_dwordx4 v238, s[62:63] offset:0\n"
        "v_mfma_f32_16x16x32_bf16 a[16:19], v[4:7], v[80:83], a[16:19]\n"
        "s_waitcnt lgkmcnt(0)\n"
        "v_mfma_f32_16x16x32_bf16 a[48:51], v[12:15], v[80:83], a[48:51]\n"
        "v_mfma_f32_16x16x32_bf16 a[48:51], v[8:11], v[48:51], a[48:51]\n"
        "v_mfma_f32_16x16x32_bf16 a[52:55], v[8:11], v[52:55], a[52:55]\n"
        "global_load_lds_dwordx4 v239, s[62:63] offset:1024\n"
        "v_mfma_f32_16x16x32_bf16 a[52:55], v[12:15], v[84:87], a[52:55]\n"
        "v_cndmask_b32 v98, v98, 0, s[78:79]\n"
        "v_mfma_f32_16x16x32_bf16 a[20:23], v[4:7], v[84:87], a[20:23]\n"
        "v_cndmask_b32 v99, v99, 0, s[78:79]\n"
        "v_lshlrev_b32 v175, 16, v98\n"
        "v_mfma_f32_16x16x32_bf16 a[20:23], v[0:3], v[52:55], a[20:23]\n"
        "v_mul_f32 v189, v174, v188\n"
        "v_mul_f32 v191, v175, v188\n"
        "v_mfma_f32_16x16x32_bf16 a[24:27], v[0:3], v[56:59], a[24:27]\n"
        "global_load_lds_dwordx4 v240, s[62:63] offset:2048\n"
        "v_mfma_f32_16x16x32_bf16 a[24:27], v[4:7], v[88:91], a[24:27]\n"
        "v_cvt_pk_bf16_f32 v189, 0, v189\n"
        "v_cvt_pk_bf16_f32 v191, 0, v191\n"
        "v_mfma_f32_16x16x32_bf16 a[56:59], v[12:15], v[88:91], a[56:59]\n"
        "v_mul_f32 v190, v175, v187\n"
        "v_cvt_pk_bf16_f32 v190, 0, v190\n"
        "v_mfma_f32_16x16x32_bf16 a[56:59], v[8:11], v[56:59], a[56:59]\n"
        "v_add_f32 v189, v189, v190\n"
        "v_cvt_pk_bf16_f32 v189, 0, v189\n"
        "v_mfma_f32_16x16x32_bf16 a[60:63], v[8:11], v[60:63], a[60:63]\n"
        "global_load_lds_dwordx4 v241, s[62:63] offset:3072\n"
        "s_add_u32 m0, s72, 0x0\n"
        "v_mfma_f32_16x16x32_bf16 a[60:63], v[12:15], v[92:95], a[60:63]\n"
        "ds_read_b128 v[8:11], v224 offset:10240\n"
        "s_add_u32 s62, s62, 0x80000\n"
        "s_addc_u32 s63, s63, 0\n"
        "v_mfma_f32_16x16x32_bf16 a[28:31], v[4:7], v[92:95], a[28:31]\n"
        "ds_read_b128 v[4:7], v225 offset:8192\n"
        "v_and_b32 v174, 0xffff0000, v96\n"
        "v_mfma_f32_16x16x32_bf16 a[28:31], v[0:3], v[60:63], a[28:31]\n"
        "ds_read_b128 v[0:3], v224 offset:8192\n"
        "v_and_b32 v175, 0xffff0000, v98\n"
        "v_mfma_f32_16x16x32_bf16 a[64:67], v[32:35], v[16:19], a[64:67]\n"
        "ds_read_b128 v[12:15], v225 offset:10240\n"
        "s_waitcnt vmcnt(40)\n"
        "v_mfma_f32_16x16x32_bf16 a[64:67], v[64:67], v[20:23], a[64:67]\n"
        "v_cndmask_b32 v100, v100, 0, s[78:79]\n"
        "v_cndmask_b32 v101, v101, 0, s[78:79]\n"
        "v_mfma_f32_16x16x32_bf16 a[96:99], v[64:67], v[28:31], a[96:99]\n"
        "v_lshlrev_b32 v176, 16, v100\n"
        "v_mul_f32 v192, v176, v187\n"
        "v_mfma_f32_16x16x32_bf16 a[96:99], v[32:35], v[24:27], a[96:99]\n"
        "v_cvt_pk_bf16_f32 v192, 0, v192\n"
        "v_add_f32 v191, v191, v192\n"
        "v_mfma_f32_16x16x32_bf16 a[100:103], v[36:39], v[24:27], a[100:103]\n"
        "v_cvt_pk_bf16_f32 v191, 0, v191\n"
        "v_mul_f32 v190, v176, v186\n"
        "v_mfma_f32_16x16x32_bf16 a[100:103], v[68:71], v[28:31], a[100:103]\n"
        "v_cvt_pk_bf16_f32 v190, 0, v190\n"
        "v_add_f32 v189, v189, v190\n"
        "v_mfma_f32_16x16x32_bf16 a[68:71], v[68:71], v[20:23], a[68:71]\n"
        "v_cvt_pk_bf16_f32 v189, 0, v189\n"
        "v_mul_f32 v193, v176, v188\n"
        "v_mfma_f32_16x16x32_bf16 a[68:71], v[36:39], v[16:19], a[68:71]\n"
        "global_load_dword v253, v173, s[82:83] offset:0\n"
        "s_add_u32 s82, s12, 0x100000\n"
        "s_addc_u32 s83, s13, 0\n"
        "v_mfma_f32_16x16x32_bf16 a[72:75], v[40:43], v[16:19], a[72:75]\n"
        "s_waitcnt vmcnt(38) lgkmcnt(0)\n"
        "v_lshlrev_b32 v177, 16, v102\n"
        "v_mul_f32 v192, v177, v186\n"
        "v_mfma_f32_16x16x32_bf16 a[72:75], v[72:75], v[20:23], a[72:75]\n"
        "v_cvt_pk_bf16_f32 v192, 0, v192\n"
        "v_add_f32 v191, v191, v192\n"
        "v_mfma_f32_16x16x32_bf16 a[104:107], v[72:75], v[28:31], a[104:107]\n"
        "v_cvt_pk_bf16_f32 v191, 0, v191\n"
        "v_mul_f32 v190, v177, v185\n"
        "v_mfma_f32_16x16x32_bf16 a[104:107], v[40:43], v[24:27], a[104:107]\n"
        "v_cvt_pk_bf16_f32 v190, 0, v190\n"
        "v_add_f32 v189, v189, v190\n"
        "v_mfma_f32_16x16x32_bf16 a[108:111], v[44:47], v[24:27], a[108:111]\n"
        "v_cvt_pk_bf16_f32 v189, 0, v189\n"
        "v_mul_f32 v195, v177, v188\n"
        "v_mfma_f32_16x16x32_bf16 a[108:111], v[76:79], v[28:31], a[108:111]\n"
        "v_cvt_pk_bf16_f32 v193, 0, v193\n"
        "v_cvt_pk_bf16_f32 v195, 0, v195\n"
        "v_mfma_f32_16x16x32_bf16 a[76:79], v[76:79], v[20:23], a[76:79]\n"
        "v_mul_f32 v194, v177, v187\n"
        "v_cvt_pk_bf16_f32 v194, 0, v194\n"
        "v_mfma_f32_16x16x32_bf16 a[76:79], v[44:47], v[16:19], a[76:79]\n"
        "v_add_f32 v193, v193, v194\n"
        "v_cvt_pk_bf16_f32 v193, 0, v193\n"
        "v_mfma_f32_16x16x32_bf16 a[80:83], v[16:19], v[48:51], a[80:83]\n"
        "s_waitcnt vmcnt(35)\n"
        "v_lshlrev_b32 v178, 16, v104\n"
        "v_mul_f32 v192, v178, v185\n"
        "v_mfma_f32_16x16x32_bf16 a[80:83], v[20:23], v[80:83], a[80:83]\n"
        "v_cvt_pk_bf16_f32 v192, 0, v192\n"
        "v_add_f32 v191, v191, v192\n"
        "v_mfma_f32_16x16x32_bf16 a[112:115], v[28:31], v[80:83], a[112:115]\n"
        "v_cvt_pk_bf16_f32 v191, 0, v191\n"
        "v_mul_f32 v196, v178, v187\n"
        "v_mfma_f32_16x16x32_bf16 a[112:115], v[24:27], v[48:51], a[112:115]\n"
        "v_cvt_pk_bf16_f32 v196, 0, v196\n"
        "v_add_f32 v195, v195, v196\n"
        "v_mfma_f32_16x16x32_bf16 a[116:119], v[24:27], v[52:55], a[116:119]\n"
        "v_cvt_pk_bf16_f32 v195, 0, v195\n"
        "v_mul_f32 v194, v178, v186\n"
        "v_mfma_f32_16x16x32_bf16 a[116:119], v[28:31], v[84:87], a[116:119]\n"
        "v_cvt_pk_bf16_f32 v194, 0, v194\n"
        "v_add_f32 v193, v193, v194\n"
        "v_mfma_f32_16x16x32_bf16 a[84:87], v[20:23], v[84:87], a[84:87]\n"
        "v_cvt_pk_bf16_f32 v193, 0, v193\n"
        "v_and_b32 v176, 0xffff0000, v100\n"
        "v_mfma_f32_16x16x32_bf16 a[84:87], v[16:19], v[52:55], a[84:87]\n"
        "v_and_b32 v177, 0xffff0000, v102\n"
        "v_mfma_f32_16x16x32_bf16 a[88:91], v[16:19], v[56:59], a[88:91]\n"
        "v_mfma_f32_16x16x32_bf16 a[88:91], v[20:23], v[88:91], a[88:91]\n"
        "s_waitcnt vmcnt(32)\n"
        "v_lshlrev_b32 v179, 16, v106\n"
        "v_mul_f32 v196, v179, v186\n"
        "v_mfma_f32_16x16x32_bf16 a[120:123], v[28:31], v[88:91], a[120:123]\n"
        "v_cvt_pk_bf16_f32 v196, 0, v196\n"
        "v_add_f32 v195, v195, v196\n"
        "v_mfma_f32_16x16x32_bf16 a[120:123], v[24:27], v[56:59], a[120:123]\n"
        "v_cvt_pk_bf16_f32 v195, 0, v195\n"
        "v_mul_f32 v194, v179, v185\n"
        "v_mfma_f32_16x16x32_bf16 a[124:127], v[24:27], v[60:63], a[124:127]\n"
        "ds_read_b128 v[24:27], v224 offset:14336\n"
        "v_cvt_pk_bf16_f32 v194, 0, v194\n"
        "v_mfma_f32_16x16x32_bf16 a[124:127], v[28:31], v[92:95], a[124:127]\n"
        "ds_read_b128 v[28:31], v225 offset:14336\n"
        "v_add_f32 v193, v193, v194\n"
        "v_mfma_f32_16x16x32_bf16 a[92:95], v[20:23], v[92:95], a[92:95]\n"
        "ds_read_b128 v[20:23], v225 offset:12288\n"
        "v_cvt_pk_bf16_f32 v193, 0, v193\n"
        "v_mfma_f32_16x16x32_bf16 a[92:95], v[16:19], v[60:63], a[92:95]\n"
        "ds_read_b128 v[16:19], v224 offset:12288\n"
        "v_mfma_f32_16x16x32_bf16 a[128:131], v[32:35], v[0:3], a[128:131]\n"
        "v_mfma_f32_16x16x32_bf16 a[128:131], v[64:67], v[4:7], a[128:131]\n"
        "v_mfma_f32_16x16x32_bf16 a[160:163], v[64:67], v[12:15], a[160:163]\n"
        "s_waitcnt vmcnt(29)\n"
        "v_lshlrev_b32 v180, 16, v108\n"
        "v_mul_f32 v196, v180, v185\n"
        "v_mfma_f32_16x16x32_bf16 a[160:163], v[32:35], v[8:11], a[160:163]\n"
        "s_waitcnt vmcnt(28)\n"
        "v_lshlrev_b32 v181, 16, v110\n"
        "v_cvt_pk_bf16_f32 v196, 0, v196\n"
        "v_mfma_f32_16x16x32_bf16 a[164:167], v[36:39], v[8:11], a[164:167]\n"
        "s_waitcnt vmcnt(27) lgkmcnt(3)\n"
        "v_lshlrev_b32 v182, 16, v112\n"
        "v_add_f32 v195, v195, v196\n"
        "v_mfma_f32_16x16x32_bf16 a[164:167], v[68:71], v[12:15], a[164:167]\n"
        "v_cvt_pk_bf16_f32 v195, 0, v195\n"
        "v_mfma_f32_16x16x32_bf16 a[132:135], v[68:71], v[4:7], a[132:135]\n"
        "v_mfma_f32_16x16x32_bf16 a[132:135], v[36:39], v[0:3], a[132:135]\n"
        "v_mfma_f32_16x16x32_bf16 a[136:139], v[40:43], v[0:3], a[136:139]\n"
        "v_mfma_f32_16x16x32_bf16 a[136:139], v[72:75], v[4:7], a[136:139]\n"
        "v_mfma_f32_16x16x32_bf16 a[168:171], v[72:75], v[12:15], a[168:171]\n"
        "v_mfma_f32_16x16x32_bf16 a[168:171], v[40:43], v[8:11], a[168:171]\n"
        "s_waitcnt vmcnt(26) lgkmcnt(0)\n"
        "v_lshlrev_b32 v183, 16, v114\n"
        "v_mfma_f32_16x16x32_bf16 a[172:175], v[44:47], v[8:11], a[172:175]\n"
        "v_mfma_f32_16x16x32_bf16 a[172:175], v[76:79], v[12:15], a[172:175]\n"
        "v_mfma_f32_16x16x32_bf16 a[140:143], v[76:79], v[4:7], a[140:143]\n"
        "v_mfma_f32_16x16x32_bf16 a[140:143], v[44:47], v[0:3], a[140:143]\n"
        "v_mfma_f32_16x16x32_bf16 a[144:147], v[0:3], v[48:51], a[144:147]\n"
        "v_mfma_f32_16x16x32_bf16 a[144:147], v[4:7], v[80:83], a[144:147]\n"
        "v_mfma_f32_16x16x32_bf16 a[176:179], v[12:15], v[80:83], a[176:179]\n"
        "v_mfma_f32_16x16x32_bf16 a[176:179], v[8:11], v[48:51], a[176:179]\n"
        "s_waitcnt vmcnt(25)\n"
        "v_lshlrev_b32 v184, 16, v116\n"
        "v_mfma_f32_16x16x32_bf16 a[180:183], v[8:11], v[52:55], a[180:183]\n"
        "v_mfma_f32_16x16x32_bf16 a[180:183], v[12:15], v[84:87], a[180:183]\n"
        "v_mfma_f32_16x16x32_bf16 a[148:151], v[4:7], v[84:87], a[148:151]\n"
        "v_mfma_f32_16x16x32_bf16 a[148:151], v[0:3], v[52:55], a[148:151]\n"
        "v_mfma_f32_16x16x32_bf16 a[152:155], v[0:3], v[56:59], a[152:155]\n"
        "v_mfma_f32_16x16x32_bf16 a[152:155], v[4:7], v[88:91], a[152:155]\n"
        "v_mfma_f32_16x16x32_bf16 a[184:187], v[12:15], v[88:91], a[184:187]\n"
        "v_mfma_f32_16x16x32_bf16 a[184:187], v[8:11], v[56:59], a[184:187]\n"
        "s_waitcnt vmcnt(10)\n"
        "s_barrier\n"
        "v_lshlrev_b32 v190, 16, v118\n"
        "v_mfma_f32_16x16x32_bf16 a[188:191], v[8:11], v[60:63], a[188:191]\n"
        "global_load_lds_dwordx4 v234, s[56:57] offset:0\n"
        "ds_read_b128 v[8:11], v224 offset:34816\n"
        "v_mfma_f32_16x16x32_bf16 a[188:191], v[12:15], v[92:95], a[188:191]\n"
        "ds_read_b128 v[12:15], v225 offset:34816\n"
        "v_mul_f32 v189, v189, v190\n"
        "v_mfma_f32_16x16x32_bf16 a[156:159], v[4:7], v[92:95], a[156:159]\n"
        "ds_read_b128 v[4:7], v225 offset:32768\n"
        "v_mul_f32 v190, v179, v187\n"
        "v_mfma_f32_16x16x32_bf16 a[156:159], v[0:3], v[60:63], a[156:159]\n"
        "ds_read_b128 v[0:3], v224 offset:32768\n"
        "v_cvt_pk_bf16_f32 v190, 0, v190\n"
        "v_mfma_f32_16x16x32_bf16 a[192:195], v[32:35], v[16:19], a[192:195]\n"
        "global_load_lds_dwordx4 v235, s[56:57] offset:1024\n"
        "v_mfma_f32_16x16x32_bf16 a[192:195], v[64:67], v[20:23], a[192:195]\n"
        "v_mfma_f32_16x16x32_bf16 a[224:227], v[64:67], v[28:31], a[224:227]\n"
        "ds_read_b64_tr_b16 v[64:65], v226 offset:8192\n"
        "v_mfma_f32_16x16x32_bf16 a[224:227], v[32:35], v[24:27], a[224:227]\n"
        "ds_read_b64_tr_b16 v[32:33], v226 offset:0\n"
        "v_lshlrev_b32 v192, 16, v120\n"
        "v_mfma_f32_16x16x32_bf16 a[228:231], v[36:39], v[24:27], a[228:231]\n"
        "ds_read_b64_tr_b16 v[34:35], v227 offset:0\n"
        "global_load_lds_dwordx4 v236, s[56:57] offset:2048\n"
        "v_mfma_f32_16x16x32_bf16 a[228:231], v[68:71], v[28:31], a[228:231]\n"
        "ds_read_b64_tr_b16 v[66:67], v227 offset:8192\n"
        "v_mul_f32 v191, v191, v192\n"
        "v_mfma_f32_16x16x32_bf16 a[196:199], v[68:71], v[20:23], a[196:199]\n"
        "ds_read_b64_tr_b16 v[68:69], v228 offset:8192\n"
        "v_cvt_pk_bf16_f32 v203, v191, v189\n"
        "v_mfma_f32_16x16x32_bf16 a[196:199], v[36:39], v[16:19], a[196:199]\n"
        "ds_read_b64_tr_b16 v[36:37], v228 offset:0\n"
        "v_mul_f32 v189, v178, v188\n"
        "v_mfma_f32_16x16x32_bf16 a[200:203], v[40:43], v[16:19], a[200:203]\n"
        "ds_read_b64_tr_b16 v[38:39], v229 offset:0\n"
        "global_load_lds_dwordx4 v237, s[56:57] offset:3072\n"
        "s_add_u32 m0, s72, 0x1000\n"
        "v_mfma_f32_16x16x32_bf16 a[200:203], v[72:75], v[20:23], a[200:203]\n"
        "ds_read_b64_tr_b16 v[70:71], v229 offset:8192\n"
        "s_add_u32 s56, s56, 0x80\n"
        "s_addc_u32 s57, s57, 0\n"
        "v_mfma_f32_16x16x32_bf16 a[232:235], v[72:75], v[28:31], a[232:235]\n"
        "ds_read_b64_tr_b16 v[72:73], v230 offset:8192\n"
        "v_mul_f32 v191, v179, v188\n"
        "v_mfma_f32_16x16x32_bf16 a[232:235], v[40:43], v[24:27], a[232:235]\n"
        "ds_read_b64_tr_b16 v[40:41], v230 offset:0\n"
        "v_lshlrev_b32 v194, 16, v122\n"
        "v_mfma_f32_16x16x32_bf16 a[236:239], v[44:47], v[24:27], a[236:239]\n"
        "ds_read_b64_tr_b16 v[42:43], v231 offset:0\n"
        "global_load_lds_dwordx4 v234, s[58:59] offset:0\n"
        "v_mfma_f32_16x16x32_bf16 a[236:239], v[76:79], v[28:31], a[236:239]\n"
        "ds_read_b64_tr_b16 v[74:75], v231 offset:8192\n"
        "v_mul_f32 v193, v193, v194\n"
        "v_mfma_f32_16x16x32_bf16 a[204:207], v[76:79], v[20:23], a[204:207]\n"
        "ds_read_b64_tr_b16 v[76:77], v232 offset:8192\n"
        "v_cvt_pk_bf16_f32 v189, 0, v189\n"
        "v_mfma_f32_16x16x32_bf16 a[204:207], v[44:47], v[16:19], a[204:207]\n"
        "ds_read_b64_tr_b16 v[44:45], v232 offset:0\n"
        "v_cvt_pk_bf16_f32 v191, 0, v191\n"
        "v_mfma_f32_16x16x32_bf16 a[208:211], v[16:19], v[48:51], a[208:211]\n"
        "ds_read_b64_tr_b16 v[46:47], v233 offset:0\n"
        "global_load_lds_dwordx4 v235, s[58:59] offset:1024\n"
        "v_mfma_f32_16x16x32_bf16 a[208:211], v[20:23], v[80:83], a[208:211]\n"
        "ds_read_b64_tr_b16 v[78:79], v233 offset:8192\n"
        "v_mul_f32 v192, v180, v187\n"
        "v_mfma_f32_16x16x32_bf16 a[240:243], v[28:31], v[80:83], a[240:243]\n"
        "ds_read_b64_tr_b16 v[80:81], v226 offset:24576\n"
        "v_cvt_pk_bf16_f32 v192, 0, v192\n"
        "v_mfma_f32_16x16x32_bf16 a[240:243], v[24:27], v[48:51], a[240:243]\n"
        "ds_read_b64_tr_b16 v[48:49], v226 offset:16384\n"
        "v_lshlrev_b32 v196, 16, v124\n"
        "v_mfma_f32_16x16x32_bf16 a[244:247], v[24:27], v[52:55], a[244:247]\n"
        "ds_read_b64_tr_b16 v[50:51], v227 offset:16384\n"
        "global_load_lds_dwordx4 v236, s[58:59] offset:2048\n"
        "v_mfma_f32_16x16x32_bf16 a[244:247], v[28:31], v[84:87], a[244:247]\n"
        "ds_read_b64_tr_b16 v[82:83], v227 offset:24576\n"
        "v_mul_f32 v195, v195, v196\n"
        "v_mfma_f32_16x16x32_bf16 a[212:215], v[20:23], v[84:87], a[212:215]\n"
        "ds_read_b64_tr_b16 v[84:85], v228 offset:24576\n"
        "v_cvt_pk_bf16_f32 v202, v195, v193\n"
        "v_mfma_f32_16x16x32_bf16 a[212:215], v[16:19], v[52:55], a[212:215]\n"
        "ds_read_b64_tr_b16 v[52:53], v228 offset:16384\n"
        "v_add_f32 v189, v189, v190\n"
        "v_mfma_f32_16x16x32_bf16 a[216:219], v[16:19], v[56:59], a[216:219]\n"
        "ds_read_b64_tr_b16 v[54:55], v229 offset:16384\n"
        "global_load_dword v253, v173, s[82:83] offset:0\n"
        "v_mfma_f32_16x16x32_bf16 a[216:219], v[20:23], v[88:91], a[216:219]\n"
        "ds_read_b64_tr_b16 v[86:87], v229 offset:24576\n"
        "global_load_lds_dwordx4 v237, s[58:59] offset:3072\n"
        "s_add_u32 m0, s73, 0x20000\n"
        "v_mfma_f32_16x16x32_bf16 a[248:251], v[28:31], v[88:91], a[248:251]\n"
        "ds_read_b64_tr_b16 v[88:89], v230 offset:24576\n"
        "s_add_u32 s58, s58, 0x80\n"
        "s_addc_u32 s59, s59, 0\n"
        "v_mfma_f32_16x16x32_bf16 a[248:251], v[24:27], v[56:59], a[248:251]\n"
        "ds_read_b64_tr_b16 v[56:57], v230 offset:16384\n"
        "s_add_u32 s82, s12, 0x200000\n"
        "s_addc_u32 s83, s13, 0\n"
        "v_mfma_f32_16x16x32_bf16 a[252:255], v[24:27], v[60:63], a[252:255]\n"
        "ds_read_b64_tr_b16 v[58:59], v231 offset:16384\n"
        "v_add_f32 v191, v191, v192\n"
        "v_mfma_f32_16x16x32_bf16 a[252:255], v[28:31], v[92:95], a[252:255]\n"
        "ds_read_b64_tr_b16 v[90:91], v231 offset:24576\n"
        "global_load_lds_dwordx4 v238, s[60:61] offset:0\n"
        "v_mfma_f32_16x16x32_bf16 a[220:223], v[20:23], v[92:95], a[220:223]\n"
        "ds_read_b64_tr_b16 v[92:93], v232 offset:24576\n"
        "v_cvt_pk_bf16_f32 v189, 0, v189\n"
        "v_mfma_f32_16x16x32_bf16 a[220:223], v[16:19], v[60:63], a[220:223]\n"
        "ds_read_b64_tr_b16 v[60:61], v232 offset:16384\n"
        "s_waitcnt lgkmcnt(8)\n"
        "v_mfma_f32_16x16x32_bf16 a[0:3], v[32:35], v[0:3], a[0:3]\n"
        "ds_read_b64_tr_b16 v[62:63], v233 offset:16384\n"
        "v_cvt_pk_bf16_f32 v191, 0, v191\n"
        "v_mfma_f32_16x16x32_bf16 a[0:3], v[64:67], v[4:7], a[0:3]\n"
        "ds_read_b64_tr_b16 v[94:95], v233 offset:24576\n"
        "global_load_lds_dwordx4 v239, s[60:61] offset:1024\n"
        "v_mfma_f32_16x16x32_bf16 a[32:35], v[64:67], v[12:15], a[32:35]\n"
        "ds_read_b128 v[16:19], v224 offset:36864\n"
        "v_mul_f32 v190, v180, v186\n"
        "v_mfma_f32_16x16x32_bf16 a[32:35], v[32:35], v[8:11], a[32:35]\n"
        "ds_read_b128 v[20:23], v225 offset:36864\n"
        "v_mul_f32 v192, v181, v186\n"
        "v_mfma_f32_16x16x32_bf16 a[36:39], v[36:39], v[8:11], a[36:39]\n"
        "ds_read_b128 v[24:27], v224 offset:38912\n"
        "v_cvt_pk_bf16_f32 v190, 0, v190\n"
        "v_mfma_f32_16x16x32_bf16 a[36:39], v[68:71], v[12:15], a[36:39]\n"
        "ds_read_b128 v[28:31], v225 offset:38912\n"
        "global_load_lds_dwordx4 v240, s[60:61] offset:2048\n"
        "v_mfma_f32_16x16x32_bf16 a[4:7], v[68:71], v[4:7], a[4:7]\n"
        "v_cvt_pk_bf16_f32 v192, 0, v192\n"
        "v_add_f32 v189, v189, v190\n"
        "v_mfma_f32_16x16x32_bf16 a[4:7], v[36:39], v[0:3], a[4:7]\n"
        "v_add_f32 v191, v191, v192\n"
        "v_cvt_pk_bf16_f32 v189, 0, v189\n"
        "v_mfma_f32_16x16x32_bf16 a[8:11], v[40:43], v[0:3], a[8:11]\n"
        "v_cvt_pk_bf16_f32 v191, 0, v191\n"
        "v_mul_f32 v190, v181, v185\n"
        "v_mfma_f32_16x16x32_bf16 a[8:11], v[72:75], v[4:7], a[8:11]\n"
        "global_load_lds_dwordx4 v241, s[60:61] offset:3072\n"
        "s_add_u32 m0, s73, 0x24000\n"
        "v_mfma_f32_16x16x32_bf16 a[40:43], v[72:75], v[12:15], a[40:43]\n"
        "s_add_u32 s60, s60, 0x80000\n"
        "s_addc_u32 s61, s61, 0\n"
        "v_mul_f32 v192, v182, v185\n"
        "v_mfma_f32_16x16x32_bf16 a[40:43], v[40:43], v[8:11], a[40:43]\n"
        "s_waitcnt lgkmcnt(2)\n"
        "v_cvt_pk_bf16_f32 v190, 0, v190\n"
        "v_cvt_pk_bf16_f32 v192, 0, v192\n"
        "v_mfma_f32_16x16x32_bf16 a[44:47], v[44:47], v[8:11], a[44:47]\n"
        "v_add_f32 v189, v189, v190\n"
        "v_add_f32 v191, v191, v192\n"
        "v_mfma_f32_16x16x32_bf16 a[44:47], v[76:79], v[12:15], a[44:47]\n"
        "global_load_lds_dwordx4 v238, s[62:63] offset:0\n"
        "v_mfma_f32_16x16x32_bf16 a[12:15], v[76:79], v[4:7], a[12:15]\n"
        "v_cvt_pk_bf16_f32 v189, 0, v189\n"
        "v_cvt_pk_bf16_f32 v191, 0, v191\n"
        "v_mfma_f32_16x16x32_bf16 a[12:15], v[44:47], v[0:3], a[12:15]\n"
        "v_lshlrev_b32 v190, 16, v126\n"
        "v_lshlrev_b32 v192, 16, v128\n"
        "v_mfma_f32_16x16x32_bf16 a[16:19], v[0:3], v[48:51], a[16:19]\n"
        "v_mul_f32 v189, v189, v190\n"
        "v_mul_f32 v191, v191, v192\n"
        "v_mfma_f32_16x16x32_bf16 a[16:19], v[4:7], v[80:83], a[16:19]\n"
        "global_load_lds_dwordx4 v239, s[62:63] offset:1024\n"
        "v_mfma_f32_16x16x32_bf16 a[48:51], v[12:15], v[80:83], a[48:51]\n"
        "v_cvt_pk_bf16_f32 v201, v191, v189\n"
        "v_mul_f32 v193, v180, v188\n"
        "v_mfma_f32_16x16x32_bf16 a[48:51], v[8:11], v[48:51], a[48:51]\n"
        "v_mul_f32 v195, v181, v188\n"
        "v_cvt_pk_bf16_f32 v193, 0, v193\n"
        "v_mfma_f32_16x16x32_bf16 a[52:55], v[8:11], v[52:55], a[52:55]\n"
        "v_cvt_pk_bf16_f32 v195, 0, v195\n"
        "v_mul_f32 v194, v181, v187\n"
        "v_mfma_f32_16x16x32_bf16 a[52:55], v[12:15], v[84:87], a[52:55]\n"
        "global_load_lds_dwordx4 v240, s[62:63] offset:2048\n"
        "v_mfma_f32_16x16x32_bf16 a[20:23], v[4:7], v[84:87], a[20:23]\n"
        "v_mul_f32 v196, v182, v187\n"
        "v_cvt_pk_bf16_f32 v194, 0, v194\n"
        "v_mfma_f32_16x16x32_bf16 a[20:23], v[0:3], v[52:55], a[20:23]\n"
        "s_waitcnt lgkmcnt(0)\n"
        "v_cvt_pk_bf16_f32 v196, 0, v196\n"
        "v_add_f32 v193, v193, v194\n"
        "v_mfma_f32_16x16x32_bf16 a[24:27], v[0:3], v[56:59], a[24:27]\n"
        "v_add_f32 v195, v195, v196\n"
        "v_cvt_pk_bf16_f32 v193, 0, v193\n"
        "v_mfma_f32_16x16x32_bf16 a[24:27], v[4:7], v[88:91], a[24:27]\n"
        "global_load_lds_dwordx4 v241, s[62:63] offset:3072\n"
        "s_add_u32 m0, s72, 0x8000\n"
        "v_mfma_f32_16x16x32_bf16 a[56:59], v[12:15], v[88:91], a[56:59]\n"
        "s_add_u32 s62, s62, 0x80000\n"
        "s_addc_u32 s63, s63, 0\n"
        "v_cvt_pk_bf16_f32 v195, 0, v195\n"
        "v_mfma_f32_16x16x32_bf16 a[56:59], v[8:11], v[56:59], a[56:59]\n"
        "v_mul_f32 v194, v182, v186\n"
        "v_mul_f32 v196, v183, v186\n"
        "v_mfma_f32_16x16x32_bf16 a[60:63], v[8:11], v[60:63], a[60:63]\n"
        "ds_read_b128 v[8:11], v224 offset:43008\n"
        "v_cvt_pk_bf16_f32 v194, 0, v194\n"
        "v_mfma_f32_16x16x32_bf16 a[60:63], v[12:15], v[92:95], a[60:63]\n"
        "ds_read_b128 v[12:15], v225 offset:43008\n"
        "global_load_dwordx4 v[142:145], v254, s[92:93] offset:0\n"
        "v_mfma_f32_16x16x32_bf16 a[28:31], v[4:7], v[92:95], a[28:31]\n"
        "ds_read_b128 v[4:7], v225 offset:40960\n"
        "v_cvt_pk_bf16_f32 v196, 0, v196\n"
        "v_mfma_f32_16x16x32_bf16 a[28:31], v[0:3], v[60:63], a[28:31]\n"
        "ds_read_b128 v[0:3], v224 offset:40960\n"
        "v_add_f32 v193, v193, v194\n"
        "v_mfma_f32_16x16x32_bf16 a[64:67], v[32:35], v[16:19], a[64:67]\n"
        "v_add_f32 v195, v195, v196\n"
        "v_cvt_pk_bf16_f32 v193, 0, v193\n"
        "v_mfma_f32_16x16x32_bf16 a[64:67], v[64:67], v[20:23], a[64:67]\n"
        "v_cvt_pk_bf16_f32 v195, 0, v195\n"
        "v_mul_f32 v194, v183, v185\n"
        "v_mfma_f32_16x16x32_bf16 a[96:99], v[64:67], v[28:31], a[96:99]\n"
        "v_mul_f32 v196, v184, v185\n"
        "v_cvt_pk_bf16_f32 v194, 0, v194\n"
        "v_mfma_f32_16x16x32_bf16 a[96:99], v[32:35], v[24:27], a[96:99]\n"
        "v_cvt_pk_bf16_f32 v196, 0, v196\n"
        "v_add_f32 v193, v193, v194\n"
        "v_mfma_f32_16x16x32_bf16 a[100:103], v[36:39], v[24:27], a[100:103]\n"
        "v_add_f32 v195, v195, v196\n"
        "v_cvt_pk_bf16_f32 v193, 0, v193\n"
        "v_mfma_f32_16x16x32_bf16 a[100:103], v[68:71], v[28:31], a[100:103]\n"
        "s_waitcnt vmcnt(27) lgkmcnt(2)\n"
        "v_cvt_pk_bf16_f32 v195, 0, v195\n"
        "v_lshlrev_b32 v194, 16, v130\n"
        "v_mfma_f32_16x16x32_bf16 a[68:71], v[68:71], v[20:23], a[68:71]\n"
        "v_lshlrev_b32 v196, 16, v132\n"
        "v_mul_f32 v193, v193, v194\n"
        "v_mfma_f32_16x16x32_bf16 a[68:71], v[36:39], v[16:19], a[68:71]\n"
        "v_mul_f32 v195, v195, v196\n"
        "v_cvt_pk_bf16_f32 v200, v195, v193\n"
        "v_mfma_f32_16x16x32_bf16 a[72:75], v[40:43], v[16:19], a[72:75]\n"
        "global_store_dwordx4 v171, v[200:203], s[76:77] offset:-4096 sc1\n"
        "v_lshlrev_b32 v185, 16, v136\n"
        "v_and_b32 v186, 0xffff0000, v136\n"
        "v_lshlrev_b32 v187, 16, v137\n"
        "v_and_b32 v188, 0xffff0000, v137\n"
        "v_mfma_f32_16x16x32_bf16 a[72:75], v[72:75], v[20:23], a[72:75]\n"
        "v_and_b32 v178, 0xffff0000, v104\n"
        "v_and_b32 v179, 0xffff0000, v106\n"
        "v_mfma_f32_16x16x32_bf16 a[104:107], v[72:75], v[28:31], a[104:107]\n"
        "v_and_b32 v180, 0xffff0000, v108\n"
        "v_and_b32 v181, 0xffff0000, v110\n"
        "v_mfma_f32_16x16x32_bf16 a[104:107], v[40:43], v[24:27], a[104:107]\n"
        "v_and_b32 v182, 0xffff0000, v112\n"
        "v_and_b32 v183, 0xffff0000, v114\n"
        "v_mfma_f32_16x16x32_bf16 a[108:111], v[44:47], v[24:27], a[108:111]\n"
        "v_and_b32 v184, 0xffff0000, v116\n"
        "v_mul_f32 v189, v174, v188\n"
        "v_mfma_f32_16x16x32_bf16 a[108:111], v[76:79], v[28:31], a[108:111]\n"
        "v_mul_f32 v191, v175, v188\n"
        "v_cvt_pk_bf16_f32 v189, 0, v189\n"
        "v_mfma_f32_16x16x32_bf16 a[76:79], v[76:79], v[20:23], a[76:79]\n"
        "v_cvt_pk_bf16_f32 v191, 0, v191\n"
        "v_mul_f32 v190, v175, v187\n"
        "v_mfma_f32_16x16x32_bf16 a[76:79], v[44:47], v[16:19], a[76:79]\n"
        "global_load_dwordx4 v[146:149], v254, s[92:93] offset:1024\n"
        "s_add_u32 s92, s8, s91\n"
        "s_addc_u32 s93, s9, 0\n"
        "v_mfma_f32_16x16x32_bf16 a[80:83], v[16:19], v[48:51], a[80:83]\n"
        "v_mul_f32 v192, v176, v187\n"
        "v_cvt_pk_bf16_f32 v190, 0, v190\n"
        "v_mfma_f32_16x16x32_bf16 a[80:83], v[20:23], v[80:83], a[80:83]\n"
        "v_cvt_pk_bf16_f32 v192, 0, v192\n"
        "v_add_f32 v189, v189, v190\n"
        "v_mfma_f32_16x16x32_bf16 a[112:115], v[28:31], v[80:83], a[112:115]\n"
        "v_add_f32 v191, v191, v192\n"
        "v_cvt_pk_bf16_f32 v189, 0, v189\n"
        "v_mfma_f32_16x16x32_bf16 a[112:115], v[24:27], v[48:51], a[112:115]\n"
        "v_cvt_pk_bf16_f32 v191, 0, v191\n"
        "v_mul_f32 v190, v176, v186\n"
        "v_mfma_f32_16x16x32_bf16 a[116:119], v[24:27], v[52:55], a[116:119]\n"
        "v_mul_f32 v192, v177, v186\n"
        "v_cvt_pk_bf16_f32 v190, 0, v190\n"
        "v_mfma_f32_16x16x32_bf16 a[116:119], v[28:31], v[84:87], a[116:119]\n"
        "s_waitcnt lgkmcnt(0)\n"
        "v_cvt_pk_bf16_f32 v192, 0, v192\n"
        "v_add_f32 v189, v189, v190\n"
        "v_mfma_f32_16x16x32_bf16 a[84:87], v[20:23], v[84:87], a[84:87]\n"
        "v_add_f32 v191, v191, v192\n"
        "v_cvt_pk_bf16_f32 v189, 0, v189\n"
        "v_mfma_f32_16x16x32_bf16 a[84:87], v[16:19], v[52:55], a[84:87]\n"
        "v_cvt_pk_bf16_f32 v191, 0, v191\n"
        "v_mul_f32 v190, v177, v185\n"
        "v_mfma_f32_16x16x32_bf16 a[88:91], v[16:19], v[56:59], a[88:91]\n"
        "v_mul_f32 v192, v178, v185\n"
        "v_cvt_pk_bf16_f32 v190, 0, v190\n"
        "v_mfma_f32_16x16x32_bf16 a[88:91], v[20:23], v[88:91], a[88:91]\n"
        "v_cvt_pk_bf16_f32 v192, 0, v192\n"
        "v_add_f32 v189, v189, v190\n"
        "v_mfma_f32_16x16x32_bf16 a[120:123], v[28:31], v[88:91], a[120:123]\n"
        "v_add_f32 v191, v191, v192\n"
        "v_cvt_pk_bf16_f32 v189, 0, v189\n"
        "v_mfma_f32_16x16x32_bf16 a[120:123], v[24:27], v[56:59], a[120:123]\n"
        "v_cvt_pk_bf16_f32 v191, 0, v191\n"
        "v_and_b32 v190, 0xffff0000, v118\n"
        "v_mfma_f32_16x16x32_bf16 a[124:127], v[24:27], v[60:63], a[124:127]\n"
        "ds_read_b128 v[24:27], v224 offset:47104\n"
        "v_and_b32 v192, 0xffff0000, v120\n"
        "v_mfma_f32_16x16x32_bf16 a[124:127], v[28:31], v[92:95], a[124:127]\n"
        "ds_read_b128 v[28:31], v225 offset:47104\n"
        "v_mul_f32 v189, v189, v190\n"
        "v_mfma_f32_16x16x32_bf16 a[92:95], v[20:23], v[92:95], a[92:95]\n"
        "ds_read_b128 v[20:23], v225 offset:45056\n"
        "v_mul_f32 v191, v191, v192\n"
        "v_mfma_f32_16x16x32_bf16 a[92:95], v[16:19], v[60:63], a[92:95]\n"
        "ds_read_b128 v[16:19], v224 offset:45056\n"
        "v_cvt_pk_bf16_f32 v211, v191, v189\n"
        "v_mfma_f32_16x16x32_bf16 a[128:131], v[32:35], v[0:3], a[128:131]\n"
        "v_mul_f32 v193, v176, v188\n"
        "v_mul_f32 v195, v177, v188\n"
        "v_mfma_f32_16x16x32_bf16 a[128:131], v[64:67], v[4:7], a[128:131]\n"
        "v_cvt_pk_bf16_f32 v193, 0, v193\n"
        "v_cvt_pk_bf16_f32 v195, 0, v195\n"
        "v_mfma_f32_16x16x32_bf16 a[160:163], v[64:67], v[12:15], a[160:163]\n"
        "v_mul_f32 v194, v177, v187\n"
        "v_mul_f32 v196, v178, v187\n"
        "v_mfma_f32_16x16x32_bf16 a[160:163], v[32:35], v[8:11], a[160:163]\n"
        "v_cvt_pk_bf16_f32 v194, 0, v194\n"
        "v_cvt_pk_bf16_f32 v196, 0, v196\n"
        "v_mfma_f32_16x16x32_bf16 a[164:167], v[36:39], v[8:11], a[164:167]\n"
        "v_add_f32 v193, v193, v194\n"
        "v_add_f32 v195, v195, v196\n"
        "v_mfma_f32_16x16x32_bf16 a[164:167], v[68:71], v[12:15], a[164:167]\n"
        "v_cvt_pk_bf16_f32 v193, 0, v193\n"
        "v_cvt_pk_bf16_f32 v195, 0, v195\n"
        "v_mfma_f32_16x16x32_bf16 a[132:135], v[68:71], v[4:7], a[132:135]\n"
        "v_mul_f32 v194, v178, v186\n"
        "v_mul_f32 v196, v179, v186\n"
        "v_mfma_f32_16x16x32_bf16 a[132:135], v[36:39], v[0:3], a[132:135]\n"
        "global_load_dword v253, v173, s[82:83] offset:0\n"
        "s_add_u32 s82, s12, 0x300000\n"
        "s_addc_u32 s83, s13, 0\n"
        "v_mfma_f32_16x16x32_bf16 a[136:139], v[40:43], v[0:3], a[136:139]\n"
        "global_load_dwordx4 v[150:153], v254, s[92:93] offset:0\n"
        "v_cvt_pk_bf16_f32 v194, 0, v194\n"
        "v_mfma_f32_16x16x32_bf16 a[136:139], v[72:75], v[4:7], a[136:139]\n"
        "v_cvt_pk_bf16_f32 v196, 0, v196\n"
        "v_add_f32 v193, v193, v194\n"
        "v_mfma_f32_16x16x32_bf16 a[168:171], v[72:75], v[12:15], a[168:171]\n"
        "v_add_f32 v195, v195, v196\n"
        "v_cvt_pk_bf16_f32 v193, 0, v193\n"
        "v_mfma_f32_16x16x32_bf16 a[168:171], v[40:43], v[8:11], a[168:171]\n"
        "v_cvt_pk_bf16_f32 v195, 0, v195\n"
        "v_mul_f32 v194, v179, v185\n"
        "v_mfma_f32_16x16x32_bf16 a[172:175], v[44:47], v[8:11], a[172:175]\n"
        "v_mul_f32 v196, v180, v185\n"
        "v_cvt_pk_bf16_f32 v194, 0, v194\n"
        "v_mfma_f32_16x16x32_bf16 a[172:175], v[76:79], v[12:15], a[172:175]\n"
        "v_cvt_pk_bf16_f32 v196, 0, v196\n"
        "v_add_f32 v193, v193, v194\n"
        "v_mfma_f32_16x16x32_bf16 a[140:143], v[76:79], v[4:7], a[140:143]\n"
        "v_add_f32 v195, v195, v196\n"
        "v_cvt_pk_bf16_f32 v193, 0, v193\n"
        "v_mfma_f32_16x16x32_bf16 a[140:143], v[44:47], v[0:3], a[140:143]\n"
        "v_cvt_pk_bf16_f32 v195, 0, v195\n"
        "v_and_b32 v194, 0xffff0000, v122\n"
        "v_mfma_f32_16x16x32_bf16 a[144:147], v[0:3], v[48:51], a[144:147]\n"
        "LHA2_%=:\n"
        "s_cbranch_execz LHA6_%=\n"
        "v_and_b32 v196, 0xffff0000, v124\n"
        "v_mul_f32 v193, v193, v194\n"
        "v_mfma_f32_16x16x32_bf16 a[144:147], v[4:7], v[80:83], a[144:147]\n"
        "v_mul_f32 v195, v195, v196\n"
        "v_cvt_pk_bf16_f32 v210, v195, v193\n"
        "v_mfma_f32_16x16x32_bf16 a[176:179], v[12:15], v[80:83], a[176:179]\n"
        "v_mul_f32 v189, v178, v188\n"
        "v_mul_f32 v191, v179, v188\n"
        "v_mfma_f32_16x16x32_bf16 a[176:179], v[8:11], v[48:51], a[176:179]\n"
        "v_cvt_pk_bf16_f32 v189, 0, v189\n"
        "v_cvt_pk_bf16_f32 v191, 0, v191\n"
        "v_mfma_f32_16x16x32_bf16 a[180:183], v[8:11], v[52:55], a[180:183]\n"
        "v_mul_f32 v190, v179, v187\n"
        "v_mul_f32 v192, v180, v187\n"
        "v_mfma_f32_16x16x32_bf16 a[180:183], v[12:15], v[84:87], a[180:183]\n"
        "s_waitcnt lgkmcnt(0)\n"
        "v_cvt_pk_bf16_f32 v190, 0, v190\n"
        "v_cvt_pk_bf16_f32 v192, 0, v192\n"
        "v_mfma_f32_16x16x32_bf16 a[148:151], v[4:7], v[84:87], a[148:151]\n"
        "v_add_f32 v189, v189, v190\n"
        "v_add_f32 v191, v191, v192\n"
        "v_mfma_f32_16x16x32_bf16 a[148:151], v[0:3], v[52:55], a[148:151]\n"
        "v_cvt_pk_bf16_f32 v189, 0, v189\n"
        "v_cvt_pk_bf16_f32 v191, 0, v191\n"
        "v_mfma_f32_16x16x32_bf16 a[152:155], v[0:3], v[56:59], a[152:155]\n"
        "v_mul_f32 v190, v180, v186\n"
        "v_mul_f32 v192, v181, v186\n"
        "v_mfma_f32_16x16x32_bf16 a[152:155], v[4:7], v[88:91], a[152:155]\n"
        "v_cvt_pk_bf16_f32 v190, 0, v190\n"
        "v_cvt_pk_bf16_f32 v192, 0, v192\n"
        "v_mfma_f32_16x16x32_bf16 a[184:187], v[12:15], v[88:91], a[184:187]\n"
        "v_add_f32 v189, v189, v190\n"
        "v_add_f32 v191, v191, v192\n"
        "v_mfma_f32_16x16x32_bf16 a[184:187], v[8:11], v[56:59], a[184:187]\n"
        "s_waitcnt vmcnt(13)\n"
        "s_barrier\n"
        "v_cvt_pk_bf16_f32 v189, 0, v189\n"
        "v_mfma_f32_16x16x32_bf16 a[188:191], v[8:11], v[60:63], a[188:191]\n"
        "global_load_lds_dwordx4 v234, s[56:57] offset:0\n"
        "ds_read_b128 v[8:11], v224 offset:2048\n"
        "v_mfma_f32_16x16x32_bf16 a[188:191], v[12:15], v[92:95], a[188:191]\n"
        "ds_read_b128 v[12:15], v225 offset:2048\n"
        "v_cvt_pk_bf16_f32 v191, 0, v191\n"
        "v_mfma_f32_16x16x32_bf16 a[156:159], v[4:7], v[92:95], a[156:159]\n"
        "ds_read_b128 v[4:7], v225 offset:0\n"
        "v_mul_f32 v190, v181, v185\n"
        "v_mfma_f32_16x16x32_bf16 a[156:159], v[0:3], v[60:63], a[156:159]\n"
        "ds_read_b128 v[0:3], v224 offset:0\n"
        "v_mul_f32 v192, v182, v185\n"
        "v_mfma_f32_16x16x32_bf16 a[192:195], v[32:35], v[16:19], a[192:195]\n"
        "global_load_lds_dwordx4 v235, s[56:57] offset:1024\n"
        "v_cvt_pk_bf16_f32 v190, 0, v190\n"
        "v_mfma_f32_16x16x32_bf16 a[192:195], v[64:67], v[20:23], a[192:195]\n"
        "v_cvt_pk_bf16_f32 v192, 0, v192\n"
        "v_add_f32 v189, v189, v190\n"
        "v_mfma_f32_16x16x32_bf16 a[224:227], v[64:67], v[28:31], a[224:227]\n"
        "ds_read_b64_tr_b16 v[64:65], v226 offset:40960\n"
        "v_add_f32 v191, v191, v192\n"
        "v_mfma_f32_16x16x32_bf16 a[224:227], v[32:35], v[24:27], a[224:227]\n"
        "ds_read_b64_tr_b16 v[32:33], v226 offset:32768\n"
        "v_cvt_pk_bf16_f32 v189, 0, v189\n"
        "v_mfma_f32_16x16x32_bf16 a[228:231], v[36:39], v[24:27], a[228:231]\n"
        "ds_read_b64_tr_b16 v[34:35], v227 offset:32768\n"
        "global_load_dwordx4 v[154:157], v254, s[92:93] offset:1024\n"
        "v_mfma_f32_16x16x32_bf16 a[228:231], v[68:71], v[28:31], a[228:231]\n"
        "ds_read_b64_tr_b16 v[66:67], v227 offset:40960\n"
        "v_cvt_pk_bf16_f32 v191, 0, v191\n"
        "v_mfma_f32_16x16x32_bf16 a[196:199], v[68:71], v[20:23], a[196:199]\n"
        "ds_read_b64_tr_b16 v[68:69], v228 offset:40960\n"
        "v_and_b32 v190, 0xffff0000, v126\n"
        "v_mfma_f32_16x16x32_bf16 a[196:199], v[36:39], v[16:19], a[196:199]\n"
        "ds_read_b64_tr_b16 v[36:37], v228 offset:32768\n"
        "v_and_b32 v192, 0xffff0000, v128\n"
        "v_mfma_f32_16x16x32_bf16 a[200:203], v[40:43], v[16:19], a[200:203]\n"
        "ds_read_b64_tr_b16 v[38:39], v229 offset:32768\n"
        "global_load_lds_dwordx4 v236, s[56:57] offset:2048\n"
        "v_mfma_f32_16x16x32_bf16 a[200:203], v[72:75], v[20:23], a[200:203]\n"
        "ds_read_b64_tr_b16 v[70:71], v229 offset:40960\n"
        "v_mul_f32 v189, v189, v190\n"
        "v_mfma_f32_16x16x32_bf16 a[232:235], v[72:75], v[28:31], a[232:235]\n"
        "ds_read_b64_tr_b16 v[72:73], v230 offset:40960\n"
        "v_mul_f32 v191, v191, v192\n"
        "v_mfma_f32_16x16x32_bf16 a[232:235], v[40:43], v[24:27], a[232:235]\n"
        "ds_read_b64_tr_b16 v[40:41], v230 offset:32768\n"
        "v_cvt_pk_bf16_f32 v209, v191, v189\n"
        "v_mfma_f32_16x16x32_bf16 a[236:239], v[44:47], v[24:27], a[236:239]\n"
        "ds_read_b64_tr_b16 v[42:43], v231 offset:32768\n"
        "global_load_lds_dwordx4 v237, s[56:57] offset:3072\n"
        "s_add_u32 m0, s72, 0x9000\n"
        "v_mfma_f32_16x16x32_bf16 a[236:239], v[76:79], v[28:31], a[236:239]\n"
        "ds_read_b64_tr_b16 v[74:75], v231 offset:40960\n"
        "s_add_u32 s56, s56, 0x80\n"
        "s_addc_u32 s57, s57, 0\n"
        "v_mfma_f32_16x16x32_bf16 a[204:207], v[76:79], v[20:23], a[204:207]\n"
        "ds_read_b64_tr_b16 v[76:77], v232 offset:40960\n"
        "v_mul_f32 v193, v180, v188\n"
        "v_mfma_f32_16x16x32_bf16 a[204:207], v[44:47], v[16:19], a[204:207]\n"
        "ds_read_b64_tr_b16 v[44:45], v232 offset:32768\n"
        "v_mul_f32 v195, v181, v188\n"
        "v_mfma_f32_16x16x32_bf16 a[208:211], v[16:19], v[48:51], a[208:211]\n"
        "ds_read_b64_tr_b16 v[46:47], v233 offset:32768\n"
        "global_load_lds_dwordx4 v234, s[58:59] offset:0\n"
        "v_mfma_f32_16x16x32_bf16 a[208:211], v[20:23], v[80:83], a[208:211]\n"
        "ds_read_b64_tr_b16 v[78:79], v233 offset:40960\n"
        "v_cvt_pk_bf16_f32 v193, 0, v193\n"
        "v_mfma_f32_16x16x32_bf16 a[240:243], v[28:31], v[80:83], a[240:243]\n"
        "ds_read_b64_tr_b16 v[80:81], v226 offset:57344\n"
        "v_cvt_pk_bf16_f32 v195, 0, v195\n"
        "v_mfma_f32_16x16x32_bf16 a[240:243], v[24:27], v[48:51], a[240:243]\n"
        "ds_read_b64_tr_b16 v[48:49], v226 offset:49152\n"
        "v_mul_f32 v194, v181, v187\n"
        "v_mfma_f32_16x16x32_bf16 a[244:247], v[24:27], v[52:55], a[244:247]\n"
        "ds_read_b64_tr_b16 v[50:51], v227 offset:49152\n"
        "global_load_lds_dwordx4 v235, s[58:59] offset:1024\n"
        "v_mfma_f32_16x16x32_bf16 a[244:247], v[28:31], v[84:87], a[244:247]\n"
        "ds_read_b64_tr_b16 v[82:83], v227 offset:57344\n"
        "v_mul_f32 v196, v182, v187\n"
        "v_mfma_f32_16x16x32_bf16 a[212:215], v[20:23], v[84:87], a[212:215]\n"
        "ds_read_b64_tr_b16 v[84:85], v228 offset:57344\n"
        "v_cvt_pk_bf16_f32 v194, 0, v194\n"
        "v_mfma_f32_16x16x32_bf16 a[212:215], v[16:19], v[52:55], a[212:215]\n"
        "ds_read_b64_tr_b16 v[52:53], v228 offset:49152\n"
        "v_cvt_pk_bf16_f32 v196, 0, v196\n"
        "v_mfma_f32_16x16x32_bf16 a[216:219], v[16:19], v[56:59], a[216:219]\n"
        "ds_read_b64_tr_b16 v[54:55], v229 offset:49152\n"
        "global_load_lds_dwordx4 v236, s[58:59] offset:2048\n"
        "v_mfma_f32_16x16x32_bf16 a[216:219], v[20:23], v[88:91], a[216:219]\n"
        "ds_read_b64_tr_b16 v[86:87], v229 offset:57344\n"
        "v_add_f32 v193, v193, v194\n"
        "v_mfma_f32_16x16x32_bf16 a[248:251], v[28:31], v[88:91], a[248:251]\n"
        "ds_read_b64_tr_b16 v[88:89], v230 offset:57344\n"
        "v_add_f32 v195, v195, v196\n"
        "v_mfma_f32_16x16x32_bf16 a[248:251], v[24:27], v[56:59], a[248:251]\n"
        "ds_read_b64_tr_b16 v[56:57], v230 offset:49152\n"
        "v_cvt_pk_bf16_f32 v193, 0, v193\n"
        "v_mfma_f32_16x16x32_bf16 a[252:255], v[24:27], v[60:63], a[252:255]\n"
        "ds_read_b64_tr_b16 v[58:59], v231 offset:49152\n"
        "global_load_lds_dwordx4 v237, s[58:59] offset:3072\n"
        "s_add_u32 m0, s73, 0x10000\n"
        "v_mfma_f32_16x16x32_bf16 a[252:255], v[28:31], v[92:95], a[252:255]\n"
        "ds_read_b64_tr_b16 v[90:91], v231 offset:57344\n"
        "s_add_u32 s58, s58, 0x80\n"
        "s_addc_u32 s59, s59, 0\n"
        "v_mfma_f32_16x16x32_bf16 a[220:223], v[20:23], v[92:95], a[220:223]\n"
        "ds_read_b64_tr_b16 v[92:93], v232 offset:57344\n"
        "v_cvt_pk_bf16_f32 v195, 0, v195\n"
        "v_mfma_f32_16x16x32_bf16 a[220:223], v[16:19], v[60:63], a[220:223]\n"
        "ds_read_b64_tr_b16 v[60:61], v232 offset:49152\n"
        "s_waitcnt lgkmcnt(8)\n"
        "v_mfma_f32_16x16x32_bf16 a[0:3], v[32:35], v[0:3], a[0:3]\n"
        "ds_read_b64_tr_b16 v[62:63], v233 offset:49152\n"
        "global_load_dwordx4 v[158:161], v254, s[30:31] offset:0\n"
        "v_mfma_f32_16x16x32_bf16 a[0:3], v[64:67], v[4:7], a[0:3]\n"
        "ds_read_b64_tr_b16 v[94:95], v233 offset:57344\n"
        "v_mul_f32 v194, v182, v186\n"
        "v_mfma_f32_16x16x32_bf16 a[32:35], v[64:67], v[12:15], a[32:35]\n"
        "ds_read_b128 v[16:19], v224 offset:4096\n"
        "v_mul_f32 v196, v183, v186\n"
        "v_mfma_f32_16x16x32_bf16 a[32:35], v[32:35], v[8:11], a[32:35]\n"
        "ds_read_b128 v[20:23], v225 offset:4096\n"
        "v_cvt_pk_bf16_f32 v194, 0, v194\n"
        "v_mfma_f32_16x16x32_bf16 a[36:39], v[36:39], v[8:11], a[36:39]\n"
        "ds_read_b128 v[24:27], v224 offset:6144\n"
        "global_load_lds_dwordx4 v238, s[60:61] offset:0\n"
        "v_mfma_f32_16x16x32_bf16 a[36:39], v[68:71], v[12:15], a[36:39]\n"
        "ds_read_b128 v[28:31], v225 offset:6144\n"
        "v_cvt_pk_bf16_f32 v196, 0, v196\n"
        "v_mfma_f32_16x16x32_bf16 a[4:7], v[68:71], v[4:7], a[4:7]\n"
        "v_add_f32 v193, v193, v194\n"
        "v_add_f32 v195, v195, v196\n"
        "v_mfma_f32_16x16x32_bf16 a[4:7], v[36:39], v[0:3], a[4:7]\n"
        "v_cvt_pk_bf16_f32 v193, 0, v193\n"
        "v_cvt_pk_bf16_f32 v195, 0, v195\n"
        "v_mfma_f32_16x16x32_bf16 a[8:11], v[40:43], v[0:3], a[8:11]\n"
        "global_load_lds_dwordx4 v239, s[60:61] offset:1024\n"
        "v_mfma_f32_16x16x32_bf16 a[8:11], v[72:75], v[4:7], a[8:11]\n"
        "s_waitcnt lgkmcnt(4)\n"
        "v_mul_f32 v194, v183, v185\n"
        "v_mul_f32 v196, v184, v185\n"
        "v_mfma_f32_16x16x32_bf16 a[40:43], v[72:75], v[12:15], a[40:43]\n"
        "v_cvt_pk_bf16_f32 v194, 0, v194\n"
        "v_cvt_pk_bf16_f32 v196, 0, v196\n"
        "v_mfma_f32_16x16x32_bf16 a[40:43], v[40:43], v[8:11], a[40:43]\n"
        "v_add_f32 v193, v193, v194\n"
        "v_add_f32 v195, v195, v196\n"
        "v_mfma_f32_16x16x32_bf16 a[44:47], v[44:47], v[8:11], a[44:47]\n"
        "global_load_lds_dwordx4 v240, s[60:61] offset:2048\n"
        "v_mfma_f32_16x16x32_bf16 a[44:47], v[76:79], v[12:15], a[44:47]\n"
        "v_cvt_pk_bf16_f32 v193, 0, v193\n"
        "v_cvt_pk_bf16_f32 v195, 0, v195\n"
        "v_mfma_f32_16x16x32_bf16 a[12:15], v[76:79], v[4:7], a[12:15]\n"
        "v_and_b32 v194, 0xffff0000, v130\n"
        "v_and_b32 v196, 0xffff0000, v132\n"
        "v_mfma_f32_16x16x32_bf16 a[12:15], v[44:47], v[0:3], a[12:15]\n"
        "v_mul_f32 v193, v193, v194\n"
        "v_mul_f32 v195, v195, v196\n"
        "v_mfma_f32_16x16x32_bf16 a[16:19], v[0:3], v[48:51], a[16:19]\n"
        "global_load_lds_dwordx4 v241, s[60:61] offset:3072\n"
        "s_add_u32 m0, s73, 0x14000\n"
        "v_mfma_f32_16x16x32_bf16 a[16:19], v[4:7], v[80:83], a[16:19]\n"
        "s_add_u32 s60, s60, 0x80000\n"
        "s_addc_u32 s61, s61, 0\n"
        "v_cvt_pk_bf16_f32 v208, v195, v193\n"
        "v_mfma_f32_16x16x32_bf16 a[48:51], v[12:15], v[80:83], a[48:51]\n"
        "v_lshlrev_b32 v185, 16, v138\n"
        "v_and_b32 v186, 0xffff0000, v138\n"
        "v_mfma_f32_16x16x32_bf16 a[48:51], v[8:11], v[48:51], a[48:51]\n"
        "v_lshlrev_b32 v187, 16, v139\n"
        "v_and_b32 v188, 0xffff0000, v139\n"
        "v_mfma_f32_16x16x32_bf16 a[52:55], v[8:11], v[52:55], a[52:55]\n"
        "global_load_lds_dwordx4 v238, s[62:63] offset:0\n"
        "v_mfma_f32_16x16x32_bf16 a[52:55], v[12:15], v[84:87], a[52:55]\n"
        "s_waitcnt lgkmcnt(0)\n"
        "v_lshlrev_b32 v174, 16, v97\n"
        "v_lshlrev_b32 v175, 16, v99\n"
        "v_mfma_f32_16x16x32_bf16 a[20:23], v[4:7], v[84:87], a[20:23]\n"
        "v_lshlrev_b32 v176, 16, v101\n"
        "v_lshlrev_b32 v177, 16, v103\n"
        "v_mfma_f32_16x16x32_bf16 a[20:23], v[0:3], v[52:55], a[20:23]\n"
        "v_lshlrev_b32 v178, 16, v105\n"
        "v_lshlrev_b32 v179, 16, v107\n"
        "v_mfma_f32_16x16x32_bf16 a[24:27], v[0:3], v[56:59], a[24:27]\n"
        "global_load_lds_dwordx4 v239, s[62:63] offset:1024\n"
        "v_mfma_f32_16x16x32_bf16 a[24:27], v[4:7], v[88:91], a[24:27]\n"
        "v_lshlrev_b32 v180, 16, v109\n"
        "v_lshlrev_b32 v181, 16, v111\n"
        "v_mfma_f32_16x16x32_bf16 a[56:59], v[12:15], v[88:91], a[56:59]\n"
        "v_lshlrev_b32 v182, 16, v113\n"
        "v_lshlrev_b32 v183, 16, v115\n"
        "v_mfma_f32_16x16x32_bf16 a[56:59], v[8:11], v[56:59], a[56:59]\n"
        "v_lshlrev_b32 v184, 16, v117\n"
        "v_mul_f32 v189, v174, v188\n"
        "v_mfma_f32_16x16x32_bf16 a[60:63], v[8:11], v[60:63], a[60:63]\n"
        "global_load_dwordx4 v[162:165], v254, s[30:31] offset:1024\n"
        "v_mfma_f32_16x16x32_bf16 a[60:63], v[12:15], v[92:95], a[60:63]\n"
        "ds_read_b128 v[8:11], v224 offset:10240\n"
        "v_mul_f32 v191, v175, v188\n"
        "v_mfma_f32_16x16x32_bf16 a[28:31], v[4:7], v[92:95], a[28:31]\n"
        "ds_read_b128 v[4:7], v225 offset:8192\n"
        "v_cvt_pk_bf16_f32 v189, 0, v189\n"
        "v_mfma_f32_16x16x32_bf16 a[28:31], v[0:3], v[60:63], a[28:31]\n"
        "ds_read_b128 v[0:3], v224 offset:8192\n"
        "v_cvt_pk_bf16_f32 v191, 0, v191\n"
        "v_mfma_f32_16x16x32_bf16 a[64:67], v[32:35], v[16:19], a[64:67]\n"
        "global_load_dword v253, v173, s[82:83] offset:0\n"
        "v_mfma_f32_16x16x32_bf16 a[64:67], v[64:67], v[20:23], a[64:67]\n"
        "global_load_lds_dwordx4 v240, s[62:63] offset:2048\n"
        "ds_read_b128 v[12:15], v225 offset:10240\n"
        "v_mfma_f32_16x16x32_bf16 a[96:99], v[64:67], v[28:31], a[96:99]\n"
        "s_add_u32 s82, s12, 0x400000\n"
        "s_addc_u32 s83, s13, 0\n"
        "v_mul_f32 v190, v175, v187\n"
        "v_mfma_f32_16x16x32_bf16 a[96:99], v[32:35], v[24:27], a[96:99]\n"
        "v_mul_f32 v192, v176, v187\n"
        "v_cvt_pk_bf16_f32 v190, 0, v190\n"
        "v_mfma_f32_16x16x32_bf16 a[100:103], v[36:39], v[24:27], a[100:103]\n"
        "v_cvt_pk_bf16_f32 v192, 0, v192\n"
        "v_add_f32 v189, v189, v190\n"
        "v_mfma_f32_16x16x32_bf16 a[100:103], v[68:71], v[28:31], a[100:103]\n"
        "global_load_lds_dwordx4 v241, s[62:63] offset:3072\n"
        "s_add_u32 s62, s62, 0x80000\n"
        "s_addc_u32 s63, s63, 0\n"
        "v_mfma_f32_16x16x32_bf16 a[68:71], v[68:71], v[20:23], a[68:71]\n"
        "s_add_u32 m0, s72, 0x0\n"
        "v_add_f32 v191, v191, v192\n"
        "v_cvt_pk_bf16_f32 v189, 0, v189\n"
        "v_mfma_f32_16x16x32_bf16 a[68:71], v[36:39], v[16:19], a[68:71]\n"
        "v_cvt_pk_bf16_f32 v191, 0, v191\n"
        "v_mul_f32 v190, v176, v186\n"
        "v_mfma_f32_16x16x32_bf16 a[72:75], v[40:43], v[16:19], a[72:75]\n"
        "v_mul_f32 v192, v177, v186\n"
        "v_cvt_pk_bf16_f32 v190, 0, v190\n"
        "v_mfma_f32_16x16x32_bf16 a[72:75], v[72:75], v[20:23], a[72:75]\n"
        "global_load_ushort v172, v172, s[28:29]\n"
        "v_cvt_pk_bf16_f32 v192, 0, v192\n"
        "v_mfma_f32_16x16x32_bf16 a[104:107], v[72:75], v[28:31], a[104:107]\n"
        "global_store_dwordx4 v171, v[208:211], s[76:77] offset:0 sc1\n"
        "s_add_u32 s76, s76, 0x2000\n"
        "s_addc_u32 s77, s77, 0\n"
        "v_add_f32 v189, v189, v190\n"
        "v_add_f32 v191, v191, v192\n"
        "v_cvt_pk_bf16_f32 v189, 0, v189\n"
        "v_mfma_f32_16x16x32_bf16 a[104:107], v[40:43], v[24:27], a[104:107]\n"
        "v_cvt_pk_bf16_f32 v191, 0, v191\n"
        "v_mul_f32 v190, v177, v185\n"
        "v_mfma_f32_16x16x32_bf16 a[108:111], v[44:47], v[24:27], a[108:111]\n"
        "v_mul_f32 v192, v178, v185\n"
        "v_cvt_pk_bf16_f32 v190, 0, v190\n"
        "v_mfma_f32_16x16x32_bf16 a[108:111], v[76:79], v[28:31], a[108:111]\n"
        "v_cvt_pk_bf16_f32 v192, 0, v192\n"
        "v_add_f32 v189, v189, v190\n"
        "v_mfma_f32_16x16x32_bf16 a[76:79], v[76:79], v[20:23], a[76:79]\n"
        "v_add_f32 v191, v191, v192\n"
        "v_cvt_pk_bf16_f32 v189, 0, v189\n"
        "v_mfma_f32_16x16x32_bf16 a[76:79], v[44:47], v[16:19], a[76:79]\n"
        "v_cvt_pk_bf16_f32 v191, 0, v191\n"
        "v_lshlrev_b32 v190, 16, v119\n"
        "v_mfma_f32_16x16x32_bf16 a[80:83], v[16:19], v[48:51], a[80:83]\n"
        "v_lshlrev_b32 v192, 16, v121\n"
        "v_mul_f32 v189, v189, v190\n"
        "v_mfma_f32_16x16x32_bf16 a[80:83], v[20:23], v[80:83], a[80:83]\n"
        "v_mul_f32 v191, v191, v192\n"
        "v_cvt_pk_bf16_f32 v203, v191, v189\n"
        "v_mfma_f32_16x16x32_bf16 a[112:115], v[28:31], v[80:83], a[112:115]\n"
        "v_mul_f32 v193, v176, v188\n"
        "v_mul_f32 v195, v177, v188\n"
        "v_mfma_f32_16x16x32_bf16 a[112:115], v[24:27], v[48:51], a[112:115]\n"
        "v_cvt_pk_bf16_f32 v193, 0, v193\n"
        "v_cvt_pk_bf16_f32 v195, 0, v195\n"
        "v_mfma_f32_16x16x32_bf16 a[116:119], v[24:27], v[52:55], a[116:119]\n"
        "v_mul_f32 v194, v177, v187\n"
        "v_mul_f32 v196, v178, v187\n"
        "v_mfma_f32_16x16x32_bf16 a[116:119], v[28:31], v[84:87], a[116:119]\n"
        "s_waitcnt lgkmcnt(0)\n"
        "v_cvt_pk_bf16_f32 v194, 0, v194\n"
        "v_cvt_pk_bf16_f32 v196, 0, v196\n"
        "v_mfma_f32_16x16x32_bf16 a[84:87], v[20:23], v[84:87], a[84:87]\n"
        "v_add_f32 v193, v193, v194\n"
        "v_add_f32 v195, v195, v196\n"
        "v_mfma_f32_16x16x32_bf16 a[84:87], v[16:19], v[52:55], a[84:87]\n"
        "v_cvt_pk_bf16_f32 v193, 0, v193\n"
        "v_cvt_pk_bf16_f32 v195, 0, v195\n"
        "v_mfma_f32_16x16x32_bf16 a[88:91], v[16:19], v[56:59], a[88:91]\n"
        "v_mul_f32 v194, v178, v186\n"
        "v_mul_f32 v196, v179, v186\n"
        "v_mfma_f32_16x16x32_bf16 a[88:91], v[20:23], v[88:91], a[88:91]\n"
        "v_cvt_pk_bf16_f32 v194, 0, v194\n"
        "v_cvt_pk_bf16_f32 v196, 0, v196\n"
        "v_mfma_f32_16x16x32_bf16 a[120:123], v[28:31], v[88:91], a[120:123]\n"
        "v_add_f32 v193, v193, v194\n"
        "v_add_f32 v195, v195, v196\n"
        "v_mfma_f32_16x16x32_bf16 a[120:123], v[24:27], v[56:59], a[120:123]\n"
        "v_cvt_pk_bf16_f32 v193, 0, v193\n"
        "v_cvt_pk_bf16_f32 v195, 0, v195\n"
        "v_mfma_f32_16x16x32_bf16 a[124:127], v[24:27], v[60:63], a[124:127]\n"
        "ds_read_b128 v[24:27], v224 offset:14336\n"
        "v_mul_f32 v194, v179, v185\n"
        "v_mfma_f32_16x16x32_bf16 a[124:127], v[28:31], v[92:95], a[124:127]\n"
        "ds_read_b128 v[28:31], v225 offset:14336\n"
        "v_mul_f32 v196, v180, v185\n"
        "v_mfma_f32_16x16x32_bf16 a[92:95], v[20:23], v[92:95], a[92:95]\n"
        "ds_read_b128 v[20:23], v225 offset:12288\n"
        "v_cvt_pk_bf16_f32 v194, 0, v194\n"
        "v_mfma_f32_16x16x32_bf16 a[92:95], v[16:19], v[60:63], a[92:95]\n"
        "ds_read_b128 v[16:19], v224 offset:12288\n"
        "global_load_ushort v198, v198, s[26:27]\n"
        "v_mfma_f32_16x16x32_bf16 a[128:131], v[32:35], v[0:3], a[128:131]\n"
        "v_cvt_pk_bf16_f32 v196, 0, v196\n"
        "v_add_f32 v193, v193, v194\n"
        "v_mfma_f32_16x16x32_bf16 a[128:131], v[64:67], v[4:7], a[128:131]\n"
        "v_add_f32 v195, v195, v196\n"
        "v_cvt_pk_bf16_f32 v193, 0, v193\n"
        "v_mfma_f32_16x16x32_bf16 a[160:163], v[64:67], v[12:15], a[160:163]\n"
        "v_cvt_pk_bf16_f32 v195, 0, v195\n"
        "v_lshlrev_b32 v194, 16, v123\n"
        "v_mfma_f32_16x16x32_bf16 a[160:163], v[32:35], v[8:11], a[160:163]\n"
        "v_lshlrev_b32 v196, 16, v125\n"
        "v_mul_f32 v193, v193, v194\n"
        "v_mfma_f32_16x16x32_bf16 a[164:167], v[36:39], v[8:11], a[164:167]\n"
        "v_mul_f32 v195, v195, v196\n"
        "v_cvt_pk_bf16_f32 v202, v195, v193\n"
        "v_mfma_f32_16x16x32_bf16 a[164:167], v[68:71], v[12:15], a[164:167]\n"
        "v_mul_f32 v189, v178, v188\n"
        "v_mul_f32 v191, v179, v188\n"
        "v_mfma_f32_16x16x32_bf16 a[132:135], v[68:71], v[4:7], a[132:135]\n"
        "v_cvt_pk_bf16_f32 v189, 0, v189\n"
        "v_cvt_pk_bf16_f32 v191, 0, v191\n"
        "v_mfma_f32_16x16x32_bf16 a[132:135], v[36:39], v[0:3], a[132:135]\n"
        "v_mul_f32 v190, v179, v187\n"
        "v_mul_f32 v192, v180, v187\n"
        "v_mfma_f32_16x16x32_bf16 a[136:139], v[40:43], v[0:3], a[136:139]\n"
        "v_cvt_pk_bf16_f32 v190, 0, v190\n"
        "v_cvt_pk_bf16_f32 v192, 0, v192\n"
        "v_mfma_f32_16x16x32_bf16 a[136:139], v[72:75], v[4:7], a[136:139]\n"
        "v_add_f32 v189, v189, v190\n"
        "v_add_f32 v191, v191, v192\n"
        "v_mfma_f32_16x16x32_bf16 a[168:171], v[72:75], v[12:15], a[168:171]\n"
        "v_cvt_pk_bf16_f32 v189, 0, v189\n"
        "v_cvt_pk_bf16_f32 v191, 0, v191\n"
        "v_mfma_f32_16x16x32_bf16 a[168:171], v[40:43], v[8:11], a[168:171]\n"
        "v_mul_f32 v190, v180, v186\n"
        "v_mul_f32 v192, v181, v186\n"
        "v_mfma_f32_16x16x32_bf16 a[172:175], v[44:47], v[8:11], a[172:175]\n"
        "v_cvt_pk_bf16_f32 v190, 0, v190\n"
        "v_cvt_pk_bf16_f32 v192, 0, v192\n"
        "v_mfma_f32_16x16x32_bf16 a[172:175], v[76:79], v[12:15], a[172:175]\n"
        "v_add_f32 v189, v189, v190\n"
        "v_add_f32 v191, v191, v192\n"
        "v_mfma_f32_16x16x32_bf16 a[140:143], v[76:79], v[4:7], a[140:143]\n"
        "v_cvt_pk_bf16_f32 v189, 0, v189\n"
        "v_cvt_pk_bf16_f32 v191, 0, v191\n"
        "v_mfma_f32_16x16x32_bf16 a[140:143], v[44:47], v[0:3], a[140:143]\n"
        "v_mul_f32 v190, v181, v185\n"
        "v_mul_f32 v192, v182, v185\n"
        "v_mfma_f32_16x16x32_bf16 a[144:147], v[0:3], v[48:51], a[144:147]\n"
        "v_cvt_pk_bf16_f32 v190, 0, v190\n"
        "v_cvt_pk_bf16_f32 v192, 0, v192\n"
        "v_mfma_f32_16x16x32_bf16 a[144:147], v[4:7], v[80:83], a[144:147]\n"
        "v_add_f32 v189, v189, v190\n"
        "v_add_f32 v191, v191, v192\n"
        "v_mfma_f32_16x16x32_bf16 a[176:179], v[12:15], v[80:83], a[176:179]\n"
        "v_cvt_pk_bf16_f32 v189, 0, v189\n"
        "v_cvt_pk_bf16_f32 v191, 0, v191\n"
        "v_mfma_f32_16x16x32_bf16 a[176:179], v[8:11], v[48:51], a[176:179]\n"
        "v_lshlrev_b32 v190, 16, v127\n"
        "v_lshlrev_b32 v192, 16, v129\n"
        "v_mfma_f32_16x16x32_bf16 a[180:183], v[8:11], v[52:55], a[180:183]\n"
        "v_mul_f32 v189, v189, v190\n"
        "v_mul_f32 v191, v191, v192\n"
        "v_mfma_f32_16x16x32_bf16 a[180:183], v[12:15], v[84:87], a[180:183]\n"
        "s_waitcnt lgkmcnt(0)\n"
        "v_cvt_pk_bf16_f32 v201, v191, v189\n"
        "v_mul_f32 v193, v180, v188\n"
        "v_mfma_f32_16x16x32_bf16 a[148:151], v[4:7], v[84:87], a[148:151]\n"
        "v_mul_f32 v195, v181, v188\n"
        "v_cvt_pk_bf16_f32 v193, 0, v193\n"
        "v_mfma_f32_16x16x32_bf16 a[148:151], v[0:3], v[52:55], a[148:151]\n"
        "v_cvt_pk_bf16_f32 v195, 0, v195\n"
        "v_mul_f32 v194, v181, v187\n"
        "v_mfma_f32_16x16x32_bf16 a[152:155], v[0:3], v[56:59], a[152:155]\n"
        "v_mul_f32 v196, v182, v187\n"
        "v_cvt_pk_bf16_f32 v194, 0, v194\n"
        "v_mfma_f32_16x16x32_bf16 a[152:155], v[4:7], v[88:91], a[152:155]\n"
        "v_cvt_pk_bf16_f32 v196, 0, v196\n"
        "v_add_f32 v193, v193, v194\n"
        "v_mfma_f32_16x16x32_bf16 a[184:187], v[12:15], v[88:91], a[184:187]\n"
        "v_add_f32 v195, v195, v196\n"
        "v_cvt_pk_bf16_f32 v193, 0, v193\n"
        "v_mfma_f32_16x16x32_bf16 a[184:187], v[8:11], v[56:59], a[184:187]\n"
        "s_waitcnt vmcnt(14)\n"
        "s_barrier\n"
        "v_cvt_pk_bf16_f32 v195, 0, v195\n"
        "v_mfma_f32_16x16x32_bf16 a[188:191], v[8:11], v[60:63], a[188:191]\n"
        "global_load_lds_dwordx4 v234, s[56:57] offset:0\n"
        "ds_read_b128 v[8:11], v224 offset:34816\n"
        "v_mfma_f32_16x16x32_bf16 a[188:191], v[12:15], v[92:95], a[188:191]\n"
        "ds_read_b128 v[12:15], v225 offset:34816\n"
        "v_mul_f32 v194, v182, v186\n"
        "v_mfma_f32_16x16x32_bf16 a[156:159], v[4:7], v[92:95], a[156:159]\n"
        "ds_read_b128 v[4:7], v225 offset:32768\n"
        "v_mul_f32 v196, v183, v186\n"
        "v_mfma_f32_16x16x32_bf16 a[156:159], v[0:3], v[60:63], a[156:159]\n"
        "ds_read_b128 v[0:3], v224 offset:32768\n"
        "v_cvt_pk_bf16_f32 v194, 0, v194\n"
        "v_mfma_f32_16x16x32_bf16 a[192:195], v[32:35], v[16:19], a[192:195]\n"
        "global_load_lds_dwordx4 v235, s[56:57] offset:1024\n"
        "v_cvt_pk_bf16_f32 v196, 0, v196\n"
        "v_mfma_f32_16x16x32_bf16 a[192:195], v[64:67], v[20:23], a[192:195]\n"
        "v_add_f32 v193, v193, v194\n"
        "v_add_f32 v195, v195, v196\n"
        "v_mfma_f32_16x16x32_bf16 a[224:227], v[64:67], v[28:31], a[224:227]\n"
        "ds_read_b64_tr_b16 v[64:65], v216 offset:8192\n"
        "v_cvt_pk_bf16_f32 v193, 0, v193\n"
        "v_mfma_f32_16x16x32_bf16 a[224:227], v[32:35], v[24:27], a[224:227]\n"
        "ds_read_b64_tr_b16 v[32:33], v216 offset:0\n"
        "v_cvt_pk_bf16_f32 v195, 0, v195\n"
        "v_mfma_f32_16x16x32_bf16 a[228:231], v[36:39], v[24:27], a[228:231]\n"
        "ds_read_b64_tr_b16 v[34:35], v217 offset:0\n"
        "global_load_lds_dwordx4 v236, s[56:57] offset:2048\n"
        "v_mfma_f32_16x16x32_bf16 a[228:231], v[68:71], v[28:31], a[228:231]\n"
        "ds_read_b64_tr_b16 v[66:67], v217 offset:8192\n"
        "v_mul_f32 v194, v183, v185\n"
        "v_mfma_f32_16x16x32_bf16 a[196:199], v[68:71], v[20:23], a[196:199]\n"
        "ds_read_b64_tr_b16 v[68:69], v218 offset:8192\n"
        "v_mul_f32 v196, v184, v185\n"
        "v_mfma_f32_16x16x32_bf16 a[196:199], v[36:39], v[16:19], a[196:199]\n"
        "ds_read_b64_tr_b16 v[36:37], v218 offset:0\n"
        "v_cvt_pk_bf16_f32 v194, 0, v194\n"
        "v_mfma_f32_16x16x32_bf16 a[200:203], v[40:43], v[16:19], a[200:203]\n"
        "ds_read_b64_tr_b16 v[38:39], v219 offset:0\n"
        "global_load_dword v253, v173, s[82:83] offset:0\n"
        "v_mfma_f32_16x16x32_bf16 a[200:203], v[72:75], v[20:23], a[200:203]\n"
        "ds_read_b64_tr_b16 v[70:71], v219 offset:8192\n"
        "global_load_lds_dwordx4 v237, s[56:57] offset:3072\n"
        "s_add_u32 m0, s72, 0x1000\n"
        "v_mfma_f32_16x16x32_bf16 a[232:235], v[72:75], v[28:31], a[232:235]\n"
        "ds_read_b64_tr_b16 v[72:73], v220 offset:8192\n"
        "s_add_u32 s56, s56, 0x80\n"
        "s_addc_u32 s57, s57, 0\n"
        "v_mfma_f32_16x16x32_bf16 a[232:235], v[40:43], v[24:27], a[232:235]\n"
        "ds_read_b64_tr_b16 v[40:41], v220 offset:0\n"
        "s_add_u32 s82, s12, 0x500000\n"
        "s_addc_u32 s83, s13, 0\n"
        "v_mfma_f32_16x16x32_bf16 a[236:239], v[44:47], v[24:27], a[236:239]\n"
        "ds_read_b64_tr_b16 v[42:43], v221 offset:0\n"
        "v_cvt_pk_bf16_f32 v196, 0, v196\n"
        "v_mfma_f32_16x16x32_bf16 a[236:239], v[76:79], v[28:31], a[236:239]\n"
        "ds_read_b64_tr_b16 v[74:75], v221 offset:8192\n"
        "global_load_lds_dwordx4 v234, s[58:59] offset:0\n"
        "v_mfma_f32_16x16x32_bf16 a[204:207], v[76:79], v[20:23], a[204:207]\n"
        "ds_read_b64_tr_b16 v[76:77], v222 offset:8192\n"
        "v_add_f32 v193, v193, v194\n"
        "v_mfma_f32_16x16x32_bf16 a[204:207], v[44:47], v[16:19], a[204:207]\n"
        "ds_read_b64_tr_b16 v[44:45], v222 offset:0\n"
        "v_add_f32 v195, v195, v196\n"
        "v_mfma_f32_16x16x32_bf16 a[208:211], v[16:19], v[48:51], a[208:211]\n"
        "ds_read_b64_tr_b16 v[46:47], v223 offset:0\n"
        "v_cvt_pk_bf16_f32 v193, 0, v193\n"
        "v_mfma_f32_16x16x32_bf16 a[208:211], v[20:23], v[80:83], a[208:211]\n"
        "ds_read_b64_tr_b16 v[78:79], v223 offset:8192\n"
        "global_load_lds_dwordx4 v235, s[58:59] offset:1024\n"
        "v_mfma_f32_16x16x32_bf16 a[240:243], v[28:31], v[80:83], a[240:243]\n"
        "ds_read_b64_tr_b16 v[80:81], v216 offset:24576\n"
        "v_cvt_pk_bf16_f32 v195, 0, v195\n"
        "v_mfma_f32_16x16x32_bf16 a[240:243], v[24:27], v[48:51], a[240:243]\n"
        "ds_read_b64_tr_b16 v[48:49], v216 offset:16384\n"
        "v_lshlrev_b32 v194, 16, v131\n"
        "v_mfma_f32_16x16x32_bf16 a[244:247], v[24:27], v[52:55], a[244:247]\n"
        "ds_read_b64_tr_b16 v[50:51], v217 offset:16384\n"
        "v_lshlrev_b32 v196, 16, v133\n"
        "v_mfma_f32_16x16x32_bf16 a[244:247], v[28:31], v[84:87], a[244:247]\n"
        "ds_read_b64_tr_b16 v[82:83], v217 offset:24576\n"
        "global_load_lds_dwordx4 v236, s[58:59] offset:2048\n"
        "v_mfma_f32_16x16x32_bf16 a[212:215], v[20:23], v[84:87], a[212:215]\n"
        "ds_read_b64_tr_b16 v[84:85], v218 offset:24576\n"
        "v_mul_f32 v193, v193, v194\n"
        "v_mfma_f32_16x16x32_bf16 a[212:215], v[16:19], v[52:55], a[212:215]\n"
        "ds_read_b64_tr_b16 v[52:53], v218 offset:16384\n"
        "v_mul_f32 v195, v195, v196\n"
        "v_mfma_f32_16x16x32_bf16 a[216:219], v[16:19], v[56:59], a[216:219]\n"
        "ds_read_b64_tr_b16 v[54:55], v219 offset:16384\n"
        "v_cvt_pk_bf16_f32 v200, v195, v193\n"
        "v_mfma_f32_16x16x32_bf16 a[216:219], v[20:23], v[88:91], a[216:219]\n"
        "ds_read_b64_tr_b16 v[86:87], v219 offset:24576\n"
        "global_load_lds_dwordx4 v237, s[58:59] offset:3072\n"
        "s_add_u32 m0, s73, 0x18000\n"
        "v_mfma_f32_16x16x32_bf16 a[248:251], v[28:31], v[88:91], a[248:251]\n"
        "ds_read_b64_tr_b16 v[88:89], v220 offset:24576\n"
        "s_add_u32 s58, s58, 0x80\n"
        "s_addc_u32 s59, s59, 0\n"
        "v_mfma_f32_16x16x32_bf16 a[248:251], v[24:27], v[56:59], a[248:251]\n"
        "ds_read_b64_tr_b16 v[56:57], v220 offset:16384\n"
        "v_lshlrev_b32 v185, 16, v140\n"
        "v_mfma_f32_16x16x32_bf16 a[252:255], v[24:27], v[60:63], a[252:255]\n"
        "ds_read_b64_tr_b16 v[58:59], v221 offset:16384\n"
        "v_and_b32 v186, 0xffff0000, v140\n"
        "v_mfma_f32_16x16x32_bf16 a[252:255], v[28:31], v[92:95], a[252:255]\n"
        "ds_read_b64_tr_b16 v[90:91], v221 offset:24576\n"
        "global_load_lds_dwordx4 v238, s[60:61] offset:0\n"
        "v_mfma_f32_16x16x32_bf16 a[220:223], v[20:23], v[92:95], a[220:223]\n"
        "ds_read_b64_tr_b16 v[92:93], v222 offset:24576\n"
        "v_lshlrev_b32 v187, 16, v141\n"
        "v_mfma_f32_16x16x32_bf16 a[220:223], v[16:19], v[60:63], a[220:223]\n"
        "ds_read_b64_tr_b16 v[60:61], v222 offset:16384\n"
        "s_waitcnt lgkmcnt(8)\n"
        "v_mfma_f32_16x16x32_bf16 a[0:3], v[32:35], v[0:3], a[0:3]\n"
        "ds_read_b64_tr_b16 v[62:63], v223 offset:16384\n"
        "v_and_b32 v188, 0xffff0000, v141\n"
        "v_mfma_f32_16x16x32_bf16 a[0:3], v[64:67], v[4:7], a[0:3]\n"
        "ds_read_b64_tr_b16 v[94:95], v223 offset:24576\n"
        "global_load_lds_dwordx4 v239, s[60:61] offset:1024\n"
        "v_mfma_f32_16x16x32_bf16 a[32:35], v[64:67], v[12:15], a[32:35]\n"
        "ds_read_b128 v[16:19], v224 offset:36864\n"
        "v_and_b32 v174, 0xffff0000, v97\n"
        "v_mfma_f32_16x16x32_bf16 a[32:35], v[32:35], v[8:11], a[32:35]\n"
        "ds_read_b128 v[20:23], v225 offset:36864\n"
        "v_and_b32 v175, 0xffff0000, v99\n"
        "v_mfma_f32_16x16x32_bf16 a[36:39], v[36:39], v[8:11], a[36:39]\n"
        "ds_read_b128 v[24:27], v224 offset:38912\n"
        "v_and_b32 v176, 0xffff0000, v101\n"
        "v_mfma_f32_16x16x32_bf16 a[36:39], v[68:71], v[12:15], a[36:39]\n"
        "ds_read_b128 v[28:31], v225 offset:38912\n"
        "global_load_lds_dwordx4 v240, s[60:61] offset:2048\n"
        "v_mfma_f32_16x16x32_bf16 a[4:7], v[68:71], v[4:7], a[4:7]\n"
        "v_and_b32 v177, 0xffff0000, v103\n"
        "v_and_b32 v178, 0xffff0000, v105\n"
        "v_mfma_f32_16x16x32_bf16 a[4:7], v[36:39], v[0:3], a[4:7]\n"
        "v_and_b32 v179, 0xffff0000, v107\n"
        "v_and_b32 v180, 0xffff0000, v109\n"
        "v_mfma_f32_16x16x32_bf16 a[8:11], v[40:43], v[0:3], a[8:11]\n"
        "v_and_b32 v181, 0xffff0000, v111\n"
        "v_and_b32 v182, 0xffff0000, v113\n"
        "v_mfma_f32_16x16x32_bf16 a[8:11], v[72:75], v[4:7], a[8:11]\n"
        "global_load_lds_dwordx4 v241, s[60:61] offset:3072\n"
        "s_add_u32 m0, s73, 0x1c000\n"
        "v_mfma_f32_16x16x32_bf16 a[40:43], v[72:75], v[12:15], a[40:43]\n"
        "s_waitcnt lgkmcnt(3)\n"
        "v_and_b32 v183, 0xffff0000, v115\n"
        "v_and_b32 v184, 0xffff0000, v117\n"
        "v_mfma_f32_16x16x32_bf16 a[40:43], v[40:43], v[8:11], a[40:43]\n"
        "v_mul_f32 v189, v174, v188\n"
        "v_mul_f32 v191, v175, v188\n"
        "v_mfma_f32_16x16x32_bf16 a[44:47], v[44:47], v[8:11], a[44:47]\n"
        "v_cvt_pk_bf16_f32 v189, 0, v189\n"
        "v_cvt_pk_bf16_f32 v191, 0, v191\n"
        "v_mfma_f32_16x16x32_bf16 a[44:47], v[76:79], v[12:15], a[44:47]\n"
        "global_load_lds_dwordx4 v238, s[62:63] offset:0\n"
        "v_mfma_f32_16x16x32_bf16 a[12:15], v[76:79], v[4:7], a[12:15]\n"
        "LHA3_%=:\n"
        "s_cbranch_execz LHA7_%=\n"
        "v_mul_f32 v190, v175, v187\n"
        "v_mul_f32 v192, v176, v187\n"
        "v_mfma_f32_16x16x32_bf16 a[12:15], v[44:47], v[0:3], a[12:15]\n"
        "v_cvt_pk_bf16_f32 v190, 0, v190\n"
        "v_cvt_pk_bf16_f32 v192, 0, v192\n"
        "v_mfma_f32_16x16x32_bf16 a[16:19], v[0:3], v[48:51], a[16:19]\n"
        "v_add_f32 v189, v189, v190\n"
        "v_add_f32 v191, v191, v192\n"
        "v_mfma_f32_16x16x32_bf16 a[16:19], v[4:7], v[80:83], a[16:19]\n"
        "global_load_lds_dwordx4 v239, s[62:63] offset:1024\n"
        "v_mfma_f32_16x16x32_bf16 a[48:51], v[12:15], v[80:83], a[48:51]\n"
        "v_cvt_pk_bf16_f32 v189, 0, v189\n"
        "v_cvt_pk_bf16_f32 v191, 0, v191\n"
        "v_mfma_f32_16x16x32_bf16 a[48:51], v[8:11], v[48:51], a[48:51]\n"
        "v_mul_f32 v190, v176, v186\n"
        "v_mul_f32 v192, v177, v186\n"
        "v_mfma_f32_16x16x32_bf16 a[52:55], v[8:11], v[52:55], a[52:55]\n"
        "v_cvt_pk_bf16_f32 v190, 0, v190\n"
        "v_cvt_pk_bf16_f32 v192, 0, v192\n"
        "v_mfma_f32_16x16x32_bf16 a[52:55], v[12:15], v[84:87], a[52:55]\n"
        "global_load_lds_dwordx4 v240, s[62:63] offset:2048\n"
        "v_mfma_f32_16x16x32_bf16 a[20:23], v[4:7], v[84:87], a[20:23]\n"
        "v_add_f32 v189, v189, v190\n"
        "v_add_f32 v191, v191, v192\n"
        "v_mfma_f32_16x16x32_bf16 a[20:23], v[0:3], v[52:55], a[20:23]\n"
        "s_waitcnt lgkmcnt(0)\n"
        "v_cvt_pk_bf16_f32 v189, 0, v189\n"
        "v_cvt_pk_bf16_f32 v191, 0, v191\n"
        "v_mfma_f32_16x16x32_bf16 a[24:27], v[0:3], v[56:59], a[24:27]\n"
        "v_mul_f32 v190, v177, v185\n"
        "v_mul_f32 v192, v178, v185\n"
        "v_mfma_f32_16x16x32_bf16 a[24:27], v[4:7], v[88:91], a[24:27]\n"
        "global_load_lds_dwordx4 v241, s[62:63] offset:3072\n"
        "s_add_u32 m0, s72, 0x8000\n"
        "v_mfma_f32_16x16x32_bf16 a[56:59], v[12:15], v[88:91], a[56:59]\n"
        "v_cvt_pk_bf16_f32 v190, 0, v190\n"
        "v_cvt_pk_bf16_f32 v192, 0, v192\n"
        "v_mfma_f32_16x16x32_bf16 a[56:59], v[8:11], v[56:59], a[56:59]\n"
        "v_add_f32 v189, v189, v190\n"
        "v_add_f32 v191, v191, v192\n"
        "v_mfma_f32_16x16x32_bf16 a[60:63], v[8:11], v[60:63], a[60:63]\n"
        "ds_read_b128 v[8:11], v224 offset:43008\n"
        "v_cvt_pk_bf16_f32 v189, 0, v189\n"
        "v_mfma_f32_16x16x32_bf16 a[60:63], v[12:15], v[92:95], a[60:63]\n"
        "ds_read_b128 v[12:15], v225 offset:43008\n"
        "v_cvt_pk_bf16_f32 v191, 0, v191\n"
        "v_mfma_f32_16x16x32_bf16 a[28:31], v[4:7], v[92:95], a[28:31]\n"
        "ds_read_b128 v[4:7], v225 offset:40960\n"
        "v_and_b32 v190, 0xffff0000, v119\n"
        "v_mfma_f32_16x16x32_bf16 a[28:31], v[0:3], v[60:63], a[28:31]\n"
        "ds_read_b128 v[0:3], v224 offset:40960\n"
        "v_and_b32 v192, 0xffff0000, v121\n"
        "v_mfma_f32_16x16x32_bf16 a[64:67], v[32:35], v[16:19], a[64:67]\n"
        "global_store_dwordx4 v171, v[200:203], s[76:77] offset:-4096 sc1\n"
        "s_waitcnt vmcnt(31)\n"
        "v_mul_f32 v189, v189, v190\n"
        "v_mul_f32 v191, v191, v192\n"
        "v_cvt_pk_bf16_f32 v211, v191, v189\n"
        "v_mfma_f32_16x16x32_bf16 a[64:67], v[64:67], v[20:23], a[64:67]\n"
        "v_mul_f32 v193, v176, v188\n"
        "v_mul_f32 v195, v177, v188\n"
        "v_mfma_f32_16x16x32_bf16 a[96:99], v[64:67], v[28:31], a[96:99]\n"
        "v_cvt_pk_bf16_f32 v193, 0, v193\n"
        "v_cvt_pk_bf16_f32 v195, 0, v195\n"
        "v_mfma_f32_16x16x32_bf16 a[96:99], v[32:35], v[24:27], a[96:99]\n"
        "v_mul_f32 v194, v177, v187\n"
        "v_mul_f32 v196, v178, v187\n"
        "v_mfma_f32_16x16x32_bf16 a[100:103], v[36:39], v[24:27], a[100:103]\n"
        "v_cvt_pk_bf16_f32 v194, 0, v194\n"
        "v_cvt_pk_bf16_f32 v196, 0, v196\n"
        "v_mfma_f32_16x16x32_bf16 a[100:103], v[68:71], v[28:31], a[100:103]\n"
        "v_add_f32 v193, v193, v194\n"
        "v_add_f32 v195, v195, v196\n"
        "v_mfma_f32_16x16x32_bf16 a[68:71], v[68:71], v[20:23], a[68:71]\n"
        "v_cvt_pk_bf16_f32 v193, 0, v193\n"
        "v_cvt_pk_bf16_f32 v195, 0, v195\n"
        "v_mfma_f32_16x16x32_bf16 a[68:71], v[36:39], v[16:19], a[68:71]\n"
        "v_mul_f32 v194, v178, v186\n"
        "v_mul_f32 v196, v179, v186\n"
        "v_mfma_f32_16x16x32_bf16 a[72:75], v[40:43], v[16:19], a[72:75]\n"
        "v_cvt_pk_bf16_f32 v194, 0, v194\n"
        "v_cvt_pk_bf16_f32 v196, 0, v196\n"
        "v_mfma_f32_16x16x32_bf16 a[72:75], v[72:75], v[20:23], a[72:75]\n"
        "v_add_f32 v193, v193, v194\n"
        "v_add_f32 v195, v195, v196\n"
        "v_mfma_f32_16x16x32_bf16 a[104:107], v[72:75], v[28:31], a[104:107]\n"
        "v_cvt_pk_bf16_f32 v193, 0, v193\n"
        "v_cvt_pk_bf16_f32 v195, 0, v195\n"
        "v_mfma_f32_16x16x32_bf16 a[104:107], v[40:43], v[24:27], a[104:107]\n"
        "v_mul_f32 v194, v179, v185\n"
        "v_mul_f32 v196, v180, v185\n"
        "v_mfma_f32_16x16x32_bf16 a[108:111], v[44:47], v[24:27], a[108:111]\n"
        "v_cvt_pk_bf16_f32 v194, 0, v194\n"
        "v_cvt_pk_bf16_f32 v196, 0, v196\n"
        "v_mfma_f32_16x16x32_bf16 a[108:111], v[76:79], v[28:31], a[108:111]\n"
        "v_add_f32 v193, v193, v194\n"
        "v_add_f32 v195, v195, v196\n"
        "v_mfma_f32_16x16x32_bf16 a[76:79], v[76:79], v[20:23], a[76:79]\n"
        "v_cvt_pk_bf16_f32 v193, 0, v193\n"
        "v_cvt_pk_bf16_f32 v195, 0, v195\n"
        "v_mfma_f32_16x16x32_bf16 a[76:79], v[44:47], v[16:19], a[76:79]\n"
        "v_and_b32 v194, 0xffff0000, v123\n"
        "v_and_b32 v196, 0xffff0000, v125\n"
        "v_mfma_f32_16x16x32_bf16 a[80:83], v[16:19], v[48:51], a[80:83]\n"
        "v_mul_f32 v193, v193, v194\n"
        "v_mul_f32 v195, v195, v196\n"
        "v_mfma_f32_16x16x32_bf16 a[80:83], v[20:23], v[80:83], a[80:83]\n"
        "v_cvt_pk_bf16_f32 v210, v195, v193\n"
        "v_mul_f32 v189, v178, v188\n"
        "v_mfma_f32_16x16x32_bf16 a[112:115], v[28:31], v[80:83], a[112:115]\n"
        "v_mul_f32 v191, v179, v188\n"
        "v_cvt_pk_bf16_f32 v189, 0, v189\n"
        "v_mfma_f32_16x16x32_bf16 a[112:115], v[24:27], v[48:51], a[112:115]\n"
        "v_cvt_pk_bf16_f32 v191, 0, v191\n"
        "v_mul_f32 v190, v179, v187\n"
        "v_mfma_f32_16x16x32_bf16 a[116:119], v[24:27], v[52:55], a[116:119]\n"
        "v_mul_f32 v192, v180, v187\n"
        "v_cvt_pk_bf16_f32 v190, 0, v190\n"
        "v_mfma_f32_16x16x32_bf16 a[116:119], v[28:31], v[84:87], a[116:119]\n"
        "s_waitcnt lgkmcnt(0)\n"
        "v_cvt_pk_bf16_f32 v192, 0, v192\n"
        "v_add_f32 v189, v189, v190\n"
        "v_mfma_f32_16x16x32_bf16 a[84:87], v[20:23], v[84:87], a[84:87]\n"
        "v_add_f32 v191, v191, v192\n"
        "v_cvt_pk_bf16_f32 v189, 0, v189\n"
        "v_mfma_f32_16x16x32_bf16 a[84:87], v[16:19], v[52:55], a[84:87]\n"
        "global_load_dword v253, v173, s[82:83] offset:0\n"
        "s_add_u32 s82, s12, 0x600000\n"
        "s_addc_u32 s83, s13, 0\n"
        "v_mfma_f32_16x16x32_bf16 a[88:91], v[16:19], v[56:59], a[88:91]\n"
        "v_cvt_pk_bf16_f32 v191, 0, v191\n"
        "v_mul_f32 v190, v180, v186\n"
        "v_mfma_f32_16x16x32_bf16 a[88:91], v[20:23], v[88:91], a[88:91]\n"
        "v_mul_f32 v192, v181, v186\n"
        "v_cvt_pk_bf16_f32 v190, 0, v190\n"
        "v_mfma_f32_16x16x32_bf16 a[120:123], v[28:31], v[88:91], a[120:123]\n"
        "v_cvt_pk_bf16_f32 v192, 0, v192\n"
        "v_add_f32 v189, v189, v190\n"
        "v_mfma_f32_16x16x32_bf16 a[120:123], v[24:27], v[56:59], a[120:123]\n"
        "v_add_f32 v191, v191, v192\n"
        "v_cvt_pk_bf16_f32 v189, 0, v189\n"
        "v_mfma_f32_16x16x32_bf16 a[124:127], v[24:27], v[60:63], a[124:127]\n"
        "ds_read_b128 v[24:27], v224 offset:47104\n"
        "v_cvt_pk_bf16_f32 v191, 0, v191\n"
        "v_mfma_f32_16x16x32_bf16 a[124:127], v[28:31], v[92:95], a[124:127]\n"
        "ds_read_b128 v[28:31], v225 offset:47104\n"
        "v_mul_f32 v190, v181, v185\n"
        "v_mfma_f32_16x16x32_bf16 a[92:95], v[20:23], v[92:95], a[92:95]\n"
        "ds_read_b128 v[20:23], v225 offset:45056\n"
        "v_mul_f32 v192, v182, v185\n"
        "v_mfma_f32_16x16x32_bf16 a[92:95], v[16:19], v[60:63], a[92:95]\n"
        "ds_read_b128 v[16:19], v224 offset:45056\n"
        "v_cvt_pk_bf16_f32 v190, 0, v190\n"
        "v_mfma_f32_16x16x32_bf16 a[128:131], v[32:35], v[0:3], a[128:131]\n"
        "v_cvt_pk_bf16_f32 v192, 0, v192\n"
        "v_add_f32 v189, v189, v190\n"
        "v_mfma_f32_16x16x32_bf16 a[128:131], v[64:67], v[4:7], a[128:131]\n"
        "v_add_f32 v191, v191, v192\n"
        "v_cvt_pk_bf16_f32 v189, 0, v189\n"
        "v_mfma_f32_16x16x32_bf16 a[160:163], v[64:67], v[12:15], a[160:163]\n"
        "v_cvt_pk_bf16_f32 v191, 0, v191\n"
        "v_and_b32 v190, 0xffff0000, v127\n"
        "v_mfma_f32_16x16x32_bf16 a[160:163], v[32:35], v[8:11], a[160:163]\n"
        "v_and_b32 v192, 0xffff0000, v129\n"
        "v_mul_f32 v189, v189, v190\n"
        "v_mfma_f32_16x16x32_bf16 a[164:167], v[36:39], v[8:11], a[164:167]\n"
        "v_mul_f32 v191, v191, v192\n"
        "v_cvt_pk_bf16_f32 v209, v191, v189\n"
        "v_mfma_f32_16x16x32_bf16 a[164:167], v[68:71], v[12:15], a[164:167]\n"
        "v_mul_f32 v193, v180, v188\n"
        "v_mul_f32 v195, v181, v188\n"
        "v_mfma_f32_16x16x32_bf16 a[132:135], v[68:71], v[4:7], a[132:135]\n"
        "v_cvt_pk_bf16_f32 v193, 0, v193\n"
        "v_cvt_pk_bf16_f32 v195, 0, v195\n"
        "v_mfma_f32_16x16x32_bf16 a[132:135], v[36:39], v[0:3], a[132:135]\n"
        "v_mul_f32 v194, v181, v187\n"
        "v_mul_f32 v196, v182, v187\n"
        "v_mfma_f32_16x16x32_bf16 a[136:139], v[40:43], v[0:3], a[136:139]\n"
        "v_cvt_pk_bf16_f32 v194, 0, v194\n"
        "v_cvt_pk_bf16_f32 v196, 0, v196\n"
        "v_mfma_f32_16x16x32_bf16 a[136:139], v[72:75], v[4:7], a[136:139]\n"
        "v_add_f32 v193, v193, v194\n"
        "v_add_f32 v195, v195, v196\n"
        "v_mfma_f32_16x16x32_bf16 a[168:171], v[72:75], v[12:15], a[168:171]\n"
        "v_cvt_pk_bf16_f32 v193, 0, v193\n"
        "v_cvt_pk_bf16_f32 v195, 0, v195\n"
        "v_mfma_f32_16x16x32_bf16 a[168:171], v[40:43], v[8:11], a[168:171]\n"
        "v_mul_f32 v194, v182, v186\n"
        "v_mul_f32 v196, v183, v186\n"
        "v_mfma_f32_16x16x32_bf16 a[172:175], v[44:47], v[8:11], a[172:175]\n"
        "v_cvt_pk_bf16_f32 v194, 0, v194\n"
        "v_cvt_pk_bf16_f32 v196, 0, v196\n"
        "v_mfma_f32_16x16x32_bf16 a[172:175], v[76:79], v[12:15], a[172:175]\n"
        "v_add_f32 v193, v193, v194\n"
        "v_add_f32 v195, v195, v196\n"
        "v_mfma_f32_16x16x32_bf16 a[140:143], v[76:79], v[4:7], a[140:143]\n"
        "v_cvt_pk_bf16_f32 v193, 0, v193\n"
        "v_cvt_pk_bf16_f32 v195, 0, v195\n"
        "v_mfma_f32_16x16x32_bf16 a[140:143], v[44:47], v[0:3], a[140:143]\n"
        "v_mul_f32 v194, v183, v185\n"
        "v_mul_f32 v196, v184, v185\n"
        "v_mfma_f32_16x16x32_bf16 a[144:147], v[0:3], v[48:51], a[144:147]\n"
        "v_cvt_pk_bf16_f32 v194, 0, v194\n"
        "v_cvt_pk_bf16_f32 v196, 0, v196\n"
        "v_mfma_f32_16x16x32_bf16 a[144:147], v[4:7], v[80:83], a[144:147]\n"
        "v_add_f32 v193, v193, v194\n"
        "v_add_f32 v195, v195, v196\n"
        "v_mfma_f32_16x16x32_bf16 a[176:179], v[12:15], v[80:83], a[176:179]\n"
        "v_cvt_pk_bf16_f32 v193, 0, v193\n"
        "v_cvt_pk_bf16_f32 v195, 0, v195\n"
        "v_mfma_f32_16x16x32_bf16 a[176:179], v[8:11], v[48:51], a[176:179]\n"
        "v_and_b32 v194, 0xffff0000, v131\n"
        "v_and_b32 v196, 0xffff0000, v133\n"
        "v_mfma_f32_16x16x32_bf16 a[180:183], v[8:11], v[52:55], a[180:183]\n"
        "v_mul_f32 v193, v193, v194\n"
        "v_mul_f32 v195, v195, v196\n"
        "v_mfma_f32_16x16x32_bf16 a[180:183], v[12:15], v[84:87], a[180:183]\n"
        "s_waitcnt lgkmcnt(0)\n"
        "v_cvt_pk_bf16_f32 v208, v195, v193\n"
        "v_readfirstlane_b32 s86, v250\n"
        "v_mfma_f32_16x16x32_bf16 a[148:151], v[4:7], v[84:87], a[148:151]\n"
        "global_store_dwordx4 v171, v[208:211], s[76:77] offset:0 sc1\n"
        "s_waitcnt vmcnt(26)\n"
        "v_readfirstlane_b32 s87, v251\n"
        "v_lshlrev_b32 v174, 16, v158\n"
        "v_and_b32 v175, 0xffff0000, v158\n"
        "v_mfma_f32_16x16x32_bf16 a[148:151], v[0:3], v[52:55], a[148:151]\n"
        "s_add_u32 s86, s86, 0x1\n"
        "s_addc_u32 s87, s87, 0\n"
        "v_mov_b32 v250, s86\n"
        "v_mfma_f32_16x16x32_bf16 a[152:155], v[0:3], v[56:59], a[152:155]\n"
        "s_add_u32 s76, s38, 0x3920040\n"
        "s_addc_u32 s77, s39, 0\n"
        "v_mov_b32 v251, s87\n"
        "v_mfma_f32_16x16x32_bf16 a[152:155], v[4:7], v[88:91], a[152:155]\n"
        "v_lshlrev_b32 v176, 16, v159\n"
        "v_and_b32 v177, 0xffff0000, v159\n"
        "v_mfma_f32_16x16x32_bf16 a[184:187], v[12:15], v[88:91], a[184:187]\n"
        "s_waitcnt vmcnt(20)\n"
        "v_lshl_or_b32 v172, v172, 16, v198\n"
        "v_lshlrev_b32 v178, 16, v160\n"
        "v_mfma_f32_16x16x32_bf16 a[184:187], v[8:11], v[56:59], a[184:187]\n"
        "s_waitcnt vmcnt(11)\n"
        "s_barrier\n"
        "v_readfirstlane_b32 s54, v172\n"
        "v_mfma_f32_16x16x32_bf16 a[188:191], v[8:11], v[60:63], a[188:191]\n"
        "global_load_lds_dwordx4 v234, s[56:57] offset:0\n"
        "ds_read_b128 v[8:11], v224 offset:2048\n"
        "v_mfma_f32_16x16x32_bf16 a[188:191], v[12:15], v[92:95], a[188:191]\n"
        "ds_read_b128 v[12:15], v225 offset:2048\n"
        "v_and_b32 v179, 0xffff0000, v160\n"
        "v_mfma_f32_16x16x32_bf16 a[156:159], v[4:7], v[92:95], a[156:159]\n"
        "ds_read_b128 v[4:7], v225 offset:0\n"
        "v_lshlrev_b32 v180, 16, v161\n"
        "v_mfma_f32_16x16x32_bf16 a[156:159], v[0:3], v[60:63], a[156:159]\n"
        "ds_read_b128 v[0:3], v224 offset:0\n"
        "v_and_b32 v181, 0xffff0000, v161\n"
        "v_mfma_f32_16x16x32_bf16 a[192:195], v[32:35], v[16:19], a[192:195]\n"
        "global_load_lds_dwordx4 v235, s[56:57] offset:1024\n"
        "v_lshlrev_b32 v182, 16, v162\n"
        "v_mfma_f32_16x16x32_bf16 a[192:195], v[64:67], v[20:23], a[192:195]\n"
        "v_and_b32 v183, 0xffff0000, v162\n"
        "v_lshlrev_b32 v184, 16, v163\n"
        "v_mfma_f32_16x16x32_bf16 a[224:227], v[64:67], v[28:31], a[224:227]\n"
        "ds_read_b64_tr_b16 v[64:65], v226 offset:8192\n"
        "v_and_b32 v185, 0xffff0000, v163\n"
        "v_mfma_f32_16x16x32_bf16 a[224:227], v[32:35], v[24:27], a[224:227]\n"
        "ds_read_b64_tr_b16 v[32:33], v226 offset:0\n"
        "v_lshlrev_b32 v186, 16, v164\n"
        "v_mfma_f32_16x16x32_bf16 a[228:231], v[36:39], v[24:27], a[228:231]\n"
        "ds_read_b64_tr_b16 v[34:35], v227 offset:0\n"
        "global_load_lds_dwordx4 v236, s[56:57] offset:2048\n"
        "v_mfma_f32_16x16x32_bf16 a[228:231], v[68:71], v[28:31], a[228:231]\n"
        "ds_read_b64_tr_b16 v[66:67], v227 offset:8192\n"
        "v_and_b32 v187, 0xffff0000, v164\n"
        "v_mfma_f32_16x16x32_bf16 a[196:199], v[68:71], v[20:23], a[196:199]\n"
        "ds_read_b64_tr_b16 v[68:69], v228 offset:8192\n"
        "v_lshlrev_b32 v188, 16, v165\n"
        "v_mfma_f32_16x16x32_bf16 a[196:199], v[36:39], v[16:19], a[196:199]\n"
        "ds_read_b64_tr_b16 v[36:37], v228 offset:0\n"
        "v_and_b32 v189, 0xffff0000, v165\n"
        "v_mfma_f32_16x16x32_bf16 a[200:203], v[40:43], v[16:19], a[200:203]\n"
        "ds_read_b64_tr_b16 v[38:39], v229 offset:0\n"
        "global_load_lds_dwordx4 v237, s[56:57] offset:3072\n"
        "s_add_u32 m0, s72, 0x9000\n"
        "v_mfma_f32_16x16x32_bf16 a[200:203], v[72:75], v[20:23], a[200:203]\n"
        "ds_read_b64_tr_b16 v[70:71], v229 offset:8192\n"
        "v_lshlrev_b32 v192, 16, v142\n"
        "v_mfma_f32_16x16x32_bf16 a[232:235], v[72:75], v[28:31], a[232:235]\n"
        "ds_read_b64_tr_b16 v[72:73], v230 offset:8192\n"
        "v_and_b32 v193, 0xffff0000, v142\n"
        "v_mfma_f32_16x16x32_bf16 a[232:235], v[40:43], v[24:27], a[232:235]\n"
        "ds_read_b64_tr_b16 v[40:41], v230 offset:0\n"
        "v_mul_f32 v192, v192, v174\n"
        "v_mfma_f32_16x16x32_bf16 a[236:239], v[44:47], v[24:27], a[236:239]\n"
        "ds_read_b64_tr_b16 v[42:43], v231 offset:0\n"
        "global_load_lds_dwordx4 v234, s[58:59] offset:0\n"
        "v_mfma_f32_16x16x32_bf16 a[236:239], v[76:79], v[28:31], a[236:239]\n"
        "ds_read_b64_tr_b16 v[74:75], v231 offset:8192\n"
        "v_mul_f32 v193, v193, v175\n"
        "v_mfma_f32_16x16x32_bf16 a[204:207], v[76:79], v[20:23], a[204:207]\n"
        "ds_read_b64_tr_b16 v[76:77], v232 offset:8192\n"
        "v_cvt_pk_bf16_f32 v192, 0, v192\n"
        "v_mfma_f32_16x16x32_bf16 a[204:207], v[44:47], v[16:19], a[204:207]\n"
        "ds_read_b64_tr_b16 v[44:45], v232 offset:0\n"
        "v_cvt_pk_bf16_f32 v193, 0, v193\n"
        "v_mfma_f32_16x16x32_bf16 a[208:211], v[16:19], v[48:51], a[208:211]\n"
        "ds_read_b64_tr_b16 v[46:47], v233 offset:0\n"
        "global_load_lds_dwordx4 v235, s[58:59] offset:1024\n"
        "v_mfma_f32_16x16x32_bf16 a[208:211], v[20:23], v[80:83], a[208:211]\n"
        "ds_read_b64_tr_b16 v[78:79], v233 offset:8192\n"
        "v_add_f32 v190, v192, v193\n"
        "v_mfma_f32_16x16x32_bf16 a[240:243], v[28:31], v[80:83], a[240:243]\n"
        "ds_read_b64_tr_b16 v[80:81], v226 offset:24576\n"
        "v_lshlrev_b32 v194, 16, v143\n"
        "v_mfma_f32_16x16x32_bf16 a[240:243], v[24:27], v[48:51], a[240:243]\n"
        "ds_read_b64_tr_b16 v[48:49], v226 offset:16384\n"
        "v_and_b32 v195, 0xffff0000, v143\n"
        "v_mfma_f32_16x16x32_bf16 a[244:247], v[24:27], v[52:55], a[244:247]\n"
        "ds_read_b64_tr_b16 v[50:51], v227 offset:16384\n"
        "global_load_lds_dwordx4 v236, s[58:59] offset:2048\n"
        "v_mfma_f32_16x16x32_bf16 a[244:247], v[28:31], v[84:87], a[244:247]\n"
        "ds_read_b64_tr_b16 v[82:83], v227 offset:24576\n"
        "v_mul_f32 v194, v194, v176\n"
        "v_mfma_f32_16x16x32_bf16 a[212:215], v[20:23], v[84:87], a[212:215]\n"
        "ds_read_b64_tr_b16 v[84:85], v228 offset:24576\n"
        "v_mul_f32 v195, v195, v177\n"
        "v_mfma_f32_16x16x32_bf16 a[212:215], v[16:19], v[52:55], a[212:215]\n"
        "ds_read_b64_tr_b16 v[52:53], v228 offset:16384\n"
        "v_cvt_pk_bf16_f32 v194, 0, v194\n"
        "v_mfma_f32_16x16x32_bf16 a[216:219], v[16:19], v[56:59], a[216:219]\n"
        "ds_read_b64_tr_b16 v[54:55], v229 offset:16384\n"
        "global_load_lds_dwordx4 v237, s[58:59] offset:3072\n"
        "s_add_u32 m0, s73, 0x20000\n"
        "v_mfma_f32_16x16x32_bf16 a[216:219], v[20:23], v[88:91], a[216:219]\n"
        "ds_read_b64_tr_b16 v[86:87], v229 offset:24576\n"
        "v_cvt_pk_bf16_f32 v195, 0, v195\n"
        "v_mfma_f32_16x16x32_bf16 a[248:251], v[28:31], v[88:91], a[248:251]\n"
        "ds_read_b64_tr_b16 v[88:89], v230 offset:24576\n"
        "v_add_f32 v194, v194, v195\n"
        "v_mfma_f32_16x16x32_bf16 a[248:251], v[24:27], v[56:59], a[248:251]\n"
        "ds_read_b64_tr_b16 v[56:57], v230 offset:16384\n"
        "v_add_f32 v190, v190, v194\n"
        "v_mfma_f32_16x16x32_bf16 a[252:255], v[24:27], v[60:63], a[252:255]\n"
        "ds_read_b64_tr_b16 v[58:59], v231 offset:16384\n"
        "global_load_lds_dwordx4 v238, s[68:69] offset:0\n"
        "v_mfma_f32_16x16x32_bf16 a[252:255], v[28:31], v[92:95], a[252:255]\n"
        "ds_read_b64_tr_b16 v[90:91], v231 offset:24576\n"
        "v_lshlrev_b32 v192, 16, v144\n"
        "v_mfma_f32_16x16x32_bf16 a[220:223], v[20:23], v[92:95], a[220:223]\n"
        "ds_read_b64_tr_b16 v[92:93], v232 offset:24576\n"
        "v_and_b32 v193, 0xffff0000, v144\n"
        "v_mfma_f32_16x16x32_bf16 a[220:223], v[16:19], v[60:63], a[220:223]\n"
        "ds_read_b64_tr_b16 v[60:61], v232 offset:16384\n"
        "s_waitcnt lgkmcnt(8)\n"
        "v_mfma_f32_16x16x32_bf16 a[0:3], v[32:35], v[0:3], a[0:3]\n"
        "ds_read_b64_tr_b16 v[62:63], v233 offset:16384\n"
        "global_load_lds_dwordx4 v239, s[68:69] offset:1024\n"
        "v_mfma_f32_16x16x32_bf16 a[0:3], v[64:67], v[4:7], a[0:3]\n"
        "ds_read_b64_tr_b16 v[94:95], v233 offset:24576\n"
        "v_mul_f32 v192, v192, v178\n"
        "v_mfma_f32_16x16x32_bf16 a[32:35], v[64:67], v[12:15], a[32:35]\n"
        "ds_read_b128 v[16:19], v224 offset:4096\n"
        "v_mul_f32 v193, v193, v179\n"
        "v_mfma_f32_16x16x32_bf16 a[32:35], v[32:35], v[8:11], a[32:35]\n"
        "ds_read_b128 v[20:23], v225 offset:4096\n"
        "v_cvt_pk_bf16_f32 v192, 0, v192\n"
        "v_mfma_f32_16x16x32_bf16 a[36:39], v[36:39], v[8:11], a[36:39]\n"
        "ds_read_b128 v[24:27], v224 offset:6144\n"
        "global_load_lds_dwordx4 v240, s[68:69] offset:2048\n"
        "v_mfma_f32_16x16x32_bf16 a[36:39], v[68:71], v[12:15], a[36:39]\n"
        "ds_read_b128 v[28:31], v225 offset:6144\n"
        "v_cvt_pk_bf16_f32 v193, 0, v193\n"
        "v_mfma_f32_16x16x32_bf16 a[4:7], v[68:71], v[4:7], a[4:7]\n"
        "v_add_f32 v192, v192, v193\n"
        "v_add_f32 v190, v190, v192\n"
        "v_mfma_f32_16x16x32_bf16 a[4:7], v[36:39], v[0:3], a[4:7]\n"
        "v_lshlrev_b32 v194, 16, v145\n"
        "v_and_b32 v195, 0xffff0000, v145\n"
        "v_mfma_f32_16x16x32_bf16 a[8:11], v[40:43], v[0:3], a[8:11]\n"
        "global_load_dword v253, v173, s[82:83] offset:0\n"
        "v_mfma_f32_16x16x32_bf16 a[8:11], v[72:75], v[4:7], a[8:11]\n"
        "global_load_lds_dwordx4 v241, s[68:69] offset:3072\n"
        "s_add_u32 s68, s68, 0x80000\n"
        "s_addc_u32 s69, s69, 0\n"
        "v_mfma_f32_16x16x32_bf16 a[40:43], v[72:75], v[12:15], a[40:43]\n"
        "s_add_u32 m0, s73, 0x24000\n"
        "s_add_u32 s82, s12, 0x700000\n"
        "s_addc_u32 s83, s13, 0\n"
        "v_mfma_f32_16x16x32_bf16 a[40:43], v[40:43], v[8:11], a[40:43]\n"
        "s_waitcnt lgkmcnt(2)\n"
        "v_mul_f32 v194, v194, v180\n"
        "v_mul_f32 v195, v195, v181\n"
        "v_mfma_f32_16x16x32_bf16 a[44:47], v[44:47], v[8:11], a[44:47]\n"
        "v_cvt_pk_bf16_f32 v194, 0, v194\n"
        "v_cvt_pk_bf16_f32 v195, 0, v195\n"
        "v_mfma_f32_16x16x32_bf16 a[44:47], v[76:79], v[12:15], a[44:47]\n"
        "global_load_lds_dwordx4 v238, s[68:69] offset:0\n"
        "v_add_f32 v194, v194, v195\n"
        "v_mfma_f32_16x16x32_bf16 a[12:15], v[76:79], v[4:7], a[12:15]\n"
        "v_add_f32 v190, v190, v194\n"
        "v_lshlrev_b32 v192, 16, v146\n"
        "v_mfma_f32_16x16x32_bf16 a[12:15], v[44:47], v[0:3], a[12:15]\n"
        "v_and_b32 v193, 0xffff0000, v146\n"
        "v_mul_f32 v192, v192, v182\n"
        "v_mfma_f32_16x16x32_bf16 a[16:19], v[0:3], v[48:51], a[16:19]\n"
        "v_mul_f32 v193, v193, v183\n"
        "v_cvt_pk_bf16_f32 v192, 0, v192\n"
        "v_mfma_f32_16x16x32_bf16 a[16:19], v[4:7], v[80:83], a[16:19]\n"
        "global_load_lds_dwordx4 v239, s[68:69] offset:1024\n"
        "v_cvt_pk_bf16_f32 v193, 0, v193\n"
        "v_mfma_f32_16x16x32_bf16 a[48:51], v[12:15], v[80:83], a[48:51]\n"
        "v_add_f32 v192, v192, v193\n"
        "v_add_f32 v190, v190, v192\n"
        "v_mfma_f32_16x16x32_bf16 a[48:51], v[8:11], v[48:51], a[48:51]\n"
        "v_lshlrev_b32 v194, 16, v147\n"
        "v_and_b32 v195, 0xffff0000, v147\n"
        "v_mfma_f32_16x16x32_bf16 a[52:55], v[8:11], v[52:55], a[52:55]\n"
        "v_mul_f32 v194, v194, v184\n"
        "v_mul_f32 v195, v195, v185\n"
        "v_mfma_f32_16x16x32_bf16 a[52:55], v[12:15], v[84:87], a[52:55]\n"
        "global_load_lds_dwordx4 v240, s[68:69] offset:2048\n"
        "v_cvt_pk_bf16_f32 v194, 0, v194\n"
        "v_mfma_f32_16x16x32_bf16 a[20:23], v[4:7], v[84:87], a[20:23]\n"
        "v_cvt_pk_bf16_f32 v195, 0, v195\n"
        "v_add_f32 v194, v194, v195\n"
        "v_mfma_f32_16x16x32_bf16 a[20:23], v[0:3], v[52:55], a[20:23]\n"
        "s_waitcnt lgkmcnt(0)\n"
        "v_add_f32 v190, v190, v194\n"
        "v_lshlrev_b32 v192, 16, v148\n"
        "v_mfma_f32_16x16x32_bf16 a[24:27], v[0:3], v[56:59], a[24:27]\n"
        "s_and_b32 s53, s40, 1\n"
        "v_and_b32 v193, 0xffff0000, v148\n"
        "v_mul_f32 v192, v192, v186\n"
        "v_mfma_f32_16x16x32_bf16 a[24:27], v[4:7], v[88:91], a[24:27]\n"
        "global_load_lds_dwordx4 v241, s[68:69] offset:3072\n"
        "s_add_u32 s68, s68, 0x80000\n"
        "s_addc_u32 s69, s69, 0\n"
        "v_mfma_f32_16x16x32_bf16 a[56:59], v[12:15], v[88:91], a[56:59]\n"
        "s_add_u32 m0, s72, 0x0\n"
        "v_mul_f32 v193, v193, v187\n"
        "v_cvt_pk_bf16_f32 v192, 0, v192\n"
        "v_mfma_f32_16x16x32_bf16 a[56:59], v[8:11], v[56:59], a[56:59]\n"
        "s_lshl_b32 s53, s53, 10\n"
        "v_cvt_pk_bf16_f32 v193, 0, v193\n"
        "v_add_f32 v192, v192, v193\n"
        "v_mfma_f32_16x16x32_bf16 a[60:63], v[8:11], v[60:63], a[60:63]\n"
        "ds_read_b128 v[8:11], v224 offset:10240\n"
        "s_sub_u32 s53, 0x8000, s53\n"
        "v_add_f32 v190, v190, v192\n"
        "v_mfma_f32_16x16x32_bf16 a[60:63], v[12:15], v[92:95], a[60:63]\n"
        "ds_read_b128 v[12:15], v225 offset:10240\n"
        "v_lshlrev_b32 v194, 16, v149\n"
        "v_mfma_f32_16x16x32_bf16 a[28:31], v[4:7], v[92:95], a[28:31]\n"
        "ds_read_b128 v[4:7], v225 offset:8192\n"
        "v_and_b32 v195, 0xffff0000, v149\n"
        "v_mfma_f32_16x16x32_bf16 a[28:31], v[0:3], v[60:63], a[28:31]\n"
        "ds_read_b128 v[0:3], v224 offset:8192\n"
        "v_mul_f32 v194, v194, v188\n"
        "v_mfma_f32_16x16x32_bf16 a[64:67], v[32:35], v[16:19], a[64:67]\n"
        "v_mul_f32 v195, v195, v189\n"
        "v_cvt_pk_bf16_f32 v194, 0, v194\n"
        "v_mfma_f32_16x16x32_bf16 a[64:67], v[64:67], v[20:23], a[64:67]\n"
        "v_cvt_pk_bf16_f32 v195, 0, v195\n"
        "v_add_f32 v194, v194, v195\n"
        "v_mfma_f32_16x16x32_bf16 a[96:99], v[64:67], v[28:31], a[96:99]\n"
        "v_add_f32 v190, v190, v194\n"
        "v_lshlrev_b32 v192, 16, v150\n"
        "v_mfma_f32_16x16x32_bf16 a[96:99], v[32:35], v[24:27], a[96:99]\n"
        "v_and_b32 v193, 0xffff0000, v150\n"
        "v_mul_f32 v192, v192, v174\n"
        "v_mfma_f32_16x16x32_bf16 a[100:103], v[36:39], v[24:27], a[100:103]\n"
        "v_mul_f32 v193, v193, v175\n"
        "v_cvt_pk_bf16_f32 v192, 0, v192\n"
        "v_mfma_f32_16x16x32_bf16 a[100:103], v[68:71], v[28:31], a[100:103]\n"
        "v_cvt_pk_bf16_f32 v193, 0, v193\n"
        "v_add_f32 v191, v192, v193\n"
        "v_mfma_f32_16x16x32_bf16 a[68:71], v[68:71], v[20:23], a[68:71]\n"
        "v_lshlrev_b32 v194, 16, v151\n"
        "v_and_b32 v195, 0xffff0000, v151\n"
        "v_mfma_f32_16x16x32_bf16 a[68:71], v[36:39], v[16:19], a[68:71]\n"
        "v_mul_f32 v194, v194, v176\n"
        "v_mul_f32 v195, v195, v177\n"
        "v_mfma_f32_16x16x32_bf16 a[72:75], v[40:43], v[16:19], a[72:75]\n"
        "v_cvt_pk_bf16_f32 v194, 0, v194\n"
        "v_cvt_pk_bf16_f32 v195, 0, v195\n"
        "v_mfma_f32_16x16x32_bf16 a[72:75], v[72:75], v[20:23], a[72:75]\n"
        "v_add_f32 v194, v194, v195\n"
        "v_add_f32 v191, v191, v194\n"
        "v_mfma_f32_16x16x32_bf16 a[104:107], v[72:75], v[28:31], a[104:107]\n"
        "v_lshlrev_b32 v192, 16, v152\n"
        "v_and_b32 v193, 0xffff0000, v152\n"
        "v_mfma_f32_16x16x32_bf16 a[104:107], v[40:43], v[24:27], a[104:107]\n"
        "v_mul_f32 v192, v192, v178\n"
        "v_mul_f32 v193, v193, v179\n"
        "v_mfma_f32_16x16x32_bf16 a[108:111], v[44:47], v[24:27], a[108:111]\n"
        "v_cvt_pk_bf16_f32 v192, 0, v192\n"
        "v_cvt_pk_bf16_f32 v193, 0, v193\n"
        "v_mfma_f32_16x16x32_bf16 a[108:111], v[76:79], v[28:31], a[108:111]\n"
        "v_add_f32 v192, v192, v193\n"
        "v_add_f32 v191, v191, v192\n"
        "v_mfma_f32_16x16x32_bf16 a[76:79], v[76:79], v[20:23], a[76:79]\n"
        "v_lshlrev_b32 v194, 16, v153\n"
        "v_and_b32 v195, 0xffff0000, v153\n"
        "v_mfma_f32_16x16x32_bf16 a[76:79], v[44:47], v[16:19], a[76:79]\n"
        "v_mul_f32 v194, v194, v180\n"
        "v_mul_f32 v195, v195, v181\n"
        "v_mfma_f32_16x16x32_bf16 a[80:83], v[16:19], v[48:51], a[80:83]\n"
        "v_cvt_pk_bf16_f32 v194, 0, v194\n"
        "v_cvt_pk_bf16_f32 v195, 0, v195\n"
        "v_mfma_f32_16x16x32_bf16 a[80:83], v[20:23], v[80:83], a[80:83]\n"
        "v_add_f32 v194, v194, v195\n"
        "v_add_f32 v191, v191, v194\n"
        "v_mfma_f32_16x16x32_bf16 a[112:115], v[28:31], v[80:83], a[112:115]\n"
        "v_lshlrev_b32 v192, 16, v154\n"
        "v_and_b32 v193, 0xffff0000, v154\n"
        "v_mfma_f32_16x16x32_bf16 a[112:115], v[24:27], v[48:51], a[112:115]\n"
        "v_mul_f32 v192, v192, v182\n"
        "v_mul_f32 v193, v193, v183\n"
        "v_mfma_f32_16x16x32_bf16 a[116:119], v[24:27], v[52:55], a[116:119]\n"
        "v_cvt_pk_bf16_f32 v192, 0, v192\n"
        "v_cvt_pk_bf16_f32 v193, 0, v193\n"
        "v_mfma_f32_16x16x32_bf16 a[116:119], v[28:31], v[84:87], a[116:119]\n"
        "s_waitcnt lgkmcnt(0)\n"
        "v_add_f32 v192, v192, v193\n"
        "v_add_f32 v191, v191, v192\n"
        "v_mfma_f32_16x16x32_bf16 a[84:87], v[20:23], v[84:87], a[84:87]\n"
        "v_lshlrev_b32 v194, 16, v155\n"
        "v_and_b32 v195, 0xffff0000, v155\n"
        "v_mfma_f32_16x16x32_bf16 a[84:87], v[16:19], v[52:55], a[84:87]\n"
        "s_mov_b32 s88, 0x3e0293ee\n"
        "v_mul_f32 v194, v194, v184\n"
        "v_mul_f32 v195, v195, v185\n"
        "v_mfma_f32_16x16x32_bf16 a[88:91], v[16:19], v[56:59], a[88:91]\n"
        "s_cmp_ge_u32 s40, 2\n"
        "s_cselect_b32 s50, s45, s44\n"
        "v_cvt_pk_bf16_f32 v194, 0, v194\n"
        "v_mfma_f32_16x16x32_bf16 a[88:91], v[20:23], v[88:91], a[88:91]\n"
        "s_lshl_b32 s50, s50, 15\n"
        "v_cvt_pk_bf16_f32 v195, 0, v195\n"
        "v_add_f32 v194, v194, v195\n"
        "v_mfma_f32_16x16x32_bf16 a[120:123], v[28:31], v[88:91], a[120:123]\n"
        "s_lshl_b32 s51, s43, 19\n"
        "v_add_f32 v191, v191, v194\n"
        "v_lshlrev_b32 v192, 16, v156\n"
        "v_mfma_f32_16x16x32_bf16 a[120:123], v[24:27], v[56:59], a[120:123]\n"
        "s_add_u32 s50, s50, s51\n"
        "v_and_b32 v193, 0xffff0000, v156\n"
        "v_mul_f32 v192, v192, v186\n"
        "v_mfma_f32_16x16x32_bf16 a[124:127], v[24:27], v[60:63], a[124:127]\n"
        "ds_read_b128 v[24:27], v224 offset:14336\n"
        "s_and_b32 s51, s40, 1\n"
        "v_mul_f32 v193, v193, v187\n"
        "v_mfma_f32_16x16x32_bf16 a[124:127], v[28:31], v[92:95], a[124:127]\n"
        "ds_read_b128 v[28:31], v225 offset:14336\n"
        "s_lshl_b32 s51, s51, 11\n"
        "v_cvt_pk_bf16_f32 v192, 0, v192\n"
        "v_mfma_f32_16x16x32_bf16 a[92:95], v[20:23], v[92:95], a[92:95]\n"
        "ds_read_b128 v[20:23], v225 offset:12288\n"
        "s_add_u32 s52, s50, s51\n"
        "v_cvt_pk_bf16_f32 v193, 0, v193\n"
        "v_mfma_f32_16x16x32_bf16 a[92:95], v[16:19], v[60:63], a[92:95]\n"
        "ds_read_b128 v[16:19], v224 offset:12288\n"
        "v_add_f32 v192, v192, v193\n"
        "v_mfma_f32_16x16x32_bf16 a[128:131], v[32:35], v[0:3], a[128:131]\n"
        "v_add_f32 v191, v191, v192\n"
        "v_lshlrev_b32 v194, 16, v157\n"
        "v_mfma_f32_16x16x32_bf16 a[128:131], v[64:67], v[4:7], a[128:131]\n"
        "v_and_b32 v195, 0xffff0000, v157\n"
        "v_mul_f32 v194, v194, v188\n"
        "v_mfma_f32_16x16x32_bf16 a[160:163], v[64:67], v[12:15], a[160:163]\n"
        "v_mul_f32 v195, v195, v189\n"
        "v_cvt_pk_bf16_f32 v194, 0, v194\n"
        "v_mfma_f32_16x16x32_bf16 a[160:163], v[32:35], v[8:11], a[160:163]\n"
        "v_cvt_pk_bf16_f32 v195, 0, v195\n"
        "v_add_f32 v194, v194, v195\n"
        "v_mfma_f32_16x16x32_bf16 a[164:167], v[36:39], v[8:11], a[164:167]\n"
        "v_add_f32 v191, v191, v194\n"
        "v_add_f32_dpp v190, v190, v190 row_shr:1 row_mask:0xf bank_mask:0xf bound_ctrl:1\n"
        "v_mfma_f32_16x16x32_bf16 a[164:167], v[68:71], v[12:15], a[164:167]\n"
        "v_add_f32_dpp v191, v191, v191 row_shr:1 row_mask:0xf bank_mask:0xf bound_ctrl:1\n"
        "v_add_f32_dpp v190, v190, v190 row_shr:2 row_mask:0xf bank_mask:0xf bound_ctrl:1\n"
        "v_mfma_f32_16x16x32_bf16 a[132:135], v[68:71], v[4:7], a[132:135]\n"
        "v_add_f32_dpp v191, v191, v191 row_shr:2 row_mask:0xf bank_mask:0xf bound_ctrl:1\n"
        "v_add_f32_dpp v190, v190, v190 row_shr:4 row_mask:0xf bank_mask:0xf bound_ctrl:1\n"
        "v_mfma_f32_16x16x32_bf16 a[132:135], v[36:39], v[0:3], a[132:135]\n"
        "v_add_f32_dpp v191, v191, v191 row_shr:4 row_mask:0xf bank_mask:0xf bound_ctrl:1\n"
        "v_add_f32_dpp v190, v190, v190 row_shr:8 row_mask:0xf bank_mask:0xf bound_ctrl:1\n"
        "v_mfma_f32_16x16x32_bf16 a[136:139], v[40:43], v[0:3], a[136:139]\n"
        "v_add_f32_dpp v191, v191, v191 row_shr:8 row_mask:0xf bank_mask:0xf bound_ctrl:1\n"
        "v_add_f32_dpp v190, v190, v190 row_bcast:15 row_mask:0xa bank_mask:0xf bound_ctrl:1\n"
        "v_mfma_f32_16x16x32_bf16 a[136:139], v[72:75], v[4:7], a[136:139]\n"
        "v_add_f32_dpp v191, v191, v191 row_bcast:15 row_mask:0xa bank_mask:0xf bound_ctrl:1\n"
        "v_add_f32_dpp v190, v190, v190 row_bcast:31 row_mask:0xc bank_mask:0xf bound_ctrl:1\n"
        "v_mfma_f32_16x16x32_bf16 a[168:171], v[72:75], v[12:15], a[168:171]\n"
        "v_add_f32_dpp v191, v191, v191 row_bcast:31 row_mask:0xc bank_mask:0xf bound_ctrl:1\n"
        "v_cvt_pk_bf16_f32 v190, 0, v190\n"
        "v_mfma_f32_16x16x32_bf16 a[168:171], v[40:43], v[8:11], a[168:171]\n"
        "v_mul_f32 v190, 0xbfb8aa3b, v190\n"
        "v_lshrrev_b32 v192, 2, v254\n"
        "v_mfma_f32_16x16x32_bf16 a[172:175], v[44:47], v[8:11], a[172:175]\n"
        "LHA4_%=:\n"
        "s_cbranch_execz LHA8_%=\n"
        "v_exp_f32 v190, v190\n"
        "v_mfma_f32_16x16x32_bf16 a[172:175], v[76:79], v[12:15], a[172:175]\n"
        "v_add_f32 v190, 1.0, v190\n"
        "v_add_u32 v192, s80, v192\n"
        "v_mfma_f32_16x16x32_bf16 a[140:143], v[76:79], v[4:7], a[140:143]\n"
        "v_rcp_f32 v190, v190\n"
        "v_mfma_f32_16x16x32_bf16 a[140:143], v[44:47], v[0:3], a[140:143]\n"
        "v_cvt_pk_bf16_f32 v190, 0, v190\n"
        "v_cvt_pk_bf16_f32 v191, 0, v191\n"
        "v_mfma_f32_16x16x32_bf16 a[144:147], v[0:3], v[48:51], a[144:147]\n"
        "v_readlane_b32 s84, v190, 63\n"
        "v_mul_f32 v191, 0xbfb8aa3b, v191\n"
        "v_mfma_f32_16x16x32_bf16 a[144:147], v[4:7], v[80:83], a[144:147]\n"
        "v_mov_b32 v190, s84\n"
        "v_lshrrev_b32 v193, 2, v254\n"
        "v_mfma_f32_16x16x32_bf16 a[176:179], v[12:15], v[80:83], a[176:179]\n"
        "s_mov_b64 exec, 0xffff\n"
        "global_store_dword v192, v190, s[76:77] sc1\n"
        "s_mov_b64 exec, -1\n"
        "v_mfma_f32_16x16x32_bf16 a[176:179], v[8:11], v[48:51], a[176:179]\n"
        "v_exp_f32 v191, v191\n"
        "v_mfma_f32_16x16x32_bf16 a[180:183], v[8:11], v[52:55], a[180:183]\n"
        "v_add_f32 v191, 1.0, v191\n"
        "v_add_u32 v193, s81, v193\n"
        "v_mfma_f32_16x16x32_bf16 a[180:183], v[12:15], v[84:87], a[180:183]\n"
        "s_waitcnt lgkmcnt(0)\n"
        "v_rcp_f32 v191, v191\n"
        "v_mfma_f32_16x16x32_bf16 a[148:151], v[4:7], v[84:87], a[148:151]\n"
        "v_cvt_pk_bf16_f32 v191, 0, v191\n"
        "v_mfma_f32_16x16x32_bf16 a[148:151], v[0:3], v[52:55], a[148:151]\n"
        "global_load_dword v253, v173, s[82:83] offset:0\n"
        "v_readlane_b32 s85, v191, 63\n"
        "v_mfma_f32_16x16x32_bf16 a[152:155], v[0:3], v[56:59], a[152:155]\n"
        "v_mfma_f32_16x16x32_bf16 a[152:155], v[4:7], v[88:91], a[152:155]\n"
        "v_mov_b32 v191, s85\n"
        "v_mfma_f32_16x16x32_bf16 a[184:187], v[12:15], v[88:91], a[184:187]\n"
        "s_mov_b64 exec, 0xffff\n"
        "global_store_dword v193, v191, s[76:77] sc1\n"
        "s_mov_b64 exec, -1\n"
        "v_mfma_f32_16x16x32_bf16 a[184:187], v[8:11], v[56:59], a[184:187]\n"
        "s_waitcnt vmcnt(12)\n"
        "s_barrier\n"
        "v_mfma_f32_16x16x32_bf16 a[188:191], v[8:11], v[60:63], a[188:191]\n"
        "global_load_lds_dwordx4 v242, s[64:65] offset:0\n"
        "ds_read_b128 v[8:11], v224 offset:34816\n"
        "v_mfma_f32_16x16x32_bf16 a[188:191], v[12:15], v[92:95], a[188:191]\n"
        "ds_read_b128 v[12:15], v225 offset:34816\n"
        "v_mfma_f32_16x16x32_bf16 a[156:159], v[4:7], v[92:95], a[156:159]\n"
        "ds_read_b128 v[4:7], v225 offset:32768\n"
        "v_mfma_f32_16x16x32_bf16 a[156:159], v[0:3], v[60:63], a[156:159]\n"
        "ds_read_b128 v[0:3], v224 offset:32768\n"
        "v_mfma_f32_16x16x32_bf16 a[192:195], v[32:35], v[16:19], a[192:195]\n"
        "global_load_lds_dwordx4 v243, s[64:65] offset:1024\n"
        "v_mfma_f32_16x16x32_bf16 a[192:195], v[64:67], v[20:23], a[192:195]\n"
        "v_mfma_f32_16x16x32_bf16 a[224:227], v[64:67], v[28:31], a[224:227]\n"
        "ds_read_b64_tr_b16 v[64:65], v226 offset:40960\n"
        "v_mfma_f32_16x16x32_bf16 a[224:227], v[32:35], v[24:27], a[224:227]\n"
        "ds_read_b64_tr_b16 v[32:33], v226 offset:32768\n"
        "s_add_u32 s97, s52, 0x7000\n"
        "v_mfma_f32_16x16x32_bf16 a[228:231], v[36:39], v[24:27], a[228:231]\n"
        "ds_read_b64_tr_b16 v[34:35], v227 offset:32768\n"
        "global_load_lds_dwordx4 v244, s[64:65] offset:2048\n"
        "v_mfma_f32_16x16x32_bf16 a[228:231], v[68:71], v[28:31], a[228:231]\n"
        "ds_read_b64_tr_b16 v[66:67], v227 offset:40960\n"
        "s_add_u32 s70, s38, s97\n"
        "s_addc_u32 s71, s39, 0\n"
        "v_mfma_f32_16x16x32_bf16 a[196:199], v[68:71], v[20:23], a[196:199]\n"
        "ds_read_b64_tr_b16 v[68:69], v228 offset:40960\n"
        "v_mfma_f32_16x16x32_bf16 a[196:199], v[36:39], v[16:19], a[196:199]\n"
        "ds_read_b64_tr_b16 v[36:37], v228 offset:32768\n"
        "v_mfma_f32_16x16x32_bf16 a[200:203], v[40:43], v[16:19], a[200:203]\n"
        "ds_read_b64_tr_b16 v[38:39], v229 offset:32768\n"
        "global_load_lds_dwordx4 v245, s[64:65] offset:3072\n"
        "s_add_u32 m0, s72, 0x1000\n"
        "v_mfma_f32_16x16x32_bf16 a[200:203], v[72:75], v[20:23], a[200:203]\n"
        "ds_read_b64_tr_b16 v[70:71], v229 offset:40960\n"
        "s_add_u32 s64, s64, 0x80\n"
        "s_addc_u32 s65, s65, 0\n"
        "v_mfma_f32_16x16x32_bf16 a[232:235], v[72:75], v[28:31], a[232:235]\n"
        "ds_read_b64_tr_b16 v[72:73], v230 offset:40960\n"
        "v_mfma_f32_16x16x32_bf16 a[232:235], v[40:43], v[24:27], a[232:235]\n"
        "ds_read_b64_tr_b16 v[40:41], v230 offset:32768\n"
        "v_mfma_f32_16x16x32_bf16 a[236:239], v[44:47], v[24:27], a[236:239]\n"
        "ds_read_b64_tr_b16 v[42:43], v231 offset:32768\n"
        "global_load_lds_dwordx4 v242, s[66:67] offset:0\n"
        "v_mfma_f32_16x16x32_bf16 a[236:239], v[76:79], v[28:31], a[236:239]\n"
        "ds_read_b64_tr_b16 v[74:75], v231 offset:40960\n"
        "v_mfma_f32_16x16x32_bf16 a[204:207], v[76:79], v[20:23], a[204:207]\n"
        "ds_read_b64_tr_b16 v[76:77], v232 offset:40960\n"
        "v_mfma_f32_16x16x32_bf16 a[204:207], v[44:47], v[16:19], a[204:207]\n"
        "ds_read_b64_tr_b16 v[44:45], v232 offset:32768\n"
        "v_mfma_f32_16x16x32_bf16 a[208:211], v[16:19], v[48:51], a[208:211]\n"
        "ds_read_b64_tr_b16 v[46:47], v233 offset:32768\n"
        "global_load_lds_dwordx4 v243, s[66:67] offset:1024\n"
        "v_mfma_f32_16x16x32_bf16 a[208:211], v[20:23], v[80:83], a[208:211]\n"
        "ds_read_b64_tr_b16 v[78:79], v233 offset:40960\n"
        "v_mfma_f32_16x16x32_bf16 a[240:243], v[28:31], v[80:83], a[240:243]\n"
        "ds_read_b64_tr_b16 v[80:81], v226 offset:57344\n"
        "v_mfma_f32_16x16x32_bf16 a[240:243], v[24:27], v[48:51], a[240:243]\n"
        "ds_read_b64_tr_b16 v[48:49], v226 offset:49152\n"
        "v_add_u32 v226, s53, v226\n"
        "v_mfma_f32_16x16x32_bf16 a[244:247], v[24:27], v[52:55], a[244:247]\n"
        "ds_read_b64_tr_b16 v[50:51], v227 offset:49152\n"
        "global_load_lds_dwordx4 v244, s[66:67] offset:2048\n"
        "v_mfma_f32_16x16x32_bf16 a[244:247], v[28:31], v[84:87], a[244:247]\n"
        "ds_read_b64_tr_b16 v[82:83], v227 offset:57344\n"
        "v_add_u32 v227, s53, v227\n"
        "v_mfma_f32_16x16x32_bf16 a[212:215], v[20:23], v[84:87], a[212:215]\n"
        "ds_read_b64_tr_b16 v[84:85], v228 offset:57344\n"
        "s_waitcnt lgkmcnt(8)\n"
        "v_mfma_f32_16x16x32_bf16 a[212:215], v[16:19], v[52:55], a[212:215]\n"
        "ds_read_b64_tr_b16 v[52:53], v228 offset:49152\n"
        "v_add_u32 v228, s53, v228\n"
        "v_mfma_f32_16x16x32_bf16 a[216:219], v[16:19], v[56:59], a[216:219]\n"
        "ds_read_b64_tr_b16 v[54:55], v229 offset:49152\n"
        "global_load_lds_dwordx4 v245, s[66:67] offset:3072\n"
        "s_add_u32 m0, s72, 0x10000\n"
        "v_mfma_f32_16x16x32_bf16 a[216:219], v[20:23], v[88:91], a[216:219]\n"
        "ds_read_b64_tr_b16 v[86:87], v229 offset:57344\n"
        "s_add_u32 s66, s66, 0x80\n"
        "s_addc_u32 s67, s67, 0\n"
        "v_mfma_f32_16x16x32_bf16 a[248:251], v[28:31], v[88:91], a[248:251]\n"
        "ds_read_b64_tr_b16 v[88:89], v230 offset:57344\n"
        "v_add_u32 v229, s53, v229\n"
        "v_mfma_f32_16x16x32_bf16 a[248:251], v[24:27], v[56:59], a[248:251]\n"
        "ds_read_b64_tr_b16 v[56:57], v230 offset:49152\n"
        "v_add_u32 v230, s53, v230\n"
        "v_mfma_f32_16x16x32_bf16 a[252:255], v[24:27], v[60:63], a[252:255]\n"
        "ds_read_b64_tr_b16 v[58:59], v231 offset:49152\n"
        "global_load_lds_dwordx4 v242, s[64:65] offset:0\n"
        "v_mfma_f32_16x16x32_bf16 a[252:255], v[28:31], v[92:95], a[252:255]\n"
        "ds_read_b64_tr_b16 v[90:91], v231 offset:57344\n"
        "v_add_u32 v231, s53, v231\n"
        "v_mfma_f32_16x16x32_bf16 a[220:223], v[20:23], v[92:95], a[220:223]\n"
        "ds_read_b64_tr_b16 v[92:93], v232 offset:57344\n"
        "v_mfma_f32_16x16x32_bf16 a[220:223], v[16:19], v[60:63], a[220:223]\n"
        "ds_read_b64_tr_b16 v[60:61], v232 offset:49152\n"
        "v_add_u32 v232, s53, v232\n"
        "v_mfma_f32_16x16x32_bf16 a[0:3], v[32:35], v[0:3], a[0:3]\n"
        "ds_read_b64_tr_b16 v[62:63], v233 offset:49152\n"
        "global_load_lds_dwordx4 v243, s[64:65] offset:1024\n"
        "v_mfma_f32_16x16x32_bf16 a[0:3], v[64:67], v[4:7], a[0:3]\n"
        "ds_read_b64_tr_b16 v[94:95], v233 offset:57344\n"
        "v_add_u32 v233, s53, v233\n"
        "v_mfma_f32_16x16x32_bf16 a[32:35], v[64:67], v[12:15], a[32:35]\n"
        "ds_read_b128 v[16:19], v224 offset:36864\n"
        "s_add_u32 s53, s52, s51\n"
        "s_add_u32 s53, s53, 0x1000000\n"
        "v_mfma_f32_16x16x32_bf16 a[32:35], v[32:35], v[8:11], a[32:35]\n"
        "ds_read_b128 v[20:23], v225 offset:36864\n"
        "s_waitcnt lgkmcnt(8)\n"
        "v_mfma_f32_16x16x32_bf16 a[36:39], v[36:39], v[8:11], a[36:39]\n"
        "ds_read_b128 v[24:27], v224 offset:38912\n"
        "global_load_lds_dwordx4 v244, s[64:65] offset:2048\n"
        "v_mfma_f32_16x16x32_bf16 a[36:39], v[68:71], v[12:15], a[36:39]\n"
        "ds_read_b128 v[28:31], v225 offset:38912\n"
        "v_mfma_f32_16x16x32_bf16 a[4:7], v[68:71], v[4:7], a[4:7]\n"
        "v_mfma_f32_16x16x32_bf16 a[4:7], v[36:39], v[0:3], a[4:7]\n"
        "v_mfma_f32_16x16x32_bf16 a[8:11], v[40:43], v[0:3], a[8:11]\n"
        "global_load_lds_dwordx4 v245, s[64:65] offset:3072\n"
        "s_add_u32 m0, s72, 0x11000\n"
        "v_mfma_f32_16x16x32_bf16 a[8:11], v[72:75], v[4:7], a[8:11]\n"
        "s_add_u32 s64, s64, 0x80\n"
        "s_addc_u32 s65, s65, 0\n"
        "v_mfma_f32_16x16x32_bf16 a[40:43], v[72:75], v[12:15], a[40:43]\n"
        "v_mfma_f32_16x16x32_bf16 a[40:43], v[40:43], v[8:11], a[40:43]\n"
        "v_mfma_f32_16x16x32_bf16 a[44:47], v[44:47], v[8:11], a[44:47]\n"
        "global_load_lds_dwordx4 v242, s[66:67] offset:0\n"
        "v_mfma_f32_16x16x32_bf16 a[44:47], v[76:79], v[12:15], a[44:47]\n"
        "s_waitcnt lgkmcnt(0)\n"
        "v_mfma_f32_16x16x32_bf16 a[12:15], v[76:79], v[4:7], a[12:15]\n"
        "v_mfma_f32_16x16x32_bf16 a[12:15], v[44:47], v[0:3], a[12:15]\n"
        "v_mfma_f32_16x16x32_bf16 a[16:19], v[0:3], v[48:51], a[16:19]\n"
        "global_load_lds_dwordx4 v243, s[66:67] offset:1024\n"
        "v_mfma_f32_16x16x32_bf16 a[16:19], v[4:7], v[80:83], a[16:19]\n"
        "v_mfma_f32_16x16x32_bf16 a[48:51], v[12:15], v[80:83], a[48:51]\n"
        "v_mfma_f32_16x16x32_bf16 a[48:51], v[8:11], v[48:51], a[48:51]\n"
        "v_mfma_f32_16x16x32_bf16 a[52:55], v[8:11], v[52:55], a[52:55]\n"
        "global_load_lds_dwordx4 v244, s[66:67] offset:2048\n"
        "v_mfma_f32_16x16x32_bf16 a[52:55], v[12:15], v[84:87], a[52:55]\n"
        "v_mfma_f32_16x16x32_bf16 a[20:23], v[4:7], v[84:87], a[20:23]\n"
        "v_mfma_f32_16x16x32_bf16 a[20:23], v[0:3], v[52:55], a[20:23]\n"
        "v_mfma_f32_16x16x32_bf16 a[24:27], v[0:3], v[56:59], a[24:27]\n"
        "global_load_lds_dwordx4 v245, s[66:67] offset:3072\n"
        "s_add_u32 m0, s73, 0x1c000\n"
        "v_mfma_f32_16x16x32_bf16 a[24:27], v[4:7], v[88:91], a[24:27]\n"
        "s_add_u32 s66, s66, 0x80\n"
        "s_addc_u32 s67, s67, 0\n"
        "v_mfma_f32_16x16x32_bf16 a[56:59], v[12:15], v[88:91], a[56:59]\n"
        "v_mfma_f32_16x16x32_bf16 a[56:59], v[8:11], v[56:59], a[56:59]\n"
        "v_mfma_f32_16x16x32_bf16 a[60:63], v[8:11], v[60:63], a[60:63]\n"
        "ds_read_b128 v[8:11], v224 offset:43008\n"
        "v_mfma_f32_16x16x32_bf16 a[60:63], v[12:15], v[92:95], a[60:63]\n"
        "ds_read_b128 v[12:15], v225 offset:43008\n"
        "v_mfma_f32_16x16x32_bf16 a[28:31], v[4:7], v[92:95], a[28:31]\n"
        "ds_read_b128 v[4:7], v225 offset:40960\n"
        "v_mfma_f32_16x16x32_bf16 a[28:31], v[0:3], v[60:63], a[28:31]\n"
        "ds_read_b128 v[0:3], v224 offset:40960\n"
        "v_mfma_f32_16x16x32_bf16 a[64:67], v[32:35], v[16:19], a[64:67]\n"
        "v_mfma_f32_16x16x32_bf16 a[64:67], v[64:67], v[20:23], a[64:67]\n"
        "v_mfma_f32_16x16x32_bf16 a[96:99], v[64:67], v[28:31], a[96:99]\n"
        "v_mfma_f32_16x16x32_bf16 a[96:99], v[32:35], v[24:27], a[96:99]\n"
        "v_mfma_f32_16x16x32_bf16 a[100:103], v[36:39], v[24:27], a[100:103]\n"
        "v_mfma_f32_16x16x32_bf16 a[100:103], v[68:71], v[28:31], a[100:103]\n"
        "v_mfma_f32_16x16x32_bf16 a[68:71], v[68:71], v[20:23], a[68:71]\n"
        "v_mfma_f32_16x16x32_bf16 a[68:71], v[36:39], v[16:19], a[68:71]\n"
        "v_mfma_f32_16x16x32_bf16 a[72:75], v[40:43], v[16:19], a[72:75]\n"
        "v_mfma_f32_16x16x32_bf16 a[72:75], v[72:75], v[20:23], a[72:75]\n"
        "v_mfma_f32_16x16x32_bf16 a[104:107], v[72:75], v[28:31], a[104:107]\n"
        "v_mfma_f32_16x16x32_bf16 a[104:107], v[40:43], v[24:27], a[104:107]\n"
        "v_mfma_f32_16x16x32_bf16 a[108:111], v[44:47], v[24:27], a[108:111]\n"
        "v_mfma_f32_16x16x32_bf16 a[108:111], v[76:79], v[28:31], a[108:111]\n"
        "v_mfma_f32_16x16x32_bf16 a[76:79], v[76:79], v[20:23], a[76:79]\n"
        "v_mfma_f32_16x16x32_bf16 a[76:79], v[44:47], v[16:19], a[76:79]\n"
        "v_mfma_f32_16x16x32_bf16 a[80:83], v[16:19], v[48:51], a[80:83]\n"
        "v_mfma_f32_16x16x32_bf16 a[80:83], v[20:23], v[80:83], a[80:83]\n"
        "v_mfma_f32_16x16x32_bf16 a[112:115], v[28:31], v[80:83], a[112:115]\n"
        "v_mfma_f32_16x16x32_bf16 a[112:115], v[24:27], v[48:51], a[112:115]\n"
        "v_mfma_f32_16x16x32_bf16 a[116:119], v[24:27], v[52:55], a[116:119]\n"
        "v_mfma_f32_16x16x32_bf16 a[116:119], v[28:31], v[84:87], a[116:119]\n"
        "s_waitcnt lgkmcnt(0)\n"
        "v_mfma_f32_16x16x32_bf16 a[84:87], v[20:23], v[84:87], a[84:87]\n"
        "v_mfma_f32_16x16x32_bf16 a[84:87], v[16:19], v[52:55], a[84:87]\n"
        "v_mfma_f32_16x16x32_bf16 a[88:91], v[16:19], v[56:59], a[88:91]\n"
        "v_mfma_f32_16x16x32_bf16 a[88:91], v[20:23], v[88:91], a[88:91]\n"
        "v_mfma_f32_16x16x32_bf16 a[120:123], v[28:31], v[88:91], a[120:123]\n"
        "v_mfma_f32_16x16x32_bf16 a[120:123], v[24:27], v[56:59], a[120:123]\n"
        "v_mfma_f32_16x16x32_bf16 a[124:127], v[24:27], v[60:63], a[124:127]\n"
        "ds_read_b128 v[24:27], v224 offset:47104\n"
        "v_mfma_f32_16x16x32_bf16 a[124:127], v[28:31], v[92:95], a[124:127]\n"
        "ds_read_b128 v[28:31], v225 offset:47104\n"
        "v_mfma_f32_16x16x32_bf16 a[92:95], v[20:23], v[92:95], a[92:95]\n"
        "ds_read_b128 v[20:23], v225 offset:45056\n"
        "v_mfma_f32_16x16x32_bf16 a[92:95], v[16:19], v[60:63], a[92:95]\n"
        "ds_read_b128 v[16:19], v224 offset:45056\n"
        "v_mfma_f32_16x16x32_bf16 a[128:131], v[32:35], v[0:3], a[128:131]\n"
        "v_mfma_f32_16x16x32_bf16 a[128:131], v[64:67], v[4:7], a[128:131]\n"
        "v_mfma_f32_16x16x32_bf16 a[160:163], v[64:67], v[12:15], a[160:163]\n"
        "v_mfma_f32_16x16x32_bf16 a[160:163], v[32:35], v[8:11], a[160:163]\n"
        "v_mfma_f32_16x16x32_bf16 a[164:167], v[36:39], v[8:11], a[164:167]\n"
        "v_mfma_f32_16x16x32_bf16 a[164:167], v[68:71], v[12:15], a[164:167]\n"
        "v_mfma_f32_16x16x32_bf16 a[132:135], v[68:71], v[4:7], a[132:135]\n"
        "v_mfma_f32_16x16x32_bf16 a[132:135], v[36:39], v[0:3], a[132:135]\n"
        "v_mfma_f32_16x16x32_bf16 a[136:139], v[40:43], v[0:3], a[136:139]\n"
        "v_mfma_f32_16x16x32_bf16 a[136:139], v[72:75], v[4:7], a[136:139]\n"
        "v_mfma_f32_16x16x32_bf16 a[168:171], v[72:75], v[12:15], a[168:171]\n"
        "v_mfma_f32_16x16x32_bf16 a[168:171], v[40:43], v[8:11], a[168:171]\n"
        "v_mfma_f32_16x16x32_bf16 a[172:175], v[44:47], v[8:11], a[172:175]\n"
        "v_mfma_f32_16x16x32_bf16 a[172:175], v[76:79], v[12:15], a[172:175]\n"
        "v_mfma_f32_16x16x32_bf16 a[140:143], v[76:79], v[4:7], a[140:143]\n"
        "v_mfma_f32_16x16x32_bf16 a[140:143], v[44:47], v[0:3], a[140:143]\n"
        "v_mfma_f32_16x16x32_bf16 a[144:147], v[0:3], v[48:51], a[144:147]\n"
        "v_mfma_f32_16x16x32_bf16 a[144:147], v[4:7], v[80:83], a[144:147]\n"
        "v_mfma_f32_16x16x32_bf16 a[176:179], v[12:15], v[80:83], a[176:179]\n"
        "v_mfma_f32_16x16x32_bf16 a[176:179], v[8:11], v[48:51], a[176:179]\n"
        "v_mfma_f32_16x16x32_bf16 a[180:183], v[8:11], v[52:55], a[180:183]\n"
        "v_mfma_f32_16x16x32_bf16 a[180:183], v[12:15], v[84:87], a[180:183]\n"
        "s_waitcnt lgkmcnt(0)\n"
        "v_mfma_f32_16x16x32_bf16 a[148:151], v[4:7], v[84:87], a[148:151]\n"
        "v_mfma_f32_16x16x32_bf16 a[148:151], v[0:3], v[52:55], a[148:151]\n"
        "v_mfma_f32_16x16x32_bf16 a[152:155], v[0:3], v[56:59], a[152:155]\n"
        "v_mfma_f32_16x16x32_bf16 a[152:155], v[4:7], v[88:91], a[152:155]\n"
        "v_mfma_f32_16x16x32_bf16 a[184:187], v[12:15], v[88:91], a[184:187]\n"
        "v_mfma_f32_16x16x32_bf16 a[184:187], v[8:11], v[56:59], a[184:187]\n"
        "s_waitcnt vmcnt(8)\n"
        "s_barrier\n"
        "v_mfma_f32_16x16x32_bf16 a[188:191], v[8:11], v[60:63], a[188:191]\n"
        "global_load_lds_dwordx4 v238, s[68:69] offset:0\n"
        "ds_read_b64_tr_b16 v[160:161], v226 offset:32768\n"
        "v_mfma_f32_16x16x32_bf16 a[188:191], v[12:15], v[92:95], a[188:191]\n"
        "ds_read_b64_tr_b16 v[162:163], v227 offset:32768\n"
        "v_mfma_f32_16x16x32_bf16 a[156:159], v[4:7], v[92:95], a[156:159]\n"
        "ds_read_b64_tr_b16 v[164:165], v226 offset:40960\n"
        "v_mfma_f32_16x16x32_bf16 a[156:159], v[0:3], v[60:63], a[156:159]\n"
        "ds_read_b64_tr_b16 v[166:167], v227 offset:40960\n"
        "v_mfma_f32_16x16x32_bf16 a[192:195], v[32:35], v[16:19], a[192:195]\n"
        "ds_read_b64_tr_b16 v[172:173], v228 offset:40960\n"
        "global_load_lds_dwordx4 v239, s[68:69] offset:1024\n"
        "v_mfma_f32_16x16x32_bf16 a[192:195], v[64:67], v[20:23], a[192:195]\n"
        "ds_read_b64_tr_b16 v[174:175], v229 offset:40960\n"
        "v_mfma_f32_16x16x32_bf16 a[224:227], v[64:67], v[28:31], a[224:227]\n"
        "ds_read_b64_tr_b16 v[168:169], v228 offset:32768\n"
        "v_mfma_f32_16x16x32_bf16 a[224:227], v[32:35], v[24:27], a[224:227]\n"
        "ds_read_b64_tr_b16 v[170:171], v229 offset:32768\n"
        "v_mfma_f32_16x16x32_bf16 a[228:231], v[36:39], v[24:27], a[228:231]\n"
        "ds_read_b128 v[192:195], v246 offset:2048\n"
        "global_load_lds_dwordx4 v240, s[68:69] offset:2048\n"
        "v_mfma_f32_16x16x32_bf16 a[228:231], v[68:71], v[28:31], a[228:231]\n"
        "ds_read_b128 v[208:211], v247 offset:2048\n"
        "v_mfma_f32_16x16x32_bf16 a[196:199], v[68:71], v[20:23], a[196:199]\n"
        "ds_read_b128 v[196:199], v246 offset:0\n"
        "v_accvgpr_read_b32 v177, a225\n"
        "v_mfma_f32_16x16x32_bf16 a[196:199], v[36:39], v[16:19], a[196:199]\n"
        "ds_read_b128 v[212:215], v247 offset:0\n"
        "v_accvgpr_read_b32 v178, a226\n"
        "v_mfma_f32_16x16x32_bf16 a[200:203], v[40:43], v[16:19], a[200:203]\n"
        "ds_read_b128 v[200:203], v246 offset:6144\n"
        "global_load_lds_dwordx4 v241, s[68:69] offset:3072\n"
        "s_add_u32 m0, s72, 0x8000\n"
        "v_mfma_f32_16x16x32_bf16 a[200:203], v[72:75], v[20:23], a[200:203]\n"
        "ds_read_b128 v[216:219], v247 offset:6144\n"
        "s_add_u32 s68, s68, 0x80000\n"
        "s_addc_u32 s69, s69, 0\n"
        "v_mfma_f32_16x16x32_bf16 a[232:235], v[72:75], v[28:31], a[232:235]\n"
        "ds_read_b128 v[204:207], v246 offset:4096\n"
        "v_accvgpr_read_b32 v179, a227\n"
        "v_mfma_f32_16x16x32_bf16 a[232:235], v[40:43], v[24:27], a[232:235]\n"
        "ds_read_b128 v[220:223], v247 offset:4096\n"
        "v_accvgpr_read_b32 v180, a228\n"
        "v_mfma_f32_16x16x32_bf16 a[236:239], v[44:47], v[24:27], a[236:239]\n"
        "ds_read_b64_tr_b16 v[32:33], v230 offset:32768\n"
        "global_load_lds_dwordx4 v242, s[64:65] offset:0\n"
        "v_mfma_f32_16x16x32_bf16 a[236:239], v[76:79], v[28:31], a[236:239]\n"
        "ds_read_b64_tr_b16 v[34:35], v231 offset:32768\n"
        "v_accvgpr_read_b32 v181, a229\n"
        "v_mfma_f32_16x16x32_bf16 a[204:207], v[76:79], v[20:23], a[204:207]\n"
        "ds_read_b64_tr_b16 v[36:37], v230 offset:40960\n"
        "v_accvgpr_read_b32 v182, a230\n"
        "v_mfma_f32_16x16x32_bf16 a[204:207], v[44:47], v[16:19], a[204:207]\n"
        "ds_read_b64_tr_b16 v[38:39], v231 offset:40960\n"
        "s_add_u32 s97, s52, 0x6000\n"
        "v_accvgpr_read_b32 v183, a231\n"
        "v_mfma_f32_16x16x32_bf16 a[208:211], v[16:19], v[48:51], a[208:211]\n"
        "ds_read_b64_tr_b16 v[44:45], v232 offset:40960\n"
        "global_load_lds_dwordx4 v243, s[64:65] offset:1024\n"
        "v_mfma_f32_16x16x32_bf16 a[208:211], v[20:23], v[80:83], a[208:211]\n"
        "ds_read_b64_tr_b16 v[46:47], v233 offset:40960\n"
        "v_mul_f32 v177, s88, v177\n"
        "v_mfma_f32_16x16x32_bf16 a[240:243], v[28:31], v[80:83], a[240:243]\n"
        "ds_read_b64_tr_b16 v[40:41], v232 offset:32768\n"
        "v_mul_f32 v178, s88, v178\n"
        "v_mfma_f32_16x16x32_bf16 a[240:243], v[24:27], v[48:51], a[240:243]\n"
        "ds_read_b64_tr_b16 v[42:43], v233 offset:32768\n"
        "v_mul_f32 v179, s88, v179\n"
        "v_mfma_f32_16x16x32_bf16 a[244:247], v[24:27], v[52:55], a[244:247]\n"
        "global_load_lds_dwordx4 v244, s[64:65] offset:2048\n"
        "v_mfma_f32_16x16x32_bf16 a[244:247], v[28:31], v[84:87], a[244:247]\n"
        "ds_read_b64_tr_b16 v[48:49], v226 offset:33792\n"
        "v_mul_f32 v180, s88, v180\n"
        "v_mfma_f32_16x16x32_bf16 a[212:215], v[20:23], v[84:87], a[212:215]\n"
        "ds_read_b64_tr_b16 v[50:51], v227 offset:33792\n"
        "v_mul_f32 v181, s88, v181\n"
        "v_mfma_f32_16x16x32_bf16 a[212:215], v[16:19], v[52:55], a[212:215]\n"
        "ds_read_b64_tr_b16 v[52:53], v226 offset:41984\n"
        "v_mul_f32 v182, s88, v182\n"
        "v_mfma_f32_16x16x32_bf16 a[216:219], v[16:19], v[56:59], a[216:219]\n"
        "global_load_lds_dwordx4 v245, s[64:65] offset:3072\n"
        "s_add_u32 m0, s72, 0x9000\n"
        "v_mfma_f32_16x16x32_bf16 a[216:219], v[20:23], v[88:91], a[216:219]\n"
        "ds_read_b64_tr_b16 v[54:55], v227 offset:41984\n"
        "v_mul_f32 v183, s88, v183\n"
        "v_mfma_f32_16x16x32_bf16 a[248:251], v[28:31], v[88:91], a[248:251]\n"
        "s_add_u32 s64, s64, 0x80\n"
        "s_addc_u32 s65, s65, 0\n"
        "v_accvgpr_read_b32 v185, a233\n"
        "v_mfma_f32_16x16x32_bf16 a[248:251], v[24:27], v[56:59], a[248:251]\n"
        "ds_read_b64_tr_b16 v[56:57], v228 offset:33792\n"
        "v_accvgpr_read_b32 v186, a234\n"
        "v_mfma_f32_16x16x32_bf16 a[252:255], v[24:27], v[60:63], a[252:255]\n"
        "global_load_lds_dwordx4 v242, s[66:67] offset:0\n"
        "v_mfma_f32_16x16x32_bf16 a[252:255], v[28:31], v[92:95], a[252:255]\n"
        "ds_read_b64_tr_b16 v[58:59], v229 offset:33792\n"
        "v_accvgpr_read_b32 v187, a235\n"
        "v_mfma_f32_16x16x32_bf16 a[220:223], v[20:23], v[92:95], a[220:223]\n"
        "s_waitcnt lgkmcnt(4)\n"
        "v_accvgpr_read_b32 v188, a236\n"
        "v_accvgpr_read_b32 v189, a237\n"
        "v_mfma_f32_16x16x32_bf16 a[220:223], v[16:19], v[60:63], a[220:223]\n"
        "ds_read_b64_tr_b16 v[60:61], v228 offset:41984\n"
        "v_accvgpr_read_b32 v190, a238\n"
        "v_mfma_f32_16x16x32_bf16 v[0:3], v[160:163], v[192:195], 0\n"
        "ds_read_b64_tr_b16 v[62:63], v229 offset:41984\n"
        "global_load_lds_dwordx4 v243, s[66:67] offset:1024\n"
        "v_mfma_f32_16x16x32_bf16 v[0:3], v[164:167], v[208:211], v[0:3]\n"
        "v_accvgpr_read_b32 v191, a239\n"
        "v_mul_f32 v185, s88, v185\n"
        "v_mfma_f32_16x16x32_bf16 v[4:7], v[172:175], v[208:211], 0\n"
        "v_mul_f32 v186, s88, v186\n"
        "v_mul_f32 v187, s88, v187\n"
        "v_mfma_f32_16x16x32_bf16 v[4:7], v[168:171], v[192:195], v[4:7]\n"
        "v_mul_f32 v188, s88, v188\n"
        "v_mul_f32 v189, s88, v189\n"
        "v_mfma_f32_16x16x32_bf16 v[68:71], v[168:171], v[196:199], 0\n"
        "global_load_lds_dwordx4 v244, s[66:67] offset:2048\n"
        "v_mfma_f32_16x16x32_bf16 v[68:71], v[172:175], v[212:215], v[68:71]\n"
        "v_mul_f32 v190, s88, v190\n"
        "v_mul_f32 v191, s88, v191\n"
        "v_mfma_f32_16x16x32_bf16 v[64:67], v[164:167], v[212:215], 0\n"
        "v_mfma_f32_16x16x32_bf16 v[64:67], v[160:163], v[196:199], v[64:67]\n"
        "v_accvgpr_read_b32 v176, a224\n"
        "v_mul_f32 v176, s88, v176\n"
        "v_mfma_f32_16x16x32_bf16 v[96:99], v[160:163], v[200:203], 0\n"
        "v_cvt_pk_bf16_f32 v176, v176, v177\n"
        "v_cvt_pk_bf16_f32 v177, v178, v179\n"
        "v_mfma_f32_16x16x32_bf16 v[96:99], v[164:167], v[216:219], v[96:99]\n"
        "global_load_lds_dwordx4 v245, s[66:67] offset:3072\n"
        "s_add_u32 m0, s73, 0x20000\n"
        "v_cvt_pk_bf16_f32 v178, v180, v181\n"
        "v_mfma_f32_16x16x32_bf16 v[100:103], v[172:175], v[216:219], 0\n"
        "s_add_u32 s66, s66, 0x80\n"
        "s_addc_u32 s67, s67, 0\n"
        "v_cvt_pk_bf16_f32 v179, v182, v183\n"
        "v_mfma_f32_16x16x32_bf16 v[100:103], v[168:171], v[200:203], v[100:103]\n"
        "v_accvgpr_write_b32 a224, v176\n"
        "v_accvgpr_write_b32 a225, v177\n"
        "v_mfma_f32_16x16x32_bf16 v[132:135], v[168:171], v[204:207], 0\n"
        "ds_read_b64_tr_b16 v[168:169], v232 offset:33792\n"
        "v_accvgpr_write_b32 a226, v178\n"
        "v_mfma_f32_16x16x32_bf16 v[132:135], v[172:175], v[220:223], v[132:135]\n"
        "ds_read_b64_tr_b16 v[172:173], v232 offset:41984\n"
        "v_accvgpr_write_b32 a227, v179\n"
        "v_mfma_f32_16x16x32_bf16 v[128:131], v[164:167], v[220:223], 0\n"
        "ds_read_b64_tr_b16 v[164:165], v230 offset:41984\n"
        "v_accvgpr_read_b32 v177, a193\n"
        "v_mfma_f32_16x16x32_bf16 v[128:131], v[160:163], v[204:207], v[128:131]\n"
        "ds_read_b64_tr_b16 v[160:161], v230 offset:33792\n"
        "v_accvgpr_read_b32 v178, a194\n"
        "v_mfma_f32_16x16x32_bf16 v[8:11], v[32:35], v[192:195], 0\n"
        "ds_read_b64_tr_b16 v[162:163], v231 offset:33792\n"
        "v_accvgpr_read_b32 v179, a195\n"
        "v_mfma_f32_16x16x32_bf16 v[8:11], v[36:39], v[208:211], v[8:11]\n"
        "ds_read_b64_tr_b16 v[166:167], v231 offset:41984\n"
        "v_accvgpr_read_b32 v180, a196\n"
        "v_mfma_f32_16x16x32_bf16 v[12:15], v[44:47], v[208:211], 0\n"
        "ds_read_b64_tr_b16 v[174:175], v233 offset:41984\n"
        "v_accvgpr_read_b32 v181, a197\n"
        "v_mfma_f32_16x16x32_bf16 v[12:15], v[40:43], v[192:195], v[12:15]\n"
        "ds_read_b64_tr_b16 v[170:171], v233 offset:33792\n"
        "v_accvgpr_read_b32 v182, a198\n"
        "v_mfma_f32_16x16x32_bf16 v[76:79], v[40:43], v[196:199], 0\n"
        "v_accvgpr_read_b32 v183, a199\n"
        "v_mul_f32 v177, s88, v177\n"
        "v_mfma_f32_16x16x32_bf16 v[76:79], v[44:47], v[212:215], v[76:79]\n"
        "v_mul_f32 v178, s88, v178\n"
        "v_mul_f32 v179, s88, v179\n"
        "v_mfma_f32_16x16x32_bf16 v[72:75], v[36:39], v[212:215], 0\n"
        "v_mul_f32 v180, s88, v180\n"
        "v_mul_f32 v181, s88, v181\n"
        "v_mfma_f32_16x16x32_bf16 v[72:75], v[32:35], v[196:199], v[72:75]\n"
        "s_waitcnt lgkmcnt(4)\n"
        "v_mul_f32 v182, s88, v182\n"
        "v_mul_f32 v183, s88, v183\n"
        "v_mfma_f32_16x16x32_bf16 v[104:107], v[32:35], v[200:203], 0\n"
        "v_mfma_f32_16x16x32_bf16 v[104:107], v[36:39], v[216:219], v[104:107]\n"
        "v_mfma_f32_16x16x32_bf16 v[108:111], v[44:47], v[216:219], 0\n"
        "v_mfma_f32_16x16x32_bf16 v[108:111], v[40:43], v[200:203], v[108:111]\n"
        "s_waitcnt vmcnt(12) lgkmcnt(0)\n"
        "s_barrier\n"
        "v_mfma_f32_16x16x32_bf16 v[140:143], v[40:43], v[204:207], 0\n"
        "global_load_lds_dwordx4 v238, s[68:69] offset:0\n"
        "ds_read_b64_tr_b16 v[40:41], v228 offset:49152\n"
        "v_mfma_f32_16x16x32_bf16 v[140:143], v[44:47], v[220:223], v[140:143]\n"
        "ds_read_b64_tr_b16 v[44:45], v228 offset:57344\n"
        "v_mfma_f32_16x16x32_bf16 v[136:139], v[36:39], v[220:223], 0\n"
        "ds_read_b64_tr_b16 v[36:37], v226 offset:57344\n"
        "v_mfma_f32_16x16x32_bf16 v[136:139], v[32:35], v[204:207], v[136:139]\n"
        "ds_read_b64_tr_b16 v[32:33], v226 offset:49152\n"
        "v_accvgpr_read_b32 v184, a232\n"
        "v_mfma_f32_16x16x32_bf16 v[16:19], v[48:51], v[192:195], 0\n"
        "ds_read_b64_tr_b16 v[34:35], v227 offset:49152\n"
        "v_mul_f32 v184, s88, v184\n"
        "v_mfma_f32_16x16x32_bf16 v[16:19], v[52:55], v[208:211], v[16:19]\n"
        "ds_read_b64_tr_b16 v[38:39], v227 offset:57344\n"
        "v_cvt_pk_bf16_f32 v184, v184, v185\n"
        "v_mfma_f32_16x16x32_bf16 v[20:23], v[60:63], v[208:211], 0\n"
        "ds_read_b64_tr_b16 v[46:47], v229 offset:57344\n"
        "v_cvt_pk_bf16_f32 v185, v186, v187\n"
        "v_mfma_f32_16x16x32_bf16 v[20:23], v[56:59], v[192:195], v[20:23]\n"
        "ds_read_b64_tr_b16 v[42:43], v229 offset:49152\n"
        "v_cvt_pk_bf16_f32 v186, v188, v189\n"
        "v_mfma_f32_16x16x32_bf16 v[84:87], v[56:59], v[196:199], 0\n"
        "global_load_lds_dwordx4 v239, s[68:69] offset:1024\n"
        "v_cvt_pk_bf16_f32 v187, v190, v191\n"
        "v_mfma_f32_16x16x32_bf16 v[84:87], v[60:63], v[212:215], v[84:87]\n"
        "v_accvgpr_write_b32 a228, v184\n"
        "v_accvgpr_write_b32 a229, v185\n"
        "v_mfma_f32_16x16x32_bf16 v[80:83], v[52:55], v[212:215], 0\n"
        "v_accvgpr_write_b32 a230, v186\n"
        "v_accvgpr_write_b32 a231, v187\n"
        "v_mfma_f32_16x16x32_bf16 v[80:83], v[48:51], v[196:199], v[80:83]\n"
        "v_accvgpr_read_b32 v185, a201\n"
        "v_accvgpr_read_b32 v186, a202\n"
        "v_mfma_f32_16x16x32_bf16 v[112:115], v[48:51], v[200:203], 0\n"
        "global_load_lds_dwordx4 v240, s[68:69] offset:2048\n"
        "v_accvgpr_read_b32 v187, a203\n"
        "v_mfma_f32_16x16x32_bf16 v[112:115], v[52:55], v[216:219], v[112:115]\n"
        "v_accvgpr_read_b32 v188, a204\n"
        "v_accvgpr_read_b32 v189, a205\n"
        "v_mfma_f32_16x16x32_bf16 v[116:119], v[60:63], v[216:219], 0\n"
        "v_accvgpr_read_b32 v190, a206\n"
        "v_accvgpr_read_b32 v191, a207\n"
        "v_mfma_f32_16x16x32_bf16 v[116:119], v[56:59], v[200:203], v[116:119]\n"
        "v_mul_f32 v185, s88, v185\n"
        "v_mul_f32 v186, s88, v186\n"
        "v_mfma_f32_16x16x32_bf16 v[148:151], v[56:59], v[204:207], 0\n"
        "global_store_dwordx4 v254, a[224:227], s[70:71] offset:0 sc1\n"
        "ds_read_b64_tr_b16 v[56:57], v232 offset:49152\n"
        "ds_read_b64_tr_b16 v[58:59], v233 offset:49152\n"
        "v_mul_f32 v187, s88, v187\n"
        "v_mfma_f32_16x16x32_bf16 v[148:151], v[60:63], v[220:223], v[148:151]\n"
        "ds_read_b64_tr_b16 v[60:61], v232 offset:57344\n"
        "v_mul_f32 v188, s88, v188\n"
        "v_mfma_f32_16x16x32_bf16 v[144:147], v[52:55], v[220:223], 0\n"
        "ds_read_b64_tr_b16 v[52:53], v230 offset:57344\n"
        "v_mul_f32 v189, s88, v189\n"
        "v_mfma_f32_16x16x32_bf16 v[144:147], v[48:51], v[204:207], v[144:147]\n"
        "global_load_lds_dwordx4 v241, s[68:69] offset:3072\n"
        "s_add_u32 m0, s72, 0x0\n"
        "v_mfma_f32_16x16x32_bf16 v[24:27], v[160:163], v[192:195], 0\n"
        "ds_read_b64_tr_b16 v[48:49], v230 offset:49152\n"
        "s_add_u32 s68, s68, 0x80000\n"
        "s_addc_u32 s69, s69, 0\n"
        "v_mfma_f32_16x16x32_bf16 v[24:27], v[164:167], v[208:211], v[24:27]\n"
        "ds_read_b64_tr_b16 v[50:51], v231 offset:49152\n"
        "v_mul_f32 v190, s88, v190\n"
        "v_mfma_f32_16x16x32_bf16 v[28:31], v[172:175], v[208:211], 0\n"
        "ds_read_b64_tr_b16 v[54:55], v231 offset:57344\n"
        "v_mul_f32 v191, s88, v191\n"
        "v_mfma_f32_16x16x32_bf16 v[28:31], v[168:171], v[192:195], v[28:31]\n"
        "ds_read_b64_tr_b16 v[62:63], v233 offset:57344\n"
        "global_load_lds_dwordx4 v242, s[64:65] offset:0\n"
        "v_mfma_f32_16x16x32_bf16 v[92:95], v[168:171], v[196:199], 0\n"
        "ds_read_b128 v[192:195], v248 offset:2048\n"
        "v_mfma_f32_16x16x32_bf16 v[92:95], v[172:175], v[212:215], v[92:95]\n"
        "ds_read_b128 v[208:211], v249 offset:2048\n"
        "v_mfma_f32_16x16x32_bf16 v[88:91], v[164:167], v[212:215], 0\n"
        "ds_read_b128 v[212:215], v249 offset:0\n"
        "v_mfma_f32_16x16x32_bf16 v[88:91], v[160:163], v[196:199], v[88:91]\n"
        "global_load_lds_dwordx4 v243, s[64:65] offset:1024\n"
        "v_mfma_f32_16x16x32_bf16 v[120:123], v[160:163], v[200:203], 0\n"
        "ds_read_b128 v[196:199], v248 offset:0\n"
        "v_accvgpr_read_b32 v176, a192\n"
        "v_mfma_f32_16x16x32_bf16 v[120:123], v[164:167], v[216:219], v[120:123]\n"
        "v_mul_f32 v176, s88, v176\n"
        "v_cvt_pk_bf16_f32 v176, v176, v177\n"
        "v_mfma_f32_16x16x32_bf16 v[124:127], v[172:175], v[216:219], 0\n"
        "ds_read_b128 v[216:219], v249 offset:6144\n"
        "v_cvt_pk_bf16_f32 v177, v178, v179\n"
        "v_mfma_f32_16x16x32_bf16 v[124:127], v[168:171], v[200:203], v[124:127]\n"
        "s_waitcnt lgkmcnt(5)\n"
        "v_cvt_pk_bf16_f32 v178, v180, v181\n"
        "v_cvt_pk_bf16_f32 v179, v182, v183\n"
        "v_mfma_f32_16x16x32_bf16 v[156:159], v[168:171], v[204:207], 0\n"
        "s_waitcnt lgkmcnt(4)\n"
        "v_accvgpr_write_b32 a232, v176\n"
        "v_accvgpr_write_b32 a233, v177\n"
        "v_mfma_f32_16x16x32_bf16 v[156:159], v[172:175], v[220:223], v[156:159]\n"
        "s_waitcnt lgkmcnt(3)\n"
        "v_accvgpr_write_b32 a234, v178\n"
        "v_accvgpr_write_b32 a235, v179\n"
        "v_mfma_f32_16x16x32_bf16 v[152:155], v[164:167], v[220:223], 0\n"
        "LHA5_%=:\n"
        "s_cbranch_execz LHA9_%=\n"
        "global_load_lds_dwordx4 v244, s[64:65] offset:2048\n"
        "ds_read_b128 v[200:203], v248 offset:6144\n"
        "v_mfma_f32_16x16x32_bf16 v[152:155], v[160:163], v[204:207], v[152:155]\n"
        "ds_read_b128 v[204:207], v248 offset:4096\n"
        "v_accvgpr_read_b32 v177, a161\n"
        "v_mfma_f32_16x16x32_bf16 v[0:3], v[32:35], v[192:195], v[0:3]\n"
        "ds_read_b128 v[220:223], v249 offset:4096\n"
        "v_accvgpr_read_b32 v178, a162\n"
        "v_mfma_f32_16x16x32_bf16 v[0:3], v[36:39], v[208:211], v[0:3]\n"
        "ds_read_b64_tr_b16 v[160:161], v226 offset:50176\n"
        "v_accvgpr_read_b32 v179, a163\n"
        "v_mfma_f32_16x16x32_bf16 v[4:7], v[44:47], v[208:211], v[4:7]\n"
        "global_load_lds_dwordx4 v245, s[64:65] offset:3072\n"
        "ds_read_b64_tr_b16 v[162:163], v227 offset:50176\n"
        "s_add_u32 m0, s72, 0x1000\n"
        "v_mfma_f32_16x16x32_bf16 v[4:7], v[40:43], v[192:195], v[4:7]\n"
        "ds_read_b64_tr_b16 v[164:165], v226 offset:58368\n"
        "s_waitcnt lgkmcnt(6)\n"
        "v_mfma_f32_16x16x32_bf16 v[68:71], v[40:43], v[196:199], v[68:71]\n"
        "ds_read_b64_tr_b16 v[166:167], v227 offset:58368\n"
        "s_add_u32 s64, s64, 0x80\n"
        "s_addc_u32 s65, s65, 0\n"
        "v_mfma_f32_16x16x32_bf16 v[68:71], v[44:47], v[212:215], v[68:71]\n"
        "ds_read_b64_tr_b16 v[172:173], v228 offset:58368\n"
        "v_accvgpr_read_b32 v180, a164\n"
        "v_mfma_f32_16x16x32_bf16 v[64:67], v[36:39], v[212:215], v[64:67]\n"
        "ds_read_b64_tr_b16 v[174:175], v229 offset:58368\n"
        "global_load_lds_dwordx4 v242, s[66:67] offset:0\n"
        "v_mfma_f32_16x16x32_bf16 v[64:67], v[32:35], v[196:199], v[64:67]\n"
        "ds_read_b64_tr_b16 v[168:169], v228 offset:50176\n"
        "s_waitcnt lgkmcnt(8)\n"
        "v_mfma_f32_16x16x32_bf16 v[96:99], v[32:35], v[200:203], v[96:99]\n"
        "ds_read_b64_tr_b16 v[170:171], v229 offset:50176\n"
        "v_accvgpr_read_b32 v181, a165\n"
        "v_mfma_f32_16x16x32_bf16 v[96:99], v[36:39], v[216:219], v[96:99]\n"
        "s_waitcnt lgkmcnt(7)\n"
        "v_accvgpr_read_b32 v182, a166\n"
        "v_accvgpr_read_b32 v183, a167\n"
        "v_mfma_f32_16x16x32_bf16 v[100:103], v[44:47], v[216:219], v[100:103]\n"
        "global_load_lds_dwordx4 v243, s[66:67] offset:1024\n"
        "v_mfma_f32_16x16x32_bf16 v[100:103], v[40:43], v[200:203], v[100:103]\n"
        "v_mul_f32 v177, s88, v177\n"
        "v_mul_f32 v178, s88, v178\n"
        "v_mfma_f32_16x16x32_bf16 v[132:135], v[40:43], v[204:207], v[132:135]\n"
        "ds_read_b64_tr_b16 v[40:41], v232 offset:50176\n"
        "v_mul_f32 v179, s88, v179\n"
        "v_mfma_f32_16x16x32_bf16 v[132:135], v[44:47], v[220:223], v[132:135]\n"
        "ds_read_b64_tr_b16 v[44:45], v232 offset:58368\n"
        "v_mul_f32 v180, s88, v180\n"
        "v_mfma_f32_16x16x32_bf16 v[128:131], v[36:39], v[220:223], v[128:131]\n"
        "global_load_lds_dwordx4 v244, s[66:67] offset:2048\n"
        "v_mfma_f32_16x16x32_bf16 v[128:131], v[32:35], v[204:207], v[128:131]\n"
        "ds_read_b64_tr_b16 v[32:33], v230 offset:50176\n"
        "v_accvgpr_read_b32 v184, a200\n"
        "v_mfma_f32_16x16x32_bf16 v[8:11], v[48:51], v[192:195], v[8:11]\n"
        "ds_read_b64_tr_b16 v[34:35], v231 offset:50176\n"
        "v_mul_f32 v184, s88, v184\n"
        "v_mfma_f32_16x16x32_bf16 v[8:11], v[52:55], v[208:211], v[8:11]\n"
        "ds_read_b64_tr_b16 v[36:37], v230 offset:58368\n"
        "v_cvt_pk_bf16_f32 v184, v184, v185\n"
        "v_mfma_f32_16x16x32_bf16 v[12:15], v[60:63], v[208:211], v[12:15]\n"
        "global_load_lds_dwordx4 v245, s[66:67] offset:3072\n"
        "s_add_u32 m0, s73, 0x24000\n"
        "v_mfma_f32_16x16x32_bf16 v[12:15], v[56:59], v[192:195], v[12:15]\n"
        "ds_read_b64_tr_b16 v[38:39], v231 offset:58368\n"
        "v_cvt_pk_bf16_f32 v185, v186, v187\n"
        "v_mfma_f32_16x16x32_bf16 v[76:79], v[56:59], v[196:199], v[76:79]\n"
        "ds_read_b64_tr_b16 v[46:47], v233 offset:58368\n"
        "v_cvt_pk_bf16_f32 v186, v188, v189\n"
        "v_mfma_f32_16x16x32_bf16 v[76:79], v[60:63], v[212:215], v[76:79]\n"
        "s_waitcnt lgkmcnt(5)\n"
        "v_cvt_pk_bf16_f32 v187, v190, v191\n"
        "v_accvgpr_write_b32 a236, v184\n"
        "v_mfma_f32_16x16x32_bf16 v[72:75], v[52:55], v[212:215], v[72:75]\n"
        "ds_read_b64_tr_b16 v[42:43], v233 offset:50176\n"
        "v_accvgpr_write_b32 a237, v185\n"
        "v_mfma_f32_16x16x32_bf16 v[72:75], v[48:51], v[196:199], v[72:75]\n"
        "s_add_u32 s66, s66, 0x80\n"
        "s_addc_u32 s67, s67, 0\n"
        "v_accvgpr_write_b32 a238, v186\n"
        "v_mfma_f32_16x16x32_bf16 v[104:107], v[48:51], v[200:203], v[104:107]\n"
        "v_accvgpr_write_b32 a239, v187\n"
        "v_mul_f32 v181, s88, v181\n"
        "v_mfma_f32_16x16x32_bf16 v[104:107], v[52:55], v[216:219], v[104:107]\n"
        "v_mul_f32 v182, s88, v182\n"
        "v_mul_f32 v183, s88, v183\n"
        "v_mfma_f32_16x16x32_bf16 v[108:111], v[60:63], v[216:219], v[108:111]\n"
        "v_accvgpr_read_b32 v185, a169\n"
        "v_accvgpr_read_b32 v186, a170\n"
        "v_mfma_f32_16x16x32_bf16 v[108:111], v[56:59], v[200:203], v[108:111]\n"
        "v_accvgpr_read_b32 v187, a171\n"
        "v_accvgpr_read_b32 v188, a172\n"
        "v_mfma_f32_16x16x32_bf16 v[140:143], v[56:59], v[204:207], v[140:143]\n"
        "v_accvgpr_read_b32 v189, a173\n"
        "v_accvgpr_read_b32 v190, a174\n"
        "v_mfma_f32_16x16x32_bf16 v[140:143], v[60:63], v[220:223], v[140:143]\n"
        "v_accvgpr_read_b32 v191, a175\n"
        "v_mul_f32 v185, s88, v185\n"
        "v_mfma_f32_16x16x32_bf16 v[136:139], v[52:55], v[220:223], v[136:139]\n"
        "s_waitcnt vmcnt(13) lgkmcnt(0)\n"
        "s_barrier\n"
        "v_mul_f32 v186, s88, v186\n"
        "v_mfma_f32_16x16x32_bf16 v[136:139], v[48:51], v[204:207], v[136:139]\n"
        "global_load_lds_dwordx4 v238, s[68:69] offset:0\n"
        "ds_read_b64_tr_b16 v[48:49], v226 offset:16384\n"
        "v_mfma_f32_16x16x32_bf16 v[16:19], v[160:163], v[192:195], v[16:19]\n"
        "ds_read_b64_tr_b16 v[50:51], v227 offset:16384\n"
        "v_mul_f32 v187, s88, v187\n"
        "v_mfma_f32_16x16x32_bf16 v[16:19], v[164:167], v[208:211], v[16:19]\n"
        "ds_read_b64_tr_b16 v[52:53], v226 offset:24576\n"
        "v_mul_f32 v188, s88, v188\n"
        "v_mfma_f32_16x16x32_bf16 v[20:23], v[172:175], v[208:211], v[20:23]\n"
        "ds_read_b64_tr_b16 v[54:55], v227 offset:24576\n"
        "v_mul_f32 v189, s88, v189\n"
        "v_mfma_f32_16x16x32_bf16 v[20:23], v[168:171], v[192:195], v[20:23]\n"
        "ds_read_b64_tr_b16 v[60:61], v228 offset:24576\n"
        "global_load_lds_dwordx4 v239, s[68:69] offset:1024\n"
        "v_mfma_f32_16x16x32_bf16 v[84:87], v[168:171], v[196:199], v[84:87]\n"
        "ds_read_b64_tr_b16 v[62:63], v229 offset:24576\n"
        "v_mul_f32 v190, s88, v190\n"
        "v_mfma_f32_16x16x32_bf16 v[84:87], v[172:175], v[212:215], v[84:87]\n"
        "ds_read_b64_tr_b16 v[56:57], v228 offset:16384\n"
        "v_mul_f32 v191, s88, v191\n"
        "v_mfma_f32_16x16x32_bf16 v[80:83], v[164:167], v[212:215], v[80:83]\n"
        "ds_read_b64_tr_b16 v[58:59], v229 offset:16384\n"
        "v_mfma_f32_16x16x32_bf16 v[80:83], v[160:163], v[196:199], v[80:83]\n"
        "v_accvgpr_read_b32 v176, a160\n"
        "v_mul_f32 v176, s88, v176\n"
        "v_mfma_f32_16x16x32_bf16 v[112:115], v[160:163], v[200:203], v[112:115]\n"
        "global_load_lds_dwordx4 v240, s[68:69] offset:2048\n"
        "v_cvt_pk_bf16_f32 v176, v176, v177\n"
        "v_mfma_f32_16x16x32_bf16 v[112:115], v[164:167], v[216:219], v[112:115]\n"
        "v_cvt_pk_bf16_f32 v177, v178, v179\n"
        "v_cvt_pk_bf16_f32 v178, v180, v181\n"
        "v_mfma_f32_16x16x32_bf16 v[116:119], v[172:175], v[216:219], v[116:119]\n"
        "v_cvt_pk_bf16_f32 v179, v182, v183\n"
        "v_accvgpr_write_b32 a192, v176\n"
        "v_mfma_f32_16x16x32_bf16 v[116:119], v[168:171], v[200:203], v[116:119]\n"
        "v_accvgpr_write_b32 a193, v177\n"
        "v_accvgpr_write_b32 a194, v178\n"
        "v_mfma_f32_16x16x32_bf16 v[148:151], v[168:171], v[204:207], v[148:151]\n"
        "global_load_lds_dwordx4 v241, s[68:69] offset:3072\n"
        "s_add_u32 m0, s72, 0x10000\n"
        "v_accvgpr_write_b32 a195, v179\n"
        "v_mfma_f32_16x16x32_bf16 v[148:151], v[172:175], v[220:223], v[148:151]\n"
        "ds_read_b64_tr_b16 v[172:173], v232 offset:24576\n"
        "s_add_u32 s68, s68, 0x80000\n"
        "s_addc_u32 s69, s69, 0\n"
        "v_mfma_f32_16x16x32_bf16 v[144:147], v[164:167], v[220:223], v[144:147]\n"
        "ds_read_b64_tr_b16 v[164:165], v230 offset:24576\n"
        "v_accvgpr_read_b32 v177, a1\n"
        "v_mfma_f32_16x16x32_bf16 v[144:147], v[160:163], v[204:207], v[144:147]\n"
        "ds_read_b64_tr_b16 v[160:161], v230 offset:16384\n"
        "v_accvgpr_read_b32 v178, a2\n"
        "v_mfma_f32_16x16x32_bf16 v[24:27], v[32:35], v[192:195], v[24:27]\n"
        "global_store_dwordx4 v254, a[228:231], s[70:71] offset:1024 sc1\n"
        "ds_read_b64_tr_b16 v[162:163], v231 offset:16384\n"
        "ds_read_b64_tr_b16 v[166:167], v231 offset:24576\n"
        "v_accvgpr_read_b32 v179, a3\n"
        "v_mfma_f32_16x16x32_bf16 v[24:27], v[36:39], v[208:211], v[24:27]\n"
        "ds_read_b64_tr_b16 v[174:175], v233 offset:24576\n"
        "v_accvgpr_read_b32 v180, a4\n"
        "v_mfma_f32_16x16x32_bf16 v[28:31], v[44:47], v[208:211], v[28:31]\n"
        "ds_read_b64_tr_b16 v[168:169], v232 offset:16384\n"
        "v_accvgpr_read_b32 v181, a5\n"
        "v_mfma_f32_16x16x32_bf16 v[28:31], v[40:43], v[192:195], v[28:31]\n"
        "ds_read_b64_tr_b16 v[170:171], v233 offset:16384\n"
        "global_load_lds_dwordx4 v242, s[64:65] offset:0\n"
        "v_mfma_f32_16x16x32_bf16 v[92:95], v[40:43], v[196:199], v[92:95]\n"
        "ds_read_b128 v[192:195], v246 offset:34816\n"
        "v_accvgpr_read_b32 v182, a6\n"
        "v_mfma_f32_16x16x32_bf16 v[92:95], v[44:47], v[212:215], v[92:95]\n"
        "ds_read_b128 v[208:211], v247 offset:34816\n"
        "v_accvgpr_read_b32 v183, a7\n"
        "v_mfma_f32_16x16x32_bf16 v[88:91], v[36:39], v[212:215], v[88:91]\n"
        "ds_read_b128 v[212:215], v247 offset:32768\n"
        "v_mul_f32 v177, s88, v177\n"
        "v_mfma_f32_16x16x32_bf16 v[88:91], v[32:35], v[196:199], v[88:91]\n"
        "global_load_lds_dwordx4 v243, s[64:65] offset:1024\n"
        "v_mfma_f32_16x16x32_bf16 v[120:123], v[32:35], v[200:203], v[120:123]\n"
        "ds_read_b128 v[196:199], v246 offset:32768\n"
        "v_mul_f32 v178, s88, v178\n"
        "v_mfma_f32_16x16x32_bf16 v[120:123], v[36:39], v[216:219], v[120:123]\n"
        "s_add_u32 s70, s38, s97\n"
        "s_addc_u32 s71, s39, 0\n"
        "v_mul_f32 v179, s88, v179\n"
        "v_mfma_f32_16x16x32_bf16 v[124:127], v[44:47], v[216:219], v[124:127]\n"
        "ds_read_b128 v[216:219], v247 offset:38912\n"
        "v_mul_f32 v180, s88, v180\n"
        "v_mfma_f32_16x16x32_bf16 v[124:127], v[40:43], v[200:203], v[124:127]\n"
        "global_load_lds_dwordx4 v244, s[64:65] offset:2048\n"
        "s_add_u32 s97, s52, 0x5000\n"
        "v_mfma_f32_16x16x32_bf16 v[156:159], v[40:43], v[204:207], v[156:159]\n"
        "ds_read_b128 v[200:203], v246 offset:38912\n"
        "v_mul_f32 v181, s88, v181\n"
        "v_mfma_f32_16x16x32_bf16 v[156:159], v[44:47], v[220:223], v[156:159]\n"
        "ds_read_b64_tr_b16 v[44:45], v228 offset:25600\n"
        "v_mul_f32 v182, s88, v182\n"
        "v_mfma_f32_16x16x32_bf16 v[152:155], v[36:39], v[220:223], v[152:155]\n"
        "ds_read_b128 v[220:223], v247 offset:36864\n"
        "v_mul_f32 v183, s88, v183\n"
        "v_mfma_f32_16x16x32_bf16 v[152:155], v[32:35], v[204:207], v[152:155]\n"
        "s_waitcnt lgkmcnt(5)\n"
        "global_load_lds_dwordx4 v245, s[64:65] offset:3072\n"
        "s_add_u32 m0, s72, 0x11000\n"
        "v_mfma_f32_16x16x32_bf16 v[0:3], v[48:51], v[192:195], v[0:3]\n"
        "ds_read_b128 v[204:207], v246 offset:36864\n"
        "v_accvgpr_read_b32 v184, a168\n"
        "v_mfma_f32_16x16x32_bf16 v[0:3], v[52:55], v[208:211], v[0:3]\n"
        "s_waitcnt lgkmcnt(5)\n"
        "v_mul_f32 v184, s88, v184\n"
        "v_cvt_pk_bf16_f32 v184, v184, v185\n"
        "v_mfma_f32_16x16x32_bf16 v[4:7], v[60:63], v[208:211], v[4:7]\n"
        "s_waitcnt lgkmcnt(4)\n"
        "v_cvt_pk_bf16_f32 v185, v186, v187\n"
        "v_cvt_pk_bf16_f32 v186, v188, v189\n"
        "v_mfma_f32_16x16x32_bf16 v[4:7], v[56:59], v[192:195], v[4:7]\n"
        "global_load_lds_dwordx4 v242, s[66:67] offset:0\n"
        "v_mfma_f32_16x16x32_bf16 v[68:71], v[56:59], v[196:199], v[68:71]\n"
        "s_waitcnt lgkmcnt(3)\n"
        "v_cvt_pk_bf16_f32 v187, v190, v191\n"
        "v_accvgpr_write_b32 a196, v184\n"
        "v_mfma_f32_16x16x32_bf16 v[68:71], v[60:63], v[212:215], v[68:71]\n"
        "s_waitcnt lgkmcnt(2)\n"
        "v_accvgpr_write_b32 a197, v185\n"
        "v_accvgpr_write_b32 a198, v186\n"
        "v_mfma_f32_16x16x32_bf16 v[64:67], v[52:55], v[212:215], v[64:67]\n"
        "s_waitcnt lgkmcnt(1)\n"
        "v_accvgpr_write_b32 a199, v187\n"
        "v_accvgpr_read_b32 v185, a9\n"
        "v_mfma_f32_16x16x32_bf16 v[64:67], v[48:51], v[196:199], v[64:67]\n"
        "ds_read_b64_tr_b16 v[32:33], v226 offset:17408\n"
        "global_load_lds_dwordx4 v243, s[66:67] offset:1024\n"
        "v_mfma_f32_16x16x32_bf16 v[96:99], v[48:51], v[200:203], v[96:99]\n"
        "ds_read_b64_tr_b16 v[34:35], v227 offset:17408\n"
        "s_add_u32 s64, s64, 0x80\n"
        "s_addc_u32 s65, s65, 0\n"
        "v_mfma_f32_16x16x32_bf16 v[96:99], v[52:55], v[216:219], v[96:99]\n"
        "ds_read_b64_tr_b16 v[36:37], v226 offset:25600\n"
        "v_accvgpr_read_b32 v186, a10\n"
        "v_mfma_f32_16x16x32_bf16 v[100:103], v[60:63], v[216:219], v[100:103]\n"
        "ds_read_b64_tr_b16 v[38:39], v227 offset:25600\n"
        "v_accvgpr_read_b32 v187, a11\n"
        "v_mfma_f32_16x16x32_bf16 v[100:103], v[56:59], v[200:203], v[100:103]\n"
        "ds_read_b64_tr_b16 v[46:47], v229 offset:25600\n"
        "s_waitcnt lgkmcnt(5)\n"
        "v_mfma_f32_16x16x32_bf16 v[132:135], v[56:59], v[204:207], v[132:135]\n"
        "ds_read_b64_tr_b16 v[40:41], v228 offset:17408\n"
        "global_load_lds_dwordx4 v244, s[66:67] offset:2048\n"
        "v_mfma_f32_16x16x32_bf16 v[132:135], v[60:63], v[220:223], v[132:135]\n"
        "ds_read_b64_tr_b16 v[42:43], v229 offset:17408\n"
        "v_accvgpr_read_b32 v188, a12\n"
        "v_mfma_f32_16x16x32_bf16 v[128:131], v[52:55], v[220:223], v[128:131]\n"
        "ds_read_b64_tr_b16 v[52:53], v230 offset:25600\n"
        "v_accvgpr_read_b32 v189, a13\n"
        "v_mfma_f32_16x16x32_bf16 v[128:131], v[48:51], v[204:207], v[128:131]\n"
        "ds_read_b64_tr_b16 v[48:49], v230 offset:17408\n"
        "v_accvgpr_read_b32 v190, a14\n"
        "v_mfma_f32_16x16x32_bf16 v[8:11], v[160:163], v[192:195], v[8:11]\n"
        "global_load_lds_dwordx4 v245, s[66:67] offset:3072\n"
        "s_add_u32 m0, s73, 0x1c000\n"
        "v_mfma_f32_16x16x32_bf16 v[8:11], v[164:167], v[208:211], v[8:11]\n"
        "ds_read_b64_tr_b16 v[50:51], v231 offset:17408\n"
        "s_add_u32 s66, s66, 0x80\n"
        "s_addc_u32 s67, s67, 0\n"
        "v_mfma_f32_16x16x32_bf16 v[12:15], v[172:175], v[208:211], v[12:15]\n"
        "ds_read_b64_tr_b16 v[54:55], v231 offset:25600\n"
        "v_accvgpr_read_b32 v191, a15\n"
        "v_mfma_f32_16x16x32_bf16 v[12:15], v[168:171], v[192:195], v[12:15]\n"
        "ds_read_b64_tr_b16 v[60:61], v232 offset:25600\n"
        "v_mul_f32 v185, s88, v185\n"
        "v_mfma_f32_16x16x32_bf16 v[76:79], v[168:171], v[196:199], v[76:79]\n"
        "ds_read_b64_tr_b16 v[62:63], v233 offset:25600\n"
        "v_mul_f32 v186, s88, v186\n"
        "v_mfma_f32_16x16x32_bf16 v[76:79], v[172:175], v[212:215], v[76:79]\n"
        "ds_read_b64_tr_b16 v[56:57], v232 offset:17408\n"
        "v_mul_f32 v187, s88, v187\n"
        "v_mfma_f32_16x16x32_bf16 v[72:75], v[164:167], v[212:215], v[72:75]\n"
        "ds_read_b64_tr_b16 v[58:59], v233 offset:17408\n"
        "v_mul_f32 v188, s88, v188\n"
        "v_mfma_f32_16x16x32_bf16 v[72:75], v[160:163], v[196:199], v[72:75]\n"
        "s_waitcnt lgkmcnt(6)\n"
        "v_accvgpr_read_b32 v176, a0\n"
        "v_mul_f32 v176, s88, v176\n"
        "v_mfma_f32_16x16x32_bf16 v[104:107], v[160:163], v[200:203], v[104:107]\n"
        "v_cvt_pk_bf16_f32 v176, v176, v177\n"
        "v_cvt_pk_bf16_f32 v177, v178, v179\n"
        "v_mfma_f32_16x16x32_bf16 v[104:107], v[164:167], v[216:219], v[104:107]\n"
        "v_cvt_pk_bf16_f32 v178, v180, v181\n"
        "v_cvt_pk_bf16_f32 v179, v182, v183\n"
        "v_mfma_f32_16x16x32_bf16 v[108:111], v[172:175], v[216:219], v[108:111]\n"
        "v_accvgpr_write_b32 a200, v176\n"
        "v_accvgpr_write_b32 a201, v177\n"
        "v_mfma_f32_16x16x32_bf16 v[108:111], v[168:171], v[200:203], v[108:111]\n"
        "v_accvgpr_write_b32 a202, v178\n"
        "v_accvgpr_write_b32 a203, v179\n"
        "v_mfma_f32_16x16x32_bf16 v[140:143], v[168:171], v[204:207], v[140:143]\n"
        "v_mul_f32 v189, s88, v189\n"
        "v_mul_f32 v190, s88, v190\n"
        "v_mfma_f32_16x16x32_bf16 v[140:143], v[172:175], v[220:223], v[140:143]\n"
        "v_mul_f32 v191, s88, v191\n"
        "v_accvgpr_read_b32 v177, a33\n"
        "v_mfma_f32_16x16x32_bf16 v[136:139], v[164:167], v[220:223], v[136:139]\n"
        "s_waitcnt vmcnt(13) lgkmcnt(0)\n"
        "s_barrier\n"
        "v_accvgpr_read_b32 v178, a34\n"
        "v_mfma_f32_16x16x32_bf16 v[136:139], v[160:163], v[204:207], v[136:139]\n"
        "global_load_lds_dwordx4 v238, s[68:69] offset:0\n"
        "ds_read_b64_tr_b16 v[160:161], v226 offset:32768\n"
        "v_mfma_f32_16x16x32_bf16 v[16:19], v[32:35], v[192:195], v[16:19]\n"
        "ds_read_b64_tr_b16 v[162:163], v227 offset:32768\n"
        "v_accvgpr_read_b32 v179, a35\n"
        "v_mfma_f32_16x16x32_bf16 v[16:19], v[36:39], v[208:211], v[16:19]\n"
        "ds_read_b64_tr_b16 v[164:165], v226 offset:40960\n"
        "v_accvgpr_read_b32 v180, a36\n"
        "v_mfma_f32_16x16x32_bf16 v[20:23], v[44:47], v[208:211], v[20:23]\n"
        "ds_read_b64_tr_b16 v[166:167], v227 offset:40960\n"
        "v_accvgpr_read_b32 v181, a37\n"
        "v_mfma_f32_16x16x32_bf16 v[20:23], v[40:43], v[192:195], v[20:23]\n"
        "ds_read_b64_tr_b16 v[172:173], v228 offset:40960\n"
        "global_load_lds_dwordx4 v239, s[68:69] offset:1024\n"
        "v_mfma_f32_16x16x32_bf16 v[84:87], v[40:43], v[196:199], v[84:87]\n"
        "ds_read_b64_tr_b16 v[174:175], v229 offset:40960\n"
        "v_accvgpr_read_b32 v182, a38\n"
        "v_mfma_f32_16x16x32_bf16 v[84:87], v[44:47], v[212:215], v[84:87]\n"
        "ds_read_b64_tr_b16 v[168:169], v228 offset:32768\n"
        "v_accvgpr_read_b32 v183, a39\n"
        "v_mfma_f32_16x16x32_bf16 v[80:83], v[36:39], v[212:215], v[80:83]\n"
        "ds_read_b64_tr_b16 v[170:171], v229 offset:32768\n"
        "v_mul_f32 v177, s88, v177\n"
        "v_mfma_f32_16x16x32_bf16 v[80:83], v[32:35], v[196:199], v[80:83]\n"
        "global_load_lds_dwordx4 v240, s[68:69] offset:2048\n"
        "v_mfma_f32_16x16x32_bf16 v[112:115], v[32:35], v[200:203], v[112:115]\n"
        "v_mul_f32 v178, s88, v178\n"
        "v_mul_f32 v179, s88, v179\n"
        "v_mfma_f32_16x16x32_bf16 v[112:115], v[36:39], v[216:219], v[112:115]\n"
        "v_mul_f32 v180, s88, v180\n"
        "v_mul_f32 v181, s88, v181\n"
        "v_mfma_f32_16x16x32_bf16 v[116:119], v[44:47], v[216:219], v[116:119]\n"
        "v_mul_f32 v182, s88, v182\n"
        "v_mul_f32 v183, s88, v183\n"
        "v_mfma_f32_16x16x32_bf16 v[116:119], v[40:43], v[200:203], v[116:119]\n"
        "global_load_lds_dwordx4 v241, s[68:69] offset:3072\n"
        "s_add_u32 m0, s72, 0x8000\n"
        "v_mfma_f32_16x16x32_bf16 v[148:151], v[40:43], v[204:207], v[148:151]\n"
        "s_add_u32 s68, s68, 0x80000\n"
        "s_addc_u32 s69, s69, 0\n"
        "v_mfma_f32_16x16x32_bf16 v[148:151], v[44:47], v[220:223], v[148:151]\n"
        "ds_read_b64_tr_b16 v[44:45], v232 offset:40960\n"
        "v_mfma_f32_16x16x32_bf16 v[144:147], v[36:39], v[220:223], v[144:147]\n"
        "ds_read_b64_tr_b16 v[36:37], v230 offset:40960\n"
        "v_mfma_f32_16x16x32_bf16 v[144:147], v[32:35], v[204:207], v[144:147]\n"
        "ds_read_b64_tr_b16 v[32:33], v230 offset:32768\n"
        "v_accvgpr_read_b32 v184, a8\n"
        "v_mfma_f32_16x16x32_bf16 v[24:27], v[48:51], v[192:195], v[24:27]\n"
        "global_load_lds_dwordx4 v242, s[64:65] offset:0\n"
        "v_mul_f32 v184, s88, v184\n"
        "v_mfma_f32_16x16x32_bf16 v[24:27], v[52:55], v[208:211], v[24:27]\n"
        "ds_read_b64_tr_b16 v[34:35], v231 offset:32768\n"
        "v_cvt_pk_bf16_f32 v184, v184, v185\n"
        "v_mfma_f32_16x16x32_bf16 v[28:31], v[60:63], v[208:211], v[28:31]\n"
        "ds_read_b64_tr_b16 v[38:39], v231 offset:40960\n"
        "v_cvt_pk_bf16_f32 v185, v186, v187\n"
        "v_mfma_f32_16x16x32_bf16 v[28:31], v[56:59], v[192:195], v[28:31]\n"
        "ds_read_b64_tr_b16 v[46:47], v233 offset:40960\n"
        "v_cvt_pk_bf16_f32 v186, v188, v189\n"
        "v_mfma_f32_16x16x32_bf16 v[92:95], v[56:59], v[196:199], v[92:95]\n"
        "ds_read_b64_tr_b16 v[40:41], v232 offset:32768\n"
        "v_cvt_pk_bf16_f32 v187, v190, v191\n"
        "v_mfma_f32_16x16x32_bf16 v[92:95], v[60:63], v[212:215], v[92:95]\n"
        "ds_read_b64_tr_b16 v[42:43], v233 offset:32768\n"
        "v_accvgpr_write_b32 a204, v184\n"
        "v_mfma_f32_16x16x32_bf16 v[88:91], v[52:55], v[212:215], v[88:91]\n"
        "ds_read_b128 v[192:195], v246 offset:2048\n"
        "v_accvgpr_write_b32 a205, v185\n"
        "v_mfma_f32_16x16x32_bf16 v[88:91], v[48:51], v[196:199], v[88:91]\n"
        "ds_read_b128 v[208:211], v247 offset:2048\n"
        "v_accvgpr_write_b32 a206, v186\n"
        "v_mfma_f32_16x16x32_bf16 v[120:123], v[48:51], v[200:203], v[120:123]\n"
        "global_load_lds_dwordx4 v243, s[64:65] offset:1024\n"
        "v_accvgpr_write_b32 a207, v187\n"
        "v_mfma_f32_16x16x32_bf16 v[120:123], v[52:55], v[216:219], v[120:123]\n"
        "ds_read_b128 v[196:199], v246 offset:0\n"
        "v_accvgpr_read_b32 v185, a41\n"
        "v_mfma_f32_16x16x32_bf16 v[124:127], v[60:63], v[216:219], v[124:127]\n"
        "ds_read_b128 v[212:215], v247 offset:0\n"
        "v_accvgpr_read_b32 v186, a42\n"
        "v_mfma_f32_16x16x32_bf16 v[124:127], v[56:59], v[200:203], v[124:127]\n"
        "ds_read_b128 v[200:203], v246 offset:6144\n"
        "v_accvgpr_read_b32 v187, a43\n"
        "v_mfma_f32_16x16x32_bf16 v[156:159], v[56:59], v[204:207], v[156:159]\n"
        "global_store_dwordx4 v254, a[232:235], s[70:71] offset:0 sc1\n"
        "ds_read_b128 v[216:219], v247 offset:6144\n"
        "ds_read_b64_tr_b16 v[56:57], v228 offset:33792\n"
        "v_accvgpr_read_b32 v188, a44\n"
        "v_mfma_f32_16x16x32_bf16 v[156:159], v[60:63], v[220:223], v[156:159]\n"
        "ds_read_b64_tr_b16 v[60:61], v228 offset:41984\n"
        "v_accvgpr_read_b32 v189, a45\n"
        "v_mfma_f32_16x16x32_bf16 v[152:155], v[52:55], v[220:223], v[152:155]\n"
        "ds_read_b128 v[220:223], v247 offset:4096\n"
        "v_accvgpr_read_b32 v190, a46\n"
        "v_mfma_f32_16x16x32_bf16 v[152:155], v[48:51], v[204:207], v[152:155]\n"
        "s_waitcnt lgkmcnt(7)\n"
        "global_load_lds_dwordx4 v244, s[64:65] offset:2048\n"
        "v_mfma_f32_16x16x32_bf16 v[0:3], v[160:163], v[192:195], v[0:3]\n"
        "ds_read_b128 v[204:207], v246 offset:4096\n"
        "v_accvgpr_read_b32 v191, a47\n"
        "v_mfma_f32_16x16x32_bf16 v[0:3], v[164:167], v[208:211], v[0:3]\n"
        "ds_read_b64_tr_b16 v[48:49], v226 offset:33792\n"
        "v_mul_f32 v185, s88, v185\n"
        "v_mfma_f32_16x16x32_bf16 v[4:7], v[172:175], v[208:211], v[4:7]\n"
        "ds_read_b64_tr_b16 v[50:51], v227 offset:33792\n"
        "v_mul_f32 v186, s88, v186\n"
        "v_mfma_f32_16x16x32_bf16 v[4:7], v[168:171], v[192:195], v[4:7]\n"
        "s_waitcnt lgkmcnt(7)\n"
        "global_load_lds_dwordx4 v245, s[64:65] offset:3072\n"
        "s_add_u32 m0, s72, 0x9000\n"
        "v_mfma_f32_16x16x32_bf16 v[68:71], v[168:171], v[196:199], v[68:71]\n"
        "ds_read_b64_tr_b16 v[52:53], v226 offset:41984\n"
        "s_add_u32 s64, s64, 0x80\n"
        "s_addc_u32 s65, s65, 0\n"
        "v_mfma_f32_16x16x32_bf16 v[68:71], v[172:175], v[212:215], v[68:71]\n"
        "ds_read_b64_tr_b16 v[54:55], v227 offset:41984\n"
        "v_mul_f32 v187, s88, v187\n"
        "v_mfma_f32_16x16x32_bf16 v[64:67], v[164:167], v[212:215], v[64:67]\n"
        "ds_read_b64_tr_b16 v[62:63], v229 offset:41984\n"
        "v_mul_f32 v188, s88, v188\n"
        "v_mfma_f32_16x16x32_bf16 v[64:67], v[160:163], v[196:199], v[64:67]\n"
        "ds_read_b64_tr_b16 v[58:59], v229 offset:33792\n"
        "global_load_lds_dwordx4 v242, s[66:67] offset:0\n"
        "v_mfma_f32_16x16x32_bf16 v[96:99], v[160:163], v[200:203], v[96:99]\n"
        "s_waitcnt lgkmcnt(6)\n"
        "v_accvgpr_read_b32 v176, a32\n"
        "v_mul_f32 v176, s88, v176\n"
        "v_mfma_f32_16x16x32_bf16 v[96:99], v[164:167], v[216:219], v[96:99]\n"
        "v_cvt_pk_bf16_f32 v176, v176, v177\n"
        "v_cvt_pk_bf16_f32 v177, v178, v179\n"
        "v_mfma_f32_16x16x32_bf16 v[100:103], v[172:175], v[216:219], v[100:103]\n"
        "v_cvt_pk_bf16_f32 v178, v180, v181\n"
        "v_cvt_pk_bf16_f32 v179, v182, v183\n"
        "v_mfma_f32_16x16x32_bf16 v[100:103], v[168:171], v[200:203], v[100:103]\n"
        "global_load_lds_dwordx4 v243, s[66:67] offset:1024\n"
        "v_mfma_f32_16x16x32_bf16 v[132:135], v[168:171], v[204:207], v[132:135]\n"
        "ds_read_b64_tr_b16 v[168:169], v232 offset:33792\n"
        "v_accvgpr_write_b32 a160, v176\n"
        "v_mfma_f32_16x16x32_bf16 v[132:135], v[172:175], v[220:223], v[132:135]\n"
        "ds_read_b64_tr_b16 v[172:173], v232 offset:41984\n"
        "v_accvgpr_write_b32 a161, v177\n"
        "v_mfma_f32_16x16x32_bf16 v[128:131], v[164:167], v[220:223], v[128:131]\n"
        "ds_read_b64_tr_b16 v[164:165], v230 offset:41984\n"
        "v_accvgpr_write_b32 a162, v178\n"
        "v_mfma_f32_16x16x32_bf16 v[128:131], v[160:163], v[204:207], v[128:131]\n"
        "global_load_lds_dwordx4 v244, s[66:67] offset:2048\n"
        "v_mfma_f32_16x16x32_bf16 v[8:11], v[32:35], v[192:195], v[8:11]\n"
        "ds_read_b64_tr_b16 v[160:161], v230 offset:33792\n"
        "v_accvgpr_write_b32 a163, v179\n"
        "v_mfma_f32_16x16x32_bf16 v[8:11], v[36:39], v[208:211], v[8:11]\n"
        "ds_read_b64_tr_b16 v[162:163], v231 offset:33792\n"
        "v_mul_f32 v189, s88, v189\n"
        "v_mfma_f32_16x16x32_bf16 v[12:15], v[44:47], v[208:211], v[12:15]\n"
        "ds_read_b64_tr_b16 v[166:167], v231 offset:41984\n"
        "v_mul_f32 v190, s88, v190\n"
        "v_mfma_f32_16x16x32_bf16 v[12:15], v[40:43], v[192:195], v[12:15]\n"
        "global_load_lds_dwordx4 v245, s[66:67] offset:3072\n"
        "s_add_u32 m0, s73, 0x20000\n"
        "v_mfma_f32_16x16x32_bf16 v[76:79], v[40:43], v[196:199], v[76:79]\n"
        "ds_read_b64_tr_b16 v[174:175], v233 offset:41984\n"
        "s_add_u32 s66, s66, 0x80\n"
        "s_addc_u32 s67, s67, 0\n"
        "v_mfma_f32_16x16x32_bf16 v[76:79], v[44:47], v[212:215], v[76:79]\n"
        "ds_read_b64_tr_b16 v[170:171], v233 offset:33792\n"
        "v_mul_f32 v191, s88, v191\n"
        "v_mfma_f32_16x16x32_bf16 v[72:75], v[36:39], v[212:215], v[72:75]\n"
        "s_waitcnt lgkmcnt(5)\n"
        "v_accvgpr_read_b32 v177, a65\n"
        "v_accvgpr_read_b32 v178, a66\n"
        "v_mfma_f32_16x16x32_bf16 v[72:75], v[32:35], v[196:199], v[72:75]\n"
        "v_accvgpr_read_b32 v179, a67\n"
        "v_accvgpr_read_b32 v180, a68\n"
        "v_mfma_f32_16x16x32_bf16 v[104:107], v[32:35], v[200:203], v[104:107]\n"
        "v_accvgpr_read_b32 v181, a69\n"
        "v_accvgpr_read_b32 v182, a70\n"
        "v_mfma_f32_16x16x32_bf16 v[104:107], v[36:39], v[216:219], v[104:107]\n"
        "v_accvgpr_read_b32 v183, a71\n"
        "v_mul_f32 v177, s88, v177\n"
        "v_mfma_f32_16x16x32_bf16 v[108:111], v[44:47], v[216:219], v[108:111]\n"
        "v_mul_f32 v178, s88, v178\n"
        "v_mul_f32 v179, s88, v179\n"
        "v_mfma_f32_16x16x32_bf16 v[108:111], v[40:43], v[200:203], v[108:111]\n"
        "v_mul_f32 v180, s88, v180\n"
        "v_mul_f32 v181, s88, v181\n"
        "v_mfma_f32_16x16x32_bf16 v[140:143], v[40:43], v[204:207], v[140:143]\n"
        "v_mul_f32 v182, s88, v182\n"
        "v_mul_f32 v183, s88, v183\n"
        "v_mfma_f32_16x16x32_bf16 v[140:143], v[44:47], v[220:223], v[140:143]\n"
        "s_waitcnt vmcnt(13) lgkmcnt(0)\n"
        "s_barrier\n"
        "v_mfma_f32_16x16x32_bf16 v[136:139], v[36:39], v[220:223], v[136:139]\n"
        "global_load_lds_dwordx4 v238, s[68:69] offset:0\n"
        "ds_read_b64_tr_b16 v[36:37], v226 offset:57344\n"
        "v_mfma_f32_16x16x32_bf16 v[136:139], v[32:35], v[204:207], v[136:139]\n"
        "ds_read_b64_tr_b16 v[32:33], v226 offset:49152\n"
        "v_accvgpr_read_b32 v184, a40\n"
        "v_mfma_f32_16x16x32_bf16 v[16:19], v[48:51], v[192:195], v[16:19]\n"
        "ds_read_b64_tr_b16 v[34:35], v227 offset:49152\n"
        "v_mul_f32 v184, s88, v184\n"
        "v_mfma_f32_16x16x32_bf16 v[16:19], v[52:55], v[208:211], v[16:19]\n"
        "ds_read_b64_tr_b16 v[38:39], v227 offset:57344\n"
        "v_cvt_pk_bf16_f32 v184, v184, v185\n"
        "v_mfma_f32_16x16x32_bf16 v[20:23], v[60:63], v[208:211], v[20:23]\n"
        "ds_read_b64_tr_b16 v[44:45], v228 offset:57344\n"
        "v_cvt_pk_bf16_f32 v185, v186, v187\n"
        "v_mfma_f32_16x16x32_bf16 v[20:23], v[56:59], v[192:195], v[20:23]\n"
        "ds_read_b64_tr_b16 v[46:47], v229 offset:57344\n"
        "v_cvt_pk_bf16_f32 v186, v188, v189\n"
        "v_mfma_f32_16x16x32_bf16 v[84:87], v[56:59], v[196:199], v[84:87]\n"
        "ds_read_b64_tr_b16 v[40:41], v228 offset:49152\n"
        "v_cvt_pk_bf16_f32 v187, v190, v191\n"
        "v_mfma_f32_16x16x32_bf16 v[84:87], v[60:63], v[212:215], v[84:87]\n"
        "ds_read_b64_tr_b16 v[42:43], v229 offset:49152\n"
        "v_accvgpr_write_b32 a164, v184\n"
        "v_mfma_f32_16x16x32_bf16 v[80:83], v[52:55], v[212:215], v[80:83]\n"
        "global_load_lds_dwordx4 v239, s[68:69] offset:1024\n"
        "v_accvgpr_write_b32 a165, v185\n"
        "v_mfma_f32_16x16x32_bf16 v[80:83], v[48:51], v[196:199], v[80:83]\n"
        "v_accvgpr_write_b32 a166, v186\n"
        "v_accvgpr_write_b32 a167, v187\n"
        "v_mfma_f32_16x16x32_bf16 v[112:115], v[48:51], v[200:203], v[112:115]\n"
        "v_accvgpr_read_b32 v185, a73\n"
        "v_accvgpr_read_b32 v186, a74\n"
        "v_mfma_f32_16x16x32_bf16 v[112:115], v[52:55], v[216:219], v[112:115]\n"
        "v_accvgpr_read_b32 v187, a75\n"
        "v_accvgpr_read_b32 v188, a76\n"
        "v_mfma_f32_16x16x32_bf16 v[116:119], v[60:63], v[216:219], v[116:119]\n"
        "global_load_lds_dwordx4 v240, s[68:69] offset:2048\n"
        "v_accvgpr_read_b32 v189, a77\n"
        "v_mfma_f32_16x16x32_bf16 v[116:119], v[56:59], v[200:203], v[116:119]\n"
        "v_accvgpr_read_b32 v190, a78\n"
        "v_accvgpr_read_b32 v191, a79\n"
        "v_mfma_f32_16x16x32_bf16 v[148:151], v[56:59], v[204:207], v[148:151]\n"
        "ds_read_b64_tr_b16 v[56:57], v232 offset:49152\n"
        "v_mul_f32 v185, s88, v185\n"
        "v_mfma_f32_16x16x32_bf16 v[148:151], v[60:63], v[220:223], v[148:151]\n"
        "ds_read_b64_tr_b16 v[60:61], v232 offset:57344\n"
        "v_mul_f32 v186, s88, v186\n"
        "v_mfma_f32_16x16x32_bf16 v[144:147], v[52:55], v[220:223], v[144:147]\n"
        "global_load_lds_dwordx4 v241, s[68:69] offset:3072\n"
        "ds_read_b64_tr_b16 v[52:53], v230 offset:57344\n"
        "s_add_u32 m0, s72, 0x0\n"
        "v_mfma_f32_16x16x32_bf16 v[144:147], v[48:51], v[204:207], v[144:147]\n"
        "ds_read_b64_tr_b16 v[48:49], v230 offset:49152\n"
        "s_add_u32 s68, s68, 0x80000\n"
        "s_addc_u32 s69, s69, 0\n"
        "v_mfma_f32_16x16x32_bf16 v[24:27], v[160:163], v[192:195], v[24:27]\n"
        "ds_read_b64_tr_b16 v[50:51], v231 offset:49152\n"
        "v_mul_f32 v187, s88, v187\n"
        "v_mfma_f32_16x16x32_bf16 v[24:27], v[164:167], v[208:211], v[24:27]\n"
        "ds_read_b64_tr_b16 v[54:55], v231 offset:57344\n"
        "LHA6_%=:\n"
        "s_cbranch_execz LHA10_%=\n"
        "v_mul_f32 v188, s88, v188\n"
        "v_mfma_f32_16x16x32_bf16 v[28:31], v[172:175], v[208:211], v[28:31]\n"
        "ds_read_b64_tr_b16 v[62:63], v233 offset:57344\n"
        "global_load_lds_dwordx4 v242, s[64:65] offset:0\n"
        "v_mfma_f32_16x16x32_bf16 v[28:31], v[168:171], v[192:195], v[28:31]\n"
        "ds_read_b64_tr_b16 v[58:59], v233 offset:49152\n"
        "v_mul_f32 v189, s88, v189\n"
        "v_mfma_f32_16x16x32_bf16 v[92:95], v[168:171], v[196:199], v[92:95]\n"
        "ds_read_b128 v[192:195], v248 offset:2048\n"
        "v_mul_f32 v190, s88, v190\n"
        "v_mfma_f32_16x16x32_bf16 v[92:95], v[172:175], v[212:215], v[92:95]\n"
        "ds_read_b128 v[208:211], v249 offset:2048\n"
        "v_mul_f32 v191, s88, v191\n"
        "v_mfma_f32_16x16x32_bf16 v[88:91], v[164:167], v[212:215], v[88:91]\n"
        "global_load_lds_dwordx4 v243, s[64:65] offset:1024\n"
        "v_mfma_f32_16x16x32_bf16 v[88:91], v[160:163], v[196:199], v[88:91]\n"
        "ds_read_b128 v[196:199], v248 offset:0\n"
        "v_accvgpr_read_b32 v176, a64\n"
        "v_mfma_f32_16x16x32_bf16 v[120:123], v[160:163], v[200:203], v[120:123]\n"
        "ds_read_b128 v[212:215], v249 offset:0\n"
        "v_mul_f32 v176, s88, v176\n"
        "v_mfma_f32_16x16x32_bf16 v[120:123], v[164:167], v[216:219], v[120:123]\n"
        "v_cvt_pk_bf16_f32 v176, v176, v177\n"
        "v_cvt_pk_bf16_f32 v177, v178, v179\n"
        "v_mfma_f32_16x16x32_bf16 v[124:127], v[172:175], v[216:219], v[124:127]\n"
        "ds_read_b128 v[216:219], v249 offset:6144\n"
        "v_cvt_pk_bf16_f32 v178, v180, v181\n"
        "v_mfma_f32_16x16x32_bf16 v[124:127], v[168:171], v[200:203], v[124:127]\n"
        "s_waitcnt lgkmcnt(5)\n"
        "v_cvt_pk_bf16_f32 v179, v182, v183\n"
        "v_accvgpr_write_b32 a168, v176\n"
        "v_mfma_f32_16x16x32_bf16 v[156:159], v[168:171], v[204:207], v[156:159]\n"
        "s_waitcnt lgkmcnt(4)\n"
        "v_accvgpr_write_b32 a169, v177\n"
        "v_accvgpr_write_b32 a170, v178\n"
        "v_mfma_f32_16x16x32_bf16 v[156:159], v[172:175], v[220:223], v[156:159]\n"
        "s_waitcnt lgkmcnt(3)\n"
        "v_accvgpr_write_b32 a171, v179\n"
        "v_accvgpr_read_b32 v177, a97\n"
        "v_mfma_f32_16x16x32_bf16 v[152:155], v[164:167], v[220:223], v[152:155]\n"
        "global_load_lds_dwordx4 v244, s[64:65] offset:2048\n"
        "ds_read_b128 v[200:203], v248 offset:6144\n"
        "v_mfma_f32_16x16x32_bf16 v[152:155], v[160:163], v[204:207], v[152:155]\n"
        "ds_read_b128 v[204:207], v248 offset:4096\n"
        "v_accvgpr_read_b32 v178, a98\n"
        "v_mfma_f32_16x16x32_bf16 v[0:3], v[32:35], v[192:195], v[0:3]\n"
        "ds_read_b128 v[220:223], v249 offset:4096\n"
        "v_accvgpr_read_b32 v179, a99\n"
        "v_mfma_f32_16x16x32_bf16 v[0:3], v[36:39], v[208:211], v[0:3]\n"
        "ds_read_b64_tr_b16 v[160:161], v226 offset:50176\n"
        "v_accvgpr_read_b32 v180, a100\n"
        "v_mfma_f32_16x16x32_bf16 v[4:7], v[44:47], v[208:211], v[4:7]\n"
        "global_store_dwordx4 v254, a[236:239], s[70:71] offset:1024 sc1\n"
        "ds_read_b64_tr_b16 v[162:163], v227 offset:50176\n"
        "ds_read_b64_tr_b16 v[164:165], v226 offset:58368\n"
        "v_accvgpr_read_b32 v181, a101\n"
        "v_mfma_f32_16x16x32_bf16 v[4:7], v[40:43], v[192:195], v[4:7]\n"
        "ds_read_b64_tr_b16 v[166:167], v227 offset:58368\n"
        "s_waitcnt lgkmcnt(7)\n"
        "v_mfma_f32_16x16x32_bf16 v[68:71], v[40:43], v[196:199], v[68:71]\n"
        "ds_read_b64_tr_b16 v[172:173], v228 offset:58368\n"
        "v_accvgpr_read_b32 v182, a102\n"
        "v_mfma_f32_16x16x32_bf16 v[68:71], v[44:47], v[212:215], v[68:71]\n"
        "global_load_lds_dwordx4 v245, s[64:65] offset:3072\n"
        "s_add_u32 m0, s72, 0x1000\n"
        "v_mfma_f32_16x16x32_bf16 v[64:67], v[36:39], v[212:215], v[64:67]\n"
        "ds_read_b64_tr_b16 v[174:175], v229 offset:58368\n"
        "s_add_u32 s64, s64, 0x80\n"
        "s_addc_u32 s65, s65, 0\n"
        "v_mfma_f32_16x16x32_bf16 v[64:67], v[32:35], v[196:199], v[64:67]\n"
        "ds_read_b64_tr_b16 v[168:169], v228 offset:50176\n"
        "s_waitcnt lgkmcnt(7)\n"
        "v_mfma_f32_16x16x32_bf16 v[96:99], v[32:35], v[200:203], v[96:99]\n"
        "ds_read_b64_tr_b16 v[170:171], v229 offset:50176\n"
        "v_accvgpr_read_b32 v183, a103\n"
        "v_mfma_f32_16x16x32_bf16 v[96:99], v[36:39], v[216:219], v[96:99]\n"
        "global_load_lds_dwordx4 v242, s[66:67] offset:0\n"
        "v_mfma_f32_16x16x32_bf16 v[100:103], v[44:47], v[216:219], v[100:103]\n"
        "s_add_u32 s70, s38, s97\n"
        "s_addc_u32 s71, s39, 0\n"
        "v_mul_f32 v177, s88, v177\n"
        "v_mfma_f32_16x16x32_bf16 v[100:103], v[40:43], v[200:203], v[100:103]\n"
        "v_mul_f32 v178, s88, v178\n"
        "v_mul_f32 v179, s88, v179\n"
        "v_mfma_f32_16x16x32_bf16 v[132:135], v[40:43], v[204:207], v[132:135]\n"
        "ds_read_b64_tr_b16 v[40:41], v232 offset:50176\n"
        "v_mul_f32 v180, s88, v180\n"
        "v_mfma_f32_16x16x32_bf16 v[132:135], v[44:47], v[220:223], v[132:135]\n"
        "global_load_lds_dwordx4 v243, s[66:67] offset:1024\n"
        "s_add_u32 s97, s52, 0x0\n"
        "v_mfma_f32_16x16x32_bf16 v[128:131], v[36:39], v[220:223], v[128:131]\n"
        "ds_read_b64_tr_b16 v[36:37], v230 offset:58368\n"
        "v_mul_f32 v181, s88, v181\n"
        "v_mfma_f32_16x16x32_bf16 v[128:131], v[32:35], v[204:207], v[128:131]\n"
        "ds_read_b64_tr_b16 v[32:33], v230 offset:50176\n"
        "v_accvgpr_read_b32 v184, a72\n"
        "v_mfma_f32_16x16x32_bf16 v[8:11], v[48:51], v[192:195], v[8:11]\n"
        "ds_read_b64_tr_b16 v[34:35], v231 offset:50176\n"
        "v_mul_f32 v184, s88, v184\n"
        "v_mfma_f32_16x16x32_bf16 v[8:11], v[52:55], v[208:211], v[8:11]\n"
        "global_load_lds_dwordx4 v244, s[66:67] offset:2048\n"
        "v_mfma_f32_16x16x32_bf16 v[12:15], v[60:63], v[208:211], v[12:15]\n"
        "ds_read_b64_tr_b16 v[38:39], v231 offset:58368\n"
        "v_cvt_pk_bf16_f32 v184, v184, v185\n"
        "v_mfma_f32_16x16x32_bf16 v[12:15], v[56:59], v[192:195], v[12:15]\n"
        "ds_read_b64_tr_b16 v[44:45], v232 offset:58368\n"
        "v_cvt_pk_bf16_f32 v185, v186, v187\n"
        "v_mfma_f32_16x16x32_bf16 v[76:79], v[56:59], v[196:199], v[76:79]\n"
        "ds_read_b64_tr_b16 v[46:47], v233 offset:58368\n"
        "v_cvt_pk_bf16_f32 v186, v188, v189\n"
        "v_mfma_f32_16x16x32_bf16 v[76:79], v[60:63], v[212:215], v[76:79]\n"
        "global_load_lds_dwordx4 v245, s[66:67] offset:3072\n"
        "s_add_u32 m0, s73, 0x24000\n"
        "v_mfma_f32_16x16x32_bf16 v[72:75], v[52:55], v[212:215], v[72:75]\n"
        "s_waitcnt lgkmcnt(5)\n"
        "v_cvt_pk_bf16_f32 v187, v190, v191\n"
        "v_accvgpr_write_b32 a172, v184\n"
        "v_mfma_f32_16x16x32_bf16 v[72:75], v[48:51], v[196:199], v[72:75]\n"
        "ds_read_b64_tr_b16 v[42:43], v233 offset:50176\n"
        "v_accvgpr_write_b32 a173, v185\n"
        "v_mfma_f32_16x16x32_bf16 v[104:107], v[48:51], v[200:203], v[104:107]\n"
        "s_add_u32 s66, s66, 0x80\n"
        "s_addc_u32 s67, s67, 0\n"
        "v_accvgpr_write_b32 a174, v186\n"
        "v_mfma_f32_16x16x32_bf16 v[104:107], v[52:55], v[216:219], v[104:107]\n"
        "v_accvgpr_write_b32 a175, v187\n"
        "v_mul_f32 v182, s88, v182\n"
        "v_mfma_f32_16x16x32_bf16 v[108:111], v[60:63], v[216:219], v[108:111]\n"
        "v_mul_f32 v183, s88, v183\n"
        "v_accvgpr_read_b32 v185, a105\n"
        "v_mfma_f32_16x16x32_bf16 v[108:111], v[56:59], v[200:203], v[108:111]\n"
        "v_accvgpr_read_b32 v186, a106\n"
        "v_accvgpr_read_b32 v187, a107\n"
        "v_mfma_f32_16x16x32_bf16 v[140:143], v[56:59], v[204:207], v[140:143]\n"
        "v_accvgpr_read_b32 v188, a108\n"
        "v_accvgpr_read_b32 v189, a109\n"
        "v_mfma_f32_16x16x32_bf16 v[140:143], v[60:63], v[220:223], v[140:143]\n"
        "v_accvgpr_read_b32 v190, a110\n"
        "v_accvgpr_read_b32 v191, a111\n"
        "v_mfma_f32_16x16x32_bf16 v[136:139], v[52:55], v[220:223], v[136:139]\n"
        "v_mul_f32 v185, s88, v185\n"
        "v_mul_f32 v186, s88, v186\n"
        "v_mfma_f32_16x16x32_bf16 v[136:139], v[48:51], v[204:207], v[136:139]\n"
        "s_waitcnt vmcnt(13) lgkmcnt(0)\n"
        "s_barrier\n"
        "v_mul_f32 v187, s88, v187\n"
        "v_mfma_f32_16x16x32_bf16 v[16:19], v[160:163], v[192:195], v[16:19]\n"
        "global_load_lds_dwordx4 v238, s[68:69] offset:0\n"
        "ds_read_b64_tr_b16 v[48:49], v226 offset:16384\n"
        "v_mfma_f32_16x16x32_bf16 v[16:19], v[164:167], v[208:211], v[16:19]\n"
        "ds_read_b64_tr_b16 v[50:51], v227 offset:16384\n"
        "v_mul_f32 v188, s88, v188\n"
        "v_mfma_f32_16x16x32_bf16 v[20:23], v[172:175], v[208:211], v[20:23]\n"
        "ds_read_b64_tr_b16 v[52:53], v226 offset:24576\n"
        "v_mul_f32 v189, s88, v189\n"
        "v_mfma_f32_16x16x32_bf16 v[20:23], v[168:171], v[192:195], v[20:23]\n"
        "ds_read_b64_tr_b16 v[54:55], v227 offset:24576\n"
        "v_mul_f32 v190, s88, v190\n"
        "v_mfma_f32_16x16x32_bf16 v[84:87], v[168:171], v[196:199], v[84:87]\n"
        "ds_read_b64_tr_b16 v[60:61], v228 offset:24576\n"
        "global_load_lds_dwordx4 v239, s[68:69] offset:1024\n"
        "v_mfma_f32_16x16x32_bf16 v[84:87], v[172:175], v[212:215], v[84:87]\n"
        "ds_read_b64_tr_b16 v[62:63], v229 offset:24576\n"
        "v_mul_f32 v191, s88, v191\n"
        "v_mfma_f32_16x16x32_bf16 v[80:83], v[164:167], v[212:215], v[80:83]\n"
        "ds_read_b64_tr_b16 v[56:57], v228 offset:16384\n"
        "v_mfma_f32_16x16x32_bf16 v[80:83], v[160:163], v[196:199], v[80:83]\n"
        "ds_read_b64_tr_b16 v[58:59], v229 offset:16384\n"
        "v_accvgpr_read_b32 v176, a96\n"
        "v_mfma_f32_16x16x32_bf16 v[112:115], v[160:163], v[200:203], v[112:115]\n"
        "global_load_lds_dwordx4 v240, s[68:69] offset:2048\n"
        "v_mfma_f32_16x16x32_bf16 v[112:115], v[164:167], v[216:219], v[112:115]\n"
        "v_mul_f32 v176, s88, v176\n"
        "v_cvt_pk_bf16_f32 v176, v176, v177\n"
        "v_mfma_f32_16x16x32_bf16 v[116:119], v[172:175], v[216:219], v[116:119]\n"
        "v_cvt_pk_bf16_f32 v177, v178, v179\n"
        "v_cvt_pk_bf16_f32 v178, v180, v181\n"
        "v_mfma_f32_16x16x32_bf16 v[116:119], v[168:171], v[200:203], v[116:119]\n"
        "v_cvt_pk_bf16_f32 v179, v182, v183\n"
        "v_accvgpr_write_b32 a0, v176\n"
        "v_mfma_f32_16x16x32_bf16 v[148:151], v[168:171], v[204:207], v[148:151]\n"
        "ds_read_b64_tr_b16 v[168:169], v232 offset:16384\n"
        "v_accvgpr_write_b32 a1, v177\n"
        "v_mfma_f32_16x16x32_bf16 v[148:151], v[172:175], v[220:223], v[148:151]\n"
        "global_load_lds_dwordx4 v241, s[68:69] offset:3072\n"
        "s_add_u32 m0, s72, 0x10000\n"
        "v_accvgpr_write_b32 a2, v178\n"
        "v_mfma_f32_16x16x32_bf16 v[144:147], v[164:167], v[220:223], v[144:147]\n"
        "ds_read_b64_tr_b16 v[164:165], v230 offset:24576\n"
        "v_accvgpr_write_b32 a3, v179\n"
        "v_mfma_f32_16x16x32_bf16 v[144:147], v[160:163], v[204:207], v[144:147]\n"
        "ds_read_b64_tr_b16 v[160:161], v230 offset:16384\n"
        "s_add_u32 s68, s68, 0x80000\n"
        "s_addc_u32 s69, s69, 0\n"
        "v_mfma_f32_16x16x32_bf16 v[24:27], v[32:35], v[192:195], v[24:27]\n"
        "ds_read_b64_tr_b16 v[162:163], v231 offset:16384\n"
        "v_accvgpr_read_b32 v177, a129\n"
        "v_mfma_f32_16x16x32_bf16 v[24:27], v[36:39], v[208:211], v[24:27]\n"
        "ds_read_b64_tr_b16 v[166:167], v231 offset:24576\n"
        "global_load_lds_dwordx4 v242, s[64:65] offset:0\n"
        "v_mfma_f32_16x16x32_bf16 v[28:31], v[44:47], v[208:211], v[28:31]\n"
        "ds_read_b64_tr_b16 v[172:173], v232 offset:24576\n"
        "v_accvgpr_read_b32 v178, a130\n"
        "v_mfma_f32_16x16x32_bf16 v[28:31], v[40:43], v[192:195], v[28:31]\n"
        "ds_read_b64_tr_b16 v[174:175], v233 offset:24576\n"
        "v_accvgpr_read_b32 v179, a131\n"
        "v_mfma_f32_16x16x32_bf16 v[92:95], v[40:43], v[196:199], v[92:95]\n"
        "ds_read_b64_tr_b16 v[170:171], v233 offset:16384\n"
        "v_accvgpr_read_b32 v180, a132\n"
        "v_mfma_f32_16x16x32_bf16 v[92:95], v[44:47], v[212:215], v[92:95]\n"
        "ds_read_b128 v[192:195], v246 offset:34816\n"
        "global_load_lds_dwordx4 v243, s[64:65] offset:1024\n"
        "v_mfma_f32_16x16x32_bf16 v[88:91], v[36:39], v[212:215], v[88:91]\n"
        "ds_read_b128 v[208:211], v247 offset:34816\n"
        "v_accvgpr_read_b32 v181, a133\n"
        "v_mfma_f32_16x16x32_bf16 v[88:91], v[32:35], v[196:199], v[88:91]\n"
        "ds_read_b128 v[196:199], v246 offset:32768\n"
        "v_accvgpr_read_b32 v182, a134\n"
        "v_mfma_f32_16x16x32_bf16 v[120:123], v[32:35], v[200:203], v[120:123]\n"
        "ds_read_b128 v[212:215], v247 offset:32768\n"
        "v_accvgpr_read_b32 v183, a135\n"
        "v_mfma_f32_16x16x32_bf16 v[120:123], v[36:39], v[216:219], v[120:123]\n"
        "global_load_lds_dwordx4 v244, s[64:65] offset:2048\n"
        "v_mfma_f32_16x16x32_bf16 v[124:127], v[44:47], v[216:219], v[124:127]\n"
        "ds_read_b128 v[216:219], v247 offset:38912\n"
        "v_mul_f32 v177, s88, v177\n"
        "v_mfma_f32_16x16x32_bf16 v[124:127], v[40:43], v[200:203], v[124:127]\n"
        "ds_read_b128 v[200:203], v246 offset:38912\n"
        "v_mul_f32 v178, s88, v178\n"
        "v_mfma_f32_16x16x32_bf16 v[156:159], v[40:43], v[204:207], v[156:159]\n"
        "ds_read_b64_tr_b16 v[40:41], v228 offset:17408\n"
        "v_mul_f32 v179, s88, v179\n"
        "v_mfma_f32_16x16x32_bf16 v[156:159], v[44:47], v[220:223], v[156:159]\n"
        "global_load_lds_dwordx4 v245, s[64:65] offset:3072\n"
        "s_add_u32 m0, s72, 0x11000\n"
        "v_mfma_f32_16x16x32_bf16 v[152:155], v[36:39], v[220:223], v[152:155]\n"
        "ds_read_b128 v[220:223], v247 offset:36864\n"
        "s_add_u32 s64, s64, 0x80\n"
        "s_addc_u32 s65, s65, 0\n"
        "v_mfma_f32_16x16x32_bf16 v[152:155], v[32:35], v[204:207], v[152:155]\n"
        "ds_read_b128 v[204:207], v246 offset:36864\n"
        "s_waitcnt lgkmcnt(6)\n"
        "v_mfma_f32_16x16x32_bf16 v[0:3], v[48:51], v[192:195], v[0:3]\n"
        "s_waitcnt lgkmcnt(5)\n"
        "v_accvgpr_read_b32 v184, a104\n"
        "v_mul_f32 v184, s88, v184\n"
        "v_mfma_f32_16x16x32_bf16 v[0:3], v[52:55], v[208:211], v[0:3]\n"
        "global_load_lds_dwordx4 v242, s[66:67] offset:0\n"
        "v_mfma_f32_16x16x32_bf16 v[4:7], v[60:63], v[208:211], v[4:7]\n"
        "s_waitcnt lgkmcnt(4)\n"
        "v_cvt_pk_bf16_f32 v184, v184, v185\n"
        "v_cvt_pk_bf16_f32 v185, v186, v187\n"
        "v_mfma_f32_16x16x32_bf16 v[4:7], v[56:59], v[192:195], v[4:7]\n"
        "s_waitcnt lgkmcnt(3)\n"
        "v_cvt_pk_bf16_f32 v186, v188, v189\n"
        "v_cvt_pk_bf16_f32 v187, v190, v191\n"
        "v_mfma_f32_16x16x32_bf16 v[68:71], v[56:59], v[196:199], v[68:71]\n"
        "ds_read_b64_tr_b16 v[32:33], v226 offset:17408\n"
        "v_accvgpr_write_b32 a4, v184\n"
        "v_mfma_f32_16x16x32_bf16 v[68:71], v[60:63], v[212:215], v[68:71]\n"
        "s_waitcnt lgkmcnt(3)\n"
        "v_accvgpr_write_b32 a5, v185\n"
        "v_accvgpr_write_b32 a6, v186\n"
        "v_mfma_f32_16x16x32_bf16 v[64:67], v[52:55], v[212:215], v[64:67]\n"
        "s_waitcnt lgkmcnt(2)\n"
        "v_accvgpr_write_b32 a7, v187\n"
        "v_mul_f32 v180, s88, v180\n"
        "v_mfma_f32_16x16x32_bf16 v[64:67], v[48:51], v[196:199], v[64:67]\n"
        "ds_read_b64_tr_b16 v[34:35], v227 offset:17408\n"
        "global_load_lds_dwordx4 v243, s[66:67] offset:1024\n"
        "v_mfma_f32_16x16x32_bf16 v[96:99], v[48:51], v[200:203], v[96:99]\n"
        "ds_read_b64_tr_b16 v[36:37], v226 offset:25600\n"
        "v_mul_f32 v181, s88, v181\n"
        "v_mfma_f32_16x16x32_bf16 v[96:99], v[52:55], v[216:219], v[96:99]\n"
        "ds_read_b64_tr_b16 v[38:39], v227 offset:25600\n"
        "v_mul_f32 v182, s88, v182\n"
        "v_mfma_f32_16x16x32_bf16 v[100:103], v[60:63], v[216:219], v[100:103]\n"
        "ds_read_b64_tr_b16 v[44:45], v228 offset:25600\n"
        "v_mul_f32 v183, s88, v183\n"
        "v_mfma_f32_16x16x32_bf16 v[100:103], v[56:59], v[200:203], v[100:103]\n"
        "ds_read_b64_tr_b16 v[46:47], v229 offset:25600\n"
        "s_waitcnt lgkmcnt(6)\n"
        "v_mfma_f32_16x16x32_bf16 v[132:135], v[56:59], v[204:207], v[132:135]\n"
        "ds_read_b64_tr_b16 v[42:43], v229 offset:17408\n"
        "global_load_lds_dwordx4 v244, s[66:67] offset:2048\n"
        "v_mfma_f32_16x16x32_bf16 v[132:135], v[60:63], v[220:223], v[132:135]\n"
        "ds_read_b64_tr_b16 v[60:61], v232 offset:25600\n"
        "v_accvgpr_read_b32 v185, a137\n"
        "v_mfma_f32_16x16x32_bf16 v[128:131], v[52:55], v[220:223], v[128:131]\n"
        "ds_read_b64_tr_b16 v[52:53], v230 offset:25600\n"
        "v_accvgpr_read_b32 v186, a138\n"
        "v_mfma_f32_16x16x32_bf16 v[128:131], v[48:51], v[204:207], v[128:131]\n"
        "ds_read_b64_tr_b16 v[48:49], v230 offset:17408\n"
        "v_accvgpr_read_b32 v187, a139\n"
        "v_mfma_f32_16x16x32_bf16 v[8:11], v[160:163], v[192:195], v[8:11]\n"
        "global_load_lds_dwordx4 v245, s[66:67] offset:3072\n"
        "s_add_u32 m0, s73, 0x1c000\n"
        "v_mfma_f32_16x16x32_bf16 v[8:11], v[164:167], v[208:211], v[8:11]\n"
        "ds_read_b64_tr_b16 v[50:51], v231 offset:17408\n"
        "s_add_u32 s66, s66, 0x80\n"
        "s_addc_u32 s67, s67, 0\n"
        "v_mfma_f32_16x16x32_bf16 v[12:15], v[172:175], v[208:211], v[12:15]\n"
        "ds_read_b64_tr_b16 v[54:55], v231 offset:25600\n"
        "v_accvgpr_read_b32 v188, a140\n"
        "v_mfma_f32_16x16x32_bf16 v[12:15], v[168:171], v[192:195], v[12:15]\n"
        "ds_read_b64_tr_b16 v[62:63], v233 offset:25600\n"
        "v_accvgpr_read_b32 v189, a141\n"
        "v_mfma_f32_16x16x32_bf16 v[76:79], v[168:171], v[196:199], v[76:79]\n"
        "ds_read_b64_tr_b16 v[56:57], v232 offset:17408\n"
        "v_accvgpr_read_b32 v190, a142\n"
        "v_mfma_f32_16x16x32_bf16 v[76:79], v[172:175], v[212:215], v[76:79]\n"
        "global_store_dwordx4 v254, a[192:195], s[70:71] offset:0 sc1\n"
        "ds_read_b64_tr_b16 v[58:59], v233 offset:17408\n"
        "s_waitcnt lgkmcnt(7)\n"
        "v_accvgpr_read_b32 v191, a143\n"
        "v_mul_f32 v185, s88, v185\n"
        "v_mfma_f32_16x16x32_bf16 v[72:75], v[164:167], v[212:215], v[72:75]\n"
        "v_mul_f32 v186, s88, v186\n"
        "v_mul_f32 v187, s88, v187\n"
        "v_mfma_f32_16x16x32_bf16 v[72:75], v[160:163], v[196:199], v[72:75]\n"
        "v_accvgpr_read_b32 v176, a128\n"
        "v_mul_f32 v176, s88, v176\n"
        "v_mfma_f32_16x16x32_bf16 v[104:107], v[160:163], v[200:203], v[104:107]\n"
        "v_cvt_pk_bf16_f32 v176, v176, v177\n"
        "v_cvt_pk_bf16_f32 v177, v178, v179\n"
        "v_mfma_f32_16x16x32_bf16 v[104:107], v[164:167], v[216:219], v[104:107]\n"
        "v_cvt_pk_bf16_f32 v178, v180, v181\n"
        "v_cvt_pk_bf16_f32 v179, v182, v183\n"
        "v_mfma_f32_16x16x32_bf16 v[108:111], v[172:175], v[216:219], v[108:111]\n"
        "v_accvgpr_write_b32 a8, v176\n"
        "v_accvgpr_write_b32 a9, v177\n"
        "v_mfma_f32_16x16x32_bf16 v[108:111], v[168:171], v[200:203], v[108:111]\n"
        "v_accvgpr_write_b32 a10, v178\n"
        "v_accvgpr_write_b32 a11, v179\n"
        "v_mfma_f32_16x16x32_bf16 v[140:143], v[168:171], v[204:207], v[140:143]\n"
        "s_waitcnt vmcnt(13) lgkmcnt(0)\n"
        "v_mfma_f32_16x16x32_bf16 v[140:143], v[172:175], v[220:223], v[140:143]\n"
        "s_barrier\n"
        "global_load_lds_dwordx4 v238, s[68:69] offset:0\n"
        "v_mfma_f32_16x16x32_bf16 v[136:139], v[164:167], v[220:223], v[136:139]\n"
        "ds_read_b64_tr_b16 v[164:165], v226 offset:40960\n"
        "v_mul_f32 v188, s88, v188\n"
        "v_mfma_f32_16x16x32_bf16 v[136:139], v[160:163], v[204:207], v[136:139]\n"
        "ds_read_b64_tr_b16 v[160:161], v226 offset:32768\n"
        "v_mul_f32 v189, s88, v189\n"
        "v_mfma_f32_16x16x32_bf16 v[16:19], v[32:35], v[192:195], v[16:19]\n"
        "ds_read_b64_tr_b16 v[162:163], v227 offset:32768\n"
        "v_mul_f32 v190, s88, v190\n"
        "v_mfma_f32_16x16x32_bf16 v[16:19], v[36:39], v[208:211], v[16:19]\n"
        "ds_read_b64_tr_b16 v[166:167], v227 offset:40960\n"
        "global_load_lds_dwordx4 v239, s[68:69] offset:1024\n"
        "v_mfma_f32_16x16x32_bf16 v[20:23], v[44:47], v[208:211], v[20:23]\n"
        "ds_read_b64_tr_b16 v[172:173], v228 offset:40960\n"
        "v_mul_f32 v191, s88, v191\n"
        "v_mfma_f32_16x16x32_bf16 v[20:23], v[40:43], v[192:195], v[20:23]\n"
        "ds_read_b64_tr_b16 v[174:175], v229 offset:40960\n"
        "v_accvgpr_read_b32 v177, a209\n"
        "v_mfma_f32_16x16x32_bf16 v[84:87], v[40:43], v[196:199], v[84:87]\n"
        "ds_read_b64_tr_b16 v[168:169], v228 offset:32768\n"
        "v_accvgpr_read_b32 v178, a210\n"
        "v_mfma_f32_16x16x32_bf16 v[84:87], v[44:47], v[212:215], v[84:87]\n"
        "ds_read_b64_tr_b16 v[170:171], v229 offset:32768\n"
        "global_load_lds_dwordx4 v240, s[68:69] offset:2048\n"
        "v_mfma_f32_16x16x32_bf16 v[80:83], v[36:39], v[212:215], v[80:83]\n"
        "v_accvgpr_read_b32 v179, a211\n"
        "v_accvgpr_read_b32 v180, a240\n"
        "v_mfma_f32_16x16x32_bf16 v[80:83], v[32:35], v[196:199], v[80:83]\n"
        "v_accvgpr_read_b32 v181, a241\n"
        "v_accvgpr_read_b32 v182, a242\n"
        "v_mfma_f32_16x16x32_bf16 v[112:115], v[32:35], v[200:203], v[112:115]\n"
        "v_accvgpr_read_b32 v183, a243\n"
        "v_mfma_f32_16x16x32_bf16 v[112:115], v[36:39], v[216:219], v[112:115]\n"
        "global_load_lds_dwordx4 v241, s[68:69] offset:3072\n"
        "s_add_u32 m0, s72, 0x8000\n"
        "v_mfma_f32_16x16x32_bf16 v[116:119], v[44:47], v[216:219], v[116:119]\n"
        "s_add_u32 s68, s68, 0x80000\n"
        "s_addc_u32 s69, s69, 0\n"
        "v_mfma_f32_16x16x32_bf16 v[116:119], v[40:43], v[200:203], v[116:119]\n"
        "v_mfma_f32_16x16x32_bf16 v[148:151], v[40:43], v[204:207], v[148:151]\n"
        "ds_read_b64_tr_b16 v[40:41], v232 offset:32768\n"
        "v_mfma_f32_16x16x32_bf16 v[148:151], v[44:47], v[220:223], v[148:151]\n"
        "global_load_lds_dwordx4 v242, s[64:65] offset:0\n"
        "v_mfma_f32_16x16x32_bf16 v[144:147], v[36:39], v[220:223], v[144:147]\n"
        "ds_read_b64_tr_b16 v[36:37], v230 offset:40960\n"
        "v_mfma_f32_16x16x32_bf16 v[144:147], v[32:35], v[204:207], v[144:147]\n"
        "ds_read_b64_tr_b16 v[32:33], v230 offset:32768\n"
        "v_accvgpr_read_b32 v184, a136\n"
        "v_mfma_f32_16x16x32_bf16 v[24:27], v[48:51], v[192:195], v[24:27]\n"
        "ds_read_b64_tr_b16 v[34:35], v231 offset:32768\n"
        "v_mul_f32 v184, s88, v184\n"
        "v_mfma_f32_16x16x32_bf16 v[24:27], v[52:55], v[208:211], v[24:27]\n"
        "ds_read_b64_tr_b16 v[38:39], v231 offset:40960\n"
        "v_cvt_pk_bf16_f32 v184, v184, v185\n"
        "v_mfma_f32_16x16x32_bf16 v[28:31], v[60:63], v[208:211], v[28:31]\n"
        "ds_read_b64_tr_b16 v[44:45], v232 offset:40960\n"
        "v_cvt_pk_bf16_f32 v185, v186, v187\n"
        "v_mfma_f32_16x16x32_bf16 v[28:31], v[56:59], v[192:195], v[28:31]\n"
        "ds_read_b64_tr_b16 v[46:47], v233 offset:40960\n"
        "v_cvt_pk_bf16_f32 v186, v188, v189\n"
        "v_mfma_f32_16x16x32_bf16 v[92:95], v[56:59], v[196:199], v[92:95]\n"
        "ds_read_b64_tr_b16 v[42:43], v233 offset:32768\n"
        "v_cvt_pk_bf16_f32 v187, v190, v191\n"
        "v_mfma_f32_16x16x32_bf16 v[92:95], v[60:63], v[212:215], v[92:95]\n"
        "ds_read_b128 v[192:195], v246 offset:2048\n"
        "v_accvgpr_write_b32 a12, v184\n"
        "v_mfma_f32_16x16x32_bf16 v[88:91], v[52:55], v[212:215], v[88:91]\n"
        "ds_read_b128 v[208:211], v247 offset:2048\n"
        "v_accvgpr_write_b32 a13, v185\n"
        "v_mfma_f32_16x16x32_bf16 v[88:91], v[48:51], v[196:199], v[88:91]\n"
        "global_load_lds_dwordx4 v243, s[64:65] offset:1024\n"
        "v_accvgpr_write_b32 a14, v186\n"
        "v_mfma_f32_16x16x32_bf16 v[120:123], v[48:51], v[200:203], v[120:123]\n"
        "ds_read_b128 v[196:199], v246 offset:0\n"
        "v_accvgpr_write_b32 a15, v187\n"
        "v_mfma_f32_16x16x32_bf16 v[120:123], v[52:55], v[216:219], v[120:123]\n"
        "ds_read_b128 v[212:215], v247 offset:0\n"
        "v_accvgpr_read_b32 v185, a213\n"
        "v_mfma_f32_16x16x32_bf16 v[124:127], v[60:63], v[216:219], v[124:127]\n"
        "ds_read_b128 v[216:219], v247 offset:6144\n"
        "v_accvgpr_read_b32 v186, a214\n"
        "v_mfma_f32_16x16x32_bf16 v[124:127], v[56:59], v[200:203], v[124:127]\n"
        "global_load_lds_dwordx4 v244, s[64:65] offset:2048\n"
        "ds_read_b128 v[200:203], v246 offset:6144\n"
        "v_mfma_f32_16x16x32_bf16 v[156:159], v[56:59], v[204:207], v[156:159]\n"
        "ds_read_b64_tr_b16 v[56:57], v228 offset:33792\n"
        "v_accvgpr_read_b32 v187, a215\n"
        "v_mfma_f32_16x16x32_bf16 v[156:159], v[60:63], v[220:223], v[156:159]\n"
        "ds_read_b64_tr_b16 v[60:61], v228 offset:41984\n"
        "v_accvgpr_read_b32 v188, a244\n"
        "v_mfma_f32_16x16x32_bf16 v[152:155], v[52:55], v[220:223], v[152:155]\n"
        "ds_read_b128 v[220:223], v247 offset:4096\n"
        "v_accvgpr_read_b32 v189, a245\n"
        "v_mfma_f32_16x16x32_bf16 v[152:155], v[48:51], v[204:207], v[152:155]\n"
        "global_load_lds_dwordx4 v245, s[64:65] offset:3072\n"
        "s_add_u32 m0, s72, 0x9000\n"
        "s_waitcnt lgkmcnt(7)\n"
        "v_mfma_f32_16x16x32_bf16 v[0:3], v[160:163], v[192:195], v[0:3]\n"
        "ds_read_b128 v[204:207], v246 offset:4096\n"
        "s_add_u32 s64, s64, 0x80\n"
        "s_addc_u32 s65, s65, 0\n"
        "v_mfma_f32_16x16x32_bf16 v[0:3], v[164:167], v[208:211], v[0:3]\n"
        "ds_read_b64_tr_b16 v[48:49], v226 offset:33792\n"
        "v_accvgpr_read_b32 v190, a246\n"
        "v_mfma_f32_16x16x32_bf16 v[4:7], v[172:175], v[208:211], v[4:7]\n"
        "ds_read_b64_tr_b16 v[50:51], v227 offset:33792\n"
        "v_accvgpr_read_b32 v191, a247\n"
        "v_mfma_f32_16x16x32_bf16 v[4:7], v[168:171], v[192:195], v[4:7]\n"
        "ds_read_b64_tr_b16 v[52:53], v226 offset:41984\n"
        "s_waitcnt lgkmcnt(7)\n"
        "v_mfma_f32_16x16x32_bf16 v[68:71], v[168:171], v[196:199], v[68:71]\n"
        "ds_read_b64_tr_b16 v[54:55], v227 offset:41984\n"
        "global_load_lds_dwordx4 v242, s[66:67] offset:0\n"
        "v_mfma_f32_16x16x32_bf16 v[68:71], v[172:175], v[212:215], v[68:71]\n"
        "ds_read_b64_tr_b16 v[62:63], v229 offset:41984\n"
        "s_waitcnt lgkmcnt(7)\n"
        "v_mfma_f32_16x16x32_bf16 v[64:67], v[164:167], v[212:215], v[64:67]\n"
        "ds_read_b64_tr_b16 v[58:59], v229 offset:33792\n"
        "s_waitcnt lgkmcnt(7)\n"
        "v_mfma_f32_16x16x32_bf16 v[64:67], v[160:163], v[196:199], v[64:67]\n"
        "v_accvgpr_read_b32 v176, a208\n"
        "v_cvt_pk_bf16_f32 v176, v176, v177\n"
        "v_mfma_f32_16x16x32_bf16 v[96:99], v[160:163], v[200:203], v[96:99]\n"
        "global_load_lds_dwordx4 v243, s[66:67] offset:1024\n"
        "v_mfma_f32_16x16x32_bf16 v[96:99], v[164:167], v[216:219], v[96:99]\n"
        "s_waitcnt lgkmcnt(5)\n"
        "v_cvt_pk_bf16_f32 v177, v178, v179\n"
        "v_cvt_pk_bf16_f32 v178, v180, v181\n"
        "v_mfma_f32_16x16x32_bf16 v[100:103], v[172:175], v[216:219], v[100:103]\n"
        "v_cvt_pk_bf16_f32 v179, v182, v183\n"
        "v_accvgpr_write_b32 a208, v176\n"
        "v_mfma_f32_16x16x32_bf16 v[100:103], v[168:171], v[200:203], v[100:103]\n"
        "v_accvgpr_write_b32 a209, v177\n"
        "v_accvgpr_write_b32 a210, v178\n"
        "v_mfma_f32_16x16x32_bf16 v[132:135], v[168:171], v[204:207], v[132:135]\n"
        "ds_read_b64_tr_b16 v[168:169], v232 offset:33792\n"
        "v_accvgpr_write_b32 a211, v179\n"
        "v_mfma_f32_16x16x32_bf16 v[132:135], v[172:175], v[220:223], v[132:135]\n"
        "global_load_lds_dwordx4 v244, s[66:67] offset:2048\n"
        "ds_read_b64_tr_b16 v[172:173], v232 offset:41984\n"
        "v_mfma_f32_16x16x32_bf16 v[128:131], v[164:167], v[220:223], v[128:131]\n"
        "ds_read_b64_tr_b16 v[164:165], v230 offset:41984\n"
        "v_accvgpr_read_b32 v177, a217\n"
        "v_mfma_f32_16x16x32_bf16 v[128:131], v[160:163], v[204:207], v[128:131]\n"
        "ds_read_b64_tr_b16 v[160:161], v230 offset:33792\n"
        "v_accvgpr_read_b32 v178, a218\n"
        "v_mfma_f32_16x16x32_bf16 v[8:11], v[32:35], v[192:195], v[8:11]\n"
        "ds_read_b64_tr_b16 v[162:163], v231 offset:33792\n"
        "v_accvgpr_read_b32 v179, a219\n"
        "v_mfma_f32_16x16x32_bf16 v[8:11], v[36:39], v[208:211], v[8:11]\n"
        "global_store_dwordx4 v254, a[196:199], s[70:71] offset:1024 sc1\n"
        "ds_read_b64_tr_b16 v[166:167], v231 offset:41984\n"
        "ds_read_b64_tr_b16 v[174:175], v233 offset:41984\n"
        "v_accvgpr_read_b32 v180, a248\n"
        "v_mfma_f32_16x16x32_bf16 v[12:15], v[44:47], v[208:211], v[12:15]\n"
        "ds_read_b64_tr_b16 v[170:171], v233 offset:33792\n"
        "v_accvgpr_read_b32 v181, a249\n"
        "v_mfma_f32_16x16x32_bf16 v[12:15], v[40:43], v[192:195], v[12:15]\n"
        "s_add_u32 s70, s38, s97\n"
        "s_addc_u32 s71, s39, 0\n"
        "v_accvgpr_read_b32 v182, a250\n"
        "v_mfma_f32_16x16x32_bf16 v[76:79], v[40:43], v[196:199], v[76:79]\n"
        "global_load_lds_dwordx4 v245, s[66:67] offset:3072\n"
        "s_add_u32 m0, s73, 0x20000\n"
        "v_mfma_f32_16x16x32_bf16 v[76:79], v[44:47], v[212:215], v[76:79]\n"
        "s_add_u32 s66, s66, 0x80\n"
        "s_addc_u32 s67, s67, 0\n"
        "v_accvgpr_read_b32 v183, a251\n"
        "v_mfma_f32_16x16x32_bf16 v[72:75], v[36:39], v[212:215], v[72:75]\n"
        "s_add_u32 s97, s52, 0x1000\n"
        "s_waitcnt lgkmcnt(4)\n"
        "v_mfma_f32_16x16x32_bf16 v[72:75], v[32:35], v[196:199], v[72:75]\n"
        "v_mfma_f32_16x16x32_bf16 v[104:107], v[32:35], v[200:203], v[104:107]\n"
        "v_mfma_f32_16x16x32_bf16 v[104:107], v[36:39], v[216:219], v[104:107]\n"
        "v_mfma_f32_16x16x32_bf16 v[108:111], v[44:47], v[216:219], v[108:111]\n"
        "s_waitcnt vmcnt(14) lgkmcnt(0)\n"
        "s_barrier\n"
        "v_mfma_f32_16x16x32_bf16 v[108:111], v[40:43], v[200:203], v[108:111]\n"
        "global_load_lds_dwordx4 v238, s[68:69] offset:0\n"
        "v_mfma_f32_16x16x32_bf16 v[140:143], v[40:43], v[204:207], v[140:143]\n"
        "ds_read_b64_tr_b16 v[40:41], v228 offset:49152\n"
        "v_mfma_f32_16x16x32_bf16 v[140:143], v[44:47], v[220:223], v[140:143]\n"
        "ds_read_b64_tr_b16 v[44:45], v228 offset:57344\n"
        "v_mfma_f32_16x16x32_bf16 v[136:139], v[36:39], v[220:223], v[136:139]\n"
        "ds_read_b64_tr_b16 v[36:37], v226 offset:57344\n"
        "v_mfma_f32_16x16x32_bf16 v[136:139], v[32:35], v[204:207], v[136:139]\n"
        "ds_read_b64_tr_b16 v[32:33], v226 offset:49152\n"
        "v_accvgpr_read_b32 v184, a212\n"
        "v_mfma_f32_16x16x32_bf16 v[16:19], v[48:51], v[192:195], v[16:19]\n"
        "ds_read_b64_tr_b16 v[34:35], v227 offset:49152\n"
        "v_cvt_pk_bf16_f32 v184, v184, v185\n"
        "v_mfma_f32_16x16x32_bf16 v[16:19], v[52:55], v[208:211], v[16:19]\n"
        "ds_read_b64_tr_b16 v[38:39], v227 offset:57344\n"
        "v_cvt_pk_bf16_f32 v185, v186, v187\n"
        "v_mfma_f32_16x16x32_bf16 v[20:23], v[60:63], v[208:211], v[20:23]\n"
        "ds_read_b64_tr_b16 v[46:47], v229 offset:57344\n"
        "v_cvt_pk_bf16_f32 v186, v188, v189\n"
        "v_mfma_f32_16x16x32_bf16 v[20:23], v[56:59], v[192:195], v[20:23]\n"
        "ds_read_b64_tr_b16 v[42:43], v229 offset:49152\n"
        "v_cvt_pk_bf16_f32 v187, v190, v191\n"
        "v_mfma_f32_16x16x32_bf16 v[84:87], v[56:59], v[196:199], v[84:87]\n"
        "global_load_lds_dwordx4 v239, s[68:69] offset:1024\n"
        "v_accvgpr_write_b32 a240, v184\n"
        "v_mfma_f32_16x16x32_bf16 v[84:87], v[60:63], v[212:215], v[84:87]\n"
        "v_accvgpr_write_b32 a241, v185\n"
        "v_accvgpr_write_b32 a242, v186\n"
        "v_mfma_f32_16x16x32_bf16 v[80:83], v[52:55], v[212:215], v[80:83]\n"
        "v_accvgpr_write_b32 a243, v187\n"
        "v_accvgpr_read_b32 v185, a221\n"
        "v_mfma_f32_16x16x32_bf16 v[80:83], v[48:51], v[196:199], v[80:83]\n"
        "v_accvgpr_read_b32 v186, a222\n"
        "v_accvgpr_read_b32 v187, a223\n"
        "v_mfma_f32_16x16x32_bf16 v[112:115], v[48:51], v[200:203], v[112:115]\n"
        "global_load_lds_dwordx4 v240, s[68:69] offset:2048\n"
        "v_accvgpr_read_b32 v188, a252\n"
        "v_mfma_f32_16x16x32_bf16 v[112:115], v[52:55], v[216:219], v[112:115]\n"
        "LHA7_%=:\n"
        "s_cbranch_execz LHA11_%=\n"
        "v_accvgpr_read_b32 v189, a253\n"
        "v_accvgpr_read_b32 v190, a254\n"
        "v_mfma_f32_16x16x32_bf16 v[116:119], v[60:63], v[216:219], v[116:119]\n"
        "v_accvgpr_read_b32 v191, a255\n"
        "v_mfma_f32_16x16x32_bf16 v[116:119], v[56:59], v[200:203], v[116:119]\n"
        "v_mfma_f32_16x16x32_bf16 v[148:151], v[56:59], v[204:207], v[148:151]\n"
        "global_load_lds_dwordx4 v241, s[68:69] offset:3072\n"
        "ds_read_b64_tr_b16 v[56:57], v232 offset:49152\n"
        "s_add_u32 m0, s72, 0x0\n"
        "v_mfma_f32_16x16x32_bf16 v[148:151], v[60:63], v[220:223], v[148:151]\n"
        "ds_read_b64_tr_b16 v[60:61], v232 offset:57344\n"
        "s_add_u32 s68, s68, 0x80000\n"
        "s_addc_u32 s69, s69, 0\n"
        "v_mfma_f32_16x16x32_bf16 v[144:147], v[52:55], v[220:223], v[144:147]\n"
        "ds_read_b64_tr_b16 v[52:53], v230 offset:57344\n"
        "v_mfma_f32_16x16x32_bf16 v[144:147], v[48:51], v[204:207], v[144:147]\n"
        "ds_read_b64_tr_b16 v[48:49], v230 offset:49152\n"
        "v_mfma_f32_16x16x32_bf16 v[24:27], v[160:163], v[192:195], v[24:27]\n"
        "ds_read_b64_tr_b16 v[50:51], v231 offset:49152\n"
        "global_load_lds_dwordx4 v242, s[64:65] offset:0\n"
        "v_mfma_f32_16x16x32_bf16 v[24:27], v[164:167], v[208:211], v[24:27]\n"
        "ds_read_b64_tr_b16 v[54:55], v231 offset:57344\n"
        "v_mfma_f32_16x16x32_bf16 v[28:31], v[172:175], v[208:211], v[28:31]\n"
        "ds_read_b64_tr_b16 v[62:63], v233 offset:57344\n"
        "v_mfma_f32_16x16x32_bf16 v[28:31], v[168:171], v[192:195], v[28:31]\n"
        "ds_read_b64_tr_b16 v[58:59], v233 offset:49152\n"
        "v_mfma_f32_16x16x32_bf16 v[92:95], v[168:171], v[196:199], v[92:95]\n"
        "ds_read_b128 v[192:195], v248 offset:2048\n"
        "global_load_lds_dwordx4 v243, s[64:65] offset:1024\n"
        "v_mfma_f32_16x16x32_bf16 v[92:95], v[172:175], v[212:215], v[92:95]\n"
        "ds_read_b128 v[208:211], v249 offset:2048\n"
        "v_mfma_f32_16x16x32_bf16 v[88:91], v[164:167], v[212:215], v[88:91]\n"
        "ds_read_b128 v[212:215], v249 offset:0\n"
        "v_mfma_f32_16x16x32_bf16 v[88:91], v[160:163], v[196:199], v[88:91]\n"
        "ds_read_b128 v[196:199], v248 offset:0\n"
        "v_accvgpr_read_b32 v176, a216\n"
        "v_mfma_f32_16x16x32_bf16 v[120:123], v[160:163], v[200:203], v[120:123]\n"
        "v_cvt_pk_bf16_f32 v176, v176, v177\n"
        "v_cvt_pk_bf16_f32 v177, v178, v179\n"
        "v_mfma_f32_16x16x32_bf16 v[120:123], v[164:167], v[216:219], v[120:123]\n"
        "global_load_lds_dwordx4 v244, s[64:65] offset:2048\n"
        "v_cvt_pk_bf16_f32 v178, v180, v181\n"
        "v_mfma_f32_16x16x32_bf16 v[124:127], v[172:175], v[216:219], v[124:127]\n"
        "ds_read_b128 v[216:219], v249 offset:6144\n"
        "v_cvt_pk_bf16_f32 v179, v182, v183\n"
        "v_mfma_f32_16x16x32_bf16 v[124:127], v[168:171], v[200:203], v[124:127]\n"
        "s_waitcnt lgkmcnt(5)\n"
        "v_accvgpr_write_b32 a212, v176\n"
        "v_accvgpr_write_b32 a213, v177\n"
        "v_mfma_f32_16x16x32_bf16 v[156:159], v[168:171], v[204:207], v[156:159]\n"
        "s_waitcnt lgkmcnt(4)\n"
        "v_accvgpr_write_b32 a214, v178\n"
        "v_accvgpr_write_b32 a215, v179\n"
        "v_mfma_f32_16x16x32_bf16 v[156:159], v[172:175], v[220:223], v[156:159]\n"
        "ds_read_b128 v[200:203], v248 offset:6144\n"
        "global_load_lds_dwordx4 v245, s[64:65] offset:3072\n"
        "s_add_u32 m0, s72, 0x1000\n"
        "v_mfma_f32_16x16x32_bf16 v[152:155], v[164:167], v[220:223], v[152:155]\n"
        "ds_read_b128 v[220:223], v249 offset:4096\n"
        "s_add_u32 s64, s64, 0x80\n"
        "s_addc_u32 s65, s65, 0\n"
        "v_mfma_f32_16x16x32_bf16 v[152:155], v[160:163], v[204:207], v[152:155]\n"
        "ds_read_b128 v[204:207], v248 offset:4096\n"
        "v_accvgpr_read_b32 v177, a145\n"
        "v_mfma_f32_16x16x32_bf16 v[0:3], v[32:35], v[192:195], v[0:3]\n"
        "ds_read_b64_tr_b16 v[160:161], v226 offset:50176\n"
        "s_waitcnt lgkmcnt(5)\n"
        "v_mfma_f32_16x16x32_bf16 v[0:3], v[36:39], v[208:211], v[0:3]\n"
        "ds_read_b64_tr_b16 v[162:163], v227 offset:50176\n"
        "global_load_lds_dwordx4 v242, s[66:67] offset:0\n"
        "v_mfma_f32_16x16x32_bf16 v[4:7], v[44:47], v[208:211], v[4:7]\n"
        "ds_read_b64_tr_b16 v[164:165], v226 offset:58368\n"
        "v_accvgpr_read_b32 v178, a146\n"
        "v_mfma_f32_16x16x32_bf16 v[4:7], v[40:43], v[192:195], v[4:7]\n"
        "ds_read_b64_tr_b16 v[166:167], v227 offset:58368\n"
        "v_accvgpr_read_b32 v179, a147\n"
        "v_mfma_f32_16x16x32_bf16 v[68:71], v[40:43], v[196:199], v[68:71]\n"
        "ds_read_b64_tr_b16 v[172:173], v228 offset:58368\n"
        "v_accvgpr_read_b32 v180, a176\n"
        "v_mfma_f32_16x16x32_bf16 v[68:71], v[44:47], v[212:215], v[68:71]\n"
        "ds_read_b64_tr_b16 v[174:175], v229 offset:58368\n"
        "global_load_lds_dwordx4 v243, s[66:67] offset:1024\n"
        "v_mfma_f32_16x16x32_bf16 v[64:67], v[36:39], v[212:215], v[64:67]\n"
        "ds_read_b64_tr_b16 v[168:169], v228 offset:50176\n"
        "v_accvgpr_read_b32 v181, a177\n"
        "v_mfma_f32_16x16x32_bf16 v[64:67], v[32:35], v[196:199], v[64:67]\n"
        "ds_read_b64_tr_b16 v[170:171], v229 offset:50176\n"
        "s_waitcnt lgkmcnt(8)\n"
        "v_mfma_f32_16x16x32_bf16 v[96:99], v[32:35], v[200:203], v[96:99]\n"
        "v_accvgpr_read_b32 v182, a178\n"
        "v_accvgpr_read_b32 v183, a179\n"
        "v_mfma_f32_16x16x32_bf16 v[96:99], v[36:39], v[216:219], v[96:99]\n"
        "global_load_lds_dwordx4 v244, s[66:67] offset:2048\n"
        "v_mfma_f32_16x16x32_bf16 v[100:103], v[44:47], v[216:219], v[100:103]\n"
        "v_mfma_f32_16x16x32_bf16 v[100:103], v[40:43], v[200:203], v[100:103]\n"
        "v_mfma_f32_16x16x32_bf16 v[132:135], v[40:43], v[204:207], v[132:135]\n"
        "ds_read_b64_tr_b16 v[40:41], v232 offset:50176\n"
        "v_mfma_f32_16x16x32_bf16 v[132:135], v[44:47], v[220:223], v[132:135]\n"
        "global_load_lds_dwordx4 v245, s[66:67] offset:3072\n"
        "s_add_u32 m0, s73, 0x24000\n"
        "v_mfma_f32_16x16x32_bf16 v[128:131], v[36:39], v[220:223], v[128:131]\n"
        "ds_read_b64_tr_b16 v[36:37], v230 offset:58368\n"
        "s_add_u32 s66, s66, 0x80\n"
        "s_addc_u32 s67, s67, 0\n"
        "v_mfma_f32_16x16x32_bf16 v[128:131], v[32:35], v[204:207], v[128:131]\n"
        "ds_read_b64_tr_b16 v[32:33], v230 offset:50176\n"
        "v_accvgpr_read_b32 v184, a220\n"
        "v_mfma_f32_16x16x32_bf16 v[8:11], v[48:51], v[192:195], v[8:11]\n"
        "ds_read_b64_tr_b16 v[34:35], v231 offset:50176\n"
        "v_cvt_pk_bf16_f32 v184, v184, v185\n"
        "v_mfma_f32_16x16x32_bf16 v[8:11], v[52:55], v[208:211], v[8:11]\n"
        "ds_read_b64_tr_b16 v[38:39], v231 offset:58368\n"
        "v_cvt_pk_bf16_f32 v185, v186, v187\n"
        "v_mfma_f32_16x16x32_bf16 v[12:15], v[60:63], v[208:211], v[12:15]\n"
        "ds_read_b64_tr_b16 v[44:45], v232 offset:58368\n"
        "v_cvt_pk_bf16_f32 v186, v188, v189\n"
        "v_mfma_f32_16x16x32_bf16 v[12:15], v[56:59], v[192:195], v[12:15]\n"
        "ds_read_b64_tr_b16 v[46:47], v233 offset:58368\n"
        "v_cvt_pk_bf16_f32 v187, v190, v191\n"
        "v_mfma_f32_16x16x32_bf16 v[76:79], v[56:59], v[196:199], v[76:79]\n"
        "ds_read_b64_tr_b16 v[42:43], v233 offset:50176\n"
        "v_accvgpr_write_b32 a244, v184\n"
        "v_mfma_f32_16x16x32_bf16 v[76:79], v[60:63], v[212:215], v[76:79]\n"
        "s_waitcnt lgkmcnt(7)\n"
        "v_accvgpr_write_b32 a245, v185\n"
        "v_accvgpr_write_b32 a246, v186\n"
        "v_mfma_f32_16x16x32_bf16 v[72:75], v[52:55], v[212:215], v[72:75]\n"
        "v_accvgpr_write_b32 a247, v187\n"
        "v_accvgpr_read_b32 v185, a149\n"
        "v_mfma_f32_16x16x32_bf16 v[72:75], v[48:51], v[196:199], v[72:75]\n"
        "global_store_dwordx4 v254, a[200:203], s[70:71] offset:0 sc1\n"
        "v_accvgpr_read_b32 v186, a150\n"
        "v_accvgpr_read_b32 v187, a151\n"
        "v_accvgpr_read_b32 v188, a180\n"
        "v_accvgpr_read_b32 v189, a181\n"
        "v_mfma_f32_16x16x32_bf16 v[104:107], v[48:51], v[200:203], v[104:107]\n"
        "v_accvgpr_read_b32 v190, a182\n"
        "v_accvgpr_read_b32 v191, a183\n"
        "v_mfma_f32_16x16x32_bf16 v[104:107], v[52:55], v[216:219], v[104:107]\n"
        "v_mfma_f32_16x16x32_bf16 v[108:111], v[60:63], v[216:219], v[108:111]\n"
        "v_mfma_f32_16x16x32_bf16 v[108:111], v[56:59], v[200:203], v[108:111]\n"
        "s_waitcnt vmcnt(13) lgkmcnt(0)\n"
        "s_barrier\n"
        "v_mfma_f32_16x16x32_bf16 v[140:143], v[56:59], v[204:207], v[140:143]\n"
        "global_load_lds_dwordx4 v238, s[68:69] offset:0\n"
        "ds_read_b64_tr_b16 v[56:57], v228 offset:16384\n"
        "v_mfma_f32_16x16x32_bf16 v[140:143], v[60:63], v[220:223], v[140:143]\n"
        "ds_read_b64_tr_b16 v[60:61], v228 offset:24576\n"
        "v_mfma_f32_16x16x32_bf16 v[136:139], v[52:55], v[220:223], v[136:139]\n"
        "ds_read_b64_tr_b16 v[52:53], v226 offset:24576\n"
        "v_mfma_f32_16x16x32_bf16 v[136:139], v[48:51], v[204:207], v[136:139]\n"
        "ds_read_b64_tr_b16 v[48:49], v226 offset:16384\n"
        "v_mfma_f32_16x16x32_bf16 v[16:19], v[160:163], v[192:195], v[16:19]\n"
        "ds_read_b64_tr_b16 v[50:51], v227 offset:16384\n"
        "global_load_lds_dwordx4 v239, s[68:69] offset:1024\n"
        "v_mfma_f32_16x16x32_bf16 v[16:19], v[164:167], v[208:211], v[16:19]\n"
        "ds_read_b64_tr_b16 v[54:55], v227 offset:24576\n"
        "v_mfma_f32_16x16x32_bf16 v[20:23], v[172:175], v[208:211], v[20:23]\n"
        "ds_read_b64_tr_b16 v[62:63], v229 offset:24576\n"
        "v_mfma_f32_16x16x32_bf16 v[20:23], v[168:171], v[192:195], v[20:23]\n"
        "ds_read_b64_tr_b16 v[58:59], v229 offset:16384\n"
        "v_mfma_f32_16x16x32_bf16 v[84:87], v[168:171], v[196:199], v[84:87]\n"
        "global_load_lds_dwordx4 v240, s[68:69] offset:2048\n"
        "v_mfma_f32_16x16x32_bf16 v[84:87], v[172:175], v[212:215], v[84:87]\n"
        "v_mfma_f32_16x16x32_bf16 v[80:83], v[164:167], v[212:215], v[80:83]\n"
        "v_mfma_f32_16x16x32_bf16 v[80:83], v[160:163], v[196:199], v[80:83]\n"
        "v_accvgpr_read_b32 v176, a144\n"
        "v_cvt_pk_bf16_f32 v176, v176, v177\n"
        "v_mfma_f32_16x16x32_bf16 v[112:115], v[160:163], v[200:203], v[112:115]\n"
        "v_cvt_pk_bf16_f32 v177, v178, v179\n"
        "v_cvt_pk_bf16_f32 v178, v180, v181\n"
        "v_mfma_f32_16x16x32_bf16 v[112:115], v[164:167], v[216:219], v[112:115]\n"
        "global_load_lds_dwordx4 v241, s[68:69] offset:3072\n"
        "s_add_u32 m0, s72, 0x10000\n"
        "v_cvt_pk_bf16_f32 v179, v182, v183\n"
        "v_mfma_f32_16x16x32_bf16 v[116:119], v[172:175], v[216:219], v[116:119]\n"
        "s_add_u32 s68, s68, 0x80000\n"
        "s_addc_u32 s69, s69, 0\n"
        "v_accvgpr_write_b32 a216, v176\n"
        "v_mfma_f32_16x16x32_bf16 v[116:119], v[168:171], v[200:203], v[116:119]\n"
        "v_accvgpr_write_b32 a217, v177\n"
        "v_accvgpr_write_b32 a218, v178\n"
        "v_mfma_f32_16x16x32_bf16 v[148:151], v[168:171], v[204:207], v[148:151]\n"
        "ds_read_b64_tr_b16 v[168:169], v232 offset:16384\n"
        "v_accvgpr_write_b32 a219, v179\n"
        "v_mfma_f32_16x16x32_bf16 v[148:151], v[172:175], v[220:223], v[148:151]\n"
        "ds_read_b64_tr_b16 v[172:173], v232 offset:24576\n"
        "global_load_lds_dwordx4 v242, s[64:65] offset:0\n"
        "v_mfma_f32_16x16x32_bf16 v[144:147], v[164:167], v[220:223], v[144:147]\n"
        "ds_read_b64_tr_b16 v[164:165], v230 offset:24576\n"
        "v_accvgpr_read_b32 v177, a153\n"
        "v_mfma_f32_16x16x32_bf16 v[144:147], v[160:163], v[204:207], v[144:147]\n"
        "ds_read_b64_tr_b16 v[160:161], v230 offset:16384\n"
        "v_accvgpr_read_b32 v178, a154\n"
        "v_mfma_f32_16x16x32_bf16 v[24:27], v[32:35], v[192:195], v[24:27]\n"
        "ds_read_b64_tr_b16 v[162:163], v231 offset:16384\n"
        "v_accvgpr_read_b32 v179, a155\n"
        "v_mfma_f32_16x16x32_bf16 v[24:27], v[36:39], v[208:211], v[24:27]\n"
        "ds_read_b64_tr_b16 v[166:167], v231 offset:24576\n"
        "global_load_lds_dwordx4 v243, s[64:65] offset:1024\n"
        "v_mfma_f32_16x16x32_bf16 v[28:31], v[44:47], v[208:211], v[28:31]\n"
        "ds_read_b64_tr_b16 v[174:175], v233 offset:24576\n"
        "v_accvgpr_read_b32 v180, a184\n"
        "v_mfma_f32_16x16x32_bf16 v[28:31], v[40:43], v[192:195], v[28:31]\n"
        "ds_read_b64_tr_b16 v[170:171], v233 offset:16384\n"
        "v_accvgpr_read_b32 v181, a185\n"
        "v_mfma_f32_16x16x32_bf16 v[92:95], v[40:43], v[196:199], v[92:95]\n"
        "ds_read_b128 v[192:195], v246 offset:34816\n"
        "v_accvgpr_read_b32 v182, a186\n"
        "v_mfma_f32_16x16x32_bf16 v[92:95], v[44:47], v[212:215], v[92:95]\n"
        "ds_read_b128 v[208:211], v247 offset:34816\n"
        "global_load_lds_dwordx4 v244, s[64:65] offset:2048\n"
        "v_mfma_f32_16x16x32_bf16 v[88:91], v[36:39], v[212:215], v[88:91]\n"
        "ds_read_b128 v[212:215], v247 offset:32768\n"
        "v_accvgpr_read_b32 v183, a187\n"
        "v_mfma_f32_16x16x32_bf16 v[88:91], v[32:35], v[196:199], v[88:91]\n"
        "ds_read_b128 v[196:199], v246 offset:32768\n"
        "v_mfma_f32_16x16x32_bf16 v[120:123], v[32:35], v[200:203], v[120:123]\n"
        "v_mfma_f32_16x16x32_bf16 v[120:123], v[36:39], v[216:219], v[120:123]\n"
        "global_load_lds_dwordx4 v245, s[64:65] offset:3072\n"
        "s_add_u32 m0, s72, 0x11000\n"
        "v_mfma_f32_16x16x32_bf16 v[124:127], v[44:47], v[216:219], v[124:127]\n"
        "ds_read_b128 v[216:219], v247 offset:38912\n"
        "s_add_u32 s64, s64, 0x80\n"
        "s_addc_u32 s65, s65, 0\n"
        "v_mfma_f32_16x16x32_bf16 v[124:127], v[40:43], v[200:203], v[124:127]\n"
        "ds_read_b128 v[200:203], v246 offset:38912\n"
        "s_waitcnt lgkmcnt(6)\n"
        "v_mfma_f32_16x16x32_bf16 v[156:159], v[40:43], v[204:207], v[156:159]\n"
        "ds_read_b64_tr_b16 v[40:41], v228 offset:17408\n"
        "s_waitcnt lgkmcnt(6)\n"
        "v_mfma_f32_16x16x32_bf16 v[156:159], v[44:47], v[220:223], v[156:159]\n"
        "global_load_lds_dwordx4 v242, s[66:67] offset:0\n"
        "v_mfma_f32_16x16x32_bf16 v[152:155], v[36:39], v[220:223], v[152:155]\n"
        "ds_read_b128 v[220:223], v247 offset:36864\n"
        "s_waitcnt lgkmcnt(5)\n"
        "v_mfma_f32_16x16x32_bf16 v[152:155], v[32:35], v[204:207], v[152:155]\n"
        "ds_read_b128 v[204:207], v246 offset:36864\n"
        "v_accvgpr_read_b32 v184, a148\n"
        "v_mfma_f32_16x16x32_bf16 v[0:3], v[48:51], v[192:195], v[0:3]\n"
        "s_waitcnt lgkmcnt(5)\n"
        "v_cvt_pk_bf16_f32 v184, v184, v185\n"
        "v_cvt_pk_bf16_f32 v185, v186, v187\n"
        "v_mfma_f32_16x16x32_bf16 v[0:3], v[52:55], v[208:211], v[0:3]\n"
        "ds_read_b64_tr_b16 v[32:33], v226 offset:17408\n"
        "v_cvt_pk_bf16_f32 v186, v188, v189\n"
        "v_mfma_f32_16x16x32_bf16 v[4:7], v[60:63], v[208:211], v[4:7]\n"
        "s_waitcnt lgkmcnt(5)\n"
        "v_cvt_pk_bf16_f32 v187, v190, v191\n"
        "v_accvgpr_write_b32 a248, v184\n"
        "v_mfma_f32_16x16x32_bf16 v[4:7], v[56:59], v[192:195], v[4:7]\n"
        "s_waitcnt lgkmcnt(4)\n"
        "v_accvgpr_write_b32 a249, v185\n"
        "v_accvgpr_write_b32 a250, v186\n"
        "v_mfma_f32_16x16x32_bf16 v[68:71], v[56:59], v[196:199], v[68:71]\n"
        "ds_read_b64_tr_b16 v[34:35], v227 offset:17408\n"
        "v_accvgpr_write_b32 a251, v187\n"
        "v_mfma_f32_16x16x32_bf16 v[68:71], v[60:63], v[212:215], v[68:71]\n"
        "ds_read_b64_tr_b16 v[36:37], v226 offset:25600\n"
        "global_load_lds_dwordx4 v243, s[66:67] offset:1024\n"
        "v_mfma_f32_16x16x32_bf16 v[64:67], v[52:55], v[212:215], v[64:67]\n"
        "ds_read_b64_tr_b16 v[38:39], v227 offset:25600\n"
        "v_accvgpr_read_b32 v185, a157\n"
        "v_mfma_f32_16x16x32_bf16 v[64:67], v[48:51], v[196:199], v[64:67]\n"
        "ds_read_b64_tr_b16 v[44:45], v228 offset:25600\n"
        "v_accvgpr_read_b32 v186, a158\n"
        "v_mfma_f32_16x16x32_bf16 v[96:99], v[48:51], v[200:203], v[96:99]\n"
        "ds_read_b64_tr_b16 v[46:47], v229 offset:25600\n"
        "v_accvgpr_read_b32 v187, a159\n"
        "v_mfma_f32_16x16x32_bf16 v[96:99], v[52:55], v[216:219], v[96:99]\n"
        "ds_read_b64_tr_b16 v[42:43], v229 offset:17408\n"
        "global_load_lds_dwordx4 v244, s[66:67] offset:2048\n"
        "v_mfma_f32_16x16x32_bf16 v[100:103], v[60:63], v[216:219], v[100:103]\n"
        "s_waitcnt lgkmcnt(6)\n"
        "v_accvgpr_read_b32 v188, a188\n"
        "v_accvgpr_read_b32 v189, a189\n"
        "v_mfma_f32_16x16x32_bf16 v[100:103], v[56:59], v[200:203], v[100:103]\n"
        "v_accvgpr_read_b32 v190, a190\n"
        "v_accvgpr_read_b32 v191, a191\n"
        "v_mfma_f32_16x16x32_bf16 v[132:135], v[56:59], v[204:207], v[132:135]\n"
        "ds_read_b64_tr_b16 v[56:57], v232 offset:17408\n"
        "v_mfma_f32_16x16x32_bf16 v[132:135], v[60:63], v[220:223], v[132:135]\n"
        "global_load_lds_dwordx4 v245, s[66:67] offset:3072\n"
        "s_add_u32 m0, s73, 0x1c000\n"
        "v_mfma_f32_16x16x32_bf16 v[128:131], v[52:55], v[220:223], v[128:131]\n"
        "ds_read_b64_tr_b16 v[52:53], v230 offset:25600\n"
        "s_add_u32 s66, s66, 0x80\n"
        "s_addc_u32 s67, s67, 0\n"
        "v_mfma_f32_16x16x32_bf16 v[128:131], v[48:51], v[204:207], v[128:131]\n"
        "ds_read_b64_tr_b16 v[48:49], v230 offset:17408\n"
        "v_mfma_f32_16x16x32_bf16 v[8:11], v[160:163], v[192:195], v[8:11]\n"
        "ds_read_b64_tr_b16 v[50:51], v231 offset:17408\n"
        "v_mfma_f32_16x16x32_bf16 v[8:11], v[164:167], v[208:211], v[8:11]\n"
        "ds_read_b64_tr_b16 v[54:55], v231 offset:25600\n"
        "v_mfma_f32_16x16x32_bf16 v[12:15], v[172:175], v[208:211], v[12:15]\n"
        "ds_read_b64_tr_b16 v[60:61], v232 offset:25600\n"
        "v_mfma_f32_16x16x32_bf16 v[12:15], v[168:171], v[192:195], v[12:15]\n"
        "ds_read_b64_tr_b16 v[62:63], v233 offset:25600\n"
        "v_mfma_f32_16x16x32_bf16 v[76:79], v[168:171], v[196:199], v[76:79]\n"
        "ds_read_b64_tr_b16 v[58:59], v233 offset:17408\n"
        "v_mfma_f32_16x16x32_bf16 v[76:79], v[172:175], v[212:215], v[76:79]\n"
        "global_store_dwordx4 v254, a[204:207], s[70:71] offset:1024 sc1\n"
        "s_add_u32 s70, s38, s97\n"
        "s_addc_u32 s71, s39, 0\n"
        "s_waitcnt lgkmcnt(7)\n"
        "v_mfma_f32_16x16x32_bf16 v[72:75], v[164:167], v[212:215], v[72:75]\n"
        "v_mfma_f32_16x16x32_bf16 v[72:75], v[160:163], v[196:199], v[72:75]\n"
        "v_accvgpr_read_b32 v176, a152\n"
        "v_cvt_pk_bf16_f32 v176, v176, v177\n"
        "v_mfma_f32_16x16x32_bf16 v[104:107], v[160:163], v[200:203], v[104:107]\n"
        "v_cvt_pk_bf16_f32 v177, v178, v179\n"
        "v_cvt_pk_bf16_f32 v178, v180, v181\n"
        "v_mfma_f32_16x16x32_bf16 v[104:107], v[164:167], v[216:219], v[104:107]\n"
        "v_cvt_pk_bf16_f32 v179, v182, v183\n"
        "v_accvgpr_write_b32 a220, v176\n"
        "v_mfma_f32_16x16x32_bf16 v[108:111], v[172:175], v[216:219], v[108:111]\n"
        "v_accvgpr_write_b32 a221, v177\n"
        "v_accvgpr_write_b32 a222, v178\n"
        "v_mfma_f32_16x16x32_bf16 v[108:111], v[168:171], v[200:203], v[108:111]\n"
        "s_waitcnt vmcnt(14) lgkmcnt(0)\n"
        "v_accvgpr_write_b32 a223, v179\n"
        "v_mfma_f32_16x16x32_bf16 v[140:143], v[168:171], v[204:207], v[140:143]\n"
        "s_barrier\n"
        "global_load_lds_dwordx4 v238, s[68:69] offset:0\n"
        "v_mfma_f32_16x16x32_bf16 v[140:143], v[172:175], v[220:223], v[140:143]\n"
        "ds_read_b64_tr_b16 v[172:173], v228 offset:40960\n"
        "v_accvgpr_read_b32 v177, a17\n"
        "v_mfma_f32_16x16x32_bf16 v[136:139], v[164:167], v[220:223], v[136:139]\n"
        "ds_read_b64_tr_b16 v[164:165], v226 offset:40960\n"
        "v_accvgpr_read_b32 v178, a18\n"
        "v_mfma_f32_16x16x32_bf16 v[136:139], v[160:163], v[204:207], v[136:139]\n"
        "ds_read_b64_tr_b16 v[160:161], v226 offset:32768\n"
        "s_add_u32 s97, s52, 0x2000\n"
        "v_accvgpr_read_b32 v179, a19\n"
        "v_mfma_f32_16x16x32_bf16 v[16:19], v[32:35], v[192:195], v[16:19]\n"
        "ds_read_b64_tr_b16 v[162:163], v227 offset:32768\n"
        "global_load_lds_dwordx4 v239, s[68:69] offset:1024\n"
        "v_mfma_f32_16x16x32_bf16 v[16:19], v[36:39], v[208:211], v[16:19]\n"
        "ds_read_b64_tr_b16 v[166:167], v227 offset:40960\n"
        "v_accvgpr_read_b32 v180, a48\n"
        "v_mfma_f32_16x16x32_bf16 v[20:23], v[44:47], v[208:211], v[20:23]\n"
        "ds_read_b64_tr_b16 v[174:175], v229 offset:40960\n"
        "v_accvgpr_read_b32 v181, a49\n"
        "v_mfma_f32_16x16x32_bf16 v[20:23], v[40:43], v[192:195], v[20:23]\n"
        "ds_read_b64_tr_b16 v[168:169], v228 offset:32768\n"
        "v_accvgpr_read_b32 v182, a50\n"
        "v_mfma_f32_16x16x32_bf16 v[84:87], v[40:43], v[196:199], v[84:87]\n"
        "ds_read_b64_tr_b16 v[170:171], v229 offset:32768\n"
        "global_load_lds_dwordx4 v240, s[68:69] offset:2048\n"
        "v_mfma_f32_16x16x32_bf16 v[84:87], v[44:47], v[212:215], v[84:87]\n"
        "v_accvgpr_read_b32 v183, a51\n"
        "v_mfma_f32_16x16x32_bf16 v[80:83], v[36:39], v[212:215], v[80:83]\n"
        "v_mfma_f32_16x16x32_bf16 v[80:83], v[32:35], v[196:199], v[80:83]\n"
        "v_mfma_f32_16x16x32_bf16 v[112:115], v[32:35], v[200:203], v[112:115]\n"
        "global_load_lds_dwordx4 v241, s[68:69] offset:3072\n"
        "s_add_u32 m0, s72, 0x8000\n"
        "v_mfma_f32_16x16x32_bf16 v[112:115], v[36:39], v[216:219], v[112:115]\n"
        "s_add_u32 s68, s68, 0x80000\n"
        "s_addc_u32 s69, s69, 0\n"
        "v_mfma_f32_16x16x32_bf16 v[116:119], v[44:47], v[216:219], v[116:119]\n"
        "v_mfma_f32_16x16x32_bf16 v[116:119], v[40:43], v[200:203], v[116:119]\n"
        "v_mfma_f32_16x16x32_bf16 v[148:151], v[40:43], v[204:207], v[148:151]\n"
        "global_load_lds_dwordx4 v242, s[64:65] offset:0\n"
        "v_mfma_f32_16x16x32_bf16 v[148:151], v[44:47], v[220:223], v[148:151]\n"
        "ds_read_b64_tr_b16 v[44:45], v232 offset:40960\n"
        "v_mfma_f32_16x16x32_bf16 v[144:147], v[36:39], v[220:223], v[144:147]\n"
        "ds_read_b64_tr_b16 v[36:37], v230 offset:40960\n"
        "v_mfma_f32_16x16x32_bf16 v[144:147], v[32:35], v[204:207], v[144:147]\n"
        "ds_read_b64_tr_b16 v[32:33], v230 offset:32768\n"
        "v_accvgpr_read_b32 v184, a156\n"
        "v_mfma_f32_16x16x32_bf16 v[24:27], v[48:51], v[192:195], v[24:27]\n"
        "ds_read_b64_tr_b16 v[34:35], v231 offset:32768\n"
        "v_cvt_pk_bf16_f32 v184, v184, v185\n"
        "v_mfma_f32_16x16x32_bf16 v[24:27], v[52:55], v[208:211], v[24:27]\n"
        "ds_read_b64_tr_b16 v[38:39], v231 offset:40960\n"
        "v_cvt_pk_bf16_f32 v185, v186, v187\n"
        "v_mfma_f32_16x16x32_bf16 v[28:31], v[60:63], v[208:211], v[28:31]\n"
        "ds_read_b64_tr_b16 v[46:47], v233 offset:40960\n"
        "v_cvt_pk_bf16_f32 v186, v188, v189\n"
        "v_mfma_f32_16x16x32_bf16 v[28:31], v[56:59], v[192:195], v[28:31]\n"
        "ds_read_b64_tr_b16 v[40:41], v232 offset:32768\n"
        "v_cvt_pk_bf16_f32 v187, v190, v191\n"
        "v_mfma_f32_16x16x32_bf16 v[92:95], v[56:59], v[196:199], v[92:95]\n"
        "ds_read_b64_tr_b16 v[42:43], v233 offset:32768\n"
        "v_accvgpr_write_b32 a252, v184\n"
        "v_mfma_f32_16x16x32_bf16 v[92:95], v[60:63], v[212:215], v[92:95]\n"
        "ds_read_b128 v[192:195], v246 offset:2048\n"
        "v_accvgpr_write_b32 a253, v185\n"
        "v_mfma_f32_16x16x32_bf16 v[88:91], v[52:55], v[212:215], v[88:91]\n"
        "ds_read_b128 v[208:211], v247 offset:2048\n"
        "v_accvgpr_write_b32 a254, v186\n"
        "v_mfma_f32_16x16x32_bf16 v[88:91], v[48:51], v[196:199], v[88:91]\n"
        "global_load_lds_dwordx4 v243, s[64:65] offset:1024\n"
        "v_accvgpr_write_b32 a255, v187\n"
        "v_mfma_f32_16x16x32_bf16 v[120:123], v[48:51], v[200:203], v[120:123]\n"
        "ds_read_b128 v[196:199], v246 offset:0\n"
        "v_accvgpr_read_b32 v185, a21\n"
        "v_mfma_f32_16x16x32_bf16 v[120:123], v[52:55], v[216:219], v[120:123]\n"
        "ds_read_b128 v[212:215], v247 offset:0\n"
        "v_accvgpr_read_b32 v186, a22\n"
        "v_mfma_f32_16x16x32_bf16 v[124:127], v[60:63], v[216:219], v[124:127]\n"
        "ds_read_b128 v[216:219], v247 offset:6144\n"
        "v_accvgpr_read_b32 v187, a23\n"
        "v_mfma_f32_16x16x32_bf16 v[124:127], v[56:59], v[200:203], v[124:127]\n"
        "global_load_lds_dwordx4 v244, s[64:65] offset:2048\n"
        "ds_read_b128 v[200:203], v246 offset:6144\n"
        "v_mfma_f32_16x16x32_bf16 v[156:159], v[56:59], v[204:207], v[156:159]\n"
        "ds_read_b64_tr_b16 v[56:57], v228 offset:33792\n"
        "v_accvgpr_read_b32 v188, a52\n"
        "v_mfma_f32_16x16x32_bf16 v[156:159], v[60:63], v[220:223], v[156:159]\n"
        "ds_read_b64_tr_b16 v[60:61], v228 offset:41984\n"
        "v_accvgpr_read_b32 v189, a53\n"
        "v_mfma_f32_16x16x32_bf16 v[152:155], v[52:55], v[220:223], v[152:155]\n"
        "ds_read_b128 v[220:223], v247 offset:4096\n"
        "v_accvgpr_read_b32 v190, a54\n"
        "v_mfma_f32_16x16x32_bf16 v[152:155], v[48:51], v[204:207], v[152:155]\n"
        "global_load_lds_dwordx4 v245, s[64:65] offset:3072\n"
        "s_add_u32 m0, s72, 0x9000\n"
        "s_waitcnt lgkmcnt(7)\n"
        "v_mfma_f32_16x16x32_bf16 v[0:3], v[160:163], v[192:195], v[0:3]\n"
        "ds_read_b128 v[204:207], v246 offset:4096\n"
        "s_add_u32 s64, s64, 0x80\n"
        "s_addc_u32 s65, s65, 0\n"
        "v_mfma_f32_16x16x32_bf16 v[0:3], v[164:167], v[208:211], v[0:3]\n"
        "ds_read_b64_tr_b16 v[48:49], v226 offset:33792\n"
        "v_accvgpr_read_b32 v191, a55\n"
        "v_mfma_f32_16x16x32_bf16 v[4:7], v[172:175], v[208:211], v[4:7]\n"
        "ds_read_b64_tr_b16 v[50:51], v227 offset:33792\n"
        "s_waitcnt lgkmcnt(7)\n"
        "v_mfma_f32_16x16x32_bf16 v[4:7], v[168:171], v[192:195], v[4:7]\n"
        "ds_read_b64_tr_b16 v[52:53], v226 offset:41984\n"
        "global_load_lds_dwordx4 v242, s[66:67] offset:0\n"
        "v_mfma_f32_16x16x32_bf16 v[68:71], v[168:171], v[196:199], v[68:71]\n"
        "ds_read_b64_tr_b16 v[54:55], v227 offset:41984\n"
        "s_waitcnt lgkmcnt(7)\n"
        "v_mfma_f32_16x16x32_bf16 v[68:71], v[172:175], v[212:215], v[68:71]\n"
        "ds_read_b64_tr_b16 v[62:63], v229 offset:41984\n"
        "s_waitcnt lgkmcnt(7)\n"
        "v_mfma_f32_16x16x32_bf16 v[64:67], v[164:167], v[212:215], v[64:67]\n"
        "ds_read_b64_tr_b16 v[58:59], v229 offset:33792\n"
        "s_waitcnt lgkmcnt(7)\n"
        "v_mfma_f32_16x16x32_bf16 v[64:67], v[160:163], v[196:199], v[64:67]\n"
        "global_load_lds_dwordx4 v243, s[66:67] offset:1024\n"
        "v_mfma_f32_16x16x32_bf16 v[96:99], v[160:163], v[200:203], v[96:99]\n"
        "s_waitcnt lgkmcnt(6)\n"
        "v_accvgpr_read_b32 v176, a16\n"
        "v_cvt_pk_bf16_f32 v176, v176, v177\n"
        "v_mfma_f32_16x16x32_bf16 v[96:99], v[164:167], v[216:219], v[96:99]\n"
        "v_cvt_pk_bf16_f32 v177, v178, v179\n"
        "v_cvt_pk_bf16_f32 v178, v180, v181\n"
        "v_mfma_f32_16x16x32_bf16 v[100:103], v[172:175], v[216:219], v[100:103]\n"
        "v_cvt_pk_bf16_f32 v179, v182, v183\n"
        "v_accvgpr_write_b32 a176, v176\n"
        "v_mfma_f32_16x16x32_bf16 v[100:103], v[168:171], v[200:203], v[100:103]\n"
        "v_accvgpr_write_b32 a177, v177\n"
        "v_accvgpr_write_b32 a178, v178\n"
        "v_mfma_f32_16x16x32_bf16 v[132:135], v[168:171], v[204:207], v[132:135]\n"
        "global_load_lds_dwordx4 v244, s[66:67] offset:2048\n"
        "v_accvgpr_write_b32 a179, v179\n"
        "v_mfma_f32_16x16x32_bf16 v[132:135], v[172:175], v[220:223], v[132:135]\n"
        "ds_read_b64_tr_b16 v[172:173], v232 offset:41984\n"
        "v_accvgpr_read_b32 v177, a25\n"
        "v_mfma_f32_16x16x32_bf16 v[128:131], v[164:167], v[220:223], v[128:131]\n"
        "ds_read_b64_tr_b16 v[164:165], v230 offset:41984\n"
        "v_accvgpr_read_b32 v178, a26\n"
        "v_mfma_f32_16x16x32_bf16 v[128:131], v[160:163], v[204:207], v[128:131]\n"
        "ds_read_b64_tr_b16 v[160:161], v230 offset:33792\n"
        "v_accvgpr_read_b32 v179, a27\n"
        "v_mfma_f32_16x16x32_bf16 v[8:11], v[32:35], v[192:195], v[8:11]\n"
        "global_load_lds_dwordx4 v245, s[66:67] offset:3072\n"
        "ds_read_b64_tr_b16 v[162:163], v231 offset:33792\n"
        "s_add_u32 m0, s73, 0x20000\n"
        "v_mfma_f32_16x16x32_bf16 v[8:11], v[36:39], v[208:211], v[8:11]\n"
        "ds_read_b64_tr_b16 v[166:167], v231 offset:41984\n"
        "s_add_u32 s66, s66, 0x80\n"
        "s_addc_u32 s67, s67, 0\n"
        "v_mfma_f32_16x16x32_bf16 v[12:15], v[44:47], v[208:211], v[12:15]\n"
        "ds_read_b64_tr_b16 v[174:175], v233 offset:41984\n"
        "v_accvgpr_read_b32 v180, a56\n"
        "v_mfma_f32_16x16x32_bf16 v[12:15], v[40:43], v[192:195], v[12:15]\n"
        "ds_read_b64_tr_b16 v[168:169], v232 offset:33792\n"
        "v_accvgpr_read_b32 v181, a57\n"
        "v_mfma_f32_16x16x32_bf16 v[76:79], v[40:43], v[196:199], v[76:79]\n"
        "ds_read_b64_tr_b16 v[170:171], v233 offset:33792\n"
        "v_accvgpr_read_b32 v182, a58\n"
        "v_mfma_f32_16x16x32_bf16 v[76:79], v[44:47], v[212:215], v[76:79]\n"
        "s_waitcnt lgkmcnt(7)\n"
        "v_accvgpr_read_b32 v183, a59\n"
        "v_mfma_f32_16x16x32_bf16 v[72:75], v[36:39], v[212:215], v[72:75]\n"
        "v_mfma_f32_16x16x32_bf16 v[72:75], v[32:35], v[196:199], v[72:75]\n"
        "v_mfma_f32_16x16x32_bf16 v[104:107], v[32:35], v[200:203], v[104:107]\n"
        "v_mfma_f32_16x16x32_bf16 v[104:107], v[36:39], v[216:219], v[104:107]\n"
        "v_mfma_f32_16x16x32_bf16 v[108:111], v[44:47], v[216:219], v[108:111]\n"
        "v_mfma_f32_16x16x32_bf16 v[108:111], v[40:43], v[200:203], v[108:111]\n"
        "global_store_dwordx4 v254, a[160:163], s[70:71] offset:0 sc1\n"
        "s_waitcnt vmcnt(14) lgkmcnt(0)\n"
        "s_barrier\n"
        "v_mfma_f32_16x16x32_bf16 v[140:143], v[40:43], v[204:207], v[140:143]\n"
        "ds_read_b64_tr_b16 v[40:41], v228 offset:49152\n"
        "v_mfma_f32_16x16x32_bf16 v[140:143], v[44:47], v[220:223], v[140:143]\n"
        "ds_read_b64_tr_b16 v[44:45], v228 offset:57344\n"
        "v_mfma_f32_16x16x32_bf16 v[136:139], v[36:39], v[220:223], v[136:139]\n"
        "global_load_lds_dwordx4 v238, s[68:69] offset:0\n"
        "v_mfma_f32_16x16x32_bf16 v[136:139], v[32:35], v[204:207], v[136:139]\n"
        "ds_read_b64_tr_b16 v[32:33], v226 offset:49152\n"
        "v_accvgpr_read_b32 v184, a20\n"
        "v_mfma_f32_16x16x32_bf16 v[16:19], v[48:51], v[192:195], v[16:19]\n"
        "ds_read_b64_tr_b16 v[34:35], v227 offset:49152\n"
        "v_cvt_pk_bf16_f32 v184, v184, v185\n"
        "v_mfma_f32_16x16x32_bf16 v[16:19], v[52:55], v[208:211], v[16:19]\n"
        "ds_read_b64_tr_b16 v[36:37], v226 offset:57344\n"
        "v_cvt_pk_bf16_f32 v185, v186, v187\n"
        "v_mfma_f32_16x16x32_bf16 v[20:23], v[60:63], v[208:211], v[20:23]\n"
        "ds_read_b64_tr_b16 v[38:39], v227 offset:57344\n"
        "v_cvt_pk_bf16_f32 v186, v188, v189\n"
        "v_mfma_f32_16x16x32_bf16 v[20:23], v[56:59], v[192:195], v[20:23]\n"
        "ds_read_b64_tr_b16 v[46:47], v229 offset:57344\n"
        "v_cvt_pk_bf16_f32 v187, v190, v191\n"
        "v_mfma_f32_16x16x32_bf16 v[84:87], v[56:59], v[196:199], v[84:87]\n"
        "ds_read_b64_tr_b16 v[42:43], v229 offset:49152\n"
        "v_accvgpr_write_b32 a180, v184\n"
        "v_mfma_f32_16x16x32_bf16 v[84:87], v[60:63], v[212:215], v[84:87]\n"
        "global_load_lds_dwordx4 v239, s[68:69] offset:1024\n"
        "v_accvgpr_write_b32 a181, v185\n"
        "v_mfma_f32_16x16x32_bf16 v[80:83], v[52:55], v[212:215], v[80:83]\n"
        "LHA8_%=:\n"
        "s_cbranch_execz LHA12_%=\n"
        "v_accvgpr_write_b32 a182, v186\n"
        "v_accvgpr_write_b32 a183, v187\n"
        "v_mfma_f32_16x16x32_bf16 v[80:83], v[48:51], v[196:199], v[80:83]\n"
        "v_accvgpr_read_b32 v185, a29\n"
        "v_accvgpr_read_b32 v186, a30\n"
        "v_mfma_f32_16x16x32_bf16 v[112:115], v[48:51], v[200:203], v[112:115]\n"
        "v_accvgpr_read_b32 v187, a31\n"
        "v_accvgpr_read_b32 v188, a60\n"
        "v_mfma_f32_16x16x32_bf16 v[112:115], v[52:55], v[216:219], v[112:115]\n"
        "global_load_lds_dwordx4 v240, s[68:69] offset:2048\n"
        "v_accvgpr_read_b32 v189, a61\n"
        "v_mfma_f32_16x16x32_bf16 v[116:119], v[60:63], v[216:219], v[116:119]\n"
        "v_accvgpr_read_b32 v190, a62\n"
        "v_accvgpr_read_b32 v191, a63\n"
        "v_mfma_f32_16x16x32_bf16 v[116:119], v[56:59], v[200:203], v[116:119]\n"
        "v_mfma_f32_16x16x32_bf16 v[148:151], v[56:59], v[204:207], v[148:151]\n"
        "ds_read_b64_tr_b16 v[56:57], v232 offset:49152\n"
        "v_mfma_f32_16x16x32_bf16 v[148:151], v[60:63], v[220:223], v[148:151]\n"
        "global_load_lds_dwordx4 v241, s[68:69] offset:3072\n"
        "ds_read_b64_tr_b16 v[60:61], v232 offset:57344\n"
        "s_add_u32 m0, s72, 0x0\n"
        "v_mfma_f32_16x16x32_bf16 v[144:147], v[52:55], v[220:223], v[144:147]\n"
        "ds_read_b64_tr_b16 v[52:53], v230 offset:57344\n"
        "s_add_u32 s68, s68, 0x80000\n"
        "s_addc_u32 s69, s69, 0\n"
        "v_mfma_f32_16x16x32_bf16 v[144:147], v[48:51], v[204:207], v[144:147]\n"
        "ds_read_b64_tr_b16 v[48:49], v230 offset:49152\n"
        "v_mfma_f32_16x16x32_bf16 v[24:27], v[160:163], v[192:195], v[24:27]\n"
        "ds_read_b64_tr_b16 v[50:51], v231 offset:49152\n"
        "v_mfma_f32_16x16x32_bf16 v[24:27], v[164:167], v[208:211], v[24:27]\n"
        "ds_read_b64_tr_b16 v[54:55], v231 offset:57344\n"
        "global_load_lds_dwordx4 v242, s[64:65] offset:0\n"
        "v_mfma_f32_16x16x32_bf16 v[28:31], v[172:175], v[208:211], v[28:31]\n"
        "ds_read_b64_tr_b16 v[62:63], v233 offset:57344\n"
        "v_mfma_f32_16x16x32_bf16 v[28:31], v[168:171], v[192:195], v[28:31]\n"
        "ds_read_b64_tr_b16 v[58:59], v233 offset:49152\n"
        "v_mfma_f32_16x16x32_bf16 v[92:95], v[168:171], v[196:199], v[92:95]\n"
        "ds_read_b128 v[192:195], v248 offset:2048\n"
        "v_mfma_f32_16x16x32_bf16 v[92:95], v[172:175], v[212:215], v[92:95]\n"
        "ds_read_b128 v[208:211], v249 offset:2048\n"
        "global_load_lds_dwordx4 v243, s[64:65] offset:1024\n"
        "v_mfma_f32_16x16x32_bf16 v[88:91], v[164:167], v[212:215], v[88:91]\n"
        "ds_read_b128 v[212:215], v249 offset:0\n"
        "v_mfma_f32_16x16x32_bf16 v[88:91], v[160:163], v[196:199], v[88:91]\n"
        "ds_read_b128 v[196:199], v248 offset:0\n"
        "v_accvgpr_read_b32 v176, a24\n"
        "v_mfma_f32_16x16x32_bf16 v[120:123], v[160:163], v[200:203], v[120:123]\n"
        "v_cvt_pk_bf16_f32 v176, v176, v177\n"
        "v_cvt_pk_bf16_f32 v177, v178, v179\n"
        "v_mfma_f32_16x16x32_bf16 v[120:123], v[164:167], v[216:219], v[120:123]\n"
        "v_cvt_pk_bf16_f32 v178, v180, v181\n"
        "v_cvt_pk_bf16_f32 v179, v182, v183\n"
        "v_mfma_f32_16x16x32_bf16 v[124:127], v[172:175], v[216:219], v[124:127]\n"
        "global_load_lds_dwordx4 v244, s[64:65] offset:2048\n"
        "v_accvgpr_write_b32 a184, v176\n"
        "v_mfma_f32_16x16x32_bf16 v[124:127], v[168:171], v[200:203], v[124:127]\n"
        "s_waitcnt lgkmcnt(4)\n"
        "v_accvgpr_write_b32 a185, v177\n"
        "v_accvgpr_write_b32 a186, v178\n"
        "v_mfma_f32_16x16x32_bf16 v[156:159], v[168:171], v[204:207], v[156:159]\n"
        "s_waitcnt lgkmcnt(3)\n"
        "v_accvgpr_write_b32 a187, v179\n"
        "v_accvgpr_read_b32 v177, a81\n"
        "v_mfma_f32_16x16x32_bf16 v[156:159], v[172:175], v[220:223], v[156:159]\n"
        "ds_read_b128 v[200:203], v248 offset:6144\n"
        "v_accvgpr_read_b32 v178, a82\n"
        "v_mfma_f32_16x16x32_bf16 v[152:155], v[164:167], v[220:223], v[152:155]\n"
        "ds_read_b128 v[216:219], v249 offset:6144\n"
        "global_load_lds_dwordx4 v245, s[64:65] offset:3072\n"
        "s_add_u32 m0, s72, 0x1000\n"
        "v_mfma_f32_16x16x32_bf16 v[152:155], v[160:163], v[204:207], v[152:155]\n"
        "ds_read_b128 v[204:207], v248 offset:4096\n"
        "s_add_u32 s64, s64, 0x80\n"
        "s_addc_u32 s65, s65, 0\n"
        "v_mfma_f32_16x16x32_bf16 v[0:3], v[32:35], v[192:195], v[0:3]\n"
        "ds_read_b128 v[220:223], v249 offset:4096\n"
        "s_waitcnt lgkmcnt(4)\n"
        "v_mfma_f32_16x16x32_bf16 v[0:3], v[36:39], v[208:211], v[0:3]\n"
        "ds_read_b64_tr_b16 v[160:161], v226 offset:50176\n"
        "v_accvgpr_read_b32 v179, a83\n"
        "v_mfma_f32_16x16x32_bf16 v[4:7], v[44:47], v[208:211], v[4:7]\n"
        "ds_read_b64_tr_b16 v[162:163], v227 offset:50176\n"
        "global_load_lds_dwordx4 v242, s[66:67] offset:0\n"
        "v_mfma_f32_16x16x32_bf16 v[4:7], v[40:43], v[192:195], v[4:7]\n"
        "ds_read_b64_tr_b16 v[164:165], v226 offset:58368\n"
        "v_accvgpr_read_b32 v180, a112\n"
        "v_mfma_f32_16x16x32_bf16 v[68:71], v[40:43], v[196:199], v[68:71]\n"
        "ds_read_b64_tr_b16 v[166:167], v227 offset:58368\n"
        "v_accvgpr_read_b32 v181, a113\n"
        "v_mfma_f32_16x16x32_bf16 v[68:71], v[44:47], v[212:215], v[68:71]\n"
        "ds_read_b64_tr_b16 v[172:173], v228 offset:58368\n"
        "v_accvgpr_read_b32 v182, a114\n"
        "v_mfma_f32_16x16x32_bf16 v[64:67], v[36:39], v[212:215], v[64:67]\n"
        "ds_read_b64_tr_b16 v[174:175], v229 offset:58368\n"
        "global_load_lds_dwordx4 v243, s[66:67] offset:1024\n"
        "v_mfma_f32_16x16x32_bf16 v[64:67], v[32:35], v[196:199], v[64:67]\n"
        "ds_read_b64_tr_b16 v[168:169], v228 offset:50176\n"
        "s_waitcnt lgkmcnt(8)\n"
        "v_mfma_f32_16x16x32_bf16 v[96:99], v[32:35], v[200:203], v[96:99]\n"
        "ds_read_b64_tr_b16 v[170:171], v229 offset:50176\n"
        "v_accvgpr_read_b32 v183, a115\n"
        "v_mfma_f32_16x16x32_bf16 v[96:99], v[36:39], v[216:219], v[96:99]\n"
        "s_waitcnt lgkmcnt(7)\n"
        "v_mfma_f32_16x16x32_bf16 v[100:103], v[44:47], v[216:219], v[100:103]\n"
        "global_load_lds_dwordx4 v244, s[66:67] offset:2048\n"
        "v_mfma_f32_16x16x32_bf16 v[100:103], v[40:43], v[200:203], v[100:103]\n"
        "v_mfma_f32_16x16x32_bf16 v[132:135], v[40:43], v[204:207], v[132:135]\n"
        "ds_read_b64_tr_b16 v[40:41], v232 offset:50176\n"
        "v_mfma_f32_16x16x32_bf16 v[132:135], v[44:47], v[220:223], v[132:135]\n"
        "ds_read_b64_tr_b16 v[44:45], v232 offset:58368\n"
        "v_mfma_f32_16x16x32_bf16 v[128:131], v[36:39], v[220:223], v[128:131]\n"
        "global_load_lds_dwordx4 v245, s[66:67] offset:3072\n"
        "s_add_u32 m0, s73, 0x24000\n"
        "v_mfma_f32_16x16x32_bf16 v[128:131], v[32:35], v[204:207], v[128:131]\n"
        "ds_read_b64_tr_b16 v[32:33], v230 offset:50176\n"
        "v_accvgpr_read_b32 v184, a28\n"
        "v_mfma_f32_16x16x32_bf16 v[8:11], v[48:51], v[192:195], v[8:11]\n"
        "ds_read_b64_tr_b16 v[34:35], v231 offset:50176\n"
        "v_cvt_pk_bf16_f32 v184, v184, v185\n"
        "v_mfma_f32_16x16x32_bf16 v[8:11], v[52:55], v[208:211], v[8:11]\n"
        "ds_read_b64_tr_b16 v[36:37], v230 offset:58368\n"
        "v_cvt_pk_bf16_f32 v185, v186, v187\n"
        "v_mfma_f32_16x16x32_bf16 v[12:15], v[60:63], v[208:211], v[12:15]\n"
        "ds_read_b64_tr_b16 v[38:39], v231 offset:58368\n"
        "v_cvt_pk_bf16_f32 v186, v188, v189\n"
        "v_mfma_f32_16x16x32_bf16 v[12:15], v[56:59], v[192:195], v[12:15]\n"
        "ds_read_b64_tr_b16 v[46:47], v233 offset:58368\n"
        "v_cvt_pk_bf16_f32 v187, v190, v191\n"
        "v_mfma_f32_16x16x32_bf16 v[76:79], v[56:59], v[196:199], v[76:79]\n"
        "ds_read_b64_tr_b16 v[42:43], v233 offset:50176\n"
        "v_accvgpr_write_b32 a188, v184\n"
        "v_mfma_f32_16x16x32_bf16 v[76:79], v[60:63], v[212:215], v[76:79]\n"
        "s_waitcnt lgkmcnt(6)\n"
        "v_accvgpr_write_b32 a189, v185\n"
        "v_accvgpr_write_b32 a190, v186\n"
        "v_mfma_f32_16x16x32_bf16 v[72:75], v[52:55], v[212:215], v[72:75]\n"
        "s_add_u32 s66, s66, 0x80\n"
        "s_addc_u32 s67, s67, 0\n"
        "v_accvgpr_write_b32 a191, v187\n"
        "v_mfma_f32_16x16x32_bf16 v[72:75], v[48:51], v[196:199], v[72:75]\n"
        "v_accvgpr_read_b32 v185, a85\n"
        "v_accvgpr_read_b32 v186, a86\n"
        "v_mfma_f32_16x16x32_bf16 v[104:107], v[48:51], v[200:203], v[104:107]\n"
        "v_accvgpr_read_b32 v187, a87\n"
        "v_accvgpr_read_b32 v188, a116\n"
        "v_mfma_f32_16x16x32_bf16 v[104:107], v[52:55], v[216:219], v[104:107]\n"
        "v_accvgpr_read_b32 v189, a117\n"
        "v_accvgpr_read_b32 v190, a118\n"
        "v_mfma_f32_16x16x32_bf16 v[108:111], v[60:63], v[216:219], v[108:111]\n"
        "v_accvgpr_read_b32 v191, a119\n"
        "v_mfma_f32_16x16x32_bf16 v[108:111], v[56:59], v[200:203], v[108:111]\n"
        "v_mfma_f32_16x16x32_bf16 v[140:143], v[56:59], v[204:207], v[140:143]\n"
        "s_waitcnt vmcnt(13) lgkmcnt(0)\n"
        "s_barrier\n"
        "v_mfma_f32_16x16x32_bf16 v[140:143], v[60:63], v[220:223], v[140:143]\n"
        "global_load_lds_dwordx4 v238, s[68:69] offset:0\n"
        "ds_read_b64_tr_b16 v[60:61], v228 offset:24576\n"
        "v_mfma_f32_16x16x32_bf16 v[136:139], v[52:55], v[220:223], v[136:139]\n"
        "ds_read_b64_tr_b16 v[52:53], v226 offset:24576\n"
        "v_mfma_f32_16x16x32_bf16 v[136:139], v[48:51], v[204:207], v[136:139]\n"
        "ds_read_b64_tr_b16 v[48:49], v226 offset:16384\n"
        "v_mfma_f32_16x16x32_bf16 v[16:19], v[160:163], v[192:195], v[16:19]\n"
        "ds_read_b64_tr_b16 v[50:51], v227 offset:16384\n"
        "v_mfma_f32_16x16x32_bf16 v[16:19], v[164:167], v[208:211], v[16:19]\n"
        "global_store_dwordx4 v254, a[164:167], s[70:71] offset:1024 sc1\n"
        "ds_read_b64_tr_b16 v[54:55], v227 offset:24576\n"
        "ds_read_b64_tr_b16 v[62:63], v229 offset:24576\n"
        "s_add_u32 s70, s38, s97\n"
        "s_addc_u32 s71, s39, 0\n"
        "v_mfma_f32_16x16x32_bf16 v[20:23], v[172:175], v[208:211], v[20:23]\n"
        "ds_read_b64_tr_b16 v[56:57], v228 offset:16384\n"
        "s_add_u32 s97, s52, 0x3000\n"
        "v_mfma_f32_16x16x32_bf16 v[20:23], v[168:171], v[192:195], v[20:23]\n"
        "ds_read_b64_tr_b16 v[58:59], v229 offset:16384\n"
        "v_mfma_f32_16x16x32_bf16 v[84:87], v[168:171], v[196:199], v[84:87]\n"
        "global_load_lds_dwordx4 v239, s[68:69] offset:1024\n"
        "v_mfma_f32_16x16x32_bf16 v[84:87], v[172:175], v[212:215], v[84:87]\n"
        "v_mfma_f32_16x16x32_bf16 v[80:83], v[164:167], v[212:215], v[80:83]\n"
        "v_mfma_f32_16x16x32_bf16 v[80:83], v[160:163], v[196:199], v[80:83]\n"
        "v_accvgpr_read_b32 v176, a80\n"
        "v_cvt_pk_bf16_f32 v176, v176, v177\n"
        "v_mfma_f32_16x16x32_bf16 v[112:115], v[160:163], v[200:203], v[112:115]\n"
        "global_load_lds_dwordx4 v240, s[68:69] offset:2048\n"
        "v_mfma_f32_16x16x32_bf16 v[112:115], v[164:167], v[216:219], v[112:115]\n"
        "v_cvt_pk_bf16_f32 v177, v178, v179\n"
        "v_cvt_pk_bf16_f32 v178, v180, v181\n"
        "v_mfma_f32_16x16x32_bf16 v[116:119], v[172:175], v[216:219], v[116:119]\n"
        "v_cvt_pk_bf16_f32 v179, v182, v183\n"
        "v_accvgpr_write_b32 a16, v176\n"
        "v_mfma_f32_16x16x32_bf16 v[116:119], v[168:171], v[200:203], v[116:119]\n"
        "v_accvgpr_write_b32 a17, v177\n"
        "v_accvgpr_write_b32 a18, v178\n"
        "v_mfma_f32_16x16x32_bf16 v[148:151], v[168:171], v[204:207], v[148:151]\n"
        "ds_read_b64_tr_b16 v[168:169], v232 offset:16384\n"
        "v_accvgpr_write_b32 a19, v179\n"
        "v_mfma_f32_16x16x32_bf16 v[148:151], v[172:175], v[220:223], v[148:151]\n"
        "global_load_lds_dwordx4 v241, s[68:69] offset:3072\n"
        "ds_read_b64_tr_b16 v[172:173], v232 offset:24576\n"
        "s_add_u32 m0, s72, 0x10000\n"
        "v_mfma_f32_16x16x32_bf16 v[144:147], v[164:167], v[220:223], v[144:147]\n"
        "ds_read_b64_tr_b16 v[164:165], v230 offset:24576\n"
        "s_add_u32 s68, s68, 0x80000\n"
        "s_addc_u32 s69, s69, 0\n"
        "v_mfma_f32_16x16x32_bf16 v[144:147], v[160:163], v[204:207], v[144:147]\n"
        "ds_read_b64_tr_b16 v[160:161], v230 offset:16384\n"
        "v_accvgpr_read_b32 v177, a89\n"
        "v_mfma_f32_16x16x32_bf16 v[24:27], v[32:35], v[192:195], v[24:27]\n"
        "ds_read_b64_tr_b16 v[162:163], v231 offset:16384\n"
        "v_accvgpr_read_b32 v178, a90\n"
        "v_mfma_f32_16x16x32_bf16 v[24:27], v[36:39], v[208:211], v[24:27]\n"
        "ds_read_b64_tr_b16 v[166:167], v231 offset:24576\n"
        "global_load_lds_dwordx4 v242, s[64:65] offset:0\n"
        "v_mfma_f32_16x16x32_bf16 v[28:31], v[44:47], v[208:211], v[28:31]\n"
        "ds_read_b64_tr_b16 v[174:175], v233 offset:24576\n"
        "v_accvgpr_read_b32 v179, a91\n"
        "v_mfma_f32_16x16x32_bf16 v[28:31], v[40:43], v[192:195], v[28:31]\n"
        "ds_read_b64_tr_b16 v[170:171], v233 offset:16384\n"
        "v_accvgpr_read_b32 v180, a120\n"
        "v_mfma_f32_16x16x32_bf16 v[92:95], v[40:43], v[196:199], v[92:95]\n"
        "ds_read_b128 v[192:195], v246 offset:34816\n"
        "v_accvgpr_read_b32 v181, a121\n"
        "v_mfma_f32_16x16x32_bf16 v[92:95], v[44:47], v[212:215], v[92:95]\n"
        "ds_read_b128 v[208:211], v247 offset:34816\n"
        "global_load_lds_dwordx4 v243, s[64:65] offset:1024\n"
        "v_mfma_f32_16x16x32_bf16 v[88:91], v[36:39], v[212:215], v[88:91]\n"
        "ds_read_b128 v[212:215], v247 offset:32768\n"
        "v_accvgpr_read_b32 v182, a122\n"
        "v_mfma_f32_16x16x32_bf16 v[88:91], v[32:35], v[196:199], v[88:91]\n"
        "ds_read_b128 v[196:199], v246 offset:32768\n"
        "v_accvgpr_read_b32 v183, a123\n"
        "v_mfma_f32_16x16x32_bf16 v[120:123], v[32:35], v[200:203], v[120:123]\n"
        "v_mfma_f32_16x16x32_bf16 v[120:123], v[36:39], v[216:219], v[120:123]\n"
        "global_load_lds_dwordx4 v244, s[64:65] offset:2048\n"
        "v_mfma_f32_16x16x32_bf16 v[124:127], v[44:47], v[216:219], v[124:127]\n"
        "ds_read_b128 v[216:219], v247 offset:38912\n"
        "v_mfma_f32_16x16x32_bf16 v[124:127], v[40:43], v[200:203], v[124:127]\n"
        "ds_read_b128 v[200:203], v246 offset:38912\n"
        "s_waitcnt lgkmcnt(6)\n"
        "v_mfma_f32_16x16x32_bf16 v[156:159], v[40:43], v[204:207], v[156:159]\n"
        "ds_read_b64_tr_b16 v[40:41], v228 offset:17408\n"
        "s_waitcnt lgkmcnt(6)\n"
        "v_mfma_f32_16x16x32_bf16 v[156:159], v[44:47], v[220:223], v[156:159]\n"
        "global_load_lds_dwordx4 v245, s[64:65] offset:3072\n"
        "s_add_u32 m0, s72, 0x11000\n"
        "v_mfma_f32_16x16x32_bf16 v[152:155], v[36:39], v[220:223], v[152:155]\n"
        "ds_read_b128 v[220:223], v247 offset:36864\n"
        "s_add_u32 s64, s64, 0x80\n"
        "s_addc_u32 s65, s65, 0\n"
        "v_mfma_f32_16x16x32_bf16 v[152:155], v[32:35], v[204:207], v[152:155]\n"
        "ds_read_b128 v[204:207], v246 offset:36864\n"
        "v_accvgpr_read_b32 v184, a84\n"
        "v_mfma_f32_16x16x32_bf16 v[0:3], v[48:51], v[192:195], v[0:3]\n"
        "s_waitcnt lgkmcnt(5)\n"
        "v_cvt_pk_bf16_f32 v184, v184, v185\n"
        "v_cvt_pk_bf16_f32 v185, v186, v187\n"
        "v_mfma_f32_16x16x32_bf16 v[0:3], v[52:55], v[208:211], v[0:3]\n"
        "ds_read_b64_tr_b16 v[32:33], v226 offset:17408\n"
        "v_cvt_pk_bf16_f32 v186, v188, v189\n"
        "v_mfma_f32_16x16x32_bf16 v[4:7], v[60:63], v[208:211], v[4:7]\n"
        "s_waitcnt lgkmcnt(5)\n"
        "v_cvt_pk_bf16_f32 v187, v190, v191\n"
        "v_accvgpr_write_b32 a20, v184\n"
        "v_mfma_f32_16x16x32_bf16 v[4:7], v[56:59], v[192:195], v[4:7]\n"
        "global_load_lds_dwordx4 v242, s[66:67] offset:0\n"
        "v_accvgpr_write_b32 a21, v185\n"
        "v_mfma_f32_16x16x32_bf16 v[68:71], v[56:59], v[196:199], v[68:71]\n"
        "s_waitcnt lgkmcnt(3)\n"
        "v_accvgpr_write_b32 a22, v186\n"
        "v_accvgpr_write_b32 a23, v187\n"
        "v_mfma_f32_16x16x32_bf16 v[68:71], v[60:63], v[212:215], v[68:71]\n"
        "ds_read_b64_tr_b16 v[34:35], v227 offset:17408\n"
        "v_accvgpr_read_b32 v185, a93\n"
        "v_mfma_f32_16x16x32_bf16 v[64:67], v[52:55], v[212:215], v[64:67]\n"
        "ds_read_b64_tr_b16 v[36:37], v226 offset:25600\n"
        "v_accvgpr_read_b32 v186, a94\n"
        "v_mfma_f32_16x16x32_bf16 v[64:67], v[48:51], v[196:199], v[64:67]\n"
        "ds_read_b64_tr_b16 v[38:39], v227 offset:25600\n"
        "global_load_lds_dwordx4 v243, s[66:67] offset:1024\n"
        "v_mfma_f32_16x16x32_bf16 v[96:99], v[48:51], v[200:203], v[96:99]\n"
        "ds_read_b64_tr_b16 v[44:45], v228 offset:25600\n"
        "v_accvgpr_read_b32 v187, a95\n"
        "v_mfma_f32_16x16x32_bf16 v[96:99], v[52:55], v[216:219], v[96:99]\n"
        "ds_read_b64_tr_b16 v[46:47], v229 offset:25600\n"
        "v_accvgpr_read_b32 v188, a124\n"
        "v_mfma_f32_16x16x32_bf16 v[100:103], v[60:63], v[216:219], v[100:103]\n"
        "ds_read_b64_tr_b16 v[42:43], v229 offset:17408\n"
        "v_accvgpr_read_b32 v189, a125\n"
        "v_mfma_f32_16x16x32_bf16 v[100:103], v[56:59], v[200:203], v[100:103]\n"
        "s_waitcnt lgkmcnt(6)\n"
        "global_load_lds_dwordx4 v244, s[66:67] offset:2048\n"
        "v_mfma_f32_16x16x32_bf16 v[132:135], v[56:59], v[204:207], v[132:135]\n"
        "ds_read_b64_tr_b16 v[56:57], v232 offset:17408\n"
        "v_accvgpr_read_b32 v190, a126\n"
        "v_mfma_f32_16x16x32_bf16 v[132:135], v[60:63], v[220:223], v[132:135]\n"
        "ds_read_b64_tr_b16 v[60:61], v232 offset:25600\n"
        "v_accvgpr_read_b32 v191, a127\n"
        "v_mfma_f32_16x16x32_bf16 v[128:131], v[52:55], v[220:223], v[128:131]\n"
        "ds_read_b64_tr_b16 v[52:53], v230 offset:25600\n"
        "v_mfma_f32_16x16x32_bf16 v[128:131], v[48:51], v[204:207], v[128:131]\n"
        "global_load_lds_dwordx4 v245, s[66:67] offset:3072\n"
        "s_add_u32 m0, s73, 0x1c000\n"
        "v_mfma_f32_16x16x32_bf16 v[8:11], v[160:163], v[192:195], v[8:11]\n"
        "ds_read_b64_tr_b16 v[48:49], v230 offset:17408\n"
        "s_add_u32 s66, s66, 0x80\n"
        "s_addc_u32 s67, s67, 0\n"
        "v_mfma_f32_16x16x32_bf16 v[8:11], v[164:167], v[208:211], v[8:11]\n"
        "ds_read_b64_tr_b16 v[50:51], v231 offset:17408\n"
        "v_mfma_f32_16x16x32_bf16 v[12:15], v[172:175], v[208:211], v[12:15]\n"
        "ds_read_b64_tr_b16 v[54:55], v231 offset:25600\n"
        "v_mfma_f32_16x16x32_bf16 v[12:15], v[168:171], v[192:195], v[12:15]\n"
        "ds_read_b64_tr_b16 v[62:63], v233 offset:25600\n"
        "v_mfma_f32_16x16x32_bf16 v[76:79], v[168:171], v[196:199], v[76:79]\n"
        "ds_read_b64_tr_b16 v[58:59], v233 offset:17408\n"
        "v_mfma_f32_16x16x32_bf16 v[76:79], v[172:175], v[212:215], v[76:79]\n"
        "s_waitcnt lgkmcnt(6)\n"
        "v_mfma_f32_16x16x32_bf16 v[72:75], v[164:167], v[212:215], v[72:75]\n"
        "v_mfma_f32_16x16x32_bf16 v[72:75], v[160:163], v[196:199], v[72:75]\n"
        "v_accvgpr_read_b32 v176, a88\n"
        "v_cvt_pk_bf16_f32 v176, v176, v177\n"
        "v_mfma_f32_16x16x32_bf16 v[104:107], v[160:163], v[200:203], v[104:107]\n"
        "s_lshl_b32 s90, s43, 19\n"
        "v_cvt_pk_bf16_f32 v177, v178, v179\n"
        "v_cvt_pk_bf16_f32 v178, v180, v181\n"
        "v_mfma_f32_16x16x32_bf16 v[104:107], v[164:167], v[216:219], v[104:107]\n"
        "s_add_u32 s90, s90, s72\n"
        "v_cvt_pk_bf16_f32 v179, v182, v183\n"
        "v_accvgpr_write_b32 a24, v176\n"
        "v_mfma_f32_16x16x32_bf16 v[108:111], v[172:175], v[216:219], v[108:111]\n"
        "s_and_b32 s78, s45, 1\n"
        "v_accvgpr_write_b32 a25, v177\n"
        "v_accvgpr_write_b32 a26, v178\n"
        "v_mfma_f32_16x16x32_bf16 v[108:111], v[168:171], v[200:203], v[108:111]\n"
        "s_lshl_b32 s78, s78, 15\n"
        "v_accvgpr_write_b32 a27, v179\n"
        "s_add_u32 s78, s78, 0x10000\n"
        "v_mfma_f32_16x16x32_bf16 v[140:143], v[168:171], v[204:207], v[140:143]\n"
        "s_waitcnt vmcnt(13) lgkmcnt(0)\n"
        "s_barrier\n"
        "s_and_b32 s80, s40, 1\n"
        "v_mfma_f32_16x16x32_bf16 v[140:143], v[172:175], v[220:223], v[140:143]\n"
        "global_load_lds_dwordx4 v238, s[68:69] offset:0\n"
        "ds_read_b64_tr_b16 v[172:173], v228 offset:40960\n"
        "s_lshl_b32 s80, s80, 11\n"
        "v_mfma_f32_16x16x32_bf16 v[136:139], v[164:167], v[220:223], v[136:139]\n"
        "ds_read_b64_tr_b16 v[164:165], v226 offset:40960\n"
        "s_lshl_b32 s82, s40, 4\n"
        "s_sub_u32 s83, 14, s44\n"
        "v_mfma_f32_16x16x32_bf16 v[136:139], v[160:163], v[204:207], v[136:139]\n"
        "ds_read_b64_tr_b16 v[160:161], v226 offset:32768\n"
        "s_lshl_b32 s50, s41, 7\n"
        "s_lshl2_add_u32 s50, s42, s50\n"
        "v_mfma_f32_16x16x32_bf16 v[16:19], v[32:35], v[192:195], v[16:19]\n"
        "ds_read_b64_tr_b16 v[162:163], v227 offset:32768\n"
        "s_add_u32 s50, s50, s40\n"
        "s_lshl_b32 s50, s50, 12\n"
        "v_mfma_f32_16x16x32_bf16 v[16:19], v[36:39], v[208:211], v[16:19]\n"
        "ds_read_b64_tr_b16 v[166:167], v227 offset:40960\n"
        "global_load_lds_dwordx4 v239, s[68:69] offset:1024\n"
        "s_mul_i32 s51, s40, 8320\n"
        "v_mfma_f32_16x16x32_bf16 v[20:23], v[44:47], v[208:211], v[20:23]\n"
        "ds_read_b64_tr_b16 v[174:175], v229 offset:40960\n"
        "s_add_u32 s92, s26, s50\n"
        "s_addc_u32 s93, s27, 0\n"
        "v_mfma_f32_16x16x32_bf16 v[20:23], v[40:43], v[192:195], v[20:23]\n"
        "ds_read_b64_tr_b16 v[168:169], v228 offset:32768\n"
        "s_add_u32 s50, s50, 0x3000000\n"
        "s_add_u32 s51, s51, 0x18000\n"
        "v_mfma_f32_16x16x32_bf16 v[84:87], v[40:43], v[196:199], v[84:87]\n"
        "ds_read_b64_tr_b16 v[170:171], v229 offset:32768\n"
        "s_add_u32 s94, s38, s50\n"
        "s_addc_u32 s95, s39, 0\n"
        "v_mfma_f32_16x16x32_bf16 v[84:87], v[44:47], v[212:215], v[84:87]\n"
        "global_load_lds_dwordx4 v240, s[68:69] offset:2048\n"
        "s_add_u32 s50, s51, 4128\n"
        "v_mfma_f32_16x16x32_bf16 v[80:83], v[36:39], v[212:215], v[80:83]\n"
        "v_mfma_f32_16x16x32_bf16 v[80:83], v[32:35], v[196:199], v[80:83]\n"
        "v_mfma_f32_16x16x32_bf16 v[112:115], v[32:35], v[200:203], v[112:115]\n"
        "v_mfma_f32_16x16x32_bf16 v[112:115], v[36:39], v[216:219], v[112:115]\n"
        "global_load_lds_dwordx4 v241, s[68:69] offset:3072\n"
        "s_add_u32 m0, s72, 0x8000\n"
        "v_mfma_f32_16x16x32_bf16 v[116:119], v[44:47], v[216:219], v[116:119]\n"
        "s_add_u32 s68, s68, 0x80000\n"
        "s_addc_u32 s69, s69, 0\n"
        "v_mfma_f32_16x16x32_bf16 v[116:119], v[40:43], v[200:203], v[116:119]\n"
        "v_mfma_f32_16x16x32_bf16 v[148:151], v[40:43], v[204:207], v[148:151]\n"
        "ds_read_b64_tr_b16 v[40:41], v232 offset:32768\n"
        "v_mfma_f32_16x16x32_bf16 v[148:151], v[44:47], v[220:223], v[148:151]\n"
        "global_load_lds_dwordx4 v242, s[64:65] offset:0\n"
        "v_mfma_f32_16x16x32_bf16 v[144:147], v[36:39], v[220:223], v[144:147]\n"
        "ds_read_b64_tr_b16 v[36:37], v230 offset:40960\n"
        "v_mfma_f32_16x16x32_bf16 v[144:147], v[32:35], v[204:207], v[144:147]\n"
        "ds_read_b64_tr_b16 v[32:33], v230 offset:32768\n"
        "v_accvgpr_read_b32 v184, a92\n"
        "v_mfma_f32_16x16x32_bf16 v[24:27], v[48:51], v[192:195], v[24:27]\n"
        "ds_read_b64_tr_b16 v[34:35], v231 offset:32768\n"
        "v_cvt_pk_bf16_f32 v184, v184, v185\n"
        "v_mfma_f32_16x16x32_bf16 v[24:27], v[52:55], v[208:211], v[24:27]\n"
        "ds_read_b64_tr_b16 v[38:39], v231 offset:40960\n"
        "v_cvt_pk_bf16_f32 v185, v186, v187\n"
        "v_mfma_f32_16x16x32_bf16 v[28:31], v[60:63], v[208:211], v[28:31]\n"
        "global_store_dwordx4 v254, a[168:171], s[70:71] offset:0 sc1\n"
        "ds_read_b64_tr_b16 v[44:45], v232 offset:40960\n"
        "ds_read_b64_tr_b16 v[46:47], v233 offset:40960\n"
        "v_cvt_pk_bf16_f32 v186, v188, v189\n"
        "v_mfma_f32_16x16x32_bf16 v[28:31], v[56:59], v[192:195], v[28:31]\n"
        "ds_read_b64_tr_b16 v[42:43], v233 offset:32768\n"
        "v_cvt_pk_bf16_f32 v187, v190, v191\n"
        "v_mfma_f32_16x16x32_bf16 v[92:95], v[56:59], v[196:199], v[92:95]\n"
        "ds_read_b128 v[192:195], v246 offset:2048\n"
        "v_accvgpr_write_b32 a28, v184\n"
        "v_mfma_f32_16x16x32_bf16 v[92:95], v[60:63], v[212:215], v[92:95]\n"
        "ds_read_b128 v[208:211], v247 offset:2048\n"
        "v_accvgpr_write_b32 a29, v185\n"
        "v_mfma_f32_16x16x32_bf16 v[88:91], v[52:55], v[212:215], v[88:91]\n"
        "global_load_lds_dwordx4 v243, s[64:65] offset:1024\n"
        "v_accvgpr_write_b32 a30, v186\n"
        "v_mfma_f32_16x16x32_bf16 v[88:91], v[48:51], v[196:199], v[88:91]\n"
        "ds_read_b128 v[196:199], v246 offset:0\n"
        "v_accvgpr_write_b32 a31, v187\n"
        "v_mfma_f32_16x16x32_bf16 v[120:123], v[48:51], v[200:203], v[120:123]\n"
        "ds_read_b128 v[212:215], v247 offset:0\n"
        "v_mfma_f32_16x16x32_bf16 v[120:123], v[52:55], v[216:219], v[120:123]\n"
        "v_mfma_f32_16x16x32_bf16 v[124:127], v[60:63], v[216:219], v[124:127]\n"
        "global_load_lds_dwordx4 v244, s[64:65] offset:2048\n"
        "ds_read_b128 v[216:219], v247 offset:6144\n"
        "v_mfma_f32_16x16x32_bf16 v[124:127], v[56:59], v[200:203], v[124:127]\n"
        "ds_read_b128 v[200:203], v246 offset:6144\n"
        "s_waitcnt lgkmcnt(6)\n"
        "v_mfma_f32_16x16x32_bf16 v[156:159], v[56:59], v[204:207], v[156:159]\n"
        "ds_read_b64_tr_b16 v[56:57], v228 offset:33792\n"
        "s_waitcnt lgkmcnt(6)\n"
        "v_mfma_f32_16x16x32_bf16 v[156:159], v[60:63], v[220:223], v[156:159]\n"
        "ds_read_b64_tr_b16 v[60:61], v228 offset:41984\n"
        "s_waitcnt lgkmcnt(6)\n"
        "v_mfma_f32_16x16x32_bf16 v[152:155], v[52:55], v[220:223], v[152:155]\n"
        "global_load_lds_dwordx4 v245, s[64:65] offset:3072\n"
        "ds_read_b128 v[220:223], v247 offset:4096\n"
        "s_add_u32 m0, s72, 0x9000\n"
        "v_mfma_f32_16x16x32_bf16 v[152:155], v[48:51], v[204:207], v[152:155]\n"
        "ds_read_b128 v[204:207], v246 offset:4096\n"
        "s_add_u32 s64, s64, 0x80\n"
        "s_addc_u32 s65, s65, 0\n"
        "v_mfma_f32_16x16x32_bf16 v[0:3], v[160:163], v[192:195], v[0:3]\n"
        "ds_read_b64_tr_b16 v[48:49], v226 offset:33792\n"
        "s_waitcnt lgkmcnt(7)\n"
        "v_mfma_f32_16x16x32_bf16 v[0:3], v[164:167], v[208:211], v[0:3]\n"
        "ds_read_b64_tr_b16 v[50:51], v227 offset:33792\n"
        "v_mfma_f32_16x16x32_bf16 v[4:7], v[172:175], v[208:211], v[4:7]\n"
        "ds_read_b64_tr_b16 v[52:53], v226 offset:41984\n"
        "global_load_lds_dwordx4 v242, s[66:67] offset:0\n"
        "v_mfma_f32_16x16x32_bf16 v[4:7], v[168:171], v[192:195], v[4:7]\n"
        "ds_read_b64_tr_b16 v[54:55], v227 offset:41984\n"
        "s_waitcnt lgkmcnt(8)\n"
        "v_mfma_f32_16x16x32_bf16 v[68:71], v[168:171], v[196:199], v[68:71]\n"
        "ds_read_b64_tr_b16 v[62:63], v229 offset:41984\n"
        "v_mfma_f32_16x16x32_bf16 v[68:71], v[172:175], v[212:215], v[68:71]\n"
        "ds_read_b64_tr_b16 v[58:59], v229 offset:33792\n"
        "s_waitcnt lgkmcnt(8)\n"
        "v_mfma_f32_16x16x32_bf16 v[64:67], v[164:167], v[212:215], v[64:67]\n"
        "global_load_lds_dwordx4 v243, s[66:67] offset:1024\n"
        "v_mfma_f32_16x16x32_bf16 v[64:67], v[160:163], v[196:199], v[64:67]\n"
        "s_waitcnt lgkmcnt(6)\n"
        "v_mfma_f32_16x16x32_bf16 v[96:99], v[160:163], v[200:203], v[96:99]\n"
        "v_mfma_f32_16x16x32_bf16 v[96:99], v[164:167], v[216:219], v[96:99]\n"
        "v_mfma_f32_16x16x32_bf16 v[100:103], v[172:175], v[216:219], v[100:103]\n"
        "global_load_lds_dwordx4 v244, s[66:67] offset:2048\n"
        "v_mfma_f32_16x16x32_bf16 v[100:103], v[168:171], v[200:203], v[100:103]\n"
        "v_mfma_f32_16x16x32_bf16 v[132:135], v[168:171], v[204:207], v[132:135]\n"
        "ds_read_b64_tr_b16 v[168:169], v232 offset:33792\n"
        "v_mfma_f32_16x16x32_bf16 v[132:135], v[172:175], v[220:223], v[132:135]\n"
        "ds_read_b64_tr_b16 v[172:173], v232 offset:41984\n"
        "v_mfma_f32_16x16x32_bf16 v[128:131], v[164:167], v[220:223], v[128:131]\n"
        "global_load_lds_dwordx4 v245, s[66:67] offset:3072\n"
        "s_add_u32 m0, s73, 0x20000\n"
        "v_mfma_f32_16x16x32_bf16 v[128:131], v[160:163], v[204:207], v[128:131]\n"
        "ds_read_b64_tr_b16 v[160:161], v230 offset:33792\n"
        "s_add_u32 s66, s66, 0x80\n"
        "s_addc_u32 s67, s67, 0\n"
        "v_mfma_f32_16x16x32_bf16 v[8:11], v[32:35], v[192:195], v[8:11]\n"
        "ds_read_b64_tr_b16 v[162:163], v231 offset:33792\n"
        "v_mfma_f32_16x16x32_bf16 v[8:11], v[36:39], v[208:211], v[8:11]\n"
        "ds_read_b64_tr_b16 v[164:165], v230 offset:41984\n"
        "v_mfma_f32_16x16x32_bf16 v[12:15], v[44:47], v[208:211], v[12:15]\n"
        "ds_read_b64_tr_b16 v[166:167], v231 offset:41984\n"
        "v_mfma_f32_16x16x32_bf16 v[12:15], v[40:43], v[192:195], v[12:15]\n"
        "ds_read_b64_tr_b16 v[174:175], v233 offset:41984\n"
        "v_mfma_f32_16x16x32_bf16 v[76:79], v[40:43], v[196:199], v[76:79]\n"
        "ds_read_b64_tr_b16 v[170:171], v233 offset:33792\n"
        "v_mfma_f32_16x16x32_bf16 v[76:79], v[44:47], v[212:215], v[76:79]\n"
        "s_waitcnt lgkmcnt(6)\n"
        "v_mfma_f32_16x16x32_bf16 v[72:75], v[36:39], v[212:215], v[72:75]\n"
        "v_mfma_f32_16x16x32_bf16 v[72:75], v[32:35], v[196:199], v[72:75]\n"
        "s_mov_b32 s79, s72\n"
        "s_xor_b32 s79, s79, 0x8000\n"
        "v_mfma_f32_16x16x32_bf16 v[104:107], v[32:35], v[200:203], v[104:107]\n"
        "v_mfma_f32_16x16x32_bf16 v[104:107], v[36:39], v[216:219], v[104:107]\n"
        "v_mfma_f32_16x16x32_bf16 v[108:111], v[44:47], v[216:219], v[108:111]\n"
        "v_mfma_f32_16x16x32_bf16 v[108:111], v[40:43], v[200:203], v[108:111]\n"
        "v_mfma_f32_16x16x32_bf16 v[140:143], v[40:43], v[204:207], v[140:143]\n"
        "s_waitcnt vmcnt(13) lgkmcnt(0)\n"
        "s_barrier\n"
        "v_mfma_f32_16x16x32_bf16 v[140:143], v[44:47], v[220:223], v[140:143]\n"
        "global_load_lds_dwordx4 v238, s[68:69] offset:0\n"
        "ds_read_b64_tr_b16 v[44:45], v228 offset:57344\n"
        "v_mfma_f32_16x16x32_bf16 v[136:139], v[36:39], v[220:223], v[136:139]\n"
        "ds_read_b64_tr_b16 v[36:37], v226 offset:57344\n"
        "v_mfma_f32_16x16x32_bf16 v[136:139], v[32:35], v[204:207], v[136:139]\n"
        "ds_read_b64_tr_b16 v[32:33], v226 offset:49152\n"
        "v_mfma_f32_16x16x32_bf16 v[16:19], v[48:51], v[192:195], v[16:19]\n"
        "ds_read_b64_tr_b16 v[34:35], v227 offset:49152\n"
        "v_mfma_f32_16x16x32_bf16 v[16:19], v[52:55], v[208:211], v[16:19]\n"
        "ds_read_b64_tr_b16 v[38:39], v227 offset:57344\n"
        "global_load_lds_dwordx4 v239, s[68:69] offset:1024\n"
        "v_mfma_f32_16x16x32_bf16 v[20:23], v[60:63], v[208:211], v[20:23]\n"
        "ds_read_b64_tr_b16 v[46:47], v229 offset:57344\n"
        "v_mfma_f32_16x16x32_bf16 v[20:23], v[56:59], v[192:195], v[20:23]\n"
        "ds_read_b64_tr_b16 v[40:41], v228 offset:49152\n"
        "v_mfma_f32_16x16x32_bf16 v[84:87], v[56:59], v[196:199], v[84:87]\n"
        "ds_read_b64_tr_b16 v[42:43], v229 offset:49152\n"
        "v_mfma_f32_16x16x32_bf16 v[84:87], v[60:63], v[212:215], v[84:87]\n"
        "global_load_lds_dwordx4 v240, s[68:69] offset:2048\n"
        "v_mfma_f32_16x16x32_bf16 v[80:83], v[52:55], v[212:215], v[80:83]\n"
        "v_add_u32 v240, s78, v254\n"
        "s_xor_b32 s78, s78, 0x8000\n"
        "v_mfma_f32_16x16x32_bf16 v[80:83], v[48:51], v[196:199], v[80:83]\n"
        "v_mfma_f32_16x16x32_bf16 v[112:115], v[48:51], v[200:203], v[112:115]\n"
        "v_mfma_f32_16x16x32_bf16 v[112:115], v[52:55], v[216:219], v[112:115]\n"
        "global_load_lds_dwordx4 v241, s[68:69] offset:3072\n"
        "s_add_u32 m0, s72, 0x0\n"
        "v_mfma_f32_16x16x32_bf16 v[116:119], v[60:63], v[216:219], v[116:119]\n"
        "v_mov_b32 v241, v254\n"
        "v_mfma_f32_16x16x32_bf16 v[116:119], v[56:59], v[200:203], v[116:119]\n"
        "v_mfma_f32_16x16x32_bf16 v[148:151], v[56:59], v[204:207], v[148:151]\n"
        "ds_read_b64_tr_b16 v[56:57], v232 offset:49152\n"
        "v_mfma_f32_16x16x32_bf16 v[148:151], v[60:63], v[220:223], v[148:151]\n"
        "ds_read_b64_tr_b16 v[60:61], v232 offset:57344\n"
        "global_load_lds_dwordx4 v242, s[64:65] offset:0\n"
        "v_mfma_f32_16x16x32_bf16 v[144:147], v[52:55], v[220:223], v[144:147]\n"
        "ds_read_b64_tr_b16 v[52:53], v230 offset:57344\n"
        "v_mfma_f32_16x16x32_bf16 v[144:147], v[48:51], v[204:207], v[144:147]\n"
        "ds_read_b64_tr_b16 v[48:49], v230 offset:49152\n"
        "v_mfma_f32_16x16x32_bf16 v[24:27], v[160:163], v[192:195], v[24:27]\n"
        "LHA9_%=:\n"
        "s_cbranch_execz LHA13_%=\n"
        "ds_read_b64_tr_b16 v[50:51], v231 offset:49152\n"
        "v_mfma_f32_16x16x32_bf16 v[24:27], v[164:167], v[208:211], v[24:27]\n"
        "ds_read_b64_tr_b16 v[54:55], v231 offset:57344\n"
        "global_load_lds_dwordx4 v243, s[64:65] offset:1024\n"
        "v_mfma_f32_16x16x32_bf16 v[28:31], v[172:175], v[208:211], v[28:31]\n"
        "ds_read_b64_tr_b16 v[62:63], v233 offset:57344\n"
        "v_mfma_f32_16x16x32_bf16 v[28:31], v[168:171], v[192:195], v[28:31]\n"
        "ds_read_b64_tr_b16 v[58:59], v233 offset:49152\n"
        "v_mfma_f32_16x16x32_bf16 v[92:95], v[168:171], v[196:199], v[92:95]\n"
        "ds_read_b128 v[192:195], v248 offset:2048\n"
        "v_mfma_f32_16x16x32_bf16 v[92:95], v[172:175], v[212:215], v[92:95]\n"
        "ds_read_b128 v[208:211], v249 offset:2048\n"
        "global_load_lds_dwordx4 v244, s[64:65] offset:2048\n"
        "v_mfma_f32_16x16x32_bf16 v[88:91], v[164:167], v[212:215], v[88:91]\n"
        "ds_read_b128 v[212:215], v249 offset:0\n"
        "v_mfma_f32_16x16x32_bf16 v[88:91], v[160:163], v[196:199], v[88:91]\n"
        "ds_read_b128 v[196:199], v248 offset:0\n"
        "v_mfma_f32_16x16x32_bf16 v[120:123], v[160:163], v[200:203], v[120:123]\n"
        "v_mfma_f32_16x16x32_bf16 v[120:123], v[164:167], v[216:219], v[120:123]\n"
        "global_load_lds_dwordx4 v245, s[64:65] offset:3072\n"
        "s_add_u32 m0, s72, 0x1000\n"
        "v_mfma_f32_16x16x32_bf16 v[124:127], v[172:175], v[216:219], v[124:127]\n"
        "ds_read_b128 v[216:219], v249 offset:6144\n"
        "s_add_u32 s64, s38, s90\n"
        "s_addc_u32 s65, s39, 0\n"
        "v_mfma_f32_16x16x32_bf16 v[124:127], v[168:171], v[200:203], v[124:127]\n"
        "ds_read_b128 v[200:203], v248 offset:6144\n"
        "s_lshl_b32 s90, s40, 14\n"
        "s_and_b32 s90, s90, 0x8000\n"
        "v_mfma_f32_16x16x32_bf16 v[156:159], v[168:171], v[204:207], v[156:159]\n"
        "ds_read_b64_tr_b16 v[168:169], v228 offset:50176\n"
        "s_xor_b32 s91, s78, s90\n"
        "s_add_u32 s91, s91, s80\n"
        "v_mfma_f32_16x16x32_bf16 v[156:159], v[172:175], v[220:223], v[156:159]\n"
        "ds_read_b64_tr_b16 v[172:173], v228 offset:58368\n"
        "global_load_lds_dwordx4 v242, s[66:67] offset:0\n"
        "s_add_u32 s78, s78, s72\n"
        "v_mfma_f32_16x16x32_bf16 v[152:155], v[164:167], v[220:223], v[152:155]\n"
        "ds_read_b128 v[220:223], v249 offset:4096\n"
        "s_xor_b32 s78, s78, 0x8000\n"
        "v_lshrrev_b32 v242, 6, v254\n"
        "v_mfma_f32_16x16x32_bf16 v[152:155], v[160:163], v[204:207], v[152:155]\n"
        "ds_read_b128 v[204:207], v248 offset:4096\n"
        "s_waitcnt lgkmcnt(6)\n"
        "v_mfma_f32_16x16x32_bf16 v[0:3], v[32:35], v[192:195], v[0:3]\n"
        "ds_read_b64_tr_b16 v[160:161], v226 offset:50176\n"
        "v_and_b32 v242, 12, v242\n"
        "v_mfma_f32_16x16x32_bf16 v[0:3], v[36:39], v[208:211], v[0:3]\n"
        "ds_read_b64_tr_b16 v[162:163], v227 offset:50176\n"
        "global_load_lds_dwordx4 v243, s[66:67] offset:1024\n"
        "v_mfma_f32_16x16x32_bf16 v[4:7], v[44:47], v[208:211], v[4:7]\n"
        "ds_read_b64_tr_b16 v[164:165], v226 offset:58368\n"
        "v_bfe_u32 v243, v254, 4, 4\n"
        "v_mfma_f32_16x16x32_bf16 v[4:7], v[40:43], v[192:195], v[4:7]\n"
        "ds_read_b64_tr_b16 v[166:167], v227 offset:58368\n"
        "v_sub_u32 v243, v243, v242\n"
        "v_mfma_f32_16x16x32_bf16 v[68:71], v[40:43], v[196:199], v[68:71]\n"
        "ds_read_b64_tr_b16 v[174:175], v229 offset:58368\n"
        "v_add_u32 v243, s82, v243\n"
        "v_mfma_f32_16x16x32_bf16 v[68:71], v[44:47], v[212:215], v[68:71]\n"
        "ds_read_b64_tr_b16 v[170:171], v229 offset:50176\n"
        "global_load_lds_dwordx4 v244, s[66:67] offset:2048\n"
        "s_mov_b32 s82, -1\n"
        "v_mfma_f32_16x16x32_bf16 v[64:67], v[36:39], v[212:215], v[64:67]\n"
        "s_waitcnt lgkmcnt(7)\n"
        "v_add_u32 v244, s91, v254\n"
        "s_lshl1_add_u32 s91, s80, s90\n"
        "v_mfma_f32_16x16x32_bf16 v[64:67], v[32:35], v[196:199], v[64:67]\n"
        "s_waitcnt lgkmcnt(6)\n"
        "v_cmp_gt_i32 s[10:11], 0, v243\n"
        "v_cmp_gt_i32 s[12:13], 1, v243\n"
        "v_mfma_f32_16x16x32_bf16 v[96:99], v[32:35], v[200:203], v[96:99]\n"
        "s_add_u32 s82, s82, 1\n"
        "v_cmp_gt_i32 s[14:15], 2, v243\n"
        "v_cmp_gt_i32 s[16:17], 3, v243\n"
        "v_mfma_f32_16x16x32_bf16 v[96:99], v[36:39], v[216:219], v[96:99]\n"
        "global_load_lds_dwordx4 v245, s[66:67] offset:3072\n"
        "v_mfma_f32_16x16x32_bf16 v[100:103], v[44:47], v[216:219], v[100:103]\n"
        "s_add_u32 s66, s64, 0x1000000\n"
        "s_addc_u32 s67, s65, 0\n"
        "v_add_u32 v245, s91, v254\n"
        "v_mfma_f32_16x16x32_bf16 v[100:103], v[40:43], v[200:203], v[100:103]\n"
        "s_cmp_ge_u32 s82, s44\n"
        "s_addc_u32 s90, s82, 0\n"
        "v_cmp_gt_i32 s[20:21], 16, v243\n"
        "v_mfma_f32_16x16x32_bf16 v[132:135], v[40:43], v[204:207], v[132:135]\n"
        "ds_read_b64_tr_b16 v[40:41], v232 offset:50176\n"
        "s_lshl_b32 s90, s90, 15\n"
        "v_cmp_gt_i32 s[22:23], 17, v243\n"
        "v_mfma_f32_16x16x32_bf16 v[132:135], v[44:47], v[220:223], v[132:135]\n"
        "ds_read_b64_tr_b16 v[44:45], v232 offset:58368\n"
        "v_cmp_gt_i32 s[24:25], 18, v243\n"
        "v_mfma_f32_16x16x32_bf16 v[128:131], v[36:39], v[220:223], v[128:131]\n"
        "global_store_dwordx4 v254, a[172:175], s[70:71] offset:1024 sc1\n"
        "ds_read_b64_tr_b16 v[36:37], v230 offset:58368\n"
        "ds_read_b64_tr_b16 v[38:39], v231 offset:58368\n"
        "s_add_u32 s70, s38, s97\n"
        "s_addc_u32 s71, s39, 0\n"
        "v_mfma_f32_16x16x32_bf16 v[128:131], v[32:35], v[204:207], v[128:131]\n"
        "ds_read_b64_tr_b16 v[32:33], v230 offset:50176\n"
        "s_add_u32 s97, s52, 0x4000\n"
        "v_cmp_gt_i32 s[28:29], 19, v243\n"
        "v_mfma_f32_16x16x32_bf16 v[8:11], v[48:51], v[192:195], v[8:11]\n"
        "ds_read_b64_tr_b16 v[34:35], v231 offset:50176\n"
        "v_cmp_gt_i32 s[30:31], 32, v243\n"
        "v_mfma_f32_16x16x32_bf16 v[8:11], v[52:55], v[208:211], v[8:11]\n"
        "ds_read_b64_tr_b16 v[46:47], v233 offset:58368\n"
        "v_cmp_gt_i32 s[56:57], 33, v243\n"
        "v_mfma_f32_16x16x32_bf16 v[12:15], v[60:63], v[208:211], v[12:15]\n"
        "ds_read_b64_tr_b16 v[42:43], v233 offset:50176\n"
        "v_cmp_gt_i32 s[58:59], 34, v243\n"
        "v_mfma_f32_16x16x32_bf16 v[12:15], v[56:59], v[192:195], v[12:15]\n"
        "global_store_dwordx4 v254, a[0:3], s[70:71] offset:0 sc1\n"
        "s_add_u32 s76, s66, s90\n"
        "s_addc_u32 s77, s67, 0\n"
        "v_cmp_gt_i32 s[60:61], 35, v243\n"
        "v_cmp_gt_i32 s[62:63], 48, v243\n"
        "v_cmp_gt_i32 s[84:85], 49, v243\n"
        "v_mfma_f32_16x16x32_bf16 v[76:79], v[56:59], v[196:199], v[76:79]\n"
        "v_cmp_gt_i32 s[86:87], 50, v243\n"
        "v_cmp_gt_i32 s[88:89], 51, v243\n"
        "v_mfma_f32_16x16x32_bf16 v[76:79], v[60:63], v[212:215], v[76:79]\n"
        "s_waitcnt lgkmcnt(4)\n"
        "v_mov_b32 v242, 0x3f803f80\n"
        "v_accvgpr_write_b32 a80, v242\n"
        "v_mfma_f32_16x16x32_bf16 v[72:75], v[52:55], v[212:215], v[72:75]\n"
        "v_accvgpr_write_b32 a81, v242\n"
        "v_accvgpr_write_b32 a82, v242\n"
        "v_mfma_f32_16x16x32_bf16 v[72:75], v[48:51], v[196:199], v[72:75]\n"
        "v_accvgpr_write_b32 a83, v242\n"
        "v_mov_b32 v242, 0xff800000\n"
        "v_mfma_f32_16x16x32_bf16 v[104:107], v[48:51], v[200:203], v[104:107]\n"
        "v_xor_b32 v241, 0x8000, v241\n"
        "v_mfma_f32_16x16x32_bf16 v[104:107], v[52:55], v[216:219], v[104:107]\n"
        "s_waitcnt vmcnt(14) lgkmcnt(0)\n"
        "s_barrier\n"
        "v_mfma_f32_16x16x32_bf16 v[108:111], v[60:63], v[216:219], v[108:111]\n"
        "v_mfma_f32_16x16x32_bf16 v[108:111], v[56:59], v[200:203], v[108:111]\n"
        "v_mfma_f32_16x16x32_bf16 v[140:143], v[56:59], v[204:207], v[140:143]\n"
        "ds_read_b64_tr_b16 v[56:57], v228 offset:16384\n"
        "v_mfma_f32_16x16x32_bf16 v[140:143], v[60:63], v[220:223], v[140:143]\n"
        "ds_read_b64_tr_b16 v[60:61], v228 offset:24576\n"
        "v_mfma_f32_16x16x32_bf16 v[136:139], v[52:55], v[220:223], v[136:139]\n"
        "ds_read_b64_tr_b16 v[52:53], v226 offset:24576\n"
        "v_mfma_f32_16x16x32_bf16 v[136:139], v[48:51], v[204:207], v[136:139]\n"
        "global_store_dwordx4 v254, a[4:7], s[70:71] offset:1024 sc1\n"
        "ds_read_b64_tr_b16 v[48:49], v226 offset:16384\n"
        "ds_read_b64_tr_b16 v[50:51], v227 offset:16384\n"
        "s_add_u32 s70, s38, s97\n"
        "s_addc_u32 s71, s39, 0\n"
        "v_mfma_f32_16x16x32_bf16 v[16:19], v[160:163], v[192:195], v[16:19]\n"
        "ds_read_b64_tr_b16 v[54:55], v227 offset:24576\n"
        "s_add_u32 s97, s53, 0x6000\n"
        "v_mfma_f32_16x16x32_bf16 v[16:19], v[164:167], v[208:211], v[16:19]\n"
        "ds_read_b64_tr_b16 v[62:63], v229 offset:24576\n"
        "v_mfma_f32_16x16x32_bf16 v[20:23], v[172:175], v[208:211], v[20:23]\n"
        "ds_read_b64_tr_b16 v[58:59], v229 offset:16384\n"
        "v_mfma_f32_16x16x32_bf16 v[20:23], v[168:171], v[192:195], v[20:23]\n"
        "v_mfma_f32_16x16x32_bf16 v[84:87], v[168:171], v[196:199], v[84:87]\n"
        "v_mfma_f32_16x16x32_bf16 v[84:87], v[172:175], v[212:215], v[84:87]\n"
        "v_mfma_f32_16x16x32_bf16 v[80:83], v[164:167], v[212:215], v[80:83]\n"
        "v_mfma_f32_16x16x32_bf16 v[80:83], v[160:163], v[196:199], v[80:83]\n"
        "v_mfma_f32_16x16x32_bf16 v[112:115], v[160:163], v[200:203], v[112:115]\n"
        "v_mfma_f32_16x16x32_bf16 v[112:115], v[164:167], v[216:219], v[112:115]\n"
        "v_mfma_f32_16x16x32_bf16 v[116:119], v[172:175], v[216:219], v[116:119]\n"
        "v_mfma_f32_16x16x32_bf16 v[116:119], v[168:171], v[200:203], v[116:119]\n"
        "global_store_dwordx4 v254, a[8:11], s[70:71] offset:0 sc1\n"
        "v_mfma_f32_16x16x32_bf16 v[148:151], v[168:171], v[204:207], v[148:151]\n"
        "ds_read_b64_tr_b16 v[168:169], v232 offset:16384\n"
        "v_mfma_f32_16x16x32_bf16 v[148:151], v[172:175], v[220:223], v[148:151]\n"
        "ds_read_b64_tr_b16 v[172:173], v232 offset:24576\n"
        "v_mfma_f32_16x16x32_bf16 v[144:147], v[164:167], v[220:223], v[144:147]\n"
        "ds_read_b64_tr_b16 v[164:165], v230 offset:24576\n"
        "v_mfma_f32_16x16x32_bf16 v[144:147], v[160:163], v[204:207], v[144:147]\n"
        "ds_read_b64_tr_b16 v[160:161], v230 offset:16384\n"
        "v_mfma_f32_16x16x32_bf16 v[24:27], v[32:35], v[192:195], v[24:27]\n"
        "ds_read_b64_tr_b16 v[162:163], v231 offset:16384\n"
        "v_mfma_f32_16x16x32_bf16 v[24:27], v[36:39], v[208:211], v[24:27]\n"
        "ds_read_b64_tr_b16 v[166:167], v231 offset:24576\n"
        "v_mfma_f32_16x16x32_bf16 v[28:31], v[44:47], v[208:211], v[28:31]\n"
        "ds_read_b64_tr_b16 v[174:175], v233 offset:24576\n"
        "v_mfma_f32_16x16x32_bf16 v[28:31], v[40:43], v[192:195], v[28:31]\n"
        "ds_read_b64_tr_b16 v[170:171], v233 offset:16384\n"
        "v_mfma_f32_16x16x32_bf16 v[92:95], v[40:43], v[196:199], v[92:95]\n"
        "ds_read_b128 v[192:195], v246 offset:34816\n"
        "v_mfma_f32_16x16x32_bf16 v[92:95], v[44:47], v[212:215], v[92:95]\n"
        "ds_read_b128 v[208:211], v247 offset:34816\n"
        "v_mfma_f32_16x16x32_bf16 v[88:91], v[36:39], v[212:215], v[88:91]\n"
        "ds_read_b128 v[212:215], v247 offset:32768\n"
        "v_mfma_f32_16x16x32_bf16 v[88:91], v[32:35], v[196:199], v[88:91]\n"
        "ds_read_b128 v[196:199], v246 offset:32768\n"
        "v_mfma_f32_16x16x32_bf16 v[120:123], v[32:35], v[200:203], v[120:123]\n"
        "global_store_dwordx4 v254, a[12:15], s[70:71] offset:1024 sc1\n"
        "s_add_u32 s70, s38, s97\n"
        "s_addc_u32 s71, s39, 0\n"
        "s_add_u32 s97, s53, 0x4000\n"
        "v_mfma_f32_16x16x32_bf16 v[120:123], v[36:39], v[216:219], v[120:123]\n"
        "v_mfma_f32_16x16x32_bf16 v[124:127], v[44:47], v[216:219], v[124:127]\n"
        "ds_read_b128 v[216:219], v247 offset:38912\n"
        "s_waitcnt lgkmcnt(5)\n"
        "v_mfma_f32_16x16x32_bf16 v[124:127], v[40:43], v[200:203], v[124:127]\n"
        "ds_read_b128 v[200:203], v246 offset:38912\n"
        "s_waitcnt lgkmcnt(5)\n"
        "v_mfma_f32_16x16x32_bf16 v[156:159], v[40:43], v[204:207], v[156:159]\n"
        "ds_read_b64_tr_b16 v[40:41], v228 offset:17408\n"
        "s_waitcnt lgkmcnt(5)\n"
        "v_mfma_f32_16x16x32_bf16 v[156:159], v[44:47], v[220:223], v[156:159]\n"
        "ds_read_b64_tr_b16 v[44:45], v228 offset:25600\n"
        "s_waitcnt lgkmcnt(5)\n"
        "v_mfma_f32_16x16x32_bf16 v[152:155], v[36:39], v[220:223], v[152:155]\n"
        "ds_read_b128 v[220:223], v247 offset:36864\n"
        "s_waitcnt lgkmcnt(5)\n"
        "v_mfma_f32_16x16x32_bf16 v[152:155], v[32:35], v[204:207], v[152:155]\n"
        "ds_read_b128 v[204:207], v246 offset:36864\n"
        "v_mfma_f32_16x16x32_bf16 v[0:3], v[48:51], v[192:195], v[0:3]\n"
        "ds_read_b64_tr_b16 v[32:33], v226 offset:17408\n"
        "v_mfma_f32_16x16x32_bf16 v[0:3], v[52:55], v[208:211], v[0:3]\n"
        "ds_read_b64_tr_b16 v[34:35], v227 offset:17408\n"
        "v_mfma_f32_16x16x32_bf16 v[4:7], v[60:63], v[208:211], v[4:7]\n"
        "ds_read_b64_tr_b16 v[36:37], v226 offset:25600\n"
        "s_waitcnt lgkmcnt(8)\n"
        "v_mfma_f32_16x16x32_bf16 v[4:7], v[56:59], v[192:195], v[4:7]\n"
        "ds_read_b64_tr_b16 v[38:39], v227 offset:25600\n"
        "s_waitcnt lgkmcnt(8)\n"
        "v_mfma_f32_16x16x32_bf16 v[68:71], v[56:59], v[196:199], v[68:71]\n"
        "ds_read_b64_tr_b16 v[46:47], v229 offset:25600\n"
        "v_mfma_f32_16x16x32_bf16 v[68:71], v[60:63], v[212:215], v[68:71]\n"
        "ds_read_b64_tr_b16 v[42:43], v229 offset:17408\n"
        "s_mul_i32 s80, s40, 4224\n"
        "s_waitcnt lgkmcnt(8)\n"
        "v_mfma_f32_16x16x32_bf16 v[64:67], v[52:55], v[212:215], v[64:67]\n"
        "global_store_dwordx4 v254, a[208:211], s[70:71] offset:0 sc1\n"
        "s_add_u32 s68, s92, 0x2\n"
        "s_addc_u32 s69, s93, 0\n"
        "s_add_u32 m0, s80, 0x20260\n"
        "s_waitcnt lgkmcnt(6)\n"
        "v_mfma_f32_16x16x32_bf16 v[64:67], v[48:51], v[196:199], v[64:67]\n"
        "v_mfma_f32_16x16x32_bf16 v[96:99], v[48:51], v[200:203], v[96:99]\n"
        "v_mfma_f32_16x16x32_bf16 v[96:99], v[52:55], v[216:219], v[96:99]\n"
        "v_mfma_f32_16x16x32_bf16 v[100:103], v[60:63], v[216:219], v[100:103]\n"
        "v_mfma_f32_16x16x32_bf16 v[100:103], v[56:59], v[200:203], v[100:103]\n"
        "v_mfma_f32_16x16x32_bf16 v[132:135], v[56:59], v[204:207], v[132:135]\n"
        "ds_read_b64_tr_b16 v[56:57], v232 offset:17408\n"
        "v_mfma_f32_16x16x32_bf16 v[132:135], v[60:63], v[220:223], v[132:135]\n"
        "ds_read_b64_tr_b16 v[60:61], v232 offset:25600\n"
        "v_mfma_f32_16x16x32_bf16 v[128:131], v[52:55], v[220:223], v[128:131]\n"
        "ds_read_b64_tr_b16 v[52:53], v230 offset:25600\n"
        "v_mfma_f32_16x16x32_bf16 v[128:131], v[48:51], v[204:207], v[128:131]\n"
        "global_store_dwordx4 v254, a[240:243], s[70:71] offset:1024 sc1\n"
        "ds_read_b64_tr_b16 v[48:49], v230 offset:17408\n"
        "ds_read_b64_tr_b16 v[50:51], v231 offset:17408\n"
        "v_mfma_f32_16x16x32_bf16 v[8:11], v[160:163], v[192:195], v[8:11]\n"
        "ds_read_b64_tr_b16 v[54:55], v231 offset:25600\n"
        "v_mfma_f32_16x16x32_bf16 v[8:11], v[164:167], v[208:211], v[8:11]\n"
        "ds_read_b64_tr_b16 v[62:63], v233 offset:25600\n"
        "v_mfma_f32_16x16x32_bf16 v[12:15], v[172:175], v[208:211], v[12:15]\n"
        "ds_read_b64_tr_b16 v[58:59], v233 offset:17408\n"
        "v_mfma_f32_16x16x32_bf16 v[12:15], v[168:171], v[192:195], v[12:15]\n"
        "v_mfma_f32_16x16x32_bf16 v[76:79], v[168:171], v[196:199], v[76:79]\n"
        "v_mfma_f32_16x16x32_bf16 v[76:79], v[172:175], v[212:215], v[76:79]\n"
        "s_waitcnt lgkmcnt(6)\n"
        "v_mfma_f32_16x16x32_bf16 v[72:75], v[164:167], v[212:215], v[72:75]\n"
        "v_mfma_f32_16x16x32_bf16 v[72:75], v[160:163], v[196:199], v[72:75]\n"
        "v_mfma_f32_16x16x32_bf16 v[104:107], v[160:163], v[200:203], v[104:107]\n"
        "v_mfma_f32_16x16x32_bf16 v[104:107], v[164:167], v[216:219], v[104:107]\n"
        "v_mfma_f32_16x16x32_bf16 v[108:111], v[172:175], v[216:219], v[108:111]\n"
        "s_waitcnt vmcnt(7) lgkmcnt(0)\n"
        "s_barrier\n"
        "v_mfma_f32_16x16x32_bf16 v[108:111], v[168:171], v[200:203], v[108:111]\n"
        "global_store_dwordx4 v254, a[212:215], s[70:71] offset:2048 sc1\n"
        "v_mfma_f32_16x16x32_bf16 v[140:143], v[168:171], v[204:207], v[140:143]\n"
        "ds_write_b128 v244, a[224:227] offset:12288\n"
        "v_mfma_f32_16x16x32_bf16 v[140:143], v[172:175], v[220:223], v[140:143]\n"
        "ds_read_b64_tr_b16 v[172:173], v228 offset:40960\n"
        "v_mfma_f32_16x16x32_bf16 v[136:139], v[164:167], v[220:223], v[136:139]\n"
        "ds_read_b64_tr_b16 v[164:165], v226 offset:40960\n"
        "v_mfma_f32_16x16x32_bf16 v[136:139], v[160:163], v[204:207], v[136:139]\n"
        "ds_read_b64_tr_b16 v[160:161], v226 offset:32768\n"
        "v_mfma_f32_16x16x32_bf16 v[16:19], v[32:35], v[192:195], v[16:19]\n"
        "ds_read_b64_tr_b16 v[162:163], v227 offset:32768\n"
        "v_mfma_f32_16x16x32_bf16 v[16:19], v[36:39], v[208:211], v[16:19]\n"
        "ds_read_b64_tr_b16 v[166:167], v227 offset:40960\n"
        "v_mfma_f32_16x16x32_bf16 v[20:23], v[44:47], v[208:211], v[20:23]\n"
        "ds_read_b64_tr_b16 v[174:175], v229 offset:40960\n"
        "v_mfma_f32_16x16x32_bf16 v[20:23], v[40:43], v[192:195], v[20:23]\n"
        "ds_read_b64_tr_b16 v[168:169], v228 offset:32768\n"
        "v_mfma_f32_16x16x32_bf16 v[84:87], v[40:43], v[196:199], v[84:87]\n"
        "ds_read_b64_tr_b16 v[170:171], v229 offset:32768\n"
        "s_lshl_b32 s80, s41, 5\n"
        "s_add_u32 s80, s80, 256\n"
        "v_mfma_f32_16x16x32_bf16 v[84:87], v[44:47], v[212:215], v[84:87]\n"
        "ds_write_b128 v244, a[228:231] offset:13312\n"
        "v_mfma_f32_16x16x32_bf16 v[80:83], v[36:39], v[212:215], v[80:83]\n"
        "ds_write_b128 v244, a[232:235] offset:8192\n"
        "v_mfma_f32_16x16x32_bf16 v[80:83], v[32:35], v[196:199], v[80:83]\n"
        "global_store_dwordx4 v254, a[244:247], s[70:71] offset:3072 sc1\n"
        "s_add_u32 s70, s38, s97\n"
        "s_addc_u32 s71, s39, 0\n"
        "s_add_u32 s97, s53, 0x0\n"
        "s_add_u32 s81, s80, s42\n"
        "s_lshl_b32 s81, s81, 7\n"
        "v_mfma_f32_16x16x32_bf16 v[112:115], v[32:35], v[200:203], v[112:115]\n"
        "ds_write_b128 v244, a[236:239] offset:9216\n"
        "v_mfma_f32_16x16x32_bf16 v[112:115], v[36:39], v[216:219], v[112:115]\n"
        "ds_write_b128 v244, a[192:195] offset:4096\n"
        "v_mfma_f32_16x16x32_bf16 v[116:119], v[44:47], v[216:219], v[116:119]\n"
        "ds_write_b128 v244, a[196:199] offset:5120\n"
        "v_mfma_f32_16x16x32_bf16 v[116:119], v[40:43], v[200:203], v[116:119]\n"
        "ds_write_b128 v244, a[200:203] offset:16384\n"
        "v_mfma_f32_16x16x32_bf16 v[148:151], v[40:43], v[204:207], v[148:151]\n"
        "ds_read_b64_tr_b16 v[40:41], v232 offset:32768\n"
        "s_and_b32 s80, s81, 0xfffffc7f\n"
        "v_mfma_f32_16x16x32_bf16 v[148:151], v[44:47], v[220:223], v[148:151]\n"
        "ds_read_b64_tr_b16 v[44:45], v232 offset:40960\n"
        "v_mfma_f32_16x16x32_bf16 v[144:147], v[36:39], v[220:223], v[144:147]\n"
        "ds_read_b64_tr_b16 v[36:37], v230 offset:40960\n"
        "v_mfma_f32_16x16x32_bf16 v[144:147], v[32:35], v[204:207], v[144:147]\n"
        "ds_read_b64_tr_b16 v[32:33], v230 offset:32768\n"
        "v_mfma_f32_16x16x32_bf16 v[24:27], v[48:51], v[192:195], v[24:27]\n"
        "ds_read_b64_tr_b16 v[34:35], v231 offset:32768\n"
        "v_mfma_f32_16x16x32_bf16 v[24:27], v[52:55], v[208:211], v[24:27]\n"
        "ds_read_b64_tr_b16 v[38:39], v231 offset:40960\n"
        "v_mfma_f32_16x16x32_bf16 v[28:31], v[60:63], v[208:211], v[28:31]\n"
        "ds_read_b64_tr_b16 v[46:47], v233 offset:40960\n"
        "v_mfma_f32_16x16x32_bf16 v[28:31], v[56:59], v[192:195], v[28:31]\n"
        "ds_read_b64_tr_b16 v[42:43], v233 offset:32768\n"
        "v_mfma_f32_16x16x32_bf16 v[92:95], v[56:59], v[196:199], v[92:95]\n"
        "ds_read_b128 v[192:195], v246 offset:2048\n"
        "v_mfma_f32_16x16x32_bf16 v[92:95], v[60:63], v[212:215], v[92:95]\n"
        "ds_read_b128 v[208:211], v247 offset:2048\n"
        "v_mfma_f32_16x16x32_bf16 v[88:91], v[52:55], v[212:215], v[88:91]\n"
        "global_store_dwordx4 v254, a[216:219], s[70:71] offset:0 sc1\n"
        "ds_read_b128 v[212:215], v247 offset:0\n"
        "v_mfma_f32_16x16x32_bf16 v[88:91], v[48:51], v[196:199], v[88:91]\n"
        "ds_write_b128 v244, a[204:207] offset:17408\n"
        "v_mfma_f32_16x16x32_bf16 v[120:123], v[48:51], v[200:203], v[120:123]\n"
        "ds_read_b128 v[196:199], v246 offset:0\n"
        "v_mfma_f32_16x16x32_bf16 v[120:123], v[52:55], v[216:219], v[120:123]\n"
        "ds_write_b128 v244, a[160:163] offset:20480\n"
        "v_mfma_f32_16x16x32_bf16 v[124:127], v[60:63], v[216:219], v[124:127]\n"
        "ds_write_b128 v244, a[164:167] offset:21504\n"
        "v_mfma_f32_16x16x32_bf16 v[124:127], v[56:59], v[200:203], v[124:127]\n"
        "ds_read_b128 v[200:203], v246 offset:6144\n"
        "s_waitcnt lgkmcnt(5)\n"
        "v_mfma_f32_16x16x32_bf16 v[156:159], v[56:59], v[204:207], v[156:159]\n"
        "ds_read_b128 v[216:219], v247 offset:6144\n"
        "v_mfma_f32_16x16x32_bf16 v[156:159], v[60:63], v[220:223], v[156:159]\n"
        "ds_write_b128 v244, a[168:171] offset:24576\n"
        "v_mfma_f32_16x16x32_bf16 v[152:155], v[52:55], v[220:223], v[152:155]\n"
        "ds_read_b128 v[220:223], v247 offset:4096\n"
        "s_waitcnt lgkmcnt(6)\n"
        "v_mfma_f32_16x16x32_bf16 v[152:155], v[48:51], v[204:207], v[152:155]\n"
        "ds_read_b128 v[204:207], v246 offset:4096\n"
        "v_mfma_f32_16x16x32_bf16 v[0:3], v[160:163], v[192:195], v[0:3]\n"
        "global_store_dwordx4 v254, a[248:251], s[70:71] offset:1024 sc1\n"
        "ds_read_b64_tr_b16 v[48:49], v226 offset:33792\n"
        "ds_read_b64_tr_b16 v[50:51], v227 offset:33792\n"
        "s_waitcnt lgkmcnt(7)\n"
        "v_mfma_f32_16x16x32_bf16 v[0:3], v[164:167], v[208:211], v[0:3]\n"
        "ds_read_b64_tr_b16 v[52:53], v226 offset:41984\n"
        "s_waitcnt lgkmcnt(7)\n"
        "v_mfma_f32_16x16x32_bf16 v[4:7], v[172:175], v[208:211], v[4:7]\n"
        "ds_read_b64_tr_b16 v[54:55], v227 offset:41984\n"
        "s_waitcnt lgkmcnt(7)\n"
        "v_mfma_f32_16x16x32_bf16 v[4:7], v[168:171], v[192:195], v[4:7]\n"
        "ds_read_b64_tr_b16 v[60:61], v228 offset:41984\n"
        "v_mfma_f32_16x16x32_bf16 v[68:71], v[168:171], v[196:199], v[68:71]\n"
        "ds_read_b64_tr_b16 v[62:63], v229 offset:41984\n"
        "v_cvt_pk_bf16_f32 v0, v0, v1\n"
        "v_mfma_f32_16x16x32_bf16 v[68:71], v[172:175], v[212:215], v[68:71]\n"
        "ds_read_b64_tr_b16 v[56:57], v228 offset:33792\n"
        "v_cvt_pk_bf16_f32 v1, v2, v3\n"
        "v_mfma_f32_16x16x32_bf16 v[64:67], v[164:167], v[212:215], v[64:67]\n"
        "ds_read_b64_tr_b16 v[58:59], v229 offset:33792\n"
        "v_cvt_pk_bf16_f32 v2, v4, v5\n"
        "v_mfma_f32_16x16x32_bf16 v[64:67], v[160:163], v[196:199], v[64:67]\n"
        "ds_write_b128 v244, a[172:175] offset:25600\n"
        "v_mfma_f32_16x16x32_bf16 v[96:99], v[160:163], v[200:203], v[96:99]\n"
        "ds_write_b128 v244, a[0:3] offset:28672\n"
        "v_mfma_f32_16x16x32_bf16 v[96:99], v[164:167], v[216:219], v[96:99]\n"
        "ds_write_b128 v244, a[4:7] offset:29696\n"
        "v_mfma_f32_16x16x32_bf16 v[100:103], v[172:175], v[216:219], v[100:103]\n"
        "s_waitcnt lgkmcnt(5)\n"
        "v_cvt_pk_bf16_f32 v3, v6, v7\n"
        "v_cvt_pk_bf16_f32 v64, v64, v65\n"
        "v_mfma_f32_16x16x32_bf16 v[100:103], v[168:171], v[200:203], v[100:103]\n"
        "ds_write_b128 v244, a[8:11] offset:0\n"
        "v_mfma_f32_16x16x32_bf16 v[132:135], v[168:171], v[204:207], v[132:135]\n"
        "global_store_dwordx4 v254, a[220:223], s[70:71] offset:2048 sc1\n"
        "ds_read_b64_tr_b16 v[168:169], v232 offset:33792\n"
        "ds_read_b64_tr_b16 v[170:171], v233 offset:33792\n"
        "v_cvt_pk_bf16_f32 v65, v66, v67\n"
        "v_mfma_f32_16x16x32_bf16 v[132:135], v[172:175], v[220:223], v[132:135]\n"
        "ds_write_b128 v244, a[12:15] offset:1024\n"
        "v_mfma_f32_16x16x32_bf16 v[128:131], v[164:167], v[220:223], v[128:131]\n"
        "ds_read_b64_tr_b16 v[164:165], v230 offset:41984\n"
        "v_cvt_pk_bf16_f32 v66, v68, v69\n"
        "v_mfma_f32_16x16x32_bf16 v[128:131], v[160:163], v[204:207], v[128:131]\n"
        "ds_read_b64_tr_b16 v[160:161], v230 offset:33792\n"
        "v_cvt_pk_bf16_f32 v67, v70, v71\n"
        "v_mfma_f32_16x16x32_bf16 v[8:11], v[32:35], v[192:195], v[8:11]\n"
        "ds_read_b64_tr_b16 v[162:163], v231 offset:33792\n"
        "v_cvt_pk_bf16_f32 v96, v96, v97\n"
        "v_mfma_f32_16x16x32_bf16 v[8:11], v[36:39], v[208:211], v[8:11]\n"
        "ds_read_b64_tr_b16 v[166:167], v231 offset:41984\n"
        "v_cvt_pk_bf16_f32 v97, v98, v99\n"
        "v_mfma_f32_16x16x32_bf16 v[12:15], v[44:47], v[208:211], v[12:15]\n"
        "ds_read_b64_tr_b16 v[172:173], v232 offset:41984\n"
        "v_cvt_pk_bf16_f32 v98, v100, v101\n"
        "v_mfma_f32_16x16x32_bf16 v[12:15], v[40:43], v[192:195], v[12:15]\n"
        "ds_read_b64_tr_b16 v[174:175], v233 offset:41984\n"
        "v_cvt_pk_bf16_f32 v99, v102, v103\n"
        "v_mfma_f32_16x16x32_bf16 v[76:79], v[40:43], v[196:199], v[76:79]\n"
        "v_cvt_pk_bf16_f32 v4, v8, v9\n"
        "v_cvt_pk_bf16_f32 v5, v10, v11\n"
        "v_mfma_f32_16x16x32_bf16 v[76:79], v[44:47], v[212:215], v[76:79]\n"
        "v_cvt_pk_bf16_f32 v128, v128, v129\n"
        "v_cvt_pk_bf16_f32 v129, v130, v131\n"
        "v_mfma_f32_16x16x32_bf16 v[72:75], v[36:39], v[212:215], v[72:75]\n"
        "v_cvt_pk_bf16_f32 v6, v12, v13\n"
        "v_cvt_pk_bf16_f32 v7, v14, v15\n"
        "v_mfma_f32_16x16x32_bf16 v[72:75], v[32:35], v[196:199], v[72:75]\n"
        "global_store_dwordx4 v254, a[252:255], s[70:71] offset:3072 sc1\n"
        "s_add_u32 s70, s38, s97\n"
        "s_addc_u32 s71, s39, 0\n"
        "s_add_u32 s97, s53, 0x2000\n"
        "v_cvt_pk_bf16_f32 v70, v76, v77\n"
        "v_cvt_pk_bf16_f32 v71, v78, v79\n"
        "v_mfma_f32_16x16x32_bf16 v[104:107], v[32:35], v[200:203], v[104:107]\n"
        "v_cvt_pk_bf16_f32 v130, v132, v133\n"
        "v_cvt_pk_bf16_f32 v131, v134, v135\n"
        "v_mfma_f32_16x16x32_bf16 v[104:107], v[36:39], v[216:219], v[104:107]\n"
        "s_waitcnt lgkmcnt(2)\n"
        "v_cvt_pk_bf16_f32 v68, v72, v73\n"
        "v_cvt_pk_bf16_f32 v69, v74, v75\n"
        "v_mfma_f32_16x16x32_bf16 v[108:111], v[44:47], v[216:219], v[108:111]\n"
        "s_waitcnt lgkmcnt(0)\n"
        "s_barrier\n"
        "v_mfma_f32_16x16x32_bf16 v[108:111], v[40:43], v[200:203], v[108:111]\n"
        "ds_write_b128 v245, a[208:211] offset:8192\n"
        "v_mfma_f32_16x16x32_bf16 v[140:143], v[40:43], v[204:207], v[140:143]\n"
        "ds_write_b128 v245, a[240:243] offset:9216\n"
        "v_mfma_f32_16x16x32_bf16 v[140:143], v[44:47], v[220:223], v[140:143]\n"
        "ds_write_b128 v245, a[212:215] offset:10240\n"
        "v_mfma_f32_16x16x32_bf16 v[136:139], v[36:39], v[220:223], v[136:139]\n"
        "ds_write_b128 v245, a[244:247] offset:11264\n"
        "v_mfma_f32_16x16x32_bf16 v[136:139], v[32:35], v[204:207], v[136:139]\n"
        "ds_write_b128 v245, a[216:219] offset:0\n"
        "v_mfma_f32_16x16x32_bf16 v[16:19], v[48:51], v[192:195], v[16:19]\n"
        "ds_write_b128 v245, a[248:251] offset:1024\n"
        "v_mfma_f32_16x16x32_bf16 v[16:19], v[52:55], v[208:211], v[16:19]\n"
        "ds_write_b128 v245, a[220:223] offset:2048\n"
        "v_mfma_f32_16x16x32_bf16 v[20:23], v[60:63], v[208:211], v[20:23]\n"
        "ds_write_b128 v245, a[252:255] offset:3072\n"
        "v_mfma_f32_16x16x32_bf16 v[20:23], v[56:59], v[192:195], v[20:23]\n"
        "global_store_dwordx4 v254, a[176:179], s[70:71] offset:0 sc1\n"
        "s_barrier\n"
        "ds_read_b128 a[96:99], v240 offset:0\n"
        "v_mfma_f32_16x16x32_bf16 v[84:87], v[56:59], v[196:199], v[84:87]\n"
        "ds_read_b128 a[100:103], v240 offset:1024\n"
        "v_cvt_pk_bf16_f32 v8, v16, v17\n"
        "v_mfma_f32_16x16x32_bf16 v[84:87], v[60:63], v[212:215], v[84:87]\n"
        "ds_read_b128 a[104:107], v240 offset:2048\n"
        "v_cvt_pk_bf16_f32 v9, v18, v19\n"
        "v_mfma_f32_16x16x32_bf16 v[80:83], v[52:55], v[212:215], v[80:83]\n"
        "ds_read_b128 a[108:111], v240 offset:3072\n"
        "v_cvt_pk_bf16_f32 v10, v20, v21\n"
        "v_mfma_f32_16x16x32_bf16 v[80:83], v[48:51], v[196:199], v[80:83]\n"
        "ds_read_b128 a[112:115], v240 offset:4096\n"
        "v_cvt_pk_bf16_f32 v11, v22, v23\n"
        "v_mfma_f32_16x16x32_bf16 v[112:115], v[48:51], v[200:203], v[112:115]\n"
        "ds_read_b128 a[116:119], v240 offset:5120\n"
        "v_cndmask_b32 v32, 0, v242, s[10:11]\n"
        "v_mfma_f32_16x16x32_bf16 v[112:115], v[52:55], v[216:219], v[112:115]\n"
        "ds_read_b128 a[120:123], v240 offset:6144\n"
        "v_cndmask_b32 v33, 0, v242, s[12:13]\n"
        "v_mfma_f32_16x16x32_bf16 v[116:119], v[60:63], v[216:219], v[116:119]\n"
        "ds_write_b128 v245, a[176:179] offset:16384\n"
        "v_mfma_f32_16x16x32_bf16 v[116:119], v[56:59], v[200:203], v[116:119]\n"
        "ds_write_b128 v245, a[180:183] offset:17408\n"
        "v_mfma_f32_16x16x32_bf16 v[148:151], v[56:59], v[204:207], v[148:151]\n"
        "ds_write_b128 v245, a[184:187] offset:18432\n"
        "v_mfma_f32_16x16x32_bf16 v[148:151], v[60:63], v[220:223], v[148:151]\n"
        "ds_write_b128 v245, a[188:191] offset:19456\n"
        "v_mfma_f32_16x16x32_bf16 v[144:147], v[52:55], v[220:223], v[144:147]\n"
        "ds_write_b128 v245, a[16:19] offset:24576\n"
        "v_mfma_f32_16x16x32_bf16 v[144:147], v[48:51], v[204:207], v[144:147]\n"
        "global_store_dwordx4 v254, a[180:183], s[70:71] offset:1024 sc1\n"
        "ds_read_b128 a[124:127], v240 offset:7168\n"
        "s_and_b32 s52, s41, 1\n"
        "v_cndmask_b32 v34, 0, v242, s[14:15]\n"
        "v_cndmask_b32 v35, 0, v242, s[16:17]\n"
        "v_cndmask_b32 v48, 0, v242, s[20:21]\n"
        "v_mfma_f32_16x16x32_bf16 v[24:27], v[160:163], v[192:195], v[24:27]\n"
        "ds_write_b128 v245, a[20:23] offset:25600\n"
        "v_mfma_f32_16x16x32_bf16 v[24:27], v[164:167], v[208:211], v[24:27]\n"
        "ds_write_b128 v245, a[24:27] offset:26624\n"
        "v_mfma_f32_16x16x32_bf16 v[28:31], v[172:175], v[208:211], v[28:31]\n"
        "ds_write_b128 v245, a[28:31] offset:27648\n"
        "v_mfma_f32_16x16x32_bf16 v[28:31], v[168:171], v[192:195], v[28:31]\n"
        "global_store_dwordx4 v254, a[188:191], s[70:71] offset:3072 sc1\n"
        "s_lshl_b32 s52, s52, 11\n"
        "v_cndmask_b32 v49, 0, v242, s[22:23]\n"
        "v_cndmask_b32 v50, 0, v242, s[24:25]\n"
        "v_cvt_pk_bf16_f32 v12, v24, v25\n"
        "v_cvt_pk_bf16_f32 v13, v26, v27\n"
        "v_mfma_f32_16x16x32_bf16 v[92:95], v[168:171], v[196:199], v[92:95]\n"
        "s_lshl2_add_u32 s53, s42, s40\n"
        "v_cvt_pk_bf16_f32 v14, v28, v29\n"
        "v_mfma_f32_16x16x32_bf16 v[92:95], v[172:175], v[212:215], v[92:95]\n"
        "s_waitcnt lgkmcnt(2)\n"
        "v_cvt_pk_bf16_f32 v15, v30, v31\n"
        "v_cndmask_b32 v51, 0, v242, s[28:29]\n"
        "v_mfma_f32_16x16x32_bf16 v[88:91], v[164:167], v[212:215], v[88:91]\n"
        "s_add_u32 s52, s52, s53\n"
        "v_cvt_pk_bf16_f32 v72, v80, v81\n"
        "v_cvt_pk_bf16_f32 v73, v82, v83\n"
        "v_mfma_f32_16x16x32_bf16 v[88:91], v[160:163], v[196:199], v[88:91]\n"
        "global_load_lds_dwordx4 v254, s[68:69] offset:0\n"
        "s_add_u32 s52, s52, 1920\n"
        "v_cvt_pk_bf16_f32 v74, v84, v85\n"
        "v_mfma_f32_16x16x32_bf16 v[120:123], v[160:163], v[200:203], v[120:123]\n"
        "s_waitcnt lgkmcnt(0)\n"
        "v_mfma_f32_16x16x32_bf16 v[120:123], v[164:167], v[216:219], v[120:123]\n"
        "s_barrier\n"
        "ds_read_b128 a[144:147], v241 offset:0\n"
        "s_lshr_b32 s53, s41, 2\n"
        "v_mfma_f32_16x16x32_bf16 v[124:127], v[172:175], v[216:219], v[124:127]\n"
        "ds_read_b128 a[148:151], v241 offset:1024\n"
        "s_lshl1_add_u32 s52, s52, s53\n"
        "v_cvt_pk_bf16_f32 v75, v86, v87\n"
        "v_mfma_f32_16x16x32_bf16 v[124:127], v[168:171], v[200:203], v[124:127]\n"
        "global_store_dwordx4 v254, a[184:187], s[70:71] offset:2048 sc1\n"
        "s_add_u32 s70, s38, s97\n"
        "s_addc_u32 s71, s39, 0\n"
        "ds_read_b128 a[152:155], v241 offset:2048\n"
        "s_lshl_b32 s52, s52, 10\n"
        "v_cvt_pk_bf16_f32 v76, v88, v89\n"
        "v_mfma_f32_16x16x32_bf16 v[156:159], v[168:171], v[204:207], v[156:159]\n"
        "ds_read_b128 a[156:159], v241 offset:3072\n"
        "v_cvt_pk_bf16_f32 v77, v90, v91\n"
        "v_mfma_f32_16x16x32_bf16 v[156:159], v[172:175], v[220:223], v[156:159]\n"
        "ds_read_b128 a[128:131], v241 offset:4096\n"
        "v_cvt_pk_bf16_f32 v78, v92, v93\n"
        "v_mfma_f32_16x16x32_bf16 v[152:155], v[164:167], v[220:223], v[152:155]\n"
        "ds_read_b128 a[132:135], v241 offset:5120\n"
        "v_cvt_pk_bf16_f32 v79, v94, v95\n"
        "v_mfma_f32_16x16x32_bf16 v[152:155], v[160:163], v[204:207], v[152:155]\n"
        "ds_read_b128 a[136:139], v241 offset:6144\n"
        "LHA10_%=:\n"
        "s_cbranch_execz LHRA_%=\n"
        "v_cvt_pk_bf16_f32 v100, v104, v105\n"
        "v_mfma_f32_16x16x32_bf16 v[32:35], a[96:99], v[0:3], v[32:35]\n"
        "ds_read_b128 a[96:99], v240 offset:8192\n"
        "v_cvt_pk_bf16_f32 v101, v106, v107\n"
        "v_mfma_f32_16x16x32_bf16 v[32:35], a[100:103], v[4:7], v[32:35]\n"
        "ds_read_b128 a[100:103], v240 offset:9216\n"
        "v_cvt_pk_bf16_f32 v102, v108, v109\n"
        "v_mfma_f32_16x16x32_bf16 v[32:35], a[104:107], v[8:11], v[32:35]\n"
        "ds_read_b128 a[104:107], v240 offset:10240\n"
        "v_cvt_pk_bf16_f32 v103, v110, v111\n"
        "v_mfma_f32_16x16x32_bf16 v[32:35], a[108:111], v[12:15], v[32:35]\n"
        "global_store_dwordx4 v254, a[16:19], s[70:71] offset:0 sc1\n"
        "ds_read_b128 a[108:111], v240 offset:11264\n"
        "ds_read_b128 a[140:143], v241 offset:7168\n"
        "v_cndmask_b32 v36, 0, v242, s[10:11]\n"
        "v_mfma_f32_16x16x32_bf16 v[48:51], a[112:115], v[0:3], v[48:51]\n"
        "ds_read_b128 a[112:115], v240 offset:12288\n"
        "v_cndmask_b32 v37, 0, v242, s[12:13]\n"
        "v_mfma_f32_16x16x32_bf16 v[48:51], a[116:119], v[4:7], v[48:51]\n"
        "ds_read_b128 a[116:119], v240 offset:13312\n"
        "v_max_f32 v246, v32, v33\n"
        "v_mfma_f32_16x16x32_bf16 v[48:51], a[120:123], v[8:11], v[48:51]\n"
        "ds_read_b128 a[120:123], v240 offset:14336\n"
        "v_max3_f32 v246, v34, v35, v246\n"
        "v_mfma_f32_16x16x32_bf16 v[48:51], a[124:127], v[12:15], v[48:51]\n"
        "v_mov_b32 v247, v246\n"
        "global_store_dwordx4 v254, a[20:23], s[70:71] offset:1024 sc1\n"
        "ds_read_b128 a[124:127], v240 offset:15360\n"
        "v_permlane16_swap_b32 v246, v247\n"
        "v_max_f32 v246, v246, v247\n"
        "v_mov_b32 v247, v246\n"
        "v_cndmask_b32 v38, 0, v242, s[14:15]\n"
        "v_cndmask_b32 v39, 0, v242, s[16:17]\n"
        "v_permlane32_swap_b32 v246, v247\n"
        "v_max_f32 v246, v246, v247\n"
        "v_sub_f32 v224, 0, v246\n"
        "v_sub_f32 v225, 0, v246\n"
        "v_sub_f32 v226, 0, v246\n"
        "v_sub_f32 v227, 0, v246\n"
        "v_add_f32 v32, v32, v224\n"
        "v_exp_f32 v32, v32\n"
        "v_add_f32 v33, v33, v224\n"
        "v_exp_f32 v33, v33\n"
        "v_add_f32 v34, v34, v224\n"
        "v_cvt_pk_bf16_f32 v16, v32, v33\n"
        "v_exp_f32 v34, v34\n"
        "v_add_f32 v35, v35, v224\n"
        "v_exp_f32 v35, v35\n"
        "v_cndmask_b32 v32, v224, v242, s[30:31]\n"
        "v_cvt_pk_bf16_f32 v17, v34, v35\n"
        "v_cndmask_b32 v33, v224, v242, s[56:57]\n"
        "v_cndmask_b32 v34, v224, v242, s[58:59]\n"
        "v_cndmask_b32 v35, v224, v242, s[60:61]\n"
        "s_waitcnt lgkmcnt(0)\n"
        "s_nop 0\n"
        "v_mfma_f32_16x16x32_bf16 v[32:35], a[96:99], v[0:3], v[32:35]\n"
        "ds_read_b128 a[96:99], v240 offset:16384\n"
        "v_add_f32 v48, v48, v224\n"
        "v_mfma_f32_16x16x32_bf16 v[32:35], a[100:103], v[4:7], v[32:35]\n"
        "v_exp_f32 v48, v48\n"
        "v_mfma_f32_16x16x32_bf16 v[32:35], a[104:107], v[8:11], v[32:35]\n"
        "ds_read_b128 a[100:103], v240 offset:17408\n"
        "v_add_f32 v49, v49, v224\n"
        "v_mfma_f32_16x16x32_bf16 v[32:35], a[108:111], v[12:15], v[32:35]\n"
        "v_exp_f32 v49, v49\n"
        "global_store_dwordx4 v254, a[24:27], s[70:71] offset:2048 sc1\n"
        "v_cvt_pk_bf16_f32 v18, v48, v49\n"
        "v_add_f32 v50, v50, v224\n"
        "v_exp_f32 v50, v50\n"
        "v_add_f32 v51, v51, v224\n"
        "v_exp_f32 v51, v51\n"
        "ds_read_b128 a[104:107], v240 offset:18432\n"
        "v_cvt_pk_bf16_f32 v19, v50, v51\n"
        "s_nop 1\n"
        "v_mfma_f32_16x16x32_bf16 a[64:67], a[80:83], v[16:19], 0\n"
        "ds_read_b128 a[108:111], v240 offset:19456\n"
        "v_cndmask_b32 v48, v224, v242, s[62:63]\n"
        "v_mfma_f32_16x16x32_bf16 v[160:163], a[144:147], v[16:19], 0\n"
        "global_store_dwordx4 v254, a[28:31], s[70:71] offset:3072 sc1\n"
        "ds_read_b128 a[144:147], v241 offset:8192\n"
        "s_add_u32 s26, s18, s52\n"
        "s_addc_u32 s27, s19, 0\n"
        "v_cndmask_b32 v49, v224, v242, s[84:85]\n"
        "v_cndmask_b32 v50, v224, v242, s[86:87]\n"
        "v_mfma_f32_16x16x32_bf16 v[164:167], a[148:151], v[16:19], 0\n"
        "ds_read_b128 a[148:151], v241 offset:9216\n"
        "v_cndmask_b32 v51, v224, v242, s[88:89]\n"
        "s_nop 1\n"
        "v_mfma_f32_16x16x32_bf16 v[48:51], a[112:115], v[0:3], v[48:51]\n"
        "v_exp_f32 v32, v32\n"
        "v_mfma_f32_16x16x32_bf16 v[48:51], a[116:119], v[4:7], v[48:51]\n"
        "v_exp_f32 v33, v33\n"
        "v_mfma_f32_16x16x32_bf16 v[48:51], a[120:123], v[8:11], v[48:51]\n"
        "s_waitcnt lgkmcnt(4)\n"
        "v_cvt_pk_bf16_f32 v80, v32, v33\n"
        "v_cndmask_b32 v52, 0, v242, s[20:21]\n"
        "v_mfma_f32_16x16x32_bf16 v[48:51], a[124:127], v[12:15], v[48:51]\n"
        "ds_read_b128 a[112:115], v240 offset:20480\n"
        "v_cndmask_b32 v53, 0, v242, s[22:23]\n"
        "v_mfma_f32_16x16x32_bf16 v[168:171], a[152:155], v[16:19], 0\n"
        "s_waitcnt lgkmcnt(3)\n"
        "v_exp_f32 v34, v34\n"
        "v_mfma_f32_16x16x32_bf16 v[172:175], a[156:159], v[16:19], 0\n"
        "v_exp_f32 v35, v35\n"
        "v_mfma_f32_16x16x32_bf16 v[36:39], a[108:111], v[76:79], v[36:39]\n"
        "ds_read_b128 a[116:119], v240 offset:21504\n"
        "v_cvt_pk_bf16_f32 v81, v34, v35\n"
        "v_mfma_f32_16x16x32_bf16 v[32:35], a[96:99], v[0:3], v[224:227]\n"
        "ds_read_b128 a[152:155], v241 offset:10240\n"
        "v_cndmask_b32 v54, 0, v242, s[24:25]\n"
        "v_mfma_f32_16x16x32_bf16 v[32:35], a[100:103], v[4:7], v[32:35]\n"
        "ds_read_b128 a[120:123], v240 offset:22528\n"
        "v_cndmask_b32 v55, 0, v242, s[28:29]\n"
        "v_mfma_f32_16x16x32_bf16 v[32:35], a[104:107], v[8:11], v[32:35]\n"
        "ds_read_b128 a[156:159], v241 offset:11264\n"
        "v_cvt_pk_bf16_f32 v104, v112, v113\n"
        "v_mfma_f32_16x16x32_bf16 v[32:35], a[108:111], v[12:15], v[32:35]\n"
        "v_exp_f32 v48, v48\n"
        "v_mfma_f32_16x16x32_bf16 v[36:39], a[104:107], v[72:75], v[36:39]\n"
        "v_exp_f32 v49, v49\n"
        "v_mfma_f32_16x16x32_bf16 v[36:39], a[100:103], v[68:71], v[36:39]\n"
        "ds_read_b128 a[124:127], v240 offset:23552\n"
        "v_cvt_pk_bf16_f32 v82, v48, v49\n"
        "v_mfma_f32_16x16x32_bf16 v[36:39], a[96:99], v[64:67], v[36:39]\n"
        "v_exp_f32 v50, v50\n"
        "v_mfma_f32_16x16x32_bf16 v[176:179], a[128:131], v[16:19], 0\n"
        "v_exp_f32 v51, v51\n"
        "v_mfma_f32_16x16x32_bf16 v[180:183], a[132:135], v[16:19], 0\n"
        "ds_read_b128 a[96:99], v240 offset:24576\n"
        "v_cvt_pk_bf16_f32 v83, v50, v51\n"
        "v_mfma_f32_16x16x32_bf16 v[184:187], a[136:139], v[16:19], 0\n"
        "ds_read_b128 a[100:103], v240 offset:25600\n"
        "v_max_f32 v248, v36, v37\n"
        "v_mfma_f32_16x16x32_bf16 v[188:191], a[140:143], v[16:19], 0\n"
        "ds_read_b128 a[128:131], v241 offset:12288\n"
        "s_waitcnt lgkmcnt(4)\n"
        "v_mfma_f32_16x16x32_bf16 v[48:51], a[112:115], v[0:3], v[224:227]\n"
        "ds_read_b128 a[132:135], v241 offset:13312\n"
        "v_cvt_pk_bf16_f32 v105, v114, v115\n"
        "v_mfma_f32_16x16x32_bf16 v[48:51], a[116:119], v[4:7], v[48:51]\n"
        "ds_read_b128 a[136:139], v241 offset:14336\n"
        "v_max3_f32 v248, v38, v39, v248\n"
        "v_mfma_f32_16x16x32_bf16 v[48:51], a[120:123], v[8:11], v[48:51]\n"
        "ds_read_b128 a[104:107], v240 offset:26624\n"
        "s_waitcnt lgkmcnt(6)\n"
        "v_mfma_f32_16x16x32_bf16 v[48:51], a[124:127], v[12:15], v[48:51]\n"
        "ds_read_b128 a[140:143], v241 offset:15360\n"
        "v_cvt_pk_bf16_f32 v106, v116, v117\n"
        "v_mfma_f32_16x16x32_bf16 v[52:55], a[124:127], v[76:79], v[52:55]\n"
        "v_exp_f32 v32, v32\n"
        "v_mfma_f32_16x16x32_bf16 v[52:55], a[120:123], v[72:75], v[52:55]\n"
        "v_exp_f32 v33, v33\n"
        "v_mfma_f32_16x16x32_bf16 v[52:55], a[116:119], v[68:71], v[52:55]\n"
        "ds_read_b128 a[108:111], v240 offset:27648\n"
        "v_cvt_pk_bf16_f32 v16, v32, v33\n"
        "v_mfma_f32_16x16x32_bf16 v[52:55], a[112:115], v[64:67], v[52:55]\n"
        "s_waitcnt lgkmcnt(5)\n"
        "v_exp_f32 v34, v34\n"
        "v_mfma_f32_16x16x32_bf16 a[64:67], a[80:83], v[80:83], a[64:67]\n"
        "s_waitcnt lgkmcnt(4)\n"
        "v_exp_f32 v35, v35\n"
        "v_mfma_f32_16x16x32_bf16 v[160:163], a[144:147], v[80:83], v[160:163]\n"
        "s_waitcnt lgkmcnt(3)\n"
        "v_cvt_pk_bf16_f32 v17, v34, v35\n"
        "v_mov_b32 v249, v248\n"
        "v_mfma_f32_16x16x32_bf16 v[164:167], a[148:151], v[80:83], v[164:167]\n"
        "ds_read_b128 a[112:115], v240 offset:28672\n"
        "s_add_u32 s82, s82, 1\n"
        "v_cvt_pk_bf16_f32 v107, v118, v119\n"
        "v_mfma_f32_16x16x32_bf16 v[168:171], a[152:155], v[80:83], v[168:171]\n"
        "s_waitcnt lgkmcnt(2)\n"
        "v_permlane16_swap_b32 v248, v249\n"
        "v_mfma_f32_16x16x32_bf16 v[172:175], a[156:159], v[80:83], v[172:175]\n"
        "ds_read_b128 a[116:119], v240 offset:29696\n"
        "v_max_f32 v248, v248, v249\n"
        "v_mfma_f32_16x16x32_bf16 v[32:35], a[96:99], v[0:3], v[224:227]\n"
        "ds_read_b128 a[144:147], v241 offset:16384\n"
        "v_mov_b32 v249, v248\n"
        "v_mfma_f32_16x16x32_bf16 v[32:35], a[100:103], v[4:7], v[32:35]\n"
        "ds_read_b128 a[148:151], v241 offset:17408\n"
        "v_cvt_pk_bf16_f32 v108, v120, v121\n"
        "v_mfma_f32_16x16x32_bf16 v[32:35], a[104:107], v[8:11], v[32:35]\n"
        "s_waitcnt lgkmcnt(4)\n"
        "v_permlane32_swap_b32 v248, v249\n"
        "v_mfma_f32_16x16x32_bf16 v[32:35], a[108:111], v[12:15], v[32:35]\n"
        "ds_read_b128 a[152:155], v241 offset:18432\n"
        "v_max_f32 v248, v248, v249\n"
        "v_mfma_f32_16x16x32_bf16 v[176:179], a[128:131], v[80:83], v[176:179]\n"
        "ds_read_b128 a[124:127], v240 offset:31744\n"
        "v_sub_f32 v228, 0, v248\n"
        "v_mfma_f32_16x16x32_bf16 v[180:183], a[132:135], v[80:83], v[180:183]\n"
        "ds_read_b128 a[120:123], v240 offset:30720\n"
        "v_sub_f32 v229, 0, v248\n"
        "v_mfma_f32_16x16x32_bf16 v[184:187], a[136:139], v[80:83], v[184:187]\n"
        "ds_read_b128 a[156:159], v241 offset:19456\n"
        "v_sub_f32 v230, 0, v248\n"
        "v_mfma_f32_16x16x32_bf16 v[188:191], a[140:143], v[80:83], v[188:191]\n"
        "v_sub_f32 v231, 0, v248\n"
        "v_add_f32 v36, v36, v228\n"
        "v_exp_f32 v36, v36\n"
        "v_add_f32 v37, v37, v228\n"
        "v_exp_f32 v37, v37\n"
        "v_add_f32 v38, v38, v228\n"
        "v_cvt_pk_bf16_f32 v20, v36, v37\n"
        "v_exp_f32 v38, v38\n"
        "v_add_f32 v39, v39, v228\n"
        "v_exp_f32 v39, v39\n"
        "v_exp_f32 v48, v48\n"
        "v_cvt_pk_bf16_f32 v21, v38, v39\n"
        "v_exp_f32 v49, v49\n"
        "v_exp_f32 v50, v50\n"
        "v_cvt_pk_bf16_f32 v18, v48, v49\n"
        "v_exp_f32 v51, v51\n"
        "v_cndmask_b32 v36, v228, v242, s[30:31]\n"
        "v_cvt_pk_bf16_f32 v19, v50, v51\n"
        "s_waitcnt lgkmcnt(1)\n"
        "v_mfma_f32_16x16x32_bf16 v[48:51], a[112:115], v[0:3], v[224:227]\n"
        "s_waitcnt lgkmcnt(0)\n"
        "v_cndmask_b32 v37, v228, v242, s[56:57]\n"
        "v_cndmask_b32 v38, v228, v242, s[58:59]\n"
        "v_mfma_f32_16x16x32_bf16 v[48:51], a[116:119], v[4:7], v[48:51]\n"
        "ds_read_b128 a[128:131], v241 offset:20480\n"
        "v_cndmask_b32 v39, v228, v242, s[60:61]\n"
        "s_nop 1\n"
        "v_mfma_f32_16x16x32_bf16 v[36:39], a[108:111], v[76:79], v[36:39]\n"
        "ds_read_b128 a[132:135], v241 offset:21504\n"
        "v_add_f32 v52, v52, v228\n"
        "v_mfma_f32_16x16x32_bf16 v[36:39], a[104:107], v[72:75], v[36:39]\n"
        "v_exp_f32 v52, v52\n"
        "v_mfma_f32_16x16x32_bf16 v[36:39], a[100:103], v[68:71], v[36:39]\n"
        "ds_read_b128 a[136:139], v241 offset:22528\n"
        "s_barrier\n"
        "v_add_f32 v53, v53, v228\n"
        "v_mfma_f32_16x16x32_bf16 v[36:39], a[96:99], v[64:67], v[36:39]\n"
        "v_exp_f32 v53, v53\n"
        "v_mfma_f32_16x16x32_bf16 v[48:51], a[120:123], v[8:11], v[48:51]\n"
        "ds_read_b128 a[140:143], v241 offset:23552\n"
        "v_cvt_pk_bf16_f32 v22, v52, v53\n"
        "v_mfma_f32_16x16x32_bf16 v[48:51], a[124:127], v[12:15], v[48:51]\n"
        "global_load_lds_dwordx4 v254, s[68:69] offset:1024\n"
        "v_xor_b32 v240, 0x8000, v240\n"
        "v_mfma_f32_16x16x32_bf16 a[64:67], a[80:83], v[16:19], a[64:67]\n"
        "ds_read_b128 a[96:99], v240 offset:0\n"
        "v_add_f32 v54, v54, v228\n"
        "v_mfma_f32_16x16x32_bf16 v[160:163], a[144:147], v[16:19], v[160:163]\n"
        "ds_read_b128 a[100:103], v240 offset:1024\n"
        "v_cvt_pk_bf16_f32 v109, v122, v123\n"
        "v_mfma_f32_16x16x32_bf16 v[164:167], a[148:151], v[16:19], v[164:167]\n"
        "s_waitcnt lgkmcnt(4)\n"
        "v_exp_f32 v54, v54\n"
        "v_mfma_f32_16x16x32_bf16 v[168:171], a[152:155], v[16:19], v[168:171]\n"
        "ds_read_b128 a[104:107], v240 offset:2048\n"
        "v_cvt_pk_bf16_f32 v110, v124, v125\n"
        "v_mfma_f32_16x16x32_bf16 v[172:175], a[156:159], v[16:19], v[172:175]\n"
        "s_waitcnt lgkmcnt(4)\n"
        "v_add_f32 v55, v55, v228\n"
        "v_cvt_pk_bf16_f32 v111, v126, v127\n"
        "v_mfma_f32_16x16x32_bf16 v[176:179], a[128:131], v[16:19], v[176:179]\n"
        "v_exp_f32 v55, v55\n"
        "v_mfma_f32_16x16x32_bf16 v[180:183], a[132:135], v[16:19], v[180:183]\n"
        "s_waitcnt lgkmcnt(3)\n"
        "v_cvt_pk_bf16_f32 v23, v54, v55\n"
        "s_nop 1\n"
        "v_mfma_f32_16x16x32_bf16 a[68:71], a[80:83], v[20:23], 0\n"
        "ds_read_b128 a[108:111], v240 offset:3072\n"
        "v_cvt_pk_bf16_f32 v132, v136, v137\n"
        "v_mfma_f32_16x16x32_bf16 v[192:195], a[144:147], v[20:23], 0\n"
        "ds_read_b128 a[144:147], v241 offset:24576\n"
        "v_cvt_pk_bf16_f32 v133, v138, v139\n"
        "v_mfma_f32_16x16x32_bf16 v[196:199], a[148:151], v[20:23], 0\n"
        "ds_read_b128 a[148:151], v241 offset:25600\n"
        "v_cvt_pk_bf16_f32 v134, v140, v141\n"
        "v_mfma_f32_16x16x32_bf16 v[200:203], a[152:155], v[20:23], 0\n"
        "ds_read_b128 a[152:155], v241 offset:26624\n"
        "v_cvt_pk_bf16_f32 v135, v142, v143\n"
        "v_mfma_f32_16x16x32_bf16 v[204:207], a[156:159], v[20:23], 0\n"
        "v_exp_f32 v32, v32\n"
        "v_mfma_f32_16x16x32_bf16 v[208:211], a[128:131], v[20:23], 0\n"
        "v_exp_f32 v33, v33\n"
        "v_mfma_f32_16x16x32_bf16 v[212:215], a[132:135], v[20:23], 0\n"
        "ds_read_b128 a[156:159], v241 offset:27648\n"
        "v_cvt_pk_bf16_f32 v80, v32, v33\n"
        "v_mfma_f32_16x16x32_bf16 v[184:187], a[136:139], v[16:19], v[184:187]\n"
        "global_load_lds_dwordx4 v254, s[68:69] offset:2048\n"
        "v_cndmask_b32 v52, v228, v242, s[62:63]\n"
        "v_mfma_f32_16x16x32_bf16 v[216:219], a[136:139], v[20:23], 0\n"
        "ds_read_b128 a[128:131], v241 offset:28672\n"
        "v_cndmask_b32 v53, v228, v242, s[84:85]\n"
        "v_mfma_f32_16x16x32_bf16 v[220:223], a[140:143], v[20:23], 0\n"
        "ds_read_b128 a[132:135], v241 offset:29696\n"
        "v_cndmask_b32 v54, v228, v242, s[86:87]\n"
        "v_mfma_f32_16x16x32_bf16 v[188:191], a[140:143], v[16:19], v[188:191]\n"
        "v_cndmask_b32 v55, v228, v242, s[88:89]\n"
        "s_nop 1\n"
        "v_mfma_f32_16x16x32_bf16 v[52:55], a[124:127], v[76:79], v[52:55]\n"
        "v_exp_f32 v34, v34\n"
        "v_mfma_f32_16x16x32_bf16 v[52:55], a[120:123], v[72:75], v[52:55]\n"
        "v_exp_f32 v35, v35\n"
        "v_mfma_f32_16x16x32_bf16 v[52:55], a[116:119], v[68:71], v[52:55]\n"
        "ds_read_b128 a[116:119], v240 offset:5120\n"
        "v_cvt_pk_bf16_f32 v81, v34, v35\n"
        "v_mfma_f32_16x16x32_bf16 v[52:55], a[112:115], v[64:67], v[52:55]\n"
        "ds_read_b128 a[112:115], v240 offset:4096\n"
        "s_waitcnt lgkmcnt(4)\n"
        "v_mfma_f32_16x16x32_bf16 v[32:35], a[96:99], v[0:3], v[224:227]\n"
        "v_exp_f32 v36, v36\n"
        "v_mfma_f32_16x16x32_bf16 v[32:35], a[100:103], v[4:7], v[32:35]\n"
        "v_exp_f32 v37, v37\n"
        "v_mfma_f32_16x16x32_bf16 v[32:35], a[104:107], v[8:11], v[32:35]\n"
        "ds_read_b128 a[120:123], v240 offset:6144\n"
        "v_cvt_pk_bf16_f32 v84, v36, v37\n"
        "v_mfma_f32_16x16x32_bf16 v[32:35], a[108:111], v[12:15], v[32:35]\n"
        "v_exp_f32 v38, v38\n"
        "v_exp_f32 v39, v39\n"
        "v_cvt_pk_bf16_f32 v136, v144, v145\n"
        "v_cvt_pk_bf16_f32 v85, v38, v39\n"
        "v_mfma_f32_16x16x32_bf16 v[36:39], a[108:111], v[76:79], v[228:231]\n"
        "ds_read_b128 a[124:127], v240 offset:7168\n"
        "v_cvt_pk_bf16_f32 v137, v146, v147\n"
        "v_mfma_f32_16x16x32_bf16 v[36:39], a[104:107], v[72:75], v[36:39]\n"
        "ds_read_b128 a[136:139], v241 offset:30720\n"
        "v_cvt_pk_bf16_f32 v138, v148, v149\n"
        "v_mfma_f32_16x16x32_bf16 v[36:39], a[100:103], v[68:71], v[36:39]\n"
        "ds_read_b128 a[140:143], v241 offset:31744\n"
        "v_cvt_pk_bf16_f32 v139, v150, v151\n"
        "v_mfma_f32_16x16x32_bf16 v[36:39], a[96:99], v[64:67], v[36:39]\n"
        "v_exp_f32 v48, v48\n"
        "v_exp_f32 v49, v49\n"
        "v_exp_f32 v50, v50\n"
        "v_cvt_pk_bf16_f32 v82, v48, v49\n"
        "v_exp_f32 v51, v51\n"
        "v_cndmask_b32 v40, 0, v242, s[10:11]\n"
        "v_cvt_pk_bf16_f32 v83, v50, v51\n"
        "s_waitcnt lgkmcnt(3)\n"
        "v_mfma_f32_16x16x32_bf16 v[48:51], a[112:115], v[0:3], v[224:227]\n"
        "v_cndmask_b32 v41, 0, v242, s[12:13]\n"
        "v_cndmask_b32 v42, 0, v242, s[14:15]\n"
        "v_mfma_f32_16x16x32_bf16 v[48:51], a[116:119], v[4:7], v[48:51]\n"
        "v_cndmask_b32 v43, 0, v242, s[16:17]\n"
        "s_nop 1\n"
        "v_mfma_f32_16x16x32_bf16 v[40:43], a[96:99], v[96:99], v[40:43]\n"
        "ds_read_b128 a[96:99], v240 offset:8192\n"
        "v_cvt_pk_bf16_f32 v140, v152, v153\n"
        "v_mfma_f32_16x16x32_bf16 v[40:43], a[100:103], v[100:103], v[40:43]\n"
        "v_exp_f32 v52, v52\n"
        "v_mfma_f32_16x16x32_bf16 v[40:43], a[104:107], v[104:107], v[40:43]\n"
        "v_exp_f32 v53, v53\n"
        "v_mfma_f32_16x16x32_bf16 v[40:43], a[108:111], v[108:111], v[40:43]\n"
        "ds_read_b128 a[100:103], v240 offset:9216\n"
        "v_cvt_pk_bf16_f32 v86, v52, v53\n"
        "v_mfma_f32_16x16x32_bf16 v[48:51], a[120:123], v[8:11], v[48:51]\n"
        "s_waitcnt lgkmcnt(2)\n"
        "v_cvt_pk_bf16_f32 v141, v154, v155\n"
        "v_cvt_pk_bf16_f32 v142, v156, v157\n"
        "v_mfma_f32_16x16x32_bf16 v[48:51], a[124:127], v[12:15], v[48:51]\n"
        "ds_read_b128 a[104:107], v240 offset:10240\n"
        "s_mov_b32 s91, s44\n"
        "v_cvt_pk_bf16_f32 v143, v158, v159\n"
        "v_mfma_f32_16x16x32_bf16 a[64:67], a[80:83], v[80:83], a[64:67]\n"
        "v_exp_f32 v54, v54\n"
        "v_mfma_f32_16x16x32_bf16 v[160:163], a[144:147], v[80:83], v[160:163]\n"
        "v_exp_f32 v55, v55\n"
        "v_mfma_f32_16x16x32_bf16 v[164:167], a[148:151], v[80:83], v[164:167]\n"
        "ds_read_b128 a[108:111], v240 offset:11264\n"
        "v_cvt_pk_bf16_f32 v87, v54, v55\n"
        "v_mfma_f32_16x16x32_bf16 v[52:55], a[124:127], v[76:79], v[228:231]\n"
        "v_exp_f32 v32, v32\n"
        "v_mfma_f32_16x16x32_bf16 v[52:55], a[120:123], v[72:75], v[52:55]\n"
        "v_exp_f32 v33, v33\n"
        "v_mfma_f32_16x16x32_bf16 v[52:55], a[116:119], v[68:71], v[52:55]\n"
        "v_cvt_pk_bf16_f32 v16, v32, v33\n"
        "v_cndmask_b32 v56, 0, v242, s[20:21]\n"
        "v_mfma_f32_16x16x32_bf16 v[52:55], a[112:115], v[64:67], v[52:55]\n"
        "v_exp_f32 v34, v34\n"
        "v_mfma_f32_16x16x32_bf16 a[68:71], a[80:83], v[84:87], a[68:71]\n"
        "v_exp_f32 v35, v35\n"
        "v_mfma_f32_16x16x32_bf16 v[192:195], a[144:147], v[84:87], v[192:195]\n"
        "v_cvt_pk_bf16_f32 v17, v34, v35\n"
        "v_cndmask_b32 v57, 0, v242, s[22:23]\n"
        "v_mfma_f32_16x16x32_bf16 v[196:199], a[148:151], v[84:87], v[196:199]\n"
        "s_waitcnt lgkmcnt(1)\n"
        "v_exp_f32 v36, v36\n"
        "v_mfma_f32_16x16x32_bf16 v[168:171], a[152:155], v[80:83], v[168:171]\n"
        "v_exp_f32 v37, v37\n"
        "v_mfma_f32_16x16x32_bf16 v[200:203], a[152:155], v[84:87], v[200:203]\n"
        "v_cvt_pk_bf16_f32 v20, v36, v37\n"
        "v_cndmask_b32 v58, 0, v242, s[24:25]\n"
        "v_mfma_f32_16x16x32_bf16 v[204:207], a[156:159], v[84:87], v[204:207]\n"
        "v_cndmask_b32 v59, 0, v242, s[28:29]\n"
        "s_nop 1\n"
        "v_mfma_f32_16x16x32_bf16 v[56:59], a[112:115], v[96:99], v[56:59]\n"
        "ds_read_b128 a[112:115], v240 offset:12288\n"
        "v_lshrrev_b32 v244, 1, v254\n"
        "v_mfma_f32_16x16x32_bf16 v[56:59], a[116:119], v[100:103], v[56:59]\n"
        "s_waitcnt vmcnt(2)\n"
        "s_barrier\n"
        "v_add_u32 v244, s81, v244\n"
        "v_mfma_f32_16x16x32_bf16 v[56:59], a[120:123], v[104:107], v[56:59]\n"
        "ds_read_b128 a[116:119], v240 offset:13312\n"
        "s_add_u32 s81, s78, 4096\n"
        "v_bfe_u32 v245, v254, 4, 3\n"
        "v_mfma_f32_16x16x32_bf16 v[56:59], a[124:127], v[108:111], v[56:59]\n"
        "ds_read_b128 a[120:123], v240 offset:14336\n"
        "v_lshl_add_u32 v245, v245, 7, s80\n"
        "v_mfma_f32_16x16x32_bf16 v[172:175], a[156:159], v[80:83], v[172:175]\n"
        "v_exp_f32 v38, v38\n"
        "v_mfma_f32_16x16x32_bf16 v[32:35], a[96:99], v[0:3], v[224:227]\n"
        "s_barrier\n"
        "v_exp_f32 v39, v39\n"
        "v_mfma_f32_16x16x32_bf16 v[32:35], a[100:103], v[4:7], v[32:35]\n"
        "ds_read_b128 a[124:127], v240 offset:15360\n"
        "s_mov_b32 s80, s78\n"
        "v_cvt_pk_bf16_f32 v21, v38, v39\n"
        "v_mfma_f32_16x16x32_bf16 v[32:35], a[104:107], v[8:11], v[32:35]\n"
        "s_waitcnt lgkmcnt(4)\n"
        "v_xor_b32 v241, 0x8000, v241\n"
        "v_max_f32 v246, v40, v41\n"
        "v_mfma_f32_16x16x32_bf16 v[32:35], a[108:111], v[12:15], v[32:35]\n"
        "ds_read_b128 a[144:147], v241 offset:0\n"
        "s_xor_b32 s78, s78, 0x8000\n"
        "v_max3_f32 v246, v42, v43, v246\n"
        "v_mfma_f32_16x16x32_bf16 v[36:39], a[108:111], v[76:79], v[228:231]\n"
        "ds_read_b128 a[148:151], v241 offset:1024\n"
        "v_mov_b32 v247, v246\n"
        "v_mfma_f32_16x16x32_bf16 v[36:39], a[104:107], v[72:75], v[36:39]\n"
        "ds_read_b128 a[152:155], v241 offset:2048\n"
        "v_cndmask_b32 v44, 0, v242, s[10:11]\n"
        "v_mfma_f32_16x16x32_bf16 v[36:39], a[100:103], v[68:71], v[36:39]\n"
        "s_sub_u32 s10, s83, 1\n"
        "v_permlane16_swap_b32 v246, v247\n"
        "v_mfma_f32_16x16x32_bf16 v[36:39], a[96:99], v[64:67], v[36:39]\n"
        "ds_read_b128 a[156:159], v241 offset:3072\n"
        "v_max_f32 v246, v246, v247\n"
        "v_mfma_f32_16x16x32_bf16 v[176:179], a[128:131], v[80:83], v[176:179]\n"
        "v_mov_b32 v247, v246\n"
        "v_cndmask_b32 v45, 0, v242, s[12:13]\n"
        "v_mfma_f32_16x16x32_bf16 v[208:211], a[128:131], v[84:87], v[208:211]\n"
        "v_permlane32_swap_b32 v246, v247\n"
        "v_mfma_f32_16x16x32_bf16 v[212:215], a[132:135], v[84:87], v[212:215]\n"
        "ds_read_b128 a[128:131], v241 offset:4096\n"
        "v_max_f32 v246, v246, v247\n"
        "v_mfma_f32_16x16x32_bf16 v[180:183], a[132:135], v[80:83], v[180:183]\n"
        "ds_read_b128 a[132:135], v241 offset:5120\n"
        "v_sub_f32 v232, 0, v246\n"
        "v_mfma_f32_16x16x32_bf16 v[184:187], a[136:139], v[80:83], v[184:187]\n"
        "v_sub_f32 v233, 0, v246\n"
        "v_sub_f32 v234, 0, v246\n"
        "v_mfma_f32_16x16x32_bf16 v[216:219], a[136:139], v[84:87], v[216:219]\n"
        "ds_read_b128 a[136:139], v241 offset:6144\n"
        "v_sub_f32 v235, 0, v246\n"
        "v_mfma_f32_16x16x32_bf16 v[220:223], a[140:143], v[84:87], v[220:223]\n"
        "v_add_f32 v40, v40, v232\n"
        "v_add_f32 v41, v41, v232\n"
        "v_mfma_f32_16x16x32_bf16 v[188:191], a[140:143], v[80:83], v[188:191]\n"
        "s_mov_b32 exec_hi, 0x7fffffff\n"
        "global_load_lds_dwordx4 v254, s[68:69] offset:3072\n"
        "s_mov_b64 exec, -1\n"
        "v_exp_f32 v40, v40\n"
        "v_exp_f32 v41, v41\n"
        "v_add_f32 v42, v42, v232\n"
        "v_cvt_pk_bf16_f32 v24, v40, v41\n"
        "v_exp_f32 v42, v42\n"
        "v_add_f32 v43, v43, v232\n"
        "v_exp_f32 v43, v43\n"
        "v_exp_f32 v48, v48\n"
        "s_mov_b64 exec, 7\n"
        "global_load_lds_dword v252, s[68:69] offset:4080\n"
        "s_mov_b64 exec, -1\n"
        "v_cvt_pk_bf16_f32 v25, v42, v43\n"
        "v_exp_f32 v49, v49\n"
        "v_exp_f32 v50, v50\n"
        "v_cvt_pk_bf16_f32 v18, v48, v49\n"
        "v_exp_f32 v51, v51\n"
        "v_exp_f32 v52, v52\n"
        "v_cvt_pk_bf16_f32 v19, v50, v51\n"
        "s_waitcnt lgkmcnt(0)\n"
        "v_mfma_f32_16x16x32_bf16 v[48:51], a[112:115], v[0:3], v[224:227]\n"
        "s_cmp_lg_u32 s40, 0\n"
        "s_cbranch_scc1 LW1_%=\n"
        "s_mov_b64 exec, 0xffff\n"
        "global_store_dwordx2 v244, v[250:251], s[48:49] sc1\n"
        "s_mov_b64 exec, -1\n"
        "LW1_%=:\n"
        "s_add_u32 m0, s80, 0x0\n"
        "v_mfma_f32_16x16x32_bf16 v[48:51], a[116:119], v[4:7], v[48:51]\n"
        "s_mov_b32 s80, s78\n"
        "v_exp_f32 v53, v53\n"
        "v_mfma_f32_16x16x32_bf16 v[48:51], a[120:123], v[8:11], v[48:51]\n"
        "ds_read_b128 a[140:143], v241 offset:7168\n"
        "v_cvt_pk_bf16_f32 v22, v52, v53\n"
        "v_mfma_f32_16x16x32_bf16 v[48:51], a[124:127], v[12:15], v[48:51]\n"
        "s_add_u32 s68, s64, s90\n"
        "s_addc_u32 s69, s65, 0\n"
        "v_cndmask_b32 v40, v232, v242, s[30:31]\n"
        "v_mfma_f32_16x16x32_bf16 a[64:67], a[80:83], v[16:19], a[64:67]\n"
        "s_add_u32 s74, s68, 0x1000\n"
        "s_addc_u32 s75, s69, 0\n"
        "v_cndmask_b32 v41, v232, v242, s[56:57]\n"
        "v_mfma_f32_16x16x32_bf16 v[160:163], a[144:147], v[16:19], v[160:163]\n"
        "s_cmp_ge_u32 s82, s44\n"
        "s_addc_u32 s90, s82, 0\n"
        "v_cndmask_b32 v42, v232, v242, s[58:59]\n"
        "v_mfma_f32_16x16x32_bf16 v[164:167], a[148:151], v[16:19], v[164:167]\n"
        "s_lshl_b32 s90, s90, 15\n"
        "v_cndmask_b32 v43, v232, v242, s[60:61]\n"
        "s_nop 1\n"
        "v_mfma_f32_16x16x32_bf16 v[40:43], a[96:99], v[96:99], v[40:43]\n"
        "ds_read_b128 a[96:99], v240 offset:16384\n"
        "v_add_f32 v56, v56, v232\n"
        "v_mfma_f32_16x16x32_bf16 v[40:43], a[100:103], v[100:103], v[40:43]\n"
        "v_exp_f32 v54, v54\n"
        "v_mfma_f32_16x16x32_bf16 v[40:43], a[104:107], v[104:107], v[40:43]\n"
        "ds_read_b128 a[104:107], v240 offset:18432\n"
        "v_add_f32 v57, v57, v232\n"
        "v_mfma_f32_16x16x32_bf16 v[40:43], a[108:111], v[108:111], v[40:43]\n"
        "ds_read_b128 a[108:111], v240 offset:19456\n"
        "v_cndmask_b32 v46, 0, v242, s[14:15]\n"
        "v_mfma_f32_16x16x32_bf16 v[168:171], a[152:155], v[16:19], v[168:171]\n"
        "s_waitcnt lgkmcnt(3)\n"
        "v_exp_f32 v55, v55\n"
        "v_mfma_f32_16x16x32_bf16 v[172:175], a[156:159], v[16:19], v[172:175]\n"
        "ds_read_b128 a[100:103], v240 offset:17408\n"
        "v_cvt_pk_bf16_f32 v23, v54, v55\n"
        "v_mfma_f32_16x16x32_bf16 v[52:55], a[124:127], v[76:79], v[228:231]\n"
        "v_exp_f32 v56, v56\n"
        "v_mfma_f32_16x16x32_bf16 v[52:55], a[120:123], v[72:75], v[52:55]\n"
        "v_exp_f32 v57, v57\n"
        "v_mfma_f32_16x16x32_bf16 v[52:55], a[116:119], v[68:71], v[52:55]\n"
        "v_cvt_pk_bf16_f32 v26, v56, v57\n"
        "v_cndmask_b32 v47, 0, v242, s[16:17]\n"
        "v_mfma_f32_16x16x32_bf16 v[52:55], a[112:115], v[64:67], v[52:55]\n"
        "v_add_f32 v58, v58, v232\n"
        "v_add_f32 v59, v59, v232\n"
        "v_mfma_f32_16x16x32_bf16 a[68:71], a[80:83], v[20:23], a[68:71]\n"
        "s_waitcnt lgkmcnt(2)\n"
        "v_exp_f32 v58, v58\n"
        "v_mfma_f32_16x16x32_bf16 v[192:195], a[144:147], v[20:23], v[192:195]\n"
        "s_waitcnt lgkmcnt(1)\n"
        "v_exp_f32 v59, v59\n"
        "v_mfma_f32_16x16x32_bf16 v[44:47], a[108:111], v[140:143], v[44:47]\n"
        "v_cvt_pk_bf16_f32 v27, v58, v59\n"
        "v_cndmask_b32 v56, v232, v242, s[62:63]\n"
        "v_mfma_f32_16x16x32_bf16 v[44:47], a[104:107], v[136:139], v[44:47]\n"
        "global_load_dwordx2 v[252:253], v245, s[48:49] sc1\n"
        "v_cndmask_b32 v57, v232, v242, s[84:85]\n"
        "v_mfma_f32_16x16x32_bf16 a[72:75], a[80:83], v[24:27], 0\n"
        "v_exp_f32 v32, v32\n"
        "v_mfma_f32_16x16x32_bf16 a[0:3], a[144:147], v[24:27], 0\n"
        "v_exp_f32 v33, v33\n"
        "v_mfma_f32_16x16x32_bf16 a[4:7], a[148:151], v[24:27], 0\n"
        "s_waitcnt lgkmcnt(0)\n"
        "v_cvt_pk_bf16_f32 v80, v32, v33\n"
        "v_cndmask_b32 v58, v232, v242, s[86:87]\n"
        "v_mfma_f32_16x16x32_bf16 v[196:199], a[148:151], v[20:23], v[196:199]\n"
        "v_exp_f32 v34, v34\n"
        "v_mfma_f32_16x16x32_bf16 v[200:203], a[152:155], v[20:23], v[200:203]\n"
        "v_exp_f32 v35, v35\n"
        "v_mfma_f32_16x16x32_bf16 a[8:11], a[152:155], v[24:27], 0\n"
        "ds_read_b128 a[144:147], v241 offset:8192\n"
        "v_cvt_pk_bf16_f32 v81, v34, v35\n"
        "v_mfma_f32_16x16x32_bf16 a[12:15], a[156:159], v[24:27], 0\n"
        "ds_read_b128 a[148:151], v241 offset:9216\n"
        "v_cndmask_b32 v59, v232, v242, s[88:89]\n"
        "s_nop 1\n"
        "v_mfma_f32_16x16x32_bf16 v[56:59], a[112:115], v[96:99], v[56:59]\n"
        "v_exp_f32 v36, v36\n"
        "v_mfma_f32_16x16x32_bf16 v[56:59], a[116:119], v[100:103], v[56:59]\n"
        "v_exp_f32 v37, v37\n"
        "v_mfma_f32_16x16x32_bf16 v[56:59], a[120:123], v[104:107], v[56:59]\n"
        "ds_read_b128 a[112:115], v240 offset:20480\n"
        "v_cvt_pk_bf16_f32 v84, v36, v37\n"
        "v_mfma_f32_16x16x32_bf16 v[56:59], a[124:127], v[108:111], v[56:59]\n"
        "v_exp_f32 v38, v38\n"
        "v_mfma_f32_16x16x32_bf16 v[204:207], a[156:159], v[20:23], v[204:207]\n"
        "v_exp_f32 v39, v39\n"
        "v_mfma_f32_16x16x32_bf16 v[32:35], a[96:99], v[0:3], v[224:227]\n"
        "ds_read_b128 a[116:119], v240 offset:21504\n"
        "v_cvt_pk_bf16_f32 v85, v38, v39\n"
        "v_mfma_f32_16x16x32_bf16 v[32:35], a[100:103], v[4:7], v[32:35]\n"
        "v_exp_f32 v40, v40\n"
        "v_mfma_f32_16x16x32_bf16 v[32:35], a[104:107], v[8:11], v[32:35]\n"
        "v_exp_f32 v41, v41\n"
        "v_mfma_f32_16x16x32_bf16 v[32:35], a[108:111], v[12:15], v[32:35]\n"
        "ds_read_b128 a[152:155], v241 offset:10240\n"
        "v_cvt_pk_bf16_f32 v88, v40, v41\n"
        "v_mfma_f32_16x16x32_bf16 v[36:39], a[108:111], v[76:79], v[228:231]\n"
        "v_exp_f32 v42, v42\n"
        "v_mfma_f32_16x16x32_bf16 v[36:39], a[104:107], v[72:75], v[36:39]\n"
        "v_exp_f32 v43, v43\n"
        "v_mfma_f32_16x16x32_bf16 v[36:39], a[100:103], v[68:71], v[36:39]\n"
        "ds_read_b128 a[120:123], v240 offset:22528\n"
        "v_cvt_pk_bf16_f32 v89, v42, v43\n"
        "v_mfma_f32_16x16x32_bf16 v[36:39], a[96:99], v[64:67], v[36:39]\n"
        "ds_read_b128 a[156:159], v241 offset:11264\n"
        "v_cndmask_b32 v60, 0, v242, s[20:21]\n"
        "v_mfma_f32_16x16x32_bf16 v[40:43], a[96:99], v[96:99], v[232:235]\n"
        "v_exp_f32 v48, v48\n"
        "v_mfma_f32_16x16x32_bf16 v[40:43], a[100:103], v[100:103], v[40:43]\n"
        "v_exp_f32 v49, v49\n"
        "v_mfma_f32_16x16x32_bf16 v[40:43], a[104:107], v[104:107], v[40:43]\n"
        "ds_read_b128 a[124:127], v240 offset:23552\n"
        "v_cvt_pk_bf16_f32 v82, v48, v49\n"
        "v_mfma_f32_16x16x32_bf16 v[40:43], a[108:111], v[108:111], v[40:43]\n"
        "v_exp_f32 v50, v50\n"
        "v_mfma_f32_16x16x32_bf16 v[44:47], a[100:103], v[132:135], v[44:47]\n"
        "v_exp_f32 v51, v51\n"
        "v_mfma_f32_16x16x32_bf16 v[44:47], a[96:99], v[128:131], v[44:47]\n"
        "ds_read_b128 a[96:99], v240 offset:24576\n"
        "v_cvt_pk_bf16_f32 v83, v50, v51\n"
        "v_mfma_f32_16x16x32_bf16 v[176:179], a[128:131], v[16:19], v[176:179]\n"
        "v_exp_f32 v52, v52\n"
        "v_mfma_f32_16x16x32_bf16 v[208:211], a[128:131], v[20:23], v[208:211]\n"
        "v_exp_f32 v53, v53\n"
        "v_mfma_f32_16x16x32_bf16 a[16:19], a[128:131], v[24:27], 0\n"
        "ds_read_b128 a[128:131], v241 offset:12288\n"
        "v_cvt_pk_bf16_f32 v86, v52, v53\n"
        "v_mfma_f32_16x16x32_bf16 a[20:23], a[132:135], v[24:27], 0\n"
        "LHA11_%=:\n"
        "s_cbranch_execz LHRA_%=\n"
        "v_exp_f32 v54, v54\n"
        "v_mfma_f32_16x16x32_bf16 v[212:215], a[132:135], v[20:23], v[212:215]\n"
        "v_exp_f32 v55, v55\n"
        "v_mfma_f32_16x16x32_bf16 v[180:183], a[132:135], v[16:19], v[180:183]\n"
        "ds_read_b128 a[100:103], v240 offset:25600\n"
        "v_cvt_pk_bf16_f32 v87, v54, v55\n"
        "v_mfma_f32_16x16x32_bf16 v[184:187], a[136:139], v[16:19], v[184:187]\n"
        "s_waitcnt lgkmcnt(3)\n"
        "v_exp_f32 v56, v56\n"
        "v_mfma_f32_16x16x32_bf16 v[216:219], a[136:139], v[20:23], v[216:219]\n"
        "v_exp_f32 v57, v57\n"
        "v_mfma_f32_16x16x32_bf16 a[24:27], a[136:139], v[24:27], 0\n"
        "ds_read_b128 a[132:135], v241 offset:13312\n"
        "v_cvt_pk_bf16_f32 v90, v56, v57\n"
        "v_mfma_f32_16x16x32_bf16 a[28:31], a[140:143], v[24:27], 0\n"
        "ds_read_b128 a[136:139], v241 offset:14336\n"
        "v_cndmask_b32 v61, 0, v242, s[22:23]\n"
        "v_mfma_f32_16x16x32_bf16 v[220:223], a[140:143], v[20:23], v[220:223]\n"
        "v_exp_f32 v58, v58\n"
        "v_mfma_f32_16x16x32_bf16 v[188:191], a[140:143], v[16:19], v[188:191]\n"
        "v_exp_f32 v59, v59\n"
        "v_mfma_f32_16x16x32_bf16 v[48:51], a[112:115], v[0:3], v[224:227]\n"
        "ds_read_b128 a[104:107], v240 offset:26624\n"
        "v_cvt_pk_bf16_f32 v91, v58, v59\n"
        "v_mfma_f32_16x16x32_bf16 v[48:51], a[116:119], v[4:7], v[48:51]\n"
        "ds_read_b128 a[140:143], v241 offset:15360\n"
        "v_cndmask_b32 v62, 0, v242, s[24:25]\n"
        "v_mfma_f32_16x16x32_bf16 v[48:51], a[120:123], v[8:11], v[48:51]\n"
        "v_exp_f32 v32, v32\n"
        "v_mfma_f32_16x16x32_bf16 v[48:51], a[124:127], v[12:15], v[48:51]\n"
        "v_exp_f32 v33, v33\n"
        "v_mfma_f32_16x16x32_bf16 v[52:55], a[124:127], v[76:79], v[228:231]\n"
        "s_waitcnt vmcnt(0) lgkmcnt(3)\n"
        "v_cmp_ne_u64 vcc, v[252:253], v[250:251]\n"
        "v_cvt_pk_bf16_f32 v16, v32, v33\n"
        "v_mfma_f32_16x16x32_bf16 v[52:55], a[120:123], v[72:75], v[52:55]\n"
        "s_cbranch_vccnz LPKV_%=\n"
        "LBKV_%=:\n"
        "v_and_b32 v245, 0x70, v254\n"
        "v_lshlrev_b32 v245, 3, v245\n"
        "v_mfma_f32_16x16x32_bf16 v[52:55], a[116:119], v[68:71], v[52:55]\n"
        "global_load_lds_dwordx4 v254, s[68:69] offset:0 sc1\n"
        "ds_read_b128 a[108:111], v240 offset:27648\n"
        "v_mfma_f32_16x16x32_bf16 v[52:55], a[112:115], v[64:67], v[52:55]\n"
        "v_exp_f32 v34, v34\n"
        "v_mfma_f32_16x16x32_bf16 v[56:59], a[112:115], v[96:99], v[232:235]\n"
        "v_exp_f32 v35, v35\n"
        "v_mfma_f32_16x16x32_bf16 v[56:59], a[116:119], v[100:103], v[56:59]\n"
        "v_cvt_pk_bf16_f32 v17, v34, v35\n"
        "v_cndmask_b32 v63, 0, v242, s[28:29]\n"
        "v_mfma_f32_16x16x32_bf16 v[56:59], a[120:123], v[104:107], v[56:59]\n"
        "global_load_lds_dwordx4 v254, s[68:69] offset:1024 sc1\n"
        "v_max_f32 v248, v44, v45\n"
        "v_mfma_f32_16x16x32_bf16 v[56:59], a[124:127], v[108:111], v[56:59]\n"
        "v_exp_f32 v36, v36\n"
        "v_mfma_f32_16x16x32_bf16 v[60:63], a[124:127], v[140:143], v[60:63]\n"
        "v_exp_f32 v37, v37\n"
        "v_mfma_f32_16x16x32_bf16 v[60:63], a[120:123], v[136:139], v[60:63]\n"
        "ds_read_b128 a[120:123], v240 offset:30720\n"
        "v_cvt_pk_bf16_f32 v20, v36, v37\n"
        "v_mfma_f32_16x16x32_bf16 v[60:63], a[116:119], v[132:135], v[60:63]\n"
        "global_load_lds_dwordx4 v254, s[68:69] offset:2048 sc1\n"
        "ds_read_b128 a[116:119], v240 offset:29696\n"
        "v_mfma_f32_16x16x32_bf16 v[60:63], a[112:115], v[128:131], v[60:63]\n"
        "v_exp_f32 v38, v38\n"
        "v_mfma_f32_16x16x32_bf16 a[64:67], a[80:83], v[80:83], a[64:67]\n"
        "v_exp_f32 v39, v39\n"
        "v_mfma_f32_16x16x32_bf16 a[68:71], a[80:83], v[84:87], a[68:71]\n"
        "ds_read_b128 a[112:115], v240 offset:28672\n"
        "v_cvt_pk_bf16_f32 v21, v38, v39\n"
        "v_mfma_f32_16x16x32_bf16 a[72:75], a[80:83], v[88:91], a[72:75]\n"
        "global_load_lds_dwordx4 v254, s[68:69] offset:3072 sc1\n"
        "s_add_u32 m0, s81, 0x0\n"
        "v_max3_f32 v248, v46, v47, v248\n"
        "v_mfma_f32_16x16x32_bf16 v[160:163], a[144:147], v[80:83], v[160:163]\n"
        "s_add_u32 s81, s78, 4096\n"
        "v_exp_f32 v40, v40\n"
        "v_mfma_f32_16x16x32_bf16 v[192:195], a[144:147], v[84:87], v[192:195]\n"
        "v_exp_f32 v41, v41\n"
        "v_mfma_f32_16x16x32_bf16 a[0:3], a[144:147], v[88:91], a[0:3]\n"
        "ds_read_b128 a[144:147], v241 offset:16384\n"
        "v_cvt_pk_bf16_f32 v24, v40, v41\n"
        "v_mfma_f32_16x16x32_bf16 a[4:7], a[148:151], v[88:91], a[4:7]\n"
        "global_load_lds_dwordx4 v254, s[74:75] offset:0 sc1\n"
        "v_mov_b32 v249, v248\n"
        "v_mfma_f32_16x16x32_bf16 v[196:199], a[148:151], v[84:87], v[196:199]\n"
        "v_exp_f32 v42, v42\n"
        "v_mfma_f32_16x16x32_bf16 v[164:167], a[148:151], v[80:83], v[164:167]\n"
        "v_exp_f32 v43, v43\n"
        "v_mfma_f32_16x16x32_bf16 v[168:171], a[152:155], v[80:83], v[168:171]\n"
        "ds_read_b128 a[148:151], v241 offset:17408\n"
        "v_cvt_pk_bf16_f32 v25, v42, v43\n"
        "v_mfma_f32_16x16x32_bf16 v[200:203], a[152:155], v[84:87], v[200:203]\n"
        "global_load_lds_dwordx4 v254, s[74:75] offset:1024 sc1\n"
        "v_accvgpr_write_b32 a48, 0\n"
        "v_mfma_f32_16x16x32_bf16 a[8:11], a[152:155], v[88:91], a[8:11]\n"
        "s_waitcnt lgkmcnt(2)\n"
        "v_permlane16_swap_b32 v248, v249\n"
        "v_mfma_f32_16x16x32_bf16 a[12:15], a[156:159], v[88:91], a[12:15]\n"
        "ds_read_b128 a[152:155], v241 offset:18432\n"
        "v_max_f32 v248, v248, v249\n"
        "v_mfma_f32_16x16x32_bf16 v[204:207], a[156:159], v[84:87], v[204:207]\n"
        "ds_read_b128 a[124:127], v240 offset:31744\n"
        "v_mov_b32 v249, v248\n"
        "v_mfma_f32_16x16x32_bf16 v[172:175], a[156:159], v[80:83], v[172:175]\n"
        "global_load_lds_dwordx4 v254, s[74:75] offset:2048 sc1\n"
        "v_accvgpr_write_b32 a49, 0\n"
        "v_mfma_f32_16x16x32_bf16 v[32:35], a[96:99], v[0:3], v[224:227]\n"
        "v_permlane32_swap_b32 v248, v249\n"
        "v_mfma_f32_16x16x32_bf16 v[32:35], a[100:103], v[4:7], v[32:35]\n"
        "ds_read_b128 a[156:159], v241 offset:19456\n"
        "v_max_f32 v248, v248, v249\n"
        "v_mfma_f32_16x16x32_bf16 v[32:35], a[104:107], v[8:11], v[32:35]\n"
        "s_add_u32 s68, s64, s90\n"
        "s_addc_u32 s69, s65, 0\n"
        "v_sub_f32 v236, 0, v248\n"
        "v_mfma_f32_16x16x32_bf16 v[32:35], a[108:111], v[12:15], v[32:35]\n"
        "global_load_lds_dwordx4 v254, s[74:75] offset:3072 sc1\n"
        "s_add_u32 m0, s79, 0x0\n"
        "v_sub_f32 v237, 0, v248\n"
        "v_mfma_f32_16x16x32_bf16 v[36:39], a[108:111], v[76:79], v[228:231]\n"
        "s_add_u32 s74, s68, 0x1000\n"
        "s_addc_u32 s75, s69, 0\n"
        "v_sub_f32 v238, 0, v248\n"
        "v_mfma_f32_16x16x32_bf16 v[36:39], a[104:107], v[72:75], v[36:39]\n"
        "v_sub_f32 v239, 0, v248\n"
        "v_add_f32 v44, v44, v236\n"
        "v_mfma_f32_16x16x32_bf16 v[36:39], a[100:103], v[68:71], v[36:39]\n"
        "v_exp_f32 v44, v44\n"
        "v_mfma_f32_16x16x32_bf16 v[36:39], a[96:99], v[64:67], v[36:39]\n"
        "global_load_lds_dwordx4 v254, s[76:77] offset:0 sc1\n"
        "v_add_f32 v45, v45, v236\n"
        "v_mfma_f32_16x16x32_bf16 v[40:43], a[96:99], v[96:99], v[232:235]\n"
        "v_exp_f32 v45, v45\n"
        "v_mfma_f32_16x16x32_bf16 v[40:43], a[100:103], v[100:103], v[40:43]\n"
        "v_cvt_pk_bf16_f32 v28, v44, v45\n"
        "v_add_f32 v46, v46, v236\n"
        "v_mfma_f32_16x16x32_bf16 v[40:43], a[104:107], v[104:107], v[40:43]\n"
        "v_exp_f32 v46, v46\n"
        "v_mfma_f32_16x16x32_bf16 v[40:43], a[108:111], v[108:111], v[40:43]\n"
        "global_load_lds_dwordx4 v254, s[76:77] offset:1024 sc1\n"
        "v_add_f32 v47, v47, v236\n"
        "v_mfma_f32_16x16x32_bf16 v[176:179], a[128:131], v[80:83], v[176:179]\n"
        "v_exp_f32 v47, v47\n"
        "v_mfma_f32_16x16x32_bf16 v[208:211], a[128:131], v[84:87], v[208:211]\n"
        "v_cvt_pk_bf16_f32 v29, v46, v47\n"
        "v_accvgpr_write_b32 a50, 0\n"
        "v_mfma_f32_16x16x32_bf16 a[16:19], a[128:131], v[88:91], a[16:19]\n"
        "ds_read_b128 a[128:131], v241 offset:20480\n"
        "v_accvgpr_write_b32 a51, 0\n"
        "v_mfma_f32_16x16x32_bf16 a[20:23], a[132:135], v[88:91], a[20:23]\n"
        "global_load_lds_dwordx4 v254, s[76:77] offset:2048 sc1\n"
        "v_accvgpr_write_b32 a52, 0\n"
        "v_mfma_f32_16x16x32_bf16 v[212:215], a[132:135], v[84:87], v[212:215]\n"
        "v_accvgpr_write_b32 a53, 0\n"
        "v_accvgpr_write_b32 a54, 0\n"
        "v_mfma_f32_16x16x32_bf16 v[180:183], a[132:135], v[80:83], v[180:183]\n"
        "ds_read_b128 a[132:135], v241 offset:21504\n"
        "v_accvgpr_write_b32 a55, 0\n"
        "v_mfma_f32_16x16x32_bf16 v[184:187], a[136:139], v[80:83], v[184:187]\n"
        "v_accvgpr_write_b32 a56, 0\n"
        "v_accvgpr_write_b32 a57, 0\n"
        "v_mfma_f32_16x16x32_bf16 v[216:219], a[136:139], v[84:87], v[216:219]\n"
        "global_load_lds_dwordx4 v254, s[76:77] offset:3072 sc1\n"
        "s_add_u32 m0, s79, 0x1000\n"
        "v_accvgpr_write_b32 a58, 0\n"
        "v_mfma_f32_16x16x32_bf16 a[24:27], a[136:139], v[88:91], a[24:27]\n"
        "ds_read_b128 a[136:139], v241 offset:22528\n"
        "v_accvgpr_write_b32 a59, 0\n"
        "v_mfma_f32_16x16x32_bf16 a[28:31], a[140:143], v[88:91], a[28:31]\n"
        "s_add_u32 s76, s76, 0x1000\n"
        "s_addc_u32 s77, s77, 0\n"
        "v_accvgpr_write_b32 a60, 0\n"
        "v_mfma_f32_16x16x32_bf16 v[220:223], a[140:143], v[84:87], v[220:223]\n"
        "v_accvgpr_write_b32 a61, 0\n"
        "v_accvgpr_write_b32 a62, 0\n"
        "v_mfma_f32_16x16x32_bf16 v[188:191], a[140:143], v[80:83], v[188:191]\n"
        "v_accvgpr_write_b32 a63, 0\n"
        "v_exp_f32 v48, v48\n"
        "v_exp_f32 v49, v49\n"
        "v_exp_f32 v50, v50\n"
        "v_cvt_pk_bf16_f32 v18, v48, v49\n"
        "v_exp_f32 v51, v51\n"
        "v_exp_f32 v52, v52\n"
        "v_cvt_pk_bf16_f32 v19, v50, v51\n"
        "v_mfma_f32_16x16x32_bf16 v[48:51], a[112:115], v[0:3], v[224:227]\n"
        "s_waitcnt lgkmcnt(1)\n"
        "v_exp_f32 v53, v53\n"
        "v_mfma_f32_16x16x32_bf16 v[48:51], a[116:119], v[4:7], v[48:51]\n"
        "global_load_lds_dwordx4 v254, s[76:77] offset:0 sc1\n"
        "v_cvt_pk_bf16_f32 v22, v52, v53\n"
        "v_mfma_f32_16x16x32_bf16 v[48:51], a[120:123], v[8:11], v[48:51]\n"
        "v_exp_f32 v54, v54\n"
        "v_mfma_f32_16x16x32_bf16 v[48:51], a[124:127], v[12:15], v[48:51]\n"
        "v_exp_f32 v55, v55\n"
        "v_mfma_f32_16x16x32_bf16 a[64:67], a[80:83], v[16:19], a[64:67]\n"
        "ds_read_b128 a[140:143], v241 offset:23552\n"
        "v_cvt_pk_bf16_f32 v23, v54, v55\n"
        "v_mfma_f32_16x16x32_bf16 v[52:55], a[124:127], v[76:79], v[228:231]\n"
        "global_load_lds_dwordx4 v254, s[76:77] offset:1024 sc1\n"
        "v_cndmask_b32 v44, v236, v242, s[30:31]\n"
        "v_mfma_f32_16x16x32_bf16 v[52:55], a[120:123], v[72:75], v[52:55]\n"
        "v_cndmask_b32 v45, v236, v242, s[56:57]\n"
        "v_cndmask_b32 v46, v236, v242, s[58:59]\n"
        "v_mfma_f32_16x16x32_bf16 v[52:55], a[116:119], v[68:71], v[52:55]\n"
        "v_cndmask_b32 v47, v236, v242, s[60:61]\n"
        "s_nop 1\n"
        "v_mfma_f32_16x16x32_bf16 v[44:47], a[108:111], v[140:143], v[44:47]\n"
        "v_exp_f32 v56, v56\n"
        "v_mfma_f32_16x16x32_bf16 v[44:47], a[104:107], v[136:139], v[44:47]\n"
        "v_exp_f32 v57, v57\n"
        "v_mfma_f32_16x16x32_bf16 v[44:47], a[100:103], v[132:135], v[44:47]\n"
        "global_load_lds_dwordx4 v254, s[76:77] offset:2048 sc1\n"
        "v_cvt_pk_bf16_f32 v26, v56, v57\n"
        "v_mfma_f32_16x16x32_bf16 v[44:47], a[96:99], v[128:131], v[44:47]\n"
        "v_xor_b32 v240, 0x8000, v240\n"
        "v_add_f32 v60, v60, v236\n"
        "v_mfma_f32_16x16x32_bf16 v[52:55], a[112:115], v[64:67], v[52:55]\n"
        "v_exp_f32 v58, v58\n"
        "v_mfma_f32_16x16x32_bf16 a[68:71], a[80:83], v[20:23], a[68:71]\n"
        "v_exp_f32 v59, v59\n"
        "v_mfma_f32_16x16x32_bf16 v[160:163], a[144:147], v[16:19], v[160:163]\n"
        "global_load_lds_dwordx4 v254, s[76:77] offset:3072 sc1\n"
        "s_add_u32 m0, s80, 0x0\n"
        "v_cvt_pk_bf16_f32 v27, v58, v59\n"
        "v_mfma_f32_16x16x32_bf16 v[56:59], a[112:115], v[96:99], v[232:235]\n"
        "v_exp_f32 v60, v60\n"
        "v_mfma_f32_16x16x32_bf16 v[56:59], a[116:119], v[100:103], v[56:59]\n"
        "v_add_f32 v61, v61, v236\n"
        "v_add_f32 v62, v62, v236\n"
        "v_mfma_f32_16x16x32_bf16 v[56:59], a[120:123], v[104:107], v[56:59]\n"
        "v_exp_f32 v61, v61\n"
        "v_mfma_f32_16x16x32_bf16 v[56:59], a[124:127], v[108:111], v[56:59]\n"
        "global_load_dword v244, v245, s[26:27] offset:0\n"
        "v_cvt_pk_bf16_f32 v30, v60, v61\n"
        "v_mfma_f32_16x16x32_bf16 a[72:75], a[80:83], v[24:27], a[72:75]\n"
        "v_exp_f32 v62, v62\n"
        "v_mfma_f32_16x16x32_bf16 v[192:195], a[144:147], v[20:23], v[192:195]\n"
        "s_sub_u32 s26, s26, 262144\n"
        "s_subb_u32 s27, s27, 0\n"
        "v_add_f32 v63, v63, v236\n"
        "v_mfma_f32_16x16x32_bf16 a[0:3], a[144:147], v[24:27], a[0:3]\n"
        "v_exp_f32 v63, v63\n"
        "v_mfma_f32_16x16x32_bf16 a[4:7], a[148:151], v[24:27], a[4:7]\n"
        "global_load_dword v244, v245, s[26:27] offset:0\n"
        "v_cvt_pk_bf16_f32 v31, v62, v63\n"
        "s_nop 1\n"
        "v_mfma_f32_16x16x32_bf16 a[76:79], a[80:83], v[28:31], 0\n"
        "s_sub_u32 s26, s26, 262144\n"
        "s_subb_u32 s27, s27, 0\n"
        "v_cndmask_b32 v60, v236, v242, s[62:63]\n"
        "v_mfma_f32_16x16x32_bf16 a[32:35], a[144:147], v[28:31], 0\n"
        "v_exp_f32 v32, v32\n"
        "v_mfma_f32_16x16x32_bf16 a[36:39], a[148:151], v[28:31], 0\n"
        "v_exp_f32 v33, v33\n"
        "v_mfma_f32_16x16x32_bf16 v[196:199], a[148:151], v[20:23], v[196:199]\n"
        "ds_read_b128 a[144:147], v241 offset:24576\n"
        "v_cvt_pk_bf16_f32 v80, v32, v33\n"
        "v_mfma_f32_16x16x32_bf16 v[164:167], a[148:151], v[16:19], v[164:167]\n"
        "v_exp_f32 v34, v34\n"
        "v_mfma_f32_16x16x32_bf16 v[168:171], a[152:155], v[16:19], v[168:171]\n"
        "v_exp_f32 v35, v35\n"
        "v_mfma_f32_16x16x32_bf16 v[200:203], a[152:155], v[20:23], v[200:203]\n"
        "ds_read_b128 a[148:151], v241 offset:25600\n"
        "v_cvt_pk_bf16_f32 v81, v34, v35\n"
        "v_mfma_f32_16x16x32_bf16 a[8:11], a[152:155], v[24:27], a[8:11]\n"
        "v_exp_f32 v36, v36\n"
        "v_mfma_f32_16x16x32_bf16 a[40:43], a[152:155], v[28:31], 0\n"
        "v_exp_f32 v37, v37\n"
        "v_mfma_f32_16x16x32_bf16 a[44:47], a[156:159], v[28:31], 0\n"
        "s_waitcnt vmcnt(10)\n"
        "s_barrier\n"
        "v_cvt_pk_bf16_f32 v84, v36, v37\n"
        "v_mfma_f32_16x16x32_bf16 a[12:15], a[156:159], v[24:27], a[12:15]\n"
        "global_load_lds_dwordx4 v254, s[68:69] offset:0 sc1\n"
        "ds_read_b128 a[96:99], v240 offset:0\n"
        "v_mfma_f32_16x16x32_bf16 v[204:207], a[156:159], v[20:23], v[204:207]\n"
        "ds_read_b128 a[100:103], v240 offset:1024\n"
        "v_cndmask_b32 v61, v236, v242, s[84:85]\n"
        "v_mfma_f32_16x16x32_bf16 v[172:175], a[156:159], v[16:19], v[172:175]\n"
        "ds_read_b128 a[104:107], v240 offset:2048\n"
        "ds_read_b128 a[108:111], v240 offset:3072\n"
        "v_cndmask_b32 v62, v236, v242, s[86:87]\n"
        "v_cndmask_b32 v63, v236, v242, s[88:89]\n"
        "s_nop 1\n"
        "v_mfma_f32_16x16x32_bf16 v[60:63], a[124:127], v[140:143], v[60:63]\n"
        "global_load_lds_dwordx4 v254, s[68:69] offset:1024 sc1\n"
        "ds_read_b128 a[152:155], v241 offset:26624\n"
        "v_mfma_f32_16x16x32_bf16 v[60:63], a[120:123], v[136:139], v[60:63]\n"
        "v_exp_f32 v38, v38\n"
        "v_mfma_f32_16x16x32_bf16 v[60:63], a[116:119], v[132:135], v[60:63]\n"
        "v_exp_f32 v39, v39\n"
        "v_mfma_f32_16x16x32_bf16 v[60:63], a[112:115], v[128:131], v[60:63]\n"
        "v_cvt_pk_bf16_f32 v85, v38, v39\n"
        "ds_read_b128 a[112:115], v240 offset:4096\n"
        "global_load_lds_dwordx4 v254, s[68:69] offset:2048 sc1\n"
        "v_exp_f32 v40, v40\n"
        "v_exp_f32 v41, v41\n"
        "v_exp_f32 v42, v42\n"
        "v_cvt_pk_bf16_f32 v88, v40, v41\n"
        "v_exp_f32 v43, v43\n"
        "ds_read_b128 a[116:119], v240 offset:5120\n"
        "v_cvt_pk_bf16_f32 v89, v42, v43\n"
        "v_exp_f32 v44, v44\n"
        "global_load_lds_dwordx4 v254, s[68:69] offset:3072 sc1\n"
        "v_exp_f32 v45, v45\n"
        "v_exp_f32 v46, v46\n"
        "v_cvt_pk_bf16_f32 v92, v44, v45\n"
        "v_exp_f32 v47, v47\n"
        "ds_read_b128 a[120:123], v240 offset:6144\n"
        "v_cvt_pk_bf16_f32 v93, v46, v47\n"
        "s_waitcnt lgkmcnt(7)\n"
        "s_cmp_eq_u32 s91, 0\n"
        "s_cbranch_scc1 LLD_%=\n"
        "; ---- DUAL\n"
        "s_nop 0\n"
        "s_nop 0\n"
        "s_nop 0\n"
        "s_nop 0\n"
        "s_nop 0\n"
        "s_nop 0\n"
        "s_nop 0\n"
        "s_nop 0\n"
        "s_nop 0\n"
        "s_nop 0\n"
        "s_nop 0\n"
        "s_nop 0\n"
        "s_nop 0\n"
        "LDU_%=:\n"
        "v_mfma_f32_16x16x32_bf16 v[32:35], a[96:99], v[0:3], v[224:227]\n"
        "s_add_u32 m0, s81, 0x0\n"
        "s_xor_b32 s79, s79, 0x8000\n"
        "s_waitcnt lgkmcnt(1)\n"
        "v_mfma_f32_16x16x32_bf16 v[32:35], a[100:103], v[4:7], v[32:35]\n"
        "global_load_lds_dwordx4 v254, s[74:75] offset:0 sc1\n"
        "ds_read_b128 a[156:159], v241 offset:27648\n"
        "v_mfma_f32_16x16x32_bf16 v[32:35], a[104:107], v[8:11], v[32:35]\n"
        "v_exp_f32 v48, v48\n"
        "v_mfma_f32_16x16x32_bf16 v[32:35], a[108:111], v[12:15], v[32:35]\n"
        "v_exp_f32 v49, v49\n"
        "v_mfma_f32_16x16x32_bf16 v[36:39], a[108:111], v[76:79], v[228:231]\n"
        "ds_read_b128 a[124:127], v240 offset:7168\n"
        "v_cvt_pk_bf16_f32 v82, v48, v49\n"
        "v_mfma_f32_16x16x32_bf16 v[36:39], a[104:107], v[72:75], v[36:39]\n"
        "global_load_lds_dwordx4 v254, s[74:75] offset:1024 sc1\n"
        "s_add_u32 s76, s66, s90\n"
        "s_addc_u32 s77, s67, 0\n"
        "v_mfma_f32_16x16x32_bf16 v[36:39], a[100:103], v[68:71], v[36:39]\n"
        "v_exp_f32 v50, v50\n"
        "v_mfma_f32_16x16x32_bf16 v[36:39], a[96:99], v[64:67], v[36:39]\n"
        "v_exp_f32 v51, v51\n"
        "v_mfma_f32_16x16x32_bf16 v[40:43], a[96:99], v[96:99], v[232:235]\n"
        "v_cvt_pk_bf16_f32 v83, v50, v51\n"
        "v_mfma_f32_16x16x32_bf16 v[40:43], a[100:103], v[100:103], v[40:43]\n"
        "global_load_lds_dwordx4 v254, s[74:75] offset:2048 sc1\n"
        "v_mfma_f32_16x16x32_bf16 v[40:43], a[104:107], v[104:107], v[40:43]\n"
        "v_exp_f32 v52, v52\n"
        "v_mfma_f32_16x16x32_bf16 v[40:43], a[108:111], v[108:111], v[40:43]\n"
        "v_exp_f32 v53, v53\n"
        "v_mfma_f32_16x16x32_bf16 v[44:47], a[108:111], v[140:143], v[236:239]\n"
        "ds_read_b128 a[108:111], v240 offset:11264\n"
        "v_cvt_pk_bf16_f32 v86, v52, v53\n"
        "v_mfma_f32_16x16x32_bf16 v[44:47], a[104:107], v[136:139], v[44:47]\n"
        "global_load_lds_dwordx4 v254, s[74:75] offset:3072 sc1\n"
        "ds_read_b128 a[104:107], v240 offset:10240\n"
        "s_add_u32 m0, s79, 0x0\n"
        "v_mfma_f32_16x16x32_bf16 v[44:47], a[100:103], v[132:135], v[44:47]\n"
        "v_exp_f32 v54, v54\n"
        "v_mfma_f32_16x16x32_bf16 v[44:47], a[96:99], v[128:131], v[44:47]\n"
        "v_exp_f32 v55, v55\n"
        "v_mfma_f32_16x16x32_bf16 v[176:179], a[128:131], v[16:19], v[176:179]\n"
        "ds_read_b128 a[96:99], v240 offset:8192\n"
        "v_cvt_pk_bf16_f32 v87, v54, v55\n"
        "v_mfma_f32_16x16x32_bf16 v[208:211], a[128:131], v[20:23], v[208:211]\n"
        "v_exp_f32 v56, v56\n"
        "v_mfma_f32_16x16x32_bf16 a[16:19], a[128:131], v[24:27], a[16:19]\n"
        "v_exp_f32 v57, v57\n"
        "v_mfma_f32_16x16x32_bf16 a[48:51], a[128:131], v[28:31], a[48:51]\n"
        "ds_read_b128 a[128:131], v241 offset:28672\n"
        "v_cvt_pk_bf16_f32 v90, v56, v57\n"
        "v_mfma_f32_16x16x32_bf16 a[52:55], a[132:135], v[28:31], a[52:55]\n"
        "s_add_u32 s82, s82, 1\n"
        "v_exp_f32 v58, v58\n"
        "v_mfma_f32_16x16x32_bf16 a[20:23], a[132:135], v[24:27], a[20:23]\n"
        "s_xor_b32 s78, s78, 0x8000\n"
        "v_exp_f32 v59, v59\n"
        "v_mfma_f32_16x16x32_bf16 v[212:215], a[132:135], v[20:23], v[212:215]\n"
        "ds_read_b128 a[100:103], v240 offset:9216\n"
        "s_add_u32 s81, s78, 4096\n"
        "v_cvt_pk_bf16_f32 v91, v58, v59\n"
        "v_mfma_f32_16x16x32_bf16 v[180:183], a[132:135], v[16:19], v[180:183]\n"
        "ds_read_b128 a[132:135], v241 offset:29696\n"
        "s_cmp_ge_u32 s82, s44\n"
        "s_addc_u32 s90, s82, 0\n"
        "v_mfma_f32_16x16x32_bf16 v[184:187], a[136:139], v[16:19], v[184:187]\n"
        "s_lshl_b32 s90, s90, 15\n"
        "v_exp_f32 v60, v60\n"
        "v_mfma_f32_16x16x32_bf16 v[216:219], a[136:139], v[20:23], v[216:219]\n"
        "s_mov_b32 s80, s78\n"
        "v_exp_f32 v61, v61\n"
        "v_mfma_f32_16x16x32_bf16 a[24:27], a[136:139], v[24:27], a[24:27]\n"
        "s_add_u32 s68, s64, s90\n"
        "s_addc_u32 s69, s65, 0\n"
        "v_cvt_pk_bf16_f32 v94, v60, v61\n"
        "v_mfma_f32_16x16x32_bf16 a[56:59], a[136:139], v[28:31], a[56:59]\n"
        "ds_read_b128 a[136:139], v241 offset:30720\n"
        "s_add_u32 s74, s68, 0x1000\n"
        "s_addc_u32 s75, s69, 0\n"
        "v_mfma_f32_16x16x32_bf16 a[60:63], a[140:143], v[28:31], a[60:63]\n"
        "s_waitcnt lgkmcnt(3)\n"
        "v_exp_f32 v62, v62\n"
        "v_mfma_f32_16x16x32_bf16 a[28:31], a[140:143], v[24:27], a[28:31]\n"
        "v_exp_f32 v63, v63\n"
        "v_mfma_f32_16x16x32_bf16 v[220:223], a[140:143], v[20:23], v[220:223]\n"
        "v_cvt_pk_bf16_f32 v95, v62, v63\n"
        "v_mfma_f32_16x16x32_bf16 v[188:191], a[140:143], v[16:19], v[188:191]\n"
        "ds_read_b128 a[140:143], v241 offset:31744\n"
        "v_xor_b32 v241, 0x8000, v241\n"
        "v_mfma_f32_16x16x32_bf16 v[48:51], a[112:115], v[0:3], v[224:227]\n"
        "v_exp_f32 v32, v32\n"
        "v_mfma_f32_16x16x32_bf16 v[48:51], a[116:119], v[4:7], v[48:51]\n"
        "v_exp_f32 v33, v33\n"
        "v_mfma_f32_16x16x32_bf16 v[48:51], a[120:123], v[8:11], v[48:51]\n"
        "v_cvt_pk_bf16_f32 v16, v32, v33\n"
        "v_mfma_f32_16x16x32_bf16 v[48:51], a[124:127], v[12:15], v[48:51]\n"
        "v_exp_f32 v34, v34\n"
        "v_mfma_f32_16x16x32_bf16 v[52:55], a[124:127], v[76:79], v[228:231]\n"
        "v_exp_f32 v35, v35\n"
        "v_mfma_f32_16x16x32_bf16 v[52:55], a[120:123], v[72:75], v[52:55]\n"
        "v_cvt_pk_bf16_f32 v17, v34, v35\n"
        "v_mfma_f32_16x16x32_bf16 v[52:55], a[116:119], v[68:71], v[52:55]\n"
        "v_exp_f32 v36, v36\n"
        "v_mfma_f32_16x16x32_bf16 v[52:55], a[112:115], v[64:67], v[52:55]\n"
        "s_waitcnt vmcnt(10) lgkmcnt(0)\n"
        "v_exp_f32 v37, v37\n"
        "v_mfma_f32_16x16x32_bf16 v[56:59], a[112:115], v[96:99], v[232:235]\n"
        "s_barrier\n"
        "v_cvt_pk_bf16_f32 v20, v36, v37\n"
        "v_mfma_f32_16x16x32_bf16 v[56:59], a[116:119], v[100:103], v[56:59]\n"
        "v_exp_f32 v38, v38\n"
        "v_mfma_f32_16x16x32_bf16 v[56:59], a[120:123], v[104:107], v[56:59]\n"
        "v_exp_f32 v39, v39\n"
        "v_mfma_f32_16x16x32_bf16 v[56:59], a[124:127], v[108:111], v[56:59]\n"
        "global_load_lds_dwordx4 v254, s[76:77] offset:0 sc1\n"
        "v_cvt_pk_bf16_f32 v21, v38, v39\n"
        "v_mfma_f32_16x16x32_bf16 v[60:63], a[124:127], v[140:143], v[236:239]\n"
        "v_exp_f32 v40, v40\n"
        "v_mfma_f32_16x16x32_bf16 v[60:63], a[120:123], v[136:139], v[60:63]\n"
        "v_exp_f32 v41, v41\n"
        "v_mfma_f32_16x16x32_bf16 v[60:63], a[116:119], v[132:135], v[60:63]\n"
        "ds_read_b128 a[116:119], v240 offset:13312\n"
        "v_cvt_pk_bf16_f32 v24, v40, v41\n"
        "v_mfma_f32_16x16x32_bf16 v[60:63], a[112:115], v[128:131], v[60:63]\n"
        "ds_read_b128 a[112:115], v240 offset:12288\n"
        "global_load_lds_dwordx4 v254, s[76:77] offset:1024 sc1\n"
        "v_mfma_f32_16x16x32_bf16 a[64:67], a[80:83], v[80:83], a[64:67]\n"
        "v_exp_f32 v42, v42\n"
        "v_mfma_f32_16x16x32_bf16 a[68:71], a[80:83], v[84:87], a[68:71]\n"
        "v_exp_f32 v43, v43\n"
        "v_mfma_f32_16x16x32_bf16 a[72:75], a[80:83], v[88:91], a[72:75]\n"
        "ds_read_b128 a[120:123], v240 offset:14336\n"
        "v_cvt_pk_bf16_f32 v25, v42, v43\n"
        "v_mfma_f32_16x16x32_bf16 a[76:79], a[80:83], v[92:95], a[76:79]\n"
        "v_exp_f32 v44, v44\n"
        "v_mfma_f32_16x16x32_bf16 v[160:163], a[144:147], v[80:83], v[160:163]\n"
        "v_exp_f32 v45, v45\n"
        "v_mfma_f32_16x16x32_bf16 v[192:195], a[144:147], v[84:87], v[192:195]\n"
        "global_load_lds_dwordx4 v254, s[76:77] offset:2048 sc1\n"
        "v_cvt_pk_bf16_f32 v28, v44, v45\n"
        "v_mfma_f32_16x16x32_bf16 a[0:3], a[144:147], v[88:91], a[0:3]\n"
        "v_exp_f32 v46, v46\n"
        "v_mfma_f32_16x16x32_bf16 a[32:35], a[144:147], v[92:95], a[32:35]\n"
        "ds_read_b128 a[144:147], v241 offset:0\n"
        "v_mfma_f32_16x16x32_bf16 a[36:39], a[148:151], v[92:95], a[36:39]\n"
        "v_exp_f32 v47, v47\n"
        "v_mfma_f32_16x16x32_bf16 a[4:7], a[148:151], v[88:91], a[4:7]\n"
        "global_load_lds_dwordx4 v254, s[76:77] offset:3072 sc1\n"
        "s_add_u32 m0, s79, 0x1000\n"
        "v_cvt_pk_bf16_f32 v29, v46, v47\n"
        "v_mfma_f32_16x16x32_bf16 v[196:199], a[148:151], v[84:87], v[196:199]\n"
        "v_exp_f32 v48, v48\n"
        "v_mfma_f32_16x16x32_bf16 v[164:167], a[148:151], v[80:83], v[164:167]\n"
        "ds_read_b128 a[148:151], v241 offset:1024\n"
        "s_add_u32 s76, s76, 0x1000\n"
        "s_addc_u32 s77, s77, 0\n"
        "v_mfma_f32_16x16x32_bf16 v[168:171], a[152:155], v[80:83], v[168:171]\n"
        "v_exp_f32 v49, v49\n"
        "v_mfma_f32_16x16x32_bf16 v[200:203], a[152:155], v[84:87], v[200:203]\n"
        "global_load_lds_dwordx4 v254, s[76:77] offset:0 sc1\n"
        "v_cvt_pk_bf16_f32 v18, v48, v49\n"
        "v_mfma_f32_16x16x32_bf16 a[8:11], a[152:155], v[88:91], a[8:11]\n"
        "v_exp_f32 v50, v50\n"
        "v_mfma_f32_16x16x32_bf16 a[40:43], a[152:155], v[92:95], a[40:43]\n"
        "ds_read_b128 a[152:155], v241 offset:2048\n"
        "v_mfma_f32_16x16x32_bf16 a[44:47], a[156:159], v[92:95], a[44:47]\n"
        "v_exp_f32 v51, v51\n"
        "v_mfma_f32_16x16x32_bf16 a[12:15], a[156:159], v[88:91], a[12:15]\n"
        "ds_read_b128 a[124:127], v240 offset:15360\n"
        "v_cvt_pk_bf16_f32 v19, v50, v51\n"
        "v_mfma_f32_16x16x32_bf16 v[204:207], a[156:159], v[84:87], v[204:207]\n"
        "global_load_lds_dwordx4 v254, s[76:77] offset:1024 sc1\n"
        "v_mfma_f32_16x16x32_bf16 v[172:175], a[156:159], v[80:83], v[172:175]\n"
        "ds_read_b128 a[156:159], v241 offset:3072\n"
        "v_mfma_f32_16x16x32_bf16 v[32:35], a[96:99], v[0:3], v[224:227]\n"
        "v_exp_f32 v52, v52\n"
        "v_mfma_f32_16x16x32_bf16 v[32:35], a[100:103], v[4:7], v[32:35]\n"
        "v_exp_f32 v53, v53\n"
        "v_mfma_f32_16x16x32_bf16 v[32:35], a[104:107], v[8:11], v[32:35]\n"
        "global_load_lds_dwordx4 v254, s[76:77] offset:2048 sc1\n"
        "v_cvt_pk_bf16_f32 v22, v52, v53\n"
        "v_mfma_f32_16x16x32_bf16 v[32:35], a[108:111], v[12:15], v[32:35]\n"
        "s_sub_u32 s91, s91, 1\n"
        "v_exp_f32 v54, v54\n"
        "v_mfma_f32_16x16x32_bf16 v[36:39], a[108:111], v[76:79], v[228:231]\n"
        "v_exp_f32 v55, v55\n"
        "v_mfma_f32_16x16x32_bf16 v[36:39], a[104:107], v[72:75], v[36:39]\n"
        "v_cvt_pk_bf16_f32 v23, v54, v55\n"
        "v_mfma_f32_16x16x32_bf16 v[36:39], a[100:103], v[68:71], v[36:39]\n"
        "global_load_lds_dwordx4 v254, s[76:77] offset:3072 sc1\n"
        "v_mfma_f32_16x16x32_bf16 v[36:39], a[96:99], v[64:67], v[36:39]\n"
        "v_exp_f32 v56, v56\n"
        "v_mfma_f32_16x16x32_bf16 v[40:43], a[96:99], v[96:99], v[232:235]\n"
        "v_exp_f32 v57, v57\n"
        "v_mfma_f32_16x16x32_bf16 v[40:43], a[100:103], v[100:103], v[40:43]\n"
        "v_cvt_pk_bf16_f32 v26, v56, v57\n"
        "v_mfma_f32_16x16x32_bf16 v[40:43], a[104:107], v[104:107], v[40:43]\n"
        "v_exp_f32 v58, v58\n"
        "v_mfma_f32_16x16x32_bf16 v[40:43], a[108:111], v[108:111], v[40:43]\n"
        "v_exp_f32 v59, v59\n"
        "v_mfma_f32_16x16x32_bf16 v[44:47], a[108:111], v[140:143], v[236:239]\n"
        "ds_read_b128 a[108:111], v240 offset:19456\n"
        "v_cvt_pk_bf16_f32 v27, v58, v59\n"
        "v_mfma_f32_16x16x32_bf16 v[44:47], a[104:107], v[136:139], v[44:47]\n"
        "v_exp_f32 v60, v60\n"
        "v_mfma_f32_16x16x32_bf16 v[44:47], a[100:103], v[132:135], v[44:47]\n"
        "ds_read_b128 a[100:103], v240 offset:17408\n"
        "v_mfma_f32_16x16x32_bf16 v[44:47], a[96:99], v[128:131], v[44:47]\n"
        "ds_read_b128 a[96:99], v240 offset:16384\n"
        "v_mfma_f32_16x16x32_bf16 v[176:179], a[128:131], v[80:83], v[176:179]\n"
        "v_exp_f32 v61, v61\n"
        "v_mfma_f32_16x16x32_bf16 v[208:211], a[128:131], v[84:87], v[208:211]\n"
        "ds_read_b128 a[104:107], v240 offset:18432\n"
        "v_cvt_pk_bf16_f32 v30, v60, v61\n"
        "v_mfma_f32_16x16x32_bf16 a[16:19], a[128:131], v[88:91], a[16:19]\n"
        "v_exp_f32 v62, v62\n"
        "v_mfma_f32_16x16x32_bf16 a[48:51], a[128:131], v[92:95], a[48:51]\n"
        "ds_read_b128 a[128:131], v241 offset:4096\n"
        "v_mfma_f32_16x16x32_bf16 a[52:55], a[132:135], v[92:95], a[52:55]\n"
        "v_exp_f32 v63, v63\n"
        "v_mfma_f32_16x16x32_bf16 a[20:23], a[132:135], v[88:91], a[20:23]\n"
        "v_cvt_pk_bf16_f32 v31, v62, v63\n"
        "v_mfma_f32_16x16x32_bf16 v[212:215], a[132:135], v[84:87], v[212:215]\n"
        "v_exp_f32 v32, v32\n"
        "v_mfma_f32_16x16x32_bf16 v[180:183], a[132:135], v[80:83], v[180:183]\n"
        "ds_read_b128 a[132:135], v241 offset:5120\n"
        "v_mfma_f32_16x16x32_bf16 v[184:187], a[136:139], v[80:83], v[184:187]\n"
        "v_exp_f32 v33, v33\n"
        "v_mfma_f32_16x16x32_bf16 v[216:219], a[136:139], v[84:87], v[216:219]\n"
        "v_exp_f32 v34, v34\n"
        "v_mfma_f32_16x16x32_bf16 a[24:27], a[136:139], v[88:91], a[24:27]\n"
        "s_waitcnt lgkmcnt(2)\n"
        "v_exp_f32 v35, v35\n"
        "v_mfma_f32_16x16x32_bf16 a[56:59], a[136:139], v[92:95], a[56:59]\n"
        "ds_read_b128 a[136:139], v241 offset:6144\n"
        "v_mfma_f32_16x16x32_bf16 a[60:63], a[140:143], v[92:95], a[60:63]\n"
        "v_exp_f32 v36, v36\n"
        "v_mfma_f32_16x16x32_bf16 a[28:31], a[140:143], v[88:91], a[28:31]\n"
        "v_exp_f32 v37, v37\n"
        "v_mfma_f32_16x16x32_bf16 v[220:223], a[140:143], v[84:87], v[220:223]\n"
        "v_cvt_pk_bf16_f32 v84, v36, v37\n"
        "v_mfma_f32_16x16x32_bf16 v[188:191], a[140:143], v[80:83], v[188:191]\n"
        "global_load_dword v244, v245, s[26:27] offset:0\n"
        "s_sub_u32 s26, s26, 262144\n"
        "s_subb_u32 s27, s27, 0\n"
        "v_mfma_f32_16x16x32_bf16 v[48:51], a[112:115], v[0:3], v[224:227]\n"
        "ds_read_b128 a[140:143], v241 offset:7168\n"
        "v_cvt_pk_bf16_f32 v80, v32, v33\n"
        "v_mfma_f32_16x16x32_bf16 v[48:51], a[116:119], v[4:7], v[48:51]\n"
        "v_cvt_pk_bf16_f32 v81, v34, v35\n"
        "v_mfma_f32_16x16x32_bf16 v[48:51], a[120:123], v[8:11], v[48:51]\n"
        "v_exp_f32 v38, v38\n"
        "v_mfma_f32_16x16x32_bf16 v[48:51], a[124:127], v[12:15], v[48:51]\n"
        "v_exp_f32 v39, v39\n"
        "v_mfma_f32_16x16x32_bf16 v[52:55], a[124:127], v[76:79], v[228:231]\n"
        "v_cvt_pk_bf16_f32 v85, v38, v39\n"
        "v_mfma_f32_16x16x32_bf16 v[52:55], a[120:123], v[72:75], v[52:55]\n"
        "v_exp_f32 v40, v40\n"
        "v_mfma_f32_16x16x32_bf16 v[52:55], a[116:119], v[68:71], v[52:55]\n"
        "v_exp_f32 v41, v41\n"
        "v_mfma_f32_16x16x32_bf16 v[52:55], a[112:115], v[64:67], v[52:55]\n"
        "v_cvt_pk_bf16_f32 v88, v40, v41\n"
        "v_mfma_f32_16x16x32_bf16 v[56:59], a[112:115], v[96:99], v[232:235]\n"
        "v_exp_f32 v42, v42\n"
        "v_mfma_f32_16x16x32_bf16 v[56:59], a[116:119], v[100:103], v[56:59]\n"
        "v_exp_f32 v43, v43\n"
        "v_mfma_f32_16x16x32_bf16 v[56:59], a[120:123], v[104:107], v[56:59]\n"
        "v_cvt_pk_bf16_f32 v89, v42, v43\n"
        "v_mfma_f32_16x16x32_bf16 v[56:59], a[124:127], v[108:111], v[56:59]\n"
        "v_exp_f32 v44, v44\n"
        "v_mfma_f32_16x16x32_bf16 v[60:63], a[124:127], v[140:143], v[236:239]\n"
        "v_exp_f32 v45, v45\n"
        "v_mfma_f32_16x16x32_bf16 v[60:63], a[120:123], v[136:139], v[60:63]\n"
        "ds_read_b128 a[120:123], v240 offset:22528\n"
        "v_cvt_pk_bf16_f32 v92, v44, v45\n"
        "v_mfma_f32_16x16x32_bf16 v[60:63], a[116:119], v[132:135], v[60:63]\n"
        "ds_read_b128 a[116:119], v240 offset:21504\n"
        "v_mfma_f32_16x16x32_bf16 v[60:63], a[112:115], v[128:131], v[60:63]\n"
        "ds_read_b128 a[112:115], v240 offset:20480\n"
        "v_mfma_f32_16x16x32_bf16 a[64:67], a[80:83], v[16:19], a[64:67]\n"
        "v_exp_f32 v46, v46\n"
        "v_mfma_f32_16x16x32_bf16 a[68:71], a[80:83], v[20:23], a[68:71]\n"
        "LHA12_%=:\n"
        "s_cbranch_execz LHRA_%=\n"
        "v_exp_f32 v47, v47\n"
        "v_mfma_f32_16x16x32_bf16 a[72:75], a[80:83], v[24:27], a[72:75]\n"
        "ds_read_b128 a[124:127], v240 offset:23552\n"
        "v_cvt_pk_bf16_f32 v93, v46, v47\n"
        "v_mfma_f32_16x16x32_bf16 a[76:79], a[80:83], v[28:31], a[76:79]\n"
        "v_exp_f32 v48, v48\n"
        "v_mfma_f32_16x16x32_bf16 v[160:163], a[144:147], v[16:19], v[160:163]\n"
        "v_exp_f32 v49, v49\n"
        "v_mfma_f32_16x16x32_bf16 v[192:195], a[144:147], v[20:23], v[192:195]\n"
        "v_cvt_pk_bf16_f32 v82, v48, v49\n"
        "v_mfma_f32_16x16x32_bf16 a[0:3], a[144:147], v[24:27], a[0:3]\n"
        "v_exp_f32 v50, v50\n"
        "v_mfma_f32_16x16x32_bf16 a[32:35], a[144:147], v[28:31], a[32:35]\n"
        "ds_read_b128 a[144:147], v241 offset:8192\n"
        "v_mfma_f32_16x16x32_bf16 a[36:39], a[148:151], v[28:31], a[36:39]\n"
        "v_exp_f32 v51, v51\n"
        "v_mfma_f32_16x16x32_bf16 a[4:7], a[148:151], v[24:27], a[4:7]\n"
        "v_cvt_pk_bf16_f32 v83, v50, v51\n"
        "v_mfma_f32_16x16x32_bf16 v[196:199], a[148:151], v[20:23], v[196:199]\n"
        "v_exp_f32 v52, v52\n"
        "v_mfma_f32_16x16x32_bf16 v[164:167], a[148:151], v[16:19], v[164:167]\n"
        "ds_read_b128 a[148:151], v241 offset:9216\n"
        "v_mfma_f32_16x16x32_bf16 v[168:171], a[152:155], v[16:19], v[168:171]\n"
        "v_exp_f32 v53, v53\n"
        "v_mfma_f32_16x16x32_bf16 v[200:203], a[152:155], v[20:23], v[200:203]\n"
        "v_cvt_pk_bf16_f32 v86, v52, v53\n"
        "v_mfma_f32_16x16x32_bf16 a[8:11], a[152:155], v[24:27], a[8:11]\n"
        "v_exp_f32 v54, v54\n"
        "v_mfma_f32_16x16x32_bf16 a[40:43], a[152:155], v[28:31], a[40:43]\n"
        "ds_read_b128 a[152:155], v241 offset:10240\n"
        "v_mfma_f32_16x16x32_bf16 a[44:47], a[156:159], v[28:31], a[44:47]\n"
        "v_exp_f32 v55, v55\n"
        "v_mfma_f32_16x16x32_bf16 a[12:15], a[156:159], v[24:27], a[12:15]\n"
        "v_cvt_pk_bf16_f32 v87, v54, v55\n"
        "v_mfma_f32_16x16x32_bf16 v[204:207], a[156:159], v[20:23], v[204:207]\n"
        "s_waitcnt lgkmcnt(2)\n"
        "v_exp_f32 v56, v56\n"
        "v_mfma_f32_16x16x32_bf16 v[172:175], a[156:159], v[16:19], v[172:175]\n"
        "ds_read_b128 a[156:159], v241 offset:11264\n"
        "v_mfma_f32_16x16x32_bf16 v[32:35], a[96:99], v[0:3], v[224:227]\n"
        "v_exp_f32 v57, v57\n"
        "v_mfma_f32_16x16x32_bf16 v[32:35], a[100:103], v[4:7], v[32:35]\n"
        "v_cvt_pk_bf16_f32 v90, v56, v57\n"
        "v_mfma_f32_16x16x32_bf16 v[32:35], a[104:107], v[8:11], v[32:35]\n"
        "v_exp_f32 v58, v58\n"
        "v_mfma_f32_16x16x32_bf16 v[32:35], a[108:111], v[12:15], v[32:35]\n"
        "v_exp_f32 v59, v59\n"
        "v_mfma_f32_16x16x32_bf16 v[36:39], a[108:111], v[76:79], v[228:231]\n"
        "v_cvt_pk_bf16_f32 v91, v58, v59\n"
        "v_mfma_f32_16x16x32_bf16 v[36:39], a[104:107], v[72:75], v[36:39]\n"
        "v_exp_f32 v60, v60\n"
        "v_mfma_f32_16x16x32_bf16 v[36:39], a[100:103], v[68:71], v[36:39]\n"
        "v_exp_f32 v61, v61\n"
        "v_mfma_f32_16x16x32_bf16 v[36:39], a[96:99], v[64:67], v[36:39]\n"
        "v_cvt_pk_bf16_f32 v94, v60, v61\n"
        "v_mfma_f32_16x16x32_bf16 v[40:43], a[96:99], v[96:99], v[232:235]\n"
        "v_exp_f32 v62, v62\n"
        "v_mfma_f32_16x16x32_bf16 v[40:43], a[100:103], v[100:103], v[40:43]\n"
        "v_exp_f32 v63, v63\n"
        "v_mfma_f32_16x16x32_bf16 v[40:43], a[104:107], v[104:107], v[40:43]\n"
        "v_cvt_pk_bf16_f32 v95, v62, v63\n"
        "v_mfma_f32_16x16x32_bf16 v[40:43], a[108:111], v[108:111], v[40:43]\n"
        "v_exp_f32 v32, v32\n"
        "v_mfma_f32_16x16x32_bf16 v[44:47], a[108:111], v[140:143], v[236:239]\n"
        "v_exp_f32 v33, v33\n"
        "v_mfma_f32_16x16x32_bf16 v[44:47], a[104:107], v[136:139], v[44:47]\n"
        "ds_read_b128 a[104:107], v240 offset:26624\n"
        "v_mfma_f32_16x16x32_bf16 v[44:47], a[100:103], v[132:135], v[44:47]\n"
        "ds_read_b128 a[100:103], v240 offset:25600\n"
        "v_mfma_f32_16x16x32_bf16 v[44:47], a[96:99], v[128:131], v[44:47]\n"
        "ds_read_b128 a[96:99], v240 offset:24576\n"
        "v_mfma_f32_16x16x32_bf16 v[176:179], a[128:131], v[16:19], v[176:179]\n"
        "v_exp_f32 v34, v34\n"
        "v_mfma_f32_16x16x32_bf16 v[208:211], a[128:131], v[20:23], v[208:211]\n"
        "v_exp_f32 v35, v35\n"
        "v_mfma_f32_16x16x32_bf16 a[16:19], a[128:131], v[24:27], a[16:19]\n"
        "ds_read_b128 a[108:111], v240 offset:27648\n"
        "v_mfma_f32_16x16x32_bf16 a[48:51], a[128:131], v[28:31], a[48:51]\n"
        "ds_read_b128 a[128:131], v241 offset:12288\n"
        "v_mfma_f32_16x16x32_bf16 a[52:55], a[132:135], v[28:31], a[52:55]\n"
        "v_exp_f32 v36, v36\n"
        "v_mfma_f32_16x16x32_bf16 a[20:23], a[132:135], v[24:27], a[20:23]\n"
        "v_exp_f32 v37, v37\n"
        "v_mfma_f32_16x16x32_bf16 v[212:215], a[132:135], v[20:23], v[212:215]\n"
        "v_exp_f32 v38, v38\n"
        "v_mfma_f32_16x16x32_bf16 v[180:183], a[132:135], v[16:19], v[180:183]\n"
        "ds_read_b128 a[132:135], v241 offset:13312\n"
        "v_mfma_f32_16x16x32_bf16 v[184:187], a[136:139], v[16:19], v[184:187]\n"
        "v_exp_f32 v39, v39\n"
        "v_mfma_f32_16x16x32_bf16 v[216:219], a[136:139], v[20:23], v[216:219]\n"
        "v_exp_f32 v40, v40\n"
        "v_mfma_f32_16x16x32_bf16 a[24:27], a[136:139], v[24:27], a[24:27]\n"
        "v_exp_f32 v41, v41\n"
        "v_mfma_f32_16x16x32_bf16 a[56:59], a[136:139], v[28:31], a[56:59]\n"
        "ds_read_b128 a[136:139], v241 offset:14336\n"
        "v_mfma_f32_16x16x32_bf16 a[60:63], a[140:143], v[28:31], a[60:63]\n"
        "v_exp_f32 v42, v42\n"
        "v_mfma_f32_16x16x32_bf16 a[28:31], a[140:143], v[24:27], a[28:31]\n"
        "v_cvt_pk_bf16_f32 v24, v40, v41\n"
        "v_mfma_f32_16x16x32_bf16 v[220:223], a[140:143], v[20:23], v[220:223]\n"
        "v_cvt_pk_bf16_f32 v20, v36, v37\n"
        "v_cvt_pk_bf16_f32 v21, v38, v39\n"
        "v_mfma_f32_16x16x32_bf16 v[188:191], a[140:143], v[16:19], v[188:191]\n"
        "global_load_dword v244, v245, s[26:27] offset:0\n"
        "s_sub_u32 s26, s26, 262144\n"
        "s_subb_u32 s27, s27, 0\n"
        "v_mfma_f32_16x16x32_bf16 v[48:51], a[112:115], v[0:3], v[224:227]\n"
        "ds_read_b128 a[140:143], v241 offset:15360\n"
        "v_cvt_pk_bf16_f32 v16, v32, v33\n"
        "v_mfma_f32_16x16x32_bf16 v[48:51], a[116:119], v[4:7], v[48:51]\n"
        "v_cvt_pk_bf16_f32 v17, v34, v35\n"
        "v_mfma_f32_16x16x32_bf16 v[48:51], a[120:123], v[8:11], v[48:51]\n"
        "v_exp_f32 v43, v43\n"
        "v_mfma_f32_16x16x32_bf16 v[48:51], a[124:127], v[12:15], v[48:51]\n"
        "v_cvt_pk_bf16_f32 v25, v42, v43\n"
        "v_mfma_f32_16x16x32_bf16 v[52:55], a[124:127], v[76:79], v[228:231]\n"
        "v_exp_f32 v44, v44\n"
        "v_mfma_f32_16x16x32_bf16 v[52:55], a[120:123], v[72:75], v[52:55]\n"
        "v_exp_f32 v45, v45\n"
        "v_mfma_f32_16x16x32_bf16 v[52:55], a[116:119], v[68:71], v[52:55]\n"
        "v_cvt_pk_bf16_f32 v28, v44, v45\n"
        "v_mfma_f32_16x16x32_bf16 v[52:55], a[112:115], v[64:67], v[52:55]\n"
        "v_exp_f32 v46, v46\n"
        "v_mfma_f32_16x16x32_bf16 v[56:59], a[112:115], v[96:99], v[232:235]\n"
        "v_exp_f32 v47, v47\n"
        "v_mfma_f32_16x16x32_bf16 v[56:59], a[116:119], v[100:103], v[56:59]\n"
        "v_cvt_pk_bf16_f32 v29, v46, v47\n"
        "v_mfma_f32_16x16x32_bf16 v[56:59], a[120:123], v[104:107], v[56:59]\n"
        "v_exp_f32 v48, v48\n"
        "v_mfma_f32_16x16x32_bf16 v[56:59], a[124:127], v[108:111], v[56:59]\n"
        "v_exp_f32 v49, v49\n"
        "v_mfma_f32_16x16x32_bf16 v[60:63], a[124:127], v[140:143], v[236:239]\n"
        "ds_read_b128 a[124:127], v240 offset:31744\n"
        "v_cvt_pk_bf16_f32 v18, v48, v49\n"
        "v_mfma_f32_16x16x32_bf16 v[60:63], a[120:123], v[136:139], v[60:63]\n"
        "ds_read_b128 a[120:123], v240 offset:30720\n"
        "v_mfma_f32_16x16x32_bf16 v[60:63], a[116:119], v[132:135], v[60:63]\n"
        "ds_read_b128 a[116:119], v240 offset:29696\n"
        "v_mfma_f32_16x16x32_bf16 v[60:63], a[112:115], v[128:131], v[60:63]\n"
        "ds_read_b128 a[112:115], v240 offset:28672\n"
        "v_xor_b32 v240, 0x8000, v240\n"
        "v_mfma_f32_16x16x32_bf16 a[64:67], a[80:83], v[80:83], a[64:67]\n"
        "v_exp_f32 v50, v50\n"
        "v_mfma_f32_16x16x32_bf16 a[68:71], a[80:83], v[84:87], a[68:71]\n"
        "v_exp_f32 v51, v51\n"
        "v_mfma_f32_16x16x32_bf16 a[72:75], a[80:83], v[88:91], a[72:75]\n"
        "s_waitcnt lgkmcnt(4)\n"
        "v_cvt_pk_bf16_f32 v19, v50, v51\n"
        "v_mfma_f32_16x16x32_bf16 a[76:79], a[80:83], v[92:95], a[76:79]\n"
        "v_exp_f32 v52, v52\n"
        "v_mfma_f32_16x16x32_bf16 v[160:163], a[144:147], v[80:83], v[160:163]\n"
        "v_exp_f32 v53, v53\n"
        "v_mfma_f32_16x16x32_bf16 v[192:195], a[144:147], v[84:87], v[192:195]\n"
        "v_cvt_pk_bf16_f32 v22, v52, v53\n"
        "v_mfma_f32_16x16x32_bf16 a[0:3], a[144:147], v[88:91], a[0:3]\n"
        "v_exp_f32 v54, v54\n"
        "v_mfma_f32_16x16x32_bf16 a[32:35], a[144:147], v[92:95], a[32:35]\n"
        "ds_read_b128 a[144:147], v241 offset:16384\n"
        "s_waitcnt vmcnt(10) lgkmcnt(1)\n"
        "v_mfma_f32_16x16x32_bf16 a[36:39], a[148:151], v[92:95], a[36:39]\n"
        "s_barrier\n"
        "v_exp_f32 v55, v55\n"
        "v_mfma_f32_16x16x32_bf16 a[4:7], a[148:151], v[88:91], a[4:7]\n"
        "s_cmp_eq_u32 s82, s10\n"
        "s_cbranch_scc1 LEVD_%=\n"
        "LEWD_%=:\n"
        "v_cvt_pk_bf16_f32 v23, v54, v55\n"
        "v_mfma_f32_16x16x32_bf16 v[196:199], a[148:151], v[84:87], v[196:199]\n"
        "s_add_u32 m0, s80, 0x0\n"
        "v_exp_f32 v56, v56\n"
        "v_mfma_f32_16x16x32_bf16 v[164:167], a[148:151], v[80:83], v[164:167]\n"
        "ds_read_b128 a[148:151], v241 offset:17408\n"
        "global_load_lds_dwordx4 v254, s[68:69] offset:0 sc1\n"
        "v_mfma_f32_16x16x32_bf16 v[168:171], a[152:155], v[80:83], v[168:171]\n"
        "v_exp_f32 v57, v57\n"
        "v_mfma_f32_16x16x32_bf16 v[200:203], a[152:155], v[84:87], v[200:203]\n"
        "v_cvt_pk_bf16_f32 v26, v56, v57\n"
        "v_mfma_f32_16x16x32_bf16 a[8:11], a[152:155], v[88:91], a[8:11]\n"
        "v_exp_f32 v58, v58\n"
        "v_mfma_f32_16x16x32_bf16 a[40:43], a[152:155], v[92:95], a[40:43]\n"
        "ds_read_b128 a[152:155], v241 offset:18432\n"
        "global_load_lds_dwordx4 v254, s[68:69] offset:1024 sc1\n"
        "v_mfma_f32_16x16x32_bf16 a[44:47], a[156:159], v[92:95], a[44:47]\n"
        "v_exp_f32 v59, v59\n"
        "v_mfma_f32_16x16x32_bf16 a[12:15], a[156:159], v[88:91], a[12:15]\n"
        "v_cvt_pk_bf16_f32 v27, v58, v59\n"
        "v_mfma_f32_16x16x32_bf16 v[204:207], a[156:159], v[84:87], v[204:207]\n"
        "v_exp_f32 v60, v60\n"
        "v_mfma_f32_16x16x32_bf16 v[172:175], a[156:159], v[80:83], v[172:175]\n"
        "ds_read_b128 a[156:159], v241 offset:19456\n"
        "global_load_lds_dwordx4 v254, s[68:69] offset:2048 sc1\n"
        "v_mfma_f32_16x16x32_bf16 v[32:35], a[96:99], v[0:3], v[224:227]\n"
        "v_exp_f32 v61, v61\n"
        "v_mfma_f32_16x16x32_bf16 v[32:35], a[100:103], v[4:7], v[32:35]\n"
        "v_cvt_pk_bf16_f32 v30, v60, v61\n"
        "v_mfma_f32_16x16x32_bf16 v[32:35], a[104:107], v[8:11], v[32:35]\n"
        "v_exp_f32 v62, v62\n"
        "v_mfma_f32_16x16x32_bf16 v[32:35], a[108:111], v[12:15], v[32:35]\n"
        "global_load_lds_dwordx4 v254, s[68:69] offset:3072 sc1\n"
        "v_mfma_f32_16x16x32_bf16 v[36:39], a[108:111], v[76:79], v[228:231]\n"
        "v_exp_f32 v63, v63\n"
        "v_mfma_f32_16x16x32_bf16 v[36:39], a[104:107], v[72:75], v[36:39]\n"
        "v_cvt_pk_bf16_f32 v31, v62, v63\n"
        "v_mfma_f32_16x16x32_bf16 v[36:39], a[100:103], v[68:71], v[36:39]\n"
        "v_mfma_f32_16x16x32_bf16 v[36:39], a[96:99], v[64:67], v[36:39]\n"
        "v_mfma_f32_16x16x32_bf16 v[40:43], a[96:99], v[96:99], v[232:235]\n"
        "v_exp_f32 v32, v32\n"
        "v_mfma_f32_16x16x32_bf16 v[40:43], a[100:103], v[100:103], v[40:43]\n"
        "v_exp_f32 v33, v33\n"
        "v_mfma_f32_16x16x32_bf16 v[40:43], a[104:107], v[104:107], v[40:43]\n"
        "v_exp_f32 v34, v34\n"
        "v_mfma_f32_16x16x32_bf16 v[40:43], a[108:111], v[108:111], v[40:43]\n"
        "v_exp_f32 v35, v35\n"
        "v_mfma_f32_16x16x32_bf16 v[44:47], a[108:111], v[140:143], v[236:239]\n"
        "ds_read_b128 a[108:111], v240 offset:3072\n"
        "v_mfma_f32_16x16x32_bf16 v[44:47], a[104:107], v[136:139], v[44:47]\n"
        "ds_read_b128 a[104:107], v240 offset:2048\n"
        "v_mfma_f32_16x16x32_bf16 v[44:47], a[100:103], v[132:135], v[44:47]\n"
        "ds_read_b128 a[100:103], v240 offset:1024\n"
        "v_mfma_f32_16x16x32_bf16 v[44:47], a[96:99], v[128:131], v[44:47]\n"
        "ds_read_b128 a[96:99], v240 offset:0\n"
        "v_mfma_f32_16x16x32_bf16 v[176:179], a[128:131], v[80:83], v[176:179]\n"
        "v_exp_f32 v36, v36\n"
        "v_mfma_f32_16x16x32_bf16 v[208:211], a[128:131], v[84:87], v[208:211]\n"
        "v_exp_f32 v37, v37\n"
        "v_mfma_f32_16x16x32_bf16 a[16:19], a[128:131], v[88:91], a[16:19]\n"
        "v_exp_f32 v38, v38\n"
        "v_mfma_f32_16x16x32_bf16 a[48:51], a[128:131], v[92:95], a[48:51]\n"
        "ds_read_b128 a[128:131], v241 offset:20480\n"
        "v_mfma_f32_16x16x32_bf16 a[52:55], a[132:135], v[92:95], a[52:55]\n"
        "v_exp_f32 v39, v39\n"
        "v_mfma_f32_16x16x32_bf16 a[20:23], a[132:135], v[88:91], a[20:23]\n"
        "v_exp_f32 v40, v40\n"
        "v_mfma_f32_16x16x32_bf16 v[212:215], a[132:135], v[84:87], v[212:215]\n"
        "v_exp_f32 v41, v41\n"
        "v_mfma_f32_16x16x32_bf16 v[180:183], a[132:135], v[80:83], v[180:183]\n"
        "ds_read_b128 a[132:135], v241 offset:21504\n"
        "v_mfma_f32_16x16x32_bf16 v[184:187], a[136:139], v[80:83], v[184:187]\n"
        "v_exp_f32 v42, v42\n"
        "v_mfma_f32_16x16x32_bf16 v[216:219], a[136:139], v[84:87], v[216:219]\n"
        "v_exp_f32 v43, v43\n"
        "v_mfma_f32_16x16x32_bf16 a[24:27], a[136:139], v[88:91], a[24:27]\n"
        "v_exp_f32 v44, v44\n"
        "v_mfma_f32_16x16x32_bf16 a[56:59], a[136:139], v[92:95], a[56:59]\n"
        "ds_read_b128 a[136:139], v241 offset:22528\n"
        "s_waitcnt lgkmcnt(2)\n"
        "v_mfma_f32_16x16x32_bf16 a[60:63], a[140:143], v[92:95], a[60:63]\n"
        "v_exp_f32 v45, v45\n"
        "v_mfma_f32_16x16x32_bf16 a[28:31], a[140:143], v[88:91], a[28:31]\n"
        "v_cvt_pk_bf16_f32 v88, v40, v41\n"
        "v_cvt_pk_bf16_f32 v89, v42, v43\n"
        "v_mfma_f32_16x16x32_bf16 v[220:223], a[140:143], v[84:87], v[220:223]\n"
        "v_cvt_pk_bf16_f32 v84, v36, v37\n"
        "v_cvt_pk_bf16_f32 v85, v38, v39\n"
        "v_mfma_f32_16x16x32_bf16 v[188:191], a[140:143], v[80:83], v[188:191]\n"
        "ds_read_b128 a[140:143], v241 offset:23552\n"
        "v_cvt_pk_bf16_f32 v80, v32, v33\n"
        "v_mfma_f32_16x16x32_bf16 v[48:51], a[112:115], v[0:3], v[224:227]\n"
        "v_cvt_pk_bf16_f32 v81, v34, v35\n"
        "v_cvt_pk_bf16_f32 v92, v44, v45\n"
        "v_mfma_f32_16x16x32_bf16 v[48:51], a[116:119], v[4:7], v[48:51]\n"
        "v_exp_f32 v46, v46\n"
        "v_mfma_f32_16x16x32_bf16 v[48:51], a[120:123], v[8:11], v[48:51]\n"
        "v_exp_f32 v47, v47\n"
        "v_mfma_f32_16x16x32_bf16 v[48:51], a[124:127], v[12:15], v[48:51]\n"
        "v_cvt_pk_bf16_f32 v93, v46, v47\n"
        "v_mfma_f32_16x16x32_bf16 v[52:55], a[124:127], v[76:79], v[228:231]\n"
        "v_mfma_f32_16x16x32_bf16 v[52:55], a[120:123], v[72:75], v[52:55]\n"
        "v_mfma_f32_16x16x32_bf16 v[52:55], a[116:119], v[68:71], v[52:55]\n"
        "v_mfma_f32_16x16x32_bf16 v[52:55], a[112:115], v[64:67], v[52:55]\n"
        "v_mfma_f32_16x16x32_bf16 v[56:59], a[112:115], v[96:99], v[232:235]\n"
        "v_mfma_f32_16x16x32_bf16 v[56:59], a[116:119], v[100:103], v[56:59]\n"
        "v_mfma_f32_16x16x32_bf16 v[56:59], a[120:123], v[104:107], v[56:59]\n"
        "v_mfma_f32_16x16x32_bf16 v[56:59], a[124:127], v[108:111], v[56:59]\n"
        "v_mfma_f32_16x16x32_bf16 v[60:63], a[124:127], v[140:143], v[236:239]\n"
        "v_mfma_f32_16x16x32_bf16 v[60:63], a[120:123], v[136:139], v[60:63]\n"
        "ds_read_b128 a[120:123], v240 offset:6144\n"
        "v_mfma_f32_16x16x32_bf16 v[60:63], a[116:119], v[132:135], v[60:63]\n"
        "ds_read_b128 a[116:119], v240 offset:5120\n"
        "v_mfma_f32_16x16x32_bf16 v[60:63], a[112:115], v[128:131], v[60:63]\n"
        "ds_read_b128 a[112:115], v240 offset:4096\n"
        "v_mfma_f32_16x16x32_bf16 a[64:67], a[80:83], v[16:19], a[64:67]\n"
        "v_mfma_f32_16x16x32_bf16 a[68:71], a[80:83], v[20:23], a[68:71]\n"
        "v_mfma_f32_16x16x32_bf16 a[72:75], a[80:83], v[24:27], a[72:75]\n"
        "v_mfma_f32_16x16x32_bf16 a[76:79], a[80:83], v[28:31], a[76:79]\n"
        "v_mfma_f32_16x16x32_bf16 v[160:163], a[144:147], v[16:19], v[160:163]\n"
        "v_mfma_f32_16x16x32_bf16 v[192:195], a[144:147], v[20:23], v[192:195]\n"
        "v_mfma_f32_16x16x32_bf16 a[0:3], a[144:147], v[24:27], a[0:3]\n"
        "v_mfma_f32_16x16x32_bf16 a[32:35], a[144:147], v[28:31], a[32:35]\n"
        "ds_read_b128 a[144:147], v241 offset:24576\n"
        "v_mfma_f32_16x16x32_bf16 a[36:39], a[148:151], v[28:31], a[36:39]\n"
        "v_mfma_f32_16x16x32_bf16 a[4:7], a[148:151], v[24:27], a[4:7]\n"
        "v_mfma_f32_16x16x32_bf16 v[196:199], a[148:151], v[20:23], v[196:199]\n"
        "v_mfma_f32_16x16x32_bf16 v[164:167], a[148:151], v[16:19], v[164:167]\n"
        "ds_read_b128 a[148:151], v241 offset:25600\n"
        "v_mfma_f32_16x16x32_bf16 v[168:171], a[152:155], v[16:19], v[168:171]\n"
        "v_mfma_f32_16x16x32_bf16 v[200:203], a[152:155], v[20:23], v[200:203]\n"
        "v_mfma_f32_16x16x32_bf16 a[8:11], a[152:155], v[24:27], a[8:11]\n"
        "v_mfma_f32_16x16x32_bf16 a[40:43], a[152:155], v[28:31], a[40:43]\n"
        "ds_read_b128 a[152:155], v241 offset:26624\n"
        "v_mfma_f32_16x16x32_bf16 a[44:47], a[156:159], v[28:31], a[44:47]\n"
        "v_mfma_f32_16x16x32_bf16 a[12:15], a[156:159], v[24:27], a[12:15]\n"
        "v_mfma_f32_16x16x32_bf16 v[204:207], a[156:159], v[20:23], v[204:207]\n"
        "v_mfma_f32_16x16x32_bf16 v[172:175], a[156:159], v[16:19], v[172:175]\n"
        "s_cmp_lg_u32 s91, 0\n"
        "s_cbranch_scc1 LDU_%=\n"
        "s_branch LLD_%=\n"
        "LEVD_%=:\n"
        "s_lshl_b32 s97, s41, 12\n"
        "s_add_u32 s97, s97, 32768\n"
        "v_bfe_u32 v246, v254, 4, 5\n"
        "v_lshl_add_u32 v246, v246, 7, s97\n"
        "s_cmp_eq_u32 s82, s83\n"
        "s_cbranch_scc1 LE3D_%=\n"
        "global_load_dwordx2 v[252:253], v246, s[48:49] sc1\n"
        "s_mov_b32 s10, s83\n"
        "s_branch LEWD_%=\n"
        "LE3D_%=:\n"
        "s_mov_b32 s97, 0x200000\n"
        "LE4D_%=:\n"
        "v_cmp_ne_u64 vcc, v[252:253], v[250:251]\n"
        "s_nop 1\n"
        "s_cbranch_vccz LE5D_%=\n"
        "s_sleep 2\n"
        "global_load_dwordx2 v[252:253], v246, s[48:49] sc1\n"
        "s_waitcnt vmcnt(0)\n"
        "s_sub_u32 s97, s97, 1\n"
        "s_cmp_lg_u32 s97, 0\n"
        "s_cbranch_scc1 LE4D_%=\n"
        "LE5D_%=:\n"
        "s_mov_b64 s[68:69], s[92:93]\n"
        "s_mov_b64 s[74:75], s[94:95]\n"
        "s_mov_b32 s80, s50\n"
        "s_mov_b32 s81, s51\n"
        "s_branch LEWD_%=\n"
        "; ---- LDRAIN\n"
        "LLD_%=:\n"
        "v_mfma_f32_16x16x32_bf16 a[16:19], a[128:131], v[24:27], a[16:19]\n"
        "s_lshl_b32 s53, s44, 20\n"
        "s_lshl_b32 s96, s43, 15\n"
        "s_add_u32 s53, s53, s96\n"
        "v_mfma_f32_16x16x32_bf16 a[48:51], a[128:131], v[28:31], a[48:51]\n"
        "s_lshl_b32 s96, s40, 10\n"
        "s_add_u32 s53, s53, s96\n"
        "s_waitcnt lgkmcnt(1)\n"
        "v_mfma_f32_16x16x32_bf16 a[52:55], a[132:135], v[28:31], a[52:55]\n"
        "s_add_u32 s53, s53, 0x2001000\n"
        "s_add_u32 s70, s38, s53\n"
        "s_addc_u32 s71, s39, 0\n"
        "v_mfma_f32_16x16x32_bf16 a[20:23], a[132:135], v[24:27], a[20:23]\n"
        "ds_read_b128 a[160:163], v241 offset:24576\n"
        "s_sub_u32 s91, s83, s44\n"
        "v_mfma_f32_16x16x32_bf16 a[24:27], a[136:139], v[24:27], a[24:27]\n"
        "ds_read_b128 a[164:167], v241 offset:25600\n"
        "v_mfma_f32_16x16x32_bf16 a[56:59], a[136:139], v[28:31], a[56:59]\n"
        "ds_read_b128 a[168:171], v241 offset:26624\n"
        "v_mfma_f32_16x16x32_bf16 a[60:63], a[140:143], v[28:31], a[60:63]\n"
        "ds_read_b128 a[172:175], v241 offset:27648\n"
        "v_mfma_f32_16x16x32_bf16 a[28:31], a[140:143], v[24:27], a[28:31]\n"
        "ds_read_b128 a[176:179], v241 offset:28672\n"
        "ds_read_b128 a[180:183], v241 offset:29696\n"
        "ds_read_b128 a[184:187], v241 offset:30720\n"
        "ds_read_b128 a[188:191], v241 offset:31744\n"
        "v_exp_f32 v56, v56\n"
        "v_exp_f32 v57, v57\n"
        "v_exp_f32 v58, v58\n"
        "v_cvt_pk_bf16_f32 v90, v56, v57\n"
        "v_exp_f32 v59, v59\n"
        "v_exp_f32 v60, v60\n"
        "v_cvt_pk_bf16_f32 v91, v58, v59\n"
        "s_waitcnt lgkmcnt(6)\n"
        "s_nop 0\n"
        "v_mfma_f32_16x16x32_bf16 a[0:3], a[160:163], v[88:91], a[0:3]\n"
        "s_waitcnt lgkmcnt(5)\n"
        "v_exp_f32 v61, v61\n"
        "v_mfma_f32_16x16x32_bf16 a[72:75], a[80:83], v[88:91], a[72:75]\n"
        "s_waitcnt lgkmcnt(4)\n"
        "v_cvt_pk_bf16_f32 v94, v60, v61\n"
        "v_mfma_f32_16x16x32_bf16 a[4:7], a[164:167], v[88:91], a[4:7]\n"
        "s_waitcnt lgkmcnt(3)\n"
        "v_exp_f32 v62, v62\n"
        "v_mfma_f32_16x16x32_bf16 a[8:11], a[168:171], v[88:91], a[8:11]\n"
        "s_waitcnt lgkmcnt(2)\n"
        "v_exp_f32 v63, v63\n"
        "v_mfma_f32_16x16x32_bf16 a[12:15], a[172:175], v[88:91], a[12:15]\n"
        "s_waitcnt lgkmcnt(0)\n"
        "v_cvt_pk_bf16_f32 v95, v62, v63\n"
        "s_nop 1\n"
        "v_mfma_f32_16x16x32_bf16 a[76:79], a[80:83], v[92:95], a[76:79]\n"
        "v_accvgpr_read_b32 v248, a72\n"
        "v_accvgpr_read_b32 v120, a0\n"
        "v_mfma_f32_16x16x32_bf16 a[32:35], a[160:163], v[92:95], a[32:35]\n"
        "v_rcp_f32 v246, v248\n"
        "v_mfma_f32_16x16x32_bf16 a[36:39], a[164:167], v[92:95], a[36:39]\n"
        "v_accvgpr_read_b32 v121, a1\n"
        "v_accvgpr_read_b32 v122, a2\n"
        "v_mfma_f32_16x16x32_bf16 a[40:43], a[168:171], v[92:95], a[40:43]\n"
        "v_accvgpr_read_b32 v249, a76\n"
        "v_accvgpr_read_b32 v123, a3\n"
        "v_mfma_f32_16x16x32_bf16 a[44:47], a[172:175], v[92:95], a[44:47]\n"
        "v_rcp_f32 v247, v249\n"
        "v_mfma_f32_16x16x32_bf16 a[16:19], a[176:179], v[88:91], a[16:19]\n"
        "v_accvgpr_read_b32 v112, a32\n"
        "v_accvgpr_read_b32 v113, a33\n"
        "v_mfma_f32_16x16x32_bf16 a[48:51], a[176:179], v[92:95], a[48:51]\n"
        "v_accvgpr_read_b32 v114, a34\n"
        "v_accvgpr_read_b32 v115, a35\n"
        "v_mfma_f32_16x16x32_bf16 a[52:55], a[180:183], v[92:95], a[52:55]\n"
        "v_accvgpr_read_b32 v116, a36\n"
        "v_accvgpr_read_b32 v117, a37\n"
        "v_mfma_f32_16x16x32_bf16 a[20:23], a[180:183], v[88:91], a[20:23]\n"
        "v_accvgpr_read_b32 v118, a38\n"
        "v_accvgpr_read_b32 v119, a39\n"
        "v_mfma_f32_16x16x32_bf16 a[24:27], a[184:187], v[88:91], a[24:27]\n"
        "v_mul_f32 v112, v112, v247\n"
        "v_mul_f32 v113, v113, v247\n"
        "v_mfma_f32_16x16x32_bf16 a[56:59], a[184:187], v[92:95], a[56:59]\n"
        "v_mul_f32 v114, v114, v247\n"
        "v_mul_f32 v115, v115, v247\n"
        "v_mfma_f32_16x16x32_bf16 a[60:63], a[188:191], v[92:95], a[60:63]\n"
        "v_mul_f32 v116, v116, v247\n"
        "v_mul_f32 v117, v117, v247\n"
        "v_mfma_f32_16x16x32_bf16 a[28:31], a[188:191], v[88:91], a[28:31]\n"
        "v_mul_f32 v118, v118, v247\n"
        "v_mul_f32 v119, v119, v247\n"
        "v_cvt_pk_bf16_f32 v112, v112, v113\n"
        "v_cvt_pk_bf16_f32 v113, v114, v115\n"
        "v_cvt_pk_bf16_f32 v114, v116, v117\n"
        "v_cvt_pk_bf16_f32 v115, v118, v119\n"
        "global_store_dwordx4 v254, v[112:115], s[70:71] offset:-4096 sc1\n"
        "v_accvgpr_read_b32 v124, a4\n"
        "v_accvgpr_read_b32 v125, a5\n"
        "v_accvgpr_read_b32 v126, a6\n"
        "v_accvgpr_read_b32 v127, a7\n"
        "v_mul_f32 v120, v120, v246\n"
        "v_mul_f32 v121, v121, v246\n"
        "v_mul_f32 v122, v122, v246\n"
        "v_mul_f32 v123, v123, v246\n"
        "v_mul_f32 v124, v124, v246\n"
        "v_mul_f32 v125, v125, v246\n"
        "v_mul_f32 v126, v126, v246\n"
        "v_mul_f32 v127, v127, v246\n"
        "v_cvt_pk_bf16_f32 v120, v120, v121\n"
        "v_cvt_pk_bf16_f32 v121, v122, v123\n"
        "v_cvt_pk_bf16_f32 v122, v124, v125\n"
        "v_cvt_pk_bf16_f32 v123, v126, v127\n"
        "global_store_dwordx4 v254, v[120:123], s[70:71] offset:0 sc1\n"
        "s_add_u32 s70, s70, 0x2000\n"
        "s_addc_u32 s71, s71, 0\n"
        "v_accvgpr_read_b32 v144, a40\n"
        "v_accvgpr_read_b32 v145, a41\n"
        "v_accvgpr_read_b32 v146, a42\n"
        "v_accvgpr_read_b32 v147, a43\n"
        "v_accvgpr_read_b32 v148, a44\n"
        "v_accvgpr_read_b32 v149, a45\n"
        "v_accvgpr_read_b32 v150, a46\n"
        "v_accvgpr_read_b32 v151, a47\n"
        "v_mul_f32 v144, v144, v247\n"
        "v_mul_f32 v145, v145, v247\n"
        "v_mul_f32 v146, v146, v247\n"
        "v_mul_f32 v147, v147, v247\n"
        "v_mul_f32 v148, v148, v247\n"
        "v_mul_f32 v149, v149, v247\n"
        "v_mul_f32 v150, v150, v247\n"
        "v_mul_f32 v151, v151, v247\n"
        "v_cvt_pk_bf16_f32 v144, v144, v145\n"
        "v_cvt_pk_bf16_f32 v145, v146, v147\n"
        "v_cvt_pk_bf16_f32 v146, v148, v149\n"
        "v_cvt_pk_bf16_f32 v147, v150, v151\n"
        "global_store_dwordx4 v254, v[144:147], s[70:71] offset:-4096 sc1\n"
        "v_accvgpr_read_b32 v152, a8\n"
        "v_accvgpr_read_b32 v153, a9\n"
        "v_accvgpr_read_b32 v154, a10\n"
        "v_accvgpr_read_b32 v155, a11\n"
        "v_accvgpr_read_b32 v156, a12\n"
        "v_accvgpr_read_b32 v157, a13\n"
        "v_accvgpr_read_b32 v158, a14\n"
        "v_accvgpr_read_b32 v159, a15\n"
        "v_mul_f32 v152, v152, v246\n"
        "v_mul_f32 v153, v153, v246\n"
        "v_mul_f32 v154, v154, v246\n"
        "v_mul_f32 v155, v155, v246\n"
        "v_mul_f32 v156, v156, v246\n"
        "v_mul_f32 v157, v157, v246\n"
        "v_mul_f32 v158, v158, v246\n"
        "v_mul_f32 v159, v159, v246\n"
        "v_cvt_pk_bf16_f32 v152, v152, v153\n"
        "v_cvt_pk_bf16_f32 v153, v154, v155\n"
        "v_cvt_pk_bf16_f32 v154, v156, v157\n"
        "v_cvt_pk_bf16_f32 v155, v158, v159\n"
        "global_store_dwordx4 v254, v[152:155], s[70:71] offset:0 sc1\n"
        "s_add_u32 s70, s70, 0x2000\n"
        "s_addc_u32 s71, s71, 0\n"
        "v_accvgpr_read_b32 v112, a48\n"
        "v_accvgpr_read_b32 v113, a49\n"
        "v_accvgpr_read_b32 v114, a50\n"
        "v_accvgpr_read_b32 v115, a51\n"
        "v_accvgpr_read_b32 v116, a52\n"
        "v_accvgpr_read_b32 v117, a53\n"
        "v_accvgpr_read_b32 v118, a54\n"
        "v_accvgpr_read_b32 v119, a55\n"
        "v_mul_f32 v112, v112, v247\n"
        "v_mul_f32 v113, v113, v247\n"
        "v_mul_f32 v114, v114, v247\n"
        "v_mul_f32 v115, v115, v247\n"
        "v_mul_f32 v116, v116, v247\n"
        "v_mul_f32 v117, v117, v247\n"
        "v_mul_f32 v118, v118, v247\n"
        "v_mul_f32 v119, v119, v247\n"
        "v_cvt_pk_bf16_f32 v112, v112, v113\n"
        "v_cvt_pk_bf16_f32 v113, v114, v115\n"
        "v_cvt_pk_bf16_f32 v114, v116, v117\n"
        "v_cvt_pk_bf16_f32 v115, v118, v119\n"
        "global_store_dwordx4 v254, v[112:115], s[70:71] offset:-4096 sc1\n"
        "v_accvgpr_read_b32 v120, a16\n"
        "v_accvgpr_read_b32 v121, a17\n"
        "v_accvgpr_read_b32 v122, a18\n"
        "v_accvgpr_read_b32 v123, a19\n"
        "v_accvgpr_read_b32 v124, a20\n"
        "v_accvgpr_read_b32 v125, a21\n"
        "v_accvgpr_read_b32 v126, a22\n"
        "v_accvgpr_read_b32 v127, a23\n"
        "v_mul_f32 v120, v120, v246\n"
        "v_mul_f32 v121, v121, v246\n"
        "v_mul_f32 v122, v122, v246\n"
        "v_mul_f32 v123, v123, v246\n"
        "v_mul_f32 v124, v124, v246\n"
        "v_mul_f32 v125, v125, v246\n"
        "v_mul_f32 v126, v126, v246\n"
        "v_mul_f32 v127, v127, v246\n"
        "v_cvt_pk_bf16_f32 v120, v120, v121\n"
        "v_cvt_pk_bf16_f32 v121, v122, v123\n"
        "v_cvt_pk_bf16_f32 v122, v124, v125\n"
        "v_cvt_pk_bf16_f32 v123, v126, v127\n"
        "global_store_dwordx4 v254, v[120:123], s[70:71] offset:0 sc1\n"
        "s_add_u32 s70, s70, 0x2000\n"
        "s_addc_u32 s71, s71, 0\n"
        "v_accvgpr_read_b32 v144, a56\n"
        "v_accvgpr_read_b32 v145, a57\n"
        "v_accvgpr_read_b32 v146, a58\n"
        "v_accvgpr_read_b32 v147, a59\n"
        "v_accvgpr_read_b32 v148, a60\n"
        "v_accvgpr_read_b32 v149, a61\n"
        "v_accvgpr_read_b32 v150, a62\n"
        "v_accvgpr_read_b32 v151, a63\n"
        "v_mul_f32 v144, v144, v247\n"
        "v_mul_f32 v145, v145, v247\n"
        "v_mul_f32 v146, v146, v247\n"
        "v_mul_f32 v147, v147, v247\n"
        "v_mul_f32 v148, v148, v247\n"
        "v_mul_f32 v149, v149, v247\n"
        "v_mul_f32 v150, v150, v247\n"
        "v_mul_f32 v151, v151, v247\n"
        "v_cvt_pk_bf16_f32 v144, v144, v145\n"
        "v_cvt_pk_bf16_f32 v145, v146, v147\n"
        "v_cvt_pk_bf16_f32 v146, v148, v149\n"
        "v_cvt_pk_bf16_f32 v147, v150, v151\n"
        "global_store_dwordx4 v254, v[144:147], s[70:71] offset:-4096 sc1\n"
        "v_accvgpr_read_b32 v152, a24\n"
        "v_accvgpr_read_b32 v153, a25\n"
        "v_accvgpr_read_b32 v154, a26\n"
        "v_accvgpr_read_b32 v155, a27\n"
        "v_accvgpr_read_b32 v156, a28\n"
        "v_accvgpr_read_b32 v157, a29\n"
        "v_accvgpr_read_b32 v158, a30\n"
        "v_accvgpr_read_b32 v159, a31\n"
        "v_mul_f32 v152, v152, v246\n"
        "v_mul_f32 v153, v153, v246\n"
        "v_mul_f32 v154, v154, v246\n"
        "v_mul_f32 v155, v155, v246\n"
        "v_mul_f32 v156, v156, v246\n"
        "v_mul_f32 v157, v157, v246\n"
        "v_mul_f32 v158, v158, v246\n"
        "v_mul_f32 v159, v159, v246\n"
        "v_cvt_pk_bf16_f32 v152, v152, v153\n"
        "v_cvt_pk_bf16_f32 v153, v154, v155\n"
        "v_cvt_pk_bf16_f32 v154, v156, v157\n"
        "v_cvt_pk_bf16_f32 v155, v158, v159\n"
        "global_store_dwordx4 v254, v[152:155], s[70:71] offset:0 sc1\n"
        "v_add_f32 v247, v248, v249\n"
        "v_cmp_nge_f32 vcc, 0x4e6e6b28, v247\n"
        "s_nop 0\n"
        "s_cbranch_vccnz LRL_%=\n"
        "LSL_%=:\n"
        "s_mov_b64 exec, 0\n"
        "s_cmp_eq_u32 s40, 0\n"
        "s_cbranch_scc1 LHB0_%=\n"
        "s_cmp_eq_u32 s40, 1\n"
        "s_cbranch_scc1 LHB1_%=\n"
        "s_cmp_eq_u32 s40, 2\n"
        "s_cbranch_scc1 LHB2_%=\n"
        "s_branch LHB3_%=\n"
        "LHRB_%=:\n"
        "s_mov_b64 exec, -1\n"
        "s_cmp_eq_u32 s91, 0\n"
        "s_cbranch_scc1 LTL_%=\n"
        "; ---- SINGLE\n"
        "s_nop 0\n"
        "s_nop 0\n"
        "s_nop 0\n"
        "LSI_%=:\n"
        "v_mfma_f32_16x16x32_bf16 v[32:35], a[96:99], v[0:3], v[224:227]\n"
        "s_add_u32 m0, s81, 0x0\n"
        "s_xor_b32 s79, s79, 0x8000\n"
        "s_waitcnt lgkmcnt(3)\n"
        "v_mfma_f32_16x16x32_bf16 v[32:35], a[100:103], v[4:7], v[32:35]\n"
        "global_load_lds_dwordx4 v254, s[74:75] offset:0 sc1\n"
        "ds_read_b128 a[156:159], v241 offset:27648\n"
        "s_add_u32 s82, s82, 1\n"
        "v_mfma_f32_16x16x32_bf16 v[32:35], a[104:107], v[8:11], v[32:35]\n"
        "s_xor_b32 s78, s78, 0x8000\n"
        "v_exp_f32 v48, v48\n"
        "v_mfma_f32_16x16x32_bf16 v[32:35], a[108:111], v[12:15], v[32:35]\n"
        "s_add_u32 s81, s78, 4096\n"
        "v_exp_f32 v49, v49\n"
        "v_mfma_f32_16x16x32_bf16 v[36:39], a[108:111], v[76:79], v[228:231]\n"
        "ds_read_b128 a[124:127], v240 offset:7168\n"
        "s_mov_b32 s80, s78\n"
        "v_cvt_pk_bf16_f32 v82, v48, v49\n"
        "v_mfma_f32_16x16x32_bf16 v[36:39], a[104:107], v[72:75], v[36:39]\n"
        "global_load_lds_dwordx4 v254, s[74:75] offset:1024 sc1\n"
        "ds_read_b128 a[104:107], v240 offset:10240\n"
        "s_sub_u32 s91, s91, 1\n"
        "v_mfma_f32_16x16x32_bf16 v[36:39], a[100:103], v[68:71], v[36:39]\n"
        "v_exp_f32 v50, v50\n"
        "v_mfma_f32_16x16x32_bf16 v[36:39], a[96:99], v[64:67], v[36:39]\n"
        "v_exp_f32 v51, v51\n"
        "v_mfma_f32_16x16x32_bf16 v[176:179], a[128:131], v[16:19], v[176:179]\n"
        "ds_read_b128 a[96:99], v240 offset:8192\n"
        "v_cvt_pk_bf16_f32 v83, v50, v51\n"
        "v_mfma_f32_16x16x32_bf16 v[208:211], a[128:131], v[20:23], v[208:211]\n"
        "global_load_lds_dwordx4 v254, s[74:75] offset:2048 sc1\n"
        "ds_read_b128 a[128:131], v241 offset:28672\n"
        "v_mfma_f32_16x16x32_bf16 v[212:215], a[132:135], v[20:23], v[212:215]\n"
        "LHA13_%=:\n"
        "s_cbranch_execz LHRA_%=\n"
        "v_exp_f32 v52, v52\n"
        "v_mfma_f32_16x16x32_bf16 v[180:183], a[132:135], v[16:19], v[180:183]\n"
        "v_exp_f32 v53, v53\n"
        "v_mfma_f32_16x16x32_bf16 v[184:187], a[136:139], v[16:19], v[184:187]\n"
        "ds_read_b128 a[100:103], v240 offset:9216\n"
        "v_cvt_pk_bf16_f32 v86, v52, v53\n"
        "v_mfma_f32_16x16x32_bf16 v[216:219], a[136:139], v[20:23], v[216:219]\n"
        "ds_read_b128 a[132:135], v241 offset:29696\n"
        "global_load_lds_dwordx4 v254, s[74:75] offset:3072 sc1\n"
        "s_add_u32 m0, s79, 0x0\n"
        "v_mfma_f32_16x16x32_bf16 v[220:223], a[140:143], v[20:23], v[220:223]\n"
        "s_waitcnt lgkmcnt(4)\n"
        "v_exp_f32 v54, v54\n"
        "v_mfma_f32_16x16x32_bf16 v[188:191], a[140:143], v[16:19], v[188:191]\n"
        "v_exp_f32 v55, v55\n"
        "v_mfma_f32_16x16x32_bf16 v[48:51], a[112:115], v[0:3], v[224:227]\n"
        "ds_read_b128 a[136:139], v241 offset:30720\n"
        "v_cvt_pk_bf16_f32 v87, v54, v55\n"
        "v_mfma_f32_16x16x32_bf16 v[48:51], a[116:119], v[4:7], v[48:51]\n"
        "ds_read_b128 a[140:143], v241 offset:31744\n"
        "v_xor_b32 v241, 0x8000, v241\n"
        "v_mfma_f32_16x16x32_bf16 v[48:51], a[120:123], v[8:11], v[48:51]\n"
        "v_exp_f32 v32, v32\n"
        "v_mfma_f32_16x16x32_bf16 v[48:51], a[124:127], v[12:15], v[48:51]\n"
        "v_exp_f32 v33, v33\n"
        "v_mfma_f32_16x16x32_bf16 v[52:55], a[124:127], v[76:79], v[228:231]\n"
        "ds_read_b128 a[108:111], v240 offset:11264\n"
        "v_cvt_pk_bf16_f32 v16, v32, v33\n"
        "v_mfma_f32_16x16x32_bf16 v[52:55], a[120:123], v[72:75], v[52:55]\n"
        "v_exp_f32 v34, v34\n"
        "v_mfma_f32_16x16x32_bf16 v[52:55], a[116:119], v[68:71], v[52:55]\n"
        "v_exp_f32 v35, v35\n"
        "v_mfma_f32_16x16x32_bf16 v[52:55], a[112:115], v[64:67], v[52:55]\n"
        "ds_read_b128 a[112:115], v240 offset:12288\n"
        "v_cvt_pk_bf16_f32 v17, v34, v35\n"
        "v_mfma_f32_16x16x32_bf16 a[64:67], a[80:83], v[80:83], a[64:67]\n"
        "v_exp_f32 v36, v36\n"
        "v_mfma_f32_16x16x32_bf16 a[68:71], a[80:83], v[84:87], a[68:71]\n"
        "s_waitcnt vmcnt(9) lgkmcnt(2)\n"
        "v_exp_f32 v37, v37\n"
        "v_mfma_f32_16x16x32_bf16 v[160:163], a[144:147], v[80:83], v[160:163]\n"
        "ds_read_b128 a[116:119], v240 offset:13312\n"
        "s_barrier\n"
        "v_cvt_pk_bf16_f32 v20, v36, v37\n"
        "v_mfma_f32_16x16x32_bf16 v[192:195], a[144:147], v[84:87], v[192:195]\n"
        "s_add_u32 s76, s66, s90\n"
        "s_addc_u32 s77, s67, 0\n"
        "v_mfma_f32_16x16x32_bf16 v[196:199], a[148:151], v[84:87], v[196:199]\n"
        "global_load_lds_dwordx4 v254, s[76:77] offset:0 sc1\n"
        "ds_read_b128 a[144:147], v241 offset:0\n"
        "v_mfma_f32_16x16x32_bf16 v[164:167], a[148:151], v[80:83], v[164:167]\n"
        "ds_read_b128 a[148:151], v241 offset:1024\n"
        "s_cmp_ge_u32 s82, s44\n"
        "s_addc_u32 s90, s82, 0\n"
        "v_mfma_f32_16x16x32_bf16 v[168:171], a[152:155], v[80:83], v[168:171]\n"
        "s_lshl_b32 s90, s90, 15\n"
        "v_exp_f32 v38, v38\n"
        "v_mfma_f32_16x16x32_bf16 v[200:203], a[152:155], v[84:87], v[200:203]\n"
        "v_exp_f32 v39, v39\n"
        "v_mfma_f32_16x16x32_bf16 v[204:207], a[156:159], v[84:87], v[204:207]\n"
        "ds_read_b128 a[152:155], v241 offset:2048\n"
        "v_cvt_pk_bf16_f32 v21, v38, v39\n"
        "v_mfma_f32_16x16x32_bf16 v[172:175], a[156:159], v[80:83], v[172:175]\n"
        "global_load_lds_dwordx4 v254, s[76:77] offset:1024 sc1\n"
        "ds_read_b128 a[120:123], v240 offset:14336\n"
        "v_mfma_f32_16x16x32_bf16 v[32:35], a[96:99], v[0:3], v[224:227]\n"
        "ds_read_b128 a[156:159], v241 offset:3072\n"
        "s_add_u32 s68, s64, s90\n"
        "s_addc_u32 s69, s65, 0\n"
        "v_mfma_f32_16x16x32_bf16 v[32:35], a[100:103], v[4:7], v[32:35]\n"
        "s_waitcnt lgkmcnt(5)\n"
        "v_exp_f32 v48, v48\n"
        "v_mfma_f32_16x16x32_bf16 v[32:35], a[104:107], v[8:11], v[32:35]\n"
        "v_exp_f32 v49, v49\n"
        "v_mfma_f32_16x16x32_bf16 v[32:35], a[108:111], v[12:15], v[32:35]\n"
        "global_load_lds_dwordx4 v254, s[76:77] offset:2048 sc1\n"
        "v_cvt_pk_bf16_f32 v18, v48, v49\n"
        "v_mfma_f32_16x16x32_bf16 v[36:39], a[108:111], v[76:79], v[228:231]\n"
        "ds_read_b128 a[124:127], v240 offset:15360\n"
        "s_add_u32 s74, s68, 0x1000\n"
        "s_addc_u32 s75, s69, 0\n"
        "v_mfma_f32_16x16x32_bf16 v[36:39], a[104:107], v[72:75], v[36:39]\n"
        "v_exp_f32 v50, v50\n"
        "v_mfma_f32_16x16x32_bf16 v[36:39], a[100:103], v[68:71], v[36:39]\n"
        "v_exp_f32 v51, v51\n"
        "v_mfma_f32_16x16x32_bf16 v[36:39], a[96:99], v[64:67], v[36:39]\n"
        "global_load_lds_dwordx4 v254, s[76:77] offset:3072 sc1\n"
        "s_add_u32 m0, s79, 0x1000\n"
        "v_cvt_pk_bf16_f32 v19, v50, v51\n"
        "v_mfma_f32_16x16x32_bf16 v[176:179], a[128:131], v[80:83], v[176:179]\n"
        "ds_read_b128 a[96:99], v240 offset:16384\n"
        "s_add_u32 s76, s76, 0x1000\n"
        "s_addc_u32 s77, s77, 0\n"
        "v_mfma_f32_16x16x32_bf16 v[208:211], a[128:131], v[84:87], v[208:211]\n"
        "v_exp_f32 v52, v52\n"
        "v_mfma_f32_16x16x32_bf16 v[212:215], a[132:135], v[84:87], v[212:215]\n"
        "v_exp_f32 v53, v53\n"
        "v_mfma_f32_16x16x32_bf16 v[180:183], a[132:135], v[80:83], v[180:183]\n"
        "ds_read_b128 a[128:131], v241 offset:4096\n"
        "v_cvt_pk_bf16_f32 v22, v52, v53\n"
        "v_mfma_f32_16x16x32_bf16 v[184:187], a[136:139], v[80:83], v[184:187]\n"
        "global_load_lds_dwordx4 v254, s[76:77] offset:0 sc1\n"
        "ds_read_b128 a[100:103], v240 offset:17408\n"
        "v_mfma_f32_16x16x32_bf16 v[216:219], a[136:139], v[84:87], v[216:219]\n"
        "ds_read_b128 a[132:135], v241 offset:5120\n"
        "s_waitcnt lgkmcnt(4)\n"
        "v_mfma_f32_16x16x32_bf16 v[220:223], a[140:143], v[84:87], v[220:223]\n"
        "v_exp_f32 v54, v54\n"
        "v_mfma_f32_16x16x32_bf16 v[188:191], a[140:143], v[80:83], v[188:191]\n"
        "v_exp_f32 v55, v55\n"
        "v_mfma_f32_16x16x32_bf16 v[48:51], a[112:115], v[0:3], v[224:227]\n"
        "ds_read_b128 a[136:139], v241 offset:6144\n"
        "v_cvt_pk_bf16_f32 v23, v54, v55\n"
        "v_mfma_f32_16x16x32_bf16 v[48:51], a[116:119], v[4:7], v[48:51]\n"
        "global_load_lds_dwordx4 v254, s[76:77] offset:1024 sc1\n"
        "ds_read_b128 a[104:107], v240 offset:18432\n"
        "v_mfma_f32_16x16x32_bf16 v[48:51], a[120:123], v[8:11], v[48:51]\n"
        "ds_read_b128 a[140:143], v241 offset:7168\n"
        "v_mfma_f32_16x16x32_bf16 v[48:51], a[124:127], v[12:15], v[48:51]\n"
        "v_exp_f32 v32, v32\n"
        "v_mfma_f32_16x16x32_bf16 v[52:55], a[124:127], v[76:79], v[228:231]\n"
        "v_exp_f32 v33, v33\n"
        "v_mfma_f32_16x16x32_bf16 v[52:55], a[120:123], v[72:75], v[52:55]\n"
        "global_load_lds_dwordx4 v254, s[76:77] offset:2048 sc1\n"
        "v_cvt_pk_bf16_f32 v80, v32, v33\n"
        "v_mfma_f32_16x16x32_bf16 v[52:55], a[116:119], v[68:71], v[52:55]\n"
        "ds_read_b128 a[108:111], v240 offset:19456\n"
        "v_mfma_f32_16x16x32_bf16 v[52:55], a[112:115], v[64:67], v[52:55]\n"
        "v_exp_f32 v34, v34\n"
        "v_mfma_f32_16x16x32_bf16 a[64:67], a[80:83], v[16:19], a[64:67]\n"
        "v_exp_f32 v35, v35\n"
        "v_mfma_f32_16x16x32_bf16 a[68:71], a[80:83], v[20:23], a[68:71]\n"
        "global_load_lds_dwordx4 v254, s[76:77] offset:3072 sc1\n"
        "v_cvt_pk_bf16_f32 v81, v34, v35\n"
        "v_mfma_f32_16x16x32_bf16 v[160:163], a[144:147], v[16:19], v[160:163]\n"
        "ds_read_b128 a[112:115], v240 offset:20480\n"
        "v_mfma_f32_16x16x32_bf16 v[192:195], a[144:147], v[20:23], v[192:195]\n"
        "v_exp_f32 v36, v36\n"
        "v_mfma_f32_16x16x32_bf16 v[196:199], a[148:151], v[20:23], v[196:199]\n"
        "s_waitcnt lgkmcnt(2)\n"
        "v_exp_f32 v37, v37\n"
        "v_mfma_f32_16x16x32_bf16 v[164:167], a[148:151], v[16:19], v[164:167]\n"
        "ds_read_b128 a[144:147], v241 offset:8192\n"
        "v_cvt_pk_bf16_f32 v84, v36, v37\n"
        "v_mfma_f32_16x16x32_bf16 v[168:171], a[152:155], v[16:19], v[168:171]\n"
        "ds_read_b128 a[116:119], v240 offset:21504\n"
        "v_mfma_f32_16x16x32_bf16 v[200:203], a[152:155], v[20:23], v[200:203]\n"
        "ds_read_b128 a[148:151], v241 offset:9216\n"
        "v_mfma_f32_16x16x32_bf16 v[204:207], a[156:159], v[20:23], v[204:207]\n"
        "v_exp_f32 v38, v38\n"
        "v_mfma_f32_16x16x32_bf16 v[172:175], a[156:159], v[16:19], v[172:175]\n"
        "v_exp_f32 v39, v39\n"
        "v_mfma_f32_16x16x32_bf16 v[32:35], a[96:99], v[0:3], v[224:227]\n"
        "ds_read_b128 a[152:155], v241 offset:10240\n"
        "v_cvt_pk_bf16_f32 v85, v38, v39\n"
        "v_mfma_f32_16x16x32_bf16 v[32:35], a[100:103], v[4:7], v[32:35]\n"
        "ds_read_b128 a[120:123], v240 offset:22528\n"
        "s_waitcnt lgkmcnt(5)\n"
        "v_mfma_f32_16x16x32_bf16 v[32:35], a[104:107], v[8:11], v[32:35]\n"
        "ds_read_b128 a[156:159], v241 offset:11264\n"
        "v_mfma_f32_16x16x32_bf16 v[32:35], a[108:111], v[12:15], v[32:35]\n"
        "v_exp_f32 v48, v48\n"
        "v_mfma_f32_16x16x32_bf16 v[36:39], a[108:111], v[76:79], v[228:231]\n"
        "v_exp_f32 v49, v49\n"
        "v_mfma_f32_16x16x32_bf16 v[36:39], a[104:107], v[72:75], v[36:39]\n"
        "ds_read_b128 a[124:127], v240 offset:23552\n"
        "v_cvt_pk_bf16_f32 v82, v48, v49\n"
        "v_mfma_f32_16x16x32_bf16 v[36:39], a[100:103], v[68:71], v[36:39]\n"
        "v_exp_f32 v50, v50\n"
        "v_mfma_f32_16x16x32_bf16 v[36:39], a[96:99], v[64:67], v[36:39]\n"
        "v_exp_f32 v51, v51\n"
        "v_mfma_f32_16x16x32_bf16 v[176:179], a[128:131], v[16:19], v[176:179]\n"
        "ds_read_b128 a[96:99], v240 offset:24576\n"
        "v_cvt_pk_bf16_f32 v83, v50, v51\n"
        "v_mfma_f32_16x16x32_bf16 v[208:211], a[128:131], v[20:23], v[208:211]\n"
        "v_exp_f32 v52, v52\n"
        "v_mfma_f32_16x16x32_bf16 v[212:215], a[132:135], v[20:23], v[212:215]\n"
        "v_exp_f32 v53, v53\n"
        "v_mfma_f32_16x16x32_bf16 v[180:183], a[132:135], v[16:19], v[180:183]\n"
        "ds_read_b128 a[128:131], v241 offset:12288\n"
        "v_cvt_pk_bf16_f32 v86, v52, v53\n"
        "v_mfma_f32_16x16x32_bf16 v[184:187], a[136:139], v[16:19], v[184:187]\n"
        "ds_read_b128 a[100:103], v240 offset:25600\n"
        "s_waitcnt lgkmcnt(4)\n"
        "v_mfma_f32_16x16x32_bf16 v[216:219], a[136:139], v[20:23], v[216:219]\n"
        "ds_read_b128 a[132:135], v241 offset:13312\n"
        "v_mfma_f32_16x16x32_bf16 v[220:223], a[140:143], v[20:23], v[220:223]\n"
        "v_exp_f32 v54, v54\n"
        "v_mfma_f32_16x16x32_bf16 v[188:191], a[140:143], v[16:19], v[188:191]\n"
        "v_exp_f32 v55, v55\n"
        "v_mfma_f32_16x16x32_bf16 v[48:51], a[112:115], v[0:3], v[224:227]\n"
        "ds_read_b128 a[136:139], v241 offset:14336\n"
        "v_cvt_pk_bf16_f32 v87, v54, v55\n"
        "v_mfma_f32_16x16x32_bf16 v[48:51], a[116:119], v[4:7], v[48:51]\n"
        "ds_read_b128 a[104:107], v240 offset:26624\n"
        "global_load_dword v244, v245, s[26:27] offset:0\n"
        "v_mfma_f32_16x16x32_bf16 v[48:51], a[120:123], v[8:11], v[48:51]\n"
        "s_sub_u32 s26, s26, 262144\n"
        "s_subb_u32 s27, s27, 0\n"
        "s_waitcnt lgkmcnt(5)\n"
        "v_mfma_f32_16x16x32_bf16 v[48:51], a[124:127], v[12:15], v[48:51]\n"
        "ds_read_b128 a[140:143], v241 offset:15360\n"
        "v_mfma_f32_16x16x32_bf16 v[52:55], a[124:127], v[76:79], v[228:231]\n"
        "v_exp_f32 v32, v32\n"
        "v_mfma_f32_16x16x32_bf16 v[52:55], a[120:123], v[72:75], v[52:55]\n"
        "v_exp_f32 v33, v33\n"
        "v_mfma_f32_16x16x32_bf16 v[52:55], a[116:119], v[68:71], v[52:55]\n"
        "ds_read_b128 a[108:111], v240 offset:27648\n"
        "v_cvt_pk_bf16_f32 v16, v32, v33\n"
        "v_mfma_f32_16x16x32_bf16 v[52:55], a[112:115], v[64:67], v[52:55]\n"
        "v_exp_f32 v34, v34\n"
        "v_mfma_f32_16x16x32_bf16 a[64:67], a[80:83], v[80:83], a[64:67]\n"
        "v_exp_f32 v35, v35\n"
        "v_mfma_f32_16x16x32_bf16 a[68:71], a[80:83], v[84:87], a[68:71]\n"
        "ds_read_b128 a[112:115], v240 offset:28672\n"
        "v_cvt_pk_bf16_f32 v17, v34, v35\n"
        "v_mfma_f32_16x16x32_bf16 v[160:163], a[144:147], v[80:83], v[160:163]\n"
        "v_exp_f32 v36, v36\n"
        "v_mfma_f32_16x16x32_bf16 v[192:195], a[144:147], v[84:87], v[192:195]\n"
        "v_exp_f32 v37, v37\n"
        "v_mfma_f32_16x16x32_bf16 v[196:199], a[148:151], v[84:87], v[196:199]\n"
        "ds_read_b128 a[144:147], v241 offset:16384\n"
        "v_cvt_pk_bf16_f32 v20, v36, v37\n"
        "v_mfma_f32_16x16x32_bf16 v[164:167], a[148:151], v[80:83], v[164:167]\n"
        "ds_read_b128 a[116:119], v240 offset:29696\n"
        "s_waitcnt lgkmcnt(4)\n"
        "v_mfma_f32_16x16x32_bf16 v[168:171], a[152:155], v[80:83], v[168:171]\n"
        "ds_read_b128 a[148:151], v241 offset:17408\n"
        "v_mfma_f32_16x16x32_bf16 v[200:203], a[152:155], v[84:87], v[200:203]\n"
        "v_exp_f32 v38, v38\n"
        "v_mfma_f32_16x16x32_bf16 v[204:207], a[156:159], v[84:87], v[204:207]\n"
        "v_exp_f32 v39, v39\n"
        "v_mfma_f32_16x16x32_bf16 v[172:175], a[156:159], v[80:83], v[172:175]\n"
        "ds_read_b128 a[152:155], v241 offset:18432\n"
        "v_cvt_pk_bf16_f32 v21, v38, v39\n"
        "v_mfma_f32_16x16x32_bf16 v[32:35], a[96:99], v[0:3], v[224:227]\n"
        "ds_read_b128 a[124:127], v240 offset:31744\n"
        "s_waitcnt lgkmcnt(5)\n"
        "v_mfma_f32_16x16x32_bf16 v[32:35], a[100:103], v[4:7], v[32:35]\n"
        "ds_read_b128 a[120:123], v240 offset:30720\n"
        "v_xor_b32 v240, 0x8000, v240\n"
        "v_mfma_f32_16x16x32_bf16 v[32:35], a[104:107], v[8:11], v[32:35]\n"
        "ds_read_b128 a[156:159], v241 offset:19456\n"
        "v_mfma_f32_16x16x32_bf16 v[32:35], a[108:111], v[12:15], v[32:35]\n"
        "v_exp_f32 v48, v48\n"
        "v_mfma_f32_16x16x32_bf16 v[36:39], a[108:111], v[76:79], v[228:231]\n"
        "v_exp_f32 v49, v49\n"
        "v_mfma_f32_16x16x32_bf16 v[36:39], a[104:107], v[72:75], v[36:39]\n"
        "v_cvt_pk_bf16_f32 v18, v48, v49\n"
        "v_mfma_f32_16x16x32_bf16 v[36:39], a[100:103], v[68:71], v[36:39]\n"
        "v_exp_f32 v50, v50\n"
        "v_mfma_f32_16x16x32_bf16 v[36:39], a[96:99], v[64:67], v[36:39]\n"
        "v_exp_f32 v51, v51\n"
        "v_mfma_f32_16x16x32_bf16 v[176:179], a[128:131], v[80:83], v[176:179]\n"
        "v_cvt_pk_bf16_f32 v19, v50, v51\n"
        "v_mfma_f32_16x16x32_bf16 v[208:211], a[128:131], v[84:87], v[208:211]\n"
        "s_waitcnt vmcnt(9) lgkmcnt(1)\n"
        "s_barrier\n"
        "v_mfma_f32_16x16x32_bf16 v[212:215], a[132:135], v[84:87], v[212:215]\n"
        "s_cmp_eq_u32 s82, s10\n"
        "s_cbranch_scc1 LEVS_%=\n"
        "LEWS_%=:\n"
        "s_add_u32 m0, s80, 0x0\n"
        "v_mfma_f32_16x16x32_bf16 v[180:183], a[132:135], v[80:83], v[180:183]\n"
        "global_load_lds_dwordx4 v254, s[68:69] offset:0 sc1\n"
        "ds_read_b128 a[96:99], v240 offset:0\n"
        "v_mfma_f32_16x16x32_bf16 v[184:187], a[136:139], v[80:83], v[184:187]\n"
        "v_exp_f32 v52, v52\n"
        "v_mfma_f32_16x16x32_bf16 v[216:219], a[136:139], v[84:87], v[216:219]\n"
        "v_exp_f32 v53, v53\n"
        "v_mfma_f32_16x16x32_bf16 v[220:223], a[140:143], v[84:87], v[220:223]\n"
        "ds_read_b128 a[128:131], v241 offset:20480\n"
        "v_cvt_pk_bf16_f32 v22, v52, v53\n"
        "v_mfma_f32_16x16x32_bf16 v[188:191], a[140:143], v[80:83], v[188:191]\n"
        "ds_read_b128 a[100:103], v240 offset:1024\n"
        "global_load_lds_dwordx4 v254, s[68:69] offset:1024 sc1\n"
        "v_mfma_f32_16x16x32_bf16 v[48:51], a[112:115], v[0:3], v[224:227]\n"
        "ds_read_b128 a[132:135], v241 offset:21504\n"
        "s_waitcnt lgkmcnt(4)\n"
        "v_mfma_f32_16x16x32_bf16 v[48:51], a[116:119], v[4:7], v[48:51]\n"
        "v_exp_f32 v54, v54\n"
        "v_mfma_f32_16x16x32_bf16 v[48:51], a[120:123], v[8:11], v[48:51]\n"
        "v_exp_f32 v55, v55\n"
        "v_mfma_f32_16x16x32_bf16 v[48:51], a[124:127], v[12:15], v[48:51]\n"
        "ds_read_b128 a[136:139], v241 offset:22528\n"
        "v_cvt_pk_bf16_f32 v23, v54, v55\n"
        "v_mfma_f32_16x16x32_bf16 v[52:55], a[124:127], v[76:79], v[228:231]\n"
        "ds_read_b128 a[104:107], v240 offset:2048\n"
        "global_load_lds_dwordx4 v254, s[68:69] offset:2048 sc1\n"
        "v_mfma_f32_16x16x32_bf16 v[52:55], a[120:123], v[72:75], v[52:55]\n"
        "ds_read_b128 a[140:143], v241 offset:23552\n"
        "v_mfma_f32_16x16x32_bf16 v[52:55], a[116:119], v[68:71], v[52:55]\n"
        "s_waitcnt lgkmcnt(5)\n"
        "v_exp_f32 v32, v32\n"
        "v_mfma_f32_16x16x32_bf16 v[52:55], a[112:115], v[64:67], v[52:55]\n"
        "v_exp_f32 v33, v33\n"
        "v_mfma_f32_16x16x32_bf16 a[64:67], a[80:83], v[16:19], a[64:67]\n"
        "ds_read_b128 a[108:111], v240 offset:3072\n"
        "v_cvt_pk_bf16_f32 v80, v32, v33\n"
        "v_mfma_f32_16x16x32_bf16 a[68:71], a[80:83], v[20:23], a[68:71]\n"
        "global_load_lds_dwordx4 v254, s[68:69] offset:3072 sc1\n"
        "ds_read_b128 a[112:115], v240 offset:4096\n"
        "v_mfma_f32_16x16x32_bf16 v[160:163], a[144:147], v[16:19], v[160:163]\n"
        "v_exp_f32 v34, v34\n"
        "v_mfma_f32_16x16x32_bf16 v[192:195], a[144:147], v[20:23], v[192:195]\n"
        "v_exp_f32 v35, v35\n"
        "v_mfma_f32_16x16x32_bf16 v[196:199], a[148:151], v[20:23], v[196:199]\n"
        "ds_read_b128 a[144:147], v241 offset:24576\n"
        "v_cvt_pk_bf16_f32 v81, v34, v35\n"
        "v_mfma_f32_16x16x32_bf16 v[164:167], a[148:151], v[16:19], v[164:167]\n"
        "v_exp_f32 v36, v36\n"
        "v_mfma_f32_16x16x32_bf16 v[168:171], a[152:155], v[16:19], v[168:171]\n"
        "v_exp_f32 v37, v37\n"
        "v_mfma_f32_16x16x32_bf16 v[200:203], a[152:155], v[20:23], v[200:203]\n"
        "ds_read_b128 a[116:119], v240 offset:5120\n"
        "v_cvt_pk_bf16_f32 v84, v36, v37\n"
        "v_mfma_f32_16x16x32_bf16 v[204:207], a[156:159], v[20:23], v[204:207]\n"
        "ds_read_b128 a[148:151], v241 offset:25600\n"
        "v_mfma_f32_16x16x32_bf16 v[172:175], a[156:159], v[16:19], v[172:175]\n"
        "v_exp_f32 v38, v38\n"
        "v_exp_f32 v39, v39\n"
        "ds_read_b128 a[152:155], v241 offset:26624\n"
        "v_cvt_pk_bf16_f32 v85, v38, v39\n"
        "ds_read_b128 a[120:123], v240 offset:6144\n"
        "s_cmp_lg_u32 s91, 0\n"
        "s_cbranch_scc1 LSI_%=\n"
        "s_branch LTL_%=\n"
        "LEVS_%=:\n"
        "s_lshl_b32 s97, s41, 12\n"
        "s_add_u32 s97, s97, 32768\n"
        "v_bfe_u32 v246, v254, 4, 5\n"
        "v_lshl_add_u32 v246, v246, 7, s97\n"
        "s_cmp_eq_u32 s82, s83\n"
        "s_cbranch_scc1 LE3S_%=\n"
        "global_load_dwordx2 v[252:253], v246, s[48:49] sc1\n"
        "s_mov_b32 s10, s83\n"
        "s_branch LEWS_%=\n"
        "LE3S_%=:\n"
        "s_mov_b32 s97, 0x200000\n"
        "LE4S_%=:\n"
        "v_cmp_ne_u64 vcc, v[252:253], v[250:251]\n"
        "s_nop 1\n"
        "s_cbranch_vccz LE5S_%=\n"
        "s_sleep 2\n"
        "global_load_dwordx2 v[252:253], v246, s[48:49] sc1\n"
        "s_waitcnt vmcnt(0)\n"
        "s_sub_u32 s97, s97, 1\n"
        "s_cmp_lg_u32 s97, 0\n"
        "s_cbranch_scc1 LE4S_%=\n"
        "LE5S_%=:\n"
        "s_mov_b64 s[68:69], s[92:93]\n"
        "s_mov_b64 s[74:75], s[94:95]\n"
        "s_mov_b32 s80, s50\n"
        "s_mov_b32 s81, s51\n"
        "s_branch LEWS_%=\n"
        "; ---- BACK\n"
        "LTL_%=:\n"
        "v_mfma_f32_16x16x32_bf16 v[176:179], a[128:131], v[16:19], v[176:179]\n"
        "s_lshl_b32 s53, s45, 20\n"
        "s_lshl_b32 s96, s43, 15\n"
        "s_add_u32 s53, s53, s96\n"
        "v_mfma_f32_16x16x32_bf16 v[208:211], a[128:131], v[20:23], v[208:211]\n"
        "s_lshl_b32 s96, s40, 10\n"
        "s_add_u32 s53, s53, s96\n"
        "s_waitcnt lgkmcnt(3)\n"
        "v_mfma_f32_16x16x32_bf16 v[212:215], a[132:135], v[20:23], v[212:215]\n"
        "s_add_u32 s53, s53, 0x2001000\n"
        "s_add_u32 s70, s38, s53\n"
        "s_addc_u32 s71, s39, 0\n"
        "v_mfma_f32_16x16x32_bf16 v[180:183], a[132:135], v[16:19], v[180:183]\n"
        "ds_read_b128 a[156:159], v241 offset:27648\n"
        "s_mul_i32 s14, s40, 4224\n"
        "s_add_u32 s15, s51, 4098\n"
        "v_mfma_f32_16x16x32_bf16 v[184:187], a[136:139], v[16:19], v[184:187]\n"
        "s_add_u32 s14, s14, 0x20240\n"
        "v_exp_f32 v48, v48\n"
        "v_mfma_f32_16x16x32_bf16 v[216:219], a[136:139], v[20:23], v[216:219]\n"
        "s_add_u32 s16, s51, 4096\n"
        "v_exp_f32 v49, v49\n"
        "v_mfma_f32_16x16x32_bf16 v[220:223], a[140:143], v[20:23], v[220:223]\n"
        "ds_read_b128 a[128:131], v241 offset:28672\n"
        "s_sub_u32 s15, s15, s14\n"
        "v_cvt_pk_bf16_f32 v82, v48, v49\n"
        "v_mfma_f32_16x16x32_bf16 v[188:191], a[140:143], v[16:19], v[188:191]\n"
        "v_exp_f32 v50, v50\n"
        "v_exp_f32 v51, v51\n"
        "ds_read_b128 a[132:135], v241 offset:29696\n"
        "v_cvt_pk_bf16_f32 v83, v50, v51\n"
        "s_nop 1\n"
        "v_mfma_f32_16x16x32_bf16 v[160:163], a[144:147], v[80:83], v[160:163]\n"
        "s_waitcnt lgkmcnt(4)\n"
        "v_exp_f32 v52, v52\n"
        "v_mfma_f32_16x16x32_bf16 a[64:67], a[80:83], v[80:83], a[64:67]\n"
        "s_waitcnt lgkmcnt(2)\n"
        "v_exp_f32 v53, v53\n"
        "v_mfma_f32_16x16x32_bf16 v[164:167], a[148:151], v[80:83], v[164:167]\n"
        "ds_read_b128 a[136:139], v241 offset:30720\n"
        "s_sub_u32 s56, s16, s15\n"
        "v_cvt_pk_bf16_f32 v86, v52, v53\n"
        "v_mfma_f32_16x16x32_bf16 v[168:171], a[152:155], v[80:83], v[168:171]\n"
        "ds_read_b128 a[140:143], v241 offset:31744\n"
        "s_add_u32 s56, s56, 2\n"
        "v_bfe_u32 v232, v254, 4, 4\n"
        "v_mfma_f32_16x16x32_bf16 v[172:175], a[156:159], v[80:83], v[172:175]\n"
        "s_waitcnt lgkmcnt(3)\n"
        "v_and_b32 v224, 1, v232\n"
        "v_add_u32 v229, s16, v254\n"
        "v_mfma_f32_16x16x32_bf16 v[176:179], a[128:131], v[80:83], v[176:179]\n"
        "s_lshl_b32 s57, s54, 16\n"
        "v_mul_lo_u32 v224, v224, s15\n"
        "v_writelane_b32 v229, s56, 2\n"
        "v_add_u32 v224, s14, v224\n"
        "v_lshrrev_b32 v233, 8, v254\n"
        "s_waitcnt lgkmcnt(2)\n"
        "v_mfma_f32_16x16x32_bf16 v[180:183], a[132:135], v[80:83], v[180:183]\n"
        "s_add_u32 s56, s56, 16\n"
        "v_mov_b32 v239, 0\n"
        "v_lshlrev_b32 v228, 5, v232\n"
        "v_lshl_add_u32 v224, v232, 1, v224\n"
        "s_add_u32 s14, s51, 480\n"
        "v_mov_b32 v236, 0\n"
        "v_mov_b32 v237, 0\n"
        "v_mov_b32 v238, 0\n"
        "v_writelane_b32 v229, s56, 3\n"
        "v_writelane_b32 v239, s57, 3\n"
        "v_lshl_add_u32 v224, v233, 4, v224\n"
        "v_sub_u32 v228, s14, v228\n"
        "s_waitcnt lgkmcnt(1)\n"
        "v_mfma_f32_16x16x32_bf16 v[184:187], a[136:139], v[80:83], v[184:187]\n"
        "s_and_b32 s54, s54, 0xffff0000\n"
        "v_add_u32 v225, 1024, v224\n"
        "s_waitcnt lgkmcnt(0)\n"
        "v_mfma_f32_16x16x32_bf16 v[188:191], a[140:143], v[80:83], v[188:191]\n"
        "s_mov_b64 exec, 15\n"
        "ds_write_b128 v229, v[236:239]\n"
        "s_mov_b64 exec, -1\n"
        "v_add_u32 v226, 2048, v224\n"
        "v_add_u32 v227, 3072, v224\n"
        "v_lshl_add_u32 v228, v233, 4, v228\n"
        "v_exp_f32 v54, v54\n"
        "v_exp_f32 v55, v55\n"
        "ds_read2_b32 a[0:1], v224 offset0:0 offset1:1\n"
        "v_cvt_pk_bf16_f32 v87, v54, v55\n"
        "s_nop 1\n"
        "v_mfma_f32_16x16x32_bf16 a[68:71], a[80:83], v[84:87], a[68:71]\n"
        "ds_read2_b32 a[2:3], v224 offset0:2 offset1:3\n"
        "s_and_b32 s10, s41, 1\n"
        "v_accvgpr_read_b32 v248, a64\n"
        "v_mfma_f32_16x16x32_bf16 v[192:195], a[144:147], v[84:87], v[192:195]\n"
        "s_and_b32 s11, s41, 4\n"
        "v_rcp_f32 v246, v248\n"
        "v_mfma_f32_16x16x32_bf16 v[196:199], a[148:151], v[84:87], v[196:199]\n"
        "ds_read2_b32 a[4:5], v224 offset0:128 offset1:129\n"
        "s_and_b32 s14, s42, 3\n"
        "v_add_u32 v230, 3584, v228\n"
        "v_mfma_f32_16x16x32_bf16 v[200:203], a[152:155], v[84:87], v[200:203]\n"
        "ds_read2_b32 a[6:7], v224 offset0:130 offset1:131\n"
        "s_add_u32 s11, s11, s14\n"
        "v_accvgpr_read_b32 v249, a68\n"
        "v_mfma_f32_16x16x32_bf16 v[204:207], a[156:159], v[84:87], v[204:207]\n"
        "s_and_b32 s12, s41, 2\n"
        "v_rcp_f32 v247, v249\n"
        "v_mfma_f32_16x16x32_bf16 v[208:211], a[128:131], v[84:87], v[208:211]\n"
        "ds_read_b128 a[196:199], v228 offset:3072\n"
        "s_lshr_b32 s14, s42, 2\n"
        "v_min_u32 v230, s16, v230\n"
        "v_mfma_f32_16x16x32_bf16 v[212:215], a[132:135], v[84:87], v[212:215]\n"
        "ds_read_b128 a[192:195], v230 offset:0\n"
        "s_lshl2_add_u32 s12, s12, s14\n"
        "v_mul_f32 v192, v192, v247\n"
        "v_mfma_f32_16x16x32_bf16 v[216:219], a[136:139], v[84:87], v[216:219]\n"
        "ds_read2_b32 a[8:9], v225 offset0:0 offset1:1\n"
        "s_lshl3_add_u32 s13, s12, s11\n"
        "v_mul_f32 v193, v193, v247\n"
        "v_mfma_f32_16x16x32_bf16 v[220:223], a[140:143], v[84:87], v[220:223]\n"
        "ds_read2_b32 a[10:11], v225 offset0:2 offset1:3\n"
        "ds_read_b128 a[200:203], v228 offset:2560\n"
        "ds_read2_b32 a[12:13], v225 offset0:128 offset1:129\n"
        "ds_read2_b32 a[14:15], v225 offset0:130 offset1:131\n"
        "ds_read_b128 a[204:207], v228 offset:2048\n"
        "ds_read2_b32 a[16:17], v226 offset0:0 offset1:1\n"
        "ds_read2_b32 a[18:19], v226 offset0:2 offset1:3\n"
        "ds_read_b128 a[208:211], v228 offset:1536\n"
        "ds_read2_b32 a[20:21], v226 offset0:128 offset1:129\n"
        "s_lshl1_add_u32 s13, s13, s10\n"
        "v_mul_f32 v194, v194, v247\n"
        "s_waitcnt lgkmcnt(11)\n"
        "v_mfma_f32_16x16x32_bf16 v[100:103], a[0:3], a[196:199], 0\n"
        "s_lshl_b32 s14, s10, 22\n"
        "v_mul_f32 v195, v195, v247\n"
        "s_waitcnt lgkmcnt(10)\n"
        "v_mfma_f32_16x16x32_bf16 v[96:99], a[0:3], a[192:195], 0\n"
        "ds_read2_b32 a[22:23], v226 offset0:130 offset1:131\n"
        "s_lshl_b32 s15, s40, 15\n"
        "v_mul_f32 v196, v196, v247\n"
        "v_mfma_f32_16x16x32_bf16 v[100:103], a[4:7], a[192:195], v[100:103]\n"
        "s_add_u32 s14, s14, s15\n"
        "v_mul_f32 v197, v197, v247\n"
        "s_waitcnt lgkmcnt(9)\n"
        "v_mfma_f32_16x16x32_bf16 v[104:107], a[8:11], a[192:195], 0\n"
        "ds_read_b128 a[212:215], v228 offset:1024\n"
        "s_lshl_b32 s15, s11, 8\n"
        "v_mul_f32 v198, v198, v247\n"
        "v_mfma_f32_16x16x32_bf16 v[104:107], a[4:7], a[196:199], v[104:107]\n"
        "s_waitcnt lgkmcnt(8)\n"
        "v_mul_f32 v199, v199, v247\n"
        "v_cvt_pk_bf16_f32 v192, v192, v193\n"
        "v_mfma_f32_16x16x32_bf16 v[104:107], a[0:3], a[200:203], v[104:107]\n"
        "s_waitcnt lgkmcnt(6)\n"
        "v_cvt_pk_bf16_f32 v193, v194, v195\n"
        "v_cvt_pk_bf16_f32 v194, v196, v197\n"
        "v_mfma_f32_16x16x32_bf16 v[108:111], a[0:3], a[204:207], 0\n"
        "s_waitcnt lgkmcnt(4)\n"
        "v_cvt_pk_bf16_f32 v195, v198, v199\n"
        "v_mul_f32 v160, v160, v246\n"
        "v_mfma_f32_16x16x32_bf16 v[108:111], a[4:7], a[200:203], v[108:111]\n"
        "global_store_dwordx4 v254, v[192:195], s[70:71] offset:-4096 sc1\n"
        "ds_read2_b32 a[24:25], v227 offset0:0 offset1:1\n"
        "ds_read2_b32 a[26:27], v227 offset0:2 offset1:3\n"
        "s_add_u32 s14, s14, s15\n"
        "v_mul_f32 v161, v161, v246\n"
        "v_mfma_f32_16x16x32_bf16 v[108:111], a[8:11], a[196:199], v[108:111]\n"
        "ds_read_b128 a[216:219], v228 offset:512\n"
        "s_lshl_b32 s15, s10, 19\n"
        "v_mul_f32 v162, v162, v246\n"
        "v_mfma_f32_16x16x32_bf16 v[108:111], a[12:15], a[192:195], v[108:111]\n"
        "s_waitcnt lgkmcnt(4)\n"
        "v_mul_f32 v163, v163, v246\n"
        "v_mul_f32 v164, v164, v246\n"
        "v_mfma_f32_16x16x32_bf16 v[128:131], a[16:19], a[192:195], 0\n"
        "s_waitcnt lgkmcnt(3)\n"
        "v_mul_f32 v165, v165, v246\n"
        "v_mul_f32 v166, v166, v246\n"
        "v_mfma_f32_16x16x32_bf16 v[128:131], a[12:15], a[196:199], v[128:131]\n"
        "ds_read2_b32 a[28:29], v227 offset0:128 offset1:129\n"
        "s_add_u32 m0, s73, 0x0\n"
        "v_mul_f32 v167, v167, v246\n"
        "v_mfma_f32_16x16x32_bf16 v[128:131], a[8:11], a[200:203], v[128:131]\n"
        "ds_read2_b32 a[30:31], v227 offset0:130 offset1:131\n"
        "s_lshl1_add_u32 s62, s12, s10\n"
        "v_cvt_pk_bf16_f32 v160, v160, v161\n"
        "v_mfma_f32_16x16x32_bf16 v[128:131], a[4:7], a[204:207], v[128:131]\n"
        "ds_read_b128 a[220:223], v228 offset:0\n"
        "s_lshl_b32 s62, s62, 6\n"
        "v_cvt_pk_bf16_f32 v161, v162, v163\n"
        "v_mfma_f32_16x16x32_bf16 v[128:131], a[0:3], a[208:211], v[128:131]\n"
        "ds_read2_b32 a[32:33], v224 offset0:16 offset1:17\n"
        "s_lshl2_add_u32 s63, s11, s40\n"
        "v_cvt_pk_bf16_f32 v162, v164, v165\n"
        "v_mfma_f32_16x16x32_bf16 v[132:135], a[0:3], a[212:215], 0\n"
        "s_waitcnt lgkmcnt(5)\n"
        "v_cvt_pk_bf16_f32 v163, v166, v167\n"
        "v_mul_f32 v200, v200, v247\n"
        "v_mfma_f32_16x16x32_bf16 v[132:135], a[4:7], a[208:211], v[132:135]\n"
        "global_store_dwordx4 v254, v[160:163], s[70:71] offset:0 sc1\n"
        "s_add_u32 s70, s70, 0x2000\n"
        "s_addc_u32 s71, s71, 0\n"
        "ds_read2_b32 a[34:35], v224 offset0:18 offset1:19\n"
        "s_lshl_b32 s63, s63, 5\n"
        "v_mul_f32 v201, v201, v247\n"
        "v_mfma_f32_16x16x32_bf16 v[132:135], a[8:11], a[204:207], v[132:135]\n"
        "ds_read2_b32 a[36:37], v224 offset0:144 offset1:145\n"
        "v_mul_f32 v202, v202, v247\n"
        "v_mfma_f32_16x16x32_bf16 v[132:135], a[12:15], a[200:203], v[132:135]\n"
        "ds_read2_b32 a[38:39], v224 offset0:146 offset1:147\n"
        "v_mul_f32 v203, v203, v247\n"
        "v_mfma_f32_16x16x32_bf16 v[132:135], a[16:19], a[196:199], v[132:135]\n"
        "s_waitcnt lgkmcnt(6)\n"
        "v_mul_f32 v204, v204, v247\n"
        "v_mul_f32 v205, v205, v247\n"
        "v_mfma_f32_16x16x32_bf16 v[132:135], a[20:23], a[192:195], v[132:135]\n"
        "s_waitcnt lgkmcnt(4)\n"
        "v_mul_f32 v206, v206, v247\n"
        "v_mul_f32 v207, v207, v247\n"
        "v_mfma_f32_16x16x32_bf16 v[136:139], a[24:27], a[192:195], 0\n"
        "ds_read_b128 a[228:231], v228 offset:3136\n"
        "v_cvt_pk_bf16_f32 v200, v200, v201\n"
        "v_mfma_f32_16x16x32_bf16 v[136:139], a[20:23], a[196:199], v[136:139]\n"
        "ds_read2_b32 a[40:41], v225 offset0:16 offset1:17\n"
        "v_cvt_pk_bf16_f32 v201, v202, v203\n"
        "v_mfma_f32_16x16x32_bf16 v[136:139], a[16:19], a[200:203], v[136:139]\n"
        "ds_read2_b32 a[42:43], v225 offset0:18 offset1:19\n"
        "v_cvt_pk_bf16_f32 v202, v204, v205\n"
        "v_mfma_f32_16x16x32_bf16 v[136:139], a[12:15], a[204:207], v[136:139]\n"
        "ds_read_b128 a[232:235], v228 offset:2624\n"
        "v_cvt_pk_bf16_f32 v203, v206, v207\n"
        "v_mfma_f32_16x16x32_bf16 v[136:139], a[8:11], a[208:211], v[136:139]\n"
        "global_store_dwordx4 v254, v[200:203], s[70:71] offset:-4096 sc1\n"
        "ds_read2_b32 a[44:45], v225 offset0:144 offset1:145\n"
        "ds_read2_b32 a[46:47], v225 offset0:146 offset1:147\n"
        "v_mul_f32 v168, v168, v246\n"
        "v_mfma_f32_16x16x32_bf16 v[136:139], a[4:7], a[212:215], v[136:139]\n"
        "ds_read_b128 a[236:239], v228 offset:2112\n"
        "v_mul_f32 v169, v169, v246\n"
        "v_mfma_f32_16x16x32_bf16 v[136:139], a[0:3], a[216:219], v[136:139]\n"
        "s_waitcnt lgkmcnt(7)\n"
        "v_mul_f32 v170, v170, v246\n"
        "v_mul_f32 v171, v171, v246\n"
        "v_mfma_f32_16x16x32_bf16 v[140:143], a[0:3], a[220:223], 0\n"
        "s_waitcnt lgkmcnt(6)\n"
        "v_mul_f32 v172, v172, v246\n"
        "v_mul_f32 v173, v173, v246\n"
        "v_mfma_f32_16x16x32_bf16 v[140:143], a[4:7], a[216:219], v[140:143]\n"
        "ds_read2_b32 a[48:49], v226 offset0:16 offset1:17\n"
        "v_mul_f32 v174, v174, v246\n"
        "v_mfma_f32_16x16x32_bf16 v[140:143], a[8:11], a[212:215], v[140:143]\n"
        "ds_read2_b32 a[50:51], v226 offset0:18 offset1:19\n"
        "v_mul_f32 v175, v175, v246\n"
        "v_mfma_f32_16x16x32_bf16 v[140:143], a[12:15], a[208:211], v[140:143]\n"
        "ds_read_b128 a[240:243], v228 offset:1600\n"
        "v_cvt_pk_bf16_f32 v168, v168, v169\n"
        "v_mfma_f32_16x16x32_bf16 v[140:143], a[16:19], a[204:207], v[140:143]\n"
        "ds_read2_b32 a[52:53], v226 offset0:144 offset1:145\n"
        "v_cvt_pk_bf16_f32 v169, v170, v171\n"
        "v_mfma_f32_16x16x32_bf16 v[140:143], a[20:23], a[200:203], v[140:143]\n"
        "s_waitcnt lgkmcnt(5)\n"
        "v_cvt_pk_bf16_f32 v170, v172, v173\n"
        "v_cvt_pk_bf16_f32 v171, v174, v175\n"
        "v_mfma_f32_16x16x32_bf16 v[140:143], a[24:27], a[196:199], v[140:143]\n"
        "global_store_dwordx4 v254, v[168:171], s[70:71] offset:0 sc1\n"
        "s_add_u32 s70, s70, 0x2000\n"
        "s_addc_u32 s71, s71, 0\n"
        "s_waitcnt lgkmcnt(4)\n"
        "v_mul_f32 v208, v208, v247\n"
        "v_mul_f32 v209, v209, v247\n"
        "v_mfma_f32_16x16x32_bf16 v[140:143], a[28:31], a[192:195], v[140:143]\n"
        "ds_read2_b32 a[54:55], v226 offset0:146 offset1:147\n"
        "LHB0_%=:\n"
        "s_cbranch_execz LHB4_%=\n"
        "v_mul_f32 v210, v210, v247\n"
        "v_mfma_f32_16x16x32_bf16 v[100:103], a[32:35], a[228:231], v[100:103]\n"
        "ds_read_b128 a[244:247], v228 offset:1088\n"
        "v_mul_f32 v211, v211, v247\n"
        "v_mfma_f32_16x16x32_bf16 v[108:111], a[32:35], a[236:239], v[108:111]\n"
        "ds_read2_b32 a[56:57], v227 offset0:16 offset1:17\n"
        "v_mul_f32 v212, v212, v247\n"
        "v_mfma_f32_16x16x32_bf16 v[108:111], a[36:39], a[232:235], v[108:111]\n"
        "ds_read2_b32 a[58:59], v227 offset0:18 offset1:19\n"
        "v_mul_f32 v213, v213, v247\n"
        "v_mfma_f32_16x16x32_bf16 v[108:111], a[40:43], a[228:231], v[108:111]\n"
        "ds_read_b128 a[248:251], v228 offset:576\n"
        "ds_read2_b32 a[60:61], v227 offset0:144 offset1:145\n"
        "ds_read2_b32 a[62:63], v227 offset0:146 offset1:147\n"
        "ds_read_b128 a[252:255], v228 offset:64\n"
        "ds_read2_b32 a[0:1], v224 offset0:32 offset1:33\n"
        "ds_read2_b32 a[2:3], v224 offset0:34 offset1:35\n"
        "ds_read2_b32 a[4:5], v224 offset0:160 offset1:161\n"
        "ds_read2_b32 a[6:7], v224 offset0:162 offset1:163\n"
        "ds_read_b128 a[196:199], v228 offset:3200\n"
        "v_mul_f32 v214, v214, v247\n"
        "s_waitcnt lgkmcnt(11)\n"
        "v_mfma_f32_16x16x32_bf16 v[132:135], a[32:35], a[244:247], v[132:135]\n"
        "ds_read2_b32 a[8:9], v225 offset0:32 offset1:33\n"
        "v_mul_f32 v215, v215, v247\n"
        "v_mfma_f32_16x16x32_bf16 v[132:135], a[36:39], a[240:243], v[132:135]\n"
        "ds_read2_b32 a[10:11], v225 offset0:34 offset1:35\n"
        "v_cvt_pk_bf16_f32 v208, v208, v209\n"
        "v_mfma_f32_16x16x32_bf16 v[132:135], a[40:43], a[236:239], v[132:135]\n"
        "ds_read_b128 a[200:203], v228 offset:2688\n"
        "v_cvt_pk_bf16_f32 v209, v210, v211\n"
        "v_mfma_f32_16x16x32_bf16 v[132:135], a[44:47], a[232:235], v[132:135]\n"
        "s_waitcnt lgkmcnt(11)\n"
        "v_cvt_pk_bf16_f32 v210, v212, v213\n"
        "v_cvt_pk_bf16_f32 v211, v214, v215\n"
        "v_mfma_f32_16x16x32_bf16 v[132:135], a[48:51], a[228:231], v[132:135]\n"
        "s_waitcnt lgkmcnt(8)\n"
        "v_mul_f32 v176, v176, v246\n"
        "v_mul_f32 v177, v177, v246\n"
        "global_store_dwordx4 v254, v[208:211], s[70:71] offset:-4096 sc1\n"
        "ds_read2_b32 a[12:13], v225 offset0:160 offset1:161\n"
        "ds_read2_b32 a[14:15], v225 offset0:162 offset1:163\n"
        "v_mul_f32 v178, v178, v246\n"
        "v_mul_f32 v179, v179, v246\n"
        "v_mul_f32 v180, v180, v246\n"
        "v_mul_f32 v181, v181, v246\n"
        "v_mul_f32 v182, v182, v246\n"
        "v_mul_f32 v183, v183, v246\n"
        "v_cvt_pk_bf16_f32 v176, v176, v177\n"
        "v_cvt_pk_bf16_f32 v177, v178, v179\n"
        "v_cvt_pk_bf16_f32 v178, v180, v181\n"
        "v_cvt_pk_bf16_f32 v179, v182, v183\n"
        "global_store_dwordx4 v254, v[176:179], s[70:71] offset:0 sc1\n"
        "s_add_u32 s70, s70, 0x2000\n"
        "s_addc_u32 s71, s71, 0\n"
        "ds_read_b128 a[204:207], v228 offset:2176\n"
        "v_mul_f32 v216, v216, v247\n"
        "v_mul_f32 v217, v217, v247\n"
        "v_mul_f32 v218, v218, v247\n"
        "v_mul_f32 v219, v219, v247\n"
        "v_mul_f32 v220, v220, v247\n"
        "v_mul_f32 v221, v221, v247\n"
        "v_mul_f32 v222, v222, v247\n"
        "v_mul_f32 v223, v223, v247\n"
        "v_cvt_pk_bf16_f32 v216, v216, v217\n"
        "v_cvt_pk_bf16_f32 v217, v218, v219\n"
        "v_cvt_pk_bf16_f32 v218, v220, v221\n"
        "v_cvt_pk_bf16_f32 v219, v222, v223\n"
        "global_store_dwordx4 v254, v[216:219], s[70:71] offset:-4096 sc1\n"
        "ds_read2_b32 a[16:17], v226 offset0:32 offset1:33\n"
        "ds_read2_b32 a[18:19], v226 offset0:34 offset1:35\n"
        "v_mul_f32 v184, v184, v246\n"
        "v_mul_f32 v185, v185, v246\n"
        "v_mul_f32 v186, v186, v246\n"
        "v_mul_f32 v187, v187, v246\n"
        "v_mul_f32 v188, v188, v246\n"
        "v_mul_f32 v189, v189, v246\n"
        "v_mul_f32 v190, v190, v246\n"
        "v_mul_f32 v191, v191, v246\n"
        "v_cvt_pk_bf16_f32 v184, v184, v185\n"
        "v_cvt_pk_bf16_f32 v185, v186, v187\n"
        "v_cvt_pk_bf16_f32 v186, v188, v189\n"
        "v_cvt_pk_bf16_f32 v187, v190, v191\n"
        "global_store_dwordx4 v254, v[184:187], s[70:71] offset:0 sc1\n"
        "ds_read_b128 a[208:211], v228 offset:1664\n"
        "ds_read2_b32 a[20:21], v226 offset0:160 offset1:161\n"
        "v_add_f32 v247, v248, v249\n"
        "v_cmp_nge_f32 vcc, 0x4e6e6b28, v247\n"
        "v_mfma_f32_16x16x32_bf16 v[140:143], a[32:35], a[252:255], v[140:143]\n"
        "s_cbranch_vccnz LRH_%=\n"
        "LSH_%=:\n"
        "v_add_u32 v231, 3648, v228\n"
        "v_min_u32 v231, s16, v231\n"
        "v_mfma_f32_16x16x32_bf16 v[140:143], a[36:39], a[248:251], v[140:143]\n"
        "ds_read_b128 a[224:227], v231 offset:0\n"
        "s_add_u32 s22, s18, s14\n"
        "s_addc_u32 s23, s19, 0\n"
        "v_mfma_f32_16x16x32_bf16 v[140:143], a[40:43], a[244:247], v[140:143]\n"
        "s_sub_u32 s22, s22, 4096\n"
        "s_subb_u32 s23, s23, 0\n"
        "s_lshl_b32 s14, s12, 20\n"
        "v_mfma_f32_16x16x32_bf16 v[140:143], a[44:47], a[240:243], v[140:143]\n"
        "s_add_u32 s14, s14, s15\n"
        "s_add_u32 s14, s14, s73\n"
        "s_add_u32 s14, s14, 0x2000000\n"
        "v_mfma_f32_16x16x32_bf16 v[140:143], a[48:51], a[236:239], v[140:143]\n"
        "s_add_u32 s20, s38, s14\n"
        "s_addc_u32 s21, s39, 0\n"
        "v_lshrrev_b32 v246, 7, v254\n"
        "v_mfma_f32_16x16x32_bf16 v[140:143], a[52:55], a[232:235], v[140:143]\n"
        "ds_read2_b32 a[22:23], v226 offset0:162 offset1:163\n"
        "s_lshl_b32 s14, s10, 12\n"
        "v_bfe_u32 v247, v254, 4, 3\n"
        "v_mfma_f32_16x16x32_bf16 v[140:143], a[56:59], a[228:231], v[140:143]\n"
        "ds_read_b128 a[212:215], v228 offset:1152\n"
        "ds_read2_b32 a[24:25], v227 offset0:32 offset1:33\n"
        "ds_read2_b32 a[26:27], v227 offset0:34 offset1:35\n"
        "ds_read_b128 a[216:219], v228 offset:640\n"
        "ds_read2_b32 a[28:29], v227 offset0:160 offset1:161\n"
        "v_and_b32 v248, 6, v246\n"
        "s_waitcnt lgkmcnt(6)\n"
        "v_mfma_f32_16x16x32_bf16 v[96:99], a[32:35], a[224:227], v[96:99]\n"
        "ds_read2_b32 a[30:31], v227 offset0:162 offset1:163\n"
        "v_xor_b32 v248, v248, v247\n"
        "v_mfma_f32_16x16x32_bf16 v[100:103], a[36:39], a[224:227], v[100:103]\n"
        "ds_read_b128 a[220:223], v228 offset:128\n"
        "v_lshlrev_b32 v248, 4, v248\n"
        "v_mfma_f32_16x16x32_bf16 v[104:107], a[40:43], a[224:227], v[104:107]\n"
        "s_waitcnt vmcnt(8)\n"
        "v_lshl_add_u32 v249, v246, 11, v248\n"
        "v_xor_b32 v246, 4, v246\n"
        "v_mfma_f32_16x16x32_bf16 v[104:107], a[36:39], a[228:231], v[104:107]\n"
        "s_barrier\n"
        "v_lshl_add_u32 v246, v246, 11, v248\n"
        "v_add_u32 v144, 4096, v249\n"
        "v_mfma_f32_16x16x32_bf16 v[104:107], a[32:35], a[232:235], v[104:107]\n"
        "s_add_u32 s60, s22, 0x0\n"
        "s_addc_u32 s61, s23, 0\n"
        "v_add_u32 v145, 3200, v249\n"
        "v_mfma_f32_16x16x32_bf16 v[108:111], a[44:47], a[224:227], v[108:111]\n"
        "global_load_lds_dwordx4 v144, s[60:61] offset:0\n"
        "v_add_u32 v146, 18432, v246\n"
        "v_mfma_f32_16x16x32_bf16 v[128:131], a[48:51], a[224:227], v[128:131]\n"
        "ds_read_b128 a[252:255], v228 offset:192\n"
        "v_add_u32 v147, 17536, v246\n"
        "v_mfma_f32_16x16x32_bf16 v[128:131], a[44:47], a[228:231], v[128:131]\n"
        "s_add_u32 s24, s94, 0x400000\n"
        "s_addc_u32 s25, s95, 0\n"
        "v_add_u32 v151, s14, v254\n"
        "v_mfma_f32_16x16x32_bf16 v[128:131], a[40:43], a[232:235], v[128:131]\n"
        "global_load_lds_dwordx4 v145, s[60:61] offset:1024\n"
        "s_lshr_b32 s14, s40, 1\n"
        "v_xor_b32 v148, 0x1000, v151\n"
        "v_mfma_f32_16x16x32_bf16 v[128:131], a[36:39], a[236:239], v[128:131]\n"
        "s_lshl_b32 s14, s14, 10\n"
        "v_lshrrev_b32 v246, 1, v233\n"
        "v_lshlrev_b32 v247, 11, v246\n"
        "v_mfma_f32_16x16x32_bf16 v[128:131], a[32:35], a[240:243], v[128:131]\n"
        "v_add_u32 v247, s14, v247\n"
        "v_bfe_u32 v248, v254, 6, 3\n"
        "v_mfma_f32_16x16x32_bf16 v[132:135], a[52:55], a[224:227], v[132:135]\n"
        "s_waitcnt lgkmcnt(1)\n"
        "v_bfe_u32 v249, v254, 4, 2\n"
        "v_lshlrev_b32 v240, 2, v246\n"
        "v_mfma_f32_16x16x32_bf16 v[136:139], a[56:59], a[224:227], v[136:139]\n"
        "global_load_lds_dwordx4 v146, s[60:61] offset:2048\n"
        "s_and_b32 s14, s40, 1\n"
        "v_xor_b32 v240, v240, v248\n"
        "v_mfma_f32_16x16x32_bf16 v[136:139], a[52:55], a[228:231], v[136:139]\n"
        "ds_read_b128 a[228:231], v228 offset:3264\n"
        "s_lshl_b32 s14, s14, 6\n"
        "v_bfe_u32 v241, v240, 1, 2\n"
        "v_mfma_f32_16x16x32_bf16 v[136:139], a[48:51], a[232:235], v[136:139]\n"
        "ds_read_b128 a[232:235], v228 offset:2752\n"
        "v_xor_b32 v241, 0, v241\n"
        "v_mfma_f32_16x16x32_bf16 v[136:139], a[44:47], a[236:239], v[136:139]\n"
        "global_load_lds_dwordx4 v147, s[60:61] offset:3072\n"
        "s_add_u32 m0, s73, 0x4000\n"
        "v_lshlrev_b32 v241, 5, v241\n"
        "v_mfma_f32_16x16x32_bf16 v[136:139], a[40:43], a[240:243], v[136:139]\n"
        "ds_read2_b32 a[40:41], v225 offset0:48 offset1:49\n"
        "v_lshl_add_u32 v240, v240, 7, v241\n"
        "v_mfma_f32_16x16x32_bf16 v[136:139], a[36:39], a[244:247], v[136:139]\n"
        "ds_read2_b32 a[36:37], v224 offset0:176 offset1:177\n"
        "v_lshl_add_u32 v240, v249, 3, v240\n"
        "v_mfma_f32_16x16x32_bf16 v[136:139], a[32:35], a[248:251], v[136:139]\n"
        "ds_read2_b32 a[32:33], v224 offset0:48 offset1:49\n"
        "v_add_u32 v154, v240, v247\n"
        "v_mfma_f32_16x16x32_bf16 v[140:143], a[60:63], a[224:227], v[140:143]\n"
        "ds_read2_b32 a[34:35], v224 offset0:50 offset1:51\n"
        "v_xor_b32 v154, s14, v154\n"
        "v_mfma_f32_16x16x32_bf16 v[100:103], a[0:3], a[196:199], v[100:103]\n"
        "ds_read2_b32 a[38:39], v224 offset0:178 offset1:179\n"
        "s_lshl_b32 s14, s44, 5\n"
        "v_xor_b32 v155, 32, v154\n"
        "v_mfma_f32_16x16x32_bf16 v[108:111], a[0:3], a[204:207], v[108:111]\n"
        "ds_read2_b32 a[42:43], v225 offset0:50 offset1:51\n"
        "s_add_u32 s14, s14, s43\n"
        "v_add_u32 v156, 0x10000, v154\n"
        "v_mfma_f32_16x16x32_bf16 v[108:111], a[4:7], a[200:203], v[108:111]\n"
        "ds_read2_b32 a[44:45], v225 offset0:176 offset1:177\n"
        "s_lshl2_add_u32 s14, s14, s40\n"
        "v_add_u32 v157, 0x10000, v155\n"
        "v_mfma_f32_16x16x32_bf16 v[108:111], a[8:11], a[196:199], v[108:111]\n"
        "ds_read2_b32 a[46:47], v225 offset0:178 offset1:179\n"
        "s_add_u32 s14, s14, 1024\n"
        "v_add_u32 v149, 0x10000, v148\n"
        "v_mfma_f32_16x16x32_bf16 v[132:135], a[0:3], a[212:215], v[132:135]\n"
        "ds_read_b128 a[236:239], v228 offset:2240\n"
        "s_lshl_b32 s14, s14, 7\n"
        "v_add_u32 v152, 0x10000, v151\n"
        "v_mfma_f32_16x16x32_bf16 v[132:135], a[4:7], a[208:211], v[132:135]\n"
        "ds_read2_b32 a[48:49], v226 offset0:48 offset1:49\n"
        "v_add_u32 v158, 0x20000, v154\n"
        "v_mfma_f32_16x16x32_bf16 v[132:135], a[8:11], a[204:207], v[132:135]\n"
        "ds_read2_b32 a[50:51], v226 offset0:50 offset1:51\n"
        "v_add_u32 v159, 0x20000, v155\n"
        "v_mfma_f32_16x16x32_bf16 v[132:135], a[12:15], a[200:203], v[132:135]\n"
        "ds_read_b128 a[240:243], v228 offset:1728\n"
        "v_add_u32 v150, 0x20000, v148\n"
        "v_mfma_f32_16x16x32_bf16 v[132:135], a[16:19], a[196:199], v[132:135]\n"
        "ds_read2_b32 a[52:53], v226 offset0:176 offset1:177\n"
        "v_add_u32 v153, 0x20000, v151\n"
        "v_mfma_f32_16x16x32_bf16 v[140:143], a[0:3], a[220:223], v[140:143]\n"
        "ds_read2_b32 a[54:55], v226 offset0:178 offset1:179\n"
        "v_add_u32 v230, 3712, v228\n"
        "v_mfma_f32_16x16x32_bf16 v[140:143], a[4:7], a[216:219], v[140:143]\n"
        "ds_read_b128 a[244:247], v228 offset:1216\n"
        "v_min_u32 v230, s16, v230\n"
        "v_mfma_f32_16x16x32_bf16 v[140:143], a[8:11], a[212:215], v[140:143]\n"
        "ds_read_b128 a[192:195], v230 offset:0\n"
        "s_add_u32 s60, s22, 0x40000\n"
        "s_addc_u32 s61, s23, 0\n"
        "v_mfma_f32_16x16x32_bf16 v[140:143], a[12:15], a[208:211], v[140:143]\n"
        "global_load_lds_dwordx4 v144, s[60:61] offset:0\n"
        "s_waitcnt vmcnt(5)\n"
        "v_mfma_f32_16x16x32_bf16 v[140:143], a[16:19], a[204:207], v[140:143]\n"
        "ds_read2_b32 a[56:57], v227 offset0:48 offset1:49\n"
        "v_lshrrev_b32 v234, 1, v254\n"
        "v_mfma_f32_16x16x32_bf16 v[140:143], a[20:23], a[200:203], v[140:143]\n"
        "ds_read2_b32 a[58:59], v227 offset0:50 offset1:51\n"
        "v_add_u32 v234, s14, v234\n"
        "v_mfma_f32_16x16x32_bf16 v[140:143], a[24:27], a[196:199], v[140:143]\n"
        "global_load_lds_dwordx4 v145, s[60:61] offset:1024\n"
        "global_load_lds_dwordx4 v146, s[60:61] offset:2048\n"
        "s_lshl_b32 s14, s45, 5\n"
        "s_add_u32 s14, s14, s43\n"
        "s_waitcnt lgkmcnt(2)\n"
        "v_mfma_f32_16x16x32_bf16 v[96:99], a[0:3], a[192:195], v[96:99]\n"
        "s_lshl2_add_u32 s14, s14, s40\n"
        "s_add_u32 s14, s14, 1024\n"
        "s_lshl_b32 s14, s14, 7\n"
        "v_mfma_f32_16x16x32_bf16 v[100:103], a[4:7], a[192:195], v[100:103]\n"
        "ds_read_b128 a[248:251], v228 offset:704\n"
        "v_add_u32 v231, 3776, v228\n"
        "v_mfma_f32_16x16x32_bf16 v[104:107], a[8:11], a[192:195], v[104:107]\n"
        "global_load_lds_dwordx4 v147, s[60:61] offset:3072\n"
        "s_add_u32 m0, s73, 0x8000\n"
        "v_min_u32 v231, s16, v231\n"
        "v_mfma_f32_16x16x32_bf16 v[104:107], a[4:7], a[196:199], v[104:107]\n"
        "s_add_u32 s60, s22, 0xa0000\n"
        "s_addc_u32 s61, s23, 0\n"
        "v_add_u32 v230, 3840, v228\n"
        "v_mfma_f32_16x16x32_bf16 v[104:107], a[0:3], a[200:203], v[104:107]\n"
        "ds_read_b128 a[224:227], v231 offset:0\n"
        "v_min_u32 v230, s16, v230\n"
        "v_mfma_f32_16x16x32_bf16 v[108:111], a[12:15], a[192:195], v[108:111]\n"
        "s_mov_b64 exec, 0xff\n"
        "global_store_dwordx2 v234, v[250:251], s[48:49] sc1\n"
        "s_mov_b64 exec, -1\n"
        "v_mfma_f32_16x16x32_bf16 v[128:131], a[16:19], a[192:195], v[128:131]\n"
        "global_load_lds_dwordx4 v144, s[60:61] offset:0\n"
        "v_lshrrev_b32 v234, 1, v254\n"
        "v_mfma_f32_16x16x32_bf16 v[128:131], a[12:15], a[196:199], v[128:131]\n"
        "ds_read2_b32 a[60:61], v227 offset0:176 offset1:177\n"
        "v_add_u32 v234, s14, v234\n"
        "v_mfma_f32_16x16x32_bf16 v[128:131], a[8:11], a[200:203], v[128:131]\n"
        "ds_read2_b32 a[62:63], v227 offset0:178 offset1:179\n"
        "s_lshl_b32 s14, s62, 11\n"
        "s_lshl1_add_u32 s14, s63, s14\n"
        "v_mfma_f32_16x16x32_bf16 v[128:131], a[4:7], a[204:207], v[128:131]\n"
        "s_mov_b64 exec, 0xff\n"
        "global_store_dwordx2 v234, v[250:251], s[48:49] sc1\n"
        "s_mov_b64 exec, -1\n"
        "v_mfma_f32_16x16x32_bf16 v[128:131], a[0:3], a[208:211], v[128:131]\n"
        "global_load_lds_dwordx4 v145, s[60:61] offset:1024\n"
        "s_add_u32 s64, s8, s14\n"
        "s_addc_u32 s65, s9, 0\n"
        "v_mfma_f32_16x16x32_bf16 v[132:135], a[20:23], a[192:195], v[132:135]\n"
        "s_add_u32 s56, s36, s14\n"
        "s_addc_u32 s57, s37, 0\n"
        "v_add_u32 v231, 3904, v228\n"
        "v_mfma_f32_16x16x32_bf16 v[136:139], a[24:27], a[192:195], v[136:139]\n"
        "ds_read2_b32 a[24:25], v227 offset0:64 offset1:65\n"
        "s_lshl1_add_u32 s14, s12, s10\n"
        "v_min_u32 v231, s16, v231\n"
        "v_mfma_f32_16x16x32_bf16 v[136:139], a[20:23], a[196:199], v[136:139]\n"
        "global_load_lds_dwordx4 v146, s[60:61] offset:2048\n"
        "ds_read_b128 a[196:199], v228 offset:3328\n"
        "s_lshl_b32 s14, s14, 6\n"
        "v_mfma_f32_16x16x32_bf16 v[136:139], a[16:19], a[200:203], v[136:139]\n"
        "ds_read_b128 a[200:203], v228 offset:2816\n"
        "s_add_u32 s14, s14, 1024\n"
        "v_lshlrev_b32 v238, 3, v254\n"
        "v_mfma_f32_16x16x32_bf16 v[136:139], a[12:15], a[204:207], v[136:139]\n"
        "ds_read2_b32 a[12:13], v225 offset0:192 offset1:193\n"
        "s_lshl_b32 s14, s14, 7\n"
        "v_add_u32 v238, s14, v238\n"
        "v_mfma_f32_16x16x32_bf16 v[136:139], a[8:11], a[208:211], v[136:139]\n"
        "global_load_lds_dwordx4 v147, s[60:61] offset:3072\n"
        "s_add_u32 m0, s73, 0xc000\n"
        "v_lshlrev_b32 v229, 5, v232\n"
        "v_mfma_f32_16x16x32_bf16 v[136:139], a[4:7], a[212:215], v[136:139]\n"
        "s_add_u32 s60, s22, 0x100000\n"
        "s_addc_u32 s61, s23, 0\n"
        "s_add_u32 s14, s51, 504\n"
        "v_mfma_f32_16x16x32_bf16 v[136:139], a[0:3], a[216:219], v[136:139]\n"
        "ds_read2_b32 a[0:1], v224 offset0:64 offset1:65\n"
        "v_lshl_add_u32 v229, v233, 3, v229\n"
        "v_mfma_f32_16x16x32_bf16 v[140:143], a[28:31], a[192:195], v[140:143]\n"
        "global_load_lds_dwordx4 v144, s[60:61] offset:0\n"
        "s_waitcnt lgkmcnt(5)\n"
        "v_mfma_f32_16x16x32_bf16 v[96:99], a[32:35], a[224:227], v[96:99]\n"
        "s_waitcnt vmcnt(7)\n"
        "s_barrier\n"
        "v_sub_u32 v229, s14, v229\n"
        "v_mfma_f32_16x16x32_bf16 v[100:103], a[32:35], a[228:231], v[100:103]\n"
        "ds_read_b64_tr_b16 v[160:161], v154 offset:0\n"
        "s_lshl_b32 s14, s41, 5\n"
        "s_add_u32 s14, s14, s42\n"
        "v_mfma_f32_16x16x32_bf16 v[100:103], a[36:39], a[224:227], v[100:103]\n"
        "global_load_lds_dwordx4 v145, s[60:61] offset:1024\n"
        "ds_read_b64_tr_b16 v[162:163], v154 offset:4096\n"
        "s_add_u32 s14, s14, 512\n"
        "v_mfma_f32_16x16x32_bf16 v[104:107], a[40:43], a[224:227], v[104:107]\n"
        "ds_read2_b32 a[2:3], v224 offset0:66 offset1:67\n"
        "s_lshl_b32 s14, s14, 7\n"
        "v_lshrrev_b32 v234, 1, v254\n"
        "v_mfma_f32_16x16x32_bf16 v[104:107], a[36:39], a[228:231], v[104:107]\n"
        "ds_read_b128 a[192:195], v230 offset:0\n"
        "v_add_u32 v230, 3968, v228\n"
        "v_mfma_f32_16x16x32_bf16 v[104:107], a[32:35], a[232:235], v[104:107]\n"
        "global_load_lds_dwordx4 v146, s[60:61] offset:2048\n"
        "ds_read_b64_tr_b16 v[164:165], v155 offset:0\n"
        "v_mfma_f32_16x16x32_bf16 v[108:111], a[32:35], a[236:239], v[108:111]\n"
        "ds_read_b64_tr_b16 v[166:167], v155 offset:4096\n"
        "v_min_u32 v230, s16, v230\n"
        "v_mfma_f32_16x16x32_bf16 v[108:111], a[36:39], a[232:235], v[108:111]\n"
        "ds_read2_b32 a[4:5], v224 offset0:192 offset1:193\n"
        "v_add_u32 v234, s14, v234\n"
        "v_mfma_f32_16x16x32_bf16 v[108:111], a[40:43], a[228:231], v[108:111]\n"
        "global_load_lds_dwordx4 v147, s[60:61] offset:3072\n"
        "s_add_u32 m0, s73, 0x10000\n"
        "s_lshr_b32 s14, s63, 2\n"
        "v_mfma_f32_16x16x32_bf16 v[108:111], a[44:47], a[224:227], v[108:111]\n"
        "s_add_u32 s60, s22, 0x140000\n"
        "s_addc_u32 s61, s23, 0\n"
        "s_add_u32 s14, s14, 512\n"
        "v_mfma_f32_16x16x32_bf16 v[128:131], a[48:51], a[224:227], v[128:131]\n"
        "ds_read2_b32 a[6:7], v224 offset0:194 offset1:195\n"
        "s_lshl_b32 s14, s14, 7\n"
        "v_and_b32 v246, 1, v232\n"
        "v_mfma_f32_16x16x32_bf16 v[128:131], a[44:47], a[228:231], v[128:131]\n"
        "global_load_lds_dwordx4 v144, s[60:61] offset:0\n"
        "ds_read_b64_tr_b16 v[168:169], v154 offset:8192\n"
        "v_mfma_f32_16x16x32_bf16 v[128:131], a[40:43], a[232:235], v[128:131]\n"
        "ds_read_b64_tr_b16 v[170:171], v154 offset:12288\n"
        "v_lshlrev_b32 v246, 4, v246\n"
        "v_mfma_f32_16x16x32_bf16 v[128:131], a[36:39], a[236:239], v[128:131]\n"
        "ds_read2_b32 a[8:9], v225 offset0:64 offset1:65\n"
        "v_mov_b32 v247, 0x3f80\n"
        "v_mfma_f32_16x16x32_bf16 v[128:131], a[32:35], a[240:243], v[128:131]\n"
        "global_load_lds_dwordx4 v145, s[60:61] offset:1024\n"
        "ds_read2_b32 a[10:11], v225 offset0:66 offset1:67\n"
        "v_mfma_f32_16x16x32_bf16 v[132:135], a[32:35], a[244:247], v[132:135]\n"
        "ds_read_b64_tr_b16 v[172:173], v155 offset:8192\n"
        "v_lshlrev_b32 v246, v246, v247\n"
        "v_mfma_f32_16x16x32_bf16 v[132:135], a[36:39], a[240:243], v[132:135]\n"
        "ds_read_b64_tr_b16 v[174:175], v155 offset:12288\n"
        "v_lshrrev_b32 v247, 1, v232\n"
        "v_mfma_f32_16x16x32_bf16 v[132:135], a[40:43], a[236:239], v[132:135]\n"
        "global_load_lds_dwordx4 v146, s[60:61] offset:2048\n"
        "ds_read2_b32 a[14:15], v225 offset0:194 offset1:195\n"
        "v_mfma_f32_16x16x32_bf16 v[132:135], a[44:47], a[232:235], v[132:135]\n"
        "ds_read_b128 a[204:207], v228 offset:2304\n"
        "v_lshlrev_b32 v248, 1, v233\n"
        "v_mfma_f32_16x16x32_bf16 v[132:135], a[48:51], a[228:231], v[132:135]\n"
        "ds_read_b64_tr_b16 v[176:177], v154 offset:16384\n"
        "v_or_b32 v249, 1, v248\n"
        "v_mfma_f32_16x16x32_bf16 v[132:135], a[52:55], a[224:227], v[132:135]\n"
        "global_load_lds_dwordx4 v147, s[60:61] offset:3072\n"
        "s_add_u32 m0, s73, 0x14000\n"
        "s_waitcnt lgkmcnt(7)\n"
        "v_mfma_f32_16x16x32_bf16 v[136:139], a[56:59], a[224:227], v[136:139]\n"
        "s_add_u32 s60, s22, 0x1a0000\n"
        "s_addc_u32 s61, s23, 0\n"
        "s_waitcnt lgkmcnt(6)\n"
        "v_mfma_f32_16x16x32_bf16 v[136:139], a[52:55], a[228:231], v[136:139]\n"
        "ds_read_b64_tr_b16 v[178:179], v154 offset:20480\n"
        "s_waitcnt lgkmcnt(6)\n"
        "v_mfma_f32_16x16x32_bf16 v[136:139], a[48:51], a[232:235], v[136:139]\n"
        "global_load_lds_dwordx4 v144, s[60:61] offset:0\n"
        "ds_read2_b32 a[16:17], v226 offset0:64 offset1:65\n"
        "v_mfma_f32_16x16x32_bf16 v[136:139], a[44:47], a[236:239], v[136:139]\n"
        "ds_read2_b32 a[18:19], v226 offset0:66 offset1:67\n"
        "v_mfma_f32_16x16x32_bf16 v[136:139], a[40:43], a[240:243], v[136:139]\n"
        "ds_read_b128 a[208:211], v228 offset:1792\n"
        "s_waitcnt lgkmcnt(6)\n"
        "v_mfma_f32_16x16x32_bf16 v[136:139], a[36:39], a[244:247], v[136:139]\n"
        "global_load_lds_dwordx4 v145, s[60:61] offset:1024\n"
        "ds_read_b64_tr_b16 v[180:181], v155 offset:16384\n"
        "v_mfma_f32_16x16x32_bf16 v[136:139], a[32:35], a[248:251], v[136:139]\n"
        "ds_read_b64_tr_b16 v[182:183], v155 offset:20480\n"
        "s_waitcnt lgkmcnt(6)\n"
        "v_mfma_f32_16x16x32_bf16 v[140:143], a[32:35], a[252:255], v[140:143]\n"
        "ds_read2_b32 a[20:21], v226 offset0:192 offset1:193\n"
        "v_mfma_f32_16x16x32_bf16 v[140:143], a[36:39], a[248:251], v[140:143]\n"
        "global_load_lds_dwordx4 v146, s[60:61] offset:2048\n"
        "ds_read2_b32 a[22:23], v226 offset0:194 offset1:195\n"
        "v_mfma_f32_16x16x32_bf16 v[140:143], a[40:43], a[244:247], v[140:143]\n"
        "ds_read_b128 a[212:215], v228 offset:1280\n"
        "v_mfma_f32_16x16x32_bf16 v[140:143], a[44:47], a[240:243], v[140:143]\n"
        "ds_read_b64_tr_b16 v[184:185], v154 offset:24576\n"
        "s_waitcnt lgkmcnt(8)\n"
        "v_mfma_f32_16x16x32_bf16 v[140:143], a[48:51], a[236:239], v[140:143]\n"
        "global_load_lds_dwordx4 v147, s[60:61] offset:3072\n"
        "ds_read_b64_tr_b16 v[186:187], v154 offset:28672\n"
        "s_add_u32 m0, s73, 0x0\n"
        "v_mfma_f32_16x16x32_bf16 v[140:143], a[52:55], a[232:235], v[140:143]\n"
        "ds_read_b64_tr_b16 v[188:189], v155 offset:24576\n"
        "s_add_u32 s60, s22, 0x200000\n"
        "s_addc_u32 s61, s23, 0\n"
        "v_mfma_f32_16x16x32_bf16 v[140:143], a[56:59], a[228:231], v[140:143]\n"
        "ds_read_b64_tr_b16 v[190:191], v155 offset:28672\n"
        "s_waitcnt lgkmcnt(8)\n"
        "v_mfma_f32_16x16x32_bf16 v[140:143], a[60:63], a[224:227], v[140:143]\n"
        "ds_read2_b32 a[26:27], v227 offset0:66 offset1:67\n"
        "v_mfma_f32_16x16x32_bf16 v[96:99], a[0:3], a[192:195], v[96:99]\n"
        "ds_read_b128 a[216:219], v228 offset:768\n"
        "v_mfma_f32_16x16x32_bf16 v[100:103], a[0:3], a[196:199], v[100:103]\n"
        "ds_read2_b32 a[28:29], v227 offset0:192 offset1:193\n"
        "s_waitcnt lgkmcnt(8)\n"
        "v_mfma_f32_16x16x32_bf16 v[100:103], a[4:7], a[192:195], v[100:103]\n"
        "ds_read2_b32 a[30:31], v227 offset0:194 offset1:195\n"
        "s_waitcnt lgkmcnt(8)\n"
        "v_mfma_f32_16x16x32_bf16 v[104:107], a[8:11], a[192:195], v[104:107]\n"
        "ds_read_b128 a[220:223], v228 offset:256\n"
        "v_mfma_f32_16x16x32_bf16 v[104:107], a[4:7], a[196:199], v[104:107]\n"
        "ds_read2_b32 a[32:33], v224 offset0:80 offset1:81\n"
        "v_mfma_f32_16x16x32_bf16 v[104:107], a[0:3], a[200:203], v[104:107]\n"
        "ds_read2_b32 a[34:35], v224 offset0:82 offset1:83\n"
        "v_mfma_f32_16x16x32_bf16 v[108:111], a[0:3], a[204:207], v[108:111]\n"
        "s_waitcnt vmcnt(8) lgkmcnt(7)\n"
        "s_barrier\n"
        "v_mfma_f32_16x16x32_bf16 v[108:111], a[4:7], a[200:203], v[108:111]\n"
        "global_load_lds_dwordx4 v144, s[60:61] offset:0\n"
        "ds_read_b64_tr_b16 v[192:193], v154 offset:32768\n"
        "v_mfma_f32_16x16x32_bf16 v[108:111], a[8:11], a[196:199], v[108:111]\n"
        "ds_read_b64_tr_b16 v[194:195], v154 offset:36864\n"
        "s_waitcnt lgkmcnt(7)\n"
        "v_mfma_f32_16x16x32_bf16 v[108:111], a[12:15], a[192:195], v[108:111]\n"
        "ds_read_b128 a[224:227], v231 offset:0\n"
        "v_add_u32 v231, 4032, v228\n"
        "v_mfma_f32_16x16x32_bf16 v[128:131], a[16:19], a[192:195], v[128:131]\n"
        "global_load_lds_dwordx4 v145, s[60:61] offset:1024\n"
        "ds_read_b64_tr_b16 v[196:197], v155 offset:32768\n"
        "v_mfma_f32_16x16x32_bf16 v[128:131], a[12:15], a[196:199], v[128:131]\n"
        "ds_read_b64_tr_b16 v[198:199], v155 offset:36864\n"
        "v_min_u32 v231, s16, v231\n"
        "v_mfma_f32_16x16x32_bf16 v[128:131], a[8:11], a[200:203], v[128:131]\n"
        "ds_read2_b32 a[36:37], v224 offset0:208 offset1:209\n"
        "s_lshl_b32 s16, s63, 12\n"
        "s_lshl1_add_u32 s16, s62, s16\n"
        "v_mfma_f32_16x16x32_bf16 v[128:131], a[4:7], a[204:207], v[128:131]\n"
        "global_load_lds_dwordx4 v146, s[60:61] offset:2048\n"
        "ds_read2_b32 a[38:39], v224 offset0:210 offset1:211\n"
        "s_add_u32 s16, s16, 0x3400000\n"
        "v_mfma_f32_16x16x32_bf16 v[128:131], a[0:3], a[208:211], v[128:131]\n"
        "ds_read_b128 a[228:231], v228 offset:3392\n"
        "s_add_u32 s58, s38, s16\n"
        "s_addc_u32 s59, s39, 0\n"
        "v_mfma_f32_16x16x32_bf16 v[132:135], a[0:3], a[212:215], v[132:135]\n"
        "ds_read_b64_tr_b16 v[200:201], v154 offset:40960\n"
        "s_lshl_b32 s16, s13, 14\n"
        "s_add_u32 s16, s16, s73\n"
        "v_mfma_f32_16x16x32_bf16 v[132:135], a[4:7], a[208:211], v[132:135]\n"
        "global_load_lds_dwordx4 v147, s[60:61] offset:3072\n"
        "ds_read_b64_tr_b16 v[202:203], v154 offset:45056\n"
        "s_add_u32 m0, s73, 0x4000\n"
        "v_mfma_f32_16x16x32_bf16 v[132:135], a[8:11], a[204:207], v[132:135]\n"
        "ds_read2_b32 a[40:41], v225 offset0:80 offset1:81\n"
        "s_add_u32 s60, s22, 0x240000\n"
        "s_addc_u32 s61, s23, 0\n"
        "v_mfma_f32_16x16x32_bf16 v[132:135], a[12:15], a[200:203], v[132:135]\n"
        "ds_read2_b32 a[42:43], v225 offset0:82 offset1:83\n"
        "s_add_u32 s16, s16, 0x4000000\n"
        "s_waitcnt lgkmcnt(8)\n"
        "v_mfma_f32_16x16x32_bf16 v[132:135], a[16:19], a[196:199], v[132:135]\n"
        "ds_read_b128 a[232:235], v228 offset:2880\n"
        "global_load_lds_dwordx4 v144, s[60:61] offset:0\n"
        "v_mfma_f32_16x16x32_bf16 v[132:135], a[20:23], a[192:195], v[132:135]\n"
        "ds_read_b64_tr_b16 v[204:205], v155 offset:40960\n"
        "s_add_u32 s28, s38, s16\n"
        "s_addc_u32 s29, s39, 0\n"
        "v_mfma_f32_16x16x32_bf16 v[136:139], a[24:27], a[192:195], v[136:139]\n"
        "ds_read_b64_tr_b16 v[206:207], v155 offset:45056\n"
        "s_xor_b32 s16, s16, 0x4000\n"
        "s_waitcnt lgkmcnt(8)\n"
        "v_mfma_f32_16x16x32_bf16 v[136:139], a[20:23], a[196:199], v[136:139]\n"
        "global_load_lds_dwordx4 v145, s[60:61] offset:1024\n"
        "v_mfma_f32_16x16x32_bf16 v[136:139], a[16:19], a[200:203], v[136:139]\n"
        "ds_read2_b32 a[44:45], v225 offset0:208 offset1:209\n"
        "s_add_u32 s30, s38, s16\n"
        "s_addc_u32 s31, s39, 0\n"
        "v_mfma_f32_16x16x32_bf16 v[136:139], a[12:15], a[204:207], v[136:139]\n"
        "ds_read2_b32 a[46:47], v225 offset0:210 offset1:211\n"
        "s_add_u32 s16, s73, 0x18000\n"
        "s_waitcnt lgkmcnt(7)\n"
        "v_mfma_f32_16x16x32_bf16 v[136:139], a[8:11], a[208:211], v[136:139]\n"
        "ds_read_b128 a[236:239], v228 offset:2368\n"
        "global_load_lds_dwordx4 v146, s[60:61] offset:2048\n"
        "v_mfma_f32_16x16x32_bf16 v[136:139], a[4:7], a[212:215], v[136:139]\n"
        "ds_read_b64_tr_b16 v[208:209], v154 offset:49152\n"
        "s_waitcnt lgkmcnt(7)\n"
        "v_mfma_f32_16x16x32_bf16 v[136:139], a[0:3], a[216:219], v[136:139]\n"
        "ds_read_b64_tr_b16 v[210:211], v154 offset:53248\n"
        "v_mfma_f32_16x16x32_bf16 v[140:143], a[0:3], a[220:223], v[140:143]\n"
        "ds_read2_b32 a[48:49], v226 offset0:80 offset1:81\n"
        "global_load_lds_dwordx4 v147, s[60:61] offset:3072\n"
        "s_add_u32 m0, s73, 0x8000\n"
        "v_mfma_f32_16x16x32_bf16 v[140:143], a[4:7], a[216:219], v[140:143]\n"
        "ds_read2_b32 a[50:51], v226 offset0:82 offset1:83\n"
        "s_add_u32 s60, s22, 0x2a0000\n"
        "s_addc_u32 s61, s23, 0\n"
        "v_mfma_f32_16x16x32_bf16 v[140:143], a[8:11], a[212:215], v[140:143]\n"
        "ds_read_b128 a[240:243], v228 offset:1856\n"
        "v_mfma_f32_16x16x32_bf16 v[140:143], a[12:15], a[208:211], v[140:143]\n"
        "ds_read_b64_tr_b16 v[212:213], v155 offset:49152\n"
        "v_mfma_f32_16x16x32_bf16 v[140:143], a[16:19], a[204:207], v[140:143]\n"
        "ds_read_b64_tr_b16 v[214:215], v155 offset:53248\n"
        "s_waitcnt lgkmcnt(8)\n"
        "v_mfma_f32_16x16x32_bf16 v[140:143], a[20:23], a[200:203], v[140:143]\n"
        "ds_read2_b32 a[52:53], v226 offset0:208 offset1:209\n"
        "s_waitcnt lgkmcnt(8)\n"
        "v_mfma_f32_16x16x32_bf16 v[140:143], a[24:27], a[196:199], v[140:143]\n"
        "ds_read2_b32 a[54:55], v226 offset0:210 offset1:211\n"
        "v_mfma_f32_16x16x32_bf16 v[140:143], a[28:31], a[192:195], v[140:143]\n"
        "ds_read_b128 a[244:247], v228 offset:1344\n"
        "v_mfma_f32_16x16x32_bf16 v[96:99], a[32:35], a[224:227], v[96:99]\n"
        "ds_read_b64_tr_b16 v[216:217], v154 offset:57344\n"
        "s_waitcnt lgkmcnt(8)\n"
        "v_mfma_f32_16x16x32_bf16 v[100:103], a[32:35], a[228:231], v[100:103]\n"
        "ds_read_b64_tr_b16 v[218:219], v154 offset:61440\n"
        "s_waitcnt lgkmcnt(8)\n"
        "v_mfma_f32_16x16x32_bf16 v[100:103], a[36:39], a[224:227], v[100:103]\n"
        "ds_read_b64_tr_b16 v[220:221], v155 offset:57344\n"
        "v_mfma_f32_16x16x32_bf16 v[104:107], a[40:43], a[224:227], v[104:107]\n"
        "ds_read_b64_tr_b16 v[222:223], v155 offset:61440\n"
        "v_mfma_f32_16x16x32_bf16 v[104:107], a[36:39], a[228:231], v[104:107]\n"
        "ds_read2_b32 a[56:57], v227 offset0:80 offset1:81\n"
        "v_mfma_f32_16x16x32_bf16 v[104:107], a[32:35], a[232:235], v[104:107]\n"
        "ds_read2_b32 a[58:59], v227 offset0:82 offset1:83\n"
        "v_mfma_f32_16x16x32_bf16 v[108:111], a[32:35], a[236:239], v[108:111]\n"
        "ds_read_b128 a[248:251], v228 offset:832\n"
        "s_waitcnt lgkmcnt(8)\n"
        "v_mfma_f32_16x16x32_bf16 v[108:111], a[36:39], a[232:235], v[108:111]\n"
        "ds_read2_b32 a[60:61], v227 offset0:208 offset1:209\n"
        "s_waitcnt lgkmcnt(8)\n"
        "v_mfma_f32_16x16x32_bf16 v[108:111], a[40:43], a[228:231], v[108:111]\n"
        "ds_read2_b32 a[62:63], v227 offset0:210 offset1:211\n"
        "v_mfma_f32_16x16x32_bf16 v[108:111], a[44:47], a[224:227], v[108:111]\n"
        "ds_read_b128 a[252:255], v228 offset:320\n"
        "v_mfma_f32_16x16x32_bf16 v[128:131], a[48:51], a[224:227], v[128:131]\n"
        "ds_read2_b32 a[0:1], v224 offset0:96 offset1:97\n"
        "v_mfma_f32_16x16x32_bf16 v[128:131], a[44:47], a[228:231], v[128:131]\n"
        "s_waitcnt vmcnt(8) lgkmcnt(7)\n"
        "s_barrier\n"
        "v_mfma_f32_16x16x32_bf16 v[128:131], a[40:43], a[232:235], v[128:131]\n"
        "global_load_lds_dwordx4 v144, s[60:61] offset:0\n"
        "ds_read_b64_tr_b16 a[64:65], v156 offset:0\n"
        "v_mfma_f32_16x16x32_bf16 v[128:131], a[36:39], a[236:239], v[128:131]\n"
        "ds_read_b64_tr_b16 a[66:67], v156 offset:4096\n"
        "s_waitcnt lgkmcnt(7)\n"
        "v_mfma_f32_16x16x32_bf16 v[128:131], a[32:35], a[240:243], v[128:131]\n"
        "ds_read2_b32 a[2:3], v224 offset0:98 offset1:99\n"
        "v_mfma_f32_16x16x32_bf16 v[132:135], a[32:35], a[244:247], v[132:135]\n"
        "global_load_lds_dwordx4 v145, s[60:61] offset:1024\n"
        "ds_read_b128 a[192:195], v230 offset:0\n"
        "v_mfma_f32_16x16x32_bf16 v[132:135], a[36:39], a[240:243], v[132:135]\n"
        "ds_read_b64_tr_b16 a[68:69], v157 offset:0\n"
        "v_lshrrev_b32 v230, 1, v233\n"
        "v_mfma_f32_16x16x32_bf16 v[132:135], a[40:43], a[236:239], v[132:135]\n"
        "ds_read_b64_tr_b16 a[70:71], v157 offset:4096\n"
        "s_waitcnt lgkmcnt(7)\n"
        "v_mfma_f32_16x16x32_bf16 v[140:143], a[32:35], a[252:255], v[140:143]\n"
        "global_load_lds_dwordx4 v146, s[60:61] offset:2048\n"
        "ds_read2_b32 a[4:5], v224 offset0:224 offset1:225\n"
        "v_mfma_f32_16x16x32_bf16 v[140:143], a[36:39], a[248:251], v[140:143]\n"
        "LHB1_%=:\n"
        "s_cbranch_execz LHB5_%=\n"
        "ds_read2_b32 a[6:7], v224 offset0:226 offset1:227\n"
        "v_mfma_f32_16x16x32_bf16 v[140:143], a[40:43], a[244:247], v[140:143]\n"
        "ds_read_b128 a[196:199], v228 offset:3456\n"
        "v_mfma_f32_16x16x32_bf16 v[140:143], a[44:47], a[240:243], v[140:143]\n"
        "global_load_lds_dwordx4 v147, s[60:61] offset:3072\n"
        "ds_read_b64_tr_b16 a[72:73], v156 offset:8192\n"
        "s_add_u32 m0, s73, 0xc000\n"
        "v_mfma_f32_16x16x32_bf16 v[140:143], a[48:51], a[236:239], v[140:143]\n"
        "ds_read_b64_tr_b16 a[74:75], v156 offset:12288\n"
        "s_add_u32 s60, s22, 0x300000\n"
        "s_addc_u32 s61, s23, 0\n"
        "v_mfma_f32_16x16x32_bf16 v[140:143], a[52:55], a[232:235], v[140:143]\n"
        "ds_read2_b32 a[8:9], v225 offset0:96 offset1:97\n"
        "s_waitcnt lgkmcnt(8)\n"
        "v_mfma_f32_16x16x32_bf16 v[140:143], a[56:59], a[228:231], v[140:143]\n"
        "ds_read2_b32 a[10:11], v225 offset0:98 offset1:99\n"
        "global_load_lds_dwordx4 v144, s[60:61] offset:0\n"
        "v_mfma_f32_16x16x32_bf16 v[140:143], a[60:63], a[224:227], v[140:143]\n"
        "ds_read_b128 a[200:203], v228 offset:2944\n"
        "v_mfma_f32_16x16x32_bf16 v[132:135], a[44:47], a[232:235], v[132:135]\n"
        "ds_read_b64_tr_b16 a[76:77], v157 offset:8192\n"
        "v_mfma_f32_16x16x32_bf16 v[132:135], a[48:51], a[228:231], v[132:135]\n"
        "global_load_dwordx2 v[236:237], v238, s[48:49] sc1\n"
        "v_mfma_f32_16x16x32_bf16 v[132:135], a[52:55], a[224:227], v[132:135]\n"
        "ds_read_b64_tr_b16 a[78:79], v157 offset:12288\n"
        "global_load_lds_dwordx4 v145, s[60:61] offset:1024\n"
        "v_mfma_f32_16x16x32_bf16 v[136:139], a[56:59], a[224:227], v[136:139]\n"
        "ds_read2_b32 a[12:13], v225 offset0:224 offset1:225\n"
        "s_waitcnt lgkmcnt(7)\n"
        "v_mfma_f32_16x16x32_bf16 v[136:139], a[52:55], a[228:231], v[136:139]\n"
        "ds_read2_b32 a[14:15], v225 offset0:226 offset1:227\n"
        "v_mfma_f32_16x16x32_bf16 v[136:139], a[48:51], a[232:235], v[136:139]\n"
        "ds_read_b128 a[204:207], v228 offset:2432\n"
        "global_load_lds_dwordx4 v146, s[60:61] offset:2048\n"
        "v_mfma_f32_16x16x32_bf16 v[136:139], a[44:47], a[236:239], v[136:139]\n"
        "ds_read_b64_tr_b16 a[80:81], v156 offset:16384\n"
        "s_waitcnt lgkmcnt(7)\n"
        "v_mfma_f32_16x16x32_bf16 v[136:139], a[40:43], a[240:243], v[136:139]\n"
        "ds_read_b64_tr_b16 a[82:83], v156 offset:20480\n"
        "v_mfma_f32_16x16x32_bf16 v[136:139], a[36:39], a[244:247], v[136:139]\n"
        "ds_read2_b32 a[16:17], v226 offset0:96 offset1:97\n"
        "global_load_lds_dwordx4 v147, s[60:61] offset:3072\n"
        "s_add_u32 m0, s73, 0x10000\n"
        "v_mfma_f32_16x16x32_bf16 v[136:139], a[32:35], a[248:251], v[136:139]\n"
        "ds_read2_b32 a[18:19], v226 offset0:98 offset1:99\n"
        "s_add_u32 s60, s22, 0x340000\n"
        "s_addc_u32 s61, s23, 0\n"
        "v_mfma_f32_16x16x32_bf16 v[96:99], a[0:3], a[192:195], v[96:99]\n"
        "ds_read_b128 a[208:211], v228 offset:1920\n"
        "v_mfma_f32_16x16x32_bf16 v[100:103], a[0:3], a[196:199], v[100:103]\n"
        "ds_read_b64_tr_b16 a[84:85], v157 offset:16384\n"
        "v_mfma_f32_16x16x32_bf16 v[100:103], a[4:7], a[192:195], v[100:103]\n"
        "ds_read_b64_tr_b16 a[86:87], v157 offset:20480\n"
        "s_waitcnt lgkmcnt(8)\n"
        "v_mfma_f32_16x16x32_bf16 v[104:107], a[8:11], a[192:195], v[104:107]\n"
        "ds_read_b64_tr_b16 a[88:89], v156 offset:24576\n"
        "s_waitcnt lgkmcnt(8)\n"
        "v_mfma_f32_16x16x32_bf16 v[104:107], a[4:7], a[196:199], v[104:107]\n"
        "ds_read_b64_tr_b16 a[90:91], v156 offset:28672\n"
        "v_mfma_f32_16x16x32_bf16 v[104:107], a[0:3], a[200:203], v[104:107]\n"
        "ds_read_b64_tr_b16 a[92:93], v157 offset:24576\n"
        "v_mfma_f32_16x16x32_bf16 v[108:111], a[0:3], a[204:207], v[108:111]\n"
        "ds_read_b64_tr_b16 a[94:95], v157 offset:28672\n"
        "s_waitcnt lgkmcnt(8)\n"
        "v_mfma_f32_16x16x32_bf16 v[108:111], a[4:7], a[200:203], v[108:111]\n"
        "ds_read2_b32 a[20:21], v226 offset0:224 offset1:225\n"
        "s_waitcnt lgkmcnt(8)\n"
        "v_mfma_f32_16x16x32_bf16 v[108:111], a[8:11], a[196:199], v[108:111]\n"
        "ds_read2_b32 a[22:23], v226 offset0:226 offset1:227\n"
        "v_mfma_f32_16x16x32_bf16 v[108:111], a[12:15], a[192:195], v[108:111]\n"
        "ds_read_b128 a[212:215], v228 offset:1408\n"
        "v_mfma_f32_16x16x32_bf16 v[128:131], a[16:19], a[192:195], v[128:131]\n"
        "ds_read2_b32 a[24:25], v227 offset0:96 offset1:97\n"
        "v_mfma_f32_16x16x32_bf16 v[128:131], a[12:15], a[196:199], v[128:131]\n"
        "ds_read2_b32 a[26:27], v227 offset0:98 offset1:99\n"
        "v_mfma_f32_16x16x32_bf16 v[128:131], a[8:11], a[200:203], v[128:131]\n"
        "ds_read_b128 a[216:219], v228 offset:896\n"
        "v_mfma_f32_16x16x32_bf16 v[128:131], a[4:7], a[204:207], v[128:131]\n"
        "ds_read_b128 a[220:223], v228 offset:384\n"
        "s_waitcnt lgkmcnt(8)\n"
        "v_mfma_f32_16x16x32_bf16 v[128:131], a[0:3], a[208:211], v[128:131]\n"
        "s_waitcnt vmcnt(9) lgkmcnt(7)\n"
        "ds_read2_b32 a[28:29], v227 offset0:224 offset1:225\n"
        "ds_read2_b32 a[30:31], v227 offset0:226 offset1:227\n"
        "s_barrier\n"
        "ds_read_b64_tr_b16 a[96:97], v154 offset:0\n"
        "ds_read_b64_tr_b16 a[98:99], v154 offset:4096\n"
        "ds_read_b64_tr_b16 a[100:101], v155 offset:0\n"
        "s_waitcnt lgkmcnt(9)\n"
        "v_mfma_f32_16x16x32_bf16 v[132:135], a[0:3], a[212:215], v[132:135]\n"
        "ds_read_b64_tr_b16 a[102:103], v155 offset:4096\n"
        "global_load_lds_dwordx4 v144, s[60:61] offset:0\n"
        "v_mfma_f32_16x16x32_bf16 v[132:135], a[4:7], a[208:211], v[132:135]\n"
        "ds_read_b64_tr_b16 a[104:105], v154 offset:8192\n"
        "s_waitcnt lgkmcnt(10)\n"
        "v_mfma_f32_16x16x32_bf16 v[132:135], a[8:11], a[204:207], v[132:135]\n"
        "ds_read_b64_tr_b16 a[106:107], v154 offset:12288\n"
        "s_waitcnt lgkmcnt(10)\n"
        "v_mfma_f32_16x16x32_bf16 v[132:135], a[12:15], a[200:203], v[132:135]\n"
        "ds_read_b64_tr_b16 a[108:109], v155 offset:8192\n"
        "s_waitcnt lgkmcnt(9)\n"
        "v_mfma_f32_16x16x32_bf16 v[140:143], a[0:3], a[220:223], v[140:143]\n"
        "ds_read_b64_tr_b16 a[110:111], v155 offset:12288\n"
        "global_load_lds_dwordx4 v145, s[60:61] offset:1024\n"
        "v_mfma_f32_16x16x32_bf16 v[140:143], a[4:7], a[216:219], v[140:143]\n"
        "ds_read_b64_tr_b16 a[112:113], v154 offset:16384\n"
        "v_mfma_f32_16x16x32_bf16 v[140:143], a[8:11], a[212:215], v[140:143]\n"
        "ds_read_b64_tr_b16 a[114:115], v154 offset:20480\n"
        "v_mfma_f32_16x16x32_bf16 v[140:143], a[12:15], a[208:211], v[140:143]\n"
        "ds_read_b64_tr_b16 a[116:117], v155 offset:16384\n"
        "global_load_lds_dwordx4 v146, s[60:61] offset:2048\n"
        "v_mfma_f32_16x16x32_bf16 v[140:143], a[16:19], a[204:207], v[140:143]\n"
        "ds_read_b64_tr_b16 a[118:119], v155 offset:20480\n"
        "v_mfma_f32_16x16x32_bf16 v[140:143], a[20:23], a[200:203], v[140:143]\n"
        "ds_read_b64_tr_b16 a[120:121], v154 offset:24576\n"
        "v_mfma_f32_16x16x32_bf16 v[140:143], a[24:27], a[196:199], v[140:143]\n"
        "ds_read_b64_tr_b16 a[122:123], v154 offset:28672\n"
        "s_waitcnt lgkmcnt(8)\n"
        "v_mfma_f32_16x16x32_bf16 v[140:143], a[28:31], a[192:195], v[140:143]\n"
        "ds_read_b64_tr_b16 a[124:125], v155 offset:24576\n"
        "global_load_lds_dwordx4 v147, s[60:61] offset:3072\n"
        "s_add_u32 m0, s73, 0x14000\n"
        "s_waitcnt vmcnt(7) lgkmcnt(8)\n"
        "v_mfma_f32_16x16x32_bf16 v[132:135], a[16:19], a[196:199], v[132:135]\n"
        "ds_read_b64_tr_b16 a[126:127], v155 offset:28672\n"
        "s_add_u32 s60, s22, 0x3a0000\n"
        "s_addc_u32 s61, s23, 0\n"
        "v_mfma_f32_16x16x32_bf16 v[132:135], a[20:23], a[192:195], v[132:135]\n"
        "ds_read2_b32 a[32:33], v224 offset0:112 offset1:113\n"
        "v_cmp_ne_u64 vcc, v[236:237], v[250:251]\n"
        "v_mfma_f32_16x16x32_bf16 v[136:139], a[24:27], a[192:195], v[136:139]\n"
        "global_load_lds_dwordx4 v144, s[60:61] offset:0\n"
        "s_cbranch_vccnz LPCX_%=\n"
        "LBCX_%=:\n"
        "v_mfma_f32_16x16x32_bf16 v[136:139], a[20:23], a[196:199], v[136:139]\n"
        "ds_read2_b32 a[34:35], v224 offset0:114 offset1:115\n"
        "v_mfma_f32_16x16x32_bf16 v[136:139], a[16:19], a[200:203], v[136:139]\n"
        "ds_read_b128 a[224:227], v231 offset:0\n"
        "v_mfma_f32_16x16x32_bf16 v[136:139], a[12:15], a[204:207], v[136:139]\n"
        "global_load_lds_dwordx4 v145, s[60:61] offset:1024\n"
        "ds_read2_b32 a[36:37], v224 offset0:240 offset1:241\n"
        "v_mfma_f32_16x16x32_bf16 v[136:139], a[8:11], a[208:211], v[136:139]\n"
        "ds_read2_b32 a[38:39], v224 offset0:242 offset1:243\n"
        "v_lshrrev_b32 v224, 7, v254\n"
        "v_mfma_f32_16x16x32_bf16 v[136:139], a[4:7], a[212:215], v[136:139]\n"
        "ds_read_b128 a[228:231], v228 offset:3520\n"
        "v_lshlrev_b32 v224, 12, v224\n"
        "v_mfma_f32_16x16x32_bf16 v[136:139], a[0:3], a[216:219], v[136:139]\n"
        "s_waitcnt vmcnt(6) lgkmcnt(6)\n"
        "global_load_lds_dwordx4 v146, s[60:61] offset:2048\n"
        "global_load_lds_dwordx4 v147, s[60:61] offset:3072\n"
        "s_barrier\n"
        "s_waitcnt lgkmcnt(3)\n"
        "v_mfma_f32_16x16x32_bf16 v[96:99], a[32:35], a[224:227], v[96:99]\n"
        "s_add_u32 m0, s73, 0x0\n"
        "s_waitcnt lgkmcnt(0)\n"
        "global_load_lds_dwordx4 v254, s[20:21] offset:0 sc1\n"
        "ds_read2_b32 a[40:41], v225 offset0:112 offset1:113\n"
        "v_mfma_f32_16x16x32_bf16 v[100:103], a[32:35], a[228:231], v[100:103]\n"
        "ds_read2_b32 a[42:43], v225 offset0:114 offset1:115\n"
        "s_add_u32 s60, s22, 0x20000\n"
        "s_addc_u32 s61, s23, 0\n"
        "v_mfma_f32_16x16x32_bf16 v[100:103], a[36:39], a[224:227], v[100:103]\n"
        "global_load_lds_dwordx4 v254, s[20:21] offset:1024 sc1\n"
        "global_load_lds_dwordx4 v254, s[20:21] offset:2048 sc1\n"
        "ds_read_b128 a[232:235], v228 offset:3008\n"
        "ds_read_b64 v[64:65], v229 offset:3584\n"
        "ds_read_b64 v[66:67], v229 offset:3072\n"
        "ds_read_b64 v[68:69], v229 offset:2560\n"
        "ds_read_b64 v[70:71], v229 offset:2048\n"
        "s_waitcnt lgkmcnt(5)\n"
        "v_mfma_f32_16x16x32_bf16 v[104:107], a[40:43], a[224:227], v[104:107]\n"
        "global_load_lds_dwordx4 v254, s[20:21] offset:3072 sc1\n"
        "ds_read_b64 v[72:73], v229 offset:1536\n"
        "s_add_u32 m0, s73, 0x4000\n"
        "v_mfma_f32_16x16x32_bf16 v[104:107], a[36:39], a[228:231], v[104:107]\n"
        "ds_read_b64 v[74:75], v229 offset:1024\n"
        "ds_read_b64 v[76:77], v229 offset:512\n"
        "ds_read_b64 v[78:79], v229 offset:0\n"
        "ds_read_b128 a[252:255], v228 offset:448\n"
        "ds_read2_b32 a[60:61], v227 offset0:240 offset1:241\n"
        "ds_read2_b32 a[62:63], v227 offset0:242 offset1:243\n"
        "ds_read2_b32 a[56:57], v227 offset0:112 offset1:113\n"
        "global_load_lds_dwordx4 v144, s[60:61] offset:0\n"
        "s_waitcnt lgkmcnt(12)\n"
        "v_mfma_f32_16x16x32_bf16 v[104:107], a[32:35], a[232:235], v[104:107]\n"
        "ds_read2_b32 a[58:59], v227 offset0:114 offset1:115\n"
        "ds_read2_b32 a[52:53], v226 offset0:240 offset1:241\n"
        "ds_read2_b32 a[54:55], v226 offset0:242 offset1:243\n"
        "ds_read2_b32 a[48:49], v226 offset0:112 offset1:113\n"
        "ds_read2_b32 a[50:51], v226 offset0:114 offset1:115\n"
        "ds_read2_b32 a[44:45], v225 offset0:240 offset1:241\n"
        "ds_read2_b32 a[46:47], v225 offset0:242 offset1:243\n"
        "ds_read_b128 a[236:239], v228 offset:2496\n"
        "ds_read_b128 a[240:243], v228 offset:1984\n"
        "ds_read_b128 a[244:247], v228 offset:1472\n"
        "s_waitcnt lgkmcnt(13)\n"
        "v_mfma_f32_16x16x32_bf16 v[140:143], a[32:35], a[252:255], v[140:143]\n"
        "ds_read_b128 a[248:251], v228 offset:960\n"
        "global_load_lds_dwordx4 v145, s[60:61] offset:1024\n"
        "s_add_u32 s20, s20, 0x4000\n"
        "s_addc_u32 s21, s21, 0\n"
        "ds_read_b64_tr_b16 a[128:129], v154 offset:32768\n"
        "ds_read_b64_tr_b16 a[130:131], v154 offset:36864\n"
        "v_and_b32 v229, 1, v233\n"
        "s_waitcnt lgkmcnt(12)\n"
        "v_mfma_f32_16x16x32_bf16 v[136:139], a[56:59], a[224:227], v[136:139]\n"
        "ds_read_b64_tr_b16 a[132:133], v155 offset:32768\n"
        "s_waitcnt lgkmcnt(11)\n"
        "v_mfma_f32_16x16x32_bf16 v[136:139], a[52:55], a[228:231], v[136:139]\n"
        "global_load_lds_dwordx4 v146, s[60:61] offset:2048\n"
        "s_waitcnt lgkmcnt(9)\n"
        "v_mfma_f32_16x16x32_bf16 v[128:131], a[48:51], a[224:227], v[128:131]\n"
        "ds_read_b64_tr_b16 a[134:135], v155 offset:36864\n"
        "s_waitcnt lgkmcnt(8)\n"
        "v_mfma_f32_16x16x32_bf16 v[128:131], a[44:47], a[228:231], v[128:131]\n"
        "ds_read_b64_tr_b16 a[136:137], v154 offset:40960\n"
        "s_waitcnt lgkmcnt(8)\n"
        "v_mfma_f32_16x16x32_bf16 v[108:111], a[32:35], a[236:239], v[108:111]\n"
        "global_load_lds_dwordx4 v147, s[60:61] offset:3072\n"
        "s_add_u32 m0, s73, 0x18000\n"
        "v_lshlrev_b32 v229, 9, v229\n"
        "v_mfma_f32_16x16x32_bf16 v[108:111], a[36:39], a[232:235], v[108:111]\n"
        "s_waitcnt lgkmcnt(5)\n"
        "s_barrier\n"
        "v_mfma_f32_16x16x32_bf16 v[108:111], a[40:43], a[228:231], v[108:111]\n"
        "ds_read_b64_tr_b16 a[138:139], v154 offset:45056\n"
        "s_add_u32 s60, s22, 0x60000\n"
        "s_addc_u32 s61, s23, 0\n"
        "v_mfma_f32_16x16x32_bf16 v[108:111], a[44:47], a[224:227], v[108:111]\n"
        "global_load_lds_dwordx4 v254, s[20:21] offset:0 sc1\n"
        "ds_read_b64_tr_b16 a[140:141], v155 offset:40960\n"
        "v_mfma_f32_16x16x32_bf16 v[128:131], a[40:43], a[232:235], v[128:131]\n"
        "ds_read_b64_tr_b16 a[142:143], v155 offset:45056\n"
        "v_lshl_add_u32 v229, v232, 5, v229\n"
        "v_mfma_f32_16x16x32_bf16 v[128:131], a[36:39], a[236:239], v[128:131]\n"
        "ds_read_b64_tr_b16 a[144:145], v154 offset:49152\n"
        "v_lshl_add_u32 v229, v230, 4, v229\n"
        "v_mfma_f32_16x16x32_bf16 v[128:131], a[32:35], a[240:243], v[128:131]\n"
        "global_load_lds_dwordx4 v254, s[20:21] offset:1024 sc1\n"
        "ds_read_b64_tr_b16 a[146:147], v154 offset:53248\n"
        "v_mfma_f32_16x16x32_bf16 v[132:135], a[32:35], a[244:247], v[132:135]\n"
        "ds_read_b64_tr_b16 a[148:149], v155 offset:49152\n"
        "v_and_b32 v80, 0xffff0000, v65\n"
        "v_mfma_f32_16x16x32_bf16 v[132:135], a[36:39], a[240:243], v[132:135]\n"
        "ds_read_b64_tr_b16 a[150:151], v155 offset:53248\n"
        "v_lshlrev_b32 v81, 16, v65\n"
        "v_mfma_f32_16x16x32_bf16 v[132:135], a[40:43], a[236:239], v[132:135]\n"
        "global_load_lds_dwordx4 v254, s[20:21] offset:2048 sc1\n"
        "ds_read_b64_tr_b16 a[152:153], v154 offset:57344\n"
        "v_mfma_f32_16x16x32_bf16 v[132:135], a[44:47], a[232:235], v[132:135]\n"
        "ds_read_b64_tr_b16 a[154:155], v154 offset:61440\n"
        "v_and_b32 v82, 0xffff0000, v64\n"
        "v_mfma_f32_16x16x32_bf16 v[132:135], a[48:51], a[228:231], v[132:135]\n"
        "ds_read_b64_tr_b16 a[156:157], v155 offset:57344\n"
        "v_lshlrev_b32 v83, 16, v64\n"
        "v_mfma_f32_16x16x32_bf16 v[132:135], a[52:55], a[224:227], v[132:135]\n"
        "global_load_lds_dwordx4 v254, s[20:21] offset:3072 sc1\n"
        "s_add_u32 s20, s20, 0x4000\n"
        "s_addc_u32 s21, s21, 0\n"
        "v_mfma_f32_16x16x32_bf16 v[136:139], a[48:51], a[232:235], v[136:139]\n"
        "s_add_u32 m0, s73, 0x1c000\n"
        "ds_read_b64_tr_b16 a[158:159], v155 offset:61440\n"
        "v_mfma_f32_16x16x32_bf16 v[136:139], a[44:47], a[236:239], v[136:139]\n"
        "v_mul_f32 v80, s54, v80\n"
        "v_mul_f32 v81, s54, v81\n"
        "v_mfma_f32_16x16x32_bf16 v[136:139], a[40:43], a[240:243], v[136:139]\n"
        "global_load_lds_dwordx4 v254, s[20:21] offset:0 sc1\n"
        "v_mul_f32 v82, s54, v82\n"
        "v_mfma_f32_16x16x32_bf16 v[136:139], a[36:39], a[244:247], v[136:139]\n"
        "v_mul_f32 v83, s54, v83\n"
        "v_cvt_pk_bf16_f32 v80, 0, v80\n"
        "v_mfma_f32_16x16x32_bf16 v[136:139], a[32:35], a[248:251], v[136:139]\n"
        "v_cvt_pk_bf16_f32 v81, 0, v81\n"
        "v_cvt_pk_bf16_f32 v82, 0, v82\n"
        "v_mfma_f32_16x16x32_bf16 v[140:143], a[36:39], a[248:251], v[140:143]\n"
        "global_load_lds_dwordx4 v254, s[20:21] offset:1024 sc1\n"
        "v_cvt_pk_bf16_f32 v83, 0, v83\n"
        "v_mfma_f32_16x16x32_bf16 v[140:143], a[40:43], a[244:247], v[140:143]\n"
        "v_cvt_pk_bf16_f32 v96, 0, v96\n"
        "v_cvt_pk_bf16_f32 v97, 0, v97\n"
        "v_mfma_f32_16x16x32_bf16 v[140:143], a[44:47], a[240:243], v[140:143]\n"
        "v_cvt_pk_bf16_f32 v98, 0, v98\n"
        "v_cvt_pk_bf16_f32 v99, 0, v99\n"
        "v_mfma_f32_16x16x32_bf16 v[140:143], a[48:51], a[236:239], v[140:143]\n"
        "global_load_lds_dwordx4 v254, s[20:21] offset:2048 sc1\n"
        "v_add_f32 v80, v80, v96\n"
        "v_mfma_f32_16x16x32_bf16 v[140:143], a[52:55], a[232:235], v[140:143]\n"
        "v_add_f32 v81, v81, v97\n"
        "v_add_f32 v82, v82, v98\n"
        "v_mfma_f32_16x16x32_bf16 v[140:143], a[56:59], a[228:231], v[140:143]\n"
        "v_add_f32 v83, v83, v99\n"
        "v_cvt_pk_bf16_f32 v88, v80, v81\n"
        "v_mfma_f32_16x16x32_bf16 v[140:143], a[60:63], a[224:227], v[140:143]\n"
        "global_load_lds_dwordx4 v254, s[20:21] offset:3072 sc1\n"
        "s_add_u32 m0, s73, 0x20000\n"
        "s_add_u32 s20, s20, 0x4000\n"
        "s_addc_u32 s21, s21, 0\n"
        "v_cvt_pk_bf16_f32 v89, v82, v83\n"
        "v_and_b32 v84, 0xffff0000, v67\n"
        "v_lshlrev_b32 v85, 16, v67\n"
        "v_and_b32 v86, 0xffff0000, v66\n"
        "v_lshlrev_b32 v87, 16, v66\n"
        "v_mul_f32 v84, s54, v84\n"
        "v_mul_f32 v85, s54, v85\n"
        "global_load_lds_dwordx4 v144, s[60:61] offset:0\n"
        "v_mul_f32 v86, s54, v86\n"
        "v_mul_f32 v87, s54, v87\n"
        "v_cvt_pk_bf16_f32 v84, 0, v84\n"
        "v_cvt_pk_bf16_f32 v85, 0, v85\n"
        "v_cvt_pk_bf16_f32 v86, 0, v86\n"
        "v_cvt_pk_bf16_f32 v87, 0, v87\n"
        "v_cvt_pk_bf16_f32 v100, 0, v100\n"
        "v_cvt_pk_bf16_f32 v101, 0, v101\n"
        "v_cvt_pk_bf16_f32 v102, 0, v102\n"
        "global_load_lds_dwordx4 v145, s[60:61] offset:1024\n"
        "v_cvt_pk_bf16_f32 v103, 0, v103\n"
        "v_add_f32 v84, v84, v100\n"
        "v_add_f32 v85, v85, v101\n"
        "v_add_f32 v86, v86, v102\n"
        "v_add_f32 v87, v87, v103\n"
        "v_cvt_pk_bf16_f32 v90, v84, v85\n"
        "v_cvt_pk_bf16_f32 v91, v86, v87\n"
        "v_and_b32 v80, 0xffff0000, v69\n"
        "v_permlane16_swap_b32 v88, v90\n"
        "global_load_lds_dwordx4 v146, s[60:61] offset:2048\n"
        "v_permlane16_swap_b32 v89, v91\n"
        "v_lshlrev_b32 v81, 16, v69\n"
        "v_and_b32 v82, 0xffff0000, v68\n"
        "v_lshlrev_b32 v83, 16, v68\n"
        "v_mul_f32 v80, s54, v80\n"
        "v_mul_f32 v81, s54, v81\n"
        "v_mul_f32 v82, s54, v82\n"
        "v_mul_f32 v83, s54, v83\n"
        "global_load_lds_dwordx4 v147, s[60:61] offset:3072\n"
        "s_add_u32 m0, s73, 0x24000\n"
        "s_add_u32 s60, s22, 0x80000\n"
        "s_addc_u32 s61, s23, 0\n"
        "v_cvt_pk_bf16_f32 v80, 0, v80\n"
        "v_cvt_pk_bf16_f32 v81, 0, v81\n"
        "v_cvt_pk_bf16_f32 v82, 0, v82\n"
        "v_cvt_pk_bf16_f32 v83, 0, v83\n"
        "v_cvt_pk_bf16_f32 v104, 0, v104\n"
        "v_cvt_pk_bf16_f32 v105, 0, v105\n"
        "v_cvt_pk_bf16_f32 v106, 0, v106\n"
        "global_load_lds_dwordx4 v254, s[20:21] offset:0 sc1\n"
        "v_cvt_pk_bf16_f32 v107, 0, v107\n"
        "v_add_f32 v80, v80, v104\n"
        "v_add_f32 v81, v81, v105\n"
        "v_add_f32 v82, v82, v106\n"
        "v_add_f32 v83, v83, v107\n"
        "v_cvt_pk_bf16_f32 v92, v80, v81\n"
        "v_cvt_pk_bf16_f32 v93, v82, v83\n"
        "v_and_b32 v84, 0xffff0000, v71\n"
        "v_lshlrev_b32 v85, 16, v71\n"
        "global_load_lds_dwordx4 v254, s[20:21] offset:1024 sc1\n"
        "v_and_b32 v86, 0xffff0000, v70\n"
        "v_lshlrev_b32 v87, 16, v70\n"
        "v_mul_f32 v84, s54, v84\n"
        "v_mul_f32 v85, s54, v85\n"
        "v_mul_f32 v86, s54, v86\n"
        "v_mul_f32 v87, s54, v87\n"
        "v_cvt_pk_bf16_f32 v84, 0, v84\n"
        "v_cvt_pk_bf16_f32 v85, 0, v85\n"
        "v_cvt_pk_bf16_f32 v86, 0, v86\n"
        "global_load_lds_dwordx4 v254, s[20:21] offset:2048 sc1\n"
        "s_waitcnt vmcnt(19) lgkmcnt(0)\n"
        "s_barrier\n"
        "ds_read_b128 a[0:3], v148 offset:0\n"
        "ds_read_b128 a[4:7], v148 offset:8192\n"
        "ds_read_b128 a[8:11], v148 offset:1024\n"
        "ds_read_b128 a[12:15], v148 offset:9216\n"
        "ds_read_b64_tr_b16 a[160:161], v156 offset:0\n"
        "ds_read_b64_tr_b16 a[162:163], v156 offset:4096\n"
        "ds_read_b64_tr_b16 a[164:165], v157 offset:0\n"
        "ds_read_b64_tr_b16 a[166:167], v157 offset:4096\n"
        "ds_read_b64_tr_b16 a[168:169], v156 offset:8192\n"
        "ds_read_b64_tr_b16 a[170:171], v156 offset:12288\n"
        "ds_read_b64_tr_b16 a[172:173], v157 offset:8192\n"
        "ds_read_b64_tr_b16 a[174:175], v157 offset:12288\n"
        "ds_read_b64_tr_b16 a[176:177], v156 offset:16384\n"
        "ds_read_b64_tr_b16 a[178:179], v156 offset:20480\n"
        "ds_read_b64_tr_b16 a[180:181], v157 offset:16384\n"
        "global_load_lds_dwordx4 v254, s[20:21] offset:3072 sc1\n"
        "s_add_u32 m0, s73, 0x8000\n"
        "s_waitcnt lgkmcnt(14)\n"
        "v_mfma_f32_16x16x32_bf16 v[0:3], v[160:163], a[0:3], 0\n"
        "ds_read_b64_tr_b16 a[182:183], v157 offset:20480\n"
        "s_waitcnt lgkmcnt(14)\n"
        "v_mfma_f32_16x16x32_bf16 v[0:3], v[168:171], a[4:7], v[0:3]\n"
        "ds_read_b64_tr_b16 a[184:185], v156 offset:24576\n"
        "s_add_u32 s20, s20, 0x4000\n"
        "s_addc_u32 s21, s21, 0\n"
        "v_mfma_f32_16x16x32_bf16 v[4:7], v[172:175], a[4:7], 0\n"
        "ds_read_b64_tr_b16 a[186:187], v156 offset:28672\n"
        "global_load_lds_dwordx4 v144, s[60:61] offset:0\n"
        "v_mfma_f32_16x16x32_bf16 v[4:7], v[164:167], a[0:3], v[4:7]\n"
        "ds_read_b64_tr_b16 a[188:189], v157 offset:24576\n"
        "s_waitcnt lgkmcnt(11)\n"
        "v_mfma_f32_16x16x32_bf16 v[12:15], v[164:167], a[8:11], 0\n"
        "ds_read_b64_tr_b16 a[190:191], v157 offset:28672\n"
        "v_cvt_pk_bf16_f32 v87, 0, v87\n"
        "v_mfma_f32_16x16x32_bf16 v[12:15], v[172:175], a[12:15], v[12:15]\n"
        "ds_read_b128 a[16:19], v148 offset:2048\n"
        "global_load_lds_dwordx4 v145, s[60:61] offset:1024\n"
        "v_mfma_f32_16x16x32_bf16 v[8:11], v[168:171], a[12:15], 0\n"
        "ds_read_b128 a[20:23], v148 offset:10240\n"
        "v_cvt_pk_bf16_f32 v108, 0, v108\n"
        "v_mfma_f32_16x16x32_bf16 v[8:11], v[160:163], a[8:11], v[8:11]\n"
        "ds_read_b128 a[24:27], v148 offset:3072\n"
        "ds_read_b128 a[28:31], v148 offset:11264\n"
        "ds_read_b128 a[192:195], v151 offset:0\n"
        "ds_read_b128 a[196:199], v151 offset:8192\n"
        "ds_read_b128 a[200:203], v151 offset:1024\n"
        "ds_read_b128 a[204:207], v151 offset:9216\n"
        "ds_read_b128 a[208:211], v151 offset:2048\n"
        "ds_read_b128 a[212:215], v151 offset:10240\n"
        "global_load_lds_dwordx4 v146, s[60:61] offset:2048\n"
        "ds_read_b128 a[216:219], v151 offset:3072\n"
        "ds_read_b128 a[220:223], v151 offset:11264\n"
        "v_cvt_pk_bf16_f32 v109, 0, v109\n"
        "v_cvt_pk_bf16_f32 v110, 0, v110\n"
        "v_cvt_pk_bf16_f32 v111, 0, v111\n"
        "s_waitcnt lgkmcnt(11)\n"
        "v_mfma_f32_16x16x32_bf16 v[16:19], v[160:163], a[16:19], 0\n"
        "v_add_f32 v84, v84, v108\n"
        "s_waitcnt lgkmcnt(10)\n"
        "v_mfma_f32_16x16x32_bf16 v[16:19], v[168:171], a[20:23], v[16:19]\n"
        "global_load_lds_dwordx4 v147, s[60:61] offset:3072\n"
        "s_add_u32 m0, s73, 0xc000\n"
        "v_add_f32 v85, v85, v109\n"
        "v_mfma_f32_16x16x32_bf16 v[20:23], v[172:175], a[20:23], 0\n"
        "s_add_u32 s60, s22, 0xc0000\n"
        "s_addc_u32 s61, s23, 0\n"
        "v_add_f32 v86, v86, v110\n"
        "v_mfma_f32_16x16x32_bf16 v[20:23], v[164:167], a[16:19], v[20:23]\n"
        "s_waitcnt lgkmcnt(7)\n"
        "v_add_f32 v87, v87, v111\n"
        "v_cvt_pk_bf16_f32 v94, v84, v85\n"
        "v_mfma_f32_16x16x32_bf16 v[28:31], v[164:167], a[24:27], 0\n"
        "global_load_lds_dwordx4 v254, s[20:21] offset:0 sc1\n"
        "v_cvt_pk_bf16_f32 v95, v86, v87\n"
        "v_mfma_f32_16x16x32_bf16 v[28:31], v[172:175], a[28:31], v[28:31]\n"
        "s_waitcnt lgkmcnt(3)\n"
        "v_permlane16_swap_b32 v92, v94\n"
        "v_mfma_f32_16x16x32_bf16 v[24:27], v[168:171], a[28:31], 0\n"
        "v_permlane16_swap_b32 v93, v95\n"
        "v_mfma_f32_16x16x32_bf16 v[24:27], v[160:163], a[24:27], v[24:27]\n"
        "s_waitcnt vmcnt(17)\n"
        "s_barrier\n"
        "v_and_b32 v80, 0xffff0000, v73\n"
        "v_mfma_f32_16x16x32_bf16 v[32:35], v[160:163], a[192:195], 0\n"
        "global_load_lds_dwordx4 v254, s[20:21] offset:1024 sc1\n"
        "ds_read_b64_tr_b16 a[32:33], v154 offset:16384\n"
        "v_mfma_f32_16x16x32_bf16 v[32:35], v[168:171], a[196:199], v[32:35]\n"
        "ds_read_b64_tr_b16 a[34:35], v154 offset:20480\n"
        "v_lshlrev_b32 v81, 16, v73\n"
        "v_mfma_f32_16x16x32_bf16 v[36:39], v[172:175], a[196:199], 0\n"
        "ds_read_b64_tr_b16 a[40:41], v154 offset:24576\n"
        "v_and_b32 v82, 0xffff0000, v72\n"
        "v_mfma_f32_16x16x32_bf16 v[36:39], v[164:167], a[192:195], v[36:39]\n"
        "global_load_lds_dwordx4 v254, s[20:21] offset:2048 sc1\n"
        "ds_read_b64_tr_b16 a[42:43], v154 offset:28672\n"
        "v_mfma_f32_16x16x32_bf16 v[44:47], v[164:167], a[200:203], 0\n"
        "ds_read_b64_tr_b16 a[36:37], v155 offset:16384\n"
        "v_lshlrev_b32 v83, 16, v72\n"
        "v_mfma_f32_16x16x32_bf16 v[44:47], v[172:175], a[204:207], v[44:47]\n"
        "ds_read_b64_tr_b16 a[38:39], v155 offset:20480\n"
        "v_mul_f32 v80, s54, v80\n"
        "v_mfma_f32_16x16x32_bf16 v[40:43], v[168:171], a[204:207], 0\n"
        "global_load_lds_dwordx4 v254, s[20:21] offset:3072 sc1\n"
        "s_add_u32 m0, s73, 0x10000\n"
        "v_mul_f32 v81, s54, v81\n"
        "v_mfma_f32_16x16x32_bf16 v[40:43], v[160:163], a[200:203], v[40:43]\n"
        "s_add_u32 s20, s20, 0x4000\n"
        "s_addc_u32 s21, s21, 0\n"
        "v_mul_f32 v82, s54, v82\n"
        "v_mfma_f32_16x16x32_bf16 v[48:51], v[160:163], a[208:211], 0\n"
        "ds_read_b64_tr_b16 a[44:45], v155 offset:24576\n"
        "s_waitcnt lgkmcnt(6)\n"
        "v_mfma_f32_16x16x32_bf16 v[48:51], v[168:171], a[212:215], v[48:51]\n"
        "global_load_lds_dwordx4 v254, s[20:21] offset:0 sc1\n"
        "ds_read_b64_tr_b16 a[46:47], v155 offset:28672\n"
        "v_mfma_f32_16x16x32_bf16 v[52:55], v[172:175], a[212:215], 0\n"
        "ds_read_b128 a[0:3], v149 offset:32768\n"
        "v_mul_f32 v83, s54, v83\n"
        "v_mfma_f32_16x16x32_bf16 v[52:55], v[164:167], a[208:211], v[52:55]\n"
        "ds_read_b128 a[4:7], v149 offset:40960\n"
        "v_cvt_pk_bf16_f32 v80, 0, v80\n"
        "v_mfma_f32_16x16x32_bf16 v[60:63], v[164:167], a[216:219], 0\n"
        "global_load_lds_dwordx4 v254, s[20:21] offset:1024 sc1\n"
        "ds_read_b128 a[8:11], v149 offset:33792\n"
        "v_mfma_f32_16x16x32_bf16 v[60:63], v[172:175], a[220:223], v[60:63]\n"
        "ds_read_b128 a[12:15], v149 offset:41984\n"
        "v_cvt_pk_bf16_f32 v81, 0, v81\n"
        "v_mfma_f32_16x16x32_bf16 v[56:59], v[168:171], a[220:223], 0\n"
        "ds_read_b128 a[16:19], v149 offset:34816\n"
        "v_cvt_pk_bf16_f32 v82, 0, v82\n"
        "v_mfma_f32_16x16x32_bf16 v[56:59], v[160:163], a[216:219], v[56:59]\n"
        "global_load_lds_dwordx4 v254, s[20:21] offset:2048 sc1\n"
        "ds_read_b128 a[20:23], v149 offset:43008\n"
        "ds_read_b128 a[24:27], v149 offset:35840\n"
        "ds_read_b128 a[28:31], v149 offset:44032\n"
        "ds_read_b128 a[192:195], v152 offset:32768\n"
        "ds_read_b128 a[196:199], v152 offset:40960\n"
        "s_waitcnt lgkmcnt(9)\n"
        "v_mfma_f32_16x16x32_bf16 v[0:3], a[32:35], a[0:3], v[0:3]\n"
        "global_load_lds_dwordx4 v254, s[20:21] offset:3072 sc1\n"
        "s_add_u32 m0, s73, 0x14000\n"
        "s_waitcnt lgkmcnt(8)\n"
        "v_mfma_f32_16x16x32_bf16 v[0:3], a[40:43], a[4:7], v[0:3]\n"
        "s_add_u32 s20, s20, 0x4000\n"
        "s_addc_u32 s21, s21, 0\n"
        "v_cvt_pk_bf16_f32 v83, 0, v83\n"
        "v_mfma_f32_16x16x32_bf16 v[4:7], a[44:47], a[4:7], v[4:7]\n"
        "ds_read_b128 a[200:203], v152 offset:33792\n"
        "v_cvt_pk_bf16_f32 v128, 0, v128\n"
        "v_mfma_f32_16x16x32_bf16 v[4:7], a[36:39], a[0:3], v[4:7]\n"
        "global_load_lds_dwordx4 v144, s[60:61] offset:0\n"
        "s_waitcnt lgkmcnt(7)\n"
        "v_mfma_f32_16x16x32_bf16 v[12:15], a[36:39], a[8:11], v[12:15]\n"
        "ds_read_b128 a[204:207], v152 offset:41984\n"
        "v_cvt_pk_bf16_f32 v129, 0, v129\n"
        "v_mfma_f32_16x16x32_bf16 v[12:15], a[44:47], a[12:15], v[12:15]\n"
        "ds_read_b128 a[208:211], v152 offset:34816\n"
        "v_cvt_pk_bf16_f32 v130, 0, v130\n"
        "v_mfma_f32_16x16x32_bf16 v[8:11], a[40:43], a[12:15], v[8:11]\n"
        "global_load_lds_dwordx4 v145, s[60:61] offset:1024\n"
        "ds_read_b128 a[212:215], v152 offset:43008\n"
        "v_mfma_f32_16x16x32_bf16 v[8:11], a[32:35], a[8:11], v[8:11]\n"
        "ds_read_b128 a[216:219], v152 offset:35840\n"
        "s_waitcnt lgkmcnt(5)\n"
        "v_mfma_f32_16x16x32_bf16 v[16:19], a[32:35], a[16:19], v[16:19]\n"
        "ds_read_b128 a[220:223], v152 offset:44032\n"
        "v_cvt_pk_bf16_f32 v131, 0, v131\n"
        "v_mfma_f32_16x16x32_bf16 v[16:19], a[40:43], a[20:23], v[16:19]\n"
        "global_load_lds_dwordx4 v146, s[60:61] offset:2048\n"
        "v_add_f32 v80, v80, v128\n"
        "v_mfma_f32_16x16x32_bf16 v[20:23], a[44:47], a[20:23], v[20:23]\n"
        "s_waitcnt lgkmcnt(5)\n"
        "v_add_f32 v81, v81, v129\n"
        "v_add_f32 v82, v82, v130\n"
        "v_mfma_f32_16x16x32_bf16 v[20:23], a[36:39], a[16:19], v[20:23]\n"
        "s_waitcnt vmcnt(23)\n"
        "s_barrier\n"
        "v_add_f32 v83, v83, v131\n"
        "v_mfma_f32_16x16x32_bf16 v[28:31], a[36:39], a[24:27], v[28:31]\n"
        "global_load_lds_dwordx4 v147, s[60:61] offset:3072\n"
        "s_add_u32 m0, s73, 0x0\n"
        "v_and_b32 v84, 0xffff0000, v75\n"
        "v_mfma_f32_16x16x32_bf16 v[28:31], a[44:47], a[28:31], v[28:31]\n"
        "ds_read_b128 a[0:3], v149 offset:49152\n"
        "v_lshlrev_b32 v85, 16, v75\n"
        "v_mfma_f32_16x16x32_bf16 v[24:27], a[40:43], a[28:31], v[24:27]\n"
        "ds_read_b128 a[4:7], v149 offset:57344\n"
        "v_and_b32 v86, 0xffff0000, v74\n"
        "v_mfma_f32_16x16x32_bf16 v[24:27], a[32:35], a[24:27], v[24:27]\n"
        "global_load_lds_dwordx4 v254, s[20:21] offset:0 sc1\n"
        "ds_read_b128 a[8:11], v149 offset:50176\n"
        "v_mfma_f32_16x16x32_bf16 v[32:35], a[32:35], a[192:195], v[32:35]\n"
        "ds_read_b128 a[12:15], v149 offset:58368\n"
        "v_lshlrev_b32 v87, 16, v74\n"
        "v_mfma_f32_16x16x32_bf16 v[32:35], a[40:43], a[196:199], v[32:35]\n"
        "ds_read_b128 a[16:19], v149 offset:51200\n"
        "v_mul_f32 v84, s54, v84\n"
        "v_mfma_f32_16x16x32_bf16 v[36:39], a[44:47], a[196:199], v[36:39]\n"
        "global_load_lds_dwordx4 v254, s[20:21] offset:1024 sc1\n"
        "ds_read_b128 a[20:23], v149 offset:59392\n"
        "v_mfma_f32_16x16x32_bf16 v[36:39], a[36:39], a[192:195], v[36:39]\n"
        "ds_read_b128 a[24:27], v149 offset:52224\n"
        "v_mul_f32 v85, s54, v85\n"
        "v_mfma_f32_16x16x32_bf16 v[44:47], a[36:39], a[200:203], v[44:47]\n"
        "ds_read_b128 a[28:31], v149 offset:60416\n"
        "s_waitcnt lgkmcnt(8)\n"
        "v_mfma_f32_16x16x32_bf16 v[44:47], a[44:47], a[204:207], v[44:47]\n"
        "global_load_lds_dwordx4 v254, s[20:21] offset:2048 sc1\n"
        "LHB2_%=:\n"
        "s_cbranch_execz LHB6_%=\n"
        "ds_read_b128 a[192:195], v152 offset:49152\n"
        "v_mfma_f32_16x16x32_bf16 v[40:43], a[40:43], a[204:207], v[40:43]\n"
        "ds_read_b128 a[196:199], v152 offset:57344\n"
        "v_mul_f32 v86, s54, v86\n"
        "v_mfma_f32_16x16x32_bf16 v[40:43], a[32:35], a[200:203], v[40:43]\n"
        "s_waitcnt lgkmcnt(8)\n"
        "v_mul_f32 v87, s54, v87\n"
        "v_cvt_pk_bf16_f32 v84, 0, v84\n"
        "v_mfma_f32_16x16x32_bf16 v[48:51], a[32:35], a[208:211], v[48:51]\n"
        "global_load_lds_dwordx4 v254, s[20:21] offset:3072 sc1\n"
        "s_add_u32 s20, s20, 0x4000\n"
        "s_addc_u32 s21, s21, 0\n"
        "v_mfma_f32_16x16x32_bf16 v[48:51], a[40:43], a[212:215], v[48:51]\n"
        "s_waitcnt lgkmcnt(5)\n"
        "v_cvt_pk_bf16_f32 v85, 0, v85\n"
        "v_cvt_pk_bf16_f32 v86, 0, v86\n"
        "v_mfma_f32_16x16x32_bf16 v[52:55], a[44:47], a[212:215], v[52:55]\n"
        "ds_read_b128 a[200:203], v152 offset:50176\n"
        "s_add_u32 m0, s73, 0x4000\n"
        "v_cvt_pk_bf16_f32 v87, 0, v87\n"
        "v_mfma_f32_16x16x32_bf16 v[52:55], a[36:39], a[208:211], v[52:55]\n"
        "global_store_dwordx4 v229, v[88:91], s[24:25] offset:0 sc1\n"
        "ds_read_b128 a[204:207], v152 offset:58368\n"
        "ds_read_b128 a[208:211], v152 offset:51200\n"
        "v_cvt_pk_bf16_f32 v88, v80, v81\n"
        "v_mfma_f32_16x16x32_bf16 v[60:63], a[36:39], a[216:219], v[60:63]\n"
        "ds_read_b128 a[212:215], v152 offset:59392\n"
        "v_cvt_pk_bf16_f32 v89, v82, v83\n"
        "v_mfma_f32_16x16x32_bf16 v[60:63], a[44:47], a[220:223], v[60:63]\n"
        "s_add_u32 s60, s22, 0xe0000\n"
        "s_addc_u32 s61, s23, 0\n"
        "v_cvt_pk_bf16_f32 v132, 0, v132\n"
        "v_mfma_f32_16x16x32_bf16 v[56:59], a[40:43], a[220:223], v[56:59]\n"
        "global_store_dwordx4 v229, v[92:95], s[24:25] offset:1024 sc1\n"
        "ds_read_b128 a[220:223], v152 offset:60416\n"
        "v_cvt_pk_bf16_f32 v133, 0, v133\n"
        "v_cvt_pk_bf16_f32 v134, 0, v134\n"
        "v_cvt_pk_bf16_f32 v135, 0, v135\n"
        "v_mfma_f32_16x16x32_bf16 v[56:59], a[32:35], a[216:219], v[56:59]\n"
        "s_waitcnt lgkmcnt(5)\n"
        "v_add_f32 v84, v84, v132\n"
        "v_add_f32 v85, v85, v133\n"
        "v_mfma_f32_16x16x32_bf16 v[0:3], v[176:179], a[0:3], v[0:3]\n"
        "ds_read_b128 a[216:219], v152 offset:52224\n"
        "v_add_f32 v86, v86, v134\n"
        "v_mfma_f32_16x16x32_bf16 v[0:3], v[184:187], a[4:7], v[0:3]\n"
        "v_add_f32 v87, v87, v135\n"
        "v_cvt_pk_bf16_f32 v90, v84, v85\n"
        "v_mfma_f32_16x16x32_bf16 v[4:7], v[188:191], a[4:7], v[4:7]\n"
        "v_cvt_pk_bf16_f32 v91, v86, v87\n"
        "v_and_b32 v80, 0xffff0000, v77\n"
        "v_mfma_f32_16x16x32_bf16 v[4:7], v[180:183], a[0:3], v[4:7]\n"
        "v_permlane16_swap_b32 v88, v90\n"
        "v_mfma_f32_16x16x32_bf16 v[12:15], v[180:183], a[8:11], v[12:15]\n"
        "v_permlane16_swap_b32 v89, v91\n"
        "v_mfma_f32_16x16x32_bf16 v[12:15], v[188:191], a[12:15], v[12:15]\n"
        "global_store_dwordx4 v229, v[88:91], s[24:25] offset:2048 sc1\n"
        "v_lshlrev_b32 v81, 16, v77\n"
        "v_and_b32 v82, 0xffff0000, v76\n"
        "v_lshlrev_b32 v83, 16, v76\n"
        "v_mul_f32 v80, s54, v80\n"
        "v_mfma_f32_16x16x32_bf16 v[8:11], v[184:187], a[12:15], v[8:11]\n"
        "v_mul_f32 v81, s54, v81\n"
        "v_mul_f32 v82, s54, v82\n"
        "v_mfma_f32_16x16x32_bf16 v[8:11], v[176:179], a[8:11], v[8:11]\n"
        "v_mul_f32 v83, s54, v83\n"
        "v_cvt_pk_bf16_f32 v80, 0, v80\n"
        "v_mfma_f32_16x16x32_bf16 v[16:19], v[176:179], a[16:19], v[16:19]\n"
        "v_cvt_pk_bf16_f32 v81, 0, v81\n"
        "v_cvt_pk_bf16_f32 v82, 0, v82\n"
        "v_mfma_f32_16x16x32_bf16 v[16:19], v[184:187], a[20:23], v[16:19]\n"
        "s_waitcnt lgkmcnt(0)\n"
        "v_cvt_pk_bf16_f32 v83, 0, v83\n"
        "v_cvt_pk_bf16_f32 v136, 0, v136\n"
        "v_mfma_f32_16x16x32_bf16 v[20:23], v[188:191], a[20:23], v[20:23]\n"
        "v_cvt_pk_bf16_f32 v137, 0, v137\n"
        "v_cvt_pk_bf16_f32 v138, 0, v138\n"
        "v_mfma_f32_16x16x32_bf16 v[20:23], v[180:183], a[16:19], v[20:23]\n"
        "s_waitcnt vmcnt(23)\n"
        "v_cvt_pk_bf16_f32 v139, 0, v139\n"
        "v_add_f32 v80, v80, v136\n"
        "v_mfma_f32_16x16x32_bf16 v[28:31], v[180:183], a[24:27], v[28:31]\n"
        "s_barrier\n"
        "v_add_f32 v81, v81, v137\n"
        "v_add_f32 v82, v82, v138\n"
        "v_mfma_f32_16x16x32_bf16 v[28:31], v[188:191], a[28:31], v[28:31]\n"
        "global_load_lds_dwordx4 v144, s[60:61] offset:0\n"
        "v_add_f32 v83, v83, v139\n"
        "v_mfma_f32_16x16x32_bf16 v[24:27], v[184:187], a[28:31], v[24:27]\n"
        "ds_read_b64_tr_b16 a[48:49], v158 offset:0\n"
        "v_cvt_pk_bf16_f32 v92, v80, v81\n"
        "v_mfma_f32_16x16x32_bf16 v[24:27], v[176:179], a[24:27], v[24:27]\n"
        "ds_read_b64_tr_b16 a[50:51], v158 offset:4096\n"
        "v_cvt_pk_bf16_f32 v93, v82, v83\n"
        "v_mfma_f32_16x16x32_bf16 v[32:35], v[176:179], a[192:195], v[32:35]\n"
        "ds_read_b64_tr_b16 a[56:57], v158 offset:8192\n"
        "v_and_b32 v84, 0xffff0000, v79\n"
        "v_mfma_f32_16x16x32_bf16 v[32:35], v[184:187], a[196:199], v[32:35]\n"
        "ds_read_b64_tr_b16 a[58:59], v158 offset:12288\n"
        "v_lshlrev_b32 v85, 16, v79\n"
        "v_mfma_f32_16x16x32_bf16 v[36:39], v[188:191], a[196:199], v[36:39]\n"
        "ds_read_b64_tr_b16 a[52:53], v159 offset:0\n"
        "v_and_b32 v86, 0xffff0000, v78\n"
        "v_mfma_f32_16x16x32_bf16 v[36:39], v[180:183], a[192:195], v[36:39]\n"
        "ds_read_b64_tr_b16 a[54:55], v159 offset:4096\n"
        "v_lshlrev_b32 v87, 16, v78\n"
        "v_mfma_f32_16x16x32_bf16 v[44:47], v[180:183], a[200:203], v[44:47]\n"
        "ds_read_b64_tr_b16 a[60:61], v159 offset:8192\n"
        "v_mul_f32 v84, s54, v84\n"
        "v_mfma_f32_16x16x32_bf16 v[44:47], v[188:191], a[204:207], v[44:47]\n"
        "ds_read_b64_tr_b16 a[62:63], v159 offset:12288\n"
        "v_mul_f32 v85, s54, v85\n"
        "v_mfma_f32_16x16x32_bf16 v[40:43], v[184:187], a[204:207], v[40:43]\n"
        "ds_read_b128 a[0:3], v150 offset:16384\n"
        "v_mul_f32 v86, s54, v86\n"
        "v_mfma_f32_16x16x32_bf16 v[40:43], v[176:179], a[200:203], v[40:43]\n"
        "ds_read_b128 a[4:7], v150 offset:24576\n"
        "v_mul_f32 v87, s54, v87\n"
        "v_mfma_f32_16x16x32_bf16 v[48:51], v[176:179], a[208:211], v[48:51]\n"
        "global_load_lds_dwordx4 v145, s[60:61] offset:1024\n"
        "v_cvt_pk_bf16_f32 v84, 0, v84\n"
        "v_mfma_f32_16x16x32_bf16 v[48:51], v[184:187], a[212:215], v[48:51]\n"
        "ds_read_b128 a[8:11], v150 offset:17408\n"
        "v_cvt_pk_bf16_f32 v85, 0, v85\n"
        "v_mfma_f32_16x16x32_bf16 v[52:55], v[188:191], a[212:215], v[52:55]\n"
        "ds_read_b128 a[12:15], v150 offset:25600\n"
        "v_cvt_pk_bf16_f32 v86, 0, v86\n"
        "v_mfma_f32_16x16x32_bf16 v[52:55], v[180:183], a[208:211], v[52:55]\n"
        "global_load_lds_dwordx4 v146, s[60:61] offset:2048\n"
        "v_cvt_pk_bf16_f32 v87, 0, v87\n"
        "v_mfma_f32_16x16x32_bf16 v[60:63], v[180:183], a[216:219], v[60:63]\n"
        "ds_read_b128 a[16:19], v150 offset:18432\n"
        "v_cvt_pk_bf16_f32 v140, 0, v140\n"
        "v_mfma_f32_16x16x32_bf16 v[60:63], v[188:191], a[220:223], v[60:63]\n"
        "s_waitcnt lgkmcnt(5)\n"
        "v_cvt_pk_bf16_f32 v141, 0, v141\n"
        "v_cvt_pk_bf16_f32 v142, 0, v142\n"
        "v_mfma_f32_16x16x32_bf16 v[56:59], v[184:187], a[220:223], v[56:59]\n"
        "s_waitcnt lgkmcnt(4)\n"
        "v_cvt_pk_bf16_f32 v143, 0, v143\n"
        "v_add_f32 v84, v84, v140\n"
        "v_mfma_f32_16x16x32_bf16 v[56:59], v[176:179], a[216:219], v[56:59]\n"
        "s_waitcnt lgkmcnt(3)\n"
        "v_add_f32 v85, v85, v141\n"
        "v_add_f32 v86, v86, v142\n"
        "v_mfma_f32_16x16x32_bf16 v[0:3], a[48:51], a[0:3], v[0:3]\n"
        "ds_read_b128 a[20:23], v150 offset:26624\n"
        "v_add_f32 v87, v87, v143\n"
        "v_mfma_f32_16x16x32_bf16 v[0:3], a[56:59], a[4:7], v[0:3]\n"
        "s_waitcnt lgkmcnt(3)\n"
        "v_cvt_pk_bf16_f32 v94, v84, v85\n"
        "v_cvt_pk_bf16_f32 v95, v86, v87\n"
        "v_mfma_f32_16x16x32_bf16 v[4:7], a[60:63], a[4:7], v[4:7]\n"
        "s_waitcnt lgkmcnt(2)\n"
        "v_permlane16_swap_b32 v92, v94\n"
        "v_mfma_f32_16x16x32_bf16 v[4:7], a[52:55], a[0:3], v[4:7]\n"
        "v_permlane16_swap_b32 v93, v95\n"
        "v_mfma_f32_16x16x32_bf16 v[12:15], a[52:55], a[8:11], v[12:15]\n"
        "global_store_dwordx4 v229, v[92:95], s[24:25] offset:3072 sc1\n"
        "ds_read_b128 a[24:27], v150 offset:19456\n"
        "ds_read_b128 a[28:31], v150 offset:27648\n"
        "v_and_b32 v226, 1, v233\n"
        "v_mfma_f32_16x16x32_bf16 v[12:15], a[60:63], a[12:15], v[12:15]\n"
        "ds_read_b128 a[192:195], v153 offset:16384\n"
        "v_lshlrev_b32 v227, 11, v232\n"
        "v_mfma_f32_16x16x32_bf16 v[8:11], a[56:59], a[12:15], v[8:11]\n"
        "global_load_lds_dwordx4 v147, s[60:61] offset:3072\n"
        "s_add_u32 m0, s73, 0x18000\n"
        "v_mfma_f32_16x16x32_bf16 v[8:11], a[48:51], a[8:11], v[8:11]\n"
        "ds_read_b128 a[196:199], v153 offset:24576\n"
        "s_waitcnt lgkmcnt(4)\n"
        "v_mfma_f32_16x16x32_bf16 v[16:19], a[48:51], a[16:19], v[16:19]\n"
        "ds_read_b128 a[200:203], v153 offset:17408\n"
        "s_add_u32 s60, s22, 0x120000\n"
        "s_addc_u32 s61, s23, 0\n"
        "v_mfma_f32_16x16x32_bf16 v[16:19], a[56:59], a[20:23], v[16:19]\n"
        "global_load_lds_dwordx4 v254, s[20:21] offset:0 sc1\n"
        "v_mfma_f32_16x16x32_bf16 v[20:23], a[60:63], a[20:23], v[20:23]\n"
        "ds_read_b128 a[204:207], v153 offset:25600\n"
        "v_lshl_add_u32 v227, v226, 5, v227\n"
        "v_mfma_f32_16x16x32_bf16 v[20:23], a[52:55], a[16:19], v[20:23]\n"
        "ds_read_b128 a[208:211], v153 offset:18432\n"
        "s_waitcnt lgkmcnt(6)\n"
        "v_mfma_f32_16x16x32_bf16 v[28:31], a[52:55], a[24:27], v[28:31]\n"
        "s_waitcnt lgkmcnt(5)\n"
        "global_load_lds_dwordx4 v254, s[20:21] offset:1024 sc1\n"
        "v_mfma_f32_16x16x32_bf16 v[28:31], a[60:63], a[28:31], v[28:31]\n"
        "ds_read_b128 a[212:215], v153 offset:26624\n"
        "s_waitcnt vmcnt(22)\n"
        "v_mfma_f32_16x16x32_bf16 v[24:27], a[56:59], a[28:31], v[24:27]\n"
        "ds_read_b128 a[216:219], v153 offset:19456\n"
        "s_barrier\n"
        "v_lshrrev_b32 v226, 1, v233\n"
        "v_mfma_f32_16x16x32_bf16 v[24:27], a[48:51], a[24:27], v[24:27]\n"
        "s_waitcnt lgkmcnt(5)\n"
        "global_load_lds_dwordx4 v254, s[20:21] offset:2048 sc1\n"
        "v_mfma_f32_16x16x32_bf16 v[32:35], a[48:51], a[192:195], v[32:35]\n"
        "ds_read_b128 a[220:223], v153 offset:27648\n"
        "v_lshl_add_u32 v227, v226, 4, v227\n"
        "v_mfma_f32_16x16x32_bf16 v[32:35], a[56:59], a[196:199], v[32:35]\n"
        "ds_read_b64_tr_b16 a[224:225], v154 offset:32768\n"
        "v_and_b32 v226, 0x70, v254\n"
        "v_mfma_f32_16x16x32_bf16 v[36:39], a[60:63], a[196:199], v[36:39]\n"
        "global_load_lds_dwordx4 v254, s[20:21] offset:3072 sc1\n"
        "s_add_u32 m0, s73, 0x1c000\n"
        "v_mfma_f32_16x16x32_bf16 v[36:39], a[52:55], a[192:195], v[36:39]\n"
        "s_add_u32 s20, s20, 0x4000\n"
        "s_addc_u32 s21, s21, 0\n"
        "s_waitcnt lgkmcnt(4)\n"
        "v_mfma_f32_16x16x32_bf16 v[44:47], a[52:55], a[200:203], v[44:47]\n"
        "ds_read_b64_tr_b16 a[226:227], v154 offset:36864\n"
        "v_add_u32 v224, v224, v226\n"
        "v_mfma_f32_16x16x32_bf16 v[44:47], a[60:63], a[204:207], v[44:47]\n"
        "global_load_lds_dwordx4 v254, s[20:21] offset:0 sc1\n"
        "v_mfma_f32_16x16x32_bf16 v[40:43], a[56:59], a[204:207], v[40:43]\n"
        "ds_read_b64_tr_b16 a[232:233], v154 offset:40960\n"
        "v_lshrrev_b32 v225, 6, v254\n"
        "v_mfma_f32_16x16x32_bf16 v[40:43], a[48:51], a[200:203], v[40:43]\n"
        "ds_read_b64_tr_b16 a[234:235], v154 offset:45056\n"
        "v_lshlrev_b32 v225, 7, v225\n"
        "v_mfma_f32_16x16x32_bf16 v[48:51], a[48:51], a[208:211], v[48:51]\n"
        "ds_read_b64_tr_b16 a[228:229], v155 offset:32768\n"
        "s_waitcnt lgkmcnt(5)\n"
        "v_mfma_f32_16x16x32_bf16 v[48:51], a[56:59], a[212:215], v[48:51]\n"
        "ds_read_b64_tr_b16 a[230:231], v155 offset:36864\n"
        "global_load_lds_dwordx4 v254, s[20:21] offset:1024 sc1\n"
        "v_mfma_f32_16x16x32_bf16 v[52:55], a[60:63], a[212:215], v[52:55]\n"
        "ds_read_b64_tr_b16 a[236:237], v155 offset:40960\n"
        "v_bfe_u32 v226, v254, 4, 2\n"
        "v_mfma_f32_16x16x32_bf16 v[52:55], a[52:55], a[208:211], v[52:55]\n"
        "ds_read_b64_tr_b16 a[238:239], v155 offset:45056\n"
        "v_lshl_add_u32 v225, v226, 3, v225\n"
        "v_mfma_f32_16x16x32_bf16 v[60:63], a[52:55], a[216:219], v[60:63]\n"
        "ds_read_b128 a[0:3], v148 offset:49152\n"
        "global_load_lds_dwordx4 v254, s[20:21] offset:2048 sc1\n"
        "v_mfma_f32_16x16x32_bf16 v[60:63], a[60:63], a[220:223], v[60:63]\n"
        "ds_read_b128 a[4:7], v148 offset:57344\n"
        "v_mov_b32 v136, 0\n"
        "v_mfma_f32_16x16x32_bf16 v[56:59], a[56:59], a[220:223], v[56:59]\n"
        "ds_read_b128 a[8:11], v148 offset:50176\n"
        "v_mov_b32 v137, 0\n"
        "v_mfma_f32_16x16x32_bf16 v[56:59], a[48:51], a[216:219], v[56:59]\n"
        "ds_read_b128 a[12:15], v148 offset:58368\n"
        "ds_read_b128 a[16:19], v148 offset:51200\n"
        "ds_read_b128 a[20:23], v148 offset:59392\n"
        "global_load_lds_dwordx4 v254, s[20:21] offset:3072 sc1\n"
        "ds_read_b128 a[24:27], v148 offset:52224\n"
        "ds_read_b128 a[28:31], v148 offset:60416\n"
        "s_add_u32 s20, s20, 0x4000\n"
        "s_addc_u32 s21, s21, 0\n"
        "ds_read_b128 a[192:195], v151 offset:49152\n"
        "ds_read_b128 a[196:199], v151 offset:57344\n"
        "ds_read_b128 a[200:203], v151 offset:50176\n"
        "ds_read_b128 a[204:207], v151 offset:58368\n"
        "s_add_u32 m0, s73, 0x20000\n"
        "s_waitcnt lgkmcnt(11)\n"
        "v_mfma_f32_16x16x32_bf16 v[0:3], a[224:227], a[0:3], v[0:3]\n"
        "ds_read_b128 a[208:211], v151 offset:51200\n"
        "s_waitcnt lgkmcnt(11)\n"
        "v_mfma_f32_16x16x32_bf16 v[0:3], a[232:235], a[4:7], v[0:3]\n"
        "ds_read_b128 a[212:215], v151 offset:59392\n"
        "v_mov_b32 v140, 0\n"
        "v_mfma_f32_16x16x32_bf16 v[4:7], a[236:239], a[4:7], v[4:7]\n"
        "ds_read_b128 a[216:219], v151 offset:52224\n"
        "v_mov_b32 v141, 0\n"
        "v_mfma_f32_16x16x32_bf16 v[4:7], a[228:231], a[0:3], v[4:7]\n"
        "ds_read_b128 a[220:223], v151 offset:60416\n"
        "s_waitcnt lgkmcnt(11)\n"
        "v_mfma_f32_16x16x32_bf16 v[12:15], a[228:231], a[8:11], v[12:15]\n"
        "s_waitcnt lgkmcnt(10)\n"
        "v_add_u32 v225, s16, v225\n"
        "s_waitcnt lgkmcnt(9)\n"
        "v_mfma_f32_16x16x32_bf16 v[12:15], a[236:239], a[12:15], v[12:15]\n"
        "s_waitcnt lgkmcnt(8)\n"
        "s_waitcnt lgkmcnt(7)\n"
        "v_mfma_f32_16x16x32_bf16 v[8:11], a[232:235], a[12:15], v[8:11]\n"
        "v_mfma_f32_16x16x32_bf16 v[8:11], a[224:227], a[8:11], v[8:11]\n"
        "s_waitcnt lgkmcnt(4)\n"
        "v_mfma_f32_16x16x32_bf16 v[16:19], a[224:227], a[16:19], v[16:19]\n"
        "v_mfma_f32_16x16x32_bf16 v[16:19], a[232:235], a[20:23], v[16:19]\n"
        "v_mfma_f32_16x16x32_bf16 v[20:23], a[236:239], a[20:23], v[20:23]\n"
        "v_mfma_f32_16x16x32_bf16 v[20:23], a[228:231], a[16:19], v[20:23]\n"
        "s_waitcnt vmcnt(24)\n"
        "s_barrier\n"
        "v_mfma_f32_16x16x32_bf16 v[28:31], a[228:231], a[24:27], v[28:31]\n"
        "global_load_lds_dwordx4 v144, s[60:61] offset:0\n"
        "ds_read_b128 a[0:3], v149 offset:0\n"
        "v_mfma_f32_16x16x32_bf16 v[28:31], a[236:239], a[28:31], v[28:31]\n"
        "ds_read_b128 a[4:7], v149 offset:8192\n"
        "s_waitcnt lgkmcnt(2)\n"
        "v_mfma_f32_16x16x32_bf16 v[24:27], a[232:235], a[28:31], v[24:27]\n"
        "ds_read_b128 a[8:11], v149 offset:1024\n"
        "v_mfma_f32_16x16x32_bf16 v[24:27], a[224:227], a[24:27], v[24:27]\n"
        "global_load_lds_dwordx4 v145, s[60:61] offset:1024\n"
        "ds_read_b128 a[12:15], v149 offset:9216\n"
        "v_mfma_f32_16x16x32_bf16 v[32:35], a[224:227], a[192:195], v[32:35]\n"
        "ds_read_b128 a[16:19], v149 offset:2048\n"
        "v_mfma_f32_16x16x32_bf16 v[32:35], a[232:235], a[196:199], v[32:35]\n"
        "ds_read_b128 a[20:23], v149 offset:10240\n"
        "v_mfma_f32_16x16x32_bf16 v[36:39], a[236:239], a[196:199], v[36:39]\n"
        "global_load_lds_dwordx4 v146, s[60:61] offset:2048\n"
        "ds_read_b128 a[24:27], v149 offset:3072\n"
        "v_mfma_f32_16x16x32_bf16 v[36:39], a[228:231], a[192:195], v[36:39]\n"
        "ds_read_b128 a[28:31], v149 offset:11264\n"
        "v_mfma_f32_16x16x32_bf16 v[44:47], a[228:231], a[200:203], v[44:47]\n"
        "ds_read_b128 a[192:195], v152 offset:0\n"
        "v_mfma_f32_16x16x32_bf16 v[44:47], a[236:239], a[204:207], v[44:47]\n"
        "global_load_lds_dwordx4 v147, s[60:61] offset:3072\n"
        "s_add_u32 m0, s73, 0x24000\n"
        "s_waitcnt lgkmcnt(7)\n"
        "v_mfma_f32_16x16x32_bf16 v[40:43], a[232:235], a[204:207], v[40:43]\n"
        "ds_read_b128 a[196:199], v152 offset:8192\n"
        "s_add_u32 s60, s22, 0x160000\n"
        "s_addc_u32 s61, s23, 0\n"
        "v_mfma_f32_16x16x32_bf16 v[40:43], a[224:227], a[200:203], v[40:43]\n"
        "ds_read_b128 a[200:203], v152 offset:1024\n"
        "v_mfma_f32_16x16x32_bf16 v[48:51], a[224:227], a[208:211], v[48:51]\n"
        "global_load_lds_dwordx4 v254, s[20:21] offset:0 sc1\n"
        "ds_read_b128 a[204:207], v152 offset:9216\n"
        "v_mfma_f32_16x16x32_bf16 v[48:51], a[232:235], a[212:215], v[48:51]\n"
        "s_waitcnt lgkmcnt(6)\n"
        "v_mfma_f32_16x16x32_bf16 v[52:55], a[236:239], a[212:215], v[52:55]\n"
        "ds_read_b128 a[212:215], v152 offset:10240\n"
        "v_mfma_f32_16x16x32_bf16 v[52:55], a[228:231], a[208:211], v[52:55]\n"
        "global_load_lds_dwordx4 v254, s[20:21] offset:1024 sc1\n"
        "ds_read_b128 a[208:211], v152 offset:2048\n"
        "v_mfma_f32_16x16x32_bf16 v[60:63], a[228:231], a[216:219], v[60:63]\n"
        "v_mfma_f32_16x16x32_bf16 v[60:63], a[236:239], a[220:223], v[60:63]\n"
        "v_mfma_f32_16x16x32_bf16 v[56:59], a[232:235], a[220:223], v[56:59]\n"
        "global_load_lds_dwordx4 v254, s[20:21] offset:2048 sc1\n"
        "ds_read_b128 a[220:223], v152 offset:11264\n"
        "v_mfma_f32_16x16x32_bf16 v[56:59], a[224:227], a[216:219], v[56:59]\n"
        "ds_read_b128 a[216:219], v152 offset:3072\n"
        "v_mfma_f32_16x16x32_bf16 v[0:3], v[192:195], a[0:3], v[0:3]\n"
        "v_mfma_f32_16x16x32_bf16 v[0:3], v[200:203], a[4:7], v[0:3]\n"
        "global_load_lds_dwordx4 v254, s[20:21] offset:3072 sc1\n"
        "s_add_u32 s20, s20, 0x4000\n"
        "s_addc_u32 s21, s21, 0\n"
        "v_mfma_f32_16x16x32_bf16 v[4:7], v[204:207], a[4:7], v[4:7]\n"
        "s_add_u32 m0, s73, 0x8000\n"
        "s_waitcnt lgkmcnt(3)\n"
        "v_mfma_f32_16x16x32_bf16 v[4:7], v[196:199], a[0:3], v[4:7]\n"
        "v_mfma_f32_16x16x32_bf16 v[12:15], v[196:199], a[8:11], v[12:15]\n"
        "v_mfma_f32_16x16x32_bf16 v[12:15], v[204:207], a[12:15], v[12:15]\n"
        "v_mfma_f32_16x16x32_bf16 v[8:11], v[200:203], a[12:15], v[8:11]\n"
        "v_mfma_f32_16x16x32_bf16 v[8:11], v[192:195], a[8:11], v[8:11]\n"
        "v_mfma_f32_16x16x32_bf16 v[16:19], v[192:195], a[16:19], v[16:19]\n"
        "v_mfma_f32_16x16x32_bf16 v[16:19], v[200:203], a[20:23], v[16:19]\n"
        "v_mfma_f32_16x16x32_bf16 v[20:23], v[204:207], a[20:23], v[20:23]\n"
        "v_mfma_f32_16x16x32_bf16 v[20:23], v[196:199], a[16:19], v[20:23]\n"
        "s_waitcnt vmcnt(24)\n"
        "s_barrier\n"
        "v_mfma_f32_16x16x32_bf16 v[28:31], v[196:199], a[24:27], v[28:31]\n"
        "global_load_lds_dwordx4 v254, s[20:21] offset:0 sc1\n"
        "ds_read_b64_tr_b16 a[240:241], v156 offset:16384\n"
        "v_mfma_f32_16x16x32_bf16 v[28:31], v[204:207], a[28:31], v[28:31]\n"
        "ds_read_b64_tr_b16 a[242:243], v156 offset:20480\n"
        "s_waitcnt lgkmcnt(2)\n"
        "v_mfma_f32_16x16x32_bf16 v[24:27], v[200:203], a[28:31], v[24:27]\n"
        "ds_read_b64_tr_b16 a[248:249], v156 offset:24576\n"
        "v_mfma_f32_16x16x32_bf16 v[24:27], v[192:195], a[24:27], v[24:27]\n"
        "ds_read_b64_tr_b16 a[250:251], v156 offset:28672\n"
        "global_load_lds_dwordx4 v254, s[20:21] offset:1024 sc1\n"
        "v_mfma_f32_16x16x32_bf16 v[32:35], v[192:195], a[192:195], v[32:35]\n"
        "ds_read_b64_tr_b16 a[244:245], v157 offset:16384\n"
        "v_mfma_f32_16x16x32_bf16 v[32:35], v[200:203], a[196:199], v[32:35]\n"
        "ds_read_b64_tr_b16 a[246:247], v157 offset:20480\n"
        "v_mfma_f32_16x16x32_bf16 v[36:39], v[204:207], a[196:199], v[36:39]\n"
        "ds_read_b64_tr_b16 a[252:253], v157 offset:24576\n"
        "global_load_lds_dwordx4 v254, s[20:21] offset:2048 sc1\n"
        "v_mfma_f32_16x16x32_bf16 v[36:39], v[196:199], a[192:195], v[36:39]\n"
        "ds_read_b64_tr_b16 a[254:255], v157 offset:28672\n"
        "v_mfma_f32_16x16x32_bf16 v[44:47], v[196:199], a[200:203], v[44:47]\n"
        "ds_read_b128 a[0:3], v148 offset:0\n"
        "v_mfma_f32_16x16x32_bf16 v[44:47], v[204:207], a[204:207], v[44:47]\n"
        "ds_read_b128 a[4:7], v148 offset:8192\n"
        "global_load_lds_dwordx4 v254, s[20:21] offset:3072 sc1\n"
        "s_add_u32 m0, s73, 0xc000\n"
        "v_mfma_f32_16x16x32_bf16 v[40:43], v[200:203], a[204:207], v[40:43]\n"
        "ds_read_b128 a[8:11], v148 offset:1024\n"
        "s_add_u32 s20, s20, 0x4000\n"
        "s_addc_u32 s21, s21, 0\n"
        "v_mfma_f32_16x16x32_bf16 v[40:43], v[192:195], a[200:203], v[40:43]\n"
        "ds_read_b128 a[12:15], v148 offset:9216\n"
        "v_mfma_f32_16x16x32_bf16 v[48:51], v[192:195], a[208:211], v[48:51]\n"
        "ds_read_b128 a[16:19], v148 offset:2048\n"
        "global_load_lds_dwordx4 v144, s[60:61] offset:0\n"
        "v_mfma_f32_16x16x32_bf16 v[48:51], v[200:203], a[212:215], v[48:51]\n"
        "ds_read_b128 a[20:23], v148 offset:10240\n"
        "v_mfma_f32_16x16x32_bf16 v[52:55], v[204:207], a[212:215], v[52:55]\n"
        "ds_read_b128 a[24:27], v148 offset:3072\n"
        "v_mfma_f32_16x16x32_bf16 v[52:55], v[196:199], a[208:211], v[52:55]\n"
        "ds_read_b128 a[28:31], v148 offset:11264\n"
        "global_load_lds_dwordx4 v145, s[60:61] offset:1024\n"
        "v_mfma_f32_16x16x32_bf16 v[60:63], v[196:199], a[216:219], v[60:63]\n"
        "ds_read_b128 a[192:195], v151 offset:0\n"
        "s_waitcnt lgkmcnt(8)\n"
        "v_mfma_f32_16x16x32_bf16 v[60:63], v[204:207], a[220:223], v[60:63]\n"
        "ds_read_b128 a[196:199], v151 offset:8192\n"
        "s_waitcnt lgkmcnt(8)\n"
        "v_mfma_f32_16x16x32_bf16 v[56:59], v[200:203], a[220:223], v[56:59]\n"
        "global_load_lds_dwordx4 v146, s[60:61] offset:2048\n"
        "v_mfma_f32_16x16x32_bf16 v[56:59], v[192:195], a[216:219], v[56:59]\n"
        "ds_read_b128 a[200:203], v151 offset:1024\n"
        "s_waitcnt lgkmcnt(7)\n"
        "v_mfma_f32_16x16x32_bf16 v[0:3], a[240:243], a[0:3], v[0:3]\n"
        "ds_read_b128 a[204:207], v151 offset:9216\n"
        "s_waitcnt lgkmcnt(7)\n"
        "v_mfma_f32_16x16x32_bf16 v[0:3], a[248:251], a[4:7], v[0:3]\n"
        "global_load_lds_dwordx4 v147, s[60:61] offset:3072\n"
        "s_add_u32 m0, s73, 0x10000\n"
        "v_mfma_f32_16x16x32_bf16 v[4:7], a[252:255], a[4:7], v[4:7]\n"
        "ds_read_b128 a[208:211], v151 offset:2048\n"
        "s_add_u32 s60, s22, 0x180000\n"
        "s_addc_u32 s61, s23, 0\n"
        "v_mfma_f32_16x16x32_bf16 v[4:7], a[244:247], a[0:3], v[4:7]\n"
        "ds_read_b128 a[212:215], v151 offset:10240\n"
        "s_waitcnt lgkmcnt(6)\n"
        "v_mfma_f32_16x16x32_bf16 v[12:15], a[244:247], a[8:11], v[12:15]\n"
        "ds_read_b128 a[216:219], v151 offset:3072\n"
        "v_mfma_f32_16x16x32_bf16 v[12:15], a[252:255], a[12:15], v[12:15]\n"
        "ds_read_b128 a[220:223], v151 offset:11264\n"
        "s_waitcnt lgkmcnt(6)\n"
        "v_mfma_f32_16x16x32_bf16 v[8:11], a[248:251], a[12:15], v[8:11]\n"
        "v_mfma_f32_16x16x32_bf16 v[8:11], a[240:243], a[8:11], v[8:11]\n"
        "v_mfma_f32_16x16x32_bf16 v[16:19], a[240:243], a[16:19], v[16:19]\n"
        "v_mfma_f32_16x16x32_bf16 v[16:19], a[248:251], a[20:23], v[16:19]\n"
        "s_waitcnt lgkmcnt(4)\n"
        "v_mfma_f32_16x16x32_bf16 v[20:23], a[252:255], a[20:23], v[20:23]\n"
        "v_mfma_f32_16x16x32_bf16 v[20:23], a[244:247], a[16:19], v[20:23]\n"
        "s_waitcnt vmcnt(20)\n"
        "s_barrier\n"
        "v_mfma_f32_16x16x32_bf16 v[28:31], a[244:247], a[24:27], v[28:31]\n"
        "global_load_lds_dwordx4 v254, s[20:21] offset:0 sc1\n"
        "ds_read_b64_tr_b16 a[32:33], v154 offset:16384\n"
        "v_mfma_f32_16x16x32_bf16 v[28:31], a[252:255], a[28:31], v[28:31]\n"
        "ds_read_b64_tr_b16 a[34:35], v154 offset:20480\n"
        "s_waitcnt lgkmcnt(2)\n"
        "v_mfma_f32_16x16x32_bf16 v[24:27], a[248:251], a[28:31], v[24:27]\n"
        "ds_read_b64_tr_b16 a[40:41], v154 offset:24576\n"
        "v_mfma_f32_16x16x32_bf16 v[24:27], a[240:243], a[24:27], v[24:27]\n"
        "ds_read_b64_tr_b16 a[42:43], v154 offset:28672\n"
        "global_load_lds_dwordx4 v254, s[20:21] offset:1024 sc1\n"
        "v_mfma_f32_16x16x32_bf16 v[32:35], a[240:243], a[192:195], v[32:35]\n"
        "ds_read_b64_tr_b16 a[36:37], v155 offset:16384\n"
        "v_mfma_f32_16x16x32_bf16 v[32:35], a[248:251], a[196:199], v[32:35]\n"
        "ds_read_b64_tr_b16 a[38:39], v155 offset:20480\n"
        "v_mfma_f32_16x16x32_bf16 v[36:39], a[252:255], a[196:199], v[36:39]\n"
        "ds_read_b64_tr_b16 a[44:45], v155 offset:24576\n"
        "global_load_lds_dwordx4 v254, s[20:21] offset:2048 sc1\n"
        "v_mfma_f32_16x16x32_bf16 v[36:39], a[244:247], a[192:195], v[36:39]\n"
        "ds_read_b64_tr_b16 a[46:47], v155 offset:28672\n"
        "v_mfma_f32_16x16x32_bf16 v[44:47], a[244:247], a[200:203], v[44:47]\n"
        "ds_read_b128 a[0:3], v149 offset:32768\n"
        "v_mfma_f32_16x16x32_bf16 v[44:47], a[252:255], a[204:207], v[44:47]\n"
        "ds_read_b128 a[4:7], v149 offset:40960\n"
        "global_load_lds_dwordx4 v254, s[20:21] offset:3072 sc1\n"
        "s_add_u32 m0, s73, 0x14000\n"
        "v_mfma_f32_16x16x32_bf16 v[40:43], a[248:251], a[204:207], v[40:43]\n"
        "ds_read_b128 a[8:11], v149 offset:33792\n"
        "s_add_u32 s20, s20, 0x4000\n"
        "s_addc_u32 s21, s21, 0\n"
        "v_mfma_f32_16x16x32_bf16 v[40:43], a[240:243], a[200:203], v[40:43]\n"
        "ds_read_b128 a[12:15], v149 offset:41984\n"
        "v_mfma_f32_16x16x32_bf16 v[48:51], a[240:243], a[208:211], v[48:51]\n"
        "ds_read_b128 a[16:19], v149 offset:34816\n"
        "v_mfma_f32_16x16x32_bf16 v[48:51], a[248:251], a[212:215], v[48:51]\n"
        "ds_read_b128 a[20:23], v149 offset:43008\n"
        "v_mfma_f32_16x16x32_bf16 v[52:55], a[252:255], a[212:215], v[52:55]\n"
        "ds_read_b128 a[24:27], v149 offset:35840\n"
        "v_mfma_f32_16x16x32_bf16 v[52:55], a[244:247], a[208:211], v[52:55]\n"
        "ds_read_b128 a[28:31], v149 offset:44032\n"
        "s_waitcnt lgkmcnt(8)\n"
        "v_mfma_f32_16x16x32_bf16 v[60:63], a[244:247], a[216:219], v[60:63]\n"
        "ds_read_b128 a[192:195], v152 offset:32768\n"
        "s_waitcnt lgkmcnt(8)\n"
        "v_mfma_f32_16x16x32_bf16 v[60:63], a[252:255], a[220:223], v[60:63]\n"
        "ds_read_b128 a[196:199], v152 offset:40960\n"
        "s_waitcnt lgkmcnt(8)\n"
        "v_mfma_f32_16x16x32_bf16 v[56:59], a[248:251], a[220:223], v[56:59]\n"
        "ds_read_b128 a[200:203], v152 offset:33792\n"
        "s_waitcnt lgkmcnt(8)\n"
        "v_mfma_f32_16x16x32_bf16 v[56:59], a[240:243], a[216:219], v[56:59]\n"
        "ds_read_b128 a[204:207], v152 offset:41984\n"
        "s_waitcnt lgkmcnt(8)\n"
        "v_mfma_f32_16x16x32_bf16 v[0:3], a[32:35], a[0:3], v[0:3]\n"
        "ds_read_b128 a[208:211], v152 offset:34816\n"
        "s_waitcnt lgkmcnt(8)\n"
        "v_mfma_f32_16x16x32_bf16 v[0:3], a[40:43], a[4:7], v[0:3]\n"
        "ds_read_b128 a[212:215], v152 offset:43008\n"
        "s_waitcnt lgkmcnt(8)\n"
        "v_mfma_f32_16x16x32_bf16 v[4:7], a[44:47], a[4:7], v[4:7]\n"
        "ds_read_b128 a[216:219], v152 offset:35840\n"
        "s_waitcnt lgkmcnt(8)\n"
        "v_mfma_f32_16x16x32_bf16 v[4:7], a[36:39], a[0:3], v[4:7]\n"
        "ds_read_b128 a[220:223], v152 offset:44032\n"
        "s_waitcnt lgkmcnt(8)\n"
        "v_mfma_f32_16x16x32_bf16 v[12:15], a[36:39], a[8:11], v[12:15]\n"
        "v_mfma_f32_16x16x32_bf16 v[12:15], a[44:47], a[12:15], v[12:15]\n"
        "s_waitcnt lgkmcnt(6)\n"
        "v_mfma_f32_16x16x32_bf16 v[8:11], a[40:43], a[12:15], v[8:11]\n"
        "v_mfma_f32_16x16x32_bf16 v[8:11], a[32:35], a[8:11], v[8:11]\n"
        "v_mfma_f32_16x16x32_bf16 v[16:19], a[32:35], a[16:19], v[16:19]\n"
        "v_mfma_f32_16x16x32_bf16 v[16:19], a[40:43], a[20:23], v[16:19]\n"
        "s_waitcnt lgkmcnt(2)\n"
        "v_mfma_f32_16x16x32_bf16 v[20:23], a[44:47], a[20:23], v[20:23]\n"
        "v_mfma_f32_16x16x32_bf16 v[20:23], a[36:39], a[16:19], v[20:23]\n"
        "s_waitcnt vmcnt(20)\n"
        "s_barrier\n"
        "v_mfma_f32_16x16x32_bf16 v[28:31], a[36:39], a[24:27], v[28:31]\n"
        "global_load_lds_dwordx4 v144, s[60:61] offset:0\n"
        "ds_read_b128 a[0:3], v149 offset:49152\n"
        "v_mfma_f32_16x16x32_bf16 v[28:31], a[44:47], a[28:31], v[28:31]\n"
        "ds_read_b128 a[4:7], v149 offset:57344\n"
        "v_mfma_f32_16x16x32_bf16 v[24:27], a[40:43], a[28:31], v[24:27]\n"
        "ds_read_b128 a[8:11], v149 offset:50176\n"
        "v_mfma_f32_16x16x32_bf16 v[24:27], a[32:35], a[24:27], v[24:27]\n"
        "global_load_lds_dwordx4 v145, s[60:61] offset:1024\n"
        "ds_read_b128 a[12:15], v149 offset:58368\n"
        "v_mfma_f32_16x16x32_bf16 v[32:35], a[32:35], a[192:195], v[32:35]\n"
        "ds_read_b128 a[16:19], v149 offset:51200\n"
        "v_mfma_f32_16x16x32_bf16 v[32:35], a[40:43], a[196:199], v[32:35]\n"
        "ds_read_b128 a[20:23], v149 offset:59392\n"
        "s_waitcnt lgkmcnt(6)\n"
        "v_mfma_f32_16x16x32_bf16 v[36:39], a[44:47], a[196:199], v[36:39]\n"
        "global_load_lds_dwordx4 v146, s[60:61] offset:2048\n"
        "ds_read_b128 a[24:27], v149 offset:52224\n"
        "v_mfma_f32_16x16x32_bf16 v[36:39], a[36:39], a[192:195], v[36:39]\n"
        "ds_read_b128 a[28:31], v149 offset:60416\n"
        "v_mfma_f32_16x16x32_bf16 v[44:47], a[36:39], a[200:203], v[44:47]\n"
        "ds_read_b128 a[192:195], v152 offset:49152\n"
        "v_mfma_f32_16x16x32_bf16 v[44:47], a[44:47], a[204:207], v[44:47]\n"
        "global_load_lds_dwordx4 v147, s[60:61] offset:3072\n"
        "s_add_u32 m0, s73, 0x0\n"
        "s_waitcnt lgkmcnt(7)\n"
        "v_mfma_f32_16x16x32_bf16 v[40:43], a[40:43], a[204:207], v[40:43]\n"
        "ds_read_b128 a[196:199], v152 offset:57344\n"
        "s_add_u32 s60, s22, 0x1c0000\n"
        "s_addc_u32 s61, s23, 0\n"
        "v_mfma_f32_16x16x32_bf16 v[40:43], a[32:35], a[200:203], v[40:43]\n"
        "ds_read_b128 a[200:203], v152 offset:50176\n"
        "v_mfma_f32_16x16x32_bf16 v[48:51], a[32:35], a[208:211], v[48:51]\n"
        "global_load_lds_dwordx4 v254, s[20:21] offset:0 sc1\n"
        "ds_read_b128 a[204:207], v152 offset:58368\n"
        "v_mfma_f32_16x16x32_bf16 v[48:51], a[40:43], a[212:215], v[48:51]\n"
        "s_waitcnt lgkmcnt(6)\n"
        "v_mfma_f32_16x16x32_bf16 v[52:55], a[44:47], a[212:215], v[52:55]\n"
        "ds_read_b128 a[212:215], v152 offset:59392\n"
        "v_mfma_f32_16x16x32_bf16 v[52:55], a[36:39], a[208:211], v[52:55]\n"
        "global_load_lds_dwordx4 v254, s[20:21] offset:1024 sc1\n"
        "ds_read_b128 a[208:211], v152 offset:51200\n"
        "v_mfma_f32_16x16x32_bf16 v[60:63], a[36:39], a[216:219], v[60:63]\n"
        "v_mfma_f32_16x16x32_bf16 v[60:63], a[44:47], a[220:223], v[60:63]\n"
        "v_mfma_f32_16x16x32_bf16 v[56:59], a[40:43], a[220:223], v[56:59]\n"
        "global_load_lds_dwordx4 v254, s[20:21] offset:2048 sc1\n"
        "ds_read_b128 a[220:223], v152 offset:60416\n"
        "v_mfma_f32_16x16x32_bf16 v[56:59], a[32:35], a[216:219], v[56:59]\n"
        "ds_read_b128 a[216:219], v152 offset:52224\n"
        "v_mfma_f32_16x16x32_bf16 v[0:3], v[208:211], a[0:3], v[0:3]\n"
        "v_mfma_f32_16x16x32_bf16 v[0:3], v[216:219], a[4:7], v[0:3]\n"
        "global_load_lds_dwordx4 v254, s[20:21] offset:3072 sc1\n"
        "s_add_u32 s20, s20, 0x4000\n"
        "s_addc_u32 s21, s21, 0\n"
        "v_mfma_f32_16x16x32_bf16 v[4:7], v[220:223], a[4:7], v[4:7]\n"
        "s_add_u32 m0, s73, 0x4000\n"
        "s_waitcnt lgkmcnt(3)\n"
        "v_mfma_f32_16x16x32_bf16 v[4:7], v[212:215], a[0:3], v[4:7]\n"
        "v_mfma_f32_16x16x32_bf16 v[12:15], v[212:215], a[8:11], v[12:15]\n"
        "v_mfma_f32_16x16x32_bf16 v[12:15], v[220:223], a[12:15], v[12:15]\n"
        "v_mfma_f32_16x16x32_bf16 v[8:11], v[216:219], a[12:15], v[8:11]\n"
        "LHB3_%=:\n"
        "s_cbranch_execz LHRB_%=\n"
        "v_mfma_f32_16x16x32_bf16 v[8:11], v[208:211], a[8:11], v[8:11]\n"
        "v_mfma_f32_16x16x32_bf16 v[16:19], v[208:211], a[16:19], v[16:19]\n"
        "v_mfma_f32_16x16x32_bf16 v[16:19], v[216:219], a[20:23], v[16:19]\n"
        "v_mfma_f32_16x16x32_bf16 v[20:23], v[220:223], a[20:23], v[20:23]\n"
        "v_mfma_f32_16x16x32_bf16 v[20:23], v[212:215], a[16:19], v[20:23]\n"
        "s_waitcnt vmcnt(20)\n"
        "s_barrier\n"
        "v_mfma_f32_16x16x32_bf16 v[28:31], v[212:215], a[24:27], v[28:31]\n"
        "global_load_lds_dwordx4 v254, s[20:21] offset:0 sc1\n"
        "ds_read_b64_tr_b16 a[48:49], v158 offset:0\n"
        "v_mfma_f32_16x16x32_bf16 v[28:31], v[220:223], a[28:31], v[28:31]\n"
        "ds_read_b64_tr_b16 a[50:51], v158 offset:4096\n"
        "s_waitcnt lgkmcnt(2)\n"
        "v_mfma_f32_16x16x32_bf16 v[24:27], v[216:219], a[28:31], v[24:27]\n"
        "ds_read_b64_tr_b16 a[56:57], v158 offset:8192\n"
        "v_mfma_f32_16x16x32_bf16 v[24:27], v[208:211], a[24:27], v[24:27]\n"
        "ds_read_b64_tr_b16 a[58:59], v158 offset:12288\n"
        "global_load_lds_dwordx4 v254, s[20:21] offset:1024 sc1\n"
        "v_mfma_f32_16x16x32_bf16 v[32:35], v[208:211], a[192:195], v[32:35]\n"
        "ds_read_b64_tr_b16 a[52:53], v159 offset:0\n"
        "v_mfma_f32_16x16x32_bf16 v[32:35], v[216:219], a[196:199], v[32:35]\n"
        "ds_read_b64_tr_b16 a[54:55], v159 offset:4096\n"
        "v_mfma_f32_16x16x32_bf16 v[36:39], v[220:223], a[196:199], v[36:39]\n"
        "ds_read_b64_tr_b16 a[60:61], v159 offset:8192\n"
        "global_load_lds_dwordx4 v254, s[20:21] offset:2048 sc1\n"
        "v_mfma_f32_16x16x32_bf16 v[36:39], v[212:215], a[192:195], v[36:39]\n"
        "ds_read_b64_tr_b16 a[62:63], v159 offset:12288\n"
        "v_mfma_f32_16x16x32_bf16 v[44:47], v[212:215], a[200:203], v[44:47]\n"
        "ds_read_b128 a[0:3], v150 offset:16384\n"
        "v_mfma_f32_16x16x32_bf16 v[44:47], v[220:223], a[204:207], v[44:47]\n"
        "ds_read_b128 a[4:7], v150 offset:24576\n"
        "global_load_lds_dwordx4 v254, s[20:21] offset:3072 sc1\n"
        "s_add_u32 m0, s73, 0x18000\n"
        "v_mfma_f32_16x16x32_bf16 v[40:43], v[216:219], a[204:207], v[40:43]\n"
        "ds_read_b128 a[8:11], v150 offset:17408\n"
        "s_add_u32 s20, s20, 0x4000\n"
        "s_addc_u32 s21, s21, 0\n"
        "v_mfma_f32_16x16x32_bf16 v[40:43], v[208:211], a[200:203], v[40:43]\n"
        "ds_read_b128 a[12:15], v150 offset:25600\n"
        "v_mfma_f32_16x16x32_bf16 v[48:51], v[208:211], a[208:211], v[48:51]\n"
        "ds_read_b128 a[16:19], v150 offset:18432\n"
        "global_load_lds_dwordx4 v144, s[60:61] offset:0\n"
        "v_mfma_f32_16x16x32_bf16 v[48:51], v[216:219], a[212:215], v[48:51]\n"
        "ds_read_b128 a[20:23], v150 offset:26624\n"
        "v_mfma_f32_16x16x32_bf16 v[52:55], v[220:223], a[212:215], v[52:55]\n"
        "ds_read_b128 a[24:27], v150 offset:19456\n"
        "v_mfma_f32_16x16x32_bf16 v[52:55], v[212:215], a[208:211], v[52:55]\n"
        "ds_read_b128 a[28:31], v150 offset:27648\n"
        "global_load_lds_dwordx4 v145, s[60:61] offset:1024\n"
        "v_mfma_f32_16x16x32_bf16 v[60:63], v[212:215], a[216:219], v[60:63]\n"
        "ds_read_b128 a[192:195], v153 offset:16384\n"
        "s_waitcnt lgkmcnt(8)\n"
        "v_mfma_f32_16x16x32_bf16 v[60:63], v[220:223], a[220:223], v[60:63]\n"
        "ds_read_b128 a[196:199], v153 offset:24576\n"
        "s_waitcnt lgkmcnt(8)\n"
        "v_mfma_f32_16x16x32_bf16 v[56:59], v[216:219], a[220:223], v[56:59]\n"
        "global_load_lds_dwordx4 v146, s[60:61] offset:2048\n"
        "v_mfma_f32_16x16x32_bf16 v[56:59], v[208:211], a[216:219], v[56:59]\n"
        "ds_read_b128 a[200:203], v153 offset:17408\n"
        "s_waitcnt lgkmcnt(7)\n"
        "v_mfma_f32_16x16x32_bf16 v[0:3], a[48:51], a[0:3], v[0:3]\n"
        "ds_read_b128 a[204:207], v153 offset:25600\n"
        "s_waitcnt lgkmcnt(7)\n"
        "v_mfma_f32_16x16x32_bf16 v[0:3], a[56:59], a[4:7], v[0:3]\n"
        "global_load_lds_dwordx4 v147, s[60:61] offset:3072\n"
        "s_add_u32 m0, s73, 0x1c000\n"
        "v_mfma_f32_16x16x32_bf16 v[4:7], a[60:63], a[4:7], v[4:7]\n"
        "ds_read_b128 a[208:211], v153 offset:18432\n"
        "s_add_u32 s60, s22, 0x1e0000\n"
        "s_addc_u32 s61, s23, 0\n"
        "v_mfma_f32_16x16x32_bf16 v[4:7], a[52:55], a[0:3], v[4:7]\n"
        "ds_read_b128 a[212:215], v153 offset:26624\n"
        "s_waitcnt lgkmcnt(6)\n"
        "v_mfma_f32_16x16x32_bf16 v[12:15], a[52:55], a[8:11], v[12:15]\n"
        "ds_read_b128 a[216:219], v153 offset:19456\n"
        "v_mfma_f32_16x16x32_bf16 v[12:15], a[60:63], a[12:15], v[12:15]\n"
        "ds_read_b128 a[220:223], v153 offset:27648\n"
        "s_waitcnt lgkmcnt(6)\n"
        "v_mfma_f32_16x16x32_bf16 v[8:11], a[56:59], a[12:15], v[8:11]\n"
        "v_mfma_f32_16x16x32_bf16 v[8:11], a[48:51], a[8:11], v[8:11]\n"
        "v_mfma_f32_16x16x32_bf16 v[16:19], a[48:51], a[16:19], v[16:19]\n"
        "v_mfma_f32_16x16x32_bf16 v[16:19], a[56:59], a[20:23], v[16:19]\n"
        "s_waitcnt lgkmcnt(4)\n"
        "v_mfma_f32_16x16x32_bf16 v[20:23], a[60:63], a[20:23], v[20:23]\n"
        "v_mfma_f32_16x16x32_bf16 v[20:23], a[52:55], a[16:19], v[20:23]\n"
        "s_waitcnt vmcnt(24)\n"
        "s_barrier\n"
        "v_mfma_f32_16x16x32_bf16 v[28:31], a[52:55], a[24:27], v[28:31]\n"
        "global_load_lds_dwordx4 v254, s[20:21] offset:0 sc1\n"
        "ds_read_b128 a[0:3], v148 offset:32768\n"
        "v_mfma_f32_16x16x32_bf16 v[28:31], a[60:63], a[28:31], v[28:31]\n"
        "ds_read_b128 a[4:7], v148 offset:40960\n"
        "s_waitcnt lgkmcnt(2)\n"
        "v_mfma_f32_16x16x32_bf16 v[24:27], a[56:59], a[28:31], v[24:27]\n"
        "ds_read_b128 a[8:11], v148 offset:33792\n"
        "v_mfma_f32_16x16x32_bf16 v[24:27], a[48:51], a[24:27], v[24:27]\n"
        "global_load_lds_dwordx4 v254, s[20:21] offset:1024 sc1\n"
        "ds_read_b128 a[12:15], v148 offset:41984\n"
        "v_mfma_f32_16x16x32_bf16 v[32:35], a[48:51], a[192:195], v[32:35]\n"
        "ds_read_b128 a[16:19], v148 offset:34816\n"
        "v_mfma_f32_16x16x32_bf16 v[32:35], a[56:59], a[196:199], v[32:35]\n"
        "ds_read_b128 a[20:23], v148 offset:43008\n"
        "v_mfma_f32_16x16x32_bf16 v[36:39], a[60:63], a[196:199], v[36:39]\n"
        "global_load_lds_dwordx4 v254, s[20:21] offset:2048 sc1\n"
        "ds_read_b128 a[24:27], v148 offset:35840\n"
        "v_mfma_f32_16x16x32_bf16 v[36:39], a[52:55], a[192:195], v[36:39]\n"
        "ds_read_b128 a[28:31], v148 offset:44032\n"
        "v_mfma_f32_16x16x32_bf16 v[44:47], a[52:55], a[200:203], v[44:47]\n"
        "ds_read_b128 a[192:195], v151 offset:32768\n"
        "v_mfma_f32_16x16x32_bf16 v[44:47], a[60:63], a[204:207], v[44:47]\n"
        "global_load_lds_dwordx4 v254, s[20:21] offset:3072 sc1\n"
        "s_add_u32 s20, s20, 0x4000\n"
        "s_addc_u32 s21, s21, 0\n"
        "v_mfma_f32_16x16x32_bf16 v[40:43], a[56:59], a[204:207], v[40:43]\n"
        "ds_read_b128 a[196:199], v151 offset:40960\n"
        "s_add_u32 m0, s73, 0x20000\n"
        "s_waitcnt lgkmcnt(7)\n"
        "v_mfma_f32_16x16x32_bf16 v[40:43], a[48:51], a[200:203], v[40:43]\n"
        "ds_read_b128 a[200:203], v151 offset:33792\n"
        "v_mfma_f32_16x16x32_bf16 v[48:51], a[48:51], a[208:211], v[48:51]\n"
        "s_cmp_lg_u32 s40, 0\n"
        "s_cbranch_scc1 LW2_%=\n"
        "s_mov_b64 exec, 0xffff\n"
        "global_store_dwordx2 v234, v[250:251], s[48:49] sc1\n"
        "s_mov_b64 exec, -1\n"
        "LW2_%=:\n"
        "v_mfma_f32_16x16x32_bf16 v[48:51], a[56:59], a[212:215], v[48:51]\n"
        "ds_read_b128 a[204:207], v151 offset:41984\n"
        "v_bfe_u32 v234, v254, 4, 3\n"
        "v_mfma_f32_16x16x32_bf16 v[52:55], a[60:63], a[212:215], v[52:55]\n"
        "ds_read_b128 a[212:215], v151 offset:43008\n"
        "v_lshl_add_u32 v234, v234, 7, s14\n"
        "v_mfma_f32_16x16x32_bf16 v[52:55], a[52:55], a[208:211], v[52:55]\n"
        "ds_read_b128 a[208:211], v151 offset:34816\n"
        "v_cmp_eq_u32 s[14:15], v247, v248\n"
        "v_mfma_f32_16x16x32_bf16 v[60:63], a[52:55], a[216:219], v[60:63]\n"
        "s_waitcnt lgkmcnt(5)\n"
        "v_cndmask_b32 v138, 0, v246, s[14:15]\n"
        "v_cmp_eq_u32 s[14:15], v247, v249\n"
        "v_mfma_f32_16x16x32_bf16 v[60:63], a[60:63], a[220:223], v[60:63]\n"
        "v_mfma_f32_16x16x32_bf16 v[56:59], a[56:59], a[220:223], v[56:59]\n"
        "ds_read_b128 a[220:223], v151 offset:44032\n"
        "v_cndmask_b32 v139, 0, v246, s[14:15]\n"
        "v_mfma_f32_16x16x32_bf16 v[56:59], a[48:51], a[216:219], v[56:59]\n"
        "ds_read_b128 a[216:219], v151 offset:35840\n"
        "s_lshl2_add_u32 s14, s13, s40\n"
        "s_add_u32 s14, s14, 3072\n"
        "v_mfma_f32_16x16x32_bf16 v[0:3], a[64:67], a[0:3], v[0:3]\n"
        "s_lshl_b32 s14, s14, 7\n"
        "v_mfma_f32_16x16x32_bf16 v[0:3], a[72:75], a[4:7], v[0:3]\n"
        "v_mfma_f32_16x16x32_bf16 v[4:7], a[76:79], a[4:7], v[4:7]\n"
        "v_mfma_f32_16x16x32_bf16 v[4:7], a[68:71], a[0:3], v[4:7]\n"
        "v_mfma_f32_16x16x32_bf16 v[12:15], a[68:71], a[8:11], v[12:15]\n"
        "v_mfma_f32_16x16x32_bf16 v[12:15], a[76:79], a[12:15], v[12:15]\n"
        "v_mfma_f32_16x16x32_bf16 v[8:11], a[72:75], a[12:15], v[8:11]\n"
        "v_mfma_f32_16x16x32_bf16 v[8:11], a[64:67], a[8:11], v[8:11]\n"
        "s_waitcnt lgkmcnt(0)\n"
        "v_mfma_f32_16x16x32_bf16 v[16:19], a[64:67], a[16:19], v[16:19]\n"
        "v_mfma_f32_16x16x32_bf16 v[16:19], a[72:75], a[20:23], v[16:19]\n"
        "v_mfma_f32_16x16x32_bf16 v[20:23], a[76:79], a[20:23], v[20:23]\n"
        "v_mfma_f32_16x16x32_bf16 v[20:23], a[68:71], a[16:19], v[20:23]\n"
        "s_waitcnt vmcnt(20)\n"
        "s_barrier\n"
        "v_mfma_f32_16x16x32_bf16 v[28:31], a[68:71], a[24:27], v[28:31]\n"
        "global_load_lds_dwordx4 v144, s[60:61] offset:0\n"
        "ds_read_b64_tr_b16 a[224:225], v154 offset:49152\n"
        "v_mfma_f32_16x16x32_bf16 v[28:31], a[76:79], a[28:31], v[28:31]\n"
        "ds_read_b64_tr_b16 a[226:227], v154 offset:53248\n"
        "v_mfma_f32_16x16x32_bf16 v[24:27], a[72:75], a[28:31], v[24:27]\n"
        "ds_read_b64_tr_b16 a[232:233], v154 offset:57344\n"
        "v_mfma_f32_16x16x32_bf16 v[24:27], a[64:67], a[24:27], v[24:27]\n"
        "ds_read_b64_tr_b16 a[234:235], v154 offset:61440\n"
        "global_load_lds_dwordx4 v145, s[60:61] offset:1024\n"
        "v_mfma_f32_16x16x32_bf16 v[32:35], a[64:67], a[192:195], v[32:35]\n"
        "ds_read_b64_tr_b16 a[228:229], v155 offset:49152\n"
        "v_mfma_f32_16x16x32_bf16 v[32:35], a[72:75], a[196:199], v[32:35]\n"
        "ds_read_b64_tr_b16 a[230:231], v155 offset:53248\n"
        "v_mfma_f32_16x16x32_bf16 v[36:39], a[76:79], a[196:199], v[36:39]\n"
        "ds_read_b64_tr_b16 a[236:237], v155 offset:57344\n"
        "global_load_lds_dwordx4 v146, s[60:61] offset:2048\n"
        "v_mfma_f32_16x16x32_bf16 v[36:39], a[68:71], a[192:195], v[36:39]\n"
        "ds_read_b64_tr_b16 a[238:239], v155 offset:61440\n"
        "v_mfma_f32_16x16x32_bf16 v[44:47], a[68:71], a[200:203], v[44:47]\n"
        "ds_read_b128 a[0:3], v149 offset:0\n"
        "v_mfma_f32_16x16x32_bf16 v[44:47], a[76:79], a[204:207], v[44:47]\n"
        "ds_read_b128 a[4:7], v149 offset:8192\n"
        "global_load_lds_dwordx4 v147, s[60:61] offset:3072\n"
        "s_add_u32 m0, s73, 0x24000\n"
        "v_mfma_f32_16x16x32_bf16 v[40:43], a[72:75], a[204:207], v[40:43]\n"
        "ds_read_b128 a[8:11], v149 offset:1024\n"
        "s_add_u32 s60, s22, 0x220000\n"
        "s_addc_u32 s61, s23, 0\n"
        "v_mfma_f32_16x16x32_bf16 v[40:43], a[64:67], a[200:203], v[40:43]\n"
        "ds_read_b128 a[12:15], v149 offset:9216\n"
        "v_mfma_f32_16x16x32_bf16 v[48:51], a[64:67], a[208:211], v[48:51]\n"
        "ds_read_b128 a[16:19], v149 offset:2048\n"
        "global_load_lds_dwordx4 v254, s[20:21] offset:0 sc1\n"
        "v_mfma_f32_16x16x32_bf16 v[48:51], a[72:75], a[212:215], v[48:51]\n"
        "ds_read_b128 a[20:23], v149 offset:10240\n"
        "v_mfma_f32_16x16x32_bf16 v[52:55], a[76:79], a[212:215], v[52:55]\n"
        "ds_read_b128 a[24:27], v149 offset:3072\n"
        "v_mfma_f32_16x16x32_bf16 v[52:55], a[68:71], a[208:211], v[52:55]\n"
        "ds_read_b128 a[28:31], v149 offset:11264\n"
        "global_load_lds_dwordx4 v254, s[20:21] offset:1024 sc1\n"
        "v_mfma_f32_16x16x32_bf16 v[60:63], a[68:71], a[216:219], v[60:63]\n"
        "ds_read_b128 a[192:195], v152 offset:0\n"
        "s_waitcnt lgkmcnt(8)\n"
        "v_mfma_f32_16x16x32_bf16 v[60:63], a[76:79], a[220:223], v[60:63]\n"
        "ds_read_b128 a[196:199], v152 offset:8192\n"
        "s_waitcnt lgkmcnt(8)\n"
        "v_mfma_f32_16x16x32_bf16 v[56:59], a[72:75], a[220:223], v[56:59]\n"
        "global_load_lds_dwordx4 v254, s[20:21] offset:2048 sc1\n"
        "v_mfma_f32_16x16x32_bf16 v[56:59], a[64:67], a[216:219], v[56:59]\n"
        "ds_read_b128 a[200:203], v152 offset:1024\n"
        "s_waitcnt lgkmcnt(7)\n"
        "v_mfma_f32_16x16x32_bf16 v[0:3], a[224:227], a[0:3], v[0:3]\n"
        "ds_read_b128 a[204:207], v152 offset:9216\n"
        "s_waitcnt lgkmcnt(7)\n"
        "v_mfma_f32_16x16x32_bf16 v[0:3], a[232:235], a[4:7], v[0:3]\n"
        "global_load_lds_dwordx4 v254, s[20:21] offset:3072 sc1\n"
        "s_add_u32 m0, s73, 0x8000\n"
        "v_mfma_f32_16x16x32_bf16 v[4:7], a[236:239], a[4:7], v[4:7]\n"
        "ds_read_b128 a[208:211], v152 offset:2048\n"
        "s_add_u32 s20, s20, 0x4000\n"
        "s_addc_u32 s21, s21, 0\n"
        "v_mfma_f32_16x16x32_bf16 v[4:7], a[228:231], a[0:3], v[4:7]\n"
        "ds_read_b128 a[212:215], v152 offset:10240\n"
        "s_waitcnt lgkmcnt(6)\n"
        "v_mfma_f32_16x16x32_bf16 v[12:15], a[228:231], a[8:11], v[12:15]\n"
        "ds_read_b128 a[216:219], v152 offset:3072\n"
        "v_mfma_f32_16x16x32_bf16 v[12:15], a[236:239], a[12:15], v[12:15]\n"
        "ds_read_b128 a[220:223], v152 offset:11264\n"
        "s_waitcnt lgkmcnt(6)\n"
        "v_mfma_f32_16x16x32_bf16 v[8:11], a[232:235], a[12:15], v[8:11]\n"
        "v_mfma_f32_16x16x32_bf16 v[8:11], a[224:227], a[8:11], v[8:11]\n"
        "v_mfma_f32_16x16x32_bf16 v[16:19], a[224:227], a[16:19], v[16:19]\n"
        "v_mfma_f32_16x16x32_bf16 v[16:19], a[232:235], a[20:23], v[16:19]\n"
        "s_waitcnt lgkmcnt(4)\n"
        "v_mfma_f32_16x16x32_bf16 v[20:23], a[236:239], a[20:23], v[20:23]\n"
        "v_mfma_f32_16x16x32_bf16 v[20:23], a[228:231], a[16:19], v[20:23]\n"
        "s_waitcnt vmcnt(20)\n"
        "s_barrier\n"
        "v_mfma_f32_16x16x32_bf16 v[28:31], a[228:231], a[24:27], v[28:31]\n"
        "global_load_lds_dwordx4 v254, s[20:21] offset:0 sc1\n"
        "ds_read_b64_tr_b16 a[240:241], v156 offset:16384\n"
        "v_mfma_f32_16x16x32_bf16 v[28:31], a[236:239], a[28:31], v[28:31]\n"
        "ds_read_b64_tr_b16 a[242:243], v156 offset:20480\n"
        "s_waitcnt lgkmcnt(2)\n"
        "v_mfma_f32_16x16x32_bf16 v[24:27], a[232:235], a[28:31], v[24:27]\n"
        "ds_read_b64_tr_b16 a[248:249], v156 offset:24576\n"
        "v_mfma_f32_16x16x32_bf16 v[24:27], a[224:227], a[24:27], v[24:27]\n"
        "ds_read_b64_tr_b16 a[250:251], v156 offset:28672\n"
        "global_load_lds_dwordx4 v254, s[20:21] offset:1024 sc1\n"
        "v_mfma_f32_16x16x32_bf16 v[32:35], a[224:227], a[192:195], v[32:35]\n"
        "ds_read_b64_tr_b16 a[244:245], v157 offset:16384\n"
        "v_mfma_f32_16x16x32_bf16 v[32:35], a[232:235], a[196:199], v[32:35]\n"
        "ds_read_b64_tr_b16 a[246:247], v157 offset:20480\n"
        "v_mfma_f32_16x16x32_bf16 v[36:39], a[236:239], a[196:199], v[36:39]\n"
        "ds_read_b64_tr_b16 a[252:253], v157 offset:24576\n"
        "global_load_lds_dwordx4 v254, s[20:21] offset:2048 sc1\n"
        "v_mfma_f32_16x16x32_bf16 v[36:39], a[228:231], a[192:195], v[36:39]\n"
        "ds_read_b64_tr_b16 a[254:255], v157 offset:28672\n"
        "v_mfma_f32_16x16x32_bf16 v[44:47], a[228:231], a[200:203], v[44:47]\n"
        "ds_read_b128 a[0:3], v148 offset:0\n"
        "v_mfma_f32_16x16x32_bf16 v[44:47], a[236:239], a[204:207], v[44:47]\n"
        "ds_read_b128 a[4:7], v148 offset:8192\n"
        "global_load_lds_dwordx4 v254, s[20:21] offset:3072 sc1\n"
        "s_add_u32 m0, s73, 0xc000\n"
        "v_mfma_f32_16x16x32_bf16 v[40:43], a[232:235], a[204:207], v[40:43]\n"
        "ds_read_b128 a[8:11], v148 offset:1024\n"
        "s_add_u32 s20, s20, 0x4000\n"
        "s_addc_u32 s21, s21, 0\n"
        "v_mfma_f32_16x16x32_bf16 v[40:43], a[224:227], a[200:203], v[40:43]\n"
        "ds_read_b128 a[12:15], v148 offset:9216\n"
        "v_mfma_f32_16x16x32_bf16 v[48:51], a[224:227], a[208:211], v[48:51]\n"
        "ds_read_b128 a[16:19], v148 offset:2048\n"
        "v_mfma_f32_16x16x32_bf16 v[48:51], a[232:235], a[212:215], v[48:51]\n"
        "ds_read_b128 a[20:23], v148 offset:10240\n"
        "v_mfma_f32_16x16x32_bf16 v[52:55], a[236:239], a[212:215], v[52:55]\n"
        "ds_read_b128 a[24:27], v148 offset:3072\n"
        "v_mfma_f32_16x16x32_bf16 v[52:55], a[228:231], a[208:211], v[52:55]\n"
        "ds_read_b128 a[28:31], v148 offset:11264\n"
        "s_waitcnt lgkmcnt(8)\n"
        "v_mfma_f32_16x16x32_bf16 v[60:63], a[228:231], a[216:219], v[60:63]\n"
        "ds_read_b128 a[192:195], v151 offset:0\n"
        "s_waitcnt lgkmcnt(8)\n"
        "v_mfma_f32_16x16x32_bf16 v[60:63], a[236:239], a[220:223], v[60:63]\n"
        "ds_read_b128 a[196:199], v151 offset:8192\n"
        "s_waitcnt lgkmcnt(8)\n"
        "v_mfma_f32_16x16x32_bf16 v[56:59], a[232:235], a[220:223], v[56:59]\n"
        "ds_read_b128 a[200:203], v151 offset:1024\n"
        "s_waitcnt lgkmcnt(8)\n"
        "v_mfma_f32_16x16x32_bf16 v[56:59], a[224:227], a[216:219], v[56:59]\n"
        "ds_read_b128 a[204:207], v151 offset:9216\n"
        "s_waitcnt lgkmcnt(8)\n"
        "v_mfma_f32_16x16x32_bf16 v[0:3], a[240:243], a[0:3], v[0:3]\n"
        "ds_read_b128 a[208:211], v151 offset:2048\n"
        "s_waitcnt lgkmcnt(8)\n"
        "v_mfma_f32_16x16x32_bf16 v[0:3], a[248:251], a[4:7], v[0:3]\n"
        "ds_read_b128 a[212:215], v151 offset:10240\n"
        "s_waitcnt lgkmcnt(8)\n"
        "v_mfma_f32_16x16x32_bf16 v[4:7], a[252:255], a[4:7], v[4:7]\n"
        "ds_read_b128 a[216:219], v151 offset:3072\n"
        "s_waitcnt lgkmcnt(8)\n"
        "v_mfma_f32_16x16x32_bf16 v[4:7], a[244:247], a[0:3], v[4:7]\n"
        "ds_read_b128 a[220:223], v151 offset:11264\n"
        "s_waitcnt lgkmcnt(8)\n"
        "v_mfma_f32_16x16x32_bf16 v[12:15], a[244:247], a[8:11], v[12:15]\n"
        "v_mfma_f32_16x16x32_bf16 v[12:15], a[252:255], a[12:15], v[12:15]\n"
        "s_waitcnt lgkmcnt(6)\n"
        "v_mfma_f32_16x16x32_bf16 v[8:11], a[248:251], a[12:15], v[8:11]\n"
        "v_mfma_f32_16x16x32_bf16 v[8:11], a[240:243], a[8:11], v[8:11]\n"
        "v_mfma_f32_16x16x32_bf16 v[16:19], a[240:243], a[16:19], v[16:19]\n"
        "v_mfma_f32_16x16x32_bf16 v[16:19], a[248:251], a[20:23], v[16:19]\n"
        "s_waitcnt lgkmcnt(2)\n"
        "v_mfma_f32_16x16x32_bf16 v[20:23], a[252:255], a[20:23], v[20:23]\n"
        "v_mfma_f32_16x16x32_bf16 v[20:23], a[244:247], a[16:19], v[20:23]\n"
        "s_waitcnt vmcnt(20)\n"
        "s_barrier\n"
        "v_mfma_f32_16x16x32_bf16 v[28:31], a[244:247], a[24:27], v[28:31]\n"
        "global_load_lds_dwordx4 v144, s[60:61] offset:0\n"
        "ds_read_b128 a[0:3], v148 offset:16384\n"
        "v_mfma_f32_16x16x32_bf16 v[28:31], a[252:255], a[28:31], v[28:31]\n"
        "ds_read_b128 a[4:7], v148 offset:24576\n"
        "v_mfma_f32_16x16x32_bf16 v[24:27], a[248:251], a[28:31], v[24:27]\n"
        "ds_read_b128 a[8:11], v148 offset:17408\n"
        "v_mfma_f32_16x16x32_bf16 v[24:27], a[240:243], a[24:27], v[24:27]\n"
        "global_load_lds_dwordx4 v145, s[60:61] offset:1024\n"
        "ds_read_b128 a[12:15], v148 offset:25600\n"
        "v_mfma_f32_16x16x32_bf16 v[32:35], a[240:243], a[192:195], v[32:35]\n"
        "ds_read_b128 a[16:19], v148 offset:18432\n"
        "v_mfma_f32_16x16x32_bf16 v[32:35], a[248:251], a[196:199], v[32:35]\n"
        "ds_read_b128 a[20:23], v148 offset:26624\n"
        "s_waitcnt lgkmcnt(6)\n"
        "v_mfma_f32_16x16x32_bf16 v[36:39], a[252:255], a[196:199], v[36:39]\n"
        "global_load_lds_dwordx4 v146, s[60:61] offset:2048\n"
        "ds_read_b128 a[24:27], v148 offset:19456\n"
        "v_mfma_f32_16x16x32_bf16 v[36:39], a[244:247], a[192:195], v[36:39]\n"
        "ds_read_b128 a[28:31], v148 offset:27648\n"
        "v_mfma_f32_16x16x32_bf16 v[44:47], a[244:247], a[200:203], v[44:47]\n"
        "ds_read_b128 a[192:195], v151 offset:16384\n"
        "v_mfma_f32_16x16x32_bf16 v[44:47], a[252:255], a[204:207], v[44:47]\n"
        "global_load_lds_dwordx4 v147, s[60:61] offset:3072\n"
        "s_add_u32 m0, s73, 0x10000\n"
        "s_waitcnt lgkmcnt(7)\n"
        "v_mfma_f32_16x16x32_bf16 v[40:43], a[248:251], a[204:207], v[40:43]\n"
        "ds_read_b128 a[196:199], v151 offset:24576\n"
        "s_add_u32 s60, s22, 0x260000\n"
        "s_addc_u32 s61, s23, 0\n"
        "v_mfma_f32_16x16x32_bf16 v[40:43], a[240:243], a[200:203], v[40:43]\n"
        "ds_read_b128 a[200:203], v151 offset:17408\n"
        "v_mfma_f32_16x16x32_bf16 v[48:51], a[240:243], a[208:211], v[48:51]\n"
        "global_load_lds_dwordx4 v254, s[20:21] offset:0 sc1\n"
        "ds_read_b128 a[204:207], v151 offset:25600\n"
        "v_mfma_f32_16x16x32_bf16 v[48:51], a[248:251], a[212:215], v[48:51]\n"
        "s_waitcnt lgkmcnt(6)\n"
        "v_mfma_f32_16x16x32_bf16 v[52:55], a[252:255], a[212:215], v[52:55]\n"
        "ds_read_b128 a[212:215], v151 offset:26624\n"
        "v_mfma_f32_16x16x32_bf16 v[52:55], a[244:247], a[208:211], v[52:55]\n"
        "global_load_lds_dwordx4 v254, s[20:21] offset:1024 sc1\n"
        "ds_read_b128 a[208:211], v151 offset:18432\n"
        "v_mfma_f32_16x16x32_bf16 v[60:63], a[244:247], a[216:219], v[60:63]\n"
        "v_mfma_f32_16x16x32_bf16 v[60:63], a[252:255], a[220:223], v[60:63]\n"
        "v_mfma_f32_16x16x32_bf16 v[56:59], a[248:251], a[220:223], v[56:59]\n"
        "global_load_lds_dwordx4 v254, s[20:21] offset:2048 sc1\n"
        "ds_read_b128 a[220:223], v151 offset:27648\n"
        "v_mfma_f32_16x16x32_bf16 v[56:59], a[240:243], a[216:219], v[56:59]\n"
        "ds_read_b128 a[216:219], v151 offset:19456\n"
        "v_mfma_f32_16x16x32_bf16 v[0:3], a[80:83], a[0:3], v[0:3]\n"
        "v_mfma_f32_16x16x32_bf16 v[0:3], a[88:91], a[4:7], v[0:3]\n"
        "global_load_lds_dwordx4 v254, s[20:21] offset:3072 sc1\n"
        "s_add_u32 s20, s20, 0x4000\n"
        "s_addc_u32 s21, s21, 0\n"
        "v_mfma_f32_16x16x32_bf16 v[4:7], a[92:95], a[4:7], v[4:7]\n"
        "s_add_u32 m0, s73, 0x14000\n"
        "s_waitcnt lgkmcnt(3)\n"
        "v_mfma_f32_16x16x32_bf16 v[4:7], a[84:87], a[0:3], v[4:7]\n"
        "v_mfma_f32_16x16x32_bf16 v[12:15], a[84:87], a[8:11], v[12:15]\n"
        "v_mfma_f32_16x16x32_bf16 v[12:15], a[92:95], a[12:15], v[12:15]\n"
        "v_mfma_f32_16x16x32_bf16 v[8:11], a[88:91], a[12:15], v[8:11]\n"
        "v_mfma_f32_16x16x32_bf16 v[8:11], a[80:83], a[8:11], v[8:11]\n"
        "v_mfma_f32_16x16x32_bf16 v[16:19], a[80:83], a[16:19], v[16:19]\n"
        "v_mfma_f32_16x16x32_bf16 v[16:19], a[88:91], a[20:23], v[16:19]\n"
        "v_mfma_f32_16x16x32_bf16 v[20:23], a[92:95], a[20:23], v[20:23]\n"
        "v_mfma_f32_16x16x32_bf16 v[20:23], a[84:87], a[16:19], v[20:23]\n"
        "s_waitcnt vmcnt(20)\n"
        "s_barrier\n"
        "v_mfma_f32_16x16x32_bf16 v[28:31], a[84:87], a[24:27], v[28:31]\n"
        "global_load_lds_dwordx4 v254, s[20:21] offset:0 sc1\n"
        "ds_read_b64_tr_b16 a[32:33], v156 offset:32768\n"
        "v_mfma_f32_16x16x32_bf16 v[28:31], a[92:95], a[28:31], v[28:31]\n"
        "ds_read_b64_tr_b16 a[34:35], v156 offset:36864\n"
        "s_waitcnt lgkmcnt(2)\n"
        "v_mfma_f32_16x16x32_bf16 v[24:27], a[88:91], a[28:31], v[24:27]\n"
        "ds_read_b64_tr_b16 a[40:41], v156 offset:40960\n"
        "v_mfma_f32_16x16x32_bf16 v[24:27], a[80:83], a[24:27], v[24:27]\n"
        "ds_read_b64_tr_b16 a[42:43], v156 offset:45056\n"
        "global_load_lds_dwordx4 v254, s[20:21] offset:1024 sc1\n"
        "v_mfma_f32_16x16x32_bf16 v[32:35], a[80:83], a[192:195], v[32:35]\n"
        "ds_read_b64_tr_b16 a[36:37], v157 offset:32768\n"
        "v_mfma_f32_16x16x32_bf16 v[32:35], a[88:91], a[196:199], v[32:35]\n"
        "ds_read_b64_tr_b16 a[38:39], v157 offset:36864\n"
        "v_mfma_f32_16x16x32_bf16 v[36:39], a[92:95], a[196:199], v[36:39]\n"
        "ds_read_b64_tr_b16 a[44:45], v157 offset:40960\n"
        "global_load_lds_dwordx4 v254, s[20:21] offset:2048 sc1\n"
        "v_mfma_f32_16x16x32_bf16 v[36:39], a[84:87], a[192:195], v[36:39]\n"
        "ds_read_b64_tr_b16 a[46:47], v157 offset:45056\n"
        "v_mfma_f32_16x16x32_bf16 v[44:47], a[84:87], a[200:203], v[44:47]\n"
        "ds_read_b128 a[0:3], v149 offset:49152\n"
        "v_mfma_f32_16x16x32_bf16 v[44:47], a[92:95], a[204:207], v[44:47]\n"
        "ds_read_b128 a[4:7], v149 offset:57344\n"
        "global_load_lds_dwordx4 v254, s[20:21] offset:3072 sc1\n"
        "s_add_u32 m0, s73, 0x0\n"
        "v_mfma_f32_16x16x32_bf16 v[40:43], a[88:91], a[204:207], v[40:43]\n"
        "ds_read_b128 a[8:11], v149 offset:50176\n"
        "s_add_u32 s20, s20, 0x4000\n"
        "s_addc_u32 s21, s21, 0\n"
        "v_mfma_f32_16x16x32_bf16 v[40:43], a[80:83], a[200:203], v[40:43]\n"
        "ds_read_b128 a[12:15], v149 offset:58368\n"
        "v_mfma_f32_16x16x32_bf16 v[48:51], a[80:83], a[208:211], v[48:51]\n"
        "ds_read_b128 a[16:19], v149 offset:51200\n"
        "global_load_lds_dwordx4 v144, s[60:61] offset:0\n"
        "v_mfma_f32_16x16x32_bf16 v[48:51], a[88:91], a[212:215], v[48:51]\n"
        "ds_read_b128 a[20:23], v149 offset:59392\n"
        "v_mfma_f32_16x16x32_bf16 v[52:55], a[92:95], a[212:215], v[52:55]\n"
        "ds_read_b128 a[24:27], v149 offset:52224\n"
        "v_mfma_f32_16x16x32_bf16 v[52:55], a[84:87], a[208:211], v[52:55]\n"
        "ds_read_b128 a[28:31], v149 offset:60416\n"
        "global_load_lds_dwordx4 v145, s[60:61] offset:1024\n"
        "v_mfma_f32_16x16x32_bf16 v[60:63], a[84:87], a[216:219], v[60:63]\n"
        "ds_read_b128 a[192:195], v152 offset:49152\n"
        "s_waitcnt lgkmcnt(8)\n"
        "v_mfma_f32_16x16x32_bf16 v[60:63], a[92:95], a[220:223], v[60:63]\n"
        "ds_read_b128 a[196:199], v152 offset:57344\n"
        "s_waitcnt lgkmcnt(8)\n"
        "v_mfma_f32_16x16x32_bf16 v[56:59], a[88:91], a[220:223], v[56:59]\n"
        "global_load_lds_dwordx4 v146, s[60:61] offset:2048\n"
        "v_mfma_f32_16x16x32_bf16 v[56:59], a[80:83], a[216:219], v[56:59]\n"
        "ds_read_b128 a[200:203], v152 offset:50176\n"
        "s_waitcnt lgkmcnt(7)\n"
        "v_mfma_f32_16x16x32_bf16 v[0:3], a[32:35], a[0:3], v[0:3]\n"
        "ds_read_b128 a[204:207], v152 offset:58368\n"
        "s_waitcnt lgkmcnt(7)\n"
        "v_mfma_f32_16x16x32_bf16 v[0:3], a[40:43], a[4:7], v[0:3]\n"
        "global_load_lds_dwordx4 v147, s[60:61] offset:3072\n"
        "s_add_u32 m0, s73, 0x4000\n"
        "v_mfma_f32_16x16x32_bf16 v[4:7], a[44:47], a[4:7], v[4:7]\n"
        "ds_read_b128 a[208:211], v152 offset:51200\n"
        "s_add_u32 s60, s22, 0x280000\n"
        "s_addc_u32 s61, s23, 0\n"
        "v_mfma_f32_16x16x32_bf16 v[4:7], a[36:39], a[0:3], v[4:7]\n"
        "ds_read_b128 a[212:215], v152 offset:59392\n"
        "s_waitcnt lgkmcnt(6)\n"
        "v_mfma_f32_16x16x32_bf16 v[12:15], a[36:39], a[8:11], v[12:15]\n"
        "ds_read_b128 a[216:219], v152 offset:52224\n"
        "v_mfma_f32_16x16x32_bf16 v[12:15], a[44:47], a[12:15], v[12:15]\n"
        "ds_read_b128 a[220:223], v152 offset:60416\n"
        "s_waitcnt lgkmcnt(6)\n"
        "v_mfma_f32_16x16x32_bf16 v[8:11], a[40:43], a[12:15], v[8:11]\n"
        "v_mfma_f32_16x16x32_bf16 v[8:11], a[32:35], a[8:11], v[8:11]\n"
        "v_mfma_f32_16x16x32_bf16 v[16:19], a[32:35], a[16:19], v[16:19]\n"
        "v_mfma_f32_16x16x32_bf16 v[16:19], a[40:43], a[20:23], v[16:19]\n"
        "s_waitcnt lgkmcnt(4)\n"
        "v_mfma_f32_16x16x32_bf16 v[20:23], a[44:47], a[20:23], v[20:23]\n"
        "v_mfma_f32_16x16x32_bf16 v[20:23], a[36:39], a[16:19], v[20:23]\n"
        "s_waitcnt vmcnt(20)\n"
        "s_barrier\n"
        "v_mfma_f32_16x16x32_bf16 v[28:31], a[36:39], a[24:27], v[28:31]\n"
        "global_load_lds_dwordx4 v254, s[20:21] offset:0 sc1\n"
        "ds_read_b64_tr_b16 a[48:49], v158 offset:0\n"
        "v_mfma_f32_16x16x32_bf16 v[28:31], a[44:47], a[28:31], v[28:31]\n"
        "ds_read_b64_tr_b16 a[50:51], v158 offset:4096\n"
        "s_waitcnt lgkmcnt(2)\n"
        "v_mfma_f32_16x16x32_bf16 v[24:27], a[40:43], a[28:31], v[24:27]\n"
        "ds_read_b64_tr_b16 a[56:57], v158 offset:8192\n"
        "v_mfma_f32_16x16x32_bf16 v[24:27], a[32:35], a[24:27], v[24:27]\n"
        "ds_read_b64_tr_b16 a[58:59], v158 offset:12288\n"
        "global_load_lds_dwordx4 v254, s[20:21] offset:1024 sc1\n"
        "v_mfma_f32_16x16x32_bf16 v[32:35], a[32:35], a[192:195], v[32:35]\n"
        "ds_read_b64_tr_b16 a[52:53], v159 offset:0\n"
        "v_mfma_f32_16x16x32_bf16 v[32:35], a[40:43], a[196:199], v[32:35]\n"
        "ds_read_b64_tr_b16 a[54:55], v159 offset:4096\n"
        "v_mfma_f32_16x16x32_bf16 v[36:39], a[44:47], a[196:199], v[36:39]\n"
        "ds_read_b64_tr_b16 a[60:61], v159 offset:8192\n"
        "global_load_lds_dwordx4 v254, s[20:21] offset:2048 sc1\n"
        "v_mfma_f32_16x16x32_bf16 v[36:39], a[36:39], a[192:195], v[36:39]\n"
        "ds_read_b64_tr_b16 a[62:63], v159 offset:12288\n"
        "v_mfma_f32_16x16x32_bf16 v[44:47], a[36:39], a[200:203], v[44:47]\n"
        "ds_read_b128 a[0:3], v150 offset:16384\n"
        "v_mfma_f32_16x16x32_bf16 v[44:47], a[44:47], a[204:207], v[44:47]\n"
        "ds_read_b128 a[4:7], v150 offset:24576\n"
        "global_load_lds_dwordx4 v254, s[20:21] offset:3072 sc1\n"
        "s_add_u32 m0, s73, 0x18000\n"
        "v_mfma_f32_16x16x32_bf16 v[40:43], a[40:43], a[204:207], v[40:43]\n"
        "ds_read_b128 a[8:11], v150 offset:17408\n"
        "s_add_u32 s20, s20, 0x4000\n"
        "s_addc_u32 s21, s21, 0\n"
        "v_mfma_f32_16x16x32_bf16 v[40:43], a[32:35], a[200:203], v[40:43]\n"
        "ds_read_b128 a[12:15], v150 offset:25600\n"
        "v_mfma_f32_16x16x32_bf16 v[48:51], a[32:35], a[208:211], v[48:51]\n"
        "ds_read_b128 a[16:19], v150 offset:18432\n"
        "v_mfma_f32_16x16x32_bf16 v[48:51], a[40:43], a[212:215], v[48:51]\n"
        "ds_read_b128 a[20:23], v150 offset:26624\n"
        "v_mfma_f32_16x16x32_bf16 v[52:55], a[44:47], a[212:215], v[52:55]\n"
        "ds_read_b128 a[24:27], v150 offset:19456\n"
        "v_mfma_f32_16x16x32_bf16 v[52:55], a[36:39], a[208:211], v[52:55]\n"
        "ds_read_b128 a[28:31], v150 offset:27648\n"
        "s_waitcnt lgkmcnt(8)\n"
        "v_mfma_f32_16x16x32_bf16 v[60:63], a[36:39], a[216:219], v[60:63]\n"
        "ds_read_b128 a[192:195], v153 offset:16384\n"
        "s_waitcnt lgkmcnt(8)\n"
        "v_mfma_f32_16x16x32_bf16 v[60:63], a[44:47], a[220:223], v[60:63]\n"
        "ds_read_b128 a[196:199], v153 offset:24576\n"
        "s_waitcnt lgkmcnt(8)\n"
        "v_mfma_f32_16x16x32_bf16 v[56:59], a[40:43], a[220:223], v[56:59]\n"
        "ds_read_b128 a[200:203], v153 offset:17408\n"
        "s_waitcnt lgkmcnt(8)\n"
        "v_mfma_f32_16x16x32_bf16 v[56:59], a[32:35], a[216:219], v[56:59]\n"
        "ds_read_b128 a[204:207], v153 offset:25600\n"
        "s_waitcnt lgkmcnt(8)\n"
        "v_mfma_f32_16x16x32_bf16 v[0:3], a[48:51], a[0:3], v[0:3]\n"
        "ds_read_b128 a[208:211], v153 offset:18432\n"
        "s_waitcnt lgkmcnt(8)\n"
        "v_mfma_f32_16x16x32_bf16 v[0:3], a[56:59], a[4:7], v[0:3]\n"
        "ds_read_b128 a[212:215], v153 offset:26624\n"
        "s_waitcnt lgkmcnt(8)\n"
        "v_mfma_f32_16x16x32_bf16 v[4:7], a[60:63], a[4:7], v[4:7]\n"
        "ds_read_b128 a[216:219], v153 offset:19456\n"
        "s_waitcnt lgkmcnt(8)\n"
        "v_mfma_f32_16x16x32_bf16 v[4:7], a[52:55], a[0:3], v[4:7]\n"
        "ds_read_b128 a[220:223], v153 offset:27648\n"
        "s_waitcnt lgkmcnt(8)\n"
        "v_mfma_f32_16x16x32_bf16 v[12:15], a[52:55], a[8:11], v[12:15]\n"
        "v_mfma_f32_16x16x32_bf16 v[12:15], a[60:63], a[12:15], v[12:15]\n"
        "s_waitcnt lgkmcnt(6)\n"
        "v_mfma_f32_16x16x32_bf16 v[8:11], a[56:59], a[12:15], v[8:11]\n"
        "v_mfma_f32_16x16x32_bf16 v[8:11], a[48:51], a[8:11], v[8:11]\n"
        "v_mfma_f32_16x16x32_bf16 v[16:19], a[48:51], a[16:19], v[16:19]\n"
        "v_mfma_f32_16x16x32_bf16 v[16:19], a[56:59], a[20:23], v[16:19]\n"
        "s_waitcnt lgkmcnt(2)\n"
        "v_mfma_f32_16x16x32_bf16 v[20:23], a[60:63], a[20:23], v[20:23]\n"
        "v_mfma_f32_16x16x32_bf16 v[20:23], a[52:55], a[16:19], v[20:23]\n"
        "s_waitcnt vmcnt(20)\n"
        "s_barrier\n"
        "v_mfma_f32_16x16x32_bf16 v[28:31], a[52:55], a[24:27], v[28:31]\n"
        "global_load_lds_dwordx4 v144, s[60:61] offset:0\n"
        "ds_read_b128 a[0:3], v148 offset:32768\n"
        "v_mfma_f32_16x16x32_bf16 v[28:31], a[60:63], a[28:31], v[28:31]\n"
        "ds_read_b128 a[4:7], v148 offset:40960\n"
        "v_mfma_f32_16x16x32_bf16 v[24:27], a[56:59], a[28:31], v[24:27]\n"
        "ds_read_b128 a[8:11], v148 offset:33792\n"
        "v_mfma_f32_16x16x32_bf16 v[24:27], a[48:51], a[24:27], v[24:27]\n"
        "global_load_lds_dwordx4 v145, s[60:61] offset:1024\n"
        "ds_read_b128 a[12:15], v148 offset:41984\n"
        "v_mfma_f32_16x16x32_bf16 v[32:35], a[48:51], a[192:195], v[32:35]\n"
        "ds_read_b128 a[16:19], v148 offset:34816\n"
        "v_mfma_f32_16x16x32_bf16 v[32:35], a[56:59], a[196:199], v[32:35]\n"
        "ds_read_b128 a[20:23], v148 offset:43008\n"
        "s_waitcnt lgkmcnt(6)\n"
        "v_mfma_f32_16x16x32_bf16 v[36:39], a[60:63], a[196:199], v[36:39]\n"
        "global_load_lds_dwordx4 v146, s[60:61] offset:2048\n"
        "ds_read_b128 a[24:27], v148 offset:35840\n"
        "v_mfma_f32_16x16x32_bf16 v[36:39], a[52:55], a[192:195], v[36:39]\n"
        "LHB4_%=:\n"
        "s_cbranch_execz LHRB_%=\n"
        "ds_read_b128 a[28:31], v148 offset:44032\n"
        "v_mfma_f32_16x16x32_bf16 v[44:47], a[52:55], a[200:203], v[44:47]\n"
        "ds_read_b128 a[192:195], v151 offset:32768\n"
        "v_mfma_f32_16x16x32_bf16 v[44:47], a[60:63], a[204:207], v[44:47]\n"
        "global_load_lds_dwordx4 v147, s[60:61] offset:3072\n"
        "s_add_u32 m0, s73, 0x1c000\n"
        "s_waitcnt lgkmcnt(7)\n"
        "v_mfma_f32_16x16x32_bf16 v[40:43], a[56:59], a[204:207], v[40:43]\n"
        "ds_read_b128 a[196:199], v151 offset:40960\n"
        "s_add_u32 s60, s22, 0x2c0000\n"
        "s_addc_u32 s61, s23, 0\n"
        "v_mfma_f32_16x16x32_bf16 v[40:43], a[48:51], a[200:203], v[40:43]\n"
        "ds_read_b128 a[200:203], v151 offset:33792\n"
        "v_mfma_f32_16x16x32_bf16 v[48:51], a[48:51], a[208:211], v[48:51]\n"
        "global_load_lds_dwordx4 v254, s[20:21] offset:0 sc1\n"
        "ds_read_b128 a[204:207], v151 offset:41984\n"
        "v_mfma_f32_16x16x32_bf16 v[48:51], a[56:59], a[212:215], v[48:51]\n"
        "s_waitcnt lgkmcnt(6)\n"
        "v_mfma_f32_16x16x32_bf16 v[52:55], a[60:63], a[212:215], v[52:55]\n"
        "ds_read_b128 a[212:215], v151 offset:43008\n"
        "v_mfma_f32_16x16x32_bf16 v[52:55], a[52:55], a[208:211], v[52:55]\n"
        "global_load_lds_dwordx4 v254, s[20:21] offset:1024 sc1\n"
        "ds_read_b128 a[208:211], v151 offset:34816\n"
        "v_mfma_f32_16x16x32_bf16 v[60:63], a[52:55], a[216:219], v[60:63]\n"
        "v_mfma_f32_16x16x32_bf16 v[60:63], a[60:63], a[220:223], v[60:63]\n"
        "v_mfma_f32_16x16x32_bf16 v[56:59], a[56:59], a[220:223], v[56:59]\n"
        "global_load_lds_dwordx4 v254, s[20:21] offset:2048 sc1\n"
        "ds_read_b128 a[220:223], v151 offset:44032\n"
        "v_mfma_f32_16x16x32_bf16 v[56:59], a[48:51], a[216:219], v[56:59]\n"
        "ds_read_b128 a[216:219], v151 offset:35840\n"
        "v_mfma_f32_16x16x32_bf16 v[0:3], a[96:99], a[0:3], v[0:3]\n"
        "v_mfma_f32_16x16x32_bf16 v[0:3], a[104:107], a[4:7], v[0:3]\n"
        "global_load_lds_dwordx4 v254, s[20:21] offset:3072 sc1\n"
        "s_add_u32 s20, s20, 0x4000\n"
        "s_addc_u32 s21, s21, 0\n"
        "v_mfma_f32_16x16x32_bf16 v[4:7], a[108:111], a[4:7], v[4:7]\n"
        "s_add_u32 m0, s73, 0x20000\n"
        "s_waitcnt lgkmcnt(3)\n"
        "v_mfma_f32_16x16x32_bf16 v[4:7], a[100:103], a[0:3], v[4:7]\n"
        "v_mfma_f32_16x16x32_bf16 v[12:15], a[100:103], a[8:11], v[12:15]\n"
        "v_mfma_f32_16x16x32_bf16 v[12:15], a[108:111], a[12:15], v[12:15]\n"
        "v_mfma_f32_16x16x32_bf16 v[8:11], a[104:107], a[12:15], v[8:11]\n"
        "v_mfma_f32_16x16x32_bf16 v[8:11], a[96:99], a[8:11], v[8:11]\n"
        "v_mfma_f32_16x16x32_bf16 v[16:19], a[96:99], a[16:19], v[16:19]\n"
        "v_mfma_f32_16x16x32_bf16 v[16:19], a[104:107], a[20:23], v[16:19]\n"
        "v_mfma_f32_16x16x32_bf16 v[20:23], a[108:111], a[20:23], v[20:23]\n"
        "v_mfma_f32_16x16x32_bf16 v[20:23], a[100:103], a[16:19], v[20:23]\n"
        "s_waitcnt vmcnt(20)\n"
        "s_barrier\n"
        "v_mfma_f32_16x16x32_bf16 v[28:31], a[100:103], a[24:27], v[28:31]\n"
        "global_load_lds_dwordx4 v254, s[20:21] offset:0 sc1\n"
        "ds_read_b64_tr_b16 a[224:225], v154 offset:49152\n"
        "v_mfma_f32_16x16x32_bf16 v[28:31], a[108:111], a[28:31], v[28:31]\n"
        "ds_read_b64_tr_b16 a[226:227], v154 offset:53248\n"
        "s_waitcnt lgkmcnt(2)\n"
        "v_mfma_f32_16x16x32_bf16 v[24:27], a[104:107], a[28:31], v[24:27]\n"
        "ds_read_b64_tr_b16 a[232:233], v154 offset:57344\n"
        "v_mfma_f32_16x16x32_bf16 v[24:27], a[96:99], a[24:27], v[24:27]\n"
        "ds_read_b64_tr_b16 a[234:235], v154 offset:61440\n"
        "global_load_lds_dwordx4 v254, s[20:21] offset:1024 sc1\n"
        "v_mfma_f32_16x16x32_bf16 v[32:35], a[96:99], a[192:195], v[32:35]\n"
        "ds_read_b64_tr_b16 a[228:229], v155 offset:49152\n"
        "v_mfma_f32_16x16x32_bf16 v[32:35], a[104:107], a[196:199], v[32:35]\n"
        "ds_read_b64_tr_b16 a[230:231], v155 offset:53248\n"
        "v_mfma_f32_16x16x32_bf16 v[36:39], a[108:111], a[196:199], v[36:39]\n"
        "ds_read_b64_tr_b16 a[236:237], v155 offset:57344\n"
        "global_load_lds_dwordx4 v254, s[20:21] offset:2048 sc1\n"
        "v_mfma_f32_16x16x32_bf16 v[36:39], a[100:103], a[192:195], v[36:39]\n"
        "ds_read_b64_tr_b16 a[238:239], v155 offset:61440\n"
        "v_mfma_f32_16x16x32_bf16 v[44:47], a[100:103], a[200:203], v[44:47]\n"
        "ds_read_b128 a[0:3], v149 offset:0\n"
        "v_mfma_f32_16x16x32_bf16 v[44:47], a[108:111], a[204:207], v[44:47]\n"
        "ds_read_b128 a[4:7], v149 offset:8192\n"
        "global_load_lds_dwordx4 v254, s[20:21] offset:3072 sc1\n"
        "s_add_u32 m0, s73, 0x24000\n"
        "v_mfma_f32_16x16x32_bf16 v[40:43], a[104:107], a[204:207], v[40:43]\n"
        "ds_read_b128 a[8:11], v149 offset:1024\n"
        "s_add_u32 s20, s20, 0x4000\n"
        "s_addc_u32 s21, s21, 0\n"
        "v_mfma_f32_16x16x32_bf16 v[40:43], a[96:99], a[200:203], v[40:43]\n"
        "ds_read_b128 a[12:15], v149 offset:9216\n"
        "v_mfma_f32_16x16x32_bf16 v[48:51], a[96:99], a[208:211], v[48:51]\n"
        "ds_read_b128 a[16:19], v149 offset:2048\n"
        "global_load_lds_dwordx4 v144, s[60:61] offset:0\n"
        "v_mfma_f32_16x16x32_bf16 v[48:51], a[104:107], a[212:215], v[48:51]\n"
        "ds_read_b128 a[20:23], v149 offset:10240\n"
        "v_mfma_f32_16x16x32_bf16 v[52:55], a[108:111], a[212:215], v[52:55]\n"
        "ds_read_b128 a[24:27], v149 offset:3072\n"
        "v_mfma_f32_16x16x32_bf16 v[52:55], a[100:103], a[208:211], v[52:55]\n"
        "ds_read_b128 a[28:31], v149 offset:11264\n"
        "global_load_lds_dwordx4 v145, s[60:61] offset:1024\n"
        "v_mfma_f32_16x16x32_bf16 v[60:63], a[100:103], a[216:219], v[60:63]\n"
        "ds_read_b128 a[192:195], v152 offset:0\n"
        "s_waitcnt lgkmcnt(8)\n"
        "v_mfma_f32_16x16x32_bf16 v[60:63], a[108:111], a[220:223], v[60:63]\n"
        "ds_read_b128 a[196:199], v152 offset:8192\n"
        "s_waitcnt lgkmcnt(8)\n"
        "v_mfma_f32_16x16x32_bf16 v[56:59], a[104:107], a[220:223], v[56:59]\n"
        "global_load_lds_dwordx4 v146, s[60:61] offset:2048\n"
        "v_mfma_f32_16x16x32_bf16 v[56:59], a[96:99], a[216:219], v[56:59]\n"
        "ds_read_b128 a[200:203], v152 offset:1024\n"
        "s_waitcnt lgkmcnt(7)\n"
        "v_mfma_f32_16x16x32_bf16 v[0:3], a[224:227], a[0:3], v[0:3]\n"
        "ds_read_b128 a[204:207], v152 offset:9216\n"
        "s_waitcnt lgkmcnt(7)\n"
        "v_mfma_f32_16x16x32_bf16 v[0:3], a[232:235], a[4:7], v[0:3]\n"
        "global_load_lds_dwordx4 v147, s[60:61] offset:3072\n"
        "s_add_u32 m0, s73, 0x8000\n"
        "v_mfma_f32_16x16x32_bf16 v[4:7], a[236:239], a[4:7], v[4:7]\n"
        "ds_read_b128 a[208:211], v152 offset:2048\n"
        "s_add_u32 s60, s22, 0x2e0000\n"
        "s_addc_u32 s61, s23, 0\n"
        "v_mfma_f32_16x16x32_bf16 v[4:7], a[228:231], a[0:3], v[4:7]\n"
        "ds_read_b128 a[212:215], v152 offset:10240\n"
        "s_waitcnt lgkmcnt(6)\n"
        "v_mfma_f32_16x16x32_bf16 v[12:15], a[228:231], a[8:11], v[12:15]\n"
        "ds_read_b128 a[216:219], v152 offset:3072\n"
        "v_mfma_f32_16x16x32_bf16 v[12:15], a[236:239], a[12:15], v[12:15]\n"
        "ds_read_b128 a[220:223], v152 offset:11264\n"
        "s_waitcnt lgkmcnt(6)\n"
        "v_mfma_f32_16x16x32_bf16 v[8:11], a[232:235], a[12:15], v[8:11]\n"
        "v_mfma_f32_16x16x32_bf16 v[8:11], a[224:227], a[8:11], v[8:11]\n"
        "v_mfma_f32_16x16x32_bf16 v[16:19], a[224:227], a[16:19], v[16:19]\n"
        "v_mfma_f32_16x16x32_bf16 v[16:19], a[232:235], a[20:23], v[16:19]\n"
        "s_waitcnt lgkmcnt(4)\n"
        "v_mfma_f32_16x16x32_bf16 v[20:23], a[236:239], a[20:23], v[20:23]\n"
        "v_mfma_f32_16x16x32_bf16 v[20:23], a[228:231], a[16:19], v[20:23]\n"
        "s_waitcnt vmcnt(24)\n"
        "s_barrier\n"
        "v_mfma_f32_16x16x32_bf16 v[28:31], a[228:231], a[24:27], v[28:31]\n"
        "global_load_lds_dwordx4 v254, s[20:21] offset:0 sc1\n"
        "ds_read_b128 a[0:3], v149 offset:16384\n"
        "v_mfma_f32_16x16x32_bf16 v[28:31], a[236:239], a[28:31], v[28:31]\n"
        "ds_read_b128 a[4:7], v149 offset:24576\n"
        "s_waitcnt lgkmcnt(2)\n"
        "v_mfma_f32_16x16x32_bf16 v[24:27], a[232:235], a[28:31], v[24:27]\n"
        "ds_read_b128 a[8:11], v149 offset:17408\n"
        "v_mfma_f32_16x16x32_bf16 v[24:27], a[224:227], a[24:27], v[24:27]\n"
        "global_load_lds_dwordx4 v254, s[20:21] offset:1024 sc1\n"
        "ds_read_b128 a[12:15], v149 offset:25600\n"
        "v_mfma_f32_16x16x32_bf16 v[32:35], a[224:227], a[192:195], v[32:35]\n"
        "ds_read_b128 a[16:19], v149 offset:18432\n"
        "v_mfma_f32_16x16x32_bf16 v[32:35], a[232:235], a[196:199], v[32:35]\n"
        "ds_read_b128 a[20:23], v149 offset:26624\n"
        "v_mfma_f32_16x16x32_bf16 v[36:39], a[236:239], a[196:199], v[36:39]\n"
        "global_load_lds_dwordx4 v254, s[20:21] offset:2048 sc1\n"
        "ds_read_b128 a[24:27], v149 offset:19456\n"
        "v_mfma_f32_16x16x32_bf16 v[36:39], a[228:231], a[192:195], v[36:39]\n"
        "ds_read_b128 a[28:31], v149 offset:27648\n"
        "v_mfma_f32_16x16x32_bf16 v[44:47], a[228:231], a[200:203], v[44:47]\n"
        "ds_read_b128 a[192:195], v152 offset:16384\n"
        "v_mfma_f32_16x16x32_bf16 v[44:47], a[236:239], a[204:207], v[44:47]\n"
        "global_load_lds_dwordx4 v254, s[20:21] offset:3072 sc1\n"
        "s_add_u32 s20, s20, 0x4000\n"
        "s_addc_u32 s21, s21, 0\n"
        "v_mfma_f32_16x16x32_bf16 v[40:43], a[232:235], a[204:207], v[40:43]\n"
        "ds_read_b128 a[196:199], v152 offset:24576\n"
        "s_add_u32 m0, s73, 0xc000\n"
        "s_waitcnt lgkmcnt(7)\n"
        "v_mfma_f32_16x16x32_bf16 v[40:43], a[224:227], a[200:203], v[40:43]\n"
        "ds_read_b128 a[200:203], v152 offset:17408\n"
        "v_mfma_f32_16x16x32_bf16 v[48:51], a[224:227], a[208:211], v[48:51]\n"
        "global_load_dwordx2 v[236:237], v234, s[48:49] sc1\n"
        "ds_read_b128 a[204:207], v152 offset:25600\n"
        "v_mfma_f32_16x16x32_bf16 v[48:51], a[232:235], a[212:215], v[48:51]\n"
        "v_mfma_f32_16x16x32_bf16 v[52:55], a[236:239], a[212:215], v[52:55]\n"
        "ds_read_b128 a[212:215], v152 offset:26624\n"
        "v_mfma_f32_16x16x32_bf16 v[52:55], a[228:231], a[208:211], v[52:55]\n"
        "ds_read_b128 a[208:211], v152 offset:18432\n"
        "s_waitcnt lgkmcnt(6)\n"
        "v_mfma_f32_16x16x32_bf16 v[60:63], a[228:231], a[216:219], v[60:63]\n"
        "v_mfma_f32_16x16x32_bf16 v[60:63], a[236:239], a[220:223], v[60:63]\n"
        "v_mfma_f32_16x16x32_bf16 v[56:59], a[232:235], a[220:223], v[56:59]\n"
        "ds_read_b128 a[220:223], v152 offset:27648\n"
        "v_mfma_f32_16x16x32_bf16 v[56:59], a[224:227], a[216:219], v[56:59]\n"
        "ds_read_b128 a[216:219], v152 offset:19456\n"
        "v_mfma_f32_16x16x32_bf16 v[0:3], a[112:115], a[0:3], v[0:3]\n"
        "v_mfma_f32_16x16x32_bf16 v[0:3], a[120:123], a[4:7], v[0:3]\n"
        "v_mfma_f32_16x16x32_bf16 v[4:7], a[124:127], a[4:7], v[4:7]\n"
        "v_mfma_f32_16x16x32_bf16 v[4:7], a[116:119], a[0:3], v[4:7]\n"
        "v_mfma_f32_16x16x32_bf16 v[12:15], a[116:119], a[8:11], v[12:15]\n"
        "v_mfma_f32_16x16x32_bf16 v[12:15], a[124:127], a[12:15], v[12:15]\n"
        "s_waitcnt lgkmcnt(2)\n"
        "v_mfma_f32_16x16x32_bf16 v[8:11], a[120:123], a[12:15], v[8:11]\n"
        "v_mfma_f32_16x16x32_bf16 v[8:11], a[112:115], a[8:11], v[8:11]\n"
        "v_mfma_f32_16x16x32_bf16 v[16:19], a[112:115], a[16:19], v[16:19]\n"
        "v_mfma_f32_16x16x32_bf16 v[16:19], a[120:123], a[20:23], v[16:19]\n"
        "v_mfma_f32_16x16x32_bf16 v[20:23], a[124:127], a[20:23], v[20:23]\n"
        "v_mfma_f32_16x16x32_bf16 v[20:23], a[116:119], a[16:19], v[20:23]\n"
        "s_waitcnt vmcnt(21)\n"
        "s_barrier\n"
        "v_mfma_f32_16x16x32_bf16 v[28:31], a[116:119], a[24:27], v[28:31]\n"
        "global_load_lds_dwordx4 v144, s[60:61] offset:0\n"
        "ds_read_b64_tr_b16 a[240:241], v154 offset:0\n"
        "v_mfma_f32_16x16x32_bf16 v[28:31], a[124:127], a[28:31], v[28:31]\n"
        "ds_read_b64_tr_b16 a[242:243], v154 offset:4096\n"
        "v_mfma_f32_16x16x32_bf16 v[24:27], a[120:123], a[28:31], v[24:27]\n"
        "ds_read_b64_tr_b16 a[248:249], v154 offset:8192\n"
        "v_mfma_f32_16x16x32_bf16 v[24:27], a[112:115], a[24:27], v[24:27]\n"
        "ds_read_b64_tr_b16 a[250:251], v154 offset:12288\n"
        "global_load_lds_dwordx4 v145, s[60:61] offset:1024\n"
        "v_mfma_f32_16x16x32_bf16 v[32:35], a[112:115], a[192:195], v[32:35]\n"
        "ds_read_b64_tr_b16 a[244:245], v155 offset:0\n"
        "v_mfma_f32_16x16x32_bf16 v[32:35], a[120:123], a[196:199], v[32:35]\n"
        "ds_read_b64_tr_b16 a[246:247], v155 offset:4096\n"
        "s_waitcnt lgkmcnt(6)\n"
        "v_mfma_f32_16x16x32_bf16 v[36:39], a[124:127], a[196:199], v[36:39]\n"
        "ds_read_b64_tr_b16 a[252:253], v155 offset:8192\n"
        "global_load_lds_dwordx4 v146, s[60:61] offset:2048\n"
        "v_mfma_f32_16x16x32_bf16 v[36:39], a[116:119], a[192:195], v[36:39]\n"
        "ds_read_b64_tr_b16 a[254:255], v155 offset:12288\n"
        "v_mfma_f32_16x16x32_bf16 v[44:47], a[116:119], a[200:203], v[44:47]\n"
        "ds_read_b128 a[0:3], v148 offset:16384\n"
        "v_mfma_f32_16x16x32_bf16 v[44:47], a[124:127], a[204:207], v[44:47]\n"
        "ds_read_b128 a[4:7], v148 offset:24576\n"
        "global_load_lds_dwordx4 v147, s[60:61] offset:3072\n"
        "s_add_u32 m0, s73, 0x10000\n"
        "v_mfma_f32_16x16x32_bf16 v[40:43], a[120:123], a[204:207], v[40:43]\n"
        "ds_read_b128 a[8:11], v148 offset:17408\n"
        "s_add_u32 s60, s22, 0x320000\n"
        "s_addc_u32 s61, s23, 0\n"
        "v_mfma_f32_16x16x32_bf16 v[40:43], a[112:115], a[200:203], v[40:43]\n"
        "ds_read_b128 a[12:15], v148 offset:25600\n"
        "v_mfma_f32_16x16x32_bf16 v[48:51], a[112:115], a[208:211], v[48:51]\n"
        "ds_read_b128 a[16:19], v148 offset:18432\n"
        "global_load_lds_dwordx4 v254, s[20:21] offset:0 sc1\n"
        "v_mfma_f32_16x16x32_bf16 v[48:51], a[120:123], a[212:215], v[48:51]\n"
        "ds_read_b128 a[20:23], v148 offset:26624\n"
        "v_mfma_f32_16x16x32_bf16 v[52:55], a[124:127], a[212:215], v[52:55]\n"
        "ds_read_b128 a[24:27], v148 offset:19456\n"
        "v_mfma_f32_16x16x32_bf16 v[52:55], a[116:119], a[208:211], v[52:55]\n"
        "ds_read_b128 a[28:31], v148 offset:27648\n"
        "global_load_lds_dwordx4 v254, s[20:21] offset:1024 sc1\n"
        "v_mfma_f32_16x16x32_bf16 v[60:63], a[116:119], a[216:219], v[60:63]\n"
        "ds_read_b128 a[192:195], v151 offset:16384\n"
        "s_waitcnt lgkmcnt(8)\n"
        "v_mfma_f32_16x16x32_bf16 v[60:63], a[124:127], a[220:223], v[60:63]\n"
        "ds_read_b128 a[196:199], v151 offset:24576\n"
        "s_waitcnt lgkmcnt(8)\n"
        "v_mfma_f32_16x16x32_bf16 v[56:59], a[120:123], a[220:223], v[56:59]\n"
        "global_load_lds_dwordx4 v254, s[20:21] offset:2048 sc1\n"
        "v_mfma_f32_16x16x32_bf16 v[56:59], a[112:115], a[216:219], v[56:59]\n"
        "ds_read_b128 a[200:203], v151 offset:17408\n"
        "s_waitcnt lgkmcnt(7)\n"
        "v_mfma_f32_16x16x32_bf16 v[0:3], a[240:243], a[0:3], v[0:3]\n"
        "ds_read_b128 a[204:207], v151 offset:25600\n"
        "s_waitcnt lgkmcnt(7)\n"
        "v_mfma_f32_16x16x32_bf16 v[0:3], a[248:251], a[4:7], v[0:3]\n"
        "global_load_lds_dwordx4 v254, s[20:21] offset:3072 sc1\n"
        "s_add_u32 m0, s73, 0x14000\n"
        "v_mfma_f32_16x16x32_bf16 v[4:7], a[252:255], a[4:7], v[4:7]\n"
        "ds_read_b128 a[208:211], v151 offset:18432\n"
        "s_add_u32 s20, s20, 0x4000\n"
        "s_addc_u32 s21, s21, 0\n"
        "v_mfma_f32_16x16x32_bf16 v[4:7], a[244:247], a[0:3], v[4:7]\n"
        "ds_read_b128 a[212:215], v151 offset:26624\n"
        "s_waitcnt lgkmcnt(6)\n"
        "v_mfma_f32_16x16x32_bf16 v[12:15], a[244:247], a[8:11], v[12:15]\n"
        "ds_read_b128 a[216:219], v151 offset:19456\n"
        "v_mfma_f32_16x16x32_bf16 v[12:15], a[252:255], a[12:15], v[12:15]\n"
        "ds_read_b128 a[220:223], v151 offset:27648\n"
        "s_waitcnt lgkmcnt(6)\n"
        "v_mfma_f32_16x16x32_bf16 v[8:11], a[248:251], a[12:15], v[8:11]\n"
        "v_mfma_f32_16x16x32_bf16 v[8:11], a[240:243], a[8:11], v[8:11]\n"
        "v_mfma_f32_16x16x32_bf16 v[16:19], a[240:243], a[16:19], v[16:19]\n"
        "v_mfma_f32_16x16x32_bf16 v[16:19], a[248:251], a[20:23], v[16:19]\n"
        "s_waitcnt lgkmcnt(4)\n"
        "v_mfma_f32_16x16x32_bf16 v[20:23], a[252:255], a[20:23], v[20:23]\n"
        "v_mfma_f32_16x16x32_bf16 v[20:23], a[244:247], a[16:19], v[20:23]\n"
        "s_waitcnt vmcnt(21)\n"
        "s_barrier\n"
        "v_mfma_f32_16x16x32_bf16 v[28:31], a[244:247], a[24:27], v[28:31]\n"
        "global_load_lds_dwordx4 v254, s[20:21] offset:0 sc1\n"
        "ds_read_b64_tr_b16 a[32:33], v156 offset:32768\n"
        "v_mfma_f32_16x16x32_bf16 v[28:31], a[252:255], a[28:31], v[28:31]\n"
        "ds_read_b64_tr_b16 a[34:35], v156 offset:36864\n"
        "s_waitcnt lgkmcnt(2)\n"
        "v_mfma_f32_16x16x32_bf16 v[24:27], a[248:251], a[28:31], v[24:27]\n"
        "ds_read_b64_tr_b16 a[40:41], v156 offset:40960\n"
        "v_mfma_f32_16x16x32_bf16 v[24:27], a[240:243], a[24:27], v[24:27]\n"
        "ds_read_b64_tr_b16 a[42:43], v156 offset:45056\n"
        "global_load_lds_dwordx4 v254, s[20:21] offset:1024 sc1\n"
        "v_mfma_f32_16x16x32_bf16 v[32:35], a[240:243], a[192:195], v[32:35]\n"
        "ds_read_b64_tr_b16 a[36:37], v157 offset:32768\n"
        "v_mfma_f32_16x16x32_bf16 v[32:35], a[248:251], a[196:199], v[32:35]\n"
        "ds_read_b64_tr_b16 a[38:39], v157 offset:36864\n"
        "v_mfma_f32_16x16x32_bf16 v[36:39], a[252:255], a[196:199], v[36:39]\n"
        "ds_read_b64_tr_b16 a[44:45], v157 offset:40960\n"
        "global_load_lds_dwordx4 v254, s[20:21] offset:2048 sc1\n"
        "v_mfma_f32_16x16x32_bf16 v[36:39], a[244:247], a[192:195], v[36:39]\n"
        "ds_read_b64_tr_b16 a[46:47], v157 offset:45056\n"
        "v_mfma_f32_16x16x32_bf16 v[44:47], a[244:247], a[200:203], v[44:47]\n"
        "ds_read_b128 a[0:3], v149 offset:49152\n"
        "v_mfma_f32_16x16x32_bf16 v[44:47], a[252:255], a[204:207], v[44:47]\n"
        "ds_read_b128 a[4:7], v149 offset:57344\n"
        "global_load_lds_dwordx4 v254, s[20:21] offset:3072 sc1\n"
        "s_add_u32 m0, s73, 0x0\n"
        "v_mfma_f32_16x16x32_bf16 v[40:43], a[248:251], a[204:207], v[40:43]\n"
        "ds_read_b128 a[8:11], v149 offset:50176\n"
        "s_add_u32 s20, s20, 0x4000\n"
        "s_addc_u32 s21, s21, 0\n"
        "v_mfma_f32_16x16x32_bf16 v[40:43], a[240:243], a[200:203], v[40:43]\n"
        "ds_read_b128 a[12:15], v149 offset:58368\n"
        "v_mfma_f32_16x16x32_bf16 v[48:51], a[240:243], a[208:211], v[48:51]\n"
        "ds_read_b128 a[16:19], v149 offset:51200\n"
        "v_mfma_f32_16x16x32_bf16 v[48:51], a[248:251], a[212:215], v[48:51]\n"
        "ds_read_b128 a[20:23], v149 offset:59392\n"
        "v_mfma_f32_16x16x32_bf16 v[52:55], a[252:255], a[212:215], v[52:55]\n"
        "ds_read_b128 a[24:27], v149 offset:52224\n"
        "v_mfma_f32_16x16x32_bf16 v[52:55], a[244:247], a[208:211], v[52:55]\n"
        "ds_read_b128 a[28:31], v149 offset:60416\n"
        "s_waitcnt lgkmcnt(8)\n"
        "v_mfma_f32_16x16x32_bf16 v[60:63], a[244:247], a[216:219], v[60:63]\n"
        "ds_read_b128 a[192:195], v152 offset:49152\n"
        "s_waitcnt lgkmcnt(8)\n"
        "v_mfma_f32_16x16x32_bf16 v[60:63], a[252:255], a[220:223], v[60:63]\n"
        "ds_read_b128 a[196:199], v152 offset:57344\n"
        "s_waitcnt lgkmcnt(8)\n"
        "v_mfma_f32_16x16x32_bf16 v[56:59], a[248:251], a[220:223], v[56:59]\n"
        "ds_read_b128 a[200:203], v152 offset:50176\n"
        "s_waitcnt lgkmcnt(8)\n"
        "v_mfma_f32_16x16x32_bf16 v[56:59], a[240:243], a[216:219], v[56:59]\n"
        "ds_read_b128 a[204:207], v152 offset:58368\n"
        "s_waitcnt lgkmcnt(8)\n"
        "v_mfma_f32_16x16x32_bf16 v[0:3], a[32:35], a[0:3], v[0:3]\n"
        "ds_read_b128 a[208:211], v152 offset:51200\n"
        "s_waitcnt lgkmcnt(8)\n"
        "v_mfma_f32_16x16x32_bf16 v[0:3], a[40:43], a[4:7], v[0:3]\n"
        "ds_read_b128 a[212:215], v152 offset:59392\n"
        "s_waitcnt lgkmcnt(8)\n"
        "v_mfma_f32_16x16x32_bf16 v[4:7], a[44:47], a[4:7], v[4:7]\n"
        "ds_read_b128 a[216:219], v152 offset:52224\n"
        "s_waitcnt lgkmcnt(8)\n"
        "v_mfma_f32_16x16x32_bf16 v[4:7], a[36:39], a[0:3], v[4:7]\n"
        "ds_read_b128 a[220:223], v152 offset:60416\n"
        "s_waitcnt lgkmcnt(8)\n"
        "v_mfma_f32_16x16x32_bf16 v[12:15], a[36:39], a[8:11], v[12:15]\n"
        "v_mfma_f32_16x16x32_bf16 v[12:15], a[44:47], a[12:15], v[12:15]\n"
        "s_waitcnt lgkmcnt(6)\n"
        "v_mfma_f32_16x16x32_bf16 v[8:11], a[40:43], a[12:15], v[8:11]\n"
        "v_mfma_f32_16x16x32_bf16 v[8:11], a[32:35], a[8:11], v[8:11]\n"
        "v_mfma_f32_16x16x32_bf16 v[16:19], a[32:35], a[16:19], v[16:19]\n"
        "v_mfma_f32_16x16x32_bf16 v[16:19], a[40:43], a[20:23], v[16:19]\n"
        "s_waitcnt lgkmcnt(2)\n"
        "v_mfma_f32_16x16x32_bf16 v[20:23], a[44:47], a[20:23], v[20:23]\n"
        "v_mfma_f32_16x16x32_bf16 v[20:23], a[36:39], a[16:19], v[20:23]\n"
        "s_waitcnt vmcnt(21)\n"
        "s_barrier\n"
        "v_mfma_f32_16x16x32_bf16 v[28:31], a[36:39], a[24:27], v[28:31]\n"
        "global_load_lds_dwordx4 v144, s[60:61] offset:0\n"
        "ds_read_b128 a[0:3], v150 offset:0\n"
        "v_mfma_f32_16x16x32_bf16 v[28:31], a[44:47], a[28:31], v[28:31]\n"
        "ds_read_b128 a[4:7], v150 offset:8192\n"
        "v_mfma_f32_16x16x32_bf16 v[24:27], a[40:43], a[28:31], v[24:27]\n"
        "ds_read_b128 a[8:11], v150 offset:1024\n"
        "v_mfma_f32_16x16x32_bf16 v[24:27], a[32:35], a[24:27], v[24:27]\n"
        "global_load_lds_dwordx4 v145, s[60:61] offset:1024\n"
        "ds_read_b128 a[12:15], v150 offset:9216\n"
        "v_mfma_f32_16x16x32_bf16 v[32:35], a[32:35], a[192:195], v[32:35]\n"
        "ds_read_b128 a[16:19], v150 offset:2048\n"
        "v_mfma_f32_16x16x32_bf16 v[32:35], a[40:43], a[196:199], v[32:35]\n"
        "ds_read_b128 a[20:23], v150 offset:10240\n"
        "s_waitcnt lgkmcnt(6)\n"
        "v_mfma_f32_16x16x32_bf16 v[36:39], a[44:47], a[196:199], v[36:39]\n"
        "global_load_lds_dwordx4 v146, s[60:61] offset:2048\n"
        "ds_read_b128 a[24:27], v150 offset:3072\n"
        "v_mfma_f32_16x16x32_bf16 v[36:39], a[36:39], a[192:195], v[36:39]\n"
        "ds_read_b128 a[28:31], v150 offset:11264\n"
        "v_mfma_f32_16x16x32_bf16 v[44:47], a[36:39], a[200:203], v[44:47]\n"
        "ds_read_b128 a[192:195], v153 offset:0\n"
        "v_mfma_f32_16x16x32_bf16 v[44:47], a[44:47], a[204:207], v[44:47]\n"
        "global_load_lds_dwordx4 v147, s[60:61] offset:3072\n"
        "s_add_u32 m0, s73, 0x4000\n"
        "s_waitcnt lgkmcnt(7)\n"
        "v_mfma_f32_16x16x32_bf16 v[40:43], a[40:43], a[204:207], v[40:43]\n"
        "ds_read_b128 a[196:199], v153 offset:8192\n"
        "s_add_u32 s60, s22, 0x360000\n"
        "s_addc_u32 s61, s23, 0\n"
        "v_mfma_f32_16x16x32_bf16 v[40:43], a[32:35], a[200:203], v[40:43]\n"
        "ds_read_b128 a[200:203], v153 offset:1024\n"
        "v_mfma_f32_16x16x32_bf16 v[48:51], a[32:35], a[208:211], v[48:51]\n"
        "global_load_lds_dwordx4 v254, s[20:21] offset:0 sc1\n"
        "ds_read_b128 a[204:207], v153 offset:9216\n"
        "v_mfma_f32_16x16x32_bf16 v[48:51], a[40:43], a[212:215], v[48:51]\n"
        "s_waitcnt lgkmcnt(6)\n"
        "v_mfma_f32_16x16x32_bf16 v[52:55], a[44:47], a[212:215], v[52:55]\n"
        "ds_read_b128 a[212:215], v153 offset:10240\n"
        "v_mfma_f32_16x16x32_bf16 v[52:55], a[36:39], a[208:211], v[52:55]\n"
        "global_load_lds_dwordx4 v254, s[20:21] offset:1024 sc1\n"
        "ds_read_b128 a[208:211], v153 offset:2048\n"
        "v_mfma_f32_16x16x32_bf16 v[60:63], a[36:39], a[216:219], v[60:63]\n"
        "v_mfma_f32_16x16x32_bf16 v[60:63], a[44:47], a[220:223], v[60:63]\n"
        "v_mfma_f32_16x16x32_bf16 v[56:59], a[40:43], a[220:223], v[56:59]\n"
        "global_load_lds_dwordx4 v254, s[20:21] offset:2048 sc1\n"
        "ds_read_b128 a[220:223], v153 offset:11264\n"
        "v_mfma_f32_16x16x32_bf16 v[56:59], a[32:35], a[216:219], v[56:59]\n"
        "ds_read_b128 a[216:219], v153 offset:3072\n"
        "v_mfma_f32_16x16x32_bf16 v[0:3], a[128:131], a[0:3], v[0:3]\n"
        "v_mfma_f32_16x16x32_bf16 v[0:3], a[136:139], a[4:7], v[0:3]\n"
        "global_load_lds_dwordx4 v254, s[20:21] offset:3072 sc1\n"
        "s_add_u32 s20, s20, 0x4000\n"
        "s_addc_u32 s21, s21, 0\n"
        "v_mfma_f32_16x16x32_bf16 v[4:7], a[140:143], a[4:7], v[4:7]\n"
        "s_add_u32 m0, s73, 0x18000\n"
        "s_waitcnt lgkmcnt(3)\n"
        "v_mfma_f32_16x16x32_bf16 v[4:7], a[132:135], a[0:3], v[4:7]\n"
        "v_mfma_f32_16x16x32_bf16 v[12:15], a[132:135], a[8:11], v[12:15]\n"
        "v_mfma_f32_16x16x32_bf16 v[12:15], a[140:143], a[12:15], v[12:15]\n"
        "v_mfma_f32_16x16x32_bf16 v[8:11], a[136:139], a[12:15], v[8:11]\n"
        "v_mfma_f32_16x16x32_bf16 v[8:11], a[128:131], a[8:11], v[8:11]\n"
        "v_mfma_f32_16x16x32_bf16 v[16:19], a[128:131], a[16:19], v[16:19]\n"
        "v_mfma_f32_16x16x32_bf16 v[16:19], a[136:139], a[20:23], v[16:19]\n"
        "v_mfma_f32_16x16x32_bf16 v[20:23], a[140:143], a[20:23], v[20:23]\n"
        "v_mfma_f32_16x16x32_bf16 v[20:23], a[132:135], a[16:19], v[20:23]\n"
        "s_waitcnt vmcnt(21)\n"
        "s_barrier\n"
        "v_mfma_f32_16x16x32_bf16 v[28:31], a[132:135], a[24:27], v[28:31]\n"
        "global_load_lds_dwordx4 v254, s[20:21] offset:0 sc1\n"
        "ds_read_b64_tr_b16 a[48:49], v158 offset:16384\n"
        "v_mfma_f32_16x16x32_bf16 v[28:31], a[140:143], a[28:31], v[28:31]\n"
        "ds_read_b64_tr_b16 a[50:51], v158 offset:20480\n"
        "s_waitcnt lgkmcnt(2)\n"
        "v_mfma_f32_16x16x32_bf16 v[24:27], a[136:139], a[28:31], v[24:27]\n"
        "ds_read_b64_tr_b16 a[56:57], v158 offset:24576\n"
        "v_mfma_f32_16x16x32_bf16 v[24:27], a[128:131], a[24:27], v[24:27]\n"
        "ds_read_b64_tr_b16 a[58:59], v158 offset:28672\n"
        "global_load_lds_dwordx4 v254, s[20:21] offset:1024 sc1\n"
        "v_mfma_f32_16x16x32_bf16 v[32:35], a[128:131], a[192:195], v[32:35]\n"
        "ds_read_b64_tr_b16 a[52:53], v159 offset:16384\n"
        "v_mfma_f32_16x16x32_bf16 v[32:35], a[136:139], a[196:199], v[32:35]\n"
        "ds_read_b64_tr_b16 a[54:55], v159 offset:20480\n"
        "v_mfma_f32_16x16x32_bf16 v[36:39], a[140:143], a[196:199], v[36:39]\n"
        "ds_read_b64_tr_b16 a[60:61], v159 offset:24576\n"
        "global_load_lds_dwordx4 v254, s[20:21] offset:2048 sc1\n"
        "v_mfma_f32_16x16x32_bf16 v[36:39], a[132:135], a[192:195], v[36:39]\n"
        "ds_read_b64_tr_b16 a[62:63], v159 offset:28672\n"
        "v_mfma_f32_16x16x32_bf16 v[44:47], a[132:135], a[200:203], v[44:47]\n"
        "ds_read_b128 a[0:3], v148 offset:32768\n"
        "v_mfma_f32_16x16x32_bf16 v[44:47], a[140:143], a[204:207], v[44:47]\n"
        "ds_read_b128 a[4:7], v148 offset:40960\n"
        "global_load_lds_dwordx4 v254, s[20:21] offset:3072 sc1\n"
        "s_add_u32 m0, s73, 0x1c000\n"
        "v_mfma_f32_16x16x32_bf16 v[40:43], a[136:139], a[204:207], v[40:43]\n"
        "ds_read_b128 a[8:11], v148 offset:33792\n"
        "s_add_u32 s20, s20, 0x4000\n"
        "s_addc_u32 s21, s21, 0\n"
        "v_mfma_f32_16x16x32_bf16 v[40:43], a[128:131], a[200:203], v[40:43]\n"
        "ds_read_b128 a[12:15], v148 offset:41984\n"
        "v_mfma_f32_16x16x32_bf16 v[48:51], a[128:131], a[208:211], v[48:51]\n"
        "ds_read_b128 a[16:19], v148 offset:34816\n"
        "global_load_lds_dwordx4 v144, s[60:61] offset:0\n"
        "v_mfma_f32_16x16x32_bf16 v[48:51], a[136:139], a[212:215], v[48:51]\n"
        "ds_read_b128 a[20:23], v148 offset:43008\n"
        "v_mfma_f32_16x16x32_bf16 v[52:55], a[140:143], a[212:215], v[52:55]\n"
        "ds_read_b128 a[24:27], v148 offset:35840\n"
        "v_mfma_f32_16x16x32_bf16 v[52:55], a[132:135], a[208:211], v[52:55]\n"
        "ds_read_b128 a[28:31], v148 offset:44032\n"
        "global_load_lds_dwordx4 v145, s[60:61] offset:1024\n"
        "v_mfma_f32_16x16x32_bf16 v[60:63], a[132:135], a[216:219], v[60:63]\n"
        "ds_read_b128 a[192:195], v151 offset:32768\n"
        "s_waitcnt lgkmcnt(8)\n"
        "v_mfma_f32_16x16x32_bf16 v[60:63], a[140:143], a[220:223], v[60:63]\n"
        "ds_read_b128 a[196:199], v151 offset:40960\n"
        "s_waitcnt lgkmcnt(8)\n"
        "v_mfma_f32_16x16x32_bf16 v[56:59], a[136:139], a[220:223], v[56:59]\n"
        "global_load_lds_dwordx4 v146, s[60:61] offset:2048\n"
        "v_mfma_f32_16x16x32_bf16 v[56:59], a[128:131], a[216:219], v[56:59]\n"
        "ds_read_b128 a[200:203], v151 offset:33792\n"
        "s_waitcnt lgkmcnt(7)\n"
        "v_mfma_f32_16x16x32_bf16 v[0:3], a[48:51], a[0:3], v[0:3]\n"
        "ds_read_b128 a[204:207], v151 offset:41984\n"
        "s_waitcnt lgkmcnt(7)\n"
        "v_mfma_f32_16x16x32_bf16 v[0:3], a[56:59], a[4:7], v[0:3]\n"
        "global_load_lds_dwordx4 v147, s[60:61] offset:3072\n"
        "s_add_u32 m0, s73, 0x20000\n"
        "v_mfma_f32_16x16x32_bf16 v[4:7], a[60:63], a[4:7], v[4:7]\n"
        "ds_read_b128 a[208:211], v151 offset:34816\n"
        "s_add_u32 s60, s22, 0x380000\n"
        "s_addc_u32 s61, s23, 0\n"
        "v_mfma_f32_16x16x32_bf16 v[4:7], a[52:55], a[0:3], v[4:7]\n"
        "ds_read_b128 a[212:215], v151 offset:43008\n"
        "s_waitcnt lgkmcnt(6)\n"
        "v_mfma_f32_16x16x32_bf16 v[12:15], a[52:55], a[8:11], v[12:15]\n"
        "ds_read_b128 a[216:219], v151 offset:35840\n"
        "v_mfma_f32_16x16x32_bf16 v[12:15], a[60:63], a[12:15], v[12:15]\n"
        "ds_read_b128 a[220:223], v151 offset:44032\n"
        "s_waitcnt lgkmcnt(6)\n"
        "v_mfma_f32_16x16x32_bf16 v[8:11], a[56:59], a[12:15], v[8:11]\n"
        "v_mfma_f32_16x16x32_bf16 v[8:11], a[48:51], a[8:11], v[8:11]\n"
        "v_mfma_f32_16x16x32_bf16 v[16:19], a[48:51], a[16:19], v[16:19]\n"
        "v_mfma_f32_16x16x32_bf16 v[16:19], a[56:59], a[20:23], v[16:19]\n"
        "s_waitcnt lgkmcnt(4)\n"
        "v_mfma_f32_16x16x32_bf16 v[20:23], a[60:63], a[20:23], v[20:23]\n"
        "v_mfma_f32_16x16x32_bf16 v[20:23], a[52:55], a[16:19], v[20:23]\n"
        "s_waitcnt vmcnt(20)\n"
        "s_barrier\n"
        "v_mfma_f32_16x16x32_bf16 v[28:31], a[52:55], a[24:27], v[28:31]\n"
        "global_load_lds_dwordx4 v254, s[20:21] offset:0 sc1\n"
        "ds_read_b64_tr_b16 a[224:225], v154 offset:49152\n"
        "v_mfma_f32_16x16x32_bf16 v[28:31], a[60:63], a[28:31], v[28:31]\n"
        "ds_read_b64_tr_b16 a[226:227], v154 offset:53248\n"
        "s_waitcnt lgkmcnt(2)\n"
        "v_mfma_f32_16x16x32_bf16 v[24:27], a[56:59], a[28:31], v[24:27]\n"
        "ds_read_b64_tr_b16 a[232:233], v154 offset:57344\n"
        "v_mfma_f32_16x16x32_bf16 v[24:27], a[48:51], a[24:27], v[24:27]\n"
        "ds_read_b64_tr_b16 a[234:235], v154 offset:61440\n"
        "global_load_lds_dwordx4 v254, s[20:21] offset:1024 sc1\n"
        "v_mfma_f32_16x16x32_bf16 v[32:35], a[48:51], a[192:195], v[32:35]\n"
        "ds_read_b64_tr_b16 a[228:229], v155 offset:49152\n"
        "v_mfma_f32_16x16x32_bf16 v[32:35], a[56:59], a[196:199], v[32:35]\n"
        "ds_read_b64_tr_b16 a[230:231], v155 offset:53248\n"
        "v_mfma_f32_16x16x32_bf16 v[36:39], a[60:63], a[196:199], v[36:39]\n"
        "ds_read_b64_tr_b16 a[236:237], v155 offset:57344\n"
        "global_load_lds_dwordx4 v254, s[20:21] offset:2048 sc1\n"
        "v_mfma_f32_16x16x32_bf16 v[36:39], a[52:55], a[192:195], v[36:39]\n"
        "ds_read_b64_tr_b16 a[238:239], v155 offset:61440\n"
        "v_mfma_f32_16x16x32_bf16 v[44:47], a[52:55], a[200:203], v[44:47]\n"
        "ds_read_b128 a[0:3], v149 offset:0\n"
        "v_cmp_ne_u64 vcc, v[236:237], v[250:251]\n"
        "v_mfma_f32_16x16x32_bf16 v[44:47], a[60:63], a[204:207], v[44:47]\n"
        "ds_read_b128 a[4:7], v149 offset:8192\n"
        "global_load_lds_dwordx4 v254, s[20:21] offset:3072 sc1\n"
        "s_add_u32 m0, s73, 0x24000\n"
        "v_mfma_f32_16x16x32_bf16 v[40:43], a[56:59], a[204:207], v[40:43]\n"
        "ds_read_b128 a[8:11], v149 offset:1024\n"
        "s_add_u32 s20, s20, 0x4000\n"
        "s_addc_u32 s21, s21, 0\n"
        "v_mfma_f32_16x16x32_bf16 v[40:43], a[48:51], a[200:203], v[40:43]\n"
        "ds_read_b128 a[12:15], v149 offset:9216\n"
        "s_cbranch_vccnz LPZZ_%=\n"
        "LBZZ_%=:\n"
        "v_lshrrev_b32 v234, 1, v254\n"
        "v_mfma_f32_16x16x32_bf16 v[48:51], a[48:51], a[208:211], v[48:51]\n"
        "ds_read_b128 a[16:19], v149 offset:2048\n"
        "v_add_u32 v234, s14, v234\n"
        "v_mfma_f32_16x16x32_bf16 v[48:51], a[56:59], a[212:215], v[48:51]\n"
        "ds_read_b128 a[20:23], v149 offset:10240\n"
        "s_xor_b32 s14, s14, 0x200\n"
        "v_mov_b32 v235, s14\n"
        "v_mfma_f32_16x16x32_bf16 v[52:55], a[60:63], a[212:215], v[52:55]\n"
        "ds_read_b128 a[24:27], v149 offset:3072\n"
        "v_mfma_f32_16x16x32_bf16 v[52:55], a[52:55], a[208:211], v[52:55]\n"
        "ds_read_b128 a[28:31], v149 offset:11264\n"
        "s_waitcnt lgkmcnt(8)\n"
        "v_mfma_f32_16x16x32_bf16 v[60:63], a[52:55], a[216:219], v[60:63]\n"
        "ds_read_b128 a[192:195], v152 offset:0\n"
        "s_waitcnt lgkmcnt(8)\n"
        "v_mfma_f32_16x16x32_bf16 v[60:63], a[60:63], a[220:223], v[60:63]\n"
        "LHB5_%=:\n"
        "s_cbranch_execz LHRB_%=\n"
        "ds_read_b128 a[196:199], v152 offset:8192\n"
        "s_waitcnt lgkmcnt(8)\n"
        "v_mfma_f32_16x16x32_bf16 v[56:59], a[56:59], a[220:223], v[56:59]\n"
        "ds_read_b128 a[200:203], v152 offset:1024\n"
        "s_waitcnt lgkmcnt(8)\n"
        "v_mfma_f32_16x16x32_bf16 v[56:59], a[48:51], a[216:219], v[56:59]\n"
        "ds_read_b128 a[204:207], v152 offset:9216\n"
        "s_waitcnt lgkmcnt(8)\n"
        "v_mfma_f32_16x16x32_bf16 v[0:3], a[224:227], a[0:3], v[0:3]\n"
        "ds_read_b128 a[208:211], v152 offset:2048\n"
        "s_waitcnt lgkmcnt(8)\n"
        "v_mfma_f32_16x16x32_bf16 v[0:3], a[232:235], a[4:7], v[0:3]\n"
        "ds_read_b128 a[212:215], v152 offset:10240\n"
        "s_waitcnt lgkmcnt(8)\n"
        "v_mfma_f32_16x16x32_bf16 v[4:7], a[236:239], a[4:7], v[4:7]\n"
        "ds_read_b128 a[216:219], v152 offset:3072\n"
        "s_waitcnt lgkmcnt(8)\n"
        "v_mfma_f32_16x16x32_bf16 v[4:7], a[228:231], a[0:3], v[4:7]\n"
        "ds_read_b128 a[220:223], v152 offset:11264\n"
        "s_waitcnt lgkmcnt(8)\n"
        "v_mfma_f32_16x16x32_bf16 v[12:15], a[228:231], a[8:11], v[12:15]\n"
        "v_mfma_f32_16x16x32_bf16 v[12:15], a[236:239], a[12:15], v[12:15]\n"
        "s_waitcnt lgkmcnt(6)\n"
        "v_mfma_f32_16x16x32_bf16 v[8:11], a[232:235], a[12:15], v[8:11]\n"
        "v_mfma_f32_16x16x32_bf16 v[8:11], a[224:227], a[8:11], v[8:11]\n"
        "v_mfma_f32_16x16x32_bf16 v[16:19], a[224:227], a[16:19], v[16:19]\n"
        "v_mfma_f32_16x16x32_bf16 v[16:19], a[232:235], a[20:23], v[16:19]\n"
        "s_waitcnt lgkmcnt(2)\n"
        "v_mfma_f32_16x16x32_bf16 v[20:23], a[236:239], a[20:23], v[20:23]\n"
        "v_mfma_f32_16x16x32_bf16 v[20:23], a[228:231], a[16:19], v[20:23]\n"
        "s_waitcnt vmcnt(20)\n"
        "s_barrier\n"
        "v_mfma_f32_16x16x32_bf16 v[28:31], a[228:231], a[24:27], v[28:31]\n"
        "global_load_lds_dwordx4 v144, s[60:61] offset:0\n"
        "ds_read_b128 a[0:3], v149 offset:16384\n"
        "v_mfma_f32_16x16x32_bf16 v[28:31], a[236:239], a[28:31], v[28:31]\n"
        "ds_read_b128 a[4:7], v149 offset:24576\n"
        "v_mfma_f32_16x16x32_bf16 v[24:27], a[232:235], a[28:31], v[24:27]\n"
        "ds_read_b128 a[8:11], v149 offset:17408\n"
        "v_mfma_f32_16x16x32_bf16 v[24:27], a[224:227], a[24:27], v[24:27]\n"
        "global_load_lds_dwordx4 v145, s[60:61] offset:1024\n"
        "ds_read_b128 a[12:15], v149 offset:25600\n"
        "v_mfma_f32_16x16x32_bf16 v[32:35], a[224:227], a[192:195], v[32:35]\n"
        "ds_read_b128 a[16:19], v149 offset:18432\n"
        "v_mfma_f32_16x16x32_bf16 v[32:35], a[232:235], a[196:199], v[32:35]\n"
        "ds_read_b128 a[20:23], v149 offset:26624\n"
        "s_waitcnt lgkmcnt(6)\n"
        "v_mfma_f32_16x16x32_bf16 v[36:39], a[236:239], a[196:199], v[36:39]\n"
        "global_load_lds_dwordx4 v146, s[60:61] offset:2048\n"
        "ds_read_b128 a[24:27], v149 offset:19456\n"
        "v_mfma_f32_16x16x32_bf16 v[36:39], a[228:231], a[192:195], v[36:39]\n"
        "ds_read_b128 a[28:31], v149 offset:27648\n"
        "v_mfma_f32_16x16x32_bf16 v[44:47], a[228:231], a[200:203], v[44:47]\n"
        "ds_read_b128 a[192:195], v152 offset:16384\n"
        "v_mfma_f32_16x16x32_bf16 v[44:47], a[236:239], a[204:207], v[44:47]\n"
        "global_load_lds_dwordx4 v147, s[60:61] offset:3072\n"
        "s_add_u32 m0, s73, 0x8000\n"
        "s_waitcnt lgkmcnt(7)\n"
        "v_mfma_f32_16x16x32_bf16 v[40:43], a[232:235], a[204:207], v[40:43]\n"
        "ds_read_b128 a[196:199], v152 offset:24576\n"
        "s_add_u32 s60, s22, 0x3c0000\n"
        "s_addc_u32 s61, s23, 0\n"
        "v_mfma_f32_16x16x32_bf16 v[40:43], a[224:227], a[200:203], v[40:43]\n"
        "ds_read_b128 a[200:203], v152 offset:17408\n"
        "v_mfma_f32_16x16x32_bf16 v[48:51], a[224:227], a[208:211], v[48:51]\n"
        "global_load_lds_dwordx4 v254, s[20:21] offset:0 sc1\n"
        "ds_read_b128 a[204:207], v152 offset:25600\n"
        "v_mfma_f32_16x16x32_bf16 v[48:51], a[232:235], a[212:215], v[48:51]\n"
        "s_waitcnt lgkmcnt(6)\n"
        "v_mfma_f32_16x16x32_bf16 v[52:55], a[236:239], a[212:215], v[52:55]\n"
        "ds_read_b128 a[212:215], v152 offset:26624\n"
        "v_mfma_f32_16x16x32_bf16 v[52:55], a[228:231], a[208:211], v[52:55]\n"
        "global_load_lds_dwordx4 v254, s[20:21] offset:1024 sc1\n"
        "ds_read_b128 a[208:211], v152 offset:18432\n"
        "v_mfma_f32_16x16x32_bf16 v[60:63], a[228:231], a[216:219], v[60:63]\n"
        "v_mfma_f32_16x16x32_bf16 v[60:63], a[236:239], a[220:223], v[60:63]\n"
        "v_mfma_f32_16x16x32_bf16 v[56:59], a[232:235], a[220:223], v[56:59]\n"
        "global_load_lds_dwordx4 v254, s[20:21] offset:2048 sc1\n"
        "ds_read_b128 a[220:223], v152 offset:27648\n"
        "v_mfma_f32_16x16x32_bf16 v[56:59], a[224:227], a[216:219], v[56:59]\n"
        "ds_read_b128 a[216:219], v152 offset:19456\n"
        "v_mfma_f32_16x16x32_bf16 v[0:3], a[144:147], a[0:3], v[0:3]\n"
        "v_mfma_f32_16x16x32_bf16 v[0:3], a[152:155], a[4:7], v[0:3]\n"
        "global_load_lds_dwordx4 v254, s[20:21] offset:3072 sc1\n"
        "s_add_u32 s20, s20, 0x4000\n"
        "s_addc_u32 s21, s21, 0\n"
        "v_mfma_f32_16x16x32_bf16 v[4:7], a[156:159], a[4:7], v[4:7]\n"
        "s_add_u32 m0, s73, 0xc000\n"
        "s_waitcnt lgkmcnt(3)\n"
        "v_mfma_f32_16x16x32_bf16 v[4:7], a[148:151], a[0:3], v[4:7]\n"
        "v_mfma_f32_16x16x32_bf16 v[12:15], a[148:151], a[8:11], v[12:15]\n"
        "v_mfma_f32_16x16x32_bf16 v[12:15], a[156:159], a[12:15], v[12:15]\n"
        "v_mfma_f32_16x16x32_bf16 v[8:11], a[152:155], a[12:15], v[8:11]\n"
        "v_mfma_f32_16x16x32_bf16 v[8:11], a[144:147], a[8:11], v[8:11]\n"
        "v_mfma_f32_16x16x32_bf16 v[16:19], a[144:147], a[16:19], v[16:19]\n"
        "v_mfma_f32_16x16x32_bf16 v[16:19], a[152:155], a[20:23], v[16:19]\n"
        "v_mfma_f32_16x16x32_bf16 v[20:23], a[156:159], a[20:23], v[20:23]\n"
        "v_mfma_f32_16x16x32_bf16 v[20:23], a[148:151], a[16:19], v[20:23]\n"
        "s_waitcnt vmcnt(20)\n"
        "s_barrier\n"
        "v_mfma_f32_16x16x32_bf16 v[28:31], a[148:151], a[24:27], v[28:31]\n"
        "global_load_lds_dwordx4 v254, s[20:21] offset:0 sc1\n"
        "ds_read_b64_tr_b16 a[240:241], v154 offset:0\n"
        "v_mfma_f32_16x16x32_bf16 v[28:31], a[156:159], a[28:31], v[28:31]\n"
        "ds_read_b64_tr_b16 a[242:243], v154 offset:4096\n"
        "s_waitcnt lgkmcnt(2)\n"
        "v_mfma_f32_16x16x32_bf16 v[24:27], a[152:155], a[28:31], v[24:27]\n"
        "ds_read_b64_tr_b16 a[248:249], v154 offset:8192\n"
        "v_mfma_f32_16x16x32_bf16 v[24:27], a[144:147], a[24:27], v[24:27]\n"
        "ds_read_b64_tr_b16 a[250:251], v154 offset:12288\n"
        "global_load_lds_dwordx4 v254, s[20:21] offset:1024 sc1\n"
        "v_mfma_f32_16x16x32_bf16 v[32:35], a[144:147], a[192:195], v[32:35]\n"
        "ds_read_b64_tr_b16 a[244:245], v155 offset:0\n"
        "v_mfma_f32_16x16x32_bf16 v[32:35], a[152:155], a[196:199], v[32:35]\n"
        "ds_read_b64_tr_b16 a[246:247], v155 offset:4096\n"
        "v_mfma_f32_16x16x32_bf16 v[36:39], a[156:159], a[196:199], v[36:39]\n"
        "ds_read_b64_tr_b16 a[252:253], v155 offset:8192\n"
        "global_load_lds_dwordx4 v254, s[20:21] offset:2048 sc1\n"
        "v_mfma_f32_16x16x32_bf16 v[36:39], a[148:151], a[192:195], v[36:39]\n"
        "ds_read_b64_tr_b16 a[254:255], v155 offset:12288\n"
        "v_mfma_f32_16x16x32_bf16 v[44:47], a[148:151], a[200:203], v[44:47]\n"
        "ds_read_b128 a[0:3], v148 offset:16384\n"
        "v_mfma_f32_16x16x32_bf16 v[44:47], a[156:159], a[204:207], v[44:47]\n"
        "ds_read_b128 a[4:7], v148 offset:24576\n"
        "global_load_lds_dwordx4 v254, s[20:21] offset:3072 sc1\n"
        "s_add_u32 m0, s73, 0x10000\n"
        "v_mfma_f32_16x16x32_bf16 v[40:43], a[152:155], a[204:207], v[40:43]\n"
        "ds_read_b128 a[8:11], v148 offset:17408\n"
        "s_add_u32 s20, s20, 0x4000\n"
        "s_addc_u32 s21, s21, 0\n"
        "v_mfma_f32_16x16x32_bf16 v[40:43], a[144:147], a[200:203], v[40:43]\n"
        "ds_read_b128 a[12:15], v148 offset:25600\n"
        "v_mfma_f32_16x16x32_bf16 v[48:51], a[144:147], a[208:211], v[48:51]\n"
        "ds_read_b128 a[16:19], v148 offset:18432\n"
        "global_load_lds_dwordx4 v144, s[60:61] offset:0\n"
        "v_mfma_f32_16x16x32_bf16 v[48:51], a[152:155], a[212:215], v[48:51]\n"
        "ds_read_b128 a[20:23], v148 offset:26624\n"
        "v_mfma_f32_16x16x32_bf16 v[52:55], a[156:159], a[212:215], v[52:55]\n"
        "ds_read_b128 a[24:27], v148 offset:19456\n"
        "v_mfma_f32_16x16x32_bf16 v[52:55], a[148:151], a[208:211], v[52:55]\n"
        "ds_read_b128 a[28:31], v148 offset:27648\n"
        "global_load_lds_dwordx4 v145, s[60:61] offset:1024\n"
        "v_mfma_f32_16x16x32_bf16 v[60:63], a[148:151], a[216:219], v[60:63]\n"
        "ds_read_b128 a[192:195], v151 offset:16384\n"
        "s_waitcnt lgkmcnt(8)\n"
        "v_mfma_f32_16x16x32_bf16 v[60:63], a[156:159], a[220:223], v[60:63]\n"
        "ds_read_b128 a[196:199], v151 offset:24576\n"
        "s_waitcnt lgkmcnt(8)\n"
        "v_mfma_f32_16x16x32_bf16 v[56:59], a[152:155], a[220:223], v[56:59]\n"
        "global_load_lds_dwordx4 v146, s[60:61] offset:2048\n"
        "v_mfma_f32_16x16x32_bf16 v[56:59], a[144:147], a[216:219], v[56:59]\n"
        "ds_read_b128 a[200:203], v151 offset:17408\n"
        "s_waitcnt lgkmcnt(7)\n"
        "v_mfma_f32_16x16x32_bf16 v[0:3], a[240:243], a[0:3], v[0:3]\n"
        "ds_read_b128 a[204:207], v151 offset:25600\n"
        "s_waitcnt lgkmcnt(7)\n"
        "v_mfma_f32_16x16x32_bf16 v[0:3], a[248:251], a[4:7], v[0:3]\n"
        "global_load_lds_dwordx4 v147, s[60:61] offset:3072\n"
        "s_add_u32 m0, s73, 0x14000\n"
        "v_mfma_f32_16x16x32_bf16 v[4:7], a[252:255], a[4:7], v[4:7]\n"
        "ds_read_b128 a[208:211], v151 offset:18432\n"
        "s_add_u32 s60, s22, 0x3e0000\n"
        "s_addc_u32 s61, s23, 0\n"
        "v_mfma_f32_16x16x32_bf16 v[4:7], a[244:247], a[0:3], v[4:7]\n"
        "ds_read_b128 a[212:215], v151 offset:26624\n"
        "s_waitcnt lgkmcnt(6)\n"
        "v_mfma_f32_16x16x32_bf16 v[12:15], a[244:247], a[8:11], v[12:15]\n"
        "ds_read_b128 a[216:219], v151 offset:19456\n"
        "v_mfma_f32_16x16x32_bf16 v[12:15], a[252:255], a[12:15], v[12:15]\n"
        "ds_read_b128 a[220:223], v151 offset:27648\n"
        "s_waitcnt lgkmcnt(6)\n"
        "v_mfma_f32_16x16x32_bf16 v[8:11], a[248:251], a[12:15], v[8:11]\n"
        "v_mfma_f32_16x16x32_bf16 v[8:11], a[240:243], a[8:11], v[8:11]\n"
        "v_mfma_f32_16x16x32_bf16 v[16:19], a[240:243], a[16:19], v[16:19]\n"
        "v_mfma_f32_16x16x32_bf16 v[16:19], a[248:251], a[20:23], v[16:19]\n"
        "s_waitcnt lgkmcnt(4)\n"
        "v_mfma_f32_16x16x32_bf16 v[20:23], a[252:255], a[20:23], v[20:23]\n"
        "v_mfma_f32_16x16x32_bf16 v[20:23], a[244:247], a[16:19], v[20:23]\n"
        "s_waitcnt vmcnt(24)\n"
        "s_barrier\n"
        "v_mfma_f32_16x16x32_bf16 v[28:31], a[244:247], a[24:27], v[28:31]\n"
        "global_load_lds_dwordx4 v254, s[20:21] offset:0 sc1\n"
        "ds_read_b128 a[0:3], v149 offset:32768\n"
        "v_mfma_f32_16x16x32_bf16 v[28:31], a[252:255], a[28:31], v[28:31]\n"
        "ds_read_b128 a[4:7], v149 offset:40960\n"
        "s_waitcnt lgkmcnt(2)\n"
        "v_mfma_f32_16x16x32_bf16 v[24:27], a[248:251], a[28:31], v[24:27]\n"
        "ds_read_b128 a[8:11], v149 offset:33792\n"
        "v_mfma_f32_16x16x32_bf16 v[24:27], a[240:243], a[24:27], v[24:27]\n"
        "global_load_lds_dwordx4 v254, s[20:21] offset:1024 sc1\n"
        "ds_read_b128 a[12:15], v149 offset:41984\n"
        "v_mfma_f32_16x16x32_bf16 v[32:35], a[240:243], a[192:195], v[32:35]\n"
        "ds_read_b128 a[16:19], v149 offset:34816\n"
        "v_mfma_f32_16x16x32_bf16 v[32:35], a[248:251], a[196:199], v[32:35]\n"
        "ds_read_b128 a[20:23], v149 offset:43008\n"
        "v_mfma_f32_16x16x32_bf16 v[36:39], a[252:255], a[196:199], v[36:39]\n"
        "global_load_lds_dwordx4 v254, s[20:21] offset:2048 sc1\n"
        "ds_read_b128 a[24:27], v149 offset:35840\n"
        "v_mfma_f32_16x16x32_bf16 v[36:39], a[244:247], a[192:195], v[36:39]\n"
        "ds_read_b128 a[28:31], v149 offset:44032\n"
        "v_mfma_f32_16x16x32_bf16 v[44:47], a[244:247], a[200:203], v[44:47]\n"
        "ds_read_b128 a[192:195], v152 offset:32768\n"
        "v_mfma_f32_16x16x32_bf16 v[44:47], a[252:255], a[204:207], v[44:47]\n"
        "global_load_lds_dwordx4 v254, s[20:21] offset:3072 sc1\n"
        "s_add_u32 s20, s20, 0x4000\n"
        "s_addc_u32 s21, s21, 0\n"
        "v_mfma_f32_16x16x32_bf16 v[40:43], a[248:251], a[204:207], v[40:43]\n"
        "ds_read_b128 a[196:199], v152 offset:40960\n"
        "s_add_u32 m0, s73, 0x0\n"
        "s_waitcnt lgkmcnt(7)\n"
        "v_mfma_f32_16x16x32_bf16 v[40:43], a[240:243], a[200:203], v[40:43]\n"
        "ds_read_b128 a[200:203], v152 offset:33792\n"
        "v_mfma_f32_16x16x32_bf16 v[48:51], a[240:243], a[208:211], v[48:51]\n"
        "ds_read_b128 a[204:207], v152 offset:41984\n"
        "v_mfma_f32_16x16x32_bf16 v[48:51], a[248:251], a[212:215], v[48:51]\n"
        "v_mfma_f32_16x16x32_bf16 v[52:55], a[252:255], a[212:215], v[52:55]\n"
        "ds_read_b128 a[212:215], v152 offset:43008\n"
        "v_mfma_f32_16x16x32_bf16 v[52:55], a[244:247], a[208:211], v[52:55]\n"
        "ds_read_b128 a[208:211], v152 offset:34816\n"
        "s_waitcnt lgkmcnt(6)\n"
        "v_mfma_f32_16x16x32_bf16 v[60:63], a[244:247], a[216:219], v[60:63]\n"
        "v_mfma_f32_16x16x32_bf16 v[60:63], a[252:255], a[220:223], v[60:63]\n"
        "v_mfma_f32_16x16x32_bf16 v[56:59], a[248:251], a[220:223], v[56:59]\n"
        "ds_read_b128 a[220:223], v152 offset:44032\n"
        "v_mfma_f32_16x16x32_bf16 v[56:59], a[240:243], a[216:219], v[56:59]\n"
        "ds_read_b128 a[216:219], v152 offset:35840\n"
        "v_mfma_f32_16x16x32_bf16 v[0:3], a[160:163], a[0:3], v[0:3]\n"
        "v_mfma_f32_16x16x32_bf16 v[0:3], a[168:171], a[4:7], v[0:3]\n"
        "v_mfma_f32_16x16x32_bf16 v[4:7], a[172:175], a[4:7], v[4:7]\n"
        "v_mfma_f32_16x16x32_bf16 v[4:7], a[164:167], a[0:3], v[4:7]\n"
        "v_mfma_f32_16x16x32_bf16 v[12:15], a[164:167], a[8:11], v[12:15]\n"
        "v_mfma_f32_16x16x32_bf16 v[12:15], a[172:175], a[12:15], v[12:15]\n"
        "s_waitcnt lgkmcnt(2)\n"
        "v_mfma_f32_16x16x32_bf16 v[8:11], a[168:171], a[12:15], v[8:11]\n"
        "v_mfma_f32_16x16x32_bf16 v[8:11], a[160:163], a[8:11], v[8:11]\n"
        "v_mfma_f32_16x16x32_bf16 v[16:19], a[160:163], a[16:19], v[16:19]\n"
        "v_mfma_f32_16x16x32_bf16 v[20:23], a[172:175], a[20:23], v[20:23]\n"
        "v_mfma_f32_16x16x32_bf16 v[20:23], a[164:167], a[16:19], v[20:23]\n"
        "s_waitcnt vmcnt(20)\n"
        "s_barrier\n"
        "v_mfma_f32_16x16x32_bf16 v[16:19], a[168:171], a[20:23], v[16:19]\n"
        "global_load_lds_dwordx4 v144, s[60:61] offset:0\n"
        "ds_read_b64_tr_b16 a[32:33], v156 offset:49152\n"
        "v_mfma_f32_16x16x32_bf16 v[28:31], a[164:167], a[24:27], v[28:31]\n"
        "ds_read_b64_tr_b16 a[34:35], v156 offset:53248\n"
        "v_mfma_f32_16x16x32_bf16 v[28:31], a[172:175], a[28:31], v[28:31]\n"
        "ds_read_b64_tr_b16 a[40:41], v156 offset:57344\n"
        "v_mfma_f32_16x16x32_bf16 v[24:27], a[168:171], a[28:31], v[24:27]\n"
        "global_load_lds_dwordx4 v145, s[60:61] offset:1024\n"
        "ds_read_b64_tr_b16 a[42:43], v156 offset:61440\n"
        "v_mfma_f32_16x16x32_bf16 v[24:27], a[160:163], a[24:27], v[24:27]\n"
        "ds_read_b64_tr_b16 a[36:37], v157 offset:49152\n"
        "v_mfma_f32_16x16x32_bf16 v[32:35], a[160:163], a[192:195], v[32:35]\n"
        "ds_read_b64_tr_b16 a[38:39], v157 offset:53248\n"
        "v_mfma_f32_16x16x32_bf16 v[32:35], a[168:171], a[196:199], v[32:35]\n"
        "global_load_lds_dwordx4 v146, s[60:61] offset:2048\n"
        "ds_read_b64_tr_b16 a[44:45], v157 offset:57344\n"
        "v_mfma_f32_16x16x32_bf16 v[36:39], a[172:175], a[196:199], v[36:39]\n"
        "ds_read_b64_tr_b16 a[46:47], v157 offset:61440\n"
        "s_waitcnt lgkmcnt(8)\n"
        "v_mfma_f32_16x16x32_bf16 v[36:39], a[164:167], a[192:195], v[36:39]\n"
        "ds_read_b128 a[0:3], v150 offset:0\n"
        "v_mfma_f32_16x16x32_bf16 v[44:47], a[164:167], a[200:203], v[44:47]\n"
        "global_load_lds_dwordx4 v147, s[60:61] offset:3072\n"
        "s_add_u32 m0, s73, 0x4000\n"
        "v_mfma_f32_16x16x32_bf16 v[44:47], a[172:175], a[204:207], v[44:47]\n"
        "ds_read_b128 a[4:7], v150 offset:8192\n"
        "v_mfma_f32_16x16x32_bf16 v[40:43], a[168:171], a[204:207], v[40:43]\n"
        "ds_read_b128 a[8:11], v150 offset:1024\n"
        "v_mfma_f32_16x16x32_bf16 v[40:43], a[160:163], a[200:203], v[40:43]\n"
        "global_load_lds_dwordx4 v254, s[20:21] offset:0 sc1\n"
        "ds_read_b128 a[12:15], v150 offset:9216\n"
        "v_mfma_f32_16x16x32_bf16 v[48:51], a[160:163], a[208:211], v[48:51]\n"
        "ds_read_b128 a[16:19], v150 offset:2048\n"
        "v_mfma_f32_16x16x32_bf16 v[48:51], a[168:171], a[212:215], v[48:51]\n"
        "ds_read_b128 a[20:23], v150 offset:10240\n"
        "v_mfma_f32_16x16x32_bf16 v[52:55], a[172:175], a[212:215], v[52:55]\n"
        "global_load_lds_dwordx4 v254, s[20:21] offset:1024 sc1\n"
        "ds_read_b128 a[24:27], v150 offset:3072\n"
        "v_mfma_f32_16x16x32_bf16 v[52:55], a[164:167], a[208:211], v[52:55]\n"
        "ds_read_b128 a[28:31], v150 offset:11264\n"
        "s_waitcnt lgkmcnt(7)\n"
        "v_mfma_f32_16x16x32_bf16 v[60:63], a[164:167], a[216:219], v[60:63]\n"
        "ds_read_b128 a[192:195], v153 offset:0\n"
        "v_mfma_f32_16x16x32_bf16 v[60:63], a[172:175], a[220:223], v[60:63]\n"
        "global_load_lds_dwordx4 v254, s[20:21] offset:2048 sc1\n"
        "ds_read_b128 a[196:199], v153 offset:8192\n"
        "v_mfma_f32_16x16x32_bf16 v[56:59], a[168:171], a[220:223], v[56:59]\n"
        "ds_read_b128 a[200:203], v153 offset:1024\n"
        "s_waitcnt lgkmcnt(8)\n"
        "v_mfma_f32_16x16x32_bf16 v[56:59], a[160:163], a[216:219], v[56:59]\n"
        "ds_read_b128 a[204:207], v153 offset:9216\n"
        "s_waitcnt lgkmcnt(8)\n"
        "v_mfma_f32_16x16x32_bf16 v[0:3], a[32:35], a[0:3], v[0:3]\n"
        "global_load_lds_dwordx4 v254, s[20:21] offset:3072 sc1\n"
        "ds_read_b128 a[208:211], v153 offset:2048\n"
        "s_add_u32 m0, s73, 0x18000\n"
        "v_mfma_f32_16x16x32_bf16 v[0:3], a[40:43], a[4:7], v[0:3]\n"
        "ds_read_b128 a[212:215], v153 offset:10240\n"
        "s_waitcnt lgkmcnt(8)\n"
        "v_mfma_f32_16x16x32_bf16 v[4:7], a[44:47], a[4:7], v[4:7]\n"
        "ds_read_b128 a[216:219], v153 offset:3072\n"
        "s_waitcnt lgkmcnt(8)\n"
        "v_mfma_f32_16x16x32_bf16 v[4:7], a[36:39], a[0:3], v[4:7]\n"
        "ds_read_b128 a[220:223], v153 offset:11264\n"
        "s_waitcnt lgkmcnt(8)\n"
        "v_mfma_f32_16x16x32_bf16 v[12:15], a[36:39], a[8:11], v[12:15]\n"
        "v_mfma_f32_16x16x32_bf16 v[12:15], a[44:47], a[12:15], v[12:15]\n"
        "s_waitcnt lgkmcnt(7)\n"
        "v_mfma_f32_16x16x32_bf16 v[8:11], a[40:43], a[12:15], v[8:11]\n"
        "v_mfma_f32_16x16x32_bf16 v[8:11], a[32:35], a[8:11], v[8:11]\n"
        "s_waitcnt lgkmcnt(4)\n"
        "v_mfma_f32_16x16x32_bf16 v[16:19], a[32:35], a[16:19], v[16:19]\n"
        "v_mfma_f32_16x16x32_bf16 v[16:19], a[40:43], a[20:23], v[16:19]\n"
        "v_mfma_f32_16x16x32_bf16 v[20:23], a[44:47], a[20:23], v[20:23]\n"
        "v_mfma_f32_16x16x32_bf16 v[20:23], a[36:39], a[16:19], v[20:23]\n"
        "s_waitcnt vmcnt(20)\n"
        "s_barrier\n"
        "v_mfma_f32_16x16x32_bf16 v[28:31], a[36:39], a[24:27], v[28:31]\n"
        "global_load_lds_dwordx4 v224, s[58:59] offset:0 sc1\n"
        "ds_read_b64_tr_b16 a[48:49], v158 offset:16384\n"
        "s_add_u32 m0, s73, 0x18400\n"
        "v_mfma_f32_16x16x32_bf16 v[28:31], a[44:47], a[28:31], v[28:31]\n"
        "ds_read_b64_tr_b16 a[50:51], v158 offset:20480\n"
        "s_add_u32 s58, s58, 0x8000\n"
        "s_addc_u32 s59, s59, 0\n"
        "v_mfma_f32_16x16x32_bf16 v[24:27], a[40:43], a[28:31], v[24:27]\n"
        "ds_read_b64_tr_b16 a[56:57], v158 offset:24576\n"
        "s_waitcnt lgkmcnt(3)\n"
        "v_mfma_f32_16x16x32_bf16 v[24:27], a[32:35], a[24:27], v[24:27]\n"
        "ds_read_b64_tr_b16 a[58:59], v158 offset:28672\n"
        "global_load_lds_dwordx4 v224, s[58:59] offset:0 sc1\n"
        "s_add_u32 m0, s73, 0x18800\n"
        "v_mfma_f32_16x16x32_bf16 v[32:35], a[32:35], a[192:195], v[32:35]\n"
        "ds_read_b64_tr_b16 a[52:53], v159 offset:16384\n"
        "s_add_u32 s58, s58, 0x8000\n"
        "s_addc_u32 s59, s59, 0\n"
        "v_mfma_f32_16x16x32_bf16 v[32:35], a[40:43], a[196:199], v[32:35]\n"
        "ds_read_b64_tr_b16 a[54:55], v159 offset:20480\n"
        "v_mfma_f32_16x16x32_bf16 v[36:39], a[44:47], a[196:199], v[36:39]\n"
        "ds_read_b64_tr_b16 a[60:61], v159 offset:24576\n"
        "global_load_lds_dwordx4 v224, s[58:59] offset:0 sc1\n"
        "s_add_u32 m0, s73, 0x18c00\n"
        "v_mfma_f32_16x16x32_bf16 v[36:39], a[36:39], a[192:195], v[36:39]\n"
        "ds_read_b64_tr_b16 a[62:63], v159 offset:28672\n"
        "s_add_u32 s58, s58, 0x8000\n"
        "s_addc_u32 s59, s59, 0\n"
        "v_mfma_f32_16x16x32_bf16 v[44:47], a[36:39], a[200:203], v[44:47]\n"
        "ds_read_b128 a[0:3], v148 offset:32768\n"
        "v_mfma_f32_16x16x32_bf16 v[44:47], a[44:47], a[204:207], v[44:47]\n"
        "ds_read_b128 a[4:7], v148 offset:40960\n"
        "global_load_lds_dwordx4 v224, s[58:59] offset:0 sc1\n"
        "v_mfma_f32_16x16x32_bf16 v[40:43], a[40:43], a[204:207], v[40:43]\n"
        "ds_read_b128 a[8:11], v148 offset:33792\n"
        "v_mfma_f32_16x16x32_bf16 v[40:43], a[32:35], a[200:203], v[40:43]\n"
        "ds_read_b128 a[12:15], v148 offset:41984\n"
        "v_mfma_f32_16x16x32_bf16 v[48:51], a[32:35], a[208:211], v[48:51]\n"
        "ds_read_b128 a[16:19], v148 offset:34816\n"
        "global_load_dwordx4 v[112:115], v227, s[64:65]\n"
        "v_mfma_f32_16x16x32_bf16 v[48:51], a[40:43], a[212:215], v[48:51]\n"
        "ds_read_b128 a[20:23], v148 offset:43008\n"
        "s_add_u32 s64, s64, 0x8000\n"
        "s_addc_u32 s65, s65, 0\n"
        "v_mfma_f32_16x16x32_bf16 v[52:55], a[44:47], a[212:215], v[52:55]\n"
        "ds_read_b128 a[24:27], v148 offset:35840\n"
        "v_mfma_f32_16x16x32_bf16 v[52:55], a[36:39], a[208:211], v[52:55]\n"
        "ds_read_b128 a[28:31], v148 offset:44032\n"
        "global_load_dwordx4 v[116:119], v227, s[64:65]\n"
        "v_mfma_f32_16x16x32_bf16 v[60:63], a[36:39], a[216:219], v[60:63]\n"
        "ds_read_b128 a[192:195], v151 offset:32768\n"
        "s_add_u32 s64, s64, 0x8000\n"
        "s_addc_u32 s65, s65, 0\n"
        "v_mfma_f32_16x16x32_bf16 v[60:63], a[44:47], a[220:223], v[60:63]\n"
        "ds_read_b128 a[196:199], v151 offset:40960\n"
        "s_waitcnt lgkmcnt(8)\n"
        "v_mfma_f32_16x16x32_bf16 v[56:59], a[40:43], a[220:223], v[56:59]\n"
        "global_load_dwordx4 v[120:123], v227, s[64:65]\n"
        "v_mfma_f32_16x16x32_bf16 v[56:59], a[32:35], a[216:219], v[56:59]\n"
        "ds_read_b128 a[200:203], v151 offset:33792\n"
        "s_add_u32 s64, s64, 0x8000\n"
        "s_addc_u32 s65, s65, 0\n"
        "v_mfma_f32_16x16x32_bf16 v[0:3], a[48:51], a[0:3], v[0:3]\n"
        "ds_read_b128 a[204:207], v151 offset:41984\n"
        "s_waitcnt lgkmcnt(7)\n"
        "v_mfma_f32_16x16x32_bf16 v[16:19], a[48:51], a[16:19], v[16:19]\n"
        "ds_read_b128 a[212:215], v151 offset:43008\n"
        "s_waitcnt lgkmcnt(7)\n"
        "v_mfma_f32_16x16x32_bf16 v[20:23], a[60:63], a[20:23], v[20:23]\n"
        "ds_read_b128 a[208:211], v151 offset:34816\n"
        "global_load_dwordx4 v[124:127], v227, s[64:65]\n"
        "v_mfma_f32_16x16x32_bf16 v[20:23], a[52:55], a[16:19], v[20:23]\n"
        "s_waitcnt vmcnt(24)\n"
        "s_barrier\n"
        "s_waitcnt lgkmcnt(6)\n"
        "v_mfma_f32_16x16x32_bf16 v[16:19], a[56:59], a[20:23], v[16:19]\n"
        "ds_read_b128 a[220:223], v151 offset:44032\n"
        "v_mfma_f32_16x16x32_bf16 v[8:11], a[56:59], a[12:15], v[8:11]\n"
        "ds_read_b128 a[216:219], v151 offset:35840\n"
        "global_load_dword v128, v238, s[48:49] offset:64 sc1\n"
        "v_mfma_f32_16x16x32_bf16 v[24:27], a[56:59], a[28:31], v[24:27]\n"
        "ds_read_b128 a[20:23], v148 offset:59392\n"
        "v_mfma_f32_16x16x32_bf16 v[4:7], a[60:63], a[4:7], v[4:7]\n"
        "ds_read_b128 a[16:19], v148 offset:51200\n"
        "v_mfma_f32_16x16x32_bf16 v[12:15], a[52:55], a[8:11], v[12:15]\n"
        "v_mfma_f32_16x16x32_bf16 v[28:31], a[52:55], a[24:27], v[28:31]\n"
        "v_mfma_f32_16x16x32_bf16 v[0:3], a[56:59], a[4:7], v[0:3]\n"
        "ds_read_b128 a[4:7], v148 offset:57344\n"
        "s_waitcnt lgkmcnt(5)\n"
        "v_mfma_f32_16x16x32_bf16 v[8:11], a[48:51], a[8:11], v[8:11]\n"
        "ds_read_b128 a[8:11], v148 offset:50176\n"
        "v_mfma_f32_16x16x32_bf16 v[24:27], a[48:51], a[24:27], v[24:27]\n"
        "ds_read_b128 a[24:27], v148 offset:52224\n"
        "v_mfma_f32_16x16x32_bf16 v[4:7], a[52:55], a[0:3], v[4:7]\n"
        "ds_read_b128 a[0:3], v148 offset:49152\n"
        "s_waitcnt lgkmcnt(6)\n"
        "v_mfma_f32_16x16x32_bf16 v[12:15], a[60:63], a[12:15], v[12:15]\n"
        "ds_read_b128 a[12:15], v148 offset:58368\n"
        "s_waitcnt lgkmcnt(6)\n"
        "v_mfma_f32_16x16x32_bf16 v[28:31], a[60:63], a[28:31], v[28:31]\n"
        "ds_read_b128 a[28:31], v148 offset:60416\n"
        "v_mfma_f32_16x16x32_bf16 v[52:55], a[60:63], a[212:215], v[52:55]\n"
        "v_mfma_f32_16x16x32_bf16 v[52:55], a[52:55], a[208:211], v[52:55]\n"
        "s_waitcnt vmcnt(17)\n"
        "s_barrier\n"
        "v_mfma_f32_16x16x32_bf16 v[36:39], a[60:63], a[196:199], v[36:39]\n"
        "ds_read_b64_tr_b16 a[224:225], v156 offset:0\n"
        "v_mfma_f32_16x16x32_bf16 v[44:47], a[52:55], a[200:203], v[44:47]\n"
        "ds_read_b64_tr_b16 a[226:227], v156 offset:4096\n"
        "v_mfma_f32_16x16x32_bf16 v[60:63], a[52:55], a[216:219], v[60:63]\n"
        "ds_read_b64_tr_b16 a[232:233], v156 offset:8192\n"
        "v_mfma_f32_16x16x32_bf16 v[20:23], a[188:191], a[20:23], v[20:23]\n"
        "ds_read_b64_tr_b16 a[234:235], v156 offset:12288\n"
        "v_mfma_f32_16x16x32_bf16 v[32:35], a[48:51], a[192:195], v[32:35]\n"
        "ds_read_b64_tr_b16 a[228:229], v157 offset:0\n"
        "v_mfma_f32_16x16x32_bf16 v[40:43], a[56:59], a[204:207], v[40:43]\n"
        "ds_read_b64_tr_b16 a[230:231], v157 offset:4096\n"
        "s_waitcnt lgkmcnt(6)\n"
        "v_mfma_f32_16x16x32_bf16 v[48:51], a[48:51], a[208:211], v[48:51]\n"
        "ds_read_b128 a[208:211], v149 offset:18432\n"
        "v_mfma_f32_16x16x32_bf16 v[56:59], a[56:59], a[220:223], v[56:59]\n"
        "ds_read_b64_tr_b16 a[236:237], v157 offset:8192\n"
        "v_mfma_f32_16x16x32_bf16 v[36:39], a[52:55], a[192:195], v[36:39]\n"
        "ds_read_b128 a[192:195], v149 offset:16384\n"
        "v_mfma_f32_16x16x32_bf16 v[44:47], a[60:63], a[204:207], v[44:47]\n"
        "ds_read_b128 a[204:207], v149 offset:25600\n"
        "v_mfma_f32_16x16x32_bf16 v[60:63], a[60:63], a[220:223], v[60:63]\n"
        "ds_read_b128 a[220:223], v149 offset:27648\n"
        "v_mfma_f32_16x16x32_bf16 v[20:23], a[180:183], a[16:19], v[20:23]\n"
        "ds_read_b64_tr_b16 a[238:239], v157 offset:12288\n"
        "v_mfma_f32_16x16x32_bf16 v[4:7], a[188:191], a[4:7], v[4:7]\n"
        "s_waitcnt vmcnt(9)\n"
        "v_mfma_f32_16x16x32_bf16 v[12:15], a[180:183], a[8:11], v[12:15]\n"
        "s_barrier\n"
        "ds_read_b64_tr_b16 a[240:241], v154 offset:0\n"
        "v_mfma_f32_16x16x32_bf16 v[28:31], a[180:183], a[24:27], v[28:31]\n"
        "ds_read_b64_tr_b16 a[242:243], v154 offset:4096\n"
        "v_mfma_f32_16x16x32_bf16 v[32:35], a[56:59], a[196:199], v[32:35]\n"
        "ds_read_b128 a[196:199], v149 offset:24576\n"
        "v_mfma_f32_16x16x32_bf16 v[40:43], a[48:51], a[200:203], v[40:43]\n"
        "ds_read_b128 a[200:203], v149 offset:17408\n"
        "v_mfma_f32_16x16x32_bf16 v[48:51], a[56:59], a[212:215], v[48:51]\n"
        "ds_read_b128 a[212:215], v149 offset:26624\n"
        "v_mfma_f32_16x16x32_bf16 v[56:59], a[48:51], a[216:219], v[56:59]\n"
        "ds_read_b128 a[216:219], v149 offset:19456\n"
        "v_mfma_f32_16x16x32_bf16 v[16:19], a[176:179], a[16:19], v[16:19]\n"
        "ds_read_b64_tr_b16 a[248:249], v154 offset:8192\n"
        "v_mfma_f32_16x16x32_bf16 v[0:3], a[176:179], a[0:3], v[0:3]\n"
        "ds_read_b64_tr_b16 a[250:251], v154 offset:12288\n"
        "v_mfma_f32_16x16x32_bf16 v[8:11], a[184:187], a[12:15], v[8:11]\n"
        "ds_read_b128 a[16:19], v148 offset:18432\n"
        "v_mfma_f32_16x16x32_bf16 v[24:27], a[184:187], a[28:31], v[24:27]\n"
        "ds_read_b64_tr_b16 a[244:245], v155 offset:0\n"
        "v_mfma_f32_16x16x32_bf16 v[4:7], a[180:183], a[0:3], v[4:7]\n"
        "ds_read_b128 a[0:3], v148 offset:16384\n"
        "v_mfma_f32_16x16x32_bf16 v[12:15], a[188:191], a[12:15], v[12:15]\n"
        "ds_read_b128 a[12:15], v148 offset:25600\n"
        "s_waitcnt lgkmcnt(8)\n"
        "v_mfma_f32_16x16x32_bf16 v[28:31], a[188:191], a[28:31], v[28:31]\n"
        "ds_read_b128 a[28:31], v148 offset:27648\n"
        "s_waitcnt lgkmcnt(8)\n"
        "v_mfma_f32_16x16x32_bf16 v[16:19], a[184:187], a[20:23], v[16:19]\n"
        "ds_read_b128 a[20:23], v148 offset:26624\n"
        "v_mfma_f32_16x16x32_bf16 v[0:3], a[184:187], a[4:7], v[0:3]\n"
        "ds_read_b128 a[4:7], v148 offset:24576\n"
        "v_mfma_f32_16x16x32_bf16 v[8:11], a[176:179], a[8:11], v[8:11]\n"
        "ds_read_b128 a[8:11], v148 offset:17408\n"
        "v_mfma_f32_16x16x32_bf16 v[24:27], a[176:179], a[24:27], v[24:27]\n"
        "ds_read_b128 a[24:27], v148 offset:19456\n"
        "v_mfma_f32_16x16x32_bf16 v[16:19], a[224:227], a[208:211], v[16:19]\n"
        "ds_read_b64_tr_b16 a[246:247], v155 offset:4096\n"
        "v_mfma_f32_16x16x32_bf16 v[0:3], a[224:227], a[192:195], v[0:3]\n"
        "ds_read_b64_tr_b16 a[252:253], v155 offset:8192\n"
        "v_mfma_f32_16x16x32_bf16 v[8:11], a[232:235], a[204:207], v[8:11]\n"
        "ds_read_b64_tr_b16 a[254:255], v155 offset:12288\n"
        "v_mfma_f32_16x16x32_bf16 v[24:27], a[232:235], a[220:223], v[24:27]\n"
        "ds_read_b64_tr_b16 a[40:41], v156 offset:8192\n"
        "s_waitcnt lgkmcnt(8)\n"
        "v_mfma_f32_16x16x32_bf16 v[16:19], a[232:235], a[212:215], v[16:19]\n"
        "ds_read_b64_tr_b16 a[42:43], v156 offset:12288\n"
        "v_mfma_f32_16x16x32_bf16 v[0:3], a[232:235], a[196:199], v[0:3]\n"
        "ds_read_b64_tr_b16 a[32:33], v156 offset:0\n"
        "v_mfma_f32_16x16x32_bf16 v[8:11], a[224:227], a[200:203], v[8:11]\n"
        "ds_read_b64_tr_b16 a[34:35], v156 offset:4096\n"
        "v_mfma_f32_16x16x32_bf16 v[24:27], a[224:227], a[216:219], v[24:27]\n"
        "ds_read_b64_tr_b16 a[56:57], v154 offset:8192\n"
        "v_mfma_f32_16x16x32_bf16 v[20:23], a[236:239], a[212:215], v[20:23]\n"
        "ds_read_b64_tr_b16 a[58:59], v154 offset:12288\n"
        "v_mfma_f32_16x16x32_bf16 v[4:7], a[236:239], a[196:199], v[4:7]\n"
        "ds_read_b128 a[196:199], v151 offset:57344\n"
        "v_mfma_f32_16x16x32_bf16 v[12:15], a[228:231], a[200:203], v[12:15]\n"
        "ds_read_b128 a[200:203], v151 offset:50176\n"
        "v_mfma_f32_16x16x32_bf16 v[28:31], a[228:231], a[216:219], v[28:31]\n"
        "ds_read_b128 a[216:219], v151 offset:52224\n"
        "v_mfma_f32_16x16x32_bf16 v[16:19], a[240:243], a[16:19], v[16:19]\n"
        "ds_read_b64_tr_b16 a[48:49], v154 offset:0\n"
        "v_mfma_f32_16x16x32_bf16 v[0:3], a[240:243], a[0:3], v[0:3]\n"
        "ds_read_b64_tr_b16 a[50:51], v154 offset:4096\n"
        "v_mfma_f32_16x16x32_bf16 v[8:11], a[248:251], a[12:15], v[8:11]\n"
        "ds_read_b128 a[212:215], v151 offset:59392\n"
        "s_waitcnt lgkmcnt(8)\n"
        "v_mfma_f32_16x16x32_bf16 v[24:27], a[248:251], a[28:31], v[24:27]\n"
        "ds_read_b64_tr_b16 a[36:37], v157 offset:0\n"
        "v_mfma_f32_16x16x32_bf16 v[20:23], a[228:231], a[208:211], v[20:23]\n"
        "s_waitcnt vmcnt(5)\n"
        "v_mfma_f32_16x16x32_bf16 v[4:7], a[228:231], a[192:195], v[4:7]\n"
        "ds_read_b128 a[192:195], v151 offset:49152\n"
        "v_mfma_f32_16x16x32_bf16 v[12:15], a[236:239], a[204:207], v[12:15]\n"
        "ds_read_b128 a[204:207], v151 offset:58368\n"
        "v_mfma_f32_16x16x32_bf16 v[28:31], a[236:239], a[220:223], v[28:31]\n"
        "ds_read_b128 a[220:223], v151 offset:60416\n"
        "s_waitcnt vmcnt(4) lgkmcnt(7)\n"
        "v_mfma_f32_16x16x32_bf16 v[16:19], a[248:251], a[20:23], v[16:19]\n"
        "ds_read_b64_tr_b16 v[66:67], v225 offset:2048\n"
        "v_mfma_f32_16x16x32_bf16 v[0:3], a[248:251], a[4:7], v[0:3]\n"
        "ds_read_b64_tr_b16 v[70:71], v225 offset:2080\n"
        "v_mfma_f32_16x16x32_bf16 v[8:11], a[240:243], a[8:11], v[8:11]\n"
        "ds_read_b64_tr_b16 v[74:75], v225 offset:2112\n"
        "s_waitcnt vmcnt(3) lgkmcnt(7)\n"
        "v_mfma_f32_16x16x32_bf16 v[24:27], a[240:243], a[24:27], v[24:27]\n"
        "ds_read_b128 a[208:211], v151 offset:51200\n"
        "v_cvt_pk_bf16_f32 v16, v16, v17\n"
        "v_mfma_f32_16x16x32_bf16 v[20:23], a[252:255], a[20:23], v[20:23]\n"
        "ds_read_b64_tr_b16 a[38:39], v157 offset:4096\n"
        "v_cvt_pk_bf16_f32 v0, v0, v1\n"
        "v_mfma_f32_16x16x32_bf16 v[4:7], a[252:255], a[4:7], v[4:7]\n"
        "s_waitcnt vmcnt(2) lgkmcnt(7)\n"
        "v_cvt_pk_bf16_f32 v8, v8, v9\n"
        "v_cvt_pk_bf16_f32 v24, v24, v25\n"
        "v_mfma_f32_16x16x32_bf16 v[12:15], a[244:247], a[8:11], v[12:15]\n"
        "s_waitcnt lgkmcnt(6)\n"
        "v_cvt_pk_bf16_f32 v17, v18, v19\n"
        "v_cvt_pk_bf16_f32 v1, v2, v3\n"
        "LHB6_%=:\n"
        "s_cbranch_execz LHRB_%=\n"
        "v_mfma_f32_16x16x32_bf16 v[28:31], a[244:247], a[24:27], v[28:31]\n"
        "s_waitcnt lgkmcnt(5)\n"
        "v_cvt_pk_bf16_f32 v9, v10, v11\n"
        "v_cvt_pk_bf16_f32 v25, v26, v27\n"
        "v_mfma_f32_16x16x32_bf16 v[20:23], a[244:247], a[16:19], v[20:23]\n"
        "ds_read_b128 a[24:27], v152 offset:19456\n"
        "s_waitcnt lgkmcnt(5)\n"
        "v_mfma_f32_16x16x32_bf16 v[4:7], a[244:247], a[0:3], v[4:7]\n"
        "s_waitcnt vmcnt(1) lgkmcnt(4)\n"
        "v_permlane16_swap_b32 v112, v114\n"
        "v_mfma_f32_16x16x32_bf16 v[12:15], a[252:255], a[12:15], v[12:15]\n"
        "ds_read_b64_tr_b16 a[44:45], v157 offset:8192\n"
        "v_lshlrev_b32 v84, 16, v66\n"
        "v_mfma_f32_16x16x32_bf16 v[28:31], a[252:255], a[28:31], v[28:31]\n"
        "ds_read_b128 a[28:31], v152 offset:27648\n"
        "v_cvt_pk_bf16_f32 v18, v20, v21\n"
        "v_mfma_f32_16x16x32_bf16 v[60:63], a[180:183], a[216:219], v[60:63]\n"
        "s_waitcnt vmcnt(0) lgkmcnt(3)\n"
        "v_cvt_pk_bf16_f32 v19, v22, v23\n"
        "v_cvt_pk_bf16_f32 v2, v4, v5\n"
        "v_mfma_f32_16x16x32_bf16 v[56:59], a[184:187], a[220:223], v[56:59]\n"
        "ds_read_b64_tr_b16 a[46:47], v157 offset:12288\n"
        "v_cvt_pk_bf16_f32 v3, v6, v7\n"
        "v_mfma_f32_16x16x32_bf16 v[60:63], a[188:191], a[220:223], v[60:63]\n"
        "ds_read_b128 a[220:223], v151 offset:27648\n"
        "v_cvt_pk_bf16_f32 v10, v12, v13\n"
        "v_mfma_f32_16x16x32_bf16 v[56:59], a[176:179], a[216:219], v[56:59]\n"
        "ds_read_b128 a[216:219], v151 offset:19456\n"
        "v_cvt_pk_bf16_f32 v11, v14, v15\n"
        "v_cvt_pk_bf16_f32 v26, v28, v29\n"
        "v_cvt_pk_bf16_f32 v27, v30, v31\n"
        "global_store_dwordx4 v254, v[16:19], s[28:29] offset:2048 sc1\n"
        "s_waitcnt lgkmcnt(5)\n"
        "v_lshlrev_b32 v196, 16, v114\n"
        "v_permlane16_swap_b32 v116, v118\n"
        "v_mfma_f32_16x16x32_bf16 v[32:35], a[176:179], a[192:195], v[32:35]\n"
        "v_mul_f32 v196, v196, v84\n"
        "s_waitcnt lgkmcnt(3)\n"
        "v_mfma_f32_16x16x32_bf16 v[56:59], a[40:43], a[28:31], v[56:59]\n"
        "ds_read_b128 a[0:3], v152 offset:16384\n"
        "v_lshlrev_b32 v204, 16, v118\n"
        "v_mfma_f32_16x16x32_bf16 v[56:59], a[32:35], a[24:27], v[56:59]\n"
        "global_store_dwordx4 v254, v[0:3], s[28:29] offset:0 sc1\n"
        "ds_read_b128 a[4:7], v152 offset:24576\n"
        "v_lshlrev_b32 v84, 16, v70\n"
        "v_mul_f32 v204, v204, v84\n"
        "s_waitcnt lgkmcnt(3)\n"
        "v_mfma_f32_16x16x32_bf16 v[56:59], a[56:59], a[220:223], v[56:59]\n"
        "s_waitcnt lgkmcnt(2)\n"
        "v_permlane16_swap_b32 v120, v122\n"
        "v_mfma_f32_16x16x32_bf16 v[56:59], a[48:51], a[216:219], v[56:59]\n"
        "ds_read_b128 a[8:11], v152 offset:17408\n"
        "v_lshlrev_b32 v84, 16, v74\n"
        "global_store_dwordx4 v254, v[8:11], s[28:29] offset:1024 sc1\n"
        "ds_read_b128 a[12:15], v152 offset:25600\n"
        "ds_read_b64_tr_b16 v[78:79], v225 offset:2144\n"
        "v_lshlrev_b32 v212, 16, v122\n"
        "v_mfma_f32_16x16x32_bf16 v[32:35], a[184:187], a[196:199], v[32:35]\n"
        "ds_read_b128 a[16:19], v152 offset:18432\n"
        "v_mul_f32 v212, v212, v84\n"
        "v_mfma_f32_16x16x32_bf16 v[36:39], a[188:191], a[196:199], v[36:39]\n"
        "s_waitcnt lgkmcnt(5)\n"
        "v_cvt_pk_bf16_f32 v212, 0, v212\n"
        "v_and_b32 v87, 0xffff0000, v67\n"
        "global_store_dwordx4 v254, v[24:27], s[28:29] offset:3072 sc1\n"
        "ds_read_b128 a[20:23], v152 offset:26624\n"
        "v_permlane16_swap_b32 v113, v115\n"
        "v_and_b32 v199, 0xffff0000, v115\n"
        "v_mfma_f32_16x16x32_bf16 v[36:39], a[180:183], a[192:195], v[36:39]\n"
        "s_waitcnt lgkmcnt(5)\n"
        "v_permlane16_swap_b32 v117, v119\n"
        "v_mfma_f32_16x16x32_bf16 v[44:47], a[180:183], a[200:203], v[44:47]\n"
        "ds_read_b64_tr_b16 a[52:53], v155 offset:0\n"
        "v_mul_f32 v199, v199, v87\n"
        "v_mfma_f32_16x16x32_bf16 v[44:47], a[188:191], a[204:207], v[44:47]\n"
        "s_waitcnt lgkmcnt(5)\n"
        "v_and_b32 v207, 0xffff0000, v119\n"
        "v_and_b32 v87, 0xffff0000, v71\n"
        "v_mfma_f32_16x16x32_bf16 v[40:43], a[184:187], a[204:207], v[40:43]\n"
        "v_permlane16_swap_b32 v121, v123\n"
        "v_mfma_f32_16x16x32_bf16 v[40:43], a[176:179], a[200:203], v[40:43]\n"
        "s_waitcnt lgkmcnt(3)\n"
        "v_mov_b32 v129, v128\n"
        "v_mul_f32 v207, v207, v87\n"
        "v_mfma_f32_16x16x32_bf16 v[48:51], a[176:179], a[208:211], v[48:51]\n"
        "ds_read_b64_tr_b16 a[54:55], v155 offset:4096\n"
        "v_and_b32 v215, 0xffff0000, v123\n"
        "v_mfma_f32_16x16x32_bf16 v[48:51], a[184:187], a[212:215], v[48:51]\n"
        "s_waitcnt lgkmcnt(3)\n"
        "v_permlane16_swap_b32 v128, v129\n"
        "v_mfma_f32_16x16x32_bf16 v[52:55], a[188:191], a[212:215], v[52:55]\n"
        "ds_read_b64_tr_b16 a[60:61], v155 offset:8192\n"
        "v_and_b32 v87, 0xffff0000, v75\n"
        "v_mfma_f32_16x16x32_bf16 v[52:55], a[180:183], a[208:211], v[52:55]\n"
        "s_waitcnt lgkmcnt(3)\n"
        "v_permlane16_swap_b32 v125, v127\n"
        "v_mfma_f32_16x16x32_bf16 v[32:35], a[32:35], a[0:3], v[32:35]\n"
        "ds_read_b64_tr_b16 a[62:63], v155 offset:12288\n"
        "v_mov_b32 v131, v129\n"
        "v_mfma_f32_16x16x32_bf16 v[32:35], a[40:43], a[4:7], v[32:35]\n"
        "ds_read_b128 a[192:195], v151 offset:16384\n"
        "v_mul_f32 v215, v215, v87\n"
        "v_mfma_f32_16x16x32_bf16 v[36:39], a[44:47], a[4:7], v[36:39]\n"
        "ds_read_b128 a[196:199], v151 offset:24576\n"
        "v_and_b32 v223, 0xffff0000, v127\n"
        "v_mfma_f32_16x16x32_bf16 v[36:39], a[36:39], a[0:3], v[36:39]\n"
        "v_permlane32_swap_b32 v129, v131\n"
        "v_mfma_f32_16x16x32_bf16 v[44:47], a[36:39], a[8:11], v[44:47]\n"
        "ds_read_b128 a[200:203], v151 offset:17408\n"
        "v_and_b32 v87, 0xffff0000, v79\n"
        "v_mfma_f32_16x16x32_bf16 v[44:47], a[44:47], a[12:15], v[44:47]\n"
        "ds_read_b128 a[204:207], v151 offset:25600\n"
        "v_sub_f32 v135, 1.0, v131\n"
        "v_mfma_f32_16x16x32_bf16 v[40:43], a[40:43], a[12:15], v[40:43]\n"
        "ds_read_b128 a[208:211], v151 offset:18432\n"
        "v_mul_f32 v223, v223, v87\n"
        "v_mfma_f32_16x16x32_bf16 v[40:43], a[32:35], a[8:11], v[40:43]\n"
        "ds_read_b128 a[212:215], v151 offset:26624\n"
        "v_cvt_pk_bf16_f32 v135, 0, v135\n"
        "v_mfma_f32_16x16x32_bf16 v[48:51], a[32:35], a[16:19], v[48:51]\n"
        "s_waitcnt lgkmcnt(6)\n"
        "v_cvt_pk_bf16_f32 v223, 0, v223\n"
        "v_mul_f32 v223, v223, v135\n"
        "v_mfma_f32_16x16x32_bf16 v[48:51], a[40:43], a[20:23], v[48:51]\n"
        "s_waitcnt lgkmcnt(5)\n"
        "v_cvt_pk_bf16_f32 v223, 0, v223\n"
        "v_mov_b32 v130, v128\n"
        "v_mfma_f32_16x16x32_bf16 v[52:55], a[44:47], a[20:23], v[52:55]\n"
        "ds_read_b64_tr_b16 v[64:65], v225 offset:0\n"
        "v_sub_f32 v133, 1.0, v129\n"
        "v_mfma_f32_16x16x32_bf16 v[52:55], a[36:39], a[16:19], v[52:55]\n"
        "ds_read_b64_tr_b16 v[68:69], v225 offset:32\n"
        "v_cvt_pk_bf16_f32 v133, 0, v133\n"
        "v_mfma_f32_16x16x32_bf16 v[60:63], a[36:39], a[24:27], v[60:63]\n"
        "ds_read_b64_tr_b16 v[72:73], v225 offset:64\n"
        "v_lshlrev_b32 v192, 16, v112\n"
        "v_mfma_f32_16x16x32_bf16 v[60:63], a[44:47], a[28:31], v[60:63]\n"
        "ds_read_b64_tr_b16 v[76:77], v225 offset:96\n"
        "v_and_b32 v193, 0xffff0000, v112\n"
        "v_mfma_f32_16x16x32_bf16 v[32:35], a[48:51], a[192:195], v[32:35]\n"
        "s_waitcnt lgkmcnt(5)\n"
        "v_permlane32_swap_b32 v128, v130\n"
        "v_mfma_f32_16x16x32_bf16 v[32:35], a[56:59], a[196:199], v[32:35]\n"
        "s_waitcnt lgkmcnt(4)\n"
        "v_permlane16_swap_b32 v124, v126\n"
        "v_mfma_f32_16x16x32_bf16 v[36:39], a[60:63], a[196:199], v[36:39]\n"
        "v_sub_f32 v132, 1.0, v128\n"
        "v_cvt_pk_bf16_f32 v132, 0, v132\n"
        "v_mfma_f32_16x16x32_bf16 v[36:39], a[52:55], a[192:195], v[36:39]\n"
        "v_sub_f32 v134, 1.0, v130\n"
        "v_cvt_pk_bf16_f32 v134, 0, v134\n"
        "v_mfma_f32_16x16x32_bf16 v[44:47], a[52:55], a[200:203], v[44:47]\n"
        "s_waitcnt lgkmcnt(3)\n"
        "v_lshlrev_b32 v194, 16, v113\n"
        "v_and_b32 v195, 0xffff0000, v113\n"
        "v_mfma_f32_16x16x32_bf16 v[44:47], a[60:63], a[204:207], v[44:47]\n"
        "v_lshlrev_b32 v80, 16, v64\n"
        "v_and_b32 v81, 0xffff0000, v64\n"
        "v_mfma_f32_16x16x32_bf16 v[40:43], a[56:59], a[204:207], v[40:43]\n"
        "v_lshlrev_b32 v82, 16, v65\n"
        "v_and_b32 v83, 0xffff0000, v65\n"
        "v_mfma_f32_16x16x32_bf16 v[40:43], a[48:51], a[200:203], v[40:43]\n"
        "v_mul_f32 v192, v192, v80\n"
        "v_mul_f32 v193, v193, v81\n"
        "v_mfma_f32_16x16x32_bf16 v[48:51], a[48:51], a[208:211], v[48:51]\n"
        "v_mul_f32 v194, v194, v82\n"
        "v_mul_f32 v195, v195, v83\n"
        "v_mfma_f32_16x16x32_bf16 v[48:51], a[56:59], a[212:215], v[48:51]\n"
        "v_cvt_pk_bf16_f32 v192, 0, v192\n"
        "v_cvt_pk_bf16_f32 v193, 0, v193\n"
        "v_mfma_f32_16x16x32_bf16 v[52:55], a[60:63], a[212:215], v[52:55]\n"
        "v_cvt_pk_bf16_f32 v194, 0, v194\n"
        "v_cvt_pk_bf16_f32 v195, 0, v195\n"
        "v_mfma_f32_16x16x32_bf16 v[52:55], a[52:55], a[208:211], v[52:55]\n"
        "v_mul_f32 v192, v192, v132\n"
        "v_mul_f32 v193, v193, v132\n"
        "v_mfma_f32_16x16x32_bf16 v[60:63], a[52:55], a[216:219], v[60:63]\n"
        "v_mul_f32 v194, v194, v132\n"
        "v_mul_f32 v195, v195, v132\n"
        "v_mfma_f32_16x16x32_bf16 v[60:63], a[60:63], a[220:223], v[60:63]\n"
        "v_cvt_pk_bf16_f32 v192, 0, v192\n"
        "v_cvt_pk_bf16_f32 v193, 0, v193\n"
        "v_cvt_pk_bf16_f32 v194, 0, v194\n"
        "v_cvt_pk_bf16_f32 v195, 0, v195\n"
        "v_and_b32 v197, 0xffff0000, v114\n"
        "v_lshlrev_b32 v198, 16, v115\n"
        "v_and_b32 v85, 0xffff0000, v66\n"
        "v_lshlrev_b32 v86, 16, v67\n"
        "v_mul_f32 v197, v197, v85\n"
        "v_mul_f32 v198, v198, v86\n"
        "v_cvt_pk_bf16_f32 v196, 0, v196\n"
        "v_cvt_pk_bf16_f32 v197, 0, v197\n"
        "v_cvt_pk_bf16_f32 v198, 0, v198\n"
        "v_cvt_pk_bf16_f32 v199, 0, v199\n"
        "v_mul_f32 v196, v196, v132\n"
        "v_mul_f32 v197, v197, v132\n"
        "v_mul_f32 v198, v198, v132\n"
        "v_mul_f32 v199, v199, v132\n"
        "v_cvt_pk_bf16_f32 v196, 0, v196\n"
        "v_cvt_pk_bf16_f32 v197, 0, v197\n"
        "v_cvt_pk_bf16_f32 v198, 0, v198\n"
        "v_cvt_pk_bf16_f32 v199, 0, v199\n"
        "v_lshlrev_b32 v200, 16, v116\n"
        "v_and_b32 v201, 0xffff0000, v116\n"
        "v_lshlrev_b32 v202, 16, v117\n"
        "v_and_b32 v203, 0xffff0000, v117\n"
        "s_waitcnt lgkmcnt(0)\n"
        "v_lshlrev_b32 v80, 16, v68\n"
        "v_and_b32 v81, 0xffff0000, v68\n"
        "v_lshlrev_b32 v82, 16, v69\n"
        "v_and_b32 v83, 0xffff0000, v69\n"
        "v_mul_f32 v200, v200, v80\n"
        "v_mul_f32 v201, v201, v81\n"
        "v_mul_f32 v202, v202, v82\n"
        "v_mul_f32 v203, v203, v83\n"
        "v_cvt_pk_bf16_f32 v200, 0, v200\n"
        "v_cvt_pk_bf16_f32 v201, 0, v201\n"
        "v_cvt_pk_bf16_f32 v202, 0, v202\n"
        "v_cvt_pk_bf16_f32 v203, 0, v203\n"
        "v_mul_f32 v200, v200, v133\n"
        "v_mul_f32 v201, v201, v133\n"
        "v_mul_f32 v202, v202, v133\n"
        "v_mul_f32 v203, v203, v133\n"
        "v_cvt_pk_bf16_f32 v200, 0, v200\n"
        "v_cvt_pk_bf16_f32 v201, 0, v201\n"
        "v_cvt_pk_bf16_f32 v202, 0, v202\n"
        "v_cvt_pk_bf16_f32 v203, 0, v203\n"
        "v_and_b32 v205, 0xffff0000, v118\n"
        "v_lshlrev_b32 v206, 16, v119\n"
        "v_and_b32 v85, 0xffff0000, v70\n"
        "v_lshlrev_b32 v86, 16, v71\n"
        "v_mul_f32 v205, v205, v85\n"
        "v_mul_f32 v206, v206, v86\n"
        "v_cvt_pk_bf16_f32 v204, 0, v204\n"
        "v_cvt_pk_bf16_f32 v205, 0, v205\n"
        "v_cvt_pk_bf16_f32 v206, 0, v206\n"
        "v_cvt_pk_bf16_f32 v207, 0, v207\n"
        "v_mul_f32 v204, v204, v133\n"
        "v_mul_f32 v205, v205, v133\n"
        "v_mul_f32 v206, v206, v133\n"
        "v_mul_f32 v207, v207, v133\n"
        "v_cvt_pk_bf16_f32 v204, 0, v204\n"
        "v_cvt_pk_bf16_f32 v205, 0, v205\n"
        "v_cvt_pk_bf16_f32 v206, 0, v206\n"
        "v_cvt_pk_bf16_f32 v207, 0, v207\n"
        "v_lshlrev_b32 v208, 16, v120\n"
        "v_and_b32 v209, 0xffff0000, v120\n"
        "v_lshlrev_b32 v210, 16, v121\n"
        "v_and_b32 v211, 0xffff0000, v121\n"
        "v_lshlrev_b32 v80, 16, v72\n"
        "v_and_b32 v81, 0xffff0000, v72\n"
        "v_lshlrev_b32 v82, 16, v73\n"
        "v_and_b32 v83, 0xffff0000, v73\n"
        "s_waitcnt vmcnt(0)\n"
        "s_mov_b64 exec, 0xffff\n"
        "global_store_dwordx2 v234, v[250:251], s[48:49] sc1\n"
        "s_mov_b64 exec, -1\n"
        "s_or_b32 s97, s55, s40\n"
        "s_cbranch_scc0 LEP_%=\n"
        "LEB_%=:\n"
        "global_load_dwordx2 v[236:237], v235, s[48:49] sc1\n"
        "v_mul_f32 v208, v208, v80\n"
        "v_mul_f32 v209, v209, v81\n"
        "v_mul_f32 v210, v210, v82\n"
        "v_mul_f32 v211, v211, v83\n"
        "v_cvt_pk_bf16_f32 v208, 0, v208\n"
        "v_cvt_pk_bf16_f32 v209, 0, v209\n"
        "v_cvt_pk_bf16_f32 v210, 0, v210\n"
        "v_cvt_pk_bf16_f32 v211, 0, v211\n"
        "v_mul_f32 v208, v208, v134\n"
        "v_mul_f32 v209, v209, v134\n"
        "v_mul_f32 v210, v210, v134\n"
        "v_mul_f32 v211, v211, v134\n"
        "v_cvt_pk_bf16_f32 v208, 0, v208\n"
        "v_cvt_pk_bf16_f32 v209, 0, v209\n"
        "v_cvt_pk_bf16_f32 v210, 0, v210\n"
        "v_cvt_pk_bf16_f32 v211, 0, v211\n"
        "v_and_b32 v213, 0xffff0000, v122\n"
        "v_lshlrev_b32 v214, 16, v123\n"
        "v_and_b32 v85, 0xffff0000, v74\n"
        "v_lshlrev_b32 v86, 16, v75\n"
        "v_mul_f32 v213, v213, v85\n"
        "v_mul_f32 v214, v214, v86\n"
        "v_cvt_pk_bf16_f32 v213, 0, v213\n"
        "v_cvt_pk_bf16_f32 v214, 0, v214\n"
        "v_cvt_pk_bf16_f32 v215, 0, v215\n"
        "v_mul_f32 v212, v212, v134\n"
        "v_mul_f32 v213, v213, v134\n"
        "v_mul_f32 v214, v214, v134\n"
        "v_mul_f32 v215, v215, v134\n"
        "v_cvt_pk_bf16_f32 v212, 0, v212\n"
        "v_cvt_pk_bf16_f32 v213, 0, v213\n"
        "v_cvt_pk_bf16_f32 v214, 0, v214\n"
        "v_cvt_pk_bf16_f32 v215, 0, v215\n"
        "v_lshlrev_b32 v216, 16, v124\n"
        "v_and_b32 v217, 0xffff0000, v124\n"
        "v_lshlrev_b32 v218, 16, v125\n"
        "v_and_b32 v219, 0xffff0000, v125\n"
        "v_lshlrev_b32 v80, 16, v76\n"
        "v_and_b32 v81, 0xffff0000, v76\n"
        "v_lshlrev_b32 v82, 16, v77\n"
        "v_and_b32 v83, 0xffff0000, v77\n"
        "v_mul_f32 v216, v216, v80\n"
        "v_mul_f32 v217, v217, v81\n"
        "v_mul_f32 v218, v218, v82\n"
        "v_mul_f32 v219, v219, v83\n"
        "v_cvt_pk_bf16_f32 v216, 0, v216\n"
        "v_cvt_pk_bf16_f32 v217, 0, v217\n"
        "v_cvt_pk_bf16_f32 v218, 0, v218\n"
        "v_cvt_pk_bf16_f32 v219, 0, v219\n"
        "v_mul_f32 v216, v216, v135\n"
        "v_mul_f32 v217, v217, v135\n"
        "v_mul_f32 v218, v218, v135\n"
        "v_mul_f32 v219, v219, v135\n"
        "v_cvt_pk_bf16_f32 v216, 0, v216\n"
        "v_cvt_pk_bf16_f32 v217, 0, v217\n"
        "v_cvt_pk_bf16_f32 v218, 0, v218\n"
        "v_cvt_pk_bf16_f32 v219, 0, v219\n"
        "v_lshlrev_b32 v220, 16, v126\n"
        "v_and_b32 v221, 0xffff0000, v126\n"
        "v_lshlrev_b32 v222, 16, v127\n"
        "v_lshlrev_b32 v84, 16, v78\n"
        "v_and_b32 v85, 0xffff0000, v78\n"
        "v_lshlrev_b32 v86, 16, v79\n"
        "v_mul_f32 v220, v220, v84\n"
        "v_mul_f32 v221, v221, v85\n"
        "v_mul_f32 v222, v222, v86\n"
        "v_cvt_pk_bf16_f32 v220, 0, v220\n"
        "v_cvt_pk_bf16_f32 v221, 0, v221\n"
        "v_cvt_pk_bf16_f32 v222, 0, v222\n"
        "v_mul_f32 v220, v220, v135\n"
        "v_mul_f32 v221, v221, v135\n"
        "v_mul_f32 v222, v222, v135\n"
        "v_cvt_pk_bf16_f32 v220, 0, v220\n"
        "s_waitcnt vmcnt(0)\n"
        "v_cmp_ne_u64 vcc, v[236:237], v[250:251]\n"
        "v_cvt_pk_bf16_f32 v221, 0, v221\n"
        "s_cbranch_vccnz LPPR_%=\n"
        "LBPR_%=:\n"
        "global_load_dwordx4 v[96:99], v254, s[30:31] offset:0 sc1\n"
        "global_load_dwordx4 v[100:103], v254, s[30:31] offset:1024 sc1\n"
        "global_load_dwordx4 v[104:107], v254, s[30:31] offset:2048 sc1\n"
        "global_load_dwordx4 v[108:111], v254, s[30:31] offset:3072 sc1\n"
        "v_cvt_pk_bf16_f32 v222, 0, v222\n"
        "s_waitcnt vmcnt(3)\n"
        "v_mfma_f32_16x16x32_bf16 v[32:35], v[138:141], v[96:99], v[32:35]\n"
        "v_mfma_f32_16x16x32_bf16 v[36:39], v[136:139], v[96:99], v[36:39]\n"
        "s_waitcnt vmcnt(2)\n"
        "v_mfma_f32_16x16x32_bf16 v[40:43], v[138:141], v[100:103], v[40:43]\n"
        "v_mfma_f32_16x16x32_bf16 v[44:47], v[136:139], v[100:103], v[44:47]\n"
        "s_waitcnt vmcnt(1)\n"
        "v_mfma_f32_16x16x32_bf16 v[48:51], v[138:141], v[104:107], v[48:51]\n"
        "v_mfma_f32_16x16x32_bf16 v[52:55], v[136:139], v[104:107], v[52:55]\n"
        "s_waitcnt vmcnt(0)\n"
        "v_fmac_f32 v192, v32, v128\n"
        "v_fmac_f32 v193, v33, v128\n"
        "v_fmac_f32 v194, v34, v128\n"
        "v_fmac_f32 v195, v35, v128\n"
        "v_fmac_f32 v196, v36, v128\n"
        "v_fmac_f32 v197, v37, v128\n"
        "v_mfma_f32_16x16x32_bf16 v[56:59], v[138:141], v[108:111], v[56:59]\n"
        "v_fmac_f32 v198, v38, v128\n"
        "v_fmac_f32 v199, v39, v128\n"
        "v_mfma_f32_16x16x32_bf16 v[60:63], v[136:139], v[108:111], v[60:63]\n"
        "v_cvt_pk_bf16_f32 v192, v192, v193\n"
        "v_cvt_pk_bf16_f32 v193, v194, v195\n"
        "v_cvt_pk_bf16_f32 v194, v196, v197\n"
        "v_cvt_pk_bf16_f32 v195, v198, v199\n"
        "v_fmac_f32 v200, v40, v129\n"
        "v_permlane16_swap_b32 v192, v194\n"
        "v_permlane16_swap_b32 v193, v195\n"
        "global_store_dwordx4 v227, v[192:195], s[56:57] sc0 sc1\n"
        "s_add_u32 s56, s56, 0x8000\n"
        "s_addc_u32 s57, s57, 0\n"
        "v_fmac_f32 v201, v41, v129\n"
        "v_fmac_f32 v202, v42, v129\n"
        "v_fmac_f32 v203, v43, v129\n"
        "v_fmac_f32 v204, v44, v129\n"
        "v_fmac_f32 v205, v45, v129\n"
        "v_fmac_f32 v206, v46, v129\n"
        "v_fmac_f32 v207, v47, v129\n"
        "v_cvt_pk_bf16_f32 v200, v200, v201\n"
        "v_cvt_pk_bf16_f32 v201, v202, v203\n"
        "v_cvt_pk_bf16_f32 v202, v204, v205\n"
        "v_cvt_pk_bf16_f32 v203, v206, v207\n"
        "v_fmac_f32 v208, v48, v130\n"
        "v_permlane16_swap_b32 v200, v202\n"
        "v_permlane16_swap_b32 v201, v203\n"
        "global_store_dwordx4 v227, v[200:203], s[56:57] sc0 sc1\n"
        "s_add_u32 s56, s56, 0x8000\n"
        "s_addc_u32 s57, s57, 0\n"
        "v_fmac_f32 v209, v49, v130\n"
        "v_fmac_f32 v210, v50, v130\n"
        "v_fmac_f32 v211, v51, v130\n"
        "v_fmac_f32 v212, v52, v130\n"
        "v_fmac_f32 v213, v53, v130\n"
        "v_fmac_f32 v214, v54, v130\n"
        "v_fmac_f32 v215, v55, v130\n"
        "v_cvt_pk_bf16_f32 v208, v208, v209\n"
        "v_cvt_pk_bf16_f32 v209, v210, v211\n"
        "v_cvt_pk_bf16_f32 v210, v212, v213\n"
        "v_cvt_pk_bf16_f32 v211, v214, v215\n"
        "v_fmac_f32 v216, v56, v131\n"
        "v_permlane16_swap_b32 v208, v210\n"
        "v_permlane16_swap_b32 v209, v211\n"
        "global_store_dwordx4 v227, v[208:211], s[56:57] sc0 sc1\n"
        "s_add_u32 s56, s56, 0x8000\n"
        "s_addc_u32 s57, s57, 0\n"
        "v_fmac_f32 v217, v57, v131\n"
        "v_fmac_f32 v218, v58, v131\n"
        "v_fmac_f32 v219, v59, v131\n"
        "v_fmac_f32 v220, v60, v131\n"
        "v_fmac_f32 v221, v61, v131\n"
        "v_fmac_f32 v222, v62, v131\n"
        "v_fmac_f32 v223, v63, v131\n"
        "v_cvt_pk_bf16_f32 v216, v216, v217\n"
        "v_cvt_pk_bf16_f32 v217, v218, v219\n"
        "v_cvt_pk_bf16_f32 v218, v220, v221\n"
        "v_cvt_pk_bf16_f32 v219, v222, v223\n"
        "s_nop 0\n"
        "v_permlane16_swap_b32 v216, v218\n"
        "v_permlane16_swap_b32 v217, v219\n"
        "global_store_dwordx4 v227, v[216:219], s[56:57] sc0 sc1\n"
        "; ---- END\n"
        "s_branch LEND_%=\n"
        "LEND_%=:\n"
        :
        : "s"(__builtin_amdgcn_kernarg_segment_ptr()), "s"(blockIdx.x), "s"(__builtin_amdgcn_readfirstlane(threadIdx.x >> 6))
        : "v0", "v1", "v2", "v3", "v4", "v5", "v6", "v7", "v8", "v9", "v10", "v11", "v12", "v13", "v14", "v15", "v16", "v17", "v18", "v19", "v20", "v21", "v22", "v23", "v24", "v25", "v26", "v27", "v28", "v29", "v30", "v31",
        "v32", "v33", "v34", "v35", "v36", "v37", "v38", "v39", "v40", "v41", "v42", "v43", "v44", "v45", "v46", "v47", "v48", "v49", "v50", "v51", "v52", "v53", "v54", "v55", "v56", "v57", "v58", "v59", "v60", "v61", "v62", "v63",
        "v64", "v65", "v66", "v67", "v68", "v69", "v70", "v71", "v72", "v73", "v74", "v75", "v76", "v77", "v78", "v79", "v80", "v81", "v82", "v83", "v84", "v85", "v86", "v87", "v88", "v89", "v90", "v91", "v92", "v93", "v94", "v95",
        "v96", "v97", "v98", "v99", "v100", "v101", "v102", "v103", "v104", "v105", "v106", "v107", "v108", "v109", "v110", "v111", "v112", "v113", "v114", "v115", "v116", "v117", "v118", "v119", "v120", "v121", "v122", "v123", "v124", "v125", "v126", "v127",
        "v128", "v129", "v130", "v131", "v132", "v133", "v134", "v135", "v136", "v137", "v138", "v139", "v140", "v141", "v142", "v143", "v144", "v145", "v146", "v147", "v148", "v149", "v150", "v151", "v152", "v153", "v154", "v155", "v156", "v157", "v158", "v159",
        "v160", "v161", "v162", "v163", "v164", "v165", "v166", "v167", "v168", "v169", "v170", "v171", "v172", "v173", "v174", "v175", "v176", "v177", "v178", "v179", "v180", "v181", "v182", "v183", "v184", "v185", "v186", "v187", "v188", "v189", "v190", "v191",
        "v192", "v193", "v194", "v195", "v196", "v197", "v198", "v199", "v200", "v201", "v202", "v203", "v204", "v205", "v206", "v207", "v208", "v209", "v210", "v211", "v212", "v213", "v214", "v215", "v216", "v217", "v218", "v219", "v220", "v221", "v222", "v223",
        "v224", "v225", "v226", "v227", "v228", "v229", "v230", "v231", "v232", "v233", "v234", "v235", "v236", "v237", "v238", "v239", "v240", "v241", "v242", "v243", "v244", "v245", "v246", "v247", "v248", "v249", "v250", "v251", "v252", "v253", "v254", "v255",
        "a0", "a1", "a2", "a3", "a4", "a5", "a6", "a7", "a8", "a9", "a10", "a11", "a12", "a13", "a14", "a15", "a16", "a17", "a18", "a19", "a20", "a21", "a22", "a23", "a24", "a25", "a26", "a27", "a28", "a29", "a30", "a31",
        "a32", "a33", "a34", "a35", "a36", "a37", "a38", "a39", "a40", "a41", "a42", "a43", "a44", "a45", "a46", "a47", "a48", "a49", "a50", "a51", "a52", "a53", "a54", "a55", "a56", "a57", "a58", "a59", "a60", "a61", "a62", "a63",
        "a64", "a65", "a66", "a67", "a68", "a69", "a70", "a71", "a72", "a73", "a74", "a75", "a76", "a77", "a78", "a79", "a80", "a81", "a82", "a83", "a84", "a85", "a86", "a87", "a88", "a89", "a90", "a91", "a92", "a93", "a94", "a95",
        "a96", "a97", "a98", "a99", "a100", "a101", "a102", "a103", "a104", "a105", "a106", "a107", "a108", "a109", "a110", "a111", "a112", "a113", "a114", "a115", "a116", "a117", "a118", "a119", "a120", "a121", "a122", "a123", "a124", "a125", "a126", "a127",
        "a128", "a129", "a130", "a131", "a132", "a133", "a134", "a135", "a136", "a137", "a138", "a139", "a140", "a141", "a142", "a143", "a144", "a145", "a146", "a147", "a148", "a149", "a150", "a151", "a152", "a153", "a154", "a155", "a156", "a157", "a158", "a159",
        "a160", "a161", "a162", "a163", "a164", "a165", "a166", "a167", "a168", "a169", "a170", "a171", "a172", "a173", "a174", "a175", "a176", "a177", "a178", "a179", "a180", "a181", "a182", "a183", "a184", "a185", "a186", "a187", "a188", "a189", "a190", "a191",
        "a192", "a193", "a194", "a195", "a196", "a197", "a198", "a199", "a200", "a201", "a202", "a203", "a204", "a205", "a206", "a207", "a208", "a209", "a210", "a211", "a212", "a213", "a214", "a215", "a216", "a217", "a218", "a219", "a220", "a221", "a222", "a223",
        "a224", "a225", "a226", "a227", "a228", "a229", "a230", "a231", "a232", "a233", "a234", "a235", "a236", "a237", "a238", "a239", "a240", "a241", "a242", "a243", "a244", "a245", "a246", "a247", "a248", "a249", "a250", "a251", "a252", "a253", "a254", "a255",
        "s8", "s9", "s10", "s11", "s12", "s13", "s14", "s15", "s16", "s17", "s18", "s19", "s20", "s21", "s22", "s23", "s24", "s25", "s26", "s27", "s28", "s29", "s30", "s31", "s36", "s37", "s38", "s39", "s40", "s41", "s42", "s43",
        "s44", "s45", "s46", "s47", "s48", "s49", "s50", "s51", "s52", "s53", "s54", "s55", "s56", "s57", "s58", "s59", "s60", "s61", "s62", "s63", "s64", "s65", "s66", "s67", "s68", "s69", "s70", "s71", "s72", "s73", "s74", "s75",
        "s76", "s77", "s78", "s79", "s80", "s81", "s82", "s83", "s84", "s85", "s86", "s87", "s88", "s89", "s90", "s91", "s92", "s93", "s94", "s95", "s96", "s97", "s98", "s99", "s100", "s101", "vcc", "scc", "memory");
}

extern "C" void kernel_launch(void* const* d_in, const int* in_sizes, int n_in, void* d_out, int out_size, void* d_ws, size_t ws_size, hipStream_t stream) {
    (void)in_sizes; (void)n_in; (void)out_size;
    constexpr size_t WS_USED = (size_t)72 << 20;
    Params P;
    P.x = d_in[0]; P.ckv = d_in[1]; P.wq = d_in[2]; P.wuk = d_in[3]; P.wuv = d_in[4]; P.wo = d_in[5];
    P.bk = d_in[7]; P.bv = d_in[8]; P.shk = d_in[9]; P.ssk = d_in[10]; P.ssd = d_in[11]; P.gw = d_in[12];
    P.out = d_out; P.ws = (char*)d_ws;
    if (ws_size > WS_USED) P.ws += (ws_size - WS_USED) & ~(size_t)0xfffff;
    constexpr int LDS_MAX = 163840;
    (void)hipFuncSetAttribute((const void*)k_main, hipFuncAttributeMaxDynamicSharedMemorySize, LDS_MAX);
    k_main<<<256, 256, LDS_MAX, stream>>>(P);
}
```

```cpp
#include <hip/hip_runtime.h>
#include <stdint.h>

struct Params {
    const void *x, *ckv, *wq, *wuk, *wuv, *wo, *bk, *bv, *shk, *ssk, *ssd, *gw;
    void* out; char* ws;
};

__global__ __launch_bounds__(256, 1) void k_main(Params P) {
    asm volatile(
        "; ---- PRO\n"
        "s_load_dwordx16 s[8:23], %0, 0\n"
        "s_load_dwordx8 s[24:31], %0, 64\n"
        "s_load_dwordx4 s[36:39], %0, 96\n"
        "s_mov_b32 s55, %1\n"
        "s_mov_b32 s40, %2\n"
        "v_mbcnt_lo_u32_b32 v0, -1, 0\n"
        "v_mbcnt_hi_u32_b32 v0, -1, v0\n"
        "s_and_b32 s41, s55, 7\n"
        "s_lshr_b32 s42, s55, 3\n"
        "s_lshr_b32 s50, s42, 3\n"
        "s_lshl2_add_u32 s43, s41, s50\n"
        "s_and_b32 s44, s42, 7\n"
        "s_sub_u32 s45, 15, s44\n"
        "s_lshl_b32 s72, s40, 13\n"
        "s_lshl_b32 s73, s40, 12\n"
        "v_lshlrev_b32 v254, 4, v0\n"
        "v_lshrrev_b32 v2, 4, v0\n"
        "v_and_b32 v1, 15, v0\n"
        "v_lshrrev_b32 v4, 3, v0\n"
        "v_and_b32 v5, 7, v0\n"
        "v_and_b32 v8, 6, v4\n"
        "v_xor_b32 v8, v8, v5\n"
        "v_lshlrev_b32 v8, 4, v8\n"
        "v_lshl_add_u32 v9, v4, 13, v8\n"
        "v_xor_b32 v10, 4, v4\n"
        "v_lshl_add_u32 v10, v10, 13, v8\n"
        "v_add_u32 v238, 4096, v9\n"
        "v_add_u32 v239, 3200, v9\n"
        "v_add_u32 v240, 67584, v10\n"
        "v_add_u32 v241, 66688, v10\n"
        "s_cmp_ge_u32 s40, 2\n"
        "s_cselect_b32 s50, s45, s44\n"
        "s_and_b32 s51, s40, 1\n"
        "s_lshl1_add_u32 s50, s50, s51\n"
        "s_lshl_b32 s50, s50, 16\n"
        "s_lshl_b32 s51, s40, 17\n"
        "s_lshl_b32 s52, s43, 8\n"
        "s_add_u32 s51, s51, s52\n"
        "s_waitcnt lgkmcnt(0)\n"
        "s_add_u32 s60, s14, s51\n"
        "s_addc_u32 s61, s15, 0\n"
        "s_sub_u32 s60, s60, 4096\n"
        "s_subb_u32 s61, s61, 0\n"
        "s_add_u32 m0, s73, 0x10000\n"
        "s_nop 0\n"
        "global_load_lds_dwordx4 v238, s[60:61] offset:0\n"
        "global_load_lds_dwordx4 v239, s[60:61] offset:1024\n"
        "global_load_lds_dwordx4 v240, s[60:61] offset:2048\n"
        "global_load_lds_dwordx4 v241, s[60:61] offset:3072\n"
        "v_xor_b32 v6, v5, v2\n"
        "v_lshlrev_b32 v6, 4, v6\n"
        "v_xor_b32 v7, 64, v6\n"
        "v_lshl_add_u32 v234, v4, 10, v6\n"
        "v_lshl_add_u32 v8, v4, 10, v7\n"
        "v_add_u32 v235, 7168, v8\n"
        "v_add_u32 v236, 14336, v234\n"
        "v_add_u32 v237, 21504, v8\n"
        "s_add_u32 s56, s10, s50\n"
        "s_addc_u32 s57, s11, 0\n"
        "s_add_u32 m0, s72, 0x0\n"
        "s_nop 0\n"
        "global_load_lds_dwordx4 v234, s[56:57] offset:0\n"
        "global_load_lds_dwordx4 v235, s[56:57] offset:1024\n"
        "global_load_lds_dwordx4 v236, s[56:57] offset:2048\n"
        "global_load_lds_dwordx4 v237, s[56:57] offset:3072\n"
        "s_getpc_b64 s[46:47]\n"
        "LPC_%=:\n"
        "s_lshl2_add_u32 s53, s42, s40\n"
        "s_lshl_b32 s53, s53, 10\n"
        "v_lshlrev_b32 v12, 6, v1\n"
        "v_add_u32 v12, s53, v12\n"
        "v_min_u32 v252, LEND_%=-LPC_%=-4, v12\n"
        "global_load_dword v253, v252, s[46:47] offset:0\n"
        "s_add_u32 s62, s16, s51\n"
        "s_addc_u32 s63, s17, 0\n"
        "s_sub_u32 s62, s62, 4096\n"
        "s_subb_u32 s63, s63, 0\n"
        "s_add_u32 m0, s73, 0x14000\n"
        "s_nop 0\n"
        "global_load_lds_dwordx4 v238, s[62:63] offset:0\n"
        "global_load_lds_dwordx4 v239, s[62:63] offset:1024\n"
        "global_load_lds_dwordx4 v240, s[62:63] offset:2048\n"
        "global_load_lds_dwordx4 v241, s[62:63] offset:3072\n"
        "s_add_u32 s60, s60, 0x80000\n"
        "s_addc_u32 s61, s61, 0\n"
        "s_add_u32 s62, s62, 0x80000\n"
        "s_addc_u32 s63, s63, 0\n"
        "s_add_u32 s58, s56, 0x8000\n"
        "s_addc_u32 s59, s57, 0\n"
        "s_add_u32 s68, s12, s51\n"
        "s_addc_u32 s69, s13, 0\n"
        "s_add_u32 m0, s72, 0x1000\n"
        "s_nop 0\n"
        "global_load_lds_dwordx4 v234, s[58:59] offset:0\n"
        "global_load_lds_dwordx4 v235, s[58:59] offset:1024\n"
        "global_load_lds_dwordx4 v236, s[58:59] offset:2048\n"
        "global_load_lds_dwordx4 v237, s[58:59] offset:3072\n"
        "s_sub_u32 s68, s68, 4096\n"
        "s_subb_u32 s69, s69, 0\n"
        "v_lshrrev_b32 v8, 1, v1\n"
        "v_xor_b32 v8, v8, v2\n"
        "v_lshlrev_b32 v8, 4, v8\n"
        "v_lshl_add_u32 v8, v1, 7, v8\n"
        "v_xor_b32 v9, 64, v8\n"
        "s_lshr_b32 s50, s40, 1\n"
        "s_lshl_b32 s50, s50, 14\n"
        "v_add_u32 v224, s50, v8\n"
        "v_add_u32 v246, s72, v8\n"
        "v_add_u32 v248, 0x10000, v246\n"
        "v_add_u32 v225, s50, v9\n"
        "v_add_u32 v247, s72, v9\n"
        "v_add_u32 v249, 0x10000, v247\n"
        "s_and_b32 s50, s40, 1\n"
        "s_lshl_b32 s50, s50, 10\n"
        "s_add_u32 s50, s50, 0x10000\n"
        "v_lshlrev_b32 v8, 11, v2\n"
        "v_add_u32 v8, s50, v8\n"
        "v_lshrrev_b32 v9, 2, v1\n"
        "v_or_b32 v10, 4, v9\n"
        "v_and_b32 v11, 1, v2\n"
        "v_and_b32 v13, 3, v0\n"
        "v_lshlrev_b32 v14, 2, v11\n"
        "v_xor_b32 v14, v14, v9\n"
        "v_bfe_u32 v15, v14, 1, 2\n"
        "v_xor_b32 v15, 0, v15\n"
        "v_lshlrev_b32 v15, 5, v15\n"
        "v_lshl_add_u32 v14, v14, 7, v15\n"
        "v_lshl_add_u32 v14, v13, 3, v14\n"
        "v_add_u32 v226, v14, v8\n"
        "v_xor_b32 v228, 32, v226\n"
        "v_xor_b32 v230, 64, v226\n"
        "v_xor_b32 v232, 96, v226\n"
        "v_lshlrev_b32 v14, 2, v11\n"
        "v_xor_b32 v14, v14, v10\n"
        "v_bfe_u32 v15, v14, 1, 2\n"
        "v_xor_b32 v15, 0, v15\n"
        "v_lshlrev_b32 v15, 5, v15\n"
        "v_lshl_add_u32 v14, v14, 7, v15\n"
        "v_lshl_add_u32 v14, v13, 3, v14\n"
        "v_add_u32 v227, v14, v8\n"
        "v_xor_b32 v229, 32, v227\n"
        "v_xor_b32 v231, 64, v227\n"
        "v_xor_b32 v233, 96, v227\n"
        "v_add_u32 v216, 0x10000, v226\n"
        "v_add_u32 v217, 0x10000, v227\n"
        "v_add_u32 v218, 0x10000, v228\n"
        "v_add_u32 v219, 0x10000, v229\n"
        "v_add_u32 v220, 0x10000, v230\n"
        "v_add_u32 v221, 0x10000, v231\n"
        "v_add_u32 v222, 0x10000, v232\n"
        "v_add_u32 v223, 0x10000, v233\n"
        "v_lshl_add_u32 v242, v4, 11, v6\n"
        "v_lshl_add_u32 v8, v4, 11, v7\n"
        "v_add_u32 v243, 15360, v8\n"
        "v_add_u32 v244, 129024, v242\n"
        "v_add_u32 v245, 144384, v8\n"
        "s_lshl_b32 s50, s45, 18\n"
        "s_lshl_b32 s51, s40, 15\n"
        "s_add_u32 s50, s50, s51\n"
        "s_add_u32 s64, s8, s50\n"
        "s_addc_u32 s65, s9, 0\n"
        "s_lshl_b32 s50, s44, 18\n"
        "s_add_u32 s50, s50, s51\n"
        "s_add_u32 s66, s8, s50\n"
        "s_addc_u32 s67, s9, 0\n"
        "s_add_u32 s48, s38, 0x3900000\n"
        "s_addc_u32 s49, s39, 0\n"
        "v_mov_b32 v11, 0\n"
        "global_load_dwordx2 v[250:251], v11, s[48:49] sc1\n"
        "s_lshl2_add_u32 s82, s55, s40\n"
        "s_lshl_b32 s82, s82, 11\n"
        "v_and_b32 v20, 0xf0, v254\n"
        "v_lshl_add_u32 v20, v20, 3, s82\n"
        "global_load_dword v253, v20, s[10:11] offset:0\n"
        "s_lshl2_add_u32 s82, s42, s40\n"
        "s_lshl3_add_u32 s82, s82, s41\n"
        "s_lshl_b32 s82, s82, 10\n"
        "v_and_b32 v21, 0x70, v254\n"
        "v_lshl_add_u32 v173, v21, 3, s82\n"
        "s_lshl2_add_u32 s82, s55, s40\n"
        "s_lshl_b32 s82, s82, 12\n"
        "v_bfe_u32 v20, v254, 4, 1\n"
        "v_bfe_u32 v21, v254, 5, 1\n"
        "v_lshlrev_b32 v20, 7, v20\n"
        "v_lshl_add_u32 v20, v21, 11, v20\n"
        "v_add_u32 v199, s82, v20\n"
        "s_mov_b64 exec, 0\n"
        "s_cmp_eq_u32 s40, 0\n"
        "s_cbranch_scc1 LHA0_%=\n"
        "s_cmp_eq_u32 s40, 1\n"
        "s_cbranch_scc1 LHA1_%=\n"
        "s_cmp_eq_u32 s40, 2\n"
        "s_cbranch_scc1 LHA2_%=\n"
        "s_branch LHA3_%=\n"
        "LHRA_%=:\n"
        "s_mov_b64 exec, -1\n"
        "s_add_u32 s56, s56, 0x80\n"
        "s_addc_u32 s57, s57, 0\n"
        "s_add_u32 s58, s58, 0x80\n"
        "s_addc_u32 s59, s59, 0\n"
        "s_add_u32 m0, s73, 0x18000\n"
        "s_nop 0\n"
        "global_load_lds_dwordx4 v238, s[60:61] offset:0\n"
        "global_load_lds_dwordx4 v239, s[60:61] offset:1024\n"
        "global_load_lds_dwordx4 v240, s[60:61] offset:2048\n"
        "global_load_lds_dwordx4 v241, s[60:61] offset:3072\n"
        "s_add_u32 m0, s73, 0x1c000\n"
        "s_nop 0\n"
        "global_load_lds_dwordx4 v238, s[62:63] offset:0\n"
        "global_load_lds_dwordx4 v239, s[62:63] offset:1024\n"
        "global_load_lds_dwordx4 v240, s[62:63] offset:2048\n"
        "global_load_lds_dwordx4 v241, s[62:63] offset:3072\n"
        "s_add_u32 s60, s60, 0x80000\n"
        "s_addc_u32 s61, s61, 0\n"
        "s_add_u32 s62, s62, 0x80000\n"
        "s_addc_u32 s63, s63, 0\n"
        "s_branch LFRONT_%=\n"
        "LREDO_%=:\n"
        "s_lshl2_add_u32 s97, s55, s40\n"
        "s_lshl_b32 s97, s97, 12\n"
        "s_add_u32 s97, s97, 0x4400000\n"
        "s_add_u32 s88, s38, s97\n"
        "s_addc_u32 s89, s39, 0\n"
        "global_store_dwordx4 v254, v[144:147], s[88:89] offset:0\n"
        "global_store_dwordx4 v254, v[148:151], s[88:89] offset:1024\n"
        "global_store_dwordx4 v254, v[152:155], s[88:89] offset:2048\n"
        "global_store_dwordx4 v254, v[156:159], s[88:89] offset:3072\n"
        "s_waitcnt vmcnt(0)\n"
        "v_mov_b32 v112, 0\n"
        "v_mov_b32 v113, 0\n"
        "v_mov_b32 v114, 0\n"
        "v_mov_b32 v115, 0\n"
        "v_mov_b32 v116, 0\n"
        "v_mov_b32 v117, 0\n"
        "v_mov_b32 v118, 0\n"
        "v_mov_b32 v119, 0\n"
        "v_mov_b32 v120, 0\n"
        "v_mov_b32 v121, 0\n"
        "v_mov_b32 v122, 0\n"
        "v_mov_b32 v123, 0\n"
        "v_mov_b32 v124, 0\n"
        "v_mov_b32 v125, 0\n"
        "v_mov_b32 v126, 0\n"
        "v_mov_b32 v127, 0\n"
        "v_mov_b32 v144, 0\n"
        "v_mov_b32 v145, 0\n"
        "v_mov_b32 v146, 0\n"
        "v_mov_b32 v147, 0\n"
        "v_mov_b32 v148, 0\n"
        "v_mov_b32 v149, 0\n"
        "v_mov_b32 v150, 0\n"
        "v_mov_b32 v151, 0\n"
        "v_mov_b32 v152, 0\n"
        "v_mov_b32 v153, 0\n"
        "v_mov_b32 v154, 0\n"
        "v_mov_b32 v155, 0\n"
        "v_mov_b32 v156, 0\n"
        "v_mov_b32 v157, 0\n"
        "v_mov_b32 v158, 0\n"
        "v_mov_b32 v159, 0\n"
        "v_mov_b32 v60, 0\n"
        "v_mov_b32 v61, 0\n"
        "v_mov_b32 v62, 0\n"
        "v_mov_b32 v63, 0\n"
        "v_mov_b32 v24, 0xf149f2ca\n"
        "s_lshl_b32 s97, s53, 7\n"
        "s_add_u32 s97, s97, s96\n"
        "v_add_u32 v27, s97, v243\n"
        "s_lshl_b32 s97, s43, 19\n"
        "s_add_u32 s84, s38, s97\n"
        "s_addc_u32 s85, s39, 0\n"
        "s_add_u32 s84, s84, 0x1000\n"
        "s_addc_u32 s85, s85, 0\n"
        "s_add_u32 s86, s84, 0x1000000\n"
        "s_addc_u32 s87, s85, 0\n"
        "s_lshl_b32 s97, s53, 2\n"
        "s_add_u32 s97, s97, 4\n"
        "LRLOOP_%=:\n"
        "global_load_dwordx4 a[160:163], v254, s[84:85] offset:-4096 sc1\n"
        "global_load_dwordx4 a[164:167], v254, s[84:85] offset:-3072 sc1\n"
        "global_load_dwordx4 a[168:171], v254, s[84:85] offset:-2048 sc1\n"
        "global_load_dwordx4 a[172:175], v254, s[84:85] offset:-1024 sc1\n"
        "s_waitcnt vmcnt(0)\n"
        "v_mfma_f32_16x16x32_bf16 v[40:43], a[160:163], a[176:179], 0\n"
        "v_mfma_f32_16x16x32_bf16 v[40:43], a[164:167], a[180:183], v[40:43]\n"
        "v_mfma_f32_16x16x32_bf16 v[40:43], a[168:171], a[184:187], v[40:43]\n"
        "v_mfma_f32_16x16x32_bf16 v[40:43], a[172:175], a[188:191], v[40:43]\n"
        "s_nop 7\n"
        "global_load_dwordx4 a[160:163], v254, s[84:85] offset:0 sc1\n"
        "global_load_dwordx4 a[164:167], v254, s[84:85] offset:1024 sc1\n"
        "global_load_dwordx4 a[168:171], v254, s[84:85] offset:2048 sc1\n"
        "global_load_dwordx4 a[172:175], v254, s[84:85] offset:3072 sc1\n"
        "s_waitcnt vmcnt(0)\n"
        "v_mfma_f32_16x16x32_bf16 v[44:47], a[160:163], a[176:179], 0\n"
        "v_mfma_f32_16x16x32_bf16 v[44:47], a[164:167], a[180:183], v[44:47]\n"
        "v_mfma_f32_16x16x32_bf16 v[44:47], a[168:171], a[184:187], v[44:47]\n"
        "v_mfma_f32_16x16x32_bf16 v[44:47], a[172:175], a[188:191], v[44:47]\n"
        "s_nop 7\n"
        "v_cmp_gt_i32 vcc, 0, v27\n"
        "s_nop 1\n"
        "v_cndmask_b32 v40, v40, v242, vcc\n"
        "v_cmp_gt_i32 vcc, 1, v27\n"
        "s_nop 1\n"
        "v_cndmask_b32 v41, v41, v242, vcc\n"
        "v_cmp_gt_i32 vcc, 2, v27\n"
        "s_nop 1\n"
        "v_cndmask_b32 v42, v42, v242, vcc\n"
        "v_cmp_gt_i32 vcc, 3, v27\n"
        "s_nop 1\n"
        "v_cndmask_b32 v43, v43, v242, vcc\n"
        "v_cmp_gt_i32 vcc, 16, v27\n"
        "s_nop 1\n"
        "v_cndmask_b32 v44, v44, v242, vcc\n"
        "v_cmp_gt_i32 vcc, 17, v27\n"
        "s_nop 1\n"
        "v_cndmask_b32 v45, v45, v242, vcc\n"
        "v_cmp_gt_i32 vcc, 18, v27\n"
        "s_nop 1\n"
        "v_cndmask_b32 v46, v46, v242, vcc\n"
        "v_cmp_gt_i32 vcc, 19, v27\n"
        "s_nop 1\n"
        "v_cndmask_b32 v47, v47, v242, vcc\n"
        "v_max3_f32 v25, v40, v41, v24\n"
        "v_max3_f32 v25, v42, v43, v25\n"
        "v_max3_f32 v25, v44, v45, v25\n"
        "v_max3_f32 v25, v46, v47, v25\n"
        "v_mov_b32 v28, v25\n"
        "s_nop 1\n"
        "v_permlane16_swap_b32 v25, v28\n"
        "v_max_f32 v25, v25, v28\n"
        "v_mov_b32 v28, v25\n"
        "s_nop 1\n"
        "v_permlane32_swap_b32 v25, v28\n"
        "v_max_f32 v25, v25, v28\n"
        "v_sub_f32 v26, v24, v25\n"
        "v_exp_f32 v26, v26\n"
        "v_mov_b32 v24, v25\n"
        "v_sub_f32 v40, v40, v25\n"
        "v_exp_f32 v40, v40\n"
        "v_sub_f32 v41, v41, v25\n"
        "v_exp_f32 v41, v41\n"
        "v_sub_f32 v42, v42, v25\n"
        "v_exp_f32 v42, v42\n"
        "v_sub_f32 v43, v43, v25\n"
        "v_exp_f32 v43, v43\n"
        "v_sub_f32 v44, v44, v25\n"
        "v_exp_f32 v44, v44\n"
        "v_sub_f32 v45, v45, v25\n"
        "v_exp_f32 v45, v45\n"
        "v_sub_f32 v46, v46, v25\n"
        "v_exp_f32 v46, v46\n"
        "v_sub_f32 v47, v47, v25\n"
        "v_exp_f32 v47, v47\n"
        "v_cvt_pk_bf16_f32 v56, v40, v41\n"
        "v_cvt_pk_bf16_f32 v57, v42, v43\n"
        "v_cvt_pk_bf16_f32 v58, v44, v45\n"
        "v_cvt_pk_bf16_f32 v59, v46, v47\n"
        "v_mul_f32 v112, v112, v26\n"
        "v_mul_f32 v113, v113, v26\n"
        "v_mul_f32 v114, v114, v26\n"
        "v_mul_f32 v115, v115, v26\n"
        "v_mul_f32 v116, v116, v26\n"
        "v_mul_f32 v117, v117, v26\n"
        "v_mul_f32 v118, v118, v26\n"
        "v_mul_f32 v119, v119, v26\n"
        "v_mul_f32 v120, v120, v26\n"
        "v_mul_f32 v121, v121, v26\n"
        "v_mul_f32 v122, v122, v26\n"
        "v_mul_f32 v123, v123, v26\n"
        "v_mul_f32 v124, v124, v26\n"
        "v_mul_f32 v125, v125, v26\n"
        "v_mul_f32 v126, v126, v26\n"
        "v_mul_f32 v127, v127, v26\n"
        "v_mul_f32 v144, v144, v26\n"
        "v_mul_f32 v145, v145, v26\n"
        "v_mul_f32 v146, v146, v26\n"
        "v_mul_f32 v147, v147, v26\n"
        "v_mul_f32 v148, v148, v26\n"
        "v_mul_f32 v149, v149, v26\n"
        "v_mul_f32 v150, v150, v26\n"
        "v_mul_f32 v151, v151, v26\n"
        "v_mul_f32 v152, v152, v26\n"
        "v_mul_f32 v153, v153, v26\n"
        "v_mul_f32 v154, v154, v26\n"
        "v_mul_f32 v155, v155, v26\n"
        "v_mul_f32 v156, v156, v26\n"
        "v_mul_f32 v157, v157, v26\n"
        "v_mul_f32 v158, v158, v26\n"
        "v_mul_f32 v159, v159, v26\n"
        "v_mul_f32 v60, v60, v26\n"
        "v_mul_f32 v61, v61, v26\n"
        "v_mul_f32 v62, v62, v26\n"
        "v_mul_f32 v63, v63, v26\n"
        "s_nop 1\n"
        "v_mfma_f32_16x16x32_bf16 v[60:63], a[80:83], v[56:59], v[60:63]\n"
        "global_load_dwordx4 a[160:163], v254, s[86:87] offset:-4096 sc1\n"
        "global_load_dwordx4 a[164:167], v254, s[86:87] offset:-3072 sc1\n"
        "global_load_dwordx4 a[168:171], v254, s[86:87] offset:-2048 sc1\n"
        "global_load_dwordx4 a[172:175], v254, s[86:87] offset:-1024 sc1\n"
        "s_waitcnt vmcnt(0)\n"
        "v_mfma_f32_16x16x32_bf16 v[112:115], a[160:163], v[56:59], v[112:115]\n"
        "v_mfma_f32_16x16x32_bf16 v[116:119], a[164:167], v[56:59], v[116:119]\n"
        "v_mfma_f32_16x16x32_bf16 v[120:123], a[168:171], v[56:59], v[120:123]\n"
        "v_mfma_f32_16x16x32_bf16 v[124:127], a[172:175], v[56:59], v[124:127]\n"
        "s_nop 7\n"
        "global_load_dwordx4 a[160:163], v254, s[86:87] offset:0 sc1\n"
        "global_load_dwordx4 a[164:167], v254, s[86:87] offset:1024 sc1\n"
        "global_load_dwordx4 a[168:171], v254, s[86:87] offset:2048 sc1\n"
        "global_load_dwordx4 a[172:175], v254, s[86:87] offset:3072 sc1\n"
        "s_waitcnt vmcnt(0)\n"
        "v_mfma_f32_16x16x32_bf16 v[144:147], a[160:163], v[56:59], v[144:147]\n"
        "v_mfma_f32_16x16x32_bf16 v[148:151], a[164:167], v[56:59], v[148:151]\n"
        "v_mfma_f32_16x16x32_bf16 v[152:155], a[168:171], v[56:59], v[152:155]\n"
        "v_mfma_f32_16x16x32_bf16 v[156:159], a[172:175], v[56:59], v[156:159]\n"
        "s_nop 7\n"
        "s_add_u32 s84, s84, 0x2000\n"
        "s_addc_u32 s85, s85, 0\n"
        "s_add_u32 s86, s86, 0x2000\n"
        "s_addc_u32 s87, s87, 0\n"
        "v_subrev_u32 v27, 32, v27\n"
        "s_sub_u32 s97, s97, 1\n"
        "s_cmp_lg_u32 s97, 0\n"
        "s_cbranch_scc1 LRLOOP_%=\n"
        "s_lshl_b32 s97, s53, 20\n"
        "s_lshl_b32 s86, s43, 15\n"
        "s_add_u32 s97, s97, s86\n"
        "s_lshl_b32 s86, s40, 10\n"
        "s_add_u32 s97, s97, s86\n"
        "s_lshl_b32 s86, s96, 6\n"
        "s_add_u32 s97, s97, s86\n"
        "s_add_u32 s97, s97, 0x2000000\n"
        "s_add_u32 s84, s38, s97\n"
        "s_addc_u32 s85, s39, 0\n"
        "v_rcp_f32 v26, v60\n"
        "s_nop 0\n"
        "v_mul_f32 v112, v112, v26\n"
        "v_mul_f32 v113, v113, v26\n"
        "v_mul_f32 v114, v114, v26\n"
        "v_mul_f32 v115, v115, v26\n"
        "v_mul_f32 v116, v116, v26\n"
        "v_mul_f32 v117, v117, v26\n"
        "v_mul_f32 v118, v118, v26\n"
        "v_mul_f32 v119, v119, v26\n"
        "v_cvt_pk_bf16_f32 v112, v112, v113\n"
        "v_cvt_pk_bf16_f32 v113, v114, v115\n"
        "v_cvt_pk_bf16_f32 v114, v116, v117\n"
        "v_cvt_pk_bf16_f32 v115, v118, v119\n"
        "global_store_dwordx4 v254, v[112:115], s[84:85] sc1\n"
        "s_add_u32 s84, s84, 0x2000\n"
        "s_addc_u32 s85, s85, 0\n"
        "v_mul_f32 v120, v120, v26\n"
        "v_mul_f32 v121, v121, v26\n"
        "v_mul_f32 v122, v122, v26\n"
        "v_mul_f32 v123, v123, v26\n"
        "v_mul_f32 v124, v124, v26\n"
        "v_mul_f32 v125, v125, v26\n"
        "v_mul_f32 v126, v126, v26\n"
        "v_mul_f32 v127, v127, v26\n"
        "v_cvt_pk_bf16_f32 v120, v120, v121\n"
        "v_cvt_pk_bf16_f32 v121, v122, v123\n"
        "v_cvt_pk_bf16_f32 v122, v124, v125\n"
        "v_cvt_pk_bf16_f32 v123, v126, v127\n"
        "global_store_dwordx4 v254, v[120:123], s[84:85] sc1\n"
        "s_add_u32 s84, s84, 0x2000\n"
        "s_addc_u32 s85, s85, 0\n"
        "v_mul_f32 v144, v144, v26\n"
        "v_mul_f32 v145, v145, v26\n"
        "v_mul_f32 v146, v146, v26\n"
        "v_mul_f32 v147, v147, v26\n"
        "v_mul_f32 v148, v148, v26\n"
        "v_mul_f32 v149, v149, v26\n"
        "v_mul_f32 v150, v150, v26\n"
        "v_mul_f32 v151, v151, v26\n"
        "v_cvt_pk_bf16_f32 v144, v144, v145\n"
        "v_cvt_pk_bf16_f32 v145, v146, v147\n"
        "v_cvt_pk_bf16_f32 v146, v148, v149\n"
        "v_cvt_pk_bf16_f32 v147, v150, v151\n"
        "global_store_dwordx4 v254, v[144:147], s[84:85] sc1\n"
        "s_add_u32 s84, s84, 0x2000\n"
        "s_addc_u32 s85, s85, 0\n"
        "v_mul_f32 v152, v152, v26\n"
        "v_mul_f32 v153, v153, v26\n"
        "v_mul_f32 v154, v154, v26\n"
        "v_mul_f32 v155, v155, v26\n"
        "v_mul_f32 v156, v156, v26\n"
        "v_mul_f32 v157, v157, v26\n"
        "v_mul_f32 v158, v158, v26\n"
        "v_mul_f32 v159, v159, v26\n"
        "v_cvt_pk_bf16_f32 v152, v152, v153\n"
        "v_cvt_pk_bf16_f32 v153, v154, v155\n"
        "v_cvt_pk_bf16_f32 v154, v156, v157\n"
        "v_cvt_pk_bf16_f32 v155, v158, v159\n"
        "global_store_dwordx4 v254, v[152:155], s[84:85] sc1\n"
        "s_add_u32 s84, s84, 0x2000\n"
        "s_addc_u32 s85, s85, 0\n"
        "s_waitcnt vmcnt(0)\n"
        "global_load_dwordx4 v[144:147], v254, s[88:89] offset:0 sc1\n"
        "global_load_dwordx4 v[148:151], v254, s[88:89] offset:1024 sc1\n"
        "global_load_dwordx4 v[152:155], v254, s[88:89] offset:2048 sc1\n"
        "global_load_dwordx4 v[156:159], v254, s[88:89] offset:3072 sc1\n"
        "s_waitcnt vmcnt(0)\n"
        "s_cmp_eq_u32 s52, 0\n"
        "s_cbranch_scc1 LRET0_%=\n"
        "s_cmp_eq_u32 s52, 1\n"
        "s_cbranch_scc1 LRET1_%=\n"
        "s_cmp_eq_u32 s52, 2\n"
        "s_cbranch_scc1 LRET2_%=\n"
        "s_branch LRET3_%=\n"
        "LRL_%=:\n"
        "s_waitcnt vmcnt(0) lgkmcnt(0)\n"
        "s_nop 7\n"
        "v_accvgpr_write_b32 a176, v96\n"
        "v_accvgpr_write_b32 a177, v97\n"
        "v_accvgpr_write_b32 a178, v98\n"
        "v_accvgpr_write_b32 a179, v99\n"
        "v_accvgpr_write_b32 a180, v100\n"
        "v_accvgpr_write_b32 a181, v101\n"
        "v_accvgpr_write_b32 a182, v102\n"
        "v_accvgpr_write_b32 a183, v103\n"
        "v_accvgpr_write_b32 a184, v104\n"
        "v_accvgpr_write_b32 a185, v105\n"
        "v_accvgpr_write_b32 a186, v106\n"
        "v_accvgpr_write_b32 a187, v107\n"
        "v_accvgpr_write_b32 a188, v108\n"
        "v_accvgpr_write_b32 a189, v109\n"
        "v_accvgpr_write_b32 a190, v110\n"
        "v_accvgpr_write_b32 a191, v111\n"
        "s_mov_b32 s53, s44\n"
        "s_mov_b32 s96, 64\n"
        "s_mov_b32 s52, 0\n"
        "s_branch LREDO_%=\n"
        "LRET0_%=:\n"
        "v_accvgpr_write_b32 a176, v128\n"
        "v_accvgpr_write_b32 a177, v129\n"
        "v_accvgpr_write_b32 a178, v130\n"
        "v_accvgpr_write_b32 a179, v131\n"
        "v_accvgpr_write_b32 a180, v132\n"
        "v_accvgpr_write_b32 a181, v133\n"
        "v_accvgpr_write_b32 a182, v134\n"
        "v_accvgpr_write_b32 a183, v135\n"
        "v_accvgpr_write_b32 a184, v136\n"
        "v_accvgpr_write_b32 a185, v137\n"
        "v_accvgpr_write_b32 a186, v138\n"
        "v_accvgpr_write_b32 a187, v139\n"
        "v_accvgpr_write_b32 a188, v140\n"
        "v_accvgpr_write_b32 a189, v141\n"
        "v_accvgpr_write_b32 a190, v142\n"
        "v_accvgpr_write_b32 a191, v143\n"
        "s_mov_b32 s53, s44\n"
        "s_mov_b32 s96, 0\n"
        "s_mov_b32 s52, 1\n"
        "s_branch LREDO_%=\n"
        "LRET1_%=:\n"
        "s_branch LSL_%=\n"
        "LRH_%=:\n"
        "s_waitcnt vmcnt(0) lgkmcnt(0)\n"
        "s_nop 7\n"
        "v_accvgpr_write_b32 a176, v0\n"
        "v_accvgpr_write_b32 a177, v1\n"
        "v_accvgpr_write_b32 a178, v2\n"
        "v_accvgpr_write_b32 a179, v3\n"
        "v_accvgpr_write_b32 a180, v4\n"
        "v_accvgpr_write_b32 a181, v5\n"
        "v_accvgpr_write_b32 a182, v6\n"
        "v_accvgpr_write_b32 a183, v7\n"
        "v_accvgpr_write_b32 a184, v8\n"
        "v_accvgpr_write_b32 a185, v9\n"
        "v_accvgpr_write_b32 a186, v10\n"
        "v_accvgpr_write_b32 a187, v11\n"
        "v_accvgpr_write_b32 a188, v12\n"
        "v_accvgpr_write_b32 a189, v13\n"
        "v_accvgpr_write_b32 a190, v14\n"
        "v_accvgpr_write_b32 a191, v15\n"
        "s_mov_b32 s53, s45\n"
        "s_mov_b32 s96, 64\n"
        "s_mov_b32 s52, 2\n"
        "s_branch LREDO_%=\n"
        "LRET2_%=:\n"
        "v_accvgpr_write_b32 a176, v64\n"
        "v_accvgpr_write_b32 a177, v65\n"
        "v_accvgpr_write_b32 a178, v66\n"
        "v_accvgpr_write_b32 a179, v67\n"
        "v_accvgpr_write_b32 a180, v68\n"
        "v_accvgpr_write_b32 a181, v69\n"
        "v_accvgpr_write_b32 a182, v70\n"
        "v_accvgpr_write_b32 a183, v71\n"
        "v_accvgpr_write_b32 a184, v72\n"
        "v_accvgpr_write_b32 a185, v73\n"
        "v_accvgpr_write_b32 a186, v74\n"
        "v_accvgpr_write_b32 a187, v75\n"
        "v_accvgpr_write_b32 a188, v76\n"
        "v_accvgpr_write_b32 a189, v77\n"
        "v_accvgpr_write_b32 a190, v78\n"
        "v_accvgpr_write_b32 a191, v79\n"
        "s_mov_b32 s53, s45\n"
        "s_mov_b32 s96, 0\n"
        "s_mov_b32 s52, 3\n"
        "s_branch LREDO_%=\n"
        "LRET3_%=:\n"
        "s_branch LSH_%=\n"
        "LPKV_%=:\n"
        "s_mov_b32 s97, 0x200000\n"
        "LQKV_%=:\n"
        "global_load_dwordx2 v[252:253], v245, s[48:49] sc1\n"
        "s_waitcnt vmcnt(0)\n"
        "v_cmp_ne_u64 vcc, v[252:253], v[250:251]\n"
        "s_nop 1\n"
        "s_cbranch_vccz LBKV_%=\n"
        "s_sleep 2\n"
        "s_sub_u32 s97, s97, 1\n"
        "s_cmp_lg_u32 s97, 0\n"
        "s_cbranch_scc1 LQKV_%=\n"
        "s_branch LBKV_%=\n"
        "LPCX_%=:\n"
        "s_mov_b32 s97, 0x200000\n"
        "LQCX_%=:\n"
        "global_load_dwordx2 v[236:237], v159, s[48:49] sc1\n"
        "s_waitcnt vmcnt(0)\n"
        "v_cmp_ne_u64 vcc, v[236:237], v[250:251]\n"
        "s_nop 1\n"
        "s_cbranch_vccz LBCX_%=\n"
        "s_sleep 2\n"
        "s_sub_u32 s97, s97, 1\n"
        "s_cmp_lg_u32 s97, 0\n"
        "s_cbranch_scc1 LQCX_%=\n"
        "s_branch LBCX_%=\n"
        "LPZZ_%=:\n"
        "s_mov_b32 s97, 0x200000\n"
        "LQZZ_%=:\n"
        "global_load_dwordx2 v[236:237], v234, s[48:49] sc1\n"
        "s_waitcnt vmcnt(0)\n"
        "v_cmp_ne_u64 vcc, v[236:237], v[250:251]\n"
        "s_nop 1\n"
        "s_cbranch_vccz LBZZ_%=\n"
        "s_sleep 2\n"
        "s_sub_u32 s97, s97, 1\n"
        "s_cmp_lg_u32 s97, 0\n"
        "s_cbranch_scc1 LQZZ_%=\n"
        "s_branch LBZZ_%=\n"
        "LPPR_%=:\n"
        "s_mov_b32 s97, 0x200000\n"
        "LQPR_%=:\n"
        "global_load_dwordx2 v[236:237], v235, s[48:49] sc1\n"
        "s_waitcnt vmcnt(0)\n"
        "v_cmp_ne_u64 vcc, v[236:237], v[250:251]\n"
        "s_nop 1\n"
        "s_cbranch_vccz LBPR_%=\n"
        "s_sleep 2\n"
        "s_sub_u32 s97, s97, 1\n"
        "s_cmp_lg_u32 s97, 0\n"
        "s_cbranch_scc1 LQPR_%=\n"
        "s_branch LBPR_%=\n"
        ".p2align 12\n"
        "LFRONT_%=:\n"
        "LHA0_%=:\n"
        "s_cbranch_execz LHA4_%=\n"
        "; ---- FRONT\n"
        "s_add_u32 m0, s72, 0x8000\n"
        "s_waitcnt vmcnt(19)\n"
        "global_load_lds_dwordx4 v234, s[56:57] offset:0\n"
        "s_barrier\n"
        "ds_read_b64_tr_b16 v[32:33], v226 offset:0\n"
        "ds_read_b64_tr_b16 v[34:35], v227 offset:0\n"
        "ds_read_b128 v[0:3], v224 offset:0\n"
        "global_load_lds_dwordx4 v235, s[56:57] offset:1024\n"
        "global_load_lds_dwordx4 v236, s[56:57] offset:2048\n"
        "ds_read_b128 v[4:7], v225 offset:0\n"
        "ds_read_b64_tr_b16 v[64:65], v226 offset:8192\n"
        "ds_read_b64_tr_b16 v[66:67], v227 offset:8192\n"
        "s_lshl_b32 s50, s42, 6\n"
        "s_waitcnt lgkmcnt(3)\n"
        "v_mfma_f32_16x16x32_bf16 a[0:3], v[32:35], v[0:3], 0\n"
        "ds_read_b128 v[12:15], v225 offset:2048\n"
        "global_load_lds_dwordx4 v237, s[56:57] offset:3072\n"
        "ds_read_b128 v[8:11], v224 offset:2048\n"
        "s_add_u32 m0, s72, 0x9000\n"
        "s_waitcnt lgkmcnt(2)\n"
        "global_load_lds_dwordx4 v234, s[58:59] offset:0\n"
        "ds_read_b64_tr_b16 v[36:37], v228 offset:0\n"
        "ds_read_b64_tr_b16 v[38:39], v229 offset:0\n"
        "s_lshl_b32 s51, s41, 7\n"
        "v_mfma_f32_16x16x32_bf16 a[0:3], v[64:67], v[4:7], a[0:3]\n"
        "ds_read_b64_tr_b16 v[68:69], v228 offset:8192\n"
        "s_lshl_b32 s52, s40, 5\n"
        "s_waitcnt lgkmcnt(4)\n"
        "v_mfma_f32_16x16x32_bf16 a[32:35], v[64:67], v[12:15], 0\n"
        "ds_read_b64_tr_b16 v[70:71], v229 offset:8192\n"
        "ds_read_b64_tr_b16 v[40:41], v230 offset:0\n"
        "ds_read_b64_tr_b16 v[42:43], v231 offset:0\n"
        "s_add_u32 s51, s51, s52\n"
        "s_waitcnt lgkmcnt(6)\n"
        "v_mfma_f32_16x16x32_bf16 a[32:35], v[32:35], v[8:11], a[32:35]\n"
        "ds_read_b64_tr_b16 v[72:73], v230 offset:8192\n"
        "ds_read_b64_tr_b16 v[74:75], v231 offset:8192\n"
        "ds_read_b64_tr_b16 v[44:45], v232 offset:0\n"
        "ds_read_b64_tr_b16 v[46:47], v233 offset:0\n"
        "ds_read_b64_tr_b16 v[76:77], v232 offset:8192\n"
        "ds_read_b64_tr_b16 v[78:79], v233 offset:8192\n"
        "ds_read_b128 v[16:19], v224 offset:8192\n"
        "ds_read_b128 v[20:23], v225 offset:8192\n"
        "s_lshl2_add_u32 s90, s43, s40\n"
        "s_waitcnt lgkmcnt(12)\n"
        "v_mfma_f32_16x16x32_bf16 a[36:39], v[36:39], v[8:11], 0\n"
        "ds_read_b128 v[24:27], v224 offset:10240\n"
        "ds_read_b128 v[28:31], v225 offset:10240\n"
        "global_load_lds_dwordx4 v235, s[58:59] offset:1024\n"
        "s_lshl_b32 s91, s44, 7\n"
        "s_waitcnt lgkmcnt(12)\n"
        "v_mfma_f32_16x16x32_bf16 a[36:39], v[68:71], v[12:15], a[36:39]\n"
        "s_add_u32 s56, s56, 0x80\n"
        "s_addc_u32 s57, s57, 0\n"
        "v_lshrrev_b32 v166, 4, v254\n"
        "v_mfma_f32_16x16x32_bf16 a[4:7], v[68:71], v[4:7], 0\n"
        "s_waitcnt lgkmcnt(10)\n"
        "v_lshrrev_b32 v167, 3, v166\n"
        "v_and_b32 v168, 7, v166\n"
        "v_mfma_f32_16x16x32_bf16 a[4:7], v[36:39], v[0:3], a[4:7]\n"
        "s_waitcnt lgkmcnt(8)\n"
        "v_lshl_add_u32 v167, v167, 3, s50\n"
        "v_lshl_add_u32 v168, v168, 2, s51\n"
        "v_mfma_f32_16x16x32_bf16 a[8:11], v[40:43], v[0:3], 0\n"
        "global_load_lds_dwordx4 v236, s[58:59] offset:2048\n"
        "s_add_u32 s80, s91, s90\n"
        "v_lshlrev_b32 v169, 11, v167\n"
        "v_mfma_f32_16x16x32_bf16 a[8:11], v[72:75], v[4:7], a[8:11]\n"
        "s_waitcnt lgkmcnt(5)\n"
        "v_lshl_add_u32 v169, v168, 1, v169\n"
        "v_cmp_eq_u32 s[78:79], 0, v167\n"
        "v_mfma_f32_16x16x32_bf16 a[40:43], v[72:75], v[12:15], 0\n"
        "s_waitcnt lgkmcnt(3)\n"
        "v_mov_b32 v171, 6144\n"
        "v_cndmask_b32 v170, 0, v171, s[78:79]\n"
        "v_mfma_f32_16x16x32_bf16 a[40:43], v[40:43], v[8:11], a[40:43]\n"
        "s_waitcnt vmcnt(21)\n"
        "v_add_u32 v170, v170, v169\n"
        "v_sub_u32 v171, 2040, v167\n"
        "v_mfma_f32_16x16x32_bf16 a[44:47], v[44:47], v[8:11], 0\n"
        "s_barrier\n"
        "v_mfma_f32_16x16x32_bf16 a[44:47], v[76:79], v[12:15], a[44:47]\n"
        "ds_read_b64_tr_b16 v[48:49], v226 offset:16384\n"
        "global_load_lds_dwordx4 v237, s[58:59] offset:3072\n"
        "s_add_u32 m0, s73, 0x20000\n"
        "v_mfma_f32_16x16x32_bf16 a[12:15], v[76:79], v[4:7], 0\n"
        "ds_read_b64_tr_b16 v[50:51], v227 offset:16384\n"
        "s_add_u32 s58, s58, 0x80\n"
        "s_addc_u32 s59, s59, 0\n"
        "v_mfma_f32_16x16x32_bf16 a[12:15], v[44:47], v[0:3], a[12:15]\n"
        "ds_read_b64_tr_b16 v[80:81], v226 offset:24576\n"
        "s_lshl_b32 s91, s80, 11\n"
        "v_lshlrev_b32 v171, 1, v171\n"
        "v_mfma_f32_16x16x32_bf16 a[128:131], v[32:35], v[16:19], 0\n"
        "ds_read_b64_tr_b16 v[82:83], v227 offset:24576\n"
        "s_lshl_b32 s80, s80, 7\n"
        "s_waitcnt lgkmcnt(4)\n"
        "v_mfma_f32_16x16x32_bf16 a[128:131], v[64:67], v[20:23], a[128:131]\n"
        "ds_read_b64_tr_b16 v[52:53], v228 offset:16384\n"
        "global_load_lds_dwordx4 v238, s[60:61] offset:0\n"
        "s_mul_i32 s96, s40, 4224\n"
        "v_mfma_f32_16x16x32_bf16 a[160:163], v[64:67], v[28:31], 0\n"
        "ds_read_b64_tr_b16 v[54:55], v229 offset:16384\n"
        "s_add_u32 s96, s96, 0x20240\n"
        "v_lshl_add_u32 v171, v168, 12, v171\n"
        "v_mfma_f32_16x16x32_bf16 a[160:163], v[32:35], v[24:27], a[160:163]\n"
        "ds_read_b64_tr_b16 v[84:85], v228 offset:24576\n"
        "v_lshlrev_b32 v168, 3, v168\n"
        "v_mfma_f32_16x16x32_bf16 a[164:167], v[36:39], v[24:27], 0\n"
        "ds_read_b64_tr_b16 v[86:87], v229 offset:24576\n"
        "s_sub_u32 s74, s20, 2048\n"
        "s_subb_u32 s75, s21, 0\n"
        "v_mfma_f32_16x16x32_bf16 a[164:167], v[68:71], v[28:31], a[164:167]\n"
        "ds_read_b64_tr_b16 v[56:57], v230 offset:16384\n"
        "global_load_lds_dwordx4 v239, s[60:61] offset:1024\n"
        "v_mfma_f32_16x16x32_bf16 a[132:135], v[68:71], v[20:23], 0\n"
        "ds_read_b64_tr_b16 v[58:59], v231 offset:16384\n"
        "s_add_u32 s92, s8, s91\n"
        "s_addc_u32 s93, s9, 0\n"
        "v_mfma_f32_16x16x32_bf16 a[132:135], v[36:39], v[16:19], a[132:135]\n"
        "ds_read_b64_tr_b16 v[88:89], v230 offset:24576\n"
        "s_lshl_b32 s91, s45, 7\n"
        "s_add_u32 s81, s91, s90\n"
        "v_mfma_f32_16x16x32_bf16 a[136:139], v[40:43], v[16:19], 0\n"
        "ds_read_b64_tr_b16 v[90:91], v231 offset:24576\n"
        "s_lshl_b32 s91, s81, 11\n"
        "s_lshl_b32 s90, s41, 7\n"
        "v_mfma_f32_16x16x32_bf16 a[136:139], v[72:75], v[20:23], a[136:139]\n"
        "ds_read_b64_tr_b16 v[60:61], v232 offset:16384\n"
        "global_load_lds_dwordx4 v240, s[60:61] offset:2048\n"
        "s_lshl2_add_u32 s90, s42, s90\n"
        "v_mfma_f32_16x16x32_bf16 a[168:171], v[72:75], v[28:31], 0\n"
        "ds_read_b64_tr_b16 v[62:63], v233 offset:16384\n"
        "s_add_u32 s90, s90, s40\n"
        "s_lshl_b32 s90, s90, 1\n"
        "v_mfma_f32_16x16x32_bf16 a[168:171], v[40:43], v[24:27], a[168:171]\n"
        "ds_read_b64_tr_b16 v[92:93], v232 offset:24576\n"
        "s_lshl_b32 s81, s81, 7\n"
        "v_mov_b32 v172, s90\n"
        "v_mfma_f32_16x16x32_bf16 a[172:175], v[44:47], v[24:27], 0\n"
        "ds_read_b64_tr_b16 v[94:95], v233 offset:24576\n"
        "v_lshlrev_b32 v198, 11, v172\n"
        "v_mfma_f32_16x16x32_bf16 a[172:175], v[76:79], v[28:31], a[172:175]\n"
        "global_load_lds_dwordx4 v241, s[60:61] offset:3072\n"
        "s_add_u32 m0, s73, 0x24000\n"
        "v_mfma_f32_16x16x32_bf16 a[140:143], v[76:79], v[20:23], 0\n"
        "s_add_u32 s60, s60, 0x80000\n"
        "s_addc_u32 s61, s61, 0\n"
        "s_waitcnt lgkmcnt(6)\n"
        "v_mfma_f32_16x16x32_bf16 a[140:143], v[44:47], v[16:19], a[140:143]\n"
        "s_add_u32 s76, s38, 0x3001000\n"
        "s_addc_u32 s77, s39, 0\n"
        "s_waitcnt lgkmcnt(5)\n"
        "v_mfma_f32_16x16x32_bf16 a[16:19], v[0:3], v[48:51], 0\n"
        "s_waitcnt lgkmcnt(4)\n"
        "v_mfma_f32_16x16x32_bf16 a[16:19], v[4:7], v[80:83], a[16:19]\n"
        "global_load_lds_dwordx4 v238, s[62:63] offset:0\n"
        "v_mfma_f32_16x16x32_bf16 a[48:51], v[12:15], v[80:83], 0\n"
        "s_waitcnt lgkmcnt(2)\n"
        "v_mfma_f32_16x16x32_bf16 a[48:51], v[8:11], v[48:51], a[48:51]\n"
        "s_waitcnt lgkmcnt(1)\n"
        "v_mfma_f32_16x16x32_bf16 a[52:55], v[8:11], v[52:55], 0\n"
        "s_waitcnt lgkmcnt(0)\n"
        "v_mfma_f32_16x16x32_bf16 a[52:55], v[12:15], v[84:87], a[52:55]\n"
        "global_load_lds_dwordx4 v239, s[62:63] offset:1024\n"
        "v_mfma_f32_16x16x32_bf16 a[20:23], v[4:7], v[84:87], 0\n"
        "v_mfma_f32_16x16x32_bf16 a[20:23], v[0:3], v[52:55], a[20:23]\n"
        "v_mfma_f32_16x16x32_bf16 a[24:27], v[0:3], v[56:59], 0\n"
        "v_mfma_f32_16x16x32_bf16 a[24:27], v[4:7], v[88:91], a[24:27]\n"
        "global_load_lds_dwordx4 v240, s[62:63] offset:2048\n"
        "v_mfma_f32_16x16x32_bf16 a[56:59], v[12:15], v[88:91], 0\n"
        "v_mfma_f32_16x16x32_bf16 a[56:59], v[8:11], v[56:59], a[56:59]\n"
        "v_mfma_f32_16x16x32_bf16 a[60:63], v[8:11], v[60:63], 0\n"
        "v_mfma_f32_16x16x32_bf16 a[60:63], v[12:15], v[92:95], a[60:63]\n"
        "global_load_lds_dwordx4 v241, s[62:63] offset:3072\n"
        "s_add_u32 m0, s72, 0x0\n"
        "v_mfma_f32_16x16x32_bf16 a[28:31], v[4:7], v[92:95], 0\n"
        "s_add_u32 s62, s62, 0x80000\n"
        "s_addc_u32 s63, s63, 0\n"
        "v_mfma_f32_16x16x32_bf16 a[28:31], v[0:3], v[60:63], a[28:31]\n"
        "s_waitcnt vmcnt(26)\n"
        "v_mfma_f32_16x16x32_bf16 a[144:147], v[16:19], v[48:51], 0\n"
        "s_barrier\n"
        "v_mfma_f32_16x16x32_bf16 a[144:147], v[20:23], v[80:83], a[144:147]\n"
        "ds_read_b128 v[0:3], v224 offset:4096\n"
        "v_mfma_f32_16x16x32_bf16 a[176:179], v[28:31], v[80:83], 0\n"
        "ds_read_b128 v[4:7], v225 offset:4096\n"
        "v_mfma_f32_16x16x32_bf16 a[176:179], v[24:27], v[48:51], a[176:179]\n"
        "ds_read_b128 v[8:11], v224 offset:6144\n"
        "v_mfma_f32_16x16x32_bf16 a[180:183], v[24:27], v[52:55], 0\n"
        "ds_read_b128 v[12:15], v225 offset:6144\n"
        "v_mfma_f32_16x16x32_bf16 a[180:183], v[28:31], v[84:87], a[180:183]\n"
        "v_mfma_f32_16x16x32_bf16 a[148:151], v[20:23], v[84:87], 0\n"
        "v_mfma_f32_16x16x32_bf16 a[148:151], v[16:19], v[52:55], a[148:151]\n"
        "v_mfma_f32_16x16x32_bf16 a[152:155], v[16:19], v[56:59], 0\n"
        "v_mfma_f32_16x16x32_bf16 a[152:155], v[20:23], v[88:91], a[152:155]\n"
        "s_waitcnt lgkmcnt(3)\n"
        "v_mfma_f32_16x16x32_bf16 a[184:187], v[28:31], v[88:91], 0\n"
        "s_waitcnt lgkmcnt(2)\n"
        "v_mfma_f32_16x16x32_bf16 a[184:187], v[24:27], v[56:59], a[184:187]\n"
        "s_waitcnt lgkmcnt(1)\n"
        "v_mfma_f32_16x16x32_bf16 a[188:191], v[24:27], v[60:63], 0\n"
        "ds_read_b128 v[24:27], v224 offset:14336\n"
        "s_waitcnt lgkmcnt(1)\n"
        "v_mfma_f32_16x16x32_bf16 a[188:191], v[28:31], v[92:95], a[188:191]\n"
        "ds_read_b128 v[28:31], v225 offset:14336\n"
        "v_mfma_f32_16x16x32_bf16 a[156:159], v[20:23], v[92:95], 0\n"
        "ds_read_b128 v[20:23], v225 offset:12288\n"
        "v_mfma_f32_16x16x32_bf16 a[156:159], v[16:19], v[60:63], a[156:159]\n"
        "ds_read_b128 v[16:19], v224 offset:12288\n"
        "v_mfma_f32_16x16x32_bf16 a[64:67], v[32:35], v[0:3], 0\n"
        "v_mfma_f32_16x16x32_bf16 a[64:67], v[64:67], v[4:7], a[64:67]\n"
        "v_mfma_f32_16x16x32_bf16 a[96:99], v[64:67], v[12:15], 0\n"
        "v_mfma_f32_16x16x32_bf16 a[96:99], v[32:35], v[8:11], a[96:99]\n"
        "v_mfma_f32_16x16x32_bf16 a[100:103], v[36:39], v[8:11], 0\n"
        "v_mfma_f32_16x16x32_bf16 a[100:103], v[68:71], v[12:15], a[100:103]\n"
        "v_mfma_f32_16x16x32_bf16 a[68:71], v[68:71], v[4:7], 0\n"
        "v_mfma_f32_16x16x32_bf16 a[68:71], v[36:39], v[0:3], a[68:71]\n"
        "v_mfma_f32_16x16x32_bf16 a[72:75], v[40:43], v[0:3], 0\n"
        "v_mfma_f32_16x16x32_bf16 a[72:75], v[72:75], v[4:7], a[72:75]\n"
        "v_mfma_f32_16x16x32_bf16 a[104:107], v[72:75], v[12:15], 0\n"
        "v_mfma_f32_16x16x32_bf16 a[104:107], v[40:43], v[8:11], a[104:107]\n"
        "v_mfma_f32_16x16x32_bf16 a[108:111], v[44:47], v[8:11], 0\n"
        "v_mfma_f32_16x16x32_bf16 a[108:111], v[76:79], v[12:15], a[108:111]\n"
        "v_mfma_f32_16x16x32_bf16 a[76:79], v[76:79], v[4:7], 0\n"
        "v_mfma_f32_16x16x32_bf16 a[76:79], v[44:47], v[0:3], a[76:79]\n"
        "v_mfma_f32_16x16x32_bf16 a[80:83], v[0:3], v[48:51], 0\n"
        "v_mfma_f32_16x16x32_bf16 a[80:83], v[4:7], v[80:83], a[80:83]\n"
        "v_mfma_f32_16x16x32_bf16 a[112:115], v[12:15], v[80:83], 0\n"
        "v_mfma_f32_16x16x32_bf16 a[112:115], v[8:11], v[48:51], a[112:115]\n"
        "v_mfma_f32_16x16x32_bf16 a[116:119], v[8:11], v[52:55], 0\n"
        "v_mfma_f32_16x16x32_bf16 a[116:119], v[12:15], v[84:87], a[116:119]\n"
        "s_waitcnt lgkmcnt(0)\n"
        "v_mfma_f32_16x16x32_bf16 a[84:87], v[4:7], v[84:87], 0\n"
        "v_mfma_f32_16x16x32_bf16 a[84:87], v[0:3], v[52:55], a[84:87]\n"
        "global_load_dwordx4 v[134:137], v168, s[24:25] offset:0\n"
        "v_mfma_f32_16x16x32_bf16 a[88:91], v[0:3], v[56:59], 0\n"
        "v_mfma_f32_16x16x32_bf16 a[88:91], v[4:7], v[88:91], a[88:91]\n"
        "v_mfma_f32_16x16x32_bf16 a[120:123], v[12:15], v[88:91], 0\n"
        "v_mfma_f32_16x16x32_bf16 a[120:123], v[8:11], v[56:59], a[120:123]\n"
        "s_waitcnt vmcnt(9)\n"
        "v_mfma_f32_16x16x32_bf16 a[124:127], v[8:11], v[60:63], 0\n"
        "s_barrier\n"
        "v_mfma_f32_16x16x32_bf16 a[124:127], v[12:15], v[92:95], a[124:127]\n"
        "ds_read_b128 v[8:11], v224 offset:34816\n"
        "global_load_lds_dwordx4 v234, s[56:57] offset:0\n"
        "v_mfma_f32_16x16x32_bf16 a[92:95], v[4:7], v[92:95], 0\n"
        "ds_read_b128 v[4:7], v225 offset:32768\n"
        "v_mfma_f32_16x16x32_bf16 a[92:95], v[0:3], v[60:63], a[92:95]\n"
        "ds_read_b128 v[0:3], v224 offset:32768\n"
        "s_add_u32 s82, s12, 0x0\n"
        "s_addc_u32 s83, s13, 0\n"
        "v_mfma_f32_16x16x32_bf16 a[192:195], v[32:35], v[16:19], 0\n"
        "ds_read_b128 v[12:15], v225 offset:34816\n"
        "v_mfma_f32_16x16x32_bf16 a[192:195], v[64:67], v[20:23], a[192:195]\n"
        "global_load_lds_dwordx4 v235, s[56:57] offset:1024\n"
        "v_mfma_f32_16x16x32_bf16 a[224:227], v[64:67], v[28:31], 0\n"
        "ds_read_b64_tr_b16 v[64:65], v226 offset:40960\n"
        "v_mfma_f32_16x16x32_bf16 a[224:227], v[32:35], v[24:27], a[224:227]\n"
        "ds_read_b64_tr_b16 v[32:33], v226 offset:32768\n"
        "v_mfma_f32_16x16x32_bf16 a[228:231], v[36:39], v[24:27], 0\n"
        "ds_read_b64_tr_b16 v[34:35], v227 offset:32768\n"
        "v_mfma_f32_16x16x32_bf16 a[228:231], v[68:71], v[28:31], a[228:231]\n"
        "ds_read_b64_tr_b16 v[66:67], v227 offset:40960\n"
        "global_load_dwordx4 v[138:141], v168, s[24:25] offset:16\n"
        "v_mfma_f32_16x16x32_bf16 a[196:199], v[68:71], v[20:23], 0\n"
        "ds_read_b64_tr_b16 v[68:69], v228 offset:40960\n"
        "v_mfma_f32_16x16x32_bf16 a[196:199], v[36:39], v[16:19], a[196:199]\n"
        "ds_read_b64_tr_b16 v[36:37], v228 offset:32768\n"
        "v_mfma_f32_16x16x32_bf16 a[200:203], v[40:43], v[16:19], 0\n"
        "ds_read_b64_tr_b16 v[38:39], v229 offset:32768\n"
        "v_mfma_f32_16x16x32_bf16 a[200:203], v[72:75], v[20:23], a[200:203]\n"
        "ds_read_b64_tr_b16 v[70:71], v229 offset:40960\n"
        "global_load_lds_dwordx4 v236, s[56:57] offset:2048\n"
        "v_mfma_f32_16x16x32_bf16 a[232:235], v[72:75], v[28:31], 0\n"
        "ds_read_b64_tr_b16 v[72:73], v230 offset:40960\n"
        "v_mfma_f32_16x16x32_bf16 a[232:235], v[40:43], v[24:27], a[232:235]\n"
        "ds_read_b64_tr_b16 v[40:41], v230 offset:32768\n"
        "v_mfma_f32_16x16x32_bf16 a[236:239], v[44:47], v[24:27], 0\n"
        "ds_read_b64_tr_b16 v[42:43], v231 offset:32768\n"
        "v_mfma_f32_16x16x32_bf16 a[236:239], v[76:79], v[28:31], a[236:239]\n"
        "ds_read_b64_tr_b16 v[74:75], v231 offset:40960\n"
        "global_load_lds_dwordx4 v237, s[56:57] offset:3072\n"
        "s_add_u32 m0, s72, 0x1000\n"
        "v_mfma_f32_16x16x32_bf16 a[204:207], v[76:79], v[20:23], 0\n"
        "ds_read_b64_tr_b16 v[76:77], v232 offset:40960\n"
        "s_add_u32 s56, s56, 0x80\n"
        "s_addc_u32 s57, s57, 0\n"
        "v_mfma_f32_16x16x32_bf16 a[204:207], v[44:47], v[16:19], a[204:207]\n"
        "ds_read_b64_tr_b16 v[44:45], v232 offset:32768\n"
        "v_mfma_f32_16x16x32_bf16 a[208:211], v[16:19], v[48:51], 0\n"
        "ds_read_b64_tr_b16 v[46:47], v233 offset:32768\n"
        "v_mfma_f32_16x16x32_bf16 a[208:211], v[20:23], v[80:83], a[208:211]\n"
        "ds_read_b64_tr_b16 v[78:79], v233 offset:40960\n"
        "global_load_dwordx2 v[96:97], v170, s[74:75] offset:-4096\n"
        "v_mfma_f32_16x16x32_bf16 a[240:243], v[28:31], v[80:83], 0\n"
        "ds_read_b64_tr_b16 v[80:81], v226 offset:57344\n"
        "global_load_lds_dwordx4 v234, s[58:59] offset:0\n"
        "v_mfma_f32_16x16x32_bf16 a[240:243], v[24:27], v[48:51], a[240:243]\n"
        "ds_read_b64_tr_b16 v[48:49], v226 offset:49152\n"
        "v_mfma_f32_16x16x32_bf16 a[244:247], v[24:27], v[52:55], 0\n"
        "ds_read_b64_tr_b16 v[50:51], v227 offset:49152\n"
        "v_mfma_f32_16x16x32_bf16 a[244:247], v[28:31], v[84:87], a[244:247]\n"
        "ds_read_b64_tr_b16 v[82:83], v227 offset:57344\n"
        "s_waitcnt lgkmcnt(8)\n"
        "v_mfma_f32_16x16x32_bf16 a[212:215], v[20:23], v[84:87], 0\n"
        "ds_read_b64_tr_b16 v[84:85], v228 offset:57344\n"
        "global_load_lds_dwordx4 v235, s[58:59] offset:1024\n"
        "v_mfma_f32_16x16x32_bf16 a[212:215], v[16:19], v[52:55], a[212:215]\n"
        "ds_read_b64_tr_b16 v[52:53], v228 offset:49152\n"
        "v_mfma_f32_16x16x32_bf16 a[216:219], v[16:19], v[56:59], 0\n"
        "ds_read_b64_tr_b16 v[54:55], v229 offset:49152\n"
        "v_mfma_f32_16x16x32_bf16 a[216:219], v[20:23], v[88:91], a[216:219]\n"
        "ds_read_b64_tr_b16 v[86:87], v229 offset:57344\n"
        "v_mfma_f32_16x16x32_bf16 a[248:251], v[28:31], v[88:91], 0\n"
        "ds_read_b64_tr_b16 v[88:89], v230 offset:57344\n"
        "global_load_dwordx2 v[98:99], v170, s[74:75] offset:-2048\n"
        "v_mfma_f32_16x16x32_bf16 a[248:251], v[24:27], v[56:59], a[248:251]\n"
        "ds_read_b64_tr_b16 v[56:57], v230 offset:49152\n"
        "global_load_lds_dwordx4 v236, s[58:59] offset:2048\n"
        "v_mfma_f32_16x16x32_bf16 a[252:255], v[24:27], v[60:63], 0\n"
        "ds_read_b64_tr_b16 v[58:59], v231 offset:49152\n"
        "v_mfma_f32_16x16x32_bf16 a[252:255], v[28:31], v[92:95], a[252:255]\n"
        "ds_read_b64_tr_b16 v[90:91], v231 offset:57344\n"
        "v_mfma_f32_16x16x32_bf16 a[220:223], v[20:23], v[92:95], 0\n"
        "ds_read_b64_tr_b16 v[92:93], v232 offset:57344\n"
        "v_mfma_f32_16x16x32_bf16 a[220:223], v[16:19], v[60:63], a[220:223]\n"
        "ds_read_b64_tr_b16 v[60:61], v232 offset:49152\n"
        "global_load_lds_dwordx4 v237, s[58:59] offset:3072\n"
        "s_add_u32 m0, s73, 0x10000\n"
        "v_mfma_f32_16x16x32_bf16 a[0:3], v[32:35], v[0:3], a[0:3]\n"
        "ds_read_b64_tr_b16 v[62:63], v233 offset:49152\n"
        "s_add_u32 s58, s58, 0x80\n"
        "s_addc_u32 s59, s59, 0\n"
        "v_mfma_f32_16x16x32_bf16 a[0:3], v[64:67], v[4:7], a[0:3]\n"
        "ds_read_b64_tr_b16 v[94:95], v233 offset:57344\n"
        "s_waitcnt lgkmcnt(8)\n"
        "v_mfma_f32_16x16x32_bf16 a[32:35], v[64:67], v[12:15], a[32:35]\n"
        "ds_read_b128 v[16:19], v224 offset:36864\n"
        "v_mfma_f32_16x16x32_bf16 a[32:35], v[32:35], v[8:11], a[32:35]\n"
        "ds_read_b128 v[20:23], v225 offset:36864\n"
        "global_load_dwordx2 v[100:101], v170, s[74:75] offset:0\n"
        "v_mfma_f32_16x16x32_bf16 a[36:39], v[36:39], v[8:11], a[36:39]\n"
        "ds_read_b128 v[24:27], v224 offset:38912\n"
        "global_load_lds_dwordx4 v238, s[60:61] offset:0\n"
        "v_mfma_f32_16x16x32_bf16 a[36:39], v[68:71], v[12:15], a[36:39]\n"
        "ds_read_b128 v[28:31], v225 offset:38912\n"
        "v_mfma_f32_16x16x32_bf16 a[4:7], v[68:71], v[4:7], a[4:7]\n"
        "v_mfma_f32_16x16x32_bf16 a[4:7], v[36:39], v[0:3], a[4:7]\n"
        "v_mfma_f32_16x16x32_bf16 a[8:11], v[40:43], v[0:3], a[8:11]\n"
        "global_load_lds_dwordx4 v239, s[60:61] offset:1024\n"
        "v_mfma_f32_16x16x32_bf16 a[8:11], v[72:75], v[4:7], a[8:11]\n"
        "v_mfma_f32_16x16x32_bf16 a[40:43], v[72:75], v[12:15], a[40:43]\n"
        "v_mfma_f32_16x16x32_bf16 a[40:43], v[40:43], v[8:11], a[40:43]\n"
        "v_mfma_f32_16x16x32_bf16 a[44:47], v[44:47], v[8:11], a[44:47]\n"
        "global_load_dwordx2 v[102:103], v169, s[74:75] offset:2048\n"
        "v_mfma_f32_16x16x32_bf16 a[44:47], v[76:79], v[12:15], a[44:47]\n"
        "global_load_lds_dwordx4 v240, s[60:61] offset:2048\n"
        "s_add_u32 s74, s20, 0x1800\n"
        "s_addc_u32 s75, s21, 0\n"
        "v_mfma_f32_16x16x32_bf16 a[12:15], v[76:79], v[4:7], a[12:15]\n"
        "s_waitcnt lgkmcnt(0)\n"
        "v_mfma_f32_16x16x32_bf16 a[12:15], v[44:47], v[0:3], a[12:15]\n"
        "v_mfma_f32_16x16x32_bf16 a[16:19], v[0:3], v[48:51], a[16:19]\n"
        "v_mfma_f32_16x16x32_bf16 a[16:19], v[4:7], v[80:83], a[16:19]\n"
        "global_load_lds_dwordx4 v241, s[60:61] offset:3072\n"
        "s_add_u32 m0, s73, 0x14000\n"
        "v_mfma_f32_16x16x32_bf16 a[48:51], v[12:15], v[80:83], a[48:51]\n"
        "s_add_u32 s60, s60, 0x80000\n"
        "s_addc_u32 s61, s61, 0\n"
        "v_mfma_f32_16x16x32_bf16 a[48:51], v[8:11], v[48:51], a[48:51]\n"
        "v_mfma_f32_16x16x32_bf16 a[52:55], v[8:11], v[52:55], a[52:55]\n"
        "v_mfma_f32_16x16x32_bf16 a[52:55], v[12:15], v[84:87], a[52:55]\n"
        "global_load_dwordx2 v[104:105], v169, s[74:75] offset:-4096\n"
        "v_mfma_f32_16x16x32_bf16 a[20:23], v[4:7], v[84:87], a[20:23]\n"
        "global_load_lds_dwordx4 v238, s[62:63] offset:0\n"
        "v_mfma_f32_16x16x32_bf16 a[20:23], v[0:3], v[52:55], a[20:23]\n"
        "v_mfma_f32_16x16x32_bf16 a[24:27], v[0:3], v[56:59], a[24:27]\n"
        "v_mfma_f32_16x16x32_bf16 a[24:27], v[4:7], v[88:91], a[24:27]\n"
        "v_mfma_f32_16x16x32_bf16 a[56:59], v[12:15], v[88:91], a[56:59]\n"
        "global_load_lds_dwordx4 v239, s[62:63] offset:1024\n"
        "v_mfma_f32_16x16x32_bf16 a[56:59], v[8:11], v[56:59], a[56:59]\n"
        "v_mfma_f32_16x16x32_bf16 a[60:63], v[8:11], v[60:63], a[60:63]\n"
        "ds_read_b128 v[8:11], v224 offset:43008\n"
        "v_mfma_f32_16x16x32_bf16 a[60:63], v[12:15], v[92:95], a[60:63]\n"
        "ds_read_b128 v[12:15], v225 offset:43008\n"
        "v_mfma_f32_16x16x32_bf16 a[28:31], v[4:7], v[92:95], a[28:31]\n"
        "ds_read_b128 v[4:7], v225 offset:40960\n"
        "global_load_dwordx2 v[106:107], v169, s[74:75] offset:-2048\n"
        "v_mfma_f32_16x16x32_bf16 a[28:31], v[0:3], v[60:63], a[28:31]\n"
        "ds_read_b128 v[0:3], v224 offset:40960\n"
        "global_load_lds_dwordx4 v240, s[62:63] offset:2048\n"
        "v_mfma_f32_16x16x32_bf16 a[64:67], v[32:35], v[16:19], a[64:67]\n"
        "v_mfma_f32_16x16x32_bf16 a[64:67], v[64:67], v[20:23], a[64:67]\n"
        "v_mfma_f32_16x16x32_bf16 a[96:99], v[64:67], v[28:31], a[96:99]\n"
        "v_mfma_f32_16x16x32_bf16 a[96:99], v[32:35], v[24:27], a[96:99]\n"
        "global_load_lds_dwordx4 v241, s[62:63] offset:3072\n"
        "s_add_u32 m0, s72, 0x8000\n"
        "v_mfma_f32_16x16x32_bf16 a[100:103], v[36:39], v[24:27], a[100:103]\n"
        "s_add_u32 s62, s62, 0x80000\n"
        "s_addc_u32 s63, s63, 0\n"
        "v_mfma_f32_16x16x32_bf16 a[100:103], v[68:71], v[28:31], a[100:103]\n"
        "v_mfma_f32_16x16x32_bf16 a[68:71], v[68:71], v[20:23], a[68:71]\n"
        "v_mfma_f32_16x16x32_bf16 a[68:71], v[36:39], v[16:19], a[68:71]\n"
        "global_load_dwordx2 v[108:109], v169, s[74:75] offset:0\n"
        "v_mfma_f32_16x16x32_bf16 a[72:75], v[40:43], v[16:19], a[72:75]\n"
        "global_load_dwordx2 v[110:111], v169, s[74:75] offset:2048\n"
        "s_add_u32 s74, s20, 0x3800\n"
        "s_addc_u32 s75, s21, 0\n"
        "v_mfma_f32_16x16x32_bf16 a[72:75], v[72:75], v[20:23], a[72:75]\n"
        "global_load_dwordx2 v[112:113], v169, s[74:75] offset:-4096\n"
        "v_mfma_f32_16x16x32_bf16 a[104:107], v[72:75], v[28:31], a[104:107]\n"
        "v_mfma_f32_16x16x32_bf16 a[104:107], v[40:43], v[24:27], a[104:107]\n"
        "v_mfma_f32_16x16x32_bf16 a[108:111], v[44:47], v[24:27], a[108:111]\n"
        "v_mfma_f32_16x16x32_bf16 a[108:111], v[76:79], v[28:31], a[108:111]\n"
        "v_mfma_f32_16x16x32_bf16 a[76:79], v[76:79], v[20:23], a[76:79]\n"
        "v_mfma_f32_16x16x32_bf16 a[76:79], v[44:47], v[16:19], a[76:79]\n"
        "global_load_dwordx2 v[114:115], v169, s[74:75] offset:-2048\n"
        "v_mfma_f32_16x16x32_bf16 a[80:83], v[16:19], v[48:51], a[80:83]\n"
        "v_mfma_f32_16x16x32_bf16 a[80:83], v[20:23], v[80:83], a[80:83]\n"
        "v_mfma_f32_16x16x32_bf16 a[112:115], v[28:31], v[80:83], a[112:115]\n"
        "v_mfma_f32_16x16x32_bf16 a[112:115], v[24:27], v[48:51], a[112:115]\n"
        "v_mfma_f32_16x16x32_bf16 a[116:119], v[24:27], v[52:55], a[116:119]\n"
        "v_mfma_f32_16x16x32_bf16 a[116:119], v[28:31], v[84:87], a[116:119]\n"
        "s_waitcnt lgkmcnt(0)\n"
        "v_mfma_f32_16x16x32_bf16 a[84:87], v[20:23], v[84:87], a[84:87]\n"
        "v_mfma_f32_16x16x32_bf16 a[84:87], v[16:19], v[52:55], a[84:87]\n"
        "global_load_dwordx2 v[116:117], v169, s[74:75] offset:0\n"
        "s_add_u32 s74, s22, 0x1000\n"
        "s_addc_u32 s75, s23, 0\n"
        "v_mfma_f32_16x16x32_bf16 a[88:91], v[16:19], v[56:59], a[88:91]\n"
        "v_mfma_f32_16x16x32_bf16 a[88:91], v[20:23], v[88:91], a[88:91]\n"
        "v_mfma_f32_16x16x32_bf16 a[120:123], v[28:31], v[88:91], a[120:123]\n"
        "v_mfma_f32_16x16x32_bf16 a[120:123], v[24:27], v[56:59], a[120:123]\n"
        "v_mfma_f32_16x16x32_bf16 a[124:127], v[24:27], v[60:63], a[124:127]\n"
        "ds_read_b128 v[24:27], v224 offset:47104\n"
        "v_mfma_f32_16x16x32_bf16 a[124:127], v[28:31], v[92:95], a[124:127]\n"
        "ds_read_b128 v[28:31], v225 offset:47104\n"
        "v_mfma_f32_16x16x32_bf16 a[92:95], v[20:23], v[92:95], a[92:95]\n"
        "ds_read_b128 v[20:23], v225 offset:45056\n"
        "v_mfma_f32_16x16x32_bf16 a[92:95], v[16:19], v[60:63], a[92:95]\n"
        "ds_read_b128 v[16:19], v224 offset:45056\n"
        "global_load_dwordx2 v[118:119], v169, s[74:75] offset:-4096\n"
        "v_mfma_f32_16x16x32_bf16 a[128:131], v[32:35], v[0:3], a[128:131]\n"
        "v_mfma_f32_16x16x32_bf16 a[128:131], v[64:67], v[4:7], a[128:131]\n"
        "v_mfma_f32_16x16x32_bf16 a[160:163], v[64:67], v[12:15], a[160:163]\n"
        "v_mfma_f32_16x16x32_bf16 a[160:163], v[32:35], v[8:11], a[160:163]\n"
        "v_mfma_f32_16x16x32_bf16 a[164:167], v[36:39], v[8:11], a[164:167]\n"
        "v_mfma_f32_16x16x32_bf16 a[164:167], v[68:71], v[12:15], a[164:167]\n"
        "v_mfma_f32_16x16x32_bf16 a[132:135], v[68:71], v[4:7], a[132:135]\n"
        "v_mfma_f32_16x16x32_bf16 a[132:135], v[36:39], v[0:3], a[132:135]\n"
        "global_load_dwordx2 v[120:121], v169, s[74:75] offset:-2048\n"
        "v_mfma_f32_16x16x32_bf16 a[136:139], v[40:43], v[0:3], a[136:139]\n"
        "v_mfma_f32_16x16x32_bf16 a[136:139], v[72:75], v[4:7], a[136:139]\n"
        "v_mfma_f32_16x16x32_bf16 a[168:171], v[72:75], v[12:15], a[168:171]\n"
        "v_mfma_f32_16x16x32_bf16 a[168:171], v[40:43], v[8:11], a[168:171]\n"
        "v_mfma_f32_16x16x32_bf16 a[172:175], v[44:47], v[8:11], a[172:175]\n"
        "v_mfma_f32_16x16x32_bf16 a[172:175], v[76:79], v[12:15], a[172:175]\n"
        "v_mfma_f32_16x16x32_bf16 a[140:143], v[76:79], v[4:7], a[140:143]\n"
        "v_mfma_f32_16x16x32_bf16 a[140:143], v[44:47], v[0:3], a[140:143]\n"
        "global_load_dwordx2 v[122:123], v169, s[74:75] offset:0\n"
        "v_mfma_f32_16x16x32_bf16 a[144:147], v[0:3], v[48:51], a[144:147]\n"
        "v_mfma_f32_16x16x32_bf16 a[144:147], v[4:7], v[80:83], a[144:147]\n"
        "v_mfma_f32_16x16x32_bf16 a[176:179], v[12:15], v[80:83], a[176:179]\n"
        "v_mfma_f32_16x16x32_bf16 a[176:179], v[8:11], v[48:51], a[176:179]\n"
        "v_mfma_f32_16x16x32_bf16 a[180:183], v[8:11], v[52:55], a[180:183]\n"
        "v_mfma_f32_16x16x32_bf16 a[180:183], v[12:15], v[84:87], a[180:183]\n"
        "s_waitcnt lgkmcnt(0)\n"
        "v_mfma_f32_16x16x32_bf16 a[148:151], v[4:7], v[84:87], a[148:151]\n"
        "v_mfma_f32_16x16x32_bf16 a[148:151], v[0:3], v[52:55], a[148:151]\n"
        "global_load_dwordx2 v[124:125], v169, s[74:75] offset:2048\n"
        "s_add_u32 s74, s22, 0x3000\n"
        "s_addc_u32 s75, s23, 0\n"
        "v_mfma_f32_16x16x32_bf16 a[152:155], v[0:3], v[56:59], a[152:155]\n"
        "v_mfma_f32_16x16x32_bf16 a[152:155], v[4:7], v[88:91], a[152:155]\n"
        "v_mfma_f32_16x16x32_bf16 a[184:187], v[12:15], v[88:91], a[184:187]\n"
        "v_mfma_f32_16x16x32_bf16 a[184:187], v[8:11], v[56:59], a[184:187]\n"
        "s_waitcnt vmcnt(21)\n"
        "v_mfma_f32_16x16x32_bf16 a[188:191], v[8:11], v[60:63], a[188:191]\n"
        "s_barrier\n"
        "v_mfma_f32_16x16x32_bf16 a[188:191], v[12:15], v[92:95], a[188:191]\n"
        "ds_read_b128 v[8:11], v224 offset:2048\n"
        "global_load_lds_dwordx4 v234, s[56:57] offset:0\n"
        "v_mfma_f32_16x16x32_bf16 a[156:159], v[4:7], v[92:95], a[156:159]\n"
        "ds_read_b128 v[4:7], v225 offset:0\n"
        "v_mfma_f32_16x16x32_bf16 a[156:159], v[0:3], v[60:63], a[156:159]\n"
        "ds_read_b128 v[0:3], v224 offset:0\n"
        "v_mfma_f32_16x16x32_bf16 a[192:195], v[32:35], v[16:19], a[192:195]\n"
        "ds_read_b128 v[12:15], v225 offset:2048\n"
        "v_mfma_f32_16x16x32_bf16 a[192:195], v[64:67], v[20:23], a[192:195]\n"
        "global_load_lds_dwordx4 v235, s[56:57] offset:1024\n"
        "v_mfma_f32_16x16x32_bf16 a[224:227], v[64:67], v[28:31], a[224:227]\n"
        "ds_read_b64_tr_b16 v[64:65], v216 offset:8192\n"
        "v_mfma_f32_16x16x32_bf16 a[224:227], v[32:35], v[24:27], a[224:227]\n"
        "ds_read_b64_tr_b16 v[32:33], v216 offset:0\n"
        "v_mfma_f32_16x16x32_bf16 a[228:231], v[36:39], v[24:27], a[228:231]\n"
        "ds_read_b64_tr_b16 v[34:35], v217 offset:0\n"
        "v_mfma_f32_16x16x32_bf16 a[228:231], v[68:71], v[28:31], a[228:231]\n"
        "ds_read_b64_tr_b16 v[66:67], v217 offset:8192\n"
        "global_load_dwordx2 v[126:127], v169, s[74:75] offset:-4096\n"
        "v_mfma_f32_16x16x32_bf16 a[196:199], v[68:71], v[20:23], a[196:199]\n"
        "ds_read_b64_tr_b16 v[68:69], v218 offset:8192\n"
        "global_load_lds_dwordx4 v236, s[56:57] offset:2048\n"
        "v_mfma_f32_16x16x32_bf16 a[196:199], v[36:39], v[16:19], a[196:199]\n"
        "ds_read_b64_tr_b16 v[36:37], v218 offset:0\n"
        "v_mfma_f32_16x16x32_bf16 a[200:203], v[40:43], v[16:19], a[200:203]\n"
        "ds_read_b64_tr_b16 v[38:39], v219 offset:0\n"
        "v_mfma_f32_16x16x32_bf16 a[200:203], v[72:75], v[20:23], a[200:203]\n"
        "ds_read_b64_tr_b16 v[70:71], v219 offset:8192\n"
        "v_mfma_f32_16x16x32_bf16 a[232:235], v[72:75], v[28:31], a[232:235]\n"
        "ds_read_b64_tr_b16 v[72:73], v220 offset:8192\n"
        "global_load_lds_dwordx4 v237, s[56:57] offset:3072\n"
        "s_add_u32 m0, s72, 0x9000\n"
        "v_mfma_f32_16x16x32_bf16 a[232:235], v[40:43], v[24:27], a[232:235]\n"
        "ds_read_b64_tr_b16 v[40:41], v220 offset:0\n"
        "s_add_u32 s56, s56, 0x80\n"
        "s_addc_u32 s57, s57, 0\n"
        "v_mfma_f32_16x16x32_bf16 a[236:239], v[44:47], v[24:27], a[236:239]\n"
        "ds_read_b64_tr_b16 v[42:43], v221 offset:0\n"
        "v_mfma_f32_16x16x32_bf16 a[236:239], v[76:79], v[28:31], a[236:239]\n"
        "ds_read_b64_tr_b16 v[74:75], v221 offset:8192\n"
        "v_mfma_f32_16x16x32_bf16 a[204:207], v[76:79], v[20:23], a[204:207]\n"
        "ds_read_b64_tr_b16 v[76:77], v222 offset:8192\n"
        "global_load_dwordx2 v[128:129], v169, s[74:75] offset:-2048\n"
        "v_mfma_f32_16x16x32_bf16 a[204:207], v[44:47], v[16:19], a[204:207]\n"
        "ds_read_b64_tr_b16 v[44:45], v222 offset:0\n"
        "global_load_lds_dwordx4 v234, s[58:59] offset:0\n"
        "v_mfma_f32_16x16x32_bf16 a[208:211], v[16:19], v[48:51], a[208:211]\n"
        "ds_read_b64_tr_b16 v[46:47], v223 offset:0\n"
        "v_mfma_f32_16x16x32_bf16 a[208:211], v[20:23], v[80:83], a[208:211]\n"
        "ds_read_b64_tr_b16 v[78:79], v223 offset:8192\n"
        "v_mfma_f32_16x16x32_bf16 a[240:243], v[28:31], v[80:83], a[240:243]\n"
        "ds_read_b64_tr_b16 v[80:81], v216 offset:24576\n"
        "v_mfma_f32_16x16x32_bf16 a[240:243], v[24:27], v[48:51], a[240:243]\n"
        "ds_read_b64_tr_b16 v[48:49], v216 offset:16384\n"
        "global_load_lds_dwordx4 v235, s[58:59] offset:1024\n"
        "v_mfma_f32_16x16x32_bf16 a[244:247], v[24:27], v[52:55], a[244:247]\n"
        "ds_read_b64_tr_b16 v[50:51], v217 offset:16384\n"
        "v_lshlrev_b32 v185, 16, v134\n"
        "v_mfma_f32_16x16x32_bf16 a[244:247], v[28:31], v[84:87], a[244:247]\n"
        "ds_read_b64_tr_b16 v[82:83], v217 offset:24576\n"
        "v_and_b32 v186, 0xffff0000, v134\n"
        "v_mfma_f32_16x16x32_bf16 a[212:215], v[20:23], v[84:87], a[212:215]\n"
        "ds_read_b64_tr_b16 v[84:85], v218 offset:24576\n"
        "v_lshlrev_b32 v187, 16, v135\n"
        "v_mfma_f32_16x16x32_bf16 a[212:215], v[16:19], v[52:55], a[212:215]\n"
        "ds_read_b64_tr_b16 v[52:53], v218 offset:16384\n"
        "global_load_dwordx2 v[130:131], v169, s[74:75] offset:0\n"
        "v_mfma_f32_16x16x32_bf16 a[216:219], v[16:19], v[56:59], a[216:219]\n"
        "ds_read_b64_tr_b16 v[54:55], v219 offset:16384\n"
        "global_load_lds_dwordx4 v236, s[58:59] offset:2048\n"
        "v_mfma_f32_16x16x32_bf16 a[216:219], v[20:23], v[88:91], a[216:219]\n"
        "ds_read_b64_tr_b16 v[86:87], v219 offset:24576\n"
        "v_and_b32 v188, 0xffff0000, v135\n"
        "v_mfma_f32_16x16x32_bf16 a[248:251], v[28:31], v[88:91], a[248:251]\n"
        "ds_read_b64_tr_b16 v[88:89], v220 offset:24576\n"
        "s_waitcnt lgkmcnt(8)\n"
        "v_mfma_f32_16x16x32_bf16 a[248:251], v[24:27], v[56:59], a[248:251]\n"
        "LHA1_%=:\n"
        "s_cbranch_execz LHA5_%=\n"
        "ds_read_b64_tr_b16 v[56:57], v220 offset:16384\n"
        "v_mfma_f32_16x16x32_bf16 a[252:255], v[24:27], v[60:63], a[252:255]\n"
        "ds_read_b64_tr_b16 v[58:59], v221 offset:16384\n"
        "global_load_lds_dwordx4 v237, s[58:59] offset:3072\n"
        "s_add_u32 m0, s73, 0x18000\n"
        "v_mfma_f32_16x16x32_bf16 a[252:255], v[28:31], v[92:95], a[252:255]\n"
        "ds_read_b64_tr_b16 v[90:91], v221 offset:24576\n"
        "s_add_u32 s58, s58, 0x80\n"
        "s_addc_u32 s59, s59, 0\n"
        "v_mfma_f32_16x16x32_bf16 a[220:223], v[20:23], v[92:95], a[220:223]\n"
        "ds_read_b64_tr_b16 v[92:93], v222 offset:24576\n"
        "v_mfma_f32_16x16x32_bf16 a[220:223], v[16:19], v[60:63], a[220:223]\n"
        "ds_read_b64_tr_b16 v[60:61], v222 offset:16384\n"
        "v_mfma_f32_16x16x32_bf16 a[0:3], v[32:35], v[0:3], a[0:3]\n"
        "ds_read_b64_tr_b16 v[62:63], v223 offset:16384\n"
        "global_load_dwordx2 v[132:133], v169, s[74:75] offset:2048\n"
        "v_mfma_f32_16x16x32_bf16 a[0:3], v[64:67], v[4:7], a[0:3]\n"
        "ds_read_b64_tr_b16 v[94:95], v223 offset:24576\n"
        "global_load_lds_dwordx4 v238, s[60:61] offset:0\n"
        "v_mfma_f32_16x16x32_bf16 a[32:35], v[64:67], v[12:15], a[32:35]\n"
        "ds_read_b128 v[16:19], v224 offset:4096\n"
        "v_mfma_f32_16x16x32_bf16 a[32:35], v[32:35], v[8:11], a[32:35]\n"
        "ds_read_b128 v[20:23], v225 offset:4096\n"
        "v_mfma_f32_16x16x32_bf16 a[36:39], v[36:39], v[8:11], a[36:39]\n"
        "ds_read_b128 v[24:27], v224 offset:6144\n"
        "v_mfma_f32_16x16x32_bf16 a[36:39], v[68:71], v[12:15], a[36:39]\n"
        "ds_read_b128 v[28:31], v225 offset:6144\n"
        "global_load_lds_dwordx4 v239, s[60:61] offset:1024\n"
        "v_mfma_f32_16x16x32_bf16 a[4:7], v[68:71], v[4:7], a[4:7]\n"
        "s_waitcnt lgkmcnt(7)\n"
        "v_mfma_f32_16x16x32_bf16 a[4:7], v[36:39], v[0:3], a[4:7]\n"
        "v_mfma_f32_16x16x32_bf16 a[8:11], v[40:43], v[0:3], a[8:11]\n"
        "v_mfma_f32_16x16x32_bf16 a[8:11], v[72:75], v[4:7], a[8:11]\n"
        "global_load_lds_dwordx4 v240, s[60:61] offset:2048\n"
        "v_mfma_f32_16x16x32_bf16 a[40:43], v[72:75], v[12:15], a[40:43]\n"
        "v_mfma_f32_16x16x32_bf16 a[40:43], v[40:43], v[8:11], a[40:43]\n"
        "v_mfma_f32_16x16x32_bf16 a[44:47], v[44:47], v[8:11], a[44:47]\n"
        "v_mfma_f32_16x16x32_bf16 a[44:47], v[76:79], v[12:15], a[44:47]\n"
        "global_load_lds_dwordx4 v241, s[60:61] offset:3072\n"
        "s_add_u32 m0, s73, 0x1c000\n"
        "v_mfma_f32_16x16x32_bf16 a[12:15], v[76:79], v[4:7], a[12:15]\n"
        "s_add_u32 s60, s60, 0x80000\n"
        "s_addc_u32 s61, s61, 0\n"
        "v_cndmask_b32 v96, v96, 0, s[78:79]\n"
        "v_mfma_f32_16x16x32_bf16 a[12:15], v[44:47], v[0:3], a[12:15]\n"
        "v_cndmask_b32 v97, v97, 0, s[78:79]\n"
        "v_lshlrev_b32 v174, 16, v96\n"
        "v_mfma_f32_16x16x32_bf16 a[16:19], v[0:3], v[48:51], a[16:19]\n"
        "v_mfma_f32_16x16x32_bf16 a[16:19], v[4:7], v[80:83], a[16:19]\n"
        "global_load_lds_dwordx4 v238, s[62:63] offset:0\n"
        "v_mfma_f32_16x16x32_bf16 a[48:51], v[12:15], v[80:83], a[48:51]\n"
        "s_waitcnt lgkmcnt(0)\n"
        "v_mfma_f32_16x16x32_bf16 a[48:51], v[8:11], v[48:51], a[48:51]\n"
        "v_mfma_f32_16x16x32_bf16 a[52:55], v[8:11], v[52:55], a[52:55]\n"
        "v_mfma_f32_16x16x32_bf16 a[52:55], v[12:15], v[84:87], a[52:55]\n"
        "global_load_lds_dwordx4 v239, s[62:63] offset:1024\n"
        "v_mfma_f32_16x16x32_bf16 a[20:23], v[4:7], v[84:87], a[20:23]\n"
        "v_cndmask_b32 v98, v98, 0, s[78:79]\n"
        "v_mfma_f32_16x16x32_bf16 a[20:23], v[0:3], v[52:55], a[20:23]\n"
        "v_cndmask_b32 v99, v99, 0, s[78:79]\n"
        "v_lshlrev_b32 v175, 16, v98\n"
        "v_mfma_f32_16x16x32_bf16 a[24:27], v[0:3], v[56:59], a[24:27]\n"
        "v_mul_f32 v189, v174, v188\n"
        "v_mul_f32 v191, v175, v188\n"
        "v_mfma_f32_16x16x32_bf16 a[24:27], v[4:7], v[88:91], a[24:27]\n"
        "global_load_lds_dwordx4 v240, s[62:63] offset:2048\n"
        "v_mfma_f32_16x16x32_bf16 a[56:59], v[12:15], v[88:91], a[56:59]\n"
        "v_cvt_pk_bf16_f32 v189, 0, v189\n"
        "v_cvt_pk_bf16_f32 v191, 0, v191\n"
        "v_mfma_f32_16x16x32_bf16 a[56:59], v[8:11], v[56:59], a[56:59]\n"
        "v_mul_f32 v190, v175, v187\n"
        "v_cvt_pk_bf16_f32 v190, 0, v190\n"
        "v_mfma_f32_16x16x32_bf16 a[60:63], v[8:11], v[60:63], a[60:63]\n"
        "ds_read_b128 v[8:11], v224 offset:10240\n"
        "v_add_f32 v189, v189, v190\n"
        "v_mfma_f32_16x16x32_bf16 a[60:63], v[12:15], v[92:95], a[60:63]\n"
        "ds_read_b128 v[12:15], v225 offset:10240\n"
        "global_load_lds_dwordx4 v241, s[62:63] offset:3072\n"
        "s_add_u32 m0, s72, 0x0\n"
        "v_mfma_f32_16x16x32_bf16 a[28:31], v[4:7], v[92:95], a[28:31]\n"
        "ds_read_b128 v[4:7], v225 offset:8192\n"
        "s_add_u32 s62, s62, 0x80000\n"
        "s_addc_u32 s63, s63, 0\n"
        "v_mfma_f32_16x16x32_bf16 a[28:31], v[0:3], v[60:63], a[28:31]\n"
        "ds_read_b128 v[0:3], v224 offset:8192\n"
        "v_cvt_pk_bf16_f32 v189, 0, v189\n"
        "v_mfma_f32_16x16x32_bf16 a[64:67], v[32:35], v[16:19], a[64:67]\n"
        "s_waitcnt vmcnt(40)\n"
        "v_cndmask_b32 v100, v100, 0, s[78:79]\n"
        "v_cndmask_b32 v101, v101, 0, s[78:79]\n"
        "v_mfma_f32_16x16x32_bf16 a[64:67], v[64:67], v[20:23], a[64:67]\n"
        "v_lshlrev_b32 v176, 16, v100\n"
        "v_mul_f32 v192, v176, v187\n"
        "v_mfma_f32_16x16x32_bf16 a[96:99], v[64:67], v[28:31], a[96:99]\n"
        "v_cvt_pk_bf16_f32 v192, 0, v192\n"
        "v_add_f32 v191, v191, v192\n"
        "v_mfma_f32_16x16x32_bf16 a[96:99], v[32:35], v[24:27], a[96:99]\n"
        "v_cvt_pk_bf16_f32 v191, 0, v191\n"
        "v_mul_f32 v190, v176, v186\n"
        "v_mfma_f32_16x16x32_bf16 a[100:103], v[36:39], v[24:27], a[100:103]\n"
        "v_cvt_pk_bf16_f32 v190, 0, v190\n"
        "v_add_f32 v189, v189, v190\n"
        "v_mfma_f32_16x16x32_bf16 a[100:103], v[68:71], v[28:31], a[100:103]\n"
        "v_cvt_pk_bf16_f32 v189, 0, v189\n"
        "v_mul_f32 v193, v176, v188\n"
        "v_mfma_f32_16x16x32_bf16 a[68:71], v[68:71], v[20:23], a[68:71]\n"
        "v_cvt_pk_bf16_f32 v193, 0, v193\n"
        "v_and_b32 v174, 0xffff0000, v96\n"
        "v_mfma_f32_16x16x32_bf16 a[68:71], v[36:39], v[16:19], a[68:71]\n"
        "global_load_dword v253, v173, s[82:83] offset:0\n"
        "s_add_u32 s82, s12, 0x100000\n"
        "s_addc_u32 s83, s13, 0\n"
        "v_mfma_f32_16x16x32_bf16 a[72:75], v[40:43], v[16:19], a[72:75]\n"
        "v_and_b32 v175, 0xffff0000, v98\n"
        "v_and_b32 v176, 0xffff0000, v100\n"
        "v_mfma_f32_16x16x32_bf16 a[72:75], v[72:75], v[20:23], a[72:75]\n"
        "s_waitcnt vmcnt(38) lgkmcnt(0)\n"
        "v_lshlrev_b32 v177, 16, v102\n"
        "v_mul_f32 v192, v177, v186\n"
        "v_mfma_f32_16x16x32_bf16 a[104:107], v[72:75], v[28:31], a[104:107]\n"
        "v_cvt_pk_bf16_f32 v192, 0, v192\n"
        "v_add_f32 v191, v191, v192\n"
        "v_mfma_f32_16x16x32_bf16 a[104:107], v[40:43], v[24:27], a[104:107]\n"
        "v_cvt_pk_bf16_f32 v191, 0, v191\n"
        "v_mul_f32 v190, v177, v185\n"
        "v_mfma_f32_16x16x32_bf16 a[108:111], v[44:47], v[24:27], a[108:111]\n"
        "v_cvt_pk_bf16_f32 v190, 0, v190\n"
        "v_add_f32 v189, v189, v190\n"
        "v_mfma_f32_16x16x32_bf16 a[108:111], v[76:79], v[28:31], a[108:111]\n"
        "v_cvt_pk_bf16_f32 v189, 0, v189\n"
        "v_mul_f32 v195, v177, v188\n"
        "v_mfma_f32_16x16x32_bf16 a[76:79], v[76:79], v[20:23], a[76:79]\n"
        "v_cvt_pk_bf16_f32 v195, 0, v195\n"
        "v_mul_f32 v194, v177, v187\n"
        "v_mfma_f32_16x16x32_bf16 a[76:79], v[44:47], v[16:19], a[76:79]\n"
        "v_cvt_pk_bf16_f32 v194, 0, v194\n"
        "v_add_f32 v193, v193, v194\n"
        "v_mfma_f32_16x16x32_bf16 a[80:83], v[16:19], v[48:51], a[80:83]\n"
        "v_cvt_pk_bf16_f32 v193, 0, v193\n"
        "v_and_b32 v177, 0xffff0000, v102\n"
        "v_mfma_f32_16x16x32_bf16 a[80:83], v[20:23], v[80:83], a[80:83]\n"
        "s_waitcnt vmcnt(35)\n"
        "v_lshlrev_b32 v178, 16, v104\n"
        "v_mul_f32 v192, v178, v185\n"
        "v_mfma_f32_16x16x32_bf16 a[112:115], v[28:31], v[80:83], a[112:115]\n"
        "v_cvt_pk_bf16_f32 v192, 0, v192\n"
        "v_add_f32 v191, v191, v192\n"
        "v_mfma_f32_16x16x32_bf16 a[112:115], v[24:27], v[48:51], a[112:115]\n"
        "v_cvt_pk_bf16_f32 v191, 0, v191\n"
        "v_mul_f32 v196, v178, v187\n"
        "v_mfma_f32_16x16x32_bf16 a[116:119], v[24:27], v[52:55], a[116:119]\n"
        "v_cvt_pk_bf16_f32 v196, 0, v196\n"
        "v_add_f32 v195, v195, v196\n"
        "v_mfma_f32_16x16x32_bf16 a[116:119], v[28:31], v[84:87], a[116:119]\n"
        "v_cvt_pk_bf16_f32 v195, 0, v195\n"
        "v_mul_f32 v194, v178, v186\n"
        "v_mfma_f32_16x16x32_bf16 a[84:87], v[20:23], v[84:87], a[84:87]\n"
        "v_cvt_pk_bf16_f32 v194, 0, v194\n"
        "v_add_f32 v193, v193, v194\n"
        "v_mfma_f32_16x16x32_bf16 a[84:87], v[16:19], v[52:55], a[84:87]\n"
        "v_cvt_pk_bf16_f32 v193, 0, v193\n"
        "v_mfma_f32_16x16x32_bf16 a[88:91], v[16:19], v[56:59], a[88:91]\n"
        "v_mfma_f32_16x16x32_bf16 a[88:91], v[20:23], v[88:91], a[88:91]\n"
        "v_mfma_f32_16x16x32_bf16 a[120:123], v[28:31], v[88:91], a[120:123]\n"
        "v_mfma_f32_16x16x32_bf16 a[120:123], v[24:27], v[56:59], a[120:123]\n"
        "s_waitcnt vmcnt(32)\n"
        "v_lshlrev_b32 v179, 16, v106\n"
        "v_mul_f32 v196, v179, v186\n"
        "v_mfma_f32_16x16x32_bf16 a[124:127], v[24:27], v[60:63], a[124:127]\n"
        "ds_read_b128 v[24:27], v224 offset:14336\n"
        "v_cvt_pk_bf16_f32 v196, 0, v196\n"
        "v_mfma_f32_16x16x32_bf16 a[124:127], v[28:31], v[92:95], a[124:127]\n"
        "ds_read_b128 v[28:31], v225 offset:14336\n"
        "v_add_f32 v195, v195, v196\n"
        "v_mfma_f32_16x16x32_bf16 a[92:95], v[20:23], v[92:95], a[92:95]\n"
        "ds_read_b128 v[20:23], v225 offset:12288\n"
        "v_cvt_pk_bf16_f32 v195, 0, v195\n"
        "v_mfma_f32_16x16x32_bf16 a[92:95], v[16:19], v[60:63], a[92:95]\n"
        "ds_read_b128 v[16:19], v224 offset:12288\n"
        "v_mul_f32 v194, v179, v185\n"
        "v_mfma_f32_16x16x32_bf16 a[128:131], v[32:35], v[0:3], a[128:131]\n"
        "v_cvt_pk_bf16_f32 v194, 0, v194\n"
        "v_add_f32 v193, v193, v194\n"
        "v_mfma_f32_16x16x32_bf16 a[128:131], v[64:67], v[4:7], a[128:131]\n"
        "v_cvt_pk_bf16_f32 v193, 0, v193\n"
        "v_mfma_f32_16x16x32_bf16 a[160:163], v[64:67], v[12:15], a[160:163]\n"
        "v_mfma_f32_16x16x32_bf16 a[160:163], v[32:35], v[8:11], a[160:163]\n"
        "v_mfma_f32_16x16x32_bf16 a[164:167], v[36:39], v[8:11], a[164:167]\n"
        "s_waitcnt vmcnt(29) lgkmcnt(3)\n"
        "v_lshlrev_b32 v180, 16, v108\n"
        "v_mul_f32 v196, v180, v185\n"
        "v_mfma_f32_16x16x32_bf16 a[164:167], v[68:71], v[12:15], a[164:167]\n"
        "s_waitcnt vmcnt(28) lgkmcnt(2)\n"
        "v_lshlrev_b32 v181, 16, v110\n"
        "v_cvt_pk_bf16_f32 v196, 0, v196\n"
        "v_mfma_f32_16x16x32_bf16 a[132:135], v[68:71], v[4:7], a[132:135]\n"
        "s_waitcnt vmcnt(27) lgkmcnt(1)\n"
        "v_lshlrev_b32 v182, 16, v112\n"
        "v_add_f32 v195, v195, v196\n"
        "v_mfma_f32_16x16x32_bf16 a[132:135], v[36:39], v[0:3], a[132:135]\n"
        "v_cvt_pk_bf16_f32 v195, 0, v195\n"
        "v_mfma_f32_16x16x32_bf16 a[136:139], v[40:43], v[0:3], a[136:139]\n"
        "v_mfma_f32_16x16x32_bf16 a[136:139], v[72:75], v[4:7], a[136:139]\n"
        "v_mfma_f32_16x16x32_bf16 a[168:171], v[72:75], v[12:15], a[168:171]\n"
        "v_mfma_f32_16x16x32_bf16 a[168:171], v[40:43], v[8:11], a[168:171]\n"
        "s_waitcnt vmcnt(26) lgkmcnt(0)\n"
        "v_lshlrev_b32 v183, 16, v114\n"
        "v_mfma_f32_16x16x32_bf16 a[172:175], v[44:47], v[8:11], a[172:175]\n"
        "v_mfma_f32_16x16x32_bf16 a[172:175], v[76:79], v[12:15], a[172:175]\n"
        "v_mfma_f32_16x16x32_bf16 a[140:143], v[76:79], v[4:7], a[140:143]\n"
        "v_mfma_f32_16x16x32_bf16 a[140:143], v[44:47], v[0:3], a[140:143]\n"
        "v_mfma_f32_16x16x32_bf16 a[144:147], v[0:3], v[48:51], a[144:147]\n"
        "v_mfma_f32_16x16x32_bf16 a[144:147], v[4:7], v[80:83], a[144:147]\n"
        "v_mfma_f32_16x16x32_bf16 a[176:179], v[12:15], v[80:83], a[176:179]\n"
        "v_mfma_f32_16x16x32_bf16 a[176:179], v[8:11], v[48:51], a[176:179]\n"
        "s_waitcnt vmcnt(25)\n"
        "v_lshlrev_b32 v184, 16, v116\n"
        "v_mfma_f32_16x16x32_bf16 a[180:183], v[8:11], v[52:55], a[180:183]\n"
        "v_mfma_f32_16x16x32_bf16 a[180:183], v[12:15], v[84:87], a[180:183]\n"
        "v_mfma_f32_16x16x32_bf16 a[148:151], v[4:7], v[84:87], a[148:151]\n"
        "v_mfma_f32_16x16x32_bf16 a[148:151], v[0:3], v[52:55], a[148:151]\n"
        "v_mfma_f32_16x16x32_bf16 a[152:155], v[0:3], v[56:59], a[152:155]\n"
        "v_mfma_f32_16x16x32_bf16 a[152:155], v[4:7], v[88:91], a[152:155]\n"
        "v_mfma_f32_16x16x32_bf16 a[184:187], v[12:15], v[88:91], a[184:187]\n"
        "v_mfma_f32_16x16x32_bf16 a[184:187], v[8:11], v[56:59], a[184:187]\n"
        "s_waitcnt vmcnt(10)\n"
        "v_lshlrev_b32 v190, 16, v118\n"
        "v_mfma_f32_16x16x32_bf16 a[188:191], v[8:11], v[60:63], a[188:191]\n"
        "s_barrier\n"
        "v_mfma_f32_16x16x32_bf16 a[188:191], v[12:15], v[92:95], a[188:191]\n"
        "ds_read_b128 v[8:11], v224 offset:34816\n"
        "global_load_lds_dwordx4 v234, s[56:57] offset:0\n"
        "v_mfma_f32_16x16x32_bf16 a[156:159], v[4:7], v[92:95], a[156:159]\n"
        "ds_read_b128 v[4:7], v225 offset:32768\n"
        "v_mul_f32 v189, v189, v190\n"
        "v_mfma_f32_16x16x32_bf16 a[156:159], v[0:3], v[60:63], a[156:159]\n"
        "ds_read_b128 v[0:3], v224 offset:32768\n"
        "v_mul_f32 v190, v179, v187\n"
        "v_mfma_f32_16x16x32_bf16 a[192:195], v[32:35], v[16:19], a[192:195]\n"
        "ds_read_b128 v[12:15], v225 offset:34816\n"
        "v_cvt_pk_bf16_f32 v190, 0, v190\n"
        "v_mfma_f32_16x16x32_bf16 a[192:195], v[64:67], v[20:23], a[192:195]\n"
        "global_load_lds_dwordx4 v235, s[56:57] offset:1024\n"
        "v_mfma_f32_16x16x32_bf16 a[224:227], v[64:67], v[28:31], a[224:227]\n"
        "ds_read_b64_tr_b16 v[64:65], v226 offset:8192\n"
        "v_mfma_f32_16x16x32_bf16 a[224:227], v[32:35], v[24:27], a[224:227]\n"
        "ds_read_b64_tr_b16 v[32:33], v226 offset:0\n"
        "v_lshlrev_b32 v192, 16, v120\n"
        "v_mfma_f32_16x16x32_bf16 a[228:231], v[36:39], v[24:27], a[228:231]\n"
        "ds_read_b64_tr_b16 v[34:35], v227 offset:0\n"
        "v_mul_f32 v191, v191, v192\n"
        "v_mfma_f32_16x16x32_bf16 a[228:231], v[68:71], v[28:31], a[228:231]\n"
        "ds_read_b64_tr_b16 v[66:67], v227 offset:8192\n"
        "global_load_lds_dwordx4 v236, s[56:57] offset:2048\n"
        "v_mfma_f32_16x16x32_bf16 a[196:199], v[68:71], v[20:23], a[196:199]\n"
        "ds_read_b64_tr_b16 v[68:69], v228 offset:8192\n"
        "v_cvt_pk_bf16_f32 v203, v191, v189\n"
        "v_mfma_f32_16x16x32_bf16 a[196:199], v[36:39], v[16:19], a[196:199]\n"
        "ds_read_b64_tr_b16 v[36:37], v228 offset:0\n"
        "v_mul_f32 v189, v178, v188\n"
        "v_mfma_f32_16x16x32_bf16 a[200:203], v[40:43], v[16:19], a[200:203]\n"
        "ds_read_b64_tr_b16 v[38:39], v229 offset:0\n"
        "v_mul_f32 v191, v179, v188\n"
        "v_mfma_f32_16x16x32_bf16 a[200:203], v[72:75], v[20:23], a[200:203]\n"
        "ds_read_b64_tr_b16 v[70:71], v229 offset:8192\n"
        "global_load_lds_dwordx4 v237, s[56:57] offset:3072\n"
        "s_add_u32 m0, s72, 0x1000\n"
        "v_mfma_f32_16x16x32_bf16 a[232:235], v[72:75], v[28:31], a[232:235]\n"
        "ds_read_b64_tr_b16 v[72:73], v230 offset:8192\n"
        "s_add_u32 s56, s56, 0x80\n"
        "s_addc_u32 s57, s57, 0\n"
        "v_mfma_f32_16x16x32_bf16 a[232:235], v[40:43], v[24:27], a[232:235]\n"
        "ds_read_b64_tr_b16 v[40:41], v230 offset:0\n"
        "v_lshlrev_b32 v194, 16, v122\n"
        "v_mfma_f32_16x16x32_bf16 a[236:239], v[44:47], v[24:27], a[236:239]\n"
        "ds_read_b64_tr_b16 v[42:43], v231 offset:0\n"
        "v_mul_f32 v193, v193, v194\n"
        "v_mfma_f32_16x16x32_bf16 a[236:239], v[76:79], v[28:31], a[236:239]\n"
        "ds_read_b64_tr_b16 v[74:75], v231 offset:8192\n"
        "global_load_lds_dwordx4 v234, s[58:59] offset:0\n"
        "v_mfma_f32_16x16x32_bf16 a[204:207], v[76:79], v[20:23], a[204:207]\n"
        "ds_read_b64_tr_b16 v[76:77], v232 offset:8192\n"
        "v_cvt_pk_bf16_f32 v189, 0, v189\n"
        "v_mfma_f32_16x16x32_bf16 a[204:207], v[44:47], v[16:19], a[204:207]\n"
        "ds_read_b64_tr_b16 v[44:45], v232 offset:0\n"
        "v_cvt_pk_bf16_f32 v191, 0, v191\n"
        "v_mfma_f32_16x16x32_bf16 a[208:211], v[16:19], v[48:51], a[208:211]\n"
        "ds_read_b64_tr_b16 v[46:47], v233 offset:0\n"
        "v_mul_f32 v192, v180, v187\n"
        "v_mfma_f32_16x16x32_bf16 a[208:211], v[20:23], v[80:83], a[208:211]\n"
        "ds_read_b64_tr_b16 v[78:79], v233 offset:8192\n"
        "global_load_lds_dwordx4 v235, s[58:59] offset:1024\n"
        "v_mfma_f32_16x16x32_bf16 a[240:243], v[28:31], v[80:83], a[240:243]\n"
        "ds_read_b64_tr_b16 v[80:81], v226 offset:24576\n"
        "v_cvt_pk_bf16_f32 v192, 0, v192\n"
        "v_mfma_f32_16x16x32_bf16 a[240:243], v[24:27], v[48:51], a[240:243]\n"
        "ds_read_b64_tr_b16 v[48:49], v226 offset:16384\n"
        "v_lshlrev_b32 v196, 16, v124\n"
        "v_mfma_f32_16x16x32_bf16 a[244:247], v[24:27], v[52:55], a[244:247]\n"
        "ds_read_b64_tr_b16 v[50:51], v227 offset:16384\n"
        "v_mul_f32 v195, v195, v196\n"
        "v_mfma_f32_16x16x32_bf16 a[244:247], v[28:31], v[84:87], a[244:247]\n"
        "ds_read_b64_tr_b16 v[82:83], v227 offset:24576\n"
        "global_load_lds_dwordx4 v236, s[58:59] offset:2048\n"
        "v_mfma_f32_16x16x32_bf16 a[212:215], v[20:23], v[84:87], a[212:215]\n"
        "ds_read_b64_tr_b16 v[84:85], v228 offset:24576\n"
        "v_cvt_pk_bf16_f32 v202, v195, v193\n"
        "v_mfma_f32_16x16x32_bf16 a[212:215], v[16:19], v[52:55], a[212:215]\n"
        "ds_read_b64_tr_b16 v[52:53], v228 offset:16384\n"
        "v_add_f32 v189, v189, v190\n"
        "v_mfma_f32_16x16x32_bf16 a[216:219], v[16:19], v[56:59], a[216:219]\n"
        "ds_read_b64_tr_b16 v[54:55], v229 offset:16384\n"
        "v_add_f32 v191, v191, v192\n"
        "v_mfma_f32_16x16x32_bf16 a[216:219], v[20:23], v[88:91], a[216:219]\n"
        "ds_read_b64_tr_b16 v[86:87], v229 offset:24576\n"
        "global_load_dword v253, v173, s[82:83] offset:0\n"
        "v_mfma_f32_16x16x32_bf16 a[248:251], v[28:31], v[88:91], a[248:251]\n"
        "ds_read_b64_tr_b16 v[88:89], v230 offset:24576\n"
        "global_load_lds_dwordx4 v237, s[58:59] offset:3072\n"
        "s_add_u32 m0, s73, 0x20000\n"
        "v_mfma_f32_16x16x32_bf16 a[248:251], v[24:27], v[56:59], a[248:251]\n"
        "ds_read_b64_tr_b16 v[56:57], v230 offset:16384\n"
        "s_add_u32 s58, s58, 0x80\n"
        "s_addc_u32 s59, s59, 0\n"
        "v_mfma_f32_16x16x32_bf16 a[252:255], v[24:27], v[60:63], a[252:255]\n"
        "ds_read_b64_tr_b16 v[58:59], v231 offset:16384\n"
        "s_add_u32 s82, s12, 0x200000\n"
        "s_addc_u32 s83, s13, 0\n"
        "v_mfma_f32_16x16x32_bf16 a[252:255], v[28:31], v[92:95], a[252:255]\n"
        "ds_read_b64_tr_b16 v[90:91], v231 offset:24576\n"
        "v_cvt_pk_bf16_f32 v189, 0, v189\n"
        "v_mfma_f32_16x16x32_bf16 a[220:223], v[20:23], v[92:95], a[220:223]\n"
        "ds_read_b64_tr_b16 v[92:93], v232 offset:24576\n"
        "global_load_lds_dwordx4 v238, s[60:61] offset:0\n"
        "v_mfma_f32_16x16x32_bf16 a[220:223], v[16:19], v[60:63], a[220:223]\n"
        "ds_read_b64_tr_b16 v[60:61], v232 offset:16384\n"
        "s_waitcnt lgkmcnt(8)\n"
        "v_mfma_f32_16x16x32_bf16 a[0:3], v[32:35], v[0:3], a[0:3]\n"
        "ds_read_b64_tr_b16 v[62:63], v233 offset:16384\n"
        "v_cvt_pk_bf16_f32 v191, 0, v191\n"
        "v_mfma_f32_16x16x32_bf16 a[0:3], v[64:67], v[4:7], a[0:3]\n"
        "ds_read_b64_tr_b16 v[94:95], v233 offset:24576\n"
        "v_mul_f32 v190, v180, v186\n"
        "v_mfma_f32_16x16x32_bf16 a[32:35], v[64:67], v[12:15], a[32:35]\n"
        "ds_read_b128 v[16:19], v224 offset:36864\n"
        "global_load_lds_dwordx4 v239, s[60:61] offset:1024\n"
        "v_mfma_f32_16x16x32_bf16 a[32:35], v[32:35], v[8:11], a[32:35]\n"
        "ds_read_b128 v[20:23], v225 offset:36864\n"
        "v_mul_f32 v192, v181, v186\n"
        "v_mfma_f32_16x16x32_bf16 a[36:39], v[36:39], v[8:11], a[36:39]\n"
        "ds_read_b128 v[24:27], v224 offset:38912\n"
        "v_cvt_pk_bf16_f32 v190, 0, v190\n"
        "v_mfma_f32_16x16x32_bf16 a[36:39], v[68:71], v[12:15], a[36:39]\n"
        "ds_read_b128 v[28:31], v225 offset:38912\n"
        "v_cvt_pk_bf16_f32 v192, 0, v192\n"
        "v_mfma_f32_16x16x32_bf16 a[4:7], v[68:71], v[4:7], a[4:7]\n"
        "global_load_lds_dwordx4 v240, s[60:61] offset:2048\n"
        "v_mfma_f32_16x16x32_bf16 a[4:7], v[36:39], v[0:3], a[4:7]\n"
        "v_add_f32 v189, v189, v190\n"
        "v_add_f32 v191, v191, v192\n"
        "v_mfma_f32_16x16x32_bf16 a[8:11], v[40:43], v[0:3], a[8:11]\n"
        "v_cvt_pk_bf16_f32 v189, 0, v189\n"
        "v_cvt_pk_bf16_f32 v191, 0, v191\n"
        "v_mfma_f32_16x16x32_bf16 a[8:11], v[72:75], v[4:7], a[8:11]\n"
        "s_waitcnt lgkmcnt(4)\n"
        "v_mul_f32 v190, v181, v185\n"
        "v_mul_f32 v192, v182, v185\n"
        "v_mfma_f32_16x16x32_bf16 a[40:43], v[72:75], v[12:15], a[40:43]\n"
        "global_load_lds_dwordx4 v241, s[60:61] offset:3072\n"
        "s_add_u32 m0, s73, 0x24000\n"
        "v_mfma_f32_16x16x32_bf16 a[40:43], v[40:43], v[8:11], a[40:43]\n"
        "s_add_u32 s60, s60, 0x80000\n"
        "s_addc_u32 s61, s61, 0\n"
        "v_cvt_pk_bf16_f32 v190, 0, v190\n"
        "v_mfma_f32_16x16x32_bf16 a[44:47], v[44:47], v[8:11], a[44:47]\n"
        "v_cvt_pk_bf16_f32 v192, 0, v192\n"
        "v_add_f32 v189, v189, v190\n"
        "v_mfma_f32_16x16x32_bf16 a[44:47], v[76:79], v[12:15], a[44:47]\n"
        "v_add_f32 v191, v191, v192\n"
        "v_cvt_pk_bf16_f32 v189, 0, v189\n"
        "v_mfma_f32_16x16x32_bf16 a[12:15], v[76:79], v[4:7], a[12:15]\n"
        "global_load_lds_dwordx4 v238, s[62:63] offset:0\n"
        "v_mfma_f32_16x16x32_bf16 a[12:15], v[44:47], v[0:3], a[12:15]\n"
        "v_cvt_pk_bf16_f32 v191, 0, v191\n"
        "v_lshlrev_b32 v190, 16, v126\n"
        "v_mfma_f32_16x16x32_bf16 a[16:19], v[0:3], v[48:51], a[16:19]\n"
        "v_lshlrev_b32 v192, 16, v128\n"
        "v_mul_f32 v189, v189, v190\n"
        "v_mfma_f32_16x16x32_bf16 a[16:19], v[4:7], v[80:83], a[16:19]\n"
        "v_mul_f32 v191, v191, v192\n"
        "v_cvt_pk_bf16_f32 v201, v191, v189\n"
        "v_mfma_f32_16x16x32_bf16 a[48:51], v[12:15], v[80:83], a[48:51]\n"
        "global_load_lds_dwordx4 v239, s[62:63] offset:1024\n"
        "v_mfma_f32_16x16x32_bf16 a[48:51], v[8:11], v[48:51], a[48:51]\n"
        "v_mul_f32 v193, v180, v188\n"
        "v_mul_f32 v195, v181, v188\n"
        "v_mfma_f32_16x16x32_bf16 a[52:55], v[8:11], v[52:55], a[52:55]\n"
        "v_cvt_pk_bf16_f32 v193, 0, v193\n"
        "v_cvt_pk_bf16_f32 v195, 0, v195\n"
        "v_mfma_f32_16x16x32_bf16 a[52:55], v[12:15], v[84:87], a[52:55]\n"
        "s_waitcnt lgkmcnt(0)\n"
        "v_mul_f32 v194, v181, v187\n"
        "v_mul_f32 v196, v182, v187\n"
        "v_mfma_f32_16x16x32_bf16 a[20:23], v[4:7], v[84:87], a[20:23]\n"
        "global_load_lds_dwordx4 v240, s[62:63] offset:2048\n"
        "v_mfma_f32_16x16x32_bf16 a[20:23], v[0:3], v[52:55], a[20:23]\n"
        "v_cvt_pk_bf16_f32 v194, 0, v194\n"
        "v_cvt_pk_bf16_f32 v196, 0, v196\n"
        "v_mfma_f32_16x16x32_bf16 a[24:27], v[0:3], v[56:59], a[24:27]\n"
        "v_add_f32 v193, v193, v194\n"
        "v_add_f32 v195, v195, v196\n"
        "v_mfma_f32_16x16x32_bf16 a[24:27], v[4:7], v[88:91], a[24:27]\n"
        "v_cvt_pk_bf16_f32 v193, 0, v193\n"
        "v_cvt_pk_bf16_f32 v195, 0, v195\n"
        "v_mfma_f32_16x16x32_bf16 a[56:59], v[12:15], v[88:91], a[56:59]\n"
        "global_load_lds_dwordx4 v241, s[62:63] offset:3072\n"
        "s_add_u32 m0, s72, 0x8000\n"
        "v_mfma_f32_16x16x32_bf16 a[56:59], v[8:11], v[56:59], a[56:59]\n"
        "s_add_u32 s62, s62, 0x80000\n"
        "s_addc_u32 s63, s63, 0\n"
        "v_mul_f32 v194, v182, v186\n"
        "v_mfma_f32_16x16x32_bf16 a[60:63], v[8:11], v[60:63], a[60:63]\n"
        "ds_read_b128 v[8:11], v224 offset:43008\n"
        "v_mul_f32 v196, v183, v186\n"
        "v_mfma_f32_16x16x32_bf16 a[60:63], v[12:15], v[92:95], a[60:63]\n"
        "ds_read_b128 v[12:15], v225 offset:43008\n"
        "v_cvt_pk_bf16_f32 v194, 0, v194\n"
        "v_mfma_f32_16x16x32_bf16 a[28:31], v[4:7], v[92:95], a[28:31]\n"
        "ds_read_b128 v[4:7], v225 offset:40960\n"
        "global_load_dwordx4 v[142:145], v254, s[92:93] offset:0\n"
        "v_mfma_f32_16x16x32_bf16 a[28:31], v[0:3], v[60:63], a[28:31]\n"
        "ds_read_b128 v[0:3], v224 offset:40960\n"
        "v_cvt_pk_bf16_f32 v196, 0, v196\n"
        "v_mfma_f32_16x16x32_bf16 a[64:67], v[32:35], v[16:19], a[64:67]\n"
        "v_add_f32 v193, v193, v194\n"
        "v_add_f32 v195, v195, v196\n"
        "v_mfma_f32_16x16x32_bf16 a[64:67], v[64:67], v[20:23], a[64:67]\n"
        "v_cvt_pk_bf16_f32 v193, 0, v193\n"
        "v_cvt_pk_bf16_f32 v195, 0, v195\n"
        "v_mfma_f32_16x16x32_bf16 a[96:99], v[64:67], v[28:31], a[96:99]\n"
        "v_mul_f32 v194, v183, v185\n"
        "v_mul_f32 v196, v184, v185\n"
        "v_mfma_f32_16x16x32_bf16 a[96:99], v[32:35], v[24:27], a[96:99]\n"
        "v_cvt_pk_bf16_f32 v194, 0, v194\n"
        "v_cvt_pk_bf16_f32 v196, 0, v196\n"
        "v_mfma_f32_16x16x32_bf16 a[100:103], v[36:39], v[24:27], a[100:103]\n"
        "v_add_f32 v193, v193, v194\n"
        "v_add_f32 v195, v195, v196\n"
        "v_mfma_f32_16x16x32_bf16 a[100:103], v[68:71], v[28:31], a[100:103]\n"
        "s_waitcnt vmcnt(27) lgkmcnt(2)\n"
        "v_cvt_pk_bf16_f32 v193, 0, v193\n"
        "v_cvt_pk_bf16_f32 v195, 0, v195\n"
        "v_mfma_f32_16x16x32_bf16 a[68:71], v[68:71], v[20:23], a[68:71]\n"
        "v_lshlrev_b32 v194, 16, v130\n"
        "v_lshlrev_b32 v196, 16, v132\n"
        "v_mfma_f32_16x16x32_bf16 a[68:71], v[36:39], v[16:19], a[68:71]\n"
        "v_mul_f32 v193, v193, v194\n"
        "v_mul_f32 v195, v195, v196\n"
        "v_mfma_f32_16x16x32_bf16 a[72:75], v[40:43], v[16:19], a[72:75]\n"
        "v_cvt_pk_bf16_f32 v200, v195, v193\n"
        "v_lshlrev_b32 v185, 16, v136\n"
        "v_mfma_f32_16x16x32_bf16 a[72:75], v[72:75], v[20:23], a[72:75]\n"
        "global_store_dwordx4 v171, v[200:203], s[76:77] offset:-4096 sc1\n"
        "v_and_b32 v186, 0xffff0000, v136\n"
        "v_lshlrev_b32 v187, 16, v137\n"
        "v_and_b32 v188, 0xffff0000, v137\n"
        "v_and_b32 v178, 0xffff0000, v104\n"
        "v_mfma_f32_16x16x32_bf16 a[104:107], v[72:75], v[28:31], a[104:107]\n"
        "v_and_b32 v179, 0xffff0000, v106\n"
        "v_and_b32 v180, 0xffff0000, v108\n"
        "v_mfma_f32_16x16x32_bf16 a[104:107], v[40:43], v[24:27], a[104:107]\n"
        "v_and_b32 v181, 0xffff0000, v110\n"
        "v_and_b32 v182, 0xffff0000, v112\n"
        "v_mfma_f32_16x16x32_bf16 a[108:111], v[44:47], v[24:27], a[108:111]\n"
        "v_and_b32 v183, 0xffff0000, v114\n"
        "v_and_b32 v184, 0xffff0000, v116\n"
        "v_mfma_f32_16x16x32_bf16 a[108:111], v[76:79], v[28:31], a[108:111]\n"
        "v_mul_f32 v189, v174, v188\n"
        "v_mul_f32 v191, v175, v188\n"
        "v_mfma_f32_16x16x32_bf16 a[76:79], v[76:79], v[20:23], a[76:79]\n"
        "v_cvt_pk_bf16_f32 v189, 0, v189\n"
        "v_cvt_pk_bf16_f32 v191, 0, v191\n"
        "v_mfma_f32_16x16x32_bf16 a[76:79], v[44:47], v[16:19], a[76:79]\n"
        "global_load_dwordx4 v[146:149], v254, s[92:93] offset:1024\n"
        "s_add_u32 s92, s8, s91\n"
        "s_addc_u32 s93, s9, 0\n"
        "v_mfma_f32_16x16x32_bf16 a[80:83], v[16:19], v[48:51], a[80:83]\n"
        "v_mul_f32 v190, v175, v187\n"
        "v_mul_f32 v192, v176, v187\n"
        "v_mfma_f32_16x16x32_bf16 a[80:83], v[20:23], v[80:83], a[80:83]\n"
        "v_cvt_pk_bf16_f32 v190, 0, v190\n"
        "v_cvt_pk_bf16_f32 v192, 0, v192\n"
        "v_mfma_f32_16x16x32_bf16 a[112:115], v[28:31], v[80:83], a[112:115]\n"
        "v_add_f32 v189, v189, v190\n"
        "v_add_f32 v191, v191, v192\n"
        "v_mfma_f32_16x16x32_bf16 a[112:115], v[24:27], v[48:51], a[112:115]\n"
        "v_cvt_pk_bf16_f32 v189, 0, v189\n"
        "v_cvt_pk_bf16_f32 v191, 0, v191\n"
        "v_mfma_f32_16x16x32_bf16 a[116:119], v[24:27], v[52:55], a[116:119]\n"
        "v_mul_f32 v190, v176, v186\n"
        "v_mul_f32 v192, v177, v186\n"
        "v_mfma_f32_16x16x32_bf16 a[116:119], v[28:31], v[84:87], a[116:119]\n"
        "s_waitcnt lgkmcnt(0)\n"
        "v_cvt_pk_bf16_f32 v190, 0, v190\n"
        "v_cvt_pk_bf16_f32 v192, 0, v192\n"
        "v_mfma_f32_16x16x32_bf16 a[84:87], v[20:23], v[84:87], a[84:87]\n"
        "v_add_f32 v189, v189, v190\n"
        "v_add_f32 v191, v191, v192\n"
        "v_mfma_f32_16x16x32_bf16 a[84:87], v[16:19], v[52:55], a[84:87]\n"
        "v_cvt_pk_bf16_f32 v189, 0, v189\n"
        "v_cvt_pk_bf16_f32 v191, 0, v191\n"
        "v_mfma_f32_16x16x32_bf16 a[88:91], v[16:19], v[56:59], a[88:91]\n"
        "v_mul_f32 v190, v177, v185\n"
        "v_mul_f32 v192, v178, v185\n"
        "v_mfma_f32_16x16x32_bf16 a[88:91], v[20:23], v[88:91], a[88:91]\n"
        "v_cvt_pk_bf16_f32 v190, 0, v190\n"
        "v_cvt_pk_bf16_f32 v192, 0, v192\n"
        "v_mfma_f32_16x16x32_bf16 a[120:123], v[28:31], v[88:91], a[120:123]\n"
        "v_add_f32 v189, v189, v190\n"
        "v_add_f32 v191, v191, v192\n"
        "v_mfma_f32_16x16x32_bf16 a[120:123], v[24:27], v[56:59], a[120:123]\n"
        "v_cvt_pk_bf16_f32 v189, 0, v189\n"
        "v_cvt_pk_bf16_f32 v191, 0, v191\n"
        "v_mfma_f32_16x16x32_bf16 a[124:127], v[24:27], v[60:63], a[124:127]\n"
        "ds_read_b128 v[24:27], v224 offset:47104\n"
        "v_and_b32 v190, 0xffff0000, v118\n"
        "v_mfma_f32_16x16x32_bf16 a[124:127], v[28:31], v[92:95], a[124:127]\n"
        "ds_read_b128 v[28:31], v225 offset:47104\n"
        "v_and_b32 v192, 0xffff0000, v120\n"
        "v_mfma_f32_16x16x32_bf16 a[92:95], v[20:23], v[92:95], a[92:95]\n"
        "ds_read_b128 v[20:23], v225 offset:45056\n"
        "v_mul_f32 v189, v189, v190\n"
        "v_mfma_f32_16x16x32_bf16 a[92:95], v[16:19], v[60:63], a[92:95]\n"
        "ds_read_b128 v[16:19], v224 offset:45056\n"
        "v_mul_f32 v191, v191, v192\n"
        "v_mfma_f32_16x16x32_bf16 a[128:131], v[32:35], v[0:3], a[128:131]\n"
        "v_cvt_pk_bf16_f32 v211, v191, v189\n"
        "v_mul_f32 v193, v176, v188\n"
        "v_mfma_f32_16x16x32_bf16 a[128:131], v[64:67], v[4:7], a[128:131]\n"
        "v_mul_f32 v195, v177, v188\n"
        "v_cvt_pk_bf16_f32 v193, 0, v193\n"
        "v_mfma_f32_16x16x32_bf16 a[160:163], v[64:67], v[12:15], a[160:163]\n"
        "v_cvt_pk_bf16_f32 v195, 0, v195\n"
        "v_mul_f32 v194, v177, v187\n"
        "v_mfma_f32_16x16x32_bf16 a[160:163], v[32:35], v[8:11], a[160:163]\n"
        "v_mul_f32 v196, v178, v187\n"
        "v_cvt_pk_bf16_f32 v194, 0, v194\n"
        "v_mfma_f32_16x16x32_bf16 a[164:167], v[36:39], v[8:11], a[164:167]\n"
        "v_cvt_pk_bf16_f32 v196, 0, v196\n"
        "v_add_f32 v193, v193, v194\n"
        "v_mfma_f32_16x16x32_bf16 a[164:167], v[68:71], v[12:15], a[164:167]\n"
        "v_add_f32 v195, v195, v196\n"
        "v_cvt_pk_bf16_f32 v193, 0, v193\n"
        "v_mfma_f32_16x16x32_bf16 a[132:135], v[68:71], v[4:7], a[132:135]\n"
        "v_cvt_pk_bf16_f32 v195, 0, v195\n"
        "v_mul_f32 v194, v178, v186\n"
        "v_mfma_f32_16x16x32_bf16 a[132:135], v[36:39], v[0:3], a[132:135]\n"
        "global_load_dword v253, v173, s[82:83] offset:0\n"
        "s_add_u32 s82, s12, 0x300000\n"
        "s_addc_u32 s83, s13, 0\n"
        "v_mfma_f32_16x16x32_bf16 a[136:139], v[40:43], v[0:3], a[136:139]\n"
        "global_load_dwordx4 v[150:153], v254, s[92:93] offset:0\n"
        "v_mul_f32 v196, v179, v186\n"
        "v_mfma_f32_16x16x32_bf16 a[136:139], v[72:75], v[4:7], a[136:139]\n"
        "v_cvt_pk_bf16_f32 v194, 0, v194\n"
        "v_cvt_pk_bf16_f32 v196, 0, v196\n"
        "v_mfma_f32_16x16x32_bf16 a[168:171], v[72:75], v[12:15], a[168:171]\n"
        "v_add_f32 v193, v193, v194\n"
        "v_add_f32 v195, v195, v196\n"
        "v_mfma_f32_16x16x32_bf16 a[168:171], v[40:43], v[8:11], a[168:171]\n"
        "v_cvt_pk_bf16_f32 v193, 0, v193\n"
        "v_cvt_pk_bf16_f32 v195, 0, v195\n"
        "v_mfma_f32_16x16x32_bf16 a[172:175], v[44:47], v[8:11], a[172:175]\n"
        "v_mul_f32 v194, v179, v185\n"
        "v_mul_f32 v196, v180, v185\n"
        "v_mfma_f32_16x16x32_bf16 a[172:175], v[76:79], v[12:15], a[172:175]\n"
        "v_cvt_pk_bf16_f32 v194, 0, v194\n"
        "v_cvt_pk_bf16_f32 v196, 0, v196\n"
        "v_mfma_f32_16x16x32_bf16 a[140:143], v[76:79], v[4:7], a[140:143]\n"
        "v_add_f32 v193, v193, v194\n"
        "v_add_f32 v195, v195, v196\n"
        "v_mfma_f32_16x16x32_bf16 a[140:143], v[44:47], v[0:3], a[140:143]\n"
        "LHA2_%=:\n"
        "s_cbranch_execz LHA6_%=\n"
        "v_cvt_pk_bf16_f32 v193, 0, v193\n"
        "v_cvt_pk_bf16_f32 v195, 0, v195\n"
        "v_mfma_f32_16x16x32_bf16 a[144:147], v[0:3], v[48:51], a[144:147]\n"
        "v_and_b32 v194, 0xffff0000, v122\n"
        "v_and_b32 v196, 0xffff0000, v124\n"
        "v_mfma_f32_16x16x32_bf16 a[144:147], v[4:7], v[80:83], a[144:147]\n"
        "v_mul_f32 v193, v193, v194\n"
        "v_mul_f32 v195, v195, v196\n"
        "v_mfma_f32_16x16x32_bf16 a[176:179], v[12:15], v[80:83], a[176:179]\n"
        "v_cvt_pk_bf16_f32 v210, v195, v193\n"
        "v_mul_f32 v189, v178, v188\n"
        "v_mfma_f32_16x16x32_bf16 a[176:179], v[8:11], v[48:51], a[176:179]\n"
        "v_mul_f32 v191, v179, v188\n"
        "v_cvt_pk_bf16_f32 v189, 0, v189\n"
        "v_mfma_f32_16x16x32_bf16 a[180:183], v[8:11], v[52:55], a[180:183]\n"
        "v_cvt_pk_bf16_f32 v191, 0, v191\n"
        "v_mul_f32 v190, v179, v187\n"
        "v_mfma_f32_16x16x32_bf16 a[180:183], v[12:15], v[84:87], a[180:183]\n"
        "s_waitcnt lgkmcnt(0)\n"
        "v_mul_f32 v192, v180, v187\n"
        "v_cvt_pk_bf16_f32 v190, 0, v190\n"
        "v_mfma_f32_16x16x32_bf16 a[148:151], v[4:7], v[84:87], a[148:151]\n"
        "v_cvt_pk_bf16_f32 v192, 0, v192\n"
        "v_add_f32 v189, v189, v190\n"
        "v_mfma_f32_16x16x32_bf16 a[148:151], v[0:3], v[52:55], a[148:151]\n"
        "v_add_f32 v191, v191, v192\n"
        "v_cvt_pk_bf16_f32 v189, 0, v189\n"
        "v_mfma_f32_16x16x32_bf16 a[152:155], v[0:3], v[56:59], a[152:155]\n"
        "v_cvt_pk_bf16_f32 v191, 0, v191\n"
        "v_mul_f32 v190, v180, v186\n"
        "v_mfma_f32_16x16x32_bf16 a[152:155], v[4:7], v[88:91], a[152:155]\n"
        "v_mul_f32 v192, v181, v186\n"
        "v_cvt_pk_bf16_f32 v190, 0, v190\n"
        "v_mfma_f32_16x16x32_bf16 a[184:187], v[12:15], v[88:91], a[184:187]\n"
        "v_cvt_pk_bf16_f32 v192, 0, v192\n"
        "v_add_f32 v189, v189, v190\n"
        "v_mfma_f32_16x16x32_bf16 a[184:187], v[8:11], v[56:59], a[184:187]\n"
        "s_waitcnt vmcnt(13)\n"
        "v_add_f32 v191, v191, v192\n"
        "v_cvt_pk_bf16_f32 v189, 0, v189\n"
        "v_mfma_f32_16x16x32_bf16 a[188:191], v[8:11], v[60:63], a[188:191]\n"
        "s_barrier\n"
        "v_mfma_f32_16x16x32_bf16 a[188:191], v[12:15], v[92:95], a[188:191]\n"
        "ds_read_b128 v[8:11], v224 offset:2048\n"
        "global_load_lds_dwordx4 v234, s[56:57] offset:0\n"
        "v_mfma_f32_16x16x32_bf16 a[156:159], v[4:7], v[92:95], a[156:159]\n"
        "ds_read_b128 v[4:7], v225 offset:0\n"
        "v_cvt_pk_bf16_f32 v191, 0, v191\n"
        "v_mfma_f32_16x16x32_bf16 a[156:159], v[0:3], v[60:63], a[156:159]\n"
        "ds_read_b128 v[0:3], v224 offset:0\n"
        "v_mul_f32 v190, v181, v185\n"
        "v_mfma_f32_16x16x32_bf16 a[192:195], v[32:35], v[16:19], a[192:195]\n"
        "ds_read_b128 v[12:15], v225 offset:2048\n"
        "v_mul_f32 v192, v182, v185\n"
        "v_mfma_f32_16x16x32_bf16 a[192:195], v[64:67], v[20:23], a[192:195]\n"
        "global_load_lds_dwordx4 v235, s[56:57] offset:1024\n"
        "v_mfma_f32_16x16x32_bf16 a[224:227], v[64:67], v[28:31], a[224:227]\n"
        "ds_read_b64_tr_b16 v[64:65], v226 offset:40960\n"
        "v_cvt_pk_bf16_f32 v190, 0, v190\n"
        "v_mfma_f32_16x16x32_bf16 a[224:227], v[32:35], v[24:27], a[224:227]\n"
        "ds_read_b64_tr_b16 v[32:33], v226 offset:32768\n"
        "v_cvt_pk_bf16_f32 v192, 0, v192\n"
        "v_mfma_f32_16x16x32_bf16 a[228:231], v[36:39], v[24:27], a[228:231]\n"
        "ds_read_b64_tr_b16 v[34:35], v227 offset:32768\n"
        "v_add_f32 v189, v189, v190\n"
        "v_mfma_f32_16x16x32_bf16 a[228:231], v[68:71], v[28:31], a[228:231]\n"
        "ds_read_b64_tr_b16 v[66:67], v227 offset:40960\n"
        "global_load_dwordx4 v[154:157], v254, s[92:93] offset:1024\n"
        "v_mfma_f32_16x16x32_bf16 a[196:199], v[68:71], v[20:23], a[196:199]\n"
        "ds_read_b64_tr_b16 v[68:69], v228 offset:40960\n"
        "v_add_f32 v191, v191, v192\n"
        "v_mfma_f32_16x16x32_bf16 a[196:199], v[36:39], v[16:19], a[196:199]\n"
        "ds_read_b64_tr_b16 v[36:37], v228 offset:32768\n"
        "v_cvt_pk_bf16_f32 v189, 0, v189\n"
        "v_mfma_f32_16x16x32_bf16 a[200:203], v[40:43], v[16:19], a[200:203]\n"
        "ds_read_b64_tr_b16 v[38:39], v229 offset:32768\n"
        "v_cvt_pk_bf16_f32 v191, 0, v191\n"
        "v_mfma_f32_16x16x32_bf16 a[200:203], v[72:75], v[20:23], a[200:203]\n"
        "ds_read_b64_tr_b16 v[70:71], v229 offset:40960\n"
        "global_load_lds_dwordx4 v236, s[56:57] offset:2048\n"
        "v_mfma_f32_16x16x32_bf16 a[232:235], v[72:75], v[28:31], a[232:235]\n"
        "ds_read_b64_tr_b16 v[72:73], v230 offset:40960\n"
        "v_and_b32 v190, 0xffff0000, v126\n"
        "v_mfma_f32_16x16x32_bf16 a[232:235], v[40:43], v[24:27], a[232:235]\n"
        "ds_read_b64_tr_b16 v[40:41], v230 offset:32768\n"
        "v_and_b32 v192, 0xffff0000, v128\n"
        "v_mfma_f32_16x16x32_bf16 a[236:239], v[44:47], v[24:27], a[236:239]\n"
        "ds_read_b64_tr_b16 v[42:43], v231 offset:32768\n"
        "v_mul_f32 v189, v189, v190\n"
        "v_mfma_f32_16x16x32_bf16 a[236:239], v[76:79], v[28:31], a[236:239]\n"
        "ds_read_b64_tr_b16 v[74:75], v231 offset:40960\n"
        "global_load_lds_dwordx4 v237, s[56:57] offset:3072\n"
        "s_add_u32 m0, s72, 0x9000\n"
        "v_mfma_f32_16x16x32_bf16 a[204:207], v[76:79], v[20:23], a[204:207]\n"
        "ds_read_b64_tr_b16 v[76:77], v232 offset:40960\n"
        "s_add_u32 s56, s56, 0x80\n"
        "s_addc_u32 s57, s57, 0\n"
        "v_mfma_f32_16x16x32_bf16 a[204:207], v[44:47], v[16:19], a[204:207]\n"
        "ds_read_b64_tr_b16 v[44:45], v232 offset:32768\n"
        "v_mul_f32 v191, v191, v192\n"
        "v_mfma_f32_16x16x32_bf16 a[208:211], v[16:19], v[48:51], a[208:211]\n"
        "ds_read_b64_tr_b16 v[46:47], v233 offset:32768\n"
        "v_cvt_pk_bf16_f32 v209, v191, v189\n"
        "v_mfma_f32_16x16x32_bf16 a[208:211], v[20:23], v[80:83], a[208:211]\n"
        "ds_read_b64_tr_b16 v[78:79], v233 offset:40960\n"
        "global_load_lds_dwordx4 v234, s[58:59] offset:0\n"
        "v_mfma_f32_16x16x32_bf16 a[240:243], v[28:31], v[80:83], a[240:243]\n"
        "ds_read_b64_tr_b16 v[80:81], v226 offset:57344\n"
        "v_mul_f32 v193, v180, v188\n"
        "v_mfma_f32_16x16x32_bf16 a[240:243], v[24:27], v[48:51], a[240:243]\n"
        "ds_read_b64_tr_b16 v[48:49], v226 offset:49152\n"
        "v_mul_f32 v195, v181, v188\n"
        "v_mfma_f32_16x16x32_bf16 a[244:247], v[24:27], v[52:55], a[244:247]\n"
        "ds_read_b64_tr_b16 v[50:51], v227 offset:49152\n"
        "v_cvt_pk_bf16_f32 v193, 0, v193\n"
        "v_mfma_f32_16x16x32_bf16 a[244:247], v[28:31], v[84:87], a[244:247]\n"
        "ds_read_b64_tr_b16 v[82:83], v227 offset:57344\n"
        "global_load_lds_dwordx4 v235, s[58:59] offset:1024\n"
        "v_mfma_f32_16x16x32_bf16 a[212:215], v[20:23], v[84:87], a[212:215]\n"
        "ds_read_b64_tr_b16 v[84:85], v228 offset:57344\n"
        "v_cvt_pk_bf16_f32 v195, 0, v195\n"
        "v_mfma_f32_16x16x32_bf16 a[212:215], v[16:19], v[52:55], a[212:215]\n"
        "ds_read_b64_tr_b16 v[52:53], v228 offset:49152\n"
        "v_mul_f32 v194, v181, v187\n"
        "v_mfma_f32_16x16x32_bf16 a[216:219], v[16:19], v[56:59], a[216:219]\n"
        "ds_read_b64_tr_b16 v[54:55], v229 offset:49152\n"
        "v_mul_f32 v196, v182, v187\n"
        "v_mfma_f32_16x16x32_bf16 a[216:219], v[20:23], v[88:91], a[216:219]\n"
        "ds_read_b64_tr_b16 v[86:87], v229 offset:57344\n"
        "global_load_lds_dwordx4 v236, s[58:59] offset:2048\n"
        "v_mfma_f32_16x16x32_bf16 a[248:251], v[28:31], v[88:91], a[248:251]\n"
        "ds_read_b64_tr_b16 v[88:89], v230 offset:57344\n"
        "v_cvt_pk_bf16_f32 v194, 0, v194\n"
        "v_mfma_f32_16x16x32_bf16 a[248:251], v[24:27], v[56:59], a[248:251]\n"
        "ds_read_b64_tr_b16 v[56:57], v230 offset:49152\n"
        "v_cvt_pk_bf16_f32 v196, 0, v196\n"
        "v_mfma_f32_16x16x32_bf16 a[252:255], v[24:27], v[60:63], a[252:255]\n"
        "ds_read_b64_tr_b16 v[58:59], v231 offset:49152\n"
        "v_add_f32 v193, v193, v194\n"
        "v_mfma_f32_16x16x32_bf16 a[252:255], v[28:31], v[92:95], a[252:255]\n"
        "ds_read_b64_tr_b16 v[90:91], v231 offset:57344\n"
        "global_load_lds_dwordx4 v237, s[58:59] offset:3072\n"
        "s_add_u32 m0, s73, 0x10000\n"
        "v_mfma_f32_16x16x32_bf16 a[220:223], v[20:23], v[92:95], a[220:223]\n"
        "ds_read_b64_tr_b16 v[92:93], v232 offset:57344\n"
        "s_add_u32 s58, s58, 0x80\n"
        "s_addc_u32 s59, s59, 0\n"
        "v_mfma_f32_16x16x32_bf16 a[220:223], v[16:19], v[60:63], a[220:223]\n"
        "ds_read_b64_tr_b16 v[60:61], v232 offset:49152\n"
        "s_waitcnt lgkmcnt(8)\n"
        "v_mfma_f32_16x16x32_bf16 a[0:3], v[32:35], v[0:3], a[0:3]\n"
        "ds_read_b64_tr_b16 v[62:63], v233 offset:49152\n"
        "v_add_f32 v195, v195, v196\n"
        "v_mfma_f32_16x16x32_bf16 a[0:3], v[64:67], v[4:7], a[0:3]\n"
        "ds_read_b64_tr_b16 v[94:95], v233 offset:57344\n"
        "global_load_dwordx4 v[158:161], v254, s[30:31] offset:0\n"
        "v_mfma_f32_16x16x32_bf16 a[32:35], v[64:67], v[12:15], a[32:35]\n"
        "ds_read_b128 v[16:19], v224 offset:4096\n"
        "v_cvt_pk_bf16_f32 v193, 0, v193\n"
        "v_mfma_f32_16x16x32_bf16 a[32:35], v[32:35], v[8:11], a[32:35]\n"
        "ds_read_b128 v[20:23], v225 offset:4096\n"
        "v_cvt_pk_bf16_f32 v195, 0, v195\n"
        "v_mfma_f32_16x16x32_bf16 a[36:39], v[36:39], v[8:11], a[36:39]\n"
        "ds_read_b128 v[24:27], v224 offset:6144\n"
        "v_mul_f32 v194, v182, v186\n"
        "v_mfma_f32_16x16x32_bf16 a[36:39], v[68:71], v[12:15], a[36:39]\n"
        "ds_read_b128 v[28:31], v225 offset:6144\n"
        "global_load_lds_dwordx4 v238, s[60:61] offset:0\n"
        "v_mfma_f32_16x16x32_bf16 a[4:7], v[68:71], v[4:7], a[4:7]\n"
        "v_mul_f32 v196, v183, v186\n"
        "v_cvt_pk_bf16_f32 v194, 0, v194\n"
        "v_mfma_f32_16x16x32_bf16 a[4:7], v[36:39], v[0:3], a[4:7]\n"
        "v_cvt_pk_bf16_f32 v196, 0, v196\n"
        "v_add_f32 v193, v193, v194\n"
        "v_mfma_f32_16x16x32_bf16 a[8:11], v[40:43], v[0:3], a[8:11]\n"
        "v_add_f32 v195, v195, v196\n"
        "v_cvt_pk_bf16_f32 v193, 0, v193\n"
        "v_mfma_f32_16x16x32_bf16 a[8:11], v[72:75], v[4:7], a[8:11]\n"
        "global_load_lds_dwordx4 v239, s[60:61] offset:1024\n"
        "v_mfma_f32_16x16x32_bf16 a[40:43], v[72:75], v[12:15], a[40:43]\n"
        "s_waitcnt lgkmcnt(3)\n"
        "v_cvt_pk_bf16_f32 v195, 0, v195\n"
        "v_mul_f32 v194, v183, v185\n"
        "v_mfma_f32_16x16x32_bf16 a[40:43], v[40:43], v[8:11], a[40:43]\n"
        "v_mul_f32 v196, v184, v185\n"
        "v_cvt_pk_bf16_f32 v194, 0, v194\n"
        "v_mfma_f32_16x16x32_bf16 a[44:47], v[44:47], v[8:11], a[44:47]\n"
        "v_cvt_pk_bf16_f32 v196, 0, v196\n"
        "v_add_f32 v193, v193, v194\n"
        "v_mfma_f32_16x16x32_bf16 a[44:47], v[76:79], v[12:15], a[44:47]\n"
        "global_load_lds_dwordx4 v240, s[60:61] offset:2048\n"
        "v_mfma_f32_16x16x32_bf16 a[12:15], v[76:79], v[4:7], a[12:15]\n"
        "v_add_f32 v195, v195, v196\n"
        "v_cvt_pk_bf16_f32 v193, 0, v193\n"
        "v_mfma_f32_16x16x32_bf16 a[12:15], v[44:47], v[0:3], a[12:15]\n"
        "v_cvt_pk_bf16_f32 v195, 0, v195\n"
        "v_and_b32 v194, 0xffff0000, v130\n"
        "v_mfma_f32_16x16x32_bf16 a[16:19], v[0:3], v[48:51], a[16:19]\n"
        "v_and_b32 v196, 0xffff0000, v132\n"
        "v_mul_f32 v193, v193, v194\n"
        "v_mfma_f32_16x16x32_bf16 a[16:19], v[4:7], v[80:83], a[16:19]\n"
        "global_load_lds_dwordx4 v241, s[60:61] offset:3072\n"
        "s_add_u32 m0, s73, 0x14000\n"
        "v_mfma_f32_16x16x32_bf16 a[48:51], v[12:15], v[80:83], a[48:51]\n"
        "s_add_u32 s60, s60, 0x80000\n"
        "s_addc_u32 s61, s61, 0\n"
        "v_mul_f32 v195, v195, v196\n"
        "v_mfma_f32_16x16x32_bf16 a[48:51], v[8:11], v[48:51], a[48:51]\n"
        "v_cvt_pk_bf16_f32 v208, v195, v193\n"
        "v_lshlrev_b32 v185, 16, v138\n"
        "v_mfma_f32_16x16x32_bf16 a[52:55], v[8:11], v[52:55], a[52:55]\n"
        "v_and_b32 v186, 0xffff0000, v138\n"
        "v_lshlrev_b32 v187, 16, v139\n"
        "v_mfma_f32_16x16x32_bf16 a[52:55], v[12:15], v[84:87], a[52:55]\n"
        "global_load_lds_dwordx4 v238, s[62:63] offset:0\n"
        "v_mfma_f32_16x16x32_bf16 a[20:23], v[4:7], v[84:87], a[20:23]\n"
        "v_and_b32 v188, 0xffff0000, v139\n"
        "v_lshlrev_b32 v174, 16, v97\n"
        "v_mfma_f32_16x16x32_bf16 a[20:23], v[0:3], v[52:55], a[20:23]\n"
        "s_waitcnt lgkmcnt(0)\n"
        "v_lshlrev_b32 v175, 16, v99\n"
        "v_lshlrev_b32 v176, 16, v101\n"
        "v_mfma_f32_16x16x32_bf16 a[24:27], v[0:3], v[56:59], a[24:27]\n"
        "v_lshlrev_b32 v177, 16, v103\n"
        "v_lshlrev_b32 v178, 16, v105\n"
        "v_mfma_f32_16x16x32_bf16 a[24:27], v[4:7], v[88:91], a[24:27]\n"
        "global_load_lds_dwordx4 v239, s[62:63] offset:1024\n"
        "v_mfma_f32_16x16x32_bf16 a[56:59], v[12:15], v[88:91], a[56:59]\n"
        "v_lshlrev_b32 v179, 16, v107\n"
        "v_lshlrev_b32 v180, 16, v109\n"
        "v_mfma_f32_16x16x32_bf16 a[56:59], v[8:11], v[56:59], a[56:59]\n"
        "v_lshlrev_b32 v181, 16, v111\n"
        "v_lshlrev_b32 v182, 16, v113\n"
        "v_mfma_f32_16x16x32_bf16 a[60:63], v[8:11], v[60:63], a[60:63]\n"
        "ds_read_b128 v[8:11], v224 offset:10240\n"
        "v_lshlrev_b32 v183, 16, v115\n"
        "v_mfma_f32_16x16x32_bf16 a[60:63], v[12:15], v[92:95], a[60:63]\n"
        "ds_read_b128 v[12:15], v225 offset:10240\n"
        "global_load_dwordx4 v[162:165], v254, s[30:31] offset:1024\n"
        "v_mfma_f32_16x16x32_bf16 a[28:31], v[4:7], v[92:95], a[28:31]\n"
        "ds_read_b128 v[4:7], v225 offset:8192\n"
        "v_lshlrev_b32 v184, 16, v117\n"
        "v_mfma_f32_16x16x32_bf16 a[28:31], v[0:3], v[60:63], a[28:31]\n"
        "ds_read_b128 v[0:3], v224 offset:8192\n"
        "v_mul_f32 v189, v174, v188\n"
        "v_mfma_f32_16x16x32_bf16 a[64:67], v[32:35], v[16:19], a[64:67]\n"
        "v_mul_f32 v191, v175, v188\n"
        "v_cvt_pk_bf16_f32 v189, 0, v189\n"
        "v_mfma_f32_16x16x32_bf16 a[64:67], v[64:67], v[20:23], a[64:67]\n"
        "global_load_dword v253, v173, s[82:83] offset:0\n"
        "v_mfma_f32_16x16x32_bf16 a[96:99], v[64:67], v[28:31], a[96:99]\n"
        "global_load_lds_dwordx4 v240, s[62:63] offset:2048\n"
        "s_add_u32 s82, s12, 0x400000\n"
        "s_addc_u32 s83, s13, 0\n"
        "v_mfma_f32_16x16x32_bf16 a[96:99], v[32:35], v[24:27], a[96:99]\n"
        "v_cvt_pk_bf16_f32 v191, 0, v191\n"
        "v_mul_f32 v190, v175, v187\n"
        "v_mfma_f32_16x16x32_bf16 a[100:103], v[36:39], v[24:27], a[100:103]\n"
        "v_mul_f32 v192, v176, v187\n"
        "v_cvt_pk_bf16_f32 v190, 0, v190\n"
        "v_mfma_f32_16x16x32_bf16 a[100:103], v[68:71], v[28:31], a[100:103]\n"
        "v_cvt_pk_bf16_f32 v192, 0, v192\n"
        "v_add_f32 v189, v189, v190\n"
        "v_mfma_f32_16x16x32_bf16 a[68:71], v[68:71], v[20:23], a[68:71]\n"
        "global_load_lds_dwordx4 v241, s[62:63] offset:3072\n"
        "s_add_u32 s62, s62, 0x80000\n"
        "s_addc_u32 s63, s63, 0\n"
        "v_mfma_f32_16x16x32_bf16 a[68:71], v[36:39], v[16:19], a[68:71]\n"
        "s_add_u32 m0, s72, 0x0\n"
        "v_add_f32 v191, v191, v192\n"
        "v_cvt_pk_bf16_f32 v189, 0, v189\n"
        "v_mfma_f32_16x16x32_bf16 a[72:75], v[40:43], v[16:19], a[72:75]\n"
        "v_cvt_pk_bf16_f32 v191, 0, v191\n"
        "v_mul_f32 v190, v176, v186\n"
        "v_mfma_f32_16x16x32_bf16 a[72:75], v[72:75], v[20:23], a[72:75]\n"
        "v_mul_f32 v192, v177, v186\n"
        "v_cvt_pk_bf16_f32 v190, 0, v190\n"
        "v_mfma_f32_16x16x32_bf16 a[104:107], v[72:75], v[28:31], a[104:107]\n"
        "global_load_ushort v172, v172, s[28:29]\n"
        "v_cvt_pk_bf16_f32 v192, 0, v192\n"
        "v_mfma_f32_16x16x32_bf16 a[104:107], v[40:43], v[24:27], a[104:107]\n"
        "global_store_dwordx4 v171, v[208:211], s[76:77] offset:0 sc1\n"
        "s_add_u32 s76, s76, 0x2000\n"
        "s_addc_u32 s77, s77, 0\n"
        "v_add_f32 v189, v189, v190\n"
        "v_add_f32 v191, v191, v192\n"
        "v_cvt_pk_bf16_f32 v189, 0, v189\n"
        "v_mfma_f32_16x16x32_bf16 a[108:111], v[44:47], v[24:27], a[108:111]\n"
        "v_cvt_pk_bf16_f32 v191, 0, v191\n"
        "v_mul_f32 v190, v177, v185\n"
        "v_mfma_f32_16x16x32_bf16 a[108:111], v[76:79], v[28:31], a[108:111]\n"
        "v_mul_f32 v192, v178, v185\n"
        "v_cvt_pk_bf16_f32 v190, 0, v190\n"
        "v_mfma_f32_16x16x32_bf16 a[76:79], v[76:79], v[20:23], a[76:79]\n"
        "v_cvt_pk_bf16_f32 v192, 0, v192\n"
        "v_add_f32 v189, v189, v190\n"
        "v_mfma_f32_16x16x32_bf16 a[76:79], v[44:47], v[16:19], a[76:79]\n"
        "v_add_f32 v191, v191, v192\n"
        "v_cvt_pk_bf16_f32 v189, 0, v189\n"
        "v_mfma_f32_16x16x32_bf16 a[80:83], v[16:19], v[48:51], a[80:83]\n"
        "v_cvt_pk_bf16_f32 v191, 0, v191\n"
        "v_lshlrev_b32 v190, 16, v119\n"
        "v_mfma_f32_16x16x32_bf16 a[80:83], v[20:23], v[80:83], a[80:83]\n"
        "v_lshlrev_b32 v192, 16, v121\n"
        "v_mul_f32 v189, v189, v190\n"
        "v_mfma_f32_16x16x32_bf16 a[112:115], v[28:31], v[80:83], a[112:115]\n"
        "v_mul_f32 v191, v191, v192\n"
        "v_cvt_pk_bf16_f32 v203, v191, v189\n"
        "v_mfma_f32_16x16x32_bf16 a[112:115], v[24:27], v[48:51], a[112:115]\n"
        "v_mul_f32 v193, v176, v188\n"
        "v_mul_f32 v195, v177, v188\n"
        "v_mfma_f32_16x16x32_bf16 a[116:119], v[24:27], v[52:55], a[116:119]\n"
        "v_cvt_pk_bf16_f32 v193, 0, v193\n"
        "v_cvt_pk_bf16_f32 v195, 0, v195\n"
        "v_mfma_f32_16x16x32_bf16 a[116:119], v[28:31], v[84:87], a[116:119]\n"
        "s_waitcnt lgkmcnt(0)\n"
        "v_mul_f32 v194, v177, v187\n"
        "v_mul_f32 v196, v178, v187\n"
        "v_mfma_f32_16x16x32_bf16 a[84:87], v[20:23], v[84:87], a[84:87]\n"
        "v_cvt_pk_bf16_f32 v194, 0, v194\n"
        "v_cvt_pk_bf16_f32 v196, 0, v196\n"
        "v_mfma_f32_16x16x32_bf16 a[84:87], v[16:19], v[52:55], a[84:87]\n"
        "v_add_f32 v193, v193, v194\n"
        "v_add_f32 v195, v195, v196\n"
        "v_mfma_f32_16x16x32_bf16 a[88:91], v[16:19], v[56:59], a[88:91]\n"
        "v_cvt_pk_bf16_f32 v193, 0, v193\n"
        "v_cvt_pk_bf16_f32 v195, 0, v195\n"
        "v_mfma_f32_16x16x32_bf16 a[88:91], v[20:23], v[88:91], a[88:91]\n"
        "v_mul_f32 v194, v178, v186\n"
        "v_mul_f32 v196, v179, v186\n"
        "v_mfma_f32_16x16x32_bf16 a[120:123], v[28:31], v[88:91], a[120:123]\n"
        "v_cvt_pk_bf16_f32 v194, 0, v194\n"
        "v_cvt_pk_bf16_f32 v196, 0, v196\n"
        "v_mfma_f32_16x16x32_bf16 a[120:123], v[24:27], v[56:59], a[120:123]\n"
        "v_add_f32 v193, v193, v194\n"
        "v_add_f32 v195, v195, v196\n"
        "v_mfma_f32_16x16x32_bf16 a[124:127], v[24:27], v[60:63], a[124:127]\n"
        "ds_read_b128 v[24:27], v224 offset:14336\n"
        "v_cvt_pk_bf16_f32 v193, 0, v193\n"
        "v_mfma_f32_16x16x32_bf16 a[124:127], v[28:31], v[92:95], a[124:127]\n"
        "ds_read_b128 v[28:31], v225 offset:14336\n"
        "v_cvt_pk_bf16_f32 v195, 0, v195\n"
        "v_mfma_f32_16x16x32_bf16 a[92:95], v[20:23], v[92:95], a[92:95]\n"
        "ds_read_b128 v[20:23], v225 offset:12288\n"
        "v_mul_f32 v194, v179, v185\n"
        "v_mfma_f32_16x16x32_bf16 a[92:95], v[16:19], v[60:63], a[92:95]\n"
        "ds_read_b128 v[16:19], v224 offset:12288\n"
        "global_load_ushort v198, v198, s[26:27]\n"
        "v_mfma_f32_16x16x32_bf16 a[128:131], v[32:35], v[0:3], a[128:131]\n"
        "v_mul_f32 v196, v180, v185\n"
        "v_cvt_pk_bf16_f32 v194, 0, v194\n"
        "v_mfma_f32_16x16x32_bf16 a[128:131], v[64:67], v[4:7], a[128:131]\n"
        "v_cvt_pk_bf16_f32 v196, 0, v196\n"
        "v_add_f32 v193, v193, v194\n"
        "v_mfma_f32_16x16x32_bf16 a[160:163], v[64:67], v[12:15], a[160:163]\n"
        "v_add_f32 v195, v195, v196\n"
        "v_cvt_pk_bf16_f32 v193, 0, v193\n"
        "v_mfma_f32_16x16x32_bf16 a[160:163], v[32:35], v[8:11], a[160:163]\n"
        "v_cvt_pk_bf16_f32 v195, 0, v195\n"
        "v_lshlrev_b32 v194, 16, v123\n"
        "v_mfma_f32_16x16x32_bf16 a[164:167], v[36:39], v[8:11], a[164:167]\n"
        "v_lshlrev_b32 v196, 16, v125\n"
        "v_mul_f32 v193, v193, v194\n"
        "v_mfma_f32_16x16x32_bf16 a[164:167], v[68:71], v[12:15], a[164:167]\n"
        "v_mul_f32 v195, v195, v196\n"
        "v_cvt_pk_bf16_f32 v202, v195, v193\n"
        "v_mfma_f32_16x16x32_bf16 a[132:135], v[68:71], v[4:7], a[132:135]\n"
        "v_mul_f32 v189, v178, v188\n"
        "v_mul_f32 v191, v179, v188\n"
        "v_mfma_f32_16x16x32_bf16 a[132:135], v[36:39], v[0:3], a[132:135]\n"
        "v_cvt_pk_bf16_f32 v189, 0, v189\n"
        "v_cvt_pk_bf16_f32 v191, 0, v191\n"
        "v_mfma_f32_16x16x32_bf16 a[136:139], v[40:43], v[0:3], a[136:139]\n"
        "v_mul_f32 v190, v179, v187\n"
        "v_mul_f32 v192, v180, v187\n"
        "v_mfma_f32_16x16x32_bf16 a[136:139], v[72:75], v[4:7], a[136:139]\n"
        "v_cvt_pk_bf16_f32 v190, 0, v190\n"
        "v_cvt_pk_bf16_f32 v192, 0, v192\n"
        "v_mfma_f32_16x16x32_bf16 a[168:171], v[72:75], v[12:15], a[168:171]\n"
        "v_add_f32 v189, v189, v190\n"
        "v_add_f32 v191, v191, v192\n"
        "v_mfma_f32_16x16x32_bf16 a[168:171], v[40:43], v[8:11], a[168:171]\n"
        "v_cvt_pk_bf16_f32 v189, 0, v189\n"
        "v_cvt_pk_bf16_f32 v191, 0, v191\n"
        "v_mfma_f32_16x16x32_bf16 a[172:175], v[44:47], v[8:11], a[172:175]\n"
        "v_mul_f32 v190, v180, v186\n"
        "v_mul_f32 v192, v181, v186\n"
        "v_mfma_f32_16x16x32_bf16 a[172:175], v[76:79], v[12:15], a[172:175]\n"
        "v_cvt_pk_bf16_f32 v190, 0, v190\n"
        "v_cvt_pk_bf16_f32 v192, 0, v192\n"
        "v_mfma_f32_16x16x32_bf16 a[140:143], v[76:79], v[4:7], a[140:143]\n"
        "v_add_f32 v189, v189, v190\n"
        "v_add_f32 v191, v191, v192\n"
        "v_mfma_f32_16x16x32_bf16 a[140:143], v[44:47], v[0:3], a[140:143]\n"
        "v_cvt_pk_bf16_f32 v189, 0, v189\n"
        "v_cvt_pk_bf16_f32 v191, 0, v191\n"
        "v_mfma_f32_16x16x32_bf16 a[144:147], v[0:3], v[48:51], a[144:147]\n"
        "v_mul_f32 v190, v181, v185\n"
        "v_mul_f32 v192, v182, v185\n"
        "v_mfma_f32_16x16x32_bf16 a[144:147], v[4:7], v[80:83], a[144:147]\n"
        "v_cvt_pk_bf16_f32 v190, 0, v190\n"
        "v_cvt_pk_bf16_f32 v192, 0, v192\n"
        "v_mfma_f32_16x16x32_bf16 a[176:179], v[12:15], v[80:83], a[176:179]\n"
        "v_add_f32 v189, v189, v190\n"
        "v_add_f32 v191, v191, v192\n"
        "v_mfma_f32_16x16x32_bf16 a[176:179], v[8:11], v[48:51], a[176:179]\n"
        "v_cvt_pk_bf16_f32 v189, 0, v189\n"
        "v_cvt_pk_bf16_f32 v191, 0, v191\n"
        "v_mfma_f32_16x16x32_bf16 a[180:183], v[8:11], v[52:55], a[180:183]\n"
        "v_lshlrev_b32 v190, 16, v127\n"
        "v_lshlrev_b32 v192, 16, v129\n"
        "v_mfma_f32_16x16x32_bf16 a[180:183], v[12:15], v[84:87], a[180:183]\n"
        "s_waitcnt lgkmcnt(0)\n"
        "v_mul_f32 v189, v189, v190\n"
        "v_mul_f32 v191, v191, v192\n"
        "v_mfma_f32_16x16x32_bf16 a[148:151], v[4:7], v[84:87], a[148:151]\n"
        "v_cvt_pk_bf16_f32 v201, v191, v189\n"
        "v_mul_f32 v193, v180, v188\n"
        "v_mfma_f32_16x16x32_bf16 a[148:151], v[0:3], v[52:55], a[148:151]\n"
        "v_mul_f32 v195, v181, v188\n"
        "v_cvt_pk_bf16_f32 v193, 0, v193\n"
        "v_mfma_f32_16x16x32_bf16 a[152:155], v[0:3], v[56:59], a[152:155]\n"
        "v_cvt_pk_bf16_f32 v195, 0, v195\n"
        "v_mul_f32 v194, v181, v187\n"
        "v_mfma_f32_16x16x32_bf16 a[152:155], v[4:7], v[88:91], a[152:155]\n"
        "v_mul_f32 v196, v182, v187\n"
        "v_cvt_pk_bf16_f32 v194, 0, v194\n"
        "v_mfma_f32_16x16x32_bf16 a[184:187], v[12:15], v[88:91], a[184:187]\n"
        "v_cvt_pk_bf16_f32 v196, 0, v196\n"
        "v_add_f32 v193, v193, v194\n"
        "v_mfma_f32_16x16x32_bf16 a[184:187], v[8:11], v[56:59], a[184:187]\n"
        "s_waitcnt vmcnt(14)\n"
        "v_add_f32 v195, v195, v196\n"
        "v_cvt_pk_bf16_f32 v193, 0, v193\n"
        "v_mfma_f32_16x16x32_bf16 a[188:191], v[8:11], v[60:63], a[188:191]\n"
        "s_barrier\n"
        "v_mfma_f32_16x16x32_bf16 a[188:191], v[12:15], v[92:95], a[188:191]\n"
        "ds_read_b128 v[8:11], v224 offset:34816\n"
        "global_load_lds_dwordx4 v234, s[56:57] offset:0\n"
        "v_mfma_f32_16x16x32_bf16 a[156:159], v[4:7], v[92:95], a[156:159]\n"
        "ds_read_b128 v[4:7], v225 offset:32768\n"
        "v_cvt_pk_bf16_f32 v195, 0, v195\n"
        "v_mfma_f32_16x16x32_bf16 a[156:159], v[0:3], v[60:63], a[156:159]\n"
        "ds_read_b128 v[0:3], v224 offset:32768\n"
        "v_mul_f32 v194, v182, v186\n"
        "v_mfma_f32_16x16x32_bf16 a[192:195], v[32:35], v[16:19], a[192:195]\n"
        "ds_read_b128 v[12:15], v225 offset:34816\n"
        "v_mul_f32 v196, v183, v186\n"
        "v_mfma_f32_16x16x32_bf16 a[192:195], v[64:67], v[20:23], a[192:195]\n"
        "global_load_lds_dwordx4 v235, s[56:57] offset:1024\n"
        "v_mfma_f32_16x16x32_bf16 a[224:227], v[64:67], v[28:31], a[224:227]\n"
        "ds_read_b64_tr_b16 v[64:65], v216 offset:8192\n"
        "v_cvt_pk_bf16_f32 v194, 0, v194\n"
        "v_mfma_f32_16x16x32_bf16 a[224:227], v[32:35], v[24:27], a[224:227]\n"
        "ds_read_b64_tr_b16 v[32:33], v216 offset:0\n"
        "v_cvt_pk_bf16_f32 v196, 0, v196\n"
        "v_mfma_f32_16x16x32_bf16 a[228:231], v[36:39], v[24:27], a[228:231]\n"
        "ds_read_b64_tr_b16 v[34:35], v217 offset:0\n"
        "v_add_f32 v193, v193, v194\n"
        "v_mfma_f32_16x16x32_bf16 a[228:231], v[68:71], v[28:31], a[228:231]\n"
        "ds_read_b64_tr_b16 v[66:67], v217 offset:8192\n"
        "global_load_lds_dwordx4 v236, s[56:57] offset:2048\n"
        "v_mfma_f32_16x16x32_bf16 a[196:199], v[68:71], v[20:23], a[196:199]\n"
        "ds_read_b64_tr_b16 v[68:69], v218 offset:8192\n"
        "v_add_f32 v195, v195, v196\n"
        "v_mfma_f32_16x16x32_bf16 a[196:199], v[36:39], v[16:19], a[196:199]\n"
        "ds_read_b64_tr_b16 v[36:37], v218 offset:0\n"
        "v_cvt_pk_bf16_f32 v193, 0, v193\n"
        "v_mfma_f32_16x16x32_bf16 a[200:203], v[40:43], v[16:19], a[200:203]\n"
        "ds_read_b64_tr_b16 v[38:39], v219 offset:0\n"
        "v_cvt_pk_bf16_f32 v195, 0, v195\n"
        "v_mfma_f32_16x16x32_bf16 a[200:203], v[72:75], v[20:23], a[200:203]\n"
        "ds_read_b64_tr_b16 v[70:71], v219 offset:8192\n"
        "global_load_dword v253, v173, s[82:83] offset:0\n"
        "v_mfma_f32_16x16x32_bf16 a[232:235], v[72:75], v[28:31], a[232:235]\n"
        "ds_read_b64_tr_b16 v[72:73], v220 offset:8192\n"
        "global_load_lds_dwordx4 v237, s[56:57] offset:3072\n"
        "s_add_u32 m0, s72, 0x1000\n"
        "v_mfma_f32_16x16x32_bf16 a[232:235], v[40:43], v[24:27], a[232:235]\n"
        "ds_read_b64_tr_b16 v[40:41], v220 offset:0\n"
        "s_add_u32 s56, s56, 0x80\n"
        "s_addc_u32 s57, s57, 0\n"
        "v_mfma_f32_16x16x32_bf16 a[236:239], v[44:47], v[24:27], a[236:239]\n"
        "ds_read_b64_tr_b16 v[42:43], v221 offset:0\n"
        "s_add_u32 s82, s12, 0x500000\n"
        "s_addc_u32 s83, s13, 0\n"
        "v_mfma_f32_16x16x32_bf16 a[236:239], v[76:79], v[28:31], a[236:239]\n"
        "ds_read_b64_tr_b16 v[74:75], v221 offset:8192\n"
        "v_mul_f32 v194, v183, v185\n"
        "v_mfma_f32_16x16x32_bf16 a[204:207], v[76:79], v[20:23], a[204:207]\n"
        "ds_read_b64_tr_b16 v[76:77], v222 offset:8192\n"
        "global_load_lds_dwordx4 v234, s[58:59] offset:0\n"
        "v_mfma_f32_16x16x32_bf16 a[204:207], v[44:47], v[16:19], a[204:207]\n"
        "ds_read_b64_tr_b16 v[44:45], v222 offset:0\n"
        "v_mul_f32 v196, v184, v185\n"
        "v_mfma_f32_16x16x32_bf16 a[208:211], v[16:19], v[48:51], a[208:211]\n"
        "ds_read_b64_tr_b16 v[46:47], v223 offset:0\n"
        "v_cvt_pk_bf16_f32 v194, 0, v194\n"
        "v_mfma_f32_16x16x32_bf16 a[208:211], v[20:23], v[80:83], a[208:211]\n"
        "ds_read_b64_tr_b16 v[78:79], v223 offset:8192\n"
        "v_cvt_pk_bf16_f32 v196, 0, v196\n"
        "v_mfma_f32_16x16x32_bf16 a[240:243], v[28:31], v[80:83], a[240:243]\n"
        "ds_read_b64_tr_b16 v[80:81], v216 offset:24576\n"
        "global_load_lds_dwordx4 v235, s[58:59] offset:1024\n"
        "v_mfma_f32_16x16x32_bf16 a[240:243], v[24:27], v[48:51], a[240:243]\n"
        "ds_read_b64_tr_b16 v[48:49], v216 offset:16384\n"
        "v_add_f32 v193, v193, v194\n"
        "v_mfma_f32_16x16x32_bf16 a[244:247], v[24:27], v[52:55], a[244:247]\n"
        "ds_read_b64_tr_b16 v[50:51], v217 offset:16384\n"
        "v_add_f32 v195, v195, v196\n"
        "v_mfma_f32_16x16x32_bf16 a[244:247], v[28:31], v[84:87], a[244:247]\n"
        "ds_read_b64_tr_b16 v[82:83], v217 offset:24576\n"
        "v_cvt_pk_bf16_f32 v193, 0, v193\n"
        "v_mfma_f32_16x16x32_bf16 a[212:215], v[20:23], v[84:87], a[212:215]\n"
        "ds_read_b64_tr_b16 v[84:85], v218 offset:24576\n"
        "global_load_lds_dwordx4 v236, s[58:59] offset:2048\n"
        "v_mfma_f32_16x16x32_bf16 a[212:215], v[16:19], v[52:55], a[212:215]\n"
        "ds_read_b64_tr_b16 v[52:53], v218 offset:16384\n"
        "v_cvt_pk_bf16_f32 v195, 0, v195\n"
        "v_mfma_f32_16x16x32_bf16 a[216:219], v[16:19], v[56:59], a[216:219]\n"
        "ds_read_b64_tr_b16 v[54:55], v219 offset:16384\n"
        "v_lshlrev_b32 v194, 16, v131\n"
        "v_mfma_f32_16x16x32_bf16 a[216:219], v[20:23], v[88:91], a[216:219]\n"
        "ds_read_b64_tr_b16 v[86:87], v219 offset:24576\n"
        "v_lshlrev_b32 v196, 16, v133\n"
        "v_mfma_f32_16x16x32_bf16 a[248:251], v[28:31], v[88:91], a[248:251]\n"
        "ds_read_b64_tr_b16 v[88:89], v220 offset:24576\n"
        "global_load_lds_dwordx4 v237, s[58:59] offset:3072\n"
        "s_add_u32 m0, s73, 0x18000\n"
        "v_mfma_f32_16x16x32_bf16 a[248:251], v[24:27], v[56:59], a[248:251]\n"
        "ds_read_b64_tr_b16 v[56:57], v220 offset:16384\n"
        "s_add_u32 s58, s58, 0x80\n"
        "s_addc_u32 s59, s59, 0\n"
        "v_mfma_f32_16x16x32_bf16 a[252:255], v[24:27], v[60:63], a[252:255]\n"
        "ds_read_b64_tr_b16 v[58:59], v221 offset:16384\n"
        "v_mul_f32 v193, v193, v194\n"
        "v_mfma_f32_16x16x32_bf16 a[252:255], v[28:31], v[92:95], a[252:255]\n"
        "ds_read_b64_tr_b16 v[90:91], v221 offset:24576\n"
        "v_mul_f32 v195, v195, v196\n"
        "v_mfma_f32_16x16x32_bf16 a[220:223], v[20:23], v[92:95], a[220:223]\n"
        "ds_read_b64_tr_b16 v[92:93], v222 offset:24576\n"
        "global_load_lds_dwordx4 v238, s[60:61] offset:0\n"
        "v_mfma_f32_16x16x32_bf16 a[220:223], v[16:19], v[60:63], a[220:223]\n"
        "ds_read_b64_tr_b16 v[60:61], v222 offset:16384\n"
        "s_waitcnt lgkmcnt(8)\n"
        "v_mfma_f32_16x16x32_bf16 a[0:3], v[32:35], v[0:3], a[0:3]\n"
        "ds_read_b64_tr_b16 v[62:63], v223 offset:16384\n"
        "v_cvt_pk_bf16_f32 v200, v195, v193\n"
        "v_mfma_f32_16x16x32_bf16 a[0:3], v[64:67], v[4:7], a[0:3]\n"
        "ds_read_b64_tr_b16 v[94:95], v223 offset:24576\n"
        "v_lshlrev_b32 v185, 16, v140\n"
        "v_mfma_f32_16x16x32_bf16 a[32:35], v[64:67], v[12:15], a[32:35]\n"
        "ds_read_b128 v[16:19], v224 offset:36864\n"
        "global_load_lds_dwordx4 v239, s[60:61] offset:1024\n"
        "v_mfma_f32_16x16x32_bf16 a[32:35], v[32:35], v[8:11], a[32:35]\n"
        "ds_read_b128 v[20:23], v225 offset:36864\n"
        "v_and_b32 v186, 0xffff0000, v140\n"
        "v_mfma_f32_16x16x32_bf16 a[36:39], v[36:39], v[8:11], a[36:39]\n"
        "ds_read_b128 v[24:27], v224 offset:38912\n"
        "v_lshlrev_b32 v187, 16, v141\n"
        "v_mfma_f32_16x16x32_bf16 a[36:39], v[68:71], v[12:15], a[36:39]\n"
        "ds_read_b128 v[28:31], v225 offset:38912\n"
        "v_and_b32 v188, 0xffff0000, v141\n"
        "v_mfma_f32_16x16x32_bf16 a[4:7], v[68:71], v[4:7], a[4:7]\n"
        "global_load_lds_dwordx4 v240, s[60:61] offset:2048\n"
        "v_mfma_f32_16x16x32_bf16 a[4:7], v[36:39], v[0:3], a[4:7]\n"
        "v_and_b32 v174, 0xffff0000, v97\n"
        "v_and_b32 v175, 0xffff0000, v99\n"
        "v_mfma_f32_16x16x32_bf16 a[8:11], v[40:43], v[0:3], a[8:11]\n"
        "v_and_b32 v176, 0xffff0000, v101\n"
        "v_and_b32 v177, 0xffff0000, v103\n"
        "v_mfma_f32_16x16x32_bf16 a[8:11], v[72:75], v[4:7], a[8:11]\n"
        "s_waitcnt lgkmcnt(4)\n"
        "v_and_b32 v178, 0xffff0000, v105\n"
        "v_and_b32 v179, 0xffff0000, v107\n"
        "v_mfma_f32_16x16x32_bf16 a[40:43], v[72:75], v[12:15], a[40:43]\n"
        "global_load_lds_dwordx4 v241, s[60:61] offset:3072\n"
        "s_add_u32 m0, s73, 0x1c000\n"
        "v_mfma_f32_16x16x32_bf16 a[40:43], v[40:43], v[8:11], a[40:43]\n"
        "v_and_b32 v180, 0xffff0000, v109\n"
        "v_and_b32 v181, 0xffff0000, v111\n"
        "v_mfma_f32_16x16x32_bf16 a[44:47], v[44:47], v[8:11], a[44:47]\n"
        "LHA3_%=:\n"
        "s_cbranch_execz LHA7_%=\n"
        "v_and_b32 v182, 0xffff0000, v113\n"
        "v_and_b32 v183, 0xffff0000, v115\n"
        "v_mfma_f32_16x16x32_bf16 a[44:47], v[76:79], v[12:15], a[44:47]\n"
        "v_and_b32 v184, 0xffff0000, v117\n"
        "v_mul_f32 v189, v174, v188\n"
        "v_mfma_f32_16x16x32_bf16 a[12:15], v[76:79], v[4:7], a[12:15]\n"
        "global_load_lds_dwordx4 v238, s[62:63] offset:0\n"
        "v_mfma_f32_16x16x32_bf16 a[12:15], v[44:47], v[0:3], a[12:15]\n"
        "v_mul_f32 v191, v175, v188\n"
        "v_cvt_pk_bf16_f32 v189, 0, v189\n"
        "v_mfma_f32_16x16x32_bf16 a[16:19], v[0:3], v[48:51], a[16:19]\n"
        "v_cvt_pk_bf16_f32 v191, 0, v191\n"
        "v_mul_f32 v190, v175, v187\n"
        "v_mfma_f32_16x16x32_bf16 a[16:19], v[4:7], v[80:83], a[16:19]\n"
        "v_mul_f32 v192, v176, v187\n"
        "v_cvt_pk_bf16_f32 v190, 0, v190\n"
        "v_mfma_f32_16x16x32_bf16 a[48:51], v[12:15], v[80:83], a[48:51]\n"
        "global_load_lds_dwordx4 v239, s[62:63] offset:1024\n"
        "v_mfma_f32_16x16x32_bf16 a[48:51], v[8:11], v[48:51], a[48:51]\n"
        "v_cvt_pk_bf16_f32 v192, 0, v192\n"
        "v_add_f32 v189, v189, v190\n"
        "v_mfma_f32_16x16x32_bf16 a[52:55], v[8:11], v[52:55], a[52:55]\n"
        "v_add_f32 v191, v191, v192\n"
        "v_cvt_pk_bf16_f32 v189, 0, v189\n"
        "v_mfma_f32_16x16x32_bf16 a[52:55], v[12:15], v[84:87], a[52:55]\n"
        "s_waitcnt lgkmcnt(0)\n"
        "v_cvt_pk_bf16_f32 v191, 0, v191\n"
        "v_mul_f32 v190, v176, v186\n"
        "v_mfma_f32_16x16x32_bf16 a[20:23], v[4:7], v[84:87], a[20:23]\n"
        "global_load_lds_dwordx4 v240, s[62:63] offset:2048\n"
        "v_mfma_f32_16x16x32_bf16 a[20:23], v[0:3], v[52:55], a[20:23]\n"
        "v_mul_f32 v192, v177, v186\n"
        "v_cvt_pk_bf16_f32 v190, 0, v190\n"
        "v_mfma_f32_16x16x32_bf16 a[24:27], v[0:3], v[56:59], a[24:27]\n"
        "v_cvt_pk_bf16_f32 v192, 0, v192\n"
        "v_add_f32 v189, v189, v190\n"
        "v_mfma_f32_16x16x32_bf16 a[24:27], v[4:7], v[88:91], a[24:27]\n"
        "v_add_f32 v191, v191, v192\n"
        "v_cvt_pk_bf16_f32 v189, 0, v189\n"
        "v_mfma_f32_16x16x32_bf16 a[56:59], v[12:15], v[88:91], a[56:59]\n"
        "global_load_lds_dwordx4 v241, s[62:63] offset:3072\n"
        "s_add_u32 m0, s72, 0x8000\n"
        "v_mfma_f32_16x16x32_bf16 a[56:59], v[8:11], v[56:59], a[56:59]\n"
        "v_cvt_pk_bf16_f32 v191, 0, v191\n"
        "v_mul_f32 v190, v177, v185\n"
        "v_mfma_f32_16x16x32_bf16 a[60:63], v[8:11], v[60:63], a[60:63]\n"
        "ds_read_b128 v[8:11], v224 offset:43008\n"
        "v_mul_f32 v192, v178, v185\n"
        "v_mfma_f32_16x16x32_bf16 a[60:63], v[12:15], v[92:95], a[60:63]\n"
        "ds_read_b128 v[12:15], v225 offset:43008\n"
        "v_cvt_pk_bf16_f32 v190, 0, v190\n"
        "v_mfma_f32_16x16x32_bf16 a[28:31], v[4:7], v[92:95], a[28:31]\n"
        "ds_read_b128 v[4:7], v225 offset:40960\n"
        "v_cvt_pk_bf16_f32 v192, 0, v192\n"
        "v_mfma_f32_16x16x32_bf16 a[28:31], v[0:3], v[60:63], a[28:31]\n"
        "ds_read_b128 v[0:3], v224 offset:40960\n"
        "v_add_f32 v189, v189, v190\n"
        "v_mfma_f32_16x16x32_bf16 a[64:67], v[32:35], v[16:19], a[64:67]\n"
        "global_store_dwordx4 v171, v[200:203], s[76:77] offset:-4096 sc1\n"
        "s_waitcnt vmcnt(31)\n"
        "v_add_f32 v191, v191, v192\n"
        "v_cvt_pk_bf16_f32 v189, 0, v189\n"
        "v_cvt_pk_bf16_f32 v191, 0, v191\n"
        "v_mfma_f32_16x16x32_bf16 a[64:67], v[64:67], v[20:23], a[64:67]\n"
        "v_and_b32 v190, 0xffff0000, v119\n"
        "v_and_b32 v192, 0xffff0000, v121\n"
        "v_mfma_f32_16x16x32_bf16 a[96:99], v[64:67], v[28:31], a[96:99]\n"
        "v_mul_f32 v189, v189, v190\n"
        "v_mul_f32 v191, v191, v192\n"
        "v_mfma_f32_16x16x32_bf16 a[96:99], v[32:35], v[24:27], a[96:99]\n"
        "v_cvt_pk_bf16_f32 v211, v191, v189\n"
        "v_mul_f32 v193, v176, v188\n"
        "v_mfma_f32_16x16x32_bf16 a[100:103], v[36:39], v[24:27], a[100:103]\n"
        "v_mul_f32 v195, v177, v188\n"
        "v_cvt_pk_bf16_f32 v193, 0, v193\n"
        "v_mfma_f32_16x16x32_bf16 a[100:103], v[68:71], v[28:31], a[100:103]\n"
        "v_cvt_pk_bf16_f32 v195, 0, v195\n"
        "v_mul_f32 v194, v177, v187\n"
        "v_mfma_f32_16x16x32_bf16 a[68:71], v[68:71], v[20:23], a[68:71]\n"
        "v_mul_f32 v196, v178, v187\n"
        "v_cvt_pk_bf16_f32 v194, 0, v194\n"
        "v_mfma_f32_16x16x32_bf16 a[68:71], v[36:39], v[16:19], a[68:71]\n"
        "v_cvt_pk_bf16_f32 v196, 0, v196\n"
        "v_add_f32 v193, v193, v194\n"
        "v_mfma_f32_16x16x32_bf16 a[72:75], v[40:43], v[16:19], a[72:75]\n"
        "v_add_f32 v195, v195, v196\n"
        "v_cvt_pk_bf16_f32 v193, 0, v193\n"
        "v_mfma_f32_16x16x32_bf16 a[72:75], v[72:75], v[20:23], a[72:75]\n"
        "v_cvt_pk_bf16_f32 v195, 0, v195\n"
        "v_mul_f32 v194, v178, v186\n"
        "v_mfma_f32_16x16x32_bf16 a[104:107], v[72:75], v[28:31], a[104:107]\n"
        "v_mul_f32 v196, v179, v186\n"
        "v_cvt_pk_bf16_f32 v194, 0, v194\n"
        "v_mfma_f32_16x16x32_bf16 a[104:107], v[40:43], v[24:27], a[104:107]\n"
        "v_cvt_pk_bf16_f32 v196, 0, v196\n"
        "v_add_f32 v193, v193, v194\n"
        "v_mfma_f32_16x16x32_bf16 a[108:111], v[44:47], v[24:27], a[108:111]\n"
        "v_add_f32 v195, v195, v196\n"
        "v_cvt_pk_bf16_f32 v193, 0, v193\n"
        "v_mfma_f32_16x16x32_bf16 a[108:111], v[76:79], v[28:31], a[108:111]\n"
        "v_cvt_pk_bf16_f32 v195, 0, v195\n"
        "v_mul_f32 v194, v179, v185\n"
        "v_mfma_f32_16x16x32_bf16 a[76:79], v[76:79], v[20:23], a[76:79]\n"
        "v_mul_f32 v196, v180, v185\n"
        "v_cvt_pk_bf16_f32 v194, 0, v194\n"
        "v_mfma_f32_16x16x32_bf16 a[76:79], v[44:47], v[16:19], a[76:79]\n"
        "v_cvt_pk_bf16_f32 v196, 0, v196\n"
        "v_add_f32 v193, v193, v194\n"
        "v_mfma_f32_16x16x32_bf16 a[80:83], v[16:19], v[48:51], a[80:83]\n"
        "v_add_f32 v195, v195, v196\n"
        "v_cvt_pk_bf16_f32 v193, 0, v193\n"
        "v_mfma_f32_16x16x32_bf16 a[80:83], v[20:23], v[80:83], a[80:83]\n"
        "v_cvt_pk_bf16_f32 v195, 0, v195\n"
        "v_and_b32 v194, 0xffff0000, v123\n"
        "v_mfma_f32_16x16x32_bf16 a[112:115], v[28:31], v[80:83], a[112:115]\n"
        "v_and_b32 v196, 0xffff0000, v125\n"
        "v_mul_f32 v193, v193, v194\n"
        "v_mfma_f32_16x16x32_bf16 a[112:115], v[24:27], v[48:51], a[112:115]\n"
        "v_mul_f32 v195, v195, v196\n"
        "v_cvt_pk_bf16_f32 v210, v195, v193\n"
        "v_mfma_f32_16x16x32_bf16 a[116:119], v[24:27], v[52:55], a[116:119]\n"
        "v_mul_f32 v189, v178, v188\n"
        "v_mul_f32 v191, v179, v188\n"
        "v_mfma_f32_16x16x32_bf16 a[116:119], v[28:31], v[84:87], a[116:119]\n"
        "s_waitcnt lgkmcnt(0)\n"
        "v_cvt_pk_bf16_f32 v189, 0, v189\n"
        "v_cvt_pk_bf16_f32 v191, 0, v191\n"
        "v_mfma_f32_16x16x32_bf16 a[84:87], v[20:23], v[84:87], a[84:87]\n"
        "v_mul_f32 v190, v179, v187\n"
        "v_mul_f32 v192, v180, v187\n"
        "v_mfma_f32_16x16x32_bf16 a[84:87], v[16:19], v[52:55], a[84:87]\n"
        "global_load_dword v253, v173, s[82:83] offset:0\n"
        "s_add_u32 s82, s12, 0x600000\n"
        "s_addc_u32 s83, s13, 0\n"
        "v_mfma_f32_16x16x32_bf16 a[88:91], v[16:19], v[56:59], a[88:91]\n"
        "v_cvt_pk_bf16_f32 v190, 0, v190\n"
        "v_cvt_pk_bf16_f32 v192, 0, v192\n"
        "v_mfma_f32_16x16x32_bf16 a[88:91], v[20:23], v[88:91], a[88:91]\n"
        "v_add_f32 v189, v189, v190\n"
        "v_add_f32 v191, v191, v192\n"
        "v_mfma_f32_16x16x32_bf16 a[120:123], v[28:31], v[88:91], a[120:123]\n"
        "v_cvt_pk_bf16_f32 v189, 0, v189\n"
        "v_cvt_pk_bf16_f32 v191, 0, v191\n"
        "v_mfma_f32_16x16x32_bf16 a[120:123], v[24:27], v[56:59], a[120:123]\n"
        "v_mul_f32 v190, v180, v186\n"
        "v_mul_f32 v192, v181, v186\n"
        "v_mfma_f32_16x16x32_bf16 a[124:127], v[24:27], v[60:63], a[124:127]\n"
        "ds_read_b128 v[24:27], v224 offset:47104\n"
        "v_cvt_pk_bf16_f32 v190, 0, v190\n"
        "v_mfma_f32_16x16x32_bf16 a[124:127], v[28:31], v[92:95], a[124:127]\n"
        "ds_read_b128 v[28:31], v225 offset:47104\n"
        "v_cvt_pk_bf16_f32 v192, 0, v192\n"
        "v_mfma_f32_16x16x32_bf16 a[92:95], v[20:23], v[92:95], a[92:95]\n"
        "ds_read_b128 v[20:23], v225 offset:45056\n"
        "v_add_f32 v189, v189, v190\n"
        "v_mfma_f32_16x16x32_bf16 a[92:95], v[16:19], v[60:63], a[92:95]\n"
        "ds_read_b128 v[16:19], v224 offset:45056\n"
        "v_add_f32 v191, v191, v192\n"
        "v_mfma_f32_16x16x32_bf16 a[128:131], v[32:35], v[0:3], a[128:131]\n"
        "v_cvt_pk_bf16_f32 v189, 0, v189\n"
        "v_cvt_pk_bf16_f32 v191, 0, v191\n"
        "v_mfma_f32_16x16x32_bf16 a[128:131], v[64:67], v[4:7], a[128:131]\n"
        "v_mul_f32 v190, v181, v185\n"
        "v_mul_f32 v192, v182, v185\n"
        "v_mfma_f32_16x16x32_bf16 a[160:163], v[64:67], v[12:15], a[160:163]\n"
        "v_cvt_pk_bf16_f32 v190, 0, v190\n"
        "v_cvt_pk_bf16_f32 v192, 0, v192\n"
        "v_mfma_f32_16x16x32_bf16 a[160:163], v[32:35], v[8:11], a[160:163]\n"
        "v_add_f32 v189, v189, v190\n"
        "v_add_f32 v191, v191, v192\n"
        "v_mfma_f32_16x16x32_bf16 a[164:167], v[36:39], v[8:11], a[164:167]\n"
        "v_cvt_pk_bf16_f32 v189, 0, v189\n"
        "v_cvt_pk_bf16_f32 v191, 0, v191\n"
        "v_mfma_f32_16x16x32_bf16 a[164:167], v[68:71], v[12:15], a[164:167]\n"
        "v_and_b32 v190, 0xffff0000, v127\n"
        "v_and_b32 v192, 0xffff0000, v129\n"
        "v_mfma_f32_16x16x32_bf16 a[132:135], v[68:71], v[4:7], a[132:135]\n"
        "v_mul_f32 v189, v189, v190\n"
        "v_mul_f32 v191, v191, v192\n"
        "v_mfma_f32_16x16x32_bf16 a[132:135], v[36:39], v[0:3], a[132:135]\n"
        "v_cvt_pk_bf16_f32 v209, v191, v189\n"
        "v_mul_f32 v193, v180, v188\n"
        "v_mfma_f32_16x16x32_bf16 a[136:139], v[40:43], v[0:3], a[136:139]\n"
        "v_mul_f32 v195, v181, v188\n"
        "v_cvt_pk_bf16_f32 v193, 0, v193\n"
        "v_mfma_f32_16x16x32_bf16 a[136:139], v[72:75], v[4:7], a[136:139]\n"
        "v_cvt_pk_bf16_f32 v195, 0, v195\n"
        "v_mul_f32 v194, v181, v187\n"
        "v_mfma_f32_16x16x32_bf16 a[168:171], v[72:75], v[12:15], a[168:171]\n"
        "v_mul_f32 v196, v182, v187\n"
        "v_cvt_pk_bf16_f32 v194, 0, v194\n"
        "v_mfma_f32_16x16x32_bf16 a[168:171], v[40:43], v[8:11], a[168:171]\n"
        "v_cvt_pk_bf16_f32 v196, 0, v196\n"
        "v_add_f32 v193, v193, v194\n"
        "v_mfma_f32_16x16x32_bf16 a[172:175], v[44:47], v[8:11], a[172:175]\n"
        "v_add_f32 v195, v195, v196\n"
        "v_cvt_pk_bf16_f32 v193, 0, v193\n"
        "v_mfma_f32_16x16x32_bf16 a[172:175], v[76:79], v[12:15], a[172:175]\n"
        "v_cvt_pk_bf16_f32 v195, 0, v195\n"
        "v_mul_f32 v194, v182, v186\n"
        "v_mfma_f32_16x16x32_bf16 a[140:143], v[76:79], v[4:7], a[140:143]\n"
        "v_mul_f32 v196, v183, v186\n"
        "v_cvt_pk_bf16_f32 v194, 0, v194\n"
        "v_mfma_f32_16x16x32_bf16 a[140:143], v[44:47], v[0:3], a[140:143]\n"
        "v_cvt_pk_bf16_f32 v196, 0, v196\n"
        "v_add_f32 v193, v193, v194\n"
        "v_mfma_f32_16x16x32_bf16 a[144:147], v[0:3], v[48:51], a[144:147]\n"
        "v_add_f32 v195, v195, v196\n"
        "v_cvt_pk_bf16_f32 v193, 0, v193\n"
        "v_mfma_f32_16x16x32_bf16 a[144:147], v[4:7], v[80:83], a[144:147]\n"
        "v_cvt_pk_bf16_f32 v195, 0, v195\n"
        "v_mul_f32 v194, v183, v185\n"
        "v_mfma_f32_16x16x32_bf16 a[176:179], v[12:15], v[80:83], a[176:179]\n"
        "v_mul_f32 v196, v184, v185\n"
        "v_cvt_pk_bf16_f32 v194, 0, v194\n"
        "v_mfma_f32_16x16x32_bf16 a[176:179], v[8:11], v[48:51], a[176:179]\n"
        "v_cvt_pk_bf16_f32 v196, 0, v196\n"
        "v_add_f32 v193, v193, v194\n"
        "v_mfma_f32_16x16x32_bf16 a[180:183], v[8:11], v[52:55], a[180:183]\n"
        "v_add_f32 v195, v195, v196\n"
        "v_cvt_pk_bf16_f32 v193, 0, v193\n"
        "v_mfma_f32_16x16x32_bf16 a[180:183], v[12:15], v[84:87], a[180:183]\n"
        "s_waitcnt lgkmcnt(0)\n"
        "v_cvt_pk_bf16_f32 v195, 0, v195\n"
        "v_and_b32 v194, 0xffff0000, v131\n"
        "v_mfma_f32_16x16x32_bf16 a[148:151], v[4:7], v[84:87], a[148:151]\n"
        "v_and_b32 v196, 0xffff0000, v133\n"
        "v_mul_f32 v193, v193, v194\n"
        "v_mfma_f32_16x16x32_bf16 a[148:151], v[0:3], v[52:55], a[148:151]\n"
        "v_mul_f32 v195, v195, v196\n"
        "v_cvt_pk_bf16_f32 v208, v195, v193\n"
        "v_mfma_f32_16x16x32_bf16 a[152:155], v[0:3], v[56:59], a[152:155]\n"
        "global_store_dwordx4 v171, v[208:211], s[76:77] offset:0 sc1\n"
        "s_waitcnt vmcnt(26)\n"
        "v_readfirstlane_b32 s86, v250\n"
        "v_readfirstlane_b32 s87, v251\n"
        "v_lshlrev_b32 v174, 16, v158\n"
        "v_mfma_f32_16x16x32_bf16 a[152:155], v[4:7], v[88:91], a[152:155]\n"
        "s_add_u32 s86, s86, 0x1\n"
        "s_addc_u32 s87, s87, 0\n"
        "v_mov_b32 v250, s86\n"
        "v_mfma_f32_16x16x32_bf16 a[184:187], v[12:15], v[88:91], a[184:187]\n"
        "s_waitcnt vmcnt(20)\n"
        "v_lshl_or_b32 v172, v172, 16, v198\n"
        "v_mov_b32 v251, s87\n"
        "v_mfma_f32_16x16x32_bf16 a[184:187], v[8:11], v[56:59], a[184:187]\n"
        "s_waitcnt vmcnt(11)\n"
        "v_readfirstlane_b32 s54, v172\n"
        "v_and_b32 v175, 0xffff0000, v158\n"
        "v_mfma_f32_16x16x32_bf16 a[188:191], v[8:11], v[60:63], a[188:191]\n"
        "s_barrier\n"
        "v_mfma_f32_16x16x32_bf16 a[188:191], v[12:15], v[92:95], a[188:191]\n"
        "ds_read_b128 v[8:11], v224 offset:2048\n"
        "global_load_lds_dwordx4 v234, s[56:57] offset:0\n"
        "v_mfma_f32_16x16x32_bf16 a[156:159], v[4:7], v[92:95], a[156:159]\n"
        "ds_read_b128 v[4:7], v225 offset:0\n"
        "v_lshlrev_b32 v176, 16, v159\n"
        "v_mfma_f32_16x16x32_bf16 a[156:159], v[0:3], v[60:63], a[156:159]\n"
        "ds_read_b128 v[0:3], v224 offset:0\n"
        "v_and_b32 v177, 0xffff0000, v159\n"
        "v_mfma_f32_16x16x32_bf16 a[192:195], v[32:35], v[16:19], a[192:195]\n"
        "ds_read_b128 v[12:15], v225 offset:2048\n"
        "v_lshlrev_b32 v178, 16, v160\n"
        "v_mfma_f32_16x16x32_bf16 a[192:195], v[64:67], v[20:23], a[192:195]\n"
        "global_load_lds_dwordx4 v235, s[56:57] offset:1024\n"
        "v_mfma_f32_16x16x32_bf16 a[224:227], v[64:67], v[28:31], a[224:227]\n"
        "ds_read_b64_tr_b16 v[64:65], v226 offset:8192\n"
        "v_and_b32 v179, 0xffff0000, v160\n"
        "v_mfma_f32_16x16x32_bf16 a[224:227], v[32:35], v[24:27], a[224:227]\n"
        "ds_read_b64_tr_b16 v[32:33], v226 offset:0\n"
        "v_lshlrev_b32 v180, 16, v161\n"
        "v_mfma_f32_16x16x32_bf16 a[228:231], v[36:39], v[24:27], a[228:231]\n"
        "ds_read_b64_tr_b16 v[34:35], v227 offset:0\n"
        "v_and_b32 v181, 0xffff0000, v161\n"
        "v_mfma_f32_16x16x32_bf16 a[228:231], v[68:71], v[28:31], a[228:231]\n"
        "ds_read_b64_tr_b16 v[66:67], v227 offset:8192\n"
        "global_load_lds_dwordx4 v236, s[56:57] offset:2048\n"
        "v_mfma_f32_16x16x32_bf16 a[196:199], v[68:71], v[20:23], a[196:199]\n"
        "ds_read_b64_tr_b16 v[68:69], v228 offset:8192\n"
        "v_lshlrev_b32 v182, 16, v162\n"
        "v_mfma_f32_16x16x32_bf16 a[196:199], v[36:39], v[16:19], a[196:199]\n"
        "ds_read_b64_tr_b16 v[36:37], v228 offset:0\n"
        "v_and_b32 v183, 0xffff0000, v162\n"
        "v_mfma_f32_16x16x32_bf16 a[200:203], v[40:43], v[16:19], a[200:203]\n"
        "ds_read_b64_tr_b16 v[38:39], v229 offset:0\n"
        "v_lshlrev_b32 v184, 16, v163\n"
        "v_mfma_f32_16x16x32_bf16 a[200:203], v[72:75], v[20:23], a[200:203]\n"
        "ds_read_b64_tr_b16 v[70:71], v229 offset:8192\n"
        "global_load_lds_dwordx4 v237, s[56:57] offset:3072\n"
        "s_add_u32 m0, s72, 0x9000\n"
        "v_mfma_f32_16x16x32_bf16 a[232:235], v[72:75], v[28:31], a[232:235]\n"
        "ds_read_b64_tr_b16 v[72:73], v230 offset:8192\n"
        "v_and_b32 v185, 0xffff0000, v163\n"
        "v_mfma_f32_16x16x32_bf16 a[232:235], v[40:43], v[24:27], a[232:235]\n"
        "ds_read_b64_tr_b16 v[40:41], v230 offset:0\n"
        "v_lshlrev_b32 v186, 16, v164\n"
        "v_mfma_f32_16x16x32_bf16 a[236:239], v[44:47], v[24:27], a[236:239]\n"
        "ds_read_b64_tr_b16 v[42:43], v231 offset:0\n"
        "v_and_b32 v187, 0xffff0000, v164\n"
        "v_mfma_f32_16x16x32_bf16 a[236:239], v[76:79], v[28:31], a[236:239]\n"
        "ds_read_b64_tr_b16 v[74:75], v231 offset:8192\n"
        "global_load_lds_dwordx4 v234, s[58:59] offset:0\n"
        "v_mfma_f32_16x16x32_bf16 a[204:207], v[76:79], v[20:23], a[204:207]\n"
        "ds_read_b64_tr_b16 v[76:77], v232 offset:8192\n"
        "v_lshlrev_b32 v188, 16, v165\n"
        "v_mfma_f32_16x16x32_bf16 a[204:207], v[44:47], v[16:19], a[204:207]\n"
        "ds_read_b64_tr_b16 v[44:45], v232 offset:0\n"
        "v_and_b32 v189, 0xffff0000, v165\n"
        "v_mfma_f32_16x16x32_bf16 a[208:211], v[16:19], v[48:51], a[208:211]\n"
        "ds_read_b64_tr_b16 v[46:47], v233 offset:0\n"
        "s_add_u32 s76, s38, 0x3920040\n"
        "s_addc_u32 s77, s39, 0\n"
        "v_mfma_f32_16x16x32_bf16 a[208:211], v[20:23], v[80:83], a[208:211]\n"
        "ds_read_b64_tr_b16 v[78:79], v233 offset:8192\n"
        "global_load_lds_dwordx4 v235, s[58:59] offset:1024\n"
        "v_mfma_f32_16x16x32_bf16 a[240:243], v[28:31], v[80:83], a[240:243]\n"
        "ds_read_b64_tr_b16 v[80:81], v226 offset:24576\n"
        "v_lshlrev_b32 v192, 16, v142\n"
        "v_mfma_f32_16x16x32_bf16 a[240:243], v[24:27], v[48:51], a[240:243]\n"
        "ds_read_b64_tr_b16 v[48:49], v226 offset:16384\n"
        "v_and_b32 v193, 0xffff0000, v142\n"
        "v_mfma_f32_16x16x32_bf16 a[244:247], v[24:27], v[52:55], a[244:247]\n"
        "ds_read_b64_tr_b16 v[50:51], v227 offset:16384\n"
        "v_mul_f32 v192, v192, v174\n"
        "v_mfma_f32_16x16x32_bf16 a[244:247], v[28:31], v[84:87], a[244:247]\n"
        "ds_read_b64_tr_b16 v[82:83], v227 offset:24576\n"
        "global_load_lds_dwordx4 v236, s[58:59] offset:2048\n"
        "v_mfma_f32_16x16x32_bf16 a[212:215], v[20:23], v[84:87], a[212:215]\n"
        "ds_read_b64_tr_b16 v[84:85], v228 offset:24576\n"
        "v_mul_f32 v193, v193, v175\n"
        "v_mfma_f32_16x16x32_bf16 a[212:215], v[16:19], v[52:55], a[212:215]\n"
        "ds_read_b64_tr_b16 v[52:53], v228 offset:16384\n"
        "v_cvt_pk_bf16_f32 v192, 0, v192\n"
        "v_mfma_f32_16x16x32_bf16 a[216:219], v[16:19], v[56:59], a[216:219]\n"
        "ds_read_b64_tr_b16 v[54:55], v229 offset:16384\n"
        "v_cvt_pk_bf16_f32 v193, 0, v193\n"
        "v_mfma_f32_16x16x32_bf16 a[216:219], v[20:23], v[88:91], a[216:219]\n"
        "ds_read_b64_tr_b16 v[86:87], v229 offset:24576\n"
        "global_load_lds_dwordx4 v237, s[58:59] offset:3072\n"
        "s_add_u32 m0, s73, 0x20000\n"
        "v_mfma_f32_16x16x32_bf16 a[248:251], v[28:31], v[88:91], a[248:251]\n"
        "ds_read_b64_tr_b16 v[88:89], v230 offset:24576\n"
        "v_add_f32 v190, v192, v193\n"
        "v_mfma_f32_16x16x32_bf16 a[248:251], v[24:27], v[56:59], a[248:251]\n"
        "ds_read_b64_tr_b16 v[56:57], v230 offset:16384\n"
        "v_lshlrev_b32 v194, 16, v143\n"
        "v_mfma_f32_16x16x32_bf16 a[252:255], v[24:27], v[60:63], a[252:255]\n"
        "ds_read_b64_tr_b16 v[58:59], v231 offset:16384\n"
        "v_and_b32 v195, 0xffff0000, v143\n"
        "v_mfma_f32_16x16x32_bf16 a[252:255], v[28:31], v[92:95], a[252:255]\n"
        "ds_read_b64_tr_b16 v[90:91], v231 offset:24576\n"
        "global_load_lds_dwordx4 v238, s[68:69] offset:0\n"
        "v_mfma_f32_16x16x32_bf16 a[220:223], v[20:23], v[92:95], a[220:223]\n"
        "ds_read_b64_tr_b16 v[92:93], v232 offset:24576\n"
        "v_mul_f32 v194, v194, v176\n"
        "v_mfma_f32_16x16x32_bf16 a[220:223], v[16:19], v[60:63], a[220:223]\n"
        "ds_read_b64_tr_b16 v[60:61], v232 offset:16384\n"
        "s_waitcnt lgkmcnt(8)\n"
        "v_mfma_f32_16x16x32_bf16 a[0:3], v[32:35], v[0:3], a[0:3]\n"
        "ds_read_b64_tr_b16 v[62:63], v233 offset:16384\n"
        "v_mul_f32 v195, v195, v177\n"
        "v_mfma_f32_16x16x32_bf16 a[0:3], v[64:67], v[4:7], a[0:3]\n"
        "ds_read_b64_tr_b16 v[94:95], v233 offset:24576\n"
        "global_load_lds_dwordx4 v239, s[68:69] offset:1024\n"
        "v_mfma_f32_16x16x32_bf16 a[32:35], v[64:67], v[12:15], a[32:35]\n"
        "ds_read_b128 v[16:19], v224 offset:4096\n"
        "v_cvt_pk_bf16_f32 v194, 0, v194\n"
        "v_mfma_f32_16x16x32_bf16 a[32:35], v[32:35], v[8:11], a[32:35]\n"
        "ds_read_b128 v[20:23], v225 offset:4096\n"
        "v_cvt_pk_bf16_f32 v195, 0, v195\n"
        "v_mfma_f32_16x16x32_bf16 a[36:39], v[36:39], v[8:11], a[36:39]\n"
        "ds_read_b128 v[24:27], v224 offset:6144\n"
        "v_add_f32 v194, v194, v195\n"
        "v_mfma_f32_16x16x32_bf16 a[36:39], v[68:71], v[12:15], a[36:39]\n"
        "ds_read_b128 v[28:31], v225 offset:6144\n"
        "global_load_lds_dwordx4 v240, s[68:69] offset:2048\n"
        "v_mfma_f32_16x16x32_bf16 a[4:7], v[68:71], v[4:7], a[4:7]\n"
        "v_add_f32 v190, v190, v194\n"
        "v_lshlrev_b32 v192, 16, v144\n"
        "v_mfma_f32_16x16x32_bf16 a[4:7], v[36:39], v[0:3], a[4:7]\n"
        "v_and_b32 v193, 0xffff0000, v144\n"
        "v_mul_f32 v192, v192, v178\n"
        "v_mfma_f32_16x16x32_bf16 a[8:11], v[40:43], v[0:3], a[8:11]\n"
        "v_mul_f32 v193, v193, v179\n"
        "v_cvt_pk_bf16_f32 v192, 0, v192\n"
        "v_mfma_f32_16x16x32_bf16 a[8:11], v[72:75], v[4:7], a[8:11]\n"
        "global_load_dword v253, v173, s[82:83] offset:0\n"
        "v_mfma_f32_16x16x32_bf16 a[40:43], v[72:75], v[12:15], a[40:43]\n"
        "global_load_lds_dwordx4 v241, s[68:69] offset:3072\n"
        "s_add_u32 s68, s68, 0x80000\n"
        "s_addc_u32 s69, s69, 0\n"
        "v_mfma_f32_16x16x32_bf16 a[40:43], v[40:43], v[8:11], a[40:43]\n"
        "s_add_u32 m0, s73, 0x24000\n"
        "s_add_u32 s82, s12, 0x700000\n"
        "s_addc_u32 s83, s13, 0\n"
        "v_mfma_f32_16x16x32_bf16 a[44:47], v[44:47], v[8:11], a[44:47]\n"
        "s_waitcnt lgkmcnt(1)\n"
        "v_cvt_pk_bf16_f32 v193, 0, v193\n"
        "v_add_f32 v192, v192, v193\n"
        "v_mfma_f32_16x16x32_bf16 a[44:47], v[76:79], v[12:15], a[44:47]\n"
        "v_add_f32 v190, v190, v192\n"
        "v_lshlrev_b32 v194, 16, v145\n"
        "v_mfma_f32_16x16x32_bf16 a[12:15], v[76:79], v[4:7], a[12:15]\n"
        "global_load_lds_dwordx4 v238, s[68:69] offset:0\n"
        "v_and_b32 v195, 0xffff0000, v145\n"
        "v_mfma_f32_16x16x32_bf16 a[12:15], v[44:47], v[0:3], a[12:15]\n"
        "v_mul_f32 v194, v194, v180\n"
        "v_mul_f32 v195, v195, v181\n"
        "v_mfma_f32_16x16x32_bf16 a[16:19], v[0:3], v[48:51], a[16:19]\n"
        "v_cvt_pk_bf16_f32 v194, 0, v194\n"
        "v_cvt_pk_bf16_f32 v195, 0, v195\n"
        "v_mfma_f32_16x16x32_bf16 a[16:19], v[4:7], v[80:83], a[16:19]\n"
        "v_add_f32 v194, v194, v195\n"
        "v_add_f32 v190, v190, v194\n"
        "v_mfma_f32_16x16x32_bf16 a[48:51], v[12:15], v[80:83], a[48:51]\n"
        "global_load_lds_dwordx4 v239, s[68:69] offset:1024\n"
        "v_lshlrev_b32 v192, 16, v146\n"
        "v_mfma_f32_16x16x32_bf16 a[48:51], v[8:11], v[48:51], a[48:51]\n"
        "v_and_b32 v193, 0xffff0000, v146\n"
        "v_mul_f32 v192, v192, v182\n"
        "v_mfma_f32_16x16x32_bf16 a[52:55], v[8:11], v[52:55], a[52:55]\n"
        "v_mul_f32 v193, v193, v183\n"
        "v_cvt_pk_bf16_f32 v192, 0, v192\n"
        "v_mfma_f32_16x16x32_bf16 a[52:55], v[12:15], v[84:87], a[52:55]\n"
        "v_cvt_pk_bf16_f32 v193, 0, v193\n"
        "v_add_f32 v192, v192, v193\n"
        "v_mfma_f32_16x16x32_bf16 a[20:23], v[4:7], v[84:87], a[20:23]\n"
        "global_load_lds_dwordx4 v240, s[68:69] offset:2048\n"
        "v_add_f32 v190, v190, v192\n"
        "v_mfma_f32_16x16x32_bf16 a[20:23], v[0:3], v[52:55], a[20:23]\n"
        "s_waitcnt lgkmcnt(0)\n"
        "v_lshlrev_b32 v194, 16, v147\n"
        "v_and_b32 v195, 0xffff0000, v147\n"
        "v_mfma_f32_16x16x32_bf16 a[24:27], v[0:3], v[56:59], a[24:27]\n"
        "s_and_b32 s53, s40, 1\n"
        "v_mul_f32 v194, v194, v184\n"
        "v_mul_f32 v195, v195, v185\n"
        "v_mfma_f32_16x16x32_bf16 a[24:27], v[4:7], v[88:91], a[24:27]\n"
        "s_lshl_b32 s53, s53, 10\n"
        "v_cvt_pk_bf16_f32 v194, 0, v194\n"
        "v_cvt_pk_bf16_f32 v195, 0, v195\n"
        "v_mfma_f32_16x16x32_bf16 a[56:59], v[12:15], v[88:91], a[56:59]\n"
        "global_load_lds_dwordx4 v241, s[68:69] offset:3072\n"
        "s_add_u32 s68, s68, 0x80000\n"
        "s_addc_u32 s69, s69, 0\n"
        "v_mfma_f32_16x16x32_bf16 a[56:59], v[8:11], v[56:59], a[56:59]\n"
        "s_add_u32 m0, s72, 0x0\n"
        "v_add_f32 v194, v194, v195\n"
        "v_add_f32 v190, v190, v194\n"
        "v_mfma_f32_16x16x32_bf16 a[60:63], v[8:11], v[60:63], a[60:63]\n"
        "ds_read_b128 v[8:11], v224 offset:10240\n"
        "s_sub_u32 s53, 0x8000, s53\n"
        "v_lshlrev_b32 v192, 16, v148\n"
        "v_mfma_f32_16x16x32_bf16 a[60:63], v[12:15], v[92:95], a[60:63]\n"
        "ds_read_b128 v[12:15], v225 offset:10240\n"
        "v_and_b32 v193, 0xffff0000, v148\n"
        "v_mfma_f32_16x16x32_bf16 a[28:31], v[4:7], v[92:95], a[28:31]\n"
        "ds_read_b128 v[4:7], v225 offset:8192\n"
        "v_mul_f32 v192, v192, v186\n"
        "v_mfma_f32_16x16x32_bf16 a[28:31], v[0:3], v[60:63], a[28:31]\n"
        "ds_read_b128 v[0:3], v224 offset:8192\n"
        "v_mul_f32 v193, v193, v187\n"
        "v_mfma_f32_16x16x32_bf16 a[64:67], v[32:35], v[16:19], a[64:67]\n"
        "v_cvt_pk_bf16_f32 v192, 0, v192\n"
        "v_cvt_pk_bf16_f32 v193, 0, v193\n"
        "v_mfma_f32_16x16x32_bf16 a[64:67], v[64:67], v[20:23], a[64:67]\n"
        "v_add_f32 v192, v192, v193\n"
        "v_add_f32 v190, v190, v192\n"
        "v_mfma_f32_16x16x32_bf16 a[96:99], v[64:67], v[28:31], a[96:99]\n"
        "v_lshlrev_b32 v194, 16, v149\n"
        "v_and_b32 v195, 0xffff0000, v149\n"
        "v_mfma_f32_16x16x32_bf16 a[96:99], v[32:35], v[24:27], a[96:99]\n"
        "v_mul_f32 v194, v194, v188\n"
        "v_mul_f32 v195, v195, v189\n"
        "v_mfma_f32_16x16x32_bf16 a[100:103], v[36:39], v[24:27], a[100:103]\n"
        "v_cvt_pk_bf16_f32 v194, 0, v194\n"
        "v_cvt_pk_bf16_f32 v195, 0, v195\n"
        "v_mfma_f32_16x16x32_bf16 a[100:103], v[68:71], v[28:31], a[100:103]\n"
        "v_add_f32 v194, v194, v195\n"
        "v_add_f32 v190, v190, v194\n"
        "v_mfma_f32_16x16x32_bf16 a[68:71], v[68:71], v[20:23], a[68:71]\n"
        "v_lshlrev_b32 v192, 16, v150\n"
        "v_and_b32 v193, 0xffff0000, v150\n"
        "v_mfma_f32_16x16x32_bf16 a[68:71], v[36:39], v[16:19], a[68:71]\n"
        "v_mul_f32 v192, v192, v174\n"
        "v_mul_f32 v193, v193, v175\n"
        "v_mfma_f32_16x16x32_bf16 a[72:75], v[40:43], v[16:19], a[72:75]\n"
        "v_cvt_pk_bf16_f32 v192, 0, v192\n"
        "v_cvt_pk_bf16_f32 v193, 0, v193\n"
        "v_mfma_f32_16x16x32_bf16 a[72:75], v[72:75], v[20:23], a[72:75]\n"
        "v_add_f32 v191, v192, v193\n"
        "v_lshlrev_b32 v194, 16, v151\n"
        "v_mfma_f32_16x16x32_bf16 a[104:107], v[72:75], v[28:31], a[104:107]\n"
        "v_and_b32 v195, 0xffff0000, v151\n"
        "v_mul_f32 v194, v194, v176\n"
        "v_mfma_f32_16x16x32_bf16 a[104:107], v[40:43], v[24:27], a[104:107]\n"
        "v_mul_f32 v195, v195, v177\n"
        "v_cvt_pk_bf16_f32 v194, 0, v194\n"
        "v_mfma_f32_16x16x32_bf16 a[108:111], v[44:47], v[24:27], a[108:111]\n"
        "v_cvt_pk_bf16_f32 v195, 0, v195\n"
        "v_add_f32 v194, v194, v195\n"
        "v_mfma_f32_16x16x32_bf16 a[108:111], v[76:79], v[28:31], a[108:111]\n"
        "v_add_f32 v191, v191, v194\n"
        "v_lshlrev_b32 v192, 16, v152\n"
        "v_mfma_f32_16x16x32_bf16 a[76:79], v[76:79], v[20:23], a[76:79]\n"
        "v_and_b32 v193, 0xffff0000, v152\n"
        "v_mul_f32 v192, v192, v178\n"
        "v_mfma_f32_16x16x32_bf16 a[76:79], v[44:47], v[16:19], a[76:79]\n"
        "v_mul_f32 v193, v193, v179\n"
        "v_cvt_pk_bf16_f32 v192, 0, v192\n"
        "v_mfma_f32_16x16x32_bf16 a[80:83], v[16:19], v[48:51], a[80:83]\n"
        "v_cvt_pk_bf16_f32 v193, 0, v193\n"
        "v_add_f32 v192, v192, v193\n"
        "v_mfma_f32_16x16x32_bf16 a[80:83], v[20:23], v[80:83], a[80:83]\n"
        "v_add_f32 v191, v191, v192\n"
        "v_lshlrev_b32 v194, 16, v153\n"
        "v_mfma_f32_16x16x32_bf16 a[112:115], v[28:31], v[80:83], a[112:115]\n"
        "v_and_b32 v195, 0xffff0000, v153\n"
        "v_mul_f32 v194, v194, v180\n"
        "v_mfma_f32_16x16x32_bf16 a[112:115], v[24:27], v[48:51], a[112:115]\n"
        "v_mul_f32 v195, v195, v181\n"
        "v_cvt_pk_bf16_f32 v194, 0, v194\n"
        "v_mfma_f32_16x16x32_bf16 a[116:119], v[24:27], v[52:55], a[116:119]\n"
        "v_cvt_pk_bf16_f32 v195, 0, v195\n"
        "v_add_f32 v194, v194, v195\n"
        "v_mfma_f32_16x16x32_bf16 a[116:119], v[28:31], v[84:87], a[116:119]\n"
        "s_waitcnt lgkmcnt(0)\n"
        "v_add_f32 v191, v191, v194\n"
        "v_lshlrev_b32 v192, 16, v154\n"
        "v_mfma_f32_16x16x32_bf16 a[84:87], v[20:23], v[84:87], a[84:87]\n"
        "v_and_b32 v193, 0xffff0000, v154\n"
        "v_mul_f32 v192, v192, v182\n"
        "v_mfma_f32_16x16x32_bf16 a[84:87], v[16:19], v[52:55], a[84:87]\n"
        "s_mov_b32 s88, 0x3e0293ee\n"
        "v_mul_f32 v193, v193, v183\n"
        "v_cvt_pk_bf16_f32 v192, 0, v192\n"
        "v_mfma_f32_16x16x32_bf16 a[88:91], v[16:19], v[56:59], a[88:91]\n"
        "s_cmp_ge_u32 s40, 2\n"
        "s_cselect_b32 s50, s45, s44\n"
        "v_cvt_pk_bf16_f32 v193, 0, v193\n"
        "v_mfma_f32_16x16x32_bf16 a[88:91], v[20:23], v[88:91], a[88:91]\n"
        "s_lshl_b32 s50, s50, 15\n"
        "v_add_f32 v192, v192, v193\n"
        "v_add_f32 v191, v191, v192\n"
        "v_mfma_f32_16x16x32_bf16 a[120:123], v[28:31], v[88:91], a[120:123]\n"
        "s_lshl_b32 s51, s43, 19\n"
        "v_lshlrev_b32 v194, 16, v155\n"
        "v_and_b32 v195, 0xffff0000, v155\n"
        "v_mfma_f32_16x16x32_bf16 a[120:123], v[24:27], v[56:59], a[120:123]\n"
        "s_add_u32 s50, s50, s51\n"
        "v_mul_f32 v194, v194, v184\n"
        "v_mul_f32 v195, v195, v185\n"
        "v_mfma_f32_16x16x32_bf16 a[124:127], v[24:27], v[60:63], a[124:127]\n"
        "ds_read_b128 v[24:27], v224 offset:14336\n"
        "s_and_b32 s51, s40, 1\n"
        "v_cvt_pk_bf16_f32 v194, 0, v194\n"
        "v_mfma_f32_16x16x32_bf16 a[124:127], v[28:31], v[92:95], a[124:127]\n"
        "ds_read_b128 v[28:31], v225 offset:14336\n"
        "s_lshl_b32 s51, s51, 11\n"
        "v_cvt_pk_bf16_f32 v195, 0, v195\n"
        "v_mfma_f32_16x16x32_bf16 a[92:95], v[20:23], v[92:95], a[92:95]\n"
        "ds_read_b128 v[20:23], v225 offset:12288\n"
        "s_add_u32 s52, s50, s51\n"
        "v_add_f32 v194, v194, v195\n"
        "v_mfma_f32_16x16x32_bf16 a[92:95], v[16:19], v[60:63], a[92:95]\n"
        "ds_read_b128 v[16:19], v224 offset:12288\n"
        "v_add_f32 v191, v191, v194\n"
        "v_mfma_f32_16x16x32_bf16 a[128:131], v[32:35], v[0:3], a[128:131]\n"
        "v_lshlrev_b32 v192, 16, v156\n"
        "v_and_b32 v193, 0xffff0000, v156\n"
        "v_mfma_f32_16x16x32_bf16 a[128:131], v[64:67], v[4:7], a[128:131]\n"
        "v_mul_f32 v192, v192, v186\n"
        "v_mul_f32 v193, v193, v187\n"
        "v_mfma_f32_16x16x32_bf16 a[160:163], v[64:67], v[12:15], a[160:163]\n"
        "v_cvt_pk_bf16_f32 v192, 0, v192\n"
        "v_cvt_pk_bf16_f32 v193, 0, v193\n"
        "v_mfma_f32_16x16x32_bf16 a[160:163], v[32:35], v[8:11], a[160:163]\n"
        "v_add_f32 v192, v192, v193\n"
        "v_add_f32 v191, v191, v192\n"
        "v_mfma_f32_16x16x32_bf16 a[164:167], v[36:39], v[8:11], a[164:167]\n"
        "v_lshlrev_b32 v194, 16, v157\n"
        "v_and_b32 v195, 0xffff0000, v157\n"
        "v_mfma_f32_16x16x32_bf16 a[164:167], v[68:71], v[12:15], a[164:167]\n"
        "v_mul_f32 v194, v194, v188\n"
        "v_mul_f32 v195, v195, v189\n"
        "v_mfma_f32_16x16x32_bf16 a[132:135], v[68:71], v[4:7], a[132:135]\n"
        "v_cvt_pk_bf16_f32 v194, 0, v194\n"
        "v_cvt_pk_bf16_f32 v195, 0, v195\n"
        "v_mfma_f32_16x16x32_bf16 a[132:135], v[36:39], v[0:3], a[132:135]\n"
        "v_add_f32 v194, v194, v195\n"
        "v_add_f32 v191, v191, v194\n"
        "v_mfma_f32_16x16x32_bf16 a[136:139], v[40:43], v[0:3], a[136:139]\n"
        "v_add_f32_dpp v190, v190, v190 row_shr:1 row_mask:0xf bank_mask:0xf bound_ctrl:1\n"
        "v_add_f32_dpp v191, v191, v191 row_shr:1 row_mask:0xf bank_mask:0xf bound_ctrl:1\n"
        "v_mfma_f32_16x16x32_bf16 a[136:139], v[72:75], v[4:7], a[136:139]\n"
        "v_add_f32_dpp v190, v190, v190 row_shr:2 row_mask:0xf bank_mask:0xf bound_ctrl:1\n"
        "v_add_f32_dpp v191, v191, v191 row_shr:2 row_mask:0xf bank_mask:0xf bound_ctrl:1\n"
        "v_mfma_f32_16x16x32_bf16 a[168:171], v[72:75], v[12:15], a[168:171]\n"
        "v_add_f32_dpp v190, v190, v190 row_shr:4 row_mask:0xf bank_mask:0xf bound_ctrl:1\n"
        "v_add_f32_dpp v191, v191, v191 row_shr:4 row_mask:0xf bank_mask:0xf bound_ctrl:1\n"
        "v_mfma_f32_16x16x32_bf16 a[168:171], v[40:43], v[8:11], a[168:171]\n"
        "v_add_f32_dpp v190, v190, v190 row_shr:8 row_mask:0xf bank_mask:0xf bound_ctrl:1\n"
        "v_add_f32_dpp v191, v191, v191 row_shr:8 row_mask:0xf bank_mask:0xf bound_ctrl:1\n"
        "v_mfma_f32_16x16x32_bf16 a[172:175], v[44:47], v[8:11], a[172:175]\n"
        "LHA4_%=:\n"
        "s_cbranch_execz LHA8_%=\n"
        "v_add_f32_dpp v190, v190, v190 row_bcast:15 row_mask:0xa bank_mask:0xf bound_ctrl:1\n"
        "v_add_f32_dpp v191, v191, v191 row_bcast:15 row_mask:0xa bank_mask:0xf bound_ctrl:1\n"
        "v_mfma_f32_16x16x32_bf16 a[172:175], v[76:79], v[12:15], a[172:175]\n"
        "v_add_f32_dpp v190, v190, v190 row_bcast:31 row_mask:0xc bank_mask:0xf bound_ctrl:1\n"
        "v_add_f32_dpp v191, v191, v191 row_bcast:31 row_mask:0xc bank_mask:0xf bound_ctrl:1\n"
        "v_mfma_f32_16x16x32_bf16 a[140:143], v[76:79], v[4:7], a[140:143]\n"
        "v_cvt_pk_bf16_f32 v190, 0, v190\n"
        "v_mul_f32 v190, 0xbfb8aa3b, v190\n"
        "v_mfma_f32_16x16x32_bf16 a[140:143], v[44:47], v[0:3], a[140:143]\n"
        "v_exp_f32 v190, v190\n"
        "v_mfma_f32_16x16x32_bf16 a[144:147], v[0:3], v[48:51], a[144:147]\n"
        "v_add_f32 v190, 1.0, v190\n"
        "v_lshrrev_b32 v192, 2, v254\n"
        "v_mfma_f32_16x16x32_bf16 a[144:147], v[4:7], v[80:83], a[144:147]\n"
        "v_rcp_f32 v190, v190\n"
        "v_mfma_f32_16x16x32_bf16 a[176:179], v[12:15], v[80:83], a[176:179]\n"
        "v_cvt_pk_bf16_f32 v190, 0, v190\n"
        "v_add_u32 v192, s80, v192\n"
        "v_mfma_f32_16x16x32_bf16 a[176:179], v[8:11], v[48:51], a[176:179]\n"
        "v_readlane_b32 s84, v190, 63\n"
        "v_cvt_pk_bf16_f32 v191, 0, v191\n"
        "v_mfma_f32_16x16x32_bf16 a[180:183], v[8:11], v[52:55], a[180:183]\n"
        "v_mov_b32 v190, s84\n"
        "v_mul_f32 v191, 0xbfb8aa3b, v191\n"
        "v_mfma_f32_16x16x32_bf16 a[180:183], v[12:15], v[84:87], a[180:183]\n"
        "s_mov_b64 exec, 0xffff\n"
        "global_store_dword v192, v190, s[76:77] sc1\n"
        "s_mov_b64 exec, -1\n"
        "v_mfma_f32_16x16x32_bf16 a[148:151], v[4:7], v[84:87], a[148:151]\n"
        "s_waitcnt lgkmcnt(0)\n"
        "v_exp_f32 v191, v191\n"
        "v_mfma_f32_16x16x32_bf16 a[148:151], v[0:3], v[52:55], a[148:151]\n"
        "global_load_dword v253, v173, s[82:83] offset:0\n"
        "v_add_f32 v191, 1.0, v191\n"
        "v_mfma_f32_16x16x32_bf16 a[152:155], v[0:3], v[56:59], a[152:155]\n"
        "v_rcp_f32 v191, v191\n"
        "v_mfma_f32_16x16x32_bf16 a[152:155], v[4:7], v[88:91], a[152:155]\n"
        "v_cvt_pk_bf16_f32 v191, 0, v191\n"
        "v_lshrrev_b32 v193, 2, v254\n"
        "v_mfma_f32_16x16x32_bf16 a[184:187], v[12:15], v[88:91], a[184:187]\n"
        "v_readlane_b32 s85, v191, 63\n"
        "v_add_u32 v193, s81, v193\n"
        "v_mfma_f32_16x16x32_bf16 a[184:187], v[8:11], v[56:59], a[184:187]\n"
        "s_waitcnt vmcnt(11)\n"
        "v_mov_b32 v191, s85\n"
        "v_mfma_f32_16x16x32_bf16 a[188:191], v[8:11], v[60:63], a[188:191]\n"
        "s_barrier\n"
        "v_mfma_f32_16x16x32_bf16 a[188:191], v[12:15], v[92:95], a[188:191]\n"
        "ds_read_b128 v[8:11], v224 offset:34816\n"
        "global_load_lds_dwordx4 v242, s[64:65] offset:0\n"
        "v_mfma_f32_16x16x32_bf16 a[156:159], v[4:7], v[92:95], a[156:159]\n"
        "ds_read_b128 v[4:7], v225 offset:32768\n"
        "v_mfma_f32_16x16x32_bf16 a[156:159], v[0:3], v[60:63], a[156:159]\n"
        "ds_read_b128 v[0:3], v224 offset:32768\n"
        "v_mfma_f32_16x16x32_bf16 a[192:195], v[32:35], v[16:19], a[192:195]\n"
        "ds_read_b128 v[12:15], v225 offset:34816\n"
        "v_mfma_f32_16x16x32_bf16 a[192:195], v[64:67], v[20:23], a[192:195]\n"
        "global_load_lds_dwordx4 v243, s[64:65] offset:1024\n"
        "v_mfma_f32_16x16x32_bf16 a[224:227], v[64:67], v[28:31], a[224:227]\n"
        "ds_read_b64_tr_b16 v[64:65], v226 offset:40960\n"
        "v_mfma_f32_16x16x32_bf16 a[224:227], v[32:35], v[24:27], a[224:227]\n"
        "ds_read_b64_tr_b16 v[32:33], v226 offset:32768\n"
        "s_add_u32 s97, s52, 0x7000\n"
        "v_mfma_f32_16x16x32_bf16 a[228:231], v[36:39], v[24:27], a[228:231]\n"
        "ds_read_b64_tr_b16 v[34:35], v227 offset:32768\n"
        "s_add_u32 s70, s38, s97\n"
        "s_addc_u32 s71, s39, 0\n"
        "v_mfma_f32_16x16x32_bf16 a[228:231], v[68:71], v[28:31], a[228:231]\n"
        "ds_read_b64_tr_b16 v[66:67], v227 offset:40960\n"
        "global_load_lds_dwordx4 v244, s[64:65] offset:2048\n"
        "v_mfma_f32_16x16x32_bf16 a[196:199], v[68:71], v[20:23], a[196:199]\n"
        "ds_read_b64_tr_b16 v[68:69], v228 offset:40960\n"
        "v_mfma_f32_16x16x32_bf16 a[196:199], v[36:39], v[16:19], a[196:199]\n"
        "ds_read_b64_tr_b16 v[36:37], v228 offset:32768\n"
        "v_mfma_f32_16x16x32_bf16 a[200:203], v[40:43], v[16:19], a[200:203]\n"
        "ds_read_b64_tr_b16 v[38:39], v229 offset:32768\n"
        "v_mfma_f32_16x16x32_bf16 a[200:203], v[72:75], v[20:23], a[200:203]\n"
        "ds_read_b64_tr_b16 v[70:71], v229 offset:40960\n"
        "global_load_lds_dwordx4 v245, s[64:65] offset:3072\n"
        "s_add_u32 m0, s72, 0x1000\n"
        "v_mfma_f32_16x16x32_bf16 a[232:235], v[72:75], v[28:31], a[232:235]\n"
        "ds_read_b64_tr_b16 v[72:73], v230 offset:40960\n"
        "s_add_u32 s64, s64, 0x80\n"
        "s_addc_u32 s65, s65, 0\n"
        "v_mfma_f32_16x16x32_bf16 a[232:235], v[40:43], v[24:27], a[232:235]\n"
        "ds_read_b64_tr_b16 v[40:41], v230 offset:32768\n"
        "v_mfma_f32_16x16x32_bf16 a[236:239], v[44:47], v[24:27], a[236:239]\n"
        "ds_read_b64_tr_b16 v[42:43], v231 offset:32768\n"
        "v_mfma_f32_16x16x32_bf16 a[236:239], v[76:79], v[28:31], a[236:239]\n"
        "ds_read_b64_tr_b16 v[74:75], v231 offset:40960\n"
        "global_load_lds_dwordx4 v242, s[66:67] offset:0\n"
        "v_mfma_f32_16x16x32_bf16 a[204:207], v[76:79], v[20:23], a[204:207]\n"
        "ds_read_b64_tr_b16 v[76:77], v232 offset:40960\n"
        "v_mfma_f32_16x16x32_bf16 a[204:207], v[44:47], v[16:19], a[204:207]\n"
        "ds_read_b64_tr_b16 v[44:45], v232 offset:32768\n"
        "v_mfma_f32_16x16x32_bf16 a[208:211], v[16:19], v[48:51], a[208:211]\n"
        "ds_read_b64_tr_b16 v[46:47], v233 offset:32768\n"
        "v_mfma_f32_16x16x32_bf16 a[208:211], v[20:23], v[80:83], a[208:211]\n"
        "ds_read_b64_tr_b16 v[78:79], v233 offset:40960\n"
        "global_load_lds_dwordx4 v243, s[66:67] offset:1024\n"
        "v_mfma_f32_16x16x32_bf16 a[240:243], v[28:31], v[80:83], a[240:243]\n"
        "ds_read_b64_tr_b16 v[80:81], v226 offset:57344\n"
        "v_mfma_f32_16x16x32_bf16 a[240:243], v[24:27], v[48:51], a[240:243]\n"
        "ds_read_b64_tr_b16 v[48:49], v226 offset:49152\n"
        "v_add_u32 v226, s53, v226\n"
        "v_mfma_f32_16x16x32_bf16 a[244:247], v[24:27], v[52:55], a[244:247]\n"
        "ds_read_b64_tr_b16 v[50:51], v227 offset:49152\n"
        "v_mfma_f32_16x16x32_bf16 a[244:247], v[28:31], v[84:87], a[244:247]\n"
        "ds_read_b64_tr_b16 v[82:83], v227 offset:57344\n"
        "global_load_lds_dwordx4 v244, s[66:67] offset:2048\n"
        "v_mfma_f32_16x16x32_bf16 a[212:215], v[20:23], v[84:87], a[212:215]\n"
        "ds_read_b64_tr_b16 v[84:85], v228 offset:57344\n"
        "v_add_u32 v227, s53, v227\n"
        "v_mfma_f32_16x16x32_bf16 a[212:215], v[16:19], v[52:55], a[212:215]\n"
        "ds_read_b64_tr_b16 v[52:53], v228 offset:49152\n"
        "v_add_u32 v228, s53, v228\n"
        "v_mfma_f32_16x16x32_bf16 a[216:219], v[16:19], v[56:59], a[216:219]\n"
        "ds_read_b64_tr_b16 v[54:55], v229 offset:49152\n"
        "s_waitcnt lgkmcnt(8)\n"
        "v_mfma_f32_16x16x32_bf16 a[216:219], v[20:23], v[88:91], a[216:219]\n"
        "ds_read_b64_tr_b16 v[86:87], v229 offset:57344\n"
        "global_load_lds_dwordx4 v245, s[66:67] offset:3072\n"
        "s_add_u32 m0, s72, 0x10000\n"
        "v_mfma_f32_16x16x32_bf16 a[248:251], v[28:31], v[88:91], a[248:251]\n"
        "ds_read_b64_tr_b16 v[88:89], v230 offset:57344\n"
        "s_add_u32 s66, s66, 0x80\n"
        "s_addc_u32 s67, s67, 0\n"
        "v_mfma_f32_16x16x32_bf16 a[248:251], v[24:27], v[56:59], a[248:251]\n"
        "ds_read_b64_tr_b16 v[56:57], v230 offset:49152\n"
        "v_add_u32 v229, s53, v229\n"
        "v_mfma_f32_16x16x32_bf16 a[252:255], v[24:27], v[60:63], a[252:255]\n"
        "ds_read_b64_tr_b16 v[58:59], v231 offset:49152\n"
        "v_add_u32 v230, s53, v230\n"
        "v_mfma_f32_16x16x32_bf16 a[252:255], v[28:31], v[92:95], a[252:255]\n"
        "ds_read_b64_tr_b16 v[90:91], v231 offset:57344\n"
        "global_load_lds_dwordx4 v242, s[64:65] offset:0\n"
        "v_mfma_f32_16x16x32_bf16 a[220:223], v[20:23], v[92:95], a[220:223]\n"
        "ds_read_b64_tr_b16 v[92:93], v232 offset:57344\n"
        "v_add_u32 v231, s53, v231\n"
        "v_mfma_f32_16x16x32_bf16 a[220:223], v[16:19], v[60:63], a[220:223]\n"
        "ds_read_b64_tr_b16 v[60:61], v232 offset:49152\n"
        "v_add_u32 v232, s53, v232\n"
        "v_mfma_f32_16x16x32_bf16 a[0:3], v[32:35], v[0:3], a[0:3]\n"
        "ds_read_b64_tr_b16 v[62:63], v233 offset:49152\n"
        "v_mfma_f32_16x16x32_bf16 a[0:3], v[64:67], v[4:7], a[0:3]\n"
        "ds_read_b64_tr_b16 v[94:95], v233 offset:57344\n"
        "global_load_lds_dwordx4 v243, s[64:65] offset:1024\n"
        "v_mfma_f32_16x16x32_bf16 a[32:35], v[64:67], v[12:15], a[32:35]\n"
        "ds_read_b128 v[16:19], v224 offset:36864\n"
        "v_add_u32 v233, s53, v233\n"
        "v_mfma_f32_16x16x32_bf16 a[32:35], v[32:35], v[8:11], a[32:35]\n"
        "ds_read_b128 v[20:23], v225 offset:36864\n"
        "s_add_u32 s53, s52, s51\n"
        "s_add_u32 s53, s53, 0x1000000\n"
        "v_mfma_f32_16x16x32_bf16 a[36:39], v[36:39], v[8:11], a[36:39]\n"
        "ds_read_b128 v[24:27], v224 offset:38912\n"
        "s_waitcnt lgkmcnt(8)\n"
        "v_mfma_f32_16x16x32_bf16 a[36:39], v[68:71], v[12:15], a[36:39]\n"
        "ds_read_b128 v[28:31], v225 offset:38912\n"
        "global_load_lds_dwordx4 v244, s[64:65] offset:2048\n"
        "v_mfma_f32_16x16x32_bf16 a[4:7], v[68:71], v[4:7], a[4:7]\n"
        "v_mfma_f32_16x16x32_bf16 a[4:7], v[36:39], v[0:3], a[4:7]\n"
        "v_mfma_f32_16x16x32_bf16 a[8:11], v[40:43], v[0:3], a[8:11]\n"
        "v_mfma_f32_16x16x32_bf16 a[8:11], v[72:75], v[4:7], a[8:11]\n"
        "global_load_lds_dwordx4 v245, s[64:65] offset:3072\n"
        "s_add_u32 m0, s72, 0x11000\n"
        "v_mfma_f32_16x16x32_bf16 a[40:43], v[72:75], v[12:15], a[40:43]\n"
        "s_add_u32 s64, s64, 0x80\n"
        "s_addc_u32 s65, s65, 0\n"
        "v_mfma_f32_16x16x32_bf16 a[40:43], v[40:43], v[8:11], a[40:43]\n"
        "v_mfma_f32_16x16x32_bf16 a[44:47], v[44:47], v[8:11], a[44:47]\n"
        "v_mfma_f32_16x16x32_bf16 a[44:47], v[76:79], v[12:15], a[44:47]\n"
        "global_load_lds_dwordx4 v242, s[66:67] offset:0\n"
        "v_mfma_f32_16x16x32_bf16 a[12:15], v[76:79], v[4:7], a[12:15]\n"
        "v_mfma_f32_16x16x32_bf16 a[12:15], v[44:47], v[0:3], a[12:15]\n"
        "s_waitcnt lgkmcnt(0)\n"
        "v_mfma_f32_16x16x32_bf16 a[16:19], v[0:3], v[48:51], a[16:19]\n"
        "v_mfma_f32_16x16x32_bf16 a[16:19], v[4:7], v[80:83], a[16:19]\n"
        "global_load_lds_dwordx4 v243, s[66:67] offset:1024\n"
        "v_mfma_f32_16x16x32_bf16 a[48:51], v[12:15], v[80:83], a[48:51]\n"
        "v_mfma_f32_16x16x32_bf16 a[48:51], v[8:11], v[48:51], a[48:51]\n"
        "v_mfma_f32_16x16x32_bf16 a[52:55], v[8:11], v[52:55], a[52:55]\n"
        "v_mfma_f32_16x16x32_bf16 a[52:55], v[12:15], v[84:87], a[52:55]\n"
        "global_load_lds_dwordx4 v244, s[66:67] offset:2048\n"
        "v_mfma_f32_16x16x32_bf16 a[20:23], v[4:7], v[84:87], a[20:23]\n"
        "v_mfma_f32_16x16x32_bf16 a[20:23], v[0:3], v[52:55], a[20:23]\n"
        "v_mfma_f32_16x16x32_bf16 a[24:27], v[0:3], v[56:59], a[24:27]\n"
        "v_mfma_f32_16x16x32_bf16 a[24:27], v[4:7], v[88:91], a[24:27]\n"
        "global_load_lds_dwordx4 v245, s[66:67] offset:3072\n"
        "s_add_u32 m0, s73, 0x1c000\n"
        "v_mfma_f32_16x16x32_bf16 a[56:59], v[12:15], v[88:91], a[56:59]\n"
        "s_add_u32 s66, s66, 0x80\n"
        "s_addc_u32 s67, s67, 0\n"
        "v_mfma_f32_16x16x32_bf16 a[56:59], v[8:11], v[56:59], a[56:59]\n"
        "v_mfma_f32_16x16x32_bf16 a[60:63], v[8:11], v[60:63], a[60:63]\n"
        "ds_read_b128 v[8:11], v224 offset:43008\n"
        "v_mfma_f32_16x16x32_bf16 a[60:63], v[12:15], v[92:95], a[60:63]\n"
        "ds_read_b128 v[12:15], v225 offset:43008\n"
        "v_mfma_f32_16x16x32_bf16 a[28:31], v[4:7], v[92:95], a[28:31]\n"
        "ds_read_b128 v[4:7], v225 offset:40960\n"
        "v_mfma_f32_16x16x32_bf16 a[28:31], v[0:3], v[60:63], a[28:31]\n"
        "ds_read_b128 v[0:3], v224 offset:40960\n"
        "v_mfma_f32_16x16x32_bf16 a[64:67], v[32:35], v[16:19], a[64:67]\n"
        "s_mov_b64 exec, 0xffff\n"
        "global_store_dword v193, v191, s[76:77] sc1\n"
        "s_mov_b64 exec, -1\n"
        "v_mfma_f32_16x16x32_bf16 a[64:67], v[64:67], v[20:23], a[64:67]\n"
        "v_mfma_f32_16x16x32_bf16 a[96:99], v[64:67], v[28:31], a[96:99]\n"
        "v_mfma_f32_16x16x32_bf16 a[96:99], v[32:35], v[24:27], a[96:99]\n"
        "v_mfma_f32_16x16x32_bf16 a[100:103], v[36:39], v[24:27], a[100:103]\n"
        "v_mfma_f32_16x16x32_bf16 a[100:103], v[68:71], v[28:31], a[100:103]\n"
        "v_mfma_f32_16x16x32_bf16 a[68:71], v[68:71], v[20:23], a[68:71]\n"
        "v_mfma_f32_16x16x32_bf16 a[68:71], v[36:39], v[16:19], a[68:71]\n"
        "v_mfma_f32_16x16x32_bf16 a[72:75], v[40:43], v[16:19], a[72:75]\n"
        "v_mfma_f32_16x16x32_bf16 a[72:75], v[72:75], v[20:23], a[72:75]\n"
        "v_mfma_f32_16x16x32_bf16 a[104:107], v[72:75], v[28:31], a[104:107]\n"
        "v_mfma_f32_16x16x32_bf16 a[104:107], v[40:43], v[24:27], a[104:107]\n"
        "v_mfma_f32_16x16x32_bf16 a[108:111], v[44:47], v[24:27], a[108:111]\n"
        "v_mfma_f32_16x16x32_bf16 a[108:111], v[76:79], v[28:31], a[108:111]\n"
        "v_mfma_f32_16x16x32_bf16 a[76:79], v[76:79], v[20:23], a[76:79]\n"
        "v_mfma_f32_16x16x32_bf16 a[76:79], v[44:47], v[16:19], a[76:79]\n"
        "v_mfma_f32_16x16x32_bf16 a[80:83], v[16:19], v[48:51], a[80:83]\n"
        "v_mfma_f32_16x16x32_bf16 a[80:83], v[20:23], v[80:83], a[80:83]\n"
        "v_mfma_f32_16x16x32_bf16 a[112:115], v[28:31], v[80:83], a[112:115]\n"
        "v_mfma_f32_16x16x32_bf16 a[112:115], v[24:27], v[48:51], a[112:115]\n"
        "v_mfma_f32_16x16x32_bf16 a[116:119], v[24:27], v[52:55], a[116:119]\n"
        "v_mfma_f32_16x16x32_bf16 a[116:119], v[28:31], v[84:87], a[116:119]\n"
        "s_waitcnt lgkmcnt(0)\n"
        "v_mfma_f32_16x16x32_bf16 a[84:87], v[20:23], v[84:87], a[84:87]\n"
        "v_mfma_f32_16x16x32_bf16 a[84:87], v[16:19], v[52:55], a[84:87]\n"
        "v_mfma_f32_16x16x32_bf16 a[88:91], v[16:19], v[56:59], a[88:91]\n"
        "v_mfma_f32_16x16x32_bf16 a[88:91], v[20:23], v[88:91], a[88:91]\n"
        "v_mfma_f32_16x16x32_bf16 a[120:123], v[28:31], v[88:91], a[120:123]\n"
        "v_mfma_f32_16x16x32_bf16 a[120:123], v[24:27], v[56:59], a[120:123]\n"
        "v_mfma_f32_16x16x32_bf16 a[124:127], v[24:27], v[60:63], a[124:127]\n"
        "ds_read_b128 v[24:27], v224 offset:47104\n"
        "v_mfma_f32_16x16x32_bf16 a[124:127], v[28:31], v[92:95], a[124:127]\n"
        "ds_read_b128 v[28:31], v225 offset:47104\n"
        "v_mfma_f32_16x16x32_bf16 a[92:95], v[20:23], v[92:95], a[92:95]\n"
        "ds_read_b128 v[20:23], v225 offset:45056\n"
        "v_mfma_f32_16x16x32_bf16 a[92:95], v[16:19], v[60:63], a[92:95]\n"
        "ds_read_b128 v[16:19], v224 offset:45056\n"
        "v_mfma_f32_16x16x32_bf16 a[128:131], v[32:35], v[0:3], a[128:131]\n"
        "v_mfma_f32_16x16x32_bf16 a[128:131], v[64:67], v[4:7], a[128:131]\n"
        "v_mfma_f32_16x16x32_bf16 a[160:163], v[64:67], v[12:15], a[160:163]\n"
        "v_mfma_f32_16x16x32_bf16 a[160:163], v[32:35], v[8:11], a[160:163]\n"
        "v_mfma_f32_16x16x32_bf16 a[164:167], v[36:39], v[8:11], a[164:167]\n"
        "v_mfma_f32_16x16x32_bf16 a[164:167], v[68:71], v[12:15], a[164:167]\n"
        "v_mfma_f32_16x16x32_bf16 a[132:135], v[68:71], v[4:7], a[132:135]\n"
        "v_mfma_f32_16x16x32_bf16 a[132:135], v[36:39], v[0:3], a[132:135]\n"
        "v_mfma_f32_16x16x32_bf16 a[136:139], v[40:43], v[0:3], a[136:139]\n"
        "v_mfma_f32_16x16x32_bf16 a[136:139], v[72:75], v[4:7], a[136:139]\n"
        "v_mfma_f32_16x16x32_bf16 a[168:171], v[72:75], v[12:15], a[168:171]\n"
        "v_mfma_f32_16x16x32_bf16 a[168:171], v[40:43], v[8:11], a[168:171]\n"
        "v_mfma_f32_16x16x32_bf16 a[172:175], v[44:47], v[8:11], a[172:175]\n"
        "v_mfma_f32_16x16x32_bf16 a[172:175], v[76:79], v[12:15], a[172:175]\n"
        "v_mfma_f32_16x16x32_bf16 a[140:143], v[76:79], v[4:7], a[140:143]\n"
        "v_mfma_f32_16x16x32_bf16 a[140:143], v[44:47], v[0:3], a[140:143]\n"
        "v_mfma_f32_16x16x32_bf16 a[144:147], v[0:3], v[48:51], a[144:147]\n"
        "v_mfma_f32_16x16x32_bf16 a[144:147], v[4:7], v[80:83], a[144:147]\n"
        "v_mfma_f32_16x16x32_bf16 a[176:179], v[12:15], v[80:83], a[176:179]\n"
        "v_mfma_f32_16x16x32_bf16 a[176:179], v[8:11], v[48:51], a[176:179]\n"
        "v_mfma_f32_16x16x32_bf16 a[180:183], v[8:11], v[52:55], a[180:183]\n"
        "v_mfma_f32_16x16x32_bf16 a[180:183], v[12:15], v[84:87], a[180:183]\n"
        "s_waitcnt lgkmcnt(0)\n"
        "v_mfma_f32_16x16x32_bf16 a[148:151], v[4:7], v[84:87], a[148:151]\n"
        "v_mfma_f32_16x16x32_bf16 a[148:151], v[0:3], v[52:55], a[148:151]\n"
        "v_mfma_f32_16x16x32_bf16 a[152:155], v[0:3], v[56:59], a[152:155]\n"
        "v_mfma_f32_16x16x32_bf16 a[152:155], v[4:7], v[88:91], a[152:155]\n"
        "v_mfma_f32_16x16x32_bf16 a[184:187], v[12:15], v[88:91], a[184:187]\n"
        "v_mfma_f32_16x16x32_bf16 a[184:187], v[8:11], v[56:59], a[184:187]\n"
        "s_waitcnt vmcnt(9)\n"
        "v_mfma_f32_16x16x32_bf16 a[188:191], v[8:11], v[60:63], a[188:191]\n"
        "s_barrier\n"
        "v_mfma_f32_16x16x32_bf16 a[188:191], v[12:15], v[92:95], a[188:191]\n"
        "ds_read_b64_tr_b16 v[160:161], v226 offset:32768\n"
        "global_load_lds_dwordx4 v238, s[68:69] offset:0\n"
        "v_mfma_f32_16x16x32_bf16 a[156:159], v[4:7], v[92:95], a[156:159]\n"
        "ds_read_b64_tr_b16 v[162:163], v227 offset:32768\n"
        "v_mfma_f32_16x16x32_bf16 a[156:159], v[0:3], v[60:63], a[156:159]\n"
        "ds_read_b64_tr_b16 v[164:165], v226 offset:40960\n"
        "v_mfma_f32_16x16x32_bf16 a[192:195], v[32:35], v[16:19], a[192:195]\n"
        "ds_read_b64_tr_b16 v[166:167], v227 offset:40960\n"
        "v_mfma_f32_16x16x32_bf16 a[192:195], v[64:67], v[20:23], a[192:195]\n"
        "ds_read_b64_tr_b16 v[172:173], v228 offset:40960\n"
        "global_load_lds_dwordx4 v239, s[68:69] offset:1024\n"
        "v_mfma_f32_16x16x32_bf16 a[224:227], v[64:67], v[28:31], a[224:227]\n"
        "ds_read_b64_tr_b16 v[174:175], v229 offset:40960\n"
        "v_mfma_f32_16x16x32_bf16 a[224:227], v[32:35], v[24:27], a[224:227]\n"
        "ds_read_b64_tr_b16 v[168:169], v228 offset:32768\n"
        "v_mfma_f32_16x16x32_bf16 a[228:231], v[36:39], v[24:27], a[228:231]\n"
        "ds_read_b64_tr_b16 v[170:171], v229 offset:32768\n"
        "v_mfma_f32_16x16x32_bf16 a[228:231], v[68:71], v[28:31], a[228:231]\n"
        "ds_read_b128 v[192:195], v246 offset:2048\n"
        "global_load_lds_dwordx4 v240, s[68:69] offset:2048\n"
        "v_mfma_f32_16x16x32_bf16 a[196:199], v[68:71], v[20:23], a[196:199]\n"
        "ds_read_b128 v[208:211], v247 offset:2048\n"
        "v_accvgpr_read_b32 v177, a225\n"
        "v_mfma_f32_16x16x32_bf16 a[196:199], v[36:39], v[16:19], a[196:199]\n"
        "ds_read_b128 v[196:199], v246 offset:0\n"
        "v_accvgpr_read_b32 v178, a226\n"
        "v_mfma_f32_16x16x32_bf16 a[200:203], v[40:43], v[16:19], a[200:203]\n"
        "ds_read_b128 v[212:215], v247 offset:0\n"
        "v_accvgpr_read_b32 v179, a227\n"
        "v_mfma_f32_16x16x32_bf16 a[200:203], v[72:75], v[20:23], a[200:203]\n"
        "ds_read_b128 v[200:203], v246 offset:6144\n"
        "global_load_lds_dwordx4 v241, s[68:69] offset:3072\n"
        "s_add_u32 m0, s72, 0x8000\n"
        "v_mfma_f32_16x16x32_bf16 a[232:235], v[72:75], v[28:31], a[232:235]\n"
        "ds_read_b128 v[216:219], v247 offset:6144\n"
        "s_add_u32 s68, s68, 0x80000\n"
        "s_addc_u32 s69, s69, 0\n"
        "v_mfma_f32_16x16x32_bf16 a[232:235], v[40:43], v[24:27], a[232:235]\n"
        "ds_read_b128 v[204:207], v246 offset:4096\n"
        "v_accvgpr_read_b32 v180, a228\n"
        "v_mfma_f32_16x16x32_bf16 a[236:239], v[44:47], v[24:27], a[236:239]\n"
        "ds_read_b128 v[220:223], v247 offset:4096\n"
        "v_accvgpr_read_b32 v181, a229\n"
        "v_mfma_f32_16x16x32_bf16 a[236:239], v[76:79], v[28:31], a[236:239]\n"
        "ds_read_b64_tr_b16 v[32:33], v230 offset:32768\n"
        "global_load_lds_dwordx4 v242, s[64:65] offset:0\n"
        "v_mfma_f32_16x16x32_bf16 a[204:207], v[76:79], v[20:23], a[204:207]\n"
        "ds_read_b64_tr_b16 v[34:35], v231 offset:32768\n"
        "v_accvgpr_read_b32 v182, a230\n"
        "v_mfma_f32_16x16x32_bf16 a[204:207], v[44:47], v[16:19], a[204:207]\n"
        "ds_read_b64_tr_b16 v[36:37], v230 offset:40960\n"
        "s_add_u32 s97, s52, 0x6000\n"
        "v_accvgpr_read_b32 v183, a231\n"
        "v_mfma_f32_16x16x32_bf16 a[208:211], v[16:19], v[48:51], a[208:211]\n"
        "ds_read_b64_tr_b16 v[38:39], v231 offset:40960\n"
        "v_mul_f32 v177, s88, v177\n"
        "v_mfma_f32_16x16x32_bf16 a[208:211], v[20:23], v[80:83], a[208:211]\n"
        "ds_read_b64_tr_b16 v[44:45], v232 offset:40960\n"
        "global_load_lds_dwordx4 v243, s[64:65] offset:1024\n"
        "v_mfma_f32_16x16x32_bf16 a[240:243], v[28:31], v[80:83], a[240:243]\n"
        "ds_read_b64_tr_b16 v[46:47], v233 offset:40960\n"
        "v_mul_f32 v178, s88, v178\n"
        "v_mfma_f32_16x16x32_bf16 a[240:243], v[24:27], v[48:51], a[240:243]\n"
        "ds_read_b64_tr_b16 v[40:41], v232 offset:32768\n"
        "v_mul_f32 v179, s88, v179\n"
        "v_mfma_f32_16x16x32_bf16 a[244:247], v[24:27], v[52:55], a[244:247]\n"
        "ds_read_b64_tr_b16 v[42:43], v233 offset:32768\n"
        "v_mul_f32 v180, s88, v180\n"
        "v_mfma_f32_16x16x32_bf16 a[244:247], v[28:31], v[84:87], a[244:247]\n"
        "ds_read_b64_tr_b16 v[48:49], v226 offset:33792\n"
        "global_load_lds_dwordx4 v244, s[64:65] offset:2048\n"
        "v_mfma_f32_16x16x32_bf16 a[212:215], v[20:23], v[84:87], a[212:215]\n"
        "ds_read_b64_tr_b16 v[50:51], v227 offset:33792\n"
        "v_mul_f32 v181, s88, v181\n"
        "v_mfma_f32_16x16x32_bf16 a[212:215], v[16:19], v[52:55], a[212:215]\n"
        "ds_read_b64_tr_b16 v[52:53], v226 offset:41984\n"
        "v_mul_f32 v182, s88, v182\n"
        "v_mfma_f32_16x16x32_bf16 a[216:219], v[16:19], v[56:59], a[216:219]\n"
        "ds_read_b64_tr_b16 v[54:55], v227 offset:41984\n"
        "v_mul_f32 v183, s88, v183\n"
        "v_mfma_f32_16x16x32_bf16 a[216:219], v[20:23], v[88:91], a[216:219]\n"
        "global_load_lds_dwordx4 v245, s[64:65] offset:3072\n"
        "s_add_u32 m0, s72, 0x9000\n"
        "v_mfma_f32_16x16x32_bf16 a[248:251], v[28:31], v[88:91], a[248:251]\n"
        "s_add_u32 s64, s64, 0x80\n"
        "s_addc_u32 s65, s65, 0\n"
        "v_accvgpr_read_b32 v185, a233\n"
        "v_mfma_f32_16x16x32_bf16 a[248:251], v[24:27], v[56:59], a[248:251]\n"
        "ds_read_b64_tr_b16 v[56:57], v228 offset:33792\n"
        "v_accvgpr_read_b32 v186, a234\n"
        "v_mfma_f32_16x16x32_bf16 a[252:255], v[24:27], v[60:63], a[252:255]\n"
        "ds_read_b64_tr_b16 v[58:59], v229 offset:33792\n"
        "v_accvgpr_read_b32 v187, a235\n"
        "v_mfma_f32_16x16x32_bf16 a[252:255], v[28:31], v[92:95], a[252:255]\n"
        "global_load_lds_dwordx4 v242, s[66:67] offset:0\n"
        "v_mfma_f32_16x16x32_bf16 a[220:223], v[20:23], v[92:95], a[220:223]\n"
        "s_waitcnt lgkmcnt(4)\n"
        "v_accvgpr_read_b32 v188, a236\n"
        "v_accvgpr_read_b32 v189, a237\n"
        "v_mfma_f32_16x16x32_bf16 a[220:223], v[16:19], v[60:63], a[220:223]\n"
        "ds_read_b64_tr_b16 v[60:61], v228 offset:41984\n"
        "v_accvgpr_read_b32 v190, a238\n"
        "v_mfma_f32_16x16x32_bf16 v[0:3], v[160:163], v[192:195], 0\n"
        "ds_read_b64_tr_b16 v[62:63], v229 offset:41984\n"
        "v_accvgpr_read_b32 v191, a239\n"
        "v_mfma_f32_16x16x32_bf16 v[0:3], v[164:167], v[208:211], v[0:3]\n"
        "global_load_lds_dwordx4 v243, s[66:67] offset:1024\n"
        "v_mfma_f32_16x16x32_bf16 v[4:7], v[172:175], v[208:211], 0\n"
        "v_mul_f32 v185, s88, v185\n"
        "v_mul_f32 v186, s88, v186\n"
        "v_mfma_f32_16x16x32_bf16 v[4:7], v[168:171], v[192:195], v[4:7]\n"
        "v_mul_f32 v187, s88, v187\n"
        "v_mul_f32 v188, s88, v188\n"
        "v_mfma_f32_16x16x32_bf16 v[68:71], v[168:171], v[196:199], 0\n"
        "v_mul_f32 v189, s88, v189\n"
        "v_mul_f32 v190, s88, v190\n"
        "v_mfma_f32_16x16x32_bf16 v[68:71], v[172:175], v[212:215], v[68:71]\n"
        "global_load_lds_dwordx4 v244, s[66:67] offset:2048\n"
        "v_mfma_f32_16x16x32_bf16 v[64:67], v[164:167], v[212:215], 0\n"
        "v_mul_f32 v191, s88, v191\n"
        "v_mfma_f32_16x16x32_bf16 v[64:67], v[160:163], v[196:199], v[64:67]\n"
        "v_accvgpr_read_b32 v176, a224\n"
        "v_mul_f32 v176, s88, v176\n"
        "v_mfma_f32_16x16x32_bf16 v[96:99], v[160:163], v[200:203], 0\n"
        "v_cvt_pk_bf16_f32 v176, v176, v177\n"
        "v_cvt_pk_bf16_f32 v177, v178, v179\n"
        "v_mfma_f32_16x16x32_bf16 v[96:99], v[164:167], v[216:219], v[96:99]\n"
        "v_cvt_pk_bf16_f32 v178, v180, v181\n"
        "v_cvt_pk_bf16_f32 v179, v182, v183\n"
        "v_mfma_f32_16x16x32_bf16 v[100:103], v[172:175], v[216:219], 0\n"
        "global_load_lds_dwordx4 v245, s[66:67] offset:3072\n"
        "s_add_u32 m0, s73, 0x20000\n"
        "v_accvgpr_write_b32 a224, v176\n"
        "v_mfma_f32_16x16x32_bf16 v[100:103], v[168:171], v[200:203], v[100:103]\n"
        "s_add_u32 s66, s66, 0x80\n"
        "s_addc_u32 s67, s67, 0\n"
        "v_accvgpr_write_b32 a225, v177\n"
        "v_mfma_f32_16x16x32_bf16 v[132:135], v[168:171], v[204:207], 0\n"
        "ds_read_b64_tr_b16 v[168:169], v232 offset:33792\n"
        "v_accvgpr_write_b32 a226, v178\n"
        "v_mfma_f32_16x16x32_bf16 v[132:135], v[172:175], v[220:223], v[132:135]\n"
        "ds_read_b64_tr_b16 v[172:173], v232 offset:41984\n"
        "v_accvgpr_write_b32 a227, v179\n"
        "v_mfma_f32_16x16x32_bf16 v[128:131], v[164:167], v[220:223], 0\n"
        "ds_read_b64_tr_b16 v[164:165], v230 offset:41984\n"
        "v_accvgpr_read_b32 v177, a193\n"
        "v_mfma_f32_16x16x32_bf16 v[128:131], v[160:163], v[204:207], v[128:131]\n"
        "ds_read_b64_tr_b16 v[160:161], v230 offset:33792\n"
        "v_accvgpr_read_b32 v178, a194\n"
        "v_mfma_f32_16x16x32_bf16 v[8:11], v[32:35], v[192:195], 0\n"
        "ds_read_b64_tr_b16 v[162:163], v231 offset:33792\n"
        "v_accvgpr_read_b32 v179, a195\n"
        "v_mfma_f32_16x16x32_bf16 v[8:11], v[36:39], v[208:211], v[8:11]\n"
        "ds_read_b64_tr_b16 v[166:167], v231 offset:41984\n"
        "v_accvgpr_read_b32 v180, a196\n"
        "v_mfma_f32_16x16x32_bf16 v[12:15], v[44:47], v[208:211], 0\n"
        "ds_read_b64_tr_b16 v[174:175], v233 offset:41984\n"
        "v_accvgpr_read_b32 v181, a197\n"
        "v_mfma_f32_16x16x32_bf16 v[12:15], v[40:43], v[192:195], v[12:15]\n"
        "ds_read_b64_tr_b16 v[170:171], v233 offset:33792\n"
        "v_accvgpr_read_b32 v182, a198\n"
        "v_mfma_f32_16x16x32_bf16 v[76:79], v[40:43], v[196:199], 0\n"
        "v_accvgpr_read_b32 v183, a199\n"
        "v_mul_f32 v177, s88, v177\n"
        "v_mfma_f32_16x16x32_bf16 v[76:79], v[44:47], v[212:215], v[76:79]\n"
        "v_mul_f32 v178, s88, v178\n"
        "v_mul_f32 v179, s88, v179\n"
        "v_mfma_f32_16x16x32_bf16 v[72:75], v[36:39], v[212:215], 0\n"
        "v_mul_f32 v180, s88, v180\n"
        "v_mul_f32 v181, s88, v181\n"
        "v_mfma_f32_16x16x32_bf16 v[72:75], v[32:35], v[196:199], v[72:75]\n"
        "s_waitcnt lgkmcnt(4)\n"
        "v_mul_f32 v182, s88, v182\n"
        "v_mul_f32 v183, s88, v183\n"
        "v_mfma_f32_16x16x32_bf16 v[104:107], v[32:35], v[200:203], 0\n"
        "v_mfma_f32_16x16x32_bf16 v[104:107], v[36:39], v[216:219], v[104:107]\n"
        "v_mfma_f32_16x16x32_bf16 v[108:111], v[44:47], v[216:219], 0\n"
        "v_mfma_f32_16x16x32_bf16 v[108:111], v[40:43], v[200:203], v[108:111]\n"
        "s_waitcnt vmcnt(13) lgkmcnt(0)\n"
        "v_mfma_f32_16x16x32_bf16 v[140:143], v[40:43], v[204:207], 0\n"
        "s_barrier\n"
        "v_mfma_f32_16x16x32_bf16 v[140:143], v[44:47], v[220:223], v[140:143]\n"
        "ds_read_b64_tr_b16 v[44:45], v228 offset:57344\n"
        "global_load_lds_dwordx4 v238, s[68:69] offset:0\n"
        "v_mfma_f32_16x16x32_bf16 v[136:139], v[36:39], v[220:223], 0\n"
        "ds_read_b64_tr_b16 v[36:37], v226 offset:57344\n"
        "v_mfma_f32_16x16x32_bf16 v[136:139], v[32:35], v[204:207], v[136:139]\n"
        "ds_read_b64_tr_b16 v[32:33], v226 offset:49152\n"
        "v_accvgpr_read_b32 v184, a232\n"
        "v_mfma_f32_16x16x32_bf16 v[16:19], v[48:51], v[192:195], 0\n"
        "ds_read_b64_tr_b16 v[34:35], v227 offset:49152\n"
        "v_mul_f32 v184, s88, v184\n"
        "v_mfma_f32_16x16x32_bf16 v[16:19], v[52:55], v[208:211], v[16:19]\n"
        "ds_read_b64_tr_b16 v[38:39], v227 offset:57344\n"
        "v_cvt_pk_bf16_f32 v184, v184, v185\n"
        "v_mfma_f32_16x16x32_bf16 v[20:23], v[60:63], v[208:211], 0\n"
        "ds_read_b64_tr_b16 v[46:47], v229 offset:57344\n"
        "v_cvt_pk_bf16_f32 v185, v186, v187\n"
        "v_mfma_f32_16x16x32_bf16 v[20:23], v[56:59], v[192:195], v[20:23]\n"
        "ds_read_b64_tr_b16 v[40:41], v228 offset:49152\n"
        "v_cvt_pk_bf16_f32 v186, v188, v189\n"
        "v_mfma_f32_16x16x32_bf16 v[84:87], v[56:59], v[196:199], 0\n"
        "ds_read_b64_tr_b16 v[42:43], v229 offset:49152\n"
        "v_cvt_pk_bf16_f32 v187, v190, v191\n"
        "v_mfma_f32_16x16x32_bf16 v[84:87], v[60:63], v[212:215], v[84:87]\n"
        "global_load_lds_dwordx4 v239, s[68:69] offset:1024\n"
        "v_accvgpr_write_b32 a228, v184\n"
        "v_mfma_f32_16x16x32_bf16 v[80:83], v[52:55], v[212:215], 0\n"
        "v_accvgpr_write_b32 a229, v185\n"
        "v_accvgpr_write_b32 a230, v186\n"
        "v_mfma_f32_16x16x32_bf16 v[80:83], v[48:51], v[196:199], v[80:83]\n"
        "v_accvgpr_write_b32 a231, v187\n"
        "v_accvgpr_read_b32 v185, a201\n"
        "v_mfma_f32_16x16x32_bf16 v[112:115], v[48:51], v[200:203], 0\n"
        "v_accvgpr_read_b32 v186, a202\n"
        "v_accvgpr_read_b32 v187, a203\n"
        "v_mfma_f32_16x16x32_bf16 v[112:115], v[52:55], v[216:219], v[112:115]\n"
        "global_load_lds_dwordx4 v240, s[68:69] offset:2048\n"
        "v_accvgpr_read_b32 v188, a204\n"
        "v_mfma_f32_16x16x32_bf16 v[116:119], v[60:63], v[216:219], 0\n"
        "v_accvgpr_read_b32 v189, a205\n"
        "v_accvgpr_read_b32 v190, a206\n"
        "v_mfma_f32_16x16x32_bf16 v[116:119], v[56:59], v[200:203], v[116:119]\n"
        "v_accvgpr_read_b32 v191, a207\n"
        "v_mul_f32 v185, s88, v185\n"
        "v_mfma_f32_16x16x32_bf16 v[148:151], v[56:59], v[204:207], 0\n"
        "ds_read_b64_tr_b16 v[56:57], v232 offset:49152\n"
        "v_mul_f32 v186, s88, v186\n"
        "v_mfma_f32_16x16x32_bf16 v[148:151], v[60:63], v[220:223], v[148:151]\n"
        "global_store_dwordx4 v254, a[224:227], s[70:71] offset:0 sc1\n"
        "ds_read_b64_tr_b16 v[60:61], v232 offset:57344\n"
        "ds_read_b64_tr_b16 v[62:63], v233 offset:57344\n"
        "v_mul_f32 v187, s88, v187\n"
        "v_mfma_f32_16x16x32_bf16 v[144:147], v[52:55], v[220:223], 0\n"
        "ds_read_b64_tr_b16 v[52:53], v230 offset:57344\n"
        "v_mul_f32 v188, s88, v188\n"
        "v_mfma_f32_16x16x32_bf16 v[144:147], v[48:51], v[204:207], v[144:147]\n"
        "ds_read_b64_tr_b16 v[48:49], v230 offset:49152\n"
        "v_mul_f32 v189, s88, v189\n"
        "v_mfma_f32_16x16x32_bf16 v[24:27], v[160:163], v[192:195], 0\n"
        "ds_read_b64_tr_b16 v[50:51], v231 offset:49152\n"
        "global_load_lds_dwordx4 v241, s[68:69] offset:3072\n"
        "s_add_u32 m0, s72, 0x0\n"
        "v_mfma_f32_16x16x32_bf16 v[24:27], v[164:167], v[208:211], v[24:27]\n"
        "ds_read_b64_tr_b16 v[54:55], v231 offset:57344\n"
        "s_add_u32 s68, s68, 0x80000\n"
        "s_addc_u32 s69, s69, 0\n"
        "v_mfma_f32_16x16x32_bf16 v[28:31], v[172:175], v[208:211], 0\n"
        "ds_read_b64_tr_b16 v[58:59], v233 offset:49152\n"
        "v_mul_f32 v190, s88, v190\n"
        "v_mfma_f32_16x16x32_bf16 v[28:31], v[168:171], v[192:195], v[28:31]\n"
        "ds_read_b128 v[192:195], v248 offset:2048\n"
        "v_mul_f32 v191, s88, v191\n"
        "v_mfma_f32_16x16x32_bf16 v[92:95], v[168:171], v[196:199], 0\n"
        "ds_read_b128 v[208:211], v249 offset:2048\n"
        "global_load_lds_dwordx4 v242, s[64:65] offset:0\n"
        "v_mfma_f32_16x16x32_bf16 v[92:95], v[172:175], v[212:215], v[92:95]\n"
        "v_mfma_f32_16x16x32_bf16 v[88:91], v[164:167], v[212:215], 0\n"
        "ds_read_b128 v[212:215], v249 offset:0\n"
        "v_mfma_f32_16x16x32_bf16 v[88:91], v[160:163], v[196:199], v[88:91]\n"
        "ds_read_b128 v[196:199], v248 offset:0\n"
        "v_accvgpr_read_b32 v176, a192\n"
        "v_mfma_f32_16x16x32_bf16 v[120:123], v[160:163], v[200:203], 0\n"
        "global_load_lds_dwordx4 v243, s[64:65] offset:1024\n"
        "v_mfma_f32_16x16x32_bf16 v[120:123], v[164:167], v[216:219], v[120:123]\n"
        "v_mul_f32 v176, s88, v176\n"
        "v_cvt_pk_bf16_f32 v176, v176, v177\n"
        "v_mfma_f32_16x16x32_bf16 v[124:127], v[172:175], v[216:219], 0\n"
        "ds_read_b128 v[216:219], v249 offset:6144\n"
        "v_cvt_pk_bf16_f32 v177, v178, v179\n"
        "v_mfma_f32_16x16x32_bf16 v[124:127], v[168:171], v[200:203], v[124:127]\n"
        "ds_read_b128 v[200:203], v248 offset:6144\n"
        "v_cvt_pk_bf16_f32 v178, v180, v181\n"
        "v_mfma_f32_16x16x32_bf16 v[156:159], v[168:171], v[204:207], 0\n"
        "ds_read_b64_tr_b16 v[168:169], v228 offset:50176\n"
        "v_cvt_pk_bf16_f32 v179, v182, v183\n"
        "LHA5_%=:\n"
        "s_cbranch_execz LHA9_%=\n"
        "v_mfma_f32_16x16x32_bf16 v[156:159], v[172:175], v[220:223], v[156:159]\n"
        "ds_read_b64_tr_b16 v[172:173], v228 offset:58368\n"
        "v_accvgpr_write_b32 a232, v176\n"
        "v_mfma_f32_16x16x32_bf16 v[152:155], v[164:167], v[220:223], 0\n"
        "ds_read_b128 v[220:223], v249 offset:4096\n"
        "v_accvgpr_write_b32 a233, v177\n"
        "v_mfma_f32_16x16x32_bf16 v[152:155], v[160:163], v[204:207], v[152:155]\n"
        "ds_read_b128 v[204:207], v248 offset:4096\n"
        "s_waitcnt lgkmcnt(6)\n"
        "v_mfma_f32_16x16x32_bf16 v[0:3], v[32:35], v[192:195], v[0:3]\n"
        "ds_read_b64_tr_b16 v[160:161], v226 offset:50176\n"
        "v_accvgpr_write_b32 a234, v178\n"
        "v_mfma_f32_16x16x32_bf16 v[0:3], v[36:39], v[208:211], v[0:3]\n"
        "ds_read_b64_tr_b16 v[162:163], v227 offset:50176\n"
        "v_accvgpr_write_b32 a235, v179\n"
        "v_mfma_f32_16x16x32_bf16 v[4:7], v[44:47], v[208:211], v[4:7]\n"
        "ds_read_b64_tr_b16 v[164:165], v226 offset:58368\n"
        "global_load_lds_dwordx4 v244, s[64:65] offset:2048\n"
        "v_mfma_f32_16x16x32_bf16 v[4:7], v[40:43], v[192:195], v[4:7]\n"
        "ds_read_b64_tr_b16 v[166:167], v227 offset:58368\n"
        "v_accvgpr_read_b32 v177, a161\n"
        "v_mfma_f32_16x16x32_bf16 v[68:71], v[40:43], v[196:199], v[68:71]\n"
        "ds_read_b64_tr_b16 v[174:175], v229 offset:58368\n"
        "v_accvgpr_read_b32 v178, a162\n"
        "v_mfma_f32_16x16x32_bf16 v[68:71], v[44:47], v[212:215], v[68:71]\n"
        "ds_read_b64_tr_b16 v[170:171], v229 offset:50176\n"
        "v_accvgpr_read_b32 v179, a163\n"
        "v_mfma_f32_16x16x32_bf16 v[64:67], v[36:39], v[212:215], v[64:67]\n"
        "global_load_lds_dwordx4 v245, s[64:65] offset:3072\n"
        "s_add_u32 m0, s72, 0x1000\n"
        "v_mfma_f32_16x16x32_bf16 v[64:67], v[32:35], v[196:199], v[64:67]\n"
        "s_add_u32 s64, s64, 0x80\n"
        "s_addc_u32 s65, s65, 0\n"
        "s_waitcnt lgkmcnt(6)\n"
        "v_mfma_f32_16x16x32_bf16 v[96:99], v[32:35], v[200:203], v[96:99]\n"
        "v_accvgpr_read_b32 v180, a164\n"
        "v_accvgpr_read_b32 v181, a165\n"
        "v_mfma_f32_16x16x32_bf16 v[96:99], v[36:39], v[216:219], v[96:99]\n"
        "v_accvgpr_read_b32 v182, a166\n"
        "v_accvgpr_read_b32 v183, a167\n"
        "v_mfma_f32_16x16x32_bf16 v[100:103], v[44:47], v[216:219], v[100:103]\n"
        "global_load_lds_dwordx4 v242, s[66:67] offset:0\n"
        "v_mfma_f32_16x16x32_bf16 v[100:103], v[40:43], v[200:203], v[100:103]\n"
        "v_mul_f32 v177, s88, v177\n"
        "v_mul_f32 v178, s88, v178\n"
        "v_mfma_f32_16x16x32_bf16 v[132:135], v[40:43], v[204:207], v[132:135]\n"
        "ds_read_b64_tr_b16 v[40:41], v232 offset:50176\n"
        "v_mul_f32 v179, s88, v179\n"
        "v_mfma_f32_16x16x32_bf16 v[132:135], v[44:47], v[220:223], v[132:135]\n"
        "ds_read_b64_tr_b16 v[44:45], v232 offset:58368\n"
        "v_mul_f32 v180, s88, v180\n"
        "v_mfma_f32_16x16x32_bf16 v[128:131], v[36:39], v[220:223], v[128:131]\n"
        "ds_read_b64_tr_b16 v[36:37], v230 offset:58368\n"
        "global_load_lds_dwordx4 v243, s[66:67] offset:1024\n"
        "v_mfma_f32_16x16x32_bf16 v[128:131], v[32:35], v[204:207], v[128:131]\n"
        "ds_read_b64_tr_b16 v[32:33], v230 offset:50176\n"
        "v_accvgpr_read_b32 v184, a200\n"
        "v_mfma_f32_16x16x32_bf16 v[8:11], v[48:51], v[192:195], v[8:11]\n"
        "ds_read_b64_tr_b16 v[34:35], v231 offset:50176\n"
        "v_mul_f32 v184, s88, v184\n"
        "v_mfma_f32_16x16x32_bf16 v[8:11], v[52:55], v[208:211], v[8:11]\n"
        "ds_read_b64_tr_b16 v[38:39], v231 offset:58368\n"
        "v_cvt_pk_bf16_f32 v184, v184, v185\n"
        "v_mfma_f32_16x16x32_bf16 v[12:15], v[60:63], v[208:211], v[12:15]\n"
        "ds_read_b64_tr_b16 v[46:47], v233 offset:58368\n"
        "global_load_lds_dwordx4 v244, s[66:67] offset:2048\n"
        "v_mfma_f32_16x16x32_bf16 v[12:15], v[56:59], v[192:195], v[12:15]\n"
        "ds_read_b64_tr_b16 v[42:43], v233 offset:50176\n"
        "v_cvt_pk_bf16_f32 v185, v186, v187\n"
        "v_mfma_f32_16x16x32_bf16 v[76:79], v[56:59], v[196:199], v[76:79]\n"
        "v_cvt_pk_bf16_f32 v186, v188, v189\n"
        "v_cvt_pk_bf16_f32 v187, v190, v191\n"
        "v_mfma_f32_16x16x32_bf16 v[76:79], v[60:63], v[212:215], v[76:79]\n"
        "s_waitcnt lgkmcnt(6)\n"
        "v_accvgpr_write_b32 a236, v184\n"
        "v_accvgpr_write_b32 a237, v185\n"
        "v_mfma_f32_16x16x32_bf16 v[72:75], v[52:55], v[212:215], v[72:75]\n"
        "global_load_lds_dwordx4 v245, s[66:67] offset:3072\n"
        "s_add_u32 m0, s73, 0x24000\n"
        "v_mfma_f32_16x16x32_bf16 v[72:75], v[48:51], v[196:199], v[72:75]\n"
        "s_add_u32 s66, s66, 0x80\n"
        "s_addc_u32 s67, s67, 0\n"
        "v_accvgpr_write_b32 a238, v186\n"
        "v_mfma_f32_16x16x32_bf16 v[104:107], v[48:51], v[200:203], v[104:107]\n"
        "v_accvgpr_write_b32 a239, v187\n"
        "v_mul_f32 v181, s88, v181\n"
        "v_mfma_f32_16x16x32_bf16 v[104:107], v[52:55], v[216:219], v[104:107]\n"
        "v_mul_f32 v182, s88, v182\n"
        "v_mul_f32 v183, s88, v183\n"
        "v_mfma_f32_16x16x32_bf16 v[108:111], v[60:63], v[216:219], v[108:111]\n"
        "v_accvgpr_read_b32 v185, a169\n"
        "v_accvgpr_read_b32 v186, a170\n"
        "v_mfma_f32_16x16x32_bf16 v[108:111], v[56:59], v[200:203], v[108:111]\n"
        "s_waitcnt vmcnt(13) lgkmcnt(0)\n"
        "v_accvgpr_read_b32 v187, a171\n"
        "v_accvgpr_read_b32 v188, a172\n"
        "v_mfma_f32_16x16x32_bf16 v[140:143], v[56:59], v[204:207], v[140:143]\n"
        "s_barrier\n"
        "v_mfma_f32_16x16x32_bf16 v[140:143], v[60:63], v[220:223], v[140:143]\n"
        "ds_read_b64_tr_b16 v[60:61], v228 offset:24576\n"
        "global_load_lds_dwordx4 v238, s[68:69] offset:0\n"
        "v_mfma_f32_16x16x32_bf16 v[136:139], v[52:55], v[220:223], v[136:139]\n"
        "ds_read_b64_tr_b16 v[52:53], v226 offset:24576\n"
        "v_accvgpr_read_b32 v189, a173\n"
        "v_mfma_f32_16x16x32_bf16 v[136:139], v[48:51], v[204:207], v[136:139]\n"
        "ds_read_b64_tr_b16 v[48:49], v226 offset:16384\n"
        "v_accvgpr_read_b32 v190, a174\n"
        "v_mfma_f32_16x16x32_bf16 v[16:19], v[160:163], v[192:195], v[16:19]\n"
        "ds_read_b64_tr_b16 v[50:51], v227 offset:16384\n"
        "v_accvgpr_read_b32 v191, a175\n"
        "v_mfma_f32_16x16x32_bf16 v[16:19], v[164:167], v[208:211], v[16:19]\n"
        "ds_read_b64_tr_b16 v[54:55], v227 offset:24576\n"
        "global_load_lds_dwordx4 v239, s[68:69] offset:1024\n"
        "v_mfma_f32_16x16x32_bf16 v[20:23], v[172:175], v[208:211], v[20:23]\n"
        "ds_read_b64_tr_b16 v[62:63], v229 offset:24576\n"
        "v_mul_f32 v185, s88, v185\n"
        "v_mfma_f32_16x16x32_bf16 v[20:23], v[168:171], v[192:195], v[20:23]\n"
        "ds_read_b64_tr_b16 v[56:57], v228 offset:16384\n"
        "v_mul_f32 v186, s88, v186\n"
        "v_mfma_f32_16x16x32_bf16 v[84:87], v[168:171], v[196:199], v[84:87]\n"
        "ds_read_b64_tr_b16 v[58:59], v229 offset:16384\n"
        "v_mul_f32 v187, s88, v187\n"
        "v_mfma_f32_16x16x32_bf16 v[84:87], v[172:175], v[212:215], v[84:87]\n"
        "global_load_lds_dwordx4 v240, s[68:69] offset:2048\n"
        "v_mfma_f32_16x16x32_bf16 v[80:83], v[164:167], v[212:215], v[80:83]\n"
        "v_mul_f32 v188, s88, v188\n"
        "v_mul_f32 v189, s88, v189\n"
        "v_mfma_f32_16x16x32_bf16 v[80:83], v[160:163], v[196:199], v[80:83]\n"
        "v_accvgpr_read_b32 v176, a160\n"
        "v_mul_f32 v176, s88, v176\n"
        "v_mfma_f32_16x16x32_bf16 v[112:115], v[160:163], v[200:203], v[112:115]\n"
        "v_cvt_pk_bf16_f32 v176, v176, v177\n"
        "v_cvt_pk_bf16_f32 v177, v178, v179\n"
        "v_mfma_f32_16x16x32_bf16 v[112:115], v[164:167], v[216:219], v[112:115]\n"
        "v_cvt_pk_bf16_f32 v178, v180, v181\n"
        "v_cvt_pk_bf16_f32 v179, v182, v183\n"
        "v_mfma_f32_16x16x32_bf16 v[116:119], v[172:175], v[216:219], v[116:119]\n"
        "global_load_lds_dwordx4 v241, s[68:69] offset:3072\n"
        "s_add_u32 m0, s72, 0x10000\n"
        "v_accvgpr_write_b32 a192, v176\n"
        "v_mfma_f32_16x16x32_bf16 v[116:119], v[168:171], v[200:203], v[116:119]\n"
        "s_add_u32 s68, s68, 0x80000\n"
        "s_addc_u32 s69, s69, 0\n"
        "v_accvgpr_write_b32 a193, v177\n"
        "v_mfma_f32_16x16x32_bf16 v[148:151], v[168:171], v[204:207], v[148:151]\n"
        "ds_read_b64_tr_b16 v[168:169], v232 offset:16384\n"
        "v_accvgpr_write_b32 a194, v178\n"
        "v_mfma_f32_16x16x32_bf16 v[148:151], v[172:175], v[220:223], v[148:151]\n"
        "ds_read_b64_tr_b16 v[172:173], v232 offset:24576\n"
        "v_accvgpr_write_b32 a195, v179\n"
        "v_mfma_f32_16x16x32_bf16 v[144:147], v[164:167], v[220:223], v[144:147]\n"
        "ds_read_b64_tr_b16 v[164:165], v230 offset:24576\n"
        "global_load_lds_dwordx4 v242, s[64:65] offset:0\n"
        "v_mfma_f32_16x16x32_bf16 v[144:147], v[160:163], v[204:207], v[144:147]\n"
        "ds_read_b64_tr_b16 v[160:161], v230 offset:16384\n"
        "v_mul_f32 v190, s88, v190\n"
        "v_mfma_f32_16x16x32_bf16 v[24:27], v[32:35], v[192:195], v[24:27]\n"
        "ds_read_b64_tr_b16 v[162:163], v231 offset:16384\n"
        "v_mul_f32 v191, s88, v191\n"
        "v_mfma_f32_16x16x32_bf16 v[24:27], v[36:39], v[208:211], v[24:27]\n"
        "ds_read_b64_tr_b16 v[166:167], v231 offset:24576\n"
        "v_accvgpr_read_b32 v177, a1\n"
        "v_mfma_f32_16x16x32_bf16 v[28:31], v[44:47], v[208:211], v[28:31]\n"
        "ds_read_b64_tr_b16 v[174:175], v233 offset:24576\n"
        "global_load_lds_dwordx4 v243, s[64:65] offset:1024\n"
        "v_mfma_f32_16x16x32_bf16 v[28:31], v[40:43], v[192:195], v[28:31]\n"
        "ds_read_b64_tr_b16 v[170:171], v233 offset:16384\n"
        "v_accvgpr_read_b32 v178, a2\n"
        "v_mfma_f32_16x16x32_bf16 v[92:95], v[40:43], v[196:199], v[92:95]\n"
        "ds_read_b128 v[192:195], v246 offset:34816\n"
        "v_accvgpr_read_b32 v179, a3\n"
        "v_mfma_f32_16x16x32_bf16 v[92:95], v[44:47], v[212:215], v[92:95]\n"
        "ds_read_b128 v[208:211], v247 offset:34816\n"
        "v_accvgpr_read_b32 v180, a4\n"
        "v_mfma_f32_16x16x32_bf16 v[88:91], v[36:39], v[212:215], v[88:91]\n"
        "ds_read_b128 v[212:215], v247 offset:32768\n"
        "global_load_lds_dwordx4 v244, s[64:65] offset:2048\n"
        "v_mfma_f32_16x16x32_bf16 v[88:91], v[32:35], v[196:199], v[88:91]\n"
        "ds_read_b128 v[196:199], v246 offset:32768\n"
        "v_accvgpr_read_b32 v181, a5\n"
        "v_mfma_f32_16x16x32_bf16 v[120:123], v[32:35], v[200:203], v[120:123]\n"
        "v_accvgpr_read_b32 v182, a6\n"
        "v_accvgpr_read_b32 v183, a7\n"
        "v_mfma_f32_16x16x32_bf16 v[120:123], v[36:39], v[216:219], v[120:123]\n"
        "v_mul_f32 v177, s88, v177\n"
        "v_mul_f32 v178, s88, v178\n"
        "v_mfma_f32_16x16x32_bf16 v[124:127], v[44:47], v[216:219], v[124:127]\n"
        "ds_read_b128 v[216:219], v247 offset:38912\n"
        "global_load_lds_dwordx4 v245, s[64:65] offset:3072\n"
        "s_add_u32 m0, s72, 0x11000\n"
        "v_mfma_f32_16x16x32_bf16 v[124:127], v[40:43], v[200:203], v[124:127]\n"
        "ds_read_b128 v[200:203], v246 offset:38912\n"
        "s_add_u32 s64, s64, 0x80\n"
        "s_addc_u32 s65, s65, 0\n"
        "v_mfma_f32_16x16x32_bf16 v[156:159], v[40:43], v[204:207], v[156:159]\n"
        "ds_read_b64_tr_b16 v[40:41], v228 offset:17408\n"
        "v_mul_f32 v179, s88, v179\n"
        "v_mfma_f32_16x16x32_bf16 v[156:159], v[44:47], v[220:223], v[156:159]\n"
        "ds_read_b64_tr_b16 v[44:45], v228 offset:25600\n"
        "v_mul_f32 v180, s88, v180\n"
        "v_mfma_f32_16x16x32_bf16 v[152:155], v[36:39], v[220:223], v[152:155]\n"
        "ds_read_b128 v[220:223], v247 offset:36864\n"
        "global_load_lds_dwordx4 v242, s[66:67] offset:0\n"
        "v_mfma_f32_16x16x32_bf16 v[152:155], v[32:35], v[204:207], v[152:155]\n"
        "ds_read_b128 v[204:207], v246 offset:36864\n"
        "s_waitcnt lgkmcnt(6)\n"
        "v_mfma_f32_16x16x32_bf16 v[0:3], v[48:51], v[192:195], v[0:3]\n"
        "ds_read_b64_tr_b16 v[32:33], v226 offset:17408\n"
        "v_accvgpr_read_b32 v184, a168\n"
        "v_mfma_f32_16x16x32_bf16 v[0:3], v[52:55], v[208:211], v[0:3]\n"
        "ds_read_b64_tr_b16 v[34:35], v227 offset:17408\n"
        "v_mul_f32 v184, s88, v184\n"
        "v_mfma_f32_16x16x32_bf16 v[4:7], v[60:63], v[208:211], v[4:7]\n"
        "ds_read_b64_tr_b16 v[36:37], v226 offset:25600\n"
        "v_cvt_pk_bf16_f32 v184, v184, v185\n"
        "v_mfma_f32_16x16x32_bf16 v[4:7], v[56:59], v[192:195], v[4:7]\n"
        "global_store_dwordx4 v254, a[228:231], s[70:71] offset:1024 sc1\n"
        "ds_read_b64_tr_b16 v[38:39], v227 offset:25600\n"
        "s_waitcnt lgkmcnt(8)\n"
        "v_cvt_pk_bf16_f32 v185, v186, v187\n"
        "v_cvt_pk_bf16_f32 v186, v188, v189\n"
        "v_mfma_f32_16x16x32_bf16 v[68:71], v[56:59], v[196:199], v[68:71]\n"
        "ds_read_b64_tr_b16 v[46:47], v229 offset:25600\n"
        "v_cvt_pk_bf16_f32 v187, v190, v191\n"
        "v_mfma_f32_16x16x32_bf16 v[68:71], v[60:63], v[212:215], v[68:71]\n"
        "ds_read_b64_tr_b16 v[42:43], v229 offset:17408\n"
        "v_accvgpr_write_b32 a196, v184\n"
        "v_mfma_f32_16x16x32_bf16 v[64:67], v[52:55], v[212:215], v[64:67]\n"
        "s_waitcnt lgkmcnt(6)\n"
        "v_accvgpr_write_b32 a197, v185\n"
        "v_accvgpr_write_b32 a198, v186\n"
        "v_mfma_f32_16x16x32_bf16 v[64:67], v[48:51], v[196:199], v[64:67]\n"
        "global_load_lds_dwordx4 v243, s[66:67] offset:1024\n"
        "v_accvgpr_write_b32 a199, v187\n"
        "v_mfma_f32_16x16x32_bf16 v[96:99], v[48:51], v[200:203], v[96:99]\n"
        "s_add_u32 s70, s38, s97\n"
        "s_addc_u32 s71, s39, 0\n"
        "v_mul_f32 v181, s88, v181\n"
        "v_mfma_f32_16x16x32_bf16 v[96:99], v[52:55], v[216:219], v[96:99]\n"
        "s_add_u32 s97, s52, 0x5000\n"
        "v_mul_f32 v182, s88, v182\n"
        "v_mul_f32 v183, s88, v183\n"
        "v_mfma_f32_16x16x32_bf16 v[100:103], v[60:63], v[216:219], v[100:103]\n"
        "v_accvgpr_read_b32 v185, a9\n"
        "v_accvgpr_read_b32 v186, a10\n"
        "v_mfma_f32_16x16x32_bf16 v[100:103], v[56:59], v[200:203], v[100:103]\n"
        "global_load_lds_dwordx4 v244, s[66:67] offset:2048\n"
        "v_accvgpr_read_b32 v187, a11\n"
        "v_mfma_f32_16x16x32_bf16 v[132:135], v[56:59], v[204:207], v[132:135]\n"
        "ds_read_b64_tr_b16 v[56:57], v232 offset:17408\n"
        "v_accvgpr_read_b32 v188, a12\n"
        "v_mfma_f32_16x16x32_bf16 v[132:135], v[60:63], v[220:223], v[132:135]\n"
        "ds_read_b64_tr_b16 v[60:61], v232 offset:25600\n"
        "v_accvgpr_read_b32 v189, a13\n"
        "v_mfma_f32_16x16x32_bf16 v[128:131], v[52:55], v[220:223], v[128:131]\n"
        "ds_read_b64_tr_b16 v[52:53], v230 offset:25600\n"
        "v_accvgpr_read_b32 v190, a14\n"
        "v_mfma_f32_16x16x32_bf16 v[128:131], v[48:51], v[204:207], v[128:131]\n"
        "ds_read_b64_tr_b16 v[48:49], v230 offset:17408\n"
        "global_load_lds_dwordx4 v245, s[66:67] offset:3072\n"
        "s_add_u32 m0, s73, 0x1c000\n"
        "v_mfma_f32_16x16x32_bf16 v[8:11], v[160:163], v[192:195], v[8:11]\n"
        "ds_read_b64_tr_b16 v[50:51], v231 offset:17408\n"
        "s_add_u32 s66, s66, 0x80\n"
        "s_addc_u32 s67, s67, 0\n"
        "v_mfma_f32_16x16x32_bf16 v[8:11], v[164:167], v[208:211], v[8:11]\n"
        "ds_read_b64_tr_b16 v[54:55], v231 offset:25600\n"
        "v_accvgpr_read_b32 v191, a15\n"
        "v_mfma_f32_16x16x32_bf16 v[12:15], v[172:175], v[208:211], v[12:15]\n"
        "ds_read_b64_tr_b16 v[62:63], v233 offset:25600\n"
        "v_mul_f32 v185, s88, v185\n"
        "v_mfma_f32_16x16x32_bf16 v[12:15], v[168:171], v[192:195], v[12:15]\n"
        "ds_read_b64_tr_b16 v[58:59], v233 offset:17408\n"
        "v_mul_f32 v186, s88, v186\n"
        "v_mfma_f32_16x16x32_bf16 v[76:79], v[168:171], v[196:199], v[76:79]\n"
        "v_mul_f32 v187, s88, v187\n"
        "v_mul_f32 v188, s88, v188\n"
        "v_mfma_f32_16x16x32_bf16 v[76:79], v[172:175], v[212:215], v[76:79]\n"
        "s_waitcnt lgkmcnt(6)\n"
        "v_mul_f32 v189, s88, v189\n"
        "v_mul_f32 v190, s88, v190\n"
        "v_mfma_f32_16x16x32_bf16 v[72:75], v[164:167], v[212:215], v[72:75]\n"
        "v_mul_f32 v191, s88, v191\n"
        "v_mfma_f32_16x16x32_bf16 v[72:75], v[160:163], v[196:199], v[72:75]\n"
        "v_accvgpr_read_b32 v176, a0\n"
        "v_mul_f32 v176, s88, v176\n"
        "v_mfma_f32_16x16x32_bf16 v[104:107], v[160:163], v[200:203], v[104:107]\n"
        "v_cvt_pk_bf16_f32 v176, v176, v177\n"
        "v_cvt_pk_bf16_f32 v177, v178, v179\n"
        "v_mfma_f32_16x16x32_bf16 v[104:107], v[164:167], v[216:219], v[104:107]\n"
        "v_cvt_pk_bf16_f32 v178, v180, v181\n"
        "v_cvt_pk_bf16_f32 v179, v182, v183\n"
        "v_mfma_f32_16x16x32_bf16 v[108:111], v[172:175], v[216:219], v[108:111]\n"
        "v_accvgpr_write_b32 a200, v176\n"
        "v_accvgpr_write_b32 a201, v177\n"
        "v_mfma_f32_16x16x32_bf16 v[108:111], v[168:171], v[200:203], v[108:111]\n"
        "s_waitcnt vmcnt(13) lgkmcnt(0)\n"
        "v_accvgpr_write_b32 a202, v178\n"
        "v_accvgpr_write_b32 a203, v179\n"
        "v_mfma_f32_16x16x32_bf16 v[140:143], v[168:171], v[204:207], v[140:143]\n"
        "s_barrier\n"
        "v_mfma_f32_16x16x32_bf16 v[140:143], v[172:175], v[220:223], v[140:143]\n"
        "ds_read_b64_tr_b16 v[172:173], v228 offset:40960\n"
        "global_load_lds_dwordx4 v238, s[68:69] offset:0\n"
        "v_mfma_f32_16x16x32_bf16 v[136:139], v[164:167], v[220:223], v[136:139]\n"
        "ds_read_b64_tr_b16 v[164:165], v226 offset:40960\n"
        "v_accvgpr_read_b32 v177, a33\n"
        "v_mfma_f32_16x16x32_bf16 v[136:139], v[160:163], v[204:207], v[136:139]\n"
        "ds_read_b64_tr_b16 v[160:161], v226 offset:32768\n"
        "v_accvgpr_read_b32 v178, a34\n"
        "v_mfma_f32_16x16x32_bf16 v[16:19], v[32:35], v[192:195], v[16:19]\n"
        "ds_read_b64_tr_b16 v[162:163], v227 offset:32768\n"
        "v_accvgpr_read_b32 v179, a35\n"
        "v_mfma_f32_16x16x32_bf16 v[16:19], v[36:39], v[208:211], v[16:19]\n"
        "ds_read_b64_tr_b16 v[166:167], v227 offset:40960\n"
        "global_load_lds_dwordx4 v239, s[68:69] offset:1024\n"
        "v_mfma_f32_16x16x32_bf16 v[20:23], v[44:47], v[208:211], v[20:23]\n"
        "ds_read_b64_tr_b16 v[174:175], v229 offset:40960\n"
        "v_accvgpr_read_b32 v180, a36\n"
        "v_mfma_f32_16x16x32_bf16 v[20:23], v[40:43], v[192:195], v[20:23]\n"
        "ds_read_b64_tr_b16 v[168:169], v228 offset:32768\n"
        "v_accvgpr_read_b32 v181, a37\n"
        "v_mfma_f32_16x16x32_bf16 v[84:87], v[40:43], v[196:199], v[84:87]\n"
        "ds_read_b64_tr_b16 v[170:171], v229 offset:32768\n"
        "v_accvgpr_read_b32 v182, a38\n"
        "v_mfma_f32_16x16x32_bf16 v[84:87], v[44:47], v[212:215], v[84:87]\n"
        "global_load_lds_dwordx4 v240, s[68:69] offset:2048\n"
        "v_mfma_f32_16x16x32_bf16 v[80:83], v[36:39], v[212:215], v[80:83]\n"
        "v_accvgpr_read_b32 v183, a39\n"
        "v_mul_f32 v177, s88, v177\n"
        "v_mfma_f32_16x16x32_bf16 v[80:83], v[32:35], v[196:199], v[80:83]\n"
        "v_mul_f32 v178, s88, v178\n"
        "v_mul_f32 v179, s88, v179\n"
        "v_mfma_f32_16x16x32_bf16 v[112:115], v[32:35], v[200:203], v[112:115]\n"
        "v_mul_f32 v180, s88, v180\n"
        "v_mul_f32 v181, s88, v181\n"
        "v_mfma_f32_16x16x32_bf16 v[112:115], v[36:39], v[216:219], v[112:115]\n"
        "global_load_lds_dwordx4 v241, s[68:69] offset:3072\n"
        "s_add_u32 m0, s72, 0x8000\n"
        "v_mfma_f32_16x16x32_bf16 v[116:119], v[44:47], v[216:219], v[116:119]\n"
        "s_add_u32 s68, s68, 0x80000\n"
        "s_addc_u32 s69, s69, 0\n"
        "v_mul_f32 v182, s88, v182\n"
        "v_mfma_f32_16x16x32_bf16 v[116:119], v[40:43], v[200:203], v[116:119]\n"
        "v_mul_f32 v183, s88, v183\n"
        "v_mfma_f32_16x16x32_bf16 v[148:151], v[40:43], v[204:207], v[148:151]\n"
        "ds_read_b64_tr_b16 v[40:41], v232 offset:32768\n"
        "v_mfma_f32_16x16x32_bf16 v[148:151], v[44:47], v[220:223], v[148:151]\n"
        "ds_read_b64_tr_b16 v[44:45], v232 offset:40960\n"
        "global_load_lds_dwordx4 v242, s[64:65] offset:0\n"
        "v_mfma_f32_16x16x32_bf16 v[144:147], v[36:39], v[220:223], v[144:147]\n"
        "ds_read_b64_tr_b16 v[36:37], v230 offset:40960\n"
        "v_mfma_f32_16x16x32_bf16 v[144:147], v[32:35], v[204:207], v[144:147]\n"
        "ds_read_b64_tr_b16 v[32:33], v230 offset:32768\n"
        "v_accvgpr_read_b32 v184, a8\n"
        "v_mfma_f32_16x16x32_bf16 v[24:27], v[48:51], v[192:195], v[24:27]\n"
        "ds_read_b64_tr_b16 v[34:35], v231 offset:32768\n"
        "v_mul_f32 v184, s88, v184\n"
        "v_mfma_f32_16x16x32_bf16 v[24:27], v[52:55], v[208:211], v[24:27]\n"
        "ds_read_b64_tr_b16 v[38:39], v231 offset:40960\n"
        "v_cvt_pk_bf16_f32 v184, v184, v185\n"
        "v_mfma_f32_16x16x32_bf16 v[28:31], v[60:63], v[208:211], v[28:31]\n"
        "ds_read_b64_tr_b16 v[46:47], v233 offset:40960\n"
        "v_cvt_pk_bf16_f32 v185, v186, v187\n"
        "v_mfma_f32_16x16x32_bf16 v[28:31], v[56:59], v[192:195], v[28:31]\n"
        "ds_read_b64_tr_b16 v[42:43], v233 offset:32768\n"
        "v_cvt_pk_bf16_f32 v186, v188, v189\n"
        "v_mfma_f32_16x16x32_bf16 v[92:95], v[56:59], v[196:199], v[92:95]\n"
        "ds_read_b128 v[192:195], v246 offset:2048\n"
        "v_cvt_pk_bf16_f32 v187, v190, v191\n"
        "v_mfma_f32_16x16x32_bf16 v[92:95], v[60:63], v[212:215], v[92:95]\n"
        "ds_read_b128 v[208:211], v247 offset:2048\n"
        "v_accvgpr_write_b32 a204, v184\n"
        "v_mfma_f32_16x16x32_bf16 v[88:91], v[52:55], v[212:215], v[88:91]\n"
        "ds_read_b128 v[212:215], v247 offset:0\n"
        "v_accvgpr_write_b32 a205, v185\n"
        "v_mfma_f32_16x16x32_bf16 v[88:91], v[48:51], v[196:199], v[88:91]\n"
        "ds_read_b128 v[196:199], v246 offset:0\n"
        "v_accvgpr_write_b32 a206, v186\n"
        "v_mfma_f32_16x16x32_bf16 v[120:123], v[48:51], v[200:203], v[120:123]\n"
        "global_load_lds_dwordx4 v243, s[64:65] offset:1024\n"
        "v_accvgpr_write_b32 a207, v187\n"
        "v_mfma_f32_16x16x32_bf16 v[120:123], v[52:55], v[216:219], v[120:123]\n"
        "v_accvgpr_read_b32 v185, a41\n"
        "v_accvgpr_read_b32 v186, a42\n"
        "v_mfma_f32_16x16x32_bf16 v[124:127], v[60:63], v[216:219], v[124:127]\n"
        "ds_read_b128 v[216:219], v247 offset:6144\n"
        "v_accvgpr_read_b32 v187, a43\n"
        "v_mfma_f32_16x16x32_bf16 v[124:127], v[56:59], v[200:203], v[124:127]\n"
        "ds_read_b128 v[200:203], v246 offset:6144\n"
        "v_accvgpr_read_b32 v188, a44\n"
        "v_mfma_f32_16x16x32_bf16 v[156:159], v[56:59], v[204:207], v[156:159]\n"
        "global_store_dwordx4 v254, a[232:235], s[70:71] offset:0 sc1\n"
        "ds_read_b64_tr_b16 v[56:57], v228 offset:33792\n"
        "ds_read_b64_tr_b16 v[58:59], v229 offset:33792\n"
        "v_accvgpr_read_b32 v189, a45\n"
        "v_mfma_f32_16x16x32_bf16 v[156:159], v[60:63], v[220:223], v[156:159]\n"
        "ds_read_b64_tr_b16 v[60:61], v228 offset:41984\n"
        "v_accvgpr_read_b32 v190, a46\n"
        "v_mfma_f32_16x16x32_bf16 v[152:155], v[52:55], v[220:223], v[152:155]\n"
        "ds_read_b128 v[220:223], v247 offset:4096\n"
        "v_accvgpr_read_b32 v191, a47\n"
        "v_mfma_f32_16x16x32_bf16 v[152:155], v[48:51], v[204:207], v[152:155]\n"
        "ds_read_b128 v[204:207], v246 offset:4096\n"
        "s_waitcnt lgkmcnt(7)\n"
        "v_mfma_f32_16x16x32_bf16 v[0:3], v[160:163], v[192:195], v[0:3]\n"
        "ds_read_b64_tr_b16 v[48:49], v226 offset:33792\n"
        "global_load_lds_dwordx4 v244, s[64:65] offset:2048\n"
        "v_mfma_f32_16x16x32_bf16 v[0:3], v[164:167], v[208:211], v[0:3]\n"
        "ds_read_b64_tr_b16 v[50:51], v227 offset:33792\n"
        "v_mul_f32 v185, s88, v185\n"
        "v_mfma_f32_16x16x32_bf16 v[4:7], v[172:175], v[208:211], v[4:7]\n"
        "ds_read_b64_tr_b16 v[52:53], v226 offset:41984\n"
        "v_mul_f32 v186, s88, v186\n"
        "v_mfma_f32_16x16x32_bf16 v[4:7], v[168:171], v[192:195], v[4:7]\n"
        "ds_read_b64_tr_b16 v[54:55], v227 offset:41984\n"
        "v_mul_f32 v187, s88, v187\n"
        "v_mfma_f32_16x16x32_bf16 v[68:71], v[168:171], v[196:199], v[68:71]\n"
        "ds_read_b64_tr_b16 v[62:63], v229 offset:41984\n"
        "global_load_lds_dwordx4 v245, s[64:65] offset:3072\n"
        "s_add_u32 m0, s72, 0x9000\n"
        "v_mfma_f32_16x16x32_bf16 v[68:71], v[172:175], v[212:215], v[68:71]\n"
        "s_add_u32 s64, s64, 0x80\n"
        "s_addc_u32 s65, s65, 0\n"
        "v_mul_f32 v188, s88, v188\n"
        "v_mfma_f32_16x16x32_bf16 v[64:67], v[164:167], v[212:215], v[64:67]\n"
        "s_waitcnt lgkmcnt(6)\n"
        "v_mul_f32 v189, s88, v189\n"
        "v_mul_f32 v190, s88, v190\n"
        "v_mfma_f32_16x16x32_bf16 v[64:67], v[160:163], v[196:199], v[64:67]\n"
        "s_waitcnt lgkmcnt(5)\n"
        "v_accvgpr_read_b32 v176, a32\n"
        "v_mul_f32 v176, s88, v176\n"
        "v_mfma_f32_16x16x32_bf16 v[96:99], v[160:163], v[200:203], v[96:99]\n"
        "global_load_lds_dwordx4 v242, s[66:67] offset:0\n"
        "v_mfma_f32_16x16x32_bf16 v[96:99], v[164:167], v[216:219], v[96:99]\n"
        "v_cvt_pk_bf16_f32 v176, v176, v177\n"
        "v_cvt_pk_bf16_f32 v177, v178, v179\n"
        "v_mfma_f32_16x16x32_bf16 v[100:103], v[172:175], v[216:219], v[100:103]\n"
        "v_cvt_pk_bf16_f32 v178, v180, v181\n"
        "v_cvt_pk_bf16_f32 v179, v182, v183\n"
        "v_mfma_f32_16x16x32_bf16 v[100:103], v[168:171], v[200:203], v[100:103]\n"
        "v_accvgpr_write_b32 a160, v176\n"
        "v_accvgpr_write_b32 a161, v177\n"
        "v_mfma_f32_16x16x32_bf16 v[132:135], v[168:171], v[204:207], v[132:135]\n"
        "ds_read_b64_tr_b16 v[168:169], v232 offset:33792\n"
        "global_load_lds_dwordx4 v243, s[66:67] offset:1024\n"
        "v_mfma_f32_16x16x32_bf16 v[132:135], v[172:175], v[220:223], v[132:135]\n"
        "ds_read_b64_tr_b16 v[172:173], v232 offset:41984\n"
        "v_accvgpr_write_b32 a162, v178\n"
        "v_mfma_f32_16x16x32_bf16 v[128:131], v[164:167], v[220:223], v[128:131]\n"
        "ds_read_b64_tr_b16 v[164:165], v230 offset:41984\n"
        "v_accvgpr_write_b32 a163, v179\n"
        "v_mfma_f32_16x16x32_bf16 v[128:131], v[160:163], v[204:207], v[128:131]\n"
        "ds_read_b64_tr_b16 v[160:161], v230 offset:33792\n"
        "v_mul_f32 v191, s88, v191\n"
        "v_mfma_f32_16x16x32_bf16 v[8:11], v[32:35], v[192:195], v[8:11]\n"
        "ds_read_b64_tr_b16 v[162:163], v231 offset:33792\n"
        "global_load_lds_dwordx4 v244, s[66:67] offset:2048\n"
        "v_mfma_f32_16x16x32_bf16 v[8:11], v[36:39], v[208:211], v[8:11]\n"
        "ds_read_b64_tr_b16 v[166:167], v231 offset:41984\n"
        "v_accvgpr_read_b32 v177, a65\n"
        "v_mfma_f32_16x16x32_bf16 v[12:15], v[44:47], v[208:211], v[12:15]\n"
        "ds_read_b64_tr_b16 v[174:175], v233 offset:41984\n"
        "v_accvgpr_read_b32 v178, a66\n"
        "v_mfma_f32_16x16x32_bf16 v[12:15], v[40:43], v[192:195], v[12:15]\n"
        "ds_read_b64_tr_b16 v[170:171], v233 offset:33792\n"
        "v_accvgpr_read_b32 v179, a67\n"
        "v_mfma_f32_16x16x32_bf16 v[76:79], v[40:43], v[196:199], v[76:79]\n"
        "global_load_lds_dwordx4 v245, s[66:67] offset:3072\n"
        "s_add_u32 m0, s73, 0x20000\n"
        "v_mfma_f32_16x16x32_bf16 v[76:79], v[44:47], v[212:215], v[76:79]\n"
        "s_add_u32 s66, s66, 0x80\n"
        "s_addc_u32 s67, s67, 0\n"
        "v_accvgpr_read_b32 v180, a68\n"
        "v_mfma_f32_16x16x32_bf16 v[72:75], v[36:39], v[212:215], v[72:75]\n"
        "s_waitcnt lgkmcnt(5)\n"
        "v_accvgpr_read_b32 v181, a69\n"
        "v_accvgpr_read_b32 v182, a70\n"
        "v_mfma_f32_16x16x32_bf16 v[72:75], v[32:35], v[196:199], v[72:75]\n"
        "v_accvgpr_read_b32 v183, a71\n"
        "v_mul_f32 v177, s88, v177\n"
        "v_mfma_f32_16x16x32_bf16 v[104:107], v[32:35], v[200:203], v[104:107]\n"
        "v_mul_f32 v178, s88, v178\n"
        "v_mul_f32 v179, s88, v179\n"
        "v_mfma_f32_16x16x32_bf16 v[104:107], v[36:39], v[216:219], v[104:107]\n"
        "v_mul_f32 v180, s88, v180\n"
        "v_mul_f32 v181, s88, v181\n"
        "v_mfma_f32_16x16x32_bf16 v[108:111], v[44:47], v[216:219], v[108:111]\n"
        "v_mul_f32 v182, s88, v182\n"
        "v_mul_f32 v183, s88, v183\n"
        "v_mfma_f32_16x16x32_bf16 v[108:111], v[40:43], v[200:203], v[108:111]\n"
        "s_waitcnt vmcnt(13) lgkmcnt(0)\n"
        "v_mfma_f32_16x16x32_bf16 v[140:143], v[40:43], v[204:207], v[140:143]\n"
        "s_barrier\n"
        "v_mfma_f32_16x16x32_bf16 v[140:143], v[44:47], v[220:223], v[140:143]\n"
        "ds_read_b64_tr_b16 v[44:45], v228 offset:57344\n"
        "global_load_lds_dwordx4 v238, s[68:69] offset:0\n"
        "v_mfma_f32_16x16x32_bf16 v[136:139], v[36:39], v[220:223], v[136:139]\n"
        "ds_read_b64_tr_b16 v[36:37], v226 offset:57344\n"
        "v_mfma_f32_16x16x32_bf16 v[136:139], v[32:35], v[204:207], v[136:139]\n"
        "ds_read_b64_tr_b16 v[32:33], v226 offset:49152\n"
        "v_accvgpr_read_b32 v184, a40\n"
        "v_mfma_f32_16x16x32_bf16 v[16:19], v[48:51], v[192:195], v[16:19]\n"
        "ds_read_b64_tr_b16 v[34:35], v227 offset:49152\n"
        "v_mul_f32 v184, s88, v184\n"
        "v_mfma_f32_16x16x32_bf16 v[16:19], v[52:55], v[208:211], v[16:19]\n"
        "ds_read_b64_tr_b16 v[38:39], v227 offset:57344\n"
        "v_cvt_pk_bf16_f32 v184, v184, v185\n"
        "v_mfma_f32_16x16x32_bf16 v[20:23], v[60:63], v[208:211], v[20:23]\n"
        "ds_read_b64_tr_b16 v[46:47], v229 offset:57344\n"
        "v_cvt_pk_bf16_f32 v185, v186, v187\n"
        "v_mfma_f32_16x16x32_bf16 v[20:23], v[56:59], v[192:195], v[20:23]\n"
        "ds_read_b64_tr_b16 v[40:41], v228 offset:49152\n"
        "v_cvt_pk_bf16_f32 v186, v188, v189\n"
        "v_mfma_f32_16x16x32_bf16 v[84:87], v[56:59], v[196:199], v[84:87]\n"
        "ds_read_b64_tr_b16 v[42:43], v229 offset:49152\n"
        "v_cvt_pk_bf16_f32 v187, v190, v191\n"
        "v_mfma_f32_16x16x32_bf16 v[84:87], v[60:63], v[212:215], v[84:87]\n"
        "global_load_lds_dwordx4 v239, s[68:69] offset:1024\n"
        "v_accvgpr_write_b32 a164, v184\n"
        "v_mfma_f32_16x16x32_bf16 v[80:83], v[52:55], v[212:215], v[80:83]\n"
        "v_accvgpr_write_b32 a165, v185\n"
        "v_accvgpr_write_b32 a166, v186\n"
        "v_mfma_f32_16x16x32_bf16 v[80:83], v[48:51], v[196:199], v[80:83]\n"
        "v_accvgpr_write_b32 a167, v187\n"
        "v_accvgpr_read_b32 v185, a73\n"
        "v_mfma_f32_16x16x32_bf16 v[112:115], v[48:51], v[200:203], v[112:115]\n"
        "v_accvgpr_read_b32 v186, a74\n"
        "v_accvgpr_read_b32 v187, a75\n"
        "v_mfma_f32_16x16x32_bf16 v[112:115], v[52:55], v[216:219], v[112:115]\n"
        "global_load_lds_dwordx4 v240, s[68:69] offset:2048\n"
        "v_accvgpr_read_b32 v188, a76\n"
        "v_mfma_f32_16x16x32_bf16 v[116:119], v[60:63], v[216:219], v[116:119]\n"
        "v_accvgpr_read_b32 v189, a77\n"
        "v_accvgpr_read_b32 v190, a78\n"
        "v_mfma_f32_16x16x32_bf16 v[116:119], v[56:59], v[200:203], v[116:119]\n"
        "v_accvgpr_read_b32 v191, a79\n"
        "v_mul_f32 v185, s88, v185\n"
        "v_mfma_f32_16x16x32_bf16 v[148:151], v[56:59], v[204:207], v[148:151]\n"
        "ds_read_b64_tr_b16 v[56:57], v232 offset:49152\n"
        "v_mul_f32 v186, s88, v186\n"
        "v_mfma_f32_16x16x32_bf16 v[148:151], v[60:63], v[220:223], v[148:151]\n"
        "ds_read_b64_tr_b16 v[60:61], v232 offset:57344\n"
        "global_load_lds_dwordx4 v241, s[68:69] offset:3072\n"
        "s_add_u32 m0, s72, 0x0\n"
        "v_mfma_f32_16x16x32_bf16 v[144:147], v[52:55], v[220:223], v[144:147]\n"
        "ds_read_b64_tr_b16 v[52:53], v230 offset:57344\n"
        "s_add_u32 s68, s68, 0x80000\n"
        "s_addc_u32 s69, s69, 0\n"
        "v_mfma_f32_16x16x32_bf16 v[144:147], v[48:51], v[204:207], v[144:147]\n"
        "ds_read_b64_tr_b16 v[48:49], v230 offset:49152\n"
        "v_mul_f32 v187, s88, v187\n"
        "v_mfma_f32_16x16x32_bf16 v[24:27], v[160:163], v[192:195], v[24:27]\n"
        "ds_read_b64_tr_b16 v[50:51], v231 offset:49152\n"
        "v_mul_f32 v188, s88, v188\n"
        "LHA6_%=:\n"
        "s_cbranch_execz LHA10_%=\n"
        "v_mfma_f32_16x16x32_bf16 v[24:27], v[164:167], v[208:211], v[24:27]\n"
        "ds_read_b64_tr_b16 v[54:55], v231 offset:57344\n"
        "global_load_lds_dwordx4 v242, s[64:65] offset:0\n"
        "v_mfma_f32_16x16x32_bf16 v[28:31], v[172:175], v[208:211], v[28:31]\n"
        "ds_read_b64_tr_b16 v[62:63], v233 offset:57344\n"
        "v_mul_f32 v189, s88, v189\n"
        "v_mfma_f32_16x16x32_bf16 v[28:31], v[168:171], v[192:195], v[28:31]\n"
        "ds_read_b64_tr_b16 v[58:59], v233 offset:49152\n"
        "v_mul_f32 v190, s88, v190\n"
        "v_mfma_f32_16x16x32_bf16 v[92:95], v[168:171], v[196:199], v[92:95]\n"
        "ds_read_b128 v[192:195], v248 offset:2048\n"
        "v_mul_f32 v191, s88, v191\n"
        "v_mfma_f32_16x16x32_bf16 v[92:95], v[172:175], v[212:215], v[92:95]\n"
        "ds_read_b128 v[208:211], v249 offset:2048\n"
        "global_load_lds_dwordx4 v243, s[64:65] offset:1024\n"
        "v_mfma_f32_16x16x32_bf16 v[88:91], v[164:167], v[212:215], v[88:91]\n"
        "ds_read_b128 v[212:215], v249 offset:0\n"
        "v_mfma_f32_16x16x32_bf16 v[88:91], v[160:163], v[196:199], v[88:91]\n"
        "ds_read_b128 v[196:199], v248 offset:0\n"
        "v_accvgpr_read_b32 v176, a64\n"
        "v_mfma_f32_16x16x32_bf16 v[120:123], v[160:163], v[200:203], v[120:123]\n"
        "v_mul_f32 v176, s88, v176\n"
        "v_cvt_pk_bf16_f32 v176, v176, v177\n"
        "v_mfma_f32_16x16x32_bf16 v[120:123], v[164:167], v[216:219], v[120:123]\n"
        "v_cvt_pk_bf16_f32 v177, v178, v179\n"
        "v_cvt_pk_bf16_f32 v178, v180, v181\n"
        "v_mfma_f32_16x16x32_bf16 v[124:127], v[172:175], v[216:219], v[124:127]\n"
        "ds_read_b128 v[216:219], v249 offset:6144\n"
        "v_cvt_pk_bf16_f32 v179, v182, v183\n"
        "v_mfma_f32_16x16x32_bf16 v[124:127], v[168:171], v[200:203], v[124:127]\n"
        "ds_read_b128 v[200:203], v248 offset:6144\n"
        "v_accvgpr_write_b32 a168, v176\n"
        "v_mfma_f32_16x16x32_bf16 v[156:159], v[168:171], v[204:207], v[156:159]\n"
        "ds_read_b64_tr_b16 v[168:169], v228 offset:50176\n"
        "v_accvgpr_write_b32 a169, v177\n"
        "v_mfma_f32_16x16x32_bf16 v[156:159], v[172:175], v[220:223], v[156:159]\n"
        "ds_read_b64_tr_b16 v[172:173], v228 offset:58368\n"
        "v_accvgpr_write_b32 a170, v178\n"
        "v_mfma_f32_16x16x32_bf16 v[152:155], v[164:167], v[220:223], v[152:155]\n"
        "ds_read_b128 v[220:223], v249 offset:4096\n"
        "v_accvgpr_write_b32 a171, v179\n"
        "v_mfma_f32_16x16x32_bf16 v[152:155], v[160:163], v[204:207], v[152:155]\n"
        "ds_read_b128 v[204:207], v248 offset:4096\n"
        "s_waitcnt lgkmcnt(6)\n"
        "v_mfma_f32_16x16x32_bf16 v[0:3], v[32:35], v[192:195], v[0:3]\n"
        "ds_read_b64_tr_b16 v[160:161], v226 offset:50176\n"
        "global_load_lds_dwordx4 v244, s[64:65] offset:2048\n"
        "v_mfma_f32_16x16x32_bf16 v[0:3], v[36:39], v[208:211], v[0:3]\n"
        "ds_read_b64_tr_b16 v[162:163], v227 offset:50176\n"
        "v_accvgpr_read_b32 v177, a97\n"
        "v_mfma_f32_16x16x32_bf16 v[4:7], v[44:47], v[208:211], v[4:7]\n"
        "ds_read_b64_tr_b16 v[164:165], v226 offset:58368\n"
        "v_accvgpr_read_b32 v178, a98\n"
        "v_mfma_f32_16x16x32_bf16 v[4:7], v[40:43], v[192:195], v[4:7]\n"
        "ds_read_b64_tr_b16 v[166:167], v227 offset:58368\n"
        "v_accvgpr_read_b32 v179, a99\n"
        "v_mfma_f32_16x16x32_bf16 v[68:71], v[40:43], v[196:199], v[68:71]\n"
        "ds_read_b64_tr_b16 v[174:175], v229 offset:58368\n"
        "global_load_lds_dwordx4 v245, s[64:65] offset:3072\n"
        "s_add_u32 m0, s72, 0x1000\n"
        "v_mfma_f32_16x16x32_bf16 v[68:71], v[44:47], v[212:215], v[68:71]\n"
        "ds_read_b64_tr_b16 v[170:171], v229 offset:50176\n"
        "s_add_u32 s64, s64, 0x80\n"
        "s_addc_u32 s65, s65, 0\n"
        "v_mfma_f32_16x16x32_bf16 v[64:67], v[36:39], v[212:215], v[64:67]\n"
        "s_waitcnt lgkmcnt(7)\n"
        "v_accvgpr_read_b32 v180, a100\n"
        "v_accvgpr_read_b32 v181, a101\n"
        "v_mfma_f32_16x16x32_bf16 v[64:67], v[32:35], v[196:199], v[64:67]\n"
        "s_waitcnt lgkmcnt(6)\n"
        "v_accvgpr_read_b32 v182, a102\n"
        "v_accvgpr_read_b32 v183, a103\n"
        "v_mfma_f32_16x16x32_bf16 v[96:99], v[32:35], v[200:203], v[96:99]\n"
        "global_load_lds_dwordx4 v242, s[66:67] offset:0\n"
        "v_mfma_f32_16x16x32_bf16 v[96:99], v[36:39], v[216:219], v[96:99]\n"
        "v_mul_f32 v177, s88, v177\n"
        "v_mul_f32 v178, s88, v178\n"
        "v_mfma_f32_16x16x32_bf16 v[100:103], v[44:47], v[216:219], v[100:103]\n"
        "v_mul_f32 v179, s88, v179\n"
        "v_mul_f32 v180, s88, v180\n"
        "v_mfma_f32_16x16x32_bf16 v[100:103], v[40:43], v[200:203], v[100:103]\n"
        "v_mul_f32 v181, s88, v181\n"
        "v_mul_f32 v182, s88, v182\n"
        "v_mfma_f32_16x16x32_bf16 v[132:135], v[40:43], v[204:207], v[132:135]\n"
        "ds_read_b64_tr_b16 v[40:41], v232 offset:50176\n"
        "global_load_lds_dwordx4 v243, s[66:67] offset:1024\n"
        "v_mfma_f32_16x16x32_bf16 v[132:135], v[44:47], v[220:223], v[132:135]\n"
        "ds_read_b64_tr_b16 v[44:45], v232 offset:58368\n"
        "v_mul_f32 v183, s88, v183\n"
        "v_mfma_f32_16x16x32_bf16 v[128:131], v[36:39], v[220:223], v[128:131]\n"
        "ds_read_b64_tr_b16 v[36:37], v230 offset:58368\n"
        "v_mfma_f32_16x16x32_bf16 v[128:131], v[32:35], v[204:207], v[128:131]\n"
        "ds_read_b64_tr_b16 v[32:33], v230 offset:50176\n"
        "v_accvgpr_read_b32 v184, a72\n"
        "v_mfma_f32_16x16x32_bf16 v[8:11], v[48:51], v[192:195], v[8:11]\n"
        "ds_read_b64_tr_b16 v[34:35], v231 offset:50176\n"
        "global_load_lds_dwordx4 v244, s[66:67] offset:2048\n"
        "v_mfma_f32_16x16x32_bf16 v[8:11], v[52:55], v[208:211], v[8:11]\n"
        "ds_read_b64_tr_b16 v[38:39], v231 offset:58368\n"
        "v_mul_f32 v184, s88, v184\n"
        "v_mfma_f32_16x16x32_bf16 v[12:15], v[60:63], v[208:211], v[12:15]\n"
        "ds_read_b64_tr_b16 v[46:47], v233 offset:58368\n"
        "v_cvt_pk_bf16_f32 v184, v184, v185\n"
        "v_mfma_f32_16x16x32_bf16 v[12:15], v[56:59], v[192:195], v[12:15]\n"
        "ds_read_b64_tr_b16 v[42:43], v233 offset:50176\n"
        "v_cvt_pk_bf16_f32 v185, v186, v187\n"
        "v_mfma_f32_16x16x32_bf16 v[76:79], v[56:59], v[196:199], v[76:79]\n"
        "global_load_lds_dwordx4 v245, s[66:67] offset:3072\n"
        "s_add_u32 m0, s73, 0x24000\n"
        "v_mfma_f32_16x16x32_bf16 v[76:79], v[60:63], v[212:215], v[76:79]\n"
        "s_waitcnt lgkmcnt(6)\n"
        "v_cvt_pk_bf16_f32 v186, v188, v189\n"
        "v_cvt_pk_bf16_f32 v187, v190, v191\n"
        "v_mfma_f32_16x16x32_bf16 v[72:75], v[52:55], v[212:215], v[72:75]\n"
        "s_add_u32 s66, s66, 0x80\n"
        "s_addc_u32 s67, s67, 0\n"
        "v_accvgpr_write_b32 a172, v184\n"
        "v_mfma_f32_16x16x32_bf16 v[72:75], v[48:51], v[196:199], v[72:75]\n"
        "v_accvgpr_write_b32 a173, v185\n"
        "v_accvgpr_write_b32 a174, v186\n"
        "v_mfma_f32_16x16x32_bf16 v[104:107], v[48:51], v[200:203], v[104:107]\n"
        "v_accvgpr_write_b32 a175, v187\n"
        "v_accvgpr_read_b32 v185, a105\n"
        "v_mfma_f32_16x16x32_bf16 v[104:107], v[52:55], v[216:219], v[104:107]\n"
        "global_store_dwordx4 v254, a[236:239], s[70:71] offset:1024 sc1\n"
        "s_add_u32 s70, s38, s97\n"
        "s_addc_u32 s71, s39, 0\n"
        "s_add_u32 s97, s52, 0x0\n"
        "v_accvgpr_read_b32 v186, a106\n"
        "v_accvgpr_read_b32 v187, a107\n"
        "v_mfma_f32_16x16x32_bf16 v[108:111], v[60:63], v[216:219], v[108:111]\n"
        "s_waitcnt vmcnt(13) lgkmcnt(0)\n"
        "v_accvgpr_read_b32 v188, a108\n"
        "v_mfma_f32_16x16x32_bf16 v[108:111], v[56:59], v[200:203], v[108:111]\n"
        "s_barrier\n"
        "v_mfma_f32_16x16x32_bf16 v[140:143], v[56:59], v[204:207], v[140:143]\n"
        "ds_read_b64_tr_b16 v[56:57], v228 offset:16384\n"
        "global_load_lds_dwordx4 v238, s[68:69] offset:0\n"
        "v_mfma_f32_16x16x32_bf16 v[140:143], v[60:63], v[220:223], v[140:143]\n"
        "ds_read_b64_tr_b16 v[60:61], v228 offset:24576\n"
        "v_accvgpr_read_b32 v189, a109\n"
        "v_mfma_f32_16x16x32_bf16 v[136:139], v[52:55], v[220:223], v[136:139]\n"
        "ds_read_b64_tr_b16 v[52:53], v226 offset:24576\n"
        "v_accvgpr_read_b32 v190, a110\n"
        "v_mfma_f32_16x16x32_bf16 v[136:139], v[48:51], v[204:207], v[136:139]\n"
        "ds_read_b64_tr_b16 v[48:49], v226 offset:16384\n"
        "v_accvgpr_read_b32 v191, a111\n"
        "v_mfma_f32_16x16x32_bf16 v[16:19], v[160:163], v[192:195], v[16:19]\n"
        "ds_read_b64_tr_b16 v[50:51], v227 offset:16384\n"
        "global_load_lds_dwordx4 v239, s[68:69] offset:1024\n"
        "v_mfma_f32_16x16x32_bf16 v[16:19], v[164:167], v[208:211], v[16:19]\n"
        "ds_read_b64_tr_b16 v[54:55], v227 offset:24576\n"
        "v_mul_f32 v185, s88, v185\n"
        "v_mfma_f32_16x16x32_bf16 v[20:23], v[172:175], v[208:211], v[20:23]\n"
        "ds_read_b64_tr_b16 v[62:63], v229 offset:24576\n"
        "v_mul_f32 v186, s88, v186\n"
        "v_mfma_f32_16x16x32_bf16 v[20:23], v[168:171], v[192:195], v[20:23]\n"
        "ds_read_b64_tr_b16 v[58:59], v229 offset:16384\n"
        "v_mul_f32 v187, s88, v187\n"
        "v_mfma_f32_16x16x32_bf16 v[84:87], v[168:171], v[196:199], v[84:87]\n"
        "global_load_lds_dwordx4 v240, s[68:69] offset:2048\n"
        "v_mfma_f32_16x16x32_bf16 v[84:87], v[172:175], v[212:215], v[84:87]\n"
        "v_mul_f32 v188, s88, v188\n"
        "v_mul_f32 v189, s88, v189\n"
        "v_mfma_f32_16x16x32_bf16 v[80:83], v[164:167], v[212:215], v[80:83]\n"
        "v_mul_f32 v190, s88, v190\n"
        "v_mul_f32 v191, s88, v191\n"
        "v_mfma_f32_16x16x32_bf16 v[80:83], v[160:163], v[196:199], v[80:83]\n"
        "v_accvgpr_read_b32 v176, a96\n"
        "v_mul_f32 v176, s88, v176\n"
        "v_mfma_f32_16x16x32_bf16 v[112:115], v[160:163], v[200:203], v[112:115]\n"
        "v_cvt_pk_bf16_f32 v176, v176, v177\n"
        "v_cvt_pk_bf16_f32 v177, v178, v179\n"
        "v_mfma_f32_16x16x32_bf16 v[112:115], v[164:167], v[216:219], v[112:115]\n"
        "global_load_lds_dwordx4 v241, s[68:69] offset:3072\n"
        "s_add_u32 m0, s72, 0x10000\n"
        "v_cvt_pk_bf16_f32 v178, v180, v181\n"
        "v_mfma_f32_16x16x32_bf16 v[116:119], v[172:175], v[216:219], v[116:119]\n"
        "s_add_u32 s68, s68, 0x80000\n"
        "s_addc_u32 s69, s69, 0\n"
        "v_cvt_pk_bf16_f32 v179, v182, v183\n"
        "v_mfma_f32_16x16x32_bf16 v[116:119], v[168:171], v[200:203], v[116:119]\n"
        "v_accvgpr_write_b32 a0, v176\n"
        "v_accvgpr_write_b32 a1, v177\n"
        "v_mfma_f32_16x16x32_bf16 v[148:151], v[168:171], v[204:207], v[148:151]\n"
        "ds_read_b64_tr_b16 v[168:169], v232 offset:16384\n"
        "v_accvgpr_write_b32 a2, v178\n"
        "v_mfma_f32_16x16x32_bf16 v[148:151], v[172:175], v[220:223], v[148:151]\n"
        "ds_read_b64_tr_b16 v[172:173], v232 offset:24576\n"
        "v_accvgpr_write_b32 a3, v179\n"
        "v_mfma_f32_16x16x32_bf16 v[144:147], v[164:167], v[220:223], v[144:147]\n"
        "ds_read_b64_tr_b16 v[164:165], v230 offset:24576\n"
        "global_load_lds_dwordx4 v242, s[64:65] offset:0\n"
        "v_mfma_f32_16x16x32_bf16 v[144:147], v[160:163], v[204:207], v[144:147]\n"
        "ds_read_b64_tr_b16 v[160:161], v230 offset:16384\n"
        "v_accvgpr_read_b32 v177, a129\n"
        "v_mfma_f32_16x16x32_bf16 v[24:27], v[32:35], v[192:195], v[24:27]\n"
        "ds_read_b64_tr_b16 v[162:163], v231 offset:16384\n"
        "v_accvgpr_read_b32 v178, a130\n"
        "v_mfma_f32_16x16x32_bf16 v[24:27], v[36:39], v[208:211], v[24:27]\n"
        "ds_read_b64_tr_b16 v[166:167], v231 offset:24576\n"
        "v_accvgpr_read_b32 v179, a131\n"
        "v_mfma_f32_16x16x32_bf16 v[28:31], v[44:47], v[208:211], v[28:31]\n"
        "ds_read_b64_tr_b16 v[174:175], v233 offset:24576\n"
        "global_load_lds_dwordx4 v243, s[64:65] offset:1024\n"
        "v_mfma_f32_16x16x32_bf16 v[28:31], v[40:43], v[192:195], v[28:31]\n"
        "ds_read_b64_tr_b16 v[170:171], v233 offset:16384\n"
        "v_accvgpr_read_b32 v180, a132\n"
        "v_mfma_f32_16x16x32_bf16 v[92:95], v[40:43], v[196:199], v[92:95]\n"
        "ds_read_b128 v[192:195], v246 offset:34816\n"
        "v_accvgpr_read_b32 v181, a133\n"
        "v_mfma_f32_16x16x32_bf16 v[92:95], v[44:47], v[212:215], v[92:95]\n"
        "ds_read_b128 v[208:211], v247 offset:34816\n"
        "v_accvgpr_read_b32 v182, a134\n"
        "v_mfma_f32_16x16x32_bf16 v[88:91], v[36:39], v[212:215], v[88:91]\n"
        "ds_read_b128 v[212:215], v247 offset:32768\n"
        "global_load_lds_dwordx4 v244, s[64:65] offset:2048\n"
        "v_mfma_f32_16x16x32_bf16 v[88:91], v[32:35], v[196:199], v[88:91]\n"
        "ds_read_b128 v[196:199], v246 offset:32768\n"
        "v_accvgpr_read_b32 v183, a135\n"
        "v_mfma_f32_16x16x32_bf16 v[120:123], v[32:35], v[200:203], v[120:123]\n"
        "v_mul_f32 v177, s88, v177\n"
        "v_mul_f32 v178, s88, v178\n"
        "v_mfma_f32_16x16x32_bf16 v[120:123], v[36:39], v[216:219], v[120:123]\n"
        "v_mul_f32 v179, s88, v179\n"
        "v_mul_f32 v180, s88, v180\n"
        "v_mfma_f32_16x16x32_bf16 v[124:127], v[44:47], v[216:219], v[124:127]\n"
        "ds_read_b128 v[216:219], v247 offset:38912\n"
        "global_load_lds_dwordx4 v245, s[64:65] offset:3072\n"
        "s_add_u32 m0, s72, 0x11000\n"
        "v_mfma_f32_16x16x32_bf16 v[124:127], v[40:43], v[200:203], v[124:127]\n"
        "ds_read_b128 v[200:203], v246 offset:38912\n"
        "s_add_u32 s64, s64, 0x80\n"
        "s_addc_u32 s65, s65, 0\n"
        "v_mfma_f32_16x16x32_bf16 v[156:159], v[40:43], v[204:207], v[156:159]\n"
        "ds_read_b64_tr_b16 v[40:41], v228 offset:17408\n"
        "v_mul_f32 v181, s88, v181\n"
        "v_mfma_f32_16x16x32_bf16 v[156:159], v[44:47], v[220:223], v[156:159]\n"
        "ds_read_b64_tr_b16 v[44:45], v228 offset:25600\n"
        "v_mul_f32 v182, s88, v182\n"
        "v_mfma_f32_16x16x32_bf16 v[152:155], v[36:39], v[220:223], v[152:155]\n"
        "ds_read_b128 v[220:223], v247 offset:36864\n"
        "global_load_lds_dwordx4 v242, s[66:67] offset:0\n"
        "v_mfma_f32_16x16x32_bf16 v[152:155], v[32:35], v[204:207], v[152:155]\n"
        "ds_read_b128 v[204:207], v246 offset:36864\n"
        "s_waitcnt lgkmcnt(6)\n"
        "v_mfma_f32_16x16x32_bf16 v[0:3], v[48:51], v[192:195], v[0:3]\n"
        "ds_read_b64_tr_b16 v[32:33], v226 offset:17408\n"
        "v_accvgpr_read_b32 v184, a104\n"
        "v_mfma_f32_16x16x32_bf16 v[0:3], v[52:55], v[208:211], v[0:3]\n"
        "ds_read_b64_tr_b16 v[34:35], v227 offset:17408\n"
        "v_mul_f32 v184, s88, v184\n"
        "v_mfma_f32_16x16x32_bf16 v[4:7], v[60:63], v[208:211], v[4:7]\n"
        "ds_read_b64_tr_b16 v[36:37], v226 offset:25600\n"
        "v_cvt_pk_bf16_f32 v184, v184, v185\n"
        "v_mfma_f32_16x16x32_bf16 v[4:7], v[56:59], v[192:195], v[4:7]\n"
        "ds_read_b64_tr_b16 v[38:39], v227 offset:25600\n"
        "v_cvt_pk_bf16_f32 v185, v186, v187\n"
        "v_mfma_f32_16x16x32_bf16 v[68:71], v[56:59], v[196:199], v[68:71]\n"
        "ds_read_b64_tr_b16 v[46:47], v229 offset:25600\n"
        "v_cvt_pk_bf16_f32 v186, v188, v189\n"
        "v_mfma_f32_16x16x32_bf16 v[68:71], v[60:63], v[212:215], v[68:71]\n"
        "ds_read_b64_tr_b16 v[42:43], v229 offset:17408\n"
        "v_cvt_pk_bf16_f32 v187, v190, v191\n"
        "v_mfma_f32_16x16x32_bf16 v[64:67], v[52:55], v[212:215], v[64:67]\n"
        "s_waitcnt lgkmcnt(7)\n"
        "v_accvgpr_write_b32 a4, v184\n"
        "v_accvgpr_write_b32 a5, v185\n"
        "v_mfma_f32_16x16x32_bf16 v[64:67], v[48:51], v[196:199], v[64:67]\n"
        "s_waitcnt lgkmcnt(6)\n"
        "v_accvgpr_write_b32 a6, v186\n"
        "v_accvgpr_write_b32 a7, v187\n"
        "v_mfma_f32_16x16x32_bf16 v[96:99], v[48:51], v[200:203], v[96:99]\n"
        "global_store_dwordx4 v254, a[192:195], s[70:71] offset:0 sc1\n"
        "v_mul_f32 v183, s88, v183\n"
        "v_accvgpr_read_b32 v185, a137\n"
        "v_accvgpr_read_b32 v186, a138\n"
        "v_accvgpr_read_b32 v187, a139\n"
        "v_mfma_f32_16x16x32_bf16 v[96:99], v[52:55], v[216:219], v[96:99]\n"
        "v_accvgpr_read_b32 v188, a140\n"
        "v_accvgpr_read_b32 v189, a141\n"
        "v_mfma_f32_16x16x32_bf16 v[100:103], v[60:63], v[216:219], v[100:103]\n"
        "v_accvgpr_read_b32 v190, a142\n"
        "v_accvgpr_read_b32 v191, a143\n"
        "v_mfma_f32_16x16x32_bf16 v[100:103], v[56:59], v[200:203], v[100:103]\n"
        "global_load_lds_dwordx4 v243, s[66:67] offset:1024\n"
        "v_mfma_f32_16x16x32_bf16 v[132:135], v[56:59], v[204:207], v[132:135]\n"
        "ds_read_b64_tr_b16 v[56:57], v232 offset:17408\n"
        "v_mul_f32 v185, s88, v185\n"
        "v_mfma_f32_16x16x32_bf16 v[132:135], v[60:63], v[220:223], v[132:135]\n"
        "ds_read_b64_tr_b16 v[60:61], v232 offset:25600\n"
        "v_mul_f32 v186, s88, v186\n"
        "v_mfma_f32_16x16x32_bf16 v[128:131], v[52:55], v[220:223], v[128:131]\n"
        "ds_read_b64_tr_b16 v[52:53], v230 offset:25600\n"
        "v_mul_f32 v187, s88, v187\n"
        "v_mfma_f32_16x16x32_bf16 v[128:131], v[48:51], v[204:207], v[128:131]\n"
        "ds_read_b64_tr_b16 v[48:49], v230 offset:17408\n"
        "global_load_lds_dwordx4 v244, s[66:67] offset:2048\n"
        "v_mfma_f32_16x16x32_bf16 v[8:11], v[160:163], v[192:195], v[8:11]\n"
        "ds_read_b64_tr_b16 v[50:51], v231 offset:17408\n"
        "v_mul_f32 v188, s88, v188\n"
        "v_mfma_f32_16x16x32_bf16 v[8:11], v[164:167], v[208:211], v[8:11]\n"
        "ds_read_b64_tr_b16 v[54:55], v231 offset:25600\n"
        "v_mul_f32 v189, s88, v189\n"
        "v_mfma_f32_16x16x32_bf16 v[12:15], v[172:175], v[208:211], v[12:15]\n"
        "ds_read_b64_tr_b16 v[62:63], v233 offset:25600\n"
        "v_mul_f32 v190, s88, v190\n"
        "v_mfma_f32_16x16x32_bf16 v[12:15], v[168:171], v[192:195], v[12:15]\n"
        "ds_read_b64_tr_b16 v[58:59], v233 offset:17408\n"
        "global_load_lds_dwordx4 v245, s[66:67] offset:3072\n"
        "s_add_u32 m0, s73, 0x1c000\n"
        "v_mfma_f32_16x16x32_bf16 v[76:79], v[168:171], v[196:199], v[76:79]\n"
        "s_add_u32 s66, s66, 0x80\n"
        "s_addc_u32 s67, s67, 0\n"
        "v_mul_f32 v191, s88, v191\n"
        "v_mfma_f32_16x16x32_bf16 v[76:79], v[172:175], v[212:215], v[76:79]\n"
        "s_waitcnt lgkmcnt(6)\n"
        "v_mfma_f32_16x16x32_bf16 v[72:75], v[164:167], v[212:215], v[72:75]\n"
        "v_mfma_f32_16x16x32_bf16 v[72:75], v[160:163], v[196:199], v[72:75]\n"
        "v_accvgpr_read_b32 v176, a128\n"
        "v_mul_f32 v176, s88, v176\n"
        "v_mfma_f32_16x16x32_bf16 v[104:107], v[160:163], v[200:203], v[104:107]\n"
        "v_cvt_pk_bf16_f32 v176, v176, v177\n"
        "v_cvt_pk_bf16_f32 v177, v178, v179\n"
        "v_mfma_f32_16x16x32_bf16 v[104:107], v[164:167], v[216:219], v[104:107]\n"
        "v_cvt_pk_bf16_f32 v178, v180, v181\n"
        "v_cvt_pk_bf16_f32 v179, v182, v183\n"
        "v_mfma_f32_16x16x32_bf16 v[108:111], v[172:175], v[216:219], v[108:111]\n"
        "v_accvgpr_write_b32 a8, v176\n"
        "v_accvgpr_write_b32 a9, v177\n"
        "v_mfma_f32_16x16x32_bf16 v[108:111], v[168:171], v[200:203], v[108:111]\n"
        "s_waitcnt vmcnt(14) lgkmcnt(0)\n"
        "v_accvgpr_write_b32 a10, v178\n"
        "v_accvgpr_write_b32 a11, v179\n"
        "v_mfma_f32_16x16x32_bf16 v[140:143], v[168:171], v[204:207], v[140:143]\n"
        "s_barrier\n"
        "v_mfma_f32_16x16x32_bf16 v[140:143], v[172:175], v[220:223], v[140:143]\n"
        "ds_read_b64_tr_b16 v[172:173], v228 offset:40960\n"
        "global_load_lds_dwordx4 v238, s[68:69] offset:0\n"
        "v_mfma_f32_16x16x32_bf16 v[136:139], v[164:167], v[220:223], v[136:139]\n"
        "ds_read_b64_tr_b16 v[164:165], v226 offset:40960\n"
        "v_accvgpr_read_b32 v177, a209\n"
        "v_mfma_f32_16x16x32_bf16 v[136:139], v[160:163], v[204:207], v[136:139]\n"
        "ds_read_b64_tr_b16 v[160:161], v226 offset:32768\n"
        "v_accvgpr_read_b32 v178, a210\n"
        "v_mfma_f32_16x16x32_bf16 v[16:19], v[32:35], v[192:195], v[16:19]\n"
        "ds_read_b64_tr_b16 v[162:163], v227 offset:32768\n"
        "v_accvgpr_read_b32 v179, a211\n"
        "v_mfma_f32_16x16x32_bf16 v[16:19], v[36:39], v[208:211], v[16:19]\n"
        "ds_read_b64_tr_b16 v[166:167], v227 offset:40960\n"
        "global_load_lds_dwordx4 v239, s[68:69] offset:1024\n"
        "v_mfma_f32_16x16x32_bf16 v[20:23], v[44:47], v[208:211], v[20:23]\n"
        "ds_read_b64_tr_b16 v[174:175], v229 offset:40960\n"
        "v_accvgpr_read_b32 v180, a240\n"
        "v_mfma_f32_16x16x32_bf16 v[20:23], v[40:43], v[192:195], v[20:23]\n"
        "ds_read_b64_tr_b16 v[168:169], v228 offset:32768\n"
        "v_accvgpr_read_b32 v181, a241\n"
        "v_mfma_f32_16x16x32_bf16 v[84:87], v[40:43], v[196:199], v[84:87]\n"
        "ds_read_b64_tr_b16 v[170:171], v229 offset:32768\n"
        "v_accvgpr_read_b32 v182, a242\n"
        "v_mfma_f32_16x16x32_bf16 v[84:87], v[44:47], v[212:215], v[84:87]\n"
        "global_load_lds_dwordx4 v240, s[68:69] offset:2048\n"
        "v_mfma_f32_16x16x32_bf16 v[80:83], v[36:39], v[212:215], v[80:83]\n"
        "v_accvgpr_read_b32 v183, a243\n"
        "v_mfma_f32_16x16x32_bf16 v[80:83], v[32:35], v[196:199], v[80:83]\n"
        "v_mfma_f32_16x16x32_bf16 v[112:115], v[32:35], v[200:203], v[112:115]\n"
        "v_mfma_f32_16x16x32_bf16 v[112:115], v[36:39], v[216:219], v[112:115]\n"
        "global_load_lds_dwordx4 v241, s[68:69] offset:3072\n"
        "s_add_u32 m0, s72, 0x8000\n"
        "v_mfma_f32_16x16x32_bf16 v[116:119], v[44:47], v[216:219], v[116:119]\n"
        "s_add_u32 s68, s68, 0x80000\n"
        "s_addc_u32 s69, s69, 0\n"
        "v_mfma_f32_16x16x32_bf16 v[116:119], v[40:43], v[200:203], v[116:119]\n"
        "v_mfma_f32_16x16x32_bf16 v[148:151], v[40:43], v[204:207], v[148:151]\n"
        "ds_read_b64_tr_b16 v[40:41], v232 offset:32768\n"
        "v_mfma_f32_16x16x32_bf16 v[148:151], v[44:47], v[220:223], v[148:151]\n"
        "ds_read_b64_tr_b16 v[44:45], v232 offset:40960\n"
        "global_load_lds_dwordx4 v242, s[64:65] offset:0\n"
        "v_mfma_f32_16x16x32_bf16 v[144:147], v[36:39], v[220:223], v[144:147]\n"
        "ds_read_b64_tr_b16 v[36:37], v230 offset:40960\n"
        "v_mfma_f32_16x16x32_bf16 v[144:147], v[32:35], v[204:207], v[144:147]\n"
        "ds_read_b64_tr_b16 v[32:33], v230 offset:32768\n"
        "v_accvgpr_read_b32 v184, a136\n"
        "v_mfma_f32_16x16x32_bf16 v[24:27], v[48:51], v[192:195], v[24:27]\n"
        "ds_read_b64_tr_b16 v[34:35], v231 offset:32768\n"
        "v_mul_f32 v184, s88, v184\n"
        "v_mfma_f32_16x16x32_bf16 v[24:27], v[52:55], v[208:211], v[24:27]\n"
        "ds_read_b64_tr_b16 v[38:39], v231 offset:40960\n"
        "v_cvt_pk_bf16_f32 v184, v184, v185\n"
        "v_mfma_f32_16x16x32_bf16 v[28:31], v[60:63], v[208:211], v[28:31]\n"
        "ds_read_b64_tr_b16 v[46:47], v233 offset:40960\n"
        "v_cvt_pk_bf16_f32 v185, v186, v187\n"
        "v_mfma_f32_16x16x32_bf16 v[28:31], v[56:59], v[192:195], v[28:31]\n"
        "ds_read_b64_tr_b16 v[42:43], v233 offset:32768\n"
        "v_cvt_pk_bf16_f32 v186, v188, v189\n"
        "v_mfma_f32_16x16x32_bf16 v[92:95], v[56:59], v[196:199], v[92:95]\n"
        "ds_read_b128 v[192:195], v246 offset:2048\n"
        "v_cvt_pk_bf16_f32 v187, v190, v191\n"
        "v_mfma_f32_16x16x32_bf16 v[92:95], v[60:63], v[212:215], v[92:95]\n"
        "ds_read_b128 v[208:211], v247 offset:2048\n"
        "v_accvgpr_write_b32 a12, v184\n"
        "v_mfma_f32_16x16x32_bf16 v[88:91], v[52:55], v[212:215], v[88:91]\n"
        "ds_read_b128 v[212:215], v247 offset:0\n"
        "v_accvgpr_write_b32 a13, v185\n"
        "v_mfma_f32_16x16x32_bf16 v[88:91], v[48:51], v[196:199], v[88:91]\n"
        "ds_read_b128 v[196:199], v246 offset:0\n"
        "v_accvgpr_write_b32 a14, v186\n"
        "v_mfma_f32_16x16x32_bf16 v[120:123], v[48:51], v[200:203], v[120:123]\n"
        "global_load_lds_dwordx4 v243, s[64:65] offset:1024\n"
        "v_accvgpr_write_b32 a15, v187\n"
        "v_mfma_f32_16x16x32_bf16 v[120:123], v[52:55], v[216:219], v[120:123]\n"
        "v_accvgpr_read_b32 v185, a213\n"
        "v_accvgpr_read_b32 v186, a214\n"
        "v_mfma_f32_16x16x32_bf16 v[124:127], v[60:63], v[216:219], v[124:127]\n"
        "ds_read_b128 v[216:219], v247 offset:6144\n"
        "v_accvgpr_read_b32 v187, a215\n"
        "v_mfma_f32_16x16x32_bf16 v[124:127], v[56:59], v[200:203], v[124:127]\n"
        "ds_read_b128 v[200:203], v246 offset:6144\n"
        "v_accvgpr_read_b32 v188, a244\n"
        "v_mfma_f32_16x16x32_bf16 v[156:159], v[56:59], v[204:207], v[156:159]\n"
        "ds_read_b64_tr_b16 v[56:57], v228 offset:33792\n"
        "global_load_lds_dwordx4 v244, s[64:65] offset:2048\n"
        "v_mfma_f32_16x16x32_bf16 v[156:159], v[60:63], v[220:223], v[156:159]\n"
        "ds_read_b64_tr_b16 v[60:61], v228 offset:41984\n"
        "v_accvgpr_read_b32 v189, a245\n"
        "v_mfma_f32_16x16x32_bf16 v[152:155], v[52:55], v[220:223], v[152:155]\n"
        "ds_read_b128 v[220:223], v247 offset:4096\n"
        "v_accvgpr_read_b32 v190, a246\n"
        "v_mfma_f32_16x16x32_bf16 v[152:155], v[48:51], v[204:207], v[152:155]\n"
        "ds_read_b128 v[204:207], v246 offset:4096\n"
        "s_waitcnt lgkmcnt(6)\n"
        "v_mfma_f32_16x16x32_bf16 v[0:3], v[160:163], v[192:195], v[0:3]\n"
        "ds_read_b64_tr_b16 v[48:49], v226 offset:33792\n"
        "global_load_lds_dwordx4 v245, s[64:65] offset:3072\n"
        "s_add_u32 m0, s72, 0x9000\n"
        "v_mfma_f32_16x16x32_bf16 v[0:3], v[164:167], v[208:211], v[0:3]\n"
        "ds_read_b64_tr_b16 v[50:51], v227 offset:33792\n"
        "s_add_u32 s64, s64, 0x80\n"
        "s_addc_u32 s65, s65, 0\n"
        "v_mfma_f32_16x16x32_bf16 v[4:7], v[172:175], v[208:211], v[4:7]\n"
        "ds_read_b64_tr_b16 v[52:53], v226 offset:41984\n"
        "v_accvgpr_read_b32 v191, a247\n"
        "v_mfma_f32_16x16x32_bf16 v[4:7], v[168:171], v[192:195], v[4:7]\n"
        "ds_read_b64_tr_b16 v[54:55], v227 offset:41984\n"
        "s_waitcnt lgkmcnt(8)\n"
        "v_mfma_f32_16x16x32_bf16 v[68:71], v[168:171], v[196:199], v[68:71]\n"
        "ds_read_b64_tr_b16 v[62:63], v229 offset:41984\n"
        "global_load_lds_dwordx4 v242, s[66:67] offset:0\n"
        "v_mfma_f32_16x16x32_bf16 v[68:71], v[172:175], v[212:215], v[68:71]\n"
        "ds_read_b64_tr_b16 v[58:59], v229 offset:33792\n"
        "s_waitcnt lgkmcnt(8)\n"
        "v_mfma_f32_16x16x32_bf16 v[64:67], v[164:167], v[212:215], v[64:67]\n"
        "s_waitcnt lgkmcnt(7)\n"
        "v_mfma_f32_16x16x32_bf16 v[64:67], v[160:163], v[196:199], v[64:67]\n"
        "s_waitcnt lgkmcnt(6)\n"
        "v_accvgpr_read_b32 v176, a208\n"
        "v_cvt_pk_bf16_f32 v176, v176, v177\n"
        "v_mfma_f32_16x16x32_bf16 v[96:99], v[160:163], v[200:203], v[96:99]\n"
        "global_load_lds_dwordx4 v243, s[66:67] offset:1024\n"
        "v_mfma_f32_16x16x32_bf16 v[96:99], v[164:167], v[216:219], v[96:99]\n"
        "v_cvt_pk_bf16_f32 v177, v178, v179\n"
        "v_cvt_pk_bf16_f32 v178, v180, v181\n"
        "v_mfma_f32_16x16x32_bf16 v[100:103], v[172:175], v[216:219], v[100:103]\n"
        "v_cvt_pk_bf16_f32 v179, v182, v183\n"
        "v_accvgpr_write_b32 a208, v176\n"
        "v_mfma_f32_16x16x32_bf16 v[100:103], v[168:171], v[200:203], v[100:103]\n"
        "v_accvgpr_write_b32 a209, v177\n"
        "v_accvgpr_write_b32 a210, v178\n"
        "v_mfma_f32_16x16x32_bf16 v[132:135], v[168:171], v[204:207], v[132:135]\n"
        "ds_read_b64_tr_b16 v[168:169], v232 offset:33792\n"
        "v_accvgpr_write_b32 a211, v179\n"
        "v_mfma_f32_16x16x32_bf16 v[132:135], v[172:175], v[220:223], v[132:135]\n"
        "ds_read_b64_tr_b16 v[172:173], v232 offset:41984\n"
        "global_load_lds_dwordx4 v244, s[66:67] offset:2048\n"
        "v_mfma_f32_16x16x32_bf16 v[128:131], v[164:167], v[220:223], v[128:131]\n"
        "ds_read_b64_tr_b16 v[164:165], v230 offset:41984\n"
        "v_accvgpr_read_b32 v177, a217\n"
        "v_mfma_f32_16x16x32_bf16 v[128:131], v[160:163], v[204:207], v[128:131]\n"
        "ds_read_b64_tr_b16 v[160:161], v230 offset:33792\n"
        "v_accvgpr_read_b32 v178, a218\n"
        "v_mfma_f32_16x16x32_bf16 v[8:11], v[32:35], v[192:195], v[8:11]\n"
        "ds_read_b64_tr_b16 v[162:163], v231 offset:33792\n"
        "v_accvgpr_read_b32 v179, a219\n"
        "v_mfma_f32_16x16x32_bf16 v[8:11], v[36:39], v[208:211], v[8:11]\n"
        "ds_read_b64_tr_b16 v[166:167], v231 offset:41984\n"
        "global_load_lds_dwordx4 v245, s[66:67] offset:3072\n"
        "s_add_u32 m0, s73, 0x20000\n"
        "v_mfma_f32_16x16x32_bf16 v[12:15], v[44:47], v[208:211], v[12:15]\n"
        "ds_read_b64_tr_b16 v[174:175], v233 offset:41984\n"
        "s_add_u32 s66, s66, 0x80\n"
        "s_addc_u32 s67, s67, 0\n"
        "v_mfma_f32_16x16x32_bf16 v[12:15], v[40:43], v[192:195], v[12:15]\n"
        "ds_read_b64_tr_b16 v[170:171], v233 offset:33792\n"
        "v_accvgpr_read_b32 v180, a248\n"
        "v_mfma_f32_16x16x32_bf16 v[76:79], v[40:43], v[196:199], v[76:79]\n"
        "v_accvgpr_read_b32 v181, a249\n"
        "v_accvgpr_read_b32 v182, a250\n"
        "v_mfma_f32_16x16x32_bf16 v[76:79], v[44:47], v[212:215], v[76:79]\n"
        "s_waitcnt lgkmcnt(6)\n"
        "v_accvgpr_read_b32 v183, a251\n"
        "v_mfma_f32_16x16x32_bf16 v[72:75], v[36:39], v[212:215], v[72:75]\n"
        "global_store_dwordx4 v254, a[196:199], s[70:71] offset:1024 sc1\n"
        "s_add_u32 s70, s38, s97\n"
        "s_addc_u32 s71, s39, 0\n"
        "s_add_u32 s97, s52, 0x1000\n"
        "v_mfma_f32_16x16x32_bf16 v[72:75], v[32:35], v[196:199], v[72:75]\n"
        "v_mfma_f32_16x16x32_bf16 v[104:107], v[32:35], v[200:203], v[104:107]\n"
        "v_mfma_f32_16x16x32_bf16 v[104:107], v[36:39], v[216:219], v[104:107]\n"
        "v_mfma_f32_16x16x32_bf16 v[108:111], v[44:47], v[216:219], v[108:111]\n"
        "s_waitcnt vmcnt(13) lgkmcnt(0)\n"
        "v_mfma_f32_16x16x32_bf16 v[108:111], v[40:43], v[200:203], v[108:111]\n"
        "s_barrier\n"
        "v_mfma_f32_16x16x32_bf16 v[140:143], v[40:43], v[204:207], v[140:143]\n"
        "ds_read_b64_tr_b16 v[40:41], v228 offset:49152\n"
        "global_load_lds_dwordx4 v238, s[68:69] offset:0\n"
        "v_mfma_f32_16x16x32_bf16 v[140:143], v[44:47], v[220:223], v[140:143]\n"
        "ds_read_b64_tr_b16 v[44:45], v228 offset:57344\n"
        "v_mfma_f32_16x16x32_bf16 v[136:139], v[36:39], v[220:223], v[136:139]\n"
        "ds_read_b64_tr_b16 v[36:37], v226 offset:57344\n"
        "v_mfma_f32_16x16x32_bf16 v[136:139], v[32:35], v[204:207], v[136:139]\n"
        "ds_read_b64_tr_b16 v[32:33], v226 offset:49152\n"
        "v_accvgpr_read_b32 v184, a212\n"
        "v_mfma_f32_16x16x32_bf16 v[16:19], v[48:51], v[192:195], v[16:19]\n"
        "ds_read_b64_tr_b16 v[34:35], v227 offset:49152\n"
        "v_cvt_pk_bf16_f32 v184, v184, v185\n"
        "v_mfma_f32_16x16x32_bf16 v[16:19], v[52:55], v[208:211], v[16:19]\n"
        "ds_read_b64_tr_b16 v[38:39], v227 offset:57344\n"
        "v_cvt_pk_bf16_f32 v185, v186, v187\n"
        "v_mfma_f32_16x16x32_bf16 v[20:23], v[60:63], v[208:211], v[20:23]\n"
        "ds_read_b64_tr_b16 v[46:47], v229 offset:57344\n"
        "v_cvt_pk_bf16_f32 v186, v188, v189\n"
        "v_mfma_f32_16x16x32_bf16 v[20:23], v[56:59], v[192:195], v[20:23]\n"
        "ds_read_b64_tr_b16 v[42:43], v229 offset:49152\n"
        "v_cvt_pk_bf16_f32 v187, v190, v191\n"
        "v_mfma_f32_16x16x32_bf16 v[84:87], v[56:59], v[196:199], v[84:87]\n"
        "global_load_lds_dwordx4 v239, s[68:69] offset:1024\n"
        "v_accvgpr_write_b32 a240, v184\n"
        "v_mfma_f32_16x16x32_bf16 v[84:87], v[60:63], v[212:215], v[84:87]\n"
        "v_accvgpr_write_b32 a241, v185\n"
        "v_accvgpr_write_b32 a242, v186\n"
        "v_mfma_f32_16x16x32_bf16 v[80:83], v[52:55], v[212:215], v[80:83]\n"
        "v_accvgpr_write_b32 a243, v187\n"
        "v_accvgpr_read_b32 v185, a221\n"
        "v_mfma_f32_16x16x32_bf16 v[80:83], v[48:51], v[196:199], v[80:83]\n"
        "v_accvgpr_read_b32 v186, a222\n"
        "v_accvgpr_read_b32 v187, a223\n"
        "v_mfma_f32_16x16x32_bf16 v[112:115], v[48:51], v[200:203], v[112:115]\n"
        "global_load_lds_dwordx4 v240, s[68:69] offset:2048\n"
        "v_accvgpr_read_b32 v188, a252\n"
        "v_mfma_f32_16x16x32_bf16 v[112:115], v[52:55], v[216:219], v[112:115]\n"
        "LHA7_%=:\n"
        "s_cbranch_execz LHA11_%=\n"
        "v_accvgpr_read_b32 v189, a253\n"
        "v_accvgpr_read_b32 v190, a254\n"
        "v_mfma_f32_16x16x32_bf16 v[116:119], v[60:63], v[216:219], v[116:119]\n"
        "v_accvgpr_read_b32 v191, a255\n"
        "v_mfma_f32_16x16x32_bf16 v[116:119], v[56:59], v[200:203], v[116:119]\n"
        "v_mfma_f32_16x16x32_bf16 v[148:151], v[56:59], v[204:207], v[148:151]\n"
        "ds_read_b64_tr_b16 v[56:57], v232 offset:49152\n"
        "global_load_lds_dwordx4 v241, s[68:69] offset:3072\n"
        "s_add_u32 m0, s72, 0x0\n"
        "v_mfma_f32_16x16x32_bf16 v[148:151], v[60:63], v[220:223], v[148:151]\n"
        "ds_read_b64_tr_b16 v[60:61], v232 offset:57344\n"
        "s_add_u32 s68, s68, 0x80000\n"
        "s_addc_u32 s69, s69, 0\n"
        "v_mfma_f32_16x16x32_bf16 v[144:147], v[52:55], v[220:223], v[144:147]\n"
        "ds_read_b64_tr_b16 v[52:53], v230 offset:57344\n"
        "v_mfma_f32_16x16x32_bf16 v[144:147], v[48:51], v[204:207], v[144:147]\n"
        "ds_read_b64_tr_b16 v[48:49], v230 offset:49152\n"
        "v_mfma_f32_16x16x32_bf16 v[24:27], v[160:163], v[192:195], v[24:27]\n"
        "ds_read_b64_tr_b16 v[50:51], v231 offset:49152\n"
        "global_load_lds_dwordx4 v242, s[64:65] offset:0\n"
        "v_mfma_f32_16x16x32_bf16 v[24:27], v[164:167], v[208:211], v[24:27]\n"
        "ds_read_b64_tr_b16 v[54:55], v231 offset:57344\n"
        "v_mfma_f32_16x16x32_bf16 v[28:31], v[172:175], v[208:211], v[28:31]\n"
        "ds_read_b64_tr_b16 v[62:63], v233 offset:57344\n"
        "v_mfma_f32_16x16x32_bf16 v[28:31], v[168:171], v[192:195], v[28:31]\n"
        "ds_read_b64_tr_b16 v[58:59], v233 offset:49152\n"
        "v_mfma_f32_16x16x32_bf16 v[92:95], v[168:171], v[196:199], v[92:95]\n"
        "ds_read_b128 v[192:195], v248 offset:2048\n"
        "global_load_lds_dwordx4 v243, s[64:65] offset:1024\n"
        "v_mfma_f32_16x16x32_bf16 v[92:95], v[172:175], v[212:215], v[92:95]\n"
        "ds_read_b128 v[208:211], v249 offset:2048\n"
        "v_mfma_f32_16x16x32_bf16 v[88:91], v[164:167], v[212:215], v[88:91]\n"
        "ds_read_b128 v[212:215], v249 offset:0\n"
        "v_mfma_f32_16x16x32_bf16 v[88:91], v[160:163], v[196:199], v[88:91]\n"
        "ds_read_b128 v[196:199], v248 offset:0\n"
        "v_accvgpr_read_b32 v176, a216\n"
        "v_mfma_f32_16x16x32_bf16 v[120:123], v[160:163], v[200:203], v[120:123]\n"
        "v_cvt_pk_bf16_f32 v176, v176, v177\n"
        "v_cvt_pk_bf16_f32 v177, v178, v179\n"
        "v_mfma_f32_16x16x32_bf16 v[120:123], v[164:167], v[216:219], v[120:123]\n"
        "global_load_lds_dwordx4 v244, s[64:65] offset:2048\n"
        "v_cvt_pk_bf16_f32 v178, v180, v181\n"
        "v_mfma_f32_16x16x32_bf16 v[124:127], v[172:175], v[216:219], v[124:127]\n"
        "ds_read_b128 v[216:219], v249 offset:6144\n"
        "v_cvt_pk_bf16_f32 v179, v182, v183\n"
        "v_mfma_f32_16x16x32_bf16 v[124:127], v[168:171], v[200:203], v[124:127]\n"
        "ds_read_b128 v[200:203], v248 offset:6144\n"
        "v_accvgpr_write_b32 a212, v176\n"
        "v_mfma_f32_16x16x32_bf16 v[156:159], v[168:171], v[204:207], v[156:159]\n"
        "ds_read_b64_tr_b16 v[168:169], v228 offset:50176\n"
        "v_accvgpr_write_b32 a213, v177\n"
        "v_mfma_f32_16x16x32_bf16 v[156:159], v[172:175], v[220:223], v[156:159]\n"
        "ds_read_b64_tr_b16 v[172:173], v228 offset:58368\n"
        "v_accvgpr_write_b32 a214, v178\n"
        "v_mfma_f32_16x16x32_bf16 v[152:155], v[164:167], v[220:223], v[152:155]\n"
        "ds_read_b128 v[220:223], v249 offset:4096\n"
        "v_accvgpr_write_b32 a215, v179\n"
        "v_mfma_f32_16x16x32_bf16 v[152:155], v[160:163], v[204:207], v[152:155]\n"
        "ds_read_b128 v[204:207], v248 offset:4096\n"
        "s_waitcnt lgkmcnt(6)\n"
        "v_mfma_f32_16x16x32_bf16 v[0:3], v[32:35], v[192:195], v[0:3]\n"
        "ds_read_b64_tr_b16 v[160:161], v226 offset:50176\n"
        "global_load_lds_dwordx4 v245, s[64:65] offset:3072\n"
        "s_add_u32 m0, s72, 0x1000\n"
        "v_mfma_f32_16x16x32_bf16 v[0:3], v[36:39], v[208:211], v[0:3]\n"
        "ds_read_b64_tr_b16 v[162:163], v227 offset:50176\n"
        "s_add_u32 s64, s64, 0x80\n"
        "s_addc_u32 s65, s65, 0\n"
        "v_mfma_f32_16x16x32_bf16 v[4:7], v[44:47], v[208:211], v[4:7]\n"
        "ds_read_b64_tr_b16 v[164:165], v226 offset:58368\n"
        "v_accvgpr_read_b32 v177, a145\n"
        "v_mfma_f32_16x16x32_bf16 v[4:7], v[40:43], v[192:195], v[4:7]\n"
        "ds_read_b64_tr_b16 v[166:167], v227 offset:58368\n"
        "v_accvgpr_read_b32 v178, a146\n"
        "v_mfma_f32_16x16x32_bf16 v[68:71], v[40:43], v[196:199], v[68:71]\n"
        "ds_read_b64_tr_b16 v[174:175], v229 offset:58368\n"
        "global_load_lds_dwordx4 v242, s[66:67] offset:0\n"
        "v_mfma_f32_16x16x32_bf16 v[68:71], v[44:47], v[212:215], v[68:71]\n"
        "ds_read_b64_tr_b16 v[170:171], v229 offset:50176\n"
        "v_accvgpr_read_b32 v179, a147\n"
        "v_mfma_f32_16x16x32_bf16 v[64:67], v[36:39], v[212:215], v[64:67]\n"
        "s_waitcnt lgkmcnt(7)\n"
        "v_accvgpr_read_b32 v180, a176\n"
        "v_accvgpr_read_b32 v181, a177\n"
        "v_mfma_f32_16x16x32_bf16 v[64:67], v[32:35], v[196:199], v[64:67]\n"
        "s_waitcnt lgkmcnt(6)\n"
        "v_accvgpr_read_b32 v182, a178\n"
        "v_accvgpr_read_b32 v183, a179\n"
        "v_mfma_f32_16x16x32_bf16 v[96:99], v[32:35], v[200:203], v[96:99]\n"
        "global_load_lds_dwordx4 v243, s[66:67] offset:1024\n"
        "v_mfma_f32_16x16x32_bf16 v[96:99], v[36:39], v[216:219], v[96:99]\n"
        "v_mfma_f32_16x16x32_bf16 v[100:103], v[44:47], v[216:219], v[100:103]\n"
        "v_mfma_f32_16x16x32_bf16 v[100:103], v[40:43], v[200:203], v[100:103]\n"
        "v_mfma_f32_16x16x32_bf16 v[132:135], v[40:43], v[204:207], v[132:135]\n"
        "ds_read_b64_tr_b16 v[40:41], v232 offset:50176\n"
        "global_load_lds_dwordx4 v244, s[66:67] offset:2048\n"
        "v_mfma_f32_16x16x32_bf16 v[132:135], v[44:47], v[220:223], v[132:135]\n"
        "ds_read_b64_tr_b16 v[44:45], v232 offset:58368\n"
        "v_mfma_f32_16x16x32_bf16 v[128:131], v[36:39], v[220:223], v[128:131]\n"
        "ds_read_b64_tr_b16 v[36:37], v230 offset:58368\n"
        "v_mfma_f32_16x16x32_bf16 v[128:131], v[32:35], v[204:207], v[128:131]\n"
        "ds_read_b64_tr_b16 v[32:33], v230 offset:50176\n"
        "v_accvgpr_read_b32 v184, a220\n"
        "v_mfma_f32_16x16x32_bf16 v[8:11], v[48:51], v[192:195], v[8:11]\n"
        "ds_read_b64_tr_b16 v[34:35], v231 offset:50176\n"
        "global_load_lds_dwordx4 v245, s[66:67] offset:3072\n"
        "s_add_u32 m0, s73, 0x24000\n"
        "v_mfma_f32_16x16x32_bf16 v[8:11], v[52:55], v[208:211], v[8:11]\n"
        "ds_read_b64_tr_b16 v[38:39], v231 offset:58368\n"
        "v_cvt_pk_bf16_f32 v184, v184, v185\n"
        "v_mfma_f32_16x16x32_bf16 v[12:15], v[60:63], v[208:211], v[12:15]\n"
        "ds_read_b64_tr_b16 v[46:47], v233 offset:58368\n"
        "v_cvt_pk_bf16_f32 v185, v186, v187\n"
        "v_mfma_f32_16x16x32_bf16 v[12:15], v[56:59], v[192:195], v[12:15]\n"
        "ds_read_b64_tr_b16 v[42:43], v233 offset:50176\n"
        "v_cvt_pk_bf16_f32 v186, v188, v189\n"
        "v_mfma_f32_16x16x32_bf16 v[76:79], v[56:59], v[196:199], v[76:79]\n"
        "s_add_u32 s66, s66, 0x80\n"
        "s_addc_u32 s67, s67, 0\n"
        "v_cvt_pk_bf16_f32 v187, v190, v191\n"
        "v_mfma_f32_16x16x32_bf16 v[76:79], v[60:63], v[212:215], v[76:79]\n"
        "s_waitcnt lgkmcnt(6)\n"
        "v_accvgpr_write_b32 a244, v184\n"
        "v_accvgpr_write_b32 a245, v185\n"
        "v_mfma_f32_16x16x32_bf16 v[72:75], v[52:55], v[212:215], v[72:75]\n"
        "v_accvgpr_write_b32 a246, v186\n"
        "v_accvgpr_write_b32 a247, v187\n"
        "v_mfma_f32_16x16x32_bf16 v[72:75], v[48:51], v[196:199], v[72:75]\n"
        "global_store_dwordx4 v254, a[200:203], s[70:71] offset:0 sc1\n"
        "v_accvgpr_read_b32 v185, a149\n"
        "v_accvgpr_read_b32 v186, a150\n"
        "v_accvgpr_read_b32 v187, a151\n"
        "v_accvgpr_read_b32 v188, a180\n"
        "v_mfma_f32_16x16x32_bf16 v[104:107], v[48:51], v[200:203], v[104:107]\n"
        "v_accvgpr_read_b32 v189, a181\n"
        "v_accvgpr_read_b32 v190, a182\n"
        "v_mfma_f32_16x16x32_bf16 v[104:107], v[52:55], v[216:219], v[104:107]\n"
        "v_accvgpr_read_b32 v191, a183\n"
        "v_mfma_f32_16x16x32_bf16 v[108:111], v[60:63], v[216:219], v[108:111]\n"
        "s_waitcnt vmcnt(14) lgkmcnt(0)\n"
        "v_mfma_f32_16x16x32_bf16 v[108:111], v[56:59], v[200:203], v[108:111]\n"
        "s_barrier\n"
        "v_mfma_f32_16x16x32_bf16 v[140:143], v[56:59], v[204:207], v[140:143]\n"
        "ds_read_b64_tr_b16 v[56:57], v228 offset:16384\n"
        "global_load_lds_dwordx4 v238, s[68:69] offset:0\n"
        "v_mfma_f32_16x16x32_bf16 v[140:143], v[60:63], v[220:223], v[140:143]\n"
        "ds_read_b64_tr_b16 v[60:61], v228 offset:24576\n"
        "v_mfma_f32_16x16x32_bf16 v[136:139], v[52:55], v[220:223], v[136:139]\n"
        "ds_read_b64_tr_b16 v[52:53], v226 offset:24576\n"
        "v_mfma_f32_16x16x32_bf16 v[136:139], v[48:51], v[204:207], v[136:139]\n"
        "ds_read_b64_tr_b16 v[48:49], v226 offset:16384\n"
        "v_mfma_f32_16x16x32_bf16 v[16:19], v[160:163], v[192:195], v[16:19]\n"
        "ds_read_b64_tr_b16 v[50:51], v227 offset:16384\n"
        "global_load_lds_dwordx4 v239, s[68:69] offset:1024\n"
        "v_mfma_f32_16x16x32_bf16 v[16:19], v[164:167], v[208:211], v[16:19]\n"
        "ds_read_b64_tr_b16 v[54:55], v227 offset:24576\n"
        "v_mfma_f32_16x16x32_bf16 v[20:23], v[172:175], v[208:211], v[20:23]\n"
        "ds_read_b64_tr_b16 v[62:63], v229 offset:24576\n"
        "v_mfma_f32_16x16x32_bf16 v[20:23], v[168:171], v[192:195], v[20:23]\n"
        "ds_read_b64_tr_b16 v[58:59], v229 offset:16384\n"
        "v_mfma_f32_16x16x32_bf16 v[84:87], v[168:171], v[196:199], v[84:87]\n"
        "global_load_lds_dwordx4 v240, s[68:69] offset:2048\n"
        "v_mfma_f32_16x16x32_bf16 v[84:87], v[172:175], v[212:215], v[84:87]\n"
        "v_mfma_f32_16x16x32_bf16 v[80:83], v[164:167], v[212:215], v[80:83]\n"
        "v_mfma_f32_16x16x32_bf16 v[80:83], v[160:163], v[196:199], v[80:83]\n"
        "v_accvgpr_read_b32 v176, a144\n"
        "v_cvt_pk_bf16_f32 v176, v176, v177\n"
        "v_mfma_f32_16x16x32_bf16 v[112:115], v[160:163], v[200:203], v[112:115]\n"
        "v_cvt_pk_bf16_f32 v177, v178, v179\n"
        "v_cvt_pk_bf16_f32 v178, v180, v181\n"
        "v_mfma_f32_16x16x32_bf16 v[112:115], v[164:167], v[216:219], v[112:115]\n"
        "global_load_lds_dwordx4 v241, s[68:69] offset:3072\n"
        "s_add_u32 m0, s72, 0x10000\n"
        "v_cvt_pk_bf16_f32 v179, v182, v183\n"
        "v_mfma_f32_16x16x32_bf16 v[116:119], v[172:175], v[216:219], v[116:119]\n"
        "s_add_u32 s68, s68, 0x80000\n"
        "s_addc_u32 s69, s69, 0\n"
        "v_accvgpr_write_b32 a216, v176\n"
        "v_mfma_f32_16x16x32_bf16 v[116:119], v[168:171], v[200:203], v[116:119]\n"
        "v_accvgpr_write_b32 a217, v177\n"
        "v_accvgpr_write_b32 a218, v178\n"
        "v_mfma_f32_16x16x32_bf16 v[148:151], v[168:171], v[204:207], v[148:151]\n"
        "ds_read_b64_tr_b16 v[168:169], v232 offset:16384\n"
        "v_accvgpr_write_b32 a219, v179\n"
        "v_mfma_f32_16x16x32_bf16 v[148:151], v[172:175], v[220:223], v[148:151]\n"
        "ds_read_b64_tr_b16 v[172:173], v232 offset:24576\n"
        "global_load_lds_dwordx4 v242, s[64:65] offset:0\n"
        "v_mfma_f32_16x16x32_bf16 v[144:147], v[164:167], v[220:223], v[144:147]\n"
        "ds_read_b64_tr_b16 v[164:165], v230 offset:24576\n"
        "v_accvgpr_read_b32 v177, a153\n"
        "v_mfma_f32_16x16x32_bf16 v[144:147], v[160:163], v[204:207], v[144:147]\n"
        "ds_read_b64_tr_b16 v[160:161], v230 offset:16384\n"
        "v_accvgpr_read_b32 v178, a154\n"
        "v_mfma_f32_16x16x32_bf16 v[24:27], v[32:35], v[192:195], v[24:27]\n"
        "ds_read_b64_tr_b16 v[162:163], v231 offset:16384\n"
        "v_accvgpr_read_b32 v179, a155\n"
        "v_mfma_f32_16x16x32_bf16 v[24:27], v[36:39], v[208:211], v[24:27]\n"
        "ds_read_b64_tr_b16 v[166:167], v231 offset:24576\n"
        "global_load_lds_dwordx4 v243, s[64:65] offset:1024\n"
        "v_mfma_f32_16x16x32_bf16 v[28:31], v[44:47], v[208:211], v[28:31]\n"
        "ds_read_b64_tr_b16 v[174:175], v233 offset:24576\n"
        "v_accvgpr_read_b32 v180, a184\n"
        "v_mfma_f32_16x16x32_bf16 v[28:31], v[40:43], v[192:195], v[28:31]\n"
        "ds_read_b64_tr_b16 v[170:171], v233 offset:16384\n"
        "v_accvgpr_read_b32 v181, a185\n"
        "v_mfma_f32_16x16x32_bf16 v[92:95], v[40:43], v[196:199], v[92:95]\n"
        "ds_read_b128 v[192:195], v246 offset:34816\n"
        "v_accvgpr_read_b32 v182, a186\n"
        "v_mfma_f32_16x16x32_bf16 v[92:95], v[44:47], v[212:215], v[92:95]\n"
        "ds_read_b128 v[208:211], v247 offset:34816\n"
        "global_load_lds_dwordx4 v244, s[64:65] offset:2048\n"
        "v_mfma_f32_16x16x32_bf16 v[88:91], v[36:39], v[212:215], v[88:91]\n"
        "ds_read_b128 v[212:215], v247 offset:32768\n"
        "v_accvgpr_read_b32 v183, a187\n"
        "v_mfma_f32_16x16x32_bf16 v[88:91], v[32:35], v[196:199], v[88:91]\n"
        "ds_read_b128 v[196:199], v246 offset:32768\n"
        "v_mfma_f32_16x16x32_bf16 v[120:123], v[32:35], v[200:203], v[120:123]\n"
        "v_mfma_f32_16x16x32_bf16 v[120:123], v[36:39], v[216:219], v[120:123]\n"
        "global_load_lds_dwordx4 v245, s[64:65] offset:3072\n"
        "s_add_u32 m0, s72, 0x11000\n"
        "v_mfma_f32_16x16x32_bf16 v[124:127], v[44:47], v[216:219], v[124:127]\n"
        "ds_read_b128 v[216:219], v247 offset:38912\n"
        "s_add_u32 s64, s64, 0x80\n"
        "s_addc_u32 s65, s65, 0\n"
        "v_mfma_f32_16x16x32_bf16 v[124:127], v[40:43], v[200:203], v[124:127]\n"
        "ds_read_b128 v[200:203], v246 offset:38912\n"
        "s_waitcnt lgkmcnt(6)\n"
        "v_mfma_f32_16x16x32_bf16 v[156:159], v[40:43], v[204:207], v[156:159]\n"
        "ds_read_b64_tr_b16 v[40:41], v228 offset:17408\n"
        "s_waitcnt lgkmcnt(6)\n"
        "v_mfma_f32_16x16x32_bf16 v[156:159], v[44:47], v[220:223], v[156:159]\n"
        "ds_read_b64_tr_b16 v[44:45], v228 offset:25600\n"
        "global_load_lds_dwordx4 v242, s[66:67] offset:0\n"
        "v_mfma_f32_16x16x32_bf16 v[152:155], v[36:39], v[220:223], v[152:155]\n"
        "ds_read_b128 v[220:223], v247 offset:36864\n"
        "s_waitcnt lgkmcnt(6)\n"
        "v_mfma_f32_16x16x32_bf16 v[152:155], v[32:35], v[204:207], v[152:155]\n"
        "ds_read_b128 v[204:207], v246 offset:36864\n"
        "v_accvgpr_read_b32 v184, a148\n"
        "v_mfma_f32_16x16x32_bf16 v[0:3], v[48:51], v[192:195], v[0:3]\n"
        "ds_read_b64_tr_b16 v[32:33], v226 offset:17408\n"
        "v_cvt_pk_bf16_f32 v184, v184, v185\n"
        "v_mfma_f32_16x16x32_bf16 v[0:3], v[52:55], v[208:211], v[0:3]\n"
        "ds_read_b64_tr_b16 v[34:35], v227 offset:17408\n"
        "v_cvt_pk_bf16_f32 v185, v186, v187\n"
        "v_mfma_f32_16x16x32_bf16 v[4:7], v[60:63], v[208:211], v[4:7]\n"
        "ds_read_b64_tr_b16 v[36:37], v226 offset:25600\n"
        "v_cvt_pk_bf16_f32 v186, v188, v189\n"
        "v_mfma_f32_16x16x32_bf16 v[4:7], v[56:59], v[192:195], v[4:7]\n"
        "ds_read_b64_tr_b16 v[38:39], v227 offset:25600\n"
        "s_waitcnt lgkmcnt(8)\n"
        "v_mfma_f32_16x16x32_bf16 v[68:71], v[56:59], v[196:199], v[68:71]\n"
        "ds_read_b64_tr_b16 v[46:47], v229 offset:25600\n"
        "v_cvt_pk_bf16_f32 v187, v190, v191\n"
        "v_mfma_f32_16x16x32_bf16 v[68:71], v[60:63], v[212:215], v[68:71]\n"
        "ds_read_b64_tr_b16 v[42:43], v229 offset:17408\n"
        "v_accvgpr_write_b32 a248, v184\n"
        "v_mfma_f32_16x16x32_bf16 v[64:67], v[52:55], v[212:215], v[64:67]\n"
        "s_waitcnt lgkmcnt(7)\n"
        "v_accvgpr_write_b32 a249, v185\n"
        "v_accvgpr_write_b32 a250, v186\n"
        "v_mfma_f32_16x16x32_bf16 v[64:67], v[48:51], v[196:199], v[64:67]\n"
        "s_waitcnt lgkmcnt(6)\n"
        "v_accvgpr_write_b32 a251, v187\n"
        "v_accvgpr_read_b32 v185, a157\n"
        "v_mfma_f32_16x16x32_bf16 v[96:99], v[48:51], v[200:203], v[96:99]\n"
        "global_load_lds_dwordx4 v243, s[66:67] offset:1024\n"
        "v_accvgpr_read_b32 v186, a158\n"
        "v_mfma_f32_16x16x32_bf16 v[96:99], v[52:55], v[216:219], v[96:99]\n"
        "v_accvgpr_read_b32 v187, a159\n"
        "v_accvgpr_read_b32 v188, a188\n"
        "v_mfma_f32_16x16x32_bf16 v[100:103], v[60:63], v[216:219], v[100:103]\n"
        "v_accvgpr_read_b32 v189, a189\n"
        "v_accvgpr_read_b32 v190, a190\n"
        "v_mfma_f32_16x16x32_bf16 v[100:103], v[56:59], v[200:203], v[100:103]\n"
        "v_accvgpr_read_b32 v191, a191\n"
        "v_mfma_f32_16x16x32_bf16 v[132:135], v[56:59], v[204:207], v[132:135]\n"
        "ds_read_b64_tr_b16 v[56:57], v232 offset:17408\n"
        "global_load_lds_dwordx4 v244, s[66:67] offset:2048\n"
        "v_mfma_f32_16x16x32_bf16 v[132:135], v[60:63], v[220:223], v[132:135]\n"
        "ds_read_b64_tr_b16 v[60:61], v232 offset:25600\n"
        "v_mfma_f32_16x16x32_bf16 v[128:131], v[52:55], v[220:223], v[128:131]\n"
        "ds_read_b64_tr_b16 v[52:53], v230 offset:25600\n"
        "v_mfma_f32_16x16x32_bf16 v[128:131], v[48:51], v[204:207], v[128:131]\n"
        "ds_read_b64_tr_b16 v[48:49], v230 offset:17408\n"
        "v_mfma_f32_16x16x32_bf16 v[8:11], v[160:163], v[192:195], v[8:11]\n"
        "ds_read_b64_tr_b16 v[50:51], v231 offset:17408\n"
        "global_load_lds_dwordx4 v245, s[66:67] offset:3072\n"
        "s_add_u32 m0, s73, 0x1c000\n"
        "v_mfma_f32_16x16x32_bf16 v[8:11], v[164:167], v[208:211], v[8:11]\n"
        "ds_read_b64_tr_b16 v[54:55], v231 offset:25600\n"
        "s_add_u32 s66, s66, 0x80\n"
        "s_addc_u32 s67, s67, 0\n"
        "v_mfma_f32_16x16x32_bf16 v[12:15], v[172:175], v[208:211], v[12:15]\n"
        "ds_read_b64_tr_b16 v[62:63], v233 offset:25600\n"
        "v_mfma_f32_16x16x32_bf16 v[12:15], v[168:171], v[192:195], v[12:15]\n"
        "ds_read_b64_tr_b16 v[58:59], v233 offset:17408\n"
        "v_mfma_f32_16x16x32_bf16 v[76:79], v[168:171], v[196:199], v[76:79]\n"
        "v_mfma_f32_16x16x32_bf16 v[76:79], v[172:175], v[212:215], v[76:79]\n"
        "global_store_dwordx4 v254, a[204:207], s[70:71] offset:1024 sc1\n"
        "s_add_u32 s70, s38, s97\n"
        "s_addc_u32 s71, s39, 0\n"
        "s_waitcnt lgkmcnt(6)\n"
        "v_mfma_f32_16x16x32_bf16 v[72:75], v[164:167], v[212:215], v[72:75]\n"
        "v_mfma_f32_16x16x32_bf16 v[72:75], v[160:163], v[196:199], v[72:75]\n"
        "v_accvgpr_read_b32 v176, a152\n"
        "v_cvt_pk_bf16_f32 v176, v176, v177\n"
        "v_mfma_f32_16x16x32_bf16 v[104:107], v[160:163], v[200:203], v[104:107]\n"
        "v_cvt_pk_bf16_f32 v177, v178, v179\n"
        "v_cvt_pk_bf16_f32 v178, v180, v181\n"
        "v_mfma_f32_16x16x32_bf16 v[104:107], v[164:167], v[216:219], v[104:107]\n"
        "v_cvt_pk_bf16_f32 v179, v182, v183\n"
        "v_accvgpr_write_b32 a220, v176\n"
        "v_mfma_f32_16x16x32_bf16 v[108:111], v[172:175], v[216:219], v[108:111]\n"
        "s_waitcnt vmcnt(14) lgkmcnt(0)\n"
        "v_accvgpr_write_b32 a221, v177\n"
        "v_mfma_f32_16x16x32_bf16 v[108:111], v[168:171], v[200:203], v[108:111]\n"
        "s_barrier\n"
        "v_mfma_f32_16x16x32_bf16 v[140:143], v[168:171], v[204:207], v[140:143]\n"
        "ds_read_b64_tr_b16 v[168:169], v228 offset:32768\n"
        "v_accvgpr_write_b32 a222, v178\n"
        "v_mfma_f32_16x16x32_bf16 v[140:143], v[172:175], v[220:223], v[140:143]\n"
        "ds_read_b64_tr_b16 v[172:173], v228 offset:40960\n"
        "v_accvgpr_write_b32 a223, v179\n"
        "v_mfma_f32_16x16x32_bf16 v[136:139], v[164:167], v[220:223], v[136:139]\n"
        "ds_read_b64_tr_b16 v[164:165], v226 offset:40960\n"
        "global_load_lds_dwordx4 v238, s[68:69] offset:0\n"
        "v_mfma_f32_16x16x32_bf16 v[136:139], v[160:163], v[204:207], v[136:139]\n"
        "ds_read_b64_tr_b16 v[160:161], v226 offset:32768\n"
        "s_add_u32 s97, s52, 0x2000\n"
        "v_accvgpr_read_b32 v177, a17\n"
        "v_mfma_f32_16x16x32_bf16 v[16:19], v[32:35], v[192:195], v[16:19]\n"
        "ds_read_b64_tr_b16 v[162:163], v227 offset:32768\n"
        "v_accvgpr_read_b32 v178, a18\n"
        "v_mfma_f32_16x16x32_bf16 v[16:19], v[36:39], v[208:211], v[16:19]\n"
        "ds_read_b64_tr_b16 v[166:167], v227 offset:40960\n"
        "v_accvgpr_read_b32 v179, a19\n"
        "v_mfma_f32_16x16x32_bf16 v[20:23], v[44:47], v[208:211], v[20:23]\n"
        "ds_read_b64_tr_b16 v[174:175], v229 offset:40960\n"
        "global_load_lds_dwordx4 v239, s[68:69] offset:1024\n"
        "v_mfma_f32_16x16x32_bf16 v[20:23], v[40:43], v[192:195], v[20:23]\n"
        "ds_read_b64_tr_b16 v[170:171], v229 offset:32768\n"
        "v_accvgpr_read_b32 v180, a48\n"
        "v_mfma_f32_16x16x32_bf16 v[84:87], v[40:43], v[196:199], v[84:87]\n"
        "v_accvgpr_read_b32 v181, a49\n"
        "v_accvgpr_read_b32 v182, a50\n"
        "v_mfma_f32_16x16x32_bf16 v[84:87], v[44:47], v[212:215], v[84:87]\n"
        "v_accvgpr_read_b32 v183, a51\n"
        "v_mfma_f32_16x16x32_bf16 v[80:83], v[36:39], v[212:215], v[80:83]\n"
        "global_load_lds_dwordx4 v240, s[68:69] offset:2048\n"
        "v_mfma_f32_16x16x32_bf16 v[80:83], v[32:35], v[196:199], v[80:83]\n"
        "v_mfma_f32_16x16x32_bf16 v[112:115], v[32:35], v[200:203], v[112:115]\n"
        "v_mfma_f32_16x16x32_bf16 v[112:115], v[36:39], v[216:219], v[112:115]\n"
        "v_mfma_f32_16x16x32_bf16 v[116:119], v[44:47], v[216:219], v[116:119]\n"
        "global_load_lds_dwordx4 v241, s[68:69] offset:3072\n"
        "s_add_u32 m0, s72, 0x8000\n"
        "v_mfma_f32_16x16x32_bf16 v[116:119], v[40:43], v[200:203], v[116:119]\n"
        "s_add_u32 s68, s68, 0x80000\n"
        "s_addc_u32 s69, s69, 0\n"
        "v_mfma_f32_16x16x32_bf16 v[148:151], v[40:43], v[204:207], v[148:151]\n"
        "ds_read_b64_tr_b16 v[40:41], v232 offset:32768\n"
        "v_mfma_f32_16x16x32_bf16 v[148:151], v[44:47], v[220:223], v[148:151]\n"
        "ds_read_b64_tr_b16 v[44:45], v232 offset:40960\n"
        "v_mfma_f32_16x16x32_bf16 v[144:147], v[36:39], v[220:223], v[144:147]\n"
        "ds_read_b64_tr_b16 v[36:37], v230 offset:40960\n"
        "global_load_lds_dwordx4 v242, s[64:65] offset:0\n"
        "v_mfma_f32_16x16x32_bf16 v[144:147], v[32:35], v[204:207], v[144:147]\n"
        "ds_read_b64_tr_b16 v[32:33], v230 offset:32768\n"
        "v_accvgpr_read_b32 v184, a156\n"
        "v_mfma_f32_16x16x32_bf16 v[24:27], v[48:51], v[192:195], v[24:27]\n"
        "ds_read_b64_tr_b16 v[34:35], v231 offset:32768\n"
        "v_cvt_pk_bf16_f32 v184, v184, v185\n"
        "v_mfma_f32_16x16x32_bf16 v[24:27], v[52:55], v[208:211], v[24:27]\n"
        "ds_read_b64_tr_b16 v[38:39], v231 offset:40960\n"
        "v_cvt_pk_bf16_f32 v185, v186, v187\n"
        "v_mfma_f32_16x16x32_bf16 v[28:31], v[60:63], v[208:211], v[28:31]\n"
        "ds_read_b64_tr_b16 v[46:47], v233 offset:40960\n"
        "v_cvt_pk_bf16_f32 v186, v188, v189\n"
        "v_mfma_f32_16x16x32_bf16 v[28:31], v[56:59], v[192:195], v[28:31]\n"
        "ds_read_b64_tr_b16 v[42:43], v233 offset:32768\n"
        "v_cvt_pk_bf16_f32 v187, v190, v191\n"
        "v_mfma_f32_16x16x32_bf16 v[92:95], v[56:59], v[196:199], v[92:95]\n"
        "ds_read_b128 v[192:195], v246 offset:2048\n"
        "v_accvgpr_write_b32 a252, v184\n"
        "v_mfma_f32_16x16x32_bf16 v[92:95], v[60:63], v[212:215], v[92:95]\n"
        "ds_read_b128 v[208:211], v247 offset:2048\n"
        "v_accvgpr_write_b32 a253, v185\n"
        "v_mfma_f32_16x16x32_bf16 v[88:91], v[52:55], v[212:215], v[88:91]\n"
        "ds_read_b128 v[212:215], v247 offset:0\n"
        "v_accvgpr_write_b32 a254, v186\n"
        "v_mfma_f32_16x16x32_bf16 v[88:91], v[48:51], v[196:199], v[88:91]\n"
        "ds_read_b128 v[196:199], v246 offset:0\n"
        "v_accvgpr_write_b32 a255, v187\n"
        "v_mfma_f32_16x16x32_bf16 v[120:123], v[48:51], v[200:203], v[120:123]\n"
        "global_load_lds_dwordx4 v243, s[64:65] offset:1024\n"
        "v_accvgpr_read_b32 v185, a21\n"
        "v_mfma_f32_16x16x32_bf16 v[120:123], v[52:55], v[216:219], v[120:123]\n"
        "v_accvgpr_read_b32 v186, a22\n"
        "v_accvgpr_read_b32 v187, a23\n"
        "v_mfma_f32_16x16x32_bf16 v[124:127], v[60:63], v[216:219], v[124:127]\n"
        "ds_read_b128 v[216:219], v247 offset:6144\n"
        "v_accvgpr_read_b32 v188, a52\n"
        "v_mfma_f32_16x16x32_bf16 v[124:127], v[56:59], v[200:203], v[124:127]\n"
        "ds_read_b128 v[200:203], v246 offset:6144\n"
        "v_accvgpr_read_b32 v189, a53\n"
        "v_mfma_f32_16x16x32_bf16 v[156:159], v[56:59], v[204:207], v[156:159]\n"
        "ds_read_b64_tr_b16 v[56:57], v228 offset:33792\n"
        "global_load_lds_dwordx4 v244, s[64:65] offset:2048\n"
        "v_mfma_f32_16x16x32_bf16 v[156:159], v[60:63], v[220:223], v[156:159]\n"
        "ds_read_b64_tr_b16 v[60:61], v228 offset:41984\n"
        "v_accvgpr_read_b32 v190, a54\n"
        "v_mfma_f32_16x16x32_bf16 v[152:155], v[52:55], v[220:223], v[152:155]\n"
        "ds_read_b128 v[220:223], v247 offset:4096\n"
        "v_accvgpr_read_b32 v191, a55\n"
        "v_mfma_f32_16x16x32_bf16 v[152:155], v[48:51], v[204:207], v[152:155]\n"
        "ds_read_b128 v[204:207], v246 offset:4096\n"
        "s_waitcnt lgkmcnt(6)\n"
        "v_mfma_f32_16x16x32_bf16 v[0:3], v[160:163], v[192:195], v[0:3]\n"
        "ds_read_b64_tr_b16 v[48:49], v226 offset:33792\n"
        "global_load_lds_dwordx4 v245, s[64:65] offset:3072\n"
        "s_add_u32 m0, s72, 0x9000\n"
        "v_mfma_f32_16x16x32_bf16 v[0:3], v[164:167], v[208:211], v[0:3]\n"
        "ds_read_b64_tr_b16 v[50:51], v227 offset:33792\n"
        "s_add_u32 s64, s64, 0x80\n"
        "s_addc_u32 s65, s65, 0\n"
        "v_mfma_f32_16x16x32_bf16 v[4:7], v[172:175], v[208:211], v[4:7]\n"
        "ds_read_b64_tr_b16 v[52:53], v226 offset:41984\n"
        "s_waitcnt lgkmcnt(8)\n"
        "v_mfma_f32_16x16x32_bf16 v[4:7], v[168:171], v[192:195], v[4:7]\n"
        "ds_read_b64_tr_b16 v[54:55], v227 offset:41984\n"
        "s_waitcnt lgkmcnt(8)\n"
        "v_mfma_f32_16x16x32_bf16 v[68:71], v[168:171], v[196:199], v[68:71]\n"
        "ds_read_b64_tr_b16 v[62:63], v229 offset:41984\n"
        "global_load_lds_dwordx4 v242, s[66:67] offset:0\n"
        "v_mfma_f32_16x16x32_bf16 v[68:71], v[172:175], v[212:215], v[68:71]\n"
        "ds_read_b64_tr_b16 v[58:59], v229 offset:33792\n"
        "s_waitcnt lgkmcnt(8)\n"
        "v_mfma_f32_16x16x32_bf16 v[64:67], v[164:167], v[212:215], v[64:67]\n"
        "s_waitcnt lgkmcnt(7)\n"
        "v_mfma_f32_16x16x32_bf16 v[64:67], v[160:163], v[196:199], v[64:67]\n"
        "s_waitcnt lgkmcnt(6)\n"
        "v_accvgpr_read_b32 v176, a16\n"
        "v_cvt_pk_bf16_f32 v176, v176, v177\n"
        "v_mfma_f32_16x16x32_bf16 v[96:99], v[160:163], v[200:203], v[96:99]\n"
        "global_load_lds_dwordx4 v243, s[66:67] offset:1024\n"
        "v_mfma_f32_16x16x32_bf16 v[96:99], v[164:167], v[216:219], v[96:99]\n"
        "v_cvt_pk_bf16_f32 v177, v178, v179\n"
        "v_cvt_pk_bf16_f32 v178, v180, v181\n"
        "v_mfma_f32_16x16x32_bf16 v[100:103], v[172:175], v[216:219], v[100:103]\n"
        "v_cvt_pk_bf16_f32 v179, v182, v183\n"
        "v_accvgpr_write_b32 a176, v176\n"
        "v_mfma_f32_16x16x32_bf16 v[100:103], v[168:171], v[200:203], v[100:103]\n"
        "v_accvgpr_write_b32 a177, v177\n"
        "v_accvgpr_write_b32 a178, v178\n"
        "v_mfma_f32_16x16x32_bf16 v[132:135], v[168:171], v[204:207], v[132:135]\n"
        "ds_read_b64_tr_b16 v[168:169], v232 offset:33792\n"
        "v_accvgpr_write_b32 a179, v179\n"
        "v_mfma_f32_16x16x32_bf16 v[132:135], v[172:175], v[220:223], v[132:135]\n"
        "ds_read_b64_tr_b16 v[172:173], v232 offset:41984\n"
        "global_load_lds_dwordx4 v244, s[66:67] offset:2048\n"
        "v_mfma_f32_16x16x32_bf16 v[128:131], v[164:167], v[220:223], v[128:131]\n"
        "ds_read_b64_tr_b16 v[164:165], v230 offset:41984\n"
        "v_accvgpr_read_b32 v177, a25\n"
        "v_mfma_f32_16x16x32_bf16 v[128:131], v[160:163], v[204:207], v[128:131]\n"
        "ds_read_b64_tr_b16 v[160:161], v230 offset:33792\n"
        "v_accvgpr_read_b32 v178, a26\n"
        "v_mfma_f32_16x16x32_bf16 v[8:11], v[32:35], v[192:195], v[8:11]\n"
        "ds_read_b64_tr_b16 v[162:163], v231 offset:33792\n"
        "v_accvgpr_read_b32 v179, a27\n"
        "v_mfma_f32_16x16x32_bf16 v[8:11], v[36:39], v[208:211], v[8:11]\n"
        "ds_read_b64_tr_b16 v[166:167], v231 offset:41984\n"
        "global_load_lds_dwordx4 v245, s[66:67] offset:3072\n"
        "s_add_u32 m0, s73, 0x20000\n"
        "v_mfma_f32_16x16x32_bf16 v[12:15], v[44:47], v[208:211], v[12:15]\n"
        "ds_read_b64_tr_b16 v[174:175], v233 offset:41984\n"
        "s_add_u32 s66, s66, 0x80\n"
        "s_addc_u32 s67, s67, 0\n"
        "v_mfma_f32_16x16x32_bf16 v[12:15], v[40:43], v[192:195], v[12:15]\n"
        "ds_read_b64_tr_b16 v[170:171], v233 offset:33792\n"
        "v_accvgpr_read_b32 v180, a56\n"
        "v_mfma_f32_16x16x32_bf16 v[76:79], v[40:43], v[196:199], v[76:79]\n"
        "v_accvgpr_read_b32 v181, a57\n"
        "v_accvgpr_read_b32 v182, a58\n"
        "v_mfma_f32_16x16x32_bf16 v[76:79], v[44:47], v[212:215], v[76:79]\n"
        "s_waitcnt lgkmcnt(6)\n"
        "v_accvgpr_read_b32 v183, a59\n"
        "v_mfma_f32_16x16x32_bf16 v[72:75], v[36:39], v[212:215], v[72:75]\n"
        "v_mfma_f32_16x16x32_bf16 v[72:75], v[32:35], v[196:199], v[72:75]\n"
        "v_mfma_f32_16x16x32_bf16 v[104:107], v[32:35], v[200:203], v[104:107]\n"
        "v_mfma_f32_16x16x32_bf16 v[104:107], v[36:39], v[216:219], v[104:107]\n"
        "v_mfma_f32_16x16x32_bf16 v[108:111], v[44:47], v[216:219], v[108:111]\n"
        "v_mfma_f32_16x16x32_bf16 v[108:111], v[40:43], v[200:203], v[108:111]\n"
        "global_store_dwordx4 v254, a[160:163], s[70:71] offset:0 sc1\n"
        "s_waitcnt vmcnt(14) lgkmcnt(0)\n"
        "s_barrier\n"
        "v_mfma_f32_16x16x32_bf16 v[140:143], v[40:43], v[204:207], v[140:143]\n"
        "ds_read_b64_tr_b16 v[40:41], v228 offset:49152\n"
        "v_mfma_f32_16x16x32_bf16 v[140:143], v[44:47], v[220:223], v[140:143]\n"
        "ds_read_b64_tr_b16 v[44:45], v228 offset:57344\n"
        "v_mfma_f32_16x16x32_bf16 v[136:139], v[36:39], v[220:223], v[136:139]\n"
        "ds_read_b64_tr_b16 v[36:37], v226 offset:57344\n"
        "global_load_lds_dwordx4 v238, s[68:69] offset:0\n"
        "v_mfma_f32_16x16x32_bf16 v[136:139], v[32:35], v[204:207], v[136:139]\n"
        "ds_read_b64_tr_b16 v[32:33], v226 offset:49152\n"
        "v_accvgpr_read_b32 v184, a20\n"
        "v_mfma_f32_16x16x32_bf16 v[16:19], v[48:51], v[192:195], v[16:19]\n"
        "ds_read_b64_tr_b16 v[34:35], v227 offset:49152\n"
        "v_cvt_pk_bf16_f32 v184, v184, v185\n"
        "v_mfma_f32_16x16x32_bf16 v[16:19], v[52:55], v[208:211], v[16:19]\n"
        "ds_read_b64_tr_b16 v[38:39], v227 offset:57344\n"
        "v_cvt_pk_bf16_f32 v185, v186, v187\n"
        "v_mfma_f32_16x16x32_bf16 v[20:23], v[60:63], v[208:211], v[20:23]\n"
        "ds_read_b64_tr_b16 v[46:47], v229 offset:57344\n"
        "v_cvt_pk_bf16_f32 v186, v188, v189\n"
        "v_mfma_f32_16x16x32_bf16 v[20:23], v[56:59], v[192:195], v[20:23]\n"
        "ds_read_b64_tr_b16 v[42:43], v229 offset:49152\n"
        "v_cvt_pk_bf16_f32 v187, v190, v191\n"
        "v_mfma_f32_16x16x32_bf16 v[84:87], v[56:59], v[196:199], v[84:87]\n"
        "global_load_lds_dwordx4 v239, s[68:69] offset:1024\n"
        "v_accvgpr_write_b32 a180, v184\n"
        "v_mfma_f32_16x16x32_bf16 v[84:87], v[60:63], v[212:215], v[84:87]\n"
        "v_accvgpr_write_b32 a181, v185\n"
        "v_accvgpr_write_b32 a182, v186\n"
        "v_mfma_f32_16x16x32_bf16 v[80:83], v[52:55], v[212:215], v[80:83]\n"
        "v_accvgpr_write_b32 a183, v187\n"
        "v_accvgpr_read_b32 v185, a29\n"
        "v_mfma_f32_16x16x32_bf16 v[80:83], v[48:51], v[196:199], v[80:83]\n"
        "LHA8_%=:\n"
        "s_cbranch_execz LHRA_%=\n"
        "v_accvgpr_read_b32 v186, a30\n"
        "v_accvgpr_read_b32 v187, a31\n"
        "v_mfma_f32_16x16x32_bf16 v[112:115], v[48:51], v[200:203], v[112:115]\n"
        "global_load_lds_dwordx4 v240, s[68:69] offset:2048\n"
        "v_accvgpr_read_b32 v188, a60\n"
        "v_mfma_f32_16x16x32_bf16 v[112:115], v[52:55], v[216:219], v[112:115]\n"
        "v_accvgpr_read_b32 v189, a61\n"
        "v_accvgpr_read_b32 v190, a62\n"
        "v_mfma_f32_16x16x32_bf16 v[116:119], v[60:63], v[216:219], v[116:119]\n"
        "v_accvgpr_read_b32 v191, a63\n"
        "v_mfma_f32_16x16x32_bf16 v[116:119], v[56:59], v[200:203], v[116:119]\n"
        "v_mfma_f32_16x16x32_bf16 v[148:151], v[56:59], v[204:207], v[148:151]\n"
        "ds_read_b64_tr_b16 v[56:57], v232 offset:49152\n"
        "global_load_lds_dwordx4 v241, s[68:69] offset:3072\n"
        "s_add_u32 m0, s72, 0x0\n"
        "v_mfma_f32_16x16x32_bf16 v[148:151], v[60:63], v[220:223], v[148:151]\n"
        "ds_read_b64_tr_b16 v[60:61], v232 offset:57344\n"
        "s_add_u32 s68, s68, 0x80000\n"
        "s_addc_u32 s69, s69, 0\n"
        "v_mfma_f32_16x16x32_bf16 v[144:147], v[52:55], v[220:223], v[144:147]\n"
        "ds_read_b64_tr_b16 v[52:53], v230 offset:57344\n"
        "v_mfma_f32_16x16x32_bf16 v[144:147], v[48:51], v[204:207], v[144:147]\n"
        "ds_read_b64_tr_b16 v[48:49], v230 offset:49152\n"
        "v_mfma_f32_16x16x32_bf16 v[24:27], v[160:163], v[192:195], v[24:27]\n"
        "ds_read_b64_tr_b16 v[50:51], v231 offset:49152\n"
        "global_load_lds_dwordx4 v242, s[64:65] offset:0\n"
        "v_mfma_f32_16x16x32_bf16 v[24:27], v[164:167], v[208:211], v[24:27]\n"
        "ds_read_b64_tr_b16 v[54:55], v231 offset:57344\n"
        "v_mfma_f32_16x16x32_bf16 v[28:31], v[172:175], v[208:211], v[28:31]\n"
        "ds_read_b64_tr_b16 v[62:63], v233 offset:57344\n"
        "v_mfma_f32_16x16x32_bf16 v[28:31], v[168:171], v[192:195], v[28:31]\n"
        "ds_read_b64_tr_b16 v[58:59], v233 offset:49152\n"
        "v_mfma_f32_16x16x32_bf16 v[92:95], v[168:171], v[196:199], v[92:95]\n"
        "ds_read_b128 v[192:195], v248 offset:2048\n"
        "global_load_lds_dwordx4 v243, s[64:65] offset:1024\n"
        "v_mfma_f32_16x16x32_bf16 v[92:95], v[172:175], v[212:215], v[92:95]\n"
        "ds_read_b128 v[208:211], v249 offset:2048\n"
        "v_mfma_f32_16x16x32_bf16 v[88:91], v[164:167], v[212:215], v[88:91]\n"
        "ds_read_b128 v[212:215], v249 offset:0\n"
        "v_mfma_f32_16x16x32_bf16 v[88:91], v[160:163], v[196:199], v[88:91]\n"
        "ds_read_b128 v[196:199], v248 offset:0\n"
        "v_accvgpr_read_b32 v176, a24\n"
        "v_mfma_f32_16x16x32_bf16 v[120:123], v[160:163], v[200:203], v[120:123]\n"
        "v_cvt_pk_bf16_f32 v176, v176, v177\n"
        "v_cvt_pk_bf16_f32 v177, v178, v179\n"
        "v_mfma_f32_16x16x32_bf16 v[120:123], v[164:167], v[216:219], v[120:123]\n"
        "global_load_lds_dwordx4 v244, s[64:65] offset:2048\n"
        "v_cvt_pk_bf16_f32 v178, v180, v181\n"
        "v_mfma_f32_16x16x32_bf16 v[124:127], v[172:175], v[216:219], v[124:127]\n"
        "ds_read_b128 v[216:219], v249 offset:6144\n"
        "v_cvt_pk_bf16_f32 v179, v182, v183\n"
        "v_mfma_f32_16x16x32_bf16 v[124:127], v[168:171], v[200:203], v[124:127]\n"
        "ds_read_b128 v[200:203], v248 offset:6144\n"
        "v_accvgpr_write_b32 a184, v176\n"
        "v_mfma_f32_16x16x32_bf16 v[156:159], v[168:171], v[204:207], v[156:159]\n"
        "ds_read_b64_tr_b16 v[168:169], v228 offset:50176\n"
        "v_accvgpr_write_b32 a185, v177\n"
        "v_mfma_f32_16x16x32_bf16 v[156:159], v[172:175], v[220:223], v[156:159]\n"
        "ds_read_b64_tr_b16 v[172:173], v228 offset:58368\n"
        "v_accvgpr_write_b32 a186, v178\n"
        "v_mfma_f32_16x16x32_bf16 v[152:155], v[164:167], v[220:223], v[152:155]\n"
        "ds_read_b128 v[220:223], v249 offset:4096\n"
        "v_accvgpr_write_b32 a187, v179\n"
        "v_mfma_f32_16x16x32_bf16 v[152:155], v[160:163], v[204:207], v[152:155]\n"
        "ds_read_b128 v[204:207], v248 offset:4096\n"
        "s_waitcnt lgkmcnt(6)\n"
        "v_mfma_f32_16x16x32_bf16 v[0:3], v[32:35], v[192:195], v[0:3]\n"
        "ds_read_b64_tr_b16 v[160:161], v226 offset:50176\n"
        "global_load_lds_dwordx4 v245, s[64:65] offset:3072\n"
        "s_add_u32 m0, s72, 0x1000\n"
        "v_mfma_f32_16x16x32_bf16 v[0:3], v[36:39], v[208:211], v[0:3]\n"
        "ds_read_b64_tr_b16 v[162:163], v227 offset:50176\n"
        "s_add_u32 s64, s64, 0x80\n"
        "s_addc_u32 s65, s65, 0\n"
        "v_mfma_f32_16x16x32_bf16 v[4:7], v[44:47], v[208:211], v[4:7]\n"
        "ds_read_b64_tr_b16 v[164:165], v226 offset:58368\n"
        "v_accvgpr_read_b32 v177, a81\n"
        "v_mfma_f32_16x16x32_bf16 v[4:7], v[40:43], v[192:195], v[4:7]\n"
        "ds_read_b64_tr_b16 v[166:167], v227 offset:58368\n"
        "v_accvgpr_read_b32 v178, a82\n"
        "v_mfma_f32_16x16x32_bf16 v[68:71], v[40:43], v[196:199], v[68:71]\n"
        "ds_read_b64_tr_b16 v[174:175], v229 offset:58368\n"
        "global_load_lds_dwordx4 v242, s[66:67] offset:0\n"
        "v_mfma_f32_16x16x32_bf16 v[68:71], v[44:47], v[212:215], v[68:71]\n"
        "ds_read_b64_tr_b16 v[170:171], v229 offset:50176\n"
        "v_accvgpr_read_b32 v179, a83\n"
        "v_mfma_f32_16x16x32_bf16 v[64:67], v[36:39], v[212:215], v[64:67]\n"
        "s_waitcnt lgkmcnt(7)\n"
        "v_accvgpr_read_b32 v180, a112\n"
        "v_accvgpr_read_b32 v181, a113\n"
        "v_mfma_f32_16x16x32_bf16 v[64:67], v[32:35], v[196:199], v[64:67]\n"
        "s_waitcnt lgkmcnt(6)\n"
        "v_accvgpr_read_b32 v182, a114\n"
        "v_accvgpr_read_b32 v183, a115\n"
        "v_mfma_f32_16x16x32_bf16 v[96:99], v[32:35], v[200:203], v[96:99]\n"
        "global_load_lds_dwordx4 v243, s[66:67] offset:1024\n"
        "v_mfma_f32_16x16x32_bf16 v[96:99], v[36:39], v[216:219], v[96:99]\n"
        "v_mfma_f32_16x16x32_bf16 v[100:103], v[44:47], v[216:219], v[100:103]\n"
        "v_mfma_f32_16x16x32_bf16 v[100:103], v[40:43], v[200:203], v[100:103]\n"
        "v_mfma_f32_16x16x32_bf16 v[132:135], v[40:43], v[204:207], v[132:135]\n"
        "ds_read_b64_tr_b16 v[40:41], v232 offset:50176\n"
        "global_load_lds_dwordx4 v244, s[66:67] offset:2048\n"
        "v_mfma_f32_16x16x32_bf16 v[132:135], v[44:47], v[220:223], v[132:135]\n"
        "ds_read_b64_tr_b16 v[44:45], v232 offset:58368\n"
        "v_mfma_f32_16x16x32_bf16 v[128:131], v[36:39], v[220:223], v[128:131]\n"
        "ds_read_b64_tr_b16 v[36:37], v230 offset:58368\n"
        "v_mfma_f32_16x16x32_bf16 v[128:131], v[32:35], v[204:207], v[128:131]\n"
        "ds_read_b64_tr_b16 v[32:33], v230 offset:50176\n"
        "v_accvgpr_read_b32 v184, a28\n"
        "v_mfma_f32_16x16x32_bf16 v[8:11], v[48:51], v[192:195], v[8:11]\n"
        "ds_read_b64_tr_b16 v[34:35], v231 offset:50176\n"
        "global_load_lds_dwordx4 v245, s[66:67] offset:3072\n"
        "s_add_u32 m0, s73, 0x24000\n"
        "v_mfma_f32_16x16x32_bf16 v[8:11], v[52:55], v[208:211], v[8:11]\n"
        "ds_read_b64_tr_b16 v[38:39], v231 offset:58368\n"
        "v_cvt_pk_bf16_f32 v184, v184, v185\n"
        "v_mfma_f32_16x16x32_bf16 v[12:15], v[60:63], v[208:211], v[12:15]\n"
        "ds_read_b64_tr_b16 v[46:47], v233 offset:58368\n"
        "v_cvt_pk_bf16_f32 v185, v186, v187\n"
        "v_mfma_f32_16x16x32_bf16 v[12:15], v[56:59], v[192:195], v[12:15]\n"
        "ds_read_b64_tr_b16 v[42:43], v233 offset:50176\n"
        "v_cvt_pk_bf16_f32 v186, v188, v189\n"
        "v_mfma_f32_16x16x32_bf16 v[76:79], v[56:59], v[196:199], v[76:79]\n"
        "s_add_u32 s66, s66, 0x80\n"
        "s_addc_u32 s67, s67, 0\n"
        "v_cvt_pk_bf16_f32 v187, v190, v191\n"
        "v_mfma_f32_16x16x32_bf16 v[76:79], v[60:63], v[212:215], v[76:79]\n"
        "s_waitcnt lgkmcnt(6)\n"
        "v_accvgpr_write_b32 a188, v184\n"
        "v_accvgpr_write_b32 a189, v185\n"
        "v_mfma_f32_16x16x32_bf16 v[72:75], v[52:55], v[212:215], v[72:75]\n"
        "v_accvgpr_write_b32 a190, v186\n"
        "v_accvgpr_write_b32 a191, v187\n"
        "v_mfma_f32_16x16x32_bf16 v[72:75], v[48:51], v[196:199], v[72:75]\n"
        "v_accvgpr_read_b32 v185, a85\n"
        "v_accvgpr_read_b32 v186, a86\n"
        "v_mfma_f32_16x16x32_bf16 v[104:107], v[48:51], v[200:203], v[104:107]\n"
        "v_accvgpr_read_b32 v187, a87\n"
        "v_accvgpr_read_b32 v188, a116\n"
        "v_mfma_f32_16x16x32_bf16 v[104:107], v[52:55], v[216:219], v[104:107]\n"
        "v_accvgpr_read_b32 v189, a117\n"
        "v_accvgpr_read_b32 v190, a118\n"
        "v_mfma_f32_16x16x32_bf16 v[108:111], v[60:63], v[216:219], v[108:111]\n"
        "v_accvgpr_read_b32 v191, a119\n"
        "v_mfma_f32_16x16x32_bf16 v[108:111], v[56:59], v[200:203], v[108:111]\n"
        "s_waitcnt vmcnt(13) lgkmcnt(0)\n"
        "v_mfma_f32_16x16x32_bf16 v[140:143], v[56:59], v[204:207], v[140:143]\n"
        "s_barrier\n"
        "v_mfma_f32_16x16x32_bf16 v[140:143], v[60:63], v[220:223], v[140:143]\n"
        "ds_read_b64_tr_b16 v[60:61], v228 offset:24576\n"
        "global_load_lds_dwordx4 v238, s[68:69] offset:0\n"
        "v_mfma_f32_16x16x32_bf16 v[136:139], v[52:55], v[220:223], v[136:139]\n"
        "ds_read_b64_tr_b16 v[52:53], v226 offset:24576\n"
        "v_mfma_f32_16x16x32_bf16 v[136:139], v[48:51], v[204:207], v[136:139]\n"
        "ds_read_b64_tr_b16 v[48:49], v226 offset:16384\n"
        "v_mfma_f32_16x16x32_bf16 v[16:19], v[160:163], v[192:195], v[16:19]\n"
        "ds_read_b64_tr_b16 v[50:51], v227 offset:16384\n"
        "v_mfma_f32_16x16x32_bf16 v[16:19], v[164:167], v[208:211], v[16:19]\n"
        "ds_read_b64_tr_b16 v[54:55], v227 offset:24576\n"
        "global_load_lds_dwordx4 v239, s[68:69] offset:1024\n"
        "v_mfma_f32_16x16x32_bf16 v[20:23], v[172:175], v[208:211], v[20:23]\n"
        "ds_read_b64_tr_b16 v[62:63], v229 offset:24576\n"
        "v_mfma_f32_16x16x32_bf16 v[20:23], v[168:171], v[192:195], v[20:23]\n"
        "ds_read_b64_tr_b16 v[56:57], v228 offset:16384\n"
        "v_mfma_f32_16x16x32_bf16 v[84:87], v[168:171], v[196:199], v[84:87]\n"
        "ds_read_b64_tr_b16 v[58:59], v229 offset:16384\n"
        "v_mfma_f32_16x16x32_bf16 v[84:87], v[172:175], v[212:215], v[84:87]\n"
        "global_load_lds_dwordx4 v240, s[68:69] offset:2048\n"
        "v_mfma_f32_16x16x32_bf16 v[80:83], v[164:167], v[212:215], v[80:83]\n"
        "v_mfma_f32_16x16x32_bf16 v[80:83], v[160:163], v[196:199], v[80:83]\n"
        "v_accvgpr_read_b32 v176, a80\n"
        "v_cvt_pk_bf16_f32 v176, v176, v177\n"
        "v_mfma_f32_16x16x32_bf16 v[112:115], v[160:163], v[200:203], v[112:115]\n"
        "v_cvt_pk_bf16_f32 v177, v178, v179\n"
        "v_cvt_pk_bf16_f32 v178, v180, v181\n"
        "v_mfma_f32_16x16x32_bf16 v[112:115], v[164:167], v[216:219], v[112:115]\n"
        "v_cvt_pk_bf16_f32 v179, v182, v183\n"
        "v_accvgpr_write_b32 a16, v176\n"
        "v_mfma_f32_16x16x32_bf16 v[116:119], v[172:175], v[216:219], v[116:119]\n"
        "global_store_dwordx4 v254, a[164:167], s[70:71] offset:1024 sc1\n"
        "s_add_u32 s70, s38, s97\n"
        "s_addc_u32 s71, s39, 0\n"
        "s_add_u32 s97, s52, 0x3000\n"
        "v_accvgpr_write_b32 a17, v177\n"
        "v_accvgpr_write_b32 a18, v178\n"
        "v_mfma_f32_16x16x32_bf16 v[116:119], v[168:171], v[200:203], v[116:119]\n"
        "v_accvgpr_write_b32 a19, v179\n"
        "v_accvgpr_read_b32 v177, a89\n"
        "v_mfma_f32_16x16x32_bf16 v[148:151], v[168:171], v[204:207], v[148:151]\n"
        "ds_read_b64_tr_b16 v[168:169], v232 offset:16384\n"
        "v_accvgpr_read_b32 v178, a90\n"
        "v_mfma_f32_16x16x32_bf16 v[148:151], v[172:175], v[220:223], v[148:151]\n"
        "ds_read_b64_tr_b16 v[172:173], v232 offset:24576\n"
        "global_load_lds_dwordx4 v241, s[68:69] offset:3072\n"
        "s_add_u32 m0, s72, 0x10000\n"
        "v_mfma_f32_16x16x32_bf16 v[144:147], v[164:167], v[220:223], v[144:147]\n"
        "ds_read_b64_tr_b16 v[164:165], v230 offset:24576\n"
        "s_add_u32 s68, s68, 0x80000\n"
        "s_addc_u32 s69, s69, 0\n"
        "v_mfma_f32_16x16x32_bf16 v[144:147], v[160:163], v[204:207], v[144:147]\n"
        "ds_read_b64_tr_b16 v[160:161], v230 offset:16384\n"
        "v_accvgpr_read_b32 v179, a91\n"
        "v_mfma_f32_16x16x32_bf16 v[24:27], v[32:35], v[192:195], v[24:27]\n"
        "ds_read_b64_tr_b16 v[162:163], v231 offset:16384\n"
        "v_accvgpr_read_b32 v180, a120\n"
        "v_mfma_f32_16x16x32_bf16 v[24:27], v[36:39], v[208:211], v[24:27]\n"
        "ds_read_b64_tr_b16 v[166:167], v231 offset:24576\n"
        "global_load_lds_dwordx4 v242, s[64:65] offset:0\n"
        "v_mfma_f32_16x16x32_bf16 v[28:31], v[44:47], v[208:211], v[28:31]\n"
        "ds_read_b64_tr_b16 v[174:175], v233 offset:24576\n"
        "v_accvgpr_read_b32 v181, a121\n"
        "v_mfma_f32_16x16x32_bf16 v[28:31], v[40:43], v[192:195], v[28:31]\n"
        "ds_read_b64_tr_b16 v[170:171], v233 offset:16384\n"
        "v_accvgpr_read_b32 v182, a122\n"
        "v_mfma_f32_16x16x32_bf16 v[92:95], v[40:43], v[196:199], v[92:95]\n"
        "ds_read_b128 v[192:195], v246 offset:34816\n"
        "v_accvgpr_read_b32 v183, a123\n"
        "v_mfma_f32_16x16x32_bf16 v[92:95], v[44:47], v[212:215], v[92:95]\n"
        "ds_read_b128 v[208:211], v247 offset:34816\n"
        "global_load_lds_dwordx4 v243, s[64:65] offset:1024\n"
        "v_mfma_f32_16x16x32_bf16 v[88:91], v[36:39], v[212:215], v[88:91]\n"
        "ds_read_b128 v[212:215], v247 offset:32768\n"
        "v_mfma_f32_16x16x32_bf16 v[88:91], v[32:35], v[196:199], v[88:91]\n"
        "ds_read_b128 v[196:199], v246 offset:32768\n"
        "v_mfma_f32_16x16x32_bf16 v[120:123], v[32:35], v[200:203], v[120:123]\n"
        "v_mfma_f32_16x16x32_bf16 v[120:123], v[36:39], v[216:219], v[120:123]\n"
        "global_load_lds_dwordx4 v244, s[64:65] offset:2048\n"
        "v_mfma_f32_16x16x32_bf16 v[124:127], v[44:47], v[216:219], v[124:127]\n"
        "ds_read_b128 v[216:219], v247 offset:38912\n"
        "v_mfma_f32_16x16x32_bf16 v[124:127], v[40:43], v[200:203], v[124:127]\n"
        "ds_read_b128 v[200:203], v246 offset:38912\n"
        "s_waitcnt lgkmcnt(6)\n"
        "v_mfma_f32_16x16x32_bf16 v[156:159], v[40:43], v[204:207], v[156:159]\n"
        "ds_read_b64_tr_b16 v[40:41], v228 offset:17408\n"
        "s_waitcnt lgkmcnt(6)\n"
        "v_mfma_f32_16x16x32_bf16 v[156:159], v[44:47], v[220:223], v[156:159]\n"
        "ds_read_b64_tr_b16 v[44:45], v228 offset:25600\n"
        "global_load_lds_dwordx4 v245, s[64:65] offset:3072\n"
        "s_add_u32 m0, s72, 0x11000\n"
        "v_mfma_f32_16x16x32_bf16 v[152:155], v[36:39], v[220:223], v[152:155]\n"
        "ds_read_b128 v[220:223], v247 offset:36864\n"
        "s_add_u32 s64, s64, 0x80\n"
        "s_addc_u32 s65, s65, 0\n"
        "v_mfma_f32_16x16x32_bf16 v[152:155], v[32:35], v[204:207], v[152:155]\n"
        "ds_read_b128 v[204:207], v246 offset:36864\n"
        "v_accvgpr_read_b32 v184, a84\n"
        "v_mfma_f32_16x16x32_bf16 v[0:3], v[48:51], v[192:195], v[0:3]\n"
        "ds_read_b64_tr_b16 v[32:33], v226 offset:17408\n"
        "s_waitcnt lgkmcnt(7)\n"
        "v_mfma_f32_16x16x32_bf16 v[0:3], v[52:55], v[208:211], v[0:3]\n"
        "ds_read_b64_tr_b16 v[34:35], v227 offset:17408\n"
        "v_cvt_pk_bf16_f32 v184, v184, v185\n"
        "v_mfma_f32_16x16x32_bf16 v[4:7], v[60:63], v[208:211], v[4:7]\n"
        "ds_read_b64_tr_b16 v[36:37], v226 offset:25600\n"
        "v_cvt_pk_bf16_f32 v185, v186, v187\n"
        "v_mfma_f32_16x16x32_bf16 v[4:7], v[56:59], v[192:195], v[4:7]\n"
        "ds_read_b64_tr_b16 v[38:39], v227 offset:25600\n"
        "v_cvt_pk_bf16_f32 v186, v188, v189\n"
        "v_mfma_f32_16x16x32_bf16 v[68:71], v[56:59], v[196:199], v[68:71]\n"
        "ds_read_b64_tr_b16 v[46:47], v229 offset:25600\n"
        "v_cvt_pk_bf16_f32 v187, v190, v191\n"
        "v_mfma_f32_16x16x32_bf16 v[68:71], v[60:63], v[212:215], v[68:71]\n"
        "ds_read_b64_tr_b16 v[42:43], v229 offset:17408\n"
        "v_accvgpr_write_b32 a20, v184\n"
        "v_mfma_f32_16x16x32_bf16 v[64:67], v[52:55], v[212:215], v[64:67]\n"
        "global_load_lds_dwordx4 v242, s[66:67] offset:0\n"
        "v_accvgpr_write_b32 a21, v185\n"
        "v_mfma_f32_16x16x32_bf16 v[64:67], v[48:51], v[196:199], v[64:67]\n"
        "s_waitcnt lgkmcnt(6)\n"
        "v_accvgpr_write_b32 a22, v186\n"
        "v_accvgpr_write_b32 a23, v187\n"
        "v_mfma_f32_16x16x32_bf16 v[96:99], v[48:51], v[200:203], v[96:99]\n"
        "v_accvgpr_read_b32 v185, a93\n"
        "v_accvgpr_read_b32 v186, a94\n"
        "v_mfma_f32_16x16x32_bf16 v[96:99], v[52:55], v[216:219], v[96:99]\n"
        "v_accvgpr_read_b32 v187, a95\n"
        "v_accvgpr_read_b32 v188, a124\n"
        "v_mfma_f32_16x16x32_bf16 v[100:103], v[60:63], v[216:219], v[100:103]\n"
        "global_load_lds_dwordx4 v243, s[66:67] offset:1024\n"
        "v_accvgpr_read_b32 v189, a125\n"
        "v_mfma_f32_16x16x32_bf16 v[100:103], v[56:59], v[200:203], v[100:103]\n"
        "v_accvgpr_read_b32 v190, a126\n"
        "v_accvgpr_read_b32 v191, a127\n"
        "v_mfma_f32_16x16x32_bf16 v[132:135], v[56:59], v[204:207], v[132:135]\n"
        "ds_read_b64_tr_b16 v[56:57], v232 offset:17408\n"
        "v_accvgpr_write_b32 a93, v252\n"
        "v_mfma_f32_16x16x32_bf16 v[132:135], v[60:63], v[220:223], v[132:135]\n"
        "ds_read_b64_tr_b16 v[60:61], v232 offset:25600\n"
        "v_mfma_f32_16x16x32_bf16 v[128:131], v[52:55], v[220:223], v[128:131]\n"
        "ds_read_b64_tr_b16 v[52:53], v230 offset:25600\n"
        "global_load_lds_dwordx4 v244, s[66:67] offset:2048\n"
        "v_mfma_f32_16x16x32_bf16 v[128:131], v[48:51], v[204:207], v[128:131]\n"
        "ds_read_b64_tr_b16 v[48:49], v230 offset:17408\n"
        "v_mfma_f32_16x16x32_bf16 v[8:11], v[160:163], v[192:195], v[8:11]\n"
        "ds_read_b64_tr_b16 v[50:51], v231 offset:17408\n"
        "v_mfma_f32_16x16x32_bf16 v[8:11], v[164:167], v[208:211], v[8:11]\n"
        "ds_read_b64_tr_b16 v[54:55], v231 offset:25600\n"
        "v_mfma_f32_16x16x32_bf16 v[12:15], v[172:175], v[208:211], v[12:15]\n"
        "ds_read_b64_tr_b16 v[62:63], v233 offset:25600\n"
        "global_load_lds_dwordx4 v245, s[66:67] offset:3072\n"
        "s_add_u32 m0, s73, 0x1c000\n"
        "v_mfma_f32_16x16x32_bf16 v[12:15], v[168:171], v[192:195], v[12:15]\n"
        "ds_read_b64_tr_b16 v[58:59], v233 offset:17408\n"
        "s_add_u32 s66, s66, 0x80\n"
        "s_addc_u32 s67, s67, 0\n"
        "v_mfma_f32_16x16x32_bf16 v[76:79], v[168:171], v[196:199], v[76:79]\n"
        "v_mfma_f32_16x16x32_bf16 v[76:79], v[172:175], v[212:215], v[76:79]\n"
        "s_waitcnt lgkmcnt(6)\n"
        "v_mfma_f32_16x16x32_bf16 v[72:75], v[164:167], v[212:215], v[72:75]\n"
        "v_mfma_f32_16x16x32_bf16 v[72:75], v[160:163], v[196:199], v[72:75]\n"
        "v_accvgpr_read_b32 v176, a88\n"
        "v_cvt_pk_bf16_f32 v176, v176, v177\n"
        "v_mfma_f32_16x16x32_bf16 v[104:107], v[160:163], v[200:203], v[104:107]\n"
        "s_lshl_b32 s90, s43, 19\n"
        "v_cvt_pk_bf16_f32 v177, v178, v179\n"
        "v_cvt_pk_bf16_f32 v178, v180, v181\n"
        "v_mfma_f32_16x16x32_bf16 v[104:107], v[164:167], v[216:219], v[104:107]\n"
        "s_add_u32 s90, s90, s72\n"
        "v_cvt_pk_bf16_f32 v179, v182, v183\n"
        "v_accvgpr_write_b32 a24, v176\n"
        "v_mfma_f32_16x16x32_bf16 v[108:111], v[172:175], v[216:219], v[108:111]\n"
        "s_and_b32 s78, s45, 1\n"
        "v_accvgpr_write_b32 a25, v177\n"
        "v_accvgpr_write_b32 a26, v178\n"
        "v_mfma_f32_16x16x32_bf16 v[108:111], v[168:171], v[200:203], v[108:111]\n"
        "s_waitcnt vmcnt(13) lgkmcnt(0)\n"
        "v_accvgpr_write_b32 a27, v179\n"
        "s_lshl_b32 s78, s78, 15\n"
        "v_mfma_f32_16x16x32_bf16 v[140:143], v[168:171], v[204:207], v[140:143]\n"
        "s_barrier\n"
        "v_mfma_f32_16x16x32_bf16 v[140:143], v[172:175], v[220:223], v[140:143]\n"
        "ds_read_b64_tr_b16 v[172:173], v228 offset:40960\n"
        "global_load_lds_dwordx4 v238, s[68:69] offset:0\n"
        "s_add_u32 s78, s78, 0x10000\n"
        "v_mfma_f32_16x16x32_bf16 v[136:139], v[164:167], v[220:223], v[136:139]\n"
        "ds_read_b64_tr_b16 v[164:165], v226 offset:40960\n"
        "s_and_b32 s80, s40, 1\n"
        "s_lshl_b32 s80, s80, 11\n"
        "v_mfma_f32_16x16x32_bf16 v[136:139], v[160:163], v[204:207], v[136:139]\n"
        "ds_read_b64_tr_b16 v[160:161], v226 offset:32768\n"
        "s_and_b32 s79, s45, 1\n"
        "s_lshl_b32 s79, s79, 15\n"
        "v_mfma_f32_16x16x32_bf16 v[16:19], v[32:35], v[192:195], v[16:19]\n"
        "ds_read_b64_tr_b16 v[162:163], v227 offset:32768\n"
        "s_lshl_b32 s82, s40, 4\n"
        "s_sub_u32 s83, 14, s44\n"
        "v_mfma_f32_16x16x32_bf16 v[16:19], v[36:39], v[208:211], v[16:19]\n"
        "ds_read_b64_tr_b16 v[166:167], v227 offset:40960\n"
        "global_load_lds_dwordx4 v239, s[68:69] offset:1024\n"
        "s_lshl_b32 s50, s41, 7\n"
        "v_mfma_f32_16x16x32_bf16 v[20:23], v[44:47], v[208:211], v[20:23]\n"
        "ds_read_b64_tr_b16 v[174:175], v229 offset:40960\n"
        "s_lshl2_add_u32 s50, s42, s50\n"
        "s_add_u32 s50, s50, s40\n"
        "v_mfma_f32_16x16x32_bf16 v[20:23], v[40:43], v[192:195], v[20:23]\n"
        "ds_read_b64_tr_b16 v[168:169], v228 offset:32768\n"
        "s_lshl_b32 s50, s50, 12\n"
        "s_mul_i32 s51, s40, 8320\n"
        "v_mfma_f32_16x16x32_bf16 v[84:87], v[40:43], v[196:199], v[84:87]\n"
        "ds_read_b64_tr_b16 v[170:171], v229 offset:32768\n"
        "s_add_u32 s92, s26, s50\n"
        "s_addc_u32 s93, s27, 0\n"
        "v_mfma_f32_16x16x32_bf16 v[84:87], v[44:47], v[212:215], v[84:87]\n"
        "global_load_lds_dwordx4 v240, s[68:69] offset:2048\n"
        "s_add_u32 s50, s50, 0x3000000\n"
        "v_mfma_f32_16x16x32_bf16 v[80:83], v[36:39], v[212:215], v[80:83]\n"
        "s_add_u32 s94, s38, s50\n"
        "s_addc_u32 s95, s39, 0\n"
        "s_add_u32 s51, s51, 0x18000\n"
        "v_mfma_f32_16x16x32_bf16 v[80:83], v[32:35], v[196:199], v[80:83]\n"
        "s_add_u32 s50, s51, 4128\n"
        "v_bfe_u32 v176, v254, 4, 4\n"
        "v_lshrrev_b32 v177, 8, v254\n"
        "v_mfma_f32_16x16x32_bf16 v[112:115], v[32:35], v[200:203], v[112:115]\n"
        "v_lshrrev_b32 v178, 7, v254\n"
        "v_bfe_u32 v179, v254, 4, 3\n"
        "v_mfma_f32_16x16x32_bf16 v[112:115], v[36:39], v[216:219], v[112:115]\n"
        "global_load_lds_dwordx4 v241, s[68:69] offset:3072\n"
        "s_add_u32 m0, s72, 0x8000\n"
        "v_mfma_f32_16x16x32_bf16 v[116:119], v[44:47], v[216:219], v[116:119]\n"
        "s_add_u32 s68, s68, 0x80000\n"
        "s_addc_u32 s69, s69, 0\n"
        "v_and_b32 v180, 6, v178\n"
        "v_mfma_f32_16x16x32_bf16 v[116:119], v[40:43], v[200:203], v[116:119]\n"
        "v_xor_b32 v180, v180, v179\n"
        "v_lshlrev_b32 v180, 4, v180\n"
        "v_mfma_f32_16x16x32_bf16 v[148:151], v[40:43], v[204:207], v[148:151]\n"
        "ds_read_b64_tr_b16 v[40:41], v232 offset:32768\n"
        "v_lshl_add_u32 v181, v178, 11, v180\n"
        "v_mfma_f32_16x16x32_bf16 v[148:151], v[44:47], v[220:223], v[148:151]\n"
        "ds_read_b64_tr_b16 v[44:45], v232 offset:40960\n"
        "global_load_lds_dwordx4 v242, s[64:65] offset:0\n"
        "v_mfma_f32_16x16x32_bf16 v[144:147], v[36:39], v[220:223], v[144:147]\n"
        "ds_read_b64_tr_b16 v[36:37], v230 offset:40960\n"
        "v_xor_b32 v178, 4, v178\n"
        "v_mfma_f32_16x16x32_bf16 v[144:147], v[32:35], v[204:207], v[144:147]\n"
        "ds_read_b64_tr_b16 v[32:33], v230 offset:32768\n"
        "v_accvgpr_read_b32 v184, a92\n"
        "v_mfma_f32_16x16x32_bf16 v[24:27], v[48:51], v[192:195], v[24:27]\n"
        "ds_read_b64_tr_b16 v[34:35], v231 offset:32768\n"
        "v_cvt_pk_bf16_f32 v184, v184, v185\n"
        "v_mfma_f32_16x16x32_bf16 v[24:27], v[52:55], v[208:211], v[24:27]\n"
        "ds_read_b64_tr_b16 v[38:39], v231 offset:40960\n"
        "v_cvt_pk_bf16_f32 v185, v186, v187\n"
        "v_mfma_f32_16x16x32_bf16 v[28:31], v[60:63], v[208:211], v[28:31]\n"
        "global_store_dwordx4 v254, a[168:171], s[70:71] offset:0 sc1\n"
        "ds_read_b64_tr_b16 v[46:47], v233 offset:40960\n"
        "ds_read_b64_tr_b16 v[42:43], v233 offset:32768\n"
        "v_cvt_pk_bf16_f32 v186, v188, v189\n"
        "v_mfma_f32_16x16x32_bf16 v[28:31], v[56:59], v[192:195], v[28:31]\n"
        "ds_read_b128 v[192:195], v246 offset:2048\n"
        "v_cvt_pk_bf16_f32 v187, v190, v191\n"
        "v_mfma_f32_16x16x32_bf16 v[92:95], v[56:59], v[196:199], v[92:95]\n"
        "ds_read_b128 v[208:211], v247 offset:2048\n"
        "v_accvgpr_write_b32 a28, v184\n"
        "v_mfma_f32_16x16x32_bf16 v[92:95], v[60:63], v[212:215], v[92:95]\n"
        "v_accvgpr_write_b32 a29, v185\n"
        "v_accvgpr_write_b32 a30, v186\n"
        "v_mfma_f32_16x16x32_bf16 v[88:91], v[52:55], v[212:215], v[88:91]\n"
        "ds_read_b128 v[212:215], v247 offset:0\n"
        "v_accvgpr_write_b32 a31, v187\n"
        "v_mfma_f32_16x16x32_bf16 v[88:91], v[48:51], v[196:199], v[88:91]\n"
        "ds_read_b128 v[196:199], v246 offset:0\n"
        "global_load_lds_dwordx4 v243, s[64:65] offset:1024\n"
        "v_mfma_f32_16x16x32_bf16 v[120:123], v[48:51], v[200:203], v[120:123]\n"
        "v_lshl_add_u32 v178, v178, 11, v180\n"
        "v_add_u32 v182, 4096, v181\n"
        "v_mfma_f32_16x16x32_bf16 v[120:123], v[52:55], v[216:219], v[120:123]\n"
        "v_accvgpr_write_b32 a84, v182\n"
        "v_add_u32 v182, 3200, v181\n"
        "v_mfma_f32_16x16x32_bf16 v[124:127], v[60:63], v[216:219], v[124:127]\n"
        "ds_read_b128 v[216:219], v247 offset:6144\n"
        "v_accvgpr_write_b32 a85, v182\n"
        "v_mfma_f32_16x16x32_bf16 v[124:127], v[56:59], v[200:203], v[124:127]\n"
        "ds_read_b128 v[200:203], v246 offset:6144\n"
        "global_load_lds_dwordx4 v244, s[64:65] offset:2048\n"
        "v_mfma_f32_16x16x32_bf16 v[156:159], v[56:59], v[204:207], v[156:159]\n"
        "ds_read_b64_tr_b16 v[56:57], v228 offset:33792\n"
        "v_add_u32 v182, 18432, v178\n"
        "v_mfma_f32_16x16x32_bf16 v[156:159], v[60:63], v[220:223], v[156:159]\n"
        "ds_read_b64_tr_b16 v[60:61], v228 offset:41984\n"
        "v_accvgpr_write_b32 a86, v182\n"
        "v_mfma_f32_16x16x32_bf16 v[152:155], v[52:55], v[220:223], v[152:155]\n"
        "ds_read_b128 v[220:223], v247 offset:4096\n"
        "v_add_u32 v182, 17536, v178\n"
        "v_mfma_f32_16x16x32_bf16 v[152:155], v[48:51], v[204:207], v[152:155]\n"
        "ds_read_b128 v[204:207], v246 offset:4096\n"
        "s_waitcnt lgkmcnt(6)\n"
        "v_mfma_f32_16x16x32_bf16 v[0:3], v[160:163], v[192:195], v[0:3]\n"
        "ds_read_b64_tr_b16 v[48:49], v226 offset:33792\n"
        "global_load_lds_dwordx4 v245, s[64:65] offset:3072\n"
        "s_add_u32 m0, s72, 0x9000\n"
        "v_mfma_f32_16x16x32_bf16 v[0:3], v[164:167], v[208:211], v[0:3]\n"
        "ds_read_b64_tr_b16 v[50:51], v227 offset:33792\n"
        "s_add_u32 s64, s64, 0x80\n"
        "s_addc_u32 s65, s65, 0\n"
        "v_mfma_f32_16x16x32_bf16 v[4:7], v[172:175], v[208:211], v[4:7]\n"
        "ds_read_b64_tr_b16 v[52:53], v226 offset:41984\n"
        "v_accvgpr_write_b32 a87, v182\n"
        "v_mfma_f32_16x16x32_bf16 v[4:7], v[168:171], v[192:195], v[4:7]\n"
        "ds_read_b64_tr_b16 v[54:55], v227 offset:41984\n"
        "v_mov_b32 v178, s96\n"
        "v_mfma_f32_16x16x32_bf16 v[68:71], v[168:171], v[196:199], v[68:71]\n"
        "ds_read_b64_tr_b16 v[62:63], v229 offset:41984\n"
        "global_load_lds_dwordx4 v242, s[66:67] offset:0\n"
        "v_mfma_f32_16x16x32_bf16 v[68:71], v[172:175], v[212:215], v[68:71]\n"
        "ds_read_b64_tr_b16 v[58:59], v229 offset:33792\n"
        "v_add_u32 v180, s51, v254\n"
        "v_mfma_f32_16x16x32_bf16 v[64:67], v[164:167], v[212:215], v[64:67]\n"
        "s_waitcnt lgkmcnt(7)\n"
        "v_add_u32 v180, 4096, v180\n"
        "v_writelane_b32 v180, s96, 2\n"
        "v_mfma_f32_16x16x32_bf16 v[64:67], v[160:163], v[196:199], v[64:67]\n"
        "s_add_u32 s96, s96, 16\n"
        "v_writelane_b32 v180, s96, 3\n"
        "v_accvgpr_write_b32 a90, v180\n"
        "v_mfma_f32_16x16x32_bf16 v[96:99], v[160:163], v[200:203], v[96:99]\n"
        "global_load_lds_dwordx4 v243, s[66:67] offset:1024\n"
        "s_add_u32 s96, s51, 4098\n"
        "v_mfma_f32_16x16x32_bf16 v[96:99], v[164:167], v[216:219], v[96:99]\n"
        "s_waitcnt lgkmcnt(4)\n"
        "v_sub_u32 v179, s96, v178\n"
        "v_and_b32 v181, 1, v176\n"
        "v_mfma_f32_16x16x32_bf16 v[100:103], v[172:175], v[216:219], v[100:103]\n"
        "s_add_u32 s96, s51, 480\n"
        "v_mul_lo_u32 v179, v179, v181\n"
        "v_add_u32 v178, v178, v179\n"
        "v_mfma_f32_16x16x32_bf16 v[100:103], v[168:171], v[200:203], v[100:103]\n"
        "v_lshl_add_u32 v178, v176, 1, v178\n"
        "v_lshl_add_u32 v178, v177, 4, v178\n"
        "v_mfma_f32_16x16x32_bf16 v[132:135], v[168:171], v[204:207], v[132:135]\n"
        "ds_read_b64_tr_b16 v[168:169], v232 offset:33792\n"
        "global_load_lds_dwordx4 v244, s[66:67] offset:2048\n"
        "v_mfma_f32_16x16x32_bf16 v[132:135], v[172:175], v[220:223], v[132:135]\n"
        "ds_read_b64_tr_b16 v[172:173], v232 offset:41984\n"
        "v_accvgpr_write_b32 a88, v178\n"
        "v_mfma_f32_16x16x32_bf16 v[128:131], v[164:167], v[220:223], v[128:131]\n"
        "ds_read_b64_tr_b16 v[164:165], v230 offset:41984\n"
        "v_lshlrev_b32 v179, 5, v176\n"
        "v_mfma_f32_16x16x32_bf16 v[128:131], v[160:163], v[204:207], v[128:131]\n"
        "ds_read_b64_tr_b16 v[160:161], v230 offset:33792\n"
        "v_sub_u32 v179, s96, v179\n"
        "v_mfma_f32_16x16x32_bf16 v[8:11], v[32:35], v[192:195], v[8:11]\n"
        "ds_read_b64_tr_b16 v[162:163], v231 offset:33792\n"
        "global_load_lds_dwordx4 v245, s[66:67] offset:3072\n"
        "s_add_u32 m0, s73, 0x20000\n"
        "v_mfma_f32_16x16x32_bf16 v[8:11], v[36:39], v[208:211], v[8:11]\n"
        "ds_read_b64_tr_b16 v[166:167], v231 offset:41984\n"
        "s_add_u32 s66, s66, 0x80\n"
        "s_addc_u32 s67, s67, 0\n"
        "v_mfma_f32_16x16x32_bf16 v[12:15], v[44:47], v[208:211], v[12:15]\n"
        "ds_read_b64_tr_b16 v[174:175], v233 offset:41984\n"
        "s_lshl_b32 s96, s54, 16\n"
        "v_lshl_add_u32 v179, v177, 4, v179\n"
        "v_mfma_f32_16x16x32_bf16 v[12:15], v[40:43], v[192:195], v[12:15]\n"
        "ds_read_b64_tr_b16 v[170:171], v233 offset:33792\n"
        "s_and_b32 s54, s54, 0xffff0000\n"
        "v_accvgpr_write_b32 a89, v179\n"
        "v_mfma_f32_16x16x32_bf16 v[76:79], v[40:43], v[196:199], v[76:79]\n"
        "v_mov_b32 v181, 0\n"
        "v_writelane_b32 v181, s96, 3\n"
        "v_mfma_f32_16x16x32_bf16 v[76:79], v[44:47], v[212:215], v[76:79]\n"
        "s_lshr_b32 s96, s40, 1\n"
        "v_accvgpr_write_b32 a91, v181\n"
        "s_lshl_b32 s96, s96, 10\n"
        "v_mfma_f32_16x16x32_bf16 v[72:75], v[36:39], v[212:215], v[72:75]\n"
        "v_lshrrev_b32 v178, 1, v177\n"
        "v_lshlrev_b32 v179, 11, v178\n"
        "v_mfma_f32_16x16x32_bf16 v[72:75], v[32:35], v[196:199], v[72:75]\n"
        "s_waitcnt lgkmcnt(4)\n"
        "v_add_u32 v179, s96, v179\n"
        "v_bfe_u32 v180, v254, 6, 3\n"
        "v_mfma_f32_16x16x32_bf16 v[104:107], v[32:35], v[200:203], v[104:107]\n"
        "s_and_b32 s96, s40, 1\n"
        "v_bfe_u32 v181, v254, 4, 2\n"
        "v_lshlrev_b32 v183, 2, v178\n"
        "v_mfma_f32_16x16x32_bf16 v[104:107], v[36:39], v[216:219], v[104:107]\n"
        "s_lshl_b32 s96, s96, 6\n"
        "v_xor_b32 v183, v183, v180\n"
        "v_bfe_u32 v184, v183, 1, 2\n"
        "v_mfma_f32_16x16x32_bf16 v[108:111], v[44:47], v[216:219], v[108:111]\n"
        "v_xor_b32 v184, 0, v184\n"
        "v_lshlrev_b32 v184, 5, v184\n"
        "v_mfma_f32_16x16x32_bf16 v[108:111], v[40:43], v[200:203], v[108:111]\n"
        "s_waitcnt vmcnt(13) lgkmcnt(0)\n"
        "v_lshl_add_u32 v183, v183, 7, v184\n"
        "v_lshl_add_u32 v183, v181, 3, v183\n"
        "v_mfma_f32_16x16x32_bf16 v[140:143], v[40:43], v[204:207], v[140:143]\n"
        "LHA9_%=:\n"
        "s_cbranch_execz LHRA_%=\n"
        "s_barrier\n"
        "v_mfma_f32_16x16x32_bf16 v[140:143], v[44:47], v[220:223], v[140:143]\n"
        "ds_read_b64_tr_b16 v[44:45], v228 offset:57344\n"
        "global_load_lds_dwordx4 v238, s[68:69] offset:0\n"
        "v_mfma_f32_16x16x32_bf16 v[136:139], v[36:39], v[220:223], v[136:139]\n"
        "ds_read_b64_tr_b16 v[36:37], v226 offset:57344\n"
        "v_add_u32 v182, v183, v179\n"
        "v_mfma_f32_16x16x32_bf16 v[136:139], v[32:35], v[204:207], v[136:139]\n"
        "ds_read_b64_tr_b16 v[32:33], v226 offset:49152\n"
        "v_xor_b32 v182, s96, v182\n"
        "v_mfma_f32_16x16x32_bf16 v[16:19], v[48:51], v[192:195], v[16:19]\n"
        "ds_read_b64_tr_b16 v[34:35], v227 offset:49152\n"
        "v_accvgpr_write_b32 a92, v182\n"
        "v_mfma_f32_16x16x32_bf16 v[16:19], v[52:55], v[208:211], v[16:19]\n"
        "ds_read_b64_tr_b16 v[38:39], v227 offset:57344\n"
        "global_load_lds_dwordx4 v239, s[68:69] offset:1024\n"
        "v_mfma_f32_16x16x32_bf16 v[20:23], v[60:63], v[208:211], v[20:23]\n"
        "ds_read_b64_tr_b16 v[46:47], v229 offset:57344\n"
        "v_mfma_f32_16x16x32_bf16 v[20:23], v[56:59], v[192:195], v[20:23]\n"
        "ds_read_b64_tr_b16 v[40:41], v228 offset:49152\n"
        "v_mfma_f32_16x16x32_bf16 v[84:87], v[56:59], v[196:199], v[84:87]\n"
        "ds_read_b64_tr_b16 v[42:43], v229 offset:49152\n"
        "v_mfma_f32_16x16x32_bf16 v[84:87], v[60:63], v[212:215], v[84:87]\n"
        "global_load_lds_dwordx4 v240, s[68:69] offset:2048\n"
        "v_mfma_f32_16x16x32_bf16 v[80:83], v[52:55], v[212:215], v[80:83]\n"
        "v_add_u32 v240, s78, v254\n"
        "s_xor_b32 s78, s78, 0x8000\n"
        "v_mfma_f32_16x16x32_bf16 v[80:83], v[48:51], v[196:199], v[80:83]\n"
        "v_mfma_f32_16x16x32_bf16 v[112:115], v[48:51], v[200:203], v[112:115]\n"
        "v_mfma_f32_16x16x32_bf16 v[112:115], v[52:55], v[216:219], v[112:115]\n"
        "global_load_lds_dwordx4 v241, s[68:69] offset:3072\n"
        "s_add_u32 m0, s72, 0x0\n"
        "v_mfma_f32_16x16x32_bf16 v[116:119], v[60:63], v[216:219], v[116:119]\n"
        "v_add_u32 v241, s79, v254\n"
        "v_mfma_f32_16x16x32_bf16 v[116:119], v[56:59], v[200:203], v[116:119]\n"
        "v_mfma_f32_16x16x32_bf16 v[148:151], v[56:59], v[204:207], v[148:151]\n"
        "ds_read_b64_tr_b16 v[56:57], v232 offset:49152\n"
        "v_mfma_f32_16x16x32_bf16 v[148:151], v[60:63], v[220:223], v[148:151]\n"
        "ds_read_b64_tr_b16 v[60:61], v232 offset:57344\n"
        "global_load_lds_dwordx4 v242, s[64:65] offset:0\n"
        "v_mfma_f32_16x16x32_bf16 v[144:147], v[52:55], v[220:223], v[144:147]\n"
        "ds_read_b64_tr_b16 v[52:53], v230 offset:57344\n"
        "v_mfma_f32_16x16x32_bf16 v[144:147], v[48:51], v[204:207], v[144:147]\n"
        "ds_read_b64_tr_b16 v[48:49], v230 offset:49152\n"
        "v_mfma_f32_16x16x32_bf16 v[24:27], v[160:163], v[192:195], v[24:27]\n"
        "ds_read_b64_tr_b16 v[50:51], v231 offset:49152\n"
        "v_mfma_f32_16x16x32_bf16 v[24:27], v[164:167], v[208:211], v[24:27]\n"
        "ds_read_b64_tr_b16 v[54:55], v231 offset:57344\n"
        "global_load_lds_dwordx4 v243, s[64:65] offset:1024\n"
        "v_mfma_f32_16x16x32_bf16 v[28:31], v[172:175], v[208:211], v[28:31]\n"
        "ds_read_b64_tr_b16 v[62:63], v233 offset:57344\n"
        "v_mfma_f32_16x16x32_bf16 v[28:31], v[168:171], v[192:195], v[28:31]\n"
        "ds_read_b64_tr_b16 v[58:59], v233 offset:49152\n"
        "v_mfma_f32_16x16x32_bf16 v[92:95], v[168:171], v[196:199], v[92:95]\n"
        "ds_read_b128 v[192:195], v248 offset:2048\n"
        "v_mfma_f32_16x16x32_bf16 v[92:95], v[172:175], v[212:215], v[92:95]\n"
        "ds_read_b128 v[208:211], v249 offset:2048\n"
        "global_load_lds_dwordx4 v244, s[64:65] offset:2048\n"
        "v_mfma_f32_16x16x32_bf16 v[88:91], v[164:167], v[212:215], v[88:91]\n"
        "ds_read_b128 v[212:215], v249 offset:0\n"
        "v_mfma_f32_16x16x32_bf16 v[88:91], v[160:163], v[196:199], v[88:91]\n"
        "ds_read_b128 v[196:199], v248 offset:0\n"
        "v_mfma_f32_16x16x32_bf16 v[120:123], v[160:163], v[200:203], v[120:123]\n"
        "v_mfma_f32_16x16x32_bf16 v[120:123], v[164:167], v[216:219], v[120:123]\n"
        "global_load_lds_dwordx4 v245, s[64:65] offset:3072\n"
        "s_add_u32 m0, s72, 0x1000\n"
        "v_mfma_f32_16x16x32_bf16 v[124:127], v[172:175], v[216:219], v[124:127]\n"
        "ds_read_b128 v[216:219], v249 offset:6144\n"
        "s_add_u32 s64, s38, s90\n"
        "s_addc_u32 s65, s39, 0\n"
        "v_mfma_f32_16x16x32_bf16 v[124:127], v[168:171], v[200:203], v[124:127]\n"
        "ds_read_b128 v[200:203], v248 offset:6144\n"
        "s_lshl_b32 s90, s40, 14\n"
        "s_and_b32 s90, s90, 0x8000\n"
        "v_mfma_f32_16x16x32_bf16 v[156:159], v[168:171], v[204:207], v[156:159]\n"
        "ds_read_b64_tr_b16 v[168:169], v228 offset:50176\n"
        "s_xor_b32 s91, s78, s90\n"
        "s_add_u32 s91, s91, s80\n"
        "v_mfma_f32_16x16x32_bf16 v[156:159], v[172:175], v[220:223], v[156:159]\n"
        "ds_read_b64_tr_b16 v[172:173], v228 offset:58368\n"
        "global_load_lds_dwordx4 v242, s[66:67] offset:0\n"
        "s_xor_b32 s90, s90, s79\n"
        "v_mfma_f32_16x16x32_bf16 v[152:155], v[164:167], v[220:223], v[152:155]\n"
        "ds_read_b128 v[220:223], v249 offset:4096\n"
        "s_add_u32 s78, s78, s72\n"
        "v_lshrrev_b32 v242, 6, v254\n"
        "v_mfma_f32_16x16x32_bf16 v[152:155], v[160:163], v[204:207], v[152:155]\n"
        "ds_read_b128 v[204:207], v248 offset:4096\n"
        "s_add_u32 s79, s79, s72\n"
        "s_waitcnt lgkmcnt(6)\n"
        "v_mfma_f32_16x16x32_bf16 v[0:3], v[32:35], v[192:195], v[0:3]\n"
        "ds_read_b64_tr_b16 v[160:161], v226 offset:50176\n"
        "s_xor_b32 s79, s79, 0x8000\n"
        "v_and_b32 v242, 12, v242\n"
        "v_mfma_f32_16x16x32_bf16 v[0:3], v[36:39], v[208:211], v[0:3]\n"
        "ds_read_b64_tr_b16 v[162:163], v227 offset:50176\n"
        "global_load_lds_dwordx4 v243, s[66:67] offset:1024\n"
        "v_mfma_f32_16x16x32_bf16 v[4:7], v[44:47], v[208:211], v[4:7]\n"
        "ds_read_b64_tr_b16 v[164:165], v226 offset:58368\n"
        "v_bfe_u32 v243, v254, 4, 4\n"
        "v_mfma_f32_16x16x32_bf16 v[4:7], v[40:43], v[192:195], v[4:7]\n"
        "ds_read_b64_tr_b16 v[166:167], v227 offset:58368\n"
        "v_sub_u32 v243, v243, v242\n"
        "v_mfma_f32_16x16x32_bf16 v[68:71], v[40:43], v[196:199], v[68:71]\n"
        "ds_read_b64_tr_b16 v[174:175], v229 offset:58368\n"
        "v_add_u32 v243, s82, v243\n"
        "v_mfma_f32_16x16x32_bf16 v[68:71], v[44:47], v[212:215], v[68:71]\n"
        "ds_read_b64_tr_b16 v[170:171], v229 offset:50176\n"
        "global_load_lds_dwordx4 v244, s[66:67] offset:2048\n"
        "s_mov_b32 s82, -1\n"
        "v_mfma_f32_16x16x32_bf16 v[64:67], v[36:39], v[212:215], v[64:67]\n"
        "s_waitcnt lgkmcnt(7)\n"
        "v_add_u32 v244, s91, v254\n"
        "s_lshl1_add_u32 s91, s80, s90\n"
        "v_mfma_f32_16x16x32_bf16 v[64:67], v[32:35], v[196:199], v[64:67]\n"
        "s_waitcnt lgkmcnt(6)\n"
        "v_cmp_gt_i32 s[10:11], 0, v243\n"
        "v_cmp_gt_i32 s[12:13], 1, v243\n"
        "v_mfma_f32_16x16x32_bf16 v[96:99], v[32:35], v[200:203], v[96:99]\n"
        "v_cmp_gt_i32 s[14:15], 2, v243\n"
        "v_cmp_gt_i32 s[16:17], 3, v243\n"
        "v_mfma_f32_16x16x32_bf16 v[96:99], v[36:39], v[216:219], v[96:99]\n"
        "global_load_lds_dwordx4 v245, s[66:67] offset:3072\n"
        "v_mfma_f32_16x16x32_bf16 v[100:103], v[44:47], v[216:219], v[100:103]\n"
        "s_add_u32 s66, s64, 0x1000000\n"
        "s_addc_u32 s67, s65, 0\n"
        "v_add_u32 v245, s91, v254\n"
        "v_mfma_f32_16x16x32_bf16 v[100:103], v[40:43], v[200:203], v[100:103]\n"
        "v_cmp_gt_i32 s[20:21], 16, v243\n"
        "v_cmp_gt_i32 s[22:23], 17, v243\n"
        "v_mfma_f32_16x16x32_bf16 v[132:135], v[40:43], v[204:207], v[132:135]\n"
        "ds_read_b64_tr_b16 v[40:41], v232 offset:50176\n"
        "v_cmp_gt_i32 s[24:25], 18, v243\n"
        "v_mfma_f32_16x16x32_bf16 v[132:135], v[44:47], v[220:223], v[132:135]\n"
        "ds_read_b64_tr_b16 v[44:45], v232 offset:58368\n"
        "v_cmp_gt_i32 s[28:29], 19, v243\n"
        "v_mfma_f32_16x16x32_bf16 v[128:131], v[36:39], v[220:223], v[128:131]\n"
        "global_store_dwordx4 v254, a[172:175], s[70:71] offset:1024 sc1\n"
        "ds_read_b64_tr_b16 v[36:37], v230 offset:58368\n"
        "ds_read_b64_tr_b16 v[38:39], v231 offset:58368\n"
        "s_add_u32 s70, s38, s97\n"
        "s_addc_u32 s71, s39, 0\n"
        "v_mfma_f32_16x16x32_bf16 v[128:131], v[32:35], v[204:207], v[128:131]\n"
        "ds_read_b64_tr_b16 v[32:33], v230 offset:50176\n"
        "s_add_u32 s97, s52, 0x4000\n"
        "v_cmp_gt_i32 s[30:31], 32, v243\n"
        "v_mfma_f32_16x16x32_bf16 v[8:11], v[48:51], v[192:195], v[8:11]\n"
        "ds_read_b64_tr_b16 v[34:35], v231 offset:50176\n"
        "v_cmp_gt_i32 s[56:57], 33, v243\n"
        "v_mfma_f32_16x16x32_bf16 v[8:11], v[52:55], v[208:211], v[8:11]\n"
        "ds_read_b64_tr_b16 v[46:47], v233 offset:58368\n"
        "v_cmp_gt_i32 s[58:59], 34, v243\n"
        "v_mfma_f32_16x16x32_bf16 v[12:15], v[60:63], v[208:211], v[12:15]\n"
        "ds_read_b64_tr_b16 v[42:43], v233 offset:50176\n"
        "v_cmp_gt_i32 s[60:61], 35, v243\n"
        "v_mfma_f32_16x16x32_bf16 v[12:15], v[56:59], v[192:195], v[12:15]\n"
        "global_store_dwordx4 v254, a[0:3], s[70:71] offset:0 sc1\n"
        "v_cmp_gt_i32 s[62:63], 48, v243\n"
        "v_cmp_gt_i32 s[84:85], 49, v243\n"
        "v_cmp_gt_i32 s[86:87], 50, v243\n"
        "v_cmp_gt_i32 s[88:89], 51, v243\n"
        "v_mfma_f32_16x16x32_bf16 v[76:79], v[56:59], v[196:199], v[76:79]\n"
        "v_mov_b32 v242, 0x3f803f80\n"
        "v_accvgpr_write_b32 a80, v242\n"
        "v_mfma_f32_16x16x32_bf16 v[76:79], v[60:63], v[212:215], v[76:79]\n"
        "s_waitcnt lgkmcnt(4)\n"
        "v_accvgpr_write_b32 a81, v242\n"
        "v_accvgpr_write_b32 a82, v242\n"
        "v_mfma_f32_16x16x32_bf16 v[72:75], v[52:55], v[212:215], v[72:75]\n"
        "v_accvgpr_write_b32 a83, v242\n"
        "v_mov_b32 v242, 0xff800000\n"
        "v_mfma_f32_16x16x32_bf16 v[72:75], v[48:51], v[196:199], v[72:75]\n"
        "v_xor_b32 v241, 0x8000, v241\n"
        "v_mfma_f32_16x16x32_bf16 v[104:107], v[48:51], v[200:203], v[104:107]\n"
        "v_mfma_f32_16x16x32_bf16 v[104:107], v[52:55], v[216:219], v[104:107]\n"
        "s_waitcnt vmcnt(14) lgkmcnt(0)\n"
        "v_mfma_f32_16x16x32_bf16 v[108:111], v[60:63], v[216:219], v[108:111]\n"
        "s_barrier\n"
        "v_mfma_f32_16x16x32_bf16 v[108:111], v[56:59], v[200:203], v[108:111]\n"
        "v_mfma_f32_16x16x32_bf16 v[140:143], v[56:59], v[204:207], v[140:143]\n"
        "ds_read_b64_tr_b16 v[56:57], v228 offset:16384\n"
        "v_mfma_f32_16x16x32_bf16 v[140:143], v[60:63], v[220:223], v[140:143]\n"
        "ds_read_b64_tr_b16 v[60:61], v228 offset:24576\n"
        "v_mfma_f32_16x16x32_bf16 v[136:139], v[52:55], v[220:223], v[136:139]\n"
        "ds_read_b64_tr_b16 v[52:53], v226 offset:24576\n"
        "v_mfma_f32_16x16x32_bf16 v[136:139], v[48:51], v[204:207], v[136:139]\n"
        "global_store_dwordx4 v254, a[4:7], s[70:71] offset:1024 sc1\n"
        "ds_read_b64_tr_b16 v[48:49], v226 offset:16384\n"
        "ds_read_b64_tr_b16 v[50:51], v227 offset:16384\n"
        "s_add_u32 s70, s38, s97\n"
        "s_addc_u32 s71, s39, 0\n"
        "v_mfma_f32_16x16x32_bf16 v[16:19], v[160:163], v[192:195], v[16:19]\n"
        "ds_read_b64_tr_b16 v[54:55], v227 offset:24576\n"
        "s_add_u32 s97, s53, 0x6000\n"
        "v_mfma_f32_16x16x32_bf16 v[16:19], v[164:167], v[208:211], v[16:19]\n"
        "ds_read_b64_tr_b16 v[62:63], v229 offset:24576\n"
        "v_mfma_f32_16x16x32_bf16 v[20:23], v[172:175], v[208:211], v[20:23]\n"
        "ds_read_b64_tr_b16 v[58:59], v229 offset:16384\n"
        "v_mfma_f32_16x16x32_bf16 v[20:23], v[168:171], v[192:195], v[20:23]\n"
        "v_mfma_f32_16x16x32_bf16 v[84:87], v[168:171], v[196:199], v[84:87]\n"
        "v_mfma_f32_16x16x32_bf16 v[84:87], v[172:175], v[212:215], v[84:87]\n"
        "v_mfma_f32_16x16x32_bf16 v[80:83], v[164:167], v[212:215], v[80:83]\n"
        "v_mfma_f32_16x16x32_bf16 v[80:83], v[160:163], v[196:199], v[80:83]\n"
        "v_mfma_f32_16x16x32_bf16 v[112:115], v[160:163], v[200:203], v[112:115]\n"
        "v_mfma_f32_16x16x32_bf16 v[112:115], v[164:167], v[216:219], v[112:115]\n"
        "v_mfma_f32_16x16x32_bf16 v[116:119], v[172:175], v[216:219], v[116:119]\n"
        "v_mfma_f32_16x16x32_bf16 v[116:119], v[168:171], v[200:203], v[116:119]\n"
        "global_store_dwordx4 v254, a[8:11], s[70:71] offset:0 sc1\n"
        "v_mfma_f32_16x16x32_bf16 v[148:151], v[168:171], v[204:207], v[148:151]\n"
        "ds_read_b64_tr_b16 v[168:169], v232 offset:16384\n"
        "v_mfma_f32_16x16x32_bf16 v[148:151], v[172:175], v[220:223], v[148:151]\n"
        "ds_read_b64_tr_b16 v[172:173], v232 offset:24576\n"
        "v_mfma_f32_16x16x32_bf16 v[144:147], v[164:167], v[220:223], v[144:147]\n"
        "ds_read_b64_tr_b16 v[164:165], v230 offset:24576\n"
        "v_mfma_f32_16x16x32_bf16 v[144:147], v[160:163], v[204:207], v[144:147]\n"
        "ds_read_b64_tr_b16 v[160:161], v230 offset:16384\n"
        "v_mfma_f32_16x16x32_bf16 v[24:27], v[32:35], v[192:195], v[24:27]\n"
        "ds_read_b64_tr_b16 v[162:163], v231 offset:16384\n"
        "v_mfma_f32_16x16x32_bf16 v[24:27], v[36:39], v[208:211], v[24:27]\n"
        "ds_read_b64_tr_b16 v[166:167], v231 offset:24576\n"
        "v_mfma_f32_16x16x32_bf16 v[28:31], v[44:47], v[208:211], v[28:31]\n"
        "ds_read_b64_tr_b16 v[174:175], v233 offset:24576\n"
        "v_mfma_f32_16x16x32_bf16 v[28:31], v[40:43], v[192:195], v[28:31]\n"
        "ds_read_b64_tr_b16 v[170:171], v233 offset:16384\n"
        "v_mfma_f32_16x16x32_bf16 v[92:95], v[40:43], v[196:199], v[92:95]\n"
        "ds_read_b128 v[192:195], v246 offset:34816\n"
        "v_mfma_f32_16x16x32_bf16 v[92:95], v[44:47], v[212:215], v[92:95]\n"
        "ds_read_b128 v[208:211], v247 offset:34816\n"
        "v_mfma_f32_16x16x32_bf16 v[88:91], v[36:39], v[212:215], v[88:91]\n"
        "ds_read_b128 v[212:215], v247 offset:32768\n"
        "v_mfma_f32_16x16x32_bf16 v[88:91], v[32:35], v[196:199], v[88:91]\n"
        "ds_read_b128 v[196:199], v246 offset:32768\n"
        "v_mfma_f32_16x16x32_bf16 v[120:123], v[32:35], v[200:203], v[120:123]\n"
        "global_store_dwordx4 v254, a[12:15], s[70:71] offset:1024 sc1\n"
        "s_add_u32 s70, s38, s97\n"
        "s_addc_u32 s71, s39, 0\n"
        "s_add_u32 s97, s53, 0x4000\n"
        "v_mfma_f32_16x16x32_bf16 v[120:123], v[36:39], v[216:219], v[120:123]\n"
        "v_mfma_f32_16x16x32_bf16 v[124:127], v[44:47], v[216:219], v[124:127]\n"
        "ds_read_b128 v[216:219], v247 offset:38912\n"
        "s_waitcnt lgkmcnt(5)\n"
        "v_mfma_f32_16x16x32_bf16 v[124:127], v[40:43], v[200:203], v[124:127]\n"
        "ds_read_b128 v[200:203], v246 offset:38912\n"
        "s_waitcnt lgkmcnt(5)\n"
        "v_mfma_f32_16x16x32_bf16 v[156:159], v[40:43], v[204:207], v[156:159]\n"
        "ds_read_b64_tr_b16 v[40:41], v228 offset:17408\n"
        "s_waitcnt lgkmcnt(5)\n"
        "v_mfma_f32_16x16x32_bf16 v[156:159], v[44:47], v[220:223], v[156:159]\n"
        "ds_read_b64_tr_b16 v[44:45], v228 offset:25600\n"
        "s_waitcnt lgkmcnt(5)\n"
        "v_mfma_f32_16x16x32_bf16 v[152:155], v[36:39], v[220:223], v[152:155]\n"
        "ds_read_b128 v[220:223], v247 offset:36864\n"
        "s_waitcnt lgkmcnt(5)\n"
        "v_mfma_f32_16x16x32_bf16 v[152:155], v[32:35], v[204:207], v[152:155]\n"
        "ds_read_b128 v[204:207], v246 offset:36864\n"
        "v_mfma_f32_16x16x32_bf16 v[0:3], v[48:51], v[192:195], v[0:3]\n"
        "ds_read_b64_tr_b16 v[32:33], v226 offset:17408\n"
        "v_mfma_f32_16x16x32_bf16 v[0:3], v[52:55], v[208:211], v[0:3]\n"
        "ds_read_b64_tr_b16 v[34:35], v227 offset:17408\n"
        "v_mfma_f32_16x16x32_bf16 v[4:7], v[60:63], v[208:211], v[4:7]\n"
        "ds_read_b64_tr_b16 v[36:37], v226 offset:25600\n"
        "s_waitcnt lgkmcnt(8)\n"
        "v_mfma_f32_16x16x32_bf16 v[4:7], v[56:59], v[192:195], v[4:7]\n"
        "ds_read_b64_tr_b16 v[38:39], v227 offset:25600\n"
        "s_waitcnt lgkmcnt(8)\n"
        "v_mfma_f32_16x16x32_bf16 v[68:71], v[56:59], v[196:199], v[68:71]\n"
        "ds_read_b64_tr_b16 v[46:47], v229 offset:25600\n"
        "v_mfma_f32_16x16x32_bf16 v[68:71], v[60:63], v[212:215], v[68:71]\n"
        "ds_read_b64_tr_b16 v[42:43], v229 offset:17408\n"
        "s_mul_i32 s80, s40, 4224\n"
        "s_waitcnt lgkmcnt(8)\n"
        "v_mfma_f32_16x16x32_bf16 v[64:67], v[52:55], v[212:215], v[64:67]\n"
        "global_store_dwordx4 v254, a[208:211], s[70:71] offset:0 sc1\n"
        "s_add_u32 s68, s92, 0x2\n"
        "s_addc_u32 s69, s93, 0\n"
        "s_add_u32 m0, s80, 0x20260\n"
        "s_lshl_b32 s80, s41, 5\n"
        "s_add_u32 s80, s80, 256\n"
        "v_mfma_f32_16x16x32_bf16 v[64:67], v[48:51], v[196:199], v[64:67]\n"
        "s_add_u32 s81, s80, s42\n"
        "s_lshl_b32 s81, s81, 7\n"
        "s_and_b32 s80, s81, 0xfffffc7f\n"
        "v_mfma_f32_16x16x32_bf16 v[96:99], v[48:51], v[200:203], v[96:99]\n"
        "s_waitcnt lgkmcnt(5)\n"
        "v_mfma_f32_16x16x32_bf16 v[96:99], v[52:55], v[216:219], v[96:99]\n"
        "v_mfma_f32_16x16x32_bf16 v[100:103], v[60:63], v[216:219], v[100:103]\n"
        "v_mfma_f32_16x16x32_bf16 v[100:103], v[56:59], v[200:203], v[100:103]\n"
        "v_mfma_f32_16x16x32_bf16 v[132:135], v[56:59], v[204:207], v[132:135]\n"
        "ds_read_b64_tr_b16 v[56:57], v232 offset:17408\n"
        "v_mfma_f32_16x16x32_bf16 v[132:135], v[60:63], v[220:223], v[132:135]\n"
        "ds_read_b64_tr_b16 v[60:61], v232 offset:25600\n"
        "v_mfma_f32_16x16x32_bf16 v[128:131], v[52:55], v[220:223], v[128:131]\n"
        "ds_read_b64_tr_b16 v[52:53], v230 offset:25600\n"
        "v_mfma_f32_16x16x32_bf16 v[128:131], v[48:51], v[204:207], v[128:131]\n"
        "global_store_dwordx4 v254, a[240:243], s[70:71] offset:1024 sc1\n"
        "ds_read_b64_tr_b16 v[48:49], v230 offset:17408\n"
        "ds_read_b64_tr_b16 v[50:51], v231 offset:17408\n"
        "v_mfma_f32_16x16x32_bf16 v[8:11], v[160:163], v[192:195], v[8:11]\n"
        "ds_read_b64_tr_b16 v[54:55], v231 offset:25600\n"
        "v_mfma_f32_16x16x32_bf16 v[8:11], v[164:167], v[208:211], v[8:11]\n"
        "ds_read_b64_tr_b16 v[62:63], v233 offset:25600\n"
        "v_mfma_f32_16x16x32_bf16 v[12:15], v[172:175], v[208:211], v[12:15]\n"
        "ds_read_b64_tr_b16 v[58:59], v233 offset:17408\n"
        "v_mfma_f32_16x16x32_bf16 v[12:15], v[168:171], v[192:195], v[12:15]\n"
        "v_mfma_f32_16x16x32_bf16 v[76:79], v[168:171], v[196:199], v[76:79]\n"
        "v_mfma_f32_16x16x32_bf16 v[76:79], v[172:175], v[212:215], v[76:79]\n"
        "s_waitcnt lgkmcnt(6)\n"
        "v_mfma_f32_16x16x32_bf16 v[72:75], v[164:167], v[212:215], v[72:75]\n"
        "v_mfma_f32_16x16x32_bf16 v[72:75], v[160:163], v[196:199], v[72:75]\n"
        "v_mfma_f32_16x16x32_bf16 v[104:107], v[160:163], v[200:203], v[104:107]\n"
        "v_mfma_f32_16x16x32_bf16 v[104:107], v[164:167], v[216:219], v[104:107]\n"
        "v_mfma_f32_16x16x32_bf16 v[108:111], v[172:175], v[216:219], v[108:111]\n"
        "s_waitcnt vmcnt(7) lgkmcnt(0)\n"
        "v_mfma_f32_16x16x32_bf16 v[108:111], v[168:171], v[200:203], v[108:111]\n"
        "global_store_dwordx4 v254, a[212:215], s[70:71] offset:2048 sc1\n"
        "s_barrier\n"
        "v_mfma_f32_16x16x32_bf16 v[140:143], v[168:171], v[204:207], v[140:143]\n"
        "ds_write_b128 v244, a[224:227] offset:12288\n"
        "v_mfma_f32_16x16x32_bf16 v[140:143], v[172:175], v[220:223], v[140:143]\n"
        "ds_read_b64_tr_b16 v[172:173], v228 offset:40960\n"
        "v_mfma_f32_16x16x32_bf16 v[136:139], v[164:167], v[220:223], v[136:139]\n"
        "ds_read_b64_tr_b16 v[164:165], v226 offset:40960\n"
        "v_mfma_f32_16x16x32_bf16 v[136:139], v[160:163], v[204:207], v[136:139]\n"
        "ds_read_b64_tr_b16 v[160:161], v226 offset:32768\n"
        "global_load_dword v253, v252, s[46:47] offset:0\n"
        "v_mfma_f32_16x16x32_bf16 v[16:19], v[32:35], v[192:195], v[16:19]\n"
        "ds_read_b64_tr_b16 v[162:163], v227 offset:32768\n"
        "v_lshrrev_b32 v252, 2, v254\n"
        "v_mfma_f32_16x16x32_bf16 v[16:19], v[36:39], v[208:211], v[16:19]\n"
        "ds_read_b64_tr_b16 v[166:167], v227 offset:40960\n"
        "v_mfma_f32_16x16x32_bf16 v[20:23], v[44:47], v[208:211], v[20:23]\n"
        "ds_read_b64_tr_b16 v[174:175], v229 offset:40960\n"
        "v_mfma_f32_16x16x32_bf16 v[20:23], v[40:43], v[192:195], v[20:23]\n"
        "ds_read_b64_tr_b16 v[168:169], v228 offset:32768\n"
        "v_mfma_f32_16x16x32_bf16 v[84:87], v[40:43], v[196:199], v[84:87]\n"
        "ds_read_b64_tr_b16 v[170:171], v229 offset:32768\n"
        "v_mfma_f32_16x16x32_bf16 v[84:87], v[44:47], v[212:215], v[84:87]\n"
        "ds_write_b128 v244, a[228:231] offset:13312\n"
        "v_mfma_f32_16x16x32_bf16 v[80:83], v[36:39], v[212:215], v[80:83]\n"
        "ds_write_b128 v244, a[232:235] offset:8192\n"
        "v_mfma_f32_16x16x32_bf16 v[80:83], v[32:35], v[196:199], v[80:83]\n"
        "global_store_dwordx4 v254, a[244:247], s[70:71] offset:3072 sc1\n"
        "s_add_u32 s70, s38, s97\n"
        "s_addc_u32 s71, s39, 0\n"
        "s_add_u32 s97, s53, 0x0\n"
        "v_mfma_f32_16x16x32_bf16 v[112:115], v[32:35], v[200:203], v[112:115]\n"
        "ds_write_b128 v244, a[236:239] offset:9216\n"
        "v_mfma_f32_16x16x32_bf16 v[112:115], v[36:39], v[216:219], v[112:115]\n"
        "ds_write_b128 v244, a[192:195] offset:4096\n"
        "v_mfma_f32_16x16x32_bf16 v[116:119], v[44:47], v[216:219], v[116:119]\n"
        "ds_write_b128 v244, a[196:199] offset:5120\n"
        "v_mfma_f32_16x16x32_bf16 v[116:119], v[40:43], v[200:203], v[116:119]\n"
        "ds_write_b128 v244, a[200:203] offset:16384\n"
        "v_mfma_f32_16x16x32_bf16 v[148:151], v[40:43], v[204:207], v[148:151]\n"
        "ds_read_b64_tr_b16 v[40:41], v232 offset:32768\n"
        "v_mfma_f32_16x16x32_bf16 v[148:151], v[44:47], v[220:223], v[148:151]\n"
        "ds_read_b64_tr_b16 v[44:45], v232 offset:40960\n"
        "v_mfma_f32_16x16x32_bf16 v[144:147], v[36:39], v[220:223], v[144:147]\n"
        "ds_read_b64_tr_b16 v[36:37], v230 offset:40960\n"
        "v_mfma_f32_16x16x32_bf16 v[144:147], v[32:35], v[204:207], v[144:147]\n"
        "ds_read_b64_tr_b16 v[32:33], v230 offset:32768\n"
        "v_mfma_f32_16x16x32_bf16 v[24:27], v[48:51], v[192:195], v[24:27]\n"
        "ds_read_b64_tr_b16 v[34:35], v231 offset:32768\n"
        "v_mfma_f32_16x16x32_bf16 v[24:27], v[52:55], v[208:211], v[24:27]\n"
        "ds_read_b64_tr_b16 v[38:39], v231 offset:40960\n"
        "v_mfma_f32_16x16x32_bf16 v[28:31], v[60:63], v[208:211], v[28:31]\n"
        "ds_read_b64_tr_b16 v[46:47], v233 offset:40960\n"
        "v_mfma_f32_16x16x32_bf16 v[28:31], v[56:59], v[192:195], v[28:31]\n"
        "ds_read_b64_tr_b16 v[42:43], v233 offset:32768\n"
        "v_mfma_f32_16x16x32_bf16 v[92:95], v[56:59], v[196:199], v[92:95]\n"
        "ds_read_b128 v[192:195], v246 offset:2048\n"
        "v_mfma_f32_16x16x32_bf16 v[92:95], v[60:63], v[212:215], v[92:95]\n"
        "ds_read_b128 v[208:211], v247 offset:2048\n"
        "v_mfma_f32_16x16x32_bf16 v[88:91], v[52:55], v[212:215], v[88:91]\n"
        "global_store_dwordx4 v254, a[216:219], s[70:71] offset:0 sc1\n"
        "ds_read_b128 v[212:215], v247 offset:0\n"
        "v_mfma_f32_16x16x32_bf16 v[88:91], v[48:51], v[196:199], v[88:91]\n"
        "ds_write_b128 v244, a[204:207] offset:17408\n"
        "v_mfma_f32_16x16x32_bf16 v[120:123], v[48:51], v[200:203], v[120:123]\n"
        "ds_read_b128 v[196:199], v246 offset:0\n"
        "v_mfma_f32_16x16x32_bf16 v[120:123], v[52:55], v[216:219], v[120:123]\n"
        "ds_write_b128 v244, a[160:163] offset:20480\n"
        "v_mfma_f32_16x16x32_bf16 v[124:127], v[60:63], v[216:219], v[124:127]\n"
        "ds_write_b128 v244, a[164:167] offset:21504\n"
        "v_mfma_f32_16x16x32_bf16 v[124:127], v[56:59], v[200:203], v[124:127]\n"
        "ds_read_b128 v[200:203], v246 offset:6144\n"
        "s_waitcnt lgkmcnt(5)\n"
        "v_mfma_f32_16x16x32_bf16 v[156:159], v[56:59], v[204:207], v[156:159]\n"
        "ds_read_b128 v[216:219], v247 offset:6144\n"
        "v_mfma_f32_16x16x32_bf16 v[156:159], v[60:63], v[220:223], v[156:159]\n"
        "ds_write_b128 v244, a[168:171] offset:24576\n"
        "v_mfma_f32_16x16x32_bf16 v[152:155], v[52:55], v[220:223], v[152:155]\n"
        "ds_read_b128 v[220:223], v247 offset:4096\n"
        "s_waitcnt lgkmcnt(6)\n"
        "v_mfma_f32_16x16x32_bf16 v[152:155], v[48:51], v[204:207], v[152:155]\n"
        "ds_read_b128 v[204:207], v246 offset:4096\n"
        "v_mfma_f32_16x16x32_bf16 v[0:3], v[160:163], v[192:195], v[0:3]\n"
        "global_store_dwordx4 v254, a[248:251], s[70:71] offset:1024 sc1\n"
        "ds_read_b64_tr_b16 v[48:49], v226 offset:33792\n"
        "ds_read_b64_tr_b16 v[50:51], v227 offset:33792\n"
        "s_waitcnt lgkmcnt(7)\n"
        "v_mfma_f32_16x16x32_bf16 v[0:3], v[164:167], v[208:211], v[0:3]\n"
        "ds_read_b64_tr_b16 v[52:53], v226 offset:41984\n"
        "s_waitcnt lgkmcnt(7)\n"
        "v_mfma_f32_16x16x32_bf16 v[4:7], v[172:175], v[208:211], v[4:7]\n"
        "ds_read_b64_tr_b16 v[54:55], v227 offset:41984\n"
        "s_waitcnt lgkmcnt(7)\n"
        "v_mfma_f32_16x16x32_bf16 v[4:7], v[168:171], v[192:195], v[4:7]\n"
        "ds_read_b64_tr_b16 v[60:61], v228 offset:41984\n"
        "v_mfma_f32_16x16x32_bf16 v[68:71], v[168:171], v[196:199], v[68:71]\n"
        "ds_read_b64_tr_b16 v[62:63], v229 offset:41984\n"
        "v_cvt_pk_bf16_f32 v0, v0, v1\n"
        "v_mfma_f32_16x16x32_bf16 v[68:71], v[172:175], v[212:215], v[68:71]\n"
        "ds_read_b64_tr_b16 v[56:57], v228 offset:33792\n"
        "v_cvt_pk_bf16_f32 v1, v2, v3\n"
        "v_mfma_f32_16x16x32_bf16 v[64:67], v[164:167], v[212:215], v[64:67]\n"
        "ds_read_b64_tr_b16 v[58:59], v229 offset:33792\n"
        "v_cvt_pk_bf16_f32 v2, v4, v5\n"
        "v_mfma_f32_16x16x32_bf16 v[64:67], v[160:163], v[196:199], v[64:67]\n"
        "ds_write_b128 v244, a[172:175] offset:25600\n"
        "v_mfma_f32_16x16x32_bf16 v[96:99], v[160:163], v[200:203], v[96:99]\n"
        "ds_write_b128 v244, a[0:3] offset:28672\n"
        "v_mfma_f32_16x16x32_bf16 v[96:99], v[164:167], v[216:219], v[96:99]\n"
        "ds_write_b128 v244, a[4:7] offset:29696\n"
        "v_mfma_f32_16x16x32_bf16 v[100:103], v[172:175], v[216:219], v[100:103]\n"
        "s_waitcnt lgkmcnt(5)\n"
        "v_cvt_pk_bf16_f32 v3, v6, v7\n"
        "v_cvt_pk_bf16_f32 v64, v64, v65\n"
        "v_mfma_f32_16x16x32_bf16 v[100:103], v[168:171], v[200:203], v[100:103]\n"
        "ds_write_b128 v244, a[8:11] offset:0\n"
        "v_mfma_f32_16x16x32_bf16 v[132:135], v[168:171], v[204:207], v[132:135]\n"
        "global_store_dwordx4 v254, a[220:223], s[70:71] offset:2048 sc1\n"
        "ds_read_b64_tr_b16 v[168:169], v232 offset:33792\n"
        "ds_read_b64_tr_b16 v[170:171], v233 offset:33792\n"
        "v_cvt_pk_bf16_f32 v65, v66, v67\n"
        "v_mfma_f32_16x16x32_bf16 v[132:135], v[172:175], v[220:223], v[132:135]\n"
        "ds_write_b128 v244, a[12:15] offset:1024\n"
        "v_mfma_f32_16x16x32_bf16 v[128:131], v[164:167], v[220:223], v[128:131]\n"
        "ds_read_b64_tr_b16 v[164:165], v230 offset:41984\n"
        "v_cvt_pk_bf16_f32 v66, v68, v69\n"
        "v_mfma_f32_16x16x32_bf16 v[128:131], v[160:163], v[204:207], v[128:131]\n"
        "ds_read_b64_tr_b16 v[160:161], v230 offset:33792\n"
        "v_cvt_pk_bf16_f32 v67, v70, v71\n"
        "v_mfma_f32_16x16x32_bf16 v[8:11], v[32:35], v[192:195], v[8:11]\n"
        "ds_read_b64_tr_b16 v[162:163], v231 offset:33792\n"
        "v_cvt_pk_bf16_f32 v96, v96, v97\n"
        "v_mfma_f32_16x16x32_bf16 v[8:11], v[36:39], v[208:211], v[8:11]\n"
        "ds_read_b64_tr_b16 v[166:167], v231 offset:41984\n"
        "v_cvt_pk_bf16_f32 v97, v98, v99\n"
        "v_mfma_f32_16x16x32_bf16 v[12:15], v[44:47], v[208:211], v[12:15]\n"
        "ds_read_b64_tr_b16 v[172:173], v232 offset:41984\n"
        "v_cvt_pk_bf16_f32 v98, v100, v101\n"
        "v_mfma_f32_16x16x32_bf16 v[12:15], v[40:43], v[192:195], v[12:15]\n"
        "ds_read_b64_tr_b16 v[174:175], v233 offset:41984\n"
        "v_cvt_pk_bf16_f32 v99, v102, v103\n"
        "v_mfma_f32_16x16x32_bf16 v[76:79], v[40:43], v[196:199], v[76:79]\n"
        "v_cvt_pk_bf16_f32 v4, v8, v9\n"
        "v_cvt_pk_bf16_f32 v5, v10, v11\n"
        "v_mfma_f32_16x16x32_bf16 v[76:79], v[44:47], v[212:215], v[76:79]\n"
        "v_cvt_pk_bf16_f32 v128, v128, v129\n"
        "v_cvt_pk_bf16_f32 v129, v130, v131\n"
        "v_mfma_f32_16x16x32_bf16 v[72:75], v[36:39], v[212:215], v[72:75]\n"
        "s_add_u32 s82, s82, 1\n"
        "v_cvt_pk_bf16_f32 v6, v12, v13\n"
        "v_cvt_pk_bf16_f32 v7, v14, v15\n"
        "v_mfma_f32_16x16x32_bf16 v[72:75], v[32:35], v[196:199], v[72:75]\n"
        "global_store_dwordx4 v254, a[252:255], s[70:71] offset:3072 sc1\n"
        "s_add_u32 s70, s38, s97\n"
        "s_addc_u32 s71, s39, 0\n"
        "s_add_u32 s97, s53, 0x2000\n"
        "v_cvt_pk_bf16_f32 v70, v76, v77\n"
        "v_cvt_pk_bf16_f32 v71, v78, v79\n"
        "v_mfma_f32_16x16x32_bf16 v[104:107], v[32:35], v[200:203], v[104:107]\n"
        "s_cmp_ge_u32 s82, s44\n"
        "s_addc_u32 s90, s82, 0\n"
        "v_cvt_pk_bf16_f32 v130, v132, v133\n"
        "v_mfma_f32_16x16x32_bf16 v[104:107], v[36:39], v[216:219], v[104:107]\n"
        "s_waitcnt lgkmcnt(2)\n"
        "v_cvt_pk_bf16_f32 v68, v72, v73\n"
        "v_cvt_pk_bf16_f32 v69, v74, v75\n"
        "v_mfma_f32_16x16x32_bf16 v[108:111], v[44:47], v[216:219], v[108:111]\n"
        "s_waitcnt lgkmcnt(0)\n"
        "v_cvt_pk_bf16_f32 v131, v134, v135\n"
        "v_mfma_f32_16x16x32_bf16 v[108:111], v[40:43], v[200:203], v[108:111]\n"
        "s_barrier\n"
        "v_mfma_f32_16x16x32_bf16 v[140:143], v[40:43], v[204:207], v[140:143]\n"
        "ds_write_b128 v245, a[208:211] offset:8192\n"
        "v_mfma_f32_16x16x32_bf16 v[140:143], v[44:47], v[220:223], v[140:143]\n"
        "ds_write_b128 v245, a[240:243] offset:9216\n"
        "v_mfma_f32_16x16x32_bf16 v[136:139], v[36:39], v[220:223], v[136:139]\n"
        "ds_write_b128 v245, a[212:215] offset:10240\n"
        "v_mfma_f32_16x16x32_bf16 v[136:139], v[32:35], v[204:207], v[136:139]\n"
        "ds_write_b128 v245, a[244:247] offset:11264\n"
        "v_mfma_f32_16x16x32_bf16 v[16:19], v[48:51], v[192:195], v[16:19]\n"
        "ds_write_b128 v245, a[216:219] offset:0\n"
        "v_mfma_f32_16x16x32_bf16 v[16:19], v[52:55], v[208:211], v[16:19]\n"
        "ds_write_b128 v245, a[248:251] offset:1024\n"
        "v_mfma_f32_16x16x32_bf16 v[20:23], v[60:63], v[208:211], v[20:23]\n"
        "ds_write_b128 v245, a[220:223] offset:2048\n"
        "v_mfma_f32_16x16x32_bf16 v[20:23], v[56:59], v[192:195], v[20:23]\n"
        "global_store_dwordx4 v254, a[176:179], s[70:71] offset:0 sc1\n"
        "s_barrier\n"
        "v_mfma_f32_16x16x32_bf16 v[84:87], v[56:59], v[196:199], v[84:87]\n"
        "ds_read_b128 a[96:99], v240 offset:0\n"
        "s_lshl_b32 s90, s90, 15\n"
        "v_cvt_pk_bf16_f32 v8, v16, v17\n"
        "v_mfma_f32_16x16x32_bf16 v[84:87], v[60:63], v[212:215], v[84:87]\n"
        "ds_read_b128 a[100:103], v240 offset:1024\n"
        "s_xor_b32 s78, s78, 0x8000\n"
        "v_cvt_pk_bf16_f32 v9, v18, v19\n"
        "v_mfma_f32_16x16x32_bf16 v[80:83], v[52:55], v[212:215], v[80:83]\n"
        "ds_read_b128 a[104:107], v240 offset:2048\n"
        "s_and_b32 s52, s41, 1\n"
        "v_cvt_pk_bf16_f32 v10, v20, v21\n"
        "v_mfma_f32_16x16x32_bf16 v[80:83], v[48:51], v[196:199], v[80:83]\n"
        "ds_read_b128 a[108:111], v240 offset:3072\n"
        "s_lshl_b32 s52, s52, 11\n"
        "v_cvt_pk_bf16_f32 v11, v22, v23\n"
        "v_mfma_f32_16x16x32_bf16 v[112:115], v[48:51], v[200:203], v[112:115]\n"
        "ds_read_b128 a[112:115], v240 offset:4096\n"
        "s_lshl2_add_u32 s53, s42, s40\n"
        "v_cndmask_b32 v32, 0, v242, s[10:11]\n"
        "v_mfma_f32_16x16x32_bf16 v[112:115], v[52:55], v[216:219], v[112:115]\n"
        "ds_read_b128 a[116:119], v240 offset:5120\n"
        "s_add_u32 s52, s52, s53\n"
        "v_cndmask_b32 v33, 0, v242, s[12:13]\n"
        "v_mfma_f32_16x16x32_bf16 v[116:119], v[60:63], v[216:219], v[116:119]\n"
        "ds_read_b128 a[120:123], v240 offset:6144\n"
        "s_add_u32 s52, s52, 1920\n"
        "v_cndmask_b32 v34, 0, v242, s[14:15]\n"
        "v_mfma_f32_16x16x32_bf16 v[116:119], v[56:59], v[200:203], v[116:119]\n"
        "ds_write_b128 v245, a[252:255] offset:3072\n"
        "v_mfma_f32_16x16x32_bf16 v[148:151], v[56:59], v[204:207], v[148:151]\n"
        "ds_write_b128 v245, a[176:179] offset:16384\n"
        "v_mfma_f32_16x16x32_bf16 v[148:151], v[60:63], v[220:223], v[148:151]\n"
        "ds_write_b128 v245, a[180:183] offset:17408\n"
        "v_mfma_f32_16x16x32_bf16 v[144:147], v[52:55], v[220:223], v[144:147]\n"
        "ds_write_b128 v245, a[184:187] offset:18432\n"
        "v_mfma_f32_16x16x32_bf16 v[144:147], v[48:51], v[204:207], v[144:147]\n"
        "global_store_dwordx4 v254, a[180:183], s[70:71] offset:1024 sc1\n"
        "ds_read_b128 a[124:127], v240 offset:7168\n"
        "s_lshr_b32 s53, s41, 2\n"
        "v_cndmask_b32 v35, 0, v242, s[16:17]\n"
        "v_cndmask_b32 v48, 0, v242, s[20:21]\n"
        "v_cndmask_b32 v49, 0, v242, s[22:23]\n"
        "v_mfma_f32_16x16x32_bf16 v[24:27], v[160:163], v[192:195], v[24:27]\n"
        "ds_write_b128 v245, a[188:191] offset:19456\n"
        "v_mfma_f32_16x16x32_bf16 v[24:27], v[164:167], v[208:211], v[24:27]\n"
        "ds_write_b128 v245, a[16:19] offset:24576\n"
        "v_mfma_f32_16x16x32_bf16 v[28:31], v[172:175], v[208:211], v[28:31]\n"
        "LHA10_%=:\n"
        "s_cbranch_execz LHRA_%=\n"
        "ds_write_b128 v245, a[20:23] offset:25600\n"
        "v_mfma_f32_16x16x32_bf16 v[28:31], v[168:171], v[192:195], v[28:31]\n"
        "ds_write_b128 v245, a[24:27] offset:26624\n"
        "v_mfma_f32_16x16x32_bf16 v[92:95], v[168:171], v[196:199], v[92:95]\n"
        "ds_write_b128 v245, a[28:31] offset:27648\n"
        "v_mfma_f32_16x16x32_bf16 v[92:95], v[172:175], v[212:215], v[92:95]\n"
        "s_waitcnt lgkmcnt(4)\n"
        "v_cvt_pk_bf16_f32 v12, v24, v25\n"
        "v_cvt_pk_bf16_f32 v13, v26, v27\n"
        "v_mfma_f32_16x16x32_bf16 v[88:91], v[164:167], v[212:215], v[88:91]\n"
        "global_load_lds_dwordx4 v254, s[68:69] offset:0\n"
        "s_lshl1_add_u32 s52, s52, s53\n"
        "v_cvt_pk_bf16_f32 v14, v28, v29\n"
        "v_mfma_f32_16x16x32_bf16 v[88:91], v[160:163], v[196:199], v[88:91]\n"
        "s_lshl_b32 s52, s52, 10\n"
        "v_cvt_pk_bf16_f32 v15, v30, v31\n"
        "v_cndmask_b32 v50, 0, v242, s[24:25]\n"
        "v_mfma_f32_16x16x32_bf16 v[120:123], v[160:163], v[200:203], v[120:123]\n"
        "s_add_u32 s26, s18, s52\n"
        "s_addc_u32 s27, s19, 0\n"
        "v_cndmask_b32 v51, 0, v242, s[28:29]\n"
        "v_mfma_f32_16x16x32_bf16 v[120:123], v[164:167], v[216:219], v[120:123]\n"
        "s_add_u32 s76, s66, s90\n"
        "s_addc_u32 s77, s67, 0\n"
        "v_cvt_pk_bf16_f32 v72, v80, v81\n"
        "v_mfma_f32_16x16x32_bf16 v[124:127], v[172:175], v[216:219], v[124:127]\n"
        "global_store_dwordx4 v254, a[188:191], s[70:71] offset:3072 sc1\n"
        "s_waitcnt lgkmcnt(0)\n"
        "v_mfma_f32_16x16x32_bf16 v[124:127], v[168:171], v[200:203], v[124:127]\n"
        "s_barrier\n"
        "v_mfma_f32_16x16x32_bf16 v[156:159], v[168:171], v[204:207], v[156:159]\n"
        "ds_read_b128 a[144:147], v241 offset:0\n"
        "v_cvt_pk_bf16_f32 v73, v82, v83\n"
        "v_mfma_f32_16x16x32_bf16 v[156:159], v[172:175], v[220:223], v[156:159]\n"
        "ds_read_b128 a[148:151], v241 offset:1024\n"
        "v_cvt_pk_bf16_f32 v74, v84, v85\n"
        "v_mfma_f32_16x16x32_bf16 v[152:155], v[164:167], v[220:223], v[152:155]\n"
        "global_store_dwordx4 v254, a[184:187], s[70:71] offset:2048 sc1\n"
        "ds_read_b128 a[152:155], v241 offset:2048\n"
        "s_add_u32 s70, s38, s97\n"
        "s_addc_u32 s71, s39, 0\n"
        "v_cvt_pk_bf16_f32 v75, v86, v87\n"
        "v_cvt_pk_bf16_f32 v76, v88, v89\n"
        "v_mfma_f32_16x16x32_bf16 v[152:155], v[160:163], v[204:207], v[152:155]\n"
        "ds_read_b128 a[156:159], v241 offset:3072\n"
        "v_cvt_pk_bf16_f32 v77, v90, v91\n"
        "v_mfma_f32_16x16x32_bf16 v[32:35], a[96:99], v[0:3], v[32:35]\n"
        "ds_read_b128 a[96:99], v240 offset:8192\n"
        "v_cvt_pk_bf16_f32 v78, v92, v93\n"
        "v_mfma_f32_16x16x32_bf16 v[32:35], a[100:103], v[4:7], v[32:35]\n"
        "ds_read_b128 a[100:103], v240 offset:9216\n"
        "v_cvt_pk_bf16_f32 v79, v94, v95\n"
        "v_mfma_f32_16x16x32_bf16 v[32:35], a[104:107], v[8:11], v[32:35]\n"
        "ds_read_b128 a[104:107], v240 offset:10240\n"
        "v_cvt_pk_bf16_f32 v100, v104, v105\n"
        "v_mfma_f32_16x16x32_bf16 v[32:35], a[108:111], v[12:15], v[32:35]\n"
        "global_store_dwordx4 v254, a[16:19], s[70:71] offset:0 sc1\n"
        "ds_read_b128 a[108:111], v240 offset:11264\n"
        "ds_read_b128 a[128:131], v241 offset:4096\n"
        "v_cvt_pk_bf16_f32 v101, v106, v107\n"
        "v_mfma_f32_16x16x32_bf16 v[48:51], a[112:115], v[0:3], v[48:51]\n"
        "ds_read_b128 a[112:115], v240 offset:12288\n"
        "v_cvt_pk_bf16_f32 v102, v108, v109\n"
        "v_mfma_f32_16x16x32_bf16 v[48:51], a[116:119], v[4:7], v[48:51]\n"
        "ds_read_b128 a[116:119], v240 offset:13312\n"
        "v_max_f32 v246, v32, v33\n"
        "v_mfma_f32_16x16x32_bf16 v[48:51], a[120:123], v[8:11], v[48:51]\n"
        "ds_read_b128 a[120:123], v240 offset:14336\n"
        "v_max3_f32 v246, v34, v35, v246\n"
        "v_mfma_f32_16x16x32_bf16 v[48:51], a[124:127], v[12:15], v[48:51]\n"
        "v_mov_b32 v247, v246\n"
        "global_store_dwordx4 v254, a[20:23], s[70:71] offset:1024 sc1\n"
        "ds_read_b128 a[124:127], v240 offset:15360\n"
        "v_permlane16_swap_b32 v246, v247\n"
        "v_max_f32 v246, v246, v247\n"
        "v_mov_b32 v247, v246\n"
        "ds_read_b128 a[132:135], v241 offset:5120\n"
        "ds_read_b128 a[136:139], v241 offset:6144\n"
        "v_permlane32_swap_b32 v246, v247\n"
        "v_max_f32 v246, v246, v247\n"
        "v_sub_f32 v224, 0, v246\n"
        "v_sub_f32 v225, 0, v246\n"
        "v_sub_f32 v226, 0, v246\n"
        "v_sub_f32 v227, 0, v246\n"
        "v_add_f32 v32, v32, v224\n"
        "v_exp_f32 v32, v32\n"
        "v_add_f32 v33, v33, v224\n"
        "v_exp_f32 v33, v33\n"
        "v_add_f32 v34, v34, v224\n"
        "v_cvt_pk_bf16_f32 v16, v32, v33\n"
        "v_exp_f32 v34, v34\n"
        "v_add_f32 v35, v35, v224\n"
        "v_exp_f32 v35, v35\n"
        "v_cndmask_b32 v32, v224, v242, s[30:31]\n"
        "v_cvt_pk_bf16_f32 v17, v34, v35\n"
        "v_cndmask_b32 v33, v224, v242, s[56:57]\n"
        "v_cndmask_b32 v34, v224, v242, s[58:59]\n"
        "v_cndmask_b32 v35, v224, v242, s[60:61]\n"
        "s_waitcnt lgkmcnt(2)\n"
        "s_nop 0\n"
        "v_mfma_f32_16x16x32_bf16 v[32:35], a[96:99], v[0:3], v[32:35]\n"
        "ds_read_b128 a[96:99], v240 offset:16384\n"
        "v_add_f32 v48, v48, v224\n"
        "v_mfma_f32_16x16x32_bf16 v[32:35], a[100:103], v[4:7], v[32:35]\n"
        "v_exp_f32 v48, v48\n"
        "v_mfma_f32_16x16x32_bf16 v[32:35], a[104:107], v[8:11], v[32:35]\n"
        "ds_read_b128 a[100:103], v240 offset:17408\n"
        "v_add_f32 v49, v49, v224\n"
        "v_mfma_f32_16x16x32_bf16 v[32:35], a[108:111], v[12:15], v[32:35]\n"
        "v_exp_f32 v49, v49\n"
        "global_store_dwordx4 v254, a[24:27], s[70:71] offset:2048 sc1\n"
        "v_cvt_pk_bf16_f32 v18, v48, v49\n"
        "v_add_f32 v50, v50, v224\n"
        "v_exp_f32 v50, v50\n"
        "v_add_f32 v51, v51, v224\n"
        "v_exp_f32 v51, v51\n"
        "ds_read_b128 a[104:107], v240 offset:18432\n"
        "v_cvt_pk_bf16_f32 v19, v50, v51\n"
        "s_nop 1\n"
        "v_mfma_f32_16x16x32_bf16 a[64:67], a[80:83], v[16:19], 0\n"
        "s_waitcnt lgkmcnt(3)\n"
        "ds_read_b128 a[140:143], v241 offset:7168\n"
        "v_mfma_f32_16x16x32_bf16 v[160:163], a[144:147], v[16:19], 0\n"
        "global_store_dwordx4 v254, a[28:31], s[70:71] offset:3072 sc1\n"
        "ds_read_b128 a[108:111], v240 offset:19456\n"
        "ds_read_b128 a[144:147], v241 offset:8192\n"
        "v_cndmask_b32 v48, v224, v242, s[62:63]\n"
        "v_mfma_f32_16x16x32_bf16 v[164:167], a[148:151], v[16:19], 0\n"
        "ds_read_b128 a[148:151], v241 offset:9216\n"
        "v_cndmask_b32 v49, v224, v242, s[84:85]\n"
        "v_mfma_f32_16x16x32_bf16 v[168:171], a[152:155], v[16:19], 0\n"
        "ds_read_b128 a[152:155], v241 offset:10240\n"
        "v_cndmask_b32 v50, v224, v242, s[86:87]\n"
        "v_mfma_f32_16x16x32_bf16 v[172:175], a[156:159], v[16:19], 0\n"
        "ds_read_b128 a[156:159], v241 offset:11264\n"
        "v_cndmask_b32 v51, v224, v242, s[88:89]\n"
        "s_nop 1\n"
        "v_mfma_f32_16x16x32_bf16 v[48:51], a[112:115], v[0:3], v[48:51]\n"
        "v_exp_f32 v32, v32\n"
        "v_mfma_f32_16x16x32_bf16 v[48:51], a[116:119], v[4:7], v[48:51]\n"
        "s_waitcnt lgkmcnt(6)\n"
        "v_exp_f32 v33, v33\n"
        "v_mfma_f32_16x16x32_bf16 v[48:51], a[120:123], v[8:11], v[48:51]\n"
        "s_waitcnt lgkmcnt(5)\n"
        "ds_read_b128 a[112:115], v240 offset:20480\n"
        "v_mfma_f32_16x16x32_bf16 v[48:51], a[124:127], v[12:15], v[48:51]\n"
        "ds_read_b128 a[116:119], v240 offset:21504\n"
        "v_cvt_pk_bf16_f32 v80, v32, v33\n"
        "v_mfma_f32_16x16x32_bf16 v[176:179], a[128:131], v[16:19], 0\n"
        "v_exp_f32 v34, v34\n"
        "v_mfma_f32_16x16x32_bf16 v[180:183], a[132:135], v[16:19], 0\n"
        "v_exp_f32 v35, v35\n"
        "v_mfma_f32_16x16x32_bf16 v[184:187], a[136:139], v[16:19], 0\n"
        "ds_read_b128 a[120:123], v240 offset:22528\n"
        "v_cvt_pk_bf16_f32 v81, v34, v35\n"
        "v_mfma_f32_16x16x32_bf16 v[32:35], a[96:99], v[0:3], v[224:227]\n"
        "v_exp_f32 v48, v48\n"
        "v_mfma_f32_16x16x32_bf16 v[32:35], a[100:103], v[4:7], v[32:35]\n"
        "v_exp_f32 v49, v49\n"
        "v_mfma_f32_16x16x32_bf16 v[32:35], a[104:107], v[8:11], v[32:35]\n"
        "s_waitcnt lgkmcnt(3)\n"
        "ds_read_b128 a[124:127], v240 offset:23552\n"
        "v_mfma_f32_16x16x32_bf16 v[32:35], a[108:111], v[12:15], v[32:35]\n"
        "ds_read_b128 a[128:131], v241 offset:12288\n"
        "v_cvt_pk_bf16_f32 v82, v48, v49\n"
        "v_mfma_f32_16x16x32_bf16 v[188:191], a[140:143], v[16:19], 0\n"
        "v_cvt_pk_bf16_f32 v103, v110, v111\n"
        "v_cndmask_b32 v36, 0, v242, s[10:11]\n"
        "v_cndmask_b32 v37, 0, v242, s[12:13]\n"
        "v_cndmask_b32 v38, 0, v242, s[14:15]\n"
        "v_cndmask_b32 v39, 0, v242, s[16:17]\n"
        "s_nop 1\n"
        "v_mfma_f32_16x16x32_bf16 v[36:39], a[108:111], v[76:79], v[36:39]\n"
        "v_exp_f32 v50, v50\n"
        "v_mfma_f32_16x16x32_bf16 v[36:39], a[104:107], v[72:75], v[36:39]\n"
        "v_exp_f32 v51, v51\n"
        "v_mfma_f32_16x16x32_bf16 v[36:39], a[100:103], v[68:71], v[36:39]\n"
        "ds_read_b128 a[100:103], v240 offset:25600\n"
        "v_cvt_pk_bf16_f32 v83, v50, v51\n"
        "v_mfma_f32_16x16x32_bf16 v[36:39], a[96:99], v[64:67], v[36:39]\n"
        "s_waitcnt lgkmcnt(3)\n"
        "ds_read_b128 a[96:99], v240 offset:24576\n"
        "v_mfma_f32_16x16x32_bf16 v[48:51], a[112:115], v[0:3], v[224:227]\n"
        "ds_read_b128 a[132:135], v241 offset:13312\n"
        "v_cndmask_b32 v52, 0, v242, s[20:21]\n"
        "v_mfma_f32_16x16x32_bf16 v[48:51], a[116:119], v[4:7], v[48:51]\n"
        "ds_read_b128 a[136:139], v241 offset:14336\n"
        "v_cndmask_b32 v53, 0, v242, s[22:23]\n"
        "v_mfma_f32_16x16x32_bf16 v[48:51], a[120:123], v[8:11], v[48:51]\n"
        "s_waitcnt lgkmcnt(4)\n"
        "ds_read_b128 a[104:107], v240 offset:26624\n"
        "v_mfma_f32_16x16x32_bf16 v[48:51], a[124:127], v[12:15], v[48:51]\n"
        "ds_read_b128 a[140:143], v241 offset:15360\n"
        "v_cndmask_b32 v54, 0, v242, s[24:25]\n"
        "v_mfma_f32_16x16x32_bf16 a[64:67], a[80:83], v[80:83], a[64:67]\n"
        "v_exp_f32 v32, v32\n"
        "v_mfma_f32_16x16x32_bf16 v[160:163], a[144:147], v[80:83], v[160:163]\n"
        "v_exp_f32 v33, v33\n"
        "v_mfma_f32_16x16x32_bf16 v[164:167], a[148:151], v[80:83], v[164:167]\n"
        "ds_read_b128 a[108:111], v240 offset:27648\n"
        "v_cvt_pk_bf16_f32 v16, v32, v33\n"
        "v_mfma_f32_16x16x32_bf16 v[168:171], a[152:155], v[80:83], v[168:171]\n"
        "s_waitcnt lgkmcnt(6)\n"
        "v_exp_f32 v34, v34\n"
        "v_mfma_f32_16x16x32_bf16 v[172:175], a[156:159], v[80:83], v[172:175]\n"
        "s_waitcnt lgkmcnt(4)\n"
        "v_exp_f32 v35, v35\n"
        "v_mfma_f32_16x16x32_bf16 v[176:179], a[128:131], v[80:83], v[176:179]\n"
        "s_waitcnt lgkmcnt(3)\n"
        "ds_read_b128 a[144:147], v241 offset:16384\n"
        "v_mfma_f32_16x16x32_bf16 v[180:183], a[132:135], v[80:83], v[180:183]\n"
        "ds_read_b128 a[148:151], v241 offset:17408\n"
        "v_cvt_pk_bf16_f32 v17, v34, v35\n"
        "v_mfma_f32_16x16x32_bf16 v[32:35], a[96:99], v[0:3], v[224:227]\n"
        "s_waitcnt lgkmcnt(3)\n"
        "ds_read_b128 a[152:155], v241 offset:18432\n"
        "v_mfma_f32_16x16x32_bf16 v[32:35], a[100:103], v[4:7], v[32:35]\n"
        "ds_read_b128 a[156:159], v241 offset:19456\n"
        "v_cndmask_b32 v55, 0, v242, s[28:29]\n"
        "s_nop 1\n"
        "v_mfma_f32_16x16x32_bf16 v[52:55], a[124:127], v[76:79], v[52:55]\n"
        "ds_read_b128 a[124:127], v240 offset:31744\n"
        "v_cvt_pk_bf16_f32 v104, v112, v113\n"
        "v_mfma_f32_16x16x32_bf16 v[52:55], a[120:123], v[72:75], v[52:55]\n"
        "ds_read_b128 a[120:123], v240 offset:30720\n"
        "v_max_f32 v248, v36, v37\n"
        "v_mfma_f32_16x16x32_bf16 v[52:55], a[116:119], v[68:71], v[52:55]\n"
        "ds_read_b128 a[116:119], v240 offset:29696\n"
        "v_cvt_pk_bf16_f32 v105, v114, v115\n"
        "v_mfma_f32_16x16x32_bf16 v[52:55], a[112:115], v[64:67], v[52:55]\n"
        "ds_read_b128 a[112:115], v240 offset:28672\n"
        "v_max3_f32 v248, v38, v39, v248\n"
        "v_mfma_f32_16x16x32_bf16 v[32:35], a[104:107], v[8:11], v[32:35]\n"
        "s_waitcnt lgkmcnt(7)\n"
        "ds_read_b128 a[128:131], v241 offset:20480\n"
        "v_mfma_f32_16x16x32_bf16 v[32:35], a[108:111], v[12:15], v[32:35]\n"
        "ds_read_b128 a[132:135], v241 offset:21504\n"
        "v_cvt_pk_bf16_f32 v106, v116, v117\n"
        "v_mfma_f32_16x16x32_bf16 v[184:187], a[136:139], v[80:83], v[184:187]\n"
        "ds_read_b128 a[136:139], v241 offset:22528\n"
        "v_mov_b32 v249, v248\n"
        "v_mfma_f32_16x16x32_bf16 v[188:191], a[140:143], v[80:83], v[188:191]\n"
        "v_cvt_pk_bf16_f32 v107, v118, v119\n"
        "v_permlane16_swap_b32 v248, v249\n"
        "v_max_f32 v248, v248, v249\n"
        "v_mov_b32 v249, v248\n"
        "v_exp_f32 v48, v48\n"
        "v_exp_f32 v49, v49\n"
        "v_permlane32_swap_b32 v248, v249\n"
        "v_max_f32 v248, v248, v249\n"
        "v_sub_f32 v228, 0, v248\n"
        "v_sub_f32 v229, 0, v248\n"
        "v_sub_f32 v230, 0, v248\n"
        "v_sub_f32 v231, 0, v248\n"
        "v_add_f32 v36, v36, v228\n"
        "v_exp_f32 v36, v36\n"
        "v_add_f32 v37, v37, v228\n"
        "v_exp_f32 v37, v37\n"
        "v_add_f32 v38, v38, v228\n"
        "v_cvt_pk_bf16_f32 v20, v36, v37\n"
        "v_exp_f32 v38, v38\n"
        "v_add_f32 v39, v39, v228\n"
        "v_exp_f32 v39, v39\n"
        "v_cvt_pk_bf16_f32 v18, v48, v49\n"
        "v_cvt_pk_bf16_f32 v21, v38, v39\n"
        "v_exp_f32 v50, v50\n"
        "v_exp_f32 v51, v51\n"
        "v_cndmask_b32 v36, v228, v242, s[30:31]\n"
        "v_cvt_pk_bf16_f32 v19, v50, v51\n"
        "s_waitcnt lgkmcnt(0)\n"
        "v_mfma_f32_16x16x32_bf16 v[48:51], a[112:115], v[0:3], v[224:227]\n"
        "ds_read_b128 a[140:143], v241 offset:23552\n"
        "s_add_u32 s82, s82, 1\n"
        "v_cndmask_b32 v37, v228, v242, s[56:57]\n"
        "v_mfma_f32_16x16x32_bf16 v[48:51], a[116:119], v[4:7], v[48:51]\n"
        "v_exp_f32 v32, v32\n"
        "v_mfma_f32_16x16x32_bf16 v[48:51], a[120:123], v[8:11], v[48:51]\n"
        "s_barrier\n"
        "v_mfma_f32_16x16x32_bf16 v[48:51], a[124:127], v[12:15], v[48:51]\n"
        "v_exp_f32 v33, v33\n"
        "v_mfma_f32_16x16x32_bf16 a[64:67], a[80:83], v[16:19], a[64:67]\n"
        "v_exp_f32 v34, v34\n"
        "v_mfma_f32_16x16x32_bf16 v[160:163], a[144:147], v[16:19], v[160:163]\n"
        "v_exp_f32 v35, v35\n"
        "v_mfma_f32_16x16x32_bf16 v[164:167], a[148:151], v[16:19], v[164:167]\n"
        "global_load_lds_dwordx4 v254, s[68:69] offset:1024\n"
        "v_cndmask_b32 v38, v228, v242, s[58:59]\n"
        "v_mfma_f32_16x16x32_bf16 v[168:171], a[152:155], v[16:19], v[168:171]\n"
        "v_exp_f32 v48, v48\n"
        "v_mfma_f32_16x16x32_bf16 v[172:175], a[156:159], v[16:19], v[172:175]\n"
        "s_waitcnt lgkmcnt(0)\n"
        "v_exp_f32 v49, v49\n"
        "v_mfma_f32_16x16x32_bf16 v[176:179], a[128:131], v[16:19], v[176:179]\n"
        "v_exp_f32 v50, v50\n"
        "v_mfma_f32_16x16x32_bf16 v[180:183], a[132:135], v[16:19], v[180:183]\n"
        "v_exp_f32 v51, v51\n"
        "v_mfma_f32_16x16x32_bf16 v[184:187], a[136:139], v[16:19], v[184:187]\n"
        "global_load_lds_dwordx4 v254, s[68:69] offset:2048\n"
        "v_cndmask_b32 v39, v228, v242, s[60:61]\n"
        "s_nop 1\n"
        "v_mfma_f32_16x16x32_bf16 v[36:39], a[108:111], v[76:79], v[36:39]\n"
        "v_add_f32 v52, v52, v228\n"
        "v_add_f32 v53, v53, v228\n"
        "v_mfma_f32_16x16x32_bf16 v[36:39], a[104:107], v[72:75], v[36:39]\n"
        "v_exp_f32 v52, v52\n"
        "v_mfma_f32_16x16x32_bf16 v[36:39], a[100:103], v[68:71], v[36:39]\n"
        "v_exp_f32 v53, v53\n"
        "v_mfma_f32_16x16x32_bf16 v[36:39], a[96:99], v[64:67], v[36:39]\n"
        "v_cvt_pk_bf16_f32 v22, v52, v53\n"
        "v_xor_b32 v240, 0x8000, v240\n"
        "v_mfma_f32_16x16x32_bf16 v[188:191], a[140:143], v[16:19], v[188:191]\n"
        "ds_read_b128 a[96:99], v240 offset:0\n"
        "ds_read_b128 a[100:103], v240 offset:1024\n"
        "v_cvt_pk_bf16_f32 v108, v120, v121\n"
        "v_add_f32 v54, v54, v228\n"
        "v_cvt_pk_bf16_f32 v109, v122, v123\n"
        "v_exp_f32 v54, v54\n"
        "v_cvt_pk_bf16_f32 v110, v124, v125\n"
        "v_add_f32 v55, v55, v228\n"
        "v_exp_f32 v55, v55\n"
        "v_cvt_pk_bf16_f32 v111, v126, v127\n"
        "v_cvt_pk_bf16_f32 v23, v54, v55\n"
        "s_nop 1\n"
        "v_mfma_f32_16x16x32_bf16 a[68:71], a[80:83], v[20:23], 0\n"
        "ds_read_b128 a[104:107], v240 offset:2048\n"
        "v_cvt_pk_bf16_f32 v132, v136, v137\n"
        "v_mfma_f32_16x16x32_bf16 v[192:195], a[144:147], v[20:23], 0\n"
        "ds_read_b128 a[108:111], v240 offset:3072\n"
        "v_cvt_pk_bf16_f32 v133, v138, v139\n"
        "v_mfma_f32_16x16x32_bf16 v[196:199], a[148:151], v[20:23], 0\n"
        "ds_read_b128 a[144:147], v241 offset:24576\n"
        "v_cvt_pk_bf16_f32 v134, v140, v141\n"
        "v_mfma_f32_16x16x32_bf16 v[200:203], a[152:155], v[20:23], 0\n"
        "ds_read_b128 a[148:151], v241 offset:25600\n"
        "v_cvt_pk_bf16_f32 v135, v142, v143\n"
        "v_mfma_f32_16x16x32_bf16 v[204:207], a[156:159], v[20:23], 0\n"
        "ds_read_b128 a[152:155], v241 offset:26624\n"
        "v_cvt_pk_bf16_f32 v80, v32, v33\n"
        "v_mfma_f32_16x16x32_bf16 v[208:211], a[128:131], v[20:23], 0\n"
        "ds_read_b128 a[156:159], v241 offset:27648\n"
        "v_cndmask_b32 v52, v228, v242, s[62:63]\n"
        "v_mfma_f32_16x16x32_bf16 v[212:215], a[132:135], v[20:23], 0\n"
        "ds_read_b128 a[128:131], v241 offset:28672\n"
        "v_cndmask_b32 v53, v228, v242, s[84:85]\n"
        "v_mfma_f32_16x16x32_bf16 v[216:219], a[136:139], v[20:23], 0\n"
        "ds_read_b128 a[132:135], v241 offset:29696\n"
        "v_cndmask_b32 v54, v228, v242, s[86:87]\n"
        "v_mfma_f32_16x16x32_bf16 v[220:223], a[140:143], v[20:23], 0\n"
        "v_cndmask_b32 v55, v228, v242, s[88:89]\n"
        "s_nop 1\n"
        "v_mfma_f32_16x16x32_bf16 v[52:55], a[124:127], v[76:79], v[52:55]\n"
        "ds_read_b128 a[124:127], v240 offset:7168\n"
        "v_cvt_pk_bf16_f32 v81, v34, v35\n"
        "v_mfma_f32_16x16x32_bf16 v[52:55], a[120:123], v[72:75], v[52:55]\n"
        "s_waitcnt lgkmcnt(6)\n"
        "v_exp_f32 v36, v36\n"
        "v_mfma_f32_16x16x32_bf16 v[52:55], a[116:119], v[68:71], v[52:55]\n"
        "ds_read_b128 a[116:119], v240 offset:5120\n"
        "v_cvt_pk_bf16_f32 v136, v144, v145\n"
        "v_mfma_f32_16x16x32_bf16 v[52:55], a[112:115], v[64:67], v[52:55]\n"
        "ds_read_b128 a[112:115], v240 offset:4096\n"
        "v_cvt_pk_bf16_f32 v137, v146, v147\n"
        "v_mfma_f32_16x16x32_bf16 v[32:35], a[96:99], v[0:3], v[224:227]\n"
        "v_exp_f32 v37, v37\n"
        "v_mfma_f32_16x16x32_bf16 v[32:35], a[100:103], v[4:7], v[32:35]\n"
        "ds_read_b128 a[120:123], v240 offset:6144\n"
        "v_cvt_pk_bf16_f32 v84, v36, v37\n"
        "v_mfma_f32_16x16x32_bf16 v[32:35], a[104:107], v[8:11], v[32:35]\n"
        "v_exp_f32 v38, v38\n"
        "v_mfma_f32_16x16x32_bf16 v[32:35], a[108:111], v[12:15], v[32:35]\n"
        "v_exp_f32 v39, v39\n"
        "v_cvt_pk_bf16_f32 v138, v148, v149\n"
        "v_cvt_pk_bf16_f32 v85, v38, v39\n"
        "v_mfma_f32_16x16x32_bf16 v[36:39], a[108:111], v[76:79], v[228:231]\n"
        "ds_read_b128 a[136:139], v241 offset:30720\n"
        "v_cvt_pk_bf16_f32 v139, v150, v151\n"
        "v_mfma_f32_16x16x32_bf16 v[36:39], a[104:107], v[72:75], v[36:39]\n"
        "ds_read_b128 a[140:143], v241 offset:31744\n"
        "v_cndmask_b32 v56, 0, v242, s[20:21]\n"
        "v_mfma_f32_16x16x32_bf16 v[36:39], a[100:103], v[68:71], v[36:39]\n"
        "v_exp_f32 v52, v52\n"
        "v_mfma_f32_16x16x32_bf16 v[36:39], a[96:99], v[64:67], v[36:39]\n"
        "v_cndmask_b32 v57, 0, v242, s[22:23]\n"
        "v_cndmask_b32 v58, 0, v242, s[24:25]\n"
        "v_cndmask_b32 v59, 0, v242, s[28:29]\n"
        "s_waitcnt lgkmcnt(3)\n"
        "s_nop 0\n"
        "v_mfma_f32_16x16x32_bf16 v[56:59], a[112:115], v[96:99], v[56:59]\n"
        "v_exp_f32 v53, v53\n"
        "v_mfma_f32_16x16x32_bf16 v[56:59], a[116:119], v[100:103], v[56:59]\n"
        "s_waitcnt lgkmcnt(2)\n"
        "v_exp_f32 v54, v54\n"
        "v_mfma_f32_16x16x32_bf16 v[56:59], a[120:123], v[104:107], v[56:59]\n"
        "s_waitcnt vmcnt(2)\n"
        "v_exp_f32 v55, v55\n"
        "v_mfma_f32_16x16x32_bf16 v[56:59], a[124:127], v[108:111], v[56:59]\n"
        "s_barrier\n"
        "v_lshrrev_b32 v244, 1, v254\n"
        "v_add_u32 v244, s81, v244\n"
        "s_cmp_lg_u32 s40, 0\n"
        "s_cbranch_scc1 LW1_%=\n"
        "s_mov_b64 exec, 0xffff\n"
        "global_store_dwordx2 v244, v[250:251], s[48:49] sc1\n"
        "s_mov_b64 exec, -1\n"
        "LW1_%=:\n"
        "v_bfe_u32 v245, v254, 4, 3\n"
        "v_lshl_add_u32 v245, v245, 7, s80\n"
        "v_cvt_pk_bf16_f32 v82, v48, v49\n"
        "v_cvt_pk_bf16_f32 v83, v50, v51\n"
        "s_nop 1\n"
        "v_mfma_f32_16x16x32_bf16 v[164:167], a[148:151], v[80:83], v[164:167]\n"
        "s_waitcnt lgkmcnt(0)\n"
        "v_exp_f32 v32, v32\n"
        "v_mfma_f32_16x16x32_bf16 v[48:51], a[112:115], v[0:3], v[224:227]\n"
        "s_barrier\n"
        "v_mfma_f32_16x16x32_bf16 v[48:51], a[116:119], v[4:7], v[48:51]\n"
        "s_add_u32 s81, s78, 4096\n"
        "v_exp_f32 v33, v33\n"
        "v_mfma_f32_16x16x32_bf16 v[48:51], a[120:123], v[8:11], v[48:51]\n"
        "s_mov_b32 s80, s78\n"
        "v_exp_f32 v34, v34\n"
        "v_mfma_f32_16x16x32_bf16 v[48:51], a[124:127], v[12:15], v[48:51]\n"
        "s_xor_b32 s78, s78, 0x8000\n"
        "v_exp_f32 v35, v35\n"
        "v_mfma_f32_16x16x32_bf16 a[64:67], a[80:83], v[80:83], a[64:67]\n"
        "v_exp_f32 v36, v36\n"
        "v_mfma_f32_16x16x32_bf16 v[160:163], a[144:147], v[80:83], v[160:163]\n"
        "v_exp_f32 v37, v37\n"
        "v_mfma_f32_16x16x32_bf16 v[168:171], a[152:155], v[80:83], v[168:171]\n"
        "v_exp_f32 v38, v38\n"
        "v_mfma_f32_16x16x32_bf16 v[172:175], a[156:159], v[80:83], v[172:175]\n"
        "v_exp_f32 v48, v48\n"
        "v_mfma_f32_16x16x32_bf16 v[176:179], a[128:131], v[80:83], v[176:179]\n"
        "v_exp_f32 v49, v49\n"
        "v_mfma_f32_16x16x32_bf16 v[180:183], a[132:135], v[80:83], v[180:183]\n"
        "v_exp_f32 v50, v50\n"
        "v_mfma_f32_16x16x32_bf16 v[184:187], a[136:139], v[80:83], v[184:187]\n"
        "v_exp_f32 v51, v51\n"
        "v_mfma_f32_16x16x32_bf16 v[188:191], a[140:143], v[80:83], v[188:191]\n"
        "v_cndmask_b32 v40, 0, v242, s[10:11]\n"
        "v_cndmask_b32 v41, 0, v242, s[12:13]\n"
        "v_cndmask_b32 v42, 0, v242, s[14:15]\n"
        "v_cndmask_b32 v43, 0, v242, s[16:17]\n"
        "s_nop 1\n"
        "v_mfma_f32_16x16x32_bf16 v[40:43], a[96:99], v[96:99], v[40:43]\n"
        "ds_read_b128 a[96:99], v240 offset:8192\n"
        "v_cvt_pk_bf16_f32 v86, v52, v53\n"
        "v_mfma_f32_16x16x32_bf16 v[40:43], a[100:103], v[100:103], v[40:43]\n"
        "ds_read_b128 a[100:103], v240 offset:9216\n"
        "v_cvt_pk_bf16_f32 v140, v152, v153\n"
        "v_mfma_f32_16x16x32_bf16 v[40:43], a[104:107], v[104:107], v[40:43]\n"
        "ds_read_b128 a[104:107], v240 offset:10240\n"
        "v_cvt_pk_bf16_f32 v141, v154, v155\n"
        "v_mfma_f32_16x16x32_bf16 v[40:43], a[108:111], v[108:111], v[40:43]\n"
        "s_mov_b32 exec_hi, 0x7fffffff\n"
        "global_load_lds_dwordx4 v254, s[68:69] offset:3072\n"
        "s_mov_b64 exec, -1\n"
        "v_cvt_pk_bf16_f32 v142, v156, v157\n"
        "v_cvt_pk_bf16_f32 v143, v158, v159\n"
        "v_cvt_pk_bf16_f32 v87, v54, v55\n"
        "s_nop 1\n"
        "v_mfma_f32_16x16x32_bf16 v[196:199], a[148:151], v[84:87], v[196:199]\n"
        "ds_read_b128 a[108:111], v240 offset:11264\n"
        "s_mov_b32 s91, s44\n"
        "v_xor_b32 v241, 0x8000, v241\n"
        "v_mfma_f32_16x16x32_bf16 v[52:55], a[124:127], v[76:79], v[228:231]\n"
        "ds_read_b128 a[148:151], v241 offset:1024\n"
        "v_cvt_pk_bf16_f32 v16, v32, v33\n"
        "v_mfma_f32_16x16x32_bf16 v[52:55], a[120:123], v[72:75], v[52:55]\n"
        "ds_read_b128 a[120:123], v240 offset:14336\n"
        "v_cvt_pk_bf16_f32 v17, v34, v35\n"
        "v_mfma_f32_16x16x32_bf16 v[52:55], a[116:119], v[68:71], v[52:55]\n"
        "s_waitcnt lgkmcnt(5)\n"
        "ds_read_b128 a[116:119], v240 offset:13312\n"
        "v_mfma_f32_16x16x32_bf16 v[52:55], a[112:115], v[64:67], v[52:55]\n"
        "ds_read_b128 a[112:115], v240 offset:12288\n"
        "v_cvt_pk_bf16_f32 v20, v36, v37\n"
        "v_mfma_f32_16x16x32_bf16 a[68:71], a[80:83], v[84:87], a[68:71]\n"
        "ds_read_b128 a[124:127], v240 offset:15360\n"
        "v_cvt_pk_bf16_f32 v18, v48, v49\n"
        "v_mfma_f32_16x16x32_bf16 v[192:195], a[144:147], v[84:87], v[192:195]\n"
        "ds_read_b128 a[144:147], v241 offset:0\n"
        "v_cvt_pk_bf16_f32 v19, v50, v51\n"
        "v_mfma_f32_16x16x32_bf16 v[200:203], a[152:155], v[84:87], v[200:203]\n"
        "ds_read_b128 a[152:155], v241 offset:2048\n"
        "v_max_f32 v246, v40, v41\n"
        "v_mfma_f32_16x16x32_bf16 v[204:207], a[156:159], v[84:87], v[204:207]\n"
        "s_waitcnt lgkmcnt(7)\n"
        "ds_read_b128 a[156:159], v241 offset:3072\n"
        "v_mfma_f32_16x16x32_bf16 v[32:35], a[96:99], v[0:3], v[224:227]\n"
        "s_waitcnt lgkmcnt(7)\n"
        "v_exp_f32 v39, v39\n"
        "v_mfma_f32_16x16x32_bf16 v[164:167], a[148:151], v[16:19], v[164:167]\n"
        "v_exp_f32 v52, v52\n"
        "v_mfma_f32_16x16x32_bf16 v[32:35], a[100:103], v[4:7], v[32:35]\n"
        "s_waitcnt lgkmcnt(6)\n"
        "v_exp_f32 v53, v53\n"
        "v_mfma_f32_16x16x32_bf16 v[32:35], a[104:107], v[8:11], v[32:35]\n"
        "s_waitcnt lgkmcnt(4)\n"
        "v_exp_f32 v54, v54\n"
        "v_mfma_f32_16x16x32_bf16 v[32:35], a[108:111], v[12:15], v[32:35]\n"
        "v_exp_f32 v55, v55\n"
        "v_mfma_f32_16x16x32_bf16 v[208:211], a[128:131], v[84:87], v[208:211]\n"
        "ds_read_b128 a[128:131], v241 offset:4096\n"
        "v_cvt_pk_bf16_f32 v21, v38, v39\n"
        "v_mfma_f32_16x16x32_bf16 v[36:39], a[108:111], v[76:79], v[228:231]\n"
        "v_max3_f32 v246, v42, v43, v246\n"
        "v_mov_b32 v247, v246\n"
        "v_mfma_f32_16x16x32_bf16 v[36:39], a[104:107], v[72:75], v[36:39]\n"
        "v_exp_f32 v32, v32\n"
        "v_mfma_f32_16x16x32_bf16 v[36:39], a[100:103], v[68:71], v[36:39]\n"
        "v_exp_f32 v33, v33\n"
        "v_mfma_f32_16x16x32_bf16 v[36:39], a[96:99], v[64:67], v[36:39]\n"
        "s_waitcnt lgkmcnt(1)\n"
        "v_exp_f32 v34, v34\n"
        "v_mfma_f32_16x16x32_bf16 v[212:215], a[132:135], v[84:87], v[212:215]\n"
        "ds_read_b128 a[132:135], v241 offset:5120\n"
        "v_cvt_pk_bf16_f32 v22, v52, v53\n"
        "v_mfma_f32_16x16x32_bf16 v[216:219], a[136:139], v[84:87], v[216:219]\n"
        "ds_read_b128 a[136:139], v241 offset:6144\n"
        "v_cvt_pk_bf16_f32 v23, v54, v55\n"
        "v_mfma_f32_16x16x32_bf16 v[220:223], a[140:143], v[84:87], v[220:223]\n"
        "ds_read_b128 a[140:143], v241 offset:7168\n"
        "v_cvt_pk_bf16_f32 v80, v32, v33\n"
        "v_mfma_f32_16x16x32_bf16 v[48:51], a[112:115], v[0:3], v[224:227]\n"
        "v_exp_f32 v35, v35\n"
        "v_mfma_f32_16x16x32_bf16 v[48:51], a[116:119], v[4:7], v[48:51]\n"
        "v_exp_f32 v36, v36\n"
        "v_mfma_f32_16x16x32_bf16 v[48:51], a[120:123], v[8:11], v[48:51]\n"
        "v_exp_f32 v37, v37\n"
        "v_mfma_f32_16x16x32_bf16 v[48:51], a[124:127], v[12:15], v[48:51]\n"
        "v_exp_f32 v38, v38\n"
        "v_mfma_f32_16x16x32_bf16 v[52:55], a[124:127], v[76:79], v[228:231]\n"
        "v_exp_f32 v39, v39\n"
        "v_mfma_f32_16x16x32_bf16 v[52:55], a[120:123], v[72:75], v[52:55]\n"
        "v_permlane16_swap_b32 v246, v247\n"
        "v_mfma_f32_16x16x32_bf16 v[52:55], a[116:119], v[68:71], v[52:55]\n"
        "v_max_f32 v246, v246, v247\n"
        "v_mov_b32 v247, v246\n"
        "v_mfma_f32_16x16x32_bf16 v[52:55], a[112:115], v[64:67], v[52:55]\n"
        "v_exp_f32 v48, v48\n"
        "v_mfma_f32_16x16x32_bf16 a[64:67], a[80:83], v[16:19], a[64:67]\n"
        "v_exp_f32 v49, v49\n"
        "v_mfma_f32_16x16x32_bf16 a[68:71], a[80:83], v[20:23], a[68:71]\n"
        "v_exp_f32 v50, v50\n"
        "v_mfma_f32_16x16x32_bf16 v[160:163], a[144:147], v[16:19], v[160:163]\n"
        "v_exp_f32 v51, v51\n"
        "v_mfma_f32_16x16x32_bf16 v[192:195], a[144:147], v[20:23], v[192:195]\n"
        "s_waitcnt lgkmcnt(0)\n"
        "v_exp_f32 v52, v52\n"
        "s_mov_b64 exec, 7\n"
        "global_load_lds_dword v252, s[68:69] offset:4080\n"
        "s_mov_b64 exec, -1\n"
        "s_add_u32 m0, s80, 0x0\n"
        "v_mfma_f32_16x16x32_bf16 v[196:199], a[148:151], v[20:23], v[196:199]\n"
        "s_mov_b32 s80, s78\n"
        "v_exp_f32 v53, v53\n"
        "v_mfma_f32_16x16x32_bf16 v[168:171], a[152:155], v[16:19], v[168:171]\n"
        "v_exp_f32 v54, v54\n"
        "v_mfma_f32_16x16x32_bf16 v[200:203], a[152:155], v[20:23], v[200:203]\n"
        "v_exp_f32 v55, v55\n"
        "v_mfma_f32_16x16x32_bf16 v[204:207], a[156:159], v[20:23], v[204:207]\n"
        "global_load_dwordx2 v[252:253], v245, s[48:49] sc1\n"
        "v_cvt_pk_bf16_f32 v81, v34, v35\n"
        "v_mfma_f32_16x16x32_bf16 v[172:175], a[156:159], v[16:19], v[172:175]\n"
        "v_permlane32_swap_b32 v246, v247\n"
        "v_max_f32 v246, v246, v247\n"
        "v_sub_f32 v232, 0, v246\n"
        "v_sub_f32 v233, 0, v246\n"
        "v_sub_f32 v234, 0, v246\n"
        "v_sub_f32 v235, 0, v246\n"
        "v_add_f32 v40, v40, v232\n"
        "v_exp_f32 v40, v40\n"
        "v_add_f32 v41, v41, v232\n"
        "v_exp_f32 v41, v41\n"
        "v_add_f32 v42, v42, v232\n"
        "v_cvt_pk_bf16_f32 v24, v40, v41\n"
        "v_exp_f32 v42, v42\n"
        "v_add_f32 v43, v43, v232\n"
        "v_exp_f32 v43, v43\n"
        "v_mfma_f32_16x16x32_bf16 v[176:179], a[128:131], v[16:19], v[176:179]\n"
        "s_add_u32 s68, s64, s90\n"
        "s_addc_u32 s69, s65, 0\n"
        "v_cvt_pk_bf16_f32 v25, v42, v43\n"
        "v_mfma_f32_16x16x32_bf16 v[208:211], a[128:131], v[20:23], v[208:211]\n"
        "s_add_u32 s74, s68, 0x1000\n"
        "s_addc_u32 s75, s69, 0\n"
        "v_cndmask_b32 v40, v232, v242, s[30:31]\n"
        "v_mfma_f32_16x16x32_bf16 v[212:215], a[132:135], v[20:23], v[212:215]\n"
        "s_cmp_ge_u32 s82, s44\n"
        "s_addc_u32 s90, s82, 0\n"
        "v_cndmask_b32 v41, v232, v242, s[56:57]\n"
        "v_mfma_f32_16x16x32_bf16 v[180:183], a[132:135], v[16:19], v[180:183]\n"
        "s_lshl_b32 s90, s90, 15\n"
        "v_cndmask_b32 v42, v232, v242, s[58:59]\n"
        "v_cndmask_b32 v43, v232, v242, s[60:61]\n"
        "s_nop 1\n"
        "v_mfma_f32_16x16x32_bf16 v[40:43], a[96:99], v[96:99], v[40:43]\n"
        "ds_read_b128 a[96:99], v240 offset:16384\n"
        "v_add_f32 v56, v56, v232\n"
        "v_mfma_f32_16x16x32_bf16 v[40:43], a[100:103], v[100:103], v[40:43]\n"
        "s_mov_b64 exec, 0\n"
        "s_cmp_eq_u32 s40, 0\n"
        "s_cbranch_scc1 LHC0_%=\n"
        "s_cmp_eq_u32 s40, 1\n"
        "s_cbranch_scc1 LHC1_%=\n"
        "s_cmp_eq_u32 s40, 2\n"
        "s_cbranch_scc1 LHC2_%=\n"
        "s_branch LHC3_%=\n"
        "LHRC_%=:\n"
        "s_mov_b64 exec, -1\n"
        "v_mfma_f32_16x16x32_bf16 v[40:43], a[104:107], v[104:107], v[40:43]\n"
        "ds_read_b128 a[100:103], v240 offset:17408\n"
        "v_add_f32 v57, v57, v232\n"
        "v_mfma_f32_16x16x32_bf16 v[40:43], a[108:111], v[108:111], v[40:43]\n"
        "v_exp_f32 v56, v56\n"
        "v_mfma_f32_16x16x32_bf16 v[184:187], a[136:139], v[16:19], v[184:187]\n"
        "v_exp_f32 v57, v57\n"
        "v_mfma_f32_16x16x32_bf16 v[216:219], a[136:139], v[20:23], v[216:219]\n"
        "ds_read_b128 a[104:107], v240 offset:18432\n"
        "v_cvt_pk_bf16_f32 v26, v56, v57\n"
        "v_mfma_f32_16x16x32_bf16 v[220:223], a[140:143], v[20:23], v[220:223]\n"
        "ds_read_b128 a[108:111], v240 offset:19456\n"
        "v_add_f32 v58, v58, v232\n"
        "v_mfma_f32_16x16x32_bf16 v[188:191], a[140:143], v[16:19], v[188:191]\n"
        "s_waitcnt lgkmcnt(3)\n"
        "v_exp_f32 v58, v58\n"
        "v_mfma_f32_16x16x32_bf16 v[32:35], a[96:99], v[0:3], v[224:227]\n"
        "LHA11_%=:\n"
        "s_cbranch_execz LHRA_%=\n"
        "v_add_f32 v59, v59, v232\n"
        "v_exp_f32 v59, v59\n"
        "s_waitcnt lgkmcnt(2)\n"
        "v_cvt_pk_bf16_f32 v27, v58, v59\n"
        "s_nop 1\n"
        "v_mfma_f32_16x16x32_bf16 a[72:75], a[80:83], v[24:27], 0\n"
        "v_exp_f32 v40, v40\n"
        "v_mfma_f32_16x16x32_bf16 a[0:3], a[144:147], v[24:27], 0\n"
        "ds_read_b128 a[144:147], v241 offset:8192\n"
        "v_cndmask_b32 v56, v232, v242, s[62:63]\n"
        "v_mfma_f32_16x16x32_bf16 a[4:7], a[148:151], v[24:27], 0\n"
        "ds_read_b128 a[148:151], v241 offset:9216\n"
        "v_cndmask_b32 v57, v232, v242, s[84:85]\n"
        "v_mfma_f32_16x16x32_bf16 a[8:11], a[152:155], v[24:27], 0\n"
        "ds_read_b128 a[152:155], v241 offset:10240\n"
        "v_cndmask_b32 v58, v232, v242, s[86:87]\n"
        "s_waitcnt vmcnt(0) lgkmcnt(3)\n"
        "v_mfma_f32_16x16x32_bf16 a[12:15], a[156:159], v[24:27], 0\n"
        "ds_read_b128 a[156:159], v241 offset:11264\n"
        "v_cmp_ne_u64 vcc, v[252:253], v[250:251]\n"
        "v_mfma_f32_16x16x32_bf16 v[32:35], a[100:103], v[4:7], v[32:35]\n"
        "s_cbranch_vccnz LPKV_%=\n"
        "LBKV_%=:\n"
        "v_exp_f32 v41, v41\n"
        "v_mfma_f32_16x16x32_bf16 v[32:35], a[104:107], v[8:11], v[32:35]\n"
        "v_exp_f32 v42, v42\n"
        "v_mfma_f32_16x16x32_bf16 v[32:35], a[108:111], v[12:15], v[32:35]\n"
        "v_exp_f32 v43, v43\n"
        "v_mfma_f32_16x16x32_bf16 a[16:19], a[128:131], v[24:27], 0\n"
        "ds_read_b128 a[128:131], v241 offset:12288\n"
        "v_cndmask_b32 v59, v232, v242, s[88:89]\n"
        "s_nop 1\n"
        "v_mfma_f32_16x16x32_bf16 v[56:59], a[112:115], v[96:99], v[56:59]\n"
        "ds_read_b128 a[112:115], v240 offset:20480\n"
        "v_cvt_pk_bf16_f32 v84, v36, v37\n"
        "v_mfma_f32_16x16x32_bf16 v[56:59], a[116:119], v[100:103], v[56:59]\n"
        "ds_read_b128 a[116:119], v240 offset:21504\n"
        "v_cvt_pk_bf16_f32 v85, v38, v39\n"
        "v_mfma_f32_16x16x32_bf16 v[56:59], a[120:123], v[104:107], v[56:59]\n"
        "ds_read_b128 a[120:123], v240 offset:22528\n"
        "v_cvt_pk_bf16_f32 v88, v40, v41\n"
        "v_mfma_f32_16x16x32_bf16 v[56:59], a[124:127], v[108:111], v[56:59]\n"
        "ds_read_b128 a[124:127], v240 offset:23552\n"
        "global_load_lds_dwordx4 v254, s[68:69] offset:0 sc1\n"
        "v_mfma_f32_16x16x32_bf16 v[36:39], a[108:111], v[76:79], v[228:231]\n"
        "v_exp_f32 v32, v32\n"
        "v_mfma_f32_16x16x32_bf16 v[36:39], a[104:107], v[72:75], v[36:39]\n"
        "v_exp_f32 v33, v33\n"
        "v_mfma_f32_16x16x32_bf16 v[36:39], a[100:103], v[68:71], v[36:39]\n"
        "v_exp_f32 v34, v34\n"
        "v_mfma_f32_16x16x32_bf16 v[36:39], a[96:99], v[64:67], v[36:39]\n"
        "v_exp_f32 v56, v56\n"
        "v_mfma_f32_16x16x32_bf16 a[20:23], a[132:135], v[24:27], 0\n"
        "ds_read_b128 a[132:135], v241 offset:13312\n"
        "v_and_b32 v245, 0x70, v254\n"
        "v_mfma_f32_16x16x32_bf16 a[24:27], a[136:139], v[24:27], 0\n"
        "ds_read_b128 a[136:139], v241 offset:14336\n"
        "v_lshlrev_b32 v245, 3, v245\n"
        "v_mfma_f32_16x16x32_bf16 a[28:31], a[140:143], v[24:27], 0\n"
        "v_cvt_pk_bf16_f32 v89, v42, v43\n"
        "v_cvt_pk_bf16_f32 v82, v48, v49\n"
        "v_mfma_f32_16x16x32_bf16 v[40:43], a[96:99], v[96:99], v[232:235]\n"
        "ds_read_b128 a[140:143], v241 offset:15360\n"
        "global_load_lds_dwordx4 v254, s[68:69] offset:1024 sc1\n"
        "v_mfma_f32_16x16x32_bf16 v[40:43], a[100:103], v[100:103], v[40:43]\n"
        "v_exp_f32 v57, v57\n"
        "v_mfma_f32_16x16x32_bf16 v[40:43], a[104:107], v[104:107], v[40:43]\n"
        "v_exp_f32 v58, v58\n"
        "v_mfma_f32_16x16x32_bf16 v[40:43], a[108:111], v[108:111], v[40:43]\n"
        "v_cvt_pk_bf16_f32 v83, v50, v51\n"
        "s_waitcnt lgkmcnt(3)\n"
        "v_mfma_f32_16x16x32_bf16 v[48:51], a[112:115], v[0:3], v[224:227]\n"
        "v_exp_f32 v59, v59\n"
        "v_mfma_f32_16x16x32_bf16 v[48:51], a[116:119], v[4:7], v[48:51]\n"
        "v_exp_f32 v35, v35\n"
        "v_mfma_f32_16x16x32_bf16 v[48:51], a[120:123], v[8:11], v[48:51]\n"
        "v_exp_f32 v36, v36\n"
        "v_mfma_f32_16x16x32_bf16 v[48:51], a[124:127], v[12:15], v[48:51]\n"
        "v_exp_f32 v37, v37\n"
        "v_mfma_f32_16x16x32_bf16 a[64:67], a[80:83], v[80:83], a[64:67]\n"
        "s_waitcnt lgkmcnt(0)\n"
        "v_exp_f32 v38, v38\n"
        "v_mfma_f32_16x16x32_bf16 v[160:163], a[144:147], v[80:83], v[160:163]\n"
        "v_exp_f32 v39, v39\n"
        "v_mfma_f32_16x16x32_bf16 v[164:167], a[148:151], v[80:83], v[164:167]\n"
        "v_exp_f32 v40, v40\n"
        "v_mfma_f32_16x16x32_bf16 v[168:171], a[152:155], v[80:83], v[168:171]\n"
        "v_exp_f32 v41, v41\n"
        "v_mfma_f32_16x16x32_bf16 v[172:175], a[156:159], v[80:83], v[172:175]\n"
        "v_exp_f32 v42, v42\n"
        "global_load_lds_dwordx4 v254, s[68:69] offset:2048 sc1\n"
        "v_cvt_pk_bf16_f32 v86, v52, v53\n"
        "v_mfma_f32_16x16x32_bf16 v[176:179], a[128:131], v[80:83], v[176:179]\n"
        "v_exp_f32 v43, v43\n"
        "v_cndmask_b32 v44, 0, v242, s[10:11]\n"
        "v_cndmask_b32 v45, 0, v242, s[12:13]\n"
        "v_cndmask_b32 v46, 0, v242, s[14:15]\n"
        "v_cndmask_b32 v47, 0, v242, s[16:17]\n"
        "s_nop 1\n"
        "v_mfma_f32_16x16x32_bf16 v[44:47], a[108:111], v[140:143], v[44:47]\n"
        "ds_read_b128 a[108:111], v240 offset:27648\n"
        "s_and_b32 s10, s41, 1\n"
        "v_cvt_pk_bf16_f32 v87, v54, v55\n"
        "v_mfma_f32_16x16x32_bf16 v[44:47], a[104:107], v[136:139], v[44:47]\n"
        "ds_read_b128 a[104:107], v240 offset:26624\n"
        "global_load_lds_dwordx4 v254, s[68:69] offset:3072 sc1\n"
        "s_add_u32 m0, s81, 0x0\n"
        "v_mfma_f32_16x16x32_bf16 v[44:47], a[100:103], v[132:135], v[44:47]\n"
        "ds_read_b128 a[100:103], v240 offset:25600\n"
        "s_add_u32 s81, s78, 4096\n"
        "v_cvt_pk_bf16_f32 v90, v56, v57\n"
        "v_mfma_f32_16x16x32_bf16 v[44:47], a[96:99], v[128:131], v[44:47]\n"
        "ds_read_b128 a[96:99], v240 offset:24576\n"
        "s_and_b32 s11, s41, 4\n"
        "v_cvt_pk_bf16_f32 v91, v58, v59\n"
        "v_mfma_f32_16x16x32_bf16 v[52:55], a[124:127], v[76:79], v[228:231]\n"
        "s_and_b32 s14, s42, 3\n"
        "v_exp_f32 v48, v48\n"
        "v_mfma_f32_16x16x32_bf16 v[52:55], a[120:123], v[72:75], v[52:55]\n"
        "s_add_u32 s11, s11, s14\n"
        "v_exp_f32 v49, v49\n"
        "v_mfma_f32_16x16x32_bf16 v[52:55], a[116:119], v[68:71], v[52:55]\n"
        "s_and_b32 s12, s41, 2\n"
        "v_exp_f32 v50, v50\n"
        "v_mfma_f32_16x16x32_bf16 v[52:55], a[112:115], v[64:67], v[52:55]\n"
        "s_lshr_b32 s14, s42, 2\n"
        "v_exp_f32 v51, v51\n"
        "v_mfma_f32_16x16x32_bf16 v[56:59], a[112:115], v[96:99], v[232:235]\n"
        "global_load_lds_dwordx4 v254, s[74:75] offset:0 sc1\n"
        "s_lshl2_add_u32 s12, s12, s14\n"
        "v_cvt_pk_bf16_f32 v16, v32, v33\n"
        "v_mfma_f32_16x16x32_bf16 v[56:59], a[116:119], v[100:103], v[56:59]\n"
        "s_add_u32 s68, s64, s90\n"
        "s_addc_u32 s69, s65, 0\n"
        "v_cvt_pk_bf16_f32 v17, v34, v35\n"
        "v_mfma_f32_16x16x32_bf16 v[56:59], a[120:123], v[104:107], v[56:59]\n"
        "s_lshl3_add_u32 s13, s12, s11\n"
        "v_exp_f32 v52, v52\n"
        "v_mfma_f32_16x16x32_bf16 v[56:59], a[124:127], v[108:111], v[56:59]\n"
        "s_lshl1_add_u32 s13, s13, s10\n"
        "v_exp_f32 v53, v53\n"
        "v_mfma_f32_16x16x32_bf16 a[68:71], a[80:83], v[84:87], a[68:71]\n"
        "s_lshl_b32 s14, s10, 22\n"
        "v_exp_f32 v54, v54\n"
        "v_mfma_f32_16x16x32_bf16 a[72:75], a[80:83], v[88:91], a[72:75]\n"
        "s_lshl_b32 s15, s40, 15\n"
        "v_exp_f32 v55, v55\n"
        "v_mfma_f32_16x16x32_bf16 v[192:195], a[144:147], v[84:87], v[192:195]\n"
        "s_add_u32 s14, s14, s15\n"
        "v_exp_f32 v56, v56\n"
        "v_mfma_f32_16x16x32_bf16 a[0:3], a[144:147], v[88:91], a[0:3]\n"
        "ds_read_b128 a[144:147], v241 offset:16384\n"
        "global_load_lds_dwordx4 v254, s[74:75] offset:1024 sc1\n"
        "s_lshl_b32 s15, s11, 8\n"
        "v_mfma_f32_16x16x32_bf16 a[4:7], a[148:151], v[88:91], a[4:7]\n"
        "s_waitcnt lgkmcnt(1)\n"
        "v_exp_f32 v57, v57\n"
        "v_mfma_f32_16x16x32_bf16 v[196:199], a[148:151], v[84:87], v[196:199]\n"
        "ds_read_b128 a[148:151], v241 offset:17408\n"
        "s_add_u32 s14, s14, s15\n"
        "v_cvt_pk_bf16_f32 v20, v36, v37\n"
        "v_mfma_f32_16x16x32_bf16 v[200:203], a[152:155], v[84:87], v[200:203]\n"
        "s_lshl_b32 s15, s10, 19\n"
        "v_exp_f32 v58, v58\n"
        "v_mfma_f32_16x16x32_bf16 a[8:11], a[152:155], v[88:91], a[8:11]\n"
        "ds_read_b128 a[152:155], v241 offset:18432\n"
        "global_load_lds_dwordx4 v254, s[74:75] offset:2048 sc1\n"
        "s_add_u32 s17, s11, s12\n"
        "v_mfma_f32_16x16x32_bf16 a[12:15], a[156:159], v[88:91], a[12:15]\n"
        "s_and_b32 s17, s17, 1\n"
        "v_exp_f32 v59, v59\n"
        "v_mfma_f32_16x16x32_bf16 v[204:207], a[156:159], v[84:87], v[204:207]\n"
        "ds_read_b128 a[156:159], v241 offset:19456\n"
        "v_cvt_pk_bf16_f32 v21, v38, v39\n"
        "v_mfma_f32_16x16x32_bf16 v[32:35], a[96:99], v[0:3], v[224:227]\n"
        "v_cndmask_b32 v60, 0, v242, s[20:21]\n"
        "v_cndmask_b32 v61, 0, v242, s[22:23]\n"
        "v_mfma_f32_16x16x32_bf16 v[32:35], a[100:103], v[4:7], v[32:35]\n"
        "global_load_lds_dwordx4 v254, s[74:75] offset:3072 sc1\n"
        "s_add_u32 m0, s79, 0x0\n"
        "v_mfma_f32_16x16x32_bf16 v[32:35], a[104:107], v[8:11], v[32:35]\n"
        "s_add_u32 s74, s68, 0x1000\n"
        "s_addc_u32 s75, s69, 0\n"
        "v_cndmask_b32 v62, 0, v242, s[24:25]\n"
        "v_mfma_f32_16x16x32_bf16 v[32:35], a[108:111], v[12:15], v[32:35]\n"
        "s_sub_u32 s24, s83, 1\n"
        "v_cndmask_b32 v63, 0, v242, s[28:29]\n"
        "s_nop 1\n"
        "v_mfma_f32_16x16x32_bf16 v[60:63], a[124:127], v[140:143], v[60:63]\n"
        "ds_read_b128 a[124:127], v240 offset:31744\n"
        "s_mov_b32 s25, 4096\n"
        "v_cvt_pk_bf16_f32 v24, v40, v41\n"
        "v_mfma_f32_16x16x32_bf16 v[60:63], a[120:123], v[136:139], v[60:63]\n"
        "ds_read_b128 a[120:123], v240 offset:30720\n"
        "global_load_lds_dwordx4 v254, s[76:77] offset:0 sc1\n"
        "s_mov_b32 s28, 4096\n"
        "v_mfma_f32_16x16x32_bf16 v[60:63], a[116:119], v[132:135], v[60:63]\n"
        "ds_read_b128 a[116:119], v240 offset:29696\n"
        "s_xor_b32 s29, s17, 0\n"
        "v_cvt_pk_bf16_f32 v25, v42, v43\n"
        "v_mfma_f32_16x16x32_bf16 v[60:63], a[112:115], v[128:131], v[60:63]\n"
        "ds_read_b128 a[112:115], v240 offset:28672\n"
        "v_max_f32 v248, v44, v45\n"
        "v_mfma_f32_16x16x32_bf16 v[36:39], a[108:111], v[76:79], v[228:231]\n"
        "v_exp_f32 v32, v32\n"
        "v_mfma_f32_16x16x32_bf16 v[36:39], a[104:107], v[72:75], v[36:39]\n"
        "v_exp_f32 v33, v33\n"
        "v_mfma_f32_16x16x32_bf16 v[36:39], a[100:103], v[68:71], v[36:39]\n"
        "v_exp_f32 v34, v34\n"
        "v_mfma_f32_16x16x32_bf16 v[36:39], a[96:99], v[64:67], v[36:39]\n"
        "v_exp_f32 v35, v35\n"
        "v_mfma_f32_16x16x32_bf16 v[40:43], a[96:99], v[96:99], v[232:235]\n"
        "global_load_lds_dwordx4 v254, s[76:77] offset:1024 sc1\n"
        "v_max3_f32 v248, v46, v47, v248\n"
        "v_mfma_f32_16x16x32_bf16 v[40:43], a[100:103], v[100:103], v[40:43]\n"
        "s_add_u32 s22, s18, s14\n"
        "s_addc_u32 s23, s19, 0\n"
        "v_mov_b32 v249, v248\n"
        "v_mfma_f32_16x16x32_bf16 v[40:43], a[104:107], v[104:107], v[40:43]\n"
        "s_lshl_b32 s14, s12, 20\n"
        "v_exp_f32 v36, v36\n"
        "v_mfma_f32_16x16x32_bf16 v[40:43], a[108:111], v[108:111], v[40:43]\n"
        "s_add_u32 s14, s14, s15\n"
        "v_exp_f32 v37, v37\n"
        "v_mfma_f32_16x16x32_bf16 v[208:211], a[128:131], v[84:87], v[208:211]\n"
        "s_add_u32 s14, s14, s73\n"
        "v_exp_f32 v38, v38\n"
        "v_mfma_f32_16x16x32_bf16 a[16:19], a[128:131], v[88:91], a[16:19]\n"
        "ds_read_b128 a[128:131], v241 offset:20480\n"
        "global_load_lds_dwordx4 v254, s[76:77] offset:2048 sc1\n"
        "s_add_u32 s14, s14, 0x2000000\n"
        "v_mfma_f32_16x16x32_bf16 a[20:23], a[132:135], v[88:91], a[20:23]\n"
        "v_exp_f32 v39, v39\n"
        "v_mfma_f32_16x16x32_bf16 v[212:215], a[132:135], v[84:87], v[212:215]\n"
        "v_exp_f32 v40, v40\n"
        "v_mfma_f32_16x16x32_bf16 v[180:183], a[132:135], v[80:83], v[180:183]\n"
        "ds_read_b128 a[132:135], v241 offset:21504\n"
        "v_accvgpr_write_b32 a48, 0\n"
        "v_mfma_f32_16x16x32_bf16 v[184:187], a[136:139], v[80:83], v[184:187]\n"
        "v_exp_f32 v41, v41\n"
        "v_mfma_f32_16x16x32_bf16 v[216:219], a[136:139], v[84:87], v[216:219]\n"
        "v_exp_f32 v42, v42\n"
        "v_mfma_f32_16x16x32_bf16 a[24:27], a[136:139], v[88:91], a[24:27]\n"
        "ds_read_b128 a[136:139], v241 offset:22528\n"
        "global_load_lds_dwordx4 v254, s[76:77] offset:3072 sc1\n"
        "s_add_u32 m0, s79, 0x1000\n"
        "v_mfma_f32_16x16x32_bf16 a[28:31], a[140:143], v[88:91], a[28:31]\n"
        "v_exp_f32 v43, v43\n"
        "v_mfma_f32_16x16x32_bf16 v[220:223], a[140:143], v[84:87], v[220:223]\n"
        "s_add_u32 s76, s76, 0x1000\n"
        "s_addc_u32 s77, s77, 0\n"
        "v_accvgpr_write_b32 a49, 0\n"
        "v_mfma_f32_16x16x32_bf16 v[188:191], a[140:143], v[80:83], v[188:191]\n"
        "v_permlane16_swap_b32 v248, v249\n"
        "v_max_f32 v248, v248, v249\n"
        "global_load_lds_dwordx4 v254, s[76:77] offset:0 sc1\n"
        "v_mov_b32 v249, v248\n"
        "v_accvgpr_write_b32 a50, 0\n"
        "v_accvgpr_write_b32 a51, 0\n"
        "v_permlane32_swap_b32 v248, v249\n"
        "v_max_f32 v248, v248, v249\n"
        "v_sub_f32 v236, 0, v248\n"
        "v_sub_f32 v237, 0, v248\n"
        "v_sub_f32 v238, 0, v248\n"
        "v_sub_f32 v239, 0, v248\n"
        "v_add_f32 v44, v44, v236\n"
        "v_exp_f32 v44, v44\n"
        "v_add_f32 v45, v45, v236\n"
        "global_load_lds_dwordx4 v254, s[76:77] offset:1024 sc1\n"
        "v_exp_f32 v45, v45\n"
        "v_add_f32 v46, v46, v236\n"
        "v_cvt_pk_bf16_f32 v28, v44, v45\n"
        "v_exp_f32 v46, v46\n"
        "v_add_f32 v47, v47, v236\n"
        "v_exp_f32 v47, v47\n"
        "v_accvgpr_write_b32 a52, 0\n"
        "v_cvt_pk_bf16_f32 v29, v46, v47\n"
        "v_accvgpr_write_b32 a53, 0\n"
        "v_accvgpr_write_b32 a54, 0\n"
        "global_load_lds_dwordx4 v254, s[76:77] offset:2048 sc1\n"
        "v_accvgpr_write_b32 a55, 0\n"
        "v_accvgpr_write_b32 a56, 0\n"
        "v_accvgpr_write_b32 a57, 0\n"
        "v_accvgpr_write_b32 a58, 0\n"
        "v_accvgpr_write_b32 a59, 0\n"
        "v_accvgpr_write_b32 a60, 0\n"
        "v_accvgpr_write_b32 a61, 0\n"
        "v_accvgpr_write_b32 a62, 0\n"
        "v_accvgpr_write_b32 a63, 0\n"
        "v_cvt_pk_bf16_f32 v18, v48, v49\n"
        "v_cvt_pk_bf16_f32 v19, v50, v51\n"
        "s_waitcnt lgkmcnt(0)\n"
        "v_mfma_f32_16x16x32_bf16 v[48:51], a[112:115], v[0:3], v[224:227]\n"
        "ds_read_b128 a[140:143], v241 offset:23552\n"
        "global_load_lds_dwordx4 v254, s[76:77] offset:3072 sc1\n"
        "s_add_u32 m0, s80, 0x0\n"
        "v_mfma_f32_16x16x32_bf16 v[48:51], a[116:119], v[4:7], v[48:51]\n"
        "s_sub_u32 s22, s22, 4096\n"
        "s_subb_u32 s23, s23, 0\n"
        "v_cvt_pk_bf16_f32 v22, v52, v53\n"
        "v_mfma_f32_16x16x32_bf16 v[48:51], a[120:123], v[8:11], v[48:51]\n"
        "s_add_u32 s20, s38, s14\n"
        "s_addc_u32 s21, s39, 0\n"
        "v_cvt_pk_bf16_f32 v23, v54, v55\n"
        "v_mfma_f32_16x16x32_bf16 v[48:51], a[124:127], v[12:15], v[48:51]\n"
        "v_cndmask_b32 v44, v236, v242, s[30:31]\n"
        "v_cndmask_b32 v45, v236, v242, s[56:57]\n"
        "v_mfma_f32_16x16x32_bf16 v[52:55], a[124:127], v[76:79], v[228:231]\n"
        "s_xor_b32 s30, s17, 4\n"
        "v_cndmask_b32 v46, v236, v242, s[58:59]\n"
        "v_cndmask_b32 v47, v236, v242, s[60:61]\n"
        "s_nop 1\n"
        "v_mfma_f32_16x16x32_bf16 v[44:47], a[108:111], v[140:143], v[44:47]\n"
        "global_load_dword v244, v245, s[26:27] offset:0\n"
        "s_sub_u32 s30, s30, s29\n"
        "v_cvt_pk_bf16_f32 v26, v56, v57\n"
        "v_mfma_f32_16x16x32_bf16 v[44:47], a[104:107], v[136:139], v[44:47]\n"
        "s_sub_u32 s26, s26, 262144\n"
        "s_subb_u32 s27, s27, 0\n"
        "v_xor_b32 v240, 0x8000, v240\n"
        "v_mfma_f32_16x16x32_bf16 v[44:47], a[100:103], v[132:135], v[44:47]\n"
        "global_load_dword v244, v245, s[26:27] offset:0\n"
        "s_lshl_b32 s29, s29, 17\n"
        "v_cvt_pk_bf16_f32 v27, v58, v59\n"
        "v_mfma_f32_16x16x32_bf16 v[44:47], a[96:99], v[128:131], v[44:47]\n"
        "s_sub_u32 s26, s26, 262144\n"
        "s_subb_u32 s27, s27, 0\n"
        "v_add_f32 v60, v60, v236\n"
        "v_mfma_f32_16x16x32_bf16 v[52:55], a[120:123], v[72:75], v[52:55]\n"
        "s_lshl_b32 s30, s30, 17\n"
        "v_exp_f32 v60, v60\n"
        "v_mfma_f32_16x16x32_bf16 v[52:55], a[116:119], v[68:71], v[52:55]\n"
        "v_add_f32 v61, v61, v236\n"
        "v_add_f32 v62, v62, v236\n"
        "v_mfma_f32_16x16x32_bf16 v[52:55], a[112:115], v[64:67], v[52:55]\n"
        "v_exp_f32 v61, v61\n"
        "v_mfma_f32_16x16x32_bf16 v[56:59], a[112:115], v[96:99], v[232:235]\n"
        "v_exp_f32 v62, v62\n"
        "v_mfma_f32_16x16x32_bf16 v[56:59], a[116:119], v[100:103], v[56:59]\n"
        "v_exp_f32 v44, v44\n"
        "v_mfma_f32_16x16x32_bf16 v[56:59], a[120:123], v[104:107], v[56:59]\n"
        "v_exp_f32 v45, v45\n"
        "v_mfma_f32_16x16x32_bf16 v[56:59], a[124:127], v[108:111], v[56:59]\n"
        "v_exp_f32 v46, v46\n"
        "v_mfma_f32_16x16x32_bf16 a[64:67], a[80:83], v[16:19], a[64:67]\n"
        "v_exp_f32 v47, v47\n"
        "v_mfma_f32_16x16x32_bf16 a[68:71], a[80:83], v[20:23], a[68:71]\n"
        "s_waitcnt vmcnt(10)\n"
        "v_mfma_f32_16x16x32_bf16 a[72:75], a[80:83], v[24:27], a[72:75]\n"
        "s_barrier\n"
        "v_mfma_f32_16x16x32_bf16 v[160:163], a[144:147], v[16:19], v[160:163]\n"
        "ds_read_b128 a[96:99], v240 offset:0\n"
        "global_load_lds_dwordx4 v254, s[68:69] offset:0 sc1\n"
        "v_mfma_f32_16x16x32_bf16 v[192:195], a[144:147], v[20:23], v[192:195]\n"
        "ds_read_b128 a[100:103], v240 offset:1024\n"
        "v_cvt_pk_bf16_f32 v30, v60, v61\n"
        "v_mfma_f32_16x16x32_bf16 a[0:3], a[144:147], v[24:27], a[0:3]\n"
        "ds_read_b128 a[104:107], v240 offset:2048\n"
        "v_add_f32 v63, v63, v236\n"
        "v_mfma_f32_16x16x32_bf16 a[4:7], a[148:151], v[24:27], a[4:7]\n"
        "v_exp_f32 v63, v63\n"
        "v_mfma_f32_16x16x32_bf16 v[196:199], a[148:151], v[20:23], v[196:199]\n"
        "ds_read_b128 a[108:111], v240 offset:3072\n"
        "global_load_lds_dwordx4 v254, s[68:69] offset:1024 sc1\n"
        "v_mfma_f32_16x16x32_bf16 v[164:167], a[148:151], v[16:19], v[164:167]\n"
        "v_cvt_pk_bf16_f32 v31, v62, v63\n"
        "s_nop 1\n"
        "v_mfma_f32_16x16x32_bf16 a[76:79], a[80:83], v[28:31], 0\n"
        "v_cvt_pk_bf16_f32 v80, v32, v33\n"
        "v_cvt_pk_bf16_f32 v81, v34, v35\n"
        "v_mfma_f32_16x16x32_bf16 a[32:35], a[144:147], v[28:31], 0\n"
        "ds_read_b128 a[144:147], v241 offset:24576\n"
        "v_cvt_pk_bf16_f32 v84, v36, v37\n"
        "v_mfma_f32_16x16x32_bf16 a[36:39], a[148:151], v[28:31], 0\n"
        "ds_read_b128 a[148:151], v241 offset:25600\n"
        "global_load_lds_dwordx4 v254, s[68:69] offset:2048 sc1\n"
        "v_mfma_f32_16x16x32_bf16 v[168:171], a[152:155], v[16:19], v[168:171]\n"
        "s_waitcnt lgkmcnt(5)\n"
        "v_cndmask_b32 v60, v236, v242, s[62:63]\n"
        "v_cndmask_b32 v61, v236, v242, s[84:85]\n"
        "v_mfma_f32_16x16x32_bf16 v[200:203], a[152:155], v[20:23], v[200:203]\n"
        "v_cndmask_b32 v62, v236, v242, s[86:87]\n"
        "v_cndmask_b32 v63, v236, v242, s[88:89]\n"
        "s_nop 1\n"
        "v_mfma_f32_16x16x32_bf16 v[60:63], a[124:127], v[140:143], v[60:63]\n"
        "v_cvt_pk_bf16_f32 v85, v38, v39\n"
        "v_cvt_pk_bf16_f32 v88, v40, v41\n"
        "v_mfma_f32_16x16x32_bf16 v[60:63], a[120:123], v[136:139], v[60:63]\n"
        "ds_read_b128 a[120:123], v240 offset:6144\n"
        "global_load_lds_dwordx4 v254, s[68:69] offset:3072 sc1\n"
        "v_mfma_f32_16x16x32_bf16 v[60:63], a[116:119], v[132:135], v[60:63]\n"
        "ds_read_b128 a[116:119], v240 offset:5120\n"
        "v_cvt_pk_bf16_f32 v89, v42, v43\n"
        "v_mfma_f32_16x16x32_bf16 v[60:63], a[112:115], v[128:131], v[60:63]\n"
        "ds_read_b128 a[112:115], v240 offset:4096\n"
        "v_cvt_pk_bf16_f32 v92, v44, v45\n"
        "v_mfma_f32_16x16x32_bf16 a[8:11], a[152:155], v[24:27], a[8:11]\n"
        "v_cvt_pk_bf16_f32 v93, v46, v47\n"
        "v_mov_b32 v144, v254\n"
        "v_mfma_f32_16x16x32_bf16 a[40:43], a[152:155], v[28:31], 0\n"
        "ds_read_b128 a[152:155], v241 offset:26624\n"
        "v_mov_b32 v145, v254\n"
        "v_mfma_f32_16x16x32_bf16 a[44:47], a[156:159], v[28:31], 0\n"
        "v_mov_b32 v146, v254\n"
        "v_mov_b32 v147, v254\n"
        "v_mfma_f32_16x16x32_bf16 a[12:15], a[156:159], v[24:27], a[12:15]\n"
        "v_mfma_f32_16x16x32_bf16 v[204:207], a[156:159], v[20:23], v[204:207]\n"
        "v_mfma_f32_16x16x32_bf16 v[172:175], a[156:159], v[16:19], v[172:175]\n"
        "s_cmp_eq_u32 s91, 0\n"
        "s_cbranch_scc1 LLD_%=\n"
        "; ---- DUAL\n"
        "s_nop 0\n"
        "s_nop 0\n"
        "s_nop 0\n"
        "s_nop 0\n"
        "s_nop 0\n"
        "s_nop 0\n"
        "s_nop 0\n"
        "LDU_%=:\n"
        "v_mfma_f32_16x16x32_bf16 v[32:35], a[96:99], v[0:3], v[224:227]\n"
        "s_waitcnt lgkmcnt(2)\n"
        "ds_read_b128 a[156:159], v241 offset:27648\n"
        "s_add_u32 m0, s81, 0x0\n"
        "v_mfma_f32_16x16x32_bf16 v[32:35], a[100:103], v[4:7], v[32:35]\n"
        "ds_read_b128 a[124:127], v240 offset:7168\n"
        "global_load_lds_dwordx4 v254, s[74:75] offset:0 sc1\n"
        "s_xor_b32 s79, s79, 0x8000\n"
        "v_mfma_f32_16x16x32_bf16 v[32:35], a[104:107], v[8:11], v[32:35]\n"
        "v_exp_f32 v48, v48\n"
        "v_mfma_f32_16x16x32_bf16 v[32:35], a[108:111], v[12:15], v[32:35]\n"
        "v_exp_f32 v49, v49\n"
        "v_mfma_f32_16x16x32_bf16 v[36:39], a[108:111], v[76:79], v[228:231]\n"
        "v_exp_f32 v50, v50\n"
        "v_mfma_f32_16x16x32_bf16 v[36:39], a[104:107], v[72:75], v[36:39]\n"
        "v_exp_f32 v51, v51\n"
        "v_mfma_f32_16x16x32_bf16 v[36:39], a[100:103], v[68:71], v[36:39]\n"
        "v_exp_f32 v52, v52\n"
        "v_mfma_f32_16x16x32_bf16 v[36:39], a[96:99], v[64:67], v[36:39]\n"
        "v_exp_f32 v53, v53\n"
        "v_mfma_f32_16x16x32_bf16 v[40:43], a[96:99], v[96:99], v[232:235]\n"
        "v_exp_f32 v54, v54\n"
        "v_mfma_f32_16x16x32_bf16 v[40:43], a[100:103], v[100:103], v[40:43]\n"
        "v_exp_f32 v55, v55\n"
        "v_mfma_f32_16x16x32_bf16 v[40:43], a[104:107], v[104:107], v[40:43]\n"
        "v_exp_f32 v56, v56\n"
        "v_mfma_f32_16x16x32_bf16 v[40:43], a[108:111], v[108:111], v[40:43]\n"
        "v_exp_f32 v57, v57\n"
        "v_mfma_f32_16x16x32_bf16 v[44:47], a[108:111], v[140:143], v[236:239]\n"
        "ds_read_b128 a[108:111], v240 offset:11264\n"
        "v_cvt_pk_bf16_f32 v82, v48, v49\n"
        "v_mfma_f32_16x16x32_bf16 v[44:47], a[104:107], v[136:139], v[44:47]\n"
        "ds_read_b128 a[104:107], v240 offset:10240\n"
        "global_load_lds_dwordx4 v254, s[74:75] offset:1024 sc1\n"
        "v_mfma_f32_16x16x32_bf16 v[44:47], a[100:103], v[132:135], v[44:47]\n"
        "ds_read_b128 a[100:103], v240 offset:9216\n"
        "v_cvt_pk_bf16_f32 v83, v50, v51\n"
        "v_mfma_f32_16x16x32_bf16 v[44:47], a[96:99], v[128:131], v[44:47]\n"
        "ds_read_b128 a[96:99], v240 offset:8192\n"
        "v_cvt_pk_bf16_f32 v86, v52, v53\n"
        "v_mfma_f32_16x16x32_bf16 v[176:179], a[128:131], v[16:19], v[176:179]\n"
        "v_exp_f32 v58, v58\n"
        "v_mfma_f32_16x16x32_bf16 v[208:211], a[128:131], v[20:23], v[208:211]\n"
        "v_exp_f32 v59, v59\n"
        "v_mfma_f32_16x16x32_bf16 a[16:19], a[128:131], v[24:27], a[16:19]\n"
        "v_exp_f32 v60, v60\n"
        "v_mfma_f32_16x16x32_bf16 a[48:51], a[128:131], v[28:31], a[48:51]\n"
        "ds_read_b128 a[128:131], v241 offset:28672\n"
        "global_load_lds_dwordx4 v254, s[74:75] offset:2048 sc1\n"
        "v_mfma_f32_16x16x32_bf16 a[52:55], a[132:135], v[28:31], a[52:55]\n"
        "v_exp_f32 v61, v61\n"
        "v_mfma_f32_16x16x32_bf16 a[20:23], a[132:135], v[24:27], a[20:23]\n"
        "v_exp_f32 v62, v62\n"
        "v_mfma_f32_16x16x32_bf16 v[212:215], a[132:135], v[20:23], v[212:215]\n"
        "v_exp_f32 v63, v63\n"
        "v_mfma_f32_16x16x32_bf16 v[180:183], a[132:135], v[16:19], v[180:183]\n"
        "ds_read_b128 a[132:135], v241 offset:29696\n"
        "global_load_lds_dwordx4 v254, s[74:75] offset:3072 sc1\n"
        "s_add_u32 m0, s79, 0x0\n"
        "v_mfma_f32_16x16x32_bf16 v[184:187], a[136:139], v[16:19], v[184:187]\n"
        "v_exp_f32 v32, v32\n"
        "v_mfma_f32_16x16x32_bf16 v[216:219], a[136:139], v[20:23], v[216:219]\n"
        "v_exp_f32 v33, v33\n"
        "v_mfma_f32_16x16x32_bf16 a[24:27], a[136:139], v[24:27], a[24:27]\n"
        "v_exp_f32 v34, v34\n"
        "v_mfma_f32_16x16x32_bf16 a[56:59], a[136:139], v[28:31], a[56:59]\n"
        "ds_read_b128 a[136:139], v241 offset:30720\n"
        "v_cvt_pk_bf16_f32 v87, v54, v55\n"
        "v_mfma_f32_16x16x32_bf16 a[60:63], a[140:143], v[28:31], a[60:63]\n"
        "s_waitcnt lgkmcnt(2)\n"
        "v_exp_f32 v35, v35\n"
        "v_mfma_f32_16x16x32_bf16 a[28:31], a[140:143], v[24:27], a[28:31]\n"
        "v_exp_f32 v36, v36\n"
        "v_mfma_f32_16x16x32_bf16 v[220:223], a[140:143], v[20:23], v[220:223]\n"
        "v_exp_f32 v37, v37\n"
        "v_mfma_f32_16x16x32_bf16 v[188:191], a[140:143], v[16:19], v[188:191]\n"
        "ds_read_b128 a[140:143], v241 offset:31744\n"
        "v_cvt_pk_bf16_f32 v90, v56, v57\n"
        "v_mfma_f32_16x16x32_bf16 v[48:51], a[112:115], v[0:3], v[224:227]\n"
        "s_add_u32 s82, s82, 1\n"
        "v_exp_f32 v38, v38\n"
        "v_mfma_f32_16x16x32_bf16 v[48:51], a[116:119], v[4:7], v[48:51]\n"
        "s_xor_b32 s78, s78, 0x8000\n"
        "v_exp_f32 v39, v39\n"
        "v_mfma_f32_16x16x32_bf16 v[48:51], a[120:123], v[8:11], v[48:51]\n"
        "s_add_u32 s81, s78, 4096\n"
        "v_exp_f32 v40, v40\n"
        "v_mfma_f32_16x16x32_bf16 v[48:51], a[124:127], v[12:15], v[48:51]\n"
        "s_mov_b32 s80, s78\n"
        "v_exp_f32 v41, v41\n"
        "v_mfma_f32_16x16x32_bf16 v[52:55], a[124:127], v[76:79], v[228:231]\n"
        "v_exp_f32 v42, v42\n"
        "v_mfma_f32_16x16x32_bf16 v[52:55], a[120:123], v[72:75], v[52:55]\n"
        "v_exp_f32 v43, v43\n"
        "v_mfma_f32_16x16x32_bf16 v[52:55], a[116:119], v[68:71], v[52:55]\n"
        "v_exp_f32 v44, v44\n"
        "v_mfma_f32_16x16x32_bf16 v[52:55], a[112:115], v[64:67], v[52:55]\n"
        "s_waitcnt vmcnt(10) lgkmcnt(0)\n"
        "v_exp_f32 v45, v45\n"
        "v_mfma_f32_16x16x32_bf16 a[64:67], a[80:83], v[80:83], a[64:67]\n"
        "s_barrier\n"
        "v_mfma_f32_16x16x32_bf16 a[68:71], a[80:83], v[84:87], a[68:71]\n"
        "v_exp_f32 v46, v46\n"
        "v_mfma_f32_16x16x32_bf16 v[160:163], a[144:147], v[80:83], v[160:163]\n"
        "v_exp_f32 v47, v47\n"
        "v_mfma_f32_16x16x32_bf16 v[192:195], a[144:147], v[84:87], v[192:195]\n"
        "v_exp_f32 v48, v48\n"
        "v_mfma_f32_16x16x32_bf16 v[196:199], a[148:151], v[84:87], v[196:199]\n"
        "v_exp_f32 v49, v49\n"
        "v_mfma_f32_16x16x32_bf16 v[164:167], a[148:151], v[80:83], v[164:167]\n"
        "v_exp_f32 v50, v50\n"
        "v_mfma_f32_16x16x32_bf16 v[168:171], a[152:155], v[80:83], v[168:171]\n"
        "v_exp_f32 v51, v51\n"
        "v_mfma_f32_16x16x32_bf16 v[200:203], a[152:155], v[84:87], v[200:203]\n"
        "v_exp_f32 v52, v52\n"
        "v_mfma_f32_16x16x32_bf16 v[204:207], a[156:159], v[84:87], v[204:207]\n"
        "v_exp_f32 v53, v53\n"
        "v_mfma_f32_16x16x32_bf16 v[172:175], a[156:159], v[80:83], v[172:175]\n"
        "v_exp_f32 v54, v54\n"
        "v_mfma_f32_16x16x32_bf16 v[176:179], a[128:131], v[80:83], v[176:179]\n"
        "v_exp_f32 v55, v55\n"
        "v_cvt_pk_bf16_f32 v91, v58, v59\n"
        "v_mfma_f32_16x16x32_bf16 v[56:59], a[112:115], v[96:99], v[232:235]\n"
        "s_add_u32 s76, s66, s90\n"
        "s_addc_u32 s77, s67, 0\n"
        "v_cvt_pk_bf16_f32 v94, v60, v61\n"
        "v_mfma_f32_16x16x32_bf16 v[56:59], a[116:119], v[100:103], v[56:59]\n"
        "global_load_lds_dwordx4 v144, s[76:77] offset:0 sc1\n"
        "v_cvt_pk_bf16_f32 v95, v62, v63\n"
        "v_mfma_f32_16x16x32_bf16 v[56:59], a[120:123], v[104:107], v[56:59]\n"
        "s_cmp_ge_u32 s82, s44\n"
        "s_addc_u32 s90, s82, 0\n"
        "v_cvt_pk_bf16_f32 v16, v32, v33\n"
        "v_mfma_f32_16x16x32_bf16 v[56:59], a[124:127], v[108:111], v[56:59]\n"
        "s_lshl_b32 s90, s90, 15\n"
        "v_cvt_pk_bf16_f32 v17, v34, v35\n"
        "v_cvt_pk_bf16_f32 v20, v36, v37\n"
        "v_mfma_f32_16x16x32_bf16 v[60:63], a[124:127], v[140:143], v[236:239]\n"
        "ds_read_b128 a[124:127], v240 offset:15360\n"
        "v_cvt_pk_bf16_f32 v21, v38, v39\n"
        "v_mfma_f32_16x16x32_bf16 v[60:63], a[120:123], v[136:139], v[60:63]\n"
        "ds_read_b128 a[120:123], v240 offset:14336\n"
        "v_cvt_pk_bf16_f32 v24, v40, v41\n"
        "v_mfma_f32_16x16x32_bf16 v[60:63], a[116:119], v[132:135], v[60:63]\n"
        "ds_read_b128 a[116:119], v240 offset:13312\n"
        "v_xor_b32 v241, 0x8000, v241\n"
        "v_mfma_f32_16x16x32_bf16 v[60:63], a[112:115], v[128:131], v[60:63]\n"
        "ds_read_b128 a[112:115], v240 offset:12288\n"
        "v_cvt_pk_bf16_f32 v25, v42, v43\n"
        "v_mfma_f32_16x16x32_bf16 a[72:75], a[80:83], v[88:91], a[72:75]\n"
        "v_exp_f32 v56, v56\n"
        "v_mfma_f32_16x16x32_bf16 a[76:79], a[80:83], v[92:95], a[76:79]\n"
        "v_exp_f32 v57, v57\n"
        "v_mfma_f32_16x16x32_bf16 a[0:3], a[144:147], v[88:91], a[0:3]\n"
        "v_exp_f32 v58, v58\n"
        "v_mfma_f32_16x16x32_bf16 a[32:35], a[144:147], v[92:95], a[32:35]\n"
        "ds_read_b128 a[144:147], v241 offset:0\n"
        "global_load_lds_dwordx4 v145, s[76:77] offset:1024 sc1\n"
        "v_mfma_f32_16x16x32_bf16 a[36:39], a[148:151], v[92:95], a[36:39]\n"
        "v_exp_f32 v59, v59\n"
        "v_mfma_f32_16x16x32_bf16 a[4:7], a[148:151], v[88:91], a[4:7]\n"
        "ds_read_b128 a[148:151], v241 offset:1024\n"
        "v_cvt_pk_bf16_f32 v28, v44, v45\n"
        "v_mfma_f32_16x16x32_bf16 a[8:11], a[152:155], v[88:91], a[8:11]\n"
        "v_exp_f32 v60, v60\n"
        "v_mfma_f32_16x16x32_bf16 a[40:43], a[152:155], v[92:95], a[40:43]\n"
        "ds_read_b128 a[152:155], v241 offset:2048\n"
        "global_load_lds_dwordx4 v146, s[76:77] offset:2048 sc1\n"
        "v_mfma_f32_16x16x32_bf16 a[44:47], a[156:159], v[92:95], a[44:47]\n"
        "v_exp_f32 v61, v61\n"
        "v_mfma_f32_16x16x32_bf16 a[12:15], a[156:159], v[88:91], a[12:15]\n"
        "ds_read_b128 a[156:159], v241 offset:3072\n"
        "v_cvt_pk_bf16_f32 v29, v46, v47\n"
        "v_mfma_f32_16x16x32_bf16 v[32:35], a[96:99], v[0:3], v[224:227]\n"
        "v_exp_f32 v62, v62\n"
        "v_mfma_f32_16x16x32_bf16 v[32:35], a[100:103], v[4:7], v[32:35]\n"
        "v_exp_f32 v63, v63\n"
        "v_mfma_f32_16x16x32_bf16 v[32:35], a[104:107], v[8:11], v[32:35]\n"
        "global_load_lds_dwordx4 v147, s[76:77] offset:3072 sc1\n"
        "s_add_u32 m0, s79, s28\n"
        "v_cvt_pk_bf16_f32 v18, v48, v49\n"
        "v_mfma_f32_16x16x32_bf16 v[32:35], a[108:111], v[12:15], v[32:35]\n"
        "s_add_u32 s76, s76, s25\n"
        "s_addc_u32 s77, s77, 0\n"
        "v_cvt_pk_bf16_f32 v19, v50, v51\n"
        "v_mfma_f32_16x16x32_bf16 v[36:39], a[108:111], v[76:79], v[228:231]\n"
        "s_add_u32 s68, s64, s90\n"
        "s_addc_u32 s69, s65, 0\n"
        "v_cvt_pk_bf16_f32 v22, v52, v53\n"
        "v_mfma_f32_16x16x32_bf16 v[36:39], a[104:107], v[72:75], v[36:39]\n"
        "s_add_u32 s74, s68, 0x1000\n"
        "s_addc_u32 s75, s69, 0\n"
        "v_cvt_pk_bf16_f32 v23, v54, v55\n"
        "v_mfma_f32_16x16x32_bf16 v[36:39], a[100:103], v[68:71], v[36:39]\n"
        "s_sub_u32 s91, s91, 1\n"
        "v_exp_f32 v32, v32\n"
        "v_mfma_f32_16x16x32_bf16 v[36:39], a[96:99], v[64:67], v[36:39]\n"
        "v_exp_f32 v33, v33\n"
        "v_mfma_f32_16x16x32_bf16 v[40:43], a[96:99], v[96:99], v[232:235]\n"
        "v_exp_f32 v34, v34\n"
        "v_mfma_f32_16x16x32_bf16 v[40:43], a[100:103], v[100:103], v[40:43]\n"
        "v_exp_f32 v35, v35\n"
        "v_mfma_f32_16x16x32_bf16 v[40:43], a[104:107], v[104:107], v[40:43]\n"
        "global_load_lds_dwordx4 v144, s[76:77] offset:0 sc1\n"
        "v_cvt_pk_bf16_f32 v26, v56, v57\n"
        "v_mfma_f32_16x16x32_bf16 v[40:43], a[108:111], v[108:111], v[40:43]\n"
        "LHC0_%=:\n"
        "s_cbranch_execz LHRC_%=\n"
        "v_exp_f32 v36, v36\n"
        "v_mfma_f32_16x16x32_bf16 v[44:47], a[108:111], v[140:143], v[236:239]\n"
        "ds_read_b128 a[108:111], v240 offset:19456\n"
        "v_cvt_pk_bf16_f32 v27, v58, v59\n"
        "v_mfma_f32_16x16x32_bf16 v[44:47], a[104:107], v[136:139], v[44:47]\n"
        "ds_read_b128 a[104:107], v240 offset:18432\n"
        "v_cvt_pk_bf16_f32 v30, v60, v61\n"
        "v_mfma_f32_16x16x32_bf16 v[44:47], a[100:103], v[132:135], v[44:47]\n"
        "ds_read_b128 a[100:103], v240 offset:17408\n"
        "global_load_lds_dwordx4 v145, s[76:77] offset:1024 sc1\n"
        "v_mfma_f32_16x16x32_bf16 v[44:47], a[96:99], v[128:131], v[44:47]\n"
        "ds_read_b128 a[96:99], v240 offset:16384\n"
        "v_cvt_pk_bf16_f32 v31, v62, v63\n"
        "v_mfma_f32_16x16x32_bf16 v[208:211], a[128:131], v[84:87], v[208:211]\n"
        "v_exp_f32 v37, v37\n"
        "v_mfma_f32_16x16x32_bf16 a[16:19], a[128:131], v[88:91], a[16:19]\n"
        "v_exp_f32 v38, v38\n"
        "v_mfma_f32_16x16x32_bf16 a[48:51], a[128:131], v[92:95], a[48:51]\n"
        "ds_read_b128 a[128:131], v241 offset:4096\n"
        "global_load_lds_dwordx4 v146, s[76:77] offset:2048 sc1\n"
        "v_mfma_f32_16x16x32_bf16 a[52:55], a[132:135], v[92:95], a[52:55]\n"
        "v_exp_f32 v39, v39\n"
        "v_mfma_f32_16x16x32_bf16 a[20:23], a[132:135], v[88:91], a[20:23]\n"
        "v_exp_f32 v40, v40\n"
        "v_mfma_f32_16x16x32_bf16 v[212:215], a[132:135], v[84:87], v[212:215]\n"
        "v_exp_f32 v41, v41\n"
        "v_mfma_f32_16x16x32_bf16 v[180:183], a[132:135], v[80:83], v[180:183]\n"
        "ds_read_b128 a[132:135], v241 offset:5120\n"
        "global_load_lds_dwordx4 v147, s[76:77] offset:3072 sc1\n"
        "v_mfma_f32_16x16x32_bf16 v[184:187], a[136:139], v[80:83], v[184:187]\n"
        "v_exp_f32 v42, v42\n"
        "v_mfma_f32_16x16x32_bf16 v[216:219], a[136:139], v[84:87], v[216:219]\n"
        "v_exp_f32 v43, v43\n"
        "v_mfma_f32_16x16x32_bf16 a[24:27], a[136:139], v[88:91], a[24:27]\n"
        "s_waitcnt lgkmcnt(2)\n"
        "v_exp_f32 v44, v44\n"
        "v_mfma_f32_16x16x32_bf16 a[56:59], a[136:139], v[92:95], a[56:59]\n"
        "ds_read_b128 a[136:139], v241 offset:6144\n"
        "v_mfma_f32_16x16x32_bf16 a[60:63], a[140:143], v[92:95], a[60:63]\n"
        "v_exp_f32 v45, v45\n"
        "v_mfma_f32_16x16x32_bf16 a[28:31], a[140:143], v[88:91], a[28:31]\n"
        "v_exp_f32 v46, v46\n"
        "v_mfma_f32_16x16x32_bf16 v[220:223], a[140:143], v[84:87], v[220:223]\n"
        "v_exp_f32 v47, v47\n"
        "v_mfma_f32_16x16x32_bf16 v[188:191], a[140:143], v[80:83], v[188:191]\n"
        "ds_read_b128 a[140:143], v241 offset:7168\n"
        "global_load_dword v244, v245, s[26:27] offset:0\n"
        "v_mfma_f32_16x16x32_bf16 v[48:51], a[112:115], v[0:3], v[224:227]\n"
        "s_sub_u32 s26, s26, 262144\n"
        "s_subb_u32 s27, s27, 0\n"
        "v_cvt_pk_bf16_f32 v80, v32, v33\n"
        "v_mfma_f32_16x16x32_bf16 v[48:51], a[116:119], v[4:7], v[48:51]\n"
        "v_cvt_pk_bf16_f32 v81, v34, v35\n"
        "v_cvt_pk_bf16_f32 v84, v36, v37\n"
        "v_mfma_f32_16x16x32_bf16 v[48:51], a[120:123], v[8:11], v[48:51]\n"
        "v_cvt_pk_bf16_f32 v85, v38, v39\n"
        "v_cvt_pk_bf16_f32 v88, v40, v41\n"
        "v_mfma_f32_16x16x32_bf16 v[48:51], a[124:127], v[12:15], v[48:51]\n"
        "v_cvt_pk_bf16_f32 v89, v42, v43\n"
        "v_cvt_pk_bf16_f32 v92, v44, v45\n"
        "v_mfma_f32_16x16x32_bf16 v[52:55], a[124:127], v[76:79], v[228:231]\n"
        "v_cvt_pk_bf16_f32 v93, v46, v47\n"
        "v_mfma_f32_16x16x32_bf16 v[52:55], a[120:123], v[72:75], v[52:55]\n"
        "v_mfma_f32_16x16x32_bf16 v[52:55], a[116:119], v[68:71], v[52:55]\n"
        "v_mfma_f32_16x16x32_bf16 v[52:55], a[112:115], v[64:67], v[52:55]\n"
        "v_mfma_f32_16x16x32_bf16 v[56:59], a[112:115], v[96:99], v[232:235]\n"
        "v_exp_f32 v48, v48\n"
        "v_mfma_f32_16x16x32_bf16 v[56:59], a[116:119], v[100:103], v[56:59]\n"
        "v_exp_f32 v49, v49\n"
        "v_mfma_f32_16x16x32_bf16 v[56:59], a[120:123], v[104:107], v[56:59]\n"
        "v_exp_f32 v50, v50\n"
        "v_mfma_f32_16x16x32_bf16 v[56:59], a[124:127], v[108:111], v[56:59]\n"
        "v_exp_f32 v51, v51\n"
        "v_mfma_f32_16x16x32_bf16 v[60:63], a[124:127], v[140:143], v[236:239]\n"
        "ds_read_b128 a[124:127], v240 offset:23552\n"
        "v_cvt_pk_bf16_f32 v82, v48, v49\n"
        "v_mfma_f32_16x16x32_bf16 v[60:63], a[120:123], v[136:139], v[60:63]\n"
        "ds_read_b128 a[120:123], v240 offset:22528\n"
        "v_cvt_pk_bf16_f32 v83, v50, v51\n"
        "v_mfma_f32_16x16x32_bf16 v[60:63], a[116:119], v[132:135], v[60:63]\n"
        "ds_read_b128 a[116:119], v240 offset:21504\n"
        "v_mfma_f32_16x16x32_bf16 v[60:63], a[112:115], v[128:131], v[60:63]\n"
        "ds_read_b128 a[112:115], v240 offset:20480\n"
        "v_mfma_f32_16x16x32_bf16 a[64:67], a[80:83], v[16:19], a[64:67]\n"
        "v_exp_f32 v52, v52\n"
        "v_mfma_f32_16x16x32_bf16 a[68:71], a[80:83], v[20:23], a[68:71]\n"
        "v_exp_f32 v53, v53\n"
        "v_mfma_f32_16x16x32_bf16 a[72:75], a[80:83], v[24:27], a[72:75]\n"
        "v_exp_f32 v54, v54\n"
        "v_mfma_f32_16x16x32_bf16 a[76:79], a[80:83], v[28:31], a[76:79]\n"
        "v_exp_f32 v55, v55\n"
        "v_mfma_f32_16x16x32_bf16 v[160:163], a[144:147], v[16:19], v[160:163]\n"
        "v_exp_f32 v56, v56\n"
        "v_mfma_f32_16x16x32_bf16 v[192:195], a[144:147], v[20:23], v[192:195]\n"
        "v_exp_f32 v57, v57\n"
        "v_mfma_f32_16x16x32_bf16 a[0:3], a[144:147], v[24:27], a[0:3]\n"
        "v_exp_f32 v58, v58\n"
        "v_mfma_f32_16x16x32_bf16 a[32:35], a[144:147], v[28:31], a[32:35]\n"
        "ds_read_b128 a[144:147], v241 offset:8192\n"
        "v_cvt_pk_bf16_f32 v86, v52, v53\n"
        "v_mfma_f32_16x16x32_bf16 a[36:39], a[148:151], v[28:31], a[36:39]\n"
        "v_exp_f32 v59, v59\n"
        "v_mfma_f32_16x16x32_bf16 a[4:7], a[148:151], v[24:27], a[4:7]\n"
        "v_exp_f32 v60, v60\n"
        "v_mfma_f32_16x16x32_bf16 v[196:199], a[148:151], v[20:23], v[196:199]\n"
        "v_exp_f32 v61, v61\n"
        "v_mfma_f32_16x16x32_bf16 v[164:167], a[148:151], v[16:19], v[164:167]\n"
        "ds_read_b128 a[148:151], v241 offset:9216\n"
        "v_cvt_pk_bf16_f32 v87, v54, v55\n"
        "v_mfma_f32_16x16x32_bf16 v[168:171], a[152:155], v[16:19], v[168:171]\n"
        "v_exp_f32 v62, v62\n"
        "v_mfma_f32_16x16x32_bf16 v[200:203], a[152:155], v[20:23], v[200:203]\n"
        "v_exp_f32 v63, v63\n"
        "v_mfma_f32_16x16x32_bf16 a[8:11], a[152:155], v[24:27], a[8:11]\n"
        "v_cvt_pk_bf16_f32 v90, v56, v57\n"
        "v_cvt_pk_bf16_f32 v91, v58, v59\n"
        "v_mfma_f32_16x16x32_bf16 a[40:43], a[152:155], v[28:31], a[40:43]\n"
        "ds_read_b128 a[152:155], v241 offset:10240\n"
        "v_cvt_pk_bf16_f32 v94, v60, v61\n"
        "v_mfma_f32_16x16x32_bf16 a[44:47], a[156:159], v[28:31], a[44:47]\n"
        "v_cvt_pk_bf16_f32 v95, v62, v63\n"
        "v_mfma_f32_16x16x32_bf16 a[12:15], a[156:159], v[24:27], a[12:15]\n"
        "v_mfma_f32_16x16x32_bf16 v[204:207], a[156:159], v[20:23], v[204:207]\n"
        "s_waitcnt lgkmcnt(2)\n"
        "v_mfma_f32_16x16x32_bf16 v[172:175], a[156:159], v[16:19], v[172:175]\n"
        "ds_read_b128 a[156:159], v241 offset:11264\n"
        "v_mfma_f32_16x16x32_bf16 v[32:35], a[96:99], v[0:3], v[224:227]\n"
        "v_mfma_f32_16x16x32_bf16 v[32:35], a[100:103], v[4:7], v[32:35]\n"
        "v_mfma_f32_16x16x32_bf16 v[32:35], a[104:107], v[8:11], v[32:35]\n"
        "v_mfma_f32_16x16x32_bf16 v[32:35], a[108:111], v[12:15], v[32:35]\n"
        "v_mfma_f32_16x16x32_bf16 v[36:39], a[108:111], v[76:79], v[228:231]\n"
        "v_mfma_f32_16x16x32_bf16 v[36:39], a[104:107], v[72:75], v[36:39]\n"
        "v_mfma_f32_16x16x32_bf16 v[36:39], a[100:103], v[68:71], v[36:39]\n"
        "v_mfma_f32_16x16x32_bf16 v[36:39], a[96:99], v[64:67], v[36:39]\n"
        "v_mfma_f32_16x16x32_bf16 v[40:43], a[96:99], v[96:99], v[232:235]\n"
        "v_mfma_f32_16x16x32_bf16 v[40:43], a[100:103], v[100:103], v[40:43]\n"
        "v_mfma_f32_16x16x32_bf16 v[40:43], a[104:107], v[104:107], v[40:43]\n"
        "v_mfma_f32_16x16x32_bf16 v[40:43], a[108:111], v[108:111], v[40:43]\n"
        "v_exp_f32 v32, v32\n"
        "v_mfma_f32_16x16x32_bf16 v[44:47], a[108:111], v[140:143], v[236:239]\n"
        "v_exp_f32 v33, v33\n"
        "v_mfma_f32_16x16x32_bf16 v[44:47], a[104:107], v[136:139], v[44:47]\n"
        "ds_read_b128 a[104:107], v240 offset:26624\n"
        "v_mfma_f32_16x16x32_bf16 v[44:47], a[100:103], v[132:135], v[44:47]\n"
        "ds_read_b128 a[100:103], v240 offset:25600\n"
        "v_mfma_f32_16x16x32_bf16 v[44:47], a[96:99], v[128:131], v[44:47]\n"
        "ds_read_b128 a[96:99], v240 offset:24576\n"
        "v_mfma_f32_16x16x32_bf16 v[176:179], a[128:131], v[16:19], v[176:179]\n"
        "v_exp_f32 v34, v34\n"
        "v_mfma_f32_16x16x32_bf16 v[208:211], a[128:131], v[20:23], v[208:211]\n"
        "v_exp_f32 v35, v35\n"
        "v_mfma_f32_16x16x32_bf16 a[16:19], a[128:131], v[24:27], a[16:19]\n"
        "ds_read_b128 a[108:111], v240 offset:27648\n"
        "v_mfma_f32_16x16x32_bf16 a[48:51], a[128:131], v[28:31], a[48:51]\n"
        "ds_read_b128 a[128:131], v241 offset:12288\n"
        "v_mfma_f32_16x16x32_bf16 a[52:55], a[132:135], v[28:31], a[52:55]\n"
        "v_exp_f32 v36, v36\n"
        "v_mfma_f32_16x16x32_bf16 a[20:23], a[132:135], v[24:27], a[20:23]\n"
        "v_exp_f32 v37, v37\n"
        "v_mfma_f32_16x16x32_bf16 v[212:215], a[132:135], v[20:23], v[212:215]\n"
        "v_exp_f32 v38, v38\n"
        "v_mfma_f32_16x16x32_bf16 v[180:183], a[132:135], v[16:19], v[180:183]\n"
        "ds_read_b128 a[132:135], v241 offset:13312\n"
        "v_mfma_f32_16x16x32_bf16 v[184:187], a[136:139], v[16:19], v[184:187]\n"
        "v_exp_f32 v39, v39\n"
        "v_mfma_f32_16x16x32_bf16 v[216:219], a[136:139], v[20:23], v[216:219]\n"
        "v_exp_f32 v40, v40\n"
        "v_mfma_f32_16x16x32_bf16 a[24:27], a[136:139], v[24:27], a[24:27]\n"
        "v_exp_f32 v41, v41\n"
        "v_mfma_f32_16x16x32_bf16 a[56:59], a[136:139], v[28:31], a[56:59]\n"
        "ds_read_b128 a[136:139], v241 offset:14336\n"
        "v_mfma_f32_16x16x32_bf16 a[60:63], a[140:143], v[28:31], a[60:63]\n"
        "v_exp_f32 v42, v42\n"
        "v_mfma_f32_16x16x32_bf16 a[28:31], a[140:143], v[24:27], a[28:31]\n"
        "v_exp_f32 v43, v43\n"
        "v_mfma_f32_16x16x32_bf16 v[220:223], a[140:143], v[20:23], v[220:223]\n"
        "v_exp_f32 v44, v44\n"
        "v_mfma_f32_16x16x32_bf16 v[188:191], a[140:143], v[16:19], v[188:191]\n"
        "ds_read_b128 a[140:143], v241 offset:15360\n"
        "global_load_dword v244, v245, s[26:27] offset:0\n"
        "v_mfma_f32_16x16x32_bf16 v[48:51], a[112:115], v[0:3], v[224:227]\n"
        "v_exp_f32 v45, v45\n"
        "v_mfma_f32_16x16x32_bf16 v[48:51], a[116:119], v[4:7], v[48:51]\n"
        "v_exp_f32 v46, v46\n"
        "v_mfma_f32_16x16x32_bf16 v[48:51], a[120:123], v[8:11], v[48:51]\n"
        "v_exp_f32 v47, v47\n"
        "v_mfma_f32_16x16x32_bf16 v[48:51], a[124:127], v[12:15], v[48:51]\n"
        "s_sub_u32 s26, s26, 262144\n"
        "s_subb_u32 s27, s27, 0\n"
        "v_cvt_pk_bf16_f32 v16, v32, v33\n"
        "v_mfma_f32_16x16x32_bf16 v[52:55], a[124:127], v[76:79], v[228:231]\n"
        "v_cvt_pk_bf16_f32 v17, v34, v35\n"
        "v_cvt_pk_bf16_f32 v20, v36, v37\n"
        "v_mfma_f32_16x16x32_bf16 v[52:55], a[120:123], v[72:75], v[52:55]\n"
        "v_cvt_pk_bf16_f32 v21, v38, v39\n"
        "v_cvt_pk_bf16_f32 v24, v40, v41\n"
        "v_mfma_f32_16x16x32_bf16 v[52:55], a[116:119], v[68:71], v[52:55]\n"
        "v_exp_f32 v48, v48\n"
        "v_mfma_f32_16x16x32_bf16 v[52:55], a[112:115], v[64:67], v[52:55]\n"
        "v_exp_f32 v49, v49\n"
        "v_mfma_f32_16x16x32_bf16 v[56:59], a[112:115], v[96:99], v[232:235]\n"
        "v_exp_f32 v50, v50\n"
        "v_mfma_f32_16x16x32_bf16 v[56:59], a[116:119], v[100:103], v[56:59]\n"
        "v_exp_f32 v51, v51\n"
        "v_mfma_f32_16x16x32_bf16 v[56:59], a[120:123], v[104:107], v[56:59]\n"
        "v_cvt_pk_bf16_f32 v25, v42, v43\n"
        "v_cvt_pk_bf16_f32 v28, v44, v45\n"
        "v_mfma_f32_16x16x32_bf16 v[56:59], a[124:127], v[108:111], v[56:59]\n"
        "v_exp_f32 v52, v52\n"
        "v_mfma_f32_16x16x32_bf16 v[60:63], a[124:127], v[140:143], v[236:239]\n"
        "ds_read_b128 a[124:127], v240 offset:31744\n"
        "v_cvt_pk_bf16_f32 v29, v46, v47\n"
        "v_mfma_f32_16x16x32_bf16 v[60:63], a[120:123], v[136:139], v[60:63]\n"
        "ds_read_b128 a[120:123], v240 offset:30720\n"
        "v_cvt_pk_bf16_f32 v18, v48, v49\n"
        "v_mfma_f32_16x16x32_bf16 v[60:63], a[116:119], v[132:135], v[60:63]\n"
        "ds_read_b128 a[116:119], v240 offset:29696\n"
        "v_cvt_pk_bf16_f32 v19, v50, v51\n"
        "v_mfma_f32_16x16x32_bf16 v[60:63], a[112:115], v[128:131], v[60:63]\n"
        "ds_read_b128 a[112:115], v240 offset:28672\n"
        "v_xor_b32 v240, 0x8000, v240\n"
        "v_mfma_f32_16x16x32_bf16 a[64:67], a[80:83], v[80:83], a[64:67]\n"
        "v_exp_f32 v53, v53\n"
        "v_mfma_f32_16x16x32_bf16 a[68:71], a[80:83], v[84:87], a[68:71]\n"
        "v_exp_f32 v54, v54\n"
        "v_mfma_f32_16x16x32_bf16 a[72:75], a[80:83], v[88:91], a[72:75]\n"
        "s_waitcnt lgkmcnt(4)\n"
        "v_exp_f32 v55, v55\n"
        "v_mfma_f32_16x16x32_bf16 a[76:79], a[80:83], v[92:95], a[76:79]\n"
        "v_exp_f32 v56, v56\n"
        "v_mfma_f32_16x16x32_bf16 v[160:163], a[144:147], v[80:83], v[160:163]\n"
        "v_exp_f32 v57, v57\n"
        "v_mfma_f32_16x16x32_bf16 v[192:195], a[144:147], v[84:87], v[192:195]\n"
        "v_exp_f32 v58, v58\n"
        "v_mfma_f32_16x16x32_bf16 a[0:3], a[144:147], v[88:91], a[0:3]\n"
        "v_exp_f32 v59, v59\n"
        "v_mfma_f32_16x16x32_bf16 a[32:35], a[144:147], v[92:95], a[32:35]\n"
        "ds_read_b128 a[144:147], v241 offset:16384\n"
        "v_cvt_pk_bf16_f32 v22, v52, v53\n"
        "v_mfma_f32_16x16x32_bf16 a[36:39], a[148:151], v[92:95], a[36:39]\n"
        "s_waitcnt vmcnt(10) lgkmcnt(1)\n"
        "v_exp_f32 v60, v60\n"
        "v_mfma_f32_16x16x32_bf16 a[4:7], a[148:151], v[88:91], a[4:7]\n"
        "s_barrier\n"
        "v_mfma_f32_16x16x32_bf16 v[196:199], a[148:151], v[84:87], v[196:199]\n"
        "v_exp_f32 v61, v61\n"
        "v_mfma_f32_16x16x32_bf16 v[164:167], a[148:151], v[80:83], v[164:167]\n"
        "ds_read_b128 a[148:151], v241 offset:17408\n"
        "v_cvt_pk_bf16_f32 v23, v54, v55\n"
        "v_mfma_f32_16x16x32_bf16 v[168:171], a[152:155], v[80:83], v[168:171]\n"
        "v_exp_f32 v62, v62\n"
        "v_mfma_f32_16x16x32_bf16 v[200:203], a[152:155], v[84:87], v[200:203]\n"
        "v_exp_f32 v63, v63\n"
        "v_mfma_f32_16x16x32_bf16 a[8:11], a[152:155], v[88:91], a[8:11]\n"
        "s_cmp_eq_u32 s82, s24\n"
        "s_cbranch_scc1 LEVD0_%=\n"
        "LEWD0_%=:\n"
        "s_add_u32 m0, s80, 0x0\n"
        "v_mfma_f32_16x16x32_bf16 a[40:43], a[152:155], v[92:95], a[40:43]\n"
        "ds_read_b128 a[152:155], v241 offset:18432\n"
        "global_load_lds_dwordx4 v254, s[68:69] offset:0 sc1\n"
        "v_mfma_f32_16x16x32_bf16 a[44:47], a[156:159], v[92:95], a[44:47]\n"
        "v_cvt_pk_bf16_f32 v26, v56, v57\n"
        "v_cvt_pk_bf16_f32 v27, v58, v59\n"
        "v_mfma_f32_16x16x32_bf16 a[12:15], a[156:159], v[88:91], a[12:15]\n"
        "v_cvt_pk_bf16_f32 v30, v60, v61\n"
        "v_cvt_pk_bf16_f32 v31, v62, v63\n"
        "v_mfma_f32_16x16x32_bf16 v[204:207], a[156:159], v[84:87], v[204:207]\n"
        "v_mfma_f32_16x16x32_bf16 v[172:175], a[156:159], v[80:83], v[172:175]\n"
        "ds_read_b128 a[156:159], v241 offset:19456\n"
        "global_load_lds_dwordx4 v254, s[68:69] offset:1024 sc1\n"
        "v_mfma_f32_16x16x32_bf16 v[32:35], a[96:99], v[0:3], v[224:227]\n"
        "v_mfma_f32_16x16x32_bf16 v[32:35], a[100:103], v[4:7], v[32:35]\n"
        "v_mfma_f32_16x16x32_bf16 v[32:35], a[104:107], v[8:11], v[32:35]\n"
        "v_mfma_f32_16x16x32_bf16 v[32:35], a[108:111], v[12:15], v[32:35]\n"
        "global_load_lds_dwordx4 v254, s[68:69] offset:2048 sc1\n"
        "v_mfma_f32_16x16x32_bf16 v[36:39], a[108:111], v[76:79], v[228:231]\n"
        "v_mfma_f32_16x16x32_bf16 v[36:39], a[104:107], v[72:75], v[36:39]\n"
        "v_mfma_f32_16x16x32_bf16 v[36:39], a[100:103], v[68:71], v[36:39]\n"
        "v_mfma_f32_16x16x32_bf16 v[36:39], a[96:99], v[64:67], v[36:39]\n"
        "global_load_lds_dwordx4 v254, s[68:69] offset:3072 sc1\n"
        "v_mfma_f32_16x16x32_bf16 v[40:43], a[96:99], v[96:99], v[232:235]\n"
        "v_mfma_f32_16x16x32_bf16 v[40:43], a[100:103], v[100:103], v[40:43]\n"
        "v_exp_f32 v32, v32\n"
        "v_mfma_f32_16x16x32_bf16 v[40:43], a[104:107], v[104:107], v[40:43]\n"
        "v_exp_f32 v33, v33\n"
        "v_mfma_f32_16x16x32_bf16 v[40:43], a[108:111], v[108:111], v[40:43]\n"
        "v_exp_f32 v34, v34\n"
        "v_mfma_f32_16x16x32_bf16 v[44:47], a[108:111], v[140:143], v[236:239]\n"
        "v_exp_f32 v35, v35\n"
        "v_mfma_f32_16x16x32_bf16 v[44:47], a[104:107], v[136:139], v[44:47]\n"
        "ds_read_b128 a[104:107], v240 offset:2048\n"
        "v_mfma_f32_16x16x32_bf16 v[44:47], a[100:103], v[132:135], v[44:47]\n"
        "ds_read_b128 a[100:103], v240 offset:1024\n"
        "v_mfma_f32_16x16x32_bf16 v[44:47], a[96:99], v[128:131], v[44:47]\n"
        "ds_read_b128 a[96:99], v240 offset:0\n"
        "v_mfma_f32_16x16x32_bf16 v[176:179], a[128:131], v[80:83], v[176:179]\n"
        "ds_read_b128 a[108:111], v240 offset:3072\n"
        "v_mfma_f32_16x16x32_bf16 v[208:211], a[128:131], v[84:87], v[208:211]\n"
        "v_exp_f32 v36, v36\n"
        "v_mfma_f32_16x16x32_bf16 a[16:19], a[128:131], v[88:91], a[16:19]\n"
        "v_exp_f32 v37, v37\n"
        "v_mfma_f32_16x16x32_bf16 a[48:51], a[128:131], v[92:95], a[48:51]\n"
        "ds_read_b128 a[128:131], v241 offset:20480\n"
        "v_mfma_f32_16x16x32_bf16 a[52:55], a[132:135], v[92:95], a[52:55]\n"
        "v_exp_f32 v38, v38\n"
        "v_mfma_f32_16x16x32_bf16 a[20:23], a[132:135], v[88:91], a[20:23]\n"
        "v_exp_f32 v39, v39\n"
        "v_mfma_f32_16x16x32_bf16 v[212:215], a[132:135], v[84:87], v[212:215]\n"
        "v_exp_f32 v40, v40\n"
        "v_mfma_f32_16x16x32_bf16 v[180:183], a[132:135], v[80:83], v[180:183]\n"
        "ds_read_b128 a[132:135], v241 offset:21504\n"
        "v_mfma_f32_16x16x32_bf16 v[184:187], a[136:139], v[80:83], v[184:187]\n"
        "v_exp_f32 v41, v41\n"
        "v_mfma_f32_16x16x32_bf16 v[216:219], a[136:139], v[84:87], v[216:219]\n"
        "v_exp_f32 v42, v42\n"
        "v_mfma_f32_16x16x32_bf16 a[24:27], a[136:139], v[88:91], a[24:27]\n"
        "v_exp_f32 v43, v43\n"
        "v_mfma_f32_16x16x32_bf16 a[56:59], a[136:139], v[92:95], a[56:59]\n"
        "ds_read_b128 a[136:139], v241 offset:22528\n"
        "s_waitcnt lgkmcnt(2)\n"
        "v_mfma_f32_16x16x32_bf16 a[60:63], a[140:143], v[92:95], a[60:63]\n"
        "v_exp_f32 v44, v44\n"
        "v_mfma_f32_16x16x32_bf16 a[28:31], a[140:143], v[88:91], a[28:31]\n"
        "v_exp_f32 v45, v45\n"
        "v_mfma_f32_16x16x32_bf16 v[220:223], a[140:143], v[84:87], v[220:223]\n"
        "v_exp_f32 v46, v46\n"
        "v_mfma_f32_16x16x32_bf16 v[188:191], a[140:143], v[80:83], v[188:191]\n"
        "ds_read_b128 a[140:143], v241 offset:23552\n"
        "v_cvt_pk_bf16_f32 v80, v32, v33\n"
        "v_mfma_f32_16x16x32_bf16 v[48:51], a[112:115], v[0:3], v[224:227]\n"
        "v_exp_f32 v47, v47\n"
        "v_mfma_f32_16x16x32_bf16 v[48:51], a[116:119], v[4:7], v[48:51]\n"
        "v_cvt_pk_bf16_f32 v81, v34, v35\n"
        "v_cvt_pk_bf16_f32 v84, v36, v37\n"
        "v_mfma_f32_16x16x32_bf16 v[48:51], a[120:123], v[8:11], v[48:51]\n"
        "v_cvt_pk_bf16_f32 v85, v38, v39\n"
        "v_cvt_pk_bf16_f32 v88, v40, v41\n"
        "v_mfma_f32_16x16x32_bf16 v[48:51], a[124:127], v[12:15], v[48:51]\n"
        "v_cvt_pk_bf16_f32 v89, v42, v43\n"
        "v_cvt_pk_bf16_f32 v92, v44, v45\n"
        "v_mfma_f32_16x16x32_bf16 v[52:55], a[124:127], v[76:79], v[228:231]\n"
        "v_cvt_pk_bf16_f32 v93, v46, v47\n"
        "v_mfma_f32_16x16x32_bf16 v[52:55], a[120:123], v[72:75], v[52:55]\n"
        "v_mfma_f32_16x16x32_bf16 v[52:55], a[116:119], v[68:71], v[52:55]\n"
        "v_mfma_f32_16x16x32_bf16 v[52:55], a[112:115], v[64:67], v[52:55]\n"
        "v_mfma_f32_16x16x32_bf16 v[56:59], a[112:115], v[96:99], v[232:235]\n"
        "v_mfma_f32_16x16x32_bf16 v[56:59], a[116:119], v[100:103], v[56:59]\n"
        "v_mfma_f32_16x16x32_bf16 v[56:59], a[120:123], v[104:107], v[56:59]\n"
        "v_mfma_f32_16x16x32_bf16 v[56:59], a[124:127], v[108:111], v[56:59]\n"
        "v_mfma_f32_16x16x32_bf16 v[60:63], a[124:127], v[140:143], v[236:239]\n"
        "v_mfma_f32_16x16x32_bf16 v[60:63], a[120:123], v[136:139], v[60:63]\n"
        "ds_read_b128 a[120:123], v240 offset:6144\n"
        "v_mfma_f32_16x16x32_bf16 v[60:63], a[116:119], v[132:135], v[60:63]\n"
        "ds_read_b128 a[116:119], v240 offset:5120\n"
        "v_mfma_f32_16x16x32_bf16 v[60:63], a[112:115], v[128:131], v[60:63]\n"
        "ds_read_b128 a[112:115], v240 offset:4096\n"
        "v_mfma_f32_16x16x32_bf16 a[64:67], a[80:83], v[16:19], a[64:67]\n"
        "v_mfma_f32_16x16x32_bf16 a[68:71], a[80:83], v[20:23], a[68:71]\n"
        "v_mfma_f32_16x16x32_bf16 a[72:75], a[80:83], v[24:27], a[72:75]\n"
        "v_mfma_f32_16x16x32_bf16 a[76:79], a[80:83], v[28:31], a[76:79]\n"
        "v_mfma_f32_16x16x32_bf16 v[160:163], a[144:147], v[16:19], v[160:163]\n"
        "v_mfma_f32_16x16x32_bf16 v[192:195], a[144:147], v[20:23], v[192:195]\n"
        "v_mfma_f32_16x16x32_bf16 a[0:3], a[144:147], v[24:27], a[0:3]\n"
        "v_mfma_f32_16x16x32_bf16 a[32:35], a[144:147], v[28:31], a[32:35]\n"
        "ds_read_b128 a[144:147], v241 offset:24576\n"
        "v_mfma_f32_16x16x32_bf16 a[36:39], a[148:151], v[28:31], a[36:39]\n"
        "v_mfma_f32_16x16x32_bf16 a[4:7], a[148:151], v[24:27], a[4:7]\n"
        "v_mfma_f32_16x16x32_bf16 v[196:199], a[148:151], v[20:23], v[196:199]\n"
        "v_mfma_f32_16x16x32_bf16 v[164:167], a[148:151], v[16:19], v[164:167]\n"
        "ds_read_b128 a[148:151], v241 offset:25600\n"
        "v_mfma_f32_16x16x32_bf16 v[168:171], a[152:155], v[16:19], v[168:171]\n"
        "v_mfma_f32_16x16x32_bf16 v[200:203], a[152:155], v[20:23], v[200:203]\n"
        "v_mfma_f32_16x16x32_bf16 a[8:11], a[152:155], v[24:27], a[8:11]\n"
        "v_mfma_f32_16x16x32_bf16 a[40:43], a[152:155], v[28:31], a[40:43]\n"
        "ds_read_b128 a[152:155], v241 offset:26624\n"
        "v_mfma_f32_16x16x32_bf16 a[44:47], a[156:159], v[28:31], a[44:47]\n"
        "v_mfma_f32_16x16x32_bf16 a[12:15], a[156:159], v[24:27], a[12:15]\n"
        "v_mfma_f32_16x16x32_bf16 v[204:207], a[156:159], v[20:23], v[204:207]\n"
        "s_cmp_lg_u32 s91, 0\n"
        "v_mfma_f32_16x16x32_bf16 v[172:175], a[156:159], v[16:19], v[172:175]\n"
        "s_cbranch_scc1 LDU_%=\n"
        "s_branch LLD_%=\n"
        "LEVD0_%=:\n"
        "s_lshl_b32 s97, s41, 12\n"
        "s_add_u32 s97, s97, 32768\n"
        "v_bfe_u32 v246, v254, 4, 5\n"
        "v_lshl_add_u32 v246, v246, 7, s97\n"
        "s_cmp_eq_u32 s82, s83\n"
        "s_cbranch_scc1 LE3D0_%=\n"
        "global_load_dwordx2 v[252:253], v246, s[48:49] sc1\n"
        "s_mov_b32 s24, s83\n"
        "v_accvgpr_read_b32 v246, a93\n"
        "global_load_dword v244, v246, s[46:47]\n"
        "s_branch LEWD0_%=\n"
        "LE3D0_%=:\n"
        "s_mov_b32 s97, 0x200000\n"
        "LE4D0_%=:\n"
        "v_cmp_ne_u64 vcc, v[252:253], v[250:251]\n"
        "s_nop 1\n"
        "s_cbranch_vccz LE5D0_%=\n"
        "s_sleep 2\n"
        "global_load_dwordx2 v[252:253], v246, s[48:49] sc1\n"
        "s_waitcnt vmcnt(0)\n"
        "s_sub_u32 s97, s97, 1\n"
        "s_cmp_lg_u32 s97, 0\n"
        "s_cbranch_scc1 LE4D0_%=\n"
        "LE5D0_%=:\n"
        "s_mov_b64 s[68:69], s[92:93]\n"
        "s_mov_b64 s[74:75], s[94:95]\n"
        "s_mov_b32 s80, s50\n"
        "s_mov_b32 s81, s51\n"
        "s_mov_b64 s[66:67], s[22:23]\n"
        "s_mov_b32 s90, s29\n"
        "s_mov_b32 s25, s30\n"
        "s_mov_b32 s28, 16384\n"
        "s_xor_b32 s79, s73, 0x8000\n"
        "v_accvgpr_read_b32 v144, a84\n"
        "v_accvgpr_read_b32 v145, a85\n"
        "v_accvgpr_read_b32 v146, a86\n"
        "v_accvgpr_read_b32 v147, a87\n"
        "v_accvgpr_read_b32 v148, a88\n"
        "v_accvgpr_read_b32 v152, a89\n"
        "v_accvgpr_read_b32 v252, a90\n"
        "v_accvgpr_read_b32 v249, a91\n"
        "v_accvgpr_read_b32 v153, a92\n"
        "v_mov_b32 v246, 0\n"
        "v_mov_b32 v247, 0\n"
        "v_mov_b32 v248, 0\n"
        "v_add_u32 v149, 1024, v148\n"
        "v_add_u32 v150, 2048, v148\n"
        "v_add_u32 v151, 3072, v148\n"
        "s_mov_b64 exec, 15\n"
        "ds_write_b128 v252, v[246:249]\n"
        "s_mov_b64 exec, -1\n"
        "s_branch LEWD0_%=\n"
        "; ---- LDRAIN\n"
        "LLD_%=:\n"
        "v_mfma_f32_16x16x32_bf16 a[16:19], a[128:131], v[24:27], a[16:19]\n"
        "s_lshl_b32 s53, s44, 20\n"
        "s_lshl_b32 s96, s43, 15\n"
        "s_add_u32 s53, s53, s96\n"
        "v_mfma_f32_16x16x32_bf16 a[48:51], a[128:131], v[28:31], a[48:51]\n"
        "s_lshl_b32 s96, s40, 10\n"
        "s_add_u32 s53, s53, s96\n"
        "s_waitcnt lgkmcnt(2)\n"
        "v_mfma_f32_16x16x32_bf16 a[52:55], a[132:135], v[28:31], a[52:55]\n"
        "s_add_u32 s53, s53, 0x2001000\n"
        "s_add_u32 s70, s38, s53\n"
        "s_addc_u32 s71, s39, 0\n"
        "v_mfma_f32_16x16x32_bf16 a[20:23], a[132:135], v[24:27], a[20:23]\n"
        "ds_read_b128 a[160:163], v241 offset:24576\n"
        "s_sub_u32 s91, s83, s44\n"
        "v_mfma_f32_16x16x32_bf16 a[24:27], a[136:139], v[24:27], a[24:27]\n"
        "ds_read_b128 a[164:167], v241 offset:25600\n"
        "v_mfma_f32_16x16x32_bf16 a[56:59], a[136:139], v[28:31], a[56:59]\n"
        "ds_read_b128 a[168:171], v241 offset:26624\n"
        "v_mfma_f32_16x16x32_bf16 a[60:63], a[140:143], v[28:31], a[60:63]\n"
        "ds_read_b128 a[172:175], v241 offset:27648\n"
        "v_mfma_f32_16x16x32_bf16 a[28:31], a[140:143], v[24:27], a[28:31]\n"
        "ds_read_b128 a[176:179], v241 offset:28672\n"
        "ds_read_b128 a[180:183], v241 offset:29696\n"
        "ds_read_b128 a[184:187], v241 offset:30720\n"
        "ds_read_b128 a[188:191], v241 offset:31744\n"
        "v_exp_f32 v56, v56\n"
        "v_exp_f32 v57, v57\n"
        "v_exp_f32 v58, v58\n"
        "v_cvt_pk_bf16_f32 v90, v56, v57\n"
        "v_exp_f32 v59, v59\n"
        "v_exp_f32 v60, v60\n"
        "v_cvt_pk_bf16_f32 v91, v58, v59\n"
        "s_waitcnt lgkmcnt(6)\n"
        "s_nop 0\n"
        "v_mfma_f32_16x16x32_bf16 a[0:3], a[160:163], v[88:91], a[0:3]\n"
        "s_waitcnt lgkmcnt(5)\n"
        "v_exp_f32 v61, v61\n"
        "v_mfma_f32_16x16x32_bf16 a[72:75], a[80:83], v[88:91], a[72:75]\n"
        "s_waitcnt lgkmcnt(4)\n"
        "v_cvt_pk_bf16_f32 v94, v60, v61\n"
        "v_mfma_f32_16x16x32_bf16 a[4:7], a[164:167], v[88:91], a[4:7]\n"
        "s_waitcnt lgkmcnt(3)\n"
        "v_exp_f32 v62, v62\n"
        "v_mfma_f32_16x16x32_bf16 a[8:11], a[168:171], v[88:91], a[8:11]\n"
        "s_waitcnt lgkmcnt(2)\n"
        "v_exp_f32 v63, v63\n"
        "v_mfma_f32_16x16x32_bf16 a[12:15], a[172:175], v[88:91], a[12:15]\n"
        "s_waitcnt lgkmcnt(0)\n"
        "v_cvt_pk_bf16_f32 v95, v62, v63\n"
        "s_nop 1\n"
        "v_mfma_f32_16x16x32_bf16 a[76:79], a[80:83], v[92:95], a[76:79]\n"
        "v_accvgpr_read_b32 v248, a72\n"
        "v_accvgpr_read_b32 v120, a0\n"
        "v_mfma_f32_16x16x32_bf16 a[32:35], a[160:163], v[92:95], a[32:35]\n"
        "v_rcp_f32 v246, v248\n"
        "v_mfma_f32_16x16x32_bf16 a[36:39], a[164:167], v[92:95], a[36:39]\n"
        "v_accvgpr_read_b32 v121, a1\n"
        "v_accvgpr_read_b32 v122, a2\n"
        "v_mfma_f32_16x16x32_bf16 a[40:43], a[168:171], v[92:95], a[40:43]\n"
        "v_accvgpr_read_b32 v249, a76\n"
        "v_accvgpr_read_b32 v123, a3\n"
        "v_mfma_f32_16x16x32_bf16 a[44:47], a[172:175], v[92:95], a[44:47]\n"
        "v_rcp_f32 v247, v249\n"
        "v_mfma_f32_16x16x32_bf16 a[16:19], a[176:179], v[88:91], a[16:19]\n"
        "v_accvgpr_read_b32 v112, a32\n"
        "v_accvgpr_read_b32 v113, a33\n"
        "v_mfma_f32_16x16x32_bf16 a[48:51], a[176:179], v[92:95], a[48:51]\n"
        "v_accvgpr_read_b32 v114, a34\n"
        "v_accvgpr_read_b32 v115, a35\n"
        "v_mfma_f32_16x16x32_bf16 a[52:55], a[180:183], v[92:95], a[52:55]\n"
        "v_accvgpr_read_b32 v116, a36\n"
        "v_accvgpr_read_b32 v117, a37\n"
        "v_mfma_f32_16x16x32_bf16 a[20:23], a[180:183], v[88:91], a[20:23]\n"
        "v_accvgpr_read_b32 v118, a38\n"
        "v_accvgpr_read_b32 v119, a39\n"
        "v_mfma_f32_16x16x32_bf16 a[24:27], a[184:187], v[88:91], a[24:27]\n"
        "v_mul_f32 v112, v112, v247\n"
        "v_mul_f32 v113, v113, v247\n"
        "v_mfma_f32_16x16x32_bf16 a[56:59], a[184:187], v[92:95], a[56:59]\n"
        "v_mul_f32 v114, v114, v247\n"
        "v_mul_f32 v115, v115, v247\n"
        "v_mfma_f32_16x16x32_bf16 a[60:63], a[188:191], v[92:95], a[60:63]\n"
        "v_mul_f32 v116, v116, v247\n"
        "v_mul_f32 v117, v117, v247\n"
        "v_mfma_f32_16x16x32_bf16 a[28:31], a[188:191], v[88:91], a[28:31]\n"
        "v_mul_f32 v118, v118, v247\n"
        "v_mul_f32 v119, v119, v247\n"
        "v_cvt_pk_bf16_f32 v112, v112, v113\n"
        "v_cvt_pk_bf16_f32 v113, v114, v115\n"
        "v_cvt_pk_bf16_f32 v114, v116, v117\n"
        "v_cvt_pk_bf16_f32 v115, v118, v119\n"
        "global_store_dwordx4 v254, v[112:115], s[70:71] offset:-4096 sc1\n"
        "v_accvgpr_read_b32 v124, a4\n"
        "v_accvgpr_read_b32 v125, a5\n"
        "v_accvgpr_read_b32 v126, a6\n"
        "v_accvgpr_read_b32 v127, a7\n"
        "v_mul_f32 v120, v120, v246\n"
        "v_mul_f32 v121, v121, v246\n"
        "v_mul_f32 v122, v122, v246\n"
        "v_mul_f32 v123, v123, v246\n"
        "v_mul_f32 v124, v124, v246\n"
        "v_mul_f32 v125, v125, v246\n"
        "v_mul_f32 v126, v126, v246\n"
        "v_mul_f32 v127, v127, v246\n"
        "v_cvt_pk_bf16_f32 v120, v120, v121\n"
        "v_cvt_pk_bf16_f32 v121, v122, v123\n"
        "v_cvt_pk_bf16_f32 v122, v124, v125\n"
        "v_cvt_pk_bf16_f32 v123, v126, v127\n"
        "global_store_dwordx4 v254, v[120:123], s[70:71] offset:0 sc1\n"
        "s_add_u32 s70, s70, 0x2000\n"
        "s_addc_u32 s71, s71, 0\n"
        "v_accvgpr_read_b32 v40, a40\n"
        "v_accvgpr_read_b32 v41, a41\n"
        "v_accvgpr_read_b32 v42, a42\n"
        "v_accvgpr_read_b32 v43, a43\n"
        "v_accvgpr_read_b32 v44, a44\n"
        "v_accvgpr_read_b32 v45, a45\n"
        "v_accvgpr_read_b32 v46, a46\n"
        "v_accvgpr_read_b32 v47, a47\n"
        "v_mul_f32 v40, v40, v247\n"
        "v_mul_f32 v41, v41, v247\n"
        "v_mul_f32 v42, v42, v247\n"
        "v_mul_f32 v43, v43, v247\n"
        "v_mul_f32 v44, v44, v247\n"
        "v_mul_f32 v45, v45, v247\n"
        "v_mul_f32 v46, v46, v247\n"
        "v_mul_f32 v47, v47, v247\n"
        "v_cvt_pk_bf16_f32 v40, v40, v41\n"
        "v_cvt_pk_bf16_f32 v41, v42, v43\n"
        "v_cvt_pk_bf16_f32 v42, v44, v45\n"
        "v_cvt_pk_bf16_f32 v43, v46, v47\n"
        "global_store_dwordx4 v254, v[40:43], s[70:71] offset:-4096 sc1\n"
        "v_accvgpr_read_b32 v56, a8\n"
        "v_accvgpr_read_b32 v57, a9\n"
        "v_accvgpr_read_b32 v58, a10\n"
        "v_accvgpr_read_b32 v59, a11\n"
        "v_accvgpr_read_b32 v60, a12\n"
        "v_accvgpr_read_b32 v61, a13\n"
        "v_accvgpr_read_b32 v62, a14\n"
        "v_accvgpr_read_b32 v63, a15\n"
        "v_mul_f32 v56, v56, v246\n"
        "v_mul_f32 v57, v57, v246\n"
        "v_mul_f32 v58, v58, v246\n"
        "v_mul_f32 v59, v59, v246\n"
        "v_mul_f32 v60, v60, v246\n"
        "v_mul_f32 v61, v61, v246\n"
        "v_mul_f32 v62, v62, v246\n"
        "v_mul_f32 v63, v63, v246\n"
        "v_cvt_pk_bf16_f32 v56, v56, v57\n"
        "v_cvt_pk_bf16_f32 v57, v58, v59\n"
        "v_cvt_pk_bf16_f32 v58, v60, v61\n"
        "v_cvt_pk_bf16_f32 v59, v62, v63\n"
        "global_store_dwordx4 v254, v[56:59], s[70:71] offset:0 sc1\n"
        "s_add_u32 s70, s70, 0x2000\n"
        "s_addc_u32 s71, s71, 0\n"
        "v_accvgpr_read_b32 v112, a48\n"
        "v_accvgpr_read_b32 v113, a49\n"
        "v_accvgpr_read_b32 v114, a50\n"
        "v_accvgpr_read_b32 v115, a51\n"
        "v_accvgpr_read_b32 v116, a52\n"
        "v_accvgpr_read_b32 v117, a53\n"
        "v_accvgpr_read_b32 v118, a54\n"
        "v_accvgpr_read_b32 v119, a55\n"
        "v_mul_f32 v112, v112, v247\n"
        "v_mul_f32 v113, v113, v247\n"
        "v_mul_f32 v114, v114, v247\n"
        "v_mul_f32 v115, v115, v247\n"
        "v_mul_f32 v116, v116, v247\n"
        "v_mul_f32 v117, v117, v247\n"
        "LHC1_%=:\n"
        "s_cbranch_execz LHRC_%=\n"
        "v_mul_f32 v118, v118, v247\n"
        "v_mul_f32 v119, v119, v247\n"
        "v_cvt_pk_bf16_f32 v112, v112, v113\n"
        "v_cvt_pk_bf16_f32 v113, v114, v115\n"
        "v_cvt_pk_bf16_f32 v114, v116, v117\n"
        "v_cvt_pk_bf16_f32 v115, v118, v119\n"
        "global_store_dwordx4 v254, v[112:115], s[70:71] offset:-4096 sc1\n"
        "v_accvgpr_read_b32 v120, a16\n"
        "v_accvgpr_read_b32 v121, a17\n"
        "v_accvgpr_read_b32 v122, a18\n"
        "v_accvgpr_read_b32 v123, a19\n"
        "v_accvgpr_read_b32 v124, a20\n"
        "v_accvgpr_read_b32 v125, a21\n"
        "v_accvgpr_read_b32 v126, a22\n"
        "v_accvgpr_read_b32 v127, a23\n"
        "v_mul_f32 v120, v120, v246\n"
        "v_mul_f32 v121, v121, v246\n"
        "v_mul_f32 v122, v122, v246\n"
        "v_mul_f32 v123, v123, v246\n"
        "v_mul_f32 v124, v124, v246\n"
        "v_mul_f32 v125, v125, v246\n"
        "v_mul_f32 v126, v126, v246\n"
        "v_mul_f32 v127, v127, v246\n"
        "v_cvt_pk_bf16_f32 v120, v120, v121\n"
        "v_cvt_pk_bf16_f32 v121, v122, v123\n"
        "v_cvt_pk_bf16_f32 v122, v124, v125\n"
        "v_cvt_pk_bf16_f32 v123, v126, v127\n"
        "global_store_dwordx4 v254, v[120:123], s[70:71] offset:0 sc1\n"
        "s_add_u32 s70, s70, 0x2000\n"
        "s_addc_u32 s71, s71, 0\n"
        "v_accvgpr_read_b32 v40, a56\n"
        "v_accvgpr_read_b32 v41, a57\n"
        "v_accvgpr_read_b32 v42, a58\n"
        "v_accvgpr_read_b32 v43, a59\n"
        "v_accvgpr_read_b32 v44, a60\n"
        "v_accvgpr_read_b32 v45, a61\n"
        "v_accvgpr_read_b32 v46, a62\n"
        "v_accvgpr_read_b32 v47, a63\n"
        "v_mul_f32 v40, v40, v247\n"
        "v_mul_f32 v41, v41, v247\n"
        "v_mul_f32 v42, v42, v247\n"
        "v_mul_f32 v43, v43, v247\n"
        "v_mul_f32 v44, v44, v247\n"
        "v_mul_f32 v45, v45, v247\n"
        "v_mul_f32 v46, v46, v247\n"
        "v_mul_f32 v47, v47, v247\n"
        "v_cvt_pk_bf16_f32 v40, v40, v41\n"
        "v_cvt_pk_bf16_f32 v41, v42, v43\n"
        "v_cvt_pk_bf16_f32 v42, v44, v45\n"
        "v_cvt_pk_bf16_f32 v43, v46, v47\n"
        "global_store_dwordx4 v254, v[40:43], s[70:71] offset:-4096 sc1\n"
        "v_accvgpr_read_b32 v56, a24\n"
        "v_accvgpr_read_b32 v57, a25\n"
        "v_accvgpr_read_b32 v58, a26\n"
        "v_accvgpr_read_b32 v59, a27\n"
        "v_accvgpr_read_b32 v60, a28\n"
        "v_accvgpr_read_b32 v61, a29\n"
        "v_accvgpr_read_b32 v62, a30\n"
        "v_accvgpr_read_b32 v63, a31\n"
        "v_mul_f32 v56, v56, v246\n"
        "v_mul_f32 v57, v57, v246\n"
        "v_mul_f32 v58, v58, v246\n"
        "v_mul_f32 v59, v59, v246\n"
        "v_mul_f32 v60, v60, v246\n"
        "v_mul_f32 v61, v61, v246\n"
        "v_mul_f32 v62, v62, v246\n"
        "v_mul_f32 v63, v63, v246\n"
        "v_cvt_pk_bf16_f32 v56, v56, v57\n"
        "v_cvt_pk_bf16_f32 v57, v58, v59\n"
        "v_cvt_pk_bf16_f32 v58, v60, v61\n"
        "v_cvt_pk_bf16_f32 v59, v62, v63\n"
        "global_store_dwordx4 v254, v[56:59], s[70:71] offset:0 sc1\n"
        "v_add_f32 v247, v248, v249\n"
        "v_cmp_nge_f32 vcc, 0x4e6e6b28, v247\n"
        "s_nop 0\n"
        "s_cbranch_vccnz LRL_%=\n"
        "LSL_%=:\n"
        "s_cmp_eq_u32 s91, 0\n"
        "s_cbranch_scc1 LTL_%=\n"
        "; ---- SINGLE\n"
        "s_nop 0\n"
        "s_nop 0\n"
        "s_nop 0\n"
        "LSI_%=:\n"
        "v_mfma_f32_16x16x32_bf16 v[32:35], a[96:99], v[0:3], v[224:227]\n"
        "s_waitcnt lgkmcnt(4)\n"
        "ds_read_b128 a[156:159], v241 offset:27648\n"
        "s_add_u32 m0, s81, 0x0\n"
        "v_mfma_f32_16x16x32_bf16 v[32:35], a[100:103], v[4:7], v[32:35]\n"
        "ds_read_b128 a[124:127], v240 offset:7168\n"
        "global_load_lds_dwordx4 v254, s[74:75] offset:0 sc1\n"
        "s_xor_b32 s79, s79, 0x8000\n"
        "v_mfma_f32_16x16x32_bf16 v[32:35], a[104:107], v[8:11], v[32:35]\n"
        "s_add_u32 s82, s82, 1\n"
        "v_exp_f32 v48, v48\n"
        "v_mfma_f32_16x16x32_bf16 v[32:35], a[108:111], v[12:15], v[32:35]\n"
        "s_xor_b32 s78, s78, 0x8000\n"
        "v_exp_f32 v49, v49\n"
        "v_mfma_f32_16x16x32_bf16 v[36:39], a[108:111], v[76:79], v[228:231]\n"
        "ds_read_b128 a[108:111], v240 offset:11264\n"
        "s_add_u32 s81, s78, 4096\n"
        "v_cvt_pk_bf16_f32 v82, v48, v49\n"
        "v_mfma_f32_16x16x32_bf16 v[36:39], a[104:107], v[72:75], v[36:39]\n"
        "ds_read_b128 a[104:107], v240 offset:10240\n"
        "global_load_lds_dwordx4 v254, s[74:75] offset:1024 sc1\n"
        "s_mov_b32 s80, s78\n"
        "v_mfma_f32_16x16x32_bf16 v[36:39], a[100:103], v[68:71], v[36:39]\n"
        "s_sub_u32 s91, s91, 1\n"
        "v_exp_f32 v50, v50\n"
        "v_mfma_f32_16x16x32_bf16 v[36:39], a[96:99], v[64:67], v[36:39]\n"
        "v_exp_f32 v51, v51\n"
        "v_mfma_f32_16x16x32_bf16 v[176:179], a[128:131], v[16:19], v[176:179]\n"
        "ds_read_b128 a[96:99], v240 offset:8192\n"
        "v_cvt_pk_bf16_f32 v83, v50, v51\n"
        "v_mfma_f32_16x16x32_bf16 v[208:211], a[128:131], v[20:23], v[208:211]\n"
        "ds_read_b128 a[128:131], v241 offset:28672\n"
        "global_load_lds_dwordx4 v254, s[74:75] offset:2048 sc1\n"
        "v_mfma_f32_16x16x32_bf16 v[212:215], a[132:135], v[20:23], v[212:215]\n"
        "v_exp_f32 v52, v52\n"
        "v_mfma_f32_16x16x32_bf16 v[180:183], a[132:135], v[16:19], v[180:183]\n"
        "v_exp_f32 v53, v53\n"
        "v_mfma_f32_16x16x32_bf16 v[184:187], a[136:139], v[16:19], v[184:187]\n"
        "ds_read_b128 a[100:103], v240 offset:9216\n"
        "v_cvt_pk_bf16_f32 v86, v52, v53\n"
        "v_mfma_f32_16x16x32_bf16 v[216:219], a[136:139], v[20:23], v[216:219]\n"
        "s_waitcnt lgkmcnt(3)\n"
        "ds_read_b128 a[132:135], v241 offset:29696\n"
        "v_mfma_f32_16x16x32_bf16 v[220:223], a[140:143], v[20:23], v[220:223]\n"
        "ds_read_b128 a[136:139], v241 offset:30720\n"
        "global_load_lds_dwordx4 v254, s[74:75] offset:3072 sc1\n"
        "s_add_u32 m0, s79, 0x0\n"
        "v_mfma_f32_16x16x32_bf16 v[188:191], a[140:143], v[16:19], v[188:191]\n"
        "v_exp_f32 v54, v54\n"
        "v_mfma_f32_16x16x32_bf16 v[48:51], a[112:115], v[0:3], v[224:227]\n"
        "v_exp_f32 v55, v55\n"
        "v_mfma_f32_16x16x32_bf16 v[48:51], a[116:119], v[4:7], v[48:51]\n"
        "ds_read_b128 a[140:143], v241 offset:31744\n"
        "v_cvt_pk_bf16_f32 v87, v54, v55\n"
        "v_mfma_f32_16x16x32_bf16 v[48:51], a[120:123], v[8:11], v[48:51]\n"
        "v_exp_f32 v32, v32\n"
        "v_mfma_f32_16x16x32_bf16 v[48:51], a[124:127], v[12:15], v[48:51]\n"
        "v_exp_f32 v33, v33\n"
        "v_mfma_f32_16x16x32_bf16 v[52:55], a[124:127], v[76:79], v[228:231]\n"
        "ds_read_b128 a[124:127], v240 offset:15360\n"
        "v_cvt_pk_bf16_f32 v16, v32, v33\n"
        "v_mfma_f32_16x16x32_bf16 v[52:55], a[120:123], v[72:75], v[52:55]\n"
        "v_exp_f32 v34, v34\n"
        "v_mfma_f32_16x16x32_bf16 v[52:55], a[116:119], v[68:71], v[52:55]\n"
        "v_exp_f32 v35, v35\n"
        "v_mfma_f32_16x16x32_bf16 v[52:55], a[112:115], v[64:67], v[52:55]\n"
        "ds_read_b128 a[112:115], v240 offset:12288\n"
        "v_cvt_pk_bf16_f32 v17, v34, v35\n"
        "v_mfma_f32_16x16x32_bf16 a[64:67], a[80:83], v[80:83], a[64:67]\n"
        "v_exp_f32 v36, v36\n"
        "v_mfma_f32_16x16x32_bf16 a[68:71], a[80:83], v[84:87], a[68:71]\n"
        "s_waitcnt vmcnt(9) lgkmcnt(2)\n"
        "v_exp_f32 v37, v37\n"
        "v_mfma_f32_16x16x32_bf16 v[160:163], a[144:147], v[80:83], v[160:163]\n"
        "s_barrier\n"
        "v_mfma_f32_16x16x32_bf16 v[192:195], a[144:147], v[84:87], v[192:195]\n"
        "ds_read_b128 a[116:119], v240 offset:13312\n"
        "v_cvt_pk_bf16_f32 v20, v36, v37\n"
        "v_mfma_f32_16x16x32_bf16 v[196:199], a[148:151], v[84:87], v[196:199]\n"
        "ds_read_b128 a[120:123], v240 offset:14336\n"
        "v_xor_b32 v241, 0x8000, v241\n"
        "v_mfma_f32_16x16x32_bf16 v[164:167], a[148:151], v[80:83], v[164:167]\n"
        "ds_read_b128 a[144:147], v241 offset:0\n"
        "s_add_u32 s76, s66, s90\n"
        "s_addc_u32 s77, s67, 0\n"
        "v_mfma_f32_16x16x32_bf16 v[168:171], a[152:155], v[80:83], v[168:171]\n"
        "ds_read_b128 a[148:151], v241 offset:1024\n"
        "global_load_lds_dwordx4 v144, s[76:77] offset:0 sc1\n"
        "v_mfma_f32_16x16x32_bf16 v[200:203], a[152:155], v[84:87], v[200:203]\n"
        "v_exp_f32 v38, v38\n"
        "v_mfma_f32_16x16x32_bf16 v[204:207], a[156:159], v[84:87], v[204:207]\n"
        "v_exp_f32 v39, v39\n"
        "v_mfma_f32_16x16x32_bf16 v[172:175], a[156:159], v[80:83], v[172:175]\n"
        "ds_read_b128 a[152:155], v241 offset:2048\n"
        "v_cvt_pk_bf16_f32 v21, v38, v39\n"
        "v_mfma_f32_16x16x32_bf16 v[32:35], a[96:99], v[0:3], v[224:227]\n"
        "ds_read_b128 a[156:159], v241 offset:3072\n"
        "global_load_lds_dwordx4 v145, s[76:77] offset:1024 sc1\n"
        "v_mfma_f32_16x16x32_bf16 v[32:35], a[100:103], v[4:7], v[32:35]\n"
        "v_exp_f32 v48, v48\n"
        "v_mfma_f32_16x16x32_bf16 v[32:35], a[104:107], v[8:11], v[32:35]\n"
        "v_exp_f32 v49, v49\n"
        "v_mfma_f32_16x16x32_bf16 v[32:35], a[108:111], v[12:15], v[32:35]\n"
        "v_exp_f32 v50, v50\n"
        "v_mfma_f32_16x16x32_bf16 v[36:39], a[108:111], v[76:79], v[228:231]\n"
        "ds_read_b128 a[108:111], v240 offset:19456\n"
        "v_cvt_pk_bf16_f32 v18, v48, v49\n"
        "v_mfma_f32_16x16x32_bf16 v[36:39], a[104:107], v[72:75], v[36:39]\n"
        "ds_read_b128 a[104:107], v240 offset:18432\n"
        "global_load_lds_dwordx4 v146, s[76:77] offset:2048 sc1\n"
        "v_mfma_f32_16x16x32_bf16 v[36:39], a[100:103], v[68:71], v[36:39]\n"
        "v_exp_f32 v51, v51\n"
        "v_mfma_f32_16x16x32_bf16 v[36:39], a[96:99], v[64:67], v[36:39]\n"
        "ds_read_b128 a[96:99], v240 offset:16384\n"
        "v_cvt_pk_bf16_f32 v19, v50, v51\n"
        "v_mfma_f32_16x16x32_bf16 v[176:179], a[128:131], v[80:83], v[176:179]\n"
        "v_exp_f32 v52, v52\n"
        "v_mfma_f32_16x16x32_bf16 v[208:211], a[128:131], v[84:87], v[208:211]\n"
        "ds_read_b128 a[128:131], v241 offset:4096\n"
        "global_load_lds_dwordx4 v147, s[76:77] offset:3072 sc1\n"
        "s_add_u32 m0, s79, s28\n"
        "v_mfma_f32_16x16x32_bf16 v[212:215], a[132:135], v[84:87], v[212:215]\n"
        "s_waitcnt lgkmcnt(4)\n"
        "v_exp_f32 v53, v53\n"
        "v_mfma_f32_16x16x32_bf16 v[180:183], a[132:135], v[80:83], v[180:183]\n"
        "ds_read_b128 a[100:103], v240 offset:17408\n"
        "v_cvt_pk_bf16_f32 v22, v52, v53\n"
        "v_mfma_f32_16x16x32_bf16 v[184:187], a[136:139], v[80:83], v[184:187]\n"
        "ds_read_b128 a[132:135], v241 offset:5120\n"
        "s_add_u32 s76, s76, s25\n"
        "s_addc_u32 s77, s77, 0\n"
        "v_mfma_f32_16x16x32_bf16 v[216:219], a[136:139], v[84:87], v[216:219]\n"
        "ds_read_b128 a[136:139], v241 offset:6144\n"
        "global_load_lds_dwordx4 v144, s[76:77] offset:0 sc1\n"
        "v_mfma_f32_16x16x32_bf16 v[220:223], a[140:143], v[84:87], v[220:223]\n"
        "v_exp_f32 v54, v54\n"
        "v_mfma_f32_16x16x32_bf16 v[188:191], a[140:143], v[80:83], v[188:191]\n"
        "v_exp_f32 v55, v55\n"
        "v_mfma_f32_16x16x32_bf16 v[48:51], a[112:115], v[0:3], v[224:227]\n"
        "ds_read_b128 a[140:143], v241 offset:7168\n"
        "v_cvt_pk_bf16_f32 v23, v54, v55\n"
        "v_mfma_f32_16x16x32_bf16 v[48:51], a[116:119], v[4:7], v[48:51]\n"
        "v_exp_f32 v32, v32\n"
        "v_mfma_f32_16x16x32_bf16 v[48:51], a[120:123], v[8:11], v[48:51]\n"
        "v_exp_f32 v33, v33\n"
        "v_mfma_f32_16x16x32_bf16 v[48:51], a[124:127], v[12:15], v[48:51]\n"
        "v_exp_f32 v34, v34\n"
        "v_mfma_f32_16x16x32_bf16 v[52:55], a[124:127], v[76:79], v[228:231]\n"
        "ds_read_b128 a[124:127], v240 offset:23552\n"
        "global_load_lds_dwordx4 v145, s[76:77] offset:1024 sc1\n"
        "v_mfma_f32_16x16x32_bf16 v[52:55], a[120:123], v[72:75], v[52:55]\n"
        "ds_read_b128 a[120:123], v240 offset:22528\n"
        "v_cvt_pk_bf16_f32 v80, v32, v33\n"
        "v_mfma_f32_16x16x32_bf16 v[52:55], a[116:119], v[68:71], v[52:55]\n"
        "v_exp_f32 v35, v35\n"
        "v_mfma_f32_16x16x32_bf16 v[52:55], a[112:115], v[64:67], v[52:55]\n"
        "ds_read_b128 a[112:115], v240 offset:20480\n"
        "v_cvt_pk_bf16_f32 v81, v34, v35\n"
        "v_mfma_f32_16x16x32_bf16 a[64:67], a[80:83], v[16:19], a[64:67]\n"
        "ds_read_b128 a[116:119], v240 offset:21504\n"
        "global_load_lds_dwordx4 v146, s[76:77] offset:2048 sc1\n"
        "v_mfma_f32_16x16x32_bf16 a[68:71], a[80:83], v[20:23], a[68:71]\n"
        "v_exp_f32 v36, v36\n"
        "v_mfma_f32_16x16x32_bf16 v[160:163], a[144:147], v[16:19], v[160:163]\n"
        "v_exp_f32 v37, v37\n"
        "v_mfma_f32_16x16x32_bf16 v[192:195], a[144:147], v[20:23], v[192:195]\n"
        "ds_read_b128 a[144:147], v241 offset:8192\n"
        "v_cvt_pk_bf16_f32 v84, v36, v37\n"
        "v_mfma_f32_16x16x32_bf16 v[196:199], a[148:151], v[20:23], v[196:199]\n"
        "s_waitcnt lgkmcnt(4)\n"
        "v_exp_f32 v38, v38\n"
        "v_mfma_f32_16x16x32_bf16 v[164:167], a[148:151], v[16:19], v[164:167]\n"
        "ds_read_b128 a[148:151], v241 offset:9216\n"
        "global_load_lds_dwordx4 v147, s[76:77] offset:3072 sc1\n"
        "v_mfma_f32_16x16x32_bf16 v[168:171], a[152:155], v[16:19], v[168:171]\n"
        "v_exp_f32 v39, v39\n"
        "v_mfma_f32_16x16x32_bf16 v[200:203], a[152:155], v[20:23], v[200:203]\n"
        "ds_read_b128 a[152:155], v241 offset:10240\n"
        "v_cvt_pk_bf16_f32 v85, v38, v39\n"
        "v_mfma_f32_16x16x32_bf16 v[204:207], a[156:159], v[20:23], v[204:207]\n"
        "v_exp_f32 v48, v48\n"
        "v_mfma_f32_16x16x32_bf16 v[172:175], a[156:159], v[16:19], v[172:175]\n"
        "ds_read_b128 a[156:159], v241 offset:11264\n"
        "s_cmp_ge_u32 s82, s44\n"
        "s_addc_u32 s90, s82, 0\n"
        "v_mfma_f32_16x16x32_bf16 v[32:35], a[96:99], v[0:3], v[224:227]\n"
        "s_lshl_b32 s90, s90, 15\n"
        "v_exp_f32 v49, v49\n"
        "v_mfma_f32_16x16x32_bf16 v[32:35], a[100:103], v[4:7], v[32:35]\n"
        "v_exp_f32 v50, v50\n"
        "v_mfma_f32_16x16x32_bf16 v[32:35], a[104:107], v[8:11], v[32:35]\n"
        "v_exp_f32 v51, v51\n"
        "v_mfma_f32_16x16x32_bf16 v[32:35], a[108:111], v[12:15], v[32:35]\n"
        "v_exp_f32 v52, v52\n"
        "v_mfma_f32_16x16x32_bf16 v[36:39], a[108:111], v[76:79], v[228:231]\n"
        "ds_read_b128 a[108:111], v240 offset:27648\n"
        "v_cvt_pk_bf16_f32 v82, v48, v49\n"
        "v_mfma_f32_16x16x32_bf16 v[36:39], a[104:107], v[72:75], v[36:39]\n"
        "ds_read_b128 a[104:107], v240 offset:26624\n"
        "v_cvt_pk_bf16_f32 v83, v50, v51\n"
        "v_mfma_f32_16x16x32_bf16 v[36:39], a[100:103], v[68:71], v[36:39]\n"
        "v_exp_f32 v53, v53\n"
        "v_mfma_f32_16x16x32_bf16 v[36:39], a[96:99], v[64:67], v[36:39]\n"
        "ds_read_b128 a[96:99], v240 offset:24576\n"
        "v_cvt_pk_bf16_f32 v86, v52, v53\n"
        "v_mfma_f32_16x16x32_bf16 v[176:179], a[128:131], v[16:19], v[176:179]\n"
        "ds_read_b128 a[100:103], v240 offset:25600\n"
        "s_add_u32 s68, s64, s90\n"
        "s_addc_u32 s69, s65, 0\n"
        "v_mfma_f32_16x16x32_bf16 v[208:211], a[128:131], v[20:23], v[208:211]\n"
        "ds_read_b128 a[128:131], v241 offset:12288\n"
        "s_add_u32 s74, s68, 0x1000\n"
        "s_addc_u32 s75, s69, 0\n"
        "v_mfma_f32_16x16x32_bf16 v[212:215], a[132:135], v[20:23], v[212:215]\n"
        "s_waitcnt lgkmcnt(5)\n"
        "v_exp_f32 v54, v54\n"
        "v_mfma_f32_16x16x32_bf16 v[180:183], a[132:135], v[16:19], v[180:183]\n"
        "ds_read_b128 a[132:135], v241 offset:13312\n"
        "v_mfma_f32_16x16x32_bf16 v[184:187], a[136:139], v[16:19], v[184:187]\n"
        "v_exp_f32 v55, v55\n"
        "v_mfma_f32_16x16x32_bf16 v[216:219], a[136:139], v[20:23], v[216:219]\n"
        "ds_read_b128 a[136:139], v241 offset:14336\n"
        "v_cvt_pk_bf16_f32 v87, v54, v55\n"
        "v_mfma_f32_16x16x32_bf16 v[220:223], a[140:143], v[20:23], v[220:223]\n"
        "v_exp_f32 v32, v32\n"
        "v_mfma_f32_16x16x32_bf16 v[188:191], a[140:143], v[16:19], v[188:191]\n"
        "ds_read_b128 a[140:143], v241 offset:15360\n"
        "global_load_dword v244, v245, s[26:27] offset:0\n"
        "v_mfma_f32_16x16x32_bf16 v[48:51], a[112:115], v[0:3], v[224:227]\n"
        "v_exp_f32 v33, v33\n"
        "v_mfma_f32_16x16x32_bf16 v[48:51], a[116:119], v[4:7], v[48:51]\n"
        "v_exp_f32 v34, v34\n"
        "v_mfma_f32_16x16x32_bf16 v[48:51], a[120:123], v[8:11], v[48:51]\n"
        "v_exp_f32 v35, v35\n"
        "v_mfma_f32_16x16x32_bf16 v[48:51], a[124:127], v[12:15], v[48:51]\n"
        "v_exp_f32 v36, v36\n"
        "v_mfma_f32_16x16x32_bf16 v[52:55], a[124:127], v[76:79], v[228:231]\n"
        "ds_read_b128 a[124:127], v240 offset:31744\n"
        "s_sub_u32 s26, s26, 262144\n"
        "s_subb_u32 s27, s27, 0\n"
        "v_mfma_f32_16x16x32_bf16 v[52:55], a[120:123], v[72:75], v[52:55]\n"
        "ds_read_b128 a[120:123], v240 offset:30720\n"
        "v_cvt_pk_bf16_f32 v16, v32, v33\n"
        "v_mfma_f32_16x16x32_bf16 v[52:55], a[116:119], v[68:71], v[52:55]\n"
        "ds_read_b128 a[116:119], v240 offset:29696\n"
        "v_cvt_pk_bf16_f32 v17, v34, v35\n"
        "v_mfma_f32_16x16x32_bf16 v[52:55], a[112:115], v[64:67], v[52:55]\n"
        "ds_read_b128 a[112:115], v240 offset:28672\n"
        "v_xor_b32 v240, 0x8000, v240\n"
        "v_mfma_f32_16x16x32_bf16 a[64:67], a[80:83], v[80:83], a[64:67]\n"
        "v_exp_f32 v37, v37\n"
        "v_mfma_f32_16x16x32_bf16 a[68:71], a[80:83], v[84:87], a[68:71]\n"
        "v_exp_f32 v38, v38\n"
        "v_mfma_f32_16x16x32_bf16 v[160:163], a[144:147], v[80:83], v[160:163]\n"
        "v_exp_f32 v39, v39\n"
        "v_mfma_f32_16x16x32_bf16 v[192:195], a[144:147], v[84:87], v[192:195]\n"
        "ds_read_b128 a[144:147], v241 offset:16384\n"
        "v_cvt_pk_bf16_f32 v20, v36, v37\n"
        "v_mfma_f32_16x16x32_bf16 v[196:199], a[148:151], v[84:87], v[196:199]\n"
        "s_waitcnt lgkmcnt(4)\n"
        "v_exp_f32 v48, v48\n"
        "v_mfma_f32_16x16x32_bf16 v[164:167], a[148:151], v[80:83], v[164:167]\n"
        "ds_read_b128 a[148:151], v241 offset:17408\n"
        "v_cvt_pk_bf16_f32 v21, v38, v39\n"
        "v_mfma_f32_16x16x32_bf16 v[168:171], a[152:155], v[80:83], v[168:171]\n"
        "v_exp_f32 v49, v49\n"
        "v_mfma_f32_16x16x32_bf16 v[200:203], a[152:155], v[84:87], v[200:203]\n"
        "ds_read_b128 a[152:155], v241 offset:18432\n"
        "v_cvt_pk_bf16_f32 v18, v48, v49\n"
        "v_mfma_f32_16x16x32_bf16 v[204:207], a[156:159], v[84:87], v[204:207]\n"
        "s_waitcnt vmcnt(9) lgkmcnt(3)\n"
        "v_exp_f32 v50, v50\n"
        "v_mfma_f32_16x16x32_bf16 v[172:175], a[156:159], v[80:83], v[172:175]\n"
        "s_barrier\n"
        "v_mfma_f32_16x16x32_bf16 v[32:35], a[96:99], v[0:3], v[224:227]\n"
        "ds_read_b128 a[156:159], v241 offset:19456\n"
        "s_cmp_eq_u32 s82, s24\n"
        "s_cbranch_scc1 LEVS0_%=\n"
        "LEWS0_%=:\n"
        "v_mfma_f32_16x16x32_bf16 v[32:35], a[100:103], v[4:7], v[32:35]\n"
        "s_add_u32 m0, s80, 0x0\n"
        "v_exp_f32 v51, v51\n"
        "v_mfma_f32_16x16x32_bf16 v[32:35], a[104:107], v[8:11], v[32:35]\n"
        "v_exp_f32 v52, v52\n"
        "v_mfma_f32_16x16x32_bf16 v[32:35], a[108:111], v[12:15], v[32:35]\n"
        "v_exp_f32 v53, v53\n"
        "v_mfma_f32_16x16x32_bf16 v[36:39], a[108:111], v[76:79], v[228:231]\n"
        "ds_read_b128 a[108:111], v240 offset:3072\n"
        "v_cvt_pk_bf16_f32 v19, v50, v51\n"
        "v_mfma_f32_16x16x32_bf16 v[36:39], a[104:107], v[72:75], v[36:39]\n"
        "ds_read_b128 a[104:107], v240 offset:2048\n"
        "global_load_lds_dwordx4 v254, s[68:69] offset:0 sc1\n"
        "v_mfma_f32_16x16x32_bf16 v[36:39], a[100:103], v[68:71], v[36:39]\n"
        "ds_read_b128 a[100:103], v240 offset:1024\n"
        "v_cvt_pk_bf16_f32 v22, v52, v53\n"
        "v_mfma_f32_16x16x32_bf16 v[36:39], a[96:99], v[64:67], v[36:39]\n"
        "ds_read_b128 a[96:99], v240 offset:0\n"
        "v_mfma_f32_16x16x32_bf16 v[176:179], a[128:131], v[80:83], v[176:179]\n"
        "v_exp_f32 v54, v54\n"
        "v_mfma_f32_16x16x32_bf16 v[208:211], a[128:131], v[84:87], v[208:211]\n"
        "ds_read_b128 a[128:131], v241 offset:20480\n"
        "global_load_lds_dwordx4 v254, s[68:69] offset:1024 sc1\n"
        "v_mfma_f32_16x16x32_bf16 v[212:215], a[132:135], v[84:87], v[212:215]\n"
        "v_exp_f32 v55, v55\n"
        "v_mfma_f32_16x16x32_bf16 v[180:183], a[132:135], v[80:83], v[180:183]\n"
        "ds_read_b128 a[132:135], v241 offset:21504\n"
        "v_cvt_pk_bf16_f32 v23, v54, v55\n"
        "v_mfma_f32_16x16x32_bf16 v[184:187], a[136:139], v[80:83], v[184:187]\n"
        "v_exp_f32 v32, v32\n"
        "v_mfma_f32_16x16x32_bf16 v[216:219], a[136:139], v[84:87], v[216:219]\n"
        "ds_read_b128 a[136:139], v241 offset:22528\n"
        "global_load_lds_dwordx4 v254, s[68:69] offset:2048 sc1\n"
        "v_mfma_f32_16x16x32_bf16 v[220:223], a[140:143], v[84:87], v[220:223]\n"
        "s_waitcnt lgkmcnt(4)\n"
        "v_exp_f32 v33, v33\n"
        "v_mfma_f32_16x16x32_bf16 v[188:191], a[140:143], v[80:83], v[188:191]\n"
        "ds_read_b128 a[140:143], v241 offset:23552\n"
        "v_cvt_pk_bf16_f32 v80, v32, v33\n"
        "v_mfma_f32_16x16x32_bf16 v[48:51], a[112:115], v[0:3], v[224:227]\n"
        "v_exp_f32 v34, v34\n"
        "v_mfma_f32_16x16x32_bf16 v[48:51], a[116:119], v[4:7], v[48:51]\n"
        "s_waitcnt lgkmcnt(3)\n"
        "v_exp_f32 v35, v35\n"
        "v_mfma_f32_16x16x32_bf16 v[48:51], a[120:123], v[8:11], v[48:51]\n"
        "v_exp_f32 v36, v36\n"
        "v_mfma_f32_16x16x32_bf16 v[48:51], a[124:127], v[12:15], v[48:51]\n"
        "v_exp_f32 v37, v37\n"
        "v_mfma_f32_16x16x32_bf16 v[52:55], a[124:127], v[76:79], v[228:231]\n"
        "v_exp_f32 v38, v38\n"
        "v_mfma_f32_16x16x32_bf16 v[52:55], a[120:123], v[72:75], v[52:55]\n"
        "ds_read_b128 a[120:123], v240 offset:6144\n"
        "global_load_lds_dwordx4 v254, s[68:69] offset:3072 sc1\n"
        "v_mfma_f32_16x16x32_bf16 v[52:55], a[116:119], v[68:71], v[52:55]\n"
        "ds_read_b128 a[116:119], v240 offset:5120\n"
        "v_cvt_pk_bf16_f32 v81, v34, v35\n"
        "v_mfma_f32_16x16x32_bf16 v[52:55], a[112:115], v[64:67], v[52:55]\n"
        "ds_read_b128 a[112:115], v240 offset:4096\n"
        "v_cvt_pk_bf16_f32 v84, v36, v37\n"
        "v_mfma_f32_16x16x32_bf16 a[64:67], a[80:83], v[16:19], a[64:67]\n"
        "v_exp_f32 v39, v39\n"
        "v_mfma_f32_16x16x32_bf16 a[68:71], a[80:83], v[20:23], a[68:71]\n"
        "v_cvt_pk_bf16_f32 v85, v38, v39\n"
        "v_mfma_f32_16x16x32_bf16 v[160:163], a[144:147], v[16:19], v[160:163]\n"
        "v_mfma_f32_16x16x32_bf16 v[192:195], a[144:147], v[20:23], v[192:195]\n"
        "ds_read_b128 a[144:147], v241 offset:24576\n"
        "v_mfma_f32_16x16x32_bf16 v[196:199], a[148:151], v[20:23], v[196:199]\n"
        "v_mfma_f32_16x16x32_bf16 v[164:167], a[148:151], v[16:19], v[164:167]\n"
        "ds_read_b128 a[148:151], v241 offset:25600\n"
        "v_mfma_f32_16x16x32_bf16 v[168:171], a[152:155], v[16:19], v[168:171]\n"
        "v_mfma_f32_16x16x32_bf16 v[200:203], a[152:155], v[20:23], v[200:203]\n"
        "ds_read_b128 a[152:155], v241 offset:26624\n"
        "v_mfma_f32_16x16x32_bf16 v[204:207], a[156:159], v[20:23], v[204:207]\n"
        "s_cmp_lg_u32 s91, 0\n"
        "v_mfma_f32_16x16x32_bf16 v[172:175], a[156:159], v[16:19], v[172:175]\n"
        "s_cbranch_scc1 LSI_%=\n"
        "s_branch LTL_%=\n"
        "LEVS0_%=:\n"
        "s_lshl_b32 s97, s41, 12\n"
        "s_add_u32 s97, s97, 32768\n"
        "v_bfe_u32 v246, v254, 4, 5\n"
        "v_lshl_add_u32 v246, v246, 7, s97\n"
        "s_cmp_eq_u32 s82, s83\n"
        "s_cbranch_scc1 LE3S0_%=\n"
        "global_load_dwordx2 v[252:253], v246, s[48:49] sc1\n"
        "s_mov_b32 s24, s83\n"
        "v_accvgpr_read_b32 v246, a93\n"
        "global_load_dword v244, v246, s[46:47]\n"
        "s_branch LEWS0_%=\n"
        "LE3S0_%=:\n"
        "s_mov_b32 s97, 0x200000\n"
        "LE4S0_%=:\n"
        "v_cmp_ne_u64 vcc, v[252:253], v[250:251]\n"
        "s_nop 1\n"
        "s_cbranch_vccz LE5S0_%=\n"
        "s_sleep 2\n"
        "global_load_dwordx2 v[252:253], v246, s[48:49] sc1\n"
        "s_waitcnt vmcnt(0)\n"
        "s_sub_u32 s97, s97, 1\n"
        "s_cmp_lg_u32 s97, 0\n"
        "s_cbranch_scc1 LE4S0_%=\n"
        "LE5S0_%=:\n"
        "s_mov_b64 s[68:69], s[92:93]\n"
        "s_mov_b64 s[74:75], s[94:95]\n"
        "s_mov_b32 s80, s50\n"
        "s_mov_b32 s81, s51\n"
        "s_mov_b64 s[66:67], s[22:23]\n"
        "s_mov_b32 s90, s29\n"
        "s_mov_b32 s25, s30\n"
        "s_mov_b32 s28, 16384\n"
        "s_xor_b32 s79, s73, 0x8000\n"
        "v_accvgpr_read_b32 v144, a84\n"
        "v_accvgpr_read_b32 v145, a85\n"
        "v_accvgpr_read_b32 v146, a86\n"
        "v_accvgpr_read_b32 v147, a87\n"
        "v_accvgpr_read_b32 v148, a88\n"
        "v_accvgpr_read_b32 v152, a89\n"
        "v_accvgpr_read_b32 v252, a90\n"
        "v_accvgpr_read_b32 v249, a91\n"
        "v_accvgpr_read_b32 v153, a92\n"
        "v_mov_b32 v246, 0\n"
        "v_mov_b32 v247, 0\n"
        "v_mov_b32 v248, 0\n"
        "v_add_u32 v149, 1024, v148\n"
        "v_add_u32 v150, 2048, v148\n"
        "v_add_u32 v151, 3072, v148\n"
        "s_mov_b64 exec, 15\n"
        "ds_write_b128 v252, v[246:249]\n"
        "s_mov_b64 exec, -1\n"
        "s_branch LEWS0_%=\n"
        "; ---- BACK\n"
        "LTL_%=:\n"
        "v_mfma_f32_16x16x32_bf16 v[176:179], a[128:131], v[16:19], v[176:179]\n"
        "s_lshl_b32 s53, s45, 20\n"
        "s_lshl_b32 s96, s43, 15\n"
        "s_add_u32 s53, s53, s96\n"
        "v_mfma_f32_16x16x32_bf16 v[208:211], a[128:131], v[20:23], v[208:211]\n"
        "s_lshl_b32 s96, s40, 10\n"
        "s_add_u32 s53, s53, s96\n"
        "s_waitcnt lgkmcnt(4)\n"
        "v_mfma_f32_16x16x32_bf16 v[212:215], a[132:135], v[20:23], v[212:215]\n"
        "s_add_u32 s53, s53, 0x2001000\n"
        "s_add_u32 s70, s38, s53\n"
        "s_addc_u32 s71, s39, 0\n"
        "v_mfma_f32_16x16x32_bf16 v[180:183], a[132:135], v[16:19], v[180:183]\n"
        "ds_read_b128 a[156:159], v241 offset:27648\n"
        "s_add_u32 s16, s51, 4096\n"
        "v_add_u32 v230, 3584, v152\n"
        "v_mfma_f32_16x16x32_bf16 v[184:187], a[136:139], v[16:19], v[184:187]\n"
        "s_add_u32 m0, s73, 0x8000\n"
        "v_exp_f32 v48, v48\n"
        "v_mfma_f32_16x16x32_bf16 v[216:219], a[136:139], v[20:23], v[216:219]\n"
        "s_xor_b32 s66, s17, 9\n"
        "v_exp_f32 v49, v49\n"
        "v_mfma_f32_16x16x32_bf16 v[220:223], a[140:143], v[20:23], v[220:223]\n"
        "ds_read_b128 a[128:131], v241 offset:28672\n"
        "s_lshl_b32 s66, s66, 17\n"
        "v_cvt_pk_bf16_f32 v82, v48, v49\n"
        "v_mfma_f32_16x16x32_bf16 v[188:191], a[140:143], v[16:19], v[188:191]\n"
        "v_exp_f32 v50, v50\n"
        "v_exp_f32 v51, v51\n"
        "ds_read_b128 a[132:135], v241 offset:29696\n"
        "v_cvt_pk_bf16_f32 v83, v50, v51\n"
        "s_nop 1\n"
        "s_waitcnt lgkmcnt(5)\n"
        "v_mfma_f32_16x16x32_bf16 v[160:163], a[144:147], v[80:83], v[160:163]\n"
        "s_waitcnt lgkmcnt(3)\n"
        "v_exp_f32 v52, v52\n"
        "v_mfma_f32_16x16x32_bf16 a[64:67], a[80:83], v[80:83], a[64:67]\n"
        "s_waitcnt lgkmcnt(2)\n"
        "v_exp_f32 v53, v53\n"
        "v_mfma_f32_16x16x32_bf16 v[164:167], a[148:151], v[80:83], v[164:167]\n"
        "ds_read_b128 a[136:139], v241 offset:30720\n"
        "s_lshl_b32 s14, s10, 12\n"
        "v_cvt_pk_bf16_f32 v86, v52, v53\n"
        "v_mfma_f32_16x16x32_bf16 v[168:171], a[152:155], v[80:83], v[168:171]\n"
        "s_lshl1_add_u32 s62, s12, s10\n"
        "v_exp_f32 v54, v54\n"
        "v_mfma_f32_16x16x32_bf16 v[172:175], a[156:159], v[80:83], v[172:175]\n"
        "s_waitcnt lgkmcnt(2)\n"
        "v_exp_f32 v55, v55\n"
        "v_mfma_f32_16x16x32_bf16 v[176:179], a[128:131], v[80:83], v[176:179]\n"
        "ds_read_b128 a[140:143], v241 offset:31744\n"
        "ds_read2_b32 a[0:1], v148 offset0:0 offset1:1\n"
        "s_lshl_b32 s62, s62, 6\n"
        "v_cvt_pk_bf16_f32 v87, v54, v55\n"
        "s_nop 1\n"
        "v_mfma_f32_16x16x32_bf16 a[68:71], a[80:83], v[84:87], a[68:71]\n"
        "s_waitcnt lgkmcnt(3)\n"
        "v_accvgpr_read_b32 v248, a64\n"
        "v_min_u32 v230, s16, v230\n"
        "v_mfma_f32_16x16x32_bf16 v[192:195], a[144:147], v[84:87], v[192:195]\n"
        "s_lshl2_add_u32 s63, s11, s40\n"
        "v_rcp_f32 v246, v248\n"
        "v_mfma_f32_16x16x32_bf16 v[196:199], a[148:151], v[84:87], v[196:199]\n"
        "ds_read2_b32 a[2:3], v148 offset0:2 offset1:3\n"
        "s_lshl_b32 s63, s63, 5\n"
        "v_accvgpr_read_b32 v249, a68\n"
        "v_mfma_f32_16x16x32_bf16 v[200:203], a[152:155], v[84:87], v[200:203]\n"
        "s_waitcnt lgkmcnt(3)\n"
        "v_rcp_f32 v247, v249\n"
        "v_mfma_f32_16x16x32_bf16 v[204:207], a[156:159], v[84:87], v[204:207]\n"
        "ds_read_b128 a[192:195], v230 offset:0\n"
        "s_lshr_b32 s67, s40, 1\n"
        "v_mul_f32 v192, v192, v247\n"
        "v_mfma_f32_16x16x32_bf16 v[208:211], a[128:131], v[84:87], v[208:211]\n"
        "ds_read2_b32 a[4:5], v148 offset0:128 offset1:129\n"
        "s_xor_b32 s67, s67, s17\n"
        "v_mul_f32 v193, v193, v247\n"
        "v_mfma_f32_16x16x32_bf16 v[212:215], a[132:135], v[84:87], v[212:215]\n"
        "ds_read2_b32 a[6:7], v148 offset0:130 offset1:131\n"
        "v_mul_f32 v194, v194, v247\n"
        "v_mfma_f32_16x16x32_bf16 v[180:183], a[132:135], v[80:83], v[180:183]\n"
        "s_waitcnt lgkmcnt(4)\n"
        "v_mul_f32 v195, v195, v247\n"
        "v_mul_f32 v196, v196, v247\n"
        "v_mfma_f32_16x16x32_bf16 v[184:187], a[136:139], v[80:83], v[184:187]\n"
        "ds_read_b128 a[196:199], v152 offset:3072\n"
        "v_mul_f32 v197, v197, v247\n"
        "v_mfma_f32_16x16x32_bf16 v[216:219], a[136:139], v[84:87], v[216:219]\n"
        "ds_read2_b32 a[8:9], v149 offset0:0 offset1:1\n"
        "v_mul_f32 v198, v198, v247\n"
        "v_mfma_f32_16x16x32_bf16 v[220:223], a[140:143], v[84:87], v[220:223]\n"
        "ds_read2_b32 a[10:11], v149 offset0:2 offset1:3\n"
        "v_mul_f32 v199, v199, v247\n"
        "v_mfma_f32_16x16x32_bf16 v[188:191], a[140:143], v[80:83], v[188:191]\n"
        "ds_read_b128 a[200:203], v152 offset:2560\n"
        "v_cvt_pk_bf16_f32 v192, v192, v193\n"
        "s_waitcnt lgkmcnt(6)\n"
        "v_mfma_f32_16x16x32_bf16 v[96:99], a[0:3], a[192:195], 0\n"
        "ds_read2_b32 a[12:13], v149 offset0:128 offset1:129\n"
        "ds_read2_b32 a[14:15], v149 offset0:130 offset1:131\n"
        "ds_read_b128 a[204:207], v152 offset:2048\n"
        "ds_read2_b32 a[16:17], v150 offset0:0 offset1:1\n"
        "ds_read2_b32 a[18:19], v150 offset0:2 offset1:3\n"
        "ds_read_b128 a[208:211], v152 offset:1536\n"
        "s_add_u32 s60, s22, s66\n"
        "s_addc_u32 s61, s23, 0\n"
        "v_cvt_pk_bf16_f32 v193, v194, v195\n"
        "s_waitcnt lgkmcnt(9)\n"
        "v_mfma_f32_16x16x32_bf16 v[100:103], a[0:3], a[196:199], 0\n"
        "ds_read2_b32 a[20:21], v150 offset0:128 offset1:129\n"
        "s_xor_b32 s66, s17, 14\n"
        "v_cvt_pk_bf16_f32 v194, v196, v197\n"
        "v_mfma_f32_16x16x32_bf16 v[100:103], a[4:7], a[192:195], v[100:103]\n"
        "s_lshl_b32 s66, s66, 17\n"
        "v_cvt_pk_bf16_f32 v195, v198, v199\n"
        "s_waitcnt lgkmcnt(8)\n"
        "v_mfma_f32_16x16x32_bf16 v[104:107], a[8:11], a[192:195], 0\n"
        "global_store_dwordx4 v254, v[192:195], s[70:71] offset:-4096 sc1\n"
        "ds_read2_b32 a[22:23], v150 offset0:130 offset1:131\n"
        "ds_read_b128 a[212:215], v152 offset:1024\n"
        "v_mul_f32 v160, v160, v246\n"
        "v_mfma_f32_16x16x32_bf16 v[104:107], a[4:7], a[196:199], v[104:107]\n"
        "s_waitcnt lgkmcnt(9)\n"
        "v_mul_f32 v161, v161, v246\n"
        "v_mul_f32 v162, v162, v246\n"
        "v_mfma_f32_16x16x32_bf16 v[104:107], a[0:3], a[200:203], v[104:107]\n"
        "s_waitcnt lgkmcnt(6)\n"
        "v_mul_f32 v163, v163, v246\n"
        "v_mul_f32 v164, v164, v246\n"
        "v_mfma_f32_16x16x32_bf16 v[108:111], a[0:3], a[204:207], 0\n"
        "s_waitcnt lgkmcnt(4)\n"
        "v_mul_f32 v165, v165, v246\n"
        "v_mul_f32 v166, v166, v246\n"
        "v_mfma_f32_16x16x32_bf16 v[108:111], a[4:7], a[200:203], v[108:111]\n"
        "ds_read2_b32 a[24:25], v151 offset0:0 offset1:1\n"
        "v_mul_f32 v167, v167, v246\n"
        "v_mfma_f32_16x16x32_bf16 v[108:111], a[8:11], a[196:199], v[108:111]\n"
        "ds_read2_b32 a[26:27], v151 offset0:2 offset1:3\n"
        "LHC2_%=:\n"
        "s_cbranch_execz LHRC_%=\n"
        "v_cvt_pk_bf16_f32 v160, v160, v161\n"
        "v_mfma_f32_16x16x32_bf16 v[108:111], a[12:15], a[192:195], v[108:111]\n"
        "ds_read_b128 a[216:219], v152 offset:512\n"
        "v_cvt_pk_bf16_f32 v161, v162, v163\n"
        "v_mfma_f32_16x16x32_bf16 v[128:131], a[16:19], a[192:195], 0\n"
        "s_waitcnt lgkmcnt(5)\n"
        "v_cvt_pk_bf16_f32 v162, v164, v165\n"
        "v_cvt_pk_bf16_f32 v163, v166, v167\n"
        "v_mfma_f32_16x16x32_bf16 v[128:131], a[12:15], a[196:199], v[128:131]\n"
        "global_store_dwordx4 v254, v[160:163], s[70:71] offset:0 sc1\n"
        "s_add_u32 s70, s70, 0x2000\n"
        "s_addc_u32 s71, s71, 0\n"
        "s_waitcnt lgkmcnt(3)\n"
        "v_mul_f32 v200, v200, v247\n"
        "v_mul_f32 v201, v201, v247\n"
        "v_mfma_f32_16x16x32_bf16 v[128:131], a[8:11], a[200:203], v[128:131]\n"
        "ds_read2_b32 a[28:29], v151 offset0:128 offset1:129\n"
        "v_mul_f32 v202, v202, v247\n"
        "v_mfma_f32_16x16x32_bf16 v[128:131], a[4:7], a[204:207], v[128:131]\n"
        "ds_read2_b32 a[30:31], v151 offset0:130 offset1:131\n"
        "v_mul_f32 v203, v203, v247\n"
        "v_mfma_f32_16x16x32_bf16 v[128:131], a[0:3], a[208:211], v[128:131]\n"
        "ds_read_b128 a[220:223], v152 offset:0\n"
        "v_mul_f32 v204, v204, v247\n"
        "v_mfma_f32_16x16x32_bf16 v[132:135], a[0:3], a[212:215], 0\n"
        "s_waitcnt lgkmcnt(4)\n"
        "v_mul_f32 v205, v205, v247\n"
        "v_mul_f32 v206, v206, v247\n"
        "v_mfma_f32_16x16x32_bf16 v[132:135], a[4:7], a[208:211], v[132:135]\n"
        "ds_read2_b32 a[32:33], v148 offset0:16 offset1:17\n"
        "v_mul_f32 v207, v207, v247\n"
        "v_mfma_f32_16x16x32_bf16 v[132:135], a[8:11], a[204:207], v[132:135]\n"
        "ds_read2_b32 a[34:35], v148 offset0:18 offset1:19\n"
        "v_cvt_pk_bf16_f32 v200, v200, v201\n"
        "v_mfma_f32_16x16x32_bf16 v[132:135], a[12:15], a[200:203], v[132:135]\n"
        "ds_read2_b32 a[36:37], v148 offset0:144 offset1:145\n"
        "v_cvt_pk_bf16_f32 v201, v202, v203\n"
        "v_mfma_f32_16x16x32_bf16 v[132:135], a[16:19], a[196:199], v[132:135]\n"
        "ds_read2_b32 a[38:39], v148 offset0:146 offset1:147\n"
        "v_cvt_pk_bf16_f32 v202, v204, v205\n"
        "v_mfma_f32_16x16x32_bf16 v[132:135], a[20:23], a[192:195], v[132:135]\n"
        "ds_read_b128 a[228:231], v152 offset:3136\n"
        "v_cvt_pk_bf16_f32 v203, v206, v207\n"
        "v_mfma_f32_16x16x32_bf16 v[136:139], a[24:27], a[192:195], 0\n"
        "global_store_dwordx4 v254, v[200:203], s[70:71] offset:-4096 sc1\n"
        "s_waitcnt lgkmcnt(6)\n"
        "v_mul_f32 v168, v168, v246\n"
        "v_mul_f32 v169, v169, v246\n"
        "v_mul_f32 v170, v170, v246\n"
        "v_mfma_f32_16x16x32_bf16 v[136:139], a[20:23], a[196:199], v[136:139]\n"
        "s_waitcnt lgkmcnt(5)\n"
        "v_mul_f32 v171, v171, v246\n"
        "v_mul_f32 v172, v172, v246\n"
        "v_mfma_f32_16x16x32_bf16 v[136:139], a[16:19], a[200:203], v[136:139]\n"
        "ds_read2_b32 a[40:41], v149 offset0:16 offset1:17\n"
        "v_mul_f32 v173, v173, v246\n"
        "v_mfma_f32_16x16x32_bf16 v[136:139], a[12:15], a[204:207], v[136:139]\n"
        "ds_read2_b32 a[42:43], v149 offset0:18 offset1:19\n"
        "v_mul_f32 v174, v174, v246\n"
        "v_mfma_f32_16x16x32_bf16 v[136:139], a[8:11], a[208:211], v[136:139]\n"
        "ds_read_b128 a[232:235], v152 offset:2624\n"
        "v_mul_f32 v175, v175, v246\n"
        "v_mfma_f32_16x16x32_bf16 v[136:139], a[4:7], a[212:215], v[136:139]\n"
        "s_waitcnt lgkmcnt(5)\n"
        "v_cvt_pk_bf16_f32 v168, v168, v169\n"
        "v_cvt_pk_bf16_f32 v169, v170, v171\n"
        "v_mfma_f32_16x16x32_bf16 v[136:139], a[0:3], a[216:219], v[136:139]\n"
        "s_waitcnt lgkmcnt(3)\n"
        "v_cvt_pk_bf16_f32 v170, v172, v173\n"
        "v_cvt_pk_bf16_f32 v171, v174, v175\n"
        "v_mfma_f32_16x16x32_bf16 v[140:143], a[0:3], a[220:223], 0\n"
        "global_store_dwordx4 v254, v[168:171], s[70:71] offset:0 sc1\n"
        "s_add_u32 s70, s70, 0x2000\n"
        "s_addc_u32 s71, s71, 0\n"
        "ds_read2_b32 a[44:45], v149 offset0:144 offset1:145\n"
        "v_mul_f32 v208, v208, v247\n"
        "v_mfma_f32_16x16x32_bf16 v[140:143], a[4:7], a[216:219], v[140:143]\n"
        "ds_read2_b32 a[46:47], v149 offset0:146 offset1:147\n"
        "v_mul_f32 v209, v209, v247\n"
        "v_mfma_f32_16x16x32_bf16 v[140:143], a[8:11], a[212:215], v[140:143]\n"
        "ds_read_b128 a[236:239], v152 offset:2112\n"
        "v_mul_f32 v210, v210, v247\n"
        "v_mfma_f32_16x16x32_bf16 v[140:143], a[12:15], a[208:211], v[140:143]\n"
        "ds_read2_b32 a[48:49], v150 offset0:16 offset1:17\n"
        "v_mul_f32 v211, v211, v247\n"
        "v_mfma_f32_16x16x32_bf16 v[140:143], a[16:19], a[204:207], v[140:143]\n"
        "ds_read2_b32 a[50:51], v150 offset0:18 offset1:19\n"
        "v_mul_f32 v212, v212, v247\n"
        "v_mfma_f32_16x16x32_bf16 v[140:143], a[20:23], a[200:203], v[140:143]\n"
        "ds_read_b128 a[240:243], v152 offset:1600\n"
        "v_mul_f32 v213, v213, v247\n"
        "v_mfma_f32_16x16x32_bf16 v[140:143], a[24:27], a[196:199], v[140:143]\n"
        "ds_read2_b32 a[52:53], v150 offset0:144 offset1:145\n"
        "v_mul_f32 v214, v214, v247\n"
        "v_mfma_f32_16x16x32_bf16 v[140:143], a[28:31], a[192:195], v[140:143]\n"
        "ds_read2_b32 a[54:55], v150 offset0:146 offset1:147\n"
        "v_mul_f32 v215, v215, v247\n"
        "v_mfma_f32_16x16x32_bf16 v[100:103], a[32:35], a[228:231], v[100:103]\n"
        "ds_read_b128 a[244:247], v152 offset:1088\n"
        "ds_read2_b32 a[56:57], v151 offset0:16 offset1:17\n"
        "ds_read2_b32 a[58:59], v151 offset0:18 offset1:19\n"
        "v_cvt_pk_bf16_f32 v208, v208, v209\n"
        "s_waitcnt lgkmcnt(8)\n"
        "v_mfma_f32_16x16x32_bf16 v[108:111], a[32:35], a[236:239], v[108:111]\n"
        "ds_read_b128 a[248:251], v152 offset:576\n"
        "v_cvt_pk_bf16_f32 v209, v210, v211\n"
        "v_mfma_f32_16x16x32_bf16 v[108:111], a[36:39], a[232:235], v[108:111]\n"
        "ds_read2_b32 a[60:61], v151 offset0:144 offset1:145\n"
        "v_cvt_pk_bf16_f32 v210, v212, v213\n"
        "v_mfma_f32_16x16x32_bf16 v[108:111], a[40:43], a[228:231], v[108:111]\n"
        "ds_read2_b32 a[62:63], v151 offset0:146 offset1:147\n"
        "ds_read_b128 a[252:255], v152 offset:64\n"
        "ds_read2_b32 a[0:1], v148 offset0:32 offset1:33\n"
        "ds_read2_b32 a[2:3], v148 offset0:34 offset1:35\n"
        "ds_read2_b32 a[4:5], v148 offset0:160 offset1:161\n"
        "ds_read2_b32 a[6:7], v148 offset0:162 offset1:163\n"
        "s_waitcnt vmcnt(4)\n"
        "v_cvt_pk_bf16_f32 v211, v214, v215\n"
        "s_waitcnt lgkmcnt(10)\n"
        "v_mfma_f32_16x16x32_bf16 v[132:135], a[32:35], a[244:247], v[132:135]\n"
        "global_store_dwordx4 v254, v[208:211], s[70:71] offset:-4096 sc1\n"
        "ds_read_b128 a[196:199], v152 offset:3200\n"
        "ds_read2_b32 a[8:9], v149 offset0:32 offset1:33\n"
        "v_mul_f32 v176, v176, v246\n"
        "v_mfma_f32_16x16x32_bf16 v[132:135], a[36:39], a[240:243], v[132:135]\n"
        "ds_read2_b32 a[10:11], v149 offset0:34 offset1:35\n"
        "v_mul_f32 v177, v177, v246\n"
        "v_mfma_f32_16x16x32_bf16 v[132:135], a[40:43], a[236:239], v[132:135]\n"
        "s_waitcnt lgkmcnt(10)\n"
        "v_mul_f32 v178, v178, v246\n"
        "v_mul_f32 v179, v179, v246\n"
        "v_mfma_f32_16x16x32_bf16 v[132:135], a[44:47], a[232:235], v[132:135]\n"
        "s_waitcnt lgkmcnt(9)\n"
        "v_mul_f32 v180, v180, v246\n"
        "v_mul_f32 v181, v181, v246\n"
        "v_mfma_f32_16x16x32_bf16 v[132:135], a[48:51], a[228:231], v[132:135]\n"
        "s_waitcnt lgkmcnt(7)\n"
        "v_mul_f32 v182, v182, v246\n"
        "v_mul_f32 v183, v183, v246\n"
        "v_cvt_pk_bf16_f32 v176, v176, v177\n"
        "v_cvt_pk_bf16_f32 v177, v178, v179\n"
        "v_cvt_pk_bf16_f32 v178, v180, v181\n"
        "v_cvt_pk_bf16_f32 v179, v182, v183\n"
        "global_store_dwordx4 v254, v[176:179], s[70:71] offset:0 sc1\n"
        "s_add_u32 s70, s70, 0x2000\n"
        "s_addc_u32 s71, s71, 0\n"
        "ds_read_b128 a[200:203], v152 offset:2688\n"
        "v_mul_f32 v216, v216, v247\n"
        "v_mul_f32 v217, v217, v247\n"
        "v_mul_f32 v218, v218, v247\n"
        "v_mul_f32 v219, v219, v247\n"
        "v_mul_f32 v220, v220, v247\n"
        "v_mul_f32 v221, v221, v247\n"
        "v_mul_f32 v222, v222, v247\n"
        "v_mul_f32 v223, v223, v247\n"
        "v_cvt_pk_bf16_f32 v216, v216, v217\n"
        "v_cvt_pk_bf16_f32 v217, v218, v219\n"
        "v_cvt_pk_bf16_f32 v218, v220, v221\n"
        "v_cvt_pk_bf16_f32 v219, v222, v223\n"
        "global_store_dwordx4 v254, v[216:219], s[70:71] offset:-4096 sc1\n"
        "ds_read2_b32 a[12:13], v149 offset0:160 offset1:161\n"
        "ds_read2_b32 a[14:15], v149 offset0:162 offset1:163\n"
        "v_mul_f32 v184, v184, v246\n"
        "v_mul_f32 v185, v185, v246\n"
        "v_mul_f32 v186, v186, v246\n"
        "v_mul_f32 v187, v187, v246\n"
        "v_mul_f32 v188, v188, v246\n"
        "v_mul_f32 v189, v189, v246\n"
        "v_mul_f32 v190, v190, v246\n"
        "v_mul_f32 v191, v191, v246\n"
        "v_cvt_pk_bf16_f32 v184, v184, v185\n"
        "v_cvt_pk_bf16_f32 v185, v186, v187\n"
        "v_cvt_pk_bf16_f32 v186, v188, v189\n"
        "v_cvt_pk_bf16_f32 v187, v190, v191\n"
        "global_store_dwordx4 v254, v[184:187], s[70:71] offset:0 sc1\n"
        "ds_read_b128 a[204:207], v152 offset:2176\n"
        "ds_read2_b32 a[16:17], v150 offset0:32 offset1:33\n"
        "v_add_f32 v247, v248, v249\n"
        "v_cmp_nge_f32 vcc, 0x4e6e6b28, v247\n"
        "v_mfma_f32_16x16x32_bf16 v[140:143], a[32:35], a[252:255], v[140:143]\n"
        "s_cbranch_vccnz LRH_%=\n"
        "LSH_%=:\n"
        "v_add_u32 v231, 3648, v152\n"
        "v_min_u32 v231, s16, v231\n"
        "v_mfma_f32_16x16x32_bf16 v[140:143], a[36:39], a[248:251], v[140:143]\n"
        "ds_read_b128 a[224:227], v231 offset:0\n"
        "v_xor_b32 v154, 32, v153\n"
        "v_mfma_f32_16x16x32_bf16 v[140:143], a[40:43], a[244:247], v[140:143]\n"
        "ds_read2_b32 a[18:19], v150 offset0:34 offset1:35\n"
        "v_add_u32 v155, 0x10000, v153\n"
        "v_mfma_f32_16x16x32_bf16 v[140:143], a[44:47], a[240:243], v[140:143]\n"
        "ds_read_b128 a[208:211], v152 offset:1664\n"
        "v_add_u32 v156, 0x10000, v154\n"
        "v_mfma_f32_16x16x32_bf16 v[140:143], a[48:51], a[236:239], v[140:143]\n"
        "ds_read2_b32 a[20:21], v150 offset0:160 offset1:161\n"
        "v_add_u32 v157, 0x20000, v153\n"
        "v_mfma_f32_16x16x32_bf16 v[140:143], a[52:55], a[232:235], v[140:143]\n"
        "ds_read2_b32 a[22:23], v150 offset0:162 offset1:163\n"
        "v_add_u32 v158, 0x20000, v154\n"
        "v_mfma_f32_16x16x32_bf16 v[140:143], a[56:59], a[228:231], v[140:143]\n"
        "s_barrier\n"
        "global_load_lds_dwordx4 v144, s[60:61] offset:0\n"
        "ds_read_b64_tr_b16 v[160:161], v153 offset:0\n"
        "ds_read_b64_tr_b16 v[162:163], v153 offset:4096\n"
        "ds_read_b64_tr_b16 v[164:165], v154 offset:0\n"
        "v_add_u32 v230, 3712, v152\n"
        "s_waitcnt lgkmcnt(7)\n"
        "v_mfma_f32_16x16x32_bf16 v[96:99], a[32:35], a[224:227], v[96:99]\n"
        "ds_read_b64_tr_b16 v[166:167], v154 offset:4096\n"
        "v_min_u32 v230, s16, v230\n"
        "v_mfma_f32_16x16x32_bf16 v[100:103], a[36:39], a[224:227], v[100:103]\n"
        "ds_read_b128 a[192:195], v230 offset:0\n"
        "v_bfe_u32 v232, v254, 4, 4\n"
        "v_mfma_f32_16x16x32_bf16 v[104:107], a[40:43], a[224:227], v[104:107]\n"
        "global_load_lds_dwordx4 v145, s[60:61] offset:1024\n"
        "ds_read_b64_tr_b16 v[168:169], v153 offset:8192\n"
        "v_mfma_f32_16x16x32_bf16 v[104:107], a[36:39], a[228:231], v[104:107]\n"
        "ds_read_b64_tr_b16 v[170:171], v153 offset:12288\n"
        "v_lshrrev_b32 v233, 8, v254\n"
        "v_mfma_f32_16x16x32_bf16 v[104:107], a[32:35], a[232:235], v[104:107]\n"
        "ds_read_b64_tr_b16 v[172:173], v154 offset:8192\n"
        "v_add_u32 v238, s14, v254\n"
        "v_mfma_f32_16x16x32_bf16 v[108:111], a[44:47], a[224:227], v[108:111]\n"
        "ds_read_b64_tr_b16 v[174:175], v154 offset:12288\n"
        "s_lshl_b32 s14, s44, 5\n"
        "s_add_u32 s14, s14, s43\n"
        "v_mfma_f32_16x16x32_bf16 v[128:131], a[48:51], a[224:227], v[128:131]\n"
        "global_load_lds_dwordx4 v146, s[60:61] offset:2048\n"
        "ds_read_b64_tr_b16 v[176:177], v153 offset:16384\n"
        "s_lshl2_add_u32 s14, s14, s40\n"
        "v_mfma_f32_16x16x32_bf16 v[128:131], a[44:47], a[228:231], v[128:131]\n"
        "ds_read_b64_tr_b16 v[178:179], v153 offset:20480\n"
        "s_add_u32 s14, s14, 1024\n"
        "s_lshl_b32 s14, s14, 7\n"
        "v_mfma_f32_16x16x32_bf16 v[128:131], a[40:43], a[232:235], v[128:131]\n"
        "ds_read_b64_tr_b16 v[180:181], v154 offset:16384\n"
        "v_lshrrev_b32 v234, 1, v254\n"
        "v_mfma_f32_16x16x32_bf16 v[128:131], a[36:39], a[236:239], v[128:131]\n"
        "ds_read_b64_tr_b16 v[182:183], v154 offset:20480\n"
        "v_add_u32 v234, s14, v234\n"
        "v_mfma_f32_16x16x32_bf16 v[128:131], a[32:35], a[240:243], v[128:131]\n"
        "global_load_lds_dwordx4 v147, s[60:61] offset:3072\n"
        "s_add_u32 m0, s73, 0xc000\n"
        "s_lshl_b32 s14, s45, 5\n"
        "v_mfma_f32_16x16x32_bf16 v[132:135], a[52:55], a[224:227], v[132:135]\n"
        "s_add_u32 s60, s22, s66\n"
        "s_addc_u32 s61, s23, 0\n"
        "s_add_u32 s14, s14, s43\n"
        "v_mfma_f32_16x16x32_bf16 v[136:139], a[56:59], a[224:227], v[136:139]\n"
        "ds_read_b128 a[212:215], v152 offset:1152\n"
        "s_lshl2_add_u32 s14, s14, s40\n"
        "s_add_u32 s14, s14, 1024\n"
        "v_mfma_f32_16x16x32_bf16 v[136:139], a[52:55], a[228:231], v[136:139]\n"
        "ds_read_b64_tr_b16 v[184:185], v153 offset:24576\n"
        "s_lshl_b32 s14, s14, 7\n"
        "s_xor_b32 s66, s17, 19\n"
        "v_mfma_f32_16x16x32_bf16 v[136:139], a[48:51], a[232:235], v[136:139]\n"
        "global_load_lds_dwordx4 v144, s[60:61] offset:0\n"
        "ds_read_b64_tr_b16 v[186:187], v153 offset:28672\n"
        "s_lshl_b32 s66, s66, 17\n"
        "v_mfma_f32_16x16x32_bf16 v[136:139], a[44:47], a[236:239], v[136:139]\n"
        "ds_read2_b32 a[24:25], v151 offset0:32 offset1:33\n"
        "v_add_u32 v231, 3776, v152\n"
        "v_mfma_f32_16x16x32_bf16 v[136:139], a[40:43], a[240:243], v[136:139]\n"
        "ds_read2_b32 a[26:27], v151 offset0:34 offset1:35\n"
        "v_min_u32 v231, s16, v231\n"
        "v_mfma_f32_16x16x32_bf16 v[136:139], a[36:39], a[244:247], v[136:139]\n"
        "ds_read_b128 a[216:219], v152 offset:640\n"
        "v_add_u32 v230, 3840, v152\n"
        "v_mfma_f32_16x16x32_bf16 v[136:139], a[32:35], a[248:251], v[136:139]\n"
        "global_load_lds_dwordx4 v145, s[60:61] offset:1024\n"
        "ds_read_b64_tr_b16 v[188:189], v154 offset:24576\n"
        "v_mfma_f32_16x16x32_bf16 v[140:143], a[60:63], a[224:227], v[140:143]\n"
        "ds_read_b64_tr_b16 v[190:191], v154 offset:28672\n"
        "s_waitcnt lgkmcnt(8)\n"
        "v_mfma_f32_16x16x32_bf16 v[96:99], a[0:3], a[192:195], v[96:99]\n"
        "ds_read2_b32 a[28:29], v151 offset0:160 offset1:161\n"
        "v_min_u32 v230, s16, v230\n"
        "v_mfma_f32_16x16x32_bf16 v[100:103], a[0:3], a[196:199], v[100:103]\n"
        "ds_read2_b32 a[30:31], v151 offset0:162 offset1:163\n"
        "v_xor_b32 v224, 0x1000, v238\n"
        "v_mfma_f32_16x16x32_bf16 v[100:103], a[4:7], a[192:195], v[100:103]\n"
        "global_load_lds_dwordx4 v146, s[60:61] offset:2048\n"
        "ds_read_b128 a[220:223], v152 offset:128\n"
        "v_mfma_f32_16x16x32_bf16 v[104:107], a[8:11], a[192:195], v[104:107]\n"
        "s_mov_b64 exec, 0\n"
        "s_cmp_eq_u32 s40, 0\n"
        "s_cbranch_scc1 LHB0_%=\n"
        "s_cmp_eq_u32 s40, 1\n"
        "s_cbranch_scc1 LHB1_%=\n"
        "s_cmp_eq_u32 s40, 2\n"
        "s_cbranch_scc1 LHB2_%=\n"
        "s_branch LHB3_%=\n"
        "LHRB_%=:\n"
        "s_mov_b64 exec, -1\n"
        "v_mfma_f32_16x16x32_bf16 v[104:107], a[4:7], a[196:199], v[104:107]\n"
        "s_waitcnt vmcnt(7)\n"
        "ds_read2_b32 a[32:33], v148 offset0:48 offset1:49\n"
        "v_mfma_f32_16x16x32_bf16 v[104:107], a[0:3], a[200:203], v[104:107]\n"
        "s_mov_b64 exec, 0xff\n"
        "global_store_dwordx2 v234, v[250:251], s[48:49] sc1\n"
        "s_mov_b64 exec, -1\n"
        "v_mfma_f32_16x16x32_bf16 v[108:111], a[0:3], a[204:207], v[108:111]\n"
        "global_load_lds_dwordx4 v147, s[60:61] offset:3072\n"
        "s_add_u32 m0, s73, 0x10000\n"
        "v_lshrrev_b32 v234, 1, v254\n"
        "v_mfma_f32_16x16x32_bf16 v[108:111], a[4:7], a[200:203], v[108:111]\n"
        "s_waitcnt lgkmcnt(4)\n"
        "v_add_u32 v234, s14, v234\n"
        "v_mfma_f32_16x16x32_bf16 v[108:111], a[8:11], a[196:199], v[108:111]\n"
        "s_barrier\n"
        "v_mfma_f32_16x16x32_bf16 v[108:111], a[12:15], a[192:195], v[108:111]\n"
        "s_mov_b64 exec, 0xff\n"
        "global_store_dwordx2 v234, v[250:251], s[48:49] sc1\n"
        "s_mov_b64 exec, -1\n"
        "s_lshl_b32 s14, s62, 11\n"
        "v_mfma_f32_16x16x32_bf16 v[128:131], a[16:19], a[192:195], v[128:131]\n"
        "s_add_u32 s60, s22, s66\n"
        "s_addc_u32 s61, s23, 0\n"
        "s_xor_b32 s66, s17, 24\n"
        "v_mfma_f32_16x16x32_bf16 v[128:131], a[12:15], a[196:199], v[128:131]\n"
        "global_load_lds_dwordx4 v144, s[60:61] offset:0\n"
        "ds_read2_b32 a[34:35], v148 offset0:50 offset1:51\n"
        "s_lshl_b32 s66, s66, 17\n"
        "v_mfma_f32_16x16x32_bf16 v[128:131], a[8:11], a[200:203], v[128:131]\n"
        "ds_read_b128 a[224:227], v231 offset:0\n"
        "s_lshl1_add_u32 s14, s63, s14\n"
        "v_add_u32 v225, 0x10000, v224\n"
        "v_mfma_f32_16x16x32_bf16 v[128:131], a[4:7], a[204:207], v[128:131]\n"
        "ds_read2_b32 a[36:37], v148 offset0:176 offset1:177\n"
        "v_add_u32 v239, 0x10000, v238\n"
        "v_mfma_f32_16x16x32_bf16 v[128:131], a[0:3], a[208:211], v[128:131]\n"
        "ds_read2_b32 a[38:39], v148 offset0:178 offset1:179\n"
        "v_add_u32 v242, 0x10000, v254\n"
        "v_mfma_f32_16x16x32_bf16 v[132:135], a[0:3], a[212:215], v[132:135]\n"
        "global_load_lds_dwordx4 v145, s[60:61] offset:1024\n"
        "ds_read_b128 a[228:231], v152 offset:3264\n"
        "v_mfma_f32_16x16x32_bf16 v[132:135], a[4:7], a[208:211], v[132:135]\n"
        "ds_read2_b32 a[40:41], v149 offset0:48 offset1:49\n"
        "v_add_u32 v226, 0x20000, v224\n"
        "v_mfma_f32_16x16x32_bf16 v[132:135], a[8:11], a[204:207], v[132:135]\n"
        "ds_read2_b32 a[42:43], v149 offset0:50 offset1:51\n"
        "v_add_u32 v240, 0x20000, v238\n"
        "v_mfma_f32_16x16x32_bf16 v[132:135], a[12:15], a[200:203], v[132:135]\n"
        "ds_read_b128 a[232:235], v152 offset:2752\n"
        "v_add_u32 v243, 0x20000, v254\n"
        "v_mfma_f32_16x16x32_bf16 v[132:135], a[16:19], a[196:199], v[132:135]\n"
        "global_load_lds_dwordx4 v146, s[60:61] offset:2048\n"
        "ds_read2_b32 a[44:45], v149 offset0:176 offset1:177\n"
        "v_mfma_f32_16x16x32_bf16 v[132:135], a[20:23], a[192:195], v[132:135]\n"
        "ds_read2_b32 a[46:47], v149 offset0:178 offset1:179\n"
        "s_add_u32 s64, s8, s14\n"
        "s_addc_u32 s65, s9, 0\n"
        "v_mfma_f32_16x16x32_bf16 v[136:139], a[24:27], a[192:195], v[136:139]\n"
        "ds_read_b128 a[236:239], v152 offset:2240\n"
        "s_add_u32 s56, s36, s14\n"
        "s_addc_u32 s57, s37, 0\n"
        "v_mfma_f32_16x16x32_bf16 v[136:139], a[20:23], a[196:199], v[136:139]\n"
        "ds_read2_b32 a[48:49], v150 offset0:48 offset1:49\n"
        "s_lshl1_add_u32 s14, s12, s10\n"
        "v_add_u32 v231, 3904, v152\n"
        "v_mfma_f32_16x16x32_bf16 v[136:139], a[16:19], a[200:203], v[136:139]\n"
        "global_load_lds_dwordx4 v147, s[60:61] offset:3072\n"
        "s_add_u32 m0, s73, 0x14000\n"
        "v_min_u32 v231, s16, v231\n"
        "v_mfma_f32_16x16x32_bf16 v[136:139], a[12:15], a[204:207], v[136:139]\n"
        "s_add_u32 s60, s22, s66\n"
        "s_addc_u32 s61, s23, 0\n"
        "s_xor_b32 s66, s17, 25\n"
        "v_mfma_f32_16x16x32_bf16 v[136:139], a[8:11], a[208:211], v[136:139]\n"
        "ds_read2_b32 a[50:51], v150 offset0:50 offset1:51\n"
        "s_lshl_b32 s66, s66, 17\n"
        "s_lshl_b32 s14, s14, 6\n"
        "v_mfma_f32_16x16x32_bf16 v[136:139], a[4:7], a[212:215], v[136:139]\n"
        "ds_read_b128 a[240:243], v152 offset:1728\n"
        "s_add_u32 s14, s14, 1024\n"
        "v_lshlrev_b32 v159, 3, v254\n"
        "v_mfma_f32_16x16x32_bf16 v[136:139], a[0:3], a[216:219], v[136:139]\n"
        "global_load_lds_dwordx4 v144, s[60:61] offset:0\n"
        "s_lshl_b32 s14, s14, 7\n"
        "s_waitcnt lgkmcnt(5)\n"
        "v_mfma_f32_16x16x32_bf16 v[140:143], a[0:3], a[220:223], v[140:143]\n"
        "ds_read2_b32 a[52:53], v150 offset0:176 offset1:177\n"
        "v_add_u32 v159, s14, v159\n"
        "v_mfma_f32_16x16x32_bf16 v[140:143], a[4:7], a[216:219], v[140:143]\n"
        "ds_read2_b32 a[54:55], v150 offset0:178 offset1:179\n"
        "s_add_u32 s14, s51, 504\n"
        "v_lshlrev_b32 v229, 5, v232\n"
        "v_mfma_f32_16x16x32_bf16 v[140:143], a[8:11], a[212:215], v[140:143]\n"
        "ds_read_b128 a[244:247], v152 offset:1216\n"
        "v_lshl_add_u32 v229, v233, 3, v229\n"
        "v_mfma_f32_16x16x32_bf16 v[140:143], a[12:15], a[208:211], v[140:143]\n"
        "global_load_lds_dwordx4 v145, s[60:61] offset:1024\n"
        "ds_read2_b32 a[56:57], v151 offset0:48 offset1:49\n"
        "v_mfma_f32_16x16x32_bf16 v[140:143], a[16:19], a[204:207], v[140:143]\n"
        "ds_read2_b32 a[58:59], v151 offset0:50 offset1:51\n"
        "v_sub_u32 v229, s14, v229\n"
        "v_mfma_f32_16x16x32_bf16 v[140:143], a[20:23], a[200:203], v[140:143]\n"
        "ds_read_b128 a[248:251], v152 offset:704\n"
        "s_add_u32 s24, s94, 0x400000\n"
        "s_addc_u32 s25, s95, 0\n"
        "v_mfma_f32_16x16x32_bf16 v[140:143], a[24:27], a[196:199], v[140:143]\n"
        "ds_read2_b32 a[60:61], v151 offset0:176 offset1:177\n"
        "s_lshl_b32 s14, s41, 5\n"
        "s_add_u32 s14, s14, s42\n"
        "v_mfma_f32_16x16x32_bf16 v[140:143], a[28:31], a[192:195], v[140:143]\n"
        "global_load_lds_dwordx4 v146, s[60:61] offset:2048\n"
        "ds_read2_b32 a[62:63], v151 offset0:178 offset1:179\n"
        "s_add_u32 s14, s14, 512\n"
        "v_mfma_f32_16x16x32_bf16 v[96:99], a[32:35], a[224:227], v[96:99]\n"
        "ds_read_b128 a[252:255], v152 offset:192\n"
        "s_lshl_b32 s14, s14, 7\n"
        "v_lshrrev_b32 v234, 1, v254\n"
        "v_mfma_f32_16x16x32_bf16 v[100:103], a[32:35], a[228:231], v[100:103]\n"
        "ds_read2_b32 a[0:1], v148 offset0:64 offset1:65\n"
        "v_add_u32 v234, s14, v234\n"
        "v_mfma_f32_16x16x32_bf16 v[100:103], a[36:39], a[224:227], v[100:103]\n"
        "ds_read2_b32 a[2:3], v148 offset0:66 offset1:67\n"
        "s_lshr_b32 s14, s63, 2\n"
        "s_add_u32 s14, s14, 512\n"
        "v_mfma_f32_16x16x32_bf16 v[104:107], a[40:43], a[224:227], v[104:107]\n"
        "global_load_lds_dwordx4 v147, s[60:61] offset:3072\n"
        "s_add_u32 m0, s73, 0x0\n"
        "s_lshl_b32 s14, s14, 7\n"
        "v_mfma_f32_16x16x32_bf16 v[104:107], a[36:39], a[228:231], v[104:107]\n"
        "s_add_u32 s60, s22, s66\n"
        "s_addc_u32 s61, s23, 0\n"
        "s_xor_b32 s66, s17, 26\n"
        "v_mfma_f32_16x16x32_bf16 v[104:107], a[32:35], a[232:235], v[104:107]\n"
        "ds_read_b128 a[192:195], v230 offset:0\n"
        "s_lshl_b32 s66, s66, 17\n"
        "s_waitcnt lgkmcnt(6)\n"
        "v_mfma_f32_16x16x32_bf16 v[108:111], a[32:35], a[236:239], v[108:111]\n"
        "ds_read2_b32 a[4:5], v148 offset0:192 offset1:193\n"
        "v_add_u32 v230, 3968, v152\n"
        "v_mfma_f32_16x16x32_bf16 v[108:111], a[36:39], a[232:235], v[108:111]\n"
        "global_load_lds_dwordx4 v144, s[60:61] offset:0\n"
        "ds_read2_b32 a[6:7], v148 offset0:194 offset1:195\n"
        "v_mfma_f32_16x16x32_bf16 v[108:111], a[40:43], a[228:231], v[108:111]\n"
        "ds_read_b128 a[196:199], v152 offset:3328\n"
        "v_min_u32 v230, s16, v230\n"
        "v_mfma_f32_16x16x32_bf16 v[108:111], a[44:47], a[224:227], v[108:111]\n"
        "ds_read2_b32 a[8:9], v149 offset0:64 offset1:65\n"
        "v_and_b32 v246, 1, v232\n"
        "v_mfma_f32_16x16x32_bf16 v[128:131], a[48:51], a[224:227], v[128:131]\n"
        "ds_read2_b32 a[10:11], v149 offset0:66 offset1:67\n"
        "v_lshlrev_b32 v246, 4, v246\n"
        "v_mfma_f32_16x16x32_bf16 v[128:131], a[44:47], a[228:231], v[128:131]\n"
        "global_load_lds_dwordx4 v145, s[60:61] offset:1024\n"
        "ds_read_b128 a[200:203], v152 offset:2816\n"
        "v_mfma_f32_16x16x32_bf16 v[128:131], a[40:43], a[232:235], v[128:131]\n"
        "ds_read2_b32 a[12:13], v149 offset0:192 offset1:193\n"
        "v_mov_b32 v247, 0x3f80\n"
        "v_mfma_f32_16x16x32_bf16 v[128:131], a[36:39], a[236:239], v[128:131]\n"
        "ds_read2_b32 a[14:15], v149 offset0:194 offset1:195\n"
        "v_lshlrev_b32 v246, v246, v247\n"
        "v_mfma_f32_16x16x32_bf16 v[128:131], a[32:35], a[240:243], v[128:131]\n"
        "ds_read_b128 a[204:207], v152 offset:2304\n"
        "v_lshrrev_b32 v247, 1, v232\n"
        "v_mfma_f32_16x16x32_bf16 v[132:135], a[32:35], a[244:247], v[132:135]\n"
        "global_load_lds_dwordx4 v146, s[60:61] offset:2048\n"
        "ds_read2_b32 a[16:17], v150 offset0:64 offset1:65\n"
        "v_mfma_f32_16x16x32_bf16 v[132:135], a[36:39], a[240:243], v[132:135]\n"
        "ds_read2_b32 a[18:19], v150 offset0:66 offset1:67\n"
        "v_lshlrev_b32 v248, 1, v233\n"
        "v_mfma_f32_16x16x32_bf16 v[132:135], a[40:43], a[236:239], v[132:135]\n"
        "ds_read_b128 a[208:211], v152 offset:1792\n"
        "v_or_b32 v249, 1, v248\n"
        "v_mfma_f32_16x16x32_bf16 v[132:135], a[44:47], a[232:235], v[132:135]\n"
        "ds_read2_b32 a[20:21], v150 offset0:192 offset1:193\n"
        "s_waitcnt lgkmcnt(8)\n"
        "v_mfma_f32_16x16x32_bf16 v[132:135], a[48:51], a[228:231], v[132:135]\n"
        "global_load_lds_dwordx4 v147, s[60:61] offset:3072\n"
        "s_add_u32 m0, s73, 0x4000\n"
        "v_mfma_f32_16x16x32_bf16 v[132:135], a[52:55], a[224:227], v[132:135]\n"
        "s_waitcnt vmcnt(13)\n"
        "s_add_u32 s60, s22, s66\n"
        "s_addc_u32 s61, s23, 0\n"
        "v_mfma_f32_16x16x32_bf16 v[136:139], a[56:59], a[224:227], v[136:139]\n"
        "s_barrier\n"
        "v_mfma_f32_16x16x32_bf16 v[136:139], a[52:55], a[228:231], v[136:139]\n"
        "ds_read_b64_tr_b16 v[192:193], v153 offset:32768\n"
        "s_xor_b32 s66, s17, 27\n"
        "s_lshl_b32 s66, s66, 17\n"
        "v_mfma_f32_16x16x32_bf16 v[136:139], a[48:51], a[232:235], v[136:139]\n"
        "ds_read_b64_tr_b16 v[194:195], v153 offset:36864\n"
        "global_load_lds_dwordx4 v144, s[60:61] offset:0\n"
        "v_mfma_f32_16x16x32_bf16 v[136:139], a[44:47], a[236:239], v[136:139]\n"
        "ds_read_b64_tr_b16 v[196:197], v154 offset:32768\n"
        "s_waitcnt lgkmcnt(5)\n"
        "v_mfma_f32_16x16x32_bf16 v[136:139], a[40:43], a[240:243], v[136:139]\n"
        "ds_read_b64_tr_b16 v[198:199], v154 offset:36864\n"
        "v_mfma_f32_16x16x32_bf16 v[136:139], a[36:39], a[244:247], v[136:139]\n"
        "ds_read_b64_tr_b16 v[200:201], v153 offset:40960\n"
        "v_mfma_f32_16x16x32_bf16 v[136:139], a[32:35], a[248:251], v[136:139]\n"
        "ds_read_b64_tr_b16 v[202:203], v153 offset:45056\n"
        "global_load_lds_dwordx4 v145, s[60:61] offset:1024\n"
        "v_mfma_f32_16x16x32_bf16 v[140:143], a[32:35], a[252:255], v[140:143]\n"
        "ds_read_b64_tr_b16 v[204:205], v154 offset:40960\n"
        "v_mfma_f32_16x16x32_bf16 v[140:143], a[36:39], a[248:251], v[140:143]\n"
        "ds_read_b64_tr_b16 v[206:207], v154 offset:45056\n"
        "v_mfma_f32_16x16x32_bf16 v[140:143], a[40:43], a[244:247], v[140:143]\n"
        "ds_read_b64_tr_b16 v[208:209], v153 offset:49152\n"
        "v_mfma_f32_16x16x32_bf16 v[140:143], a[44:47], a[240:243], v[140:143]\n"
        "ds_read_b64_tr_b16 v[210:211], v153 offset:53248\n"
        "global_load_lds_dwordx4 v146, s[60:61] offset:2048\n"
        "v_mfma_f32_16x16x32_bf16 v[140:143], a[48:51], a[236:239], v[140:143]\n"
        "ds_read_b64_tr_b16 v[212:213], v154 offset:49152\n"
        "v_mfma_f32_16x16x32_bf16 v[140:143], a[52:55], a[232:235], v[140:143]\n"
        "ds_read_b64_tr_b16 v[214:215], v154 offset:53248\n"
        "v_mfma_f32_16x16x32_bf16 v[140:143], a[56:59], a[228:231], v[140:143]\n"
        "ds_read2_b32 a[22:23], v150 offset0:194 offset1:195\n"
        "v_mfma_f32_16x16x32_bf16 v[140:143], a[60:63], a[224:227], v[140:143]\n"
        "global_load_lds_dwordx4 v147, s[60:61] offset:3072\n"
        "s_add_u32 m0, s73, 0x8000\n"
        "v_mfma_f32_16x16x32_bf16 v[96:99], a[0:3], a[192:195], v[96:99]\n"
        "ds_read_b128 a[212:215], v152 offset:1280\n"
        "s_add_u32 s60, s22, s66\n"
        "s_addc_u32 s61, s23, 0\n"
        "v_mfma_f32_16x16x32_bf16 v[100:103], a[0:3], a[196:199], v[100:103]\n"
        "ds_read_b64_tr_b16 v[216:217], v153 offset:57344\n"
        "s_xor_b32 s66, s17, 28\n"
        "s_lshl_b32 s66, s66, 17\n"
        "v_mfma_f32_16x16x32_bf16 v[100:103], a[4:7], a[192:195], v[100:103]\n"
        "ds_read_b64_tr_b16 v[218:219], v153 offset:61440\n"
        "v_mfma_f32_16x16x32_bf16 v[104:107], a[8:11], a[192:195], v[104:107]\n"
        "ds_read_b64_tr_b16 v[220:221], v154 offset:57344\n"
        "v_mfma_f32_16x16x32_bf16 v[104:107], a[4:7], a[196:199], v[104:107]\n"
        "ds_read_b64_tr_b16 v[222:223], v154 offset:61440\n"
        "v_mfma_f32_16x16x32_bf16 v[104:107], a[0:3], a[200:203], v[104:107]\n"
        "ds_read2_b32 a[24:25], v151 offset0:64 offset1:65\n"
        "v_mfma_f32_16x16x32_bf16 v[108:111], a[0:3], a[204:207], v[108:111]\n"
        "ds_read2_b32 a[26:27], v151 offset0:66 offset1:67\n"
        "v_mfma_f32_16x16x32_bf16 v[108:111], a[4:7], a[200:203], v[108:111]\n"
        "ds_read_b128 a[216:219], v152 offset:768\n"
        "s_waitcnt lgkmcnt(8)\n"
        "v_mfma_f32_16x16x32_bf16 v[108:111], a[8:11], a[196:199], v[108:111]\n"
        "ds_read2_b32 a[28:29], v151 offset0:192 offset1:193\n"
        "s_waitcnt lgkmcnt(8)\n"
        "v_mfma_f32_16x16x32_bf16 v[108:111], a[12:15], a[192:195], v[108:111]\n"
        "ds_read2_b32 a[30:31], v151 offset0:194 offset1:195\n"
        "v_mfma_f32_16x16x32_bf16 v[128:131], a[16:19], a[192:195], v[128:131]\n"
        "ds_read_b128 a[220:223], v152 offset:256\n"
        "v_mfma_f32_16x16x32_bf16 v[128:131], a[12:15], a[196:199], v[128:131]\n"
        "ds_read2_b32 a[32:33], v148 offset0:80 offset1:81\n"
        "v_mfma_f32_16x16x32_bf16 v[128:131], a[8:11], a[200:203], v[128:131]\n"
        "ds_read2_b32 a[34:35], v148 offset0:82 offset1:83\n"
        "v_mfma_f32_16x16x32_bf16 v[128:131], a[4:7], a[204:207], v[128:131]\n"
        "ds_read_b128 a[224:227], v231 offset:0\n"
        "v_add_u32 v231, 4032, v152\n"
        "v_mfma_f32_16x16x32_bf16 v[128:131], a[0:3], a[208:211], v[128:131]\n"
        "ds_read2_b32 a[36:37], v148 offset0:208 offset1:209\n"
        "v_min_u32 v231, s16, v231\n"
        "v_mfma_f32_16x16x32_bf16 v[132:135], a[0:3], a[212:215], v[132:135]\n"
        "ds_read2_b32 a[38:39], v148 offset0:210 offset1:211\n"
        "s_lshl_b32 s16, s63, 12\n"
        "s_lshl1_add_u32 s16, s62, s16\n"
        "v_mfma_f32_16x16x32_bf16 v[132:135], a[4:7], a[208:211], v[132:135]\n"
        "ds_read_b128 a[228:231], v152 offset:3392\n"
        "s_add_u32 s16, s16, 0x3400000\n"
        "s_waitcnt lgkmcnt(8)\n"
        "v_mfma_f32_16x16x32_bf16 v[132:135], a[8:11], a[204:207], v[132:135]\n"
        "s_waitcnt vmcnt(8)\n"
        "ds_read2_b32 a[40:41], v149 offset0:80 offset1:81\n"
        "v_mfma_f32_16x16x32_bf16 v[132:135], a[12:15], a[200:203], v[132:135]\n"
        "s_barrier\n"
        "v_mfma_f32_16x16x32_bf16 v[132:135], a[16:19], a[196:199], v[132:135]\n"
        "global_load_lds_dwordx4 v144, s[60:61] offset:0\n"
        "ds_read_b64_tr_b16 v[112:113], v155 offset:0\n"
        "v_mfma_f32_16x16x32_bf16 v[132:135], a[20:23], a[192:195], v[132:135]\n"
        "ds_read_b64_tr_b16 v[114:115], v155 offset:4096\n"
        "s_add_u32 s58, s38, s16\n"
        "s_addc_u32 s59, s39, 0\n"
        "v_mfma_f32_16x16x32_bf16 v[136:139], a[24:27], a[192:195], v[136:139]\n"
        "ds_read_b64_tr_b16 v[116:117], v156 offset:0\n"
        "s_lshl_b32 s16, s13, 14\n"
        "s_add_u32 s16, s16, s73\n"
        "v_mfma_f32_16x16x32_bf16 v[136:139], a[20:23], a[196:199], v[136:139]\n"
        "ds_read_b64_tr_b16 v[118:119], v156 offset:4096\n"
        "s_add_u32 s16, s16, 0x4000000\n"
        "s_waitcnt lgkmcnt(7)\n"
        "v_mfma_f32_16x16x32_bf16 v[136:139], a[16:19], a[200:203], v[136:139]\n"
        "global_load_lds_dwordx4 v145, s[60:61] offset:1024\n"
        "ds_read_b64_tr_b16 v[120:121], v155 offset:8192\n"
        "v_mfma_f32_16x16x32_bf16 v[136:139], a[12:15], a[204:207], v[136:139]\n"
        "ds_read_b64_tr_b16 v[122:123], v155 offset:12288\n"
        "s_add_u32 s28, s38, s16\n"
        "s_addc_u32 s29, s39, 0\n"
        "v_mfma_f32_16x16x32_bf16 v[136:139], a[8:11], a[208:211], v[136:139]\n"
        "ds_read2_b32 a[42:43], v149 offset0:82 offset1:83\n"
        "s_xor_b32 s16, s16, 0x4000\n"
        "s_waitcnt lgkmcnt(7)\n"
        "v_mfma_f32_16x16x32_bf16 v[136:139], a[4:7], a[212:215], v[136:139]\n"
        "ds_read_b128 a[232:235], v152 offset:2880\n"
        "s_add_u32 s30, s38, s16\n"
        "s_addc_u32 s31, s39, 0\n"
        "v_mfma_f32_16x16x32_bf16 v[136:139], a[0:3], a[216:219], v[136:139]\n"
        "global_load_lds_dwordx4 v146, s[60:61] offset:2048\n"
        "ds_read_b64_tr_b16 v[124:125], v156 offset:8192\n"
        "s_add_u32 s16, s73, 0x8000\n"
        "v_mfma_f32_16x16x32_bf16 v[140:143], a[0:3], a[220:223], v[140:143]\n"
        "ds_read_b64_tr_b16 v[126:127], v156 offset:12288\n"
        "v_mfma_f32_16x16x32_bf16 v[140:143], a[4:7], a[216:219], v[140:143]\n"
        "ds_read2_b32 a[44:45], v149 offset0:208 offset1:209\n"
        "v_mfma_f32_16x16x32_bf16 v[140:143], a[8:11], a[212:215], v[140:143]\n"
        "LHC3_%=:\n"
        "s_cbranch_execz LHRC_%=\n"
        "ds_read2_b32 a[46:47], v149 offset0:210 offset1:211\n"
        "v_mfma_f32_16x16x32_bf16 v[140:143], a[12:15], a[208:211], v[140:143]\n"
        "global_load_lds_dwordx4 v147, s[60:61] offset:3072\n"
        "ds_read_b128 a[236:239], v152 offset:2368\n"
        "s_add_u32 m0, s73, 0xc000\n"
        "v_mfma_f32_16x16x32_bf16 v[140:143], a[16:19], a[204:207], v[140:143]\n"
        "s_add_u32 s60, s22, s66\n"
        "s_addc_u32 s61, s23, 0\n"
        "s_xor_b32 s66, s17, 29\n"
        "v_mfma_f32_16x16x32_bf16 v[140:143], a[20:23], a[200:203], v[140:143]\n"
        "ds_read_b64_tr_b16 a[64:65], v155 offset:16384\n"
        "s_lshl_b32 s66, s66, 17\n"
        "s_waitcnt lgkmcnt(7)\n"
        "v_mfma_f32_16x16x32_bf16 v[140:143], a[24:27], a[196:199], v[140:143]\n"
        "ds_read_b64_tr_b16 a[66:67], v155 offset:20480\n"
        "v_mfma_f32_16x16x32_bf16 v[140:143], a[28:31], a[192:195], v[140:143]\n"
        "global_load_lds_dwordx4 v144, s[60:61] offset:0\n"
        "ds_read2_b32 a[48:49], v150 offset0:80 offset1:81\n"
        "v_mfma_f32_16x16x32_bf16 v[96:99], a[32:35], a[224:227], v[96:99]\n"
        "ds_read2_b32 a[50:51], v150 offset0:82 offset1:83\n"
        "v_mfma_f32_16x16x32_bf16 v[100:103], a[32:35], a[228:231], v[100:103]\n"
        "ds_read_b128 a[240:243], v152 offset:1856\n"
        "v_mfma_f32_16x16x32_bf16 v[100:103], a[36:39], a[224:227], v[100:103]\n"
        "ds_read_b64_tr_b16 a[68:69], v156 offset:16384\n"
        "s_waitcnt lgkmcnt(7)\n"
        "v_mfma_f32_16x16x32_bf16 v[104:107], a[40:43], a[224:227], v[104:107]\n"
        "global_load_lds_dwordx4 v145, s[60:61] offset:1024\n"
        "ds_read_b64_tr_b16 a[70:71], v156 offset:20480\n"
        "v_mfma_f32_16x16x32_bf16 v[104:107], a[36:39], a[228:231], v[104:107]\n"
        "ds_read2_b32 a[52:53], v150 offset0:208 offset1:209\n"
        "s_waitcnt lgkmcnt(8)\n"
        "v_mfma_f32_16x16x32_bf16 v[104:107], a[32:35], a[232:235], v[104:107]\n"
        "ds_read2_b32 a[54:55], v150 offset0:210 offset1:211\n"
        "v_mfma_f32_16x16x32_bf16 v[108:111], a[32:35], a[236:239], v[108:111]\n"
        "ds_read_b128 a[244:247], v152 offset:1344\n"
        "v_mfma_f32_16x16x32_bf16 v[108:111], a[36:39], a[232:235], v[108:111]\n"
        "global_load_lds_dwordx4 v146, s[60:61] offset:2048\n"
        "ds_read_b64_tr_b16 a[72:73], v155 offset:24576\n"
        "v_mfma_f32_16x16x32_bf16 v[108:111], a[40:43], a[228:231], v[108:111]\n"
        "ds_read_b64_tr_b16 a[74:75], v155 offset:28672\n"
        "s_waitcnt lgkmcnt(8)\n"
        "v_mfma_f32_16x16x32_bf16 v[108:111], a[44:47], a[224:227], v[108:111]\n"
        "ds_read_b64_tr_b16 a[76:77], v156 offset:24576\n"
        "v_mfma_f32_16x16x32_bf16 v[128:131], a[48:51], a[224:227], v[128:131]\n"
        "ds_read_b64_tr_b16 a[78:79], v156 offset:28672\n"
        "v_mfma_f32_16x16x32_bf16 v[128:131], a[44:47], a[228:231], v[128:131]\n"
        "global_load_lds_dwordx4 v147, s[60:61] offset:3072\n"
        "ds_read2_b32 a[56:57], v151 offset0:80 offset1:81\n"
        "s_add_u32 m0, s73, 0x10000\n"
        "v_mfma_f32_16x16x32_bf16 v[128:131], a[40:43], a[232:235], v[128:131]\n"
        "ds_read2_b32 a[58:59], v151 offset0:82 offset1:83\n"
        "s_add_u32 s60, s22, s66\n"
        "s_addc_u32 s61, s23, 0\n"
        "v_mfma_f32_16x16x32_bf16 v[128:131], a[36:39], a[236:239], v[128:131]\n"
        "ds_read_b128 a[248:251], v152 offset:832\n"
        "s_xor_b32 s66, s17, 30\n"
        "s_waitcnt lgkmcnt(8)\n"
        "v_mfma_f32_16x16x32_bf16 v[128:131], a[32:35], a[240:243], v[128:131]\n"
        "ds_read2_b32 a[60:61], v151 offset0:208 offset1:209\n"
        "s_lshl_b32 s66, s66, 17\n"
        "s_waitcnt lgkmcnt(8)\n"
        "v_mfma_f32_16x16x32_bf16 v[132:135], a[32:35], a[244:247], v[132:135]\n"
        "ds_read2_b32 a[62:63], v151 offset0:210 offset1:211\n"
        "v_mfma_f32_16x16x32_bf16 v[132:135], a[36:39], a[240:243], v[132:135]\n"
        "ds_read_b128 a[252:255], v152 offset:320\n"
        "v_mfma_f32_16x16x32_bf16 v[132:135], a[40:43], a[236:239], v[132:135]\n"
        "ds_read2_b32 a[0:1], v148 offset0:96 offset1:97\n"
        "v_mfma_f32_16x16x32_bf16 v[132:135], a[44:47], a[232:235], v[132:135]\n"
        "s_waitcnt vmcnt(8) lgkmcnt(7)\n"
        "ds_read2_b32 a[2:3], v148 offset0:98 offset1:99\n"
        "v_mfma_f32_16x16x32_bf16 v[132:135], a[48:51], a[228:231], v[132:135]\n"
        "s_barrier\n"
        "v_mfma_f32_16x16x32_bf16 v[132:135], a[52:55], a[224:227], v[132:135]\n"
        "global_load_lds_dwordx4 v144, s[60:61] offset:0\n"
        "s_waitcnt lgkmcnt(6)\n"
        "v_mfma_f32_16x16x32_bf16 v[136:139], a[56:59], a[224:227], v[136:139]\n"
        "ds_read_b64_tr_b16 a[80:81], v153 offset:0\n"
        "v_mfma_f32_16x16x32_bf16 v[136:139], a[52:55], a[228:231], v[136:139]\n"
        "ds_read_b64_tr_b16 a[82:83], v153 offset:4096\n"
        "v_mfma_f32_16x16x32_bf16 v[136:139], a[48:51], a[232:235], v[136:139]\n"
        "ds_read_b128 a[192:195], v230 offset:0\n"
        "v_lshrrev_b32 v230, 1, v233\n"
        "v_mfma_f32_16x16x32_bf16 v[136:139], a[44:47], a[236:239], v[136:139]\n"
        "global_load_lds_dwordx4 v145, s[60:61] offset:1024\n"
        "s_waitcnt lgkmcnt(5)\n"
        "v_mfma_f32_16x16x32_bf16 v[140:143], a[32:35], a[252:255], v[140:143]\n"
        "ds_read_b64_tr_b16 a[84:85], v154 offset:0\n"
        "v_mfma_f32_16x16x32_bf16 v[140:143], a[36:39], a[248:251], v[140:143]\n"
        "ds_read_b64_tr_b16 a[86:87], v154 offset:4096\n"
        "v_mfma_f32_16x16x32_bf16 v[140:143], a[40:43], a[244:247], v[140:143]\n"
        "ds_read2_b32 a[4:5], v148 offset0:224 offset1:225\n"
        "v_mfma_f32_16x16x32_bf16 v[140:143], a[44:47], a[240:243], v[140:143]\n"
        "global_load_lds_dwordx4 v146, s[60:61] offset:2048\n"
        "ds_read2_b32 a[6:7], v148 offset0:226 offset1:227\n"
        "v_mfma_f32_16x16x32_bf16 v[140:143], a[48:51], a[236:239], v[140:143]\n"
        "ds_read_b128 a[196:199], v152 offset:3456\n"
        "v_mfma_f32_16x16x32_bf16 v[140:143], a[52:55], a[232:235], v[140:143]\n"
        "ds_read_b64_tr_b16 a[88:89], v153 offset:8192\n"
        "s_waitcnt lgkmcnt(7)\n"
        "v_mfma_f32_16x16x32_bf16 v[140:143], a[56:59], a[228:231], v[140:143]\n"
        "ds_read_b64_tr_b16 a[90:91], v153 offset:12288\n"
        "s_waitcnt lgkmcnt(7)\n"
        "v_mfma_f32_16x16x32_bf16 v[140:143], a[60:63], a[224:227], v[140:143]\n"
        "global_load_dwordx2 v[236:237], v159, s[48:49] sc1\n"
        "ds_read2_b32 a[8:9], v149 offset0:96 offset1:97\n"
        "v_mfma_f32_16x16x32_bf16 v[136:139], a[40:43], a[240:243], v[136:139]\n"
        "global_load_lds_dwordx4 v147, s[60:61] offset:3072\n"
        "s_add_u32 m0, s73, 0x14000\n"
        "v_mfma_f32_16x16x32_bf16 v[136:139], a[36:39], a[244:247], v[136:139]\n"
        "s_add_u32 s60, s22, s66\n"
        "s_addc_u32 s61, s23, 0\n"
        "s_xor_b32 s66, s17, 31\n"
        "v_mfma_f32_16x16x32_bf16 v[136:139], a[32:35], a[248:251], v[136:139]\n"
        "ds_read2_b32 a[10:11], v149 offset0:98 offset1:99\n"
        "s_lshl_b32 s66, s66, 17\n"
        "v_mfma_f32_16x16x32_bf16 v[96:99], a[0:3], a[192:195], v[96:99]\n"
        "ds_read_b128 a[200:203], v152 offset:2944\n"
        "s_waitcnt lgkmcnt(5)\n"
        "v_mfma_f32_16x16x32_bf16 v[100:103], a[0:3], a[196:199], v[100:103]\n"
        "global_load_lds_dwordx4 v144, s[60:61] offset:0\n"
        "ds_read_b64_tr_b16 a[92:93], v154 offset:8192\n"
        "v_mfma_f32_16x16x32_bf16 v[100:103], a[4:7], a[192:195], v[100:103]\n"
        "global_load_lds_dwordx4 v145, s[60:61] offset:1024\n"
        "ds_read_b64_tr_b16 a[94:95], v154 offset:12288\n"
        "ds_read2_b32 a[12:13], v149 offset0:224 offset1:225\n"
        "s_waitcnt lgkmcnt(4)\n"
        "v_mfma_f32_16x16x32_bf16 v[104:107], a[8:11], a[192:195], v[104:107]\n"
        "ds_read2_b32 a[14:15], v149 offset0:226 offset1:227\n"
        "v_mfma_f32_16x16x32_bf16 v[104:107], a[4:7], a[196:199], v[104:107]\n"
        "ds_read_b128 a[204:207], v152 offset:2432\n"
        "s_waitcnt lgkmcnt(5)\n"
        "v_mfma_f32_16x16x32_bf16 v[104:107], a[0:3], a[200:203], v[104:107]\n"
        "global_load_lds_dwordx4 v146, s[60:61] offset:2048\n"
        "global_load_lds_dwordx4 v147, s[60:61] offset:3072\n"
        "ds_read_b64_tr_b16 a[96:97], v153 offset:16384\n"
        "ds_read_b64_tr_b16 a[98:99], v153 offset:20480\n"
        "ds_read2_b32 a[16:17], v150 offset0:96 offset1:97\n"
        "ds_read2_b32 a[18:19], v150 offset0:98 offset1:99\n"
        "ds_read_b128 a[208:211], v152 offset:1920\n"
        "s_add_u32 m0, s73, 0x0\n"
        "s_waitcnt lgkmcnt(5)\n"
        "v_mfma_f32_16x16x32_bf16 v[108:111], a[0:3], a[204:207], v[108:111]\n"
        "ds_read_b64_tr_b16 a[100:101], v154 offset:16384\n"
        "s_add_u32 s60, s22, s66\n"
        "s_addc_u32 s61, s23, 0\n"
        "v_mfma_f32_16x16x32_bf16 v[108:111], a[4:7], a[200:203], v[108:111]\n"
        "ds_read_b64_tr_b16 a[102:103], v154 offset:20480\n"
        "s_xor_b32 s66, s17, 0\n"
        "s_lshl_b32 s66, s66, 14\n"
        "v_mfma_f32_16x16x32_bf16 v[108:111], a[8:11], a[196:199], v[108:111]\n"
        "ds_read2_b32 a[20:21], v150 offset0:224 offset1:225\n"
        "v_mfma_f32_16x16x32_bf16 v[108:111], a[12:15], a[192:195], v[108:111]\n"
        "ds_read2_b32 a[22:23], v150 offset0:226 offset1:227\n"
        "ds_read_b128 a[212:215], v152 offset:1408\n"
        "ds_read_b64_tr_b16 a[104:105], v153 offset:24576\n"
        "ds_read_b64_tr_b16 a[106:107], v153 offset:28672\n"
        "ds_read2_b32 a[24:25], v151 offset0:96 offset1:97\n"
        "ds_read2_b32 a[26:27], v151 offset0:98 offset1:99\n"
        "s_waitcnt lgkmcnt(10)\n"
        "v_mfma_f32_16x16x32_bf16 v[128:131], a[16:19], a[192:195], v[128:131]\n"
        "ds_read_b128 a[216:219], v152 offset:896\n"
        "v_mfma_f32_16x16x32_bf16 v[128:131], a[12:15], a[196:199], v[128:131]\n"
        "ds_read_b64_tr_b16 a[108:109], v154 offset:24576\n"
        "v_mfma_f32_16x16x32_bf16 v[128:131], a[8:11], a[200:203], v[128:131]\n"
        "ds_read_b64_tr_b16 a[110:111], v154 offset:28672\n"
        "v_mfma_f32_16x16x32_bf16 v[128:131], a[4:7], a[204:207], v[128:131]\n"
        "ds_read_b128 a[220:223], v152 offset:384\n"
        "s_waitcnt lgkmcnt(10)\n"
        "v_mfma_f32_16x16x32_bf16 v[128:131], a[0:3], a[208:211], v[128:131]\n"
        "ds_read2_b32 a[28:29], v151 offset0:224 offset1:225\n"
        "s_waitcnt lgkmcnt(9)\n"
        "v_mfma_f32_16x16x32_bf16 v[132:135], a[0:3], a[212:215], v[132:135]\n"
        "ds_read2_b32 a[30:31], v151 offset0:226 offset1:227\n"
        "s_waitcnt lgkmcnt(9)\n"
        "v_mfma_f32_16x16x32_bf16 v[132:135], a[4:7], a[208:211], v[132:135]\n"
        "s_mov_b64 exec, 0\n"
        "s_cmp_eq_u32 s40, 0\n"
        "s_cbranch_scc1 LHD0_%=\n"
        "s_cmp_eq_u32 s40, 1\n"
        "s_cbranch_scc1 LHRD_%=\n"
        "s_cmp_eq_u32 s40, 2\n"
        "s_cbranch_scc1 LHRD_%=\n"
        "s_branch LHRD_%=\n"
        "LHRD_%=:\n"
        "s_mov_b64 exec, -1\n"
        "v_mfma_f32_16x16x32_bf16 v[132:135], a[8:11], a[204:207], v[132:135]\n"
        "ds_read2_b32 a[32:33], v148 offset0:112 offset1:113\n"
        "s_waitcnt lgkmcnt(7)\n"
        "v_mfma_f32_16x16x32_bf16 v[132:135], a[12:15], a[200:203], v[132:135]\n"
        "ds_read2_b32 a[34:35], v148 offset0:114 offset1:115\n"
        "v_mfma_f32_16x16x32_bf16 v[132:135], a[16:19], a[196:199], v[132:135]\n"
        "s_waitcnt vmcnt(9) lgkmcnt(5)\n"
        "v_mfma_f32_16x16x32_bf16 v[132:135], a[20:23], a[192:195], v[132:135]\n"
        "ds_read_b128 a[224:227], v231 offset:0\n"
        "s_waitcnt lgkmcnt(5)\n"
        "v_mfma_f32_16x16x32_bf16 v[140:143], a[0:3], a[220:223], v[140:143]\n"
        "s_barrier\n"
        "v_mfma_f32_16x16x32_bf16 v[140:143], a[4:7], a[216:219], v[140:143]\n"
        "global_load_lds_dwordx4 v144, s[60:61] offset:0\n"
        "ds_read_b64_tr_b16 a[112:113], v153 offset:32768\n"
        "v_mfma_f32_16x16x32_bf16 v[140:143], a[8:11], a[212:215], v[140:143]\n"
        "ds_read_b64_tr_b16 a[114:115], v153 offset:36864\n"
        "v_mfma_f32_16x16x32_bf16 v[140:143], a[12:15], a[208:211], v[140:143]\n"
        "ds_read_b64_tr_b16 a[116:117], v154 offset:32768\n"
        "v_mfma_f32_16x16x32_bf16 v[140:143], a[16:19], a[204:207], v[140:143]\n"
        "ds_read_b64_tr_b16 a[118:119], v154 offset:36864\n"
        "v_mfma_f32_16x16x32_bf16 v[140:143], a[20:23], a[200:203], v[140:143]\n"
        "global_load_lds_dwordx4 v145, s[60:61] offset:1024\n"
        "ds_read2_b32 a[36:37], v148 offset0:240 offset1:241\n"
        "v_mfma_f32_16x16x32_bf16 v[140:143], a[24:27], a[196:199], v[140:143]\n"
        "ds_read2_b32 a[38:39], v148 offset0:242 offset1:243\n"
        "s_waitcnt lgkmcnt(7)\n"
        "v_mfma_f32_16x16x32_bf16 v[140:143], a[28:31], a[192:195], v[140:143]\n"
        "ds_read_b128 a[228:231], v152 offset:3520\n"
        "v_lshrrev_b32 v148, 7, v254\n"
        "s_waitcnt vmcnt(7) lgkmcnt(7)\n"
        "v_mfma_f32_16x16x32_bf16 v[136:139], a[24:27], a[192:195], v[136:139]\n"
        "ds_read_b64_tr_b16 a[120:121], v153 offset:40960\n"
        "v_cmp_ne_u64 vcc, v[236:237], v[250:251]\n"
        "v_mfma_f32_16x16x32_bf16 v[136:139], a[20:23], a[196:199], v[136:139]\n"
        "global_load_lds_dwordx4 v146, s[60:61] offset:2048\n"
        "s_cbranch_vccnz LPCX_%=\n"
        "LBCX_%=:\n"
        "v_lshlrev_b32 v148, 12, v148\n"
        "v_mfma_f32_16x16x32_bf16 v[136:139], a[16:19], a[200:203], v[136:139]\n"
        "ds_read_b64_tr_b16 a[122:123], v153 offset:45056\n"
        "v_mfma_f32_16x16x32_bf16 v[136:139], a[12:15], a[204:207], v[136:139]\n"
        "ds_read2_b32 a[40:41], v149 offset0:112 offset1:113\n"
        "v_mfma_f32_16x16x32_bf16 v[136:139], a[8:11], a[208:211], v[136:139]\n"
        "ds_read2_b32 a[42:43], v149 offset0:114 offset1:115\n"
        "v_mfma_f32_16x16x32_bf16 v[136:139], a[4:7], a[212:215], v[136:139]\n"
        "global_load_lds_dwordx4 v147, s[60:61] offset:3072\n"
        "s_add_u32 m0, s73, 0x4000\n"
        "s_waitcnt lgkmcnt(6)\n"
        "v_mfma_f32_16x16x32_bf16 v[136:139], a[0:3], a[216:219], v[136:139]\n"
        "s_add_u32 s60, s20, s66\n"
        "s_addc_u32 s61, s21, 0\n"
        "s_xor_b32 s66, s17, 1\n"
        "v_mfma_f32_16x16x32_bf16 v[96:99], a[32:35], a[224:227], v[96:99]\n"
        "ds_read_b128 a[232:235], v152 offset:3008\n"
        "s_lshl_b32 s66, s66, 17\n"
        "s_waitcnt lgkmcnt(5)\n"
        "v_mfma_f32_16x16x32_bf16 v[100:103], a[32:35], a[228:231], v[100:103]\n"
        "global_load_lds_dwordx4 v254, s[60:61] offset:0 sc1\n"
        "ds_read_b64_tr_b16 a[124:125], v154 offset:40960\n"
        "v_mfma_f32_16x16x32_bf16 v[100:103], a[36:39], a[224:227], v[100:103]\n"
        "global_load_lds_dwordx4 v254, s[60:61] offset:1024 sc1\n"
        "ds_read_b64_tr_b16 a[126:127], v154 offset:45056\n"
        "s_waitcnt lgkmcnt(3)\n"
        "v_mfma_f32_16x16x32_bf16 v[104:107], a[40:43], a[224:227], v[104:107]\n"
        "ds_read_b64_tr_b16 a[128:129], v153 offset:49152\n"
        "v_cvt_pk_bf16_f32 v96, 0, v96\n"
        "v_mfma_f32_16x16x32_bf16 v[104:107], a[36:39], a[228:231], v[104:107]\n"
        "global_load_lds_dwordx4 v254, s[60:61] offset:2048 sc1\n"
        "ds_read_b64_tr_b16 a[130:131], v153 offset:53248\n"
        "s_waitcnt lgkmcnt(4)\n"
        "v_mfma_f32_16x16x32_bf16 v[104:107], a[32:35], a[232:235], v[104:107]\n"
        "ds_read_b64_tr_b16 a[132:133], v154 offset:49152\n"
        "ds_read_b64_tr_b16 a[134:135], v154 offset:53248\n"
        "ds_read_b64_tr_b16 a[136:137], v153 offset:57344\n"
        "ds_read_b64_tr_b16 a[138:139], v153 offset:61440\n"
        "global_load_lds_dwordx4 v254, s[60:61] offset:3072 sc1\n"
        "ds_read_b64_tr_b16 a[140:141], v154 offset:57344\n"
        "ds_read_b64_tr_b16 a[142:143], v154 offset:61440\n"
        "ds_read2_b32 a[44:45], v149 offset0:240 offset1:241\n"
        "ds_read2_b32 a[46:47], v149 offset0:242 offset1:243\n"
        "ds_read_b128 a[236:239], v152 offset:2496\n"
        "ds_read2_b32 a[48:49], v150 offset0:112 offset1:113\n"
        "ds_read2_b32 a[50:51], v150 offset0:114 offset1:115\n"
        "ds_read_b128 a[240:243], v152 offset:1984\n"
        "s_add_u32 s60, s22, s66\n"
        "s_addc_u32 s61, s23, 0\n"
        "s_waitcnt vmcnt(8) lgkmcnt(6)\n"
        "s_add_u32 m0, s73, 0x8000\n"
        "s_xor_b32 s66, s17, 1\n"
        "s_lshl_b32 s66, s66, 14\n"
        "v_cvt_pk_bf16_f32 v97, 0, v97\n"
        "s_waitcnt lgkmcnt(3)\n"
        "v_mfma_f32_16x16x32_bf16 v[108:111], a[32:35], a[236:239], v[108:111]\n"
        "s_barrier\n"
        "v_mfma_f32_16x16x32_bf16 v[108:111], a[36:39], a[232:235], v[108:111]\n"
        "global_load_lds_dwordx4 v144, s[60:61] offset:0\n"
        "ds_read2_b32 a[52:53], v150 offset0:240 offset1:241\n"
        "v_mfma_f32_16x16x32_bf16 v[108:111], a[40:43], a[228:231], v[108:111]\n"
        "ds_read2_b32 a[54:55], v150 offset0:242 offset1:243\n"
        "v_cvt_pk_bf16_f32 v98, 0, v98\n"
        "v_mfma_f32_16x16x32_bf16 v[108:111], a[44:47], a[224:227], v[108:111]\n"
        "ds_read_b128 a[244:247], v152 offset:1472\n"
        "s_waitcnt lgkmcnt(3)\n"
        "v_mfma_f32_16x16x32_bf16 v[128:131], a[48:51], a[224:227], v[128:131]\n"
        "global_load_lds_dwordx4 v145, s[60:61] offset:1024\n"
        "ds_read2_b32 a[56:57], v151 offset0:112 offset1:113\n"
        "v_mfma_f32_16x16x32_bf16 v[128:131], a[44:47], a[228:231], v[128:131]\n"
        "ds_read2_b32 a[58:59], v151 offset0:114 offset1:115\n"
        "v_cvt_pk_bf16_f32 v99, 0, v99\n"
        "v_mfma_f32_16x16x32_bf16 v[128:131], a[40:43], a[232:235], v[128:131]\n"
        "ds_read_b128 a[248:251], v152 offset:960\n"
        "v_cvt_pk_bf16_f32 v100, 0, v100\n"
        "v_mfma_f32_16x16x32_bf16 v[128:131], a[36:39], a[236:239], v[128:131]\n"
        "global_load_lds_dwordx4 v146, s[60:61] offset:2048\n"
        "ds_read_b64 v[64:65], v229 offset:3584\n"
        "v_mfma_f32_16x16x32_bf16 v[128:131], a[32:35], a[240:243], v[128:131]\n"
        "global_load_lds_dwordx4 v147, s[60:61] offset:3072\n"
        "s_add_u32 m0, s73, 0xc000\n"
        "v_cvt_pk_bf16_f32 v101, 0, v101\n"
        "s_waitcnt lgkmcnt(4)\n"
        "v_mfma_f32_16x16x32_bf16 v[132:135], a[32:35], a[244:247], v[132:135]\n"
        "s_add_u32 s60, s20, s66\n"
        "s_addc_u32 s61, s21, 0\n"
        "s_xor_b32 s66, s17, 2\n"
        "v_mfma_f32_16x16x32_bf16 v[132:135], a[36:39], a[240:243], v[132:135]\n"
        "ds_read_b64 v[66:67], v229 offset:3072\n"
        "s_lshl_b32 s66, s66, 17\n"
        "v_cvt_pk_bf16_f32 v102, 0, v102\n"
        "v_mfma_f32_16x16x32_bf16 v[132:135], a[40:43], a[236:239], v[132:135]\n"
        "global_load_lds_dwordx4 v254, s[60:61] offset:0 sc1\n"
        "ds_read_b64 v[68:69], v229 offset:2560\n"
        "v_mfma_f32_16x16x32_bf16 v[132:135], a[44:47], a[232:235], v[132:135]\n"
        "ds_read_b64 v[70:71], v229 offset:2048\n"
        "v_cvt_pk_bf16_f32 v103, 0, v103\n"
        "v_mfma_f32_16x16x32_bf16 v[132:135], a[48:51], a[228:231], v[132:135]\n"
        "ds_read_b64 v[72:73], v229 offset:1536\n"
        "v_cvt_pk_bf16_f32 v104, 0, v104\n"
        "v_mfma_f32_16x16x32_bf16 v[132:135], a[52:55], a[224:227], v[132:135]\n"
        "global_load_lds_dwordx4 v254, s[60:61] offset:1024 sc1\n"
        "s_waitcnt lgkmcnt(4)\n"
        "v_mfma_f32_16x16x32_bf16 v[136:139], a[56:59], a[224:227], v[136:139]\n"
        "ds_read_b64 v[74:75], v229 offset:1024\n"
        "v_and_b32 v80, 0xffff0000, v65\n"
        "v_mfma_f32_16x16x32_bf16 v[136:139], a[52:55], a[228:231], v[136:139]\n"
        "ds_read_b64 v[76:77], v229 offset:512\n"
        "v_lshlrev_b32 v81, 16, v65\n"
        "v_mfma_f32_16x16x32_bf16 v[136:139], a[48:51], a[232:235], v[136:139]\n"
        "global_load_lds_dwordx4 v254, s[60:61] offset:2048 sc1\n"
        "ds_read_b64 v[78:79], v229 offset:0\n"
        "v_mfma_f32_16x16x32_bf16 v[136:139], a[44:47], a[236:239], v[136:139]\n"
        "ds_read_b128 a[252:255], v152 offset:448\n"
        "v_and_b32 v229, 1, v233\n"
        "v_mfma_f32_16x16x32_bf16 v[136:139], a[40:43], a[240:243], v[136:139]\n"
        "ds_read2_b32 a[60:61], v151 offset0:240 offset1:241\n"
        "v_lshlrev_b32 v229, 9, v229\n"
        "v_mfma_f32_16x16x32_bf16 v[136:139], a[36:39], a[244:247], v[136:139]\n"
        "global_load_lds_dwordx4 v254, s[60:61] offset:3072 sc1\n"
        "ds_read2_b32 a[62:63], v151 offset0:242 offset1:243\n"
        "s_add_u32 m0, s73, 0x18000\n"
        "v_mfma_f32_16x16x32_bf16 v[136:139], a[32:35], a[248:251], v[136:139]\n"
        "ds_read_b64_tr_b16 a[144:145], v155 offset:0\n"
        "ds_read_b64_tr_b16 a[146:147], v155 offset:4096\n"
        "ds_read_b64_tr_b16 a[148:149], v156 offset:0\n"
        "ds_read_b64_tr_b16 a[150:151], v156 offset:4096\n"
        "ds_read_b64_tr_b16 a[152:153], v155 offset:8192\n"
        "ds_read_b64_tr_b16 a[154:155], v155 offset:12288\n"
        "ds_read_b64_tr_b16 a[156:157], v156 offset:8192\n"
        "ds_read_b64_tr_b16 a[158:159], v156 offset:12288\n"
        "s_add_u32 s60, s22, s66\n"
        "s_addc_u32 s61, s23, 0\n"
        "s_xor_b32 s66, s17, 2\n"
        "s_lshl_b32 s66, s66, 14\n"
        "s_waitcnt lgkmcnt(10)\n"
        "v_mfma_f32_16x16x32_bf16 v[140:143], a[32:35], a[252:255], v[140:143]\n"
        "ds_read_b64_tr_b16 a[160:161], v155 offset:16384\n"
        "v_lshl_add_u32 v229, v232, 5, v229\n"
        "v_mfma_f32_16x16x32_bf16 v[140:143], a[36:39], a[248:251], v[140:143]\n"
        "ds_read_b64_tr_b16 a[162:163], v155 offset:20480\n"
        "v_lshl_add_u32 v229, v230, 4, v229\n"
        "v_mfma_f32_16x16x32_bf16 v[140:143], a[40:43], a[244:247], v[140:143]\n"
        "s_waitcnt lgkmcnt(10)\n"
        "ds_read_b64_tr_b16 a[164:165], v156 offset:16384\n"
        "v_mfma_f32_16x16x32_bf16 v[140:143], a[44:47], a[240:243], v[140:143]\n"
        "s_barrier\n"
        "v_mfma_f32_16x16x32_bf16 v[140:143], a[48:51], a[236:239], v[140:143]\n"
        "global_load_lds_dwordx4 v144, s[60:61] offset:0\n"
        "ds_read_b64_tr_b16 a[166:167], v156 offset:20480\n"
        "v_mfma_f32_16x16x32_bf16 v[140:143], a[52:55], a[232:235], v[140:143]\n"
        "ds_read_b64_tr_b16 a[168:169], v155 offset:24576\n"
        "v_and_b32 v82, 0xffff0000, v64\n"
        "v_mfma_f32_16x16x32_bf16 v[140:143], a[56:59], a[228:231], v[140:143]\n"
        "ds_read_b64_tr_b16 a[170:171], v155 offset:28672\n"
        "v_lshlrev_b32 v83, 16, v64\n"
        "v_mfma_f32_16x16x32_bf16 v[140:143], a[60:63], a[224:227], v[140:143]\n"
        "global_load_lds_dwordx4 v145, s[60:61] offset:1024\n"
        "ds_read_b64_tr_b16 a[172:173], v156 offset:24576\n"
        "ds_read_b64_tr_b16 a[174:175], v156 offset:28672\n"
        "v_mul_f32 v80, s54, v80\n"
        "v_mul_f32 v81, s54, v81\n"
        "v_mul_f32 v82, s54, v82\n"
        "v_mul_f32 v83, s54, v83\n"
        "v_cvt_pk_bf16_f32 v80, 0, v80\n"
        "v_cvt_pk_bf16_f32 v81, 0, v81\n"
        "global_load_lds_dwordx4 v146, s[60:61] offset:2048\n"
        "v_cvt_pk_bf16_f32 v82, 0, v82\n"
        "v_cvt_pk_bf16_f32 v83, 0, v83\n"
        "v_add_f32 v80, v80, v96\n"
        "v_add_f32 v81, v81, v97\n"
        "v_add_f32 v82, v82, v98\n"
        "v_add_f32 v83, v83, v99\n"
        "v_cvt_pk_bf16_f32 v88, v80, v81\n"
        "v_cvt_pk_bf16_f32 v89, v82, v83\n"
        "v_and_b32 v84, 0xffff0000, v67\n"
        "global_load_lds_dwordx4 v147, s[60:61] offset:3072\n"
        "s_add_u32 m0, s73, 0x1c000\n"
        "s_add_u32 s60, s20, s66\n"
        "s_addc_u32 s61, s21, 0\n"
        "s_xor_b32 s66, s17, 3\n"
        "s_lshl_b32 s66, s66, 17\n"
        "v_lshlrev_b32 v85, 16, v67\n"
        "v_and_b32 v86, 0xffff0000, v66\n"
        "v_lshlrev_b32 v87, 16, v66\n"
        "v_mul_f32 v84, s54, v84\n"
        "v_mul_f32 v85, s54, v85\n"
        "global_load_lds_dwordx4 v254, s[60:61] offset:0 sc1\n"
        "v_mul_f32 v86, s54, v86\n"
        "v_mul_f32 v87, s54, v87\n"
        "v_cvt_pk_bf16_f32 v84, 0, v84\n"
        "v_cvt_pk_bf16_f32 v85, 0, v85\n"
        "v_cvt_pk_bf16_f32 v86, 0, v86\n"
        "v_cvt_pk_bf16_f32 v87, 0, v87\n"
        "v_add_f32 v84, v84, v100\n"
        "v_add_f32 v85, v85, v101\n"
        "v_add_f32 v86, v86, v102\n"
        "global_load_lds_dwordx4 v254, s[60:61] offset:1024 sc1\n"
        "v_add_f32 v87, v87, v103\n"
        "v_cvt_pk_bf16_f32 v90, v84, v85\n"
        "v_cvt_pk_bf16_f32 v91, v86, v87\n"
        "v_and_b32 v80, 0xffff0000, v69\n"
        "v_permlane16_swap_b32 v88, v90\n"
        "v_permlane16_swap_b32 v89, v91\n"
        "v_lshlrev_b32 v81, 16, v69\n"
        "global_load_lds_dwordx4 v254, s[60:61] offset:2048 sc1\n"
        "v_and_b32 v82, 0xffff0000, v68\n"
        "v_lshlrev_b32 v83, 16, v68\n"
        "v_mul_f32 v80, s54, v80\n"
        "v_mul_f32 v81, s54, v81\n"
        "s_waitcnt vmcnt(15) lgkmcnt(0)\n"
        "s_barrier\n"
        "ds_read_b128 a[0:3], v224 offset:16384\n"
        "global_load_lds_dwordx4 v254, s[60:61] offset:3072 sc1\n"
        "ds_read_b128 a[4:7], v224 offset:24576\n"
        "ds_read_b128 a[8:11], v224 offset:17408\n"
        "ds_read_b128 a[12:15], v224 offset:25600\n"
        "ds_read_b64_tr_b16 a[176:177], v153 offset:0\n"
        "ds_read_b64_tr_b16 a[178:179], v153 offset:4096\n"
        "ds_read_b64_tr_b16 a[180:181], v154 offset:0\n"
        "ds_read_b64_tr_b16 a[182:183], v154 offset:4096\n"
        "ds_read_b64_tr_b16 a[184:185], v153 offset:8192\n"
        "ds_read_b64_tr_b16 a[186:187], v153 offset:12288\n"
        "ds_read_b64_tr_b16 a[188:189], v154 offset:8192\n"
        "ds_read_b64_tr_b16 a[190:191], v154 offset:12288\n"
        "s_add_u32 s60, s22, s66\n"
        "s_addc_u32 s61, s23, 0\n"
        "s_add_u32 m0, s73, 0x20000\n"
        "s_xor_b32 s66, s17, 3\n"
        "global_load_lds_dwordx4 v144, s[60:61] offset:0\n"
        "s_lshl_b32 s66, s66, 14\n"
        "v_mul_f32 v82, s54, v82\n"
        "s_waitcnt lgkmcnt(11)\n"
        "v_mfma_f32_16x16x32_bf16 v[0:3], v[160:163], a[0:3], 0\n"
        "ds_read_b128 a[16:19], v224 offset:18432\n"
        "s_waitcnt lgkmcnt(11)\n"
        "v_mfma_f32_16x16x32_bf16 v[0:3], v[168:171], a[4:7], v[0:3]\n"
        "ds_read_b128 a[20:23], v224 offset:26624\n"
        "v_mul_f32 v83, s54, v83\n"
        "v_mfma_f32_16x16x32_bf16 v[4:7], v[172:175], a[4:7], 0\n"
        "global_load_lds_dwordx4 v145, s[60:61] offset:1024\n"
        "ds_read_b128 a[24:27], v224 offset:19456\n"
        "v_mfma_f32_16x16x32_bf16 v[4:7], v[164:167], a[0:3], v[4:7]\n"
        "ds_read_b128 a[28:31], v224 offset:27648\n"
        "s_waitcnt lgkmcnt(9)\n"
        "v_mfma_f32_16x16x32_bf16 v[12:15], v[164:167], a[8:11], 0\n"
        "ds_read_b128 a[192:195], v238 offset:16384\n"
        "v_cvt_pk_bf16_f32 v80, 0, v80\n"
        "v_mfma_f32_16x16x32_bf16 v[12:15], v[172:175], a[12:15], v[12:15]\n"
        "global_load_lds_dwordx4 v146, s[60:61] offset:2048\n"
        "ds_read_b128 a[196:199], v238 offset:24576\n"
        "v_mfma_f32_16x16x32_bf16 v[8:11], v[168:171], a[12:15], 0\n"
        "ds_read_b128 a[200:203], v238 offset:17408\n"
        "v_cvt_pk_bf16_f32 v81, 0, v81\n"
        "v_mfma_f32_16x16x32_bf16 v[8:11], v[160:163], a[8:11], v[8:11]\n"
        "ds_read_b128 a[204:207], v238 offset:25600\n"
        "v_cvt_pk_bf16_f32 v82, 0, v82\n"
        "s_waitcnt lgkmcnt(7)\n"
        "v_mfma_f32_16x16x32_bf16 v[16:19], v[160:163], a[16:19], 0\n"
        "global_load_lds_dwordx4 v147, s[60:61] offset:3072\n"
        "s_add_u32 m0, s73, 0x24000\n"
        "s_waitcnt lgkmcnt(6)\n"
        "v_mfma_f32_16x16x32_bf16 v[16:19], v[168:171], a[20:23], v[16:19]\n"
        "s_add_u32 s60, s20, s66\n"
        "s_addc_u32 s61, s21, 0\n"
        "s_xor_b32 s66, s17, 4\n"
        "v_mfma_f32_16x16x32_bf16 v[20:23], v[172:175], a[20:23], 0\n"
        "s_lshl_b32 s66, s66, 14\n"
        "ds_read_b128 a[208:211], v238 offset:18432\n"
        "v_mfma_f32_16x16x32_bf16 v[20:23], v[164:167], a[16:19], v[20:23]\n"
        "global_load_lds_dwordx4 v254, s[60:61] offset:0 sc1\n"
        "s_waitcnt lgkmcnt(5)\n"
        "v_mfma_f32_16x16x32_bf16 v[28:31], v[164:167], a[24:27], 0\n"
        "ds_read_b128 a[212:215], v238 offset:26624\n"
        "v_cvt_pk_bf16_f32 v83, 0, v83\n"
        "v_mfma_f32_16x16x32_bf16 v[28:31], v[172:175], a[28:31], v[28:31]\n"
        "ds_read_b128 a[216:219], v238 offset:19456\n"
        "v_cvt_pk_bf16_f32 v105, 0, v105\n"
        "v_mfma_f32_16x16x32_bf16 v[24:27], v[168:171], a[28:31], 0\n"
        "global_load_lds_dwordx4 v254, s[60:61] offset:1024 sc1\n"
        "ds_read_b128 a[220:223], v238 offset:27648\n"
        "v_mfma_f32_16x16x32_bf16 v[24:27], v[160:163], a[24:27], v[24:27]\n"
        "s_waitcnt lgkmcnt(4)\n"
        "v_cvt_pk_bf16_f32 v106, 0, v106\n"
        "v_cvt_pk_bf16_f32 v107, 0, v107\n"
        "v_mfma_f32_16x16x32_bf16 v[32:35], v[160:163], a[192:195], 0\n"
        "v_add_f32 v80, v80, v104\n"
        "v_add_f32 v81, v81, v105\n"
        "v_mfma_f32_16x16x32_bf16 v[32:35], v[168:171], a[196:199], v[32:35]\n"
        "global_load_lds_dwordx4 v254, s[60:61] offset:2048 sc1\n"
        "v_add_f32 v82, v82, v106\n"
        "v_mfma_f32_16x16x32_bf16 v[36:39], v[172:175], a[196:199], 0\n"
        "s_waitcnt lgkmcnt(3)\n"
        "v_add_f32 v83, v83, v107\n"
        "v_cvt_pk_bf16_f32 v92, v80, v81\n"
        "v_mfma_f32_16x16x32_bf16 v[36:39], v[164:167], a[192:195], v[36:39]\n"
        "v_cvt_pk_bf16_f32 v93, v82, v83\n"
        "v_and_b32 v84, 0xffff0000, v71\n"
        "v_mfma_f32_16x16x32_bf16 v[44:47], v[164:167], a[200:203], 0\n"
        "global_load_lds_dwordx4 v254, s[60:61] offset:3072 sc1\n"
        "s_add_u32 m0, s73, 0x10000\n"
        "v_lshlrev_b32 v85, 16, v71\n"
        "v_mfma_f32_16x16x32_bf16 v[44:47], v[172:175], a[204:207], v[44:47]\n"
        "s_add_u32 s60, s20, s66\n"
        "s_addc_u32 s61, s21, 0\n"
        "s_xor_b32 s66, s17, 5\n"
        "v_mfma_f32_16x16x32_bf16 v[40:43], v[168:171], a[204:207], 0\n"
        "s_lshl_b32 s66, s66, 17\n"
        "v_and_b32 v86, 0xffff0000, v70\n"
        "v_lshlrev_b32 v87, 16, v70\n"
        "v_mfma_f32_16x16x32_bf16 v[40:43], v[160:163], a[200:203], v[40:43]\n"
        "global_load_lds_dwordx4 v254, s[60:61] offset:0 sc1\n"
        "s_waitcnt vmcnt(17)\n"
        "v_mfma_f32_16x16x32_bf16 v[48:51], v[160:163], a[208:211], 0\n"
        "s_waitcnt lgkmcnt(0)\n"
        "v_mul_f32 v84, s54, v84\n"
        "v_mul_f32 v85, s54, v85\n"
        "v_mfma_f32_16x16x32_bf16 v[48:51], v[168:171], a[212:215], v[48:51]\n"
        "s_barrier\n"
        "v_mfma_f32_16x16x32_bf16 v[52:55], v[172:175], a[212:215], 0\n"
        "global_load_lds_dwordx4 v254, s[60:61] offset:1024 sc1\n"
        "ds_read_b64_tr_b16 a[32:33], v153 offset:32768\n"
        "v_mfma_f32_16x16x32_bf16 v[52:55], v[164:167], a[208:211], v[52:55]\n"
        "ds_read_b64_tr_b16 a[34:35], v153 offset:36864\n"
        "v_mul_f32 v86, s54, v86\n"
        "v_mfma_f32_16x16x32_bf16 v[60:63], v[164:167], a[216:219], 0\n"
        "ds_read_b64_tr_b16 a[40:41], v153 offset:40960\n"
        "v_mul_f32 v87, s54, v87\n"
        "v_mfma_f32_16x16x32_bf16 v[60:63], v[172:175], a[220:223], v[60:63]\n"
        "global_load_lds_dwordx4 v254, s[60:61] offset:2048 sc1\n"
        "ds_read_b64_tr_b16 a[42:43], v153 offset:45056\n"
        "v_mfma_f32_16x16x32_bf16 v[56:59], v[168:171], a[220:223], 0\n"
        "ds_read_b64_tr_b16 a[36:37], v154 offset:32768\n"
        "v_cvt_pk_bf16_f32 v84, 0, v84\n"
        "v_mfma_f32_16x16x32_bf16 v[56:59], v[160:163], a[216:219], v[56:59]\n"
        "ds_read_b64_tr_b16 a[38:39], v154 offset:36864\n"
        "ds_read_b64_tr_b16 a[44:45], v154 offset:40960\n"
        "ds_read_b64_tr_b16 a[46:47], v154 offset:45056\n"
        "global_load_lds_dwordx4 v254, s[60:61] offset:3072 sc1\n"
        "s_add_u32 m0, s73, 0x14000\n"
        "s_add_u32 s60, s22, s66\n"
        "s_addc_u32 s61, s23, 0\n"
        "s_xor_b32 s66, s17, 5\n"
        "s_lshl_b32 s66, s66, 14\n"
        "ds_read_b128 a[0:3], v224 offset:49152\n"
        "global_load_lds_dwordx4 v144, s[60:61] offset:0\n"
        "global_load_lds_dwordx4 v145, s[60:61] offset:1024\n"
        "s_waitcnt lgkmcnt(0)\n"
        "global_load_lds_dwordx4 v146, s[60:61] offset:2048\n"
        "v_mfma_f32_16x16x32_bf16 v[0:3], a[32:35], a[0:3], v[0:3]\n"
        "ds_read_b128 a[4:7], v224 offset:57344\n"
        "global_load_lds_dwordx4 v147, s[60:61] offset:3072\n"
        "s_add_u32 m0, s73, 0x0\n"
        "s_add_u32 s60, s20, s66\n"
        "s_addc_u32 s61, s21, 0\n"
        "global_load_lds_dwordx4 v254, s[60:61] offset:0 sc1\n"
        "ds_read_b128 a[8:11], v224 offset:50176\n"
        "ds_read_b128 a[12:15], v224 offset:58368\n"
        "ds_read_b128 a[16:19], v224 offset:51200\n"
        "ds_read_b128 a[20:23], v224 offset:59392\n"
        "s_xor_b32 s66, s17, 6\n"
        "s_lshl_b32 s66, s66, 17\n"
        "s_waitcnt lgkmcnt(4)\n"
        "v_mfma_f32_16x16x32_bf16 v[0:3], a[40:43], a[4:7], v[0:3]\n"
        "global_load_lds_dwordx4 v254, s[60:61] offset:1024 sc1\n"
        "ds_read_b128 a[24:27], v224 offset:52224\n"
        "v_mfma_f32_16x16x32_bf16 v[4:7], a[44:47], a[4:7], v[4:7]\n"
        "ds_read_b128 a[28:31], v224 offset:60416\n"
        "v_cvt_pk_bf16_f32 v85, 0, v85\n"
        "v_mfma_f32_16x16x32_bf16 v[4:7], a[36:39], a[0:3], v[4:7]\n"
        "global_load_lds_dwordx4 v254, s[60:61] offset:2048 sc1\n"
        "ds_read_b128 a[192:195], v238 offset:49152\n"
        "LHB0_%=:\n"
        "s_cbranch_execz LHRB_%=\n"
        "ds_read_b128 a[196:199], v238 offset:57344\n"
        "ds_read_b128 a[200:203], v238 offset:50176\n"
        "v_cvt_pk_bf16_f32 v86, 0, v86\n"
        "s_waitcnt lgkmcnt(8)\n"
        "v_mfma_f32_16x16x32_bf16 v[12:15], a[36:39], a[8:11], v[12:15]\n"
        "ds_read_b128 a[204:207], v238 offset:58368\n"
        "s_waitcnt lgkmcnt(8)\n"
        "v_mfma_f32_16x16x32_bf16 v[12:15], a[44:47], a[12:15], v[12:15]\n"
        "global_load_lds_dwordx4 v254, s[60:61] offset:3072 sc1\n"
        "ds_read_b128 a[208:211], v238 offset:51200\n"
        "s_add_u32 m0, s73, 0x4000\n"
        "v_mfma_f32_16x16x32_bf16 v[8:11], a[40:43], a[12:15], v[8:11]\n"
        "ds_read_b128 a[212:215], v238 offset:59392\n"
        "s_add_u32 s60, s22, s66\n"
        "s_addc_u32 s61, s23, 0\n"
        "v_mfma_f32_16x16x32_bf16 v[8:11], a[32:35], a[8:11], v[8:11]\n"
        "ds_read_b128 a[216:219], v238 offset:52224\n"
        "s_xor_b32 s66, s17, 6\n"
        "s_waitcnt lgkmcnt(7)\n"
        "v_mfma_f32_16x16x32_bf16 v[16:19], a[32:35], a[16:19], v[16:19]\n"
        "ds_read_b128 a[220:223], v238 offset:60416\n"
        "s_lshl_b32 s66, s66, 14\n"
        "v_cvt_pk_bf16_f32 v87, 0, v87\n"
        "v_mfma_f32_16x16x32_bf16 v[16:19], a[40:43], a[20:23], v[16:19]\n"
        "global_store_dwordx4 v229, v[88:91], s[24:25] offset:0 sc1\n"
        "v_cvt_pk_bf16_f32 v108, 0, v108\n"
        "v_cvt_pk_bf16_f32 v109, 0, v109\n"
        "v_cvt_pk_bf16_f32 v110, 0, v110\n"
        "v_cvt_pk_bf16_f32 v111, 0, v111\n"
        "v_mfma_f32_16x16x32_bf16 v[20:23], a[44:47], a[20:23], v[20:23]\n"
        "s_waitcnt lgkmcnt(5)\n"
        "v_add_f32 v84, v84, v108\n"
        "v_add_f32 v85, v85, v109\n"
        "v_mfma_f32_16x16x32_bf16 v[20:23], a[36:39], a[16:19], v[20:23]\n"
        "s_waitcnt vmcnt(21)\n"
        "v_add_f32 v86, v86, v110\n"
        "v_add_f32 v87, v87, v111\n"
        "v_mfma_f32_16x16x32_bf16 v[28:31], a[36:39], a[24:27], v[28:31]\n"
        "s_barrier\n"
        "v_mfma_f32_16x16x32_bf16 v[28:31], a[44:47], a[28:31], v[28:31]\n"
        "global_load_lds_dwordx4 v144, s[60:61] offset:0\n"
        "ds_read_b64_tr_b16 a[48:49], v155 offset:32768\n"
        "v_mfma_f32_16x16x32_bf16 v[24:27], a[40:43], a[28:31], v[24:27]\n"
        "ds_read_b64_tr_b16 a[50:51], v155 offset:36864\n"
        "v_cvt_pk_bf16_f32 v94, v84, v85\n"
        "v_mfma_f32_16x16x32_bf16 v[24:27], a[32:35], a[24:27], v[24:27]\n"
        "ds_read_b64_tr_b16 a[56:57], v155 offset:40960\n"
        "v_cvt_pk_bf16_f32 v95, v86, v87\n"
        "v_mfma_f32_16x16x32_bf16 v[32:35], a[32:35], a[192:195], v[32:35]\n"
        "ds_read_b64_tr_b16 a[58:59], v155 offset:45056\n"
        "global_load_lds_dwordx4 v145, s[60:61] offset:1024\n"
        "v_mfma_f32_16x16x32_bf16 v[32:35], a[40:43], a[196:199], v[32:35]\n"
        "ds_read_b64_tr_b16 a[52:53], v156 offset:32768\n"
        "v_and_b32 v80, 0xffff0000, v73\n"
        "v_mfma_f32_16x16x32_bf16 v[36:39], a[44:47], a[196:199], v[36:39]\n"
        "ds_read_b64_tr_b16 a[54:55], v156 offset:36864\n"
        "v_lshlrev_b32 v81, 16, v73\n"
        "v_mfma_f32_16x16x32_bf16 v[36:39], a[36:39], a[192:195], v[36:39]\n"
        "ds_read_b64_tr_b16 a[60:61], v156 offset:40960\n"
        "global_load_lds_dwordx4 v146, s[60:61] offset:2048\n"
        "v_mfma_f32_16x16x32_bf16 v[44:47], a[36:39], a[200:203], v[44:47]\n"
        "ds_read_b64_tr_b16 a[62:63], v156 offset:45056\n"
        "s_waitcnt lgkmcnt(8)\n"
        "v_mfma_f32_16x16x32_bf16 v[44:47], a[44:47], a[204:207], v[44:47]\n"
        "ds_read_b128 a[0:3], v225 offset:49152\n"
        "v_and_b32 v82, 0xffff0000, v72\n"
        "v_mfma_f32_16x16x32_bf16 v[40:43], a[40:43], a[204:207], v[40:43]\n"
        "ds_read_b128 a[4:7], v225 offset:57344\n"
        "global_load_lds_dwordx4 v147, s[60:61] offset:3072\n"
        "s_add_u32 m0, s73, 0x8000\n"
        "v_mfma_f32_16x16x32_bf16 v[40:43], a[32:35], a[200:203], v[40:43]\n"
        "ds_read_b128 a[8:11], v225 offset:50176\n"
        "v_lshlrev_b32 v83, 16, v72\n"
        "v_mfma_f32_16x16x32_bf16 v[48:51], a[32:35], a[208:211], v[48:51]\n"
        "ds_read_b128 a[12:15], v225 offset:58368\n"
        "v_mul_f32 v80, s54, v80\n"
        "v_mfma_f32_16x16x32_bf16 v[48:51], a[40:43], a[212:215], v[48:51]\n"
        "ds_read_b128 a[16:19], v225 offset:51200\n"
        "v_mul_f32 v81, s54, v81\n"
        "v_mfma_f32_16x16x32_bf16 v[52:55], a[44:47], a[212:215], v[52:55]\n"
        "ds_read_b128 a[20:23], v225 offset:59392\n"
        "v_mul_f32 v82, s54, v82\n"
        "v_mfma_f32_16x16x32_bf16 v[52:55], a[36:39], a[208:211], v[52:55]\n"
        "ds_read_b128 a[24:27], v225 offset:52224\n"
        "v_mul_f32 v83, s54, v83\n"
        "v_mfma_f32_16x16x32_bf16 v[60:63], a[36:39], a[216:219], v[60:63]\n"
        "ds_read_b128 a[28:31], v225 offset:60416\n"
        "v_cvt_pk_bf16_f32 v80, 0, v80\n"
        "v_mfma_f32_16x16x32_bf16 v[60:63], a[44:47], a[220:223], v[60:63]\n"
        "ds_read_b128 a[192:195], v239 offset:49152\n"
        "v_cvt_pk_bf16_f32 v81, 0, v81\n"
        "v_mfma_f32_16x16x32_bf16 v[56:59], a[40:43], a[220:223], v[56:59]\n"
        "ds_read_b128 a[196:199], v239 offset:57344\n"
        "v_cvt_pk_bf16_f32 v82, 0, v82\n"
        "v_mfma_f32_16x16x32_bf16 v[56:59], a[32:35], a[216:219], v[56:59]\n"
        "s_waitcnt lgkmcnt(7)\n"
        "v_permlane16_swap_b32 v92, v94\n"
        "v_mfma_f32_16x16x32_bf16 v[0:3], a[48:51], a[0:3], v[0:3]\n"
        "s_waitcnt lgkmcnt(6)\n"
        "v_permlane16_swap_b32 v93, v95\n"
        "v_mfma_f32_16x16x32_bf16 v[0:3], a[56:59], a[4:7], v[0:3]\n"
        "global_store_dwordx4 v229, v[92:95], s[24:25] offset:1024 sc1\n"
        "s_waitcnt lgkmcnt(5)\n"
        "v_cvt_pk_bf16_f32 v83, 0, v83\n"
        "v_cvt_pk_bf16_f32 v128, 0, v128\n"
        "v_cvt_pk_bf16_f32 v129, 0, v129\n"
        "v_mfma_f32_16x16x32_bf16 v[4:7], a[60:63], a[4:7], v[4:7]\n"
        "s_waitcnt lgkmcnt(4)\n"
        "v_cvt_pk_bf16_f32 v130, 0, v130\n"
        "v_cvt_pk_bf16_f32 v131, 0, v131\n"
        "v_mfma_f32_16x16x32_bf16 v[4:7], a[52:55], a[0:3], v[4:7]\n"
        "s_waitcnt lgkmcnt(3)\n"
        "v_add_f32 v80, v80, v128\n"
        "v_add_f32 v81, v81, v129\n"
        "v_mfma_f32_16x16x32_bf16 v[12:15], a[52:55], a[8:11], v[12:15]\n"
        "s_waitcnt lgkmcnt(2)\n"
        "v_add_f32 v82, v82, v130\n"
        "v_add_f32 v83, v83, v131\n"
        "v_mfma_f32_16x16x32_bf16 v[12:15], a[60:63], a[12:15], v[12:15]\n"
        "s_waitcnt lgkmcnt(1)\n"
        "v_cvt_pk_bf16_f32 v88, v80, v81\n"
        "v_cvt_pk_bf16_f32 v89, v82, v83\n"
        "v_mfma_f32_16x16x32_bf16 v[8:11], a[56:59], a[12:15], v[8:11]\n"
        "ds_read_b128 a[200:203], v239 offset:50176\n"
        "v_and_b32 v84, 0xffff0000, v75\n"
        "v_mfma_f32_16x16x32_bf16 v[8:11], a[48:51], a[8:11], v[8:11]\n"
        "s_waitcnt lgkmcnt(1)\n"
        "v_lshlrev_b32 v85, 16, v75\n"
        "v_and_b32 v86, 0xffff0000, v74\n"
        "v_mfma_f32_16x16x32_bf16 v[16:19], a[48:51], a[16:19], v[16:19]\n"
        "ds_read_b128 a[204:207], v239 offset:58368\n"
        "v_lshlrev_b32 v87, 16, v74\n"
        "v_mfma_f32_16x16x32_bf16 v[16:19], a[56:59], a[20:23], v[16:19]\n"
        "ds_read_b128 a[208:211], v239 offset:51200\n"
        "v_mul_f32 v84, s54, v84\n"
        "v_mfma_f32_16x16x32_bf16 v[20:23], a[60:63], a[20:23], v[20:23]\n"
        "ds_read_b128 a[212:215], v239 offset:59392\n"
        "v_mul_f32 v85, s54, v85\n"
        "v_mfma_f32_16x16x32_bf16 v[20:23], a[52:55], a[16:19], v[20:23]\n"
        "ds_read_b128 a[216:219], v239 offset:52224\n"
        "v_mul_f32 v86, s54, v86\n"
        "v_mfma_f32_16x16x32_bf16 v[28:31], a[52:55], a[24:27], v[28:31]\n"
        "ds_read_b128 a[220:223], v239 offset:60416\n"
        "v_mul_f32 v87, s54, v87\n"
        "v_mfma_f32_16x16x32_bf16 v[28:31], a[60:63], a[28:31], v[28:31]\n"
        "s_waitcnt vmcnt(18)\n"
        "v_cvt_pk_bf16_f32 v84, 0, v84\n"
        "v_cvt_pk_bf16_f32 v85, 0, v85\n"
        "v_mfma_f32_16x16x32_bf16 v[24:27], a[56:59], a[28:31], v[24:27]\n"
        "s_waitcnt lgkmcnt(5)\n"
        "v_cvt_pk_bf16_f32 v86, 0, v86\n"
        "v_cvt_pk_bf16_f32 v87, 0, v87\n"
        "v_mfma_f32_16x16x32_bf16 v[24:27], a[48:51], a[24:27], v[24:27]\n"
        "s_add_u32 s60, s20, s66\n"
        "s_addc_u32 s61, s21, 0\n"
        "v_cvt_pk_bf16_f32 v132, 0, v132\n"
        "v_mfma_f32_16x16x32_bf16 v[32:35], a[48:51], a[192:195], v[32:35]\n"
        "s_waitcnt lgkmcnt(4)\n"
        "v_cvt_pk_bf16_f32 v133, 0, v133\n"
        "v_cvt_pk_bf16_f32 v134, 0, v134\n"
        "v_mfma_f32_16x16x32_bf16 v[32:35], a[56:59], a[196:199], v[32:35]\n"
        "s_waitcnt lgkmcnt(3)\n"
        "v_cvt_pk_bf16_f32 v135, 0, v135\n"
        "v_add_f32 v84, v84, v132\n"
        "v_mfma_f32_16x16x32_bf16 v[36:39], a[60:63], a[196:199], v[36:39]\n"
        "s_waitcnt lgkmcnt(2)\n"
        "v_add_f32 v85, v85, v133\n"
        "v_add_f32 v86, v86, v134\n"
        "v_mfma_f32_16x16x32_bf16 v[36:39], a[52:55], a[192:195], v[36:39]\n"
        "s_waitcnt lgkmcnt(1)\n"
        "v_add_f32 v87, v87, v135\n"
        "v_cvt_pk_bf16_f32 v90, v84, v85\n"
        "v_mfma_f32_16x16x32_bf16 v[44:47], a[52:55], a[200:203], v[44:47]\n"
        "s_waitcnt lgkmcnt(0)\n"
        "v_cvt_pk_bf16_f32 v91, v86, v87\n"
        "v_and_b32 v80, 0xffff0000, v77\n"
        "v_mfma_f32_16x16x32_bf16 v[44:47], a[60:63], a[204:207], v[44:47]\n"
        "s_xor_b32 s66, s17, 7\n"
        "v_permlane16_swap_b32 v88, v90\n"
        "v_mfma_f32_16x16x32_bf16 v[40:43], a[56:59], a[204:207], v[40:43]\n"
        "s_lshl_b32 s66, s66, 17\n"
        "v_permlane16_swap_b32 v89, v91\n"
        "v_mfma_f32_16x16x32_bf16 v[40:43], a[48:51], a[200:203], v[40:43]\n"
        "global_store_dwordx4 v229, v[88:91], s[24:25] offset:2048 sc1\n"
        "s_barrier\n"
        "v_lshlrev_b32 v81, 16, v77\n"
        "v_and_b32 v82, 0xffff0000, v76\n"
        "v_mfma_f32_16x16x32_bf16 v[48:51], a[48:51], a[208:211], v[48:51]\n"
        "ds_read_b64_tr_b16 a[224:225], v157 offset:0\n"
        "v_lshlrev_b32 v83, 16, v76\n"
        "v_mfma_f32_16x16x32_bf16 v[48:51], a[56:59], a[212:215], v[48:51]\n"
        "ds_read_b64_tr_b16 a[226:227], v157 offset:4096\n"
        "v_mul_f32 v80, s54, v80\n"
        "v_mfma_f32_16x16x32_bf16 v[52:55], a[60:63], a[212:215], v[52:55]\n"
        "global_load_lds_dwordx4 v254, s[60:61] offset:0 sc1\n"
        "v_mul_f32 v81, s54, v81\n"
        "v_mfma_f32_16x16x32_bf16 v[52:55], a[52:55], a[208:211], v[52:55]\n"
        "ds_read_b64_tr_b16 a[232:233], v157 offset:8192\n"
        "v_mul_f32 v82, s54, v82\n"
        "v_mfma_f32_16x16x32_bf16 v[60:63], a[52:55], a[216:219], v[60:63]\n"
        "ds_read_b64_tr_b16 a[234:235], v157 offset:12288\n"
        "v_mul_f32 v83, s54, v83\n"
        "v_mfma_f32_16x16x32_bf16 v[60:63], a[60:63], a[220:223], v[60:63]\n"
        "ds_read_b64_tr_b16 a[228:229], v158 offset:0\n"
        "v_cvt_pk_bf16_f32 v80, 0, v80\n"
        "v_mfma_f32_16x16x32_bf16 v[56:59], a[56:59], a[220:223], v[56:59]\n"
        "ds_read_b64_tr_b16 a[230:231], v158 offset:4096\n"
        "v_cvt_pk_bf16_f32 v81, 0, v81\n"
        "v_mfma_f32_16x16x32_bf16 v[56:59], a[48:51], a[216:219], v[56:59]\n"
        "v_cvt_pk_bf16_f32 v82, 0, v82\n"
        "v_cvt_pk_bf16_f32 v83, 0, v83\n"
        "v_cvt_pk_bf16_f32 v136, 0, v136\n"
        "v_cvt_pk_bf16_f32 v137, 0, v137\n"
        "v_cvt_pk_bf16_f32 v138, 0, v138\n"
        "v_cvt_pk_bf16_f32 v139, 0, v139\n"
        "v_add_f32 v80, v80, v136\n"
        "v_add_f32 v81, v81, v137\n"
        "v_add_f32 v82, v82, v138\n"
        "v_add_f32 v83, v83, v139\n"
        "v_cvt_pk_bf16_f32 v92, v80, v81\n"
        "v_cvt_pk_bf16_f32 v93, v82, v83\n"
        "v_and_b32 v84, 0xffff0000, v79\n"
        "v_lshlrev_b32 v85, 16, v79\n"
        "v_and_b32 v86, 0xffff0000, v78\n"
        "v_lshlrev_b32 v87, 16, v78\n"
        "v_mul_f32 v84, s54, v84\n"
        "v_mul_f32 v85, s54, v85\n"
        "v_mul_f32 v86, s54, v86\n"
        "v_mul_f32 v87, s54, v87\n"
        "v_cvt_pk_bf16_f32 v84, 0, v84\n"
        "v_cvt_pk_bf16_f32 v85, 0, v85\n"
        "v_cvt_pk_bf16_f32 v86, 0, v86\n"
        "v_cvt_pk_bf16_f32 v87, 0, v87\n"
        "v_cvt_pk_bf16_f32 v140, 0, v140\n"
        "v_cvt_pk_bf16_f32 v141, 0, v141\n"
        "v_cvt_pk_bf16_f32 v142, 0, v142\n"
        "v_cvt_pk_bf16_f32 v143, 0, v143\n"
        "v_add_f32 v84, v84, v140\n"
        "v_add_f32 v85, v85, v141\n"
        "v_add_f32 v86, v86, v142\n"
        "v_add_f32 v87, v87, v143\n"
        "v_cvt_pk_bf16_f32 v94, v84, v85\n"
        "v_cvt_pk_bf16_f32 v95, v86, v87\n"
        "ds_read_b64_tr_b16 a[236:237], v158 offset:8192\n"
        "v_permlane16_swap_b32 v92, v94\n"
        "v_permlane16_swap_b32 v93, v95\n"
        "global_store_dwordx4 v229, v[92:95], s[24:25] offset:3072 sc1\n"
        "ds_read_b64_tr_b16 a[238:239], v158 offset:12288\n"
        "ds_read_b128 a[0:3], v226 offset:16384\n"
        "ds_read_b128 a[4:7], v226 offset:24576\n"
        "global_load_lds_dwordx4 v254, s[60:61] offset:1024 sc1\n"
        "ds_read_b128 a[8:11], v226 offset:17408\n"
        "ds_read_b128 a[12:15], v226 offset:25600\n"
        "global_load_lds_dwordx4 v254, s[60:61] offset:2048 sc1\n"
        "ds_read_b128 a[16:19], v226 offset:18432\n"
        "ds_read_b128 a[20:23], v226 offset:26624\n"
        "ds_read_b128 a[24:27], v226 offset:19456\n"
        "ds_read_b128 a[28:31], v226 offset:27648\n"
        "v_and_b32 v150, 1, v233\n"
        "s_waitcnt lgkmcnt(7)\n"
        "v_mfma_f32_16x16x32_bf16 v[0:3], a[224:227], a[0:3], v[0:3]\n"
        "global_load_lds_dwordx4 v254, s[60:61] offset:3072 sc1\n"
        "s_add_u32 m0, s73, 0xc000\n"
        "s_waitcnt lgkmcnt(6)\n"
        "v_mfma_f32_16x16x32_bf16 v[0:3], a[232:235], a[4:7], v[0:3]\n"
        "s_add_u32 s60, s22, s66\n"
        "s_addc_u32 s61, s23, 0\n"
        "s_xor_b32 s66, s17, 7\n"
        "v_mfma_f32_16x16x32_bf16 v[4:7], a[236:239], a[4:7], v[4:7]\n"
        "ds_read_b128 a[192:195], v240 offset:16384\n"
        "s_lshl_b32 s66, s66, 14\n"
        "v_lshlrev_b32 v151, 11, v232\n"
        "v_mfma_f32_16x16x32_bf16 v[4:7], a[228:231], a[0:3], v[4:7]\n"
        "global_load_lds_dwordx4 v144, s[60:61] offset:0\n"
        "s_waitcnt lgkmcnt(5)\n"
        "v_mfma_f32_16x16x32_bf16 v[12:15], a[228:231], a[8:11], v[12:15]\n"
        "ds_read_b128 a[196:199], v240 offset:24576\n"
        "v_lshl_add_u32 v151, v150, 5, v151\n"
        "v_mfma_f32_16x16x32_bf16 v[12:15], a[236:239], a[12:15], v[12:15]\n"
        "ds_read_b128 a[200:203], v240 offset:17408\n"
        "v_lshrrev_b32 v150, 1, v233\n"
        "v_mfma_f32_16x16x32_bf16 v[8:11], a[232:235], a[12:15], v[8:11]\n"
        "global_load_lds_dwordx4 v145, s[60:61] offset:1024\n"
        "ds_read_b128 a[204:207], v240 offset:25600\n"
        "v_mfma_f32_16x16x32_bf16 v[8:11], a[224:227], a[8:11], v[8:11]\n"
        "ds_read_b128 a[208:211], v240 offset:18432\n"
        "s_waitcnt lgkmcnt(5)\n"
        "v_mfma_f32_16x16x32_bf16 v[16:19], a[224:227], a[16:19], v[16:19]\n"
        "ds_read_b128 a[212:215], v240 offset:26624\n"
        "v_lshl_add_u32 v151, v150, 4, v151\n"
        "v_mfma_f32_16x16x32_bf16 v[16:19], a[232:235], a[20:23], v[16:19]\n"
        "global_load_lds_dwordx4 v146, s[60:61] offset:2048\n"
        "ds_read_b128 a[216:219], v240 offset:19456\n"
        "v_mfma_f32_16x16x32_bf16 v[20:23], a[236:239], a[20:23], v[20:23]\n"
        "ds_read_b128 a[220:223], v240 offset:27648\n"
        "v_and_b32 v150, 0x70, v254\n"
        "v_mfma_f32_16x16x32_bf16 v[20:23], a[228:231], a[16:19], v[20:23]\n"
        "s_waitcnt vmcnt(23)\n"
        "v_add_u32 v148, v148, v150\n"
        "v_lshrrev_b32 v149, 6, v254\n"
        "v_mfma_f32_16x16x32_bf16 v[28:31], a[228:231], a[24:27], v[28:31]\n"
        "global_load_lds_dwordx4 v147, s[60:61] offset:3072\n"
        "s_add_u32 m0, s73, 0x18000\n"
        "v_lshlrev_b32 v149, 7, v149\n"
        "v_mfma_f32_16x16x32_bf16 v[28:31], a[236:239], a[28:31], v[28:31]\n"
        "s_barrier\n"
        "v_mfma_f32_16x16x32_bf16 v[24:27], a[232:235], a[28:31], v[24:27]\n"
        "s_add_u32 s60, s20, s66\n"
        "s_addc_u32 s61, s21, 0\n"
        "s_xor_b32 s66, s17, 8\n"
        "v_mfma_f32_16x16x32_bf16 v[24:27], a[224:227], a[24:27], v[24:27]\n"
        "global_load_lds_dwordx4 v254, s[60:61] offset:0 sc1\n"
        "s_lshl_b32 s66, s66, 17\n"
        "s_waitcnt lgkmcnt(3)\n"
        "v_mfma_f32_16x16x32_bf16 v[32:35], a[224:227], a[192:195], v[32:35]\n"
        "ds_read_b128 a[0:3], v225 offset:0\n"
        "v_bfe_u32 v150, v254, 4, 2\n"
        "v_mfma_f32_16x16x32_bf16 v[32:35], a[232:235], a[196:199], v[32:35]\n"
        "ds_read_b128 a[4:7], v225 offset:8192\n"
        "v_lshl_add_u32 v149, v150, 3, v149\n"
        "v_mfma_f32_16x16x32_bf16 v[36:39], a[236:239], a[196:199], v[36:39]\n"
        "global_load_lds_dwordx4 v254, s[60:61] offset:1024 sc1\n"
        "ds_read_b128 a[8:11], v225 offset:1024\n"
        "v_mfma_f32_16x16x32_bf16 v[36:39], a[228:231], a[192:195], v[36:39]\n"
        "ds_read_b128 a[12:15], v225 offset:9216\n"
        "v_mov_b32 v136, 0\n"
        "v_mfma_f32_16x16x32_bf16 v[44:47], a[228:231], a[200:203], v[44:47]\n"
        "ds_read_b128 a[16:19], v225 offset:2048\n"
        "v_mov_b32 v137, 0\n"
        "v_mfma_f32_16x16x32_bf16 v[44:47], a[236:239], a[204:207], v[44:47]\n"
        "global_load_lds_dwordx4 v254, s[60:61] offset:2048 sc1\n"
        "ds_read_b128 a[20:23], v225 offset:10240\n"
        "v_mfma_f32_16x16x32_bf16 v[40:43], a[232:235], a[204:207], v[40:43]\n"
        "ds_read_b128 a[24:27], v225 offset:3072\n"
        "v_mov_b32 v140, 0\n"
        "v_mfma_f32_16x16x32_bf16 v[40:43], a[224:227], a[200:203], v[40:43]\n"
        "ds_read_b128 a[28:31], v225 offset:11264\n"
        "v_mov_b32 v141, 0\n"
        "v_mfma_f32_16x16x32_bf16 v[48:51], a[224:227], a[208:211], v[48:51]\n"
        "global_load_lds_dwordx4 v254, s[60:61] offset:3072 sc1\n"
        "s_add_u32 m0, s73, 0x1c000\n"
        "s_waitcnt lgkmcnt(7)\n"
        "v_mfma_f32_16x16x32_bf16 v[48:51], a[232:235], a[212:215], v[48:51]\n"
        "s_add_u32 s60, s22, s66\n"
        "s_addc_u32 s61, s23, 0\n"
        "s_xor_b32 s66, s17, 8\n"
        "v_mfma_f32_16x16x32_bf16 v[52:55], a[236:239], a[212:215], v[52:55]\n"
        "ds_read_b128 a[192:195], v239 offset:0\n"
        "s_lshl_b32 s66, s66, 14\n"
        "v_add_u32 v149, s16, v149\n"
        "v_mfma_f32_16x16x32_bf16 v[52:55], a[228:231], a[208:211], v[52:55]\n"
        "global_load_lds_dwordx4 v144, s[60:61] offset:0\n"
        "ds_read_b128 a[196:199], v239 offset:8192\n"
        "v_mfma_f32_16x16x32_bf16 v[60:63], a[228:231], a[216:219], v[60:63]\n"
        "ds_read_b128 a[200:203], v239 offset:1024\n"
        "s_waitcnt lgkmcnt(6)\n"
        "v_mfma_f32_16x16x32_bf16 v[60:63], a[236:239], a[220:223], v[60:63]\n"
        "ds_read_b128 a[204:207], v239 offset:9216\n"
        "v_mfma_f32_16x16x32_bf16 v[56:59], a[232:235], a[220:223], v[56:59]\n"
        "global_load_lds_dwordx4 v145, s[60:61] offset:1024\n"
        "ds_read_b128 a[208:211], v239 offset:2048\n"
        "v_mfma_f32_16x16x32_bf16 v[56:59], a[224:227], a[216:219], v[56:59]\n"
        "ds_read_b128 a[212:215], v239 offset:10240\n"
        "s_waitcnt lgkmcnt(6)\n"
        "v_mfma_f32_16x16x32_bf16 v[0:3], v[176:179], a[0:3], v[0:3]\n"
        "ds_read_b128 a[216:219], v239 offset:3072\n"
        "v_mfma_f32_16x16x32_bf16 v[0:3], v[184:187], a[4:7], v[0:3]\n"
        "global_load_lds_dwordx4 v146, s[60:61] offset:2048\n"
        "ds_read_b128 a[220:223], v239 offset:11264\n"
        "v_mfma_f32_16x16x32_bf16 v[4:7], v[188:191], a[4:7], v[4:7]\n"
        "v_mfma_f32_16x16x32_bf16 v[4:7], v[180:183], a[0:3], v[4:7]\n"
        "v_mfma_f32_16x16x32_bf16 v[12:15], v[180:183], a[8:11], v[12:15]\n"
        "global_load_lds_dwordx4 v147, s[60:61] offset:3072\n"
        "s_add_u32 m0, s73, 0x20000\n"
        "v_mfma_f32_16x16x32_bf16 v[12:15], v[188:191], a[12:15], v[12:15]\n"
        "s_add_u32 s60, s20, s66\n"
        "s_addc_u32 s61, s21, 0\n"
        "s_xor_b32 s66, s17, 9\n"
        "v_mfma_f32_16x16x32_bf16 v[8:11], v[184:187], a[12:15], v[8:11]\n"
        "s_lshl_b32 s66, s66, 14\n"
        "s_waitcnt lgkmcnt(3)\n"
        "v_mfma_f32_16x16x32_bf16 v[8:11], v[176:179], a[8:11], v[8:11]\n"
        "v_mfma_f32_16x16x32_bf16 v[16:19], v[176:179], a[16:19], v[16:19]\n"
        "v_mfma_f32_16x16x32_bf16 v[16:19], v[184:187], a[20:23], v[16:19]\n"
        "v_mfma_f32_16x16x32_bf16 v[20:23], v[188:191], a[20:23], v[20:23]\n"
        "v_mfma_f32_16x16x32_bf16 v[20:23], v[180:183], a[16:19], v[20:23]\n"
        "s_waitcnt vmcnt(24)\n"
        "v_mfma_f32_16x16x32_bf16 v[28:31], v[180:183], a[24:27], v[28:31]\n"
        "s_barrier\n"
        "v_mfma_f32_16x16x32_bf16 v[28:31], v[188:191], a[28:31], v[28:31]\n"
        "global_load_lds_dwordx4 v254, s[60:61] offset:0 sc1\n"
        "ds_read_b64_tr_b16 a[240:241], v155 offset:16384\n"
        "v_mfma_f32_16x16x32_bf16 v[24:27], v[184:187], a[28:31], v[24:27]\n"
        "ds_read_b64_tr_b16 a[242:243], v155 offset:20480\n"
        "v_mfma_f32_16x16x32_bf16 v[24:27], v[176:179], a[24:27], v[24:27]\n"
        "ds_read_b64_tr_b16 a[248:249], v155 offset:24576\n"
        "s_waitcnt lgkmcnt(3)\n"
        "v_mfma_f32_16x16x32_bf16 v[32:35], v[176:179], a[192:195], v[32:35]\n"
        "ds_read_b64_tr_b16 a[250:251], v155 offset:28672\n"
        "global_load_lds_dwordx4 v254, s[60:61] offset:1024 sc1\n"
        "v_mfma_f32_16x16x32_bf16 v[32:35], v[184:187], a[196:199], v[32:35]\n"
        "ds_read_b64_tr_b16 a[244:245], v156 offset:16384\n"
        "v_mfma_f32_16x16x32_bf16 v[36:39], v[188:191], a[196:199], v[36:39]\n"
        "ds_read_b64_tr_b16 a[246:247], v156 offset:20480\n"
        "v_mfma_f32_16x16x32_bf16 v[36:39], v[180:183], a[192:195], v[36:39]\n"
        "ds_read_b64_tr_b16 a[252:253], v156 offset:24576\n"
        "global_load_lds_dwordx4 v254, s[60:61] offset:2048 sc1\n"
        "v_mfma_f32_16x16x32_bf16 v[44:47], v[180:183], a[200:203], v[44:47]\n"
        "ds_read_b64_tr_b16 a[254:255], v156 offset:28672\n"
        "v_mfma_f32_16x16x32_bf16 v[44:47], v[188:191], a[204:207], v[44:47]\n"
        "ds_read_b128 a[0:3], v224 offset:0\n"
        "v_mfma_f32_16x16x32_bf16 v[40:43], v[184:187], a[204:207], v[40:43]\n"
        "ds_read_b128 a[4:7], v224 offset:8192\n"
        "global_load_lds_dwordx4 v254, s[60:61] offset:3072 sc1\n"
        "s_add_u32 m0, s73, 0x24000\n"
        "v_mfma_f32_16x16x32_bf16 v[40:43], v[176:179], a[200:203], v[40:43]\n"
        "ds_read_b128 a[8:11], v224 offset:1024\n"
        "s_add_u32 s60, s20, s66\n"
        "s_addc_u32 s61, s21, 0\n"
        "v_mfma_f32_16x16x32_bf16 v[48:51], v[176:179], a[208:211], v[48:51]\n"
        "ds_read_b128 a[12:15], v224 offset:9216\n"
        "s_xor_b32 s66, s17, 10\n"
        "s_lshl_b32 s66, s66, 17\n"
        "v_mfma_f32_16x16x32_bf16 v[48:51], v[184:187], a[212:215], v[48:51]\n"
        "ds_read_b128 a[16:19], v224 offset:2048\n"
        "global_load_lds_dwordx4 v254, s[60:61] offset:0 sc1\n"
        "v_mfma_f32_16x16x32_bf16 v[52:55], v[188:191], a[212:215], v[52:55]\n"
        "ds_read_b128 a[20:23], v224 offset:10240\n"
        "v_mfma_f32_16x16x32_bf16 v[52:55], v[180:183], a[208:211], v[52:55]\n"
        "ds_read_b128 a[24:27], v224 offset:3072\n"
        "v_mfma_f32_16x16x32_bf16 v[60:63], v[180:183], a[216:219], v[60:63]\n"
        "ds_read_b128 a[28:31], v224 offset:11264\n"
        "global_load_lds_dwordx4 v254, s[60:61] offset:1024 sc1\n"
        "v_mfma_f32_16x16x32_bf16 v[60:63], v[188:191], a[220:223], v[60:63]\n"
        "ds_read_b128 a[192:195], v238 offset:0\n"
        "s_waitcnt lgkmcnt(8)\n"
        "v_mfma_f32_16x16x32_bf16 v[56:59], v[184:187], a[220:223], v[56:59]\n"
        "ds_read_b128 a[196:199], v238 offset:8192\n"
        "s_waitcnt lgkmcnt(8)\n"
        "v_mfma_f32_16x16x32_bf16 v[56:59], v[176:179], a[216:219], v[56:59]\n"
        "global_load_lds_dwordx4 v254, s[60:61] offset:2048 sc1\n"
        "v_mfma_f32_16x16x32_bf16 v[0:3], a[240:243], a[0:3], v[0:3]\n"
        "ds_read_b128 a[200:203], v238 offset:1024\n"
        "s_waitcnt lgkmcnt(7)\n"
        "v_mfma_f32_16x16x32_bf16 v[0:3], a[248:251], a[4:7], v[0:3]\n"
        "ds_read_b128 a[204:207], v238 offset:9216\n"
        "s_waitcnt lgkmcnt(7)\n"
        "v_mfma_f32_16x16x32_bf16 v[4:7], a[252:255], a[4:7], v[4:7]\n"
        "global_load_lds_dwordx4 v254, s[60:61] offset:3072 sc1\n"
        "s_add_u32 m0, s73, 0x10000\n"
        "v_mfma_f32_16x16x32_bf16 v[4:7], a[244:247], a[0:3], v[4:7]\n"
        "ds_read_b128 a[208:211], v238 offset:2048\n"
        "s_add_u32 s60, s22, s66\n"
        "s_addc_u32 s61, s23, 0\n"
        "v_mfma_f32_16x16x32_bf16 v[12:15], a[244:247], a[8:11], v[12:15]\n"
        "ds_read_b128 a[212:215], v238 offset:10240\n"
        "s_xor_b32 s66, s17, 10\n"
        "s_lshl_b32 s66, s66, 14\n"
        "v_mfma_f32_16x16x32_bf16 v[12:15], a[252:255], a[12:15], v[12:15]\n"
        "ds_read_b128 a[216:219], v238 offset:3072\n"
        "s_waitcnt lgkmcnt(6)\n"
        "v_mfma_f32_16x16x32_bf16 v[8:11], a[248:251], a[12:15], v[8:11]\n"
        "ds_read_b128 a[220:223], v238 offset:11264\n"
        "v_mfma_f32_16x16x32_bf16 v[8:11], a[240:243], a[8:11], v[8:11]\n"
        "s_waitcnt lgkmcnt(6)\n"
        "v_mfma_f32_16x16x32_bf16 v[16:19], a[240:243], a[16:19], v[16:19]\n"
        "v_mfma_f32_16x16x32_bf16 v[16:19], a[248:251], a[20:23], v[16:19]\n"
        "s_waitcnt lgkmcnt(4)\n"
        "v_mfma_f32_16x16x32_bf16 v[20:23], a[252:255], a[20:23], v[20:23]\n"
        "v_mfma_f32_16x16x32_bf16 v[20:23], a[244:247], a[16:19], v[20:23]\n"
        "s_waitcnt vmcnt(20)\n"
        "v_mfma_f32_16x16x32_bf16 v[28:31], a[244:247], a[24:27], v[28:31]\n"
        "s_barrier\n"
        "v_mfma_f32_16x16x32_bf16 v[28:31], a[252:255], a[28:31], v[28:31]\n"
        "global_load_lds_dwordx4 v144, s[60:61] offset:0\n"
        "ds_read_b64_tr_b16 a[32:33], v153 offset:16384\n"
        "v_mfma_f32_16x16x32_bf16 v[24:27], a[248:251], a[28:31], v[24:27]\n"
        "ds_read_b64_tr_b16 a[34:35], v153 offset:20480\n"
        "s_waitcnt lgkmcnt(2)\n"
        "v_mfma_f32_16x16x32_bf16 v[24:27], a[240:243], a[24:27], v[24:27]\n"
        "ds_read_b64_tr_b16 a[40:41], v153 offset:24576\n"
        "v_mfma_f32_16x16x32_bf16 v[32:35], a[240:243], a[192:195], v[32:35]\n"
        "ds_read_b64_tr_b16 a[42:43], v153 offset:28672\n"
        "global_load_lds_dwordx4 v145, s[60:61] offset:1024\n"
        "v_mfma_f32_16x16x32_bf16 v[32:35], a[248:251], a[196:199], v[32:35]\n"
        "ds_read_b64_tr_b16 a[36:37], v154 offset:16384\n"
        "v_mfma_f32_16x16x32_bf16 v[36:39], a[252:255], a[196:199], v[36:39]\n"
        "ds_read_b64_tr_b16 a[38:39], v154 offset:20480\n"
        "v_mfma_f32_16x16x32_bf16 v[36:39], a[244:247], a[192:195], v[36:39]\n"
        "ds_read_b64_tr_b16 a[44:45], v154 offset:24576\n"
        "global_load_lds_dwordx4 v146, s[60:61] offset:2048\n"
        "v_mfma_f32_16x16x32_bf16 v[44:47], a[244:247], a[200:203], v[44:47]\n"
        "ds_read_b64_tr_b16 a[46:47], v154 offset:28672\n"
        "v_mfma_f32_16x16x32_bf16 v[44:47], a[252:255], a[204:207], v[44:47]\n"
        "ds_read_b128 a[0:3], v224 offset:32768\n"
        "v_mfma_f32_16x16x32_bf16 v[40:43], a[248:251], a[204:207], v[40:43]\n"
        "ds_read_b128 a[4:7], v224 offset:40960\n"
        "global_load_lds_dwordx4 v147, s[60:61] offset:3072\n"
        "s_add_u32 m0, s73, 0x14000\n"
        "v_mfma_f32_16x16x32_bf16 v[40:43], a[240:243], a[200:203], v[40:43]\n"
        "ds_read_b128 a[8:11], v224 offset:33792\n"
        "s_add_u32 s60, s20, s66\n"
        "s_addc_u32 s61, s21, 0\n"
        "v_mfma_f32_16x16x32_bf16 v[48:51], a[240:243], a[208:211], v[48:51]\n"
        "ds_read_b128 a[12:15], v224 offset:41984\n"
        "s_xor_b32 s66, s17, 11\n"
        "s_lshl_b32 s66, s66, 17\n"
        "v_mfma_f32_16x16x32_bf16 v[48:51], a[248:251], a[212:215], v[48:51]\n"
        "ds_read_b128 a[16:19], v224 offset:34816\n"
        "v_mfma_f32_16x16x32_bf16 v[52:55], a[252:255], a[212:215], v[52:55]\n"
        "ds_read_b128 a[20:23], v224 offset:43008\n"
        "v_mfma_f32_16x16x32_bf16 v[52:55], a[244:247], a[208:211], v[52:55]\n"
        "ds_read_b128 a[24:27], v224 offset:35840\n"
        "v_mfma_f32_16x16x32_bf16 v[60:63], a[244:247], a[216:219], v[60:63]\n"
        "ds_read_b128 a[28:31], v224 offset:44032\n"
        "s_waitcnt lgkmcnt(8)\n"
        "v_mfma_f32_16x16x32_bf16 v[60:63], a[252:255], a[220:223], v[60:63]\n"
        "ds_read_b128 a[192:195], v238 offset:32768\n"
        "s_waitcnt lgkmcnt(8)\n"
        "v_mfma_f32_16x16x32_bf16 v[56:59], a[248:251], a[220:223], v[56:59]\n"
        "ds_read_b128 a[196:199], v238 offset:40960\n"
        "s_waitcnt lgkmcnt(8)\n"
        "v_mfma_f32_16x16x32_bf16 v[56:59], a[240:243], a[216:219], v[56:59]\n"
        "ds_read_b128 a[200:203], v238 offset:33792\n"
        "s_waitcnt lgkmcnt(8)\n"
        "v_mfma_f32_16x16x32_bf16 v[0:3], a[32:35], a[0:3], v[0:3]\n"
        "ds_read_b128 a[204:207], v238 offset:41984\n"
        "s_waitcnt lgkmcnt(8)\n"
        "v_mfma_f32_16x16x32_bf16 v[0:3], a[40:43], a[4:7], v[0:3]\n"
        "ds_read_b128 a[208:211], v238 offset:34816\n"
        "s_waitcnt lgkmcnt(8)\n"
        "v_mfma_f32_16x16x32_bf16 v[4:7], a[44:47], a[4:7], v[4:7]\n"
        "ds_read_b128 a[212:215], v238 offset:43008\n"
        "s_waitcnt lgkmcnt(8)\n"
        "v_mfma_f32_16x16x32_bf16 v[4:7], a[36:39], a[0:3], v[4:7]\n"
        "ds_read_b128 a[216:219], v238 offset:35840\n"
        "s_waitcnt lgkmcnt(8)\n"
        "v_mfma_f32_16x16x32_bf16 v[12:15], a[36:39], a[8:11], v[12:15]\n"
        "ds_read_b128 a[220:223], v238 offset:44032\n"
        "s_waitcnt lgkmcnt(8)\n"
        "v_mfma_f32_16x16x32_bf16 v[12:15], a[44:47], a[12:15], v[12:15]\n"
        "s_waitcnt lgkmcnt(7)\n"
        "v_mfma_f32_16x16x32_bf16 v[8:11], a[40:43], a[12:15], v[8:11]\n"
        "v_mfma_f32_16x16x32_bf16 v[8:11], a[32:35], a[8:11], v[8:11]\n"
        "s_waitcnt lgkmcnt(5)\n"
        "v_mfma_f32_16x16x32_bf16 v[16:19], a[32:35], a[16:19], v[16:19]\n"
        "v_mfma_f32_16x16x32_bf16 v[16:19], a[40:43], a[20:23], v[16:19]\n"
        "v_mfma_f32_16x16x32_bf16 v[20:23], a[44:47], a[20:23], v[20:23]\n"
        "v_mfma_f32_16x16x32_bf16 v[20:23], a[36:39], a[16:19], v[20:23]\n"
        "s_waitcnt vmcnt(16)\n"
        "s_waitcnt lgkmcnt(1)\n"
        "v_mfma_f32_16x16x32_bf16 v[28:31], a[36:39], a[24:27], v[28:31]\n"
        "s_barrier\n"
        "v_mfma_f32_16x16x32_bf16 v[28:31], a[44:47], a[28:31], v[28:31]\n"
        "global_load_lds_dwordx4 v254, s[60:61] offset:0 sc1\n"
        "ds_read_b64_tr_b16 a[48:49], v153 offset:49152\n"
        "v_mfma_f32_16x16x32_bf16 v[24:27], a[40:43], a[28:31], v[24:27]\n"
        "ds_read_b64_tr_b16 a[50:51], v153 offset:53248\n"
        "v_mfma_f32_16x16x32_bf16 v[24:27], a[32:35], a[24:27], v[24:27]\n"
        "ds_read_b64_tr_b16 a[56:57], v153 offset:57344\n"
        "v_mfma_f32_16x16x32_bf16 v[32:35], a[32:35], a[192:195], v[32:35]\n"
        "ds_read_b64_tr_b16 a[58:59], v153 offset:61440\n"
        "global_load_lds_dwordx4 v254, s[60:61] offset:1024 sc1\n"
        "v_mfma_f32_16x16x32_bf16 v[32:35], a[40:43], a[196:199], v[32:35]\n"
        "ds_read_b64_tr_b16 a[52:53], v154 offset:49152\n"
        "v_mfma_f32_16x16x32_bf16 v[36:39], a[44:47], a[196:199], v[36:39]\n"
        "ds_read_b64_tr_b16 a[54:55], v154 offset:53248\n"
        "v_mfma_f32_16x16x32_bf16 v[36:39], a[36:39], a[192:195], v[36:39]\n"
        "ds_read_b64_tr_b16 a[60:61], v154 offset:57344\n"
        "global_load_lds_dwordx4 v254, s[60:61] offset:2048 sc1\n"
        "v_mfma_f32_16x16x32_bf16 v[44:47], a[36:39], a[200:203], v[44:47]\n"
        "ds_read_b64_tr_b16 a[62:63], v154 offset:61440\n"
        "s_waitcnt lgkmcnt(8)\n"
        "v_mfma_f32_16x16x32_bf16 v[44:47], a[44:47], a[204:207], v[44:47]\n"
        "ds_read_b128 a[0:3], v225 offset:32768\n"
        "v_mfma_f32_16x16x32_bf16 v[40:43], a[40:43], a[204:207], v[40:43]\n"
        "ds_read_b128 a[4:7], v225 offset:40960\n"
        "global_load_lds_dwordx4 v254, s[60:61] offset:3072 sc1\n"
        "s_add_u32 m0, s73, 0x0\n"
        "v_mfma_f32_16x16x32_bf16 v[40:43], a[32:35], a[200:203], v[40:43]\n"
        "ds_read_b128 a[8:11], v225 offset:33792\n"
        "s_add_u32 s60, s22, s66\n"
        "s_addc_u32 s61, s23, 0\n"
        "v_mfma_f32_16x16x32_bf16 v[48:51], a[32:35], a[208:211], v[48:51]\n"
        "ds_read_b128 a[12:15], v225 offset:41984\n"
        "s_xor_b32 s66, s17, 11\n"
        "s_lshl_b32 s66, s66, 14\n"
        "v_mfma_f32_16x16x32_bf16 v[48:51], a[40:43], a[212:215], v[48:51]\n"
        "ds_read_b128 a[16:19], v225 offset:34816\n"
        "global_load_lds_dwordx4 v144, s[60:61] offset:0\n"
        "v_mfma_f32_16x16x32_bf16 v[52:55], a[44:47], a[212:215], v[52:55]\n"
        "ds_read_b128 a[20:23], v225 offset:43008\n"
        "v_mfma_f32_16x16x32_bf16 v[52:55], a[36:39], a[208:211], v[52:55]\n"
        "ds_read_b128 a[24:27], v225 offset:35840\n"
        "LHB1_%=:\n"
        "s_cbranch_execz LHRB_%=\n"
        "v_mfma_f32_16x16x32_bf16 v[60:63], a[36:39], a[216:219], v[60:63]\n"
        "ds_read_b128 a[28:31], v225 offset:44032\n"
        "global_load_lds_dwordx4 v145, s[60:61] offset:1024\n"
        "v_mfma_f32_16x16x32_bf16 v[60:63], a[44:47], a[220:223], v[60:63]\n"
        "ds_read_b128 a[192:195], v239 offset:32768\n"
        "s_waitcnt lgkmcnt(8)\n"
        "v_mfma_f32_16x16x32_bf16 v[56:59], a[40:43], a[220:223], v[56:59]\n"
        "ds_read_b128 a[196:199], v239 offset:40960\n"
        "s_waitcnt lgkmcnt(8)\n"
        "v_mfma_f32_16x16x32_bf16 v[56:59], a[32:35], a[216:219], v[56:59]\n"
        "global_load_lds_dwordx4 v146, s[60:61] offset:2048\n"
        "v_mfma_f32_16x16x32_bf16 v[0:3], a[48:51], a[0:3], v[0:3]\n"
        "ds_read_b128 a[200:203], v239 offset:33792\n"
        "s_waitcnt lgkmcnt(7)\n"
        "v_mfma_f32_16x16x32_bf16 v[0:3], a[56:59], a[4:7], v[0:3]\n"
        "ds_read_b128 a[204:207], v239 offset:41984\n"
        "s_waitcnt lgkmcnt(7)\n"
        "v_mfma_f32_16x16x32_bf16 v[4:7], a[60:63], a[4:7], v[4:7]\n"
        "global_load_lds_dwordx4 v147, s[60:61] offset:3072\n"
        "s_add_u32 m0, s73, 0x4000\n"
        "v_mfma_f32_16x16x32_bf16 v[4:7], a[52:55], a[0:3], v[4:7]\n"
        "ds_read_b128 a[208:211], v239 offset:34816\n"
        "s_add_u32 s60, s20, s66\n"
        "s_addc_u32 s61, s21, 0\n"
        "v_mfma_f32_16x16x32_bf16 v[12:15], a[52:55], a[8:11], v[12:15]\n"
        "ds_read_b128 a[212:215], v239 offset:43008\n"
        "s_xor_b32 s66, s17, 12\n"
        "s_lshl_b32 s66, s66, 17\n"
        "v_mfma_f32_16x16x32_bf16 v[12:15], a[60:63], a[12:15], v[12:15]\n"
        "ds_read_b128 a[216:219], v239 offset:35840\n"
        "s_waitcnt lgkmcnt(6)\n"
        "v_mfma_f32_16x16x32_bf16 v[8:11], a[56:59], a[12:15], v[8:11]\n"
        "ds_read_b128 a[220:223], v239 offset:44032\n"
        "v_mfma_f32_16x16x32_bf16 v[8:11], a[48:51], a[8:11], v[8:11]\n"
        "s_waitcnt lgkmcnt(6)\n"
        "v_mfma_f32_16x16x32_bf16 v[16:19], a[48:51], a[16:19], v[16:19]\n"
        "v_mfma_f32_16x16x32_bf16 v[16:19], a[56:59], a[20:23], v[16:19]\n"
        "s_waitcnt lgkmcnt(4)\n"
        "v_mfma_f32_16x16x32_bf16 v[20:23], a[60:63], a[20:23], v[20:23]\n"
        "v_mfma_f32_16x16x32_bf16 v[20:23], a[52:55], a[16:19], v[20:23]\n"
        "s_waitcnt vmcnt(16)\n"
        "v_mfma_f32_16x16x32_bf16 v[28:31], a[52:55], a[24:27], v[28:31]\n"
        "s_barrier\n"
        "v_mfma_f32_16x16x32_bf16 v[28:31], a[60:63], a[28:31], v[28:31]\n"
        "global_load_lds_dwordx4 v254, s[60:61] offset:0 sc1\n"
        "ds_read_b64_tr_b16 a[224:225], v155 offset:49152\n"
        "v_mfma_f32_16x16x32_bf16 v[24:27], a[56:59], a[28:31], v[24:27]\n"
        "ds_read_b64_tr_b16 a[226:227], v155 offset:53248\n"
        "s_waitcnt lgkmcnt(2)\n"
        "v_mfma_f32_16x16x32_bf16 v[24:27], a[48:51], a[24:27], v[24:27]\n"
        "ds_read_b64_tr_b16 a[232:233], v155 offset:57344\n"
        "v_mfma_f32_16x16x32_bf16 v[32:35], a[48:51], a[192:195], v[32:35]\n"
        "ds_read_b64_tr_b16 a[234:235], v155 offset:61440\n"
        "global_load_lds_dwordx4 v254, s[60:61] offset:1024 sc1\n"
        "v_mfma_f32_16x16x32_bf16 v[32:35], a[56:59], a[196:199], v[32:35]\n"
        "ds_read_b64_tr_b16 a[228:229], v156 offset:49152\n"
        "v_mfma_f32_16x16x32_bf16 v[36:39], a[60:63], a[196:199], v[36:39]\n"
        "ds_read_b64_tr_b16 a[230:231], v156 offset:53248\n"
        "v_mfma_f32_16x16x32_bf16 v[36:39], a[52:55], a[192:195], v[36:39]\n"
        "ds_read_b64_tr_b16 a[236:237], v156 offset:57344\n"
        "global_load_lds_dwordx4 v254, s[60:61] offset:2048 sc1\n"
        "v_mfma_f32_16x16x32_bf16 v[44:47], a[52:55], a[200:203], v[44:47]\n"
        "ds_read_b64_tr_b16 a[238:239], v156 offset:61440\n"
        "v_mfma_f32_16x16x32_bf16 v[44:47], a[60:63], a[204:207], v[44:47]\n"
        "ds_read_b128 a[0:3], v226 offset:0\n"
        "v_mfma_f32_16x16x32_bf16 v[40:43], a[56:59], a[204:207], v[40:43]\n"
        "ds_read_b128 a[4:7], v226 offset:8192\n"
        "global_load_lds_dwordx4 v254, s[60:61] offset:3072 sc1\n"
        "s_add_u32 m0, s73, 0x8000\n"
        "v_mfma_f32_16x16x32_bf16 v[40:43], a[48:51], a[200:203], v[40:43]\n"
        "ds_read_b128 a[8:11], v226 offset:1024\n"
        "s_add_u32 s60, s22, s66\n"
        "s_addc_u32 s61, s23, 0\n"
        "v_mfma_f32_16x16x32_bf16 v[48:51], a[48:51], a[208:211], v[48:51]\n"
        "ds_read_b128 a[12:15], v226 offset:9216\n"
        "s_xor_b32 s66, s17, 12\n"
        "s_lshl_b32 s66, s66, 14\n"
        "v_mfma_f32_16x16x32_bf16 v[48:51], a[56:59], a[212:215], v[48:51]\n"
        "ds_read_b128 a[16:19], v226 offset:2048\n"
        "global_load_lds_dwordx4 v144, s[60:61] offset:0\n"
        "v_mfma_f32_16x16x32_bf16 v[52:55], a[60:63], a[212:215], v[52:55]\n"
        "ds_read_b128 a[20:23], v226 offset:10240\n"
        "v_mfma_f32_16x16x32_bf16 v[52:55], a[52:55], a[208:211], v[52:55]\n"
        "ds_read_b128 a[24:27], v226 offset:3072\n"
        "v_mfma_f32_16x16x32_bf16 v[60:63], a[52:55], a[216:219], v[60:63]\n"
        "ds_read_b128 a[28:31], v226 offset:11264\n"
        "global_load_lds_dwordx4 v145, s[60:61] offset:1024\n"
        "v_mfma_f32_16x16x32_bf16 v[60:63], a[60:63], a[220:223], v[60:63]\n"
        "ds_read_b128 a[192:195], v240 offset:0\n"
        "s_waitcnt lgkmcnt(8)\n"
        "v_mfma_f32_16x16x32_bf16 v[56:59], a[56:59], a[220:223], v[56:59]\n"
        "ds_read_b128 a[196:199], v240 offset:8192\n"
        "s_waitcnt lgkmcnt(8)\n"
        "v_mfma_f32_16x16x32_bf16 v[56:59], a[48:51], a[216:219], v[56:59]\n"
        "global_load_lds_dwordx4 v146, s[60:61] offset:2048\n"
        "v_mfma_f32_16x16x32_bf16 v[0:3], a[224:227], a[0:3], v[0:3]\n"
        "ds_read_b128 a[200:203], v240 offset:1024\n"
        "s_waitcnt lgkmcnt(7)\n"
        "v_mfma_f32_16x16x32_bf16 v[0:3], a[232:235], a[4:7], v[0:3]\n"
        "ds_read_b128 a[204:207], v240 offset:9216\n"
        "s_waitcnt lgkmcnt(7)\n"
        "v_mfma_f32_16x16x32_bf16 v[4:7], a[236:239], a[4:7], v[4:7]\n"
        "global_load_lds_dwordx4 v147, s[60:61] offset:3072\n"
        "s_add_u32 m0, s73, 0xc000\n"
        "v_mfma_f32_16x16x32_bf16 v[4:7], a[228:231], a[0:3], v[4:7]\n"
        "ds_read_b128 a[208:211], v240 offset:2048\n"
        "s_add_u32 s60, s20, s66\n"
        "s_addc_u32 s61, s21, 0\n"
        "v_mfma_f32_16x16x32_bf16 v[12:15], a[228:231], a[8:11], v[12:15]\n"
        "ds_read_b128 a[212:215], v240 offset:10240\n"
        "s_xor_b32 s66, s17, 13\n"
        "s_lshl_b32 s66, s66, 17\n"
        "v_mfma_f32_16x16x32_bf16 v[12:15], a[236:239], a[12:15], v[12:15]\n"
        "ds_read_b128 a[216:219], v240 offset:3072\n"
        "s_waitcnt lgkmcnt(6)\n"
        "v_mfma_f32_16x16x32_bf16 v[8:11], a[232:235], a[12:15], v[8:11]\n"
        "ds_read_b128 a[220:223], v240 offset:11264\n"
        "v_mfma_f32_16x16x32_bf16 v[8:11], a[224:227], a[8:11], v[8:11]\n"
        "s_waitcnt lgkmcnt(6)\n"
        "v_mfma_f32_16x16x32_bf16 v[16:19], a[224:227], a[16:19], v[16:19]\n"
        "v_mfma_f32_16x16x32_bf16 v[16:19], a[232:235], a[20:23], v[16:19]\n"
        "s_waitcnt lgkmcnt(4)\n"
        "v_mfma_f32_16x16x32_bf16 v[20:23], a[236:239], a[20:23], v[20:23]\n"
        "v_mfma_f32_16x16x32_bf16 v[20:23], a[228:231], a[16:19], v[20:23]\n"
        "s_waitcnt vmcnt(20)\n"
        "v_mfma_f32_16x16x32_bf16 v[28:31], a[228:231], a[24:27], v[28:31]\n"
        "s_barrier\n"
        "v_mfma_f32_16x16x32_bf16 v[28:31], a[236:239], a[28:31], v[28:31]\n"
        "global_load_lds_dwordx4 v254, s[60:61] offset:0 sc1\n"
        "ds_read_b128 a[0:3], v226 offset:16384\n"
        "v_mfma_f32_16x16x32_bf16 v[24:27], a[232:235], a[28:31], v[24:27]\n"
        "ds_read_b128 a[4:7], v226 offset:24576\n"
        "s_waitcnt lgkmcnt(2)\n"
        "v_mfma_f32_16x16x32_bf16 v[24:27], a[224:227], a[24:27], v[24:27]\n"
        "ds_read_b128 a[8:11], v226 offset:17408\n"
        "v_mfma_f32_16x16x32_bf16 v[32:35], a[224:227], a[192:195], v[32:35]\n"
        "global_load_lds_dwordx4 v254, s[60:61] offset:1024 sc1\n"
        "ds_read_b128 a[12:15], v226 offset:25600\n"
        "v_mfma_f32_16x16x32_bf16 v[32:35], a[232:235], a[196:199], v[32:35]\n"
        "ds_read_b128 a[16:19], v226 offset:18432\n"
        "v_mfma_f32_16x16x32_bf16 v[36:39], a[236:239], a[196:199], v[36:39]\n"
        "ds_read_b128 a[20:23], v226 offset:26624\n"
        "v_mfma_f32_16x16x32_bf16 v[36:39], a[228:231], a[192:195], v[36:39]\n"
        "global_load_lds_dwordx4 v254, s[60:61] offset:2048 sc1\n"
        "ds_read_b128 a[24:27], v226 offset:19456\n"
        "v_mfma_f32_16x16x32_bf16 v[44:47], a[228:231], a[200:203], v[44:47]\n"
        "ds_read_b128 a[28:31], v226 offset:27648\n"
        "v_mfma_f32_16x16x32_bf16 v[44:47], a[236:239], a[204:207], v[44:47]\n"
        "ds_read_b128 a[192:195], v240 offset:16384\n"
        "s_waitcnt lgkmcnt(8)\n"
        "v_mfma_f32_16x16x32_bf16 v[40:43], a[232:235], a[204:207], v[40:43]\n"
        "global_load_lds_dwordx4 v254, s[60:61] offset:3072 sc1\n"
        "s_add_u32 m0, s73, 0x18000\n"
        "v_mfma_f32_16x16x32_bf16 v[40:43], a[224:227], a[200:203], v[40:43]\n"
        "s_add_u32 s60, s22, s66\n"
        "s_addc_u32 s61, s23, 0\n"
        "s_xor_b32 s66, s17, 13\n"
        "v_mfma_f32_16x16x32_bf16 v[48:51], a[224:227], a[208:211], v[48:51]\n"
        "ds_read_b128 a[196:199], v240 offset:24576\n"
        "s_lshl_b32 s66, s66, 14\n"
        "s_waitcnt lgkmcnt(6)\n"
        "v_mfma_f32_16x16x32_bf16 v[48:51], a[232:235], a[212:215], v[48:51]\n"
        "global_load_lds_dwordx4 v144, s[60:61] offset:0\n"
        "ds_read_b128 a[200:203], v240 offset:17408\n"
        "v_mfma_f32_16x16x32_bf16 v[52:55], a[236:239], a[212:215], v[52:55]\n"
        "ds_read_b128 a[204:207], v240 offset:25600\n"
        "v_mfma_f32_16x16x32_bf16 v[52:55], a[228:231], a[208:211], v[52:55]\n"
        "ds_read_b128 a[208:211], v240 offset:18432\n"
        "v_mfma_f32_16x16x32_bf16 v[60:63], a[228:231], a[216:219], v[60:63]\n"
        "global_load_lds_dwordx4 v145, s[60:61] offset:1024\n"
        "ds_read_b128 a[212:215], v240 offset:26624\n"
        "v_mfma_f32_16x16x32_bf16 v[60:63], a[236:239], a[220:223], v[60:63]\n"
        "s_waitcnt lgkmcnt(5)\n"
        "v_mfma_f32_16x16x32_bf16 v[56:59], a[232:235], a[220:223], v[56:59]\n"
        "ds_read_b128 a[220:223], v240 offset:27648\n"
        "v_mfma_f32_16x16x32_bf16 v[56:59], a[224:227], a[216:219], v[56:59]\n"
        "global_load_lds_dwordx4 v146, s[60:61] offset:2048\n"
        "ds_read_b128 a[216:219], v240 offset:19456\n"
        "v_mfma_f32_16x16x32_bf16 v[0:3], v[192:195], a[0:3], v[0:3]\n"
        "v_mfma_f32_16x16x32_bf16 v[0:3], v[200:203], a[4:7], v[0:3]\n"
        "v_mfma_f32_16x16x32_bf16 v[4:7], v[204:207], a[4:7], v[4:7]\n"
        "global_load_lds_dwordx4 v147, s[60:61] offset:3072\n"
        "s_add_u32 m0, s73, 0x1c000\n"
        "v_mfma_f32_16x16x32_bf16 v[4:7], v[196:199], a[0:3], v[4:7]\n"
        "s_add_u32 s60, s20, s66\n"
        "s_addc_u32 s61, s21, 0\n"
        "s_xor_b32 s66, s17, 14\n"
        "v_mfma_f32_16x16x32_bf16 v[12:15], v[196:199], a[8:11], v[12:15]\n"
        "s_lshl_b32 s66, s66, 14\n"
        "v_mfma_f32_16x16x32_bf16 v[12:15], v[204:207], a[12:15], v[12:15]\n"
        "v_mfma_f32_16x16x32_bf16 v[8:11], v[200:203], a[12:15], v[8:11]\n"
        "v_mfma_f32_16x16x32_bf16 v[8:11], v[192:195], a[8:11], v[8:11]\n"
        "s_waitcnt lgkmcnt(1)\n"
        "v_mfma_f32_16x16x32_bf16 v[16:19], v[192:195], a[16:19], v[16:19]\n"
        "v_mfma_f32_16x16x32_bf16 v[16:19], v[200:203], a[20:23], v[16:19]\n"
        "v_mfma_f32_16x16x32_bf16 v[20:23], v[204:207], a[20:23], v[20:23]\n"
        "v_mfma_f32_16x16x32_bf16 v[20:23], v[196:199], a[16:19], v[20:23]\n"
        "s_waitcnt vmcnt(20)\n"
        "v_mfma_f32_16x16x32_bf16 v[28:31], v[196:199], a[24:27], v[28:31]\n"
        "s_barrier\n"
        "v_mfma_f32_16x16x32_bf16 v[28:31], v[204:207], a[28:31], v[28:31]\n"
        "global_load_lds_dwordx4 v254, s[60:61] offset:0 sc1\n"
        "ds_read_b64_tr_b16 a[240:241], v155 offset:0\n"
        "v_mfma_f32_16x16x32_bf16 v[24:27], v[200:203], a[28:31], v[24:27]\n"
        "ds_read_b64_tr_b16 a[242:243], v155 offset:4096\n"
        "v_mfma_f32_16x16x32_bf16 v[24:27], v[192:195], a[24:27], v[24:27]\n"
        "ds_read_b64_tr_b16 a[248:249], v155 offset:8192\n"
        "v_mfma_f32_16x16x32_bf16 v[32:35], v[192:195], a[192:195], v[32:35]\n"
        "ds_read_b64_tr_b16 a[250:251], v155 offset:12288\n"
        "global_load_lds_dwordx4 v254, s[60:61] offset:1024 sc1\n"
        "v_mfma_f32_16x16x32_bf16 v[32:35], v[200:203], a[196:199], v[32:35]\n"
        "ds_read_b64_tr_b16 a[244:245], v156 offset:0\n"
        "s_waitcnt lgkmcnt(5)\n"
        "v_mfma_f32_16x16x32_bf16 v[36:39], v[204:207], a[196:199], v[36:39]\n"
        "ds_read_b64_tr_b16 a[246:247], v156 offset:4096\n"
        "v_mfma_f32_16x16x32_bf16 v[36:39], v[196:199], a[192:195], v[36:39]\n"
        "ds_read_b64_tr_b16 a[252:253], v156 offset:8192\n"
        "global_load_lds_dwordx4 v254, s[60:61] offset:2048 sc1\n"
        "v_mfma_f32_16x16x32_bf16 v[44:47], v[196:199], a[200:203], v[44:47]\n"
        "ds_read_b64_tr_b16 a[254:255], v156 offset:12288\n"
        "v_mfma_f32_16x16x32_bf16 v[44:47], v[204:207], a[204:207], v[44:47]\n"
        "ds_read_b128 a[0:3], v225 offset:16384\n"
        "v_mfma_f32_16x16x32_bf16 v[40:43], v[200:203], a[204:207], v[40:43]\n"
        "ds_read_b128 a[4:7], v225 offset:24576\n"
        "global_load_lds_dwordx4 v254, s[60:61] offset:3072 sc1\n"
        "s_add_u32 m0, s73, 0x20000\n"
        "v_mfma_f32_16x16x32_bf16 v[40:43], v[192:195], a[200:203], v[40:43]\n"
        "ds_read_b128 a[8:11], v225 offset:17408\n"
        "s_add_u32 s60, s20, s66\n"
        "s_addc_u32 s61, s21, 0\n"
        "v_mfma_f32_16x16x32_bf16 v[48:51], v[192:195], a[208:211], v[48:51]\n"
        "ds_read_b128 a[12:15], v225 offset:25600\n"
        "s_xor_b32 s66, s17, 15\n"
        "s_lshl_b32 s66, s66, 17\n"
        "v_mfma_f32_16x16x32_bf16 v[48:51], v[200:203], a[212:215], v[48:51]\n"
        "ds_read_b128 a[16:19], v225 offset:18432\n"
        "global_load_lds_dwordx4 v254, s[60:61] offset:0 sc1\n"
        "v_mfma_f32_16x16x32_bf16 v[52:55], v[204:207], a[212:215], v[52:55]\n"
        "ds_read_b128 a[20:23], v225 offset:26624\n"
        "v_mfma_f32_16x16x32_bf16 v[52:55], v[196:199], a[208:211], v[52:55]\n"
        "ds_read_b128 a[24:27], v225 offset:19456\n"
        "v_mfma_f32_16x16x32_bf16 v[60:63], v[196:199], a[216:219], v[60:63]\n"
        "ds_read_b128 a[28:31], v225 offset:27648\n"
        "global_load_lds_dwordx4 v254, s[60:61] offset:1024 sc1\n"
        "v_mfma_f32_16x16x32_bf16 v[60:63], v[204:207], a[220:223], v[60:63]\n"
        "ds_read_b128 a[192:195], v239 offset:16384\n"
        "s_waitcnt lgkmcnt(8)\n"
        "v_mfma_f32_16x16x32_bf16 v[56:59], v[200:203], a[220:223], v[56:59]\n"
        "ds_read_b128 a[196:199], v239 offset:24576\n"
        "s_waitcnt lgkmcnt(8)\n"
        "v_mfma_f32_16x16x32_bf16 v[56:59], v[192:195], a[216:219], v[56:59]\n"
        "global_load_lds_dwordx4 v254, s[60:61] offset:2048 sc1\n"
        "v_mfma_f32_16x16x32_bf16 v[0:3], a[240:243], a[0:3], v[0:3]\n"
        "ds_read_b128 a[200:203], v239 offset:17408\n"
        "s_waitcnt lgkmcnt(7)\n"
        "v_mfma_f32_16x16x32_bf16 v[0:3], a[248:251], a[4:7], v[0:3]\n"
        "ds_read_b128 a[204:207], v239 offset:25600\n"
        "s_waitcnt lgkmcnt(7)\n"
        "v_mfma_f32_16x16x32_bf16 v[4:7], a[252:255], a[4:7], v[4:7]\n"
        "global_load_lds_dwordx4 v254, s[60:61] offset:3072 sc1\n"
        "s_add_u32 m0, s73, 0x24000\n"
        "v_mfma_f32_16x16x32_bf16 v[4:7], a[244:247], a[0:3], v[4:7]\n"
        "ds_read_b128 a[208:211], v239 offset:18432\n"
        "s_add_u32 s60, s22, s66\n"
        "s_addc_u32 s61, s23, 0\n"
        "v_mfma_f32_16x16x32_bf16 v[12:15], a[244:247], a[8:11], v[12:15]\n"
        "ds_read_b128 a[212:215], v239 offset:26624\n"
        "s_xor_b32 s66, s17, 15\n"
        "s_lshl_b32 s66, s66, 14\n"
        "v_mfma_f32_16x16x32_bf16 v[12:15], a[252:255], a[12:15], v[12:15]\n"
        "ds_read_b128 a[216:219], v239 offset:19456\n"
        "s_waitcnt lgkmcnt(6)\n"
        "v_mfma_f32_16x16x32_bf16 v[8:11], a[248:251], a[12:15], v[8:11]\n"
        "s_cmp_lg_u32 s40, 0\n"
        "s_cbranch_scc1 LW2_%=\n"
        "s_mov_b64 exec, 0xffff\n"
        "global_store_dwordx2 v234, v[250:251], s[48:49] sc1\n"
        "s_mov_b64 exec, -1\n"
        "LW2_%=:\n"
        "v_mfma_f32_16x16x32_bf16 v[8:11], a[240:243], a[8:11], v[8:11]\n"
        "ds_read_b128 a[220:223], v239 offset:27648\n"
        "v_bfe_u32 v234, v254, 4, 3\n"
        "v_mfma_f32_16x16x32_bf16 v[16:19], a[240:243], a[16:19], v[16:19]\n"
        "s_waitcnt lgkmcnt(5)\n"
        "v_lshl_add_u32 v234, v234, 7, s14\n"
        "s_and_b32 s14, s40, 1\n"
        "v_mfma_f32_16x16x32_bf16 v[16:19], a[248:251], a[20:23], v[16:19]\n"
        "s_lshl1_add_u32 s14, s14, 5\n"
        "s_sub_u32 s14, s14, s10\n"
        "s_sub_u32 s14, s14, s40\n"
        "v_mfma_f32_16x16x32_bf16 v[20:23], a[252:255], a[20:23], v[20:23]\n"
        "s_lshl_b32 s14, s14, 12\n"
        "v_mfma_f32_16x16x32_bf16 v[20:23], a[244:247], a[16:19], v[20:23]\n"
        "s_waitcnt vmcnt(20)\n"
        "s_waitcnt lgkmcnt(2)\n"
        "v_mfma_f32_16x16x32_bf16 v[28:31], a[244:247], a[24:27], v[28:31]\n"
        "s_barrier\n"
        "v_mfma_f32_16x16x32_bf16 v[28:31], a[252:255], a[28:31], v[28:31]\n"
        "global_load_lds_dwordx4 v144, s[60:61] offset:0\n"
        "ds_read_b64_tr_b16 a[32:33], v153 offset:0\n"
        "v_mfma_f32_16x16x32_bf16 v[24:27], a[248:251], a[28:31], v[24:27]\n"
        "ds_read_b64_tr_b16 a[34:35], v153 offset:4096\n"
        "v_mfma_f32_16x16x32_bf16 v[24:27], a[240:243], a[24:27], v[24:27]\n"
        "ds_read_b64_tr_b16 a[40:41], v153 offset:8192\n"
        "v_mfma_f32_16x16x32_bf16 v[32:35], a[240:243], a[192:195], v[32:35]\n"
        "ds_read_b64_tr_b16 a[42:43], v153 offset:12288\n"
        "global_load_lds_dwordx4 v145, s[60:61] offset:1024\n"
        "v_mfma_f32_16x16x32_bf16 v[32:35], a[248:251], a[196:199], v[32:35]\n"
        "ds_read_b64_tr_b16 a[36:37], v154 offset:0\n"
        "s_waitcnt lgkmcnt(5)\n"
        "v_mfma_f32_16x16x32_bf16 v[36:39], a[252:255], a[196:199], v[36:39]\n"
        "ds_read_b64_tr_b16 a[38:39], v154 offset:4096\n"
        "v_mfma_f32_16x16x32_bf16 v[36:39], a[244:247], a[192:195], v[36:39]\n"
        "ds_read_b64_tr_b16 a[44:45], v154 offset:8192\n"
        "global_load_lds_dwordx4 v146, s[60:61] offset:2048\n"
        "v_mfma_f32_16x16x32_bf16 v[44:47], a[244:247], a[200:203], v[44:47]\n"
        "ds_read_b64_tr_b16 a[46:47], v154 offset:12288\n"
        "v_mfma_f32_16x16x32_bf16 v[44:47], a[252:255], a[204:207], v[44:47]\n"
        "ds_read_b128 a[0:3], v224 offset:16384\n"
        "v_mfma_f32_16x16x32_bf16 v[40:43], a[248:251], a[204:207], v[40:43]\n"
        "ds_read_b128 a[4:7], v224 offset:24576\n"
        "global_load_lds_dwordx4 v147, s[60:61] offset:3072\n"
        "s_add_u32 m0, s73, 0x10000\n"
        "v_mfma_f32_16x16x32_bf16 v[40:43], a[240:243], a[200:203], v[40:43]\n"
        "ds_read_b128 a[8:11], v224 offset:17408\n"
        "s_add_u32 s60, s20, s66\n"
        "s_addc_u32 s61, s21, 0\n"
        "v_mfma_f32_16x16x32_bf16 v[48:51], a[240:243], a[208:211], v[48:51]\n"
        "ds_read_b128 a[12:15], v224 offset:25600\n"
        "s_xor_b32 s66, s17, 16\n"
        "s_lshl_b32 s66, s66, 17\n"
        "v_mfma_f32_16x16x32_bf16 v[48:51], a[248:251], a[212:215], v[48:51]\n"
        "ds_read_b128 a[16:19], v224 offset:18432\n"
        "v_mfma_f32_16x16x32_bf16 v[52:55], a[252:255], a[212:215], v[52:55]\n"
        "ds_read_b128 a[20:23], v224 offset:26624\n"
        "v_mfma_f32_16x16x32_bf16 v[52:55], a[244:247], a[208:211], v[52:55]\n"
        "ds_read_b128 a[24:27], v224 offset:19456\n"
        "v_mfma_f32_16x16x32_bf16 v[60:63], a[244:247], a[216:219], v[60:63]\n"
        "ds_read_b128 a[28:31], v224 offset:27648\n"
        "s_waitcnt lgkmcnt(8)\n"
        "v_mfma_f32_16x16x32_bf16 v[60:63], a[252:255], a[220:223], v[60:63]\n"
        "ds_read_b128 a[192:195], v238 offset:16384\n"
        "s_waitcnt lgkmcnt(8)\n"
        "v_mfma_f32_16x16x32_bf16 v[56:59], a[248:251], a[220:223], v[56:59]\n"
        "ds_read_b128 a[196:199], v238 offset:24576\n"
        "s_waitcnt lgkmcnt(8)\n"
        "v_mfma_f32_16x16x32_bf16 v[56:59], a[240:243], a[216:219], v[56:59]\n"
        "ds_read_b128 a[200:203], v238 offset:17408\n"
        "s_waitcnt lgkmcnt(8)\n"
        "v_mfma_f32_16x16x32_bf16 v[0:3], a[32:35], a[0:3], v[0:3]\n"
        "ds_read_b128 a[204:207], v238 offset:25600\n"
        "s_waitcnt lgkmcnt(8)\n"
        "v_mfma_f32_16x16x32_bf16 v[0:3], a[40:43], a[4:7], v[0:3]\n"
        "ds_read_b128 a[208:211], v238 offset:18432\n"
        "s_waitcnt lgkmcnt(8)\n"
        "v_mfma_f32_16x16x32_bf16 v[4:7], a[44:47], a[4:7], v[4:7]\n"
        "ds_read_b128 a[212:215], v238 offset:26624\n"
        "s_waitcnt lgkmcnt(8)\n"
        "v_mfma_f32_16x16x32_bf16 v[4:7], a[36:39], a[0:3], v[4:7]\n"
        "ds_read_b128 a[216:219], v238 offset:19456\n"
        "s_waitcnt lgkmcnt(8)\n"
        "v_mfma_f32_16x16x32_bf16 v[12:15], a[36:39], a[8:11], v[12:15]\n"
        "ds_read_b128 a[220:223], v238 offset:27648\n"
        "s_waitcnt lgkmcnt(8)\n"
        "v_mfma_f32_16x16x32_bf16 v[12:15], a[44:47], a[12:15], v[12:15]\n"
        "s_waitcnt lgkmcnt(7)\n"
        "v_mfma_f32_16x16x32_bf16 v[8:11], a[40:43], a[12:15], v[8:11]\n"
        "v_mfma_f32_16x16x32_bf16 v[8:11], a[32:35], a[8:11], v[8:11]\n"
        "s_waitcnt lgkmcnt(5)\n"
        "v_mfma_f32_16x16x32_bf16 v[16:19], a[32:35], a[16:19], v[16:19]\n"
        "v_mfma_f32_16x16x32_bf16 v[16:19], a[40:43], a[20:23], v[16:19]\n"
        "v_mfma_f32_16x16x32_bf16 v[20:23], a[44:47], a[20:23], v[20:23]\n"
        "v_mfma_f32_16x16x32_bf16 v[20:23], a[36:39], a[16:19], v[20:23]\n"
        "s_waitcnt vmcnt(16)\n"
        "s_waitcnt lgkmcnt(1)\n"
        "v_mfma_f32_16x16x32_bf16 v[28:31], a[36:39], a[24:27], v[28:31]\n"
        "s_barrier\n"
        "v_mfma_f32_16x16x32_bf16 v[28:31], a[44:47], a[28:31], v[28:31]\n"
        "global_load_lds_dwordx4 v254, s[60:61] offset:0 sc1\n"
        "ds_read_b64_tr_b16 a[48:49], v153 offset:32768\n"
        "v_mfma_f32_16x16x32_bf16 v[24:27], a[40:43], a[28:31], v[24:27]\n"
        "ds_read_b64_tr_b16 a[50:51], v153 offset:36864\n"
        "v_mfma_f32_16x16x32_bf16 v[24:27], a[32:35], a[24:27], v[24:27]\n"
        "ds_read_b64_tr_b16 a[56:57], v153 offset:40960\n"
        "v_mfma_f32_16x16x32_bf16 v[32:35], a[32:35], a[192:195], v[32:35]\n"
        "ds_read_b64_tr_b16 a[58:59], v153 offset:45056\n"
        "global_load_lds_dwordx4 v254, s[60:61] offset:1024 sc1\n"
        "v_mfma_f32_16x16x32_bf16 v[32:35], a[40:43], a[196:199], v[32:35]\n"
        "ds_read_b64_tr_b16 a[52:53], v154 offset:32768\n"
        "v_mfma_f32_16x16x32_bf16 v[36:39], a[44:47], a[196:199], v[36:39]\n"
        "ds_read_b64_tr_b16 a[54:55], v154 offset:36864\n"
        "v_mfma_f32_16x16x32_bf16 v[36:39], a[36:39], a[192:195], v[36:39]\n"
        "ds_read_b64_tr_b16 a[60:61], v154 offset:40960\n"
        "global_load_lds_dwordx4 v254, s[60:61] offset:2048 sc1\n"
        "v_mfma_f32_16x16x32_bf16 v[44:47], a[36:39], a[200:203], v[44:47]\n"
        "ds_read_b64_tr_b16 a[62:63], v154 offset:45056\n"
        "s_waitcnt lgkmcnt(8)\n"
        "v_mfma_f32_16x16x32_bf16 v[44:47], a[44:47], a[204:207], v[44:47]\n"
        "ds_read_b128 a[0:3], v224 offset:49152\n"
        "v_mfma_f32_16x16x32_bf16 v[40:43], a[40:43], a[204:207], v[40:43]\n"
        "ds_read_b128 a[4:7], v224 offset:57344\n"
        "global_load_lds_dwordx4 v254, s[60:61] offset:3072 sc1\n"
        "s_add_u32 m0, s73, 0x14000\n"
        "v_mfma_f32_16x16x32_bf16 v[40:43], a[32:35], a[200:203], v[40:43]\n"
        "ds_read_b128 a[8:11], v224 offset:50176\n"
        "s_add_u32 s60, s22, s66\n"
        "s_addc_u32 s61, s23, 0\n"
        "v_mfma_f32_16x16x32_bf16 v[48:51], a[32:35], a[208:211], v[48:51]\n"
        "ds_read_b128 a[12:15], v224 offset:58368\n"
        "s_xor_b32 s66, s17, 16\n"
        "s_lshl_b32 s66, s66, 14\n"
        "v_mfma_f32_16x16x32_bf16 v[48:51], a[40:43], a[212:215], v[48:51]\n"
        "ds_read_b128 a[16:19], v224 offset:51200\n"
        "global_load_lds_dwordx4 v144, s[60:61] offset:0\n"
        "v_mfma_f32_16x16x32_bf16 v[52:55], a[44:47], a[212:215], v[52:55]\n"
        "ds_read_b128 a[20:23], v224 offset:59392\n"
        "v_mfma_f32_16x16x32_bf16 v[52:55], a[36:39], a[208:211], v[52:55]\n"
        "ds_read_b128 a[24:27], v224 offset:52224\n"
        "v_mfma_f32_16x16x32_bf16 v[60:63], a[36:39], a[216:219], v[60:63]\n"
        "ds_read_b128 a[28:31], v224 offset:60416\n"
        "global_load_lds_dwordx4 v145, s[60:61] offset:1024\n"
        "v_mfma_f32_16x16x32_bf16 v[60:63], a[44:47], a[220:223], v[60:63]\n"
        "ds_read_b128 a[192:195], v238 offset:49152\n"
        "s_waitcnt lgkmcnt(8)\n"
        "v_mfma_f32_16x16x32_bf16 v[56:59], a[40:43], a[220:223], v[56:59]\n"
        "ds_read_b128 a[196:199], v238 offset:57344\n"
        "s_waitcnt lgkmcnt(8)\n"
        "v_mfma_f32_16x16x32_bf16 v[56:59], a[32:35], a[216:219], v[56:59]\n"
        "global_load_lds_dwordx4 v146, s[60:61] offset:2048\n"
        "v_mfma_f32_16x16x32_bf16 v[0:3], a[48:51], a[0:3], v[0:3]\n"
        "ds_read_b128 a[200:203], v238 offset:50176\n"
        "s_waitcnt lgkmcnt(7)\n"
        "v_mfma_f32_16x16x32_bf16 v[0:3], a[56:59], a[4:7], v[0:3]\n"
        "ds_read_b128 a[204:207], v238 offset:58368\n"
        "s_waitcnt lgkmcnt(7)\n"
        "v_mfma_f32_16x16x32_bf16 v[4:7], a[60:63], a[4:7], v[4:7]\n"
        "global_load_lds_dwordx4 v147, s[60:61] offset:3072\n"
        "s_add_u32 m0, s73, 0x0\n"
        "v_mfma_f32_16x16x32_bf16 v[4:7], a[52:55], a[0:3], v[4:7]\n"
        "ds_read_b128 a[208:211], v238 offset:51200\n"
        "s_add_u32 s60, s20, s66\n"
        "s_addc_u32 s61, s21, 0\n"
        "v_mfma_f32_16x16x32_bf16 v[12:15], a[52:55], a[8:11], v[12:15]\n"
        "ds_read_b128 a[212:215], v238 offset:59392\n"
        "s_xor_b32 s66, s17, 17\n"
        "s_lshl_b32 s66, s66, 17\n"
        "v_mfma_f32_16x16x32_bf16 v[12:15], a[60:63], a[12:15], v[12:15]\n"
        "ds_read_b128 a[216:219], v238 offset:52224\n"
        "s_waitcnt lgkmcnt(6)\n"
        "v_mfma_f32_16x16x32_bf16 v[8:11], a[56:59], a[12:15], v[8:11]\n"
        "ds_read_b128 a[220:223], v238 offset:60416\n"
        "v_mfma_f32_16x16x32_bf16 v[8:11], a[48:51], a[8:11], v[8:11]\n"
        "s_waitcnt lgkmcnt(6)\n"
        "v_mfma_f32_16x16x32_bf16 v[16:19], a[48:51], a[16:19], v[16:19]\n"
        "v_mfma_f32_16x16x32_bf16 v[16:19], a[56:59], a[20:23], v[16:19]\n"
        "s_waitcnt lgkmcnt(4)\n"
        "v_mfma_f32_16x16x32_bf16 v[20:23], a[60:63], a[20:23], v[20:23]\n"
        "v_mfma_f32_16x16x32_bf16 v[20:23], a[52:55], a[16:19], v[20:23]\n"
        "s_waitcnt vmcnt(16)\n"
        "v_mfma_f32_16x16x32_bf16 v[28:31], a[52:55], a[24:27], v[28:31]\n"
        "s_barrier\n"
        "v_mfma_f32_16x16x32_bf16 v[28:31], a[60:63], a[28:31], v[28:31]\n"
        "global_load_lds_dwordx4 v254, s[60:61] offset:0 sc1\n"
        "ds_read_b64_tr_b16 a[224:225], v155 offset:32768\n"
        "v_mfma_f32_16x16x32_bf16 v[24:27], a[56:59], a[28:31], v[24:27]\n"
        "ds_read_b64_tr_b16 a[226:227], v155 offset:36864\n"
        "s_waitcnt lgkmcnt(2)\n"
        "v_mfma_f32_16x16x32_bf16 v[24:27], a[48:51], a[24:27], v[24:27]\n"
        "ds_read_b64_tr_b16 a[232:233], v155 offset:40960\n"
        "v_mfma_f32_16x16x32_bf16 v[32:35], a[48:51], a[192:195], v[32:35]\n"
        "ds_read_b64_tr_b16 a[234:235], v155 offset:45056\n"
        "global_load_lds_dwordx4 v254, s[60:61] offset:1024 sc1\n"
        "v_mfma_f32_16x16x32_bf16 v[32:35], a[56:59], a[196:199], v[32:35]\n"
        "ds_read_b64_tr_b16 a[228:229], v156 offset:32768\n"
        "v_mfma_f32_16x16x32_bf16 v[36:39], a[60:63], a[196:199], v[36:39]\n"
        "ds_read_b64_tr_b16 a[230:231], v156 offset:36864\n"
        "v_mfma_f32_16x16x32_bf16 v[36:39], a[52:55], a[192:195], v[36:39]\n"
        "ds_read_b64_tr_b16 a[236:237], v156 offset:40960\n"
        "global_load_lds_dwordx4 v254, s[60:61] offset:2048 sc1\n"
        "v_mfma_f32_16x16x32_bf16 v[44:47], a[52:55], a[200:203], v[44:47]\n"
        "ds_read_b64_tr_b16 a[238:239], v156 offset:45056\n"
        "v_mfma_f32_16x16x32_bf16 v[44:47], a[60:63], a[204:207], v[44:47]\n"
        "ds_read_b128 a[0:3], v225 offset:49152\n"
        "v_mfma_f32_16x16x32_bf16 v[40:43], a[56:59], a[204:207], v[40:43]\n"
        "ds_read_b128 a[4:7], v225 offset:57344\n"
        "global_load_lds_dwordx4 v254, s[60:61] offset:3072 sc1\n"
        "s_add_u32 m0, s73, 0x4000\n"
        "v_mfma_f32_16x16x32_bf16 v[40:43], a[48:51], a[200:203], v[40:43]\n"
        "ds_read_b128 a[8:11], v225 offset:50176\n"
        "s_add_u32 s60, s22, s66\n"
        "s_addc_u32 s61, s23, 0\n"
        "v_mfma_f32_16x16x32_bf16 v[48:51], a[48:51], a[208:211], v[48:51]\n"
        "ds_read_b128 a[12:15], v225 offset:58368\n"
        "s_xor_b32 s66, s17, 17\n"
        "s_lshl_b32 s66, s66, 14\n"
        "v_mfma_f32_16x16x32_bf16 v[48:51], a[56:59], a[212:215], v[48:51]\n"
        "ds_read_b128 a[16:19], v225 offset:51200\n"
        "global_load_lds_dwordx4 v144, s[60:61] offset:0\n"
        "v_mfma_f32_16x16x32_bf16 v[52:55], a[60:63], a[212:215], v[52:55]\n"
        "ds_read_b128 a[20:23], v225 offset:59392\n"
        "v_mfma_f32_16x16x32_bf16 v[52:55], a[52:55], a[208:211], v[52:55]\n"
        "ds_read_b128 a[24:27], v225 offset:52224\n"
        "v_mfma_f32_16x16x32_bf16 v[60:63], a[52:55], a[216:219], v[60:63]\n"
        "ds_read_b128 a[28:31], v225 offset:60416\n"
        "global_load_lds_dwordx4 v145, s[60:61] offset:1024\n"
        "v_mfma_f32_16x16x32_bf16 v[60:63], a[60:63], a[220:223], v[60:63]\n"
        "ds_read_b128 a[192:195], v239 offset:49152\n"
        "s_waitcnt lgkmcnt(8)\n"
        "v_mfma_f32_16x16x32_bf16 v[56:59], a[56:59], a[220:223], v[56:59]\n"
        "ds_read_b128 a[196:199], v239 offset:57344\n"
        "s_waitcnt lgkmcnt(8)\n"
        "v_mfma_f32_16x16x32_bf16 v[56:59], a[48:51], a[216:219], v[56:59]\n"
        "global_load_lds_dwordx4 v146, s[60:61] offset:2048\n"
        "v_mfma_f32_16x16x32_bf16 v[0:3], a[224:227], a[0:3], v[0:3]\n"
        "ds_read_b128 a[200:203], v239 offset:50176\n"
        "s_waitcnt lgkmcnt(7)\n"
        "v_mfma_f32_16x16x32_bf16 v[0:3], a[232:235], a[4:7], v[0:3]\n"
        "ds_read_b128 a[204:207], v239 offset:58368\n"
        "s_waitcnt lgkmcnt(7)\n"
        "v_mfma_f32_16x16x32_bf16 v[4:7], a[236:239], a[4:7], v[4:7]\n"
        "global_load_lds_dwordx4 v147, s[60:61] offset:3072\n"
        "s_add_u32 m0, s73, 0x8000\n"
        "v_mfma_f32_16x16x32_bf16 v[4:7], a[228:231], a[0:3], v[4:7]\n"
        "ds_read_b128 a[208:211], v239 offset:51200\n"
        "s_add_u32 s60, s20, s66\n"
        "s_addc_u32 s61, s21, 0\n"
        "v_mfma_f32_16x16x32_bf16 v[12:15], a[228:231], a[8:11], v[12:15]\n"
        "ds_read_b128 a[212:215], v239 offset:59392\n"
        "s_xor_b32 s66, s17, 18\n"
        "s_lshl_b32 s66, s66, 17\n"
        "v_mfma_f32_16x16x32_bf16 v[12:15], a[236:239], a[12:15], v[12:15]\n"
        "ds_read_b128 a[216:219], v239 offset:52224\n"
        "s_waitcnt lgkmcnt(6)\n"
        "v_mfma_f32_16x16x32_bf16 v[8:11], a[232:235], a[12:15], v[8:11]\n"
        "ds_read_b128 a[220:223], v239 offset:60416\n"
        "v_mfma_f32_16x16x32_bf16 v[8:11], a[224:227], a[8:11], v[8:11]\n"
        "s_waitcnt lgkmcnt(6)\n"
        "v_mfma_f32_16x16x32_bf16 v[16:19], a[224:227], a[16:19], v[16:19]\n"
        "v_mfma_f32_16x16x32_bf16 v[16:19], a[232:235], a[20:23], v[16:19]\n"
        "s_waitcnt lgkmcnt(4)\n"
        "v_mfma_f32_16x16x32_bf16 v[20:23], a[236:239], a[20:23], v[20:23]\n"
        "v_mfma_f32_16x16x32_bf16 v[20:23], a[228:231], a[16:19], v[20:23]\n"
        "s_waitcnt vmcnt(20)\n"
        "v_mfma_f32_16x16x32_bf16 v[28:31], a[228:231], a[24:27], v[28:31]\n"
        "s_barrier\n"
        "v_mfma_f32_16x16x32_bf16 v[28:31], a[236:239], a[28:31], v[28:31]\n"
        "global_load_lds_dwordx4 v254, s[60:61] offset:0 sc1\n"
        "ds_read_b128 a[0:3], v226 offset:0\n"
        "v_mfma_f32_16x16x32_bf16 v[24:27], a[232:235], a[28:31], v[24:27]\n"
        "ds_read_b128 a[4:7], v226 offset:8192\n"
        "s_waitcnt lgkmcnt(2)\n"
        "v_mfma_f32_16x16x32_bf16 v[24:27], a[224:227], a[24:27], v[24:27]\n"
        "ds_read_b128 a[8:11], v226 offset:1024\n"
        "v_mfma_f32_16x16x32_bf16 v[32:35], a[224:227], a[192:195], v[32:35]\n"
        "global_load_lds_dwordx4 v254, s[60:61] offset:1024 sc1\n"
        "ds_read_b128 a[12:15], v226 offset:9216\n"
        "v_mfma_f32_16x16x32_bf16 v[32:35], a[232:235], a[196:199], v[32:35]\n"
        "ds_read_b128 a[16:19], v226 offset:2048\n"
        "v_mfma_f32_16x16x32_bf16 v[36:39], a[236:239], a[196:199], v[36:39]\n"
        "ds_read_b128 a[20:23], v226 offset:10240\n"
        "v_mfma_f32_16x16x32_bf16 v[36:39], a[228:231], a[192:195], v[36:39]\n"
        "global_load_lds_dwordx4 v254, s[60:61] offset:2048 sc1\n"
        "ds_read_b128 a[24:27], v226 offset:3072\n"
        "v_mfma_f32_16x16x32_bf16 v[44:47], a[228:231], a[200:203], v[44:47]\n"
        "ds_read_b128 a[28:31], v226 offset:11264\n"
        "v_mfma_f32_16x16x32_bf16 v[44:47], a[236:239], a[204:207], v[44:47]\n"
        "ds_read_b128 a[192:195], v240 offset:0\n"
        "s_waitcnt lgkmcnt(8)\n"
        "v_mfma_f32_16x16x32_bf16 v[40:43], a[232:235], a[204:207], v[40:43]\n"
        "global_load_lds_dwordx4 v254, s[60:61] offset:3072 sc1\n"
        "s_add_u32 m0, s73, 0xc000\n"
        "v_mfma_f32_16x16x32_bf16 v[40:43], a[224:227], a[200:203], v[40:43]\n"
        "s_add_u32 s60, s22, s66\n"
        "s_addc_u32 s61, s23, 0\n"
        "s_xor_b32 s66, s17, 18\n"
        "v_mfma_f32_16x16x32_bf16 v[48:51], a[224:227], a[208:211], v[48:51]\n"
        "ds_read_b128 a[196:199], v240 offset:8192\n"
        "s_lshl_b32 s66, s66, 14\n"
        "s_waitcnt lgkmcnt(6)\n"
        "v_mfma_f32_16x16x32_bf16 v[48:51], a[232:235], a[212:215], v[48:51]\n"
        "global_load_lds_dwordx4 v144, s[60:61] offset:0\n"
        "ds_read_b128 a[200:203], v240 offset:1024\n"
        "v_mfma_f32_16x16x32_bf16 v[52:55], a[236:239], a[212:215], v[52:55]\n"
        "LHB2_%=:\n"
        "s_cbranch_execz LHRB_%=\n"
        "ds_read_b128 a[204:207], v240 offset:9216\n"
        "v_mfma_f32_16x16x32_bf16 v[52:55], a[228:231], a[208:211], v[52:55]\n"
        "ds_read_b128 a[208:211], v240 offset:2048\n"
        "v_mfma_f32_16x16x32_bf16 v[60:63], a[228:231], a[216:219], v[60:63]\n"
        "global_load_lds_dwordx4 v145, s[60:61] offset:1024\n"
        "ds_read_b128 a[212:215], v240 offset:10240\n"
        "v_mfma_f32_16x16x32_bf16 v[60:63], a[236:239], a[220:223], v[60:63]\n"
        "s_waitcnt lgkmcnt(5)\n"
        "v_mfma_f32_16x16x32_bf16 v[56:59], a[232:235], a[220:223], v[56:59]\n"
        "ds_read_b128 a[220:223], v240 offset:11264\n"
        "v_mfma_f32_16x16x32_bf16 v[56:59], a[224:227], a[216:219], v[56:59]\n"
        "global_load_lds_dwordx4 v146, s[60:61] offset:2048\n"
        "ds_read_b128 a[216:219], v240 offset:3072\n"
        "v_mfma_f32_16x16x32_bf16 v[0:3], v[208:211], a[0:3], v[0:3]\n"
        "v_mfma_f32_16x16x32_bf16 v[0:3], v[216:219], a[4:7], v[0:3]\n"
        "v_mfma_f32_16x16x32_bf16 v[4:7], v[220:223], a[4:7], v[4:7]\n"
        "global_load_lds_dwordx4 v147, s[60:61] offset:3072\n"
        "s_add_u32 m0, s73, 0x18000\n"
        "v_mfma_f32_16x16x32_bf16 v[4:7], v[212:215], a[0:3], v[4:7]\n"
        "s_add_u32 s60, s20, s66\n"
        "s_addc_u32 s61, s21, 0\n"
        "s_xor_b32 s66, s17, 19\n"
        "v_mfma_f32_16x16x32_bf16 v[12:15], v[212:215], a[8:11], v[12:15]\n"
        "s_lshl_b32 s66, s66, 14\n"
        "v_mfma_f32_16x16x32_bf16 v[12:15], v[220:223], a[12:15], v[12:15]\n"
        "v_mfma_f32_16x16x32_bf16 v[8:11], v[216:219], a[12:15], v[8:11]\n"
        "v_mfma_f32_16x16x32_bf16 v[8:11], v[208:211], a[8:11], v[8:11]\n"
        "s_waitcnt lgkmcnt(1)\n"
        "v_mfma_f32_16x16x32_bf16 v[16:19], v[208:211], a[16:19], v[16:19]\n"
        "v_mfma_f32_16x16x32_bf16 v[16:19], v[216:219], a[20:23], v[16:19]\n"
        "v_mfma_f32_16x16x32_bf16 v[20:23], v[220:223], a[20:23], v[20:23]\n"
        "v_mfma_f32_16x16x32_bf16 v[20:23], v[212:215], a[16:19], v[20:23]\n"
        "s_waitcnt vmcnt(20)\n"
        "v_mfma_f32_16x16x32_bf16 v[28:31], v[212:215], a[24:27], v[28:31]\n"
        "s_barrier\n"
        "v_mfma_f32_16x16x32_bf16 v[28:31], v[220:223], a[28:31], v[28:31]\n"
        "global_load_lds_dwordx4 v254, s[60:61] offset:0 sc1\n"
        "ds_read_b64_tr_b16 a[240:241], v157 offset:16384\n"
        "v_mfma_f32_16x16x32_bf16 v[24:27], v[216:219], a[28:31], v[24:27]\n"
        "ds_read_b64_tr_b16 a[242:243], v157 offset:20480\n"
        "v_mfma_f32_16x16x32_bf16 v[24:27], v[208:211], a[24:27], v[24:27]\n"
        "ds_read_b64_tr_b16 a[248:249], v157 offset:24576\n"
        "v_mfma_f32_16x16x32_bf16 v[32:35], v[208:211], a[192:195], v[32:35]\n"
        "ds_read_b64_tr_b16 a[250:251], v157 offset:28672\n"
        "global_load_lds_dwordx4 v254, s[60:61] offset:1024 sc1\n"
        "v_mfma_f32_16x16x32_bf16 v[32:35], v[216:219], a[196:199], v[32:35]\n"
        "ds_read_b64_tr_b16 a[244:245], v158 offset:16384\n"
        "s_waitcnt lgkmcnt(5)\n"
        "v_mfma_f32_16x16x32_bf16 v[36:39], v[220:223], a[196:199], v[36:39]\n"
        "ds_read_b64_tr_b16 a[246:247], v158 offset:20480\n"
        "v_mfma_f32_16x16x32_bf16 v[36:39], v[212:215], a[192:195], v[36:39]\n"
        "ds_read_b64_tr_b16 a[252:253], v158 offset:24576\n"
        "global_load_lds_dwordx4 v254, s[60:61] offset:2048 sc1\n"
        "v_mfma_f32_16x16x32_bf16 v[44:47], v[212:215], a[200:203], v[44:47]\n"
        "ds_read_b64_tr_b16 a[254:255], v158 offset:28672\n"
        "v_mfma_f32_16x16x32_bf16 v[44:47], v[220:223], a[204:207], v[44:47]\n"
        "ds_read_b128 a[0:3], v225 offset:0\n"
        "v_mfma_f32_16x16x32_bf16 v[40:43], v[216:219], a[204:207], v[40:43]\n"
        "ds_read_b128 a[4:7], v225 offset:8192\n"
        "global_load_lds_dwordx4 v254, s[60:61] offset:3072 sc1\n"
        "s_add_u32 m0, s73, 0x1c000\n"
        "v_mfma_f32_16x16x32_bf16 v[40:43], v[208:211], a[200:203], v[40:43]\n"
        "ds_read_b128 a[8:11], v225 offset:1024\n"
        "s_add_u32 s60, s20, s66\n"
        "s_addc_u32 s61, s21, 0\n"
        "v_mfma_f32_16x16x32_bf16 v[48:51], v[208:211], a[208:211], v[48:51]\n"
        "ds_read_b128 a[12:15], v225 offset:9216\n"
        "s_xor_b32 s66, s17, 20\n"
        "s_lshl_b32 s66, s66, 17\n"
        "v_mfma_f32_16x16x32_bf16 v[48:51], v[216:219], a[212:215], v[48:51]\n"
        "ds_read_b128 a[16:19], v225 offset:2048\n"
        "global_load_lds_dwordx4 v254, s[60:61] offset:0 sc1\n"
        "v_mfma_f32_16x16x32_bf16 v[52:55], v[220:223], a[212:215], v[52:55]\n"
        "ds_read_b128 a[20:23], v225 offset:10240\n"
        "v_mfma_f32_16x16x32_bf16 v[52:55], v[212:215], a[208:211], v[52:55]\n"
        "ds_read_b128 a[24:27], v225 offset:3072\n"
        "v_mfma_f32_16x16x32_bf16 v[60:63], v[212:215], a[216:219], v[60:63]\n"
        "ds_read_b128 a[28:31], v225 offset:11264\n"
        "global_load_lds_dwordx4 v254, s[60:61] offset:1024 sc1\n"
        "v_mfma_f32_16x16x32_bf16 v[60:63], v[220:223], a[220:223], v[60:63]\n"
        "ds_read_b128 a[192:195], v239 offset:0\n"
        "s_waitcnt lgkmcnt(8)\n"
        "v_mfma_f32_16x16x32_bf16 v[56:59], v[216:219], a[220:223], v[56:59]\n"
        "ds_read_b128 a[196:199], v239 offset:8192\n"
        "s_waitcnt lgkmcnt(8)\n"
        "v_mfma_f32_16x16x32_bf16 v[56:59], v[208:211], a[216:219], v[56:59]\n"
        "global_load_lds_dwordx4 v254, s[60:61] offset:2048 sc1\n"
        "v_mfma_f32_16x16x32_bf16 v[0:3], a[240:243], a[0:3], v[0:3]\n"
        "ds_read_b128 a[200:203], v239 offset:1024\n"
        "s_waitcnt lgkmcnt(7)\n"
        "v_mfma_f32_16x16x32_bf16 v[0:3], a[248:251], a[4:7], v[0:3]\n"
        "ds_read_b128 a[204:207], v239 offset:9216\n"
        "s_waitcnt lgkmcnt(7)\n"
        "v_mfma_f32_16x16x32_bf16 v[4:7], a[252:255], a[4:7], v[4:7]\n"
        "global_load_lds_dwordx4 v254, s[60:61] offset:3072 sc1\n"
        "s_add_u32 m0, s73, 0x20000\n"
        "v_mfma_f32_16x16x32_bf16 v[4:7], a[244:247], a[0:3], v[4:7]\n"
        "ds_read_b128 a[208:211], v239 offset:2048\n"
        "s_add_u32 s60, s22, s66\n"
        "s_addc_u32 s61, s23, 0\n"
        "v_mfma_f32_16x16x32_bf16 v[12:15], a[244:247], a[8:11], v[12:15]\n"
        "ds_read_b128 a[212:215], v239 offset:10240\n"
        "s_xor_b32 s66, s17, 20\n"
        "s_lshl_b32 s66, s66, 14\n"
        "v_mfma_f32_16x16x32_bf16 v[12:15], a[252:255], a[12:15], v[12:15]\n"
        "ds_read_b128 a[216:219], v239 offset:3072\n"
        "s_waitcnt lgkmcnt(6)\n"
        "v_mfma_f32_16x16x32_bf16 v[8:11], a[248:251], a[12:15], v[8:11]\n"
        "ds_read_b128 a[220:223], v239 offset:11264\n"
        "v_mfma_f32_16x16x32_bf16 v[8:11], a[240:243], a[8:11], v[8:11]\n"
        "s_waitcnt lgkmcnt(6)\n"
        "v_mfma_f32_16x16x32_bf16 v[16:19], a[240:243], a[16:19], v[16:19]\n"
        "v_mfma_f32_16x16x32_bf16 v[16:19], a[248:251], a[20:23], v[16:19]\n"
        "s_waitcnt lgkmcnt(4)\n"
        "v_mfma_f32_16x16x32_bf16 v[20:23], a[252:255], a[20:23], v[20:23]\n"
        "v_mfma_f32_16x16x32_bf16 v[20:23], a[244:247], a[16:19], v[20:23]\n"
        "s_waitcnt vmcnt(20)\n"
        "global_load_dwordx2 v[236:237], v234, s[48:49] sc1\n"
        "v_mfma_f32_16x16x32_bf16 v[28:31], a[244:247], a[24:27], v[28:31]\n"
        "s_barrier\n"
        "v_mfma_f32_16x16x32_bf16 v[28:31], a[252:255], a[28:31], v[28:31]\n"
        "global_load_lds_dwordx4 v144, s[60:61] offset:0\n"
        "ds_read_b64_tr_b16 a[32:33], v155 offset:16384\n"
        "v_mfma_f32_16x16x32_bf16 v[24:27], a[248:251], a[28:31], v[24:27]\n"
        "ds_read_b64_tr_b16 a[34:35], v155 offset:20480\n"
        "s_waitcnt lgkmcnt(2)\n"
        "v_mfma_f32_16x16x32_bf16 v[24:27], a[240:243], a[24:27], v[24:27]\n"
        "ds_read_b64_tr_b16 a[40:41], v155 offset:24576\n"
        "v_mfma_f32_16x16x32_bf16 v[32:35], a[240:243], a[192:195], v[32:35]\n"
        "ds_read_b64_tr_b16 a[42:43], v155 offset:28672\n"
        "global_load_lds_dwordx4 v145, s[60:61] offset:1024\n"
        "v_mfma_f32_16x16x32_bf16 v[32:35], a[248:251], a[196:199], v[32:35]\n"
        "ds_read_b64_tr_b16 a[36:37], v156 offset:16384\n"
        "v_mfma_f32_16x16x32_bf16 v[36:39], a[252:255], a[196:199], v[36:39]\n"
        "ds_read_b64_tr_b16 a[38:39], v156 offset:20480\n"
        "v_mfma_f32_16x16x32_bf16 v[36:39], a[244:247], a[192:195], v[36:39]\n"
        "ds_read_b64_tr_b16 a[44:45], v156 offset:24576\n"
        "global_load_lds_dwordx4 v146, s[60:61] offset:2048\n"
        "v_mfma_f32_16x16x32_bf16 v[44:47], a[244:247], a[200:203], v[44:47]\n"
        "ds_read_b64_tr_b16 a[46:47], v156 offset:28672\n"
        "v_mfma_f32_16x16x32_bf16 v[44:47], a[252:255], a[204:207], v[44:47]\n"
        "ds_read_b128 a[0:3], v224 offset:0\n"
        "v_mfma_f32_16x16x32_bf16 v[40:43], a[248:251], a[204:207], v[40:43]\n"
        "ds_read_b128 a[4:7], v224 offset:8192\n"
        "global_load_lds_dwordx4 v147, s[60:61] offset:3072\n"
        "s_add_u32 m0, s73, 0x24000\n"
        "v_mfma_f32_16x16x32_bf16 v[40:43], a[240:243], a[200:203], v[40:43]\n"
        "ds_read_b128 a[8:11], v224 offset:1024\n"
        "s_add_u32 s60, s20, s66\n"
        "s_addc_u32 s61, s21, 0\n"
        "v_mfma_f32_16x16x32_bf16 v[48:51], a[240:243], a[208:211], v[48:51]\n"
        "ds_read_b128 a[12:15], v224 offset:9216\n"
        "s_xor_b32 s66, s17, 21\n"
        "s_lshl_b32 s66, s66, 17\n"
        "v_mfma_f32_16x16x32_bf16 v[48:51], a[248:251], a[212:215], v[48:51]\n"
        "ds_read_b128 a[16:19], v224 offset:2048\n"
        "v_mfma_f32_16x16x32_bf16 v[52:55], a[252:255], a[212:215], v[52:55]\n"
        "ds_read_b128 a[20:23], v224 offset:10240\n"
        "v_mfma_f32_16x16x32_bf16 v[52:55], a[244:247], a[208:211], v[52:55]\n"
        "ds_read_b128 a[24:27], v224 offset:3072\n"
        "v_mfma_f32_16x16x32_bf16 v[60:63], a[244:247], a[216:219], v[60:63]\n"
        "ds_read_b128 a[28:31], v224 offset:11264\n"
        "s_waitcnt lgkmcnt(8)\n"
        "v_mfma_f32_16x16x32_bf16 v[60:63], a[252:255], a[220:223], v[60:63]\n"
        "ds_read_b128 a[192:195], v238 offset:0\n"
        "s_waitcnt lgkmcnt(8)\n"
        "v_mfma_f32_16x16x32_bf16 v[56:59], a[248:251], a[220:223], v[56:59]\n"
        "ds_read_b128 a[196:199], v238 offset:8192\n"
        "s_waitcnt lgkmcnt(8)\n"
        "v_mfma_f32_16x16x32_bf16 v[56:59], a[240:243], a[216:219], v[56:59]\n"
        "ds_read_b128 a[200:203], v238 offset:1024\n"
        "s_waitcnt lgkmcnt(8)\n"
        "v_mfma_f32_16x16x32_bf16 v[0:3], a[32:35], a[0:3], v[0:3]\n"
        "ds_read_b128 a[204:207], v238 offset:9216\n"
        "s_waitcnt lgkmcnt(8)\n"
        "v_mfma_f32_16x16x32_bf16 v[0:3], a[40:43], a[4:7], v[0:3]\n"
        "ds_read_b128 a[208:211], v238 offset:2048\n"
        "s_waitcnt lgkmcnt(8)\n"
        "v_mfma_f32_16x16x32_bf16 v[4:7], a[44:47], a[4:7], v[4:7]\n"
        "ds_read_b128 a[212:215], v238 offset:10240\n"
        "s_waitcnt lgkmcnt(8)\n"
        "v_mfma_f32_16x16x32_bf16 v[4:7], a[36:39], a[0:3], v[4:7]\n"
        "ds_read_b128 a[216:219], v238 offset:3072\n"
        "s_waitcnt lgkmcnt(8)\n"
        "v_mfma_f32_16x16x32_bf16 v[12:15], a[36:39], a[8:11], v[12:15]\n"
        "ds_read_b128 a[220:223], v238 offset:11264\n"
        "s_waitcnt lgkmcnt(8)\n"
        "v_mfma_f32_16x16x32_bf16 v[12:15], a[44:47], a[12:15], v[12:15]\n"
        "s_waitcnt lgkmcnt(7)\n"
        "v_mfma_f32_16x16x32_bf16 v[8:11], a[40:43], a[12:15], v[8:11]\n"
        "v_mfma_f32_16x16x32_bf16 v[8:11], a[32:35], a[8:11], v[8:11]\n"
        "s_waitcnt lgkmcnt(5)\n"
        "v_mfma_f32_16x16x32_bf16 v[16:19], a[32:35], a[16:19], v[16:19]\n"
        "v_mfma_f32_16x16x32_bf16 v[16:19], a[40:43], a[20:23], v[16:19]\n"
        "v_mfma_f32_16x16x32_bf16 v[20:23], a[44:47], a[20:23], v[20:23]\n"
        "v_mfma_f32_16x16x32_bf16 v[20:23], a[36:39], a[16:19], v[20:23]\n"
        "s_waitcnt vmcnt(17)\n"
        "s_waitcnt lgkmcnt(1)\n"
        "v_mfma_f32_16x16x32_bf16 v[28:31], a[36:39], a[24:27], v[28:31]\n"
        "s_barrier\n"
        "v_mfma_f32_16x16x32_bf16 v[28:31], a[44:47], a[28:31], v[28:31]\n"
        "global_load_lds_dwordx4 v254, s[60:61] offset:0 sc1\n"
        "ds_read_b64_tr_b16 a[48:49], v153 offset:16384\n"
        "v_mfma_f32_16x16x32_bf16 v[24:27], a[40:43], a[28:31], v[24:27]\n"
        "ds_read_b64_tr_b16 a[50:51], v153 offset:20480\n"
        "v_mfma_f32_16x16x32_bf16 v[24:27], a[32:35], a[24:27], v[24:27]\n"
        "ds_read_b64_tr_b16 a[56:57], v153 offset:24576\n"
        "v_mfma_f32_16x16x32_bf16 v[32:35], a[32:35], a[192:195], v[32:35]\n"
        "ds_read_b64_tr_b16 a[58:59], v153 offset:28672\n"
        "global_load_lds_dwordx4 v254, s[60:61] offset:1024 sc1\n"
        "v_mfma_f32_16x16x32_bf16 v[32:35], a[40:43], a[196:199], v[32:35]\n"
        "ds_read_b64_tr_b16 a[52:53], v154 offset:16384\n"
        "v_mfma_f32_16x16x32_bf16 v[36:39], a[44:47], a[196:199], v[36:39]\n"
        "ds_read_b64_tr_b16 a[54:55], v154 offset:20480\n"
        "v_mfma_f32_16x16x32_bf16 v[36:39], a[36:39], a[192:195], v[36:39]\n"
        "ds_read_b64_tr_b16 a[60:61], v154 offset:24576\n"
        "global_load_lds_dwordx4 v254, s[60:61] offset:2048 sc1\n"
        "v_mfma_f32_16x16x32_bf16 v[44:47], a[36:39], a[200:203], v[44:47]\n"
        "ds_read_b64_tr_b16 a[62:63], v154 offset:28672\n"
        "s_waitcnt lgkmcnt(8)\n"
        "v_mfma_f32_16x16x32_bf16 v[44:47], a[44:47], a[204:207], v[44:47]\n"
        "ds_read_b128 a[0:3], v224 offset:32768\n"
        "v_mfma_f32_16x16x32_bf16 v[40:43], a[40:43], a[204:207], v[40:43]\n"
        "ds_read_b128 a[4:7], v224 offset:40960\n"
        "global_load_lds_dwordx4 v254, s[60:61] offset:3072 sc1\n"
        "s_add_u32 m0, s73, 0x10000\n"
        "v_mfma_f32_16x16x32_bf16 v[40:43], a[32:35], a[200:203], v[40:43]\n"
        "ds_read_b128 a[8:11], v224 offset:33792\n"
        "s_add_u32 s60, s22, s66\n"
        "s_addc_u32 s61, s23, 0\n"
        "v_mfma_f32_16x16x32_bf16 v[48:51], a[32:35], a[208:211], v[48:51]\n"
        "ds_read_b128 a[12:15], v224 offset:41984\n"
        "s_xor_b32 s66, s17, 21\n"
        "s_lshl_b32 s66, s66, 14\n"
        "v_mfma_f32_16x16x32_bf16 v[48:51], a[40:43], a[212:215], v[48:51]\n"
        "ds_read_b128 a[16:19], v224 offset:34816\n"
        "global_load_lds_dwordx4 v144, s[60:61] offset:0\n"
        "v_mfma_f32_16x16x32_bf16 v[52:55], a[44:47], a[212:215], v[52:55]\n"
        "ds_read_b128 a[20:23], v224 offset:43008\n"
        "v_mfma_f32_16x16x32_bf16 v[52:55], a[36:39], a[208:211], v[52:55]\n"
        "ds_read_b128 a[24:27], v224 offset:35840\n"
        "v_mfma_f32_16x16x32_bf16 v[60:63], a[36:39], a[216:219], v[60:63]\n"
        "ds_read_b128 a[28:31], v224 offset:44032\n"
        "global_load_lds_dwordx4 v145, s[60:61] offset:1024\n"
        "v_mfma_f32_16x16x32_bf16 v[60:63], a[44:47], a[220:223], v[60:63]\n"
        "ds_read_b128 a[192:195], v238 offset:32768\n"
        "s_waitcnt lgkmcnt(8)\n"
        "v_mfma_f32_16x16x32_bf16 v[56:59], a[40:43], a[220:223], v[56:59]\n"
        "ds_read_b128 a[196:199], v238 offset:40960\n"
        "s_waitcnt lgkmcnt(8)\n"
        "v_mfma_f32_16x16x32_bf16 v[56:59], a[32:35], a[216:219], v[56:59]\n"
        "global_load_lds_dwordx4 v146, s[60:61] offset:2048\n"
        "v_mfma_f32_16x16x32_bf16 v[0:3], a[48:51], a[0:3], v[0:3]\n"
        "ds_read_b128 a[200:203], v238 offset:33792\n"
        "s_waitcnt lgkmcnt(7)\n"
        "v_mfma_f32_16x16x32_bf16 v[0:3], a[56:59], a[4:7], v[0:3]\n"
        "ds_read_b128 a[204:207], v238 offset:41984\n"
        "s_waitcnt lgkmcnt(7)\n"
        "v_mfma_f32_16x16x32_bf16 v[4:7], a[60:63], a[4:7], v[4:7]\n"
        "global_load_lds_dwordx4 v147, s[60:61] offset:3072\n"
        "s_add_u32 m0, s73, 0x14000\n"
        "v_mfma_f32_16x16x32_bf16 v[4:7], a[52:55], a[0:3], v[4:7]\n"
        "ds_read_b128 a[208:211], v238 offset:34816\n"
        "s_add_u32 s60, s20, s66\n"
        "s_addc_u32 s61, s21, 0\n"
        "v_mfma_f32_16x16x32_bf16 v[12:15], a[52:55], a[8:11], v[12:15]\n"
        "ds_read_b128 a[212:215], v238 offset:43008\n"
        "s_xor_b32 s66, s17, 22\n"
        "s_lshl_b32 s66, s66, 17\n"
        "v_mfma_f32_16x16x32_bf16 v[12:15], a[60:63], a[12:15], v[12:15]\n"
        "ds_read_b128 a[216:219], v238 offset:35840\n"
        "s_waitcnt lgkmcnt(6)\n"
        "v_mfma_f32_16x16x32_bf16 v[8:11], a[56:59], a[12:15], v[8:11]\n"
        "ds_read_b128 a[220:223], v238 offset:44032\n"
        "v_mfma_f32_16x16x32_bf16 v[8:11], a[48:51], a[8:11], v[8:11]\n"
        "s_waitcnt lgkmcnt(6)\n"
        "v_mfma_f32_16x16x32_bf16 v[16:19], a[48:51], a[16:19], v[16:19]\n"
        "v_mfma_f32_16x16x32_bf16 v[16:19], a[56:59], a[20:23], v[16:19]\n"
        "s_waitcnt lgkmcnt(4)\n"
        "v_mfma_f32_16x16x32_bf16 v[20:23], a[60:63], a[20:23], v[20:23]\n"
        "v_mfma_f32_16x16x32_bf16 v[20:23], a[52:55], a[16:19], v[20:23]\n"
        "s_waitcnt vmcnt(17)\n"
        "v_mfma_f32_16x16x32_bf16 v[28:31], a[52:55], a[24:27], v[28:31]\n"
        "s_barrier\n"
        "v_mfma_f32_16x16x32_bf16 v[28:31], a[60:63], a[28:31], v[28:31]\n"
        "global_load_lds_dwordx4 v254, s[60:61] offset:0 sc1\n"
        "ds_read_b64_tr_b16 a[224:225], v153 offset:49152\n"
        "v_mfma_f32_16x16x32_bf16 v[24:27], a[56:59], a[28:31], v[24:27]\n"
        "ds_read_b64_tr_b16 a[226:227], v153 offset:53248\n"
        "s_waitcnt lgkmcnt(2)\n"
        "v_mfma_f32_16x16x32_bf16 v[24:27], a[48:51], a[24:27], v[24:27]\n"
        "ds_read_b64_tr_b16 a[232:233], v153 offset:57344\n"
        "v_mfma_f32_16x16x32_bf16 v[32:35], a[48:51], a[192:195], v[32:35]\n"
        "ds_read_b64_tr_b16 a[234:235], v153 offset:61440\n"
        "global_load_lds_dwordx4 v254, s[60:61] offset:1024 sc1\n"
        "v_mfma_f32_16x16x32_bf16 v[32:35], a[56:59], a[196:199], v[32:35]\n"
        "ds_read_b64_tr_b16 a[228:229], v154 offset:49152\n"
        "v_mfma_f32_16x16x32_bf16 v[36:39], a[60:63], a[196:199], v[36:39]\n"
        "ds_read_b64_tr_b16 a[230:231], v154 offset:53248\n"
        "v_mfma_f32_16x16x32_bf16 v[36:39], a[52:55], a[192:195], v[36:39]\n"
        "ds_read_b64_tr_b16 a[236:237], v154 offset:57344\n"
        "global_load_lds_dwordx4 v254, s[60:61] offset:2048 sc1\n"
        "v_mfma_f32_16x16x32_bf16 v[44:47], a[52:55], a[200:203], v[44:47]\n"
        "ds_read_b64_tr_b16 a[238:239], v154 offset:61440\n"
        "v_mfma_f32_16x16x32_bf16 v[44:47], a[60:63], a[204:207], v[44:47]\n"
        "ds_read_b128 a[0:3], v225 offset:32768\n"
        "v_mfma_f32_16x16x32_bf16 v[40:43], a[56:59], a[204:207], v[40:43]\n"
        "ds_read_b128 a[4:7], v225 offset:40960\n"
        "global_load_lds_dwordx4 v254, s[60:61] offset:3072 sc1\n"
        "s_add_u32 m0, s73, 0x0\n"
        "v_mfma_f32_16x16x32_bf16 v[40:43], a[48:51], a[200:203], v[40:43]\n"
        "ds_read_b128 a[8:11], v225 offset:33792\n"
        "s_add_u32 s60, s22, s66\n"
        "s_addc_u32 s61, s23, 0\n"
        "v_mfma_f32_16x16x32_bf16 v[48:51], a[48:51], a[208:211], v[48:51]\n"
        "ds_read_b128 a[12:15], v225 offset:41984\n"
        "s_xor_b32 s66, s17, 22\n"
        "s_lshl_b32 s66, s66, 14\n"
        "v_mfma_f32_16x16x32_bf16 v[48:51], a[56:59], a[212:215], v[48:51]\n"
        "ds_read_b128 a[16:19], v225 offset:34816\n"
        "global_load_lds_dwordx4 v144, s[60:61] offset:0\n"
        "v_mfma_f32_16x16x32_bf16 v[52:55], a[60:63], a[212:215], v[52:55]\n"
        "ds_read_b128 a[20:23], v225 offset:43008\n"
        "v_mfma_f32_16x16x32_bf16 v[52:55], a[52:55], a[208:211], v[52:55]\n"
        "ds_read_b128 a[24:27], v225 offset:35840\n"
        "v_mfma_f32_16x16x32_bf16 v[60:63], a[52:55], a[216:219], v[60:63]\n"
        "ds_read_b128 a[28:31], v225 offset:44032\n"
        "global_load_lds_dwordx4 v145, s[60:61] offset:1024\n"
        "v_mfma_f32_16x16x32_bf16 v[60:63], a[60:63], a[220:223], v[60:63]\n"
        "ds_read_b128 a[192:195], v239 offset:32768\n"
        "s_waitcnt lgkmcnt(8)\n"
        "v_mfma_f32_16x16x32_bf16 v[56:59], a[56:59], a[220:223], v[56:59]\n"
        "ds_read_b128 a[196:199], v239 offset:40960\n"
        "s_waitcnt lgkmcnt(8)\n"
        "v_mfma_f32_16x16x32_bf16 v[56:59], a[48:51], a[216:219], v[56:59]\n"
        "global_load_lds_dwordx4 v146, s[60:61] offset:2048\n"
        "v_mfma_f32_16x16x32_bf16 v[0:3], a[224:227], a[0:3], v[0:3]\n"
        "ds_read_b128 a[200:203], v239 offset:33792\n"
        "s_waitcnt lgkmcnt(7)\n"
        "v_mfma_f32_16x16x32_bf16 v[0:3], a[232:235], a[4:7], v[0:3]\n"
        "ds_read_b128 a[204:207], v239 offset:41984\n"
        "s_waitcnt lgkmcnt(7)\n"
        "v_mfma_f32_16x16x32_bf16 v[4:7], a[236:239], a[4:7], v[4:7]\n"
        "global_load_lds_dwordx4 v147, s[60:61] offset:3072\n"
        "s_add_u32 m0, s73, 0x4000\n"
        "v_mfma_f32_16x16x32_bf16 v[4:7], a[228:231], a[0:3], v[4:7]\n"
        "ds_read_b128 a[208:211], v239 offset:34816\n"
        "s_add_u32 s60, s20, s66\n"
        "s_addc_u32 s61, s21, 0\n"
        "v_mfma_f32_16x16x32_bf16 v[12:15], a[228:231], a[8:11], v[12:15]\n"
        "ds_read_b128 a[212:215], v239 offset:43008\n"
        "s_xor_b32 s66, s17, 23\n"
        "s_lshl_b32 s66, s66, 17\n"
        "v_mfma_f32_16x16x32_bf16 v[12:15], a[236:239], a[12:15], v[12:15]\n"
        "ds_read_b128 a[216:219], v239 offset:35840\n"
        "s_waitcnt lgkmcnt(6)\n"
        "v_mfma_f32_16x16x32_bf16 v[8:11], a[232:235], a[12:15], v[8:11]\n"
        "ds_read_b128 a[220:223], v239 offset:44032\n"
        "v_mfma_f32_16x16x32_bf16 v[8:11], a[224:227], a[8:11], v[8:11]\n"
        "s_waitcnt lgkmcnt(6)\n"
        "v_mfma_f32_16x16x32_bf16 v[16:19], a[224:227], a[16:19], v[16:19]\n"
        "v_mfma_f32_16x16x32_bf16 v[16:19], a[232:235], a[20:23], v[16:19]\n"
        "s_waitcnt lgkmcnt(4)\n"
        "v_mfma_f32_16x16x32_bf16 v[20:23], a[236:239], a[20:23], v[20:23]\n"
        "v_mfma_f32_16x16x32_bf16 v[20:23], a[228:231], a[16:19], v[20:23]\n"
        "s_waitcnt vmcnt(21)\n"
        "v_mfma_f32_16x16x32_bf16 v[28:31], a[228:231], a[24:27], v[28:31]\n"
        "s_barrier\n"
        "v_mfma_f32_16x16x32_bf16 v[28:31], a[236:239], a[28:31], v[28:31]\n"
        "global_load_lds_dwordx4 v254, s[60:61] offset:0 sc1\n"
        "ds_read_b128 a[0:3], v225 offset:49152\n"
        "v_mfma_f32_16x16x32_bf16 v[24:27], a[232:235], a[28:31], v[24:27]\n"
        "ds_read_b128 a[4:7], v225 offset:57344\n"
        "s_waitcnt lgkmcnt(2)\n"
        "v_mfma_f32_16x16x32_bf16 v[24:27], a[224:227], a[24:27], v[24:27]\n"
        "ds_read_b128 a[8:11], v225 offset:50176\n"
        "v_mfma_f32_16x16x32_bf16 v[32:35], a[224:227], a[192:195], v[32:35]\n"
        "global_load_lds_dwordx4 v254, s[60:61] offset:1024 sc1\n"
        "ds_read_b128 a[12:15], v225 offset:58368\n"
        "v_mfma_f32_16x16x32_bf16 v[32:35], a[232:235], a[196:199], v[32:35]\n"
        "ds_read_b128 a[16:19], v225 offset:51200\n"
        "v_mfma_f32_16x16x32_bf16 v[36:39], a[236:239], a[196:199], v[36:39]\n"
        "ds_read_b128 a[20:23], v225 offset:59392\n"
        "v_mfma_f32_16x16x32_bf16 v[36:39], a[228:231], a[192:195], v[36:39]\n"
        "global_load_lds_dwordx4 v254, s[60:61] offset:2048 sc1\n"
        "ds_read_b128 a[24:27], v225 offset:52224\n"
        "v_mfma_f32_16x16x32_bf16 v[44:47], a[228:231], a[200:203], v[44:47]\n"
        "ds_read_b128 a[28:31], v225 offset:60416\n"
        "v_mfma_f32_16x16x32_bf16 v[44:47], a[236:239], a[204:207], v[44:47]\n"
        "ds_read_b128 a[192:195], v239 offset:49152\n"
        "s_waitcnt lgkmcnt(8)\n"
        "v_mfma_f32_16x16x32_bf16 v[40:43], a[232:235], a[204:207], v[40:43]\n"
        "global_load_lds_dwordx4 v254, s[60:61] offset:3072 sc1\n"
        "s_add_u32 m0, s73, 0x8000\n"
        "v_mfma_f32_16x16x32_bf16 v[40:43], a[224:227], a[200:203], v[40:43]\n"
        "s_add_u32 s60, s22, s66\n"
        "s_addc_u32 s61, s23, 0\n"
        "s_xor_b32 s66, s17, 23\n"
        "v_mfma_f32_16x16x32_bf16 v[48:51], a[224:227], a[208:211], v[48:51]\n"
        "ds_read_b128 a[196:199], v239 offset:57344\n"
        "s_lshl_b32 s66, s66, 14\n"
        "s_waitcnt lgkmcnt(6)\n"
        "v_mfma_f32_16x16x32_bf16 v[48:51], a[232:235], a[212:215], v[48:51]\n"
        "global_load_lds_dwordx4 v144, s[60:61] offset:0\n"
        "ds_read_b128 a[200:203], v239 offset:50176\n"
        "v_mfma_f32_16x16x32_bf16 v[52:55], a[236:239], a[212:215], v[52:55]\n"
        "ds_read_b128 a[204:207], v239 offset:58368\n"
        "v_mfma_f32_16x16x32_bf16 v[52:55], a[228:231], a[208:211], v[52:55]\n"
        "ds_read_b128 a[208:211], v239 offset:51200\n"
        "v_mfma_f32_16x16x32_bf16 v[60:63], a[228:231], a[216:219], v[60:63]\n"
        "global_load_lds_dwordx4 v145, s[60:61] offset:1024\n"
        "ds_read_b128 a[212:215], v239 offset:59392\n"
        "v_mfma_f32_16x16x32_bf16 v[60:63], a[236:239], a[220:223], v[60:63]\n"
        "s_waitcnt lgkmcnt(5)\n"
        "v_mfma_f32_16x16x32_bf16 v[56:59], a[232:235], a[220:223], v[56:59]\n"
        "ds_read_b128 a[220:223], v239 offset:60416\n"
        "v_mfma_f32_16x16x32_bf16 v[56:59], a[224:227], a[216:219], v[56:59]\n"
        "global_load_lds_dwordx4 v146, s[60:61] offset:2048\n"
        "ds_read_b128 a[216:219], v239 offset:52224\n"
        "v_mfma_f32_16x16x32_bf16 v[0:3], v[112:115], a[0:3], v[0:3]\n"
        "v_mfma_f32_16x16x32_bf16 v[0:3], v[120:123], a[4:7], v[0:3]\n"
        "v_mfma_f32_16x16x32_bf16 v[4:7], v[124:127], a[4:7], v[4:7]\n"
        "global_load_lds_dwordx4 v147, s[60:61] offset:3072\n"
        "s_add_u32 m0, s73, 0xc000\n"
        "v_mfma_f32_16x16x32_bf16 v[4:7], v[116:119], a[0:3], v[4:7]\n"
        "s_add_u32 s60, s20, s66\n"
        "s_addc_u32 s61, s21, 0\n"
        "s_xor_b32 s66, s67, 24\n"
        "v_mfma_f32_16x16x32_bf16 v[12:15], v[116:119], a[8:11], v[12:15]\n"
        "s_add_u32 s20, s20, s14\n"
        "s_addc_u32 s21, s21, 0\n"
        "s_lshl_b32 s66, s66, 14\n"
        "v_mfma_f32_16x16x32_bf16 v[12:15], v[124:127], a[12:15], v[12:15]\n"
        "s_sub_u32 s20, s20, 16384\n"
        "s_subb_u32 s21, s21, 0\n"
        "v_cmp_eq_u32 s[14:15], v247, v248\n"
        "v_mfma_f32_16x16x32_bf16 v[8:11], v[120:123], a[12:15], v[8:11]\n"
        "v_mfma_f32_16x16x32_bf16 v[8:11], v[112:115], a[8:11], v[8:11]\n"
        "s_waitcnt lgkmcnt(1)\n"
        "v_cndmask_b32 v138, 0, v246, s[14:15]\n"
        "v_cmp_eq_u32 s[14:15], v247, v249\n"
        "v_mfma_f32_16x16x32_bf16 v[16:19], v[112:115], a[16:19], v[16:19]\n"
        "v_mfma_f32_16x16x32_bf16 v[16:19], v[120:123], a[20:23], v[16:19]\n"
        "v_cndmask_b32 v139, 0, v246, s[14:15]\n"
        "s_lshl_b32 s14, s10, 13\n"
        "v_mfma_f32_16x16x32_bf16 v[20:23], v[124:127], a[20:23], v[20:23]\n"
        "v_mfma_f32_16x16x32_bf16 v[20:23], v[116:119], a[16:19], v[20:23]\n"
        "s_waitcnt vmcnt(20)\n"
        "v_mfma_f32_16x16x32_bf16 v[28:31], v[116:119], a[24:27], v[28:31]\n"
        "s_barrier\n"
        "v_mfma_f32_16x16x32_bf16 v[28:31], v[124:127], a[28:31], v[28:31]\n"
        "global_load_lds_dwordx4 v254, s[60:61] offset:0 sc1\n"
        "ds_read_b64_tr_b16 a[240:241], v157 offset:0\n"
        "v_mfma_f32_16x16x32_bf16 v[24:27], v[120:123], a[28:31], v[24:27]\n"
        "ds_read_b64_tr_b16 a[242:243], v157 offset:4096\n"
        "v_mfma_f32_16x16x32_bf16 v[24:27], v[112:115], a[24:27], v[24:27]\n"
        "ds_read_b64_tr_b16 a[248:249], v157 offset:8192\n"
        "v_mfma_f32_16x16x32_bf16 v[32:35], v[112:115], a[192:195], v[32:35]\n"
        "ds_read_b64_tr_b16 a[250:251], v157 offset:12288\n"
        "global_load_lds_dwordx4 v254, s[60:61] offset:1024 sc1\n"
        "v_mfma_f32_16x16x32_bf16 v[32:35], v[120:123], a[196:199], v[32:35]\n"
        "ds_read_b64_tr_b16 a[244:245], v158 offset:0\n"
        "s_waitcnt lgkmcnt(5)\n"
        "v_mfma_f32_16x16x32_bf16 v[36:39], v[124:127], a[196:199], v[36:39]\n"
        "ds_read_b64_tr_b16 a[246:247], v158 offset:4096\n"
        "v_mfma_f32_16x16x32_bf16 v[36:39], v[116:119], a[192:195], v[36:39]\n"
        "ds_read_b64_tr_b16 a[252:253], v158 offset:8192\n"
        "global_load_lds_dwordx4 v254, s[60:61] offset:2048 sc1\n"
        "v_mfma_f32_16x16x32_bf16 v[44:47], v[116:119], a[200:203], v[44:47]\n"
        "ds_read_b64_tr_b16 a[254:255], v158 offset:12288\n"
        "v_mfma_f32_16x16x32_bf16 v[44:47], v[124:127], a[204:207], v[44:47]\n"
        "ds_read_b128 a[0:3], v226 offset:16384\n"
        "v_mfma_f32_16x16x32_bf16 v[40:43], v[120:123], a[204:207], v[40:43]\n"
        "ds_read_b128 a[4:7], v226 offset:24576\n"
        "global_load_lds_dwordx4 v254, s[60:61] offset:3072 sc1\n"
        "s_add_u32 m0, s73, 0x18000\n"
        "v_mfma_f32_16x16x32_bf16 v[40:43], v[112:115], a[200:203], v[40:43]\n"
        "ds_read_b128 a[8:11], v226 offset:17408\n"
        "s_add_u32 s60, s20, s66\n"
        "s_addc_u32 s61, s21, 0\n"
        "v_mfma_f32_16x16x32_bf16 v[48:51], v[112:115], a[208:211], v[48:51]\n"
        "ds_read_b128 a[12:15], v226 offset:25600\n"
        "s_xor_b32 s66, s67, 26\n"
        "s_lshl_b32 s66, s66, 14\n"
        "v_mfma_f32_16x16x32_bf16 v[48:51], v[120:123], a[212:215], v[48:51]\n"
        "ds_read_b128 a[16:19], v226 offset:18432\n"
        "global_load_lds_dwordx4 v254, s[60:61] offset:0 sc1\n"
        "v_mfma_f32_16x16x32_bf16 v[52:55], v[124:127], a[212:215], v[52:55]\n"
        "ds_read_b128 a[20:23], v226 offset:26624\n"
        "v_mfma_f32_16x16x32_bf16 v[52:55], v[116:119], a[208:211], v[52:55]\n"
        "ds_read_b128 a[24:27], v226 offset:19456\n"
        "v_mfma_f32_16x16x32_bf16 v[60:63], v[116:119], a[216:219], v[60:63]\n"
        "ds_read_b128 a[28:31], v226 offset:27648\n"
        "global_load_lds_dwordx4 v254, s[60:61] offset:1024 sc1\n"
        "v_mfma_f32_16x16x32_bf16 v[60:63], v[124:127], a[220:223], v[60:63]\n"
        "ds_read_b128 a[192:195], v240 offset:16384\n"
        "s_waitcnt lgkmcnt(8)\n"
        "v_mfma_f32_16x16x32_bf16 v[56:59], v[120:123], a[220:223], v[56:59]\n"
        "ds_read_b128 a[196:199], v240 offset:24576\n"
        "s_waitcnt lgkmcnt(8)\n"
        "v_mfma_f32_16x16x32_bf16 v[56:59], v[112:115], a[216:219], v[56:59]\n"
        "global_load_lds_dwordx4 v254, s[60:61] offset:2048 sc1\n"
        "v_mfma_f32_16x16x32_bf16 v[0:3], a[240:243], a[0:3], v[0:3]\n"
        "ds_read_b128 a[200:203], v240 offset:17408\n"
        "s_waitcnt lgkmcnt(7)\n"
        "v_mfma_f32_16x16x32_bf16 v[0:3], a[248:251], a[4:7], v[0:3]\n"
        "ds_read_b128 a[204:207], v240 offset:25600\n"
        "s_waitcnt lgkmcnt(7)\n"
        "v_mfma_f32_16x16x32_bf16 v[4:7], a[252:255], a[4:7], v[4:7]\n"
        "global_load_lds_dwordx4 v254, s[60:61] offset:3072 sc1\n"
        "s_add_u32 m0, s73, 0x1c000\n"
        "v_mfma_f32_16x16x32_bf16 v[4:7], a[244:247], a[0:3], v[4:7]\n"
        "ds_read_b128 a[208:211], v240 offset:18432\n"
        "s_add_u32 s60, s20, s66\n"
        "s_addc_u32 s61, s21, 0\n"
        "v_mfma_f32_16x16x32_bf16 v[12:15], a[244:247], a[8:11], v[12:15]\n"
        "ds_read_b128 a[212:215], v240 offset:26624\n"
        "s_xor_b32 s66, s67, 28\n"
        "s_lshl_b32 s66, s66, 14\n"
        "v_mfma_f32_16x16x32_bf16 v[12:15], a[252:255], a[12:15], v[12:15]\n"
        "ds_read_b128 a[216:219], v240 offset:19456\n"
        "s_waitcnt lgkmcnt(6)\n"
        "v_mfma_f32_16x16x32_bf16 v[8:11], a[248:251], a[12:15], v[8:11]\n"
        "ds_read_b128 a[220:223], v240 offset:27648\n"
        "v_mfma_f32_16x16x32_bf16 v[8:11], a[240:243], a[8:11], v[8:11]\n"
        "s_waitcnt lgkmcnt(6)\n"
        "v_cmp_ne_u64 vcc, v[236:237], v[250:251]\n"
        "v_mfma_f32_16x16x32_bf16 v[16:19], a[240:243], a[16:19], v[16:19]\n"
        "s_cbranch_vccnz LPZZ_%=\n"
        "LBZZ_%=:\n"
        "v_lshrrev_b32 v234, 1, v254\n"
        "v_mfma_f32_16x16x32_bf16 v[16:19], a[248:251], a[20:23], v[16:19]\n"
        "s_waitcnt lgkmcnt(4)\n"
        "v_mfma_f32_16x16x32_bf16 v[20:23], a[252:255], a[20:23], v[20:23]\n"
        "v_mfma_f32_16x16x32_bf16 v[20:23], a[244:247], a[16:19], v[20:23]\n"
        "s_waitcnt vmcnt(20)\n"
        "v_mfma_f32_16x16x32_bf16 v[28:31], a[244:247], a[24:27], v[28:31]\n"
        "s_barrier\n"
        "v_mfma_f32_16x16x32_bf16 v[28:31], a[252:255], a[28:31], v[28:31]\n"
        "global_load_lds_dwordx4 v254, s[60:61] offset:0 sc1\n"
        "ds_read_b64_tr_b16 a[32:33], v155 offset:0\n"
        "v_mfma_f32_16x16x32_bf16 v[24:27], a[248:251], a[28:31], v[24:27]\n"
        "ds_read_b64_tr_b16 a[34:35], v155 offset:4096\n"
        "s_waitcnt lgkmcnt(2)\n"
        "v_mfma_f32_16x16x32_bf16 v[24:27], a[240:243], a[24:27], v[24:27]\n"
        "ds_read_b64_tr_b16 a[40:41], v155 offset:8192\n"
        "v_mfma_f32_16x16x32_bf16 v[32:35], a[240:243], a[192:195], v[32:35]\n"
        "ds_read_b64_tr_b16 a[42:43], v155 offset:12288\n"
        "global_load_lds_dwordx4 v254, s[60:61] offset:1024 sc1\n"
        "v_mfma_f32_16x16x32_bf16 v[32:35], a[248:251], a[196:199], v[32:35]\n"
        "ds_read_b64_tr_b16 a[36:37], v156 offset:0\n"
        "v_mfma_f32_16x16x32_bf16 v[36:39], a[252:255], a[196:199], v[36:39]\n"
        "ds_read_b64_tr_b16 a[38:39], v156 offset:4096\n"
        "v_mfma_f32_16x16x32_bf16 v[36:39], a[244:247], a[192:195], v[36:39]\n"
        "ds_read_b64_tr_b16 a[44:45], v156 offset:8192\n"
        "global_load_lds_dwordx4 v254, s[60:61] offset:2048 sc1\n"
        "v_mfma_f32_16x16x32_bf16 v[44:47], a[244:247], a[200:203], v[44:47]\n"
        "ds_read_b64_tr_b16 a[46:47], v156 offset:12288\n"
        "v_mfma_f32_16x16x32_bf16 v[44:47], a[252:255], a[204:207], v[44:47]\n"
        "ds_read_b128 a[0:3], v225 offset:16384\n"
        "v_mfma_f32_16x16x32_bf16 v[40:43], a[248:251], a[204:207], v[40:43]\n"
        "ds_read_b128 a[4:7], v225 offset:24576\n"
        "global_load_lds_dwordx4 v254, s[60:61] offset:3072 sc1\n"
        "s_add_u32 m0, s73, 0x20000\n"
        "v_mfma_f32_16x16x32_bf16 v[40:43], a[240:243], a[200:203], v[40:43]\n"
        "ds_read_b128 a[8:11], v225 offset:17408\n"
        "s_add_u32 s60, s20, s66\n"
        "s_addc_u32 s61, s21, 0\n"
        "v_mfma_f32_16x16x32_bf16 v[48:51], a[240:243], a[208:211], v[48:51]\n"
        "ds_read_b128 a[12:15], v225 offset:25600\n"
        "s_xor_b32 s66, s67, 30\n"
        "s_lshl_b32 s66, s66, 14\n"
        "v_mfma_f32_16x16x32_bf16 v[48:51], a[248:251], a[212:215], v[48:51]\n"
        "LHB3_%=:\n"
        "s_cbranch_execz LHRB_%=\n"
        "ds_read_b128 a[16:19], v225 offset:18432\n"
        "v_mfma_f32_16x16x32_bf16 v[52:55], a[252:255], a[212:215], v[52:55]\n"
        "ds_read_b128 a[20:23], v225 offset:26624\n"
        "v_mfma_f32_16x16x32_bf16 v[52:55], a[244:247], a[208:211], v[52:55]\n"
        "ds_read_b128 a[24:27], v225 offset:19456\n"
        "v_mfma_f32_16x16x32_bf16 v[60:63], a[244:247], a[216:219], v[60:63]\n"
        "ds_read_b128 a[28:31], v225 offset:27648\n"
        "s_waitcnt lgkmcnt(8)\n"
        "v_mfma_f32_16x16x32_bf16 v[60:63], a[252:255], a[220:223], v[60:63]\n"
        "ds_read_b128 a[192:195], v239 offset:16384\n"
        "s_waitcnt lgkmcnt(8)\n"
        "v_mfma_f32_16x16x32_bf16 v[56:59], a[248:251], a[220:223], v[56:59]\n"
        "ds_read_b128 a[196:199], v239 offset:24576\n"
        "s_waitcnt lgkmcnt(8)\n"
        "v_mfma_f32_16x16x32_bf16 v[56:59], a[240:243], a[216:219], v[56:59]\n"
        "ds_read_b128 a[200:203], v239 offset:17408\n"
        "s_waitcnt lgkmcnt(8)\n"
        "v_mfma_f32_16x16x32_bf16 v[0:3], a[32:35], a[0:3], v[0:3]\n"
        "ds_read_b128 a[204:207], v239 offset:25600\n"
        "s_waitcnt lgkmcnt(8)\n"
        "v_mfma_f32_16x16x32_bf16 v[0:3], a[40:43], a[4:7], v[0:3]\n"
        "ds_read_b128 a[208:211], v239 offset:18432\n"
        "s_waitcnt lgkmcnt(8)\n"
        "v_mfma_f32_16x16x32_bf16 v[4:7], a[44:47], a[4:7], v[4:7]\n"
        "ds_read_b128 a[212:215], v239 offset:26624\n"
        "s_waitcnt lgkmcnt(8)\n"
        "v_mfma_f32_16x16x32_bf16 v[4:7], a[36:39], a[0:3], v[4:7]\n"
        "ds_read_b128 a[216:219], v239 offset:19456\n"
        "s_waitcnt lgkmcnt(8)\n"
        "v_mfma_f32_16x16x32_bf16 v[12:15], a[36:39], a[8:11], v[12:15]\n"
        "ds_read_b128 a[220:223], v239 offset:27648\n"
        "s_waitcnt lgkmcnt(8)\n"
        "v_mfma_f32_16x16x32_bf16 v[12:15], a[44:47], a[12:15], v[12:15]\n"
        "s_waitcnt lgkmcnt(7)\n"
        "v_mfma_f32_16x16x32_bf16 v[8:11], a[40:43], a[12:15], v[8:11]\n"
        "v_mfma_f32_16x16x32_bf16 v[8:11], a[32:35], a[8:11], v[8:11]\n"
        "s_waitcnt lgkmcnt(5)\n"
        "v_mfma_f32_16x16x32_bf16 v[16:19], a[32:35], a[16:19], v[16:19]\n"
        "v_mfma_f32_16x16x32_bf16 v[16:19], a[40:43], a[20:23], v[16:19]\n"
        "v_mfma_f32_16x16x32_bf16 v[20:23], a[44:47], a[20:23], v[20:23]\n"
        "v_mfma_f32_16x16x32_bf16 v[20:23], a[36:39], a[16:19], v[20:23]\n"
        "s_waitcnt vmcnt(16)\n"
        "s_waitcnt lgkmcnt(1)\n"
        "v_mfma_f32_16x16x32_bf16 v[28:31], a[36:39], a[24:27], v[28:31]\n"
        "s_barrier\n"
        "v_mfma_f32_16x16x32_bf16 v[28:31], a[44:47], a[28:31], v[28:31]\n"
        "global_load_lds_dwordx4 v254, s[60:61] offset:0 sc1\n"
        "ds_read_b64_tr_b16 a[48:49], v153 offset:0\n"
        "v_mfma_f32_16x16x32_bf16 v[24:27], a[40:43], a[28:31], v[24:27]\n"
        "ds_read_b64_tr_b16 a[50:51], v153 offset:4096\n"
        "v_mfma_f32_16x16x32_bf16 v[24:27], a[32:35], a[24:27], v[24:27]\n"
        "ds_read_b64_tr_b16 a[56:57], v153 offset:8192\n"
        "v_mfma_f32_16x16x32_bf16 v[32:35], a[32:35], a[192:195], v[32:35]\n"
        "ds_read_b64_tr_b16 a[58:59], v153 offset:12288\n"
        "global_load_lds_dwordx4 v254, s[60:61] offset:1024 sc1\n"
        "v_mfma_f32_16x16x32_bf16 v[32:35], a[40:43], a[196:199], v[32:35]\n"
        "ds_read_b64_tr_b16 a[52:53], v154 offset:0\n"
        "v_mfma_f32_16x16x32_bf16 v[36:39], a[44:47], a[196:199], v[36:39]\n"
        "ds_read_b64_tr_b16 a[54:55], v154 offset:4096\n"
        "v_mfma_f32_16x16x32_bf16 v[36:39], a[36:39], a[192:195], v[36:39]\n"
        "ds_read_b64_tr_b16 a[60:61], v154 offset:8192\n"
        "global_load_lds_dwordx4 v254, s[60:61] offset:2048 sc1\n"
        "v_mfma_f32_16x16x32_bf16 v[44:47], a[36:39], a[200:203], v[44:47]\n"
        "ds_read_b64_tr_b16 a[62:63], v154 offset:12288\n"
        "s_waitcnt lgkmcnt(8)\n"
        "v_mfma_f32_16x16x32_bf16 v[44:47], a[44:47], a[204:207], v[44:47]\n"
        "ds_read_b128 a[0:3], v224 offset:16384\n"
        "v_mfma_f32_16x16x32_bf16 v[40:43], a[40:43], a[204:207], v[40:43]\n"
        "ds_read_b128 a[4:7], v224 offset:24576\n"
        "global_load_lds_dwordx4 v254, s[60:61] offset:3072 sc1\n"
        "s_add_u32 m0, s73, 0x24000\n"
        "v_mfma_f32_16x16x32_bf16 v[40:43], a[32:35], a[200:203], v[40:43]\n"
        "ds_read_b128 a[8:11], v224 offset:17408\n"
        "s_add_u32 s60, s20, s66\n"
        "s_addc_u32 s61, s21, 0\n"
        "v_mfma_f32_16x16x32_bf16 v[48:51], a[32:35], a[208:211], v[48:51]\n"
        "ds_read_b128 a[12:15], v224 offset:25600\n"
        "s_add_u32 s20, s20, s14\n"
        "s_addc_u32 s21, s21, 0\n"
        "v_mfma_f32_16x16x32_bf16 v[48:51], a[40:43], a[212:215], v[48:51]\n"
        "ds_read_b128 a[16:19], v224 offset:18432\n"
        "global_load_lds_dwordx4 v254, s[60:61] offset:0 sc1\n"
        "s_xor_b32 s66, s67, 24\n"
        "v_mfma_f32_16x16x32_bf16 v[52:55], a[44:47], a[212:215], v[52:55]\n"
        "ds_read_b128 a[20:23], v224 offset:26624\n"
        "s_sub_u32 s20, s20, 4096\n"
        "s_subb_u32 s21, s21, 0\n"
        "v_mfma_f32_16x16x32_bf16 v[52:55], a[36:39], a[208:211], v[52:55]\n"
        "ds_read_b128 a[24:27], v224 offset:19456\n"
        "s_lshl_b32 s66, s66, 14\n"
        "s_lshl2_add_u32 s14, s13, s40\n"
        "v_mfma_f32_16x16x32_bf16 v[60:63], a[36:39], a[216:219], v[60:63]\n"
        "ds_read_b128 a[28:31], v224 offset:27648\n"
        "global_load_lds_dwordx4 v254, s[60:61] offset:1024 sc1\n"
        "s_add_u32 s14, s14, 3072\n"
        "v_mfma_f32_16x16x32_bf16 v[60:63], a[44:47], a[220:223], v[60:63]\n"
        "ds_read_b128 a[192:195], v238 offset:16384\n"
        "s_lshl_b32 s14, s14, 7\n"
        "v_add_u32 v234, s14, v234\n"
        "v_mfma_f32_16x16x32_bf16 v[56:59], a[40:43], a[220:223], v[56:59]\n"
        "ds_read_b128 a[196:199], v238 offset:24576\n"
        "s_xor_b32 s14, s14, 0x200\n"
        "v_mov_b32 v235, s14\n"
        "v_mfma_f32_16x16x32_bf16 v[56:59], a[32:35], a[216:219], v[56:59]\n"
        "s_waitcnt lgkmcnt(7)\n"
        "global_load_lds_dwordx4 v254, s[60:61] offset:2048 sc1\n"
        "v_mfma_f32_16x16x32_bf16 v[0:3], a[48:51], a[0:3], v[0:3]\n"
        "ds_read_b128 a[200:203], v238 offset:17408\n"
        "s_waitcnt lgkmcnt(7)\n"
        "v_mfma_f32_16x16x32_bf16 v[0:3], a[56:59], a[4:7], v[0:3]\n"
        "ds_read_b128 a[204:207], v238 offset:25600\n"
        "s_waitcnt lgkmcnt(7)\n"
        "v_mfma_f32_16x16x32_bf16 v[4:7], a[60:63], a[4:7], v[4:7]\n"
        "global_load_lds_dwordx4 v254, s[60:61] offset:3072 sc1\n"
        "s_add_u32 m0, s73, 0x10000\n"
        "v_mfma_f32_16x16x32_bf16 v[4:7], a[52:55], a[0:3], v[4:7]\n"
        "ds_read_b128 a[208:211], v238 offset:18432\n"
        "s_add_u32 s60, s20, s66\n"
        "s_addc_u32 s61, s21, 0\n"
        "v_mfma_f32_16x16x32_bf16 v[12:15], a[52:55], a[8:11], v[12:15]\n"
        "ds_read_b128 a[212:215], v238 offset:26624\n"
        "s_xor_b32 s66, s67, 26\n"
        "s_lshl_b32 s66, s66, 14\n"
        "v_mfma_f32_16x16x32_bf16 v[12:15], a[60:63], a[12:15], v[12:15]\n"
        "ds_read_b128 a[216:219], v238 offset:19456\n"
        "s_waitcnt lgkmcnt(6)\n"
        "v_mfma_f32_16x16x32_bf16 v[8:11], a[56:59], a[12:15], v[8:11]\n"
        "ds_read_b128 a[220:223], v238 offset:27648\n"
        "v_mfma_f32_16x16x32_bf16 v[8:11], a[48:51], a[8:11], v[8:11]\n"
        "s_waitcnt lgkmcnt(6)\n"
        "v_mfma_f32_16x16x32_bf16 v[16:19], a[48:51], a[16:19], v[16:19]\n"
        "v_mfma_f32_16x16x32_bf16 v[16:19], a[56:59], a[20:23], v[16:19]\n"
        "s_waitcnt lgkmcnt(4)\n"
        "v_mfma_f32_16x16x32_bf16 v[20:23], a[60:63], a[20:23], v[20:23]\n"
        "v_mfma_f32_16x16x32_bf16 v[20:23], a[52:55], a[16:19], v[20:23]\n"
        "s_waitcnt vmcnt(16)\n"
        "v_mfma_f32_16x16x32_bf16 v[28:31], a[52:55], a[24:27], v[28:31]\n"
        "s_barrier\n"
        "v_mfma_f32_16x16x32_bf16 v[28:31], a[60:63], a[28:31], v[28:31]\n"
        "global_load_lds_dwordx4 v254, s[60:61] offset:0 sc1\n"
        "ds_read_b64_tr_b16 a[224:225], v153 offset:32768\n"
        "v_mfma_f32_16x16x32_bf16 v[24:27], a[56:59], a[28:31], v[24:27]\n"
        "ds_read_b64_tr_b16 a[226:227], v153 offset:36864\n"
        "s_waitcnt lgkmcnt(2)\n"
        "v_mfma_f32_16x16x32_bf16 v[24:27], a[48:51], a[24:27], v[24:27]\n"
        "ds_read_b64_tr_b16 a[232:233], v153 offset:40960\n"
        "v_mfma_f32_16x16x32_bf16 v[32:35], a[48:51], a[192:195], v[32:35]\n"
        "ds_read_b64_tr_b16 a[234:235], v153 offset:45056\n"
        "global_load_lds_dwordx4 v254, s[60:61] offset:1024 sc1\n"
        "v_mfma_f32_16x16x32_bf16 v[32:35], a[56:59], a[196:199], v[32:35]\n"
        "ds_read_b64_tr_b16 a[228:229], v154 offset:32768\n"
        "v_mfma_f32_16x16x32_bf16 v[36:39], a[60:63], a[196:199], v[36:39]\n"
        "ds_read_b64_tr_b16 a[230:231], v154 offset:36864\n"
        "v_mfma_f32_16x16x32_bf16 v[36:39], a[52:55], a[192:195], v[36:39]\n"
        "ds_read_b64_tr_b16 a[236:237], v154 offset:40960\n"
        "global_load_lds_dwordx4 v254, s[60:61] offset:2048 sc1\n"
        "v_mfma_f32_16x16x32_bf16 v[44:47], a[52:55], a[200:203], v[44:47]\n"
        "ds_read_b64_tr_b16 a[238:239], v154 offset:45056\n"
        "v_mfma_f32_16x16x32_bf16 v[44:47], a[60:63], a[204:207], v[44:47]\n"
        "ds_read_b128 a[0:3], v224 offset:49152\n"
        "v_mfma_f32_16x16x32_bf16 v[40:43], a[56:59], a[204:207], v[40:43]\n"
        "ds_read_b128 a[4:7], v224 offset:57344\n"
        "global_load_lds_dwordx4 v254, s[60:61] offset:3072 sc1\n"
        "s_add_u32 m0, s73, 0x14000\n"
        "v_mfma_f32_16x16x32_bf16 v[40:43], a[48:51], a[200:203], v[40:43]\n"
        "ds_read_b128 a[8:11], v224 offset:50176\n"
        "s_add_u32 s60, s20, s66\n"
        "s_addc_u32 s61, s21, 0\n"
        "v_mfma_f32_16x16x32_bf16 v[48:51], a[48:51], a[208:211], v[48:51]\n"
        "ds_read_b128 a[12:15], v224 offset:58368\n"
        "s_xor_b32 s66, s67, 28\n"
        "s_lshl_b32 s66, s66, 14\n"
        "v_mfma_f32_16x16x32_bf16 v[48:51], a[56:59], a[212:215], v[48:51]\n"
        "ds_read_b128 a[16:19], v224 offset:51200\n"
        "global_load_lds_dwordx4 v254, s[60:61] offset:0 sc1\n"
        "v_mfma_f32_16x16x32_bf16 v[52:55], a[60:63], a[212:215], v[52:55]\n"
        "ds_read_b128 a[20:23], v224 offset:59392\n"
        "v_mfma_f32_16x16x32_bf16 v[52:55], a[52:55], a[208:211], v[52:55]\n"
        "ds_read_b128 a[24:27], v224 offset:52224\n"
        "v_mfma_f32_16x16x32_bf16 v[60:63], a[52:55], a[216:219], v[60:63]\n"
        "ds_read_b128 a[28:31], v224 offset:60416\n"
        "global_load_lds_dwordx4 v254, s[60:61] offset:1024 sc1\n"
        "v_mfma_f32_16x16x32_bf16 v[60:63], a[60:63], a[220:223], v[60:63]\n"
        "ds_read_b128 a[192:195], v238 offset:49152\n"
        "s_waitcnt lgkmcnt(8)\n"
        "v_mfma_f32_16x16x32_bf16 v[56:59], a[56:59], a[220:223], v[56:59]\n"
        "ds_read_b128 a[196:199], v238 offset:57344\n"
        "s_waitcnt lgkmcnt(8)\n"
        "v_mfma_f32_16x16x32_bf16 v[56:59], a[48:51], a[216:219], v[56:59]\n"
        "global_load_lds_dwordx4 v254, s[60:61] offset:2048 sc1\n"
        "v_mfma_f32_16x16x32_bf16 v[0:3], a[224:227], a[0:3], v[0:3]\n"
        "ds_read_b128 a[200:203], v238 offset:50176\n"
        "s_waitcnt lgkmcnt(7)\n"
        "v_mfma_f32_16x16x32_bf16 v[0:3], a[232:235], a[4:7], v[0:3]\n"
        "ds_read_b128 a[204:207], v238 offset:58368\n"
        "s_waitcnt lgkmcnt(7)\n"
        "v_mfma_f32_16x16x32_bf16 v[4:7], a[236:239], a[4:7], v[4:7]\n"
        "global_load_lds_dwordx4 v254, s[60:61] offset:3072 sc1\n"
        "s_add_u32 m0, s73, 0x0\n"
        "v_mfma_f32_16x16x32_bf16 v[4:7], a[228:231], a[0:3], v[4:7]\n"
        "ds_read_b128 a[208:211], v238 offset:51200\n"
        "s_add_u32 s60, s20, s66\n"
        "s_addc_u32 s61, s21, 0\n"
        "v_mfma_f32_16x16x32_bf16 v[12:15], a[228:231], a[8:11], v[12:15]\n"
        "ds_read_b128 a[212:215], v238 offset:59392\n"
        "s_xor_b32 s66, s67, 30\n"
        "s_lshl_b32 s66, s66, 14\n"
        "v_mfma_f32_16x16x32_bf16 v[12:15], a[236:239], a[12:15], v[12:15]\n"
        "ds_read_b128 a[216:219], v238 offset:52224\n"
        "s_waitcnt lgkmcnt(6)\n"
        "v_mfma_f32_16x16x32_bf16 v[8:11], a[232:235], a[12:15], v[8:11]\n"
        "ds_read_b128 a[220:223], v238 offset:60416\n"
        "v_mfma_f32_16x16x32_bf16 v[8:11], a[224:227], a[8:11], v[8:11]\n"
        "s_waitcnt lgkmcnt(6)\n"
        "v_mfma_f32_16x16x32_bf16 v[16:19], a[224:227], a[16:19], v[16:19]\n"
        "v_mfma_f32_16x16x32_bf16 v[16:19], a[232:235], a[20:23], v[16:19]\n"
        "s_waitcnt lgkmcnt(4)\n"
        "v_mfma_f32_16x16x32_bf16 v[20:23], a[236:239], a[20:23], v[20:23]\n"
        "v_mfma_f32_16x16x32_bf16 v[20:23], a[228:231], a[16:19], v[20:23]\n"
        "s_waitcnt vmcnt(20)\n"
        "v_mfma_f32_16x16x32_bf16 v[28:31], a[228:231], a[24:27], v[28:31]\n"
        "s_barrier\n"
        "v_mfma_f32_16x16x32_bf16 v[28:31], a[236:239], a[28:31], v[28:31]\n"
        "global_load_lds_dwordx4 v254, s[60:61] offset:0 sc1\n"
        "ds_read_b128 a[0:3], v242 offset:32768\n"
        "v_mfma_f32_16x16x32_bf16 v[24:27], a[232:235], a[28:31], v[24:27]\n"
        "ds_read_b128 a[4:7], v242 offset:36864\n"
        "s_waitcnt lgkmcnt(2)\n"
        "v_mfma_f32_16x16x32_bf16 v[24:27], a[224:227], a[24:27], v[24:27]\n"
        "ds_read_b128 a[8:11], v242 offset:33792\n"
        "v_mfma_f32_16x16x32_bf16 v[32:35], a[224:227], a[192:195], v[32:35]\n"
        "global_load_lds_dwordx4 v254, s[60:61] offset:1024 sc1\n"
        "ds_read_b128 a[12:15], v242 offset:37888\n"
        "v_mfma_f32_16x16x32_bf16 v[32:35], a[232:235], a[196:199], v[32:35]\n"
        "ds_read_b128 a[16:19], v242 offset:34816\n"
        "v_mfma_f32_16x16x32_bf16 v[36:39], a[236:239], a[196:199], v[36:39]\n"
        "ds_read_b128 a[20:23], v242 offset:38912\n"
        "v_mfma_f32_16x16x32_bf16 v[36:39], a[228:231], a[192:195], v[36:39]\n"
        "global_load_lds_dwordx4 v254, s[60:61] offset:2048 sc1\n"
        "ds_read_b128 a[24:27], v242 offset:35840\n"
        "v_mfma_f32_16x16x32_bf16 v[44:47], a[228:231], a[200:203], v[44:47]\n"
        "ds_read_b128 a[28:31], v242 offset:39936\n"
        "v_mfma_f32_16x16x32_bf16 v[44:47], a[236:239], a[204:207], v[44:47]\n"
        "ds_read_b128 a[192:195], v242 offset:40960\n"
        "s_waitcnt lgkmcnt(8)\n"
        "v_mfma_f32_16x16x32_bf16 v[40:43], a[232:235], a[204:207], v[40:43]\n"
        "global_load_lds_dwordx4 v254, s[60:61] offset:3072 sc1\n"
        "s_add_u32 m0, s73, 0x4000\n"
        "v_mfma_f32_16x16x32_bf16 v[40:43], a[224:227], a[200:203], v[40:43]\n"
        "s_add_u32 s60, s20, s66\n"
        "s_addc_u32 s61, s21, 0\n"
        "s_waitcnt lgkmcnt(6)\n"
        "v_mfma_f32_16x16x32_bf16 v[48:51], a[224:227], a[208:211], v[48:51]\n"
        "ds_read_b128 a[196:199], v242 offset:45056\n"
        "v_mfma_f32_16x16x32_bf16 v[48:51], a[232:235], a[212:215], v[48:51]\n"
        "global_load_lds_dwordx4 v254, s[60:61] offset:0 sc1\n"
        "ds_read_b128 a[200:203], v242 offset:41984\n"
        "v_mfma_f32_16x16x32_bf16 v[52:55], a[236:239], a[212:215], v[52:55]\n"
        "ds_read_b128 a[204:207], v242 offset:46080\n"
        "v_mfma_f32_16x16x32_bf16 v[52:55], a[228:231], a[208:211], v[52:55]\n"
        "ds_read_b128 a[208:211], v242 offset:43008\n"
        "s_waitcnt lgkmcnt(6)\n"
        "v_mfma_f32_16x16x32_bf16 v[60:63], a[228:231], a[216:219], v[60:63]\n"
        "global_load_lds_dwordx4 v254, s[60:61] offset:1024 sc1\n"
        "ds_read_b128 a[212:215], v242 offset:47104\n"
        "v_mfma_f32_16x16x32_bf16 v[60:63], a[236:239], a[220:223], v[60:63]\n"
        "v_mfma_f32_16x16x32_bf16 v[56:59], a[232:235], a[220:223], v[56:59]\n"
        "ds_read_b128 a[220:223], v242 offset:48128\n"
        "v_mfma_f32_16x16x32_bf16 v[56:59], a[224:227], a[216:219], v[56:59]\n"
        "global_load_lds_dwordx4 v254, s[60:61] offset:2048 sc1\n"
        "ds_read_b128 a[216:219], v242 offset:44032\n"
        "v_mfma_f32_16x16x32_bf16 v[0:3], a[64:67], a[0:3], v[0:3]\n"
        "v_mfma_f32_16x16x32_bf16 v[0:3], a[72:75], a[4:7], v[0:3]\n"
        "v_mfma_f32_16x16x32_bf16 v[4:7], a[76:79], a[4:7], v[4:7]\n"
        "global_load_lds_dwordx4 v254, s[60:61] offset:3072 sc1\n"
        "s_add_u32 m0, s73, 0x8000\n"
        "v_mfma_f32_16x16x32_bf16 v[4:7], a[68:71], a[0:3], v[4:7]\n"
        "s_waitcnt lgkmcnt(3)\n"
        "v_mfma_f32_16x16x32_bf16 v[12:15], a[68:71], a[8:11], v[12:15]\n"
        "v_mfma_f32_16x16x32_bf16 v[12:15], a[76:79], a[12:15], v[12:15]\n"
        "v_mfma_f32_16x16x32_bf16 v[8:11], a[72:75], a[12:15], v[8:11]\n"
        "v_mfma_f32_16x16x32_bf16 v[8:11], a[64:67], a[8:11], v[8:11]\n"
        "v_mfma_f32_16x16x32_bf16 v[16:19], a[64:67], a[16:19], v[16:19]\n"
        "v_mfma_f32_16x16x32_bf16 v[16:19], a[72:75], a[20:23], v[16:19]\n"
        "v_mfma_f32_16x16x32_bf16 v[20:23], a[76:79], a[20:23], v[20:23]\n"
        "v_mfma_f32_16x16x32_bf16 v[20:23], a[68:71], a[16:19], v[20:23]\n"
        "s_waitcnt vmcnt(24)\n"
        "v_mfma_f32_16x16x32_bf16 v[28:31], a[68:71], a[24:27], v[28:31]\n"
        "s_barrier\n"
        "v_mfma_f32_16x16x32_bf16 v[28:31], a[76:79], a[28:31], v[28:31]\n"
        "global_load_lds_dwordx4 v148, s[58:59] offset:0 sc1\n"
        "ds_read_b128 a[0:3], v242 offset:49152\n"
        "s_add_u32 m0, s73, 0x8400\n"
        "v_mfma_f32_16x16x32_bf16 v[24:27], a[72:75], a[28:31], v[24:27]\n"
        "ds_read_b128 a[4:7], v242 offset:53248\n"
        "s_add_u32 s58, s58, 0x8000\n"
        "s_addc_u32 s59, s59, 0\n"
        "v_mfma_f32_16x16x32_bf16 v[24:27], a[64:67], a[24:27], v[24:27]\n"
        "ds_read_b128 a[8:11], v242 offset:50176\n"
        "s_waitcnt lgkmcnt(3)\n"
        "v_mfma_f32_16x16x32_bf16 v[0:3], a[80:83], a[192:195], v[0:3]\n"
        "global_load_lds_dwordx4 v148, s[58:59] offset:0 sc1\n"
        "ds_read_b128 a[12:15], v242 offset:54272\n"
        "s_add_u32 m0, s73, 0x8800\n"
        "v_mfma_f32_16x16x32_bf16 v[0:3], a[88:91], a[196:199], v[0:3]\n"
        "s_add_u32 s58, s58, 0x8000\n"
        "s_addc_u32 s59, s59, 0\n"
        "v_mfma_f32_16x16x32_bf16 v[4:7], a[92:95], a[196:199], v[4:7]\n"
        "ds_read_b128 a[16:19], v242 offset:51200\n"
        "v_mfma_f32_16x16x32_bf16 v[4:7], a[84:87], a[192:195], v[4:7]\n"
        "global_load_lds_dwordx4 v148, s[58:59] offset:0 sc1\n"
        "ds_read_b128 a[20:23], v242 offset:55296\n"
        "s_add_u32 m0, s73, 0x8c00\n"
        "v_mfma_f32_16x16x32_bf16 v[12:15], a[84:87], a[200:203], v[12:15]\n"
        "s_add_u32 s58, s58, 0x8000\n"
        "s_addc_u32 s59, s59, 0\n"
        "v_mfma_f32_16x16x32_bf16 v[12:15], a[92:95], a[204:207], v[12:15]\n"
        "ds_read_b128 a[24:27], v242 offset:52224\n"
        "s_waitcnt lgkmcnt(6)\n"
        "v_mfma_f32_16x16x32_bf16 v[8:11], a[88:91], a[204:207], v[8:11]\n"
        "global_load_lds_dwordx4 v148, s[58:59] offset:0 sc1\n"
        "ds_read_b128 a[28:31], v242 offset:56320\n"
        "v_mfma_f32_16x16x32_bf16 v[8:11], a[80:83], a[200:203], v[8:11]\n"
        "ds_read_b128 a[192:195], v242 offset:57344\n"
        "s_waitcnt lgkmcnt(6)\n"
        "v_mfma_f32_16x16x32_bf16 v[16:19], a[80:83], a[208:211], v[16:19]\n"
        "ds_read_b128 a[196:199], v242 offset:61440\n"
        "v_mfma_f32_16x16x32_bf16 v[16:19], a[88:91], a[212:215], v[16:19]\n"
        "global_load_dwordx4 v[112:115], v151, s[64:65]\n"
        "s_add_u32 s64, s64, 0x8000\n"
        "s_addc_u32 s65, s65, 0\n"
        "v_mfma_f32_16x16x32_bf16 v[20:23], a[92:95], a[212:215], v[20:23]\n"
        "ds_read_b128 a[200:203], v242 offset:58368\n"
        "v_mfma_f32_16x16x32_bf16 v[20:23], a[84:87], a[208:211], v[20:23]\n"
        "ds_read_b128 a[204:207], v242 offset:62464\n"
        "s_waitcnt lgkmcnt(6)\n"
        "v_mfma_f32_16x16x32_bf16 v[28:31], a[84:87], a[216:219], v[28:31]\n"
        "global_load_dwordx4 v[116:119], v151, s[64:65]\n"
        "s_add_u32 s64, s64, 0x8000\n"
        "s_addc_u32 s65, s65, 0\n"
        "v_mfma_f32_16x16x32_bf16 v[28:31], a[92:95], a[220:223], v[28:31]\n"
        "ds_read_b128 a[208:211], v242 offset:59392\n"
        "v_mfma_f32_16x16x32_bf16 v[24:27], a[88:91], a[220:223], v[24:27]\n"
        "ds_read_b128 a[212:215], v242 offset:63488\n"
        "v_mfma_f32_16x16x32_bf16 v[24:27], a[80:83], a[216:219], v[24:27]\n"
        "global_load_dwordx4 v[120:123], v151, s[64:65]\n"
        "s_add_u32 s64, s64, 0x8000\n"
        "s_addc_u32 s65, s65, 0\n"
        "v_mfma_f32_16x16x32_bf16 v[0:3], a[96:99], a[0:3], v[0:3]\n"
        "ds_read_b128 a[216:219], v242 offset:60416\n"
        "v_mfma_f32_16x16x32_bf16 v[0:3], a[104:107], a[4:7], v[0:3]\n"
        "ds_read_b128 a[220:223], v242 offset:64512\n"
        "s_waitcnt lgkmcnt(6)\n"
        "v_mfma_f32_16x16x32_bf16 v[4:7], a[108:111], a[4:7], v[4:7]\n"
        "global_load_dwordx4 v[124:127], v151, s[64:65]\n"
        "v_mfma_f32_16x16x32_bf16 v[4:7], a[100:103], a[0:3], v[4:7]\n"
        "v_mfma_f32_16x16x32_bf16 v[12:15], a[100:103], a[8:11], v[12:15]\n"
        "v_mfma_f32_16x16x32_bf16 v[12:15], a[108:111], a[12:15], v[12:15]\n"
        "global_load_dword v128, v159, s[48:49] offset:64 sc1\n"
        "v_mfma_f32_16x16x32_bf16 v[8:11], a[104:107], a[12:15], v[8:11]\n"
        "v_mfma_f32_16x16x32_bf16 v[8:11], a[96:99], a[8:11], v[8:11]\n"
        "v_mfma_f32_16x16x32_bf16 v[16:19], a[96:99], a[16:19], v[16:19]\n"
        "v_mfma_f32_16x16x32_bf16 v[16:19], a[104:107], a[20:23], v[16:19]\n"
        "s_waitcnt lgkmcnt(0)\n"
        "v_mfma_f32_16x16x32_bf16 v[20:23], a[108:111], a[20:23], v[20:23]\n"
        "v_mfma_f32_16x16x32_bf16 v[20:23], a[100:103], a[16:19], v[20:23]\n"
        "s_waitcnt vmcnt(29)\n"
        "v_mfma_f32_16x16x32_bf16 v[28:31], a[100:103], a[24:27], v[28:31]\n"
        "s_barrier\n"
        "v_mfma_f32_16x16x32_bf16 v[28:31], a[108:111], a[28:31], v[28:31]\n"
        "ds_read_b128 a[0:3], v243 offset:0\n"
        "v_mfma_f32_16x16x32_bf16 v[24:27], a[104:107], a[28:31], v[24:27]\n"
        "ds_read_b128 a[4:7], v243 offset:4096\n"
        "v_mfma_f32_16x16x32_bf16 v[24:27], a[96:99], a[24:27], v[24:27]\n"
        "ds_read_b128 a[8:11], v243 offset:1024\n"
        "v_mfma_f32_16x16x32_bf16 v[0:3], a[112:115], a[192:195], v[0:3]\n"
        "ds_read_b128 a[12:15], v243 offset:5120\n"
        "v_mfma_f32_16x16x32_bf16 v[0:3], a[120:123], a[196:199], v[0:3]\n"
        "ds_read_b128 a[16:19], v243 offset:2048\n"
        "v_mfma_f32_16x16x32_bf16 v[4:7], a[124:127], a[196:199], v[4:7]\n"
        "ds_read_b128 a[20:23], v243 offset:6144\n"
        "v_mfma_f32_16x16x32_bf16 v[4:7], a[116:119], a[192:195], v[4:7]\n"
        "ds_read_b128 a[24:27], v243 offset:3072\n"
        "v_mfma_f32_16x16x32_bf16 v[12:15], a[116:119], a[200:203], v[12:15]\n"
        "ds_read_b128 a[28:31], v243 offset:7168\n"
        "v_mfma_f32_16x16x32_bf16 v[12:15], a[124:127], a[204:207], v[12:15]\n"
        "ds_read_b128 a[192:195], v243 offset:8192\n"
        "s_waitcnt lgkmcnt(8)\n"
        "v_mfma_f32_16x16x32_bf16 v[8:11], a[120:123], a[204:207], v[8:11]\n"
        "ds_read_b128 a[196:199], v243 offset:12288\n"
        "v_mfma_f32_16x16x32_bf16 v[8:11], a[112:115], a[200:203], v[8:11]\n"
        "ds_read_b128 a[200:203], v243 offset:9216\n"
        "s_waitcnt lgkmcnt(8)\n"
        "v_mfma_f32_16x16x32_bf16 v[16:19], a[112:115], a[208:211], v[16:19]\n"
        "ds_read_b128 a[204:207], v243 offset:13312\n"
        "v_mfma_f32_16x16x32_bf16 v[16:19], a[120:123], a[212:215], v[16:19]\n"
        "v_mfma_f32_16x16x32_bf16 v[20:23], a[124:127], a[212:215], v[20:23]\n"
        "ds_read_b128 a[212:215], v243 offset:14336\n"
        "v_mfma_f32_16x16x32_bf16 v[20:23], a[116:119], a[208:211], v[20:23]\n"
        "ds_read_b128 a[208:211], v243 offset:10240\n"
        "s_waitcnt lgkmcnt(7)\n"
        "v_mfma_f32_16x16x32_bf16 v[28:31], a[116:119], a[216:219], v[28:31]\n"
        "v_mfma_f32_16x16x32_bf16 v[28:31], a[124:127], a[220:223], v[28:31]\n"
        "v_mfma_f32_16x16x32_bf16 v[24:27], a[120:123], a[220:223], v[24:27]\n"
        "ds_read_b128 a[220:223], v243 offset:15360\n"
        "v_mfma_f32_16x16x32_bf16 v[24:27], a[112:115], a[216:219], v[24:27]\n"
        "ds_read_b128 a[216:219], v243 offset:11264\n"
        "v_mfma_f32_16x16x32_bf16 v[0:3], a[128:131], a[0:3], v[0:3]\n"
        "v_mfma_f32_16x16x32_bf16 v[0:3], a[136:139], a[4:7], v[0:3]\n"
        "v_mfma_f32_16x16x32_bf16 v[4:7], a[140:143], a[4:7], v[4:7]\n"
        "v_mfma_f32_16x16x32_bf16 v[4:7], a[132:135], a[0:3], v[4:7]\n"
        "s_waitcnt lgkmcnt(2)\n"
        "v_mfma_f32_16x16x32_bf16 v[12:15], a[132:135], a[8:11], v[12:15]\n"
        "v_mfma_f32_16x16x32_bf16 v[12:15], a[140:143], a[12:15], v[12:15]\n"
        "v_mfma_f32_16x16x32_bf16 v[8:11], a[136:139], a[12:15], v[8:11]\n"
        "v_mfma_f32_16x16x32_bf16 v[8:11], a[128:131], a[8:11], v[8:11]\n"
        "v_mfma_f32_16x16x32_bf16 v[16:19], a[128:131], a[16:19], v[16:19]\n"
        "v_mfma_f32_16x16x32_bf16 v[16:19], a[136:139], a[20:23], v[16:19]\n"
        "v_mfma_f32_16x16x32_bf16 v[20:23], a[140:143], a[20:23], v[20:23]\n"
        "v_mfma_f32_16x16x32_bf16 v[20:23], a[132:135], a[16:19], v[20:23]\n"
        "s_waitcnt vmcnt(25)\n"
        "v_mfma_f32_16x16x32_bf16 v[28:31], a[132:135], a[24:27], v[28:31]\n"
        "s_barrier\n"
        "v_mfma_f32_16x16x32_bf16 v[28:31], a[140:143], a[28:31], v[28:31]\n"
        "ds_read_b128 a[0:3], v243 offset:16384\n"
        "v_mfma_f32_16x16x32_bf16 v[24:27], a[136:139], a[28:31], v[24:27]\n"
        "ds_read_b128 a[4:7], v243 offset:20480\n"
        "v_mfma_f32_16x16x32_bf16 v[24:27], a[128:131], a[24:27], v[24:27]\n"
        "ds_read_b128 a[8:11], v243 offset:17408\n"
        "v_mfma_f32_16x16x32_bf16 v[0:3], a[144:147], a[192:195], v[0:3]\n"
        "ds_read_b128 a[12:15], v243 offset:21504\n"
        "v_mfma_f32_16x16x32_bf16 v[0:3], a[152:155], a[196:199], v[0:3]\n"
        "ds_read_b128 a[16:19], v243 offset:18432\n"
        "s_waitcnt lgkmcnt(5)\n"
        "v_mfma_f32_16x16x32_bf16 v[4:7], a[156:159], a[196:199], v[4:7]\n"
        "ds_read_b128 a[20:23], v243 offset:22528\n"
        "v_mfma_f32_16x16x32_bf16 v[4:7], a[148:151], a[192:195], v[4:7]\n"
        "ds_read_b128 a[24:27], v243 offset:19456\n"
        "v_mfma_f32_16x16x32_bf16 v[12:15], a[148:151], a[200:203], v[12:15]\n"
        "ds_read_b128 a[28:31], v243 offset:23552\n"
        "v_mfma_f32_16x16x32_bf16 v[12:15], a[156:159], a[204:207], v[12:15]\n"
        "ds_read_b128 a[192:195], v243 offset:24576\n"
        "s_waitcnt lgkmcnt(8)\n"
        "v_mfma_f32_16x16x32_bf16 v[8:11], a[152:155], a[204:207], v[8:11]\n"
        "ds_read_b128 a[196:199], v243 offset:28672\n"
        "v_mfma_f32_16x16x32_bf16 v[8:11], a[144:147], a[200:203], v[8:11]\n"
        "ds_read_b128 a[200:203], v243 offset:25600\n"
        "s_waitcnt lgkmcnt(8)\n"
        "v_mfma_f32_16x16x32_bf16 v[16:19], a[144:147], a[208:211], v[16:19]\n"
        "ds_read_b128 a[204:207], v243 offset:29696\n"
        "v_mfma_f32_16x16x32_bf16 v[16:19], a[152:155], a[212:215], v[16:19]\n"
        "v_mfma_f32_16x16x32_bf16 v[20:23], a[156:159], a[212:215], v[20:23]\n"
        "ds_read_b128 a[212:215], v243 offset:30720\n"
        "v_mfma_f32_16x16x32_bf16 v[20:23], a[148:151], a[208:211], v[20:23]\n"
        "ds_read_b128 a[208:211], v243 offset:26624\n"
        "s_waitcnt lgkmcnt(7)\n"
        "v_mfma_f32_16x16x32_bf16 v[28:31], a[148:151], a[216:219], v[28:31]\n"
        "v_mfma_f32_16x16x32_bf16 v[28:31], a[156:159], a[220:223], v[28:31]\n"
        "v_mfma_f32_16x16x32_bf16 v[24:27], a[152:155], a[220:223], v[24:27]\n"
        "ds_read_b128 a[220:223], v243 offset:31744\n"
        "v_mfma_f32_16x16x32_bf16 v[24:27], a[144:147], a[216:219], v[24:27]\n"
        "ds_read_b128 a[216:219], v243 offset:27648\n"
        "v_mfma_f32_16x16x32_bf16 v[0:3], a[160:163], a[0:3], v[0:3]\n"
        "v_mfma_f32_16x16x32_bf16 v[0:3], a[168:171], a[4:7], v[0:3]\n"
        "v_mfma_f32_16x16x32_bf16 v[4:7], a[172:175], a[4:7], v[4:7]\n"
        "v_mfma_f32_16x16x32_bf16 v[4:7], a[164:167], a[0:3], v[4:7]\n"
        "s_waitcnt lgkmcnt(2)\n"
        "v_mfma_f32_16x16x32_bf16 v[12:15], a[164:167], a[8:11], v[12:15]\n"
        "v_mfma_f32_16x16x32_bf16 v[12:15], a[172:175], a[12:15], v[12:15]\n"
        "v_mfma_f32_16x16x32_bf16 v[8:11], a[168:171], a[12:15], v[8:11]\n"
        "v_mfma_f32_16x16x32_bf16 v[8:11], a[160:163], a[8:11], v[8:11]\n"
        "v_mfma_f32_16x16x32_bf16 v[16:19], a[160:163], a[16:19], v[16:19]\n"
        "v_mfma_f32_16x16x32_bf16 v[16:19], a[168:171], a[20:23], v[16:19]\n"
        "v_mfma_f32_16x16x32_bf16 v[20:23], a[172:175], a[20:23], v[20:23]\n"
        "v_mfma_f32_16x16x32_bf16 v[20:23], a[164:167], a[16:19], v[20:23]\n"
        "s_waitcnt vmcnt(21)\n"
        "v_mfma_f32_16x16x32_bf16 v[28:31], a[164:167], a[24:27], v[28:31]\n"
        "s_barrier\n"
        "v_mfma_f32_16x16x32_bf16 v[28:31], a[172:175], a[28:31], v[28:31]\n"
        "ds_read_b128 a[0:3], v242 offset:0\n"
        "v_mfma_f32_16x16x32_bf16 v[24:27], a[168:171], a[28:31], v[24:27]\n"
        "ds_read_b128 a[4:7], v242 offset:4096\n"
        "v_mfma_f32_16x16x32_bf16 v[24:27], a[160:163], a[24:27], v[24:27]\n"
        "ds_read_b128 a[8:11], v242 offset:1024\n"
        "s_waitcnt lgkmcnt(3)\n"
        "v_mfma_f32_16x16x32_bf16 v[0:3], a[176:179], a[192:195], v[0:3]\n"
        "ds_read_b128 a[12:15], v242 offset:5120\n"
        "v_mfma_f32_16x16x32_bf16 v[0:3], a[184:187], a[196:199], v[0:3]\n"
        "ds_read_b128 a[16:19], v242 offset:2048\n"
        "v_mfma_f32_16x16x32_bf16 v[4:7], a[188:191], a[196:199], v[4:7]\n"
        "ds_read_b128 a[20:23], v242 offset:6144\n"
        "v_mfma_f32_16x16x32_bf16 v[4:7], a[180:183], a[192:195], v[4:7]\n"
        "ds_read_b128 a[24:27], v242 offset:3072\n"
        "v_mfma_f32_16x16x32_bf16 v[12:15], a[180:183], a[200:203], v[12:15]\n"
        "ds_read_b128 a[28:31], v242 offset:7168\n"
        "s_waitcnt vmcnt(5)\n"
        "v_mfma_f32_16x16x32_bf16 v[12:15], a[188:191], a[204:207], v[12:15]\n"
        "s_waitcnt lgkmcnt(7)\n"
        "v_cvt_pk_bf16_f32 v0, v0, v1\n"
        "v_cvt_pk_bf16_f32 v1, v2, v3\n"
        "v_mfma_f32_16x16x32_bf16 v[8:11], a[184:187], a[204:207], v[8:11]\n"
        "ds_read_b128 a[192:195], v242 offset:8192\n"
        "v_cvt_pk_bf16_f32 v2, v4, v5\n"
        "v_mfma_f32_16x16x32_bf16 v[16:19], a[176:179], a[208:211], v[16:19]\n"
        "s_waitcnt vmcnt(4) lgkmcnt(6)\n"
        "v_cvt_pk_bf16_f32 v3, v6, v7\n"
        "v_mfma_f32_16x16x32_bf16 v[24:27], a[184:187], a[220:223], v[24:27]\n"
        "global_store_dwordx4 v254, v[0:3], s[28:29] offset:0 sc1\n"
        "ds_read_b128 a[196:199], v242 offset:12288\n"
        "ds_read_b128 a[204:207], v242 offset:13312\n"
        "v_mfma_f32_16x16x32_bf16 v[8:11], a[176:179], a[200:203], v[8:11]\n"
        "ds_read_b128 a[200:203], v242 offset:9216\n"
        "s_waitcnt vmcnt(4) lgkmcnt(6)\n"
        "v_mfma_f32_16x16x32_bf16 v[16:19], a[184:187], a[212:215], v[16:19]\n"
        "ds_read_b64_tr_b16 v[64:65], v149 offset:0\n"
        "v_mfma_f32_16x16x32_bf16 v[24:27], a[176:179], a[216:219], v[24:27]\n"
        "ds_read_b64_tr_b16 v[66:67], v149 offset:2048\n"
        "v_mfma_f32_16x16x32_bf16 v[20:23], a[188:191], a[212:215], v[20:23]\n"
        "ds_read_b128 a[212:215], v242 offset:14336\n"
        "v_cvt_pk_bf16_f32 v8, v8, v9\n"
        "v_mfma_f32_16x16x32_bf16 v[28:31], a[180:183], a[216:219], v[28:31]\n"
        "s_waitcnt vmcnt(3) lgkmcnt(7)\n"
        "v_cvt_pk_bf16_f32 v16, v16, v17\n"
        "v_cvt_pk_bf16_f32 v9, v10, v11\n"
        "v_mfma_f32_16x16x32_bf16 v[20:23], a[180:183], a[208:211], v[20:23]\n"
        "ds_read_b128 a[208:211], v242 offset:10240\n"
        "v_cvt_pk_bf16_f32 v24, v24, v25\n"
        "v_mfma_f32_16x16x32_bf16 v[28:31], a[188:191], a[220:223], v[28:31]\n"
        "s_waitcnt vmcnt(2) lgkmcnt(7)\n"
        "v_cvt_pk_bf16_f32 v17, v18, v19\n"
        "v_cvt_pk_bf16_f32 v25, v26, v27\n"
        "v_mfma_f32_16x16x32_bf16 v[32:35], a[64:67], a[0:3], v[32:35]\n"
        "ds_read_b128 a[216:219], v242 offset:11264\n"
        "v_cvt_pk_bf16_f32 v10, v12, v13\n"
        "v_mfma_f32_16x16x32_bf16 v[32:35], a[72:75], a[4:7], v[32:35]\n"
        "ds_read_b128 a[220:223], v242 offset:15360\n"
        "v_cvt_pk_bf16_f32 v11, v14, v15\n"
        "v_mfma_f32_16x16x32_bf16 v[36:39], a[76:79], a[4:7], v[36:39]\n"
        "s_waitcnt vmcnt(1) lgkmcnt(5)\n"
        "v_cvt_pk_bf16_f32 v18, v20, v21\n"
        "v_cvt_pk_bf16_f32 v19, v22, v23\n"
        "v_mfma_f32_16x16x32_bf16 v[36:39], a[68:71], a[0:3], v[36:39]\n"
        "s_waitcnt lgkmcnt(4)\n"
        "v_cvt_pk_bf16_f32 v26, v28, v29\n"
        "v_cvt_pk_bf16_f32 v27, v30, v31\n"
        "v_mfma_f32_16x16x32_bf16 v[44:47], a[68:71], a[8:11], v[44:47]\n"
        "global_store_dwordx4 v254, v[8:11], s[28:29] offset:1024 sc1\n"
        "ds_read_b64_tr_b16 v[68:69], v149 offset:32\n"
        "ds_read_b64_tr_b16 v[70:71], v149 offset:2080\n"
        "v_mov_b32 v129, v128\n"
        "v_mfma_f32_16x16x32_bf16 v[44:47], a[76:79], a[12:15], v[44:47]\n"
        "ds_read_b64_tr_b16 v[72:73], v149 offset:64\n"
        "v_lshlrev_b32 v80, 16, v64\n"
        "v_mfma_f32_16x16x32_bf16 v[40:43], a[72:75], a[12:15], v[40:43]\n"
        "ds_read_b64_tr_b16 v[74:75], v149 offset:2112\n"
        "v_and_b32 v81, 0xffff0000, v64\n"
        "v_mfma_f32_16x16x32_bf16 v[40:43], a[64:67], a[8:11], v[40:43]\n"
        "global_store_dwordx4 v254, v[16:19], s[28:29] offset:2048 sc1\n"
        "ds_read_b64_tr_b16 v[76:77], v149 offset:96\n"
        "ds_read_b64_tr_b16 v[78:79], v149 offset:2144\n"
        "v_lshlrev_b32 v82, 16, v65\n"
        "v_mfma_f32_16x16x32_bf16 v[48:51], a[64:67], a[16:19], v[48:51]\n"
        "v_permlane16_swap_b32 v128, v129\n"
        "v_mfma_f32_16x16x32_bf16 v[48:51], a[72:75], a[20:23], v[48:51]\n"
        "LHD0_%=:\n"
        "s_cbranch_execz LHRD_%=\n"
        "v_mov_b32 v130, v128\n"
        "v_mov_b32 v131, v129\n"
        "v_mfma_f32_16x16x32_bf16 v[52:55], a[76:79], a[20:23], v[52:55]\n"
        "global_store_dwordx4 v254, v[24:27], s[28:29] offset:3072 sc1\n"
        "v_permlane32_swap_b32 v128, v130\n"
        "v_permlane32_swap_b32 v129, v131\n"
        "v_mfma_f32_16x16x32_bf16 v[52:55], a[68:71], a[16:19], v[52:55]\n"
        "v_permlane16_swap_b32 v112, v114\n"
        "v_mfma_f32_16x16x32_bf16 v[60:63], a[68:71], a[24:27], v[60:63]\n"
        "s_barrier\n"
        "v_mfma_f32_16x16x32_bf16 v[60:63], a[76:79], a[28:31], v[60:63]\n"
        "ds_read_b128 a[0:3], v242 offset:16384\n"
        "v_sub_f32 v132, 1.0, v128\n"
        "v_mfma_f32_16x16x32_bf16 v[56:59], a[72:75], a[28:31], v[56:59]\n"
        "ds_read_b128 a[4:7], v242 offset:20480\n"
        "v_cvt_pk_bf16_f32 v132, 0, v132\n"
        "v_mfma_f32_16x16x32_bf16 v[56:59], a[64:67], a[24:27], v[56:59]\n"
        "ds_read_b128 a[8:11], v242 offset:17408\n"
        "v_sub_f32 v133, 1.0, v129\n"
        "v_mfma_f32_16x16x32_bf16 v[32:35], a[80:83], a[192:195], v[32:35]\n"
        "ds_read_b128 a[12:15], v242 offset:21504\n"
        "v_cvt_pk_bf16_f32 v133, 0, v133\n"
        "v_mfma_f32_16x16x32_bf16 v[32:35], a[88:91], a[196:199], v[32:35]\n"
        "ds_read_b128 a[16:19], v242 offset:18432\n"
        "v_sub_f32 v134, 1.0, v130\n"
        "v_mfma_f32_16x16x32_bf16 v[36:39], a[92:95], a[196:199], v[36:39]\n"
        "ds_read_b128 a[20:23], v242 offset:22528\n"
        "v_cvt_pk_bf16_f32 v134, 0, v134\n"
        "v_mfma_f32_16x16x32_bf16 v[36:39], a[84:87], a[192:195], v[36:39]\n"
        "ds_read_b128 a[24:27], v242 offset:19456\n"
        "v_sub_f32 v135, 1.0, v131\n"
        "v_mfma_f32_16x16x32_bf16 v[44:47], a[84:87], a[200:203], v[44:47]\n"
        "ds_read_b128 a[28:31], v242 offset:23552\n"
        "v_cvt_pk_bf16_f32 v135, 0, v135\n"
        "v_mfma_f32_16x16x32_bf16 v[44:47], a[92:95], a[204:207], v[44:47]\n"
        "ds_read_b128 a[192:195], v242 offset:24576\n"
        "v_lshlrev_b32 v192, 16, v112\n"
        "v_mfma_f32_16x16x32_bf16 v[40:43], a[88:91], a[204:207], v[40:43]\n"
        "ds_read_b128 a[196:199], v242 offset:28672\n"
        "v_and_b32 v193, 0xffff0000, v112\n"
        "v_mfma_f32_16x16x32_bf16 v[40:43], a[80:83], a[200:203], v[40:43]\n"
        "ds_read_b128 a[200:203], v242 offset:25600\n"
        "s_waitcnt lgkmcnt(8)\n"
        "v_mfma_f32_16x16x32_bf16 v[48:51], a[80:83], a[208:211], v[48:51]\n"
        "ds_read_b128 a[204:207], v242 offset:29696\n"
        "v_and_b32 v83, 0xffff0000, v65\n"
        "v_mfma_f32_16x16x32_bf16 v[48:51], a[88:91], a[212:215], v[48:51]\n"
        "v_permlane16_swap_b32 v113, v115\n"
        "v_mfma_f32_16x16x32_bf16 v[52:55], a[92:95], a[212:215], v[52:55]\n"
        "ds_read_b128 a[212:215], v242 offset:30720\n"
        "v_lshlrev_b32 v194, 16, v113\n"
        "v_mfma_f32_16x16x32_bf16 v[52:55], a[84:87], a[208:211], v[52:55]\n"
        "ds_read_b128 a[208:211], v242 offset:26624\n"
        "v_and_b32 v195, 0xffff0000, v113\n"
        "v_mfma_f32_16x16x32_bf16 v[60:63], a[84:87], a[216:219], v[60:63]\n"
        "s_waitcnt lgkmcnt(6)\n"
        "v_permlane16_swap_b32 v116, v118\n"
        "v_mfma_f32_16x16x32_bf16 v[60:63], a[92:95], a[220:223], v[60:63]\n"
        "v_permlane16_swap_b32 v117, v119\n"
        "v_mfma_f32_16x16x32_bf16 v[56:59], a[88:91], a[220:223], v[56:59]\n"
        "ds_read_b128 a[220:223], v242 offset:31744\n"
        "v_mul_f32 v192, v192, v80\n"
        "v_mfma_f32_16x16x32_bf16 v[56:59], a[80:83], a[216:219], v[56:59]\n"
        "ds_read_b128 a[216:219], v242 offset:27648\n"
        "v_mul_f32 v193, v193, v81\n"
        "v_mfma_f32_16x16x32_bf16 v[32:35], a[96:99], a[0:3], v[32:35]\n"
        "v_permlane16_swap_b32 v120, v122\n"
        "v_mfma_f32_16x16x32_bf16 v[32:35], a[104:107], a[4:7], v[32:35]\n"
        "v_permlane16_swap_b32 v121, v123\n"
        "v_mfma_f32_16x16x32_bf16 v[36:39], a[108:111], a[4:7], v[36:39]\n"
        "v_permlane16_swap_b32 v124, v126\n"
        "v_mfma_f32_16x16x32_bf16 v[36:39], a[100:103], a[0:3], v[36:39]\n"
        "v_permlane16_swap_b32 v125, v127\n"
        "v_mfma_f32_16x16x32_bf16 v[44:47], a[100:103], a[8:11], v[44:47]\n"
        "v_mul_f32 v194, v194, v82\n"
        "v_mul_f32 v195, v195, v83\n"
        "v_mfma_f32_16x16x32_bf16 v[44:47], a[108:111], a[12:15], v[44:47]\n"
        "s_waitcnt lgkmcnt(2)\n"
        "v_cvt_pk_bf16_f32 v192, 0, v192\n"
        "v_cvt_pk_bf16_f32 v193, 0, v193\n"
        "v_mfma_f32_16x16x32_bf16 v[40:43], a[104:107], a[12:15], v[40:43]\n"
        "v_cvt_pk_bf16_f32 v194, 0, v194\n"
        "v_cvt_pk_bf16_f32 v195, 0, v195\n"
        "v_mfma_f32_16x16x32_bf16 v[40:43], a[96:99], a[8:11], v[40:43]\n"
        "v_mul_f32 v192, v192, v132\n"
        "v_mul_f32 v193, v193, v132\n"
        "v_mfma_f32_16x16x32_bf16 v[48:51], a[96:99], a[16:19], v[48:51]\n"
        "v_mul_f32 v194, v194, v132\n"
        "v_mul_f32 v195, v195, v132\n"
        "v_mfma_f32_16x16x32_bf16 v[48:51], a[104:107], a[20:23], v[48:51]\n"
        "v_cvt_pk_bf16_f32 v192, 0, v192\n"
        "v_cvt_pk_bf16_f32 v193, 0, v193\n"
        "v_mfma_f32_16x16x32_bf16 v[52:55], a[108:111], a[20:23], v[52:55]\n"
        "v_cvt_pk_bf16_f32 v194, 0, v194\n"
        "v_cvt_pk_bf16_f32 v195, 0, v195\n"
        "v_mfma_f32_16x16x32_bf16 v[52:55], a[100:103], a[16:19], v[52:55]\n"
        "v_lshlrev_b32 v196, 16, v114\n"
        "v_and_b32 v197, 0xffff0000, v114\n"
        "v_mfma_f32_16x16x32_bf16 v[60:63], a[100:103], a[24:27], v[60:63]\n"
        "s_barrier\n"
        "v_mfma_f32_16x16x32_bf16 v[60:63], a[108:111], a[28:31], v[60:63]\n"
        "ds_read_b128 a[0:3], v254 offset:0\n"
        "v_lshlrev_b32 v198, 16, v115\n"
        "v_mfma_f32_16x16x32_bf16 v[56:59], a[104:107], a[28:31], v[56:59]\n"
        "ds_read_b128 a[4:7], v254 offset:4096\n"
        "v_and_b32 v199, 0xffff0000, v115\n"
        "v_mfma_f32_16x16x32_bf16 v[56:59], a[96:99], a[24:27], v[56:59]\n"
        "ds_read_b128 a[8:11], v254 offset:1024\n"
        "v_lshlrev_b32 v84, 16, v66\n"
        "v_mfma_f32_16x16x32_bf16 v[32:35], a[112:115], a[192:195], v[32:35]\n"
        "ds_read_b128 a[12:15], v254 offset:5120\n"
        "v_and_b32 v85, 0xffff0000, v66\n"
        "v_mfma_f32_16x16x32_bf16 v[32:35], a[120:123], a[196:199], v[32:35]\n"
        "ds_read_b128 a[16:19], v254 offset:2048\n"
        "v_lshlrev_b32 v86, 16, v67\n"
        "v_mfma_f32_16x16x32_bf16 v[36:39], a[124:127], a[196:199], v[36:39]\n"
        "ds_read_b128 a[20:23], v254 offset:6144\n"
        "v_and_b32 v87, 0xffff0000, v67\n"
        "v_mfma_f32_16x16x32_bf16 v[36:39], a[116:119], a[192:195], v[36:39]\n"
        "ds_read_b128 a[24:27], v254 offset:3072\n"
        "v_mul_f32 v196, v196, v84\n"
        "v_mfma_f32_16x16x32_bf16 v[44:47], a[116:119], a[200:203], v[44:47]\n"
        "ds_read_b128 a[28:31], v254 offset:7168\n"
        "v_mul_f32 v197, v197, v85\n"
        "v_mfma_f32_16x16x32_bf16 v[44:47], a[124:127], a[204:207], v[44:47]\n"
        "ds_read_b128 a[192:195], v254 offset:8192\n"
        "v_mul_f32 v198, v198, v86\n"
        "v_mfma_f32_16x16x32_bf16 v[40:43], a[120:123], a[204:207], v[40:43]\n"
        "ds_read_b128 a[196:199], v254 offset:12288\n"
        "v_mul_f32 v199, v199, v87\n"
        "v_mfma_f32_16x16x32_bf16 v[40:43], a[112:115], a[200:203], v[40:43]\n"
        "ds_read_b128 a[200:203], v254 offset:9216\n"
        "v_cvt_pk_bf16_f32 v196, 0, v196\n"
        "v_mfma_f32_16x16x32_bf16 v[48:51], a[112:115], a[208:211], v[48:51]\n"
        "ds_read_b128 a[204:207], v254 offset:13312\n"
        "v_cvt_pk_bf16_f32 v197, 0, v197\n"
        "v_mfma_f32_16x16x32_bf16 v[48:51], a[120:123], a[212:215], v[48:51]\n"
        "s_waitcnt lgkmcnt(7)\n"
        "v_cvt_pk_bf16_f32 v198, 0, v198\n"
        "v_cvt_pk_bf16_f32 v199, 0, v199\n"
        "v_mfma_f32_16x16x32_bf16 v[52:55], a[124:127], a[212:215], v[52:55]\n"
        "ds_read_b128 a[212:215], v254 offset:14336\n"
        "v_mul_f32 v196, v196, v132\n"
        "v_mfma_f32_16x16x32_bf16 v[52:55], a[116:119], a[208:211], v[52:55]\n"
        "ds_read_b128 a[208:211], v254 offset:10240\n"
        "v_mul_f32 v197, v197, v132\n"
        "v_mfma_f32_16x16x32_bf16 v[60:63], a[116:119], a[216:219], v[60:63]\n"
        "v_mul_f32 v198, v198, v132\n"
        "v_mul_f32 v199, v199, v132\n"
        "v_mfma_f32_16x16x32_bf16 v[60:63], a[124:127], a[220:223], v[60:63]\n"
        "v_cvt_pk_bf16_f32 v196, 0, v196\n"
        "v_cvt_pk_bf16_f32 v197, 0, v197\n"
        "v_mfma_f32_16x16x32_bf16 v[56:59], a[120:123], a[220:223], v[56:59]\n"
        "ds_read_b128 a[220:223], v254 offset:15360\n"
        "v_cvt_pk_bf16_f32 v198, 0, v198\n"
        "v_mfma_f32_16x16x32_bf16 v[56:59], a[112:115], a[216:219], v[56:59]\n"
        "ds_read_b128 a[216:219], v254 offset:11264\n"
        "v_cvt_pk_bf16_f32 v199, 0, v199\n"
        "v_mfma_f32_16x16x32_bf16 v[32:35], a[128:131], a[0:3], v[32:35]\n"
        "s_waitcnt lgkmcnt(4)\n"
        "v_lshlrev_b32 v200, 16, v116\n"
        "v_and_b32 v201, 0xffff0000, v116\n"
        "v_mfma_f32_16x16x32_bf16 v[32:35], a[136:139], a[4:7], v[32:35]\n"
        "s_waitcnt vmcnt(0)\n"
        "v_mfma_f32_16x16x32_bf16 v[36:39], a[140:143], a[4:7], v[36:39]\n"
        "s_mov_b64 exec, 0xffff\n"
        "global_store_dwordx2 v234, v[250:251], s[48:49] sc1\n"
        "s_mov_b64 exec, -1\n"
        "v_mfma_f32_16x16x32_bf16 v[36:39], a[132:135], a[0:3], v[36:39]\n"
        "global_load_dwordx2 v[236:237], v235, s[48:49] sc1\n"
        "v_lshlrev_b32 v202, 16, v117\n"
        "v_mfma_f32_16x16x32_bf16 v[44:47], a[132:135], a[8:11], v[44:47]\n"
        "v_and_b32 v203, 0xffff0000, v117\n"
        "v_lshlrev_b32 v80, 16, v68\n"
        "v_mfma_f32_16x16x32_bf16 v[44:47], a[140:143], a[12:15], v[44:47]\n"
        "v_and_b32 v81, 0xffff0000, v68\n"
        "v_lshlrev_b32 v82, 16, v69\n"
        "v_mfma_f32_16x16x32_bf16 v[40:43], a[136:139], a[12:15], v[40:43]\n"
        "v_and_b32 v83, 0xffff0000, v69\n"
        "v_mul_f32 v200, v200, v80\n"
        "v_mfma_f32_16x16x32_bf16 v[40:43], a[128:131], a[8:11], v[40:43]\n"
        "v_mul_f32 v201, v201, v81\n"
        "v_mul_f32 v202, v202, v82\n"
        "v_mfma_f32_16x16x32_bf16 v[48:51], a[128:131], a[16:19], v[48:51]\n"
        "v_mul_f32 v203, v203, v83\n"
        "v_cvt_pk_bf16_f32 v200, 0, v200\n"
        "v_mfma_f32_16x16x32_bf16 v[48:51], a[136:139], a[20:23], v[48:51]\n"
        "v_cvt_pk_bf16_f32 v201, 0, v201\n"
        "v_cvt_pk_bf16_f32 v202, 0, v202\n"
        "v_mfma_f32_16x16x32_bf16 v[52:55], a[140:143], a[20:23], v[52:55]\n"
        "v_cvt_pk_bf16_f32 v203, 0, v203\n"
        "v_mul_f32 v200, v200, v133\n"
        "v_mfma_f32_16x16x32_bf16 v[52:55], a[132:135], a[16:19], v[52:55]\n"
        "v_mul_f32 v201, v201, v133\n"
        "v_mul_f32 v202, v202, v133\n"
        "v_mfma_f32_16x16x32_bf16 v[60:63], a[132:135], a[24:27], v[60:63]\n"
        "s_barrier\n"
        "v_mfma_f32_16x16x32_bf16 v[60:63], a[140:143], a[28:31], v[60:63]\n"
        "ds_read_b128 a[0:3], v254 offset:16384\n"
        "v_mul_f32 v203, v203, v133\n"
        "v_mfma_f32_16x16x32_bf16 v[56:59], a[136:139], a[28:31], v[56:59]\n"
        "ds_read_b128 a[4:7], v254 offset:20480\n"
        "v_cvt_pk_bf16_f32 v200, 0, v200\n"
        "v_mfma_f32_16x16x32_bf16 v[56:59], a[128:131], a[24:27], v[56:59]\n"
        "ds_read_b128 a[8:11], v254 offset:17408\n"
        "v_cvt_pk_bf16_f32 v201, 0, v201\n"
        "v_mfma_f32_16x16x32_bf16 v[32:35], a[144:147], a[192:195], v[32:35]\n"
        "ds_read_b128 a[12:15], v254 offset:21504\n"
        "v_cvt_pk_bf16_f32 v202, 0, v202\n"
        "v_mfma_f32_16x16x32_bf16 v[32:35], a[152:155], a[196:199], v[32:35]\n"
        "ds_read_b128 a[16:19], v254 offset:18432\n"
        "v_cvt_pk_bf16_f32 v203, 0, v203\n"
        "v_mfma_f32_16x16x32_bf16 v[36:39], a[156:159], a[196:199], v[36:39]\n"
        "ds_read_b128 a[20:23], v254 offset:22528\n"
        "v_lshlrev_b32 v204, 16, v118\n"
        "v_mfma_f32_16x16x32_bf16 v[36:39], a[148:151], a[192:195], v[36:39]\n"
        "ds_read_b128 a[24:27], v254 offset:19456\n"
        "v_and_b32 v205, 0xffff0000, v118\n"
        "v_mfma_f32_16x16x32_bf16 v[44:47], a[148:151], a[200:203], v[44:47]\n"
        "ds_read_b128 a[28:31], v254 offset:23552\n"
        "v_lshlrev_b32 v206, 16, v119\n"
        "v_mfma_f32_16x16x32_bf16 v[44:47], a[156:159], a[204:207], v[44:47]\n"
        "ds_read_b128 a[192:195], v254 offset:24576\n"
        "v_and_b32 v207, 0xffff0000, v119\n"
        "v_mfma_f32_16x16x32_bf16 v[40:43], a[152:155], a[204:207], v[40:43]\n"
        "ds_read_b128 a[196:199], v254 offset:28672\n"
        "v_lshlrev_b32 v84, 16, v70\n"
        "v_mfma_f32_16x16x32_bf16 v[40:43], a[144:147], a[200:203], v[40:43]\n"
        "ds_read_b128 a[200:203], v254 offset:25600\n"
        "s_waitcnt lgkmcnt(8)\n"
        "v_mfma_f32_16x16x32_bf16 v[48:51], a[144:147], a[208:211], v[48:51]\n"
        "ds_read_b128 a[204:207], v254 offset:29696\n"
        "v_and_b32 v85, 0xffff0000, v70\n"
        "v_mfma_f32_16x16x32_bf16 v[48:51], a[152:155], a[212:215], v[48:51]\n"
        "v_lshlrev_b32 v86, 16, v71\n"
        "v_and_b32 v87, 0xffff0000, v71\n"
        "v_mfma_f32_16x16x32_bf16 v[52:55], a[156:159], a[212:215], v[52:55]\n"
        "ds_read_b128 a[212:215], v254 offset:30720\n"
        "v_mul_f32 v204, v204, v84\n"
        "v_mfma_f32_16x16x32_bf16 v[52:55], a[148:151], a[208:211], v[52:55]\n"
        "ds_read_b128 a[208:211], v254 offset:26624\n"
        "v_mul_f32 v205, v205, v85\n"
        "v_mfma_f32_16x16x32_bf16 v[60:63], a[148:151], a[216:219], v[60:63]\n"
        "s_waitcnt lgkmcnt(6)\n"
        "v_mul_f32 v206, v206, v86\n"
        "v_mul_f32 v207, v207, v87\n"
        "v_mfma_f32_16x16x32_bf16 v[60:63], a[156:159], a[220:223], v[60:63]\n"
        "v_cvt_pk_bf16_f32 v204, 0, v204\n"
        "v_cvt_pk_bf16_f32 v205, 0, v205\n"
        "v_mfma_f32_16x16x32_bf16 v[56:59], a[152:155], a[220:223], v[56:59]\n"
        "ds_read_b128 a[220:223], v254 offset:31744\n"
        "v_cvt_pk_bf16_f32 v206, 0, v206\n"
        "v_mfma_f32_16x16x32_bf16 v[56:59], a[144:147], a[216:219], v[56:59]\n"
        "ds_read_b128 a[216:219], v254 offset:27648\n"
        "v_cvt_pk_bf16_f32 v207, 0, v207\n"
        "v_mfma_f32_16x16x32_bf16 v[32:35], a[160:163], a[0:3], v[32:35]\n"
        "v_mul_f32 v204, v204, v133\n"
        "v_mul_f32 v205, v205, v133\n"
        "v_mfma_f32_16x16x32_bf16 v[32:35], a[168:171], a[4:7], v[32:35]\n"
        "v_mul_f32 v206, v206, v133\n"
        "v_mul_f32 v207, v207, v133\n"
        "v_mfma_f32_16x16x32_bf16 v[36:39], a[172:175], a[4:7], v[36:39]\n"
        "v_cvt_pk_bf16_f32 v204, 0, v204\n"
        "v_cvt_pk_bf16_f32 v205, 0, v205\n"
        "v_mfma_f32_16x16x32_bf16 v[36:39], a[164:167], a[0:3], v[36:39]\n"
        "v_cvt_pk_bf16_f32 v206, 0, v206\n"
        "v_cvt_pk_bf16_f32 v207, 0, v207\n"
        "v_mfma_f32_16x16x32_bf16 v[44:47], a[164:167], a[8:11], v[44:47]\n"
        "v_lshlrev_b32 v208, 16, v120\n"
        "v_and_b32 v209, 0xffff0000, v120\n"
        "v_mfma_f32_16x16x32_bf16 v[44:47], a[172:175], a[12:15], v[44:47]\n"
        "s_waitcnt lgkmcnt(2)\n"
        "v_lshlrev_b32 v210, 16, v121\n"
        "v_and_b32 v211, 0xffff0000, v121\n"
        "v_mfma_f32_16x16x32_bf16 v[40:43], a[168:171], a[12:15], v[40:43]\n"
        "v_lshlrev_b32 v80, 16, v72\n"
        "v_and_b32 v81, 0xffff0000, v72\n"
        "v_mfma_f32_16x16x32_bf16 v[40:43], a[160:163], a[8:11], v[40:43]\n"
        "v_lshlrev_b32 v82, 16, v73\n"
        "v_and_b32 v83, 0xffff0000, v73\n"
        "v_mfma_f32_16x16x32_bf16 v[48:51], a[160:163], a[16:19], v[48:51]\n"
        "v_mul_f32 v208, v208, v80\n"
        "v_mul_f32 v209, v209, v81\n"
        "s_waitcnt vmcnt(0) lgkmcnt(0)\n"
        "v_mfma_f32_16x16x32_bf16 v[48:51], a[168:171], a[20:23], v[48:51]\n"
        "v_cmp_ne_u64 vcc, v[236:237], v[250:251]\n"
        "v_mul_f32 v210, v210, v82\n"
        "v_mfma_f32_16x16x32_bf16 v[52:55], a[172:175], a[20:23], v[52:55]\n"
        "s_cbranch_vccnz LPPR_%=\n"
        "LBPR_%=:\n"
        "global_load_dwordx4 v[96:99], v254, s[30:31] offset:0 sc1\n"
        "v_mfma_f32_16x16x32_bf16 v[52:55], a[164:167], a[16:19], v[52:55]\n"
        "v_mul_f32 v211, v211, v83\n"
        "v_cvt_pk_bf16_f32 v208, 0, v208\n"
        "v_mfma_f32_16x16x32_bf16 v[60:63], a[164:167], a[24:27], v[60:63]\n"
        "v_cvt_pk_bf16_f32 v209, 0, v209\n"
        "v_cvt_pk_bf16_f32 v210, 0, v210\n"
        "v_mfma_f32_16x16x32_bf16 v[60:63], a[172:175], a[28:31], v[60:63]\n"
        "global_load_dwordx4 v[100:103], v254, s[30:31] offset:1024 sc1\n"
        "v_mfma_f32_16x16x32_bf16 v[56:59], a[168:171], a[28:31], v[56:59]\n"
        "v_cvt_pk_bf16_f32 v211, 0, v211\n"
        "v_mul_f32 v208, v208, v134\n"
        "v_mfma_f32_16x16x32_bf16 v[56:59], a[160:163], a[24:27], v[56:59]\n"
        "v_mul_f32 v209, v209, v134\n"
        "v_mul_f32 v210, v210, v134\n"
        "v_mfma_f32_16x16x32_bf16 v[32:35], a[176:179], a[192:195], v[32:35]\n"
        "global_load_dwordx4 v[104:107], v254, s[30:31] offset:2048 sc1\n"
        "v_mfma_f32_16x16x32_bf16 v[32:35], a[184:187], a[196:199], v[32:35]\n"
        "v_mul_f32 v211, v211, v134\n"
        "v_cvt_pk_bf16_f32 v208, 0, v208\n"
        "v_mfma_f32_16x16x32_bf16 v[36:39], a[188:191], a[196:199], v[36:39]\n"
        "v_cvt_pk_bf16_f32 v209, 0, v209\n"
        "v_cvt_pk_bf16_f32 v210, 0, v210\n"
        "v_mfma_f32_16x16x32_bf16 v[36:39], a[180:183], a[192:195], v[36:39]\n"
        "global_load_dwordx4 v[108:111], v254, s[30:31] offset:3072 sc1\n"
        "v_mfma_f32_16x16x32_bf16 v[44:47], a[180:183], a[200:203], v[44:47]\n"
        "v_cvt_pk_bf16_f32 v211, 0, v211\n"
        "v_lshlrev_b32 v212, 16, v122\n"
        "v_mfma_f32_16x16x32_bf16 v[44:47], a[188:191], a[204:207], v[44:47]\n"
        "v_and_b32 v213, 0xffff0000, v122\n"
        "v_lshlrev_b32 v214, 16, v123\n"
        "v_mfma_f32_16x16x32_bf16 v[40:43], a[184:187], a[204:207], v[40:43]\n"
        "v_and_b32 v215, 0xffff0000, v123\n"
        "v_lshlrev_b32 v84, 16, v74\n"
        "v_mfma_f32_16x16x32_bf16 v[40:43], a[176:179], a[200:203], v[40:43]\n"
        "v_and_b32 v85, 0xffff0000, v74\n"
        "v_lshlrev_b32 v86, 16, v75\n"
        "v_mfma_f32_16x16x32_bf16 v[48:51], a[176:179], a[208:211], v[48:51]\n"
        "v_and_b32 v87, 0xffff0000, v75\n"
        "v_mul_f32 v212, v212, v84\n"
        "v_mfma_f32_16x16x32_bf16 v[48:51], a[184:187], a[212:215], v[48:51]\n"
        "v_mul_f32 v213, v213, v85\n"
        "v_mul_f32 v214, v214, v86\n"
        "v_mfma_f32_16x16x32_bf16 v[52:55], a[188:191], a[212:215], v[52:55]\n"
        "v_mul_f32 v215, v215, v87\n"
        "v_cvt_pk_bf16_f32 v212, 0, v212\n"
        "v_mfma_f32_16x16x32_bf16 v[52:55], a[180:183], a[208:211], v[52:55]\n"
        "v_cvt_pk_bf16_f32 v213, 0, v213\n"
        "v_cvt_pk_bf16_f32 v214, 0, v214\n"
        "v_mfma_f32_16x16x32_bf16 v[60:63], a[180:183], a[216:219], v[60:63]\n"
        "v_cvt_pk_bf16_f32 v215, 0, v215\n"
        "v_mul_f32 v212, v212, v134\n"
        "v_mfma_f32_16x16x32_bf16 v[60:63], a[188:191], a[220:223], v[60:63]\n"
        "v_mul_f32 v213, v213, v134\n"
        "v_mul_f32 v214, v214, v134\n"
        "v_mfma_f32_16x16x32_bf16 v[56:59], a[184:187], a[220:223], v[56:59]\n"
        "v_mul_f32 v215, v215, v134\n"
        "v_cvt_pk_bf16_f32 v212, 0, v212\n"
        "v_mfma_f32_16x16x32_bf16 v[56:59], a[176:179], a[216:219], v[56:59]\n"
        "v_cvt_pk_bf16_f32 v213, 0, v213\n"
        "v_cvt_pk_bf16_f32 v214, 0, v214\n"
        "v_cvt_pk_bf16_f32 v215, 0, v215\n"
        "v_lshlrev_b32 v216, 16, v124\n"
        "v_and_b32 v217, 0xffff0000, v124\n"
        "v_lshlrev_b32 v218, 16, v125\n"
        "v_and_b32 v219, 0xffff0000, v125\n"
        "v_lshlrev_b32 v80, 16, v76\n"
        "v_and_b32 v81, 0xffff0000, v76\n"
        "v_lshlrev_b32 v82, 16, v77\n"
        "v_and_b32 v83, 0xffff0000, v77\n"
        "v_mul_f32 v216, v216, v80\n"
        "v_mul_f32 v217, v217, v81\n"
        "v_mul_f32 v218, v218, v82\n"
        "v_mul_f32 v219, v219, v83\n"
        "v_cvt_pk_bf16_f32 v216, 0, v216\n"
        "v_cvt_pk_bf16_f32 v217, 0, v217\n"
        "v_cvt_pk_bf16_f32 v218, 0, v218\n"
        "v_cvt_pk_bf16_f32 v219, 0, v219\n"
        "v_mul_f32 v216, v216, v135\n"
        "v_mul_f32 v217, v217, v135\n"
        "v_mul_f32 v218, v218, v135\n"
        "v_mul_f32 v219, v219, v135\n"
        "v_cvt_pk_bf16_f32 v216, 0, v216\n"
        "v_cvt_pk_bf16_f32 v217, 0, v217\n"
        "v_cvt_pk_bf16_f32 v218, 0, v218\n"
        "v_cvt_pk_bf16_f32 v219, 0, v219\n"
        "v_lshlrev_b32 v220, 16, v126\n"
        "v_and_b32 v221, 0xffff0000, v126\n"
        "v_lshlrev_b32 v222, 16, v127\n"
        "v_and_b32 v223, 0xffff0000, v127\n"
        "v_lshlrev_b32 v84, 16, v78\n"
        "v_and_b32 v85, 0xffff0000, v78\n"
        "v_lshlrev_b32 v86, 16, v79\n"
        "v_and_b32 v87, 0xffff0000, v79\n"
        "v_mul_f32 v220, v220, v84\n"
        "v_mul_f32 v221, v221, v85\n"
        "v_mul_f32 v222, v222, v86\n"
        "v_mul_f32 v223, v223, v87\n"
        "v_cvt_pk_bf16_f32 v220, 0, v220\n"
        "v_cvt_pk_bf16_f32 v221, 0, v221\n"
        "v_cvt_pk_bf16_f32 v222, 0, v222\n"
        "v_cvt_pk_bf16_f32 v223, 0, v223\n"
        "v_mul_f32 v220, v220, v135\n"
        "v_mul_f32 v221, v221, v135\n"
        "v_mul_f32 v222, v222, v135\n"
        "v_mul_f32 v223, v223, v135\n"
        "v_cvt_pk_bf16_f32 v220, 0, v220\n"
        "v_cvt_pk_bf16_f32 v221, 0, v221\n"
        "v_cvt_pk_bf16_f32 v222, 0, v222\n"
        "v_cvt_pk_bf16_f32 v223, 0, v223\n"
        "v_mov_b32 v80, 0\n"
        "s_waitcnt vmcnt(3)\n"
        "v_mfma_f32_16x16x32_bf16 v[32:35], v[138:141], v[96:99], v[32:35]\n"
        "v_mfma_f32_16x16x32_bf16 v[36:39], v[136:139], v[96:99], v[36:39]\n"
        "s_waitcnt vmcnt(2)\n"
        "v_mfma_f32_16x16x32_bf16 v[40:43], v[138:141], v[100:103], v[40:43]\n"
        "v_mfma_f32_16x16x32_bf16 v[44:47], v[136:139], v[100:103], v[44:47]\n"
        "s_waitcnt vmcnt(1)\n"
        "v_mfma_f32_16x16x32_bf16 v[48:51], v[138:141], v[104:107], v[48:51]\n"
        "v_mfma_f32_16x16x32_bf16 v[52:55], v[136:139], v[104:107], v[52:55]\n"
        "s_waitcnt vmcnt(0)\n"
        "v_fmac_f32 v192, v32, v128\n"
        "v_fmac_f32 v193, v33, v128\n"
        "v_fmac_f32 v194, v34, v128\n"
        "v_fmac_f32 v195, v35, v128\n"
        "v_fmac_f32 v196, v36, v128\n"
        "v_fmac_f32 v197, v37, v128\n"
        "v_mfma_f32_16x16x32_bf16 v[56:59], v[138:141], v[108:111], v[56:59]\n"
        "v_fmac_f32 v198, v38, v128\n"
        "v_fmac_f32 v199, v39, v128\n"
        "v_mfma_f32_16x16x32_bf16 v[60:63], v[136:139], v[108:111], v[60:63]\n"
        "v_cvt_pk_bf16_f32 v192, v192, v193\n"
        "v_cvt_pk_bf16_f32 v193, v194, v195\n"
        "v_cvt_pk_bf16_f32 v194, v196, v197\n"
        "v_cvt_pk_bf16_f32 v195, v198, v199\n"
        "v_fmac_f32 v200, v40, v129\n"
        "v_permlane16_swap_b32 v192, v194\n"
        "v_permlane16_swap_b32 v193, v195\n"
        "global_store_dwordx4 v151, v[192:195], s[56:57] sc0 sc1\n"
        "s_add_u32 s56, s56, 0x8000\n"
        "s_addc_u32 s57, s57, 0\n"
        "v_fmac_f32 v201, v41, v129\n"
        "v_fmac_f32 v202, v42, v129\n"
        "v_fmac_f32 v203, v43, v129\n"
        "v_fmac_f32 v204, v44, v129\n"
        "v_fmac_f32 v205, v45, v129\n"
        "v_fmac_f32 v206, v46, v129\n"
        "v_fmac_f32 v207, v47, v129\n"
        "v_cvt_pk_bf16_f32 v200, v200, v201\n"
        "v_cvt_pk_bf16_f32 v201, v202, v203\n"
        "v_cvt_pk_bf16_f32 v202, v204, v205\n"
        "v_cvt_pk_bf16_f32 v203, v206, v207\n"
        "v_fmac_f32 v208, v48, v130\n"
        "v_permlane16_swap_b32 v200, v202\n"
        "v_permlane16_swap_b32 v201, v203\n"
        "global_store_dwordx4 v151, v[200:203], s[56:57] sc0 sc1\n"
        "s_add_u32 s56, s56, 0x8000\n"
        "s_addc_u32 s57, s57, 0\n"
        "v_fmac_f32 v209, v49, v130\n"
        "v_fmac_f32 v210, v50, v130\n"
        "v_fmac_f32 v211, v51, v130\n"
        "v_fmac_f32 v212, v52, v130\n"
        "v_fmac_f32 v213, v53, v130\n"
        "v_fmac_f32 v214, v54, v130\n"
        "v_fmac_f32 v215, v55, v130\n"
        "v_cvt_pk_bf16_f32 v208, v208, v209\n"
        "v_cvt_pk_bf16_f32 v209, v210, v211\n"
        "v_cvt_pk_bf16_f32 v210, v212, v213\n"
        "v_cvt_pk_bf16_f32 v211, v214, v215\n"
        "v_fmac_f32 v216, v56, v131\n"
        "v_permlane16_swap_b32 v208, v210\n"
        "v_permlane16_swap_b32 v209, v211\n"
        "global_store_dwordx4 v151, v[208:211], s[56:57] sc0 sc1\n"
        "s_add_u32 s56, s56, 0x8000\n"
        "s_addc_u32 s57, s57, 0\n"
        "v_fmac_f32 v217, v57, v131\n"
        "v_fmac_f32 v218, v58, v131\n"
        "v_fmac_f32 v219, v59, v131\n"
        "v_fmac_f32 v220, v60, v131\n"
        "v_fmac_f32 v221, v61, v131\n"
        "v_fmac_f32 v222, v62, v131\n"
        "v_fmac_f32 v223, v63, v131\n"
        "v_cvt_pk_bf16_f32 v216, v216, v217\n"
        "v_cvt_pk_bf16_f32 v217, v218, v219\n"
        "v_cvt_pk_bf16_f32 v218, v220, v221\n"
        "v_cvt_pk_bf16_f32 v219, v222, v223\n"
        "s_nop 0\n"
        "v_permlane16_swap_b32 v216, v218\n"
        "v_permlane16_swap_b32 v217, v219\n"
        "global_store_dwordx4 v151, v[216:219], s[56:57] sc0 sc1\n"
        "s_or_b32 s97, s55, s40\n"
        "s_cbranch_scc1 LEB_%=\n"
        "s_mov_b64 exec, 1\n"
        "global_store_dwordx2 v80, v[250:251], s[48:49] sc1\n"
        "s_mov_b64 exec, -1\n"
        "LEB_%=:\n"
        "; ---- END\n"
        "s_branch LEND_%=\n"
        "LEND_%=:\n"
        :
        : "s"(__builtin_amdgcn_kernarg_segment_ptr()), "s"(blockIdx.x), "s"(__builtin_amdgcn_readfirstlane(threadIdx.x >> 6))
        : "v0", "v1", "v2", "v3", "v4", "v5", "v6", "v7", "v8", "v9", "v10", "v11", "v12", "v13", "v14", "v15", "v16", "v17", "v18", "v19", "v20", "v21", "v22", "v23", "v24", "v25", "v26", "v27", "v28", "v29", "v30", "v31",
        "v32", "v33", "v34", "v35", "v36", "v37", "v38", "v39", "v40", "v41", "v42", "v43", "v44", "v45", "v46", "v47", "v48", "v49", "v50", "v51", "v52", "v53", "v54", "v55", "v56", "v57", "v58", "v59", "v60", "v61", "v62", "v63",
        "v64", "v65", "v66", "v67", "v68", "v69", "v70", "v71", "v72", "v73", "v74", "v75", "v76", "v77", "v78", "v79", "v80", "v81", "v82", "v83", "v84", "v85", "v86", "v87", "v88", "v89", "v90", "v91", "v92", "v93", "v94", "v95",
        "v96", "v97", "v98", "v99", "v100", "v101", "v102", "v103", "v104", "v105", "v106", "v107", "v108", "v109", "v110", "v111", "v112", "v113", "v114", "v115", "v116", "v117", "v118", "v119", "v120", "v121", "v122", "v123", "v124", "v125", "v126", "v127",
        "v128", "v129", "v130", "v131", "v132", "v133", "v134", "v135", "v136", "v137", "v138", "v139", "v140", "v141", "v142", "v143", "v144", "v145", "v146", "v147", "v148", "v149", "v150", "v151", "v152", "v153", "v154", "v155", "v156", "v157", "v158", "v159",
        "v160", "v161", "v162", "v163", "v164", "v165", "v166", "v167", "v168", "v169", "v170", "v171", "v172", "v173", "v174", "v175", "v176", "v177", "v178", "v179", "v180", "v181", "v182", "v183", "v184", "v185", "v186", "v187", "v188", "v189", "v190", "v191",
        "v192", "v193", "v194", "v195", "v196", "v197", "v198", "v199", "v200", "v201", "v202", "v203", "v204", "v205", "v206", "v207", "v208", "v209", "v210", "v211", "v212", "v213", "v214", "v215", "v216", "v217", "v218", "v219", "v220", "v221", "v222", "v223",
        "v224", "v225", "v226", "v227", "v228", "v229", "v230", "v231", "v232", "v233", "v234", "v235", "v236", "v237", "v238", "v239", "v240", "v241", "v242", "v243", "v244", "v245", "v246", "v247", "v248", "v249", "v250", "v251", "v252", "v253", "v254", "v255",
        "a0", "a1", "a2", "a3", "a4", "a5", "a6", "a7", "a8", "a9", "a10", "a11", "a12", "a13", "a14", "a15", "a16", "a17", "a18", "a19", "a20", "a21", "a22", "a23", "a24", "a25", "a26", "a27", "a28", "a29", "a30", "a31",
        "a32", "a33", "a34", "a35", "a36", "a37", "a38", "a39", "a40", "a41", "a42", "a43", "a44", "a45", "a46", "a47", "a48", "a49", "a50", "a51", "a52", "a53", "a54", "a55", "a56", "a57", "a58", "a59", "a60", "a61", "a62", "a63",
        "a64", "a65", "a66", "a67", "a68", "a69", "a70", "a71", "a72", "a73", "a74", "a75", "a76", "a77", "a78", "a79", "a80", "a81", "a82", "a83", "a84", "a85", "a86", "a87", "a88", "a89", "a90", "a91", "a92", "a93", "a94", "a95",
        "a96", "a97", "a98", "a99", "a100", "a101", "a102", "a103", "a104", "a105", "a106", "a107", "a108", "a109", "a110", "a111", "a112", "a113", "a114", "a115", "a116", "a117", "a118", "a119", "a120", "a121", "a122", "a123", "a124", "a125", "a126", "a127",
        "a128", "a129", "a130", "a131", "a132", "a133", "a134", "a135", "a136", "a137", "a138", "a139", "a140", "a141", "a142", "a143", "a144", "a145", "a146", "a147", "a148", "a149", "a150", "a151", "a152", "a153", "a154", "a155", "a156", "a157", "a158", "a159",
        "a160", "a161", "a162", "a163", "a164", "a165", "a166", "a167", "a168", "a169", "a170", "a171", "a172", "a173", "a174", "a175", "a176", "a177", "a178", "a179", "a180", "a181", "a182", "a183", "a184", "a185", "a186", "a187", "a188", "a189", "a190", "a191",
        "a192", "a193", "a194", "a195", "a196", "a197", "a198", "a199", "a200", "a201", "a202", "a203", "a204", "a205", "a206", "a207", "a208", "a209", "a210", "a211", "a212", "a213", "a214", "a215", "a216", "a217", "a218", "a219", "a220", "a221", "a222", "a223",
        "a224", "a225", "a226", "a227", "a228", "a229", "a230", "a231", "a232", "a233", "a234", "a235", "a236", "a237", "a238", "a239", "a240", "a241", "a242", "a243", "a244", "a245", "a246", "a247", "a248", "a249", "a250", "a251", "a252", "a253", "a254", "a255",
        "s8", "s9", "s10", "s11", "s12", "s13", "s14", "s15", "s16", "s17", "s18", "s19", "s20", "s21", "s22", "s23", "s24", "s25", "s26", "s27", "s28", "s29", "s30", "s31", "s36", "s37", "s38", "s39", "s40", "s41", "s42", "s43",
        "s44", "s45", "s46", "s47", "s48", "s49", "s50", "s51", "s52", "s53", "s54", "s55", "s56", "s57", "s58", "s59", "s60", "s61", "s62", "s63", "s64", "s65", "s66", "s67", "s68", "s69", "s70", "s71", "s72", "s73", "s74", "s75",
        "s76", "s77", "s78", "s79", "s80", "s81", "s82", "s83", "s84", "s85", "s86", "s87", "s88", "s89", "s90", "s91", "s92", "s93", "s94", "s95", "s96", "s97", "s98", "s99", "vcc", "scc", "memory");
}

extern "C" void kernel_launch(void* const* d_in, const int* in_sizes, int n_in, void* d_out, int out_size, void* d_ws, size_t ws_size, hipStream_t stream) {
    (void)in_sizes; (void)n_in; (void)out_size;
    constexpr size_t WS_USED = (size_t)72 << 20;
    Params P;
    P.x = d_in[0]; P.ckv = d_in[1]; P.wq = d_in[2]; P.wuk = d_in[3]; P.wuv = d_in[4]; P.wo = d_in[5];
    P.bk = d_in[7]; P.bv = d_in[8]; P.shk = d_in[9]; P.ssk = d_in[10]; P.ssd = d_in[11]; P.gw = d_in[12];
    P.out = d_out; P.ws = (char*)d_ws;
    if (ws_size > WS_USED) P.ws += (ws_size - WS_USED) & ~(size_t)0xfffff;
    constexpr int LDS_MAX = 163840;
    (void)hipFuncSetAttribute((const void*)k_main, hipFuncAttributeMaxDynamicSharedMemorySize, LDS_MAX);
    k_main<<<256, 256, LDS_MAX, stream>>>(P);
}
```

```cpp
#include <hip/hip_runtime.h>
#include <stdint.h>

struct Params {
    const void *x, *ckv, *wq, *wuk, *wuv, *wo, *bk, *bv, *shk, *ssk, *ssd, *gw;
    void* out; char* ws;
};

__global__ __launch_bounds__(256, 1) void k_main(Params P) {
    asm volatile(
        "; ---- PRO\n"
        "s_load_dwordx16 s[8:23], %0, 0\n"
        "s_load_dwordx8 s[24:31], %0, 64\n"
        "s_load_dwordx4 s[36:39], %0, 96\n"
        "s_mov_b32 s55, %1\n"
        "s_mov_b32 s40, %2\n"
        "v_mbcnt_lo_u32_b32 v0, -1, 0\n"
        "v_mbcnt_hi_u32_b32 v0, -1, v0\n"
        "s_and_b32 s41, s55, 7\n"
        "s_lshr_b32 s42, s55, 3\n"
        "s_lshr_b32 s50, s42, 3\n"
        "s_lshl2_add_u32 s43, s41, s50\n"
        "s_and_b32 s44, s42, 7\n"
        "s_sub_u32 s45, 15, s44\n"
        "s_lshl_b32 s72, s40, 13\n"
        "s_lshl_b32 s73, s40, 12\n"
        "v_lshlrev_b32 v254, 4, v0\n"
        "v_lshrrev_b32 v2, 4, v0\n"
        "v_and_b32 v1, 15, v0\n"
        "v_lshrrev_b32 v4, 3, v0\n"
        "v_and_b32 v5, 7, v0\n"
        "v_and_b32 v8, 6, v4\n"
        "v_xor_b32 v8, v8, v5\n"
        "v_lshlrev_b32 v8, 4, v8\n"
        "v_lshl_add_u32 v9, v4, 13, v8\n"
        "v_xor_b32 v10, 4, v4\n"
        "v_lshl_add_u32 v10, v10, 13, v8\n"
        "v_add_u32 v238, 4096, v9\n"
        "v_add_u32 v239, 3200, v9\n"
        "v_add_u32 v240, 67584, v10\n"
        "v_add_u32 v241, 66688, v10\n"
        "s_cmp_ge_u32 s40, 2\n"
        "s_cselect_b32 s50, s45, s44\n"
        "s_and_b32 s51, s40, 1\n"
        "s_lshl1_add_u32 s50, s50, s51\n"
        "s_lshl_b32 s50, s50, 16\n"
        "s_lshl_b32 s51, s40, 17\n"
        "s_lshl_b32 s52, s43, 8\n"
        "s_add_u32 s51, s51, s52\n"
        "s_waitcnt lgkmcnt(0)\n"
        "s_add_u32 s60, s14, s51\n"
        "s_addc_u32 s61, s15, 0\n"
        "s_sub_u32 s60, s60, 4096\n"
        "s_subb_u32 s61, s61, 0\n"
        "s_add_u32 m0, s73, 0x10000\n"
        "s_nop 0\n"
        "global_load_lds_dwordx4 v238, s[60:61] offset:0\n"
        "global_load_lds_dwordx4 v239, s[60:61] offset:1024\n"
        "global_load_lds_dwordx4 v240, s[60:61] offset:2048\n"
        "global_load_lds_dwordx4 v241, s[60:61] offset:3072\n"
        "v_xor_b32 v6, v5, v2\n"
        "v_lshlrev_b32 v6, 4, v6\n"
        "v_xor_b32 v7, 64, v6\n"
        "v_lshl_add_u32 v234, v4, 10, v6\n"
        "v_lshl_add_u32 v8, v4, 10, v7\n"
        "v_add_u32 v235, 7168, v8\n"
        "v_add_u32 v236, 14336, v234\n"
        "v_add_u32 v237, 21504, v8\n"
        "s_add_u32 s56, s10, s50\n"
        "s_addc_u32 s57, s11, 0\n"
        "s_add_u32 m0, s72, 0x0\n"
        "s_nop 0\n"
        "global_load_lds_dwordx4 v234, s[56:57] offset:0\n"
        "global_load_lds_dwordx4 v235, s[56:57] offset:1024\n"
        "global_load_lds_dwordx4 v236, s[56:57] offset:2048\n"
        "global_load_lds_dwordx4 v237, s[56:57] offset:3072\n"
        "s_getpc_b64 s[46:47]\n"
        "LPC_%=:\n"
        "s_lshl2_add_u32 s53, s42, s40\n"
        "s_lshl_b32 s53, s53, 10\n"
        "v_lshlrev_b32 v12, 6, v1\n"
        "v_add_u32 v12, s53, v12\n"
        "v_min_u32 v252, LEND_%=-LPC_%=-4, v12\n"
        "global_load_dword v253, v252, s[46:47] offset:0\n"
        "s_add_u32 s62, s16, s51\n"
        "s_addc_u32 s63, s17, 0\n"
        "s_sub_u32 s62, s62, 4096\n"
        "s_subb_u32 s63, s63, 0\n"
        "s_add_u32 m0, s73, 0x14000\n"
        "s_nop 0\n"
        "global_load_lds_dwordx4 v238, s[62:63] offset:0\n"
        "global_load_lds_dwordx4 v239, s[62:63] offset:1024\n"
        "global_load_lds_dwordx4 v240, s[62:63] offset:2048\n"
        "global_load_lds_dwordx4 v241, s[62:63] offset:3072\n"
        "s_add_u32 s60, s60, 0x80000\n"
        "s_addc_u32 s61, s61, 0\n"
        "s_add_u32 s62, s62, 0x80000\n"
        "s_addc_u32 s63, s63, 0\n"
        "s_add_u32 s58, s56, 0x8000\n"
        "s_addc_u32 s59, s57, 0\n"
        "s_add_u32 s68, s12, s51\n"
        "s_addc_u32 s69, s13, 0\n"
        "s_add_u32 m0, s72, 0x1000\n"
        "s_nop 0\n"
        "global_load_lds_dwordx4 v234, s[58:59] offset:0\n"
        "global_load_lds_dwordx4 v235, s[58:59] offset:1024\n"
        "global_load_lds_dwordx4 v236, s[58:59] offset:2048\n"
        "global_load_lds_dwordx4 v237, s[58:59] offset:3072\n"
        "s_sub_u32 s68, s68, 4096\n"
        "s_subb_u32 s69, s69, 0\n"
        "v_lshrrev_b32 v8, 1, v1\n"
        "v_xor_b32 v8, v8, v2\n"
        "v_lshlrev_b32 v8, 4, v8\n"
        "v_lshl_add_u32 v8, v1, 7, v8\n"
        "v_xor_b32 v9, 64, v8\n"
        "s_lshr_b32 s50, s40, 1\n"
        "s_lshl_b32 s50, s50, 13\n"
        "v_add_u32 v224, s50, v8\n"
        "v_add_u32 v246, s72, v8\n"
        "v_add_u32 v248, 0x10000, v246\n"
        "v_add_u32 v225, s50, v9\n"
        "v_add_u32 v247, s72, v9\n"
        "v_add_u32 v249, 0x10000, v247\n"
        "s_and_b32 s50, s40, 1\n"
        "s_lshl_b32 s50, s50, 10\n"
        "s_add_u32 s50, s50, 0x10000\n"
        "v_lshlrev_b32 v8, 11, v2\n"
        "v_add_u32 v8, s50, v8\n"
        "v_lshrrev_b32 v9, 2, v1\n"
        "v_or_b32 v10, 4, v9\n"
        "v_and_b32 v11, 1, v2\n"
        "v_and_b32 v13, 3, v0\n"
        "v_lshlrev_b32 v14, 2, v11\n"
        "v_xor_b32 v14, v14, v9\n"
        "v_bfe_u32 v15, v14, 1, 2\n"
        "v_xor_b32 v15, 0, v15\n"
        "v_lshlrev_b32 v15, 5, v15\n"
        "v_lshl_add_u32 v14, v14, 7, v15\n"
        "v_lshl_add_u32 v14, v13, 3, v14\n"
        "v_add_u32 v226, v14, v8\n"
        "v_xor_b32 v228, 32, v226\n"
        "v_xor_b32 v230, 64, v226\n"
        "v_xor_b32 v232, 96, v226\n"
        "v_lshlrev_b32 v14, 2, v11\n"
        "v_xor_b32 v14, v14, v10\n"
        "v_bfe_u32 v15, v14, 1, 2\n"
        "v_xor_b32 v15, 0, v15\n"
        "v_lshlrev_b32 v15, 5, v15\n"
        "v_lshl_add_u32 v14, v14, 7, v15\n"
        "v_lshl_add_u32 v14, v13, 3, v14\n"
        "v_add_u32 v227, v14, v8\n"
        "v_xor_b32 v229, 32, v227\n"
        "v_xor_b32 v231, 64, v227\n"
        "v_xor_b32 v233, 96, v227\n"
        "v_add_u32 v216, 0x10000, v226\n"
        "v_add_u32 v217, 0x10000, v227\n"
        "v_add_u32 v218, 0x10000, v228\n"
        "v_add_u32 v219, 0x10000, v229\n"
        "v_add_u32 v220, 0x10000, v230\n"
        "v_add_u32 v221, 0x10000, v231\n"
        "v_add_u32 v222, 0x10000, v232\n"
        "v_add_u32 v223, 0x10000, v233\n"
        "v_lshl_add_u32 v242, v4, 11, v6\n"
        "v_lshl_add_u32 v8, v4, 11, v7\n"
        "v_add_u32 v243, 15360, v8\n"
        "v_add_u32 v244, 129024, v242\n"
        "v_add_u32 v245, 144384, v8\n"
        "s_lshl_b32 s50, s45, 18\n"
        "s_lshl_b32 s51, s40, 15\n"
        "s_add_u32 s50, s50, s51\n"
        "s_add_u32 s64, s8, s50\n"
        "s_addc_u32 s65, s9, 0\n"
        "s_lshl_b32 s50, s44, 18\n"
        "s_add_u32 s50, s50, s51\n"
        "s_add_u32 s66, s8, s50\n"
        "s_addc_u32 s67, s9, 0\n"
        "s_add_u32 s48, s38, 0x3b00000\n"
        "s_addc_u32 s49, s39, 0\n"
        "v_mov_b32 v11, 0\n"
        "global_load_dwordx2 v[250:251], v11, s[48:49] sc1\n"
        "s_lshl2_add_u32 s82, s55, s40\n"
        "s_lshl_b32 s82, s82, 11\n"
        "v_and_b32 v20, 0xf0, v254\n"
        "v_lshl_add_u32 v20, v20, 3, s82\n"
        "global_load_dword v253, v20, s[10:11] offset:0\n"
        "s_lshl2_add_u32 s82, s42, s40\n"
        "s_lshl3_add_u32 s82, s82, s41\n"
        "s_lshl_b32 s82, s82, 10\n"
        "v_and_b32 v21, 0x70, v254\n"
        "v_lshl_add_u32 v173, v21, 3, s82\n"
        "s_lshl2_add_u32 s82, s55, s40\n"
        "s_lshl_b32 s82, s82, 12\n"
        "v_bfe_u32 v20, v254, 4, 1\n"
        "v_bfe_u32 v21, v254, 5, 1\n"
        "v_lshlrev_b32 v20, 7, v20\n"
        "v_lshl_add_u32 v20, v21, 11, v20\n"
        "v_add_u32 v199, s82, v20\n"
        "s_mov_b64 exec, 0\n"
        "s_cmp_eq_u32 s40, 0\n"
        "s_cbranch_scc1 LHA0_%=\n"
        "s_cmp_eq_u32 s40, 1\n"
        "s_cbranch_scc1 LHA1_%=\n"
        "s_cmp_eq_u32 s40, 2\n"
        "s_cbranch_scc1 LHA2_%=\n"
        "s_branch LHA3_%=\n"
        "LHRA_%=:\n"
        "s_mov_b64 exec, -1\n"
        "s_add_u32 s56, s56, 0x80\n"
        "s_addc_u32 s57, s57, 0\n"
        "s_add_u32 s58, s58, 0x80\n"
        "s_addc_u32 s59, s59, 0\n"
        "s_add_u32 m0, s73, 0x18000\n"
        "s_nop 0\n"
        "global_load_lds_dwordx4 v238, s[60:61] offset:0\n"
        "global_load_lds_dwordx4 v239, s[60:61] offset:1024\n"
        "global_load_lds_dwordx4 v240, s[60:61] offset:2048\n"
        "global_load_lds_dwordx4 v241, s[60:61] offset:3072\n"
        "s_add_u32 m0, s73, 0x1c000\n"
        "s_nop 0\n"
        "global_load_lds_dwordx4 v238, s[62:63] offset:0\n"
        "global_load_lds_dwordx4 v239, s[62:63] offset:1024\n"
        "global_load_lds_dwordx4 v240, s[62:63] offset:2048\n"
        "global_load_lds_dwordx4 v241, s[62:63] offset:3072\n"
        "s_add_u32 s60, s60, 0x80000\n"
        "s_addc_u32 s61, s61, 0\n"
        "s_add_u32 s62, s62, 0x80000\n"
        "s_addc_u32 s63, s63, 0\n"
        "; ---- FRONT\n"
        "s_add_u32 m0, s72, 0x8000\n"
        "s_waitcnt vmcnt(19)\n"
        "global_load_lds_dwordx4 v234, s[56:57] offset:0\n"
        "s_barrier\n"
        "ds_read_b64_tr_b16 v[32:33], v226 offset:0\n"
        "ds_read_b64_tr_b16 v[34:35], v227 offset:0\n"
        "ds_read_b128 v[0:3], v224 offset:16384\n"
        "global_load_lds_dwordx4 v235, s[56:57] offset:1024\n"
        "global_load_lds_dwordx4 v236, s[56:57] offset:2048\n"
        "ds_read_b128 v[4:7], v225 offset:16384\n"
        "ds_read_b64_tr_b16 v[64:65], v226 offset:8192\n"
        "ds_read_b64_tr_b16 v[66:67], v227 offset:8192\n"
        "s_lshl_b32 s50, s42, 6\n"
        "s_waitcnt lgkmcnt(3)\n"
        "v_mfma_f32_16x16x32_bf16 a[0:3], v[32:35], v[0:3], 0\n"
        "ds_read_b128 v[12:15], v225 offset:18432\n"
        "global_load_lds_dwordx4 v237, s[56:57] offset:3072\n"
        "ds_read_b128 v[8:11], v224 offset:18432\n"
        "s_add_u32 m0, s72, 0x9000\n"
        "s_waitcnt lgkmcnt(2)\n"
        "global_load_lds_dwordx4 v234, s[58:59] offset:0\n"
        "ds_read_b64_tr_b16 v[36:37], v228 offset:0\n"
        "ds_read_b64_tr_b16 v[38:39], v229 offset:0\n"
        "s_lshl_b32 s51, s41, 7\n"
        "v_mfma_f32_16x16x32_bf16 a[0:3], v[64:67], v[4:7], a[0:3]\n"
        "ds_read_b64_tr_b16 v[68:69], v228 offset:8192\n"
        "s_lshl_b32 s52, s40, 5\n"
        "s_waitcnt lgkmcnt(4)\n"
        "v_mfma_f32_16x16x32_bf16 a[32:35], v[64:67], v[12:15], 0\n"
        "ds_read_b64_tr_b16 v[70:71], v229 offset:8192\n"
        "ds_read_b64_tr_b16 v[40:41], v230 offset:0\n"
        "ds_read_b64_tr_b16 v[42:43], v231 offset:0\n"
        "s_add_u32 s51, s51, s52\n"
        "s_waitcnt lgkmcnt(6)\n"
        "v_mfma_f32_16x16x32_bf16 a[32:35], v[32:35], v[8:11], a[32:35]\n"
        "ds_read_b64_tr_b16 v[72:73], v230 offset:8192\n"
        "ds_read_b64_tr_b16 v[74:75], v231 offset:8192\n"
        "ds_read_b64_tr_b16 v[44:45], v232 offset:0\n"
        "ds_read_b64_tr_b16 v[46:47], v233 offset:0\n"
        "ds_read_b64_tr_b16 v[76:77], v232 offset:8192\n"
        "ds_read_b64_tr_b16 v[78:79], v233 offset:8192\n"
        "ds_read_b128 v[16:19], v224 offset:0\n"
        "ds_read_b128 v[20:23], v225 offset:0\n"
        "s_and_b32 s91, s43, 15\n"
        "s_waitcnt lgkmcnt(12)\n"
        "v_mfma_f32_16x16x32_bf16 a[36:39], v[36:39], v[8:11], 0\n"
        "ds_read_b128 v[24:27], v224 offset:2048\n"
        "ds_read_b128 v[28:31], v225 offset:2048\n"
        "global_load_lds_dwordx4 v235, s[58:59] offset:1024\n"
        "s_mul_i32 s96, s40, 4224\n"
        "s_waitcnt lgkmcnt(12)\n"
        "v_mfma_f32_16x16x32_bf16 a[36:39], v[68:71], v[12:15], a[36:39]\n"
        "s_add_u32 s56, s56, 0x80\n"
        "s_addc_u32 s57, s57, 0\n"
        "v_lshrrev_b32 v166, 4, v254\n"
        "v_mfma_f32_16x16x32_bf16 a[4:7], v[68:71], v[4:7], 0\n"
        "s_waitcnt lgkmcnt(10)\n"
        "v_lshrrev_b32 v167, 3, v166\n"
        "v_and_b32 v168, 7, v166\n"
        "v_mfma_f32_16x16x32_bf16 a[4:7], v[36:39], v[0:3], a[4:7]\n"
        "s_waitcnt lgkmcnt(8)\n"
        "v_lshl_add_u32 v167, v167, 3, s50\n"
        "v_lshl_add_u32 v168, v168, 2, s51\n"
        "v_mfma_f32_16x16x32_bf16 a[8:11], v[40:43], v[0:3], 0\n"
        "global_load_lds_dwordx4 v236, s[58:59] offset:2048\n"
        "s_add_u32 s96, s96, 0x20240\n"
        "v_lshlrev_b32 v169, 11, v167\n"
        "v_mfma_f32_16x16x32_bf16 a[8:11], v[72:75], v[4:7], a[8:11]\n"
        "s_waitcnt lgkmcnt(5)\n"
        "v_lshl_add_u32 v169, v168, 1, v169\n"
        "v_cmp_eq_u32 s[78:79], 0, v167\n"
        "v_mfma_f32_16x16x32_bf16 a[40:43], v[72:75], v[12:15], 0\n"
        "s_waitcnt lgkmcnt(3)\n"
        "v_mov_b32 v171, 6144\n"
        "v_cndmask_b32 v170, 0, v171, s[78:79]\n"
        "v_mfma_f32_16x16x32_bf16 a[40:43], v[40:43], v[8:11], a[40:43]\n"
        "s_waitcnt vmcnt(21)\n"
        "v_add_u32 v170, v170, v169\n"
        "v_sub_u32 v171, 2040, v167\n"
        "v_mfma_f32_16x16x32_bf16 a[44:47], v[44:47], v[8:11], 0\n"
        "s_barrier\n"
        "v_mfma_f32_16x16x32_bf16 a[44:47], v[76:79], v[12:15], a[44:47]\n"
        "ds_read_b64_tr_b16 v[48:49], v226 offset:16384\n"
        "global_load_lds_dwordx4 v237, s[58:59] offset:3072\n"
        "s_add_u32 m0, s73, 0x20000\n"
        "v_mfma_f32_16x16x32_bf16 a[12:15], v[76:79], v[4:7], 0\n"
        "ds_read_b64_tr_b16 v[50:51], v227 offset:16384\n"
        "s_add_u32 s58, s58, 0x80\n"
        "s_addc_u32 s59, s59, 0\n"
        "v_mfma_f32_16x16x32_bf16 a[12:15], v[44:47], v[0:3], a[12:15]\n"
        "ds_read_b64_tr_b16 v[80:81], v226 offset:24576\n"
        "v_lshlrev_b32 v171, 1, v171\n"
        "v_mfma_f32_16x16x32_bf16 a[128:131], v[32:35], v[16:19], 0\n"
        "ds_read_b64_tr_b16 v[82:83], v227 offset:24576\n"
        "s_waitcnt lgkmcnt(4)\n"
        "v_mfma_f32_16x16x32_bf16 a[128:131], v[64:67], v[20:23], a[128:131]\n"
        "ds_read_b64_tr_b16 v[52:53], v228 offset:16384\n"
        "global_load_lds_dwordx4 v238, s[60:61] offset:0\n"
        "v_mfma_f32_16x16x32_bf16 a[160:163], v[64:67], v[28:31], 0\n"
        "ds_read_b64_tr_b16 v[54:55], v229 offset:16384\n"
        "v_lshl_add_u32 v171, v168, 12, v171\n"
        "v_mfma_f32_16x16x32_bf16 a[160:163], v[32:35], v[24:27], a[160:163]\n"
        "ds_read_b64_tr_b16 v[84:85], v228 offset:24576\n"
        "v_lshlrev_b32 v168, 3, v168\n"
        "v_mfma_f32_16x16x32_bf16 a[164:167], v[36:39], v[24:27], 0\n"
        "ds_read_b64_tr_b16 v[86:87], v229 offset:24576\n"
        "s_sub_u32 s74, s20, 2048\n"
        "s_subb_u32 s75, s21, 0\n"
        "v_mfma_f32_16x16x32_bf16 a[164:167], v[68:71], v[28:31], a[164:167]\n"
        "ds_read_b64_tr_b16 v[56:57], v230 offset:16384\n"
        "global_load_lds_dwordx4 v239, s[60:61] offset:1024\n"
        "v_mfma_f32_16x16x32_bf16 a[132:135], v[68:71], v[20:23], 0\n"
        "ds_read_b64_tr_b16 v[58:59], v231 offset:16384\n"
        "s_cmp_lt_u32 s43, 16\n"
        "s_cselect_b32 s90, s44, s45\n"
        "v_mfma_f32_16x16x32_bf16 a[132:135], v[36:39], v[16:19], a[132:135]\n"
        "ds_read_b64_tr_b16 v[88:89], v230 offset:24576\n"
        "s_lshl4_add_u32 s90, s90, s91\n"
        "s_lshl2_add_u32 s90, s90, s40\n"
        "v_mfma_f32_16x16x32_bf16 a[136:139], v[40:43], v[16:19], 0\n"
        "ds_read_b64_tr_b16 v[90:91], v231 offset:24576\n"
        "s_lshl_b32 s90, s90, 12\n"
        "s_lshl_b32 s91, s44, 5\n"
        "v_mfma_f32_16x16x32_bf16 a[136:139], v[72:75], v[20:23], a[136:139]\n"
        "ds_read_b64_tr_b16 v[60:61], v232 offset:16384\n"
        "global_load_lds_dwordx4 v240, s[60:61] offset:2048\n"
        "s_add_u32 s91, s91, s43\n"
        "v_mfma_f32_16x16x32_bf16 a[168:171], v[72:75], v[28:31], 0\n"
        "ds_read_b64_tr_b16 v[62:63], v233 offset:16384\n"
        "s_add_u32 s92, s8, s90\n"
        "s_addc_u32 s93, s9, 0\n"
        "v_mfma_f32_16x16x32_bf16 a[168:171], v[40:43], v[24:27], a[168:171]\n"
        "ds_read_b64_tr_b16 v[92:93], v232 offset:24576\n"
        "s_lshl2_add_u32 s91, s91, s40\n"
        "s_lshl_b32 s80, s91, 1\n"
        "v_mfma_f32_16x16x32_bf16 a[172:175], v[44:47], v[24:27], 0\n"
        "ds_read_b64_tr_b16 v[94:95], v233 offset:24576\n"
        "s_add_u32 s81, s80, 1\n"
        "s_lshl_b32 s90, s41, 7\n"
        "v_mfma_f32_16x16x32_bf16 a[172:175], v[76:79], v[28:31], a[172:175]\n"
        "global_load_lds_dwordx4 v241, s[60:61] offset:3072\n"
        "s_add_u32 m0, s73, 0x24000\n"
        "v_mfma_f32_16x16x32_bf16 a[140:143], v[76:79], v[20:23], 0\n"
        "s_add_u32 s60, s60, 0x80000\n"
        "s_addc_u32 s61, s61, 0\n"
        "s_lshl2_add_u32 s90, s42, s90\n"
        "v_mfma_f32_16x16x32_bf16 a[140:143], v[44:47], v[16:19], a[140:143]\n"
        "s_add_u32 s90, s90, s40\n"
        "s_lshl_b32 s90, s90, 1\n"
        "s_waitcnt lgkmcnt(5)\n"
        "v_mfma_f32_16x16x32_bf16 a[16:19], v[0:3], v[48:51], 0\n"
        "s_add_u32 s76, s38, 0x3301000\n"
        "s_addc_u32 s77, s39, 0\n"
        "v_mov_b32 v172, s90\n"
        "v_mfma_f32_16x16x32_bf16 a[16:19], v[4:7], v[80:83], a[16:19]\n"
        "global_load_lds_dwordx4 v238, s[62:63] offset:0\n"
        "s_lshl_b32 s80, s80, 7\n"
        "v_mfma_f32_16x16x32_bf16 a[48:51], v[12:15], v[80:83], 0\n"
        "s_lshl_b32 s81, s81, 7\n"
        "v_lshlrev_b32 v198, 11, v172\n"
        "v_mfma_f32_16x16x32_bf16 a[48:51], v[8:11], v[48:51], a[48:51]\n"
        "v_mfma_f32_16x16x32_bf16 a[52:55], v[8:11], v[52:55], 0\n"
        "v_mfma_f32_16x16x32_bf16 a[52:55], v[12:15], v[84:87], a[52:55]\n"
        "global_load_lds_dwordx4 v239, s[62:63] offset:1024\n"
        "v_mfma_f32_16x16x32_bf16 a[20:23], v[4:7], v[84:87], 0\n"
        "s_waitcnt lgkmcnt(0)\n"
        "v_mfma_f32_16x16x32_bf16 a[20:23], v[0:3], v[52:55], a[20:23]\n"
        "v_mfma_f32_16x16x32_bf16 a[24:27], v[0:3], v[56:59], 0\n"
        "v_mfma_f32_16x16x32_bf16 a[24:27], v[4:7], v[88:91], a[24:27]\n"
        "global_load_lds_dwordx4 v240, s[62:63] offset:2048\n"
        "v_mfma_f32_16x16x32_bf16 a[56:59], v[12:15], v[88:91], 0\n"
        "v_mfma_f32_16x16x32_bf16 a[56:59], v[8:11], v[56:59], a[56:59]\n"
        "v_mfma_f32_16x16x32_bf16 a[60:63], v[8:11], v[60:63], 0\n"
        "v_mfma_f32_16x16x32_bf16 a[60:63], v[12:15], v[92:95], a[60:63]\n"
        "global_load_lds_dwordx4 v241, s[62:63] offset:3072\n"
        "s_add_u32 m0, s72, 0x0\n"
        "v_mfma_f32_16x16x32_bf16 a[28:31], v[4:7], v[92:95], 0\n"
        "s_add_u32 s62, s62, 0x80000\n"
        "s_addc_u32 s63, s63, 0\n"
        "v_mfma_f32_16x16x32_bf16 a[28:31], v[0:3], v[60:63], a[28:31]\n"
        "s_waitcnt vmcnt(26)\n"
        "v_mfma_f32_16x16x32_bf16 a[144:147], v[16:19], v[48:51], 0\n"
        "s_barrier\n"
        "v_mfma_f32_16x16x32_bf16 a[144:147], v[20:23], v[80:83], a[144:147]\n"
        "ds_read_b128 v[0:3], v224 offset:20480\n"
        "v_mfma_f32_16x16x32_bf16 a[176:179], v[28:31], v[80:83], 0\n"
        "ds_read_b128 v[4:7], v225 offset:20480\n"
        "v_mfma_f32_16x16x32_bf16 a[176:179], v[24:27], v[48:51], a[176:179]\n"
        "ds_read_b128 v[8:11], v224 offset:22528\n"
        "v_mfma_f32_16x16x32_bf16 a[180:183], v[24:27], v[52:55], 0\n"
        "ds_read_b128 v[12:15], v225 offset:22528\n"
        "v_mfma_f32_16x16x32_bf16 a[180:183], v[28:31], v[84:87], a[180:183]\n"
        "v_mfma_f32_16x16x32_bf16 a[148:151], v[20:23], v[84:87], 0\n"
        "v_mfma_f32_16x16x32_bf16 a[148:151], v[16:19], v[52:55], a[148:151]\n"
        "global_load_dwordx4 v[134:137], v168, s[24:25] offset:0\n"
        "v_mfma_f32_16x16x32_bf16 a[152:155], v[16:19], v[56:59], 0\n"
        "v_mfma_f32_16x16x32_bf16 a[152:155], v[20:23], v[88:91], a[152:155]\n"
        "v_mfma_f32_16x16x32_bf16 a[184:187], v[28:31], v[88:91], 0\n"
        "v_mfma_f32_16x16x32_bf16 a[184:187], v[24:27], v[56:59], a[184:187]\n"
        "v_mfma_f32_16x16x32_bf16 a[188:191], v[24:27], v[60:63], 0\n"
        "ds_read_b128 v[24:27], v224 offset:6144\n"
        "s_waitcnt lgkmcnt(1)\n"
        "v_mfma_f32_16x16x32_bf16 a[188:191], v[28:31], v[92:95], a[188:191]\n"
        "ds_read_b128 v[28:31], v225 offset:6144\n"
        "v_mfma_f32_16x16x32_bf16 a[156:159], v[20:23], v[92:95], 0\n"
        "ds_read_b128 v[20:23], v225 offset:4096\n"
        "v_mfma_f32_16x16x32_bf16 a[156:159], v[16:19], v[60:63], a[156:159]\n"
        "ds_read_b128 v[16:19], v224 offset:4096\n"
        "v_mfma_f32_16x16x32_bf16 a[64:67], v[32:35], v[0:3], 0\n"
        "v_mfma_f32_16x16x32_bf16 a[64:67], v[64:67], v[4:7], a[64:67]\n"
        "v_mfma_f32_16x16x32_bf16 a[96:99], v[64:67], v[12:15], 0\n"
        "v_mfma_f32_16x16x32_bf16 a[96:99], v[32:35], v[8:11], a[96:99]\n"
        "v_mfma_f32_16x16x32_bf16 a[100:103], v[36:39], v[8:11], 0\n"
        "v_mfma_f32_16x16x32_bf16 a[100:103], v[68:71], v[12:15], a[100:103]\n"
        "v_mfma_f32_16x16x32_bf16 a[68:71], v[68:71], v[4:7], 0\n"
        "v_mfma_f32_16x16x32_bf16 a[68:71], v[36:39], v[0:3], a[68:71]\n"
        "global_load_dwordx4 v[138:141], v168, s[24:25] offset:16\n"
        "v_mfma_f32_16x16x32_bf16 a[72:75], v[40:43], v[0:3], 0\n"
        "v_mfma_f32_16x16x32_bf16 a[72:75], v[72:75], v[4:7], a[72:75]\n"
        "v_mfma_f32_16x16x32_bf16 a[104:107], v[72:75], v[12:15], 0\n"
        "v_mfma_f32_16x16x32_bf16 a[104:107], v[40:43], v[8:11], a[104:107]\n"
        "v_mfma_f32_16x16x32_bf16 a[108:111], v[44:47], v[8:11], 0\n"
        "v_mfma_f32_16x16x32_bf16 a[108:111], v[76:79], v[12:15], a[108:111]\n"
        "v_mfma_f32_16x16x32_bf16 a[76:79], v[76:79], v[4:7], 0\n"
        "v_mfma_f32_16x16x32_bf16 a[76:79], v[44:47], v[0:3], a[76:79]\n"
        "v_mfma_f32_16x16x32_bf16 a[80:83], v[0:3], v[48:51], 0\n"
        "v_mfma_f32_16x16x32_bf16 a[80:83], v[4:7], v[80:83], a[80:83]\n"
        "v_mfma_f32_16x16x32_bf16 a[112:115], v[12:15], v[80:83], 0\n"
        "v_mfma_f32_16x16x32_bf16 a[112:115], v[8:11], v[48:51], a[112:115]\n"
        "v_mfma_f32_16x16x32_bf16 a[116:119], v[8:11], v[52:55], 0\n"
        "v_mfma_f32_16x16x32_bf16 a[116:119], v[12:15], v[84:87], a[116:119]\n"
        "v_mfma_f32_16x16x32_bf16 a[84:87], v[4:7], v[84:87], 0\n"
        "v_mfma_f32_16x16x32_bf16 a[84:87], v[0:3], v[52:55], a[84:87]\n"
        "global_load_dwordx2 v[96:97], v170, s[74:75] offset:-4096\n"
        "v_mfma_f32_16x16x32_bf16 a[88:91], v[0:3], v[56:59], 0\n"
        "v_mfma_f32_16x16x32_bf16 a[88:91], v[4:7], v[88:91], a[88:91]\n"
        "v_mfma_f32_16x16x32_bf16 a[120:123], v[12:15], v[88:91], 0\n"
        "v_mfma_f32_16x16x32_bf16 a[120:123], v[8:11], v[56:59], a[120:123]\n"
        "s_waitcnt vmcnt(11) lgkmcnt(0)\n"
        "v_mfma_f32_16x16x32_bf16 a[124:127], v[8:11], v[60:63], 0\n"
        "s_barrier\n"
        "v_mfma_f32_16x16x32_bf16 a[124:127], v[12:15], v[92:95], a[124:127]\n"
        "ds_read_b128 v[8:11], v224 offset:51200\n"
        "global_load_lds_dwordx4 v234, s[56:57] offset:0\n"
        "v_mfma_f32_16x16x32_bf16 a[92:95], v[4:7], v[92:95], 0\n"
        "ds_read_b128 v[4:7], v225 offset:49152\n"
        "v_mfma_f32_16x16x32_bf16 a[92:95], v[0:3], v[60:63], a[92:95]\n"
        "ds_read_b128 v[0:3], v224 offset:49152\n"
        "s_add_u32 s82, s12, 0x0\n"
        "s_addc_u32 s83, s13, 0\n"
        "v_mfma_f32_16x16x32_bf16 a[192:195], v[32:35], v[16:19], 0\n"
        "ds_read_b128 v[12:15], v225 offset:51200\n"
        "v_mfma_f32_16x16x32_bf16 a[192:195], v[64:67], v[20:23], a[192:195]\n"
        "global_load_lds_dwordx4 v235, s[56:57] offset:1024\n"
        "v_mfma_f32_16x16x32_bf16 a[224:227], v[64:67], v[28:31], 0\n"
        "ds_read_b64_tr_b16 v[64:65], v226 offset:40960\n"
        "v_mfma_f32_16x16x32_bf16 a[224:227], v[32:35], v[24:27], a[224:227]\n"
        "ds_read_b64_tr_b16 v[32:33], v226 offset:32768\n"
        "v_mfma_f32_16x16x32_bf16 a[228:231], v[36:39], v[24:27], 0\n"
        "ds_read_b64_tr_b16 v[34:35], v227 offset:32768\n"
        "v_mfma_f32_16x16x32_bf16 a[228:231], v[68:71], v[28:31], a[228:231]\n"
        "ds_read_b64_tr_b16 v[66:67], v227 offset:40960\n"
        "global_load_lds_dwordx4 v236, s[56:57] offset:2048\n"
        "v_mfma_f32_16x16x32_bf16 a[196:199], v[68:71], v[20:23], 0\n"
        "ds_read_b64_tr_b16 v[68:69], v228 offset:40960\n"
        "v_mfma_f32_16x16x32_bf16 a[196:199], v[36:39], v[16:19], a[196:199]\n"
        "ds_read_b64_tr_b16 v[36:37], v228 offset:32768\n"
        "v_mfma_f32_16x16x32_bf16 a[200:203], v[40:43], v[16:19], 0\n"
        "ds_read_b64_tr_b16 v[38:39], v229 offset:32768\n"
        "v_mfma_f32_16x16x32_bf16 a[200:203], v[72:75], v[20:23], a[200:203]\n"
        "ds_read_b64_tr_b16 v[70:71], v229 offset:40960\n"
        "global_load_lds_dwordx4 v237, s[56:57] offset:3072\n"
        "s_add_u32 m0, s72, 0x1000\n"
        "v_mfma_f32_16x16x32_bf16 a[232:235], v[72:75], v[28:31], 0\n"
        "ds_read_b64_tr_b16 v[72:73], v230 offset:40960\n"
        "s_add_u32 s56, s56, 0x80\n"
        "s_addc_u32 s57, s57, 0\n"
        "v_mfma_f32_16x16x32_bf16 a[232:235], v[40:43], v[24:27], a[232:235]\n"
        "ds_read_b64_tr_b16 v[40:41], v230 offset:32768\n"
        "v_mfma_f32_16x16x32_bf16 a[236:239], v[44:47], v[24:27], 0\n"
        "ds_read_b64_tr_b16 v[42:43], v231 offset:32768\n"
        "v_mfma_f32_16x16x32_bf16 a[236:239], v[76:79], v[28:31], a[236:239]\n"
        "ds_read_b64_tr_b16 v[74:75], v231 offset:40960\n"
        "global_load_dwordx2 v[98:99], v170, s[74:75] offset:-2048\n"
        "v_mfma_f32_16x16x32_bf16 a[204:207], v[76:79], v[20:23], 0\n"
        "ds_read_b64_tr_b16 v[76:77], v232 offset:40960\n"
        "global_load_lds_dwordx4 v234, s[58:59] offset:0\n"
        "v_mfma_f32_16x16x32_bf16 a[204:207], v[44:47], v[16:19], a[204:207]\n"
        "ds_read_b64_tr_b16 v[44:45], v232 offset:32768\n"
        "v_mfma_f32_16x16x32_bf16 a[208:211], v[16:19], v[48:51], 0\n"
        "ds_read_b64_tr_b16 v[46:47], v233 offset:32768\n"
        "v_mfma_f32_16x16x32_bf16 a[208:211], v[20:23], v[80:83], a[208:211]\n"
        "ds_read_b64_tr_b16 v[78:79], v233 offset:40960\n"
        "v_mfma_f32_16x16x32_bf16 a[240:243], v[28:31], v[80:83], 0\n"
        "ds_read_b64_tr_b16 v[80:81], v226 offset:57344\n"
        "global_load_lds_dwordx4 v235, s[58:59] offset:1024\n"
        "v_mfma_f32_16x16x32_bf16 a[240:243], v[24:27], v[48:51], a[240:243]\n"
        "ds_read_b64_tr_b16 v[48:49], v226 offset:49152\n"
        "v_mfma_f32_16x16x32_bf16 a[244:247], v[24:27], v[52:55], 0\n"
        "ds_read_b64_tr_b16 v[50:51], v227 offset:49152\n"
        "v_mfma_f32_16x16x32_bf16 a[244:247], v[28:31], v[84:87], a[244:247]\n"
        "ds_read_b64_tr_b16 v[82:83], v227 offset:57344\n"
        "v_mfma_f32_16x16x32_bf16 a[212:215], v[20:23], v[84:87], 0\n"
        "ds_read_b64_tr_b16 v[84:85], v228 offset:57344\n"
        "global_load_lds_dwordx4 v236, s[58:59] offset:2048\n"
        "v_mfma_f32_16x16x32_bf16 a[212:215], v[16:19], v[52:55], a[212:215]\n"
        "ds_read_b64_tr_b16 v[52:53], v228 offset:49152\n"
        "v_mfma_f32_16x16x32_bf16 a[216:219], v[16:19], v[56:59], 0\n"
        "ds_read_b64_tr_b16 v[54:55], v229 offset:49152\n"
        "v_mfma_f32_16x16x32_bf16 a[216:219], v[20:23], v[88:91], a[216:219]\n"
        "ds_read_b64_tr_b16 v[86:87], v229 offset:57344\n"
        "v_mfma_f32_16x16x32_bf16 a[248:251], v[28:31], v[88:91], 0\n"
        "ds_read_b64_tr_b16 v[88:89], v230 offset:57344\n"
        "global_load_lds_dwordx4 v237, s[58:59] offset:3072\n"
        "s_add_u32 m0, s73, 0x10000\n"
        "v_mfma_f32_16x16x32_bf16 a[248:251], v[24:27], v[56:59], a[248:251]\n"
        "ds_read_b64_tr_b16 v[56:57], v230 offset:49152\n"
        "s_add_u32 s58, s58, 0x80\n"
        "s_addc_u32 s59, s59, 0\n"
        "v_mfma_f32_16x16x32_bf16 a[252:255], v[24:27], v[60:63], 0\n"
        "ds_read_b64_tr_b16 v[58:59], v231 offset:49152\n"
        "s_waitcnt lgkmcnt(8)\n"
        "v_mfma_f32_16x16x32_bf16 a[252:255], v[28:31], v[92:95], a[252:255]\n"
        "ds_read_b64_tr_b16 v[90:91], v231 offset:57344\n"
        "v_mfma_f32_16x16x32_bf16 a[220:223], v[20:23], v[92:95], 0\n"
        "ds_read_b64_tr_b16 v[92:93], v232 offset:57344\n"
        "global_load_dwordx2 v[100:101], v170, s[74:75] offset:0\n"
        "v_mfma_f32_16x16x32_bf16 a[220:223], v[16:19], v[60:63], a[220:223]\n"
        "ds_read_b64_tr_b16 v[60:61], v232 offset:49152\n"
        "global_load_lds_dwordx4 v238, s[60:61] offset:0\n"
        "v_mfma_f32_16x16x32_bf16 a[0:3], v[32:35], v[0:3], a[0:3]\n"
        "ds_read_b64_tr_b16 v[62:63], v233 offset:49152\n"
        "v_mfma_f32_16x16x32_bf16 a[0:3], v[64:67], v[4:7], a[0:3]\n"
        "ds_read_b64_tr_b16 v[94:95], v233 offset:57344\n"
        "v_mfma_f32_16x16x32_bf16 a[32:35], v[64:67], v[12:15], a[32:35]\n"
        "ds_read_b128 v[16:19], v224 offset:53248\n"
        "v_mfma_f32_16x16x32_bf16 a[32:35], v[32:35], v[8:11], a[32:35]\n"
        "ds_read_b128 v[20:23], v225 offset:53248\n"
        "global_load_lds_dwordx4 v239, s[60:61] offset:1024\n"
        "v_mfma_f32_16x16x32_bf16 a[36:39], v[36:39], v[8:11], a[36:39]\n"
        "ds_read_b128 v[24:27], v224 offset:55296\n"
        "v_mfma_f32_16x16x32_bf16 a[36:39], v[68:71], v[12:15], a[36:39]\n"
        "ds_read_b128 v[28:31], v225 offset:55296\n"
        "v_mfma_f32_16x16x32_bf16 a[4:7], v[68:71], v[4:7], a[4:7]\n"
        "v_mfma_f32_16x16x32_bf16 a[4:7], v[36:39], v[0:3], a[4:7]\n"
        "global_load_lds_dwordx4 v240, s[60:61] offset:2048\n"
        "v_mfma_f32_16x16x32_bf16 a[8:11], v[40:43], v[0:3], a[8:11]\n"
        "v_mfma_f32_16x16x32_bf16 a[8:11], v[72:75], v[4:7], a[8:11]\n"
        "v_mfma_f32_16x16x32_bf16 a[40:43], v[72:75], v[12:15], a[40:43]\n"
        "v_mfma_f32_16x16x32_bf16 a[40:43], v[40:43], v[8:11], a[40:43]\n"
        "global_load_lds_dwordx4 v241, s[60:61] offset:3072\n"
        "s_add_u32 m0, s73, 0x14000\n"
        "v_mfma_f32_16x16x32_bf16 a[44:47], v[44:47], v[8:11], a[44:47]\n"
        "s_add_u32 s60, s60, 0x80000\n"
        "s_addc_u32 s61, s61, 0\n"
        "v_mfma_f32_16x16x32_bf16 a[44:47], v[76:79], v[12:15], a[44:47]\n"
        "s_waitcnt lgkmcnt(0)\n"
        "v_mfma_f32_16x16x32_bf16 a[12:15], v[76:79], v[4:7], a[12:15]\n"
        "v_mfma_f32_16x16x32_bf16 a[12:15], v[44:47], v[0:3], a[12:15]\n"
        "global_load_dwordx2 v[102:103], v169, s[74:75] offset:2048\n"
        "v_mfma_f32_16x16x32_bf16 a[16:19], v[0:3], v[48:51], a[16:19]\n"
        "global_load_lds_dwordx4 v238, s[62:63] offset:0\n"
        "s_add_u32 s74, s20, 0x1800\n"
        "s_addc_u32 s75, s21, 0\n"
        "v_mfma_f32_16x16x32_bf16 a[16:19], v[4:7], v[80:83], a[16:19]\n"
        "v_mfma_f32_16x16x32_bf16 a[48:51], v[12:15], v[80:83], a[48:51]\n"
        "v_mfma_f32_16x16x32_bf16 a[48:51], v[8:11], v[48:51], a[48:51]\n"
        "v_mfma_f32_16x16x32_bf16 a[52:55], v[8:11], v[52:55], a[52:55]\n"
        "global_load_lds_dwordx4 v239, s[62:63] offset:1024\n"
        "v_mfma_f32_16x16x32_bf16 a[52:55], v[12:15], v[84:87], a[52:55]\n"
        "v_mfma_f32_16x16x32_bf16 a[20:23], v[4:7], v[84:87], a[20:23]\n"
        "v_mfma_f32_16x16x32_bf16 a[20:23], v[0:3], v[52:55], a[20:23]\n"
        "v_mfma_f32_16x16x32_bf16 a[24:27], v[0:3], v[56:59], a[24:27]\n"
        "global_load_lds_dwordx4 v240, s[62:63] offset:2048\n"
        "v_mfma_f32_16x16x32_bf16 a[24:27], v[4:7], v[88:91], a[24:27]\n"
        "v_mfma_f32_16x16x32_bf16 a[56:59], v[12:15], v[88:91], a[56:59]\n"
        "v_mfma_f32_16x16x32_bf16 a[56:59], v[8:11], v[56:59], a[56:59]\n"
        "v_mfma_f32_16x16x32_bf16 a[60:63], v[8:11], v[60:63], a[60:63]\n"
        "ds_read_b128 v[8:11], v224 offset:34816\n"
        "global_load_lds_dwordx4 v241, s[62:63] offset:3072\n"
        "s_add_u32 m0, s72, 0x8000\n"
        "v_mfma_f32_16x16x32_bf16 a[60:63], v[12:15], v[92:95], a[60:63]\n"
        "ds_read_b128 v[12:15], v225 offset:34816\n"
        "s_add_u32 s62, s62, 0x80000\n"
        "s_addc_u32 s63, s63, 0\n"
        "v_mfma_f32_16x16x32_bf16 a[28:31], v[4:7], v[92:95], a[28:31]\n"
        "ds_read_b128 v[4:7], v225 offset:32768\n"
        "v_mfma_f32_16x16x32_bf16 a[28:31], v[0:3], v[60:63], a[28:31]\n"
        "ds_read_b128 v[0:3], v224 offset:32768\n"
        "v_mfma_f32_16x16x32_bf16 a[64:67], v[32:35], v[16:19], a[64:67]\n"
        "global_load_dwordx2 v[104:105], v169, s[74:75] offset:-4096\n"
        "v_mfma_f32_16x16x32_bf16 a[64:67], v[64:67], v[20:23], a[64:67]\n"
        "v_mfma_f32_16x16x32_bf16 a[96:99], v[64:67], v[28:31], a[96:99]\n"
        "LHA0_%=:\n"
        "s_cbranch_execz LHA4_%=\n"
        "v_mfma_f32_16x16x32_bf16 a[96:99], v[32:35], v[24:27], a[96:99]\n"
        "v_mfma_f32_16x16x32_bf16 a[100:103], v[36:39], v[24:27], a[100:103]\n"
        "v_mfma_f32_16x16x32_bf16 a[100:103], v[68:71], v[28:31], a[100:103]\n"
        "v_mfma_f32_16x16x32_bf16 a[68:71], v[68:71], v[20:23], a[68:71]\n"
        "v_mfma_f32_16x16x32_bf16 a[68:71], v[36:39], v[16:19], a[68:71]\n"
        "global_load_dwordx2 v[106:107], v169, s[74:75] offset:-2048\n"
        "v_mfma_f32_16x16x32_bf16 a[72:75], v[40:43], v[16:19], a[72:75]\n"
        "v_mfma_f32_16x16x32_bf16 a[72:75], v[72:75], v[20:23], a[72:75]\n"
        "v_mfma_f32_16x16x32_bf16 a[104:107], v[72:75], v[28:31], a[104:107]\n"
        "v_mfma_f32_16x16x32_bf16 a[104:107], v[40:43], v[24:27], a[104:107]\n"
        "v_mfma_f32_16x16x32_bf16 a[108:111], v[44:47], v[24:27], a[108:111]\n"
        "v_mfma_f32_16x16x32_bf16 a[108:111], v[76:79], v[28:31], a[108:111]\n"
        "v_mfma_f32_16x16x32_bf16 a[76:79], v[76:79], v[20:23], a[76:79]\n"
        "v_mfma_f32_16x16x32_bf16 a[76:79], v[44:47], v[16:19], a[76:79]\n"
        "v_mfma_f32_16x16x32_bf16 a[80:83], v[16:19], v[48:51], a[80:83]\n"
        "v_mfma_f32_16x16x32_bf16 a[80:83], v[20:23], v[80:83], a[80:83]\n"
        "v_mfma_f32_16x16x32_bf16 a[112:115], v[28:31], v[80:83], a[112:115]\n"
        "v_mfma_f32_16x16x32_bf16 a[112:115], v[24:27], v[48:51], a[112:115]\n"
        "v_mfma_f32_16x16x32_bf16 a[116:119], v[24:27], v[52:55], a[116:119]\n"
        "s_waitcnt vmcnt(23) lgkmcnt(0)\n"
        "v_lshlrev_b32 v185, 16, v134\n"
        "v_and_b32 v186, 0xffff0000, v134\n"
        "v_mfma_f32_16x16x32_bf16 a[116:119], v[28:31], v[84:87], a[116:119]\n"
        "v_lshlrev_b32 v187, 16, v135\n"
        "v_and_b32 v188, 0xffff0000, v135\n"
        "v_mfma_f32_16x16x32_bf16 a[84:87], v[20:23], v[84:87], a[84:87]\n"
        "v_mfma_f32_16x16x32_bf16 a[84:87], v[16:19], v[52:55], a[84:87]\n"
        "global_load_dwordx2 v[108:109], v169, s[74:75] offset:0\n"
        "v_mfma_f32_16x16x32_bf16 a[88:91], v[16:19], v[56:59], a[88:91]\n"
        "v_mfma_f32_16x16x32_bf16 a[88:91], v[20:23], v[88:91], a[88:91]\n"
        "v_mfma_f32_16x16x32_bf16 a[120:123], v[28:31], v[88:91], a[120:123]\n"
        "v_mfma_f32_16x16x32_bf16 a[120:123], v[24:27], v[56:59], a[120:123]\n"
        "v_mfma_f32_16x16x32_bf16 a[124:127], v[24:27], v[60:63], a[124:127]\n"
        "ds_read_b128 v[24:27], v224 offset:38912\n"
        "v_mfma_f32_16x16x32_bf16 a[124:127], v[28:31], v[92:95], a[124:127]\n"
        "ds_read_b128 v[28:31], v225 offset:38912\n"
        "v_mfma_f32_16x16x32_bf16 a[92:95], v[20:23], v[92:95], a[92:95]\n"
        "ds_read_b128 v[20:23], v225 offset:36864\n"
        "v_mfma_f32_16x16x32_bf16 a[92:95], v[16:19], v[60:63], a[92:95]\n"
        "ds_read_b128 v[16:19], v224 offset:36864\n"
        "v_mfma_f32_16x16x32_bf16 a[128:131], v[32:35], v[0:3], a[128:131]\n"
        "v_mfma_f32_16x16x32_bf16 a[128:131], v[64:67], v[4:7], a[128:131]\n"
        "v_mfma_f32_16x16x32_bf16 a[160:163], v[64:67], v[12:15], a[160:163]\n"
        "v_mfma_f32_16x16x32_bf16 a[160:163], v[32:35], v[8:11], a[160:163]\n"
        "v_mfma_f32_16x16x32_bf16 a[164:167], v[36:39], v[8:11], a[164:167]\n"
        "v_mfma_f32_16x16x32_bf16 a[164:167], v[68:71], v[12:15], a[164:167]\n"
        "v_mfma_f32_16x16x32_bf16 a[132:135], v[68:71], v[4:7], a[132:135]\n"
        "v_mfma_f32_16x16x32_bf16 a[132:135], v[36:39], v[0:3], a[132:135]\n"
        "global_load_dwordx2 v[110:111], v169, s[74:75] offset:2048\n"
        "s_add_u32 s74, s20, 0x3800\n"
        "s_addc_u32 s75, s21, 0\n"
        "v_mfma_f32_16x16x32_bf16 a[136:139], v[40:43], v[0:3], a[136:139]\n"
        "v_mfma_f32_16x16x32_bf16 a[136:139], v[72:75], v[4:7], a[136:139]\n"
        "v_mfma_f32_16x16x32_bf16 a[168:171], v[72:75], v[12:15], a[168:171]\n"
        "v_mfma_f32_16x16x32_bf16 a[168:171], v[40:43], v[8:11], a[168:171]\n"
        "v_mfma_f32_16x16x32_bf16 a[172:175], v[44:47], v[8:11], a[172:175]\n"
        "v_mfma_f32_16x16x32_bf16 a[172:175], v[76:79], v[12:15], a[172:175]\n"
        "v_mfma_f32_16x16x32_bf16 a[140:143], v[76:79], v[4:7], a[140:143]\n"
        "v_mfma_f32_16x16x32_bf16 a[140:143], v[44:47], v[0:3], a[140:143]\n"
        "v_mfma_f32_16x16x32_bf16 a[144:147], v[0:3], v[48:51], a[144:147]\n"
        "v_mfma_f32_16x16x32_bf16 a[144:147], v[4:7], v[80:83], a[144:147]\n"
        "v_mfma_f32_16x16x32_bf16 a[176:179], v[12:15], v[80:83], a[176:179]\n"
        "v_mfma_f32_16x16x32_bf16 a[176:179], v[8:11], v[48:51], a[176:179]\n"
        "v_mfma_f32_16x16x32_bf16 a[180:183], v[8:11], v[52:55], a[180:183]\n"
        "s_waitcnt vmcnt(23) lgkmcnt(0)\n"
        "v_cndmask_b32 v96, v96, 0, s[78:79]\n"
        "v_cndmask_b32 v97, v97, 0, s[78:79]\n"
        "v_mfma_f32_16x16x32_bf16 a[180:183], v[12:15], v[84:87], a[180:183]\n"
        "v_lshlrev_b32 v174, 16, v96\n"
        "v_mul_f32 v189, v174, v188\n"
        "v_mfma_f32_16x16x32_bf16 a[148:151], v[4:7], v[84:87], a[148:151]\n"
        "v_cvt_pk_bf16_f32 v189, 0, v189\n"
        "v_and_b32 v174, 0xffff0000, v96\n"
        "v_mfma_f32_16x16x32_bf16 a[148:151], v[0:3], v[52:55], a[148:151]\n"
        "global_load_dwordx2 v[112:113], v169, s[74:75] offset:-4096\n"
        "v_mfma_f32_16x16x32_bf16 a[152:155], v[0:3], v[56:59], a[152:155]\n"
        "v_mfma_f32_16x16x32_bf16 a[152:155], v[4:7], v[88:91], a[152:155]\n"
        "v_mfma_f32_16x16x32_bf16 a[184:187], v[12:15], v[88:91], a[184:187]\n"
        "v_mfma_f32_16x16x32_bf16 a[184:187], v[8:11], v[56:59], a[184:187]\n"
        "s_waitcnt vmcnt(15)\n"
        "v_mfma_f32_16x16x32_bf16 a[188:191], v[8:11], v[60:63], a[188:191]\n"
        "s_barrier\n"
        "v_mfma_f32_16x16x32_bf16 a[188:191], v[12:15], v[92:95], a[188:191]\n"
        "ds_read_b128 v[8:11], v224 offset:18432\n"
        "global_load_lds_dwordx4 v234, s[56:57] offset:0\n"
        "v_mfma_f32_16x16x32_bf16 a[156:159], v[4:7], v[92:95], a[156:159]\n"
        "ds_read_b128 v[4:7], v225 offset:16384\n"
        "v_mfma_f32_16x16x32_bf16 a[156:159], v[0:3], v[60:63], a[156:159]\n"
        "ds_read_b128 v[0:3], v224 offset:16384\n"
        "v_mfma_f32_16x16x32_bf16 a[192:195], v[32:35], v[16:19], a[192:195]\n"
        "ds_read_b128 v[12:15], v225 offset:18432\n"
        "v_mfma_f32_16x16x32_bf16 a[192:195], v[64:67], v[20:23], a[192:195]\n"
        "global_load_lds_dwordx4 v235, s[56:57] offset:1024\n"
        "v_mfma_f32_16x16x32_bf16 a[224:227], v[64:67], v[28:31], a[224:227]\n"
        "ds_read_b64_tr_b16 v[64:65], v216 offset:8192\n"
        "v_mfma_f32_16x16x32_bf16 a[224:227], v[32:35], v[24:27], a[224:227]\n"
        "ds_read_b64_tr_b16 v[32:33], v216 offset:0\n"
        "v_mfma_f32_16x16x32_bf16 a[228:231], v[36:39], v[24:27], a[228:231]\n"
        "ds_read_b64_tr_b16 v[34:35], v217 offset:0\n"
        "v_mfma_f32_16x16x32_bf16 a[228:231], v[68:71], v[28:31], a[228:231]\n"
        "ds_read_b64_tr_b16 v[66:67], v217 offset:8192\n"
        "global_load_lds_dwordx4 v236, s[56:57] offset:2048\n"
        "v_mfma_f32_16x16x32_bf16 a[196:199], v[68:71], v[20:23], a[196:199]\n"
        "ds_read_b64_tr_b16 v[68:69], v218 offset:8192\n"
        "v_mfma_f32_16x16x32_bf16 a[196:199], v[36:39], v[16:19], a[196:199]\n"
        "ds_read_b64_tr_b16 v[36:37], v218 offset:0\n"
        "v_mfma_f32_16x16x32_bf16 a[200:203], v[40:43], v[16:19], a[200:203]\n"
        "ds_read_b64_tr_b16 v[38:39], v219 offset:0\n"
        "v_mfma_f32_16x16x32_bf16 a[200:203], v[72:75], v[20:23], a[200:203]\n"
        "ds_read_b64_tr_b16 v[70:71], v219 offset:8192\n"
        "global_load_lds_dwordx4 v237, s[56:57] offset:3072\n"
        "s_add_u32 m0, s72, 0x9000\n"
        "v_mfma_f32_16x16x32_bf16 a[232:235], v[72:75], v[28:31], a[232:235]\n"
        "ds_read_b64_tr_b16 v[72:73], v220 offset:8192\n"
        "s_add_u32 s56, s56, 0x80\n"
        "s_addc_u32 s57, s57, 0\n"
        "v_mfma_f32_16x16x32_bf16 a[232:235], v[40:43], v[24:27], a[232:235]\n"
        "ds_read_b64_tr_b16 v[40:41], v220 offset:0\n"
        "v_cndmask_b32 v98, v98, 0, s[78:79]\n"
        "v_mfma_f32_16x16x32_bf16 a[236:239], v[44:47], v[24:27], a[236:239]\n"
        "ds_read_b64_tr_b16 v[42:43], v221 offset:0\n"
        "v_cndmask_b32 v99, v99, 0, s[78:79]\n"
        "v_mfma_f32_16x16x32_bf16 a[236:239], v[76:79], v[28:31], a[236:239]\n"
        "ds_read_b64_tr_b16 v[74:75], v221 offset:8192\n"
        "global_load_dwordx2 v[114:115], v169, s[74:75] offset:-2048\n"
        "v_mfma_f32_16x16x32_bf16 a[204:207], v[76:79], v[20:23], a[204:207]\n"
        "ds_read_b64_tr_b16 v[76:77], v222 offset:8192\n"
        "global_load_lds_dwordx4 v234, s[58:59] offset:0\n"
        "v_mfma_f32_16x16x32_bf16 a[204:207], v[44:47], v[16:19], a[204:207]\n"
        "ds_read_b64_tr_b16 v[44:45], v222 offset:0\n"
        "v_lshlrev_b32 v175, 16, v98\n"
        "v_mfma_f32_16x16x32_bf16 a[208:211], v[16:19], v[48:51], a[208:211]\n"
        "ds_read_b64_tr_b16 v[46:47], v223 offset:0\n"
        "v_mul_f32 v191, v175, v188\n"
        "v_mfma_f32_16x16x32_bf16 a[208:211], v[20:23], v[80:83], a[208:211]\n"
        "ds_read_b64_tr_b16 v[78:79], v223 offset:8192\n"
        "v_cvt_pk_bf16_f32 v191, 0, v191\n"
        "v_mfma_f32_16x16x32_bf16 a[240:243], v[28:31], v[80:83], a[240:243]\n"
        "ds_read_b64_tr_b16 v[80:81], v216 offset:24576\n"
        "global_load_lds_dwordx4 v235, s[58:59] offset:1024\n"
        "v_mfma_f32_16x16x32_bf16 a[240:243], v[24:27], v[48:51], a[240:243]\n"
        "ds_read_b64_tr_b16 v[48:49], v216 offset:16384\n"
        "v_mul_f32 v190, v175, v187\n"
        "v_mfma_f32_16x16x32_bf16 a[244:247], v[24:27], v[52:55], a[244:247]\n"
        "ds_read_b64_tr_b16 v[50:51], v217 offset:16384\n"
        "v_cvt_pk_bf16_f32 v190, 0, v190\n"
        "v_mfma_f32_16x16x32_bf16 a[244:247], v[28:31], v[84:87], a[244:247]\n"
        "ds_read_b64_tr_b16 v[82:83], v217 offset:24576\n"
        "v_add_f32 v189, v189, v190\n"
        "v_mfma_f32_16x16x32_bf16 a[212:215], v[20:23], v[84:87], a[212:215]\n"
        "ds_read_b64_tr_b16 v[84:85], v218 offset:24576\n"
        "global_load_lds_dwordx4 v236, s[58:59] offset:2048\n"
        "v_mfma_f32_16x16x32_bf16 a[212:215], v[16:19], v[52:55], a[212:215]\n"
        "ds_read_b64_tr_b16 v[52:53], v218 offset:16384\n"
        "v_cvt_pk_bf16_f32 v189, 0, v189\n"
        "v_mfma_f32_16x16x32_bf16 a[216:219], v[16:19], v[56:59], a[216:219]\n"
        "ds_read_b64_tr_b16 v[54:55], v219 offset:16384\n"
        "v_and_b32 v175, 0xffff0000, v98\n"
        "v_mfma_f32_16x16x32_bf16 a[216:219], v[20:23], v[88:91], a[216:219]\n"
        "ds_read_b64_tr_b16 v[86:87], v219 offset:24576\n"
        "v_mfma_f32_16x16x32_bf16 a[248:251], v[28:31], v[88:91], a[248:251]\n"
        "ds_read_b64_tr_b16 v[88:89], v220 offset:24576\n"
        "global_load_lds_dwordx4 v237, s[58:59] offset:3072\n"
        "s_add_u32 m0, s73, 0x18000\n"
        "v_mfma_f32_16x16x32_bf16 a[248:251], v[24:27], v[56:59], a[248:251]\n"
        "ds_read_b64_tr_b16 v[56:57], v220 offset:16384\n"
        "s_add_u32 s58, s58, 0x80\n"
        "s_addc_u32 s59, s59, 0\n"
        "v_mfma_f32_16x16x32_bf16 a[252:255], v[24:27], v[60:63], a[252:255]\n"
        "ds_read_b64_tr_b16 v[58:59], v221 offset:16384\n"
        "s_waitcnt lgkmcnt(8)\n"
        "v_mfma_f32_16x16x32_bf16 a[252:255], v[28:31], v[92:95], a[252:255]\n"
        "ds_read_b64_tr_b16 v[90:91], v221 offset:24576\n"
        "s_waitcnt vmcnt(23) lgkmcnt(8)\n"
        "v_mfma_f32_16x16x32_bf16 a[220:223], v[20:23], v[92:95], a[220:223]\n"
        "ds_read_b64_tr_b16 v[92:93], v222 offset:24576\n"
        "global_load_dwordx2 v[116:117], v169, s[74:75] offset:0\n"
        "v_mfma_f32_16x16x32_bf16 a[220:223], v[16:19], v[60:63], a[220:223]\n"
        "ds_read_b64_tr_b16 v[60:61], v222 offset:16384\n"
        "global_load_lds_dwordx4 v238, s[60:61] offset:0\n"
        "v_mfma_f32_16x16x32_bf16 a[0:3], v[32:35], v[0:3], a[0:3]\n"
        "ds_read_b64_tr_b16 v[62:63], v223 offset:16384\n"
        "s_add_u32 s74, s22, 0x1000\n"
        "s_addc_u32 s75, s23, 0\n"
        "v_mfma_f32_16x16x32_bf16 a[0:3], v[64:67], v[4:7], a[0:3]\n"
        "ds_read_b64_tr_b16 v[94:95], v223 offset:24576\n"
        "v_cndmask_b32 v100, v100, 0, s[78:79]\n"
        "v_mfma_f32_16x16x32_bf16 a[32:35], v[64:67], v[12:15], a[32:35]\n"
        "ds_read_b128 v[16:19], v224 offset:20480\n"
        "v_cndmask_b32 v101, v101, 0, s[78:79]\n"
        "v_mfma_f32_16x16x32_bf16 a[32:35], v[32:35], v[8:11], a[32:35]\n"
        "ds_read_b128 v[20:23], v225 offset:20480\n"
        "global_load_lds_dwordx4 v239, s[60:61] offset:1024\n"
        "v_mfma_f32_16x16x32_bf16 a[36:39], v[36:39], v[8:11], a[36:39]\n"
        "ds_read_b128 v[24:27], v224 offset:22528\n"
        "v_lshlrev_b32 v176, 16, v100\n"
        "v_mfma_f32_16x16x32_bf16 a[36:39], v[68:71], v[12:15], a[36:39]\n"
        "ds_read_b128 v[28:31], v225 offset:22528\n"
        "v_mul_f32 v192, v176, v187\n"
        "v_mfma_f32_16x16x32_bf16 a[4:7], v[68:71], v[4:7], a[4:7]\n"
        "v_cvt_pk_bf16_f32 v192, 0, v192\n"
        "v_add_f32 v191, v191, v192\n"
        "v_mfma_f32_16x16x32_bf16 a[4:7], v[36:39], v[0:3], a[4:7]\n"
        "global_load_lds_dwordx4 v240, s[60:61] offset:2048\n"
        "v_mfma_f32_16x16x32_bf16 a[8:11], v[40:43], v[0:3], a[8:11]\n"
        "v_cvt_pk_bf16_f32 v191, 0, v191\n"
        "v_mul_f32 v190, v176, v186\n"
        "v_mfma_f32_16x16x32_bf16 a[8:11], v[72:75], v[4:7], a[8:11]\n"
        "v_cvt_pk_bf16_f32 v190, 0, v190\n"
        "v_add_f32 v189, v189, v190\n"
        "v_mfma_f32_16x16x32_bf16 a[40:43], v[72:75], v[12:15], a[40:43]\n"
        "v_cvt_pk_bf16_f32 v189, 0, v189\n"
        "v_mul_f32 v193, v176, v188\n"
        "v_mfma_f32_16x16x32_bf16 a[40:43], v[40:43], v[8:11], a[40:43]\n"
        "global_load_lds_dwordx4 v241, s[60:61] offset:3072\n"
        "s_add_u32 m0, s73, 0x1c000\n"
        "v_mfma_f32_16x16x32_bf16 a[44:47], v[44:47], v[8:11], a[44:47]\n"
        "s_add_u32 s60, s60, 0x80000\n"
        "s_addc_u32 s61, s61, 0\n"
        "v_cvt_pk_bf16_f32 v193, 0, v193\n"
        "v_mfma_f32_16x16x32_bf16 a[44:47], v[76:79], v[12:15], a[44:47]\n"
        "s_waitcnt vmcnt(23) lgkmcnt(0)\n"
        "v_lshlrev_b32 v177, 16, v102\n"
        "v_mul_f32 v192, v177, v186\n"
        "v_mfma_f32_16x16x32_bf16 a[12:15], v[76:79], v[4:7], a[12:15]\n"
        "v_cvt_pk_bf16_f32 v192, 0, v192\n"
        "v_add_f32 v191, v191, v192\n"
        "v_mfma_f32_16x16x32_bf16 a[12:15], v[44:47], v[0:3], a[12:15]\n"
        "global_load_dwordx2 v[118:119], v169, s[74:75] offset:-4096\n"
        "v_mfma_f32_16x16x32_bf16 a[16:19], v[0:3], v[48:51], a[16:19]\n"
        "global_load_lds_dwordx4 v238, s[62:63] offset:0\n"
        "v_cvt_pk_bf16_f32 v191, 0, v191\n"
        "v_mfma_f32_16x16x32_bf16 a[16:19], v[4:7], v[80:83], a[16:19]\n"
        "v_mul_f32 v190, v177, v185\n"
        "v_cvt_pk_bf16_f32 v190, 0, v190\n"
        "v_mfma_f32_16x16x32_bf16 a[48:51], v[12:15], v[80:83], a[48:51]\n"
        "v_add_f32 v189, v189, v190\n"
        "v_cvt_pk_bf16_f32 v189, 0, v189\n"
        "v_mfma_f32_16x16x32_bf16 a[48:51], v[8:11], v[48:51], a[48:51]\n"
        "v_mul_f32 v195, v177, v188\n"
        "v_cvt_pk_bf16_f32 v195, 0, v195\n"
        "v_mfma_f32_16x16x32_bf16 a[52:55], v[8:11], v[52:55], a[52:55]\n"
        "global_load_lds_dwordx4 v239, s[62:63] offset:1024\n"
        "v_mul_f32 v194, v177, v187\n"
        "v_mfma_f32_16x16x32_bf16 a[52:55], v[12:15], v[84:87], a[52:55]\n"
        "v_cvt_pk_bf16_f32 v194, 0, v194\n"
        "v_add_f32 v193, v193, v194\n"
        "v_mfma_f32_16x16x32_bf16 a[20:23], v[4:7], v[84:87], a[20:23]\n"
        "v_cvt_pk_bf16_f32 v193, 0, v193\n"
        "v_and_b32 v176, 0xffff0000, v100\n"
        "v_mfma_f32_16x16x32_bf16 a[20:23], v[0:3], v[52:55], a[20:23]\n"
        "v_and_b32 v177, 0xffff0000, v102\n"
        "v_mfma_f32_16x16x32_bf16 a[24:27], v[0:3], v[56:59], a[24:27]\n"
        "global_load_lds_dwordx4 v240, s[62:63] offset:2048\n"
        "v_mfma_f32_16x16x32_bf16 a[24:27], v[4:7], v[88:91], a[24:27]\n"
        "v_mfma_f32_16x16x32_bf16 a[56:59], v[12:15], v[88:91], a[56:59]\n"
        "v_mfma_f32_16x16x32_bf16 a[56:59], v[8:11], v[56:59], a[56:59]\n"
        "v_mfma_f32_16x16x32_bf16 a[60:63], v[8:11], v[60:63], a[60:63]\n"
        "ds_read_b128 v[8:11], v224 offset:2048\n"
        "global_load_lds_dwordx4 v241, s[62:63] offset:3072\n"
        "s_add_u32 m0, s72, 0x0\n"
        "v_mfma_f32_16x16x32_bf16 a[60:63], v[12:15], v[92:95], a[60:63]\n"
        "ds_read_b128 v[12:15], v225 offset:2048\n"
        "s_add_u32 s62, s62, 0x80000\n"
        "s_addc_u32 s63, s63, 0\n"
        "v_mfma_f32_16x16x32_bf16 a[28:31], v[4:7], v[92:95], a[28:31]\n"
        "ds_read_b128 v[4:7], v225 offset:0\n"
        "s_waitcnt vmcnt(23)\n"
        "v_mfma_f32_16x16x32_bf16 a[28:31], v[0:3], v[60:63], a[28:31]\n"
        "ds_read_b128 v[0:3], v224 offset:0\n"
        "v_lshlrev_b32 v178, 16, v104\n"
        "v_mfma_f32_16x16x32_bf16 a[64:67], v[32:35], v[16:19], a[64:67]\n"
        "global_load_dwordx2 v[120:121], v169, s[74:75] offset:-2048\n"
        "v_mfma_f32_16x16x32_bf16 a[64:67], v[64:67], v[20:23], a[64:67]\n"
        "v_mul_f32 v192, v178, v185\n"
        "v_cvt_pk_bf16_f32 v192, 0, v192\n"
        "v_mfma_f32_16x16x32_bf16 a[96:99], v[64:67], v[28:31], a[96:99]\n"
        "v_add_f32 v191, v191, v192\n"
        "v_cvt_pk_bf16_f32 v191, 0, v191\n"
        "v_mfma_f32_16x16x32_bf16 a[96:99], v[32:35], v[24:27], a[96:99]\n"
        "v_mul_f32 v196, v178, v187\n"
        "v_cvt_pk_bf16_f32 v196, 0, v196\n"
        "v_mfma_f32_16x16x32_bf16 a[100:103], v[36:39], v[24:27], a[100:103]\n"
        "s_waitcnt vmcnt(23) lgkmcnt(3)\n"
        "v_lshlrev_b32 v179, 16, v106\n"
        "v_add_f32 v195, v195, v196\n"
        "v_mfma_f32_16x16x32_bf16 a[100:103], v[68:71], v[28:31], a[100:103]\n"
        "v_cvt_pk_bf16_f32 v195, 0, v195\n"
        "v_mul_f32 v194, v178, v186\n"
        "v_mfma_f32_16x16x32_bf16 a[68:71], v[68:71], v[20:23], a[68:71]\n"
        "v_mul_f32 v196, v179, v186\n"
        "v_cvt_pk_bf16_f32 v194, 0, v194\n"
        "v_mfma_f32_16x16x32_bf16 a[68:71], v[36:39], v[16:19], a[68:71]\n"
        "global_load_dword v253, v173, s[82:83] offset:0\n"
        "s_add_u32 s82, s12, 0x100000\n"
        "s_addc_u32 s83, s13, 0\n"
        "v_mfma_f32_16x16x32_bf16 a[72:75], v[40:43], v[16:19], a[72:75]\n"
        "global_load_dwordx2 v[122:123], v169, s[74:75] offset:0\n"
        "v_cvt_pk_bf16_f32 v196, 0, v196\n"
        "v_mfma_f32_16x16x32_bf16 a[72:75], v[72:75], v[20:23], a[72:75]\n"
        "v_add_f32 v193, v193, v194\n"
        "v_add_f32 v195, v195, v196\n"
        "v_mfma_f32_16x16x32_bf16 a[104:107], v[72:75], v[28:31], a[104:107]\n"
        "v_cvt_pk_bf16_f32 v193, 0, v193\n"
        "v_cvt_pk_bf16_f32 v195, 0, v195\n"
        "v_mfma_f32_16x16x32_bf16 a[104:107], v[40:43], v[24:27], a[104:107]\n"
        "v_mul_f32 v194, v179, v185\n"
        "v_cvt_pk_bf16_f32 v194, 0, v194\n"
        "v_mfma_f32_16x16x32_bf16 a[108:111], v[44:47], v[24:27], a[108:111]\n"
        "v_add_f32 v193, v193, v194\n"
        "v_cvt_pk_bf16_f32 v193, 0, v193\n"
        "v_mfma_f32_16x16x32_bf16 a[108:111], v[76:79], v[28:31], a[108:111]\n"
        "v_mfma_f32_16x16x32_bf16 a[76:79], v[76:79], v[20:23], a[76:79]\n"
        "v_mfma_f32_16x16x32_bf16 a[76:79], v[44:47], v[16:19], a[76:79]\n"
        "v_mfma_f32_16x16x32_bf16 a[80:83], v[16:19], v[48:51], a[80:83]\n"
        "v_mfma_f32_16x16x32_bf16 a[80:83], v[20:23], v[80:83], a[80:83]\n"
        "v_mfma_f32_16x16x32_bf16 a[112:115], v[28:31], v[80:83], a[112:115]\n"
        "v_mfma_f32_16x16x32_bf16 a[112:115], v[24:27], v[48:51], a[112:115]\n"
        "v_mfma_f32_16x16x32_bf16 a[116:119], v[24:27], v[52:55], a[116:119]\n"
        "s_waitcnt vmcnt(24) lgkmcnt(0)\n"
        "v_lshlrev_b32 v180, 16, v108\n"
        "v_mul_f32 v196, v180, v185\n"
        "v_mfma_f32_16x16x32_bf16 a[116:119], v[28:31], v[84:87], a[116:119]\n"
        "v_cvt_pk_bf16_f32 v196, 0, v196\n"
        "v_add_f32 v195, v195, v196\n"
        "v_mfma_f32_16x16x32_bf16 a[84:87], v[20:23], v[84:87], a[84:87]\n"
        "v_cvt_pk_bf16_f32 v195, 0, v195\n"
        "v_mfma_f32_16x16x32_bf16 a[84:87], v[16:19], v[52:55], a[84:87]\n"
        "global_load_dwordx2 v[124:125], v169, s[74:75] offset:2048\n"
        "s_add_u32 s74, s22, 0x3000\n"
        "s_addc_u32 s75, s23, 0\n"
        "v_mfma_f32_16x16x32_bf16 a[88:91], v[16:19], v[56:59], a[88:91]\n"
        "v_mfma_f32_16x16x32_bf16 a[88:91], v[20:23], v[88:91], a[88:91]\n"
        "v_mfma_f32_16x16x32_bf16 a[120:123], v[28:31], v[88:91], a[120:123]\n"
        "v_mfma_f32_16x16x32_bf16 a[120:123], v[24:27], v[56:59], a[120:123]\n"
        "v_mfma_f32_16x16x32_bf16 a[124:127], v[24:27], v[60:63], a[124:127]\n"
        "ds_read_b128 v[24:27], v224 offset:6144\n"
        "v_mfma_f32_16x16x32_bf16 a[124:127], v[28:31], v[92:95], a[124:127]\n"
        "ds_read_b128 v[28:31], v225 offset:6144\n"
        "v_mfma_f32_16x16x32_bf16 a[92:95], v[20:23], v[92:95], a[92:95]\n"
        "ds_read_b128 v[20:23], v225 offset:4096\n"
        "v_mfma_f32_16x16x32_bf16 a[92:95], v[16:19], v[60:63], a[92:95]\n"
        "ds_read_b128 v[16:19], v224 offset:4096\n"
        "v_mfma_f32_16x16x32_bf16 a[128:131], v[32:35], v[0:3], a[128:131]\n"
        "v_mfma_f32_16x16x32_bf16 a[128:131], v[64:67], v[4:7], a[128:131]\n"
        "v_mfma_f32_16x16x32_bf16 a[160:163], v[64:67], v[12:15], a[160:163]\n"
        "v_mfma_f32_16x16x32_bf16 a[160:163], v[32:35], v[8:11], a[160:163]\n"
        "v_mfma_f32_16x16x32_bf16 a[164:167], v[36:39], v[8:11], a[164:167]\n"
        "s_waitcnt vmcnt(24) lgkmcnt(3)\n"
        "v_lshlrev_b32 v181, 16, v110\n"
        "v_mfma_f32_16x16x32_bf16 a[164:167], v[68:71], v[12:15], a[164:167]\n"
        "v_mfma_f32_16x16x32_bf16 a[132:135], v[68:71], v[4:7], a[132:135]\n"
        "v_mfma_f32_16x16x32_bf16 a[132:135], v[36:39], v[0:3], a[132:135]\n"
        "global_load_dwordx2 v[126:127], v169, s[74:75] offset:-4096\n"
        "v_mfma_f32_16x16x32_bf16 a[136:139], v[40:43], v[0:3], a[136:139]\n"
        "v_mfma_f32_16x16x32_bf16 a[136:139], v[72:75], v[4:7], a[136:139]\n"
        "v_mfma_f32_16x16x32_bf16 a[168:171], v[72:75], v[12:15], a[168:171]\n"
        "v_mfma_f32_16x16x32_bf16 a[168:171], v[40:43], v[8:11], a[168:171]\n"
        "v_mfma_f32_16x16x32_bf16 a[172:175], v[44:47], v[8:11], a[172:175]\n"
        "v_mfma_f32_16x16x32_bf16 a[172:175], v[76:79], v[12:15], a[172:175]\n"
        "v_mfma_f32_16x16x32_bf16 a[140:143], v[76:79], v[4:7], a[140:143]\n"
        "v_mfma_f32_16x16x32_bf16 a[140:143], v[44:47], v[0:3], a[140:143]\n"
        "v_mfma_f32_16x16x32_bf16 a[144:147], v[0:3], v[48:51], a[144:147]\n"
        "v_mfma_f32_16x16x32_bf16 a[144:147], v[4:7], v[80:83], a[144:147]\n"
        "v_mfma_f32_16x16x32_bf16 a[176:179], v[12:15], v[80:83], a[176:179]\n"
        "v_mfma_f32_16x16x32_bf16 a[176:179], v[8:11], v[48:51], a[176:179]\n"
        "v_mfma_f32_16x16x32_bf16 a[180:183], v[8:11], v[52:55], a[180:183]\n"
        "s_waitcnt vmcnt(24) lgkmcnt(0)\n"
        "v_lshlrev_b32 v182, 16, v112\n"
        "v_mfma_f32_16x16x32_bf16 a[180:183], v[12:15], v[84:87], a[180:183]\n"
        "v_mfma_f32_16x16x32_bf16 a[148:151], v[4:7], v[84:87], a[148:151]\n"
        "v_mfma_f32_16x16x32_bf16 a[148:151], v[0:3], v[52:55], a[148:151]\n"
        "global_load_dwordx2 v[128:129], v169, s[74:75] offset:-2048\n"
        "v_mfma_f32_16x16x32_bf16 a[152:155], v[0:3], v[56:59], a[152:155]\n"
        "v_mfma_f32_16x16x32_bf16 a[152:155], v[4:7], v[88:91], a[152:155]\n"
        "v_mfma_f32_16x16x32_bf16 a[184:187], v[12:15], v[88:91], a[184:187]\n"
        "v_mfma_f32_16x16x32_bf16 a[184:187], v[8:11], v[56:59], a[184:187]\n"
        "s_waitcnt vmcnt(16)\n"
        "v_mfma_f32_16x16x32_bf16 a[188:191], v[8:11], v[60:63], a[188:191]\n"
        "s_barrier\n"
        "v_mfma_f32_16x16x32_bf16 a[188:191], v[12:15], v[92:95], a[188:191]\n"
        "ds_read_b128 v[8:11], v224 offset:51200\n"
        "global_load_lds_dwordx4 v234, s[56:57] offset:0\n"
        "v_mfma_f32_16x16x32_bf16 a[156:159], v[4:7], v[92:95], a[156:159]\n"
        "ds_read_b128 v[4:7], v225 offset:49152\n"
        "v_mfma_f32_16x16x32_bf16 a[156:159], v[0:3], v[60:63], a[156:159]\n"
        "ds_read_b128 v[0:3], v224 offset:49152\n"
        "v_mfma_f32_16x16x32_bf16 a[192:195], v[32:35], v[16:19], a[192:195]\n"
        "ds_read_b128 v[12:15], v225 offset:51200\n"
        "v_mfma_f32_16x16x32_bf16 a[192:195], v[64:67], v[20:23], a[192:195]\n"
        "global_load_lds_dwordx4 v235, s[56:57] offset:1024\n"
        "v_mfma_f32_16x16x32_bf16 a[224:227], v[64:67], v[28:31], a[224:227]\n"
        "ds_read_b64_tr_b16 v[64:65], v226 offset:8192\n"
        "v_mfma_f32_16x16x32_bf16 a[224:227], v[32:35], v[24:27], a[224:227]\n"
        "ds_read_b64_tr_b16 v[32:33], v226 offset:0\n"
        "v_mfma_f32_16x16x32_bf16 a[228:231], v[36:39], v[24:27], a[228:231]\n"
        "ds_read_b64_tr_b16 v[34:35], v227 offset:0\n"
        "v_mfma_f32_16x16x32_bf16 a[228:231], v[68:71], v[28:31], a[228:231]\n"
        "ds_read_b64_tr_b16 v[66:67], v227 offset:8192\n"
        "global_load_lds_dwordx4 v236, s[56:57] offset:2048\n"
        "v_mfma_f32_16x16x32_bf16 a[196:199], v[68:71], v[20:23], a[196:199]\n"
        "ds_read_b64_tr_b16 v[68:69], v228 offset:8192\n"
        "v_mfma_f32_16x16x32_bf16 a[196:199], v[36:39], v[16:19], a[196:199]\n"
        "ds_read_b64_tr_b16 v[36:37], v228 offset:0\n"
        "v_mfma_f32_16x16x32_bf16 a[200:203], v[40:43], v[16:19], a[200:203]\n"
        "ds_read_b64_tr_b16 v[38:39], v229 offset:0\n"
        "v_mfma_f32_16x16x32_bf16 a[200:203], v[72:75], v[20:23], a[200:203]\n"
        "ds_read_b64_tr_b16 v[70:71], v229 offset:8192\n"
        "global_load_lds_dwordx4 v237, s[56:57] offset:3072\n"
        "s_add_u32 m0, s72, 0x1000\n"
        "v_mfma_f32_16x16x32_bf16 a[232:235], v[72:75], v[28:31], a[232:235]\n"
        "ds_read_b64_tr_b16 v[72:73], v230 offset:8192\n"
        "s_add_u32 s56, s56, 0x80\n"
        "s_addc_u32 s57, s57, 0\n"
        "v_mfma_f32_16x16x32_bf16 a[232:235], v[40:43], v[24:27], a[232:235]\n"
        "ds_read_b64_tr_b16 v[40:41], v230 offset:0\n"
        "v_lshlrev_b32 v183, 16, v114\n"
        "v_mfma_f32_16x16x32_bf16 a[236:239], v[44:47], v[24:27], a[236:239]\n"
        "ds_read_b64_tr_b16 v[42:43], v231 offset:0\n"
        "v_mfma_f32_16x16x32_bf16 a[236:239], v[76:79], v[28:31], a[236:239]\n"
        "ds_read_b64_tr_b16 v[74:75], v231 offset:8192\n"
        "global_load_dwordx2 v[130:131], v169, s[74:75] offset:0\n"
        "v_mfma_f32_16x16x32_bf16 a[204:207], v[76:79], v[20:23], a[204:207]\n"
        "ds_read_b64_tr_b16 v[76:77], v232 offset:8192\n"
        "global_load_lds_dwordx4 v234, s[58:59] offset:0\n"
        "v_mfma_f32_16x16x32_bf16 a[204:207], v[44:47], v[16:19], a[204:207]\n"
        "ds_read_b64_tr_b16 v[44:45], v232 offset:0\n"
        "v_mfma_f32_16x16x32_bf16 a[208:211], v[16:19], v[48:51], a[208:211]\n"
        "ds_read_b64_tr_b16 v[46:47], v233 offset:0\n"
        "v_mfma_f32_16x16x32_bf16 a[208:211], v[20:23], v[80:83], a[208:211]\n"
        "ds_read_b64_tr_b16 v[78:79], v233 offset:8192\n"
        "v_mfma_f32_16x16x32_bf16 a[240:243], v[28:31], v[80:83], a[240:243]\n"
        "ds_read_b64_tr_b16 v[80:81], v226 offset:24576\n"
        "global_load_lds_dwordx4 v235, s[58:59] offset:1024\n"
        "v_mfma_f32_16x16x32_bf16 a[240:243], v[24:27], v[48:51], a[240:243]\n"
        "ds_read_b64_tr_b16 v[48:49], v226 offset:16384\n"
        "v_mfma_f32_16x16x32_bf16 a[244:247], v[24:27], v[52:55], a[244:247]\n"
        "ds_read_b64_tr_b16 v[50:51], v227 offset:16384\n"
        "v_mfma_f32_16x16x32_bf16 a[244:247], v[28:31], v[84:87], a[244:247]\n"
        "ds_read_b64_tr_b16 v[82:83], v227 offset:24576\n"
        "v_mfma_f32_16x16x32_bf16 a[212:215], v[20:23], v[84:87], a[212:215]\n"
        "ds_read_b64_tr_b16 v[84:85], v228 offset:24576\n"
        "global_load_lds_dwordx4 v236, s[58:59] offset:2048\n"
        "v_mfma_f32_16x16x32_bf16 a[212:215], v[16:19], v[52:55], a[212:215]\n"
        "ds_read_b64_tr_b16 v[52:53], v228 offset:16384\n"
        "v_mfma_f32_16x16x32_bf16 a[216:219], v[16:19], v[56:59], a[216:219]\n"
        "ds_read_b64_tr_b16 v[54:55], v229 offset:16384\n"
        "v_mfma_f32_16x16x32_bf16 a[216:219], v[20:23], v[88:91], a[216:219]\n"
        "ds_read_b64_tr_b16 v[86:87], v229 offset:24576\n"
        "v_mfma_f32_16x16x32_bf16 a[248:251], v[28:31], v[88:91], a[248:251]\n"
        "ds_read_b64_tr_b16 v[88:89], v230 offset:24576\n"
        "global_load_dword v253, v173, s[82:83] offset:0\n"
        "v_mfma_f32_16x16x32_bf16 a[248:251], v[24:27], v[56:59], a[248:251]\n"
        "ds_read_b64_tr_b16 v[56:57], v230 offset:16384\n"
        "global_load_lds_dwordx4 v237, s[58:59] offset:3072\n"
        "s_add_u32 m0, s73, 0x20000\n"
        "v_mfma_f32_16x16x32_bf16 a[252:255], v[24:27], v[60:63], a[252:255]\n"
        "ds_read_b64_tr_b16 v[58:59], v231 offset:16384\n"
        "s_add_u32 s58, s58, 0x80\n"
        "s_addc_u32 s59, s59, 0\n"
        "v_mfma_f32_16x16x32_bf16 a[252:255], v[28:31], v[92:95], a[252:255]\n"
        "ds_read_b64_tr_b16 v[90:91], v231 offset:24576\n"
        "s_add_u32 s82, s12, 0x200000\n"
        "s_addc_u32 s83, s13, 0\n"
        "v_mfma_f32_16x16x32_bf16 a[220:223], v[20:23], v[92:95], a[220:223]\n"
        "ds_read_b64_tr_b16 v[92:93], v232 offset:24576\n"
        "s_waitcnt lgkmcnt(8)\n"
        "v_mfma_f32_16x16x32_bf16 a[220:223], v[16:19], v[60:63], a[220:223]\n"
        "ds_read_b64_tr_b16 v[60:61], v232 offset:16384\n"
        "global_load_dwordx2 v[132:133], v169, s[74:75] offset:2048\n"
        "v_mfma_f32_16x16x32_bf16 a[0:3], v[32:35], v[0:3], a[0:3]\n"
        "ds_read_b64_tr_b16 v[62:63], v233 offset:16384\n"
        "global_load_lds_dwordx4 v238, s[60:61] offset:0\n"
        "v_mfma_f32_16x16x32_bf16 a[0:3], v[64:67], v[4:7], a[0:3]\n"
        "ds_read_b64_tr_b16 v[94:95], v233 offset:24576\n"
        "s_waitcnt vmcnt(27) lgkmcnt(8)\n"
        "v_mfma_f32_16x16x32_bf16 a[32:35], v[64:67], v[12:15], a[32:35]\n"
        "ds_read_b128 v[16:19], v224 offset:53248\n"
        "v_lshlrev_b32 v184, 16, v116\n"
        "v_mfma_f32_16x16x32_bf16 a[32:35], v[32:35], v[8:11], a[32:35]\n"
        "ds_read_b128 v[20:23], v225 offset:53248\n"
        "v_mfma_f32_16x16x32_bf16 a[36:39], v[36:39], v[8:11], a[36:39]\n"
        "ds_read_b128 v[24:27], v224 offset:55296\n"
        "global_load_lds_dwordx4 v239, s[60:61] offset:1024\n"
        "v_mfma_f32_16x16x32_bf16 a[36:39], v[68:71], v[12:15], a[36:39]\n"
        "ds_read_b128 v[28:31], v225 offset:55296\n"
        "v_mfma_f32_16x16x32_bf16 a[4:7], v[68:71], v[4:7], a[4:7]\n"
        "v_mfma_f32_16x16x32_bf16 a[4:7], v[36:39], v[0:3], a[4:7]\n"
        "v_mfma_f32_16x16x32_bf16 a[8:11], v[40:43], v[0:3], a[8:11]\n"
        "global_load_lds_dwordx4 v240, s[60:61] offset:2048\n"
        "v_mfma_f32_16x16x32_bf16 a[8:11], v[72:75], v[4:7], a[8:11]\n"
        "v_mfma_f32_16x16x32_bf16 a[40:43], v[72:75], v[12:15], a[40:43]\n"
        "v_mfma_f32_16x16x32_bf16 a[40:43], v[40:43], v[8:11], a[40:43]\n"
        "v_mfma_f32_16x16x32_bf16 a[44:47], v[44:47], v[8:11], a[44:47]\n"
        "global_load_lds_dwordx4 v241, s[60:61] offset:3072\n"
        "s_add_u32 m0, s73, 0x24000\n"
        "v_mfma_f32_16x16x32_bf16 a[44:47], v[76:79], v[12:15], a[44:47]\n"
        "s_add_u32 s60, s60, 0x80000\n"
        "s_addc_u32 s61, s61, 0\n"
        "s_waitcnt vmcnt(25) lgkmcnt(0)\n"
        "v_mfma_f32_16x16x32_bf16 a[12:15], v[76:79], v[4:7], a[12:15]\n"
        "v_lshlrev_b32 v190, 16, v118\n"
        "v_mul_f32 v189, v189, v190\n"
        "v_mfma_f32_16x16x32_bf16 a[12:15], v[44:47], v[0:3], a[12:15]\n"
        "v_mul_f32 v190, v179, v187\n"
        "v_cvt_pk_bf16_f32 v190, 0, v190\n"
        "v_mfma_f32_16x16x32_bf16 a[16:19], v[0:3], v[48:51], a[16:19]\n"
        "global_load_lds_dwordx4 v238, s[62:63] offset:0\n"
        "v_mfma_f32_16x16x32_bf16 a[16:19], v[4:7], v[80:83], a[16:19]\n"
        "v_mfma_f32_16x16x32_bf16 a[48:51], v[12:15], v[80:83], a[48:51]\n"
        "v_mfma_f32_16x16x32_bf16 a[48:51], v[8:11], v[48:51], a[48:51]\n"
        "v_mfma_f32_16x16x32_bf16 a[52:55], v[8:11], v[52:55], a[52:55]\n"
        "global_load_lds_dwordx4 v239, s[62:63] offset:1024\n"
        "v_mfma_f32_16x16x32_bf16 a[52:55], v[12:15], v[84:87], a[52:55]\n"
        "v_mfma_f32_16x16x32_bf16 a[20:23], v[4:7], v[84:87], a[20:23]\n"
        "v_mfma_f32_16x16x32_bf16 a[20:23], v[0:3], v[52:55], a[20:23]\n"
        "v_mfma_f32_16x16x32_bf16 a[24:27], v[0:3], v[56:59], a[24:27]\n"
        "global_load_lds_dwordx4 v240, s[62:63] offset:2048\n"
        "v_mfma_f32_16x16x32_bf16 a[24:27], v[4:7], v[88:91], a[24:27]\n"
        "v_mfma_f32_16x16x32_bf16 a[56:59], v[12:15], v[88:91], a[56:59]\n"
        "v_mfma_f32_16x16x32_bf16 a[56:59], v[8:11], v[56:59], a[56:59]\n"
        "v_mfma_f32_16x16x32_bf16 a[60:63], v[8:11], v[60:63], a[60:63]\n"
        "ds_read_b128 v[8:11], v224 offset:34816\n"
        "global_load_lds_dwordx4 v241, s[62:63] offset:3072\n"
        "s_add_u32 m0, s72, 0x8000\n"
        "v_mfma_f32_16x16x32_bf16 a[60:63], v[12:15], v[92:95], a[60:63]\n"
        "ds_read_b128 v[12:15], v225 offset:34816\n"
        "s_add_u32 s62, s62, 0x80000\n"
        "s_addc_u32 s63, s63, 0\n"
        "v_mfma_f32_16x16x32_bf16 a[28:31], v[4:7], v[92:95], a[28:31]\n"
        "ds_read_b128 v[4:7], v225 offset:32768\n"
        "s_waitcnt vmcnt(24)\n"
        "v_mfma_f32_16x16x32_bf16 a[28:31], v[0:3], v[60:63], a[28:31]\n"
        "ds_read_b128 v[0:3], v224 offset:32768\n"
        "v_lshlrev_b32 v192, 16, v120\n"
        "v_mfma_f32_16x16x32_bf16 a[64:67], v[32:35], v[16:19], a[64:67]\n"
        "global_load_dwordx4 v[142:145], v254, s[92:93] offset:0\n"
        "v_mfma_f32_16x16x32_bf16 a[64:67], v[64:67], v[20:23], a[64:67]\n"
        "v_mul_f32 v191, v191, v192\n"
        "v_cvt_pk_bf16_f32 v203, v191, v189\n"
        "v_mfma_f32_16x16x32_bf16 a[96:99], v[64:67], v[28:31], a[96:99]\n"
        "v_mul_f32 v189, v178, v188\n"
        "v_mul_f32 v191, v179, v188\n"
        "v_mfma_f32_16x16x32_bf16 a[96:99], v[32:35], v[24:27], a[96:99]\n"
        "v_cvt_pk_bf16_f32 v189, 0, v189\n"
        "v_cvt_pk_bf16_f32 v191, 0, v191\n"
        "v_mfma_f32_16x16x32_bf16 a[100:103], v[36:39], v[24:27], a[100:103]\n"
        "v_mul_f32 v192, v180, v187\n"
        "v_cvt_pk_bf16_f32 v192, 0, v192\n"
        "v_mfma_f32_16x16x32_bf16 a[100:103], v[68:71], v[28:31], a[100:103]\n"
        "s_waitcnt vmcnt(23) lgkmcnt(2)\n"
        "v_lshlrev_b32 v194, 16, v122\n"
        "v_mul_f32 v193, v193, v194\n"
        "v_mfma_f32_16x16x32_bf16 a[68:71], v[68:71], v[20:23], a[68:71]\n"
        "LHA1_%=:\n"
        "s_cbranch_execz LHA5_%=\n"
        "v_add_f32 v189, v189, v190\n"
        "v_add_f32 v191, v191, v192\n"
        "v_mfma_f32_16x16x32_bf16 a[68:71], v[36:39], v[16:19], a[68:71]\n"
        "v_cvt_pk_bf16_f32 v189, 0, v189\n"
        "v_cvt_pk_bf16_f32 v191, 0, v191\n"
        "v_mfma_f32_16x16x32_bf16 a[72:75], v[40:43], v[16:19], a[72:75]\n"
        "v_mul_f32 v190, v180, v186\n"
        "v_mul_f32 v192, v181, v186\n"
        "v_mfma_f32_16x16x32_bf16 a[72:75], v[72:75], v[20:23], a[72:75]\n"
        "v_cvt_pk_bf16_f32 v190, 0, v190\n"
        "v_cvt_pk_bf16_f32 v192, 0, v192\n"
        "v_mfma_f32_16x16x32_bf16 a[104:107], v[72:75], v[28:31], a[104:107]\n"
        "v_add_f32 v189, v189, v190\n"
        "v_add_f32 v191, v191, v192\n"
        "v_mfma_f32_16x16x32_bf16 a[104:107], v[40:43], v[24:27], a[104:107]\n"
        "v_cvt_pk_bf16_f32 v189, 0, v189\n"
        "v_cvt_pk_bf16_f32 v191, 0, v191\n"
        "v_mfma_f32_16x16x32_bf16 a[108:111], v[44:47], v[24:27], a[108:111]\n"
        "v_mul_f32 v190, v181, v185\n"
        "v_mul_f32 v192, v182, v185\n"
        "v_mfma_f32_16x16x32_bf16 a[108:111], v[76:79], v[28:31], a[108:111]\n"
        "v_cvt_pk_bf16_f32 v190, 0, v190\n"
        "v_cvt_pk_bf16_f32 v192, 0, v192\n"
        "v_mfma_f32_16x16x32_bf16 a[76:79], v[76:79], v[20:23], a[76:79]\n"
        "v_add_f32 v189, v189, v190\n"
        "v_add_f32 v191, v191, v192\n"
        "v_mfma_f32_16x16x32_bf16 a[76:79], v[44:47], v[16:19], a[76:79]\n"
        "v_cvt_pk_bf16_f32 v189, 0, v189\n"
        "v_cvt_pk_bf16_f32 v191, 0, v191\n"
        "v_mfma_f32_16x16x32_bf16 a[80:83], v[16:19], v[48:51], a[80:83]\n"
        "v_mul_f32 v194, v181, v187\n"
        "v_cvt_pk_bf16_f32 v194, 0, v194\n"
        "v_mfma_f32_16x16x32_bf16 a[80:83], v[20:23], v[80:83], a[80:83]\n"
        "v_and_b32 v178, 0xffff0000, v104\n"
        "v_and_b32 v179, 0xffff0000, v106\n"
        "v_mfma_f32_16x16x32_bf16 a[112:115], v[28:31], v[80:83], a[112:115]\n"
        "v_mfma_f32_16x16x32_bf16 a[112:115], v[24:27], v[48:51], a[112:115]\n"
        "v_mfma_f32_16x16x32_bf16 a[116:119], v[24:27], v[52:55], a[116:119]\n"
        "s_waitcnt vmcnt(22) lgkmcnt(0)\n"
        "v_lshlrev_b32 v196, 16, v124\n"
        "v_mul_f32 v195, v195, v196\n"
        "v_mfma_f32_16x16x32_bf16 a[116:119], v[28:31], v[84:87], a[116:119]\n"
        "v_cvt_pk_bf16_f32 v202, v195, v193\n"
        "v_mul_f32 v193, v180, v188\n"
        "v_mfma_f32_16x16x32_bf16 a[84:87], v[20:23], v[84:87], a[84:87]\n"
        "v_mul_f32 v195, v181, v188\n"
        "v_cvt_pk_bf16_f32 v193, 0, v193\n"
        "v_mfma_f32_16x16x32_bf16 a[84:87], v[16:19], v[52:55], a[84:87]\n"
        "v_cvt_pk_bf16_f32 v195, 0, v195\n"
        "v_mul_f32 v196, v182, v187\n"
        "v_mfma_f32_16x16x32_bf16 a[88:91], v[16:19], v[56:59], a[88:91]\n"
        "v_cvt_pk_bf16_f32 v196, 0, v196\n"
        "v_add_f32 v193, v193, v194\n"
        "v_mfma_f32_16x16x32_bf16 a[88:91], v[20:23], v[88:91], a[88:91]\n"
        "v_add_f32 v195, v195, v196\n"
        "v_cvt_pk_bf16_f32 v193, 0, v193\n"
        "v_mfma_f32_16x16x32_bf16 a[120:123], v[28:31], v[88:91], a[120:123]\n"
        "v_cvt_pk_bf16_f32 v195, 0, v195\n"
        "v_mul_f32 v194, v182, v186\n"
        "v_mfma_f32_16x16x32_bf16 a[120:123], v[24:27], v[56:59], a[120:123]\n"
        "v_mul_f32 v196, v183, v186\n"
        "v_cvt_pk_bf16_f32 v194, 0, v194\n"
        "v_mfma_f32_16x16x32_bf16 a[124:127], v[24:27], v[60:63], a[124:127]\n"
        "ds_read_b128 v[24:27], v224 offset:38912\n"
        "v_cvt_pk_bf16_f32 v196, 0, v196\n"
        "v_mfma_f32_16x16x32_bf16 a[124:127], v[28:31], v[92:95], a[124:127]\n"
        "ds_read_b128 v[28:31], v225 offset:38912\n"
        "v_add_f32 v193, v193, v194\n"
        "v_mfma_f32_16x16x32_bf16 a[92:95], v[20:23], v[92:95], a[92:95]\n"
        "ds_read_b128 v[20:23], v225 offset:36864\n"
        "v_add_f32 v195, v195, v196\n"
        "v_mfma_f32_16x16x32_bf16 a[92:95], v[16:19], v[60:63], a[92:95]\n"
        "ds_read_b128 v[16:19], v224 offset:36864\n"
        "v_cvt_pk_bf16_f32 v193, 0, v193\n"
        "v_mfma_f32_16x16x32_bf16 a[128:131], v[32:35], v[0:3], a[128:131]\n"
        "v_cvt_pk_bf16_f32 v195, 0, v195\n"
        "v_mul_f32 v194, v183, v185\n"
        "v_mfma_f32_16x16x32_bf16 a[128:131], v[64:67], v[4:7], a[128:131]\n"
        "v_mul_f32 v196, v184, v185\n"
        "v_cvt_pk_bf16_f32 v194, 0, v194\n"
        "v_mfma_f32_16x16x32_bf16 a[160:163], v[64:67], v[12:15], a[160:163]\n"
        "v_cvt_pk_bf16_f32 v196, 0, v196\n"
        "v_add_f32 v193, v193, v194\n"
        "v_mfma_f32_16x16x32_bf16 a[160:163], v[32:35], v[8:11], a[160:163]\n"
        "v_add_f32 v195, v195, v196\n"
        "v_cvt_pk_bf16_f32 v193, 0, v193\n"
        "v_mfma_f32_16x16x32_bf16 a[164:167], v[36:39], v[8:11], a[164:167]\n"
        "s_waitcnt vmcnt(21) lgkmcnt(3)\n"
        "v_lshlrev_b32 v190, 16, v126\n"
        "v_mul_f32 v189, v189, v190\n"
        "v_mfma_f32_16x16x32_bf16 a[164:167], v[68:71], v[12:15], a[164:167]\n"
        "v_cvt_pk_bf16_f32 v195, 0, v195\n"
        "v_lshlrev_b32 v185, 16, v136\n"
        "v_mfma_f32_16x16x32_bf16 a[132:135], v[68:71], v[4:7], a[132:135]\n"
        "v_and_b32 v186, 0xffff0000, v136\n"
        "v_lshlrev_b32 v187, 16, v137\n"
        "v_mfma_f32_16x16x32_bf16 a[132:135], v[36:39], v[0:3], a[132:135]\n"
        "global_load_dword v253, v173, s[82:83] offset:0\n"
        "s_add_u32 s82, s12, 0x300000\n"
        "s_addc_u32 s83, s13, 0\n"
        "v_mfma_f32_16x16x32_bf16 a[136:139], v[40:43], v[0:3], a[136:139]\n"
        "global_load_dwordx4 v[146:149], v254, s[92:93] offset:1024\n"
        "v_and_b32 v188, 0xffff0000, v137\n"
        "v_mfma_f32_16x16x32_bf16 a[136:139], v[72:75], v[4:7], a[136:139]\n"
        "v_and_b32 v180, 0xffff0000, v108\n"
        "v_and_b32 v181, 0xffff0000, v110\n"
        "v_mfma_f32_16x16x32_bf16 a[168:171], v[72:75], v[12:15], a[168:171]\n"
        "v_and_b32 v182, 0xffff0000, v112\n"
        "v_and_b32 v183, 0xffff0000, v114\n"
        "v_mfma_f32_16x16x32_bf16 a[168:171], v[40:43], v[8:11], a[168:171]\n"
        "v_and_b32 v184, 0xffff0000, v116\n"
        "v_mul_f32 v190, v175, v187\n"
        "v_mfma_f32_16x16x32_bf16 a[172:175], v[44:47], v[8:11], a[172:175]\n"
        "v_cvt_pk_bf16_f32 v190, 0, v190\n"
        "v_mfma_f32_16x16x32_bf16 a[172:175], v[76:79], v[12:15], a[172:175]\n"
        "v_mfma_f32_16x16x32_bf16 a[140:143], v[76:79], v[4:7], a[140:143]\n"
        "v_mfma_f32_16x16x32_bf16 a[140:143], v[44:47], v[0:3], a[140:143]\n"
        "v_mfma_f32_16x16x32_bf16 a[144:147], v[0:3], v[48:51], a[144:147]\n"
        "v_mfma_f32_16x16x32_bf16 a[144:147], v[4:7], v[80:83], a[144:147]\n"
        "v_mfma_f32_16x16x32_bf16 a[176:179], v[12:15], v[80:83], a[176:179]\n"
        "v_mfma_f32_16x16x32_bf16 a[176:179], v[8:11], v[48:51], a[176:179]\n"
        "v_mfma_f32_16x16x32_bf16 a[180:183], v[8:11], v[52:55], a[180:183]\n"
        "s_waitcnt vmcnt(22) lgkmcnt(0)\n"
        "v_lshlrev_b32 v192, 16, v128\n"
        "v_mul_f32 v191, v191, v192\n"
        "v_mfma_f32_16x16x32_bf16 a[180:183], v[12:15], v[84:87], a[180:183]\n"
        "v_cvt_pk_bf16_f32 v201, v191, v189\n"
        "v_mul_f32 v189, v174, v188\n"
        "v_mfma_f32_16x16x32_bf16 a[148:151], v[4:7], v[84:87], a[148:151]\n"
        "v_mul_f32 v191, v175, v188\n"
        "v_cvt_pk_bf16_f32 v189, 0, v189\n"
        "v_mfma_f32_16x16x32_bf16 a[148:151], v[0:3], v[52:55], a[148:151]\n"
        "v_cvt_pk_bf16_f32 v191, 0, v191\n"
        "v_mul_f32 v192, v176, v187\n"
        "v_mfma_f32_16x16x32_bf16 a[152:155], v[0:3], v[56:59], a[152:155]\n"
        "v_cvt_pk_bf16_f32 v192, 0, v192\n"
        "v_add_f32 v189, v189, v190\n"
        "v_mfma_f32_16x16x32_bf16 a[152:155], v[4:7], v[88:91], a[152:155]\n"
        "v_add_f32 v191, v191, v192\n"
        "v_cvt_pk_bf16_f32 v189, 0, v189\n"
        "v_mfma_f32_16x16x32_bf16 a[184:187], v[12:15], v[88:91], a[184:187]\n"
        "v_cvt_pk_bf16_f32 v191, 0, v191\n"
        "v_mul_f32 v190, v176, v186\n"
        "v_mfma_f32_16x16x32_bf16 a[184:187], v[8:11], v[56:59], a[184:187]\n"
        "s_waitcnt vmcnt(12)\n"
        "v_mul_f32 v192, v177, v186\n"
        "v_cvt_pk_bf16_f32 v190, 0, v190\n"
        "v_mfma_f32_16x16x32_bf16 a[188:191], v[8:11], v[60:63], a[188:191]\n"
        "s_barrier\n"
        "v_mfma_f32_16x16x32_bf16 a[188:191], v[12:15], v[92:95], a[188:191]\n"
        "ds_read_b128 v[8:11], v224 offset:18432\n"
        "global_load_lds_dwordx4 v234, s[56:57] offset:0\n"
        "v_mfma_f32_16x16x32_bf16 a[156:159], v[4:7], v[92:95], a[156:159]\n"
        "ds_read_b128 v[4:7], v225 offset:16384\n"
        "v_cvt_pk_bf16_f32 v192, 0, v192\n"
        "v_mfma_f32_16x16x32_bf16 a[156:159], v[0:3], v[60:63], a[156:159]\n"
        "ds_read_b128 v[0:3], v224 offset:16384\n"
        "v_add_f32 v189, v189, v190\n"
        "v_mfma_f32_16x16x32_bf16 a[192:195], v[32:35], v[16:19], a[192:195]\n"
        "ds_read_b128 v[12:15], v225 offset:18432\n"
        "v_add_f32 v191, v191, v192\n"
        "v_mfma_f32_16x16x32_bf16 a[192:195], v[64:67], v[20:23], a[192:195]\n"
        "global_load_lds_dwordx4 v235, s[56:57] offset:1024\n"
        "v_mfma_f32_16x16x32_bf16 a[224:227], v[64:67], v[28:31], a[224:227]\n"
        "ds_read_b64_tr_b16 v[64:65], v226 offset:40960\n"
        "v_cvt_pk_bf16_f32 v189, 0, v189\n"
        "v_mfma_f32_16x16x32_bf16 a[224:227], v[32:35], v[24:27], a[224:227]\n"
        "ds_read_b64_tr_b16 v[32:33], v226 offset:32768\n"
        "v_cvt_pk_bf16_f32 v191, 0, v191\n"
        "v_mfma_f32_16x16x32_bf16 a[228:231], v[36:39], v[24:27], a[228:231]\n"
        "ds_read_b64_tr_b16 v[34:35], v227 offset:32768\n"
        "v_mul_f32 v190, v177, v185\n"
        "v_mfma_f32_16x16x32_bf16 a[228:231], v[68:71], v[28:31], a[228:231]\n"
        "ds_read_b64_tr_b16 v[66:67], v227 offset:40960\n"
        "global_load_lds_dwordx4 v236, s[56:57] offset:2048\n"
        "v_mfma_f32_16x16x32_bf16 a[196:199], v[68:71], v[20:23], a[196:199]\n"
        "ds_read_b64_tr_b16 v[68:69], v228 offset:40960\n"
        "v_mul_f32 v192, v178, v185\n"
        "v_mfma_f32_16x16x32_bf16 a[196:199], v[36:39], v[16:19], a[196:199]\n"
        "ds_read_b64_tr_b16 v[36:37], v228 offset:32768\n"
        "v_cvt_pk_bf16_f32 v190, 0, v190\n"
        "v_mfma_f32_16x16x32_bf16 a[200:203], v[40:43], v[16:19], a[200:203]\n"
        "ds_read_b64_tr_b16 v[38:39], v229 offset:32768\n"
        "v_cvt_pk_bf16_f32 v192, 0, v192\n"
        "v_mfma_f32_16x16x32_bf16 a[200:203], v[72:75], v[20:23], a[200:203]\n"
        "ds_read_b64_tr_b16 v[70:71], v229 offset:40960\n"
        "global_load_lds_dwordx4 v237, s[56:57] offset:3072\n"
        "s_add_u32 m0, s72, 0x9000\n"
        "v_mfma_f32_16x16x32_bf16 a[232:235], v[72:75], v[28:31], a[232:235]\n"
        "ds_read_b64_tr_b16 v[72:73], v230 offset:40960\n"
        "s_add_u32 s56, s56, 0x80\n"
        "s_addc_u32 s57, s57, 0\n"
        "v_mfma_f32_16x16x32_bf16 a[232:235], v[40:43], v[24:27], a[232:235]\n"
        "ds_read_b64_tr_b16 v[40:41], v230 offset:32768\n"
        "v_lshlrev_b32 v194, 16, v130\n"
        "v_mfma_f32_16x16x32_bf16 a[236:239], v[44:47], v[24:27], a[236:239]\n"
        "ds_read_b64_tr_b16 v[42:43], v231 offset:32768\n"
        "v_mul_f32 v193, v193, v194\n"
        "v_mfma_f32_16x16x32_bf16 a[236:239], v[76:79], v[28:31], a[236:239]\n"
        "ds_read_b64_tr_b16 v[74:75], v231 offset:40960\n"
        "global_load_lds_dwordx4 v234, s[58:59] offset:0\n"
        "v_mfma_f32_16x16x32_bf16 a[204:207], v[76:79], v[20:23], a[204:207]\n"
        "ds_read_b64_tr_b16 v[76:77], v232 offset:40960\n"
        "v_add_f32 v189, v189, v190\n"
        "v_mfma_f32_16x16x32_bf16 a[204:207], v[44:47], v[16:19], a[204:207]\n"
        "ds_read_b64_tr_b16 v[44:45], v232 offset:32768\n"
        "v_add_f32 v191, v191, v192\n"
        "v_mfma_f32_16x16x32_bf16 a[208:211], v[16:19], v[48:51], a[208:211]\n"
        "ds_read_b64_tr_b16 v[46:47], v233 offset:32768\n"
        "v_cvt_pk_bf16_f32 v189, 0, v189\n"
        "v_mfma_f32_16x16x32_bf16 a[208:211], v[20:23], v[80:83], a[208:211]\n"
        "ds_read_b64_tr_b16 v[78:79], v233 offset:40960\n"
        "global_load_lds_dwordx4 v235, s[58:59] offset:1024\n"
        "v_mfma_f32_16x16x32_bf16 a[240:243], v[28:31], v[80:83], a[240:243]\n"
        "ds_read_b64_tr_b16 v[80:81], v226 offset:57344\n"
        "v_cvt_pk_bf16_f32 v191, 0, v191\n"
        "v_mfma_f32_16x16x32_bf16 a[240:243], v[24:27], v[48:51], a[240:243]\n"
        "ds_read_b64_tr_b16 v[48:49], v226 offset:49152\n"
        "v_and_b32 v190, 0xffff0000, v118\n"
        "v_mfma_f32_16x16x32_bf16 a[244:247], v[24:27], v[52:55], a[244:247]\n"
        "ds_read_b64_tr_b16 v[50:51], v227 offset:49152\n"
        "v_and_b32 v192, 0xffff0000, v120\n"
        "v_mfma_f32_16x16x32_bf16 a[244:247], v[28:31], v[84:87], a[244:247]\n"
        "ds_read_b64_tr_b16 v[82:83], v227 offset:57344\n"
        "global_load_lds_dwordx4 v236, s[58:59] offset:2048\n"
        "v_mfma_f32_16x16x32_bf16 a[212:215], v[20:23], v[84:87], a[212:215]\n"
        "ds_read_b64_tr_b16 v[84:85], v228 offset:57344\n"
        "v_mul_f32 v189, v189, v190\n"
        "v_mfma_f32_16x16x32_bf16 a[212:215], v[16:19], v[52:55], a[212:215]\n"
        "ds_read_b64_tr_b16 v[52:53], v228 offset:49152\n"
        "v_mul_f32 v191, v191, v192\n"
        "v_mfma_f32_16x16x32_bf16 a[216:219], v[16:19], v[56:59], a[216:219]\n"
        "ds_read_b64_tr_b16 v[54:55], v229 offset:49152\n"
        "v_cvt_pk_bf16_f32 v211, v191, v189\n"
        "v_mfma_f32_16x16x32_bf16 a[216:219], v[20:23], v[88:91], a[216:219]\n"
        "ds_read_b64_tr_b16 v[86:87], v229 offset:57344\n"
        "global_load_lds_dwordx4 v237, s[58:59] offset:3072\n"
        "s_add_u32 m0, s73, 0x10000\n"
        "v_mfma_f32_16x16x32_bf16 a[248:251], v[28:31], v[88:91], a[248:251]\n"
        "ds_read_b64_tr_b16 v[88:89], v230 offset:57344\n"
        "s_add_u32 s58, s58, 0x80\n"
        "s_addc_u32 s59, s59, 0\n"
        "v_mfma_f32_16x16x32_bf16 a[248:251], v[24:27], v[56:59], a[248:251]\n"
        "ds_read_b64_tr_b16 v[56:57], v230 offset:49152\n"
        "v_mul_f32 v194, v177, v187\n"
        "v_mfma_f32_16x16x32_bf16 a[252:255], v[24:27], v[60:63], a[252:255]\n"
        "ds_read_b64_tr_b16 v[58:59], v231 offset:49152\n"
        "v_cvt_pk_bf16_f32 v194, 0, v194\n"
        "v_mfma_f32_16x16x32_bf16 a[252:255], v[28:31], v[92:95], a[252:255]\n"
        "ds_read_b64_tr_b16 v[90:91], v231 offset:57344\n"
        "global_load_dwordx4 v[150:153], v254, s[92:93] offset:2048\n"
        "v_mfma_f32_16x16x32_bf16 a[220:223], v[20:23], v[92:95], a[220:223]\n"
        "ds_read_b64_tr_b16 v[92:93], v232 offset:57344\n"
        "v_mul_f32 v189, v178, v188\n"
        "v_mfma_f32_16x16x32_bf16 a[220:223], v[16:19], v[60:63], a[220:223]\n"
        "ds_read_b64_tr_b16 v[60:61], v232 offset:49152\n"
        "s_waitcnt lgkmcnt(8)\n"
        "v_mfma_f32_16x16x32_bf16 a[0:3], v[32:35], v[0:3], a[0:3]\n"
        "ds_read_b64_tr_b16 v[62:63], v233 offset:49152\n"
        "v_mul_f32 v191, v179, v188\n"
        "v_mfma_f32_16x16x32_bf16 a[0:3], v[64:67], v[4:7], a[0:3]\n"
        "ds_read_b64_tr_b16 v[94:95], v233 offset:57344\n"
        "global_load_lds_dwordx4 v238, s[60:61] offset:0\n"
        "v_mfma_f32_16x16x32_bf16 a[32:35], v[64:67], v[12:15], a[32:35]\n"
        "ds_read_b128 v[16:19], v224 offset:20480\n"
        "v_cvt_pk_bf16_f32 v189, 0, v189\n"
        "v_mfma_f32_16x16x32_bf16 a[32:35], v[32:35], v[8:11], a[32:35]\n"
        "ds_read_b128 v[20:23], v225 offset:20480\n"
        "v_cvt_pk_bf16_f32 v191, 0, v191\n"
        "v_mfma_f32_16x16x32_bf16 a[36:39], v[36:39], v[8:11], a[36:39]\n"
        "ds_read_b128 v[24:27], v224 offset:22528\n"
        "v_mul_f32 v190, v179, v187\n"
        "v_mfma_f32_16x16x32_bf16 a[36:39], v[68:71], v[12:15], a[36:39]\n"
        "ds_read_b128 v[28:31], v225 offset:22528\n"
        "global_load_lds_dwordx4 v239, s[60:61] offset:1024\n"
        "v_mfma_f32_16x16x32_bf16 a[4:7], v[68:71], v[4:7], a[4:7]\n"
        "s_waitcnt vmcnt(22) lgkmcnt(7)\n"
        "v_lshlrev_b32 v196, 16, v132\n"
        "v_mul_f32 v195, v195, v196\n"
        "v_mfma_f32_16x16x32_bf16 a[4:7], v[36:39], v[0:3], a[4:7]\n"
        "v_cvt_pk_bf16_f32 v200, v195, v193\n"
        "v_mul_f32 v193, v176, v188\n"
        "v_mfma_f32_16x16x32_bf16 a[8:11], v[40:43], v[0:3], a[8:11]\n"
        "v_mul_f32 v195, v177, v188\n"
        "v_cvt_pk_bf16_f32 v193, 0, v193\n"
        "v_mfma_f32_16x16x32_bf16 a[8:11], v[72:75], v[4:7], a[8:11]\n"
        "global_load_lds_dwordx4 v240, s[60:61] offset:2048\n"
        "v_mfma_f32_16x16x32_bf16 a[40:43], v[72:75], v[12:15], a[40:43]\n"
        "v_cvt_pk_bf16_f32 v195, 0, v195\n"
        "v_mul_f32 v196, v178, v187\n"
        "v_mfma_f32_16x16x32_bf16 a[40:43], v[40:43], v[8:11], a[40:43]\n"
        "v_cvt_pk_bf16_f32 v196, 0, v196\n"
        "v_add_f32 v193, v193, v194\n"
        "v_mfma_f32_16x16x32_bf16 a[44:47], v[44:47], v[8:11], a[44:47]\n"
        "v_add_f32 v195, v195, v196\n"
        "v_cvt_pk_bf16_f32 v193, 0, v193\n"
        "v_mfma_f32_16x16x32_bf16 a[44:47], v[76:79], v[12:15], a[44:47]\n"
        "global_load_lds_dwordx4 v241, s[60:61] offset:3072\n"
        "s_add_u32 m0, s73, 0x14000\n"
        "v_mfma_f32_16x16x32_bf16 a[12:15], v[76:79], v[4:7], a[12:15]\n"
        "s_add_u32 s60, s60, 0x80000\n"
        "s_addc_u32 s61, s61, 0\n"
        "v_cvt_pk_bf16_f32 v195, 0, v195\n"
        "v_mfma_f32_16x16x32_bf16 a[12:15], v[44:47], v[0:3], a[12:15]\n"
        "v_mul_f32 v194, v178, v186\n"
        "v_mul_f32 v196, v179, v186\n"
        "v_mfma_f32_16x16x32_bf16 a[16:19], v[0:3], v[48:51], a[16:19]\n"
        "v_cvt_pk_bf16_f32 v194, 0, v194\n"
        "v_cvt_pk_bf16_f32 v196, 0, v196\n"
        "v_mfma_f32_16x16x32_bf16 a[16:19], v[4:7], v[80:83], a[16:19]\n"
        "global_load_lds_dwordx4 v238, s[62:63] offset:0\n"
        "v_mfma_f32_16x16x32_bf16 a[48:51], v[12:15], v[80:83], a[48:51]\n"
        "v_add_f32 v193, v193, v194\n"
        "v_add_f32 v195, v195, v196\n"
        "v_mfma_f32_16x16x32_bf16 a[48:51], v[8:11], v[48:51], a[48:51]\n"
        "v_cvt_pk_bf16_f32 v193, 0, v193\n"
        "v_cvt_pk_bf16_f32 v195, 0, v195\n"
        "v_mfma_f32_16x16x32_bf16 a[52:55], v[8:11], v[52:55], a[52:55]\n"
        "v_mul_f32 v194, v179, v185\n"
        "v_mul_f32 v196, v180, v185\n"
        "v_mfma_f32_16x16x32_bf16 a[52:55], v[12:15], v[84:87], a[52:55]\n"
        "global_load_lds_dwordx4 v239, s[62:63] offset:1024\n"
        "v_mfma_f32_16x16x32_bf16 a[20:23], v[4:7], v[84:87], a[20:23]\n"
        "v_cvt_pk_bf16_f32 v194, 0, v194\n"
        "v_cvt_pk_bf16_f32 v196, 0, v196\n"
        "v_mfma_f32_16x16x32_bf16 a[20:23], v[0:3], v[52:55], a[20:23]\n"
        "v_add_f32 v193, v193, v194\n"
        "v_add_f32 v195, v195, v196\n"
        "v_mfma_f32_16x16x32_bf16 a[24:27], v[0:3], v[56:59], a[24:27]\n"
        "s_waitcnt lgkmcnt(0)\n"
        "v_cvt_pk_bf16_f32 v193, 0, v193\n"
        "v_cvt_pk_bf16_f32 v195, 0, v195\n"
        "v_mfma_f32_16x16x32_bf16 a[24:27], v[4:7], v[88:91], a[24:27]\n"
        "global_load_dword v253, v173, s[82:83] offset:0\n"
        "v_mfma_f32_16x16x32_bf16 a[56:59], v[12:15], v[88:91], a[56:59]\n"
        "global_load_lds_dwordx4 v240, s[62:63] offset:2048\n"
        "s_add_u32 s82, s12, 0x400000\n"
        "s_addc_u32 s83, s13, 0\n"
        "v_mfma_f32_16x16x32_bf16 a[56:59], v[8:11], v[56:59], a[56:59]\n"
        "v_and_b32 v194, 0xffff0000, v122\n"
        "v_and_b32 v196, 0xffff0000, v124\n"
        "v_mfma_f32_16x16x32_bf16 a[60:63], v[8:11], v[60:63], a[60:63]\n"
        "ds_read_b128 v[8:11], v224 offset:2048\n"
        "v_mul_f32 v193, v193, v194\n"
        "v_mfma_f32_16x16x32_bf16 a[60:63], v[12:15], v[92:95], a[60:63]\n"
        "ds_read_b128 v[12:15], v225 offset:2048\n"
        "v_mul_f32 v195, v195, v196\n"
        "v_mfma_f32_16x16x32_bf16 a[28:31], v[4:7], v[92:95], a[28:31]\n"
        "ds_read_b128 v[4:7], v225 offset:0\n"
        "global_load_lds_dwordx4 v241, s[62:63] offset:3072\n"
        "s_add_u32 m0, s72, 0x0\n"
        "v_mfma_f32_16x16x32_bf16 a[28:31], v[0:3], v[60:63], a[28:31]\n"
        "ds_read_b128 v[0:3], v224 offset:0\n"
        "s_add_u32 s62, s62, 0x80000\n"
        "s_addc_u32 s63, s63, 0\n"
        "v_mfma_f32_16x16x32_bf16 a[64:67], v[32:35], v[16:19], a[64:67]\n"
        "v_cvt_pk_bf16_f32 v210, v195, v193\n"
        "v_mul_f32 v192, v180, v187\n"
        "v_mfma_f32_16x16x32_bf16 a[64:67], v[64:67], v[20:23], a[64:67]\n"
        "v_cvt_pk_bf16_f32 v190, 0, v190\n"
        "v_cvt_pk_bf16_f32 v192, 0, v192\n"
        "v_mfma_f32_16x16x32_bf16 a[96:99], v[64:67], v[28:31], a[96:99]\n"
        "v_add_f32 v189, v189, v190\n"
        "v_add_f32 v191, v191, v192\n"
        "v_mfma_f32_16x16x32_bf16 a[96:99], v[32:35], v[24:27], a[96:99]\n"
        "global_store_dwordx4 v171, v[200:203], s[76:77] offset:-4096 sc1\n"
        "v_cvt_pk_bf16_f32 v189, 0, v189\n"
        "v_cvt_pk_bf16_f32 v191, 0, v191\n"
        "v_mul_f32 v190, v180, v186\n"
        "v_mul_f32 v192, v181, v186\n"
        "v_mfma_f32_16x16x32_bf16 a[100:103], v[36:39], v[24:27], a[100:103]\n"
        "v_cvt_pk_bf16_f32 v190, 0, v190\n"
        "v_cvt_pk_bf16_f32 v192, 0, v192\n"
        "v_mfma_f32_16x16x32_bf16 a[100:103], v[68:71], v[28:31], a[100:103]\n"
        "v_add_f32 v189, v189, v190\n"
        "v_add_f32 v191, v191, v192\n"
        "v_mfma_f32_16x16x32_bf16 a[68:71], v[68:71], v[20:23], a[68:71]\n"
        "v_cvt_pk_bf16_f32 v189, 0, v189\n"
        "v_cvt_pk_bf16_f32 v191, 0, v191\n"
        "v_mfma_f32_16x16x32_bf16 a[68:71], v[36:39], v[16:19], a[68:71]\n"
        "global_load_dwordx4 v[154:157], v254, s[92:93] offset:3072\n"
        "v_mul_f32 v190, v181, v185\n"
        "v_mfma_f32_16x16x32_bf16 a[72:75], v[40:43], v[16:19], a[72:75]\n"
        "v_mul_f32 v192, v182, v185\n"
        "v_cvt_pk_bf16_f32 v190, 0, v190\n"
        "v_mfma_f32_16x16x32_bf16 a[72:75], v[72:75], v[20:23], a[72:75]\n"
        "v_cvt_pk_bf16_f32 v192, 0, v192\n"
        "v_add_f32 v189, v189, v190\n"
        "v_mfma_f32_16x16x32_bf16 a[104:107], v[72:75], v[28:31], a[104:107]\n"
        "v_add_f32 v191, v191, v192\n"
        "v_cvt_pk_bf16_f32 v189, 0, v189\n"
        "v_mfma_f32_16x16x32_bf16 a[104:107], v[40:43], v[24:27], a[104:107]\n"
        "v_cvt_pk_bf16_f32 v191, 0, v191\n"
        "v_and_b32 v190, 0xffff0000, v126\n"
        "v_mfma_f32_16x16x32_bf16 a[108:111], v[44:47], v[24:27], a[108:111]\n"
        "v_and_b32 v192, 0xffff0000, v128\n"
        "v_mul_f32 v189, v189, v190\n"
        "v_mfma_f32_16x16x32_bf16 a[108:111], v[76:79], v[28:31], a[108:111]\n"
        "v_mul_f32 v191, v191, v192\n"
        "v_cvt_pk_bf16_f32 v209, v191, v189\n"
        "v_mfma_f32_16x16x32_bf16 a[76:79], v[76:79], v[20:23], a[76:79]\n"
        "v_mul_f32 v193, v180, v188\n"
        "v_mul_f32 v195, v181, v188\n"
        "v_mfma_f32_16x16x32_bf16 a[76:79], v[44:47], v[16:19], a[76:79]\n"
        "v_cvt_pk_bf16_f32 v193, 0, v193\n"
        "v_cvt_pk_bf16_f32 v195, 0, v195\n"
        "v_mfma_f32_16x16x32_bf16 a[80:83], v[16:19], v[48:51], a[80:83]\n"
        "v_mul_f32 v194, v181, v187\n"
        "v_mul_f32 v196, v182, v187\n"
        "v_mfma_f32_16x16x32_bf16 a[80:83], v[20:23], v[80:83], a[80:83]\n"
        "v_cvt_pk_bf16_f32 v194, 0, v194\n"
        "v_cvt_pk_bf16_f32 v196, 0, v196\n"
        "v_mfma_f32_16x16x32_bf16 a[112:115], v[28:31], v[80:83], a[112:115]\n"
        "v_add_f32 v193, v193, v194\n"
        "v_add_f32 v195, v195, v196\n"
        "v_mfma_f32_16x16x32_bf16 a[112:115], v[24:27], v[48:51], a[112:115]\n"
        "v_cvt_pk_bf16_f32 v193, 0, v193\n"
        "v_cvt_pk_bf16_f32 v195, 0, v195\n"
        "v_mfma_f32_16x16x32_bf16 a[116:119], v[24:27], v[52:55], a[116:119]\n"
        "v_mul_f32 v194, v182, v186\n"
        "v_mul_f32 v196, v183, v186\n"
        "v_mfma_f32_16x16x32_bf16 a[116:119], v[28:31], v[84:87], a[116:119]\n"
        "v_cvt_pk_bf16_f32 v194, 0, v194\n"
        "v_cvt_pk_bf16_f32 v196, 0, v196\n"
        "v_mfma_f32_16x16x32_bf16 a[84:87], v[20:23], v[84:87], a[84:87]\n"
        "v_add_f32 v193, v193, v194\n"
        "v_add_f32 v195, v195, v196\n"
        "v_mfma_f32_16x16x32_bf16 a[84:87], v[16:19], v[52:55], a[84:87]\n"
        "v_cvt_pk_bf16_f32 v193, 0, v193\n"
        "v_cvt_pk_bf16_f32 v195, 0, v195\n"
        "v_mfma_f32_16x16x32_bf16 a[88:91], v[16:19], v[56:59], a[88:91]\n"
        "v_mul_f32 v194, v183, v185\n"
        "v_mul_f32 v196, v184, v185\n"
        "v_mfma_f32_16x16x32_bf16 a[88:91], v[20:23], v[88:91], a[88:91]\n"
        "v_cvt_pk_bf16_f32 v194, 0, v194\n"
        "v_cvt_pk_bf16_f32 v196, 0, v196\n"
        "v_mfma_f32_16x16x32_bf16 a[120:123], v[28:31], v[88:91], a[120:123]\n"
        "v_add_f32 v193, v193, v194\n"
        "v_add_f32 v195, v195, v196\n"
        "v_mfma_f32_16x16x32_bf16 a[120:123], v[24:27], v[56:59], a[120:123]\n"
        "v_cvt_pk_bf16_f32 v193, 0, v193\n"
        "v_cvt_pk_bf16_f32 v195, 0, v195\n"
        "v_mfma_f32_16x16x32_bf16 a[124:127], v[24:27], v[60:63], a[124:127]\n"
        "ds_read_b128 v[24:27], v224 offset:6144\n"
        "v_and_b32 v194, 0xffff0000, v130\n"
        "v_mfma_f32_16x16x32_bf16 a[124:127], v[28:31], v[92:95], a[124:127]\n"
        "ds_read_b128 v[28:31], v225 offset:6144\n"
        "v_and_b32 v196, 0xffff0000, v132\n"
        "v_mfma_f32_16x16x32_bf16 a[92:95], v[20:23], v[92:95], a[92:95]\n"
        "ds_read_b128 v[20:23], v225 offset:4096\n"
        "v_mul_f32 v193, v193, v194\n"
        "v_mfma_f32_16x16x32_bf16 a[92:95], v[16:19], v[60:63], a[92:95]\n"
        "ds_read_b128 v[16:19], v224 offset:4096\n"
        "s_waitcnt lgkmcnt(4)\n"
        "v_mfma_f32_16x16x32_bf16 a[128:131], v[32:35], v[0:3], a[128:131]\n"
        "v_mul_f32 v195, v195, v196\n"
        "v_cvt_pk_bf16_f32 v208, v195, v193\n"
        "v_mfma_f32_16x16x32_bf16 a[128:131], v[64:67], v[4:7], a[128:131]\n"
        "global_store_dwordx4 v171, v[208:211], s[76:77] offset:0 sc1\n"
        "s_add_u32 s76, s76, 0x2000\n"
        "s_addc_u32 s77, s77, 0\n"
        "v_lshlrev_b32 v185, 16, v138\n"
        "v_and_b32 v186, 0xffff0000, v138\n"
        "v_lshlrev_b32 v187, 16, v139\n"
        "v_mfma_f32_16x16x32_bf16 a[160:163], v[64:67], v[12:15], a[160:163]\n"
        "v_and_b32 v188, 0xffff0000, v139\n"
        "v_lshlrev_b32 v174, 16, v97\n"
        "v_mfma_f32_16x16x32_bf16 a[160:163], v[32:35], v[8:11], a[160:163]\n"
        "v_lshlrev_b32 v175, 16, v99\n"
        "v_lshlrev_b32 v176, 16, v101\n"
        "v_mfma_f32_16x16x32_bf16 a[164:167], v[36:39], v[8:11], a[164:167]\n"
        "v_lshlrev_b32 v177, 16, v103\n"
        "v_lshlrev_b32 v178, 16, v105\n"
        "v_mfma_f32_16x16x32_bf16 a[164:167], v[68:71], v[12:15], a[164:167]\n"
        "v_lshlrev_b32 v179, 16, v107\n"
        "v_lshlrev_b32 v180, 16, v109\n"
        "v_mfma_f32_16x16x32_bf16 a[132:135], v[68:71], v[4:7], a[132:135]\n"
        "v_lshlrev_b32 v181, 16, v111\n"
        "v_lshlrev_b32 v182, 16, v113\n"
        "v_mfma_f32_16x16x32_bf16 a[132:135], v[36:39], v[0:3], a[132:135]\n"
        "v_lshlrev_b32 v183, 16, v115\n"
        "v_lshlrev_b32 v184, 16, v117\n"
        "v_mfma_f32_16x16x32_bf16 a[136:139], v[40:43], v[0:3], a[136:139]\n"
        "v_mul_f32 v189, v174, v188\n"
        "v_mul_f32 v191, v175, v188\n"
        "v_mfma_f32_16x16x32_bf16 a[136:139], v[72:75], v[4:7], a[136:139]\n"
        "v_cvt_pk_bf16_f32 v189, 0, v189\n"
        "v_cvt_pk_bf16_f32 v191, 0, v191\n"
        "v_mfma_f32_16x16x32_bf16 a[168:171], v[72:75], v[12:15], a[168:171]\n"
        "v_mul_f32 v190, v175, v187\n"
        "v_mul_f32 v192, v176, v187\n"
        "v_mfma_f32_16x16x32_bf16 a[168:171], v[40:43], v[8:11], a[168:171]\n"
        "v_cvt_pk_bf16_f32 v190, 0, v190\n"
        "v_cvt_pk_bf16_f32 v192, 0, v192\n"
        "v_mfma_f32_16x16x32_bf16 a[172:175], v[44:47], v[8:11], a[172:175]\n"
        "v_add_f32 v189, v189, v190\n"
        "v_add_f32 v191, v191, v192\n"
        "v_mfma_f32_16x16x32_bf16 a[172:175], v[76:79], v[12:15], a[172:175]\n"
        "v_cvt_pk_bf16_f32 v189, 0, v189\n"
        "v_cvt_pk_bf16_f32 v191, 0, v191\n"
        "v_mfma_f32_16x16x32_bf16 a[140:143], v[76:79], v[4:7], a[140:143]\n"
        "v_mul_f32 v190, v176, v186\n"
        "v_mul_f32 v192, v177, v186\n"
        "v_mfma_f32_16x16x32_bf16 a[140:143], v[44:47], v[0:3], a[140:143]\n"
        "v_cvt_pk_bf16_f32 v190, 0, v190\n"
        "v_cvt_pk_bf16_f32 v192, 0, v192\n"
        "v_mfma_f32_16x16x32_bf16 a[144:147], v[0:3], v[48:51], a[144:147]\n"
        "v_add_f32 v189, v189, v190\n"
        "v_add_f32 v191, v191, v192\n"
        "v_mfma_f32_16x16x32_bf16 a[144:147], v[4:7], v[80:83], a[144:147]\n"
        "v_cvt_pk_bf16_f32 v189, 0, v189\n"
        "v_cvt_pk_bf16_f32 v191, 0, v191\n"
        "v_mfma_f32_16x16x32_bf16 a[176:179], v[12:15], v[80:83], a[176:179]\n"
        "v_mul_f32 v190, v177, v185\n"
        "v_mul_f32 v192, v178, v185\n"
        "v_mfma_f32_16x16x32_bf16 a[176:179], v[8:11], v[48:51], a[176:179]\n"
        "v_cvt_pk_bf16_f32 v190, 0, v190\n"
        "v_cvt_pk_bf16_f32 v192, 0, v192\n"
        "v_mfma_f32_16x16x32_bf16 a[180:183], v[8:11], v[52:55], a[180:183]\n"
        "v_add_f32 v189, v189, v190\n"
        "v_add_f32 v191, v191, v192\n"
        "v_mfma_f32_16x16x32_bf16 a[180:183], v[12:15], v[84:87], a[180:183]\n"
        "v_cvt_pk_bf16_f32 v189, 0, v189\n"
        "v_cvt_pk_bf16_f32 v191, 0, v191\n"
        "v_mfma_f32_16x16x32_bf16 a[148:151], v[4:7], v[84:87], a[148:151]\n"
        "v_lshlrev_b32 v190, 16, v119\n"
        "v_lshlrev_b32 v192, 16, v121\n"
        "v_mfma_f32_16x16x32_bf16 a[148:151], v[0:3], v[52:55], a[148:151]\n"
        "global_load_dwordx4 v[158:161], v254, s[30:31] offset:0\n"
        "v_mul_f32 v189, v189, v190\n"
        "v_mfma_f32_16x16x32_bf16 a[152:155], v[0:3], v[56:59], a[152:155]\n"
        "v_mul_f32 v191, v191, v192\n"
        "v_cvt_pk_bf16_f32 v203, v191, v189\n"
        "v_mfma_f32_16x16x32_bf16 a[152:155], v[4:7], v[88:91], a[152:155]\n"
        "v_mul_f32 v193, v176, v188\n"
        "v_mul_f32 v195, v177, v188\n"
        "v_mfma_f32_16x16x32_bf16 a[184:187], v[12:15], v[88:91], a[184:187]\n"
        "v_cvt_pk_bf16_f32 v193, 0, v193\n"
        "v_cvt_pk_bf16_f32 v195, 0, v195\n"
        "v_mfma_f32_16x16x32_bf16 a[184:187], v[8:11], v[56:59], a[184:187]\n"
        "s_waitcnt vmcnt(14) lgkmcnt(0)\n"
        "v_mul_f32 v194, v177, v187\n"
        "v_mul_f32 v196, v178, v187\n"
        "v_mfma_f32_16x16x32_bf16 a[188:191], v[8:11], v[60:63], a[188:191]\n"
        "s_barrier\n"
        "v_mfma_f32_16x16x32_bf16 a[188:191], v[12:15], v[92:95], a[188:191]\n"
        "ds_read_b128 v[8:11], v224 offset:51200\n"
        "global_load_lds_dwordx4 v234, s[56:57] offset:0\n"
        "v_mfma_f32_16x16x32_bf16 a[156:159], v[4:7], v[92:95], a[156:159]\n"
        "ds_read_b128 v[4:7], v225 offset:49152\n"
        "v_cvt_pk_bf16_f32 v194, 0, v194\n"
        "v_mfma_f32_16x16x32_bf16 a[156:159], v[0:3], v[60:63], a[156:159]\n"
        "ds_read_b128 v[0:3], v224 offset:49152\n"
        "v_cvt_pk_bf16_f32 v196, 0, v196\n"
        "v_mfma_f32_16x16x32_bf16 a[192:195], v[32:35], v[16:19], a[192:195]\n"
        "ds_read_b128 v[12:15], v225 offset:51200\n"
        "v_add_f32 v193, v193, v194\n"
        "v_mfma_f32_16x16x32_bf16 a[192:195], v[64:67], v[20:23], a[192:195]\n"
        "global_load_lds_dwordx4 v235, s[56:57] offset:1024\n"
        "v_mfma_f32_16x16x32_bf16 a[224:227], v[64:67], v[28:31], a[224:227]\n"
        "ds_read_b64_tr_b16 v[64:65], v216 offset:8192\n"
        "v_add_f32 v195, v195, v196\n"
        "v_mfma_f32_16x16x32_bf16 a[224:227], v[32:35], v[24:27], a[224:227]\n"
        "ds_read_b64_tr_b16 v[32:33], v216 offset:0\n"
        "v_cvt_pk_bf16_f32 v193, 0, v193\n"
        "v_mfma_f32_16x16x32_bf16 a[228:231], v[36:39], v[24:27], a[228:231]\n"
        "ds_read_b64_tr_b16 v[34:35], v217 offset:0\n"
        "v_cvt_pk_bf16_f32 v195, 0, v195\n"
        "v_mfma_f32_16x16x32_bf16 a[228:231], v[68:71], v[28:31], a[228:231]\n"
        "ds_read_b64_tr_b16 v[66:67], v217 offset:8192\n"
        "global_load_lds_dwordx4 v236, s[56:57] offset:2048\n"
        "v_mfma_f32_16x16x32_bf16 a[196:199], v[68:71], v[20:23], a[196:199]\n"
        "ds_read_b64_tr_b16 v[68:69], v218 offset:8192\n"
        "v_mul_f32 v194, v178, v186\n"
        "v_mfma_f32_16x16x32_bf16 a[196:199], v[36:39], v[16:19], a[196:199]\n"
        "ds_read_b64_tr_b16 v[36:37], v218 offset:0\n"
        "v_mul_f32 v196, v179, v186\n"
        "v_mfma_f32_16x16x32_bf16 a[200:203], v[40:43], v[16:19], a[200:203]\n"
        "ds_read_b64_tr_b16 v[38:39], v219 offset:0\n"
        "v_cvt_pk_bf16_f32 v194, 0, v194\n"
        "v_mfma_f32_16x16x32_bf16 a[200:203], v[72:75], v[20:23], a[200:203]\n"
        "ds_read_b64_tr_b16 v[70:71], v219 offset:8192\n"
        "global_load_dword v253, v173, s[82:83] offset:0\n"
        "v_mfma_f32_16x16x32_bf16 a[232:235], v[72:75], v[28:31], a[232:235]\n"
        "ds_read_b64_tr_b16 v[72:73], v220 offset:8192\n"
        "global_load_lds_dwordx4 v237, s[56:57] offset:3072\n"
        "s_add_u32 m0, s72, 0x1000\n"
        "v_mfma_f32_16x16x32_bf16 a[232:235], v[40:43], v[24:27], a[232:235]\n"
        "ds_read_b64_tr_b16 v[40:41], v220 offset:0\n"
        "s_add_u32 s56, s56, 0x80\n"
        "LHA2_%=:\n"
        "s_cbranch_execz LHA6_%=\n"
        "s_addc_u32 s57, s57, 0\n"
        "v_mfma_f32_16x16x32_bf16 a[236:239], v[44:47], v[24:27], a[236:239]\n"
        "ds_read_b64_tr_b16 v[42:43], v221 offset:0\n"
        "s_add_u32 s82, s12, 0x500000\n"
        "s_addc_u32 s83, s13, 0\n"
        "v_mfma_f32_16x16x32_bf16 a[236:239], v[76:79], v[28:31], a[236:239]\n"
        "ds_read_b64_tr_b16 v[74:75], v221 offset:8192\n"
        "v_cvt_pk_bf16_f32 v196, 0, v196\n"
        "v_mfma_f32_16x16x32_bf16 a[204:207], v[76:79], v[20:23], a[204:207]\n"
        "ds_read_b64_tr_b16 v[76:77], v222 offset:8192\n"
        "global_load_lds_dwordx4 v234, s[58:59] offset:0\n"
        "v_mfma_f32_16x16x32_bf16 a[204:207], v[44:47], v[16:19], a[204:207]\n"
        "ds_read_b64_tr_b16 v[44:45], v222 offset:0\n"
        "v_add_f32 v193, v193, v194\n"
        "v_mfma_f32_16x16x32_bf16 a[208:211], v[16:19], v[48:51], a[208:211]\n"
        "ds_read_b64_tr_b16 v[46:47], v223 offset:0\n"
        "v_add_f32 v195, v195, v196\n"
        "v_mfma_f32_16x16x32_bf16 a[208:211], v[20:23], v[80:83], a[208:211]\n"
        "ds_read_b64_tr_b16 v[78:79], v223 offset:8192\n"
        "v_cvt_pk_bf16_f32 v193, 0, v193\n"
        "v_mfma_f32_16x16x32_bf16 a[240:243], v[28:31], v[80:83], a[240:243]\n"
        "ds_read_b64_tr_b16 v[80:81], v216 offset:24576\n"
        "global_load_lds_dwordx4 v235, s[58:59] offset:1024\n"
        "v_mfma_f32_16x16x32_bf16 a[240:243], v[24:27], v[48:51], a[240:243]\n"
        "ds_read_b64_tr_b16 v[48:49], v216 offset:16384\n"
        "v_cvt_pk_bf16_f32 v195, 0, v195\n"
        "v_mfma_f32_16x16x32_bf16 a[244:247], v[24:27], v[52:55], a[244:247]\n"
        "ds_read_b64_tr_b16 v[50:51], v217 offset:16384\n"
        "v_mul_f32 v194, v179, v185\n"
        "v_mfma_f32_16x16x32_bf16 a[244:247], v[28:31], v[84:87], a[244:247]\n"
        "ds_read_b64_tr_b16 v[82:83], v217 offset:24576\n"
        "v_mul_f32 v196, v180, v185\n"
        "v_mfma_f32_16x16x32_bf16 a[212:215], v[20:23], v[84:87], a[212:215]\n"
        "ds_read_b64_tr_b16 v[84:85], v218 offset:24576\n"
        "global_load_lds_dwordx4 v236, s[58:59] offset:2048\n"
        "v_mfma_f32_16x16x32_bf16 a[212:215], v[16:19], v[52:55], a[212:215]\n"
        "ds_read_b64_tr_b16 v[52:53], v218 offset:16384\n"
        "v_cvt_pk_bf16_f32 v194, 0, v194\n"
        "v_mfma_f32_16x16x32_bf16 a[216:219], v[16:19], v[56:59], a[216:219]\n"
        "ds_read_b64_tr_b16 v[54:55], v219 offset:16384\n"
        "v_cvt_pk_bf16_f32 v196, 0, v196\n"
        "v_mfma_f32_16x16x32_bf16 a[216:219], v[20:23], v[88:91], a[216:219]\n"
        "ds_read_b64_tr_b16 v[86:87], v219 offset:24576\n"
        "v_add_f32 v193, v193, v194\n"
        "v_mfma_f32_16x16x32_bf16 a[248:251], v[28:31], v[88:91], a[248:251]\n"
        "ds_read_b64_tr_b16 v[88:89], v220 offset:24576\n"
        "global_load_lds_dwordx4 v237, s[58:59] offset:3072\n"
        "s_add_u32 m0, s73, 0x18000\n"
        "v_mfma_f32_16x16x32_bf16 a[248:251], v[24:27], v[56:59], a[248:251]\n"
        "ds_read_b64_tr_b16 v[56:57], v220 offset:16384\n"
        "s_add_u32 s58, s58, 0x80\n"
        "s_addc_u32 s59, s59, 0\n"
        "v_mfma_f32_16x16x32_bf16 a[252:255], v[24:27], v[60:63], a[252:255]\n"
        "ds_read_b64_tr_b16 v[58:59], v221 offset:16384\n"
        "v_add_f32 v195, v195, v196\n"
        "v_mfma_f32_16x16x32_bf16 a[252:255], v[28:31], v[92:95], a[252:255]\n"
        "ds_read_b64_tr_b16 v[90:91], v221 offset:24576\n"
        "v_cvt_pk_bf16_f32 v193, 0, v193\n"
        "v_mfma_f32_16x16x32_bf16 a[220:223], v[20:23], v[92:95], a[220:223]\n"
        "ds_read_b64_tr_b16 v[92:93], v222 offset:24576\n"
        "global_load_lds_dwordx4 v238, s[60:61] offset:0\n"
        "v_mfma_f32_16x16x32_bf16 a[220:223], v[16:19], v[60:63], a[220:223]\n"
        "ds_read_b64_tr_b16 v[60:61], v222 offset:16384\n"
        "s_waitcnt lgkmcnt(8)\n"
        "v_mfma_f32_16x16x32_bf16 a[0:3], v[32:35], v[0:3], a[0:3]\n"
        "ds_read_b64_tr_b16 v[62:63], v223 offset:16384\n"
        "v_cvt_pk_bf16_f32 v195, 0, v195\n"
        "v_mfma_f32_16x16x32_bf16 a[0:3], v[64:67], v[4:7], a[0:3]\n"
        "ds_read_b64_tr_b16 v[94:95], v223 offset:24576\n"
        "v_lshlrev_b32 v194, 16, v123\n"
        "v_mfma_f32_16x16x32_bf16 a[32:35], v[64:67], v[12:15], a[32:35]\n"
        "ds_read_b128 v[16:19], v224 offset:53248\n"
        "global_load_lds_dwordx4 v239, s[60:61] offset:1024\n"
        "v_mfma_f32_16x16x32_bf16 a[32:35], v[32:35], v[8:11], a[32:35]\n"
        "ds_read_b128 v[20:23], v225 offset:53248\n"
        "v_lshlrev_b32 v196, 16, v125\n"
        "v_mfma_f32_16x16x32_bf16 a[36:39], v[36:39], v[8:11], a[36:39]\n"
        "ds_read_b128 v[24:27], v224 offset:55296\n"
        "v_mul_f32 v193, v193, v194\n"
        "v_mfma_f32_16x16x32_bf16 a[36:39], v[68:71], v[12:15], a[36:39]\n"
        "ds_read_b128 v[28:31], v225 offset:55296\n"
        "v_mul_f32 v195, v195, v196\n"
        "v_mfma_f32_16x16x32_bf16 a[4:7], v[68:71], v[4:7], a[4:7]\n"
        "global_load_lds_dwordx4 v240, s[60:61] offset:2048\n"
        "v_mfma_f32_16x16x32_bf16 a[4:7], v[36:39], v[0:3], a[4:7]\n"
        "v_cvt_pk_bf16_f32 v202, v195, v193\n"
        "v_mul_f32 v189, v178, v188\n"
        "v_mfma_f32_16x16x32_bf16 a[8:11], v[40:43], v[0:3], a[8:11]\n"
        "v_mul_f32 v191, v179, v188\n"
        "v_cvt_pk_bf16_f32 v189, 0, v189\n"
        "v_mfma_f32_16x16x32_bf16 a[8:11], v[72:75], v[4:7], a[8:11]\n"
        "v_cvt_pk_bf16_f32 v191, 0, v191\n"
        "v_mul_f32 v190, v179, v187\n"
        "v_mfma_f32_16x16x32_bf16 a[40:43], v[72:75], v[12:15], a[40:43]\n"
        "global_load_lds_dwordx4 v241, s[60:61] offset:3072\n"
        "s_add_u32 m0, s73, 0x1c000\n"
        "v_mfma_f32_16x16x32_bf16 a[40:43], v[40:43], v[8:11], a[40:43]\n"
        "v_mul_f32 v192, v180, v187\n"
        "v_cvt_pk_bf16_f32 v190, 0, v190\n"
        "v_mfma_f32_16x16x32_bf16 a[44:47], v[44:47], v[8:11], a[44:47]\n"
        "v_cvt_pk_bf16_f32 v192, 0, v192\n"
        "v_add_f32 v189, v189, v190\n"
        "v_mfma_f32_16x16x32_bf16 a[44:47], v[76:79], v[12:15], a[44:47]\n"
        "v_add_f32 v191, v191, v192\n"
        "v_cvt_pk_bf16_f32 v189, 0, v189\n"
        "v_mfma_f32_16x16x32_bf16 a[12:15], v[76:79], v[4:7], a[12:15]\n"
        "global_load_dwordx4 v[162:165], v254, s[30:31] offset:1024\n"
        "v_mfma_f32_16x16x32_bf16 a[12:15], v[44:47], v[0:3], a[12:15]\n"
        "v_cvt_pk_bf16_f32 v191, 0, v191\n"
        "v_mul_f32 v190, v180, v186\n"
        "v_mfma_f32_16x16x32_bf16 a[16:19], v[0:3], v[48:51], a[16:19]\n"
        "s_waitcnt lgkmcnt(0)\n"
        "v_mul_f32 v192, v181, v186\n"
        "v_cvt_pk_bf16_f32 v190, 0, v190\n"
        "v_mfma_f32_16x16x32_bf16 a[16:19], v[4:7], v[80:83], a[16:19]\n"
        "v_cvt_pk_bf16_f32 v192, 0, v192\n"
        "v_add_f32 v189, v189, v190\n"
        "v_mfma_f32_16x16x32_bf16 a[48:51], v[12:15], v[80:83], a[48:51]\n"
        "global_load_lds_dwordx4 v238, s[62:63] offset:0\n"
        "v_mfma_f32_16x16x32_bf16 a[48:51], v[8:11], v[48:51], a[48:51]\n"
        "v_add_f32 v191, v191, v192\n"
        "v_cvt_pk_bf16_f32 v189, 0, v189\n"
        "v_mfma_f32_16x16x32_bf16 a[52:55], v[8:11], v[52:55], a[52:55]\n"
        "v_cvt_pk_bf16_f32 v191, 0, v191\n"
        "v_mul_f32 v190, v181, v185\n"
        "v_mfma_f32_16x16x32_bf16 a[52:55], v[12:15], v[84:87], a[52:55]\n"
        "v_mul_f32 v192, v182, v185\n"
        "v_cvt_pk_bf16_f32 v190, 0, v190\n"
        "v_mfma_f32_16x16x32_bf16 a[20:23], v[4:7], v[84:87], a[20:23]\n"
        "global_load_lds_dwordx4 v239, s[62:63] offset:1024\n"
        "v_mfma_f32_16x16x32_bf16 a[20:23], v[0:3], v[52:55], a[20:23]\n"
        "v_cvt_pk_bf16_f32 v192, 0, v192\n"
        "v_add_f32 v189, v189, v190\n"
        "v_mfma_f32_16x16x32_bf16 a[24:27], v[0:3], v[56:59], a[24:27]\n"
        "v_add_f32 v191, v191, v192\n"
        "v_cvt_pk_bf16_f32 v189, 0, v189\n"
        "v_mfma_f32_16x16x32_bf16 a[24:27], v[4:7], v[88:91], a[24:27]\n"
        "v_cvt_pk_bf16_f32 v191, 0, v191\n"
        "v_lshlrev_b32 v190, 16, v127\n"
        "v_mfma_f32_16x16x32_bf16 a[56:59], v[12:15], v[88:91], a[56:59]\n"
        "global_load_lds_dwordx4 v240, s[62:63] offset:2048\n"
        "v_mfma_f32_16x16x32_bf16 a[56:59], v[8:11], v[56:59], a[56:59]\n"
        "v_lshlrev_b32 v192, 16, v129\n"
        "v_mul_f32 v189, v189, v190\n"
        "v_mfma_f32_16x16x32_bf16 a[60:63], v[8:11], v[60:63], a[60:63]\n"
        "ds_read_b128 v[8:11], v224 offset:34816\n"
        "v_mul_f32 v191, v191, v192\n"
        "v_mfma_f32_16x16x32_bf16 a[60:63], v[12:15], v[92:95], a[60:63]\n"
        "ds_read_b128 v[12:15], v225 offset:34816\n"
        "v_cvt_pk_bf16_f32 v201, v191, v189\n"
        "v_mfma_f32_16x16x32_bf16 a[28:31], v[4:7], v[92:95], a[28:31]\n"
        "ds_read_b128 v[4:7], v225 offset:32768\n"
        "global_load_lds_dwordx4 v241, s[62:63] offset:3072\n"
        "s_add_u32 m0, s72, 0x8000\n"
        "v_mfma_f32_16x16x32_bf16 a[28:31], v[0:3], v[60:63], a[28:31]\n"
        "ds_read_b128 v[0:3], v224 offset:32768\n"
        "v_mul_f32 v193, v180, v188\n"
        "v_mfma_f32_16x16x32_bf16 a[64:67], v[32:35], v[16:19], a[64:67]\n"
        "v_mul_f32 v195, v181, v188\n"
        "v_cvt_pk_bf16_f32 v193, 0, v193\n"
        "v_mfma_f32_16x16x32_bf16 a[64:67], v[64:67], v[20:23], a[64:67]\n"
        "v_cvt_pk_bf16_f32 v195, 0, v195\n"
        "v_mul_f32 v194, v181, v187\n"
        "v_mfma_f32_16x16x32_bf16 a[96:99], v[64:67], v[28:31], a[96:99]\n"
        "v_mul_f32 v196, v182, v187\n"
        "v_cvt_pk_bf16_f32 v194, 0, v194\n"
        "v_mfma_f32_16x16x32_bf16 a[96:99], v[32:35], v[24:27], a[96:99]\n"
        "v_cvt_pk_bf16_f32 v196, 0, v196\n"
        "v_add_f32 v193, v193, v194\n"
        "v_mfma_f32_16x16x32_bf16 a[100:103], v[36:39], v[24:27], a[100:103]\n"
        "v_add_f32 v195, v195, v196\n"
        "v_cvt_pk_bf16_f32 v193, 0, v193\n"
        "v_mfma_f32_16x16x32_bf16 a[100:103], v[68:71], v[28:31], a[100:103]\n"
        "v_cvt_pk_bf16_f32 v195, 0, v195\n"
        "v_mul_f32 v194, v182, v186\n"
        "v_mfma_f32_16x16x32_bf16 a[68:71], v[68:71], v[20:23], a[68:71]\n"
        "v_mul_f32 v196, v183, v186\n"
        "v_cvt_pk_bf16_f32 v194, 0, v194\n"
        "v_mfma_f32_16x16x32_bf16 a[68:71], v[36:39], v[16:19], a[68:71]\n"
        "v_cvt_pk_bf16_f32 v196, 0, v196\n"
        "v_add_f32 v193, v193, v194\n"
        "v_mfma_f32_16x16x32_bf16 a[72:75], v[40:43], v[16:19], a[72:75]\n"
        "v_add_f32 v195, v195, v196\n"
        "v_cvt_pk_bf16_f32 v193, 0, v193\n"
        "v_mfma_f32_16x16x32_bf16 a[72:75], v[72:75], v[20:23], a[72:75]\n"
        "v_cvt_pk_bf16_f32 v195, 0, v195\n"
        "v_mul_f32 v194, v183, v185\n"
        "v_mfma_f32_16x16x32_bf16 a[104:107], v[72:75], v[28:31], a[104:107]\n"
        "v_mul_f32 v196, v184, v185\n"
        "v_cvt_pk_bf16_f32 v194, 0, v194\n"
        "v_mfma_f32_16x16x32_bf16 a[104:107], v[40:43], v[24:27], a[104:107]\n"
        "v_cvt_pk_bf16_f32 v196, 0, v196\n"
        "v_add_f32 v193, v193, v194\n"
        "v_mfma_f32_16x16x32_bf16 a[108:111], v[44:47], v[24:27], a[108:111]\n"
        "v_add_f32 v195, v195, v196\n"
        "v_cvt_pk_bf16_f32 v193, 0, v193\n"
        "v_mfma_f32_16x16x32_bf16 a[108:111], v[76:79], v[28:31], a[108:111]\n"
        "v_cvt_pk_bf16_f32 v195, 0, v195\n"
        "v_lshlrev_b32 v194, 16, v131\n"
        "v_mfma_f32_16x16x32_bf16 a[76:79], v[76:79], v[20:23], a[76:79]\n"
        "v_lshlrev_b32 v196, 16, v133\n"
        "v_mul_f32 v193, v193, v194\n"
        "v_mfma_f32_16x16x32_bf16 a[76:79], v[44:47], v[16:19], a[76:79]\n"
        "v_mul_f32 v195, v195, v196\n"
        "v_cvt_pk_bf16_f32 v200, v195, v193\n"
        "v_mfma_f32_16x16x32_bf16 a[80:83], v[16:19], v[48:51], a[80:83]\n"
        "global_store_dwordx4 v171, v[200:203], s[76:77] offset:-4096 sc1\n"
        "v_lshlrev_b32 v185, 16, v140\n"
        "v_and_b32 v186, 0xffff0000, v140\n"
        "v_lshlrev_b32 v187, 16, v141\n"
        "v_and_b32 v188, 0xffff0000, v141\n"
        "v_mfma_f32_16x16x32_bf16 a[80:83], v[20:23], v[80:83], a[80:83]\n"
        "v_and_b32 v174, 0xffff0000, v97\n"
        "v_and_b32 v175, 0xffff0000, v99\n"
        "v_mfma_f32_16x16x32_bf16 a[112:115], v[28:31], v[80:83], a[112:115]\n"
        "v_and_b32 v176, 0xffff0000, v101\n"
        "v_and_b32 v177, 0xffff0000, v103\n"
        "v_mfma_f32_16x16x32_bf16 a[112:115], v[24:27], v[48:51], a[112:115]\n"
        "v_and_b32 v178, 0xffff0000, v105\n"
        "v_and_b32 v179, 0xffff0000, v107\n"
        "v_mfma_f32_16x16x32_bf16 a[116:119], v[24:27], v[52:55], a[116:119]\n"
        "v_and_b32 v180, 0xffff0000, v109\n"
        "v_and_b32 v181, 0xffff0000, v111\n"
        "v_mfma_f32_16x16x32_bf16 a[116:119], v[28:31], v[84:87], a[116:119]\n"
        "v_and_b32 v182, 0xffff0000, v113\n"
        "v_and_b32 v183, 0xffff0000, v115\n"
        "v_mfma_f32_16x16x32_bf16 a[84:87], v[20:23], v[84:87], a[84:87]\n"
        "v_and_b32 v184, 0xffff0000, v117\n"
        "v_mul_f32 v189, v174, v188\n"
        "v_mfma_f32_16x16x32_bf16 a[84:87], v[16:19], v[52:55], a[84:87]\n"
        "global_load_dword v253, v173, s[82:83] offset:0\n"
        "s_add_u32 s82, s12, 0x600000\n"
        "s_addc_u32 s83, s13, 0\n"
        "v_mfma_f32_16x16x32_bf16 a[88:91], v[16:19], v[56:59], a[88:91]\n"
        "global_load_ushort v172, v172, s[28:29]\n"
        "v_mul_f32 v191, v175, v188\n"
        "v_mfma_f32_16x16x32_bf16 a[88:91], v[20:23], v[88:91], a[88:91]\n"
        "v_cvt_pk_bf16_f32 v189, 0, v189\n"
        "v_cvt_pk_bf16_f32 v191, 0, v191\n"
        "v_mfma_f32_16x16x32_bf16 a[120:123], v[28:31], v[88:91], a[120:123]\n"
        "v_mul_f32 v190, v175, v187\n"
        "v_mul_f32 v192, v176, v187\n"
        "v_mfma_f32_16x16x32_bf16 a[120:123], v[24:27], v[56:59], a[120:123]\n"
        "v_cvt_pk_bf16_f32 v190, 0, v190\n"
        "v_cvt_pk_bf16_f32 v192, 0, v192\n"
        "v_mfma_f32_16x16x32_bf16 a[124:127], v[24:27], v[60:63], a[124:127]\n"
        "ds_read_b128 v[24:27], v224 offset:38912\n"
        "v_add_f32 v189, v189, v190\n"
        "v_mfma_f32_16x16x32_bf16 a[124:127], v[28:31], v[92:95], a[124:127]\n"
        "ds_read_b128 v[28:31], v225 offset:38912\n"
        "v_add_f32 v191, v191, v192\n"
        "v_mfma_f32_16x16x32_bf16 a[92:95], v[20:23], v[92:95], a[92:95]\n"
        "ds_read_b128 v[20:23], v225 offset:36864\n"
        "v_cvt_pk_bf16_f32 v189, 0, v189\n"
        "v_mfma_f32_16x16x32_bf16 a[92:95], v[16:19], v[60:63], a[92:95]\n"
        "ds_read_b128 v[16:19], v224 offset:36864\n"
        "s_waitcnt lgkmcnt(4)\n"
        "v_mfma_f32_16x16x32_bf16 a[128:131], v[32:35], v[0:3], a[128:131]\n"
        "v_cvt_pk_bf16_f32 v191, 0, v191\n"
        "v_mul_f32 v190, v176, v186\n"
        "v_mfma_f32_16x16x32_bf16 a[128:131], v[64:67], v[4:7], a[128:131]\n"
        "v_mul_f32 v192, v177, v186\n"
        "v_cvt_pk_bf16_f32 v190, 0, v190\n"
        "v_mfma_f32_16x16x32_bf16 a[160:163], v[64:67], v[12:15], a[160:163]\n"
        "v_cvt_pk_bf16_f32 v192, 0, v192\n"
        "v_add_f32 v189, v189, v190\n"
        "v_mfma_f32_16x16x32_bf16 a[160:163], v[32:35], v[8:11], a[160:163]\n"
        "v_add_f32 v191, v191, v192\n"
        "v_cvt_pk_bf16_f32 v189, 0, v189\n"
        "v_mfma_f32_16x16x32_bf16 a[164:167], v[36:39], v[8:11], a[164:167]\n"
        "v_cvt_pk_bf16_f32 v191, 0, v191\n"
        "v_mul_f32 v190, v177, v185\n"
        "v_mfma_f32_16x16x32_bf16 a[164:167], v[68:71], v[12:15], a[164:167]\n"
        "v_mul_f32 v192, v178, v185\n"
        "v_cvt_pk_bf16_f32 v190, 0, v190\n"
        "v_mfma_f32_16x16x32_bf16 a[132:135], v[68:71], v[4:7], a[132:135]\n"
        "v_cvt_pk_bf16_f32 v192, 0, v192\n"
        "v_add_f32 v189, v189, v190\n"
        "v_mfma_f32_16x16x32_bf16 a[132:135], v[36:39], v[0:3], a[132:135]\n"
        "v_add_f32 v191, v191, v192\n"
        "v_cvt_pk_bf16_f32 v189, 0, v189\n"
        "v_mfma_f32_16x16x32_bf16 a[136:139], v[40:43], v[0:3], a[136:139]\n"
        "v_cvt_pk_bf16_f32 v191, 0, v191\n"
        "v_and_b32 v190, 0xffff0000, v119\n"
        "v_mfma_f32_16x16x32_bf16 a[136:139], v[72:75], v[4:7], a[136:139]\n"
        "v_and_b32 v192, 0xffff0000, v121\n"
        "v_mul_f32 v189, v189, v190\n"
        "v_mfma_f32_16x16x32_bf16 a[168:171], v[72:75], v[12:15], a[168:171]\n"
        "v_mul_f32 v191, v191, v192\n"
        "v_cvt_pk_bf16_f32 v211, v191, v189\n"
        "v_mfma_f32_16x16x32_bf16 a[168:171], v[40:43], v[8:11], a[168:171]\n"
        "v_mul_f32 v193, v176, v188\n"
        "v_mul_f32 v195, v177, v188\n"
        "v_mfma_f32_16x16x32_bf16 a[172:175], v[44:47], v[8:11], a[172:175]\n"
        "v_cvt_pk_bf16_f32 v193, 0, v193\n"
        "v_cvt_pk_bf16_f32 v195, 0, v195\n"
        "v_mfma_f32_16x16x32_bf16 a[172:175], v[76:79], v[12:15], a[172:175]\n"
        "v_mul_f32 v194, v177, v187\n"
        "v_mul_f32 v196, v178, v187\n"
        "v_mfma_f32_16x16x32_bf16 a[140:143], v[76:79], v[4:7], a[140:143]\n"
        "v_cvt_pk_bf16_f32 v194, 0, v194\n"
        "v_cvt_pk_bf16_f32 v196, 0, v196\n"
        "v_mfma_f32_16x16x32_bf16 a[140:143], v[44:47], v[0:3], a[140:143]\n"
        "v_add_f32 v193, v193, v194\n"
        "v_add_f32 v195, v195, v196\n"
        "v_mfma_f32_16x16x32_bf16 a[144:147], v[0:3], v[48:51], a[144:147]\n"
        "v_cvt_pk_bf16_f32 v193, 0, v193\n"
        "v_cvt_pk_bf16_f32 v195, 0, v195\n"
        "v_mfma_f32_16x16x32_bf16 a[144:147], v[4:7], v[80:83], a[144:147]\n"
        "v_mul_f32 v194, v178, v186\n"
        "v_mul_f32 v196, v179, v186\n"
        "v_mfma_f32_16x16x32_bf16 a[176:179], v[12:15], v[80:83], a[176:179]\n"
        "v_cvt_pk_bf16_f32 v194, 0, v194\n"
        "v_cvt_pk_bf16_f32 v196, 0, v196\n"
        "v_mfma_f32_16x16x32_bf16 a[176:179], v[8:11], v[48:51], a[176:179]\n"
        "s_waitcnt vmcnt(21) lgkmcnt(0)\n"
        "v_add_f32 v193, v193, v194\n"
        "v_add_f32 v195, v195, v196\n"
        "v_mfma_f32_16x16x32_bf16 a[180:183], v[8:11], v[52:55], a[180:183]\n"
        "v_cvt_pk_bf16_f32 v193, 0, v193\n"
        "v_cvt_pk_bf16_f32 v195, 0, v195\n"
        "v_mfma_f32_16x16x32_bf16 a[180:183], v[12:15], v[84:87], a[180:183]\n"
        "v_mul_f32 v194, v179, v185\n"
        "v_mul_f32 v196, v180, v185\n"
        "v_mfma_f32_16x16x32_bf16 a[148:151], v[4:7], v[84:87], a[148:151]\n"
        "v_cvt_pk_bf16_f32 v194, 0, v194\n"
        "v_cvt_pk_bf16_f32 v196, 0, v196\n"
        "v_mfma_f32_16x16x32_bf16 a[148:151], v[0:3], v[52:55], a[148:151]\n"
        "v_add_f32 v193, v193, v194\n"
        "v_add_f32 v195, v195, v196\n"
        "v_mfma_f32_16x16x32_bf16 a[152:155], v[0:3], v[56:59], a[152:155]\n"
        "v_cvt_pk_bf16_f32 v193, 0, v193\n"
        "v_cvt_pk_bf16_f32 v195, 0, v195\n"
        "v_mfma_f32_16x16x32_bf16 a[152:155], v[4:7], v[88:91], a[152:155]\n"
        "v_and_b32 v194, 0xffff0000, v123\n"
        "v_and_b32 v196, 0xffff0000, v125\n"
        "v_mfma_f32_16x16x32_bf16 a[184:187], v[12:15], v[88:91], a[184:187]\n"
        "v_mul_f32 v193, v193, v194\n"
        "v_mul_f32 v195, v195, v196\n"
        "v_mfma_f32_16x16x32_bf16 a[184:187], v[8:11], v[56:59], a[184:187]\n"
        "s_waitcnt vmcnt(12)\n"
        "v_cvt_pk_bf16_f32 v210, v195, v193\n"
        "v_mul_f32 v189, v178, v188\n"
        "v_mfma_f32_16x16x32_bf16 a[188:191], v[8:11], v[60:63], a[188:191]\n"
        "s_barrier\n"
        "v_mfma_f32_16x16x32_bf16 a[188:191], v[12:15], v[92:95], a[188:191]\n"
        "ds_read_b128 v[8:11], v224 offset:18432\n"
        "global_load_lds_dwordx4 v234, s[56:57] offset:0\n"
        "v_mfma_f32_16x16x32_bf16 a[156:159], v[4:7], v[92:95], a[156:159]\n"
        "ds_read_b128 v[4:7], v225 offset:16384\n"
        "v_mul_f32 v191, v179, v188\n"
        "v_mfma_f32_16x16x32_bf16 a[156:159], v[0:3], v[60:63], a[156:159]\n"
        "ds_read_b128 v[0:3], v224 offset:16384\n"
        "v_cvt_pk_bf16_f32 v189, 0, v189\n"
        "v_mfma_f32_16x16x32_bf16 a[192:195], v[32:35], v[16:19], a[192:195]\n"
        "ds_read_b128 v[12:15], v225 offset:18432\n"
        "v_cvt_pk_bf16_f32 v191, 0, v191\n"
        "v_mfma_f32_16x16x32_bf16 a[192:195], v[64:67], v[20:23], a[192:195]\n"
        "global_load_lds_dwordx4 v235, s[56:57] offset:1024\n"
        "v_mfma_f32_16x16x32_bf16 a[224:227], v[64:67], v[28:31], a[224:227]\n"
        "ds_read_b64_tr_b16 v[64:65], v226 offset:8192\n"
        "v_mul_f32 v190, v179, v187\n"
        "v_mfma_f32_16x16x32_bf16 a[224:227], v[32:35], v[24:27], a[224:227]\n"
        "ds_read_b64_tr_b16 v[32:33], v226 offset:0\n"
        "v_mul_f32 v192, v180, v187\n"
        "v_mfma_f32_16x16x32_bf16 a[228:231], v[36:39], v[24:27], a[228:231]\n"
        "ds_read_b64_tr_b16 v[34:35], v227 offset:0\n"
        "v_cvt_pk_bf16_f32 v190, 0, v190\n"
        "v_mfma_f32_16x16x32_bf16 a[228:231], v[68:71], v[28:31], a[228:231]\n"
        "ds_read_b64_tr_b16 v[66:67], v227 offset:8192\n"
        "global_load_lds_dwordx4 v236, s[56:57] offset:2048\n"
        "v_mfma_f32_16x16x32_bf16 a[196:199], v[68:71], v[20:23], a[196:199]\n"
        "ds_read_b64_tr_b16 v[68:69], v228 offset:8192\n"
        "v_cvt_pk_bf16_f32 v192, 0, v192\n"
        "v_mfma_f32_16x16x32_bf16 a[196:199], v[36:39], v[16:19], a[196:199]\n"
        "ds_read_b64_tr_b16 v[36:37], v228 offset:0\n"
        "v_add_f32 v189, v189, v190\n"
        "v_mfma_f32_16x16x32_bf16 a[200:203], v[40:43], v[16:19], a[200:203]\n"
        "ds_read_b64_tr_b16 v[38:39], v229 offset:0\n"
        "v_add_f32 v191, v191, v192\n"
        "v_mfma_f32_16x16x32_bf16 a[200:203], v[72:75], v[20:23], a[200:203]\n"
        "ds_read_b64_tr_b16 v[70:71], v229 offset:8192\n"
        "global_load_lds_dwordx4 v237, s[56:57] offset:3072\n"
        "s_add_u32 m0, s72, 0x9000\n"
        "v_mfma_f32_16x16x32_bf16 a[232:235], v[72:75], v[28:31], a[232:235]\n"
        "ds_read_b64_tr_b16 v[72:73], v230 offset:8192\n"
        "v_cvt_pk_bf16_f32 v189, 0, v189\n"
        "v_mfma_f32_16x16x32_bf16 a[232:235], v[40:43], v[24:27], a[232:235]\n"
        "ds_read_b64_tr_b16 v[40:41], v230 offset:0\n"
        "v_cvt_pk_bf16_f32 v191, 0, v191\n"
        "v_mfma_f32_16x16x32_bf16 a[236:239], v[44:47], v[24:27], a[236:239]\n"
        "ds_read_b64_tr_b16 v[42:43], v231 offset:0\n"
        "v_mul_f32 v190, v180, v186\n"
        "v_mfma_f32_16x16x32_bf16 a[236:239], v[76:79], v[28:31], a[236:239]\n"
        "ds_read_b64_tr_b16 v[74:75], v231 offset:8192\n"
        "global_load_ushort v198, v198, s[26:27]\n"
        "v_mfma_f32_16x16x32_bf16 a[204:207], v[76:79], v[20:23], a[204:207]\n"
        "ds_read_b64_tr_b16 v[76:77], v232 offset:8192\n"
        "global_load_lds_dwordx4 v234, s[58:59] offset:0\n"
        "v_mfma_f32_16x16x32_bf16 a[204:207], v[44:47], v[16:19], a[204:207]\n"
        "ds_read_b64_tr_b16 v[44:45], v232 offset:0\n"
        "v_mul_f32 v192, v181, v186\n"
        "v_mfma_f32_16x16x32_bf16 a[208:211], v[16:19], v[48:51], a[208:211]\n"
        "ds_read_b64_tr_b16 v[46:47], v233 offset:0\n"
        "v_cvt_pk_bf16_f32 v190, 0, v190\n"
        "v_mfma_f32_16x16x32_bf16 a[208:211], v[20:23], v[80:83], a[208:211]\n"
        "ds_read_b64_tr_b16 v[78:79], v233 offset:8192\n"
        "v_cvt_pk_bf16_f32 v192, 0, v192\n"
        "v_mfma_f32_16x16x32_bf16 a[240:243], v[28:31], v[80:83], a[240:243]\n"
        "ds_read_b64_tr_b16 v[80:81], v226 offset:24576\n"
        "global_load_lds_dwordx4 v235, s[58:59] offset:1024\n"
        "v_mfma_f32_16x16x32_bf16 a[240:243], v[24:27], v[48:51], a[240:243]\n"
        "ds_read_b64_tr_b16 v[48:49], v226 offset:16384\n"
        "v_add_f32 v189, v189, v190\n"
        "v_mfma_f32_16x16x32_bf16 a[244:247], v[24:27], v[52:55], a[244:247]\n"
        "ds_read_b64_tr_b16 v[50:51], v227 offset:16384\n"
        "v_add_f32 v191, v191, v192\n"
        "v_mfma_f32_16x16x32_bf16 a[244:247], v[28:31], v[84:87], a[244:247]\n"
        "ds_read_b64_tr_b16 v[82:83], v227 offset:24576\n"
        "v_cvt_pk_bf16_f32 v189, 0, v189\n"
        "v_mfma_f32_16x16x32_bf16 a[212:215], v[20:23], v[84:87], a[212:215]\n"
        "ds_read_b64_tr_b16 v[84:85], v228 offset:24576\n"
        "global_load_lds_dwordx4 v236, s[58:59] offset:2048\n"
        "v_mfma_f32_16x16x32_bf16 a[212:215], v[16:19], v[52:55], a[212:215]\n"
        "ds_read_b64_tr_b16 v[52:53], v228 offset:16384\n"
        "v_cvt_pk_bf16_f32 v191, 0, v191\n"
        "v_mfma_f32_16x16x32_bf16 a[216:219], v[16:19], v[56:59], a[216:219]\n"
        "ds_read_b64_tr_b16 v[54:55], v229 offset:16384\n"
        "v_mul_f32 v190, v181, v185\n"
        "v_mfma_f32_16x16x32_bf16 a[216:219], v[20:23], v[88:91], a[216:219]\n"
        "ds_read_b64_tr_b16 v[86:87], v229 offset:24576\n"
        "v_mul_f32 v192, v182, v185\n"
        "v_mfma_f32_16x16x32_bf16 a[248:251], v[28:31], v[88:91], a[248:251]\n"
        "ds_read_b64_tr_b16 v[88:89], v230 offset:24576\n"
        "global_load_lds_dwordx4 v237, s[58:59] offset:3072\n"
        "s_add_u32 m0, s73, 0x20000\n"
        "v_mfma_f32_16x16x32_bf16 a[248:251], v[24:27], v[56:59], a[248:251]\n"
        "ds_read_b64_tr_b16 v[56:57], v230 offset:16384\n"
        "v_cvt_pk_bf16_f32 v190, 0, v190\n"
        "v_mfma_f32_16x16x32_bf16 a[252:255], v[24:27], v[60:63], a[252:255]\n"
        "ds_read_b64_tr_b16 v[58:59], v231 offset:16384\n"
        "v_cvt_pk_bf16_f32 v192, 0, v192\n"
        "v_mfma_f32_16x16x32_bf16 a[252:255], v[28:31], v[92:95], a[252:255]\n"
        "ds_read_b64_tr_b16 v[90:91], v231 offset:24576\n"
        "v_add_f32 v189, v189, v190\n"
        "v_mfma_f32_16x16x32_bf16 a[220:223], v[20:23], v[92:95], a[220:223]\n"
        "ds_read_b64_tr_b16 v[92:93], v232 offset:24576\n"
        "global_load_lds_dwordx4 v238, s[68:69] offset:0\n"
        "v_mfma_f32_16x16x32_bf16 a[220:223], v[16:19], v[60:63], a[220:223]\n"
        "ds_read_b64_tr_b16 v[60:61], v232 offset:16384\n"
        "s_waitcnt lgkmcnt(8)\n"
        "v_mfma_f32_16x16x32_bf16 a[0:3], v[32:35], v[0:3], a[0:3]\n"
        "ds_read_b64_tr_b16 v[62:63], v233 offset:16384\n"
        "v_add_f32 v191, v191, v192\n"
        "v_mfma_f32_16x16x32_bf16 a[0:3], v[64:67], v[4:7], a[0:3]\n"
        "ds_read_b64_tr_b16 v[94:95], v233 offset:24576\n"
        "v_cvt_pk_bf16_f32 v189, 0, v189\n"
        "v_mfma_f32_16x16x32_bf16 a[32:35], v[64:67], v[12:15], a[32:35]\n"
        "ds_read_b128 v[16:19], v224 offset:20480\n"
        "global_load_lds_dwordx4 v239, s[68:69] offset:1024\n"
        "v_mfma_f32_16x16x32_bf16 a[32:35], v[32:35], v[8:11], a[32:35]\n"
        "ds_read_b128 v[20:23], v225 offset:20480\n"
        "v_cvt_pk_bf16_f32 v191, 0, v191\n"
        "v_mfma_f32_16x16x32_bf16 a[36:39], v[36:39], v[8:11], a[36:39]\n"
        "ds_read_b128 v[24:27], v224 offset:22528\n"
        "v_and_b32 v190, 0xffff0000, v127\n"
        "v_mfma_f32_16x16x32_bf16 a[36:39], v[68:71], v[12:15], a[36:39]\n"
        "ds_read_b128 v[28:31], v225 offset:22528\n"
        "v_and_b32 v192, 0xffff0000, v129\n"
        "v_mfma_f32_16x16x32_bf16 a[4:7], v[68:71], v[4:7], a[4:7]\n"
        "global_load_lds_dwordx4 v240, s[68:69] offset:2048\n"
        "v_mfma_f32_16x16x32_bf16 a[4:7], v[36:39], v[0:3], a[4:7]\n"
        "v_mul_f32 v189, v189, v190\n"
        "v_mul_f32 v191, v191, v192\n"
        "v_mfma_f32_16x16x32_bf16 a[8:11], v[40:43], v[0:3], a[8:11]\n"
        "v_cvt_pk_bf16_f32 v209, v191, v189\n"
        "v_mul_f32 v193, v180, v188\n"
        "v_mfma_f32_16x16x32_bf16 a[8:11], v[72:75], v[4:7], a[8:11]\n"
        "v_mul_f32 v195, v181, v188\n"
        "v_cvt_pk_bf16_f32 v193, 0, v193\n"
        "v_mfma_f32_16x16x32_bf16 a[40:43], v[72:75], v[12:15], a[40:43]\n"
        "global_load_dword v253, v173, s[82:83] offset:0\n"
        "v_mfma_f32_16x16x32_bf16 a[40:43], v[40:43], v[8:11], a[40:43]\n"
        "global_load_lds_dwordx4 v241, s[68:69] offset:3072\n"
        "s_add_u32 s68, s68, 0x80000\n"
        "s_addc_u32 s69, s69, 0\n"
        "v_mfma_f32_16x16x32_bf16 a[44:47], v[44:47], v[8:11], a[44:47]\n"
        "s_add_u32 m0, s73, 0x24000\n"
        "s_add_u32 s82, s12, 0x700000\n"
        "s_addc_u32 s83, s13, 0\n"
        "v_mfma_f32_16x16x32_bf16 a[44:47], v[76:79], v[12:15], a[44:47]\n"
        "v_cvt_pk_bf16_f32 v195, 0, v195\n"
        "v_mul_f32 v194, v181, v187\n"
        "v_mfma_f32_16x16x32_bf16 a[12:15], v[76:79], v[4:7], a[12:15]\n"
        "v_mul_f32 v196, v182, v187\n"
        "v_cvt_pk_bf16_f32 v194, 0, v194\n"
        "v_mfma_f32_16x16x32_bf16 a[12:15], v[44:47], v[0:3], a[12:15]\n"
        "global_load_lds_dwordx4 v238, s[68:69] offset:0\n"
        "v_cvt_pk_bf16_f32 v196, 0, v196\n"
        "v_mfma_f32_16x16x32_bf16 a[16:19], v[0:3], v[48:51], a[16:19]\n"
        "s_waitcnt lgkmcnt(0)\n"
        "v_add_f32 v193, v193, v194\n"
        "v_add_f32 v195, v195, v196\n"
        "v_mfma_f32_16x16x32_bf16 a[16:19], v[4:7], v[80:83], a[16:19]\n"
        "v_cvt_pk_bf16_f32 v193, 0, v193\n"
        "v_cvt_pk_bf16_f32 v195, 0, v195\n"
        "v_mfma_f32_16x16x32_bf16 a[48:51], v[12:15], v[80:83], a[48:51]\n"
        "v_mul_f32 v194, v182, v186\n"
        "v_mul_f32 v196, v183, v186\n"
        "v_mfma_f32_16x16x32_bf16 a[48:51], v[8:11], v[48:51], a[48:51]\n"
        "global_load_lds_dwordx4 v239, s[68:69] offset:1024\n"
        "v_cvt_pk_bf16_f32 v194, 0, v194\n"
        "v_mfma_f32_16x16x32_bf16 a[52:55], v[8:11], v[52:55], a[52:55]\n"
        "v_cvt_pk_bf16_f32 v196, 0, v196\n"
        "v_add_f32 v193, v193, v194\n"
        "v_mfma_f32_16x16x32_bf16 a[52:55], v[12:15], v[84:87], a[52:55]\n"
        "v_add_f32 v195, v195, v196\n"
        "v_cvt_pk_bf16_f32 v193, 0, v193\n"
        "v_mfma_f32_16x16x32_bf16 a[20:23], v[4:7], v[84:87], a[20:23]\n"
        "v_cvt_pk_bf16_f32 v195, 0, v195\n"
        "v_mul_f32 v194, v183, v185\n"
        "v_mfma_f32_16x16x32_bf16 a[20:23], v[0:3], v[52:55], a[20:23]\n"
        "global_load_lds_dwordx4 v240, s[68:69] offset:2048\n"
        "s_and_b32 s53, s40, 1\n"
        "v_mul_f32 v196, v184, v185\n"
        "v_mfma_f32_16x16x32_bf16 a[24:27], v[0:3], v[56:59], a[24:27]\n"
        "s_lshl_b32 s53, s53, 10\n"
        "v_cvt_pk_bf16_f32 v194, 0, v194\n"
        "v_cvt_pk_bf16_f32 v196, 0, v196\n"
        "v_mfma_f32_16x16x32_bf16 a[24:27], v[4:7], v[88:91], a[24:27]\n"
        "s_sub_u32 s53, 0x8000, s53\n"
        "v_add_f32 v193, v193, v194\n"
        "v_add_f32 v195, v195, v196\n"
        "v_mfma_f32_16x16x32_bf16 a[56:59], v[12:15], v[88:91], a[56:59]\n"
        "v_cvt_pk_bf16_f32 v193, 0, v193\n"
        "v_cvt_pk_bf16_f32 v195, 0, v195\n"
        "v_mfma_f32_16x16x32_bf16 a[56:59], v[8:11], v[56:59], a[56:59]\n"
        "global_load_lds_dwordx4 v241, s[68:69] offset:3072\n"
        "s_add_u32 s68, s68, 0x80000\n"
        "s_addc_u32 s69, s69, 0\n"
        "v_mfma_f32_16x16x32_bf16 a[60:63], v[8:11], v[60:63], a[60:63]\n"
        "ds_read_b128 v[8:11], v224 offset:2048\n"
        "s_add_u32 m0, s72, 0x0\n"
        "v_and_b32 v194, 0xffff0000, v131\n"
        "v_mfma_f32_16x16x32_bf16 a[60:63], v[12:15], v[92:95], a[60:63]\n"
        "ds_read_b128 v[12:15], v225 offset:2048\n"
        "v_and_b32 v196, 0xffff0000, v133\n"
        "v_mfma_f32_16x16x32_bf16 a[28:31], v[4:7], v[92:95], a[28:31]\n"
        "ds_read_b128 v[4:7], v225 offset:0\n"
        "v_mul_f32 v193, v193, v194\n"
        "v_mfma_f32_16x16x32_bf16 a[28:31], v[0:3], v[60:63], a[28:31]\n"
        "ds_read_b128 v[0:3], v224 offset:0\n"
        "v_mul_f32 v195, v195, v196\n"
        "v_mfma_f32_16x16x32_bf16 a[64:67], v[32:35], v[16:19], a[64:67]\n"
        "v_cvt_pk_bf16_f32 v208, v195, v193\n"
        "v_readfirstlane_b32 s86, v250\n"
        "v_mfma_f32_16x16x32_bf16 a[64:67], v[64:67], v[20:23], a[64:67]\n"
        "global_store_dwordx4 v171, v[208:211], s[76:77] offset:0 sc1\n"
        "s_waitcnt vmcnt(26)\n"
        "v_readfirstlane_b32 s87, v251\n"
        "v_lshlrev_b32 v174, 16, v158\n"
        "v_and_b32 v175, 0xffff0000, v158\n"
        "v_mfma_f32_16x16x32_bf16 a[96:99], v[64:67], v[28:31], a[96:99]\n"
        "s_add_u32 s86, s86, 0x1\n"
        "s_addc_u32 s87, s87, 0\n"
        "v_mov_b32 v250, s86\n"
        "v_mfma_f32_16x16x32_bf16 a[96:99], v[32:35], v[24:27], a[96:99]\n"
        "s_add_u32 s76, s38, 0x3b20040\n"
        "s_addc_u32 s77, s39, 0\n"
        "v_mov_b32 v251, s87\n"
        "v_mfma_f32_16x16x32_bf16 a[100:103], v[36:39], v[24:27], a[100:103]\n"
        "v_lshlrev_b32 v176, 16, v159\n"
        "v_and_b32 v177, 0xffff0000, v159\n"
        "v_mfma_f32_16x16x32_bf16 a[100:103], v[68:71], v[28:31], a[100:103]\n"
        "v_lshlrev_b32 v178, 16, v160\n"
        "v_and_b32 v179, 0xffff0000, v160\n"
        "v_mfma_f32_16x16x32_bf16 a[68:71], v[68:71], v[20:23], a[68:71]\n"
        "v_lshlrev_b32 v180, 16, v161\n"
        "v_and_b32 v181, 0xffff0000, v161\n"
        "v_mfma_f32_16x16x32_bf16 a[68:71], v[36:39], v[16:19], a[68:71]\n"
        "v_lshlrev_b32 v182, 16, v162\n"
        "v_and_b32 v183, 0xffff0000, v162\n"
        "v_mfma_f32_16x16x32_bf16 a[72:75], v[40:43], v[16:19], a[72:75]\n"
        "LHA3_%=:\n"
        "s_cbranch_execz LHA7_%=\n"
        "v_lshlrev_b32 v184, 16, v163\n"
        "v_and_b32 v185, 0xffff0000, v163\n"
        "v_mfma_f32_16x16x32_bf16 a[72:75], v[72:75], v[20:23], a[72:75]\n"
        "v_lshlrev_b32 v186, 16, v164\n"
        "v_and_b32 v187, 0xffff0000, v164\n"
        "v_mfma_f32_16x16x32_bf16 a[104:107], v[72:75], v[28:31], a[104:107]\n"
        "v_lshlrev_b32 v188, 16, v165\n"
        "v_and_b32 v189, 0xffff0000, v165\n"
        "v_mfma_f32_16x16x32_bf16 a[104:107], v[40:43], v[24:27], a[104:107]\n"
        "v_lshlrev_b32 v192, 16, v142\n"
        "v_and_b32 v193, 0xffff0000, v142\n"
        "v_mfma_f32_16x16x32_bf16 a[108:111], v[44:47], v[24:27], a[108:111]\n"
        "v_mul_f32 v192, v192, v174\n"
        "v_mul_f32 v193, v193, v175\n"
        "v_mfma_f32_16x16x32_bf16 a[108:111], v[76:79], v[28:31], a[108:111]\n"
        "v_cvt_pk_bf16_f32 v192, 0, v192\n"
        "v_cvt_pk_bf16_f32 v193, 0, v193\n"
        "v_mfma_f32_16x16x32_bf16 a[76:79], v[76:79], v[20:23], a[76:79]\n"
        "v_add_f32 v190, v192, v193\n"
        "v_lshlrev_b32 v194, 16, v143\n"
        "v_mfma_f32_16x16x32_bf16 a[76:79], v[44:47], v[16:19], a[76:79]\n"
        "v_and_b32 v195, 0xffff0000, v143\n"
        "v_mul_f32 v194, v194, v176\n"
        "v_mfma_f32_16x16x32_bf16 a[80:83], v[16:19], v[48:51], a[80:83]\n"
        "v_mul_f32 v195, v195, v177\n"
        "v_cvt_pk_bf16_f32 v194, 0, v194\n"
        "v_mfma_f32_16x16x32_bf16 a[80:83], v[20:23], v[80:83], a[80:83]\n"
        "v_cvt_pk_bf16_f32 v195, 0, v195\n"
        "v_add_f32 v194, v194, v195\n"
        "v_mfma_f32_16x16x32_bf16 a[112:115], v[28:31], v[80:83], a[112:115]\n"
        "v_add_f32 v190, v190, v194\n"
        "v_lshlrev_b32 v192, 16, v144\n"
        "v_mfma_f32_16x16x32_bf16 a[112:115], v[24:27], v[48:51], a[112:115]\n"
        "v_and_b32 v193, 0xffff0000, v144\n"
        "v_mul_f32 v192, v192, v178\n"
        "v_mfma_f32_16x16x32_bf16 a[116:119], v[24:27], v[52:55], a[116:119]\n"
        "v_mul_f32 v193, v193, v179\n"
        "v_cvt_pk_bf16_f32 v192, 0, v192\n"
        "v_mfma_f32_16x16x32_bf16 a[116:119], v[28:31], v[84:87], a[116:119]\n"
        "v_cvt_pk_bf16_f32 v193, 0, v193\n"
        "v_add_f32 v192, v192, v193\n"
        "v_mfma_f32_16x16x32_bf16 a[84:87], v[20:23], v[84:87], a[84:87]\n"
        "v_add_f32 v190, v190, v192\n"
        "v_lshlrev_b32 v194, 16, v145\n"
        "v_mfma_f32_16x16x32_bf16 a[84:87], v[16:19], v[52:55], a[84:87]\n"
        "s_mov_b32 s88, 0x3e0293ee\n"
        "v_and_b32 v195, 0xffff0000, v145\n"
        "v_mul_f32 v194, v194, v180\n"
        "v_mfma_f32_16x16x32_bf16 a[88:91], v[16:19], v[56:59], a[88:91]\n"
        "s_sub_u32 s75, s45, s44\n"
        "v_mul_f32 v195, v195, v181\n"
        "v_cvt_pk_bf16_f32 v194, 0, v194\n"
        "v_mfma_f32_16x16x32_bf16 a[88:91], v[20:23], v[88:91], a[88:91]\n"
        "s_lshl_b32 s75, s75, 15\n"
        "v_cvt_pk_bf16_f32 v195, 0, v195\n"
        "v_add_f32 v194, v194, v195\n"
        "v_mfma_f32_16x16x32_bf16 a[120:123], v[28:31], v[88:91], a[120:123]\n"
        "s_lshr_b32 s50, s40, 1\n"
        "v_add_f32 v190, v190, v194\n"
        "v_lshlrev_b32 v192, 16, v146\n"
        "v_mfma_f32_16x16x32_bf16 a[120:123], v[24:27], v[56:59], a[120:123]\n"
        "s_lshl1_add_u32 s50, s44, s50\n"
        "v_and_b32 v193, 0xffff0000, v146\n"
        "v_mul_f32 v192, v192, v182\n"
        "v_mfma_f32_16x16x32_bf16 a[124:127], v[24:27], v[60:63], a[124:127]\n"
        "ds_read_b128 v[24:27], v224 offset:6144\n"
        "s_lshl_b32 s50, s50, 14\n"
        "v_mul_f32 v193, v193, v183\n"
        "v_mfma_f32_16x16x32_bf16 a[124:127], v[28:31], v[92:95], a[124:127]\n"
        "ds_read_b128 v[28:31], v225 offset:6144\n"
        "s_mul_i32 s51, s43, 529408\n"
        "v_cvt_pk_bf16_f32 v192, 0, v192\n"
        "v_mfma_f32_16x16x32_bf16 a[92:95], v[20:23], v[92:95], a[92:95]\n"
        "ds_read_b128 v[20:23], v225 offset:4096\n"
        "s_add_u32 s50, s50, s51\n"
        "v_cvt_pk_bf16_f32 v193, 0, v193\n"
        "v_mfma_f32_16x16x32_bf16 a[92:95], v[16:19], v[60:63], a[92:95]\n"
        "ds_read_b128 v[16:19], v224 offset:4096\n"
        "s_and_b32 s51, s40, 1\n"
        "s_waitcnt lgkmcnt(4)\n"
        "v_mfma_f32_16x16x32_bf16 a[128:131], v[32:35], v[0:3], a[128:131]\n"
        "s_lshl_b32 s51, s51, 11\n"
        "v_add_f32 v192, v192, v193\n"
        "v_add_f32 v190, v190, v192\n"
        "v_mfma_f32_16x16x32_bf16 a[128:131], v[64:67], v[4:7], a[128:131]\n"
        "s_add_u32 s52, s50, s51\n"
        "v_lshlrev_b32 v194, 16, v147\n"
        "v_and_b32 v195, 0xffff0000, v147\n"
        "v_mfma_f32_16x16x32_bf16 a[160:163], v[64:67], v[12:15], a[160:163]\n"
        "v_mul_f32 v194, v194, v184\n"
        "v_mul_f32 v195, v195, v185\n"
        "v_mfma_f32_16x16x32_bf16 a[160:163], v[32:35], v[8:11], a[160:163]\n"
        "v_cvt_pk_bf16_f32 v194, 0, v194\n"
        "v_cvt_pk_bf16_f32 v195, 0, v195\n"
        "v_mfma_f32_16x16x32_bf16 a[164:167], v[36:39], v[8:11], a[164:167]\n"
        "v_add_f32 v194, v194, v195\n"
        "v_add_f32 v190, v190, v194\n"
        "v_mfma_f32_16x16x32_bf16 a[164:167], v[68:71], v[12:15], a[164:167]\n"
        "v_lshlrev_b32 v192, 16, v148\n"
        "v_and_b32 v193, 0xffff0000, v148\n"
        "v_mfma_f32_16x16x32_bf16 a[132:135], v[68:71], v[4:7], a[132:135]\n"
        "v_mul_f32 v192, v192, v186\n"
        "v_mul_f32 v193, v193, v187\n"
        "v_mfma_f32_16x16x32_bf16 a[132:135], v[36:39], v[0:3], a[132:135]\n"
        "v_cvt_pk_bf16_f32 v192, 0, v192\n"
        "v_cvt_pk_bf16_f32 v193, 0, v193\n"
        "v_mfma_f32_16x16x32_bf16 a[136:139], v[40:43], v[0:3], a[136:139]\n"
        "v_add_f32 v192, v192, v193\n"
        "v_add_f32 v190, v190, v192\n"
        "v_mfma_f32_16x16x32_bf16 a[136:139], v[72:75], v[4:7], a[136:139]\n"
        "v_lshlrev_b32 v194, 16, v149\n"
        "v_and_b32 v195, 0xffff0000, v149\n"
        "v_mfma_f32_16x16x32_bf16 a[168:171], v[72:75], v[12:15], a[168:171]\n"
        "v_mul_f32 v194, v194, v188\n"
        "v_mul_f32 v195, v195, v189\n"
        "v_mfma_f32_16x16x32_bf16 a[168:171], v[40:43], v[8:11], a[168:171]\n"
        "v_cvt_pk_bf16_f32 v194, 0, v194\n"
        "v_cvt_pk_bf16_f32 v195, 0, v195\n"
        "v_mfma_f32_16x16x32_bf16 a[172:175], v[44:47], v[8:11], a[172:175]\n"
        "v_add_f32 v194, v194, v195\n"
        "v_add_f32 v190, v190, v194\n"
        "v_mfma_f32_16x16x32_bf16 a[172:175], v[76:79], v[12:15], a[172:175]\n"
        "v_lshlrev_b32 v192, 16, v150\n"
        "v_and_b32 v193, 0xffff0000, v150\n"
        "v_mfma_f32_16x16x32_bf16 a[140:143], v[76:79], v[4:7], a[140:143]\n"
        "v_mul_f32 v192, v192, v174\n"
        "v_mul_f32 v193, v193, v175\n"
        "v_mfma_f32_16x16x32_bf16 a[140:143], v[44:47], v[0:3], a[140:143]\n"
        "v_cvt_pk_bf16_f32 v192, 0, v192\n"
        "v_cvt_pk_bf16_f32 v193, 0, v193\n"
        "v_mfma_f32_16x16x32_bf16 a[144:147], v[0:3], v[48:51], a[144:147]\n"
        "v_add_f32 v191, v192, v193\n"
        "v_lshlrev_b32 v194, 16, v151\n"
        "v_mfma_f32_16x16x32_bf16 a[144:147], v[4:7], v[80:83], a[144:147]\n"
        "v_and_b32 v195, 0xffff0000, v151\n"
        "v_mul_f32 v194, v194, v176\n"
        "v_mfma_f32_16x16x32_bf16 a[176:179], v[12:15], v[80:83], a[176:179]\n"
        "v_mul_f32 v195, v195, v177\n"
        "v_cvt_pk_bf16_f32 v194, 0, v194\n"
        "v_mfma_f32_16x16x32_bf16 a[176:179], v[8:11], v[48:51], a[176:179]\n"
        "v_cvt_pk_bf16_f32 v195, 0, v195\n"
        "v_add_f32 v194, v194, v195\n"
        "v_mfma_f32_16x16x32_bf16 a[180:183], v[8:11], v[52:55], a[180:183]\n"
        "v_add_f32 v191, v191, v194\n"
        "v_lshlrev_b32 v192, 16, v152\n"
        "v_mfma_f32_16x16x32_bf16 a[180:183], v[12:15], v[84:87], a[180:183]\n"
        "v_and_b32 v193, 0xffff0000, v152\n"
        "v_mul_f32 v192, v192, v178\n"
        "v_mfma_f32_16x16x32_bf16 a[148:151], v[4:7], v[84:87], a[148:151]\n"
        "v_mul_f32 v193, v193, v179\n"
        "v_cvt_pk_bf16_f32 v192, 0, v192\n"
        "v_mfma_f32_16x16x32_bf16 a[148:151], v[0:3], v[52:55], a[148:151]\n"
        "global_load_dword v253, v173, s[82:83] offset:0\n"
        "v_cvt_pk_bf16_f32 v193, 0, v193\n"
        "v_mfma_f32_16x16x32_bf16 a[152:155], v[0:3], v[56:59], a[152:155]\n"
        "v_add_f32 v192, v192, v193\n"
        "v_add_f32 v191, v191, v192\n"
        "v_mfma_f32_16x16x32_bf16 a[152:155], v[4:7], v[88:91], a[152:155]\n"
        "v_lshlrev_b32 v194, 16, v153\n"
        "v_and_b32 v195, 0xffff0000, v153\n"
        "v_mfma_f32_16x16x32_bf16 a[184:187], v[12:15], v[88:91], a[184:187]\n"
        "v_mul_f32 v194, v194, v180\n"
        "v_mul_f32 v195, v195, v181\n"
        "v_mfma_f32_16x16x32_bf16 a[184:187], v[8:11], v[56:59], a[184:187]\n"
        "s_waitcnt vmcnt(11) lgkmcnt(0)\n"
        "v_cvt_pk_bf16_f32 v194, 0, v194\n"
        "v_cvt_pk_bf16_f32 v195, 0, v195\n"
        "v_mfma_f32_16x16x32_bf16 a[188:191], v[8:11], v[60:63], a[188:191]\n"
        "s_barrier\n"
        "v_mfma_f32_16x16x32_bf16 a[188:191], v[12:15], v[92:95], a[188:191]\n"
        "ds_read_b128 v[8:11], v224 offset:51200\n"
        "global_load_lds_dwordx4 v242, s[64:65] offset:0\n"
        "v_mfma_f32_16x16x32_bf16 a[156:159], v[4:7], v[92:95], a[156:159]\n"
        "ds_read_b128 v[4:7], v225 offset:49152\n"
        "v_add_f32 v194, v194, v195\n"
        "v_mfma_f32_16x16x32_bf16 a[156:159], v[0:3], v[60:63], a[156:159]\n"
        "ds_read_b128 v[0:3], v224 offset:49152\n"
        "v_add_f32 v191, v191, v194\n"
        "v_mfma_f32_16x16x32_bf16 a[192:195], v[32:35], v[16:19], a[192:195]\n"
        "ds_read_b128 v[12:15], v225 offset:51200\n"
        "v_lshlrev_b32 v192, 16, v154\n"
        "v_mfma_f32_16x16x32_bf16 a[192:195], v[64:67], v[20:23], a[192:195]\n"
        "global_load_lds_dwordx4 v243, s[64:65] offset:1024\n"
        "v_mfma_f32_16x16x32_bf16 a[224:227], v[64:67], v[28:31], a[224:227]\n"
        "ds_read_b64_tr_b16 v[64:65], v226 offset:40960\n"
        "v_and_b32 v193, 0xffff0000, v154\n"
        "v_mfma_f32_16x16x32_bf16 a[224:227], v[32:35], v[24:27], a[224:227]\n"
        "ds_read_b64_tr_b16 v[32:33], v226 offset:32768\n"
        "s_add_u32 s97, s52, 0x3000\n"
        "v_mul_f32 v192, v192, v182\n"
        "v_mfma_f32_16x16x32_bf16 a[228:231], v[36:39], v[24:27], a[228:231]\n"
        "ds_read_b64_tr_b16 v[34:35], v227 offset:32768\n"
        "v_mul_f32 v193, v193, v183\n"
        "v_mfma_f32_16x16x32_bf16 a[228:231], v[68:71], v[28:31], a[228:231]\n"
        "ds_read_b64_tr_b16 v[66:67], v227 offset:40960\n"
        "global_load_lds_dwordx4 v244, s[64:65] offset:2048\n"
        "v_mfma_f32_16x16x32_bf16 a[196:199], v[68:71], v[20:23], a[196:199]\n"
        "ds_read_b64_tr_b16 v[68:69], v228 offset:40960\n"
        "v_cvt_pk_bf16_f32 v192, 0, v192\n"
        "v_mfma_f32_16x16x32_bf16 a[196:199], v[36:39], v[16:19], a[196:199]\n"
        "ds_read_b64_tr_b16 v[36:37], v228 offset:32768\n"
        "v_cvt_pk_bf16_f32 v193, 0, v193\n"
        "v_mfma_f32_16x16x32_bf16 a[200:203], v[40:43], v[16:19], a[200:203]\n"
        "ds_read_b64_tr_b16 v[38:39], v229 offset:32768\n"
        "v_add_f32 v192, v192, v193\n"
        "v_mfma_f32_16x16x32_bf16 a[200:203], v[72:75], v[20:23], a[200:203]\n"
        "ds_read_b64_tr_b16 v[70:71], v229 offset:40960\n"
        "global_load_lds_dwordx4 v245, s[64:65] offset:3072\n"
        "s_add_u32 m0, s72, 0x1000\n"
        "v_mfma_f32_16x16x32_bf16 a[232:235], v[72:75], v[28:31], a[232:235]\n"
        "ds_read_b64_tr_b16 v[72:73], v230 offset:40960\n"
        "s_add_u32 s64, s64, 0x80\n"
        "s_addc_u32 s65, s65, 0\n"
        "v_mfma_f32_16x16x32_bf16 a[232:235], v[40:43], v[24:27], a[232:235]\n"
        "ds_read_b64_tr_b16 v[40:41], v230 offset:32768\n"
        "v_lshl_or_b32 v172, v172, 16, v198\n"
        "v_mfma_f32_16x16x32_bf16 a[236:239], v[44:47], v[24:27], a[236:239]\n"
        "ds_read_b64_tr_b16 v[42:43], v231 offset:32768\n"
        "v_readfirstlane_b32 s54, v172\n"
        "v_mfma_f32_16x16x32_bf16 a[236:239], v[76:79], v[28:31], a[236:239]\n"
        "ds_read_b64_tr_b16 v[74:75], v231 offset:40960\n"
        "global_load_lds_dwordx4 v242, s[66:67] offset:0\n"
        "v_mfma_f32_16x16x32_bf16 a[204:207], v[76:79], v[20:23], a[204:207]\n"
        "ds_read_b64_tr_b16 v[76:77], v232 offset:40960\n"
        "v_add_f32 v191, v191, v192\n"
        "v_mfma_f32_16x16x32_bf16 a[204:207], v[44:47], v[16:19], a[204:207]\n"
        "ds_read_b64_tr_b16 v[44:45], v232 offset:32768\n"
        "v_lshlrev_b32 v194, 16, v155\n"
        "v_mfma_f32_16x16x32_bf16 a[208:211], v[16:19], v[48:51], a[208:211]\n"
        "ds_read_b64_tr_b16 v[46:47], v233 offset:32768\n"
        "v_and_b32 v195, 0xffff0000, v155\n"
        "v_mfma_f32_16x16x32_bf16 a[208:211], v[20:23], v[80:83], a[208:211]\n"
        "ds_read_b64_tr_b16 v[78:79], v233 offset:40960\n"
        "global_load_lds_dwordx4 v243, s[66:67] offset:1024\n"
        "v_mfma_f32_16x16x32_bf16 a[240:243], v[28:31], v[80:83], a[240:243]\n"
        "ds_read_b64_tr_b16 v[80:81], v226 offset:57344\n"
        "v_mul_f32 v194, v194, v184\n"
        "v_mfma_f32_16x16x32_bf16 a[240:243], v[24:27], v[48:51], a[240:243]\n"
        "ds_read_b64_tr_b16 v[48:49], v226 offset:49152\n"
        "v_mul_f32 v195, v195, v185\n"
        "v_mfma_f32_16x16x32_bf16 a[244:247], v[24:27], v[52:55], a[244:247]\n"
        "ds_read_b64_tr_b16 v[50:51], v227 offset:49152\n"
        "v_cvt_pk_bf16_f32 v194, 0, v194\n"
        "v_mfma_f32_16x16x32_bf16 a[244:247], v[28:31], v[84:87], a[244:247]\n"
        "ds_read_b64_tr_b16 v[82:83], v227 offset:57344\n"
        "global_load_lds_dwordx4 v244, s[66:67] offset:2048\n"
        "v_mfma_f32_16x16x32_bf16 a[212:215], v[20:23], v[84:87], a[212:215]\n"
        "ds_read_b64_tr_b16 v[84:85], v228 offset:57344\n"
        "v_cvt_pk_bf16_f32 v195, 0, v195\n"
        "v_mfma_f32_16x16x32_bf16 a[212:215], v[16:19], v[52:55], a[212:215]\n"
        "ds_read_b64_tr_b16 v[52:53], v228 offset:49152\n"
        "v_add_f32 v194, v194, v195\n"
        "v_mfma_f32_16x16x32_bf16 a[216:219], v[16:19], v[56:59], a[216:219]\n"
        "ds_read_b64_tr_b16 v[54:55], v229 offset:49152\n"
        "v_add_f32 v191, v191, v194\n"
        "v_mfma_f32_16x16x32_bf16 a[216:219], v[20:23], v[88:91], a[216:219]\n"
        "ds_read_b64_tr_b16 v[86:87], v229 offset:57344\n"
        "global_load_lds_dwordx4 v245, s[66:67] offset:3072\n"
        "s_add_u32 m0, s72, 0x10000\n"
        "v_mfma_f32_16x16x32_bf16 a[248:251], v[28:31], v[88:91], a[248:251]\n"
        "ds_read_b64_tr_b16 v[88:89], v230 offset:57344\n"
        "s_add_u32 s66, s66, 0x80\n"
        "s_addc_u32 s67, s67, 0\n"
        "v_mfma_f32_16x16x32_bf16 a[248:251], v[24:27], v[56:59], a[248:251]\n"
        "ds_read_b64_tr_b16 v[56:57], v230 offset:49152\n"
        "v_lshlrev_b32 v192, 16, v156\n"
        "v_mfma_f32_16x16x32_bf16 a[252:255], v[24:27], v[60:63], a[252:255]\n"
        "ds_read_b64_tr_b16 v[58:59], v231 offset:49152\n"
        "v_and_b32 v193, 0xffff0000, v156\n"
        "v_mfma_f32_16x16x32_bf16 a[252:255], v[28:31], v[92:95], a[252:255]\n"
        "ds_read_b64_tr_b16 v[90:91], v231 offset:57344\n"
        "global_load_lds_dwordx4 v242, s[64:65] offset:0\n"
        "v_mfma_f32_16x16x32_bf16 a[220:223], v[20:23], v[92:95], a[220:223]\n"
        "ds_read_b64_tr_b16 v[92:93], v232 offset:57344\n"
        "v_mul_f32 v192, v192, v186\n"
        "v_mfma_f32_16x16x32_bf16 a[220:223], v[16:19], v[60:63], a[220:223]\n"
        "ds_read_b64_tr_b16 v[60:61], v232 offset:49152\n"
        "s_waitcnt lgkmcnt(8)\n"
        "v_mfma_f32_16x16x32_bf16 a[0:3], v[32:35], v[0:3], a[0:3]\n"
        "ds_read_b64_tr_b16 v[62:63], v233 offset:49152\n"
        "v_mul_f32 v193, v193, v187\n"
        "v_mfma_f32_16x16x32_bf16 a[0:3], v[64:67], v[4:7], a[0:3]\n"
        "ds_read_b64_tr_b16 v[94:95], v233 offset:57344\n"
        "global_load_lds_dwordx4 v243, s[64:65] offset:1024\n"
        "v_mfma_f32_16x16x32_bf16 a[32:35], v[64:67], v[12:15], a[32:35]\n"
        "ds_read_b128 v[16:19], v224 offset:53248\n"
        "v_cvt_pk_bf16_f32 v192, 0, v192\n"
        "v_mfma_f32_16x16x32_bf16 a[32:35], v[32:35], v[8:11], a[32:35]\n"
        "ds_read_b128 v[20:23], v225 offset:53248\n"
        "v_cvt_pk_bf16_f32 v193, 0, v193\n"
        "v_mfma_f32_16x16x32_bf16 a[36:39], v[36:39], v[8:11], a[36:39]\n"
        "ds_read_b128 v[24:27], v224 offset:55296\n"
        "v_add_f32 v192, v192, v193\n"
        "v_mfma_f32_16x16x32_bf16 a[36:39], v[68:71], v[12:15], a[36:39]\n"
        "ds_read_b128 v[28:31], v225 offset:55296\n"
        "global_load_lds_dwordx4 v244, s[64:65] offset:2048\n"
        "v_mfma_f32_16x16x32_bf16 a[4:7], v[68:71], v[4:7], a[4:7]\n"
        "s_add_u32 s70, s38, s97\n"
        "s_addc_u32 s71, s39, 0\n"
        "v_add_f32 v191, v191, v192\n"
        "v_mfma_f32_16x16x32_bf16 a[4:7], v[36:39], v[0:3], a[4:7]\n"
        "v_lshlrev_b32 v194, 16, v157\n"
        "v_and_b32 v195, 0xffff0000, v157\n"
        "v_mfma_f32_16x16x32_bf16 a[8:11], v[40:43], v[0:3], a[8:11]\n"
        "v_mul_f32 v194, v194, v188\n"
        "v_mul_f32 v195, v195, v189\n"
        "v_mfma_f32_16x16x32_bf16 a[8:11], v[72:75], v[4:7], a[8:11]\n"
        "global_load_lds_dwordx4 v245, s[64:65] offset:3072\n"
        "s_add_u32 m0, s72, 0x11000\n"
        "v_mfma_f32_16x16x32_bf16 a[40:43], v[72:75], v[12:15], a[40:43]\n"
        "s_add_u32 s64, s64, 0x80\n"
        "s_addc_u32 s65, s65, 0\n"
        "v_cvt_pk_bf16_f32 v194, 0, v194\n"
        "v_mfma_f32_16x16x32_bf16 a[40:43], v[40:43], v[8:11], a[40:43]\n"
        "v_cvt_pk_bf16_f32 v195, 0, v195\n"
        "v_add_f32 v194, v194, v195\n"
        "v_mfma_f32_16x16x32_bf16 a[44:47], v[44:47], v[8:11], a[44:47]\n"
        "v_add_f32 v191, v191, v194\n"
        "v_add_f32_dpp v190, v190, v190 row_shr:1 row_mask:0xf bank_mask:0xf bound_ctrl:1\n"
        "v_mfma_f32_16x16x32_bf16 a[44:47], v[76:79], v[12:15], a[44:47]\n"
        "global_load_lds_dwordx4 v242, s[66:67] offset:0\n"
        "v_mfma_f32_16x16x32_bf16 a[12:15], v[76:79], v[4:7], a[12:15]\n"
        "v_add_f32_dpp v191, v191, v191 row_shr:1 row_mask:0xf bank_mask:0xf bound_ctrl:1\n"
        "v_add_f32_dpp v190, v190, v190 row_shr:2 row_mask:0xf bank_mask:0xf bound_ctrl:1\n"
        "v_mfma_f32_16x16x32_bf16 a[12:15], v[44:47], v[0:3], a[12:15]\n"
        "v_add_f32_dpp v191, v191, v191 row_shr:2 row_mask:0xf bank_mask:0xf bound_ctrl:1\n"
        "v_add_f32_dpp v190, v190, v190 row_shr:4 row_mask:0xf bank_mask:0xf bound_ctrl:1\n"
        "v_mfma_f32_16x16x32_bf16 a[16:19], v[0:3], v[48:51], a[16:19]\n"
        "s_waitcnt lgkmcnt(0)\n"
        "v_add_f32_dpp v191, v191, v191 row_shr:4 row_mask:0xf bank_mask:0xf bound_ctrl:1\n"
        "v_add_f32_dpp v190, v190, v190 row_shr:8 row_mask:0xf bank_mask:0xf bound_ctrl:1\n"
        "v_mfma_f32_16x16x32_bf16 a[16:19], v[4:7], v[80:83], a[16:19]\n"
        "global_load_lds_dwordx4 v243, s[66:67] offset:1024\n"
        "v_mfma_f32_16x16x32_bf16 a[48:51], v[12:15], v[80:83], a[48:51]\n"
        "v_add_f32_dpp v191, v191, v191 row_shr:8 row_mask:0xf bank_mask:0xf bound_ctrl:1\n"
        "v_add_f32_dpp v190, v190, v190 row_bcast:15 row_mask:0xa bank_mask:0xf bound_ctrl:1\n"
        "v_mfma_f32_16x16x32_bf16 a[48:51], v[8:11], v[48:51], a[48:51]\n"
        "v_add_f32_dpp v191, v191, v191 row_bcast:15 row_mask:0xa bank_mask:0xf bound_ctrl:1\n"
        "v_add_f32_dpp v190, v190, v190 row_bcast:31 row_mask:0xc bank_mask:0xf bound_ctrl:1\n"
        "v_mfma_f32_16x16x32_bf16 a[52:55], v[8:11], v[52:55], a[52:55]\n"
        "v_add_f32_dpp v191, v191, v191 row_bcast:31 row_mask:0xc bank_mask:0xf bound_ctrl:1\n"
        "v_cvt_pk_bf16_f32 v190, 0, v190\n"
        "v_mfma_f32_16x16x32_bf16 a[52:55], v[12:15], v[84:87], a[52:55]\n"
        "global_load_lds_dwordx4 v244, s[66:67] offset:2048\n"
        "v_mfma_f32_16x16x32_bf16 a[20:23], v[4:7], v[84:87], a[20:23]\n"
        "v_mul_f32 v190, 0xbfb8aa3b, v190\n"
        "v_lshrrev_b32 v192, 2, v254\n"
        "v_mfma_f32_16x16x32_bf16 a[20:23], v[0:3], v[52:55], a[20:23]\n"
        "v_exp_f32 v190, v190\n"
        "v_mfma_f32_16x16x32_bf16 a[24:27], v[0:3], v[56:59], a[24:27]\n"
        "v_add_f32 v190, 1.0, v190\n"
        "v_add_u32 v192, s80, v192\n"
        "v_mfma_f32_16x16x32_bf16 a[24:27], v[4:7], v[88:91], a[24:27]\n"
        "v_rcp_f32 v190, v190\n"
        "v_mfma_f32_16x16x32_bf16 a[56:59], v[12:15], v[88:91], a[56:59]\n"
        "global_load_lds_dwordx4 v245, s[66:67] offset:3072\n"
        "s_add_u32 s66, s66, 0x80\n"
        "s_addc_u32 s67, s67, 0\n"
        "v_mfma_f32_16x16x32_bf16 a[56:59], v[8:11], v[56:59], a[56:59]\n"
        "s_add_u32 m0, s73, 0x1c000\n"
        "v_cvt_pk_bf16_f32 v190, 0, v190\n"
        "v_cvt_pk_bf16_f32 v191, 0, v191\n"
        "v_mfma_f32_16x16x32_bf16 a[60:63], v[8:11], v[60:63], a[60:63]\n"
        "ds_read_b128 v[8:11], v224 offset:34816\n"
        "v_readlane_b32 s84, v190, 63\n"
        "v_mfma_f32_16x16x32_bf16 a[60:63], v[12:15], v[92:95], a[60:63]\n"
        "ds_read_b128 v[12:15], v225 offset:34816\n"
        "v_mov_b32 v190, s84\n"
        "v_mfma_f32_16x16x32_bf16 a[28:31], v[4:7], v[92:95], a[28:31]\n"
        "ds_read_b128 v[4:7], v225 offset:32768\n"
        "v_mul_f32 v191, 0xbfb8aa3b, v191\n"
        "v_mfma_f32_16x16x32_bf16 a[28:31], v[0:3], v[60:63], a[28:31]\n"
        "ds_read_b128 v[0:3], v224 offset:32768\n"
        "v_lshrrev_b32 v193, 2, v254\n"
        "v_mfma_f32_16x16x32_bf16 a[64:67], v[32:35], v[16:19], a[64:67]\n"
        "s_mov_b64 exec, 0xffff\n"
        "global_store_dword v192, v190, s[76:77] sc1\n"
        "s_mov_b64 exec, -1\n"
        "v_mfma_f32_16x16x32_bf16 a[64:67], v[64:67], v[20:23], a[64:67]\n"
        "v_exp_f32 v191, v191\n"
        "v_mfma_f32_16x16x32_bf16 a[96:99], v[64:67], v[28:31], a[96:99]\n"
        "v_add_f32 v191, 1.0, v191\n"
        "v_add_u32 v193, s81, v193\n"
        "v_mfma_f32_16x16x32_bf16 a[96:99], v[32:35], v[24:27], a[96:99]\n"
        "v_rcp_f32 v191, v191\n"
        "v_mfma_f32_16x16x32_bf16 a[100:103], v[36:39], v[24:27], a[100:103]\n"
        "v_cvt_pk_bf16_f32 v191, 0, v191\n"
        "v_add_u32 v226, s53, v226\n"
        "v_mfma_f32_16x16x32_bf16 a[100:103], v[68:71], v[28:31], a[100:103]\n"
        "v_readlane_b32 s85, v191, 63\n"
        "v_add_u32 v227, s53, v227\n"
        "v_mfma_f32_16x16x32_bf16 a[68:71], v[68:71], v[20:23], a[68:71]\n"
        "v_mov_b32 v191, s85\n"
        "v_add_u32 v228, s53, v228\n"
        "v_mfma_f32_16x16x32_bf16 a[68:71], v[36:39], v[16:19], a[68:71]\n"
        "s_mov_b64 exec, 0xffff\n"
        "global_store_dword v193, v191, s[76:77] sc1\n"
        "s_mov_b64 exec, -1\n"
        "v_mfma_f32_16x16x32_bf16 a[72:75], v[40:43], v[16:19], a[72:75]\n"
        "v_add_u32 v229, s53, v229\n"
        "v_add_u32 v230, s53, v230\n"
        "v_mfma_f32_16x16x32_bf16 a[72:75], v[72:75], v[20:23], a[72:75]\n"
        "v_add_u32 v231, s53, v231\n"
        "v_add_u32 v232, s53, v232\n"
        "v_mfma_f32_16x16x32_bf16 a[104:107], v[72:75], v[28:31], a[104:107]\n"
        "v_add_u32 v233, s53, v233\n"
        "s_add_u32 s53, s52, s51\n"
        "v_mfma_f32_16x16x32_bf16 a[104:107], v[40:43], v[24:27], a[104:107]\n"
        "s_add_u32 s53, s53, 0x1100000\n"
        "v_mfma_f32_16x16x32_bf16 a[108:111], v[44:47], v[24:27], a[108:111]\n"
        "v_mfma_f32_16x16x32_bf16 a[108:111], v[76:79], v[28:31], a[108:111]\n"
        "v_mfma_f32_16x16x32_bf16 a[76:79], v[76:79], v[20:23], a[76:79]\n"
        "v_mfma_f32_16x16x32_bf16 a[76:79], v[44:47], v[16:19], a[76:79]\n"
        "v_mfma_f32_16x16x32_bf16 a[80:83], v[16:19], v[48:51], a[80:83]\n"
        "v_mfma_f32_16x16x32_bf16 a[80:83], v[20:23], v[80:83], a[80:83]\n"
        "v_mfma_f32_16x16x32_bf16 a[112:115], v[28:31], v[80:83], a[112:115]\n"
        "v_mfma_f32_16x16x32_bf16 a[112:115], v[24:27], v[48:51], a[112:115]\n"
        "v_mfma_f32_16x16x32_bf16 a[116:119], v[24:27], v[52:55], a[116:119]\n"
        "v_mfma_f32_16x16x32_bf16 a[116:119], v[28:31], v[84:87], a[116:119]\n"
        "v_mfma_f32_16x16x32_bf16 a[84:87], v[20:23], v[84:87], a[84:87]\n"
        "v_mfma_f32_16x16x32_bf16 a[84:87], v[16:19], v[52:55], a[84:87]\n"
        "v_mfma_f32_16x16x32_bf16 a[88:91], v[16:19], v[56:59], a[88:91]\n"
        "v_mfma_f32_16x16x32_bf16 a[88:91], v[20:23], v[88:91], a[88:91]\n"
        "v_mfma_f32_16x16x32_bf16 a[120:123], v[28:31], v[88:91], a[120:123]\n"
        "v_mfma_f32_16x16x32_bf16 a[120:123], v[24:27], v[56:59], a[120:123]\n"
        "v_mfma_f32_16x16x32_bf16 a[124:127], v[24:27], v[60:63], a[124:127]\n"
        "ds_read_b128 v[24:27], v224 offset:38912\n"
        "s_waitcnt lgkmcnt(1)\n"
        "v_mfma_f32_16x16x32_bf16 a[124:127], v[28:31], v[92:95], a[124:127]\n"
        "ds_read_b128 v[28:31], v225 offset:38912\n"
        "v_mfma_f32_16x16x32_bf16 a[92:95], v[20:23], v[92:95], a[92:95]\n"
        "ds_read_b128 v[20:23], v225 offset:36864\n"
        "v_mfma_f32_16x16x32_bf16 a[92:95], v[16:19], v[60:63], a[92:95]\n"
        "ds_read_b128 v[16:19], v224 offset:36864\n"
        "v_mfma_f32_16x16x32_bf16 a[128:131], v[32:35], v[0:3], a[128:131]\n"
        "v_mfma_f32_16x16x32_bf16 a[128:131], v[64:67], v[4:7], a[128:131]\n"
        "v_mfma_f32_16x16x32_bf16 a[160:163], v[64:67], v[12:15], a[160:163]\n"
        "v_mfma_f32_16x16x32_bf16 a[160:163], v[32:35], v[8:11], a[160:163]\n"
        "v_mfma_f32_16x16x32_bf16 a[164:167], v[36:39], v[8:11], a[164:167]\n"
        "v_mfma_f32_16x16x32_bf16 a[164:167], v[68:71], v[12:15], a[164:167]\n"
        "v_mfma_f32_16x16x32_bf16 a[132:135], v[68:71], v[4:7], a[132:135]\n"
        "v_mfma_f32_16x16x32_bf16 a[132:135], v[36:39], v[0:3], a[132:135]\n"
        "v_mfma_f32_16x16x32_bf16 a[136:139], v[40:43], v[0:3], a[136:139]\n"
        "v_mfma_f32_16x16x32_bf16 a[136:139], v[72:75], v[4:7], a[136:139]\n"
        "v_mfma_f32_16x16x32_bf16 a[168:171], v[72:75], v[12:15], a[168:171]\n"
        "v_mfma_f32_16x16x32_bf16 a[168:171], v[40:43], v[8:11], a[168:171]\n"
        "v_mfma_f32_16x16x32_bf16 a[172:175], v[44:47], v[8:11], a[172:175]\n"
        "v_mfma_f32_16x16x32_bf16 a[172:175], v[76:79], v[12:15], a[172:175]\n"
        "v_mfma_f32_16x16x32_bf16 a[140:143], v[76:79], v[4:7], a[140:143]\n"
        "v_mfma_f32_16x16x32_bf16 a[140:143], v[44:47], v[0:3], a[140:143]\n"
        "v_mfma_f32_16x16x32_bf16 a[144:147], v[0:3], v[48:51], a[144:147]\n"
        "v_mfma_f32_16x16x32_bf16 a[144:147], v[4:7], v[80:83], a[144:147]\n"
        "v_mfma_f32_16x16x32_bf16 a[176:179], v[12:15], v[80:83], a[176:179]\n"
        "v_mfma_f32_16x16x32_bf16 a[176:179], v[8:11], v[48:51], a[176:179]\n"
        "v_mfma_f32_16x16x32_bf16 a[180:183], v[8:11], v[52:55], a[180:183]\n"
        "v_mfma_f32_16x16x32_bf16 a[180:183], v[12:15], v[84:87], a[180:183]\n"
        "v_mfma_f32_16x16x32_bf16 a[148:151], v[4:7], v[84:87], a[148:151]\n"
        "v_mfma_f32_16x16x32_bf16 a[148:151], v[0:3], v[52:55], a[148:151]\n"
        "v_mfma_f32_16x16x32_bf16 a[152:155], v[0:3], v[56:59], a[152:155]\n"
        "v_mfma_f32_16x16x32_bf16 a[152:155], v[4:7], v[88:91], a[152:155]\n"
        "v_mfma_f32_16x16x32_bf16 a[184:187], v[12:15], v[88:91], a[184:187]\n"
        "v_mfma_f32_16x16x32_bf16 a[184:187], v[8:11], v[56:59], a[184:187]\n"
        "s_waitcnt vmcnt(10) lgkmcnt(0)\n"
        "v_mfma_f32_16x16x32_bf16 a[188:191], v[8:11], v[60:63], a[188:191]\n"
        "s_barrier\n"
        "v_mfma_f32_16x16x32_bf16 a[188:191], v[12:15], v[92:95], a[188:191]\n"
        "ds_read_b64_tr_b16 v[160:161], v226 offset:32768\n"
        "global_load_lds_dwordx4 v238, s[68:69] offset:0\n"
        "v_mfma_f32_16x16x32_bf16 a[156:159], v[4:7], v[92:95], a[156:159]\n"
        "ds_read_b64_tr_b16 v[162:163], v227 offset:32768\n"
        "v_mfma_f32_16x16x32_bf16 a[156:159], v[0:3], v[60:63], a[156:159]\n"
        "ds_read_b64_tr_b16 v[164:165], v226 offset:40960\n"
        "v_mfma_f32_16x16x32_bf16 a[192:195], v[32:35], v[16:19], a[192:195]\n"
        "ds_read_b64_tr_b16 v[166:167], v227 offset:40960\n"
        "v_mfma_f32_16x16x32_bf16 a[192:195], v[64:67], v[20:23], a[192:195]\n"
        "ds_read_b64_tr_b16 v[172:173], v228 offset:40960\n"
        "global_load_lds_dwordx4 v239, s[68:69] offset:1024\n"
        "v_mfma_f32_16x16x32_bf16 a[224:227], v[64:67], v[28:31], a[224:227]\n"
        "ds_read_b64_tr_b16 v[174:175], v229 offset:40960\n"
        "v_mfma_f32_16x16x32_bf16 a[224:227], v[32:35], v[24:27], a[224:227]\n"
        "ds_read_b64_tr_b16 v[168:169], v228 offset:32768\n"
        "v_mfma_f32_16x16x32_bf16 a[228:231], v[36:39], v[24:27], a[228:231]\n"
        "ds_read_b64_tr_b16 v[170:171], v229 offset:32768\n"
        "v_mfma_f32_16x16x32_bf16 a[228:231], v[68:71], v[28:31], a[228:231]\n"
        "ds_read_b128 v[192:195], v246 offset:2048\n"
        "global_load_lds_dwordx4 v240, s[68:69] offset:2048\n"
        "v_mfma_f32_16x16x32_bf16 a[196:199], v[68:71], v[20:23], a[196:199]\n"
        "ds_read_b128 v[208:211], v247 offset:2048\n"
        "v_accvgpr_read_b32 v177, a225\n"
        "v_mfma_f32_16x16x32_bf16 a[196:199], v[36:39], v[16:19], a[196:199]\n"
        "ds_read_b128 v[196:199], v246 offset:0\n"
        "v_accvgpr_read_b32 v178, a226\n"
        "v_mfma_f32_16x16x32_bf16 a[200:203], v[40:43], v[16:19], a[200:203]\n"
        "ds_read_b128 v[212:215], v247 offset:0\n"
        "v_accvgpr_read_b32 v179, a227\n"
        "v_mfma_f32_16x16x32_bf16 a[200:203], v[72:75], v[20:23], a[200:203]\n"
        "ds_read_b128 v[200:203], v246 offset:6144\n"
        "global_load_lds_dwordx4 v241, s[68:69] offset:3072\n"
        "s_add_u32 m0, s72, 0x8000\n"
        "v_mfma_f32_16x16x32_bf16 a[232:235], v[72:75], v[28:31], a[232:235]\n"
        "ds_read_b128 v[216:219], v247 offset:6144\n"
        "s_add_u32 s68, s68, 0x80000\n"
        "s_addc_u32 s69, s69, 0\n"
        "v_mfma_f32_16x16x32_bf16 a[232:235], v[40:43], v[24:27], a[232:235]\n"
        "ds_read_b128 v[204:207], v246 offset:4096\n"
        "v_accvgpr_read_b32 v180, a228\n"
        "v_mfma_f32_16x16x32_bf16 a[236:239], v[44:47], v[24:27], a[236:239]\n"
        "ds_read_b128 v[220:223], v247 offset:4096\n"
        "v_accvgpr_read_b32 v181, a229\n"
        "v_mfma_f32_16x16x32_bf16 a[236:239], v[76:79], v[28:31], a[236:239]\n"
        "ds_read_b64_tr_b16 v[32:33], v230 offset:32768\n"
        "global_load_lds_dwordx4 v242, s[64:65] offset:0\n"
        "v_mfma_f32_16x16x32_bf16 a[204:207], v[76:79], v[20:23], a[204:207]\n"
        "ds_read_b64_tr_b16 v[34:35], v231 offset:32768\n"
        "v_accvgpr_read_b32 v182, a230\n"
        "v_mfma_f32_16x16x32_bf16 a[204:207], v[44:47], v[16:19], a[204:207]\n"
        "ds_read_b64_tr_b16 v[36:37], v230 offset:40960\n"
        "s_add_u32 s97, s52, 0x2000\n"
        "v_accvgpr_read_b32 v183, a231\n"
        "v_mfma_f32_16x16x32_bf16 a[208:211], v[16:19], v[48:51], a[208:211]\n"
        "ds_read_b64_tr_b16 v[38:39], v231 offset:40960\n"
        "v_mul_f32 v177, s88, v177\n"
        "v_mfma_f32_16x16x32_bf16 a[208:211], v[20:23], v[80:83], a[208:211]\n"
        "ds_read_b64_tr_b16 v[44:45], v232 offset:40960\n"
        "global_load_lds_dwordx4 v243, s[64:65] offset:1024\n"
        "v_mfma_f32_16x16x32_bf16 a[240:243], v[28:31], v[80:83], a[240:243]\n"
        "ds_read_b64_tr_b16 v[46:47], v233 offset:40960\n"
        "v_mul_f32 v178, s88, v178\n"
        "v_mfma_f32_16x16x32_bf16 a[240:243], v[24:27], v[48:51], a[240:243]\n"
        "ds_read_b64_tr_b16 v[40:41], v232 offset:32768\n"
        "v_mul_f32 v179, s88, v179\n"
        "v_mfma_f32_16x16x32_bf16 a[244:247], v[24:27], v[52:55], a[244:247]\n"
        "ds_read_b64_tr_b16 v[42:43], v233 offset:32768\n"
        "v_mul_f32 v180, s88, v180\n"
        "v_mfma_f32_16x16x32_bf16 a[244:247], v[28:31], v[84:87], a[244:247]\n"
        "ds_read_b64_tr_b16 v[48:49], v226 offset:33792\n"
        "global_load_lds_dwordx4 v244, s[64:65] offset:2048\n"
        "v_mfma_f32_16x16x32_bf16 a[212:215], v[20:23], v[84:87], a[212:215]\n"
        "ds_read_b64_tr_b16 v[50:51], v227 offset:33792\n"
        "v_mul_f32 v181, s88, v181\n"
        "v_mfma_f32_16x16x32_bf16 a[212:215], v[16:19], v[52:55], a[212:215]\n"
        "ds_read_b64_tr_b16 v[52:53], v226 offset:41984\n"
        "v_mul_f32 v182, s88, v182\n"
        "v_mfma_f32_16x16x32_bf16 a[216:219], v[16:19], v[56:59], a[216:219]\n"
        "ds_read_b64_tr_b16 v[54:55], v227 offset:41984\n"
        "v_mul_f32 v183, s88, v183\n"
        "v_mfma_f32_16x16x32_bf16 a[216:219], v[20:23], v[88:91], a[216:219]\n"
        "global_load_lds_dwordx4 v245, s[64:65] offset:3072\n"
        "s_add_u32 m0, s72, 0x9000\n"
        "v_mfma_f32_16x16x32_bf16 a[248:251], v[28:31], v[88:91], a[248:251]\n"
        "s_add_u32 s64, s64, 0x80\n"
        "s_addc_u32 s65, s65, 0\n"
        "v_accvgpr_read_b32 v185, a233\n"
        "v_mfma_f32_16x16x32_bf16 a[248:251], v[24:27], v[56:59], a[248:251]\n"
        "ds_read_b64_tr_b16 v[56:57], v228 offset:33792\n"
        "v_accvgpr_read_b32 v186, a234\n"
        "v_mfma_f32_16x16x32_bf16 a[252:255], v[24:27], v[60:63], a[252:255]\n"
        "ds_read_b64_tr_b16 v[58:59], v229 offset:33792\n"
        "v_accvgpr_read_b32 v187, a235\n"
        "v_mfma_f32_16x16x32_bf16 a[252:255], v[28:31], v[92:95], a[252:255]\n"
        "global_load_lds_dwordx4 v242, s[66:67] offset:0\n"
        "v_mfma_f32_16x16x32_bf16 a[220:223], v[20:23], v[92:95], a[220:223]\n"
        "s_waitcnt lgkmcnt(4)\n"
        "v_accvgpr_read_b32 v188, a236\n"
        "v_accvgpr_read_b32 v189, a237\n"
        "v_mfma_f32_16x16x32_bf16 a[220:223], v[16:19], v[60:63], a[220:223]\n"
        "ds_read_b64_tr_b16 v[60:61], v228 offset:41984\n"
        "v_accvgpr_read_b32 v190, a238\n"
        "v_mfma_f32_16x16x32_bf16 v[0:3], v[160:163], v[192:195], 0\n"
        "ds_read_b64_tr_b16 v[62:63], v229 offset:41984\n"
        "v_accvgpr_read_b32 v191, a239\n"
        "v_mfma_f32_16x16x32_bf16 v[0:3], v[164:167], v[208:211], v[0:3]\n"
        "global_load_lds_dwordx4 v243, s[66:67] offset:1024\n"
        "v_mfma_f32_16x16x32_bf16 v[4:7], v[172:175], v[208:211], 0\n"
        "v_mul_f32 v185, s88, v185\n"
        "v_mul_f32 v186, s88, v186\n"
        "v_mfma_f32_16x16x32_bf16 v[4:7], v[168:171], v[192:195], v[4:7]\n"
        "v_mul_f32 v187, s88, v187\n"
        "v_mul_f32 v188, s88, v188\n"
        "v_mfma_f32_16x16x32_bf16 v[68:71], v[168:171], v[196:199], 0\n"
        "v_mul_f32 v189, s88, v189\n"
        "v_mul_f32 v190, s88, v190\n"
        "v_mfma_f32_16x16x32_bf16 v[68:71], v[172:175], v[212:215], v[68:71]\n"
        "global_load_lds_dwordx4 v244, s[66:67] offset:2048\n"
        "v_mfma_f32_16x16x32_bf16 v[64:67], v[164:167], v[212:215], 0\n"
        "LHA4_%=:\n"
        "s_cbranch_execz LHA8_%=\n"
        "v_mul_f32 v191, s88, v191\n"
        "v_mfma_f32_16x16x32_bf16 v[64:67], v[160:163], v[196:199], v[64:67]\n"
        "v_accvgpr_read_b32 v176, a224\n"
        "v_mul_f32 v176, s88, v176\n"
        "v_mfma_f32_16x16x32_bf16 v[96:99], v[160:163], v[200:203], 0\n"
        "v_cvt_pk_bf16_f32 v176, v176, v177\n"
        "v_cvt_pk_bf16_f32 v177, v178, v179\n"
        "v_mfma_f32_16x16x32_bf16 v[96:99], v[164:167], v[216:219], v[96:99]\n"
        "v_cvt_pk_bf16_f32 v178, v180, v181\n"
        "v_cvt_pk_bf16_f32 v179, v182, v183\n"
        "v_mfma_f32_16x16x32_bf16 v[100:103], v[172:175], v[216:219], 0\n"
        "global_load_lds_dwordx4 v245, s[66:67] offset:3072\n"
        "s_add_u32 m0, s73, 0x20000\n"
        "v_accvgpr_write_b32 a224, v176\n"
        "v_mfma_f32_16x16x32_bf16 v[100:103], v[168:171], v[200:203], v[100:103]\n"
        "s_add_u32 s66, s66, 0x80\n"
        "s_addc_u32 s67, s67, 0\n"
        "v_accvgpr_write_b32 a225, v177\n"
        "v_mfma_f32_16x16x32_bf16 v[132:135], v[168:171], v[204:207], 0\n"
        "ds_read_b64_tr_b16 v[168:169], v232 offset:33792\n"
        "v_accvgpr_write_b32 a226, v178\n"
        "v_mfma_f32_16x16x32_bf16 v[132:135], v[172:175], v[220:223], v[132:135]\n"
        "ds_read_b64_tr_b16 v[172:173], v232 offset:41984\n"
        "v_accvgpr_write_b32 a227, v179\n"
        "v_mfma_f32_16x16x32_bf16 v[128:131], v[164:167], v[220:223], 0\n"
        "ds_read_b64_tr_b16 v[164:165], v230 offset:41984\n"
        "v_accvgpr_read_b32 v177, a193\n"
        "v_mfma_f32_16x16x32_bf16 v[128:131], v[160:163], v[204:207], v[128:131]\n"
        "ds_read_b64_tr_b16 v[160:161], v230 offset:33792\n"
        "v_accvgpr_read_b32 v178, a194\n"
        "v_mfma_f32_16x16x32_bf16 v[8:11], v[32:35], v[192:195], 0\n"
        "ds_read_b64_tr_b16 v[162:163], v231 offset:33792\n"
        "v_accvgpr_read_b32 v179, a195\n"
        "v_mfma_f32_16x16x32_bf16 v[8:11], v[36:39], v[208:211], v[8:11]\n"
        "ds_read_b64_tr_b16 v[166:167], v231 offset:41984\n"
        "v_accvgpr_read_b32 v180, a196\n"
        "v_mfma_f32_16x16x32_bf16 v[12:15], v[44:47], v[208:211], 0\n"
        "ds_read_b64_tr_b16 v[174:175], v233 offset:41984\n"
        "v_accvgpr_read_b32 v181, a197\n"
        "v_mfma_f32_16x16x32_bf16 v[12:15], v[40:43], v[192:195], v[12:15]\n"
        "ds_read_b64_tr_b16 v[170:171], v233 offset:33792\n"
        "v_accvgpr_read_b32 v182, a198\n"
        "v_mfma_f32_16x16x32_bf16 v[76:79], v[40:43], v[196:199], 0\n"
        "v_accvgpr_read_b32 v183, a199\n"
        "v_mul_f32 v177, s88, v177\n"
        "v_mfma_f32_16x16x32_bf16 v[76:79], v[44:47], v[212:215], v[76:79]\n"
        "v_mul_f32 v178, s88, v178\n"
        "v_mul_f32 v179, s88, v179\n"
        "v_mfma_f32_16x16x32_bf16 v[72:75], v[36:39], v[212:215], 0\n"
        "v_mul_f32 v180, s88, v180\n"
        "v_mul_f32 v181, s88, v181\n"
        "v_mfma_f32_16x16x32_bf16 v[72:75], v[32:35], v[196:199], v[72:75]\n"
        "v_mul_f32 v182, s88, v182\n"
        "v_mul_f32 v183, s88, v183\n"
        "v_mfma_f32_16x16x32_bf16 v[104:107], v[32:35], v[200:203], 0\n"
        "v_mfma_f32_16x16x32_bf16 v[104:107], v[36:39], v[216:219], v[104:107]\n"
        "v_mfma_f32_16x16x32_bf16 v[108:111], v[44:47], v[216:219], 0\n"
        "v_mfma_f32_16x16x32_bf16 v[108:111], v[40:43], v[200:203], v[108:111]\n"
        "s_waitcnt vmcnt(14) lgkmcnt(0)\n"
        "v_mfma_f32_16x16x32_bf16 v[140:143], v[40:43], v[204:207], 0\n"
        "s_barrier\n"
        "v_mfma_f32_16x16x32_bf16 v[140:143], v[44:47], v[220:223], v[140:143]\n"
        "ds_read_b64_tr_b16 v[44:45], v228 offset:57344\n"
        "global_load_lds_dwordx4 v238, s[68:69] offset:0\n"
        "v_mfma_f32_16x16x32_bf16 v[136:139], v[36:39], v[220:223], 0\n"
        "ds_read_b64_tr_b16 v[36:37], v226 offset:57344\n"
        "v_mfma_f32_16x16x32_bf16 v[136:139], v[32:35], v[204:207], v[136:139]\n"
        "ds_read_b64_tr_b16 v[32:33], v226 offset:49152\n"
        "v_accvgpr_read_b32 v184, a232\n"
        "v_mfma_f32_16x16x32_bf16 v[16:19], v[48:51], v[192:195], 0\n"
        "ds_read_b64_tr_b16 v[34:35], v227 offset:49152\n"
        "v_mul_f32 v184, s88, v184\n"
        "v_mfma_f32_16x16x32_bf16 v[16:19], v[52:55], v[208:211], v[16:19]\n"
        "ds_read_b64_tr_b16 v[38:39], v227 offset:57344\n"
        "v_cvt_pk_bf16_f32 v184, v184, v185\n"
        "v_mfma_f32_16x16x32_bf16 v[20:23], v[60:63], v[208:211], 0\n"
        "ds_read_b64_tr_b16 v[46:47], v229 offset:57344\n"
        "v_cvt_pk_bf16_f32 v185, v186, v187\n"
        "v_mfma_f32_16x16x32_bf16 v[20:23], v[56:59], v[192:195], v[20:23]\n"
        "ds_read_b64_tr_b16 v[40:41], v228 offset:49152\n"
        "v_cvt_pk_bf16_f32 v186, v188, v189\n"
        "v_mfma_f32_16x16x32_bf16 v[84:87], v[56:59], v[196:199], 0\n"
        "ds_read_b64_tr_b16 v[42:43], v229 offset:49152\n"
        "v_cvt_pk_bf16_f32 v187, v190, v191\n"
        "v_mfma_f32_16x16x32_bf16 v[84:87], v[60:63], v[212:215], v[84:87]\n"
        "global_load_lds_dwordx4 v239, s[68:69] offset:1024\n"
        "v_accvgpr_write_b32 a228, v184\n"
        "v_mfma_f32_16x16x32_bf16 v[80:83], v[52:55], v[212:215], 0\n"
        "v_accvgpr_write_b32 a229, v185\n"
        "v_accvgpr_write_b32 a230, v186\n"
        "v_mfma_f32_16x16x32_bf16 v[80:83], v[48:51], v[196:199], v[80:83]\n"
        "v_accvgpr_write_b32 a231, v187\n"
        "v_accvgpr_read_b32 v185, a201\n"
        "v_mfma_f32_16x16x32_bf16 v[112:115], v[48:51], v[200:203], 0\n"
        "v_accvgpr_read_b32 v186, a202\n"
        "v_accvgpr_read_b32 v187, a203\n"
        "v_mfma_f32_16x16x32_bf16 v[112:115], v[52:55], v[216:219], v[112:115]\n"
        "global_load_lds_dwordx4 v240, s[68:69] offset:2048\n"
        "v_accvgpr_read_b32 v188, a204\n"
        "v_mfma_f32_16x16x32_bf16 v[116:119], v[60:63], v[216:219], 0\n"
        "v_accvgpr_read_b32 v189, a205\n"
        "v_accvgpr_read_b32 v190, a206\n"
        "v_mfma_f32_16x16x32_bf16 v[116:119], v[56:59], v[200:203], v[116:119]\n"
        "v_accvgpr_read_b32 v191, a207\n"
        "v_mul_f32 v185, s88, v185\n"
        "v_mfma_f32_16x16x32_bf16 v[148:151], v[56:59], v[204:207], 0\n"
        "ds_read_b64_tr_b16 v[56:57], v232 offset:49152\n"
        "v_mul_f32 v186, s88, v186\n"
        "v_mfma_f32_16x16x32_bf16 v[148:151], v[60:63], v[220:223], v[148:151]\n"
        "global_store_dwordx4 v254, a[224:227], s[70:71] offset:0 sc1\n"
        "ds_read_b64_tr_b16 v[60:61], v232 offset:57344\n"
        "ds_read_b64_tr_b16 v[62:63], v233 offset:57344\n"
        "v_mul_f32 v187, s88, v187\n"
        "v_mfma_f32_16x16x32_bf16 v[144:147], v[52:55], v[220:223], 0\n"
        "ds_read_b64_tr_b16 v[52:53], v230 offset:57344\n"
        "v_mul_f32 v188, s88, v188\n"
        "v_mfma_f32_16x16x32_bf16 v[144:147], v[48:51], v[204:207], v[144:147]\n"
        "ds_read_b64_tr_b16 v[48:49], v230 offset:49152\n"
        "v_mul_f32 v189, s88, v189\n"
        "v_mfma_f32_16x16x32_bf16 v[24:27], v[160:163], v[192:195], 0\n"
        "ds_read_b64_tr_b16 v[50:51], v231 offset:49152\n"
        "global_load_lds_dwordx4 v241, s[68:69] offset:3072\n"
        "s_add_u32 m0, s72, 0x0\n"
        "v_mfma_f32_16x16x32_bf16 v[24:27], v[164:167], v[208:211], v[24:27]\n"
        "ds_read_b64_tr_b16 v[54:55], v231 offset:57344\n"
        "s_add_u32 s68, s68, 0x80000\n"
        "s_addc_u32 s69, s69, 0\n"
        "v_mfma_f32_16x16x32_bf16 v[28:31], v[172:175], v[208:211], 0\n"
        "ds_read_b64_tr_b16 v[58:59], v233 offset:49152\n"
        "v_mul_f32 v190, s88, v190\n"
        "v_mfma_f32_16x16x32_bf16 v[28:31], v[168:171], v[192:195], v[28:31]\n"
        "ds_read_b128 v[192:195], v248 offset:2048\n"
        "v_mul_f32 v191, s88, v191\n"
        "v_mfma_f32_16x16x32_bf16 v[92:95], v[168:171], v[196:199], 0\n"
        "ds_read_b128 v[208:211], v249 offset:2048\n"
        "global_load_lds_dwordx4 v242, s[64:65] offset:0\n"
        "v_mfma_f32_16x16x32_bf16 v[92:95], v[172:175], v[212:215], v[92:95]\n"
        "v_mfma_f32_16x16x32_bf16 v[88:91], v[164:167], v[212:215], 0\n"
        "ds_read_b128 v[212:215], v249 offset:0\n"
        "v_mfma_f32_16x16x32_bf16 v[88:91], v[160:163], v[196:199], v[88:91]\n"
        "ds_read_b128 v[196:199], v248 offset:0\n"
        "v_accvgpr_read_b32 v176, a192\n"
        "v_mfma_f32_16x16x32_bf16 v[120:123], v[160:163], v[200:203], 0\n"
        "global_load_lds_dwordx4 v243, s[64:65] offset:1024\n"
        "v_mfma_f32_16x16x32_bf16 v[120:123], v[164:167], v[216:219], v[120:123]\n"
        "v_mul_f32 v176, s88, v176\n"
        "v_cvt_pk_bf16_f32 v176, v176, v177\n"
        "v_mfma_f32_16x16x32_bf16 v[124:127], v[172:175], v[216:219], 0\n"
        "ds_read_b128 v[216:219], v249 offset:6144\n"
        "v_cvt_pk_bf16_f32 v177, v178, v179\n"
        "v_mfma_f32_16x16x32_bf16 v[124:127], v[168:171], v[200:203], v[124:127]\n"
        "ds_read_b128 v[200:203], v248 offset:6144\n"
        "v_cvt_pk_bf16_f32 v178, v180, v181\n"
        "v_mfma_f32_16x16x32_bf16 v[156:159], v[168:171], v[204:207], 0\n"
        "ds_read_b64_tr_b16 v[168:169], v228 offset:50176\n"
        "v_cvt_pk_bf16_f32 v179, v182, v183\n"
        "v_mfma_f32_16x16x32_bf16 v[156:159], v[172:175], v[220:223], v[156:159]\n"
        "ds_read_b64_tr_b16 v[172:173], v228 offset:58368\n"
        "v_accvgpr_write_b32 a232, v176\n"
        "v_mfma_f32_16x16x32_bf16 v[152:155], v[164:167], v[220:223], 0\n"
        "ds_read_b128 v[220:223], v249 offset:4096\n"
        "v_accvgpr_write_b32 a233, v177\n"
        "v_mfma_f32_16x16x32_bf16 v[152:155], v[160:163], v[204:207], v[152:155]\n"
        "ds_read_b128 v[204:207], v248 offset:4096\n"
        "s_waitcnt lgkmcnt(6)\n"
        "v_mfma_f32_16x16x32_bf16 v[0:3], v[32:35], v[192:195], v[0:3]\n"
        "ds_read_b64_tr_b16 v[160:161], v226 offset:50176\n"
        "v_accvgpr_write_b32 a234, v178\n"
        "v_mfma_f32_16x16x32_bf16 v[0:3], v[36:39], v[208:211], v[0:3]\n"
        "ds_read_b64_tr_b16 v[162:163], v227 offset:50176\n"
        "v_accvgpr_write_b32 a235, v179\n"
        "v_mfma_f32_16x16x32_bf16 v[4:7], v[44:47], v[208:211], v[4:7]\n"
        "ds_read_b64_tr_b16 v[164:165], v226 offset:58368\n"
        "global_load_lds_dwordx4 v244, s[64:65] offset:2048\n"
        "v_mfma_f32_16x16x32_bf16 v[4:7], v[40:43], v[192:195], v[4:7]\n"
        "ds_read_b64_tr_b16 v[166:167], v227 offset:58368\n"
        "v_accvgpr_read_b32 v177, a161\n"
        "v_mfma_f32_16x16x32_bf16 v[68:71], v[40:43], v[196:199], v[68:71]\n"
        "ds_read_b64_tr_b16 v[174:175], v229 offset:58368\n"
        "v_accvgpr_read_b32 v178, a162\n"
        "v_mfma_f32_16x16x32_bf16 v[68:71], v[44:47], v[212:215], v[68:71]\n"
        "ds_read_b64_tr_b16 v[170:171], v229 offset:50176\n"
        "v_accvgpr_read_b32 v179, a163\n"
        "v_mfma_f32_16x16x32_bf16 v[64:67], v[36:39], v[212:215], v[64:67]\n"
        "global_load_lds_dwordx4 v245, s[64:65] offset:3072\n"
        "s_add_u32 m0, s72, 0x1000\n"
        "v_mfma_f32_16x16x32_bf16 v[64:67], v[32:35], v[196:199], v[64:67]\n"
        "s_add_u32 s64, s64, 0x80\n"
        "s_addc_u32 s65, s65, 0\n"
        "s_waitcnt lgkmcnt(6)\n"
        "v_mfma_f32_16x16x32_bf16 v[96:99], v[32:35], v[200:203], v[96:99]\n"
        "v_accvgpr_read_b32 v180, a164\n"
        "v_accvgpr_read_b32 v181, a165\n"
        "v_mfma_f32_16x16x32_bf16 v[96:99], v[36:39], v[216:219], v[96:99]\n"
        "v_accvgpr_read_b32 v182, a166\n"
        "v_accvgpr_read_b32 v183, a167\n"
        "v_mfma_f32_16x16x32_bf16 v[100:103], v[44:47], v[216:219], v[100:103]\n"
        "global_load_lds_dwordx4 v242, s[66:67] offset:0\n"
        "v_mfma_f32_16x16x32_bf16 v[100:103], v[40:43], v[200:203], v[100:103]\n"
        "v_mul_f32 v177, s88, v177\n"
        "v_mul_f32 v178, s88, v178\n"
        "v_mfma_f32_16x16x32_bf16 v[132:135], v[40:43], v[204:207], v[132:135]\n"
        "ds_read_b64_tr_b16 v[40:41], v232 offset:50176\n"
        "v_mul_f32 v179, s88, v179\n"
        "v_mfma_f32_16x16x32_bf16 v[132:135], v[44:47], v[220:223], v[132:135]\n"
        "ds_read_b64_tr_b16 v[44:45], v232 offset:58368\n"
        "v_mul_f32 v180, s88, v180\n"
        "v_mfma_f32_16x16x32_bf16 v[128:131], v[36:39], v[220:223], v[128:131]\n"
        "ds_read_b64_tr_b16 v[36:37], v230 offset:58368\n"
        "global_load_lds_dwordx4 v243, s[66:67] offset:1024\n"
        "v_mfma_f32_16x16x32_bf16 v[128:131], v[32:35], v[204:207], v[128:131]\n"
        "ds_read_b64_tr_b16 v[32:33], v230 offset:50176\n"
        "v_accvgpr_read_b32 v184, a200\n"
        "v_mfma_f32_16x16x32_bf16 v[8:11], v[48:51], v[192:195], v[8:11]\n"
        "ds_read_b64_tr_b16 v[34:35], v231 offset:50176\n"
        "v_mul_f32 v184, s88, v184\n"
        "v_mfma_f32_16x16x32_bf16 v[8:11], v[52:55], v[208:211], v[8:11]\n"
        "ds_read_b64_tr_b16 v[38:39], v231 offset:58368\n"
        "v_cvt_pk_bf16_f32 v184, v184, v185\n"
        "v_mfma_f32_16x16x32_bf16 v[12:15], v[60:63], v[208:211], v[12:15]\n"
        "ds_read_b64_tr_b16 v[46:47], v233 offset:58368\n"
        "global_load_lds_dwordx4 v244, s[66:67] offset:2048\n"
        "v_mfma_f32_16x16x32_bf16 v[12:15], v[56:59], v[192:195], v[12:15]\n"
        "ds_read_b64_tr_b16 v[42:43], v233 offset:50176\n"
        "v_cvt_pk_bf16_f32 v185, v186, v187\n"
        "v_mfma_f32_16x16x32_bf16 v[76:79], v[56:59], v[196:199], v[76:79]\n"
        "v_cvt_pk_bf16_f32 v186, v188, v189\n"
        "v_cvt_pk_bf16_f32 v187, v190, v191\n"
        "v_mfma_f32_16x16x32_bf16 v[76:79], v[60:63], v[212:215], v[76:79]\n"
        "v_accvgpr_write_b32 a236, v184\n"
        "v_accvgpr_write_b32 a237, v185\n"
        "v_mfma_f32_16x16x32_bf16 v[72:75], v[52:55], v[212:215], v[72:75]\n"
        "global_load_lds_dwordx4 v245, s[66:67] offset:3072\n"
        "s_add_u32 m0, s73, 0x24000\n"
        "v_mfma_f32_16x16x32_bf16 v[72:75], v[48:51], v[196:199], v[72:75]\n"
        "s_add_u32 s66, s66, 0x80\n"
        "s_addc_u32 s67, s67, 0\n"
        "v_accvgpr_write_b32 a238, v186\n"
        "v_mfma_f32_16x16x32_bf16 v[104:107], v[48:51], v[200:203], v[104:107]\n"
        "v_accvgpr_write_b32 a239, v187\n"
        "v_mul_f32 v181, s88, v181\n"
        "v_mfma_f32_16x16x32_bf16 v[104:107], v[52:55], v[216:219], v[104:107]\n"
        "v_mul_f32 v182, s88, v182\n"
        "v_mul_f32 v183, s88, v183\n"
        "v_mfma_f32_16x16x32_bf16 v[108:111], v[60:63], v[216:219], v[108:111]\n"
        "v_accvgpr_read_b32 v185, a169\n"
        "v_accvgpr_read_b32 v186, a170\n"
        "v_mfma_f32_16x16x32_bf16 v[108:111], v[56:59], v[200:203], v[108:111]\n"
        "s_waitcnt vmcnt(13) lgkmcnt(0)\n"
        "v_accvgpr_read_b32 v187, a171\n"
        "v_accvgpr_read_b32 v188, a172\n"
        "v_mfma_f32_16x16x32_bf16 v[140:143], v[56:59], v[204:207], v[140:143]\n"
        "s_barrier\n"
        "v_mfma_f32_16x16x32_bf16 v[140:143], v[60:63], v[220:223], v[140:143]\n"
        "ds_read_b64_tr_b16 v[60:61], v228 offset:24576\n"
        "global_load_lds_dwordx4 v238, s[68:69] offset:0\n"
        "v_mfma_f32_16x16x32_bf16 v[136:139], v[52:55], v[220:223], v[136:139]\n"
        "ds_read_b64_tr_b16 v[52:53], v226 offset:24576\n"
        "v_accvgpr_read_b32 v189, a173\n"
        "v_mfma_f32_16x16x32_bf16 v[136:139], v[48:51], v[204:207], v[136:139]\n"
        "ds_read_b64_tr_b16 v[48:49], v226 offset:16384\n"
        "v_accvgpr_read_b32 v190, a174\n"
        "v_mfma_f32_16x16x32_bf16 v[16:19], v[160:163], v[192:195], v[16:19]\n"
        "ds_read_b64_tr_b16 v[50:51], v227 offset:16384\n"
        "v_accvgpr_read_b32 v191, a175\n"
        "v_mfma_f32_16x16x32_bf16 v[16:19], v[164:167], v[208:211], v[16:19]\n"
        "ds_read_b64_tr_b16 v[54:55], v227 offset:24576\n"
        "global_load_lds_dwordx4 v239, s[68:69] offset:1024\n"
        "v_mfma_f32_16x16x32_bf16 v[20:23], v[172:175], v[208:211], v[20:23]\n"
        "ds_read_b64_tr_b16 v[62:63], v229 offset:24576\n"
        "v_mul_f32 v185, s88, v185\n"
        "v_mfma_f32_16x16x32_bf16 v[20:23], v[168:171], v[192:195], v[20:23]\n"
        "ds_read_b64_tr_b16 v[56:57], v228 offset:16384\n"
        "v_mul_f32 v186, s88, v186\n"
        "v_mfma_f32_16x16x32_bf16 v[84:87], v[168:171], v[196:199], v[84:87]\n"
        "ds_read_b64_tr_b16 v[58:59], v229 offset:16384\n"
        "v_mul_f32 v187, s88, v187\n"
        "v_mfma_f32_16x16x32_bf16 v[84:87], v[172:175], v[212:215], v[84:87]\n"
        "global_load_lds_dwordx4 v240, s[68:69] offset:2048\n"
        "v_mfma_f32_16x16x32_bf16 v[80:83], v[164:167], v[212:215], v[80:83]\n"
        "v_mul_f32 v188, s88, v188\n"
        "v_mul_f32 v189, s88, v189\n"
        "v_mfma_f32_16x16x32_bf16 v[80:83], v[160:163], v[196:199], v[80:83]\n"
        "v_accvgpr_read_b32 v176, a160\n"
        "v_mul_f32 v176, s88, v176\n"
        "v_mfma_f32_16x16x32_bf16 v[112:115], v[160:163], v[200:203], v[112:115]\n"
        "v_cvt_pk_bf16_f32 v176, v176, v177\n"
        "v_cvt_pk_bf16_f32 v177, v178, v179\n"
        "v_mfma_f32_16x16x32_bf16 v[112:115], v[164:167], v[216:219], v[112:115]\n"
        "v_cvt_pk_bf16_f32 v178, v180, v181\n"
        "v_cvt_pk_bf16_f32 v179, v182, v183\n"
        "v_mfma_f32_16x16x32_bf16 v[116:119], v[172:175], v[216:219], v[116:119]\n"
        "global_load_lds_dwordx4 v241, s[68:69] offset:3072\n"
        "s_add_u32 m0, s72, 0x10000\n"
        "v_accvgpr_write_b32 a192, v176\n"
        "v_mfma_f32_16x16x32_bf16 v[116:119], v[168:171], v[200:203], v[116:119]\n"
        "s_add_u32 s68, s68, 0x80000\n"
        "s_addc_u32 s69, s69, 0\n"
        "v_accvgpr_write_b32 a193, v177\n"
        "v_mfma_f32_16x16x32_bf16 v[148:151], v[168:171], v[204:207], v[148:151]\n"
        "ds_read_b64_tr_b16 v[168:169], v232 offset:16384\n"
        "v_accvgpr_write_b32 a194, v178\n"
        "v_mfma_f32_16x16x32_bf16 v[148:151], v[172:175], v[220:223], v[148:151]\n"
        "ds_read_b64_tr_b16 v[172:173], v232 offset:24576\n"
        "v_accvgpr_write_b32 a195, v179\n"
        "v_mfma_f32_16x16x32_bf16 v[144:147], v[164:167], v[220:223], v[144:147]\n"
        "ds_read_b64_tr_b16 v[164:165], v230 offset:24576\n"
        "global_load_lds_dwordx4 v242, s[64:65] offset:0\n"
        "v_mfma_f32_16x16x32_bf16 v[144:147], v[160:163], v[204:207], v[144:147]\n"
        "ds_read_b64_tr_b16 v[160:161], v230 offset:16384\n"
        "v_mul_f32 v190, s88, v190\n"
        "v_mfma_f32_16x16x32_bf16 v[24:27], v[32:35], v[192:195], v[24:27]\n"
        "ds_read_b64_tr_b16 v[162:163], v231 offset:16384\n"
        "v_mul_f32 v191, s88, v191\n"
        "v_mfma_f32_16x16x32_bf16 v[24:27], v[36:39], v[208:211], v[24:27]\n"
        "ds_read_b64_tr_b16 v[166:167], v231 offset:24576\n"
        "v_accvgpr_read_b32 v177, a1\n"
        "v_mfma_f32_16x16x32_bf16 v[28:31], v[44:47], v[208:211], v[28:31]\n"
        "ds_read_b64_tr_b16 v[174:175], v233 offset:24576\n"
        "global_load_lds_dwordx4 v243, s[64:65] offset:1024\n"
        "v_mfma_f32_16x16x32_bf16 v[28:31], v[40:43], v[192:195], v[28:31]\n"
        "ds_read_b64_tr_b16 v[170:171], v233 offset:16384\n"
        "v_accvgpr_read_b32 v178, a2\n"
        "v_mfma_f32_16x16x32_bf16 v[92:95], v[40:43], v[196:199], v[92:95]\n"
        "ds_read_b128 v[192:195], v246 offset:34816\n"
        "v_accvgpr_read_b32 v179, a3\n"
        "v_mfma_f32_16x16x32_bf16 v[92:95], v[44:47], v[212:215], v[92:95]\n"
        "ds_read_b128 v[208:211], v247 offset:34816\n"
        "v_accvgpr_read_b32 v180, a4\n"
        "v_mfma_f32_16x16x32_bf16 v[88:91], v[36:39], v[212:215], v[88:91]\n"
        "ds_read_b128 v[212:215], v247 offset:32768\n"
        "global_load_lds_dwordx4 v244, s[64:65] offset:2048\n"
        "v_mfma_f32_16x16x32_bf16 v[88:91], v[32:35], v[196:199], v[88:91]\n"
        "ds_read_b128 v[196:199], v246 offset:32768\n"
        "v_accvgpr_read_b32 v181, a5\n"
        "v_mfma_f32_16x16x32_bf16 v[120:123], v[32:35], v[200:203], v[120:123]\n"
        "v_accvgpr_read_b32 v182, a6\n"
        "v_accvgpr_read_b32 v183, a7\n"
        "v_mfma_f32_16x16x32_bf16 v[120:123], v[36:39], v[216:219], v[120:123]\n"
        "v_mul_f32 v177, s88, v177\n"
        "v_mul_f32 v178, s88, v178\n"
        "v_mfma_f32_16x16x32_bf16 v[124:127], v[44:47], v[216:219], v[124:127]\n"
        "ds_read_b128 v[216:219], v247 offset:38912\n"
        "global_load_lds_dwordx4 v245, s[64:65] offset:3072\n"
        "s_add_u32 m0, s72, 0x11000\n"
        "v_mfma_f32_16x16x32_bf16 v[124:127], v[40:43], v[200:203], v[124:127]\n"
        "ds_read_b128 v[200:203], v246 offset:38912\n"
        "s_add_u32 s64, s64, 0x80\n"
        "s_addc_u32 s65, s65, 0\n"
        "v_mfma_f32_16x16x32_bf16 v[156:159], v[40:43], v[204:207], v[156:159]\n"
        "ds_read_b64_tr_b16 v[40:41], v228 offset:17408\n"
        "v_mul_f32 v179, s88, v179\n"
        "v_mfma_f32_16x16x32_bf16 v[156:159], v[44:47], v[220:223], v[156:159]\n"
        "ds_read_b64_tr_b16 v[44:45], v228 offset:25600\n"
        "v_mul_f32 v180, s88, v180\n"
        "v_mfma_f32_16x16x32_bf16 v[152:155], v[36:39], v[220:223], v[152:155]\n"
        "ds_read_b128 v[220:223], v247 offset:36864\n"
        "global_load_lds_dwordx4 v242, s[66:67] offset:0\n"
        "v_mfma_f32_16x16x32_bf16 v[152:155], v[32:35], v[204:207], v[152:155]\n"
        "ds_read_b128 v[204:207], v246 offset:36864\n"
        "s_waitcnt lgkmcnt(6)\n"
        "v_mfma_f32_16x16x32_bf16 v[0:3], v[48:51], v[192:195], v[0:3]\n"
        "ds_read_b64_tr_b16 v[32:33], v226 offset:17408\n"
        "v_accvgpr_read_b32 v184, a168\n"
        "v_mfma_f32_16x16x32_bf16 v[0:3], v[52:55], v[208:211], v[0:3]\n"
        "ds_read_b64_tr_b16 v[34:35], v227 offset:17408\n"
        "v_mul_f32 v184, s88, v184\n"
        "v_mfma_f32_16x16x32_bf16 v[4:7], v[60:63], v[208:211], v[4:7]\n"
        "ds_read_b64_tr_b16 v[36:37], v226 offset:25600\n"
        "v_cvt_pk_bf16_f32 v184, v184, v185\n"
        "v_mfma_f32_16x16x32_bf16 v[4:7], v[56:59], v[192:195], v[4:7]\n"
        "global_store_dwordx4 v254, a[228:231], s[70:71] offset:1024 sc1\n"
        "ds_read_b64_tr_b16 v[38:39], v227 offset:25600\n"
        "ds_read_b64_tr_b16 v[46:47], v229 offset:25600\n"
        "v_cvt_pk_bf16_f32 v185, v186, v187\n"
        "v_mfma_f32_16x16x32_bf16 v[68:71], v[56:59], v[196:199], v[68:71]\n"
        "ds_read_b64_tr_b16 v[42:43], v229 offset:17408\n"
        "v_cvt_pk_bf16_f32 v186, v188, v189\n"
        "v_mfma_f32_16x16x32_bf16 v[68:71], v[60:63], v[212:215], v[68:71]\n"
        "s_waitcnt lgkmcnt(8)\n"
        "v_cvt_pk_bf16_f32 v187, v190, v191\n"
        "v_accvgpr_write_b32 a196, v184\n"
        "v_mfma_f32_16x16x32_bf16 v[64:67], v[52:55], v[212:215], v[64:67]\n"
        "s_add_u32 s70, s38, s97\n"
        "s_addc_u32 s71, s39, 0\n"
        "v_accvgpr_write_b32 a197, v185\n"
        "v_mfma_f32_16x16x32_bf16 v[64:67], v[48:51], v[196:199], v[64:67]\n"
        "global_load_lds_dwordx4 v243, s[66:67] offset:1024\n"
        "s_add_u32 s97, s52, 0x1000\n"
        "v_accvgpr_write_b32 a198, v186\n"
        "v_mfma_f32_16x16x32_bf16 v[96:99], v[48:51], v[200:203], v[96:99]\n"
        "s_waitcnt lgkmcnt(5)\n"
        "v_accvgpr_write_b32 a199, v187\n"
        "v_mul_f32 v181, s88, v181\n"
        "v_mfma_f32_16x16x32_bf16 v[96:99], v[52:55], v[216:219], v[96:99]\n"
        "v_mul_f32 v182, s88, v182\n"
        "v_mul_f32 v183, s88, v183\n"
        "v_mfma_f32_16x16x32_bf16 v[100:103], v[60:63], v[216:219], v[100:103]\n"
        "v_accvgpr_read_b32 v185, a9\n"
        "v_accvgpr_read_b32 v186, a10\n"
        "v_mfma_f32_16x16x32_bf16 v[100:103], v[56:59], v[200:203], v[100:103]\n"
        "global_load_lds_dwordx4 v244, s[66:67] offset:2048\n"
        "v_accvgpr_read_b32 v187, a11\n"
        "v_mfma_f32_16x16x32_bf16 v[132:135], v[56:59], v[204:207], v[132:135]\n"
        "ds_read_b64_tr_b16 v[56:57], v232 offset:17408\n"
        "v_accvgpr_read_b32 v188, a12\n"
        "v_mfma_f32_16x16x32_bf16 v[132:135], v[60:63], v[220:223], v[132:135]\n"
        "ds_read_b64_tr_b16 v[60:61], v232 offset:25600\n"
        "v_accvgpr_read_b32 v189, a13\n"
        "v_mfma_f32_16x16x32_bf16 v[128:131], v[52:55], v[220:223], v[128:131]\n"
        "ds_read_b64_tr_b16 v[52:53], v230 offset:25600\n"
        "v_accvgpr_read_b32 v190, a14\n"
        "v_mfma_f32_16x16x32_bf16 v[128:131], v[48:51], v[204:207], v[128:131]\n"
        "ds_read_b64_tr_b16 v[48:49], v230 offset:17408\n"
        "global_load_lds_dwordx4 v245, s[66:67] offset:3072\n"
        "s_add_u32 m0, s73, 0x1c000\n"
        "v_mfma_f32_16x16x32_bf16 v[8:11], v[160:163], v[192:195], v[8:11]\n"
        "ds_read_b64_tr_b16 v[50:51], v231 offset:17408\n"
        "s_add_u32 s66, s66, 0x80\n"
        "s_addc_u32 s67, s67, 0\n"
        "v_mfma_f32_16x16x32_bf16 v[8:11], v[164:167], v[208:211], v[8:11]\n"
        "ds_read_b64_tr_b16 v[54:55], v231 offset:25600\n"
        "v_accvgpr_read_b32 v191, a15\n"
        "v_mfma_f32_16x16x32_bf16 v[12:15], v[172:175], v[208:211], v[12:15]\n"
        "ds_read_b64_tr_b16 v[62:63], v233 offset:25600\n"
        "v_mul_f32 v185, s88, v185\n"
        "v_mfma_f32_16x16x32_bf16 v[12:15], v[168:171], v[192:195], v[12:15]\n"
        "ds_read_b64_tr_b16 v[58:59], v233 offset:17408\n"
        "v_mul_f32 v186, s88, v186\n"
        "v_mfma_f32_16x16x32_bf16 v[76:79], v[168:171], v[196:199], v[76:79]\n"
        "v_mul_f32 v187, s88, v187\n"
        "v_mul_f32 v188, s88, v188\n"
        "v_mfma_f32_16x16x32_bf16 v[76:79], v[172:175], v[212:215], v[76:79]\n"
        "v_mul_f32 v189, s88, v189\n"
        "v_mul_f32 v190, s88, v190\n"
        "v_mfma_f32_16x16x32_bf16 v[72:75], v[164:167], v[212:215], v[72:75]\n"
        "v_mul_f32 v191, s88, v191\n"
        "v_mfma_f32_16x16x32_bf16 v[72:75], v[160:163], v[196:199], v[72:75]\n"
        "v_accvgpr_read_b32 v176, a0\n"
        "v_mul_f32 v176, s88, v176\n"
        "v_mfma_f32_16x16x32_bf16 v[104:107], v[160:163], v[200:203], v[104:107]\n"
        "v_cvt_pk_bf16_f32 v176, v176, v177\n"
        "v_cvt_pk_bf16_f32 v177, v178, v179\n"
        "v_mfma_f32_16x16x32_bf16 v[104:107], v[164:167], v[216:219], v[104:107]\n"
        "v_cvt_pk_bf16_f32 v178, v180, v181\n"
        "v_cvt_pk_bf16_f32 v179, v182, v183\n"
        "v_mfma_f32_16x16x32_bf16 v[108:111], v[172:175], v[216:219], v[108:111]\n"
        "v_accvgpr_write_b32 a200, v176\n"
        "v_accvgpr_write_b32 a201, v177\n"
        "v_mfma_f32_16x16x32_bf16 v[108:111], v[168:171], v[200:203], v[108:111]\n"
        "s_waitcnt vmcnt(13) lgkmcnt(0)\n"
        "v_accvgpr_write_b32 a202, v178\n"
        "v_accvgpr_write_b32 a203, v179\n"
        "v_mfma_f32_16x16x32_bf16 v[140:143], v[168:171], v[204:207], v[140:143]\n"
        "s_barrier\n"
        "v_mfma_f32_16x16x32_bf16 v[140:143], v[172:175], v[220:223], v[140:143]\n"
        "ds_read_b64_tr_b16 v[172:173], v228 offset:40960\n"
        "global_load_lds_dwordx4 v238, s[68:69] offset:0\n"
        "v_mfma_f32_16x16x32_bf16 v[136:139], v[164:167], v[220:223], v[136:139]\n"
        "ds_read_b64_tr_b16 v[164:165], v226 offset:40960\n"
        "v_accvgpr_read_b32 v177, a33\n"
        "v_mfma_f32_16x16x32_bf16 v[136:139], v[160:163], v[204:207], v[136:139]\n"
        "ds_read_b64_tr_b16 v[160:161], v226 offset:32768\n"
        "v_accvgpr_read_b32 v178, a34\n"
        "v_mfma_f32_16x16x32_bf16 v[16:19], v[32:35], v[192:195], v[16:19]\n"
        "ds_read_b64_tr_b16 v[162:163], v227 offset:32768\n"
        "v_accvgpr_read_b32 v179, a35\n"
        "v_mfma_f32_16x16x32_bf16 v[16:19], v[36:39], v[208:211], v[16:19]\n"
        "ds_read_b64_tr_b16 v[166:167], v227 offset:40960\n"
        "global_load_lds_dwordx4 v239, s[68:69] offset:1024\n"
        "v_mfma_f32_16x16x32_bf16 v[20:23], v[44:47], v[208:211], v[20:23]\n"
        "ds_read_b64_tr_b16 v[174:175], v229 offset:40960\n"
        "v_accvgpr_read_b32 v180, a36\n"
        "v_mfma_f32_16x16x32_bf16 v[20:23], v[40:43], v[192:195], v[20:23]\n"
        "ds_read_b64_tr_b16 v[168:169], v228 offset:32768\n"
        "v_accvgpr_read_b32 v181, a37\n"
        "v_mfma_f32_16x16x32_bf16 v[84:87], v[40:43], v[196:199], v[84:87]\n"
        "ds_read_b64_tr_b16 v[170:171], v229 offset:32768\n"
        "v_accvgpr_read_b32 v182, a38\n"
        "v_mfma_f32_16x16x32_bf16 v[84:87], v[44:47], v[212:215], v[84:87]\n"
        "global_load_lds_dwordx4 v240, s[68:69] offset:2048\n"
        "v_mfma_f32_16x16x32_bf16 v[80:83], v[36:39], v[212:215], v[80:83]\n"
        "v_accvgpr_read_b32 v183, a39\n"
        "v_mul_f32 v177, s88, v177\n"
        "v_mfma_f32_16x16x32_bf16 v[80:83], v[32:35], v[196:199], v[80:83]\n"
        "v_mul_f32 v178, s88, v178\n"
        "v_mul_f32 v179, s88, v179\n"
        "v_mfma_f32_16x16x32_bf16 v[112:115], v[32:35], v[200:203], v[112:115]\n"
        "v_mul_f32 v180, s88, v180\n"
        "v_mul_f32 v181, s88, v181\n"
        "v_mfma_f32_16x16x32_bf16 v[112:115], v[36:39], v[216:219], v[112:115]\n"
        "global_load_lds_dwordx4 v241, s[68:69] offset:3072\n"
        "s_add_u32 m0, s72, 0x8000\n"
        "v_mfma_f32_16x16x32_bf16 v[116:119], v[44:47], v[216:219], v[116:119]\n"
        "s_add_u32 s68, s68, 0x80000\n"
        "s_addc_u32 s69, s69, 0\n"
        "v_mul_f32 v182, s88, v182\n"
        "v_mfma_f32_16x16x32_bf16 v[116:119], v[40:43], v[200:203], v[116:119]\n"
        "v_mul_f32 v183, s88, v183\n"
        "v_mfma_f32_16x16x32_bf16 v[148:151], v[40:43], v[204:207], v[148:151]\n"
        "ds_read_b64_tr_b16 v[40:41], v232 offset:32768\n"
        "v_mfma_f32_16x16x32_bf16 v[148:151], v[44:47], v[220:223], v[148:151]\n"
        "ds_read_b64_tr_b16 v[44:45], v232 offset:40960\n"
        "global_load_lds_dwordx4 v242, s[64:65] offset:0\n"
        "v_mfma_f32_16x16x32_bf16 v[144:147], v[36:39], v[220:223], v[144:147]\n"
        "ds_read_b64_tr_b16 v[36:37], v230 offset:40960\n"
        "v_mfma_f32_16x16x32_bf16 v[144:147], v[32:35], v[204:207], v[144:147]\n"
        "ds_read_b64_tr_b16 v[32:33], v230 offset:32768\n"
        "v_accvgpr_read_b32 v184, a8\n"
        "v_mfma_f32_16x16x32_bf16 v[24:27], v[48:51], v[192:195], v[24:27]\n"
        "ds_read_b64_tr_b16 v[34:35], v231 offset:32768\n"
        "v_mul_f32 v184, s88, v184\n"
        "v_mfma_f32_16x16x32_bf16 v[24:27], v[52:55], v[208:211], v[24:27]\n"
        "ds_read_b64_tr_b16 v[38:39], v231 offset:40960\n"
        "v_cvt_pk_bf16_f32 v184, v184, v185\n"
        "v_mfma_f32_16x16x32_bf16 v[28:31], v[60:63], v[208:211], v[28:31]\n"
        "ds_read_b64_tr_b16 v[46:47], v233 offset:40960\n"
        "v_cvt_pk_bf16_f32 v185, v186, v187\n"
        "v_mfma_f32_16x16x32_bf16 v[28:31], v[56:59], v[192:195], v[28:31]\n"
        "ds_read_b64_tr_b16 v[42:43], v233 offset:32768\n"
        "v_cvt_pk_bf16_f32 v186, v188, v189\n"
        "v_mfma_f32_16x16x32_bf16 v[92:95], v[56:59], v[196:199], v[92:95]\n"
        "ds_read_b128 v[192:195], v246 offset:2048\n"
        "v_cvt_pk_bf16_f32 v187, v190, v191\n"
        "v_mfma_f32_16x16x32_bf16 v[92:95], v[60:63], v[212:215], v[92:95]\n"
        "ds_read_b128 v[208:211], v247 offset:2048\n"
        "v_accvgpr_write_b32 a204, v184\n"
        "v_mfma_f32_16x16x32_bf16 v[88:91], v[52:55], v[212:215], v[88:91]\n"
        "ds_read_b128 v[212:215], v247 offset:0\n"
        "v_accvgpr_write_b32 a205, v185\n"
        "v_mfma_f32_16x16x32_bf16 v[88:91], v[48:51], v[196:199], v[88:91]\n"
        "ds_read_b128 v[196:199], v246 offset:0\n"
        "v_accvgpr_write_b32 a206, v186\n"
        "v_mfma_f32_16x16x32_bf16 v[120:123], v[48:51], v[200:203], v[120:123]\n"
        "global_load_lds_dwordx4 v243, s[64:65] offset:1024\n"
        "v_accvgpr_write_b32 a207, v187\n"
        "v_mfma_f32_16x16x32_bf16 v[120:123], v[52:55], v[216:219], v[120:123]\n"
        "v_accvgpr_read_b32 v185, a41\n"
        "v_accvgpr_read_b32 v186, a42\n"
        "v_mfma_f32_16x16x32_bf16 v[124:127], v[60:63], v[216:219], v[124:127]\n"
        "ds_read_b128 v[216:219], v247 offset:6144\n"
        "v_accvgpr_read_b32 v187, a43\n"
        "v_mfma_f32_16x16x32_bf16 v[124:127], v[56:59], v[200:203], v[124:127]\n"
        "ds_read_b128 v[200:203], v246 offset:6144\n"
        "v_accvgpr_read_b32 v188, a44\n"
        "v_mfma_f32_16x16x32_bf16 v[156:159], v[56:59], v[204:207], v[156:159]\n"
        "global_store_dwordx4 v254, a[232:235], s[70:71] offset:0 sc1\n"
        "ds_read_b64_tr_b16 v[56:57], v228 offset:33792\n"
        "ds_read_b64_tr_b16 v[58:59], v229 offset:33792\n"
        "v_accvgpr_read_b32 v189, a45\n"
        "LHA5_%=:\n"
        "s_cbranch_execz LHA9_%=\n"
        "v_mfma_f32_16x16x32_bf16 v[156:159], v[60:63], v[220:223], v[156:159]\n"
        "ds_read_b64_tr_b16 v[60:61], v228 offset:41984\n"
        "v_accvgpr_read_b32 v190, a46\n"
        "v_mfma_f32_16x16x32_bf16 v[152:155], v[52:55], v[220:223], v[152:155]\n"
        "ds_read_b128 v[220:223], v247 offset:4096\n"
        "v_accvgpr_read_b32 v191, a47\n"
        "v_mfma_f32_16x16x32_bf16 v[152:155], v[48:51], v[204:207], v[152:155]\n"
        "ds_read_b128 v[204:207], v246 offset:4096\n"
        "s_waitcnt lgkmcnt(7)\n"
        "v_mfma_f32_16x16x32_bf16 v[0:3], v[160:163], v[192:195], v[0:3]\n"
        "ds_read_b64_tr_b16 v[48:49], v226 offset:33792\n"
        "global_load_lds_dwordx4 v244, s[64:65] offset:2048\n"
        "v_mfma_f32_16x16x32_bf16 v[0:3], v[164:167], v[208:211], v[0:3]\n"
        "ds_read_b64_tr_b16 v[50:51], v227 offset:33792\n"
        "v_mul_f32 v185, s88, v185\n"
        "v_mfma_f32_16x16x32_bf16 v[4:7], v[172:175], v[208:211], v[4:7]\n"
        "ds_read_b64_tr_b16 v[52:53], v226 offset:41984\n"
        "v_mul_f32 v186, s88, v186\n"
        "v_mfma_f32_16x16x32_bf16 v[4:7], v[168:171], v[192:195], v[4:7]\n"
        "ds_read_b64_tr_b16 v[54:55], v227 offset:41984\n"
        "v_mul_f32 v187, s88, v187\n"
        "v_mfma_f32_16x16x32_bf16 v[68:71], v[168:171], v[196:199], v[68:71]\n"
        "ds_read_b64_tr_b16 v[62:63], v229 offset:41984\n"
        "global_load_lds_dwordx4 v245, s[64:65] offset:3072\n"
        "s_add_u32 m0, s72, 0x9000\n"
        "v_mfma_f32_16x16x32_bf16 v[68:71], v[172:175], v[212:215], v[68:71]\n"
        "s_add_u32 s64, s64, 0x80\n"
        "s_addc_u32 s65, s65, 0\n"
        "v_mul_f32 v188, s88, v188\n"
        "v_mfma_f32_16x16x32_bf16 v[64:67], v[164:167], v[212:215], v[64:67]\n"
        "s_waitcnt lgkmcnt(6)\n"
        "v_mul_f32 v189, s88, v189\n"
        "v_mul_f32 v190, s88, v190\n"
        "v_mfma_f32_16x16x32_bf16 v[64:67], v[160:163], v[196:199], v[64:67]\n"
        "v_accvgpr_read_b32 v176, a32\n"
        "v_mul_f32 v176, s88, v176\n"
        "v_mfma_f32_16x16x32_bf16 v[96:99], v[160:163], v[200:203], v[96:99]\n"
        "global_load_lds_dwordx4 v242, s[66:67] offset:0\n"
        "v_mfma_f32_16x16x32_bf16 v[96:99], v[164:167], v[216:219], v[96:99]\n"
        "s_waitcnt lgkmcnt(3)\n"
        "v_cvt_pk_bf16_f32 v176, v176, v177\n"
        "v_cvt_pk_bf16_f32 v177, v178, v179\n"
        "v_mfma_f32_16x16x32_bf16 v[100:103], v[172:175], v[216:219], v[100:103]\n"
        "v_cvt_pk_bf16_f32 v178, v180, v181\n"
        "v_cvt_pk_bf16_f32 v179, v182, v183\n"
        "v_mfma_f32_16x16x32_bf16 v[100:103], v[168:171], v[200:203], v[100:103]\n"
        "v_accvgpr_write_b32 a160, v176\n"
        "v_accvgpr_write_b32 a161, v177\n"
        "v_mfma_f32_16x16x32_bf16 v[132:135], v[168:171], v[204:207], v[132:135]\n"
        "ds_read_b64_tr_b16 v[168:169], v232 offset:33792\n"
        "global_load_lds_dwordx4 v243, s[66:67] offset:1024\n"
        "v_mfma_f32_16x16x32_bf16 v[132:135], v[172:175], v[220:223], v[132:135]\n"
        "ds_read_b64_tr_b16 v[172:173], v232 offset:41984\n"
        "v_accvgpr_write_b32 a162, v178\n"
        "v_mfma_f32_16x16x32_bf16 v[128:131], v[164:167], v[220:223], v[128:131]\n"
        "ds_read_b64_tr_b16 v[164:165], v230 offset:41984\n"
        "v_accvgpr_write_b32 a163, v179\n"
        "v_mfma_f32_16x16x32_bf16 v[128:131], v[160:163], v[204:207], v[128:131]\n"
        "ds_read_b64_tr_b16 v[160:161], v230 offset:33792\n"
        "v_mul_f32 v191, s88, v191\n"
        "v_mfma_f32_16x16x32_bf16 v[8:11], v[32:35], v[192:195], v[8:11]\n"
        "ds_read_b64_tr_b16 v[162:163], v231 offset:33792\n"
        "global_load_lds_dwordx4 v244, s[66:67] offset:2048\n"
        "v_mfma_f32_16x16x32_bf16 v[8:11], v[36:39], v[208:211], v[8:11]\n"
        "ds_read_b64_tr_b16 v[166:167], v231 offset:41984\n"
        "v_accvgpr_read_b32 v177, a65\n"
        "v_mfma_f32_16x16x32_bf16 v[12:15], v[44:47], v[208:211], v[12:15]\n"
        "ds_read_b64_tr_b16 v[174:175], v233 offset:41984\n"
        "v_accvgpr_read_b32 v178, a66\n"
        "v_mfma_f32_16x16x32_bf16 v[12:15], v[40:43], v[192:195], v[12:15]\n"
        "ds_read_b64_tr_b16 v[170:171], v233 offset:33792\n"
        "v_accvgpr_read_b32 v179, a67\n"
        "v_mfma_f32_16x16x32_bf16 v[76:79], v[40:43], v[196:199], v[76:79]\n"
        "global_load_lds_dwordx4 v245, s[66:67] offset:3072\n"
        "s_add_u32 m0, s73, 0x20000\n"
        "v_mfma_f32_16x16x32_bf16 v[76:79], v[44:47], v[212:215], v[76:79]\n"
        "s_add_u32 s66, s66, 0x80\n"
        "s_addc_u32 s67, s67, 0\n"
        "v_accvgpr_read_b32 v180, a68\n"
        "v_mfma_f32_16x16x32_bf16 v[72:75], v[36:39], v[212:215], v[72:75]\n"
        "v_accvgpr_read_b32 v181, a69\n"
        "v_accvgpr_read_b32 v182, a70\n"
        "v_mfma_f32_16x16x32_bf16 v[72:75], v[32:35], v[196:199], v[72:75]\n"
        "v_accvgpr_read_b32 v183, a71\n"
        "v_mul_f32 v177, s88, v177\n"
        "v_mfma_f32_16x16x32_bf16 v[104:107], v[32:35], v[200:203], v[104:107]\n"
        "v_mul_f32 v178, s88, v178\n"
        "v_mul_f32 v179, s88, v179\n"
        "v_mfma_f32_16x16x32_bf16 v[104:107], v[36:39], v[216:219], v[104:107]\n"
        "v_mul_f32 v180, s88, v180\n"
        "v_mul_f32 v181, s88, v181\n"
        "v_mfma_f32_16x16x32_bf16 v[108:111], v[44:47], v[216:219], v[108:111]\n"
        "v_mul_f32 v182, s88, v182\n"
        "v_mul_f32 v183, s88, v183\n"
        "v_mfma_f32_16x16x32_bf16 v[108:111], v[40:43], v[200:203], v[108:111]\n"
        "s_waitcnt vmcnt(13) lgkmcnt(0)\n"
        "v_mfma_f32_16x16x32_bf16 v[140:143], v[40:43], v[204:207], v[140:143]\n"
        "s_barrier\n"
        "v_mfma_f32_16x16x32_bf16 v[140:143], v[44:47], v[220:223], v[140:143]\n"
        "ds_read_b64_tr_b16 v[44:45], v228 offset:57344\n"
        "global_load_lds_dwordx4 v238, s[68:69] offset:0\n"
        "v_mfma_f32_16x16x32_bf16 v[136:139], v[36:39], v[220:223], v[136:139]\n"
        "ds_read_b64_tr_b16 v[36:37], v226 offset:57344\n"
        "v_mfma_f32_16x16x32_bf16 v[136:139], v[32:35], v[204:207], v[136:139]\n"
        "ds_read_b64_tr_b16 v[32:33], v226 offset:49152\n"
        "v_accvgpr_read_b32 v184, a40\n"
        "v_mfma_f32_16x16x32_bf16 v[16:19], v[48:51], v[192:195], v[16:19]\n"
        "ds_read_b64_tr_b16 v[34:35], v227 offset:49152\n"
        "v_mul_f32 v184, s88, v184\n"
        "v_mfma_f32_16x16x32_bf16 v[16:19], v[52:55], v[208:211], v[16:19]\n"
        "ds_read_b64_tr_b16 v[38:39], v227 offset:57344\n"
        "v_cvt_pk_bf16_f32 v184, v184, v185\n"
        "v_mfma_f32_16x16x32_bf16 v[20:23], v[60:63], v[208:211], v[20:23]\n"
        "ds_read_b64_tr_b16 v[46:47], v229 offset:57344\n"
        "v_cvt_pk_bf16_f32 v185, v186, v187\n"
        "v_mfma_f32_16x16x32_bf16 v[20:23], v[56:59], v[192:195], v[20:23]\n"
        "ds_read_b64_tr_b16 v[40:41], v228 offset:49152\n"
        "v_cvt_pk_bf16_f32 v186, v188, v189\n"
        "v_mfma_f32_16x16x32_bf16 v[84:87], v[56:59], v[196:199], v[84:87]\n"
        "ds_read_b64_tr_b16 v[42:43], v229 offset:49152\n"
        "v_cvt_pk_bf16_f32 v187, v190, v191\n"
        "v_mfma_f32_16x16x32_bf16 v[84:87], v[60:63], v[212:215], v[84:87]\n"
        "global_load_lds_dwordx4 v239, s[68:69] offset:1024\n"
        "v_accvgpr_write_b32 a164, v184\n"
        "v_mfma_f32_16x16x32_bf16 v[80:83], v[52:55], v[212:215], v[80:83]\n"
        "v_accvgpr_write_b32 a165, v185\n"
        "v_accvgpr_write_b32 a166, v186\n"
        "v_mfma_f32_16x16x32_bf16 v[80:83], v[48:51], v[196:199], v[80:83]\n"
        "v_accvgpr_write_b32 a167, v187\n"
        "v_accvgpr_read_b32 v185, a73\n"
        "v_mfma_f32_16x16x32_bf16 v[112:115], v[48:51], v[200:203], v[112:115]\n"
        "v_accvgpr_read_b32 v186, a74\n"
        "v_accvgpr_read_b32 v187, a75\n"
        "v_mfma_f32_16x16x32_bf16 v[112:115], v[52:55], v[216:219], v[112:115]\n"
        "global_load_lds_dwordx4 v240, s[68:69] offset:2048\n"
        "v_accvgpr_read_b32 v188, a76\n"
        "v_mfma_f32_16x16x32_bf16 v[116:119], v[60:63], v[216:219], v[116:119]\n"
        "v_accvgpr_read_b32 v189, a77\n"
        "v_accvgpr_read_b32 v190, a78\n"
        "v_mfma_f32_16x16x32_bf16 v[116:119], v[56:59], v[200:203], v[116:119]\n"
        "v_accvgpr_read_b32 v191, a79\n"
        "v_mul_f32 v185, s88, v185\n"
        "v_mfma_f32_16x16x32_bf16 v[148:151], v[56:59], v[204:207], v[148:151]\n"
        "ds_read_b64_tr_b16 v[56:57], v232 offset:49152\n"
        "v_mul_f32 v186, s88, v186\n"
        "v_mfma_f32_16x16x32_bf16 v[148:151], v[60:63], v[220:223], v[148:151]\n"
        "ds_read_b64_tr_b16 v[60:61], v232 offset:57344\n"
        "global_load_lds_dwordx4 v241, s[68:69] offset:3072\n"
        "s_add_u32 m0, s72, 0x0\n"
        "v_mfma_f32_16x16x32_bf16 v[144:147], v[52:55], v[220:223], v[144:147]\n"
        "ds_read_b64_tr_b16 v[52:53], v230 offset:57344\n"
        "s_add_u32 s68, s68, 0x80000\n"
        "s_addc_u32 s69, s69, 0\n"
        "v_mfma_f32_16x16x32_bf16 v[144:147], v[48:51], v[204:207], v[144:147]\n"
        "ds_read_b64_tr_b16 v[48:49], v230 offset:49152\n"
        "v_mul_f32 v187, s88, v187\n"
        "v_mfma_f32_16x16x32_bf16 v[24:27], v[160:163], v[192:195], v[24:27]\n"
        "ds_read_b64_tr_b16 v[50:51], v231 offset:49152\n"
        "v_mul_f32 v188, s88, v188\n"
        "v_mfma_f32_16x16x32_bf16 v[24:27], v[164:167], v[208:211], v[24:27]\n"
        "ds_read_b64_tr_b16 v[54:55], v231 offset:57344\n"
        "global_load_lds_dwordx4 v242, s[64:65] offset:0\n"
        "v_mfma_f32_16x16x32_bf16 v[28:31], v[172:175], v[208:211], v[28:31]\n"
        "ds_read_b64_tr_b16 v[62:63], v233 offset:57344\n"
        "v_mul_f32 v189, s88, v189\n"
        "v_mfma_f32_16x16x32_bf16 v[28:31], v[168:171], v[192:195], v[28:31]\n"
        "ds_read_b64_tr_b16 v[58:59], v233 offset:49152\n"
        "v_mul_f32 v190, s88, v190\n"
        "v_mfma_f32_16x16x32_bf16 v[92:95], v[168:171], v[196:199], v[92:95]\n"
        "ds_read_b128 v[192:195], v248 offset:2048\n"
        "v_mul_f32 v191, s88, v191\n"
        "v_mfma_f32_16x16x32_bf16 v[92:95], v[172:175], v[212:215], v[92:95]\n"
        "ds_read_b128 v[208:211], v249 offset:2048\n"
        "global_load_lds_dwordx4 v243, s[64:65] offset:1024\n"
        "v_mfma_f32_16x16x32_bf16 v[88:91], v[164:167], v[212:215], v[88:91]\n"
        "ds_read_b128 v[212:215], v249 offset:0\n"
        "v_mfma_f32_16x16x32_bf16 v[88:91], v[160:163], v[196:199], v[88:91]\n"
        "ds_read_b128 v[196:199], v248 offset:0\n"
        "v_accvgpr_read_b32 v176, a64\n"
        "v_mfma_f32_16x16x32_bf16 v[120:123], v[160:163], v[200:203], v[120:123]\n"
        "v_mul_f32 v176, s88, v176\n"
        "v_cvt_pk_bf16_f32 v176, v176, v177\n"
        "v_mfma_f32_16x16x32_bf16 v[120:123], v[164:167], v[216:219], v[120:123]\n"
        "v_cvt_pk_bf16_f32 v177, v178, v179\n"
        "v_cvt_pk_bf16_f32 v178, v180, v181\n"
        "v_mfma_f32_16x16x32_bf16 v[124:127], v[172:175], v[216:219], v[124:127]\n"
        "ds_read_b128 v[216:219], v249 offset:6144\n"
        "v_cvt_pk_bf16_f32 v179, v182, v183\n"
        "v_mfma_f32_16x16x32_bf16 v[124:127], v[168:171], v[200:203], v[124:127]\n"
        "ds_read_b128 v[200:203], v248 offset:6144\n"
        "v_accvgpr_write_b32 a168, v176\n"
        "v_mfma_f32_16x16x32_bf16 v[156:159], v[168:171], v[204:207], v[156:159]\n"
        "ds_read_b64_tr_b16 v[168:169], v228 offset:50176\n"
        "v_accvgpr_write_b32 a169, v177\n"
        "v_mfma_f32_16x16x32_bf16 v[156:159], v[172:175], v[220:223], v[156:159]\n"
        "ds_read_b64_tr_b16 v[172:173], v228 offset:58368\n"
        "v_accvgpr_write_b32 a170, v178\n"
        "v_mfma_f32_16x16x32_bf16 v[152:155], v[164:167], v[220:223], v[152:155]\n"
        "ds_read_b128 v[220:223], v249 offset:4096\n"
        "v_accvgpr_write_b32 a171, v179\n"
        "v_mfma_f32_16x16x32_bf16 v[152:155], v[160:163], v[204:207], v[152:155]\n"
        "ds_read_b128 v[204:207], v248 offset:4096\n"
        "s_waitcnt lgkmcnt(6)\n"
        "v_mfma_f32_16x16x32_bf16 v[0:3], v[32:35], v[192:195], v[0:3]\n"
        "ds_read_b64_tr_b16 v[160:161], v226 offset:50176\n"
        "global_load_lds_dwordx4 v244, s[64:65] offset:2048\n"
        "v_mfma_f32_16x16x32_bf16 v[0:3], v[36:39], v[208:211], v[0:3]\n"
        "ds_read_b64_tr_b16 v[162:163], v227 offset:50176\n"
        "v_accvgpr_read_b32 v177, a97\n"
        "v_mfma_f32_16x16x32_bf16 v[4:7], v[44:47], v[208:211], v[4:7]\n"
        "ds_read_b64_tr_b16 v[164:165], v226 offset:58368\n"
        "v_accvgpr_read_b32 v178, a98\n"
        "v_mfma_f32_16x16x32_bf16 v[4:7], v[40:43], v[192:195], v[4:7]\n"
        "ds_read_b64_tr_b16 v[166:167], v227 offset:58368\n"
        "v_accvgpr_read_b32 v179, a99\n"
        "v_mfma_f32_16x16x32_bf16 v[68:71], v[40:43], v[196:199], v[68:71]\n"
        "ds_read_b64_tr_b16 v[174:175], v229 offset:58368\n"
        "global_load_lds_dwordx4 v245, s[64:65] offset:3072\n"
        "s_add_u32 m0, s72, 0x1000\n"
        "v_mfma_f32_16x16x32_bf16 v[68:71], v[44:47], v[212:215], v[68:71]\n"
        "ds_read_b64_tr_b16 v[170:171], v229 offset:50176\n"
        "s_add_u32 s64, s64, 0x80\n"
        "s_addc_u32 s65, s65, 0\n"
        "v_mfma_f32_16x16x32_bf16 v[64:67], v[36:39], v[212:215], v[64:67]\n"
        "s_waitcnt lgkmcnt(7)\n"
        "v_accvgpr_read_b32 v180, a100\n"
        "v_accvgpr_read_b32 v181, a101\n"
        "v_mfma_f32_16x16x32_bf16 v[64:67], v[32:35], v[196:199], v[64:67]\n"
        "v_accvgpr_read_b32 v182, a102\n"
        "v_accvgpr_read_b32 v183, a103\n"
        "v_mfma_f32_16x16x32_bf16 v[96:99], v[32:35], v[200:203], v[96:99]\n"
        "global_load_lds_dwordx4 v242, s[66:67] offset:0\n"
        "v_mfma_f32_16x16x32_bf16 v[96:99], v[36:39], v[216:219], v[96:99]\n"
        "s_waitcnt lgkmcnt(4)\n"
        "v_mul_f32 v177, s88, v177\n"
        "v_mul_f32 v178, s88, v178\n"
        "v_mfma_f32_16x16x32_bf16 v[100:103], v[44:47], v[216:219], v[100:103]\n"
        "v_mul_f32 v179, s88, v179\n"
        "v_mul_f32 v180, s88, v180\n"
        "v_mfma_f32_16x16x32_bf16 v[100:103], v[40:43], v[200:203], v[100:103]\n"
        "v_mul_f32 v181, s88, v181\n"
        "v_mul_f32 v182, s88, v182\n"
        "v_mfma_f32_16x16x32_bf16 v[132:135], v[40:43], v[204:207], v[132:135]\n"
        "ds_read_b64_tr_b16 v[40:41], v232 offset:50176\n"
        "global_load_lds_dwordx4 v243, s[66:67] offset:1024\n"
        "v_mfma_f32_16x16x32_bf16 v[132:135], v[44:47], v[220:223], v[132:135]\n"
        "ds_read_b64_tr_b16 v[44:45], v232 offset:58368\n"
        "v_mul_f32 v183, s88, v183\n"
        "v_mfma_f32_16x16x32_bf16 v[128:131], v[36:39], v[220:223], v[128:131]\n"
        "ds_read_b64_tr_b16 v[36:37], v230 offset:58368\n"
        "v_mfma_f32_16x16x32_bf16 v[128:131], v[32:35], v[204:207], v[128:131]\n"
        "ds_read_b64_tr_b16 v[32:33], v230 offset:50176\n"
        "v_accvgpr_read_b32 v184, a72\n"
        "v_mfma_f32_16x16x32_bf16 v[8:11], v[48:51], v[192:195], v[8:11]\n"
        "ds_read_b64_tr_b16 v[34:35], v231 offset:50176\n"
        "global_load_lds_dwordx4 v244, s[66:67] offset:2048\n"
        "v_mfma_f32_16x16x32_bf16 v[8:11], v[52:55], v[208:211], v[8:11]\n"
        "ds_read_b64_tr_b16 v[38:39], v231 offset:58368\n"
        "v_mul_f32 v184, s88, v184\n"
        "v_mfma_f32_16x16x32_bf16 v[12:15], v[60:63], v[208:211], v[12:15]\n"
        "ds_read_b64_tr_b16 v[46:47], v233 offset:58368\n"
        "v_cvt_pk_bf16_f32 v184, v184, v185\n"
        "v_mfma_f32_16x16x32_bf16 v[12:15], v[56:59], v[192:195], v[12:15]\n"
        "ds_read_b64_tr_b16 v[42:43], v233 offset:50176\n"
        "v_cvt_pk_bf16_f32 v185, v186, v187\n"
        "v_mfma_f32_16x16x32_bf16 v[76:79], v[56:59], v[196:199], v[76:79]\n"
        "global_load_lds_dwordx4 v245, s[66:67] offset:3072\n"
        "s_add_u32 m0, s73, 0x24000\n"
        "v_mfma_f32_16x16x32_bf16 v[76:79], v[60:63], v[212:215], v[76:79]\n"
        "s_add_u32 s66, s66, 0x80\n"
        "s_addc_u32 s67, s67, 0\n"
        "v_cvt_pk_bf16_f32 v186, v188, v189\n"
        "v_mfma_f32_16x16x32_bf16 v[72:75], v[52:55], v[212:215], v[72:75]\n"
        "v_cvt_pk_bf16_f32 v187, v190, v191\n"
        "v_accvgpr_write_b32 a172, v184\n"
        "v_mfma_f32_16x16x32_bf16 v[72:75], v[48:51], v[196:199], v[72:75]\n"
        "v_accvgpr_write_b32 a173, v185\n"
        "v_accvgpr_write_b32 a174, v186\n"
        "v_mfma_f32_16x16x32_bf16 v[104:107], v[48:51], v[200:203], v[104:107]\n"
        "v_accvgpr_write_b32 a175, v187\n"
        "v_accvgpr_read_b32 v185, a105\n"
        "v_mfma_f32_16x16x32_bf16 v[104:107], v[52:55], v[216:219], v[104:107]\n"
        "global_store_dwordx4 v254, a[236:239], s[70:71] offset:1024 sc1\n"
        "s_add_u32 s70, s38, s97\n"
        "s_addc_u32 s71, s39, 0\n"
        "s_add_u32 s97, s52, 0x0\n"
        "v_accvgpr_read_b32 v186, a106\n"
        "v_accvgpr_read_b32 v187, a107\n"
        "v_mfma_f32_16x16x32_bf16 v[108:111], v[60:63], v[216:219], v[108:111]\n"
        "s_waitcnt vmcnt(13) lgkmcnt(0)\n"
        "v_accvgpr_read_b32 v188, a108\n"
        "v_mfma_f32_16x16x32_bf16 v[108:111], v[56:59], v[200:203], v[108:111]\n"
        "s_barrier\n"
        "v_mfma_f32_16x16x32_bf16 v[140:143], v[56:59], v[204:207], v[140:143]\n"
        "ds_read_b64_tr_b16 v[56:57], v228 offset:16384\n"
        "global_load_lds_dwordx4 v238, s[68:69] offset:0\n"
        "s_add_u32 s97, s97, s75\n"
        "v_mfma_f32_16x16x32_bf16 v[140:143], v[60:63], v[220:223], v[140:143]\n"
        "ds_read_b64_tr_b16 v[60:61], v228 offset:24576\n"
        "v_accvgpr_read_b32 v189, a109\n"
        "v_mfma_f32_16x16x32_bf16 v[136:139], v[52:55], v[220:223], v[136:139]\n"
        "ds_read_b64_tr_b16 v[52:53], v226 offset:24576\n"
        "v_accvgpr_read_b32 v190, a110\n"
        "v_mfma_f32_16x16x32_bf16 v[136:139], v[48:51], v[204:207], v[136:139]\n"
        "ds_read_b64_tr_b16 v[48:49], v226 offset:16384\n"
        "v_accvgpr_read_b32 v191, a111\n"
        "v_mfma_f32_16x16x32_bf16 v[16:19], v[160:163], v[192:195], v[16:19]\n"
        "ds_read_b64_tr_b16 v[50:51], v227 offset:16384\n"
        "global_load_lds_dwordx4 v239, s[68:69] offset:1024\n"
        "v_mfma_f32_16x16x32_bf16 v[16:19], v[164:167], v[208:211], v[16:19]\n"
        "ds_read_b64_tr_b16 v[54:55], v227 offset:24576\n"
        "v_mul_f32 v185, s88, v185\n"
        "v_mfma_f32_16x16x32_bf16 v[20:23], v[172:175], v[208:211], v[20:23]\n"
        "ds_read_b64_tr_b16 v[62:63], v229 offset:24576\n"
        "v_mul_f32 v186, s88, v186\n"
        "v_mfma_f32_16x16x32_bf16 v[20:23], v[168:171], v[192:195], v[20:23]\n"
        "ds_read_b64_tr_b16 v[58:59], v229 offset:16384\n"
        "v_mul_f32 v187, s88, v187\n"
        "v_mfma_f32_16x16x32_bf16 v[84:87], v[168:171], v[196:199], v[84:87]\n"
        "global_load_lds_dwordx4 v240, s[68:69] offset:2048\n"
        "v_mfma_f32_16x16x32_bf16 v[84:87], v[172:175], v[212:215], v[84:87]\n"
        "v_mul_f32 v188, s88, v188\n"
        "v_mul_f32 v189, s88, v189\n"
        "v_mfma_f32_16x16x32_bf16 v[80:83], v[164:167], v[212:215], v[80:83]\n"
        "v_mul_f32 v190, s88, v190\n"
        "v_mul_f32 v191, s88, v191\n"
        "v_mfma_f32_16x16x32_bf16 v[80:83], v[160:163], v[196:199], v[80:83]\n"
        "v_accvgpr_read_b32 v176, a96\n"
        "v_mul_f32 v176, s88, v176\n"
        "v_mfma_f32_16x16x32_bf16 v[112:115], v[160:163], v[200:203], v[112:115]\n"
        "v_cvt_pk_bf16_f32 v176, v176, v177\n"
        "v_cvt_pk_bf16_f32 v177, v178, v179\n"
        "v_mfma_f32_16x16x32_bf16 v[112:115], v[164:167], v[216:219], v[112:115]\n"
        "global_load_lds_dwordx4 v241, s[68:69] offset:3072\n"
        "s_add_u32 m0, s72, 0x10000\n"
        "v_cvt_pk_bf16_f32 v178, v180, v181\n"
        "v_mfma_f32_16x16x32_bf16 v[116:119], v[172:175], v[216:219], v[116:119]\n"
        "s_add_u32 s68, s68, 0x80000\n"
        "s_addc_u32 s69, s69, 0\n"
        "v_cvt_pk_bf16_f32 v179, v182, v183\n"
        "v_mfma_f32_16x16x32_bf16 v[116:119], v[168:171], v[200:203], v[116:119]\n"
        "v_accvgpr_write_b32 a0, v176\n"
        "v_accvgpr_write_b32 a1, v177\n"
        "v_mfma_f32_16x16x32_bf16 v[148:151], v[168:171], v[204:207], v[148:151]\n"
        "ds_read_b64_tr_b16 v[168:169], v232 offset:16384\n"
        "v_accvgpr_write_b32 a2, v178\n"
        "v_mfma_f32_16x16x32_bf16 v[148:151], v[172:175], v[220:223], v[148:151]\n"
        "ds_read_b64_tr_b16 v[172:173], v232 offset:24576\n"
        "v_accvgpr_write_b32 a3, v179\n"
        "v_mfma_f32_16x16x32_bf16 v[144:147], v[164:167], v[220:223], v[144:147]\n"
        "ds_read_b64_tr_b16 v[164:165], v230 offset:24576\n"
        "global_load_lds_dwordx4 v242, s[64:65] offset:0\n"
        "v_mfma_f32_16x16x32_bf16 v[144:147], v[160:163], v[204:207], v[144:147]\n"
        "ds_read_b64_tr_b16 v[160:161], v230 offset:16384\n"
        "v_accvgpr_read_b32 v177, a129\n"
        "v_mfma_f32_16x16x32_bf16 v[24:27], v[32:35], v[192:195], v[24:27]\n"
        "ds_read_b64_tr_b16 v[162:163], v231 offset:16384\n"
        "v_accvgpr_read_b32 v178, a130\n"
        "v_mfma_f32_16x16x32_bf16 v[24:27], v[36:39], v[208:211], v[24:27]\n"
        "ds_read_b64_tr_b16 v[166:167], v231 offset:24576\n"
        "v_accvgpr_read_b32 v179, a131\n"
        "v_mfma_f32_16x16x32_bf16 v[28:31], v[44:47], v[208:211], v[28:31]\n"
        "ds_read_b64_tr_b16 v[174:175], v233 offset:24576\n"
        "global_load_lds_dwordx4 v243, s[64:65] offset:1024\n"
        "v_mfma_f32_16x16x32_bf16 v[28:31], v[40:43], v[192:195], v[28:31]\n"
        "ds_read_b64_tr_b16 v[170:171], v233 offset:16384\n"
        "v_accvgpr_read_b32 v180, a132\n"
        "v_mfma_f32_16x16x32_bf16 v[92:95], v[40:43], v[196:199], v[92:95]\n"
        "ds_read_b128 v[192:195], v246 offset:34816\n"
        "v_accvgpr_read_b32 v181, a133\n"
        "v_mfma_f32_16x16x32_bf16 v[92:95], v[44:47], v[212:215], v[92:95]\n"
        "ds_read_b128 v[208:211], v247 offset:34816\n"
        "v_accvgpr_read_b32 v182, a134\n"
        "v_mfma_f32_16x16x32_bf16 v[88:91], v[36:39], v[212:215], v[88:91]\n"
        "ds_read_b128 v[212:215], v247 offset:32768\n"
        "global_load_lds_dwordx4 v244, s[64:65] offset:2048\n"
        "v_mfma_f32_16x16x32_bf16 v[88:91], v[32:35], v[196:199], v[88:91]\n"
        "ds_read_b128 v[196:199], v246 offset:32768\n"
        "v_accvgpr_read_b32 v183, a135\n"
        "v_mfma_f32_16x16x32_bf16 v[120:123], v[32:35], v[200:203], v[120:123]\n"
        "v_mul_f32 v177, s88, v177\n"
        "v_mul_f32 v178, s88, v178\n"
        "v_mfma_f32_16x16x32_bf16 v[120:123], v[36:39], v[216:219], v[120:123]\n"
        "v_mul_f32 v179, s88, v179\n"
        "v_mul_f32 v180, s88, v180\n"
        "v_mfma_f32_16x16x32_bf16 v[124:127], v[44:47], v[216:219], v[124:127]\n"
        "ds_read_b128 v[216:219], v247 offset:38912\n"
        "global_load_lds_dwordx4 v245, s[64:65] offset:3072\n"
        "s_add_u32 m0, s72, 0x11000\n"
        "v_mfma_f32_16x16x32_bf16 v[124:127], v[40:43], v[200:203], v[124:127]\n"
        "ds_read_b128 v[200:203], v246 offset:38912\n"
        "s_add_u32 s64, s64, 0x80\n"
        "s_addc_u32 s65, s65, 0\n"
        "v_mfma_f32_16x16x32_bf16 v[156:159], v[40:43], v[204:207], v[156:159]\n"
        "ds_read_b64_tr_b16 v[40:41], v228 offset:17408\n"
        "v_mul_f32 v181, s88, v181\n"
        "v_mfma_f32_16x16x32_bf16 v[156:159], v[44:47], v[220:223], v[156:159]\n"
        "ds_read_b64_tr_b16 v[44:45], v228 offset:25600\n"
        "v_mul_f32 v182, s88, v182\n"
        "v_mfma_f32_16x16x32_bf16 v[152:155], v[36:39], v[220:223], v[152:155]\n"
        "ds_read_b128 v[220:223], v247 offset:36864\n"
        "global_load_lds_dwordx4 v242, s[66:67] offset:0\n"
        "v_mfma_f32_16x16x32_bf16 v[152:155], v[32:35], v[204:207], v[152:155]\n"
        "ds_read_b128 v[204:207], v246 offset:36864\n"
        "s_waitcnt lgkmcnt(6)\n"
        "v_mfma_f32_16x16x32_bf16 v[0:3], v[48:51], v[192:195], v[0:3]\n"
        "ds_read_b64_tr_b16 v[32:33], v226 offset:17408\n"
        "v_accvgpr_read_b32 v184, a104\n"
        "v_mfma_f32_16x16x32_bf16 v[0:3], v[52:55], v[208:211], v[0:3]\n"
        "ds_read_b64_tr_b16 v[34:35], v227 offset:17408\n"
        "v_mul_f32 v184, s88, v184\n"
        "v_mfma_f32_16x16x32_bf16 v[4:7], v[60:63], v[208:211], v[4:7]\n"
        "ds_read_b64_tr_b16 v[36:37], v226 offset:25600\n"
        "v_cvt_pk_bf16_f32 v184, v184, v185\n"
        "v_mfma_f32_16x16x32_bf16 v[4:7], v[56:59], v[192:195], v[4:7]\n"
        "ds_read_b64_tr_b16 v[38:39], v227 offset:25600\n"
        "v_cvt_pk_bf16_f32 v185, v186, v187\n"
        "v_mfma_f32_16x16x32_bf16 v[68:71], v[56:59], v[196:199], v[68:71]\n"
        "ds_read_b64_tr_b16 v[46:47], v229 offset:25600\n"
        "v_cvt_pk_bf16_f32 v186, v188, v189\n"
        "v_mfma_f32_16x16x32_bf16 v[68:71], v[60:63], v[212:215], v[68:71]\n"
        "ds_read_b64_tr_b16 v[42:43], v229 offset:17408\n"
        "v_cvt_pk_bf16_f32 v187, v190, v191\n"
        "v_mfma_f32_16x16x32_bf16 v[64:67], v[52:55], v[212:215], v[64:67]\n"
        "s_waitcnt lgkmcnt(7)\n"
        "v_accvgpr_write_b32 a4, v184\n"
        "v_accvgpr_write_b32 a5, v185\n"
        "v_mfma_f32_16x16x32_bf16 v[64:67], v[48:51], v[196:199], v[64:67]\n"
        "global_load_lds_dwordx4 v243, s[66:67] offset:1024\n"
        "v_accvgpr_write_b32 a6, v186\n"
        "v_mfma_f32_16x16x32_bf16 v[96:99], v[48:51], v[200:203], v[96:99]\n"
        "s_waitcnt lgkmcnt(5)\n"
        "v_accvgpr_write_b32 a7, v187\n"
        "v_mul_f32 v183, s88, v183\n"
        "v_mfma_f32_16x16x32_bf16 v[96:99], v[52:55], v[216:219], v[96:99]\n"
        "v_accvgpr_read_b32 v185, a137\n"
        "v_accvgpr_read_b32 v186, a138\n"
        "v_mfma_f32_16x16x32_bf16 v[100:103], v[60:63], v[216:219], v[100:103]\n"
        "v_accvgpr_read_b32 v187, a139\n"
        "v_accvgpr_read_b32 v188, a140\n"
        "v_mfma_f32_16x16x32_bf16 v[100:103], v[56:59], v[200:203], v[100:103]\n"
        "global_store_dwordx4 v254, a[192:195], s[70:71] offset:0 sc1\n"
        "v_accvgpr_read_b32 v189, a141\n"
        "v_accvgpr_read_b32 v190, a142\n"
        "v_accvgpr_read_b32 v191, a143\n"
        "v_mul_f32 v185, s88, v185\n"
        "v_mfma_f32_16x16x32_bf16 v[132:135], v[56:59], v[204:207], v[132:135]\n"
        "ds_read_b64_tr_b16 v[56:57], v232 offset:17408\n"
        "v_mul_f32 v186, s88, v186\n"
        "v_mfma_f32_16x16x32_bf16 v[132:135], v[60:63], v[220:223], v[132:135]\n"
        "ds_read_b64_tr_b16 v[60:61], v232 offset:25600\n"
        "v_mul_f32 v187, s88, v187\n"
        "v_mfma_f32_16x16x32_bf16 v[128:131], v[52:55], v[220:223], v[128:131]\n"
        "ds_read_b64_tr_b16 v[52:53], v230 offset:25600\n"
        "global_load_lds_dwordx4 v244, s[66:67] offset:2048\n"
        "v_mfma_f32_16x16x32_bf16 v[128:131], v[48:51], v[204:207], v[128:131]\n"
        "ds_read_b64_tr_b16 v[48:49], v230 offset:17408\n"
        "v_mul_f32 v188, s88, v188\n"
        "v_mfma_f32_16x16x32_bf16 v[8:11], v[160:163], v[192:195], v[8:11]\n"
        "ds_read_b64_tr_b16 v[50:51], v231 offset:17408\n"
        "v_mul_f32 v189, s88, v189\n"
        "v_mfma_f32_16x16x32_bf16 v[8:11], v[164:167], v[208:211], v[8:11]\n"
        "ds_read_b64_tr_b16 v[54:55], v231 offset:25600\n"
        "v_mul_f32 v190, s88, v190\n"
        "v_mfma_f32_16x16x32_bf16 v[12:15], v[172:175], v[208:211], v[12:15]\n"
        "ds_read_b64_tr_b16 v[62:63], v233 offset:25600\n"
        "global_load_lds_dwordx4 v245, s[66:67] offset:3072\n"
        "s_add_u32 m0, s73, 0x1c000\n"
        "v_mfma_f32_16x16x32_bf16 v[12:15], v[168:171], v[192:195], v[12:15]\n"
        "ds_read_b64_tr_b16 v[58:59], v233 offset:17408\n"
        "s_add_u32 s66, s66, 0x80\n"
        "s_addc_u32 s67, s67, 0\n"
        "v_mfma_f32_16x16x32_bf16 v[76:79], v[168:171], v[196:199], v[76:79]\n"
        "v_mul_f32 v191, s88, v191\n"
        "v_mfma_f32_16x16x32_bf16 v[76:79], v[172:175], v[212:215], v[76:79]\n"
        "v_mfma_f32_16x16x32_bf16 v[72:75], v[164:167], v[212:215], v[72:75]\n"
        "v_mfma_f32_16x16x32_bf16 v[72:75], v[160:163], v[196:199], v[72:75]\n"
        "v_accvgpr_read_b32 v176, a128\n"
        "v_mul_f32 v176, s88, v176\n"
        "v_mfma_f32_16x16x32_bf16 v[104:107], v[160:163], v[200:203], v[104:107]\n"
        "v_cvt_pk_bf16_f32 v176, v176, v177\n"
        "v_cvt_pk_bf16_f32 v177, v178, v179\n"
        "v_mfma_f32_16x16x32_bf16 v[104:107], v[164:167], v[216:219], v[104:107]\n"
        "v_cvt_pk_bf16_f32 v178, v180, v181\n"
        "v_cvt_pk_bf16_f32 v179, v182, v183\n"
        "v_mfma_f32_16x16x32_bf16 v[108:111], v[172:175], v[216:219], v[108:111]\n"
        "v_accvgpr_write_b32 a8, v176\n"
        "v_accvgpr_write_b32 a9, v177\n"
        "v_mfma_f32_16x16x32_bf16 v[108:111], v[168:171], v[200:203], v[108:111]\n"
        "s_waitcnt vmcnt(14) lgkmcnt(0)\n"
        "v_accvgpr_write_b32 a10, v178\n"
        "v_accvgpr_write_b32 a11, v179\n"
        "v_mfma_f32_16x16x32_bf16 v[140:143], v[168:171], v[204:207], v[140:143]\n"
        "s_barrier\n"
        "v_mfma_f32_16x16x32_bf16 v[140:143], v[172:175], v[220:223], v[140:143]\n"
        "ds_read_b64_tr_b16 v[172:173], v228 offset:40960\n"
        "global_load_lds_dwordx4 v238, s[68:69] offset:0\n"
        "v_mfma_f32_16x16x32_bf16 v[136:139], v[164:167], v[220:223], v[136:139]\n"
        "ds_read_b64_tr_b16 v[164:165], v226 offset:40960\n"
        "v_accvgpr_read_b32 v177, a209\n"
        "v_mfma_f32_16x16x32_bf16 v[136:139], v[160:163], v[204:207], v[136:139]\n"
        "ds_read_b64_tr_b16 v[160:161], v226 offset:32768\n"
        "v_accvgpr_read_b32 v178, a210\n"
        "v_mfma_f32_16x16x32_bf16 v[16:19], v[32:35], v[192:195], v[16:19]\n"
        "ds_read_b64_tr_b16 v[162:163], v227 offset:32768\n"
        "v_accvgpr_read_b32 v179, a211\n"
        "v_mfma_f32_16x16x32_bf16 v[16:19], v[36:39], v[208:211], v[16:19]\n"
        "ds_read_b64_tr_b16 v[166:167], v227 offset:40960\n"
        "global_load_lds_dwordx4 v239, s[68:69] offset:1024\n"
        "v_mfma_f32_16x16x32_bf16 v[20:23], v[44:47], v[208:211], v[20:23]\n"
        "ds_read_b64_tr_b16 v[174:175], v229 offset:40960\n"
        "v_accvgpr_read_b32 v180, a240\n"
        "v_mfma_f32_16x16x32_bf16 v[20:23], v[40:43], v[192:195], v[20:23]\n"
        "ds_read_b64_tr_b16 v[168:169], v228 offset:32768\n"
        "v_accvgpr_read_b32 v181, a241\n"
        "v_mfma_f32_16x16x32_bf16 v[84:87], v[40:43], v[196:199], v[84:87]\n"
        "ds_read_b64_tr_b16 v[170:171], v229 offset:32768\n"
        "v_accvgpr_read_b32 v182, a242\n"
        "v_mfma_f32_16x16x32_bf16 v[84:87], v[44:47], v[212:215], v[84:87]\n"
        "global_load_lds_dwordx4 v240, s[68:69] offset:2048\n"
        "v_mfma_f32_16x16x32_bf16 v[80:83], v[36:39], v[212:215], v[80:83]\n"
        "v_accvgpr_read_b32 v183, a243\n"
        "v_mfma_f32_16x16x32_bf16 v[80:83], v[32:35], v[196:199], v[80:83]\n"
        "v_mfma_f32_16x16x32_bf16 v[112:115], v[32:35], v[200:203], v[112:115]\n"
        "v_mfma_f32_16x16x32_bf16 v[112:115], v[36:39], v[216:219], v[112:115]\n"
        "global_load_lds_dwordx4 v241, s[68:69] offset:3072\n"
        "s_add_u32 m0, s72, 0x8000\n"
        "v_mfma_f32_16x16x32_bf16 v[116:119], v[44:47], v[216:219], v[116:119]\n"
        "s_add_u32 s68, s68, 0x80000\n"
        "s_addc_u32 s69, s69, 0\n"
        "v_mfma_f32_16x16x32_bf16 v[116:119], v[40:43], v[200:203], v[116:119]\n"
        "v_mfma_f32_16x16x32_bf16 v[148:151], v[40:43], v[204:207], v[148:151]\n"
        "ds_read_b64_tr_b16 v[40:41], v232 offset:32768\n"
        "v_mfma_f32_16x16x32_bf16 v[148:151], v[44:47], v[220:223], v[148:151]\n"
        "ds_read_b64_tr_b16 v[44:45], v232 offset:40960\n"
        "global_load_lds_dwordx4 v242, s[64:65] offset:0\n"
        "v_mfma_f32_16x16x32_bf16 v[144:147], v[36:39], v[220:223], v[144:147]\n"
        "ds_read_b64_tr_b16 v[36:37], v230 offset:40960\n"
        "v_mfma_f32_16x16x32_bf16 v[144:147], v[32:35], v[204:207], v[144:147]\n"
        "ds_read_b64_tr_b16 v[32:33], v230 offset:32768\n"
        "v_accvgpr_read_b32 v184, a136\n"
        "v_mfma_f32_16x16x32_bf16 v[24:27], v[48:51], v[192:195], v[24:27]\n"
        "ds_read_b64_tr_b16 v[34:35], v231 offset:32768\n"
        "v_mul_f32 v184, s88, v184\n"
        "v_mfma_f32_16x16x32_bf16 v[24:27], v[52:55], v[208:211], v[24:27]\n"
        "ds_read_b64_tr_b16 v[38:39], v231 offset:40960\n"
        "v_cvt_pk_bf16_f32 v184, v184, v185\n"
        "v_mfma_f32_16x16x32_bf16 v[28:31], v[60:63], v[208:211], v[28:31]\n"
        "ds_read_b64_tr_b16 v[46:47], v233 offset:40960\n"
        "v_cvt_pk_bf16_f32 v185, v186, v187\n"
        "v_mfma_f32_16x16x32_bf16 v[28:31], v[56:59], v[192:195], v[28:31]\n"
        "ds_read_b64_tr_b16 v[42:43], v233 offset:32768\n"
        "v_cvt_pk_bf16_f32 v186, v188, v189\n"
        "v_mfma_f32_16x16x32_bf16 v[92:95], v[56:59], v[196:199], v[92:95]\n"
        "ds_read_b128 v[192:195], v246 offset:2048\n"
        "LHA6_%=:\n"
        "s_cbranch_execz LHA10_%=\n"
        "v_cvt_pk_bf16_f32 v187, v190, v191\n"
        "v_mfma_f32_16x16x32_bf16 v[92:95], v[60:63], v[212:215], v[92:95]\n"
        "ds_read_b128 v[208:211], v247 offset:2048\n"
        "v_accvgpr_write_b32 a12, v184\n"
        "v_mfma_f32_16x16x32_bf16 v[88:91], v[52:55], v[212:215], v[88:91]\n"
        "ds_read_b128 v[212:215], v247 offset:0\n"
        "v_accvgpr_write_b32 a13, v185\n"
        "v_mfma_f32_16x16x32_bf16 v[88:91], v[48:51], v[196:199], v[88:91]\n"
        "ds_read_b128 v[196:199], v246 offset:0\n"
        "v_accvgpr_write_b32 a14, v186\n"
        "v_mfma_f32_16x16x32_bf16 v[120:123], v[48:51], v[200:203], v[120:123]\n"
        "global_load_lds_dwordx4 v243, s[64:65] offset:1024\n"
        "v_accvgpr_write_b32 a15, v187\n"
        "v_mfma_f32_16x16x32_bf16 v[120:123], v[52:55], v[216:219], v[120:123]\n"
        "v_accvgpr_read_b32 v185, a213\n"
        "v_accvgpr_read_b32 v186, a214\n"
        "v_mfma_f32_16x16x32_bf16 v[124:127], v[60:63], v[216:219], v[124:127]\n"
        "ds_read_b128 v[216:219], v247 offset:6144\n"
        "v_accvgpr_read_b32 v187, a215\n"
        "v_mfma_f32_16x16x32_bf16 v[124:127], v[56:59], v[200:203], v[124:127]\n"
        "ds_read_b128 v[200:203], v246 offset:6144\n"
        "v_accvgpr_read_b32 v188, a244\n"
        "v_mfma_f32_16x16x32_bf16 v[156:159], v[56:59], v[204:207], v[156:159]\n"
        "ds_read_b64_tr_b16 v[56:57], v228 offset:33792\n"
        "global_load_lds_dwordx4 v244, s[64:65] offset:2048\n"
        "v_mfma_f32_16x16x32_bf16 v[156:159], v[60:63], v[220:223], v[156:159]\n"
        "ds_read_b64_tr_b16 v[60:61], v228 offset:41984\n"
        "v_accvgpr_read_b32 v189, a245\n"
        "v_mfma_f32_16x16x32_bf16 v[152:155], v[52:55], v[220:223], v[152:155]\n"
        "ds_read_b128 v[220:223], v247 offset:4096\n"
        "v_accvgpr_read_b32 v190, a246\n"
        "v_mfma_f32_16x16x32_bf16 v[152:155], v[48:51], v[204:207], v[152:155]\n"
        "ds_read_b128 v[204:207], v246 offset:4096\n"
        "s_waitcnt lgkmcnt(6)\n"
        "v_mfma_f32_16x16x32_bf16 v[0:3], v[160:163], v[192:195], v[0:3]\n"
        "ds_read_b64_tr_b16 v[48:49], v226 offset:33792\n"
        "global_load_lds_dwordx4 v245, s[64:65] offset:3072\n"
        "s_add_u32 m0, s72, 0x9000\n"
        "v_mfma_f32_16x16x32_bf16 v[0:3], v[164:167], v[208:211], v[0:3]\n"
        "ds_read_b64_tr_b16 v[50:51], v227 offset:33792\n"
        "s_add_u32 s64, s64, 0x80\n"
        "s_addc_u32 s65, s65, 0\n"
        "v_mfma_f32_16x16x32_bf16 v[4:7], v[172:175], v[208:211], v[4:7]\n"
        "ds_read_b64_tr_b16 v[52:53], v226 offset:41984\n"
        "v_accvgpr_read_b32 v191, a247\n"
        "v_mfma_f32_16x16x32_bf16 v[4:7], v[168:171], v[192:195], v[4:7]\n"
        "ds_read_b64_tr_b16 v[54:55], v227 offset:41984\n"
        "v_mfma_f32_16x16x32_bf16 v[68:71], v[168:171], v[196:199], v[68:71]\n"
        "ds_read_b64_tr_b16 v[62:63], v229 offset:41984\n"
        "global_load_lds_dwordx4 v242, s[66:67] offset:0\n"
        "v_mfma_f32_16x16x32_bf16 v[68:71], v[172:175], v[212:215], v[68:71]\n"
        "ds_read_b64_tr_b16 v[58:59], v229 offset:33792\n"
        "s_waitcnt lgkmcnt(8)\n"
        "v_mfma_f32_16x16x32_bf16 v[64:67], v[164:167], v[212:215], v[64:67]\n"
        "v_mfma_f32_16x16x32_bf16 v[64:67], v[160:163], v[196:199], v[64:67]\n"
        "v_accvgpr_read_b32 v176, a208\n"
        "v_cvt_pk_bf16_f32 v176, v176, v177\n"
        "v_mfma_f32_16x16x32_bf16 v[96:99], v[160:163], v[200:203], v[96:99]\n"
        "global_load_lds_dwordx4 v243, s[66:67] offset:1024\n"
        "v_mfma_f32_16x16x32_bf16 v[96:99], v[164:167], v[216:219], v[96:99]\n"
        "s_waitcnt lgkmcnt(4)\n"
        "v_cvt_pk_bf16_f32 v177, v178, v179\n"
        "v_cvt_pk_bf16_f32 v178, v180, v181\n"
        "v_mfma_f32_16x16x32_bf16 v[100:103], v[172:175], v[216:219], v[100:103]\n"
        "v_cvt_pk_bf16_f32 v179, v182, v183\n"
        "v_accvgpr_write_b32 a208, v176\n"
        "v_mfma_f32_16x16x32_bf16 v[100:103], v[168:171], v[200:203], v[100:103]\n"
        "v_accvgpr_write_b32 a209, v177\n"
        "v_accvgpr_write_b32 a210, v178\n"
        "v_mfma_f32_16x16x32_bf16 v[132:135], v[168:171], v[204:207], v[132:135]\n"
        "ds_read_b64_tr_b16 v[168:169], v232 offset:33792\n"
        "v_accvgpr_write_b32 a211, v179\n"
        "v_mfma_f32_16x16x32_bf16 v[132:135], v[172:175], v[220:223], v[132:135]\n"
        "ds_read_b64_tr_b16 v[172:173], v232 offset:41984\n"
        "global_load_lds_dwordx4 v244, s[66:67] offset:2048\n"
        "v_mfma_f32_16x16x32_bf16 v[128:131], v[164:167], v[220:223], v[128:131]\n"
        "ds_read_b64_tr_b16 v[164:165], v230 offset:41984\n"
        "v_accvgpr_read_b32 v177, a217\n"
        "v_mfma_f32_16x16x32_bf16 v[128:131], v[160:163], v[204:207], v[128:131]\n"
        "ds_read_b64_tr_b16 v[160:161], v230 offset:33792\n"
        "v_accvgpr_read_b32 v178, a218\n"
        "v_mfma_f32_16x16x32_bf16 v[8:11], v[32:35], v[192:195], v[8:11]\n"
        "ds_read_b64_tr_b16 v[162:163], v231 offset:33792\n"
        "v_accvgpr_read_b32 v179, a219\n"
        "v_mfma_f32_16x16x32_bf16 v[8:11], v[36:39], v[208:211], v[8:11]\n"
        "ds_read_b64_tr_b16 v[166:167], v231 offset:41984\n"
        "global_load_lds_dwordx4 v245, s[66:67] offset:3072\n"
        "s_add_u32 m0, s73, 0x20000\n"
        "v_mfma_f32_16x16x32_bf16 v[12:15], v[44:47], v[208:211], v[12:15]\n"
        "ds_read_b64_tr_b16 v[174:175], v233 offset:41984\n"
        "s_add_u32 s66, s66, 0x80\n"
        "s_addc_u32 s67, s67, 0\n"
        "v_mfma_f32_16x16x32_bf16 v[12:15], v[40:43], v[192:195], v[12:15]\n"
        "ds_read_b64_tr_b16 v[170:171], v233 offset:33792\n"
        "v_accvgpr_read_b32 v180, a248\n"
        "v_mfma_f32_16x16x32_bf16 v[76:79], v[40:43], v[196:199], v[76:79]\n"
        "v_accvgpr_read_b32 v181, a249\n"
        "v_accvgpr_read_b32 v182, a250\n"
        "v_mfma_f32_16x16x32_bf16 v[76:79], v[44:47], v[212:215], v[76:79]\n"
        "v_accvgpr_read_b32 v183, a251\n"
        "v_mfma_f32_16x16x32_bf16 v[72:75], v[36:39], v[212:215], v[72:75]\n"
        "global_store_dwordx4 v254, a[196:199], s[70:71] offset:1024 sc1\n"
        "s_add_u32 s70, s38, s97\n"
        "s_addc_u32 s71, s39, 0\n"
        "s_add_u32 s97, s52, 0x1000\n"
        "s_add_u32 s97, s97, s75\n"
        "v_mfma_f32_16x16x32_bf16 v[72:75], v[32:35], v[196:199], v[72:75]\n"
        "v_mfma_f32_16x16x32_bf16 v[104:107], v[32:35], v[200:203], v[104:107]\n"
        "v_mfma_f32_16x16x32_bf16 v[104:107], v[36:39], v[216:219], v[104:107]\n"
        "v_mfma_f32_16x16x32_bf16 v[108:111], v[44:47], v[216:219], v[108:111]\n"
        "s_waitcnt vmcnt(13) lgkmcnt(0)\n"
        "v_mfma_f32_16x16x32_bf16 v[108:111], v[40:43], v[200:203], v[108:111]\n"
        "s_barrier\n"
        "v_mfma_f32_16x16x32_bf16 v[140:143], v[40:43], v[204:207], v[140:143]\n"
        "ds_read_b64_tr_b16 v[40:41], v228 offset:49152\n"
        "global_load_lds_dwordx4 v238, s[68:69] offset:0\n"
        "v_mfma_f32_16x16x32_bf16 v[140:143], v[44:47], v[220:223], v[140:143]\n"
        "ds_read_b64_tr_b16 v[44:45], v228 offset:57344\n"
        "v_mfma_f32_16x16x32_bf16 v[136:139], v[36:39], v[220:223], v[136:139]\n"
        "ds_read_b64_tr_b16 v[36:37], v226 offset:57344\n"
        "v_mfma_f32_16x16x32_bf16 v[136:139], v[32:35], v[204:207], v[136:139]\n"
        "ds_read_b64_tr_b16 v[32:33], v226 offset:49152\n"
        "v_accvgpr_read_b32 v184, a212\n"
        "v_mfma_f32_16x16x32_bf16 v[16:19], v[48:51], v[192:195], v[16:19]\n"
        "ds_read_b64_tr_b16 v[34:35], v227 offset:49152\n"
        "v_cvt_pk_bf16_f32 v184, v184, v185\n"
        "v_mfma_f32_16x16x32_bf16 v[16:19], v[52:55], v[208:211], v[16:19]\n"
        "ds_read_b64_tr_b16 v[38:39], v227 offset:57344\n"
        "v_cvt_pk_bf16_f32 v185, v186, v187\n"
        "v_mfma_f32_16x16x32_bf16 v[20:23], v[60:63], v[208:211], v[20:23]\n"
        "ds_read_b64_tr_b16 v[46:47], v229 offset:57344\n"
        "v_cvt_pk_bf16_f32 v186, v188, v189\n"
        "v_mfma_f32_16x16x32_bf16 v[20:23], v[56:59], v[192:195], v[20:23]\n"
        "ds_read_b64_tr_b16 v[42:43], v229 offset:49152\n"
        "v_cvt_pk_bf16_f32 v187, v190, v191\n"
        "v_mfma_f32_16x16x32_bf16 v[84:87], v[56:59], v[196:199], v[84:87]\n"
        "global_load_lds_dwordx4 v239, s[68:69] offset:1024\n"
        "v_accvgpr_write_b32 a240, v184\n"
        "v_mfma_f32_16x16x32_bf16 v[84:87], v[60:63], v[212:215], v[84:87]\n"
        "v_accvgpr_write_b32 a241, v185\n"
        "v_accvgpr_write_b32 a242, v186\n"
        "v_mfma_f32_16x16x32_bf16 v[80:83], v[52:55], v[212:215], v[80:83]\n"
        "v_accvgpr_write_b32 a243, v187\n"
        "v_accvgpr_read_b32 v185, a221\n"
        "v_mfma_f32_16x16x32_bf16 v[80:83], v[48:51], v[196:199], v[80:83]\n"
        "v_accvgpr_read_b32 v186, a222\n"
        "v_accvgpr_read_b32 v187, a223\n"
        "v_mfma_f32_16x16x32_bf16 v[112:115], v[48:51], v[200:203], v[112:115]\n"
        "global_load_lds_dwordx4 v240, s[68:69] offset:2048\n"
        "v_accvgpr_read_b32 v188, a252\n"
        "v_mfma_f32_16x16x32_bf16 v[112:115], v[52:55], v[216:219], v[112:115]\n"
        "v_accvgpr_read_b32 v189, a253\n"
        "v_accvgpr_read_b32 v190, a254\n"
        "v_mfma_f32_16x16x32_bf16 v[116:119], v[60:63], v[216:219], v[116:119]\n"
        "v_accvgpr_read_b32 v191, a255\n"
        "v_mfma_f32_16x16x32_bf16 v[116:119], v[56:59], v[200:203], v[116:119]\n"
        "v_mfma_f32_16x16x32_bf16 v[148:151], v[56:59], v[204:207], v[148:151]\n"
        "ds_read_b64_tr_b16 v[56:57], v232 offset:49152\n"
        "global_load_lds_dwordx4 v241, s[68:69] offset:3072\n"
        "s_add_u32 m0, s72, 0x0\n"
        "v_mfma_f32_16x16x32_bf16 v[148:151], v[60:63], v[220:223], v[148:151]\n"
        "ds_read_b64_tr_b16 v[60:61], v232 offset:57344\n"
        "s_add_u32 s68, s68, 0x80000\n"
        "s_addc_u32 s69, s69, 0\n"
        "v_mfma_f32_16x16x32_bf16 v[144:147], v[52:55], v[220:223], v[144:147]\n"
        "ds_read_b64_tr_b16 v[52:53], v230 offset:57344\n"
        "v_mfma_f32_16x16x32_bf16 v[144:147], v[48:51], v[204:207], v[144:147]\n"
        "ds_read_b64_tr_b16 v[48:49], v230 offset:49152\n"
        "v_mfma_f32_16x16x32_bf16 v[24:27], v[160:163], v[192:195], v[24:27]\n"
        "ds_read_b64_tr_b16 v[50:51], v231 offset:49152\n"
        "global_load_lds_dwordx4 v242, s[64:65] offset:0\n"
        "v_mfma_f32_16x16x32_bf16 v[24:27], v[164:167], v[208:211], v[24:27]\n"
        "ds_read_b64_tr_b16 v[54:55], v231 offset:57344\n"
        "v_mfma_f32_16x16x32_bf16 v[28:31], v[172:175], v[208:211], v[28:31]\n"
        "ds_read_b64_tr_b16 v[62:63], v233 offset:57344\n"
        "v_mfma_f32_16x16x32_bf16 v[28:31], v[168:171], v[192:195], v[28:31]\n"
        "ds_read_b64_tr_b16 v[58:59], v233 offset:49152\n"
        "v_mfma_f32_16x16x32_bf16 v[92:95], v[168:171], v[196:199], v[92:95]\n"
        "ds_read_b128 v[192:195], v248 offset:2048\n"
        "global_load_lds_dwordx4 v243, s[64:65] offset:1024\n"
        "v_mfma_f32_16x16x32_bf16 v[92:95], v[172:175], v[212:215], v[92:95]\n"
        "ds_read_b128 v[208:211], v249 offset:2048\n"
        "v_mfma_f32_16x16x32_bf16 v[88:91], v[164:167], v[212:215], v[88:91]\n"
        "ds_read_b128 v[212:215], v249 offset:0\n"
        "v_mfma_f32_16x16x32_bf16 v[88:91], v[160:163], v[196:199], v[88:91]\n"
        "ds_read_b128 v[196:199], v248 offset:0\n"
        "v_accvgpr_read_b32 v176, a216\n"
        "v_mfma_f32_16x16x32_bf16 v[120:123], v[160:163], v[200:203], v[120:123]\n"
        "v_cvt_pk_bf16_f32 v176, v176, v177\n"
        "v_cvt_pk_bf16_f32 v177, v178, v179\n"
        "v_mfma_f32_16x16x32_bf16 v[120:123], v[164:167], v[216:219], v[120:123]\n"
        "global_load_lds_dwordx4 v244, s[64:65] offset:2048\n"
        "v_cvt_pk_bf16_f32 v178, v180, v181\n"
        "v_mfma_f32_16x16x32_bf16 v[124:127], v[172:175], v[216:219], v[124:127]\n"
        "ds_read_b128 v[216:219], v249 offset:6144\n"
        "v_cvt_pk_bf16_f32 v179, v182, v183\n"
        "v_mfma_f32_16x16x32_bf16 v[124:127], v[168:171], v[200:203], v[124:127]\n"
        "ds_read_b128 v[200:203], v248 offset:6144\n"
        "v_accvgpr_write_b32 a212, v176\n"
        "v_mfma_f32_16x16x32_bf16 v[156:159], v[168:171], v[204:207], v[156:159]\n"
        "ds_read_b64_tr_b16 v[168:169], v228 offset:50176\n"
        "v_accvgpr_write_b32 a213, v177\n"
        "v_mfma_f32_16x16x32_bf16 v[156:159], v[172:175], v[220:223], v[156:159]\n"
        "ds_read_b64_tr_b16 v[172:173], v228 offset:58368\n"
        "v_accvgpr_write_b32 a214, v178\n"
        "v_mfma_f32_16x16x32_bf16 v[152:155], v[164:167], v[220:223], v[152:155]\n"
        "ds_read_b128 v[220:223], v249 offset:4096\n"
        "v_accvgpr_write_b32 a215, v179\n"
        "v_mfma_f32_16x16x32_bf16 v[152:155], v[160:163], v[204:207], v[152:155]\n"
        "ds_read_b128 v[204:207], v248 offset:4096\n"
        "s_waitcnt lgkmcnt(6)\n"
        "v_mfma_f32_16x16x32_bf16 v[0:3], v[32:35], v[192:195], v[0:3]\n"
        "ds_read_b64_tr_b16 v[160:161], v226 offset:50176\n"
        "global_load_lds_dwordx4 v245, s[64:65] offset:3072\n"
        "s_add_u32 m0, s72, 0x1000\n"
        "v_mfma_f32_16x16x32_bf16 v[0:3], v[36:39], v[208:211], v[0:3]\n"
        "ds_read_b64_tr_b16 v[162:163], v227 offset:50176\n"
        "s_add_u32 s64, s64, 0x80\n"
        "s_addc_u32 s65, s65, 0\n"
        "v_mfma_f32_16x16x32_bf16 v[4:7], v[44:47], v[208:211], v[4:7]\n"
        "ds_read_b64_tr_b16 v[164:165], v226 offset:58368\n"
        "v_accvgpr_read_b32 v177, a145\n"
        "v_mfma_f32_16x16x32_bf16 v[4:7], v[40:43], v[192:195], v[4:7]\n"
        "ds_read_b64_tr_b16 v[166:167], v227 offset:58368\n"
        "v_accvgpr_read_b32 v178, a146\n"
        "v_mfma_f32_16x16x32_bf16 v[68:71], v[40:43], v[196:199], v[68:71]\n"
        "ds_read_b64_tr_b16 v[174:175], v229 offset:58368\n"
        "global_load_lds_dwordx4 v242, s[66:67] offset:0\n"
        "v_mfma_f32_16x16x32_bf16 v[68:71], v[44:47], v[212:215], v[68:71]\n"
        "ds_read_b64_tr_b16 v[170:171], v229 offset:50176\n"
        "v_accvgpr_read_b32 v179, a147\n"
        "v_mfma_f32_16x16x32_bf16 v[64:67], v[36:39], v[212:215], v[64:67]\n"
        "s_waitcnt lgkmcnt(7)\n"
        "v_accvgpr_read_b32 v180, a176\n"
        "v_accvgpr_read_b32 v181, a177\n"
        "v_mfma_f32_16x16x32_bf16 v[64:67], v[32:35], v[196:199], v[64:67]\n"
        "v_accvgpr_read_b32 v182, a178\n"
        "v_accvgpr_read_b32 v183, a179\n"
        "v_mfma_f32_16x16x32_bf16 v[96:99], v[32:35], v[200:203], v[96:99]\n"
        "global_load_lds_dwordx4 v243, s[66:67] offset:1024\n"
        "v_mfma_f32_16x16x32_bf16 v[96:99], v[36:39], v[216:219], v[96:99]\n"
        "s_waitcnt lgkmcnt(4)\n"
        "v_mfma_f32_16x16x32_bf16 v[100:103], v[44:47], v[216:219], v[100:103]\n"
        "v_mfma_f32_16x16x32_bf16 v[100:103], v[40:43], v[200:203], v[100:103]\n"
        "v_mfma_f32_16x16x32_bf16 v[132:135], v[40:43], v[204:207], v[132:135]\n"
        "ds_read_b64_tr_b16 v[40:41], v232 offset:50176\n"
        "global_load_lds_dwordx4 v244, s[66:67] offset:2048\n"
        "v_mfma_f32_16x16x32_bf16 v[132:135], v[44:47], v[220:223], v[132:135]\n"
        "ds_read_b64_tr_b16 v[44:45], v232 offset:58368\n"
        "v_mfma_f32_16x16x32_bf16 v[128:131], v[36:39], v[220:223], v[128:131]\n"
        "ds_read_b64_tr_b16 v[36:37], v230 offset:58368\n"
        "v_mfma_f32_16x16x32_bf16 v[128:131], v[32:35], v[204:207], v[128:131]\n"
        "ds_read_b64_tr_b16 v[32:33], v230 offset:50176\n"
        "v_accvgpr_read_b32 v184, a220\n"
        "v_mfma_f32_16x16x32_bf16 v[8:11], v[48:51], v[192:195], v[8:11]\n"
        "ds_read_b64_tr_b16 v[34:35], v231 offset:50176\n"
        "global_load_lds_dwordx4 v245, s[66:67] offset:3072\n"
        "s_add_u32 m0, s73, 0x24000\n"
        "v_mfma_f32_16x16x32_bf16 v[8:11], v[52:55], v[208:211], v[8:11]\n"
        "ds_read_b64_tr_b16 v[38:39], v231 offset:58368\n"
        "v_cvt_pk_bf16_f32 v184, v184, v185\n"
        "v_mfma_f32_16x16x32_bf16 v[12:15], v[60:63], v[208:211], v[12:15]\n"
        "ds_read_b64_tr_b16 v[46:47], v233 offset:58368\n"
        "v_cvt_pk_bf16_f32 v185, v186, v187\n"
        "v_mfma_f32_16x16x32_bf16 v[12:15], v[56:59], v[192:195], v[12:15]\n"
        "ds_read_b64_tr_b16 v[42:43], v233 offset:50176\n"
        "v_cvt_pk_bf16_f32 v186, v188, v189\n"
        "v_mfma_f32_16x16x32_bf16 v[76:79], v[56:59], v[196:199], v[76:79]\n"
        "s_add_u32 s66, s66, 0x80\n"
        "s_addc_u32 s67, s67, 0\n"
        "v_cvt_pk_bf16_f32 v187, v190, v191\n"
        "v_mfma_f32_16x16x32_bf16 v[76:79], v[60:63], v[212:215], v[76:79]\n"
        "v_accvgpr_write_b32 a244, v184\n"
        "v_accvgpr_write_b32 a245, v185\n"
        "v_mfma_f32_16x16x32_bf16 v[72:75], v[52:55], v[212:215], v[72:75]\n"
        "v_accvgpr_write_b32 a246, v186\n"
        "v_accvgpr_write_b32 a247, v187\n"
        "v_mfma_f32_16x16x32_bf16 v[72:75], v[48:51], v[196:199], v[72:75]\n"
        "global_store_dwordx4 v254, a[200:203], s[70:71] offset:0 sc1\n"
        "v_accvgpr_read_b32 v185, a149\n"
        "v_accvgpr_read_b32 v186, a150\n"
        "v_accvgpr_read_b32 v187, a151\n"
        "v_accvgpr_read_b32 v188, a180\n"
        "v_mfma_f32_16x16x32_bf16 v[104:107], v[48:51], v[200:203], v[104:107]\n"
        "v_accvgpr_read_b32 v189, a181\n"
        "v_accvgpr_read_b32 v190, a182\n"
        "v_mfma_f32_16x16x32_bf16 v[104:107], v[52:55], v[216:219], v[104:107]\n"
        "v_accvgpr_read_b32 v191, a183\n"
        "v_mfma_f32_16x16x32_bf16 v[108:111], v[60:63], v[216:219], v[108:111]\n"
        "s_waitcnt vmcnt(14) lgkmcnt(0)\n"
        "v_mfma_f32_16x16x32_bf16 v[108:111], v[56:59], v[200:203], v[108:111]\n"
        "s_barrier\n"
        "v_mfma_f32_16x16x32_bf16 v[140:143], v[56:59], v[204:207], v[140:143]\n"
        "ds_read_b64_tr_b16 v[56:57], v228 offset:16384\n"
        "global_load_lds_dwordx4 v238, s[68:69] offset:0\n"
        "v_mfma_f32_16x16x32_bf16 v[140:143], v[60:63], v[220:223], v[140:143]\n"
        "ds_read_b64_tr_b16 v[60:61], v228 offset:24576\n"
        "v_mfma_f32_16x16x32_bf16 v[136:139], v[52:55], v[220:223], v[136:139]\n"
        "ds_read_b64_tr_b16 v[52:53], v226 offset:24576\n"
        "v_mfma_f32_16x16x32_bf16 v[136:139], v[48:51], v[204:207], v[136:139]\n"
        "ds_read_b64_tr_b16 v[48:49], v226 offset:16384\n"
        "v_mfma_f32_16x16x32_bf16 v[16:19], v[160:163], v[192:195], v[16:19]\n"
        "ds_read_b64_tr_b16 v[50:51], v227 offset:16384\n"
        "global_load_lds_dwordx4 v239, s[68:69] offset:1024\n"
        "v_mfma_f32_16x16x32_bf16 v[16:19], v[164:167], v[208:211], v[16:19]\n"
        "ds_read_b64_tr_b16 v[54:55], v227 offset:24576\n"
        "v_mfma_f32_16x16x32_bf16 v[20:23], v[172:175], v[208:211], v[20:23]\n"
        "ds_read_b64_tr_b16 v[62:63], v229 offset:24576\n"
        "v_mfma_f32_16x16x32_bf16 v[20:23], v[168:171], v[192:195], v[20:23]\n"
        "ds_read_b64_tr_b16 v[58:59], v229 offset:16384\n"
        "v_mfma_f32_16x16x32_bf16 v[84:87], v[168:171], v[196:199], v[84:87]\n"
        "global_load_lds_dwordx4 v240, s[68:69] offset:2048\n"
        "v_mfma_f32_16x16x32_bf16 v[84:87], v[172:175], v[212:215], v[84:87]\n"
        "v_mfma_f32_16x16x32_bf16 v[80:83], v[164:167], v[212:215], v[80:83]\n"
        "v_mfma_f32_16x16x32_bf16 v[80:83], v[160:163], v[196:199], v[80:83]\n"
        "v_accvgpr_read_b32 v176, a144\n"
        "v_cvt_pk_bf16_f32 v176, v176, v177\n"
        "v_mfma_f32_16x16x32_bf16 v[112:115], v[160:163], v[200:203], v[112:115]\n"
        "v_cvt_pk_bf16_f32 v177, v178, v179\n"
        "v_cvt_pk_bf16_f32 v178, v180, v181\n"
        "v_mfma_f32_16x16x32_bf16 v[112:115], v[164:167], v[216:219], v[112:115]\n"
        "global_load_lds_dwordx4 v241, s[68:69] offset:3072\n"
        "s_add_u32 m0, s72, 0x10000\n"
        "v_cvt_pk_bf16_f32 v179, v182, v183\n"
        "v_mfma_f32_16x16x32_bf16 v[116:119], v[172:175], v[216:219], v[116:119]\n"
        "s_add_u32 s68, s68, 0x80000\n"
        "s_addc_u32 s69, s69, 0\n"
        "v_accvgpr_write_b32 a216, v176\n"
        "v_mfma_f32_16x16x32_bf16 v[116:119], v[168:171], v[200:203], v[116:119]\n"
        "v_accvgpr_write_b32 a217, v177\n"
        "v_accvgpr_write_b32 a218, v178\n"
        "v_mfma_f32_16x16x32_bf16 v[148:151], v[168:171], v[204:207], v[148:151]\n"
        "ds_read_b64_tr_b16 v[168:169], v232 offset:16384\n"
        "v_accvgpr_write_b32 a219, v179\n"
        "v_mfma_f32_16x16x32_bf16 v[148:151], v[172:175], v[220:223], v[148:151]\n"
        "ds_read_b64_tr_b16 v[172:173], v232 offset:24576\n"
        "global_load_lds_dwordx4 v242, s[64:65] offset:0\n"
        "v_mfma_f32_16x16x32_bf16 v[144:147], v[164:167], v[220:223], v[144:147]\n"
        "ds_read_b64_tr_b16 v[164:165], v230 offset:24576\n"
        "v_accvgpr_read_b32 v177, a153\n"
        "v_mfma_f32_16x16x32_bf16 v[144:147], v[160:163], v[204:207], v[144:147]\n"
        "ds_read_b64_tr_b16 v[160:161], v230 offset:16384\n"
        "v_accvgpr_read_b32 v178, a154\n"
        "v_mfma_f32_16x16x32_bf16 v[24:27], v[32:35], v[192:195], v[24:27]\n"
        "ds_read_b64_tr_b16 v[162:163], v231 offset:16384\n"
        "v_accvgpr_read_b32 v179, a155\n"
        "v_mfma_f32_16x16x32_bf16 v[24:27], v[36:39], v[208:211], v[24:27]\n"
        "ds_read_b64_tr_b16 v[166:167], v231 offset:24576\n"
        "global_load_lds_dwordx4 v243, s[64:65] offset:1024\n"
        "v_mfma_f32_16x16x32_bf16 v[28:31], v[44:47], v[208:211], v[28:31]\n"
        "ds_read_b64_tr_b16 v[174:175], v233 offset:24576\n"
        "v_accvgpr_read_b32 v180, a184\n"
        "v_mfma_f32_16x16x32_bf16 v[28:31], v[40:43], v[192:195], v[28:31]\n"
        "ds_read_b64_tr_b16 v[170:171], v233 offset:16384\n"
        "v_accvgpr_read_b32 v181, a185\n"
        "v_mfma_f32_16x16x32_bf16 v[92:95], v[40:43], v[196:199], v[92:95]\n"
        "ds_read_b128 v[192:195], v246 offset:34816\n"
        "v_accvgpr_read_b32 v182, a186\n"
        "v_mfma_f32_16x16x32_bf16 v[92:95], v[44:47], v[212:215], v[92:95]\n"
        "ds_read_b128 v[208:211], v247 offset:34816\n"
        "global_load_lds_dwordx4 v244, s[64:65] offset:2048\n"
        "v_mfma_f32_16x16x32_bf16 v[88:91], v[36:39], v[212:215], v[88:91]\n"
        "ds_read_b128 v[212:215], v247 offset:32768\n"
        "v_accvgpr_read_b32 v183, a187\n"
        "v_mfma_f32_16x16x32_bf16 v[88:91], v[32:35], v[196:199], v[88:91]\n"
        "ds_read_b128 v[196:199], v246 offset:32768\n"
        "v_mfma_f32_16x16x32_bf16 v[120:123], v[32:35], v[200:203], v[120:123]\n"
        "v_mfma_f32_16x16x32_bf16 v[120:123], v[36:39], v[216:219], v[120:123]\n"
        "global_load_lds_dwordx4 v245, s[64:65] offset:3072\n"
        "s_add_u32 m0, s72, 0x11000\n"
        "v_mfma_f32_16x16x32_bf16 v[124:127], v[44:47], v[216:219], v[124:127]\n"
        "ds_read_b128 v[216:219], v247 offset:38912\n"
        "s_add_u32 s64, s64, 0x80\n"
        "s_addc_u32 s65, s65, 0\n"
        "v_mfma_f32_16x16x32_bf16 v[124:127], v[40:43], v[200:203], v[124:127]\n"
        "ds_read_b128 v[200:203], v246 offset:38912\n"
        "v_mfma_f32_16x16x32_bf16 v[156:159], v[40:43], v[204:207], v[156:159]\n"
        "ds_read_b64_tr_b16 v[40:41], v228 offset:17408\n"
        "s_waitcnt lgkmcnt(6)\n"
        "v_mfma_f32_16x16x32_bf16 v[156:159], v[44:47], v[220:223], v[156:159]\n"
        "ds_read_b64_tr_b16 v[44:45], v228 offset:25600\n"
        "global_load_lds_dwordx4 v242, s[66:67] offset:0\n"
        "v_mfma_f32_16x16x32_bf16 v[152:155], v[36:39], v[220:223], v[152:155]\n"
        "ds_read_b128 v[220:223], v247 offset:36864\n"
        "s_waitcnt lgkmcnt(6)\n"
        "v_mfma_f32_16x16x32_bf16 v[152:155], v[32:35], v[204:207], v[152:155]\n"
        "ds_read_b128 v[204:207], v246 offset:36864\n"
        "v_accvgpr_read_b32 v184, a148\n"
        "v_mfma_f32_16x16x32_bf16 v[0:3], v[48:51], v[192:195], v[0:3]\n"
        "ds_read_b64_tr_b16 v[32:33], v226 offset:17408\n"
        "v_cvt_pk_bf16_f32 v184, v184, v185\n"
        "v_mfma_f32_16x16x32_bf16 v[0:3], v[52:55], v[208:211], v[0:3]\n"
        "ds_read_b64_tr_b16 v[34:35], v227 offset:17408\n"
        "v_cvt_pk_bf16_f32 v185, v186, v187\n"
        "v_mfma_f32_16x16x32_bf16 v[4:7], v[60:63], v[208:211], v[4:7]\n"
        "ds_read_b64_tr_b16 v[36:37], v226 offset:25600\n"
        "v_cvt_pk_bf16_f32 v186, v188, v189\n"
        "v_mfma_f32_16x16x32_bf16 v[4:7], v[56:59], v[192:195], v[4:7]\n"
        "ds_read_b64_tr_b16 v[38:39], v227 offset:25600\n"
        "s_waitcnt lgkmcnt(8)\n"
        "v_mfma_f32_16x16x32_bf16 v[68:71], v[56:59], v[196:199], v[68:71]\n"
        "ds_read_b64_tr_b16 v[46:47], v229 offset:25600\n"
        "v_cvt_pk_bf16_f32 v187, v190, v191\n"
        "v_mfma_f32_16x16x32_bf16 v[68:71], v[60:63], v[212:215], v[68:71]\n"
        "ds_read_b64_tr_b16 v[42:43], v229 offset:17408\n"
        "v_accvgpr_write_b32 a248, v184\n"
        "v_mfma_f32_16x16x32_bf16 v[64:67], v[52:55], v[212:215], v[64:67]\n"
        "global_load_lds_dwordx4 v243, s[66:67] offset:1024\n"
        "v_accvgpr_write_b32 a249, v185\n"
        "v_mfma_f32_16x16x32_bf16 v[64:67], v[48:51], v[196:199], v[64:67]\n"
        "v_accvgpr_write_b32 a250, v186\n"
        "v_accvgpr_write_b32 a251, v187\n"
        "v_mfma_f32_16x16x32_bf16 v[96:99], v[48:51], v[200:203], v[96:99]\n"
        "s_waitcnt lgkmcnt(5)\n"
        "v_accvgpr_read_b32 v185, a157\n"
        "v_accvgpr_read_b32 v186, a158\n"
        "v_mfma_f32_16x16x32_bf16 v[96:99], v[52:55], v[216:219], v[96:99]\n"
        "v_accvgpr_read_b32 v187, a159\n"
        "v_accvgpr_read_b32 v188, a188\n"
        "v_mfma_f32_16x16x32_bf16 v[100:103], v[60:63], v[216:219], v[100:103]\n"
        "global_load_lds_dwordx4 v244, s[66:67] offset:2048\n"
        "v_accvgpr_read_b32 v189, a189\n"
        "v_mfma_f32_16x16x32_bf16 v[100:103], v[56:59], v[200:203], v[100:103]\n"
        "v_accvgpr_read_b32 v190, a190\n"
        "v_accvgpr_read_b32 v191, a191\n"
        "v_mfma_f32_16x16x32_bf16 v[132:135], v[56:59], v[204:207], v[132:135]\n"
        "ds_read_b64_tr_b16 v[56:57], v232 offset:17408\n"
        "v_mfma_f32_16x16x32_bf16 v[132:135], v[60:63], v[220:223], v[132:135]\n"
        "ds_read_b64_tr_b16 v[60:61], v232 offset:25600\n"
        "v_mfma_f32_16x16x32_bf16 v[128:131], v[52:55], v[220:223], v[128:131]\n"
        "ds_read_b64_tr_b16 v[52:53], v230 offset:25600\n"
        "global_load_lds_dwordx4 v245, s[66:67] offset:3072\n"
        "s_add_u32 m0, s73, 0x1c000\n"
        "v_mfma_f32_16x16x32_bf16 v[128:131], v[48:51], v[204:207], v[128:131]\n"
        "ds_read_b64_tr_b16 v[48:49], v230 offset:17408\n"
        "s_add_u32 s66, s66, 0x80\n"
        "s_addc_u32 s67, s67, 0\n"
        "v_mfma_f32_16x16x32_bf16 v[8:11], v[160:163], v[192:195], v[8:11]\n"
        "ds_read_b64_tr_b16 v[50:51], v231 offset:17408\n"
        "v_mfma_f32_16x16x32_bf16 v[8:11], v[164:167], v[208:211], v[8:11]\n"
        "ds_read_b64_tr_b16 v[54:55], v231 offset:25600\n"
        "v_mfma_f32_16x16x32_bf16 v[12:15], v[172:175], v[208:211], v[12:15]\n"
        "ds_read_b64_tr_b16 v[62:63], v233 offset:25600\n"
        "v_mfma_f32_16x16x32_bf16 v[12:15], v[168:171], v[192:195], v[12:15]\n"
        "ds_read_b64_tr_b16 v[58:59], v233 offset:17408\n"
        "v_mfma_f32_16x16x32_bf16 v[76:79], v[168:171], v[196:199], v[76:79]\n"
        "global_store_dwordx4 v254, a[204:207], s[70:71] offset:1024 sc1\n"
        "s_add_u32 s70, s38, s97\n"
        "s_addc_u32 s71, s39, 0\n"
        "v_mfma_f32_16x16x32_bf16 v[76:79], v[172:175], v[212:215], v[76:79]\n"
        "v_mfma_f32_16x16x32_bf16 v[72:75], v[164:167], v[212:215], v[72:75]\n"
        "v_mfma_f32_16x16x32_bf16 v[72:75], v[160:163], v[196:199], v[72:75]\n"
        "v_accvgpr_read_b32 v176, a152\n"
        "v_cvt_pk_bf16_f32 v176, v176, v177\n"
        "v_mfma_f32_16x16x32_bf16 v[104:107], v[160:163], v[200:203], v[104:107]\n"
        "v_cvt_pk_bf16_f32 v177, v178, v179\n"
        "v_cvt_pk_bf16_f32 v178, v180, v181\n"
        "v_mfma_f32_16x16x32_bf16 v[104:107], v[164:167], v[216:219], v[104:107]\n"
        "v_cvt_pk_bf16_f32 v179, v182, v183\n"
        "v_accvgpr_write_b32 a220, v176\n"
        "v_mfma_f32_16x16x32_bf16 v[108:111], v[172:175], v[216:219], v[108:111]\n"
        "s_waitcnt vmcnt(14) lgkmcnt(0)\n"
        "v_accvgpr_write_b32 a221, v177\n"
        "v_mfma_f32_16x16x32_bf16 v[108:111], v[168:171], v[200:203], v[108:111]\n"
        "s_barrier\n"
        "v_mfma_f32_16x16x32_bf16 v[140:143], v[168:171], v[204:207], v[140:143]\n"
        "ds_read_b64_tr_b16 v[168:169], v228 offset:32768\n"
        "v_accvgpr_write_b32 a222, v178\n"
        "v_mfma_f32_16x16x32_bf16 v[140:143], v[172:175], v[220:223], v[140:143]\n"
        "ds_read_b64_tr_b16 v[172:173], v228 offset:40960\n"
        "v_accvgpr_write_b32 a223, v179\n"
        "v_mfma_f32_16x16x32_bf16 v[136:139], v[164:167], v[220:223], v[136:139]\n"
        "ds_read_b64_tr_b16 v[164:165], v226 offset:40960\n"
        "global_load_lds_dwordx4 v238, s[68:69] offset:0\n"
        "v_mfma_f32_16x16x32_bf16 v[136:139], v[160:163], v[204:207], v[136:139]\n"
        "ds_read_b64_tr_b16 v[160:161], v226 offset:32768\n"
        "s_add_u32 s97, s52, 0x2000\n"
        "v_accvgpr_read_b32 v177, a17\n"
        "v_mfma_f32_16x16x32_bf16 v[16:19], v[32:35], v[192:195], v[16:19]\n"
        "ds_read_b64_tr_b16 v[162:163], v227 offset:32768\n"
        "s_add_u32 s97, s97, s75\n"
        "v_accvgpr_read_b32 v178, a18\n"
        "v_mfma_f32_16x16x32_bf16 v[16:19], v[36:39], v[208:211], v[16:19]\n"
        "ds_read_b64_tr_b16 v[166:167], v227 offset:40960\n"
        "v_accvgpr_read_b32 v179, a19\n"
        "v_mfma_f32_16x16x32_bf16 v[20:23], v[44:47], v[208:211], v[20:23]\n"
        "ds_read_b64_tr_b16 v[174:175], v229 offset:40960\n"
        "global_load_lds_dwordx4 v239, s[68:69] offset:1024\n"
        "v_mfma_f32_16x16x32_bf16 v[20:23], v[40:43], v[192:195], v[20:23]\n"
        "ds_read_b64_tr_b16 v[170:171], v229 offset:32768\n"
        "v_accvgpr_read_b32 v180, a48\n"
        "v_mfma_f32_16x16x32_bf16 v[84:87], v[40:43], v[196:199], v[84:87]\n"
        "v_accvgpr_read_b32 v181, a49\n"
        "v_accvgpr_read_b32 v182, a50\n"
        "v_mfma_f32_16x16x32_bf16 v[84:87], v[44:47], v[212:215], v[84:87]\n"
        "v_accvgpr_read_b32 v183, a51\n"
        "v_accvgpr_write_b32 a48, 0\n"
        "v_mfma_f32_16x16x32_bf16 v[80:83], v[36:39], v[212:215], v[80:83]\n"
        "global_load_lds_dwordx4 v240, s[68:69] offset:2048\n"
        "v_mfma_f32_16x16x32_bf16 v[80:83], v[32:35], v[196:199], v[80:83]\n"
        "v_accvgpr_write_b32 a49, 0\n"
        "v_accvgpr_write_b32 a50, 0\n"
        "v_mfma_f32_16x16x32_bf16 v[112:115], v[32:35], v[200:203], v[112:115]\n"
        "v_accvgpr_write_b32 a51, 0\n"
        "v_mfma_f32_16x16x32_bf16 v[112:115], v[36:39], v[216:219], v[112:115]\n"
        "v_mfma_f32_16x16x32_bf16 v[116:119], v[44:47], v[216:219], v[116:119]\n"
        "global_load_lds_dwordx4 v241, s[68:69] offset:3072\n"
        "s_add_u32 m0, s72, 0x8000\n"
        "v_mfma_f32_16x16x32_bf16 v[116:119], v[40:43], v[200:203], v[116:119]\n"
        "s_add_u32 s68, s68, 0x80000\n"
        "s_addc_u32 s69, s69, 0\n"
        "v_mfma_f32_16x16x32_bf16 v[148:151], v[40:43], v[204:207], v[148:151]\n"
        "ds_read_b64_tr_b16 v[40:41], v232 offset:32768\n"
        "v_mfma_f32_16x16x32_bf16 v[148:151], v[44:47], v[220:223], v[148:151]\n"
        "ds_read_b64_tr_b16 v[44:45], v232 offset:40960\n"
        "v_mfma_f32_16x16x32_bf16 v[144:147], v[36:39], v[220:223], v[144:147]\n"
        "ds_read_b64_tr_b16 v[36:37], v230 offset:40960\n"
        "global_load_lds_dwordx4 v242, s[64:65] offset:0\n"
        "v_mfma_f32_16x16x32_bf16 v[144:147], v[32:35], v[204:207], v[144:147]\n"
        "ds_read_b64_tr_b16 v[32:33], v230 offset:32768\n"
        "v_accvgpr_read_b32 v184, a156\n"
        "v_mfma_f32_16x16x32_bf16 v[24:27], v[48:51], v[192:195], v[24:27]\n"
        "ds_read_b64_tr_b16 v[34:35], v231 offset:32768\n"
        "v_cvt_pk_bf16_f32 v184, v184, v185\n"
        "v_mfma_f32_16x16x32_bf16 v[24:27], v[52:55], v[208:211], v[24:27]\n"
        "ds_read_b64_tr_b16 v[38:39], v231 offset:40960\n"
        "v_cvt_pk_bf16_f32 v185, v186, v187\n"
        "LHA7_%=:\n"
        "s_cbranch_execz LHA11_%=\n"
        "v_mfma_f32_16x16x32_bf16 v[28:31], v[60:63], v[208:211], v[28:31]\n"
        "ds_read_b64_tr_b16 v[46:47], v233 offset:40960\n"
        "v_cvt_pk_bf16_f32 v186, v188, v189\n"
        "v_mfma_f32_16x16x32_bf16 v[28:31], v[56:59], v[192:195], v[28:31]\n"
        "ds_read_b64_tr_b16 v[42:43], v233 offset:32768\n"
        "v_cvt_pk_bf16_f32 v187, v190, v191\n"
        "v_mfma_f32_16x16x32_bf16 v[92:95], v[56:59], v[196:199], v[92:95]\n"
        "ds_read_b128 v[192:195], v246 offset:2048\n"
        "v_accvgpr_write_b32 a252, v184\n"
        "v_mfma_f32_16x16x32_bf16 v[92:95], v[60:63], v[212:215], v[92:95]\n"
        "ds_read_b128 v[208:211], v247 offset:2048\n"
        "v_accvgpr_write_b32 a253, v185\n"
        "v_mfma_f32_16x16x32_bf16 v[88:91], v[52:55], v[212:215], v[88:91]\n"
        "ds_read_b128 v[212:215], v247 offset:0\n"
        "v_accvgpr_write_b32 a254, v186\n"
        "v_mfma_f32_16x16x32_bf16 v[88:91], v[48:51], v[196:199], v[88:91]\n"
        "ds_read_b128 v[196:199], v246 offset:0\n"
        "v_accvgpr_write_b32 a255, v187\n"
        "v_mfma_f32_16x16x32_bf16 v[120:123], v[48:51], v[200:203], v[120:123]\n"
        "global_load_lds_dwordx4 v243, s[64:65] offset:1024\n"
        "v_accvgpr_read_b32 v185, a21\n"
        "v_mfma_f32_16x16x32_bf16 v[120:123], v[52:55], v[216:219], v[120:123]\n"
        "v_accvgpr_read_b32 v186, a22\n"
        "v_accvgpr_read_b32 v187, a23\n"
        "v_mfma_f32_16x16x32_bf16 v[124:127], v[60:63], v[216:219], v[124:127]\n"
        "ds_read_b128 v[216:219], v247 offset:6144\n"
        "v_accvgpr_read_b32 v188, a52\n"
        "v_mfma_f32_16x16x32_bf16 v[124:127], v[56:59], v[200:203], v[124:127]\n"
        "ds_read_b128 v[200:203], v246 offset:6144\n"
        "v_accvgpr_read_b32 v189, a53\n"
        "v_mfma_f32_16x16x32_bf16 v[156:159], v[56:59], v[204:207], v[156:159]\n"
        "ds_read_b64_tr_b16 v[56:57], v228 offset:33792\n"
        "global_load_lds_dwordx4 v244, s[64:65] offset:2048\n"
        "v_mfma_f32_16x16x32_bf16 v[156:159], v[60:63], v[220:223], v[156:159]\n"
        "ds_read_b64_tr_b16 v[60:61], v228 offset:41984\n"
        "v_accvgpr_read_b32 v190, a54\n"
        "v_mfma_f32_16x16x32_bf16 v[152:155], v[52:55], v[220:223], v[152:155]\n"
        "ds_read_b128 v[220:223], v247 offset:4096\n"
        "v_accvgpr_read_b32 v191, a55\n"
        "v_mfma_f32_16x16x32_bf16 v[152:155], v[48:51], v[204:207], v[152:155]\n"
        "ds_read_b128 v[204:207], v246 offset:4096\n"
        "s_waitcnt lgkmcnt(6)\n"
        "v_mfma_f32_16x16x32_bf16 v[0:3], v[160:163], v[192:195], v[0:3]\n"
        "ds_read_b64_tr_b16 v[48:49], v226 offset:33792\n"
        "global_load_lds_dwordx4 v245, s[64:65] offset:3072\n"
        "s_add_u32 m0, s72, 0x9000\n"
        "v_mfma_f32_16x16x32_bf16 v[0:3], v[164:167], v[208:211], v[0:3]\n"
        "ds_read_b64_tr_b16 v[50:51], v227 offset:33792\n"
        "s_add_u32 s64, s64, 0x80\n"
        "s_addc_u32 s65, s65, 0\n"
        "v_mfma_f32_16x16x32_bf16 v[4:7], v[172:175], v[208:211], v[4:7]\n"
        "ds_read_b64_tr_b16 v[52:53], v226 offset:41984\n"
        "v_accvgpr_write_b32 a52, 0\n"
        "v_mfma_f32_16x16x32_bf16 v[4:7], v[168:171], v[192:195], v[4:7]\n"
        "ds_read_b64_tr_b16 v[54:55], v227 offset:41984\n"
        "v_accvgpr_write_b32 a53, 0\n"
        "v_mfma_f32_16x16x32_bf16 v[68:71], v[168:171], v[196:199], v[68:71]\n"
        "ds_read_b64_tr_b16 v[62:63], v229 offset:41984\n"
        "global_load_lds_dwordx4 v242, s[66:67] offset:0\n"
        "v_mfma_f32_16x16x32_bf16 v[68:71], v[172:175], v[212:215], v[68:71]\n"
        "ds_read_b64_tr_b16 v[58:59], v229 offset:33792\n"
        "v_accvgpr_write_b32 a54, 0\n"
        "v_mfma_f32_16x16x32_bf16 v[64:67], v[164:167], v[212:215], v[64:67]\n"
        "s_waitcnt lgkmcnt(7)\n"
        "v_accvgpr_write_b32 a55, 0\n"
        "v_mfma_f32_16x16x32_bf16 v[64:67], v[160:163], v[196:199], v[64:67]\n"
        "v_accvgpr_read_b32 v176, a16\n"
        "v_cvt_pk_bf16_f32 v176, v176, v177\n"
        "v_mfma_f32_16x16x32_bf16 v[96:99], v[160:163], v[200:203], v[96:99]\n"
        "global_load_lds_dwordx4 v243, s[66:67] offset:1024\n"
        "v_mfma_f32_16x16x32_bf16 v[96:99], v[164:167], v[216:219], v[96:99]\n"
        "s_waitcnt lgkmcnt(4)\n"
        "v_cvt_pk_bf16_f32 v177, v178, v179\n"
        "v_cvt_pk_bf16_f32 v178, v180, v181\n"
        "v_mfma_f32_16x16x32_bf16 v[100:103], v[172:175], v[216:219], v[100:103]\n"
        "v_cvt_pk_bf16_f32 v179, v182, v183\n"
        "v_accvgpr_write_b32 a176, v176\n"
        "v_mfma_f32_16x16x32_bf16 v[100:103], v[168:171], v[200:203], v[100:103]\n"
        "v_accvgpr_write_b32 a177, v177\n"
        "v_accvgpr_write_b32 a178, v178\n"
        "v_mfma_f32_16x16x32_bf16 v[132:135], v[168:171], v[204:207], v[132:135]\n"
        "ds_read_b64_tr_b16 v[168:169], v232 offset:33792\n"
        "v_accvgpr_write_b32 a179, v179\n"
        "v_mfma_f32_16x16x32_bf16 v[132:135], v[172:175], v[220:223], v[132:135]\n"
        "ds_read_b64_tr_b16 v[172:173], v232 offset:41984\n"
        "global_load_lds_dwordx4 v244, s[66:67] offset:2048\n"
        "v_mfma_f32_16x16x32_bf16 v[128:131], v[164:167], v[220:223], v[128:131]\n"
        "ds_read_b64_tr_b16 v[164:165], v230 offset:41984\n"
        "v_accvgpr_read_b32 v177, a25\n"
        "v_mfma_f32_16x16x32_bf16 v[128:131], v[160:163], v[204:207], v[128:131]\n"
        "ds_read_b64_tr_b16 v[160:161], v230 offset:33792\n"
        "v_accvgpr_read_b32 v178, a26\n"
        "v_mfma_f32_16x16x32_bf16 v[8:11], v[32:35], v[192:195], v[8:11]\n"
        "ds_read_b64_tr_b16 v[162:163], v231 offset:33792\n"
        "v_accvgpr_read_b32 v179, a27\n"
        "v_mfma_f32_16x16x32_bf16 v[8:11], v[36:39], v[208:211], v[8:11]\n"
        "ds_read_b64_tr_b16 v[166:167], v231 offset:41984\n"
        "global_load_lds_dwordx4 v245, s[66:67] offset:3072\n"
        "s_add_u32 m0, s73, 0x20000\n"
        "v_mfma_f32_16x16x32_bf16 v[12:15], v[44:47], v[208:211], v[12:15]\n"
        "ds_read_b64_tr_b16 v[174:175], v233 offset:41984\n"
        "s_add_u32 s66, s66, 0x80\n"
        "s_addc_u32 s67, s67, 0\n"
        "v_mfma_f32_16x16x32_bf16 v[12:15], v[40:43], v[192:195], v[12:15]\n"
        "ds_read_b64_tr_b16 v[170:171], v233 offset:33792\n"
        "v_accvgpr_read_b32 v180, a56\n"
        "v_mfma_f32_16x16x32_bf16 v[76:79], v[40:43], v[196:199], v[76:79]\n"
        "v_accvgpr_read_b32 v181, a57\n"
        "v_accvgpr_read_b32 v182, a58\n"
        "v_mfma_f32_16x16x32_bf16 v[76:79], v[44:47], v[212:215], v[76:79]\n"
        "v_accvgpr_read_b32 v183, a59\n"
        "v_accvgpr_write_b32 a56, 0\n"
        "v_mfma_f32_16x16x32_bf16 v[72:75], v[36:39], v[212:215], v[72:75]\n"
        "v_accvgpr_write_b32 a57, 0\n"
        "v_accvgpr_write_b32 a58, 0\n"
        "v_mfma_f32_16x16x32_bf16 v[72:75], v[32:35], v[196:199], v[72:75]\n"
        "v_accvgpr_write_b32 a59, 0\n"
        "v_mfma_f32_16x16x32_bf16 v[104:107], v[32:35], v[200:203], v[104:107]\n"
        "v_mfma_f32_16x16x32_bf16 v[104:107], v[36:39], v[216:219], v[104:107]\n"
        "v_mfma_f32_16x16x32_bf16 v[108:111], v[44:47], v[216:219], v[108:111]\n"
        "v_mfma_f32_16x16x32_bf16 v[108:111], v[40:43], v[200:203], v[108:111]\n"
        "global_store_dwordx4 v254, a[160:163], s[70:71] offset:0 sc1\n"
        "s_waitcnt vmcnt(14) lgkmcnt(0)\n"
        "s_barrier\n"
        "v_mfma_f32_16x16x32_bf16 v[140:143], v[40:43], v[204:207], v[140:143]\n"
        "ds_read_b64_tr_b16 v[40:41], v228 offset:49152\n"
        "v_mfma_f32_16x16x32_bf16 v[140:143], v[44:47], v[220:223], v[140:143]\n"
        "ds_read_b64_tr_b16 v[44:45], v228 offset:57344\n"
        "v_mfma_f32_16x16x32_bf16 v[136:139], v[36:39], v[220:223], v[136:139]\n"
        "ds_read_b64_tr_b16 v[36:37], v226 offset:57344\n"
        "global_load_lds_dwordx4 v238, s[68:69] offset:0\n"
        "v_mfma_f32_16x16x32_bf16 v[136:139], v[32:35], v[204:207], v[136:139]\n"
        "ds_read_b64_tr_b16 v[32:33], v226 offset:49152\n"
        "v_accvgpr_read_b32 v184, a20\n"
        "v_mfma_f32_16x16x32_bf16 v[16:19], v[48:51], v[192:195], v[16:19]\n"
        "ds_read_b64_tr_b16 v[34:35], v227 offset:49152\n"
        "v_cvt_pk_bf16_f32 v184, v184, v185\n"
        "v_mfma_f32_16x16x32_bf16 v[16:19], v[52:55], v[208:211], v[16:19]\n"
        "ds_read_b64_tr_b16 v[38:39], v227 offset:57344\n"
        "v_cvt_pk_bf16_f32 v185, v186, v187\n"
        "v_mfma_f32_16x16x32_bf16 v[20:23], v[60:63], v[208:211], v[20:23]\n"
        "ds_read_b64_tr_b16 v[46:47], v229 offset:57344\n"
        "v_cvt_pk_bf16_f32 v186, v188, v189\n"
        "v_mfma_f32_16x16x32_bf16 v[20:23], v[56:59], v[192:195], v[20:23]\n"
        "ds_read_b64_tr_b16 v[42:43], v229 offset:49152\n"
        "v_cvt_pk_bf16_f32 v187, v190, v191\n"
        "v_mfma_f32_16x16x32_bf16 v[84:87], v[56:59], v[196:199], v[84:87]\n"
        "global_load_lds_dwordx4 v239, s[68:69] offset:1024\n"
        "v_accvgpr_write_b32 a180, v184\n"
        "v_mfma_f32_16x16x32_bf16 v[84:87], v[60:63], v[212:215], v[84:87]\n"
        "v_accvgpr_write_b32 a181, v185\n"
        "v_accvgpr_write_b32 a182, v186\n"
        "v_mfma_f32_16x16x32_bf16 v[80:83], v[52:55], v[212:215], v[80:83]\n"
        "v_accvgpr_write_b32 a183, v187\n"
        "v_accvgpr_read_b32 v185, a29\n"
        "v_mfma_f32_16x16x32_bf16 v[80:83], v[48:51], v[196:199], v[80:83]\n"
        "v_accvgpr_read_b32 v186, a30\n"
        "v_accvgpr_read_b32 v187, a31\n"
        "v_mfma_f32_16x16x32_bf16 v[112:115], v[48:51], v[200:203], v[112:115]\n"
        "global_load_lds_dwordx4 v240, s[68:69] offset:2048\n"
        "v_accvgpr_read_b32 v188, a60\n"
        "v_mfma_f32_16x16x32_bf16 v[112:115], v[52:55], v[216:219], v[112:115]\n"
        "v_accvgpr_read_b32 v189, a61\n"
        "v_accvgpr_read_b32 v190, a62\n"
        "v_mfma_f32_16x16x32_bf16 v[116:119], v[60:63], v[216:219], v[116:119]\n"
        "v_accvgpr_read_b32 v191, a63\n"
        "v_accvgpr_write_b32 a60, 0\n"
        "v_mfma_f32_16x16x32_bf16 v[116:119], v[56:59], v[200:203], v[116:119]\n"
        "v_accvgpr_write_b32 a61, 0\n"
        "v_accvgpr_write_b32 a62, 0\n"
        "v_mfma_f32_16x16x32_bf16 v[148:151], v[56:59], v[204:207], v[148:151]\n"
        "ds_read_b64_tr_b16 v[56:57], v232 offset:49152\n"
        "global_load_lds_dwordx4 v241, s[68:69] offset:3072\n"
        "s_add_u32 m0, s72, 0x0\n"
        "v_mfma_f32_16x16x32_bf16 v[148:151], v[60:63], v[220:223], v[148:151]\n"
        "ds_read_b64_tr_b16 v[60:61], v232 offset:57344\n"
        "s_add_u32 s68, s68, 0x80000\n"
        "s_addc_u32 s69, s69, 0\n"
        "v_mfma_f32_16x16x32_bf16 v[144:147], v[52:55], v[220:223], v[144:147]\n"
        "ds_read_b64_tr_b16 v[52:53], v230 offset:57344\n"
        "v_accvgpr_write_b32 a63, 0\n"
        "v_mfma_f32_16x16x32_bf16 v[144:147], v[48:51], v[204:207], v[144:147]\n"
        "ds_read_b64_tr_b16 v[48:49], v230 offset:49152\n"
        "v_mfma_f32_16x16x32_bf16 v[24:27], v[160:163], v[192:195], v[24:27]\n"
        "ds_read_b64_tr_b16 v[50:51], v231 offset:49152\n"
        "global_load_lds_dwordx4 v242, s[64:65] offset:0\n"
        "v_mfma_f32_16x16x32_bf16 v[24:27], v[164:167], v[208:211], v[24:27]\n"
        "ds_read_b64_tr_b16 v[54:55], v231 offset:57344\n"
        "v_mfma_f32_16x16x32_bf16 v[28:31], v[172:175], v[208:211], v[28:31]\n"
        "ds_read_b64_tr_b16 v[62:63], v233 offset:57344\n"
        "v_mfma_f32_16x16x32_bf16 v[28:31], v[168:171], v[192:195], v[28:31]\n"
        "ds_read_b64_tr_b16 v[58:59], v233 offset:49152\n"
        "v_mfma_f32_16x16x32_bf16 v[92:95], v[168:171], v[196:199], v[92:95]\n"
        "ds_read_b128 v[192:195], v248 offset:2048\n"
        "global_load_lds_dwordx4 v243, s[64:65] offset:1024\n"
        "v_mfma_f32_16x16x32_bf16 v[92:95], v[172:175], v[212:215], v[92:95]\n"
        "ds_read_b128 v[208:211], v249 offset:2048\n"
        "v_mfma_f32_16x16x32_bf16 v[88:91], v[164:167], v[212:215], v[88:91]\n"
        "ds_read_b128 v[212:215], v249 offset:0\n"
        "v_mfma_f32_16x16x32_bf16 v[88:91], v[160:163], v[196:199], v[88:91]\n"
        "ds_read_b128 v[196:199], v248 offset:0\n"
        "v_accvgpr_read_b32 v176, a24\n"
        "v_mfma_f32_16x16x32_bf16 v[120:123], v[160:163], v[200:203], v[120:123]\n"
        "v_cvt_pk_bf16_f32 v176, v176, v177\n"
        "v_cvt_pk_bf16_f32 v177, v178, v179\n"
        "v_mfma_f32_16x16x32_bf16 v[120:123], v[164:167], v[216:219], v[120:123]\n"
        "global_load_lds_dwordx4 v244, s[64:65] offset:2048\n"
        "v_cvt_pk_bf16_f32 v178, v180, v181\n"
        "v_mfma_f32_16x16x32_bf16 v[124:127], v[172:175], v[216:219], v[124:127]\n"
        "ds_read_b128 v[216:219], v249 offset:6144\n"
        "v_cvt_pk_bf16_f32 v179, v182, v183\n"
        "v_mfma_f32_16x16x32_bf16 v[124:127], v[168:171], v[200:203], v[124:127]\n"
        "ds_read_b128 v[200:203], v248 offset:6144\n"
        "v_accvgpr_write_b32 a184, v176\n"
        "v_mfma_f32_16x16x32_bf16 v[156:159], v[168:171], v[204:207], v[156:159]\n"
        "ds_read_b64_tr_b16 v[168:169], v228 offset:50176\n"
        "v_accvgpr_write_b32 a185, v177\n"
        "v_mfma_f32_16x16x32_bf16 v[156:159], v[172:175], v[220:223], v[156:159]\n"
        "ds_read_b64_tr_b16 v[172:173], v228 offset:58368\n"
        "v_accvgpr_write_b32 a186, v178\n"
        "v_mfma_f32_16x16x32_bf16 v[152:155], v[164:167], v[220:223], v[152:155]\n"
        "ds_read_b128 v[220:223], v249 offset:4096\n"
        "v_accvgpr_write_b32 a187, v179\n"
        "v_mfma_f32_16x16x32_bf16 v[152:155], v[160:163], v[204:207], v[152:155]\n"
        "ds_read_b128 v[204:207], v248 offset:4096\n"
        "s_waitcnt lgkmcnt(6)\n"
        "v_mfma_f32_16x16x32_bf16 v[0:3], v[32:35], v[192:195], v[0:3]\n"
        "ds_read_b64_tr_b16 v[160:161], v226 offset:50176\n"
        "global_load_lds_dwordx4 v245, s[64:65] offset:3072\n"
        "s_add_u32 m0, s72, 0x1000\n"
        "v_mfma_f32_16x16x32_bf16 v[0:3], v[36:39], v[208:211], v[0:3]\n"
        "ds_read_b64_tr_b16 v[162:163], v227 offset:50176\n"
        "s_add_u32 s64, s64, 0x80\n"
        "s_addc_u32 s65, s65, 0\n"
        "v_mfma_f32_16x16x32_bf16 v[4:7], v[44:47], v[208:211], v[4:7]\n"
        "ds_read_b64_tr_b16 v[164:165], v226 offset:58368\n"
        "v_accvgpr_read_b32 v177, a81\n"
        "v_mfma_f32_16x16x32_bf16 v[4:7], v[40:43], v[192:195], v[4:7]\n"
        "ds_read_b64_tr_b16 v[166:167], v227 offset:58368\n"
        "v_accvgpr_read_b32 v178, a82\n"
        "v_mfma_f32_16x16x32_bf16 v[68:71], v[40:43], v[196:199], v[68:71]\n"
        "ds_read_b64_tr_b16 v[174:175], v229 offset:58368\n"
        "global_load_lds_dwordx4 v242, s[66:67] offset:0\n"
        "v_mfma_f32_16x16x32_bf16 v[68:71], v[44:47], v[212:215], v[68:71]\n"
        "ds_read_b64_tr_b16 v[170:171], v229 offset:50176\n"
        "v_accvgpr_read_b32 v179, a83\n"
        "v_mfma_f32_16x16x32_bf16 v[64:67], v[36:39], v[212:215], v[64:67]\n"
        "s_waitcnt lgkmcnt(7)\n"
        "v_accvgpr_read_b32 v180, a112\n"
        "v_accvgpr_read_b32 v181, a113\n"
        "v_mfma_f32_16x16x32_bf16 v[64:67], v[32:35], v[196:199], v[64:67]\n"
        "v_accvgpr_read_b32 v182, a114\n"
        "v_accvgpr_read_b32 v183, a115\n"
        "v_mfma_f32_16x16x32_bf16 v[96:99], v[32:35], v[200:203], v[96:99]\n"
        "global_load_lds_dwordx4 v243, s[66:67] offset:1024\n"
        "v_mfma_f32_16x16x32_bf16 v[96:99], v[36:39], v[216:219], v[96:99]\n"
        "s_waitcnt lgkmcnt(4)\n"
        "v_mfma_f32_16x16x32_bf16 v[100:103], v[44:47], v[216:219], v[100:103]\n"
        "v_mfma_f32_16x16x32_bf16 v[100:103], v[40:43], v[200:203], v[100:103]\n"
        "v_mfma_f32_16x16x32_bf16 v[132:135], v[40:43], v[204:207], v[132:135]\n"
        "ds_read_b64_tr_b16 v[40:41], v232 offset:50176\n"
        "global_load_lds_dwordx4 v244, s[66:67] offset:2048\n"
        "v_mfma_f32_16x16x32_bf16 v[132:135], v[44:47], v[220:223], v[132:135]\n"
        "ds_read_b64_tr_b16 v[44:45], v232 offset:58368\n"
        "v_mfma_f32_16x16x32_bf16 v[128:131], v[36:39], v[220:223], v[128:131]\n"
        "ds_read_b64_tr_b16 v[36:37], v230 offset:58368\n"
        "v_mfma_f32_16x16x32_bf16 v[128:131], v[32:35], v[204:207], v[128:131]\n"
        "ds_read_b64_tr_b16 v[32:33], v230 offset:50176\n"
        "v_accvgpr_read_b32 v184, a28\n"
        "v_mfma_f32_16x16x32_bf16 v[8:11], v[48:51], v[192:195], v[8:11]\n"
        "ds_read_b64_tr_b16 v[34:35], v231 offset:50176\n"
        "global_load_lds_dwordx4 v245, s[66:67] offset:3072\n"
        "s_add_u32 m0, s73, 0x24000\n"
        "v_mfma_f32_16x16x32_bf16 v[8:11], v[52:55], v[208:211], v[8:11]\n"
        "ds_read_b64_tr_b16 v[38:39], v231 offset:58368\n"
        "v_cvt_pk_bf16_f32 v184, v184, v185\n"
        "v_mfma_f32_16x16x32_bf16 v[12:15], v[60:63], v[208:211], v[12:15]\n"
        "ds_read_b64_tr_b16 v[46:47], v233 offset:58368\n"
        "v_cvt_pk_bf16_f32 v185, v186, v187\n"
        "v_mfma_f32_16x16x32_bf16 v[12:15], v[56:59], v[192:195], v[12:15]\n"
        "ds_read_b64_tr_b16 v[42:43], v233 offset:50176\n"
        "v_cvt_pk_bf16_f32 v186, v188, v189\n"
        "v_mfma_f32_16x16x32_bf16 v[76:79], v[56:59], v[196:199], v[76:79]\n"
        "s_add_u32 s66, s66, 0x80\n"
        "s_addc_u32 s67, s67, 0\n"
        "v_cvt_pk_bf16_f32 v187, v190, v191\n"
        "v_mfma_f32_16x16x32_bf16 v[76:79], v[60:63], v[212:215], v[76:79]\n"
        "v_accvgpr_write_b32 a188, v184\n"
        "v_accvgpr_write_b32 a189, v185\n"
        "v_mfma_f32_16x16x32_bf16 v[72:75], v[52:55], v[212:215], v[72:75]\n"
        "v_accvgpr_write_b32 a190, v186\n"
        "v_accvgpr_write_b32 a191, v187\n"
        "v_mfma_f32_16x16x32_bf16 v[72:75], v[48:51], v[196:199], v[72:75]\n"
        "v_accvgpr_read_b32 v185, a85\n"
        "v_accvgpr_read_b32 v186, a86\n"
        "v_mfma_f32_16x16x32_bf16 v[104:107], v[48:51], v[200:203], v[104:107]\n"
        "v_accvgpr_read_b32 v187, a87\n"
        "v_accvgpr_read_b32 v188, a116\n"
        "v_mfma_f32_16x16x32_bf16 v[104:107], v[52:55], v[216:219], v[104:107]\n"
        "v_accvgpr_read_b32 v189, a117\n"
        "v_accvgpr_read_b32 v190, a118\n"
        "v_mfma_f32_16x16x32_bf16 v[108:111], v[60:63], v[216:219], v[108:111]\n"
        "v_accvgpr_read_b32 v191, a119\n"
        "v_mfma_f32_16x16x32_bf16 v[108:111], v[56:59], v[200:203], v[108:111]\n"
        "s_waitcnt vmcnt(13) lgkmcnt(0)\n"
        "v_mfma_f32_16x16x32_bf16 v[140:143], v[56:59], v[204:207], v[140:143]\n"
        "s_barrier\n"
        "v_mfma_f32_16x16x32_bf16 v[140:143], v[60:63], v[220:223], v[140:143]\n"
        "ds_read_b64_tr_b16 v[60:61], v228 offset:24576\n"
        "global_load_lds_dwordx4 v238, s[68:69] offset:0\n"
        "v_mfma_f32_16x16x32_bf16 v[136:139], v[52:55], v[220:223], v[136:139]\n"
        "ds_read_b64_tr_b16 v[52:53], v226 offset:24576\n"
        "v_mfma_f32_16x16x32_bf16 v[136:139], v[48:51], v[204:207], v[136:139]\n"
        "ds_read_b64_tr_b16 v[48:49], v226 offset:16384\n"
        "v_mfma_f32_16x16x32_bf16 v[16:19], v[160:163], v[192:195], v[16:19]\n"
        "ds_read_b64_tr_b16 v[50:51], v227 offset:16384\n"
        "v_mfma_f32_16x16x32_bf16 v[16:19], v[164:167], v[208:211], v[16:19]\n"
        "ds_read_b64_tr_b16 v[54:55], v227 offset:24576\n"
        "global_load_lds_dwordx4 v239, s[68:69] offset:1024\n"
        "v_mfma_f32_16x16x32_bf16 v[20:23], v[172:175], v[208:211], v[20:23]\n"
        "ds_read_b64_tr_b16 v[62:63], v229 offset:24576\n"
        "v_mfma_f32_16x16x32_bf16 v[20:23], v[168:171], v[192:195], v[20:23]\n"
        "ds_read_b64_tr_b16 v[56:57], v228 offset:16384\n"
        "v_mfma_f32_16x16x32_bf16 v[84:87], v[168:171], v[196:199], v[84:87]\n"
        "ds_read_b64_tr_b16 v[58:59], v229 offset:16384\n"
        "v_mfma_f32_16x16x32_bf16 v[84:87], v[172:175], v[212:215], v[84:87]\n"
        "global_load_lds_dwordx4 v240, s[68:69] offset:2048\n"
        "v_mfma_f32_16x16x32_bf16 v[80:83], v[164:167], v[212:215], v[80:83]\n"
        "v_mfma_f32_16x16x32_bf16 v[80:83], v[160:163], v[196:199], v[80:83]\n"
        "v_accvgpr_read_b32 v176, a80\n"
        "v_cvt_pk_bf16_f32 v176, v176, v177\n"
        "v_mfma_f32_16x16x32_bf16 v[112:115], v[160:163], v[200:203], v[112:115]\n"
        "v_cvt_pk_bf16_f32 v177, v178, v179\n"
        "v_cvt_pk_bf16_f32 v178, v180, v181\n"
        "v_mfma_f32_16x16x32_bf16 v[112:115], v[164:167], v[216:219], v[112:115]\n"
        "v_cvt_pk_bf16_f32 v179, v182, v183\n"
        "v_accvgpr_write_b32 a16, v176\n"
        "v_mfma_f32_16x16x32_bf16 v[116:119], v[172:175], v[216:219], v[116:119]\n"
        "global_store_dwordx4 v254, a[164:167], s[70:71] offset:1024 sc1\n"
        "s_add_u32 s70, s38, s97\n"
        "s_addc_u32 s71, s39, 0\n"
        "s_add_u32 s97, s52, 0x3000\n"
        "v_accvgpr_write_b32 a17, v177\n"
        "v_accvgpr_write_b32 a18, v178\n"
        "v_mfma_f32_16x16x32_bf16 v[116:119], v[168:171], v[200:203], v[116:119]\n"
        "s_add_u32 s97, s97, s75\n"
        "v_accvgpr_write_b32 a19, v179\n"
        "v_accvgpr_read_b32 v177, a89\n"
        "v_mfma_f32_16x16x32_bf16 v[148:151], v[168:171], v[204:207], v[148:151]\n"
        "ds_read_b64_tr_b16 v[168:169], v232 offset:16384\n"
        "v_accvgpr_read_b32 v178, a90\n"
        "v_mfma_f32_16x16x32_bf16 v[148:151], v[172:175], v[220:223], v[148:151]\n"
        "ds_read_b64_tr_b16 v[172:173], v232 offset:24576\n"
        "global_load_lds_dwordx4 v241, s[68:69] offset:3072\n"
        "s_add_u32 m0, s72, 0x10000\n"
        "v_mfma_f32_16x16x32_bf16 v[144:147], v[164:167], v[220:223], v[144:147]\n"
        "ds_read_b64_tr_b16 v[164:165], v230 offset:24576\n"
        "s_add_u32 s68, s68, 0x80000\n"
        "s_addc_u32 s69, s69, 0\n"
        "v_mfma_f32_16x16x32_bf16 v[144:147], v[160:163], v[204:207], v[144:147]\n"
        "ds_read_b64_tr_b16 v[160:161], v230 offset:16384\n"
        "v_accvgpr_read_b32 v179, a91\n"
        "v_mfma_f32_16x16x32_bf16 v[24:27], v[32:35], v[192:195], v[24:27]\n"
        "ds_read_b64_tr_b16 v[162:163], v231 offset:16384\n"
        "v_accvgpr_read_b32 v180, a120\n"
        "v_mfma_f32_16x16x32_bf16 v[24:27], v[36:39], v[208:211], v[24:27]\n"
        "ds_read_b64_tr_b16 v[166:167], v231 offset:24576\n"
        "global_load_lds_dwordx4 v242, s[64:65] offset:0\n"
        "v_mfma_f32_16x16x32_bf16 v[28:31], v[44:47], v[208:211], v[28:31]\n"
        "ds_read_b64_tr_b16 v[174:175], v233 offset:24576\n"
        "v_accvgpr_read_b32 v181, a121\n"
        "v_mfma_f32_16x16x32_bf16 v[28:31], v[40:43], v[192:195], v[28:31]\n"
        "ds_read_b64_tr_b16 v[170:171], v233 offset:16384\n"
        "v_accvgpr_read_b32 v182, a122\n"
        "v_mfma_f32_16x16x32_bf16 v[92:95], v[40:43], v[196:199], v[92:95]\n"
        "ds_read_b128 v[192:195], v246 offset:34816\n"
        "v_accvgpr_read_b32 v183, a123\n"
        "v_mfma_f32_16x16x32_bf16 v[92:95], v[44:47], v[212:215], v[92:95]\n"
        "ds_read_b128 v[208:211], v247 offset:34816\n"
        "global_load_lds_dwordx4 v243, s[64:65] offset:1024\n"
        "v_mfma_f32_16x16x32_bf16 v[88:91], v[36:39], v[212:215], v[88:91]\n"
        "ds_read_b128 v[212:215], v247 offset:32768\n"
        "v_mfma_f32_16x16x32_bf16 v[88:91], v[32:35], v[196:199], v[88:91]\n"
        "ds_read_b128 v[196:199], v246 offset:32768\n"
        "v_mfma_f32_16x16x32_bf16 v[120:123], v[32:35], v[200:203], v[120:123]\n"
        "v_mfma_f32_16x16x32_bf16 v[120:123], v[36:39], v[216:219], v[120:123]\n"
        "global_load_lds_dwordx4 v244, s[64:65] offset:2048\n"
        "v_mfma_f32_16x16x32_bf16 v[124:127], v[44:47], v[216:219], v[124:127]\n"
        "ds_read_b128 v[216:219], v247 offset:38912\n"
        "v_mfma_f32_16x16x32_bf16 v[124:127], v[40:43], v[200:203], v[124:127]\n"
        "ds_read_b128 v[200:203], v246 offset:38912\n"
        "v_mfma_f32_16x16x32_bf16 v[156:159], v[40:43], v[204:207], v[156:159]\n"
        "ds_read_b64_tr_b16 v[40:41], v228 offset:17408\n"
        "s_waitcnt lgkmcnt(6)\n"
        "v_mfma_f32_16x16x32_bf16 v[156:159], v[44:47], v[220:223], v[156:159]\n"
        "ds_read_b64_tr_b16 v[44:45], v228 offset:25600\n"
        "global_load_lds_dwordx4 v245, s[64:65] offset:3072\n"
        "s_add_u32 m0, s72, 0x11000\n"
        "v_mfma_f32_16x16x32_bf16 v[152:155], v[36:39], v[220:223], v[152:155]\n"
        "ds_read_b128 v[220:223], v247 offset:36864\n"
        "s_add_u32 s64, s64, 0x80\n"
        "s_addc_u32 s65, s65, 0\n"
        "v_mfma_f32_16x16x32_bf16 v[152:155], v[32:35], v[204:207], v[152:155]\n"
        "ds_read_b128 v[204:207], v246 offset:36864\n"
        "v_accvgpr_read_b32 v184, a84\n"
        "v_mfma_f32_16x16x32_bf16 v[0:3], v[48:51], v[192:195], v[0:3]\n"
        "ds_read_b64_tr_b16 v[32:33], v226 offset:17408\n"
        "s_waitcnt lgkmcnt(7)\n"
        "v_mfma_f32_16x16x32_bf16 v[0:3], v[52:55], v[208:211], v[0:3]\n"
        "ds_read_b64_tr_b16 v[34:35], v227 offset:17408\n"
        "v_cvt_pk_bf16_f32 v184, v184, v185\n"
        "v_mfma_f32_16x16x32_bf16 v[4:7], v[60:63], v[208:211], v[4:7]\n"
        "ds_read_b64_tr_b16 v[36:37], v226 offset:25600\n"
        "v_cvt_pk_bf16_f32 v185, v186, v187\n"
        "v_mfma_f32_16x16x32_bf16 v[4:7], v[56:59], v[192:195], v[4:7]\n"
        "ds_read_b64_tr_b16 v[38:39], v227 offset:25600\n"
        "v_cvt_pk_bf16_f32 v186, v188, v189\n"
        "v_mfma_f32_16x16x32_bf16 v[68:71], v[56:59], v[196:199], v[68:71]\n"
        "ds_read_b64_tr_b16 v[46:47], v229 offset:25600\n"
        "v_cvt_pk_bf16_f32 v187, v190, v191\n"
        "v_mfma_f32_16x16x32_bf16 v[68:71], v[60:63], v[212:215], v[68:71]\n"
        "ds_read_b64_tr_b16 v[42:43], v229 offset:17408\n"
        "v_accvgpr_write_b32 a20, v184\n"
        "v_mfma_f32_16x16x32_bf16 v[64:67], v[52:55], v[212:215], v[64:67]\n"
        "global_load_lds_dwordx4 v242, s[66:67] offset:0\n"
        "v_accvgpr_write_b32 a21, v185\n"
        "v_mfma_f32_16x16x32_bf16 v[64:67], v[48:51], v[196:199], v[64:67]\n"
        "s_waitcnt lgkmcnt(6)\n"
        "v_accvgpr_write_b32 a22, v186\n"
        "v_accvgpr_write_b32 a23, v187\n"
        "v_mfma_f32_16x16x32_bf16 v[96:99], v[48:51], v[200:203], v[96:99]\n"
        "v_accvgpr_read_b32 v185, a93\n"
        "v_accvgpr_read_b32 v186, a94\n"
        "v_mfma_f32_16x16x32_bf16 v[96:99], v[52:55], v[216:219], v[96:99]\n"
        "v_accvgpr_read_b32 v187, a95\n"
        "v_accvgpr_read_b32 v188, a124\n"
        "v_mfma_f32_16x16x32_bf16 v[100:103], v[60:63], v[216:219], v[100:103]\n"
        "global_load_lds_dwordx4 v243, s[66:67] offset:1024\n"
        "v_accvgpr_read_b32 v189, a125\n"
        "v_mfma_f32_16x16x32_bf16 v[100:103], v[56:59], v[200:203], v[100:103]\n"
        "v_accvgpr_read_b32 v190, a126\n"
        "v_accvgpr_read_b32 v191, a127\n"
        "v_mfma_f32_16x16x32_bf16 v[132:135], v[56:59], v[204:207], v[132:135]\n"
        "ds_read_b64_tr_b16 v[56:57], v232 offset:17408\n"
        "v_accvgpr_write_b32 a93, v252\n"
        "v_mfma_f32_16x16x32_bf16 v[132:135], v[60:63], v[220:223], v[132:135]\n"
        "ds_read_b64_tr_b16 v[60:61], v232 offset:25600\n"
        "v_mfma_f32_16x16x32_bf16 v[128:131], v[52:55], v[220:223], v[128:131]\n"
        "ds_read_b64_tr_b16 v[52:53], v230 offset:25600\n"
        "global_load_lds_dwordx4 v244, s[66:67] offset:2048\n"
        "v_mfma_f32_16x16x32_bf16 v[128:131], v[48:51], v[204:207], v[128:131]\n"
        "ds_read_b64_tr_b16 v[48:49], v230 offset:17408\n"
        "v_mfma_f32_16x16x32_bf16 v[8:11], v[160:163], v[192:195], v[8:11]\n"
        "ds_read_b64_tr_b16 v[50:51], v231 offset:17408\n"
        "v_mfma_f32_16x16x32_bf16 v[8:11], v[164:167], v[208:211], v[8:11]\n"
        "ds_read_b64_tr_b16 v[54:55], v231 offset:25600\n"
        "v_mfma_f32_16x16x32_bf16 v[12:15], v[172:175], v[208:211], v[12:15]\n"
        "ds_read_b64_tr_b16 v[62:63], v233 offset:25600\n"
        "global_load_lds_dwordx4 v245, s[66:67] offset:3072\n"
        "s_add_u32 m0, s73, 0x1c000\n"
        "v_mfma_f32_16x16x32_bf16 v[12:15], v[168:171], v[192:195], v[12:15]\n"
        "ds_read_b64_tr_b16 v[58:59], v233 offset:17408\n"
        "s_add_u32 s66, s66, 0x80\n"
        "s_addc_u32 s67, s67, 0\n"
        "v_mfma_f32_16x16x32_bf16 v[76:79], v[168:171], v[196:199], v[76:79]\n"
        "v_mfma_f32_16x16x32_bf16 v[76:79], v[172:175], v[212:215], v[76:79]\n"
        "v_mfma_f32_16x16x32_bf16 v[72:75], v[164:167], v[212:215], v[72:75]\n"
        "v_mfma_f32_16x16x32_bf16 v[72:75], v[160:163], v[196:199], v[72:75]\n"
        "v_accvgpr_read_b32 v176, a88\n"
        "v_cvt_pk_bf16_f32 v176, v176, v177\n"
        "v_mfma_f32_16x16x32_bf16 v[104:107], v[160:163], v[200:203], v[104:107]\n"
        "s_mul_i32 s90, s43, 529408\n"
        "v_cvt_pk_bf16_f32 v177, v178, v179\n"
        "v_cvt_pk_bf16_f32 v178, v180, v181\n"
        "v_mfma_f32_16x16x32_bf16 v[104:107], v[164:167], v[216:219], v[104:107]\n"
        "s_add_u32 s90, s90, s72\n"
        "v_cvt_pk_bf16_f32 v179, v182, v183\n"
        "v_accvgpr_write_b32 a24, v176\n"
        "v_mfma_f32_16x16x32_bf16 v[108:111], v[172:175], v[216:219], v[108:111]\n"
        "s_and_b32 s78, s45, 1\n"
        "v_accvgpr_write_b32 a25, v177\n"
        "v_accvgpr_write_b32 a26, v178\n"
        "v_mfma_f32_16x16x32_bf16 v[108:111], v[168:171], v[200:203], v[108:111]\n"
        "s_waitcnt vmcnt(13) lgkmcnt(0)\n"
        "v_accvgpr_write_b32 a27, v179\n"
        "s_lshl_b32 s78, s78, 15\n"
        "v_mfma_f32_16x16x32_bf16 v[140:143], v[168:171], v[204:207], v[140:143]\n"
        "s_barrier\n"
        "v_mfma_f32_16x16x32_bf16 v[140:143], v[172:175], v[220:223], v[140:143]\n"
        "ds_read_b64_tr_b16 v[172:173], v228 offset:40960\n"
        "global_load_lds_dwordx4 v238, s[68:69] offset:0\n"
        "s_add_u32 s78, s78, 0x10000\n"
        "v_mfma_f32_16x16x32_bf16 v[136:139], v[164:167], v[220:223], v[136:139]\n"
        "ds_read_b64_tr_b16 v[164:165], v226 offset:40960\n"
        "s_and_b32 s80, s40, 1\n"
        "s_lshl_b32 s80, s80, 11\n"
        "v_mfma_f32_16x16x32_bf16 v[136:139], v[160:163], v[204:207], v[136:139]\n"
        "ds_read_b64_tr_b16 v[160:161], v226 offset:32768\n"
        "s_and_b32 s79, s45, 1\n"
        "s_lshl_b32 s79, s79, 15\n"
        "v_mfma_f32_16x16x32_bf16 v[16:19], v[32:35], v[192:195], v[16:19]\n"
        "ds_read_b64_tr_b16 v[162:163], v227 offset:32768\n"
        "s_lshl_b32 s82, s40, 4\n"
        "s_sub_u32 s83, 14, s44\n"
        "v_mfma_f32_16x16x32_bf16 v[16:19], v[36:39], v[208:211], v[16:19]\n"
        "ds_read_b64_tr_b16 v[166:167], v227 offset:40960\n"
        "global_load_lds_dwordx4 v239, s[68:69] offset:1024\n"
        "s_lshl_b32 s50, s41, 7\n"
        "v_mfma_f32_16x16x32_bf16 v[20:23], v[44:47], v[208:211], v[20:23]\n"
        "ds_read_b64_tr_b16 v[174:175], v229 offset:40960\n"
        "s_lshl2_add_u32 s50, s42, s50\n"
        "s_add_u32 s50, s50, s40\n"
        "v_mfma_f32_16x16x32_bf16 v[20:23], v[40:43], v[192:195], v[20:23]\n"
        "ds_read_b64_tr_b16 v[168:169], v228 offset:32768\n"
        "s_lshl_b32 s50, s50, 12\n"
        "s_mul_i32 s51, s40, 8320\n"
        "v_mfma_f32_16x16x32_bf16 v[84:87], v[40:43], v[196:199], v[84:87]\n"
        "ds_read_b64_tr_b16 v[170:171], v229 offset:32768\n"
        "s_add_u32 s92, s26, s50\n"
        "s_addc_u32 s93, s27, 0\n"
        "v_mfma_f32_16x16x32_bf16 v[84:87], v[44:47], v[212:215], v[84:87]\n"
        "global_load_lds_dwordx4 v240, s[68:69] offset:2048\n"
        "s_add_u32 s50, s50, 0x3300000\n"
        "v_mfma_f32_16x16x32_bf16 v[80:83], v[36:39], v[212:215], v[80:83]\n"
        "s_add_u32 s94, s38, s50\n"
        "s_addc_u32 s95, s39, 0\n"
        "s_add_u32 s51, s51, 0x18000\n"
        "v_mfma_f32_16x16x32_bf16 v[80:83], v[32:35], v[196:199], v[80:83]\n"
        "s_add_u32 s50, s51, 4128\n"
        "v_bfe_u32 v176, v254, 4, 4\n"
        "v_lshrrev_b32 v177, 8, v254\n"
        "v_mfma_f32_16x16x32_bf16 v[112:115], v[32:35], v[200:203], v[112:115]\n"
        "v_lshrrev_b32 v178, 7, v254\n"
        "v_bfe_u32 v179, v254, 4, 3\n"
        "v_mfma_f32_16x16x32_bf16 v[112:115], v[36:39], v[216:219], v[112:115]\n"
        "global_load_lds_dwordx4 v241, s[68:69] offset:3072\n"
        "s_add_u32 m0, s72, 0x8000\n"
        "v_mfma_f32_16x16x32_bf16 v[116:119], v[44:47], v[216:219], v[116:119]\n"
        "s_add_u32 s68, s68, 0x80000\n"
        "s_addc_u32 s69, s69, 0\n"
        "v_and_b32 v180, 6, v178\n"
        "v_mfma_f32_16x16x32_bf16 v[116:119], v[40:43], v[200:203], v[116:119]\n"
        "LHA8_%=:\n"
        "s_cbranch_execz LHRA_%=\n"
        "v_xor_b32 v180, v180, v179\n"
        "v_lshlrev_b32 v180, 4, v180\n"
        "v_mfma_f32_16x16x32_bf16 v[148:151], v[40:43], v[204:207], v[148:151]\n"
        "ds_read_b64_tr_b16 v[40:41], v232 offset:32768\n"
        "v_lshl_add_u32 v181, v178, 11, v180\n"
        "v_mfma_f32_16x16x32_bf16 v[148:151], v[44:47], v[220:223], v[148:151]\n"
        "ds_read_b64_tr_b16 v[44:45], v232 offset:40960\n"
        "global_load_lds_dwordx4 v242, s[64:65] offset:0\n"
        "v_mfma_f32_16x16x32_bf16 v[144:147], v[36:39], v[220:223], v[144:147]\n"
        "ds_read_b64_tr_b16 v[36:37], v230 offset:40960\n"
        "v_xor_b32 v178, 4, v178\n"
        "v_mfma_f32_16x16x32_bf16 v[144:147], v[32:35], v[204:207], v[144:147]\n"
        "ds_read_b64_tr_b16 v[32:33], v230 offset:32768\n"
        "v_accvgpr_read_b32 v184, a92\n"
        "v_mfma_f32_16x16x32_bf16 v[24:27], v[48:51], v[192:195], v[24:27]\n"
        "ds_read_b64_tr_b16 v[34:35], v231 offset:32768\n"
        "v_cvt_pk_bf16_f32 v184, v184, v185\n"
        "v_mfma_f32_16x16x32_bf16 v[24:27], v[52:55], v[208:211], v[24:27]\n"
        "ds_read_b64_tr_b16 v[38:39], v231 offset:40960\n"
        "v_cvt_pk_bf16_f32 v185, v186, v187\n"
        "v_mfma_f32_16x16x32_bf16 v[28:31], v[60:63], v[208:211], v[28:31]\n"
        "global_store_dwordx4 v254, a[168:171], s[70:71] offset:0 sc1\n"
        "ds_read_b64_tr_b16 v[46:47], v233 offset:40960\n"
        "ds_read_b64_tr_b16 v[42:43], v233 offset:32768\n"
        "v_cvt_pk_bf16_f32 v186, v188, v189\n"
        "v_mfma_f32_16x16x32_bf16 v[28:31], v[56:59], v[192:195], v[28:31]\n"
        "ds_read_b128 v[192:195], v246 offset:2048\n"
        "v_cvt_pk_bf16_f32 v187, v190, v191\n"
        "v_mfma_f32_16x16x32_bf16 v[92:95], v[56:59], v[196:199], v[92:95]\n"
        "ds_read_b128 v[208:211], v247 offset:2048\n"
        "v_accvgpr_write_b32 a28, v184\n"
        "v_mfma_f32_16x16x32_bf16 v[92:95], v[60:63], v[212:215], v[92:95]\n"
        "v_accvgpr_write_b32 a29, v185\n"
        "v_accvgpr_write_b32 a30, v186\n"
        "v_mfma_f32_16x16x32_bf16 v[88:91], v[52:55], v[212:215], v[88:91]\n"
        "ds_read_b128 v[212:215], v247 offset:0\n"
        "v_accvgpr_write_b32 a31, v187\n"
        "v_mfma_f32_16x16x32_bf16 v[88:91], v[48:51], v[196:199], v[88:91]\n"
        "ds_read_b128 v[196:199], v246 offset:0\n"
        "global_load_lds_dwordx4 v243, s[64:65] offset:1024\n"
        "v_mfma_f32_16x16x32_bf16 v[120:123], v[48:51], v[200:203], v[120:123]\n"
        "v_lshl_add_u32 v178, v178, 11, v180\n"
        "v_add_u32 v182, 4096, v181\n"
        "v_mfma_f32_16x16x32_bf16 v[120:123], v[52:55], v[216:219], v[120:123]\n"
        "v_accvgpr_write_b32 a84, v182\n"
        "v_add_u32 v182, 3200, v181\n"
        "v_mfma_f32_16x16x32_bf16 v[124:127], v[60:63], v[216:219], v[124:127]\n"
        "ds_read_b128 v[216:219], v247 offset:6144\n"
        "v_accvgpr_write_b32 a85, v182\n"
        "v_mfma_f32_16x16x32_bf16 v[124:127], v[56:59], v[200:203], v[124:127]\n"
        "ds_read_b128 v[200:203], v246 offset:6144\n"
        "global_load_lds_dwordx4 v244, s[64:65] offset:2048\n"
        "v_mfma_f32_16x16x32_bf16 v[156:159], v[56:59], v[204:207], v[156:159]\n"
        "ds_read_b64_tr_b16 v[56:57], v228 offset:33792\n"
        "v_add_u32 v182, 18432, v178\n"
        "v_mfma_f32_16x16x32_bf16 v[156:159], v[60:63], v[220:223], v[156:159]\n"
        "ds_read_b64_tr_b16 v[60:61], v228 offset:41984\n"
        "v_accvgpr_write_b32 a86, v182\n"
        "v_mfma_f32_16x16x32_bf16 v[152:155], v[52:55], v[220:223], v[152:155]\n"
        "ds_read_b128 v[220:223], v247 offset:4096\n"
        "v_add_u32 v182, 17536, v178\n"
        "v_mfma_f32_16x16x32_bf16 v[152:155], v[48:51], v[204:207], v[152:155]\n"
        "ds_read_b128 v[204:207], v246 offset:4096\n"
        "s_waitcnt lgkmcnt(6)\n"
        "v_mfma_f32_16x16x32_bf16 v[0:3], v[160:163], v[192:195], v[0:3]\n"
        "ds_read_b64_tr_b16 v[48:49], v226 offset:33792\n"
        "global_load_lds_dwordx4 v245, s[64:65] offset:3072\n"
        "s_add_u32 m0, s72, 0x9000\n"
        "v_mfma_f32_16x16x32_bf16 v[0:3], v[164:167], v[208:211], v[0:3]\n"
        "ds_read_b64_tr_b16 v[50:51], v227 offset:33792\n"
        "s_add_u32 s64, s64, 0x80\n"
        "s_addc_u32 s65, s65, 0\n"
        "v_mfma_f32_16x16x32_bf16 v[4:7], v[172:175], v[208:211], v[4:7]\n"
        "ds_read_b64_tr_b16 v[52:53], v226 offset:41984\n"
        "v_accvgpr_write_b32 a87, v182\n"
        "v_mfma_f32_16x16x32_bf16 v[4:7], v[168:171], v[192:195], v[4:7]\n"
        "ds_read_b64_tr_b16 v[54:55], v227 offset:41984\n"
        "v_mov_b32 v178, s96\n"
        "v_mfma_f32_16x16x32_bf16 v[68:71], v[168:171], v[196:199], v[68:71]\n"
        "ds_read_b64_tr_b16 v[62:63], v229 offset:41984\n"
        "global_load_lds_dwordx4 v242, s[66:67] offset:0\n"
        "v_mfma_f32_16x16x32_bf16 v[68:71], v[172:175], v[212:215], v[68:71]\n"
        "ds_read_b64_tr_b16 v[58:59], v229 offset:33792\n"
        "v_add_u32 v180, s51, v254\n"
        "v_mfma_f32_16x16x32_bf16 v[64:67], v[164:167], v[212:215], v[64:67]\n"
        "s_waitcnt lgkmcnt(7)\n"
        "v_add_u32 v180, 4096, v180\n"
        "v_writelane_b32 v180, s96, 2\n"
        "v_mfma_f32_16x16x32_bf16 v[64:67], v[160:163], v[196:199], v[64:67]\n"
        "s_add_u32 s96, s96, 16\n"
        "v_writelane_b32 v180, s96, 3\n"
        "v_accvgpr_write_b32 a90, v180\n"
        "v_mfma_f32_16x16x32_bf16 v[96:99], v[160:163], v[200:203], v[96:99]\n"
        "global_load_lds_dwordx4 v243, s[66:67] offset:1024\n"
        "s_add_u32 s96, s51, 4098\n"
        "v_mfma_f32_16x16x32_bf16 v[96:99], v[164:167], v[216:219], v[96:99]\n"
        "s_waitcnt lgkmcnt(4)\n"
        "v_sub_u32 v179, s96, v178\n"
        "v_and_b32 v181, 1, v176\n"
        "v_mfma_f32_16x16x32_bf16 v[100:103], v[172:175], v[216:219], v[100:103]\n"
        "s_add_u32 s96, s51, 480\n"
        "v_mul_lo_u32 v179, v179, v181\n"
        "v_add_u32 v178, v178, v179\n"
        "v_mfma_f32_16x16x32_bf16 v[100:103], v[168:171], v[200:203], v[100:103]\n"
        "v_lshl_add_u32 v178, v176, 1, v178\n"
        "v_lshl_add_u32 v178, v177, 4, v178\n"
        "v_mfma_f32_16x16x32_bf16 v[132:135], v[168:171], v[204:207], v[132:135]\n"
        "ds_read_b64_tr_b16 v[168:169], v232 offset:33792\n"
        "global_load_lds_dwordx4 v244, s[66:67] offset:2048\n"
        "v_mfma_f32_16x16x32_bf16 v[132:135], v[172:175], v[220:223], v[132:135]\n"
        "ds_read_b64_tr_b16 v[172:173], v232 offset:41984\n"
        "v_accvgpr_write_b32 a88, v178\n"
        "v_mfma_f32_16x16x32_bf16 v[128:131], v[164:167], v[220:223], v[128:131]\n"
        "ds_read_b64_tr_b16 v[164:165], v230 offset:41984\n"
        "v_lshlrev_b32 v179, 5, v176\n"
        "v_mfma_f32_16x16x32_bf16 v[128:131], v[160:163], v[204:207], v[128:131]\n"
        "ds_read_b64_tr_b16 v[160:161], v230 offset:33792\n"
        "v_sub_u32 v179, s96, v179\n"
        "v_mfma_f32_16x16x32_bf16 v[8:11], v[32:35], v[192:195], v[8:11]\n"
        "ds_read_b64_tr_b16 v[162:163], v231 offset:33792\n"
        "global_load_lds_dwordx4 v245, s[66:67] offset:3072\n"
        "s_add_u32 m0, s73, 0x20000\n"
        "v_mfma_f32_16x16x32_bf16 v[8:11], v[36:39], v[208:211], v[8:11]\n"
        "ds_read_b64_tr_b16 v[166:167], v231 offset:41984\n"
        "s_add_u32 s66, s66, 0x80\n"
        "s_addc_u32 s67, s67, 0\n"
        "v_mfma_f32_16x16x32_bf16 v[12:15], v[44:47], v[208:211], v[12:15]\n"
        "ds_read_b64_tr_b16 v[174:175], v233 offset:41984\n"
        "s_lshl_b32 s96, s54, 16\n"
        "v_lshl_add_u32 v179, v177, 4, v179\n"
        "v_mfma_f32_16x16x32_bf16 v[12:15], v[40:43], v[192:195], v[12:15]\n"
        "ds_read_b64_tr_b16 v[170:171], v233 offset:33792\n"
        "s_and_b32 s54, s54, 0xffff0000\n"
        "v_accvgpr_write_b32 a89, v179\n"
        "v_mfma_f32_16x16x32_bf16 v[76:79], v[40:43], v[196:199], v[76:79]\n"
        "v_mov_b32 v181, 0\n"
        "v_writelane_b32 v181, s96, 3\n"
        "v_mfma_f32_16x16x32_bf16 v[76:79], v[44:47], v[212:215], v[76:79]\n"
        "s_lshr_b32 s96, s40, 1\n"
        "v_accvgpr_write_b32 a91, v181\n"
        "s_lshl_b32 s96, s96, 10\n"
        "v_mfma_f32_16x16x32_bf16 v[72:75], v[36:39], v[212:215], v[72:75]\n"
        "v_lshrrev_b32 v178, 1, v177\n"
        "v_lshlrev_b32 v179, 11, v178\n"
        "v_mfma_f32_16x16x32_bf16 v[72:75], v[32:35], v[196:199], v[72:75]\n"
        "v_add_u32 v179, s96, v179\n"
        "v_bfe_u32 v180, v254, 6, 3\n"
        "v_mfma_f32_16x16x32_bf16 v[104:107], v[32:35], v[200:203], v[104:107]\n"
        "s_and_b32 s96, s40, 1\n"
        "v_bfe_u32 v181, v254, 4, 2\n"
        "v_lshlrev_b32 v183, 2, v178\n"
        "v_mfma_f32_16x16x32_bf16 v[104:107], v[36:39], v[216:219], v[104:107]\n"
        "s_lshl_b32 s96, s96, 6\n"
        "v_xor_b32 v183, v183, v180\n"
        "v_bfe_u32 v184, v183, 1, 2\n"
        "v_mfma_f32_16x16x32_bf16 v[108:111], v[44:47], v[216:219], v[108:111]\n"
        "v_xor_b32 v184, 0, v184\n"
        "v_lshlrev_b32 v184, 5, v184\n"
        "v_mfma_f32_16x16x32_bf16 v[108:111], v[40:43], v[200:203], v[108:111]\n"
        "s_waitcnt vmcnt(13) lgkmcnt(0)\n"
        "v_lshl_add_u32 v183, v183, 7, v184\n"
        "v_lshl_add_u32 v183, v181, 3, v183\n"
        "v_mfma_f32_16x16x32_bf16 v[140:143], v[40:43], v[204:207], v[140:143]\n"
        "s_barrier\n"
        "v_mfma_f32_16x16x32_bf16 v[140:143], v[44:47], v[220:223], v[140:143]\n"
        "ds_read_b64_tr_b16 v[44:45], v228 offset:57344\n"
        "global_load_lds_dwordx4 v238, s[68:69] offset:0\n"
        "v_mfma_f32_16x16x32_bf16 v[136:139], v[36:39], v[220:223], v[136:139]\n"
        "ds_read_b64_tr_b16 v[36:37], v226 offset:57344\n"
        "v_add_u32 v182, v183, v179\n"
        "v_mfma_f32_16x16x32_bf16 v[136:139], v[32:35], v[204:207], v[136:139]\n"
        "ds_read_b64_tr_b16 v[32:33], v226 offset:49152\n"
        "v_xor_b32 v182, s96, v182\n"
        "v_mfma_f32_16x16x32_bf16 v[16:19], v[48:51], v[192:195], v[16:19]\n"
        "ds_read_b64_tr_b16 v[34:35], v227 offset:49152\n"
        "v_accvgpr_write_b32 a92, v182\n"
        "v_mfma_f32_16x16x32_bf16 v[16:19], v[52:55], v[208:211], v[16:19]\n"
        "ds_read_b64_tr_b16 v[38:39], v227 offset:57344\n"
        "global_load_lds_dwordx4 v239, s[68:69] offset:1024\n"
        "v_mfma_f32_16x16x32_bf16 v[20:23], v[60:63], v[208:211], v[20:23]\n"
        "ds_read_b64_tr_b16 v[46:47], v229 offset:57344\n"
        "v_mfma_f32_16x16x32_bf16 v[20:23], v[56:59], v[192:195], v[20:23]\n"
        "ds_read_b64_tr_b16 v[40:41], v228 offset:49152\n"
        "v_mfma_f32_16x16x32_bf16 v[84:87], v[56:59], v[196:199], v[84:87]\n"
        "ds_read_b64_tr_b16 v[42:43], v229 offset:49152\n"
        "v_mfma_f32_16x16x32_bf16 v[84:87], v[60:63], v[212:215], v[84:87]\n"
        "global_load_lds_dwordx4 v240, s[68:69] offset:2048\n"
        "v_mfma_f32_16x16x32_bf16 v[80:83], v[52:55], v[212:215], v[80:83]\n"
        "v_add_u32 v240, s78, v254\n"
        "v_mfma_f32_16x16x32_bf16 v[80:83], v[48:51], v[196:199], v[80:83]\n"
        "v_mfma_f32_16x16x32_bf16 v[112:115], v[48:51], v[200:203], v[112:115]\n"
        "v_mfma_f32_16x16x32_bf16 v[112:115], v[52:55], v[216:219], v[112:115]\n"
        "global_load_lds_dwordx4 v241, s[68:69] offset:3072\n"
        "s_add_u32 m0, s72, 0x0\n"
        "v_mfma_f32_16x16x32_bf16 v[116:119], v[60:63], v[216:219], v[116:119]\n"
        "v_add_u32 v241, s79, v254\n"
        "v_mfma_f32_16x16x32_bf16 v[116:119], v[56:59], v[200:203], v[116:119]\n"
        "v_mfma_f32_16x16x32_bf16 v[148:151], v[56:59], v[204:207], v[148:151]\n"
        "ds_read_b64_tr_b16 v[56:57], v232 offset:49152\n"
        "v_mfma_f32_16x16x32_bf16 v[148:151], v[60:63], v[220:223], v[148:151]\n"
        "ds_read_b64_tr_b16 v[60:61], v232 offset:57344\n"
        "global_load_lds_dwordx4 v242, s[64:65] offset:0\n"
        "v_mfma_f32_16x16x32_bf16 v[144:147], v[52:55], v[220:223], v[144:147]\n"
        "ds_read_b64_tr_b16 v[52:53], v230 offset:57344\n"
        "v_mfma_f32_16x16x32_bf16 v[144:147], v[48:51], v[204:207], v[144:147]\n"
        "ds_read_b64_tr_b16 v[48:49], v230 offset:49152\n"
        "v_mfma_f32_16x16x32_bf16 v[24:27], v[160:163], v[192:195], v[24:27]\n"
        "ds_read_b64_tr_b16 v[50:51], v231 offset:49152\n"
        "v_mfma_f32_16x16x32_bf16 v[24:27], v[164:167], v[208:211], v[24:27]\n"
        "ds_read_b64_tr_b16 v[54:55], v231 offset:57344\n"
        "global_load_lds_dwordx4 v243, s[64:65] offset:1024\n"
        "v_mfma_f32_16x16x32_bf16 v[28:31], v[172:175], v[208:211], v[28:31]\n"
        "ds_read_b64_tr_b16 v[62:63], v233 offset:57344\n"
        "v_mfma_f32_16x16x32_bf16 v[28:31], v[168:171], v[192:195], v[28:31]\n"
        "ds_read_b64_tr_b16 v[58:59], v233 offset:49152\n"
        "v_mfma_f32_16x16x32_bf16 v[92:95], v[168:171], v[196:199], v[92:95]\n"
        "ds_read_b128 v[192:195], v248 offset:2048\n"
        "v_mfma_f32_16x16x32_bf16 v[92:95], v[172:175], v[212:215], v[92:95]\n"
        "ds_read_b128 v[208:211], v249 offset:2048\n"
        "global_load_lds_dwordx4 v244, s[64:65] offset:2048\n"
        "v_mfma_f32_16x16x32_bf16 v[88:91], v[164:167], v[212:215], v[88:91]\n"
        "ds_read_b128 v[212:215], v249 offset:0\n"
        "v_mfma_f32_16x16x32_bf16 v[88:91], v[160:163], v[196:199], v[88:91]\n"
        "ds_read_b128 v[196:199], v248 offset:0\n"
        "v_mfma_f32_16x16x32_bf16 v[120:123], v[160:163], v[200:203], v[120:123]\n"
        "v_mfma_f32_16x16x32_bf16 v[120:123], v[164:167], v[216:219], v[120:123]\n"
        "global_load_lds_dwordx4 v245, s[64:65] offset:3072\n"
        "s_add_u32 m0, s72, 0x1000\n"
        "v_mfma_f32_16x16x32_bf16 v[124:127], v[172:175], v[216:219], v[124:127]\n"
        "ds_read_b128 v[216:219], v249 offset:6144\n"
        "s_add_u32 s64, s38, s90\n"
        "s_addc_u32 s65, s39, 0\n"
        "v_mfma_f32_16x16x32_bf16 v[124:127], v[168:171], v[200:203], v[124:127]\n"
        "ds_read_b128 v[200:203], v248 offset:6144\n"
        "s_lshl_b32 s90, s40, 13\n"
        "s_and_b32 s90, s90, 0x4000\n"
        "v_mfma_f32_16x16x32_bf16 v[156:159], v[168:171], v[204:207], v[156:159]\n"
        "ds_read_b64_tr_b16 v[168:169], v228 offset:50176\n"
        "s_add_u32 s91, s78, s90\n"
        "s_add_u32 s91, s91, s80\n"
        "v_mfma_f32_16x16x32_bf16 v[156:159], v[172:175], v[220:223], v[156:159]\n"
        "ds_read_b64_tr_b16 v[172:173], v228 offset:58368\n"
        "global_load_lds_dwordx4 v242, s[66:67] offset:0\n"
        "s_xor_b32 s78, s78, 0x8000\n"
        "v_mfma_f32_16x16x32_bf16 v[152:155], v[164:167], v[220:223], v[152:155]\n"
        "ds_read_b128 v[220:223], v249 offset:4096\n"
        "s_add_u32 s78, s78, s72\n"
        "v_lshrrev_b32 v242, 6, v254\n"
        "v_mfma_f32_16x16x32_bf16 v[152:155], v[160:163], v[204:207], v[152:155]\n"
        "ds_read_b128 v[204:207], v248 offset:4096\n"
        "s_waitcnt lgkmcnt(6)\n"
        "v_mfma_f32_16x16x32_bf16 v[0:3], v[32:35], v[192:195], v[0:3]\n"
        "ds_read_b64_tr_b16 v[160:161], v226 offset:50176\n"
        "v_and_b32 v242, 12, v242\n"
        "v_mfma_f32_16x16x32_bf16 v[0:3], v[36:39], v[208:211], v[0:3]\n"
        "ds_read_b64_tr_b16 v[162:163], v227 offset:50176\n"
        "global_load_lds_dwordx4 v243, s[66:67] offset:1024\n"
        "v_mfma_f32_16x16x32_bf16 v[4:7], v[44:47], v[208:211], v[4:7]\n"
        "ds_read_b64_tr_b16 v[164:165], v226 offset:58368\n"
        "v_bfe_u32 v243, v254, 4, 4\n"
        "v_mfma_f32_16x16x32_bf16 v[4:7], v[40:43], v[192:195], v[4:7]\n"
        "ds_read_b64_tr_b16 v[166:167], v227 offset:58368\n"
        "v_sub_u32 v243, v243, v242\n"
        "v_mfma_f32_16x16x32_bf16 v[68:71], v[40:43], v[196:199], v[68:71]\n"
        "ds_read_b64_tr_b16 v[174:175], v229 offset:58368\n"
        "v_add_u32 v243, s82, v243\n"
        "v_mfma_f32_16x16x32_bf16 v[68:71], v[44:47], v[212:215], v[68:71]\n"
        "ds_read_b64_tr_b16 v[170:171], v229 offset:50176\n"
        "global_load_lds_dwordx4 v244, s[66:67] offset:2048\n"
        "s_mov_b32 s82, -1\n"
        "v_mfma_f32_16x16x32_bf16 v[64:67], v[36:39], v[212:215], v[64:67]\n"
        "s_waitcnt lgkmcnt(7)\n"
        "v_add_u32 v244, s91, v254\n"
        "s_xor_b32 s91, s79, 0x8000\n"
        "v_mfma_f32_16x16x32_bf16 v[64:67], v[32:35], v[196:199], v[64:67]\n"
        "s_add_u32 s91, s91, s90\n"
        "s_lshl1_add_u32 s91, s80, s91\n"
        "v_cmp_gt_i32 s[10:11], 0, v243\n"
        "v_mfma_f32_16x16x32_bf16 v[96:99], v[32:35], v[200:203], v[96:99]\n"
        "s_waitcnt lgkmcnt(5)\n"
        "v_cmp_gt_i32 s[12:13], 1, v243\n"
        "v_cmp_gt_i32 s[14:15], 2, v243\n"
        "v_mfma_f32_16x16x32_bf16 v[96:99], v[36:39], v[216:219], v[96:99]\n"
        "global_load_lds_dwordx4 v245, s[66:67] offset:3072\n"
        "s_add_u32 s79, s79, s72\n"
        "v_mfma_f32_16x16x32_bf16 v[100:103], v[44:47], v[216:219], v[100:103]\n"
        "s_add_u32 s66, s64, 0x1100000\n"
        "s_addc_u32 s67, s65, 0\n"
        "v_add_u32 v245, s91, v254\n"
        "v_mfma_f32_16x16x32_bf16 v[100:103], v[40:43], v[200:203], v[100:103]\n"
        "s_lshl_b32 s80, s41, 5\n"
        "v_cmp_gt_i32 s[16:17], 3, v243\n"
        "v_cmp_gt_i32 s[20:21], 16, v243\n"
        "v_mfma_f32_16x16x32_bf16 v[132:135], v[40:43], v[204:207], v[132:135]\n"
        "ds_read_b64_tr_b16 v[40:41], v232 offset:50176\n"
        "s_add_u32 s80, s80, 256\n"
        "v_cmp_gt_i32 s[22:23], 17, v243\n"
        "v_mfma_f32_16x16x32_bf16 v[132:135], v[44:47], v[220:223], v[132:135]\n"
        "ds_read_b64_tr_b16 v[44:45], v232 offset:58368\n"
        "s_add_u32 s81, s80, s42\n"
        "v_cmp_gt_i32 s[24:25], 18, v243\n"
        "v_mfma_f32_16x16x32_bf16 v[128:131], v[36:39], v[220:223], v[128:131]\n"
        "global_store_dwordx4 v254, a[172:175], s[70:71] offset:1024 sc1\n"
        "ds_read_b64_tr_b16 v[36:37], v230 offset:58368\n"
        "ds_read_b64_tr_b16 v[38:39], v231 offset:58368\n"
        "s_add_u32 s70, s38, s97\n"
        "s_addc_u32 s71, s39, 0\n"
        "v_mfma_f32_16x16x32_bf16 v[128:131], v[32:35], v[204:207], v[128:131]\n"
        "ds_read_b64_tr_b16 v[32:33], v230 offset:50176\n"
        "s_add_u32 s97, s52, 0x0\n"
        "v_cmp_gt_i32 s[28:29], 19, v243\n"
        "v_mfma_f32_16x16x32_bf16 v[8:11], v[48:51], v[192:195], v[8:11]\n"
        "ds_read_b64_tr_b16 v[34:35], v231 offset:50176\n"
        "s_lshl_b32 s81, s81, 7\n"
        "v_cmp_gt_i32 s[30:31], 32, v243\n"
        "v_mfma_f32_16x16x32_bf16 v[8:11], v[52:55], v[208:211], v[8:11]\n"
        "ds_read_b64_tr_b16 v[46:47], v233 offset:58368\n"
        "s_and_b32 s80, s81, 0xfffffc7f\n"
        "v_cmp_gt_i32 s[56:57], 33, v243\n"
        "v_mfma_f32_16x16x32_bf16 v[12:15], v[60:63], v[208:211], v[12:15]\n"
        "ds_read_b64_tr_b16 v[42:43], v233 offset:50176\n"
        "s_xor_b32 s79, s79, 0x8000\n"
        "v_cmp_gt_i32 s[58:59], 34, v243\n"
        "v_mfma_f32_16x16x32_bf16 v[12:15], v[56:59], v[192:195], v[12:15]\n"
        "global_store_dwordx4 v254, a[0:3], s[70:71] offset:0 sc1\n"
        "v_cmp_gt_i32 s[60:61], 35, v243\n"
        "v_cmp_gt_i32 s[62:63], 48, v243\n"
        "v_cmp_gt_i32 s[84:85], 49, v243\n"
        "v_cmp_gt_i32 s[86:87], 50, v243\n"
        "v_mfma_f32_16x16x32_bf16 v[76:79], v[56:59], v[196:199], v[76:79]\n"
        "v_cmp_gt_i32 s[88:89], 51, v243\n"
        "v_mov_b32 v242, 0x3f803f80\n"
        "v_mfma_f32_16x16x32_bf16 v[76:79], v[60:63], v[212:215], v[76:79]\n"
        "v_accvgpr_write_b32 a80, v242\n"
        "v_accvgpr_write_b32 a81, v242\n"
        "v_mfma_f32_16x16x32_bf16 v[72:75], v[52:55], v[212:215], v[72:75]\n"
        "v_accvgpr_write_b32 a82, v242\n"
        "v_accvgpr_write_b32 a83, v242\n"
        "v_mfma_f32_16x16x32_bf16 v[72:75], v[48:51], v[196:199], v[72:75]\n"
        "v_mov_b32 v242, 0xff800000\n"
        "v_xor_b32 v241, 0x8000, v241\n"
        "v_mfma_f32_16x16x32_bf16 v[104:107], v[48:51], v[200:203], v[104:107]\n"
        "v_mfma_f32_16x16x32_bf16 v[104:107], v[52:55], v[216:219], v[104:107]\n"
        "s_waitcnt vmcnt(14) lgkmcnt(0)\n"
        "v_mfma_f32_16x16x32_bf16 v[108:111], v[60:63], v[216:219], v[108:111]\n"
        "s_barrier\n"
        "v_mfma_f32_16x16x32_bf16 v[108:111], v[56:59], v[200:203], v[108:111]\n"
        "v_mfma_f32_16x16x32_bf16 v[140:143], v[56:59], v[204:207], v[140:143]\n"
        "ds_read_b64_tr_b16 v[56:57], v228 offset:16384\n"
        "v_mfma_f32_16x16x32_bf16 v[140:143], v[60:63], v[220:223], v[140:143]\n"
        "ds_read_b64_tr_b16 v[60:61], v228 offset:24576\n"
        "v_mfma_f32_16x16x32_bf16 v[136:139], v[52:55], v[220:223], v[136:139]\n"
        "ds_read_b64_tr_b16 v[52:53], v226 offset:24576\n"
        "v_mfma_f32_16x16x32_bf16 v[136:139], v[48:51], v[204:207], v[136:139]\n"
        "ds_read_b64_tr_b16 v[48:49], v226 offset:16384\n"
        "v_mfma_f32_16x16x32_bf16 v[16:19], v[160:163], v[192:195], v[16:19]\n"
        "ds_read_b64_tr_b16 v[50:51], v227 offset:16384\n"
        "v_mfma_f32_16x16x32_bf16 v[16:19], v[164:167], v[208:211], v[16:19]\n"
        "ds_read_b64_tr_b16 v[54:55], v227 offset:24576\n"
        "v_mfma_f32_16x16x32_bf16 v[20:23], v[172:175], v[208:211], v[20:23]\n"
        "ds_read_b64_tr_b16 v[62:63], v229 offset:24576\n"
        "v_mfma_f32_16x16x32_bf16 v[20:23], v[168:171], v[192:195], v[20:23]\n"
        "ds_read_b64_tr_b16 v[58:59], v229 offset:16384\n"
        "v_mfma_f32_16x16x32_bf16 v[84:87], v[168:171], v[196:199], v[84:87]\n"
        "global_store_dwordx4 v254, a[4:7], s[70:71] offset:1024 sc1\n"
        "s_add_u32 s70, s38, s97\n"
        "s_addc_u32 s71, s39, 0\n"
        "s_add_u32 s97, s53, 0x2000\n"
        "v_mfma_f32_16x16x32_bf16 v[84:87], v[172:175], v[212:215], v[84:87]\n"
        "v_mfma_f32_16x16x32_bf16 v[80:83], v[164:167], v[212:215], v[80:83]\n"
        "v_mfma_f32_16x16x32_bf16 v[80:83], v[160:163], v[196:199], v[80:83]\n"
        "v_mfma_f32_16x16x32_bf16 v[112:115], v[160:163], v[200:203], v[112:115]\n"
        "v_mfma_f32_16x16x32_bf16 v[112:115], v[164:167], v[216:219], v[112:115]\n"
        "v_mfma_f32_16x16x32_bf16 v[116:119], v[172:175], v[216:219], v[116:119]\n"
        "v_mfma_f32_16x16x32_bf16 v[116:119], v[168:171], v[200:203], v[116:119]\n"
        "v_mfma_f32_16x16x32_bf16 v[148:151], v[168:171], v[204:207], v[148:151]\n"
        "ds_read_b64_tr_b16 v[168:169], v232 offset:16384\n"
        "v_mfma_f32_16x16x32_bf16 v[148:151], v[172:175], v[220:223], v[148:151]\n"
        "ds_read_b64_tr_b16 v[172:173], v232 offset:24576\n"
        "v_mfma_f32_16x16x32_bf16 v[144:147], v[164:167], v[220:223], v[144:147]\n"
        "ds_read_b64_tr_b16 v[164:165], v230 offset:24576\n"
        "v_mfma_f32_16x16x32_bf16 v[144:147], v[160:163], v[204:207], v[144:147]\n"
        "global_store_dwordx4 v254, a[8:11], s[70:71] offset:0 sc1\n"
        "ds_read_b64_tr_b16 v[160:161], v230 offset:16384\n"
        "ds_read_b64_tr_b16 v[162:163], v231 offset:16384\n"
        "v_mfma_f32_16x16x32_bf16 v[24:27], v[32:35], v[192:195], v[24:27]\n"
        "ds_read_b64_tr_b16 v[166:167], v231 offset:24576\n"
        "v_mfma_f32_16x16x32_bf16 v[24:27], v[36:39], v[208:211], v[24:27]\n"
        "ds_read_b64_tr_b16 v[174:175], v233 offset:24576\n"
        "v_mfma_f32_16x16x32_bf16 v[28:31], v[44:47], v[208:211], v[28:31]\n"
        "ds_read_b64_tr_b16 v[170:171], v233 offset:16384\n"
        "v_mfma_f32_16x16x32_bf16 v[28:31], v[40:43], v[192:195], v[28:31]\n"
        "ds_read_b128 v[192:195], v246 offset:34816\n"
        "v_mfma_f32_16x16x32_bf16 v[92:95], v[40:43], v[196:199], v[92:95]\n"
        "ds_read_b128 v[208:211], v247 offset:34816\n"
        "v_mfma_f32_16x16x32_bf16 v[92:95], v[44:47], v[212:215], v[92:95]\n"
        "v_mfma_f32_16x16x32_bf16 v[88:91], v[36:39], v[212:215], v[88:91]\n"
        "ds_read_b128 v[212:215], v247 offset:32768\n"
        "v_mfma_f32_16x16x32_bf16 v[88:91], v[32:35], v[196:199], v[88:91]\n"
        "ds_read_b128 v[196:199], v246 offset:32768\n"
        "v_mfma_f32_16x16x32_bf16 v[120:123], v[32:35], v[200:203], v[120:123]\n"
        "v_mfma_f32_16x16x32_bf16 v[120:123], v[36:39], v[216:219], v[120:123]\n"
        "v_mfma_f32_16x16x32_bf16 v[124:127], v[44:47], v[216:219], v[124:127]\n"
        "ds_read_b128 v[216:219], v247 offset:38912\n"
        "v_mfma_f32_16x16x32_bf16 v[124:127], v[40:43], v[200:203], v[124:127]\n"
        "ds_read_b128 v[200:203], v246 offset:38912\n"
        "v_mfma_f32_16x16x32_bf16 v[156:159], v[40:43], v[204:207], v[156:159]\n"
        "ds_read_b64_tr_b16 v[40:41], v228 offset:17408\n"
        "s_waitcnt lgkmcnt(5)\n"
        "v_mfma_f32_16x16x32_bf16 v[156:159], v[44:47], v[220:223], v[156:159]\n"
        "ds_read_b64_tr_b16 v[44:45], v228 offset:25600\n"
        "v_mfma_f32_16x16x32_bf16 v[152:155], v[36:39], v[220:223], v[152:155]\n"
        "ds_read_b128 v[220:223], v247 offset:36864\n"
        "v_mfma_f32_16x16x32_bf16 v[152:155], v[32:35], v[204:207], v[152:155]\n"
        "ds_read_b128 v[204:207], v246 offset:36864\n"
        "v_mfma_f32_16x16x32_bf16 v[0:3], v[48:51], v[192:195], v[0:3]\n"
        "global_store_dwordx4 v254, a[12:15], s[70:71] offset:1024 sc1\n"
        "ds_read_b64_tr_b16 v[32:33], v226 offset:17408\n"
        "ds_read_b64_tr_b16 v[34:35], v227 offset:17408\n"
        "s_add_u32 s70, s38, s97\n"
        "s_addc_u32 s71, s39, 0\n"
        "v_mfma_f32_16x16x32_bf16 v[0:3], v[52:55], v[208:211], v[0:3]\n"
        "ds_read_b64_tr_b16 v[36:37], v226 offset:25600\n"
        "s_add_u32 s97, s53, 0x0\n"
        "s_waitcnt lgkmcnt(8)\n"
        "v_mfma_f32_16x16x32_bf16 v[4:7], v[60:63], v[208:211], v[4:7]\n"
        "ds_read_b64_tr_b16 v[38:39], v227 offset:25600\n"
        "v_mfma_f32_16x16x32_bf16 v[4:7], v[56:59], v[192:195], v[4:7]\n"
        "ds_read_b64_tr_b16 v[46:47], v229 offset:25600\n"
        "v_mfma_f32_16x16x32_bf16 v[68:71], v[56:59], v[196:199], v[68:71]\n"
        "ds_read_b64_tr_b16 v[42:43], v229 offset:17408\n"
        "s_waitcnt lgkmcnt(8)\n"
        "v_mfma_f32_16x16x32_bf16 v[68:71], v[60:63], v[212:215], v[68:71]\n"
        "v_mfma_f32_16x16x32_bf16 v[64:67], v[52:55], v[212:215], v[64:67]\n"
        "v_mfma_f32_16x16x32_bf16 v[64:67], v[48:51], v[196:199], v[64:67]\n"
        "v_mfma_f32_16x16x32_bf16 v[96:99], v[48:51], v[200:203], v[96:99]\n"
        "s_waitcnt lgkmcnt(4)\n"
        "v_mfma_f32_16x16x32_bf16 v[96:99], v[52:55], v[216:219], v[96:99]\n"
        "v_mfma_f32_16x16x32_bf16 v[100:103], v[60:63], v[216:219], v[100:103]\n"
        "v_mfma_f32_16x16x32_bf16 v[100:103], v[56:59], v[200:203], v[100:103]\n"
        "global_store_dwordx4 v254, a[208:211], s[70:71] offset:0 sc1\n"
        "v_mfma_f32_16x16x32_bf16 v[132:135], v[56:59], v[204:207], v[132:135]\n"
        "ds_read_b64_tr_b16 v[56:57], v232 offset:17408\n"
        "v_mfma_f32_16x16x32_bf16 v[132:135], v[60:63], v[220:223], v[132:135]\n"
        "ds_read_b64_tr_b16 v[60:61], v232 offset:25600\n"
        "v_mfma_f32_16x16x32_bf16 v[128:131], v[52:55], v[220:223], v[128:131]\n"
        "ds_read_b64_tr_b16 v[52:53], v230 offset:25600\n"
        "v_mfma_f32_16x16x32_bf16 v[128:131], v[48:51], v[204:207], v[128:131]\n"
        "ds_read_b64_tr_b16 v[48:49], v230 offset:17408\n"
        "v_mfma_f32_16x16x32_bf16 v[8:11], v[160:163], v[192:195], v[8:11]\n"
        "ds_read_b64_tr_b16 v[50:51], v231 offset:17408\n"
        "v_mfma_f32_16x16x32_bf16 v[8:11], v[164:167], v[208:211], v[8:11]\n"
        "ds_read_b64_tr_b16 v[54:55], v231 offset:25600\n"
        "v_mfma_f32_16x16x32_bf16 v[12:15], v[172:175], v[208:211], v[12:15]\n"
        "ds_read_b64_tr_b16 v[62:63], v233 offset:25600\n"
        "v_mfma_f32_16x16x32_bf16 v[12:15], v[168:171], v[192:195], v[12:15]\n"
        "ds_read_b64_tr_b16 v[58:59], v233 offset:17408\n"
        "v_mfma_f32_16x16x32_bf16 v[76:79], v[168:171], v[196:199], v[76:79]\n"
        "v_mfma_f32_16x16x32_bf16 v[76:79], v[172:175], v[212:215], v[76:79]\n"
        "v_mfma_f32_16x16x32_bf16 v[72:75], v[164:167], v[212:215], v[72:75]\n"
        "v_mfma_f32_16x16x32_bf16 v[72:75], v[160:163], v[196:199], v[72:75]\n"
        "v_mfma_f32_16x16x32_bf16 v[104:107], v[160:163], v[200:203], v[104:107]\n"
        "v_mfma_f32_16x16x32_bf16 v[104:107], v[164:167], v[216:219], v[104:107]\n"
        "v_mfma_f32_16x16x32_bf16 v[108:111], v[172:175], v[216:219], v[108:111]\n"
        "v_mfma_f32_16x16x32_bf16 v[108:111], v[168:171], v[200:203], v[108:111]\n"
        "global_store_dwordx4 v254, a[240:243], s[70:71] offset:1024 sc1\n"
        "s_waitcnt vmcnt(7) lgkmcnt(0)\n"
        "s_barrier\n"
        "v_mfma_f32_16x16x32_bf16 v[140:143], v[168:171], v[204:207], v[140:143]\n"
        "ds_write_b128 v244, a[200:203] offset:0\n"
        "v_mfma_f32_16x16x32_bf16 v[140:143], v[172:175], v[220:223], v[140:143]\n"
        "ds_read_b64_tr_b16 v[172:173], v228 offset:40960\n"
        "v_mfma_f32_16x16x32_bf16 v[136:139], v[164:167], v[220:223], v[136:139]\n"
        "ds_read_b64_tr_b16 v[164:165], v226 offset:40960\n"
        "v_mfma_f32_16x16x32_bf16 v[136:139], v[160:163], v[204:207], v[136:139]\n"
        "ds_write_b128 v244, a[204:207] offset:1024\n"
        "v_mfma_f32_16x16x32_bf16 v[16:19], v[32:35], v[192:195], v[16:19]\n"
        "ds_read_b64_tr_b16 v[160:161], v226 offset:32768\n"
        "global_load_dword v253, v252, s[46:47] offset:0\n"
        "v_mfma_f32_16x16x32_bf16 v[16:19], v[36:39], v[208:211], v[16:19]\n"
        "ds_read_b64_tr_b16 v[162:163], v227 offset:32768\n"
        "v_mfma_f32_16x16x32_bf16 v[20:23], v[44:47], v[208:211], v[20:23]\n"
        "ds_write_b128 v244, a[160:163] offset:4096\n"
        "v_mfma_f32_16x16x32_bf16 v[20:23], v[40:43], v[192:195], v[20:23]\n"
        "ds_read_b64_tr_b16 v[166:167], v227 offset:40960\n"
        "v_mfma_f32_16x16x32_bf16 v[84:87], v[40:43], v[196:199], v[84:87]\n"
        "ds_read_b64_tr_b16 v[174:175], v229 offset:40960\n"
        "v_mfma_f32_16x16x32_bf16 v[84:87], v[44:47], v[212:215], v[84:87]\n"
        "ds_write_b128 v244, a[164:167] offset:5120\n"
        "v_mfma_f32_16x16x32_bf16 v[80:83], v[36:39], v[212:215], v[80:83]\n"
        "ds_read_b64_tr_b16 v[168:169], v228 offset:32768\n"
        "v_mfma_f32_16x16x32_bf16 v[80:83], v[32:35], v[196:199], v[80:83]\n"
        "ds_read_b64_tr_b16 v[170:171], v229 offset:32768\n"
        "v_mfma_f32_16x16x32_bf16 v[112:115], v[32:35], v[200:203], v[112:115]\n"
        "ds_write_b128 v244, a[168:171] offset:8192\n"
        "v_mfma_f32_16x16x32_bf16 v[112:115], v[36:39], v[216:219], v[112:115]\n"
        "global_store_dwordx4 v254, a[212:215], s[70:71] offset:2048 sc1\n"
        "v_mfma_f32_16x16x32_bf16 v[116:119], v[44:47], v[216:219], v[116:119]\n"
        "v_mfma_f32_16x16x32_bf16 v[116:119], v[40:43], v[200:203], v[116:119]\n"
        "ds_write_b128 v244, a[172:175] offset:9216\n"
        "v_mfma_f32_16x16x32_bf16 v[148:151], v[40:43], v[204:207], v[148:151]\n"
        "ds_read_b64_tr_b16 v[40:41], v232 offset:32768\n"
        "v_mfma_f32_16x16x32_bf16 v[148:151], v[44:47], v[220:223], v[148:151]\n"
        "ds_read_b64_tr_b16 v[44:45], v232 offset:40960\n"
        "v_mfma_f32_16x16x32_bf16 v[144:147], v[36:39], v[220:223], v[144:147]\n"
        "ds_write_b128 v244, a[0:3] offset:12288\n"
        "v_mfma_f32_16x16x32_bf16 v[144:147], v[32:35], v[204:207], v[144:147]\n"
        "ds_read_b64_tr_b16 v[32:33], v230 offset:32768\n"
        "v_mfma_f32_16x16x32_bf16 v[24:27], v[48:51], v[192:195], v[24:27]\n"
        "ds_read_b64_tr_b16 v[34:35], v231 offset:32768\n"
        "v_mfma_f32_16x16x32_bf16 v[24:27], v[52:55], v[208:211], v[24:27]\n"
        "ds_write_b128 v244, a[4:7] offset:13312\n"
        "v_mfma_f32_16x16x32_bf16 v[28:31], v[60:63], v[208:211], v[28:31]\n"
        "ds_read_b64_tr_b16 v[36:37], v230 offset:40960\n"
        "v_xor_b32 v244, 0xc000, v244\n"
        "v_mfma_f32_16x16x32_bf16 v[28:31], v[56:59], v[192:195], v[28:31]\n"
        "ds_read_b64_tr_b16 v[38:39], v231 offset:40960\n"
        "v_mfma_f32_16x16x32_bf16 v[92:95], v[56:59], v[196:199], v[92:95]\n"
        "ds_read_b64_tr_b16 v[46:47], v233 offset:40960\n"
        "v_mfma_f32_16x16x32_bf16 v[92:95], v[60:63], v[212:215], v[92:95]\n"
        "ds_read_b64_tr_b16 v[42:43], v233 offset:32768\n"
        "v_mfma_f32_16x16x32_bf16 v[88:91], v[52:55], v[212:215], v[88:91]\n"
        "ds_read_b128 v[192:195], v246 offset:2048\n"
        "v_mfma_f32_16x16x32_bf16 v[88:91], v[48:51], v[196:199], v[88:91]\n"
        "ds_read_b128 v[208:211], v247 offset:2048\n"
        "v_mfma_f32_16x16x32_bf16 v[120:123], v[48:51], v[200:203], v[120:123]\n"
        "ds_read_b128 v[196:199], v246 offset:0\n"
        "v_mfma_f32_16x16x32_bf16 v[120:123], v[52:55], v[216:219], v[120:123]\n"
        "ds_read_b128 v[212:215], v247 offset:0\n"
        "v_mfma_f32_16x16x32_bf16 v[124:127], v[60:63], v[216:219], v[124:127]\n"
        "global_store_dwordx4 v254, a[244:247], s[70:71] offset:3072 sc1\n"
        "ds_read_b128 v[216:219], v247 offset:6144\n"
        "s_add_u32 s70, s38, s97\n"
        "s_addc_u32 s71, s39, 0\n"
        "s_add_u32 s97, s53, 0x0\n"
        "s_add_u32 s97, s97, s75\n"
        "v_mfma_f32_16x16x32_bf16 v[124:127], v[56:59], v[200:203], v[124:127]\n"
        "ds_read_b128 v[200:203], v246 offset:6144\n"
        "v_mfma_f32_16x16x32_bf16 v[156:159], v[56:59], v[204:207], v[156:159]\n"
        "ds_read_b64_tr_b16 v[56:57], v228 offset:33792\n"
        "v_mfma_f32_16x16x32_bf16 v[156:159], v[60:63], v[220:223], v[156:159]\n"
        "ds_read_b64_tr_b16 v[60:61], v228 offset:41984\n"
        "s_waitcnt lgkmcnt(7)\n"
        "v_mfma_f32_16x16x32_bf16 v[152:155], v[52:55], v[220:223], v[152:155]\n"
        "ds_read_b128 v[220:223], v247 offset:4096\n"
        "s_waitcnt lgkmcnt(7)\n"
        "v_mfma_f32_16x16x32_bf16 v[152:155], v[48:51], v[204:207], v[152:155]\n"
        "LHA9_%=:\n"
        "s_cbranch_execz LHRA_%=\n"
        "ds_read_b128 v[204:207], v246 offset:4096\n"
        "v_mfma_f32_16x16x32_bf16 v[0:3], v[160:163], v[192:195], v[0:3]\n"
        "ds_read_b64_tr_b16 v[48:49], v226 offset:33792\n"
        "s_waitcnt lgkmcnt(7)\n"
        "v_mfma_f32_16x16x32_bf16 v[0:3], v[164:167], v[208:211], v[0:3]\n"
        "ds_read_b64_tr_b16 v[50:51], v227 offset:33792\n"
        "v_mfma_f32_16x16x32_bf16 v[4:7], v[172:175], v[208:211], v[4:7]\n"
        "ds_read_b64_tr_b16 v[52:53], v226 offset:41984\n"
        "v_mfma_f32_16x16x32_bf16 v[4:7], v[168:171], v[192:195], v[4:7]\n"
        "ds_read_b64_tr_b16 v[54:55], v227 offset:41984\n"
        "v_mfma_f32_16x16x32_bf16 v[68:71], v[168:171], v[196:199], v[68:71]\n"
        "ds_read_b64_tr_b16 v[62:63], v229 offset:41984\n"
        "s_waitcnt lgkmcnt(8)\n"
        "v_mfma_f32_16x16x32_bf16 v[68:71], v[172:175], v[212:215], v[68:71]\n"
        "ds_read_b64_tr_b16 v[58:59], v229 offset:33792\n"
        "v_cvt_pk_bf16_f32 v0, v0, v1\n"
        "v_mfma_f32_16x16x32_bf16 v[64:67], v[164:167], v[212:215], v[64:67]\n"
        "ds_write_b128 v244, a[8:11] offset:0\n"
        "v_mfma_f32_16x16x32_bf16 v[64:67], v[160:163], v[196:199], v[64:67]\n"
        "global_store_dwordx4 v254, a[216:219], s[70:71] offset:0 sc1\n"
        "v_cvt_pk_bf16_f32 v1, v2, v3\n"
        "v_cvt_pk_bf16_f32 v2, v4, v5\n"
        "v_cvt_pk_bf16_f32 v3, v6, v7\n"
        "v_mfma_f32_16x16x32_bf16 v[96:99], v[160:163], v[200:203], v[96:99]\n"
        "s_waitcnt lgkmcnt(5)\n"
        "v_mfma_f32_16x16x32_bf16 v[96:99], v[164:167], v[216:219], v[96:99]\n"
        "ds_write_b128 v244, a[12:15] offset:1024\n"
        "v_mfma_f32_16x16x32_bf16 v[100:103], v[172:175], v[216:219], v[100:103]\n"
        "v_cvt_pk_bf16_f32 v64, v64, v65\n"
        "v_cvt_pk_bf16_f32 v65, v66, v67\n"
        "v_mfma_f32_16x16x32_bf16 v[100:103], v[168:171], v[200:203], v[100:103]\n"
        "v_cvt_pk_bf16_f32 v66, v68, v69\n"
        "v_cvt_pk_bf16_f32 v67, v70, v71\n"
        "v_mfma_f32_16x16x32_bf16 v[132:135], v[168:171], v[204:207], v[132:135]\n"
        "ds_write_b128 v244, a[192:195] offset:4096\n"
        "v_mfma_f32_16x16x32_bf16 v[132:135], v[172:175], v[220:223], v[132:135]\n"
        "ds_read_b64_tr_b16 v[172:173], v232 offset:41984\n"
        "v_cvt_pk_bf16_f32 v96, v96, v97\n"
        "v_mfma_f32_16x16x32_bf16 v[128:131], v[164:167], v[220:223], v[128:131]\n"
        "ds_read_b64_tr_b16 v[164:165], v230 offset:41984\n"
        "v_cvt_pk_bf16_f32 v97, v98, v99\n"
        "v_mfma_f32_16x16x32_bf16 v[128:131], v[160:163], v[204:207], v[128:131]\n"
        "ds_write_b128 v244, a[196:199] offset:5120\n"
        "v_mfma_f32_16x16x32_bf16 v[8:11], v[32:35], v[192:195], v[8:11]\n"
        "ds_read_b64_tr_b16 v[160:161], v230 offset:33792\n"
        "v_cvt_pk_bf16_f32 v98, v100, v101\n"
        "v_mfma_f32_16x16x32_bf16 v[8:11], v[36:39], v[208:211], v[8:11]\n"
        "ds_read_b64_tr_b16 v[162:163], v231 offset:33792\n"
        "v_cvt_pk_bf16_f32 v99, v102, v103\n"
        "v_mfma_f32_16x16x32_bf16 v[12:15], v[44:47], v[208:211], v[12:15]\n"
        "ds_read_b64_tr_b16 v[166:167], v231 offset:41984\n"
        "v_cvt_pk_bf16_f32 v128, v128, v129\n"
        "v_mfma_f32_16x16x32_bf16 v[12:15], v[40:43], v[192:195], v[12:15]\n"
        "ds_read_b64_tr_b16 v[174:175], v233 offset:41984\n"
        "v_cvt_pk_bf16_f32 v129, v130, v131\n"
        "v_mfma_f32_16x16x32_bf16 v[76:79], v[40:43], v[196:199], v[76:79]\n"
        "ds_read_b64_tr_b16 v[168:169], v232 offset:33792\n"
        "v_cvt_pk_bf16_f32 v4, v8, v9\n"
        "v_mfma_f32_16x16x32_bf16 v[76:79], v[44:47], v[212:215], v[76:79]\n"
        "ds_read_b64_tr_b16 v[170:171], v233 offset:33792\n"
        "v_cvt_pk_bf16_f32 v5, v10, v11\n"
        "v_mfma_f32_16x16x32_bf16 v[72:75], v[36:39], v[212:215], v[72:75]\n"
        "ds_write_b128 v244, a[232:235] offset:8192\n"
        "v_mfma_f32_16x16x32_bf16 v[72:75], v[32:35], v[196:199], v[72:75]\n"
        "global_store_dwordx4 v254, a[248:251], s[70:71] offset:1024 sc1\n"
        "v_cvt_pk_bf16_f32 v6, v12, v13\n"
        "v_cvt_pk_bf16_f32 v7, v14, v15\n"
        "v_cvt_pk_bf16_f32 v70, v76, v77\n"
        "v_cvt_pk_bf16_f32 v71, v78, v79\n"
        "v_mfma_f32_16x16x32_bf16 v[104:107], v[32:35], v[200:203], v[104:107]\n"
        "v_cvt_pk_bf16_f32 v130, v132, v133\n"
        "v_cvt_pk_bf16_f32 v131, v134, v135\n"
        "v_mfma_f32_16x16x32_bf16 v[104:107], v[36:39], v[216:219], v[104:107]\n"
        "ds_write_b128 v244, a[236:239] offset:9216\n"
        "v_mfma_f32_16x16x32_bf16 v[108:111], v[44:47], v[216:219], v[108:111]\n"
        "s_add_u32 s82, s82, 1\n"
        "v_cvt_pk_bf16_f32 v68, v72, v73\n"
        "v_cvt_pk_bf16_f32 v69, v74, v75\n"
        "v_mfma_f32_16x16x32_bf16 v[108:111], v[40:43], v[200:203], v[108:111]\n"
        "s_waitcnt lgkmcnt(2)\n"
        "s_cmp_ge_u32 s82, s44\n"
        "s_addc_u32 s90, s82, 0\n"
        "v_mfma_f32_16x16x32_bf16 v[140:143], v[40:43], v[204:207], v[140:143]\n"
        "ds_write_b128 v244, a[224:227] offset:12288\n"
        "v_mfma_f32_16x16x32_bf16 v[140:143], v[44:47], v[220:223], v[140:143]\n"
        "s_barrier\n"
        "v_mfma_f32_16x16x32_bf16 v[136:139], v[36:39], v[220:223], v[136:139]\n"
        "s_lshl_b32 s90, s90, 15\n"
        "v_cndmask_b32 v36, 0, v242, s[10:11]\n"
        "v_cndmask_b32 v37, 0, v242, s[12:13]\n"
        "v_mfma_f32_16x16x32_bf16 v[136:139], v[32:35], v[204:207], v[136:139]\n"
        "ds_write_b128 v244, a[228:231] offset:13312\n"
        "v_mfma_f32_16x16x32_bf16 v[16:19], v[48:51], v[192:195], v[16:19]\n"
        "global_store_dwordx4 v254, a[220:223], s[70:71] offset:2048 sc1\n"
        "s_add_u32 s68, s64, s90\n"
        "s_addc_u32 s69, s65, 0\n"
        "s_xor_b32 s78, s78, 0x8000\n"
        "v_cndmask_b32 v38, 0, v242, s[14:15]\n"
        "v_cndmask_b32 v39, 0, v242, s[16:17]\n"
        "v_mfma_f32_16x16x32_bf16 v[16:19], v[52:55], v[208:211], v[16:19]\n"
        "s_lshr_b32 s52, s41, 1\n"
        "v_cvt_pk_bf16_f32 v100, v104, v105\n"
        "v_cvt_pk_bf16_f32 v101, v106, v107\n"
        "v_mfma_f32_16x16x32_bf16 v[20:23], v[60:63], v[208:211], v[20:23]\n"
        "ds_write_b128 v245, a[176:179] offset:0\n"
        "v_mfma_f32_16x16x32_bf16 v[20:23], v[56:59], v[192:195], v[20:23]\n"
        "v_cvt_pk_bf16_f32 v102, v108, v109\n"
        "s_nop 0\n"
        "v_cvt_pk_bf16_f32 v8, v16, v17\n"
        "v_mfma_f32_16x16x32_bf16 v[84:87], v[56:59], v[196:199], v[84:87]\n"
        "s_barrier\n"
        "v_mfma_f32_16x16x32_bf16 v[84:87], v[60:63], v[212:215], v[84:87]\n"
        "ds_read_b128 a[96:99], v240 offset:0\n"
        "s_lshl_b32 s52, s52, 11\n"
        "v_cvt_pk_bf16_f32 v9, v18, v19\n"
        "v_mfma_f32_16x16x32_bf16 v[80:83], v[52:55], v[212:215], v[80:83]\n"
        "ds_read_b128 a[100:103], v240 offset:1024\n"
        "v_cvt_pk_bf16_f32 v10, v20, v21\n"
        "v_mfma_f32_16x16x32_bf16 v[80:83], v[48:51], v[196:199], v[80:83]\n"
        "ds_read_b128 a[104:107], v240 offset:2048\n"
        "v_cvt_pk_bf16_f32 v11, v22, v23\n"
        "v_mfma_f32_16x16x32_bf16 v[112:115], v[48:51], v[200:203], v[112:115]\n"
        "ds_read_b128 a[108:111], v240 offset:3072\n"
        "v_cvt_pk_bf16_f32 v74, v84, v85\n"
        "v_mfma_f32_16x16x32_bf16 v[112:115], v[52:55], v[216:219], v[112:115]\n"
        "ds_write_b128 v245, a[180:183] offset:1024\n"
        "v_mfma_f32_16x16x32_bf16 v[116:119], v[60:63], v[216:219], v[116:119]\n"
        "ds_read_b128 a[112:115], v240 offset:4096\n"
        "v_cvt_pk_bf16_f32 v72, v80, v81\n"
        "v_mfma_f32_16x16x32_bf16 v[116:119], v[56:59], v[200:203], v[116:119]\n"
        "ds_read_b128 a[116:119], v240 offset:5120\n"
        "v_cvt_pk_bf16_f32 v73, v82, v83\n"
        "v_mfma_f32_16x16x32_bf16 v[148:151], v[56:59], v[204:207], v[148:151]\n"
        "ds_read_b128 a[120:123], v240 offset:6144\n"
        "v_cvt_pk_bf16_f32 v75, v86, v87\n"
        "v_mfma_f32_16x16x32_bf16 v[148:151], v[60:63], v[220:223], v[148:151]\n"
        "ds_write_b128 v245, a[184:187] offset:2048\n"
        "v_mfma_f32_16x16x32_bf16 v[144:147], v[52:55], v[220:223], v[144:147]\n"
        "ds_read_b128 a[124:127], v240 offset:7168\n"
        "v_cndmask_b32 v52, 0, v242, s[20:21]\n"
        "v_mfma_f32_16x16x32_bf16 v[144:147], v[48:51], v[204:207], v[144:147]\n"
        "global_store_dwordx4 v254, a[252:255], s[70:71] offset:3072 sc1\n"
        "s_add_u32 s70, s38, s97\n"
        "s_addc_u32 s71, s39, 0\n"
        "s_add_u32 s97, s53, 0x2000\n"
        "v_cndmask_b32 v53, 0, v242, s[22:23]\n"
        "v_cndmask_b32 v54, 0, v242, s[24:25]\n"
        "v_mfma_f32_16x16x32_bf16 v[24:27], v[160:163], v[192:195], v[24:27]\n"
        "ds_write_b128 v245, a[188:191] offset:3072\n"
        "v_mfma_f32_16x16x32_bf16 v[24:27], v[164:167], v[208:211], v[24:27]\n"
        "s_add_u32 s97, s97, s75\n"
        "v_cndmask_b32 v55, 0, v242, s[28:29]\n"
        "v_cvt_pk_bf16_f32 v103, v110, v111\n"
        "v_mfma_f32_16x16x32_bf16 v[28:31], v[172:175], v[208:211], v[28:31]\n"
        "global_store_dwordx4 v254, a[180:183], s[70:71] offset:1024 sc1\n"
        "s_add_u32 s74, s68, 0x1000\n"
        "s_addc_u32 s75, s69, 0\n"
        "s_lshl2_add_u32 s53, s42, s40\n"
        "v_cvt_pk_bf16_f32 v104, v112, v113\n"
        "v_cvt_pk_bf16_f32 v12, v24, v25\n"
        "v_mfma_f32_16x16x32_bf16 v[28:31], v[168:171], v[192:195], v[28:31]\n"
        "ds_write_b128 v245, a[16:19] offset:8192\n"
        "v_mfma_f32_16x16x32_bf16 v[92:95], v[168:171], v[196:199], v[92:95]\n"
        "s_add_u32 s52, s52, s53\n"
        "v_cvt_pk_bf16_f32 v13, v26, v27\n"
        "v_cvt_pk_bf16_f32 v105, v114, v115\n"
        "v_mfma_f32_16x16x32_bf16 v[92:95], v[172:175], v[212:215], v[92:95]\n"
        "s_waitcnt lgkmcnt(2)\n"
        "v_cvt_pk_bf16_f32 v106, v116, v117\n"
        "v_cvt_pk_bf16_f32 v14, v28, v29\n"
        "v_mfma_f32_16x16x32_bf16 v[88:91], v[164:167], v[212:215], v[88:91]\n"
        "ds_write_b128 v245, a[20:23] offset:9216\n"
        "v_mfma_f32_16x16x32_bf16 v[88:91], v[160:163], v[196:199], v[88:91]\n"
        "s_add_u32 s52, s52, 0\n"
        "v_cvt_pk_bf16_f32 v15, v30, v31\n"
        "v_cvt_pk_bf16_f32 v78, v92, v93\n"
        "v_mfma_f32_16x16x32_bf16 v[120:123], v[160:163], v[200:203], v[120:123]\n"
        "s_lshl_b32 s52, s52, 10\n"
        "v_cvt_pk_bf16_f32 v79, v94, v95\n"
        "v_cvt_pk_bf16_f32 v107, v118, v119\n"
        "v_mfma_f32_16x16x32_bf16 v[32:35], a[96:99], v[0:3], 0\n"
        "ds_write_b128 v245, a[24:27] offset:10240\n"
        "v_mfma_f32_16x16x32_bf16 v[32:35], a[100:103], v[4:7], v[32:35]\n"
        "s_add_u32 s26, s18, s52\n"
        "s_addc_u32 s27, s19, 0\n"
        "v_cvt_pk_bf16_f32 v76, v88, v89\n"
        "v_mfma_f32_16x16x32_bf16 v[32:35], a[104:107], v[8:11], v[32:35]\n"
        "s_add_u32 s76, s66, s90\n"
        "s_addc_u32 s77, s67, 0\n"
        "v_cvt_pk_bf16_f32 v77, v90, v91\n"
        "v_mfma_f32_16x16x32_bf16 v[32:35], a[108:111], v[12:15], v[32:35]\n"
        "ds_write_b128 v245, a[28:31] offset:11264\n"
        "v_mfma_f32_16x16x32_bf16 v[120:123], v[164:167], v[216:219], v[120:123]\n"
        "v_xor_b32 v245, 0xc000, v245\n"
        "v_cvt_pk_bf16_f32 v132, v136, v137\n"
        "v_mfma_f32_16x16x32_bf16 v[124:127], v[172:175], v[216:219], v[124:127]\n"
        "v_cvt_pk_bf16_f32 v133, v138, v139\n"
        "v_cvt_pk_bf16_f32 v134, v140, v141\n"
        "v_mfma_f32_16x16x32_bf16 v[124:127], v[168:171], v[200:203], v[124:127]\n"
        "v_max_f32 v246, v32, v33\n"
        "v_max3_f32 v246, v34, v35, v246\n"
        "v_mfma_f32_16x16x32_bf16 v[156:159], v[168:171], v[204:207], v[156:159]\n"
        "v_mov_b32 v247, v246\n"
        "v_cvt_pk_bf16_f32 v108, v120, v121\n"
        "v_mfma_f32_16x16x32_bf16 v[36:39], a[108:111], v[76:79], v[36:39]\n"
        "v_permlane16_swap_b32 v246, v247\n"
        "v_mfma_f32_16x16x32_bf16 v[36:39], a[104:107], v[72:75], v[36:39]\n"
        "ds_read_b128 a[104:107], v240 offset:10240\n"
        "v_max_f32 v246, v246, v247\n"
        "v_mfma_f32_16x16x32_bf16 v[36:39], a[100:103], v[68:71], v[36:39]\n"
        "ds_read_b128 a[100:103], v240 offset:9216\n"
        "v_mov_b32 v247, v246\n"
        "v_mfma_f32_16x16x32_bf16 v[36:39], a[96:99], v[64:67], v[36:39]\n"
        "global_store_dwordx4 v254, a[176:179], s[70:71] offset:0 sc1\n"
        "s_waitcnt lgkmcnt(2)\n"
        "v_permlane32_swap_b32 v246, v247\n"
        "v_max_f32 v246, v246, v247\n"
        "v_mfma_f32_16x16x32_bf16 v[156:159], v[172:175], v[220:223], v[156:159]\n"
        "s_barrier\n"
        "v_mfma_f32_16x16x32_bf16 v[152:155], v[164:167], v[220:223], v[152:155]\n"
        "ds_read_b128 a[96:99], v240 offset:8192\n"
        "v_sub_f32 v224, 0, v246\n"
        "v_mfma_f32_16x16x32_bf16 v[152:155], v[160:163], v[204:207], v[152:155]\n"
        "ds_read_b128 a[144:147], v241 offset:0\n"
        "v_sub_f32 v225, 0, v246\n"
        "v_mfma_f32_16x16x32_bf16 v[48:51], a[112:115], v[0:3], 0\n"
        "ds_read_b128 a[108:111], v240 offset:11264\n"
        "v_sub_f32 v226, 0, v246\n"
        "v_mfma_f32_16x16x32_bf16 v[48:51], a[116:119], v[4:7], v[48:51]\n"
        "ds_read_b128 a[148:151], v241 offset:1024\n"
        "v_sub_f32 v227, 0, v246\n"
        "v_mfma_f32_16x16x32_bf16 v[48:51], a[120:123], v[8:11], v[48:51]\n"
        "ds_read_b128 a[152:155], v241 offset:2048\n"
        "v_max_f32 v248, v36, v37\n"
        "v_mfma_f32_16x16x32_bf16 v[48:51], a[124:127], v[12:15], v[48:51]\n"
        "ds_read_b128 a[156:159], v241 offset:3072\n"
        "v_max3_f32 v248, v38, v39, v248\n"
        "v_mfma_f32_16x16x32_bf16 v[52:55], a[124:127], v[76:79], v[52:55]\n"
        "ds_read_b128 a[128:131], v241 offset:4096\n"
        "v_mov_b32 v249, v248\n"
        "v_mfma_f32_16x16x32_bf16 v[52:55], a[120:123], v[72:75], v[52:55]\n"
        "global_store_dwordx4 v254, a[184:187], s[70:71] offset:2048 sc1\n"
        "v_permlane16_swap_b32 v248, v249\n"
        "s_waitcnt lgkmcnt(6)\n"
        "v_max_f32 v248, v248, v249\n"
        "v_mfma_f32_16x16x32_bf16 v[52:55], a[116:119], v[68:71], v[52:55]\n"
        "ds_read_b128 a[116:119], v240 offset:13312\n"
        "v_mov_b32 v249, v248\n"
        "v_mfma_f32_16x16x32_bf16 v[52:55], a[112:115], v[64:67], v[52:55]\n"
        "v_add_f32 v32, v32, v224\n"
        "v_permlane32_swap_b32 v248, v249\n"
        "v_max_f32 v248, v248, v249\n"
        "v_sub_f32 v228, 0, v248\n"
        "v_sub_f32 v229, 0, v248\n"
        "v_sub_f32 v230, 0, v248\n"
        "v_sub_f32 v231, 0, v248\n"
        "v_exp_f32 v32, v32\n"
        "v_add_f32 v33, v33, v224\n"
        "v_exp_f32 v33, v33\n"
        "v_add_f32 v34, v34, v224\n"
        "v_cvt_pk_bf16_f32 v16, v32, v33\n"
        "v_exp_f32 v34, v34\n"
        "v_add_f32 v35, v35, v224\n"
        "v_exp_f32 v35, v35\n"
        "global_store_dwordx4 v254, a[188:191], s[70:71] offset:3072 sc1\n"
        "v_cvt_pk_bf16_f32 v17, v34, v35\n"
        "v_add_f32 v36, v36, v228\n"
        "v_exp_f32 v36, v36\n"
        "v_mfma_f32_16x16x32_bf16 v[32:35], a[96:99], v[0:3], v[224:227]\n"
        "ds_read_b128 a[112:115], v240 offset:12288\n"
        "v_add_f32 v37, v37, v228\n"
        "v_mfma_f32_16x16x32_bf16 v[32:35], a[100:103], v[4:7], v[32:35]\n"
        "s_waitcnt lgkmcnt(1)\n"
        "v_exp_f32 v37, v37\n"
        "v_mfma_f32_16x16x32_bf16 v[32:35], a[104:107], v[8:11], v[32:35]\n"
        "ds_read_b128 a[120:123], v240 offset:14336\n"
        "v_cvt_pk_bf16_f32 v20, v36, v37\n"
        "v_mfma_f32_16x16x32_bf16 v[32:35], a[108:111], v[12:15], v[32:35]\n"
        "v_add_f32 v38, v38, v228\n"
        "v_exp_f32 v38, v38\n"
        "v_add_f32 v39, v39, v228\n"
        "v_exp_f32 v39, v39\n"
        "v_add_f32 v48, v48, v224\n"
        "v_cvt_pk_bf16_f32 v21, v38, v39\n"
        "v_exp_f32 v48, v48\n"
        "v_add_f32 v49, v49, v224\n"
        "v_exp_f32 v49, v49\n"
        "ds_read_b128 a[124:127], v240 offset:15360\n"
        "v_cvt_pk_bf16_f32 v18, v48, v49\n"
        "ds_read_b128 a[132:135], v241 offset:5120\n"
        "s_add_u32 s70, s38, s97\n"
        "s_addc_u32 s71, s39, 0\n"
        "global_store_dwordx4 v254, a[16:19], s[70:71] offset:0 sc1\n"
        "v_add_f32 v50, v50, v224\n"
        "v_exp_f32 v50, v50\n"
        "v_add_f32 v51, v51, v224\n"
        "v_exp_f32 v51, v51\n"
        "ds_read_b128 a[136:139], v241 offset:6144\n"
        "v_cvt_pk_bf16_f32 v19, v50, v51\n"
        "s_waitcnt lgkmcnt(3)\n"
        "v_mfma_f32_16x16x32_bf16 v[48:51], a[112:115], v[0:3], v[224:227]\n"
        "ds_read_b128 a[140:143], v241 offset:7168\n"
        "v_cndmask_b32 v36, v228, v242, s[30:31]\n"
        "v_mfma_f32_16x16x32_bf16 v[48:51], a[116:119], v[4:7], v[48:51]\n"
        "v_exp_f32 v32, v32\n"
        "v_mfma_f32_16x16x32_bf16 v[48:51], a[120:123], v[8:11], v[48:51]\n"
        "v_exp_f32 v33, v33\n"
        "v_mfma_f32_16x16x32_bf16 a[64:67], a[80:83], v[16:19], 0\n"
        "s_waitcnt lgkmcnt(3)\n"
        "v_exp_f32 v34, v34\n"
        "v_mfma_f32_16x16x32_bf16 v[48:51], a[124:127], v[12:15], v[48:51]\n"
        "v_cndmask_b32 v37, v228, v242, s[56:57]\n"
        "v_cndmask_b32 v38, v228, v242, s[58:59]\n"
        "v_mfma_f32_16x16x32_bf16 v[160:163], a[144:147], v[16:19], 0\n"
        "v_cndmask_b32 v39, v228, v242, s[60:61]\n"
        "s_nop 1\n"
        "v_mfma_f32_16x16x32_bf16 v[36:39], a[108:111], v[76:79], v[36:39]\n"
        "ds_read_b128 a[108:111], v240 offset:19456\n"
        "v_add_f32 v52, v52, v228\n"
        "v_mfma_f32_16x16x32_bf16 v[36:39], a[104:107], v[72:75], v[36:39]\n"
        "v_exp_f32 v52, v52\n"
        "v_mfma_f32_16x16x32_bf16 v[36:39], a[100:103], v[68:71], v[36:39]\n"
        "ds_read_b128 a[100:103], v240 offset:17408\n"
        "v_add_f32 v53, v53, v228\n"
        "v_mfma_f32_16x16x32_bf16 v[36:39], a[96:99], v[64:67], v[36:39]\n"
        "v_exp_f32 v53, v53\n"
        "v_mfma_f32_16x16x32_bf16 v[164:167], a[148:151], v[16:19], 0\n"
        "ds_read_b128 a[96:99], v240 offset:16384\n"
        "v_cvt_pk_bf16_f32 v22, v52, v53\n"
        "v_mfma_f32_16x16x32_bf16 v[168:171], a[152:155], v[16:19], 0\n"
        "ds_read_b128 a[104:107], v240 offset:18432\n"
        "v_add_f32 v54, v54, v228\n"
        "v_mfma_f32_16x16x32_bf16 v[172:175], a[156:159], v[16:19], 0\n"
        "v_exp_f32 v54, v54\n"
        "v_mfma_f32_16x16x32_bf16 v[176:179], a[128:131], v[16:19], 0\n"
        "s_waitcnt lgkmcnt(4)\n"
        "v_exp_f32 v35, v35\n"
        "v_mfma_f32_16x16x32_bf16 v[180:183], a[132:135], v[16:19], 0\n"
        "v_add_f32 v55, v55, v228\n"
        "v_cvt_pk_bf16_f32 v80, v32, v33\n"
        "v_mfma_f32_16x16x32_bf16 v[184:187], a[136:139], v[16:19], 0\n"
        "v_exp_f32 v55, v55\n"
        "v_mfma_f32_16x16x32_bf16 v[188:191], a[140:143], v[16:19], 0\n"
        "v_cvt_pk_bf16_f32 v23, v54, v55\n"
        "s_nop 1\n"
        "v_mfma_f32_16x16x32_bf16 a[68:71], a[80:83], v[20:23], 0\n"
        "global_store_dwordx4 v254, a[20:23], s[70:71] offset:1024 sc1\n"
        "s_mul_i32 s96, s40, 4224\n"
        "v_exp_f32 v36, v36\n"
        "v_cndmask_b32 v52, v228, v242, s[62:63]\n"
        "v_cndmask_b32 v53, v228, v242, s[84:85]\n"
        "v_mfma_f32_16x16x32_bf16 v[192:195], a[144:147], v[20:23], 0\n"
        "ds_read_b128 a[144:147], v241 offset:8192\n"
        "v_cndmask_b32 v54, v228, v242, s[86:87]\n"
        "v_mfma_f32_16x16x32_bf16 v[196:199], a[148:151], v[20:23], 0\n"
        "ds_read_b128 a[148:151], v241 offset:9216\n"
        "v_cndmask_b32 v55, v228, v242, s[88:89]\n"
        "s_nop 1\n"
        "v_mfma_f32_16x16x32_bf16 v[52:55], a[124:127], v[76:79], v[52:55]\n"
        "ds_read_b128 a[124:127], v240 offset:23552\n"
        "v_cvt_pk_bf16_f32 v81, v34, v35\n"
        "v_mfma_f32_16x16x32_bf16 v[52:55], a[120:123], v[72:75], v[52:55]\n"
        "v_exp_f32 v37, v37\n"
        "v_mfma_f32_16x16x32_bf16 v[52:55], a[116:119], v[68:71], v[52:55]\n"
        "ds_read_b128 a[116:119], v240 offset:21504\n"
        "v_cvt_pk_bf16_f32 v84, v36, v37\n"
        "v_mfma_f32_16x16x32_bf16 v[52:55], a[112:115], v[64:67], v[52:55]\n"
        "s_waitcnt lgkmcnt(4)\n"
        "ds_read_b128 a[112:115], v240 offset:20480\n"
        "v_mfma_f32_16x16x32_bf16 v[200:203], a[152:155], v[20:23], 0\n"
        "global_store_dwordx4 v254, a[24:27], s[70:71] offset:2048 sc1\n"
        "v_exp_f32 v38, v38\n"
        "v_exp_f32 v39, v39\n"
        "v_mfma_f32_16x16x32_bf16 v[204:207], a[156:159], v[20:23], 0\n"
        "ds_read_b128 a[152:155], v241 offset:10240\n"
        "v_cvt_pk_bf16_f32 v85, v38, v39\n"
        "v_mfma_f32_16x16x32_bf16 v[208:211], a[128:131], v[20:23], 0\n"
        "ds_write_b128 v245, a[216:219] offset:0\n"
        "v_mfma_f32_16x16x32_bf16 v[212:215], a[132:135], v[20:23], 0\n"
        "ds_read_b128 a[120:123], v240 offset:22528\n"
        "s_add_u32 s82, s82, 1\n"
        "v_cndmask_b32 v32, v224, v242, s[10:11]\n"
        "v_mfma_f32_16x16x32_bf16 v[216:219], a[136:139], v[20:23], 0\n"
        "ds_read_b128 a[156:159], v241 offset:11264\n"
        "v_cndmask_b32 v33, v224, v242, s[12:13]\n"
        "v_mfma_f32_16x16x32_bf16 v[220:223], a[140:143], v[20:23], 0\n"
        "v_cndmask_b32 v34, v224, v242, s[14:15]\n"
        "v_cndmask_b32 v35, v224, v242, s[16:17]\n"
        "s_nop 1\n"
        "v_mfma_f32_16x16x32_bf16 v[32:35], a[96:99], v[0:3], v[32:35]\n"
        "v_exp_f32 v48, v48\n"
        "v_mfma_f32_16x16x32_bf16 v[32:35], a[100:103], v[4:7], v[32:35]\n"
        "v_exp_f32 v49, v49\n"
        "v_mfma_f32_16x16x32_bf16 v[32:35], a[104:107], v[8:11], v[32:35]\n"
        "ds_read_b128 a[96:99], v240 offset:24576\n"
        "v_cvt_pk_bf16_f32 v82, v48, v49\n"
        "v_mfma_f32_16x16x32_bf16 v[32:35], a[108:111], v[12:15], v[32:35]\n"
        "v_exp_f32 v50, v50\n"
        "v_exp_f32 v51, v51\n"
        "ds_write_b128 v245, a[248:251] offset:1024\n"
        "v_cvt_pk_bf16_f32 v83, v50, v51\n"
        "s_nop 1\n"
        "v_mfma_f32_16x16x32_bf16 a[64:67], a[80:83], v[80:83], a[64:67]\n"
        "s_waitcnt lgkmcnt(2)\n"
        "ds_read_b128 a[128:131], v241 offset:12288\n"
        "v_mfma_f32_16x16x32_bf16 v[160:163], a[144:147], v[80:83], v[160:163]\n"
        "global_store_dwordx4 v254, a[28:31], s[70:71] offset:3072 sc1\n"
        "s_add_u32 s70, s92, 0x2\n"
        "s_addc_u32 s71, s93, 0\n"
        "v_exp_f32 v52, v52\n"
        "v_cndmask_b32 v48, v224, v242, s[20:21]\n"
        "v_mfma_f32_16x16x32_bf16 v[164:167], a[148:151], v[80:83], v[164:167]\n"
        "v_exp_f32 v53, v53\n"
        "v_mfma_f32_16x16x32_bf16 v[168:171], a[152:155], v[80:83], v[168:171]\n"
        "ds_read_b128 a[132:135], v241 offset:13312\n"
        "v_cvt_pk_bf16_f32 v86, v52, v53\n"
        "v_mfma_f32_16x16x32_bf16 v[172:175], a[156:159], v[80:83], v[172:175]\n"
        "s_cmp_ge_u32 s82, s44\n"
        "s_addc_u32 s90, s82, 0\n"
        "s_lshl_b32 s90, s90, 15\n"
        "ds_read_b128 a[100:103], v240 offset:25600\n"
        "ds_read_b128 a[136:139], v241 offset:14336\n"
        "ds_read_b128 a[104:107], v240 offset:26624\n"
        "v_exp_f32 v54, v54\n"
        "v_cndmask_b32 v49, v224, v242, s[22:23]\n"
        "s_waitcnt lgkmcnt(4)\n"
        "v_mfma_f32_16x16x32_bf16 v[176:179], a[128:131], v[80:83], v[176:179]\n"
        "ds_read_b128 a[140:143], v241 offset:15360\n"
        "ds_write_b128 v245, a[220:223] offset:2048\n"
        "ds_read_b128 a[108:111], v240 offset:27648\n"
        "v_exp_f32 v55, v55\n"
        "s_waitcnt lgkmcnt(6)\n"
        "v_mfma_f32_16x16x32_bf16 v[180:183], a[132:135], v[80:83], v[180:183]\n"
        "v_cvt_pk_bf16_f32 v87, v54, v55\n"
        "s_nop 1\n"
        "v_mfma_f32_16x16x32_bf16 a[68:71], a[80:83], v[84:87], a[68:71]\n"
        "v_exp_f32 v32, v32\n"
        "v_mfma_f32_16x16x32_bf16 v[192:195], a[144:147], v[84:87], v[192:195]\n"
        "ds_read_b128 a[144:147], v241 offset:16384\n"
        "v_cndmask_b32 v50, v224, v242, s[24:25]\n"
        "v_mfma_f32_16x16x32_bf16 v[196:199], a[148:151], v[84:87], v[196:199]\n"
        "ds_read_b128 a[148:151], v241 offset:17408\n"
        "v_cndmask_b32 v51, v224, v242, s[28:29]\n"
        "s_nop 1\n"
        "v_mfma_f32_16x16x32_bf16 v[48:51], a[112:115], v[0:3], v[48:51]\n"
        "v_exp_f32 v33, v33\n"
        "v_mfma_f32_16x16x32_bf16 v[48:51], a[116:119], v[4:7], v[48:51]\n"
        "ds_read_b128 a[112:115], v240 offset:28672\n"
        "v_cvt_pk_bf16_f32 v16, v32, v33\n"
        "v_mfma_f32_16x16x32_bf16 v[48:51], a[120:123], v[8:11], v[48:51]\n"
        "ds_read_b128 a[116:119], v240 offset:29696\n"
        "v_cvt_pk_bf16_f32 v109, v122, v123\n"
        "v_mfma_f32_16x16x32_bf16 v[48:51], a[124:127], v[12:15], v[48:51]\n"
        "s_waitcnt lgkmcnt(4)\n"
        "v_cvt_pk_bf16_f32 v110, v124, v125\n"
        "v_cvt_pk_bf16_f32 v111, v126, v127\n"
        "v_mfma_f32_16x16x32_bf16 v[200:203], a[152:155], v[84:87], v[200:203]\n"
        "ds_read_b128 a[120:123], v240 offset:30720\n"
        "v_cvt_pk_bf16_f32 v135, v142, v143\n"
        "v_mfma_f32_16x16x32_bf16 v[204:207], a[156:159], v[84:87], v[204:207]\n"
        "v_exp_f32 v34, v34\n"
        "v_mfma_f32_16x16x32_bf16 v[208:211], a[128:131], v[84:87], v[208:211]\n"
        "v_exp_f32 v35, v35\n"
        "v_mfma_f32_16x16x32_bf16 v[212:215], a[132:135], v[84:87], v[212:215]\n"
        "ds_read_b128 a[124:127], v240 offset:31744\n"
        "v_cvt_pk_bf16_f32 v17, v34, v35\n"
        "v_mfma_f32_16x16x32_bf16 v[184:187], a[136:139], v[80:83], v[184:187]\n"
        "ds_write_b128 v245, a[252:255] offset:3072\n"
        "v_mfma_f32_16x16x32_bf16 v[216:219], a[136:139], v[84:87], v[216:219]\n"
        "ds_read_b128 a[152:155], v241 offset:18432\n"
        "v_cndmask_b32 v32, v224, v242, s[30:31]\n"
        "v_mfma_f32_16x16x32_bf16 v[220:223], a[140:143], v[84:87], v[220:223]\n"
        "ds_read_b128 a[156:159], v241 offset:19456\n"
        "v_cndmask_b32 v33, v224, v242, s[56:57]\n"
        "v_mfma_f32_16x16x32_bf16 v[188:191], a[140:143], v[80:83], v[188:191]\n"
        "v_cndmask_b32 v34, v224, v242, s[58:59]\n"
        "v_cndmask_b32 v35, v224, v242, s[60:61]\n"
        "s_nop 1\n"
        "v_mfma_f32_16x16x32_bf16 v[32:35], a[96:99], v[0:3], v[32:35]\n"
        "v_exp_f32 v48, v48\n"
        "v_mfma_f32_16x16x32_bf16 v[32:35], a[100:103], v[4:7], v[32:35]\n"
        "v_exp_f32 v49, v49\n"
        "v_mfma_f32_16x16x32_bf16 v[32:35], a[104:107], v[8:11], v[32:35]\n"
        "s_waitcnt lgkmcnt(3)\n"
        "ds_read_b128 a[128:131], v241 offset:20480\n"
        "v_mfma_f32_16x16x32_bf16 v[32:35], a[108:111], v[12:15], v[32:35]\n"
        "v_cvt_pk_bf16_f32 v18, v48, v49\n"
        "ds_write_b128 v245, a[208:211] offset:8192\n"
        "v_cvt_pk_bf16_f32 v136, v144, v145\n"
        "s_barrier\n"
        "v_cvt_pk_bf16_f32 v137, v146, v147\n"
        "v_xor_b32 v240, 0x8000, v240\n"
        "v_cvt_pk_bf16_f32 v138, v148, v149\n"
        "ds_read_b128 a[96:99], v240 offset:0\n"
        "v_cvt_pk_bf16_f32 v139, v150, v151\n"
        "v_exp_f32 v50, v50\n"
        "v_exp_f32 v51, v51\n"
        "v_cndmask_b32 v48, v224, v242, s[62:63]\n"
        "v_cvt_pk_bf16_f32 v19, v50, v51\n"
        "s_nop 1\n"
        "v_mfma_f32_16x16x32_bf16 a[64:67], a[80:83], v[16:19], a[64:67]\n"
        "ds_read_b128 a[132:135], v241 offset:21504\n"
        "v_cndmask_b32 v49, v224, v242, s[84:85]\n"
        "v_mfma_f32_16x16x32_bf16 v[160:163], a[144:147], v[16:19], v[160:163]\n"
        "ds_read_b128 a[100:103], v240 offset:1024\n"
        "v_cndmask_b32 v50, v224, v242, s[86:87]\n"
        "v_mfma_f32_16x16x32_bf16 v[164:167], a[148:151], v[16:19], v[164:167]\n"
        "s_waitcnt lgkmcnt(3)\n"
        "ds_read_b128 a[136:139], v241 offset:22528\n"
        "v_mfma_f32_16x16x32_bf16 v[168:171], a[152:155], v[16:19], v[168:171]\n"
        "ds_read_b128 a[104:107], v240 offset:2048\n"
        "v_cndmask_b32 v51, v224, v242, s[88:89]\n"
        "s_nop 1\n"
        "v_mfma_f32_16x16x32_bf16 v[48:51], a[112:115], v[0:3], v[48:51]\n"
        "ds_read_b128 a[112:115], v240 offset:4096\n"
        "v_lshrrev_b32 v244, 1, v254\n"
        "v_mfma_f32_16x16x32_bf16 v[48:51], a[116:119], v[4:7], v[48:51]\n"
        "ds_read_b128 a[116:119], v240 offset:5120\n"
        "v_add_u32 v244, s81, v244\n"
        "v_mfma_f32_16x16x32_bf16 v[48:51], a[120:123], v[8:11], v[48:51]\n"
        "ds_read_b128 a[120:123], v240 offset:6144\n"
        "s_add_u32 s81, s78, 4096\n"
        "v_cvt_pk_bf16_f32 v140, v152, v153\n"
        "v_mfma_f32_16x16x32_bf16 v[48:51], a[124:127], v[12:15], v[48:51]\n"
        "ds_read_b128 a[124:127], v240 offset:7168\n"
        "v_cvt_pk_bf16_f32 v141, v154, v155\n"
        "v_mfma_f32_16x16x32_bf16 v[172:175], a[156:159], v[16:19], v[172:175]\n"
        "ds_write_b128 v245, a[240:243] offset:9216\n"
        "v_mfma_f32_16x16x32_bf16 v[176:179], a[128:131], v[16:19], v[176:179]\n"
        "s_waitcnt lgkmcnt(6)\n"
        "ds_read_b128 a[140:143], v241 offset:23552\n"
        "v_mfma_f32_16x16x32_bf16 v[180:183], a[132:135], v[16:19], v[180:183]\n"
        "v_exp_f32 v32, v32\n"
        "v_mfma_f32_16x16x32_bf16 v[184:187], a[136:139], v[16:19], v[184:187]\n"
        "ds_read_b128 a[108:111], v240 offset:3072\n"
        "ds_write_b128 v245, a[212:215] offset:10240\n"
        "ds_read_b128 a[144:147], v241 offset:24576\n"
        "v_exp_f32 v33, v33\n"
        "s_waitcnt lgkmcnt(5)\n"
        "v_mfma_f32_16x16x32_bf16 v[52:55], a[124:127], v[76:79], v[228:231]\n"
        "ds_read_b128 a[148:151], v241 offset:25600\n"
        "v_cvt_pk_bf16_f32 v80, v32, v33\n"
        "v_mfma_f32_16x16x32_bf16 v[52:55], a[120:123], v[72:75], v[52:55]\n"
        "ds_write_b128 v245, a[244:247] offset:11264\n"
        "v_mfma_f32_16x16x32_bf16 v[52:55], a[116:119], v[68:71], v[52:55]\n"
        "ds_read_b128 a[152:155], v241 offset:26624\n"
        "v_bfe_u32 v245, v254, 4, 3\n"
        "v_mfma_f32_16x16x32_bf16 v[52:55], a[112:115], v[64:67], v[52:55]\n"
        "s_waitcnt lgkmcnt(6)\n"
        "v_exp_f32 v34, v34\n"
        "v_mfma_f32_16x16x32_bf16 v[188:191], a[140:143], v[16:19], v[188:191]\n"
        "s_waitcnt lgkmcnt(5)\n"
        "v_exp_f32 v35, v35\n"
        "v_mfma_f32_16x16x32_bf16 v[36:39], a[108:111], v[76:79], v[228:231]\n"
        "ds_read_b128 a[156:159], v241 offset:27648\n"
        "s_waitcnt vmcnt(0)\n"
        "v_mfma_f32_16x16x32_bf16 v[36:39], a[104:107], v[72:75], v[36:39]\n"
        "s_barrier\n"
        "v_mfma_f32_16x16x32_bf16 v[36:39], a[100:103], v[68:71], v[36:39]\n"
        "s_cmp_lg_u32 s40, 0\n"
        "s_cbranch_scc1 LW1_%=\n"
        "s_mov_b64 exec, 0xffff\n"
        "global_store_dwordx2 v244, v[250:251], s[48:49] sc1\n"
        "s_mov_b64 exec, -1\n"
        "LW1_%=:\n"
        "v_mfma_f32_16x16x32_bf16 v[36:39], a[96:99], v[64:67], v[36:39]\n"
        "v_lshl_add_u32 v245, v245, 7, s80\n"
        "v_cvt_pk_bf16_f32 v81, v34, v35\n"
        "v_mfma_f32_16x16x32_bf16 v[32:35], a[96:99], v[0:3], v[224:227]\n"
        "ds_read_b128 a[128:131], v241 offset:28672\n"
        "s_mov_b32 s80, s78\n"
        "v_cvt_pk_bf16_f32 v142, v156, v157\n"
        "v_mfma_f32_16x16x32_bf16 v[32:35], a[100:103], v[4:7], v[32:35]\n"
        "ds_read_b128 a[132:135], v241 offset:29696\n"
        "s_add_u32 m0, s80, 0x0\n"
        "v_cvt_pk_bf16_f32 v143, v158, v159\n"
        "v_mfma_f32_16x16x32_bf16 v[32:35], a[104:107], v[8:11], v[32:35]\n"
        "s_waitcnt lgkmcnt(3)\n"
        "ds_read_b128 a[136:139], v241 offset:30720\n"
        "s_xor_b32 s78, s78, 0x8000\n"
        "v_mfma_f32_16x16x32_bf16 v[32:35], a[108:111], v[12:15], v[32:35]\n"
        "ds_read_b128 a[140:143], v241 offset:31744\n"
        "v_exp_f32 v48, v48\n"
        "v_exp_f32 v49, v49\n"
        "v_exp_f32 v50, v50\n"
        "v_cvt_pk_bf16_f32 v82, v48, v49\n"
        "v_exp_f32 v51, v51\n"
        "v_xor_b32 v241, 0x8000, v241\n"
        "v_cvt_pk_bf16_f32 v83, v50, v51\n"
        "s_nop 1\n"
        "v_mfma_f32_16x16x32_bf16 v[164:167], a[148:151], v[80:83], v[164:167]\n"
        "s_mov_b32 s80, s78\n"
        "v_exp_f32 v32, v32\n"
        "v_mfma_f32_16x16x32_bf16 v[48:51], a[112:115], v[0:3], v[224:227]\n"
        "s_mov_b32 s91, s44\n"
        "v_exp_f32 v33, v33\n"
        "v_mfma_f32_16x16x32_bf16 v[48:51], a[116:119], v[4:7], v[48:51]\n"
        "LHA10_%=:\n"
        "s_cbranch_execz LHRA_%=\n"
        "v_exp_f32 v34, v34\n"
        "v_mfma_f32_16x16x32_bf16 v[48:51], a[120:123], v[8:11], v[48:51]\n"
        "v_exp_f32 v35, v35\n"
        "v_mfma_f32_16x16x32_bf16 v[48:51], a[124:127], v[12:15], v[48:51]\n"
        "s_waitcnt lgkmcnt(0)\n"
        "v_cndmask_b32 v40, 0, v242, s[10:11]\n"
        "v_cndmask_b32 v41, 0, v242, s[12:13]\n"
        "v_mfma_f32_16x16x32_bf16 a[64:67], a[80:83], v[80:83], a[64:67]\n"
        "s_barrier\n"
        "v_mfma_f32_16x16x32_bf16 v[160:163], a[144:147], v[80:83], v[160:163]\n"
        "ds_read_b128 a[148:151], v241 offset:1024\n"
        "v_cndmask_b32 v42, 0, v242, s[14:15]\n"
        "v_mfma_f32_16x16x32_bf16 v[168:171], a[152:155], v[80:83], v[168:171]\n"
        "ds_read_b128 a[144:147], v241 offset:0\n"
        "v_cndmask_b32 v43, 0, v242, s[16:17]\n"
        "s_nop 1\n"
        "v_mfma_f32_16x16x32_bf16 v[40:43], a[96:99], v[96:99], v[40:43]\n"
        "ds_read_b128 a[96:99], v240 offset:8192\n"
        "v_cvt_pk_bf16_f32 v16, v32, v33\n"
        "v_mfma_f32_16x16x32_bf16 v[40:43], a[100:103], v[100:103], v[40:43]\n"
        "ds_read_b128 a[100:103], v240 offset:9216\n"
        "v_cvt_pk_bf16_f32 v17, v34, v35\n"
        "v_mfma_f32_16x16x32_bf16 v[40:43], a[104:107], v[104:107], v[40:43]\n"
        "v_exp_f32 v48, v48\n"
        "v_mfma_f32_16x16x32_bf16 v[40:43], a[108:111], v[108:111], v[40:43]\n"
        "v_exp_f32 v49, v49\n"
        "v_mfma_f32_16x16x32_bf16 v[172:175], a[156:159], v[80:83], v[172:175]\n"
        "v_exp_f32 v50, v50\n"
        "v_mfma_f32_16x16x32_bf16 v[176:179], a[128:131], v[80:83], v[176:179]\n"
        "ds_read_b128 a[104:107], v240 offset:10240\n"
        "v_cvt_pk_bf16_f32 v18, v48, v49\n"
        "v_mfma_f32_16x16x32_bf16 v[180:183], a[132:135], v[80:83], v[180:183]\n"
        "s_waitcnt lgkmcnt(4)\n"
        "ds_read_b128 a[108:111], v240 offset:11264\n"
        "v_mfma_f32_16x16x32_bf16 v[184:187], a[136:139], v[80:83], v[184:187]\n"
        "ds_read_b128 a[152:155], v241 offset:2048\n"
        "v_max_f32 v246, v40, v41\n"
        "v_mfma_f32_16x16x32_bf16 v[188:191], a[140:143], v[80:83], v[188:191]\n"
        "s_waitcnt lgkmcnt(4)\n"
        "v_max3_f32 v246, v42, v43, v246\n"
        "v_mov_b32 v247, v246\n"
        "v_mfma_f32_16x16x32_bf16 v[32:35], a[96:99], v[0:3], v[224:227]\n"
        "s_waitcnt lgkmcnt(3)\n"
        "v_permlane16_swap_b32 v246, v247\n"
        "v_mfma_f32_16x16x32_bf16 v[32:35], a[100:103], v[4:7], v[32:35]\n"
        "ds_read_b128 a[156:159], v241 offset:3072\n"
        "ds_read_b128 a[128:131], v241 offset:4096\n"
        "ds_read_b128 a[132:135], v241 offset:5120\n"
        "ds_read_b128 a[136:139], v241 offset:6144\n"
        "ds_read_b128 a[140:143], v241 offset:7168\n"
        "v_max_f32 v246, v246, v247\n"
        "s_waitcnt lgkmcnt(7)\n"
        "v_mfma_f32_16x16x32_bf16 v[32:35], a[104:107], v[8:11], v[32:35]\n"
        "v_mov_b32 v247, v246\n"
        "v_cndmask_b32 v56, 0, v242, s[20:21]\n"
        "s_waitcnt lgkmcnt(6)\n"
        "v_mfma_f32_16x16x32_bf16 v[32:35], a[108:111], v[12:15], v[32:35]\n"
        "v_permlane32_swap_b32 v246, v247\n"
        "v_max_f32 v246, v246, v247\n"
        "v_sub_f32 v232, 0, v246\n"
        "v_sub_f32 v233, 0, v246\n"
        "v_sub_f32 v234, 0, v246\n"
        "v_sub_f32 v235, 0, v246\n"
        "v_exp_f32 v51, v51\n"
        "v_exp_f32 v36, v36\n"
        "v_cvt_pk_bf16_f32 v19, v50, v51\n"
        "s_nop 1\n"
        "v_mfma_f32_16x16x32_bf16 v[164:167], a[148:151], v[16:19], v[164:167]\n"
        "v_exp_f32 v37, v37\n"
        "v_mfma_f32_16x16x32_bf16 a[64:67], a[80:83], v[16:19], a[64:67]\n"
        "v_exp_f32 v38, v38\n"
        "v_mfma_f32_16x16x32_bf16 v[160:163], a[144:147], v[16:19], v[160:163]\n"
        "s_waitcnt lgkmcnt(0)\n"
        "v_exp_f32 v39, v39\n"
        "v_mfma_f32_16x16x32_bf16 v[168:171], a[152:155], v[16:19], v[168:171]\n"
        "global_load_dwordx2 v[252:253], v245, s[48:49] sc1\n"
        "v_cvt_pk_bf16_f32 v20, v36, v37\n"
        "v_mfma_f32_16x16x32_bf16 v[172:175], a[156:159], v[16:19], v[172:175]\n"
        "v_cndmask_b32 v57, 0, v242, s[22:23]\n"
        "v_cndmask_b32 v58, 0, v242, s[24:25]\n"
        "v_cndmask_b32 v59, 0, v242, s[28:29]\n"
        "s_nop 1\n"
        "v_mfma_f32_16x16x32_bf16 v[56:59], a[112:115], v[96:99], v[56:59]\n"
        "ds_read_b128 a[112:115], v240 offset:12288\n"
        "v_cvt_pk_bf16_f32 v21, v38, v39\n"
        "v_mfma_f32_16x16x32_bf16 v[56:59], a[116:119], v[100:103], v[56:59]\n"
        "ds_read_b128 a[116:119], v240 offset:13312\n"
        "v_add_f32 v40, v40, v232\n"
        "v_mfma_f32_16x16x32_bf16 v[56:59], a[120:123], v[104:107], v[56:59]\n"
        "v_exp_f32 v40, v40\n"
        "v_mfma_f32_16x16x32_bf16 v[56:59], a[124:127], v[108:111], v[56:59]\n"
        "ds_read_b128 a[120:123], v240 offset:14336\n"
        "v_add_f32 v41, v41, v232\n"
        "v_mfma_f32_16x16x32_bf16 v[36:39], a[108:111], v[76:79], v[228:231]\n"
        "v_exp_f32 v41, v41\n"
        "v_mfma_f32_16x16x32_bf16 v[36:39], a[104:107], v[72:75], v[36:39]\n"
        "ds_read_b128 a[124:127], v240 offset:15360\n"
        "v_cvt_pk_bf16_f32 v24, v40, v41\n"
        "v_mfma_f32_16x16x32_bf16 v[36:39], a[100:103], v[68:71], v[36:39]\n"
        "v_exp_f32 v52, v52\n"
        "v_mfma_f32_16x16x32_bf16 v[36:39], a[96:99], v[64:67], v[36:39]\n"
        "v_exp_f32 v53, v53\n"
        "v_mfma_f32_16x16x32_bf16 v[176:179], a[128:131], v[16:19], v[176:179]\n"
        "s_waitcnt lgkmcnt(3)\n"
        "v_add_f32 v42, v42, v232\n"
        "v_add_f32 v43, v43, v232\n"
        "v_mfma_f32_16x16x32_bf16 v[48:51], a[112:115], v[0:3], v[224:227]\n"
        "s_waitcnt lgkmcnt(2)\n"
        "v_exp_f32 v42, v42\n"
        "v_mfma_f32_16x16x32_bf16 v[48:51], a[116:119], v[4:7], v[48:51]\n"
        "v_exp_f32 v43, v43\n"
        "v_mfma_f32_16x16x32_bf16 v[180:183], a[132:135], v[16:19], v[180:183]\n"
        "s_waitcnt lgkmcnt(1)\n"
        "v_cvt_pk_bf16_f32 v25, v42, v43\n"
        "v_cvt_pk_bf16_f32 v22, v52, v53\n"
        "v_mfma_f32_16x16x32_bf16 v[48:51], a[120:123], v[8:11], v[48:51]\n"
        "s_mov_b64 exec, 0\n"
        "s_cmp_eq_u32 s40, 0\n"
        "s_cbranch_scc1 LHC0_%=\n"
        "s_cmp_eq_u32 s40, 1\n"
        "s_cbranch_scc1 LHC1_%=\n"
        "s_cmp_eq_u32 s40, 2\n"
        "s_cbranch_scc1 LHC2_%=\n"
        "s_branch LHC3_%=\n"
        "LHRC_%=:\n"
        "s_mov_b64 exec, -1\n"
        "s_waitcnt lgkmcnt(0)\n"
        "v_mfma_f32_16x16x32_bf16 v[48:51], a[124:127], v[12:15], v[48:51]\n"
        "v_exp_f32 v54, v54\n"
        "v_mfma_f32_16x16x32_bf16 v[184:187], a[136:139], v[16:19], v[184:187]\n"
        "v_exp_f32 v55, v55\n"
        "v_mfma_f32_16x16x32_bf16 v[188:191], a[140:143], v[16:19], v[188:191]\n"
        "v_cndmask_b32 v40, v232, v242, s[30:31]\n"
        "v_cndmask_b32 v41, v232, v242, s[56:57]\n"
        "v_cndmask_b32 v42, v232, v242, s[58:59]\n"
        "v_cndmask_b32 v43, v232, v242, s[60:61]\n"
        "s_nop 1\n"
        "v_mfma_f32_16x16x32_bf16 v[40:43], a[96:99], v[96:99], v[40:43]\n"
        "ds_read_b128 a[96:99], v240 offset:16384\n"
        "v_cvt_pk_bf16_f32 v23, v54, v55\n"
        "v_mfma_f32_16x16x32_bf16 v[40:43], a[100:103], v[100:103], v[40:43]\n"
        "ds_read_b128 a[100:103], v240 offset:17408\n"
        "v_add_f32 v56, v56, v232\n"
        "v_mfma_f32_16x16x32_bf16 v[40:43], a[104:107], v[104:107], v[40:43]\n"
        "v_exp_f32 v56, v56\n"
        "v_mfma_f32_16x16x32_bf16 v[40:43], a[108:111], v[108:111], v[40:43]\n"
        "ds_read_b128 a[104:107], v240 offset:18432\n"
        "v_add_f32 v57, v57, v232\n"
        "v_mfma_f32_16x16x32_bf16 v[52:55], a[124:127], v[76:79], v[228:231]\n"
        "v_exp_f32 v57, v57\n"
        "v_mfma_f32_16x16x32_bf16 v[52:55], a[120:123], v[72:75], v[52:55]\n"
        "s_waitcnt vmcnt(0)\n"
        "v_cmp_ne_u64 vcc, v[252:253], v[250:251]\n"
        "v_cvt_pk_bf16_f32 v26, v56, v57\n"
        "v_mfma_f32_16x16x32_bf16 v[52:55], a[116:119], v[68:71], v[52:55]\n"
        "s_cbranch_vccnz LPKV_%=\n"
        "LBKV_%=:\n"
        "ds_read_b128 a[108:111], v240 offset:19456\n"
        "v_mfma_f32_16x16x32_bf16 v[52:55], a[112:115], v[64:67], v[52:55]\n"
        "v_exp_f32 v32, v32\n"
        "v_mfma_f32_16x16x32_bf16 a[68:71], a[80:83], v[20:23], a[68:71]\n"
        "v_exp_f32 v33, v33\n"
        "v_mfma_f32_16x16x32_bf16 v[192:195], a[144:147], v[20:23], v[192:195]\n"
        "v_exp_f32 v34, v34\n"
        "v_mfma_f32_16x16x32_bf16 v[196:199], a[148:151], v[20:23], v[196:199]\n"
        "v_exp_f32 v35, v35\n"
        "v_mfma_f32_16x16x32_bf16 v[200:203], a[152:155], v[20:23], v[200:203]\n"
        "global_load_lds_dwordx4 v254, s[68:69] offset:0 sc1\n"
        "v_add_f32 v58, v58, v232\n"
        "v_mfma_f32_16x16x32_bf16 v[204:207], a[156:159], v[20:23], v[204:207]\n"
        "v_exp_f32 v58, v58\n"
        "v_mfma_f32_16x16x32_bf16 v[208:211], a[128:131], v[20:23], v[208:211]\n"
        "v_add_f32 v59, v59, v232\n"
        "v_cvt_pk_bf16_f32 v80, v32, v33\n"
        "v_mfma_f32_16x16x32_bf16 v[212:215], a[132:135], v[20:23], v[212:215]\n"
        "v_exp_f32 v59, v59\n"
        "v_mfma_f32_16x16x32_bf16 v[216:219], a[136:139], v[20:23], v[216:219]\n"
        "global_load_lds_dwordx4 v254, s[68:69] offset:1024 sc1\n"
        "v_cvt_pk_bf16_f32 v27, v58, v59\n"
        "s_nop 1\n"
        "v_mfma_f32_16x16x32_bf16 a[72:75], a[80:83], v[24:27], 0\n"
        "v_exp_f32 v36, v36\n"
        "v_mfma_f32_16x16x32_bf16 a[0:3], a[144:147], v[24:27], 0\n"
        "ds_read_b128 a[144:147], v241 offset:8192\n"
        "v_cvt_pk_bf16_f32 v81, v34, v35\n"
        "v_mfma_f32_16x16x32_bf16 a[4:7], a[148:151], v[24:27], 0\n"
        "ds_read_b128 a[148:151], v241 offset:9216\n"
        "v_cndmask_b32 v56, v232, v242, s[62:63]\n"
        "v_mfma_f32_16x16x32_bf16 a[8:11], a[152:155], v[24:27], 0\n"
        "ds_read_b128 a[152:155], v241 offset:10240\n"
        "v_cndmask_b32 v57, v232, v242, s[84:85]\n"
        "v_mfma_f32_16x16x32_bf16 a[12:15], a[156:159], v[24:27], 0\n"
        "s_waitcnt lgkmcnt(3)\n"
        "v_cndmask_b32 v58, v232, v242, s[86:87]\n"
        "v_cndmask_b32 v59, v232, v242, s[88:89]\n"
        "s_nop 1\n"
        "v_mfma_f32_16x16x32_bf16 v[56:59], a[112:115], v[96:99], v[56:59]\n"
        "v_exp_f32 v37, v37\n"
        "v_mfma_f32_16x16x32_bf16 v[56:59], a[116:119], v[100:103], v[56:59]\n"
        "ds_read_b128 a[112:115], v240 offset:20480\n"
        "v_cvt_pk_bf16_f32 v84, v36, v37\n"
        "v_mfma_f32_16x16x32_bf16 v[56:59], a[120:123], v[104:107], v[56:59]\n"
        "v_exp_f32 v38, v38\n"
        "v_mfma_f32_16x16x32_bf16 v[56:59], a[124:127], v[108:111], v[56:59]\n"
        "v_exp_f32 v39, v39\n"
        "v_mfma_f32_16x16x32_bf16 v[32:35], a[96:99], v[0:3], v[224:227]\n"
        "ds_read_b128 a[116:119], v240 offset:21504\n"
        "v_cvt_pk_bf16_f32 v85, v38, v39\n"
        "v_mfma_f32_16x16x32_bf16 v[32:35], a[100:103], v[4:7], v[32:35]\n"
        "v_exp_f32 v40, v40\n"
        "v_mfma_f32_16x16x32_bf16 v[32:35], a[104:107], v[8:11], v[32:35]\n"
        "v_exp_f32 v41, v41\n"
        "v_mfma_f32_16x16x32_bf16 v[32:35], a[108:111], v[12:15], v[32:35]\n"
        "ds_read_b128 a[120:123], v240 offset:22528\n"
        "v_cvt_pk_bf16_f32 v88, v40, v41\n"
        "v_mfma_f32_16x16x32_bf16 v[36:39], a[108:111], v[76:79], v[228:231]\n"
        "ds_read_b128 a[156:159], v241 offset:11264\n"
        "v_and_b32 v245, 0x70, v254\n"
        "v_mfma_f32_16x16x32_bf16 v[36:39], a[104:107], v[72:75], v[36:39]\n"
        "global_load_lds_dwordx4 v254, s[68:69] offset:2048 sc1\n"
        "v_lshlrev_b32 v245, 3, v245\n"
        "v_mfma_f32_16x16x32_bf16 v[36:39], a[100:103], v[68:71], v[36:39]\n"
        "v_exp_f32 v42, v42\n"
        "v_mfma_f32_16x16x32_bf16 v[36:39], a[96:99], v[64:67], v[36:39]\n"
        "v_exp_f32 v43, v43\n"
        "v_mfma_f32_16x16x32_bf16 a[16:19], a[128:131], v[24:27], 0\n"
        "ds_read_b128 a[124:127], v240 offset:23552\n"
        "v_cvt_pk_bf16_f32 v89, v42, v43\n"
        "v_mfma_f32_16x16x32_bf16 v[40:43], a[96:99], v[96:99], v[232:235]\n"
        "v_exp_f32 v48, v48\n"
        "v_mfma_f32_16x16x32_bf16 v[40:43], a[100:103], v[100:103], v[40:43]\n"
        "v_exp_f32 v49, v49\n"
        "v_mfma_f32_16x16x32_bf16 v[40:43], a[104:107], v[104:107], v[40:43]\n"
        "global_load_lds_dwordx4 v254, s[68:69] offset:3072 sc1\n"
        "s_add_u32 m0, s81, 0x0\n"
        "v_cvt_pk_bf16_f32 v82, v48, v49\n"
        "v_mfma_f32_16x16x32_bf16 v[40:43], a[108:111], v[108:111], v[40:43]\n"
        "s_add_u32 s81, s78, 4096\n"
        "v_exp_f32 v50, v50\n"
        "v_mfma_f32_16x16x32_bf16 a[20:23], a[132:135], v[24:27], 0\n"
        "v_exp_f32 v51, v51\n"
        "v_mfma_f32_16x16x32_bf16 a[24:27], a[136:139], v[24:27], 0\n"
        "ds_read_b128 a[128:131], v241 offset:12288\n"
        "v_cvt_pk_bf16_f32 v83, v50, v51\n"
        "v_mfma_f32_16x16x32_bf16 a[28:31], a[140:143], v[24:27], 0\n"
        "global_load_lds_dwordx4 v254, s[74:75] offset:0 sc1\n"
        "v_cndmask_b32 v44, 0, v242, s[10:11]\n"
        "v_mfma_f32_16x16x32_bf16 v[220:223], a[140:143], v[20:23], v[220:223]\n"
        "s_waitcnt lgkmcnt(1)\n"
        "v_exp_f32 v52, v52\n"
        "v_mfma_f32_16x16x32_bf16 v[48:51], a[112:115], v[0:3], v[224:227]\n"
        "s_lshr_b32 s10, s41, 1\n"
        "v_exp_f32 v53, v53\n"
        "v_mfma_f32_16x16x32_bf16 v[48:51], a[116:119], v[4:7], v[48:51]\n"
        "ds_read_b128 a[132:135], v241 offset:13312\n"
        "s_and_b32 s11, s42, 7\n"
        "v_cvt_pk_bf16_f32 v86, v52, v53\n"
        "v_mfma_f32_16x16x32_bf16 v[48:51], a[120:123], v[8:11], v[48:51]\n"
        "global_load_lds_dwordx4 v254, s[74:75] offset:1024 sc1\n"
        "v_cndmask_b32 v45, 0, v242, s[12:13]\n"
        "v_mfma_f32_16x16x32_bf16 v[48:51], a[124:127], v[12:15], v[48:51]\n"
        "ds_read_b128 a[136:139], v241 offset:14336\n"
        "s_and_b32 s12, s41, 1\n"
        "v_cndmask_b32 v46, 0, v242, s[14:15]\n"
        "v_mfma_f32_16x16x32_bf16 a[64:67], a[80:83], v[80:83], a[64:67]\n"
        "ds_read_b128 a[140:143], v241 offset:15360\n"
        "s_lshr_b32 s14, s42, 3\n"
        "v_cndmask_b32 v47, 0, v242, s[16:17]\n"
        "s_nop 1\n"
        "v_mfma_f32_16x16x32_bf16 v[44:47], a[108:111], v[140:143], v[44:47]\n"
        "s_lshl2_add_u32 s12, s12, s14\n"
        "v_exp_f32 v54, v54\n"
        "v_mfma_f32_16x16x32_bf16 v[44:47], a[104:107], v[136:139], v[44:47]\n"
        "s_lshl3_add_u32 s13, s12, s11\n"
        "v_exp_f32 v55, v55\n"
        "v_mfma_f32_16x16x32_bf16 v[44:47], a[100:103], v[132:135], v[44:47]\n"
        "ds_read_b128 a[100:103], v240 offset:25600\n"
        "s_lshl_b32 s14, s10, 21\n"
        "v_cvt_pk_bf16_f32 v87, v54, v55\n"
        "v_mfma_f32_16x16x32_bf16 v[44:47], a[96:99], v[128:131], v[44:47]\n"
        "ds_read_b128 a[96:99], v240 offset:24576\n"
        "global_load_lds_dwordx4 v254, s[74:75] offset:2048 sc1\n"
        "s_lshl_b32 s15, s40, 15\n"
        "v_mfma_f32_16x16x32_bf16 v[52:55], a[124:127], v[76:79], v[228:231]\n"
        "s_add_u32 s14, s14, s15\n"
        "v_exp_f32 v56, v56\n"
        "v_mfma_f32_16x16x32_bf16 v[52:55], a[120:123], v[72:75], v[52:55]\n"
        "s_lshl_b32 s15, s11, 8\n"
        "v_exp_f32 v57, v57\n"
        "v_mfma_f32_16x16x32_bf16 v[52:55], a[116:119], v[68:71], v[52:55]\n"
        "ds_read_b128 a[104:107], v240 offset:26624\n"
        "s_add_u32 s14, s14, s15\n"
        "v_cvt_pk_bf16_f32 v90, v56, v57\n"
        "v_mfma_f32_16x16x32_bf16 v[52:55], a[112:115], v[64:67], v[52:55]\n"
        "ds_read_b128 a[108:111], v240 offset:27648\n"
        "global_load_lds_dwordx4 v254, s[74:75] offset:3072 sc1\n"
        "s_add_u32 m0, s79, 0x0\n"
        "v_mfma_f32_16x16x32_bf16 a[68:71], a[80:83], v[84:87], a[68:71]\n"
        "s_add_u32 s17, s11, s12\n"
        "v_exp_f32 v58, v58\n"
        "v_mfma_f32_16x16x32_bf16 v[160:163], a[144:147], v[80:83], v[160:163]\n"
        "s_and_b32 s17, s17, 1\n"
        "v_exp_f32 v59, v59\n"
        "v_mfma_f32_16x16x32_bf16 v[192:195], a[144:147], v[84:87], v[192:195]\n"
        "v_exp_f32 v32, v32\n"
        "v_mfma_f32_16x16x32_bf16 v[196:199], a[148:151], v[84:87], v[196:199]\n"
        "v_exp_f32 v33, v33\n"
        "v_mfma_f32_16x16x32_bf16 v[164:167], a[148:151], v[80:83], v[164:167]\n"
        "v_exp_f32 v34, v34\n"
        "v_mfma_f32_16x16x32_bf16 v[168:171], a[152:155], v[80:83], v[168:171]\n"
        "v_exp_f32 v35, v35\n"
        "v_mfma_f32_16x16x32_bf16 v[200:203], a[152:155], v[84:87], v[200:203]\n"
        "global_load_lds_dwordx4 v254, s[76:77] offset:0 sc1\n"
        "v_cvt_pk_bf16_f32 v91, v58, v59\n"
        "v_mfma_f32_16x16x32_bf16 v[56:59], a[112:115], v[96:99], v[232:235]\n"
        "v_exp_f32 v36, v36\n"
        "v_mfma_f32_16x16x32_bf16 v[56:59], a[116:119], v[100:103], v[56:59]\n"
        "v_exp_f32 v37, v37\n"
        "v_mfma_f32_16x16x32_bf16 v[56:59], a[120:123], v[104:107], v[56:59]\n"
        "s_add_u32 s68, s64, s90\n"
        "s_addc_u32 s69, s65, 0\n"
        "v_cvt_pk_bf16_f32 v16, v32, v33\n"
        "v_mfma_f32_16x16x32_bf16 v[56:59], a[124:127], v[108:111], v[56:59]\n"
        "v_exp_f32 v38, v38\n"
        "v_mfma_f32_16x16x32_bf16 a[72:75], a[80:83], v[88:91], a[72:75]\n"
        "v_exp_f32 v39, v39\n"
        "v_mfma_f32_16x16x32_bf16 a[0:3], a[144:147], v[88:91], a[0:3]\n"
        "global_load_lds_dwordx4 v254, s[76:77] offset:1024 sc1\n"
        "v_cvt_pk_bf16_f32 v17, v34, v35\n"
        "v_mfma_f32_16x16x32_bf16 a[4:7], a[148:151], v[88:91], a[4:7]\n"
        "ds_read_b128 a[144:147], v241 offset:16384\n"
        "v_cvt_pk_bf16_f32 v20, v36, v37\n"
        "v_mfma_f32_16x16x32_bf16 a[8:11], a[152:155], v[88:91], a[8:11]\n"
        "ds_read_b128 a[148:151], v241 offset:17408\n"
        "v_max_f32 v248, v44, v45\n"
        "v_mfma_f32_16x16x32_bf16 a[12:15], a[156:159], v[88:91], a[12:15]\n"
        "ds_read_b128 a[152:155], v241 offset:18432\n"
        "v_max3_f32 v248, v46, v47, v248\n"
        "v_mfma_f32_16x16x32_bf16 v[204:207], a[156:159], v[84:87], v[204:207]\n"
        "v_exp_f32 v40, v40\n"
        "v_mfma_f32_16x16x32_bf16 v[172:175], a[156:159], v[80:83], v[172:175]\n"
        "s_waitcnt lgkmcnt(3)\n"
        "global_load_lds_dwordx4 v254, s[76:77] offset:2048 sc1\n"
        "v_mfma_f32_16x16x32_bf16 v[32:35], a[96:99], v[0:3], v[224:227]\n"
        "ds_read_b128 a[156:159], v241 offset:19456\n"
        "v_mov_b32 v249, v248\n"
        "v_mfma_f32_16x16x32_bf16 v[32:35], a[100:103], v[4:7], v[32:35]\n"
        "v_exp_f32 v41, v41\n"
        "v_mfma_f32_16x16x32_bf16 v[32:35], a[104:107], v[8:11], v[32:35]\n"
        "v_permlane16_swap_b32 v248, v249\n"
        "v_mfma_f32_16x16x32_bf16 v[32:35], a[108:111], v[12:15], v[32:35]\n"
        "s_add_u32 s74, s68, 0x1000\n"
        "s_addc_u32 s75, s69, 0\n"
        "v_max_f32 v248, v248, v249\n"
        "v_mfma_f32_16x16x32_bf16 v[176:179], a[128:131], v[80:83], v[176:179]\n"
        "global_load_lds_dwordx4 v254, s[76:77] offset:3072 sc1\n"
        "s_add_u32 m0, s79, 0x1000\n"
        "v_mov_b32 v249, v248\n"
        "v_mfma_f32_16x16x32_bf16 v[208:211], a[128:131], v[84:87], v[208:211]\n"
        "s_add_u32 s76, s76, 0x1000\n"
        "s_addc_u32 s77, s77, 0\n"
        "v_cvt_pk_bf16_f32 v21, v38, v39\n"
        "v_mfma_f32_16x16x32_bf16 v[36:39], a[108:111], v[76:79], v[228:231]\n"
        "v_permlane32_swap_b32 v248, v249\n"
        "v_mfma_f32_16x16x32_bf16 v[36:39], a[104:107], v[72:75], v[36:39]\n"
        "v_max_f32 v248, v248, v249\n"
        "v_sub_f32 v236, 0, v248\n"
        "v_mfma_f32_16x16x32_bf16 v[36:39], a[100:103], v[68:71], v[36:39]\n"
        "global_load_lds_dwordx4 v254, s[76:77] offset:0 sc1\n"
        "v_sub_f32 v237, 0, v248\n"
        "v_mfma_f32_16x16x32_bf16 v[36:39], a[96:99], v[64:67], v[36:39]\n"
        "v_sub_f32 v238, 0, v248\n"
        "v_sub_f32 v239, 0, v248\n"
        "v_mfma_f32_16x16x32_bf16 a[16:19], a[128:131], v[88:91], a[16:19]\n"
        "ds_read_b128 a[128:131], v241 offset:20480\n"
        "v_cndmask_b32 v60, 0, v242, s[20:21]\n"
        "v_mfma_f32_16x16x32_bf16 a[20:23], a[132:135], v[88:91], a[20:23]\n"
        "v_cndmask_b32 v61, 0, v242, s[22:23]\n"
        "v_cndmask_b32 v62, 0, v242, s[24:25]\n"
        "v_mfma_f32_16x16x32_bf16 v[212:215], a[132:135], v[84:87], v[212:215]\n"
        "global_load_lds_dwordx4 v254, s[76:77] offset:1024 sc1\n"
        "s_sub_u32 s24, s83, 1\n"
        "v_cndmask_b32 v63, 0, v242, s[28:29]\n"
        "s_nop 1\n"
        "v_mfma_f32_16x16x32_bf16 v[60:63], a[124:127], v[140:143], v[60:63]\n"
        "ds_read_b128 a[124:127], v240 offset:31744\n"
        "s_mov_b32 s25, 4096\n"
        "v_cvt_pk_bf16_f32 v24, v40, v41\n"
        "v_mfma_f32_16x16x32_bf16 v[60:63], a[120:123], v[136:139], v[60:63]\n"
        "s_mov_b32 s28, 4096\n"
        "v_exp_f32 v42, v42\n"
        "v_mfma_f32_16x16x32_bf16 v[60:63], a[116:119], v[132:135], v[60:63]\n"
        "s_lshl_b32 s29, s17, 18\n"
        "v_exp_f32 v43, v43\n"
        "v_mfma_f32_16x16x32_bf16 v[60:63], a[112:115], v[128:131], v[60:63]\n"
        "ds_read_b128 a[112:115], v240 offset:28672\n"
        "v_cvt_pk_bf16_f32 v25, v42, v43\n"
        "v_mfma_f32_16x16x32_bf16 v[40:43], a[96:99], v[96:99], v[232:235]\n"
        "ds_read_b128 a[116:119], v240 offset:29696\n"
        "global_load_lds_dwordx4 v254, s[76:77] offset:2048 sc1\n"
        "v_mfma_f32_16x16x32_bf16 v[40:43], a[100:103], v[100:103], v[40:43]\n"
        "ds_read_b128 a[120:123], v240 offset:30720\n"
        "v_add_f32 v44, v44, v236\n"
        "v_mfma_f32_16x16x32_bf16 v[40:43], a[104:107], v[104:107], v[40:43]\n"
        "v_exp_f32 v44, v44\n"
        "v_mfma_f32_16x16x32_bf16 v[40:43], a[108:111], v[108:111], v[40:43]\n"
        "v_exp_f32 v48, v48\n"
        "v_mfma_f32_16x16x32_bf16 v[180:183], a[132:135], v[80:83], v[180:183]\n"
        "ds_read_b128 a[132:135], v241 offset:21504\n"
        "v_add_f32 v45, v45, v236\n"
        "v_mfma_f32_16x16x32_bf16 v[184:187], a[136:139], v[80:83], v[184:187]\n"
        "v_exp_f32 v45, v45\n"
        "v_mfma_f32_16x16x32_bf16 v[216:219], a[136:139], v[84:87], v[216:219]\n"
        "v_exp_f32 v49, v49\n"
        "v_mfma_f32_16x16x32_bf16 a[24:27], a[136:139], v[88:91], a[24:27]\n"
        "global_load_lds_dwordx4 v254, s[76:77] offset:3072 sc1\n"
        "s_add_u32 m0, s96, 0x20260\n"
        "v_cvt_pk_bf16_f32 v28, v44, v45\n"
        "v_mfma_f32_16x16x32_bf16 a[28:31], a[140:143], v[88:91], a[28:31]\n"
        "ds_read_b128 a[136:139], v241 offset:22528\n"
        "v_add_f32 v46, v46, v236\n"
        "v_mfma_f32_16x16x32_bf16 v[220:223], a[140:143], v[84:87], v[220:223]\n"
        "v_exp_f32 v46, v46\n"
        "v_mfma_f32_16x16x32_bf16 v[188:191], a[140:143], v[80:83], v[188:191]\n"
        "v_add_f32 v47, v47, v236\n"
        "v_exp_f32 v47, v47\n"
        "global_load_lds_dwordx4 v254, s[70:71] offset:0\n"
        "v_cvt_pk_bf16_f32 v29, v46, v47\n"
        "v_cvt_pk_bf16_f32 v18, v48, v49\n"
        "v_exp_f32 v50, v50\n"
        "v_exp_f32 v51, v51\n"
        "v_exp_f32 v52, v52\n"
        "v_cvt_pk_bf16_f32 v19, v50, v51\n"
        "s_waitcnt lgkmcnt(1)\n"
        "v_mfma_f32_16x16x32_bf16 v[48:51], a[112:115], v[0:3], v[224:227]\n"
        "v_exp_f32 v53, v53\n"
        "v_mfma_f32_16x16x32_bf16 v[48:51], a[116:119], v[4:7], v[48:51]\n"
        "global_load_lds_dwordx4 v254, s[70:71] offset:1024\n"
        "v_cvt_pk_bf16_f32 v22, v52, v53\n"
        "v_mfma_f32_16x16x32_bf16 v[48:51], a[120:123], v[8:11], v[48:51]\n"
        "v_exp_f32 v54, v54\n"
        "v_mfma_f32_16x16x32_bf16 v[48:51], a[124:127], v[12:15], v[48:51]\n"
        "v_exp_f32 v55, v55\n"
        "v_mfma_f32_16x16x32_bf16 a[64:67], a[80:83], v[16:19], a[64:67]\n"
        "ds_read_b128 a[140:143], v241 offset:23552\n"
        "v_cvt_pk_bf16_f32 v23, v54, v55\n"
        "v_mfma_f32_16x16x32_bf16 v[52:55], a[124:127], v[76:79], v[228:231]\n"
        "v_exp_f32 v56, v56\n"
        "v_mfma_f32_16x16x32_bf16 v[52:55], a[120:123], v[72:75], v[52:55]\n"
        "s_waitcnt vmcnt(10)\n"
        "v_exp_f32 v57, v57\n"
        "v_mfma_f32_16x16x32_bf16 v[52:55], a[116:119], v[68:71], v[52:55]\n"
        "s_barrier\n"
        "v_mfma_f32_16x16x32_bf16 v[52:55], a[112:115], v[64:67], v[52:55]\n"
        "v_exp_f32 v58, v58\n"
        "v_mfma_f32_16x16x32_bf16 a[68:71], a[80:83], v[20:23], a[68:71]\n"
        "v_exp_f32 v59, v59\n"
        "v_mfma_f32_16x16x32_bf16 v[160:163], a[144:147], v[16:19], v[160:163]\n"
        "global_load_lds_dwordx4 v254, s[70:71] offset:2048\n"
        "v_cndmask_b32 v44, v236, v242, s[30:31]\n"
        "v_mfma_f32_16x16x32_bf16 v[192:195], a[144:147], v[20:23], v[192:195]\n"
        "s_add_u32 s22, s18, s14\n"
        "s_addc_u32 s23, s19, 0\n"
        "v_cndmask_b32 v45, v236, v242, s[56:57]\n"
        "v_mfma_f32_16x16x32_bf16 v[196:199], a[148:151], v[20:23], v[196:199]\n"
        "s_sub_u32 s22, s22, 4096\n"
        "s_subb_u32 s23, s23, 0\n"
        "v_cndmask_b32 v46, v236, v242, s[58:59]\n"
        "v_mfma_f32_16x16x32_bf16 v[164:167], a[148:151], v[16:19], v[164:167]\n"
        "s_lshl_b32 s14, s10, 18\n"
        "v_cndmask_b32 v47, v236, v242, s[60:61]\n"
        "s_nop 1\n"
        "v_mfma_f32_16x16x32_bf16 v[44:47], a[108:111], v[140:143], v[44:47]\n"
        "s_add_u32 s14, s14, s72\n"
        "v_exp_f32 v32, v32\n"
        "v_mfma_f32_16x16x32_bf16 v[44:47], a[104:107], v[136:139], v[44:47]\n"
        "s_add_u32 s14, s14, 0x2200000\n"
        "v_exp_f32 v33, v33\n"
        "v_mfma_f32_16x16x32_bf16 v[44:47], a[100:103], v[132:135], v[44:47]\n"
        "s_mov_b32 s30, 131072\n"
        "v_exp_f32 v34, v34\n"
        "v_mfma_f32_16x16x32_bf16 v[44:47], a[96:99], v[128:131], v[44:47]\n"
        "global_load_dword v244, v245, s[26:27] offset:0\n"
        "v_cvt_pk_bf16_f32 v26, v56, v57\n"
        "v_mfma_f32_16x16x32_bf16 v[168:171], a[152:155], v[16:19], v[168:171]\n"
        "s_add_u32 s26, s26, 0x20000\n"
        "s_addc_u32 s27, s27, 0\n"
        "v_xor_b32 v240, 0x8000, v240\n"
        "v_mfma_f32_16x16x32_bf16 v[200:203], a[152:155], v[20:23], v[200:203]\n"
        "ds_read_b128 a[96:99], v240 offset:0\n"
        "v_lshrrev_b32 v252, 2, v254\n"
        "v_mfma_f32_16x16x32_bf16 v[204:207], a[156:159], v[20:23], v[204:207]\n"
        "ds_read_b128 a[100:103], v240 offset:1024\n"
        "v_cvt_pk_bf16_f32 v27, v58, v59\n"
        "v_mfma_f32_16x16x32_bf16 v[56:59], a[112:115], v[96:99], v[232:235]\n"
        "ds_read_b128 a[104:107], v240 offset:2048\n"
        "v_add_f32 v60, v60, v236\n"
        "v_mfma_f32_16x16x32_bf16 v[56:59], a[116:119], v[100:103], v[56:59]\n"
        "v_exp_f32 v60, v60\n"
        "v_mfma_f32_16x16x32_bf16 v[56:59], a[120:123], v[104:107], v[56:59]\n"
        "global_load_dword v244, v245, s[26:27] offset:0\n"
        "v_add_f32 v61, v61, v236\n"
        "v_mfma_f32_16x16x32_bf16 v[56:59], a[124:127], v[108:111], v[56:59]\n"
        "v_exp_f32 v61, v61\n"
        "v_mfma_f32_16x16x32_bf16 a[72:75], a[80:83], v[24:27], a[72:75]\n"
        "ds_read_b128 a[108:111], v240 offset:3072\n"
        "v_cvt_pk_bf16_f32 v30, v60, v61\n"
        "v_mfma_f32_16x16x32_bf16 a[0:3], a[144:147], v[24:27], a[0:3]\n"
        "v_exp_f32 v35, v35\n"
        "v_mfma_f32_16x16x32_bf16 a[4:7], a[148:151], v[24:27], a[4:7]\n"
        "s_add_u32 s26, s26, 0x20000\n"
        "s_addc_u32 s27, s27, 0\n"
        "v_add_f32 v62, v62, v236\n"
        "v_mfma_f32_16x16x32_bf16 a[8:11], a[152:155], v[24:27], a[8:11]\n"
        "s_waitcnt lgkmcnt(3)\n"
        "v_exp_f32 v62, v62\n"
        "v_mfma_f32_16x16x32_bf16 a[12:15], a[156:159], v[24:27], a[12:15]\n"
        "s_add_u32 s20, s38, s14\n"
        "s_addc_u32 s21, s39, 0\n"
        "v_add_f32 v63, v63, v236\n"
        "v_mfma_f32_16x16x32_bf16 v[172:175], a[156:159], v[16:19], v[172:175]\n"
        "s_mov_b32 exec_hi, 0x7fffffff\n"
        "global_load_lds_dwordx4 v254, s[70:71] offset:3072\n"
        "s_mov_b64 exec, -1\n"
        "v_exp_f32 v63, v63\n"
        "v_cvt_pk_bf16_f32 v80, v32, v33\n"
        "v_cvt_pk_bf16_f32 v31, v62, v63\n"
        "s_nop 1\n"
        "v_mfma_f32_16x16x32_bf16 a[76:79], a[80:83], v[28:31], 0\n"
        "v_exp_f32 v36, v36\n"
        "v_mfma_f32_16x16x32_bf16 a[32:35], a[144:147], v[28:31], 0\n"
        "ds_read_b128 a[144:147], v241 offset:24576\n"
        "v_cvt_pk_bf16_f32 v81, v34, v35\n"
        "v_mfma_f32_16x16x32_bf16 a[36:39], a[148:151], v[28:31], 0\n"
        "v_exp_f32 v37, v37\n"
        "v_mfma_f32_16x16x32_bf16 a[40:43], a[152:155], v[28:31], 0\n"
        "ds_read_b128 a[148:151], v241 offset:25600\n"
        "v_cvt_pk_bf16_f32 v84, v36, v37\n"
        "v_mfma_f32_16x16x32_bf16 a[44:47], a[156:159], v[28:31], 0\n"
        "s_mov_b64 exec, 7\n"
        "global_load_lds_dword v252, s[70:71] offset:4080\n"
        "s_mov_b64 exec, -1\n"
        "s_add_u32 m0, s80, 0x0\n"
        "v_cndmask_b32 v60, v236, v242, s[62:63]\n"
        "v_cndmask_b32 v61, v236, v242, s[84:85]\n"
        "v_cndmask_b32 v62, v236, v242, s[86:87]\n"
        "v_cndmask_b32 v63, v236, v242, s[88:89]\n"
        "s_nop 1\n"
        "v_mfma_f32_16x16x32_bf16 v[60:63], a[124:127], v[140:143], v[60:63]\n"
        "v_exp_f32 v38, v38\n"
        "v_mfma_f32_16x16x32_bf16 v[60:63], a[120:123], v[136:139], v[60:63]\n"
        "ds_read_b128 a[152:155], v241 offset:26624\n"
        "global_load_lds_dwordx4 v254, s[68:69] offset:0 sc1\n"
        "v_mfma_f32_16x16x32_bf16 v[60:63], a[116:119], v[132:135], v[60:63]\n"
        "v_exp_f32 v39, v39\n"
        "v_mfma_f32_16x16x32_bf16 v[60:63], a[112:115], v[128:131], v[60:63]\n"
        "v_cvt_pk_bf16_f32 v85, v38, v39\n"
        "ds_read_b128 a[112:115], v240 offset:4096\n"
        "v_exp_f32 v40, v40\n"
        "v_exp_f32 v41, v41\n"
        "global_load_lds_dwordx4 v254, s[68:69] offset:1024 sc1\n"
        "v_cvt_pk_bf16_f32 v88, v40, v41\n"
        "v_exp_f32 v42, v42\n"
        "v_exp_f32 v43, v43\n"
        "ds_read_b128 a[116:119], v240 offset:5120\n"
        "v_cvt_pk_bf16_f32 v89, v42, v43\n"
        "v_exp_f32 v44, v44\n"
        "v_exp_f32 v45, v45\n"
        "v_exp_f32 v46, v46\n"
        "global_load_lds_dwordx4 v254, s[68:69] offset:2048 sc1\n"
        "v_cvt_pk_bf16_f32 v92, v44, v45\n"
        "v_exp_f32 v47, v47\n"
        "ds_read_b128 a[120:123], v240 offset:6144\n"
        "v_cvt_pk_bf16_f32 v93, v46, v47\n"
        "v_mov_b32 v144, v254\n"
        "v_mov_b32 v145, v254\n"
        "v_mov_b32 v146, v254\n"
        "v_mov_b32 v147, v254\n"
        "global_load_lds_dwordx4 v254, s[68:69] offset:3072 sc1\n"
        "s_cmp_eq_u32 s91, 0\n"
        "s_cbranch_scc1 LLD_%=\n"
        "; ---- DUAL\n"
        "s_nop 0\n"
        "s_nop 0\n"
        "s_nop 0\n"
        "s_nop 0\n"
        "s_nop 0\n"
        "s_nop 0\n"
        "s_nop 0\n"
        "s_nop 0\n"
        "s_nop 0\n"
        "s_nop 0\n"
        "s_nop 0\n"
        "LDU_%=:\n"
        "v_mfma_f32_16x16x32_bf16 v[32:35], a[96:99], v[0:3], v[224:227]\n"
        "s_waitcnt lgkmcnt(1)\n"
        "ds_read_b128 a[156:159], v241 offset:27648\n"
        "s_add_u32 m0, s81, 0x0\n"
        "v_mfma_f32_16x16x32_bf16 v[32:35], a[100:103], v[4:7], v[32:35]\n"
        "ds_read_b128 a[124:127], v240 offset:7168\n"
        "global_load_lds_dwordx4 v254, s[74:75] offset:0 sc1\n"
        "s_xor_b32 s79, s79, 0x8000\n"
        "v_mfma_f32_16x16x32_bf16 v[32:35], a[104:107], v[8:11], v[32:35]\n"
        "v_exp_f32 v48, v48\n"
        "v_mfma_f32_16x16x32_bf16 v[32:35], a[108:111], v[12:15], v[32:35]\n"
        "v_exp_f32 v49, v49\n"
        "v_mfma_f32_16x16x32_bf16 v[36:39], a[108:111], v[76:79], v[228:231]\n"
        "v_exp_f32 v50, v50\n"
        "v_mfma_f32_16x16x32_bf16 v[36:39], a[104:107], v[72:75], v[36:39]\n"
        "v_exp_f32 v51, v51\n"
        "v_mfma_f32_16x16x32_bf16 v[36:39], a[100:103], v[68:71], v[36:39]\n"
        "v_exp_f32 v52, v52\n"
        "v_mfma_f32_16x16x32_bf16 v[36:39], a[96:99], v[64:67], v[36:39]\n"
        "v_exp_f32 v53, v53\n"
        "v_mfma_f32_16x16x32_bf16 v[40:43], a[96:99], v[96:99], v[232:235]\n"
        "v_exp_f32 v54, v54\n"
        "v_mfma_f32_16x16x32_bf16 v[40:43], a[100:103], v[100:103], v[40:43]\n"
        "v_exp_f32 v55, v55\n"
        "v_mfma_f32_16x16x32_bf16 v[40:43], a[104:107], v[104:107], v[40:43]\n"
        "v_exp_f32 v56, v56\n"
        "v_mfma_f32_16x16x32_bf16 v[40:43], a[108:111], v[108:111], v[40:43]\n"
        "v_exp_f32 v57, v57\n"
        "v_mfma_f32_16x16x32_bf16 v[44:47], a[108:111], v[140:143], v[236:239]\n"
        "ds_read_b128 a[108:111], v240 offset:11264\n"
        "v_cvt_pk_bf16_f32 v82, v48, v49\n"
        "v_mfma_f32_16x16x32_bf16 v[44:47], a[104:107], v[136:139], v[44:47]\n"
        "ds_read_b128 a[104:107], v240 offset:10240\n"
        "global_load_lds_dwordx4 v254, s[74:75] offset:1024 sc1\n"
        "v_mfma_f32_16x16x32_bf16 v[44:47], a[100:103], v[132:135], v[44:47]\n"
        "ds_read_b128 a[100:103], v240 offset:9216\n"
        "v_cvt_pk_bf16_f32 v83, v50, v51\n"
        "v_mfma_f32_16x16x32_bf16 v[44:47], a[96:99], v[128:131], v[44:47]\n"
        "ds_read_b128 a[96:99], v240 offset:8192\n"
        "v_cvt_pk_bf16_f32 v86, v52, v53\n"
        "v_mfma_f32_16x16x32_bf16 v[176:179], a[128:131], v[16:19], v[176:179]\n"
        "LHA11_%=:\n"
        "s_cbranch_execz LHRA_%=\n"
        "v_exp_f32 v58, v58\n"
        "v_mfma_f32_16x16x32_bf16 v[208:211], a[128:131], v[20:23], v[208:211]\n"
        "v_exp_f32 v59, v59\n"
        "v_mfma_f32_16x16x32_bf16 a[16:19], a[128:131], v[24:27], a[16:19]\n"
        "v_exp_f32 v60, v60\n"
        "v_mfma_f32_16x16x32_bf16 a[48:51], a[128:131], v[28:31], a[48:51]\n"
        "ds_read_b128 a[128:131], v241 offset:28672\n"
        "global_load_lds_dwordx4 v254, s[74:75] offset:2048 sc1\n"
        "v_mfma_f32_16x16x32_bf16 a[52:55], a[132:135], v[28:31], a[52:55]\n"
        "v_exp_f32 v61, v61\n"
        "v_mfma_f32_16x16x32_bf16 a[20:23], a[132:135], v[24:27], a[20:23]\n"
        "v_exp_f32 v62, v62\n"
        "v_mfma_f32_16x16x32_bf16 v[212:215], a[132:135], v[20:23], v[212:215]\n"
        "v_exp_f32 v63, v63\n"
        "v_mfma_f32_16x16x32_bf16 v[180:183], a[132:135], v[16:19], v[180:183]\n"
        "ds_read_b128 a[132:135], v241 offset:29696\n"
        "global_load_lds_dwordx4 v254, s[74:75] offset:3072 sc1\n"
        "s_add_u32 m0, s79, 0x0\n"
        "v_mfma_f32_16x16x32_bf16 v[184:187], a[136:139], v[16:19], v[184:187]\n"
        "v_exp_f32 v32, v32\n"
        "v_mfma_f32_16x16x32_bf16 v[216:219], a[136:139], v[20:23], v[216:219]\n"
        "v_exp_f32 v33, v33\n"
        "v_mfma_f32_16x16x32_bf16 a[24:27], a[136:139], v[24:27], a[24:27]\n"
        "v_exp_f32 v34, v34\n"
        "v_mfma_f32_16x16x32_bf16 a[56:59], a[136:139], v[28:31], a[56:59]\n"
        "ds_read_b128 a[136:139], v241 offset:30720\n"
        "v_cvt_pk_bf16_f32 v87, v54, v55\n"
        "v_mfma_f32_16x16x32_bf16 a[60:63], a[140:143], v[28:31], a[60:63]\n"
        "v_exp_f32 v35, v35\n"
        "v_mfma_f32_16x16x32_bf16 a[28:31], a[140:143], v[24:27], a[28:31]\n"
        "v_exp_f32 v36, v36\n"
        "v_mfma_f32_16x16x32_bf16 v[220:223], a[140:143], v[20:23], v[220:223]\n"
        "s_waitcnt lgkmcnt(2)\n"
        "v_exp_f32 v37, v37\n"
        "v_mfma_f32_16x16x32_bf16 v[188:191], a[140:143], v[16:19], v[188:191]\n"
        "ds_read_b128 a[140:143], v241 offset:31744\n"
        "v_cvt_pk_bf16_f32 v90, v56, v57\n"
        "v_mfma_f32_16x16x32_bf16 v[48:51], a[112:115], v[0:3], v[224:227]\n"
        "s_add_u32 s82, s82, 1\n"
        "v_exp_f32 v38, v38\n"
        "v_mfma_f32_16x16x32_bf16 v[48:51], a[116:119], v[4:7], v[48:51]\n"
        "s_xor_b32 s78, s78, 0x8000\n"
        "v_exp_f32 v39, v39\n"
        "v_mfma_f32_16x16x32_bf16 v[48:51], a[120:123], v[8:11], v[48:51]\n"
        "s_add_u32 s81, s78, 4096\n"
        "v_exp_f32 v40, v40\n"
        "v_mfma_f32_16x16x32_bf16 v[48:51], a[124:127], v[12:15], v[48:51]\n"
        "s_mov_b32 s80, s78\n"
        "v_exp_f32 v41, v41\n"
        "v_mfma_f32_16x16x32_bf16 v[52:55], a[124:127], v[76:79], v[228:231]\n"
        "v_exp_f32 v42, v42\n"
        "v_mfma_f32_16x16x32_bf16 v[52:55], a[120:123], v[72:75], v[52:55]\n"
        "v_exp_f32 v43, v43\n"
        "v_mfma_f32_16x16x32_bf16 v[52:55], a[116:119], v[68:71], v[52:55]\n"
        "v_exp_f32 v44, v44\n"
        "v_mfma_f32_16x16x32_bf16 v[52:55], a[112:115], v[64:67], v[52:55]\n"
        "s_waitcnt vmcnt(10) lgkmcnt(0)\n"
        "v_exp_f32 v45, v45\n"
        "v_mfma_f32_16x16x32_bf16 a[64:67], a[80:83], v[80:83], a[64:67]\n"
        "s_barrier\n"
        "v_mfma_f32_16x16x32_bf16 a[68:71], a[80:83], v[84:87], a[68:71]\n"
        "v_exp_f32 v46, v46\n"
        "v_mfma_f32_16x16x32_bf16 v[160:163], a[144:147], v[80:83], v[160:163]\n"
        "v_exp_f32 v47, v47\n"
        "v_mfma_f32_16x16x32_bf16 v[192:195], a[144:147], v[84:87], v[192:195]\n"
        "v_exp_f32 v48, v48\n"
        "v_mfma_f32_16x16x32_bf16 v[196:199], a[148:151], v[84:87], v[196:199]\n"
        "v_exp_f32 v49, v49\n"
        "v_mfma_f32_16x16x32_bf16 v[164:167], a[148:151], v[80:83], v[164:167]\n"
        "v_exp_f32 v50, v50\n"
        "v_mfma_f32_16x16x32_bf16 v[168:171], a[152:155], v[80:83], v[168:171]\n"
        "v_exp_f32 v51, v51\n"
        "v_mfma_f32_16x16x32_bf16 v[200:203], a[152:155], v[84:87], v[200:203]\n"
        "v_exp_f32 v52, v52\n"
        "v_mfma_f32_16x16x32_bf16 v[204:207], a[156:159], v[84:87], v[204:207]\n"
        "v_exp_f32 v53, v53\n"
        "v_mfma_f32_16x16x32_bf16 v[172:175], a[156:159], v[80:83], v[172:175]\n"
        "v_exp_f32 v54, v54\n"
        "v_mfma_f32_16x16x32_bf16 v[176:179], a[128:131], v[80:83], v[176:179]\n"
        "v_exp_f32 v55, v55\n"
        "v_cvt_pk_bf16_f32 v91, v58, v59\n"
        "v_mfma_f32_16x16x32_bf16 v[56:59], a[112:115], v[96:99], v[232:235]\n"
        "s_add_u32 s76, s66, s90\n"
        "s_addc_u32 s77, s67, 0\n"
        "v_cvt_pk_bf16_f32 v94, v60, v61\n"
        "v_mfma_f32_16x16x32_bf16 v[56:59], a[116:119], v[100:103], v[56:59]\n"
        "global_load_lds_dwordx4 v144, s[76:77] offset:0 sc1\n"
        "v_cvt_pk_bf16_f32 v95, v62, v63\n"
        "v_mfma_f32_16x16x32_bf16 v[56:59], a[120:123], v[104:107], v[56:59]\n"
        "s_cmp_ge_u32 s82, s44\n"
        "s_addc_u32 s90, s82, 0\n"
        "v_cvt_pk_bf16_f32 v16, v32, v33\n"
        "v_mfma_f32_16x16x32_bf16 v[56:59], a[124:127], v[108:111], v[56:59]\n"
        "s_lshl_b32 s90, s90, 15\n"
        "v_cvt_pk_bf16_f32 v17, v34, v35\n"
        "v_cvt_pk_bf16_f32 v20, v36, v37\n"
        "v_mfma_f32_16x16x32_bf16 v[60:63], a[124:127], v[140:143], v[236:239]\n"
        "ds_read_b128 a[124:127], v240 offset:15360\n"
        "v_cvt_pk_bf16_f32 v21, v38, v39\n"
        "v_mfma_f32_16x16x32_bf16 v[60:63], a[120:123], v[136:139], v[60:63]\n"
        "ds_read_b128 a[120:123], v240 offset:14336\n"
        "v_cvt_pk_bf16_f32 v24, v40, v41\n"
        "v_mfma_f32_16x16x32_bf16 v[60:63], a[116:119], v[132:135], v[60:63]\n"
        "ds_read_b128 a[116:119], v240 offset:13312\n"
        "v_xor_b32 v241, 0x8000, v241\n"
        "v_mfma_f32_16x16x32_bf16 v[60:63], a[112:115], v[128:131], v[60:63]\n"
        "ds_read_b128 a[112:115], v240 offset:12288\n"
        "v_cvt_pk_bf16_f32 v25, v42, v43\n"
        "v_mfma_f32_16x16x32_bf16 a[72:75], a[80:83], v[88:91], a[72:75]\n"
        "v_exp_f32 v56, v56\n"
        "v_mfma_f32_16x16x32_bf16 a[76:79], a[80:83], v[92:95], a[76:79]\n"
        "v_exp_f32 v57, v57\n"
        "v_mfma_f32_16x16x32_bf16 a[0:3], a[144:147], v[88:91], a[0:3]\n"
        "v_exp_f32 v58, v58\n"
        "v_mfma_f32_16x16x32_bf16 a[32:35], a[144:147], v[92:95], a[32:35]\n"
        "ds_read_b128 a[144:147], v241 offset:0\n"
        "global_load_lds_dwordx4 v145, s[76:77] offset:1024 sc1\n"
        "v_mfma_f32_16x16x32_bf16 a[36:39], a[148:151], v[92:95], a[36:39]\n"
        "v_exp_f32 v59, v59\n"
        "v_mfma_f32_16x16x32_bf16 a[4:7], a[148:151], v[88:91], a[4:7]\n"
        "ds_read_b128 a[148:151], v241 offset:1024\n"
        "v_cvt_pk_bf16_f32 v28, v44, v45\n"
        "v_mfma_f32_16x16x32_bf16 a[8:11], a[152:155], v[88:91], a[8:11]\n"
        "v_exp_f32 v60, v60\n"
        "v_mfma_f32_16x16x32_bf16 a[40:43], a[152:155], v[92:95], a[40:43]\n"
        "ds_read_b128 a[152:155], v241 offset:2048\n"
        "global_load_lds_dwordx4 v146, s[76:77] offset:2048 sc1\n"
        "v_mfma_f32_16x16x32_bf16 a[44:47], a[156:159], v[92:95], a[44:47]\n"
        "v_exp_f32 v61, v61\n"
        "v_mfma_f32_16x16x32_bf16 a[12:15], a[156:159], v[88:91], a[12:15]\n"
        "ds_read_b128 a[156:159], v241 offset:3072\n"
        "v_cvt_pk_bf16_f32 v29, v46, v47\n"
        "v_mfma_f32_16x16x32_bf16 v[32:35], a[96:99], v[0:3], v[224:227]\n"
        "v_exp_f32 v62, v62\n"
        "v_mfma_f32_16x16x32_bf16 v[32:35], a[100:103], v[4:7], v[32:35]\n"
        "v_exp_f32 v63, v63\n"
        "v_mfma_f32_16x16x32_bf16 v[32:35], a[104:107], v[8:11], v[32:35]\n"
        "global_load_lds_dwordx4 v147, s[76:77] offset:3072 sc1\n"
        "s_add_u32 m0, s79, s28\n"
        "v_cvt_pk_bf16_f32 v18, v48, v49\n"
        "v_mfma_f32_16x16x32_bf16 v[32:35], a[108:111], v[12:15], v[32:35]\n"
        "s_add_u32 s76, s76, s25\n"
        "s_addc_u32 s77, s77, 0\n"
        "v_cvt_pk_bf16_f32 v19, v50, v51\n"
        "v_mfma_f32_16x16x32_bf16 v[36:39], a[108:111], v[76:79], v[228:231]\n"
        "s_add_u32 s68, s64, s90\n"
        "s_addc_u32 s69, s65, 0\n"
        "v_cvt_pk_bf16_f32 v22, v52, v53\n"
        "v_mfma_f32_16x16x32_bf16 v[36:39], a[104:107], v[72:75], v[36:39]\n"
        "s_add_u32 s74, s68, 0x1000\n"
        "s_addc_u32 s75, s69, 0\n"
        "v_cvt_pk_bf16_f32 v23, v54, v55\n"
        "v_mfma_f32_16x16x32_bf16 v[36:39], a[100:103], v[68:71], v[36:39]\n"
        "s_sub_u32 s91, s91, 1\n"
        "v_exp_f32 v32, v32\n"
        "v_mfma_f32_16x16x32_bf16 v[36:39], a[96:99], v[64:67], v[36:39]\n"
        "v_exp_f32 v33, v33\n"
        "v_mfma_f32_16x16x32_bf16 v[40:43], a[96:99], v[96:99], v[232:235]\n"
        "v_exp_f32 v34, v34\n"
        "v_mfma_f32_16x16x32_bf16 v[40:43], a[100:103], v[100:103], v[40:43]\n"
        "v_exp_f32 v35, v35\n"
        "v_mfma_f32_16x16x32_bf16 v[40:43], a[104:107], v[104:107], v[40:43]\n"
        "global_load_lds_dwordx4 v144, s[76:77] offset:0 sc1\n"
        "v_cvt_pk_bf16_f32 v26, v56, v57\n"
        "v_mfma_f32_16x16x32_bf16 v[40:43], a[108:111], v[108:111], v[40:43]\n"
        "v_exp_f32 v36, v36\n"
        "v_mfma_f32_16x16x32_bf16 v[44:47], a[108:111], v[140:143], v[236:239]\n"
        "ds_read_b128 a[108:111], v240 offset:19456\n"
        "v_cvt_pk_bf16_f32 v27, v58, v59\n"
        "v_mfma_f32_16x16x32_bf16 v[44:47], a[104:107], v[136:139], v[44:47]\n"
        "ds_read_b128 a[104:107], v240 offset:18432\n"
        "v_cvt_pk_bf16_f32 v30, v60, v61\n"
        "v_mfma_f32_16x16x32_bf16 v[44:47], a[100:103], v[132:135], v[44:47]\n"
        "ds_read_b128 a[100:103], v240 offset:17408\n"
        "global_load_lds_dwordx4 v145, s[76:77] offset:1024 sc1\n"
        "v_mfma_f32_16x16x32_bf16 v[44:47], a[96:99], v[128:131], v[44:47]\n"
        "ds_read_b128 a[96:99], v240 offset:16384\n"
        "v_cvt_pk_bf16_f32 v31, v62, v63\n"
        "v_mfma_f32_16x16x32_bf16 v[208:211], a[128:131], v[84:87], v[208:211]\n"
        "v_exp_f32 v37, v37\n"
        "v_mfma_f32_16x16x32_bf16 a[16:19], a[128:131], v[88:91], a[16:19]\n"
        "v_exp_f32 v38, v38\n"
        "v_mfma_f32_16x16x32_bf16 a[48:51], a[128:131], v[92:95], a[48:51]\n"
        "ds_read_b128 a[128:131], v241 offset:4096\n"
        "global_load_lds_dwordx4 v146, s[76:77] offset:2048 sc1\n"
        "v_mfma_f32_16x16x32_bf16 a[52:55], a[132:135], v[92:95], a[52:55]\n"
        "v_exp_f32 v39, v39\n"
        "v_mfma_f32_16x16x32_bf16 a[20:23], a[132:135], v[88:91], a[20:23]\n"
        "v_exp_f32 v40, v40\n"
        "v_mfma_f32_16x16x32_bf16 v[212:215], a[132:135], v[84:87], v[212:215]\n"
        "v_exp_f32 v41, v41\n"
        "v_mfma_f32_16x16x32_bf16 v[180:183], a[132:135], v[80:83], v[180:183]\n"
        "ds_read_b128 a[132:135], v241 offset:5120\n"
        "global_load_lds_dwordx4 v147, s[76:77] offset:3072 sc1\n"
        "v_mfma_f32_16x16x32_bf16 v[184:187], a[136:139], v[80:83], v[184:187]\n"
        "v_exp_f32 v42, v42\n"
        "v_mfma_f32_16x16x32_bf16 v[216:219], a[136:139], v[84:87], v[216:219]\n"
        "v_exp_f32 v43, v43\n"
        "v_mfma_f32_16x16x32_bf16 a[24:27], a[136:139], v[88:91], a[24:27]\n"
        "v_exp_f32 v44, v44\n"
        "v_mfma_f32_16x16x32_bf16 a[56:59], a[136:139], v[92:95], a[56:59]\n"
        "ds_read_b128 a[136:139], v241 offset:6144\n"
        "v_mfma_f32_16x16x32_bf16 a[60:63], a[140:143], v[92:95], a[60:63]\n"
        "s_waitcnt lgkmcnt(2)\n"
        "v_exp_f32 v45, v45\n"
        "v_mfma_f32_16x16x32_bf16 a[28:31], a[140:143], v[88:91], a[28:31]\n"
        "v_exp_f32 v46, v46\n"
        "v_mfma_f32_16x16x32_bf16 v[220:223], a[140:143], v[84:87], v[220:223]\n"
        "v_exp_f32 v47, v47\n"
        "v_mfma_f32_16x16x32_bf16 v[188:191], a[140:143], v[80:83], v[188:191]\n"
        "ds_read_b128 a[140:143], v241 offset:7168\n"
        "global_load_dword v244, v245, s[26:27] offset:0\n"
        "v_mfma_f32_16x16x32_bf16 v[48:51], a[112:115], v[0:3], v[224:227]\n"
        "s_add_u32 s26, s26, 0x20000\n"
        "s_addc_u32 s27, s27, 0\n"
        "v_cvt_pk_bf16_f32 v80, v32, v33\n"
        "v_mfma_f32_16x16x32_bf16 v[48:51], a[116:119], v[4:7], v[48:51]\n"
        "v_cvt_pk_bf16_f32 v81, v34, v35\n"
        "v_cvt_pk_bf16_f32 v84, v36, v37\n"
        "v_mfma_f32_16x16x32_bf16 v[48:51], a[120:123], v[8:11], v[48:51]\n"
        "v_cvt_pk_bf16_f32 v85, v38, v39\n"
        "v_cvt_pk_bf16_f32 v88, v40, v41\n"
        "v_mfma_f32_16x16x32_bf16 v[48:51], a[124:127], v[12:15], v[48:51]\n"
        "v_cvt_pk_bf16_f32 v89, v42, v43\n"
        "v_cvt_pk_bf16_f32 v92, v44, v45\n"
        "v_mfma_f32_16x16x32_bf16 v[52:55], a[124:127], v[76:79], v[228:231]\n"
        "v_cvt_pk_bf16_f32 v93, v46, v47\n"
        "v_mfma_f32_16x16x32_bf16 v[52:55], a[120:123], v[72:75], v[52:55]\n"
        "v_mfma_f32_16x16x32_bf16 v[52:55], a[116:119], v[68:71], v[52:55]\n"
        "v_mfma_f32_16x16x32_bf16 v[52:55], a[112:115], v[64:67], v[52:55]\n"
        "v_mfma_f32_16x16x32_bf16 v[56:59], a[112:115], v[96:99], v[232:235]\n"
        "v_exp_f32 v48, v48\n"
        "v_mfma_f32_16x16x32_bf16 v[56:59], a[116:119], v[100:103], v[56:59]\n"
        "v_exp_f32 v49, v49\n"
        "v_mfma_f32_16x16x32_bf16 v[56:59], a[120:123], v[104:107], v[56:59]\n"
        "v_exp_f32 v50, v50\n"
        "v_mfma_f32_16x16x32_bf16 v[56:59], a[124:127], v[108:111], v[56:59]\n"
        "v_exp_f32 v51, v51\n"
        "v_mfma_f32_16x16x32_bf16 v[60:63], a[124:127], v[140:143], v[236:239]\n"
        "ds_read_b128 a[124:127], v240 offset:23552\n"
        "v_cvt_pk_bf16_f32 v82, v48, v49\n"
        "v_mfma_f32_16x16x32_bf16 v[60:63], a[120:123], v[136:139], v[60:63]\n"
        "ds_read_b128 a[120:123], v240 offset:22528\n"
        "v_cvt_pk_bf16_f32 v83, v50, v51\n"
        "v_mfma_f32_16x16x32_bf16 v[60:63], a[116:119], v[132:135], v[60:63]\n"
        "ds_read_b128 a[116:119], v240 offset:21504\n"
        "v_mfma_f32_16x16x32_bf16 v[60:63], a[112:115], v[128:131], v[60:63]\n"
        "ds_read_b128 a[112:115], v240 offset:20480\n"
        "v_mfma_f32_16x16x32_bf16 a[64:67], a[80:83], v[16:19], a[64:67]\n"
        "v_exp_f32 v52, v52\n"
        "v_mfma_f32_16x16x32_bf16 a[68:71], a[80:83], v[20:23], a[68:71]\n"
        "v_exp_f32 v53, v53\n"
        "v_mfma_f32_16x16x32_bf16 a[72:75], a[80:83], v[24:27], a[72:75]\n"
        "v_exp_f32 v54, v54\n"
        "v_mfma_f32_16x16x32_bf16 a[76:79], a[80:83], v[28:31], a[76:79]\n"
        "v_exp_f32 v55, v55\n"
        "v_mfma_f32_16x16x32_bf16 v[160:163], a[144:147], v[16:19], v[160:163]\n"
        "v_exp_f32 v56, v56\n"
        "v_mfma_f32_16x16x32_bf16 v[192:195], a[144:147], v[20:23], v[192:195]\n"
        "v_exp_f32 v57, v57\n"
        "v_mfma_f32_16x16x32_bf16 a[0:3], a[144:147], v[24:27], a[0:3]\n"
        "v_exp_f32 v58, v58\n"
        "v_mfma_f32_16x16x32_bf16 a[32:35], a[144:147], v[28:31], a[32:35]\n"
        "ds_read_b128 a[144:147], v241 offset:8192\n"
        "v_cvt_pk_bf16_f32 v86, v52, v53\n"
        "v_mfma_f32_16x16x32_bf16 a[36:39], a[148:151], v[28:31], a[36:39]\n"
        "v_exp_f32 v59, v59\n"
        "v_mfma_f32_16x16x32_bf16 a[4:7], a[148:151], v[24:27], a[4:7]\n"
        "v_exp_f32 v60, v60\n"
        "v_mfma_f32_16x16x32_bf16 v[196:199], a[148:151], v[20:23], v[196:199]\n"
        "v_exp_f32 v61, v61\n"
        "v_mfma_f32_16x16x32_bf16 v[164:167], a[148:151], v[16:19], v[164:167]\n"
        "ds_read_b128 a[148:151], v241 offset:9216\n"
        "v_cvt_pk_bf16_f32 v87, v54, v55\n"
        "v_mfma_f32_16x16x32_bf16 v[168:171], a[152:155], v[16:19], v[168:171]\n"
        "v_exp_f32 v62, v62\n"
        "v_mfma_f32_16x16x32_bf16 v[200:203], a[152:155], v[20:23], v[200:203]\n"
        "v_exp_f32 v63, v63\n"
        "v_mfma_f32_16x16x32_bf16 a[8:11], a[152:155], v[24:27], a[8:11]\n"
        "v_cvt_pk_bf16_f32 v90, v56, v57\n"
        "v_cvt_pk_bf16_f32 v91, v58, v59\n"
        "v_mfma_f32_16x16x32_bf16 a[40:43], a[152:155], v[28:31], a[40:43]\n"
        "ds_read_b128 a[152:155], v241 offset:10240\n"
        "v_cvt_pk_bf16_f32 v94, v60, v61\n"
        "v_mfma_f32_16x16x32_bf16 a[44:47], a[156:159], v[28:31], a[44:47]\n"
        "v_cvt_pk_bf16_f32 v95, v62, v63\n"
        "v_mfma_f32_16x16x32_bf16 a[12:15], a[156:159], v[24:27], a[12:15]\n"
        "v_mfma_f32_16x16x32_bf16 v[204:207], a[156:159], v[20:23], v[204:207]\n"
        "v_mfma_f32_16x16x32_bf16 v[172:175], a[156:159], v[16:19], v[172:175]\n"
        "ds_read_b128 a[156:159], v241 offset:11264\n"
        "v_mfma_f32_16x16x32_bf16 v[32:35], a[96:99], v[0:3], v[224:227]\n"
        "v_mfma_f32_16x16x32_bf16 v[32:35], a[100:103], v[4:7], v[32:35]\n"
        "v_mfma_f32_16x16x32_bf16 v[32:35], a[104:107], v[8:11], v[32:35]\n"
        "v_mfma_f32_16x16x32_bf16 v[32:35], a[108:111], v[12:15], v[32:35]\n"
        "v_mfma_f32_16x16x32_bf16 v[36:39], a[108:111], v[76:79], v[228:231]\n"
        "v_mfma_f32_16x16x32_bf16 v[36:39], a[104:107], v[72:75], v[36:39]\n"
        "v_mfma_f32_16x16x32_bf16 v[36:39], a[100:103], v[68:71], v[36:39]\n"
        "v_mfma_f32_16x16x32_bf16 v[36:39], a[96:99], v[64:67], v[36:39]\n"
        "v_mfma_f32_16x16x32_bf16 v[40:43], a[96:99], v[96:99], v[232:235]\n"
        "v_mfma_f32_16x16x32_bf16 v[40:43], a[100:103], v[100:103], v[40:43]\n"
        "v_mfma_f32_16x16x32_bf16 v[40:43], a[104:107], v[104:107], v[40:43]\n"
        "v_mfma_f32_16x16x32_bf16 v[40:43], a[108:111], v[108:111], v[40:43]\n"
        "v_exp_f32 v32, v32\n"
        "v_mfma_f32_16x16x32_bf16 v[44:47], a[108:111], v[140:143], v[236:239]\n"
        "v_exp_f32 v33, v33\n"
        "v_mfma_f32_16x16x32_bf16 v[44:47], a[104:107], v[136:139], v[44:47]\n"
        "ds_read_b128 a[104:107], v240 offset:26624\n"
        "v_mfma_f32_16x16x32_bf16 v[44:47], a[100:103], v[132:135], v[44:47]\n"
        "ds_read_b128 a[100:103], v240 offset:25600\n"
        "v_mfma_f32_16x16x32_bf16 v[44:47], a[96:99], v[128:131], v[44:47]\n"
        "ds_read_b128 a[96:99], v240 offset:24576\n"
        "v_mfma_f32_16x16x32_bf16 v[176:179], a[128:131], v[16:19], v[176:179]\n"
        "s_waitcnt lgkmcnt(3)\n"
        "v_exp_f32 v34, v34\n"
        "v_mfma_f32_16x16x32_bf16 v[208:211], a[128:131], v[20:23], v[208:211]\n"
        "v_exp_f32 v35, v35\n"
        "v_mfma_f32_16x16x32_bf16 a[16:19], a[128:131], v[24:27], a[16:19]\n"
        "ds_read_b128 a[108:111], v240 offset:27648\n"
        "v_mfma_f32_16x16x32_bf16 a[48:51], a[128:131], v[28:31], a[48:51]\n"
        "ds_read_b128 a[128:131], v241 offset:12288\n"
        "v_mfma_f32_16x16x32_bf16 a[52:55], a[132:135], v[28:31], a[52:55]\n"
        "v_exp_f32 v36, v36\n"
        "v_mfma_f32_16x16x32_bf16 a[20:23], a[132:135], v[24:27], a[20:23]\n"
        "v_exp_f32 v37, v37\n"
        "v_mfma_f32_16x16x32_bf16 v[212:215], a[132:135], v[20:23], v[212:215]\n"
        "v_exp_f32 v38, v38\n"
        "v_mfma_f32_16x16x32_bf16 v[180:183], a[132:135], v[16:19], v[180:183]\n"
        "ds_read_b128 a[132:135], v241 offset:13312\n"
        "v_mfma_f32_16x16x32_bf16 v[184:187], a[136:139], v[16:19], v[184:187]\n"
        "v_exp_f32 v39, v39\n"
        "v_mfma_f32_16x16x32_bf16 v[216:219], a[136:139], v[20:23], v[216:219]\n"
        "v_exp_f32 v40, v40\n"
        "v_mfma_f32_16x16x32_bf16 a[24:27], a[136:139], v[24:27], a[24:27]\n"
        "v_exp_f32 v41, v41\n"
        "v_mfma_f32_16x16x32_bf16 a[56:59], a[136:139], v[28:31], a[56:59]\n"
        "ds_read_b128 a[136:139], v241 offset:14336\n"
        "v_mfma_f32_16x16x32_bf16 a[60:63], a[140:143], v[28:31], a[60:63]\n"
        "v_exp_f32 v42, v42\n"
        "v_mfma_f32_16x16x32_bf16 a[28:31], a[140:143], v[24:27], a[28:31]\n"
        "v_exp_f32 v43, v43\n"
        "v_mfma_f32_16x16x32_bf16 v[220:223], a[140:143], v[20:23], v[220:223]\n"
        "v_exp_f32 v44, v44\n"
        "v_mfma_f32_16x16x32_bf16 v[188:191], a[140:143], v[16:19], v[188:191]\n"
        "ds_read_b128 a[140:143], v241 offset:15360\n"
        "global_load_dword v244, v245, s[26:27] offset:0\n"
        "v_mfma_f32_16x16x32_bf16 v[48:51], a[112:115], v[0:3], v[224:227]\n"
        "v_exp_f32 v45, v45\n"
        "v_mfma_f32_16x16x32_bf16 v[48:51], a[116:119], v[4:7], v[48:51]\n"
        "v_exp_f32 v46, v46\n"
        "v_mfma_f32_16x16x32_bf16 v[48:51], a[120:123], v[8:11], v[48:51]\n"
        "v_exp_f32 v47, v47\n"
        "v_mfma_f32_16x16x32_bf16 v[48:51], a[124:127], v[12:15], v[48:51]\n"
        "s_add_u32 s26, s26, 0x20000\n"
        "s_addc_u32 s27, s27, 0\n"
        "v_cvt_pk_bf16_f32 v16, v32, v33\n"
        "v_mfma_f32_16x16x32_bf16 v[52:55], a[124:127], v[76:79], v[228:231]\n"
        "v_cvt_pk_bf16_f32 v17, v34, v35\n"
        "v_cvt_pk_bf16_f32 v20, v36, v37\n"
        "v_mfma_f32_16x16x32_bf16 v[52:55], a[120:123], v[72:75], v[52:55]\n"
        "v_cvt_pk_bf16_f32 v21, v38, v39\n"
        "v_cvt_pk_bf16_f32 v24, v40, v41\n"
        "v_mfma_f32_16x16x32_bf16 v[52:55], a[116:119], v[68:71], v[52:55]\n"
        "v_exp_f32 v48, v48\n"
        "v_mfma_f32_16x16x32_bf16 v[52:55], a[112:115], v[64:67], v[52:55]\n"
        "v_exp_f32 v49, v49\n"
        "v_mfma_f32_16x16x32_bf16 v[56:59], a[112:115], v[96:99], v[232:235]\n"
        "v_exp_f32 v50, v50\n"
        "v_mfma_f32_16x16x32_bf16 v[56:59], a[116:119], v[100:103], v[56:59]\n"
        "v_exp_f32 v51, v51\n"
        "v_mfma_f32_16x16x32_bf16 v[56:59], a[120:123], v[104:107], v[56:59]\n"
        "v_cvt_pk_bf16_f32 v25, v42, v43\n"
        "v_cvt_pk_bf16_f32 v28, v44, v45\n"
        "v_mfma_f32_16x16x32_bf16 v[56:59], a[124:127], v[108:111], v[56:59]\n"
        "v_exp_f32 v52, v52\n"
        "v_mfma_f32_16x16x32_bf16 v[60:63], a[124:127], v[140:143], v[236:239]\n"
        "ds_read_b128 a[124:127], v240 offset:31744\n"
        "v_cvt_pk_bf16_f32 v29, v46, v47\n"
        "v_mfma_f32_16x16x32_bf16 v[60:63], a[120:123], v[136:139], v[60:63]\n"
        "ds_read_b128 a[120:123], v240 offset:30720\n"
        "v_cvt_pk_bf16_f32 v18, v48, v49\n"
        "v_mfma_f32_16x16x32_bf16 v[60:63], a[116:119], v[132:135], v[60:63]\n"
        "ds_read_b128 a[116:119], v240 offset:29696\n"
        "v_cvt_pk_bf16_f32 v19, v50, v51\n"
        "v_mfma_f32_16x16x32_bf16 v[60:63], a[112:115], v[128:131], v[60:63]\n"
        "ds_read_b128 a[112:115], v240 offset:28672\n"
        "v_xor_b32 v240, 0x8000, v240\n"
        "v_mfma_f32_16x16x32_bf16 a[64:67], a[80:83], v[80:83], a[64:67]\n"
        "v_exp_f32 v53, v53\n"
        "v_mfma_f32_16x16x32_bf16 a[68:71], a[80:83], v[84:87], a[68:71]\n"
        "v_exp_f32 v54, v54\n"
        "v_mfma_f32_16x16x32_bf16 a[72:75], a[80:83], v[88:91], a[72:75]\n"
        "v_exp_f32 v55, v55\n"
        "v_mfma_f32_16x16x32_bf16 a[76:79], a[80:83], v[92:95], a[76:79]\n"
        "v_exp_f32 v56, v56\n"
        "v_mfma_f32_16x16x32_bf16 v[160:163], a[144:147], v[80:83], v[160:163]\n"
        "v_exp_f32 v57, v57\n"
        "v_mfma_f32_16x16x32_bf16 v[192:195], a[144:147], v[84:87], v[192:195]\n"
        "v_exp_f32 v58, v58\n"
        "v_mfma_f32_16x16x32_bf16 a[0:3], a[144:147], v[88:91], a[0:3]\n"
        "v_exp_f32 v59, v59\n"
        "v_mfma_f32_16x16x32_bf16 a[32:35], a[144:147], v[92:95], a[32:35]\n"
        "ds_read_b128 a[144:147], v241 offset:16384\n"
        "v_cvt_pk_bf16_f32 v22, v52, v53\n"
        "v_mfma_f32_16x16x32_bf16 a[36:39], a[148:151], v[92:95], a[36:39]\n"
        "s_waitcnt vmcnt(10) lgkmcnt(1)\n"
        "v_exp_f32 v60, v60\n"
        "v_mfma_f32_16x16x32_bf16 a[4:7], a[148:151], v[88:91], a[4:7]\n"
        "s_barrier\n"
        "v_mfma_f32_16x16x32_bf16 v[196:199], a[148:151], v[84:87], v[196:199]\n"
        "v_exp_f32 v61, v61\n"
        "v_mfma_f32_16x16x32_bf16 v[164:167], a[148:151], v[80:83], v[164:167]\n"
        "ds_read_b128 a[148:151], v241 offset:17408\n"
        "v_cvt_pk_bf16_f32 v23, v54, v55\n"
        "v_mfma_f32_16x16x32_bf16 v[168:171], a[152:155], v[80:83], v[168:171]\n"
        "v_exp_f32 v62, v62\n"
        "v_mfma_f32_16x16x32_bf16 v[200:203], a[152:155], v[84:87], v[200:203]\n"
        "v_exp_f32 v63, v63\n"
        "v_mfma_f32_16x16x32_bf16 a[8:11], a[152:155], v[88:91], a[8:11]\n"
        "s_cmp_eq_u32 s82, s24\n"
        "s_cbranch_scc1 LEVD0_%=\n"
        "LEWD0_%=:\n"
        "s_add_u32 m0, s80, 0x0\n"
        "v_mfma_f32_16x16x32_bf16 a[40:43], a[152:155], v[92:95], a[40:43]\n"
        "ds_read_b128 a[152:155], v241 offset:18432\n"
        "global_load_lds_dwordx4 v254, s[68:69] offset:0 sc1\n"
        "v_mfma_f32_16x16x32_bf16 a[44:47], a[156:159], v[92:95], a[44:47]\n"
        "v_cvt_pk_bf16_f32 v26, v56, v57\n"
        "v_cvt_pk_bf16_f32 v27, v58, v59\n"
        "v_mfma_f32_16x16x32_bf16 a[12:15], a[156:159], v[88:91], a[12:15]\n"
        "v_cvt_pk_bf16_f32 v30, v60, v61\n"
        "v_cvt_pk_bf16_f32 v31, v62, v63\n"
        "v_mfma_f32_16x16x32_bf16 v[204:207], a[156:159], v[84:87], v[204:207]\n"
        "v_mfma_f32_16x16x32_bf16 v[172:175], a[156:159], v[80:83], v[172:175]\n"
        "ds_read_b128 a[156:159], v241 offset:19456\n"
        "global_load_lds_dwordx4 v254, s[68:69] offset:1024 sc1\n"
        "v_mfma_f32_16x16x32_bf16 v[32:35], a[96:99], v[0:3], v[224:227]\n"
        "v_mfma_f32_16x16x32_bf16 v[32:35], a[100:103], v[4:7], v[32:35]\n"
        "v_mfma_f32_16x16x32_bf16 v[32:35], a[104:107], v[8:11], v[32:35]\n"
        "v_mfma_f32_16x16x32_bf16 v[32:35], a[108:111], v[12:15], v[32:35]\n"
        "global_load_lds_dwordx4 v254, s[68:69] offset:2048 sc1\n"
        "v_mfma_f32_16x16x32_bf16 v[36:39], a[108:111], v[76:79], v[228:231]\n"
        "v_mfma_f32_16x16x32_bf16 v[36:39], a[104:107], v[72:75], v[36:39]\n"
        "v_mfma_f32_16x16x32_bf16 v[36:39], a[100:103], v[68:71], v[36:39]\n"
        "v_mfma_f32_16x16x32_bf16 v[36:39], a[96:99], v[64:67], v[36:39]\n"
        "global_load_lds_dwordx4 v254, s[68:69] offset:3072 sc1\n"
        "v_mfma_f32_16x16x32_bf16 v[40:43], a[96:99], v[96:99], v[232:235]\n"
        "v_mfma_f32_16x16x32_bf16 v[40:43], a[100:103], v[100:103], v[40:43]\n"
        "v_exp_f32 v32, v32\n"
        "v_mfma_f32_16x16x32_bf16 v[40:43], a[104:107], v[104:107], v[40:43]\n"
        "v_exp_f32 v33, v33\n"
        "v_mfma_f32_16x16x32_bf16 v[40:43], a[108:111], v[108:111], v[40:43]\n"
        "v_exp_f32 v34, v34\n"
        "v_mfma_f32_16x16x32_bf16 v[44:47], a[108:111], v[140:143], v[236:239]\n"
        "v_exp_f32 v35, v35\n"
        "v_mfma_f32_16x16x32_bf16 v[44:47], a[104:107], v[136:139], v[44:47]\n"
        "ds_read_b128 a[104:107], v240 offset:2048\n"
        "v_mfma_f32_16x16x32_bf16 v[44:47], a[100:103], v[132:135], v[44:47]\n"
        "ds_read_b128 a[100:103], v240 offset:1024\n"
        "v_mfma_f32_16x16x32_bf16 v[44:47], a[96:99], v[128:131], v[44:47]\n"
        "ds_read_b128 a[96:99], v240 offset:0\n"
        "v_mfma_f32_16x16x32_bf16 v[176:179], a[128:131], v[80:83], v[176:179]\n"
        "ds_read_b128 a[108:111], v240 offset:3072\n"
        "v_mfma_f32_16x16x32_bf16 v[208:211], a[128:131], v[84:87], v[208:211]\n"
        "v_exp_f32 v36, v36\n"
        "v_mfma_f32_16x16x32_bf16 a[16:19], a[128:131], v[88:91], a[16:19]\n"
        "v_exp_f32 v37, v37\n"
        "v_mfma_f32_16x16x32_bf16 a[48:51], a[128:131], v[92:95], a[48:51]\n"
        "ds_read_b128 a[128:131], v241 offset:20480\n"
        "v_mfma_f32_16x16x32_bf16 a[52:55], a[132:135], v[92:95], a[52:55]\n"
        "v_exp_f32 v38, v38\n"
        "v_mfma_f32_16x16x32_bf16 a[20:23], a[132:135], v[88:91], a[20:23]\n"
        "v_exp_f32 v39, v39\n"
        "v_mfma_f32_16x16x32_bf16 v[212:215], a[132:135], v[84:87], v[212:215]\n"
        "v_exp_f32 v40, v40\n"
        "v_mfma_f32_16x16x32_bf16 v[180:183], a[132:135], v[80:83], v[180:183]\n"
        "ds_read_b128 a[132:135], v241 offset:21504\n"
        "v_mfma_f32_16x16x32_bf16 v[184:187], a[136:139], v[80:83], v[184:187]\n"
        "v_exp_f32 v41, v41\n"
        "v_mfma_f32_16x16x32_bf16 v[216:219], a[136:139], v[84:87], v[216:219]\n"
        "v_exp_f32 v42, v42\n"
        "v_mfma_f32_16x16x32_bf16 a[24:27], a[136:139], v[88:91], a[24:27]\n"
        "v_exp_f32 v43, v43\n"
        "v_mfma_f32_16x16x32_bf16 a[56:59], a[136:139], v[92:95], a[56:59]\n"
        "ds_read_b128 a[136:139], v241 offset:22528\n"
        "s_waitcnt lgkmcnt(2)\n"
        "v_mfma_f32_16x16x32_bf16 a[60:63], a[140:143], v[92:95], a[60:63]\n"
        "v_exp_f32 v44, v44\n"
        "v_mfma_f32_16x16x32_bf16 a[28:31], a[140:143], v[88:91], a[28:31]\n"
        "v_exp_f32 v45, v45\n"
        "v_mfma_f32_16x16x32_bf16 v[220:223], a[140:143], v[84:87], v[220:223]\n"
        "v_exp_f32 v46, v46\n"
        "v_mfma_f32_16x16x32_bf16 v[188:191], a[140:143], v[80:83], v[188:191]\n"
        "ds_read_b128 a[140:143], v241 offset:23552\n"
        "v_cvt_pk_bf16_f32 v80, v32, v33\n"
        "v_mfma_f32_16x16x32_bf16 v[48:51], a[112:115], v[0:3], v[224:227]\n"
        "v_exp_f32 v47, v47\n"
        "v_mfma_f32_16x16x32_bf16 v[48:51], a[116:119], v[4:7], v[48:51]\n"
        "v_cvt_pk_bf16_f32 v81, v34, v35\n"
        "v_cvt_pk_bf16_f32 v84, v36, v37\n"
        "v_mfma_f32_16x16x32_bf16 v[48:51], a[120:123], v[8:11], v[48:51]\n"
        "v_cvt_pk_bf16_f32 v85, v38, v39\n"
        "v_cvt_pk_bf16_f32 v88, v40, v41\n"
        "v_mfma_f32_16x16x32_bf16 v[48:51], a[124:127], v[12:15], v[48:51]\n"
        "v_cvt_pk_bf16_f32 v89, v42, v43\n"
        "v_cvt_pk_bf16_f32 v92, v44, v45\n"
        "v_mfma_f32_16x16x32_bf16 v[52:55], a[124:127], v[76:79], v[228:231]\n"
        "v_cvt_pk_bf16_f32 v93, v46, v47\n"
        "v_mfma_f32_16x16x32_bf16 v[52:55], a[120:123], v[72:75], v[52:55]\n"
        "v_mfma_f32_16x16x32_bf16 v[52:55], a[116:119], v[68:71], v[52:55]\n"
        "v_mfma_f32_16x16x32_bf16 v[52:55], a[112:115], v[64:67], v[52:55]\n"
        "v_mfma_f32_16x16x32_bf16 v[56:59], a[112:115], v[96:99], v[232:235]\n"
        "v_mfma_f32_16x16x32_bf16 v[56:59], a[116:119], v[100:103], v[56:59]\n"
        "v_mfma_f32_16x16x32_bf16 v[56:59], a[120:123], v[104:107], v[56:59]\n"
        "v_mfma_f32_16x16x32_bf16 v[56:59], a[124:127], v[108:111], v[56:59]\n"
        "v_mfma_f32_16x16x32_bf16 v[60:63], a[124:127], v[140:143], v[236:239]\n"
        "v_mfma_f32_16x16x32_bf16 v[60:63], a[120:123], v[136:139], v[60:63]\n"
        "ds_read_b128 a[120:123], v240 offset:6144\n"
        "v_mfma_f32_16x16x32_bf16 v[60:63], a[116:119], v[132:135], v[60:63]\n"
        "ds_read_b128 a[116:119], v240 offset:5120\n"
        "v_mfma_f32_16x16x32_bf16 v[60:63], a[112:115], v[128:131], v[60:63]\n"
        "ds_read_b128 a[112:115], v240 offset:4096\n"
        "v_mfma_f32_16x16x32_bf16 a[64:67], a[80:83], v[16:19], a[64:67]\n"
        "v_mfma_f32_16x16x32_bf16 a[68:71], a[80:83], v[20:23], a[68:71]\n"
        "v_mfma_f32_16x16x32_bf16 a[72:75], a[80:83], v[24:27], a[72:75]\n"
        "v_mfma_f32_16x16x32_bf16 a[76:79], a[80:83], v[28:31], a[76:79]\n"
        "v_mfma_f32_16x16x32_bf16 v[160:163], a[144:147], v[16:19], v[160:163]\n"
        "v_mfma_f32_16x16x32_bf16 v[192:195], a[144:147], v[20:23], v[192:195]\n"
        "v_mfma_f32_16x16x32_bf16 a[0:3], a[144:147], v[24:27], a[0:3]\n"
        "v_mfma_f32_16x16x32_bf16 a[32:35], a[144:147], v[28:31], a[32:35]\n"
        "ds_read_b128 a[144:147], v241 offset:24576\n"
        "v_mfma_f32_16x16x32_bf16 a[36:39], a[148:151], v[28:31], a[36:39]\n"
        "v_mfma_f32_16x16x32_bf16 a[4:7], a[148:151], v[24:27], a[4:7]\n"
        "v_mfma_f32_16x16x32_bf16 v[196:199], a[148:151], v[20:23], v[196:199]\n"
        "v_mfma_f32_16x16x32_bf16 v[164:167], a[148:151], v[16:19], v[164:167]\n"
        "ds_read_b128 a[148:151], v241 offset:25600\n"
        "v_mfma_f32_16x16x32_bf16 v[168:171], a[152:155], v[16:19], v[168:171]\n"
        "v_mfma_f32_16x16x32_bf16 v[200:203], a[152:155], v[20:23], v[200:203]\n"
        "v_mfma_f32_16x16x32_bf16 a[8:11], a[152:155], v[24:27], a[8:11]\n"
        "v_mfma_f32_16x16x32_bf16 a[40:43], a[152:155], v[28:31], a[40:43]\n"
        "ds_read_b128 a[152:155], v241 offset:26624\n"
        "v_mfma_f32_16x16x32_bf16 a[44:47], a[156:159], v[28:31], a[44:47]\n"
        "v_mfma_f32_16x16x32_bf16 a[12:15], a[156:159], v[24:27], a[12:15]\n"
        "v_mfma_f32_16x16x32_bf16 v[204:207], a[156:159], v[20:23], v[204:207]\n"
        "s_cmp_lg_u32 s91, 0\n"
        "v_mfma_f32_16x16x32_bf16 v[172:175], a[156:159], v[16:19], v[172:175]\n"
        "s_cbranch_scc1 LDU_%=\n"
        "s_branch LLD_%=\n"
        "LEVD0_%=:\n"
        "s_lshl_b32 s97, s41, 12\n"
        "s_add_u32 s97, s97, 32768\n"
        "v_bfe_u32 v246, v254, 4, 5\n"
        "v_lshl_add_u32 v246, v246, 7, s97\n"
        "s_cmp_eq_u32 s82, s83\n"
        "s_cbranch_scc1 LE3D0_%=\n"
        "global_load_dwordx2 v[252:253], v246, s[48:49] sc1\n"
        "s_mov_b32 s24, s83\n"
        "v_accvgpr_read_b32 v246, a93\n"
        "global_load_dword v244, v246, s[46:47]\n"
        "s_branch LEWD0_%=\n"
        "LE3D0_%=:\n"
        "s_mov_b32 s97, 0x200000\n"
        "LE4D0_%=:\n"
        "v_cmp_ne_u64 vcc, v[252:253], v[250:251]\n"
        "s_nop 1\n"
        "s_cbranch_vccz LE5D0_%=\n"
        "s_sleep 2\n"
        "global_load_dwordx2 v[252:253], v246, s[48:49] sc1\n"
        "s_waitcnt vmcnt(0)\n"
        "s_sub_u32 s97, s97, 1\n"
        "s_cmp_lg_u32 s97, 0\n"
        "s_cbranch_scc1 LE4D0_%=\n"
        "LE5D0_%=:\n"
        "s_mov_b64 s[68:69], s[92:93]\n"
        "s_mov_b64 s[74:75], s[94:95]\n"
        "s_mov_b32 s80, s50\n"
        "s_mov_b32 s81, s51\n"
        "s_mov_b64 s[66:67], s[22:23]\n"
        "s_mov_b32 s90, s29\n"
        "s_mov_b32 s25, s30\n"
        "s_mov_b32 s28, 16384\n"
        "s_xor_b32 s79, s73, 0x8000\n"
        "v_accvgpr_read_b32 v144, a84\n"
        "v_accvgpr_read_b32 v145, a85\n"
        "v_accvgpr_read_b32 v146, a86\n"
        "v_accvgpr_read_b32 v147, a87\n"
        "v_accvgpr_read_b32 v148, a88\n"
        "v_accvgpr_read_b32 v152, a89\n"
        "v_accvgpr_read_b32 v252, a90\n"
        "v_accvgpr_read_b32 v249, a91\n"
        "v_accvgpr_read_b32 v153, a92\n"
        "v_mov_b32 v246, 0\n"
        "v_mov_b32 v247, 0\n"
        "v_mov_b32 v248, 0\n"
        "v_add_u32 v149, 1024, v148\n"
        "v_add_u32 v150, 2048, v148\n"
        "v_add_u32 v151, 3072, v148\n"
        "s_mov_b64 exec, 15\n"
        "ds_write_b128 v252, v[246:249]\n"
        "s_mov_b64 exec, -1\n"
        "s_branch LEWD0_%=\n"
        "; ---- LDRAIN\n"
        "LLD_%=:\n"
        "v_mfma_f32_16x16x32_bf16 a[16:19], a[128:131], v[24:27], a[16:19]\n"
        "LHC0_%=:\n"
        "s_cbranch_execz LHRC_%=\n"
        "s_waitcnt lgkmcnt(1)\n"
        "ds_read_b128 a[160:163], v241 offset:24576\n"
        "s_mul_i32 s53, s44, 1052672\n"
        "v_mfma_f32_16x16x32_bf16 a[48:51], a[128:131], v[28:31], a[48:51]\n"
        "ds_read_b128 a[164:167], v241 offset:25600\n"
        "s_lshl_b32 s96, s43, 15\n"
        "s_add_u32 s53, s53, s96\n"
        "v_mfma_f32_16x16x32_bf16 a[52:55], a[132:135], v[28:31], a[52:55]\n"
        "ds_read_b128 a[168:171], v241 offset:26624\n"
        "s_lshl_b32 s96, s40, 10\n"
        "s_add_u32 s53, s53, s96\n"
        "v_mfma_f32_16x16x32_bf16 a[20:23], a[132:135], v[24:27], a[20:23]\n"
        "ds_read_b128 a[172:175], v241 offset:27648\n"
        "s_add_u32 s53, s53, 0x2201000\n"
        "s_sub_u32 s91, s83, s44\n"
        "v_mfma_f32_16x16x32_bf16 a[24:27], a[136:139], v[24:27], a[24:27]\n"
        "ds_read_b128 a[176:179], v241 offset:28672\n"
        "s_add_u32 s70, s38, s53\n"
        "s_addc_u32 s71, s39, 0\n"
        "v_mfma_f32_16x16x32_bf16 a[56:59], a[136:139], v[28:31], a[56:59]\n"
        "ds_read_b128 a[180:183], v241 offset:29696\n"
        "v_mfma_f32_16x16x32_bf16 a[60:63], a[140:143], v[28:31], a[60:63]\n"
        "ds_read_b128 a[184:187], v241 offset:30720\n"
        "v_mfma_f32_16x16x32_bf16 a[28:31], a[140:143], v[24:27], a[28:31]\n"
        "ds_read_b128 a[188:191], v241 offset:31744\n"
        "v_exp_f32 v56, v56\n"
        "v_exp_f32 v57, v57\n"
        "v_exp_f32 v58, v58\n"
        "v_cvt_pk_bf16_f32 v90, v56, v57\n"
        "v_exp_f32 v59, v59\n"
        "v_exp_f32 v60, v60\n"
        "v_cvt_pk_bf16_f32 v91, v58, v59\n"
        "s_waitcnt lgkmcnt(5)\n"
        "s_nop 0\n"
        "v_mfma_f32_16x16x32_bf16 a[0:3], a[160:163], v[88:91], a[0:3]\n"
        "v_exp_f32 v61, v61\n"
        "v_mfma_f32_16x16x32_bf16 a[72:75], a[80:83], v[88:91], a[72:75]\n"
        "v_exp_f32 v62, v62\n"
        "v_mfma_f32_16x16x32_bf16 a[4:7], a[164:167], v[88:91], a[4:7]\n"
        "v_exp_f32 v63, v63\n"
        "v_mfma_f32_16x16x32_bf16 a[8:11], a[168:171], v[88:91], a[8:11]\n"
        "s_waitcnt lgkmcnt(1)\n"
        "v_cvt_pk_bf16_f32 v94, v60, v61\n"
        "v_cvt_pk_bf16_f32 v95, v62, v63\n"
        "s_nop 1\n"
        "v_mfma_f32_16x16x32_bf16 a[76:79], a[80:83], v[92:95], a[76:79]\n"
        "v_accvgpr_read_b32 v248, a72\n"
        "v_accvgpr_read_b32 v120, a0\n"
        "v_mfma_f32_16x16x32_bf16 a[32:35], a[160:163], v[92:95], a[32:35]\n"
        "v_rcp_f32 v246, v248\n"
        "v_mfma_f32_16x16x32_bf16 a[36:39], a[164:167], v[92:95], a[36:39]\n"
        "v_accvgpr_read_b32 v121, a1\n"
        "v_accvgpr_read_b32 v122, a2\n"
        "v_mfma_f32_16x16x32_bf16 a[40:43], a[168:171], v[92:95], a[40:43]\n"
        "v_accvgpr_read_b32 v249, a76\n"
        "v_accvgpr_read_b32 v123, a3\n"
        "v_mfma_f32_16x16x32_bf16 a[44:47], a[172:175], v[92:95], a[44:47]\n"
        "v_rcp_f32 v247, v249\n"
        "v_mfma_f32_16x16x32_bf16 a[12:15], a[172:175], v[88:91], a[12:15]\n"
        "v_accvgpr_read_b32 v112, a32\n"
        "v_accvgpr_read_b32 v113, a33\n"
        "v_mfma_f32_16x16x32_bf16 a[16:19], a[176:179], v[88:91], a[16:19]\n"
        "v_accvgpr_read_b32 v114, a34\n"
        "v_accvgpr_read_b32 v115, a35\n"
        "v_mfma_f32_16x16x32_bf16 a[48:51], a[176:179], v[92:95], a[48:51]\n"
        "v_accvgpr_read_b32 v116, a36\n"
        "v_accvgpr_read_b32 v117, a37\n"
        "v_mfma_f32_16x16x32_bf16 a[52:55], a[180:183], v[92:95], a[52:55]\n"
        "s_waitcnt lgkmcnt(0)\n"
        "v_accvgpr_read_b32 v118, a38\n"
        "v_accvgpr_read_b32 v119, a39\n"
        "v_mfma_f32_16x16x32_bf16 a[20:23], a[180:183], v[88:91], a[20:23]\n"
        "v_mul_f32 v112, v112, v247\n"
        "v_mul_f32 v113, v113, v247\n"
        "v_mfma_f32_16x16x32_bf16 a[24:27], a[184:187], v[88:91], a[24:27]\n"
        "v_mul_f32 v114, v114, v247\n"
        "v_mul_f32 v115, v115, v247\n"
        "v_mfma_f32_16x16x32_bf16 a[56:59], a[184:187], v[92:95], a[56:59]\n"
        "v_mul_f32 v116, v116, v247\n"
        "v_mul_f32 v117, v117, v247\n"
        "v_mfma_f32_16x16x32_bf16 a[60:63], a[188:191], v[92:95], a[60:63]\n"
        "v_mul_f32 v118, v118, v247\n"
        "v_mul_f32 v119, v119, v247\n"
        "v_mfma_f32_16x16x32_bf16 a[28:31], a[188:191], v[88:91], a[28:31]\n"
        "v_cvt_pk_bf16_f32 v112, v112, v113\n"
        "v_cvt_pk_bf16_f32 v113, v114, v115\n"
        "v_cvt_pk_bf16_f32 v114, v116, v117\n"
        "v_cvt_pk_bf16_f32 v115, v118, v119\n"
        "global_store_dwordx4 v254, v[112:115], s[70:71] offset:-4096 sc1\n"
        "v_accvgpr_read_b32 v124, a4\n"
        "v_accvgpr_read_b32 v125, a5\n"
        "v_accvgpr_read_b32 v126, a6\n"
        "v_accvgpr_read_b32 v127, a7\n"
        "v_mul_f32 v120, v120, v246\n"
        "v_mul_f32 v121, v121, v246\n"
        "v_mul_f32 v122, v122, v246\n"
        "v_mul_f32 v123, v123, v246\n"
        "v_mul_f32 v124, v124, v246\n"
        "v_mul_f32 v125, v125, v246\n"
        "v_mul_f32 v126, v126, v246\n"
        "v_mul_f32 v127, v127, v246\n"
        "v_cvt_pk_bf16_f32 v120, v120, v121\n"
        "v_cvt_pk_bf16_f32 v121, v122, v123\n"
        "v_cvt_pk_bf16_f32 v122, v124, v125\n"
        "v_cvt_pk_bf16_f32 v123, v126, v127\n"
        "global_store_dwordx4 v254, v[120:123], s[70:71] offset:0 sc1\n"
        "s_add_u32 s70, s70, 0x2000\n"
        "s_addc_u32 s71, s71, 0\n"
        "v_accvgpr_read_b32 v40, a40\n"
        "v_accvgpr_read_b32 v41, a41\n"
        "v_accvgpr_read_b32 v42, a42\n"
        "v_accvgpr_read_b32 v43, a43\n"
        "v_accvgpr_read_b32 v44, a44\n"
        "v_accvgpr_read_b32 v45, a45\n"
        "v_accvgpr_read_b32 v46, a46\n"
        "v_accvgpr_read_b32 v47, a47\n"
        "v_mul_f32 v40, v40, v247\n"
        "v_mul_f32 v41, v41, v247\n"
        "v_mul_f32 v42, v42, v247\n"
        "v_mul_f32 v43, v43, v247\n"
        "v_mul_f32 v44, v44, v247\n"
        "v_mul_f32 v45, v45, v247\n"
        "v_mul_f32 v46, v46, v247\n"
        "v_mul_f32 v47, v47, v247\n"
        "v_cvt_pk_bf16_f32 v40, v40, v41\n"
        "v_cvt_pk_bf16_f32 v41, v42, v43\n"
        "v_cvt_pk_bf16_f32 v42, v44, v45\n"
        "v_cvt_pk_bf16_f32 v43, v46, v47\n"
        "global_store_dwordx4 v254, v[40:43], s[70:71] offset:-4096 sc1\n"
        "v_accvgpr_read_b32 v56, a8\n"
        "v_accvgpr_read_b32 v57, a9\n"
        "v_accvgpr_read_b32 v58, a10\n"
        "v_accvgpr_read_b32 v59, a11\n"
        "v_accvgpr_read_b32 v60, a12\n"
        "v_accvgpr_read_b32 v61, a13\n"
        "v_accvgpr_read_b32 v62, a14\n"
        "v_accvgpr_read_b32 v63, a15\n"
        "v_mul_f32 v56, v56, v246\n"
        "v_mul_f32 v57, v57, v246\n"
        "v_mul_f32 v58, v58, v246\n"
        "v_mul_f32 v59, v59, v246\n"
        "v_mul_f32 v60, v60, v246\n"
        "v_mul_f32 v61, v61, v246\n"
        "v_mul_f32 v62, v62, v246\n"
        "v_mul_f32 v63, v63, v246\n"
        "v_cvt_pk_bf16_f32 v56, v56, v57\n"
        "v_cvt_pk_bf16_f32 v57, v58, v59\n"
        "v_cvt_pk_bf16_f32 v58, v60, v61\n"
        "v_cvt_pk_bf16_f32 v59, v62, v63\n"
        "global_store_dwordx4 v254, v[56:59], s[70:71] offset:0 sc1\n"
        "s_add_u32 s70, s70, 0x2000\n"
        "s_addc_u32 s71, s71, 0\n"
        "v_accvgpr_read_b32 v112, a48\n"
        "v_accvgpr_read_b32 v113, a49\n"
        "v_accvgpr_read_b32 v114, a50\n"
        "v_accvgpr_read_b32 v115, a51\n"
        "v_accvgpr_read_b32 v116, a52\n"
        "v_accvgpr_read_b32 v117, a53\n"
        "v_accvgpr_read_b32 v118, a54\n"
        "v_accvgpr_read_b32 v119, a55\n"
        "v_mul_f32 v112, v112, v247\n"
        "v_mul_f32 v113, v113, v247\n"
        "v_mul_f32 v114, v114, v247\n"
        "v_mul_f32 v115, v115, v247\n"
        "v_mul_f32 v116, v116, v247\n"
        "v_mul_f32 v117, v117, v247\n"
        "v_mul_f32 v118, v118, v247\n"
        "v_mul_f32 v119, v119, v247\n"
        "v_cvt_pk_bf16_f32 v112, v112, v113\n"
        "v_cvt_pk_bf16_f32 v113, v114, v115\n"
        "v_cvt_pk_bf16_f32 v114, v116, v117\n"
        "v_cvt_pk_bf16_f32 v115, v118, v119\n"
        "global_store_dwordx4 v254, v[112:115], s[70:71] offset:-4096 sc1\n"
        "v_accvgpr_read_b32 v120, a16\n"
        "v_accvgpr_read_b32 v121, a17\n"
        "v_accvgpr_read_b32 v122, a18\n"
        "v_accvgpr_read_b32 v123, a19\n"
        "v_accvgpr_read_b32 v124, a20\n"
        "v_accvgpr_read_b32 v125, a21\n"
        "v_accvgpr_read_b32 v126, a22\n"
        "v_accvgpr_read_b32 v127, a23\n"
        "v_mul_f32 v120, v120, v246\n"
        "v_mul_f32 v121, v121, v246\n"
        "v_mul_f32 v122, v122, v246\n"
        "v_mul_f32 v123, v123, v246\n"
        "v_mul_f32 v124, v124, v246\n"
        "v_mul_f32 v125, v125, v246\n"
        "v_mul_f32 v126, v126, v246\n"
        "v_mul_f32 v127, v127, v246\n"
        "v_cvt_pk_bf16_f32 v120, v120, v121\n"
        "v_cvt_pk_bf16_f32 v121, v122, v123\n"
        "v_cvt_pk_bf16_f32 v122, v124, v125\n"
        "v_cvt_pk_bf16_f32 v123, v126, v127\n"
        "global_store_dwordx4 v254, v[120:123], s[70:71] offset:0 sc1\n"
        "s_add_u32 s70, s70, 0x2000\n"
        "s_addc_u32 s71, s71, 0\n"
        "v_accvgpr_read_b32 v40, a56\n"
        "v_accvgpr_read_b32 v41, a57\n"
        "v_accvgpr_read_b32 v42, a58\n"
        "v_accvgpr_read_b32 v43, a59\n"
        "v_accvgpr_read_b32 v44, a60\n"
        "v_accvgpr_read_b32 v45, a61\n"
        "v_accvgpr_read_b32 v46, a62\n"
        "v_accvgpr_read_b32 v47, a63\n"
        "v_mul_f32 v40, v40, v247\n"
        "v_mul_f32 v41, v41, v247\n"
        "v_mul_f32 v42, v42, v247\n"
        "v_mul_f32 v43, v43, v247\n"
        "v_mul_f32 v44, v44, v247\n"
        "v_mul_f32 v45, v45, v247\n"
        "v_mul_f32 v46, v46, v247\n"
        "v_mul_f32 v47, v47, v247\n"
        "v_cvt_pk_bf16_f32 v40, v40, v41\n"
        "v_cvt_pk_bf16_f32 v41, v42, v43\n"
        "v_cvt_pk_bf16_f32 v42, v44, v45\n"
        "v_cvt_pk_bf16_f32 v43, v46, v47\n"
        "global_store_dwordx4 v254, v[40:43], s[70:71] offset:-4096 sc1\n"
        "v_accvgpr_read_b32 v56, a24\n"
        "v_accvgpr_read_b32 v57, a25\n"
        "v_accvgpr_read_b32 v58, a26\n"
        "v_accvgpr_read_b32 v59, a27\n"
        "v_accvgpr_read_b32 v60, a28\n"
        "v_accvgpr_read_b32 v61, a29\n"
        "v_accvgpr_read_b32 v62, a30\n"
        "v_accvgpr_read_b32 v63, a31\n"
        "v_mul_f32 v56, v56, v246\n"
        "v_mul_f32 v57, v57, v246\n"
        "v_mul_f32 v58, v58, v246\n"
        "v_mul_f32 v59, v59, v246\n"
        "v_mul_f32 v60, v60, v246\n"
        "v_mul_f32 v61, v61, v246\n"
        "v_mul_f32 v62, v62, v246\n"
        "v_mul_f32 v63, v63, v246\n"
        "v_cvt_pk_bf16_f32 v56, v56, v57\n"
        "v_cvt_pk_bf16_f32 v57, v58, v59\n"
        "v_cvt_pk_bf16_f32 v58, v60, v61\n"
        "v_cvt_pk_bf16_f32 v59, v62, v63\n"
        "global_store_dwordx4 v254, v[56:59], s[70:71] offset:0 sc1\n"
        "v_add_f32 v247, v248, v249\n"
        "v_cmp_nge_f32 vcc, 0x4e6e6b28, v247\n"
        "s_nop 0\n"
        "s_cbranch_vccnz LRL_%=\n"
        "LSL_%=:\n"
        "s_cmp_eq_u32 s91, 0\n"
        "s_cbranch_scc1 LTL_%=\n"
        "; ---- SINGLE\n"
        "s_nop 0\n"
        "s_nop 0\n"
        "s_nop 0\n"
        "s_nop 0\n"
        "s_nop 0\n"
        "s_nop 0\n"
        "LSI_%=:\n"
        "v_mfma_f32_16x16x32_bf16 v[32:35], a[96:99], v[0:3], v[224:227]\n"
        "s_waitcnt lgkmcnt(4)\n"
        "ds_read_b128 a[156:159], v241 offset:27648\n"
        "s_add_u32 m0, s81, 0x0\n"
        "v_mfma_f32_16x16x32_bf16 v[32:35], a[100:103], v[4:7], v[32:35]\n"
        "ds_read_b128 a[124:127], v240 offset:7168\n"
        "global_load_lds_dwordx4 v254, s[74:75] offset:0 sc1\n"
        "s_xor_b32 s79, s79, 0x8000\n"
        "v_mfma_f32_16x16x32_bf16 v[32:35], a[104:107], v[8:11], v[32:35]\n"
        "s_add_u32 s82, s82, 1\n"
        "v_exp_f32 v48, v48\n"
        "v_mfma_f32_16x16x32_bf16 v[32:35], a[108:111], v[12:15], v[32:35]\n"
        "s_xor_b32 s78, s78, 0x8000\n"
        "v_exp_f32 v49, v49\n"
        "v_mfma_f32_16x16x32_bf16 v[36:39], a[108:111], v[76:79], v[228:231]\n"
        "ds_read_b128 a[108:111], v240 offset:11264\n"
        "s_add_u32 s81, s78, 4096\n"
        "v_cvt_pk_bf16_f32 v82, v48, v49\n"
        "v_mfma_f32_16x16x32_bf16 v[36:39], a[104:107], v[72:75], v[36:39]\n"
        "ds_read_b128 a[104:107], v240 offset:10240\n"
        "global_load_lds_dwordx4 v254, s[74:75] offset:1024 sc1\n"
        "s_mov_b32 s80, s78\n"
        "v_mfma_f32_16x16x32_bf16 v[36:39], a[100:103], v[68:71], v[36:39]\n"
        "s_sub_u32 s91, s91, 1\n"
        "v_exp_f32 v50, v50\n"
        "v_mfma_f32_16x16x32_bf16 v[36:39], a[96:99], v[64:67], v[36:39]\n"
        "v_exp_f32 v51, v51\n"
        "v_mfma_f32_16x16x32_bf16 v[176:179], a[128:131], v[16:19], v[176:179]\n"
        "ds_read_b128 a[96:99], v240 offset:8192\n"
        "v_cvt_pk_bf16_f32 v83, v50, v51\n"
        "v_mfma_f32_16x16x32_bf16 v[208:211], a[128:131], v[20:23], v[208:211]\n"
        "ds_read_b128 a[128:131], v241 offset:28672\n"
        "global_load_lds_dwordx4 v254, s[74:75] offset:2048 sc1\n"
        "v_mfma_f32_16x16x32_bf16 v[212:215], a[132:135], v[20:23], v[212:215]\n"
        "v_exp_f32 v52, v52\n"
        "v_mfma_f32_16x16x32_bf16 v[180:183], a[132:135], v[16:19], v[180:183]\n"
        "v_exp_f32 v53, v53\n"
        "v_mfma_f32_16x16x32_bf16 v[184:187], a[136:139], v[16:19], v[184:187]\n"
        "ds_read_b128 a[100:103], v240 offset:9216\n"
        "v_cvt_pk_bf16_f32 v86, v52, v53\n"
        "v_mfma_f32_16x16x32_bf16 v[216:219], a[136:139], v[20:23], v[216:219]\n"
        "ds_read_b128 a[132:135], v241 offset:29696\n"
        "global_load_lds_dwordx4 v254, s[74:75] offset:3072 sc1\n"
        "s_add_u32 m0, s79, 0x0\n"
        "v_mfma_f32_16x16x32_bf16 v[220:223], a[140:143], v[20:23], v[220:223]\n"
        "s_waitcnt lgkmcnt(4)\n"
        "v_exp_f32 v54, v54\n"
        "v_mfma_f32_16x16x32_bf16 v[188:191], a[140:143], v[16:19], v[188:191]\n"
        "v_exp_f32 v55, v55\n"
        "v_mfma_f32_16x16x32_bf16 v[48:51], a[112:115], v[0:3], v[224:227]\n"
        "ds_read_b128 a[136:139], v241 offset:30720\n"
        "v_cvt_pk_bf16_f32 v87, v54, v55\n"
        "v_mfma_f32_16x16x32_bf16 v[48:51], a[116:119], v[4:7], v[48:51]\n"
        "ds_read_b128 a[140:143], v241 offset:31744\n"
        "v_xor_b32 v241, 0x8000, v241\n"
        "v_mfma_f32_16x16x32_bf16 v[48:51], a[120:123], v[8:11], v[48:51]\n"
        "v_exp_f32 v32, v32\n"
        "v_mfma_f32_16x16x32_bf16 v[48:51], a[124:127], v[12:15], v[48:51]\n"
        "v_exp_f32 v33, v33\n"
        "v_mfma_f32_16x16x32_bf16 v[52:55], a[124:127], v[76:79], v[228:231]\n"
        "ds_read_b128 a[124:127], v240 offset:15360\n"
        "v_cvt_pk_bf16_f32 v16, v32, v33\n"
        "v_mfma_f32_16x16x32_bf16 v[52:55], a[120:123], v[72:75], v[52:55]\n"
        "v_exp_f32 v34, v34\n"
        "v_mfma_f32_16x16x32_bf16 v[52:55], a[116:119], v[68:71], v[52:55]\n"
        "v_exp_f32 v35, v35\n"
        "v_mfma_f32_16x16x32_bf16 v[52:55], a[112:115], v[64:67], v[52:55]\n"
        "ds_read_b128 a[112:115], v240 offset:12288\n"
        "v_cvt_pk_bf16_f32 v17, v34, v35\n"
        "v_mfma_f32_16x16x32_bf16 a[64:67], a[80:83], v[80:83], a[64:67]\n"
        "v_exp_f32 v36, v36\n"
        "v_mfma_f32_16x16x32_bf16 a[68:71], a[80:83], v[84:87], a[68:71]\n"
        "s_waitcnt vmcnt(9) lgkmcnt(2)\n"
        "v_exp_f32 v37, v37\n"
        "v_mfma_f32_16x16x32_bf16 v[160:163], a[144:147], v[80:83], v[160:163]\n"
        "s_barrier\n"
        "v_mfma_f32_16x16x32_bf16 v[192:195], a[144:147], v[84:87], v[192:195]\n"
        "ds_read_b128 a[144:147], v241 offset:0\n"
        "v_cvt_pk_bf16_f32 v20, v36, v37\n"
        "v_mfma_f32_16x16x32_bf16 v[196:199], a[148:151], v[84:87], v[196:199]\n"
        "ds_read_b128 a[116:119], v240 offset:13312\n"
        "s_add_u32 s76, s66, s90\n"
        "s_addc_u32 s77, s67, 0\n"
        "v_mfma_f32_16x16x32_bf16 v[164:167], a[148:151], v[80:83], v[164:167]\n"
        "ds_read_b128 a[148:151], v241 offset:1024\n"
        "global_load_lds_dwordx4 v144, s[76:77] offset:0 sc1\n"
        "v_mfma_f32_16x16x32_bf16 v[168:171], a[152:155], v[80:83], v[168:171]\n"
        "v_exp_f32 v38, v38\n"
        "v_mfma_f32_16x16x32_bf16 v[200:203], a[152:155], v[84:87], v[200:203]\n"
        "v_exp_f32 v39, v39\n"
        "v_mfma_f32_16x16x32_bf16 v[204:207], a[156:159], v[84:87], v[204:207]\n"
        "ds_read_b128 a[152:155], v241 offset:2048\n"
        "v_cvt_pk_bf16_f32 v21, v38, v39\n"
        "v_mfma_f32_16x16x32_bf16 v[172:175], a[156:159], v[80:83], v[172:175]\n"
        "ds_read_b128 a[120:123], v240 offset:14336\n"
        "global_load_lds_dwordx4 v145, s[76:77] offset:1024 sc1\n"
        "v_mfma_f32_16x16x32_bf16 v[32:35], a[96:99], v[0:3], v[224:227]\n"
        "ds_read_b128 a[156:159], v241 offset:3072\n"
        "s_cmp_ge_u32 s82, s44\n"
        "s_addc_u32 s90, s82, 0\n"
        "v_mfma_f32_16x16x32_bf16 v[32:35], a[100:103], v[4:7], v[32:35]\n"
        "s_lshl_b32 s90, s90, 15\n"
        "v_exp_f32 v48, v48\n"
        "v_mfma_f32_16x16x32_bf16 v[32:35], a[104:107], v[8:11], v[32:35]\n"
        "v_exp_f32 v49, v49\n"
        "v_mfma_f32_16x16x32_bf16 v[32:35], a[108:111], v[12:15], v[32:35]\n"
        "v_exp_f32 v50, v50\n"
        "v_mfma_f32_16x16x32_bf16 v[36:39], a[108:111], v[76:79], v[228:231]\n"
        "ds_read_b128 a[108:111], v240 offset:19456\n"
        "v_cvt_pk_bf16_f32 v18, v48, v49\n"
        "v_mfma_f32_16x16x32_bf16 v[36:39], a[104:107], v[72:75], v[36:39]\n"
        "ds_read_b128 a[104:107], v240 offset:18432\n"
        "global_load_lds_dwordx4 v146, s[76:77] offset:2048 sc1\n"
        "v_mfma_f32_16x16x32_bf16 v[36:39], a[100:103], v[68:71], v[36:39]\n"
        "v_exp_f32 v51, v51\n"
        "v_mfma_f32_16x16x32_bf16 v[36:39], a[96:99], v[64:67], v[36:39]\n"
        "ds_read_b128 a[96:99], v240 offset:16384\n"
        "v_cvt_pk_bf16_f32 v19, v50, v51\n"
        "v_mfma_f32_16x16x32_bf16 v[176:179], a[128:131], v[80:83], v[176:179]\n"
        "v_exp_f32 v52, v52\n"
        "v_mfma_f32_16x16x32_bf16 v[208:211], a[128:131], v[84:87], v[208:211]\n"
        "ds_read_b128 a[128:131], v241 offset:4096\n"
        "global_load_lds_dwordx4 v147, s[76:77] offset:3072 sc1\n"
        "s_add_u32 m0, s79, s28\n"
        "v_mfma_f32_16x16x32_bf16 v[212:215], a[132:135], v[84:87], v[212:215]\n"
        "v_exp_f32 v53, v53\n"
        "v_mfma_f32_16x16x32_bf16 v[180:183], a[132:135], v[80:83], v[180:183]\n"
        "ds_read_b128 a[100:103], v240 offset:17408\n"
        "v_cvt_pk_bf16_f32 v22, v52, v53\n"
        "v_mfma_f32_16x16x32_bf16 v[184:187], a[136:139], v[80:83], v[184:187]\n"
        "s_waitcnt lgkmcnt(4)\n"
        "ds_read_b128 a[132:135], v241 offset:5120\n"
        "v_mfma_f32_16x16x32_bf16 v[216:219], a[136:139], v[84:87], v[216:219]\n"
        "ds_read_b128 a[136:139], v241 offset:6144\n"
        "s_add_u32 s76, s76, s25\n"
        "s_addc_u32 s77, s77, 0\n"
        "v_mfma_f32_16x16x32_bf16 v[220:223], a[140:143], v[84:87], v[220:223]\n"
        "v_exp_f32 v54, v54\n"
        "v_mfma_f32_16x16x32_bf16 v[188:191], a[140:143], v[80:83], v[188:191]\n"
        "ds_read_b128 a[140:143], v241 offset:7168\n"
        "global_load_lds_dwordx4 v144, s[76:77] offset:0 sc1\n"
        "v_mfma_f32_16x16x32_bf16 v[48:51], a[112:115], v[0:3], v[224:227]\n"
        "v_exp_f32 v55, v55\n"
        "v_mfma_f32_16x16x32_bf16 v[48:51], a[116:119], v[4:7], v[48:51]\n"
        "v_exp_f32 v32, v32\n"
        "v_mfma_f32_16x16x32_bf16 v[48:51], a[120:123], v[8:11], v[48:51]\n"
        "v_exp_f32 v33, v33\n"
        "v_mfma_f32_16x16x32_bf16 v[48:51], a[124:127], v[12:15], v[48:51]\n"
        "v_exp_f32 v34, v34\n"
        "v_mfma_f32_16x16x32_bf16 a[64:67], a[80:83], v[16:19], a[64:67]\n"
        "v_exp_f32 v35, v35\n"
        "v_mfma_f32_16x16x32_bf16 v[160:163], a[144:147], v[16:19], v[160:163]\n"
        "v_exp_f32 v36, v36\n"
        "v_mfma_f32_16x16x32_bf16 v[164:167], a[148:151], v[16:19], v[164:167]\n"
        "v_exp_f32 v37, v37\n"
        "v_mfma_f32_16x16x32_bf16 v[168:171], a[152:155], v[16:19], v[168:171]\n"
        "v_exp_f32 v38, v38\n"
        "v_mfma_f32_16x16x32_bf16 v[172:175], a[156:159], v[16:19], v[172:175]\n"
        "s_waitcnt lgkmcnt(0)\n"
        "v_exp_f32 v39, v39\n"
        "v_mfma_f32_16x16x32_bf16 v[176:179], a[128:131], v[16:19], v[176:179]\n"
        "v_exp_f32 v48, v48\n"
        "v_mfma_f32_16x16x32_bf16 v[180:183], a[132:135], v[16:19], v[180:183]\n"
        "v_exp_f32 v49, v49\n"
        "v_mfma_f32_16x16x32_bf16 v[184:187], a[136:139], v[16:19], v[184:187]\n"
        "v_exp_f32 v50, v50\n"
        "v_mfma_f32_16x16x32_bf16 v[188:191], a[140:143], v[16:19], v[188:191]\n"
        "v_cvt_pk_bf16_f32 v23, v54, v55\n"
        "v_cvt_pk_bf16_f32 v80, v32, v33\n"
        "v_mfma_f32_16x16x32_bf16 v[52:55], a[124:127], v[76:79], v[228:231]\n"
        "ds_read_b128 a[124:127], v240 offset:23552\n"
        "global_load_lds_dwordx4 v145, s[76:77] offset:1024 sc1\n"
        "v_mfma_f32_16x16x32_bf16 v[52:55], a[120:123], v[72:75], v[52:55]\n"
        "ds_read_b128 a[120:123], v240 offset:22528\n"
        "v_cvt_pk_bf16_f32 v81, v34, v35\n"
        "v_mfma_f32_16x16x32_bf16 v[52:55], a[116:119], v[68:71], v[52:55]\n"
        "ds_read_b128 a[116:119], v240 offset:21504\n"
        "v_cvt_pk_bf16_f32 v84, v36, v37\n"
        "v_mfma_f32_16x16x32_bf16 v[52:55], a[112:115], v[64:67], v[52:55]\n"
        "ds_read_b128 a[112:115], v240 offset:20480\n"
        "v_cvt_pk_bf16_f32 v85, v38, v39\n"
        "v_mfma_f32_16x16x32_bf16 a[68:71], a[80:83], v[20:23], a[68:71]\n"
        "v_exp_f32 v51, v51\n"
        "v_mfma_f32_16x16x32_bf16 v[192:195], a[144:147], v[20:23], v[192:195]\n"
        "ds_read_b128 a[144:147], v241 offset:8192\n"
        "global_load_lds_dwordx4 v146, s[76:77] offset:2048 sc1\n"
        "v_mfma_f32_16x16x32_bf16 v[196:199], a[148:151], v[20:23], v[196:199]\n"
        "ds_read_b128 a[148:151], v241 offset:9216\n"
        "v_cvt_pk_bf16_f32 v82, v48, v49\n"
        "v_mfma_f32_16x16x32_bf16 v[200:203], a[152:155], v[20:23], v[200:203]\n"
        "ds_read_b128 a[152:155], v241 offset:10240\n"
        "v_cvt_pk_bf16_f32 v83, v50, v51\n"
        "v_mfma_f32_16x16x32_bf16 v[204:207], a[156:159], v[20:23], v[204:207]\n"
        "ds_read_b128 a[156:159], v241 offset:11264\n"
        "s_add_u32 s68, s64, s90\n"
        "s_addc_u32 s69, s65, 0\n"
        "v_mfma_f32_16x16x32_bf16 v[32:35], a[96:99], v[0:3], v[224:227]\n"
        "v_exp_f32 v52, v52\n"
        "v_mfma_f32_16x16x32_bf16 v[32:35], a[100:103], v[4:7], v[32:35]\n"
        "v_exp_f32 v53, v53\n"
        "v_mfma_f32_16x16x32_bf16 v[32:35], a[104:107], v[8:11], v[32:35]\n"
        "v_exp_f32 v54, v54\n"
        "v_mfma_f32_16x16x32_bf16 v[32:35], a[108:111], v[12:15], v[32:35]\n"
        "v_exp_f32 v55, v55\n"
        "v_mfma_f32_16x16x32_bf16 v[36:39], a[108:111], v[76:79], v[228:231]\n"
        "ds_read_b128 a[108:111], v240 offset:27648\n"
        "global_load_lds_dwordx4 v147, s[76:77] offset:3072 sc1\n"
        "v_mfma_f32_16x16x32_bf16 v[36:39], a[104:107], v[72:75], v[36:39]\n"
        "ds_read_b128 a[104:107], v240 offset:26624\n"
        "v_cvt_pk_bf16_f32 v86, v52, v53\n"
        "v_mfma_f32_16x16x32_bf16 v[36:39], a[100:103], v[68:71], v[36:39]\n"
        "ds_read_b128 a[100:103], v240 offset:25600\n"
        "v_cvt_pk_bf16_f32 v87, v54, v55\n"
        "v_mfma_f32_16x16x32_bf16 v[36:39], a[96:99], v[64:67], v[36:39]\n"
        "ds_read_b128 a[96:99], v240 offset:24576\n"
        "s_add_u32 s74, s68, 0x1000\n"
        "s_addc_u32 s75, s69, 0\n"
        "v_mfma_f32_16x16x32_bf16 v[208:211], a[128:131], v[20:23], v[208:211]\n"
        "ds_read_b128 a[128:131], v241 offset:12288\n"
        "global_load_dword v244, v245, s[26:27] offset:0\n"
        "v_mfma_f32_16x16x32_bf16 v[212:215], a[132:135], v[20:23], v[212:215]\n"
        "s_waitcnt lgkmcnt(5)\n"
        "ds_read_b128 a[132:135], v241 offset:13312\n"
        "v_mfma_f32_16x16x32_bf16 v[216:219], a[136:139], v[20:23], v[216:219]\n"
        "ds_read_b128 a[136:139], v241 offset:14336\n"
        "s_add_u32 s26, s26, 0x20000\n"
        "s_addc_u32 s27, s27, 0\n"
        "v_mfma_f32_16x16x32_bf16 v[220:223], a[140:143], v[20:23], v[220:223]\n"
        "ds_read_b128 a[140:143], v241 offset:15360\n"
        "v_mfma_f32_16x16x32_bf16 v[48:51], a[112:115], v[0:3], v[224:227]\n"
        "v_exp_f32 v32, v32\n"
        "v_mfma_f32_16x16x32_bf16 v[48:51], a[116:119], v[4:7], v[48:51]\n"
        "v_exp_f32 v33, v33\n"
        "v_mfma_f32_16x16x32_bf16 v[48:51], a[120:123], v[8:11], v[48:51]\n"
        "v_exp_f32 v34, v34\n"
        "v_mfma_f32_16x16x32_bf16 v[48:51], a[124:127], v[12:15], v[48:51]\n"
        "v_exp_f32 v35, v35\n"
        "v_mfma_f32_16x16x32_bf16 v[52:55], a[124:127], v[76:79], v[228:231]\n"
        "ds_read_b128 a[124:127], v240 offset:31744\n"
        "v_cvt_pk_bf16_f32 v16, v32, v33\n"
        "v_mfma_f32_16x16x32_bf16 v[52:55], a[120:123], v[72:75], v[52:55]\n"
        "ds_read_b128 a[120:123], v240 offset:30720\n"
        "v_cvt_pk_bf16_f32 v17, v34, v35\n"
        "v_mfma_f32_16x16x32_bf16 v[52:55], a[116:119], v[68:71], v[52:55]\n"
        "v_exp_f32 v36, v36\n"
        "v_mfma_f32_16x16x32_bf16 v[52:55], a[112:115], v[64:67], v[52:55]\n"
        "ds_read_b128 a[112:115], v240 offset:28672\n"
        "v_mfma_f32_16x16x32_bf16 a[64:67], a[80:83], v[80:83], a[64:67]\n"
        "v_exp_f32 v37, v37\n"
        "v_mfma_f32_16x16x32_bf16 a[68:71], a[80:83], v[84:87], a[68:71]\n"
        "ds_read_b128 a[116:119], v240 offset:29696\n"
        "v_cvt_pk_bf16_f32 v20, v36, v37\n"
        "v_mfma_f32_16x16x32_bf16 v[160:163], a[144:147], v[80:83], v[160:163]\n"
        "v_exp_f32 v38, v38\n"
        "v_mfma_f32_16x16x32_bf16 v[192:195], a[144:147], v[84:87], v[192:195]\n"
        "ds_read_b128 a[144:147], v241 offset:16384\n"
        "v_xor_b32 v240, 0x8000, v240\n"
        "v_mfma_f32_16x16x32_bf16 v[196:199], a[148:151], v[84:87], v[196:199]\n"
        "v_exp_f32 v39, v39\n"
        "v_mfma_f32_16x16x32_bf16 v[164:167], a[148:151], v[80:83], v[164:167]\n"
        "ds_read_b128 a[148:151], v241 offset:17408\n"
        "v_cvt_pk_bf16_f32 v21, v38, v39\n"
        "v_mfma_f32_16x16x32_bf16 v[168:171], a[152:155], v[80:83], v[168:171]\n"
        "s_waitcnt lgkmcnt(4)\n"
        "v_exp_f32 v48, v48\n"
        "v_mfma_f32_16x16x32_bf16 v[200:203], a[152:155], v[84:87], v[200:203]\n"
        "ds_read_b128 a[152:155], v241 offset:18432\n"
        "v_mfma_f32_16x16x32_bf16 v[204:207], a[156:159], v[84:87], v[204:207]\n"
        "v_exp_f32 v49, v49\n"
        "v_mfma_f32_16x16x32_bf16 v[172:175], a[156:159], v[80:83], v[172:175]\n"
        "ds_read_b128 a[156:159], v241 offset:19456\n"
        "v_cvt_pk_bf16_f32 v18, v48, v49\n"
        "v_mfma_f32_16x16x32_bf16 v[32:35], a[96:99], v[0:3], v[224:227]\n"
        "s_waitcnt vmcnt(9) lgkmcnt(4)\n"
        "v_exp_f32 v50, v50\n"
        "v_mfma_f32_16x16x32_bf16 v[32:35], a[100:103], v[4:7], v[32:35]\n"
        "s_barrier\n"
        "v_mfma_f32_16x16x32_bf16 v[32:35], a[104:107], v[8:11], v[32:35]\n"
        "v_exp_f32 v51, v51\n"
        "v_mfma_f32_16x16x32_bf16 v[32:35], a[108:111], v[12:15], v[32:35]\n"
        "v_exp_f32 v52, v52\n"
        "v_mfma_f32_16x16x32_bf16 v[36:39], a[108:111], v[76:79], v[228:231]\n"
        "ds_read_b128 a[108:111], v240 offset:3072\n"
        "v_cvt_pk_bf16_f32 v19, v50, v51\n"
        "v_mfma_f32_16x16x32_bf16 v[36:39], a[104:107], v[72:75], v[36:39]\n"
        "ds_read_b128 a[104:107], v240 offset:2048\n"
        "s_cmp_eq_u32 s82, s24\n"
        "s_cbranch_scc1 LEVS0_%=\n"
        "LEWS0_%=:\n"
        "v_mfma_f32_16x16x32_bf16 v[36:39], a[100:103], v[68:71], v[36:39]\n"
        "s_add_u32 m0, s80, 0x0\n"
        "ds_read_b128 a[100:103], v240 offset:1024\n"
        "v_mfma_f32_16x16x32_bf16 v[36:39], a[96:99], v[64:67], v[36:39]\n"
        "ds_read_b128 a[96:99], v240 offset:0\n"
        "global_load_lds_dwordx4 v254, s[68:69] offset:0 sc1\n"
        "v_mfma_f32_16x16x32_bf16 v[176:179], a[128:131], v[80:83], v[176:179]\n"
        "v_exp_f32 v53, v53\n"
        "v_mfma_f32_16x16x32_bf16 v[208:211], a[128:131], v[84:87], v[208:211]\n"
        "ds_read_b128 a[128:131], v241 offset:20480\n"
        "v_cvt_pk_bf16_f32 v22, v52, v53\n"
        "v_mfma_f32_16x16x32_bf16 v[212:215], a[132:135], v[84:87], v[212:215]\n"
        "v_exp_f32 v54, v54\n"
        "v_mfma_f32_16x16x32_bf16 v[180:183], a[132:135], v[80:83], v[180:183]\n"
        "ds_read_b128 a[132:135], v241 offset:21504\n"
        "global_load_lds_dwordx4 v254, s[68:69] offset:1024 sc1\n"
        "v_mfma_f32_16x16x32_bf16 v[184:187], a[136:139], v[80:83], v[184:187]\n"
        "v_exp_f32 v55, v55\n"
        "v_mfma_f32_16x16x32_bf16 v[216:219], a[136:139], v[84:87], v[216:219]\n"
        "ds_read_b128 a[136:139], v241 offset:22528\n"
        "v_cvt_pk_bf16_f32 v23, v54, v55\n"
        "v_mfma_f32_16x16x32_bf16 v[220:223], a[140:143], v[84:87], v[220:223]\n"
        "v_exp_f32 v32, v32\n"
        "v_mfma_f32_16x16x32_bf16 v[188:191], a[140:143], v[80:83], v[188:191]\n"
        "ds_read_b128 a[140:143], v241 offset:23552\n"
        "global_load_lds_dwordx4 v254, s[68:69] offset:2048 sc1\n"
        "v_mfma_f32_16x16x32_bf16 v[48:51], a[112:115], v[0:3], v[224:227]\n"
        "v_exp_f32 v33, v33\n"
        "v_mfma_f32_16x16x32_bf16 v[48:51], a[116:119], v[4:7], v[48:51]\n"
        "s_waitcnt lgkmcnt(3)\n"
        "v_exp_f32 v34, v34\n"
        "v_mfma_f32_16x16x32_bf16 v[48:51], a[120:123], v[8:11], v[48:51]\n"
        "v_exp_f32 v35, v35\n"
        "v_mfma_f32_16x16x32_bf16 v[48:51], a[124:127], v[12:15], v[48:51]\n"
        "v_exp_f32 v36, v36\n"
        "v_mfma_f32_16x16x32_bf16 v[52:55], a[124:127], v[76:79], v[228:231]\n"
        "v_exp_f32 v37, v37\n"
        "v_mfma_f32_16x16x32_bf16 v[52:55], a[120:123], v[72:75], v[52:55]\n"
        "ds_read_b128 a[120:123], v240 offset:6144\n"
        "v_cvt_pk_bf16_f32 v80, v32, v33\n"
        "v_mfma_f32_16x16x32_bf16 v[52:55], a[116:119], v[68:71], v[52:55]\n"
        "ds_read_b128 a[116:119], v240 offset:5120\n"
        "global_load_lds_dwordx4 v254, s[68:69] offset:3072 sc1\n"
        "v_mfma_f32_16x16x32_bf16 v[52:55], a[112:115], v[64:67], v[52:55]\n"
        "ds_read_b128 a[112:115], v240 offset:4096\n"
        "v_cvt_pk_bf16_f32 v81, v34, v35\n"
        "v_mfma_f32_16x16x32_bf16 a[64:67], a[80:83], v[16:19], a[64:67]\n"
        "v_exp_f32 v38, v38\n"
        "v_mfma_f32_16x16x32_bf16 a[68:71], a[80:83], v[20:23], a[68:71]\n"
        "v_exp_f32 v39, v39\n"
        "v_mfma_f32_16x16x32_bf16 v[160:163], a[144:147], v[16:19], v[160:163]\n"
        "v_cvt_pk_bf16_f32 v84, v36, v37\n"
        "v_cvt_pk_bf16_f32 v85, v38, v39\n"
        "v_mfma_f32_16x16x32_bf16 v[192:195], a[144:147], v[20:23], v[192:195]\n"
        "ds_read_b128 a[144:147], v241 offset:24576\n"
        "v_mfma_f32_16x16x32_bf16 v[196:199], a[148:151], v[20:23], v[196:199]\n"
        "v_mfma_f32_16x16x32_bf16 v[164:167], a[148:151], v[16:19], v[164:167]\n"
        "ds_read_b128 a[148:151], v241 offset:25600\n"
        "v_mfma_f32_16x16x32_bf16 v[168:171], a[152:155], v[16:19], v[168:171]\n"
        "v_mfma_f32_16x16x32_bf16 v[200:203], a[152:155], v[20:23], v[200:203]\n"
        "ds_read_b128 a[152:155], v241 offset:26624\n"
        "v_mfma_f32_16x16x32_bf16 v[204:207], a[156:159], v[20:23], v[204:207]\n"
        "s_cmp_lg_u32 s91, 0\n"
        "v_mfma_f32_16x16x32_bf16 v[172:175], a[156:159], v[16:19], v[172:175]\n"
        "s_cbranch_scc1 LSI_%=\n"
        "s_branch LTL_%=\n"
        "LEVS0_%=:\n"
        "s_lshl_b32 s97, s41, 12\n"
        "s_add_u32 s97, s97, 32768\n"
        "v_bfe_u32 v246, v254, 4, 5\n"
        "v_lshl_add_u32 v246, v246, 7, s97\n"
        "s_cmp_eq_u32 s82, s83\n"
        "s_cbranch_scc1 LE3S0_%=\n"
        "global_load_dwordx2 v[252:253], v246, s[48:49] sc1\n"
        "s_mov_b32 s24, s83\n"
        "v_accvgpr_read_b32 v246, a93\n"
        "LHC1_%=:\n"
        "s_cbranch_execz LHRC_%=\n"
        "global_load_dword v244, v246, s[46:47]\n"
        "s_branch LEWS0_%=\n"
        "LE3S0_%=:\n"
        "s_mov_b32 s97, 0x200000\n"
        "LE4S0_%=:\n"
        "v_cmp_ne_u64 vcc, v[252:253], v[250:251]\n"
        "s_nop 1\n"
        "s_cbranch_vccz LE5S0_%=\n"
        "s_sleep 2\n"
        "global_load_dwordx2 v[252:253], v246, s[48:49] sc1\n"
        "s_waitcnt vmcnt(0)\n"
        "s_sub_u32 s97, s97, 1\n"
        "s_cmp_lg_u32 s97, 0\n"
        "s_cbranch_scc1 LE4S0_%=\n"
        "LE5S0_%=:\n"
        "s_mov_b64 s[68:69], s[92:93]\n"
        "s_mov_b64 s[74:75], s[94:95]\n"
        "s_mov_b32 s80, s50\n"
        "s_mov_b32 s81, s51\n"
        "s_mov_b64 s[66:67], s[22:23]\n"
        "s_mov_b32 s90, s29\n"
        "s_mov_b32 s25, s30\n"
        "s_mov_b32 s28, 16384\n"
        "s_xor_b32 s79, s73, 0x8000\n"
        "v_accvgpr_read_b32 v144, a84\n"
        "v_accvgpr_read_b32 v145, a85\n"
        "v_accvgpr_read_b32 v146, a86\n"
        "v_accvgpr_read_b32 v147, a87\n"
        "v_accvgpr_read_b32 v148, a88\n"
        "v_accvgpr_read_b32 v152, a89\n"
        "v_accvgpr_read_b32 v252, a90\n"
        "v_accvgpr_read_b32 v249, a91\n"
        "v_accvgpr_read_b32 v153, a92\n"
        "v_mov_b32 v246, 0\n"
        "v_mov_b32 v247, 0\n"
        "v_mov_b32 v248, 0\n"
        "v_add_u32 v149, 1024, v148\n"
        "v_add_u32 v150, 2048, v148\n"
        "v_add_u32 v151, 3072, v148\n"
        "s_mov_b64 exec, 15\n"
        "ds_write_b128 v252, v[246:249]\n"
        "s_mov_b64 exec, -1\n"
        "s_branch LEWS0_%=\n"
        "; ---- BACK\n"
        "LTL_%=:\n"
        "v_mfma_f32_16x16x32_bf16 v[176:179], a[128:131], v[16:19], v[176:179]\n"
        "s_waitcnt lgkmcnt(4)\n"
        "ds_read_b128 a[156:159], v241 offset:27648\n"
        "s_mul_i32 s53, s45, 1052672\n"
        "v_mfma_f32_16x16x32_bf16 v[208:211], a[128:131], v[20:23], v[208:211]\n"
        "s_lshl_b32 s96, s43, 15\n"
        "v_exp_f32 v48, v48\n"
        "v_mfma_f32_16x16x32_bf16 v[212:215], a[132:135], v[20:23], v[212:215]\n"
        "s_add_u32 s53, s53, s96\n"
        "v_exp_f32 v49, v49\n"
        "v_mfma_f32_16x16x32_bf16 v[180:183], a[132:135], v[16:19], v[180:183]\n"
        "ds_read_b128 a[128:131], v241 offset:28672\n"
        "s_lshl_b32 s96, s40, 10\n"
        "v_cvt_pk_bf16_f32 v82, v48, v49\n"
        "v_mfma_f32_16x16x32_bf16 v[184:187], a[136:139], v[16:19], v[184:187]\n"
        "s_add_u32 s53, s53, s96\n"
        "v_exp_f32 v50, v50\n"
        "v_mfma_f32_16x16x32_bf16 v[216:219], a[136:139], v[20:23], v[216:219]\n"
        "s_add_u32 s53, s53, 0x2201000\n"
        "v_exp_f32 v51, v51\n"
        "v_mfma_f32_16x16x32_bf16 v[220:223], a[140:143], v[20:23], v[220:223]\n"
        "ds_read_b128 a[132:135], v241 offset:29696\n"
        "s_add_u32 s16, s51, 4096\n"
        "v_cvt_pk_bf16_f32 v83, v50, v51\n"
        "v_mfma_f32_16x16x32_bf16 v[188:191], a[140:143], v[16:19], v[188:191]\n"
        "s_waitcnt lgkmcnt(3)\n"
        "v_exp_f32 v52, v52\n"
        "v_mfma_f32_16x16x32_bf16 a[64:67], a[80:83], v[80:83], a[64:67]\n"
        "s_add_u32 m0, s73, 0x8000\n"
        "v_exp_f32 v53, v53\n"
        "v_mfma_f32_16x16x32_bf16 v[160:163], a[144:147], v[80:83], v[160:163]\n"
        "ds_read_b128 a[136:139], v241 offset:30720\n"
        "s_xor_b32 s66, s29, 0x40000\n"
        "v_cvt_pk_bf16_f32 v86, v52, v53\n"
        "v_mfma_f32_16x16x32_bf16 v[164:167], a[148:151], v[80:83], v[164:167]\n"
        "s_lshl_b32 s14, s44, 5\n"
        "v_exp_f32 v54, v54\n"
        "v_mfma_f32_16x16x32_bf16 v[168:171], a[152:155], v[80:83], v[168:171]\n"
        "s_waitcnt lgkmcnt(2)\n"
        "v_exp_f32 v55, v55\n"
        "v_mfma_f32_16x16x32_bf16 v[172:175], a[156:159], v[80:83], v[172:175]\n"
        "ds_read_b128 a[140:143], v241 offset:31744\n"
        "s_add_u32 s14, s14, s43\n"
        "v_cvt_pk_bf16_f32 v87, v54, v55\n"
        "s_nop 1\n"
        "v_mfma_f32_16x16x32_bf16 a[68:71], a[80:83], v[84:87], a[68:71]\n"
        "ds_read2_b32 a[0:1], v148 offset0:0 offset1:1\n"
        "s_lshl2_add_u32 s14, s14, s40\n"
        "v_accvgpr_read_b32 v248, a64\n"
        "v_mfma_f32_16x16x32_bf16 v[192:195], a[144:147], v[84:87], v[192:195]\n"
        "s_lshl_b32 s14, s14, 8\n"
        "v_rcp_f32 v246, v248\n"
        "v_mfma_f32_16x16x32_bf16 v[196:199], a[148:151], v[84:87], v[196:199]\n"
        "s_add_u32 s70, s38, s53\n"
        "s_addc_u32 s71, s39, 0\n"
        "v_accvgpr_read_b32 v249, a68\n"
        "v_mfma_f32_16x16x32_bf16 v[200:203], a[152:155], v[84:87], v[200:203]\n"
        "s_waitcnt lgkmcnt(3)\n"
        "v_rcp_f32 v247, v249\n"
        "v_mfma_f32_16x16x32_bf16 v[204:207], a[156:159], v[84:87], v[204:207]\n"
        "ds_read2_b32 a[2:3], v148 offset0:2 offset1:3\n"
        "s_add_u32 s14, s14, 131072\n"
        "v_mul_f32 v192, v192, v247\n"
        "v_mfma_f32_16x16x32_bf16 v[208:211], a[128:131], v[84:87], v[208:211]\n"
        "s_waitcnt lgkmcnt(3)\n"
        "v_mul_f32 v193, v193, v247\n"
        "v_mul_f32 v194, v194, v247\n"
        "v_mfma_f32_16x16x32_bf16 v[212:215], a[132:135], v[84:87], v[212:215]\n"
        "ds_read2_b32 a[4:5], v148 offset0:128 offset1:129\n"
        "s_sub_u32 s62, 15, s12\n"
        "v_mul_f32 v195, v195, v247\n"
        "v_mfma_f32_16x16x32_bf16 v[176:179], a[128:131], v[80:83], v[176:179]\n"
        "s_waitcnt lgkmcnt(3)\n"
        "v_mul_f32 v196, v196, v247\n"
        "v_mul_f32 v197, v197, v247\n"
        "v_mfma_f32_16x16x32_bf16 v[180:183], a[132:135], v[80:83], v[180:183]\n"
        "ds_read2_b32 a[6:7], v148 offset0:130 offset1:131\n"
        "s_lshl2_add_u32 s63, s11, s40\n"
        "v_mul_f32 v198, v198, v247\n"
        "v_mfma_f32_16x16x32_bf16 v[216:219], a[136:139], v[84:87], v[216:219]\n"
        "ds_read_b128 a[196:199], v152 offset:3072\n"
        "s_lshl_b32 s63, s63, 5\n"
        "v_mul_f32 v199, v199, v247\n"
        "v_mfma_f32_16x16x32_bf16 v[220:223], a[140:143], v[84:87], v[220:223]\n"
        "ds_read2_b32 a[8:9], v149 offset0:0 offset1:1\n"
        "v_cvt_pk_bf16_f32 v192, v192, v193\n"
        "v_mfma_f32_16x16x32_bf16 v[184:187], a[136:139], v[80:83], v[184:187]\n"
        "ds_read2_b32 a[10:11], v149 offset0:2 offset1:3\n"
        "v_cvt_pk_bf16_f32 v193, v194, v195\n"
        "v_mfma_f32_16x16x32_bf16 v[188:191], a[140:143], v[80:83], v[188:191]\n"
        "ds_read_b128 a[200:203], v152 offset:2560\n"
        "ds_read2_b32 a[12:13], v149 offset0:128 offset1:129\n"
        "ds_read2_b32 a[14:15], v149 offset0:130 offset1:131\n"
        "ds_read_b128 a[204:207], v152 offset:2048\n"
        "ds_read2_b32 a[16:17], v150 offset0:0 offset1:1\n"
        "ds_read2_b32 a[18:19], v150 offset0:2 offset1:3\n"
        "ds_read_b128 a[208:211], v152 offset:1536\n"
        "ds_read2_b32 a[20:21], v150 offset0:128 offset1:129\n"
        "ds_read2_b32 a[22:23], v150 offset0:130 offset1:131\n"
        "v_cvt_pk_bf16_f32 v194, v196, v197\n"
        "s_waitcnt lgkmcnt(11)\n"
        "v_cvt_pk_bf16_f32 v195, v198, v199\n"
        "global_store_dwordx4 v254, v[192:195], s[70:71] offset:-4096 sc1\n"
        "ds_read_b128 a[212:215], v152 offset:1024\n"
        "ds_read2_b32 a[24:25], v151 offset0:0 offset1:1\n"
        "v_mul_f32 v160, v160, v246\n"
        "v_mul_f32 v161, v161, v246\n"
        "v_mul_f32 v162, v162, v246\n"
        "v_mul_f32 v163, v163, v246\n"
        "v_mul_f32 v164, v164, v246\n"
        "v_mul_f32 v165, v165, v246\n"
        "v_mul_f32 v166, v166, v246\n"
        "v_mul_f32 v167, v167, v246\n"
        "v_cvt_pk_bf16_f32 v160, v160, v161\n"
        "v_cvt_pk_bf16_f32 v161, v162, v163\n"
        "v_cvt_pk_bf16_f32 v162, v164, v165\n"
        "v_cvt_pk_bf16_f32 v163, v166, v167\n"
        "global_store_dwordx4 v254, v[160:163], s[70:71] offset:0 sc1\n"
        "s_add_u32 s70, s70, 0x2000\n"
        "s_addc_u32 s71, s71, 0\n"
        "ds_read2_b32 a[26:27], v151 offset0:2 offset1:3\n"
        "v_mul_f32 v200, v200, v247\n"
        "v_mul_f32 v201, v201, v247\n"
        "v_mul_f32 v202, v202, v247\n"
        "v_mul_f32 v203, v203, v247\n"
        "v_mul_f32 v204, v204, v247\n"
        "v_mul_f32 v205, v205, v247\n"
        "v_mul_f32 v206, v206, v247\n"
        "v_mul_f32 v207, v207, v247\n"
        "v_cvt_pk_bf16_f32 v200, v200, v201\n"
        "v_cvt_pk_bf16_f32 v201, v202, v203\n"
        "v_cvt_pk_bf16_f32 v202, v204, v205\n"
        "v_cvt_pk_bf16_f32 v203, v206, v207\n"
        "global_store_dwordx4 v254, v[200:203], s[70:71] offset:-4096 sc1\n"
        "ds_read_b128 a[216:219], v152 offset:512\n"
        "ds_read2_b32 a[28:29], v151 offset0:128 offset1:129\n"
        "v_mul_f32 v168, v168, v246\n"
        "v_mul_f32 v169, v169, v246\n"
        "v_mul_f32 v170, v170, v246\n"
        "v_mul_f32 v171, v171, v246\n"
        "v_mul_f32 v172, v172, v246\n"
        "v_mul_f32 v173, v173, v246\n"
        "v_mul_f32 v174, v174, v246\n"
        "v_mul_f32 v175, v175, v246\n"
        "v_cvt_pk_bf16_f32 v168, v168, v169\n"
        "v_cvt_pk_bf16_f32 v169, v170, v171\n"
        "v_cvt_pk_bf16_f32 v170, v172, v173\n"
        "v_cvt_pk_bf16_f32 v171, v174, v175\n"
        "global_store_dwordx4 v254, v[168:171], s[70:71] offset:0 sc1\n"
        "s_add_u32 s70, s70, 0x2000\n"
        "s_addc_u32 s71, s71, 0\n"
        "ds_read2_b32 a[30:31], v151 offset0:130 offset1:131\n"
        "v_mul_f32 v208, v208, v247\n"
        "v_mul_f32 v209, v209, v247\n"
        "v_mul_f32 v210, v210, v247\n"
        "v_mul_f32 v211, v211, v247\n"
        "v_mul_f32 v212, v212, v247\n"
        "v_mul_f32 v213, v213, v247\n"
        "v_mul_f32 v214, v214, v247\n"
        "v_mul_f32 v215, v215, v247\n"
        "v_cvt_pk_bf16_f32 v208, v208, v209\n"
        "v_cvt_pk_bf16_f32 v209, v210, v211\n"
        "v_cvt_pk_bf16_f32 v210, v212, v213\n"
        "v_cvt_pk_bf16_f32 v211, v214, v215\n"
        "global_store_dwordx4 v254, v[208:211], s[70:71] offset:-4096 sc1\n"
        "ds_read_b128 a[220:223], v152 offset:0\n"
        "ds_read2_b32 a[32:33], v148 offset0:16 offset1:17\n"
        "v_mul_f32 v176, v176, v246\n"
        "v_mul_f32 v177, v177, v246\n"
        "v_mul_f32 v178, v178, v246\n"
        "v_mul_f32 v179, v179, v246\n"
        "v_mul_f32 v180, v180, v246\n"
        "v_mul_f32 v181, v181, v246\n"
        "v_mul_f32 v182, v182, v246\n"
        "v_mul_f32 v183, v183, v246\n"
        "v_cvt_pk_bf16_f32 v176, v176, v177\n"
        "v_cvt_pk_bf16_f32 v177, v178, v179\n"
        "v_cvt_pk_bf16_f32 v178, v180, v181\n"
        "v_cvt_pk_bf16_f32 v179, v182, v183\n"
        "global_store_dwordx4 v254, v[176:179], s[70:71] offset:0 sc1\n"
        "s_add_u32 s70, s70, 0x2000\n"
        "s_addc_u32 s71, s71, 0\n"
        "ds_read2_b32 a[34:35], v148 offset0:18 offset1:19\n"
        "v_add_u32 v230, 3584, v152\n"
        "v_mfma_f32_16x16x32_bf16 v[100:103], a[0:3], a[196:199], 0\n"
        "s_waitcnt lgkmcnt(3)\n"
        "v_min_u32 v230, s16, v230\n"
        "v_mul_f32 v216, v216, v247\n"
        "v_mul_f32 v217, v217, v247\n"
        "v_mul_f32 v218, v218, v247\n"
        "v_mul_f32 v219, v219, v247\n"
        "v_mul_f32 v220, v220, v247\n"
        "v_mul_f32 v221, v221, v247\n"
        "v_mul_f32 v222, v222, v247\n"
        "v_mul_f32 v223, v223, v247\n"
        "v_cvt_pk_bf16_f32 v216, v216, v217\n"
        "v_cvt_pk_bf16_f32 v217, v218, v219\n"
        "v_cvt_pk_bf16_f32 v218, v220, v221\n"
        "v_cvt_pk_bf16_f32 v219, v222, v223\n"
        "global_store_dwordx4 v254, v[216:219], s[70:71] offset:-4096 sc1\n"
        "ds_read_b128 a[192:195], v230 offset:0\n"
        "ds_read2_b32 a[36:37], v148 offset0:144 offset1:145\n"
        "v_mul_f32 v184, v184, v246\n"
        "v_mul_f32 v185, v185, v246\n"
        "v_mul_f32 v186, v186, v246\n"
        "v_mul_f32 v187, v187, v246\n"
        "v_mul_f32 v188, v188, v246\n"
        "v_mul_f32 v189, v189, v246\n"
        "v_mul_f32 v190, v190, v246\n"
        "v_mul_f32 v191, v191, v246\n"
        "v_cvt_pk_bf16_f32 v184, v184, v185\n"
        "v_cvt_pk_bf16_f32 v185, v186, v187\n"
        "v_cvt_pk_bf16_f32 v186, v188, v189\n"
        "v_cvt_pk_bf16_f32 v187, v190, v191\n"
        "global_store_dwordx4 v254, v[184:187], s[70:71] offset:0 sc1\n"
        "ds_read2_b32 a[38:39], v148 offset0:146 offset1:147\n"
        "ds_read_b128 a[228:231], v152 offset:3136\n"
        "v_add_f32 v247, v248, v249\n"
        "v_mfma_f32_16x16x32_bf16 v[108:111], a[0:3], a[204:207], 0\n"
        "ds_read2_b32 a[40:41], v149 offset0:16 offset1:17\n"
        "v_cmp_nge_f32 vcc, 0x4e6e6b28, v247\n"
        "v_mfma_f32_16x16x32_bf16 v[108:111], a[4:7], a[200:203], v[108:111]\n"
        "s_cbranch_vccnz LRH_%=\n"
        "LSH_%=:\n"
        "v_add_u32 v231, 3648, v152\n"
        "v_min_u32 v231, s16, v231\n"
        "v_mfma_f32_16x16x32_bf16 v[108:111], a[8:11], a[196:199], v[108:111]\n"
        "ds_read_b128 a[224:227], v231 offset:0\n"
        "s_waitcnt lgkmcnt(5)\n"
        "v_mfma_f32_16x16x32_bf16 v[96:99], a[0:3], a[192:195], 0\n"
        "ds_read2_b32 a[42:43], v149 offset0:18 offset1:19\n"
        "v_xor_b32 v154, 32, v153\n"
        "v_mfma_f32_16x16x32_bf16 v[100:103], a[4:7], a[192:195], v[100:103]\n"
        "ds_read_b128 a[232:235], v152 offset:2624\n"
        "v_add_u32 v155, 0x10000, v153\n"
        "v_mfma_f32_16x16x32_bf16 v[104:107], a[8:11], a[192:195], 0\n"
        "ds_read2_b32 a[44:45], v149 offset0:144 offset1:145\n"
        "v_add_u32 v156, 0x10000, v154\n"
        "v_mfma_f32_16x16x32_bf16 v[104:107], a[4:7], a[196:199], v[104:107]\n"
        "ds_read2_b32 a[46:47], v149 offset0:146 offset1:147\n"
        "v_add_u32 v157, 0x20000, v153\n"
        "v_mfma_f32_16x16x32_bf16 v[104:107], a[0:3], a[200:203], v[104:107]\n"
        "ds_read_b128 a[236:239], v152 offset:2112\n"
        "v_add_u32 v158, 0x20000, v154\n"
        "v_mfma_f32_16x16x32_bf16 v[108:111], a[12:15], a[192:195], v[108:111]\n"
        "ds_read2_b32 a[48:49], v150 offset0:16 offset1:17\n"
        "s_add_u32 s60, s22, s66\n"
        "s_addc_u32 s61, s23, 0\n"
        "v_mfma_f32_16x16x32_bf16 v[128:131], a[16:19], a[192:195], 0\n"
        "ds_read2_b32 a[50:51], v150 offset0:18 offset1:19\n"
        "s_xor_b32 s66, s29, 0x60000\n"
        "v_add_u32 v230, 3712, v152\n"
        "v_mfma_f32_16x16x32_bf16 v[128:131], a[12:15], a[196:199], v[128:131]\n"
        "ds_read_b128 a[240:243], v152 offset:1600\n"
        "v_min_u32 v230, s16, v230\n"
        "v_mfma_f32_16x16x32_bf16 v[128:131], a[8:11], a[200:203], v[128:131]\n"
        "ds_read2_b32 a[52:53], v150 offset0:144 offset1:145\n"
        "v_bfe_u32 v232, v254, 4, 4\n"
        "v_mfma_f32_16x16x32_bf16 v[128:131], a[4:7], a[204:207], v[128:131]\n"
        "ds_read2_b32 a[54:55], v150 offset0:146 offset1:147\n"
        "v_lshrrev_b32 v233, 8, v254\n"
        "v_mfma_f32_16x16x32_bf16 v[128:131], a[0:3], a[208:211], v[128:131]\n"
        "ds_read_b128 a[244:247], v152 offset:1088\n"
        "v_and_b32 v234, 0x70, v254\n"
        "v_mfma_f32_16x16x32_bf16 v[132:135], a[0:3], a[212:215], 0\n"
        "ds_read2_b32 a[56:57], v151 offset0:16 offset1:17\n"
        "v_lshrrev_b32 v234, 1, v234\n"
        "v_mfma_f32_16x16x32_bf16 v[132:135], a[4:7], a[208:211], v[132:135]\n"
        "ds_read2_b32 a[58:59], v151 offset0:18 offset1:19\n"
        "v_and_b32 v235, 0x80, v254\n"
        "v_mfma_f32_16x16x32_bf16 v[132:135], a[8:11], a[204:207], v[132:135]\n"
        "ds_read_b128 a[248:251], v152 offset:576\n"
        "v_add3_u32 v234, v234, v235, s14\n"
        "v_mfma_f32_16x16x32_bf16 v[132:135], a[12:15], a[200:203], v[132:135]\n"
        "ds_read2_b32 a[60:61], v151 offset0:144 offset1:145\n"
        "s_and_b32 s14, s10, 1\n"
        "v_add_u32 v231, 3776, v152\n"
        "v_mfma_f32_16x16x32_bf16 v[132:135], a[16:19], a[196:199], v[132:135]\n"
        "ds_read2_b32 a[62:63], v151 offset0:146 offset1:147\n"
        "s_waitcnt vmcnt(8)\n"
        "v_mfma_f32_16x16x32_bf16 v[132:135], a[20:23], a[192:195], v[132:135]\n"
        "s_barrier\n"
        "v_mfma_f32_16x16x32_bf16 v[136:139], a[24:27], a[192:195], 0\n"
        "ds_read_b128 a[252:255], v152 offset:64\n"
        "v_min_u32 v231, s16, v231\n"
        "v_mfma_f32_16x16x32_bf16 v[136:139], a[20:23], a[196:199], v[136:139]\n"
        "ds_read_b64_tr_b16 a[96:97], v153 offset:0\n"
        "v_add_u32 v244, 0x10000, v254\n"
        "v_mfma_f32_16x16x32_bf16 v[136:139], a[16:19], a[200:203], v[136:139]\n"
        "ds_read_b64_tr_b16 a[98:99], v153 offset:4096\n"
        "v_add_u32 v245, 0x20000, v254\n"
        "v_mfma_f32_16x16x32_bf16 v[136:139], a[12:15], a[204:207], v[136:139]\n"
        "ds_read_b64_tr_b16 a[100:101], v154 offset:0\n"
        "s_cmp_le_u32 s10, 0\n"
        "s_addc_u32 s74, 0, 0\n"
        "v_mfma_f32_16x16x32_bf16 v[136:139], a[8:11], a[208:211], v[136:139]\n"
        "ds_read_b64_tr_b16 a[102:103], v154 offset:4096\n"
        "s_cmp_le_u32 s10, 1\n"
        "s_addc_u32 s75, 1, 0\n"
        "v_mfma_f32_16x16x32_bf16 v[136:139], a[4:7], a[212:215], v[136:139]\n"
        "ds_read_b64_tr_b16 a[104:105], v153 offset:8192\n"
        "s_cmp_le_u32 s10, 2\n"
        "s_addc_u32 s76, 2, 0\n"
        "v_mfma_f32_16x16x32_bf16 v[136:139], a[0:3], a[216:219], v[136:139]\n"
        "ds_read_b64_tr_b16 a[106:107], v153 offset:12288\n"
        "s_cmp_lt_u32 s10, 2\n"
        "s_cselect_b32 s62, s12, s62\n"
        "v_mfma_f32_16x16x32_bf16 v[140:143], a[0:3], a[220:223], 0\n"
        "ds_read2_b32 a[0:1], v148 offset0:32 offset1:33\n"
        "s_lshl1_add_u32 s62, s62, s14\n"
        "s_lshl_b32 s62, s62, 6\n"
        "v_mfma_f32_16x16x32_bf16 v[140:143], a[4:7], a[216:219], v[140:143]\n"
        "ds_read2_b32 a[2:3], v148 offset0:34 offset1:35\n"
        "s_lshl_b32 s14, s62, 11\n"
        "s_lshl1_add_u32 s14, s63, s14\n"
        "v_mfma_f32_16x16x32_bf16 v[140:143], a[8:11], a[212:215], v[140:143]\n"
        "ds_read2_b32 a[4:5], v148 offset0:160 offset1:161\n"
        "s_add_u32 s64, s8, s14\n"
        "s_addc_u32 s65, s9, 0\n"
        "v_mfma_f32_16x16x32_bf16 v[140:143], a[12:15], a[208:211], v[140:143]\n"
        "ds_read2_b32 a[6:7], v148 offset0:162 offset1:163\n"
        "s_add_u32 s56, s36, s14\n"
        "s_addc_u32 s57, s37, 0\n"
        "v_mfma_f32_16x16x32_bf16 v[140:143], a[16:19], a[204:207], v[140:143]\n"
        "ds_read2_b32 a[8:9], v149 offset0:32 offset1:33\n"
        "s_lshl2_add_u32 s14, s12, s10\n"
        "s_lshl_b32 s14, s14, 6\n"
        "v_mfma_f32_16x16x32_bf16 v[140:143], a[20:23], a[200:203], v[140:143]\n"
        "ds_read2_b32 a[10:11], v149 offset0:34 offset1:35\n"
        "s_lshl4_add_u32 s14, s40, s14\n"
        "s_add_u32 s14, s14, 1024\n"
        "v_mfma_f32_16x16x32_bf16 v[140:143], a[24:27], a[196:199], v[140:143]\n"
        "ds_read_b128 a[196:199], v152 offset:3200\n"
        "s_lshl_b32 s14, s14, 7\n"
        "v_lshl_add_u32 v159, v232, 7, s14\n"
        "v_mfma_f32_16x16x32_bf16 v[140:143], a[28:31], a[192:195], v[140:143]\n"
        "ds_read_b128 a[192:195], v230 offset:0\n"
        "s_and_b32 s67, s74, 1\n"
        "s_waitcnt lgkmcnt(8)\n"
        "v_mfma_f32_16x16x32_bf16 v[96:99], a[32:35], a[224:227], v[96:99]\n"
        "ds_read_b128 a[200:203], v152 offset:2688\n"
        "global_load_lds_dwordx4 v144, s[60:61] offset:0\n"
        "s_lshl_b32 s67, s67, 12\n"
        "v_mfma_f32_16x16x32_bf16 v[100:103], a[32:35], a[228:231], v[100:103]\n"
        "ds_read_b64_tr_b16 a[108:109], v154 offset:8192\n"
        "v_add_u32 v230, 3840, v152\n"
        "v_mfma_f32_16x16x32_bf16 v[100:103], a[36:39], a[224:227], v[100:103]\n"
        "ds_read_b64_tr_b16 a[110:111], v154 offset:12288\n"
        "v_min_u32 v230, s16, v230\n"
        "v_mfma_f32_16x16x32_bf16 v[104:107], a[40:43], a[224:227], v[104:107]\n"
        "ds_read2_b32 a[12:13], v149 offset0:160 offset1:161\n"
        "s_add_u32 s24, s94, 0x400000\n"
        "s_addc_u32 s25, s95, 0\n"
        "v_mfma_f32_16x16x32_bf16 v[104:107], a[36:39], a[228:231], v[104:107]\n"
        "ds_read2_b32 a[14:15], v149 offset0:162 offset1:163\n"
        "global_load_lds_dwordx4 v145, s[60:61] offset:1024\n"
        "v_mfma_f32_16x16x32_bf16 v[104:107], a[32:35], a[232:235], v[104:107]\n"
        "ds_read_b128 a[204:207], v152 offset:2176\n"
        "v_mfma_f32_16x16x32_bf16 v[108:111], a[32:35], a[236:239], v[108:111]\n"
        "ds_read_b64_tr_b16 a[112:113], v153 offset:16384\n"
        "v_mfma_f32_16x16x32_bf16 v[108:111], a[36:39], a[232:235], v[108:111]\n"
        "ds_read_b64_tr_b16 a[114:115], v153 offset:20480\n"
        "s_add_u32 s14, s51, 504\n"
        "v_lshlrev_b32 v229, 5, v232\n"
        "v_mfma_f32_16x16x32_bf16 v[108:111], a[40:43], a[228:231], v[108:111]\n"
        "ds_read2_b32 a[16:17], v150 offset0:32 offset1:33\n"
        "global_load_lds_dwordx4 v146, s[60:61] offset:2048\n"
        "v_mfma_f32_16x16x32_bf16 v[108:111], a[44:47], a[224:227], v[108:111]\n"
        "ds_read2_b32 a[18:19], v150 offset0:34 offset1:35\n"
        "v_lshl_add_u32 v229, v233, 3, v229\n"
        "v_mfma_f32_16x16x32_bf16 v[128:131], a[48:51], a[224:227], v[128:131]\n"
        "ds_read_b128 a[208:211], v152 offset:1664\n"
        "v_sub_u32 v229, s14, v229\n"
        "v_mfma_f32_16x16x32_bf16 v[128:131], a[44:47], a[228:231], v[128:131]\n"
        "ds_read_b64_tr_b16 a[116:117], v154 offset:16384\n"
        "s_lshl_b32 s14, s41, 5\n"
        "s_add_u32 s14, s14, s42\n"
        "v_mfma_f32_16x16x32_bf16 v[128:131], a[40:43], a[232:235], v[128:131]\n"
        "ds_read_b64_tr_b16 a[118:119], v154 offset:20480\n"
        "global_load_lds_dwordx4 v147, s[60:61] offset:3072\n"
        "s_add_u32 m0, s73, 0xc000\n"
        "v_mfma_f32_16x16x32_bf16 v[128:131], a[36:39], a[236:239], v[128:131]\n"
        "ds_read2_b32 a[20:21], v150 offset0:160 offset1:161\n"
        "s_add_u32 s60, s22, s66\n"
        "s_addc_u32 s61, s23, 0\n"
        "v_mfma_f32_16x16x32_bf16 v[128:131], a[32:35], a[240:243], v[128:131]\n"
        "ds_read2_b32 a[22:23], v150 offset0:162 offset1:163\n"
        "s_xor_b32 s66, s29, 0x80000\n"
        "s_add_u32 s14, s14, 512\n"
        "v_mfma_f32_16x16x32_bf16 v[132:135], a[32:35], a[244:247], v[132:135]\n"
        "ds_read_b128 a[212:215], v152 offset:1152\n"
        "s_lshl_b32 s14, s14, 7\n"
        "v_mfma_f32_16x16x32_bf16 v[132:135], a[36:39], a[240:243], v[132:135]\n"
        "ds_read_b64_tr_b16 a[120:121], v153 offset:24576\n"
        "global_load_lds_dwordx4 v144, s[60:61] offset:0\n"
        "v_mfma_f32_16x16x32_bf16 v[132:135], a[40:43], a[236:239], v[132:135]\n"
        "ds_read_b64_tr_b16 a[122:123], v153 offset:28672\n"
        "v_mfma_f32_16x16x32_bf16 v[132:135], a[44:47], a[232:235], v[132:135]\n"
        "ds_read2_b32 a[24:25], v151 offset0:32 offset1:33\n"
        "v_mfma_f32_16x16x32_bf16 v[132:135], a[48:51], a[228:231], v[132:135]\n"
        "ds_read2_b32 a[26:27], v151 offset0:34 offset1:35\n"
        "v_mfma_f32_16x16x32_bf16 v[132:135], a[52:55], a[224:227], v[132:135]\n"
        "ds_read_b128 a[216:219], v152 offset:640\n"
        "global_load_lds_dwordx4 v145, s[60:61] offset:1024\n"
        "v_mfma_f32_16x16x32_bf16 v[136:139], a[56:59], a[224:227], v[136:139]\n"
        "ds_read_b64_tr_b16 a[124:125], v154 offset:24576\n"
        "v_mfma_f32_16x16x32_bf16 v[136:139], a[52:55], a[228:231], v[136:139]\n"
        "ds_read_b64_tr_b16 a[126:127], v154 offset:28672\n"
        "v_mfma_f32_16x16x32_bf16 v[136:139], a[48:51], a[232:235], v[136:139]\n"
        "ds_read2_b32 a[28:29], v151 offset0:160 offset1:161\n"
        "v_mfma_f32_16x16x32_bf16 v[136:139], a[44:47], a[236:239], v[136:139]\n"
        "ds_read2_b32 a[30:31], v151 offset0:162 offset1:163\n"
        "global_load_lds_dwordx4 v146, s[60:61] offset:2048\n"
        "v_mfma_f32_16x16x32_bf16 v[136:139], a[40:43], a[240:243], v[136:139]\n"
        "ds_read_b128 a[220:223], v152 offset:128\n"
        "v_mfma_f32_16x16x32_bf16 v[136:139], a[36:39], a[244:247], v[136:139]\n"
        "ds_read_b64 v[64:65], v229 offset:3584\n"
        "v_mfma_f32_16x16x32_bf16 v[136:139], a[32:35], a[248:251], v[136:139]\n"
        "ds_read_b64 v[66:67], v229 offset:3072\n"
        "v_mfma_f32_16x16x32_bf16 v[140:143], a[32:35], a[252:255], v[140:143]\n"
        "ds_read2_b32 a[32:33], v148 offset0:48 offset1:49\n"
        "global_load_lds_dwordx4 v147, s[60:61] offset:3072\n"
        "s_add_u32 m0, s73, 0x10000\n"
        "v_mfma_f32_16x16x32_bf16 v[140:143], a[36:39], a[248:251], v[140:143]\n"
        "ds_read2_b32 a[34:35], v148 offset0:50 offset1:51\n"
        "s_add_u32 s60, s22, s66\n"
        "s_addc_u32 s61, s23, 0\n"
        "v_mfma_f32_16x16x32_bf16 v[140:143], a[40:43], a[244:247], v[140:143]\n"
        "ds_read2_b32 a[36:37], v148 offset0:176 offset1:177\n"
        "s_xor_b32 s66, s29, 0xa0000\n"
        "v_mfma_f32_16x16x32_bf16 v[140:143], a[44:47], a[240:243], v[140:143]\n"
        "ds_read2_b32 a[38:39], v148 offset0:178 offset1:179\n"
        "v_mfma_f32_16x16x32_bf16 v[140:143], a[48:51], a[236:239], v[140:143]\n"
        "ds_read2_b32 a[40:41], v149 offset0:48 offset1:49\n"
        "global_load_lds_dwordx4 v144, s[60:61] offset:0\n"
        "v_mfma_f32_16x16x32_bf16 v[140:143], a[52:55], a[232:235], v[140:143]\n"
        "ds_read2_b32 a[42:43], v149 offset0:50 offset1:51\n"
        "s_waitcnt lgkmcnt(8)\n"
        "v_mfma_f32_16x16x32_bf16 v[140:143], a[56:59], a[228:231], v[140:143]\n"
        "ds_read_b128 a[228:231], v152 offset:3264\n"
        "s_waitcnt lgkmcnt(8)\n"
        "v_mfma_f32_16x16x32_bf16 v[140:143], a[60:63], a[224:227], v[140:143]\n"
        "s_mov_b64 exec, 0\n"
        "s_cmp_eq_u32 s40, 0\n"
        "s_cbranch_scc1 LHB0_%=\n"
        "s_cmp_eq_u32 s40, 1\n"
        "s_cbranch_scc1 LHB1_%=\n"
        "s_cmp_eq_u32 s40, 2\n"
        "s_cbranch_scc1 LHB2_%=\n"
        "s_branch LHB3_%=\n"
        "LHRB_%=:\n"
        "s_mov_b64 exec, -1\n"
        "v_mfma_f32_16x16x32_bf16 v[96:99], a[0:3], a[192:195], v[96:99]\n"
        "ds_read_b128 a[224:227], v231 offset:0\n"
        "s_waitcnt vmcnt(9)\n"
        "v_mfma_f32_16x16x32_bf16 v[100:103], a[0:3], a[196:199], v[100:103]\n"
        "s_mov_b64 exec, 0xffff\n"
        "global_store_dwordx2 v234, v[250:251], s[48:49] sc1\n"
        "s_mov_b64 exec, -1\n"
        "v_mfma_f32_16x16x32_bf16 v[100:103], a[4:7], a[192:195], v[100:103]\n"
        "ds_read_b128 a[232:235], v152 offset:2752\n"
        "v_mfma_f32_16x16x32_bf16 v[104:107], a[8:11], a[192:195], v[104:107]\n"
        "s_barrier\n"
        "v_mfma_f32_16x16x32_bf16 v[104:107], a[4:7], a[196:199], v[104:107]\n"
        "ds_read2_b32 a[44:45], v149 offset0:176 offset1:177\n"
        "global_load_lds_dwordx4 v145, s[60:61] offset:1024\n"
        "v_mfma_f32_16x16x32_bf16 v[104:107], a[0:3], a[200:203], v[104:107]\n"
        "ds_read2_b32 a[46:47], v149 offset0:178 offset1:179\n"
        "v_add_u32 v231, 3904, v152\n"
        "v_mfma_f32_16x16x32_bf16 v[108:111], a[0:3], a[204:207], v[108:111]\n"
        "ds_read_b128 a[236:239], v152 offset:2240\n"
        "v_min_u32 v231, s16, v231\n"
        "v_mfma_f32_16x16x32_bf16 v[108:111], a[4:7], a[200:203], v[108:111]\n"
        "ds_read2_b32 a[48:49], v150 offset0:48 offset1:49\n"
        "v_and_b32 v80, 0xffff0000, v65\n"
        "v_mfma_f32_16x16x32_bf16 v[108:111], a[8:11], a[196:199], v[108:111]\n"
        "ds_read2_b32 a[50:51], v150 offset0:50 offset1:51\n"
        "global_load_lds_dwordx4 v146, s[60:61] offset:2048\n"
        "v_mfma_f32_16x16x32_bf16 v[108:111], a[12:15], a[192:195], v[108:111]\n"
        "ds_read_b128 a[240:243], v152 offset:1728\n"
        "v_lshlrev_b32 v81, 16, v65\n"
        "v_mfma_f32_16x16x32_bf16 v[128:131], a[16:19], a[192:195], v[128:131]\n"
        "ds_read2_b32 a[52:53], v150 offset0:176 offset1:177\n"
        "v_and_b32 v82, 0xffff0000, v64\n"
        "v_mfma_f32_16x16x32_bf16 v[128:131], a[12:15], a[196:199], v[128:131]\n"
        "ds_read2_b32 a[54:55], v150 offset0:178 offset1:179\n"
        "v_lshlrev_b32 v83, 16, v64\n"
        "v_mfma_f32_16x16x32_bf16 v[128:131], a[8:11], a[200:203], v[128:131]\n"
        "ds_read_b128 a[244:247], v152 offset:1216\n"
        "global_load_lds_dwordx4 v147, s[60:61] offset:3072\n"
        "s_add_u32 m0, s73, 0x14000\n"
        "v_mfma_f32_16x16x32_bf16 v[128:131], a[4:7], a[204:207], v[128:131]\n"
        "ds_read2_b32 a[56:57], v151 offset0:48 offset1:49\n"
        "s_add_u32 s60, s22, s66\n"
        "s_addc_u32 s61, s23, 0\n"
        "v_mfma_f32_16x16x32_bf16 v[128:131], a[0:3], a[208:211], v[128:131]\n"
        "ds_read2_b32 a[58:59], v151 offset0:50 offset1:51\n"
        "s_xor_b32 s66, s29, 0xc0000\n"
        "v_mul_f32 v80, s54, v80\n"
        "v_mfma_f32_16x16x32_bf16 v[132:135], a[0:3], a[212:215], v[132:135]\n"
        "ds_read_b128 a[248:251], v152 offset:704\n"
        "v_mul_f32 v81, s54, v81\n"
        "v_mfma_f32_16x16x32_bf16 v[132:135], a[4:7], a[208:211], v[132:135]\n"
        "ds_read2_b32 a[60:61], v151 offset0:176 offset1:177\n"
        "global_load_lds_dwordx4 v144, s[60:61] offset:0\n"
        "v_mfma_f32_16x16x32_bf16 v[132:135], a[8:11], a[204:207], v[132:135]\n"
        "ds_read2_b32 a[62:63], v151 offset0:178 offset1:179\n"
        "v_mul_f32 v82, s54, v82\n"
        "v_mfma_f32_16x16x32_bf16 v[132:135], a[12:15], a[200:203], v[132:135]\n"
        "ds_read_b128 a[252:255], v152 offset:192\n"
        "v_mul_f32 v83, s54, v83\n"
        "v_mfma_f32_16x16x32_bf16 v[132:135], a[16:19], a[196:199], v[132:135]\n"
        "ds_read_b64 v[68:69], v229 offset:2560\n"
        "v_cvt_pk_bf16_f32 v80, 0, v80\n"
        "v_mfma_f32_16x16x32_bf16 v[132:135], a[20:23], a[192:195], v[132:135]\n"
        "ds_read_b64 v[70:71], v229 offset:2048\n"
        "global_load_lds_dwordx4 v145, s[60:61] offset:1024\n"
        "v_mfma_f32_16x16x32_bf16 v[136:139], a[24:27], a[192:195], v[136:139]\n"
        "ds_read_b64 v[72:73], v229 offset:1536\n"
        "v_cvt_pk_bf16_f32 v81, 0, v81\n"
        "v_mfma_f32_16x16x32_bf16 v[136:139], a[20:23], a[196:199], v[136:139]\n"
        "ds_read_b64 v[74:75], v229 offset:1024\n"
        "v_cvt_pk_bf16_f32 v82, 0, v82\n"
        "v_mfma_f32_16x16x32_bf16 v[136:139], a[16:19], a[200:203], v[136:139]\n"
        "ds_read_b64 v[76:77], v229 offset:512\n"
        "v_cvt_pk_bf16_f32 v83, 0, v83\n"
        "v_mfma_f32_16x16x32_bf16 v[136:139], a[12:15], a[204:207], v[136:139]\n"
        "ds_read_b64 v[78:79], v229 offset:0\n"
        "global_load_lds_dwordx4 v146, s[60:61] offset:2048\n"
        "v_mfma_f32_16x16x32_bf16 v[136:139], a[8:11], a[208:211], v[136:139]\n"
        "s_waitcnt lgkmcnt(7)\n"
        "v_and_b32 v229, 1, v233\n"
        "v_lshlrev_b32 v229, 9, v229\n"
        "v_mfma_f32_16x16x32_bf16 v[136:139], a[4:7], a[212:215], v[136:139]\n"
        "v_lshl_add_u32 v229, v232, 5, v229\n"
        "v_and_b32 v84, 0xffff0000, v67\n"
        "v_mfma_f32_16x16x32_bf16 v[136:139], a[0:3], a[216:219], v[136:139]\n"
        "v_lshlrev_b32 v85, 16, v67\n"
        "v_and_b32 v86, 0xffff0000, v66\n"
        "v_mfma_f32_16x16x32_bf16 v[140:143], a[0:3], a[220:223], v[140:143]\n"
        "ds_read2_b32 a[0:1], v148 offset0:64 offset1:65\n"
        "global_load_lds_dwordx4 v147, s[60:61] offset:3072\n"
        "s_add_u32 m0, s73, 0x0\n"
        "v_mfma_f32_16x16x32_bf16 v[140:143], a[4:7], a[216:219], v[140:143]\n"
        "ds_read2_b32 a[2:3], v148 offset0:66 offset1:67\n"
        "s_add_u32 s60, s22, s66\n"
        "s_addc_u32 s61, s23, 0\n"
        "v_mfma_f32_16x16x32_bf16 v[140:143], a[8:11], a[212:215], v[140:143]\n"
        "ds_read2_b32 a[4:5], v148 offset0:192 offset1:193\n"
        "s_xor_b32 s66, s29, 0xe0000\n"
        "v_lshlrev_b32 v87, 16, v66\n"
        "v_mfma_f32_16x16x32_bf16 v[140:143], a[12:15], a[208:211], v[140:143]\n"
        "ds_read2_b32 a[6:7], v148 offset0:194 offset1:195\n"
        "v_mul_f32 v84, s54, v84\n"
        "v_mfma_f32_16x16x32_bf16 v[140:143], a[16:19], a[204:207], v[140:143]\n"
        "ds_read2_b32 a[8:9], v149 offset0:64 offset1:65\n"
        "global_load_lds_dwordx4 v144, s[60:61] offset:0\n"
        "v_mfma_f32_16x16x32_bf16 v[140:143], a[20:23], a[200:203], v[140:143]\n"
        "ds_read2_b32 a[10:11], v149 offset0:66 offset1:67\n"
        "v_mul_f32 v85, s54, v85\n"
        "v_mfma_f32_16x16x32_bf16 v[140:143], a[24:27], a[196:199], v[140:143]\n"
        "ds_read_b128 a[196:199], v152 offset:3328\n"
        "v_mul_f32 v86, s54, v86\n"
        "v_mfma_f32_16x16x32_bf16 v[140:143], a[28:31], a[192:195], v[140:143]\n"
        "ds_read_b128 a[192:195], v230 offset:0\n"
        "v_add_u32 v230, 3968, v152\n"
        "v_mfma_f32_16x16x32_bf16 v[96:99], a[32:35], a[224:227], v[96:99]\n"
        "ds_read_b128 a[200:203], v152 offset:2816\n"
        "global_load_lds_dwordx4 v145, s[60:61] offset:1024\n"
        "v_mfma_f32_16x16x32_bf16 v[100:103], a[32:35], a[228:231], v[100:103]\n"
        "ds_read2_b32 a[12:13], v149 offset0:192 offset1:193\n"
        "v_min_u32 v230, s16, v230\n"
        "v_mfma_f32_16x16x32_bf16 v[100:103], a[36:39], a[224:227], v[100:103]\n"
        "ds_read2_b32 a[14:15], v149 offset0:194 offset1:195\n"
        "v_mul_f32 v87, s54, v87\n"
        "v_mfma_f32_16x16x32_bf16 v[104:107], a[40:43], a[224:227], v[104:107]\n"
        "ds_read_b128 a[204:207], v152 offset:2304\n"
        "LHC2_%=:\n"
        "s_cbranch_execz LHRC_%=\n"
        "v_cvt_pk_bf16_f32 v84, 0, v84\n"
        "v_mfma_f32_16x16x32_bf16 v[104:107], a[36:39], a[228:231], v[104:107]\n"
        "ds_read2_b32 a[16:17], v150 offset0:64 offset1:65\n"
        "global_load_lds_dwordx4 v146, s[60:61] offset:2048\n"
        "v_mfma_f32_16x16x32_bf16 v[104:107], a[32:35], a[232:235], v[104:107]\n"
        "ds_read2_b32 a[18:19], v150 offset0:66 offset1:67\n"
        "s_waitcnt vmcnt(12)\n"
        "v_mfma_f32_16x16x32_bf16 v[108:111], a[32:35], a[236:239], v[108:111]\n"
        "s_barrier\n"
        "v_mfma_f32_16x16x32_bf16 v[108:111], a[36:39], a[232:235], v[108:111]\n"
        "ds_read_b64_tr_b16 a[128:129], v153 offset:32768\n"
        "v_cvt_pk_bf16_f32 v85, 0, v85\n"
        "v_mfma_f32_16x16x32_bf16 v[108:111], a[40:43], a[228:231], v[108:111]\n"
        "ds_read_b64_tr_b16 a[130:131], v153 offset:36864\n"
        "global_load_lds_dwordx4 v147, s[60:61] offset:3072\n"
        "s_add_u32 m0, s73, 0x4000\n"
        "v_mfma_f32_16x16x32_bf16 v[108:111], a[44:47], a[224:227], v[108:111]\n"
        "ds_read_b64_tr_b16 a[132:133], v154 offset:32768\n"
        "s_add_u32 s60, s22, s66\n"
        "s_addc_u32 s61, s23, 0\n"
        "v_mfma_f32_16x16x32_bf16 v[128:131], a[48:51], a[224:227], v[128:131]\n"
        "ds_read_b64_tr_b16 a[134:135], v154 offset:36864\n"
        "s_xor_b32 s66, s29, 0x100000\n"
        "v_cvt_pk_bf16_f32 v86, 0, v86\n"
        "v_mfma_f32_16x16x32_bf16 v[128:131], a[44:47], a[228:231], v[128:131]\n"
        "ds_read_b64_tr_b16 a[136:137], v153 offset:40960\n"
        "v_cvt_pk_bf16_f32 v87, 0, v87\n"
        "v_mfma_f32_16x16x32_bf16 v[128:131], a[40:43], a[232:235], v[128:131]\n"
        "ds_read_b64_tr_b16 a[138:139], v153 offset:45056\n"
        "global_load_lds_dwordx4 v144, s[60:61] offset:0\n"
        "v_mfma_f32_16x16x32_bf16 v[128:131], a[36:39], a[236:239], v[128:131]\n"
        "ds_read_b64_tr_b16 a[140:141], v154 offset:40960\n"
        "v_lshrrev_b32 v234, 1, v254\n"
        "v_mfma_f32_16x16x32_bf16 v[128:131], a[32:35], a[240:243], v[128:131]\n"
        "ds_read_b64_tr_b16 a[142:143], v154 offset:45056\n"
        "v_add_u32 v234, s14, v234\n"
        "v_mfma_f32_16x16x32_bf16 v[132:135], a[32:35], a[244:247], v[132:135]\n"
        "ds_read_b64_tr_b16 a[144:145], v153 offset:49152\n"
        "s_lshr_b32 s14, s63, 2\n"
        "s_add_u32 s14, s14, 512\n"
        "v_mfma_f32_16x16x32_bf16 v[132:135], a[36:39], a[240:243], v[132:135]\n"
        "ds_read_b64_tr_b16 a[146:147], v153 offset:53248\n"
        "global_load_lds_dwordx4 v145, s[60:61] offset:1024\n"
        "s_lshl_b32 s14, s14, 7\n"
        "v_mfma_f32_16x16x32_bf16 v[132:135], a[40:43], a[236:239], v[132:135]\n"
        "ds_read_b128 a[208:211], v152 offset:1792\n"
        "v_mfma_f32_16x16x32_bf16 v[132:135], a[44:47], a[232:235], v[132:135]\n"
        "ds_read_b64_tr_b16 a[148:149], v154 offset:49152\n"
        "v_mfma_f32_16x16x32_bf16 v[132:135], a[48:51], a[228:231], v[132:135]\n"
        "ds_read_b64_tr_b16 a[150:151], v154 offset:53248\n"
        "v_mfma_f32_16x16x32_bf16 v[132:135], a[52:55], a[224:227], v[132:135]\n"
        "ds_read2_b32 a[20:21], v150 offset0:192 offset1:193\n"
        "global_load_lds_dwordx4 v146, s[60:61] offset:2048\n"
        "v_mfma_f32_16x16x32_bf16 v[136:139], a[56:59], a[224:227], v[136:139]\n"
        "ds_read2_b32 a[22:23], v150 offset0:194 offset1:195\n"
        "v_mfma_f32_16x16x32_bf16 v[136:139], a[52:55], a[228:231], v[136:139]\n"
        "ds_read_b128 a[212:215], v152 offset:1280\n"
        "v_mfma_f32_16x16x32_bf16 v[136:139], a[48:51], a[232:235], v[136:139]\n"
        "ds_read_b64_tr_b16 a[152:153], v153 offset:57344\n"
        "v_mfma_f32_16x16x32_bf16 v[136:139], a[44:47], a[236:239], v[136:139]\n"
        "ds_read_b64_tr_b16 a[154:155], v153 offset:61440\n"
        "global_load_lds_dwordx4 v147, s[60:61] offset:3072\n"
        "s_add_u32 m0, s73, 0x8000\n"
        "v_mfma_f32_16x16x32_bf16 v[136:139], a[40:43], a[240:243], v[136:139]\n"
        "ds_read2_b32 a[24:25], v151 offset0:64 offset1:65\n"
        "s_add_u32 s60, s22, s66\n"
        "s_addc_u32 s61, s23, 0\n"
        "v_mfma_f32_16x16x32_bf16 v[136:139], a[36:39], a[244:247], v[136:139]\n"
        "ds_read2_b32 a[26:27], v151 offset0:66 offset1:67\n"
        "s_xor_b32 s66, s29, 0x120000\n"
        "s_waitcnt lgkmcnt(8)\n"
        "v_mfma_f32_16x16x32_bf16 v[136:139], a[32:35], a[248:251], v[136:139]\n"
        "ds_read_b128 a[216:219], v152 offset:768\n"
        "v_mfma_f32_16x16x32_bf16 v[140:143], a[32:35], a[252:255], v[140:143]\n"
        "ds_read_b64_tr_b16 a[156:157], v154 offset:57344\n"
        "v_mfma_f32_16x16x32_bf16 v[140:143], a[36:39], a[248:251], v[140:143]\n"
        "ds_read_b64_tr_b16 a[158:159], v154 offset:61440\n"
        "v_mfma_f32_16x16x32_bf16 v[140:143], a[40:43], a[244:247], v[140:143]\n"
        "ds_read2_b32 a[28:29], v151 offset0:192 offset1:193\n"
        "v_mfma_f32_16x16x32_bf16 v[140:143], a[44:47], a[240:243], v[140:143]\n"
        "ds_read2_b32 a[30:31], v151 offset0:194 offset1:195\n"
        "v_mfma_f32_16x16x32_bf16 v[140:143], a[48:51], a[236:239], v[140:143]\n"
        "ds_read_b128 a[220:223], v152 offset:256\n"
        "v_mfma_f32_16x16x32_bf16 v[140:143], a[52:55], a[232:235], v[140:143]\n"
        "ds_read2_b32 a[32:33], v148 offset0:80 offset1:81\n"
        "v_mfma_f32_16x16x32_bf16 v[140:143], a[56:59], a[228:231], v[140:143]\n"
        "ds_read2_b32 a[34:35], v148 offset0:82 offset1:83\n"
        "v_mfma_f32_16x16x32_bf16 v[140:143], a[60:63], a[224:227], v[140:143]\n"
        "ds_read_b128 a[224:227], v231 offset:0\n"
        "global_load_dwordx2 v[236:237], v159, s[48:49] sc1\n"
        "v_mfma_f32_16x16x32_bf16 v[96:99], a[0:3], a[192:195], v[96:99]\n"
        "ds_read2_b32 a[36:37], v148 offset0:208 offset1:209\n"
        "v_add_u32 v231, 4032, v152\n"
        "v_mfma_f32_16x16x32_bf16 v[100:103], a[0:3], a[196:199], v[100:103]\n"
        "ds_read2_b32 a[38:39], v148 offset0:210 offset1:211\n"
        "v_min_u32 v231, s16, v231\n"
        "v_mfma_f32_16x16x32_bf16 v[100:103], a[4:7], a[192:195], v[100:103]\n"
        "ds_read_b128 a[228:231], v152 offset:3392\n"
        "v_mfma_f32_16x16x32_bf16 v[104:107], a[8:11], a[192:195], v[104:107]\n"
        "ds_read2_b32 a[40:41], v149 offset0:80 offset1:81\n"
        "v_mfma_f32_16x16x32_bf16 v[104:107], a[4:7], a[196:199], v[104:107]\n"
        "ds_read2_b32 a[42:43], v149 offset0:82 offset1:83\n"
        "v_mfma_f32_16x16x32_bf16 v[104:107], a[0:3], a[200:203], v[104:107]\n"
        "ds_read_b128 a[232:235], v152 offset:2880\n"
        "v_mfma_f32_16x16x32_bf16 v[108:111], a[0:3], a[204:207], v[108:111]\n"
        "ds_read2_b32 a[44:45], v149 offset0:208 offset1:209\n"
        "s_waitcnt vmcnt(9) lgkmcnt(13)\n"
        "v_mfma_f32_16x16x32_bf16 v[108:111], a[4:7], a[200:203], v[108:111]\n"
        "s_barrier\n"
        "v_mfma_f32_16x16x32_bf16 v[108:111], a[8:11], a[196:199], v[108:111]\n"
        "ds_read_b64_tr_b16 v[160:161], v155 offset:0\n"
        "global_load_lds_dwordx4 v144, s[60:61] offset:0\n"
        "v_mfma_f32_16x16x32_bf16 v[108:111], a[12:15], a[192:195], v[108:111]\n"
        "ds_read_b64_tr_b16 v[162:163], v155 offset:4096\n"
        "v_mfma_f32_16x16x32_bf16 v[128:131], a[16:19], a[192:195], v[128:131]\n"
        "ds_read_b64_tr_b16 v[164:165], v156 offset:0\n"
        "v_mfma_f32_16x16x32_bf16 v[128:131], a[12:15], a[196:199], v[128:131]\n"
        "ds_read_b64_tr_b16 v[166:167], v156 offset:4096\n"
        "v_mfma_f32_16x16x32_bf16 v[128:131], a[8:11], a[200:203], v[128:131]\n"
        "ds_read_b64_tr_b16 v[168:169], v155 offset:8192\n"
        "global_load_lds_dwordx4 v145, s[60:61] offset:1024\n"
        "v_mfma_f32_16x16x32_bf16 v[128:131], a[4:7], a[204:207], v[128:131]\n"
        "ds_read_b64_tr_b16 v[170:171], v155 offset:12288\n"
        "v_mfma_f32_16x16x32_bf16 v[128:131], a[0:3], a[208:211], v[128:131]\n"
        "ds_read_b64_tr_b16 v[172:173], v156 offset:8192\n"
        "v_mfma_f32_16x16x32_bf16 v[132:135], a[0:3], a[212:215], v[132:135]\n"
        "ds_read_b64_tr_b16 v[174:175], v156 offset:12288\n"
        "v_mfma_f32_16x16x32_bf16 v[132:135], a[4:7], a[208:211], v[132:135]\n"
        "ds_read2_b32 a[46:47], v149 offset0:210 offset1:211\n"
        "global_load_lds_dwordx4 v146, s[60:61] offset:2048\n"
        "v_mfma_f32_16x16x32_bf16 v[132:135], a[8:11], a[204:207], v[132:135]\n"
        "ds_read_b128 a[236:239], v152 offset:2368\n"
        "v_mfma_f32_16x16x32_bf16 v[132:135], a[12:15], a[200:203], v[132:135]\n"
        "ds_read_b64_tr_b16 v[176:177], v155 offset:16384\n"
        "v_mfma_f32_16x16x32_bf16 v[132:135], a[16:19], a[196:199], v[132:135]\n"
        "ds_read_b64_tr_b16 v[178:179], v155 offset:20480\n"
        "v_mfma_f32_16x16x32_bf16 v[132:135], a[20:23], a[192:195], v[132:135]\n"
        "ds_read2_b32 a[48:49], v150 offset0:80 offset1:81\n"
        "global_load_lds_dwordx4 v147, s[60:61] offset:3072\n"
        "s_add_u32 m0, s73, 0xc000\n"
        "v_mfma_f32_16x16x32_bf16 v[136:139], a[24:27], a[192:195], v[136:139]\n"
        "ds_read2_b32 a[50:51], v150 offset0:82 offset1:83\n"
        "s_add_u32 s60, s22, s66\n"
        "s_addc_u32 s61, s23, 0\n"
        "v_mfma_f32_16x16x32_bf16 v[136:139], a[20:23], a[196:199], v[136:139]\n"
        "ds_read_b128 a[240:243], v152 offset:1856\n"
        "s_sub_u32 s66, 15, s12\n"
        "v_mfma_f32_16x16x32_bf16 v[136:139], a[16:19], a[200:203], v[136:139]\n"
        "ds_read_b64_tr_b16 v[180:181], v156 offset:16384\n"
        "s_cmp_lt_u32 s74, 2\n"
        "s_cselect_b32 s66, s12, s66\n"
        "v_mfma_f32_16x16x32_bf16 v[136:139], a[12:15], a[204:207], v[136:139]\n"
        "ds_read_b64_tr_b16 v[182:183], v156 offset:20480\n"
        "global_load_lds_dwordx4 v144, s[60:61] offset:0\n"
        "s_mul_i32 s66, s66, 1052672\n"
        "v_mfma_f32_16x16x32_bf16 v[136:139], a[8:11], a[208:211], v[136:139]\n"
        "ds_read2_b32 a[52:53], v150 offset0:208 offset1:209\n"
        "s_add_u32 s66, s66, s67\n"
        "s_waitcnt lgkmcnt(8)\n"
        "v_mfma_f32_16x16x32_bf16 v[136:139], a[4:7], a[212:215], v[136:139]\n"
        "ds_read2_b32 a[54:55], v150 offset0:210 offset1:211\n"
        "s_add_u32 s68, s20, s66\n"
        "s_addc_u32 s69, s21, 0\n"
        "v_mfma_f32_16x16x32_bf16 v[136:139], a[0:3], a[216:219], v[136:139]\n"
        "ds_read_b128 a[244:247], v152 offset:1344\n"
        "s_xor_b32 s66, s17, 0\n"
        "s_lshl_b32 s66, s66, 15\n"
        "v_mfma_f32_16x16x32_bf16 v[140:143], a[0:3], a[220:223], v[140:143]\n"
        "ds_read_b64_tr_b16 v[184:185], v155 offset:24576\n"
        "global_load_lds_dwordx4 v145, s[60:61] offset:1024\n"
        "v_mfma_f32_16x16x32_bf16 v[140:143], a[4:7], a[216:219], v[140:143]\n"
        "ds_read_b64_tr_b16 v[186:187], v155 offset:28672\n"
        "v_mfma_f32_16x16x32_bf16 v[140:143], a[8:11], a[212:215], v[140:143]\n"
        "ds_read2_b32 a[56:57], v151 offset0:80 offset1:81\n"
        "s_waitcnt vmcnt(6) lgkmcnt(8)\n"
        "v_mfma_f32_16x16x32_bf16 v[140:143], a[12:15], a[208:211], v[140:143]\n"
        "ds_read2_b32 a[58:59], v151 offset0:82 offset1:83\n"
        "v_cmp_ne_u64 vcc, v[236:237], v[250:251]\n"
        "v_mfma_f32_16x16x32_bf16 v[140:143], a[16:19], a[204:207], v[140:143]\n"
        "ds_read_b128 a[248:251], v152 offset:832\n"
        "global_load_lds_dwordx4 v146, s[60:61] offset:2048\n"
        "s_cbranch_vccnz LPCX_%=\n"
        "LBCX_%=:\n"
        "v_mfma_f32_16x16x32_bf16 v[140:143], a[20:23], a[200:203], v[140:143]\n"
        "ds_read_b64_tr_b16 v[188:189], v156 offset:24576\n"
        "v_mfma_f32_16x16x32_bf16 v[140:143], a[24:27], a[196:199], v[140:143]\n"
        "ds_read_b64_tr_b16 v[190:191], v156 offset:28672\n"
        "v_mfma_f32_16x16x32_bf16 v[140:143], a[28:31], a[192:195], v[140:143]\n"
        "ds_read2_b32 a[60:61], v151 offset0:208 offset1:209\n"
        "global_load_dword v224, v159, s[48:49] offset:64 sc1\n"
        "v_mfma_f32_16x16x32_bf16 v[96:99], a[32:35], a[224:227], v[96:99]\n"
        "ds_read2_b32 a[62:63], v151 offset0:210 offset1:211\n"
        "global_load_lds_dwordx4 v147, s[60:61] offset:3072\n"
        "s_add_u32 m0, s73, 0x10000\n"
        "v_mfma_f32_16x16x32_bf16 v[100:103], a[32:35], a[228:231], v[100:103]\n"
        "ds_read_b128 a[252:255], v152 offset:320\n"
        "s_add_u32 s60, s68, s66\n"
        "s_addc_u32 s61, s69, 0\n"
        "v_mfma_f32_16x16x32_bf16 v[100:103], a[36:39], a[224:227], v[100:103]\n"
        "ds_read2_b32 a[0:1], v148 offset0:96 offset1:97\n"
        "s_xor_b32 s66, s17, 1\n"
        "s_lshl_b32 s66, s66, 15\n"
        "v_mfma_f32_16x16x32_bf16 v[104:107], a[40:43], a[224:227], v[104:107]\n"
        "ds_read2_b32 a[2:3], v148 offset0:98 offset1:99\n"
        "v_mfma_f32_16x16x32_bf16 v[104:107], a[36:39], a[228:231], v[104:107]\n"
        "ds_read_b128 a[192:195], v230 offset:0\n"
        "v_lshrrev_b32 v230, 1, v233\n"
        "v_mfma_f32_16x16x32_bf16 v[104:107], a[32:35], a[232:235], v[104:107]\n"
        "ds_read2_b32 a[4:5], v148 offset0:224 offset1:225\n"
        "v_lshl_add_u32 v229, v230, 4, v229\n"
        "v_mfma_f32_16x16x32_bf16 v[108:111], a[32:35], a[236:239], v[108:111]\n"
        "ds_read2_b32 a[6:7], v148 offset0:226 offset1:227\n"
        "s_waitcnt lgkmcnt(8)\n"
        "v_mfma_f32_16x16x32_bf16 v[108:111], a[36:39], a[232:235], v[108:111]\n"
        "s_barrier\n"
        "v_mfma_f32_16x16x32_bf16 v[108:111], a[40:43], a[228:231], v[108:111]\n"
        "ds_read_b64_tr_b16 v[192:193], v153 offset:0\n"
        "global_load_lds_dwordx4 v254, s[60:61] offset:0 sc1\n"
        "v_mfma_f32_16x16x32_bf16 v[108:111], a[44:47], a[224:227], v[108:111]\n"
        "ds_read_b64_tr_b16 v[194:195], v153 offset:4096\n"
        "v_mfma_f32_16x16x32_bf16 v[128:131], a[48:51], a[224:227], v[128:131]\n"
        "ds_read_b64_tr_b16 v[196:197], v154 offset:0\n"
        "v_mfma_f32_16x16x32_bf16 v[128:131], a[44:47], a[228:231], v[128:131]\n"
        "ds_read_b64_tr_b16 v[198:199], v154 offset:4096\n"
        "v_mfma_f32_16x16x32_bf16 v[128:131], a[40:43], a[232:235], v[128:131]\n"
        "ds_read_b128 a[196:199], v152 offset:3456\n"
        "global_load_lds_dwordx4 v254, s[60:61] offset:1024 sc1\n"
        "v_mfma_f32_16x16x32_bf16 v[128:131], a[36:39], a[236:239], v[128:131]\n"
        "ds_read_b64_tr_b16 v[200:201], v153 offset:8192\n"
        "v_mfma_f32_16x16x32_bf16 v[128:131], a[32:35], a[240:243], v[128:131]\n"
        "ds_read_b64_tr_b16 v[202:203], v153 offset:12288\n"
        "v_mfma_f32_16x16x32_bf16 v[132:135], a[32:35], a[244:247], v[132:135]\n"
        "ds_read2_b32 a[8:9], v149 offset0:96 offset1:97\n"
        "v_mfma_f32_16x16x32_bf16 v[132:135], a[36:39], a[240:243], v[132:135]\n"
        "ds_read2_b32 a[10:11], v149 offset0:98 offset1:99\n"
        "global_load_lds_dwordx4 v254, s[60:61] offset:2048 sc1\n"
        "v_mfma_f32_16x16x32_bf16 v[132:135], a[40:43], a[236:239], v[132:135]\n"
        "ds_read_b128 a[200:203], v152 offset:2944\n"
        "v_mfma_f32_16x16x32_bf16 v[132:135], a[44:47], a[232:235], v[132:135]\n"
        "ds_read_b64_tr_b16 v[204:205], v154 offset:8192\n"
        "v_mfma_f32_16x16x32_bf16 v[132:135], a[48:51], a[228:231], v[132:135]\n"
        "ds_read_b64_tr_b16 v[206:207], v154 offset:12288\n"
        "v_mfma_f32_16x16x32_bf16 v[132:135], a[52:55], a[224:227], v[132:135]\n"
        "ds_read2_b32 a[12:13], v149 offset0:224 offset1:225\n"
        "global_load_lds_dwordx4 v254, s[60:61] offset:3072 sc1\n"
        "s_add_u32 m0, s73, 0x14000\n"
        "v_mfma_f32_16x16x32_bf16 v[136:139], a[56:59], a[224:227], v[136:139]\n"
        "ds_read2_b32 a[14:15], v149 offset0:226 offset1:227\n"
        "s_add_u32 s60, s68, s66\n"
        "s_addc_u32 s61, s69, 0\n"
        "v_mfma_f32_16x16x32_bf16 v[136:139], a[52:55], a[228:231], v[136:139]\n"
        "ds_read_b128 a[204:207], v152 offset:2432\n"
        "s_xor_b32 s66, s29, 0x140000\n"
        "v_mfma_f32_16x16x32_bf16 v[136:139], a[48:51], a[232:235], v[136:139]\n"
        "ds_read_b64_tr_b16 v[208:209], v153 offset:16384\n"
        "v_mfma_f32_16x16x32_bf16 v[136:139], a[44:47], a[236:239], v[136:139]\n"
        "ds_read_b64_tr_b16 v[210:211], v153 offset:20480\n"
        "global_load_lds_dwordx4 v254, s[60:61] offset:0 sc1\n"
        "v_mfma_f32_16x16x32_bf16 v[136:139], a[40:43], a[240:243], v[136:139]\n"
        "ds_read2_b32 a[16:17], v150 offset0:96 offset1:97\n"
        "s_waitcnt lgkmcnt(8)\n"
        "v_mfma_f32_16x16x32_bf16 v[136:139], a[36:39], a[244:247], v[136:139]\n"
        "ds_read2_b32 a[18:19], v150 offset0:98 offset1:99\n"
        "v_mfma_f32_16x16x32_bf16 v[136:139], a[32:35], a[248:251], v[136:139]\n"
        "ds_read_b128 a[208:211], v152 offset:1920\n"
        "s_and_b32 s67, s75, 1\n"
        "s_lshl_b32 s67, s67, 12\n"
        "v_mfma_f32_16x16x32_bf16 v[140:143], a[32:35], a[252:255], v[140:143]\n"
        "ds_read_b64_tr_b16 v[212:213], v154 offset:16384\n"
        "global_load_lds_dwordx4 v254, s[60:61] offset:1024 sc1\n"
        "v_mfma_f32_16x16x32_bf16 v[140:143], a[36:39], a[248:251], v[140:143]\n"
        "ds_read_b64_tr_b16 v[214:215], v154 offset:20480\n"
        "v_mfma_f32_16x16x32_bf16 v[140:143], a[40:43], a[244:247], v[140:143]\n"
        "ds_read2_b32 a[20:21], v150 offset0:224 offset1:225\n"
        "v_mfma_f32_16x16x32_bf16 v[140:143], a[44:47], a[240:243], v[140:143]\n"
        "ds_read2_b32 a[22:23], v150 offset0:226 offset1:227\n"
        "v_mfma_f32_16x16x32_bf16 v[140:143], a[48:51], a[236:239], v[140:143]\n"
        "ds_read_b128 a[212:215], v152 offset:1408\n"
        "global_load_lds_dwordx4 v254, s[60:61] offset:2048 sc1\n"
        "v_mfma_f32_16x16x32_bf16 v[140:143], a[52:55], a[232:235], v[140:143]\n"
        "ds_read_b64_tr_b16 v[216:217], v153 offset:24576\n"
        "v_mfma_f32_16x16x32_bf16 v[140:143], a[56:59], a[228:231], v[140:143]\n"
        "ds_read_b64_tr_b16 v[218:219], v153 offset:28672\n"
        "v_mfma_f32_16x16x32_bf16 v[140:143], a[60:63], a[224:227], v[140:143]\n"
        "ds_read_b64_tr_b16 v[220:221], v154 offset:24576\n"
        "v_mfma_f32_16x16x32_bf16 v[96:99], a[0:3], a[192:195], v[96:99]\n"
        "ds_read_b64_tr_b16 v[222:223], v154 offset:28672\n"
        "global_load_lds_dwordx4 v254, s[60:61] offset:3072 sc1\n"
        "s_add_u32 m0, s73, 0x0\n"
        "v_mfma_f32_16x16x32_bf16 v[100:103], a[0:3], a[196:199], v[100:103]\n"
        "ds_read2_b32 a[24:25], v151 offset0:96 offset1:97\n"
        "s_add_u32 s60, s22, s66\n"
        "s_addc_u32 s61, s23, 0\n"
        "v_mfma_f32_16x16x32_bf16 v[100:103], a[4:7], a[192:195], v[100:103]\n"
        "ds_read2_b32 a[26:27], v151 offset0:98 offset1:99\n"
        "s_xor_b32 s66, s17, 2\n"
        "s_lshl_b32 s66, s66, 15\n"
        "v_mfma_f32_16x16x32_bf16 v[104:107], a[8:11], a[192:195], v[104:107]\n"
        "ds_read_b128 a[216:219], v152 offset:896\n"
        "s_waitcnt lgkmcnt(8)\n"
        "v_mfma_f32_16x16x32_bf16 v[104:107], a[4:7], a[196:199], v[104:107]\n"
        "ds_read2_b32 a[28:29], v151 offset0:224 offset1:225\n"
        "v_mfma_f32_16x16x32_bf16 v[104:107], a[0:3], a[200:203], v[104:107]\n"
        "ds_read2_b32 a[30:31], v151 offset0:226 offset1:227\n"
        "v_mfma_f32_16x16x32_bf16 v[108:111], a[0:3], a[204:207], v[108:111]\n"
        "ds_read_b128 a[220:223], v152 offset:384\n"
        "v_mfma_f32_16x16x32_bf16 v[108:111], a[4:7], a[200:203], v[108:111]\n"
        "ds_read2_b32 a[32:33], v148 offset0:112 offset1:113\n"
        "v_mfma_f32_16x16x32_bf16 v[108:111], a[8:11], a[196:199], v[108:111]\n"
        "ds_read2_b32 a[34:35], v148 offset0:114 offset1:115\n"
        "v_mfma_f32_16x16x32_bf16 v[108:111], a[12:15], a[192:195], v[108:111]\n"
        "ds_read_b128 a[224:227], v231 offset:0\n"
        "v_mfma_f32_16x16x32_bf16 v[128:131], a[16:19], a[192:195], v[128:131]\n"
        "ds_read2_b32 a[36:37], v148 offset0:240 offset1:241\n"
        "v_mfma_f32_16x16x32_bf16 v[128:131], a[12:15], a[196:199], v[128:131]\n"
        "ds_read2_b32 a[38:39], v148 offset0:242 offset1:243\n"
        "s_waitcnt lgkmcnt(8)\n"
        "v_mfma_f32_16x16x32_bf16 v[128:131], a[8:11], a[200:203], v[128:131]\n"
        "ds_read_b128 a[228:231], v152 offset:3520\n"
        "v_mfma_f32_16x16x32_bf16 v[128:131], a[4:7], a[204:207], v[128:131]\n"
        "ds_read2_b32 a[40:41], v149 offset0:112 offset1:113\n"
        "v_mfma_f32_16x16x32_bf16 v[128:131], a[0:3], a[208:211], v[128:131]\n"
        "ds_read2_b32 a[42:43], v149 offset0:114 offset1:115\n"
        "s_waitcnt vmcnt(8)\n"
        "v_mfma_f32_16x16x32_bf16 v[132:135], a[0:3], a[212:215], v[132:135]\n"
        "s_barrier\n"
        "v_mfma_f32_16x16x32_bf16 v[132:135], a[4:7], a[208:211], v[132:135]\n"
        "ds_read_b64_tr_b16 v[112:113], v153 offset:32768\n"
        "global_load_lds_dwordx4 v144, s[60:61] offset:0\n"
        "v_mfma_f32_16x16x32_bf16 v[132:135], a[8:11], a[204:207], v[132:135]\n"
        "ds_read_b64_tr_b16 v[114:115], v153 offset:36864\n"
        "v_mfma_f32_16x16x32_bf16 v[132:135], a[12:15], a[200:203], v[132:135]\n"
        "ds_read_b64_tr_b16 v[116:117], v154 offset:32768\n"
        "v_mfma_f32_16x16x32_bf16 v[132:135], a[16:19], a[196:199], v[132:135]\n"
        "ds_read_b64_tr_b16 v[118:119], v154 offset:36864\n"
        "v_mfma_f32_16x16x32_bf16 v[132:135], a[20:23], a[192:195], v[132:135]\n"
        "ds_read_b64_tr_b16 v[120:121], v153 offset:40960\n"
        "global_load_lds_dwordx4 v145, s[60:61] offset:1024\n"
        "v_mfma_f32_16x16x32_bf16 v[136:139], a[24:27], a[192:195], v[136:139]\n"
        "ds_read_b64_tr_b16 v[122:123], v153 offset:45056\n"
        "v_mfma_f32_16x16x32_bf16 v[136:139], a[20:23], a[196:199], v[136:139]\n"
        "ds_read_b128 a[232:235], v152 offset:3008\n"
        "v_mfma_f32_16x16x32_bf16 v[136:139], a[16:19], a[200:203], v[136:139]\n"
        "ds_read_b64_tr_b16 v[124:125], v154 offset:40960\n"
        "v_mfma_f32_16x16x32_bf16 v[136:139], a[12:15], a[204:207], v[136:139]\n"
        "ds_read_b64_tr_b16 v[126:127], v154 offset:45056\n"
        "global_load_lds_dwordx4 v146, s[60:61] offset:2048\n"
        "v_mfma_f32_16x16x32_bf16 v[136:139], a[8:11], a[208:211], v[136:139]\n"
        "ds_read2_b32 a[44:45], v149 offset0:240 offset1:241\n"
        "s_waitcnt lgkmcnt(8)\n"
        "v_mfma_f32_16x16x32_bf16 v[136:139], a[4:7], a[212:215], v[136:139]\n"
        "ds_read2_b32 a[46:47], v149 offset0:242 offset1:243\n"
        "v_mfma_f32_16x16x32_bf16 v[136:139], a[0:3], a[216:219], v[136:139]\n"
        "ds_read_b128 a[236:239], v152 offset:2496\n"
        "v_mfma_f32_16x16x32_bf16 v[140:143], a[0:3], a[220:223], v[140:143]\n"
        "ds_read_b64_tr_b16 a[64:65], v153 offset:49152\n"
        "global_load_lds_dwordx4 v147, s[60:61] offset:3072\n"
        "s_add_u32 m0, s73, 0x4000\n"
        "v_mfma_f32_16x16x32_bf16 v[140:143], a[4:7], a[216:219], v[140:143]\n"
        "ds_read_b64_tr_b16 a[66:67], v153 offset:53248\n"
        "s_add_u32 s60, s68, s66\n"
        "s_addc_u32 s61, s69, 0\n"
        "v_mfma_f32_16x16x32_bf16 v[140:143], a[8:11], a[212:215], v[140:143]\n"
        "ds_read2_b32 a[48:49], v150 offset0:112 offset1:113\n"
        "s_xor_b32 s66, s29, 0x160000\n"
        "v_mfma_f32_16x16x32_bf16 v[140:143], a[12:15], a[208:211], v[140:143]\n"
        "ds_read2_b32 a[50:51], v150 offset0:114 offset1:115\n"
        "v_mfma_f32_16x16x32_bf16 v[140:143], a[16:19], a[204:207], v[140:143]\n"
        "ds_read_b128 a[240:243], v152 offset:1984\n"
        "global_load_lds_dwordx4 v254, s[60:61] offset:0 sc1\n"
        "v_mfma_f32_16x16x32_bf16 v[140:143], a[20:23], a[200:203], v[140:143]\n"
        "ds_read_b64_tr_b16 a[68:69], v154 offset:49152\n"
        "v_mfma_f32_16x16x32_bf16 v[140:143], a[24:27], a[196:199], v[140:143]\n"
        "ds_read_b64_tr_b16 a[70:71], v154 offset:53248\n"
        "v_mfma_f32_16x16x32_bf16 v[140:143], a[28:31], a[192:195], v[140:143]\n"
        "ds_read2_b32 a[52:53], v150 offset0:240 offset1:241\n"
        "v_mfma_f32_16x16x32_bf16 v[96:99], a[32:35], a[224:227], v[96:99]\n"
        "ds_read2_b32 a[54:55], v150 offset0:242 offset1:243\n"
        "global_load_lds_dwordx4 v254, s[60:61] offset:1024 sc1\n"
        "v_mfma_f32_16x16x32_bf16 v[100:103], a[32:35], a[228:231], v[100:103]\n"
        "ds_read_b128 a[244:247], v152 offset:1472\n"
        "s_waitcnt lgkmcnt(8)\n"
        "v_mfma_f32_16x16x32_bf16 v[100:103], a[36:39], a[224:227], v[100:103]\n"
        "ds_read_b64_tr_b16 a[72:73], v153 offset:57344\n"
        "v_mfma_f32_16x16x32_bf16 v[104:107], a[40:43], a[224:227], v[104:107]\n"
        "ds_read_b64_tr_b16 a[74:75], v153 offset:61440\n"
        "v_cvt_pk_bf16_f32 v96, 0, v96\n"
        "v_mfma_f32_16x16x32_bf16 v[104:107], a[36:39], a[228:231], v[104:107]\n"
        "ds_read_b64_tr_b16 a[76:77], v154 offset:57344\n"
        "global_load_lds_dwordx4 v254, s[60:61] offset:2048 sc1\n"
        "v_mfma_f32_16x16x32_bf16 v[104:107], a[32:35], a[232:235], v[104:107]\n"
        "ds_read_b64_tr_b16 a[78:79], v154 offset:61440\n"
        "v_cvt_pk_bf16_f32 v97, 0, v97\n"
        "v_mfma_f32_16x16x32_bf16 v[108:111], a[32:35], a[236:239], v[108:111]\n"
        "ds_read2_b32 a[56:57], v151 offset0:112 offset1:113\n"
        "v_cvt_pk_bf16_f32 v98, 0, v98\n"
        "v_mfma_f32_16x16x32_bf16 v[108:111], a[36:39], a[232:235], v[108:111]\n"
        "ds_read2_b32 a[58:59], v151 offset0:114 offset1:115\n"
        "v_cvt_pk_bf16_f32 v99, 0, v99\n"
        "v_mfma_f32_16x16x32_bf16 v[108:111], a[40:43], a[228:231], v[108:111]\n"
        "ds_read_b128 a[248:251], v152 offset:960\n"
        "global_load_lds_dwordx4 v254, s[60:61] offset:3072 sc1\n"
        "s_add_u32 m0, s73, 0x8000\n"
        "v_mfma_f32_16x16x32_bf16 v[108:111], a[44:47], a[224:227], v[108:111]\n"
        "ds_read_b128 a[252:255], v152 offset:448\n"
        "s_waitcnt lgkmcnt(8)\n"
        "v_mfma_f32_16x16x32_bf16 v[128:131], a[48:51], a[224:227], v[128:131]\n"
        "ds_read2_b32 a[60:61], v151 offset0:240 offset1:241\n"
        "s_add_u32 s60, s22, s66\n"
        "s_addc_u32 s61, s23, 0\n"
        "v_mfma_f32_16x16x32_bf16 v[128:131], a[44:47], a[228:231], v[128:131]\n"
        "ds_read2_b32 a[62:63], v151 offset0:242 offset1:243\n"
        "s_xor_b32 s66, s17, 3\n"
        "s_lshl_b32 s66, s66, 15\n"
        "v_mfma_f32_16x16x32_bf16 v[128:131], a[40:43], a[232:235], v[128:131]\n"
        "v_add_f32 v80, v80, v96\n"
        "v_add_f32 v81, v81, v97\n"
        "v_mfma_f32_16x16x32_bf16 v[128:131], a[36:39], a[236:239], v[128:131]\n"
        "s_waitcnt lgkmcnt(6)\n"
        "v_add_f32 v82, v82, v98\n"
        "v_add_f32 v83, v83, v99\n"
        "v_mfma_f32_16x16x32_bf16 v[128:131], a[32:35], a[240:243], v[128:131]\n"
        "s_barrier\n"
        "v_mfma_f32_16x16x32_bf16 v[132:135], a[32:35], a[244:247], v[132:135]\n"
        "global_load_lds_dwordx4 v144, s[60:61] offset:0\n"
        "v_cvt_pk_bf16_f32 v88, v80, v81\n"
        "v_mfma_f32_16x16x32_bf16 v[132:135], a[36:39], a[240:243], v[132:135]\n"
        "v_cvt_pk_bf16_f32 v89, v82, v83\n"
        "v_cvt_pk_bf16_f32 v100, 0, v100\n"
        "v_mfma_f32_16x16x32_bf16 v[132:135], a[40:43], a[236:239], v[132:135]\n"
        "s_waitcnt lgkmcnt(2)\n"
        "v_cvt_pk_bf16_f32 v101, 0, v101\n"
        "v_cvt_pk_bf16_f32 v102, 0, v102\n"
        "v_mfma_f32_16x16x32_bf16 v[132:135], a[44:47], a[232:235], v[132:135]\n"
        "global_load_lds_dwordx4 v145, s[60:61] offset:1024\n"
        "v_cvt_pk_bf16_f32 v103, 0, v103\n"
        "v_mfma_f32_16x16x32_bf16 v[132:135], a[48:51], a[228:231], v[132:135]\n"
        "s_waitcnt lgkmcnt(0)\n"
        "v_add_f32 v84, v84, v100\n"
        "v_add_f32 v85, v85, v101\n"
        "v_mfma_f32_16x16x32_bf16 v[132:135], a[52:55], a[224:227], v[132:135]\n"
        "s_barrier\n"
        "v_mfma_f32_16x16x32_bf16 v[136:139], a[56:59], a[224:227], v[136:139]\n"
        "global_load_lds_dwordx4 v146, s[60:61] offset:2048\n"
        "s_waitcnt vmcnt(15)\n"
        "v_mfma_f32_16x16x32_bf16 v[136:139], a[52:55], a[228:231], v[136:139]\n"
        "s_barrier\n"
        "v_mfma_f32_16x16x32_bf16 v[136:139], a[48:51], a[232:235], v[136:139]\n"
        "ds_read_b128 a[0:3], v244 offset:0\n"
        "v_add_f32 v86, v86, v102\n"
        "v_mfma_f32_16x16x32_bf16 v[136:139], a[44:47], a[236:239], v[136:139]\n"
        "ds_read_b128 a[4:7], v244 offset:4096\n"
        "global_load_lds_dwordx4 v147, s[60:61] offset:3072\n"
        "s_add_u32 m0, s73, 0xc000\n"
        "v_mfma_f32_16x16x32_bf16 v[136:139], a[40:43], a[240:243], v[136:139]\n"
        "ds_read_b128 a[8:11], v244 offset:8192\n"
        "s_add_u32 s60, s68, s66\n"
        "s_addc_u32 s61, s69, 0\n"
        "v_mfma_f32_16x16x32_bf16 v[136:139], a[36:39], a[244:247], v[136:139]\n"
        "ds_read_b128 a[12:15], v244 offset:12288\n"
        "s_xor_b32 s66, s29, 0x180000\n"
        "v_add_f32 v87, v87, v103\n"
        "v_mfma_f32_16x16x32_bf16 v[136:139], a[32:35], a[248:251], v[136:139]\n"
        "ds_read_b128 a[16:19], v244 offset:1024\n"
        "global_load_lds_dwordx4 v254, s[60:61] offset:0 sc1\n"
        "v_mfma_f32_16x16x32_bf16 v[140:143], a[32:35], a[252:255], v[140:143]\n"
        "ds_read_b128 a[20:23], v244 offset:5120\n"
        "v_cvt_pk_bf16_f32 v90, v84, v85\n"
        "v_mfma_f32_16x16x32_bf16 v[140:143], a[36:39], a[248:251], v[140:143]\n"
        "ds_read_b128 a[24:27], v244 offset:9216\n"
        "v_cvt_pk_bf16_f32 v91, v86, v87\n"
        "v_mfma_f32_16x16x32_bf16 v[140:143], a[40:43], a[244:247], v[140:143]\n"
        "ds_read_b128 a[28:31], v244 offset:13312\n"
        "global_load_lds_dwordx4 v254, s[60:61] offset:1024 sc1\n"
        "v_mfma_f32_16x16x32_bf16 v[140:143], a[44:47], a[240:243], v[140:143]\n"
        "ds_read_b128 a[192:195], v244 offset:2048\n"
        "v_and_b32 v80, 0xffff0000, v69\n"
        "v_mfma_f32_16x16x32_bf16 v[140:143], a[48:51], a[236:239], v[140:143]\n"
        "s_waitcnt lgkmcnt(7)\n"
        "ds_read_b128 a[196:199], v244 offset:6144\n"
        "v_mfma_f32_16x16x32_bf16 v[140:143], a[52:55], a[232:235], v[140:143]\n"
        "ds_read_b128 a[200:203], v244 offset:10240\n"
        "global_load_lds_dwordx4 v254, s[60:61] offset:2048 sc1\n"
        "v_mfma_f32_16x16x32_bf16 v[140:143], a[56:59], a[228:231], v[140:143]\n"
        "s_waitcnt lgkmcnt(7)\n"
        "ds_read_b128 a[204:207], v244 offset:14336\n"
        "v_mfma_f32_16x16x32_bf16 v[140:143], a[60:63], a[224:227], v[140:143]\n"
        "ds_read_b128 a[208:211], v244 offset:3072\n"
        "v_lshlrev_b32 v81, 16, v69\n"
        "v_mfma_f32_16x16x32_bf16 v[0:3], a[96:99], a[0:3], 0\n"
        "ds_read_b128 a[212:215], v244 offset:7168\n"
        "global_load_lds_dwordx4 v254, s[60:61] offset:3072 sc1\n"
        "s_add_u32 m0, s73, 0x18000\n"
        "v_mfma_f32_16x16x32_bf16 v[0:3], a[104:107], a[4:7], v[0:3]\n"
        "ds_read_b128 a[216:219], v244 offset:11264\n"
        "s_add_u32 s60, s22, s66\n"
        "s_addc_u32 s61, s23, 0\n"
        "v_mfma_f32_16x16x32_bf16 v[0:3], a[112:115], a[8:11], v[0:3]\n"
        "ds_read_b128 a[220:223], v244 offset:15360\n"
        "s_xor_b32 s66, s17, 4\n"
        "s_lshl_b32 s66, s66, 15\n"
        "v_mfma_f32_16x16x32_bf16 v[0:3], a[120:123], a[12:15], v[0:3]\n"
        "v_permlane16_swap_b32 v88, v90\n"
        "v_mfma_f32_16x16x32_bf16 v[4:7], a[124:127], a[12:15], 0\n"
        "s_waitcnt lgkmcnt(7)\n"
        "v_permlane16_swap_b32 v89, v91\n"
        "v_mfma_f32_16x16x32_bf16 v[4:7], a[116:119], a[8:11], v[4:7]\n"
        "global_load_lds_dwordx4 v144, s[60:61] offset:0\n"
        "v_and_b32 v82, 0xffff0000, v68\n"
        "v_mfma_f32_16x16x32_bf16 v[4:7], a[108:111], a[4:7], v[4:7]\n"
        "v_lshlrev_b32 v83, 16, v68\n"
        "v_mul_f32 v80, s54, v80\n"
        "v_mfma_f32_16x16x32_bf16 v[4:7], a[100:103], a[0:3], v[4:7]\n"
        "v_mul_f32 v81, s54, v81\n"
        "v_mul_f32 v82, s54, v82\n"
        "v_mfma_f32_16x16x32_bf16 v[12:15], a[100:103], a[16:19], 0\n"
        "global_load_lds_dwordx4 v145, s[60:61] offset:1024\n"
        "v_mul_f32 v83, s54, v83\n"
        "v_mfma_f32_16x16x32_bf16 v[12:15], a[108:111], a[20:23], v[12:15]\n"
        "v_cvt_pk_bf16_f32 v80, 0, v80\n"
        "v_cvt_pk_bf16_f32 v81, 0, v81\n"
        "v_mfma_f32_16x16x32_bf16 v[12:15], a[116:119], a[24:27], v[12:15]\n"
        "v_cvt_pk_bf16_f32 v82, 0, v82\n"
        "v_cvt_pk_bf16_f32 v83, 0, v83\n"
        "v_mfma_f32_16x16x32_bf16 v[12:15], a[124:127], a[28:31], v[12:15]\n"
        "global_load_lds_dwordx4 v146, s[60:61] offset:2048\n"
        "v_cvt_pk_bf16_f32 v104, 0, v104\n"
        "v_mfma_f32_16x16x32_bf16 v[8:11], a[120:123], a[28:31], 0\n"
        "v_cvt_pk_bf16_f32 v105, 0, v105\n"
        "v_cvt_pk_bf16_f32 v106, 0, v106\n"
        "v_mfma_f32_16x16x32_bf16 v[8:11], a[112:115], a[24:27], v[8:11]\n"
        "s_waitcnt lgkmcnt(0)\n"
        "v_cvt_pk_bf16_f32 v107, 0, v107\n"
        "v_add_f32 v80, v80, v104\n"
        "v_mfma_f32_16x16x32_bf16 v[8:11], a[104:107], a[20:23], v[8:11]\n"
        "global_load_lds_dwordx4 v147, s[60:61] offset:3072\n"
        "s_add_u32 m0, s73, 0x1c000\n"
        "s_lshl2_add_u32 s15, s74, s10\n"
        "v_mfma_f32_16x16x32_bf16 v[8:11], a[96:99], a[16:19], v[8:11]\n"
        "s_add_u32 s60, s68, s66\n"
        "s_addc_u32 s61, s69, 0\n"
        "s_waitcnt vmcnt(16)\n"
        "v_mfma_f32_16x16x32_bf16 v[16:19], a[96:99], a[192:195], 0\n"
        "s_barrier\n"
        "v_mfma_f32_16x16x32_bf16 v[16:19], a[104:107], a[196:199], v[16:19]\n"
        "ds_read_b64_tr_b16 a[80:81], v153 offset:0\n"
        "global_load_lds_dwordx4 v254, s[60:61] offset:0 sc1\n"
        "s_xor_b32 s66, s29, 0x1a0000\n"
        "v_mfma_f32_16x16x32_bf16 v[16:19], a[112:115], a[200:203], v[16:19]\n"
        "ds_read_b64_tr_b16 a[82:83], v153 offset:4096\n"
        "s_mul_i32 s16, s13, 263168\n"
        "s_lshl_b32 s15, s15, 14\n"
        "v_mfma_f32_16x16x32_bf16 v[16:19], a[120:123], a[204:207], v[16:19]\n"
        "ds_read_b64_tr_b16 a[84:85], v154 offset:0\n"
        "s_add_u32 s16, s16, s15\n"
        "s_add_u32 s16, s16, s73\n"
        "v_mfma_f32_16x16x32_bf16 v[20:23], a[124:127], a[204:207], 0\n"
        "ds_read_b64_tr_b16 a[86:87], v154 offset:4096\n"
        "global_load_lds_dwordx4 v254, s[60:61] offset:1024 sc1\n"
        "s_add_u32 s16, s16, 0x4000000\n"
        "v_mfma_f32_16x16x32_bf16 v[20:23], a[116:119], a[200:203], v[20:23]\n"
        "ds_read_b64_tr_b16 a[88:89], v153 offset:8192\n"
        "LHC3_%=:\n"
        "s_cbranch_execz LHRC_%=\n"
        "v_add_f32 v81, v81, v105\n"
        "v_mfma_f32_16x16x32_bf16 v[20:23], a[108:111], a[196:199], v[20:23]\n"
        "ds_read_b64_tr_b16 a[90:91], v153 offset:12288\n"
        "v_add_f32 v82, v82, v106\n"
        "v_mfma_f32_16x16x32_bf16 v[20:23], a[100:103], a[192:195], v[20:23]\n"
        "ds_read_b64_tr_b16 a[92:93], v154 offset:8192\n"
        "global_load_lds_dwordx4 v254, s[60:61] offset:2048 sc1\n"
        "v_mfma_f32_16x16x32_bf16 v[28:31], a[100:103], a[208:211], 0\n"
        "ds_read_b64_tr_b16 a[94:95], v154 offset:12288\n"
        "v_add_f32 v83, v83, v107\n"
        "v_mfma_f32_16x16x32_bf16 v[28:31], a[108:111], a[212:215], v[28:31]\n"
        "ds_read_b128 a[0:3], v244 offset:16384\n"
        "v_cvt_pk_bf16_f32 v92, v80, v81\n"
        "v_mfma_f32_16x16x32_bf16 v[28:31], a[116:119], a[216:219], v[28:31]\n"
        "ds_read_b128 a[4:7], v244 offset:20480\n"
        "global_load_lds_dwordx4 v254, s[60:61] offset:3072 sc1\n"
        "s_add_u32 m0, s73, 0x20000\n"
        "v_mfma_f32_16x16x32_bf16 v[28:31], a[124:127], a[220:223], v[28:31]\n"
        "ds_read_b128 a[8:11], v244 offset:24576\n"
        "s_add_u32 s60, s22, s66\n"
        "s_addc_u32 s61, s23, 0\n"
        "v_mfma_f32_16x16x32_bf16 v[24:27], a[120:123], a[220:223], 0\n"
        "ds_read_b128 a[12:15], v244 offset:28672\n"
        "s_xor_b32 s66, s17, 5\n"
        "s_lshl_b32 s66, s66, 15\n"
        "v_mfma_f32_16x16x32_bf16 v[24:27], a[112:115], a[216:219], v[24:27]\n"
        "ds_read_b128 a[16:19], v244 offset:17408\n"
        "global_load_lds_dwordx4 v144, s[60:61] offset:0\n"
        "v_mfma_f32_16x16x32_bf16 v[24:27], a[104:107], a[212:215], v[24:27]\n"
        "ds_read_b128 a[20:23], v244 offset:21504\n"
        "v_cvt_pk_bf16_f32 v93, v82, v83\n"
        "v_mfma_f32_16x16x32_bf16 v[24:27], a[96:99], a[208:211], v[24:27]\n"
        "ds_read_b128 a[24:27], v244 offset:25600\n"
        "ds_read_b128 a[28:31], v244 offset:29696\n"
        "ds_read_b128 a[192:195], v244 offset:18432\n"
        "global_load_lds_dwordx4 v145, s[60:61] offset:1024\n"
        "s_waitcnt lgkmcnt(8)\n"
        "v_mfma_f32_16x16x32_bf16 v[0:3], a[128:131], a[0:3], v[0:3]\n"
        "ds_read_b128 a[196:199], v244 offset:22528\n"
        "s_waitcnt lgkmcnt(8)\n"
        "v_mfma_f32_16x16x32_bf16 v[0:3], a[136:139], a[4:7], v[0:3]\n"
        "ds_read_b128 a[200:203], v244 offset:26624\n"
        "s_waitcnt lgkmcnt(8)\n"
        "v_mfma_f32_16x16x32_bf16 v[0:3], a[144:147], a[8:11], v[0:3]\n"
        "ds_read_b128 a[204:207], v244 offset:30720\n"
        "s_waitcnt lgkmcnt(8)\n"
        "v_mfma_f32_16x16x32_bf16 v[0:3], a[152:155], a[12:15], v[0:3]\n"
        "ds_read_b128 a[208:211], v244 offset:19456\n"
        "global_load_lds_dwordx4 v146, s[60:61] offset:2048\n"
        "v_mfma_f32_16x16x32_bf16 v[4:7], a[156:159], a[12:15], v[4:7]\n"
        "ds_read_b128 a[212:215], v244 offset:23552\n"
        "v_and_b32 v84, 0xffff0000, v71\n"
        "v_mfma_f32_16x16x32_bf16 v[4:7], a[148:151], a[8:11], v[4:7]\n"
        "ds_read_b128 a[216:219], v244 offset:27648\n"
        "v_lshlrev_b32 v85, 16, v71\n"
        "v_mfma_f32_16x16x32_bf16 v[4:7], a[140:143], a[4:7], v[4:7]\n"
        "ds_read_b128 a[220:223], v244 offset:31744\n"
        "global_load_lds_dwordx4 v147, s[60:61] offset:3072\n"
        "s_add_u32 m0, s73, 0x24000\n"
        "v_mfma_f32_16x16x32_bf16 v[4:7], a[132:135], a[0:3], v[4:7]\n"
        "s_add_u32 s60, s68, s66\n"
        "s_addc_u32 s61, s69, 0\n"
        "s_waitcnt lgkmcnt(7)\n"
        "v_mfma_f32_16x16x32_bf16 v[12:15], a[132:135], a[16:19], v[12:15]\n"
        "s_xor_b32 s66, s29, 0x1c0000\n"
        "v_and_b32 v86, 0xffff0000, v70\n"
        "v_lshlrev_b32 v87, 16, v70\n"
        "v_mfma_f32_16x16x32_bf16 v[12:15], a[140:143], a[20:23], v[12:15]\n"
        "global_load_lds_dwordx4 v254, s[60:61] offset:0 sc1\n"
        "v_mfma_f32_16x16x32_bf16 v[12:15], a[148:151], a[24:27], v[12:15]\n"
        "v_mul_f32 v84, s54, v84\n"
        "v_mul_f32 v85, s54, v85\n"
        "v_mfma_f32_16x16x32_bf16 v[12:15], a[156:159], a[28:31], v[12:15]\n"
        "v_mul_f32 v86, s54, v86\n"
        "v_mul_f32 v87, s54, v87\n"
        "v_mfma_f32_16x16x32_bf16 v[8:11], a[152:155], a[28:31], v[8:11]\n"
        "global_load_lds_dwordx4 v254, s[60:61] offset:1024 sc1\n"
        "v_mfma_f32_16x16x32_bf16 v[8:11], a[144:147], a[24:27], v[8:11]\n"
        "s_waitcnt lgkmcnt(1)\n"
        "v_cvt_pk_bf16_f32 v84, 0, v84\n"
        "v_cvt_pk_bf16_f32 v85, 0, v85\n"
        "v_mfma_f32_16x16x32_bf16 v[8:11], a[136:139], a[20:23], v[8:11]\n"
        "v_cvt_pk_bf16_f32 v86, 0, v86\n"
        "v_cvt_pk_bf16_f32 v87, 0, v87\n"
        "v_mfma_f32_16x16x32_bf16 v[8:11], a[128:131], a[16:19], v[8:11]\n"
        "s_waitcnt vmcnt(18)\n"
        "global_load_lds_dwordx4 v254, s[60:61] offset:2048 sc1\n"
        "v_mfma_f32_16x16x32_bf16 v[16:19], a[128:131], a[192:195], v[16:19]\n"
        "s_barrier\n"
        "v_mfma_f32_16x16x32_bf16 v[16:19], a[136:139], a[196:199], v[16:19]\n"
        "ds_read_b64_tr_b16 a[160:161], v153 offset:32768\n"
        "v_cvt_pk_bf16_f32 v108, 0, v108\n"
        "v_mfma_f32_16x16x32_bf16 v[16:19], a[144:147], a[200:203], v[16:19]\n"
        "ds_read_b64_tr_b16 a[162:163], v153 offset:36864\n"
        "global_load_lds_dwordx4 v254, s[60:61] offset:3072 sc1\n"
        "s_add_u32 m0, s73, 0x10000\n"
        "v_mfma_f32_16x16x32_bf16 v[16:19], a[152:155], a[204:207], v[16:19]\n"
        "ds_read_b64_tr_b16 a[164:165], v154 offset:32768\n"
        "s_add_u32 s60, s22, s66\n"
        "s_addc_u32 s61, s23, 0\n"
        "v_mfma_f32_16x16x32_bf16 v[20:23], a[156:159], a[204:207], v[20:23]\n"
        "ds_read_b64_tr_b16 a[166:167], v154 offset:36864\n"
        "s_xor_b32 s66, s17, 6\n"
        "s_lshl_b32 s66, s66, 15\n"
        "v_mfma_f32_16x16x32_bf16 v[20:23], a[148:151], a[200:203], v[20:23]\n"
        "ds_read_b64_tr_b16 a[168:169], v153 offset:40960\n"
        "global_load_lds_dwordx4 v144, s[60:61] offset:0\n"
        "v_mfma_f32_16x16x32_bf16 v[20:23], a[140:143], a[196:199], v[20:23]\n"
        "ds_read_b64_tr_b16 a[170:171], v153 offset:45056\n"
        "v_cvt_pk_bf16_f32 v109, 0, v109\n"
        "v_mfma_f32_16x16x32_bf16 v[20:23], a[132:135], a[192:195], v[20:23]\n"
        "ds_read_b64_tr_b16 a[172:173], v154 offset:40960\n"
        "v_cvt_pk_bf16_f32 v110, 0, v110\n"
        "v_mfma_f32_16x16x32_bf16 v[28:31], a[132:135], a[208:211], v[28:31]\n"
        "ds_read_b64_tr_b16 a[174:175], v154 offset:45056\n"
        "global_load_lds_dwordx4 v145, s[60:61] offset:1024\n"
        "v_mfma_f32_16x16x32_bf16 v[28:31], a[140:143], a[212:215], v[28:31]\n"
        "ds_read_b128 a[0:3], v254 offset:16384\n"
        "v_cvt_pk_bf16_f32 v111, 0, v111\n"
        "v_mfma_f32_16x16x32_bf16 v[28:31], a[148:151], a[216:219], v[28:31]\n"
        "ds_read_b128 a[4:7], v254 offset:20480\n"
        "s_waitcnt lgkmcnt(8)\n"
        "v_mfma_f32_16x16x32_bf16 v[28:31], a[156:159], a[220:223], v[28:31]\n"
        "ds_read_b128 a[8:11], v254 offset:24576\n"
        "global_load_lds_dwordx4 v146, s[60:61] offset:2048\n"
        "v_mfma_f32_16x16x32_bf16 v[24:27], a[152:155], a[220:223], v[24:27]\n"
        "ds_read_b128 a[12:15], v254 offset:28672\n"
        "v_add_f32 v84, v84, v108\n"
        "v_mfma_f32_16x16x32_bf16 v[24:27], a[144:147], a[216:219], v[24:27]\n"
        "ds_read_b128 a[16:19], v254 offset:17408\n"
        "v_add_f32 v85, v85, v109\n"
        "v_mfma_f32_16x16x32_bf16 v[24:27], a[136:139], a[212:215], v[24:27]\n"
        "ds_read_b128 a[20:23], v254 offset:21504\n"
        "global_load_lds_dwordx4 v147, s[60:61] offset:3072\n"
        "s_add_u32 m0, s73, 0x0\n"
        "v_mfma_f32_16x16x32_bf16 v[24:27], a[128:131], a[208:211], v[24:27]\n"
        "ds_read_b128 a[24:27], v254 offset:25600\n"
        "ds_read_b128 a[28:31], v254 offset:29696\n"
        "s_add_u32 s60, s68, s66\n"
        "s_addc_u32 s61, s69, 0\n"
        "ds_read_b128 a[192:195], v254 offset:18432\n"
        "s_xor_b32 s66, s29, 0x1e0000\n"
        "s_waitcnt lgkmcnt(8)\n"
        "v_mfma_f32_16x16x32_bf16 v[0:3], v[160:163], a[0:3], v[0:3]\n"
        "ds_read_b128 a[196:199], v254 offset:22528\n"
        "s_waitcnt lgkmcnt(8)\n"
        "v_mfma_f32_16x16x32_bf16 v[0:3], v[168:171], a[4:7], v[0:3]\n"
        "ds_read_b128 a[200:203], v254 offset:26624\n"
        "s_waitcnt lgkmcnt(8)\n"
        "v_mfma_f32_16x16x32_bf16 v[0:3], v[176:179], a[8:11], v[0:3]\n"
        "ds_read_b128 a[204:207], v254 offset:30720\n"
        "s_waitcnt lgkmcnt(8)\n"
        "v_mfma_f32_16x16x32_bf16 v[0:3], v[184:187], a[12:15], v[0:3]\n"
        "ds_read_b128 a[208:211], v254 offset:19456\n"
        "global_load_lds_dwordx4 v254, s[60:61] offset:0 sc1\n"
        "v_mfma_f32_16x16x32_bf16 v[4:7], v[188:191], a[12:15], v[4:7]\n"
        "ds_read_b128 a[212:215], v254 offset:23552\n"
        "s_add_u32 s28, s38, s16\n"
        "s_addc_u32 s29, s39, 0\n"
        "v_mfma_f32_16x16x32_bf16 v[4:7], v[180:183], a[8:11], v[4:7]\n"
        "ds_read_b128 a[216:219], v254 offset:27648\n"
        "v_add_f32 v86, v86, v110\n"
        "v_mfma_f32_16x16x32_bf16 v[4:7], v[172:175], a[4:7], v[4:7]\n"
        "ds_read_b128 a[220:223], v254 offset:31744\n"
        "global_load_lds_dwordx4 v254, s[60:61] offset:1024 sc1\n"
        "v_mfma_f32_16x16x32_bf16 v[4:7], v[164:167], a[0:3], v[4:7]\n"
        "s_waitcnt lgkmcnt(7)\n"
        "v_add_f32 v87, v87, v111\n"
        "v_cvt_pk_bf16_f32 v94, v84, v85\n"
        "v_mfma_f32_16x16x32_bf16 v[12:15], v[164:167], a[16:19], v[12:15]\n"
        "v_cvt_pk_bf16_f32 v95, v86, v87\n"
        "v_and_b32 v80, 0xffff0000, v73\n"
        "v_mfma_f32_16x16x32_bf16 v[12:15], v[172:175], a[20:23], v[12:15]\n"
        "v_permlane16_swap_b32 v92, v94\n"
        "v_mfma_f32_16x16x32_bf16 v[12:15], v[180:183], a[24:27], v[12:15]\n"
        "v_permlane16_swap_b32 v93, v95\n"
        "v_mfma_f32_16x16x32_bf16 v[12:15], v[188:191], a[28:31], v[12:15]\n"
        "global_load_lds_dwordx4 v254, s[60:61] offset:2048 sc1\n"
        "v_lshlrev_b32 v81, 16, v73\n"
        "v_mfma_f32_16x16x32_bf16 v[8:11], v[184:187], a[28:31], v[8:11]\n"
        "v_and_b32 v82, 0xffff0000, v72\n"
        "v_lshlrev_b32 v83, 16, v72\n"
        "v_mfma_f32_16x16x32_bf16 v[8:11], v[176:179], a[24:27], v[8:11]\n"
        "s_waitcnt lgkmcnt(1)\n"
        "v_mul_f32 v80, s54, v80\n"
        "v_mul_f32 v81, s54, v81\n"
        "v_mfma_f32_16x16x32_bf16 v[8:11], v[168:171], a[20:23], v[8:11]\n"
        "global_load_lds_dwordx4 v254, s[60:61] offset:3072 sc1\n"
        "s_add_u32 m0, s73, 0x14000\n"
        "v_mul_f32 v82, s54, v82\n"
        "v_mfma_f32_16x16x32_bf16 v[8:11], v[160:163], a[16:19], v[8:11]\n"
        "s_waitcnt vmcnt(20)\n"
        "s_add_u32 s60, s22, s66\n"
        "s_addc_u32 s61, s23, 0\n"
        "v_mfma_f32_16x16x32_bf16 v[16:19], v[160:163], a[192:195], v[16:19]\n"
        "s_barrier\n"
        "v_mfma_f32_16x16x32_bf16 v[16:19], v[168:171], a[196:199], v[16:19]\n"
        "ds_read_b64_tr_b16 a[176:177], v155 offset:32768\n"
        "global_load_lds_dwordx4 v144, s[60:61] offset:0\n"
        "s_xor_b32 s66, s17, 7\n"
        "v_mfma_f32_16x16x32_bf16 v[16:19], v[176:179], a[200:203], v[16:19]\n"
        "ds_read_b64_tr_b16 a[178:179], v155 offset:36864\n"
        "s_lshl_b32 s66, s66, 15\n"
        "v_mul_f32 v83, s54, v83\n"
        "v_mfma_f32_16x16x32_bf16 v[16:19], v[184:187], a[204:207], v[16:19]\n"
        "ds_read_b64_tr_b16 a[180:181], v156 offset:32768\n"
        "v_cvt_pk_bf16_f32 v80, 0, v80\n"
        "v_mfma_f32_16x16x32_bf16 v[20:23], v[188:191], a[204:207], v[20:23]\n"
        "ds_read_b64_tr_b16 a[182:183], v156 offset:36864\n"
        "global_load_lds_dwordx4 v145, s[60:61] offset:1024\n"
        "v_mfma_f32_16x16x32_bf16 v[20:23], v[180:183], a[200:203], v[20:23]\n"
        "ds_read_b64_tr_b16 a[184:185], v155 offset:40960\n"
        "v_cvt_pk_bf16_f32 v81, 0, v81\n"
        "v_mfma_f32_16x16x32_bf16 v[20:23], v[172:175], a[196:199], v[20:23]\n"
        "ds_read_b64_tr_b16 a[186:187], v155 offset:45056\n"
        "v_cvt_pk_bf16_f32 v82, 0, v82\n"
        "v_mfma_f32_16x16x32_bf16 v[20:23], v[164:167], a[192:195], v[20:23]\n"
        "ds_read_b64_tr_b16 a[188:189], v156 offset:40960\n"
        "global_load_lds_dwordx4 v146, s[60:61] offset:2048\n"
        "v_mfma_f32_16x16x32_bf16 v[28:31], v[164:167], a[208:211], v[28:31]\n"
        "ds_read_b64_tr_b16 a[190:191], v156 offset:45056\n"
        "v_cvt_pk_bf16_f32 v83, 0, v83\n"
        "v_mfma_f32_16x16x32_bf16 v[28:31], v[172:175], a[212:215], v[28:31]\n"
        "ds_read_b128 a[0:3], v254 offset:49152\n"
        "v_cvt_pk_bf16_f32 v128, 0, v128\n"
        "v_mfma_f32_16x16x32_bf16 v[28:31], v[180:183], a[216:219], v[28:31]\n"
        "ds_read_b128 a[4:7], v254 offset:53248\n"
        "s_waitcnt lgkmcnt(8)\n"
        "v_mfma_f32_16x16x32_bf16 v[28:31], v[188:191], a[220:223], v[28:31]\n"
        "ds_read_b128 a[8:11], v254 offset:57344\n"
        "global_load_lds_dwordx4 v147, s[60:61] offset:3072\n"
        "s_add_u32 m0, s73, 0x8000\n"
        "v_mfma_f32_16x16x32_bf16 v[24:27], v[184:187], a[220:223], v[24:27]\n"
        "ds_read_b128 a[12:15], v254 offset:61440\n"
        "s_add_u32 s60, s68, s66\n"
        "s_addc_u32 s61, s69, 0\n"
        "v_mfma_f32_16x16x32_bf16 v[24:27], v[176:179], a[216:219], v[24:27]\n"
        "ds_read_b128 a[16:19], v254 offset:50176\n"
        "s_sub_u32 s66, 15, s12\n"
        "v_cvt_pk_bf16_f32 v129, 0, v129\n"
        "v_mfma_f32_16x16x32_bf16 v[24:27], v[168:171], a[212:215], v[24:27]\n"
        "ds_read_b128 a[20:23], v254 offset:54272\n"
        "global_load_lds_dwordx4 v254, s[60:61] offset:0 sc1\n"
        "v_mfma_f32_16x16x32_bf16 v[24:27], v[160:163], a[208:211], v[24:27]\n"
        "ds_read_b128 a[24:27], v254 offset:58368\n"
        "ds_read_b128 a[28:31], v254 offset:62464\n"
        "ds_read_b128 a[192:195], v254 offset:51200\n"
        "s_cmp_lt_u32 s75, 2\n"
        "s_cselect_b32 s66, s12, s66\n"
        "s_waitcnt lgkmcnt(8)\n"
        "v_mfma_f32_16x16x32_bf16 v[0:3], v[192:195], a[0:3], v[0:3]\n"
        "ds_read_b128 a[196:199], v254 offset:55296\n"
        "s_mul_i32 s66, s66, 1052672\n"
        "s_waitcnt lgkmcnt(8)\n"
        "v_mfma_f32_16x16x32_bf16 v[0:3], v[200:203], a[4:7], v[0:3]\n"
        "ds_read_b128 a[200:203], v254 offset:59392\n"
        "s_add_u32 s66, s66, s67\n"
        "s_waitcnt lgkmcnt(8)\n"
        "v_mfma_f32_16x16x32_bf16 v[0:3], v[208:211], a[8:11], v[0:3]\n"
        "ds_read_b128 a[204:207], v254 offset:63488\n"
        "s_and_b32 s67, s76, 1\n"
        "s_waitcnt lgkmcnt(8)\n"
        "v_mfma_f32_16x16x32_bf16 v[0:3], v[216:219], a[12:15], v[0:3]\n"
        "ds_read_b128 a[208:211], v254 offset:52224\n"
        "global_load_lds_dwordx4 v254, s[60:61] offset:1024 sc1\n"
        "s_lshl_b32 s67, s67, 12\n"
        "v_mfma_f32_16x16x32_bf16 v[4:7], v[220:223], a[12:15], v[4:7]\n"
        "ds_read_b128 a[212:215], v254 offset:56320\n"
        "s_add_u32 s68, s20, s66\n"
        "s_addc_u32 s69, s21, 0\n"
        "v_mfma_f32_16x16x32_bf16 v[4:7], v[212:215], a[8:11], v[4:7]\n"
        "ds_read_b128 a[216:219], v254 offset:60416\n"
        "s_xor_b32 s66, s17, 0\n"
        "s_lshl_b32 s66, s66, 15\n"
        "v_mfma_f32_16x16x32_bf16 v[4:7], v[204:207], a[4:7], v[4:7]\n"
        "ds_read_b128 a[220:223], v254 offset:64512\n"
        "global_load_lds_dwordx4 v254, s[60:61] offset:2048 sc1\n"
        "v_mfma_f32_16x16x32_bf16 v[4:7], v[196:199], a[0:3], v[4:7]\n"
        "s_waitcnt lgkmcnt(7)\n"
        "v_cvt_pk_bf16_f32 v130, 0, v130\n"
        "v_cvt_pk_bf16_f32 v131, 0, v131\n"
        "v_mfma_f32_16x16x32_bf16 v[12:15], v[196:199], a[16:19], v[12:15]\n"
        "v_add_f32 v80, v80, v128\n"
        "v_add_f32 v81, v81, v129\n"
        "v_mfma_f32_16x16x32_bf16 v[12:15], v[204:207], a[20:23], v[12:15]\n"
        "global_load_lds_dwordx4 v254, s[60:61] offset:3072 sc1\n"
        "s_add_u32 m0, s73, 0x4000\n"
        "v_mfma_f32_16x16x32_bf16 v[12:15], v[212:215], a[24:27], v[12:15]\n"
        "s_add_u32 s60, s68, s66\n"
        "s_addc_u32 s61, s69, 0\n"
        "s_xor_b32 s66, s17, 1\n"
        "v_mfma_f32_16x16x32_bf16 v[12:15], v[220:223], a[28:31], v[12:15]\n"
        "s_lshl_b32 s66, s66, 15\n"
        "v_add_f32 v82, v82, v130\n"
        "v_add_f32 v83, v83, v131\n"
        "v_mfma_f32_16x16x32_bf16 v[8:11], v[216:219], a[28:31], v[8:11]\n"
        "v_and_b32 v84, 0xffff0000, v75\n"
        "v_lshlrev_b32 v85, 16, v75\n"
        "v_mfma_f32_16x16x32_bf16 v[8:11], v[208:211], a[24:27], v[8:11]\n"
        "s_waitcnt lgkmcnt(1)\n"
        "v_and_b32 v86, 0xffff0000, v74\n"
        "v_lshlrev_b32 v87, 16, v74\n"
        "v_mfma_f32_16x16x32_bf16 v[8:11], v[200:203], a[20:23], v[8:11]\n"
        "v_mul_f32 v84, s54, v84\n"
        "v_mul_f32 v85, s54, v85\n"
        "v_mfma_f32_16x16x32_bf16 v[8:11], v[192:195], a[16:19], v[8:11]\n"
        "s_waitcnt vmcnt(20)\n"
        "v_mul_f32 v86, s54, v86\n"
        "v_mul_f32 v87, s54, v87\n"
        "v_mfma_f32_16x16x32_bf16 v[16:19], v[192:195], a[192:195], v[16:19]\n"
        "s_barrier\n"
        "v_mfma_f32_16x16x32_bf16 v[16:19], v[200:203], a[196:199], v[16:19]\n"
        "ds_read_b64_tr_b16 a[32:33], v157 offset:0\n"
        "global_load_lds_dwordx4 v254, s[60:61] offset:0 sc1\n"
        "v_mfma_f32_16x16x32_bf16 v[16:19], v[208:211], a[200:203], v[16:19]\n"
        "ds_read_b64_tr_b16 a[34:35], v157 offset:4096\n"
        "v_cvt_pk_bf16_f32 v84, 0, v84\n"
        "v_mfma_f32_16x16x32_bf16 v[16:19], v[216:219], a[204:207], v[16:19]\n"
        "ds_read_b64_tr_b16 a[36:37], v158 offset:0\n"
        "v_cvt_pk_bf16_f32 v85, 0, v85\n"
        "v_mfma_f32_16x16x32_bf16 v[20:23], v[220:223], a[204:207], v[20:23]\n"
        "ds_read_b64_tr_b16 a[38:39], v158 offset:4096\n"
        "global_load_lds_dwordx4 v254, s[60:61] offset:1024 sc1\n"
        "v_mfma_f32_16x16x32_bf16 v[20:23], v[212:215], a[200:203], v[20:23]\n"
        "ds_read_b64_tr_b16 a[40:41], v157 offset:8192\n"
        "v_cvt_pk_bf16_f32 v86, 0, v86\n"
        "v_mfma_f32_16x16x32_bf16 v[20:23], v[204:207], a[196:199], v[20:23]\n"
        "ds_read_b64_tr_b16 a[42:43], v157 offset:12288\n"
        "v_cvt_pk_bf16_f32 v87, 0, v87\n"
        "v_mfma_f32_16x16x32_bf16 v[20:23], v[196:199], a[192:195], v[20:23]\n"
        "ds_read_b64_tr_b16 a[44:45], v158 offset:8192\n"
        "global_load_lds_dwordx4 v254, s[60:61] offset:2048 sc1\n"
        "v_mfma_f32_16x16x32_bf16 v[28:31], v[196:199], a[208:211], v[28:31]\n"
        "ds_read_b64_tr_b16 a[46:47], v158 offset:12288\n"
        "v_cvt_pk_bf16_f32 v132, 0, v132\n"
        "v_mfma_f32_16x16x32_bf16 v[28:31], v[204:207], a[212:215], v[28:31]\n"
        "ds_read_b128 a[0:3], v244 offset:49152\n"
        "v_cvt_pk_bf16_f32 v133, 0, v133\n"
        "v_mfma_f32_16x16x32_bf16 v[28:31], v[212:215], a[216:219], v[28:31]\n"
        "ds_read_b128 a[4:7], v244 offset:53248\n"
        "s_waitcnt lgkmcnt(8)\n"
        "v_mfma_f32_16x16x32_bf16 v[28:31], v[220:223], a[220:223], v[28:31]\n"
        "ds_read_b128 a[8:11], v244 offset:57344\n"
        "global_load_lds_dwordx4 v254, s[60:61] offset:3072 sc1\n"
        "s_add_u32 m0, s73, 0x18000\n"
        "v_mfma_f32_16x16x32_bf16 v[24:27], v[216:219], a[220:223], v[24:27]\n"
        "ds_read_b128 a[12:15], v244 offset:61440\n"
        "s_add_u32 s60, s68, s66\n"
        "s_addc_u32 s61, s69, 0\n"
        "v_mfma_f32_16x16x32_bf16 v[24:27], v[208:211], a[216:219], v[24:27]\n"
        "ds_read_b128 a[16:19], v244 offset:50176\n"
        "s_xor_b32 s66, s17, 2\n"
        "s_lshl_b32 s66, s66, 15\n"
        "v_mfma_f32_16x16x32_bf16 v[24:27], v[200:203], a[212:215], v[24:27]\n"
        "ds_read_b128 a[20:23], v244 offset:54272\n"
        "global_load_lds_dwordx4 v254, s[60:61] offset:0 sc1\n"
        "v_mfma_f32_16x16x32_bf16 v[24:27], v[192:195], a[208:211], v[24:27]\n"
        "ds_read_b128 a[24:27], v244 offset:58368\n"
        "ds_read_b128 a[28:31], v244 offset:62464\n"
        "ds_read_b128 a[192:195], v244 offset:51200\n"
        "v_cvt_pk_bf16_f32 v134, 0, v134\n"
        "s_waitcnt lgkmcnt(8)\n"
        "v_mfma_f32_16x16x32_bf16 v[0:3], v[112:115], a[0:3], v[0:3]\n"
        "ds_read_b128 a[196:199], v244 offset:55296\n"
        "s_waitcnt lgkmcnt(8)\n"
        "v_mfma_f32_16x16x32_bf16 v[0:3], v[120:123], a[4:7], v[0:3]\n"
        "ds_read_b128 a[200:203], v244 offset:59392\n"
        "s_waitcnt lgkmcnt(8)\n"
        "v_mfma_f32_16x16x32_bf16 v[0:3], a[64:67], a[8:11], v[0:3]\n"
        "ds_read_b128 a[204:207], v244 offset:63488\n"
        "s_waitcnt lgkmcnt(8)\n"
        "v_mfma_f32_16x16x32_bf16 v[0:3], a[72:75], a[12:15], v[0:3]\n"
        "ds_read_b128 a[208:211], v244 offset:52224\n"
        "global_load_lds_dwordx4 v254, s[60:61] offset:1024 sc1\n"
        "v_mfma_f32_16x16x32_bf16 v[4:7], a[76:79], a[12:15], v[4:7]\n"
        "ds_read_b128 a[212:215], v244 offset:56320\n"
        "v_cvt_pk_bf16_f32 v135, 0, v135\n"
        "v_mfma_f32_16x16x32_bf16 v[4:7], a[68:71], a[8:11], v[4:7]\n"
        "ds_read_b128 a[216:219], v244 offset:60416\n"
        "v_add_f32 v84, v84, v132\n"
        "v_mfma_f32_16x16x32_bf16 v[4:7], v[124:127], a[4:7], v[4:7]\n"
        "ds_read_b128 a[220:223], v244 offset:64512\n"
        "global_load_lds_dwordx4 v254, s[60:61] offset:2048 sc1\n"
        "v_mfma_f32_16x16x32_bf16 v[4:7], v[116:119], a[0:3], v[4:7]\n"
        "s_waitcnt lgkmcnt(7)\n"
        "v_add_f32 v85, v85, v133\n"
        "v_add_f32 v86, v86, v134\n"
        "v_mfma_f32_16x16x32_bf16 v[12:15], v[116:119], a[16:19], v[12:15]\n"
        "v_add_f32 v87, v87, v135\n"
        "v_cvt_pk_bf16_f32 v136, 0, v136\n"
        "v_mfma_f32_16x16x32_bf16 v[12:15], v[124:127], a[20:23], v[12:15]\n"
        "global_load_lds_dwordx4 v254, s[60:61] offset:3072 sc1\n"
        "s_add_u32 m0, s73, 0xc000\n"
        "v_mfma_f32_16x16x32_bf16 v[12:15], a[68:71], a[24:27], v[12:15]\n"
        "s_add_u32 s60, s68, s66\n"
        "s_addc_u32 s61, s69, 0\n"
        "s_xor_b32 s66, s17, 3\n"
        "v_mfma_f32_16x16x32_bf16 v[12:15], a[76:79], a[28:31], v[12:15]\n"
        "s_lshl_b32 s66, s66, 15\n"
        "v_cvt_pk_bf16_f32 v137, 0, v137\n"
        "v_cvt_pk_bf16_f32 v138, 0, v138\n"
        "v_mfma_f32_16x16x32_bf16 v[8:11], a[72:75], a[28:31], v[8:11]\n"
        "v_cvt_pk_bf16_f32 v139, 0, v139\n"
        "v_cvt_pk_bf16_f32 v140, 0, v140\n"
        "v_mfma_f32_16x16x32_bf16 v[8:11], a[64:67], a[24:27], v[8:11]\n"
        "s_waitcnt lgkmcnt(1)\n"
        "v_cvt_pk_bf16_f32 v141, 0, v141\n"
        "v_cvt_pk_bf16_f32 v142, 0, v142\n"
        "v_mfma_f32_16x16x32_bf16 v[8:11], v[120:123], a[20:23], v[8:11]\n"
        "v_cvt_pk_bf16_f32 v143, 0, v143\n"
        "v_mfma_f32_16x16x32_bf16 v[8:11], v[112:115], a[16:19], v[8:11]\n"
        "s_waitcnt vmcnt(20)\n"
        "v_mfma_f32_16x16x32_bf16 v[16:19], v[112:115], a[192:195], v[16:19]\n"
        "s_barrier\n"
        "v_mfma_f32_16x16x32_bf16 v[16:19], v[120:123], a[196:199], v[16:19]\n"
        "ds_read_b64_tr_b16 a[48:49], v155 offset:0\n"
        "global_load_lds_dwordx4 v254, s[60:61] offset:0 sc1\n"
        "v_mfma_f32_16x16x32_bf16 v[16:19], a[64:67], a[200:203], v[16:19]\n"
        "ds_read_b64_tr_b16 a[50:51], v155 offset:4096\n"
        "v_mfma_f32_16x16x32_bf16 v[16:19], a[72:75], a[204:207], v[16:19]\n"
        "ds_read_b64_tr_b16 a[52:53], v156 offset:0\n"
        "v_mfma_f32_16x16x32_bf16 v[20:23], a[76:79], a[204:207], v[20:23]\n"
        "ds_read_b64_tr_b16 a[54:55], v156 offset:4096\n"
        "global_load_lds_dwordx4 v254, s[60:61] offset:1024 sc1\n"
        "v_mfma_f32_16x16x32_bf16 v[20:23], a[68:71], a[200:203], v[20:23]\n"
        "ds_read_b64_tr_b16 a[56:57], v155 offset:8192\n"
        "v_mfma_f32_16x16x32_bf16 v[20:23], v[124:127], a[196:199], v[20:23]\n"
        "ds_read_b64_tr_b16 a[58:59], v155 offset:12288\n"
        "v_mfma_f32_16x16x32_bf16 v[20:23], v[116:119], a[192:195], v[20:23]\n"
        "ds_read_b64_tr_b16 a[60:61], v156 offset:8192\n"
        "global_load_lds_dwordx4 v254, s[60:61] offset:2048 sc1\n"
        "v_mfma_f32_16x16x32_bf16 v[28:31], v[116:119], a[208:211], v[28:31]\n"
        "ds_read_b64_tr_b16 a[62:63], v156 offset:12288\n"
        "s_waitcnt lgkmcnt(8)\n"
        "v_mfma_f32_16x16x32_bf16 v[28:31], v[124:127], a[212:215], v[28:31]\n"
        "ds_read_b128 a[0:3], v245 offset:16384\n"
        "v_mfma_f32_16x16x32_bf16 v[28:31], a[68:71], a[216:219], v[28:31]\n"
        "ds_read_b128 a[4:7], v245 offset:20480\n"
        "global_load_lds_dwordx4 v254, s[60:61] offset:3072 sc1\n"
        "s_add_u32 m0, s73, 0x20000\n"
        "v_mfma_f32_16x16x32_bf16 v[28:31], a[76:79], a[220:223], v[28:31]\n"
        "ds_read_b128 a[8:11], v245 offset:24576\n"
        "s_add_u32 s60, s68, s66\n"
        "s_addc_u32 s61, s69, 0\n"
        "v_mfma_f32_16x16x32_bf16 v[24:27], a[72:75], a[220:223], v[24:27]\n"
        "ds_read_b128 a[12:15], v245 offset:28672\n"
        "s_xor_b32 s66, s17, 4\n"
        "s_lshl_b32 s66, s66, 15\n"
        "v_mfma_f32_16x16x32_bf16 v[24:27], a[64:67], a[216:219], v[24:27]\n"
        "ds_read_b128 a[16:19], v245 offset:17408\n"
        "global_load_lds_dwordx4 v254, s[60:61] offset:0 sc1\n"
        "v_mfma_f32_16x16x32_bf16 v[24:27], v[120:123], a[212:215], v[24:27]\n"
        "ds_read_b128 a[20:23], v245 offset:21504\n"
        "v_mfma_f32_16x16x32_bf16 v[24:27], v[112:115], a[208:211], v[24:27]\n"
        "ds_read_b128 a[24:27], v245 offset:25600\n"
        "ds_read_b128 a[28:31], v245 offset:29696\n"
        "global_load_lds_dwordx4 v254, s[60:61] offset:1024 sc1\n"
        "s_waitcnt lgkmcnt(7)\n"
        "v_mfma_f32_16x16x32_bf16 v[0:3], a[80:83], a[0:3], v[0:3]\n"
        "ds_read_b128 a[192:195], v245 offset:18432\n"
        "s_waitcnt lgkmcnt(7)\n"
        "v_mfma_f32_16x16x32_bf16 v[0:3], a[88:91], a[4:7], v[0:3]\n"
        "ds_read_b128 a[196:199], v245 offset:22528\n"
        "s_waitcnt lgkmcnt(7)\n"
        "v_mfma_f32_16x16x32_bf16 v[0:3], a[160:163], a[8:11], v[0:3]\n"
        "ds_read_b128 a[200:203], v245 offset:26624\n"
        "s_waitcnt lgkmcnt(7)\n"
        "v_mfma_f32_16x16x32_bf16 v[0:3], a[168:171], a[12:15], v[0:3]\n"
        "ds_read_b128 a[204:207], v245 offset:30720\n"
        "global_load_lds_dwordx4 v254, s[60:61] offset:2048 sc1\n"
        "v_mfma_f32_16x16x32_bf16 v[4:7], a[172:175], a[12:15], v[4:7]\n"
        "ds_read_b128 a[208:211], v245 offset:19456\n"
        "s_waitcnt lgkmcnt(8)\n"
        "v_mfma_f32_16x16x32_bf16 v[4:7], a[164:167], a[8:11], v[4:7]\n"
        "ds_read_b128 a[212:215], v245 offset:23552\n"
        "s_waitcnt lgkmcnt(8)\n"
        "v_mfma_f32_16x16x32_bf16 v[4:7], a[92:95], a[4:7], v[4:7]\n"
        "ds_read_b128 a[216:219], v245 offset:27648\n"
        "global_load_lds_dwordx4 v254, s[60:61] offset:3072 sc1\n"
        "s_add_u32 m0, s73, 0x1c000\n"
        "v_mfma_f32_16x16x32_bf16 v[4:7], a[84:87], a[0:3], v[4:7]\n"
        "ds_read_b128 a[220:223], v245 offset:31744\n"
        "s_add_u32 s60, s68, s66\n"
        "s_addc_u32 s61, s69, 0\n"
        "v_mfma_f32_16x16x32_bf16 v[12:15], a[84:87], a[16:19], v[12:15]\n"
        "s_xor_b32 s66, s17, 5\n"
        "s_lshl_b32 s66, s66, 15\n"
        "s_waitcnt lgkmcnt(7)\n"
        "v_mfma_f32_16x16x32_bf16 v[12:15], a[92:95], a[20:23], v[12:15]\n"
        "v_mfma_f32_16x16x32_bf16 v[12:15], a[164:167], a[24:27], v[12:15]\n"
        "v_mfma_f32_16x16x32_bf16 v[12:15], a[172:175], a[28:31], v[12:15]\n"
        "v_mfma_f32_16x16x32_bf16 v[8:11], a[168:171], a[28:31], v[8:11]\n"
        "v_mfma_f32_16x16x32_bf16 v[8:11], a[160:163], a[24:27], v[8:11]\n"
        "s_waitcnt lgkmcnt(2)\n"
        "v_mfma_f32_16x16x32_bf16 v[8:11], a[88:91], a[20:23], v[8:11]\n"
        "v_mfma_f32_16x16x32_bf16 v[8:11], a[80:83], a[16:19], v[8:11]\n"
        "s_waitcnt vmcnt(20)\n"
        "v_mfma_f32_16x16x32_bf16 v[16:19], a[80:83], a[192:195], v[16:19]\n"
        "s_barrier\n"
        "v_mfma_f32_16x16x32_bf16 v[16:19], a[88:91], a[196:199], v[16:19]\n"
        "ds_read_b64_tr_b16 a[224:225], v155 offset:16384\n"
        "global_load_lds_dwordx4 v254, s[60:61] offset:0 sc1\n"
        "v_mfma_f32_16x16x32_bf16 v[16:19], a[160:163], a[200:203], v[16:19]\n"
        "ds_read_b64_tr_b16 a[226:227], v155 offset:20480\n"
        "v_mfma_f32_16x16x32_bf16 v[16:19], a[168:171], a[204:207], v[16:19]\n"
        "ds_read_b64_tr_b16 a[228:229], v156 offset:16384\n"
        "v_mfma_f32_16x16x32_bf16 v[20:23], a[172:175], a[204:207], v[20:23]\n"
        "ds_read_b64_tr_b16 a[230:231], v156 offset:20480\n"
        "global_load_lds_dwordx4 v254, s[60:61] offset:1024 sc1\n"
        "v_mfma_f32_16x16x32_bf16 v[20:23], a[164:167], a[200:203], v[20:23]\n"
        "ds_read_b64_tr_b16 a[232:233], v155 offset:24576\n"
        "v_mfma_f32_16x16x32_bf16 v[20:23], a[92:95], a[196:199], v[20:23]\n"
        "ds_read_b64_tr_b16 a[234:235], v155 offset:28672\n"
        "s_waitcnt lgkmcnt(6)\n"
        "v_mfma_f32_16x16x32_bf16 v[20:23], a[84:87], a[192:195], v[20:23]\n"
        "ds_read_b64_tr_b16 a[236:237], v156 offset:24576\n"
        "global_load_lds_dwordx4 v254, s[60:61] offset:2048 sc1\n"
        "v_mfma_f32_16x16x32_bf16 v[28:31], a[84:87], a[208:211], v[28:31]\n"
        "ds_read_b64_tr_b16 a[238:239], v156 offset:28672\n"
        "v_mfma_f32_16x16x32_bf16 v[28:31], a[92:95], a[212:215], v[28:31]\n"
        "ds_read_b128 a[0:3], v254 offset:0\n"
        "v_mfma_f32_16x16x32_bf16 v[28:31], a[164:167], a[216:219], v[28:31]\n"
        "ds_read_b128 a[4:7], v254 offset:4096\n"
        "global_load_lds_dwordx4 v254, s[60:61] offset:3072 sc1\n"
        "s_add_u32 m0, s73, 0x10000\n"
        "v_mfma_f32_16x16x32_bf16 v[28:31], a[172:175], a[220:223], v[28:31]\n"
        "ds_read_b128 a[8:11], v254 offset:8192\n"
        "s_add_u32 s60, s68, s66\n"
        "s_addc_u32 s61, s69, 0\n"
        "v_mfma_f32_16x16x32_bf16 v[24:27], a[168:171], a[220:223], v[24:27]\n"
        "ds_read_b128 a[12:15], v254 offset:12288\n"
        "s_xor_b32 s66, s17, 6\n"
        "s_lshl_b32 s66, s66, 15\n"
        "v_mfma_f32_16x16x32_bf16 v[24:27], a[160:163], a[216:219], v[24:27]\n"
        "ds_read_b128 a[16:19], v254 offset:1024\n"
        "global_load_lds_dwordx4 v254, s[60:61] offset:0 sc1\n"
        "v_mfma_f32_16x16x32_bf16 v[24:27], a[88:91], a[212:215], v[24:27]\n"
        "ds_read_b128 a[20:23], v254 offset:5120\n"
        "v_mfma_f32_16x16x32_bf16 v[24:27], a[80:83], a[208:211], v[24:27]\n"
        "ds_read_b128 a[24:27], v254 offset:9216\n"
        "ds_read_b128 a[28:31], v254 offset:13312\n"
        "global_load_lds_dwordx4 v254, s[60:61] offset:1024 sc1\n"
        "s_waitcnt lgkmcnt(7)\n"
        "v_mfma_f32_16x16x32_bf16 v[0:3], a[176:179], a[0:3], v[0:3]\n"
        "ds_read_b128 a[192:195], v254 offset:2048\n"
        "s_waitcnt lgkmcnt(7)\n"
        "v_mfma_f32_16x16x32_bf16 v[0:3], a[184:187], a[4:7], v[0:3]\n"
        "ds_read_b128 a[196:199], v254 offset:6144\n"
        "s_waitcnt lgkmcnt(7)\n"
        "v_mfma_f32_16x16x32_bf16 v[0:3], a[32:35], a[8:11], v[0:3]\n"
        "ds_read_b128 a[200:203], v254 offset:10240\n"
        "s_waitcnt lgkmcnt(7)\n"
        "v_mfma_f32_16x16x32_bf16 v[0:3], a[40:43], a[12:15], v[0:3]\n"
        "ds_read_b128 a[204:207], v254 offset:14336\n"
        "global_load_lds_dwordx4 v254, s[60:61] offset:2048 sc1\n"
        "v_mfma_f32_16x16x32_bf16 v[4:7], a[44:47], a[12:15], v[4:7]\n"
        "ds_read_b128 a[208:211], v254 offset:3072\n"
        "s_waitcnt lgkmcnt(8)\n"
        "v_mfma_f32_16x16x32_bf16 v[4:7], a[36:39], a[8:11], v[4:7]\n"
        "ds_read_b128 a[212:215], v254 offset:7168\n"
        "s_waitcnt lgkmcnt(8)\n"
        "v_mfma_f32_16x16x32_bf16 v[4:7], a[188:191], a[4:7], v[4:7]\n"
        "ds_read_b128 a[216:219], v254 offset:11264\n"
        "global_load_lds_dwordx4 v254, s[60:61] offset:3072 sc1\n"
        "s_add_u32 m0, s73, 0x24000\n"
        "v_mfma_f32_16x16x32_bf16 v[4:7], a[180:183], a[0:3], v[4:7]\n"
        "ds_read_b128 a[220:223], v254 offset:15360\n"
        "s_add_u32 s60, s68, s66\n"
        "s_addc_u32 s61, s69, 0\n"
        "v_mfma_f32_16x16x32_bf16 v[12:15], a[180:183], a[16:19], v[12:15]\n"
        "s_xor_b32 s66, s17, 7\n"
        "s_lshl_b32 s66, s66, 15\n"
        "s_waitcnt lgkmcnt(7)\n"
        "v_mfma_f32_16x16x32_bf16 v[12:15], a[188:191], a[20:23], v[12:15]\n"
        "v_mfma_f32_16x16x32_bf16 v[12:15], a[36:39], a[24:27], v[12:15]\n"
        "v_mfma_f32_16x16x32_bf16 v[12:15], a[44:47], a[28:31], v[12:15]\n"
        "v_mfma_f32_16x16x32_bf16 v[8:11], a[40:43], a[28:31], v[8:11]\n"
        "v_mfma_f32_16x16x32_bf16 v[8:11], a[32:35], a[24:27], v[8:11]\n"
        "s_waitcnt lgkmcnt(2)\n"
        "v_mfma_f32_16x16x32_bf16 v[8:11], a[184:187], a[20:23], v[8:11]\n"
        "v_mfma_f32_16x16x32_bf16 v[8:11], a[176:179], a[16:19], v[8:11]\n"
        "s_waitcnt vmcnt(24)\n"
        "v_mfma_f32_16x16x32_bf16 v[16:19], a[176:179], a[192:195], v[16:19]\n"
        "s_barrier\n"
        "v_mfma_f32_16x16x32_bf16 v[16:19], a[184:187], a[196:199], v[16:19]\n"
        "ds_read_b128 a[0:3], v254 offset:32768\n"
        "global_load_lds_dwordx4 v254, s[60:61] offset:0 sc1\n"
        "v_mfma_f32_16x16x32_bf16 v[16:19], a[32:35], a[200:203], v[16:19]\n"
        "LHB0_%=:\n"
        "s_cbranch_execz LHRB_%=\n"
        "ds_read_b128 a[4:7], v254 offset:36864\n"
        "v_mfma_f32_16x16x32_bf16 v[16:19], a[40:43], a[204:207], v[16:19]\n"
        "ds_read_b128 a[8:11], v254 offset:40960\n"
        "v_mfma_f32_16x16x32_bf16 v[20:23], a[44:47], a[204:207], v[20:23]\n"
        "ds_read_b128 a[12:15], v254 offset:45056\n"
        "global_load_lds_dwordx4 v254, s[60:61] offset:1024 sc1\n"
        "v_mfma_f32_16x16x32_bf16 v[20:23], a[36:39], a[200:203], v[20:23]\n"
        "ds_read_b128 a[28:31], v254 offset:46080\n"
        "v_mfma_f32_16x16x32_bf16 v[20:23], a[188:191], a[196:199], v[20:23]\n"
        "ds_read_b128 a[24:27], v254 offset:41984\n"
        "v_mfma_f32_16x16x32_bf16 v[20:23], a[180:183], a[192:195], v[20:23]\n"
        "ds_read_b128 a[20:23], v254 offset:37888\n"
        "global_load_lds_dwordx4 v254, s[60:61] offset:2048 sc1\n"
        "v_mfma_f32_16x16x32_bf16 v[28:31], a[180:183], a[208:211], v[28:31]\n"
        "ds_read_b128 a[16:19], v254 offset:33792\n"
        "v_mfma_f32_16x16x32_bf16 v[28:31], a[188:191], a[212:215], v[28:31]\n"
        "ds_read_b128 a[192:195], v254 offset:34816\n"
        "s_waitcnt lgkmcnt(8)\n"
        "v_mfma_f32_16x16x32_bf16 v[0:3], a[48:51], a[0:3], v[0:3]\n"
        "ds_read_b128 a[196:199], v254 offset:38912\n"
        "s_waitcnt lgkmcnt(8)\n"
        "v_mfma_f32_16x16x32_bf16 v[0:3], a[56:59], a[4:7], v[0:3]\n"
        "ds_read_b128 a[200:203], v254 offset:43008\n"
        "s_waitcnt lgkmcnt(8)\n"
        "v_mfma_f32_16x16x32_bf16 v[0:3], a[224:227], a[8:11], v[0:3]\n"
        "ds_read_b128 a[204:207], v254 offset:47104\n"
        "s_waitcnt lgkmcnt(8)\n"
        "v_mfma_f32_16x16x32_bf16 v[0:3], a[232:235], a[12:15], v[0:3]\n"
        "global_load_lds_dwordx4 v254, s[60:61] offset:3072 sc1\n"
        "s_add_u32 m0, s73, 0x14000\n"
        "v_mfma_f32_16x16x32_bf16 v[4:7], a[236:239], a[12:15], v[4:7]\n"
        "s_add_u32 s60, s68, s66\n"
        "s_addc_u32 s61, s69, 0\n"
        "s_sub_u32 s66, 15, s12\n"
        "v_mfma_f32_16x16x32_bf16 v[4:7], a[228:231], a[8:11], v[4:7]\n"
        "s_cmp_lt_u32 s76, 2\n"
        "s_cselect_b32 s66, s12, s66\n"
        "s_mul_i32 s66, s66, 1052672\n"
        "v_mfma_f32_16x16x32_bf16 v[4:7], a[60:63], a[4:7], v[4:7]\n"
        "global_load_lds_dwordx4 v254, s[60:61] offset:0 sc1\n"
        "s_add_u32 s66, s66, s67\n"
        "v_cvt_pk_bf16_f32 v0, v0, v1\n"
        "v_mfma_f32_16x16x32_bf16 v[4:7], a[52:55], a[0:3], v[4:7]\n"
        "s_waitcnt lgkmcnt(3)\n"
        "v_cvt_pk_bf16_f32 v1, v2, v3\n"
        "v_mfma_f32_16x16x32_bf16 v[28:31], a[36:39], a[216:219], v[28:31]\n"
        "s_add_u32 s68, s20, s66\n"
        "s_addc_u32 s69, s21, 0\n"
        "s_xor_b32 s66, s17, 0\n"
        "v_mfma_f32_16x16x32_bf16 v[28:31], a[44:47], a[220:223], v[28:31]\n"
        "global_load_lds_dwordx4 v254, s[60:61] offset:1024 sc1\n"
        "s_lshl_b32 s66, s66, 15\n"
        "v_cvt_pk_bf16_f32 v2, v4, v5\n"
        "v_mfma_f32_16x16x32_bf16 v[8:11], a[232:235], a[28:31], v[8:11]\n"
        "v_cvt_pk_bf16_f32 v3, v6, v7\n"
        "v_mfma_f32_16x16x32_bf16 v[8:11], a[224:227], a[24:27], v[8:11]\n"
        "v_mfma_f32_16x16x32_bf16 v[8:11], a[56:59], a[20:23], v[8:11]\n"
        "global_store_dwordx4 v254, v[0:3], s[28:29] offset:0 sc1\n"
        "v_mfma_f32_16x16x32_bf16 v[8:11], a[48:51], a[16:19], v[8:11]\n"
        "s_waitcnt vmcnt(27)\n"
        "v_mfma_f32_16x16x32_bf16 v[12:15], a[52:55], a[16:19], v[12:15]\n"
        "s_barrier\n"
        "v_mfma_f32_16x16x32_bf16 v[12:15], a[60:63], a[20:23], v[12:15]\n"
        "ds_read_b128 a[0:3], v254 offset:16384\n"
        "global_load_lds_dwordx4 v254, s[60:61] offset:2048 sc1\n"
        "v_mfma_f32_16x16x32_bf16 v[12:15], a[228:231], a[24:27], v[12:15]\n"
        "ds_read_b128 a[4:7], v254 offset:20480\n"
        "v_cvt_pk_bf16_f32 v8, v8, v9\n"
        "v_mfma_f32_16x16x32_bf16 v[12:15], a[236:239], a[28:31], v[12:15]\n"
        "ds_read_b128 a[8:11], v254 offset:24576\n"
        "v_cvt_pk_bf16_f32 v9, v10, v11\n"
        "v_mfma_f32_16x16x32_bf16 v[24:27], a[40:43], a[220:223], v[24:27]\n"
        "ds_read_b128 a[220:223], v254 offset:48128\n"
        "global_load_lds_dwordx4 v254, s[60:61] offset:3072 sc1\n"
        "s_add_u32 m0, s73, 0x0\n"
        "v_mfma_f32_16x16x32_bf16 v[24:27], a[32:35], a[216:219], v[24:27]\n"
        "ds_read_b128 a[216:219], v254 offset:44032\n"
        "v_cvt_pk_bf16_f32 v10, v12, v13\n"
        "v_mfma_f32_16x16x32_bf16 v[24:27], a[184:187], a[212:215], v[24:27]\n"
        "ds_read_b128 a[212:215], v254 offset:39936\n"
        "v_cvt_pk_bf16_f32 v11, v14, v15\n"
        "v_mfma_f32_16x16x32_bf16 v[24:27], a[176:179], a[208:211], v[24:27]\n"
        "ds_read_b128 a[208:211], v254 offset:35840\n"
        "s_add_u32 s60, s68, s66\n"
        "s_addc_u32 s61, s69, 0\n"
        "v_mfma_f32_16x16x32_bf16 v[16:19], a[48:51], a[192:195], v[16:19]\n"
        "ds_read_b128 a[12:15], v254 offset:28672\n"
        "s_xor_b32 s66, s17, 1\n"
        "s_waitcnt lgkmcnt(8)\n"
        "v_mfma_f32_16x16x32_bf16 v[16:19], a[56:59], a[196:199], v[16:19]\n"
        "global_store_dwordx4 v254, v[8:11], s[28:29] offset:1024 sc1\n"
        "ds_read_b128 a[16:19], v254 offset:17408\n"
        "ds_read_b128 a[20:23], v254 offset:21504\n"
        "s_lshl_b32 s66, s66, 15\n"
        "v_mfma_f32_16x16x32_bf16 v[16:19], a[224:227], a[200:203], v[16:19]\n"
        "ds_read_b128 a[24:27], v254 offset:25600\n"
        "v_mfma_f32_16x16x32_bf16 v[16:19], a[232:235], a[204:207], v[16:19]\n"
        "ds_read_b128 a[28:31], v254 offset:29696\n"
        "global_load_lds_dwordx4 v254, s[60:61] offset:0 sc1\n"
        "v_mfma_f32_16x16x32_bf16 v[20:23], a[236:239], a[204:207], v[20:23]\n"
        "ds_read_b128 a[204:207], v254 offset:30720\n"
        "s_waitcnt lgkmcnt(8)\n"
        "v_mfma_f32_16x16x32_bf16 v[20:23], a[228:231], a[200:203], v[20:23]\n"
        "ds_read_b128 a[200:203], v254 offset:26624\n"
        "v_mfma_f32_16x16x32_bf16 v[20:23], a[60:63], a[196:199], v[20:23]\n"
        "ds_read_b128 a[196:199], v254 offset:22528\n"
        "v_cvt_pk_bf16_f32 v16, v16, v17\n"
        "v_mfma_f32_16x16x32_bf16 v[20:23], a[52:55], a[192:195], v[20:23]\n"
        "s_waitcnt lgkmcnt(8)\n"
        "v_cvt_pk_bf16_f32 v17, v18, v19\n"
        "v_mfma_f32_16x16x32_bf16 v[24:27], a[232:235], a[220:223], v[24:27]\n"
        "ds_read_b128 a[192:195], v254 offset:18432\n"
        "global_load_lds_dwordx4 v254, s[60:61] offset:1024 sc1\n"
        "v_mfma_f32_16x16x32_bf16 v[24:27], a[224:227], a[216:219], v[24:27]\n"
        "v_mfma_f32_16x16x32_bf16 v[24:27], a[56:59], a[212:215], v[24:27]\n"
        "v_mfma_f32_16x16x32_bf16 v[24:27], a[48:51], a[208:211], v[24:27]\n"
        "v_cvt_pk_bf16_f32 v18, v20, v21\n"
        "v_cvt_pk_bf16_f32 v19, v22, v23\n"
        "v_mfma_f32_16x16x32_bf16 v[28:31], a[52:55], a[208:211], v[28:31]\n"
        "global_store_dwordx4 v254, v[16:19], s[28:29] offset:2048 sc1\n"
        "ds_read_b128 a[208:211], v254 offset:19456\n"
        "v_mfma_f32_16x16x32_bf16 v[28:31], a[60:63], a[212:215], v[28:31]\n"
        "ds_read_b128 a[212:215], v254 offset:23552\n"
        "v_mfma_f32_16x16x32_bf16 v[28:31], a[228:231], a[216:219], v[28:31]\n"
        "ds_read_b128 a[216:219], v254 offset:27648\n"
        "v_cvt_pk_bf16_f32 v24, v24, v25\n"
        "v_mfma_f32_16x16x32_bf16 v[28:31], a[236:239], a[220:223], v[28:31]\n"
        "s_waitcnt lgkmcnt(4)\n"
        "v_cvt_pk_bf16_f32 v25, v26, v27\n"
        "v_mfma_f32_16x16x32_bf16 v[32:35], a[96:99], a[0:3], 0\n"
        "ds_read_b128 a[220:223], v254 offset:31744\n"
        "global_load_lds_dwordx4 v254, s[60:61] offset:2048 sc1\n"
        "v_mfma_f32_16x16x32_bf16 v[32:35], a[104:107], a[4:7], v[32:35]\n"
        "v_mfma_f32_16x16x32_bf16 v[32:35], a[112:115], a[8:11], v[32:35]\n"
        "v_mfma_f32_16x16x32_bf16 v[32:35], a[120:123], a[12:15], v[32:35]\n"
        "v_cvt_pk_bf16_f32 v26, v28, v29\n"
        "v_cvt_pk_bf16_f32 v27, v30, v31\n"
        "v_mfma_f32_16x16x32_bf16 v[36:39], a[124:127], a[12:15], 0\n"
        "global_store_dwordx4 v254, v[24:27], s[28:29] offset:3072 sc1\n"
        "v_mfma_f32_16x16x32_bf16 v[36:39], a[116:119], a[8:11], v[36:39]\n"
        "v_mfma_f32_16x16x32_bf16 v[36:39], a[108:111], a[4:7], v[36:39]\n"
        "global_load_lds_dwordx4 v254, s[60:61] offset:3072 sc1\n"
        "s_add_u32 m0, s73, 0x8000\n"
        "v_mfma_f32_16x16x32_bf16 v[36:39], a[100:103], a[0:3], v[36:39]\n"
        "s_add_u32 s60, s68, s66\n"
        "s_addc_u32 s61, s69, 0\n"
        "s_xor_b32 s66, s17, 2\n"
        "v_mfma_f32_16x16x32_bf16 v[44:47], a[100:103], a[16:19], 0\n"
        "s_lshl_b32 s66, s66, 15\n"
        "v_mfma_f32_16x16x32_bf16 v[44:47], a[108:111], a[20:23], v[44:47]\n"
        "v_mfma_f32_16x16x32_bf16 v[44:47], a[116:119], a[24:27], v[44:47]\n"
        "global_store_dwordx4 v229, v[88:91], s[24:25] offset:0 sc1\n"
        "v_mfma_f32_16x16x32_bf16 v[44:47], a[124:127], a[28:31], v[44:47]\n"
        "s_lshl2_add_u32 s15, s75, s10\n"
        "v_cvt_pk_bf16_f32 v88, v80, v81\n"
        "v_cvt_pk_bf16_f32 v89, v82, v83\n"
        "v_mfma_f32_16x16x32_bf16 v[40:43], a[120:123], a[28:31], 0\n"
        "s_waitcnt lgkmcnt(0)\n"
        "v_cvt_pk_bf16_f32 v90, v84, v85\n"
        "v_cvt_pk_bf16_f32 v91, v86, v87\n"
        "v_mfma_f32_16x16x32_bf16 v[40:43], a[112:115], a[24:27], v[40:43]\n"
        "global_store_dwordx4 v229, v[92:95], s[24:25] offset:1024 sc1\n"
        "s_mul_i32 s16, s13, 263168\n"
        "s_lshl_b32 s15, s15, 14\n"
        "s_add_u32 s16, s16, s15\n"
        "v_permlane16_swap_b32 v88, v90\n"
        "v_mfma_f32_16x16x32_bf16 v[40:43], a[104:107], a[20:23], v[40:43]\n"
        "s_add_u32 s16, s16, s73\n"
        "v_permlane16_swap_b32 v89, v91\n"
        "v_mfma_f32_16x16x32_bf16 v[40:43], a[96:99], a[16:19], v[40:43]\n"
        "s_waitcnt vmcnt(34)\n"
        "v_and_b32 v80, 0xffff0000, v77\n"
        "v_lshlrev_b32 v81, 16, v77\n"
        "v_mfma_f32_16x16x32_bf16 v[48:51], a[96:99], a[192:195], 0\n"
        "s_barrier\n"
        "v_mfma_f32_16x16x32_bf16 v[48:51], a[104:107], a[196:199], v[48:51]\n"
        "ds_read_b128 a[0:3], v244 offset:32768\n"
        "global_load_lds_dwordx4 v254, s[60:61] offset:0 sc1\n"
        "s_add_u32 s16, s16, 0x4000000\n"
        "v_mfma_f32_16x16x32_bf16 v[48:51], a[112:115], a[200:203], v[48:51]\n"
        "ds_read_b128 a[4:7], v244 offset:36864\n"
        "v_and_b32 v82, 0xffff0000, v76\n"
        "v_mfma_f32_16x16x32_bf16 v[48:51], a[120:123], a[204:207], v[48:51]\n"
        "ds_read_b128 a[8:11], v244 offset:40960\n"
        "v_lshlrev_b32 v83, 16, v76\n"
        "v_mfma_f32_16x16x32_bf16 v[52:55], a[124:127], a[204:207], 0\n"
        "ds_read_b128 a[12:15], v244 offset:45056\n"
        "global_load_lds_dwordx4 v254, s[60:61] offset:1024 sc1\n"
        "v_mfma_f32_16x16x32_bf16 v[52:55], a[116:119], a[200:203], v[52:55]\n"
        "ds_read_b128 a[16:19], v244 offset:33792\n"
        "v_mul_f32 v80, s54, v80\n"
        "v_mfma_f32_16x16x32_bf16 v[52:55], a[108:111], a[196:199], v[52:55]\n"
        "ds_read_b128 a[20:23], v244 offset:37888\n"
        "v_mul_f32 v81, s54, v81\n"
        "v_mfma_f32_16x16x32_bf16 v[52:55], a[100:103], a[192:195], v[52:55]\n"
        "ds_read_b128 a[24:27], v244 offset:41984\n"
        "global_load_lds_dwordx4 v254, s[60:61] offset:2048 sc1\n"
        "v_mfma_f32_16x16x32_bf16 v[60:63], a[100:103], a[208:211], 0\n"
        "ds_read_b128 a[28:31], v244 offset:46080\n"
        "v_mul_f32 v82, s54, v82\n"
        "v_mfma_f32_16x16x32_bf16 v[60:63], a[108:111], a[212:215], v[60:63]\n"
        "ds_read_b128 a[192:195], v244 offset:34816\n"
        "v_mul_f32 v83, s54, v83\n"
        "v_mfma_f32_16x16x32_bf16 v[60:63], a[116:119], a[216:219], v[60:63]\n"
        "ds_read_b128 a[196:199], v244 offset:38912\n"
        "global_load_lds_dwordx4 v254, s[60:61] offset:3072 sc1\n"
        "s_add_u32 m0, s73, 0x4000\n"
        "v_mfma_f32_16x16x32_bf16 v[60:63], a[124:127], a[220:223], v[60:63]\n"
        "ds_read_b128 a[200:203], v244 offset:43008\n"
        "s_add_u32 s60, s68, s66\n"
        "s_addc_u32 s61, s69, 0\n"
        "v_mfma_f32_16x16x32_bf16 v[56:59], a[120:123], a[220:223], 0\n"
        "ds_read_b128 a[204:207], v244 offset:47104\n"
        "s_xor_b32 s66, s17, 3\n"
        "s_lshl_b32 s66, s66, 15\n"
        "v_mfma_f32_16x16x32_bf16 v[56:59], a[112:115], a[216:219], v[56:59]\n"
        "ds_read_b128 a[216:219], v244 offset:44032\n"
        "v_cvt_pk_bf16_f32 v80, 0, v80\n"
        "v_mfma_f32_16x16x32_bf16 v[56:59], a[104:107], a[212:215], v[56:59]\n"
        "ds_read_b128 a[212:215], v244 offset:39936\n"
        "v_cvt_pk_bf16_f32 v81, 0, v81\n"
        "v_mfma_f32_16x16x32_bf16 v[56:59], a[96:99], a[208:211], v[56:59]\n"
        "ds_read_b128 a[208:211], v244 offset:35840\n"
        "s_waitcnt lgkmcnt(8)\n"
        "v_mfma_f32_16x16x32_bf16 v[32:35], a[128:131], a[0:3], v[32:35]\n"
        "ds_read_b128 a[220:223], v244 offset:48128\n"
        "v_cvt_pk_bf16_f32 v82, 0, v82\n"
        "v_mfma_f32_16x16x32_bf16 v[32:35], a[136:139], a[4:7], v[32:35]\n"
        "s_add_u32 s28, s38, s16\n"
        "s_addc_u32 s29, s39, 0\n"
        "v_cvt_pk_bf16_f32 v83, 0, v83\n"
        "v_mfma_f32_16x16x32_bf16 v[32:35], a[144:147], a[8:11], v[32:35]\n"
        "v_add_f32 v80, v80, v136\n"
        "v_add_f32 v81, v81, v137\n"
        "v_mfma_f32_16x16x32_bf16 v[32:35], a[152:155], a[12:15], v[32:35]\n"
        "v_add_f32 v82, v82, v138\n"
        "v_add_f32 v83, v83, v139\n"
        "v_mfma_f32_16x16x32_bf16 v[36:39], a[156:159], a[12:15], v[36:39]\n"
        "v_cvt_pk_bf16_f32 v92, v80, v81\n"
        "v_cvt_pk_bf16_f32 v93, v82, v83\n"
        "v_mfma_f32_16x16x32_bf16 v[36:39], a[148:151], a[8:11], v[36:39]\n"
        "v_and_b32 v84, 0xffff0000, v79\n"
        "v_lshlrev_b32 v85, 16, v79\n"
        "v_mfma_f32_16x16x32_bf16 v[36:39], a[140:143], a[4:7], v[36:39]\n"
        "v_and_b32 v86, 0xffff0000, v78\n"
        "v_lshlrev_b32 v87, 16, v78\n"
        "v_mfma_f32_16x16x32_bf16 v[36:39], a[132:135], a[0:3], v[36:39]\n"
        "global_store_dwordx4 v229, v[88:91], s[24:25] offset:2048 sc1\n"
        "s_waitcnt lgkmcnt(1)\n"
        "v_mul_f32 v84, s54, v84\n"
        "v_mul_f32 v85, s54, v85\n"
        "v_mul_f32 v86, s54, v86\n"
        "v_mfma_f32_16x16x32_bf16 v[44:47], a[132:135], a[16:19], v[44:47]\n"
        "s_and_b32 s67, s10, 1\n"
        "v_mul_f32 v87, s54, v87\n"
        "v_cvt_pk_bf16_f32 v84, 0, v84\n"
        "v_mfma_f32_16x16x32_bf16 v[44:47], a[140:143], a[20:23], v[44:47]\n"
        "s_lshl_b32 s67, s67, 12\n"
        "v_cvt_pk_bf16_f32 v85, 0, v85\n"
        "v_cvt_pk_bf16_f32 v86, 0, v86\n"
        "v_mfma_f32_16x16x32_bf16 v[44:47], a[148:151], a[24:27], v[44:47]\n"
        "v_cvt_pk_bf16_f32 v87, 0, v87\n"
        "v_add_f32 v84, v84, v140\n"
        "v_mfma_f32_16x16x32_bf16 v[44:47], a[156:159], a[28:31], v[44:47]\n"
        "v_add_f32 v85, v85, v141\n"
        "v_add_f32 v86, v86, v142\n"
        "v_mfma_f32_16x16x32_bf16 v[40:43], a[152:155], a[28:31], v[40:43]\n"
        "v_add_f32 v87, v87, v143\n"
        "v_cvt_pk_bf16_f32 v94, v84, v85\n"
        "v_mfma_f32_16x16x32_bf16 v[40:43], a[144:147], a[24:27], v[40:43]\n"
        "v_cvt_pk_bf16_f32 v95, v86, v87\n"
        "v_mfma_f32_16x16x32_bf16 v[40:43], a[136:139], a[20:23], v[40:43]\n"
        "v_permlane16_swap_b32 v92, v94\n"
        "v_mfma_f32_16x16x32_bf16 v[40:43], a[128:131], a[16:19], v[40:43]\n"
        "s_waitcnt vmcnt(35)\n"
        "v_permlane16_swap_b32 v93, v95\n"
        "v_mfma_f32_16x16x32_bf16 v[48:51], a[128:131], a[192:195], v[48:51]\n"
        "s_barrier\n"
        "v_mfma_f32_16x16x32_bf16 v[48:51], a[136:139], a[196:199], v[48:51]\n"
        "ds_read_b128 a[0:3], v254 offset:49152\n"
        "global_load_lds_dwordx4 v254, s[60:61] offset:0 sc1\n"
        "v_mfma_f32_16x16x32_bf16 v[48:51], a[144:147], a[200:203], v[48:51]\n"
        "ds_read_b128 a[4:7], v254 offset:53248\n"
        "v_mfma_f32_16x16x32_bf16 v[48:51], a[152:155], a[204:207], v[48:51]\n"
        "ds_read_b128 a[8:11], v254 offset:57344\n"
        "v_mfma_f32_16x16x32_bf16 v[52:55], a[156:159], a[204:207], v[52:55]\n"
        "ds_read_b128 a[12:15], v254 offset:61440\n"
        "global_load_lds_dwordx4 v254, s[60:61] offset:1024 sc1\n"
        "v_mfma_f32_16x16x32_bf16 v[52:55], a[148:151], a[200:203], v[52:55]\n"
        "ds_read_b128 a[16:19], v254 offset:50176\n"
        "v_mfma_f32_16x16x32_bf16 v[52:55], a[140:143], a[196:199], v[52:55]\n"
        "ds_read_b128 a[20:23], v254 offset:54272\n"
        "v_mfma_f32_16x16x32_bf16 v[52:55], a[132:135], a[192:195], v[52:55]\n"
        "ds_read_b128 a[24:27], v254 offset:58368\n"
        "global_load_lds_dwordx4 v254, s[60:61] offset:2048 sc1\n"
        "v_mfma_f32_16x16x32_bf16 v[60:63], a[132:135], a[208:211], v[60:63]\n"
        "ds_read_b128 a[28:31], v254 offset:62464\n"
        "s_waitcnt lgkmcnt(8)\n"
        "v_mfma_f32_16x16x32_bf16 v[60:63], a[140:143], a[212:215], v[60:63]\n"
        "ds_read_b128 a[192:195], v254 offset:51200\n"
        "v_mfma_f32_16x16x32_bf16 v[60:63], a[148:151], a[216:219], v[60:63]\n"
        "ds_read_b128 a[196:199], v254 offset:55296\n"
        "global_load_lds_dwordx4 v254, s[60:61] offset:3072 sc1\n"
        "s_add_u32 m0, s73, 0x18000\n"
        "v_mfma_f32_16x16x32_bf16 v[60:63], a[156:159], a[220:223], v[60:63]\n"
        "ds_read_b128 a[200:203], v254 offset:59392\n"
        "s_add_u32 s60, s68, s66\n"
        "s_addc_u32 s61, s69, 0\n"
        "v_mfma_f32_16x16x32_bf16 v[56:59], a[152:155], a[220:223], v[56:59]\n"
        "ds_read_b128 a[204:207], v254 offset:63488\n"
        "s_xor_b32 s66, s17, 4\n"
        "s_lshl_b32 s66, s66, 15\n"
        "v_mfma_f32_16x16x32_bf16 v[56:59], a[144:147], a[216:219], v[56:59]\n"
        "ds_read_b128 a[216:219], v254 offset:60416\n"
        "s_waitcnt lgkmcnt(8)\n"
        "v_mfma_f32_16x16x32_bf16 v[56:59], a[136:139], a[212:215], v[56:59]\n"
        "ds_read_b128 a[212:215], v254 offset:56320\n"
        "v_mfma_f32_16x16x32_bf16 v[56:59], a[128:131], a[208:211], v[56:59]\n"
        "ds_read_b128 a[208:211], v254 offset:52224\n"
        "v_mfma_f32_16x16x32_bf16 v[32:35], v[160:163], a[0:3], v[32:35]\n"
        "ds_read_b128 a[220:223], v254 offset:64512\n"
        "v_mfma_f32_16x16x32_bf16 v[32:35], v[168:171], a[4:7], v[32:35]\n"
        "v_mfma_f32_16x16x32_bf16 v[32:35], v[176:179], a[8:11], v[32:35]\n"
        "v_mfma_f32_16x16x32_bf16 v[32:35], v[184:187], a[12:15], v[32:35]\n"
        "v_mfma_f32_16x16x32_bf16 v[36:39], v[188:191], a[12:15], v[36:39]\n"
        "v_mfma_f32_16x16x32_bf16 v[36:39], v[180:183], a[8:11], v[36:39]\n"
        "s_waitcnt lgkmcnt(3)\n"
        "v_mfma_f32_16x16x32_bf16 v[36:39], v[172:175], a[4:7], v[36:39]\n"
        "v_mfma_f32_16x16x32_bf16 v[36:39], v[164:167], a[0:3], v[36:39]\n"
        "global_store_dwordx4 v229, v[92:95], s[24:25] offset:3072 sc1\n"
        "v_mfma_f32_16x16x32_bf16 v[44:47], v[164:167], a[16:19], v[44:47]\n"
        "v_mfma_f32_16x16x32_bf16 v[44:47], v[172:175], a[20:23], v[44:47]\n"
        "v_mfma_f32_16x16x32_bf16 v[44:47], v[180:183], a[24:27], v[44:47]\n"
        "v_mfma_f32_16x16x32_bf16 v[44:47], v[188:191], a[28:31], v[44:47]\n"
        "v_mfma_f32_16x16x32_bf16 v[40:43], v[184:187], a[28:31], v[40:43]\n"
        "v_mfma_f32_16x16x32_bf16 v[40:43], v[176:179], a[24:27], v[40:43]\n"
        "v_mfma_f32_16x16x32_bf16 v[40:43], v[168:171], a[20:23], v[40:43]\n"
        "v_mfma_f32_16x16x32_bf16 v[40:43], v[160:163], a[16:19], v[40:43]\n"
        "s_waitcnt vmcnt(36)\n"
        "v_mfma_f32_16x16x32_bf16 v[48:51], v[160:163], a[192:195], v[48:51]\n"
        "s_barrier\n"
        "v_mfma_f32_16x16x32_bf16 v[48:51], v[168:171], a[196:199], v[48:51]\n"
        "ds_read_b128 a[0:3], v245 offset:0\n"
        "global_load_lds_dwordx4 v254, s[60:61] offset:0 sc1\n"
        "v_mfma_f32_16x16x32_bf16 v[48:51], v[176:179], a[200:203], v[48:51]\n"
        "ds_read_b128 a[4:7], v245 offset:4096\n"
        "v_mfma_f32_16x16x32_bf16 v[48:51], v[184:187], a[204:207], v[48:51]\n"
        "ds_read_b128 a[8:11], v245 offset:8192\n"
        "v_mfma_f32_16x16x32_bf16 v[52:55], v[188:191], a[204:207], v[52:55]\n"
        "ds_read_b128 a[12:15], v245 offset:12288\n"
        "global_load_lds_dwordx4 v254, s[60:61] offset:1024 sc1\n"
        "v_mfma_f32_16x16x32_bf16 v[52:55], v[180:183], a[200:203], v[52:55]\n"
        "ds_read_b128 a[16:19], v245 offset:1024\n"
        "s_waitcnt lgkmcnt(5)\n"
        "v_mfma_f32_16x16x32_bf16 v[52:55], v[172:175], a[196:199], v[52:55]\n"
        "ds_read_b128 a[20:23], v245 offset:5120\n"
        "v_mfma_f32_16x16x32_bf16 v[52:55], v[164:167], a[192:195], v[52:55]\n"
        "ds_read_b128 a[24:27], v245 offset:9216\n"
        "global_load_lds_dwordx4 v254, s[60:61] offset:2048 sc1\n"
        "v_mfma_f32_16x16x32_bf16 v[60:63], v[164:167], a[208:211], v[60:63]\n"
        "ds_read_b128 a[28:31], v245 offset:13312\n"
        "v_mfma_f32_16x16x32_bf16 v[60:63], v[172:175], a[212:215], v[60:63]\n"
        "ds_read_b128 a[192:195], v245 offset:2048\n"
        "v_mfma_f32_16x16x32_bf16 v[60:63], v[180:183], a[216:219], v[60:63]\n"
        "ds_read_b128 a[196:199], v245 offset:6144\n"
        "global_load_lds_dwordx4 v254, s[60:61] offset:3072 sc1\n"
        "s_add_u32 m0, s73, 0xc000\n"
        "v_mfma_f32_16x16x32_bf16 v[60:63], v[188:191], a[220:223], v[60:63]\n"
        "ds_read_b128 a[200:203], v245 offset:10240\n"
        "s_add_u32 s60, s68, s66\n"
        "s_addc_u32 s61, s69, 0\n"
        "v_mfma_f32_16x16x32_bf16 v[56:59], v[184:187], a[220:223], v[56:59]\n"
        "ds_read_b128 a[204:207], v245 offset:14336\n"
        "s_xor_b32 s66, s17, 5\n"
        "s_lshl_b32 s66, s66, 15\n"
        "v_mfma_f32_16x16x32_bf16 v[56:59], v[176:179], a[216:219], v[56:59]\n"
        "ds_read_b128 a[216:219], v245 offset:11264\n"
        "s_waitcnt lgkmcnt(8)\n"
        "v_mfma_f32_16x16x32_bf16 v[56:59], v[168:171], a[212:215], v[56:59]\n"
        "ds_read_b128 a[212:215], v245 offset:7168\n"
        "v_mfma_f32_16x16x32_bf16 v[56:59], v[160:163], a[208:211], v[56:59]\n"
        "ds_read_b128 a[208:211], v245 offset:3072\n"
        "v_mfma_f32_16x16x32_bf16 v[32:35], v[192:195], a[0:3], v[32:35]\n"
        "ds_read_b128 a[220:223], v245 offset:15360\n"
        "v_mfma_f32_16x16x32_bf16 v[32:35], v[200:203], a[4:7], v[32:35]\n"
        "v_mfma_f32_16x16x32_bf16 v[32:35], v[208:211], a[8:11], v[32:35]\n"
        "v_mfma_f32_16x16x32_bf16 v[32:35], v[216:219], a[12:15], v[32:35]\n"
        "v_mfma_f32_16x16x32_bf16 v[36:39], v[220:223], a[12:15], v[36:39]\n"
        "v_mfma_f32_16x16x32_bf16 v[36:39], v[212:215], a[8:11], v[36:39]\n"
        "s_waitcnt lgkmcnt(3)\n"
        "v_mfma_f32_16x16x32_bf16 v[36:39], v[204:207], a[4:7], v[36:39]\n"
        "v_mfma_f32_16x16x32_bf16 v[36:39], v[196:199], a[0:3], v[36:39]\n"
        "v_mfma_f32_16x16x32_bf16 v[44:47], v[196:199], a[16:19], v[44:47]\n"
        "v_mfma_f32_16x16x32_bf16 v[44:47], v[204:207], a[20:23], v[44:47]\n"
        "v_mfma_f32_16x16x32_bf16 v[44:47], v[212:215], a[24:27], v[44:47]\n"
        "v_mfma_f32_16x16x32_bf16 v[44:47], v[220:223], a[28:31], v[44:47]\n"
        "v_mfma_f32_16x16x32_bf16 v[40:43], v[216:219], a[28:31], v[40:43]\n"
        "v_mfma_f32_16x16x32_bf16 v[40:43], v[208:211], a[24:27], v[40:43]\n"
        "v_mfma_f32_16x16x32_bf16 v[40:43], v[200:203], a[20:23], v[40:43]\n"
        "v_mfma_f32_16x16x32_bf16 v[40:43], v[192:195], a[16:19], v[40:43]\n"
        "s_waitcnt vmcnt(36)\n"
        "v_mfma_f32_16x16x32_bf16 v[48:51], v[192:195], a[192:195], v[48:51]\n"
        "s_barrier\n"
        "v_mfma_f32_16x16x32_bf16 v[48:51], v[200:203], a[196:199], v[48:51]\n"
        "ds_read_b128 a[0:3], v244 offset:49152\n"
        "global_load_lds_dwordx4 v254, s[60:61] offset:0 sc1\n"
        "v_mfma_f32_16x16x32_bf16 v[48:51], v[208:211], a[200:203], v[48:51]\n"
        "ds_read_b128 a[4:7], v244 offset:53248\n"
        "v_mfma_f32_16x16x32_bf16 v[48:51], v[216:219], a[204:207], v[48:51]\n"
        "ds_read_b128 a[8:11], v244 offset:57344\n"
        "v_mfma_f32_16x16x32_bf16 v[52:55], v[220:223], a[204:207], v[52:55]\n"
        "ds_read_b128 a[12:15], v244 offset:61440\n"
        "global_load_lds_dwordx4 v254, s[60:61] offset:1024 sc1\n"
        "v_mfma_f32_16x16x32_bf16 v[52:55], v[212:215], a[200:203], v[52:55]\n"
        "ds_read_b128 a[16:19], v244 offset:50176\n"
        "s_waitcnt lgkmcnt(5)\n"
        "v_mfma_f32_16x16x32_bf16 v[52:55], v[204:207], a[196:199], v[52:55]\n"
        "ds_read_b128 a[20:23], v244 offset:54272\n"
        "v_mfma_f32_16x16x32_bf16 v[52:55], v[196:199], a[192:195], v[52:55]\n"
        "ds_read_b128 a[24:27], v244 offset:58368\n"
        "global_load_lds_dwordx4 v254, s[60:61] offset:2048 sc1\n"
        "v_mfma_f32_16x16x32_bf16 v[60:63], v[196:199], a[208:211], v[60:63]\n"
        "ds_read_b128 a[28:31], v244 offset:62464\n"
        "v_mfma_f32_16x16x32_bf16 v[60:63], v[204:207], a[212:215], v[60:63]\n"
        "ds_read_b128 a[192:195], v244 offset:51200\n"
        "v_mfma_f32_16x16x32_bf16 v[60:63], v[212:215], a[216:219], v[60:63]\n"
        "ds_read_b128 a[196:199], v244 offset:55296\n"
        "global_load_lds_dwordx4 v254, s[60:61] offset:3072 sc1\n"
        "s_add_u32 m0, s73, 0x20000\n"
        "v_mfma_f32_16x16x32_bf16 v[60:63], v[220:223], a[220:223], v[60:63]\n"
        "ds_read_b128 a[200:203], v244 offset:59392\n"
        "s_add_u32 s60, s68, s66\n"
        "s_addc_u32 s61, s69, 0\n"
        "v_mfma_f32_16x16x32_bf16 v[56:59], v[216:219], a[220:223], v[56:59]\n"
        "ds_read_b128 a[204:207], v244 offset:63488\n"
        "s_xor_b32 s66, s17, 6\n"
        "s_lshl_b32 s66, s66, 15\n"
        "v_mfma_f32_16x16x32_bf16 v[56:59], v[208:211], a[216:219], v[56:59]\n"
        "ds_read_b128 a[216:219], v244 offset:60416\n"
        "s_waitcnt lgkmcnt(8)\n"
        "v_mfma_f32_16x16x32_bf16 v[56:59], v[200:203], a[212:215], v[56:59]\n"
        "ds_read_b128 a[212:215], v244 offset:56320\n"
        "v_mfma_f32_16x16x32_bf16 v[56:59], v[192:195], a[208:211], v[56:59]\n"
        "ds_read_b128 a[208:211], v244 offset:52224\n"
        "v_mfma_f32_16x16x32_bf16 v[32:35], v[112:115], a[0:3], v[32:35]\n"
        "ds_read_b128 a[220:223], v244 offset:64512\n"
        "v_mfma_f32_16x16x32_bf16 v[32:35], v[120:123], a[4:7], v[32:35]\n"
        "v_mfma_f32_16x16x32_bf16 v[32:35], a[64:67], a[8:11], v[32:35]\n"
        "v_mfma_f32_16x16x32_bf16 v[32:35], a[72:75], a[12:15], v[32:35]\n"
        "v_mfma_f32_16x16x32_bf16 v[36:39], a[76:79], a[12:15], v[36:39]\n"
        "v_mfma_f32_16x16x32_bf16 v[36:39], a[68:71], a[8:11], v[36:39]\n"
        "s_waitcnt lgkmcnt(3)\n"
        "v_mfma_f32_16x16x32_bf16 v[36:39], v[124:127], a[4:7], v[36:39]\n"
        "v_mfma_f32_16x16x32_bf16 v[36:39], v[116:119], a[0:3], v[36:39]\n"
        "v_lshlrev_b32 v159, 2, v232\n"
        "v_add_u32 v159, 0x8000, v159\n"
        "v_mfma_f32_16x16x32_bf16 v[44:47], v[116:119], a[16:19], v[44:47]\n"
        "v_mfma_f32_16x16x32_bf16 v[44:47], v[124:127], a[20:23], v[44:47]\n"
        "v_mfma_f32_16x16x32_bf16 v[44:47], a[68:71], a[24:27], v[44:47]\n"
        "v_mfma_f32_16x16x32_bf16 v[44:47], a[76:79], a[28:31], v[44:47]\n"
        "v_mfma_f32_16x16x32_bf16 v[40:43], a[72:75], a[28:31], v[40:43]\n"
        "v_mfma_f32_16x16x32_bf16 v[40:43], a[64:67], a[24:27], v[40:43]\n"
        "v_mfma_f32_16x16x32_bf16 v[40:43], v[120:123], a[20:23], v[40:43]\n"
        "v_mfma_f32_16x16x32_bf16 v[40:43], v[112:115], a[16:19], v[40:43]\n"
        "s_waitcnt vmcnt(36)\n"
        "v_mfma_f32_16x16x32_bf16 v[48:51], v[112:115], a[192:195], v[48:51]\n"
        "s_barrier\n"
        "v_mfma_f32_16x16x32_bf16 v[48:51], v[120:123], a[196:199], v[48:51]\n"
        "ds_read_b128 a[0:3], v244 offset:0\n"
        "global_load_lds_dwordx4 v254, s[60:61] offset:0 sc1\n"
        "v_mfma_f32_16x16x32_bf16 v[48:51], a[64:67], a[200:203], v[48:51]\n"
        "ds_read_b128 a[4:7], v244 offset:4096\n"
        "v_mfma_f32_16x16x32_bf16 v[48:51], a[72:75], a[204:207], v[48:51]\n"
        "ds_read_b128 a[8:11], v244 offset:8192\n"
        "v_mfma_f32_16x16x32_bf16 v[52:55], a[76:79], a[204:207], v[52:55]\n"
        "ds_read_b128 a[12:15], v244 offset:12288\n"
        "global_load_lds_dwordx4 v254, s[60:61] offset:1024 sc1\n"
        "v_mfma_f32_16x16x32_bf16 v[52:55], a[68:71], a[200:203], v[52:55]\n"
        "ds_read_b128 a[16:19], v244 offset:1024\n"
        "s_waitcnt lgkmcnt(5)\n"
        "v_mfma_f32_16x16x32_bf16 v[52:55], v[124:127], a[196:199], v[52:55]\n"
        "ds_read_b128 a[20:23], v244 offset:5120\n"
        "v_mfma_f32_16x16x32_bf16 v[52:55], v[116:119], a[192:195], v[52:55]\n"
        "ds_read_b128 a[24:27], v244 offset:9216\n"
        "global_load_lds_dwordx4 v254, s[60:61] offset:2048 sc1\n"
        "v_mfma_f32_16x16x32_bf16 v[60:63], v[116:119], a[208:211], v[60:63]\n"
        "ds_read_b128 a[28:31], v244 offset:13312\n"
        "v_mfma_f32_16x16x32_bf16 v[60:63], v[124:127], a[212:215], v[60:63]\n"
        "ds_read_b128 a[192:195], v244 offset:2048\n"
        "v_mfma_f32_16x16x32_bf16 v[60:63], a[68:71], a[216:219], v[60:63]\n"
        "ds_read_b128 a[196:199], v244 offset:6144\n"
        "global_load_lds_dwordx4 v254, s[60:61] offset:3072 sc1\n"
        "s_add_u32 m0, s73, 0x1c000\n"
        "v_mfma_f32_16x16x32_bf16 v[60:63], a[76:79], a[220:223], v[60:63]\n"
        "ds_read_b128 a[200:203], v244 offset:10240\n"
        "s_add_u32 s60, s68, s66\n"
        "s_addc_u32 s61, s69, 0\n"
        "v_mfma_f32_16x16x32_bf16 v[56:59], a[72:75], a[220:223], v[56:59]\n"
        "ds_read_b128 a[204:207], v244 offset:14336\n"
        "s_xor_b32 s66, s17, 7\n"
        "s_lshl_b32 s66, s66, 15\n"
        "v_mfma_f32_16x16x32_bf16 v[56:59], a[64:67], a[216:219], v[56:59]\n"
        "ds_read_b128 a[216:219], v244 offset:11264\n"
        "s_waitcnt lgkmcnt(8)\n"
        "v_mfma_f32_16x16x32_bf16 v[56:59], v[120:123], a[212:215], v[56:59]\n"
        "ds_read_b128 a[212:215], v244 offset:7168\n"
        "v_mfma_f32_16x16x32_bf16 v[56:59], v[112:115], a[208:211], v[56:59]\n"
        "ds_read_b128 a[208:211], v244 offset:3072\n"
        "v_mfma_f32_16x16x32_bf16 v[32:35], a[80:83], a[0:3], v[32:35]\n"
        "ds_read_b128 a[220:223], v244 offset:15360\n"
        "v_mfma_f32_16x16x32_bf16 v[32:35], a[88:91], a[4:7], v[32:35]\n"
        "v_mfma_f32_16x16x32_bf16 v[32:35], a[160:163], a[8:11], v[32:35]\n"
        "v_mfma_f32_16x16x32_bf16 v[32:35], a[168:171], a[12:15], v[32:35]\n"
        "v_mfma_f32_16x16x32_bf16 v[36:39], a[172:175], a[12:15], v[36:39]\n"
        "v_mfma_f32_16x16x32_bf16 v[36:39], a[164:167], a[8:11], v[36:39]\n"
        "s_waitcnt lgkmcnt(3)\n"
        "v_mfma_f32_16x16x32_bf16 v[36:39], a[92:95], a[4:7], v[36:39]\n"
        "v_mfma_f32_16x16x32_bf16 v[36:39], a[84:87], a[0:3], v[36:39]\n"
        "s_lshl_b32 s16, s63, 12\n"
        "v_and_b32 v150, 1, v233\n"
        "v_lshlrev_b32 v151, 11, v232\n"
        "v_mfma_f32_16x16x32_bf16 v[44:47], a[84:87], a[16:19], v[44:47]\n"
        "s_lshl1_add_u32 s16, s62, s16\n"
        "v_lshl_add_u32 v151, v150, 5, v151\n"
        "v_lshrrev_b32 v150, 1, v233\n"
        "v_mfma_f32_16x16x32_bf16 v[44:47], a[92:95], a[20:23], v[44:47]\n"
        "s_add_u32 s16, s16, 0x3700000\n"
        "v_lshl_add_u32 v151, v150, 4, v151\n"
        "v_lshrrev_b32 v148, 7, v254\n"
        "v_mfma_f32_16x16x32_bf16 v[44:47], a[164:167], a[24:27], v[44:47]\n"
        "s_add_u32 s58, s38, s16\n"
        "s_addc_u32 s59, s39, 0\n"
        "v_lshlrev_b32 v148, 12, v148\n"
        "v_mfma_f32_16x16x32_bf16 v[44:47], a[172:175], a[28:31], v[44:47]\n"
        "s_add_u32 s16, s73, 0x4000\n"
        "v_and_b32 v150, 0x70, v254\n"
        "v_add_u32 v148, v148, v150\n"
        "v_mfma_f32_16x16x32_bf16 v[40:43], a[168:171], a[28:31], v[40:43]\n"
        "v_lshrrev_b32 v149, 6, v254\n"
        "v_lshlrev_b32 v149, 7, v149\n"
        "v_mfma_f32_16x16x32_bf16 v[40:43], a[160:163], a[24:27], v[40:43]\n"
        "v_bfe_u32 v150, v254, 4, 2\n"
        "v_lshl_add_u32 v149, v150, 3, v149\n"
        "v_mfma_f32_16x16x32_bf16 v[40:43], a[88:91], a[20:23], v[40:43]\n"
        "v_mov_b32 v238, 0\n"
        "v_mov_b32 v239, 0\n"
        "v_mfma_f32_16x16x32_bf16 v[40:43], a[80:83], a[16:19], v[40:43]\n"
        "s_waitcnt vmcnt(12)\n"
        "v_mov_b32 v242, 0\n"
        "v_mov_b32 v243, 0\n"
        "v_mfma_f32_16x16x32_bf16 v[48:51], a[80:83], a[192:195], v[48:51]\n"
        "s_barrier\n"
        "v_mfma_f32_16x16x32_bf16 v[48:51], a[88:91], a[196:199], v[48:51]\n"
        "ds_read_b128 a[0:3], v245 offset:16384\n"
        "global_load_lds_dwordx4 v254, s[60:61] offset:0 sc1\n"
        "v_mfma_f32_16x16x32_bf16 v[48:51], a[160:163], a[200:203], v[48:51]\n"
        "ds_read_b128 a[4:7], v245 offset:20480\n"
        "v_and_b32 v246, 1, v232\n"
        "v_mfma_f32_16x16x32_bf16 v[48:51], a[168:171], a[204:207], v[48:51]\n"
        "ds_read_b128 a[8:11], v245 offset:24576\n"
        "v_lshlrev_b32 v246, 4, v246\n"
        "v_mfma_f32_16x16x32_bf16 v[52:55], a[172:175], a[204:207], v[52:55]\n"
        "ds_read_b128 a[12:15], v245 offset:28672\n"
        "global_load_lds_dwordx4 v254, s[60:61] offset:1024 sc1\n"
        "v_mfma_f32_16x16x32_bf16 v[52:55], a[164:167], a[200:203], v[52:55]\n"
        "ds_read_b128 a[16:19], v245 offset:17408\n"
        "v_mov_b32 v247, 0x3f80\n"
        "v_mfma_f32_16x16x32_bf16 v[52:55], a[92:95], a[196:199], v[52:55]\n"
        "ds_read_b128 a[20:23], v245 offset:21504\n"
        "v_lshlrev_b32 v246, v246, v247\n"
        "v_mfma_f32_16x16x32_bf16 v[52:55], a[84:87], a[192:195], v[52:55]\n"
        "ds_read_b128 a[24:27], v245 offset:25600\n"
        "s_waitcnt lgkmcnt(7)\n"
        "v_mfma_f32_16x16x32_bf16 v[60:63], a[84:87], a[208:211], v[60:63]\n"
        "ds_read_b128 a[28:31], v245 offset:29696\n"
        "global_load_lds_dwordx4 v254, s[60:61] offset:2048 sc1\n"
        "LHB1_%=:\n"
        "s_cbranch_execz LHRB_%=\n"
        "v_mfma_f32_16x16x32_bf16 v[60:63], a[92:95], a[212:215], v[60:63]\n"
        "ds_read_b128 a[192:195], v245 offset:18432\n"
        "v_lshrrev_b32 v247, 1, v232\n"
        "v_mfma_f32_16x16x32_bf16 v[60:63], a[164:167], a[216:219], v[60:63]\n"
        "ds_read_b128 a[196:199], v245 offset:22528\n"
        "v_lshlrev_b32 v248, 1, v233\n"
        "v_mfma_f32_16x16x32_bf16 v[60:63], a[172:175], a[220:223], v[60:63]\n"
        "ds_read_b128 a[200:203], v245 offset:26624\n"
        "global_load_lds_dwordx4 v254, s[60:61] offset:3072 sc1\n"
        "s_add_u32 m0, s73, 0x10000\n"
        "v_mfma_f32_16x16x32_bf16 v[56:59], a[168:171], a[220:223], v[56:59]\n"
        "ds_read_b128 a[204:207], v245 offset:30720\n"
        "s_add_u32 s60, s68, s66\n"
        "s_addc_u32 s61, s69, 0\n"
        "v_mfma_f32_16x16x32_bf16 v[56:59], a[160:163], a[216:219], v[56:59]\n"
        "ds_read_b128 a[216:219], v245 offset:27648\n"
        "s_sub_u32 s66, 15, s12\n"
        "v_or_b32 v249, 1, v248\n"
        "v_mfma_f32_16x16x32_bf16 v[56:59], a[88:91], a[212:215], v[56:59]\n"
        "ds_read_b128 a[212:215], v245 offset:23552\n"
        "s_cmp_lt_u32 s10, 2\n"
        "s_cselect_b32 s66, s12, s66\n"
        "v_mfma_f32_16x16x32_bf16 v[56:59], a[80:83], a[208:211], v[56:59]\n"
        "ds_read_b128 a[208:211], v245 offset:19456\n"
        "s_mul_i32 s66, s66, 1052672\n"
        "s_waitcnt lgkmcnt(8)\n"
        "v_mfma_f32_16x16x32_bf16 v[32:35], a[176:179], a[0:3], v[32:35]\n"
        "s_cmp_lg_u32 s40, 0\n"
        "s_cbranch_scc1 LW2_%=\n"
        "s_mov_b64 exec, 0xffff\n"
        "global_store_dwordx2 v234, v[250:251], s[48:49] sc1\n"
        "s_mov_b64 exec, -1\n"
        "LW2_%=:\n"
        "s_add_u32 s66, s66, s67\n"
        "v_mfma_f32_16x16x32_bf16 v[32:35], a[184:187], a[4:7], v[32:35]\n"
        "ds_read_b128 a[220:223], v245 offset:31744\n"
        "s_add_u32 s68, s20, s66\n"
        "s_addc_u32 s69, s21, 0\n"
        "v_mfma_f32_16x16x32_bf16 v[32:35], a[32:35], a[8:11], v[32:35]\n"
        "s_xor_b32 s66, s17, 0\n"
        "s_lshl_b32 s66, s66, 15\n"
        "v_bfe_u32 v234, v254, 4, 3\n"
        "v_mfma_f32_16x16x32_bf16 v[32:35], a[40:43], a[12:15], v[32:35]\n"
        "v_lshl_add_u32 v234, v234, 7, s14\n"
        "s_lshl_b32 s14, s40, 6\n"
        "v_mfma_f32_16x16x32_bf16 v[36:39], a[44:47], a[12:15], v[36:39]\n"
        "v_add_u32 v225, s14, v159\n"
        "v_cmp_eq_u32 s[14:15], v247, v248\n"
        "v_mfma_f32_16x16x32_bf16 v[36:39], a[36:39], a[8:11], v[36:39]\n"
        "v_add_u32 v149, s16, v149\n"
        "v_cndmask_b32 v240, 0, v246, s[14:15]\n"
        "v_mfma_f32_16x16x32_bf16 v[36:39], a[188:191], a[4:7], v[36:39]\n"
        "v_cmp_eq_u32 s[14:15], v247, v249\n"
        "v_mfma_f32_16x16x32_bf16 v[36:39], a[180:183], a[0:3], v[36:39]\n"
        "s_waitcnt lgkmcnt(1)\n"
        "v_cndmask_b32 v241, 0, v246, s[14:15]\n"
        "v_mfma_f32_16x16x32_bf16 v[44:47], a[180:183], a[16:19], v[44:47]\n"
        "v_mfma_f32_16x16x32_bf16 v[44:47], a[188:191], a[20:23], v[44:47]\n"
        "v_mfma_f32_16x16x32_bf16 v[44:47], a[36:39], a[24:27], v[44:47]\n"
        "v_mfma_f32_16x16x32_bf16 v[44:47], a[44:47], a[28:31], v[44:47]\n"
        "v_mfma_f32_16x16x32_bf16 v[40:43], a[40:43], a[28:31], v[40:43]\n"
        "v_mfma_f32_16x16x32_bf16 v[40:43], a[32:35], a[24:27], v[40:43]\n"
        "v_mfma_f32_16x16x32_bf16 v[40:43], a[184:187], a[20:23], v[40:43]\n"
        "v_mfma_f32_16x16x32_bf16 v[40:43], a[176:179], a[16:19], v[40:43]\n"
        "v_mfma_f32_16x16x32_bf16 v[48:51], a[176:179], a[192:195], v[48:51]\n"
        "s_barrier\n"
        "v_mfma_f32_16x16x32_bf16 v[48:51], a[184:187], a[196:199], v[48:51]\n"
        "ds_read_b128 a[0:3], v244 offset:16384\n"
        "global_load_lds_dwordx4 v254, s[60:61] offset:0 sc1\n"
        "v_mfma_f32_16x16x32_bf16 v[48:51], a[32:35], a[200:203], v[48:51]\n"
        "ds_read_b128 a[4:7], v244 offset:20480\n"
        "v_mfma_f32_16x16x32_bf16 v[48:51], a[40:43], a[204:207], v[48:51]\n"
        "ds_read_b128 a[8:11], v244 offset:24576\n"
        "v_mfma_f32_16x16x32_bf16 v[52:55], a[44:47], a[204:207], v[52:55]\n"
        "ds_read_b128 a[12:15], v244 offset:28672\n"
        "global_load_lds_dwordx4 v254, s[60:61] offset:1024 sc1\n"
        "v_mfma_f32_16x16x32_bf16 v[52:55], a[36:39], a[200:203], v[52:55]\n"
        "ds_read_b128 a[28:31], v244 offset:29696\n"
        "v_mfma_f32_16x16x32_bf16 v[52:55], a[188:191], a[196:199], v[52:55]\n"
        "ds_read_b128 a[24:27], v244 offset:25600\n"
        "v_mfma_f32_16x16x32_bf16 v[52:55], a[180:183], a[192:195], v[52:55]\n"
        "ds_read_b128 a[20:23], v244 offset:21504\n"
        "global_load_lds_dwordx4 v254, s[60:61] offset:2048 sc1\n"
        "v_mfma_f32_16x16x32_bf16 v[60:63], a[180:183], a[208:211], v[60:63]\n"
        "ds_read_b128 a[16:19], v244 offset:17408\n"
        "v_mfma_f32_16x16x32_bf16 v[60:63], a[188:191], a[212:215], v[60:63]\n"
        "ds_read_b128 a[192:195], v244 offset:18432\n"
        "s_waitcnt lgkmcnt(8)\n"
        "v_mfma_f32_16x16x32_bf16 v[32:35], a[48:51], a[0:3], v[32:35]\n"
        "ds_read_b128 a[196:199], v244 offset:22528\n"
        "s_waitcnt lgkmcnt(8)\n"
        "v_mfma_f32_16x16x32_bf16 v[32:35], a[56:59], a[4:7], v[32:35]\n"
        "ds_read_b128 a[200:203], v244 offset:26624\n"
        "s_waitcnt lgkmcnt(8)\n"
        "v_mfma_f32_16x16x32_bf16 v[32:35], a[224:227], a[8:11], v[32:35]\n"
        "ds_read_b128 a[204:207], v244 offset:30720\n"
        "s_waitcnt lgkmcnt(8)\n"
        "v_mfma_f32_16x16x32_bf16 v[32:35], a[232:235], a[12:15], v[32:35]\n"
        "global_load_lds_dwordx4 v254, s[60:61] offset:3072 sc1\n"
        "s_add_u32 m0, s73, 0x24000\n"
        "v_mfma_f32_16x16x32_bf16 v[36:39], a[236:239], a[12:15], v[36:39]\n"
        "s_add_u32 s60, s68, s66\n"
        "s_addc_u32 s61, s69, 0\n"
        "s_xor_b32 s66, s17, 1\n"
        "v_mfma_f32_16x16x32_bf16 v[36:39], a[228:231], a[8:11], v[36:39]\n"
        "s_lshl_b32 s66, s66, 15\n"
        "v_mfma_f32_16x16x32_bf16 v[36:39], a[60:63], a[4:7], v[36:39]\n"
        "v_cvt_pk_bf16_f32 v32, v32, v33\n"
        "v_cvt_pk_bf16_f32 v33, v34, v35\n"
        "v_mfma_f32_16x16x32_bf16 v[36:39], a[52:55], a[0:3], v[36:39]\n"
        "s_waitcnt lgkmcnt(3)\n"
        "v_mfma_f32_16x16x32_bf16 v[60:63], a[36:39], a[216:219], v[60:63]\n"
        "v_mfma_f32_16x16x32_bf16 v[60:63], a[44:47], a[220:223], v[60:63]\n"
        "v_mfma_f32_16x16x32_bf16 v[40:43], a[232:235], a[28:31], v[40:43]\n"
        "v_mfma_f32_16x16x32_bf16 v[40:43], a[224:227], a[24:27], v[40:43]\n"
        "v_mfma_f32_16x16x32_bf16 v[40:43], a[56:59], a[20:23], v[40:43]\n"
        "v_mfma_f32_16x16x32_bf16 v[40:43], a[48:51], a[16:19], v[40:43]\n"
        "s_nop 0\n"
        "v_cvt_pk_bf16_f32 v34, v36, v37\n"
        "v_cvt_pk_bf16_f32 v35, v38, v39\n"
        "v_mfma_f32_16x16x32_bf16 v[44:47], a[52:55], a[16:19], v[44:47]\n"
        "global_store_dwordx4 v254, v[32:35], s[28:29] offset:0 sc1\n"
        "s_barrier\n"
        "ds_read_b128 a[0:3], v254 offset:0\n"
        "v_mfma_f32_16x16x32_bf16 v[44:47], a[60:63], a[20:23], v[44:47]\n"
        "ds_read_b128 a[4:7], v254 offset:4096\n"
        "v_cvt_pk_bf16_f32 v40, v40, v41\n"
        "v_mfma_f32_16x16x32_bf16 v[44:47], a[228:231], a[24:27], v[44:47]\n"
        "ds_read_b128 a[8:11], v254 offset:8192\n"
        "v_cvt_pk_bf16_f32 v41, v42, v43\n"
        "v_mfma_f32_16x16x32_bf16 v[44:47], a[236:239], a[28:31], v[44:47]\n"
        "ds_read_b128 a[12:15], v254 offset:12288\n"
        "global_load_lds_dwordx4 v254, s[60:61] offset:0 sc1\n"
        "v_mfma_f32_16x16x32_bf16 v[56:59], a[40:43], a[220:223], v[56:59]\n"
        "ds_read_b128 a[220:223], v244 offset:31744\n"
        "v_mfma_f32_16x16x32_bf16 v[56:59], a[32:35], a[216:219], v[56:59]\n"
        "ds_read_b128 a[216:219], v244 offset:27648\n"
        "s_waitcnt lgkmcnt(6)\n"
        "v_mfma_f32_16x16x32_bf16 v[56:59], a[184:187], a[212:215], v[56:59]\n"
        "ds_read_b128 a[212:215], v244 offset:23552\n"
        "v_cvt_pk_bf16_f32 v42, v44, v45\n"
        "v_mfma_f32_16x16x32_bf16 v[56:59], a[176:179], a[208:211], v[56:59]\n"
        "ds_read_b128 a[208:211], v244 offset:19456\n"
        "v_cvt_pk_bf16_f32 v43, v46, v47\n"
        "v_mfma_f32_16x16x32_bf16 v[48:51], a[48:51], a[192:195], v[48:51]\n"
        "global_store_dwordx4 v254, v[40:43], s[28:29] offset:1024 sc1\n"
        "ds_read_b128 a[16:19], v254 offset:1024\n"
        "ds_read_b128 a[20:23], v254 offset:5120\n"
        "v_mfma_f32_16x16x32_bf16 v[48:51], a[56:59], a[196:199], v[48:51]\n"
        "ds_read_b128 a[24:27], v254 offset:9216\n"
        "v_mfma_f32_16x16x32_bf16 v[48:51], a[224:227], a[200:203], v[48:51]\n"
        "ds_read_b128 a[28:31], v254 offset:13312\n"
        "global_load_lds_dwordx4 v254, s[60:61] offset:1024 sc1\n"
        "v_mfma_f32_16x16x32_bf16 v[48:51], a[232:235], a[204:207], v[48:51]\n"
        "v_mfma_f32_16x16x32_bf16 v[52:55], a[236:239], a[204:207], v[52:55]\n"
        "ds_read_b128 a[204:207], v254 offset:14336\n"
        "s_waitcnt lgkmcnt(7)\n"
        "v_mfma_f32_16x16x32_bf16 v[52:55], a[228:231], a[200:203], v[52:55]\n"
        "ds_read_b128 a[200:203], v254 offset:10240\n"
        "global_load_lds_dwordx4 v254, s[60:61] offset:2048 sc1\n"
        "v_mfma_f32_16x16x32_bf16 v[52:55], a[60:63], a[196:199], v[52:55]\n"
        "ds_read_b128 a[196:199], v254 offset:6144\n"
        "v_cvt_pk_bf16_f32 v48, v48, v49\n"
        "v_mfma_f32_16x16x32_bf16 v[52:55], a[52:55], a[192:195], v[52:55]\n"
        "s_waitcnt lgkmcnt(7)\n"
        "v_cvt_pk_bf16_f32 v49, v50, v51\n"
        "v_mfma_f32_16x16x32_bf16 v[56:59], a[232:235], a[220:223], v[56:59]\n"
        "ds_read_b128 a[192:195], v254 offset:2048\n"
        "global_load_lds_dwordx4 v254, s[60:61] offset:3072 sc1\n"
        "s_add_u32 m0, s73, 0x14000\n"
        "v_mfma_f32_16x16x32_bf16 v[56:59], a[224:227], a[216:219], v[56:59]\n"
        "s_add_u32 s60, s68, s66\n"
        "s_addc_u32 s61, s69, 0\n"
        "v_cvt_pk_bf16_f32 v50, v52, v53\n"
        "v_mfma_f32_16x16x32_bf16 v[56:59], a[56:59], a[212:215], v[56:59]\n"
        "s_xor_b32 s66, s17, 2\n"
        "v_cvt_pk_bf16_f32 v51, v54, v55\n"
        "s_lshl_b32 s66, s66, 15\n"
        "v_mfma_f32_16x16x32_bf16 v[56:59], a[48:51], a[208:211], v[56:59]\n"
        "v_mfma_f32_16x16x32_bf16 v[60:63], a[52:55], a[208:211], v[60:63]\n"
        "global_store_dwordx4 v254, v[48:51], s[28:29] offset:2048 sc1\n"
        "ds_read_b128 a[208:211], v254 offset:3072\n"
        "v_mfma_f32_16x16x32_bf16 v[60:63], a[60:63], a[212:215], v[60:63]\n"
        "ds_read_b128 a[212:215], v254 offset:7168\n"
        "v_mfma_f32_16x16x32_bf16 v[60:63], a[228:231], a[216:219], v[60:63]\n"
        "ds_read_b128 a[216:219], v254 offset:11264\n"
        "v_mfma_f32_16x16x32_bf16 v[60:63], a[236:239], a[220:223], v[60:63]\n"
        "ds_read_b128 a[220:223], v254 offset:15360\n"
        "v_cvt_pk_bf16_f32 v56, v56, v57\n"
        "v_mfma_f32_16x16x32_bf16 v[0:3], a[96:99], a[0:3], 0\n"
        "v_cvt_pk_bf16_f32 v57, v58, v59\n"
        "v_mfma_f32_16x16x32_bf16 v[0:3], a[104:107], a[4:7], v[0:3]\n"
        "v_mfma_f32_16x16x32_bf16 v[0:3], a[112:115], a[8:11], v[0:3]\n"
        "v_mfma_f32_16x16x32_bf16 v[0:3], a[120:123], a[12:15], v[0:3]\n"
        "v_mfma_f32_16x16x32_bf16 v[4:7], a[124:127], a[12:15], 0\n"
        "s_waitcnt lgkmcnt(3)\n"
        "v_cvt_pk_bf16_f32 v58, v60, v61\n"
        "v_cvt_pk_bf16_f32 v59, v62, v63\n"
        "v_mfma_f32_16x16x32_bf16 v[4:7], a[116:119], a[8:11], v[4:7]\n"
        "global_store_dwordx4 v254, v[56:59], s[28:29] offset:3072 sc1\n"
        "v_mfma_f32_16x16x32_bf16 v[4:7], a[108:111], a[4:7], v[4:7]\n"
        "v_mfma_f32_16x16x32_bf16 v[4:7], a[100:103], a[0:3], v[4:7]\n"
        "v_mfma_f32_16x16x32_bf16 v[12:15], a[100:103], a[16:19], 0\n"
        "v_mfma_f32_16x16x32_bf16 v[12:15], a[108:111], a[20:23], v[12:15]\n"
        "v_mfma_f32_16x16x32_bf16 v[12:15], a[116:119], a[24:27], v[12:15]\n"
        "v_mfma_f32_16x16x32_bf16 v[12:15], a[124:127], a[28:31], v[12:15]\n"
        "s_lshl2_add_u32 s15, s76, s10\n"
        "s_mul_i32 s16, s13, 263168\n"
        "s_lshl_b32 s15, s15, 14\n"
        "v_mfma_f32_16x16x32_bf16 v[8:11], a[120:123], a[28:31], 0\n"
        "s_add_u32 s16, s16, s15\n"
        "s_add_u32 s16, s16, s73\n"
        "s_add_u32 s16, s16, 0x4000000\n"
        "v_mfma_f32_16x16x32_bf16 v[8:11], a[112:115], a[24:27], v[8:11]\n"
        "s_add_u32 s28, s38, s16\n"
        "s_addc_u32 s29, s39, 0\n"
        "v_mfma_f32_16x16x32_bf16 v[8:11], a[104:107], a[20:23], v[8:11]\n"
        "v_mfma_f32_16x16x32_bf16 v[8:11], a[96:99], a[16:19], v[8:11]\n"
        "global_load_dwordx2 v[236:237], v234, s[48:49] sc1\n"
        "v_mfma_f32_16x16x32_bf16 v[16:19], a[96:99], a[192:195], 0\n"
        "s_barrier\n"
        "v_mfma_f32_16x16x32_bf16 v[16:19], a[104:107], a[196:199], v[16:19]\n"
        "ds_read_b128 a[0:3], v254 offset:32768\n"
        "global_load_lds_dwordx4 v254, s[60:61] offset:0 sc1\n"
        "v_mfma_f32_16x16x32_bf16 v[16:19], a[112:115], a[200:203], v[16:19]\n"
        "ds_read_b128 a[4:7], v254 offset:36864\n"
        "v_mfma_f32_16x16x32_bf16 v[16:19], a[120:123], a[204:207], v[16:19]\n"
        "ds_read_b128 a[8:11], v254 offset:40960\n"
        "v_mfma_f32_16x16x32_bf16 v[20:23], a[124:127], a[204:207], 0\n"
        "ds_read_b128 a[12:15], v254 offset:45056\n"
        "global_load_lds_dwordx4 v254, s[60:61] offset:1024 sc1\n"
        "v_mfma_f32_16x16x32_bf16 v[20:23], a[116:119], a[200:203], v[20:23]\n"
        "ds_read_b128 a[16:19], v254 offset:33792\n"
        "s_waitcnt lgkmcnt(5)\n"
        "v_mfma_f32_16x16x32_bf16 v[20:23], a[108:111], a[196:199], v[20:23]\n"
        "ds_read_b128 a[20:23], v254 offset:37888\n"
        "v_mfma_f32_16x16x32_bf16 v[20:23], a[100:103], a[192:195], v[20:23]\n"
        "ds_read_b128 a[24:27], v254 offset:41984\n"
        "global_load_lds_dwordx4 v254, s[60:61] offset:2048 sc1\n"
        "v_mfma_f32_16x16x32_bf16 v[28:31], a[100:103], a[208:211], 0\n"
        "ds_read_b128 a[28:31], v254 offset:46080\n"
        "v_mfma_f32_16x16x32_bf16 v[28:31], a[108:111], a[212:215], v[28:31]\n"
        "ds_read_b128 a[192:195], v254 offset:34816\n"
        "v_mfma_f32_16x16x32_bf16 v[28:31], a[116:119], a[216:219], v[28:31]\n"
        "ds_read_b128 a[196:199], v254 offset:38912\n"
        "global_load_lds_dwordx4 v254, s[60:61] offset:3072 sc1\n"
        "s_add_u32 m0, s73, 0x0\n"
        "v_mfma_f32_16x16x32_bf16 v[28:31], a[124:127], a[220:223], v[28:31]\n"
        "ds_read_b128 a[200:203], v254 offset:43008\n"
        "s_add_u32 s60, s68, s66\n"
        "s_addc_u32 s61, s69, 0\n"
        "v_mfma_f32_16x16x32_bf16 v[24:27], a[120:123], a[220:223], 0\n"
        "ds_read_b128 a[204:207], v254 offset:47104\n"
        "s_xor_b32 s66, s17, 3\n"
        "s_lshl_b32 s66, s66, 15\n"
        "v_mfma_f32_16x16x32_bf16 v[24:27], a[112:115], a[216:219], v[24:27]\n"
        "ds_read_b128 a[216:219], v254 offset:44032\n"
        "s_waitcnt lgkmcnt(8)\n"
        "v_mfma_f32_16x16x32_bf16 v[24:27], a[104:107], a[212:215], v[24:27]\n"
        "ds_read_b128 a[212:215], v254 offset:39936\n"
        "v_mfma_f32_16x16x32_bf16 v[24:27], a[96:99], a[208:211], v[24:27]\n"
        "ds_read_b128 a[208:211], v254 offset:35840\n"
        "v_mfma_f32_16x16x32_bf16 v[0:3], a[128:131], a[0:3], v[0:3]\n"
        "ds_read_b128 a[220:223], v254 offset:48128\n"
        "v_mfma_f32_16x16x32_bf16 v[0:3], a[136:139], a[4:7], v[0:3]\n"
        "v_mfma_f32_16x16x32_bf16 v[0:3], a[144:147], a[8:11], v[0:3]\n"
        "v_mfma_f32_16x16x32_bf16 v[0:3], a[152:155], a[12:15], v[0:3]\n"
        "v_mfma_f32_16x16x32_bf16 v[4:7], a[156:159], a[12:15], v[4:7]\n"
        "v_mfma_f32_16x16x32_bf16 v[4:7], a[148:151], a[8:11], v[4:7]\n"
        "s_waitcnt lgkmcnt(3)\n"
        "v_mfma_f32_16x16x32_bf16 v[4:7], a[140:143], a[4:7], v[4:7]\n"
        "v_mfma_f32_16x16x32_bf16 v[4:7], a[132:135], a[0:3], v[4:7]\n"
        "v_mfma_f32_16x16x32_bf16 v[12:15], a[132:135], a[16:19], v[12:15]\n"
        "v_mfma_f32_16x16x32_bf16 v[12:15], a[140:143], a[20:23], v[12:15]\n"
        "v_mfma_f32_16x16x32_bf16 v[12:15], a[148:151], a[24:27], v[12:15]\n"
        "v_mfma_f32_16x16x32_bf16 v[12:15], a[156:159], a[28:31], v[12:15]\n"
        "v_mfma_f32_16x16x32_bf16 v[8:11], a[152:155], a[28:31], v[8:11]\n"
        "v_mfma_f32_16x16x32_bf16 v[8:11], a[144:147], a[24:27], v[8:11]\n"
        "v_mfma_f32_16x16x32_bf16 v[8:11], a[136:139], a[20:23], v[8:11]\n"
        "v_mfma_f32_16x16x32_bf16 v[8:11], a[128:131], a[16:19], v[8:11]\n"
        "v_mfma_f32_16x16x32_bf16 v[16:19], a[128:131], a[192:195], v[16:19]\n"
        "s_barrier\n"
        "v_mfma_f32_16x16x32_bf16 v[16:19], a[136:139], a[196:199], v[16:19]\n"
        "ds_read_b128 a[0:3], v254 offset:16384\n"
        "global_load_lds_dwordx4 v254, s[60:61] offset:0 sc1\n"
        "v_mfma_f32_16x16x32_bf16 v[16:19], a[144:147], a[200:203], v[16:19]\n"
        "ds_read_b128 a[4:7], v254 offset:20480\n"
        "v_mfma_f32_16x16x32_bf16 v[16:19], a[152:155], a[204:207], v[16:19]\n"
        "ds_read_b128 a[8:11], v254 offset:24576\n"
        "v_mfma_f32_16x16x32_bf16 v[20:23], a[156:159], a[204:207], v[20:23]\n"
        "ds_read_b128 a[12:15], v254 offset:28672\n"
        "global_load_lds_dwordx4 v254, s[60:61] offset:1024 sc1\n"
        "v_mfma_f32_16x16x32_bf16 v[20:23], a[148:151], a[200:203], v[20:23]\n"
        "ds_read_b128 a[16:19], v254 offset:17408\n"
        "s_waitcnt lgkmcnt(5)\n"
        "v_mfma_f32_16x16x32_bf16 v[20:23], a[140:143], a[196:199], v[20:23]\n"
        "ds_read_b128 a[20:23], v254 offset:21504\n"
        "v_mfma_f32_16x16x32_bf16 v[20:23], a[132:135], a[192:195], v[20:23]\n"
        "ds_read_b128 a[24:27], v254 offset:25600\n"
        "global_load_lds_dwordx4 v254, s[60:61] offset:2048 sc1\n"
        "v_mfma_f32_16x16x32_bf16 v[28:31], a[132:135], a[208:211], v[28:31]\n"
        "ds_read_b128 a[28:31], v254 offset:29696\n"
        "v_mfma_f32_16x16x32_bf16 v[28:31], a[140:143], a[212:215], v[28:31]\n"
        "ds_read_b128 a[192:195], v254 offset:18432\n"
        "v_mfma_f32_16x16x32_bf16 v[28:31], a[148:151], a[216:219], v[28:31]\n"
        "ds_read_b128 a[196:199], v254 offset:22528\n"
        "global_load_lds_dwordx4 v254, s[60:61] offset:3072 sc1\n"
        "s_add_u32 m0, s73, 0x4000\n"
        "v_mfma_f32_16x16x32_bf16 v[28:31], a[156:159], a[220:223], v[28:31]\n"
        "ds_read_b128 a[200:203], v254 offset:26624\n"
        "s_add_u32 s60, s68, s66\n"
        "s_addc_u32 s61, s69, 0\n"
        "v_mfma_f32_16x16x32_bf16 v[24:27], a[152:155], a[220:223], v[24:27]\n"
        "ds_read_b128 a[204:207], v254 offset:30720\n"
        "s_xor_b32 s66, s17, 4\n"
        "s_lshl_b32 s66, s66, 15\n"
        "v_mfma_f32_16x16x32_bf16 v[24:27], a[144:147], a[216:219], v[24:27]\n"
        "ds_read_b128 a[216:219], v254 offset:27648\n"
        "s_waitcnt lgkmcnt(8)\n"
        "v_mfma_f32_16x16x32_bf16 v[24:27], a[136:139], a[212:215], v[24:27]\n"
        "ds_read_b128 a[212:215], v254 offset:23552\n"
        "v_mfma_f32_16x16x32_bf16 v[24:27], a[128:131], a[208:211], v[24:27]\n"
        "ds_read_b128 a[208:211], v254 offset:19456\n"
        "v_mfma_f32_16x16x32_bf16 v[0:3], v[160:163], a[0:3], v[0:3]\n"
        "ds_read_b128 a[220:223], v254 offset:31744\n"
        "v_mfma_f32_16x16x32_bf16 v[0:3], v[168:171], a[4:7], v[0:3]\n"
        "v_mfma_f32_16x16x32_bf16 v[0:3], v[176:179], a[8:11], v[0:3]\n"
        "v_mfma_f32_16x16x32_bf16 v[0:3], v[184:187], a[12:15], v[0:3]\n"
        "v_mfma_f32_16x16x32_bf16 v[4:7], v[188:191], a[12:15], v[4:7]\n"
        "v_mfma_f32_16x16x32_bf16 v[4:7], v[180:183], a[8:11], v[4:7]\n"
        "s_waitcnt lgkmcnt(3)\n"
        "v_mfma_f32_16x16x32_bf16 v[4:7], v[172:175], a[4:7], v[4:7]\n"
        "v_mfma_f32_16x16x32_bf16 v[4:7], v[164:167], a[0:3], v[4:7]\n"
        "v_mfma_f32_16x16x32_bf16 v[12:15], v[164:167], a[16:19], v[12:15]\n"
        "v_mfma_f32_16x16x32_bf16 v[12:15], v[172:175], a[20:23], v[12:15]\n"
        "v_mfma_f32_16x16x32_bf16 v[12:15], v[180:183], a[24:27], v[12:15]\n"
        "v_mfma_f32_16x16x32_bf16 v[12:15], v[188:191], a[28:31], v[12:15]\n"
        "v_mfma_f32_16x16x32_bf16 v[8:11], v[184:187], a[28:31], v[8:11]\n"
        "v_mfma_f32_16x16x32_bf16 v[8:11], v[176:179], a[24:27], v[8:11]\n"
        "v_mfma_f32_16x16x32_bf16 v[8:11], v[168:171], a[20:23], v[8:11]\n"
        "v_mfma_f32_16x16x32_bf16 v[8:11], v[160:163], a[16:19], v[8:11]\n"
        "s_waitcnt vmcnt(33)\n"
        "v_mfma_f32_16x16x32_bf16 v[16:19], v[160:163], a[192:195], v[16:19]\n"
        "s_barrier\n"
        "v_mfma_f32_16x16x32_bf16 v[16:19], v[168:171], a[196:199], v[16:19]\n"
        "ds_write_b32 v225, v224\n"
        "v_mfma_f32_16x16x32_bf16 v[16:19], v[176:179], a[200:203], v[16:19]\n"
        "ds_read_b128 a[0:3], v244 offset:32768\n"
        "v_mfma_f32_16x16x32_bf16 v[16:19], v[184:187], a[204:207], v[16:19]\n"
        "ds_read_b128 a[4:7], v244 offset:36864\n"
        "v_mfma_f32_16x16x32_bf16 v[20:23], v[188:191], a[204:207], v[20:23]\n"
        "ds_read_b128 a[8:11], v244 offset:40960\n"
        "s_waitcnt lgkmcnt(4)\n"
        "v_mfma_f32_16x16x32_bf16 v[20:23], v[180:183], a[200:203], v[20:23]\n"
        "ds_read_b128 a[12:15], v244 offset:45056\n"
        "v_mfma_f32_16x16x32_bf16 v[20:23], v[172:175], a[196:199], v[20:23]\n"
        "ds_read_b128 a[16:19], v244 offset:33792\n"
        "v_mfma_f32_16x16x32_bf16 v[20:23], v[164:167], a[192:195], v[20:23]\n"
        "ds_read_b128 a[20:23], v244 offset:37888\n"
        "v_mfma_f32_16x16x32_bf16 v[28:31], v[164:167], a[208:211], v[28:31]\n"
        "ds_read_b128 a[24:27], v244 offset:41984\n"
        "v_mfma_f32_16x16x32_bf16 v[28:31], v[172:175], a[212:215], v[28:31]\n"
        "ds_read_b128 a[28:31], v244 offset:46080\n"
        "s_lshl2_add_u32 s14, s13, s10\n"
        "s_lshl2_add_u32 s14, s14, s40\n"
        "v_mfma_f32_16x16x32_bf16 v[28:31], v[180:183], a[216:219], v[28:31]\n"
        "ds_read_b128 a[192:195], v244 offset:34816\n"
        "s_add_u32 s14, s14, 3072\n"
        "s_lshl_b32 s14, s14, 7\n"
        "v_mfma_f32_16x16x32_bf16 v[28:31], v[188:191], a[220:223], v[28:31]\n"
        "ds_read_b128 a[196:199], v244 offset:38912\n"
        "v_mfma_f32_16x16x32_bf16 v[24:27], v[184:187], a[220:223], v[24:27]\n"
        "ds_read_b128 a[200:203], v244 offset:43008\n"
        "s_waitcnt lgkmcnt(8)\n"
        "v_mfma_f32_16x16x32_bf16 v[24:27], v[176:179], a[216:219], v[24:27]\n"
        "ds_read_b128 a[204:207], v244 offset:47104\n"
        "v_mfma_f32_16x16x32_bf16 v[24:27], v[168:171], a[212:215], v[24:27]\n"
        "ds_read_b128 a[212:215], v244 offset:39936\n"
        "v_mfma_f32_16x16x32_bf16 v[24:27], v[160:163], a[208:211], v[24:27]\n"
        "ds_read_b128 a[208:211], v244 offset:35840\n"
        "s_waitcnt lgkmcnt(8)\n"
        "v_mfma_f32_16x16x32_bf16 v[0:3], v[192:195], a[0:3], v[0:3]\n"
        "ds_read_b128 a[216:219], v244 offset:44032\n"
        "v_mfma_f32_16x16x32_bf16 v[0:3], v[200:203], a[4:7], v[0:3]\n"
        "ds_read_b128 a[220:223], v244 offset:48128\n"
        "v_mfma_f32_16x16x32_bf16 v[0:3], v[208:211], a[8:11], v[0:3]\n"
        "v_mfma_f32_16x16x32_bf16 v[0:3], v[216:219], a[12:15], v[0:3]\n"
        "v_mfma_f32_16x16x32_bf16 v[4:7], v[220:223], a[12:15], v[4:7]\n"
        "v_mfma_f32_16x16x32_bf16 v[4:7], v[212:215], a[8:11], v[4:7]\n"
        "v_mfma_f32_16x16x32_bf16 v[4:7], v[204:207], a[4:7], v[4:7]\n"
        "s_waitcnt lgkmcnt(3)\n"
        "v_mfma_f32_16x16x32_bf16 v[4:7], v[196:199], a[0:3], v[4:7]\n"
        "v_mfma_f32_16x16x32_bf16 v[12:15], v[196:199], a[16:19], v[12:15]\n"
        "v_mfma_f32_16x16x32_bf16 v[12:15], v[204:207], a[20:23], v[12:15]\n"
        "v_mfma_f32_16x16x32_bf16 v[12:15], v[212:215], a[24:27], v[12:15]\n"
        "v_mfma_f32_16x16x32_bf16 v[12:15], v[220:223], a[28:31], v[12:15]\n"
        "v_mfma_f32_16x16x32_bf16 v[8:11], v[216:219], a[28:31], v[8:11]\n"
        "v_mfma_f32_16x16x32_bf16 v[8:11], v[208:211], a[24:27], v[8:11]\n"
        "s_waitcnt vmcnt(8) lgkmcnt(0)\n"
        "v_cmp_ne_u64 vcc, v[236:237], v[250:251]\n"
        "v_mfma_f32_16x16x32_bf16 v[8:11], v[200:203], a[20:23], v[8:11]\n"
        "s_cbranch_vccnz LPZZ_%=\n"
        "LBZZ_%=:\n"
        "v_lshrrev_b32 v234, 1, v254\n"
        "v_add_u32 v234, s14, v234\n"
        "v_mfma_f32_16x16x32_bf16 v[8:11], v[192:195], a[16:19], v[8:11]\n"
        "v_mfma_f32_16x16x32_bf16 v[16:19], v[192:195], a[192:195], v[16:19]\n"
        "s_barrier\n"
        "v_mfma_f32_16x16x32_bf16 v[16:19], v[200:203], a[196:199], v[16:19]\n"
        "ds_read_b128 a[0:3], v254 offset:49152\n"
        "global_load_lds_dwordx4 v148, s[58:59] offset:0 sc1\n"
        "s_add_u32 m0, s73, 0x18000\n"
        "v_mfma_f32_16x16x32_bf16 v[16:19], v[208:211], a[200:203], v[16:19]\n"
        "ds_read_b128 a[4:7], v254 offset:53248\n"
        "s_add_u32 s58, s58, 0x8000\n"
        "s_addc_u32 s59, s59, 0\n"
        "v_mfma_f32_16x16x32_bf16 v[16:19], v[216:219], a[204:207], v[16:19]\n"
        "ds_read_b128 a[8:11], v254 offset:57344\n"
        "v_mfma_f32_16x16x32_bf16 v[20:23], v[220:223], a[204:207], v[20:23]\n"
        "ds_read_b128 a[12:15], v254 offset:61440\n"
        "global_load_dwordx4 v[96:99], v151, s[64:65]\n"
        "v_mfma_f32_16x16x32_bf16 v[20:23], v[212:215], a[200:203], v[20:23]\n"
        "ds_read_b128 a[16:19], v254 offset:50176\n"
        "s_add_u32 s64, s64, 0x8000\n"
        "s_addc_u32 s65, s65, 0\n"
        "v_mfma_f32_16x16x32_bf16 v[20:23], v[204:207], a[196:199], v[20:23]\n"
        "ds_read_b128 a[20:23], v254 offset:54272\n"
        "v_mfma_f32_16x16x32_bf16 v[20:23], v[196:199], a[192:195], v[20:23]\n"
        "ds_read_b128 a[24:27], v254 offset:58368\n"
        "v_mfma_f32_16x16x32_bf16 v[28:31], v[196:199], a[208:211], v[28:31]\n"
        "ds_read_b128 a[28:31], v254 offset:62464\n"
        "v_mfma_f32_16x16x32_bf16 v[28:31], v[204:207], a[212:215], v[28:31]\n"
        "ds_read_b128 a[192:195], v254 offset:51200\n"
        "v_mfma_f32_16x16x32_bf16 v[28:31], v[212:215], a[216:219], v[28:31]\n"
        "ds_read_b128 a[196:199], v254 offset:55296\n"
        "v_mfma_f32_16x16x32_bf16 v[28:31], v[220:223], a[220:223], v[28:31]\n"
        "ds_read_b128 a[200:203], v254 offset:59392\n"
        "v_mfma_f32_16x16x32_bf16 v[24:27], v[216:219], a[220:223], v[24:27]\n"
        "ds_read_b128 a[204:207], v254 offset:63488\n"
        "s_waitcnt lgkmcnt(8)\n"
        "v_mfma_f32_16x16x32_bf16 v[24:27], v[208:211], a[216:219], v[24:27]\n"
        "ds_read_b128 a[216:219], v254 offset:60416\n"
        "v_mfma_f32_16x16x32_bf16 v[24:27], v[200:203], a[212:215], v[24:27]\n"
        "ds_read_b128 a[212:215], v254 offset:56320\n"
        "v_mfma_f32_16x16x32_bf16 v[24:27], v[192:195], a[208:211], v[24:27]\n"
        "ds_read_b128 a[208:211], v254 offset:52224\n"
        "s_and_b32 s14, s14, 0xfffff9ff\n"
        "v_bfe_u32 v235, v254, 4, 2\n"
        "v_mfma_f32_16x16x32_bf16 v[0:3], v[112:115], a[0:3], v[0:3]\n"
        "ds_read_b128 a[220:223], v254 offset:64512\n"
        "v_lshl_add_u32 v235, v235, 9, s14\n"
        "v_mfma_f32_16x16x32_bf16 v[0:3], v[120:123], a[4:7], v[0:3]\n"
        "v_mfma_f32_16x16x32_bf16 v[0:3], a[64:67], a[8:11], v[0:3]\n"
        "v_mfma_f32_16x16x32_bf16 v[0:3], a[72:75], a[12:15], v[0:3]\n"
        "v_mfma_f32_16x16x32_bf16 v[4:7], a[76:79], a[12:15], v[4:7]\n"
        "s_waitcnt lgkmcnt(4)\n"
        "v_mfma_f32_16x16x32_bf16 v[4:7], a[68:71], a[8:11], v[4:7]\n"
        "v_mfma_f32_16x16x32_bf16 v[4:7], v[124:127], a[4:7], v[4:7]\n"
        "v_mfma_f32_16x16x32_bf16 v[4:7], v[116:119], a[0:3], v[4:7]\n"
        "v_mfma_f32_16x16x32_bf16 v[12:15], v[116:119], a[16:19], v[12:15]\n"
        "v_mfma_f32_16x16x32_bf16 v[12:15], v[124:127], a[20:23], v[12:15]\n"
        "v_mfma_f32_16x16x32_bf16 v[12:15], a[68:71], a[24:27], v[12:15]\n"
        "v_mfma_f32_16x16x32_bf16 v[12:15], a[76:79], a[28:31], v[12:15]\n"
        "v_mfma_f32_16x16x32_bf16 v[8:11], a[72:75], a[28:31], v[8:11]\n"
        "v_mfma_f32_16x16x32_bf16 v[8:11], a[64:67], a[24:27], v[8:11]\n"
        "v_mfma_f32_16x16x32_bf16 v[8:11], v[120:123], a[20:23], v[8:11]\n"
        "v_mfma_f32_16x16x32_bf16 v[8:11], v[112:115], a[16:19], v[8:11]\n"
        "v_mfma_f32_16x16x32_bf16 v[16:19], v[112:115], a[192:195], v[16:19]\n"
        "s_barrier\n"
        "v_mfma_f32_16x16x32_bf16 v[16:19], v[120:123], a[196:199], v[16:19]\n"
        "ds_read_b128 a[0:3], v245 offset:0\n"
        "global_load_lds_dwordx4 v254, s[60:61] offset:0 sc1\n"
        "v_mfma_f32_16x16x32_bf16 v[16:19], a[64:67], a[200:203], v[16:19]\n"
        "ds_read_b128 a[4:7], v245 offset:4096\n"
        "v_mfma_f32_16x16x32_bf16 v[16:19], a[72:75], a[204:207], v[16:19]\n"
        "ds_read_b128 a[8:11], v245 offset:8192\n"
        "v_mfma_f32_16x16x32_bf16 v[20:23], a[76:79], a[204:207], v[20:23]\n"
        "ds_read_b128 a[12:15], v245 offset:12288\n"
        "global_load_lds_dwordx4 v254, s[60:61] offset:1024 sc1\n"
        "v_mfma_f32_16x16x32_bf16 v[20:23], a[68:71], a[200:203], v[20:23]\n"
        "ds_read_b128 a[16:19], v245 offset:1024\n"
        "s_waitcnt lgkmcnt(5)\n"
        "v_mfma_f32_16x16x32_bf16 v[20:23], v[124:127], a[196:199], v[20:23]\n"
        "ds_read_b128 a[20:23], v245 offset:5120\n"
        "v_mfma_f32_16x16x32_bf16 v[20:23], v[116:119], a[192:195], v[20:23]\n"
        "ds_read_b128 a[24:27], v245 offset:9216\n"
        "global_load_lds_dwordx4 v254, s[60:61] offset:2048 sc1\n"
        "v_mfma_f32_16x16x32_bf16 v[28:31], v[116:119], a[208:211], v[28:31]\n"
        "ds_read_b128 a[28:31], v245 offset:13312\n"
        "v_mfma_f32_16x16x32_bf16 v[28:31], v[124:127], a[212:215], v[28:31]\n"
        "ds_read_b128 a[192:195], v245 offset:2048\n"
        "v_mfma_f32_16x16x32_bf16 v[28:31], a[68:71], a[216:219], v[28:31]\n"
        "ds_read_b128 a[196:199], v245 offset:6144\n"
        "global_load_lds_dwordx4 v254, s[60:61] offset:3072 sc1\n"
        "s_add_u32 m0, s73, 0x4400\n"
        "v_mfma_f32_16x16x32_bf16 v[28:31], a[76:79], a[220:223], v[28:31]\n"
        "ds_read_b128 a[200:203], v245 offset:10240\n"
        "s_add_u32 s60, s68, s66\n"
        "s_addc_u32 s61, s69, 0\n"
        "v_mfma_f32_16x16x32_bf16 v[24:27], a[72:75], a[220:223], v[24:27]\n"
        "ds_read_b128 a[204:207], v245 offset:14336\n"
        "s_xor_b32 s66, s17, 5\n"
        "s_lshl_b32 s66, s66, 15\n"
        "v_mfma_f32_16x16x32_bf16 v[24:27], a[64:67], a[216:219], v[24:27]\n"
        "ds_read_b128 a[216:219], v245 offset:11264\n"
        "global_load_lds_dwordx4 v148, s[58:59] offset:0 sc1\n"
        "s_add_u32 m0, s73, 0xc000\n"
        "v_mfma_f32_16x16x32_bf16 v[24:27], v[120:123], a[212:215], v[24:27]\n"
        "ds_read_b128 a[212:215], v245 offset:7168\n"
        "s_add_u32 s58, s58, 0x8000\n"
        "s_addc_u32 s59, s59, 0\n"
        "v_mfma_f32_16x16x32_bf16 v[24:27], v[112:115], a[208:211], v[24:27]\n"
        "ds_read_b128 a[208:211], v245 offset:3072\n"
        "s_waitcnt lgkmcnt(8)\n"
        "v_mfma_f32_16x16x32_bf16 v[0:3], a[80:83], a[0:3], v[0:3]\n"
        "ds_read_b128 a[220:223], v245 offset:15360\n"
        "global_load_dwordx4 v[100:103], v151, s[64:65]\n"
        "v_mfma_f32_16x16x32_bf16 v[0:3], a[88:91], a[4:7], v[0:3]\n"
        "ds_read_b32 v224, v159 offset:0\n"
        "s_add_u32 s64, s64, 0x8000\n"
        "s_addc_u32 s65, s65, 0\n"
        "v_mfma_f32_16x16x32_bf16 v[0:3], a[160:163], a[8:11], v[0:3]\n"
        "ds_read_b32 v225, v159 offset:64\n"
        "v_mfma_f32_16x16x32_bf16 v[0:3], a[168:171], a[12:15], v[0:3]\n"
        "ds_read_b32 v226, v159 offset:128\n"
        "v_mfma_f32_16x16x32_bf16 v[4:7], a[172:175], a[12:15], v[4:7]\n"
        "ds_read_b32 v227, v159 offset:192\n"
        "v_mfma_f32_16x16x32_bf16 v[4:7], a[164:167], a[8:11], v[4:7]\n"
        "v_mfma_f32_16x16x32_bf16 v[4:7], a[92:95], a[4:7], v[4:7]\n"
        "v_mfma_f32_16x16x32_bf16 v[4:7], a[84:87], a[0:3], v[4:7]\n"
        "s_waitcnt lgkmcnt(5)\n"
        "v_mfma_f32_16x16x32_bf16 v[12:15], a[84:87], a[16:19], v[12:15]\n"
        "v_mfma_f32_16x16x32_bf16 v[12:15], a[92:95], a[20:23], v[12:15]\n"
        "s_waitcnt lgkmcnt(3)\n"
        "v_sub_f32 v228, 1.0, v224\n"
        "v_cvt_pk_bf16_f32 v228, 0, v228\n"
        "v_mfma_f32_16x16x32_bf16 v[12:15], a[164:167], a[24:27], v[12:15]\n"
        "s_waitcnt lgkmcnt(2)\n"
        "v_sub_f32 v229, 1.0, v225\n"
        "v_cvt_pk_bf16_f32 v229, 0, v229\n"
        "v_mfma_f32_16x16x32_bf16 v[12:15], a[172:175], a[28:31], v[12:15]\n"
        "s_waitcnt lgkmcnt(1)\n"
        "v_sub_f32 v230, 1.0, v226\n"
        "v_cvt_pk_bf16_f32 v230, 0, v230\n"
        "v_mfma_f32_16x16x32_bf16 v[8:11], a[168:171], a[28:31], v[8:11]\n"
        "s_waitcnt lgkmcnt(0)\n"
        "v_sub_f32 v231, 1.0, v227\n"
        "v_cvt_pk_bf16_f32 v231, 0, v231\n"
        "v_mfma_f32_16x16x32_bf16 v[8:11], a[160:163], a[24:27], v[8:11]\n"
        "v_mfma_f32_16x16x32_bf16 v[8:11], a[88:91], a[20:23], v[8:11]\n"
        "v_mfma_f32_16x16x32_bf16 v[8:11], a[80:83], a[16:19], v[8:11]\n"
        "v_mfma_f32_16x16x32_bf16 v[16:19], a[80:83], a[192:195], v[16:19]\n"
        "s_barrier\n"
        "v_mfma_f32_16x16x32_bf16 v[16:19], a[88:91], a[196:199], v[16:19]\n"
        "ds_read_b128 a[0:3], v244 offset:49152\n"
        "global_load_lds_dwordx4 v254, s[60:61] offset:0 sc1\n"
        "s_mul_i32 s16, s13, 263168\n"
        "v_mfma_f32_16x16x32_bf16 v[16:19], a[160:163], a[200:203], v[16:19]\n"
        "ds_read_b128 a[4:7], v244 offset:53248\n"
        "s_lshl2_add_u32 s15, s10, s74\n"
        "s_lshl_b32 s15, s15, 14\n"
        "v_mfma_f32_16x16x32_bf16 v[16:19], a[168:171], a[204:207], v[16:19]\n"
        "ds_read_b128 a[8:11], v244 offset:57344\n"
        "s_add_u32 s16, s16, s15\n"
        "s_add_u32 s16, s16, s73\n"
        "v_mfma_f32_16x16x32_bf16 v[20:23], a[172:175], a[204:207], v[20:23]\n"
        "LHB2_%=:\n"
        "s_cbranch_execz LHRB_%=\n"
        "ds_read_b128 a[12:15], v244 offset:61440\n"
        "global_load_lds_dwordx4 v254, s[60:61] offset:1024 sc1\n"
        "s_add_u32 s16, s16, 0x4000000\n"
        "v_mfma_f32_16x16x32_bf16 v[20:23], a[164:167], a[200:203], v[20:23]\n"
        "ds_read_b128 a[16:19], v244 offset:50176\n"
        "s_add_u32 s30, s38, s16\n"
        "s_addc_u32 s31, s39, 0\n"
        "v_mfma_f32_16x16x32_bf16 v[20:23], a[92:95], a[196:199], v[20:23]\n"
        "ds_read_b128 a[20:23], v244 offset:54272\n"
        "s_mul_i32 s16, s13, 263168\n"
        "s_lshl2_add_u32 s15, s10, s75\n"
        "v_mfma_f32_16x16x32_bf16 v[20:23], a[84:87], a[192:195], v[20:23]\n"
        "ds_read_b128 a[24:27], v244 offset:58368\n"
        "global_load_lds_dwordx4 v254, s[60:61] offset:2048 sc1\n"
        "s_lshl_b32 s15, s15, 14\n"
        "v_mfma_f32_16x16x32_bf16 v[28:31], a[84:87], a[208:211], v[28:31]\n"
        "ds_read_b128 a[28:31], v244 offset:62464\n"
        "s_add_u32 s16, s16, s15\n"
        "s_add_u32 s16, s16, s73\n"
        "v_mfma_f32_16x16x32_bf16 v[28:31], a[92:95], a[212:215], v[28:31]\n"
        "ds_read_b128 a[192:195], v244 offset:51200\n"
        "s_add_u32 s16, s16, 0x4000000\n"
        "s_lshl2_add_u32 s15, s10, s76\n"
        "v_mfma_f32_16x16x32_bf16 v[28:31], a[164:167], a[216:219], v[28:31]\n"
        "ds_read_b128 a[196:199], v244 offset:55296\n"
        "global_load_lds_dwordx4 v254, s[60:61] offset:3072 sc1\n"
        "s_add_u32 m0, s73, 0x4800\n"
        "v_mfma_f32_16x16x32_bf16 v[28:31], a[172:175], a[220:223], v[28:31]\n"
        "ds_read_b128 a[200:203], v244 offset:59392\n"
        "s_add_u32 s60, s68, s66\n"
        "s_addc_u32 s61, s69, 0\n"
        "v_mfma_f32_16x16x32_bf16 v[24:27], a[168:171], a[220:223], v[24:27]\n"
        "ds_read_b128 a[204:207], v244 offset:63488\n"
        "s_xor_b32 s66, s17, 6\n"
        "s_lshl_b32 s66, s66, 15\n"
        "v_mfma_f32_16x16x32_bf16 v[24:27], a[160:163], a[216:219], v[24:27]\n"
        "ds_read_b128 a[216:219], v244 offset:60416\n"
        "global_load_lds_dwordx4 v148, s[58:59] offset:0 sc1\n"
        "s_add_u32 m0, s73, 0x20000\n"
        "v_mfma_f32_16x16x32_bf16 v[24:27], a[88:91], a[212:215], v[24:27]\n"
        "ds_read_b128 a[212:215], v244 offset:56320\n"
        "s_add_u32 s58, s58, 0x8000\n"
        "s_addc_u32 s59, s59, 0\n"
        "v_mfma_f32_16x16x32_bf16 v[24:27], a[80:83], a[208:211], v[24:27]\n"
        "ds_read_b128 a[208:211], v244 offset:52224\n"
        "s_lshl_b32 s15, s15, 14\n"
        "s_waitcnt lgkmcnt(8)\n"
        "v_mfma_f32_16x16x32_bf16 v[0:3], a[176:179], a[0:3], v[0:3]\n"
        "ds_read_b128 a[220:223], v244 offset:64512\n"
        "global_load_dwordx4 v[104:107], v151, s[64:65]\n"
        "v_mfma_f32_16x16x32_bf16 v[0:3], a[184:187], a[4:7], v[0:3]\n"
        "s_add_u32 s64, s64, 0x8000\n"
        "s_addc_u32 s65, s65, 0\n"
        "v_mfma_f32_16x16x32_bf16 v[0:3], a[32:35], a[8:11], v[0:3]\n"
        "v_mfma_f32_16x16x32_bf16 v[0:3], a[40:43], a[12:15], v[0:3]\n"
        "v_mfma_f32_16x16x32_bf16 v[4:7], a[44:47], a[12:15], v[4:7]\n"
        "v_mfma_f32_16x16x32_bf16 v[4:7], a[36:39], a[8:11], v[4:7]\n"
        "v_mfma_f32_16x16x32_bf16 v[4:7], a[188:191], a[4:7], v[4:7]\n"
        "v_mfma_f32_16x16x32_bf16 v[4:7], a[180:183], a[0:3], v[4:7]\n"
        "s_waitcnt lgkmcnt(1)\n"
        "v_mfma_f32_16x16x32_bf16 v[12:15], a[180:183], a[16:19], v[12:15]\n"
        "v_mfma_f32_16x16x32_bf16 v[12:15], a[188:191], a[20:23], v[12:15]\n"
        "v_mfma_f32_16x16x32_bf16 v[12:15], a[36:39], a[24:27], v[12:15]\n"
        "v_mfma_f32_16x16x32_bf16 v[12:15], a[44:47], a[28:31], v[12:15]\n"
        "v_mfma_f32_16x16x32_bf16 v[8:11], a[40:43], a[28:31], v[8:11]\n"
        "v_mfma_f32_16x16x32_bf16 v[8:11], a[32:35], a[24:27], v[8:11]\n"
        "v_mfma_f32_16x16x32_bf16 v[8:11], a[184:187], a[20:23], v[8:11]\n"
        "v_mfma_f32_16x16x32_bf16 v[8:11], a[176:179], a[16:19], v[8:11]\n"
        "v_mfma_f32_16x16x32_bf16 v[16:19], a[176:179], a[192:195], v[16:19]\n"
        "s_barrier\n"
        "v_mfma_f32_16x16x32_bf16 v[16:19], a[184:187], a[196:199], v[16:19]\n"
        "ds_read_b128 a[0:3], v244 offset:0\n"
        "global_load_lds_dwordx4 v254, s[60:61] offset:0 sc1\n"
        "v_mfma_f32_16x16x32_bf16 v[16:19], a[32:35], a[200:203], v[16:19]\n"
        "ds_read_b128 a[4:7], v244 offset:4096\n"
        "s_waitcnt vmcnt(13) lgkmcnt(2)\n"
        "v_mfma_f32_16x16x32_bf16 v[16:19], a[40:43], a[204:207], v[16:19]\n"
        "ds_read_b128 a[8:11], v244 offset:8192\n"
        "v_mfma_f32_16x16x32_bf16 v[20:23], a[44:47], a[204:207], v[20:23]\n"
        "ds_read_b128 a[12:15], v244 offset:12288\n"
        "global_load_lds_dwordx4 v254, s[60:61] offset:1024 sc1\n"
        "v_mfma_f32_16x16x32_bf16 v[20:23], a[36:39], a[200:203], v[20:23]\n"
        "ds_read_b128 a[28:31], v244 offset:13312\n"
        "v_mfma_f32_16x16x32_bf16 v[20:23], a[188:191], a[196:199], v[20:23]\n"
        "ds_read_b128 a[24:27], v244 offset:9216\n"
        "v_mfma_f32_16x16x32_bf16 v[20:23], a[180:183], a[192:195], v[20:23]\n"
        "ds_read_b128 a[20:23], v244 offset:5120\n"
        "global_load_lds_dwordx4 v254, s[60:61] offset:2048 sc1\n"
        "v_mfma_f32_16x16x32_bf16 v[28:31], a[180:183], a[208:211], v[28:31]\n"
        "ds_read_b128 a[16:19], v244 offset:1024\n"
        "v_mfma_f32_16x16x32_bf16 v[28:31], a[188:191], a[212:215], v[28:31]\n"
        "s_waitcnt lgkmcnt(7)\n"
        "ds_read_b128 a[192:195], v244 offset:2048\n"
        "v_mfma_f32_16x16x32_bf16 v[0:3], a[48:51], a[0:3], v[0:3]\n"
        "s_waitcnt lgkmcnt(7)\n"
        "ds_read_b128 a[196:199], v244 offset:6144\n"
        "v_mfma_f32_16x16x32_bf16 v[0:3], a[56:59], a[4:7], v[0:3]\n"
        "s_waitcnt lgkmcnt(7)\n"
        "ds_read_b128 a[200:203], v244 offset:10240\n"
        "v_mfma_f32_16x16x32_bf16 v[0:3], a[224:227], a[8:11], v[0:3]\n"
        "s_waitcnt lgkmcnt(7)\n"
        "ds_read_b128 a[204:207], v244 offset:14336\n"
        "v_mfma_f32_16x16x32_bf16 v[0:3], a[232:235], a[12:15], v[0:3]\n"
        "v_permlane16_swap_b32 v96, v98\n"
        "v_mfma_f32_16x16x32_bf16 v[4:7], a[236:239], a[12:15], v[4:7]\n"
        "v_permlane16_swap_b32 v97, v99\n"
        "v_mfma_f32_16x16x32_bf16 v[4:7], a[228:231], a[8:11], v[4:7]\n"
        "global_load_lds_dwordx4 v254, s[60:61] offset:3072 sc1\n"
        "s_add_u32 m0, s73, 0x4c00\n"
        "v_lshlrev_b32 v64, 16, v96\n"
        "v_mfma_f32_16x16x32_bf16 v[4:7], a[60:63], a[4:7], v[4:7]\n"
        "s_add_u32 s60, s68, s66\n"
        "s_addc_u32 s61, s69, 0\n"
        "v_cvt_pk_bf16_f32 v0, v0, v1\n"
        "v_mfma_f32_16x16x32_bf16 v[4:7], a[52:55], a[0:3], v[4:7]\n"
        "s_waitcnt lgkmcnt(4)\n"
        "v_cvt_pk_bf16_f32 v1, v2, v3\n"
        "s_xor_b32 s66, s17, 7\n"
        "v_mfma_f32_16x16x32_bf16 v[28:31], a[36:39], a[216:219], v[28:31]\n"
        "global_load_lds_dwordx4 v148, s[58:59] offset:0 sc1\n"
        "s_add_u32 m0, s73, 0x1c000\n"
        "s_lshl_b32 s66, s66, 15\n"
        "v_mfma_f32_16x16x32_bf16 v[28:31], a[44:47], a[220:223], v[28:31]\n"
        "v_cvt_pk_bf16_f32 v2, v4, v5\n"
        "v_cvt_pk_bf16_f32 v3, v6, v7\n"
        "v_mfma_f32_16x16x32_bf16 v[8:11], a[232:235], a[28:31], v[8:11]\n"
        "v_and_b32 v65, 0xffff0000, v96\n"
        "v_lshlrev_b32 v66, 16, v97\n"
        "v_mfma_f32_16x16x32_bf16 v[8:11], a[224:227], a[24:27], v[8:11]\n"
        "global_store_dwordx4 v254, v[0:3], s[28:29] offset:0 sc1\n"
        "v_and_b32 v67, 0xffff0000, v97\n"
        "v_lshlrev_b32 v68, 16, v98\n"
        "v_and_b32 v69, 0xffff0000, v98\n"
        "v_lshlrev_b32 v70, 16, v99\n"
        "v_mfma_f32_16x16x32_bf16 v[8:11], a[56:59], a[20:23], v[8:11]\n"
        "v_and_b32 v71, 0xffff0000, v99\n"
        "v_mfma_f32_16x16x32_bf16 v[8:11], a[48:51], a[16:19], v[8:11]\n"
        "global_load_dwordx4 v[108:111], v151, s[64:65]\n"
        "v_mfma_f32_16x16x32_bf16 v[12:15], a[52:55], a[16:19], v[12:15]\n"
        "v_mfma_f32_16x16x32_bf16 v[12:15], a[60:63], a[20:23], v[12:15]\n"
        "s_barrier\n"
        "v_mfma_f32_16x16x32_bf16 v[12:15], a[228:231], a[24:27], v[12:15]\n"
        "ds_read_b128 a[0:3], v245 offset:16384\n"
        "global_load_lds_dwordx4 v254, s[60:61] offset:0 sc1\n"
        "v_mfma_f32_16x16x32_bf16 v[12:15], a[236:239], a[28:31], v[12:15]\n"
        "ds_read_b128 a[4:7], v245 offset:20480\n"
        "v_cvt_pk_bf16_f32 v8, v8, v9\n"
        "v_mfma_f32_16x16x32_bf16 v[24:27], a[40:43], a[220:223], v[24:27]\n"
        "ds_read_b128 a[220:223], v244 offset:15360\n"
        "v_cvt_pk_bf16_f32 v9, v10, v11\n"
        "v_mfma_f32_16x16x32_bf16 v[24:27], a[32:35], a[216:219], v[24:27]\n"
        "ds_read_b128 a[216:219], v244 offset:11264\n"
        "global_load_lds_dwordx4 v254, s[60:61] offset:1024 sc1\n"
        "v_mfma_f32_16x16x32_bf16 v[24:27], a[184:187], a[212:215], v[24:27]\n"
        "ds_read_b128 a[212:215], v244 offset:7168\n"
        "v_cvt_pk_bf16_f32 v10, v12, v13\n"
        "v_mfma_f32_16x16x32_bf16 v[24:27], a[176:179], a[208:211], v[24:27]\n"
        "ds_read_b128 a[208:211], v244 offset:3072\n"
        "s_waitcnt lgkmcnt(6)\n"
        "v_mfma_f32_16x16x32_bf16 v[16:19], a[48:51], a[192:195], v[16:19]\n"
        "ds_read_b128 a[8:11], v245 offset:24576\n"
        "v_cvt_pk_bf16_f32 v11, v14, v15\n"
        "v_mfma_f32_16x16x32_bf16 v[16:19], a[56:59], a[196:199], v[16:19]\n"
        "global_store_dwordx4 v254, v[8:11], s[28:29] offset:1024 sc1\n"
        "ds_read_b128 a[12:15], v245 offset:28672\n"
        "ds_read_b128 a[16:19], v245 offset:17408\n"
        "v_mfma_f32_16x16x32_bf16 v[16:19], a[224:227], a[200:203], v[16:19]\n"
        "ds_read_b128 a[20:23], v245 offset:21504\n"
        "v_mfma_f32_16x16x32_bf16 v[16:19], a[232:235], a[204:207], v[16:19]\n"
        "ds_read_b128 a[24:27], v245 offset:25600\n"
        "global_load_lds_dwordx4 v254, s[60:61] offset:2048 sc1\n"
        "v_mfma_f32_16x16x32_bf16 v[20:23], a[236:239], a[204:207], v[20:23]\n"
        "ds_read_b128 a[28:31], v245 offset:29696\n"
        "s_waitcnt lgkmcnt(8)\n"
        "v_mfma_f32_16x16x32_bf16 v[20:23], a[228:231], a[200:203], v[20:23]\n"
        "ds_read_b128 a[200:203], v245 offset:26624\n"
        "v_mfma_f32_16x16x32_bf16 v[20:23], a[60:63], a[196:199], v[20:23]\n"
        "ds_read_b128 a[196:199], v245 offset:22528\n"
        "v_cvt_pk_bf16_f32 v16, v16, v17\n"
        "v_mfma_f32_16x16x32_bf16 v[20:23], a[52:55], a[192:195], v[20:23]\n"
        "ds_read_b128 a[192:195], v245 offset:18432\n"
        "v_cvt_pk_bf16_f32 v17, v18, v19\n"
        "v_mfma_f32_16x16x32_bf16 v[24:27], a[232:235], a[220:223], v[24:27]\n"
        "ds_read_b128 a[204:207], v245 offset:30720\n"
        "global_load_lds_dwordx4 v254, s[60:61] offset:3072 sc1\n"
        "s_add_u32 m0, s73, 0x10000\n"
        "v_mfma_f32_16x16x32_bf16 v[24:27], a[224:227], a[216:219], v[24:27]\n"
        "s_waitcnt lgkmcnt(7)\n"
        "v_cvt_pk_bf16_f32 v18, v20, v21\n"
        "v_cvt_pk_bf16_f32 v19, v22, v23\n"
        "v_mfma_f32_16x16x32_bf16 v[28:31], a[52:55], a[208:211], v[28:31]\n"
        "s_add_u32 s60, s68, s66\n"
        "s_addc_u32 s61, s69, 0\n"
        "v_mfma_f32_16x16x32_bf16 v[24:27], a[56:59], a[212:215], v[24:27]\n"
        "v_mfma_f32_16x16x32_bf16 v[28:31], a[60:63], a[212:215], v[28:31]\n"
        "global_store_dwordx4 v254, v[16:19], s[28:29] offset:2048 sc1\n"
        "ds_read_b128 a[212:215], v245 offset:23552\n"
        "s_waitcnt vmcnt(19) lgkmcnt(5)\n"
        "v_permlane16_swap_b32 v100, v102\n"
        "v_mfma_f32_16x16x32_bf16 v[24:27], a[48:51], a[208:211], v[24:27]\n"
        "ds_read_b128 a[208:211], v245 offset:19456\n"
        "v_lshlrev_b32 v72, 16, v100\n"
        "v_mfma_f32_16x16x32_bf16 v[28:31], a[228:231], a[216:219], v[28:31]\n"
        "ds_read_b128 a[216:219], v245 offset:27648\n"
        "v_and_b32 v73, 0xffff0000, v100\n"
        "v_mfma_f32_16x16x32_bf16 v[28:31], a[236:239], a[220:223], v[28:31]\n"
        "ds_read_b128 a[220:223], v245 offset:31744\n"
        "v_lshlrev_b32 v76, 16, v102\n"
        "v_mfma_f32_16x16x32_bf16 v[32:35], a[96:99], a[0:3], 0\n"
        "v_cvt_pk_bf16_f32 v24, v24, v25\n"
        "v_cvt_pk_bf16_f32 v25, v26, v27\n"
        "v_mfma_f32_16x16x32_bf16 v[32:35], a[104:107], a[4:7], v[32:35]\n"
        "v_permlane16_swap_b32 v101, v103\n"
        "v_mfma_f32_16x16x32_bf16 v[32:35], a[112:115], a[8:11], v[32:35]\n"
        "v_cvt_pk_bf16_f32 v26, v28, v29\n"
        "v_cvt_pk_bf16_f32 v27, v30, v31\n"
        "v_mfma_f32_16x16x32_bf16 v[32:35], a[120:123], a[12:15], v[32:35]\n"
        "global_store_dwordx4 v254, v[24:27], s[28:29] offset:3072 sc1\n"
        "v_lshlrev_b32 v74, 16, v101\n"
        "v_and_b32 v75, 0xffff0000, v101\n"
        "v_and_b32 v77, 0xffff0000, v102\n"
        "v_lshlrev_b32 v78, 16, v103\n"
        "v_mfma_f32_16x16x32_bf16 v[36:39], a[124:127], a[12:15], 0\n"
        "v_and_b32 v79, 0xffff0000, v103\n"
        "v_mfma_f32_16x16x32_bf16 v[36:39], a[116:119], a[8:11], v[36:39]\n"
        "v_mfma_f32_16x16x32_bf16 v[36:39], a[108:111], a[4:7], v[36:39]\n"
        "v_mfma_f32_16x16x32_bf16 v[36:39], a[100:103], a[0:3], v[36:39]\n"
        "v_mfma_f32_16x16x32_bf16 v[44:47], a[100:103], a[16:19], 0\n"
        "v_mfma_f32_16x16x32_bf16 v[44:47], a[108:111], a[20:23], v[44:47]\n"
        "v_mfma_f32_16x16x32_bf16 v[44:47], a[116:119], a[24:27], v[44:47]\n"
        "v_mfma_f32_16x16x32_bf16 v[44:47], a[124:127], a[28:31], v[44:47]\n"
        "v_mfma_f32_16x16x32_bf16 v[40:43], a[120:123], a[28:31], 0\n"
        "s_waitcnt lgkmcnt(0)\n"
        "v_mfma_f32_16x16x32_bf16 v[40:43], a[112:115], a[24:27], v[40:43]\n"
        "v_mfma_f32_16x16x32_bf16 v[40:43], a[104:107], a[20:23], v[40:43]\n"
        "v_mfma_f32_16x16x32_bf16 v[40:43], a[96:99], a[16:19], v[40:43]\n"
        "v_mfma_f32_16x16x32_bf16 v[48:51], a[96:99], a[192:195], 0\n"
        "s_barrier\n"
        "v_mfma_f32_16x16x32_bf16 v[48:51], a[104:107], a[196:199], v[48:51]\n"
        "ds_read_b128 a[0:3], v244 offset:16384\n"
        "global_load_lds_dwordx4 v254, s[60:61] offset:0 sc1\n"
        "v_mfma_f32_16x16x32_bf16 v[48:51], a[112:115], a[200:203], v[48:51]\n"
        "ds_read_b128 a[4:7], v244 offset:20480\n"
        "v_mfma_f32_16x16x32_bf16 v[48:51], a[120:123], a[204:207], v[48:51]\n"
        "ds_read_b128 a[8:11], v244 offset:24576\n"
        "v_mfma_f32_16x16x32_bf16 v[52:55], a[124:127], a[204:207], 0\n"
        "ds_read_b128 a[12:15], v244 offset:28672\n"
        "global_load_lds_dwordx4 v254, s[60:61] offset:1024 sc1\n"
        "v_mfma_f32_16x16x32_bf16 v[52:55], a[116:119], a[200:203], v[52:55]\n"
        "ds_read_b128 a[16:19], v244 offset:17408\n"
        "v_mfma_f32_16x16x32_bf16 v[52:55], a[108:111], a[196:199], v[52:55]\n"
        "ds_read_b128 a[20:23], v244 offset:21504\n"
        "v_mfma_f32_16x16x32_bf16 v[52:55], a[100:103], a[192:195], v[52:55]\n"
        "ds_read_b128 a[24:27], v244 offset:25600\n"
        "global_load_lds_dwordx4 v254, s[60:61] offset:2048 sc1\n"
        "v_mfma_f32_16x16x32_bf16 v[60:63], a[100:103], a[208:211], 0\n"
        "ds_read_b128 a[28:31], v244 offset:29696\n"
        "v_mfma_f32_16x16x32_bf16 v[60:63], a[108:111], a[212:215], v[60:63]\n"
        "ds_read_b128 a[192:195], v244 offset:18432\n"
        "v_mfma_f32_16x16x32_bf16 v[60:63], a[116:119], a[216:219], v[60:63]\n"
        "ds_read_b128 a[196:199], v244 offset:22528\n"
        "global_load_lds_dwordx4 v254, s[60:61] offset:3072 sc1\n"
        "v_mfma_f32_16x16x32_bf16 v[60:63], a[124:127], a[220:223], v[60:63]\n"
        "ds_read_b128 a[200:203], v244 offset:26624\n"
        "s_waitcnt vmcnt(18) lgkmcnt(8)\n"
        "v_mfma_f32_16x16x32_bf16 v[56:59], a[120:123], a[220:223], 0\n"
        "ds_read_b128 a[204:207], v244 offset:30720\n"
        "v_mfma_f32_16x16x32_bf16 v[56:59], a[112:115], a[216:219], v[56:59]\n"
        "ds_read_b128 a[216:219], v244 offset:27648\n"
        "v_mfma_f32_16x16x32_bf16 v[56:59], a[104:107], a[212:215], v[56:59]\n"
        "ds_read_b128 a[212:215], v244 offset:23552\n"
        "v_mfma_f32_16x16x32_bf16 v[56:59], a[96:99], a[208:211], v[56:59]\n"
        "s_waitcnt lgkmcnt(7)\n"
        "ds_read_b128 a[208:211], v244 offset:19456\n"
        "v_mfma_f32_16x16x32_bf16 v[32:35], a[128:131], a[0:3], v[32:35]\n"
        "ds_read_b128 a[220:223], v244 offset:31744\n"
        "v_mfma_f32_16x16x32_bf16 v[32:35], a[136:139], a[4:7], v[32:35]\n"
        "v_permlane16_swap_b32 v104, v106\n"
        "v_mfma_f32_16x16x32_bf16 v[32:35], a[144:147], a[8:11], v[32:35]\n"
        "v_permlane16_swap_b32 v105, v107\n"
        "v_mfma_f32_16x16x32_bf16 v[32:35], a[152:155], a[12:15], v[32:35]\n"
        "v_lshlrev_b32 v80, 16, v104\n"
        "v_and_b32 v81, 0xffff0000, v104\n"
        "v_mfma_f32_16x16x32_bf16 v[36:39], a[156:159], a[12:15], v[36:39]\n"
        "v_lshlrev_b32 v82, 16, v105\n"
        "v_and_b32 v83, 0xffff0000, v105\n"
        "v_mfma_f32_16x16x32_bf16 v[36:39], a[148:151], a[8:11], v[36:39]\n"
        "v_lshlrev_b32 v84, 16, v106\n"
        "v_and_b32 v85, 0xffff0000, v106\n"
        "v_mfma_f32_16x16x32_bf16 v[36:39], a[140:143], a[4:7], v[36:39]\n"
        "v_lshlrev_b32 v86, 16, v107\n"
        "v_and_b32 v87, 0xffff0000, v107\n"
        "v_mfma_f32_16x16x32_bf16 v[36:39], a[132:135], a[0:3], v[36:39]\n"
        "s_waitcnt lgkmcnt(2)\n"
        "v_mfma_f32_16x16x32_bf16 v[44:47], a[132:135], a[16:19], v[44:47]\n"
        "v_mfma_f32_16x16x32_bf16 v[44:47], a[140:143], a[20:23], v[44:47]\n"
        "v_mfma_f32_16x16x32_bf16 v[44:47], a[148:151], a[24:27], v[44:47]\n"
        "v_mfma_f32_16x16x32_bf16 v[44:47], a[156:159], a[28:31], v[44:47]\n"
        "v_mfma_f32_16x16x32_bf16 v[40:43], a[152:155], a[28:31], v[40:43]\n"
        "v_mfma_f32_16x16x32_bf16 v[40:43], a[144:147], a[24:27], v[40:43]\n"
        "v_mfma_f32_16x16x32_bf16 v[40:43], a[136:139], a[20:23], v[40:43]\n"
        "v_mfma_f32_16x16x32_bf16 v[40:43], a[128:131], a[16:19], v[40:43]\n"
        "v_mfma_f32_16x16x32_bf16 v[48:51], a[128:131], a[192:195], v[48:51]\n"
        "s_barrier\n"
        "v_mfma_f32_16x16x32_bf16 v[48:51], a[136:139], a[196:199], v[48:51]\n"
        "ds_read_b128 a[0:3], v254 offset:0\n"
        "v_mfma_f32_16x16x32_bf16 v[48:51], a[144:147], a[200:203], v[48:51]\n"
        "ds_read_b128 a[4:7], v254 offset:4096\n"
        "v_mfma_f32_16x16x32_bf16 v[48:51], a[152:155], a[204:207], v[48:51]\n"
        "ds_read_b128 a[8:11], v254 offset:8192\n"
        "v_mfma_f32_16x16x32_bf16 v[52:55], a[156:159], a[204:207], v[52:55]\n"
        "ds_read_b128 a[12:15], v254 offset:12288\n"
        "s_waitcnt lgkmcnt(4)\n"
        "v_mfma_f32_16x16x32_bf16 v[52:55], a[148:151], a[200:203], v[52:55]\n"
        "ds_read_b128 a[16:19], v254 offset:1024\n"
        "v_mfma_f32_16x16x32_bf16 v[52:55], a[140:143], a[196:199], v[52:55]\n"
        "ds_read_b128 a[20:23], v254 offset:5120\n"
        "v_mfma_f32_16x16x32_bf16 v[52:55], a[132:135], a[192:195], v[52:55]\n"
        "ds_read_b128 a[24:27], v254 offset:9216\n"
        "v_mfma_f32_16x16x32_bf16 v[60:63], a[132:135], a[208:211], v[60:63]\n"
        "ds_read_b128 a[28:31], v254 offset:13312\n"
        "v_mfma_f32_16x16x32_bf16 v[60:63], a[140:143], a[212:215], v[60:63]\n"
        "ds_read_b128 a[192:195], v254 offset:2048\n"
        "v_mfma_f32_16x16x32_bf16 v[60:63], a[148:151], a[216:219], v[60:63]\n"
        "ds_read_b128 a[196:199], v254 offset:6144\n"
        "s_waitcnt vmcnt(4)\n"
        "v_mfma_f32_16x16x32_bf16 v[60:63], a[156:159], a[220:223], v[60:63]\n"
        "s_mov_b64 exec, 0xffff\n"
        "global_store_dwordx2 v234, v[250:251], s[48:49] sc1\n"
        "s_mov_b64 exec, -1\n"
        "v_mfma_f32_16x16x32_bf16 v[56:59], a[152:155], a[220:223], v[56:59]\n"
        "ds_read_b128 a[200:203], v254 offset:10240\n"
        "v_mfma_f32_16x16x32_bf16 v[56:59], a[144:147], a[216:219], v[56:59]\n"
        "ds_read_b128 a[204:207], v254 offset:14336\n"
        "s_waitcnt lgkmcnt(7)\n"
        "v_mfma_f32_16x16x32_bf16 v[56:59], a[136:139], a[212:215], v[56:59]\n"
        "ds_read_b128 a[212:215], v254 offset:7168\n"
        "v_mfma_f32_16x16x32_bf16 v[56:59], a[128:131], a[208:211], v[56:59]\n"
        "ds_read_b128 a[208:211], v254 offset:3072\n"
        "v_mfma_f32_16x16x32_bf16 v[32:35], v[160:163], a[0:3], v[32:35]\n"
        "ds_read_b128 a[216:219], v254 offset:11264\n"
        "v_mfma_f32_16x16x32_bf16 v[32:35], v[168:171], a[4:7], v[32:35]\n"
        "ds_read_b128 a[220:223], v254 offset:15360\n"
        "v_mfma_f32_16x16x32_bf16 v[32:35], v[176:179], a[8:11], v[32:35]\n"
        "ds_read_b64_tr_b16 v[128:129], v149 offset:0\n"
        "v_mfma_f32_16x16x32_bf16 v[32:35], v[184:187], a[12:15], v[32:35]\n"
        "ds_read_b64_tr_b16 v[130:131], v149 offset:2048\n"
        "v_mfma_f32_16x16x32_bf16 v[36:39], v[188:191], a[12:15], v[36:39]\n"
        "ds_read_b64_tr_b16 v[132:133], v149 offset:32\n"
        "v_mfma_f32_16x16x32_bf16 v[36:39], v[180:183], a[8:11], v[36:39]\n"
        "s_waitcnt lgkmcnt(7)\n"
        "ds_read_b64_tr_b16 v[134:135], v149 offset:2080\n"
        "v_mfma_f32_16x16x32_bf16 v[36:39], v[172:175], a[4:7], v[36:39]\n"
        "ds_read_b64_tr_b16 v[136:137], v149 offset:64\n"
        "v_mfma_f32_16x16x32_bf16 v[36:39], v[164:167], a[0:3], v[36:39]\n"
        "ds_read_b64_tr_b16 v[138:139], v149 offset:2112\n"
        "v_mfma_f32_16x16x32_bf16 v[44:47], v[164:167], a[16:19], v[44:47]\n"
        "ds_read_b64_tr_b16 v[140:141], v149 offset:96\n"
        "v_mfma_f32_16x16x32_bf16 v[44:47], v[172:175], a[20:23], v[44:47]\n"
        "ds_read_b64_tr_b16 v[142:143], v149 offset:2144\n"
        "v_mfma_f32_16x16x32_bf16 v[40:43], v[184:187], a[28:31], v[40:43]\n"
        "s_waitcnt lgkmcnt(7)\n"
        "v_permlane16_swap_b32 v108, v110\n"
        "v_mfma_f32_16x16x32_bf16 v[40:43], v[176:179], a[24:27], v[40:43]\n"
        "s_waitcnt lgkmcnt(6)\n"
        "v_permlane16_swap_b32 v109, v111\n"
        "v_mfma_f32_16x16x32_bf16 v[48:51], v[160:163], a[192:195], v[48:51]\n"
        "v_lshlrev_b32 v246, 16, v128\n"
        "v_mul_f32 v64, v64, v246\n"
        "v_mfma_f32_16x16x32_bf16 v[40:43], v[168:171], a[20:23], v[40:43]\n"
        "s_waitcnt lgkmcnt(5)\n"
        "v_cvt_pk_bf16_f32 v64, 0, v64\n"
        "v_mul_f32 v64, v64, v228\n"
        "v_mfma_f32_16x16x32_bf16 v[48:51], v[168:171], a[196:199], v[48:51]\n"
        "v_cvt_pk_bf16_f32 v64, 0, v64\n"
        "v_and_b32 v247, 0xffff0000, v128\n"
        "v_mfma_f32_16x16x32_bf16 v[40:43], v[160:163], a[16:19], v[40:43]\n"
        "v_mul_f32 v65, v65, v247\n"
        "v_cvt_pk_bf16_f32 v65, 0, v65\n"
        "v_mfma_f32_16x16x32_bf16 v[48:51], v[176:179], a[200:203], v[48:51]\n"
        "s_barrier\n"
        "v_mfma_f32_16x16x32_bf16 v[52:55], v[188:191], a[204:207], v[52:55]\n"
        "ds_read_b128 a[0:3], v244 offset:32768\n"
        "v_mul_f32 v65, v65, v228\n"
        "v_mfma_f32_16x16x32_bf16 v[48:51], v[184:187], a[204:207], v[48:51]\n"
        "ds_read_b128 a[204:207], v244 offset:47104\n"
        "v_cvt_pk_bf16_f32 v65, 0, v65\n"
        "v_mfma_f32_16x16x32_bf16 v[52:55], v[180:183], a[200:203], v[52:55]\n"
        "ds_read_b128 a[200:203], v244 offset:43008\n"
        "v_lshlrev_b32 v248, 16, v129\n"
        "v_mfma_f32_16x16x32_bf16 v[52:55], v[172:175], a[196:199], v[52:55]\n"
        "ds_read_b128 a[196:199], v244 offset:38912\n"
        "v_mul_f32 v66, v66, v248\n"
        "v_mfma_f32_16x16x32_bf16 v[52:55], v[164:167], a[192:195], v[52:55]\n"
        "ds_read_b128 a[192:195], v244 offset:34816\n"
        "v_cvt_pk_bf16_f32 v66, 0, v66\n"
        "v_mfma_f32_16x16x32_bf16 v[44:47], v[180:183], a[24:27], v[44:47]\n"
        "ds_read_b128 a[4:7], v244 offset:36864\n"
        "v_mul_f32 v66, v66, v228\n"
        "v_mfma_f32_16x16x32_bf16 v[44:47], v[188:191], a[28:31], v[44:47]\n"
        "ds_read_b128 a[8:11], v244 offset:40960\n"
        "v_cvt_pk_bf16_f32 v66, 0, v66\n"
        "v_mfma_f32_16x16x32_bf16 v[60:63], v[164:167], a[208:211], v[60:63]\n"
        "ds_read_b128 a[12:15], v244 offset:45056\n"
        "v_and_b32 v249, 0xffff0000, v129\n"
        "v_mfma_f32_16x16x32_bf16 v[60:63], v[172:175], a[212:215], v[60:63]\n"
        "ds_read_b128 a[16:19], v244 offset:33792\n"
        "v_mul_f32 v67, v67, v249\n"
        "v_mfma_f32_16x16x32_bf16 v[60:63], v[180:183], a[216:219], v[60:63]\n"
        "ds_read_b128 a[20:23], v244 offset:37888\n"
        "s_waitcnt lgkmcnt(8)\n"
        "v_mfma_f32_16x16x32_bf16 v[52:55], v[220:223], a[204:207], v[52:55]\n"
        "ds_read_b128 a[24:27], v244 offset:41984\n"
        "s_waitcnt lgkmcnt(8)\n"
        "v_mfma_f32_16x16x32_bf16 v[52:55], v[212:215], a[200:203], v[52:55]\n"
        "ds_read_b128 a[28:31], v244 offset:46080\n"
        "s_waitcnt lgkmcnt(8)\n"
        "v_mfma_f32_16x16x32_bf16 v[52:55], v[204:207], a[196:199], v[52:55]\n"
        "s_waitcnt lgkmcnt(7)\n"
        "v_cvt_pk_bf16_f32 v67, 0, v67\n"
        "v_mul_f32 v67, v67, v228\n"
        "v_mfma_f32_16x16x32_bf16 v[52:55], v[196:199], a[192:195], v[52:55]\n"
        "global_load_dwordx2 v[236:237], v235, s[48:49] sc1\n"
        "v_cvt_pk_bf16_f32 v67, 0, v67\n"
        "v_mfma_f32_16x16x32_bf16 v[60:63], v[188:191], a[220:223], v[60:63]\n"
        "v_lshlrev_b32 v144, 16, v130\n"
        "v_mul_f32 v68, v68, v144\n"
        "v_mfma_f32_16x16x32_bf16 v[56:59], v[184:187], a[220:223], v[56:59]\n"
        "ds_read_b128 a[220:223], v244 offset:48128\n"
        "v_cvt_pk_bf16_f32 v68, 0, v68\n"
        "v_mfma_f32_16x16x32_bf16 v[56:59], v[176:179], a[216:219], v[56:59]\n"
        "ds_read_b128 a[216:219], v244 offset:44032\n"
        "v_mul_f32 v68, v68, v228\n"
        "v_mfma_f32_16x16x32_bf16 v[56:59], v[168:171], a[212:215], v[56:59]\n"
        "ds_read_b128 a[212:215], v244 offset:39936\n"
        "v_cvt_pk_bf16_f32 v68, 0, v68\n"
        "v_mfma_f32_16x16x32_bf16 v[56:59], v[160:163], a[208:211], v[56:59]\n"
        "ds_read_b128 a[208:211], v244 offset:35840\n"
        "v_and_b32 v145, 0xffff0000, v130\n"
        "v_mfma_f32_16x16x32_bf16 v[32:35], v[192:195], a[0:3], v[32:35]\n"
        "s_waitcnt lgkmcnt(4)\n"
        "v_mul_f32 v69, v69, v145\n"
        "v_cvt_pk_bf16_f32 v69, 0, v69\n"
        "v_mfma_f32_16x16x32_bf16 v[32:35], v[200:203], a[4:7], v[32:35]\n"
        "v_mul_f32 v69, v69, v228\n"
        "v_cvt_pk_bf16_f32 v69, 0, v69\n"
        "v_mfma_f32_16x16x32_bf16 v[32:35], v[208:211], a[8:11], v[32:35]\n"
        "v_lshlrev_b32 v146, 16, v131\n"
        "v_mul_f32 v70, v70, v146\n"
        "v_mfma_f32_16x16x32_bf16 v[32:35], v[216:219], a[12:15], v[32:35]\n"
        "v_cvt_pk_bf16_f32 v70, 0, v70\n"
        "v_mul_f32 v70, v70, v228\n"
        "v_mfma_f32_16x16x32_bf16 v[36:39], v[220:223], a[12:15], v[36:39]\n"
        "v_cvt_pk_bf16_f32 v70, 0, v70\n"
        "v_and_b32 v147, 0xffff0000, v131\n"
        "v_mfma_f32_16x16x32_bf16 v[36:39], v[212:215], a[8:11], v[36:39]\n"
        "v_mul_f32 v71, v71, v147\n"
        "v_cvt_pk_bf16_f32 v71, 0, v71\n"
        "v_mfma_f32_16x16x32_bf16 v[36:39], v[204:207], a[4:7], v[36:39]\n"
        "v_mul_f32 v71, v71, v228\n"
        "v_cvt_pk_bf16_f32 v71, 0, v71\n"
        "v_mfma_f32_16x16x32_bf16 v[36:39], v[196:199], a[0:3], v[36:39]\n"
        "v_lshlrev_b32 v246, 16, v132\n"
        "v_mul_f32 v72, v72, v246\n"
        "v_mfma_f32_16x16x32_bf16 v[44:47], v[196:199], a[16:19], v[44:47]\n"
        "v_cvt_pk_bf16_f32 v72, 0, v72\n"
        "v_mul_f32 v72, v72, v229\n"
        "v_mfma_f32_16x16x32_bf16 v[44:47], v[204:207], a[20:23], v[44:47]\n"
        "v_cvt_pk_bf16_f32 v72, 0, v72\n"
        "v_and_b32 v247, 0xffff0000, v132\n"
        "v_mfma_f32_16x16x32_bf16 v[44:47], v[212:215], a[24:27], v[44:47]\n"
        "v_mul_f32 v73, v73, v247\n"
        "v_cvt_pk_bf16_f32 v73, 0, v73\n"
        "v_mfma_f32_16x16x32_bf16 v[44:47], v[220:223], a[28:31], v[44:47]\n"
        "v_mul_f32 v73, v73, v229\n"
        "v_cvt_pk_bf16_f32 v73, 0, v73\n"
        "v_mfma_f32_16x16x32_bf16 v[40:43], v[216:219], a[28:31], v[40:43]\n"
        "v_lshlrev_b32 v248, 16, v133\n"
        "v_mul_f32 v74, v74, v248\n"
        "v_mfma_f32_16x16x32_bf16 v[40:43], v[208:211], a[24:27], v[40:43]\n"
        "v_cvt_pk_bf16_f32 v74, 0, v74\n"
        "v_mul_f32 v74, v74, v229\n"
        "v_mfma_f32_16x16x32_bf16 v[40:43], v[200:203], a[20:23], v[40:43]\n"
        "v_cvt_pk_bf16_f32 v74, 0, v74\n"
        "v_and_b32 v249, 0xffff0000, v133\n"
        "v_mfma_f32_16x16x32_bf16 v[40:43], v[192:195], a[16:19], v[40:43]\n"
        "v_mul_f32 v75, v75, v249\n"
        "v_cvt_pk_bf16_f32 v75, 0, v75\n"
        "v_mfma_f32_16x16x32_bf16 v[48:51], v[192:195], a[192:195], v[48:51]\n"
        "s_barrier\n"
        "v_mfma_f32_16x16x32_bf16 v[48:51], v[200:203], a[196:199], v[48:51]\n"
        "ds_read_b128 a[0:3], v254 offset:49152\n"
        "v_mul_f32 v75, v75, v229\n"
        "v_mfma_f32_16x16x32_bf16 v[48:51], v[208:211], a[200:203], v[48:51]\n"
        "ds_read_b128 a[4:7], v254 offset:53248\n"
        "v_cvt_pk_bf16_f32 v75, 0, v75\n"
        "v_mfma_f32_16x16x32_bf16 v[48:51], v[216:219], a[204:207], v[48:51]\n"
        "ds_read_b128 a[8:11], v254 offset:57344\n"
        "s_waitcnt lgkmcnt(3)\n"
        "v_mfma_f32_16x16x32_bf16 v[60:63], v[196:199], a[208:211], v[60:63]\n"
        "ds_read_b128 a[12:15], v254 offset:61440\n"
        "v_lshlrev_b32 v144, 16, v134\n"
        "v_mfma_f32_16x16x32_bf16 v[60:63], v[204:207], a[212:215], v[60:63]\n"
        "ds_read_b128 a[16:19], v254 offset:50176\n"
        "v_mul_f32 v76, v76, v144\n"
        "v_mfma_f32_16x16x32_bf16 v[60:63], v[212:215], a[216:219], v[60:63]\n"
        "ds_read_b128 a[20:23], v254 offset:54272\n"
        "v_cvt_pk_bf16_f32 v76, 0, v76\n"
        "v_mfma_f32_16x16x32_bf16 v[60:63], v[220:223], a[220:223], v[60:63]\n"
        "ds_read_b128 a[24:27], v254 offset:58368\n"
        "v_mul_f32 v76, v76, v229\n"
        "v_mfma_f32_16x16x32_bf16 v[56:59], v[216:219], a[220:223], v[56:59]\n"
        "ds_read_b128 a[28:31], v254 offset:62464\n"
        "v_cvt_pk_bf16_f32 v76, 0, v76\n"
        "v_mfma_f32_16x16x32_bf16 v[56:59], v[208:211], a[216:219], v[56:59]\n"
        "ds_read_b128 a[192:195], v254 offset:51200\n"
        "v_and_b32 v145, 0xffff0000, v134\n"
        "v_mfma_f32_16x16x32_bf16 v[56:59], v[200:203], a[212:215], v[56:59]\n"
        "ds_read_b128 a[196:199], v254 offset:55296\n"
        "v_mul_f32 v77, v77, v145\n"
        "v_mfma_f32_16x16x32_bf16 v[56:59], v[192:195], a[208:211], v[56:59]\n"
        "ds_read_b128 a[200:203], v254 offset:59392\n"
        "s_waitcnt lgkmcnt(8)\n"
        "v_mfma_f32_16x16x32_bf16 v[32:35], v[112:115], a[0:3], v[32:35]\n"
        "ds_read_b128 a[204:207], v254 offset:63488\n"
        "v_cvt_pk_bf16_f32 v77, 0, v77\n"
        "v_mfma_f32_16x16x32_bf16 v[32:35], v[120:123], a[4:7], v[32:35]\n"
        "ds_read_b128 a[208:211], v254 offset:52224\n"
        "v_mul_f32 v77, v77, v229\n"
        "v_mfma_f32_16x16x32_bf16 v[32:35], a[64:67], a[8:11], v[32:35]\n"
        "ds_read_b128 a[212:215], v254 offset:56320\n"
        "s_waitcnt lgkmcnt(8)\n"
        "v_mfma_f32_16x16x32_bf16 v[32:35], a[72:75], a[12:15], v[32:35]\n"
        "ds_read_b128 a[216:219], v254 offset:60416\n"
        "v_cvt_pk_bf16_f32 v77, 0, v77\n"
        "v_mfma_f32_16x16x32_bf16 v[36:39], a[76:79], a[12:15], v[36:39]\n"
        "ds_read_b128 a[220:223], v254 offset:64512\n"
        "v_lshlrev_b32 v146, 16, v135\n"
        "v_mfma_f32_16x16x32_bf16 v[36:39], a[68:71], a[8:11], v[36:39]\n"
        "s_waitcnt vmcnt(0) lgkmcnt(7)\n"
        "v_cmp_ne_u64 vcc, v[236:237], v[250:251]\n"
        "v_mul_f32 v78, v78, v146\n"
        "v_mfma_f32_16x16x32_bf16 v[36:39], v[124:127], a[4:7], v[36:39]\n"
        "s_cbranch_vccnz LPPR_%=\n"
        "LBPR_%=:\n"
        "global_load_dwordx4 a[240:243], v254, s[30:31] offset:0 sc1\n"
        "v_mfma_f32_16x16x32_bf16 v[36:39], v[116:119], a[0:3], v[36:39]\n"
        "v_cvt_pk_bf16_f32 v78, 0, v78\n"
        "v_mul_f32 v78, v78, v229\n"
        "v_mfma_f32_16x16x32_bf16 v[44:47], v[116:119], a[16:19], v[44:47]\n"
        "global_load_dwordx4 a[244:247], v254, s[30:31] offset:1024 sc1\n"
        "v_mfma_f32_16x16x32_bf16 v[44:47], v[124:127], a[20:23], v[44:47]\n"
        "v_cvt_pk_bf16_f32 v78, 0, v78\n"
        "v_and_b32 v147, 0xffff0000, v135\n"
        "v_mfma_f32_16x16x32_bf16 v[44:47], a[68:71], a[24:27], v[44:47]\n"
        "global_load_dwordx4 a[248:251], v254, s[30:31] offset:2048 sc1\n"
        "v_mfma_f32_16x16x32_bf16 v[44:47], a[76:79], a[28:31], v[44:47]\n"
        "v_mul_f32 v79, v79, v147\n"
        "v_cvt_pk_bf16_f32 v79, 0, v79\n"
        "v_mfma_f32_16x16x32_bf16 v[40:43], a[72:75], a[28:31], v[40:43]\n"
        "global_load_dwordx4 a[252:255], v254, s[30:31] offset:3072 sc1\n"
        "v_mfma_f32_16x16x32_bf16 v[40:43], a[64:67], a[24:27], v[40:43]\n"
        "s_add_u32 s30, s38, s16\n"
        "s_addc_u32 s31, s39, 0\n"
        "s_mul_i32 s16, s13, 263168\n"
        "v_mfma_f32_16x16x32_bf16 v[40:43], v[120:123], a[20:23], v[40:43]\n"
        "global_load_dwordx4 v[0:3], v254, s[30:31] offset:0 sc1\n"
        "s_add_u32 s16, s16, s15\n"
        "v_mfma_f32_16x16x32_bf16 v[40:43], v[112:115], a[16:19], v[40:43]\n"
        "s_add_u32 s16, s16, s73\n"
        "s_add_u32 s16, s16, 0x4000000\n"
        "v_mfma_f32_16x16x32_bf16 v[48:51], v[112:115], a[192:195], v[48:51]\n"
        "s_waitcnt lgkmcnt(0)\n"
        "global_load_dwordx4 v[4:7], v254, s[30:31] offset:1024 sc1\n"
        "v_mfma_f32_16x16x32_bf16 v[48:51], v[120:123], a[196:199], v[48:51]\n"
        "LHB3_%=:\n"
        "s_cbranch_execz LHRB_%=\n"
        "s_barrier\n"
        "v_mfma_f32_16x16x32_bf16 v[48:51], a[64:67], a[200:203], v[48:51]\n"
        "ds_read_b128 a[0:3], v245 offset:0\n"
        "global_load_dwordx4 v[8:11], v254, s[30:31] offset:2048 sc1\n"
        "v_mfma_f32_16x16x32_bf16 v[48:51], a[72:75], a[204:207], v[48:51]\n"
        "ds_read_b128 a[4:7], v245 offset:4096\n"
        "v_mul_f32 v79, v79, v229\n"
        "v_mfma_f32_16x16x32_bf16 v[52:55], a[76:79], a[204:207], v[52:55]\n"
        "ds_read_b128 a[8:11], v245 offset:8192\n"
        "global_load_dwordx4 v[12:15], v254, s[30:31] offset:3072 sc1\n"
        "v_mfma_f32_16x16x32_bf16 v[52:55], a[68:71], a[200:203], v[52:55]\n"
        "ds_read_b128 a[12:15], v245 offset:12288\n"
        "s_add_u32 s30, s38, s16\n"
        "s_addc_u32 s31, s39, 0\n"
        "v_mfma_f32_16x16x32_bf16 v[52:55], v[124:127], a[196:199], v[52:55]\n"
        "ds_read_b128 a[16:19], v245 offset:1024\n"
        "global_load_dwordx4 v[16:19], v254, s[30:31] offset:0 sc1\n"
        "v_mfma_f32_16x16x32_bf16 v[52:55], v[116:119], a[192:195], v[52:55]\n"
        "ds_read_b128 a[20:23], v245 offset:5120\n"
        "v_cvt_pk_bf16_f32 v79, 0, v79\n"
        "v_mfma_f32_16x16x32_bf16 v[60:63], v[116:119], a[208:211], v[60:63]\n"
        "ds_read_b128 a[24:27], v245 offset:9216\n"
        "global_load_dwordx4 v[20:23], v254, s[30:31] offset:1024 sc1\n"
        "v_mfma_f32_16x16x32_bf16 v[60:63], v[124:127], a[212:215], v[60:63]\n"
        "ds_read_b128 a[28:31], v245 offset:13312\n"
        "v_lshlrev_b32 v246, 16, v136\n"
        "v_mfma_f32_16x16x32_bf16 v[60:63], a[68:71], a[216:219], v[60:63]\n"
        "ds_read_b128 a[192:195], v245 offset:2048\n"
        "global_load_dwordx4 v[24:27], v254, s[30:31] offset:2048 sc1\n"
        "v_mfma_f32_16x16x32_bf16 v[60:63], a[76:79], a[220:223], v[60:63]\n"
        "ds_read_b128 a[196:199], v245 offset:6144\n"
        "v_mul_f32 v80, v80, v246\n"
        "v_mfma_f32_16x16x32_bf16 v[56:59], a[72:75], a[220:223], v[56:59]\n"
        "ds_read_b128 a[200:203], v245 offset:10240\n"
        "global_load_dwordx4 v[28:31], v254, s[30:31] offset:3072 sc1\n"
        "v_mfma_f32_16x16x32_bf16 v[56:59], a[64:67], a[216:219], v[56:59]\n"
        "ds_read_b128 a[204:207], v245 offset:14336\n"
        "v_cvt_pk_bf16_f32 v80, 0, v80\n"
        "v_mfma_f32_16x16x32_bf16 v[56:59], v[120:123], a[212:215], v[56:59]\n"
        "ds_read_b128 a[212:215], v245 offset:7168\n"
        "v_mul_f32 v80, v80, v230\n"
        "v_mfma_f32_16x16x32_bf16 v[56:59], v[112:115], a[208:211], v[56:59]\n"
        "ds_read_b128 a[208:211], v245 offset:3072\n"
        "s_waitcnt lgkmcnt(8)\n"
        "v_mfma_f32_16x16x32_bf16 v[32:35], a[80:83], a[0:3], v[32:35]\n"
        "ds_read_b128 a[216:219], v245 offset:11264\n"
        "v_cvt_pk_bf16_f32 v80, 0, v80\n"
        "v_mfma_f32_16x16x32_bf16 v[32:35], a[88:91], a[4:7], v[32:35]\n"
        "ds_read_b128 a[220:223], v245 offset:15360\n"
        "v_and_b32 v247, 0xffff0000, v136\n"
        "v_mfma_f32_16x16x32_bf16 v[32:35], a[160:163], a[8:11], v[32:35]\n"
        "v_mul_f32 v81, v81, v247\n"
        "v_cvt_pk_bf16_f32 v81, 0, v81\n"
        "v_mfma_f32_16x16x32_bf16 v[32:35], a[168:171], a[12:15], v[32:35]\n"
        "v_mul_f32 v81, v81, v230\n"
        "v_cvt_pk_bf16_f32 v81, 0, v81\n"
        "v_mfma_f32_16x16x32_bf16 v[36:39], a[172:175], a[12:15], v[36:39]\n"
        "v_lshlrev_b32 v248, 16, v137\n"
        "v_mul_f32 v82, v82, v248\n"
        "v_mfma_f32_16x16x32_bf16 v[36:39], a[164:167], a[8:11], v[36:39]\n"
        "v_cvt_pk_bf16_f32 v82, 0, v82\n"
        "v_mul_f32 v82, v82, v230\n"
        "v_mfma_f32_16x16x32_bf16 v[36:39], a[92:95], a[4:7], v[36:39]\n"
        "s_waitcnt lgkmcnt(3)\n"
        "v_cvt_pk_bf16_f32 v82, 0, v82\n"
        "v_and_b32 v249, 0xffff0000, v137\n"
        "v_mfma_f32_16x16x32_bf16 v[36:39], a[84:87], a[0:3], v[36:39]\n"
        "v_mul_f32 v83, v83, v249\n"
        "v_cvt_pk_bf16_f32 v83, 0, v83\n"
        "v_mfma_f32_16x16x32_bf16 v[44:47], a[84:87], a[16:19], v[44:47]\n"
        "v_mul_f32 v83, v83, v230\n"
        "v_cvt_pk_bf16_f32 v83, 0, v83\n"
        "v_mfma_f32_16x16x32_bf16 v[44:47], a[92:95], a[20:23], v[44:47]\n"
        "v_lshlrev_b32 v144, 16, v138\n"
        "v_mul_f32 v84, v84, v144\n"
        "v_mfma_f32_16x16x32_bf16 v[44:47], a[164:167], a[24:27], v[44:47]\n"
        "v_cvt_pk_bf16_f32 v84, 0, v84\n"
        "v_mul_f32 v84, v84, v230\n"
        "v_mfma_f32_16x16x32_bf16 v[44:47], a[172:175], a[28:31], v[44:47]\n"
        "v_cvt_pk_bf16_f32 v84, 0, v84\n"
        "v_and_b32 v145, 0xffff0000, v138\n"
        "v_mfma_f32_16x16x32_bf16 v[40:43], a[168:171], a[28:31], v[40:43]\n"
        "v_mul_f32 v85, v85, v145\n"
        "v_cvt_pk_bf16_f32 v85, 0, v85\n"
        "v_mfma_f32_16x16x32_bf16 v[40:43], a[160:163], a[24:27], v[40:43]\n"
        "v_mul_f32 v85, v85, v230\n"
        "v_cvt_pk_bf16_f32 v85, 0, v85\n"
        "v_mfma_f32_16x16x32_bf16 v[40:43], a[88:91], a[20:23], v[40:43]\n"
        "v_lshlrev_b32 v146, 16, v139\n"
        "v_mul_f32 v86, v86, v146\n"
        "v_mfma_f32_16x16x32_bf16 v[40:43], a[80:83], a[16:19], v[40:43]\n"
        "v_cvt_pk_bf16_f32 v86, 0, v86\n"
        "v_mul_f32 v86, v86, v230\n"
        "v_mfma_f32_16x16x32_bf16 v[48:51], a[80:83], a[192:195], v[48:51]\n"
        "s_barrier\n"
        "v_mfma_f32_16x16x32_bf16 v[48:51], a[88:91], a[196:199], v[48:51]\n"
        "ds_read_b128 a[0:3], v244 offset:49152\n"
        "v_cvt_pk_bf16_f32 v86, 0, v86\n"
        "v_mfma_f32_16x16x32_bf16 v[48:51], a[160:163], a[200:203], v[48:51]\n"
        "ds_read_b128 a[4:7], v244 offset:53248\n"
        "v_and_b32 v147, 0xffff0000, v139\n"
        "v_mfma_f32_16x16x32_bf16 v[48:51], a[168:171], a[204:207], v[48:51]\n"
        "ds_read_b128 a[8:11], v244 offset:57344\n"
        "v_mul_f32 v87, v87, v147\n"
        "v_mfma_f32_16x16x32_bf16 v[52:55], a[172:175], a[204:207], v[52:55]\n"
        "ds_read_b128 a[12:15], v244 offset:61440\n"
        "v_cvt_pk_bf16_f32 v87, 0, v87\n"
        "v_mfma_f32_16x16x32_bf16 v[52:55], a[164:167], a[200:203], v[52:55]\n"
        "ds_read_b128 a[200:203], v244 offset:58368\n"
        "v_mul_f32 v87, v87, v230\n"
        "v_mfma_f32_16x16x32_bf16 v[52:55], a[92:95], a[196:199], v[52:55]\n"
        "ds_read_b128 a[196:199], v244 offset:54272\n"
        "v_cvt_pk_bf16_f32 v87, 0, v87\n"
        "v_mfma_f32_16x16x32_bf16 v[52:55], a[84:87], a[192:195], v[52:55]\n"
        "ds_read_b128 a[192:195], v244 offset:50176\n"
        "s_waitcnt lgkmcnt(7)\n"
        "v_mfma_f32_16x16x32_bf16 v[60:63], a[84:87], a[208:211], v[60:63]\n"
        "ds_read_b128 a[204:207], v244 offset:62464\n"
        "v_mfma_f32_16x16x32_bf16 v[60:63], a[92:95], a[212:215], v[60:63]\n"
        "s_barrier\n"
        "v_mfma_f32_16x16x32_bf16 v[60:63], a[164:167], a[216:219], v[60:63]\n"
        "ds_read_b128 a[16:19], v244 offset:0\n"
        "v_lshlrev_b32 v88, 16, v108\n"
        "v_mfma_f32_16x16x32_bf16 v[60:63], a[172:175], a[220:223], v[60:63]\n"
        "ds_read_b128 a[20:23], v244 offset:4096\n"
        "v_and_b32 v89, 0xffff0000, v108\n"
        "v_mfma_f32_16x16x32_bf16 v[56:59], a[168:171], a[220:223], v[56:59]\n"
        "ds_read_b128 a[24:27], v244 offset:8192\n"
        "v_lshlrev_b32 v90, 16, v109\n"
        "v_mfma_f32_16x16x32_bf16 v[56:59], a[160:163], a[216:219], v[56:59]\n"
        "ds_read_b128 a[28:31], v244 offset:12288\n"
        "v_and_b32 v91, 0xffff0000, v109\n"
        "v_mfma_f32_16x16x32_bf16 v[56:59], a[88:91], a[212:215], v[56:59]\n"
        "ds_read_b128 a[212:215], v244 offset:5120\n"
        "v_lshlrev_b32 v246, 16, v140\n"
        "v_mfma_f32_16x16x32_bf16 v[56:59], a[80:83], a[208:211], v[56:59]\n"
        "ds_read_b128 a[208:211], v244 offset:1024\n"
        "s_waitcnt lgkmcnt(7)\n"
        "v_mfma_f32_16x16x32_bf16 v[32:35], a[176:179], a[0:3], v[32:35]\n"
        "ds_read_b128 a[216:219], v244 offset:9216\n"
        "v_and_b32 v247, 0xffff0000, v140\n"
        "v_mfma_f32_16x16x32_bf16 v[32:35], a[184:187], a[4:7], v[32:35]\n"
        "ds_read_b128 a[220:223], v244 offset:13312\n"
        "v_lshlrev_b32 v248, 16, v141\n"
        "v_mfma_f32_16x16x32_bf16 v[32:35], a[32:35], a[8:11], v[32:35]\n"
        "v_and_b32 v249, 0xffff0000, v141\n"
        "v_mul_f32 v88, v88, v246\n"
        "v_mfma_f32_16x16x32_bf16 v[32:35], a[40:43], a[12:15], v[32:35]\n"
        "v_mul_f32 v89, v89, v247\n"
        "v_mul_f32 v90, v90, v248\n"
        "v_mfma_f32_16x16x32_bf16 v[36:39], a[44:47], a[12:15], v[36:39]\n"
        "ds_read_b128 a[12:15], v244 offset:63488\n"
        "v_mul_f32 v91, v91, v249\n"
        "v_mfma_f32_16x16x32_bf16 v[36:39], a[36:39], a[8:11], v[36:39]\n"
        "ds_read_b128 a[8:11], v244 offset:59392\n"
        "v_cvt_pk_bf16_f32 v88, 0, v88\n"
        "v_mfma_f32_16x16x32_bf16 v[36:39], a[188:191], a[4:7], v[36:39]\n"
        "ds_read_b128 a[4:7], v244 offset:55296\n"
        "v_cvt_pk_bf16_f32 v89, 0, v89\n"
        "v_mfma_f32_16x16x32_bf16 v[36:39], a[180:183], a[0:3], v[36:39]\n"
        "ds_read_b128 a[0:3], v244 offset:51200\n"
        "s_waitcnt lgkmcnt(6)\n"
        "v_mfma_f32_16x16x32_bf16 v[32:35], a[48:51], a[16:19], v[32:35]\n"
        "v_cvt_pk_bf16_f32 v90, 0, v90\n"
        "v_cvt_pk_bf16_f32 v91, 0, v91\n"
        "v_mfma_f32_16x16x32_bf16 v[32:35], a[56:59], a[20:23], v[32:35]\n"
        "v_mul_f32 v88, v88, v231\n"
        "v_mul_f32 v89, v89, v231\n"
        "v_mfma_f32_16x16x32_bf16 v[32:35], a[224:227], a[24:27], v[32:35]\n"
        "v_mul_f32 v90, v90, v231\n"
        "v_mul_f32 v91, v91, v231\n"
        "v_mfma_f32_16x16x32_bf16 v[32:35], a[232:235], a[28:31], v[32:35]\n"
        "v_cvt_pk_bf16_f32 v88, 0, v88\n"
        "v_cvt_pk_bf16_f32 v89, 0, v89\n"
        "v_mfma_f32_16x16x32_bf16 v[36:39], a[236:239], a[28:31], v[36:39]\n"
        "ds_read_b128 a[28:31], v244 offset:14336\n"
        "v_cvt_pk_bf16_f32 v90, 0, v90\n"
        "v_mfma_f32_16x16x32_bf16 v[36:39], a[228:231], a[24:27], v[36:39]\n"
        "ds_read_b128 a[24:27], v244 offset:10240\n"
        "v_cvt_pk_bf16_f32 v91, 0, v91\n"
        "v_mfma_f32_16x16x32_bf16 v[36:39], a[60:63], a[20:23], v[36:39]\n"
        "ds_read_b128 a[20:23], v244 offset:6144\n"
        "v_lshlrev_b32 v92, 16, v110\n"
        "v_mfma_f32_16x16x32_bf16 v[36:39], a[52:55], a[16:19], v[36:39]\n"
        "ds_read_b128 a[16:19], v244 offset:2048\n"
        "s_waitcnt vmcnt(11) lgkmcnt(4)\n"
        "v_mfma_f32_16x16x32_bf16 v[32:35], v[240:243], a[240:243], v[32:35]\n"
        "s_waitcnt vmcnt(7)\n"
        "v_and_b32 v93, 0xffff0000, v110\n"
        "v_lshlrev_b32 v94, 16, v111\n"
        "v_mfma_f32_16x16x32_bf16 v[32:35], v[240:243], v[0:3], v[32:35]\n"
        "v_and_b32 v95, 0xffff0000, v111\n"
        "v_lshlrev_b32 v144, 16, v142\n"
        "v_mfma_f32_16x16x32_bf16 v[36:39], v[238:241], a[240:243], v[36:39]\n"
        "v_and_b32 v145, 0xffff0000, v142\n"
        "v_lshlrev_b32 v146, 16, v143\n"
        "v_mfma_f32_16x16x32_bf16 v[36:39], v[238:241], v[0:3], v[36:39]\n"
        "v_and_b32 v147, 0xffff0000, v143\n"
        "v_mul_f32 v92, v92, v144\n"
        "v_mfma_f32_16x16x32_bf16 v[40:43], a[176:179], a[192:195], v[40:43]\n"
        "v_mul_f32 v93, v93, v145\n"
        "s_waitcnt vmcnt(3) lgkmcnt(3)\n"
        "v_mfma_f32_16x16x32_bf16 v[32:35], v[240:243], v[16:19], v[32:35]\n"
        "v_mul_f32 v94, v94, v146\n"
        "v_mul_f32 v95, v95, v147\n"
        "v_mfma_f32_16x16x32_bf16 v[36:39], v[238:241], v[16:19], v[36:39]\n"
        "v_cvt_pk_bf16_f32 v92, 0, v92\n"
        "v_cvt_pk_bf16_f32 v93, 0, v93\n"
        "v_mfma_f32_16x16x32_bf16 v[40:43], a[184:187], a[196:199], v[40:43]\n"
        "v_cvt_pk_bf16_f32 v94, 0, v94\n"
        "v_cvt_pk_bf16_f32 v95, 0, v95\n"
        "v_mfma_f32_16x16x32_bf16 v[40:43], a[32:35], a[200:203], v[40:43]\n"
        "v_fmac_f32 v64, v32, v224\n"
        "v_fmac_f32 v65, v33, v224\n"
        "v_mfma_f32_16x16x32_bf16 v[40:43], a[40:43], a[204:207], v[40:43]\n"
        "v_fmac_f32 v66, v34, v224\n"
        "v_fmac_f32 v67, v35, v224\n"
        "v_mfma_f32_16x16x32_bf16 v[44:47], a[44:47], a[204:207], v[44:47]\n"
        "ds_read_b128 a[204:207], v244 offset:64512\n"
        "v_fmac_f32 v68, v36, v224\n"
        "v_mfma_f32_16x16x32_bf16 v[44:47], a[36:39], a[200:203], v[44:47]\n"
        "ds_read_b128 a[200:203], v244 offset:60416\n"
        "v_fmac_f32 v69, v37, v224\n"
        "v_mfma_f32_16x16x32_bf16 v[44:47], a[188:191], a[196:199], v[44:47]\n"
        "ds_read_b128 a[196:199], v244 offset:56320\n"
        "v_fmac_f32 v70, v38, v224\n"
        "v_mfma_f32_16x16x32_bf16 v[44:47], a[180:183], a[192:195], v[44:47]\n"
        "ds_read_b128 a[192:195], v244 offset:52224\n"
        "v_fmac_f32 v71, v39, v224\n"
        "v_mfma_f32_16x16x32_bf16 v[40:43], a[48:51], a[208:211], v[40:43]\n"
        "v_cvt_pk_bf16_f32 v64, v64, v65\n"
        "v_cvt_pk_bf16_f32 v65, v66, v67\n"
        "v_mfma_f32_16x16x32_bf16 v[40:43], a[56:59], a[212:215], v[40:43]\n"
        "v_cvt_pk_bf16_f32 v66, v68, v69\n"
        "v_cvt_pk_bf16_f32 v67, v70, v71\n"
        "v_mfma_f32_16x16x32_bf16 v[40:43], a[224:227], a[216:219], v[40:43]\n"
        "v_permlane16_swap_b32 v64, v66\n"
        "v_mfma_f32_16x16x32_bf16 v[40:43], a[232:235], a[220:223], v[40:43]\n"
        "v_permlane16_swap_b32 v65, v67\n"
        "v_mfma_f32_16x16x32_bf16 v[44:47], a[236:239], a[220:223], v[44:47]\n"
        "global_store_dwordx4 v151, v[64:67], s[56:57] sc0 sc1\n"
        "ds_read_b128 a[220:223], v244 offset:15360\n"
        "s_add_u32 s56, s56, 0x8000\n"
        "s_addc_u32 s57, s57, 0\n"
        "v_mul_f32 v92, v92, v231\n"
        "v_mul_f32 v93, v93, v231\n"
        "v_mfma_f32_16x16x32_bf16 v[44:47], a[228:231], a[216:219], v[44:47]\n"
        "ds_read_b128 a[216:219], v244 offset:11264\n"
        "v_mul_f32 v94, v94, v231\n"
        "v_mfma_f32_16x16x32_bf16 v[44:47], a[60:63], a[212:215], v[44:47]\n"
        "ds_read_b128 a[212:215], v244 offset:7168\n"
        "v_mul_f32 v95, v95, v231\n"
        "v_mfma_f32_16x16x32_bf16 v[44:47], a[52:55], a[208:211], v[44:47]\n"
        "ds_read_b128 a[208:211], v244 offset:3072\n"
        "v_cvt_pk_bf16_f32 v92, 0, v92\n"
        "v_mfma_f32_16x16x32_bf16 v[40:43], v[240:243], a[244:247], v[40:43]\n"
        "v_cvt_pk_bf16_f32 v93, 0, v93\n"
        "v_cvt_pk_bf16_f32 v94, 0, v94\n"
        "v_mfma_f32_16x16x32_bf16 v[40:43], v[240:243], v[4:7], v[40:43]\n"
        "s_waitcnt vmcnt(3) lgkmcnt(4)\n"
        "v_cvt_pk_bf16_f32 v95, 0, v95\n"
        "v_mov_b32 v144, 0\n"
        "v_mfma_f32_16x16x32_bf16 v[40:43], v[240:243], v[20:23], v[40:43]\n"
        "v_mfma_f32_16x16x32_bf16 v[44:47], v[238:241], a[244:247], v[44:47]\n"
        "v_mfma_f32_16x16x32_bf16 v[44:47], v[238:241], v[4:7], v[44:47]\n"
        "v_mfma_f32_16x16x32_bf16 v[44:47], v[238:241], v[20:23], v[44:47]\n"
        "v_mfma_f32_16x16x32_bf16 v[48:51], a[176:179], a[0:3], v[48:51]\n"
        "v_mfma_f32_16x16x32_bf16 v[48:51], a[184:187], a[4:7], v[48:51]\n"
        "v_mfma_f32_16x16x32_bf16 v[48:51], a[32:35], a[8:11], v[48:51]\n"
        "v_mfma_f32_16x16x32_bf16 v[48:51], a[40:43], a[12:15], v[48:51]\n"
        "v_mfma_f32_16x16x32_bf16 v[52:55], a[44:47], a[12:15], v[52:55]\n"
        "v_fmac_f32 v72, v40, v225\n"
        "v_fmac_f32 v73, v41, v225\n"
        "v_mfma_f32_16x16x32_bf16 v[52:55], a[36:39], a[8:11], v[52:55]\n"
        "v_fmac_f32 v74, v42, v225\n"
        "v_fmac_f32 v75, v43, v225\n"
        "v_mfma_f32_16x16x32_bf16 v[52:55], a[188:191], a[4:7], v[52:55]\n"
        "v_fmac_f32 v76, v44, v225\n"
        "v_fmac_f32 v77, v45, v225\n"
        "v_mfma_f32_16x16x32_bf16 v[52:55], a[180:183], a[0:3], v[52:55]\n"
        "v_fmac_f32 v78, v46, v225\n"
        "v_fmac_f32 v79, v47, v225\n"
        "v_mfma_f32_16x16x32_bf16 v[48:51], a[48:51], a[16:19], v[48:51]\n"
        "v_cvt_pk_bf16_f32 v72, v72, v73\n"
        "v_cvt_pk_bf16_f32 v73, v74, v75\n"
        "v_mfma_f32_16x16x32_bf16 v[48:51], a[56:59], a[20:23], v[48:51]\n"
        "v_cvt_pk_bf16_f32 v74, v76, v77\n"
        "v_cvt_pk_bf16_f32 v75, v78, v79\n"
        "v_mfma_f32_16x16x32_bf16 v[48:51], a[224:227], a[24:27], v[48:51]\n"
        "v_permlane16_swap_b32 v72, v74\n"
        "v_mfma_f32_16x16x32_bf16 v[48:51], a[232:235], a[28:31], v[48:51]\n"
        "v_permlane16_swap_b32 v73, v75\n"
        "v_mfma_f32_16x16x32_bf16 v[52:55], a[236:239], a[28:31], v[52:55]\n"
        "global_store_dwordx4 v151, v[72:75], s[56:57] sc0 sc1\n"
        "s_add_u32 s56, s56, 0x8000\n"
        "s_addc_u32 s57, s57, 0\n"
        "v_mfma_f32_16x16x32_bf16 v[52:55], a[228:231], a[24:27], v[52:55]\n"
        "v_mfma_f32_16x16x32_bf16 v[52:55], a[60:63], a[20:23], v[52:55]\n"
        "v_mfma_f32_16x16x32_bf16 v[52:55], a[52:55], a[16:19], v[52:55]\n"
        "v_mfma_f32_16x16x32_bf16 v[48:51], v[240:243], a[248:251], v[48:51]\n"
        "v_mfma_f32_16x16x32_bf16 v[48:51], v[240:243], v[8:11], v[48:51]\n"
        "s_waitcnt vmcnt(3) lgkmcnt(0)\n"
        "v_mfma_f32_16x16x32_bf16 v[48:51], v[240:243], v[24:27], v[48:51]\n"
        "v_mfma_f32_16x16x32_bf16 v[52:55], v[238:241], a[248:251], v[52:55]\n"
        "v_mfma_f32_16x16x32_bf16 v[52:55], v[238:241], v[8:11], v[52:55]\n"
        "v_mfma_f32_16x16x32_bf16 v[52:55], v[238:241], v[24:27], v[52:55]\n"
        "v_mfma_f32_16x16x32_bf16 v[56:59], a[176:179], a[192:195], v[56:59]\n"
        "v_mfma_f32_16x16x32_bf16 v[56:59], a[184:187], a[196:199], v[56:59]\n"
        "v_mfma_f32_16x16x32_bf16 v[56:59], a[32:35], a[200:203], v[56:59]\n"
        "v_mfma_f32_16x16x32_bf16 v[56:59], a[40:43], a[204:207], v[56:59]\n"
        "v_mfma_f32_16x16x32_bf16 v[60:63], a[44:47], a[204:207], v[60:63]\n"
        "v_fmac_f32 v80, v48, v226\n"
        "v_fmac_f32 v81, v49, v226\n"
        "v_mfma_f32_16x16x32_bf16 v[60:63], a[36:39], a[200:203], v[60:63]\n"
        "v_fmac_f32 v82, v50, v226\n"
        "v_fmac_f32 v83, v51, v226\n"
        "v_mfma_f32_16x16x32_bf16 v[60:63], a[188:191], a[196:199], v[60:63]\n"
        "v_fmac_f32 v84, v52, v226\n"
        "v_fmac_f32 v85, v53, v226\n"
        "v_mfma_f32_16x16x32_bf16 v[60:63], a[180:183], a[192:195], v[60:63]\n"
        "v_fmac_f32 v86, v54, v226\n"
        "v_fmac_f32 v87, v55, v226\n"
        "v_mfma_f32_16x16x32_bf16 v[56:59], a[48:51], a[208:211], v[56:59]\n"
        "v_cvt_pk_bf16_f32 v80, v80, v81\n"
        "v_cvt_pk_bf16_f32 v81, v82, v83\n"
        "v_mfma_f32_16x16x32_bf16 v[56:59], a[56:59], a[212:215], v[56:59]\n"
        "v_cvt_pk_bf16_f32 v82, v84, v85\n"
        "v_cvt_pk_bf16_f32 v83, v86, v87\n"
        "v_mfma_f32_16x16x32_bf16 v[56:59], a[224:227], a[216:219], v[56:59]\n"
        "v_permlane16_swap_b32 v80, v82\n"
        "v_mfma_f32_16x16x32_bf16 v[56:59], a[232:235], a[220:223], v[56:59]\n"
        "v_permlane16_swap_b32 v81, v83\n"
        "v_mfma_f32_16x16x32_bf16 v[60:63], a[236:239], a[220:223], v[60:63]\n"
        "global_store_dwordx4 v151, v[80:83], s[56:57] sc0 sc1\n"
        "s_add_u32 s56, s56, 0x8000\n"
        "s_addc_u32 s57, s57, 0\n"
        "v_mfma_f32_16x16x32_bf16 v[60:63], a[228:231], a[216:219], v[60:63]\n"
        "v_mfma_f32_16x16x32_bf16 v[60:63], a[60:63], a[212:215], v[60:63]\n"
        "v_mfma_f32_16x16x32_bf16 v[60:63], a[52:55], a[208:211], v[60:63]\n"
        "v_mfma_f32_16x16x32_bf16 v[56:59], v[240:243], a[252:255], v[56:59]\n"
        "v_mfma_f32_16x16x32_bf16 v[56:59], v[240:243], v[12:15], v[56:59]\n"
        "s_waitcnt vmcnt(3)\n"
        "v_mfma_f32_16x16x32_bf16 v[56:59], v[240:243], v[28:31], v[56:59]\n"
        "v_mfma_f32_16x16x32_bf16 v[60:63], v[238:241], a[252:255], v[60:63]\n"
        "v_mfma_f32_16x16x32_bf16 v[60:63], v[238:241], v[12:15], v[60:63]\n"
        "v_mfma_f32_16x16x32_bf16 v[60:63], v[238:241], v[28:31], v[60:63]\n"
        "s_nop 4\n"
        "v_fmac_f32 v88, v56, v227\n"
        "v_fmac_f32 v89, v57, v227\n"
        "v_fmac_f32 v90, v58, v227\n"
        "v_fmac_f32 v91, v59, v227\n"
        "v_fmac_f32 v92, v60, v227\n"
        "v_fmac_f32 v93, v61, v227\n"
        "v_fmac_f32 v94, v62, v227\n"
        "v_fmac_f32 v95, v63, v227\n"
        "v_cvt_pk_bf16_f32 v88, v88, v89\n"
        "v_cvt_pk_bf16_f32 v89, v90, v91\n"
        "v_cvt_pk_bf16_f32 v90, v92, v93\n"
        "v_cvt_pk_bf16_f32 v91, v94, v95\n"
        "s_nop 0\n"
        "v_permlane16_swap_b32 v88, v90\n"
        "v_permlane16_swap_b32 v89, v91\n"
        "global_store_dwordx4 v151, v[88:91], s[56:57] sc0 sc1\n"
        "s_or_b32 s97, s55, s40\n"
        "s_cbranch_scc1 LEB_%=\n"
        "s_mov_b64 exec, 1\n"
        "global_store_dwordx2 v144, v[250:251], s[48:49] sc1\n"
        "s_mov_b64 exec, -1\n"
        "LEB_%=:\n"
        "; ---- END\n"
        "s_endpgm\n"
        "LPKV_%=:\n"
        "s_mov_b32 s97, 0x200000\n"
        "LQKV_%=:\n"
        "global_load_dwordx2 v[252:253], v245, s[48:49] sc1\n"
        "s_waitcnt vmcnt(0)\n"
        "v_cmp_ne_u64 vcc, v[252:253], v[250:251]\n"
        "s_nop 1\n"
        "s_cbranch_vccz LBKV_%=\n"
        "s_sleep 2\n"
        "s_sub_u32 s97, s97, 1\n"
        "s_cmp_lg_u32 s97, 0\n"
        "s_cbranch_scc1 LQKV_%=\n"
        "s_branch LBKV_%=\n"
        "LRL_%=:\n"
        "s_waitcnt vmcnt(0) lgkmcnt(0)\n"
        "s_nop 7\n"
        "v_accvgpr_write_b32 a176, v96\n"
        "v_accvgpr_write_b32 a177, v97\n"
        "v_accvgpr_write_b32 a178, v98\n"
        "v_accvgpr_write_b32 a179, v99\n"
        "v_accvgpr_write_b32 a180, v100\n"
        "v_accvgpr_write_b32 a181, v101\n"
        "v_accvgpr_write_b32 a182, v102\n"
        "v_accvgpr_write_b32 a183, v103\n"
        "v_accvgpr_write_b32 a184, v104\n"
        "v_accvgpr_write_b32 a185, v105\n"
        "v_accvgpr_write_b32 a186, v106\n"
        "v_accvgpr_write_b32 a187, v107\n"
        "v_accvgpr_write_b32 a188, v108\n"
        "v_accvgpr_write_b32 a189, v109\n"
        "v_accvgpr_write_b32 a190, v110\n"
        "v_accvgpr_write_b32 a191, v111\n"
        "s_mov_b32 s53, s44\n"
        "s_mov_b32 s96, 64\n"
        "s_mov_b32 s52, 0\n"
        "s_branch LREDO_%=\n"
        "LRET0_%=:\n"
        "v_accvgpr_write_b32 a176, v128\n"
        "v_accvgpr_write_b32 a177, v129\n"
        "v_accvgpr_write_b32 a178, v130\n"
        "v_accvgpr_write_b32 a179, v131\n"
        "v_accvgpr_write_b32 a180, v132\n"
        "v_accvgpr_write_b32 a181, v133\n"
        "v_accvgpr_write_b32 a182, v134\n"
        "v_accvgpr_write_b32 a183, v135\n"
        "v_accvgpr_write_b32 a184, v136\n"
        "v_accvgpr_write_b32 a185, v137\n"
        "v_accvgpr_write_b32 a186, v138\n"
        "v_accvgpr_write_b32 a187, v139\n"
        "v_accvgpr_write_b32 a188, v140\n"
        "v_accvgpr_write_b32 a189, v141\n"
        "v_accvgpr_write_b32 a190, v142\n"
        "v_accvgpr_write_b32 a191, v143\n"
        "s_mov_b32 s53, s44\n"
        "s_mov_b32 s96, 0\n"
        "s_mov_b32 s52, 1\n"
        "s_branch LREDO_%=\n"
        "LRET1_%=:\n"
        "s_branch LSL_%=\n"
        "LRH_%=:\n"
        "s_waitcnt vmcnt(0) lgkmcnt(0)\n"
        "s_nop 7\n"
        "v_accvgpr_write_b32 a176, v0\n"
        "v_accvgpr_write_b32 a177, v1\n"
        "v_accvgpr_write_b32 a178, v2\n"
        "v_accvgpr_write_b32 a179, v3\n"
        "v_accvgpr_write_b32 a180, v4\n"
        "v_accvgpr_write_b32 a181, v5\n"
        "v_accvgpr_write_b32 a182, v6\n"
        "v_accvgpr_write_b32 a183, v7\n"
        "v_accvgpr_write_b32 a184, v8\n"
        "v_accvgpr_write_b32 a185, v9\n"
        "v_accvgpr_write_b32 a186, v10\n"
        "v_accvgpr_write_b32 a187, v11\n"
        "v_accvgpr_write_b32 a188, v12\n"
        "v_accvgpr_write_b32 a189, v13\n"
        "v_accvgpr_write_b32 a190, v14\n"
        "v_accvgpr_write_b32 a191, v15\n"
        "s_mov_b32 s53, s45\n"
        "s_mov_b32 s96, 64\n"
        "s_mov_b32 s52, 2\n"
        "s_branch LREDO_%=\n"
        "LRET2_%=:\n"
        "v_accvgpr_write_b32 a176, v64\n"
        "v_accvgpr_write_b32 a177, v65\n"
        "v_accvgpr_write_b32 a178, v66\n"
        "v_accvgpr_write_b32 a179, v67\n"
        "v_accvgpr_write_b32 a180, v68\n"
        "v_accvgpr_write_b32 a181, v69\n"
        "v_accvgpr_write_b32 a182, v70\n"
        "v_accvgpr_write_b32 a183, v71\n"
        "v_accvgpr_write_b32 a184, v72\n"
        "v_accvgpr_write_b32 a185, v73\n"
        "v_accvgpr_write_b32 a186, v74\n"
        "v_accvgpr_write_b32 a187, v75\n"
        "v_accvgpr_write_b32 a188, v76\n"
        "v_accvgpr_write_b32 a189, v77\n"
        "v_accvgpr_write_b32 a190, v78\n"
        "v_accvgpr_write_b32 a191, v79\n"
        "s_mov_b32 s53, s45\n"
        "s_mov_b32 s96, 0\n"
        "s_mov_b32 s52, 3\n"
        "s_branch LREDO_%=\n"
        "LRET3_%=:\n"
        "s_branch LSH_%=\n"
        "LPCX_%=:\n"
        "s_mov_b32 s97, 0x200000\n"
        "LQCX_%=:\n"
        "global_load_dwordx2 v[236:237], v159, s[48:49] sc1\n"
        "s_waitcnt vmcnt(0)\n"
        "v_cmp_ne_u64 vcc, v[236:237], v[250:251]\n"
        "s_nop 1\n"
        "s_cbranch_vccz LBCX_%=\n"
        "s_sleep 2\n"
        "s_sub_u32 s97, s97, 1\n"
        "s_cmp_lg_u32 s97, 0\n"
        "s_cbranch_scc1 LQCX_%=\n"
        "s_branch LBCX_%=\n"
        "LPZZ_%=:\n"
        "s_mov_b32 s97, 0x200000\n"
        "LQZZ_%=:\n"
        "global_load_dwordx2 v[236:237], v234, s[48:49] sc1\n"
        "s_waitcnt vmcnt(0)\n"
        "v_cmp_ne_u64 vcc, v[236:237], v[250:251]\n"
        "s_nop 1\n"
        "s_cbranch_vccz LBZZ_%=\n"
        "s_sleep 2\n"
        "s_sub_u32 s97, s97, 1\n"
        "s_cmp_lg_u32 s97, 0\n"
        "s_cbranch_scc1 LQZZ_%=\n"
        "s_branch LBZZ_%=\n"
        "LPPR_%=:\n"
        "s_mov_b32 s97, 0x200000\n"
        "LQPR_%=:\n"
        "global_load_dwordx2 v[236:237], v235, s[48:49] sc1\n"
        "s_waitcnt vmcnt(0)\n"
        "v_cmp_ne_u64 vcc, v[236:237], v[250:251]\n"
        "s_nop 1\n"
        "s_cbranch_vccz LBPR_%=\n"
        "s_sleep 2\n"
        "s_sub_u32 s97, s97, 1\n"
        "s_cmp_lg_u32 s97, 0\n"
        "s_cbranch_scc1 LQPR_%=\n"
        "s_branch LBPR_%=\n"
        "LREDO_%=:\n"
        "s_lshl2_add_u32 s97, s55, s40\n"
        "s_lshl_b32 s97, s97, 12\n"
        "s_add_u32 s97, s97, 0x5100000\n"
        "s_add_u32 s88, s38, s97\n"
        "s_addc_u32 s89, s39, 0\n"
        "global_store_dwordx4 v254, v[144:147], s[88:89] offset:0\n"
        "global_store_dwordx4 v254, v[148:151], s[88:89] offset:1024\n"
        "global_store_dwordx4 v254, v[152:155], s[88:89] offset:2048\n"
        "global_store_dwordx4 v254, v[156:159], s[88:89] offset:3072\n"
        "s_waitcnt vmcnt(0)\n"
        "v_mov_b32 v112, 0\n"
        "v_mov_b32 v113, 0\n"
        "v_mov_b32 v114, 0\n"
        "v_mov_b32 v115, 0\n"
        "v_mov_b32 v116, 0\n"
        "v_mov_b32 v117, 0\n"
        "v_mov_b32 v118, 0\n"
        "v_mov_b32 v119, 0\n"
        "v_mov_b32 v120, 0\n"
        "v_mov_b32 v121, 0\n"
        "v_mov_b32 v122, 0\n"
        "v_mov_b32 v123, 0\n"
        "v_mov_b32 v124, 0\n"
        "v_mov_b32 v125, 0\n"
        "v_mov_b32 v126, 0\n"
        "v_mov_b32 v127, 0\n"
        "v_mov_b32 v144, 0\n"
        "v_mov_b32 v145, 0\n"
        "v_mov_b32 v146, 0\n"
        "v_mov_b32 v147, 0\n"
        "v_mov_b32 v148, 0\n"
        "v_mov_b32 v149, 0\n"
        "v_mov_b32 v150, 0\n"
        "v_mov_b32 v151, 0\n"
        "v_mov_b32 v152, 0\n"
        "v_mov_b32 v153, 0\n"
        "v_mov_b32 v154, 0\n"
        "v_mov_b32 v155, 0\n"
        "v_mov_b32 v156, 0\n"
        "v_mov_b32 v157, 0\n"
        "v_mov_b32 v158, 0\n"
        "v_mov_b32 v159, 0\n"
        "v_mov_b32 v60, 0\n"
        "v_mov_b32 v61, 0\n"
        "v_mov_b32 v62, 0\n"
        "v_mov_b32 v63, 0\n"
        "v_mov_b32 v24, 0xf149f2ca\n"
        "s_lshl_b32 s97, s53, 7\n"
        "s_add_u32 s97, s97, s96\n"
        "v_add_u32 v27, s97, v243\n"
        "s_mul_i32 s97, s43, 529408\n"
        "s_add_u32 s84, s38, s97\n"
        "s_addc_u32 s85, s39, 0\n"
        "s_add_u32 s84, s84, 0x1000\n"
        "s_addc_u32 s85, s85, 0\n"
        "s_add_u32 s86, s84, 0x1100000\n"
        "s_addc_u32 s87, s85, 0\n"
        "s_lshl_b32 s97, s53, 2\n"
        "s_add_u32 s97, s97, 4\n"
        "LRLOOP_%=:\n"
        "global_load_dwordx4 a[160:163], v254, s[84:85] offset:-4096 sc1\n"
        "global_load_dwordx4 a[164:167], v254, s[84:85] offset:-3072 sc1\n"
        "global_load_dwordx4 a[168:171], v254, s[84:85] offset:-2048 sc1\n"
        "global_load_dwordx4 a[172:175], v254, s[84:85] offset:-1024 sc1\n"
        "s_waitcnt vmcnt(0)\n"
        "v_mfma_f32_16x16x32_bf16 v[40:43], a[160:163], a[176:179], 0\n"
        "v_mfma_f32_16x16x32_bf16 v[40:43], a[164:167], a[180:183], v[40:43]\n"
        "v_mfma_f32_16x16x32_bf16 v[40:43], a[168:171], a[184:187], v[40:43]\n"
        "v_mfma_f32_16x16x32_bf16 v[40:43], a[172:175], a[188:191], v[40:43]\n"
        "s_nop 7\n"
        "global_load_dwordx4 a[160:163], v254, s[84:85] offset:0 sc1\n"
        "global_load_dwordx4 a[164:167], v254, s[84:85] offset:1024 sc1\n"
        "global_load_dwordx4 a[168:171], v254, s[84:85] offset:2048 sc1\n"
        "global_load_dwordx4 a[172:175], v254, s[84:85] offset:3072 sc1\n"
        "s_waitcnt vmcnt(0)\n"
        "v_mfma_f32_16x16x32_bf16 v[44:47], a[160:163], a[176:179], 0\n"
        "v_mfma_f32_16x16x32_bf16 v[44:47], a[164:167], a[180:183], v[44:47]\n"
        "v_mfma_f32_16x16x32_bf16 v[44:47], a[168:171], a[184:187], v[44:47]\n"
        "v_mfma_f32_16x16x32_bf16 v[44:47], a[172:175], a[188:191], v[44:47]\n"
        "s_nop 7\n"
        "v_cmp_gt_i32 vcc, 0, v27\n"
        "s_nop 1\n"
        "v_cndmask_b32 v40, v40, v242, vcc\n"
        "v_cmp_gt_i32 vcc, 1, v27\n"
        "s_nop 1\n"
        "v_cndmask_b32 v41, v41, v242, vcc\n"
        "v_cmp_gt_i32 vcc, 2, v27\n"
        "s_nop 1\n"
        "v_cndmask_b32 v42, v42, v242, vcc\n"
        "v_cmp_gt_i32 vcc, 3, v27\n"
        "s_nop 1\n"
        "v_cndmask_b32 v43, v43, v242, vcc\n"
        "v_cmp_gt_i32 vcc, 16, v27\n"
        "s_nop 1\n"
        "v_cndmask_b32 v44, v44, v242, vcc\n"
        "v_cmp_gt_i32 vcc, 17, v27\n"
        "s_nop 1\n"
        "v_cndmask_b32 v45, v45, v242, vcc\n"
        "v_cmp_gt_i32 vcc, 18, v27\n"
        "s_nop 1\n"
        "v_cndmask_b32 v46, v46, v242, vcc\n"
        "v_cmp_gt_i32 vcc, 19, v27\n"
        "s_nop 1\n"
        "v_cndmask_b32 v47, v47, v242, vcc\n"
        "v_max3_f32 v25, v40, v41, v24\n"
        "v_max3_f32 v25, v42, v43, v25\n"
        "v_max3_f32 v25, v44, v45, v25\n"
        "v_max3_f32 v25, v46, v47, v25\n"
        "v_mov_b32 v28, v25\n"
        "s_nop 1\n"
        "v_permlane16_swap_b32 v25, v28\n"
        "v_max_f32 v25, v25, v28\n"
        "v_mov_b32 v28, v25\n"
        "s_nop 1\n"
        "v_permlane32_swap_b32 v25, v28\n"
        "v_max_f32 v25, v25, v28\n"
        "v_sub_f32 v26, v24, v25\n"
        "v_exp_f32 v26, v26\n"
        "v_mov_b32 v24, v25\n"
        "v_sub_f32 v40, v40, v25\n"
        "v_exp_f32 v40, v40\n"
        "v_sub_f32 v41, v41, v25\n"
        "v_exp_f32 v41, v41\n"
        "v_sub_f32 v42, v42, v25\n"
        "v_exp_f32 v42, v42\n"
        "v_sub_f32 v43, v43, v25\n"
        "v_exp_f32 v43, v43\n"
        "v_sub_f32 v44, v44, v25\n"
        "v_exp_f32 v44, v44\n"
        "v_sub_f32 v45, v45, v25\n"
        "v_exp_f32 v45, v45\n"
        "v_sub_f32 v46, v46, v25\n"
        "v_exp_f32 v46, v46\n"
        "v_sub_f32 v47, v47, v25\n"
        "v_exp_f32 v47, v47\n"
        "v_cvt_pk_bf16_f32 v56, v40, v41\n"
        "v_cvt_pk_bf16_f32 v57, v42, v43\n"
        "v_cvt_pk_bf16_f32 v58, v44, v45\n"
        "v_cvt_pk_bf16_f32 v59, v46, v47\n"
        "v_mul_f32 v112, v112, v26\n"
        "v_mul_f32 v113, v113, v26\n"
        "v_mul_f32 v114, v114, v26\n"
        "v_mul_f32 v115, v115, v26\n"
        "v_mul_f32 v116, v116, v26\n"
        "v_mul_f32 v117, v117, v26\n"
        "v_mul_f32 v118, v118, v26\n"
        "v_mul_f32 v119, v119, v26\n"
        "v_mul_f32 v120, v120, v26\n"
        "v_mul_f32 v121, v121, v26\n"
        "v_mul_f32 v122, v122, v26\n"
        "v_mul_f32 v123, v123, v26\n"
        "v_mul_f32 v124, v124, v26\n"
        "v_mul_f32 v125, v125, v26\n"
        "v_mul_f32 v126, v126, v26\n"
        "v_mul_f32 v127, v127, v26\n"
        "v_mul_f32 v144, v144, v26\n"
        "v_mul_f32 v145, v145, v26\n"
        "v_mul_f32 v146, v146, v26\n"
        "v_mul_f32 v147, v147, v26\n"
        "v_mul_f32 v148, v148, v26\n"
        "v_mul_f32 v149, v149, v26\n"
        "v_mul_f32 v150, v150, v26\n"
        "v_mul_f32 v151, v151, v26\n"
        "v_mul_f32 v152, v152, v26\n"
        "v_mul_f32 v153, v153, v26\n"
        "v_mul_f32 v154, v154, v26\n"
        "v_mul_f32 v155, v155, v26\n"
        "v_mul_f32 v156, v156, v26\n"
        "v_mul_f32 v157, v157, v26\n"
        "v_mul_f32 v158, v158, v26\n"
        "v_mul_f32 v159, v159, v26\n"
        "v_mul_f32 v60, v60, v26\n"
        "v_mul_f32 v61, v61, v26\n"
        "v_mul_f32 v62, v62, v26\n"
        "v_mul_f32 v63, v63, v26\n"
        "s_nop 1\n"
        "v_mfma_f32_16x16x32_bf16 v[60:63], a[80:83], v[56:59], v[60:63]\n"
        "global_load_dwordx4 a[160:163], v254, s[86:87] offset:-4096 sc1\n"
        "global_load_dwordx4 a[164:167], v254, s[86:87] offset:-3072 sc1\n"
        "global_load_dwordx4 a[168:171], v254, s[86:87] offset:-2048 sc1\n"
        "global_load_dwordx4 a[172:175], v254, s[86:87] offset:-1024 sc1\n"
        "s_waitcnt vmcnt(0)\n"
        "v_mfma_f32_16x16x32_bf16 v[112:115], a[160:163], v[56:59], v[112:115]\n"
        "v_mfma_f32_16x16x32_bf16 v[116:119], a[164:167], v[56:59], v[116:119]\n"
        "v_mfma_f32_16x16x32_bf16 v[120:123], a[168:171], v[56:59], v[120:123]\n"
        "v_mfma_f32_16x16x32_bf16 v[124:127], a[172:175], v[56:59], v[124:127]\n"
        "s_nop 7\n"
        "global_load_dwordx4 a[160:163], v254, s[86:87] offset:0 sc1\n"
        "global_load_dwordx4 a[164:167], v254, s[86:87] offset:1024 sc1\n"
        "global_load_dwordx4 a[168:171], v254, s[86:87] offset:2048 sc1\n"
        "global_load_dwordx4 a[172:175], v254, s[86:87] offset:3072 sc1\n"
        "s_waitcnt vmcnt(0)\n"
        "v_mfma_f32_16x16x32_bf16 v[144:147], a[160:163], v[56:59], v[144:147]\n"
        "v_mfma_f32_16x16x32_bf16 v[148:151], a[164:167], v[56:59], v[148:151]\n"
        "v_mfma_f32_16x16x32_bf16 v[152:155], a[168:171], v[56:59], v[152:155]\n"
        "v_mfma_f32_16x16x32_bf16 v[156:159], a[172:175], v[56:59], v[156:159]\n"
        "s_nop 7\n"
        "s_add_u32 s84, s84, 0x2000\n"
        "s_addc_u32 s85, s85, 0\n"
        "s_add_u32 s86, s86, 0x2000\n"
        "s_addc_u32 s87, s87, 0\n"
        "v_subrev_u32 v27, 32, v27\n"
        "s_sub_u32 s97, s97, 1\n"
        "s_cmp_lg_u32 s97, 0\n"
        "s_cbranch_scc1 LRLOOP_%=\n"
        "s_mul_i32 s97, s53, 1052672\n"
        "s_lshl_b32 s86, s43, 15\n"
        "s_add_u32 s97, s97, s86\n"
        "s_lshl_b32 s86, s40, 10\n"
        "s_add_u32 s97, s97, s86\n"
        "s_lshl_b32 s86, s96, 6\n"
        "s_add_u32 s97, s97, s86\n"
        "s_add_u32 s97, s97, 0x2200000\n"
        "s_add_u32 s84, s38, s97\n"
        "s_addc_u32 s85, s39, 0\n"
        "v_rcp_f32 v26, v60\n"
        "s_nop 0\n"
        "v_mul_f32 v112, v112, v26\n"
        "v_mul_f32 v113, v113, v26\n"
        "v_mul_f32 v114, v114, v26\n"
        "v_mul_f32 v115, v115, v26\n"
        "v_mul_f32 v116, v116, v26\n"
        "v_mul_f32 v117, v117, v26\n"
        "v_mul_f32 v118, v118, v26\n"
        "v_mul_f32 v119, v119, v26\n"
        "v_cvt_pk_bf16_f32 v112, v112, v113\n"
        "v_cvt_pk_bf16_f32 v113, v114, v115\n"
        "v_cvt_pk_bf16_f32 v114, v116, v117\n"
        "v_cvt_pk_bf16_f32 v115, v118, v119\n"
        "global_store_dwordx4 v254, v[112:115], s[84:85] sc1\n"
        "s_add_u32 s84, s84, 0x2000\n"
        "s_addc_u32 s85, s85, 0\n"
        "v_mul_f32 v120, v120, v26\n"
        "v_mul_f32 v121, v121, v26\n"
        "v_mul_f32 v122, v122, v26\n"
        "v_mul_f32 v123, v123, v26\n"
        "v_mul_f32 v124, v124, v26\n"
        "v_mul_f32 v125, v125, v26\n"
        "v_mul_f32 v126, v126, v26\n"
        "v_mul_f32 v127, v127, v26\n"
        "v_cvt_pk_bf16_f32 v120, v120, v121\n"
        "v_cvt_pk_bf16_f32 v121, v122, v123\n"
        "v_cvt_pk_bf16_f32 v122, v124, v125\n"
        "v_cvt_pk_bf16_f32 v123, v126, v127\n"
        "global_store_dwordx4 v254, v[120:123], s[84:85] sc1\n"
        "s_add_u32 s84, s84, 0x2000\n"
        "s_addc_u32 s85, s85, 0\n"
        "v_mul_f32 v144, v144, v26\n"
        "v_mul_f32 v145, v145, v26\n"
        "v_mul_f32 v146, v146, v26\n"
        "v_mul_f32 v147, v147, v26\n"
        "v_mul_f32 v148, v148, v26\n"
        "v_mul_f32 v149, v149, v26\n"
        "v_mul_f32 v150, v150, v26\n"
        "v_mul_f32 v151, v151, v26\n"
        "v_cvt_pk_bf16_f32 v144, v144, v145\n"
        "v_cvt_pk_bf16_f32 v145, v146, v147\n"
        "v_cvt_pk_bf16_f32 v146, v148, v149\n"
        "v_cvt_pk_bf16_f32 v147, v150, v151\n"
        "global_store_dwordx4 v254, v[144:147], s[84:85] sc1\n"
        "s_add_u32 s84, s84, 0x2000\n"
        "s_addc_u32 s85, s85, 0\n"
        "v_mul_f32 v152, v152, v26\n"
        "v_mul_f32 v153, v153, v26\n"
        "v_mul_f32 v154, v154, v26\n"
        "v_mul_f32 v155, v155, v26\n"
        "v_mul_f32 v156, v156, v26\n"
        "v_mul_f32 v157, v157, v26\n"
        "v_mul_f32 v158, v158, v26\n"
        "v_mul_f32 v159, v159, v26\n"
        "v_cvt_pk_bf16_f32 v152, v152, v153\n"
        "v_cvt_pk_bf16_f32 v153, v154, v155\n"
        "v_cvt_pk_bf16_f32 v154, v156, v157\n"
        "v_cvt_pk_bf16_f32 v155, v158, v159\n"
        "global_store_dwordx4 v254, v[152:155], s[84:85] sc1\n"
        "s_add_u32 s84, s84, 0x2000\n"
        "s_addc_u32 s85, s85, 0\n"
        "s_waitcnt vmcnt(0)\n"
        "global_load_dwordx4 v[144:147], v254, s[88:89] offset:0 sc1\n"
        "global_load_dwordx4 v[148:151], v254, s[88:89] offset:1024 sc1\n"
        "global_load_dwordx4 v[152:155], v254, s[88:89] offset:2048 sc1\n"
        "global_load_dwordx4 v[156:159], v254, s[88:89] offset:3072 sc1\n"
        "s_waitcnt vmcnt(0)\n"
        "s_cmp_eq_u32 s52, 0\n"
        "s_cbranch_scc1 LRET0_%=\n"
        "s_cmp_eq_u32 s52, 1\n"
        "s_cbranch_scc1 LRET1_%=\n"
        "s_cmp_eq_u32 s52, 2\n"
        "s_cbranch_scc1 LRET2_%=\n"
        "s_branch LRET3_%=\n"
        "LEND_%=:\n"
        ".p2align 12\n"
        :
        : "s"(__builtin_amdgcn_kernarg_segment_ptr()), "s"(blockIdx.x), "s"(__builtin_amdgcn_readfirstlane(threadIdx.x >> 6))
        : "v0", "v1", "v2", "v3", "v4", "v5", "v6", "v7", "v8", "v9", "v10", "v11", "v12", "v13", "v14", "v15", "v16", "v17", "v18", "v19", "v20", "v21", "v22", "v23", "v24", "v25", "v26", "v27", "v28", "v29", "v30", "v31",
        "v32", "v33", "v34", "v35", "v36", "v37", "v38", "v39", "v40", "v41", "v42", "v43", "v44", "v45", "v46", "v47", "v48", "v49", "v50", "v51", "v52", "v53", "v54", "v55", "v56", "v57", "v58", "v59", "v60", "v61", "v62", "v63",
        "v64", "v65", "v66", "v67", "v68", "v69", "v70", "v71", "v72", "v73", "v74", "v75", "v76", "v77", "v78", "v79", "v80", "v81", "v82", "v83", "v84", "v85", "v86", "v87", "v88", "v89", "v90", "v91", "v92", "v93", "v94", "v95",
        "v96", "v97", "v98", "v99", "v100", "v101", "v102", "v103", "v104", "v105", "v106", "v107", "v108", "v109", "v110", "v111", "v112", "v113", "v114", "v115", "v116", "v117", "v118", "v119", "v120", "v121", "v122", "v123", "v124", "v125", "v126", "v127",
        "v128", "v129", "v130", "v131", "v132", "v133", "v134", "v135", "v136", "v137", "v138", "v139", "v140", "v141", "v142", "v143", "v144", "v145", "v146", "v147", "v148", "v149", "v150", "v151", "v152", "v153", "v154", "v155", "v156", "v157", "v158", "v159",
        "v160", "v161", "v162", "v163", "v164", "v165", "v166", "v167", "v168", "v169", "v170", "v171", "v172", "v173", "v174", "v175", "v176", "v177", "v178", "v179", "v180", "v181", "v182", "v183", "v184", "v185", "v186", "v187", "v188", "v189", "v190", "v191",
        "v192", "v193", "v194", "v195", "v196", "v197", "v198", "v199", "v200", "v201", "v202", "v203", "v204", "v205", "v206", "v207", "v208", "v209", "v210", "v211", "v212", "v213", "v214", "v215", "v216", "v217", "v218", "v219", "v220", "v221", "v222", "v223",
        "v224", "v225", "v226", "v227", "v228", "v229", "v230", "v231", "v232", "v233", "v234", "v235", "v236", "v237", "v238", "v239", "v240", "v241", "v242", "v243", "v244", "v245", "v246", "v247", "v248", "v249", "v250", "v251", "v252", "v253", "v254", "v255",
        "a0", "a1", "a2", "a3", "a4", "a5", "a6", "a7", "a8", "a9", "a10", "a11", "a12", "a13", "a14", "a15", "a16", "a17", "a18", "a19", "a20", "a21", "a22", "a23", "a24", "a25", "a26", "a27", "a28", "a29", "a30", "a31",
        "a32", "a33", "a34", "a35", "a36", "a37", "a38", "a39", "a40", "a41", "a42", "a43", "a44", "a45", "a46", "a47", "a48", "a49", "a50", "a51", "a52", "a53", "a54", "a55", "a56", "a57", "a58", "a59", "a60", "a61", "a62", "a63",
        "a64", "a65", "a66", "a67", "a68", "a69", "a70", "a71", "a72", "a73", "a74", "a75", "a76", "a77", "a78", "a79", "a80", "a81", "a82", "a83", "a84", "a85", "a86", "a87", "a88", "a89", "a90", "a91", "a92", "a93", "a94", "a95",
        "a96", "a97", "a98", "a99", "a100", "a101", "a102", "a103", "a104", "a105", "a106", "a107", "a108", "a109", "a110", "a111", "a112", "a113", "a114", "a115", "a116", "a117", "a118", "a119", "a120", "a121", "a122", "a123", "a124", "a125", "a126", "a127",
        "a128", "a129", "a130", "a131", "a132", "a133", "a134", "a135", "a136", "a137", "a138", "a139", "a140", "a141", "a142", "a143", "a144", "a145", "a146", "a147", "a148", "a149", "a150", "a151", "a152", "a153", "a154", "a155", "a156", "a157", "a158", "a159",
        "a160", "a161", "a162", "a163", "a164", "a165", "a166", "a167", "a168", "a169", "a170", "a171", "a172", "a173", "a174", "a175", "a176", "a177", "a178", "a179", "a180", "a181", "a182", "a183", "a184", "a185", "a186", "a187", "a188", "a189", "a190", "a191",
        "a192", "a193", "a194", "a195", "a196", "a197", "a198", "a199", "a200", "a201", "a202", "a203", "a204", "a205", "a206", "a207", "a208", "a209", "a210", "a211", "a212", "a213", "a214", "a215", "a216", "a217", "a218", "a219", "a220", "a221", "a222", "a223",
        "a224", "a225", "a226", "a227", "a228", "a229", "a230", "a231", "a232", "a233", "a234", "a235", "a236", "a237", "a238", "a239", "a240", "a241", "a242", "a243", "a244", "a245", "a246", "a247", "a248", "a249", "a250", "a251", "a252", "a253", "a254", "a255",
        "s8", "s9", "s10", "s11", "s12", "s13", "s14", "s15", "s16", "s17", "s18", "s19", "s20", "s21", "s22", "s23", "s24", "s25", "s26", "s27", "s28", "s29", "s30", "s31", "s36", "s37", "s38", "s39", "s40", "s41", "s42", "s43",
        "s44", "s45", "s46", "s47", "s48", "s49", "s50", "s51", "s52", "s53", "s54", "s55", "s56", "s57", "s58", "s59", "s60", "s61", "s62", "s63", "s64", "s65", "s66", "s67", "s68", "s69", "s70", "s71", "s72", "s73", "s74", "s75",
        "s76", "s77", "s78", "s79", "s80", "s81", "s82", "s83", "s84", "s85", "s86", "s87", "s88", "s89", "s90", "s91", "s92", "s93", "s94", "s95", "s96", "s97", "s98", "s99", "vcc", "scc", "memory");
}

extern "C" void kernel_launch(void* const* d_in, const int* in_sizes, int n_in, void* d_out, int out_size, void* d_ws, size_t ws_size, hipStream_t stream) {
    (void)in_sizes; (void)n_in; (void)out_size;
    constexpr size_t WS_USED = (size_t)85 << 20;
    Params P;
    P.x = d_in[0]; P.ckv = d_in[1]; P.wq = d_in[2]; P.wuk = d_in[3]; P.wuv = d_in[4]; P.wo = d_in[5];
    P.bk = d_in[7]; P.bv = d_in[8]; P.shk = d_in[9]; P.ssk = d_in[10]; P.ssd = d_in[11]; P.gw = d_in[12];
    P.out = d_out; P.ws = (char*)d_ws;
    if (ws_size > WS_USED) P.ws += (ws_size - WS_USED) & ~(size_t)0xfffff;
    constexpr int LDS_MAX = 163840;
    (void)hipFuncSetAttribute((const void*)k_main, hipFuncAttributeMaxDynamicSharedMemorySize, LDS_MAX);
    k_main<<<256, 256, LDS_MAX, stream>>>(P);
}
```

```cpp
#include <hip/hip_runtime.h>
#include <stdint.h>

struct Params {
    const void *x, *ckv, *wq, *wuk, *wuv, *wo, *bk, *bv, *shk, *ssk, *ssd, *gw;
    void* out; char* ws;
};

__global__ __launch_bounds__(256, 1) void k_main(Params P) {
    asm volatile(
        "; ---- PRO\n"
        "s_load_dwordx16 s[8:23], %0, 0\n"
        "s_load_dwordx8 s[24:31], %0, 64\n"
        "s_load_dwordx4 s[36:39], %0, 96\n"
        "s_mov_b32 s55, %1\n"
        "s_mov_b32 s40, %2\n"
        "v_mbcnt_lo_u32_b32 v0, -1, 0\n"
        "v_mbcnt_hi_u32_b32 v0, -1, v0\n"
        "s_and_b32 s41, s55, 7\n"
        "s_lshr_b32 s42, s55, 3\n"
        "s_lshr_b32 s50, s42, 3\n"
        "s_lshl2_add_u32 s43, s41, s50\n"
        "s_and_b32 s44, s42, 7\n"
        "s_sub_u32 s45, 15, s44\n"
        "s_lshl_b32 s72, s40, 13\n"
        "s_lshl_b32 s73, s40, 12\n"
        "v_lshlrev_b32 v254, 4, v0\n"
        "v_lshrrev_b32 v2, 4, v0\n"
        "v_and_b32 v1, 15, v0\n"
        "v_lshrrev_b32 v4, 3, v0\n"
        "v_and_b32 v5, 7, v0\n"
        "v_and_b32 v8, 6, v4\n"
        "v_xor_b32 v8, v8, v5\n"
        "v_lshlrev_b32 v8, 4, v8\n"
        "v_lshl_add_u32 v9, v4, 13, v8\n"
        "v_xor_b32 v10, 4, v4\n"
        "v_lshl_add_u32 v10, v10, 13, v8\n"
        "v_add_u32 v238, 4096, v9\n"
        "v_add_u32 v239, 3200, v9\n"
        "v_add_u32 v240, 67584, v10\n"
        "v_add_u32 v241, 66688, v10\n"
        "s_cmp_ge_u32 s40, 2\n"
        "s_cselect_b32 s50, s45, s44\n"
        "s_and_b32 s51, s40, 1\n"
        "s_lshl1_add_u32 s50, s50, s51\n"
        "s_lshl_b32 s50, s50, 16\n"
        "s_lshl_b32 s51, s40, 17\n"
        "s_lshl_b32 s52, s43, 8\n"
        "s_add_u32 s51, s51, s52\n"
        "s_waitcnt lgkmcnt(0)\n"
        "s_add_u32 s60, s14, s51\n"
        "s_addc_u32 s61, s15, 0\n"
        "s_sub_u32 s60, s60, 4096\n"
        "s_subb_u32 s61, s61, 0\n"
        "s_add_u32 m0, s73, 0x10000\n"
        "s_nop 0\n"
        "global_load_lds_dwordx4 v238, s[60:61] offset:0\n"
        "global_load_lds_dwordx4 v239, s[60:61] offset:1024\n"
        "global_load_lds_dwordx4 v240, s[60:61] offset:2048\n"
        "global_load_lds_dwordx4 v241, s[60:61] offset:3072\n"
        "v_xor_b32 v6, v5, v2\n"
        "v_lshlrev_b32 v6, 4, v6\n"
        "v_xor_b32 v7, 64, v6\n"
        "v_lshl_add_u32 v234, v4, 10, v6\n"
        "v_lshl_add_u32 v8, v4, 10, v7\n"
        "v_add_u32 v235, 7168, v8\n"
        "v_add_u32 v236, 14336, v234\n"
        "v_add_u32 v237, 21504, v8\n"
        "s_add_u32 s56, s10, s50\n"
        "s_addc_u32 s57, s11, 0\n"
        "s_add_u32 m0, s72, 0x0\n"
        "s_nop 0\n"
        "global_load_lds_dwordx4 v234, s[56:57] offset:0\n"
        "global_load_lds_dwordx4 v235, s[56:57] offset:1024\n"
        "global_load_lds_dwordx4 v236, s[56:57] offset:2048\n"
        "global_load_lds_dwordx4 v237, s[56:57] offset:3072\n"
        "s_getpc_b64 s[46:47]\n"
        "LPC_%=:\n"
        "s_lshl2_add_u32 s53, s42, s40\n"
        "s_lshl_b32 s53, s53, 10\n"
        "v_lshlrev_b32 v12, 6, v1\n"
        "v_add_u32 v12, s53, v12\n"
        "v_min_u32 v252, LEND_%=-LPC_%=-4, v12\n"
        "global_load_dword v253, v252, s[46:47] offset:0\n"
        "s_add_u32 s62, s16, s51\n"
        "s_addc_u32 s63, s17, 0\n"
        "s_sub_u32 s62, s62, 4096\n"
        "s_subb_u32 s63, s63, 0\n"
        "s_add_u32 m0, s73, 0x14000\n"
        "s_nop 0\n"
        "global_load_lds_dwordx4 v238, s[62:63] offset:0\n"
        "global_load_lds_dwordx4 v239, s[62:63] offset:1024\n"
        "global_load_lds_dwordx4 v240, s[62:63] offset:2048\n"
        "global_load_lds_dwordx4 v241, s[62:63] offset:3072\n"
        "s_add_u32 s60, s60, 0x80000\n"
        "s_addc_u32 s61, s61, 0\n"
        "s_add_u32 s62, s62, 0x80000\n"
        "s_addc_u32 s63, s63, 0\n"
        "s_add_u32 s58, s56, 0x8000\n"
        "s_addc_u32 s59, s57, 0\n"
        "s_add_u32 s68, s12, s51\n"
        "s_addc_u32 s69, s13, 0\n"
        "s_add_u32 m0, s72, 0x1000\n"
        "s_nop 0\n"
        "global_load_lds_dwordx4 v234, s[58:59] offset:0\n"
        "global_load_lds_dwordx4 v235, s[58:59] offset:1024\n"
        "global_load_lds_dwordx4 v236, s[58:59] offset:2048\n"
        "global_load_lds_dwordx4 v237, s[58:59] offset:3072\n"
        "s_sub_u32 s68, s68, 4096\n"
        "s_subb_u32 s69, s69, 0\n"
        "v_lshrrev_b32 v8, 1, v1\n"
        "v_xor_b32 v8, v8, v2\n"
        "v_lshlrev_b32 v8, 4, v8\n"
        "v_lshl_add_u32 v8, v1, 7, v8\n"
        "v_xor_b32 v9, 64, v8\n"
        "s_lshr_b32 s50, s40, 1\n"
        "s_lshl_b32 s50, s50, 13\n"
        "v_add_u32 v224, s50, v8\n"
        "v_add_u32 v246, s72, v8\n"
        "v_add_u32 v248, 0x10000, v246\n"
        "v_add_u32 v225, s50, v9\n"
        "v_add_u32 v247, s72, v9\n"
        "v_add_u32 v249, 0x10000, v247\n"
        "s_and_b32 s50, s40, 1\n"
        "s_lshl_b32 s50, s50, 10\n"
        "s_add_u32 s50, s50, 0x10000\n"
        "v_lshlrev_b32 v8, 11, v2\n"
        "v_add_u32 v8, s50, v8\n"
        "v_lshrrev_b32 v9, 2, v1\n"
        "v_or_b32 v10, 4, v9\n"
        "v_and_b32 v11, 1, v2\n"
        "v_and_b32 v13, 3, v0\n"
        "v_lshlrev_b32 v14, 2, v11\n"
        "v_xor_b32 v14, v14, v9\n"
        "v_bfe_u32 v15, v14, 1, 2\n"
        "v_xor_b32 v15, 0, v15\n"
        "v_lshlrev_b32 v15, 5, v15\n"
        "v_lshl_add_u32 v14, v14, 7, v15\n"
        "v_lshl_add_u32 v14, v13, 3, v14\n"
        "v_add_u32 v226, v14, v8\n"
        "v_xor_b32 v228, 32, v226\n"
        "v_xor_b32 v230, 64, v226\n"
        "v_xor_b32 v232, 96, v226\n"
        "v_lshlrev_b32 v14, 2, v11\n"
        "v_xor_b32 v14, v14, v10\n"
        "v_bfe_u32 v15, v14, 1, 2\n"
        "v_xor_b32 v15, 0, v15\n"
        "v_lshlrev_b32 v15, 5, v15\n"
        "v_lshl_add_u32 v14, v14, 7, v15\n"
        "v_lshl_add_u32 v14, v13, 3, v14\n"
        "v_add_u32 v227, v14, v8\n"
        "v_xor_b32 v229, 32, v227\n"
        "v_xor_b32 v231, 64, v227\n"
        "v_xor_b32 v233, 96, v227\n"
        "v_add_u32 v216, 0x10000, v226\n"
        "v_add_u32 v217, 0x10000, v227\n"
        "v_add_u32 v218, 0x10000, v228\n"
        "v_add_u32 v219, 0x10000, v229\n"
        "v_add_u32 v220, 0x10000, v230\n"
        "v_add_u32 v221, 0x10000, v231\n"
        "v_add_u32 v222, 0x10000, v232\n"
        "v_add_u32 v223, 0x10000, v233\n"
        "v_lshl_add_u32 v242, v4, 11, v6\n"
        "v_lshl_add_u32 v8, v4, 11, v7\n"
        "v_add_u32 v243, 15360, v8\n"
        "v_add_u32 v244, 129024, v242\n"
        "v_add_u32 v245, 144384, v8\n"
        "s_lshl_b32 s50, s45, 18\n"
        "s_lshl_b32 s51, s40, 15\n"
        "s_add_u32 s50, s50, s51\n"
        "s_add_u32 s64, s8, s50\n"
        "s_addc_u32 s65, s9, 0\n"
        "s_lshl_b32 s50, s44, 18\n"
        "s_add_u32 s50, s50, s51\n"
        "s_add_u32 s66, s8, s50\n"
        "s_addc_u32 s67, s9, 0\n"
        "s_add_u32 s48, s38, 0x3b00000\n"
        "s_addc_u32 s49, s39, 0\n"
        "s_mov_b32 s35, 0xffff0000\n"
        "v_mov_b32 v11, 0\n"
        "global_load_dwordx2 v[250:251], v11, s[48:49] sc1\n"
        "s_lshl2_add_u32 s82, s55, s40\n"
        "s_lshl_b32 s82, s82, 11\n"
        "v_and_b32 v20, 0xf0, v254\n"
        "v_lshl_add_u32 v20, v20, 3, s82\n"
        "global_load_dword v253, v20, s[10:11] offset:0\n"
        "s_lshl2_add_u32 s82, s42, s40\n"
        "s_lshl3_add_u32 s82, s82, s41\n"
        "s_lshl_b32 s82, s82, 10\n"
        "v_and_b32 v21, 0x70, v254\n"
        "v_lshl_add_u32 v173, v21, 3, s82\n"
        "s_lshl2_add_u32 s82, s55, s40\n"
        "s_lshl_b32 s82, s82, 12\n"
        "v_bfe_u32 v20, v254, 4, 1\n"
        "v_bfe_u32 v21, v254, 5, 1\n"
        "v_lshlrev_b32 v20, 7, v20\n"
        "v_lshl_add_u32 v20, v21, 11, v20\n"
        "v_add_u32 v199, s82, v20\n"
        "s_mov_b64 exec, 0\n"
        "s_cmp_eq_u32 s40, 0\n"
        "s_cbranch_scc1 LHA0_%=\n"
        "s_cmp_eq_u32 s40, 1\n"
        "s_cbranch_scc1 LHA1_%=\n"
        "s_cmp_eq_u32 s40, 2\n"
        "s_cbranch_scc1 LHA2_%=\n"
        "s_branch LHA3_%=\n"
        "LHRA_%=:\n"
        "s_mov_b64 exec, -1\n"
        "s_add_u32 s56, s56, 0x80\n"
        "s_addc_u32 s57, s57, 0\n"
        "s_add_u32 s58, s58, 0x80\n"
        "s_addc_u32 s59, s59, 0\n"
        "s_add_u32 m0, s73, 0x18000\n"
        "s_nop 0\n"
        "global_load_lds_dwordx4 v238, s[60:61] offset:0\n"
        "global_load_lds_dwordx4 v239, s[60:61] offset:1024\n"
        "global_load_lds_dwordx4 v240, s[60:61] offset:2048\n"
        "global_load_lds_dwordx4 v241, s[60:61] offset:3072\n"
        "s_add_u32 m0, s73, 0x1c000\n"
        "s_nop 0\n"
        "global_load_lds_dwordx4 v238, s[62:63] offset:0\n"
        "global_load_lds_dwordx4 v239, s[62:63] offset:1024\n"
        "global_load_lds_dwordx4 v240, s[62:63] offset:2048\n"
        "global_load_lds_dwordx4 v241, s[62:63] offset:3072\n"
        "s_add_u32 s60, s60, 0x80000\n"
        "s_addc_u32 s61, s61, 0\n"
        "s_add_u32 s62, s62, 0x80000\n"
        "s_addc_u32 s63, s63, 0\n"
        "; ---- FRONT\n"
        "s_add_u32 m0, s72, 0x8000\n"
        "s_waitcnt vmcnt(19)\n"
        "global_load_lds_dwordx4 v234, s[56:57] offset:0\n"
        "s_barrier\n"
        "ds_read_b64_tr_b16 v[32:33], v226 offset:0\n"
        "ds_read_b64_tr_b16 v[34:35], v227 offset:0\n"
        "ds_read_b128 v[0:3], v224 offset:16384\n"
        "global_load_lds_dwordx4 v235, s[56:57] offset:1024\n"
        "global_load_lds_dwordx4 v236, s[56:57] offset:2048\n"
        "ds_read_b128 v[4:7], v225 offset:16384\n"
        "ds_read_b64_tr_b16 v[64:65], v226 offset:8192\n"
        "ds_read_b64_tr_b16 v[66:67], v227 offset:8192\n"
        "s_lshl_b32 s94, s41, 7\n"
        "s_waitcnt lgkmcnt(3)\n"
        "v_mfma_f32_16x16x32_bf16 a[0:3], v[32:35], v[0:3], 0\n"
        "global_load_lds_dwordx4 v237, s[56:57] offset:3072\n"
        "ds_read_b128 v[12:15], v225 offset:18432\n"
        "ds_read_b128 v[8:11], v224 offset:18432\n"
        "s_add_u32 m0, s72, 0x9000\n"
        "s_waitcnt lgkmcnt(2)\n"
        "global_load_lds_dwordx4 v234, s[58:59] offset:0\n"
        "ds_read_b64_tr_b16 v[36:37], v228 offset:0\n"
        "ds_read_b64_tr_b16 v[38:39], v229 offset:0\n"
        "s_lshl2_add_u32 s94, s42, s94\n"
        "v_mfma_f32_16x16x32_bf16 a[0:3], v[64:67], v[4:7], a[0:3]\n"
        "ds_read_b64_tr_b16 v[68:69], v228 offset:8192\n"
        "ds_read_b64_tr_b16 v[70:71], v229 offset:8192\n"
        "ds_read_b64_tr_b16 v[40:41], v230 offset:0\n"
        "ds_read_b64_tr_b16 v[42:43], v231 offset:0\n"
        "ds_read_b64_tr_b16 v[72:73], v230 offset:8192\n"
        "s_add_u32 s94, s94, s40\n"
        "s_waitcnt lgkmcnt(8)\n"
        "v_mfma_f32_16x16x32_bf16 a[32:35], v[64:67], v[12:15], 0\n"
        "ds_read_b64_tr_b16 v[74:75], v231 offset:8192\n"
        "s_lshl_b32 s94, s94, 1\n"
        "s_waitcnt lgkmcnt(8)\n"
        "v_mfma_f32_16x16x32_bf16 a[32:35], v[32:35], v[8:11], a[32:35]\n"
        "ds_read_b64_tr_b16 v[44:45], v232 offset:0\n"
        "ds_read_b64_tr_b16 v[46:47], v233 offset:0\n"
        "ds_read_b64_tr_b16 v[76:77], v232 offset:8192\n"
        "ds_read_b64_tr_b16 v[78:79], v233 offset:8192\n"
        "ds_read_b128 v[16:19], v224 offset:0\n"
        "ds_read_b128 v[20:23], v225 offset:0\n"
        "s_lshl_b32 s50, s42, 6\n"
        "s_waitcnt lgkmcnt(12)\n"
        "v_mfma_f32_16x16x32_bf16 a[36:39], v[36:39], v[8:11], 0\n"
        "ds_read_b128 v[24:27], v224 offset:2048\n"
        "global_load_lds_dwordx4 v235, s[58:59] offset:1024\n"
        "s_lshl_b32 s51, s41, 7\n"
        "s_waitcnt lgkmcnt(11)\n"
        "v_mfma_f32_16x16x32_bf16 a[36:39], v[68:71], v[12:15], a[36:39]\n"
        "ds_read_b128 v[28:31], v225 offset:2048\n"
        "s_add_u32 s56, s56, 0x80\n"
        "s_addc_u32 s57, s57, 0\n"
        "v_mfma_f32_16x16x32_bf16 a[4:7], v[68:71], v[4:7], 0\n"
        "s_waitcnt lgkmcnt(10)\n"
        "v_mov_b32 v172, s94\n"
        "v_lshlrev_b32 v198, 11, v172\n"
        "v_mfma_f32_16x16x32_bf16 a[4:7], v[36:39], v[0:3], a[4:7]\n"
        "s_waitcnt lgkmcnt(8)\n"
        "v_lshrrev_b32 v166, 4, v254\n"
        "v_lshrrev_b32 v167, 3, v166\n"
        "v_mfma_f32_16x16x32_bf16 a[8:11], v[40:43], v[0:3], 0\n"
        "global_load_lds_dwordx4 v236, s[58:59] offset:2048\n"
        "s_lshl_b32 s52, s40, 5\n"
        "v_and_b32 v168, 7, v166\n"
        "v_mfma_f32_16x16x32_bf16 a[8:11], v[72:75], v[4:7], a[8:11]\n"
        "s_add_u32 s51, s51, s52\n"
        "v_lshl_add_u32 v167, v167, 3, s50\n"
        "v_lshl_add_u32 v168, v168, 2, s51\n"
        "v_mfma_f32_16x16x32_bf16 a[40:43], v[72:75], v[12:15], 0\n"
        "s_waitcnt lgkmcnt(4)\n"
        "v_lshlrev_b32 v169, 11, v167\n"
        "v_lshl_add_u32 v169, v168, 1, v169\n"
        "v_mfma_f32_16x16x32_bf16 a[40:43], v[40:43], v[8:11], a[40:43]\n"
        "s_waitcnt vmcnt(21)\n"
        "v_cmp_eq_u32 s[78:79], 0, v167\n"
        "v_mov_b32 v171, 6144\n"
        "v_mfma_f32_16x16x32_bf16 a[44:47], v[44:47], v[8:11], 0\n"
        "global_load_lds_dwordx4 v237, s[58:59] offset:3072\n"
        "s_add_u32 s58, s58, 0x80\n"
        "s_addc_u32 s59, s59, 0\n"
        "v_mfma_f32_16x16x32_bf16 a[44:47], v[76:79], v[12:15], a[44:47]\n"
        "s_add_u32 m0, s73, 0x20000\n"
        "v_cndmask_b32 v170, 0, v171, s[78:79]\n"
        "v_add_u32 v170, v170, v169\n"
        "v_mfma_f32_16x16x32_bf16 a[12:15], v[76:79], v[4:7], 0\n"
        "s_barrier\n"
        "v_mfma_f32_16x16x32_bf16 a[12:15], v[44:47], v[0:3], a[12:15]\n"
        "ds_read_b64_tr_b16 v[48:49], v226 offset:16384\n"
        "s_and_b32 s91, s43, 15\n"
        "s_waitcnt lgkmcnt(1)\n"
        "v_mfma_f32_16x16x32_bf16 a[128:131], v[32:35], v[16:19], 0\n"
        "global_load_lds_dwordx4 v238, s[60:61] offset:0\n"
        "ds_read_b64_tr_b16 v[50:51], v227 offset:16384\n"
        "s_mul_i32 s96, s40, 4224\n"
        "v_mfma_f32_16x16x32_bf16 a[128:131], v[64:67], v[20:23], a[128:131]\n"
        "ds_read_b64_tr_b16 v[80:81], v226 offset:24576\n"
        "s_add_u32 s96, s96, 0x20240\n"
        "v_sub_u32 v171, 2040, v167\n"
        "v_mfma_f32_16x16x32_bf16 a[160:163], v[64:67], v[28:31], 0\n"
        "ds_read_b64_tr_b16 v[82:83], v227 offset:24576\n"
        "v_lshlrev_b32 v171, 1, v171\n"
        "v_mfma_f32_16x16x32_bf16 a[160:163], v[32:35], v[24:27], a[160:163]\n"
        "ds_read_b64_tr_b16 v[52:53], v228 offset:16384\n"
        "v_lshl_add_u32 v171, v168, 12, v171\n"
        "v_mfma_f32_16x16x32_bf16 a[164:167], v[36:39], v[24:27], 0\n"
        "ds_read_b64_tr_b16 v[54:55], v229 offset:16384\n"
        "global_load_lds_dwordx4 v239, s[60:61] offset:1024\n"
        "v_mfma_f32_16x16x32_bf16 a[164:167], v[68:71], v[28:31], a[164:167]\n"
        "ds_read_b64_tr_b16 v[84:85], v228 offset:24576\n"
        "v_lshlrev_b32 v168, 3, v168\n"
        "v_mfma_f32_16x16x32_bf16 a[132:135], v[68:71], v[20:23], 0\n"
        "ds_read_b64_tr_b16 v[86:87], v229 offset:24576\n"
        "s_sub_u32 s74, s20, 2048\n"
        "s_subb_u32 s75, s21, 0\n"
        "v_mfma_f32_16x16x32_bf16 a[132:135], v[36:39], v[16:19], a[132:135]\n"
        "ds_read_b64_tr_b16 v[56:57], v230 offset:16384\n"
        "s_cmp_lt_u32 s43, 16\n"
        "s_cselect_b32 s90, s44, s45\n"
        "v_mfma_f32_16x16x32_bf16 a[136:139], v[40:43], v[16:19], 0\n"
        "ds_read_b64_tr_b16 v[58:59], v231 offset:16384\n"
        "global_load_lds_dwordx4 v240, s[60:61] offset:2048\n"
        "s_lshl4_add_u32 s90, s90, s91\n"
        "v_mfma_f32_16x16x32_bf16 a[136:139], v[72:75], v[20:23], a[136:139]\n"
        "ds_read_b64_tr_b16 v[88:89], v230 offset:24576\n"
        "s_lshl2_add_u32 s90, s90, s40\n"
        "s_lshl_b32 s90, s90, 12\n"
        "v_mfma_f32_16x16x32_bf16 a[168:171], v[72:75], v[28:31], 0\n"
        "ds_read_b64_tr_b16 v[90:91], v231 offset:24576\n"
        "s_add_u32 s92, s8, s90\n"
        "s_addc_u32 s93, s9, 0\n"
        "v_mfma_f32_16x16x32_bf16 a[168:171], v[40:43], v[24:27], a[168:171]\n"
        "ds_read_b64_tr_b16 v[60:61], v232 offset:16384\n"
        "s_lshl_b32 s91, s44, 5\n"
        "s_add_u32 s91, s91, s43\n"
        "v_mfma_f32_16x16x32_bf16 a[172:175], v[44:47], v[24:27], 0\n"
        "ds_read_b64_tr_b16 v[62:63], v233 offset:16384\n"
        "global_load_dwordx4 v[158:161], v254, s[30:31] offset:0\n"
        "s_lshl2_add_u32 s91, s91, s40\n"
        "v_mfma_f32_16x16x32_bf16 a[172:175], v[76:79], v[28:31], a[172:175]\n"
        "ds_read_b64_tr_b16 v[92:93], v232 offset:24576\n"
        "s_lshl_b32 s80, s91, 1\n"
        "s_add_u32 s81, s80, 1\n"
        "v_mfma_f32_16x16x32_bf16 a[140:143], v[76:79], v[20:23], 0\n"
        "ds_read_b64_tr_b16 v[94:95], v233 offset:24576\n"
        "s_add_u32 s76, s38, 0x3301000\n"
        "s_addc_u32 s77, s39, 0\n"
        "v_mfma_f32_16x16x32_bf16 a[140:143], v[44:47], v[16:19], a[140:143]\n"
        "s_lshl_b32 s80, s80, 7\n"
        "s_lshl_b32 s81, s81, 7\n"
        "s_waitcnt lgkmcnt(7)\n"
        "v_mfma_f32_16x16x32_bf16 a[16:19], v[0:3], v[48:51], 0\n"
        "global_load_lds_dwordx4 v241, s[60:61] offset:3072\n"
        "s_add_u32 m0, s73, 0x24000\n"
        "v_mfma_f32_16x16x32_bf16 a[16:19], v[4:7], v[80:83], a[16:19]\n"
        "s_add_u32 s60, s60, 0x80000\n"
        "s_addc_u32 s61, s61, 0\n"
        "v_mfma_f32_16x16x32_bf16 a[48:51], v[12:15], v[80:83], 0\n"
        "v_mfma_f32_16x16x32_bf16 a[48:51], v[8:11], v[48:51], a[48:51]\n"
        "v_mfma_f32_16x16x32_bf16 a[52:55], v[8:11], v[52:55], 0\n"
        "global_load_lds_dwordx4 v238, s[62:63] offset:0\n"
        "v_mfma_f32_16x16x32_bf16 a[52:55], v[12:15], v[84:87], a[52:55]\n"
        "s_waitcnt lgkmcnt(1)\n"
        "v_mfma_f32_16x16x32_bf16 a[20:23], v[4:7], v[84:87], 0\n"
        "v_mfma_f32_16x16x32_bf16 a[20:23], v[0:3], v[52:55], a[20:23]\n"
        "v_mfma_f32_16x16x32_bf16 a[24:27], v[0:3], v[56:59], 0\n"
        "global_load_dwordx4 v[162:165], v254, s[30:31] offset:1024\n"
        "v_mfma_f32_16x16x32_bf16 a[24:27], v[4:7], v[88:91], a[24:27]\n"
        "s_waitcnt lgkmcnt(0)\n"
        "v_mfma_f32_16x16x32_bf16 a[56:59], v[12:15], v[88:91], 0\n"
        "v_mfma_f32_16x16x32_bf16 a[56:59], v[8:11], v[56:59], a[56:59]\n"
        "v_mfma_f32_16x16x32_bf16 a[60:63], v[8:11], v[60:63], 0\n"
        "global_load_lds_dwordx4 v239, s[62:63] offset:1024\n"
        "v_mfma_f32_16x16x32_bf16 a[60:63], v[12:15], v[92:95], a[60:63]\n"
        "v_mfma_f32_16x16x32_bf16 a[28:31], v[4:7], v[92:95], 0\n"
        "v_mfma_f32_16x16x32_bf16 a[28:31], v[0:3], v[60:63], a[28:31]\n"
        "s_waitcnt vmcnt(26)\n"
        "v_mfma_f32_16x16x32_bf16 a[144:147], v[16:19], v[48:51], 0\n"
        "global_load_lds_dwordx4 v240, s[62:63] offset:2048\n"
        "v_mfma_f32_16x16x32_bf16 a[144:147], v[20:23], v[80:83], a[144:147]\n"
        "s_barrier\n"
        "v_mfma_f32_16x16x32_bf16 a[176:179], v[28:31], v[80:83], 0\n"
        "ds_read_b128 v[0:3], v224 offset:20480\n"
        "v_mfma_f32_16x16x32_bf16 a[176:179], v[24:27], v[48:51], a[176:179]\n"
        "ds_read_b128 v[4:7], v225 offset:20480\n"
        "v_mfma_f32_16x16x32_bf16 a[180:183], v[24:27], v[52:55], 0\n"
        "global_load_ushort v172, v172, s[28:29]\n"
        "v_mfma_f32_16x16x32_bf16 a[180:183], v[28:31], v[84:87], a[180:183]\n"
        "global_load_lds_dwordx4 v241, s[62:63] offset:3072\n"
        "s_add_u32 s62, s62, 0x80000\n"
        "s_addc_u32 s63, s63, 0\n"
        "v_mfma_f32_16x16x32_bf16 a[148:151], v[20:23], v[84:87], 0\n"
        "ds_read_b128 v[8:11], v224 offset:22528\n"
        "s_add_u32 m0, s72, 0x0\n"
        "v_mfma_f32_16x16x32_bf16 a[148:151], v[16:19], v[52:55], a[148:151]\n"
        "ds_read_b128 v[12:15], v225 offset:22528\n"
        "v_mfma_f32_16x16x32_bf16 a[152:155], v[16:19], v[56:59], 0\n"
        "v_mfma_f32_16x16x32_bf16 a[152:155], v[20:23], v[88:91], a[152:155]\n"
        "global_load_ushort v198, v198, s[26:27]\n"
        "v_mfma_f32_16x16x32_bf16 a[184:187], v[28:31], v[88:91], 0\n"
        "global_load_dwordx4 v[134:137], v168, s[24:25] offset:0\n"
        "v_mfma_f32_16x16x32_bf16 a[184:187], v[24:27], v[56:59], a[184:187]\n"
        "v_mfma_f32_16x16x32_bf16 a[188:191], v[24:27], v[60:63], 0\n"
        "ds_read_b128 v[24:27], v224 offset:6144\n"
        "s_waitcnt lgkmcnt(3)\n"
        "v_mfma_f32_16x16x32_bf16 a[188:191], v[28:31], v[92:95], a[188:191]\n"
        "ds_read_b128 v[28:31], v225 offset:6144\n"
        "v_mfma_f32_16x16x32_bf16 a[156:159], v[20:23], v[92:95], 0\n"
        "ds_read_b128 v[20:23], v225 offset:4096\n"
        "v_mfma_f32_16x16x32_bf16 a[156:159], v[16:19], v[60:63], a[156:159]\n"
        "ds_read_b128 v[16:19], v224 offset:4096\n"
        "s_waitcnt lgkmcnt(4)\n"
        "v_mfma_f32_16x16x32_bf16 a[64:67], v[32:35], v[0:3], 0\n"
        "v_mfma_f32_16x16x32_bf16 a[64:67], v[64:67], v[4:7], a[64:67]\n"
        "v_mfma_f32_16x16x32_bf16 a[96:99], v[64:67], v[12:15], 0\n"
        "v_mfma_f32_16x16x32_bf16 a[96:99], v[32:35], v[8:11], a[96:99]\n"
        "v_mfma_f32_16x16x32_bf16 a[100:103], v[36:39], v[8:11], 0\n"
        "v_mfma_f32_16x16x32_bf16 a[100:103], v[68:71], v[12:15], a[100:103]\n"
        "v_mfma_f32_16x16x32_bf16 a[68:71], v[68:71], v[4:7], 0\n"
        "v_mfma_f32_16x16x32_bf16 a[68:71], v[36:39], v[0:3], a[68:71]\n"
        "global_load_dwordx4 v[138:141], v168, s[24:25] offset:16\n"
        "v_mfma_f32_16x16x32_bf16 a[72:75], v[40:43], v[0:3], 0\n"
        "v_mfma_f32_16x16x32_bf16 a[72:75], v[72:75], v[4:7], a[72:75]\n"
        "v_mfma_f32_16x16x32_bf16 a[104:107], v[72:75], v[12:15], 0\n"
        "v_mfma_f32_16x16x32_bf16 a[104:107], v[40:43], v[8:11], a[104:107]\n"
        "v_mfma_f32_16x16x32_bf16 a[108:111], v[44:47], v[8:11], 0\n"
        "v_mfma_f32_16x16x32_bf16 a[108:111], v[76:79], v[12:15], a[108:111]\n"
        "v_mfma_f32_16x16x32_bf16 a[76:79], v[76:79], v[4:7], 0\n"
        "v_mfma_f32_16x16x32_bf16 a[76:79], v[44:47], v[0:3], a[76:79]\n"
        "v_mfma_f32_16x16x32_bf16 a[80:83], v[0:3], v[48:51], 0\n"
        "v_mfma_f32_16x16x32_bf16 a[80:83], v[4:7], v[80:83], a[80:83]\n"
        "v_mfma_f32_16x16x32_bf16 a[112:115], v[12:15], v[80:83], 0\n"
        "v_mfma_f32_16x16x32_bf16 a[112:115], v[8:11], v[48:51], a[112:115]\n"
        "v_mfma_f32_16x16x32_bf16 a[116:119], v[8:11], v[52:55], 0\n"
        "v_mfma_f32_16x16x32_bf16 a[116:119], v[12:15], v[84:87], a[116:119]\n"
        "v_mfma_f32_16x16x32_bf16 a[84:87], v[4:7], v[84:87], 0\n"
        "v_mfma_f32_16x16x32_bf16 a[84:87], v[0:3], v[52:55], a[84:87]\n"
        "global_load_dwordx2 v[96:97], v170, s[74:75] offset:-4096\n"
        "v_mfma_f32_16x16x32_bf16 a[88:91], v[0:3], v[56:59], 0\n"
        "v_mfma_f32_16x16x32_bf16 a[88:91], v[4:7], v[88:91], a[88:91]\n"
        "v_mfma_f32_16x16x32_bf16 a[120:123], v[12:15], v[88:91], 0\n"
        "v_mfma_f32_16x16x32_bf16 a[120:123], v[8:11], v[56:59], a[120:123]\n"
        "s_waitcnt vmcnt(15) lgkmcnt(0)\n"
        "v_mfma_f32_16x16x32_bf16 a[124:127], v[8:11], v[60:63], 0\n"
        "s_barrier\n"
        "v_mfma_f32_16x16x32_bf16 a[124:127], v[12:15], v[92:95], a[124:127]\n"
        "global_load_lds_dwordx4 v234, s[56:57] offset:0\n"
        "ds_read_b128 v[8:11], v224 offset:51200\n"
        "v_mfma_f32_16x16x32_bf16 a[92:95], v[4:7], v[92:95], 0\n"
        "ds_read_b128 v[4:7], v225 offset:49152\n"
        "v_mfma_f32_16x16x32_bf16 a[92:95], v[0:3], v[60:63], a[92:95]\n"
        "ds_read_b128 v[0:3], v224 offset:49152\n"
        "s_add_u32 s82, s12, 0x0\n"
        "s_addc_u32 s83, s13, 0\n"
        "v_mfma_f32_16x16x32_bf16 a[192:195], v[32:35], v[16:19], 0\n"
        "ds_read_b128 v[12:15], v225 offset:51200\n"
        "v_mfma_f32_16x16x32_bf16 a[192:195], v[64:67], v[20:23], a[192:195]\n"
        "global_load_lds_dwordx4 v235, s[56:57] offset:1024\n"
        "v_mfma_f32_16x16x32_bf16 a[224:227], v[64:67], v[28:31], 0\n"
        "ds_read_b64_tr_b16 v[64:65], v226 offset:40960\n"
        "v_mfma_f32_16x16x32_bf16 a[224:227], v[32:35], v[24:27], a[224:227]\n"
        "ds_read_b64_tr_b16 v[32:33], v226 offset:32768\n"
        "v_mfma_f32_16x16x32_bf16 a[228:231], v[36:39], v[24:27], 0\n"
        "ds_read_b64_tr_b16 v[34:35], v227 offset:32768\n"
        "v_mfma_f32_16x16x32_bf16 a[228:231], v[68:71], v[28:31], a[228:231]\n"
        "ds_read_b64_tr_b16 v[66:67], v227 offset:40960\n"
        "global_load_lds_dwordx4 v236, s[56:57] offset:2048\n"
        "v_mfma_f32_16x16x32_bf16 a[196:199], v[68:71], v[20:23], 0\n"
        "ds_read_b64_tr_b16 v[68:69], v228 offset:40960\n"
        "v_mfma_f32_16x16x32_bf16 a[196:199], v[36:39], v[16:19], a[196:199]\n"
        "ds_read_b64_tr_b16 v[36:37], v228 offset:32768\n"
        "v_mfma_f32_16x16x32_bf16 a[200:203], v[40:43], v[16:19], 0\n"
        "ds_read_b64_tr_b16 v[38:39], v229 offset:32768\n"
        "v_mfma_f32_16x16x32_bf16 a[200:203], v[72:75], v[20:23], a[200:203]\n"
        "ds_read_b64_tr_b16 v[70:71], v229 offset:40960\n"
        "global_load_lds_dwordx4 v237, s[56:57] offset:3072\n"
        "s_add_u32 m0, s72, 0x1000\n"
        "v_mfma_f32_16x16x32_bf16 a[232:235], v[72:75], v[28:31], 0\n"
        "ds_read_b64_tr_b16 v[72:73], v230 offset:40960\n"
        "s_add_u32 s56, s56, 0x80\n"
        "s_addc_u32 s57, s57, 0\n"
        "v_mfma_f32_16x16x32_bf16 a[232:235], v[40:43], v[24:27], a[232:235]\n"
        "ds_read_b64_tr_b16 v[40:41], v230 offset:32768\n"
        "v_mfma_f32_16x16x32_bf16 a[236:239], v[44:47], v[24:27], 0\n"
        "ds_read_b64_tr_b16 v[42:43], v231 offset:32768\n"
        "v_mfma_f32_16x16x32_bf16 a[236:239], v[76:79], v[28:31], a[236:239]\n"
        "ds_read_b64_tr_b16 v[74:75], v231 offset:40960\n"
        "global_load_dwordx2 v[98:99], v170, s[74:75] offset:-2048\n"
        "v_mfma_f32_16x16x32_bf16 a[204:207], v[76:79], v[20:23], 0\n"
        "ds_read_b64_tr_b16 v[76:77], v232 offset:40960\n"
        "global_load_lds_dwordx4 v234, s[58:59] offset:0\n"
        "v_mfma_f32_16x16x32_bf16 a[204:207], v[44:47], v[16:19], a[204:207]\n"
        "ds_read_b64_tr_b16 v[44:45], v232 offset:32768\n"
        "v_mfma_f32_16x16x32_bf16 a[208:211], v[16:19], v[48:51], 0\n"
        "ds_read_b64_tr_b16 v[46:47], v233 offset:32768\n"
        "v_mfma_f32_16x16x32_bf16 a[208:211], v[20:23], v[80:83], a[208:211]\n"
        "ds_read_b64_tr_b16 v[78:79], v233 offset:40960\n"
        "v_mfma_f32_16x16x32_bf16 a[240:243], v[28:31], v[80:83], 0\n"
        "ds_read_b64_tr_b16 v[80:81], v226 offset:57344\n"
        "global_load_lds_dwordx4 v235, s[58:59] offset:1024\n"
        "v_mfma_f32_16x16x32_bf16 a[240:243], v[24:27], v[48:51], a[240:243]\n"
        "ds_read_b64_tr_b16 v[48:49], v226 offset:49152\n"
        "v_mfma_f32_16x16x32_bf16 a[244:247], v[24:27], v[52:55], 0\n"
        "ds_read_b64_tr_b16 v[50:51], v227 offset:49152\n"
        "v_mfma_f32_16x16x32_bf16 a[244:247], v[28:31], v[84:87], a[244:247]\n"
        "ds_read_b64_tr_b16 v[82:83], v227 offset:57344\n"
        "v_mfma_f32_16x16x32_bf16 a[212:215], v[20:23], v[84:87], 0\n"
        "ds_read_b64_tr_b16 v[84:85], v228 offset:57344\n"
        "global_load_lds_dwordx4 v236, s[58:59] offset:2048\n"
        "v_mfma_f32_16x16x32_bf16 a[212:215], v[16:19], v[52:55], a[212:215]\n"
        "ds_read_b64_tr_b16 v[52:53], v228 offset:49152\n"
        "v_mfma_f32_16x16x32_bf16 a[216:219], v[16:19], v[56:59], 0\n"
        "ds_read_b64_tr_b16 v[54:55], v229 offset:49152\n"
        "v_mfma_f32_16x16x32_bf16 a[216:219], v[20:23], v[88:91], a[216:219]\n"
        "ds_read_b64_tr_b16 v[86:87], v229 offset:57344\n"
        "v_mfma_f32_16x16x32_bf16 a[248:251], v[28:31], v[88:91], 0\n"
        "ds_read_b64_tr_b16 v[88:89], v230 offset:57344\n"
        "global_load_lds_dwordx4 v237, s[58:59] offset:3072\n"
        "s_add_u32 m0, s73, 0x10000\n"
        "v_mfma_f32_16x16x32_bf16 a[248:251], v[24:27], v[56:59], a[248:251]\n"
        "ds_read_b64_tr_b16 v[56:57], v230 offset:49152\n"
        "s_add_u32 s58, s58, 0x80\n"
        "s_addc_u32 s59, s59, 0\n"
        "v_mfma_f32_16x16x32_bf16 a[252:255], v[24:27], v[60:63], 0\n"
        "ds_read_b64_tr_b16 v[58:59], v231 offset:49152\n"
        "s_waitcnt lgkmcnt(8)\n"
        "v_mfma_f32_16x16x32_bf16 a[252:255], v[28:31], v[92:95], a[252:255]\n"
        "ds_read_b64_tr_b16 v[90:91], v231 offset:57344\n"
        "v_mfma_f32_16x16x32_bf16 a[220:223], v[20:23], v[92:95], 0\n"
        "ds_read_b64_tr_b16 v[92:93], v232 offset:57344\n"
        "global_load_dwordx2 v[100:101], v170, s[74:75] offset:0\n"
        "v_mfma_f32_16x16x32_bf16 a[220:223], v[16:19], v[60:63], a[220:223]\n"
        "ds_read_b64_tr_b16 v[60:61], v232 offset:49152\n"
        "global_load_lds_dwordx4 v238, s[60:61] offset:0\n"
        "v_mfma_f32_16x16x32_bf16 a[0:3], v[32:35], v[0:3], a[0:3]\n"
        "ds_read_b64_tr_b16 v[62:63], v233 offset:49152\n"
        "v_mfma_f32_16x16x32_bf16 a[0:3], v[64:67], v[4:7], a[0:3]\n"
        "ds_read_b64_tr_b16 v[94:95], v233 offset:57344\n"
        "v_mfma_f32_16x16x32_bf16 a[32:35], v[64:67], v[12:15], a[32:35]\n"
        "ds_read_b128 v[16:19], v224 offset:53248\n"
        "v_mfma_f32_16x16x32_bf16 a[32:35], v[32:35], v[8:11], a[32:35]\n"
        "ds_read_b128 v[20:23], v225 offset:53248\n"
        "global_load_lds_dwordx4 v239, s[60:61] offset:1024\n"
        "v_mfma_f32_16x16x32_bf16 a[36:39], v[36:39], v[8:11], a[36:39]\n"
        "ds_read_b128 v[24:27], v224 offset:55296\n"
        "v_mfma_f32_16x16x32_bf16 a[36:39], v[68:71], v[12:15], a[36:39]\n"
        "ds_read_b128 v[28:31], v225 offset:55296\n"
        "v_mfma_f32_16x16x32_bf16 a[4:7], v[68:71], v[4:7], a[4:7]\n"
        "v_mfma_f32_16x16x32_bf16 a[4:7], v[36:39], v[0:3], a[4:7]\n"
        "global_load_lds_dwordx4 v240, s[60:61] offset:2048\n"
        "v_mfma_f32_16x16x32_bf16 a[8:11], v[40:43], v[0:3], a[8:11]\n"
        "v_mfma_f32_16x16x32_bf16 a[8:11], v[72:75], v[4:7], a[8:11]\n"
        "v_mfma_f32_16x16x32_bf16 a[40:43], v[72:75], v[12:15], a[40:43]\n"
        "v_mfma_f32_16x16x32_bf16 a[40:43], v[40:43], v[8:11], a[40:43]\n"
        "global_load_lds_dwordx4 v241, s[60:61] offset:3072\n"
        "s_add_u32 m0, s73, 0x14000\n"
        "v_mfma_f32_16x16x32_bf16 a[44:47], v[44:47], v[8:11], a[44:47]\n"
        "s_add_u32 s60, s60, 0x80000\n"
        "s_addc_u32 s61, s61, 0\n"
        "v_mfma_f32_16x16x32_bf16 a[44:47], v[76:79], v[12:15], a[44:47]\n"
        "s_waitcnt lgkmcnt(0)\n"
        "v_mfma_f32_16x16x32_bf16 a[12:15], v[76:79], v[4:7], a[12:15]\n"
        "v_mfma_f32_16x16x32_bf16 a[12:15], v[44:47], v[0:3], a[12:15]\n"
        "global_load_dwordx2 v[102:103], v169, s[74:75] offset:2048\n"
        "v_mfma_f32_16x16x32_bf16 a[16:19], v[0:3], v[48:51], a[16:19]\n"
        "global_load_lds_dwordx4 v238, s[62:63] offset:0\n"
        "s_add_u32 s74, s20, 0x1800\n"
        "s_addc_u32 s75, s21, 0\n"
        "v_mfma_f32_16x16x32_bf16 a[16:19], v[4:7], v[80:83], a[16:19]\n"
        "v_mfma_f32_16x16x32_bf16 a[48:51], v[12:15], v[80:83], a[48:51]\n"
        "v_mfma_f32_16x16x32_bf16 a[48:51], v[8:11], v[48:51], a[48:51]\n"
        "v_mfma_f32_16x16x32_bf16 a[52:55], v[8:11], v[52:55], a[52:55]\n"
        "global_load_lds_dwordx4 v239, s[62:63] offset:1024\n"
        "v_mfma_f32_16x16x32_bf16 a[52:55], v[12:15], v[84:87], a[52:55]\n"
        "v_mfma_f32_16x16x32_bf16 a[20:23], v[4:7], v[84:87], a[20:23]\n"
        "v_mfma_f32_16x16x32_bf16 a[20:23], v[0:3], v[52:55], a[20:23]\n"
        "v_mfma_f32_16x16x32_bf16 a[24:27], v[0:3], v[56:59], a[24:27]\n"
        "global_load_lds_dwordx4 v240, s[62:63] offset:2048\n"
        "v_mfma_f32_16x16x32_bf16 a[24:27], v[4:7], v[88:91], a[24:27]\n"
        "v_mfma_f32_16x16x32_bf16 a[56:59], v[12:15], v[88:91], a[56:59]\n"
        "v_mfma_f32_16x16x32_bf16 a[56:59], v[8:11], v[56:59], a[56:59]\n"
        "v_mfma_f32_16x16x32_bf16 a[60:63], v[8:11], v[60:63], a[60:63]\n"
        "global_load_lds_dwordx4 v241, s[62:63] offset:3072\n"
        "ds_read_b128 v[8:11], v224 offset:34816\n"
        "s_add_u32 m0, s72, 0x8000\n"
        "v_mfma_f32_16x16x32_bf16 a[60:63], v[12:15], v[92:95], a[60:63]\n"
        "ds_read_b128 v[12:15], v225 offset:34816\n"
        "s_add_u32 s62, s62, 0x80000\n"
        "s_addc_u32 s63, s63, 0\n"
        "v_mfma_f32_16x16x32_bf16 a[28:31], v[4:7], v[92:95], a[28:31]\n"
        "ds_read_b128 v[4:7], v225 offset:32768\n"
        "v_mfma_f32_16x16x32_bf16 a[28:31], v[0:3], v[60:63], a[28:31]\n"
        "LHA0_%=:\n"
        "s_cbranch_execz LHA4_%=\n"
        "ds_read_b128 v[0:3], v224 offset:32768\n"
        "v_mfma_f32_16x16x32_bf16 a[64:67], v[32:35], v[16:19], a[64:67]\n"
        "global_load_dwordx2 v[104:105], v169, s[74:75] offset:-4096\n"
        "v_mfma_f32_16x16x32_bf16 a[64:67], v[64:67], v[20:23], a[64:67]\n"
        "v_mfma_f32_16x16x32_bf16 a[96:99], v[64:67], v[28:31], a[96:99]\n"
        "v_mfma_f32_16x16x32_bf16 a[96:99], v[32:35], v[24:27], a[96:99]\n"
        "v_mfma_f32_16x16x32_bf16 a[100:103], v[36:39], v[24:27], a[100:103]\n"
        "v_mfma_f32_16x16x32_bf16 a[100:103], v[68:71], v[28:31], a[100:103]\n"
        "v_mfma_f32_16x16x32_bf16 a[68:71], v[68:71], v[20:23], a[68:71]\n"
        "v_mfma_f32_16x16x32_bf16 a[68:71], v[36:39], v[16:19], a[68:71]\n"
        "global_load_dwordx2 v[106:107], v169, s[74:75] offset:-2048\n"
        "v_mfma_f32_16x16x32_bf16 a[72:75], v[40:43], v[16:19], a[72:75]\n"
        "v_mfma_f32_16x16x32_bf16 a[72:75], v[72:75], v[20:23], a[72:75]\n"
        "v_mfma_f32_16x16x32_bf16 a[104:107], v[72:75], v[28:31], a[104:107]\n"
        "v_mfma_f32_16x16x32_bf16 a[104:107], v[40:43], v[24:27], a[104:107]\n"
        "v_mfma_f32_16x16x32_bf16 a[108:111], v[44:47], v[24:27], a[108:111]\n"
        "v_mfma_f32_16x16x32_bf16 a[108:111], v[76:79], v[28:31], a[108:111]\n"
        "v_mfma_f32_16x16x32_bf16 a[76:79], v[76:79], v[20:23], a[76:79]\n"
        "v_mfma_f32_16x16x32_bf16 a[76:79], v[44:47], v[16:19], a[76:79]\n"
        "v_mfma_f32_16x16x32_bf16 a[80:83], v[16:19], v[48:51], a[80:83]\n"
        "v_mfma_f32_16x16x32_bf16 a[80:83], v[20:23], v[80:83], a[80:83]\n"
        "v_mfma_f32_16x16x32_bf16 a[112:115], v[28:31], v[80:83], a[112:115]\n"
        "v_mfma_f32_16x16x32_bf16 a[112:115], v[24:27], v[48:51], a[112:115]\n"
        "v_mfma_f32_16x16x32_bf16 a[116:119], v[24:27], v[52:55], a[116:119]\n"
        "v_mfma_f32_16x16x32_bf16 a[116:119], v[28:31], v[84:87], a[116:119]\n"
        "v_mfma_f32_16x16x32_bf16 a[84:87], v[20:23], v[84:87], a[84:87]\n"
        "s_waitcnt vmcnt(24) lgkmcnt(0)\n"
        "v_lshl_or_b32 v172, v172, 16, v198\n"
        "v_mfma_f32_16x16x32_bf16 a[84:87], v[16:19], v[52:55], a[84:87]\n"
        "global_load_dwordx2 v[108:109], v169, s[74:75] offset:0\n"
        "v_readfirstlane_b32 s54, v172\n"
        "v_mfma_f32_16x16x32_bf16 a[88:91], v[16:19], v[56:59], a[88:91]\n"
        "s_waitcnt vmcnt(24)\n"
        "v_lshlrev_b32 v185, 16, v134\n"
        "v_and_b32 v186, s35, v134\n"
        "v_mfma_f32_16x16x32_bf16 a[88:91], v[20:23], v[88:91], a[88:91]\n"
        "v_lshlrev_b32 v187, 16, v135\n"
        "v_and_b32 v188, s35, v135\n"
        "v_mfma_f32_16x16x32_bf16 a[120:123], v[28:31], v[88:91], a[120:123]\n"
        "v_mfma_f32_16x16x32_bf16 a[120:123], v[24:27], v[56:59], a[120:123]\n"
        "v_mfma_f32_16x16x32_bf16 a[124:127], v[24:27], v[60:63], a[124:127]\n"
        "ds_read_b128 v[24:27], v224 offset:38912\n"
        "v_mfma_f32_16x16x32_bf16 a[124:127], v[28:31], v[92:95], a[124:127]\n"
        "ds_read_b128 v[28:31], v225 offset:38912\n"
        "v_mfma_f32_16x16x32_bf16 a[92:95], v[20:23], v[92:95], a[92:95]\n"
        "ds_read_b128 v[20:23], v225 offset:36864\n"
        "v_mfma_f32_16x16x32_bf16 a[92:95], v[16:19], v[60:63], a[92:95]\n"
        "ds_read_b128 v[16:19], v224 offset:36864\n"
        "v_mfma_f32_16x16x32_bf16 a[128:131], v[32:35], v[0:3], a[128:131]\n"
        "v_mfma_f32_16x16x32_bf16 a[128:131], v[64:67], v[4:7], a[128:131]\n"
        "v_mfma_f32_16x16x32_bf16 a[160:163], v[64:67], v[12:15], a[160:163]\n"
        "v_mfma_f32_16x16x32_bf16 a[160:163], v[32:35], v[8:11], a[160:163]\n"
        "v_mfma_f32_16x16x32_bf16 a[164:167], v[36:39], v[8:11], a[164:167]\n"
        "v_mfma_f32_16x16x32_bf16 a[164:167], v[68:71], v[12:15], a[164:167]\n"
        "v_mfma_f32_16x16x32_bf16 a[132:135], v[68:71], v[4:7], a[132:135]\n"
        "v_mfma_f32_16x16x32_bf16 a[132:135], v[36:39], v[0:3], a[132:135]\n"
        "global_load_dwordx2 v[110:111], v169, s[74:75] offset:2048\n"
        "s_add_u32 s74, s20, 0x3800\n"
        "s_addc_u32 s75, s21, 0\n"
        "v_mfma_f32_16x16x32_bf16 a[136:139], v[40:43], v[0:3], a[136:139]\n"
        "v_mfma_f32_16x16x32_bf16 a[136:139], v[72:75], v[4:7], a[136:139]\n"
        "v_mfma_f32_16x16x32_bf16 a[168:171], v[72:75], v[12:15], a[168:171]\n"
        "v_mfma_f32_16x16x32_bf16 a[168:171], v[40:43], v[8:11], a[168:171]\n"
        "v_mfma_f32_16x16x32_bf16 a[172:175], v[44:47], v[8:11], a[172:175]\n"
        "v_mfma_f32_16x16x32_bf16 a[172:175], v[76:79], v[12:15], a[172:175]\n"
        "v_mfma_f32_16x16x32_bf16 a[140:143], v[76:79], v[4:7], a[140:143]\n"
        "v_mfma_f32_16x16x32_bf16 a[140:143], v[44:47], v[0:3], a[140:143]\n"
        "v_mfma_f32_16x16x32_bf16 a[144:147], v[0:3], v[48:51], a[144:147]\n"
        "v_mfma_f32_16x16x32_bf16 a[144:147], v[4:7], v[80:83], a[144:147]\n"
        "v_mfma_f32_16x16x32_bf16 a[176:179], v[12:15], v[80:83], a[176:179]\n"
        "v_mfma_f32_16x16x32_bf16 a[176:179], v[8:11], v[48:51], a[176:179]\n"
        "v_mfma_f32_16x16x32_bf16 a[180:183], v[8:11], v[52:55], a[180:183]\n"
        "s_waitcnt vmcnt(23) lgkmcnt(0)\n"
        "v_cndmask_b32 v96, v96, 0, s[78:79]\n"
        "v_cndmask_b32 v97, v97, 0, s[78:79]\n"
        "v_mfma_f32_16x16x32_bf16 a[180:183], v[12:15], v[84:87], a[180:183]\n"
        "v_lshlrev_b32 v174, 16, v96\n"
        "v_mul_f32 v189, v174, v188\n"
        "v_mfma_f32_16x16x32_bf16 a[148:151], v[4:7], v[84:87], a[148:151]\n"
        "v_cvt_pk_bf16_f32 v189, 0, v189\n"
        "v_and_b32 v174, s35, v96\n"
        "v_mfma_f32_16x16x32_bf16 a[148:151], v[0:3], v[52:55], a[148:151]\n"
        "global_load_dwordx2 v[112:113], v169, s[74:75] offset:-4096\n"
        "v_mfma_f32_16x16x32_bf16 a[152:155], v[0:3], v[56:59], a[152:155]\n"
        "v_mfma_f32_16x16x32_bf16 a[152:155], v[4:7], v[88:91], a[152:155]\n"
        "v_mfma_f32_16x16x32_bf16 a[184:187], v[12:15], v[88:91], a[184:187]\n"
        "v_mfma_f32_16x16x32_bf16 a[184:187], v[8:11], v[56:59], a[184:187]\n"
        "s_waitcnt vmcnt(15)\n"
        "v_mfma_f32_16x16x32_bf16 a[188:191], v[8:11], v[60:63], a[188:191]\n"
        "s_barrier\n"
        "v_mfma_f32_16x16x32_bf16 a[188:191], v[12:15], v[92:95], a[188:191]\n"
        "global_load_lds_dwordx4 v234, s[56:57] offset:0\n"
        "ds_read_b128 v[8:11], v224 offset:18432\n"
        "v_mfma_f32_16x16x32_bf16 a[156:159], v[4:7], v[92:95], a[156:159]\n"
        "ds_read_b128 v[4:7], v225 offset:16384\n"
        "v_mfma_f32_16x16x32_bf16 a[156:159], v[0:3], v[60:63], a[156:159]\n"
        "ds_read_b128 v[0:3], v224 offset:16384\n"
        "v_mfma_f32_16x16x32_bf16 a[192:195], v[32:35], v[16:19], a[192:195]\n"
        "ds_read_b128 v[12:15], v225 offset:18432\n"
        "v_mfma_f32_16x16x32_bf16 a[192:195], v[64:67], v[20:23], a[192:195]\n"
        "global_load_lds_dwordx4 v235, s[56:57] offset:1024\n"
        "v_mfma_f32_16x16x32_bf16 a[224:227], v[64:67], v[28:31], a[224:227]\n"
        "ds_read_b64_tr_b16 v[64:65], v216 offset:8192\n"
        "v_mfma_f32_16x16x32_bf16 a[224:227], v[32:35], v[24:27], a[224:227]\n"
        "ds_read_b64_tr_b16 v[32:33], v216 offset:0\n"
        "v_mfma_f32_16x16x32_bf16 a[228:231], v[36:39], v[24:27], a[228:231]\n"
        "ds_read_b64_tr_b16 v[34:35], v217 offset:0\n"
        "v_mfma_f32_16x16x32_bf16 a[228:231], v[68:71], v[28:31], a[228:231]\n"
        "ds_read_b64_tr_b16 v[66:67], v217 offset:8192\n"
        "global_load_lds_dwordx4 v236, s[56:57] offset:2048\n"
        "v_mfma_f32_16x16x32_bf16 a[196:199], v[68:71], v[20:23], a[196:199]\n"
        "ds_read_b64_tr_b16 v[68:69], v218 offset:8192\n"
        "v_mfma_f32_16x16x32_bf16 a[196:199], v[36:39], v[16:19], a[196:199]\n"
        "ds_read_b64_tr_b16 v[36:37], v218 offset:0\n"
        "v_mfma_f32_16x16x32_bf16 a[200:203], v[40:43], v[16:19], a[200:203]\n"
        "ds_read_b64_tr_b16 v[38:39], v219 offset:0\n"
        "v_mfma_f32_16x16x32_bf16 a[200:203], v[72:75], v[20:23], a[200:203]\n"
        "ds_read_b64_tr_b16 v[70:71], v219 offset:8192\n"
        "global_load_lds_dwordx4 v237, s[56:57] offset:3072\n"
        "s_add_u32 m0, s72, 0x9000\n"
        "v_mfma_f32_16x16x32_bf16 a[232:235], v[72:75], v[28:31], a[232:235]\n"
        "ds_read_b64_tr_b16 v[72:73], v220 offset:8192\n"
        "s_add_u32 s56, s56, 0x80\n"
        "s_addc_u32 s57, s57, 0\n"
        "v_mfma_f32_16x16x32_bf16 a[232:235], v[40:43], v[24:27], a[232:235]\n"
        "ds_read_b64_tr_b16 v[40:41], v220 offset:0\n"
        "v_cndmask_b32 v98, v98, 0, s[78:79]\n"
        "v_mfma_f32_16x16x32_bf16 a[236:239], v[44:47], v[24:27], a[236:239]\n"
        "ds_read_b64_tr_b16 v[42:43], v221 offset:0\n"
        "v_cndmask_b32 v99, v99, 0, s[78:79]\n"
        "v_mfma_f32_16x16x32_bf16 a[236:239], v[76:79], v[28:31], a[236:239]\n"
        "ds_read_b64_tr_b16 v[74:75], v221 offset:8192\n"
        "global_load_dwordx2 v[114:115], v169, s[74:75] offset:-2048\n"
        "v_mfma_f32_16x16x32_bf16 a[204:207], v[76:79], v[20:23], a[204:207]\n"
        "ds_read_b64_tr_b16 v[76:77], v222 offset:8192\n"
        "global_load_lds_dwordx4 v234, s[58:59] offset:0\n"
        "v_mfma_f32_16x16x32_bf16 a[204:207], v[44:47], v[16:19], a[204:207]\n"
        "ds_read_b64_tr_b16 v[44:45], v222 offset:0\n"
        "v_lshlrev_b32 v175, 16, v98\n"
        "v_mfma_f32_16x16x32_bf16 a[208:211], v[16:19], v[48:51], a[208:211]\n"
        "ds_read_b64_tr_b16 v[46:47], v223 offset:0\n"
        "v_mul_f32 v191, v175, v188\n"
        "v_mfma_f32_16x16x32_bf16 a[208:211], v[20:23], v[80:83], a[208:211]\n"
        "ds_read_b64_tr_b16 v[78:79], v223 offset:8192\n"
        "v_cvt_pk_bf16_f32 v191, 0, v191\n"
        "v_mfma_f32_16x16x32_bf16 a[240:243], v[28:31], v[80:83], a[240:243]\n"
        "ds_read_b64_tr_b16 v[80:81], v216 offset:24576\n"
        "global_load_lds_dwordx4 v235, s[58:59] offset:1024\n"
        "v_mfma_f32_16x16x32_bf16 a[240:243], v[24:27], v[48:51], a[240:243]\n"
        "ds_read_b64_tr_b16 v[48:49], v216 offset:16384\n"
        "v_mul_f32 v190, v175, v187\n"
        "v_mfma_f32_16x16x32_bf16 a[244:247], v[24:27], v[52:55], a[244:247]\n"
        "ds_read_b64_tr_b16 v[50:51], v217 offset:16384\n"
        "v_cvt_pk_bf16_f32 v190, 0, v190\n"
        "v_mfma_f32_16x16x32_bf16 a[244:247], v[28:31], v[84:87], a[244:247]\n"
        "ds_read_b64_tr_b16 v[82:83], v217 offset:24576\n"
        "v_add_f32 v189, v189, v190\n"
        "v_mfma_f32_16x16x32_bf16 a[212:215], v[20:23], v[84:87], a[212:215]\n"
        "ds_read_b64_tr_b16 v[84:85], v218 offset:24576\n"
        "global_load_lds_dwordx4 v236, s[58:59] offset:2048\n"
        "v_mfma_f32_16x16x32_bf16 a[212:215], v[16:19], v[52:55], a[212:215]\n"
        "ds_read_b64_tr_b16 v[52:53], v218 offset:16384\n"
        "v_cvt_pk_bf16_f32 v189, 0, v189\n"
        "v_mfma_f32_16x16x32_bf16 a[216:219], v[16:19], v[56:59], a[216:219]\n"
        "ds_read_b64_tr_b16 v[54:55], v219 offset:16384\n"
        "v_and_b32 v175, s35, v98\n"
        "v_mfma_f32_16x16x32_bf16 a[216:219], v[20:23], v[88:91], a[216:219]\n"
        "ds_read_b64_tr_b16 v[86:87], v219 offset:24576\n"
        "v_mfma_f32_16x16x32_bf16 a[248:251], v[28:31], v[88:91], a[248:251]\n"
        "ds_read_b64_tr_b16 v[88:89], v220 offset:24576\n"
        "global_load_lds_dwordx4 v237, s[58:59] offset:3072\n"
        "s_add_u32 m0, s73, 0x18000\n"
        "v_mfma_f32_16x16x32_bf16 a[248:251], v[24:27], v[56:59], a[248:251]\n"
        "ds_read_b64_tr_b16 v[56:57], v220 offset:16384\n"
        "s_add_u32 s58, s58, 0x80\n"
        "s_addc_u32 s59, s59, 0\n"
        "v_mfma_f32_16x16x32_bf16 a[252:255], v[24:27], v[60:63], a[252:255]\n"
        "ds_read_b64_tr_b16 v[58:59], v221 offset:16384\n"
        "s_waitcnt lgkmcnt(8)\n"
        "v_mfma_f32_16x16x32_bf16 a[252:255], v[28:31], v[92:95], a[252:255]\n"
        "ds_read_b64_tr_b16 v[90:91], v221 offset:24576\n"
        "s_waitcnt vmcnt(23) lgkmcnt(8)\n"
        "v_mfma_f32_16x16x32_bf16 a[220:223], v[20:23], v[92:95], a[220:223]\n"
        "ds_read_b64_tr_b16 v[92:93], v222 offset:24576\n"
        "global_load_dwordx2 v[116:117], v169, s[74:75] offset:0\n"
        "v_mfma_f32_16x16x32_bf16 a[220:223], v[16:19], v[60:63], a[220:223]\n"
        "ds_read_b64_tr_b16 v[60:61], v222 offset:16384\n"
        "global_load_lds_dwordx4 v238, s[60:61] offset:0\n"
        "v_mfma_f32_16x16x32_bf16 a[0:3], v[32:35], v[0:3], a[0:3]\n"
        "ds_read_b64_tr_b16 v[62:63], v223 offset:16384\n"
        "s_add_u32 s74, s22, 0x1000\n"
        "s_addc_u32 s75, s23, 0\n"
        "v_mfma_f32_16x16x32_bf16 a[0:3], v[64:67], v[4:7], a[0:3]\n"
        "ds_read_b64_tr_b16 v[94:95], v223 offset:24576\n"
        "v_cndmask_b32 v100, v100, 0, s[78:79]\n"
        "v_mfma_f32_16x16x32_bf16 a[32:35], v[64:67], v[12:15], a[32:35]\n"
        "ds_read_b128 v[16:19], v224 offset:20480\n"
        "v_cndmask_b32 v101, v101, 0, s[78:79]\n"
        "v_mfma_f32_16x16x32_bf16 a[32:35], v[32:35], v[8:11], a[32:35]\n"
        "ds_read_b128 v[20:23], v225 offset:20480\n"
        "global_load_lds_dwordx4 v239, s[60:61] offset:1024\n"
        "v_mfma_f32_16x16x32_bf16 a[36:39], v[36:39], v[8:11], a[36:39]\n"
        "ds_read_b128 v[24:27], v224 offset:22528\n"
        "v_lshlrev_b32 v176, 16, v100\n"
        "v_mfma_f32_16x16x32_bf16 a[36:39], v[68:71], v[12:15], a[36:39]\n"
        "ds_read_b128 v[28:31], v225 offset:22528\n"
        "v_mul_f32 v192, v176, v187\n"
        "v_mfma_f32_16x16x32_bf16 a[4:7], v[68:71], v[4:7], a[4:7]\n"
        "v_cvt_pk_bf16_f32 v192, 0, v192\n"
        "v_add_f32 v191, v191, v192\n"
        "v_mfma_f32_16x16x32_bf16 a[4:7], v[36:39], v[0:3], a[4:7]\n"
        "global_load_lds_dwordx4 v240, s[60:61] offset:2048\n"
        "v_mfma_f32_16x16x32_bf16 a[8:11], v[40:43], v[0:3], a[8:11]\n"
        "v_cvt_pk_bf16_f32 v191, 0, v191\n"
        "v_mul_f32 v190, v176, v186\n"
        "v_mfma_f32_16x16x32_bf16 a[8:11], v[72:75], v[4:7], a[8:11]\n"
        "v_cvt_pk_bf16_f32 v190, 0, v190\n"
        "v_add_f32 v189, v189, v190\n"
        "v_mfma_f32_16x16x32_bf16 a[40:43], v[72:75], v[12:15], a[40:43]\n"
        "v_cvt_pk_bf16_f32 v189, 0, v189\n"
        "v_mul_f32 v193, v176, v188\n"
        "v_mfma_f32_16x16x32_bf16 a[40:43], v[40:43], v[8:11], a[40:43]\n"
        "global_load_lds_dwordx4 v241, s[60:61] offset:3072\n"
        "s_add_u32 m0, s73, 0x1c000\n"
        "v_mfma_f32_16x16x32_bf16 a[44:47], v[44:47], v[8:11], a[44:47]\n"
        "s_add_u32 s60, s60, 0x80000\n"
        "s_addc_u32 s61, s61, 0\n"
        "v_cvt_pk_bf16_f32 v193, 0, v193\n"
        "v_mfma_f32_16x16x32_bf16 a[44:47], v[76:79], v[12:15], a[44:47]\n"
        "s_waitcnt vmcnt(23) lgkmcnt(0)\n"
        "v_lshlrev_b32 v177, 16, v102\n"
        "v_mul_f32 v192, v177, v186\n"
        "v_mfma_f32_16x16x32_bf16 a[12:15], v[76:79], v[4:7], a[12:15]\n"
        "v_cvt_pk_bf16_f32 v192, 0, v192\n"
        "v_add_f32 v191, v191, v192\n"
        "v_mfma_f32_16x16x32_bf16 a[12:15], v[44:47], v[0:3], a[12:15]\n"
        "global_load_dwordx2 v[118:119], v169, s[74:75] offset:-4096\n"
        "v_mfma_f32_16x16x32_bf16 a[16:19], v[0:3], v[48:51], a[16:19]\n"
        "global_load_lds_dwordx4 v238, s[62:63] offset:0\n"
        "v_cvt_pk_bf16_f32 v191, 0, v191\n"
        "v_mfma_f32_16x16x32_bf16 a[16:19], v[4:7], v[80:83], a[16:19]\n"
        "v_mul_f32 v190, v177, v185\n"
        "v_cvt_pk_bf16_f32 v190, 0, v190\n"
        "v_mfma_f32_16x16x32_bf16 a[48:51], v[12:15], v[80:83], a[48:51]\n"
        "v_add_f32 v189, v189, v190\n"
        "v_cvt_pk_bf16_f32 v189, 0, v189\n"
        "v_mfma_f32_16x16x32_bf16 a[48:51], v[8:11], v[48:51], a[48:51]\n"
        "v_mul_f32 v195, v177, v188\n"
        "v_cvt_pk_bf16_f32 v195, 0, v195\n"
        "v_mfma_f32_16x16x32_bf16 a[52:55], v[8:11], v[52:55], a[52:55]\n"
        "global_load_lds_dwordx4 v239, s[62:63] offset:1024\n"
        "v_mul_f32 v194, v177, v187\n"
        "v_mfma_f32_16x16x32_bf16 a[52:55], v[12:15], v[84:87], a[52:55]\n"
        "v_cvt_pk_bf16_f32 v194, 0, v194\n"
        "v_add_f32 v193, v193, v194\n"
        "v_mfma_f32_16x16x32_bf16 a[20:23], v[4:7], v[84:87], a[20:23]\n"
        "v_cvt_pk_bf16_f32 v193, 0, v193\n"
        "v_and_b32 v176, s35, v100\n"
        "v_mfma_f32_16x16x32_bf16 a[20:23], v[0:3], v[52:55], a[20:23]\n"
        "v_and_b32 v177, s35, v102\n"
        "v_mfma_f32_16x16x32_bf16 a[24:27], v[0:3], v[56:59], a[24:27]\n"
        "global_load_lds_dwordx4 v240, s[62:63] offset:2048\n"
        "v_mfma_f32_16x16x32_bf16 a[24:27], v[4:7], v[88:91], a[24:27]\n"
        "v_mfma_f32_16x16x32_bf16 a[56:59], v[12:15], v[88:91], a[56:59]\n"
        "v_mfma_f32_16x16x32_bf16 a[56:59], v[8:11], v[56:59], a[56:59]\n"
        "v_mfma_f32_16x16x32_bf16 a[60:63], v[8:11], v[60:63], a[60:63]\n"
        "global_load_lds_dwordx4 v241, s[62:63] offset:3072\n"
        "ds_read_b128 v[8:11], v224 offset:2048\n"
        "s_add_u32 m0, s72, 0x0\n"
        "v_mfma_f32_16x16x32_bf16 a[60:63], v[12:15], v[92:95], a[60:63]\n"
        "ds_read_b128 v[12:15], v225 offset:2048\n"
        "s_add_u32 s62, s62, 0x80000\n"
        "s_addc_u32 s63, s63, 0\n"
        "v_mfma_f32_16x16x32_bf16 a[28:31], v[4:7], v[92:95], a[28:31]\n"
        "ds_read_b128 v[4:7], v225 offset:0\n"
        "s_waitcnt vmcnt(23)\n"
        "v_mfma_f32_16x16x32_bf16 a[28:31], v[0:3], v[60:63], a[28:31]\n"
        "ds_read_b128 v[0:3], v224 offset:0\n"
        "v_lshlrev_b32 v178, 16, v104\n"
        "v_mfma_f32_16x16x32_bf16 a[64:67], v[32:35], v[16:19], a[64:67]\n"
        "global_load_dwordx2 v[120:121], v169, s[74:75] offset:-2048\n"
        "v_mul_f32 v192, v178, v185\n"
        "v_mfma_f32_16x16x32_bf16 a[64:67], v[64:67], v[20:23], a[64:67]\n"
        "v_cvt_pk_bf16_f32 v192, 0, v192\n"
        "v_add_f32 v191, v191, v192\n"
        "v_mfma_f32_16x16x32_bf16 a[96:99], v[64:67], v[28:31], a[96:99]\n"
        "v_cvt_pk_bf16_f32 v191, 0, v191\n"
        "v_mul_f32 v196, v178, v187\n"
        "v_mfma_f32_16x16x32_bf16 a[96:99], v[32:35], v[24:27], a[96:99]\n"
        "v_cvt_pk_bf16_f32 v196, 0, v196\n"
        "v_add_f32 v195, v195, v196\n"
        "v_mfma_f32_16x16x32_bf16 a[100:103], v[36:39], v[24:27], a[100:103]\n"
        "s_waitcnt vmcnt(23)\n"
        "v_lshlrev_b32 v179, 16, v106\n"
        "v_cvt_pk_bf16_f32 v195, 0, v195\n"
        "v_mfma_f32_16x16x32_bf16 a[100:103], v[68:71], v[28:31], a[100:103]\n"
        "v_mul_f32 v194, v178, v186\n"
        "v_mul_f32 v196, v179, v186\n"
        "v_mfma_f32_16x16x32_bf16 a[68:71], v[68:71], v[20:23], a[68:71]\n"
        "v_cvt_pk_bf16_f32 v194, 0, v194\n"
        "v_cvt_pk_bf16_f32 v196, 0, v196\n"
        "v_mfma_f32_16x16x32_bf16 a[68:71], v[36:39], v[16:19], a[68:71]\n"
        "global_load_dword v253, v173, s[82:83] offset:0\n"
        "s_add_u32 s82, s12, 0x100000\n"
        "s_addc_u32 s83, s13, 0\n"
        "v_mfma_f32_16x16x32_bf16 a[72:75], v[40:43], v[16:19], a[72:75]\n"
        "global_load_dwordx2 v[122:123], v169, s[74:75] offset:0\n"
        "v_add_f32 v193, v193, v194\n"
        "v_mfma_f32_16x16x32_bf16 a[72:75], v[72:75], v[20:23], a[72:75]\n"
        "v_add_f32 v195, v195, v196\n"
        "v_cvt_pk_bf16_f32 v193, 0, v193\n"
        "v_mfma_f32_16x16x32_bf16 a[104:107], v[72:75], v[28:31], a[104:107]\n"
        "v_cvt_pk_bf16_f32 v195, 0, v195\n"
        "v_mul_f32 v194, v179, v185\n"
        "v_mfma_f32_16x16x32_bf16 a[104:107], v[40:43], v[24:27], a[104:107]\n"
        "v_cvt_pk_bf16_f32 v194, 0, v194\n"
        "v_add_f32 v193, v193, v194\n"
        "v_mfma_f32_16x16x32_bf16 a[108:111], v[44:47], v[24:27], a[108:111]\n"
        "v_cvt_pk_bf16_f32 v193, 0, v193\n"
        "v_mfma_f32_16x16x32_bf16 a[108:111], v[76:79], v[28:31], a[108:111]\n"
        "v_mfma_f32_16x16x32_bf16 a[76:79], v[76:79], v[20:23], a[76:79]\n"
        "v_mfma_f32_16x16x32_bf16 a[76:79], v[44:47], v[16:19], a[76:79]\n"
        "v_mfma_f32_16x16x32_bf16 a[80:83], v[16:19], v[48:51], a[80:83]\n"
        "v_mfma_f32_16x16x32_bf16 a[80:83], v[20:23], v[80:83], a[80:83]\n"
        "v_mfma_f32_16x16x32_bf16 a[112:115], v[28:31], v[80:83], a[112:115]\n"
        "v_mfma_f32_16x16x32_bf16 a[112:115], v[24:27], v[48:51], a[112:115]\n"
        "v_mfma_f32_16x16x32_bf16 a[116:119], v[24:27], v[52:55], a[116:119]\n"
        "s_waitcnt vmcnt(24) lgkmcnt(0)\n"
        "v_lshlrev_b32 v180, 16, v108\n"
        "v_mul_f32 v196, v180, v185\n"
        "v_mfma_f32_16x16x32_bf16 a[116:119], v[28:31], v[84:87], a[116:119]\n"
        "v_cvt_pk_bf16_f32 v196, 0, v196\n"
        "v_add_f32 v195, v195, v196\n"
        "v_mfma_f32_16x16x32_bf16 a[84:87], v[20:23], v[84:87], a[84:87]\n"
        "v_cvt_pk_bf16_f32 v195, 0, v195\n"
        "v_mfma_f32_16x16x32_bf16 a[84:87], v[16:19], v[52:55], a[84:87]\n"
        "global_load_dwordx2 v[124:125], v169, s[74:75] offset:2048\n"
        "s_add_u32 s74, s22, 0x3000\n"
        "s_addc_u32 s75, s23, 0\n"
        "v_mfma_f32_16x16x32_bf16 a[88:91], v[16:19], v[56:59], a[88:91]\n"
        "v_mfma_f32_16x16x32_bf16 a[88:91], v[20:23], v[88:91], a[88:91]\n"
        "v_mfma_f32_16x16x32_bf16 a[120:123], v[28:31], v[88:91], a[120:123]\n"
        "v_mfma_f32_16x16x32_bf16 a[120:123], v[24:27], v[56:59], a[120:123]\n"
        "v_mfma_f32_16x16x32_bf16 a[124:127], v[24:27], v[60:63], a[124:127]\n"
        "ds_read_b128 v[24:27], v224 offset:6144\n"
        "v_mfma_f32_16x16x32_bf16 a[124:127], v[28:31], v[92:95], a[124:127]\n"
        "ds_read_b128 v[28:31], v225 offset:6144\n"
        "v_mfma_f32_16x16x32_bf16 a[92:95], v[20:23], v[92:95], a[92:95]\n"
        "ds_read_b128 v[20:23], v225 offset:4096\n"
        "v_mfma_f32_16x16x32_bf16 a[92:95], v[16:19], v[60:63], a[92:95]\n"
        "ds_read_b128 v[16:19], v224 offset:4096\n"
        "v_mfma_f32_16x16x32_bf16 a[128:131], v[32:35], v[0:3], a[128:131]\n"
        "v_mfma_f32_16x16x32_bf16 a[128:131], v[64:67], v[4:7], a[128:131]\n"
        "v_mfma_f32_16x16x32_bf16 a[160:163], v[64:67], v[12:15], a[160:163]\n"
        "v_mfma_f32_16x16x32_bf16 a[160:163], v[32:35], v[8:11], a[160:163]\n"
        "v_mfma_f32_16x16x32_bf16 a[164:167], v[36:39], v[8:11], a[164:167]\n"
        "s_waitcnt vmcnt(24) lgkmcnt(3)\n"
        "v_lshlrev_b32 v181, 16, v110\n"
        "v_mfma_f32_16x16x32_bf16 a[164:167], v[68:71], v[12:15], a[164:167]\n"
        "v_mfma_f32_16x16x32_bf16 a[132:135], v[68:71], v[4:7], a[132:135]\n"
        "v_mfma_f32_16x16x32_bf16 a[132:135], v[36:39], v[0:3], a[132:135]\n"
        "global_load_dwordx2 v[126:127], v169, s[74:75] offset:-4096\n"
        "v_mfma_f32_16x16x32_bf16 a[136:139], v[40:43], v[0:3], a[136:139]\n"
        "v_mfma_f32_16x16x32_bf16 a[136:139], v[72:75], v[4:7], a[136:139]\n"
        "v_mfma_f32_16x16x32_bf16 a[168:171], v[72:75], v[12:15], a[168:171]\n"
        "v_mfma_f32_16x16x32_bf16 a[168:171], v[40:43], v[8:11], a[168:171]\n"
        "v_mfma_f32_16x16x32_bf16 a[172:175], v[44:47], v[8:11], a[172:175]\n"
        "v_mfma_f32_16x16x32_bf16 a[172:175], v[76:79], v[12:15], a[172:175]\n"
        "v_mfma_f32_16x16x32_bf16 a[140:143], v[76:79], v[4:7], a[140:143]\n"
        "v_mfma_f32_16x16x32_bf16 a[140:143], v[44:47], v[0:3], a[140:143]\n"
        "v_mfma_f32_16x16x32_bf16 a[144:147], v[0:3], v[48:51], a[144:147]\n"
        "v_mfma_f32_16x16x32_bf16 a[144:147], v[4:7], v[80:83], a[144:147]\n"
        "v_mfma_f32_16x16x32_bf16 a[176:179], v[12:15], v[80:83], a[176:179]\n"
        "v_mfma_f32_16x16x32_bf16 a[176:179], v[8:11], v[48:51], a[176:179]\n"
        "v_mfma_f32_16x16x32_bf16 a[180:183], v[8:11], v[52:55], a[180:183]\n"
        "s_waitcnt vmcnt(24) lgkmcnt(0)\n"
        "v_lshlrev_b32 v182, 16, v112\n"
        "v_mfma_f32_16x16x32_bf16 a[180:183], v[12:15], v[84:87], a[180:183]\n"
        "v_mfma_f32_16x16x32_bf16 a[148:151], v[4:7], v[84:87], a[148:151]\n"
        "v_mfma_f32_16x16x32_bf16 a[148:151], v[0:3], v[52:55], a[148:151]\n"
        "global_load_dwordx2 v[128:129], v169, s[74:75] offset:-2048\n"
        "v_mfma_f32_16x16x32_bf16 a[152:155], v[0:3], v[56:59], a[152:155]\n"
        "v_mfma_f32_16x16x32_bf16 a[152:155], v[4:7], v[88:91], a[152:155]\n"
        "v_mfma_f32_16x16x32_bf16 a[184:187], v[12:15], v[88:91], a[184:187]\n"
        "v_mfma_f32_16x16x32_bf16 a[184:187], v[8:11], v[56:59], a[184:187]\n"
        "s_waitcnt vmcnt(16)\n"
        "v_mfma_f32_16x16x32_bf16 a[188:191], v[8:11], v[60:63], a[188:191]\n"
        "s_barrier\n"
        "v_mfma_f32_16x16x32_bf16 a[188:191], v[12:15], v[92:95], a[188:191]\n"
        "global_load_lds_dwordx4 v234, s[56:57] offset:0\n"
        "ds_read_b128 v[8:11], v224 offset:51200\n"
        "v_mfma_f32_16x16x32_bf16 a[156:159], v[4:7], v[92:95], a[156:159]\n"
        "ds_read_b128 v[4:7], v225 offset:49152\n"
        "v_mfma_f32_16x16x32_bf16 a[156:159], v[0:3], v[60:63], a[156:159]\n"
        "ds_read_b128 v[0:3], v224 offset:49152\n"
        "v_mfma_f32_16x16x32_bf16 a[192:195], v[32:35], v[16:19], a[192:195]\n"
        "ds_read_b128 v[12:15], v225 offset:51200\n"
        "v_mfma_f32_16x16x32_bf16 a[192:195], v[64:67], v[20:23], a[192:195]\n"
        "global_load_lds_dwordx4 v235, s[56:57] offset:1024\n"
        "v_mfma_f32_16x16x32_bf16 a[224:227], v[64:67], v[28:31], a[224:227]\n"
        "ds_read_b64_tr_b16 v[64:65], v226 offset:8192\n"
        "v_mfma_f32_16x16x32_bf16 a[224:227], v[32:35], v[24:27], a[224:227]\n"
        "ds_read_b64_tr_b16 v[32:33], v226 offset:0\n"
        "v_mfma_f32_16x16x32_bf16 a[228:231], v[36:39], v[24:27], a[228:231]\n"
        "ds_read_b64_tr_b16 v[34:35], v227 offset:0\n"
        "v_mfma_f32_16x16x32_bf16 a[228:231], v[68:71], v[28:31], a[228:231]\n"
        "ds_read_b64_tr_b16 v[66:67], v227 offset:8192\n"
        "global_load_lds_dwordx4 v236, s[56:57] offset:2048\n"
        "v_mfma_f32_16x16x32_bf16 a[196:199], v[68:71], v[20:23], a[196:199]\n"
        "ds_read_b64_tr_b16 v[68:69], v228 offset:8192\n"
        "v_mfma_f32_16x16x32_bf16 a[196:199], v[36:39], v[16:19], a[196:199]\n"
        "ds_read_b64_tr_b16 v[36:37], v228 offset:0\n"
        "v_mfma_f32_16x16x32_bf16 a[200:203], v[40:43], v[16:19], a[200:203]\n"
        "ds_read_b64_tr_b16 v[38:39], v229 offset:0\n"
        "v_mfma_f32_16x16x32_bf16 a[200:203], v[72:75], v[20:23], a[200:203]\n"
        "ds_read_b64_tr_b16 v[70:71], v229 offset:8192\n"
        "global_load_lds_dwordx4 v237, s[56:57] offset:3072\n"
        "s_add_u32 m0, s72, 0x1000\n"
        "v_mfma_f32_16x16x32_bf16 a[232:235], v[72:75], v[28:31], a[232:235]\n"
        "ds_read_b64_tr_b16 v[72:73], v230 offset:8192\n"
        "s_add_u32 s56, s56, 0x80\n"
        "s_addc_u32 s57, s57, 0\n"
        "v_mfma_f32_16x16x32_bf16 a[232:235], v[40:43], v[24:27], a[232:235]\n"
        "ds_read_b64_tr_b16 v[40:41], v230 offset:0\n"
        "v_lshlrev_b32 v183, 16, v114\n"
        "v_mfma_f32_16x16x32_bf16 a[236:239], v[44:47], v[24:27], a[236:239]\n"
        "ds_read_b64_tr_b16 v[42:43], v231 offset:0\n"
        "v_mfma_f32_16x16x32_bf16 a[236:239], v[76:79], v[28:31], a[236:239]\n"
        "ds_read_b64_tr_b16 v[74:75], v231 offset:8192\n"
        "global_load_dwordx2 v[130:131], v169, s[74:75] offset:0\n"
        "v_mfma_f32_16x16x32_bf16 a[204:207], v[76:79], v[20:23], a[204:207]\n"
        "ds_read_b64_tr_b16 v[76:77], v232 offset:8192\n"
        "global_load_lds_dwordx4 v234, s[58:59] offset:0\n"
        "v_mfma_f32_16x16x32_bf16 a[204:207], v[44:47], v[16:19], a[204:207]\n"
        "ds_read_b64_tr_b16 v[44:45], v232 offset:0\n"
        "v_mfma_f32_16x16x32_bf16 a[208:211], v[16:19], v[48:51], a[208:211]\n"
        "ds_read_b64_tr_b16 v[46:47], v233 offset:0\n"
        "v_mfma_f32_16x16x32_bf16 a[208:211], v[20:23], v[80:83], a[208:211]\n"
        "ds_read_b64_tr_b16 v[78:79], v233 offset:8192\n"
        "v_mfma_f32_16x16x32_bf16 a[240:243], v[28:31], v[80:83], a[240:243]\n"
        "ds_read_b64_tr_b16 v[80:81], v226 offset:24576\n"
        "global_load_lds_dwordx4 v235, s[58:59] offset:1024\n"
        "v_mfma_f32_16x16x32_bf16 a[240:243], v[24:27], v[48:51], a[240:243]\n"
        "ds_read_b64_tr_b16 v[48:49], v226 offset:16384\n"
        "v_mfma_f32_16x16x32_bf16 a[244:247], v[24:27], v[52:55], a[244:247]\n"
        "ds_read_b64_tr_b16 v[50:51], v227 offset:16384\n"
        "v_mfma_f32_16x16x32_bf16 a[244:247], v[28:31], v[84:87], a[244:247]\n"
        "ds_read_b64_tr_b16 v[82:83], v227 offset:24576\n"
        "v_mfma_f32_16x16x32_bf16 a[212:215], v[20:23], v[84:87], a[212:215]\n"
        "ds_read_b64_tr_b16 v[84:85], v228 offset:24576\n"
        "global_load_lds_dwordx4 v236, s[58:59] offset:2048\n"
        "v_mfma_f32_16x16x32_bf16 a[212:215], v[16:19], v[52:55], a[212:215]\n"
        "ds_read_b64_tr_b16 v[52:53], v228 offset:16384\n"
        "v_mfma_f32_16x16x32_bf16 a[216:219], v[16:19], v[56:59], a[216:219]\n"
        "ds_read_b64_tr_b16 v[54:55], v229 offset:16384\n"
        "v_mfma_f32_16x16x32_bf16 a[216:219], v[20:23], v[88:91], a[216:219]\n"
        "ds_read_b64_tr_b16 v[86:87], v229 offset:24576\n"
        "v_mfma_f32_16x16x32_bf16 a[248:251], v[28:31], v[88:91], a[248:251]\n"
        "global_load_dword v253, v173, s[82:83] offset:0\n"
        "v_mfma_f32_16x16x32_bf16 a[248:251], v[24:27], v[56:59], a[248:251]\n"
        "ds_read_b64_tr_b16 v[56:57], v230 offset:16384\n"
        "global_load_lds_dwordx4 v237, s[58:59] offset:3072\n"
        "s_add_u32 m0, s73, 0x20000\n"
        "v_mfma_f32_16x16x32_bf16 a[252:255], v[24:27], v[60:63], a[252:255]\n"
        "ds_read_b64_tr_b16 v[58:59], v231 offset:16384\n"
        "s_add_u32 s58, s58, 0x80\n"
        "s_addc_u32 s59, s59, 0\n"
        "v_mfma_f32_16x16x32_bf16 a[252:255], v[28:31], v[92:95], a[252:255]\n"
        "ds_read_b64_tr_b16 v[88:89], v230 offset:24576\n"
        "s_add_u32 s82, s12, 0x200000\n"
        "s_addc_u32 s83, s13, 0\n"
        "v_mfma_f32_16x16x32_bf16 a[220:223], v[20:23], v[92:95], a[220:223]\n"
        "ds_read_b64_tr_b16 v[90:91], v231 offset:24576\n"
        "s_waitcnt lgkmcnt(7)\n"
        "v_mfma_f32_16x16x32_bf16 a[220:223], v[16:19], v[60:63], a[220:223]\n"
        "ds_read_b64_tr_b16 v[60:61], v232 offset:16384\n"
        "global_load_dwordx2 v[132:133], v169, s[74:75] offset:2048\n"
        "v_mfma_f32_16x16x32_bf16 a[0:3], v[32:35], v[0:3], a[0:3]\n"
        "ds_read_b64_tr_b16 v[62:63], v233 offset:16384\n"
        "global_load_lds_dwordx4 v238, s[60:61] offset:0\n"
        "v_mfma_f32_16x16x32_bf16 a[0:3], v[64:67], v[4:7], a[0:3]\n"
        "ds_read_b64_tr_b16 v[92:93], v232 offset:24576\n"
        "s_waitcnt vmcnt(27) lgkmcnt(7)\n"
        "v_mfma_f32_16x16x32_bf16 a[32:35], v[64:67], v[12:15], a[32:35]\n"
        "ds_read_b64_tr_b16 v[94:95], v233 offset:24576\n"
        "v_lshlrev_b32 v184, 16, v116\n"
        "v_mfma_f32_16x16x32_bf16 a[32:35], v[32:35], v[8:11], a[32:35]\n"
        "ds_read_b128 v[16:19], v224 offset:53248\n"
        "v_mfma_f32_16x16x32_bf16 a[36:39], v[36:39], v[8:11], a[36:39]\n"
        "ds_read_b128 v[20:23], v225 offset:53248\n"
        "global_load_lds_dwordx4 v239, s[60:61] offset:1024\n"
        "v_mfma_f32_16x16x32_bf16 a[36:39], v[68:71], v[12:15], a[36:39]\n"
        "ds_read_b128 v[24:27], v224 offset:55296\n"
        "v_mfma_f32_16x16x32_bf16 a[4:7], v[68:71], v[4:7], a[4:7]\n"
        "ds_read_b128 v[28:31], v225 offset:55296\n"
        "v_mfma_f32_16x16x32_bf16 a[4:7], v[36:39], v[0:3], a[4:7]\n"
        "v_mfma_f32_16x16x32_bf16 a[8:11], v[40:43], v[0:3], a[8:11]\n"
        "global_load_lds_dwordx4 v240, s[60:61] offset:2048\n"
        "v_mfma_f32_16x16x32_bf16 a[8:11], v[72:75], v[4:7], a[8:11]\n"
        "v_mfma_f32_16x16x32_bf16 a[40:43], v[72:75], v[12:15], a[40:43]\n"
        "v_mfma_f32_16x16x32_bf16 a[40:43], v[40:43], v[8:11], a[40:43]\n"
        "v_mfma_f32_16x16x32_bf16 a[44:47], v[44:47], v[8:11], a[44:47]\n"
        "global_load_lds_dwordx4 v241, s[60:61] offset:3072\n"
        "s_add_u32 m0, s73, 0x24000\n"
        "v_mfma_f32_16x16x32_bf16 a[44:47], v[76:79], v[12:15], a[44:47]\n"
        "s_add_u32 s60, s60, 0x80000\n"
        "s_addc_u32 s61, s61, 0\n"
        "s_waitcnt vmcnt(25) lgkmcnt(1)\n"
        "v_mfma_f32_16x16x32_bf16 a[12:15], v[76:79], v[4:7], a[12:15]\n"
        "v_lshlrev_b32 v190, 16, v118\n"
        "v_mul_f32 v189, v189, v190\n"
        "v_mfma_f32_16x16x32_bf16 a[12:15], v[44:47], v[0:3], a[12:15]\n"
        "v_mul_f32 v190, v179, v187\n"
        "v_cvt_pk_bf16_f32 v190, 0, v190\n"
        "v_mfma_f32_16x16x32_bf16 a[16:19], v[0:3], v[48:51], a[16:19]\n"
        "global_load_lds_dwordx4 v238, s[62:63] offset:0\n"
        "v_mfma_f32_16x16x32_bf16 a[16:19], v[4:7], v[80:83], a[16:19]\n"
        "v_mfma_f32_16x16x32_bf16 a[48:51], v[12:15], v[80:83], a[48:51]\n"
        "v_mfma_f32_16x16x32_bf16 a[48:51], v[8:11], v[48:51], a[48:51]\n"
        "v_mfma_f32_16x16x32_bf16 a[52:55], v[8:11], v[52:55], a[52:55]\n"
        "global_load_lds_dwordx4 v239, s[62:63] offset:1024\n"
        "v_mfma_f32_16x16x32_bf16 a[52:55], v[12:15], v[84:87], a[52:55]\n"
        "v_mfma_f32_16x16x32_bf16 a[20:23], v[4:7], v[84:87], a[20:23]\n"
        "v_mfma_f32_16x16x32_bf16 a[20:23], v[0:3], v[52:55], a[20:23]\n"
        "v_mfma_f32_16x16x32_bf16 a[24:27], v[0:3], v[56:59], a[24:27]\n"
        "global_load_lds_dwordx4 v240, s[62:63] offset:2048\n"
        "v_mfma_f32_16x16x32_bf16 a[24:27], v[4:7], v[88:91], a[24:27]\n"
        "v_mfma_f32_16x16x32_bf16 a[56:59], v[12:15], v[88:91], a[56:59]\n"
        "v_mfma_f32_16x16x32_bf16 a[56:59], v[8:11], v[56:59], a[56:59]\n"
        "v_mfma_f32_16x16x32_bf16 a[60:63], v[8:11], v[60:63], a[60:63]\n"
        "global_load_lds_dwordx4 v241, s[62:63] offset:3072\n"
        "s_add_u32 m0, s72, 0x8000\n"
        "v_mfma_f32_16x16x32_bf16 a[60:63], v[12:15], v[92:95], a[60:63]\n"
        "ds_read_b128 v[8:11], v224 offset:34816\n"
        "s_add_u32 s62, s62, 0x80000\n"
        "s_addc_u32 s63, s63, 0\n"
        "v_mfma_f32_16x16x32_bf16 a[28:31], v[4:7], v[92:95], a[28:31]\n"
        "ds_read_b128 v[4:7], v225 offset:32768\n"
        "s_waitcnt lgkmcnt(2)\n"
        "v_mfma_f32_16x16x32_bf16 a[28:31], v[0:3], v[60:63], a[28:31]\n"
        "ds_read_b128 v[0:3], v224 offset:32768\n"
        "s_waitcnt vmcnt(24)\n"
        "v_mfma_f32_16x16x32_bf16 a[64:67], v[32:35], v[16:19], a[64:67]\n"
        "ds_read_b128 v[12:15], v225 offset:34816\n"
        "global_load_dwordx4 v[142:145], v254, s[92:93] offset:0\n"
        "v_mfma_f32_16x16x32_bf16 a[64:67], v[64:67], v[20:23], a[64:67]\n"
        "v_lshlrev_b32 v192, 16, v120\n"
        "v_mul_f32 v191, v191, v192\n"
        "v_mfma_f32_16x16x32_bf16 a[96:99], v[64:67], v[28:31], a[96:99]\n"
        "v_cvt_pk_bf16_f32 v203, v191, v189\n"
        "v_mul_f32 v189, v178, v188\n"
        "v_mfma_f32_16x16x32_bf16 a[96:99], v[32:35], v[24:27], a[96:99]\n"
        "v_mul_f32 v191, v179, v188\n"
        "v_cvt_pk_bf16_f32 v189, 0, v189\n"
        "v_mfma_f32_16x16x32_bf16 a[100:103], v[36:39], v[24:27], a[100:103]\n"
        "v_cvt_pk_bf16_f32 v191, 0, v191\n"
        "v_mul_f32 v192, v180, v187\n"
        "v_mfma_f32_16x16x32_bf16 a[100:103], v[68:71], v[28:31], a[100:103]\n"
        "s_waitcnt vmcnt(23) lgkmcnt(3)\n"
        "v_lshlrev_b32 v194, 16, v122\n"
        "v_mul_f32 v193, v193, v194\n"
        "v_mfma_f32_16x16x32_bf16 a[68:71], v[68:71], v[20:23], a[68:71]\n"
        "LHA1_%=:\n"
        "s_cbranch_execz LHA5_%=\n"
        "v_cvt_pk_bf16_f32 v192, 0, v192\n"
        "v_add_f32 v189, v189, v190\n"
        "v_mfma_f32_16x16x32_bf16 a[68:71], v[36:39], v[16:19], a[68:71]\n"
        "v_add_f32 v191, v191, v192\n"
        "v_cvt_pk_bf16_f32 v189, 0, v189\n"
        "v_mfma_f32_16x16x32_bf16 a[72:75], v[40:43], v[16:19], a[72:75]\n"
        "v_cvt_pk_bf16_f32 v191, 0, v191\n"
        "v_mul_f32 v190, v180, v186\n"
        "v_mfma_f32_16x16x32_bf16 a[72:75], v[72:75], v[20:23], a[72:75]\n"
        "v_mul_f32 v192, v181, v186\n"
        "v_cvt_pk_bf16_f32 v190, 0, v190\n"
        "v_mfma_f32_16x16x32_bf16 a[104:107], v[72:75], v[28:31], a[104:107]\n"
        "v_cvt_pk_bf16_f32 v192, 0, v192\n"
        "v_add_f32 v189, v189, v190\n"
        "v_mfma_f32_16x16x32_bf16 a[104:107], v[40:43], v[24:27], a[104:107]\n"
        "v_add_f32 v191, v191, v192\n"
        "v_cvt_pk_bf16_f32 v189, 0, v189\n"
        "v_mfma_f32_16x16x32_bf16 a[108:111], v[44:47], v[24:27], a[108:111]\n"
        "v_cvt_pk_bf16_f32 v191, 0, v191\n"
        "v_mul_f32 v190, v181, v185\n"
        "v_mfma_f32_16x16x32_bf16 a[108:111], v[76:79], v[28:31], a[108:111]\n"
        "v_mul_f32 v192, v182, v185\n"
        "v_cvt_pk_bf16_f32 v190, 0, v190\n"
        "v_mfma_f32_16x16x32_bf16 a[76:79], v[76:79], v[20:23], a[76:79]\n"
        "v_cvt_pk_bf16_f32 v192, 0, v192\n"
        "v_add_f32 v189, v189, v190\n"
        "v_mfma_f32_16x16x32_bf16 a[76:79], v[44:47], v[16:19], a[76:79]\n"
        "v_add_f32 v191, v191, v192\n"
        "v_cvt_pk_bf16_f32 v189, 0, v189\n"
        "v_mfma_f32_16x16x32_bf16 a[80:83], v[16:19], v[48:51], a[80:83]\n"
        "v_cvt_pk_bf16_f32 v191, 0, v191\n"
        "v_mul_f32 v194, v181, v187\n"
        "v_mfma_f32_16x16x32_bf16 a[80:83], v[20:23], v[80:83], a[80:83]\n"
        "v_cvt_pk_bf16_f32 v194, 0, v194\n"
        "v_and_b32 v178, s35, v104\n"
        "v_mfma_f32_16x16x32_bf16 a[112:115], v[28:31], v[80:83], a[112:115]\n"
        "v_and_b32 v179, s35, v106\n"
        "v_mfma_f32_16x16x32_bf16 a[112:115], v[24:27], v[48:51], a[112:115]\n"
        "v_mfma_f32_16x16x32_bf16 a[116:119], v[24:27], v[52:55], a[116:119]\n"
        "s_waitcnt vmcnt(22) lgkmcnt(0)\n"
        "v_lshlrev_b32 v196, 16, v124\n"
        "v_mul_f32 v195, v195, v196\n"
        "v_mfma_f32_16x16x32_bf16 a[116:119], v[28:31], v[84:87], a[116:119]\n"
        "v_cvt_pk_bf16_f32 v202, v195, v193\n"
        "v_mul_f32 v193, v180, v188\n"
        "v_mfma_f32_16x16x32_bf16 a[84:87], v[20:23], v[84:87], a[84:87]\n"
        "v_mul_f32 v195, v181, v188\n"
        "v_cvt_pk_bf16_f32 v193, 0, v193\n"
        "v_mfma_f32_16x16x32_bf16 a[84:87], v[16:19], v[52:55], a[84:87]\n"
        "v_cvt_pk_bf16_f32 v195, 0, v195\n"
        "v_mul_f32 v196, v182, v187\n"
        "v_mfma_f32_16x16x32_bf16 a[88:91], v[16:19], v[56:59], a[88:91]\n"
        "v_cvt_pk_bf16_f32 v196, 0, v196\n"
        "v_add_f32 v193, v193, v194\n"
        "v_mfma_f32_16x16x32_bf16 a[88:91], v[20:23], v[88:91], a[88:91]\n"
        "v_add_f32 v195, v195, v196\n"
        "v_cvt_pk_bf16_f32 v193, 0, v193\n"
        "v_mfma_f32_16x16x32_bf16 a[120:123], v[28:31], v[88:91], a[120:123]\n"
        "v_cvt_pk_bf16_f32 v195, 0, v195\n"
        "v_mul_f32 v194, v182, v186\n"
        "v_mfma_f32_16x16x32_bf16 a[120:123], v[24:27], v[56:59], a[120:123]\n"
        "v_mul_f32 v196, v183, v186\n"
        "v_cvt_pk_bf16_f32 v194, 0, v194\n"
        "v_mfma_f32_16x16x32_bf16 a[124:127], v[24:27], v[60:63], a[124:127]\n"
        "ds_read_b128 v[24:27], v224 offset:38912\n"
        "v_cvt_pk_bf16_f32 v196, 0, v196\n"
        "v_mfma_f32_16x16x32_bf16 a[124:127], v[28:31], v[92:95], a[124:127]\n"
        "ds_read_b128 v[28:31], v225 offset:38912\n"
        "v_add_f32 v193, v193, v194\n"
        "v_mfma_f32_16x16x32_bf16 a[92:95], v[20:23], v[92:95], a[92:95]\n"
        "ds_read_b128 v[20:23], v225 offset:36864\n"
        "v_add_f32 v195, v195, v196\n"
        "v_mfma_f32_16x16x32_bf16 a[92:95], v[16:19], v[60:63], a[92:95]\n"
        "ds_read_b128 v[16:19], v224 offset:36864\n"
        "v_cvt_pk_bf16_f32 v193, 0, v193\n"
        "v_mfma_f32_16x16x32_bf16 a[128:131], v[32:35], v[0:3], a[128:131]\n"
        "v_cvt_pk_bf16_f32 v195, 0, v195\n"
        "v_mul_f32 v194, v183, v185\n"
        "v_mfma_f32_16x16x32_bf16 a[128:131], v[64:67], v[4:7], a[128:131]\n"
        "v_mul_f32 v196, v184, v185\n"
        "v_cvt_pk_bf16_f32 v194, 0, v194\n"
        "v_mfma_f32_16x16x32_bf16 a[160:163], v[64:67], v[12:15], a[160:163]\n"
        "v_cvt_pk_bf16_f32 v196, 0, v196\n"
        "v_add_f32 v193, v193, v194\n"
        "v_mfma_f32_16x16x32_bf16 a[160:163], v[32:35], v[8:11], a[160:163]\n"
        "v_add_f32 v195, v195, v196\n"
        "v_cvt_pk_bf16_f32 v193, 0, v193\n"
        "v_mfma_f32_16x16x32_bf16 a[164:167], v[36:39], v[8:11], a[164:167]\n"
        "s_waitcnt vmcnt(21) lgkmcnt(3)\n"
        "v_lshlrev_b32 v190, 16, v126\n"
        "v_mul_f32 v189, v189, v190\n"
        "v_mfma_f32_16x16x32_bf16 a[164:167], v[68:71], v[12:15], a[164:167]\n"
        "v_cvt_pk_bf16_f32 v195, 0, v195\n"
        "v_lshlrev_b32 v185, 16, v136\n"
        "v_mfma_f32_16x16x32_bf16 a[132:135], v[68:71], v[4:7], a[132:135]\n"
        "v_and_b32 v186, s35, v136\n"
        "v_lshlrev_b32 v187, 16, v137\n"
        "v_mfma_f32_16x16x32_bf16 a[132:135], v[36:39], v[0:3], a[132:135]\n"
        "global_load_dword v253, v173, s[82:83] offset:0\n"
        "s_add_u32 s82, s12, 0x300000\n"
        "s_addc_u32 s83, s13, 0\n"
        "v_mfma_f32_16x16x32_bf16 a[136:139], v[40:43], v[0:3], a[136:139]\n"
        "global_load_dwordx4 v[146:149], v254, s[92:93] offset:1024\n"
        "v_and_b32 v188, s35, v137\n"
        "v_mfma_f32_16x16x32_bf16 a[136:139], v[72:75], v[4:7], a[136:139]\n"
        "v_and_b32 v180, s35, v108\n"
        "v_and_b32 v181, s35, v110\n"
        "v_mfma_f32_16x16x32_bf16 a[168:171], v[72:75], v[12:15], a[168:171]\n"
        "v_and_b32 v182, s35, v112\n"
        "v_and_b32 v183, s35, v114\n"
        "v_mfma_f32_16x16x32_bf16 a[168:171], v[40:43], v[8:11], a[168:171]\n"
        "v_and_b32 v184, s35, v116\n"
        "v_mul_f32 v190, v175, v187\n"
        "v_mfma_f32_16x16x32_bf16 a[172:175], v[44:47], v[8:11], a[172:175]\n"
        "v_cvt_pk_bf16_f32 v190, 0, v190\n"
        "v_mfma_f32_16x16x32_bf16 a[172:175], v[76:79], v[12:15], a[172:175]\n"
        "v_mfma_f32_16x16x32_bf16 a[140:143], v[76:79], v[4:7], a[140:143]\n"
        "v_mfma_f32_16x16x32_bf16 a[140:143], v[44:47], v[0:3], a[140:143]\n"
        "v_mfma_f32_16x16x32_bf16 a[144:147], v[0:3], v[48:51], a[144:147]\n"
        "v_mfma_f32_16x16x32_bf16 a[144:147], v[4:7], v[80:83], a[144:147]\n"
        "v_mfma_f32_16x16x32_bf16 a[176:179], v[12:15], v[80:83], a[176:179]\n"
        "v_mfma_f32_16x16x32_bf16 a[176:179], v[8:11], v[48:51], a[176:179]\n"
        "v_mfma_f32_16x16x32_bf16 a[180:183], v[8:11], v[52:55], a[180:183]\n"
        "s_waitcnt vmcnt(22) lgkmcnt(0)\n"
        "v_lshlrev_b32 v192, 16, v128\n"
        "v_mul_f32 v191, v191, v192\n"
        "v_mfma_f32_16x16x32_bf16 a[180:183], v[12:15], v[84:87], a[180:183]\n"
        "v_cvt_pk_bf16_f32 v201, v191, v189\n"
        "v_mul_f32 v189, v174, v188\n"
        "v_mfma_f32_16x16x32_bf16 a[148:151], v[4:7], v[84:87], a[148:151]\n"
        "v_mul_f32 v191, v175, v188\n"
        "v_cvt_pk_bf16_f32 v189, 0, v189\n"
        "v_mfma_f32_16x16x32_bf16 a[148:151], v[0:3], v[52:55], a[148:151]\n"
        "v_cvt_pk_bf16_f32 v191, 0, v191\n"
        "v_mul_f32 v192, v176, v187\n"
        "v_mfma_f32_16x16x32_bf16 a[152:155], v[0:3], v[56:59], a[152:155]\n"
        "v_cvt_pk_bf16_f32 v192, 0, v192\n"
        "v_add_f32 v189, v189, v190\n"
        "v_mfma_f32_16x16x32_bf16 a[152:155], v[4:7], v[88:91], a[152:155]\n"
        "v_add_f32 v191, v191, v192\n"
        "v_cvt_pk_bf16_f32 v189, 0, v189\n"
        "v_mfma_f32_16x16x32_bf16 a[184:187], v[12:15], v[88:91], a[184:187]\n"
        "v_cvt_pk_bf16_f32 v191, 0, v191\n"
        "v_mul_f32 v190, v176, v186\n"
        "v_mfma_f32_16x16x32_bf16 a[184:187], v[8:11], v[56:59], a[184:187]\n"
        "s_waitcnt vmcnt(12)\n"
        "v_mul_f32 v192, v177, v186\n"
        "v_cvt_pk_bf16_f32 v190, 0, v190\n"
        "v_mfma_f32_16x16x32_bf16 a[188:191], v[8:11], v[60:63], a[188:191]\n"
        "s_barrier\n"
        "v_mfma_f32_16x16x32_bf16 a[188:191], v[12:15], v[92:95], a[188:191]\n"
        "global_load_lds_dwordx4 v234, s[56:57] offset:0\n"
        "ds_read_b128 v[8:11], v224 offset:18432\n"
        "v_mfma_f32_16x16x32_bf16 a[156:159], v[4:7], v[92:95], a[156:159]\n"
        "ds_read_b128 v[4:7], v225 offset:16384\n"
        "v_cvt_pk_bf16_f32 v192, 0, v192\n"
        "v_mfma_f32_16x16x32_bf16 a[156:159], v[0:3], v[60:63], a[156:159]\n"
        "ds_read_b128 v[0:3], v224 offset:16384\n"
        "v_add_f32 v189, v189, v190\n"
        "v_mfma_f32_16x16x32_bf16 a[192:195], v[32:35], v[16:19], a[192:195]\n"
        "ds_read_b128 v[12:15], v225 offset:18432\n"
        "v_add_f32 v191, v191, v192\n"
        "v_mfma_f32_16x16x32_bf16 a[192:195], v[64:67], v[20:23], a[192:195]\n"
        "global_load_lds_dwordx4 v235, s[56:57] offset:1024\n"
        "v_cvt_pk_bf16_f32 v189, 0, v189\n"
        "v_mfma_f32_16x16x32_bf16 a[224:227], v[64:67], v[28:31], a[224:227]\n"
        "ds_read_b64_tr_b16 v[64:65], v226 offset:40960\n"
        "v_cvt_pk_bf16_f32 v191, 0, v191\n"
        "v_mfma_f32_16x16x32_bf16 a[224:227], v[32:35], v[24:27], a[224:227]\n"
        "ds_read_b64_tr_b16 v[32:33], v226 offset:32768\n"
        "v_mul_f32 v190, v177, v185\n"
        "v_mfma_f32_16x16x32_bf16 a[228:231], v[36:39], v[24:27], a[228:231]\n"
        "ds_read_b64_tr_b16 v[34:35], v227 offset:32768\n"
        "v_mul_f32 v192, v178, v185\n"
        "v_mfma_f32_16x16x32_bf16 a[228:231], v[68:71], v[28:31], a[228:231]\n"
        "ds_read_b64_tr_b16 v[66:67], v227 offset:40960\n"
        "global_load_lds_dwordx4 v236, s[56:57] offset:2048\n"
        "v_mfma_f32_16x16x32_bf16 a[196:199], v[68:71], v[20:23], a[196:199]\n"
        "ds_read_b64_tr_b16 v[68:69], v228 offset:40960\n"
        "v_cvt_pk_bf16_f32 v190, 0, v190\n"
        "v_mfma_f32_16x16x32_bf16 a[196:199], v[36:39], v[16:19], a[196:199]\n"
        "ds_read_b64_tr_b16 v[36:37], v228 offset:32768\n"
        "v_cvt_pk_bf16_f32 v192, 0, v192\n"
        "v_mfma_f32_16x16x32_bf16 a[200:203], v[40:43], v[16:19], a[200:203]\n"
        "ds_read_b64_tr_b16 v[38:39], v229 offset:32768\n"
        "v_add_f32 v189, v189, v190\n"
        "v_mfma_f32_16x16x32_bf16 a[200:203], v[72:75], v[20:23], a[200:203]\n"
        "ds_read_b64_tr_b16 v[70:71], v229 offset:40960\n"
        "global_load_lds_dwordx4 v237, s[56:57] offset:3072\n"
        "s_add_u32 m0, s72, 0x9000\n"
        "v_mfma_f32_16x16x32_bf16 a[232:235], v[72:75], v[28:31], a[232:235]\n"
        "ds_read_b64_tr_b16 v[72:73], v230 offset:40960\n"
        "s_add_u32 s56, s56, 0x80\n"
        "s_addc_u32 s57, s57, 0\n"
        "v_mfma_f32_16x16x32_bf16 a[232:235], v[40:43], v[24:27], a[232:235]\n"
        "ds_read_b64_tr_b16 v[40:41], v230 offset:32768\n"
        "v_lshlrev_b32 v194, 16, v130\n"
        "v_mfma_f32_16x16x32_bf16 a[236:239], v[44:47], v[24:27], a[236:239]\n"
        "ds_read_b64_tr_b16 v[42:43], v231 offset:32768\n"
        "v_mul_f32 v193, v193, v194\n"
        "v_mfma_f32_16x16x32_bf16 a[236:239], v[76:79], v[28:31], a[236:239]\n"
        "ds_read_b64_tr_b16 v[74:75], v231 offset:40960\n"
        "global_load_lds_dwordx4 v234, s[58:59] offset:0\n"
        "v_mfma_f32_16x16x32_bf16 a[204:207], v[76:79], v[20:23], a[204:207]\n"
        "ds_read_b64_tr_b16 v[76:77], v232 offset:40960\n"
        "v_add_f32 v191, v191, v192\n"
        "v_mfma_f32_16x16x32_bf16 a[204:207], v[44:47], v[16:19], a[204:207]\n"
        "ds_read_b64_tr_b16 v[44:45], v232 offset:32768\n"
        "v_cvt_pk_bf16_f32 v189, 0, v189\n"
        "v_mfma_f32_16x16x32_bf16 a[208:211], v[16:19], v[48:51], a[208:211]\n"
        "ds_read_b64_tr_b16 v[46:47], v233 offset:32768\n"
        "v_cvt_pk_bf16_f32 v191, 0, v191\n"
        "v_mfma_f32_16x16x32_bf16 a[208:211], v[20:23], v[80:83], a[208:211]\n"
        "ds_read_b64_tr_b16 v[78:79], v233 offset:40960\n"
        "global_load_lds_dwordx4 v235, s[58:59] offset:1024\n"
        "v_mfma_f32_16x16x32_bf16 a[240:243], v[28:31], v[80:83], a[240:243]\n"
        "ds_read_b64_tr_b16 v[80:81], v226 offset:57344\n"
        "v_and_b32 v190, s35, v118\n"
        "v_mfma_f32_16x16x32_bf16 a[240:243], v[24:27], v[48:51], a[240:243]\n"
        "ds_read_b64_tr_b16 v[48:49], v226 offset:49152\n"
        "v_and_b32 v192, s35, v120\n"
        "v_mfma_f32_16x16x32_bf16 a[244:247], v[24:27], v[52:55], a[244:247]\n"
        "ds_read_b64_tr_b16 v[50:51], v227 offset:49152\n"
        "v_mul_f32 v189, v189, v190\n"
        "v_mfma_f32_16x16x32_bf16 a[244:247], v[28:31], v[84:87], a[244:247]\n"
        "ds_read_b64_tr_b16 v[82:83], v227 offset:57344\n"
        "global_load_lds_dwordx4 v236, s[58:59] offset:2048\n"
        "v_mfma_f32_16x16x32_bf16 a[212:215], v[20:23], v[84:87], a[212:215]\n"
        "ds_read_b64_tr_b16 v[84:85], v228 offset:57344\n"
        "v_mul_f32 v191, v191, v192\n"
        "v_mfma_f32_16x16x32_bf16 a[212:215], v[16:19], v[52:55], a[212:215]\n"
        "ds_read_b64_tr_b16 v[52:53], v228 offset:49152\n"
        "v_cvt_pk_bf16_f32 v211, v191, v189\n"
        "v_mfma_f32_16x16x32_bf16 a[216:219], v[16:19], v[56:59], a[216:219]\n"
        "ds_read_b64_tr_b16 v[54:55], v229 offset:49152\n"
        "v_mul_f32 v194, v177, v187\n"
        "v_mfma_f32_16x16x32_bf16 a[216:219], v[20:23], v[88:91], a[216:219]\n"
        "ds_read_b64_tr_b16 v[86:87], v229 offset:57344\n"
        "global_load_lds_dwordx4 v237, s[58:59] offset:3072\n"
        "s_add_u32 m0, s73, 0x10000\n"
        "v_mfma_f32_16x16x32_bf16 a[248:251], v[28:31], v[88:91], a[248:251]\n"
        "ds_read_b64_tr_b16 v[88:89], v230 offset:57344\n"
        "s_add_u32 s58, s58, 0x80\n"
        "s_addc_u32 s59, s59, 0\n"
        "v_mfma_f32_16x16x32_bf16 a[248:251], v[24:27], v[56:59], a[248:251]\n"
        "ds_read_b64_tr_b16 v[56:57], v230 offset:49152\n"
        "v_cvt_pk_bf16_f32 v194, 0, v194\n"
        "v_mfma_f32_16x16x32_bf16 a[252:255], v[24:27], v[60:63], a[252:255]\n"
        "ds_read_b64_tr_b16 v[58:59], v231 offset:49152\n"
        "v_mul_f32 v189, v178, v188\n"
        "v_mfma_f32_16x16x32_bf16 a[252:255], v[28:31], v[92:95], a[252:255]\n"
        "ds_read_b64_tr_b16 v[90:91], v231 offset:57344\n"
        "global_load_dwordx4 v[150:153], v254, s[92:93] offset:2048\n"
        "v_mfma_f32_16x16x32_bf16 a[220:223], v[20:23], v[92:95], a[220:223]\n"
        "ds_read_b64_tr_b16 v[92:93], v232 offset:57344\n"
        "v_mul_f32 v191, v179, v188\n"
        "v_mfma_f32_16x16x32_bf16 a[220:223], v[16:19], v[60:63], a[220:223]\n"
        "ds_read_b64_tr_b16 v[60:61], v232 offset:49152\n"
        "s_waitcnt lgkmcnt(8)\n"
        "v_mfma_f32_16x16x32_bf16 a[0:3], v[32:35], v[0:3], a[0:3]\n"
        "ds_read_b64_tr_b16 v[62:63], v233 offset:49152\n"
        "v_cvt_pk_bf16_f32 v189, 0, v189\n"
        "v_mfma_f32_16x16x32_bf16 a[0:3], v[64:67], v[4:7], a[0:3]\n"
        "ds_read_b64_tr_b16 v[94:95], v233 offset:57344\n"
        "global_load_lds_dwordx4 v238, s[60:61] offset:0\n"
        "v_mfma_f32_16x16x32_bf16 a[32:35], v[64:67], v[12:15], a[32:35]\n"
        "ds_read_b128 v[16:19], v224 offset:20480\n"
        "v_cvt_pk_bf16_f32 v191, 0, v191\n"
        "v_mfma_f32_16x16x32_bf16 a[32:35], v[32:35], v[8:11], a[32:35]\n"
        "ds_read_b128 v[20:23], v225 offset:20480\n"
        "v_mul_f32 v190, v179, v187\n"
        "v_mfma_f32_16x16x32_bf16 a[36:39], v[36:39], v[8:11], a[36:39]\n"
        "ds_read_b128 v[24:27], v224 offset:22528\n"
        "v_mul_f32 v192, v180, v187\n"
        "v_mfma_f32_16x16x32_bf16 a[36:39], v[68:71], v[12:15], a[36:39]\n"
        "ds_read_b128 v[28:31], v225 offset:22528\n"
        "global_load_lds_dwordx4 v239, s[60:61] offset:1024\n"
        "v_mfma_f32_16x16x32_bf16 a[4:7], v[68:71], v[4:7], a[4:7]\n"
        "s_waitcnt vmcnt(22) lgkmcnt(7)\n"
        "v_lshlrev_b32 v196, 16, v132\n"
        "v_mul_f32 v195, v195, v196\n"
        "v_mfma_f32_16x16x32_bf16 a[4:7], v[36:39], v[0:3], a[4:7]\n"
        "v_cvt_pk_bf16_f32 v200, v195, v193\n"
        "v_mul_f32 v193, v176, v188\n"
        "v_mfma_f32_16x16x32_bf16 a[8:11], v[40:43], v[0:3], a[8:11]\n"
        "v_mul_f32 v195, v177, v188\n"
        "v_cvt_pk_bf16_f32 v193, 0, v193\n"
        "v_mfma_f32_16x16x32_bf16 a[8:11], v[72:75], v[4:7], a[8:11]\n"
        "global_load_lds_dwordx4 v240, s[60:61] offset:2048\n"
        "v_mfma_f32_16x16x32_bf16 a[40:43], v[72:75], v[12:15], a[40:43]\n"
        "v_cvt_pk_bf16_f32 v195, 0, v195\n"
        "v_mul_f32 v196, v178, v187\n"
        "v_mfma_f32_16x16x32_bf16 a[40:43], v[40:43], v[8:11], a[40:43]\n"
        "v_cvt_pk_bf16_f32 v196, 0, v196\n"
        "v_add_f32 v193, v193, v194\n"
        "v_mfma_f32_16x16x32_bf16 a[44:47], v[44:47], v[8:11], a[44:47]\n"
        "v_add_f32 v195, v195, v196\n"
        "v_cvt_pk_bf16_f32 v193, 0, v193\n"
        "v_mfma_f32_16x16x32_bf16 a[44:47], v[76:79], v[12:15], a[44:47]\n"
        "global_load_lds_dwordx4 v241, s[60:61] offset:3072\n"
        "s_add_u32 m0, s73, 0x14000\n"
        "v_mfma_f32_16x16x32_bf16 a[12:15], v[76:79], v[4:7], a[12:15]\n"
        "s_add_u32 s60, s60, 0x80000\n"
        "s_addc_u32 s61, s61, 0\n"
        "v_cvt_pk_bf16_f32 v195, 0, v195\n"
        "v_mfma_f32_16x16x32_bf16 a[12:15], v[44:47], v[0:3], a[12:15]\n"
        "v_mul_f32 v194, v178, v186\n"
        "v_mul_f32 v196, v179, v186\n"
        "v_mfma_f32_16x16x32_bf16 a[16:19], v[0:3], v[48:51], a[16:19]\n"
        "v_cvt_pk_bf16_f32 v194, 0, v194\n"
        "v_cvt_pk_bf16_f32 v196, 0, v196\n"
        "v_mfma_f32_16x16x32_bf16 a[16:19], v[4:7], v[80:83], a[16:19]\n"
        "global_load_lds_dwordx4 v238, s[62:63] offset:0\n"
        "v_mfma_f32_16x16x32_bf16 a[48:51], v[12:15], v[80:83], a[48:51]\n"
        "v_add_f32 v193, v193, v194\n"
        "v_add_f32 v195, v195, v196\n"
        "v_mfma_f32_16x16x32_bf16 a[48:51], v[8:11], v[48:51], a[48:51]\n"
        "v_cvt_pk_bf16_f32 v193, 0, v193\n"
        "v_cvt_pk_bf16_f32 v195, 0, v195\n"
        "v_mfma_f32_16x16x32_bf16 a[52:55], v[8:11], v[52:55], a[52:55]\n"
        "v_mul_f32 v194, v179, v185\n"
        "v_mul_f32 v196, v180, v185\n"
        "v_mfma_f32_16x16x32_bf16 a[52:55], v[12:15], v[84:87], a[52:55]\n"
        "global_load_lds_dwordx4 v239, s[62:63] offset:1024\n"
        "v_mfma_f32_16x16x32_bf16 a[20:23], v[4:7], v[84:87], a[20:23]\n"
        "v_cvt_pk_bf16_f32 v194, 0, v194\n"
        "v_cvt_pk_bf16_f32 v196, 0, v196\n"
        "v_mfma_f32_16x16x32_bf16 a[20:23], v[0:3], v[52:55], a[20:23]\n"
        "v_add_f32 v193, v193, v194\n"
        "v_add_f32 v195, v195, v196\n"
        "v_mfma_f32_16x16x32_bf16 a[24:27], v[0:3], v[56:59], a[24:27]\n"
        "s_waitcnt lgkmcnt(0)\n"
        "v_cvt_pk_bf16_f32 v193, 0, v193\n"
        "v_cvt_pk_bf16_f32 v195, 0, v195\n"
        "v_mfma_f32_16x16x32_bf16 a[24:27], v[4:7], v[88:91], a[24:27]\n"
        "global_load_dword v253, v173, s[82:83] offset:0\n"
        "v_mfma_f32_16x16x32_bf16 a[56:59], v[12:15], v[88:91], a[56:59]\n"
        "global_load_lds_dwordx4 v240, s[62:63] offset:2048\n"
        "s_add_u32 s82, s12, 0x400000\n"
        "s_addc_u32 s83, s13, 0\n"
        "v_mfma_f32_16x16x32_bf16 a[56:59], v[8:11], v[56:59], a[56:59]\n"
        "v_and_b32 v194, s35, v122\n"
        "v_and_b32 v196, s35, v124\n"
        "v_mfma_f32_16x16x32_bf16 a[60:63], v[8:11], v[60:63], a[60:63]\n"
        "ds_read_b128 v[8:11], v224 offset:2048\n"
        "v_mul_f32 v193, v193, v194\n"
        "v_mfma_f32_16x16x32_bf16 a[60:63], v[12:15], v[92:95], a[60:63]\n"
        "ds_read_b128 v[12:15], v225 offset:2048\n"
        "v_mul_f32 v195, v195, v196\n"
        "v_mfma_f32_16x16x32_bf16 a[28:31], v[4:7], v[92:95], a[28:31]\n"
        "global_load_lds_dwordx4 v241, s[62:63] offset:3072\n"
        "ds_read_b128 v[4:7], v225 offset:0\n"
        "s_add_u32 m0, s72, 0x0\n"
        "v_mfma_f32_16x16x32_bf16 a[28:31], v[0:3], v[60:63], a[28:31]\n"
        "ds_read_b128 v[0:3], v224 offset:0\n"
        "s_add_u32 s62, s62, 0x80000\n"
        "s_addc_u32 s63, s63, 0\n"
        "v_mfma_f32_16x16x32_bf16 a[64:67], v[32:35], v[16:19], a[64:67]\n"
        "v_cvt_pk_bf16_f32 v210, v195, v193\n"
        "v_cvt_pk_bf16_f32 v190, 0, v190\n"
        "v_mfma_f32_16x16x32_bf16 a[64:67], v[64:67], v[20:23], a[64:67]\n"
        "v_cvt_pk_bf16_f32 v192, 0, v192\n"
        "v_add_f32 v189, v189, v190\n"
        "v_mfma_f32_16x16x32_bf16 a[96:99], v[64:67], v[28:31], a[96:99]\n"
        "global_store_dwordx4 v171, v[200:203], s[76:77] offset:-4096 sc1\n"
        "v_add_f32 v191, v191, v192\n"
        "v_cvt_pk_bf16_f32 v189, 0, v189\n"
        "v_cvt_pk_bf16_f32 v191, 0, v191\n"
        "v_mul_f32 v190, v180, v186\n"
        "v_mfma_f32_16x16x32_bf16 a[96:99], v[32:35], v[24:27], a[96:99]\n"
        "v_mul_f32 v192, v181, v186\n"
        "v_cvt_pk_bf16_f32 v190, 0, v190\n"
        "v_mfma_f32_16x16x32_bf16 a[100:103], v[36:39], v[24:27], a[100:103]\n"
        "v_cvt_pk_bf16_f32 v192, 0, v192\n"
        "v_add_f32 v189, v189, v190\n"
        "v_mfma_f32_16x16x32_bf16 a[100:103], v[68:71], v[28:31], a[100:103]\n"
        "v_add_f32 v191, v191, v192\n"
        "v_cvt_pk_bf16_f32 v189, 0, v189\n"
        "v_mfma_f32_16x16x32_bf16 a[68:71], v[68:71], v[20:23], a[68:71]\n"
        "v_cvt_pk_bf16_f32 v191, 0, v191\n"
        "v_mul_f32 v190, v181, v185\n"
        "v_mfma_f32_16x16x32_bf16 a[68:71], v[36:39], v[16:19], a[68:71]\n"
        "global_load_dwordx4 v[154:157], v254, s[92:93] offset:3072\n"
        "v_mul_f32 v192, v182, v185\n"
        "v_mfma_f32_16x16x32_bf16 a[72:75], v[40:43], v[16:19], a[72:75]\n"
        "v_cvt_pk_bf16_f32 v190, 0, v190\n"
        "v_cvt_pk_bf16_f32 v192, 0, v192\n"
        "v_mfma_f32_16x16x32_bf16 a[72:75], v[72:75], v[20:23], a[72:75]\n"
        "v_add_f32 v189, v189, v190\n"
        "v_add_f32 v191, v191, v192\n"
        "v_mfma_f32_16x16x32_bf16 a[104:107], v[72:75], v[28:31], a[104:107]\n"
        "v_cvt_pk_bf16_f32 v189, 0, v189\n"
        "v_cvt_pk_bf16_f32 v191, 0, v191\n"
        "v_mfma_f32_16x16x32_bf16 a[104:107], v[40:43], v[24:27], a[104:107]\n"
        "v_and_b32 v190, s35, v126\n"
        "v_and_b32 v192, s35, v128\n"
        "v_mfma_f32_16x16x32_bf16 a[108:111], v[44:47], v[24:27], a[108:111]\n"
        "v_mul_f32 v189, v189, v190\n"
        "v_mul_f32 v191, v191, v192\n"
        "v_mfma_f32_16x16x32_bf16 a[108:111], v[76:79], v[28:31], a[108:111]\n"
        "v_cvt_pk_bf16_f32 v209, v191, v189\n"
        "v_mul_f32 v193, v180, v188\n"
        "v_mfma_f32_16x16x32_bf16 a[76:79], v[76:79], v[20:23], a[76:79]\n"
        "v_mul_f32 v195, v181, v188\n"
        "v_cvt_pk_bf16_f32 v193, 0, v193\n"
        "v_mfma_f32_16x16x32_bf16 a[76:79], v[44:47], v[16:19], a[76:79]\n"
        "v_cvt_pk_bf16_f32 v195, 0, v195\n"
        "v_mul_f32 v194, v181, v187\n"
        "v_mfma_f32_16x16x32_bf16 a[80:83], v[16:19], v[48:51], a[80:83]\n"
        "v_mul_f32 v196, v182, v187\n"
        "v_cvt_pk_bf16_f32 v194, 0, v194\n"
        "v_mfma_f32_16x16x32_bf16 a[80:83], v[20:23], v[80:83], a[80:83]\n"
        "v_cvt_pk_bf16_f32 v196, 0, v196\n"
        "v_add_f32 v193, v193, v194\n"
        "v_mfma_f32_16x16x32_bf16 a[112:115], v[28:31], v[80:83], a[112:115]\n"
        "v_add_f32 v195, v195, v196\n"
        "v_cvt_pk_bf16_f32 v193, 0, v193\n"
        "v_mfma_f32_16x16x32_bf16 a[112:115], v[24:27], v[48:51], a[112:115]\n"
        "v_cvt_pk_bf16_f32 v195, 0, v195\n"
        "v_mul_f32 v194, v182, v186\n"
        "v_mfma_f32_16x16x32_bf16 a[116:119], v[24:27], v[52:55], a[116:119]\n"
        "v_mul_f32 v196, v183, v186\n"
        "v_cvt_pk_bf16_f32 v194, 0, v194\n"
        "v_mfma_f32_16x16x32_bf16 a[116:119], v[28:31], v[84:87], a[116:119]\n"
        "v_cvt_pk_bf16_f32 v196, 0, v196\n"
        "v_add_f32 v193, v193, v194\n"
        "v_mfma_f32_16x16x32_bf16 a[84:87], v[20:23], v[84:87], a[84:87]\n"
        "v_add_f32 v195, v195, v196\n"
        "v_cvt_pk_bf16_f32 v193, 0, v193\n"
        "v_mfma_f32_16x16x32_bf16 a[84:87], v[16:19], v[52:55], a[84:87]\n"
        "v_cvt_pk_bf16_f32 v195, 0, v195\n"
        "v_mul_f32 v194, v183, v185\n"
        "v_mfma_f32_16x16x32_bf16 a[88:91], v[16:19], v[56:59], a[88:91]\n"
        "v_mul_f32 v196, v184, v185\n"
        "v_cvt_pk_bf16_f32 v194, 0, v194\n"
        "v_mfma_f32_16x16x32_bf16 a[88:91], v[20:23], v[88:91], a[88:91]\n"
        "v_cvt_pk_bf16_f32 v196, 0, v196\n"
        "v_add_f32 v193, v193, v194\n"
        "v_mfma_f32_16x16x32_bf16 a[120:123], v[28:31], v[88:91], a[120:123]\n"
        "v_add_f32 v195, v195, v196\n"
        "v_cvt_pk_bf16_f32 v193, 0, v193\n"
        "v_mfma_f32_16x16x32_bf16 a[120:123], v[24:27], v[56:59], a[120:123]\n"
        "v_cvt_pk_bf16_f32 v195, 0, v195\n"
        "v_and_b32 v194, s35, v130\n"
        "v_mfma_f32_16x16x32_bf16 a[124:127], v[24:27], v[60:63], a[124:127]\n"
        "ds_read_b128 v[24:27], v224 offset:6144\n"
        "v_and_b32 v196, s35, v132\n"
        "v_mfma_f32_16x16x32_bf16 a[124:127], v[28:31], v[92:95], a[124:127]\n"
        "ds_read_b128 v[28:31], v225 offset:6144\n"
        "v_mul_f32 v193, v193, v194\n"
        "v_mfma_f32_16x16x32_bf16 a[92:95], v[20:23], v[92:95], a[92:95]\n"
        "ds_read_b128 v[20:23], v225 offset:4096\n"
        "v_mul_f32 v195, v195, v196\n"
        "v_mfma_f32_16x16x32_bf16 a[92:95], v[16:19], v[60:63], a[92:95]\n"
        "ds_read_b128 v[16:19], v224 offset:4096\n"
        "s_waitcnt lgkmcnt(4)\n"
        "v_mfma_f32_16x16x32_bf16 a[128:131], v[32:35], v[0:3], a[128:131]\n"
        "v_cvt_pk_bf16_f32 v208, v195, v193\n"
        "v_lshlrev_b32 v185, 16, v138\n"
        "v_mfma_f32_16x16x32_bf16 a[128:131], v[64:67], v[4:7], a[128:131]\n"
        "global_store_dwordx4 v171, v[208:211], s[76:77] offset:0 sc1\n"
        "s_add_u32 s76, s76, 0x2000\n"
        "s_addc_u32 s77, s77, 0\n"
        "v_and_b32 v186, s35, v138\n"
        "v_lshlrev_b32 v187, 16, v139\n"
        "v_and_b32 v188, s35, v139\n"
        "v_mfma_f32_16x16x32_bf16 a[160:163], v[64:67], v[12:15], a[160:163]\n"
        "v_lshlrev_b32 v174, 16, v97\n"
        "v_lshlrev_b32 v175, 16, v99\n"
        "v_mfma_f32_16x16x32_bf16 a[160:163], v[32:35], v[8:11], a[160:163]\n"
        "v_lshlrev_b32 v176, 16, v101\n"
        "v_lshlrev_b32 v177, 16, v103\n"
        "v_mfma_f32_16x16x32_bf16 a[164:167], v[36:39], v[8:11], a[164:167]\n"
        "v_lshlrev_b32 v178, 16, v105\n"
        "v_lshlrev_b32 v179, 16, v107\n"
        "v_mfma_f32_16x16x32_bf16 a[164:167], v[68:71], v[12:15], a[164:167]\n"
        "v_lshlrev_b32 v180, 16, v109\n"
        "v_lshlrev_b32 v181, 16, v111\n"
        "v_mfma_f32_16x16x32_bf16 a[132:135], v[68:71], v[4:7], a[132:135]\n"
        "v_lshlrev_b32 v182, 16, v113\n"
        "v_lshlrev_b32 v183, 16, v115\n"
        "v_mfma_f32_16x16x32_bf16 a[132:135], v[36:39], v[0:3], a[132:135]\n"
        "v_lshlrev_b32 v184, 16, v117\n"
        "v_mul_f32 v189, v174, v188\n"
        "v_mfma_f32_16x16x32_bf16 a[136:139], v[40:43], v[0:3], a[136:139]\n"
        "v_mul_f32 v191, v175, v188\n"
        "v_cvt_pk_bf16_f32 v189, 0, v189\n"
        "v_mfma_f32_16x16x32_bf16 a[136:139], v[72:75], v[4:7], a[136:139]\n"
        "v_cvt_pk_bf16_f32 v191, 0, v191\n"
        "v_mul_f32 v190, v175, v187\n"
        "v_mfma_f32_16x16x32_bf16 a[168:171], v[72:75], v[12:15], a[168:171]\n"
        "v_mul_f32 v192, v176, v187\n"
        "v_cvt_pk_bf16_f32 v190, 0, v190\n"
        "v_mfma_f32_16x16x32_bf16 a[168:171], v[40:43], v[8:11], a[168:171]\n"
        "v_cvt_pk_bf16_f32 v192, 0, v192\n"
        "v_add_f32 v189, v189, v190\n"
        "v_mfma_f32_16x16x32_bf16 a[172:175], v[44:47], v[8:11], a[172:175]\n"
        "v_add_f32 v191, v191, v192\n"
        "v_cvt_pk_bf16_f32 v189, 0, v189\n"
        "v_mfma_f32_16x16x32_bf16 a[172:175], v[76:79], v[12:15], a[172:175]\n"
        "v_cvt_pk_bf16_f32 v191, 0, v191\n"
        "v_mul_f32 v190, v176, v186\n"
        "v_mfma_f32_16x16x32_bf16 a[140:143], v[76:79], v[4:7], a[140:143]\n"
        "v_mul_f32 v192, v177, v186\n"
        "v_cvt_pk_bf16_f32 v190, 0, v190\n"
        "v_mfma_f32_16x16x32_bf16 a[140:143], v[44:47], v[0:3], a[140:143]\n"
        "v_cvt_pk_bf16_f32 v192, 0, v192\n"
        "v_add_f32 v189, v189, v190\n"
        "v_mfma_f32_16x16x32_bf16 a[144:147], v[0:3], v[48:51], a[144:147]\n"
        "v_add_f32 v191, v191, v192\n"
        "v_cvt_pk_bf16_f32 v189, 0, v189\n"
        "v_mfma_f32_16x16x32_bf16 a[144:147], v[4:7], v[80:83], a[144:147]\n"
        "v_cvt_pk_bf16_f32 v191, 0, v191\n"
        "v_mul_f32 v190, v177, v185\n"
        "v_mfma_f32_16x16x32_bf16 a[176:179], v[12:15], v[80:83], a[176:179]\n"
        "v_mul_f32 v192, v178, v185\n"
        "v_cvt_pk_bf16_f32 v190, 0, v190\n"
        "v_mfma_f32_16x16x32_bf16 a[176:179], v[8:11], v[48:51], a[176:179]\n"
        "v_cvt_pk_bf16_f32 v192, 0, v192\n"
        "v_add_f32 v189, v189, v190\n"
        "v_mfma_f32_16x16x32_bf16 a[180:183], v[8:11], v[52:55], a[180:183]\n"
        "v_add_f32 v191, v191, v192\n"
        "v_cvt_pk_bf16_f32 v189, 0, v189\n"
        "v_mfma_f32_16x16x32_bf16 a[180:183], v[12:15], v[84:87], a[180:183]\n"
        "v_cvt_pk_bf16_f32 v191, 0, v191\n"
        "v_lshlrev_b32 v190, 16, v119\n"
        "v_mfma_f32_16x16x32_bf16 a[148:151], v[4:7], v[84:87], a[148:151]\n"
        "v_lshlrev_b32 v192, 16, v121\n"
        "v_mul_f32 v189, v189, v190\n"
        "v_mfma_f32_16x16x32_bf16 a[148:151], v[0:3], v[52:55], a[148:151]\n"
        "v_mul_f32 v191, v191, v192\n"
        "v_cvt_pk_bf16_f32 v203, v191, v189\n"
        "v_mfma_f32_16x16x32_bf16 a[152:155], v[0:3], v[56:59], a[152:155]\n"
        "v_mul_f32 v193, v176, v188\n"
        "v_mul_f32 v195, v177, v188\n"
        "v_mfma_f32_16x16x32_bf16 a[152:155], v[4:7], v[88:91], a[152:155]\n"
        "v_cvt_pk_bf16_f32 v193, 0, v193\n"
        "v_cvt_pk_bf16_f32 v195, 0, v195\n"
        "v_mfma_f32_16x16x32_bf16 a[184:187], v[12:15], v[88:91], a[184:187]\n"
        "v_mul_f32 v194, v177, v187\n"
        "v_mul_f32 v196, v178, v187\n"
        "v_mfma_f32_16x16x32_bf16 a[184:187], v[8:11], v[56:59], a[184:187]\n"
        "s_waitcnt vmcnt(13) lgkmcnt(0)\n"
        "v_cvt_pk_bf16_f32 v194, 0, v194\n"
        "v_cvt_pk_bf16_f32 v196, 0, v196\n"
        "v_mfma_f32_16x16x32_bf16 a[188:191], v[8:11], v[60:63], a[188:191]\n"
        "s_barrier\n"
        "v_mfma_f32_16x16x32_bf16 a[188:191], v[12:15], v[92:95], a[188:191]\n"
        "global_load_lds_dwordx4 v234, s[56:57] offset:0\n"
        "ds_read_b128 v[8:11], v224 offset:51200\n"
        "v_mfma_f32_16x16x32_bf16 a[156:159], v[4:7], v[92:95], a[156:159]\n"
        "ds_read_b128 v[4:7], v225 offset:49152\n"
        "v_add_f32 v193, v193, v194\n"
        "v_mfma_f32_16x16x32_bf16 a[156:159], v[0:3], v[60:63], a[156:159]\n"
        "ds_read_b128 v[0:3], v224 offset:49152\n"
        "v_add_f32 v195, v195, v196\n"
        "v_mfma_f32_16x16x32_bf16 a[192:195], v[32:35], v[16:19], a[192:195]\n"
        "ds_read_b128 v[12:15], v225 offset:51200\n"
        "v_cvt_pk_bf16_f32 v193, 0, v193\n"
        "v_mfma_f32_16x16x32_bf16 a[192:195], v[64:67], v[20:23], a[192:195]\n"
        "global_load_lds_dwordx4 v235, s[56:57] offset:1024\n"
        "v_cvt_pk_bf16_f32 v195, 0, v195\n"
        "v_mfma_f32_16x16x32_bf16 a[224:227], v[64:67], v[28:31], a[224:227]\n"
        "ds_read_b64_tr_b16 v[64:65], v216 offset:8192\n"
        "v_mul_f32 v194, v178, v186\n"
        "v_mfma_f32_16x16x32_bf16 a[224:227], v[32:35], v[24:27], a[224:227]\n"
        "ds_read_b64_tr_b16 v[32:33], v216 offset:0\n"
        "v_mul_f32 v196, v179, v186\n"
        "v_mfma_f32_16x16x32_bf16 a[228:231], v[36:39], v[24:27], a[228:231]\n"
        "ds_read_b64_tr_b16 v[34:35], v217 offset:0\n"
        "v_cvt_pk_bf16_f32 v194, 0, v194\n"
        "v_mfma_f32_16x16x32_bf16 a[228:231], v[68:71], v[28:31], a[228:231]\n"
        "ds_read_b64_tr_b16 v[66:67], v217 offset:8192\n"
        "global_load_lds_dwordx4 v236, s[56:57] offset:2048\n"
        "v_mfma_f32_16x16x32_bf16 a[196:199], v[68:71], v[20:23], a[196:199]\n"
        "ds_read_b64_tr_b16 v[68:69], v218 offset:8192\n"
        "v_cvt_pk_bf16_f32 v196, 0, v196\n"
        "v_mfma_f32_16x16x32_bf16 a[196:199], v[36:39], v[16:19], a[196:199]\n"
        "ds_read_b64_tr_b16 v[36:37], v218 offset:0\n"
        "v_add_f32 v193, v193, v194\n"
        "v_mfma_f32_16x16x32_bf16 a[200:203], v[40:43], v[16:19], a[200:203]\n"
        "ds_read_b64_tr_b16 v[38:39], v219 offset:0\n"
        "v_add_f32 v195, v195, v196\n"
        "v_mfma_f32_16x16x32_bf16 a[200:203], v[72:75], v[20:23], a[200:203]\n"
        "ds_read_b64_tr_b16 v[70:71], v219 offset:8192\n"
        "global_load_dword v253, v173, s[82:83] offset:0\n"
        "v_mfma_f32_16x16x32_bf16 a[232:235], v[72:75], v[28:31], a[232:235]\n"
        "ds_read_b64_tr_b16 v[72:73], v220 offset:8192\n"
        "global_load_lds_dwordx4 v237, s[56:57] offset:3072\n"
        "s_add_u32 m0, s72, 0x1000\n"
        "v_mfma_f32_16x16x32_bf16 a[232:235], v[40:43], v[24:27], a[232:235]\n"
        "ds_read_b64_tr_b16 v[40:41], v220 offset:0\n"
        "s_add_u32 s56, s56, 0x80\n"
        "s_addc_u32 s57, s57, 0\n"
        "v_mfma_f32_16x16x32_bf16 a[236:239], v[44:47], v[24:27], a[236:239]\n"
        "ds_read_b64_tr_b16 v[42:43], v221 offset:0\n"
        "s_add_u32 s82, s12, 0x500000\n"
        "LHA2_%=:\n"
        "s_cbranch_execz LHA6_%=\n"
        "s_addc_u32 s83, s13, 0\n"
        "v_mfma_f32_16x16x32_bf16 a[236:239], v[76:79], v[28:31], a[236:239]\n"
        "ds_read_b64_tr_b16 v[74:75], v221 offset:8192\n"
        "v_cvt_pk_bf16_f32 v193, 0, v193\n"
        "v_mfma_f32_16x16x32_bf16 a[204:207], v[76:79], v[20:23], a[204:207]\n"
        "ds_read_b64_tr_b16 v[76:77], v222 offset:8192\n"
        "global_load_lds_dwordx4 v234, s[58:59] offset:0\n"
        "v_mfma_f32_16x16x32_bf16 a[204:207], v[44:47], v[16:19], a[204:207]\n"
        "ds_read_b64_tr_b16 v[44:45], v222 offset:0\n"
        "v_cvt_pk_bf16_f32 v195, 0, v195\n"
        "v_mfma_f32_16x16x32_bf16 a[208:211], v[16:19], v[48:51], a[208:211]\n"
        "ds_read_b64_tr_b16 v[46:47], v223 offset:0\n"
        "v_mul_f32 v194, v179, v185\n"
        "v_mfma_f32_16x16x32_bf16 a[208:211], v[20:23], v[80:83], a[208:211]\n"
        "ds_read_b64_tr_b16 v[78:79], v223 offset:8192\n"
        "v_mul_f32 v196, v180, v185\n"
        "v_mfma_f32_16x16x32_bf16 a[240:243], v[28:31], v[80:83], a[240:243]\n"
        "ds_read_b64_tr_b16 v[80:81], v216 offset:24576\n"
        "global_load_lds_dwordx4 v235, s[58:59] offset:1024\n"
        "v_mfma_f32_16x16x32_bf16 a[240:243], v[24:27], v[48:51], a[240:243]\n"
        "ds_read_b64_tr_b16 v[48:49], v216 offset:16384\n"
        "v_cvt_pk_bf16_f32 v194, 0, v194\n"
        "v_mfma_f32_16x16x32_bf16 a[244:247], v[24:27], v[52:55], a[244:247]\n"
        "ds_read_b64_tr_b16 v[50:51], v217 offset:16384\n"
        "v_cvt_pk_bf16_f32 v196, 0, v196\n"
        "v_mfma_f32_16x16x32_bf16 a[244:247], v[28:31], v[84:87], a[244:247]\n"
        "ds_read_b64_tr_b16 v[82:83], v217 offset:24576\n"
        "v_add_f32 v193, v193, v194\n"
        "v_mfma_f32_16x16x32_bf16 a[212:215], v[20:23], v[84:87], a[212:215]\n"
        "ds_read_b64_tr_b16 v[84:85], v218 offset:24576\n"
        "global_load_lds_dwordx4 v236, s[58:59] offset:2048\n"
        "v_mfma_f32_16x16x32_bf16 a[212:215], v[16:19], v[52:55], a[212:215]\n"
        "ds_read_b64_tr_b16 v[52:53], v218 offset:16384\n"
        "v_add_f32 v195, v195, v196\n"
        "v_mfma_f32_16x16x32_bf16 a[216:219], v[16:19], v[56:59], a[216:219]\n"
        "ds_read_b64_tr_b16 v[54:55], v219 offset:16384\n"
        "v_cvt_pk_bf16_f32 v193, 0, v193\n"
        "v_mfma_f32_16x16x32_bf16 a[216:219], v[20:23], v[88:91], a[216:219]\n"
        "ds_read_b64_tr_b16 v[86:87], v219 offset:24576\n"
        "v_cvt_pk_bf16_f32 v195, 0, v195\n"
        "v_mfma_f32_16x16x32_bf16 a[248:251], v[28:31], v[88:91], a[248:251]\n"
        "ds_read_b64_tr_b16 v[88:89], v220 offset:24576\n"
        "global_load_lds_dwordx4 v237, s[58:59] offset:3072\n"
        "s_add_u32 m0, s73, 0x18000\n"
        "v_mfma_f32_16x16x32_bf16 a[248:251], v[24:27], v[56:59], a[248:251]\n"
        "ds_read_b64_tr_b16 v[56:57], v220 offset:16384\n"
        "s_add_u32 s58, s58, 0x80\n"
        "s_addc_u32 s59, s59, 0\n"
        "v_mfma_f32_16x16x32_bf16 a[252:255], v[24:27], v[60:63], a[252:255]\n"
        "ds_read_b64_tr_b16 v[58:59], v221 offset:16384\n"
        "v_lshlrev_b32 v194, 16, v123\n"
        "v_mfma_f32_16x16x32_bf16 a[252:255], v[28:31], v[92:95], a[252:255]\n"
        "ds_read_b64_tr_b16 v[90:91], v221 offset:24576\n"
        "v_lshlrev_b32 v196, 16, v125\n"
        "v_mfma_f32_16x16x32_bf16 a[220:223], v[20:23], v[92:95], a[220:223]\n"
        "ds_read_b64_tr_b16 v[92:93], v222 offset:24576\n"
        "global_load_lds_dwordx4 v238, s[60:61] offset:0\n"
        "v_mfma_f32_16x16x32_bf16 a[220:223], v[16:19], v[60:63], a[220:223]\n"
        "ds_read_b64_tr_b16 v[60:61], v222 offset:16384\n"
        "s_waitcnt lgkmcnt(8)\n"
        "v_mfma_f32_16x16x32_bf16 a[0:3], v[32:35], v[0:3], a[0:3]\n"
        "ds_read_b64_tr_b16 v[62:63], v223 offset:16384\n"
        "v_mul_f32 v193, v193, v194\n"
        "v_mfma_f32_16x16x32_bf16 a[0:3], v[64:67], v[4:7], a[0:3]\n"
        "ds_read_b64_tr_b16 v[94:95], v223 offset:24576\n"
        "v_mul_f32 v195, v195, v196\n"
        "v_mfma_f32_16x16x32_bf16 a[32:35], v[64:67], v[12:15], a[32:35]\n"
        "ds_read_b128 v[16:19], v224 offset:53248\n"
        "global_load_lds_dwordx4 v239, s[60:61] offset:1024\n"
        "v_mfma_f32_16x16x32_bf16 a[32:35], v[32:35], v[8:11], a[32:35]\n"
        "ds_read_b128 v[20:23], v225 offset:53248\n"
        "v_cvt_pk_bf16_f32 v202, v195, v193\n"
        "v_mfma_f32_16x16x32_bf16 a[36:39], v[36:39], v[8:11], a[36:39]\n"
        "ds_read_b128 v[24:27], v224 offset:55296\n"
        "v_mul_f32 v189, v178, v188\n"
        "v_mfma_f32_16x16x32_bf16 a[36:39], v[68:71], v[12:15], a[36:39]\n"
        "ds_read_b128 v[28:31], v225 offset:55296\n"
        "v_mul_f32 v191, v179, v188\n"
        "v_mfma_f32_16x16x32_bf16 a[4:7], v[68:71], v[4:7], a[4:7]\n"
        "global_load_lds_dwordx4 v240, s[60:61] offset:2048\n"
        "v_mfma_f32_16x16x32_bf16 a[4:7], v[36:39], v[0:3], a[4:7]\n"
        "v_cvt_pk_bf16_f32 v189, 0, v189\n"
        "v_cvt_pk_bf16_f32 v191, 0, v191\n"
        "v_mfma_f32_16x16x32_bf16 a[8:11], v[40:43], v[0:3], a[8:11]\n"
        "v_mul_f32 v190, v179, v187\n"
        "v_mul_f32 v192, v180, v187\n"
        "v_mfma_f32_16x16x32_bf16 a[8:11], v[72:75], v[4:7], a[8:11]\n"
        "v_cvt_pk_bf16_f32 v190, 0, v190\n"
        "v_cvt_pk_bf16_f32 v192, 0, v192\n"
        "v_mfma_f32_16x16x32_bf16 a[40:43], v[72:75], v[12:15], a[40:43]\n"
        "global_load_lds_dwordx4 v241, s[60:61] offset:3072\n"
        "s_add_u32 m0, s73, 0x1c000\n"
        "v_mfma_f32_16x16x32_bf16 a[40:43], v[40:43], v[8:11], a[40:43]\n"
        "v_add_f32 v189, v189, v190\n"
        "v_add_f32 v191, v191, v192\n"
        "v_mfma_f32_16x16x32_bf16 a[44:47], v[44:47], v[8:11], a[44:47]\n"
        "v_cvt_pk_bf16_f32 v189, 0, v189\n"
        "v_cvt_pk_bf16_f32 v191, 0, v191\n"
        "v_mfma_f32_16x16x32_bf16 a[44:47], v[76:79], v[12:15], a[44:47]\n"
        "v_mul_f32 v190, v180, v186\n"
        "v_mul_f32 v192, v181, v186\n"
        "v_mfma_f32_16x16x32_bf16 a[12:15], v[76:79], v[4:7], a[12:15]\n"
        "global_load_lds_dwordx4 v238, s[62:63] offset:0\n"
        "v_mfma_f32_16x16x32_bf16 a[12:15], v[44:47], v[0:3], a[12:15]\n"
        "v_cvt_pk_bf16_f32 v190, 0, v190\n"
        "v_cvt_pk_bf16_f32 v192, 0, v192\n"
        "v_mfma_f32_16x16x32_bf16 a[16:19], v[0:3], v[48:51], a[16:19]\n"
        "s_waitcnt lgkmcnt(0)\n"
        "v_add_f32 v189, v189, v190\n"
        "v_add_f32 v191, v191, v192\n"
        "v_mfma_f32_16x16x32_bf16 a[16:19], v[4:7], v[80:83], a[16:19]\n"
        "v_cvt_pk_bf16_f32 v189, 0, v189\n"
        "v_cvt_pk_bf16_f32 v191, 0, v191\n"
        "v_mfma_f32_16x16x32_bf16 a[48:51], v[12:15], v[80:83], a[48:51]\n"
        "global_load_lds_dwordx4 v239, s[62:63] offset:1024\n"
        "v_mfma_f32_16x16x32_bf16 a[48:51], v[8:11], v[48:51], a[48:51]\n"
        "v_mul_f32 v190, v181, v185\n"
        "v_mul_f32 v192, v182, v185\n"
        "v_mfma_f32_16x16x32_bf16 a[52:55], v[8:11], v[52:55], a[52:55]\n"
        "v_cvt_pk_bf16_f32 v190, 0, v190\n"
        "v_cvt_pk_bf16_f32 v192, 0, v192\n"
        "v_mfma_f32_16x16x32_bf16 a[52:55], v[12:15], v[84:87], a[52:55]\n"
        "v_add_f32 v189, v189, v190\n"
        "v_add_f32 v191, v191, v192\n"
        "v_mfma_f32_16x16x32_bf16 a[20:23], v[4:7], v[84:87], a[20:23]\n"
        "global_load_lds_dwordx4 v240, s[62:63] offset:2048\n"
        "v_mfma_f32_16x16x32_bf16 a[20:23], v[0:3], v[52:55], a[20:23]\n"
        "v_cvt_pk_bf16_f32 v189, 0, v189\n"
        "v_cvt_pk_bf16_f32 v191, 0, v191\n"
        "v_mfma_f32_16x16x32_bf16 a[24:27], v[0:3], v[56:59], a[24:27]\n"
        "v_lshlrev_b32 v190, 16, v127\n"
        "v_lshlrev_b32 v192, 16, v129\n"
        "v_mfma_f32_16x16x32_bf16 a[24:27], v[4:7], v[88:91], a[24:27]\n"
        "v_mul_f32 v189, v189, v190\n"
        "v_mul_f32 v191, v191, v192\n"
        "v_mfma_f32_16x16x32_bf16 a[56:59], v[12:15], v[88:91], a[56:59]\n"
        "global_load_lds_dwordx4 v241, s[62:63] offset:3072\n"
        "s_add_u32 m0, s72, 0x8000\n"
        "v_mfma_f32_16x16x32_bf16 a[56:59], v[8:11], v[56:59], a[56:59]\n"
        "v_cvt_pk_bf16_f32 v201, v191, v189\n"
        "v_mul_f32 v193, v180, v188\n"
        "v_mfma_f32_16x16x32_bf16 a[60:63], v[8:11], v[60:63], a[60:63]\n"
        "ds_read_b128 v[8:11], v224 offset:34816\n"
        "v_mul_f32 v195, v181, v188\n"
        "v_mfma_f32_16x16x32_bf16 a[60:63], v[12:15], v[92:95], a[60:63]\n"
        "ds_read_b128 v[12:15], v225 offset:34816\n"
        "v_cvt_pk_bf16_f32 v193, 0, v193\n"
        "v_mfma_f32_16x16x32_bf16 a[28:31], v[4:7], v[92:95], a[28:31]\n"
        "ds_read_b128 v[4:7], v225 offset:32768\n"
        "v_cvt_pk_bf16_f32 v195, 0, v195\n"
        "v_mfma_f32_16x16x32_bf16 a[28:31], v[0:3], v[60:63], a[28:31]\n"
        "ds_read_b128 v[0:3], v224 offset:32768\n"
        "v_mul_f32 v194, v181, v187\n"
        "v_mfma_f32_16x16x32_bf16 a[64:67], v[32:35], v[16:19], a[64:67]\n"
        "v_mul_f32 v196, v182, v187\n"
        "v_cvt_pk_bf16_f32 v194, 0, v194\n"
        "v_mfma_f32_16x16x32_bf16 a[64:67], v[64:67], v[20:23], a[64:67]\n"
        "v_cvt_pk_bf16_f32 v196, 0, v196\n"
        "v_add_f32 v193, v193, v194\n"
        "v_mfma_f32_16x16x32_bf16 a[96:99], v[64:67], v[28:31], a[96:99]\n"
        "v_add_f32 v195, v195, v196\n"
        "v_cvt_pk_bf16_f32 v193, 0, v193\n"
        "v_mfma_f32_16x16x32_bf16 a[96:99], v[32:35], v[24:27], a[96:99]\n"
        "v_cvt_pk_bf16_f32 v195, 0, v195\n"
        "v_mul_f32 v194, v182, v186\n"
        "v_mfma_f32_16x16x32_bf16 a[100:103], v[36:39], v[24:27], a[100:103]\n"
        "v_mul_f32 v196, v183, v186\n"
        "v_cvt_pk_bf16_f32 v194, 0, v194\n"
        "v_mfma_f32_16x16x32_bf16 a[100:103], v[68:71], v[28:31], a[100:103]\n"
        "v_cvt_pk_bf16_f32 v196, 0, v196\n"
        "v_add_f32 v193, v193, v194\n"
        "v_mfma_f32_16x16x32_bf16 a[68:71], v[68:71], v[20:23], a[68:71]\n"
        "v_add_f32 v195, v195, v196\n"
        "v_cvt_pk_bf16_f32 v193, 0, v193\n"
        "v_mfma_f32_16x16x32_bf16 a[68:71], v[36:39], v[16:19], a[68:71]\n"
        "v_cvt_pk_bf16_f32 v195, 0, v195\n"
        "v_mul_f32 v194, v183, v185\n"
        "v_mfma_f32_16x16x32_bf16 a[72:75], v[40:43], v[16:19], a[72:75]\n"
        "v_mul_f32 v196, v184, v185\n"
        "v_cvt_pk_bf16_f32 v194, 0, v194\n"
        "v_mfma_f32_16x16x32_bf16 a[72:75], v[72:75], v[20:23], a[72:75]\n"
        "v_cvt_pk_bf16_f32 v196, 0, v196\n"
        "v_add_f32 v193, v193, v194\n"
        "v_mfma_f32_16x16x32_bf16 a[104:107], v[72:75], v[28:31], a[104:107]\n"
        "v_add_f32 v195, v195, v196\n"
        "v_cvt_pk_bf16_f32 v193, 0, v193\n"
        "v_mfma_f32_16x16x32_bf16 a[104:107], v[40:43], v[24:27], a[104:107]\n"
        "v_cvt_pk_bf16_f32 v195, 0, v195\n"
        "v_lshlrev_b32 v194, 16, v131\n"
        "v_mfma_f32_16x16x32_bf16 a[108:111], v[44:47], v[24:27], a[108:111]\n"
        "v_lshlrev_b32 v196, 16, v133\n"
        "v_mul_f32 v193, v193, v194\n"
        "v_mfma_f32_16x16x32_bf16 a[108:111], v[76:79], v[28:31], a[108:111]\n"
        "v_mul_f32 v195, v195, v196\n"
        "v_cvt_pk_bf16_f32 v200, v195, v193\n"
        "v_mfma_f32_16x16x32_bf16 a[76:79], v[76:79], v[20:23], a[76:79]\n"
        "global_store_dwordx4 v171, v[200:203], s[76:77] offset:-4096 sc1\n"
        "v_lshlrev_b32 v185, 16, v140\n"
        "v_and_b32 v186, s35, v140\n"
        "v_lshlrev_b32 v187, 16, v141\n"
        "v_and_b32 v188, s35, v141\n"
        "v_mfma_f32_16x16x32_bf16 a[76:79], v[44:47], v[16:19], a[76:79]\n"
        "v_and_b32 v174, s35, v97\n"
        "v_and_b32 v175, s35, v99\n"
        "v_mfma_f32_16x16x32_bf16 a[80:83], v[16:19], v[48:51], a[80:83]\n"
        "v_and_b32 v176, s35, v101\n"
        "v_and_b32 v177, s35, v103\n"
        "v_mfma_f32_16x16x32_bf16 a[80:83], v[20:23], v[80:83], a[80:83]\n"
        "v_and_b32 v178, s35, v105\n"
        "v_and_b32 v179, s35, v107\n"
        "v_mfma_f32_16x16x32_bf16 a[112:115], v[28:31], v[80:83], a[112:115]\n"
        "v_and_b32 v180, s35, v109\n"
        "v_and_b32 v181, s35, v111\n"
        "v_mfma_f32_16x16x32_bf16 a[112:115], v[24:27], v[48:51], a[112:115]\n"
        "v_and_b32 v182, s35, v113\n"
        "v_and_b32 v183, s35, v115\n"
        "v_mfma_f32_16x16x32_bf16 a[116:119], v[24:27], v[52:55], a[116:119]\n"
        "v_and_b32 v184, s35, v117\n"
        "v_mul_f32 v189, v174, v188\n"
        "v_mfma_f32_16x16x32_bf16 a[116:119], v[28:31], v[84:87], a[116:119]\n"
        "v_mul_f32 v191, v175, v188\n"
        "v_cvt_pk_bf16_f32 v189, 0, v189\n"
        "v_mfma_f32_16x16x32_bf16 a[84:87], v[20:23], v[84:87], a[84:87]\n"
        "v_cvt_pk_bf16_f32 v191, 0, v191\n"
        "v_mul_f32 v190, v175, v187\n"
        "v_mfma_f32_16x16x32_bf16 a[84:87], v[16:19], v[52:55], a[84:87]\n"
        "global_load_dword v253, v173, s[82:83] offset:0\n"
        "s_add_u32 s82, s12, 0x600000\n"
        "s_addc_u32 s83, s13, 0\n"
        "v_mfma_f32_16x16x32_bf16 a[88:91], v[16:19], v[56:59], a[88:91]\n"
        "v_mul_f32 v192, v176, v187\n"
        "v_cvt_pk_bf16_f32 v190, 0, v190\n"
        "v_mfma_f32_16x16x32_bf16 a[88:91], v[20:23], v[88:91], a[88:91]\n"
        "v_cvt_pk_bf16_f32 v192, 0, v192\n"
        "v_add_f32 v189, v189, v190\n"
        "v_mfma_f32_16x16x32_bf16 a[120:123], v[28:31], v[88:91], a[120:123]\n"
        "v_add_f32 v191, v191, v192\n"
        "v_cvt_pk_bf16_f32 v189, 0, v189\n"
        "v_mfma_f32_16x16x32_bf16 a[120:123], v[24:27], v[56:59], a[120:123]\n"
        "v_cvt_pk_bf16_f32 v191, 0, v191\n"
        "v_mul_f32 v190, v176, v186\n"
        "v_mfma_f32_16x16x32_bf16 a[124:127], v[24:27], v[60:63], a[124:127]\n"
        "ds_read_b128 v[24:27], v224 offset:38912\n"
        "v_mul_f32 v192, v177, v186\n"
        "v_mfma_f32_16x16x32_bf16 a[124:127], v[28:31], v[92:95], a[124:127]\n"
        "ds_read_b128 v[28:31], v225 offset:38912\n"
        "v_cvt_pk_bf16_f32 v190, 0, v190\n"
        "v_mfma_f32_16x16x32_bf16 a[92:95], v[20:23], v[92:95], a[92:95]\n"
        "ds_read_b128 v[20:23], v225 offset:36864\n"
        "v_cvt_pk_bf16_f32 v192, 0, v192\n"
        "v_mfma_f32_16x16x32_bf16 a[92:95], v[16:19], v[60:63], a[92:95]\n"
        "ds_read_b128 v[16:19], v224 offset:36864\n"
        "s_waitcnt lgkmcnt(4)\n"
        "v_mfma_f32_16x16x32_bf16 a[128:131], v[32:35], v[0:3], a[128:131]\n"
        "v_add_f32 v189, v189, v190\n"
        "v_add_f32 v191, v191, v192\n"
        "v_mfma_f32_16x16x32_bf16 a[128:131], v[64:67], v[4:7], a[128:131]\n"
        "v_cvt_pk_bf16_f32 v189, 0, v189\n"
        "v_cvt_pk_bf16_f32 v191, 0, v191\n"
        "v_mfma_f32_16x16x32_bf16 a[160:163], v[64:67], v[12:15], a[160:163]\n"
        "v_mul_f32 v190, v177, v185\n"
        "v_mul_f32 v192, v178, v185\n"
        "v_mfma_f32_16x16x32_bf16 a[160:163], v[32:35], v[8:11], a[160:163]\n"
        "v_cvt_pk_bf16_f32 v190, 0, v190\n"
        "v_cvt_pk_bf16_f32 v192, 0, v192\n"
        "v_mfma_f32_16x16x32_bf16 a[164:167], v[36:39], v[8:11], a[164:167]\n"
        "v_add_f32 v189, v189, v190\n"
        "v_add_f32 v191, v191, v192\n"
        "v_mfma_f32_16x16x32_bf16 a[164:167], v[68:71], v[12:15], a[164:167]\n"
        "v_cvt_pk_bf16_f32 v189, 0, v189\n"
        "v_cvt_pk_bf16_f32 v191, 0, v191\n"
        "v_mfma_f32_16x16x32_bf16 a[132:135], v[68:71], v[4:7], a[132:135]\n"
        "v_and_b32 v190, s35, v119\n"
        "v_and_b32 v192, s35, v121\n"
        "v_mfma_f32_16x16x32_bf16 a[132:135], v[36:39], v[0:3], a[132:135]\n"
        "v_mul_f32 v189, v189, v190\n"
        "v_mul_f32 v191, v191, v192\n"
        "v_mfma_f32_16x16x32_bf16 a[136:139], v[40:43], v[0:3], a[136:139]\n"
        "v_cvt_pk_bf16_f32 v211, v191, v189\n"
        "v_mul_f32 v193, v176, v188\n"
        "v_mfma_f32_16x16x32_bf16 a[136:139], v[72:75], v[4:7], a[136:139]\n"
        "v_mul_f32 v195, v177, v188\n"
        "v_cvt_pk_bf16_f32 v193, 0, v193\n"
        "v_mfma_f32_16x16x32_bf16 a[168:171], v[72:75], v[12:15], a[168:171]\n"
        "v_cvt_pk_bf16_f32 v195, 0, v195\n"
        "v_mul_f32 v194, v177, v187\n"
        "v_mfma_f32_16x16x32_bf16 a[168:171], v[40:43], v[8:11], a[168:171]\n"
        "v_mul_f32 v196, v178, v187\n"
        "v_cvt_pk_bf16_f32 v194, 0, v194\n"
        "v_mfma_f32_16x16x32_bf16 a[172:175], v[44:47], v[8:11], a[172:175]\n"
        "v_cvt_pk_bf16_f32 v196, 0, v196\n"
        "v_add_f32 v193, v193, v194\n"
        "v_mfma_f32_16x16x32_bf16 a[172:175], v[76:79], v[12:15], a[172:175]\n"
        "v_add_f32 v195, v195, v196\n"
        "v_cvt_pk_bf16_f32 v193, 0, v193\n"
        "v_mfma_f32_16x16x32_bf16 a[140:143], v[76:79], v[4:7], a[140:143]\n"
        "v_cvt_pk_bf16_f32 v195, 0, v195\n"
        "v_mul_f32 v194, v178, v186\n"
        "v_mfma_f32_16x16x32_bf16 a[140:143], v[44:47], v[0:3], a[140:143]\n"
        "v_mul_f32 v196, v179, v186\n"
        "v_cvt_pk_bf16_f32 v194, 0, v194\n"
        "v_mfma_f32_16x16x32_bf16 a[144:147], v[0:3], v[48:51], a[144:147]\n"
        "v_cvt_pk_bf16_f32 v196, 0, v196\n"
        "v_add_f32 v193, v193, v194\n"
        "v_mfma_f32_16x16x32_bf16 a[144:147], v[4:7], v[80:83], a[144:147]\n"
        "v_add_f32 v195, v195, v196\n"
        "v_cvt_pk_bf16_f32 v193, 0, v193\n"
        "v_mfma_f32_16x16x32_bf16 a[176:179], v[12:15], v[80:83], a[176:179]\n"
        "v_cvt_pk_bf16_f32 v195, 0, v195\n"
        "v_mul_f32 v194, v179, v185\n"
        "v_mfma_f32_16x16x32_bf16 a[176:179], v[8:11], v[48:51], a[176:179]\n"
        "v_mul_f32 v196, v180, v185\n"
        "v_cvt_pk_bf16_f32 v194, 0, v194\n"
        "v_mfma_f32_16x16x32_bf16 a[180:183], v[8:11], v[52:55], a[180:183]\n"
        "v_cvt_pk_bf16_f32 v196, 0, v196\n"
        "v_add_f32 v193, v193, v194\n"
        "v_mfma_f32_16x16x32_bf16 a[180:183], v[12:15], v[84:87], a[180:183]\n"
        "v_add_f32 v195, v195, v196\n"
        "v_cvt_pk_bf16_f32 v193, 0, v193\n"
        "v_mfma_f32_16x16x32_bf16 a[148:151], v[4:7], v[84:87], a[148:151]\n"
        "v_cvt_pk_bf16_f32 v195, 0, v195\n"
        "v_and_b32 v194, s35, v123\n"
        "v_mfma_f32_16x16x32_bf16 a[148:151], v[0:3], v[52:55], a[148:151]\n"
        "v_and_b32 v196, s35, v125\n"
        "v_mul_f32 v193, v193, v194\n"
        "v_mfma_f32_16x16x32_bf16 a[152:155], v[0:3], v[56:59], a[152:155]\n"
        "v_mul_f32 v195, v195, v196\n"
        "v_cvt_pk_bf16_f32 v210, v195, v193\n"
        "v_mfma_f32_16x16x32_bf16 a[152:155], v[4:7], v[88:91], a[152:155]\n"
        "v_mul_f32 v189, v178, v188\n"
        "v_mul_f32 v191, v179, v188\n"
        "v_mfma_f32_16x16x32_bf16 a[184:187], v[12:15], v[88:91], a[184:187]\n"
        "v_cvt_pk_bf16_f32 v189, 0, v189\n"
        "v_cvt_pk_bf16_f32 v191, 0, v191\n"
        "v_mfma_f32_16x16x32_bf16 a[184:187], v[8:11], v[56:59], a[184:187]\n"
        "s_waitcnt vmcnt(10) lgkmcnt(0)\n"
        "v_mul_f32 v190, v179, v187\n"
        "v_mul_f32 v192, v180, v187\n"
        "v_mfma_f32_16x16x32_bf16 a[188:191], v[8:11], v[60:63], a[188:191]\n"
        "s_barrier\n"
        "v_mfma_f32_16x16x32_bf16 a[188:191], v[12:15], v[92:95], a[188:191]\n"
        "global_load_lds_dwordx4 v234, s[56:57] offset:0\n"
        "ds_read_b128 v[8:11], v224 offset:18432\n"
        "v_mfma_f32_16x16x32_bf16 a[156:159], v[4:7], v[92:95], a[156:159]\n"
        "ds_read_b128 v[4:7], v225 offset:16384\n"
        "v_cvt_pk_bf16_f32 v190, 0, v190\n"
        "v_mfma_f32_16x16x32_bf16 a[156:159], v[0:3], v[60:63], a[156:159]\n"
        "ds_read_b128 v[0:3], v224 offset:16384\n"
        "v_cvt_pk_bf16_f32 v192, 0, v192\n"
        "v_mfma_f32_16x16x32_bf16 a[192:195], v[32:35], v[16:19], a[192:195]\n"
        "ds_read_b128 v[12:15], v225 offset:18432\n"
        "v_add_f32 v189, v189, v190\n"
        "v_mfma_f32_16x16x32_bf16 a[192:195], v[64:67], v[20:23], a[192:195]\n"
        "global_load_lds_dwordx4 v235, s[56:57] offset:1024\n"
        "v_add_f32 v191, v191, v192\n"
        "v_mfma_f32_16x16x32_bf16 a[224:227], v[64:67], v[28:31], a[224:227]\n"
        "ds_read_b64_tr_b16 v[64:65], v226 offset:8192\n"
        "v_cvt_pk_bf16_f32 v189, 0, v189\n"
        "v_mfma_f32_16x16x32_bf16 a[224:227], v[32:35], v[24:27], a[224:227]\n"
        "ds_read_b64_tr_b16 v[32:33], v226 offset:0\n"
        "v_cvt_pk_bf16_f32 v191, 0, v191\n"
        "v_mfma_f32_16x16x32_bf16 a[228:231], v[36:39], v[24:27], a[228:231]\n"
        "ds_read_b64_tr_b16 v[34:35], v227 offset:0\n"
        "v_mul_f32 v190, v180, v186\n"
        "v_mfma_f32_16x16x32_bf16 a[228:231], v[68:71], v[28:31], a[228:231]\n"
        "ds_read_b64_tr_b16 v[66:67], v227 offset:8192\n"
        "global_load_lds_dwordx4 v236, s[56:57] offset:2048\n"
        "v_mfma_f32_16x16x32_bf16 a[196:199], v[68:71], v[20:23], a[196:199]\n"
        "ds_read_b64_tr_b16 v[68:69], v228 offset:8192\n"
        "v_mul_f32 v192, v181, v186\n"
        "v_mfma_f32_16x16x32_bf16 a[196:199], v[36:39], v[16:19], a[196:199]\n"
        "ds_read_b64_tr_b16 v[36:37], v228 offset:0\n"
        "v_cvt_pk_bf16_f32 v190, 0, v190\n"
        "v_mfma_f32_16x16x32_bf16 a[200:203], v[40:43], v[16:19], a[200:203]\n"
        "ds_read_b64_tr_b16 v[38:39], v229 offset:0\n"
        "v_cvt_pk_bf16_f32 v192, 0, v192\n"
        "v_mfma_f32_16x16x32_bf16 a[200:203], v[72:75], v[20:23], a[200:203]\n"
        "ds_read_b64_tr_b16 v[70:71], v229 offset:8192\n"
        "global_load_lds_dwordx4 v237, s[56:57] offset:3072\n"
        "s_add_u32 m0, s72, 0x9000\n"
        "v_mfma_f32_16x16x32_bf16 a[232:235], v[72:75], v[28:31], a[232:235]\n"
        "ds_read_b64_tr_b16 v[72:73], v230 offset:8192\n"
        "v_add_f32 v189, v189, v190\n"
        "v_mfma_f32_16x16x32_bf16 a[232:235], v[40:43], v[24:27], a[232:235]\n"
        "ds_read_b64_tr_b16 v[40:41], v230 offset:0\n"
        "v_add_f32 v191, v191, v192\n"
        "v_mfma_f32_16x16x32_bf16 a[236:239], v[44:47], v[24:27], a[236:239]\n"
        "ds_read_b64_tr_b16 v[42:43], v231 offset:0\n"
        "v_cvt_pk_bf16_f32 v189, 0, v189\n"
        "v_mfma_f32_16x16x32_bf16 a[236:239], v[76:79], v[28:31], a[236:239]\n"
        "ds_read_b64_tr_b16 v[74:75], v231 offset:8192\n"
        "global_load_lds_dwordx4 v234, s[58:59] offset:0\n"
        "v_mfma_f32_16x16x32_bf16 a[204:207], v[76:79], v[20:23], a[204:207]\n"
        "ds_read_b64_tr_b16 v[76:77], v232 offset:8192\n"
        "v_cvt_pk_bf16_f32 v191, 0, v191\n"
        "v_mfma_f32_16x16x32_bf16 a[204:207], v[44:47], v[16:19], a[204:207]\n"
        "ds_read_b64_tr_b16 v[44:45], v232 offset:0\n"
        "v_mul_f32 v190, v181, v185\n"
        "v_mfma_f32_16x16x32_bf16 a[208:211], v[16:19], v[48:51], a[208:211]\n"
        "ds_read_b64_tr_b16 v[46:47], v233 offset:0\n"
        "v_mul_f32 v192, v182, v185\n"
        "v_mfma_f32_16x16x32_bf16 a[208:211], v[20:23], v[80:83], a[208:211]\n"
        "ds_read_b64_tr_b16 v[78:79], v233 offset:8192\n"
        "global_load_lds_dwordx4 v235, s[58:59] offset:1024\n"
        "v_mfma_f32_16x16x32_bf16 a[240:243], v[28:31], v[80:83], a[240:243]\n"
        "ds_read_b64_tr_b16 v[80:81], v226 offset:24576\n"
        "v_cvt_pk_bf16_f32 v190, 0, v190\n"
        "v_mfma_f32_16x16x32_bf16 a[240:243], v[24:27], v[48:51], a[240:243]\n"
        "ds_read_b64_tr_b16 v[48:49], v226 offset:16384\n"
        "v_cvt_pk_bf16_f32 v192, 0, v192\n"
        "v_mfma_f32_16x16x32_bf16 a[244:247], v[24:27], v[52:55], a[244:247]\n"
        "ds_read_b64_tr_b16 v[50:51], v227 offset:16384\n"
        "v_add_f32 v189, v189, v190\n"
        "v_mfma_f32_16x16x32_bf16 a[244:247], v[28:31], v[84:87], a[244:247]\n"
        "ds_read_b64_tr_b16 v[82:83], v227 offset:24576\n"
        "global_load_lds_dwordx4 v236, s[58:59] offset:2048\n"
        "v_mfma_f32_16x16x32_bf16 a[212:215], v[20:23], v[84:87], a[212:215]\n"
        "ds_read_b64_tr_b16 v[84:85], v228 offset:24576\n"
        "v_add_f32 v191, v191, v192\n"
        "v_mfma_f32_16x16x32_bf16 a[212:215], v[16:19], v[52:55], a[212:215]\n"
        "ds_read_b64_tr_b16 v[52:53], v228 offset:16384\n"
        "v_cvt_pk_bf16_f32 v189, 0, v189\n"
        "v_mfma_f32_16x16x32_bf16 a[216:219], v[16:19], v[56:59], a[216:219]\n"
        "ds_read_b64_tr_b16 v[54:55], v229 offset:16384\n"
        "v_cvt_pk_bf16_f32 v191, 0, v191\n"
        "v_mfma_f32_16x16x32_bf16 a[216:219], v[20:23], v[88:91], a[216:219]\n"
        "ds_read_b64_tr_b16 v[86:87], v229 offset:24576\n"
        "global_load_lds_dwordx4 v237, s[58:59] offset:3072\n"
        "s_add_u32 m0, s73, 0x20000\n"
        "v_mfma_f32_16x16x32_bf16 a[248:251], v[28:31], v[88:91], a[248:251]\n"
        "ds_read_b64_tr_b16 v[88:89], v230 offset:24576\n"
        "v_and_b32 v190, s35, v127\n"
        "v_mfma_f32_16x16x32_bf16 a[248:251], v[24:27], v[56:59], a[248:251]\n"
        "ds_read_b64_tr_b16 v[56:57], v230 offset:16384\n"
        "v_and_b32 v192, s35, v129\n"
        "v_mfma_f32_16x16x32_bf16 a[252:255], v[24:27], v[60:63], a[252:255]\n"
        "ds_read_b64_tr_b16 v[58:59], v231 offset:16384\n"
        "v_mul_f32 v189, v189, v190\n"
        "v_mfma_f32_16x16x32_bf16 a[252:255], v[28:31], v[92:95], a[252:255]\n"
        "ds_read_b64_tr_b16 v[90:91], v231 offset:24576\n"
        "global_load_lds_dwordx4 v238, s[68:69] offset:0\n"
        "v_mfma_f32_16x16x32_bf16 a[220:223], v[20:23], v[92:95], a[220:223]\n"
        "ds_read_b64_tr_b16 v[92:93], v232 offset:24576\n"
        "v_mul_f32 v191, v191, v192\n"
        "v_mfma_f32_16x16x32_bf16 a[220:223], v[16:19], v[60:63], a[220:223]\n"
        "ds_read_b64_tr_b16 v[60:61], v232 offset:16384\n"
        "s_waitcnt lgkmcnt(8)\n"
        "v_mfma_f32_16x16x32_bf16 a[0:3], v[32:35], v[0:3], a[0:3]\n"
        "ds_read_b64_tr_b16 v[62:63], v233 offset:16384\n"
        "v_cvt_pk_bf16_f32 v209, v191, v189\n"
        "v_mfma_f32_16x16x32_bf16 a[0:3], v[64:67], v[4:7], a[0:3]\n"
        "ds_read_b64_tr_b16 v[94:95], v233 offset:24576\n"
        "global_load_lds_dwordx4 v239, s[68:69] offset:1024\n"
        "v_mfma_f32_16x16x32_bf16 a[32:35], v[64:67], v[12:15], a[32:35]\n"
        "ds_read_b128 v[16:19], v224 offset:20480\n"
        "v_mul_f32 v193, v180, v188\n"
        "v_mfma_f32_16x16x32_bf16 a[32:35], v[32:35], v[8:11], a[32:35]\n"
        "ds_read_b128 v[20:23], v225 offset:20480\n"
        "v_mul_f32 v195, v181, v188\n"
        "v_mfma_f32_16x16x32_bf16 a[36:39], v[36:39], v[8:11], a[36:39]\n"
        "ds_read_b128 v[24:27], v224 offset:22528\n"
        "v_cvt_pk_bf16_f32 v193, 0, v193\n"
        "v_mfma_f32_16x16x32_bf16 a[36:39], v[68:71], v[12:15], a[36:39]\n"
        "ds_read_b128 v[28:31], v225 offset:22528\n"
        "global_load_lds_dwordx4 v240, s[68:69] offset:2048\n"
        "v_mfma_f32_16x16x32_bf16 a[4:7], v[68:71], v[4:7], a[4:7]\n"
        "v_cvt_pk_bf16_f32 v195, 0, v195\n"
        "v_mul_f32 v194, v181, v187\n"
        "v_mfma_f32_16x16x32_bf16 a[4:7], v[36:39], v[0:3], a[4:7]\n"
        "v_mul_f32 v196, v182, v187\n"
        "v_cvt_pk_bf16_f32 v194, 0, v194\n"
        "v_mfma_f32_16x16x32_bf16 a[8:11], v[40:43], v[0:3], a[8:11]\n"
        "v_cvt_pk_bf16_f32 v196, 0, v196\n"
        "v_add_f32 v193, v193, v194\n"
        "v_mfma_f32_16x16x32_bf16 a[8:11], v[72:75], v[4:7], a[8:11]\n"
        "global_load_dword v253, v173, s[82:83] offset:0\n"
        "v_mfma_f32_16x16x32_bf16 a[40:43], v[72:75], v[12:15], a[40:43]\n"
        "global_load_lds_dwordx4 v241, s[68:69] offset:3072\n"
        "s_add_u32 s68, s68, 0x80000\n"
        "s_addc_u32 s69, s69, 0\n"
        "v_mfma_f32_16x16x32_bf16 a[40:43], v[40:43], v[8:11], a[40:43]\n"
        "s_add_u32 m0, s73, 0x24000\n"
        "s_add_u32 s82, s12, 0x700000\n"
        "s_addc_u32 s83, s13, 0\n"
        "v_mfma_f32_16x16x32_bf16 a[44:47], v[44:47], v[8:11], a[44:47]\n"
        "v_add_f32 v195, v195, v196\n"
        "v_cvt_pk_bf16_f32 v193, 0, v193\n"
        "v_mfma_f32_16x16x32_bf16 a[44:47], v[76:79], v[12:15], a[44:47]\n"
        "v_cvt_pk_bf16_f32 v195, 0, v195\n"
        "v_mul_f32 v194, v182, v186\n"
        "v_mfma_f32_16x16x32_bf16 a[12:15], v[76:79], v[4:7], a[12:15]\n"
        "global_load_lds_dwordx4 v238, s[68:69] offset:0\n"
        "v_mul_f32 v196, v183, v186\n"
        "v_mfma_f32_16x16x32_bf16 a[12:15], v[44:47], v[0:3], a[12:15]\n"
        "v_cvt_pk_bf16_f32 v194, 0, v194\n"
        "v_cvt_pk_bf16_f32 v196, 0, v196\n"
        "v_mfma_f32_16x16x32_bf16 a[16:19], v[0:3], v[48:51], a[16:19]\n"
        "s_waitcnt lgkmcnt(0)\n"
        "v_add_f32 v193, v193, v194\n"
        "v_add_f32 v195, v195, v196\n"
        "v_mfma_f32_16x16x32_bf16 a[16:19], v[4:7], v[80:83], a[16:19]\n"
        "v_cvt_pk_bf16_f32 v193, 0, v193\n"
        "v_cvt_pk_bf16_f32 v195, 0, v195\n"
        "v_mfma_f32_16x16x32_bf16 a[48:51], v[12:15], v[80:83], a[48:51]\n"
        "global_load_lds_dwordx4 v239, s[68:69] offset:1024\n"
        "v_mul_f32 v194, v183, v185\n"
        "v_mfma_f32_16x16x32_bf16 a[48:51], v[8:11], v[48:51], a[48:51]\n"
        "v_mul_f32 v196, v184, v185\n"
        "v_cvt_pk_bf16_f32 v194, 0, v194\n"
        "v_mfma_f32_16x16x32_bf16 a[52:55], v[8:11], v[52:55], a[52:55]\n"
        "v_cvt_pk_bf16_f32 v196, 0, v196\n"
        "v_add_f32 v193, v193, v194\n"
        "v_mfma_f32_16x16x32_bf16 a[52:55], v[12:15], v[84:87], a[52:55]\n"
        "v_add_f32 v195, v195, v196\n"
        "v_cvt_pk_bf16_f32 v193, 0, v193\n"
        "v_mfma_f32_16x16x32_bf16 a[20:23], v[4:7], v[84:87], a[20:23]\n"
        "global_load_lds_dwordx4 v240, s[68:69] offset:2048\n"
        "v_cvt_pk_bf16_f32 v195, 0, v195\n"
        "v_mfma_f32_16x16x32_bf16 a[20:23], v[0:3], v[52:55], a[20:23]\n"
        "s_and_b32 s53, s40, 1\n"
        "v_and_b32 v194, s35, v131\n"
        "v_and_b32 v196, s35, v133\n"
        "v_mfma_f32_16x16x32_bf16 a[24:27], v[0:3], v[56:59], a[24:27]\n"
        "s_lshl_b32 s53, s53, 10\n"
        "v_mul_f32 v193, v193, v194\n"
        "v_mul_f32 v195, v195, v196\n"
        "v_mfma_f32_16x16x32_bf16 a[24:27], v[4:7], v[88:91], a[24:27]\n"
        "s_sub_u32 s53, 0x8000, s53\n"
        "v_cvt_pk_bf16_f32 v208, v195, v193\n"
        "v_readfirstlane_b32 s86, v250\n"
        "v_mfma_f32_16x16x32_bf16 a[56:59], v[12:15], v[88:91], a[56:59]\n"
        "global_load_lds_dwordx4 v241, s[68:69] offset:3072\n"
        "s_add_u32 s68, s68, 0x80000\n"
        "s_addc_u32 s69, s69, 0\n"
        "v_mfma_f32_16x16x32_bf16 a[56:59], v[8:11], v[56:59], a[56:59]\n"
        "s_add_u32 m0, s72, 0x0\n"
        "v_readfirstlane_b32 s87, v251\n"
        "v_lshlrev_b32 v174, 16, v158\n"
        "v_mfma_f32_16x16x32_bf16 a[60:63], v[8:11], v[60:63], a[60:63]\n"
        "ds_read_b128 v[8:11], v224 offset:2048\n"
        "s_add_u32 s86, s86, 0x1\n"
        "s_addc_u32 s87, s87, 0\n"
        "v_mfma_f32_16x16x32_bf16 a[60:63], v[12:15], v[92:95], a[60:63]\n"
        "ds_read_b128 v[12:15], v225 offset:2048\n"
        "v_mov_b32 v250, s86\n"
        "v_mfma_f32_16x16x32_bf16 a[28:31], v[4:7], v[92:95], a[28:31]\n"
        "ds_read_b128 v[4:7], v225 offset:0\n"
        "v_mov_b32 v251, s87\n"
        "v_mfma_f32_16x16x32_bf16 a[28:31], v[0:3], v[60:63], a[28:31]\n"
        "ds_read_b128 v[0:3], v224 offset:0\n"
        "v_and_b32 v175, s35, v158\n"
        "v_mfma_f32_16x16x32_bf16 a[64:67], v[32:35], v[16:19], a[64:67]\n"
        "global_store_dwordx4 v171, v[208:211], s[76:77] offset:0 sc1\n"
        "s_add_u32 s76, s38, 0x3b20040\n"
        "s_addc_u32 s77, s39, 0\n"
        "v_lshlrev_b32 v176, 16, v159\n"
        "v_and_b32 v177, s35, v159\n"
        "v_lshlrev_b32 v178, 16, v160\n"
        "v_mfma_f32_16x16x32_bf16 a[64:67], v[64:67], v[20:23], a[64:67]\n"
        "v_and_b32 v179, s35, v160\n"
        "v_lshlrev_b32 v180, 16, v161\n"
        "v_mfma_f32_16x16x32_bf16 a[96:99], v[64:67], v[28:31], a[96:99]\n"
        "v_and_b32 v181, s35, v161\n"
        "v_lshlrev_b32 v182, 16, v162\n"
        "v_mfma_f32_16x16x32_bf16 a[96:99], v[32:35], v[24:27], a[96:99]\n"
        "v_and_b32 v183, s35, v162\n"
        "v_lshlrev_b32 v184, 16, v163\n"
        "v_mfma_f32_16x16x32_bf16 a[100:103], v[36:39], v[24:27], a[100:103]\n"
        "v_and_b32 v185, s35, v163\n"
        "v_lshlrev_b32 v186, 16, v164\n"
        "v_mfma_f32_16x16x32_bf16 a[100:103], v[68:71], v[28:31], a[100:103]\n"
        "v_and_b32 v187, s35, v164\n"
        "v_lshlrev_b32 v188, 16, v165\n"
        "v_mfma_f32_16x16x32_bf16 a[68:71], v[68:71], v[20:23], a[68:71]\n"
        "v_and_b32 v189, s35, v165\n"
        "v_lshlrev_b32 v192, 16, v142\n"
        "v_mfma_f32_16x16x32_bf16 a[68:71], v[36:39], v[16:19], a[68:71]\n"
        "v_and_b32 v193, s35, v142\n"
        "v_mul_f32 v192, v192, v174\n"
        "v_mfma_f32_16x16x32_bf16 a[72:75], v[40:43], v[16:19], a[72:75]\n"
        "v_mul_f32 v193, v193, v175\n"
        "v_cvt_pk_bf16_f32 v192, 0, v192\n"
        "v_mfma_f32_16x16x32_bf16 a[72:75], v[72:75], v[20:23], a[72:75]\n"
        "v_cvt_pk_bf16_f32 v193, 0, v193\n"
        "v_add_f32 v190, v192, v193\n"
        "v_mfma_f32_16x16x32_bf16 a[104:107], v[72:75], v[28:31], a[104:107]\n"
        "v_lshlrev_b32 v194, 16, v143\n"
        "v_and_b32 v195, s35, v143\n"
        "v_mfma_f32_16x16x32_bf16 a[104:107], v[40:43], v[24:27], a[104:107]\n"
        "v_mul_f32 v194, v194, v176\n"
        "v_mul_f32 v195, v195, v177\n"
        "v_mfma_f32_16x16x32_bf16 a[108:111], v[44:47], v[24:27], a[108:111]\n"
        "v_cvt_pk_bf16_f32 v194, 0, v194\n"
        "v_cvt_pk_bf16_f32 v195, 0, v195\n"
        "v_mfma_f32_16x16x32_bf16 a[108:111], v[76:79], v[28:31], a[108:111]\n"
        "v_add_f32 v194, v194, v195\n"
        "v_add_f32 v190, v190, v194\n"
        "v_mfma_f32_16x16x32_bf16 a[76:79], v[76:79], v[20:23], a[76:79]\n"
        "v_lshlrev_b32 v192, 16, v144\n"
        "v_and_b32 v193, s35, v144\n"
        "v_mfma_f32_16x16x32_bf16 a[76:79], v[44:47], v[16:19], a[76:79]\n"
        "v_mul_f32 v192, v192, v178\n"
        "v_mul_f32 v193, v193, v179\n"
        "v_mfma_f32_16x16x32_bf16 a[80:83], v[16:19], v[48:51], a[80:83]\n"
        "LHA3_%=:\n"
        "s_cbranch_execz LHA7_%=\n"
        "v_cvt_pk_bf16_f32 v192, 0, v192\n"
        "v_cvt_pk_bf16_f32 v193, 0, v193\n"
        "v_mfma_f32_16x16x32_bf16 a[80:83], v[20:23], v[80:83], a[80:83]\n"
        "v_add_f32 v192, v192, v193\n"
        "v_add_f32 v190, v190, v192\n"
        "v_mfma_f32_16x16x32_bf16 a[112:115], v[28:31], v[80:83], a[112:115]\n"
        "v_lshlrev_b32 v194, 16, v145\n"
        "v_and_b32 v195, s35, v145\n"
        "v_mfma_f32_16x16x32_bf16 a[112:115], v[24:27], v[48:51], a[112:115]\n"
        "v_mul_f32 v194, v194, v180\n"
        "v_mul_f32 v195, v195, v181\n"
        "v_mfma_f32_16x16x32_bf16 a[116:119], v[24:27], v[52:55], a[116:119]\n"
        "v_cvt_pk_bf16_f32 v194, 0, v194\n"
        "v_cvt_pk_bf16_f32 v195, 0, v195\n"
        "v_mfma_f32_16x16x32_bf16 a[116:119], v[28:31], v[84:87], a[116:119]\n"
        "v_add_f32 v194, v194, v195\n"
        "v_add_f32 v190, v190, v194\n"
        "v_mfma_f32_16x16x32_bf16 a[84:87], v[20:23], v[84:87], a[84:87]\n"
        "v_lshlrev_b32 v192, 16, v146\n"
        "v_and_b32 v193, s35, v146\n"
        "v_mfma_f32_16x16x32_bf16 a[84:87], v[16:19], v[52:55], a[84:87]\n"
        "s_mov_b32 s88, 0x3e0293ee\n"
        "v_mul_f32 v192, v192, v182\n"
        "v_mul_f32 v193, v193, v183\n"
        "v_mfma_f32_16x16x32_bf16 a[88:91], v[16:19], v[56:59], a[88:91]\n"
        "s_sub_u32 s75, s45, s44\n"
        "v_cvt_pk_bf16_f32 v192, 0, v192\n"
        "v_cvt_pk_bf16_f32 v193, 0, v193\n"
        "v_mfma_f32_16x16x32_bf16 a[88:91], v[20:23], v[88:91], a[88:91]\n"
        "s_lshl_b32 s75, s75, 15\n"
        "v_add_f32 v192, v192, v193\n"
        "v_add_f32 v190, v190, v192\n"
        "v_mfma_f32_16x16x32_bf16 a[120:123], v[28:31], v[88:91], a[120:123]\n"
        "s_lshr_b32 s50, s40, 1\n"
        "v_lshlrev_b32 v194, 16, v147\n"
        "v_and_b32 v195, s35, v147\n"
        "v_mfma_f32_16x16x32_bf16 a[120:123], v[24:27], v[56:59], a[120:123]\n"
        "s_lshl1_add_u32 s50, s44, s50\n"
        "v_mul_f32 v194, v194, v184\n"
        "v_mul_f32 v195, v195, v185\n"
        "v_mfma_f32_16x16x32_bf16 a[124:127], v[24:27], v[60:63], a[124:127]\n"
        "ds_read_b128 v[24:27], v224 offset:6144\n"
        "s_lshl_b32 s50, s50, 14\n"
        "v_cvt_pk_bf16_f32 v194, 0, v194\n"
        "v_mfma_f32_16x16x32_bf16 a[124:127], v[28:31], v[92:95], a[124:127]\n"
        "ds_read_b128 v[28:31], v225 offset:6144\n"
        "s_mul_i32 s51, s43, 529408\n"
        "v_cvt_pk_bf16_f32 v195, 0, v195\n"
        "v_mfma_f32_16x16x32_bf16 a[92:95], v[20:23], v[92:95], a[92:95]\n"
        "ds_read_b128 v[20:23], v225 offset:4096\n"
        "s_add_u32 s50, s50, s51\n"
        "v_add_f32 v194, v194, v195\n"
        "v_mfma_f32_16x16x32_bf16 a[92:95], v[16:19], v[60:63], a[92:95]\n"
        "ds_read_b128 v[16:19], v224 offset:4096\n"
        "s_and_b32 s51, s40, 1\n"
        "s_waitcnt lgkmcnt(4)\n"
        "v_mfma_f32_16x16x32_bf16 a[128:131], v[32:35], v[0:3], a[128:131]\n"
        "s_lshl_b32 s51, s51, 11\n"
        "v_add_f32 v190, v190, v194\n"
        "v_lshlrev_b32 v192, 16, v148\n"
        "v_mfma_f32_16x16x32_bf16 a[128:131], v[64:67], v[4:7], a[128:131]\n"
        "s_add_u32 s52, s50, s51\n"
        "v_and_b32 v193, s35, v148\n"
        "v_mul_f32 v192, v192, v186\n"
        "v_mfma_f32_16x16x32_bf16 a[160:163], v[64:67], v[12:15], a[160:163]\n"
        "v_mul_f32 v193, v193, v187\n"
        "v_cvt_pk_bf16_f32 v192, 0, v192\n"
        "v_mfma_f32_16x16x32_bf16 a[160:163], v[32:35], v[8:11], a[160:163]\n"
        "v_cvt_pk_bf16_f32 v193, 0, v193\n"
        "v_add_f32 v192, v192, v193\n"
        "v_mfma_f32_16x16x32_bf16 a[164:167], v[36:39], v[8:11], a[164:167]\n"
        "v_add_f32 v190, v190, v192\n"
        "v_lshlrev_b32 v194, 16, v149\n"
        "v_mfma_f32_16x16x32_bf16 a[164:167], v[68:71], v[12:15], a[164:167]\n"
        "v_and_b32 v195, s35, v149\n"
        "v_mul_f32 v194, v194, v188\n"
        "v_mfma_f32_16x16x32_bf16 a[132:135], v[68:71], v[4:7], a[132:135]\n"
        "v_mul_f32 v195, v195, v189\n"
        "v_cvt_pk_bf16_f32 v194, 0, v194\n"
        "v_mfma_f32_16x16x32_bf16 a[132:135], v[36:39], v[0:3], a[132:135]\n"
        "v_cvt_pk_bf16_f32 v195, 0, v195\n"
        "v_add_f32 v194, v194, v195\n"
        "v_mfma_f32_16x16x32_bf16 a[136:139], v[40:43], v[0:3], a[136:139]\n"
        "v_add_f32 v190, v190, v194\n"
        "v_lshlrev_b32 v192, 16, v150\n"
        "v_mfma_f32_16x16x32_bf16 a[136:139], v[72:75], v[4:7], a[136:139]\n"
        "v_and_b32 v193, s35, v150\n"
        "v_mul_f32 v192, v192, v174\n"
        "v_mfma_f32_16x16x32_bf16 a[168:171], v[72:75], v[12:15], a[168:171]\n"
        "v_mul_f32 v193, v193, v175\n"
        "v_cvt_pk_bf16_f32 v192, 0, v192\n"
        "v_mfma_f32_16x16x32_bf16 a[168:171], v[40:43], v[8:11], a[168:171]\n"
        "v_cvt_pk_bf16_f32 v193, 0, v193\n"
        "v_add_f32 v191, v192, v193\n"
        "v_mfma_f32_16x16x32_bf16 a[172:175], v[44:47], v[8:11], a[172:175]\n"
        "v_lshlrev_b32 v194, 16, v151\n"
        "v_and_b32 v195, s35, v151\n"
        "v_mfma_f32_16x16x32_bf16 a[172:175], v[76:79], v[12:15], a[172:175]\n"
        "v_mul_f32 v194, v194, v176\n"
        "v_mul_f32 v195, v195, v177\n"
        "v_mfma_f32_16x16x32_bf16 a[140:143], v[76:79], v[4:7], a[140:143]\n"
        "v_cvt_pk_bf16_f32 v194, 0, v194\n"
        "v_cvt_pk_bf16_f32 v195, 0, v195\n"
        "v_mfma_f32_16x16x32_bf16 a[140:143], v[44:47], v[0:3], a[140:143]\n"
        "v_add_f32 v194, v194, v195\n"
        "v_add_f32 v191, v191, v194\n"
        "v_mfma_f32_16x16x32_bf16 a[144:147], v[0:3], v[48:51], a[144:147]\n"
        "v_lshlrev_b32 v192, 16, v152\n"
        "v_and_b32 v193, s35, v152\n"
        "v_mfma_f32_16x16x32_bf16 a[144:147], v[4:7], v[80:83], a[144:147]\n"
        "v_mul_f32 v192, v192, v178\n"
        "v_mul_f32 v193, v193, v179\n"
        "v_mfma_f32_16x16x32_bf16 a[176:179], v[12:15], v[80:83], a[176:179]\n"
        "v_cvt_pk_bf16_f32 v192, 0, v192\n"
        "v_cvt_pk_bf16_f32 v193, 0, v193\n"
        "v_mfma_f32_16x16x32_bf16 a[176:179], v[8:11], v[48:51], a[176:179]\n"
        "v_add_f32 v192, v192, v193\n"
        "v_add_f32 v191, v191, v192\n"
        "v_mfma_f32_16x16x32_bf16 a[180:183], v[8:11], v[52:55], a[180:183]\n"
        "v_lshlrev_b32 v194, 16, v153\n"
        "v_and_b32 v195, s35, v153\n"
        "v_mfma_f32_16x16x32_bf16 a[180:183], v[12:15], v[84:87], a[180:183]\n"
        "v_mul_f32 v194, v194, v180\n"
        "v_mul_f32 v195, v195, v181\n"
        "v_mfma_f32_16x16x32_bf16 a[148:151], v[4:7], v[84:87], a[148:151]\n"
        "v_cvt_pk_bf16_f32 v194, 0, v194\n"
        "v_cvt_pk_bf16_f32 v195, 0, v195\n"
        "v_mfma_f32_16x16x32_bf16 a[148:151], v[0:3], v[52:55], a[148:151]\n"
        "global_load_dword v253, v173, s[82:83] offset:0\n"
        "v_add_f32 v194, v194, v195\n"
        "v_mfma_f32_16x16x32_bf16 a[152:155], v[0:3], v[56:59], a[152:155]\n"
        "v_add_f32 v191, v191, v194\n"
        "v_lshlrev_b32 v192, 16, v154\n"
        "v_mfma_f32_16x16x32_bf16 a[152:155], v[4:7], v[88:91], a[152:155]\n"
        "v_and_b32 v193, s35, v154\n"
        "v_mul_f32 v192, v192, v182\n"
        "v_mfma_f32_16x16x32_bf16 a[184:187], v[12:15], v[88:91], a[184:187]\n"
        "v_mul_f32 v193, v193, v183\n"
        "v_cvt_pk_bf16_f32 v192, 0, v192\n"
        "v_mfma_f32_16x16x32_bf16 a[184:187], v[8:11], v[56:59], a[184:187]\n"
        "s_waitcnt vmcnt(11) lgkmcnt(0)\n"
        "v_cvt_pk_bf16_f32 v193, 0, v193\n"
        "v_add_f32 v192, v192, v193\n"
        "v_mfma_f32_16x16x32_bf16 a[188:191], v[8:11], v[60:63], a[188:191]\n"
        "s_barrier\n"
        "v_mfma_f32_16x16x32_bf16 a[188:191], v[12:15], v[92:95], a[188:191]\n"
        "global_load_lds_dwordx4 v242, s[64:65] offset:0\n"
        "ds_read_b128 v[8:11], v224 offset:51200\n"
        "v_mfma_f32_16x16x32_bf16 a[156:159], v[4:7], v[92:95], a[156:159]\n"
        "ds_read_b128 v[4:7], v225 offset:49152\n"
        "v_add_f32 v191, v191, v192\n"
        "v_mfma_f32_16x16x32_bf16 a[156:159], v[0:3], v[60:63], a[156:159]\n"
        "ds_read_b128 v[0:3], v224 offset:49152\n"
        "v_lshlrev_b32 v194, 16, v155\n"
        "v_mfma_f32_16x16x32_bf16 a[192:195], v[32:35], v[16:19], a[192:195]\n"
        "ds_read_b128 v[12:15], v225 offset:51200\n"
        "v_and_b32 v195, s35, v155\n"
        "v_mfma_f32_16x16x32_bf16 a[192:195], v[64:67], v[20:23], a[192:195]\n"
        "global_load_lds_dwordx4 v243, s[64:65] offset:1024\n"
        "v_mul_f32 v194, v194, v184\n"
        "v_mfma_f32_16x16x32_bf16 a[224:227], v[64:67], v[28:31], a[224:227]\n"
        "ds_read_b64_tr_b16 v[64:65], v226 offset:40960\n"
        "v_mul_f32 v195, v195, v185\n"
        "v_mfma_f32_16x16x32_bf16 a[224:227], v[32:35], v[24:27], a[224:227]\n"
        "ds_read_b64_tr_b16 v[32:33], v226 offset:32768\n"
        "s_add_u32 s97, s52, 0x3000\n"
        "v_cvt_pk_bf16_f32 v194, 0, v194\n"
        "v_mfma_f32_16x16x32_bf16 a[228:231], v[36:39], v[24:27], a[228:231]\n"
        "ds_read_b64_tr_b16 v[34:35], v227 offset:32768\n"
        "v_cvt_pk_bf16_f32 v195, 0, v195\n"
        "v_mfma_f32_16x16x32_bf16 a[228:231], v[68:71], v[28:31], a[228:231]\n"
        "ds_read_b64_tr_b16 v[66:67], v227 offset:40960\n"
        "global_load_lds_dwordx4 v244, s[64:65] offset:2048\n"
        "v_mfma_f32_16x16x32_bf16 a[196:199], v[68:71], v[20:23], a[196:199]\n"
        "ds_read_b64_tr_b16 v[68:69], v228 offset:40960\n"
        "v_add_f32 v194, v194, v195\n"
        "v_mfma_f32_16x16x32_bf16 a[196:199], v[36:39], v[16:19], a[196:199]\n"
        "ds_read_b64_tr_b16 v[36:37], v228 offset:32768\n"
        "v_add_f32 v191, v191, v194\n"
        "v_mfma_f32_16x16x32_bf16 a[200:203], v[40:43], v[16:19], a[200:203]\n"
        "ds_read_b64_tr_b16 v[38:39], v229 offset:32768\n"
        "v_lshlrev_b32 v192, 16, v156\n"
        "v_mfma_f32_16x16x32_bf16 a[200:203], v[72:75], v[20:23], a[200:203]\n"
        "ds_read_b64_tr_b16 v[70:71], v229 offset:40960\n"
        "global_load_lds_dwordx4 v245, s[64:65] offset:3072\n"
        "s_add_u32 m0, s72, 0x1000\n"
        "v_mfma_f32_16x16x32_bf16 a[232:235], v[72:75], v[28:31], a[232:235]\n"
        "ds_read_b64_tr_b16 v[72:73], v230 offset:40960\n"
        "s_add_u32 s64, s64, 0x80\n"
        "s_addc_u32 s65, s65, 0\n"
        "v_mfma_f32_16x16x32_bf16 a[232:235], v[40:43], v[24:27], a[232:235]\n"
        "ds_read_b64_tr_b16 v[40:41], v230 offset:32768\n"
        "v_and_b32 v193, s35, v156\n"
        "v_mfma_f32_16x16x32_bf16 a[236:239], v[44:47], v[24:27], a[236:239]\n"
        "ds_read_b64_tr_b16 v[42:43], v231 offset:32768\n"
        "v_mul_f32 v192, v192, v186\n"
        "v_mfma_f32_16x16x32_bf16 a[236:239], v[76:79], v[28:31], a[236:239]\n"
        "ds_read_b64_tr_b16 v[74:75], v231 offset:40960\n"
        "global_load_lds_dwordx4 v242, s[66:67] offset:0\n"
        "v_mfma_f32_16x16x32_bf16 a[204:207], v[76:79], v[20:23], a[204:207]\n"
        "ds_read_b64_tr_b16 v[76:77], v232 offset:40960\n"
        "v_mul_f32 v193, v193, v187\n"
        "v_mfma_f32_16x16x32_bf16 a[204:207], v[44:47], v[16:19], a[204:207]\n"
        "ds_read_b64_tr_b16 v[44:45], v232 offset:32768\n"
        "v_cvt_pk_bf16_f32 v192, 0, v192\n"
        "v_mfma_f32_16x16x32_bf16 a[208:211], v[16:19], v[48:51], a[208:211]\n"
        "ds_read_b64_tr_b16 v[46:47], v233 offset:32768\n"
        "v_cvt_pk_bf16_f32 v193, 0, v193\n"
        "v_mfma_f32_16x16x32_bf16 a[208:211], v[20:23], v[80:83], a[208:211]\n"
        "ds_read_b64_tr_b16 v[78:79], v233 offset:40960\n"
        "global_load_lds_dwordx4 v243, s[66:67] offset:1024\n"
        "v_mfma_f32_16x16x32_bf16 a[240:243], v[28:31], v[80:83], a[240:243]\n"
        "ds_read_b64_tr_b16 v[80:81], v226 offset:57344\n"
        "v_add_f32 v192, v192, v193\n"
        "v_mfma_f32_16x16x32_bf16 a[240:243], v[24:27], v[48:51], a[240:243]\n"
        "ds_read_b64_tr_b16 v[48:49], v226 offset:49152\n"
        "v_add_f32 v191, v191, v192\n"
        "v_mfma_f32_16x16x32_bf16 a[244:247], v[24:27], v[52:55], a[244:247]\n"
        "ds_read_b64_tr_b16 v[50:51], v227 offset:49152\n"
        "v_lshlrev_b32 v194, 16, v157\n"
        "v_mfma_f32_16x16x32_bf16 a[244:247], v[28:31], v[84:87], a[244:247]\n"
        "ds_read_b64_tr_b16 v[82:83], v227 offset:57344\n"
        "global_load_lds_dwordx4 v244, s[66:67] offset:2048\n"
        "v_mfma_f32_16x16x32_bf16 a[212:215], v[20:23], v[84:87], a[212:215]\n"
        "ds_read_b64_tr_b16 v[84:85], v228 offset:57344\n"
        "v_and_b32 v195, s35, v157\n"
        "v_mfma_f32_16x16x32_bf16 a[212:215], v[16:19], v[52:55], a[212:215]\n"
        "ds_read_b64_tr_b16 v[52:53], v228 offset:49152\n"
        "v_mul_f32 v194, v194, v188\n"
        "v_mfma_f32_16x16x32_bf16 a[216:219], v[16:19], v[56:59], a[216:219]\n"
        "ds_read_b64_tr_b16 v[54:55], v229 offset:49152\n"
        "v_mul_f32 v195, v195, v189\n"
        "v_mfma_f32_16x16x32_bf16 a[216:219], v[20:23], v[88:91], a[216:219]\n"
        "ds_read_b64_tr_b16 v[86:87], v229 offset:57344\n"
        "global_load_lds_dwordx4 v245, s[66:67] offset:3072\n"
        "s_add_u32 m0, s72, 0x10000\n"
        "v_mfma_f32_16x16x32_bf16 a[248:251], v[28:31], v[88:91], a[248:251]\n"
        "ds_read_b64_tr_b16 v[88:89], v230 offset:57344\n"
        "s_add_u32 s66, s66, 0x80\n"
        "s_addc_u32 s67, s67, 0\n"
        "v_mfma_f32_16x16x32_bf16 a[248:251], v[24:27], v[56:59], a[248:251]\n"
        "ds_read_b64_tr_b16 v[56:57], v230 offset:49152\n"
        "v_cvt_pk_bf16_f32 v194, 0, v194\n"
        "v_mfma_f32_16x16x32_bf16 a[252:255], v[24:27], v[60:63], a[252:255]\n"
        "ds_read_b64_tr_b16 v[58:59], v231 offset:49152\n"
        "v_cvt_pk_bf16_f32 v195, 0, v195\n"
        "v_mfma_f32_16x16x32_bf16 a[252:255], v[28:31], v[92:95], a[252:255]\n"
        "ds_read_b64_tr_b16 v[90:91], v231 offset:57344\n"
        "global_load_lds_dwordx4 v242, s[64:65] offset:0\n"
        "v_mfma_f32_16x16x32_bf16 a[220:223], v[20:23], v[92:95], a[220:223]\n"
        "ds_read_b64_tr_b16 v[92:93], v232 offset:57344\n"
        "v_add_f32 v194, v194, v195\n"
        "v_mfma_f32_16x16x32_bf16 a[220:223], v[16:19], v[60:63], a[220:223]\n"
        "ds_read_b64_tr_b16 v[60:61], v232 offset:49152\n"
        "s_waitcnt lgkmcnt(8)\n"
        "v_mfma_f32_16x16x32_bf16 a[0:3], v[32:35], v[0:3], a[0:3]\n"
        "ds_read_b64_tr_b16 v[62:63], v233 offset:49152\n"
        "v_add_f32 v191, v191, v194\n"
        "v_mfma_f32_16x16x32_bf16 a[0:3], v[64:67], v[4:7], a[0:3]\n"
        "ds_read_b64_tr_b16 v[94:95], v233 offset:57344\n"
        "global_load_lds_dwordx4 v243, s[64:65] offset:1024\n"
        "v_mfma_f32_16x16x32_bf16 a[32:35], v[64:67], v[12:15], a[32:35]\n"
        "ds_read_b128 v[16:19], v224 offset:53248\n"
        "v_add_f32_dpp v190, v190, v190 row_shr:1 row_mask:0xf bank_mask:0xf bound_ctrl:1\n"
        "v_mfma_f32_16x16x32_bf16 a[32:35], v[32:35], v[8:11], a[32:35]\n"
        "ds_read_b128 v[20:23], v225 offset:53248\n"
        "v_add_f32_dpp v191, v191, v191 row_shr:1 row_mask:0xf bank_mask:0xf bound_ctrl:1\n"
        "v_mfma_f32_16x16x32_bf16 a[36:39], v[36:39], v[8:11], a[36:39]\n"
        "ds_read_b128 v[24:27], v224 offset:55296\n"
        "v_add_f32_dpp v190, v190, v190 row_shr:2 row_mask:0xf bank_mask:0xf bound_ctrl:1\n"
        "v_mfma_f32_16x16x32_bf16 a[36:39], v[68:71], v[12:15], a[36:39]\n"
        "ds_read_b128 v[28:31], v225 offset:55296\n"
        "global_load_lds_dwordx4 v244, s[64:65] offset:2048\n"
        "v_mfma_f32_16x16x32_bf16 a[4:7], v[68:71], v[4:7], a[4:7]\n"
        "s_add_u32 s70, s38, s97\n"
        "s_addc_u32 s71, s39, 0\n"
        "v_add_f32_dpp v191, v191, v191 row_shr:2 row_mask:0xf bank_mask:0xf bound_ctrl:1\n"
        "v_mfma_f32_16x16x32_bf16 a[4:7], v[36:39], v[0:3], a[4:7]\n"
        "v_add_f32_dpp v190, v190, v190 row_shr:4 row_mask:0xf bank_mask:0xf bound_ctrl:1\n"
        "v_add_f32_dpp v191, v191, v191 row_shr:4 row_mask:0xf bank_mask:0xf bound_ctrl:1\n"
        "v_mfma_f32_16x16x32_bf16 a[8:11], v[40:43], v[0:3], a[8:11]\n"
        "v_add_f32_dpp v190, v190, v190 row_shr:8 row_mask:0xf bank_mask:0xf bound_ctrl:1\n"
        "v_add_f32_dpp v191, v191, v191 row_shr:8 row_mask:0xf bank_mask:0xf bound_ctrl:1\n"
        "v_mfma_f32_16x16x32_bf16 a[8:11], v[72:75], v[4:7], a[8:11]\n"
        "global_load_lds_dwordx4 v245, s[64:65] offset:3072\n"
        "s_add_u32 m0, s72, 0x11000\n"
        "v_mfma_f32_16x16x32_bf16 a[40:43], v[72:75], v[12:15], a[40:43]\n"
        "s_add_u32 s64, s64, 0x80\n"
        "s_addc_u32 s65, s65, 0\n"
        "v_add_f32_dpp v190, v190, v190 row_bcast:15 row_mask:0xa bank_mask:0xf bound_ctrl:1\n"
        "v_mfma_f32_16x16x32_bf16 a[40:43], v[40:43], v[8:11], a[40:43]\n"
        "v_add_f32_dpp v191, v191, v191 row_bcast:15 row_mask:0xa bank_mask:0xf bound_ctrl:1\n"
        "v_add_f32_dpp v190, v190, v190 row_bcast:31 row_mask:0xc bank_mask:0xf bound_ctrl:1\n"
        "v_mfma_f32_16x16x32_bf16 a[44:47], v[44:47], v[8:11], a[44:47]\n"
        "v_add_f32_dpp v191, v191, v191 row_bcast:31 row_mask:0xc bank_mask:0xf bound_ctrl:1\n"
        "v_cvt_pk_bf16_f32 v190, 0, v190\n"
        "v_mfma_f32_16x16x32_bf16 a[44:47], v[76:79], v[12:15], a[44:47]\n"
        "global_load_lds_dwordx4 v242, s[66:67] offset:0\n"
        "v_mfma_f32_16x16x32_bf16 a[12:15], v[76:79], v[4:7], a[12:15]\n"
        "v_mul_f32 v190, 0xbfb8aa3b, v190\n"
        "v_lshrrev_b32 v192, 2, v254\n"
        "v_mfma_f32_16x16x32_bf16 a[12:15], v[44:47], v[0:3], a[12:15]\n"
        "v_exp_f32 v190, v190\n"
        "v_mfma_f32_16x16x32_bf16 a[16:19], v[0:3], v[48:51], a[16:19]\n"
        "s_waitcnt lgkmcnt(0)\n"
        "v_add_f32 v190, 1.0, v190\n"
        "v_add_u32 v192, s80, v192\n"
        "v_mfma_f32_16x16x32_bf16 a[16:19], v[4:7], v[80:83], a[16:19]\n"
        "global_load_lds_dwordx4 v243, s[66:67] offset:1024\n"
        "v_mfma_f32_16x16x32_bf16 a[48:51], v[12:15], v[80:83], a[48:51]\n"
        "v_rcp_f32 v190, v190\n"
        "v_mfma_f32_16x16x32_bf16 a[48:51], v[8:11], v[48:51], a[48:51]\n"
        "v_cvt_pk_bf16_f32 v190, 0, v190\n"
        "v_cvt_pk_bf16_f32 v191, 0, v191\n"
        "v_mfma_f32_16x16x32_bf16 a[52:55], v[8:11], v[52:55], a[52:55]\n"
        "v_readlane_b32 s84, v190, 63\n"
        "v_mul_f32 v191, 0xbfb8aa3b, v191\n"
        "v_mfma_f32_16x16x32_bf16 a[52:55], v[12:15], v[84:87], a[52:55]\n"
        "global_load_lds_dwordx4 v244, s[66:67] offset:2048\n"
        "v_mfma_f32_16x16x32_bf16 a[20:23], v[4:7], v[84:87], a[20:23]\n"
        "v_mov_b32 v190, s84\n"
        "v_lshrrev_b32 v193, 2, v254\n"
        "v_mfma_f32_16x16x32_bf16 a[20:23], v[0:3], v[52:55], a[20:23]\n"
        "v_exp_f32 v191, v191\n"
        "v_mfma_f32_16x16x32_bf16 a[24:27], v[0:3], v[56:59], a[24:27]\n"
        "v_add_f32 v191, 1.0, v191\n"
        "v_add_u32 v193, s81, v193\n"
        "v_mfma_f32_16x16x32_bf16 a[24:27], v[4:7], v[88:91], a[24:27]\n"
        "global_load_lds_dwordx4 v245, s[66:67] offset:3072\n"
        "s_add_u32 m0, s73, 0x1c000\n"
        "v_mfma_f32_16x16x32_bf16 a[56:59], v[12:15], v[88:91], a[56:59]\n"
        "s_add_u32 s66, s66, 0x80\n"
        "s_addc_u32 s67, s67, 0\n"
        "v_add_u32 v226, s53, v226\n"
        "v_mfma_f32_16x16x32_bf16 a[56:59], v[8:11], v[56:59], a[56:59]\n"
        "v_rcp_f32 v191, v191\n"
        "v_mfma_f32_16x16x32_bf16 a[60:63], v[8:11], v[60:63], a[60:63]\n"
        "ds_read_b128 v[8:11], v224 offset:34816\n"
        "v_cvt_pk_bf16_f32 v191, 0, v191\n"
        "v_mfma_f32_16x16x32_bf16 a[60:63], v[12:15], v[92:95], a[60:63]\n"
        "ds_read_b128 v[12:15], v225 offset:34816\n"
        "v_readlane_b32 s85, v191, 63\n"
        "v_mfma_f32_16x16x32_bf16 a[28:31], v[4:7], v[92:95], a[28:31]\n"
        "ds_read_b128 v[4:7], v225 offset:32768\n"
        "v_mov_b32 v191, s85\n"
        "v_mfma_f32_16x16x32_bf16 a[28:31], v[0:3], v[60:63], a[28:31]\n"
        "ds_read_b128 v[0:3], v224 offset:32768\n"
        "v_add_u32 v227, s53, v227\n"
        "v_mfma_f32_16x16x32_bf16 a[64:67], v[32:35], v[16:19], a[64:67]\n"
        "s_mov_b64 exec, 0xffff\n"
        "global_store_dword v192, v190, s[76:77] sc1\n"
        "s_mov_b64 exec, -1\n"
        "v_mfma_f32_16x16x32_bf16 a[64:67], v[64:67], v[20:23], a[64:67]\n"
        "s_mov_b64 exec, 0xffff\n"
        "global_store_dword v193, v191, s[76:77] sc1\n"
        "s_mov_b64 exec, -1\n"
        "v_mfma_f32_16x16x32_bf16 a[96:99], v[64:67], v[28:31], a[96:99]\n"
        "v_add_u32 v228, s53, v228\n"
        "v_add_u32 v229, s53, v229\n"
        "v_mfma_f32_16x16x32_bf16 a[96:99], v[32:35], v[24:27], a[96:99]\n"
        "v_add_u32 v230, s53, v230\n"
        "v_add_u32 v231, s53, v231\n"
        "v_mfma_f32_16x16x32_bf16 a[100:103], v[36:39], v[24:27], a[100:103]\n"
        "v_add_u32 v232, s53, v232\n"
        "v_add_u32 v233, s53, v233\n"
        "v_mfma_f32_16x16x32_bf16 a[100:103], v[68:71], v[28:31], a[100:103]\n"
        "s_add_u32 s53, s52, s51\n"
        "s_add_u32 s53, s53, 0x1100000\n"
        "v_mfma_f32_16x16x32_bf16 a[68:71], v[68:71], v[20:23], a[68:71]\n"
        "v_mfma_f32_16x16x32_bf16 a[68:71], v[36:39], v[16:19], a[68:71]\n"
        "v_mfma_f32_16x16x32_bf16 a[72:75], v[40:43], v[16:19], a[72:75]\n"
        "v_mfma_f32_16x16x32_bf16 a[72:75], v[72:75], v[20:23], a[72:75]\n"
        "v_mfma_f32_16x16x32_bf16 a[104:107], v[72:75], v[28:31], a[104:107]\n"
        "v_mfma_f32_16x16x32_bf16 a[104:107], v[40:43], v[24:27], a[104:107]\n"
        "v_mfma_f32_16x16x32_bf16 a[108:111], v[44:47], v[24:27], a[108:111]\n"
        "v_mfma_f32_16x16x32_bf16 a[108:111], v[76:79], v[28:31], a[108:111]\n"
        "v_mfma_f32_16x16x32_bf16 a[76:79], v[76:79], v[20:23], a[76:79]\n"
        "v_mfma_f32_16x16x32_bf16 a[76:79], v[44:47], v[16:19], a[76:79]\n"
        "v_mfma_f32_16x16x32_bf16 a[80:83], v[16:19], v[48:51], a[80:83]\n"
        "v_mfma_f32_16x16x32_bf16 a[80:83], v[20:23], v[80:83], a[80:83]\n"
        "v_mfma_f32_16x16x32_bf16 a[112:115], v[28:31], v[80:83], a[112:115]\n"
        "v_mfma_f32_16x16x32_bf16 a[112:115], v[24:27], v[48:51], a[112:115]\n"
        "v_mfma_f32_16x16x32_bf16 a[116:119], v[24:27], v[52:55], a[116:119]\n"
        "v_mfma_f32_16x16x32_bf16 a[116:119], v[28:31], v[84:87], a[116:119]\n"
        "v_mfma_f32_16x16x32_bf16 a[84:87], v[20:23], v[84:87], a[84:87]\n"
        "v_mfma_f32_16x16x32_bf16 a[84:87], v[16:19], v[52:55], a[84:87]\n"
        "v_mfma_f32_16x16x32_bf16 a[88:91], v[16:19], v[56:59], a[88:91]\n"
        "v_mfma_f32_16x16x32_bf16 a[88:91], v[20:23], v[88:91], a[88:91]\n"
        "v_mfma_f32_16x16x32_bf16 a[120:123], v[28:31], v[88:91], a[120:123]\n"
        "v_mfma_f32_16x16x32_bf16 a[120:123], v[24:27], v[56:59], a[120:123]\n"
        "v_mfma_f32_16x16x32_bf16 a[124:127], v[24:27], v[60:63], a[124:127]\n"
        "ds_read_b128 v[24:27], v224 offset:38912\n"
        "s_waitcnt lgkmcnt(1)\n"
        "v_mfma_f32_16x16x32_bf16 a[124:127], v[28:31], v[92:95], a[124:127]\n"
        "ds_read_b128 v[28:31], v225 offset:38912\n"
        "v_mfma_f32_16x16x32_bf16 a[92:95], v[20:23], v[92:95], a[92:95]\n"
        "ds_read_b128 v[20:23], v225 offset:36864\n"
        "v_mfma_f32_16x16x32_bf16 a[92:95], v[16:19], v[60:63], a[92:95]\n"
        "ds_read_b128 v[16:19], v224 offset:36864\n"
        "v_mfma_f32_16x16x32_bf16 a[128:131], v[32:35], v[0:3], a[128:131]\n"
        "v_mfma_f32_16x16x32_bf16 a[128:131], v[64:67], v[4:7], a[128:131]\n"
        "v_mfma_f32_16x16x32_bf16 a[160:163], v[64:67], v[12:15], a[160:163]\n"
        "v_mfma_f32_16x16x32_bf16 a[160:163], v[32:35], v[8:11], a[160:163]\n"
        "v_mfma_f32_16x16x32_bf16 a[164:167], v[36:39], v[8:11], a[164:167]\n"
        "v_mfma_f32_16x16x32_bf16 a[164:167], v[68:71], v[12:15], a[164:167]\n"
        "v_mfma_f32_16x16x32_bf16 a[132:135], v[68:71], v[4:7], a[132:135]\n"
        "v_mfma_f32_16x16x32_bf16 a[132:135], v[36:39], v[0:3], a[132:135]\n"
        "v_mfma_f32_16x16x32_bf16 a[136:139], v[40:43], v[0:3], a[136:139]\n"
        "v_mfma_f32_16x16x32_bf16 a[136:139], v[72:75], v[4:7], a[136:139]\n"
        "v_mfma_f32_16x16x32_bf16 a[168:171], v[72:75], v[12:15], a[168:171]\n"
        "v_mfma_f32_16x16x32_bf16 a[168:171], v[40:43], v[8:11], a[168:171]\n"
        "v_mfma_f32_16x16x32_bf16 a[172:175], v[44:47], v[8:11], a[172:175]\n"
        "v_mfma_f32_16x16x32_bf16 a[172:175], v[76:79], v[12:15], a[172:175]\n"
        "v_mfma_f32_16x16x32_bf16 a[140:143], v[76:79], v[4:7], a[140:143]\n"
        "v_mfma_f32_16x16x32_bf16 a[140:143], v[44:47], v[0:3], a[140:143]\n"
        "v_mfma_f32_16x16x32_bf16 a[144:147], v[0:3], v[48:51], a[144:147]\n"
        "v_mfma_f32_16x16x32_bf16 a[144:147], v[4:7], v[80:83], a[144:147]\n"
        "v_mfma_f32_16x16x32_bf16 a[176:179], v[12:15], v[80:83], a[176:179]\n"
        "v_mfma_f32_16x16x32_bf16 a[176:179], v[8:11], v[48:51], a[176:179]\n"
        "v_mfma_f32_16x16x32_bf16 a[180:183], v[8:11], v[52:55], a[180:183]\n"
        "v_mfma_f32_16x16x32_bf16 a[180:183], v[12:15], v[84:87], a[180:183]\n"
        "v_mfma_f32_16x16x32_bf16 a[148:151], v[4:7], v[84:87], a[148:151]\n"
        "v_mfma_f32_16x16x32_bf16 a[148:151], v[0:3], v[52:55], a[148:151]\n"
        "v_mfma_f32_16x16x32_bf16 a[152:155], v[0:3], v[56:59], a[152:155]\n"
        "v_mfma_f32_16x16x32_bf16 a[152:155], v[4:7], v[88:91], a[152:155]\n"
        "v_mfma_f32_16x16x32_bf16 a[184:187], v[12:15], v[88:91], a[184:187]\n"
        "v_mfma_f32_16x16x32_bf16 a[184:187], v[8:11], v[56:59], a[184:187]\n"
        "s_waitcnt vmcnt(10) lgkmcnt(0)\n"
        "v_mfma_f32_16x16x32_bf16 a[188:191], v[8:11], v[60:63], a[188:191]\n"
        "s_barrier\n"
        "v_mfma_f32_16x16x32_bf16 a[188:191], v[12:15], v[92:95], a[188:191]\n"
        "global_load_lds_dwordx4 v238, s[68:69] offset:0\n"
        "ds_read_b64_tr_b16 v[160:161], v226 offset:32768\n"
        "v_mfma_f32_16x16x32_bf16 a[156:159], v[4:7], v[92:95], a[156:159]\n"
        "ds_read_b64_tr_b16 v[162:163], v227 offset:32768\n"
        "v_mfma_f32_16x16x32_bf16 a[156:159], v[0:3], v[60:63], a[156:159]\n"
        "ds_read_b64_tr_b16 v[164:165], v226 offset:40960\n"
        "v_mfma_f32_16x16x32_bf16 a[192:195], v[32:35], v[16:19], a[192:195]\n"
        "ds_read_b64_tr_b16 v[166:167], v227 offset:40960\n"
        "v_mfma_f32_16x16x32_bf16 a[192:195], v[64:67], v[20:23], a[192:195]\n"
        "ds_read_b64_tr_b16 v[172:173], v228 offset:40960\n"
        "global_load_lds_dwordx4 v239, s[68:69] offset:1024\n"
        "v_mfma_f32_16x16x32_bf16 a[224:227], v[64:67], v[28:31], a[224:227]\n"
        "ds_read_b64_tr_b16 v[174:175], v229 offset:40960\n"
        "v_mfma_f32_16x16x32_bf16 a[224:227], v[32:35], v[24:27], a[224:227]\n"
        "ds_read_b64_tr_b16 v[168:169], v228 offset:32768\n"
        "v_mfma_f32_16x16x32_bf16 a[228:231], v[36:39], v[24:27], a[228:231]\n"
        "ds_read_b64_tr_b16 v[170:171], v229 offset:32768\n"
        "v_mfma_f32_16x16x32_bf16 a[228:231], v[68:71], v[28:31], a[228:231]\n"
        "ds_read_b128 v[192:195], v246 offset:2048\n"
        "global_load_lds_dwordx4 v240, s[68:69] offset:2048\n"
        "v_mfma_f32_16x16x32_bf16 a[196:199], v[68:71], v[20:23], a[196:199]\n"
        "ds_read_b128 v[208:211], v247 offset:2048\n"
        "v_accvgpr_read_b32 v177, a225\n"
        "v_mfma_f32_16x16x32_bf16 a[196:199], v[36:39], v[16:19], a[196:199]\n"
        "ds_read_b128 v[196:199], v246 offset:0\n"
        "v_accvgpr_read_b32 v178, a226\n"
        "v_mfma_f32_16x16x32_bf16 a[200:203], v[40:43], v[16:19], a[200:203]\n"
        "ds_read_b128 v[212:215], v247 offset:0\n"
        "v_accvgpr_read_b32 v179, a227\n"
        "v_mfma_f32_16x16x32_bf16 a[200:203], v[72:75], v[20:23], a[200:203]\n"
        "ds_read_b128 v[200:203], v246 offset:6144\n"
        "global_load_lds_dwordx4 v241, s[68:69] offset:3072\n"
        "s_add_u32 m0, s72, 0x8000\n"
        "v_mfma_f32_16x16x32_bf16 a[232:235], v[72:75], v[28:31], a[232:235]\n"
        "ds_read_b128 v[216:219], v247 offset:6144\n"
        "s_add_u32 s68, s68, 0x80000\n"
        "s_addc_u32 s69, s69, 0\n"
        "v_mfma_f32_16x16x32_bf16 a[232:235], v[40:43], v[24:27], a[232:235]\n"
        "ds_read_b128 v[204:207], v246 offset:4096\n"
        "v_accvgpr_read_b32 v180, a228\n"
        "v_mfma_f32_16x16x32_bf16 a[236:239], v[44:47], v[24:27], a[236:239]\n"
        "ds_read_b128 v[220:223], v247 offset:4096\n"
        "v_accvgpr_read_b32 v181, a229\n"
        "v_mfma_f32_16x16x32_bf16 a[236:239], v[76:79], v[28:31], a[236:239]\n"
        "ds_read_b64_tr_b16 v[32:33], v230 offset:32768\n"
        "global_load_lds_dwordx4 v242, s[64:65] offset:0\n"
        "v_mfma_f32_16x16x32_bf16 a[204:207], v[76:79], v[20:23], a[204:207]\n"
        "ds_read_b64_tr_b16 v[34:35], v231 offset:32768\n"
        "v_accvgpr_read_b32 v182, a230\n"
        "v_mfma_f32_16x16x32_bf16 a[204:207], v[44:47], v[16:19], a[204:207]\n"
        "ds_read_b64_tr_b16 v[36:37], v230 offset:40960\n"
        "s_add_u32 s97, s52, 0x2000\n"
        "v_accvgpr_read_b32 v183, a231\n"
        "v_mfma_f32_16x16x32_bf16 a[208:211], v[16:19], v[48:51], a[208:211]\n"
        "ds_read_b64_tr_b16 v[38:39], v231 offset:40960\n"
        "v_mul_f32 v177, s88, v177\n"
        "v_mfma_f32_16x16x32_bf16 a[208:211], v[20:23], v[80:83], a[208:211]\n"
        "ds_read_b64_tr_b16 v[44:45], v232 offset:40960\n"
        "global_load_lds_dwordx4 v243, s[64:65] offset:1024\n"
        "v_mfma_f32_16x16x32_bf16 a[240:243], v[28:31], v[80:83], a[240:243]\n"
        "ds_read_b64_tr_b16 v[46:47], v233 offset:40960\n"
        "v_mul_f32 v178, s88, v178\n"
        "v_mfma_f32_16x16x32_bf16 a[240:243], v[24:27], v[48:51], a[240:243]\n"
        "ds_read_b64_tr_b16 v[40:41], v232 offset:32768\n"
        "v_mul_f32 v179, s88, v179\n"
        "v_mfma_f32_16x16x32_bf16 a[244:247], v[24:27], v[52:55], a[244:247]\n"
        "ds_read_b64_tr_b16 v[42:43], v233 offset:32768\n"
        "v_mul_f32 v180, s88, v180\n"
        "v_mfma_f32_16x16x32_bf16 a[244:247], v[28:31], v[84:87], a[244:247]\n"
        "global_load_lds_dwordx4 v244, s[64:65] offset:2048\n"
        "v_mfma_f32_16x16x32_bf16 a[212:215], v[20:23], v[84:87], a[212:215]\n"
        "ds_read_b64_tr_b16 v[48:49], v226 offset:33792\n"
        "v_mul_f32 v181, s88, v181\n"
        "v_mfma_f32_16x16x32_bf16 a[212:215], v[16:19], v[52:55], a[212:215]\n"
        "ds_read_b64_tr_b16 v[50:51], v227 offset:33792\n"
        "v_mul_f32 v182, s88, v182\n"
        "v_mfma_f32_16x16x32_bf16 a[216:219], v[16:19], v[56:59], a[216:219]\n"
        "ds_read_b64_tr_b16 v[52:53], v226 offset:41984\n"
        "v_mul_f32 v183, s88, v183\n"
        "v_mfma_f32_16x16x32_bf16 a[216:219], v[20:23], v[88:91], a[216:219]\n"
        "global_load_lds_dwordx4 v245, s[64:65] offset:3072\n"
        "s_add_u32 m0, s72, 0x9000\n"
        "v_mfma_f32_16x16x32_bf16 a[248:251], v[28:31], v[88:91], a[248:251]\n"
        "ds_read_b64_tr_b16 v[54:55], v227 offset:41984\n"
        "s_add_u32 s64, s64, 0x80\n"
        "s_addc_u32 s65, s65, 0\n"
        "v_mfma_f32_16x16x32_bf16 a[248:251], v[24:27], v[56:59], a[248:251]\n"
        "ds_read_b64_tr_b16 v[56:57], v228 offset:33792\n"
        "v_accvgpr_read_b32 v185, a233\n"
        "v_mfma_f32_16x16x32_bf16 a[252:255], v[24:27], v[60:63], a[252:255]\n"
        "ds_read_b64_tr_b16 v[58:59], v229 offset:33792\n"
        "v_accvgpr_read_b32 v186, a234\n"
        "v_mfma_f32_16x16x32_bf16 a[252:255], v[28:31], v[92:95], a[252:255]\n"
        "global_load_lds_dwordx4 v242, s[66:67] offset:0\n"
        "v_mfma_f32_16x16x32_bf16 a[220:223], v[20:23], v[92:95], a[220:223]\n"
        "s_waitcnt lgkmcnt(5)\n"
        "v_accvgpr_read_b32 v187, a235\n"
        "v_accvgpr_read_b32 v188, a236\n"
        "v_mfma_f32_16x16x32_bf16 a[220:223], v[16:19], v[60:63], a[220:223]\n"
        "ds_read_b64_tr_b16 v[60:61], v228 offset:41984\n"
        "v_accvgpr_read_b32 v189, a237\n"
        "v_mfma_f32_16x16x32_bf16 v[0:3], v[160:163], v[192:195], 0\n"
        "ds_read_b64_tr_b16 v[62:63], v229 offset:41984\n"
        "v_accvgpr_read_b32 v190, a238\n"
        "v_mfma_f32_16x16x32_bf16 v[0:3], v[164:167], v[208:211], v[0:3]\n"
        "global_load_lds_dwordx4 v243, s[66:67] offset:1024\n"
        "v_mfma_f32_16x16x32_bf16 v[4:7], v[172:175], v[208:211], 0\n"
        "v_accvgpr_read_b32 v191, a239\n"
        "v_mul_f32 v185, s88, v185\n"
        "v_mfma_f32_16x16x32_bf16 v[4:7], v[168:171], v[192:195], v[4:7]\n"
        "v_mul_f32 v186, s88, v186\n"
        "v_mul_f32 v187, s88, v187\n"
        "v_mfma_f32_16x16x32_bf16 v[68:71], v[168:171], v[196:199], 0\n"
        "v_mul_f32 v188, s88, v188\n"
        "v_mul_f32 v189, s88, v189\n"
        "v_mfma_f32_16x16x32_bf16 v[68:71], v[172:175], v[212:215], v[68:71]\n"
        "global_load_lds_dwordx4 v244, s[66:67] offset:2048\n"
        "v_mfma_f32_16x16x32_bf16 v[64:67], v[164:167], v[212:215], 0\n"
        "v_mul_f32 v190, s88, v190\n"
        "v_mul_f32 v191, s88, v191\n"
        "v_mfma_f32_16x16x32_bf16 v[64:67], v[160:163], v[196:199], v[64:67]\n"
        "v_accvgpr_read_b32 v176, a224\n"
        "v_mul_f32 v176, s88, v176\n"
        "v_mfma_f32_16x16x32_bf16 v[96:99], v[160:163], v[200:203], 0\n"
        "v_cvt_pk_bf16_f32 v176, v176, v177\n"
        "v_cvt_pk_bf16_f32 v177, v178, v179\n"
        "v_mfma_f32_16x16x32_bf16 v[96:99], v[164:167], v[216:219], v[96:99]\n"
        "v_cvt_pk_bf16_f32 v178, v180, v181\n"
        "v_cvt_pk_bf16_f32 v179, v182, v183\n"
        "v_mfma_f32_16x16x32_bf16 v[100:103], v[172:175], v[216:219], 0\n"
        "global_load_lds_dwordx4 v245, s[66:67] offset:3072\n"
        "s_add_u32 m0, s73, 0x20000\n"
        "v_accvgpr_write_b32 a224, v176\n"
        "v_mfma_f32_16x16x32_bf16 v[100:103], v[168:171], v[200:203], v[100:103]\n"
        "s_add_u32 s66, s66, 0x80\n"
        "s_addc_u32 s67, s67, 0\n"
        "v_accvgpr_write_b32 a225, v177\n"
        "v_mfma_f32_16x16x32_bf16 v[132:135], v[168:171], v[204:207], 0\n"
        "ds_read_b64_tr_b16 v[168:169], v232 offset:33792\n"
        "v_accvgpr_write_b32 a226, v178\n"
        "v_mfma_f32_16x16x32_bf16 v[132:135], v[172:175], v[220:223], v[132:135]\n"
        "ds_read_b64_tr_b16 v[172:173], v232 offset:41984\n"
        "v_accvgpr_write_b32 a227, v179\n"
        "v_mfma_f32_16x16x32_bf16 v[128:131], v[164:167], v[220:223], 0\n"
        "ds_read_b64_tr_b16 v[164:165], v230 offset:41984\n"
        "v_accvgpr_read_b32 v177, a193\n"
        "v_mfma_f32_16x16x32_bf16 v[128:131], v[160:163], v[204:207], v[128:131]\n"
        "ds_read_b64_tr_b16 v[160:161], v230 offset:33792\n"
        "v_accvgpr_read_b32 v178, a194\n"
        "v_mfma_f32_16x16x32_bf16 v[8:11], v[32:35], v[192:195], 0\n"
        "ds_read_b64_tr_b16 v[162:163], v231 offset:33792\n"
        "v_accvgpr_read_b32 v179, a195\n"
        "v_mfma_f32_16x16x32_bf16 v[8:11], v[36:39], v[208:211], v[8:11]\n"
        "LHA4_%=:\n"
        "s_cbranch_execz LHA8_%=\n"
        "ds_read_b64_tr_b16 v[166:167], v231 offset:41984\n"
        "v_accvgpr_read_b32 v180, a196\n"
        "v_mfma_f32_16x16x32_bf16 v[12:15], v[44:47], v[208:211], 0\n"
        "ds_read_b64_tr_b16 v[174:175], v233 offset:41984\n"
        "v_accvgpr_read_b32 v181, a197\n"
        "v_mfma_f32_16x16x32_bf16 v[12:15], v[40:43], v[192:195], v[12:15]\n"
        "ds_read_b64_tr_b16 v[170:171], v233 offset:33792\n"
        "v_accvgpr_read_b32 v182, a198\n"
        "v_mfma_f32_16x16x32_bf16 v[76:79], v[40:43], v[196:199], 0\n"
        "v_accvgpr_read_b32 v183, a199\n"
        "v_mul_f32 v177, s88, v177\n"
        "v_mfma_f32_16x16x32_bf16 v[76:79], v[44:47], v[212:215], v[76:79]\n"
        "v_mul_f32 v178, s88, v178\n"
        "v_mul_f32 v179, s88, v179\n"
        "v_mfma_f32_16x16x32_bf16 v[72:75], v[36:39], v[212:215], 0\n"
        "v_mul_f32 v180, s88, v180\n"
        "v_mul_f32 v181, s88, v181\n"
        "v_mfma_f32_16x16x32_bf16 v[72:75], v[32:35], v[196:199], v[72:75]\n"
        "v_mul_f32 v182, s88, v182\n"
        "v_mul_f32 v183, s88, v183\n"
        "v_mfma_f32_16x16x32_bf16 v[104:107], v[32:35], v[200:203], 0\n"
        "v_mfma_f32_16x16x32_bf16 v[104:107], v[36:39], v[216:219], v[104:107]\n"
        "v_mfma_f32_16x16x32_bf16 v[108:111], v[44:47], v[216:219], 0\n"
        "v_mfma_f32_16x16x32_bf16 v[108:111], v[40:43], v[200:203], v[108:111]\n"
        "s_waitcnt vmcnt(14) lgkmcnt(0)\n"
        "v_mfma_f32_16x16x32_bf16 v[140:143], v[40:43], v[204:207], 0\n"
        "s_barrier\n"
        "v_mfma_f32_16x16x32_bf16 v[140:143], v[44:47], v[220:223], v[140:143]\n"
        "global_load_lds_dwordx4 v238, s[68:69] offset:0\n"
        "ds_read_b64_tr_b16 v[44:45], v228 offset:57344\n"
        "v_mfma_f32_16x16x32_bf16 v[136:139], v[36:39], v[220:223], 0\n"
        "ds_read_b64_tr_b16 v[36:37], v226 offset:57344\n"
        "v_mfma_f32_16x16x32_bf16 v[136:139], v[32:35], v[204:207], v[136:139]\n"
        "ds_read_b64_tr_b16 v[32:33], v226 offset:49152\n"
        "v_accvgpr_read_b32 v184, a232\n"
        "v_mfma_f32_16x16x32_bf16 v[16:19], v[48:51], v[192:195], 0\n"
        "ds_read_b64_tr_b16 v[34:35], v227 offset:49152\n"
        "v_mul_f32 v184, s88, v184\n"
        "v_mfma_f32_16x16x32_bf16 v[16:19], v[52:55], v[208:211], v[16:19]\n"
        "ds_read_b64_tr_b16 v[38:39], v227 offset:57344\n"
        "v_cvt_pk_bf16_f32 v184, v184, v185\n"
        "v_mfma_f32_16x16x32_bf16 v[20:23], v[60:63], v[208:211], 0\n"
        "ds_read_b64_tr_b16 v[46:47], v229 offset:57344\n"
        "v_cvt_pk_bf16_f32 v185, v186, v187\n"
        "v_mfma_f32_16x16x32_bf16 v[20:23], v[56:59], v[192:195], v[20:23]\n"
        "ds_read_b64_tr_b16 v[40:41], v228 offset:49152\n"
        "v_cvt_pk_bf16_f32 v186, v188, v189\n"
        "v_mfma_f32_16x16x32_bf16 v[84:87], v[56:59], v[196:199], 0\n"
        "ds_read_b64_tr_b16 v[42:43], v229 offset:49152\n"
        "v_cvt_pk_bf16_f32 v187, v190, v191\n"
        "v_mfma_f32_16x16x32_bf16 v[84:87], v[60:63], v[212:215], v[84:87]\n"
        "global_load_lds_dwordx4 v239, s[68:69] offset:1024\n"
        "v_accvgpr_write_b32 a228, v184\n"
        "v_mfma_f32_16x16x32_bf16 v[80:83], v[52:55], v[212:215], 0\n"
        "v_accvgpr_write_b32 a229, v185\n"
        "v_accvgpr_write_b32 a230, v186\n"
        "v_mfma_f32_16x16x32_bf16 v[80:83], v[48:51], v[196:199], v[80:83]\n"
        "v_accvgpr_write_b32 a231, v187\n"
        "v_accvgpr_read_b32 v185, a201\n"
        "v_mfma_f32_16x16x32_bf16 v[112:115], v[48:51], v[200:203], 0\n"
        "v_accvgpr_read_b32 v186, a202\n"
        "v_accvgpr_read_b32 v187, a203\n"
        "v_mfma_f32_16x16x32_bf16 v[112:115], v[52:55], v[216:219], v[112:115]\n"
        "global_load_lds_dwordx4 v240, s[68:69] offset:2048\n"
        "v_accvgpr_read_b32 v188, a204\n"
        "v_mfma_f32_16x16x32_bf16 v[116:119], v[60:63], v[216:219], 0\n"
        "v_accvgpr_read_b32 v189, a205\n"
        "v_accvgpr_read_b32 v190, a206\n"
        "v_mfma_f32_16x16x32_bf16 v[116:119], v[56:59], v[200:203], v[116:119]\n"
        "v_accvgpr_read_b32 v191, a207\n"
        "v_mul_f32 v185, s88, v185\n"
        "v_mfma_f32_16x16x32_bf16 v[148:151], v[56:59], v[204:207], 0\n"
        "ds_read_b64_tr_b16 v[56:57], v232 offset:49152\n"
        "v_mul_f32 v186, s88, v186\n"
        "v_mfma_f32_16x16x32_bf16 v[148:151], v[60:63], v[220:223], v[148:151]\n"
        "global_store_dwordx4 v254, a[224:227], s[70:71] offset:0 sc1\n"
        "ds_read_b64_tr_b16 v[60:61], v232 offset:57344\n"
        "ds_read_b64_tr_b16 v[62:63], v233 offset:57344\n"
        "v_mul_f32 v187, s88, v187\n"
        "v_mfma_f32_16x16x32_bf16 v[144:147], v[52:55], v[220:223], 0\n"
        "ds_read_b64_tr_b16 v[52:53], v230 offset:57344\n"
        "v_mul_f32 v188, s88, v188\n"
        "v_mfma_f32_16x16x32_bf16 v[144:147], v[48:51], v[204:207], v[144:147]\n"
        "ds_read_b64_tr_b16 v[48:49], v230 offset:49152\n"
        "v_mul_f32 v189, s88, v189\n"
        "v_mfma_f32_16x16x32_bf16 v[24:27], v[160:163], v[192:195], 0\n"
        "global_load_lds_dwordx4 v241, s[68:69] offset:3072\n"
        "s_add_u32 m0, s72, 0x0\n"
        "v_mfma_f32_16x16x32_bf16 v[24:27], v[164:167], v[208:211], v[24:27]\n"
        "ds_read_b64_tr_b16 v[50:51], v231 offset:49152\n"
        "s_add_u32 s68, s68, 0x80000\n"
        "s_addc_u32 s69, s69, 0\n"
        "v_mfma_f32_16x16x32_bf16 v[28:31], v[172:175], v[208:211], 0\n"
        "ds_read_b64_tr_b16 v[54:55], v231 offset:57344\n"
        "v_mul_f32 v190, s88, v190\n"
        "v_mfma_f32_16x16x32_bf16 v[28:31], v[168:171], v[192:195], v[28:31]\n"
        "ds_read_b64_tr_b16 v[58:59], v233 offset:49152\n"
        "v_mul_f32 v191, s88, v191\n"
        "v_mfma_f32_16x16x32_bf16 v[92:95], v[168:171], v[196:199], 0\n"
        "global_load_lds_dwordx4 v242, s[64:65] offset:0\n"
        "v_mfma_f32_16x16x32_bf16 v[92:95], v[172:175], v[212:215], v[92:95]\n"
        "ds_read_b128 v[192:195], v248 offset:2048\n"
        "v_mfma_f32_16x16x32_bf16 v[88:91], v[164:167], v[212:215], 0\n"
        "ds_read_b128 v[208:211], v249 offset:2048\n"
        "v_mfma_f32_16x16x32_bf16 v[88:91], v[160:163], v[196:199], v[88:91]\n"
        "ds_read_b128 v[196:199], v248 offset:0\n"
        "v_accvgpr_read_b32 v176, a192\n"
        "v_mfma_f32_16x16x32_bf16 v[120:123], v[160:163], v[200:203], 0\n"
        "global_load_lds_dwordx4 v243, s[64:65] offset:1024\n"
        "v_mfma_f32_16x16x32_bf16 v[120:123], v[164:167], v[216:219], v[120:123]\n"
        "ds_read_b128 v[212:215], v249 offset:0\n"
        "v_mul_f32 v176, s88, v176\n"
        "v_mfma_f32_16x16x32_bf16 v[124:127], v[172:175], v[216:219], 0\n"
        "ds_read_b128 v[216:219], v249 offset:6144\n"
        "v_cvt_pk_bf16_f32 v176, v176, v177\n"
        "v_mfma_f32_16x16x32_bf16 v[124:127], v[168:171], v[200:203], v[124:127]\n"
        "ds_read_b128 v[200:203], v248 offset:6144\n"
        "v_cvt_pk_bf16_f32 v177, v178, v179\n"
        "v_mfma_f32_16x16x32_bf16 v[156:159], v[168:171], v[204:207], 0\n"
        "ds_read_b64_tr_b16 v[168:169], v228 offset:50176\n"
        "v_cvt_pk_bf16_f32 v178, v180, v181\n"
        "v_mfma_f32_16x16x32_bf16 v[156:159], v[172:175], v[220:223], v[156:159]\n"
        "s_waitcnt lgkmcnt(6)\n"
        "v_cvt_pk_bf16_f32 v179, v182, v183\n"
        "v_accvgpr_write_b32 a232, v176\n"
        "v_mfma_f32_16x16x32_bf16 v[152:155], v[164:167], v[220:223], 0\n"
        "s_waitcnt lgkmcnt(5)\n"
        "v_accvgpr_write_b32 a233, v177\n"
        "v_accvgpr_write_b32 a234, v178\n"
        "v_mfma_f32_16x16x32_bf16 v[152:155], v[160:163], v[204:207], v[152:155]\n"
        "global_load_lds_dwordx4 v244, s[64:65] offset:2048\n"
        "v_accvgpr_write_b32 a235, v179\n"
        "v_mfma_f32_16x16x32_bf16 v[0:3], v[32:35], v[192:195], v[0:3]\n"
        "ds_read_b128 v[204:207], v248 offset:4096\n"
        "v_accvgpr_read_b32 v177, a161\n"
        "v_mfma_f32_16x16x32_bf16 v[0:3], v[36:39], v[208:211], v[0:3]\n"
        "ds_read_b128 v[220:223], v249 offset:4096\n"
        "v_accvgpr_read_b32 v178, a162\n"
        "v_mfma_f32_16x16x32_bf16 v[4:7], v[44:47], v[208:211], v[4:7]\n"
        "ds_read_b64_tr_b16 v[160:161], v226 offset:50176\n"
        "v_accvgpr_read_b32 v179, a163\n"
        "v_mfma_f32_16x16x32_bf16 v[4:7], v[40:43], v[192:195], v[4:7]\n"
        "global_load_lds_dwordx4 v245, s[64:65] offset:3072\n"
        "s_add_u32 m0, s72, 0x1000\n"
        "s_waitcnt lgkmcnt(4)\n"
        "v_mfma_f32_16x16x32_bf16 v[68:71], v[40:43], v[196:199], v[68:71]\n"
        "ds_read_b64_tr_b16 v[162:163], v227 offset:50176\n"
        "s_add_u32 s64, s64, 0x80\n"
        "s_addc_u32 s65, s65, 0\n"
        "v_mfma_f32_16x16x32_bf16 v[68:71], v[44:47], v[212:215], v[68:71]\n"
        "ds_read_b64_tr_b16 v[164:165], v226 offset:58368\n"
        "v_accvgpr_read_b32 v180, a164\n"
        "v_mfma_f32_16x16x32_bf16 v[64:67], v[36:39], v[212:215], v[64:67]\n"
        "ds_read_b64_tr_b16 v[166:167], v227 offset:58368\n"
        "v_accvgpr_read_b32 v181, a165\n"
        "v_mfma_f32_16x16x32_bf16 v[64:67], v[32:35], v[196:199], v[64:67]\n"
        "ds_read_b64_tr_b16 v[172:173], v228 offset:58368\n"
        "global_load_lds_dwordx4 v242, s[66:67] offset:0\n"
        "v_mfma_f32_16x16x32_bf16 v[96:99], v[32:35], v[200:203], v[96:99]\n"
        "ds_read_b64_tr_b16 v[174:175], v229 offset:58368\n"
        "v_accvgpr_read_b32 v182, a166\n"
        "v_mfma_f32_16x16x32_bf16 v[96:99], v[36:39], v[216:219], v[96:99]\n"
        "ds_read_b64_tr_b16 v[170:171], v229 offset:50176\n"
        "v_accvgpr_read_b32 v183, a167\n"
        "v_mfma_f32_16x16x32_bf16 v[100:103], v[44:47], v[216:219], v[100:103]\n"
        "s_waitcnt lgkmcnt(6)\n"
        "v_mul_f32 v177, s88, v177\n"
        "v_mul_f32 v178, s88, v178\n"
        "v_mfma_f32_16x16x32_bf16 v[100:103], v[40:43], v[200:203], v[100:103]\n"
        "global_load_lds_dwordx4 v243, s[66:67] offset:1024\n"
        "v_mfma_f32_16x16x32_bf16 v[132:135], v[40:43], v[204:207], v[132:135]\n"
        "ds_read_b64_tr_b16 v[40:41], v232 offset:50176\n"
        "v_mul_f32 v179, s88, v179\n"
        "v_mfma_f32_16x16x32_bf16 v[132:135], v[44:47], v[220:223], v[132:135]\n"
        "ds_read_b64_tr_b16 v[44:45], v232 offset:58368\n"
        "v_mul_f32 v180, s88, v180\n"
        "v_mfma_f32_16x16x32_bf16 v[128:131], v[36:39], v[220:223], v[128:131]\n"
        "ds_read_b64_tr_b16 v[36:37], v230 offset:58368\n"
        "v_mul_f32 v181, s88, v181\n"
        "v_mfma_f32_16x16x32_bf16 v[128:131], v[32:35], v[204:207], v[128:131]\n"
        "global_load_lds_dwordx4 v244, s[66:67] offset:2048\n"
        "v_mfma_f32_16x16x32_bf16 v[8:11], v[48:51], v[192:195], v[8:11]\n"
        "ds_read_b64_tr_b16 v[32:33], v230 offset:50176\n"
        "v_accvgpr_read_b32 v184, a200\n"
        "v_mfma_f32_16x16x32_bf16 v[8:11], v[52:55], v[208:211], v[8:11]\n"
        "ds_read_b64_tr_b16 v[34:35], v231 offset:50176\n"
        "v_mul_f32 v184, s88, v184\n"
        "v_mfma_f32_16x16x32_bf16 v[12:15], v[60:63], v[208:211], v[12:15]\n"
        "ds_read_b64_tr_b16 v[38:39], v231 offset:58368\n"
        "v_cvt_pk_bf16_f32 v184, v184, v185\n"
        "v_mfma_f32_16x16x32_bf16 v[12:15], v[56:59], v[192:195], v[12:15]\n"
        "global_load_lds_dwordx4 v245, s[66:67] offset:3072\n"
        "s_add_u32 m0, s73, 0x24000\n"
        "v_mfma_f32_16x16x32_bf16 v[76:79], v[56:59], v[196:199], v[76:79]\n"
        "ds_read_b64_tr_b16 v[46:47], v233 offset:58368\n"
        "v_cvt_pk_bf16_f32 v185, v186, v187\n"
        "v_mfma_f32_16x16x32_bf16 v[76:79], v[60:63], v[212:215], v[76:79]\n"
        "ds_read_b64_tr_b16 v[42:43], v233 offset:50176\n"
        "v_cvt_pk_bf16_f32 v186, v188, v189\n"
        "v_mfma_f32_16x16x32_bf16 v[72:75], v[52:55], v[212:215], v[72:75]\n"
        "s_add_u32 s66, s66, 0x80\n"
        "s_addc_u32 s67, s67, 0\n"
        "v_cvt_pk_bf16_f32 v187, v190, v191\n"
        "v_mfma_f32_16x16x32_bf16 v[72:75], v[48:51], v[196:199], v[72:75]\n"
        "v_accvgpr_write_b32 a236, v184\n"
        "v_accvgpr_write_b32 a237, v185\n"
        "v_mfma_f32_16x16x32_bf16 v[104:107], v[48:51], v[200:203], v[104:107]\n"
        "v_accvgpr_write_b32 a238, v186\n"
        "v_accvgpr_write_b32 a239, v187\n"
        "v_mfma_f32_16x16x32_bf16 v[104:107], v[52:55], v[216:219], v[104:107]\n"
        "v_mul_f32 v182, s88, v182\n"
        "v_mul_f32 v183, s88, v183\n"
        "v_mfma_f32_16x16x32_bf16 v[108:111], v[60:63], v[216:219], v[108:111]\n"
        "v_accvgpr_read_b32 v185, a169\n"
        "v_accvgpr_read_b32 v186, a170\n"
        "v_mfma_f32_16x16x32_bf16 v[108:111], v[56:59], v[200:203], v[108:111]\n"
        "v_accvgpr_read_b32 v187, a171\n"
        "v_accvgpr_read_b32 v188, a172\n"
        "v_mfma_f32_16x16x32_bf16 v[140:143], v[56:59], v[204:207], v[140:143]\n"
        "s_waitcnt lgkmcnt(1)\n"
        "v_accvgpr_read_b32 v189, a173\n"
        "v_accvgpr_read_b32 v190, a174\n"
        "v_mfma_f32_16x16x32_bf16 v[140:143], v[60:63], v[220:223], v[140:143]\n"
        "s_waitcnt vmcnt(13) lgkmcnt(0)\n"
        "v_accvgpr_read_b32 v191, a175\n"
        "v_mul_f32 v185, s88, v185\n"
        "v_mfma_f32_16x16x32_bf16 v[136:139], v[52:55], v[220:223], v[136:139]\n"
        "s_barrier\n"
        "v_mfma_f32_16x16x32_bf16 v[136:139], v[48:51], v[204:207], v[136:139]\n"
        "global_load_lds_dwordx4 v238, s[68:69] offset:0\n"
        "ds_read_b64_tr_b16 v[48:49], v226 offset:16384\n"
        "v_mfma_f32_16x16x32_bf16 v[16:19], v[160:163], v[192:195], v[16:19]\n"
        "ds_read_b64_tr_b16 v[50:51], v227 offset:16384\n"
        "v_mul_f32 v186, s88, v186\n"
        "v_mfma_f32_16x16x32_bf16 v[16:19], v[164:167], v[208:211], v[16:19]\n"
        "ds_read_b64_tr_b16 v[52:53], v226 offset:24576\n"
        "v_mul_f32 v187, s88, v187\n"
        "v_mfma_f32_16x16x32_bf16 v[20:23], v[172:175], v[208:211], v[20:23]\n"
        "ds_read_b64_tr_b16 v[54:55], v227 offset:24576\n"
        "v_mul_f32 v188, s88, v188\n"
        "v_mfma_f32_16x16x32_bf16 v[20:23], v[168:171], v[192:195], v[20:23]\n"
        "ds_read_b64_tr_b16 v[60:61], v228 offset:24576\n"
        "global_load_lds_dwordx4 v239, s[68:69] offset:1024\n"
        "v_mfma_f32_16x16x32_bf16 v[84:87], v[168:171], v[196:199], v[84:87]\n"
        "ds_read_b64_tr_b16 v[62:63], v229 offset:24576\n"
        "v_mul_f32 v189, s88, v189\n"
        "v_mfma_f32_16x16x32_bf16 v[84:87], v[172:175], v[212:215], v[84:87]\n"
        "ds_read_b64_tr_b16 v[56:57], v228 offset:16384\n"
        "v_mul_f32 v190, s88, v190\n"
        "v_mfma_f32_16x16x32_bf16 v[80:83], v[164:167], v[212:215], v[80:83]\n"
        "ds_read_b64_tr_b16 v[58:59], v229 offset:16384\n"
        "v_mul_f32 v191, s88, v191\n"
        "v_mfma_f32_16x16x32_bf16 v[80:83], v[160:163], v[196:199], v[80:83]\n"
        "v_accvgpr_read_b32 v176, a160\n"
        "v_mul_f32 v176, s88, v176\n"
        "v_mfma_f32_16x16x32_bf16 v[112:115], v[160:163], v[200:203], v[112:115]\n"
        "global_load_lds_dwordx4 v240, s[68:69] offset:2048\n"
        "v_cvt_pk_bf16_f32 v176, v176, v177\n"
        "v_mfma_f32_16x16x32_bf16 v[112:115], v[164:167], v[216:219], v[112:115]\n"
        "v_cvt_pk_bf16_f32 v177, v178, v179\n"
        "v_cvt_pk_bf16_f32 v178, v180, v181\n"
        "v_mfma_f32_16x16x32_bf16 v[116:119], v[172:175], v[216:219], v[116:119]\n"
        "v_cvt_pk_bf16_f32 v179, v182, v183\n"
        "v_accvgpr_write_b32 a192, v176\n"
        "v_mfma_f32_16x16x32_bf16 v[116:119], v[168:171], v[200:203], v[116:119]\n"
        "v_accvgpr_write_b32 a193, v177\n"
        "v_accvgpr_write_b32 a194, v178\n"
        "v_mfma_f32_16x16x32_bf16 v[148:151], v[168:171], v[204:207], v[148:151]\n"
        "global_load_lds_dwordx4 v241, s[68:69] offset:3072\n"
        "s_add_u32 m0, s72, 0x10000\n"
        "v_accvgpr_write_b32 a195, v179\n"
        "v_mfma_f32_16x16x32_bf16 v[148:151], v[172:175], v[220:223], v[148:151]\n"
        "ds_read_b64_tr_b16 v[172:173], v232 offset:24576\n"
        "s_add_u32 s68, s68, 0x80000\n"
        "s_addc_u32 s69, s69, 0\n"
        "v_mfma_f32_16x16x32_bf16 v[144:147], v[164:167], v[220:223], v[144:147]\n"
        "ds_read_b64_tr_b16 v[164:165], v230 offset:24576\n"
        "v_accvgpr_read_b32 v177, a1\n"
        "v_mfma_f32_16x16x32_bf16 v[144:147], v[160:163], v[204:207], v[144:147]\n"
        "ds_read_b64_tr_b16 v[160:161], v230 offset:16384\n"
        "v_accvgpr_read_b32 v178, a2\n"
        "v_mfma_f32_16x16x32_bf16 v[24:27], v[32:35], v[192:195], v[24:27]\n"
        "global_store_dwordx4 v254, a[228:231], s[70:71] offset:1024 sc1\n"
        "ds_read_b64_tr_b16 v[162:163], v231 offset:16384\n"
        "ds_read_b64_tr_b16 v[166:167], v231 offset:24576\n"
        "v_accvgpr_read_b32 v179, a3\n"
        "v_mfma_f32_16x16x32_bf16 v[24:27], v[36:39], v[208:211], v[24:27]\n"
        "ds_read_b64_tr_b16 v[174:175], v233 offset:24576\n"
        "v_accvgpr_read_b32 v180, a4\n"
        "v_mfma_f32_16x16x32_bf16 v[28:31], v[44:47], v[208:211], v[28:31]\n"
        "ds_read_b64_tr_b16 v[168:169], v232 offset:16384\n"
        "v_accvgpr_read_b32 v181, a5\n"
        "v_mfma_f32_16x16x32_bf16 v[28:31], v[40:43], v[192:195], v[28:31]\n"
        "ds_read_b64_tr_b16 v[170:171], v233 offset:16384\n"
        "global_load_lds_dwordx4 v242, s[64:65] offset:0\n"
        "v_mfma_f32_16x16x32_bf16 v[92:95], v[40:43], v[196:199], v[92:95]\n"
        "ds_read_b128 v[192:195], v246 offset:34816\n"
        "v_accvgpr_read_b32 v182, a6\n"
        "v_mfma_f32_16x16x32_bf16 v[92:95], v[44:47], v[212:215], v[92:95]\n"
        "ds_read_b128 v[208:211], v247 offset:34816\n"
        "v_accvgpr_read_b32 v183, a7\n"
        "v_mfma_f32_16x16x32_bf16 v[88:91], v[36:39], v[212:215], v[88:91]\n"
        "ds_read_b128 v[212:215], v247 offset:32768\n"
        "v_mul_f32 v177, s88, v177\n"
        "v_mfma_f32_16x16x32_bf16 v[88:91], v[32:35], v[196:199], v[88:91]\n"
        "global_load_lds_dwordx4 v243, s[64:65] offset:1024\n"
        "v_mfma_f32_16x16x32_bf16 v[120:123], v[32:35], v[200:203], v[120:123]\n"
        "ds_read_b128 v[196:199], v246 offset:32768\n"
        "v_mul_f32 v178, s88, v178\n"
        "v_mfma_f32_16x16x32_bf16 v[120:123], v[36:39], v[216:219], v[120:123]\n"
        "s_add_u32 s70, s38, s97\n"
        "s_addc_u32 s71, s39, 0\n"
        "v_mul_f32 v179, s88, v179\n"
        "v_mfma_f32_16x16x32_bf16 v[124:127], v[44:47], v[216:219], v[124:127]\n"
        "ds_read_b128 v[216:219], v247 offset:38912\n"
        "v_mul_f32 v180, s88, v180\n"
        "v_mfma_f32_16x16x32_bf16 v[124:127], v[40:43], v[200:203], v[124:127]\n"
        "global_load_lds_dwordx4 v244, s[64:65] offset:2048\n"
        "s_add_u32 s97, s52, 0x1000\n"
        "v_mfma_f32_16x16x32_bf16 v[156:159], v[40:43], v[204:207], v[156:159]\n"
        "ds_read_b128 v[200:203], v246 offset:38912\n"
        "v_mul_f32 v181, s88, v181\n"
        "v_mfma_f32_16x16x32_bf16 v[156:159], v[44:47], v[220:223], v[156:159]\n"
        "ds_read_b64_tr_b16 v[44:45], v228 offset:25600\n"
        "v_mul_f32 v182, s88, v182\n"
        "v_mfma_f32_16x16x32_bf16 v[152:155], v[36:39], v[220:223], v[152:155]\n"
        "ds_read_b128 v[220:223], v247 offset:36864\n"
        "v_mul_f32 v183, s88, v183\n"
        "v_mfma_f32_16x16x32_bf16 v[152:155], v[32:35], v[204:207], v[152:155]\n"
        "s_waitcnt lgkmcnt(5)\n"
        "global_load_lds_dwordx4 v245, s[64:65] offset:3072\n"
        "s_add_u32 m0, s72, 0x11000\n"
        "v_mfma_f32_16x16x32_bf16 v[0:3], v[48:51], v[192:195], v[0:3]\n"
        "ds_read_b128 v[204:207], v246 offset:36864\n"
        "v_accvgpr_read_b32 v184, a168\n"
        "v_mfma_f32_16x16x32_bf16 v[0:3], v[52:55], v[208:211], v[0:3]\n"
        "s_waitcnt lgkmcnt(5)\n"
        "v_mul_f32 v184, s88, v184\n"
        "v_cvt_pk_bf16_f32 v184, v184, v185\n"
        "v_mfma_f32_16x16x32_bf16 v[4:7], v[60:63], v[208:211], v[4:7]\n"
        "ds_read_b64_tr_b16 v[32:33], v226 offset:17408\n"
        "v_cvt_pk_bf16_f32 v185, v186, v187\n"
        "v_mfma_f32_16x16x32_bf16 v[4:7], v[56:59], v[192:195], v[4:7]\n"
        "global_load_lds_dwordx4 v242, s[66:67] offset:0\n"
        "v_mfma_f32_16x16x32_bf16 v[68:71], v[56:59], v[196:199], v[68:71]\n"
        "s_waitcnt lgkmcnt(4)\n"
        "v_cvt_pk_bf16_f32 v186, v188, v189\n"
        "v_cvt_pk_bf16_f32 v187, v190, v191\n"
        "v_mfma_f32_16x16x32_bf16 v[68:71], v[60:63], v[212:215], v[68:71]\n"
        "ds_read_b64_tr_b16 v[34:35], v227 offset:17408\n"
        "v_accvgpr_write_b32 a196, v184\n"
        "v_mfma_f32_16x16x32_bf16 v[64:67], v[52:55], v[212:215], v[64:67]\n"
        "ds_read_b64_tr_b16 v[36:37], v226 offset:25600\n"
        "v_accvgpr_write_b32 a197, v185\n"
        "v_mfma_f32_16x16x32_bf16 v[64:67], v[48:51], v[196:199], v[64:67]\n"
        "ds_read_b64_tr_b16 v[38:39], v227 offset:25600\n"
        "v_accvgpr_write_b32 a198, v186\n"
        "v_mfma_f32_16x16x32_bf16 v[96:99], v[48:51], v[200:203], v[96:99]\n"
        "s_waitcnt lgkmcnt(4)\n"
        "v_accvgpr_write_b32 a199, v187\n"
        "v_accvgpr_read_b32 v185, a9\n"
        "v_mfma_f32_16x16x32_bf16 v[96:99], v[52:55], v[216:219], v[96:99]\n"
        "ds_read_b64_tr_b16 v[46:47], v229 offset:25600\n"
        "global_load_lds_dwordx4 v243, s[66:67] offset:1024\n"
        "v_mfma_f32_16x16x32_bf16 v[100:103], v[60:63], v[216:219], v[100:103]\n"
        "ds_read_b64_tr_b16 v[40:41], v228 offset:17408\n"
        "s_add_u32 s64, s64, 0x80\n"
        "s_addc_u32 s65, s65, 0\n"
        "v_mfma_f32_16x16x32_bf16 v[100:103], v[56:59], v[200:203], v[100:103]\n"
        "ds_read_b64_tr_b16 v[42:43], v229 offset:17408\n"
        "v_accvgpr_read_b32 v186, a10\n"
        "v_mfma_f32_16x16x32_bf16 v[132:135], v[56:59], v[204:207], v[132:135]\n"
        "ds_read_b64_tr_b16 v[56:57], v232 offset:17408\n"
        "v_accvgpr_read_b32 v187, a11\n"
        "v_mfma_f32_16x16x32_bf16 v[132:135], v[60:63], v[220:223], v[132:135]\n"
        "global_load_lds_dwordx4 v244, s[66:67] offset:2048\n"
        "v_mfma_f32_16x16x32_bf16 v[128:131], v[52:55], v[220:223], v[128:131]\n"
        "ds_read_b64_tr_b16 v[52:53], v230 offset:25600\n"
        "v_accvgpr_read_b32 v188, a12\n"
        "v_mfma_f32_16x16x32_bf16 v[128:131], v[48:51], v[204:207], v[128:131]\n"
        "ds_read_b64_tr_b16 v[48:49], v230 offset:17408\n"
        "v_accvgpr_read_b32 v189, a13\n"
        "v_mfma_f32_16x16x32_bf16 v[8:11], v[160:163], v[192:195], v[8:11]\n"
        "ds_read_b64_tr_b16 v[50:51], v231 offset:17408\n"
        "v_accvgpr_read_b32 v190, a14\n"
        "v_mfma_f32_16x16x32_bf16 v[8:11], v[164:167], v[208:211], v[8:11]\n"
        "global_load_lds_dwordx4 v245, s[66:67] offset:3072\n"
        "s_add_u32 m0, s73, 0x1c000\n"
        "v_mfma_f32_16x16x32_bf16 v[12:15], v[172:175], v[208:211], v[12:15]\n"
        "ds_read_b64_tr_b16 v[54:55], v231 offset:25600\n"
        "s_add_u32 s66, s66, 0x80\n"
        "s_addc_u32 s67, s67, 0\n"
        "v_mfma_f32_16x16x32_bf16 v[12:15], v[168:171], v[192:195], v[12:15]\n"
        "ds_read_b64_tr_b16 v[60:61], v232 offset:25600\n"
        "v_accvgpr_read_b32 v191, a15\n"
        "v_mfma_f32_16x16x32_bf16 v[76:79], v[168:171], v[196:199], v[76:79]\n"
        "ds_read_b64_tr_b16 v[62:63], v233 offset:25600\n"
        "v_mul_f32 v185, s88, v185\n"
        "v_mfma_f32_16x16x32_bf16 v[76:79], v[172:175], v[212:215], v[76:79]\n"
        "ds_read_b64_tr_b16 v[58:59], v233 offset:17408\n"
        "v_mul_f32 v186, s88, v186\n"
        "v_mfma_f32_16x16x32_bf16 v[72:75], v[164:167], v[212:215], v[72:75]\n"
        "v_mul_f32 v187, s88, v187\n"
        "v_mul_f32 v188, s88, v188\n"
        "v_mfma_f32_16x16x32_bf16 v[72:75], v[160:163], v[196:199], v[72:75]\n"
        "v_accvgpr_read_b32 v176, a0\n"
        "v_mul_f32 v176, s88, v176\n"
        "v_mfma_f32_16x16x32_bf16 v[104:107], v[160:163], v[200:203], v[104:107]\n"
        "v_cvt_pk_bf16_f32 v176, v176, v177\n"
        "v_cvt_pk_bf16_f32 v177, v178, v179\n"
        "v_mfma_f32_16x16x32_bf16 v[104:107], v[164:167], v[216:219], v[104:107]\n"
        "v_cvt_pk_bf16_f32 v178, v180, v181\n"
        "v_cvt_pk_bf16_f32 v179, v182, v183\n"
        "v_mfma_f32_16x16x32_bf16 v[108:111], v[172:175], v[216:219], v[108:111]\n"
        "v_accvgpr_write_b32 a200, v176\n"
        "v_accvgpr_write_b32 a201, v177\n"
        "v_mfma_f32_16x16x32_bf16 v[108:111], v[168:171], v[200:203], v[108:111]\n"
        "v_accvgpr_write_b32 a202, v178\n"
        "v_accvgpr_write_b32 a203, v179\n"
        "v_mfma_f32_16x16x32_bf16 v[140:143], v[168:171], v[204:207], v[140:143]\n"
        "s_waitcnt lgkmcnt(1)\n"
        "v_mul_f32 v189, s88, v189\n"
        "v_mul_f32 v190, s88, v190\n"
        "v_mfma_f32_16x16x32_bf16 v[140:143], v[172:175], v[220:223], v[140:143]\n"
        "s_waitcnt vmcnt(13) lgkmcnt(0)\n"
        "v_mul_f32 v191, s88, v191\n"
        "v_accvgpr_read_b32 v177, a33\n"
        "v_mfma_f32_16x16x32_bf16 v[136:139], v[164:167], v[220:223], v[136:139]\n"
        "s_barrier\n"
        "v_mfma_f32_16x16x32_bf16 v[136:139], v[160:163], v[204:207], v[136:139]\n"
        "global_load_lds_dwordx4 v238, s[68:69] offset:0\n"
        "ds_read_b64_tr_b16 v[160:161], v226 offset:32768\n"
        "v_mfma_f32_16x16x32_bf16 v[16:19], v[32:35], v[192:195], v[16:19]\n"
        "ds_read_b64_tr_b16 v[162:163], v227 offset:32768\n"
        "v_accvgpr_read_b32 v178, a34\n"
        "v_mfma_f32_16x16x32_bf16 v[16:19], v[36:39], v[208:211], v[16:19]\n"
        "ds_read_b64_tr_b16 v[164:165], v226 offset:40960\n"
        "v_accvgpr_read_b32 v179, a35\n"
        "v_mfma_f32_16x16x32_bf16 v[20:23], v[44:47], v[208:211], v[20:23]\n"
        "ds_read_b64_tr_b16 v[166:167], v227 offset:40960\n"
        "v_accvgpr_read_b32 v180, a36\n"
        "v_mfma_f32_16x16x32_bf16 v[20:23], v[40:43], v[192:195], v[20:23]\n"
        "ds_read_b64_tr_b16 v[172:173], v228 offset:40960\n"
        "global_load_lds_dwordx4 v239, s[68:69] offset:1024\n"
        "v_mfma_f32_16x16x32_bf16 v[84:87], v[40:43], v[196:199], v[84:87]\n"
        "ds_read_b64_tr_b16 v[174:175], v229 offset:40960\n"
        "v_accvgpr_read_b32 v181, a37\n"
        "v_mfma_f32_16x16x32_bf16 v[84:87], v[44:47], v[212:215], v[84:87]\n"
        "ds_read_b64_tr_b16 v[168:169], v228 offset:32768\n"
        "v_accvgpr_read_b32 v182, a38\n"
        "v_mfma_f32_16x16x32_bf16 v[80:83], v[36:39], v[212:215], v[80:83]\n"
        "ds_read_b64_tr_b16 v[170:171], v229 offset:32768\n"
        "v_accvgpr_read_b32 v183, a39\n"
        "v_mfma_f32_16x16x32_bf16 v[80:83], v[32:35], v[196:199], v[80:83]\n"
        "global_load_lds_dwordx4 v240, s[68:69] offset:2048\n"
        "v_mfma_f32_16x16x32_bf16 v[112:115], v[32:35], v[200:203], v[112:115]\n"
        "v_mul_f32 v177, s88, v177\n"
        "v_mul_f32 v178, s88, v178\n"
        "v_mfma_f32_16x16x32_bf16 v[112:115], v[36:39], v[216:219], v[112:115]\n"
        "v_mul_f32 v179, s88, v179\n"
        "v_mul_f32 v180, s88, v180\n"
        "v_mfma_f32_16x16x32_bf16 v[116:119], v[44:47], v[216:219], v[116:119]\n"
        "v_mul_f32 v181, s88, v181\n"
        "v_mul_f32 v182, s88, v182\n"
        "v_mfma_f32_16x16x32_bf16 v[116:119], v[40:43], v[200:203], v[116:119]\n"
        "global_load_lds_dwordx4 v241, s[68:69] offset:3072\n"
        "s_add_u32 m0, s72, 0x8000\n"
        "v_mfma_f32_16x16x32_bf16 v[148:151], v[40:43], v[204:207], v[148:151]\n"
        "s_add_u32 s68, s68, 0x80000\n"
        "s_addc_u32 s69, s69, 0\n"
        "v_mul_f32 v183, s88, v183\n"
        "v_mfma_f32_16x16x32_bf16 v[148:151], v[44:47], v[220:223], v[148:151]\n"
        "ds_read_b64_tr_b16 v[44:45], v232 offset:40960\n"
        "v_mfma_f32_16x16x32_bf16 v[144:147], v[36:39], v[220:223], v[144:147]\n"
        "ds_read_b64_tr_b16 v[36:37], v230 offset:40960\n"
        "v_mfma_f32_16x16x32_bf16 v[144:147], v[32:35], v[204:207], v[144:147]\n"
        "ds_read_b64_tr_b16 v[32:33], v230 offset:32768\n"
        "v_accvgpr_read_b32 v184, a8\n"
        "v_mfma_f32_16x16x32_bf16 v[24:27], v[48:51], v[192:195], v[24:27]\n"
        "global_load_lds_dwordx4 v242, s[64:65] offset:0\n"
        "v_mul_f32 v184, s88, v184\n"
        "v_mfma_f32_16x16x32_bf16 v[24:27], v[52:55], v[208:211], v[24:27]\n"
        "ds_read_b64_tr_b16 v[34:35], v231 offset:32768\n"
        "v_cvt_pk_bf16_f32 v184, v184, v185\n"
        "v_mfma_f32_16x16x32_bf16 v[28:31], v[60:63], v[208:211], v[28:31]\n"
        "ds_read_b64_tr_b16 v[38:39], v231 offset:40960\n"
        "v_cvt_pk_bf16_f32 v185, v186, v187\n"
        "v_mfma_f32_16x16x32_bf16 v[28:31], v[56:59], v[192:195], v[28:31]\n"
        "ds_read_b64_tr_b16 v[46:47], v233 offset:40960\n"
        "v_cvt_pk_bf16_f32 v186, v188, v189\n"
        "v_mfma_f32_16x16x32_bf16 v[92:95], v[56:59], v[196:199], v[92:95]\n"
        "ds_read_b64_tr_b16 v[40:41], v232 offset:32768\n"
        "v_cvt_pk_bf16_f32 v187, v190, v191\n"
        "v_mfma_f32_16x16x32_bf16 v[92:95], v[60:63], v[212:215], v[92:95]\n"
        "ds_read_b64_tr_b16 v[42:43], v233 offset:32768\n"
        "v_accvgpr_write_b32 a204, v184\n"
        "v_mfma_f32_16x16x32_bf16 v[88:91], v[52:55], v[212:215], v[88:91]\n"
        "ds_read_b128 v[192:195], v246 offset:2048\n"
        "v_accvgpr_write_b32 a205, v185\n"
        "v_mfma_f32_16x16x32_bf16 v[88:91], v[48:51], v[196:199], v[88:91]\n"
        "ds_read_b128 v[208:211], v247 offset:2048\n"
        "v_accvgpr_write_b32 a206, v186\n"
        "v_mfma_f32_16x16x32_bf16 v[120:123], v[48:51], v[200:203], v[120:123]\n"
        "global_load_lds_dwordx4 v243, s[64:65] offset:1024\n"
        "v_accvgpr_write_b32 a207, v187\n"
        "v_mfma_f32_16x16x32_bf16 v[120:123], v[52:55], v[216:219], v[120:123]\n"
        "ds_read_b128 v[196:199], v246 offset:0\n"
        "v_accvgpr_read_b32 v185, a41\n"
        "v_mfma_f32_16x16x32_bf16 v[124:127], v[60:63], v[216:219], v[124:127]\n"
        "ds_read_b128 v[212:215], v247 offset:0\n"
        "v_accvgpr_read_b32 v186, a42\n"
        "v_mfma_f32_16x16x32_bf16 v[124:127], v[56:59], v[200:203], v[124:127]\n"
        "ds_read_b128 v[200:203], v246 offset:6144\n"
        "v_accvgpr_read_b32 v187, a43\n"
        "v_mfma_f32_16x16x32_bf16 v[156:159], v[56:59], v[204:207], v[156:159]\n"
        "global_store_dwordx4 v254, a[232:235], s[70:71] offset:0 sc1\n"
        "ds_read_b128 v[216:219], v247 offset:6144\n"
        "ds_read_b64_tr_b16 v[56:57], v228 offset:33792\n"
        "v_accvgpr_read_b32 v188, a44\n"
        "v_mfma_f32_16x16x32_bf16 v[156:159], v[60:63], v[220:223], v[156:159]\n"
        "ds_read_b64_tr_b16 v[60:61], v228 offset:41984\n"
        "v_accvgpr_read_b32 v189, a45\n"
        "v_mfma_f32_16x16x32_bf16 v[152:155], v[52:55], v[220:223], v[152:155]\n"
        "ds_read_b128 v[220:223], v247 offset:4096\n"
        "v_accvgpr_read_b32 v190, a46\n"
        "v_mfma_f32_16x16x32_bf16 v[152:155], v[48:51], v[204:207], v[152:155]\n"
        "s_waitcnt lgkmcnt(7)\n"
        "global_load_lds_dwordx4 v244, s[64:65] offset:2048\n"
        "v_mfma_f32_16x16x32_bf16 v[0:3], v[160:163], v[192:195], v[0:3]\n"
        "ds_read_b128 v[204:207], v246 offset:4096\n"
        "v_accvgpr_read_b32 v191, a47\n"
        "v_mfma_f32_16x16x32_bf16 v[0:3], v[164:167], v[208:211], v[0:3]\n"
        "ds_read_b64_tr_b16 v[48:49], v226 offset:33792\n"
        "v_mul_f32 v185, s88, v185\n"
        "v_mfma_f32_16x16x32_bf16 v[4:7], v[172:175], v[208:211], v[4:7]\n"
        "ds_read_b64_tr_b16 v[50:51], v227 offset:33792\n"
        "v_mul_f32 v186, s88, v186\n"
        "v_mfma_f32_16x16x32_bf16 v[4:7], v[168:171], v[192:195], v[4:7]\n"
        "s_waitcnt lgkmcnt(7)\n"
        "global_load_lds_dwordx4 v245, s[64:65] offset:3072\n"
        "s_add_u32 m0, s72, 0x9000\n"
        "v_mfma_f32_16x16x32_bf16 v[68:71], v[168:171], v[196:199], v[68:71]\n"
        "ds_read_b64_tr_b16 v[52:53], v226 offset:41984\n"
        "s_add_u32 s64, s64, 0x80\n"
        "s_addc_u32 s65, s65, 0\n"
        "v_mfma_f32_16x16x32_bf16 v[68:71], v[172:175], v[212:215], v[68:71]\n"
        "ds_read_b64_tr_b16 v[54:55], v227 offset:41984\n"
        "v_mul_f32 v187, s88, v187\n"
        "v_mfma_f32_16x16x32_bf16 v[64:67], v[164:167], v[212:215], v[64:67]\n"
        "ds_read_b64_tr_b16 v[62:63], v229 offset:41984\n"
        "v_mul_f32 v188, s88, v188\n"
        "v_mfma_f32_16x16x32_bf16 v[64:67], v[160:163], v[196:199], v[64:67]\n"
        "ds_read_b64_tr_b16 v[58:59], v229 offset:33792\n"
        "global_load_lds_dwordx4 v242, s[66:67] offset:0\n"
        "v_mfma_f32_16x16x32_bf16 v[96:99], v[160:163], v[200:203], v[96:99]\n"
        "s_waitcnt lgkmcnt(6)\n"
        "v_accvgpr_read_b32 v176, a32\n"
        "v_mul_f32 v176, s88, v176\n"
        "v_mfma_f32_16x16x32_bf16 v[96:99], v[164:167], v[216:219], v[96:99]\n"
        "LHA5_%=:\n"
        "s_cbranch_execz LHA9_%=\n"
        "v_cvt_pk_bf16_f32 v176, v176, v177\n"
        "v_cvt_pk_bf16_f32 v177, v178, v179\n"
        "v_mfma_f32_16x16x32_bf16 v[100:103], v[172:175], v[216:219], v[100:103]\n"
        "v_cvt_pk_bf16_f32 v178, v180, v181\n"
        "v_cvt_pk_bf16_f32 v179, v182, v183\n"
        "v_mfma_f32_16x16x32_bf16 v[100:103], v[168:171], v[200:203], v[100:103]\n"
        "global_load_lds_dwordx4 v243, s[66:67] offset:1024\n"
        "v_mfma_f32_16x16x32_bf16 v[132:135], v[168:171], v[204:207], v[132:135]\n"
        "ds_read_b64_tr_b16 v[168:169], v232 offset:33792\n"
        "v_accvgpr_write_b32 a160, v176\n"
        "v_mfma_f32_16x16x32_bf16 v[132:135], v[172:175], v[220:223], v[132:135]\n"
        "ds_read_b64_tr_b16 v[172:173], v232 offset:41984\n"
        "v_accvgpr_write_b32 a161, v177\n"
        "v_mfma_f32_16x16x32_bf16 v[128:131], v[164:167], v[220:223], v[128:131]\n"
        "ds_read_b64_tr_b16 v[164:165], v230 offset:41984\n"
        "v_accvgpr_write_b32 a162, v178\n"
        "v_mfma_f32_16x16x32_bf16 v[128:131], v[160:163], v[204:207], v[128:131]\n"
        "global_load_lds_dwordx4 v244, s[66:67] offset:2048\n"
        "v_mfma_f32_16x16x32_bf16 v[8:11], v[32:35], v[192:195], v[8:11]\n"
        "ds_read_b64_tr_b16 v[160:161], v230 offset:33792\n"
        "v_accvgpr_write_b32 a163, v179\n"
        "v_mfma_f32_16x16x32_bf16 v[8:11], v[36:39], v[208:211], v[8:11]\n"
        "ds_read_b64_tr_b16 v[162:163], v231 offset:33792\n"
        "v_mul_f32 v189, s88, v189\n"
        "v_mfma_f32_16x16x32_bf16 v[12:15], v[44:47], v[208:211], v[12:15]\n"
        "ds_read_b64_tr_b16 v[166:167], v231 offset:41984\n"
        "v_mul_f32 v190, s88, v190\n"
        "v_mfma_f32_16x16x32_bf16 v[12:15], v[40:43], v[192:195], v[12:15]\n"
        "global_load_lds_dwordx4 v245, s[66:67] offset:3072\n"
        "s_add_u32 m0, s73, 0x20000\n"
        "v_mfma_f32_16x16x32_bf16 v[76:79], v[40:43], v[196:199], v[76:79]\n"
        "ds_read_b64_tr_b16 v[174:175], v233 offset:41984\n"
        "s_add_u32 s66, s66, 0x80\n"
        "s_addc_u32 s67, s67, 0\n"
        "v_mfma_f32_16x16x32_bf16 v[76:79], v[44:47], v[212:215], v[76:79]\n"
        "ds_read_b64_tr_b16 v[170:171], v233 offset:33792\n"
        "v_mul_f32 v191, s88, v191\n"
        "v_mfma_f32_16x16x32_bf16 v[72:75], v[36:39], v[212:215], v[72:75]\n"
        "v_accvgpr_read_b32 v177, a65\n"
        "v_accvgpr_read_b32 v178, a66\n"
        "v_mfma_f32_16x16x32_bf16 v[72:75], v[32:35], v[196:199], v[72:75]\n"
        "v_accvgpr_read_b32 v179, a67\n"
        "v_accvgpr_read_b32 v180, a68\n"
        "v_mfma_f32_16x16x32_bf16 v[104:107], v[32:35], v[200:203], v[104:107]\n"
        "v_accvgpr_read_b32 v181, a69\n"
        "v_accvgpr_read_b32 v182, a70\n"
        "v_mfma_f32_16x16x32_bf16 v[104:107], v[36:39], v[216:219], v[104:107]\n"
        "v_accvgpr_read_b32 v183, a71\n"
        "v_mul_f32 v177, s88, v177\n"
        "v_mfma_f32_16x16x32_bf16 v[108:111], v[44:47], v[216:219], v[108:111]\n"
        "v_mul_f32 v178, s88, v178\n"
        "v_mul_f32 v179, s88, v179\n"
        "v_mfma_f32_16x16x32_bf16 v[108:111], v[40:43], v[200:203], v[108:111]\n"
        "v_mul_f32 v180, s88, v180\n"
        "v_mul_f32 v181, s88, v181\n"
        "v_mfma_f32_16x16x32_bf16 v[140:143], v[40:43], v[204:207], v[140:143]\n"
        "s_waitcnt lgkmcnt(1)\n"
        "v_mul_f32 v182, s88, v182\n"
        "v_mul_f32 v183, s88, v183\n"
        "v_mfma_f32_16x16x32_bf16 v[140:143], v[44:47], v[220:223], v[140:143]\n"
        "s_waitcnt vmcnt(13) lgkmcnt(0)\n"
        "v_mfma_f32_16x16x32_bf16 v[136:139], v[36:39], v[220:223], v[136:139]\n"
        "s_barrier\n"
        "v_mfma_f32_16x16x32_bf16 v[136:139], v[32:35], v[204:207], v[136:139]\n"
        "global_load_lds_dwordx4 v238, s[68:69] offset:0\n"
        "v_accvgpr_read_b32 v184, a40\n"
        "v_mfma_f32_16x16x32_bf16 v[16:19], v[48:51], v[192:195], v[16:19]\n"
        "ds_read_b64_tr_b16 v[32:33], v226 offset:49152\n"
        "v_mul_f32 v184, s88, v184\n"
        "v_mfma_f32_16x16x32_bf16 v[16:19], v[52:55], v[208:211], v[16:19]\n"
        "ds_read_b64_tr_b16 v[34:35], v227 offset:49152\n"
        "v_cvt_pk_bf16_f32 v184, v184, v185\n"
        "v_mfma_f32_16x16x32_bf16 v[20:23], v[60:63], v[208:211], v[20:23]\n"
        "ds_read_b64_tr_b16 v[36:37], v226 offset:57344\n"
        "v_cvt_pk_bf16_f32 v185, v186, v187\n"
        "v_mfma_f32_16x16x32_bf16 v[20:23], v[56:59], v[192:195], v[20:23]\n"
        "ds_read_b64_tr_b16 v[38:39], v227 offset:57344\n"
        "v_cvt_pk_bf16_f32 v186, v188, v189\n"
        "v_mfma_f32_16x16x32_bf16 v[84:87], v[56:59], v[196:199], v[84:87]\n"
        "ds_read_b64_tr_b16 v[44:45], v228 offset:57344\n"
        "v_cvt_pk_bf16_f32 v187, v190, v191\n"
        "v_mfma_f32_16x16x32_bf16 v[84:87], v[60:63], v[212:215], v[84:87]\n"
        "ds_read_b64_tr_b16 v[46:47], v229 offset:57344\n"
        "v_accvgpr_write_b32 a164, v184\n"
        "v_mfma_f32_16x16x32_bf16 v[80:83], v[52:55], v[212:215], v[80:83]\n"
        "ds_read_b64_tr_b16 v[40:41], v228 offset:49152\n"
        "v_accvgpr_write_b32 a165, v185\n"
        "v_mfma_f32_16x16x32_bf16 v[80:83], v[48:51], v[196:199], v[80:83]\n"
        "ds_read_b64_tr_b16 v[42:43], v229 offset:49152\n"
        "v_accvgpr_write_b32 a166, v186\n"
        "v_mfma_f32_16x16x32_bf16 v[112:115], v[48:51], v[200:203], v[112:115]\n"
        "global_load_lds_dwordx4 v239, s[68:69] offset:1024\n"
        "v_accvgpr_write_b32 a167, v187\n"
        "v_mfma_f32_16x16x32_bf16 v[112:115], v[52:55], v[216:219], v[112:115]\n"
        "v_accvgpr_read_b32 v185, a73\n"
        "v_accvgpr_read_b32 v186, a74\n"
        "v_mfma_f32_16x16x32_bf16 v[116:119], v[60:63], v[216:219], v[116:119]\n"
        "v_accvgpr_read_b32 v187, a75\n"
        "v_accvgpr_read_b32 v188, a76\n"
        "v_mfma_f32_16x16x32_bf16 v[116:119], v[56:59], v[200:203], v[116:119]\n"
        "v_accvgpr_read_b32 v189, a77\n"
        "v_accvgpr_read_b32 v190, a78\n"
        "v_mfma_f32_16x16x32_bf16 v[148:151], v[56:59], v[204:207], v[148:151]\n"
        "global_load_lds_dwordx4 v240, s[68:69] offset:2048\n"
        "ds_read_b64_tr_b16 v[56:57], v232 offset:49152\n"
        "v_mfma_f32_16x16x32_bf16 v[148:151], v[60:63], v[220:223], v[148:151]\n"
        "ds_read_b64_tr_b16 v[60:61], v232 offset:57344\n"
        "v_accvgpr_read_b32 v191, a79\n"
        "v_mfma_f32_16x16x32_bf16 v[144:147], v[52:55], v[220:223], v[144:147]\n"
        "ds_read_b64_tr_b16 v[52:53], v230 offset:57344\n"
        "v_mul_f32 v185, s88, v185\n"
        "v_mfma_f32_16x16x32_bf16 v[144:147], v[48:51], v[204:207], v[144:147]\n"
        "ds_read_b64_tr_b16 v[48:49], v230 offset:49152\n"
        "v_mul_f32 v186, s88, v186\n"
        "v_mfma_f32_16x16x32_bf16 v[24:27], v[160:163], v[192:195], v[24:27]\n"
        "global_load_lds_dwordx4 v241, s[68:69] offset:3072\n"
        "ds_read_b64_tr_b16 v[50:51], v231 offset:49152\n"
        "s_add_u32 m0, s72, 0x0\n"
        "v_mfma_f32_16x16x32_bf16 v[24:27], v[164:167], v[208:211], v[24:27]\n"
        "ds_read_b64_tr_b16 v[54:55], v231 offset:57344\n"
        "s_add_u32 s68, s68, 0x80000\n"
        "s_addc_u32 s69, s69, 0\n"
        "v_mfma_f32_16x16x32_bf16 v[28:31], v[172:175], v[208:211], v[28:31]\n"
        "ds_read_b64_tr_b16 v[62:63], v233 offset:57344\n"
        "v_mul_f32 v187, s88, v187\n"
        "v_mfma_f32_16x16x32_bf16 v[28:31], v[168:171], v[192:195], v[28:31]\n"
        "ds_read_b64_tr_b16 v[58:59], v233 offset:49152\n"
        "v_mul_f32 v188, s88, v188\n"
        "v_mfma_f32_16x16x32_bf16 v[92:95], v[168:171], v[196:199], v[92:95]\n"
        "global_load_lds_dwordx4 v242, s[64:65] offset:0\n"
        "ds_read_b128 v[192:195], v248 offset:2048\n"
        "v_mfma_f32_16x16x32_bf16 v[92:95], v[172:175], v[212:215], v[92:95]\n"
        "ds_read_b128 v[208:211], v249 offset:2048\n"
        "v_mul_f32 v189, s88, v189\n"
        "v_mfma_f32_16x16x32_bf16 v[88:91], v[164:167], v[212:215], v[88:91]\n"
        "ds_read_b128 v[212:215], v249 offset:0\n"
        "v_mul_f32 v190, s88, v190\n"
        "v_mfma_f32_16x16x32_bf16 v[88:91], v[160:163], v[196:199], v[88:91]\n"
        "ds_read_b128 v[196:199], v248 offset:0\n"
        "v_accvgpr_read_b32 v176, a64\n"
        "v_mfma_f32_16x16x32_bf16 v[120:123], v[160:163], v[200:203], v[120:123]\n"
        "global_load_lds_dwordx4 v243, s[64:65] offset:1024\n"
        "v_mul_f32 v176, s88, v176\n"
        "v_mfma_f32_16x16x32_bf16 v[120:123], v[164:167], v[216:219], v[120:123]\n"
        "v_cvt_pk_bf16_f32 v176, v176, v177\n"
        "v_cvt_pk_bf16_f32 v177, v178, v179\n"
        "v_mfma_f32_16x16x32_bf16 v[124:127], v[172:175], v[216:219], v[124:127]\n"
        "ds_read_b128 v[216:219], v249 offset:6144\n"
        "v_cvt_pk_bf16_f32 v178, v180, v181\n"
        "v_mfma_f32_16x16x32_bf16 v[124:127], v[168:171], v[200:203], v[124:127]\n"
        "ds_read_b128 v[200:203], v248 offset:6144\n"
        "v_cvt_pk_bf16_f32 v179, v182, v183\n"
        "v_mfma_f32_16x16x32_bf16 v[156:159], v[168:171], v[204:207], v[156:159]\n"
        "s_waitcnt lgkmcnt(5)\n"
        "v_accvgpr_write_b32 a168, v176\n"
        "v_accvgpr_write_b32 a169, v177\n"
        "v_mfma_f32_16x16x32_bf16 v[156:159], v[172:175], v[220:223], v[156:159]\n"
        "s_waitcnt lgkmcnt(4)\n"
        "v_accvgpr_write_b32 a170, v178\n"
        "v_accvgpr_write_b32 a171, v179\n"
        "v_mfma_f32_16x16x32_bf16 v[152:155], v[164:167], v[220:223], v[152:155]\n"
        "global_load_lds_dwordx4 v244, s[64:65] offset:2048\n"
        "ds_read_b128 v[220:223], v249 offset:4096\n"
        "v_mfma_f32_16x16x32_bf16 v[152:155], v[160:163], v[204:207], v[152:155]\n"
        "ds_read_b128 v[204:207], v248 offset:4096\n"
        "v_mul_f32 v191, s88, v191\n"
        "v_mfma_f32_16x16x32_bf16 v[0:3], v[32:35], v[192:195], v[0:3]\n"
        "ds_read_b64_tr_b16 v[160:161], v226 offset:50176\n"
        "v_accvgpr_read_b32 v177, a97\n"
        "v_mfma_f32_16x16x32_bf16 v[0:3], v[36:39], v[208:211], v[0:3]\n"
        "ds_read_b64_tr_b16 v[162:163], v227 offset:50176\n"
        "v_accvgpr_read_b32 v178, a98\n"
        "v_mfma_f32_16x16x32_bf16 v[4:7], v[44:47], v[208:211], v[4:7]\n"
        "global_store_dwordx4 v254, a[236:239], s[70:71] offset:1024 sc1\n"
        "ds_read_b64_tr_b16 v[164:165], v226 offset:58368\n"
        "ds_read_b64_tr_b16 v[166:167], v227 offset:58368\n"
        "v_accvgpr_read_b32 v179, a99\n"
        "v_mfma_f32_16x16x32_bf16 v[4:7], v[40:43], v[192:195], v[4:7]\n"
        "ds_read_b64_tr_b16 v[172:173], v228 offset:58368\n"
        "s_waitcnt lgkmcnt(7)\n"
        "v_mfma_f32_16x16x32_bf16 v[68:71], v[40:43], v[196:199], v[68:71]\n"
        "ds_read_b64_tr_b16 v[174:175], v229 offset:58368\n"
        "v_accvgpr_read_b32 v180, a100\n"
        "v_mfma_f32_16x16x32_bf16 v[68:71], v[44:47], v[212:215], v[68:71]\n"
        "global_load_lds_dwordx4 v245, s[64:65] offset:3072\n"
        "s_add_u32 m0, s72, 0x1000\n"
        "v_mfma_f32_16x16x32_bf16 v[64:67], v[36:39], v[212:215], v[64:67]\n"
        "ds_read_b64_tr_b16 v[168:169], v228 offset:50176\n"
        "s_add_u32 s64, s64, 0x80\n"
        "s_addc_u32 s65, s65, 0\n"
        "v_mfma_f32_16x16x32_bf16 v[64:67], v[32:35], v[196:199], v[64:67]\n"
        "ds_read_b64_tr_b16 v[170:171], v229 offset:50176\n"
        "v_accvgpr_read_b32 v181, a101\n"
        "v_mfma_f32_16x16x32_bf16 v[96:99], v[32:35], v[200:203], v[96:99]\n"
        "s_waitcnt lgkmcnt(7)\n"
        "v_accvgpr_read_b32 v182, a102\n"
        "v_accvgpr_read_b32 v183, a103\n"
        "v_mfma_f32_16x16x32_bf16 v[96:99], v[36:39], v[216:219], v[96:99]\n"
        "global_load_lds_dwordx4 v242, s[66:67] offset:0\n"
        "v_mfma_f32_16x16x32_bf16 v[100:103], v[44:47], v[216:219], v[100:103]\n"
        "s_add_u32 s70, s38, s97\n"
        "s_addc_u32 s71, s39, 0\n"
        "v_mul_f32 v177, s88, v177\n"
        "v_mfma_f32_16x16x32_bf16 v[100:103], v[40:43], v[200:203], v[100:103]\n"
        "v_mul_f32 v178, s88, v178\n"
        "v_mul_f32 v179, s88, v179\n"
        "v_mfma_f32_16x16x32_bf16 v[132:135], v[40:43], v[204:207], v[132:135]\n"
        "ds_read_b64_tr_b16 v[40:41], v232 offset:50176\n"
        "v_mul_f32 v180, s88, v180\n"
        "v_mfma_f32_16x16x32_bf16 v[132:135], v[44:47], v[220:223], v[132:135]\n"
        "global_load_lds_dwordx4 v243, s[66:67] offset:1024\n"
        "s_add_u32 s97, s52, 0x0\n"
        "v_mfma_f32_16x16x32_bf16 v[128:131], v[36:39], v[220:223], v[128:131]\n"
        "ds_read_b64_tr_b16 v[36:37], v230 offset:58368\n"
        "s_add_u32 s97, s97, s75\n"
        "v_mul_f32 v181, s88, v181\n"
        "v_mfma_f32_16x16x32_bf16 v[128:131], v[32:35], v[204:207], v[128:131]\n"
        "ds_read_b64_tr_b16 v[32:33], v230 offset:50176\n"
        "v_accvgpr_read_b32 v184, a72\n"
        "v_mfma_f32_16x16x32_bf16 v[8:11], v[48:51], v[192:195], v[8:11]\n"
        "ds_read_b64_tr_b16 v[34:35], v231 offset:50176\n"
        "v_mul_f32 v184, s88, v184\n"
        "v_mfma_f32_16x16x32_bf16 v[8:11], v[52:55], v[208:211], v[8:11]\n"
        "global_load_lds_dwordx4 v244, s[66:67] offset:2048\n"
        "v_mfma_f32_16x16x32_bf16 v[12:15], v[60:63], v[208:211], v[12:15]\n"
        "ds_read_b64_tr_b16 v[38:39], v231 offset:58368\n"
        "v_cvt_pk_bf16_f32 v184, v184, v185\n"
        "v_mfma_f32_16x16x32_bf16 v[12:15], v[56:59], v[192:195], v[12:15]\n"
        "ds_read_b64_tr_b16 v[44:45], v232 offset:58368\n"
        "v_cvt_pk_bf16_f32 v185, v186, v187\n"
        "v_mfma_f32_16x16x32_bf16 v[76:79], v[56:59], v[196:199], v[76:79]\n"
        "ds_read_b64_tr_b16 v[46:47], v233 offset:58368\n"
        "v_cvt_pk_bf16_f32 v186, v188, v189\n"
        "v_mfma_f32_16x16x32_bf16 v[76:79], v[60:63], v[212:215], v[76:79]\n"
        "global_load_lds_dwordx4 v245, s[66:67] offset:3072\n"
        "s_add_u32 m0, s73, 0x24000\n"
        "v_mfma_f32_16x16x32_bf16 v[72:75], v[52:55], v[212:215], v[72:75]\n"
        "ds_read_b64_tr_b16 v[42:43], v233 offset:50176\n"
        "v_cvt_pk_bf16_f32 v187, v190, v191\n"
        "v_mfma_f32_16x16x32_bf16 v[72:75], v[48:51], v[196:199], v[72:75]\n"
        "s_add_u32 s66, s66, 0x80\n"
        "s_addc_u32 s67, s67, 0\n"
        "v_accvgpr_write_b32 a172, v184\n"
        "v_mfma_f32_16x16x32_bf16 v[104:107], v[48:51], v[200:203], v[104:107]\n"
        "v_accvgpr_write_b32 a173, v185\n"
        "v_accvgpr_write_b32 a174, v186\n"
        "v_mfma_f32_16x16x32_bf16 v[104:107], v[52:55], v[216:219], v[104:107]\n"
        "v_accvgpr_write_b32 a175, v187\n"
        "v_mul_f32 v182, s88, v182\n"
        "v_mfma_f32_16x16x32_bf16 v[108:111], v[60:63], v[216:219], v[108:111]\n"
        "v_mul_f32 v183, s88, v183\n"
        "v_accvgpr_read_b32 v185, a105\n"
        "v_mfma_f32_16x16x32_bf16 v[108:111], v[56:59], v[200:203], v[108:111]\n"
        "v_accvgpr_read_b32 v186, a106\n"
        "v_accvgpr_read_b32 v187, a107\n"
        "v_mfma_f32_16x16x32_bf16 v[140:143], v[56:59], v[204:207], v[140:143]\n"
        "s_waitcnt lgkmcnt(1)\n"
        "v_accvgpr_read_b32 v188, a108\n"
        "v_accvgpr_read_b32 v189, a109\n"
        "v_mfma_f32_16x16x32_bf16 v[140:143], v[60:63], v[220:223], v[140:143]\n"
        "v_accvgpr_read_b32 v190, a110\n"
        "v_accvgpr_read_b32 v191, a111\n"
        "v_mfma_f32_16x16x32_bf16 v[136:139], v[52:55], v[220:223], v[136:139]\n"
        "s_waitcnt vmcnt(13) lgkmcnt(0)\n"
        "v_mul_f32 v185, s88, v185\n"
        "v_mul_f32 v186, s88, v186\n"
        "v_mfma_f32_16x16x32_bf16 v[136:139], v[48:51], v[204:207], v[136:139]\n"
        "s_barrier\n"
        "v_mfma_f32_16x16x32_bf16 v[16:19], v[160:163], v[192:195], v[16:19]\n"
        "global_load_lds_dwordx4 v238, s[68:69] offset:0\n"
        "ds_read_b64_tr_b16 v[48:49], v226 offset:16384\n"
        "v_mfma_f32_16x16x32_bf16 v[16:19], v[164:167], v[208:211], v[16:19]\n"
        "ds_read_b64_tr_b16 v[50:51], v227 offset:16384\n"
        "v_mul_f32 v187, s88, v187\n"
        "v_mfma_f32_16x16x32_bf16 v[20:23], v[172:175], v[208:211], v[20:23]\n"
        "ds_read_b64_tr_b16 v[52:53], v226 offset:24576\n"
        "v_mul_f32 v188, s88, v188\n"
        "v_mfma_f32_16x16x32_bf16 v[20:23], v[168:171], v[192:195], v[20:23]\n"
        "ds_read_b64_tr_b16 v[54:55], v227 offset:24576\n"
        "v_mul_f32 v189, s88, v189\n"
        "v_mfma_f32_16x16x32_bf16 v[84:87], v[168:171], v[196:199], v[84:87]\n"
        "ds_read_b64_tr_b16 v[60:61], v228 offset:24576\n"
        "global_load_lds_dwordx4 v239, s[68:69] offset:1024\n"
        "v_mfma_f32_16x16x32_bf16 v[84:87], v[172:175], v[212:215], v[84:87]\n"
        "ds_read_b64_tr_b16 v[62:63], v229 offset:24576\n"
        "v_mul_f32 v190, s88, v190\n"
        "v_mfma_f32_16x16x32_bf16 v[80:83], v[164:167], v[212:215], v[80:83]\n"
        "ds_read_b64_tr_b16 v[56:57], v228 offset:16384\n"
        "v_mul_f32 v191, s88, v191\n"
        "v_mfma_f32_16x16x32_bf16 v[80:83], v[160:163], v[196:199], v[80:83]\n"
        "ds_read_b64_tr_b16 v[58:59], v229 offset:16384\n"
        "v_accvgpr_read_b32 v176, a96\n"
        "v_mfma_f32_16x16x32_bf16 v[112:115], v[160:163], v[200:203], v[112:115]\n"
        "global_load_lds_dwordx4 v240, s[68:69] offset:2048\n"
        "v_mfma_f32_16x16x32_bf16 v[112:115], v[164:167], v[216:219], v[112:115]\n"
        "v_mul_f32 v176, s88, v176\n"
        "v_cvt_pk_bf16_f32 v176, v176, v177\n"
        "v_mfma_f32_16x16x32_bf16 v[116:119], v[172:175], v[216:219], v[116:119]\n"
        "v_cvt_pk_bf16_f32 v177, v178, v179\n"
        "v_cvt_pk_bf16_f32 v178, v180, v181\n"
        "v_mfma_f32_16x16x32_bf16 v[116:119], v[168:171], v[200:203], v[116:119]\n"
        "v_cvt_pk_bf16_f32 v179, v182, v183\n"
        "v_accvgpr_write_b32 a0, v176\n"
        "v_mfma_f32_16x16x32_bf16 v[148:151], v[168:171], v[204:207], v[148:151]\n"
        "ds_read_b64_tr_b16 v[168:169], v232 offset:16384\n"
        "v_accvgpr_write_b32 a1, v177\n"
        "v_mfma_f32_16x16x32_bf16 v[148:151], v[172:175], v[220:223], v[148:151]\n"
        "global_load_lds_dwordx4 v241, s[68:69] offset:3072\n"
        "s_add_u32 m0, s72, 0x10000\n"
        "v_accvgpr_write_b32 a2, v178\n"
        "v_mfma_f32_16x16x32_bf16 v[144:147], v[164:167], v[220:223], v[144:147]\n"
        "ds_read_b64_tr_b16 v[164:165], v230 offset:24576\n"
        "v_accvgpr_write_b32 a3, v179\n"
        "v_mfma_f32_16x16x32_bf16 v[144:147], v[160:163], v[204:207], v[144:147]\n"
        "ds_read_b64_tr_b16 v[160:161], v230 offset:16384\n"
        "s_add_u32 s68, s68, 0x80000\n"
        "s_addc_u32 s69, s69, 0\n"
        "v_mfma_f32_16x16x32_bf16 v[24:27], v[32:35], v[192:195], v[24:27]\n"
        "ds_read_b64_tr_b16 v[162:163], v231 offset:16384\n"
        "v_accvgpr_read_b32 v177, a129\n"
        "v_mfma_f32_16x16x32_bf16 v[24:27], v[36:39], v[208:211], v[24:27]\n"
        "ds_read_b64_tr_b16 v[166:167], v231 offset:24576\n"
        "global_load_lds_dwordx4 v242, s[64:65] offset:0\n"
        "v_mfma_f32_16x16x32_bf16 v[28:31], v[44:47], v[208:211], v[28:31]\n"
        "ds_read_b64_tr_b16 v[172:173], v232 offset:24576\n"
        "v_accvgpr_read_b32 v178, a130\n"
        "v_mfma_f32_16x16x32_bf16 v[28:31], v[40:43], v[192:195], v[28:31]\n"
        "ds_read_b64_tr_b16 v[174:175], v233 offset:24576\n"
        "v_accvgpr_read_b32 v179, a131\n"
        "v_mfma_f32_16x16x32_bf16 v[92:95], v[40:43], v[196:199], v[92:95]\n"
        "ds_read_b64_tr_b16 v[170:171], v233 offset:16384\n"
        "v_accvgpr_read_b32 v180, a132\n"
        "v_mfma_f32_16x16x32_bf16 v[92:95], v[44:47], v[212:215], v[92:95]\n"
        "ds_read_b128 v[192:195], v246 offset:34816\n"
        "global_load_lds_dwordx4 v243, s[64:65] offset:1024\n"
        "v_mfma_f32_16x16x32_bf16 v[88:91], v[36:39], v[212:215], v[88:91]\n"
        "ds_read_b128 v[208:211], v247 offset:34816\n"
        "v_accvgpr_read_b32 v181, a133\n"
        "v_mfma_f32_16x16x32_bf16 v[88:91], v[32:35], v[196:199], v[88:91]\n"
        "ds_read_b128 v[196:199], v246 offset:32768\n"
        "v_accvgpr_read_b32 v182, a134\n"
        "v_mfma_f32_16x16x32_bf16 v[120:123], v[32:35], v[200:203], v[120:123]\n"
        "ds_read_b128 v[212:215], v247 offset:32768\n"
        "v_accvgpr_read_b32 v183, a135\n"
        "v_mfma_f32_16x16x32_bf16 v[120:123], v[36:39], v[216:219], v[120:123]\n"
        "global_load_lds_dwordx4 v244, s[64:65] offset:2048\n"
        "v_mfma_f32_16x16x32_bf16 v[124:127], v[44:47], v[216:219], v[124:127]\n"
        "ds_read_b128 v[216:219], v247 offset:38912\n"
        "v_mul_f32 v177, s88, v177\n"
        "v_mfma_f32_16x16x32_bf16 v[124:127], v[40:43], v[200:203], v[124:127]\n"
        "ds_read_b128 v[200:203], v246 offset:38912\n"
        "v_mul_f32 v178, s88, v178\n"
        "v_mfma_f32_16x16x32_bf16 v[156:159], v[40:43], v[204:207], v[156:159]\n"
        "ds_read_b64_tr_b16 v[40:41], v228 offset:17408\n"
        "v_mul_f32 v179, s88, v179\n"
        "v_mfma_f32_16x16x32_bf16 v[156:159], v[44:47], v[220:223], v[156:159]\n"
        "global_load_lds_dwordx4 v245, s[64:65] offset:3072\n"
        "s_add_u32 m0, s72, 0x11000\n"
        "v_mfma_f32_16x16x32_bf16 v[152:155], v[36:39], v[220:223], v[152:155]\n"
        "ds_read_b128 v[220:223], v247 offset:36864\n"
        "s_add_u32 s64, s64, 0x80\n"
        "s_addc_u32 s65, s65, 0\n"
        "v_mfma_f32_16x16x32_bf16 v[152:155], v[32:35], v[204:207], v[152:155]\n"
        "ds_read_b128 v[204:207], v246 offset:36864\n"
        "s_waitcnt lgkmcnt(6)\n"
        "v_mfma_f32_16x16x32_bf16 v[0:3], v[48:51], v[192:195], v[0:3]\n"
        "ds_read_b64_tr_b16 v[32:33], v226 offset:17408\n"
        "v_accvgpr_read_b32 v184, a104\n"
        "v_mfma_f32_16x16x32_bf16 v[0:3], v[52:55], v[208:211], v[0:3]\n"
        "s_waitcnt lgkmcnt(6)\n"
        "v_mul_f32 v184, s88, v184\n"
        "v_cvt_pk_bf16_f32 v184, v184, v185\n"
        "v_mfma_f32_16x16x32_bf16 v[4:7], v[60:63], v[208:211], v[4:7]\n"
        "global_load_lds_dwordx4 v242, s[66:67] offset:0\n"
        "v_cvt_pk_bf16_f32 v185, v186, v187\n"
        "v_mfma_f32_16x16x32_bf16 v[4:7], v[56:59], v[192:195], v[4:7]\n"
        "ds_read_b64_tr_b16 v[34:35], v227 offset:17408\n"
        "v_cvt_pk_bf16_f32 v186, v188, v189\n"
        "v_mfma_f32_16x16x32_bf16 v[68:71], v[56:59], v[196:199], v[68:71]\n"
        "s_waitcnt lgkmcnt(4)\n"
        "v_cvt_pk_bf16_f32 v187, v190, v191\n"
        "v_accvgpr_write_b32 a4, v184\n"
        "v_mfma_f32_16x16x32_bf16 v[68:71], v[60:63], v[212:215], v[68:71]\n"
        "ds_read_b64_tr_b16 v[36:37], v226 offset:25600\n"
        "v_accvgpr_write_b32 a5, v185\n"
        "v_mfma_f32_16x16x32_bf16 v[64:67], v[52:55], v[212:215], v[64:67]\n"
        "ds_read_b64_tr_b16 v[38:39], v227 offset:25600\n"
        "v_accvgpr_write_b32 a6, v186\n"
        "v_mfma_f32_16x16x32_bf16 v[64:67], v[48:51], v[196:199], v[64:67]\n"
        "ds_read_b64_tr_b16 v[44:45], v228 offset:25600\n"
        "v_accvgpr_write_b32 a7, v187\n"
        "v_mfma_f32_16x16x32_bf16 v[96:99], v[48:51], v[200:203], v[96:99]\n"
        "ds_read_b64_tr_b16 v[46:47], v229 offset:25600\n"
        "global_load_lds_dwordx4 v243, s[66:67] offset:1024\n"
        "v_mfma_f32_16x16x32_bf16 v[96:99], v[52:55], v[216:219], v[96:99]\n"
        "ds_read_b64_tr_b16 v[42:43], v229 offset:17408\n"
        "v_mul_f32 v180, s88, v180\n"
        "v_mfma_f32_16x16x32_bf16 v[100:103], v[60:63], v[216:219], v[100:103]\n"
        "s_waitcnt lgkmcnt(6)\n"
        "v_mul_f32 v181, s88, v181\n"
        "v_mul_f32 v182, s88, v182\n"
        "v_mfma_f32_16x16x32_bf16 v[100:103], v[56:59], v[200:203], v[100:103]\n"
        "v_mul_f32 v183, s88, v183\n"
        "v_accvgpr_read_b32 v185, a137\n"
        "v_mfma_f32_16x16x32_bf16 v[132:135], v[56:59], v[204:207], v[132:135]\n"
        "global_load_lds_dwordx4 v244, s[66:67] offset:2048\n"
        "v_mfma_f32_16x16x32_bf16 v[132:135], v[60:63], v[220:223], v[132:135]\n"
        "ds_read_b64_tr_b16 v[60:61], v232 offset:25600\n"
        "v_accvgpr_read_b32 v186, a138\n"
        "v_mfma_f32_16x16x32_bf16 v[128:131], v[52:55], v[220:223], v[128:131]\n"
        "ds_read_b64_tr_b16 v[52:53], v230 offset:25600\n"
        "v_accvgpr_read_b32 v187, a139\n"
        "v_mfma_f32_16x16x32_bf16 v[128:131], v[48:51], v[204:207], v[128:131]\n"
        "ds_read_b64_tr_b16 v[48:49], v230 offset:17408\n"
        "v_accvgpr_read_b32 v188, a140\n"
        "v_mfma_f32_16x16x32_bf16 v[8:11], v[160:163], v[192:195], v[8:11]\n"
        "global_load_lds_dwordx4 v245, s[66:67] offset:3072\n"
        "s_add_u32 m0, s73, 0x1c000\n"
        "v_mfma_f32_16x16x32_bf16 v[8:11], v[164:167], v[208:211], v[8:11]\n"
        "ds_read_b64_tr_b16 v[50:51], v231 offset:17408\n"
        "s_add_u32 s66, s66, 0x80\n"
        "s_addc_u32 s67, s67, 0\n"
        "v_mfma_f32_16x16x32_bf16 v[12:15], v[172:175], v[208:211], v[12:15]\n"
        "ds_read_b64_tr_b16 v[54:55], v231 offset:25600\n"
        "v_accvgpr_read_b32 v189, a141\n"
        "v_mfma_f32_16x16x32_bf16 v[12:15], v[168:171], v[192:195], v[12:15]\n"
        "ds_read_b64_tr_b16 v[62:63], v233 offset:25600\n"
        "v_accvgpr_read_b32 v190, a142\n"
        "v_mfma_f32_16x16x32_bf16 v[76:79], v[168:171], v[196:199], v[76:79]\n"
        "ds_read_b64_tr_b16 v[56:57], v232 offset:17408\n"
        "v_accvgpr_read_b32 v191, a143\n"
        "v_mfma_f32_16x16x32_bf16 v[76:79], v[172:175], v[212:215], v[76:79]\n"
        "global_store_dwordx4 v254, a[192:195], s[70:71] offset:0 sc1\n"
        "ds_read_b64_tr_b16 v[58:59], v233 offset:17408\n"
        "v_mul_f32 v185, s88, v185\n"
        "v_mul_f32 v186, s88, v186\n"
        "v_mul_f32 v187, s88, v187\n"
        "v_mfma_f32_16x16x32_bf16 v[72:75], v[164:167], v[212:215], v[72:75]\n"
        "v_mul_f32 v188, s88, v188\n"
        "v_mul_f32 v189, s88, v189\n"
        "v_mfma_f32_16x16x32_bf16 v[72:75], v[160:163], v[196:199], v[72:75]\n"
        "v_accvgpr_read_b32 v176, a128\n"
        "v_mul_f32 v176, s88, v176\n"
        "v_mfma_f32_16x16x32_bf16 v[104:107], v[160:163], v[200:203], v[104:107]\n"
        "v_cvt_pk_bf16_f32 v176, v176, v177\n"
        "v_cvt_pk_bf16_f32 v177, v178, v179\n"
        "v_mfma_f32_16x16x32_bf16 v[104:107], v[164:167], v[216:219], v[104:107]\n"
        "v_cvt_pk_bf16_f32 v178, v180, v181\n"
        "v_cvt_pk_bf16_f32 v179, v182, v183\n"
        "v_mfma_f32_16x16x32_bf16 v[108:111], v[172:175], v[216:219], v[108:111]\n"
        "v_accvgpr_write_b32 a8, v176\n"
        "v_accvgpr_write_b32 a9, v177\n"
        "v_mfma_f32_16x16x32_bf16 v[108:111], v[168:171], v[200:203], v[108:111]\n"
        "v_accvgpr_write_b32 a10, v178\n"
        "v_accvgpr_write_b32 a11, v179\n"
        "v_mfma_f32_16x16x32_bf16 v[140:143], v[168:171], v[204:207], v[140:143]\n"
        "s_waitcnt vmcnt(13) lgkmcnt(0)\n"
        "v_mfma_f32_16x16x32_bf16 v[140:143], v[172:175], v[220:223], v[140:143]\n"
        "s_barrier\n"
        "v_mfma_f32_16x16x32_bf16 v[136:139], v[164:167], v[220:223], v[136:139]\n"
        "global_load_lds_dwordx4 v238, s[68:69] offset:0\n"
        "ds_read_b64_tr_b16 v[164:165], v226 offset:40960\n"
        "v_mfma_f32_16x16x32_bf16 v[136:139], v[160:163], v[204:207], v[136:139]\n"
        "ds_read_b64_tr_b16 v[160:161], v226 offset:32768\n"
        "v_mul_f32 v190, s88, v190\n"
        "v_mfma_f32_16x16x32_bf16 v[16:19], v[32:35], v[192:195], v[16:19]\n"
        "ds_read_b64_tr_b16 v[162:163], v227 offset:32768\n"
        "v_mul_f32 v191, s88, v191\n"
        "v_mfma_f32_16x16x32_bf16 v[16:19], v[36:39], v[208:211], v[16:19]\n"
        "ds_read_b64_tr_b16 v[166:167], v227 offset:40960\n"
        "v_accvgpr_read_b32 v177, a209\n"
        "v_mfma_f32_16x16x32_bf16 v[20:23], v[44:47], v[208:211], v[20:23]\n"
        "ds_read_b64_tr_b16 v[172:173], v228 offset:40960\n"
        "global_load_lds_dwordx4 v239, s[68:69] offset:1024\n"
        "v_mfma_f32_16x16x32_bf16 v[20:23], v[40:43], v[192:195], v[20:23]\n"
        "ds_read_b64_tr_b16 v[174:175], v229 offset:40960\n"
        "v_accvgpr_read_b32 v178, a210\n"
        "v_mfma_f32_16x16x32_bf16 v[84:87], v[40:43], v[196:199], v[84:87]\n"
        "ds_read_b64_tr_b16 v[168:169], v228 offset:32768\n"
        "v_accvgpr_read_b32 v179, a211\n"
        "v_mfma_f32_16x16x32_bf16 v[84:87], v[44:47], v[212:215], v[84:87]\n"
        "ds_read_b64_tr_b16 v[170:171], v229 offset:32768\n"
        "v_accvgpr_read_b32 v180, a240\n"
        "v_mfma_f32_16x16x32_bf16 v[80:83], v[36:39], v[212:215], v[80:83]\n"
        "global_load_lds_dwordx4 v240, s[68:69] offset:2048\n"
        "v_mfma_f32_16x16x32_bf16 v[80:83], v[32:35], v[196:199], v[80:83]\n"
        "v_accvgpr_read_b32 v181, a241\n"
        "v_accvgpr_read_b32 v182, a242\n"
        "v_mfma_f32_16x16x32_bf16 v[112:115], v[32:35], v[200:203], v[112:115]\n"
        "v_accvgpr_read_b32 v183, a243\n"
        "v_mfma_f32_16x16x32_bf16 v[112:115], v[36:39], v[216:219], v[112:115]\n"
        "v_mfma_f32_16x16x32_bf16 v[116:119], v[44:47], v[216:219], v[116:119]\n"
        "global_load_lds_dwordx4 v241, s[68:69] offset:3072\n"
        "s_add_u32 m0, s72, 0x8000\n"
        "v_mfma_f32_16x16x32_bf16 v[116:119], v[40:43], v[200:203], v[116:119]\n"
        "s_add_u32 s68, s68, 0x80000\n"
        "s_addc_u32 s69, s69, 0\n"
        "v_mfma_f32_16x16x32_bf16 v[148:151], v[40:43], v[204:207], v[148:151]\n"
        "ds_read_b64_tr_b16 v[40:41], v232 offset:32768\n"
        "v_mfma_f32_16x16x32_bf16 v[148:151], v[44:47], v[220:223], v[148:151]\n"
        "ds_read_b64_tr_b16 v[44:45], v232 offset:40960\n"
        "v_mfma_f32_16x16x32_bf16 v[144:147], v[36:39], v[220:223], v[144:147]\n"
        "global_load_lds_dwordx4 v242, s[64:65] offset:0\n"
        "v_mfma_f32_16x16x32_bf16 v[144:147], v[32:35], v[204:207], v[144:147]\n"
        "ds_read_b64_tr_b16 v[32:33], v230 offset:32768\n"
        "v_accvgpr_read_b32 v184, a136\n"
        "v_mfma_f32_16x16x32_bf16 v[24:27], v[48:51], v[192:195], v[24:27]\n"
        "ds_read_b64_tr_b16 v[34:35], v231 offset:32768\n"
        "v_mul_f32 v184, s88, v184\n"
        "v_mfma_f32_16x16x32_bf16 v[24:27], v[52:55], v[208:211], v[24:27]\n"
        "ds_read_b64_tr_b16 v[36:37], v230 offset:40960\n"
        "v_cvt_pk_bf16_f32 v184, v184, v185\n"
        "v_mfma_f32_16x16x32_bf16 v[28:31], v[60:63], v[208:211], v[28:31]\n"
        "ds_read_b64_tr_b16 v[38:39], v231 offset:40960\n"
        "v_cvt_pk_bf16_f32 v185, v186, v187\n"
        "v_mfma_f32_16x16x32_bf16 v[28:31], v[56:59], v[192:195], v[28:31]\n"
        "ds_read_b64_tr_b16 v[46:47], v233 offset:40960\n"
        "v_cvt_pk_bf16_f32 v186, v188, v189\n"
        "v_mfma_f32_16x16x32_bf16 v[92:95], v[56:59], v[196:199], v[92:95]\n"
        "ds_read_b64_tr_b16 v[42:43], v233 offset:32768\n"
        "v_cvt_pk_bf16_f32 v187, v190, v191\n"
        "v_mfma_f32_16x16x32_bf16 v[92:95], v[60:63], v[212:215], v[92:95]\n"
        "ds_read_b128 v[192:195], v246 offset:2048\n"
        "v_accvgpr_write_b32 a12, v184\n"
        "v_mfma_f32_16x16x32_bf16 v[88:91], v[52:55], v[212:215], v[88:91]\n"
        "ds_read_b128 v[208:211], v247 offset:2048\n"
        "v_accvgpr_write_b32 a13, v185\n"
        "v_mfma_f32_16x16x32_bf16 v[88:91], v[48:51], v[196:199], v[88:91]\n"
        "global_load_lds_dwordx4 v243, s[64:65] offset:1024\n"
        "v_accvgpr_write_b32 a14, v186\n"
        "v_mfma_f32_16x16x32_bf16 v[120:123], v[48:51], v[200:203], v[120:123]\n"
        "ds_read_b128 v[196:199], v246 offset:0\n"
        "v_accvgpr_write_b32 a15, v187\n"
        "v_mfma_f32_16x16x32_bf16 v[120:123], v[52:55], v[216:219], v[120:123]\n"
        "ds_read_b128 v[212:215], v247 offset:0\n"
        "v_accvgpr_read_b32 v185, a213\n"
        "v_mfma_f32_16x16x32_bf16 v[124:127], v[60:63], v[216:219], v[124:127]\n"
        "ds_read_b128 v[216:219], v247 offset:6144\n"
        "v_accvgpr_read_b32 v186, a214\n"
        "v_mfma_f32_16x16x32_bf16 v[124:127], v[56:59], v[200:203], v[124:127]\n"
        "global_load_lds_dwordx4 v244, s[64:65] offset:2048\n"
        "ds_read_b128 v[200:203], v246 offset:6144\n"
        "v_mfma_f32_16x16x32_bf16 v[156:159], v[56:59], v[204:207], v[156:159]\n"
        "ds_read_b64_tr_b16 v[56:57], v228 offset:33792\n"
        "v_accvgpr_read_b32 v187, a215\n"
        "v_mfma_f32_16x16x32_bf16 v[156:159], v[60:63], v[220:223], v[156:159]\n"
        "ds_read_b64_tr_b16 v[60:61], v228 offset:41984\n"
        "v_accvgpr_read_b32 v188, a244\n"
        "v_mfma_f32_16x16x32_bf16 v[152:155], v[52:55], v[220:223], v[152:155]\n"
        "ds_read_b128 v[220:223], v247 offset:4096\n"
        "LHA6_%=:\n"
        "s_cbranch_execz LHA10_%=\n"
        "v_accvgpr_read_b32 v189, a245\n"
        "v_mfma_f32_16x16x32_bf16 v[152:155], v[48:51], v[204:207], v[152:155]\n"
        "global_load_lds_dwordx4 v245, s[64:65] offset:3072\n"
        "s_add_u32 m0, s72, 0x9000\n"
        "s_waitcnt lgkmcnt(7)\n"
        "v_mfma_f32_16x16x32_bf16 v[0:3], v[160:163], v[192:195], v[0:3]\n"
        "ds_read_b128 v[204:207], v246 offset:4096\n"
        "s_add_u32 s64, s64, 0x80\n"
        "s_addc_u32 s65, s65, 0\n"
        "v_mfma_f32_16x16x32_bf16 v[0:3], v[164:167], v[208:211], v[0:3]\n"
        "ds_read_b64_tr_b16 v[48:49], v226 offset:33792\n"
        "v_accvgpr_read_b32 v190, a246\n"
        "v_mfma_f32_16x16x32_bf16 v[4:7], v[172:175], v[208:211], v[4:7]\n"
        "ds_read_b64_tr_b16 v[50:51], v227 offset:33792\n"
        "v_accvgpr_read_b32 v191, a247\n"
        "v_mfma_f32_16x16x32_bf16 v[4:7], v[168:171], v[192:195], v[4:7]\n"
        "ds_read_b64_tr_b16 v[52:53], v226 offset:41984\n"
        "s_waitcnt lgkmcnt(7)\n"
        "v_mfma_f32_16x16x32_bf16 v[68:71], v[168:171], v[196:199], v[68:71]\n"
        "ds_read_b64_tr_b16 v[54:55], v227 offset:41984\n"
        "global_load_lds_dwordx4 v242, s[66:67] offset:0\n"
        "v_mfma_f32_16x16x32_bf16 v[68:71], v[172:175], v[212:215], v[68:71]\n"
        "ds_read_b64_tr_b16 v[62:63], v229 offset:41984\n"
        "v_mfma_f32_16x16x32_bf16 v[64:67], v[164:167], v[212:215], v[64:67]\n"
        "ds_read_b64_tr_b16 v[58:59], v229 offset:33792\n"
        "v_mfma_f32_16x16x32_bf16 v[64:67], v[160:163], v[196:199], v[64:67]\n"
        "v_accvgpr_read_b32 v176, a208\n"
        "v_cvt_pk_bf16_f32 v176, v176, v177\n"
        "v_mfma_f32_16x16x32_bf16 v[96:99], v[160:163], v[200:203], v[96:99]\n"
        "global_load_lds_dwordx4 v243, s[66:67] offset:1024\n"
        "v_mfma_f32_16x16x32_bf16 v[96:99], v[164:167], v[216:219], v[96:99]\n"
        "s_waitcnt lgkmcnt(5)\n"
        "v_cvt_pk_bf16_f32 v177, v178, v179\n"
        "v_cvt_pk_bf16_f32 v178, v180, v181\n"
        "v_mfma_f32_16x16x32_bf16 v[100:103], v[172:175], v[216:219], v[100:103]\n"
        "v_cvt_pk_bf16_f32 v179, v182, v183\n"
        "v_accvgpr_write_b32 a208, v176\n"
        "v_mfma_f32_16x16x32_bf16 v[100:103], v[168:171], v[200:203], v[100:103]\n"
        "v_accvgpr_write_b32 a209, v177\n"
        "v_accvgpr_write_b32 a210, v178\n"
        "v_mfma_f32_16x16x32_bf16 v[132:135], v[168:171], v[204:207], v[132:135]\n"
        "ds_read_b64_tr_b16 v[168:169], v232 offset:33792\n"
        "v_accvgpr_write_b32 a211, v179\n"
        "v_mfma_f32_16x16x32_bf16 v[132:135], v[172:175], v[220:223], v[132:135]\n"
        "global_load_lds_dwordx4 v244, s[66:67] offset:2048\n"
        "ds_read_b64_tr_b16 v[172:173], v232 offset:41984\n"
        "v_mfma_f32_16x16x32_bf16 v[128:131], v[164:167], v[220:223], v[128:131]\n"
        "ds_read_b64_tr_b16 v[164:165], v230 offset:41984\n"
        "v_accvgpr_read_b32 v177, a217\n"
        "v_mfma_f32_16x16x32_bf16 v[128:131], v[160:163], v[204:207], v[128:131]\n"
        "ds_read_b64_tr_b16 v[160:161], v230 offset:33792\n"
        "v_accvgpr_read_b32 v178, a218\n"
        "v_mfma_f32_16x16x32_bf16 v[8:11], v[32:35], v[192:195], v[8:11]\n"
        "ds_read_b64_tr_b16 v[162:163], v231 offset:33792\n"
        "v_accvgpr_read_b32 v179, a219\n"
        "v_mfma_f32_16x16x32_bf16 v[8:11], v[36:39], v[208:211], v[8:11]\n"
        "global_store_dwordx4 v254, a[196:199], s[70:71] offset:1024 sc1\n"
        "ds_read_b64_tr_b16 v[166:167], v231 offset:41984\n"
        "ds_read_b64_tr_b16 v[174:175], v233 offset:41984\n"
        "v_accvgpr_read_b32 v180, a248\n"
        "v_mfma_f32_16x16x32_bf16 v[12:15], v[44:47], v[208:211], v[12:15]\n"
        "ds_read_b64_tr_b16 v[170:171], v233 offset:33792\n"
        "v_accvgpr_read_b32 v181, a249\n"
        "v_mfma_f32_16x16x32_bf16 v[12:15], v[40:43], v[192:195], v[12:15]\n"
        "s_add_u32 s70, s38, s97\n"
        "s_addc_u32 s71, s39, 0\n"
        "v_accvgpr_read_b32 v182, a250\n"
        "v_mfma_f32_16x16x32_bf16 v[76:79], v[40:43], v[196:199], v[76:79]\n"
        "global_load_lds_dwordx4 v245, s[66:67] offset:3072\n"
        "s_add_u32 m0, s73, 0x20000\n"
        "v_mfma_f32_16x16x32_bf16 v[76:79], v[44:47], v[212:215], v[76:79]\n"
        "s_add_u32 s66, s66, 0x80\n"
        "s_addc_u32 s67, s67, 0\n"
        "v_accvgpr_read_b32 v183, a251\n"
        "v_mfma_f32_16x16x32_bf16 v[72:75], v[36:39], v[212:215], v[72:75]\n"
        "s_add_u32 s97, s52, 0x1000\n"
        "s_add_u32 s97, s97, s75\n"
        "v_mfma_f32_16x16x32_bf16 v[72:75], v[32:35], v[196:199], v[72:75]\n"
        "v_mfma_f32_16x16x32_bf16 v[104:107], v[32:35], v[200:203], v[104:107]\n"
        "v_mfma_f32_16x16x32_bf16 v[104:107], v[36:39], v[216:219], v[104:107]\n"
        "v_mfma_f32_16x16x32_bf16 v[108:111], v[44:47], v[216:219], v[108:111]\n"
        "s_waitcnt vmcnt(14) lgkmcnt(0)\n"
        "v_mfma_f32_16x16x32_bf16 v[108:111], v[40:43], v[200:203], v[108:111]\n"
        "s_barrier\n"
        "v_mfma_f32_16x16x32_bf16 v[140:143], v[40:43], v[204:207], v[140:143]\n"
        "global_load_lds_dwordx4 v238, s[68:69] offset:0\n"
        "ds_read_b64_tr_b16 v[40:41], v228 offset:49152\n"
        "v_mfma_f32_16x16x32_bf16 v[140:143], v[44:47], v[220:223], v[140:143]\n"
        "ds_read_b64_tr_b16 v[44:45], v228 offset:57344\n"
        "v_mfma_f32_16x16x32_bf16 v[136:139], v[36:39], v[220:223], v[136:139]\n"
        "ds_read_b64_tr_b16 v[36:37], v226 offset:57344\n"
        "v_mfma_f32_16x16x32_bf16 v[136:139], v[32:35], v[204:207], v[136:139]\n"
        "ds_read_b64_tr_b16 v[32:33], v226 offset:49152\n"
        "v_accvgpr_read_b32 v184, a212\n"
        "v_mfma_f32_16x16x32_bf16 v[16:19], v[48:51], v[192:195], v[16:19]\n"
        "ds_read_b64_tr_b16 v[34:35], v227 offset:49152\n"
        "v_cvt_pk_bf16_f32 v184, v184, v185\n"
        "v_mfma_f32_16x16x32_bf16 v[16:19], v[52:55], v[208:211], v[16:19]\n"
        "ds_read_b64_tr_b16 v[38:39], v227 offset:57344\n"
        "v_cvt_pk_bf16_f32 v185, v186, v187\n"
        "v_mfma_f32_16x16x32_bf16 v[20:23], v[60:63], v[208:211], v[20:23]\n"
        "ds_read_b64_tr_b16 v[46:47], v229 offset:57344\n"
        "v_cvt_pk_bf16_f32 v186, v188, v189\n"
        "v_mfma_f32_16x16x32_bf16 v[20:23], v[56:59], v[192:195], v[20:23]\n"
        "ds_read_b64_tr_b16 v[42:43], v229 offset:49152\n"
        "v_cvt_pk_bf16_f32 v187, v190, v191\n"
        "v_mfma_f32_16x16x32_bf16 v[84:87], v[56:59], v[196:199], v[84:87]\n"
        "global_load_lds_dwordx4 v239, s[68:69] offset:1024\n"
        "v_accvgpr_write_b32 a240, v184\n"
        "v_mfma_f32_16x16x32_bf16 v[84:87], v[60:63], v[212:215], v[84:87]\n"
        "v_accvgpr_write_b32 a241, v185\n"
        "v_accvgpr_write_b32 a242, v186\n"
        "v_mfma_f32_16x16x32_bf16 v[80:83], v[52:55], v[212:215], v[80:83]\n"
        "v_accvgpr_write_b32 a243, v187\n"
        "v_accvgpr_read_b32 v185, a221\n"
        "v_mfma_f32_16x16x32_bf16 v[80:83], v[48:51], v[196:199], v[80:83]\n"
        "v_accvgpr_read_b32 v186, a222\n"
        "v_accvgpr_read_b32 v187, a223\n"
        "v_mfma_f32_16x16x32_bf16 v[112:115], v[48:51], v[200:203], v[112:115]\n"
        "global_load_lds_dwordx4 v240, s[68:69] offset:2048\n"
        "v_accvgpr_read_b32 v188, a252\n"
        "v_mfma_f32_16x16x32_bf16 v[112:115], v[52:55], v[216:219], v[112:115]\n"
        "v_accvgpr_read_b32 v189, a253\n"
        "v_accvgpr_read_b32 v190, a254\n"
        "v_mfma_f32_16x16x32_bf16 v[116:119], v[60:63], v[216:219], v[116:119]\n"
        "v_accvgpr_read_b32 v191, a255\n"
        "v_mfma_f32_16x16x32_bf16 v[116:119], v[56:59], v[200:203], v[116:119]\n"
        "v_mfma_f32_16x16x32_bf16 v[148:151], v[56:59], v[204:207], v[148:151]\n"
        "global_load_lds_dwordx4 v241, s[68:69] offset:3072\n"
        "ds_read_b64_tr_b16 v[56:57], v232 offset:49152\n"
        "s_add_u32 m0, s72, 0x0\n"
        "v_mfma_f32_16x16x32_bf16 v[148:151], v[60:63], v[220:223], v[148:151]\n"
        "ds_read_b64_tr_b16 v[60:61], v232 offset:57344\n"
        "s_add_u32 s68, s68, 0x80000\n"
        "s_addc_u32 s69, s69, 0\n"
        "v_mfma_f32_16x16x32_bf16 v[144:147], v[52:55], v[220:223], v[144:147]\n"
        "ds_read_b64_tr_b16 v[52:53], v230 offset:57344\n"
        "v_mfma_f32_16x16x32_bf16 v[144:147], v[48:51], v[204:207], v[144:147]\n"
        "ds_read_b64_tr_b16 v[48:49], v230 offset:49152\n"
        "v_mfma_f32_16x16x32_bf16 v[24:27], v[160:163], v[192:195], v[24:27]\n"
        "ds_read_b64_tr_b16 v[50:51], v231 offset:49152\n"
        "global_load_lds_dwordx4 v242, s[64:65] offset:0\n"
        "v_mfma_f32_16x16x32_bf16 v[24:27], v[164:167], v[208:211], v[24:27]\n"
        "ds_read_b64_tr_b16 v[54:55], v231 offset:57344\n"
        "v_mfma_f32_16x16x32_bf16 v[28:31], v[172:175], v[208:211], v[28:31]\n"
        "ds_read_b64_tr_b16 v[62:63], v233 offset:57344\n"
        "v_mfma_f32_16x16x32_bf16 v[28:31], v[168:171], v[192:195], v[28:31]\n"
        "ds_read_b64_tr_b16 v[58:59], v233 offset:49152\n"
        "v_mfma_f32_16x16x32_bf16 v[92:95], v[168:171], v[196:199], v[92:95]\n"
        "ds_read_b128 v[192:195], v248 offset:2048\n"
        "global_load_lds_dwordx4 v243, s[64:65] offset:1024\n"
        "v_mfma_f32_16x16x32_bf16 v[92:95], v[172:175], v[212:215], v[92:95]\n"
        "ds_read_b128 v[208:211], v249 offset:2048\n"
        "v_mfma_f32_16x16x32_bf16 v[88:91], v[164:167], v[212:215], v[88:91]\n"
        "ds_read_b128 v[212:215], v249 offset:0\n"
        "v_mfma_f32_16x16x32_bf16 v[88:91], v[160:163], v[196:199], v[88:91]\n"
        "ds_read_b128 v[196:199], v248 offset:0\n"
        "v_accvgpr_read_b32 v176, a216\n"
        "v_mfma_f32_16x16x32_bf16 v[120:123], v[160:163], v[200:203], v[120:123]\n"
        "v_cvt_pk_bf16_f32 v176, v176, v177\n"
        "v_cvt_pk_bf16_f32 v177, v178, v179\n"
        "v_mfma_f32_16x16x32_bf16 v[120:123], v[164:167], v[216:219], v[120:123]\n"
        "global_load_lds_dwordx4 v244, s[64:65] offset:2048\n"
        "v_cvt_pk_bf16_f32 v178, v180, v181\n"
        "v_mfma_f32_16x16x32_bf16 v[124:127], v[172:175], v[216:219], v[124:127]\n"
        "ds_read_b128 v[216:219], v249 offset:6144\n"
        "v_cvt_pk_bf16_f32 v179, v182, v183\n"
        "v_mfma_f32_16x16x32_bf16 v[124:127], v[168:171], v[200:203], v[124:127]\n"
        "ds_read_b128 v[200:203], v248 offset:6144\n"
        "v_accvgpr_write_b32 a212, v176\n"
        "v_mfma_f32_16x16x32_bf16 v[156:159], v[168:171], v[204:207], v[156:159]\n"
        "s_waitcnt lgkmcnt(5)\n"
        "v_accvgpr_write_b32 a213, v177\n"
        "v_accvgpr_write_b32 a214, v178\n"
        "v_mfma_f32_16x16x32_bf16 v[156:159], v[172:175], v[220:223], v[156:159]\n"
        "s_waitcnt lgkmcnt(4)\n"
        "v_accvgpr_write_b32 a215, v179\n"
        "v_accvgpr_read_b32 v177, a145\n"
        "v_mfma_f32_16x16x32_bf16 v[152:155], v[164:167], v[220:223], v[152:155]\n"
        "global_load_lds_dwordx4 v245, s[64:65] offset:3072\n"
        "ds_read_b128 v[220:223], v249 offset:4096\n"
        "s_add_u32 m0, s72, 0x1000\n"
        "v_mfma_f32_16x16x32_bf16 v[152:155], v[160:163], v[204:207], v[152:155]\n"
        "ds_read_b128 v[204:207], v248 offset:4096\n"
        "s_add_u32 s64, s64, 0x80\n"
        "s_addc_u32 s65, s65, 0\n"
        "v_mfma_f32_16x16x32_bf16 v[0:3], v[32:35], v[192:195], v[0:3]\n"
        "ds_read_b64_tr_b16 v[160:161], v226 offset:50176\n"
        "v_accvgpr_read_b32 v178, a146\n"
        "v_mfma_f32_16x16x32_bf16 v[0:3], v[36:39], v[208:211], v[0:3]\n"
        "ds_read_b64_tr_b16 v[162:163], v227 offset:50176\n"
        "v_accvgpr_read_b32 v179, a147\n"
        "v_mfma_f32_16x16x32_bf16 v[4:7], v[44:47], v[208:211], v[4:7]\n"
        "ds_read_b64_tr_b16 v[164:165], v226 offset:58368\n"
        "global_load_lds_dwordx4 v242, s[66:67] offset:0\n"
        "v_mfma_f32_16x16x32_bf16 v[4:7], v[40:43], v[192:195], v[4:7]\n"
        "ds_read_b64_tr_b16 v[166:167], v227 offset:58368\n"
        "s_waitcnt lgkmcnt(6)\n"
        "v_mfma_f32_16x16x32_bf16 v[68:71], v[40:43], v[196:199], v[68:71]\n"
        "ds_read_b64_tr_b16 v[172:173], v228 offset:58368\n"
        "v_accvgpr_read_b32 v180, a176\n"
        "v_mfma_f32_16x16x32_bf16 v[68:71], v[44:47], v[212:215], v[68:71]\n"
        "ds_read_b64_tr_b16 v[174:175], v229 offset:58368\n"
        "v_accvgpr_read_b32 v181, a177\n"
        "v_mfma_f32_16x16x32_bf16 v[64:67], v[36:39], v[212:215], v[64:67]\n"
        "ds_read_b64_tr_b16 v[168:169], v228 offset:50176\n"
        "global_load_lds_dwordx4 v243, s[66:67] offset:1024\n"
        "v_mfma_f32_16x16x32_bf16 v[64:67], v[32:35], v[196:199], v[64:67]\n"
        "ds_read_b64_tr_b16 v[170:171], v229 offset:50176\n"
        "v_accvgpr_read_b32 v182, a178\n"
        "v_mfma_f32_16x16x32_bf16 v[96:99], v[32:35], v[200:203], v[96:99]\n"
        "s_waitcnt lgkmcnt(7)\n"
        "v_accvgpr_read_b32 v183, a179\n"
        "v_mfma_f32_16x16x32_bf16 v[96:99], v[36:39], v[216:219], v[96:99]\n"
        "v_mfma_f32_16x16x32_bf16 v[100:103], v[44:47], v[216:219], v[100:103]\n"
        "global_load_lds_dwordx4 v244, s[66:67] offset:2048\n"
        "v_mfma_f32_16x16x32_bf16 v[100:103], v[40:43], v[200:203], v[100:103]\n"
        "v_mfma_f32_16x16x32_bf16 v[132:135], v[40:43], v[204:207], v[132:135]\n"
        "ds_read_b64_tr_b16 v[40:41], v232 offset:50176\n"
        "v_mfma_f32_16x16x32_bf16 v[132:135], v[44:47], v[220:223], v[132:135]\n"
        "ds_read_b64_tr_b16 v[44:45], v232 offset:58368\n"
        "v_mfma_f32_16x16x32_bf16 v[128:131], v[36:39], v[220:223], v[128:131]\n"
        "global_load_lds_dwordx4 v245, s[66:67] offset:3072\n"
        "s_add_u32 m0, s73, 0x24000\n"
        "v_mfma_f32_16x16x32_bf16 v[128:131], v[32:35], v[204:207], v[128:131]\n"
        "ds_read_b64_tr_b16 v[32:33], v230 offset:50176\n"
        "v_accvgpr_read_b32 v184, a220\n"
        "v_mfma_f32_16x16x32_bf16 v[8:11], v[48:51], v[192:195], v[8:11]\n"
        "ds_read_b64_tr_b16 v[34:35], v231 offset:50176\n"
        "v_cvt_pk_bf16_f32 v184, v184, v185\n"
        "v_mfma_f32_16x16x32_bf16 v[8:11], v[52:55], v[208:211], v[8:11]\n"
        "ds_read_b64_tr_b16 v[36:37], v230 offset:58368\n"
        "v_cvt_pk_bf16_f32 v185, v186, v187\n"
        "v_mfma_f32_16x16x32_bf16 v[12:15], v[60:63], v[208:211], v[12:15]\n"
        "ds_read_b64_tr_b16 v[38:39], v231 offset:58368\n"
        "v_cvt_pk_bf16_f32 v186, v188, v189\n"
        "v_mfma_f32_16x16x32_bf16 v[12:15], v[56:59], v[192:195], v[12:15]\n"
        "ds_read_b64_tr_b16 v[46:47], v233 offset:58368\n"
        "v_cvt_pk_bf16_f32 v187, v190, v191\n"
        "v_mfma_f32_16x16x32_bf16 v[76:79], v[56:59], v[196:199], v[76:79]\n"
        "ds_read_b64_tr_b16 v[42:43], v233 offset:50176\n"
        "v_accvgpr_write_b32 a244, v184\n"
        "v_mfma_f32_16x16x32_bf16 v[76:79], v[60:63], v[212:215], v[76:79]\n"
        "s_add_u32 s66, s66, 0x80\n"
        "s_addc_u32 s67, s67, 0\n"
        "v_accvgpr_write_b32 a245, v185\n"
        "v_mfma_f32_16x16x32_bf16 v[72:75], v[52:55], v[212:215], v[72:75]\n"
        "v_accvgpr_write_b32 a246, v186\n"
        "v_accvgpr_write_b32 a247, v187\n"
        "v_mfma_f32_16x16x32_bf16 v[72:75], v[48:51], v[196:199], v[72:75]\n"
        "global_store_dwordx4 v254, a[200:203], s[70:71] offset:0 sc1\n"
        "v_accvgpr_read_b32 v185, a149\n"
        "v_accvgpr_read_b32 v186, a150\n"
        "v_accvgpr_read_b32 v187, a151\n"
        "v_accvgpr_read_b32 v188, a180\n"
        "v_mfma_f32_16x16x32_bf16 v[104:107], v[48:51], v[200:203], v[104:107]\n"
        "v_accvgpr_read_b32 v189, a181\n"
        "v_accvgpr_read_b32 v190, a182\n"
        "v_mfma_f32_16x16x32_bf16 v[104:107], v[52:55], v[216:219], v[104:107]\n"
        "v_accvgpr_read_b32 v191, a183\n"
        "v_mfma_f32_16x16x32_bf16 v[108:111], v[60:63], v[216:219], v[108:111]\n"
        "v_mfma_f32_16x16x32_bf16 v[108:111], v[56:59], v[200:203], v[108:111]\n"
        "s_waitcnt vmcnt(13) lgkmcnt(0)\n"
        "v_mfma_f32_16x16x32_bf16 v[140:143], v[56:59], v[204:207], v[140:143]\n"
        "s_barrier\n"
        "v_mfma_f32_16x16x32_bf16 v[140:143], v[60:63], v[220:223], v[140:143]\n"
        "global_load_lds_dwordx4 v238, s[68:69] offset:0\n"
        "ds_read_b64_tr_b16 v[60:61], v228 offset:24576\n"
        "v_mfma_f32_16x16x32_bf16 v[136:139], v[52:55], v[220:223], v[136:139]\n"
        "ds_read_b64_tr_b16 v[52:53], v226 offset:24576\n"
        "v_mfma_f32_16x16x32_bf16 v[136:139], v[48:51], v[204:207], v[136:139]\n"
        "ds_read_b64_tr_b16 v[48:49], v226 offset:16384\n"
        "v_mfma_f32_16x16x32_bf16 v[16:19], v[160:163], v[192:195], v[16:19]\n"
        "ds_read_b64_tr_b16 v[50:51], v227 offset:16384\n"
        "v_mfma_f32_16x16x32_bf16 v[16:19], v[164:167], v[208:211], v[16:19]\n"
        "ds_read_b64_tr_b16 v[54:55], v227 offset:24576\n"
        "global_load_lds_dwordx4 v239, s[68:69] offset:1024\n"
        "v_mfma_f32_16x16x32_bf16 v[20:23], v[172:175], v[208:211], v[20:23]\n"
        "ds_read_b64_tr_b16 v[62:63], v229 offset:24576\n"
        "v_mfma_f32_16x16x32_bf16 v[20:23], v[168:171], v[192:195], v[20:23]\n"
        "ds_read_b64_tr_b16 v[56:57], v228 offset:16384\n"
        "v_mfma_f32_16x16x32_bf16 v[84:87], v[168:171], v[196:199], v[84:87]\n"
        "ds_read_b64_tr_b16 v[58:59], v229 offset:16384\n"
        "v_mfma_f32_16x16x32_bf16 v[84:87], v[172:175], v[212:215], v[84:87]\n"
        "global_load_lds_dwordx4 v240, s[68:69] offset:2048\n"
        "v_mfma_f32_16x16x32_bf16 v[80:83], v[164:167], v[212:215], v[80:83]\n"
        "v_mfma_f32_16x16x32_bf16 v[80:83], v[160:163], v[196:199], v[80:83]\n"
        "v_accvgpr_read_b32 v176, a144\n"
        "v_cvt_pk_bf16_f32 v176, v176, v177\n"
        "v_mfma_f32_16x16x32_bf16 v[112:115], v[160:163], v[200:203], v[112:115]\n"
        "v_cvt_pk_bf16_f32 v177, v178, v179\n"
        "v_cvt_pk_bf16_f32 v178, v180, v181\n"
        "v_mfma_f32_16x16x32_bf16 v[112:115], v[164:167], v[216:219], v[112:115]\n"
        "v_cvt_pk_bf16_f32 v179, v182, v183\n"
        "v_accvgpr_write_b32 a216, v176\n"
        "v_mfma_f32_16x16x32_bf16 v[116:119], v[172:175], v[216:219], v[116:119]\n"
        "global_load_lds_dwordx4 v241, s[68:69] offset:3072\n"
        "s_add_u32 m0, s72, 0x10000\n"
        "v_accvgpr_write_b32 a217, v177\n"
        "v_mfma_f32_16x16x32_bf16 v[116:119], v[168:171], v[200:203], v[116:119]\n"
        "s_add_u32 s68, s68, 0x80000\n"
        "s_addc_u32 s69, s69, 0\n"
        "v_accvgpr_write_b32 a218, v178\n"
        "v_mfma_f32_16x16x32_bf16 v[148:151], v[168:171], v[204:207], v[148:151]\n"
        "ds_read_b64_tr_b16 v[168:169], v232 offset:16384\n"
        "v_accvgpr_write_b32 a219, v179\n"
        "v_mfma_f32_16x16x32_bf16 v[148:151], v[172:175], v[220:223], v[148:151]\n"
        "ds_read_b64_tr_b16 v[172:173], v232 offset:24576\n"
        "v_accvgpr_read_b32 v177, a153\n"
        "v_mfma_f32_16x16x32_bf16 v[144:147], v[164:167], v[220:223], v[144:147]\n"
        "ds_read_b64_tr_b16 v[164:165], v230 offset:24576\n"
        "global_load_lds_dwordx4 v242, s[64:65] offset:0\n"
        "v_mfma_f32_16x16x32_bf16 v[144:147], v[160:163], v[204:207], v[144:147]\n"
        "ds_read_b64_tr_b16 v[160:161], v230 offset:16384\n"
        "v_accvgpr_read_b32 v178, a154\n"
        "v_mfma_f32_16x16x32_bf16 v[24:27], v[32:35], v[192:195], v[24:27]\n"
        "ds_read_b64_tr_b16 v[162:163], v231 offset:16384\n"
        "v_accvgpr_read_b32 v179, a155\n"
        "v_mfma_f32_16x16x32_bf16 v[24:27], v[36:39], v[208:211], v[24:27]\n"
        "ds_read_b64_tr_b16 v[166:167], v231 offset:24576\n"
        "v_accvgpr_read_b32 v180, a184\n"
        "v_mfma_f32_16x16x32_bf16 v[28:31], v[44:47], v[208:211], v[28:31]\n"
        "ds_read_b64_tr_b16 v[174:175], v233 offset:24576\n"
        "global_load_lds_dwordx4 v243, s[64:65] offset:1024\n"
        "v_mfma_f32_16x16x32_bf16 v[28:31], v[40:43], v[192:195], v[28:31]\n"
        "ds_read_b64_tr_b16 v[170:171], v233 offset:16384\n"
        "v_accvgpr_read_b32 v181, a185\n"
        "v_mfma_f32_16x16x32_bf16 v[92:95], v[40:43], v[196:199], v[92:95]\n"
        "ds_read_b128 v[192:195], v246 offset:34816\n"
        "v_accvgpr_read_b32 v182, a186\n"
        "v_mfma_f32_16x16x32_bf16 v[92:95], v[44:47], v[212:215], v[92:95]\n"
        "ds_read_b128 v[208:211], v247 offset:34816\n"
        "v_accvgpr_read_b32 v183, a187\n"
        "v_mfma_f32_16x16x32_bf16 v[88:91], v[36:39], v[212:215], v[88:91]\n"
        "ds_read_b128 v[212:215], v247 offset:32768\n"
        "global_load_lds_dwordx4 v244, s[64:65] offset:2048\n"
        "v_mfma_f32_16x16x32_bf16 v[88:91], v[32:35], v[196:199], v[88:91]\n"
        "ds_read_b128 v[196:199], v246 offset:32768\n"
        "v_mfma_f32_16x16x32_bf16 v[120:123], v[32:35], v[200:203], v[120:123]\n"
        "v_mfma_f32_16x16x32_bf16 v[120:123], v[36:39], v[216:219], v[120:123]\n"
        "v_mfma_f32_16x16x32_bf16 v[124:127], v[44:47], v[216:219], v[124:127]\n"
        "ds_read_b128 v[216:219], v247 offset:38912\n"
        "global_load_lds_dwordx4 v245, s[64:65] offset:3072\n"
        "s_add_u32 m0, s72, 0x11000\n"
        "v_mfma_f32_16x16x32_bf16 v[124:127], v[40:43], v[200:203], v[124:127]\n"
        "ds_read_b128 v[200:203], v246 offset:38912\n"
        "s_add_u32 s64, s64, 0x80\n"
        "s_addc_u32 s65, s65, 0\n"
        "v_mfma_f32_16x16x32_bf16 v[156:159], v[40:43], v[204:207], v[156:159]\n"
        "ds_read_b64_tr_b16 v[40:41], v228 offset:17408\n"
        "s_waitcnt lgkmcnt(6)\n"
        "v_mfma_f32_16x16x32_bf16 v[156:159], v[44:47], v[220:223], v[156:159]\n"
        "ds_read_b64_tr_b16 v[44:45], v228 offset:25600\n"
        "s_waitcnt lgkmcnt(6)\n"
        "v_mfma_f32_16x16x32_bf16 v[152:155], v[36:39], v[220:223], v[152:155]\n"
        "ds_read_b128 v[220:223], v247 offset:36864\n"
        "global_load_lds_dwordx4 v242, s[66:67] offset:0\n"
        "v_mfma_f32_16x16x32_bf16 v[152:155], v[32:35], v[204:207], v[152:155]\n"
        "ds_read_b128 v[204:207], v246 offset:36864\n"
        "v_accvgpr_read_b32 v184, a148\n"
        "v_mfma_f32_16x16x32_bf16 v[0:3], v[48:51], v[192:195], v[0:3]\n"
        "s_waitcnt lgkmcnt(6)\n"
        "v_cvt_pk_bf16_f32 v184, v184, v185\n"
        "v_cvt_pk_bf16_f32 v185, v186, v187\n"
        "v_mfma_f32_16x16x32_bf16 v[0:3], v[52:55], v[208:211], v[0:3]\n"
        "ds_read_b64_tr_b16 v[32:33], v226 offset:17408\n"
        "v_cvt_pk_bf16_f32 v186, v188, v189\n"
        "v_mfma_f32_16x16x32_bf16 v[4:7], v[60:63], v[208:211], v[4:7]\n"
        "ds_read_b64_tr_b16 v[34:35], v227 offset:17408\n"
        "v_cvt_pk_bf16_f32 v187, v190, v191\n"
        "v_mfma_f32_16x16x32_bf16 v[4:7], v[56:59], v[192:195], v[4:7]\n"
        "ds_read_b64_tr_b16 v[36:37], v226 offset:25600\n"
        "v_accvgpr_write_b32 a248, v184\n"
        "v_mfma_f32_16x16x32_bf16 v[68:71], v[56:59], v[196:199], v[68:71]\n"
        "s_waitcnt lgkmcnt(6)\n"
        "v_accvgpr_write_b32 a249, v185\n"
        "v_accvgpr_write_b32 a250, v186\n"
        "v_mfma_f32_16x16x32_bf16 v[68:71], v[60:63], v[212:215], v[68:71]\n"
        "ds_read_b64_tr_b16 v[38:39], v227 offset:25600\n"
        "v_accvgpr_write_b32 a251, v187\n"
        "v_mfma_f32_16x16x32_bf16 v[64:67], v[52:55], v[212:215], v[64:67]\n"
        "ds_read_b64_tr_b16 v[46:47], v229 offset:25600\n"
        "global_load_lds_dwordx4 v243, s[66:67] offset:1024\n"
        "v_mfma_f32_16x16x32_bf16 v[64:67], v[48:51], v[196:199], v[64:67]\n"
        "ds_read_b64_tr_b16 v[42:43], v229 offset:17408\n"
        "v_accvgpr_read_b32 v185, a157\n"
        "v_mfma_f32_16x16x32_bf16 v[96:99], v[48:51], v[200:203], v[96:99]\n"
        "s_waitcnt lgkmcnt(6)\n"
        "v_accvgpr_read_b32 v186, a158\n"
        "v_accvgpr_read_b32 v187, a159\n"
        "v_mfma_f32_16x16x32_bf16 v[96:99], v[52:55], v[216:219], v[96:99]\n"
        "v_accvgpr_read_b32 v188, a188\n"
        "v_accvgpr_read_b32 v189, a189\n"
        "v_mfma_f32_16x16x32_bf16 v[100:103], v[60:63], v[216:219], v[100:103]\n"
        "global_load_lds_dwordx4 v244, s[66:67] offset:2048\n"
        "v_mfma_f32_16x16x32_bf16 v[100:103], v[56:59], v[200:203], v[100:103]\n"
        "v_accvgpr_read_b32 v190, a190\n"
        "v_accvgpr_read_b32 v191, a191\n"
        "v_mfma_f32_16x16x32_bf16 v[132:135], v[56:59], v[204:207], v[132:135]\n"
        "ds_read_b64_tr_b16 v[56:57], v232 offset:17408\n"
        "v_mfma_f32_16x16x32_bf16 v[132:135], v[60:63], v[220:223], v[132:135]\n"
        "ds_read_b64_tr_b16 v[60:61], v232 offset:25600\n"
        "v_mfma_f32_16x16x32_bf16 v[128:131], v[52:55], v[220:223], v[128:131]\n"
        "global_load_lds_dwordx4 v245, s[66:67] offset:3072\n"
        "s_add_u32 m0, s73, 0x1c000\n"
        "v_mfma_f32_16x16x32_bf16 v[128:131], v[48:51], v[204:207], v[128:131]\n"
        "ds_read_b64_tr_b16 v[48:49], v230 offset:17408\n"
        "s_add_u32 s66, s66, 0x80\n"
        "s_addc_u32 s67, s67, 0\n"
        "v_mfma_f32_16x16x32_bf16 v[8:11], v[160:163], v[192:195], v[8:11]\n"
        "ds_read_b64_tr_b16 v[50:51], v231 offset:17408\n"
        "v_mfma_f32_16x16x32_bf16 v[8:11], v[164:167], v[208:211], v[8:11]\n"
        "ds_read_b64_tr_b16 v[52:53], v230 offset:25600\n"
        "v_mfma_f32_16x16x32_bf16 v[12:15], v[172:175], v[208:211], v[12:15]\n"
        "ds_read_b64_tr_b16 v[54:55], v231 offset:25600\n"
        "v_mfma_f32_16x16x32_bf16 v[12:15], v[168:171], v[192:195], v[12:15]\n"
        "ds_read_b64_tr_b16 v[62:63], v233 offset:25600\n"
        "v_mfma_f32_16x16x32_bf16 v[76:79], v[168:171], v[196:199], v[76:79]\n"
        "ds_read_b64_tr_b16 v[58:59], v233 offset:17408\n"
        "v_mfma_f32_16x16x32_bf16 v[76:79], v[172:175], v[212:215], v[76:79]\n"
        "global_store_dwordx4 v254, a[204:207], s[70:71] offset:1024 sc1\n"
        "s_add_u32 s70, s38, s97\n"
        "s_addc_u32 s71, s39, 0\n"
        "v_mfma_f32_16x16x32_bf16 v[72:75], v[164:167], v[212:215], v[72:75]\n"
        "v_mfma_f32_16x16x32_bf16 v[72:75], v[160:163], v[196:199], v[72:75]\n"
        "v_accvgpr_read_b32 v176, a152\n"
        "v_cvt_pk_bf16_f32 v176, v176, v177\n"
        "v_mfma_f32_16x16x32_bf16 v[104:107], v[160:163], v[200:203], v[104:107]\n"
        "v_cvt_pk_bf16_f32 v177, v178, v179\n"
        "v_cvt_pk_bf16_f32 v178, v180, v181\n"
        "v_mfma_f32_16x16x32_bf16 v[104:107], v[164:167], v[216:219], v[104:107]\n"
        "v_cvt_pk_bf16_f32 v179, v182, v183\n"
        "v_accvgpr_write_b32 a220, v176\n"
        "v_mfma_f32_16x16x32_bf16 v[108:111], v[172:175], v[216:219], v[108:111]\n"
        "v_accvgpr_write_b32 a221, v177\n"
        "v_accvgpr_write_b32 a222, v178\n"
        "v_mfma_f32_16x16x32_bf16 v[108:111], v[168:171], v[200:203], v[108:111]\n"
        "s_waitcnt vmcnt(14) lgkmcnt(0)\n"
        "v_accvgpr_write_b32 a223, v179\n"
        "v_mfma_f32_16x16x32_bf16 v[140:143], v[168:171], v[204:207], v[140:143]\n"
        "s_barrier\n"
        "v_mfma_f32_16x16x32_bf16 v[140:143], v[172:175], v[220:223], v[140:143]\n"
        "global_load_lds_dwordx4 v238, s[68:69] offset:0\n"
        "ds_read_b64_tr_b16 v[172:173], v228 offset:40960\n"
        "v_mfma_f32_16x16x32_bf16 v[136:139], v[164:167], v[220:223], v[136:139]\n"
        "ds_read_b64_tr_b16 v[164:165], v226 offset:40960\n"
        "v_accvgpr_read_b32 v177, a17\n"
        "v_mfma_f32_16x16x32_bf16 v[136:139], v[160:163], v[204:207], v[136:139]\n"
        "ds_read_b64_tr_b16 v[160:161], v226 offset:32768\n"
        "s_add_u32 s97, s52, 0x2000\n"
        "v_accvgpr_read_b32 v178, a18\n"
        "v_mfma_f32_16x16x32_bf16 v[16:19], v[32:35], v[192:195], v[16:19]\n"
        "ds_read_b64_tr_b16 v[162:163], v227 offset:32768\n"
        "s_add_u32 s97, s97, s75\n"
        "v_accvgpr_read_b32 v179, a19\n"
        "v_mfma_f32_16x16x32_bf16 v[16:19], v[36:39], v[208:211], v[16:19]\n"
        "ds_read_b64_tr_b16 v[166:167], v227 offset:40960\n"
        "global_load_lds_dwordx4 v239, s[68:69] offset:1024\n"
        "v_mfma_f32_16x16x32_bf16 v[20:23], v[44:47], v[208:211], v[20:23]\n"
        "ds_read_b64_tr_b16 v[174:175], v229 offset:40960\n"
        "v_accvgpr_read_b32 v180, a48\n"
        "v_mfma_f32_16x16x32_bf16 v[20:23], v[40:43], v[192:195], v[20:23]\n"
        "ds_read_b64_tr_b16 v[168:169], v228 offset:32768\n"
        "v_accvgpr_read_b32 v181, a49\n"
        "v_mfma_f32_16x16x32_bf16 v[84:87], v[40:43], v[196:199], v[84:87]\n"
        "ds_read_b64_tr_b16 v[170:171], v229 offset:32768\n"
        "v_accvgpr_read_b32 v182, a50\n"
        "v_mfma_f32_16x16x32_bf16 v[84:87], v[44:47], v[212:215], v[84:87]\n"
        "global_load_lds_dwordx4 v240, s[68:69] offset:2048\n"
        "v_mfma_f32_16x16x32_bf16 v[80:83], v[36:39], v[212:215], v[80:83]\n"
        "v_accvgpr_read_b32 v183, a51\n"
        "v_accvgpr_write_b32 a48, 0\n"
        "v_mfma_f32_16x16x32_bf16 v[80:83], v[32:35], v[196:199], v[80:83]\n"
        "v_accvgpr_write_b32 a49, 0\n"
        "v_accvgpr_write_b32 a50, 0\n"
        "v_mfma_f32_16x16x32_bf16 v[112:115], v[32:35], v[200:203], v[112:115]\n"
        "v_accvgpr_write_b32 a51, 0\n"
        "v_mfma_f32_16x16x32_bf16 v[112:115], v[36:39], v[216:219], v[112:115]\n"
        "global_load_lds_dwordx4 v241, s[68:69] offset:3072\n"
        "s_add_u32 m0, s72, 0x8000\n"
        "v_mfma_f32_16x16x32_bf16 v[116:119], v[44:47], v[216:219], v[116:119]\n"
        "s_add_u32 s68, s68, 0x80000\n"
        "s_addc_u32 s69, s69, 0\n"
        "v_mfma_f32_16x16x32_bf16 v[116:119], v[40:43], v[200:203], v[116:119]\n"
        "v_mfma_f32_16x16x32_bf16 v[148:151], v[40:43], v[204:207], v[148:151]\n"
        "ds_read_b64_tr_b16 v[40:41], v232 offset:32768\n"
        "v_mfma_f32_16x16x32_bf16 v[148:151], v[44:47], v[220:223], v[148:151]\n"
        "global_load_lds_dwordx4 v242, s[64:65] offset:0\n"
        "v_mfma_f32_16x16x32_bf16 v[144:147], v[36:39], v[220:223], v[144:147]\n"
        "ds_read_b64_tr_b16 v[36:37], v230 offset:40960\n"
        "v_mfma_f32_16x16x32_bf16 v[144:147], v[32:35], v[204:207], v[144:147]\n"
        "ds_read_b64_tr_b16 v[32:33], v230 offset:32768\n"
        "v_accvgpr_read_b32 v184, a156\n"
        "v_mfma_f32_16x16x32_bf16 v[24:27], v[48:51], v[192:195], v[24:27]\n"
        "ds_read_b64_tr_b16 v[34:35], v231 offset:32768\n"
        "v_cvt_pk_bf16_f32 v184, v184, v185\n"
        "v_mfma_f32_16x16x32_bf16 v[24:27], v[52:55], v[208:211], v[24:27]\n"
        "ds_read_b64_tr_b16 v[38:39], v231 offset:40960\n"
        "v_cvt_pk_bf16_f32 v185, v186, v187\n"
        "v_mfma_f32_16x16x32_bf16 v[28:31], v[60:63], v[208:211], v[28:31]\n"
        "ds_read_b64_tr_b16 v[44:45], v232 offset:40960\n"
        "v_cvt_pk_bf16_f32 v186, v188, v189\n"
        "v_mfma_f32_16x16x32_bf16 v[28:31], v[56:59], v[192:195], v[28:31]\n"
        "ds_read_b64_tr_b16 v[46:47], v233 offset:40960\n"
        "v_cvt_pk_bf16_f32 v187, v190, v191\n"
        "v_mfma_f32_16x16x32_bf16 v[92:95], v[56:59], v[196:199], v[92:95]\n"
        "ds_read_b64_tr_b16 v[42:43], v233 offset:32768\n"
        "v_accvgpr_write_b32 a252, v184\n"
        "v_mfma_f32_16x16x32_bf16 v[92:95], v[60:63], v[212:215], v[92:95]\n"
        "ds_read_b128 v[192:195], v246 offset:2048\n"
        "v_accvgpr_write_b32 a253, v185\n"
        "v_mfma_f32_16x16x32_bf16 v[88:91], v[52:55], v[212:215], v[88:91]\n"
        "ds_read_b128 v[208:211], v247 offset:2048\n"
        "v_accvgpr_write_b32 a254, v186\n"
        "v_mfma_f32_16x16x32_bf16 v[88:91], v[48:51], v[196:199], v[88:91]\n"
        "global_load_lds_dwordx4 v243, s[64:65] offset:1024\n"
        "v_accvgpr_write_b32 a255, v187\n"
        "v_mfma_f32_16x16x32_bf16 v[120:123], v[48:51], v[200:203], v[120:123]\n"
        "ds_read_b128 v[196:199], v246 offset:0\n"
        "v_accvgpr_read_b32 v185, a21\n"
        "v_mfma_f32_16x16x32_bf16 v[120:123], v[52:55], v[216:219], v[120:123]\n"
        "ds_read_b128 v[212:215], v247 offset:0\n"
        "v_accvgpr_read_b32 v186, a22\n"
        "v_mfma_f32_16x16x32_bf16 v[124:127], v[60:63], v[216:219], v[124:127]\n"
        "ds_read_b128 v[216:219], v247 offset:6144\n"
        "v_accvgpr_read_b32 v187, a23\n"
        "v_mfma_f32_16x16x32_bf16 v[124:127], v[56:59], v[200:203], v[124:127]\n"
        "global_load_lds_dwordx4 v244, s[64:65] offset:2048\n"
        "LHA7_%=:\n"
        "s_cbranch_execz LHA11_%=\n"
        "ds_read_b128 v[200:203], v246 offset:6144\n"
        "v_mfma_f32_16x16x32_bf16 v[156:159], v[56:59], v[204:207], v[156:159]\n"
        "ds_read_b64_tr_b16 v[56:57], v228 offset:33792\n"
        "v_accvgpr_read_b32 v188, a52\n"
        "v_mfma_f32_16x16x32_bf16 v[156:159], v[60:63], v[220:223], v[156:159]\n"
        "ds_read_b64_tr_b16 v[60:61], v228 offset:41984\n"
        "v_accvgpr_read_b32 v189, a53\n"
        "v_mfma_f32_16x16x32_bf16 v[152:155], v[52:55], v[220:223], v[152:155]\n"
        "ds_read_b128 v[220:223], v247 offset:4096\n"
        "v_accvgpr_read_b32 v190, a54\n"
        "v_mfma_f32_16x16x32_bf16 v[152:155], v[48:51], v[204:207], v[152:155]\n"
        "global_load_lds_dwordx4 v245, s[64:65] offset:3072\n"
        "s_add_u32 m0, s72, 0x9000\n"
        "s_waitcnt lgkmcnt(7)\n"
        "v_mfma_f32_16x16x32_bf16 v[0:3], v[160:163], v[192:195], v[0:3]\n"
        "ds_read_b128 v[204:207], v246 offset:4096\n"
        "s_add_u32 s64, s64, 0x80\n"
        "s_addc_u32 s65, s65, 0\n"
        "v_mfma_f32_16x16x32_bf16 v[0:3], v[164:167], v[208:211], v[0:3]\n"
        "ds_read_b64_tr_b16 v[48:49], v226 offset:33792\n"
        "v_accvgpr_read_b32 v191, a55\n"
        "v_mfma_f32_16x16x32_bf16 v[4:7], v[172:175], v[208:211], v[4:7]\n"
        "ds_read_b64_tr_b16 v[50:51], v227 offset:33792\n"
        "v_accvgpr_write_b32 a52, 0\n"
        "v_mfma_f32_16x16x32_bf16 v[4:7], v[168:171], v[192:195], v[4:7]\n"
        "ds_read_b64_tr_b16 v[52:53], v226 offset:41984\n"
        "s_waitcnt lgkmcnt(7)\n"
        "v_mfma_f32_16x16x32_bf16 v[68:71], v[168:171], v[196:199], v[68:71]\n"
        "ds_read_b64_tr_b16 v[54:55], v227 offset:41984\n"
        "global_load_lds_dwordx4 v242, s[66:67] offset:0\n"
        "v_mfma_f32_16x16x32_bf16 v[68:71], v[172:175], v[212:215], v[68:71]\n"
        "ds_read_b64_tr_b16 v[62:63], v229 offset:41984\n"
        "v_accvgpr_write_b32 a53, 0\n"
        "v_mfma_f32_16x16x32_bf16 v[64:67], v[164:167], v[212:215], v[64:67]\n"
        "ds_read_b64_tr_b16 v[58:59], v229 offset:33792\n"
        "v_accvgpr_write_b32 a54, 0\n"
        "v_mfma_f32_16x16x32_bf16 v[64:67], v[160:163], v[196:199], v[64:67]\n"
        "v_accvgpr_read_b32 v176, a16\n"
        "v_cvt_pk_bf16_f32 v176, v176, v177\n"
        "v_mfma_f32_16x16x32_bf16 v[96:99], v[160:163], v[200:203], v[96:99]\n"
        "global_load_lds_dwordx4 v243, s[66:67] offset:1024\n"
        "v_mfma_f32_16x16x32_bf16 v[96:99], v[164:167], v[216:219], v[96:99]\n"
        "s_waitcnt lgkmcnt(5)\n"
        "v_cvt_pk_bf16_f32 v177, v178, v179\n"
        "v_cvt_pk_bf16_f32 v178, v180, v181\n"
        "v_mfma_f32_16x16x32_bf16 v[100:103], v[172:175], v[216:219], v[100:103]\n"
        "v_cvt_pk_bf16_f32 v179, v182, v183\n"
        "v_accvgpr_write_b32 a176, v176\n"
        "v_mfma_f32_16x16x32_bf16 v[100:103], v[168:171], v[200:203], v[100:103]\n"
        "v_accvgpr_write_b32 a177, v177\n"
        "v_accvgpr_write_b32 a178, v178\n"
        "v_mfma_f32_16x16x32_bf16 v[132:135], v[168:171], v[204:207], v[132:135]\n"
        "ds_read_b64_tr_b16 v[168:169], v232 offset:33792\n"
        "v_accvgpr_write_b32 a179, v179\n"
        "v_mfma_f32_16x16x32_bf16 v[132:135], v[172:175], v[220:223], v[132:135]\n"
        "global_load_lds_dwordx4 v244, s[66:67] offset:2048\n"
        "ds_read_b64_tr_b16 v[172:173], v232 offset:41984\n"
        "v_mfma_f32_16x16x32_bf16 v[128:131], v[164:167], v[220:223], v[128:131]\n"
        "ds_read_b64_tr_b16 v[164:165], v230 offset:41984\n"
        "v_accvgpr_read_b32 v177, a25\n"
        "v_mfma_f32_16x16x32_bf16 v[128:131], v[160:163], v[204:207], v[128:131]\n"
        "ds_read_b64_tr_b16 v[160:161], v230 offset:33792\n"
        "v_accvgpr_read_b32 v178, a26\n"
        "v_mfma_f32_16x16x32_bf16 v[8:11], v[32:35], v[192:195], v[8:11]\n"
        "ds_read_b64_tr_b16 v[162:163], v231 offset:33792\n"
        "v_accvgpr_read_b32 v179, a27\n"
        "v_mfma_f32_16x16x32_bf16 v[8:11], v[36:39], v[208:211], v[8:11]\n"
        "global_load_lds_dwordx4 v245, s[66:67] offset:3072\n"
        "ds_read_b64_tr_b16 v[166:167], v231 offset:41984\n"
        "s_add_u32 m0, s73, 0x20000\n"
        "v_mfma_f32_16x16x32_bf16 v[12:15], v[44:47], v[208:211], v[12:15]\n"
        "ds_read_b64_tr_b16 v[174:175], v233 offset:41984\n"
        "s_add_u32 s66, s66, 0x80\n"
        "s_addc_u32 s67, s67, 0\n"
        "v_mfma_f32_16x16x32_bf16 v[12:15], v[40:43], v[192:195], v[12:15]\n"
        "ds_read_b64_tr_b16 v[170:171], v233 offset:33792\n"
        "v_accvgpr_read_b32 v180, a56\n"
        "v_mfma_f32_16x16x32_bf16 v[76:79], v[40:43], v[196:199], v[76:79]\n"
        "v_accvgpr_read_b32 v181, a57\n"
        "v_accvgpr_read_b32 v182, a58\n"
        "v_mfma_f32_16x16x32_bf16 v[76:79], v[44:47], v[212:215], v[76:79]\n"
        "v_accvgpr_read_b32 v183, a59\n"
        "v_accvgpr_write_b32 a55, 0\n"
        "v_mfma_f32_16x16x32_bf16 v[72:75], v[36:39], v[212:215], v[72:75]\n"
        "v_accvgpr_write_b32 a56, 0\n"
        "v_accvgpr_write_b32 a57, 0\n"
        "v_mfma_f32_16x16x32_bf16 v[72:75], v[32:35], v[196:199], v[72:75]\n"
        "v_accvgpr_write_b32 a58, 0\n"
        "v_accvgpr_write_b32 a59, 0\n"
        "v_mfma_f32_16x16x32_bf16 v[104:107], v[32:35], v[200:203], v[104:107]\n"
        "v_mfma_f32_16x16x32_bf16 v[104:107], v[36:39], v[216:219], v[104:107]\n"
        "v_mfma_f32_16x16x32_bf16 v[108:111], v[44:47], v[216:219], v[108:111]\n"
        "v_mfma_f32_16x16x32_bf16 v[108:111], v[40:43], v[200:203], v[108:111]\n"
        "global_store_dwordx4 v254, a[160:163], s[70:71] offset:0 sc1\n"
        "s_waitcnt vmcnt(14) lgkmcnt(0)\n"
        "s_barrier\n"
        "v_mfma_f32_16x16x32_bf16 v[140:143], v[40:43], v[204:207], v[140:143]\n"
        "ds_read_b64_tr_b16 v[40:41], v228 offset:49152\n"
        "v_mfma_f32_16x16x32_bf16 v[140:143], v[44:47], v[220:223], v[140:143]\n"
        "ds_read_b64_tr_b16 v[44:45], v228 offset:57344\n"
        "v_mfma_f32_16x16x32_bf16 v[136:139], v[36:39], v[220:223], v[136:139]\n"
        "global_load_lds_dwordx4 v238, s[68:69] offset:0\n"
        "v_mfma_f32_16x16x32_bf16 v[136:139], v[32:35], v[204:207], v[136:139]\n"
        "ds_read_b64_tr_b16 v[32:33], v226 offset:49152\n"
        "v_accvgpr_read_b32 v184, a20\n"
        "v_mfma_f32_16x16x32_bf16 v[16:19], v[48:51], v[192:195], v[16:19]\n"
        "ds_read_b64_tr_b16 v[34:35], v227 offset:49152\n"
        "v_cvt_pk_bf16_f32 v184, v184, v185\n"
        "v_mfma_f32_16x16x32_bf16 v[16:19], v[52:55], v[208:211], v[16:19]\n"
        "ds_read_b64_tr_b16 v[36:37], v226 offset:57344\n"
        "v_cvt_pk_bf16_f32 v185, v186, v187\n"
        "v_mfma_f32_16x16x32_bf16 v[20:23], v[60:63], v[208:211], v[20:23]\n"
        "ds_read_b64_tr_b16 v[38:39], v227 offset:57344\n"
        "v_cvt_pk_bf16_f32 v186, v188, v189\n"
        "v_mfma_f32_16x16x32_bf16 v[20:23], v[56:59], v[192:195], v[20:23]\n"
        "ds_read_b64_tr_b16 v[46:47], v229 offset:57344\n"
        "v_cvt_pk_bf16_f32 v187, v190, v191\n"
        "v_mfma_f32_16x16x32_bf16 v[84:87], v[56:59], v[196:199], v[84:87]\n"
        "ds_read_b64_tr_b16 v[42:43], v229 offset:49152\n"
        "v_accvgpr_write_b32 a180, v184\n"
        "v_mfma_f32_16x16x32_bf16 v[84:87], v[60:63], v[212:215], v[84:87]\n"
        "global_load_lds_dwordx4 v239, s[68:69] offset:1024\n"
        "v_accvgpr_write_b32 a181, v185\n"
        "v_mfma_f32_16x16x32_bf16 v[80:83], v[52:55], v[212:215], v[80:83]\n"
        "v_accvgpr_write_b32 a182, v186\n"
        "v_accvgpr_write_b32 a183, v187\n"
        "v_mfma_f32_16x16x32_bf16 v[80:83], v[48:51], v[196:199], v[80:83]\n"
        "v_accvgpr_read_b32 v185, a29\n"
        "v_accvgpr_read_b32 v186, a30\n"
        "v_mfma_f32_16x16x32_bf16 v[112:115], v[48:51], v[200:203], v[112:115]\n"
        "v_accvgpr_read_b32 v187, a31\n"
        "v_accvgpr_read_b32 v188, a60\n"
        "v_mfma_f32_16x16x32_bf16 v[112:115], v[52:55], v[216:219], v[112:115]\n"
        "global_load_lds_dwordx4 v240, s[68:69] offset:2048\n"
        "v_accvgpr_read_b32 v189, a61\n"
        "v_mfma_f32_16x16x32_bf16 v[116:119], v[60:63], v[216:219], v[116:119]\n"
        "v_accvgpr_read_b32 v190, a62\n"
        "v_accvgpr_read_b32 v191, a63\n"
        "v_mfma_f32_16x16x32_bf16 v[116:119], v[56:59], v[200:203], v[116:119]\n"
        "v_accvgpr_write_b32 a60, 0\n"
        "v_accvgpr_write_b32 a61, 0\n"
        "v_mfma_f32_16x16x32_bf16 v[148:151], v[56:59], v[204:207], v[148:151]\n"
        "ds_read_b64_tr_b16 v[56:57], v232 offset:49152\n"
        "v_accvgpr_write_b32 a62, 0\n"
        "v_mfma_f32_16x16x32_bf16 v[148:151], v[60:63], v[220:223], v[148:151]\n"
        "global_load_lds_dwordx4 v241, s[68:69] offset:3072\n"
        "ds_read_b64_tr_b16 v[60:61], v232 offset:57344\n"
        "s_add_u32 m0, s72, 0x0\n"
        "v_mfma_f32_16x16x32_bf16 v[144:147], v[52:55], v[220:223], v[144:147]\n"
        "ds_read_b64_tr_b16 v[52:53], v230 offset:57344\n"
        "s_add_u32 s68, s68, 0x80000\n"
        "s_addc_u32 s69, s69, 0\n"
        "v_mfma_f32_16x16x32_bf16 v[144:147], v[48:51], v[204:207], v[144:147]\n"
        "ds_read_b64_tr_b16 v[48:49], v230 offset:49152\n"
        "v_accvgpr_write_b32 a63, 0\n"
        "v_mfma_f32_16x16x32_bf16 v[24:27], v[160:163], v[192:195], v[24:27]\n"
        "ds_read_b64_tr_b16 v[50:51], v231 offset:49152\n"
        "v_mfma_f32_16x16x32_bf16 v[24:27], v[164:167], v[208:211], v[24:27]\n"
        "ds_read_b64_tr_b16 v[54:55], v231 offset:57344\n"
        "global_load_lds_dwordx4 v242, s[64:65] offset:0\n"
        "v_mfma_f32_16x16x32_bf16 v[28:31], v[172:175], v[208:211], v[28:31]\n"
        "ds_read_b64_tr_b16 v[62:63], v233 offset:57344\n"
        "v_mfma_f32_16x16x32_bf16 v[28:31], v[168:171], v[192:195], v[28:31]\n"
        "ds_read_b64_tr_b16 v[58:59], v233 offset:49152\n"
        "v_mfma_f32_16x16x32_bf16 v[92:95], v[168:171], v[196:199], v[92:95]\n"
        "ds_read_b128 v[192:195], v248 offset:2048\n"
        "v_mfma_f32_16x16x32_bf16 v[92:95], v[172:175], v[212:215], v[92:95]\n"
        "ds_read_b128 v[208:211], v249 offset:2048\n"
        "global_load_lds_dwordx4 v243, s[64:65] offset:1024\n"
        "v_mfma_f32_16x16x32_bf16 v[88:91], v[164:167], v[212:215], v[88:91]\n"
        "ds_read_b128 v[212:215], v249 offset:0\n"
        "v_mfma_f32_16x16x32_bf16 v[88:91], v[160:163], v[196:199], v[88:91]\n"
        "ds_read_b128 v[196:199], v248 offset:0\n"
        "v_accvgpr_read_b32 v176, a24\n"
        "v_mfma_f32_16x16x32_bf16 v[120:123], v[160:163], v[200:203], v[120:123]\n"
        "v_cvt_pk_bf16_f32 v176, v176, v177\n"
        "v_cvt_pk_bf16_f32 v177, v178, v179\n"
        "v_mfma_f32_16x16x32_bf16 v[120:123], v[164:167], v[216:219], v[120:123]\n"
        "v_cvt_pk_bf16_f32 v178, v180, v181\n"
        "v_cvt_pk_bf16_f32 v179, v182, v183\n"
        "v_mfma_f32_16x16x32_bf16 v[124:127], v[172:175], v[216:219], v[124:127]\n"
        "global_load_lds_dwordx4 v244, s[64:65] offset:2048\n"
        "v_accvgpr_write_b32 a184, v176\n"
        "v_mfma_f32_16x16x32_bf16 v[124:127], v[168:171], v[200:203], v[124:127]\n"
        "ds_read_b128 v[200:203], v248 offset:6144\n"
        "v_accvgpr_write_b32 a185, v177\n"
        "v_mfma_f32_16x16x32_bf16 v[156:159], v[168:171], v[204:207], v[156:159]\n"
        "s_waitcnt lgkmcnt(4)\n"
        "v_accvgpr_write_b32 a186, v178\n"
        "v_accvgpr_write_b32 a187, v179\n"
        "v_mfma_f32_16x16x32_bf16 v[156:159], v[172:175], v[220:223], v[156:159]\n"
        "ds_read_b128 v[216:219], v249 offset:6144\n"
        "v_accvgpr_read_b32 v177, a81\n"
        "v_mfma_f32_16x16x32_bf16 v[152:155], v[164:167], v[220:223], v[152:155]\n"
        "global_load_lds_dwordx4 v245, s[64:65] offset:3072\n"
        "ds_read_b128 v[220:223], v249 offset:4096\n"
        "s_add_u32 m0, s72, 0x1000\n"
        "v_mfma_f32_16x16x32_bf16 v[152:155], v[160:163], v[204:207], v[152:155]\n"
        "ds_read_b128 v[204:207], v248 offset:4096\n"
        "s_add_u32 s64, s64, 0x80\n"
        "s_addc_u32 s65, s65, 0\n"
        "v_mfma_f32_16x16x32_bf16 v[0:3], v[32:35], v[192:195], v[0:3]\n"
        "ds_read_b64_tr_b16 v[160:161], v226 offset:50176\n"
        "s_waitcnt lgkmcnt(5)\n"
        "v_mfma_f32_16x16x32_bf16 v[0:3], v[36:39], v[208:211], v[0:3]\n"
        "ds_read_b64_tr_b16 v[162:163], v227 offset:50176\n"
        "v_accvgpr_read_b32 v178, a82\n"
        "v_mfma_f32_16x16x32_bf16 v[4:7], v[44:47], v[208:211], v[4:7]\n"
        "ds_read_b64_tr_b16 v[164:165], v226 offset:58368\n"
        "global_load_lds_dwordx4 v242, s[66:67] offset:0\n"
        "v_mfma_f32_16x16x32_bf16 v[4:7], v[40:43], v[192:195], v[4:7]\n"
        "ds_read_b64_tr_b16 v[166:167], v227 offset:58368\n"
        "v_accvgpr_read_b32 v179, a83\n"
        "v_mfma_f32_16x16x32_bf16 v[68:71], v[40:43], v[196:199], v[68:71]\n"
        "ds_read_b64_tr_b16 v[172:173], v228 offset:58368\n"
        "v_accvgpr_read_b32 v180, a112\n"
        "v_mfma_f32_16x16x32_bf16 v[68:71], v[44:47], v[212:215], v[68:71]\n"
        "ds_read_b64_tr_b16 v[174:175], v229 offset:58368\n"
        "v_accvgpr_read_b32 v181, a113\n"
        "v_mfma_f32_16x16x32_bf16 v[64:67], v[36:39], v[212:215], v[64:67]\n"
        "ds_read_b64_tr_b16 v[168:169], v228 offset:50176\n"
        "global_load_lds_dwordx4 v243, s[66:67] offset:1024\n"
        "v_mfma_f32_16x16x32_bf16 v[64:67], v[32:35], v[196:199], v[64:67]\n"
        "ds_read_b64_tr_b16 v[170:171], v229 offset:50176\n"
        "s_waitcnt lgkmcnt(8)\n"
        "v_mfma_f32_16x16x32_bf16 v[96:99], v[32:35], v[200:203], v[96:99]\n"
        "v_accvgpr_read_b32 v182, a114\n"
        "v_accvgpr_read_b32 v183, a115\n"
        "v_mfma_f32_16x16x32_bf16 v[96:99], v[36:39], v[216:219], v[96:99]\n"
        "v_mfma_f32_16x16x32_bf16 v[100:103], v[44:47], v[216:219], v[100:103]\n"
        "global_load_lds_dwordx4 v244, s[66:67] offset:2048\n"
        "v_mfma_f32_16x16x32_bf16 v[100:103], v[40:43], v[200:203], v[100:103]\n"
        "v_mfma_f32_16x16x32_bf16 v[132:135], v[40:43], v[204:207], v[132:135]\n"
        "ds_read_b64_tr_b16 v[40:41], v232 offset:50176\n"
        "v_mfma_f32_16x16x32_bf16 v[132:135], v[44:47], v[220:223], v[132:135]\n"
        "ds_read_b64_tr_b16 v[44:45], v232 offset:58368\n"
        "v_mfma_f32_16x16x32_bf16 v[128:131], v[36:39], v[220:223], v[128:131]\n"
        "global_load_lds_dwordx4 v245, s[66:67] offset:3072\n"
        "s_add_u32 m0, s73, 0x24000\n"
        "v_mfma_f32_16x16x32_bf16 v[128:131], v[32:35], v[204:207], v[128:131]\n"
        "ds_read_b64_tr_b16 v[32:33], v230 offset:50176\n"
        "v_accvgpr_read_b32 v184, a28\n"
        "v_mfma_f32_16x16x32_bf16 v[8:11], v[48:51], v[192:195], v[8:11]\n"
        "ds_read_b64_tr_b16 v[34:35], v231 offset:50176\n"
        "v_cvt_pk_bf16_f32 v184, v184, v185\n"
        "v_mfma_f32_16x16x32_bf16 v[8:11], v[52:55], v[208:211], v[8:11]\n"
        "ds_read_b64_tr_b16 v[36:37], v230 offset:58368\n"
        "v_cvt_pk_bf16_f32 v185, v186, v187\n"
        "v_mfma_f32_16x16x32_bf16 v[12:15], v[60:63], v[208:211], v[12:15]\n"
        "ds_read_b64_tr_b16 v[38:39], v231 offset:58368\n"
        "v_cvt_pk_bf16_f32 v186, v188, v189\n"
        "v_mfma_f32_16x16x32_bf16 v[12:15], v[56:59], v[192:195], v[12:15]\n"
        "ds_read_b64_tr_b16 v[46:47], v233 offset:58368\n"
        "v_cvt_pk_bf16_f32 v187, v190, v191\n"
        "v_mfma_f32_16x16x32_bf16 v[76:79], v[56:59], v[196:199], v[76:79]\n"
        "ds_read_b64_tr_b16 v[42:43], v233 offset:50176\n"
        "v_accvgpr_write_b32 a188, v184\n"
        "v_mfma_f32_16x16x32_bf16 v[76:79], v[60:63], v[212:215], v[76:79]\n"
        "s_add_u32 s66, s66, 0x80\n"
        "s_addc_u32 s67, s67, 0\n"
        "v_accvgpr_write_b32 a189, v185\n"
        "v_mfma_f32_16x16x32_bf16 v[72:75], v[52:55], v[212:215], v[72:75]\n"
        "v_accvgpr_write_b32 a190, v186\n"
        "v_accvgpr_write_b32 a191, v187\n"
        "v_mfma_f32_16x16x32_bf16 v[72:75], v[48:51], v[196:199], v[72:75]\n"
        "v_accvgpr_read_b32 v185, a85\n"
        "v_accvgpr_read_b32 v186, a86\n"
        "v_mfma_f32_16x16x32_bf16 v[104:107], v[48:51], v[200:203], v[104:107]\n"
        "v_accvgpr_read_b32 v187, a87\n"
        "v_accvgpr_read_b32 v188, a116\n"
        "v_mfma_f32_16x16x32_bf16 v[104:107], v[52:55], v[216:219], v[104:107]\n"
        "v_accvgpr_read_b32 v189, a117\n"
        "v_accvgpr_read_b32 v190, a118\n"
        "v_mfma_f32_16x16x32_bf16 v[108:111], v[60:63], v[216:219], v[108:111]\n"
        "v_accvgpr_read_b32 v191, a119\n"
        "v_mfma_f32_16x16x32_bf16 v[108:111], v[56:59], v[200:203], v[108:111]\n"
        "v_mfma_f32_16x16x32_bf16 v[140:143], v[56:59], v[204:207], v[140:143]\n"
        "s_waitcnt vmcnt(13) lgkmcnt(0)\n"
        "v_mfma_f32_16x16x32_bf16 v[140:143], v[60:63], v[220:223], v[140:143]\n"
        "s_barrier\n"
        "v_mfma_f32_16x16x32_bf16 v[136:139], v[52:55], v[220:223], v[136:139]\n"
        "global_load_lds_dwordx4 v238, s[68:69] offset:0\n"
        "ds_read_b64_tr_b16 v[52:53], v226 offset:24576\n"
        "v_mfma_f32_16x16x32_bf16 v[136:139], v[48:51], v[204:207], v[136:139]\n"
        "ds_read_b64_tr_b16 v[48:49], v226 offset:16384\n"
        "v_mfma_f32_16x16x32_bf16 v[16:19], v[160:163], v[192:195], v[16:19]\n"
        "ds_read_b64_tr_b16 v[50:51], v227 offset:16384\n"
        "v_mfma_f32_16x16x32_bf16 v[16:19], v[164:167], v[208:211], v[16:19]\n"
        "ds_read_b64_tr_b16 v[54:55], v227 offset:24576\n"
        "v_mfma_f32_16x16x32_bf16 v[20:23], v[172:175], v[208:211], v[20:23]\n"
        "global_store_dwordx4 v254, a[164:167], s[70:71] offset:1024 sc1\n"
        "ds_read_b64_tr_b16 v[60:61], v228 offset:24576\n"
        "ds_read_b64_tr_b16 v[62:63], v229 offset:24576\n"
        "s_add_u32 s70, s38, s97\n"
        "s_addc_u32 s71, s39, 0\n"
        "v_mfma_f32_16x16x32_bf16 v[20:23], v[168:171], v[192:195], v[20:23]\n"
        "ds_read_b64_tr_b16 v[56:57], v228 offset:16384\n"
        "s_add_u32 s97, s52, 0x3000\n"
        "s_add_u32 s97, s97, s75\n"
        "v_mfma_f32_16x16x32_bf16 v[84:87], v[168:171], v[196:199], v[84:87]\n"
        "ds_read_b64_tr_b16 v[58:59], v229 offset:16384\n"
        "v_mfma_f32_16x16x32_bf16 v[84:87], v[172:175], v[212:215], v[84:87]\n"
        "global_load_lds_dwordx4 v239, s[68:69] offset:1024\n"
        "v_mfma_f32_16x16x32_bf16 v[80:83], v[164:167], v[212:215], v[80:83]\n"
        "v_mfma_f32_16x16x32_bf16 v[80:83], v[160:163], v[196:199], v[80:83]\n"
        "v_accvgpr_read_b32 v176, a80\n"
        "v_cvt_pk_bf16_f32 v176, v176, v177\n"
        "v_mfma_f32_16x16x32_bf16 v[112:115], v[160:163], v[200:203], v[112:115]\n"
        "v_cvt_pk_bf16_f32 v177, v178, v179\n"
        "v_cvt_pk_bf16_f32 v178, v180, v181\n"
        "v_mfma_f32_16x16x32_bf16 v[112:115], v[164:167], v[216:219], v[112:115]\n"
        "global_load_lds_dwordx4 v240, s[68:69] offset:2048\n"
        "v_mfma_f32_16x16x32_bf16 v[116:119], v[172:175], v[216:219], v[116:119]\n"
        "v_cvt_pk_bf16_f32 v179, v182, v183\n"
        "v_accvgpr_write_b32 a16, v176\n"
        "v_mfma_f32_16x16x32_bf16 v[116:119], v[168:171], v[200:203], v[116:119]\n"
        "v_accvgpr_write_b32 a17, v177\n"
        "v_accvgpr_write_b32 a18, v178\n"
        "v_mfma_f32_16x16x32_bf16 v[148:151], v[168:171], v[204:207], v[148:151]\n"
        "ds_read_b64_tr_b16 v[168:169], v232 offset:16384\n"
        "v_accvgpr_write_b32 a19, v179\n"
        "v_mfma_f32_16x16x32_bf16 v[148:151], v[172:175], v[220:223], v[148:151]\n"
        "global_load_lds_dwordx4 v241, s[68:69] offset:3072\n"
        "s_add_u32 m0, s72, 0x10000\n"
        "v_mfma_f32_16x16x32_bf16 v[144:147], v[164:167], v[220:223], v[144:147]\n"
        "ds_read_b64_tr_b16 v[164:165], v230 offset:24576\n"
        "s_add_u32 s68, s68, 0x80000\n"
        "s_addc_u32 s69, s69, 0\n"
        "v_mfma_f32_16x16x32_bf16 v[144:147], v[160:163], v[204:207], v[144:147]\n"
        "ds_read_b64_tr_b16 v[160:161], v230 offset:16384\n"
        "v_accvgpr_read_b32 v177, a89\n"
        "v_mfma_f32_16x16x32_bf16 v[24:27], v[32:35], v[192:195], v[24:27]\n"
        "ds_read_b64_tr_b16 v[162:163], v231 offset:16384\n"
        "v_accvgpr_read_b32 v178, a90\n"
        "v_mfma_f32_16x16x32_bf16 v[24:27], v[36:39], v[208:211], v[24:27]\n"
        "ds_read_b64_tr_b16 v[166:167], v231 offset:24576\n"
        "global_load_lds_dwordx4 v242, s[64:65] offset:0\n"
        "v_mfma_f32_16x16x32_bf16 v[28:31], v[44:47], v[208:211], v[28:31]\n"
        "ds_read_b64_tr_b16 v[172:173], v232 offset:24576\n"
        "v_accvgpr_read_b32 v179, a91\n"
        "v_mfma_f32_16x16x32_bf16 v[28:31], v[40:43], v[192:195], v[28:31]\n"
        "ds_read_b64_tr_b16 v[174:175], v233 offset:24576\n"
        "v_accvgpr_read_b32 v180, a120\n"
        "v_mfma_f32_16x16x32_bf16 v[92:95], v[40:43], v[196:199], v[92:95]\n"
        "ds_read_b64_tr_b16 v[170:171], v233 offset:16384\n"
        "v_accvgpr_read_b32 v181, a121\n"
        "v_mfma_f32_16x16x32_bf16 v[92:95], v[44:47], v[212:215], v[92:95]\n"
        "ds_read_b128 v[192:195], v246 offset:34816\n"
        "global_load_lds_dwordx4 v243, s[64:65] offset:1024\n"
        "v_mfma_f32_16x16x32_bf16 v[88:91], v[36:39], v[212:215], v[88:91]\n"
        "ds_read_b128 v[208:211], v247 offset:34816\n"
        "v_accvgpr_read_b32 v182, a122\n"
        "v_mfma_f32_16x16x32_bf16 v[88:91], v[32:35], v[196:199], v[88:91]\n"
        "ds_read_b128 v[196:199], v246 offset:32768\n"
        "v_accvgpr_read_b32 v183, a123\n"
        "v_mfma_f32_16x16x32_bf16 v[120:123], v[32:35], v[200:203], v[120:123]\n"
        "ds_read_b128 v[212:215], v247 offset:32768\n"
        "v_mfma_f32_16x16x32_bf16 v[120:123], v[36:39], v[216:219], v[120:123]\n"
        "global_load_lds_dwordx4 v244, s[64:65] offset:2048\n"
        "v_mfma_f32_16x16x32_bf16 v[124:127], v[44:47], v[216:219], v[124:127]\n"
        "ds_read_b128 v[216:219], v247 offset:38912\n"
        "v_mfma_f32_16x16x32_bf16 v[124:127], v[40:43], v[200:203], v[124:127]\n"
        "ds_read_b128 v[200:203], v246 offset:38912\n"
        "v_mfma_f32_16x16x32_bf16 v[156:159], v[40:43], v[204:207], v[156:159]\n"
        "ds_read_b64_tr_b16 v[40:41], v228 offset:17408\n"
        "v_mfma_f32_16x16x32_bf16 v[156:159], v[44:47], v[220:223], v[156:159]\n"
        "global_load_lds_dwordx4 v245, s[64:65] offset:3072\n"
        "s_add_u32 m0, s72, 0x11000\n"
        "v_mfma_f32_16x16x32_bf16 v[152:155], v[36:39], v[220:223], v[152:155]\n"
        "ds_read_b128 v[220:223], v247 offset:36864\n"
        "s_add_u32 s64, s64, 0x80\n"
        "s_addc_u32 s65, s65, 0\n"
        "v_mfma_f32_16x16x32_bf16 v[152:155], v[32:35], v[204:207], v[152:155]\n"
        "ds_read_b128 v[204:207], v246 offset:36864\n"
        "s_waitcnt lgkmcnt(6)\n"
        "v_mfma_f32_16x16x32_bf16 v[0:3], v[48:51], v[192:195], v[0:3]\n"
        "ds_read_b64_tr_b16 v[32:33], v226 offset:17408\n"
        "v_accvgpr_read_b32 v184, a84\n"
        "v_mfma_f32_16x16x32_bf16 v[0:3], v[52:55], v[208:211], v[0:3]\n"
        "s_waitcnt lgkmcnt(6)\n"
        "v_cvt_pk_bf16_f32 v184, v184, v185\n"
        "v_cvt_pk_bf16_f32 v185, v186, v187\n"
        "v_mfma_f32_16x16x32_bf16 v[4:7], v[60:63], v[208:211], v[4:7]\n"
        "global_load_lds_dwordx4 v242, s[66:67] offset:0\n"
        "v_cvt_pk_bf16_f32 v186, v188, v189\n"
        "v_mfma_f32_16x16x32_bf16 v[4:7], v[56:59], v[192:195], v[4:7]\n"
        "ds_read_b64_tr_b16 v[34:35], v227 offset:17408\n"
        "v_cvt_pk_bf16_f32 v187, v190, v191\n"
        "v_mfma_f32_16x16x32_bf16 v[68:71], v[56:59], v[196:199], v[68:71]\n"
        "s_waitcnt lgkmcnt(4)\n"
        "v_accvgpr_write_b32 a20, v184\n"
        "v_accvgpr_write_b32 a21, v185\n"
        "v_mfma_f32_16x16x32_bf16 v[68:71], v[60:63], v[212:215], v[68:71]\n"
        "ds_read_b64_tr_b16 v[36:37], v226 offset:25600\n"
        "v_accvgpr_write_b32 a22, v186\n"
        "v_mfma_f32_16x16x32_bf16 v[64:67], v[52:55], v[212:215], v[64:67]\n"
        "ds_read_b64_tr_b16 v[38:39], v227 offset:25600\n"
        "v_accvgpr_write_b32 a23, v187\n"
        "v_mfma_f32_16x16x32_bf16 v[64:67], v[48:51], v[196:199], v[64:67]\n"
        "ds_read_b64_tr_b16 v[44:45], v228 offset:25600\n"
        "global_load_lds_dwordx4 v243, s[66:67] offset:1024\n"
        "v_mfma_f32_16x16x32_bf16 v[96:99], v[48:51], v[200:203], v[96:99]\n"
        "ds_read_b64_tr_b16 v[46:47], v229 offset:25600\n"
        "v_accvgpr_read_b32 v185, a93\n"
        "v_mfma_f32_16x16x32_bf16 v[96:99], v[52:55], v[216:219], v[96:99]\n"
        "ds_read_b64_tr_b16 v[42:43], v229 offset:17408\n"
        "v_accvgpr_read_b32 v186, a94\n"
        "v_mfma_f32_16x16x32_bf16 v[100:103], v[60:63], v[216:219], v[100:103]\n"
        "s_waitcnt lgkmcnt(6)\n"
        "v_accvgpr_read_b32 v187, a95\n"
        "v_accvgpr_read_b32 v188, a124\n"
        "v_mfma_f32_16x16x32_bf16 v[100:103], v[56:59], v[200:203], v[100:103]\n"
        "global_load_lds_dwordx4 v244, s[66:67] offset:2048\n"
        "v_mfma_f32_16x16x32_bf16 v[132:135], v[56:59], v[204:207], v[132:135]\n"
        "ds_read_b64_tr_b16 v[56:57], v232 offset:17408\n"
        "v_accvgpr_read_b32 v189, a125\n"
        "v_mfma_f32_16x16x32_bf16 v[132:135], v[60:63], v[220:223], v[132:135]\n"
        "ds_read_b64_tr_b16 v[60:61], v232 offset:25600\n"
        "v_accvgpr_read_b32 v190, a126\n"
        "v_mfma_f32_16x16x32_bf16 v[128:131], v[52:55], v[220:223], v[128:131]\n"
        "ds_read_b64_tr_b16 v[52:53], v230 offset:25600\n"
        "v_accvgpr_read_b32 v191, a127\n"
        "v_mfma_f32_16x16x32_bf16 v[128:131], v[48:51], v[204:207], v[128:131]\n"
        "global_load_lds_dwordx4 v245, s[66:67] offset:3072\n"
        "s_add_u32 m0, s73, 0x1c000\n"
        "v_mfma_f32_16x16x32_bf16 v[8:11], v[160:163], v[192:195], v[8:11]\n"
        "ds_read_b64_tr_b16 v[48:49], v230 offset:17408\n"
        "s_add_u32 s66, s66, 0x80\n"
        "s_addc_u32 s67, s67, 0\n"
        "v_mfma_f32_16x16x32_bf16 v[8:11], v[164:167], v[208:211], v[8:11]\n"
        "ds_read_b64_tr_b16 v[50:51], v231 offset:17408\n"
        "v_accvgpr_write_b32 a93, v252\n"
        "v_mfma_f32_16x16x32_bf16 v[12:15], v[172:175], v[208:211], v[12:15]\n"
        "ds_read_b64_tr_b16 v[54:55], v231 offset:25600\n"
        "v_mfma_f32_16x16x32_bf16 v[12:15], v[168:171], v[192:195], v[12:15]\n"
        "ds_read_b64_tr_b16 v[62:63], v233 offset:25600\n"
        "v_mfma_f32_16x16x32_bf16 v[76:79], v[168:171], v[196:199], v[76:79]\n"
        "ds_read_b64_tr_b16 v[58:59], v233 offset:17408\n"
        "v_mfma_f32_16x16x32_bf16 v[76:79], v[172:175], v[212:215], v[76:79]\n"
        "v_mfma_f32_16x16x32_bf16 v[72:75], v[164:167], v[212:215], v[72:75]\n"
        "v_mfma_f32_16x16x32_bf16 v[72:75], v[160:163], v[196:199], v[72:75]\n"
        "v_accvgpr_read_b32 v176, a88\n"
        "v_cvt_pk_bf16_f32 v176, v176, v177\n"
        "v_mfma_f32_16x16x32_bf16 v[104:107], v[160:163], v[200:203], v[104:107]\n"
        "s_mul_i32 s90, s43, 529408\n"
        "v_cvt_pk_bf16_f32 v177, v178, v179\n"
        "v_cvt_pk_bf16_f32 v178, v180, v181\n"
        "v_mfma_f32_16x16x32_bf16 v[104:107], v[164:167], v[216:219], v[104:107]\n"
        "s_add_u32 s90, s90, s72\n"
        "v_cvt_pk_bf16_f32 v179, v182, v183\n"
        "v_accvgpr_write_b32 a24, v176\n"
        "v_mfma_f32_16x16x32_bf16 v[108:111], v[172:175], v[216:219], v[108:111]\n"
        "s_and_b32 s78, s45, 1\n"
        "v_accvgpr_write_b32 a25, v177\n"
        "v_accvgpr_write_b32 a26, v178\n"
        "v_mfma_f32_16x16x32_bf16 v[108:111], v[168:171], v[200:203], v[108:111]\n"
        "s_lshl_b32 s78, s78, 15\n"
        "v_accvgpr_write_b32 a27, v179\n"
        "s_add_u32 s78, s78, 0x10000\n"
        "v_mfma_f32_16x16x32_bf16 v[140:143], v[168:171], v[204:207], v[140:143]\n"
        "s_waitcnt vmcnt(13) lgkmcnt(0)\n"
        "s_and_b32 s80, s40, 1\n"
        "s_lshl_b32 s80, s80, 11\n"
        "v_mfma_f32_16x16x32_bf16 v[140:143], v[172:175], v[220:223], v[140:143]\n"
        "s_barrier\n"
        "v_mfma_f32_16x16x32_bf16 v[136:139], v[164:167], v[220:223], v[136:139]\n"
        "global_load_lds_dwordx4 v238, s[68:69] offset:0\n"
        "ds_read_b64_tr_b16 v[164:165], v226 offset:40960\n"
        "s_and_b32 s79, s45, 1\n"
        "v_mfma_f32_16x16x32_bf16 v[136:139], v[160:163], v[204:207], v[136:139]\n"
        "ds_read_b64_tr_b16 v[160:161], v226 offset:32768\n"
        "s_lshl_b32 s79, s79, 15\n"
        "s_lshl_b32 s82, s40, 4\n"
        "v_mfma_f32_16x16x32_bf16 v[16:19], v[32:35], v[192:195], v[16:19]\n"
        "ds_read_b64_tr_b16 v[162:163], v227 offset:32768\n"
        "s_sub_u32 s83, 14, s44\n"
        "v_accvgpr_write_b32 a64, 0\n"
        "v_mfma_f32_16x16x32_bf16 v[16:19], v[36:39], v[208:211], v[16:19]\n"
        "ds_read_b64_tr_b16 v[166:167], v227 offset:40960\n"
        "s_lshl_b32 s50, s41, 7\n"
        "v_accvgpr_write_b32 a65, 0\n"
        "v_mfma_f32_16x16x32_bf16 v[20:23], v[44:47], v[208:211], v[20:23]\n"
        "ds_read_b64_tr_b16 v[172:173], v228 offset:40960\n"
        "global_load_lds_dwordx4 v239, s[68:69] offset:1024\n"
        "s_lshl2_add_u32 s50, s42, s50\n"
        "v_mfma_f32_16x16x32_bf16 v[20:23], v[40:43], v[192:195], v[20:23]\n"
        "ds_read_b64_tr_b16 v[174:175], v229 offset:40960\n"
        "s_add_u32 s50, s50, s40\n"
        "v_accvgpr_write_b32 a66, 0\n"
        "v_mfma_f32_16x16x32_bf16 v[84:87], v[40:43], v[196:199], v[84:87]\n"
        "ds_read_b64_tr_b16 v[168:169], v228 offset:32768\n"
        "s_lshl_b32 s50, s50, 12\n"
        "v_accvgpr_write_b32 a67, 0\n"
        "v_mfma_f32_16x16x32_bf16 v[84:87], v[44:47], v[212:215], v[84:87]\n"
        "ds_read_b64_tr_b16 v[170:171], v229 offset:32768\n"
        "s_mul_i32 s51, s40, 8320\n"
        "v_accvgpr_write_b32 a68, 0\n"
        "v_mfma_f32_16x16x32_bf16 v[80:83], v[36:39], v[212:215], v[80:83]\n"
        "global_load_lds_dwordx4 v240, s[68:69] offset:2048\n"
        "s_add_u32 s51, s51, 0x18000\n"
        "v_mfma_f32_16x16x32_bf16 v[80:83], v[32:35], v[196:199], v[80:83]\n"
        "s_add_u32 s92, s26, s50\n"
        "s_addc_u32 s93, s27, 0\n"
        "v_accvgpr_write_b32 a69, 0\n"
        "v_mfma_f32_16x16x32_bf16 v[112:115], v[32:35], v[200:203], v[112:115]\n"
        "s_add_u32 s50, s50, 0x3300000\n"
        "v_accvgpr_write_b32 a70, 0\n"
        "v_accvgpr_write_b32 a71, 0\n"
        "v_mfma_f32_16x16x32_bf16 v[112:115], v[36:39], v[216:219], v[112:115]\n"
        "s_add_u32 s94, s38, s50\n"
        "s_addc_u32 s95, s39, 0\n"
        "s_add_u32 s50, s51, 4128\n"
        "v_mfma_f32_16x16x32_bf16 v[116:119], v[44:47], v[216:219], v[116:119]\n"
        "global_load_lds_dwordx4 v241, s[68:69] offset:3072\n"
        "s_add_u32 m0, s72, 0x8000\n"
        "v_mfma_f32_16x16x32_bf16 v[116:119], v[40:43], v[200:203], v[116:119]\n"
        "s_add_u32 s68, s68, 0x80000\n"
        "s_addc_u32 s69, s69, 0\n"
        "v_bfe_u32 v176, v254, 4, 4\n"
        "v_mfma_f32_16x16x32_bf16 v[148:151], v[40:43], v[204:207], v[148:151]\n"
        "ds_read_b64_tr_b16 v[40:41], v232 offset:32768\n"
        "v_lshrrev_b32 v177, 8, v254\n"
        "v_mfma_f32_16x16x32_bf16 v[148:151], v[44:47], v[220:223], v[148:151]\n"
        "ds_read_b64_tr_b16 v[44:45], v232 offset:40960\n"
        "v_lshrrev_b32 v178, 7, v254\n"
        "v_mfma_f32_16x16x32_bf16 v[144:147], v[36:39], v[220:223], v[144:147]\n"
        "global_load_lds_dwordx4 v242, s[64:65] offset:0\n"
        "v_mfma_f32_16x16x32_bf16 v[144:147], v[32:35], v[204:207], v[144:147]\n"
        "ds_read_b64_tr_b16 v[32:33], v230 offset:32768\n"
        "v_accvgpr_read_b32 v184, a92\n"
        "v_mfma_f32_16x16x32_bf16 v[24:27], v[48:51], v[192:195], v[24:27]\n"
        "ds_read_b64_tr_b16 v[34:35], v231 offset:32768\n"
        "v_cvt_pk_bf16_f32 v184, v184, v185\n"
        "v_mfma_f32_16x16x32_bf16 v[24:27], v[52:55], v[208:211], v[24:27]\n"
        "ds_read_b64_tr_b16 v[36:37], v230 offset:40960\n"
        "v_cvt_pk_bf16_f32 v185, v186, v187\n"
        "LHA8_%=:\n"
        "s_cbranch_execz LHRA_%=\n"
        "v_mfma_f32_16x16x32_bf16 v[28:31], v[60:63], v[208:211], v[28:31]\n"
        "ds_read_b64_tr_b16 v[38:39], v231 offset:40960\n"
        "v_cvt_pk_bf16_f32 v186, v188, v189\n"
        "v_mfma_f32_16x16x32_bf16 v[28:31], v[56:59], v[192:195], v[28:31]\n"
        "global_store_dwordx4 v254, a[168:171], s[70:71] offset:0 sc1\n"
        "ds_read_b64_tr_b16 v[46:47], v233 offset:40960\n"
        "ds_read_b64_tr_b16 v[42:43], v233 offset:32768\n"
        "v_cvt_pk_bf16_f32 v187, v190, v191\n"
        "v_mfma_f32_16x16x32_bf16 v[92:95], v[56:59], v[196:199], v[92:95]\n"
        "ds_read_b128 v[192:195], v246 offset:2048\n"
        "v_accvgpr_write_b32 a28, v184\n"
        "v_mfma_f32_16x16x32_bf16 v[92:95], v[60:63], v[212:215], v[92:95]\n"
        "ds_read_b128 v[208:211], v247 offset:2048\n"
        "v_accvgpr_write_b32 a29, v185\n"
        "v_mfma_f32_16x16x32_bf16 v[88:91], v[52:55], v[212:215], v[88:91]\n"
        "ds_read_b128 v[212:215], v247 offset:0\n"
        "v_accvgpr_write_b32 a30, v186\n"
        "v_mfma_f32_16x16x32_bf16 v[88:91], v[48:51], v[196:199], v[88:91]\n"
        "global_load_lds_dwordx4 v243, s[64:65] offset:1024\n"
        "v_accvgpr_write_b32 a31, v187\n"
        "v_mfma_f32_16x16x32_bf16 v[120:123], v[48:51], v[200:203], v[120:123]\n"
        "ds_read_b128 v[196:199], v246 offset:0\n"
        "v_lshrrev_b32 v186, 5, v254\n"
        "v_mfma_f32_16x16x32_bf16 v[120:123], v[52:55], v[216:219], v[120:123]\n"
        "v_lshrrev_b32 v187, 9, v254\n"
        "v_xor_b32 v186, v186, v187\n"
        "v_mfma_f32_16x16x32_bf16 v[124:127], v[60:63], v[216:219], v[124:127]\n"
        "ds_read_b128 v[216:219], v247 offset:6144\n"
        "v_and_b32 v186, 1, v186\n"
        "v_mfma_f32_16x16x32_bf16 v[124:127], v[56:59], v[200:203], v[124:127]\n"
        "global_load_lds_dwordx4 v244, s[64:65] offset:2048\n"
        "ds_read_b128 v[200:203], v246 offset:6144\n"
        "v_mfma_f32_16x16x32_bf16 v[156:159], v[56:59], v[204:207], v[156:159]\n"
        "ds_read_b64_tr_b16 v[56:57], v228 offset:33792\n"
        "v_cmp_eq_u32 vcc, 0, v186\n"
        "v_mfma_f32_16x16x32_bf16 v[156:159], v[60:63], v[220:223], v[156:159]\n"
        "ds_read_b64_tr_b16 v[60:61], v228 offset:41984\n"
        "v_and_b32 v187, 0x30, v254\n"
        "v_mfma_f32_16x16x32_bf16 v[152:155], v[52:55], v[220:223], v[152:155]\n"
        "ds_read_b128 v[220:223], v247 offset:4096\n"
        "v_bfe_u32 v179, v254, 4, 3\n"
        "v_mfma_f32_16x16x32_bf16 v[152:155], v[48:51], v[204:207], v[152:155]\n"
        "global_load_lds_dwordx4 v245, s[64:65] offset:3072\n"
        "s_add_u32 m0, s72, 0x9000\n"
        "s_waitcnt lgkmcnt(6)\n"
        "v_mfma_f32_16x16x32_bf16 v[0:3], v[160:163], v[192:195], v[0:3]\n"
        "ds_read_b128 v[204:207], v246 offset:4096\n"
        "s_add_u32 s64, s64, 0x80\n"
        "s_addc_u32 s65, s65, 0\n"
        "v_mfma_f32_16x16x32_bf16 v[0:3], v[164:167], v[208:211], v[0:3]\n"
        "ds_read_b64_tr_b16 v[48:49], v226 offset:33792\n"
        "v_and_b32 v180, 6, v178\n"
        "v_mfma_f32_16x16x32_bf16 v[4:7], v[172:175], v[208:211], v[4:7]\n"
        "ds_read_b64_tr_b16 v[50:51], v227 offset:33792\n"
        "v_xor_b32 v180, v180, v179\n"
        "v_mfma_f32_16x16x32_bf16 v[4:7], v[168:171], v[192:195], v[4:7]\n"
        "ds_read_b64_tr_b16 v[52:53], v226 offset:41984\n"
        "s_waitcnt lgkmcnt(7)\n"
        "v_mfma_f32_16x16x32_bf16 v[68:71], v[168:171], v[196:199], v[68:71]\n"
        "ds_read_b64_tr_b16 v[54:55], v227 offset:41984\n"
        "global_load_lds_dwordx4 v242, s[66:67] offset:0\n"
        "v_mfma_f32_16x16x32_bf16 v[68:71], v[172:175], v[212:215], v[68:71]\n"
        "ds_read_b64_tr_b16 v[62:63], v229 offset:41984\n"
        "v_lshlrev_b32 v180, 4, v180\n"
        "v_mfma_f32_16x16x32_bf16 v[64:67], v[164:167], v[212:215], v[64:67]\n"
        "ds_read_b64_tr_b16 v[58:59], v229 offset:33792\n"
        "v_lshl_add_u32 v181, v178, 11, v180\n"
        "v_mfma_f32_16x16x32_bf16 v[64:67], v[160:163], v[196:199], v[64:67]\n"
        "v_xor_b32 v178, 4, v178\n"
        "v_lshl_add_u32 v178, v178, 11, v180\n"
        "v_mfma_f32_16x16x32_bf16 v[96:99], v[160:163], v[200:203], v[96:99]\n"
        "global_load_lds_dwordx4 v243, s[66:67] offset:1024\n"
        "v_mfma_f32_16x16x32_bf16 v[96:99], v[164:167], v[216:219], v[96:99]\n"
        "s_waitcnt lgkmcnt(5)\n"
        "v_add_u32 v182, 4096, v181\n"
        "v_accvgpr_write_b32 a84, v182\n"
        "v_mfma_f32_16x16x32_bf16 v[100:103], v[172:175], v[216:219], v[100:103]\n"
        "v_add_u32 v182, 3200, v181\n"
        "v_accvgpr_write_b32 a85, v182\n"
        "v_mfma_f32_16x16x32_bf16 v[100:103], v[168:171], v[200:203], v[100:103]\n"
        "v_add_u32 v182, 18432, v178\n"
        "v_accvgpr_write_b32 a86, v182\n"
        "v_mfma_f32_16x16x32_bf16 v[132:135], v[168:171], v[204:207], v[132:135]\n"
        "global_load_lds_dwordx4 v244, s[66:67] offset:2048\n"
        "v_mfma_f32_16x16x32_bf16 v[132:135], v[172:175], v[220:223], v[132:135]\n"
        "ds_read_b64_tr_b16 v[172:173], v232 offset:41984\n"
        "v_add_u32 v182, 17536, v178\n"
        "v_mfma_f32_16x16x32_bf16 v[128:131], v[164:167], v[220:223], v[128:131]\n"
        "ds_read_b64_tr_b16 v[164:165], v230 offset:41984\n"
        "v_accvgpr_write_b32 a87, v182\n"
        "v_mfma_f32_16x16x32_bf16 v[128:131], v[160:163], v[204:207], v[128:131]\n"
        "ds_read_b64_tr_b16 v[160:161], v230 offset:33792\n"
        "v_mov_b32 v178, s96\n"
        "v_mfma_f32_16x16x32_bf16 v[8:11], v[32:35], v[192:195], v[8:11]\n"
        "global_load_lds_dwordx4 v245, s[66:67] offset:3072\n"
        "s_add_u32 m0, s73, 0x20000\n"
        "v_mfma_f32_16x16x32_bf16 v[8:11], v[36:39], v[208:211], v[8:11]\n"
        "ds_read_b64_tr_b16 v[162:163], v231 offset:33792\n"
        "s_add_u32 s66, s66, 0x80\n"
        "s_addc_u32 s67, s67, 0\n"
        "v_mfma_f32_16x16x32_bf16 v[12:15], v[44:47], v[208:211], v[12:15]\n"
        "ds_read_b64_tr_b16 v[166:167], v231 offset:41984\n"
        "v_add_u32 v180, s51, v254\n"
        "v_mfma_f32_16x16x32_bf16 v[12:15], v[40:43], v[192:195], v[12:15]\n"
        "ds_read_b64_tr_b16 v[174:175], v233 offset:41984\n"
        "v_add_u32 v180, 4096, v180\n"
        "v_mfma_f32_16x16x32_bf16 v[76:79], v[40:43], v[196:199], v[76:79]\n"
        "ds_read_b64_tr_b16 v[168:169], v232 offset:33792\n"
        "v_writelane_b32 v180, s96, 2\n"
        "v_mfma_f32_16x16x32_bf16 v[76:79], v[44:47], v[212:215], v[76:79]\n"
        "ds_read_b64_tr_b16 v[170:171], v233 offset:33792\n"
        "s_add_u32 s96, s96, 16\n"
        "v_writelane_b32 v180, s96, 3\n"
        "v_mfma_f32_16x16x32_bf16 v[72:75], v[36:39], v[212:215], v[72:75]\n"
        "s_add_u32 s96, s51, 4098\n"
        "v_accvgpr_write_b32 a90, v180\n"
        "v_sub_u32 v179, s96, v178\n"
        "v_mfma_f32_16x16x32_bf16 v[72:75], v[32:35], v[196:199], v[72:75]\n"
        "s_add_u32 s96, s51, 480\n"
        "v_and_b32 v181, 1, v176\n"
        "v_mul_lo_u32 v179, v179, v181\n"
        "v_mfma_f32_16x16x32_bf16 v[104:107], v[32:35], v[200:203], v[104:107]\n"
        "v_add_u32 v178, v178, v179\n"
        "v_lshl_add_u32 v178, v176, 1, v178\n"
        "v_mfma_f32_16x16x32_bf16 v[104:107], v[36:39], v[216:219], v[104:107]\n"
        "v_lshl_add_u32 v178, v177, 4, v178\n"
        "v_accvgpr_write_b32 a88, v178\n"
        "v_mfma_f32_16x16x32_bf16 v[108:111], v[44:47], v[216:219], v[108:111]\n"
        "v_lshlrev_b32 v179, 5, v176\n"
        "v_sub_u32 v179, s96, v179\n"
        "v_mfma_f32_16x16x32_bf16 v[108:111], v[40:43], v[200:203], v[108:111]\n"
        "s_lshl_b32 s96, s54, 16\n"
        "v_lshl_add_u32 v179, v177, 4, v179\n"
        "v_accvgpr_write_b32 a89, v179\n"
        "v_mfma_f32_16x16x32_bf16 v[140:143], v[40:43], v[204:207], v[140:143]\n"
        "s_waitcnt lgkmcnt(1)\n"
        "v_mov_b32 v181, 0\n"
        "v_writelane_b32 v181, s96, 3\n"
        "v_mfma_f32_16x16x32_bf16 v[140:143], v[44:47], v[220:223], v[140:143]\n"
        "s_waitcnt vmcnt(13) lgkmcnt(0)\n"
        "v_accvgpr_write_b32 a91, v181\n"
        "s_and_b32 s54, s54, 0xffff0000\n"
        "v_mfma_f32_16x16x32_bf16 v[136:139], v[36:39], v[220:223], v[136:139]\n"
        "s_barrier\n"
        "v_mfma_f32_16x16x32_bf16 v[136:139], v[32:35], v[204:207], v[136:139]\n"
        "global_load_lds_dwordx4 v238, s[68:69] offset:0\n"
        "ds_read_b64_tr_b16 v[32:33], v226 offset:49152\n"
        "s_lshr_b32 s96, s40, 1\n"
        "v_mfma_f32_16x16x32_bf16 v[16:19], v[48:51], v[192:195], v[16:19]\n"
        "ds_read_b64_tr_b16 v[34:35], v227 offset:49152\n"
        "s_lshl_b32 s96, s96, 10\n"
        "v_lshrrev_b32 v178, 1, v177\n"
        "v_mfma_f32_16x16x32_bf16 v[16:19], v[52:55], v[208:211], v[16:19]\n"
        "ds_read_b64_tr_b16 v[36:37], v226 offset:57344\n"
        "v_lshlrev_b32 v179, 11, v178\n"
        "v_mfma_f32_16x16x32_bf16 v[20:23], v[60:63], v[208:211], v[20:23]\n"
        "ds_read_b64_tr_b16 v[38:39], v227 offset:57344\n"
        "v_add_u32 v179, s96, v179\n"
        "v_mfma_f32_16x16x32_bf16 v[20:23], v[56:59], v[192:195], v[20:23]\n"
        "ds_read_b64_tr_b16 v[44:45], v228 offset:57344\n"
        "global_load_lds_dwordx4 v239, s[68:69] offset:1024\n"
        "s_and_b32 s96, s40, 1\n"
        "v_mfma_f32_16x16x32_bf16 v[84:87], v[56:59], v[196:199], v[84:87]\n"
        "ds_read_b64_tr_b16 v[46:47], v229 offset:57344\n"
        "s_lshl_b32 s96, s96, 6\n"
        "v_bfe_u32 v180, v254, 6, 3\n"
        "v_mfma_f32_16x16x32_bf16 v[84:87], v[60:63], v[212:215], v[84:87]\n"
        "ds_read_b64_tr_b16 v[40:41], v228 offset:49152\n"
        "v_bfe_u32 v181, v254, 4, 2\n"
        "v_mfma_f32_16x16x32_bf16 v[80:83], v[52:55], v[212:215], v[80:83]\n"
        "ds_read_b64_tr_b16 v[42:43], v229 offset:49152\n"
        "v_lshlrev_b32 v183, 2, v178\n"
        "v_mfma_f32_16x16x32_bf16 v[80:83], v[48:51], v[196:199], v[80:83]\n"
        "global_load_lds_dwordx4 v240, s[68:69] offset:2048\n"
        "v_mfma_f32_16x16x32_bf16 v[112:115], v[48:51], v[200:203], v[112:115]\n"
        "v_add_u32 v240, s78, v254\n"
        "v_xor_b32 v183, v183, v180\n"
        "v_mfma_f32_16x16x32_bf16 v[112:115], v[52:55], v[216:219], v[112:115]\n"
        "v_bfe_u32 v184, v183, 1, 2\n"
        "v_xor_b32 v184, 0, v184\n"
        "v_mfma_f32_16x16x32_bf16 v[116:119], v[60:63], v[216:219], v[116:119]\n"
        "v_lshlrev_b32 v184, 5, v184\n"
        "v_lshl_add_u32 v183, v183, 7, v184\n"
        "v_mfma_f32_16x16x32_bf16 v[116:119], v[56:59], v[200:203], v[116:119]\n"
        "global_load_lds_dwordx4 v241, s[68:69] offset:3072\n"
        "s_add_u32 m0, s72, 0x0\n"
        "v_mfma_f32_16x16x32_bf16 v[148:151], v[56:59], v[204:207], v[148:151]\n"
        "ds_read_b64_tr_b16 v[56:57], v232 offset:49152\n"
        "v_lshl_add_u32 v183, v181, 3, v183\n"
        "v_mfma_f32_16x16x32_bf16 v[148:151], v[60:63], v[220:223], v[148:151]\n"
        "ds_read_b64_tr_b16 v[60:61], v232 offset:57344\n"
        "v_add_u32 v182, v183, v179\n"
        "v_mfma_f32_16x16x32_bf16 v[144:147], v[52:55], v[220:223], v[144:147]\n"
        "ds_read_b64_tr_b16 v[52:53], v230 offset:57344\n"
        "v_xor_b32 v182, s96, v182\n"
        "v_mfma_f32_16x16x32_bf16 v[144:147], v[48:51], v[204:207], v[144:147]\n"
        "ds_read_b64_tr_b16 v[48:49], v230 offset:49152\n"
        "global_load_lds_dwordx4 v242, s[64:65] offset:0\n"
        "s_lshl_b32 s96, s41, 12\n"
        "v_mfma_f32_16x16x32_bf16 v[24:27], v[160:163], v[192:195], v[24:27]\n"
        "ds_read_b64_tr_b16 v[50:51], v231 offset:49152\n"
        "s_add_u32 s96, s96, 32768\n"
        "v_accvgpr_write_b32 a92, v182\n"
        "v_mfma_f32_16x16x32_bf16 v[24:27], v[164:167], v[208:211], v[24:27]\n"
        "ds_read_b64_tr_b16 v[54:55], v231 offset:57344\n"
        "v_bfe_u32 v188, v254, 4, 5\n"
        "v_mfma_f32_16x16x32_bf16 v[28:31], v[172:175], v[208:211], v[28:31]\n"
        "ds_read_b64_tr_b16 v[62:63], v233 offset:57344\n"
        "v_lshl_add_u32 v188, v188, 7, s96\n"
        "v_mfma_f32_16x16x32_bf16 v[28:31], v[168:171], v[192:195], v[28:31]\n"
        "ds_read_b64_tr_b16 v[58:59], v233 offset:49152\n"
        "global_load_lds_dwordx4 v243, s[64:65] offset:1024\n"
        "v_mfma_f32_16x16x32_bf16 v[92:95], v[168:171], v[196:199], v[92:95]\n"
        "ds_read_b128 v[192:195], v248 offset:2048\n"
        "v_accvgpr_write_b32 a94, v188\n"
        "v_mfma_f32_16x16x32_bf16 v[92:95], v[172:175], v[212:215], v[92:95]\n"
        "ds_read_b128 v[208:211], v249 offset:2048\n"
        "v_add_u32 v241, s79, v254\n"
        "v_mfma_f32_16x16x32_bf16 v[88:91], v[164:167], v[212:215], v[88:91]\n"
        "ds_read_b128 v[212:215], v249 offset:0\n"
        "v_mfma_f32_16x16x32_bf16 v[88:91], v[160:163], v[196:199], v[88:91]\n"
        "ds_read_b128 v[196:199], v248 offset:0\n"
        "global_load_lds_dwordx4 v244, s[64:65] offset:2048\n"
        "v_mfma_f32_16x16x32_bf16 v[120:123], v[160:163], v[200:203], v[120:123]\n"
        "v_mfma_f32_16x16x32_bf16 v[120:123], v[164:167], v[216:219], v[120:123]\n"
        "v_mfma_f32_16x16x32_bf16 v[124:127], v[172:175], v[216:219], v[124:127]\n"
        "ds_read_b128 v[216:219], v249 offset:6144\n"
        "v_mfma_f32_16x16x32_bf16 v[124:127], v[168:171], v[200:203], v[124:127]\n"
        "ds_read_b128 v[200:203], v248 offset:6144\n"
        "global_load_lds_dwordx4 v245, s[64:65] offset:3072\n"
        "s_add_u32 m0, s72, 0x1000\n"
        "v_mfma_f32_16x16x32_bf16 v[156:159], v[168:171], v[204:207], v[156:159]\n"
        "ds_read_b64_tr_b16 v[168:169], v228 offset:50176\n"
        "s_add_u32 s64, s38, s90\n"
        "s_addc_u32 s65, s39, 0\n"
        "v_mfma_f32_16x16x32_bf16 v[156:159], v[172:175], v[220:223], v[156:159]\n"
        "ds_read_b64_tr_b16 v[172:173], v228 offset:58368\n"
        "s_lshl_b32 s90, s40, 13\n"
        "s_and_b32 s90, s90, 0x4000\n"
        "v_mfma_f32_16x16x32_bf16 v[152:155], v[164:167], v[220:223], v[152:155]\n"
        "ds_read_b128 v[220:223], v249 offset:4096\n"
        "s_add_u32 s91, s78, s90\n"
        "s_add_u32 s91, s91, s80\n"
        "v_mfma_f32_16x16x32_bf16 v[152:155], v[160:163], v[204:207], v[152:155]\n"
        "ds_read_b128 v[204:207], v248 offset:4096\n"
        "s_xor_b32 s78, s78, 0x8000\n"
        "s_waitcnt lgkmcnt(6)\n"
        "v_mfma_f32_16x16x32_bf16 v[0:3], v[32:35], v[192:195], v[0:3]\n"
        "global_store_dwordx4 v254, a[172:175], s[70:71] offset:1024 sc1\n"
        "ds_read_b64_tr_b16 v[160:161], v226 offset:50176\n"
        "ds_read_b64_tr_b16 v[162:163], v227 offset:50176\n"
        "s_add_u32 s70, s38, s97\n"
        "s_addc_u32 s71, s39, 0\n"
        "v_mfma_f32_16x16x32_bf16 v[0:3], v[36:39], v[208:211], v[0:3]\n"
        "ds_read_b64_tr_b16 v[164:165], v226 offset:58368\n"
        "s_add_u32 s97, s52, 0x0\n"
        "s_add_u32 s78, s78, s72\n"
        "v_mfma_f32_16x16x32_bf16 v[4:7], v[44:47], v[208:211], v[4:7]\n"
        "ds_read_b64_tr_b16 v[166:167], v227 offset:58368\n"
        "v_mfma_f32_16x16x32_bf16 v[4:7], v[40:43], v[192:195], v[4:7]\n"
        "ds_read_b64_tr_b16 v[174:175], v229 offset:58368\n"
        "global_load_lds_dwordx4 v242, s[66:67] offset:0\n"
        "v_mfma_f32_16x16x32_bf16 v[68:71], v[40:43], v[196:199], v[68:71]\n"
        "ds_read_b64_tr_b16 v[170:171], v229 offset:50176\n"
        "v_lshrrev_b32 v242, 6, v254\n"
        "v_mfma_f32_16x16x32_bf16 v[68:71], v[44:47], v[212:215], v[68:71]\n"
        "s_waitcnt lgkmcnt(8)\n"
        "v_and_b32 v242, 12, v242\n"
        "v_xor_b32 v241, 0x8000, v241\n"
        "v_mfma_f32_16x16x32_bf16 v[64:67], v[36:39], v[212:215], v[64:67]\n"
        "v_mfma_f32_16x16x32_bf16 v[64:67], v[32:35], v[196:199], v[64:67]\n"
        "global_load_lds_dwordx4 v243, s[66:67] offset:1024\n"
        "v_mfma_f32_16x16x32_bf16 v[96:99], v[32:35], v[200:203], v[96:99]\n"
        "s_waitcnt lgkmcnt(4)\n"
        "v_bfe_u32 v243, v254, 4, 4\n"
        "v_sub_u32 v243, v243, v242\n"
        "v_mfma_f32_16x16x32_bf16 v[96:99], v[36:39], v[216:219], v[96:99]\n"
        "v_add_u32 v243, s82, v243\n"
        "v_cmp_gt_i32 s[10:11], 0, v243\n"
        "v_mfma_f32_16x16x32_bf16 v[100:103], v[44:47], v[216:219], v[100:103]\n"
        "s_mov_b32 s82, -1\n"
        "v_cmp_gt_i32 s[12:13], 1, v243\n"
        "v_cmp_gt_i32 s[14:15], 2, v243\n"
        "v_mfma_f32_16x16x32_bf16 v[100:103], v[40:43], v[200:203], v[100:103]\n"
        "global_load_lds_dwordx4 v244, s[66:67] offset:2048\n"
        "v_mfma_f32_16x16x32_bf16 v[132:135], v[40:43], v[204:207], v[132:135]\n"
        "ds_read_b64_tr_b16 v[40:41], v232 offset:50176\n"
        "v_add_u32 v244, s91, v254\n"
        "v_mfma_f32_16x16x32_bf16 v[132:135], v[44:47], v[220:223], v[132:135]\n"
        "ds_read_b64_tr_b16 v[44:45], v232 offset:58368\n"
        "s_xor_b32 s91, s79, 0x8000\n"
        "s_add_u32 s91, s91, s90\n"
        "v_mfma_f32_16x16x32_bf16 v[128:131], v[36:39], v[220:223], v[128:131]\n"
        "ds_read_b64_tr_b16 v[36:37], v230 offset:58368\n"
        "s_lshl1_add_u32 s91, s80, s91\n"
        "v_cmp_gt_i32 s[16:17], 3, v243\n"
        "v_mfma_f32_16x16x32_bf16 v[128:131], v[32:35], v[204:207], v[128:131]\n"
        "global_load_lds_dwordx4 v245, s[66:67] offset:3072\n"
        "s_add_u32 s79, s79, s72\n"
        "v_mfma_f32_16x16x32_bf16 v[8:11], v[48:51], v[192:195], v[8:11]\n"
        "ds_read_b64_tr_b16 v[32:33], v230 offset:50176\n"
        "s_add_u32 s66, s64, 0x1100000\n"
        "s_addc_u32 s67, s65, 0\n"
        "v_mfma_f32_16x16x32_bf16 v[8:11], v[52:55], v[208:211], v[8:11]\n"
        "ds_read_b64_tr_b16 v[34:35], v231 offset:50176\n"
        "s_lshl_b32 s80, s41, 5\n"
        "v_add_u32 v245, s91, v254\n"
        "v_mfma_f32_16x16x32_bf16 v[12:15], v[60:63], v[208:211], v[12:15]\n"
        "ds_read_b64_tr_b16 v[38:39], v231 offset:58368\n"
        "s_add_u32 s80, s80, 256\n"
        "v_cmp_gt_i32 s[20:21], 16, v243\n"
        "v_mfma_f32_16x16x32_bf16 v[12:15], v[56:59], v[192:195], v[12:15]\n"
        "ds_read_b64_tr_b16 v[46:47], v233 offset:58368\n"
        "s_add_u32 s81, s80, s42\n"
        "v_cmp_gt_i32 s[22:23], 17, v243\n"
        "v_mfma_f32_16x16x32_bf16 v[76:79], v[56:59], v[196:199], v[76:79]\n"
        "ds_read_b64_tr_b16 v[42:43], v233 offset:50176\n"
        "s_lshl_b32 s81, s81, 7\n"
        "v_cmp_gt_i32 s[24:25], 18, v243\n"
        "v_mfma_f32_16x16x32_bf16 v[76:79], v[60:63], v[212:215], v[76:79]\n"
        "global_store_dwordx4 v254, a[0:3], s[70:71] offset:0 sc1\n"
        "s_and_b32 s80, s81, 0xfffffc7f\n"
        "v_cmp_gt_i32 s[28:29], 19, v243\n"
        "v_cmp_gt_i32 s[30:31], 32, v243\n"
        "v_cmp_gt_i32 s[56:57], 33, v243\n"
        "v_cmp_gt_i32 s[58:59], 34, v243\n"
        "v_mfma_f32_16x16x32_bf16 v[72:75], v[52:55], v[212:215], v[72:75]\n"
        "s_xor_b32 s79, s79, 0x8000\n"
        "v_cmp_gt_i32 s[60:61], 35, v243\n"
        "v_cmp_gt_i32 s[62:63], 48, v243\n"
        "v_mfma_f32_16x16x32_bf16 v[72:75], v[48:51], v[196:199], v[72:75]\n"
        "v_cmp_gt_i32 s[84:85], 49, v243\n"
        "v_cmp_gt_i32 s[86:87], 50, v243\n"
        "v_mfma_f32_16x16x32_bf16 v[104:107], v[48:51], v[200:203], v[104:107]\n"
        "v_cmp_gt_i32 s[88:89], 51, v243\n"
        "v_mov_b32 v242, 0x3f803f80\n"
        "v_mfma_f32_16x16x32_bf16 v[104:107], v[52:55], v[216:219], v[104:107]\n"
        "v_accvgpr_write_b32 a80, v242\n"
        "v_accvgpr_write_b32 a81, v242\n"
        "v_mfma_f32_16x16x32_bf16 v[108:111], v[60:63], v[216:219], v[108:111]\n"
        "v_accvgpr_write_b32 a82, v242\n"
        "v_accvgpr_write_b32 a83, v242\n"
        "v_mfma_f32_16x16x32_bf16 v[108:111], v[56:59], v[200:203], v[108:111]\n"
        "s_waitcnt vmcnt(14) lgkmcnt(0)\n"
        "v_cndmask_b32 v186, 0, v242, vcc\n"
        "v_mfma_f32_16x16x32_bf16 v[140:143], v[56:59], v[204:207], v[140:143]\n"
        "s_barrier\n"
        "v_mfma_f32_16x16x32_bf16 v[140:143], v[60:63], v[220:223], v[140:143]\n"
        "ds_read_b64_tr_b16 v[60:61], v228 offset:24576\n"
        "v_accvgpr_write_b32 a72, v186\n"
        "v_mfma_f32_16x16x32_bf16 v[136:139], v[52:55], v[220:223], v[136:139]\n"
        "ds_read_b64_tr_b16 v[52:53], v226 offset:24576\n"
        "v_accvgpr_write_b32 a73, v186\n"
        "v_mfma_f32_16x16x32_bf16 v[136:139], v[48:51], v[204:207], v[136:139]\n"
        "ds_read_b64_tr_b16 v[48:49], v226 offset:16384\n"
        "v_accvgpr_write_b32 a74, v186\n"
        "v_mfma_f32_16x16x32_bf16 v[16:19], v[160:163], v[192:195], v[16:19]\n"
        "ds_read_b64_tr_b16 v[50:51], v227 offset:16384\n"
        "v_accvgpr_write_b32 a75, v186\n"
        "v_mfma_f32_16x16x32_bf16 v[16:19], v[164:167], v[208:211], v[16:19]\n"
        "ds_read_b64_tr_b16 v[54:55], v227 offset:24576\n"
        "v_cmp_eq_u32 vcc, 0, v187\n"
        "v_mfma_f32_16x16x32_bf16 v[20:23], v[172:175], v[208:211], v[20:23]\n"
        "ds_read_b64_tr_b16 v[62:63], v229 offset:24576\n"
        "v_cndmask_b32 v187, 0, v242, vcc\n"
        "v_mfma_f32_16x16x32_bf16 v[20:23], v[168:171], v[192:195], v[20:23]\n"
        "ds_read_b64_tr_b16 v[56:57], v228 offset:16384\n"
        "v_accvgpr_write_b32 a76, v187\n"
        "v_mfma_f32_16x16x32_bf16 v[84:87], v[168:171], v[196:199], v[84:87]\n"
        "ds_read_b64_tr_b16 v[58:59], v229 offset:16384\n"
        "v_accvgpr_write_b32 a77, v187\n"
        "v_mfma_f32_16x16x32_bf16 v[84:87], v[172:175], v[212:215], v[84:87]\n"
        "global_store_dwordx4 v254, a[4:7], s[70:71] offset:1024 sc1\n"
        "s_add_u32 s70, s38, s97\n"
        "s_addc_u32 s71, s39, 0\n"
        "s_add_u32 s97, s53, 0x2000\n"
        "v_accvgpr_write_b32 a78, v187\n"
        "v_accvgpr_write_b32 a79, v187\n"
        "v_mfma_f32_16x16x32_bf16 v[80:83], v[164:167], v[212:215], v[80:83]\n"
        "v_mov_b32 v242, 0xff800000\n"
        "v_mfma_f32_16x16x32_bf16 v[80:83], v[160:163], v[196:199], v[80:83]\n"
        "v_mfma_f32_16x16x32_bf16 v[112:115], v[160:163], v[200:203], v[112:115]\n"
        "v_mfma_f32_16x16x32_bf16 v[112:115], v[164:167], v[216:219], v[112:115]\n"
        "v_mfma_f32_16x16x32_bf16 v[116:119], v[172:175], v[216:219], v[116:119]\n"
        "v_mfma_f32_16x16x32_bf16 v[116:119], v[168:171], v[200:203], v[116:119]\n"
        "v_mfma_f32_16x16x32_bf16 v[148:151], v[168:171], v[204:207], v[148:151]\n"
        "ds_read_b64_tr_b16 v[168:169], v232 offset:16384\n"
        "v_mfma_f32_16x16x32_bf16 v[148:151], v[172:175], v[220:223], v[148:151]\n"
        "ds_read_b64_tr_b16 v[172:173], v232 offset:24576\n"
        "v_mfma_f32_16x16x32_bf16 v[144:147], v[164:167], v[220:223], v[144:147]\n"
        "ds_read_b64_tr_b16 v[164:165], v230 offset:24576\n"
        "v_mfma_f32_16x16x32_bf16 v[144:147], v[160:163], v[204:207], v[144:147]\n"
        "global_store_dwordx4 v254, a[8:11], s[70:71] offset:0 sc1\n"
        "ds_read_b64_tr_b16 v[160:161], v230 offset:16384\n"
        "ds_read_b64_tr_b16 v[162:163], v231 offset:16384\n"
        "v_mfma_f32_16x16x32_bf16 v[24:27], v[32:35], v[192:195], v[24:27]\n"
        "ds_read_b64_tr_b16 v[166:167], v231 offset:24576\n"
        "v_mfma_f32_16x16x32_bf16 v[24:27], v[36:39], v[208:211], v[24:27]\n"
        "ds_read_b64_tr_b16 v[174:175], v233 offset:24576\n"
        "v_mfma_f32_16x16x32_bf16 v[28:31], v[44:47], v[208:211], v[28:31]\n"
        "ds_read_b64_tr_b16 v[170:171], v233 offset:16384\n"
        "v_mfma_f32_16x16x32_bf16 v[28:31], v[40:43], v[192:195], v[28:31]\n"
        "ds_read_b128 v[192:195], v246 offset:34816\n"
        "v_mfma_f32_16x16x32_bf16 v[92:95], v[40:43], v[196:199], v[92:95]\n"
        "ds_read_b128 v[208:211], v247 offset:34816\n"
        "v_mfma_f32_16x16x32_bf16 v[92:95], v[44:47], v[212:215], v[92:95]\n"
        "v_mfma_f32_16x16x32_bf16 v[88:91], v[36:39], v[212:215], v[88:91]\n"
        "ds_read_b128 v[212:215], v247 offset:32768\n"
        "v_mfma_f32_16x16x32_bf16 v[88:91], v[32:35], v[196:199], v[88:91]\n"
        "ds_read_b128 v[196:199], v246 offset:32768\n"
        "v_mfma_f32_16x16x32_bf16 v[120:123], v[32:35], v[200:203], v[120:123]\n"
        "v_mfma_f32_16x16x32_bf16 v[120:123], v[36:39], v[216:219], v[120:123]\n"
        "v_mfma_f32_16x16x32_bf16 v[124:127], v[44:47], v[216:219], v[124:127]\n"
        "ds_read_b128 v[216:219], v247 offset:38912\n"
        "v_mfma_f32_16x16x32_bf16 v[124:127], v[40:43], v[200:203], v[124:127]\n"
        "ds_read_b128 v[200:203], v246 offset:38912\n"
        "v_mfma_f32_16x16x32_bf16 v[156:159], v[40:43], v[204:207], v[156:159]\n"
        "ds_read_b64_tr_b16 v[40:41], v228 offset:17408\n"
        "s_waitcnt lgkmcnt(5)\n"
        "v_mfma_f32_16x16x32_bf16 v[156:159], v[44:47], v[220:223], v[156:159]\n"
        "ds_read_b64_tr_b16 v[44:45], v228 offset:25600\n"
        "v_mfma_f32_16x16x32_bf16 v[152:155], v[36:39], v[220:223], v[152:155]\n"
        "ds_read_b128 v[220:223], v247 offset:36864\n"
        "v_mfma_f32_16x16x32_bf16 v[152:155], v[32:35], v[204:207], v[152:155]\n"
        "ds_read_b128 v[204:207], v246 offset:36864\n"
        "v_mfma_f32_16x16x32_bf16 v[0:3], v[48:51], v[192:195], v[0:3]\n"
        "global_store_dwordx4 v254, a[12:15], s[70:71] offset:1024 sc1\n"
        "ds_read_b64_tr_b16 v[32:33], v226 offset:17408\n"
        "ds_read_b64_tr_b16 v[34:35], v227 offset:17408\n"
        "s_add_u32 s70, s38, s97\n"
        "s_addc_u32 s71, s39, 0\n"
        "v_mfma_f32_16x16x32_bf16 v[0:3], v[52:55], v[208:211], v[0:3]\n"
        "ds_read_b64_tr_b16 v[36:37], v226 offset:25600\n"
        "s_add_u32 s97, s53, 0x0\n"
        "s_waitcnt lgkmcnt(8)\n"
        "v_mfma_f32_16x16x32_bf16 v[4:7], v[60:63], v[208:211], v[4:7]\n"
        "ds_read_b64_tr_b16 v[38:39], v227 offset:25600\n"
        "v_mfma_f32_16x16x32_bf16 v[4:7], v[56:59], v[192:195], v[4:7]\n"
        "ds_read_b64_tr_b16 v[46:47], v229 offset:25600\n"
        "v_mfma_f32_16x16x32_bf16 v[68:71], v[56:59], v[196:199], v[68:71]\n"
        "ds_read_b64_tr_b16 v[42:43], v229 offset:17408\n"
        "s_waitcnt lgkmcnt(8)\n"
        "v_mfma_f32_16x16x32_bf16 v[68:71], v[60:63], v[212:215], v[68:71]\n"
        "v_mfma_f32_16x16x32_bf16 v[64:67], v[52:55], v[212:215], v[64:67]\n"
        "v_mfma_f32_16x16x32_bf16 v[64:67], v[48:51], v[196:199], v[64:67]\n"
        "v_mfma_f32_16x16x32_bf16 v[96:99], v[48:51], v[200:203], v[96:99]\n"
        "s_waitcnt lgkmcnt(4)\n"
        "v_mfma_f32_16x16x32_bf16 v[96:99], v[52:55], v[216:219], v[96:99]\n"
        "v_mfma_f32_16x16x32_bf16 v[100:103], v[60:63], v[216:219], v[100:103]\n"
        "v_mfma_f32_16x16x32_bf16 v[100:103], v[56:59], v[200:203], v[100:103]\n"
        "global_store_dwordx4 v254, a[208:211], s[70:71] offset:0 sc1\n"
        "v_mfma_f32_16x16x32_bf16 v[132:135], v[56:59], v[204:207], v[132:135]\n"
        "ds_read_b64_tr_b16 v[56:57], v232 offset:17408\n"
        "v_mfma_f32_16x16x32_bf16 v[132:135], v[60:63], v[220:223], v[132:135]\n"
        "ds_read_b64_tr_b16 v[60:61], v232 offset:25600\n"
        "v_mfma_f32_16x16x32_bf16 v[128:131], v[52:55], v[220:223], v[128:131]\n"
        "ds_read_b64_tr_b16 v[52:53], v230 offset:25600\n"
        "v_mfma_f32_16x16x32_bf16 v[128:131], v[48:51], v[204:207], v[128:131]\n"
        "ds_read_b64_tr_b16 v[48:49], v230 offset:17408\n"
        "v_mfma_f32_16x16x32_bf16 v[8:11], v[160:163], v[192:195], v[8:11]\n"
        "ds_read_b64_tr_b16 v[50:51], v231 offset:17408\n"
        "v_mfma_f32_16x16x32_bf16 v[8:11], v[164:167], v[208:211], v[8:11]\n"
        "ds_read_b64_tr_b16 v[54:55], v231 offset:25600\n"
        "v_mfma_f32_16x16x32_bf16 v[12:15], v[172:175], v[208:211], v[12:15]\n"
        "ds_read_b64_tr_b16 v[62:63], v233 offset:25600\n"
        "v_mfma_f32_16x16x32_bf16 v[12:15], v[168:171], v[192:195], v[12:15]\n"
        "ds_read_b64_tr_b16 v[58:59], v233 offset:17408\n"
        "v_mfma_f32_16x16x32_bf16 v[76:79], v[168:171], v[196:199], v[76:79]\n"
        "v_mfma_f32_16x16x32_bf16 v[76:79], v[172:175], v[212:215], v[76:79]\n"
        "v_mfma_f32_16x16x32_bf16 v[72:75], v[164:167], v[212:215], v[72:75]\n"
        "v_mfma_f32_16x16x32_bf16 v[72:75], v[160:163], v[196:199], v[72:75]\n"
        "v_mfma_f32_16x16x32_bf16 v[104:107], v[160:163], v[200:203], v[104:107]\n"
        "v_mfma_f32_16x16x32_bf16 v[104:107], v[164:167], v[216:219], v[104:107]\n"
        "v_mfma_f32_16x16x32_bf16 v[108:111], v[172:175], v[216:219], v[108:111]\n"
        "v_mfma_f32_16x16x32_bf16 v[108:111], v[168:171], v[200:203], v[108:111]\n"
        "global_store_dwordx4 v254, a[240:243], s[70:71] offset:1024 sc1\n"
        "s_waitcnt vmcnt(6) lgkmcnt(0)\n"
        "s_barrier\n"
        "v_mfma_f32_16x16x32_bf16 v[140:143], v[168:171], v[204:207], v[140:143]\n"
        "ds_write_b128 v244, a[200:203] offset:0\n"
        "v_mfma_f32_16x16x32_bf16 v[140:143], v[172:175], v[220:223], v[140:143]\n"
        "ds_read_b64_tr_b16 v[172:173], v228 offset:40960\n"
        "v_mfma_f32_16x16x32_bf16 v[136:139], v[164:167], v[220:223], v[136:139]\n"
        "ds_read_b64_tr_b16 v[164:165], v226 offset:40960\n"
        "v_mfma_f32_16x16x32_bf16 v[136:139], v[160:163], v[204:207], v[136:139]\n"
        "ds_write_b128 v244, a[204:207] offset:1024\n"
        "v_mfma_f32_16x16x32_bf16 v[16:19], v[32:35], v[192:195], v[16:19]\n"
        "ds_read_b64_tr_b16 v[160:161], v226 offset:32768\n"
        "global_load_dword v253, v252, s[46:47] offset:0\n"
        "v_mfma_f32_16x16x32_bf16 v[16:19], v[36:39], v[208:211], v[16:19]\n"
        "ds_read_b64_tr_b16 v[162:163], v227 offset:32768\n"
        "v_mfma_f32_16x16x32_bf16 v[20:23], v[44:47], v[208:211], v[20:23]\n"
        "ds_write_b128 v244, a[160:163] offset:4096\n"
        "v_mfma_f32_16x16x32_bf16 v[20:23], v[40:43], v[192:195], v[20:23]\n"
        "ds_read_b64_tr_b16 v[166:167], v227 offset:40960\n"
        "v_mfma_f32_16x16x32_bf16 v[84:87], v[40:43], v[196:199], v[84:87]\n"
        "ds_read_b64_tr_b16 v[174:175], v229 offset:40960\n"
        "v_mfma_f32_16x16x32_bf16 v[84:87], v[44:47], v[212:215], v[84:87]\n"
        "ds_write_b128 v244, a[164:167] offset:5120\n"
        "v_mfma_f32_16x16x32_bf16 v[80:83], v[36:39], v[212:215], v[80:83]\n"
        "ds_read_b64_tr_b16 v[168:169], v228 offset:32768\n"
        "v_mfma_f32_16x16x32_bf16 v[80:83], v[32:35], v[196:199], v[80:83]\n"
        "ds_read_b64_tr_b16 v[170:171], v229 offset:32768\n"
        "v_mfma_f32_16x16x32_bf16 v[112:115], v[32:35], v[200:203], v[112:115]\n"
        "ds_write_b128 v244, a[168:171] offset:8192\n"
        "v_mfma_f32_16x16x32_bf16 v[112:115], v[36:39], v[216:219], v[112:115]\n"
        "global_store_dwordx4 v254, a[212:215], s[70:71] offset:2048 sc1\n"
        "v_mfma_f32_16x16x32_bf16 v[116:119], v[44:47], v[216:219], v[116:119]\n"
        "v_mfma_f32_16x16x32_bf16 v[116:119], v[40:43], v[200:203], v[116:119]\n"
        "ds_write_b128 v244, a[172:175] offset:9216\n"
        "v_mfma_f32_16x16x32_bf16 v[148:151], v[40:43], v[204:207], v[148:151]\n"
        "ds_read_b64_tr_b16 v[40:41], v232 offset:32768\n"
        "v_mfma_f32_16x16x32_bf16 v[148:151], v[44:47], v[220:223], v[148:151]\n"
        "ds_read_b64_tr_b16 v[44:45], v232 offset:40960\n"
        "v_mfma_f32_16x16x32_bf16 v[144:147], v[36:39], v[220:223], v[144:147]\n"
        "ds_write_b128 v244, a[0:3] offset:12288\n"
        "v_mfma_f32_16x16x32_bf16 v[144:147], v[32:35], v[204:207], v[144:147]\n"
        "ds_read_b64_tr_b16 v[32:33], v230 offset:32768\n"
        "v_mfma_f32_16x16x32_bf16 v[24:27], v[48:51], v[192:195], v[24:27]\n"
        "ds_read_b64_tr_b16 v[34:35], v231 offset:32768\n"
        "v_mfma_f32_16x16x32_bf16 v[24:27], v[52:55], v[208:211], v[24:27]\n"
        "ds_write_b128 v244, a[4:7] offset:13312\n"
        "v_mfma_f32_16x16x32_bf16 v[28:31], v[60:63], v[208:211], v[28:31]\n"
        "ds_read_b64_tr_b16 v[36:37], v230 offset:40960\n"
        "v_xor_b32 v244, 0xc000, v244\n"
        "v_mfma_f32_16x16x32_bf16 v[28:31], v[56:59], v[192:195], v[28:31]\n"
        "ds_read_b64_tr_b16 v[38:39], v231 offset:40960\n"
        "v_mfma_f32_16x16x32_bf16 v[92:95], v[56:59], v[196:199], v[92:95]\n"
        "ds_read_b64_tr_b16 v[46:47], v233 offset:40960\n"
        "v_mfma_f32_16x16x32_bf16 v[92:95], v[60:63], v[212:215], v[92:95]\n"
        "ds_read_b64_tr_b16 v[42:43], v233 offset:32768\n"
        "v_mfma_f32_16x16x32_bf16 v[88:91], v[52:55], v[212:215], v[88:91]\n"
        "ds_read_b128 v[192:195], v246 offset:2048\n"
        "v_mfma_f32_16x16x32_bf16 v[88:91], v[48:51], v[196:199], v[88:91]\n"
        "ds_read_b128 v[208:211], v247 offset:2048\n"
        "v_mfma_f32_16x16x32_bf16 v[120:123], v[48:51], v[200:203], v[120:123]\n"
        "ds_read_b128 v[196:199], v246 offset:0\n"
        "v_mfma_f32_16x16x32_bf16 v[120:123], v[52:55], v[216:219], v[120:123]\n"
        "ds_read_b128 v[212:215], v247 offset:0\n"
        "v_mfma_f32_16x16x32_bf16 v[124:127], v[60:63], v[216:219], v[124:127]\n"
        "global_store_dwordx4 v254, a[244:247], s[70:71] offset:3072 sc1\n"
        "ds_read_b128 v[216:219], v247 offset:6144\n"
        "s_add_u32 s70, s38, s97\n"
        "s_addc_u32 s71, s39, 0\n"
        "s_add_u32 s97, s53, 0x0\n"
        "s_add_u32 s97, s97, s75\n"
        "v_mfma_f32_16x16x32_bf16 v[124:127], v[56:59], v[200:203], v[124:127]\n"
        "ds_read_b128 v[200:203], v246 offset:6144\n"
        "v_mfma_f32_16x16x32_bf16 v[156:159], v[56:59], v[204:207], v[156:159]\n"
        "ds_read_b64_tr_b16 v[56:57], v228 offset:33792\n"
        "v_mfma_f32_16x16x32_bf16 v[156:159], v[60:63], v[220:223], v[156:159]\n"
        "ds_read_b64_tr_b16 v[60:61], v228 offset:41984\n"
        "s_waitcnt lgkmcnt(7)\n"
        "v_mfma_f32_16x16x32_bf16 v[152:155], v[52:55], v[220:223], v[152:155]\n"
        "ds_read_b128 v[220:223], v247 offset:4096\n"
        "s_waitcnt lgkmcnt(7)\n"
        "v_mfma_f32_16x16x32_bf16 v[152:155], v[48:51], v[204:207], v[152:155]\n"
        "ds_read_b128 v[204:207], v246 offset:4096\n"
        "v_mfma_f32_16x16x32_bf16 v[0:3], v[160:163], v[192:195], v[0:3]\n"
        "ds_read_b64_tr_b16 v[48:49], v226 offset:33792\n"
        "s_waitcnt lgkmcnt(7)\n"
        "v_mfma_f32_16x16x32_bf16 v[0:3], v[164:167], v[208:211], v[0:3]\n"
        "LHA9_%=:\n"
        "s_cbranch_execz LHRA_%=\n"
        "ds_read_b64_tr_b16 v[50:51], v227 offset:33792\n"
        "v_mfma_f32_16x16x32_bf16 v[4:7], v[172:175], v[208:211], v[4:7]\n"
        "ds_read_b64_tr_b16 v[52:53], v226 offset:41984\n"
        "v_mfma_f32_16x16x32_bf16 v[4:7], v[168:171], v[192:195], v[4:7]\n"
        "ds_read_b64_tr_b16 v[54:55], v227 offset:41984\n"
        "v_mfma_f32_16x16x32_bf16 v[68:71], v[168:171], v[196:199], v[68:71]\n"
        "ds_read_b64_tr_b16 v[62:63], v229 offset:41984\n"
        "s_waitcnt lgkmcnt(8)\n"
        "v_mfma_f32_16x16x32_bf16 v[68:71], v[172:175], v[212:215], v[68:71]\n"
        "ds_read_b64_tr_b16 v[58:59], v229 offset:33792\n"
        "v_cvt_pk_bf16_f32 v0, v0, v1\n"
        "v_mfma_f32_16x16x32_bf16 v[64:67], v[164:167], v[212:215], v[64:67]\n"
        "ds_write_b128 v244, a[8:11] offset:0\n"
        "v_mfma_f32_16x16x32_bf16 v[64:67], v[160:163], v[196:199], v[64:67]\n"
        "global_store_dwordx4 v254, a[216:219], s[70:71] offset:0 sc1\n"
        "v_cvt_pk_bf16_f32 v1, v2, v3\n"
        "v_cvt_pk_bf16_f32 v2, v4, v5\n"
        "v_cvt_pk_bf16_f32 v3, v6, v7\n"
        "v_mfma_f32_16x16x32_bf16 v[96:99], v[160:163], v[200:203], v[96:99]\n"
        "s_waitcnt lgkmcnt(5)\n"
        "v_mfma_f32_16x16x32_bf16 v[96:99], v[164:167], v[216:219], v[96:99]\n"
        "ds_write_b128 v244, a[12:15] offset:1024\n"
        "v_mfma_f32_16x16x32_bf16 v[100:103], v[172:175], v[216:219], v[100:103]\n"
        "v_cvt_pk_bf16_f32 v64, v64, v65\n"
        "v_cvt_pk_bf16_f32 v65, v66, v67\n"
        "v_mfma_f32_16x16x32_bf16 v[100:103], v[168:171], v[200:203], v[100:103]\n"
        "v_cvt_pk_bf16_f32 v66, v68, v69\n"
        "v_cvt_pk_bf16_f32 v67, v70, v71\n"
        "v_mfma_f32_16x16x32_bf16 v[132:135], v[168:171], v[204:207], v[132:135]\n"
        "ds_write_b128 v244, a[192:195] offset:4096\n"
        "v_mfma_f32_16x16x32_bf16 v[132:135], v[172:175], v[220:223], v[132:135]\n"
        "ds_read_b64_tr_b16 v[172:173], v232 offset:41984\n"
        "v_cvt_pk_bf16_f32 v96, v96, v97\n"
        "v_mfma_f32_16x16x32_bf16 v[128:131], v[164:167], v[220:223], v[128:131]\n"
        "ds_read_b64_tr_b16 v[164:165], v230 offset:41984\n"
        "v_cvt_pk_bf16_f32 v97, v98, v99\n"
        "v_mfma_f32_16x16x32_bf16 v[128:131], v[160:163], v[204:207], v[128:131]\n"
        "ds_write_b128 v244, a[196:199] offset:5120\n"
        "v_mfma_f32_16x16x32_bf16 v[8:11], v[32:35], v[192:195], v[8:11]\n"
        "ds_read_b64_tr_b16 v[160:161], v230 offset:33792\n"
        "v_cvt_pk_bf16_f32 v98, v100, v101\n"
        "v_mfma_f32_16x16x32_bf16 v[8:11], v[36:39], v[208:211], v[8:11]\n"
        "ds_read_b64_tr_b16 v[162:163], v231 offset:33792\n"
        "v_cvt_pk_bf16_f32 v99, v102, v103\n"
        "v_mfma_f32_16x16x32_bf16 v[12:15], v[44:47], v[208:211], v[12:15]\n"
        "ds_read_b64_tr_b16 v[166:167], v231 offset:41984\n"
        "v_cvt_pk_bf16_f32 v128, v128, v129\n"
        "v_mfma_f32_16x16x32_bf16 v[12:15], v[40:43], v[192:195], v[12:15]\n"
        "ds_read_b64_tr_b16 v[174:175], v233 offset:41984\n"
        "v_cvt_pk_bf16_f32 v129, v130, v131\n"
        "v_mfma_f32_16x16x32_bf16 v[76:79], v[40:43], v[196:199], v[76:79]\n"
        "ds_read_b64_tr_b16 v[168:169], v232 offset:33792\n"
        "v_cvt_pk_bf16_f32 v4, v8, v9\n"
        "v_mfma_f32_16x16x32_bf16 v[76:79], v[44:47], v[212:215], v[76:79]\n"
        "ds_read_b64_tr_b16 v[170:171], v233 offset:33792\n"
        "v_cvt_pk_bf16_f32 v5, v10, v11\n"
        "v_mfma_f32_16x16x32_bf16 v[72:75], v[36:39], v[212:215], v[72:75]\n"
        "ds_write_b128 v244, a[232:235] offset:8192\n"
        "v_mfma_f32_16x16x32_bf16 v[72:75], v[32:35], v[196:199], v[72:75]\n"
        "global_store_dwordx4 v254, a[248:251], s[70:71] offset:1024 sc1\n"
        "s_add_u32 s82, s82, 1\n"
        "v_cvt_pk_bf16_f32 v6, v12, v13\n"
        "v_cvt_pk_bf16_f32 v7, v14, v15\n"
        "v_cvt_pk_bf16_f32 v70, v76, v77\n"
        "v_cvt_pk_bf16_f32 v71, v78, v79\n"
        "v_mfma_f32_16x16x32_bf16 v[104:107], v[32:35], v[200:203], v[104:107]\n"
        "s_cmp_ge_u32 s82, s44\n"
        "s_addc_u32 s90, s82, 0\n"
        "v_cvt_pk_bf16_f32 v68, v72, v73\n"
        "v_mfma_f32_16x16x32_bf16 v[104:107], v[36:39], v[216:219], v[104:107]\n"
        "ds_write_b128 v244, a[236:239] offset:9216\n"
        "v_mfma_f32_16x16x32_bf16 v[108:111], v[44:47], v[216:219], v[108:111]\n"
        "s_lshl_b32 s90, s90, 15\n"
        "v_cvt_pk_bf16_f32 v69, v74, v75\n"
        "v_cvt_pk_bf16_f32 v131, v134, v135\n"
        "v_mfma_f32_16x16x32_bf16 v[108:111], v[40:43], v[200:203], v[108:111]\n"
        "s_waitcnt lgkmcnt(2)\n"
        "v_cvt_pk_bf16_f32 v130, v132, v133\n"
        "v_cvt_pk_bf16_f32 v100, v104, v105\n"
        "v_mfma_f32_16x16x32_bf16 v[140:143], v[40:43], v[204:207], v[140:143]\n"
        "ds_write_b128 v244, a[224:227] offset:12288\n"
        "v_mfma_f32_16x16x32_bf16 v[140:143], v[44:47], v[220:223], v[140:143]\n"
        "s_barrier\n"
        "v_mfma_f32_16x16x32_bf16 v[136:139], v[36:39], v[220:223], v[136:139]\n"
        "s_add_u32 s68, s64, s90\n"
        "s_addc_u32 s69, s65, 0\n"
        "v_cndmask_b32 v36, 0, v242, s[10:11]\n"
        "v_mfma_f32_16x16x32_bf16 v[136:139], v[32:35], v[204:207], v[136:139]\n"
        "ds_write_b128 v244, a[228:231] offset:13312\n"
        "v_mfma_f32_16x16x32_bf16 v[16:19], v[48:51], v[192:195], v[16:19]\n"
        "global_store_dwordx4 v254, a[220:223], s[70:71] offset:2048 sc1\n"
        "s_xor_b32 s78, s78, 0x8000\n"
        "v_cndmask_b32 v37, 0, v242, s[12:13]\n"
        "v_cndmask_b32 v38, 0, v242, s[14:15]\n"
        "v_cndmask_b32 v39, 0, v242, s[16:17]\n"
        "v_cvt_pk_bf16_f32 v134, v140, v141\n"
        "v_mfma_f32_16x16x32_bf16 v[16:19], v[52:55], v[208:211], v[16:19]\n"
        "s_lshr_b32 s52, s41, 1\n"
        "v_cvt_pk_bf16_f32 v101, v106, v107\n"
        "v_cvt_pk_bf16_f32 v102, v108, v109\n"
        "v_mfma_f32_16x16x32_bf16 v[20:23], v[60:63], v[208:211], v[20:23]\n"
        "ds_write_b128 v245, a[176:179] offset:0\n"
        "v_mfma_f32_16x16x32_bf16 v[20:23], v[56:59], v[192:195], v[20:23]\n"
        "v_cvt_pk_bf16_f32 v103, v110, v111\n"
        "s_nop 0\n"
        "v_cvt_pk_bf16_f32 v8, v16, v17\n"
        "v_mfma_f32_16x16x32_bf16 v[84:87], v[56:59], v[196:199], v[84:87]\n"
        "s_barrier\n"
        "v_mfma_f32_16x16x32_bf16 v[84:87], v[60:63], v[212:215], v[84:87]\n"
        "ds_read_b128 a[96:99], v240 offset:0\n"
        "s_lshl_b32 s52, s52, 11\n"
        "v_cvt_pk_bf16_f32 v9, v18, v19\n"
        "v_mfma_f32_16x16x32_bf16 v[80:83], v[52:55], v[212:215], v[80:83]\n"
        "ds_read_b128 a[100:103], v240 offset:1024\n"
        "v_cvt_pk_bf16_f32 v10, v20, v21\n"
        "v_mfma_f32_16x16x32_bf16 v[80:83], v[48:51], v[196:199], v[80:83]\n"
        "ds_read_b128 a[104:107], v240 offset:2048\n"
        "v_cvt_pk_bf16_f32 v11, v22, v23\n"
        "v_mfma_f32_16x16x32_bf16 v[112:115], v[48:51], v[200:203], v[112:115]\n"
        "ds_read_b128 a[108:111], v240 offset:3072\n"
        "v_cvt_pk_bf16_f32 v74, v84, v85\n"
        "v_mfma_f32_16x16x32_bf16 v[112:115], v[52:55], v[216:219], v[112:115]\n"
        "ds_write_b128 v245, a[180:183] offset:1024\n"
        "v_mfma_f32_16x16x32_bf16 v[116:119], v[60:63], v[216:219], v[116:119]\n"
        "ds_read_b128 a[112:115], v240 offset:4096\n"
        "v_cvt_pk_bf16_f32 v72, v80, v81\n"
        "v_mfma_f32_16x16x32_bf16 v[116:119], v[56:59], v[200:203], v[116:119]\n"
        "ds_read_b128 a[116:119], v240 offset:5120\n"
        "v_cvt_pk_bf16_f32 v73, v82, v83\n"
        "v_mfma_f32_16x16x32_bf16 v[148:151], v[56:59], v[204:207], v[148:151]\n"
        "ds_read_b128 a[120:123], v240 offset:6144\n"
        "v_cvt_pk_bf16_f32 v75, v86, v87\n"
        "v_mfma_f32_16x16x32_bf16 v[148:151], v[60:63], v[220:223], v[148:151]\n"
        "ds_write_b128 v245, a[184:187] offset:2048\n"
        "v_mfma_f32_16x16x32_bf16 v[144:147], v[52:55], v[220:223], v[144:147]\n"
        "ds_read_b128 a[124:127], v240 offset:7168\n"
        "v_cndmask_b32 v52, 0, v242, s[20:21]\n"
        "v_mfma_f32_16x16x32_bf16 v[144:147], v[48:51], v[204:207], v[144:147]\n"
        "global_store_dwordx4 v254, a[252:255], s[70:71] offset:3072 sc1\n"
        "s_add_u32 s70, s38, s97\n"
        "s_addc_u32 s71, s39, 0\n"
        "s_add_u32 s97, s53, 0x2000\n"
        "v_cndmask_b32 v53, 0, v242, s[22:23]\n"
        "v_cndmask_b32 v54, 0, v242, s[24:25]\n"
        "v_mfma_f32_16x16x32_bf16 v[24:27], v[160:163], v[192:195], v[24:27]\n"
        "ds_write_b128 v245, a[188:191] offset:3072\n"
        "v_mfma_f32_16x16x32_bf16 v[24:27], v[164:167], v[208:211], v[24:27]\n"
        "s_add_u32 s97, s97, s75\n"
        "v_cndmask_b32 v55, 0, v242, s[28:29]\n"
        "v_cvt_pk_bf16_f32 v104, v112, v113\n"
        "v_mfma_f32_16x16x32_bf16 v[28:31], v[172:175], v[208:211], v[28:31]\n"
        "global_store_dwordx4 v254, a[180:183], s[70:71] offset:1024 sc1\n"
        "s_add_u32 s74, s68, 0x1000\n"
        "s_addc_u32 s75, s69, 0\n"
        "s_lshl2_add_u32 s53, s42, s40\n"
        "v_cvt_pk_bf16_f32 v105, v114, v115\n"
        "v_cvt_pk_bf16_f32 v12, v24, v25\n"
        "v_mfma_f32_16x16x32_bf16 v[28:31], v[168:171], v[192:195], v[28:31]\n"
        "ds_write_b128 v245, a[16:19] offset:8192\n"
        "v_mfma_f32_16x16x32_bf16 v[92:95], v[168:171], v[196:199], v[92:95]\n"
        "s_add_u32 s52, s52, s53\n"
        "v_cvt_pk_bf16_f32 v13, v26, v27\n"
        "v_cvt_pk_bf16_f32 v106, v116, v117\n"
        "v_mfma_f32_16x16x32_bf16 v[92:95], v[172:175], v[212:215], v[92:95]\n"
        "s_waitcnt lgkmcnt(2)\n"
        "v_cvt_pk_bf16_f32 v107, v118, v119\n"
        "v_cvt_pk_bf16_f32 v14, v28, v29\n"
        "v_mfma_f32_16x16x32_bf16 v[88:91], v[164:167], v[212:215], v[88:91]\n"
        "ds_write_b128 v245, a[20:23] offset:9216\n"
        "v_mfma_f32_16x16x32_bf16 v[88:91], v[160:163], v[196:199], v[88:91]\n"
        "s_add_u32 s52, s52, 0\n"
        "v_cvt_pk_bf16_f32 v15, v30, v31\n"
        "v_cvt_pk_bf16_f32 v78, v92, v93\n"
        "v_mfma_f32_16x16x32_bf16 v[120:123], v[160:163], v[200:203], v[120:123]\n"
        "s_lshl_b32 s52, s52, 10\n"
        "v_cvt_pk_bf16_f32 v79, v94, v95\n"
        "v_cvt_pk_bf16_f32 v132, v136, v137\n"
        "v_mfma_f32_16x16x32_bf16 v[32:35], a[96:99], v[0:3], 0\n"
        "ds_write_b128 v245, a[24:27] offset:10240\n"
        "v_mfma_f32_16x16x32_bf16 v[32:35], a[100:103], v[4:7], v[32:35]\n"
        "s_add_u32 s26, s18, s52\n"
        "s_addc_u32 s27, s19, 0\n"
        "v_cvt_pk_bf16_f32 v76, v88, v89\n"
        "v_mfma_f32_16x16x32_bf16 v[32:35], a[104:107], v[8:11], v[32:35]\n"
        "s_add_u32 s76, s66, s90\n"
        "s_addc_u32 s77, s67, 0\n"
        "v_cvt_pk_bf16_f32 v77, v90, v91\n"
        "v_mfma_f32_16x16x32_bf16 v[32:35], a[108:111], v[12:15], v[32:35]\n"
        "ds_write_b128 v245, a[28:31] offset:11264\n"
        "v_mfma_f32_16x16x32_bf16 v[120:123], v[164:167], v[216:219], v[120:123]\n"
        "v_xor_b32 v245, 0xc000, v245\n"
        "v_cvt_pk_bf16_f32 v133, v138, v139\n"
        "v_mfma_f32_16x16x32_bf16 v[124:127], v[172:175], v[216:219], v[124:127]\n"
        "v_cvt_pk_bf16_f32 v135, v142, v143\n"
        "v_cvt_pk_bf16_f32 v136, v144, v145\n"
        "v_mfma_f32_16x16x32_bf16 v[124:127], v[168:171], v[200:203], v[124:127]\n"
        "v_max_f32 v246, v32, v33\n"
        "v_max3_f32 v246, v34, v35, v246\n"
        "v_mfma_f32_16x16x32_bf16 v[156:159], v[168:171], v[204:207], v[156:159]\n"
        "v_mov_b32 v247, v246\n"
        "v_cvt_pk_bf16_f32 v108, v120, v121\n"
        "v_mfma_f32_16x16x32_bf16 v[36:39], a[108:111], v[76:79], v[36:39]\n"
        "v_permlane16_swap_b32 v246, v247\n"
        "v_mfma_f32_16x16x32_bf16 v[36:39], a[104:107], v[72:75], v[36:39]\n"
        "ds_read_b128 a[104:107], v240 offset:10240\n"
        "v_max_f32 v246, v246, v247\n"
        "v_mfma_f32_16x16x32_bf16 v[36:39], a[100:103], v[68:71], v[36:39]\n"
        "ds_read_b128 a[100:103], v240 offset:9216\n"
        "v_mov_b32 v247, v246\n"
        "v_mfma_f32_16x16x32_bf16 v[36:39], a[96:99], v[64:67], v[36:39]\n"
        "global_store_dwordx4 v254, a[176:179], s[70:71] offset:0 sc1\n"
        "ds_read_b128 a[96:99], v240 offset:8192\n"
        "s_waitcnt lgkmcnt(3)\n"
        "v_permlane32_swap_b32 v246, v247\n"
        "v_mfma_f32_16x16x32_bf16 v[156:159], v[172:175], v[220:223], v[156:159]\n"
        "ds_read_b128 a[108:111], v240 offset:11264\n"
        "v_max_f32 v246, v246, v247\n"
        "v_mfma_f32_16x16x32_bf16 v[152:155], v[164:167], v[220:223], v[152:155]\n"
        "v_sub_f32 v224, 0, v246\n"
        "v_sub_f32 v225, 0, v246\n"
        "v_mfma_f32_16x16x32_bf16 v[152:155], v[160:163], v[204:207], v[152:155]\n"
        "v_sub_f32 v226, 0, v246\n"
        "v_sub_f32 v227, 0, v246\n"
        "v_mfma_f32_16x16x32_bf16 v[48:51], a[112:115], v[0:3], 0\n"
        "v_max_f32 v248, v36, v37\n"
        "v_max3_f32 v248, v38, v39, v248\n"
        "v_mfma_f32_16x16x32_bf16 v[48:51], a[116:119], v[4:7], v[48:51]\n"
        "v_mov_b32 v249, v248\n"
        "v_add_f32 v32, v32, v224\n"
        "v_mfma_f32_16x16x32_bf16 v[48:51], a[120:123], v[8:11], v[48:51]\n"
        "v_permlane16_swap_b32 v248, v249\n"
        "v_mfma_f32_16x16x32_bf16 v[48:51], a[124:127], v[12:15], v[48:51]\n"
        "v_max_f32 v248, v248, v249\n"
        "v_mov_b32 v249, v248\n"
        "v_mfma_f32_16x16x32_bf16 v[52:55], a[124:127], v[76:79], v[52:55]\n"
        "global_store_dwordx4 v254, a[184:187], s[70:71] offset:2048 sc1\n"
        "s_waitcnt lgkmcnt(1)\n"
        "v_permlane32_swap_b32 v248, v249\n"
        "v_max_f32 v248, v248, v249\n"
        "v_mfma_f32_16x16x32_bf16 v[52:55], a[120:123], v[72:75], v[52:55]\n"
        "ds_read_b128 a[120:123], v240 offset:14336\n"
        "v_sub_f32 v228, 0, v248\n"
        "v_mfma_f32_16x16x32_bf16 v[52:55], a[116:119], v[68:71], v[52:55]\n"
        "ds_read_b128 a[116:119], v240 offset:13312\n"
        "v_sub_f32 v229, 0, v248\n"
        "v_mfma_f32_16x16x32_bf16 v[52:55], a[112:115], v[64:67], v[52:55]\n"
        "v_sub_f32 v230, 0, v248\n"
        "v_sub_f32 v231, 0, v248\n"
        "s_barrier\n"
        "ds_read_b128 a[144:147], v241 offset:0\n"
        "v_exp_f32 v32, v32\n"
        "v_add_f32 v33, v33, v224\n"
        "v_exp_f32 v33, v33\n"
        "ds_read_b128 a[148:151], v241 offset:1024\n"
        "v_cvt_pk_bf16_f32 v16, v32, v33\n"
        "v_add_f32 v34, v34, v224\n"
        "v_exp_f32 v34, v34\n"
        "v_add_f32 v35, v35, v224\n"
        "v_exp_f32 v35, v35\n"
        "ds_read_b128 a[152:155], v241 offset:2048\n"
        "v_cvt_pk_bf16_f32 v17, v34, v35\n"
        "v_mfma_f32_16x16x32_bf16 v[32:35], a[96:99], v[0:3], v[224:227]\n"
        "global_store_dwordx4 v254, a[188:191], s[70:71] offset:3072 sc1\n"
        "s_waitcnt lgkmcnt(4)\n"
        "v_add_f32 v36, v36, v228\n"
        "v_exp_f32 v36, v36\n"
        "v_mfma_f32_16x16x32_bf16 v[32:35], a[100:103], v[4:7], v[32:35]\n"
        "ds_read_b128 a[112:115], v240 offset:12288\n"
        "v_add_f32 v37, v37, v228\n"
        "v_mfma_f32_16x16x32_bf16 v[32:35], a[104:107], v[8:11], v[32:35]\n"
        "v_exp_f32 v37, v37\n"
        "v_mfma_f32_16x16x32_bf16 v[32:35], a[108:111], v[12:15], v[32:35]\n"
        "v_cvt_pk_bf16_f32 v20, v36, v37\n"
        "v_add_f32 v38, v38, v228\n"
        "v_exp_f32 v38, v38\n"
        "v_add_f32 v39, v39, v228\n"
        "v_exp_f32 v39, v39\n"
        "ds_read_b128 a[156:159], v241 offset:3072\n"
        "v_cvt_pk_bf16_f32 v21, v38, v39\n"
        "ds_read_b128 a[128:131], v241 offset:4096\n"
        "v_add_f32 v48, v48, v224\n"
        "v_exp_f32 v48, v48\n"
        "v_add_f32 v49, v49, v224\n"
        "v_exp_f32 v49, v49\n"
        "ds_read_b128 a[124:127], v240 offset:15360\n"
        "v_cvt_pk_bf16_f32 v18, v48, v49\n"
        "ds_read_b128 a[132:135], v241 offset:5120\n"
        "s_add_u32 s70, s38, s97\n"
        "s_addc_u32 s71, s39, 0\n"
        "global_store_dwordx4 v254, a[16:19], s[70:71] offset:0 sc1\n"
        "v_add_f32 v50, v50, v224\n"
        "v_exp_f32 v50, v50\n"
        "v_add_f32 v51, v51, v224\n"
        "v_exp_f32 v51, v51\n"
        "ds_read_b128 a[136:139], v241 offset:6144\n"
        "v_cvt_pk_bf16_f32 v19, v50, v51\n"
        "s_waitcnt lgkmcnt(5)\n"
        "v_mfma_f32_16x16x32_bf16 v[48:51], a[112:115], v[0:3], v[224:227]\n"
        "ds_read_b128 a[140:143], v241 offset:7168\n"
        "s_mul_i32 s96, s40, 4224\n"
        "v_cndmask_b32 v36, v228, v242, s[30:31]\n"
        "v_mfma_f32_16x16x32_bf16 v[48:51], a[116:119], v[4:7], v[48:51]\n"
        "v_cndmask_b32 v37, v228, v242, s[56:57]\n"
        "v_cndmask_b32 v38, v228, v242, s[58:59]\n"
        "v_mfma_f32_16x16x32_bf16 v[48:51], a[120:123], v[8:11], v[48:51]\n"
        "v_cvt_pk_bf16_f32 v141, v154, v155\n"
        "v_cndmask_b32 v39, v228, v242, s[60:61]\n"
        "s_nop 1\n"
        "v_mfma_f32_16x16x32_bf16 v[36:39], a[108:111], v[76:79], v[36:39]\n"
        "ds_read_b128 a[108:111], v240 offset:19456\n"
        "v_bfe_u32 v154, v254, 4, 1\n"
        "v_mfma_f32_16x16x32_bf16 v[36:39], a[104:107], v[72:75], v[36:39]\n"
        "ds_read_b128 a[104:107], v240 offset:18432\n"
        "v_mul_u32_u24 v154, 0xaaaa, v154\n"
        "v_mfma_f32_16x16x32_bf16 v[36:39], a[100:103], v[68:71], v[36:39]\n"
        "s_waitcnt lgkmcnt(4)\n"
        "v_add_u32 v154, 0x4444, v154\n"
        "v_add_f32 v52, v52, v228\n"
        "v_mfma_f32_16x16x32_bf16 v[36:39], a[96:99], v[64:67], v[36:39]\n"
        "v_exp_f32 v52, v52\n"
        "v_mfma_f32_16x16x32_bf16 v[48:51], a[124:127], v[12:15], v[48:51]\n"
        "ds_read_b128 a[96:99], v240 offset:16384\n"
        "v_add_f32 v53, v53, v228\n"
        "v_mfma_f32_16x16x32_bf16 v[160:163], a[144:147], v[16:19], 0\n"
        "v_exp_f32 v53, v53\n"
        "v_mfma_f32_16x16x32_bf16 v[164:167], a[148:151], v[16:19], 0\n"
        "ds_read_b128 a[100:103], v240 offset:17408\n"
        "v_cvt_pk_bf16_f32 v22, v52, v53\n"
        "v_mfma_f32_16x16x32_bf16 v[168:171], a[152:155], v[16:19], 0\n"
        "v_add_f32 v54, v54, v228\n"
        "v_add_f32 v55, v55, v228\n"
        "v_mfma_f32_16x16x32_bf16 v[172:175], a[156:159], v[16:19], 0\n"
        "v_exp_f32 v54, v54\n"
        "v_mfma_f32_16x16x32_bf16 v[176:179], a[128:131], v[16:19], 0\n"
        "v_exp_f32 v55, v55\n"
        "v_mfma_f32_16x16x32_bf16 v[180:183], a[132:135], v[16:19], 0\n"
        "s_waitcnt lgkmcnt(2)\n"
        "v_cvt_pk_bf16_f32 v23, v54, v55\n"
        "s_nop 1\n"
        "v_smfmac_f32_16x16x64_bf16 a[64:67], a[72:75], v[16:23], v154\n"
        "global_store_dwordx4 v254, a[20:23], s[70:71] offset:1024 sc1\n"
        "v_exp_f32 v32, v32\n"
        "v_exp_f32 v33, v33\n"
        "v_mfma_f32_16x16x32_bf16 v[192:195], a[144:147], v[20:23], 0\n"
        "ds_read_b128 a[144:147], v241 offset:8192\n"
        "v_cvt_pk_bf16_f32 v80, v32, v33\n"
        "v_mfma_f32_16x16x32_bf16 v[196:199], a[148:151], v[20:23], 0\n"
        "ds_read_b128 a[148:151], v241 offset:9216\n"
        "v_cndmask_b32 v52, v228, v242, s[62:63]\n"
        "v_mfma_f32_16x16x32_bf16 v[200:203], a[152:155], v[20:23], 0\n"
        "ds_read_b128 a[152:155], v241 offset:10240\n"
        "v_cndmask_b32 v53, v228, v242, s[84:85]\n"
        "v_mfma_f32_16x16x32_bf16 v[204:207], a[156:159], v[20:23], 0\n"
        "ds_read_b128 a[156:159], v241 offset:11264\n"
        "v_cndmask_b32 v54, v228, v242, s[86:87]\n"
        "v_mfma_f32_16x16x32_bf16 v[208:211], a[128:131], v[20:23], 0\n"
        "ds_read_b128 a[128:131], v241 offset:12288\n"
        "v_cndmask_b32 v55, v228, v242, s[88:89]\n"
        "s_nop 1\n"
        "v_mfma_f32_16x16x32_bf16 v[52:55], a[124:127], v[76:79], v[52:55]\n"
        "v_exp_f32 v34, v34\n"
        "v_mfma_f32_16x16x32_bf16 v[52:55], a[120:123], v[72:75], v[52:55]\n"
        "v_exp_f32 v35, v35\n"
        "v_mfma_f32_16x16x32_bf16 v[52:55], a[116:119], v[68:71], v[52:55]\n"
        "ds_read_b128 a[116:119], v240 offset:21504\n"
        "v_cvt_pk_bf16_f32 v81, v34, v35\n"
        "v_mfma_f32_16x16x32_bf16 v[52:55], a[112:115], v[64:67], v[52:55]\n"
        "ds_read_b128 a[112:115], v240 offset:20480\n"
        "v_cndmask_b32 v32, v224, v242, s[10:11]\n"
        "v_mfma_f32_16x16x32_bf16 v[212:215], a[132:135], v[20:23], 0\n"
        "s_waitcnt lgkmcnt(5)\n"
        "v_exp_f32 v36, v36\n"
        "v_mfma_f32_16x16x32_bf16 v[184:187], a[136:139], v[16:19], 0\n"
        "v_exp_f32 v37, v37\n"
        "v_mfma_f32_16x16x32_bf16 v[216:219], a[136:139], v[20:23], 0\n"
        "ds_read_b128 a[120:123], v240 offset:22528\n"
        "v_cvt_pk_bf16_f32 v84, v36, v37\n"
        "v_mfma_f32_16x16x32_bf16 v[220:223], a[140:143], v[20:23], 0\n"
        "global_store_dwordx4 v254, a[24:27], s[70:71] offset:2048 sc1\n"
        "ds_read_b128 a[124:127], v240 offset:23552\n"
        "s_add_u32 s82, s82, 1\n"
        "v_exp_f32 v38, v38\n"
        "v_cndmask_b32 v33, v224, v242, s[12:13]\n"
        "v_mfma_f32_16x16x32_bf16 v[188:191], a[140:143], v[16:19], 0\n"
        "v_exp_f32 v39, v39\n"
        "ds_write_b128 v245, a[216:219] offset:0\n"
        "v_cvt_pk_bf16_f32 v85, v38, v39\n"
        "v_cndmask_b32 v34, v224, v242, s[14:15]\n"
        "v_cndmask_b32 v35, v224, v242, s[16:17]\n"
        "s_nop 1\n"
        "v_mfma_f32_16x16x32_bf16 v[32:35], a[96:99], v[0:3], v[32:35]\n"
        "v_exp_f32 v48, v48\n"
        "v_mfma_f32_16x16x32_bf16 v[32:35], a[100:103], v[4:7], v[32:35]\n"
        "v_exp_f32 v49, v49\n"
        "v_mfma_f32_16x16x32_bf16 v[32:35], a[104:107], v[8:11], v[32:35]\n"
        "ds_read_b128 a[96:99], v240 offset:24576\n"
        "v_cvt_pk_bf16_f32 v82, v48, v49\n"
        "v_mfma_f32_16x16x32_bf16 v[32:35], a[108:111], v[12:15], v[32:35]\n"
        "v_exp_f32 v50, v50\n"
        "v_exp_f32 v51, v51\n"
        "global_store_dwordx4 v254, a[28:31], s[70:71] offset:3072 sc1\n"
        "v_cvt_pk_bf16_f32 v83, v50, v51\n"
        "s_nop 1\n"
        "v_mfma_f32_16x16x32_bf16 v[160:163], a[144:147], v[80:83], v[160:163]\n"
        "ds_write_b128 v245, a[248:251] offset:1024\n"
        "v_mfma_f32_16x16x32_bf16 v[164:167], a[148:151], v[80:83], v[164:167]\n"
        "s_waitcnt lgkmcnt(2)\n"
        "v_exp_f32 v52, v52\n"
        "v_mfma_f32_16x16x32_bf16 v[168:171], a[152:155], v[80:83], v[168:171]\n"
        "v_exp_f32 v53, v53\n"
        "v_mfma_f32_16x16x32_bf16 v[172:175], a[156:159], v[80:83], v[172:175]\n"
        "ds_read_b128 a[132:135], v241 offset:13312\n"
        "v_cvt_pk_bf16_f32 v86, v52, v53\n"
        "v_mfma_f32_16x16x32_bf16 v[176:179], a[128:131], v[80:83], v[176:179]\n"
        "ds_read_b128 a[100:103], v240 offset:25600\n"
        "ds_read_b128 a[136:139], v241 offset:14336\n"
        "ds_read_b128 a[104:107], v240 offset:26624\n"
        "ds_read_b128 a[140:143], v241 offset:15360\n"
        "ds_write_b128 v245, a[220:223] offset:2048\n"
        "ds_read_b128 a[108:111], v240 offset:27648\n"
        "s_add_u32 s70, s92, 0x2\n"
        "s_addc_u32 s71, s93, 0\n"
        "s_cmp_ge_u32 s82, s44\n"
        "s_addc_u32 s90, s82, 0\n"
        "s_lshl_b32 s90, s90, 15\n"
        "v_exp_f32 v54, v54\n"
        "v_exp_f32 v55, v55\n"
        "s_waitcnt lgkmcnt(6)\n"
        "v_cvt_pk_bf16_f32 v87, v54, v55\n"
        "s_nop 1\n"
        "v_smfmac_f32_16x16x64_bf16 a[64:67], a[72:75], v[80:87], v154\n"
        "v_cndmask_b32 v48, v224, v242, s[20:21]\n"
        "v_mfma_f32_16x16x32_bf16 v[192:195], a[144:147], v[84:87], v[192:195]\n"
        "ds_read_b128 a[144:147], v241 offset:16384\n"
        "v_cndmask_b32 v49, v224, v242, s[22:23]\n"
        "v_mfma_f32_16x16x32_bf16 v[196:199], a[148:151], v[84:87], v[196:199]\n"
        "ds_read_b128 a[148:151], v241 offset:17408\n"
        "v_cndmask_b32 v50, v224, v242, s[24:25]\n"
        "v_mfma_f32_16x16x32_bf16 v[200:203], a[152:155], v[84:87], v[200:203]\n"
        "ds_read_b128 a[152:155], v241 offset:18432\n"
        "v_cndmask_b32 v51, v224, v242, s[28:29]\n"
        "s_nop 1\n"
        "v_mfma_f32_16x16x32_bf16 v[48:51], a[112:115], v[0:3], v[48:51]\n"
        "v_exp_f32 v32, v32\n"
        "v_mfma_f32_16x16x32_bf16 v[48:51], a[116:119], v[4:7], v[48:51]\n"
        "v_exp_f32 v33, v33\n"
        "v_mfma_f32_16x16x32_bf16 v[48:51], a[120:123], v[8:11], v[48:51]\n"
        "ds_read_b128 a[112:115], v240 offset:28672\n"
        "v_cvt_pk_bf16_f32 v16, v32, v33\n"
        "v_mfma_f32_16x16x32_bf16 v[48:51], a[124:127], v[12:15], v[48:51]\n"
        "s_waitcnt lgkmcnt(4)\n"
        "v_cvt_pk_bf16_f32 v109, v122, v123\n"
        "v_cvt_pk_bf16_f32 v110, v124, v125\n"
        "v_mfma_f32_16x16x32_bf16 v[204:207], a[156:159], v[84:87], v[204:207]\n"
        "ds_read_b128 a[116:119], v240 offset:29696\n"
        "v_cvt_pk_bf16_f32 v111, v126, v127\n"
        "v_mfma_f32_16x16x32_bf16 v[208:211], a[128:131], v[84:87], v[208:211]\n"
        "v_exp_f32 v34, v34\n"
        "v_mfma_f32_16x16x32_bf16 v[212:215], a[132:135], v[84:87], v[212:215]\n"
        "v_exp_f32 v35, v35\n"
        "v_mfma_f32_16x16x32_bf16 v[180:183], a[132:135], v[80:83], v[180:183]\n"
        "ds_read_b128 a[120:123], v240 offset:30720\n"
        "v_cvt_pk_bf16_f32 v17, v34, v35\n"
        "v_mfma_f32_16x16x32_bf16 v[184:187], a[136:139], v[80:83], v[184:187]\n"
        "ds_write_b128 v245, a[252:255] offset:3072\n"
        "v_mfma_f32_16x16x32_bf16 v[216:219], a[136:139], v[84:87], v[216:219]\n"
        "ds_read_b128 a[124:127], v240 offset:31744\n"
        "v_cndmask_b32 v32, v224, v242, s[30:31]\n"
        "v_mfma_f32_16x16x32_bf16 v[220:223], a[140:143], v[84:87], v[220:223]\n"
        "ds_read_b128 a[156:159], v241 offset:19456\n"
        "v_cndmask_b32 v33, v224, v242, s[56:57]\n"
        "v_mfma_f32_16x16x32_bf16 v[188:191], a[140:143], v[80:83], v[188:191]\n"
        "v_cndmask_b32 v34, v224, v242, s[58:59]\n"
        "v_cndmask_b32 v35, v224, v242, s[60:61]\n"
        "s_nop 1\n"
        "v_mfma_f32_16x16x32_bf16 v[32:35], a[96:99], v[0:3], v[32:35]\n"
        "v_exp_f32 v48, v48\n"
        "v_mfma_f32_16x16x32_bf16 v[32:35], a[100:103], v[4:7], v[32:35]\n"
        "v_exp_f32 v49, v49\n"
        "v_mfma_f32_16x16x32_bf16 v[32:35], a[104:107], v[8:11], v[32:35]\n"
        "s_waitcnt lgkmcnt(3)\n"
        "v_cvt_pk_bf16_f32 v18, v48, v49\n"
        "v_xor_b32 v240, 0x8000, v240\n"
        "v_mfma_f32_16x16x32_bf16 v[32:35], a[108:111], v[12:15], v[32:35]\n"
        "ds_write_b128 v245, a[208:211] offset:8192\n"
        "s_waitcnt lgkmcnt(2)\n"
        "s_barrier\n"
        "ds_read_b128 a[96:99], v240 offset:0\n"
        "ds_read_b128 a[128:131], v241 offset:20480\n"
        "v_exp_f32 v50, v50\n"
        "v_exp_f32 v51, v51\n"
        "v_cndmask_b32 v48, v224, v242, s[62:63]\n"
        "v_cvt_pk_bf16_f32 v19, v50, v51\n"
        "s_nop 1\n"
        "v_mfma_f32_16x16x32_bf16 a[64:67], a[76:79], v[16:19], a[64:67]\n"
        "ds_read_b128 a[132:135], v241 offset:21504\n"
        "v_cndmask_b32 v49, v224, v242, s[84:85]\n"
        "v_mfma_f32_16x16x32_bf16 v[160:163], a[144:147], v[16:19], v[160:163]\n"
        "ds_read_b128 a[100:103], v240 offset:1024\n"
        "v_cndmask_b32 v50, v224, v242, s[86:87]\n"
        "v_mfma_f32_16x16x32_bf16 v[164:167], a[148:151], v[16:19], v[164:167]\n"
        "ds_read_b128 a[136:139], v241 offset:22528\n"
        "v_cndmask_b32 v51, v224, v242, s[88:89]\n"
        "s_nop 1\n"
        "v_mfma_f32_16x16x32_bf16 v[48:51], a[112:115], v[0:3], v[48:51]\n"
        "ds_read_b128 a[112:115], v240 offset:4096\n"
        "v_cvt_pk_bf16_f32 v137, v146, v147\n"
        "v_mfma_f32_16x16x32_bf16 v[48:51], a[116:119], v[4:7], v[48:51]\n"
        "ds_read_b128 a[116:119], v240 offset:5120\n"
        "v_cvt_pk_bf16_f32 v138, v148, v149\n"
        "v_mfma_f32_16x16x32_bf16 v[48:51], a[120:123], v[8:11], v[48:51]\n"
        "ds_read_b128 a[120:123], v240 offset:6144\n"
        "v_cvt_pk_bf16_f32 v139, v150, v151\n"
        "v_mfma_f32_16x16x32_bf16 v[48:51], a[124:127], v[12:15], v[48:51]\n"
        "ds_read_b128 a[124:127], v240 offset:7168\n"
        "v_lshrrev_b32 v244, 1, v254\n"
        "v_mfma_f32_16x16x32_bf16 v[168:171], a[152:155], v[16:19], v[168:171]\n"
        "ds_read_b128 a[140:143], v241 offset:23552\n"
        "s_waitcnt lgkmcnt(8)\n"
        "v_mfma_f32_16x16x32_bf16 v[172:175], a[156:159], v[16:19], v[172:175]\n"
        "ds_read_b128 a[104:107], v240 offset:2048\n"
        "v_add_u32 v244, s81, v244\n"
        "v_mfma_f32_16x16x32_bf16 v[176:179], a[128:131], v[16:19], v[176:179]\n"
        "s_waitcnt lgkmcnt(7)\n"
        "v_exp_f32 v32, v32\n"
        "v_mfma_f32_16x16x32_bf16 v[180:183], a[132:135], v[16:19], v[180:183]\n"
        "s_waitcnt lgkmcnt(6)\n"
        "v_exp_f32 v33, v33\n"
        "v_mfma_f32_16x16x32_bf16 v[184:187], a[136:139], v[16:19], v[184:187]\n"
        "ds_write_b128 v245, a[240:243] offset:9216\n"
        "ds_read_b128 a[108:111], v240 offset:3072\n"
        "ds_read_b128 a[144:147], v241 offset:24576\n"
        "s_add_u32 s81, s78, 4096\n"
        "v_cvt_pk_bf16_f32 v80, v32, v33\n"
        "s_waitcnt lgkmcnt(5)\n"
        "v_mfma_f32_16x16x32_bf16 v[52:55], a[124:127], v[76:79], v[228:231]\n"
        "v_exp_f32 v34, v34\n"
        "v_mfma_f32_16x16x32_bf16 v[52:55], a[120:123], v[72:75], v[52:55]\n"
        "ds_write_b128 v245, a[212:215] offset:10240\n"
        "v_mfma_f32_16x16x32_bf16 v[52:55], a[116:119], v[68:71], v[52:55]\n"
        "v_exp_f32 v35, v35\n"
        "v_mfma_f32_16x16x32_bf16 v[52:55], a[112:115], v[64:67], v[52:55]\n"
        "s_waitcnt vmcnt(0)\n"
        "v_cvt_pk_bf16_f32 v81, v34, v35\n"
        "s_waitcnt lgkmcnt(4)\n"
        "v_mfma_f32_16x16x32_bf16 v[188:191], a[140:143], v[16:19], v[188:191]\n"
        "s_barrier\n"
        "v_mfma_f32_16x16x32_bf16 v[32:35], a[96:99], v[0:3], v[224:227]\n"
        "s_cmp_lg_u32 s40, 0\n"
        "s_cbranch_scc1 LW1_%=\n"
        "s_mov_b64 exec, 0xffff\n"
        "global_store_dwordx2 v244, v[250:251], s[48:49] sc1\n"
        "s_mov_b64 exec, -1\n"
        "LW1_%=:\n"
        "v_mfma_f32_16x16x32_bf16 v[32:35], a[100:103], v[4:7], v[32:35]\n"
        "ds_write_b128 v245, a[244:247] offset:11264\n"
        "v_mfma_f32_16x16x32_bf16 v[32:35], a[104:107], v[8:11], v[32:35]\n"
        "s_waitcnt lgkmcnt(1)\n"
        "v_bfe_u32 v245, v254, 4, 3\n"
        "v_lshl_add_u32 v245, v245, 7, s80\n"
        "v_mfma_f32_16x16x32_bf16 v[32:35], a[108:111], v[12:15], v[32:35]\n"
        "ds_read_b128 a[148:151], v241 offset:25600\n"
        "s_mov_b32 s80, s78\n"
        "v_cvt_pk_bf16_f32 v140, v152, v153\n"
        "v_mfma_f32_16x16x32_bf16 v[36:39], a[108:111], v[76:79], v[228:231]\n"
        "ds_read_b128 a[152:155], v241 offset:26624\n"
        "s_add_u32 m0, s80, 0x0\n"
        "v_cvt_pk_bf16_f32 v142, v156, v157\n"
        "v_mfma_f32_16x16x32_bf16 v[36:39], a[104:107], v[72:75], v[36:39]\n"
        "ds_read_b128 a[156:159], v241 offset:27648\n"
        "s_xor_b32 s78, s78, 0x8000\n"
        "v_cvt_pk_bf16_f32 v143, v158, v159\n"
        "v_mfma_f32_16x16x32_bf16 v[36:39], a[100:103], v[68:71], v[36:39]\n"
        "ds_read_b128 a[128:131], v241 offset:28672\n"
        "s_mov_b32 s80, s78\n"
        "v_cndmask_b32 v40, 0, v242, s[10:11]\n"
        "v_mfma_f32_16x16x32_bf16 v[36:39], a[96:99], v[64:67], v[36:39]\n"
        "ds_read_b128 a[132:135], v241 offset:29696\n"
        "ds_read_b128 a[136:139], v241 offset:30720\n"
        "ds_read_b128 a[140:143], v241 offset:31744\n"
        "v_exp_f32 v48, v48\n"
        "v_exp_f32 v49, v49\n"
        "v_exp_f32 v50, v50\n"
        "v_cvt_pk_bf16_f32 v82, v48, v49\n"
        "v_exp_f32 v51, v51\n"
        "v_xor_b32 v241, 0x8000, v241\n"
        "v_cvt_pk_bf16_f32 v83, v50, v51\n"
        "s_waitcnt lgkmcnt(6)\n"
        "s_nop 0\n"
        "v_mfma_f32_16x16x32_bf16 v[164:167], a[148:151], v[80:83], v[164:167]\n"
        "s_mov_b32 s91, s44\n"
        "v_cndmask_b32 v41, 0, v242, s[12:13]\n"
        "v_cndmask_b32 v42, 0, v242, s[14:15]\n"
        "v_mfma_f32_16x16x32_bf16 v[48:51], a[112:115], v[0:3], v[224:227]\n"
        "LHA10_%=:\n"
        "s_cbranch_execz LHRA_%=\n"
        "v_cndmask_b32 v43, 0, v242, s[16:17]\n"
        "s_nop 1\n"
        "v_mfma_f32_16x16x32_bf16 v[40:43], a[96:99], v[96:99], v[40:43]\n"
        "ds_read_b128 a[96:99], v240 offset:8192\n"
        "v_cndmask_b32 v56, 0, v242, s[20:21]\n"
        "v_mfma_f32_16x16x32_bf16 v[40:43], a[100:103], v[100:103], v[40:43]\n"
        "ds_read_b128 a[100:103], v240 offset:9216\n"
        "v_cndmask_b32 v57, 0, v242, s[22:23]\n"
        "v_mfma_f32_16x16x32_bf16 v[40:43], a[104:107], v[104:107], v[40:43]\n"
        "s_waitcnt lgkmcnt(2)\n"
        "v_exp_f32 v32, v32\n"
        "v_mfma_f32_16x16x32_bf16 v[40:43], a[108:111], v[108:111], v[40:43]\n"
        "s_barrier\n"
        "v_mfma_f32_16x16x32_bf16 v[48:51], a[116:119], v[4:7], v[48:51]\n"
        "ds_read_b128 a[148:151], v241 offset:1024\n"
        "v_cndmask_b32 v58, 0, v242, s[24:25]\n"
        "v_mfma_f32_16x16x32_bf16 v[48:51], a[120:123], v[8:11], v[48:51]\n"
        "v_exp_f32 v33, v33\n"
        "v_mfma_f32_16x16x32_bf16 v[48:51], a[124:127], v[12:15], v[48:51]\n"
        "v_exp_f32 v34, v34\n"
        "v_mfma_f32_16x16x32_bf16 a[64:67], a[76:79], v[80:83], a[64:67]\n"
        "ds_read_b128 a[104:107], v240 offset:10240\n"
        "v_max_f32 v246, v40, v41\n"
        "v_mfma_f32_16x16x32_bf16 v[160:163], a[144:147], v[80:83], v[160:163]\n"
        "ds_read_b128 a[108:111], v240 offset:11264\n"
        "v_max3_f32 v246, v42, v43, v246\n"
        "v_mfma_f32_16x16x32_bf16 v[168:171], a[152:155], v[80:83], v[168:171]\n"
        "ds_read_b128 a[144:147], v241 offset:0\n"
        "v_mov_b32 v247, v246\n"
        "v_mfma_f32_16x16x32_bf16 v[172:175], a[156:159], v[80:83], v[172:175]\n"
        "v_exp_f32 v35, v35\n"
        "v_mfma_f32_16x16x32_bf16 v[176:179], a[128:131], v[80:83], v[176:179]\n"
        "v_permlane16_swap_b32 v246, v247\n"
        "v_mfma_f32_16x16x32_bf16 v[180:183], a[132:135], v[80:83], v[180:183]\n"
        "s_waitcnt lgkmcnt(3)\n"
        "v_max_f32 v246, v246, v247\n"
        "v_mov_b32 v247, v246\n"
        "v_mfma_f32_16x16x32_bf16 v[184:187], a[136:139], v[80:83], v[184:187]\n"
        "v_exp_f32 v48, v48\n"
        "v_mfma_f32_16x16x32_bf16 v[188:191], a[140:143], v[80:83], v[188:191]\n"
        "v_permlane32_swap_b32 v246, v247\n"
        "v_max_f32 v246, v246, v247\n"
        "v_sub_f32 v232, 0, v246\n"
        "v_sub_f32 v233, 0, v246\n"
        "v_sub_f32 v234, 0, v246\n"
        "v_sub_f32 v235, 0, v246\n"
        "v_exp_f32 v49, v49\n"
        "v_exp_f32 v50, v50\n"
        "v_exp_f32 v51, v51\n"
        "v_cvt_pk_bf16_f32 v16, v32, v33\n"
        "v_cvt_pk_bf16_f32 v17, v34, v35\n"
        "v_mfma_f32_16x16x32_bf16 v[32:35], a[96:99], v[0:3], v[224:227]\n"
        "ds_read_b128 a[152:155], v241 offset:2048\n"
        "v_cvt_pk_bf16_f32 v18, v48, v49\n"
        "v_mfma_f32_16x16x32_bf16 v[32:35], a[100:103], v[4:7], v[32:35]\n"
        "s_waitcnt lgkmcnt(1)\n"
        "v_cvt_pk_bf16_f32 v19, v50, v51\n"
        "s_nop 1\n"
        "v_mfma_f32_16x16x32_bf16 v[164:167], a[148:151], v[16:19], v[164:167]\n"
        "global_load_dwordx2 v[252:253], v245, s[48:49] sc1\n"
        "v_cndmask_b32 v59, 0, v242, s[28:29]\n"
        "s_nop 1\n"
        "v_mfma_f32_16x16x32_bf16 v[56:59], a[112:115], v[96:99], v[56:59]\n"
        "v_exp_f32 v36, v36\n"
        "v_mfma_f32_16x16x32_bf16 v[56:59], a[116:119], v[100:103], v[56:59]\n"
        "v_exp_f32 v37, v37\n"
        "v_mfma_f32_16x16x32_bf16 v[56:59], a[120:123], v[104:107], v[56:59]\n"
        "ds_read_b128 a[112:115], v240 offset:12288\n"
        "v_cvt_pk_bf16_f32 v20, v36, v37\n"
        "v_mfma_f32_16x16x32_bf16 v[56:59], a[124:127], v[108:111], v[56:59]\n"
        "v_exp_f32 v38, v38\n"
        "v_mfma_f32_16x16x32_bf16 v[32:35], a[104:107], v[8:11], v[32:35]\n"
        "v_exp_f32 v39, v39\n"
        "v_mfma_f32_16x16x32_bf16 v[32:35], a[108:111], v[12:15], v[32:35]\n"
        "ds_read_b128 a[116:119], v240 offset:13312\n"
        "v_cvt_pk_bf16_f32 v21, v38, v39\n"
        "v_mfma_f32_16x16x32_bf16 v[36:39], a[108:111], v[76:79], v[228:231]\n"
        "ds_read_b128 a[120:123], v240 offset:14336\n"
        "v_add_f32 v40, v40, v232\n"
        "v_mfma_f32_16x16x32_bf16 v[36:39], a[104:107], v[72:75], v[36:39]\n"
        "v_exp_f32 v40, v40\n"
        "v_mfma_f32_16x16x32_bf16 v[36:39], a[100:103], v[68:71], v[36:39]\n"
        "ds_read_b128 a[156:159], v241 offset:3072\n"
        "v_add_f32 v41, v41, v232\n"
        "v_mfma_f32_16x16x32_bf16 v[36:39], a[96:99], v[64:67], v[36:39]\n"
        "v_exp_f32 v41, v41\n"
        "v_mfma_f32_16x16x32_bf16 v[160:163], a[144:147], v[16:19], v[160:163]\n"
        "s_waitcnt lgkmcnt(3)\n"
        "v_cvt_pk_bf16_f32 v24, v40, v41\n"
        "v_add_f32 v42, v42, v232\n"
        "v_mfma_f32_16x16x32_bf16 v[48:51], a[112:115], v[0:3], v[224:227]\n"
        "v_exp_f32 v42, v42\n"
        "v_mfma_f32_16x16x32_bf16 v[168:171], a[152:155], v[16:19], v[168:171]\n"
        "ds_read_b128 a[124:127], v240 offset:15360\n"
        "v_add_f32 v43, v43, v232\n"
        "s_waitcnt lgkmcnt(3)\n"
        "v_mfma_f32_16x16x32_bf16 v[48:51], a[116:119], v[4:7], v[48:51]\n"
        "s_waitcnt lgkmcnt(2)\n"
        "v_exp_f32 v43, v43\n"
        "v_mfma_f32_16x16x32_bf16 v[48:51], a[120:123], v[8:11], v[48:51]\n"
        "ds_read_b128 a[128:131], v241 offset:4096\n"
        "ds_read_b128 a[132:135], v241 offset:5120\n"
        "v_cvt_pk_bf16_f32 v25, v42, v43\n"
        "v_cndmask_b32 v40, v232, v242, s[30:31]\n"
        "s_waitcnt lgkmcnt(3)\n"
        "v_mfma_f32_16x16x32_bf16 v[172:175], a[156:159], v[16:19], v[172:175]\n"
        "s_mov_b64 exec, 0\n"
        "s_cmp_eq_u32 s40, 0\n"
        "s_cbranch_scc1 LHC0_%=\n"
        "s_cmp_eq_u32 s40, 1\n"
        "s_cbranch_scc1 LHC1_%=\n"
        "s_cmp_eq_u32 s40, 2\n"
        "s_cbranch_scc1 LHC2_%=\n"
        "s_branch LHC3_%=\n"
        "LHRC_%=:\n"
        "s_mov_b64 exec, -1\n"
        "ds_read_b128 a[136:139], v241 offset:6144\n"
        "ds_read_b128 a[140:143], v241 offset:7168\n"
        "v_exp_f32 v52, v52\n"
        "v_exp_f32 v53, v53\n"
        "s_waitcnt lgkmcnt(4)\n"
        "v_cvt_pk_bf16_f32 v22, v52, v53\n"
        "v_mfma_f32_16x16x32_bf16 v[48:51], a[124:127], v[12:15], v[48:51]\n"
        "v_cndmask_b32 v41, v232, v242, s[56:57]\n"
        "v_cndmask_b32 v42, v232, v242, s[58:59]\n"
        "v_cndmask_b32 v43, v232, v242, s[60:61]\n"
        "s_nop 1\n"
        "v_mfma_f32_16x16x32_bf16 v[40:43], a[96:99], v[96:99], v[40:43]\n"
        "ds_read_b128 a[96:99], v240 offset:16384\n"
        "v_add_f32 v56, v56, v232\n"
        "v_mfma_f32_16x16x32_bf16 v[40:43], a[100:103], v[100:103], v[40:43]\n"
        "v_exp_f32 v54, v54\n"
        "v_mfma_f32_16x16x32_bf16 v[40:43], a[104:107], v[104:107], v[40:43]\n"
        "s_waitcnt vmcnt(0) lgkmcnt(3)\n"
        "v_cmp_ne_u64 vcc, v[252:253], v[250:251]\n"
        "v_mov_b32 v144, v254\n"
        "v_mfma_f32_16x16x32_bf16 v[40:43], a[108:111], v[108:111], v[40:43]\n"
        "s_cbranch_vccnz LPKV_%=\n"
        "LBKV_%=:\n"
        "v_exp_f32 v55, v55\n"
        "v_mfma_f32_16x16x32_bf16 v[176:179], a[128:131], v[16:19], v[176:179]\n"
        "ds_read_b128 a[100:103], v240 offset:17408\n"
        "v_cvt_pk_bf16_f32 v23, v54, v55\n"
        "v_mfma_f32_16x16x32_bf16 v[52:55], a[124:127], v[76:79], v[228:231]\n"
        "v_exp_f32 v56, v56\n"
        "v_mfma_f32_16x16x32_bf16 v[52:55], a[120:123], v[72:75], v[52:55]\n"
        "ds_read_b128 a[104:107], v240 offset:18432\n"
        "v_add_f32 v57, v57, v232\n"
        "v_mfma_f32_16x16x32_bf16 v[52:55], a[116:119], v[68:71], v[52:55]\n"
        "ds_read_b128 a[108:111], v240 offset:19456\n"
        "v_mov_b32 v145, v254\n"
        "v_mfma_f32_16x16x32_bf16 v[52:55], a[112:115], v[64:67], v[52:55]\n"
        "v_exp_f32 v57, v57\n"
        "v_smfmac_f32_16x16x64_bf16 a[64:67], a[72:75], v[16:23], v154\n"
        "global_load_lds_dwordx4 v254, s[68:69] offset:0 sc1\n"
        "v_mov_b32 v146, v254\n"
        "v_mfma_f32_16x16x32_bf16 v[192:195], a[144:147], v[20:23], v[192:195]\n"
        "v_cvt_pk_bf16_f32 v26, v56, v57\n"
        "v_mov_b32 v147, v254\n"
        "v_mfma_f32_16x16x32_bf16 v[196:199], a[148:151], v[20:23], v[196:199]\n"
        "v_accvgpr_read_b32 v148, a88\n"
        "v_accvgpr_read_b32 v152, a89\n"
        "v_mfma_f32_16x16x32_bf16 v[200:203], a[152:155], v[20:23], v[200:203]\n"
        "v_accvgpr_read_b32 v156, a90\n"
        "v_accvgpr_read_b32 v153, a92\n"
        "v_mfma_f32_16x16x32_bf16 v[204:207], a[156:159], v[20:23], v[204:207]\n"
        "global_load_lds_dwordx4 v254, s[68:69] offset:1024 sc1\n"
        "v_accvgpr_read_b32 v155, a94\n"
        "v_mfma_f32_16x16x32_bf16 v[208:211], a[128:131], v[20:23], v[208:211]\n"
        "v_add_u32 v149, 1024, v148\n"
        "v_accvgpr_write_b32 a88, 0\n"
        "v_mfma_f32_16x16x32_bf16 v[212:215], a[132:135], v[20:23], v[212:215]\n"
        "v_add_u32 v150, 2048, v148\n"
        "v_accvgpr_write_b32 a89, 0\n"
        "v_mfma_f32_16x16x32_bf16 v[180:183], a[132:135], v[16:19], v[180:183]\n"
        "s_waitcnt lgkmcnt(0)\n"
        "v_add_u32 v151, 3072, v148\n"
        "v_accvgpr_write_b32 a90, 0\n"
        "v_mfma_f32_16x16x32_bf16 v[184:187], a[136:139], v[16:19], v[184:187]\n"
        "global_load_lds_dwordx4 v254, s[68:69] offset:2048 sc1\n"
        "v_add_f32 v58, v58, v232\n"
        "v_mfma_f32_16x16x32_bf16 v[216:219], a[136:139], v[20:23], v[216:219]\n"
        "v_exp_f32 v58, v58\n"
        "v_mfma_f32_16x16x32_bf16 v[220:223], a[140:143], v[20:23], v[220:223]\n"
        "v_add_f32 v59, v59, v232\n"
        "v_cndmask_b32 v56, v232, v242, s[62:63]\n"
        "v_mfma_f32_16x16x32_bf16 v[188:191], a[140:143], v[16:19], v[188:191]\n"
        "v_exp_f32 v59, v59\n"
        "v_exp_f32 v32, v32\n"
        "v_cvt_pk_bf16_f32 v27, v58, v59\n"
        "s_nop 1\n"
        "v_mfma_f32_16x16x32_bf16 a[68:71], a[76:79], v[24:27], a[68:71]\n"
        "v_exp_f32 v33, v33\n"
        "v_mfma_f32_16x16x32_bf16 a[0:3], a[144:147], v[24:27], 0\n"
        "ds_read_b128 a[144:147], v241 offset:8192\n"
        "v_cvt_pk_bf16_f32 v80, v32, v33\n"
        "v_mfma_f32_16x16x32_bf16 a[4:7], a[148:151], v[24:27], 0\n"
        "v_exp_f32 v34, v34\n"
        "v_mfma_f32_16x16x32_bf16 a[8:11], a[152:155], v[24:27], 0\n"
        "v_exp_f32 v35, v35\n"
        "v_mfma_f32_16x16x32_bf16 a[12:15], a[156:159], v[24:27], 0\n"
        "ds_read_b128 a[148:151], v241 offset:9216\n"
        "v_cvt_pk_bf16_f32 v81, v34, v35\n"
        "v_mfma_f32_16x16x32_bf16 v[32:35], a[96:99], v[0:3], v[224:227]\n"
        "ds_read_b128 a[152:155], v241 offset:10240\n"
        "v_cndmask_b32 v57, v232, v242, s[84:85]\n"
        "v_mfma_f32_16x16x32_bf16 v[32:35], a[100:103], v[4:7], v[32:35]\n"
        "ds_read_b128 a[156:159], v241 offset:11264\n"
        "v_cndmask_b32 v58, v232, v242, s[86:87]\n"
        "v_mfma_f32_16x16x32_bf16 v[32:35], a[104:107], v[8:11], v[32:35]\n"
        "global_load_lds_dwordx4 v254, s[68:69] offset:3072 sc1\n"
        "s_add_u32 m0, s81, 0x0\n"
        "v_cndmask_b32 v59, v232, v242, s[88:89]\n"
        "s_nop 1\n"
        "v_mfma_f32_16x16x32_bf16 v[56:59], a[112:115], v[96:99], v[56:59]\n"
        "s_add_u32 s81, s78, 4096\n"
        "v_exp_f32 v36, v36\n"
        "v_mfma_f32_16x16x32_bf16 v[56:59], a[116:119], v[100:103], v[56:59]\n"
        "v_exp_f32 v37, v37\n"
        "v_mfma_f32_16x16x32_bf16 v[56:59], a[120:123], v[104:107], v[56:59]\n"
        "ds_read_b128 a[112:115], v240 offset:20480\n"
        "v_cvt_pk_bf16_f32 v84, v36, v37\n"
        "v_mfma_f32_16x16x32_bf16 v[56:59], a[124:127], v[108:111], v[56:59]\n"
        "v_exp_f32 v38, v38\n"
        "v_mfma_f32_16x16x32_bf16 v[32:35], a[108:111], v[12:15], v[32:35]\n"
        "v_exp_f32 v39, v39\n"
        "v_mfma_f32_16x16x32_bf16 a[16:19], a[128:131], v[24:27], 0\n"
        "ds_read_b128 a[116:119], v240 offset:21504\n"
        "v_cvt_pk_bf16_f32 v85, v38, v39\n"
        "v_mfma_f32_16x16x32_bf16 v[36:39], a[108:111], v[76:79], v[228:231]\n"
        "v_exp_f32 v40, v40\n"
        "v_mfma_f32_16x16x32_bf16 v[36:39], a[104:107], v[72:75], v[36:39]\n"
        "v_exp_f32 v41, v41\n"
        "v_mfma_f32_16x16x32_bf16 v[36:39], a[100:103], v[68:71], v[36:39]\n"
        "ds_read_b128 a[120:123], v240 offset:22528\n"
        "v_cvt_pk_bf16_f32 v88, v40, v41\n"
        "v_mfma_f32_16x16x32_bf16 v[36:39], a[96:99], v[64:67], v[36:39]\n"
        "ds_read_b128 a[124:127], v240 offset:23552\n"
        "v_and_b32 v245, 0x70, v254\n"
        "v_mfma_f32_16x16x32_bf16 a[20:23], a[132:135], v[24:27], 0\n"
        "global_load_lds_dwordx4 v254, s[74:75] offset:0 sc1\n"
        "v_lshlrev_b32 v245, 3, v245\n"
        "v_mfma_f32_16x16x32_bf16 a[24:27], a[136:139], v[24:27], 0\n"
        "v_exp_f32 v42, v42\n"
        "v_mfma_f32_16x16x32_bf16 a[28:31], a[140:143], v[24:27], 0\n"
        "v_exp_f32 v43, v43\n"
        "v_exp_f32 v48, v48\n"
        "v_cvt_pk_bf16_f32 v89, v42, v43\n"
        "v_mfma_f32_16x16x32_bf16 v[40:43], a[96:99], v[96:99], v[232:235]\n"
        "v_exp_f32 v49, v49\n"
        "v_mfma_f32_16x16x32_bf16 v[40:43], a[100:103], v[100:103], v[40:43]\n"
        "global_load_lds_dwordx4 v254, s[74:75] offset:1024 sc1\n"
        "v_cvt_pk_bf16_f32 v82, v48, v49\n"
        "v_mfma_f32_16x16x32_bf16 v[40:43], a[104:107], v[104:107], v[40:43]\n"
        "v_exp_f32 v50, v50\n"
        "v_mfma_f32_16x16x32_bf16 v[40:43], a[108:111], v[108:111], v[40:43]\n"
        "v_exp_f32 v51, v51\n"
        "v_exp_f32 v52, v52\n"
        "v_cvt_pk_bf16_f32 v83, v50, v51\n"
        "s_waitcnt lgkmcnt(0)\n"
        "v_mfma_f32_16x16x32_bf16 v[48:51], a[112:115], v[0:3], v[224:227]\n"
        "v_exp_f32 v53, v53\n"
        "v_mfma_f32_16x16x32_bf16 v[48:51], a[116:119], v[4:7], v[48:51]\n"
        "global_load_lds_dwordx4 v254, s[74:75] offset:2048 sc1\n"
        "v_cvt_pk_bf16_f32 v86, v52, v53\n"
        "v_mfma_f32_16x16x32_bf16 v[48:51], a[120:123], v[8:11], v[48:51]\n"
        "ds_read_b128 a[128:131], v241 offset:12288\n"
        "v_cndmask_b32 v44, 0, v242, s[10:11]\n"
        "v_mfma_f32_16x16x32_bf16 v[48:51], a[124:127], v[12:15], v[48:51]\n"
        "ds_read_b128 a[132:135], v241 offset:13312\n"
        "s_lshr_b32 s10, s41, 1\n"
        "v_cndmask_b32 v45, 0, v242, s[12:13]\n"
        "v_mfma_f32_16x16x32_bf16 v[160:163], a[144:147], v[80:83], v[160:163]\n"
        "ds_read_b128 a[136:139], v241 offset:14336\n"
        "s_and_b32 s11, s42, 7\n"
        "v_cndmask_b32 v46, 0, v242, s[14:15]\n"
        "v_mfma_f32_16x16x32_bf16 v[164:167], a[148:151], v[80:83], v[164:167]\n"
        "global_load_lds_dwordx4 v254, s[74:75] offset:3072 sc1\n"
        "s_add_u32 m0, s79, 0x0\n"
        "v_cndmask_b32 v47, 0, v242, s[16:17]\n"
        "s_nop 1\n"
        "v_mfma_f32_16x16x32_bf16 v[44:47], a[108:111], v[140:143], v[44:47]\n"
        "s_and_b32 s12, s41, 1\n"
        "v_exp_f32 v54, v54\n"
        "v_mfma_f32_16x16x32_bf16 v[44:47], a[104:107], v[136:139], v[44:47]\n"
        "s_lshr_b32 s14, s42, 3\n"
        "v_exp_f32 v55, v55\n"
        "v_mfma_f32_16x16x32_bf16 v[44:47], a[100:103], v[132:135], v[44:47]\n"
        "ds_read_b128 a[100:103], v240 offset:25600\n"
        "s_lshl2_add_u32 s12, s12, s14\n"
        "v_cvt_pk_bf16_f32 v87, v54, v55\n"
        "v_mfma_f32_16x16x32_bf16 v[44:47], a[96:99], v[128:131], v[44:47]\n"
        "ds_read_b128 a[96:99], v240 offset:24576\n"
        "global_load_lds_dwordx4 v254, s[76:77] offset:0 sc1\n"
        "s_lshl3_add_u32 s13, s12, s11\n"
        "v_mfma_f32_16x16x32_bf16 v[52:55], a[124:127], v[76:79], v[228:231]\n"
        "s_lshl_b32 s14, s10, 21\n"
        "v_exp_f32 v56, v56\n"
        "v_mfma_f32_16x16x32_bf16 v[52:55], a[120:123], v[72:75], v[52:55]\n"
        "s_lshl_b32 s15, s40, 15\n"
        "v_exp_f32 v57, v57\n"
        "v_mfma_f32_16x16x32_bf16 v[52:55], a[116:119], v[68:71], v[52:55]\n"
        "ds_read_b128 a[104:107], v240 offset:26624\n"
        "s_add_u32 s14, s14, s15\n"
        "v_cvt_pk_bf16_f32 v90, v56, v57\n"
        "v_mfma_f32_16x16x32_bf16 v[52:55], a[112:115], v[64:67], v[52:55]\n"
        "s_lshl_b32 s15, s11, 8\n"
        "v_exp_f32 v58, v58\n"
        "v_smfmac_f32_16x16x64_bf16 a[64:67], a[72:75], v[80:87], v154\n"
        "ds_read_b128 a[140:143], v241 offset:15360\n"
        "global_load_lds_dwordx4 v254, s[76:77] offset:1024 sc1\n"
        "s_add_u32 s14, s14, s15\n"
        "v_mfma_f32_16x16x32_bf16 v[192:195], a[144:147], v[84:87], v[192:195]\n"
        "s_add_u32 s17, s11, s12\n"
        "v_exp_f32 v59, v59\n"
        "v_mfma_f32_16x16x32_bf16 v[196:199], a[148:151], v[84:87], v[196:199]\n"
        "ds_read_b128 a[108:111], v240 offset:27648\n"
        "s_and_b32 s17, s17, 7\n"
        "v_cvt_pk_bf16_f32 v91, v58, v59\n"
        "v_mfma_f32_16x16x32_bf16 v[56:59], a[112:115], v[96:99], v[232:235]\n"
        "v_exp_f32 v32, v32\n"
        "v_mfma_f32_16x16x32_bf16 v[56:59], a[116:119], v[100:103], v[56:59]\n"
        "v_exp_f32 v33, v33\n"
        "v_mfma_f32_16x16x32_bf16 v[56:59], a[120:123], v[104:107], v[56:59]\n"
        "global_load_lds_dwordx4 v254, s[76:77] offset:2048 sc1\n"
        "v_cvt_pk_bf16_f32 v16, v32, v33\n"
        "v_mfma_f32_16x16x32_bf16 v[56:59], a[124:127], v[108:111], v[56:59]\n"
        "v_exp_f32 v34, v34\n"
        "v_mfma_f32_16x16x32_bf16 a[68:71], a[76:79], v[88:91], a[68:71]\n"
        "v_exp_f32 v35, v35\n"
        "v_mfma_f32_16x16x32_bf16 a[0:3], a[144:147], v[88:91], a[0:3]\n"
        "ds_read_b128 a[144:147], v241 offset:16384\n"
        "v_cvt_pk_bf16_f32 v17, v34, v35\n"
        "v_mfma_f32_16x16x32_bf16 a[4:7], a[148:151], v[88:91], a[4:7]\n"
        "v_exp_f32 v36, v36\n"
        "v_mfma_f32_16x16x32_bf16 v[168:171], a[152:155], v[80:83], v[168:171]\n"
        "v_exp_f32 v37, v37\n"
        "v_mfma_f32_16x16x32_bf16 v[200:203], a[152:155], v[84:87], v[200:203]\n"
        "global_load_lds_dwordx4 v254, s[76:77] offset:3072 sc1\n"
        "s_add_u32 m0, s79, 0x1000\n"
        "v_max_f32 v248, v44, v45\n"
        "v_mfma_f32_16x16x32_bf16 a[8:11], a[152:155], v[88:91], a[8:11]\n"
        "ds_read_b128 a[148:151], v241 offset:17408\n"
        "v_cvt_pk_bf16_f32 v20, v36, v37\n"
        "v_mfma_f32_16x16x32_bf16 a[12:15], a[156:159], v[88:91], a[12:15]\n"
        "ds_read_b128 a[152:155], v241 offset:18432\n"
        "v_max3_f32 v248, v46, v47, v248\n"
        "v_mfma_f32_16x16x32_bf16 v[204:207], a[156:159], v[84:87], v[204:207]\n"
        "s_add_u32 s76, s76, 0x1000\n"
        "s_addc_u32 s77, s77, 0\n"
        "v_mov_b32 v249, v248\n"
        "v_mfma_f32_16x16x32_bf16 v[172:175], a[156:159], v[80:83], v[172:175]\n"
        "s_waitcnt lgkmcnt(3)\n"
        "v_permlane16_swap_b32 v248, v249\n"
        "v_mfma_f32_16x16x32_bf16 v[32:35], a[96:99], v[0:3], v[224:227]\n"
        "global_load_lds_dwordx4 v254, s[76:77] offset:0 sc1\n"
        "v_max_f32 v248, v248, v249\n"
        "v_mfma_f32_16x16x32_bf16 v[32:35], a[100:103], v[4:7], v[32:35]\n"
        "ds_read_b128 a[156:159], v241 offset:19456\n"
        "v_mov_b32 v249, v248\n"
        "v_mfma_f32_16x16x32_bf16 v[32:35], a[104:107], v[8:11], v[32:35]\n"
        "v_exp_f32 v38, v38\n"
        "v_mfma_f32_16x16x32_bf16 v[32:35], a[108:111], v[12:15], v[32:35]\n"
        "v_permlane32_swap_b32 v248, v249\n"
        "v_mfma_f32_16x16x32_bf16 v[176:179], a[128:131], v[80:83], v[176:179]\n"
        "global_load_lds_dwordx4 v254, s[76:77] offset:1024 sc1\n"
        "v_max_f32 v248, v248, v249\n"
        "v_mfma_f32_16x16x32_bf16 v[208:211], a[128:131], v[84:87], v[208:211]\n"
        "s_add_u32 s68, s64, s90\n"
        "s_addc_u32 s69, s65, 0\n"
        "v_sub_f32 v236, 0, v248\n"
        "v_mfma_f32_16x16x32_bf16 a[16:19], a[128:131], v[88:91], a[16:19]\n"
        "ds_read_b128 a[128:131], v241 offset:20480\n"
        "v_sub_f32 v237, 0, v248\n"
        "v_mfma_f32_16x16x32_bf16 a[20:23], a[132:135], v[88:91], a[20:23]\n"
        "s_add_u32 s74, s68, 0x1000\n"
        "s_addc_u32 s75, s69, 0\n"
        "v_sub_f32 v238, 0, v248\n"
        "v_mfma_f32_16x16x32_bf16 v[212:215], a[132:135], v[84:87], v[212:215]\n"
        "global_load_lds_dwordx4 v254, s[76:77] offset:2048 sc1\n"
        "v_sub_f32 v239, 0, v248\n"
        "v_mfma_f32_16x16x32_bf16 v[180:183], a[132:135], v[80:83], v[180:183]\n"
        "v_exp_f32 v39, v39\n"
        "v_mfma_f32_16x16x32_bf16 v[184:187], a[136:139], v[80:83], v[184:187]\n"
        "ds_read_b128 a[132:135], v241 offset:21504\n"
        "v_cvt_pk_bf16_f32 v21, v38, v39\n"
        "v_mfma_f32_16x16x32_bf16 v[36:39], a[108:111], v[76:79], v[228:231]\n"
        "v_cndmask_b32 v60, 0, v242, s[20:21]\n"
        "v_cndmask_b32 v61, 0, v242, s[22:23]\n"
        "v_mfma_f32_16x16x32_bf16 v[36:39], a[104:107], v[72:75], v[36:39]\n"
        "global_load_lds_dwordx4 v254, s[76:77] offset:3072 sc1\n"
        "s_add_u32 m0, s96, 0x20260\n"
        "v_cndmask_b32 v62, 0, v242, s[24:25]\n"
        "v_mfma_f32_16x16x32_bf16 v[36:39], a[100:103], v[68:71], v[36:39]\n"
        "s_sub_u32 s24, s83, 1\n"
        "v_cndmask_b32 v63, 0, v242, s[28:29]\n"
        "s_nop 1\n"
        "v_mfma_f32_16x16x32_bf16 v[60:63], a[124:127], v[140:143], v[60:63]\n"
        "s_mov_b32 s25, 4096\n"
        "v_exp_f32 v40, v40\n"
        "v_mfma_f32_16x16x32_bf16 v[60:63], a[120:123], v[136:139], v[60:63]\n"
        "s_mov_b32 s28, 4096\n"
        "v_exp_f32 v41, v41\n"
        "v_mfma_f32_16x16x32_bf16 v[60:63], a[116:119], v[132:135], v[60:63]\n"
        "ds_read_b128 a[116:119], v240 offset:29696\n"
        "s_lshl_b32 s29, s17, 18\n"
        "v_cvt_pk_bf16_f32 v24, v40, v41\n"
        "v_mfma_f32_16x16x32_bf16 v[60:63], a[112:115], v[128:131], v[60:63]\n"
        "ds_read_b128 a[112:115], v240 offset:28672\n"
        "v_add_f32 v44, v44, v236\n"
        "v_mfma_f32_16x16x32_bf16 v[36:39], a[96:99], v[64:67], v[36:39]\n"
        "v_exp_f32 v42, v42\n"
        "v_mfma_f32_16x16x32_bf16 v[216:219], a[136:139], v[84:87], v[216:219]\n"
        "v_exp_f32 v43, v43\n"
        "v_mfma_f32_16x16x32_bf16 a[24:27], a[136:139], v[88:91], a[24:27]\n"
        "ds_read_b128 a[120:123], v240 offset:30720\n"
        "v_cvt_pk_bf16_f32 v25, v42, v43\n"
        "v_mfma_f32_16x16x32_bf16 v[40:43], a[96:99], v[96:99], v[232:235]\n"
        "v_exp_f32 v44, v44\n"
        "v_mfma_f32_16x16x32_bf16 v[40:43], a[100:103], v[100:103], v[40:43]\n"
        "global_load_lds_dwordx4 v254, s[70:71] offset:0\n"
        "v_add_f32 v45, v45, v236\n"
        "v_mfma_f32_16x16x32_bf16 v[40:43], a[104:107], v[104:107], v[40:43]\n"
        "v_exp_f32 v45, v45\n"
        "v_mfma_f32_16x16x32_bf16 v[40:43], a[108:111], v[108:111], v[40:43]\n"
        "ds_read_b128 a[124:127], v240 offset:31744\n"
        "v_cvt_pk_bf16_f32 v28, v44, v45\n"
        "v_mfma_f32_16x16x32_bf16 a[28:31], a[140:143], v[88:91], a[28:31]\n"
        "ds_read_b128 a[136:139], v241 offset:22528\n"
        "v_add_f32 v46, v46, v236\n"
        "v_mfma_f32_16x16x32_bf16 v[220:223], a[140:143], v[84:87], v[220:223]\n"
        "v_exp_f32 v46, v46\n"
        "v_mfma_f32_16x16x32_bf16 v[188:191], a[140:143], v[80:83], v[188:191]\n"
        "v_add_f32 v47, v47, v236\n"
        "v_exp_f32 v47, v47\n"
        "v_exp_f32 v48, v48\n"
        "v_cvt_pk_bf16_f32 v29, v46, v47\n"
        "v_exp_f32 v49, v49\n"
        "global_load_lds_dwordx4 v254, s[70:71] offset:1024\n"
        "v_cvt_pk_bf16_f32 v18, v48, v49\n"
        "v_exp_f32 v50, v50\n"
        "v_exp_f32 v51, v51\n"
        "v_exp_f32 v52, v52\n"
        "v_cvt_pk_bf16_f32 v19, v50, v51\n"
        "s_waitcnt lgkmcnt(2)\n"
        "v_mfma_f32_16x16x32_bf16 v[48:51], a[112:115], v[0:3], v[224:227]\n"
        "s_waitcnt vmcnt(10) lgkmcnt(1)\n"
        "v_exp_f32 v53, v53\n"
        "v_mfma_f32_16x16x32_bf16 v[48:51], a[116:119], v[4:7], v[48:51]\n"
        "global_load_lds_dwordx4 v254, s[70:71] offset:2048\n"
        "v_cvt_pk_bf16_f32 v22, v52, v53\n"
        "v_mfma_f32_16x16x32_bf16 v[48:51], a[120:123], v[8:11], v[48:51]\n"
        "v_exp_f32 v54, v54\n"
        "v_mfma_f32_16x16x32_bf16 v[48:51], a[124:127], v[12:15], v[48:51]\n"
        "v_exp_f32 v55, v55\n"
        "v_mfma_f32_16x16x32_bf16 v[160:163], a[144:147], v[16:19], v[160:163]\n"
        "ds_read_b128 a[140:143], v241 offset:23552\n"
        "v_cvt_pk_bf16_f32 v23, v54, v55\n"
        "v_mfma_f32_16x16x32_bf16 v[52:55], a[124:127], v[76:79], v[228:231]\n"
        "global_load_dword v244, v245, s[26:27] offset:0\n"
        "v_cndmask_b32 v44, v236, v242, s[30:31]\n"
        "v_mfma_f32_16x16x32_bf16 v[52:55], a[120:123], v[72:75], v[52:55]\n"
        "s_add_u32 s26, s26, 0x20000\n"
        "s_addc_u32 s27, s27, 0\n"
        "v_cndmask_b32 v45, v236, v242, s[56:57]\n"
        "v_mfma_f32_16x16x32_bf16 v[52:55], a[116:119], v[68:71], v[52:55]\n"
        "global_load_dword v244, v245, s[26:27] offset:0\n"
        "s_mov_b32 s30, 131072\n"
        "v_cndmask_b32 v46, v236, v242, s[58:59]\n"
        "v_mfma_f32_16x16x32_bf16 v[52:55], a[112:115], v[64:67], v[52:55]\n"
        "s_add_u32 s26, s26, 0x20000\n"
        "s_addc_u32 s27, s27, 0\n"
        "v_cndmask_b32 v47, v236, v242, s[60:61]\n"
        "s_nop 1\n"
        "v_mfma_f32_16x16x32_bf16 v[44:47], a[108:111], v[140:143], v[44:47]\n"
        "v_exp_f32 v56, v56\n"
        "v_mfma_f32_16x16x32_bf16 v[44:47], a[104:107], v[136:139], v[44:47]\n"
        "v_exp_f32 v57, v57\n"
        "v_mfma_f32_16x16x32_bf16 v[44:47], a[100:103], v[132:135], v[44:47]\n"
        "s_add_u32 s22, s18, s14\n"
        "s_addc_u32 s23, s19, 0\n"
        "v_cvt_pk_bf16_f32 v26, v56, v57\n"
        "v_mfma_f32_16x16x32_bf16 v[44:47], a[96:99], v[128:131], v[44:47]\n"
        "s_barrier\n"
        "v_smfmac_f32_16x16x64_bf16 a[64:67], a[72:75], v[16:23], v154\n"
        "s_sub_u32 s22, s22, 4096\n"
        "s_subb_u32 s23, s23, 0\n"
        "v_xor_b32 v240, 0x8000, v240\n"
        "v_mfma_f32_16x16x32_bf16 v[192:195], a[144:147], v[20:23], v[192:195]\n"
        "ds_read_b128 a[96:99], v240 offset:0\n"
        "s_lshl_b32 s14, s10, 18\n"
        "v_lshrrev_b32 v252, 2, v254\n"
        "v_mfma_f32_16x16x32_bf16 v[196:199], a[148:151], v[20:23], v[196:199]\n"
        "s_add_u32 s14, s14, s72\n"
        "v_exp_f32 v58, v58\n"
        "v_mfma_f32_16x16x32_bf16 v[164:167], a[148:151], v[16:19], v[164:167]\n"
        "s_add_u32 s14, s14, 0x2200000\n"
        "v_exp_f32 v59, v59\n"
        "v_mfma_f32_16x16x32_bf16 v[168:171], a[152:155], v[16:19], v[168:171]\n"
        "ds_read_b128 a[100:103], v240 offset:1024\n"
        "v_cvt_pk_bf16_f32 v27, v58, v59\n"
        "v_mfma_f32_16x16x32_bf16 v[56:59], a[112:115], v[96:99], v[232:235]\n"
        "ds_read_b128 a[104:107], v240 offset:2048\n"
        "v_add_f32 v60, v60, v236\n"
        "v_mfma_f32_16x16x32_bf16 v[56:59], a[116:119], v[100:103], v[56:59]\n"
        "v_exp_f32 v60, v60\n"
        "v_mfma_f32_16x16x32_bf16 v[56:59], a[120:123], v[104:107], v[56:59]\n"
        "ds_read_b128 a[108:111], v240 offset:3072\n"
        "v_add_f32 v61, v61, v236\n"
        "v_mfma_f32_16x16x32_bf16 v[56:59], a[124:127], v[108:111], v[56:59]\n"
        "v_exp_f32 v61, v61\n"
        "v_mfma_f32_16x16x32_bf16 a[0:3], a[144:147], v[24:27], a[0:3]\n"
        "s_add_u32 s20, s38, s14\n"
        "s_addc_u32 s21, s39, 0\n"
        "v_cvt_pk_bf16_f32 v30, v60, v61\n"
        "v_mfma_f32_16x16x32_bf16 a[4:7], a[148:151], v[24:27], a[4:7]\n"
        "s_waitcnt lgkmcnt(3)\n"
        "v_add_f32 v62, v62, v236\n"
        "v_add_f32 v63, v63, v236\n"
        "v_mfma_f32_16x16x32_bf16 v[200:203], a[152:155], v[20:23], v[200:203]\n"
        "v_exp_f32 v62, v62\n"
        "v_mfma_f32_16x16x32_bf16 a[8:11], a[152:155], v[24:27], a[8:11]\n"
        "v_exp_f32 v63, v63\n"
        "v_mfma_f32_16x16x32_bf16 a[12:15], a[156:159], v[24:27], a[12:15]\n"
        "v_cvt_pk_bf16_f32 v31, v62, v63\n"
        "s_nop 1\n"
        "v_smfmac_f32_16x16x64_bf16 a[68:71], a[72:75], v[24:31], v154\n"
        "v_cndmask_b32 v60, v236, v242, s[62:63]\n"
        "v_mfma_f32_16x16x32_bf16 a[32:35], a[144:147], v[28:31], 0\n"
        "v_exp_f32 v32, v32\n"
        "v_mfma_f32_16x16x32_bf16 a[36:39], a[148:151], v[28:31], 0\n"
        "v_exp_f32 v33, v33\n"
        "v_mfma_f32_16x16x32_bf16 a[40:43], a[152:155], v[28:31], 0\n"
        "ds_read_b128 a[144:147], v241 offset:24576\n"
        "v_cvt_pk_bf16_f32 v80, v32, v33\n"
        "v_mfma_f32_16x16x32_bf16 a[44:47], a[156:159], v[28:31], 0\n"
        "v_exp_f32 v34, v34\n"
        "v_mfma_f32_16x16x32_bf16 v[204:207], a[156:159], v[20:23], v[204:207]\n"
        "v_exp_f32 v35, v35\n"
        "v_mfma_f32_16x16x32_bf16 v[172:175], a[156:159], v[16:19], v[172:175]\n"
        "s_mov_b32 exec_hi, 0x7fffffff\n"
        "global_load_lds_dwordx4 v254, s[70:71] offset:3072\n"
        "s_mov_b64 exec, -1\n"
        "v_cvt_pk_bf16_f32 v81, v34, v35\n"
        "v_exp_f32 v36, v36\n"
        "v_exp_f32 v37, v37\n"
        "v_cndmask_b32 v61, v236, v242, s[84:85]\n"
        "v_cvt_pk_bf16_f32 v84, v36, v37\n"
        "v_cndmask_b32 v62, v236, v242, s[86:87]\n"
        "v_cndmask_b32 v63, v236, v242, s[88:89]\n"
        "s_nop 1\n"
        "v_mfma_f32_16x16x32_bf16 v[60:63], a[124:127], v[140:143], v[60:63]\n"
        "v_exp_f32 v38, v38\n"
        "v_mfma_f32_16x16x32_bf16 v[60:63], a[120:123], v[136:139], v[60:63]\n"
        "v_exp_f32 v39, v39\n"
        "v_mfma_f32_16x16x32_bf16 v[60:63], a[116:119], v[132:135], v[60:63]\n"
        "ds_read_b128 a[116:119], v240 offset:5120\n"
        "v_cvt_pk_bf16_f32 v85, v38, v39\n"
        "v_mfma_f32_16x16x32_bf16 v[60:63], a[112:115], v[128:131], v[60:63]\n"
        "s_mov_b64 exec, 7\n"
        "global_load_lds_dword v252, s[70:71] offset:4080\n"
        "s_mov_b64 exec, -1\n"
        "s_add_u32 m0, s80, 0x0\n"
        "ds_read_b128 a[112:115], v240 offset:4096\n"
        "v_exp_f32 v40, v40\n"
        "v_exp_f32 v41, v41\n"
        "v_exp_f32 v42, v42\n"
        "v_cvt_pk_bf16_f32 v88, v40, v41\n"
        "v_exp_f32 v43, v43\n"
        "ds_read_b128 a[148:151], v241 offset:25600\n"
        "global_load_lds_dwordx4 v254, s[68:69] offset:0 sc1\n"
        "v_cvt_pk_bf16_f32 v89, v42, v43\n"
        "v_exp_f32 v44, v44\n"
        "v_exp_f32 v45, v45\n"
        "ds_read_b128 a[152:155], v241 offset:26624\n"
        "v_cvt_pk_bf16_f32 v92, v44, v45\n"
        "v_exp_f32 v46, v46\n"
        "v_exp_f32 v47, v47\n"
        "ds_read_b128 a[120:123], v240 offset:6144\n"
        "v_cvt_pk_bf16_f32 v93, v46, v47\n"
        "global_load_lds_dwordx4 v254, s[68:69] offset:1024 sc1\n"
        "global_load_lds_dwordx4 v254, s[68:69] offset:2048 sc1\n"
        "global_load_lds_dwordx4 v254, s[68:69] offset:3072 sc1\n"
        "s_cmp_eq_u32 s91, 0\n"
        "s_cbranch_scc1 LLD_%=\n"
        "; ---- DUAL\n"
        "s_branch LDU_%=\n"
        "LEVD0_%=:\n"
        "s_cmp_eq_u32 s82, s83\n"
        "s_cbranch_scc1 LE3D0_%=\n"
        "global_load_dwordx2 v[252:253], v155, s[48:49] sc1\n"
        "s_mov_b32 s24, s83\n"
        "v_accvgpr_read_b32 v246, a93\n"
        "global_load_dword v244, v246, s[46:47]\n"
        "s_branch LEWD0_%=\n"
        "LE3D0_%=:\n"
        "v_cmp_ne_u64 vcc, v[252:253], v[250:251]\n"
        "s_mov_b64 s[68:69], s[92:93]\n"
        "s_mov_b64 s[74:75], s[94:95]\n"
        "s_mov_b32 s80, s50\n"
        "s_mov_b32 s81, s51\n"
        "s_mov_b64 s[66:67], s[22:23]\n"
        "s_mov_b32 s90, s29\n"
        "s_mov_b32 s25, s30\n"
        "s_lshl_b32 s28, s28, 2\n"
        "s_xor_b32 s79, s73, 0x8000\n"
        "v_accvgpr_read_b32 v144, a84\n"
        "v_accvgpr_read_b32 v145, a85\n"
        "v_accvgpr_read_b32 v146, a86\n"
        "v_accvgpr_read_b32 v147, a87\n"
        "s_mov_b64 exec, 15\n"
        "ds_write_b128 v156, a[88:91]\n"
        "s_mov_b64 exec, -1\n"
        "s_cbranch_vccz LEWD0_%=\n"
        "s_mov_b32 s97, 0x200000\n"
        "LE4D0_%=:\n"
        "s_sleep 2\n"
        "global_load_dwordx2 v[252:253], v155, s[48:49] sc1\n"
        "s_waitcnt vmcnt(0)\n"
        "v_cmp_ne_u64 vcc, v[252:253], v[250:251]\n"
        "s_nop 1\n"
        "s_cbranch_vccz LEWD0_%=\n"
        "s_sub_u32 s97, s97, 1\n"
        "s_cmp_lg_u32 s97, 0\n"
        "s_cbranch_scc1 LE4D0_%=\n"
        "s_branch LEWD0_%=\n"
        "s_nop 0\n"
        "s_nop 0\n"
        "s_nop 0\n"
        "s_nop 0\n"
        "s_nop 0\n"
        "s_nop 0\n"
        "s_nop 0\n"
        "s_nop 0\n"
        "s_nop 0\n"
        "s_nop 0\n"
        "s_nop 0\n"
        "s_nop 0\n"
        "s_nop 0\n"
        "s_nop 0\n"
        "s_nop 0\n"
        "s_nop 0\n"
        "s_nop 0\n"
        "LHA11_%=:\n"
        "s_cbranch_execz LHRA_%=\n"
        "s_nop 0\n"
        "s_nop 0\n"
        "s_nop 0\n"
        "s_nop 0\n"
        "s_nop 0\n"
        "s_nop 0\n"
        "s_nop 0\n"
        "s_nop 0\n"
        "s_nop 0\n"
        "s_nop 0\n"
        "s_nop 0\n"
        "s_nop 0\n"
        "s_nop 0\n"
        "s_nop 0\n"
        "s_nop 0\n"
        "LDU_%=:\n"
        "v_mfma_f32_16x16x32_bf16 v[32:35], a[96:99], v[0:3], v[224:227]\n"
        "s_waitcnt lgkmcnt(4)\n"
        "ds_read_b128 a[156:159], v241 offset:27648\n"
        "s_add_u32 m0, s81, 0x0\n"
        "v_mfma_f32_16x16x32_bf16 v[32:35], a[100:103], v[4:7], v[32:35]\n"
        "ds_read_b128 a[124:127], v240 offset:7168\n"
        "global_load_lds_dwordx4 v254, s[74:75] offset:0 sc1\n"
        "s_xor_b32 s79, s79, 0x8000\n"
        "v_mfma_f32_16x16x32_bf16 v[32:35], a[104:107], v[8:11], v[32:35]\n"
        "v_exp_f32 v48, v48\n"
        "v_mfma_f32_16x16x32_bf16 v[32:35], a[108:111], v[12:15], v[32:35]\n"
        "v_exp_f32 v49, v49\n"
        "v_mfma_f32_16x16x32_bf16 v[36:39], a[108:111], v[76:79], v[228:231]\n"
        "v_exp_f32 v50, v50\n"
        "v_mfma_f32_16x16x32_bf16 v[36:39], a[104:107], v[72:75], v[36:39]\n"
        "v_exp_f32 v51, v51\n"
        "v_mfma_f32_16x16x32_bf16 v[36:39], a[100:103], v[68:71], v[36:39]\n"
        "v_exp_f32 v52, v52\n"
        "v_mfma_f32_16x16x32_bf16 v[36:39], a[96:99], v[64:67], v[36:39]\n"
        "v_exp_f32 v53, v53\n"
        "v_mfma_f32_16x16x32_bf16 v[40:43], a[96:99], v[96:99], v[232:235]\n"
        "v_exp_f32 v54, v54\n"
        "v_mfma_f32_16x16x32_bf16 v[40:43], a[100:103], v[100:103], v[40:43]\n"
        "v_exp_f32 v55, v55\n"
        "v_mfma_f32_16x16x32_bf16 v[40:43], a[104:107], v[104:107], v[40:43]\n"
        "v_exp_f32 v56, v56\n"
        "v_mfma_f32_16x16x32_bf16 v[40:43], a[108:111], v[108:111], v[40:43]\n"
        "v_exp_f32 v57, v57\n"
        "v_mfma_f32_16x16x32_bf16 v[44:47], a[108:111], v[140:143], v[236:239]\n"
        "ds_read_b128 a[108:111], v240 offset:11264\n"
        "v_cvt_pk_bf16_f32 v82, v48, v49\n"
        "v_mfma_f32_16x16x32_bf16 v[44:47], a[104:107], v[136:139], v[44:47]\n"
        "ds_read_b128 a[104:107], v240 offset:10240\n"
        "global_load_lds_dwordx4 v254, s[74:75] offset:1024 sc1\n"
        "v_mfma_f32_16x16x32_bf16 v[44:47], a[100:103], v[132:135], v[44:47]\n"
        "ds_read_b128 a[100:103], v240 offset:9216\n"
        "v_cvt_pk_bf16_f32 v83, v50, v51\n"
        "v_mfma_f32_16x16x32_bf16 v[44:47], a[96:99], v[128:131], v[44:47]\n"
        "ds_read_b128 a[96:99], v240 offset:8192\n"
        "v_cvt_pk_bf16_f32 v86, v52, v53\n"
        "v_mfma_f32_16x16x32_bf16 v[176:179], a[128:131], v[16:19], v[176:179]\n"
        "v_exp_f32 v58, v58\n"
        "v_mfma_f32_16x16x32_bf16 v[208:211], a[128:131], v[20:23], v[208:211]\n"
        "v_exp_f32 v59, v59\n"
        "v_mfma_f32_16x16x32_bf16 a[16:19], a[128:131], v[24:27], a[16:19]\n"
        "v_exp_f32 v60, v60\n"
        "v_mfma_f32_16x16x32_bf16 a[48:51], a[128:131], v[28:31], a[48:51]\n"
        "ds_read_b128 a[128:131], v241 offset:28672\n"
        "global_load_lds_dwordx4 v254, s[74:75] offset:2048 sc1\n"
        "v_mfma_f32_16x16x32_bf16 a[52:55], a[132:135], v[28:31], a[52:55]\n"
        "v_exp_f32 v61, v61\n"
        "v_mfma_f32_16x16x32_bf16 a[20:23], a[132:135], v[24:27], a[20:23]\n"
        "v_exp_f32 v62, v62\n"
        "v_mfma_f32_16x16x32_bf16 v[212:215], a[132:135], v[20:23], v[212:215]\n"
        "v_exp_f32 v63, v63\n"
        "v_mfma_f32_16x16x32_bf16 v[180:183], a[132:135], v[16:19], v[180:183]\n"
        "ds_read_b128 a[132:135], v241 offset:29696\n"
        "global_load_lds_dwordx4 v254, s[74:75] offset:3072 sc1\n"
        "s_add_u32 m0, s79, 0x0\n"
        "v_mfma_f32_16x16x32_bf16 v[184:187], a[136:139], v[16:19], v[184:187]\n"
        "v_exp_f32 v32, v32\n"
        "v_mfma_f32_16x16x32_bf16 v[216:219], a[136:139], v[20:23], v[216:219]\n"
        "v_exp_f32 v33, v33\n"
        "v_mfma_f32_16x16x32_bf16 a[24:27], a[136:139], v[24:27], a[24:27]\n"
        "v_exp_f32 v34, v34\n"
        "v_mfma_f32_16x16x32_bf16 a[56:59], a[136:139], v[28:31], a[56:59]\n"
        "ds_read_b128 a[136:139], v241 offset:30720\n"
        "v_cvt_pk_bf16_f32 v87, v54, v55\n"
        "v_mfma_f32_16x16x32_bf16 a[60:63], a[140:143], v[28:31], a[60:63]\n"
        "s_add_u32 s82, s82, 1\n"
        "v_exp_f32 v35, v35\n"
        "v_mfma_f32_16x16x32_bf16 a[28:31], a[140:143], v[24:27], a[28:31]\n"
        "s_xor_b32 s78, s78, 0x8000\n"
        "v_exp_f32 v36, v36\n"
        "v_mfma_f32_16x16x32_bf16 v[220:223], a[140:143], v[20:23], v[220:223]\n"
        "s_waitcnt lgkmcnt(2)\n"
        "v_exp_f32 v37, v37\n"
        "v_mfma_f32_16x16x32_bf16 v[188:191], a[140:143], v[16:19], v[188:191]\n"
        "ds_read_b128 a[140:143], v241 offset:31744\n"
        "s_add_u32 s81, s78, 4096\n"
        "v_cvt_pk_bf16_f32 v90, v56, v57\n"
        "v_mfma_f32_16x16x32_bf16 v[48:51], a[112:115], v[0:3], v[224:227]\n"
        "s_mov_b32 s80, s78\n"
        "v_exp_f32 v38, v38\n"
        "v_mfma_f32_16x16x32_bf16 v[48:51], a[116:119], v[4:7], v[48:51]\n"
        "v_exp_f32 v39, v39\n"
        "v_mfma_f32_16x16x32_bf16 v[48:51], a[120:123], v[8:11], v[48:51]\n"
        "v_exp_f32 v40, v40\n"
        "v_mfma_f32_16x16x32_bf16 v[48:51], a[124:127], v[12:15], v[48:51]\n"
        "v_exp_f32 v41, v41\n"
        "v_mfma_f32_16x16x32_bf16 v[52:55], a[124:127], v[76:79], v[228:231]\n"
        "v_exp_f32 v42, v42\n"
        "v_mfma_f32_16x16x32_bf16 v[52:55], a[120:123], v[72:75], v[52:55]\n"
        "v_exp_f32 v43, v43\n"
        "v_mfma_f32_16x16x32_bf16 v[52:55], a[116:119], v[68:71], v[52:55]\n"
        "v_exp_f32 v44, v44\n"
        "v_mfma_f32_16x16x32_bf16 v[52:55], a[112:115], v[64:67], v[52:55]\n"
        "s_waitcnt vmcnt(10) lgkmcnt(0)\n"
        "v_exp_f32 v45, v45\n"
        "v_smfmac_f32_16x16x64_bf16 a[64:67], a[72:75], v[80:87], v154\n"
        "s_barrier\n"
        "v_mfma_f32_16x16x32_bf16 v[160:163], a[144:147], v[80:83], v[160:163]\n"
        "v_exp_f32 v46, v46\n"
        "v_mfma_f32_16x16x32_bf16 v[192:195], a[144:147], v[84:87], v[192:195]\n"
        "v_exp_f32 v47, v47\n"
        "v_mfma_f32_16x16x32_bf16 v[196:199], a[148:151], v[84:87], v[196:199]\n"
        "v_exp_f32 v48, v48\n"
        "v_mfma_f32_16x16x32_bf16 v[164:167], a[148:151], v[80:83], v[164:167]\n"
        "v_exp_f32 v49, v49\n"
        "v_mfma_f32_16x16x32_bf16 v[168:171], a[152:155], v[80:83], v[168:171]\n"
        "v_exp_f32 v50, v50\n"
        "v_mfma_f32_16x16x32_bf16 v[200:203], a[152:155], v[84:87], v[200:203]\n"
        "v_exp_f32 v51, v51\n"
        "v_mfma_f32_16x16x32_bf16 v[204:207], a[156:159], v[84:87], v[204:207]\n"
        "v_exp_f32 v52, v52\n"
        "v_mfma_f32_16x16x32_bf16 v[172:175], a[156:159], v[80:83], v[172:175]\n"
        "v_exp_f32 v53, v53\n"
        "v_mfma_f32_16x16x32_bf16 v[176:179], a[128:131], v[80:83], v[176:179]\n"
        "v_exp_f32 v54, v54\n"
        "v_mfma_f32_16x16x32_bf16 v[208:211], a[128:131], v[84:87], v[208:211]\n"
        "v_exp_f32 v55, v55\n"
        "v_cvt_pk_bf16_f32 v91, v58, v59\n"
        "v_mfma_f32_16x16x32_bf16 v[56:59], a[112:115], v[96:99], v[232:235]\n"
        "s_add_u32 s76, s66, s90\n"
        "s_addc_u32 s77, s67, 0\n"
        "v_cvt_pk_bf16_f32 v94, v60, v61\n"
        "v_mfma_f32_16x16x32_bf16 v[56:59], a[116:119], v[100:103], v[56:59]\n"
        "global_load_lds_dwordx4 v144, s[76:77] offset:0 sc1\n"
        "v_cvt_pk_bf16_f32 v95, v62, v63\n"
        "v_mfma_f32_16x16x32_bf16 v[56:59], a[120:123], v[104:107], v[56:59]\n"
        "s_cmp_ge_u32 s82, s44\n"
        "s_addc_u32 s90, s82, 0\n"
        "v_cvt_pk_bf16_f32 v16, v32, v33\n"
        "v_mfma_f32_16x16x32_bf16 v[56:59], a[124:127], v[108:111], v[56:59]\n"
        "s_lshl_b32 s90, s90, 15\n"
        "v_cvt_pk_bf16_f32 v17, v34, v35\n"
        "v_cvt_pk_bf16_f32 v20, v36, v37\n"
        "v_mfma_f32_16x16x32_bf16 v[60:63], a[124:127], v[140:143], v[236:239]\n"
        "ds_read_b128 a[124:127], v240 offset:15360\n"
        "v_cvt_pk_bf16_f32 v21, v38, v39\n"
        "v_mfma_f32_16x16x32_bf16 v[60:63], a[120:123], v[136:139], v[60:63]\n"
        "ds_read_b128 a[120:123], v240 offset:14336\n"
        "v_cvt_pk_bf16_f32 v24, v40, v41\n"
        "v_mfma_f32_16x16x32_bf16 v[60:63], a[116:119], v[132:135], v[60:63]\n"
        "ds_read_b128 a[116:119], v240 offset:13312\n"
        "v_xor_b32 v241, 0x8000, v241\n"
        "v_mfma_f32_16x16x32_bf16 v[60:63], a[112:115], v[128:131], v[60:63]\n"
        "ds_read_b128 a[112:115], v240 offset:12288\n"
        "v_cvt_pk_bf16_f32 v25, v42, v43\n"
        "v_smfmac_f32_16x16x64_bf16 a[68:71], a[72:75], v[88:95], v154\n"
        "global_load_lds_dwordx4 v145, s[76:77] offset:1024 sc1\n"
        "v_cvt_pk_bf16_f32 v28, v44, v45\n"
        "v_mfma_f32_16x16x32_bf16 a[0:3], a[144:147], v[88:91], a[0:3]\n"
        "v_exp_f32 v56, v56\n"
        "v_mfma_f32_16x16x32_bf16 a[32:35], a[144:147], v[92:95], a[32:35]\n"
        "ds_read_b128 a[144:147], v241 offset:0\n"
        "v_cvt_pk_bf16_f32 v29, v46, v47\n"
        "v_mfma_f32_16x16x32_bf16 a[36:39], a[148:151], v[92:95], a[36:39]\n"
        "v_exp_f32 v57, v57\n"
        "v_mfma_f32_16x16x32_bf16 a[4:7], a[148:151], v[88:91], a[4:7]\n"
        "ds_read_b128 a[148:151], v241 offset:1024\n"
        "global_load_lds_dwordx4 v146, s[76:77] offset:2048 sc1\n"
        "v_mfma_f32_16x16x32_bf16 a[8:11], a[152:155], v[88:91], a[8:11]\n"
        "v_exp_f32 v58, v58\n"
        "v_mfma_f32_16x16x32_bf16 a[40:43], a[152:155], v[92:95], a[40:43]\n"
        "ds_read_b128 a[152:155], v241 offset:2048\n"
        "v_cvt_pk_bf16_f32 v18, v48, v49\n"
        "v_mfma_f32_16x16x32_bf16 a[44:47], a[156:159], v[92:95], a[44:47]\n"
        "v_exp_f32 v59, v59\n"
        "v_mfma_f32_16x16x32_bf16 a[12:15], a[156:159], v[88:91], a[12:15]\n"
        "ds_read_b128 a[156:159], v241 offset:3072\n"
        "global_load_lds_dwordx4 v147, s[76:77] offset:3072 sc1\n"
        "s_add_u32 m0, s79, s28\n"
        "v_mfma_f32_16x16x32_bf16 v[32:35], a[96:99], v[0:3], v[224:227]\n"
        "s_sub_u32 s91, s91, 1\n"
        "v_exp_f32 v60, v60\n"
        "v_mfma_f32_16x16x32_bf16 v[32:35], a[100:103], v[4:7], v[32:35]\n"
        "v_exp_f32 v61, v61\n"
        "v_mfma_f32_16x16x32_bf16 v[32:35], a[104:107], v[8:11], v[32:35]\n"
        "v_exp_f32 v62, v62\n"
        "v_mfma_f32_16x16x32_bf16 v[32:35], a[108:111], v[12:15], v[32:35]\n"
        "v_exp_f32 v63, v63\n"
        "v_mfma_f32_16x16x32_bf16 v[36:39], a[108:111], v[76:79], v[228:231]\n"
        "s_add_u32 s76, s76, s25\n"
        "s_addc_u32 s77, s77, 0\n"
        "v_cvt_pk_bf16_f32 v19, v50, v51\n"
        "v_mfma_f32_16x16x32_bf16 v[36:39], a[104:107], v[72:75], v[36:39]\n"
        "global_load_lds_dwordx4 v144, s[76:77] offset:0 sc1\n"
        "v_cvt_pk_bf16_f32 v22, v52, v53\n"
        "v_mfma_f32_16x16x32_bf16 v[36:39], a[100:103], v[68:71], v[36:39]\n"
        "v_exp_f32 v32, v32\n"
        "v_mfma_f32_16x16x32_bf16 v[36:39], a[96:99], v[64:67], v[36:39]\n"
        "v_exp_f32 v33, v33\n"
        "v_mfma_f32_16x16x32_bf16 v[40:43], a[96:99], v[96:99], v[232:235]\n"
        "v_exp_f32 v34, v34\n"
        "v_mfma_f32_16x16x32_bf16 v[40:43], a[100:103], v[100:103], v[40:43]\n"
        "v_exp_f32 v35, v35\n"
        "v_mfma_f32_16x16x32_bf16 v[40:43], a[104:107], v[104:107], v[40:43]\n"
        "global_load_lds_dwordx4 v145, s[76:77] offset:1024 sc1\n"
        "v_cvt_pk_bf16_f32 v23, v54, v55\n"
        "v_mfma_f32_16x16x32_bf16 v[40:43], a[108:111], v[108:111], v[40:43]\n"
        "v_exp_f32 v36, v36\n"
        "v_mfma_f32_16x16x32_bf16 v[44:47], a[108:111], v[140:143], v[236:239]\n"
        "ds_read_b128 a[108:111], v240 offset:19456\n"
        "v_cvt_pk_bf16_f32 v26, v56, v57\n"
        "v_mfma_f32_16x16x32_bf16 v[44:47], a[104:107], v[136:139], v[44:47]\n"
        "ds_read_b128 a[104:107], v240 offset:18432\n"
        "v_cvt_pk_bf16_f32 v27, v58, v59\n"
        "v_mfma_f32_16x16x32_bf16 v[44:47], a[100:103], v[132:135], v[44:47]\n"
        "ds_read_b128 a[100:103], v240 offset:17408\n"
        "global_load_lds_dwordx4 v146, s[76:77] offset:2048 sc1\n"
        "v_mfma_f32_16x16x32_bf16 v[44:47], a[96:99], v[128:131], v[44:47]\n"
        "ds_read_b128 a[96:99], v240 offset:16384\n"
        "v_cvt_pk_bf16_f32 v30, v60, v61\n"
        "v_mfma_f32_16x16x32_bf16 a[16:19], a[128:131], v[88:91], a[16:19]\n"
        "v_exp_f32 v37, v37\n"
        "v_mfma_f32_16x16x32_bf16 a[48:51], a[128:131], v[92:95], a[48:51]\n"
        "ds_read_b128 a[128:131], v241 offset:4096\n"
        "v_cvt_pk_bf16_f32 v31, v62, v63\n"
        "v_mfma_f32_16x16x32_bf16 a[52:55], a[132:135], v[92:95], a[52:55]\n"
        "v_exp_f32 v38, v38\n"
        "v_mfma_f32_16x16x32_bf16 a[20:23], a[132:135], v[88:91], a[20:23]\n"
        "v_exp_f32 v39, v39\n"
        "v_mfma_f32_16x16x32_bf16 v[212:215], a[132:135], v[84:87], v[212:215]\n"
        "v_exp_f32 v40, v40\n"
        "v_mfma_f32_16x16x32_bf16 v[180:183], a[132:135], v[80:83], v[180:183]\n"
        "ds_read_b128 a[132:135], v241 offset:5120\n"
        "global_load_lds_dwordx4 v147, s[76:77] offset:3072 sc1\n"
        "v_mfma_f32_16x16x32_bf16 v[184:187], a[136:139], v[80:83], v[184:187]\n"
        "v_exp_f32 v41, v41\n"
        "v_mfma_f32_16x16x32_bf16 v[216:219], a[136:139], v[84:87], v[216:219]\n"
        "v_exp_f32 v42, v42\n"
        "v_mfma_f32_16x16x32_bf16 a[24:27], a[136:139], v[88:91], a[24:27]\n"
        "v_exp_f32 v43, v43\n"
        "v_mfma_f32_16x16x32_bf16 a[56:59], a[136:139], v[92:95], a[56:59]\n"
        "ds_read_b128 a[136:139], v241 offset:6144\n"
        "s_add_u32 s68, s64, s90\n"
        "s_addc_u32 s69, s65, 0\n"
        "v_mfma_f32_16x16x32_bf16 a[60:63], a[140:143], v[92:95], a[60:63]\n"
        "s_waitcnt lgkmcnt(2)\n"
        "v_exp_f32 v44, v44\n"
        "v_mfma_f32_16x16x32_bf16 a[28:31], a[140:143], v[88:91], a[28:31]\n"
        "v_exp_f32 v45, v45\n"
        "v_mfma_f32_16x16x32_bf16 v[220:223], a[140:143], v[84:87], v[220:223]\n"
        "v_exp_f32 v46, v46\n"
        "v_mfma_f32_16x16x32_bf16 v[188:191], a[140:143], v[80:83], v[188:191]\n"
        "ds_read_b128 a[140:143], v241 offset:7168\n"
        "global_load_dword v244, v245, s[26:27] offset:0\n"
        "v_mfma_f32_16x16x32_bf16 v[48:51], a[112:115], v[0:3], v[224:227]\n"
        "v_exp_f32 v47, v47\n"
        "v_mfma_f32_16x16x32_bf16 v[48:51], a[116:119], v[4:7], v[48:51]\n"
        "s_add_u32 s26, s26, 0x20000\n"
        "s_addc_u32 s27, s27, 0\n"
        "v_cvt_pk_bf16_f32 v80, v32, v33\n"
        "v_mfma_f32_16x16x32_bf16 v[48:51], a[120:123], v[8:11], v[48:51]\n"
        "s_add_u32 s74, s68, 0x1000\n"
        "s_addc_u32 s75, s69, 0\n"
        "v_cvt_pk_bf16_f32 v81, v34, v35\n"
        "v_mfma_f32_16x16x32_bf16 v[48:51], a[124:127], v[12:15], v[48:51]\n"
        "v_cvt_pk_bf16_f32 v84, v36, v37\n"
        "v_cvt_pk_bf16_f32 v85, v38, v39\n"
        "v_mfma_f32_16x16x32_bf16 v[52:55], a[124:127], v[76:79], v[228:231]\n"
        "v_cvt_pk_bf16_f32 v88, v40, v41\n"
        "v_cvt_pk_bf16_f32 v89, v42, v43\n"
        "v_mfma_f32_16x16x32_bf16 v[52:55], a[120:123], v[72:75], v[52:55]\n"
        "v_cvt_pk_bf16_f32 v92, v44, v45\n"
        "v_cvt_pk_bf16_f32 v93, v46, v47\n"
        "v_mfma_f32_16x16x32_bf16 v[52:55], a[116:119], v[68:71], v[52:55]\n"
        "v_exp_f32 v48, v48\n"
        "v_mfma_f32_16x16x32_bf16 v[52:55], a[112:115], v[64:67], v[52:55]\n"
        "v_exp_f32 v49, v49\n"
        "v_mfma_f32_16x16x32_bf16 v[56:59], a[112:115], v[96:99], v[232:235]\n"
        "v_exp_f32 v50, v50\n"
        "v_mfma_f32_16x16x32_bf16 v[56:59], a[116:119], v[100:103], v[56:59]\n"
        "v_exp_f32 v51, v51\n"
        "v_mfma_f32_16x16x32_bf16 v[56:59], a[120:123], v[104:107], v[56:59]\n"
        "v_cvt_pk_bf16_f32 v82, v48, v49\n"
        "v_cvt_pk_bf16_f32 v83, v50, v51\n"
        "v_mfma_f32_16x16x32_bf16 v[56:59], a[124:127], v[108:111], v[56:59]\n"
        "v_exp_f32 v52, v52\n"
        "v_mfma_f32_16x16x32_bf16 v[60:63], a[124:127], v[140:143], v[236:239]\n"
        "ds_read_b128 a[124:127], v240 offset:23552\n"
        "v_mfma_f32_16x16x32_bf16 v[60:63], a[120:123], v[136:139], v[60:63]\n"
        "ds_read_b128 a[120:123], v240 offset:22528\n"
        "v_mfma_f32_16x16x32_bf16 v[60:63], a[116:119], v[132:135], v[60:63]\n"
        "ds_read_b128 a[116:119], v240 offset:21504\n"
        "v_mfma_f32_16x16x32_bf16 v[60:63], a[112:115], v[128:131], v[60:63]\n"
        "ds_read_b128 a[112:115], v240 offset:20480\n"
        "v_smfmac_f32_16x16x64_bf16 a[64:67], a[72:75], v[16:23], v154\n"
        "v_smfmac_f32_16x16x64_bf16 a[68:71], a[72:75], v[24:31], v154\n"
        "v_mfma_f32_16x16x32_bf16 v[160:163], a[144:147], v[16:19], v[160:163]\n"
        "v_exp_f32 v53, v53\n"
        "v_mfma_f32_16x16x32_bf16 v[192:195], a[144:147], v[20:23], v[192:195]\n"
        "v_exp_f32 v54, v54\n"
        "v_mfma_f32_16x16x32_bf16 a[0:3], a[144:147], v[24:27], a[0:3]\n"
        "v_exp_f32 v55, v55\n"
        "v_mfma_f32_16x16x32_bf16 a[32:35], a[144:147], v[28:31], a[32:35]\n"
        "ds_read_b128 a[144:147], v241 offset:8192\n"
        "v_cvt_pk_bf16_f32 v86, v52, v53\n"
        "v_mfma_f32_16x16x32_bf16 a[36:39], a[148:151], v[28:31], a[36:39]\n"
        "v_exp_f32 v56, v56\n"
        "v_mfma_f32_16x16x32_bf16 a[4:7], a[148:151], v[24:27], a[4:7]\n"
        "v_exp_f32 v57, v57\n"
        "v_mfma_f32_16x16x32_bf16 v[196:199], a[148:151], v[20:23], v[196:199]\n"
        "v_exp_f32 v58, v58\n"
        "v_mfma_f32_16x16x32_bf16 v[164:167], a[148:151], v[16:19], v[164:167]\n"
        "ds_read_b128 a[148:151], v241 offset:9216\n"
        "v_cvt_pk_bf16_f32 v87, v54, v55\n"
        "v_mfma_f32_16x16x32_bf16 v[168:171], a[152:155], v[16:19], v[168:171]\n"
        "v_exp_f32 v59, v59\n"
        "v_mfma_f32_16x16x32_bf16 v[200:203], a[152:155], v[20:23], v[200:203]\n"
        "v_exp_f32 v60, v60\n"
        "v_mfma_f32_16x16x32_bf16 a[8:11], a[152:155], v[24:27], a[8:11]\n"
        "v_exp_f32 v61, v61\n"
        "v_mfma_f32_16x16x32_bf16 a[40:43], a[152:155], v[28:31], a[40:43]\n"
        "ds_read_b128 a[152:155], v241 offset:10240\n"
        "v_cvt_pk_bf16_f32 v90, v56, v57\n"
        "v_mfma_f32_16x16x32_bf16 a[44:47], a[156:159], v[28:31], a[44:47]\n"
        "v_exp_f32 v62, v62\n"
        "v_mfma_f32_16x16x32_bf16 a[12:15], a[156:159], v[24:27], a[12:15]\n"
        "v_exp_f32 v63, v63\n"
        "v_mfma_f32_16x16x32_bf16 v[204:207], a[156:159], v[20:23], v[204:207]\n"
        "v_cvt_pk_bf16_f32 v91, v58, v59\n"
        "v_cvt_pk_bf16_f32 v94, v60, v61\n"
        "v_mfma_f32_16x16x32_bf16 v[172:175], a[156:159], v[16:19], v[172:175]\n"
        "ds_read_b128 a[156:159], v241 offset:11264\n"
        "v_cvt_pk_bf16_f32 v95, v62, v63\n"
        "v_mfma_f32_16x16x32_bf16 v[32:35], a[96:99], v[0:3], v[224:227]\n"
        "v_mfma_f32_16x16x32_bf16 v[32:35], a[100:103], v[4:7], v[32:35]\n"
        "v_mfma_f32_16x16x32_bf16 v[32:35], a[104:107], v[8:11], v[32:35]\n"
        "v_mfma_f32_16x16x32_bf16 v[32:35], a[108:111], v[12:15], v[32:35]\n"
        "v_mfma_f32_16x16x32_bf16 v[36:39], a[108:111], v[76:79], v[228:231]\n"
        "v_mfma_f32_16x16x32_bf16 v[36:39], a[104:107], v[72:75], v[36:39]\n"
        "v_mfma_f32_16x16x32_bf16 v[36:39], a[100:103], v[68:71], v[36:39]\n"
        "v_mfma_f32_16x16x32_bf16 v[36:39], a[96:99], v[64:67], v[36:39]\n"
        "v_mfma_f32_16x16x32_bf16 v[40:43], a[96:99], v[96:99], v[232:235]\n"
        "v_mfma_f32_16x16x32_bf16 v[40:43], a[100:103], v[100:103], v[40:43]\n"
        "v_mfma_f32_16x16x32_bf16 v[40:43], a[104:107], v[104:107], v[40:43]\n"
        "v_mfma_f32_16x16x32_bf16 v[40:43], a[108:111], v[108:111], v[40:43]\n"
        "v_exp_f32 v32, v32\n"
        "v_mfma_f32_16x16x32_bf16 v[44:47], a[108:111], v[140:143], v[236:239]\n"
        "v_exp_f32 v33, v33\n"
        "v_mfma_f32_16x16x32_bf16 v[44:47], a[104:107], v[136:139], v[44:47]\n"
        "ds_read_b128 a[104:107], v240 offset:26624\n"
        "v_mfma_f32_16x16x32_bf16 v[44:47], a[100:103], v[132:135], v[44:47]\n"
        "ds_read_b128 a[100:103], v240 offset:25600\n"
        "v_mfma_f32_16x16x32_bf16 v[44:47], a[96:99], v[128:131], v[44:47]\n"
        "ds_read_b128 a[96:99], v240 offset:24576\n"
        "v_mfma_f32_16x16x32_bf16 v[176:179], a[128:131], v[16:19], v[176:179]\n"
        "s_waitcnt lgkmcnt(3)\n"
        "v_exp_f32 v34, v34\n"
        "v_mfma_f32_16x16x32_bf16 v[208:211], a[128:131], v[20:23], v[208:211]\n"
        "v_exp_f32 v35, v35\n"
        "v_mfma_f32_16x16x32_bf16 a[16:19], a[128:131], v[24:27], a[16:19]\n"
        "ds_read_b128 a[108:111], v240 offset:27648\n"
        "v_mfma_f32_16x16x32_bf16 a[48:51], a[128:131], v[28:31], a[48:51]\n"
        "ds_read_b128 a[128:131], v241 offset:12288\n"
        "v_mfma_f32_16x16x32_bf16 a[52:55], a[132:135], v[28:31], a[52:55]\n"
        "v_exp_f32 v36, v36\n"
        "v_mfma_f32_16x16x32_bf16 a[20:23], a[132:135], v[24:27], a[20:23]\n"
        "v_exp_f32 v37, v37\n"
        "v_mfma_f32_16x16x32_bf16 v[212:215], a[132:135], v[20:23], v[212:215]\n"
        "v_exp_f32 v38, v38\n"
        "v_mfma_f32_16x16x32_bf16 v[180:183], a[132:135], v[16:19], v[180:183]\n"
        "ds_read_b128 a[132:135], v241 offset:13312\n"
        "v_mfma_f32_16x16x32_bf16 v[184:187], a[136:139], v[16:19], v[184:187]\n"
        "v_exp_f32 v39, v39\n"
        "v_mfma_f32_16x16x32_bf16 v[216:219], a[136:139], v[20:23], v[216:219]\n"
        "v_exp_f32 v40, v40\n"
        "v_mfma_f32_16x16x32_bf16 a[24:27], a[136:139], v[24:27], a[24:27]\n"
        "v_exp_f32 v41, v41\n"
        "v_mfma_f32_16x16x32_bf16 a[56:59], a[136:139], v[28:31], a[56:59]\n"
        "ds_read_b128 a[136:139], v241 offset:14336\n"
        "v_mfma_f32_16x16x32_bf16 a[60:63], a[140:143], v[28:31], a[60:63]\n"
        "v_exp_f32 v42, v42\n"
        "v_mfma_f32_16x16x32_bf16 a[28:31], a[140:143], v[24:27], a[28:31]\n"
        "v_exp_f32 v43, v43\n"
        "v_mfma_f32_16x16x32_bf16 v[220:223], a[140:143], v[20:23], v[220:223]\n"
        "v_exp_f32 v44, v44\n"
        "v_mfma_f32_16x16x32_bf16 v[188:191], a[140:143], v[16:19], v[188:191]\n"
        "ds_read_b128 a[140:143], v241 offset:15360\n"
        "global_load_dword v244, v245, s[26:27] offset:0\n"
        "v_mfma_f32_16x16x32_bf16 v[48:51], a[112:115], v[0:3], v[224:227]\n"
        "v_exp_f32 v45, v45\n"
        "v_mfma_f32_16x16x32_bf16 v[48:51], a[116:119], v[4:7], v[48:51]\n"
        "v_exp_f32 v46, v46\n"
        "v_mfma_f32_16x16x32_bf16 v[48:51], a[120:123], v[8:11], v[48:51]\n"
        "v_exp_f32 v47, v47\n"
        "v_mfma_f32_16x16x32_bf16 v[48:51], a[124:127], v[12:15], v[48:51]\n"
        "s_add_u32 s26, s26, 0x20000\n"
        "s_addc_u32 s27, s27, 0\n"
        "v_cvt_pk_bf16_f32 v16, v32, v33\n"
        "v_mfma_f32_16x16x32_bf16 v[52:55], a[124:127], v[76:79], v[228:231]\n"
        "v_cvt_pk_bf16_f32 v17, v34, v35\n"
        "v_cvt_pk_bf16_f32 v20, v36, v37\n"
        "v_mfma_f32_16x16x32_bf16 v[52:55], a[120:123], v[72:75], v[52:55]\n"
        "v_cvt_pk_bf16_f32 v21, v38, v39\n"
        "v_cvt_pk_bf16_f32 v24, v40, v41\n"
        "v_mfma_f32_16x16x32_bf16 v[52:55], a[116:119], v[68:71], v[52:55]\n"
        "v_exp_f32 v48, v48\n"
        "v_mfma_f32_16x16x32_bf16 v[52:55], a[112:115], v[64:67], v[52:55]\n"
        "v_exp_f32 v49, v49\n"
        "v_mfma_f32_16x16x32_bf16 v[56:59], a[112:115], v[96:99], v[232:235]\n"
        "v_exp_f32 v50, v50\n"
        "v_mfma_f32_16x16x32_bf16 v[56:59], a[116:119], v[100:103], v[56:59]\n"
        "v_exp_f32 v51, v51\n"
        "v_mfma_f32_16x16x32_bf16 v[56:59], a[120:123], v[104:107], v[56:59]\n"
        "v_cvt_pk_bf16_f32 v25, v42, v43\n"
        "v_cvt_pk_bf16_f32 v28, v44, v45\n"
        "v_mfma_f32_16x16x32_bf16 v[56:59], a[124:127], v[108:111], v[56:59]\n"
        "v_exp_f32 v52, v52\n"
        "v_mfma_f32_16x16x32_bf16 v[60:63], a[124:127], v[140:143], v[236:239]\n"
        "ds_read_b128 a[124:127], v240 offset:31744\n"
        "v_cvt_pk_bf16_f32 v29, v46, v47\n"
        "v_mfma_f32_16x16x32_bf16 v[60:63], a[120:123], v[136:139], v[60:63]\n"
        "ds_read_b128 a[120:123], v240 offset:30720\n"
        "v_cvt_pk_bf16_f32 v18, v48, v49\n"
        "v_mfma_f32_16x16x32_bf16 v[60:63], a[116:119], v[132:135], v[60:63]\n"
        "ds_read_b128 a[116:119], v240 offset:29696\n"
        "v_cvt_pk_bf16_f32 v19, v50, v51\n"
        "v_mfma_f32_16x16x32_bf16 v[60:63], a[112:115], v[128:131], v[60:63]\n"
        "ds_read_b128 a[112:115], v240 offset:28672\n"
        "v_xor_b32 v240, 0x8000, v240\n"
        "v_smfmac_f32_16x16x64_bf16 a[64:67], a[72:75], v[80:87], v154\n"
        "v_smfmac_f32_16x16x64_bf16 a[68:71], a[72:75], v[88:95], v154\n"
        "v_mfma_f32_16x16x32_bf16 v[160:163], a[144:147], v[80:83], v[160:163]\n"
        "v_exp_f32 v53, v53\n"
        "v_mfma_f32_16x16x32_bf16 v[192:195], a[144:147], v[84:87], v[192:195]\n"
        "v_exp_f32 v54, v54\n"
        "v_mfma_f32_16x16x32_bf16 a[0:3], a[144:147], v[88:91], a[0:3]\n"
        "v_exp_f32 v55, v55\n"
        "v_mfma_f32_16x16x32_bf16 a[32:35], a[144:147], v[92:95], a[32:35]\n"
        "ds_read_b128 a[144:147], v241 offset:16384\n"
        "v_cvt_pk_bf16_f32 v22, v52, v53\n"
        "v_mfma_f32_16x16x32_bf16 a[36:39], a[148:151], v[92:95], a[36:39]\n"
        "v_exp_f32 v56, v56\n"
        "v_mfma_f32_16x16x32_bf16 a[4:7], a[148:151], v[88:91], a[4:7]\n"
        "s_waitcnt vmcnt(10) lgkmcnt(1)\n"
        "v_exp_f32 v57, v57\n"
        "v_mfma_f32_16x16x32_bf16 v[196:199], a[148:151], v[84:87], v[196:199]\n"
        "s_barrier\n"
        "v_mfma_f32_16x16x32_bf16 v[164:167], a[148:151], v[80:83], v[164:167]\n"
        "ds_read_b128 a[148:151], v241 offset:17408\n"
        "v_cvt_pk_bf16_f32 v23, v54, v55\n"
        "v_mfma_f32_16x16x32_bf16 v[168:171], a[152:155], v[80:83], v[168:171]\n"
        "v_exp_f32 v58, v58\n"
        "v_mfma_f32_16x16x32_bf16 v[200:203], a[152:155], v[84:87], v[200:203]\n"
        "v_exp_f32 v59, v59\n"
        "v_mfma_f32_16x16x32_bf16 a[8:11], a[152:155], v[88:91], a[8:11]\n"
        "v_exp_f32 v60, v60\n"
        "v_mfma_f32_16x16x32_bf16 a[40:43], a[152:155], v[92:95], a[40:43]\n"
        "ds_read_b128 a[152:155], v241 offset:18432\n"
        "s_cmp_eq_u32 s82, s24\n"
        "s_cbranch_scc1 LEVD0_%=\n"
        "LEWD0_%=:\n"
        "v_mfma_f32_16x16x32_bf16 a[44:47], a[156:159], v[92:95], a[44:47]\n"
        "s_add_u32 m0, s80, 0x0\n"
        "v_exp_f32 v61, v61\n"
        "v_mfma_f32_16x16x32_bf16 a[12:15], a[156:159], v[88:91], a[12:15]\n"
        "v_exp_f32 v62, v62\n"
        "v_mfma_f32_16x16x32_bf16 v[204:207], a[156:159], v[84:87], v[204:207]\n"
        "v_exp_f32 v63, v63\n"
        "v_mfma_f32_16x16x32_bf16 v[172:175], a[156:159], v[80:83], v[172:175]\n"
        "ds_read_b128 a[156:159], v241 offset:19456\n"
        "global_load_lds_dwordx4 v254, s[68:69] offset:0 sc1\n"
        "v_mfma_f32_16x16x32_bf16 v[32:35], a[96:99], v[0:3], v[224:227]\n"
        "v_cvt_pk_bf16_f32 v26, v56, v57\n"
        "v_cvt_pk_bf16_f32 v27, v58, v59\n"
        "v_mfma_f32_16x16x32_bf16 v[32:35], a[100:103], v[4:7], v[32:35]\n"
        "v_cvt_pk_bf16_f32 v30, v60, v61\n"
        "v_cvt_pk_bf16_f32 v31, v62, v63\n"
        "v_mfma_f32_16x16x32_bf16 v[32:35], a[104:107], v[8:11], v[32:35]\n"
        "v_mfma_f32_16x16x32_bf16 v[32:35], a[108:111], v[12:15], v[32:35]\n"
        "global_load_lds_dwordx4 v254, s[68:69] offset:1024 sc1\n"
        "v_mfma_f32_16x16x32_bf16 v[36:39], a[108:111], v[76:79], v[228:231]\n"
        "v_mfma_f32_16x16x32_bf16 v[36:39], a[104:107], v[72:75], v[36:39]\n"
        "v_mfma_f32_16x16x32_bf16 v[36:39], a[100:103], v[68:71], v[36:39]\n"
        "v_mfma_f32_16x16x32_bf16 v[36:39], a[96:99], v[64:67], v[36:39]\n"
        "global_load_lds_dwordx4 v254, s[68:69] offset:2048 sc1\n"
        "v_mfma_f32_16x16x32_bf16 v[40:43], a[96:99], v[96:99], v[232:235]\n"
        "v_mfma_f32_16x16x32_bf16 v[40:43], a[100:103], v[100:103], v[40:43]\n"
        "v_exp_f32 v32, v32\n"
        "v_mfma_f32_16x16x32_bf16 v[40:43], a[104:107], v[104:107], v[40:43]\n"
        "v_exp_f32 v33, v33\n"
        "v_mfma_f32_16x16x32_bf16 v[40:43], a[108:111], v[108:111], v[40:43]\n"
        "v_exp_f32 v34, v34\n"
        "v_mfma_f32_16x16x32_bf16 v[44:47], a[108:111], v[140:143], v[236:239]\n"
        "ds_read_b128 a[108:111], v240 offset:3072\n"
        "global_load_lds_dwordx4 v254, s[68:69] offset:3072 sc1\n"
        "v_mfma_f32_16x16x32_bf16 v[44:47], a[104:107], v[136:139], v[44:47]\n"
        "ds_read_b128 a[104:107], v240 offset:2048\n"
        "v_mfma_f32_16x16x32_bf16 v[44:47], a[100:103], v[132:135], v[44:47]\n"
        "ds_read_b128 a[100:103], v240 offset:1024\n"
        "v_mfma_f32_16x16x32_bf16 v[44:47], a[96:99], v[128:131], v[44:47]\n"
        "ds_read_b128 a[96:99], v240 offset:0\n"
        "v_mfma_f32_16x16x32_bf16 v[176:179], a[128:131], v[80:83], v[176:179]\n"
        "v_exp_f32 v35, v35\n"
        "v_mfma_f32_16x16x32_bf16 v[208:211], a[128:131], v[84:87], v[208:211]\n"
        "v_exp_f32 v36, v36\n"
        "v_mfma_f32_16x16x32_bf16 a[16:19], a[128:131], v[88:91], a[16:19]\n"
        "v_exp_f32 v37, v37\n"
        "v_mfma_f32_16x16x32_bf16 a[48:51], a[128:131], v[92:95], a[48:51]\n"
        "ds_read_b128 a[128:131], v241 offset:20480\n"
        "v_mfma_f32_16x16x32_bf16 a[52:55], a[132:135], v[92:95], a[52:55]\n"
        "v_exp_f32 v38, v38\n"
        "v_mfma_f32_16x16x32_bf16 a[20:23], a[132:135], v[88:91], a[20:23]\n"
        "v_exp_f32 v39, v39\n"
        "v_mfma_f32_16x16x32_bf16 v[212:215], a[132:135], v[84:87], v[212:215]\n"
        "v_exp_f32 v40, v40\n"
        "v_mfma_f32_16x16x32_bf16 v[180:183], a[132:135], v[80:83], v[180:183]\n"
        "ds_read_b128 a[132:135], v241 offset:21504\n"
        "v_mfma_f32_16x16x32_bf16 v[184:187], a[136:139], v[80:83], v[184:187]\n"
        "v_exp_f32 v41, v41\n"
        "v_mfma_f32_16x16x32_bf16 v[216:219], a[136:139], v[84:87], v[216:219]\n"
        "v_exp_f32 v42, v42\n"
        "v_mfma_f32_16x16x32_bf16 a[24:27], a[136:139], v[88:91], a[24:27]\n"
        "v_exp_f32 v43, v43\n"
        "v_mfma_f32_16x16x32_bf16 a[56:59], a[136:139], v[92:95], a[56:59]\n"
        "ds_read_b128 a[136:139], v241 offset:22528\n"
        "s_waitcnt lgkmcnt(2)\n"
        "v_mfma_f32_16x16x32_bf16 a[60:63], a[140:143], v[92:95], a[60:63]\n"
        "v_exp_f32 v44, v44\n"
        "v_mfma_f32_16x16x32_bf16 a[28:31], a[140:143], v[88:91], a[28:31]\n"
        "v_exp_f32 v45, v45\n"
        "v_mfma_f32_16x16x32_bf16 v[220:223], a[140:143], v[84:87], v[220:223]\n"
        "v_exp_f32 v46, v46\n"
        "v_mfma_f32_16x16x32_bf16 v[188:191], a[140:143], v[80:83], v[188:191]\n"
        "ds_read_b128 a[140:143], v241 offset:23552\n"
        "v_cvt_pk_bf16_f32 v80, v32, v33\n"
        "v_mfma_f32_16x16x32_bf16 v[48:51], a[112:115], v[0:3], v[224:227]\n"
        "v_exp_f32 v47, v47\n"
        "v_mfma_f32_16x16x32_bf16 v[48:51], a[116:119], v[4:7], v[48:51]\n"
        "v_cvt_pk_bf16_f32 v81, v34, v35\n"
        "v_cvt_pk_bf16_f32 v84, v36, v37\n"
        "v_mfma_f32_16x16x32_bf16 v[48:51], a[120:123], v[8:11], v[48:51]\n"
        "v_cvt_pk_bf16_f32 v85, v38, v39\n"
        "v_cvt_pk_bf16_f32 v88, v40, v41\n"
        "v_mfma_f32_16x16x32_bf16 v[48:51], a[124:127], v[12:15], v[48:51]\n"
        "v_cvt_pk_bf16_f32 v89, v42, v43\n"
        "v_cvt_pk_bf16_f32 v92, v44, v45\n"
        "v_mfma_f32_16x16x32_bf16 v[52:55], a[124:127], v[76:79], v[228:231]\n"
        "v_cvt_pk_bf16_f32 v93, v46, v47\n"
        "v_mfma_f32_16x16x32_bf16 v[52:55], a[120:123], v[72:75], v[52:55]\n"
        "v_mfma_f32_16x16x32_bf16 v[52:55], a[116:119], v[68:71], v[52:55]\n"
        "v_mfma_f32_16x16x32_bf16 v[52:55], a[112:115], v[64:67], v[52:55]\n"
        "v_mfma_f32_16x16x32_bf16 v[56:59], a[112:115], v[96:99], v[232:235]\n"
        "v_mfma_f32_16x16x32_bf16 v[56:59], a[116:119], v[100:103], v[56:59]\n"
        "v_mfma_f32_16x16x32_bf16 v[56:59], a[120:123], v[104:107], v[56:59]\n"
        "v_mfma_f32_16x16x32_bf16 v[56:59], a[124:127], v[108:111], v[56:59]\n"
        "v_mfma_f32_16x16x32_bf16 v[60:63], a[124:127], v[140:143], v[236:239]\n"
        "v_mfma_f32_16x16x32_bf16 v[60:63], a[120:123], v[136:139], v[60:63]\n"
        "ds_read_b128 a[120:123], v240 offset:6144\n"
        "v_mfma_f32_16x16x32_bf16 v[60:63], a[116:119], v[132:135], v[60:63]\n"
        "ds_read_b128 a[116:119], v240 offset:5120\n"
        "v_mfma_f32_16x16x32_bf16 v[60:63], a[112:115], v[128:131], v[60:63]\n"
        "ds_read_b128 a[112:115], v240 offset:4096\n"
        "v_smfmac_f32_16x16x64_bf16 a[64:67], a[72:75], v[16:23], v154\n"
        "v_smfmac_f32_16x16x64_bf16 a[68:71], a[72:75], v[24:31], v154\n"
        "v_mfma_f32_16x16x32_bf16 v[160:163], a[144:147], v[16:19], v[160:163]\n"
        "v_mfma_f32_16x16x32_bf16 v[192:195], a[144:147], v[20:23], v[192:195]\n"
        "v_mfma_f32_16x16x32_bf16 a[0:3], a[144:147], v[24:27], a[0:3]\n"
        "v_mfma_f32_16x16x32_bf16 a[32:35], a[144:147], v[28:31], a[32:35]\n"
        "ds_read_b128 a[144:147], v241 offset:24576\n"
        "v_mfma_f32_16x16x32_bf16 a[36:39], a[148:151], v[28:31], a[36:39]\n"
        "v_mfma_f32_16x16x32_bf16 a[4:7], a[148:151], v[24:27], a[4:7]\n"
        "v_mfma_f32_16x16x32_bf16 v[196:199], a[148:151], v[20:23], v[196:199]\n"
        "v_mfma_f32_16x16x32_bf16 v[164:167], a[148:151], v[16:19], v[164:167]\n"
        "ds_read_b128 a[148:151], v241 offset:25600\n"
        "v_mfma_f32_16x16x32_bf16 v[168:171], a[152:155], v[16:19], v[168:171]\n"
        "v_mfma_f32_16x16x32_bf16 v[200:203], a[152:155], v[20:23], v[200:203]\n"
        "v_mfma_f32_16x16x32_bf16 a[8:11], a[152:155], v[24:27], a[8:11]\n"
        "v_mfma_f32_16x16x32_bf16 a[40:43], a[152:155], v[28:31], a[40:43]\n"
        "ds_read_b128 a[152:155], v241 offset:26624\n"
        "v_mfma_f32_16x16x32_bf16 a[44:47], a[156:159], v[28:31], a[44:47]\n"
        "v_mfma_f32_16x16x32_bf16 a[12:15], a[156:159], v[24:27], a[12:15]\n"
        "v_mfma_f32_16x16x32_bf16 v[204:207], a[156:159], v[20:23], v[204:207]\n"
        "s_cmp_lg_u32 s91, 0\n"
        "v_mfma_f32_16x16x32_bf16 v[172:175], a[156:159], v[16:19], v[172:175]\n"
        "s_cbranch_scc1 LDU_%=\n"
        "; ---- LDRAIN\n"
        "LLD_%=:\n"
        "v_mfma_f32_16x16x32_bf16 a[16:19], a[128:131], v[24:27], a[16:19]\n"
        "s_waitcnt lgkmcnt(4)\n"
        "v_exp_f32 v56, v56\n"
        "v_mfma_f32_16x16x32_bf16 a[48:51], a[128:131], v[28:31], a[48:51]\n"
        "s_mul_i32 s53, s44, 1052672\n"
        "v_exp_f32 v57, v57\n"
        "v_mfma_f32_16x16x32_bf16 a[52:55], a[132:135], v[28:31], a[52:55]\n"
        "s_lshl_b32 s96, s43, 15\n"
        "LHC0_%=:\n"
        "s_cbranch_execz LHRC_%=\n"
        "v_exp_f32 v58, v58\n"
        "v_mfma_f32_16x16x32_bf16 a[20:23], a[132:135], v[24:27], a[20:23]\n"
        "s_add_u32 s53, s53, s96\n"
        "v_exp_f32 v59, v59\n"
        "v_mfma_f32_16x16x32_bf16 a[24:27], a[136:139], v[24:27], a[24:27]\n"
        "s_lshl_b32 s96, s40, 10\n"
        "v_exp_f32 v60, v60\n"
        "v_mfma_f32_16x16x32_bf16 a[56:59], a[136:139], v[28:31], a[56:59]\n"
        "s_add_u32 s53, s53, s96\n"
        "v_exp_f32 v61, v61\n"
        "v_mfma_f32_16x16x32_bf16 a[60:63], a[140:143], v[28:31], a[60:63]\n"
        "ds_read_b128 a[172:175], v241 offset:27648\n"
        "s_add_u32 s53, s53, 0x2201000\n"
        "v_cvt_pk_bf16_f32 v90, v56, v57\n"
        "v_mfma_f32_16x16x32_bf16 a[28:31], a[140:143], v[24:27], a[28:31]\n"
        "s_add_u32 s70, s38, s53\n"
        "s_addc_u32 s71, s39, 0\n"
        "v_cvt_pk_bf16_f32 v91, v58, v59\n"
        "s_nop 1\n"
        "s_waitcnt lgkmcnt(3)\n"
        "v_mfma_f32_16x16x32_bf16 a[0:3], a[144:147], v[88:91], a[0:3]\n"
        "s_waitcnt lgkmcnt(1)\n"
        "ds_read_b128 a[176:179], v241 offset:28672\n"
        "s_sub_u32 s91, s83, s44\n"
        "v_mfma_f32_16x16x32_bf16 a[4:7], a[148:151], v[88:91], a[4:7]\n"
        "ds_read_b128 a[180:183], v241 offset:29696\n"
        "v_cvt_pk_bf16_f32 v94, v60, v61\n"
        "v_mfma_f32_16x16x32_bf16 a[8:11], a[152:155], v[88:91], a[8:11]\n"
        "ds_read_b128 a[184:187], v241 offset:30720\n"
        "ds_read_b128 a[188:191], v241 offset:31744\n"
        "v_exp_f32 v62, v62\n"
        "v_exp_f32 v63, v63\n"
        "s_waitcnt lgkmcnt(4)\n"
        "v_cvt_pk_bf16_f32 v95, v62, v63\n"
        "s_nop 1\n"
        "v_smfmac_f32_16x16x64_bf16 a[68:71], a[72:75], v[88:95], v154\n"
        "v_accvgpr_read_b32 v120, a0\n"
        "v_mfma_f32_16x16x32_bf16 a[32:35], a[144:147], v[92:95], a[32:35]\n"
        "v_accvgpr_read_b32 v121, a1\n"
        "v_accvgpr_read_b32 v122, a2\n"
        "v_mfma_f32_16x16x32_bf16 a[36:39], a[148:151], v[92:95], a[36:39]\n"
        "v_accvgpr_read_b32 v123, a3\n"
        "v_accvgpr_read_b32 v124, a4\n"
        "v_mfma_f32_16x16x32_bf16 a[40:43], a[152:155], v[92:95], a[40:43]\n"
        "s_waitcnt lgkmcnt(3)\n"
        "v_accvgpr_read_b32 v248, a68\n"
        "v_accvgpr_read_b32 v246, a69\n"
        "v_mfma_f32_16x16x32_bf16 a[44:47], a[172:175], v[92:95], a[44:47]\n"
        "s_waitcnt lgkmcnt(2)\n"
        "v_add_f32 v248, v248, v246\n"
        "v_accvgpr_read_b32 v249, a70\n"
        "v_mfma_f32_16x16x32_bf16 a[12:15], a[172:175], v[88:91], a[12:15]\n"
        "v_rcp_f32 v246, v248\n"
        "v_mfma_f32_16x16x32_bf16 a[16:19], a[176:179], v[88:91], a[16:19]\n"
        "s_waitcnt lgkmcnt(0)\n"
        "v_accvgpr_read_b32 v247, a71\n"
        "v_add_f32 v249, v249, v247\n"
        "v_mfma_f32_16x16x32_bf16 a[48:51], a[176:179], v[92:95], a[48:51]\n"
        "v_rcp_f32 v247, v249\n"
        "v_mfma_f32_16x16x32_bf16 a[52:55], a[180:183], v[92:95], a[52:55]\n"
        "v_accvgpr_read_b32 v112, a32\n"
        "v_accvgpr_read_b32 v113, a33\n"
        "v_mfma_f32_16x16x32_bf16 a[20:23], a[180:183], v[88:91], a[20:23]\n"
        "v_accvgpr_read_b32 v114, a34\n"
        "v_accvgpr_read_b32 v115, a35\n"
        "v_mfma_f32_16x16x32_bf16 a[24:27], a[184:187], v[88:91], a[24:27]\n"
        "v_accvgpr_read_b32 v116, a36\n"
        "v_accvgpr_read_b32 v117, a37\n"
        "v_mfma_f32_16x16x32_bf16 a[56:59], a[184:187], v[92:95], a[56:59]\n"
        "v_accvgpr_read_b32 v118, a38\n"
        "v_accvgpr_read_b32 v119, a39\n"
        "v_mfma_f32_16x16x32_bf16 a[60:63], a[188:191], v[92:95], a[60:63]\n"
        "v_accvgpr_read_b32 v125, a5\n"
        "v_accvgpr_read_b32 v126, a6\n"
        "v_mfma_f32_16x16x32_bf16 a[28:31], a[188:191], v[88:91], a[28:31]\n"
        "v_pk_mul_f32 v[112:113], v[112:113], v[246:247] op_sel:[0,1] op_sel_hi:[1,1]\n"
        "v_pk_mul_f32 v[114:115], v[114:115], v[246:247] op_sel:[0,1] op_sel_hi:[1,1]\n"
        "v_pk_mul_f32 v[116:117], v[116:117], v[246:247] op_sel:[0,1] op_sel_hi:[1,1]\n"
        "v_pk_mul_f32 v[118:119], v[118:119], v[246:247] op_sel:[0,1] op_sel_hi:[1,1]\n"
        "v_cvt_pk_bf16_f32 v112, v112, v113\n"
        "v_cvt_pk_bf16_f32 v113, v114, v115\n"
        "v_cvt_pk_bf16_f32 v114, v116, v117\n"
        "v_cvt_pk_bf16_f32 v115, v118, v119\n"
        "global_store_dwordx4 v254, v[112:115], s[70:71] offset:-4096 sc1\n"
        "v_accvgpr_read_b32 v127, a7\n"
        "v_pk_mul_f32 v[120:121], v[120:121], v[246:247] op_sel:[0,0] op_sel_hi:[1,0]\n"
        "v_pk_mul_f32 v[122:123], v[122:123], v[246:247] op_sel:[0,0] op_sel_hi:[1,0]\n"
        "v_pk_mul_f32 v[124:125], v[124:125], v[246:247] op_sel:[0,0] op_sel_hi:[1,0]\n"
        "v_pk_mul_f32 v[126:127], v[126:127], v[246:247] op_sel:[0,0] op_sel_hi:[1,0]\n"
        "v_cvt_pk_bf16_f32 v120, v120, v121\n"
        "v_cvt_pk_bf16_f32 v121, v122, v123\n"
        "v_cvt_pk_bf16_f32 v122, v124, v125\n"
        "v_cvt_pk_bf16_f32 v123, v126, v127\n"
        "global_store_dwordx4 v254, v[120:123], s[70:71] offset:0 sc1\n"
        "s_add_u32 s70, s70, 0x2000\n"
        "s_addc_u32 s71, s71, 0\n"
        "v_accvgpr_read_b32 v40, a40\n"
        "v_accvgpr_read_b32 v41, a41\n"
        "v_accvgpr_read_b32 v42, a42\n"
        "v_accvgpr_read_b32 v43, a43\n"
        "v_accvgpr_read_b32 v44, a44\n"
        "v_accvgpr_read_b32 v45, a45\n"
        "v_accvgpr_read_b32 v46, a46\n"
        "v_accvgpr_read_b32 v47, a47\n"
        "v_pk_mul_f32 v[40:41], v[40:41], v[246:247] op_sel:[0,1] op_sel_hi:[1,1]\n"
        "v_pk_mul_f32 v[42:43], v[42:43], v[246:247] op_sel:[0,1] op_sel_hi:[1,1]\n"
        "v_pk_mul_f32 v[44:45], v[44:45], v[246:247] op_sel:[0,1] op_sel_hi:[1,1]\n"
        "v_pk_mul_f32 v[46:47], v[46:47], v[246:247] op_sel:[0,1] op_sel_hi:[1,1]\n"
        "v_cvt_pk_bf16_f32 v40, v40, v41\n"
        "v_cvt_pk_bf16_f32 v41, v42, v43\n"
        "v_cvt_pk_bf16_f32 v42, v44, v45\n"
        "v_cvt_pk_bf16_f32 v43, v46, v47\n"
        "global_store_dwordx4 v254, v[40:43], s[70:71] offset:-4096 sc1\n"
        "v_accvgpr_read_b32 v56, a8\n"
        "v_accvgpr_read_b32 v57, a9\n"
        "v_accvgpr_read_b32 v58, a10\n"
        "v_accvgpr_read_b32 v59, a11\n"
        "v_accvgpr_read_b32 v60, a12\n"
        "v_accvgpr_read_b32 v61, a13\n"
        "v_accvgpr_read_b32 v62, a14\n"
        "v_accvgpr_read_b32 v63, a15\n"
        "v_pk_mul_f32 v[56:57], v[56:57], v[246:247] op_sel:[0,0] op_sel_hi:[1,0]\n"
        "v_pk_mul_f32 v[58:59], v[58:59], v[246:247] op_sel:[0,0] op_sel_hi:[1,0]\n"
        "v_pk_mul_f32 v[60:61], v[60:61], v[246:247] op_sel:[0,0] op_sel_hi:[1,0]\n"
        "v_pk_mul_f32 v[62:63], v[62:63], v[246:247] op_sel:[0,0] op_sel_hi:[1,0]\n"
        "v_cvt_pk_bf16_f32 v56, v56, v57\n"
        "v_cvt_pk_bf16_f32 v57, v58, v59\n"
        "v_cvt_pk_bf16_f32 v58, v60, v61\n"
        "v_cvt_pk_bf16_f32 v59, v62, v63\n"
        "global_store_dwordx4 v254, v[56:59], s[70:71] offset:0 sc1\n"
        "s_add_u32 s70, s70, 0x2000\n"
        "s_addc_u32 s71, s71, 0\n"
        "v_accvgpr_read_b32 v112, a48\n"
        "v_accvgpr_read_b32 v113, a49\n"
        "v_accvgpr_read_b32 v114, a50\n"
        "v_accvgpr_read_b32 v115, a51\n"
        "v_accvgpr_read_b32 v116, a52\n"
        "v_accvgpr_read_b32 v117, a53\n"
        "v_accvgpr_read_b32 v118, a54\n"
        "v_accvgpr_read_b32 v119, a55\n"
        "v_pk_mul_f32 v[112:113], v[112:113], v[246:247] op_sel:[0,1] op_sel_hi:[1,1]\n"
        "v_pk_mul_f32 v[114:115], v[114:115], v[246:247] op_sel:[0,1] op_sel_hi:[1,1]\n"
        "v_pk_mul_f32 v[116:117], v[116:117], v[246:247] op_sel:[0,1] op_sel_hi:[1,1]\n"
        "v_pk_mul_f32 v[118:119], v[118:119], v[246:247] op_sel:[0,1] op_sel_hi:[1,1]\n"
        "v_cvt_pk_bf16_f32 v112, v112, v113\n"
        "v_cvt_pk_bf16_f32 v113, v114, v115\n"
        "v_cvt_pk_bf16_f32 v114, v116, v117\n"
        "v_cvt_pk_bf16_f32 v115, v118, v119\n"
        "global_store_dwordx4 v254, v[112:115], s[70:71] offset:-4096 sc1\n"
        "v_accvgpr_read_b32 v120, a16\n"
        "v_accvgpr_read_b32 v121, a17\n"
        "v_accvgpr_read_b32 v122, a18\n"
        "v_accvgpr_read_b32 v123, a19\n"
        "v_accvgpr_read_b32 v124, a20\n"
        "v_accvgpr_read_b32 v125, a21\n"
        "v_accvgpr_read_b32 v126, a22\n"
        "v_accvgpr_read_b32 v127, a23\n"
        "v_pk_mul_f32 v[120:121], v[120:121], v[246:247] op_sel:[0,0] op_sel_hi:[1,0]\n"
        "v_pk_mul_f32 v[122:123], v[122:123], v[246:247] op_sel:[0,0] op_sel_hi:[1,0]\n"
        "v_pk_mul_f32 v[124:125], v[124:125], v[246:247] op_sel:[0,0] op_sel_hi:[1,0]\n"
        "v_pk_mul_f32 v[126:127], v[126:127], v[246:247] op_sel:[0,0] op_sel_hi:[1,0]\n"
        "v_cvt_pk_bf16_f32 v120, v120, v121\n"
        "v_cvt_pk_bf16_f32 v121, v122, v123\n"
        "v_cvt_pk_bf16_f32 v122, v124, v125\n"
        "v_cvt_pk_bf16_f32 v123, v126, v127\n"
        "global_store_dwordx4 v254, v[120:123], s[70:71] offset:0 sc1\n"
        "s_add_u32 s70, s70, 0x2000\n"
        "s_addc_u32 s71, s71, 0\n"
        "v_accvgpr_read_b32 v40, a56\n"
        "v_accvgpr_read_b32 v41, a57\n"
        "v_accvgpr_read_b32 v42, a58\n"
        "v_accvgpr_read_b32 v43, a59\n"
        "v_accvgpr_read_b32 v44, a60\n"
        "v_accvgpr_read_b32 v45, a61\n"
        "v_accvgpr_read_b32 v46, a62\n"
        "v_accvgpr_read_b32 v47, a63\n"
        "v_pk_mul_f32 v[40:41], v[40:41], v[246:247] op_sel:[0,1] op_sel_hi:[1,1]\n"
        "v_pk_mul_f32 v[42:43], v[42:43], v[246:247] op_sel:[0,1] op_sel_hi:[1,1]\n"
        "v_pk_mul_f32 v[44:45], v[44:45], v[246:247] op_sel:[0,1] op_sel_hi:[1,1]\n"
        "v_pk_mul_f32 v[46:47], v[46:47], v[246:247] op_sel:[0,1] op_sel_hi:[1,1]\n"
        "v_cvt_pk_bf16_f32 v40, v40, v41\n"
        "v_cvt_pk_bf16_f32 v41, v42, v43\n"
        "v_cvt_pk_bf16_f32 v42, v44, v45\n"
        "v_cvt_pk_bf16_f32 v43, v46, v47\n"
        "global_store_dwordx4 v254, v[40:43], s[70:71] offset:-4096 sc1\n"
        "v_accvgpr_read_b32 v56, a24\n"
        "v_accvgpr_read_b32 v57, a25\n"
        "v_accvgpr_read_b32 v58, a26\n"
        "v_accvgpr_read_b32 v59, a27\n"
        "v_accvgpr_read_b32 v60, a28\n"
        "v_accvgpr_read_b32 v61, a29\n"
        "v_accvgpr_read_b32 v62, a30\n"
        "v_accvgpr_read_b32 v63, a31\n"
        "v_pk_mul_f32 v[56:57], v[56:57], v[246:247] op_sel:[0,0] op_sel_hi:[1,0]\n"
        "v_pk_mul_f32 v[58:59], v[58:59], v[246:247] op_sel:[0,0] op_sel_hi:[1,0]\n"
        "v_pk_mul_f32 v[60:61], v[60:61], v[246:247] op_sel:[0,0] op_sel_hi:[1,0]\n"
        "v_pk_mul_f32 v[62:63], v[62:63], v[246:247] op_sel:[0,0] op_sel_hi:[1,0]\n"
        "v_cvt_pk_bf16_f32 v56, v56, v57\n"
        "v_cvt_pk_bf16_f32 v57, v58, v59\n"
        "v_cvt_pk_bf16_f32 v58, v60, v61\n"
        "v_cvt_pk_bf16_f32 v59, v62, v63\n"
        "global_store_dwordx4 v254, v[56:59], s[70:71] offset:0 sc1\n"
        "v_add_f32 v247, v248, v249\n"
        "v_cmp_nge_f32 vcc, 0x4e6e6b28, v247\n"
        "s_nop 0\n"
        "s_cbranch_vccnz LRL_%=\n"
        "LSL_%=:\n"
        "s_cmp_eq_u32 s91, 0\n"
        "s_cbranch_scc1 LTL_%=\n"
        "; ---- SINGLE\n"
        "s_branch LSI_%=\n"
        "s_nop 0\n"
        "s_nop 0\n"
        "s_nop 0\n"
        "s_nop 0\n"
        "s_nop 0\n"
        "s_nop 0\n"
        "s_nop 0\n"
        "s_nop 0\n"
        "s_nop 0\n"
        "s_nop 0\n"
        "s_nop 0\n"
        "s_nop 0\n"
        "s_nop 0\n"
        "s_nop 0\n"
        "LSI_%=:\n"
        "v_mfma_f32_16x16x32_bf16 v[32:35], a[96:99], v[0:3], v[224:227]\n"
        "s_waitcnt lgkmcnt(4)\n"
        "ds_read_b128 a[156:159], v241 offset:27648\n"
        "s_add_u32 m0, s81, 0x0\n"
        "v_mfma_f32_16x16x32_bf16 v[32:35], a[100:103], v[4:7], v[32:35]\n"
        "ds_read_b128 a[124:127], v240 offset:7168\n"
        "global_load_lds_dwordx4 v254, s[74:75] offset:0 sc1\n"
        "s_xor_b32 s79, s79, 0x8000\n"
        "v_mfma_f32_16x16x32_bf16 v[32:35], a[104:107], v[8:11], v[32:35]\n"
        "s_add_u32 s82, s82, 1\n"
        "v_exp_f32 v48, v48\n"
        "v_mfma_f32_16x16x32_bf16 v[32:35], a[108:111], v[12:15], v[32:35]\n"
        "s_xor_b32 s78, s78, 0x8000\n"
        "v_exp_f32 v49, v49\n"
        "v_mfma_f32_16x16x32_bf16 v[36:39], a[108:111], v[76:79], v[228:231]\n"
        "ds_read_b128 a[108:111], v240 offset:11264\n"
        "s_add_u32 s81, s78, 4096\n"
        "v_cvt_pk_bf16_f32 v82, v48, v49\n"
        "v_mfma_f32_16x16x32_bf16 v[36:39], a[104:107], v[72:75], v[36:39]\n"
        "ds_read_b128 a[104:107], v240 offset:10240\n"
        "global_load_lds_dwordx4 v254, s[74:75] offset:1024 sc1\n"
        "s_mov_b32 s80, s78\n"
        "v_mfma_f32_16x16x32_bf16 v[36:39], a[100:103], v[68:71], v[36:39]\n"
        "s_sub_u32 s91, s91, 1\n"
        "v_exp_f32 v50, v50\n"
        "v_mfma_f32_16x16x32_bf16 v[36:39], a[96:99], v[64:67], v[36:39]\n"
        "v_exp_f32 v51, v51\n"
        "v_mfma_f32_16x16x32_bf16 v[176:179], a[128:131], v[16:19], v[176:179]\n"
        "ds_read_b128 a[96:99], v240 offset:8192\n"
        "v_cvt_pk_bf16_f32 v83, v50, v51\n"
        "v_mfma_f32_16x16x32_bf16 v[208:211], a[128:131], v[20:23], v[208:211]\n"
        "ds_read_b128 a[128:131], v241 offset:28672\n"
        "global_load_lds_dwordx4 v254, s[74:75] offset:2048 sc1\n"
        "v_mfma_f32_16x16x32_bf16 v[212:215], a[132:135], v[20:23], v[212:215]\n"
        "v_exp_f32 v52, v52\n"
        "v_mfma_f32_16x16x32_bf16 v[180:183], a[132:135], v[16:19], v[180:183]\n"
        "v_exp_f32 v53, v53\n"
        "v_mfma_f32_16x16x32_bf16 v[184:187], a[136:139], v[16:19], v[184:187]\n"
        "ds_read_b128 a[100:103], v240 offset:9216\n"
        "v_cvt_pk_bf16_f32 v86, v52, v53\n"
        "v_mfma_f32_16x16x32_bf16 v[216:219], a[136:139], v[20:23], v[216:219]\n"
        "ds_read_b128 a[132:135], v241 offset:29696\n"
        "global_load_lds_dwordx4 v254, s[74:75] offset:3072 sc1\n"
        "s_add_u32 m0, s79, 0x0\n"
        "v_mfma_f32_16x16x32_bf16 v[220:223], a[140:143], v[20:23], v[220:223]\n"
        "s_waitcnt lgkmcnt(4)\n"
        "v_exp_f32 v54, v54\n"
        "v_mfma_f32_16x16x32_bf16 v[188:191], a[140:143], v[16:19], v[188:191]\n"
        "v_exp_f32 v55, v55\n"
        "v_mfma_f32_16x16x32_bf16 v[48:51], a[112:115], v[0:3], v[224:227]\n"
        "ds_read_b128 a[136:139], v241 offset:30720\n"
        "v_cvt_pk_bf16_f32 v87, v54, v55\n"
        "v_mfma_f32_16x16x32_bf16 v[48:51], a[116:119], v[4:7], v[48:51]\n"
        "ds_read_b128 a[140:143], v241 offset:31744\n"
        "v_xor_b32 v241, 0x8000, v241\n"
        "v_mfma_f32_16x16x32_bf16 v[48:51], a[120:123], v[8:11], v[48:51]\n"
        "v_exp_f32 v32, v32\n"
        "v_mfma_f32_16x16x32_bf16 v[48:51], a[124:127], v[12:15], v[48:51]\n"
        "v_exp_f32 v33, v33\n"
        "v_mfma_f32_16x16x32_bf16 v[52:55], a[124:127], v[76:79], v[228:231]\n"
        "ds_read_b128 a[124:127], v240 offset:15360\n"
        "v_cvt_pk_bf16_f32 v16, v32, v33\n"
        "v_mfma_f32_16x16x32_bf16 v[52:55], a[120:123], v[72:75], v[52:55]\n"
        "v_exp_f32 v34, v34\n"
        "v_mfma_f32_16x16x32_bf16 v[52:55], a[116:119], v[68:71], v[52:55]\n"
        "v_exp_f32 v35, v35\n"
        "v_mfma_f32_16x16x32_bf16 v[52:55], a[112:115], v[64:67], v[52:55]\n"
        "ds_read_b128 a[112:115], v240 offset:12288\n"
        "v_cvt_pk_bf16_f32 v17, v34, v35\n"
        "v_smfmac_f32_16x16x64_bf16 a[64:67], a[72:75], v[80:87], v154\n"
        "ds_read_b128 a[116:119], v240 offset:13312\n"
        "s_add_u32 s76, s66, s90\n"
        "s_addc_u32 s77, s67, 0\n"
        "v_mfma_f32_16x16x32_bf16 v[160:163], a[144:147], v[80:83], v[160:163]\n"
        "s_waitcnt vmcnt(9) lgkmcnt(3)\n"
        "v_exp_f32 v36, v36\n"
        "v_mfma_f32_16x16x32_bf16 v[192:195], a[144:147], v[84:87], v[192:195]\n"
        "s_barrier\n"
        "v_mfma_f32_16x16x32_bf16 v[196:199], a[148:151], v[84:87], v[196:199]\n"
        "v_exp_f32 v37, v37\n"
        "v_mfma_f32_16x16x32_bf16 v[164:167], a[148:151], v[80:83], v[164:167]\n"
        "ds_read_b128 a[144:147], v241 offset:0\n"
        "v_cvt_pk_bf16_f32 v20, v36, v37\n"
        "v_mfma_f32_16x16x32_bf16 v[168:171], a[152:155], v[80:83], v[168:171]\n"
        "ds_read_b128 a[148:151], v241 offset:1024\n"
        "global_load_lds_dwordx4 v144, s[76:77] offset:0 sc1\n"
        "v_mfma_f32_16x16x32_bf16 v[200:203], a[152:155], v[84:87], v[200:203]\n"
        "v_exp_f32 v38, v38\n"
        "v_mfma_f32_16x16x32_bf16 v[204:207], a[156:159], v[84:87], v[204:207]\n"
        "v_exp_f32 v39, v39\n"
        "v_mfma_f32_16x16x32_bf16 v[172:175], a[156:159], v[80:83], v[172:175]\n"
        "ds_read_b128 a[152:155], v241 offset:2048\n"
        "v_cvt_pk_bf16_f32 v21, v38, v39\n"
        "v_mfma_f32_16x16x32_bf16 v[32:35], a[96:99], v[0:3], v[224:227]\n"
        "ds_read_b128 a[120:123], v240 offset:14336\n"
        "global_load_lds_dwordx4 v145, s[76:77] offset:1024 sc1\n"
        "v_mfma_f32_16x16x32_bf16 v[32:35], a[100:103], v[4:7], v[32:35]\n"
        "ds_read_b128 a[156:159], v241 offset:3072\n"
        "s_cmp_ge_u32 s82, s44\n"
        "s_addc_u32 s90, s82, 0\n"
        "v_mfma_f32_16x16x32_bf16 v[32:35], a[104:107], v[8:11], v[32:35]\n"
        "s_lshl_b32 s90, s90, 15\n"
        "v_exp_f32 v48, v48\n"
        "v_mfma_f32_16x16x32_bf16 v[32:35], a[108:111], v[12:15], v[32:35]\n"
        "v_exp_f32 v49, v49\n"
        "v_mfma_f32_16x16x32_bf16 v[36:39], a[108:111], v[76:79], v[228:231]\n"
        "ds_read_b128 a[108:111], v240 offset:19456\n"
        "v_cvt_pk_bf16_f32 v18, v48, v49\n"
        "v_mfma_f32_16x16x32_bf16 v[36:39], a[104:107], v[72:75], v[36:39]\n"
        "ds_read_b128 a[104:107], v240 offset:18432\n"
        "global_load_lds_dwordx4 v146, s[76:77] offset:2048 sc1\n"
        "v_mfma_f32_16x16x32_bf16 v[36:39], a[100:103], v[68:71], v[36:39]\n"
        "v_exp_f32 v50, v50\n"
        "v_mfma_f32_16x16x32_bf16 v[36:39], a[96:99], v[64:67], v[36:39]\n"
        "v_exp_f32 v51, v51\n"
        "v_mfma_f32_16x16x32_bf16 v[176:179], a[128:131], v[80:83], v[176:179]\n"
        "ds_read_b128 a[96:99], v240 offset:16384\n"
        "v_cvt_pk_bf16_f32 v19, v50, v51\n"
        "v_mfma_f32_16x16x32_bf16 v[208:211], a[128:131], v[84:87], v[208:211]\n"
        "ds_read_b128 a[128:131], v241 offset:4096\n"
        "global_load_lds_dwordx4 v147, s[76:77] offset:3072 sc1\n"
        "s_add_u32 m0, s79, s28\n"
        "v_mfma_f32_16x16x32_bf16 v[212:215], a[132:135], v[84:87], v[212:215]\n"
        "v_exp_f32 v52, v52\n"
        "v_mfma_f32_16x16x32_bf16 v[180:183], a[132:135], v[80:83], v[180:183]\n"
        "v_exp_f32 v53, v53\n"
        "v_mfma_f32_16x16x32_bf16 v[184:187], a[136:139], v[80:83], v[184:187]\n"
        "s_waitcnt lgkmcnt(3)\n"
        "ds_read_b128 a[100:103], v240 offset:17408\n"
        "v_mfma_f32_16x16x32_bf16 v[216:219], a[136:139], v[84:87], v[216:219]\n"
        "ds_read_b128 a[132:135], v241 offset:5120\n"
        "v_cvt_pk_bf16_f32 v22, v52, v53\n"
        "v_mfma_f32_16x16x32_bf16 v[220:223], a[140:143], v[84:87], v[220:223]\n"
        "ds_read_b128 a[136:139], v241 offset:6144\n"
        "s_add_u32 s76, s76, s25\n"
        "s_addc_u32 s77, s77, 0\n"
        "v_mfma_f32_16x16x32_bf16 v[188:191], a[140:143], v[80:83], v[188:191]\n"
        "ds_read_b128 a[140:143], v241 offset:7168\n"
        "global_load_lds_dwordx4 v144, s[76:77] offset:0 sc1\n"
        "v_mfma_f32_16x16x32_bf16 v[48:51], a[112:115], v[0:3], v[224:227]\n"
        "v_exp_f32 v54, v54\n"
        "v_mfma_f32_16x16x32_bf16 v[48:51], a[116:119], v[4:7], v[48:51]\n"
        "v_exp_f32 v55, v55\n"
        "v_mfma_f32_16x16x32_bf16 v[48:51], a[120:123], v[8:11], v[48:51]\n"
        "v_exp_f32 v32, v32\n"
        "v_mfma_f32_16x16x32_bf16 v[48:51], a[124:127], v[12:15], v[48:51]\n"
        "v_exp_f32 v33, v33\n"
        "v_mfma_f32_16x16x32_bf16 v[160:163], a[144:147], v[16:19], v[160:163]\n"
        "v_exp_f32 v34, v34\n"
        "v_mfma_f32_16x16x32_bf16 v[164:167], a[148:151], v[16:19], v[164:167]\n"
        "v_exp_f32 v35, v35\n"
        "v_mfma_f32_16x16x32_bf16 v[168:171], a[152:155], v[16:19], v[168:171]\n"
        "v_exp_f32 v36, v36\n"
        "v_mfma_f32_16x16x32_bf16 v[172:175], a[156:159], v[16:19], v[172:175]\n"
        "s_waitcnt lgkmcnt(0)\n"
        "v_exp_f32 v37, v37\n"
        "v_mfma_f32_16x16x32_bf16 v[176:179], a[128:131], v[16:19], v[176:179]\n"
        "v_exp_f32 v38, v38\n"
        "v_mfma_f32_16x16x32_bf16 v[180:183], a[132:135], v[16:19], v[180:183]\n"
        "v_exp_f32 v39, v39\n"
        "v_mfma_f32_16x16x32_bf16 v[184:187], a[136:139], v[16:19], v[184:187]\n"
        "v_exp_f32 v48, v48\n"
        "v_mfma_f32_16x16x32_bf16 v[188:191], a[140:143], v[16:19], v[188:191]\n"
        "v_cvt_pk_bf16_f32 v23, v54, v55\n"
        "v_cvt_pk_bf16_f32 v80, v32, v33\n"
        "v_mfma_f32_16x16x32_bf16 v[52:55], a[124:127], v[76:79], v[228:231]\n"
        "ds_read_b128 a[124:127], v240 offset:23552\n"
        "global_load_lds_dwordx4 v145, s[76:77] offset:1024 sc1\n"
        "v_mfma_f32_16x16x32_bf16 v[52:55], a[120:123], v[72:75], v[52:55]\n"
        "ds_read_b128 a[120:123], v240 offset:22528\n"
        "v_cvt_pk_bf16_f32 v81, v34, v35\n"
        "v_mfma_f32_16x16x32_bf16 v[52:55], a[116:119], v[68:71], v[52:55]\n"
        "ds_read_b128 a[116:119], v240 offset:21504\n"
        "v_cvt_pk_bf16_f32 v84, v36, v37\n"
        "v_mfma_f32_16x16x32_bf16 v[52:55], a[112:115], v[64:67], v[52:55]\n"
        "ds_read_b128 a[112:115], v240 offset:20480\n"
        "v_cvt_pk_bf16_f32 v85, v38, v39\n"
        "v_smfmac_f32_16x16x64_bf16 a[64:67], a[72:75], v[16:23], v154\n"
        "global_load_lds_dwordx4 v146, s[76:77] offset:2048 sc1\n"
        "v_mfma_f32_16x16x32_bf16 v[192:195], a[144:147], v[20:23], v[192:195]\n"
        "ds_read_b128 a[144:147], v241 offset:8192\n"
        "s_add_u32 s68, s64, s90\n"
        "s_addc_u32 s69, s65, 0\n"
        "v_mfma_f32_16x16x32_bf16 v[196:199], a[148:151], v[20:23], v[196:199]\n"
        "ds_read_b128 a[148:151], v241 offset:9216\n"
        "s_add_u32 s74, s68, 0x1000\n"
        "s_addc_u32 s75, s69, 0\n"
        "v_mfma_f32_16x16x32_bf16 v[200:203], a[152:155], v[20:23], v[200:203]\n"
        "ds_read_b128 a[152:155], v241 offset:10240\n"
        "v_mfma_f32_16x16x32_bf16 v[204:207], a[156:159], v[20:23], v[204:207]\n"
        "ds_read_b128 a[156:159], v241 offset:11264\n"
        "global_load_lds_dwordx4 v147, s[76:77] offset:3072 sc1\n"
        "v_mfma_f32_16x16x32_bf16 v[32:35], a[96:99], v[0:3], v[224:227]\n"
        "v_exp_f32 v49, v49\n"
        "v_mfma_f32_16x16x32_bf16 v[32:35], a[100:103], v[4:7], v[32:35]\n"
        "v_exp_f32 v50, v50\n"
        "v_mfma_f32_16x16x32_bf16 v[32:35], a[104:107], v[8:11], v[32:35]\n"
        "v_exp_f32 v51, v51\n"
        "v_mfma_f32_16x16x32_bf16 v[32:35], a[108:111], v[12:15], v[32:35]\n"
        "v_exp_f32 v52, v52\n"
        "v_mfma_f32_16x16x32_bf16 v[36:39], a[108:111], v[76:79], v[228:231]\n"
        "ds_read_b128 a[108:111], v240 offset:27648\n"
        "v_cvt_pk_bf16_f32 v82, v48, v49\n"
        "v_mfma_f32_16x16x32_bf16 v[36:39], a[104:107], v[72:75], v[36:39]\n"
        "ds_read_b128 a[104:107], v240 offset:26624\n"
        "v_cvt_pk_bf16_f32 v83, v50, v51\n"
        "v_mfma_f32_16x16x32_bf16 v[36:39], a[100:103], v[68:71], v[36:39]\n"
        "v_exp_f32 v53, v53\n"
        "v_mfma_f32_16x16x32_bf16 v[36:39], a[96:99], v[64:67], v[36:39]\n"
        "ds_read_b128 a[96:99], v240 offset:24576\n"
        "v_cvt_pk_bf16_f32 v86, v52, v53\n"
        "v_mfma_f32_16x16x32_bf16 v[208:211], a[128:131], v[20:23], v[208:211]\n"
        "ds_read_b128 a[128:131], v241 offset:12288\n"
        "global_load_dword v244, v245, s[26:27] offset:0\n"
        "v_mfma_f32_16x16x32_bf16 v[212:215], a[132:135], v[20:23], v[212:215]\n"
        "s_waitcnt lgkmcnt(4)\n"
        "ds_read_b128 a[100:103], v240 offset:25600\n"
        "v_mfma_f32_16x16x32_bf16 v[216:219], a[136:139], v[20:23], v[216:219]\n"
        "ds_read_b128 a[132:135], v241 offset:13312\n"
        "s_add_u32 s26, s26, 0x20000\n"
        "s_addc_u32 s27, s27, 0\n"
        "v_mfma_f32_16x16x32_bf16 v[220:223], a[140:143], v[20:23], v[220:223]\n"
        "v_exp_f32 v54, v54\n"
        "v_mfma_f32_16x16x32_bf16 v[48:51], a[112:115], v[0:3], v[224:227]\n"
        "v_exp_f32 v55, v55\n"
        "v_mfma_f32_16x16x32_bf16 v[48:51], a[116:119], v[4:7], v[48:51]\n"
        "ds_read_b128 a[136:139], v241 offset:14336\n"
        "v_cvt_pk_bf16_f32 v87, v54, v55\n"
        "v_mfma_f32_16x16x32_bf16 v[48:51], a[120:123], v[8:11], v[48:51]\n"
        "ds_read_b128 a[140:143], v241 offset:15360\n"
        "v_mfma_f32_16x16x32_bf16 v[48:51], a[124:127], v[12:15], v[48:51]\n"
        "v_exp_f32 v32, v32\n"
        "v_mfma_f32_16x16x32_bf16 v[52:55], a[124:127], v[76:79], v[228:231]\n"
        "v_exp_f32 v33, v33\n"
        "v_mfma_f32_16x16x32_bf16 v[52:55], a[120:123], v[72:75], v[52:55]\n"
        "ds_read_b128 a[124:127], v240 offset:31744\n"
        "v_cvt_pk_bf16_f32 v16, v32, v33\n"
        "v_mfma_f32_16x16x32_bf16 v[52:55], a[116:119], v[68:71], v[52:55]\n"
        "v_exp_f32 v34, v34\n"
        "v_mfma_f32_16x16x32_bf16 v[52:55], a[112:115], v[64:67], v[52:55]\n"
        "v_exp_f32 v35, v35\n"
        "v_smfmac_f32_16x16x64_bf16 a[64:67], a[72:75], v[80:87], v154\n"
        "ds_read_b128 a[112:115], v240 offset:28672\n"
        "v_cvt_pk_bf16_f32 v17, v34, v35\n"
        "v_mfma_f32_16x16x32_bf16 v[160:163], a[144:147], v[80:83], v[160:163]\n"
        "v_exp_f32 v36, v36\n"
        "v_mfma_f32_16x16x32_bf16 v[192:195], a[144:147], v[84:87], v[192:195]\n"
        "v_exp_f32 v37, v37\n"
        "v_mfma_f32_16x16x32_bf16 v[196:199], a[148:151], v[84:87], v[196:199]\n"
        "ds_read_b128 a[144:147], v241 offset:16384\n"
        "v_cvt_pk_bf16_f32 v20, v36, v37\n"
        "v_mfma_f32_16x16x32_bf16 v[164:167], a[148:151], v[80:83], v[164:167]\n"
        "ds_read_b128 a[116:119], v240 offset:29696\n"
        "v_mfma_f32_16x16x32_bf16 v[168:171], a[152:155], v[80:83], v[168:171]\n"
        "s_waitcnt lgkmcnt(3)\n"
        "ds_read_b128 a[148:151], v241 offset:17408\n"
        "v_mfma_f32_16x16x32_bf16 v[200:203], a[152:155], v[84:87], v[200:203]\n"
        "v_exp_f32 v38, v38\n"
        "v_mfma_f32_16x16x32_bf16 v[204:207], a[156:159], v[84:87], v[204:207]\n"
        "v_exp_f32 v39, v39\n"
        "v_mfma_f32_16x16x32_bf16 v[172:175], a[156:159], v[80:83], v[172:175]\n"
        "ds_read_b128 a[152:155], v241 offset:18432\n"
        "v_cvt_pk_bf16_f32 v21, v38, v39\n"
        "v_mfma_f32_16x16x32_bf16 v[32:35], a[96:99], v[0:3], v[224:227]\n"
        "ds_read_b128 a[120:123], v240 offset:30720\n"
        "v_xor_b32 v240, 0x8000, v240\n"
        "v_mfma_f32_16x16x32_bf16 v[32:35], a[100:103], v[4:7], v[32:35]\n"
        "ds_read_b128 a[156:159], v241 offset:19456\n"
        "v_mfma_f32_16x16x32_bf16 v[32:35], a[104:107], v[8:11], v[32:35]\n"
        "v_exp_f32 v48, v48\n"
        "v_mfma_f32_16x16x32_bf16 v[32:35], a[108:111], v[12:15], v[32:35]\n"
        "v_exp_f32 v49, v49\n"
        "v_mfma_f32_16x16x32_bf16 v[36:39], a[108:111], v[76:79], v[228:231]\n"
        "v_exp_f32 v50, v50\n"
        "v_mfma_f32_16x16x32_bf16 v[36:39], a[104:107], v[72:75], v[36:39]\n"
        "v_exp_f32 v51, v51\n"
        "v_mfma_f32_16x16x32_bf16 v[36:39], a[100:103], v[68:71], v[36:39]\n"
        "v_exp_f32 v52, v52\n"
        "v_mfma_f32_16x16x32_bf16 v[36:39], a[96:99], v[64:67], v[36:39]\n"
        "v_exp_f32 v53, v53\n"
        "v_mfma_f32_16x16x32_bf16 v[176:179], a[128:131], v[80:83], v[176:179]\n"
        "s_waitcnt vmcnt(9) lgkmcnt(1)\n"
        "v_exp_f32 v54, v54\n"
        "v_mfma_f32_16x16x32_bf16 v[208:211], a[128:131], v[84:87], v[208:211]\n"
        "s_barrier\n"
        "v_mfma_f32_16x16x32_bf16 v[212:215], a[132:135], v[84:87], v[212:215]\n"
        "ds_read_b128 a[96:99], v240 offset:0\n"
        "v_cvt_pk_bf16_f32 v18, v48, v49\n"
        "v_mfma_f32_16x16x32_bf16 v[180:183], a[132:135], v[80:83], v[180:183]\n"
        "ds_read_b128 a[128:131], v241 offset:20480\n"
        "v_cvt_pk_bf16_f32 v19, v50, v51\n"
        "v_mfma_f32_16x16x32_bf16 v[184:187], a[136:139], v[80:83], v[184:187]\n"
        "ds_read_b128 a[100:103], v240 offset:1024\n"
        "s_cmp_eq_u32 s82, s24\n"
        "s_cbranch_scc1 LEVS0_%=\n"
        "LEWS0_%=:\n"
        "v_mfma_f32_16x16x32_bf16 v[216:219], a[136:139], v[84:87], v[216:219]\n"
        "ds_read_b128 a[132:135], v241 offset:21504\n"
        "s_add_u32 m0, s80, 0x0\n"
        "v_cvt_pk_bf16_f32 v22, v52, v53\n"
        "v_mfma_f32_16x16x32_bf16 v[220:223], a[140:143], v[84:87], v[220:223]\n"
        "ds_read_b128 a[136:139], v241 offset:22528\n"
        "global_load_lds_dwordx4 v254, s[68:69] offset:0 sc1\n"
        "v_mfma_f32_16x16x32_bf16 v[188:191], a[140:143], v[80:83], v[188:191]\n"
        "v_exp_f32 v55, v55\n"
        "v_mfma_f32_16x16x32_bf16 v[48:51], a[112:115], v[0:3], v[224:227]\n"
        "ds_read_b128 a[104:107], v240 offset:2048\n"
        "v_cvt_pk_bf16_f32 v23, v54, v55\n"
        "v_mfma_f32_16x16x32_bf16 v[48:51], a[116:119], v[4:7], v[48:51]\n"
        "ds_read_b128 a[140:143], v241 offset:23552\n"
        "v_mfma_f32_16x16x32_bf16 v[48:51], a[120:123], v[8:11], v[48:51]\n"
        "ds_read_b128 a[108:111], v240 offset:3072\n"
        "global_load_lds_dwordx4 v254, s[68:69] offset:1024 sc1\n"
        "v_mfma_f32_16x16x32_bf16 v[48:51], a[124:127], v[12:15], v[48:51]\n"
        "s_waitcnt lgkmcnt(6)\n"
        "v_exp_f32 v32, v32\n"
        "v_mfma_f32_16x16x32_bf16 v[52:55], a[124:127], v[76:79], v[228:231]\n"
        "v_exp_f32 v33, v33\n"
        "v_mfma_f32_16x16x32_bf16 v[52:55], a[120:123], v[72:75], v[52:55]\n"
        "ds_read_b128 a[120:123], v240 offset:6144\n"
        "v_cvt_pk_bf16_f32 v80, v32, v33\n"
        "v_mfma_f32_16x16x32_bf16 v[52:55], a[116:119], v[68:71], v[52:55]\n"
        "ds_read_b128 a[116:119], v240 offset:5120\n"
        "global_load_lds_dwordx4 v254, s[68:69] offset:2048 sc1\n"
        "v_mfma_f32_16x16x32_bf16 v[52:55], a[112:115], v[64:67], v[52:55]\n"
        "v_exp_f32 v34, v34\n"
        "v_smfmac_f32_16x16x64_bf16 a[64:67], a[72:75], v[16:23], v154\n"
        "ds_read_b128 a[112:115], v240 offset:4096\n"
        "v_mfma_f32_16x16x32_bf16 v[160:163], a[144:147], v[16:19], v[160:163]\n"
        "v_exp_f32 v35, v35\n"
        "v_mfma_f32_16x16x32_bf16 v[192:195], a[144:147], v[20:23], v[192:195]\n"
        "ds_read_b128 a[144:147], v241 offset:24576\n"
        "global_load_lds_dwordx4 v254, s[68:69] offset:3072 sc1\n"
        "v_mfma_f32_16x16x32_bf16 v[196:199], a[148:151], v[20:23], v[196:199]\n"
        "v_exp_f32 v36, v36\n"
        "v_mfma_f32_16x16x32_bf16 v[164:167], a[148:151], v[16:19], v[164:167]\n"
        "ds_read_b128 a[148:151], v241 offset:25600\n"
        "v_cvt_pk_bf16_f32 v81, v34, v35\n"
        "v_mfma_f32_16x16x32_bf16 v[168:171], a[152:155], v[16:19], v[168:171]\n"
        "v_exp_f32 v37, v37\n"
        "v_mfma_f32_16x16x32_bf16 v[200:203], a[152:155], v[20:23], v[200:203]\n"
        "ds_read_b128 a[152:155], v241 offset:26624\n"
        "v_cvt_pk_bf16_f32 v84, v36, v37\n"
        "v_mfma_f32_16x16x32_bf16 v[204:207], a[156:159], v[20:23], v[204:207]\n"
        "v_exp_f32 v38, v38\n"
        "v_mfma_f32_16x16x32_bf16 v[172:175], a[156:159], v[16:19], v[172:175]\n"
        "v_exp_f32 v39, v39\n"
        "s_nop 0\n"
        "v_cvt_pk_bf16_f32 v85, v38, v39\n"
        "s_cmp_lg_u32 s91, 0\n"
        "s_cbranch_scc1 LSI_%=\n"
        "s_branch LTL_%=\n"
        "LEVS0_%=:\n"
        "s_cmp_eq_u32 s82, s83\n"
        "s_cbranch_scc1 LE3S0_%=\n"
        "global_load_dwordx2 v[252:253], v155, s[48:49] sc1\n"
        "s_mov_b32 s24, s83\n"
        "v_accvgpr_read_b32 v246, a93\n"
        "global_load_dword v244, v246, s[46:47]\n"
        "s_branch LEWS0_%=\n"
        "LE3S0_%=:\n"
        "v_cmp_ne_u64 vcc, v[252:253], v[250:251]\n"
        "s_mov_b64 s[68:69], s[92:93]\n"
        "s_mov_b64 s[74:75], s[94:95]\n"
        "s_mov_b32 s80, s50\n"
        "s_mov_b32 s81, s51\n"
        "s_mov_b64 s[66:67], s[22:23]\n"
        "s_mov_b32 s90, s29\n"
        "s_mov_b32 s25, s30\n"
        "s_lshl_b32 s28, s28, 2\n"
        "s_xor_b32 s79, s73, 0x8000\n"
        "LHC1_%=:\n"
        "s_cbranch_execz LHRC_%=\n"
        "v_accvgpr_read_b32 v144, a84\n"
        "v_accvgpr_read_b32 v145, a85\n"
        "v_accvgpr_read_b32 v146, a86\n"
        "v_accvgpr_read_b32 v147, a87\n"
        "s_mov_b64 exec, 15\n"
        "ds_write_b128 v156, a[88:91]\n"
        "s_mov_b64 exec, -1\n"
        "s_cbranch_vccz LEWS0_%=\n"
        "s_mov_b32 s97, 0x200000\n"
        "LE4S0_%=:\n"
        "s_sleep 2\n"
        "global_load_dwordx2 v[252:253], v155, s[48:49] sc1\n"
        "s_waitcnt vmcnt(0)\n"
        "v_cmp_ne_u64 vcc, v[252:253], v[250:251]\n"
        "s_nop 1\n"
        "s_cbranch_vccz LEWS0_%=\n"
        "s_sub_u32 s97, s97, 1\n"
        "s_cmp_lg_u32 s97, 0\n"
        "s_cbranch_scc1 LE4S0_%=\n"
        "s_branch LEWS0_%=\n"
        "; ---- BACK\n"
        "LTL_%=:\n"
        "v_mfma_f32_16x16x32_bf16 v[176:179], a[128:131], v[16:19], v[176:179]\n"
        "s_waitcnt lgkmcnt(4)\n"
        "ds_read_b128 a[156:159], v241 offset:27648\n"
        "s_mul_i32 s53, s45, 1052672\n"
        "v_mfma_f32_16x16x32_bf16 v[208:211], a[128:131], v[20:23], v[208:211]\n"
        "s_lshl_b32 s96, s43, 15\n"
        "v_exp_f32 v48, v48\n"
        "v_mfma_f32_16x16x32_bf16 v[212:215], a[132:135], v[20:23], v[212:215]\n"
        "s_add_u32 s53, s53, s96\n"
        "v_exp_f32 v49, v49\n"
        "v_mfma_f32_16x16x32_bf16 v[180:183], a[132:135], v[16:19], v[180:183]\n"
        "ds_read_b128 a[128:131], v241 offset:28672\n"
        "s_lshl_b32 s96, s40, 10\n"
        "v_cvt_pk_bf16_f32 v82, v48, v49\n"
        "v_mfma_f32_16x16x32_bf16 v[184:187], a[136:139], v[16:19], v[184:187]\n"
        "s_add_u32 s53, s53, s96\n"
        "v_exp_f32 v50, v50\n"
        "v_mfma_f32_16x16x32_bf16 v[216:219], a[136:139], v[20:23], v[216:219]\n"
        "s_add_u32 s53, s53, 0x2201000\n"
        "v_exp_f32 v51, v51\n"
        "v_mfma_f32_16x16x32_bf16 v[220:223], a[140:143], v[20:23], v[220:223]\n"
        "s_waitcnt lgkmcnt(2)\n"
        "ds_read_b128 a[132:135], v241 offset:29696\n"
        "s_add_u32 s16, s51, 4096\n"
        "v_mfma_f32_16x16x32_bf16 v[188:191], a[140:143], v[16:19], v[188:191]\n"
        "v_cvt_pk_bf16_f32 v83, v50, v51\n"
        "s_nop 1\n"
        "v_mfma_f32_16x16x32_bf16 v[160:163], a[144:147], v[80:83], v[160:163]\n"
        "s_lshl_b32 s14, s44, 5\n"
        "v_exp_f32 v52, v52\n"
        "v_mfma_f32_16x16x32_bf16 v[164:167], a[148:151], v[80:83], v[164:167]\n"
        "s_add_u32 s14, s14, s43\n"
        "v_exp_f32 v53, v53\n"
        "v_mfma_f32_16x16x32_bf16 v[168:171], a[152:155], v[80:83], v[168:171]\n"
        "s_waitcnt lgkmcnt(2)\n"
        "ds_read_b128 a[136:139], v241 offset:30720\n"
        "s_lshl2_add_u32 s14, s14, s40\n"
        "v_mfma_f32_16x16x32_bf16 v[172:175], a[156:159], v[80:83], v[172:175]\n"
        "s_waitcnt lgkmcnt(2)\n"
        "ds_read_b128 a[140:143], v241 offset:31744\n"
        "s_lshl_b32 s14, s14, 8\n"
        "v_mfma_f32_16x16x32_bf16 v[176:179], a[128:131], v[80:83], v[176:179]\n"
        "s_add_u32 s70, s38, s53\n"
        "s_addc_u32 s71, s39, 0\n"
        "s_add_u32 s14, s14, 131072\n"
        "s_add_u32 m0, s73, 0x8000\n"
        "s_xor_b32 s66, s29, 0x40000\n"
        "v_cvt_pk_bf16_f32 v86, v52, v53\n"
        "v_add_u32 v230, 3584, v152\n"
        "s_waitcnt lgkmcnt(2)\n"
        "v_mfma_f32_16x16x32_bf16 v[180:183], a[132:135], v[80:83], v[180:183]\n"
        "s_add_u32 s60, s22, s66\n"
        "s_addc_u32 s61, s23, 0\n"
        "s_xor_b32 s66, s29, 0x60000\n"
        "s_cmp_le_u32 s10, 0\n"
        "s_addc_u32 s74, 0, 0\n"
        "s_cmp_le_u32 s10, 1\n"
        "s_addc_u32 s75, 1, 0\n"
        "s_cmp_le_u32 s10, 2\n"
        "s_addc_u32 s76, 2, 0\n"
        "s_sub_u32 s62, 15, s12\n"
        "s_cmp_lt_u32 s10, 2\n"
        "s_cselect_b32 s62, s12, s62\n"
        "s_lshl2_add_u32 s63, s11, s40\n"
        "v_exp_f32 v54, v54\n"
        "s_waitcnt lgkmcnt(1)\n"
        "v_exp_f32 v55, v55\n"
        "v_mfma_f32_16x16x32_bf16 v[184:187], a[136:139], v[80:83], v[184:187]\n"
        "s_waitcnt lgkmcnt(0)\n"
        "v_cvt_pk_bf16_f32 v87, v54, v55\n"
        "s_nop 1\n"
        "v_smfmac_f32_16x16x64_bf16 a[64:67], a[72:75], v[80:87], v154\n"
        "ds_read2_b32 a[0:1], v148 offset0:0 offset1:1\n"
        "s_lshl_b32 s63, s63, 5\n"
        "v_min_u32 v230, s16, v230\n"
        "v_mfma_f32_16x16x32_bf16 v[192:195], a[144:147], v[84:87], v[192:195]\n"
        "ds_read2_b32 a[2:3], v148 offset0:2 offset1:3\n"
        "v_add_u32 v231, 3648, v152\n"
        "v_mfma_f32_16x16x32_bf16 v[196:199], a[148:151], v[84:87], v[196:199]\n"
        "ds_read_b128 a[192:195], v230 offset:0\n"
        "v_accvgpr_read_b32 v248, a64\n"
        "v_mfma_f32_16x16x32_bf16 v[200:203], a[152:155], v[84:87], v[200:203]\n"
        "ds_read2_b32 a[4:5], v148 offset0:128 offset1:129\n"
        "v_accvgpr_read_b32 v246, a65\n"
        "v_mfma_f32_16x16x32_bf16 v[204:207], a[156:159], v[84:87], v[204:207]\n"
        "ds_read2_b32 a[6:7], v148 offset0:130 offset1:131\n"
        "v_add_f32 v248, v248, v246\n"
        "v_mfma_f32_16x16x32_bf16 v[208:211], a[128:131], v[84:87], v[208:211]\n"
        "v_rcp_f32 v246, v248\n"
        "v_mfma_f32_16x16x32_bf16 v[212:215], a[132:135], v[84:87], v[212:215]\n"
        "ds_read_b128 a[196:199], v152 offset:3072\n"
        "v_accvgpr_read_b32 v249, a66\n"
        "v_mfma_f32_16x16x32_bf16 v[216:219], a[136:139], v[84:87], v[216:219]\n"
        "ds_read2_b32 a[8:9], v149 offset0:0 offset1:1\n"
        "v_accvgpr_read_b32 v247, a67\n"
        "v_mfma_f32_16x16x32_bf16 v[220:223], a[140:143], v[84:87], v[220:223]\n"
        "ds_read2_b32 a[10:11], v149 offset0:2 offset1:3\n"
        "v_add_f32 v249, v249, v247\n"
        "v_mfma_f32_16x16x32_bf16 v[188:191], a[140:143], v[80:83], v[188:191]\n"
        "v_rcp_f32 v247, v249\n"
        "s_waitcnt lgkmcnt(5)\n"
        "v_mul_f32 v192, v192, v247\n"
        "v_mul_f32 v193, v193, v247\n"
        "v_mul_f32 v194, v194, v247\n"
        "v_mul_f32 v195, v195, v247\n"
        "v_mul_f32 v196, v196, v247\n"
        "v_mul_f32 v197, v197, v247\n"
        "v_mul_f32 v198, v198, v247\n"
        "v_mul_f32 v199, v199, v247\n"
        "v_cvt_pk_bf16_f32 v192, v192, v193\n"
        "v_cvt_pk_bf16_f32 v193, v194, v195\n"
        "v_cvt_pk_bf16_f32 v194, v196, v197\n"
        "v_cvt_pk_bf16_f32 v195, v198, v199\n"
        "global_store_dwordx4 v254, v[192:195], s[70:71] offset:-4096 sc1\n"
        "ds_read_b128 a[200:203], v152 offset:2560\n"
        "s_waitcnt lgkmcnt(3)\n"
        "v_mul_f32 v160, v160, v246\n"
        "v_mul_f32 v161, v161, v246\n"
        "v_mul_f32 v162, v162, v246\n"
        "v_mul_f32 v163, v163, v246\n"
        "v_mul_f32 v164, v164, v246\n"
        "v_mul_f32 v165, v165, v246\n"
        "v_mul_f32 v166, v166, v246\n"
        "v_mul_f32 v167, v167, v246\n"
        "v_cvt_pk_bf16_f32 v160, v160, v161\n"
        "v_cvt_pk_bf16_f32 v161, v162, v163\n"
        "v_cvt_pk_bf16_f32 v162, v164, v165\n"
        "v_cvt_pk_bf16_f32 v163, v166, v167\n"
        "global_store_dwordx4 v254, v[160:163], s[70:71] offset:0 sc1\n"
        "s_add_u32 s70, s70, 0x2000\n"
        "s_addc_u32 s71, s71, 0\n"
        "s_waitcnt lgkmcnt(1)\n"
        "v_mul_f32 v200, v200, v247\n"
        "v_mul_f32 v201, v201, v247\n"
        "v_mul_f32 v202, v202, v247\n"
        "v_mul_f32 v203, v203, v247\n"
        "v_mul_f32 v204, v204, v247\n"
        "v_mul_f32 v205, v205, v247\n"
        "v_mul_f32 v206, v206, v247\n"
        "v_mul_f32 v207, v207, v247\n"
        "v_cvt_pk_bf16_f32 v200, v200, v201\n"
        "v_cvt_pk_bf16_f32 v201, v202, v203\n"
        "v_cvt_pk_bf16_f32 v202, v204, v205\n"
        "v_cvt_pk_bf16_f32 v203, v206, v207\n"
        "global_store_dwordx4 v254, v[200:203], s[70:71] offset:-4096 sc1\n"
        "ds_read2_b32 a[12:13], v149 offset0:128 offset1:129\n"
        "ds_read2_b32 a[14:15], v149 offset0:130 offset1:131\n"
        "v_mul_f32 v168, v168, v246\n"
        "v_mul_f32 v169, v169, v246\n"
        "v_mul_f32 v170, v170, v246\n"
        "v_mul_f32 v171, v171, v246\n"
        "v_mul_f32 v172, v172, v246\n"
        "v_mul_f32 v173, v173, v246\n"
        "v_mul_f32 v174, v174, v246\n"
        "v_mul_f32 v175, v175, v246\n"
        "v_cvt_pk_bf16_f32 v168, v168, v169\n"
        "v_cvt_pk_bf16_f32 v169, v170, v171\n"
        "v_cvt_pk_bf16_f32 v170, v172, v173\n"
        "v_cvt_pk_bf16_f32 v171, v174, v175\n"
        "global_store_dwordx4 v254, v[168:171], s[70:71] offset:0 sc1\n"
        "s_add_u32 s70, s70, 0x2000\n"
        "s_addc_u32 s71, s71, 0\n"
        "ds_read_b128 a[204:207], v152 offset:2048\n"
        "s_and_b32 s67, s74, 1\n"
        "v_mul_f32 v208, v208, v247\n"
        "v_mul_f32 v209, v209, v247\n"
        "v_mul_f32 v210, v210, v247\n"
        "v_mul_f32 v211, v211, v247\n"
        "v_mul_f32 v212, v212, v247\n"
        "v_mul_f32 v213, v213, v247\n"
        "v_mul_f32 v214, v214, v247\n"
        "v_mul_f32 v215, v215, v247\n"
        "v_cvt_pk_bf16_f32 v208, v208, v209\n"
        "v_cvt_pk_bf16_f32 v209, v210, v211\n"
        "v_cvt_pk_bf16_f32 v210, v212, v213\n"
        "v_cvt_pk_bf16_f32 v211, v214, v215\n"
        "global_store_dwordx4 v254, v[208:211], s[70:71] offset:-4096 sc1\n"
        "ds_read2_b32 a[16:17], v150 offset0:0 offset1:1\n"
        "ds_read2_b32 a[18:19], v150 offset0:2 offset1:3\n"
        "s_lshl_b32 s67, s67, 12\n"
        "v_mul_f32 v176, v176, v246\n"
        "v_mul_f32 v177, v177, v246\n"
        "v_mul_f32 v178, v178, v246\n"
        "v_mul_f32 v179, v179, v246\n"
        "v_mul_f32 v180, v180, v246\n"
        "v_mul_f32 v181, v181, v246\n"
        "v_mul_f32 v182, v182, v246\n"
        "v_mul_f32 v183, v183, v246\n"
        "v_cvt_pk_bf16_f32 v176, v176, v177\n"
        "v_cvt_pk_bf16_f32 v177, v178, v179\n"
        "v_cvt_pk_bf16_f32 v178, v180, v181\n"
        "v_cvt_pk_bf16_f32 v179, v182, v183\n"
        "global_store_dwordx4 v254, v[176:179], s[70:71] offset:0 sc1\n"
        "s_add_u32 s70, s70, 0x2000\n"
        "s_addc_u32 s71, s71, 0\n"
        "ds_read_b128 a[208:211], v152 offset:1536\n"
        "v_mul_f32 v216, v216, v247\n"
        "v_mfma_f32_16x16x32_bf16 v[96:99], a[0:3], a[192:195], 0\n"
        "ds_read2_b32 a[20:21], v150 offset0:128 offset1:129\n"
        "v_mul_f32 v217, v217, v247\n"
        "v_mfma_f32_16x16x32_bf16 v[100:103], a[0:3], a[196:199], 0\n"
        "ds_read2_b32 a[22:23], v150 offset0:130 offset1:131\n"
        "v_mul_f32 v218, v218, v247\n"
        "v_mfma_f32_16x16x32_bf16 v[100:103], a[4:7], a[192:195], v[100:103]\n"
        "ds_read_b128 a[212:215], v152 offset:1024\n"
        "v_mul_f32 v219, v219, v247\n"
        "v_mul_f32 v220, v220, v247\n"
        "v_mul_f32 v221, v221, v247\n"
        "v_mul_f32 v222, v222, v247\n"
        "v_mul_f32 v223, v223, v247\n"
        "v_cvt_pk_bf16_f32 v216, v216, v217\n"
        "v_cvt_pk_bf16_f32 v217, v218, v219\n"
        "v_cvt_pk_bf16_f32 v218, v220, v221\n"
        "v_cvt_pk_bf16_f32 v219, v222, v223\n"
        "global_store_dwordx4 v254, v[216:219], s[70:71] offset:-4096 sc1\n"
        "s_waitcnt lgkmcnt(4)\n"
        "v_mul_f32 v184, v184, v246\n"
        "v_mul_f32 v185, v185, v246\n"
        "v_mul_f32 v186, v186, v246\n"
        "v_mfma_f32_16x16x32_bf16 v[104:107], a[8:11], a[192:195], 0\n"
        "ds_read2_b32 a[24:25], v151 offset0:0 offset1:1\n"
        "v_mul_f32 v187, v187, v246\n"
        "v_mul_f32 v188, v188, v246\n"
        "v_mul_f32 v189, v189, v246\n"
        "v_mul_f32 v190, v190, v246\n"
        "v_mul_f32 v191, v191, v246\n"
        "v_cvt_pk_bf16_f32 v184, v184, v185\n"
        "v_cvt_pk_bf16_f32 v185, v186, v187\n"
        "v_cvt_pk_bf16_f32 v186, v188, v189\n"
        "v_cvt_pk_bf16_f32 v187, v190, v191\n"
        "global_store_dwordx4 v254, v[184:187], s[70:71] offset:0 sc1\n"
        "ds_read2_b32 a[26:27], v151 offset0:2 offset1:3\n"
        "ds_read_b128 a[216:219], v152 offset:512\n"
        "v_add_f32 v247, v248, v249\n"
        "v_mfma_f32_16x16x32_bf16 v[104:107], a[4:7], a[196:199], v[104:107]\n"
        "ds_read2_b32 a[28:29], v151 offset0:128 offset1:129\n"
        "v_cmp_nge_f32 vcc, 0x4e6e6b28, v247\n"
        "v_mfma_f32_16x16x32_bf16 v[104:107], a[0:3], a[200:203], v[104:107]\n"
        "ds_read2_b32 a[30:31], v151 offset0:130 offset1:131\n"
        "s_cbranch_vccnz LRH_%=\n"
        "LSH_%=:\n"
        "v_min_u32 v231, s16, v231\n"
        "v_mfma_f32_16x16x32_bf16 v[108:111], a[0:3], a[204:207], 0\n"
        "ds_read_b128 a[220:223], v152 offset:0\n"
        "v_and_b32 v234, 0x70, v254\n"
        "v_mfma_f32_16x16x32_bf16 v[108:111], a[4:7], a[200:203], v[108:111]\n"
        "ds_read2_b32 a[32:33], v148 offset0:16 offset1:17\n"
        "v_lshrrev_b32 v234, 1, v234\n"
        "v_mfma_f32_16x16x32_bf16 v[108:111], a[8:11], a[196:199], v[108:111]\n"
        "ds_read2_b32 a[34:35], v148 offset0:18 offset1:19\n"
        "v_and_b32 v235, 0x80, v254\n"
        "v_mfma_f32_16x16x32_bf16 v[108:111], a[12:15], a[192:195], v[108:111]\n"
        "ds_read_b128 a[224:227], v231 offset:0\n"
        "v_add3_u32 v234, v234, v235, s14\n"
        "v_mfma_f32_16x16x32_bf16 v[128:131], a[16:19], a[192:195], 0\n"
        "ds_read2_b32 a[36:37], v148 offset0:144 offset1:145\n"
        "s_and_b32 s14, s10, 1\n"
        "v_xor_b32 v154, 32, v153\n"
        "v_mfma_f32_16x16x32_bf16 v[128:131], a[12:15], a[196:199], v[128:131]\n"
        "ds_read2_b32 a[38:39], v148 offset0:146 offset1:147\n"
        "s_lshl1_add_u32 s62, s62, s14\n"
        "v_add_u32 v155, 0x10000, v153\n"
        "v_mfma_f32_16x16x32_bf16 v[128:131], a[8:11], a[200:203], v[128:131]\n"
        "ds_read_b128 a[228:231], v152 offset:3136\n"
        "s_lshl_b32 s62, s62, 6\n"
        "v_add_u32 v156, 0x10000, v154\n"
        "v_mfma_f32_16x16x32_bf16 v[128:131], a[4:7], a[204:207], v[128:131]\n"
        "ds_read2_b32 a[40:41], v149 offset0:16 offset1:17\n"
        "s_lshl_b32 s14, s62, 11\n"
        "s_waitcnt lgkmcnt(8)\n"
        "v_mfma_f32_16x16x32_bf16 v[128:131], a[0:3], a[208:211], v[128:131]\n"
        "ds_read2_b32 a[42:43], v149 offset0:18 offset1:19\n"
        "s_lshl1_add_u32 s14, s63, s14\n"
        "v_add_u32 v157, 0x20000, v153\n"
        "v_mfma_f32_16x16x32_bf16 v[132:135], a[0:3], a[212:215], 0\n"
        "ds_read_b128 a[232:235], v152 offset:2624\n"
        "v_add_u32 v158, 0x20000, v154\n"
        "v_mfma_f32_16x16x32_bf16 v[132:135], a[4:7], a[208:211], v[132:135]\n"
        "ds_read2_b32 a[44:45], v149 offset0:144 offset1:145\n"
        "v_add_u32 v230, 3712, v152\n"
        "v_mfma_f32_16x16x32_bf16 v[132:135], a[8:11], a[204:207], v[132:135]\n"
        "ds_read2_b32 a[46:47], v149 offset0:146 offset1:147\n"
        "v_min_u32 v230, s16, v230\n"
        "v_mfma_f32_16x16x32_bf16 v[132:135], a[12:15], a[200:203], v[132:135]\n"
        "ds_read_b128 a[236:239], v152 offset:2112\n"
        "v_bfe_u32 v232, v254, 4, 4\n"
        "v_mfma_f32_16x16x32_bf16 v[132:135], a[16:19], a[196:199], v[132:135]\n"
        "ds_read2_b32 a[48:49], v150 offset0:16 offset1:17\n"
        "s_waitcnt vmcnt(8)\n"
        "v_mfma_f32_16x16x32_bf16 v[132:135], a[20:23], a[192:195], v[132:135]\n"
        "s_barrier\n"
        "v_mfma_f32_16x16x32_bf16 v[136:139], a[24:27], a[192:195], 0\n"
        "ds_read2_b32 a[50:51], v150 offset0:18 offset1:19\n"
        "v_lshrrev_b32 v233, 8, v254\n"
        "v_mfma_f32_16x16x32_bf16 v[136:139], a[20:23], a[196:199], v[136:139]\n"
        "ds_read_b128 a[240:243], v152 offset:1600\n"
        "v_add_u32 v231, 3776, v152\n"
        "v_mfma_f32_16x16x32_bf16 v[136:139], a[16:19], a[200:203], v[136:139]\n"
        "ds_read2_b32 a[52:53], v150 offset0:144 offset1:145\n"
        "v_min_u32 v231, s16, v231\n"
        "v_mfma_f32_16x16x32_bf16 v[136:139], a[12:15], a[204:207], v[136:139]\n"
        "ds_read2_b32 a[54:55], v150 offset0:146 offset1:147\n"
        "v_add_u32 v244, 0x10000, v254\n"
        "v_mfma_f32_16x16x32_bf16 v[136:139], a[8:11], a[208:211], v[136:139]\n"
        "ds_read_b128 a[244:247], v152 offset:1088\n"
        "v_add_u32 v245, 0x20000, v254\n"
        "v_mfma_f32_16x16x32_bf16 v[136:139], a[4:7], a[212:215], v[136:139]\n"
        "ds_read2_b32 a[56:57], v151 offset0:16 offset1:17\n"
        "s_add_u32 s64, s8, s14\n"
        "s_addc_u32 s65, s9, 0\n"
        "v_mfma_f32_16x16x32_bf16 v[136:139], a[0:3], a[216:219], v[136:139]\n"
        "ds_read2_b32 a[58:59], v151 offset0:18 offset1:19\n"
        "s_waitcnt lgkmcnt(7)\n"
        "v_mfma_f32_16x16x32_bf16 v[140:143], a[0:3], a[220:223], 0\n"
        "ds_read_b128 a[248:251], v152 offset:576\n"
        "s_add_u32 s56, s36, s14\n"
        "s_addc_u32 s57, s37, 0\n"
        "v_mfma_f32_16x16x32_bf16 v[140:143], a[4:7], a[216:219], v[140:143]\n"
        "ds_read2_b32 a[60:61], v151 offset0:144 offset1:145\n"
        "s_lshl2_add_u32 s14, s12, s10\n"
        "s_lshl_b32 s14, s14, 6\n"
        "v_mfma_f32_16x16x32_bf16 v[140:143], a[8:11], a[212:215], v[140:143]\n"
        "ds_read2_b32 a[62:63], v151 offset0:146 offset1:147\n"
        "s_lshl4_add_u32 s14, s40, s14\n"
        "s_add_u32 s14, s14, 1024\n"
        "v_mfma_f32_16x16x32_bf16 v[140:143], a[12:15], a[208:211], v[140:143]\n"
        "ds_read_b128 a[252:255], v152 offset:64\n"
        "s_lshl_b32 s14, s14, 7\n"
        "v_lshl_add_u32 v159, v232, 7, s14\n"
        "v_mfma_f32_16x16x32_bf16 v[140:143], a[16:19], a[204:207], v[140:143]\n"
        "ds_read_b64_tr_b16 a[96:97], v153 offset:0\n"
        "s_add_u32 s24, s94, 0x400000\n"
        "s_addc_u32 s25, s95, 0\n"
        "v_mfma_f32_16x16x32_bf16 v[140:143], a[20:23], a[200:203], v[140:143]\n"
        "ds_read_b64_tr_b16 a[98:99], v153 offset:4096\n"
        "v_mfma_f32_16x16x32_bf16 v[140:143], a[24:27], a[196:199], v[140:143]\n"
        "ds_read2_b32 a[0:1], v148 offset0:32 offset1:33\n"
        "v_mfma_f32_16x16x32_bf16 v[140:143], a[28:31], a[192:195], v[140:143]\n"
        "s_mov_b64 exec, 0\n"
        "s_cmp_eq_u32 s40, 0\n"
        "s_cbranch_scc1 LHB0_%=\n"
        "s_cmp_eq_u32 s40, 1\n"
        "s_cbranch_scc1 LHB1_%=\n"
        "s_cmp_eq_u32 s40, 2\n"
        "s_cbranch_scc1 LHB2_%=\n"
        "s_branch LHB3_%=\n"
        "LHRB_%=:\n"
        "s_mov_b64 exec, -1\n"
        "v_mfma_f32_16x16x32_bf16 v[96:99], a[32:35], a[224:227], v[96:99]\n"
        "ds_read2_b32 a[2:3], v148 offset0:34 offset1:35\n"
        "s_waitcnt vmcnt(0)\n"
        "v_mfma_f32_16x16x32_bf16 v[100:103], a[32:35], a[228:231], v[100:103]\n"
        "s_mov_b64 exec, 0xffff\n"
        "global_store_dwordx2 v234, v[250:251], s[48:49] sc1\n"
        "s_mov_b64 exec, -1\n"
        "v_mfma_f32_16x16x32_bf16 v[100:103], a[36:39], a[224:227], v[100:103]\n"
        "ds_read_b128 a[192:195], v230 offset:0\n"
        "global_load_lds_dwordx4 v144, s[60:61] offset:0\n"
        "v_mfma_f32_16x16x32_bf16 v[104:107], a[40:43], a[224:227], v[104:107]\n"
        "ds_read_b64_tr_b16 a[100:101], v154 offset:0\n"
        "v_add_u32 v230, 3840, v152\n"
        "v_mfma_f32_16x16x32_bf16 v[104:107], a[36:39], a[228:231], v[104:107]\n"
        "ds_read_b64_tr_b16 a[102:103], v154 offset:4096\n"
        "v_min_u32 v230, s16, v230\n"
        "v_mfma_f32_16x16x32_bf16 v[104:107], a[32:35], a[232:235], v[104:107]\n"
        "ds_read2_b32 a[4:5], v148 offset0:160 offset1:161\n"
        "v_lshrrev_b32 v234, 1, v254\n"
        "v_mfma_f32_16x16x32_bf16 v[108:111], a[32:35], a[236:239], v[108:111]\n"
        "ds_read2_b32 a[6:7], v148 offset0:162 offset1:163\n"
        "global_load_lds_dwordx4 v145, s[60:61] offset:1024\n"
        "v_mfma_f32_16x16x32_bf16 v[108:111], a[36:39], a[232:235], v[108:111]\n"
        "ds_read_b128 a[196:199], v152 offset:3200\n"
        "s_add_u32 s14, s51, 504\n"
        "v_lshlrev_b32 v229, 5, v232\n"
        "v_mfma_f32_16x16x32_bf16 v[108:111], a[40:43], a[228:231], v[108:111]\n"
        "ds_read_b64_tr_b16 a[104:105], v153 offset:8192\n"
        "v_lshl_add_u32 v229, v233, 3, v229\n"
        "v_mfma_f32_16x16x32_bf16 v[108:111], a[44:47], a[224:227], v[108:111]\n"
        "ds_read_b64_tr_b16 a[106:107], v153 offset:12288\n"
        "s_waitcnt lgkmcnt(8)\n"
        "v_mfma_f32_16x16x32_bf16 v[128:131], a[48:51], a[224:227], v[128:131]\n"
        "ds_read2_b32 a[8:9], v149 offset0:32 offset1:33\n"
        "global_load_lds_dwordx4 v146, s[60:61] offset:2048\n"
        "v_mfma_f32_16x16x32_bf16 v[128:131], a[44:47], a[228:231], v[128:131]\n"
        "ds_read2_b32 a[10:11], v149 offset0:34 offset1:35\n"
        "v_sub_u32 v229, s14, v229\n"
        "v_mfma_f32_16x16x32_bf16 v[128:131], a[40:43], a[232:235], v[128:131]\n"
        "ds_read_b128 a[200:203], v152 offset:2688\n"
        "s_lshl_b32 s14, s41, 5\n"
        "s_add_u32 s14, s14, s42\n"
        "v_mfma_f32_16x16x32_bf16 v[128:131], a[36:39], a[236:239], v[128:131]\n"
        "ds_read_b64_tr_b16 a[108:109], v154 offset:8192\n"
        "s_add_u32 s14, s14, 512\n"
        "s_lshl_b32 s14, s14, 7\n"
        "v_mfma_f32_16x16x32_bf16 v[128:131], a[32:35], a[240:243], v[128:131]\n"
        "ds_read_b64_tr_b16 a[110:111], v154 offset:12288\n"
        "global_load_lds_dwordx4 v147, s[60:61] offset:3072\n"
        "s_add_u32 m0, s73, 0xc000\n"
        "v_mfma_f32_16x16x32_bf16 v[132:135], a[32:35], a[244:247], v[132:135]\n"
        "ds_read2_b32 a[12:13], v149 offset0:160 offset1:161\n"
        "s_add_u32 s60, s22, s66\n"
        "s_addc_u32 s61, s23, 0\n"
        "v_mfma_f32_16x16x32_bf16 v[132:135], a[36:39], a[240:243], v[132:135]\n"
        "ds_read2_b32 a[14:15], v149 offset0:162 offset1:163\n"
        "s_xor_b32 s66, s29, 0x80000\n"
        "v_add_u32 v234, s14, v234\n"
        "v_mfma_f32_16x16x32_bf16 v[132:135], a[40:43], a[236:239], v[132:135]\n"
        "ds_read_b128 a[204:207], v152 offset:2176\n"
        "s_lshr_b32 s14, s63, 2\n"
        "s_add_u32 s14, s14, 512\n"
        "v_mfma_f32_16x16x32_bf16 v[132:135], a[44:47], a[232:235], v[132:135]\n"
        "ds_read_b64_tr_b16 a[112:113], v153 offset:16384\n"
        "global_load_lds_dwordx4 v144, s[60:61] offset:0\n"
        "s_lshl_b32 s14, s14, 7\n"
        "v_mfma_f32_16x16x32_bf16 v[132:135], a[48:51], a[228:231], v[132:135]\n"
        "ds_read_b64_tr_b16 a[114:115], v153 offset:20480\n"
        "v_mfma_f32_16x16x32_bf16 v[132:135], a[52:55], a[224:227], v[132:135]\n"
        "ds_read2_b32 a[16:17], v150 offset0:32 offset1:33\n"
        "v_mfma_f32_16x16x32_bf16 v[136:139], a[56:59], a[224:227], v[136:139]\n"
        "ds_read2_b32 a[18:19], v150 offset0:34 offset1:35\n"
        "v_mfma_f32_16x16x32_bf16 v[136:139], a[52:55], a[228:231], v[136:139]\n"
        "ds_read_b128 a[208:211], v152 offset:1664\n"
        "global_load_lds_dwordx4 v145, s[60:61] offset:1024\n"
        "v_mfma_f32_16x16x32_bf16 v[136:139], a[48:51], a[232:235], v[136:139]\n"
        "ds_read_b64_tr_b16 a[116:117], v154 offset:16384\n"
        "v_mfma_f32_16x16x32_bf16 v[136:139], a[44:47], a[236:239], v[136:139]\n"
        "ds_read_b64_tr_b16 a[118:119], v154 offset:20480\n"
        "v_mfma_f32_16x16x32_bf16 v[136:139], a[40:43], a[240:243], v[136:139]\n"
        "ds_read2_b32 a[20:21], v150 offset0:160 offset1:161\n"
        "v_mfma_f32_16x16x32_bf16 v[136:139], a[36:39], a[244:247], v[136:139]\n"
        "ds_read2_b32 a[22:23], v150 offset0:162 offset1:163\n"
        "global_load_lds_dwordx4 v146, s[60:61] offset:2048\n"
        "v_mfma_f32_16x16x32_bf16 v[136:139], a[32:35], a[248:251], v[136:139]\n"
        "ds_read_b128 a[212:215], v152 offset:1152\n"
        "v_mfma_f32_16x16x32_bf16 v[140:143], a[32:35], a[252:255], v[140:143]\n"
        "ds_read_b64_tr_b16 a[120:121], v153 offset:24576\n"
        "v_mfma_f32_16x16x32_bf16 v[140:143], a[36:39], a[248:251], v[140:143]\n"
        "ds_read_b64_tr_b16 a[122:123], v153 offset:28672\n"
        "v_mfma_f32_16x16x32_bf16 v[140:143], a[40:43], a[244:247], v[140:143]\n"
        "ds_read_b64_tr_b16 a[124:125], v154 offset:24576\n"
        "global_load_lds_dwordx4 v147, s[60:61] offset:3072\n"
        "s_add_u32 m0, s73, 0x10000\n"
        "v_mfma_f32_16x16x32_bf16 v[140:143], a[44:47], a[240:243], v[140:143]\n"
        "ds_read_b64_tr_b16 a[126:127], v154 offset:28672\n"
        "s_add_u32 s60, s22, s66\n"
        "s_addc_u32 s61, s23, 0\n"
        "v_mfma_f32_16x16x32_bf16 v[140:143], a[48:51], a[236:239], v[140:143]\n"
        "ds_read2_b32 a[24:25], v151 offset0:32 offset1:33\n"
        "s_xor_b32 s66, s29, 0xa0000\n"
        "s_waitcnt lgkmcnt(8)\n"
        "v_mfma_f32_16x16x32_bf16 v[140:143], a[52:55], a[232:235], v[140:143]\n"
        "ds_read2_b32 a[26:27], v151 offset0:34 offset1:35\n"
        "v_mfma_f32_16x16x32_bf16 v[140:143], a[56:59], a[228:231], v[140:143]\n"
        "ds_read_b128 a[216:219], v152 offset:640\n"
        "global_load_lds_dwordx4 v144, s[60:61] offset:0\n"
        "v_mfma_f32_16x16x32_bf16 v[140:143], a[60:63], a[224:227], v[140:143]\n"
        "ds_read2_b32 a[28:29], v151 offset0:160 offset1:161\n"
        "v_mfma_f32_16x16x32_bf16 v[96:99], a[0:3], a[192:195], v[96:99]\n"
        "ds_read2_b32 a[30:31], v151 offset0:162 offset1:163\n"
        "v_mfma_f32_16x16x32_bf16 v[100:103], a[0:3], a[196:199], v[100:103]\n"
        "ds_read_b128 a[220:223], v152 offset:128\n"
        "v_mfma_f32_16x16x32_bf16 v[100:103], a[4:7], a[192:195], v[100:103]\n"
        "ds_read2_b32 a[32:33], v148 offset0:48 offset1:49\n"
        "global_load_lds_dwordx4 v145, s[60:61] offset:1024\n"
        "v_mfma_f32_16x16x32_bf16 v[104:107], a[8:11], a[192:195], v[104:107]\n"
        "ds_read2_b32 a[34:35], v148 offset0:50 offset1:51\n"
        "s_waitcnt lgkmcnt(8)\n"
        "v_mfma_f32_16x16x32_bf16 v[104:107], a[4:7], a[196:199], v[104:107]\n"
        "s_barrier\n"
        "v_mfma_f32_16x16x32_bf16 v[104:107], a[0:3], a[200:203], v[104:107]\n"
        "ds_read_b128 a[224:227], v231 offset:0\n"
        "v_add_u32 v231, 3904, v152\n"
        "v_mfma_f32_16x16x32_bf16 v[108:111], a[0:3], a[204:207], v[108:111]\n"
        "ds_read2_b32 a[36:37], v148 offset0:176 offset1:177\n"
        "global_load_lds_dwordx4 v146, s[60:61] offset:2048\n"
        "v_mfma_f32_16x16x32_bf16 v[108:111], a[4:7], a[200:203], v[108:111]\n"
        "ds_read2_b32 a[38:39], v148 offset0:178 offset1:179\n"
        "v_min_u32 v231, s16, v231\n"
        "v_mfma_f32_16x16x32_bf16 v[108:111], a[8:11], a[196:199], v[108:111]\n"
        "ds_read_b128 a[228:231], v152 offset:3264\n"
        "v_mfma_f32_16x16x32_bf16 v[108:111], a[12:15], a[192:195], v[108:111]\n"
        "ds_read2_b32 a[40:41], v149 offset0:48 offset1:49\n"
        "v_mfma_f32_16x16x32_bf16 v[128:131], a[16:19], a[192:195], v[128:131]\n"
        "ds_read2_b32 a[42:43], v149 offset0:50 offset1:51\n"
        "global_load_lds_dwordx4 v147, s[60:61] offset:3072\n"
        "s_add_u32 m0, s73, 0x14000\n"
        "v_mfma_f32_16x16x32_bf16 v[128:131], a[12:15], a[196:199], v[128:131]\n"
        "ds_read_b128 a[232:235], v152 offset:2752\n"
        "s_add_u32 s60, s22, s66\n"
        "s_addc_u32 s61, s23, 0\n"
        "v_mfma_f32_16x16x32_bf16 v[128:131], a[8:11], a[200:203], v[128:131]\n"
        "ds_read2_b32 a[44:45], v149 offset0:176 offset1:177\n"
        "s_xor_b32 s66, s29, 0xc0000\n"
        "v_mfma_f32_16x16x32_bf16 v[128:131], a[4:7], a[204:207], v[128:131]\n"
        "ds_read2_b32 a[46:47], v149 offset0:178 offset1:179\n"
        "v_mfma_f32_16x16x32_bf16 v[128:131], a[0:3], a[208:211], v[128:131]\n"
        "ds_read_b128 a[236:239], v152 offset:2240\n"
        "global_load_lds_dwordx4 v144, s[60:61] offset:0\n"
        "v_mfma_f32_16x16x32_bf16 v[132:135], a[0:3], a[212:215], v[132:135]\n"
        "ds_read2_b32 a[48:49], v150 offset0:48 offset1:49\n"
        "v_mfma_f32_16x16x32_bf16 v[132:135], a[4:7], a[208:211], v[132:135]\n"
        "ds_read2_b32 a[50:51], v150 offset0:50 offset1:51\n"
        "v_mfma_f32_16x16x32_bf16 v[132:135], a[8:11], a[204:207], v[132:135]\n"
        "ds_read_b128 a[240:243], v152 offset:1728\n"
        "s_waitcnt lgkmcnt(8)\n"
        "v_mfma_f32_16x16x32_bf16 v[132:135], a[12:15], a[200:203], v[132:135]\n"
        "ds_read2_b32 a[52:53], v150 offset0:176 offset1:177\n"
        "global_load_lds_dwordx4 v145, s[60:61] offset:1024\n"
        "v_mfma_f32_16x16x32_bf16 v[132:135], a[16:19], a[196:199], v[132:135]\n"
        "ds_read2_b32 a[54:55], v150 offset0:178 offset1:179\n"
        "v_mfma_f32_16x16x32_bf16 v[132:135], a[20:23], a[192:195], v[132:135]\n"
        "ds_read_b128 a[244:247], v152 offset:1216\n"
        "v_mfma_f32_16x16x32_bf16 v[136:139], a[24:27], a[192:195], v[136:139]\n"
        "ds_read2_b32 a[56:57], v151 offset0:48 offset1:49\n"
        "v_mfma_f32_16x16x32_bf16 v[136:139], a[20:23], a[196:199], v[136:139]\n"
        "ds_read2_b32 a[58:59], v151 offset0:50 offset1:51\n"
        "global_load_lds_dwordx4 v146, s[60:61] offset:2048\n"
        "v_mfma_f32_16x16x32_bf16 v[136:139], a[16:19], a[200:203], v[136:139]\n"
        "ds_read_b128 a[248:251], v152 offset:704\n"
        "v_mfma_f32_16x16x32_bf16 v[136:139], a[12:15], a[204:207], v[136:139]\n"
        "ds_read2_b32 a[60:61], v151 offset0:176 offset1:177\n"
        "v_mfma_f32_16x16x32_bf16 v[136:139], a[8:11], a[208:211], v[136:139]\n"
        "ds_read2_b32 a[62:63], v151 offset0:178 offset1:179\n"
        "v_mfma_f32_16x16x32_bf16 v[136:139], a[4:7], a[212:215], v[136:139]\n"
        "ds_read_b128 a[252:255], v152 offset:192\n"
        "global_load_lds_dwordx4 v147, s[60:61] offset:3072\n"
        "s_add_u32 m0, s73, 0x0\n"
        "v_mfma_f32_16x16x32_bf16 v[136:139], a[0:3], a[216:219], v[136:139]\n"
        "ds_read_b64 v[64:65], v229 offset:3584\n"
        "s_add_u32 s60, s22, s66\n"
        "s_addc_u32 s61, s23, 0\n"
        "v_mfma_f32_16x16x32_bf16 v[140:143], a[0:3], a[220:223], v[140:143]\n"
        "ds_read2_b32 a[0:1], v148 offset0:64 offset1:65\n"
        "s_xor_b32 s66, s29, 0xe0000\n"
        "v_mfma_f32_16x16x32_bf16 v[140:143], a[4:7], a[216:219], v[140:143]\n"
        "ds_read2_b32 a[2:3], v148 offset0:66 offset1:67\n"
        "v_mfma_f32_16x16x32_bf16 v[140:143], a[8:11], a[212:215], v[140:143]\n"
        "ds_read2_b32 a[4:5], v148 offset0:192 offset1:193\n"
        "global_load_lds_dwordx4 v144, s[60:61] offset:0\n"
        "v_mfma_f32_16x16x32_bf16 v[140:143], a[12:15], a[208:211], v[140:143]\n"
        "ds_read2_b32 a[6:7], v148 offset0:194 offset1:195\n"
        "v_mfma_f32_16x16x32_bf16 v[140:143], a[16:19], a[204:207], v[140:143]\n"
        "ds_read2_b32 a[8:9], v149 offset0:64 offset1:65\n"
        "v_mfma_f32_16x16x32_bf16 v[140:143], a[20:23], a[200:203], v[140:143]\n"
        "ds_read2_b32 a[10:11], v149 offset0:66 offset1:67\n"
        "v_mfma_f32_16x16x32_bf16 v[140:143], a[24:27], a[196:199], v[140:143]\n"
        "ds_read_b128 a[196:199], v152 offset:3328\n"
        "global_load_lds_dwordx4 v145, s[60:61] offset:1024\n"
        "v_mfma_f32_16x16x32_bf16 v[140:143], a[28:31], a[192:195], v[140:143]\n"
        "ds_read_b128 a[192:195], v230 offset:0\n"
        "v_add_u32 v230, 3968, v152\n"
        "v_mfma_f32_16x16x32_bf16 v[96:99], a[32:35], a[224:227], v[96:99]\n"
        "ds_read_b128 a[200:203], v152 offset:2816\n"
        "v_min_u32 v230, s16, v230\n"
        "v_mfma_f32_16x16x32_bf16 v[100:103], a[32:35], a[228:231], v[100:103]\n"
        "ds_read2_b32 a[12:13], v149 offset0:192 offset1:193\n"
        "s_waitcnt lgkmcnt(8)\n"
        "v_mfma_f32_16x16x32_bf16 v[100:103], a[36:39], a[224:227], v[100:103]\n"
        "ds_read2_b32 a[14:15], v149 offset0:194 offset1:195\n"
        "global_load_lds_dwordx4 v146, s[60:61] offset:2048\n"
        "v_mfma_f32_16x16x32_bf16 v[104:107], a[40:43], a[224:227], v[104:107]\n"
        "ds_read_b128 a[204:207], v152 offset:2304\n"
        "v_and_b32 v80, s35, v65\n"
        "v_mfma_f32_16x16x32_bf16 v[104:107], a[36:39], a[228:231], v[104:107]\n"
        "ds_read2_b32 a[16:17], v150 offset0:64 offset1:65\n"
        "v_lshlrev_b32 v81, 16, v65\n"
        "v_mfma_f32_16x16x32_bf16 v[104:107], a[32:35], a[232:235], v[104:107]\n"
        "ds_read2_b32 a[18:19], v150 offset0:66 offset1:67\n"
        "v_and_b32 v82, s35, v64\n"
        "v_mfma_f32_16x16x32_bf16 v[108:111], a[32:35], a[236:239], v[108:111]\n"
        "ds_read_b128 a[208:211], v152 offset:1792\n"
        "global_load_lds_dwordx4 v147, s[60:61] offset:3072\n"
        "s_add_u32 m0, s73, 0x4000\n"
        "v_mfma_f32_16x16x32_bf16 v[108:111], a[36:39], a[232:235], v[108:111]\n"
        "ds_read2_b32 a[20:21], v150 offset0:192 offset1:193\n"
        "s_add_u32 s60, s22, s66\n"
        "s_addc_u32 s61, s23, 0\n"
        "v_mfma_f32_16x16x32_bf16 v[108:111], a[40:43], a[228:231], v[108:111]\n"
        "ds_read2_b32 a[22:23], v150 offset0:194 offset1:195\n"
        "s_waitcnt vmcnt(12)\n"
        "v_mfma_f32_16x16x32_bf16 v[108:111], a[44:47], a[224:227], v[108:111]\n"
        "s_barrier\n"
        "v_mfma_f32_16x16x32_bf16 v[128:131], a[48:51], a[224:227], v[128:131]\n"
        "ds_read_b64_tr_b16 a[128:129], v153 offset:32768\n"
        "global_load_lds_dwordx4 v144, s[60:61] offset:0\n"
        "s_xor_b32 s66, s29, 0x100000\n"
        "v_mfma_f32_16x16x32_bf16 v[128:131], a[44:47], a[228:231], v[128:131]\n"
        "ds_read_b64_tr_b16 a[130:131], v153 offset:36864\n"
        "v_lshlrev_b32 v83, 16, v64\n"
        "v_mfma_f32_16x16x32_bf16 v[128:131], a[40:43], a[232:235], v[128:131]\n"
        "ds_read_b64_tr_b16 a[132:133], v154 offset:32768\n"
        "v_mul_f32 v80, s54, v80\n"
        "v_mfma_f32_16x16x32_bf16 v[128:131], a[36:39], a[236:239], v[128:131]\n"
        "ds_read_b64_tr_b16 a[134:135], v154 offset:36864\n"
        "v_mul_f32 v81, s54, v81\n"
        "v_mfma_f32_16x16x32_bf16 v[128:131], a[32:35], a[240:243], v[128:131]\n"
        "ds_read_b64_tr_b16 a[136:137], v153 offset:40960\n"
        "LHC2_%=:\n"
        "s_cbranch_execz LHRC_%=\n"
        "global_load_lds_dwordx4 v145, s[60:61] offset:1024\n"
        "v_mfma_f32_16x16x32_bf16 v[132:135], a[32:35], a[244:247], v[132:135]\n"
        "ds_read_b64_tr_b16 a[138:139], v153 offset:45056\n"
        "v_mul_f32 v82, s54, v82\n"
        "v_mfma_f32_16x16x32_bf16 v[132:135], a[36:39], a[240:243], v[132:135]\n"
        "ds_read_b64_tr_b16 a[140:141], v154 offset:40960\n"
        "v_mul_f32 v83, s54, v83\n"
        "v_mfma_f32_16x16x32_bf16 v[132:135], a[40:43], a[236:239], v[132:135]\n"
        "ds_read_b64_tr_b16 a[142:143], v154 offset:45056\n"
        "v_cvt_pk_bf16_f32 v80, 0, v80\n"
        "v_mfma_f32_16x16x32_bf16 v[132:135], a[44:47], a[232:235], v[132:135]\n"
        "ds_read_b64_tr_b16 a[144:145], v153 offset:49152\n"
        "global_load_lds_dwordx4 v146, s[60:61] offset:2048\n"
        "v_mfma_f32_16x16x32_bf16 v[132:135], a[48:51], a[228:231], v[132:135]\n"
        "ds_read_b64_tr_b16 a[146:147], v153 offset:53248\n"
        "v_cvt_pk_bf16_f32 v81, 0, v81\n"
        "v_mfma_f32_16x16x32_bf16 v[132:135], a[52:55], a[224:227], v[132:135]\n"
        "ds_read_b64_tr_b16 a[148:149], v154 offset:49152\n"
        "v_cvt_pk_bf16_f32 v82, 0, v82\n"
        "v_mfma_f32_16x16x32_bf16 v[136:139], a[56:59], a[224:227], v[136:139]\n"
        "ds_read_b64_tr_b16 a[150:151], v154 offset:53248\n"
        "v_cvt_pk_bf16_f32 v83, 0, v83\n"
        "v_mfma_f32_16x16x32_bf16 v[136:139], a[52:55], a[228:231], v[136:139]\n"
        "ds_read_b128 a[212:215], v152 offset:1280\n"
        "global_load_lds_dwordx4 v147, s[60:61] offset:3072\n"
        "s_add_u32 m0, s73, 0x8000\n"
        "v_mfma_f32_16x16x32_bf16 v[136:139], a[48:51], a[232:235], v[136:139]\n"
        "ds_read_b64_tr_b16 a[152:153], v153 offset:57344\n"
        "s_add_u32 s60, s22, s66\n"
        "s_addc_u32 s61, s23, 0\n"
        "v_mfma_f32_16x16x32_bf16 v[136:139], a[44:47], a[236:239], v[136:139]\n"
        "ds_read_b64_tr_b16 a[154:155], v153 offset:61440\n"
        "s_xor_b32 s66, s29, 0x120000\n"
        "v_mfma_f32_16x16x32_bf16 v[136:139], a[40:43], a[240:243], v[136:139]\n"
        "ds_read2_b32 a[24:25], v151 offset0:64 offset1:65\n"
        "v_mfma_f32_16x16x32_bf16 v[136:139], a[36:39], a[244:247], v[136:139]\n"
        "ds_read2_b32 a[26:27], v151 offset0:66 offset1:67\n"
        "v_mfma_f32_16x16x32_bf16 v[136:139], a[32:35], a[248:251], v[136:139]\n"
        "ds_read_b128 a[216:219], v152 offset:768\n"
        "v_mfma_f32_16x16x32_bf16 v[140:143], a[32:35], a[252:255], v[140:143]\n"
        "ds_read_b64_tr_b16 a[156:157], v154 offset:57344\n"
        "v_mfma_f32_16x16x32_bf16 v[140:143], a[36:39], a[248:251], v[140:143]\n"
        "ds_read_b64_tr_b16 a[158:159], v154 offset:61440\n"
        "v_mfma_f32_16x16x32_bf16 v[140:143], a[40:43], a[244:247], v[140:143]\n"
        "ds_read2_b32 a[28:29], v151 offset0:192 offset1:193\n"
        "v_mfma_f32_16x16x32_bf16 v[140:143], a[44:47], a[240:243], v[140:143]\n"
        "ds_read2_b32 a[30:31], v151 offset0:194 offset1:195\n"
        "v_mfma_f32_16x16x32_bf16 v[140:143], a[48:51], a[236:239], v[140:143]\n"
        "ds_read_b128 a[220:223], v152 offset:256\n"
        "s_waitcnt lgkmcnt(8)\n"
        "v_mfma_f32_16x16x32_bf16 v[140:143], a[52:55], a[232:235], v[140:143]\n"
        "ds_read2_b32 a[32:33], v148 offset0:80 offset1:81\n"
        "v_mfma_f32_16x16x32_bf16 v[140:143], a[56:59], a[228:231], v[140:143]\n"
        "ds_read2_b32 a[34:35], v148 offset0:82 offset1:83\n"
        "v_mfma_f32_16x16x32_bf16 v[140:143], a[60:63], a[224:227], v[140:143]\n"
        "ds_read_b128 a[224:227], v231 offset:0\n"
        "global_load_dwordx2 v[236:237], v159, s[48:49] sc1\n"
        "v_mfma_f32_16x16x32_bf16 v[96:99], a[0:3], a[192:195], v[96:99]\n"
        "ds_read2_b32 a[36:37], v148 offset0:208 offset1:209\n"
        "v_add_u32 v231, 4032, v152\n"
        "v_mfma_f32_16x16x32_bf16 v[100:103], a[0:3], a[196:199], v[100:103]\n"
        "ds_read2_b32 a[38:39], v148 offset0:210 offset1:211\n"
        "v_min_u32 v231, s16, v231\n"
        "v_mfma_f32_16x16x32_bf16 v[100:103], a[4:7], a[192:195], v[100:103]\n"
        "ds_read_b128 a[228:231], v152 offset:3392\n"
        "v_mfma_f32_16x16x32_bf16 v[104:107], a[8:11], a[192:195], v[104:107]\n"
        "ds_read2_b32 a[40:41], v149 offset0:80 offset1:81\n"
        "v_mfma_f32_16x16x32_bf16 v[104:107], a[4:7], a[196:199], v[104:107]\n"
        "ds_read2_b32 a[42:43], v149 offset0:82 offset1:83\n"
        "s_waitcnt vmcnt(9) lgkmcnt(11)\n"
        "v_mfma_f32_16x16x32_bf16 v[104:107], a[0:3], a[200:203], v[104:107]\n"
        "s_barrier\n"
        "v_mfma_f32_16x16x32_bf16 v[108:111], a[0:3], a[204:207], v[108:111]\n"
        "ds_read_b64_tr_b16 v[160:161], v155 offset:0\n"
        "global_load_lds_dwordx4 v144, s[60:61] offset:0\n"
        "v_mfma_f32_16x16x32_bf16 v[108:111], a[4:7], a[200:203], v[108:111]\n"
        "ds_read_b64_tr_b16 v[162:163], v155 offset:4096\n"
        "v_mfma_f32_16x16x32_bf16 v[108:111], a[8:11], a[196:199], v[108:111]\n"
        "ds_read_b64_tr_b16 v[164:165], v156 offset:0\n"
        "v_mfma_f32_16x16x32_bf16 v[108:111], a[12:15], a[192:195], v[108:111]\n"
        "ds_read_b64_tr_b16 v[166:167], v156 offset:4096\n"
        "v_mfma_f32_16x16x32_bf16 v[128:131], a[16:19], a[192:195], v[128:131]\n"
        "ds_read_b64_tr_b16 v[168:169], v155 offset:8192\n"
        "global_load_lds_dwordx4 v145, s[60:61] offset:1024\n"
        "v_mfma_f32_16x16x32_bf16 v[128:131], a[12:15], a[196:199], v[128:131]\n"
        "ds_read_b64_tr_b16 v[170:171], v155 offset:12288\n"
        "v_mfma_f32_16x16x32_bf16 v[128:131], a[8:11], a[200:203], v[128:131]\n"
        "ds_read_b128 a[232:235], v152 offset:2880\n"
        "v_mfma_f32_16x16x32_bf16 v[128:131], a[4:7], a[204:207], v[128:131]\n"
        "ds_read_b64_tr_b16 v[172:173], v156 offset:8192\n"
        "v_mfma_f32_16x16x32_bf16 v[128:131], a[0:3], a[208:211], v[128:131]\n"
        "ds_read_b64_tr_b16 v[174:175], v156 offset:12288\n"
        "global_load_lds_dwordx4 v146, s[60:61] offset:2048\n"
        "v_mfma_f32_16x16x32_bf16 v[132:135], a[0:3], a[212:215], v[132:135]\n"
        "ds_read2_b32 a[44:45], v149 offset0:208 offset1:209\n"
        "v_mfma_f32_16x16x32_bf16 v[132:135], a[4:7], a[208:211], v[132:135]\n"
        "ds_read2_b32 a[46:47], v149 offset0:210 offset1:211\n"
        "v_mfma_f32_16x16x32_bf16 v[132:135], a[8:11], a[204:207], v[132:135]\n"
        "ds_read_b128 a[236:239], v152 offset:2368\n"
        "v_mfma_f32_16x16x32_bf16 v[132:135], a[12:15], a[200:203], v[132:135]\n"
        "ds_read_b64_tr_b16 v[176:177], v155 offset:16384\n"
        "global_load_lds_dwordx4 v147, s[60:61] offset:3072\n"
        "s_add_u32 m0, s73, 0xc000\n"
        "v_mfma_f32_16x16x32_bf16 v[132:135], a[16:19], a[196:199], v[132:135]\n"
        "ds_read_b64_tr_b16 v[178:179], v155 offset:20480\n"
        "s_add_u32 s60, s22, s66\n"
        "s_addc_u32 s61, s23, 0\n"
        "v_mfma_f32_16x16x32_bf16 v[132:135], a[20:23], a[192:195], v[132:135]\n"
        "ds_read2_b32 a[48:49], v150 offset0:80 offset1:81\n"
        "s_sub_u32 s66, 15, s12\n"
        "v_mfma_f32_16x16x32_bf16 v[136:139], a[24:27], a[192:195], v[136:139]\n"
        "ds_read2_b32 a[50:51], v150 offset0:82 offset1:83\n"
        "s_cmp_lt_u32 s74, 2\n"
        "s_cselect_b32 s66, s12, s66\n"
        "v_mfma_f32_16x16x32_bf16 v[136:139], a[20:23], a[196:199], v[136:139]\n"
        "ds_read_b128 a[240:243], v152 offset:1856\n"
        "global_load_lds_dwordx4 v144, s[60:61] offset:0\n"
        "s_mul_i32 s66, s66, 1052672\n"
        "v_mfma_f32_16x16x32_bf16 v[136:139], a[16:19], a[200:203], v[136:139]\n"
        "ds_read_b64_tr_b16 v[180:181], v156 offset:16384\n"
        "s_add_u32 s66, s66, s67\n"
        "v_mfma_f32_16x16x32_bf16 v[136:139], a[12:15], a[204:207], v[136:139]\n"
        "ds_read_b64_tr_b16 v[182:183], v156 offset:20480\n"
        "s_add_u32 s68, s20, s66\n"
        "s_addc_u32 s69, s21, 0\n"
        "v_mfma_f32_16x16x32_bf16 v[136:139], a[8:11], a[208:211], v[136:139]\n"
        "ds_read2_b32 a[52:53], v150 offset0:208 offset1:209\n"
        "s_xor_b32 s66, s17, 0\n"
        "s_lshl_b32 s66, s66, 15\n"
        "v_mfma_f32_16x16x32_bf16 v[136:139], a[4:7], a[212:215], v[136:139]\n"
        "ds_read2_b32 a[54:55], v150 offset0:210 offset1:211\n"
        "global_load_lds_dwordx4 v145, s[60:61] offset:1024\n"
        "v_mfma_f32_16x16x32_bf16 v[136:139], a[0:3], a[216:219], v[136:139]\n"
        "ds_read_b128 a[244:247], v152 offset:1344\n"
        "s_waitcnt lgkmcnt(8)\n"
        "v_mfma_f32_16x16x32_bf16 v[140:143], a[0:3], a[220:223], v[140:143]\n"
        "ds_read_b64_tr_b16 v[184:185], v155 offset:24576\n"
        "v_mfma_f32_16x16x32_bf16 v[140:143], a[4:7], a[216:219], v[140:143]\n"
        "ds_read_b64_tr_b16 v[186:187], v155 offset:28672\n"
        "v_mfma_f32_16x16x32_bf16 v[140:143], a[8:11], a[212:215], v[140:143]\n"
        "ds_read2_b32 a[56:57], v151 offset0:80 offset1:81\n"
        "global_load_lds_dwordx4 v146, s[60:61] offset:2048\n"
        "v_mfma_f32_16x16x32_bf16 v[140:143], a[12:15], a[208:211], v[140:143]\n"
        "ds_read2_b32 a[58:59], v151 offset0:82 offset1:83\n"
        "s_waitcnt vmcnt(7) lgkmcnt(8)\n"
        "v_mfma_f32_16x16x32_bf16 v[140:143], a[16:19], a[204:207], v[140:143]\n"
        "ds_read_b128 a[248:251], v152 offset:832\n"
        "v_cmp_ne_u64 vcc, v[236:237], v[250:251]\n"
        "v_mfma_f32_16x16x32_bf16 v[140:143], a[20:23], a[200:203], v[140:143]\n"
        "ds_read_b64_tr_b16 v[188:189], v156 offset:24576\n"
        "s_cbranch_vccnz LPCX_%=\n"
        "LBCX_%=:\n"
        "v_mfma_f32_16x16x32_bf16 v[140:143], a[24:27], a[196:199], v[140:143]\n"
        "ds_read_b64_tr_b16 v[190:191], v156 offset:28672\n"
        "global_load_lds_dwordx4 v147, s[60:61] offset:3072\n"
        "s_add_u32 m0, s73, 0x10000\n"
        "v_mfma_f32_16x16x32_bf16 v[140:143], a[28:31], a[192:195], v[140:143]\n"
        "ds_read2_b32 a[60:61], v151 offset0:208 offset1:209\n"
        "s_add_u32 s60, s68, s66\n"
        "s_addc_u32 s61, s69, 0\n"
        "v_mfma_f32_16x16x32_bf16 v[96:99], a[32:35], a[224:227], v[96:99]\n"
        "ds_read2_b32 a[62:63], v151 offset0:210 offset1:211\n"
        "s_xor_b32 s66, s17, 1\n"
        "s_lshl_b32 s66, s66, 15\n"
        "v_mfma_f32_16x16x32_bf16 v[100:103], a[32:35], a[228:231], v[100:103]\n"
        "ds_read_b128 a[252:255], v152 offset:320\n"
        "global_load_dword v224, v159, s[48:49] offset:64 sc1\n"
        "v_mfma_f32_16x16x32_bf16 v[100:103], a[36:39], a[224:227], v[100:103]\n"
        "ds_read2_b32 a[0:1], v148 offset0:96 offset1:97\n"
        "v_mfma_f32_16x16x32_bf16 v[104:107], a[40:43], a[224:227], v[104:107]\n"
        "ds_read2_b32 a[2:3], v148 offset0:98 offset1:99\n"
        "v_mfma_f32_16x16x32_bf16 v[104:107], a[36:39], a[228:231], v[104:107]\n"
        "ds_read_b128 a[192:195], v230 offset:0\n"
        "v_lshrrev_b32 v230, 1, v233\n"
        "v_mfma_f32_16x16x32_bf16 v[104:107], a[32:35], a[232:235], v[104:107]\n"
        "ds_read2_b32 a[4:5], v148 offset0:224 offset1:225\n"
        "v_mfma_f32_16x16x32_bf16 v[108:111], a[32:35], a[236:239], v[108:111]\n"
        "ds_read2_b32 a[6:7], v148 offset0:226 offset1:227\n"
        "s_waitcnt lgkmcnt(8)\n"
        "v_mfma_f32_16x16x32_bf16 v[108:111], a[36:39], a[232:235], v[108:111]\n"
        "s_barrier\n"
        "v_mfma_f32_16x16x32_bf16 v[108:111], a[40:43], a[228:231], v[108:111]\n"
        "ds_read_b64_tr_b16 v[192:193], v153 offset:0\n"
        "global_load_lds_dwordx4 v254, s[60:61] offset:0 sc1\n"
        "v_mfma_f32_16x16x32_bf16 v[108:111], a[44:47], a[224:227], v[108:111]\n"
        "ds_read_b64_tr_b16 v[194:195], v153 offset:4096\n"
        "v_mfma_f32_16x16x32_bf16 v[128:131], a[48:51], a[224:227], v[128:131]\n"
        "ds_read_b64_tr_b16 v[196:197], v154 offset:0\n"
        "v_mfma_f32_16x16x32_bf16 v[128:131], a[44:47], a[228:231], v[128:131]\n"
        "ds_read_b64_tr_b16 v[198:199], v154 offset:4096\n"
        "v_mfma_f32_16x16x32_bf16 v[128:131], a[40:43], a[232:235], v[128:131]\n"
        "ds_read_b128 a[196:199], v152 offset:3456\n"
        "global_load_lds_dwordx4 v254, s[60:61] offset:1024 sc1\n"
        "v_mfma_f32_16x16x32_bf16 v[128:131], a[36:39], a[236:239], v[128:131]\n"
        "ds_read_b64_tr_b16 v[200:201], v153 offset:8192\n"
        "v_mfma_f32_16x16x32_bf16 v[128:131], a[32:35], a[240:243], v[128:131]\n"
        "ds_read_b64_tr_b16 v[202:203], v153 offset:12288\n"
        "v_mfma_f32_16x16x32_bf16 v[132:135], a[32:35], a[244:247], v[132:135]\n"
        "ds_read2_b32 a[8:9], v149 offset0:96 offset1:97\n"
        "v_mfma_f32_16x16x32_bf16 v[132:135], a[36:39], a[240:243], v[132:135]\n"
        "ds_read2_b32 a[10:11], v149 offset0:98 offset1:99\n"
        "global_load_lds_dwordx4 v254, s[60:61] offset:2048 sc1\n"
        "v_mfma_f32_16x16x32_bf16 v[132:135], a[40:43], a[236:239], v[132:135]\n"
        "ds_read_b128 a[200:203], v152 offset:2944\n"
        "v_mfma_f32_16x16x32_bf16 v[132:135], a[44:47], a[232:235], v[132:135]\n"
        "ds_read_b64_tr_b16 v[204:205], v154 offset:8192\n"
        "v_mfma_f32_16x16x32_bf16 v[132:135], a[48:51], a[228:231], v[132:135]\n"
        "ds_read_b64_tr_b16 v[206:207], v154 offset:12288\n"
        "v_mfma_f32_16x16x32_bf16 v[132:135], a[52:55], a[224:227], v[132:135]\n"
        "ds_read2_b32 a[12:13], v149 offset0:224 offset1:225\n"
        "global_load_lds_dwordx4 v254, s[60:61] offset:3072 sc1\n"
        "s_add_u32 m0, s73, 0x14000\n"
        "v_mfma_f32_16x16x32_bf16 v[136:139], a[56:59], a[224:227], v[136:139]\n"
        "ds_read2_b32 a[14:15], v149 offset0:226 offset1:227\n"
        "s_add_u32 s60, s68, s66\n"
        "s_addc_u32 s61, s69, 0\n"
        "v_mfma_f32_16x16x32_bf16 v[136:139], a[52:55], a[228:231], v[136:139]\n"
        "ds_read_b128 a[204:207], v152 offset:2432\n"
        "s_xor_b32 s66, s29, 0x140000\n"
        "v_mfma_f32_16x16x32_bf16 v[136:139], a[48:51], a[232:235], v[136:139]\n"
        "ds_read_b64_tr_b16 v[208:209], v153 offset:16384\n"
        "v_mfma_f32_16x16x32_bf16 v[136:139], a[44:47], a[236:239], v[136:139]\n"
        "ds_read_b64_tr_b16 v[210:211], v153 offset:20480\n"
        "global_load_lds_dwordx4 v254, s[60:61] offset:0 sc1\n"
        "v_mfma_f32_16x16x32_bf16 v[136:139], a[40:43], a[240:243], v[136:139]\n"
        "ds_read2_b32 a[16:17], v150 offset0:96 offset1:97\n"
        "s_waitcnt lgkmcnt(8)\n"
        "v_mfma_f32_16x16x32_bf16 v[136:139], a[36:39], a[244:247], v[136:139]\n"
        "ds_read2_b32 a[18:19], v150 offset0:98 offset1:99\n"
        "v_mfma_f32_16x16x32_bf16 v[136:139], a[32:35], a[248:251], v[136:139]\n"
        "ds_read_b128 a[208:211], v152 offset:1920\n"
        "s_and_b32 s67, s75, 1\n"
        "s_lshl_b32 s67, s67, 12\n"
        "v_mfma_f32_16x16x32_bf16 v[140:143], a[32:35], a[252:255], v[140:143]\n"
        "ds_read_b64_tr_b16 v[212:213], v154 offset:16384\n"
        "global_load_lds_dwordx4 v254, s[60:61] offset:1024 sc1\n"
        "v_mfma_f32_16x16x32_bf16 v[140:143], a[36:39], a[248:251], v[140:143]\n"
        "ds_read_b64_tr_b16 v[214:215], v154 offset:20480\n"
        "v_mfma_f32_16x16x32_bf16 v[140:143], a[40:43], a[244:247], v[140:143]\n"
        "ds_read2_b32 a[20:21], v150 offset0:224 offset1:225\n"
        "v_mfma_f32_16x16x32_bf16 v[140:143], a[44:47], a[240:243], v[140:143]\n"
        "ds_read2_b32 a[22:23], v150 offset0:226 offset1:227\n"
        "v_mfma_f32_16x16x32_bf16 v[140:143], a[48:51], a[236:239], v[140:143]\n"
        "ds_read_b128 a[212:215], v152 offset:1408\n"
        "global_load_lds_dwordx4 v254, s[60:61] offset:2048 sc1\n"
        "v_mfma_f32_16x16x32_bf16 v[140:143], a[52:55], a[232:235], v[140:143]\n"
        "ds_read_b64_tr_b16 v[216:217], v153 offset:24576\n"
        "v_mfma_f32_16x16x32_bf16 v[140:143], a[56:59], a[228:231], v[140:143]\n"
        "ds_read_b64_tr_b16 v[218:219], v153 offset:28672\n"
        "v_mfma_f32_16x16x32_bf16 v[140:143], a[60:63], a[224:227], v[140:143]\n"
        "ds_read_b64_tr_b16 v[220:221], v154 offset:24576\n"
        "v_mfma_f32_16x16x32_bf16 v[96:99], a[0:3], a[192:195], v[96:99]\n"
        "ds_read_b64_tr_b16 v[222:223], v154 offset:28672\n"
        "global_load_lds_dwordx4 v254, s[60:61] offset:3072 sc1\n"
        "s_add_u32 m0, s73, 0x0\n"
        "v_mfma_f32_16x16x32_bf16 v[100:103], a[0:3], a[196:199], v[100:103]\n"
        "ds_read2_b32 a[24:25], v151 offset0:96 offset1:97\n"
        "s_add_u32 s60, s22, s66\n"
        "s_addc_u32 s61, s23, 0\n"
        "v_mfma_f32_16x16x32_bf16 v[100:103], a[4:7], a[192:195], v[100:103]\n"
        "ds_read2_b32 a[26:27], v151 offset0:98 offset1:99\n"
        "s_xor_b32 s66, s17, 2\n"
        "s_lshl_b32 s66, s66, 15\n"
        "v_mfma_f32_16x16x32_bf16 v[104:107], a[8:11], a[192:195], v[104:107]\n"
        "ds_read_b128 a[216:219], v152 offset:896\n"
        "s_waitcnt lgkmcnt(8)\n"
        "v_mfma_f32_16x16x32_bf16 v[104:107], a[4:7], a[196:199], v[104:107]\n"
        "ds_read2_b32 a[28:29], v151 offset0:224 offset1:225\n"
        "v_mfma_f32_16x16x32_bf16 v[104:107], a[0:3], a[200:203], v[104:107]\n"
        "ds_read2_b32 a[30:31], v151 offset0:226 offset1:227\n"
        "v_mfma_f32_16x16x32_bf16 v[108:111], a[0:3], a[204:207], v[108:111]\n"
        "ds_read_b128 a[220:223], v152 offset:384\n"
        "v_mfma_f32_16x16x32_bf16 v[108:111], a[4:7], a[200:203], v[108:111]\n"
        "ds_read2_b32 a[32:33], v148 offset0:112 offset1:113\n"
        "v_mfma_f32_16x16x32_bf16 v[108:111], a[8:11], a[196:199], v[108:111]\n"
        "ds_read2_b32 a[34:35], v148 offset0:114 offset1:115\n"
        "v_mfma_f32_16x16x32_bf16 v[108:111], a[12:15], a[192:195], v[108:111]\n"
        "ds_read_b128 a[224:227], v231 offset:0\n"
        "v_mfma_f32_16x16x32_bf16 v[128:131], a[16:19], a[192:195], v[128:131]\n"
        "ds_read2_b32 a[36:37], v148 offset0:240 offset1:241\n"
        "v_mfma_f32_16x16x32_bf16 v[128:131], a[12:15], a[196:199], v[128:131]\n"
        "ds_read2_b32 a[38:39], v148 offset0:242 offset1:243\n"
        "s_waitcnt lgkmcnt(8)\n"
        "v_mfma_f32_16x16x32_bf16 v[128:131], a[8:11], a[200:203], v[128:131]\n"
        "ds_read_b128 a[228:231], v152 offset:3520\n"
        "s_waitcnt vmcnt(9)\n"
        "v_mfma_f32_16x16x32_bf16 v[128:131], a[4:7], a[204:207], v[128:131]\n"
        "s_barrier\n"
        "v_mfma_f32_16x16x32_bf16 v[128:131], a[0:3], a[208:211], v[128:131]\n"
        "ds_read_b64_tr_b16 v[112:113], v153 offset:32768\n"
        "global_load_lds_dwordx4 v144, s[60:61] offset:0\n"
        "v_mfma_f32_16x16x32_bf16 v[132:135], a[0:3], a[212:215], v[132:135]\n"
        "ds_read_b64_tr_b16 v[114:115], v153 offset:36864\n"
        "v_mfma_f32_16x16x32_bf16 v[132:135], a[4:7], a[208:211], v[132:135]\n"
        "ds_read_b64_tr_b16 v[116:117], v154 offset:32768\n"
        "v_mfma_f32_16x16x32_bf16 v[132:135], a[8:11], a[204:207], v[132:135]\n"
        "ds_read_b64_tr_b16 v[118:119], v154 offset:36864\n"
        "v_mfma_f32_16x16x32_bf16 v[132:135], a[12:15], a[200:203], v[132:135]\n"
        "ds_read_b64_tr_b16 v[120:121], v153 offset:40960\n"
        "global_load_lds_dwordx4 v145, s[60:61] offset:1024\n"
        "v_mfma_f32_16x16x32_bf16 v[132:135], a[16:19], a[196:199], v[132:135]\n"
        "ds_read_b64_tr_b16 v[122:123], v153 offset:45056\n"
        "v_mfma_f32_16x16x32_bf16 v[132:135], a[20:23], a[192:195], v[132:135]\n"
        "ds_read2_b32 a[40:41], v149 offset0:112 offset1:113\n"
        "v_mfma_f32_16x16x32_bf16 v[136:139], a[24:27], a[192:195], v[136:139]\n"
        "ds_read2_b32 a[42:43], v149 offset0:114 offset1:115\n"
        "v_mfma_f32_16x16x32_bf16 v[136:139], a[20:23], a[196:199], v[136:139]\n"
        "ds_read_b128 a[232:235], v152 offset:3008\n"
        "global_load_lds_dwordx4 v146, s[60:61] offset:2048\n"
        "v_mfma_f32_16x16x32_bf16 v[136:139], a[16:19], a[200:203], v[136:139]\n"
        "ds_read_b64_tr_b16 v[124:125], v154 offset:40960\n"
        "v_mfma_f32_16x16x32_bf16 v[136:139], a[12:15], a[204:207], v[136:139]\n"
        "ds_read_b64_tr_b16 v[126:127], v154 offset:45056\n"
        "s_waitcnt lgkmcnt(8)\n"
        "v_mfma_f32_16x16x32_bf16 v[136:139], a[8:11], a[208:211], v[136:139]\n"
        "ds_read2_b32 a[44:45], v149 offset0:240 offset1:241\n"
        "v_mfma_f32_16x16x32_bf16 v[136:139], a[4:7], a[212:215], v[136:139]\n"
        "ds_read2_b32 a[46:47], v149 offset0:242 offset1:243\n"
        "global_load_lds_dwordx4 v147, s[60:61] offset:3072\n"
        "s_add_u32 m0, s73, 0x4000\n"
        "v_mfma_f32_16x16x32_bf16 v[136:139], a[0:3], a[216:219], v[136:139]\n"
        "ds_read_b128 a[236:239], v152 offset:2496\n"
        "s_add_u32 s60, s68, s66\n"
        "s_addc_u32 s61, s69, 0\n"
        "v_mfma_f32_16x16x32_bf16 v[140:143], a[0:3], a[220:223], v[140:143]\n"
        "ds_read_b64_tr_b16 a[64:65], v153 offset:49152\n"
        "s_xor_b32 s66, s29, 0x160000\n"
        "v_mfma_f32_16x16x32_bf16 v[140:143], a[4:7], a[216:219], v[140:143]\n"
        "ds_read_b64_tr_b16 a[66:67], v153 offset:53248\n"
        "v_mfma_f32_16x16x32_bf16 v[140:143], a[8:11], a[212:215], v[140:143]\n"
        "ds_read2_b32 a[48:49], v150 offset0:112 offset1:113\n"
        "global_load_lds_dwordx4 v254, s[60:61] offset:0 sc1\n"
        "v_mfma_f32_16x16x32_bf16 v[140:143], a[12:15], a[208:211], v[140:143]\n"
        "ds_read2_b32 a[50:51], v150 offset0:114 offset1:115\n"
        "v_mfma_f32_16x16x32_bf16 v[140:143], a[16:19], a[204:207], v[140:143]\n"
        "ds_read_b128 a[240:243], v152 offset:1984\n"
        "v_mfma_f32_16x16x32_bf16 v[140:143], a[20:23], a[200:203], v[140:143]\n"
        "ds_read_b64_tr_b16 a[68:69], v154 offset:49152\n"
        "v_mfma_f32_16x16x32_bf16 v[140:143], a[24:27], a[196:199], v[140:143]\n"
        "ds_read_b64_tr_b16 a[70:71], v154 offset:53248\n"
        "global_load_lds_dwordx4 v254, s[60:61] offset:1024 sc1\n"
        "v_mfma_f32_16x16x32_bf16 v[140:143], a[28:31], a[192:195], v[140:143]\n"
        "ds_read2_b32 a[52:53], v150 offset0:240 offset1:241\n"
        "s_waitcnt lgkmcnt(8)\n"
        "v_mfma_f32_16x16x32_bf16 v[96:99], a[32:35], a[224:227], v[96:99]\n"
        "ds_read2_b32 a[54:55], v150 offset0:242 offset1:243\n"
        "v_mfma_f32_16x16x32_bf16 v[100:103], a[32:35], a[228:231], v[100:103]\n"
        "ds_read_b128 a[244:247], v152 offset:1472\n"
        "v_mfma_f32_16x16x32_bf16 v[100:103], a[36:39], a[224:227], v[100:103]\n"
        "ds_read_b64_tr_b16 a[72:73], v153 offset:57344\n"
        "global_load_lds_dwordx4 v254, s[60:61] offset:2048 sc1\n"
        "v_mfma_f32_16x16x32_bf16 v[104:107], a[40:43], a[224:227], v[104:107]\n"
        "ds_read_b64_tr_b16 a[74:75], v153 offset:61440\n"
        "v_cvt_pk_bf16_f32 v96, 0, v96\n"
        "v_mfma_f32_16x16x32_bf16 v[104:107], a[36:39], a[228:231], v[104:107]\n"
        "ds_read_b64_tr_b16 a[76:77], v154 offset:57344\n"
        "v_cvt_pk_bf16_f32 v97, 0, v97\n"
        "v_mfma_f32_16x16x32_bf16 v[104:107], a[32:35], a[232:235], v[104:107]\n"
        "ds_read_b64_tr_b16 a[78:79], v154 offset:61440\n"
        "v_cvt_pk_bf16_f32 v98, 0, v98\n"
        "v_mfma_f32_16x16x32_bf16 v[108:111], a[32:35], a[236:239], v[108:111]\n"
        "ds_read2_b32 a[56:57], v151 offset0:112 offset1:113\n"
        "global_load_lds_dwordx4 v254, s[60:61] offset:3072 sc1\n"
        "s_add_u32 m0, s73, 0x8000\n"
        "v_mfma_f32_16x16x32_bf16 v[108:111], a[36:39], a[232:235], v[108:111]\n"
        "ds_read2_b32 a[58:59], v151 offset0:114 offset1:115\n"
        "s_add_u32 s60, s22, s66\n"
        "s_addc_u32 s61, s23, 0\n"
        "v_mfma_f32_16x16x32_bf16 v[108:111], a[40:43], a[228:231], v[108:111]\n"
        "ds_read_b128 a[248:251], v152 offset:960\n"
        "s_xor_b32 s66, s17, 3\n"
        "s_lshl_b32 s66, s66, 15\n"
        "v_mfma_f32_16x16x32_bf16 v[108:111], a[44:47], a[224:227], v[108:111]\n"
        "ds_read_b64 v[66:67], v229 offset:3072\n"
        "s_waitcnt lgkmcnt(8)\n"
        "v_mfma_f32_16x16x32_bf16 v[128:131], a[48:51], a[224:227], v[128:131]\n"
        "ds_read_b64 v[68:69], v229 offset:2560\n"
        "v_cvt_pk_bf16_f32 v99, 0, v99\n"
        "v_mfma_f32_16x16x32_bf16 v[128:131], a[44:47], a[228:231], v[128:131]\n"
        "ds_read_b64 v[70:71], v229 offset:2048\n"
        "v_add_f32 v80, v80, v96\n"
        "v_mfma_f32_16x16x32_bf16 v[128:131], a[40:43], a[232:235], v[128:131]\n"
        "ds_read_b64 v[72:73], v229 offset:1536\n"
        "v_add_f32 v81, v81, v97\n"
        "v_mfma_f32_16x16x32_bf16 v[128:131], a[36:39], a[236:239], v[128:131]\n"
        "ds_read_b64 v[74:75], v229 offset:1024\n"
        "s_waitcnt lgkmcnt(8)\n"
        "v_mfma_f32_16x16x32_bf16 v[128:131], a[32:35], a[240:243], v[128:131]\n"
        "s_barrier\n"
        "v_mfma_f32_16x16x32_bf16 v[132:135], a[32:35], a[244:247], v[132:135]\n"
        "ds_read_b64 v[76:77], v229 offset:512\n"
        "global_load_lds_dwordx4 v144, s[60:61] offset:0\n"
        "v_mfma_f32_16x16x32_bf16 v[132:135], a[36:39], a[240:243], v[132:135]\n"
        "ds_read_b64 v[78:79], v229 offset:0\n"
        "v_and_b32 v229, 1, v233\n"
        "v_mfma_f32_16x16x32_bf16 v[132:135], a[40:43], a[236:239], v[132:135]\n"
        "ds_read_b128 a[252:255], v152 offset:448\n"
        "v_lshlrev_b32 v229, 9, v229\n"
        "v_mfma_f32_16x16x32_bf16 v[132:135], a[44:47], a[232:235], v[132:135]\n"
        "ds_read2_b32 a[60:61], v151 offset0:240 offset1:241\n"
        "global_load_lds_dwordx4 v145, s[60:61] offset:1024\n"
        "v_mfma_f32_16x16x32_bf16 v[132:135], a[48:51], a[228:231], v[132:135]\n"
        "ds_read2_b32 a[62:63], v151 offset0:242 offset1:243\n"
        "v_lshl_add_u32 v229, v232, 5, v229\n"
        "v_mfma_f32_16x16x32_bf16 v[132:135], a[52:55], a[224:227], v[132:135]\n"
        "s_waitcnt lgkmcnt(6)\n"
        "v_lshl_add_u32 v229, v230, 4, v229\n"
        "v_add_f32 v82, v82, v98\n"
        "v_mfma_f32_16x16x32_bf16 v[136:139], a[56:59], a[224:227], v[136:139]\n"
        "global_load_lds_dwordx4 v146, s[60:61] offset:2048\n"
        "v_mfma_f32_16x16x32_bf16 v[136:139], a[52:55], a[228:231], v[136:139]\n"
        "v_add_f32 v83, v83, v99\n"
        "v_cvt_pk_bf16_f32 v88, v80, v81\n"
        "v_mfma_f32_16x16x32_bf16 v[136:139], a[48:51], a[232:235], v[136:139]\n"
        "v_cvt_pk_bf16_f32 v89, v82, v83\n"
        "v_and_b32 v84, s35, v67\n"
        "v_mfma_f32_16x16x32_bf16 v[136:139], a[44:47], a[236:239], v[136:139]\n"
        "global_load_lds_dwordx4 v147, s[60:61] offset:3072\n"
        "s_add_u32 m0, s73, 0xc000\n"
        "v_mfma_f32_16x16x32_bf16 v[136:139], a[40:43], a[240:243], v[136:139]\n"
        "s_add_u32 s60, s68, s66\n"
        "s_addc_u32 s61, s69, 0\n"
        "s_xor_b32 s66, s29, 0x180000\n"
        "v_mfma_f32_16x16x32_bf16 v[136:139], a[36:39], a[244:247], v[136:139]\n"
        "s_waitcnt lgkmcnt(1)\n"
        "v_lshlrev_b32 v85, 16, v67\n"
        "v_and_b32 v86, s35, v66\n"
        "v_mfma_f32_16x16x32_bf16 v[136:139], a[32:35], a[248:251], v[136:139]\n"
        "s_waitcnt lgkmcnt(0)\n"
        "global_load_lds_dwordx4 v254, s[60:61] offset:0 sc1\n"
        "v_mfma_f32_16x16x32_bf16 v[140:143], a[32:35], a[252:255], v[140:143]\n"
        "s_barrier\n"
        "v_mfma_f32_16x16x32_bf16 v[140:143], a[36:39], a[248:251], v[140:143]\n"
        "s_waitcnt vmcnt(17)\n"
        "v_lshlrev_b32 v87, 16, v66\n"
        "v_mul_f32 v84, s54, v84\n"
        "v_mfma_f32_16x16x32_bf16 v[140:143], a[40:43], a[244:247], v[140:143]\n"
        "s_barrier\n"
        "v_mfma_f32_16x16x32_bf16 v[140:143], a[44:47], a[240:243], v[140:143]\n"
        "ds_read_b128 a[0:3], v244 offset:0\n"
        "global_load_lds_dwordx4 v254, s[60:61] offset:1024 sc1\n"
        "v_mfma_f32_16x16x32_bf16 v[140:143], a[48:51], a[236:239], v[140:143]\n"
        "ds_read_b128 a[4:7], v244 offset:4096\n"
        "v_mul_f32 v85, s54, v85\n"
        "v_mfma_f32_16x16x32_bf16 v[140:143], a[52:55], a[232:235], v[140:143]\n"
        "ds_read_b128 a[8:11], v244 offset:8192\n"
        "v_mul_f32 v86, s54, v86\n"
        "v_mfma_f32_16x16x32_bf16 v[140:143], a[56:59], a[228:231], v[140:143]\n"
        "ds_read_b128 a[12:15], v244 offset:12288\n"
        "global_load_lds_dwordx4 v254, s[60:61] offset:2048 sc1\n"
        "v_mfma_f32_16x16x32_bf16 v[140:143], a[60:63], a[224:227], v[140:143]\n"
        "ds_read_b128 a[16:19], v244 offset:1024\n"
        "global_load_lds_dwordx4 v254, s[60:61] offset:3072 sc1\n"
        "s_add_u32 m0, s73, 0x18000\n"
        "s_add_u32 s60, s22, s66\n"
        "s_addc_u32 s61, s23, 0\n"
        "ds_read_b128 a[20:23], v244 offset:5120\n"
        "s_xor_b32 s66, s17, 4\n"
        "s_waitcnt lgkmcnt(5)\n"
        "v_mfma_f32_16x16x32_bf16 v[0:3], a[96:99], a[0:3], 0\n"
        "ds_read_b128 a[24:27], v244 offset:9216\n"
        "s_lshl_b32 s66, s66, 15\n"
        "s_waitcnt lgkmcnt(5)\n"
        "v_mfma_f32_16x16x32_bf16 v[0:3], a[104:107], a[4:7], v[0:3]\n"
        "ds_read_b128 a[28:31], v244 offset:13312\n"
        "s_waitcnt lgkmcnt(5)\n"
        "v_mfma_f32_16x16x32_bf16 v[0:3], a[112:115], a[8:11], v[0:3]\n"
        "ds_read_b128 a[192:195], v244 offset:2048\n"
        "s_waitcnt lgkmcnt(5)\n"
        "v_mfma_f32_16x16x32_bf16 v[0:3], a[120:123], a[12:15], v[0:3]\n"
        "ds_read_b128 a[196:199], v244 offset:6144\n"
        "global_load_lds_dwordx4 v144, s[60:61] offset:0\n"
        "v_mfma_f32_16x16x32_bf16 v[4:7], a[124:127], a[12:15], 0\n"
        "ds_read_b128 a[200:203], v244 offset:10240\n"
        "v_mul_f32 v87, s54, v87\n"
        "v_mfma_f32_16x16x32_bf16 v[4:7], a[116:119], a[8:11], v[4:7]\n"
        "ds_read_b128 a[204:207], v244 offset:14336\n"
        "v_cvt_pk_bf16_f32 v84, 0, v84\n"
        "v_mfma_f32_16x16x32_bf16 v[4:7], a[108:111], a[4:7], v[4:7]\n"
        "ds_read_b128 a[208:211], v244 offset:3072\n"
        "global_load_lds_dwordx4 v145, s[60:61] offset:1024\n"
        "v_mfma_f32_16x16x32_bf16 v[4:7], a[100:103], a[0:3], v[4:7]\n"
        "ds_read_b128 a[212:215], v244 offset:7168\n"
        "s_waitcnt lgkmcnt(8)\n"
        "v_mfma_f32_16x16x32_bf16 v[12:15], a[100:103], a[16:19], 0\n"
        "ds_read_b128 a[216:219], v244 offset:11264\n"
        "v_cvt_pk_bf16_f32 v85, 0, v85\n"
        "v_mfma_f32_16x16x32_bf16 v[12:15], a[108:111], a[20:23], v[12:15]\n"
        "ds_read_b128 a[220:223], v244 offset:15360\n"
        "s_waitcnt lgkmcnt(8)\n"
        "v_mfma_f32_16x16x32_bf16 v[12:15], a[116:119], a[24:27], v[12:15]\n"
        "global_load_lds_dwordx4 v146, s[60:61] offset:2048\n"
        "v_cvt_pk_bf16_f32 v86, 0, v86\n"
        "v_mfma_f32_16x16x32_bf16 v[12:15], a[124:127], a[28:31], v[12:15]\n"
        "v_cvt_pk_bf16_f32 v87, 0, v87\n"
        "v_cvt_pk_bf16_f32 v100, 0, v100\n"
        "v_mfma_f32_16x16x32_bf16 v[8:11], a[120:123], a[28:31], 0\n"
        "s_waitcnt lgkmcnt(5)\n"
        "v_cvt_pk_bf16_f32 v101, 0, v101\n"
        "v_cvt_pk_bf16_f32 v102, 0, v102\n"
        "v_mfma_f32_16x16x32_bf16 v[8:11], a[112:115], a[24:27], v[8:11]\n"
        "global_load_lds_dwordx4 v147, s[60:61] offset:3072\n"
        "s_add_u32 m0, s73, 0x1c000\n"
        "s_lshl2_add_u32 s15, s74, s10\n"
        "v_mfma_f32_16x16x32_bf16 v[8:11], a[104:107], a[20:23], v[8:11]\n"
        "s_add_u32 s60, s68, s66\n"
        "s_addc_u32 s61, s69, 0\n"
        "s_xor_b32 s66, s29, 0x1a0000\n"
        "v_mfma_f32_16x16x32_bf16 v[8:11], a[96:99], a[16:19], v[8:11]\n"
        "s_waitcnt vmcnt(16)\n"
        "s_mul_i32 s16, s13, 263168\n"
        "s_lshl_b32 s15, s15, 14\n"
        "v_mfma_f32_16x16x32_bf16 v[16:19], a[96:99], a[192:195], 0\n"
        "s_barrier\n"
        "v_mfma_f32_16x16x32_bf16 v[16:19], a[104:107], a[196:199], v[16:19]\n"
        "ds_read_b64_tr_b16 a[80:81], v153 offset:0\n"
        "global_load_lds_dwordx4 v254, s[60:61] offset:0 sc1\n"
        "s_add_u32 s16, s16, s15\n"
        "v_mfma_f32_16x16x32_bf16 v[16:19], a[112:115], a[200:203], v[16:19]\n"
        "ds_read_b64_tr_b16 a[82:83], v153 offset:4096\n"
        "s_add_u32 s16, s16, s73\n"
        "s_waitcnt lgkmcnt(2)\n"
        "v_mfma_f32_16x16x32_bf16 v[16:19], a[120:123], a[204:207], v[16:19]\n"
        "ds_read_b64_tr_b16 a[84:85], v154 offset:0\n"
        "s_add_u32 s16, s16, 0x4000000\n"
        "v_cvt_pk_bf16_f32 v103, 0, v103\n"
        "v_mfma_f32_16x16x32_bf16 v[20:23], a[124:127], a[204:207], 0\n"
        "ds_read_b64_tr_b16 a[86:87], v154 offset:4096\n"
        "global_load_lds_dwordx4 v254, s[60:61] offset:1024 sc1\n"
        "v_mfma_f32_16x16x32_bf16 v[20:23], a[116:119], a[200:203], v[20:23]\n"
        "ds_read_b64_tr_b16 a[88:89], v153 offset:8192\n"
        "v_add_f32 v84, v84, v100\n"
        "v_mfma_f32_16x16x32_bf16 v[20:23], a[108:111], a[196:199], v[20:23]\n"
        "ds_read_b64_tr_b16 a[90:91], v153 offset:12288\n"
        "v_add_f32 v85, v85, v101\n"
        "v_mfma_f32_16x16x32_bf16 v[20:23], a[100:103], a[192:195], v[20:23]\n"
        "ds_read_b64_tr_b16 a[92:93], v154 offset:8192\n"
        "global_load_lds_dwordx4 v254, s[60:61] offset:2048 sc1\n"
        "v_mfma_f32_16x16x32_bf16 v[28:31], a[100:103], a[208:211], 0\n"
        "ds_read_b64_tr_b16 a[94:95], v154 offset:12288\n"
        "v_add_f32 v86, v86, v102\n"
        "v_mfma_f32_16x16x32_bf16 v[28:31], a[108:111], a[212:215], v[28:31]\n"
        "ds_read_b128 a[0:3], v244 offset:16384\n"
        "v_add_f32 v87, v87, v103\n"
        "v_mfma_f32_16x16x32_bf16 v[28:31], a[116:119], a[216:219], v[28:31]\n"
        "ds_read_b128 a[4:7], v244 offset:20480\n"
        "global_load_lds_dwordx4 v254, s[60:61] offset:3072 sc1\n"
        "s_add_u32 m0, s73, 0x20000\n"
        "v_mfma_f32_16x16x32_bf16 v[28:31], a[124:127], a[220:223], v[28:31]\n"
        "ds_read_b128 a[8:11], v244 offset:24576\n"
        "s_add_u32 s60, s22, s66\n"
        "s_addc_u32 s61, s23, 0\n"
        "v_mfma_f32_16x16x32_bf16 v[24:27], a[120:123], a[220:223], 0\n"
        "ds_read_b128 a[12:15], v244 offset:28672\n"
        "s_xor_b32 s66, s17, 5\n"
        "s_lshl_b32 s66, s66, 15\n"
        "v_mfma_f32_16x16x32_bf16 v[24:27], a[112:115], a[216:219], v[24:27]\n"
        "ds_read_b128 a[16:19], v244 offset:17408\n"
        "global_load_lds_dwordx4 v144, s[60:61] offset:0\n"
        "v_mfma_f32_16x16x32_bf16 v[24:27], a[104:107], a[212:215], v[24:27]\n"
        "ds_read_b128 a[20:23], v244 offset:21504\n"
        "v_cvt_pk_bf16_f32 v90, v84, v85\n"
        "v_mfma_f32_16x16x32_bf16 v[24:27], a[96:99], a[208:211], v[24:27]\n"
        "ds_read_b128 a[24:27], v244 offset:25600\n"
        "LHC3_%=:\n"
        "s_cbranch_execz LHRC_%=\n"
        "ds_read_b128 a[28:31], v244 offset:29696\n"
        "ds_read_b128 a[192:195], v244 offset:18432\n"
        "global_load_lds_dwordx4 v145, s[60:61] offset:1024\n"
        "s_waitcnt lgkmcnt(8)\n"
        "v_mfma_f32_16x16x32_bf16 v[0:3], a[128:131], a[0:3], v[0:3]\n"
        "ds_read_b128 a[196:199], v244 offset:22528\n"
        "s_waitcnt lgkmcnt(8)\n"
        "v_mfma_f32_16x16x32_bf16 v[0:3], a[136:139], a[4:7], v[0:3]\n"
        "ds_read_b128 a[200:203], v244 offset:26624\n"
        "s_waitcnt lgkmcnt(8)\n"
        "v_mfma_f32_16x16x32_bf16 v[0:3], a[144:147], a[8:11], v[0:3]\n"
        "ds_read_b128 a[204:207], v244 offset:30720\n"
        "s_waitcnt lgkmcnt(8)\n"
        "v_mfma_f32_16x16x32_bf16 v[0:3], a[152:155], a[12:15], v[0:3]\n"
        "ds_read_b128 a[208:211], v244 offset:19456\n"
        "global_load_lds_dwordx4 v146, s[60:61] offset:2048\n"
        "v_mfma_f32_16x16x32_bf16 v[4:7], a[156:159], a[12:15], v[4:7]\n"
        "s_waitcnt lgkmcnt(8)\n"
        "ds_read_b128 a[212:215], v244 offset:23552\n"
        "v_mfma_f32_16x16x32_bf16 v[4:7], a[148:151], a[8:11], v[4:7]\n"
        "s_waitcnt lgkmcnt(8)\n"
        "ds_read_b128 a[216:219], v244 offset:27648\n"
        "v_mfma_f32_16x16x32_bf16 v[4:7], a[140:143], a[4:7], v[4:7]\n"
        "s_waitcnt lgkmcnt(8)\n"
        "ds_read_b128 a[220:223], v244 offset:31744\n"
        "v_mfma_f32_16x16x32_bf16 v[4:7], a[132:135], a[0:3], v[4:7]\n"
        "s_waitcnt lgkmcnt(7)\n"
        "v_permlane16_swap_b32 v88, v90\n"
        "v_mfma_f32_16x16x32_bf16 v[12:15], a[132:135], a[16:19], v[12:15]\n"
        "global_load_lds_dwordx4 v147, s[60:61] offset:3072\n"
        "s_add_u32 m0, s73, 0x24000\n"
        "v_cvt_pk_bf16_f32 v91, v86, v87\n"
        "v_mfma_f32_16x16x32_bf16 v[12:15], a[140:143], a[20:23], v[12:15]\n"
        "s_add_u32 s60, s68, s66\n"
        "s_addc_u32 s61, s69, 0\n"
        "s_xor_b32 s66, s29, 0x1c0000\n"
        "v_mfma_f32_16x16x32_bf16 v[12:15], a[148:151], a[24:27], v[12:15]\n"
        "v_permlane16_swap_b32 v89, v91\n"
        "v_mfma_f32_16x16x32_bf16 v[12:15], a[156:159], a[28:31], v[12:15]\n"
        "global_load_lds_dwordx4 v254, s[60:61] offset:0 sc1\n"
        "v_and_b32 v80, s35, v69\n"
        "v_mfma_f32_16x16x32_bf16 v[8:11], a[152:155], a[28:31], v[8:11]\n"
        "v_lshlrev_b32 v81, 16, v69\n"
        "v_and_b32 v82, s35, v68\n"
        "v_mfma_f32_16x16x32_bf16 v[8:11], a[144:147], a[24:27], v[8:11]\n"
        "s_waitcnt lgkmcnt(2)\n"
        "v_lshlrev_b32 v83, 16, v68\n"
        "v_mul_f32 v80, s54, v80\n"
        "v_mfma_f32_16x16x32_bf16 v[8:11], a[136:139], a[20:23], v[8:11]\n"
        "global_load_lds_dwordx4 v254, s[60:61] offset:1024 sc1\n"
        "v_mul_f32 v81, s54, v81\n"
        "v_mfma_f32_16x16x32_bf16 v[8:11], a[128:131], a[16:19], v[8:11]\n"
        "s_waitcnt vmcnt(18)\n"
        "v_mul_f32 v82, s54, v82\n"
        "v_mul_f32 v83, s54, v83\n"
        "v_mfma_f32_16x16x32_bf16 v[16:19], a[128:131], a[192:195], v[16:19]\n"
        "s_barrier\n"
        "v_mfma_f32_16x16x32_bf16 v[16:19], a[136:139], a[196:199], v[16:19]\n"
        "ds_read_b64_tr_b16 a[160:161], v153 offset:32768\n"
        "global_load_lds_dwordx4 v254, s[60:61] offset:2048 sc1\n"
        "v_mfma_f32_16x16x32_bf16 v[16:19], a[144:147], a[200:203], v[16:19]\n"
        "ds_read_b64_tr_b16 a[162:163], v153 offset:36864\n"
        "v_cvt_pk_bf16_f32 v80, 0, v80\n"
        "v_mfma_f32_16x16x32_bf16 v[16:19], a[152:155], a[204:207], v[16:19]\n"
        "ds_read_b64_tr_b16 a[164:165], v154 offset:32768\n"
        "v_cvt_pk_bf16_f32 v81, 0, v81\n"
        "v_mfma_f32_16x16x32_bf16 v[20:23], a[156:159], a[204:207], v[20:23]\n"
        "ds_read_b64_tr_b16 a[166:167], v154 offset:36864\n"
        "global_load_lds_dwordx4 v254, s[60:61] offset:3072 sc1\n"
        "s_add_u32 m0, s73, 0x10000\n"
        "v_mfma_f32_16x16x32_bf16 v[20:23], a[148:151], a[200:203], v[20:23]\n"
        "ds_read_b64_tr_b16 a[168:169], v153 offset:40960\n"
        "s_add_u32 s60, s22, s66\n"
        "s_addc_u32 s61, s23, 0\n"
        "v_mfma_f32_16x16x32_bf16 v[20:23], a[140:143], a[196:199], v[20:23]\n"
        "ds_read_b64_tr_b16 a[170:171], v153 offset:45056\n"
        "s_xor_b32 s66, s17, 6\n"
        "s_lshl_b32 s66, s66, 15\n"
        "v_mfma_f32_16x16x32_bf16 v[20:23], a[132:135], a[192:195], v[20:23]\n"
        "ds_read_b64_tr_b16 a[172:173], v154 offset:40960\n"
        "global_load_lds_dwordx4 v144, s[60:61] offset:0\n"
        "v_mfma_f32_16x16x32_bf16 v[28:31], a[132:135], a[208:211], v[28:31]\n"
        "ds_read_b64_tr_b16 a[174:175], v154 offset:45056\n"
        "v_cvt_pk_bf16_f32 v82, 0, v82\n"
        "v_mfma_f32_16x16x32_bf16 v[28:31], a[140:143], a[212:215], v[28:31]\n"
        "ds_read_b128 a[0:3], v254 offset:16384\n"
        "s_waitcnt lgkmcnt(8)\n"
        "v_mfma_f32_16x16x32_bf16 v[28:31], a[148:151], a[216:219], v[28:31]\n"
        "ds_read_b128 a[4:7], v254 offset:20480\n"
        "global_load_lds_dwordx4 v145, s[60:61] offset:1024\n"
        "v_mfma_f32_16x16x32_bf16 v[28:31], a[156:159], a[220:223], v[28:31]\n"
        "ds_read_b128 a[8:11], v254 offset:24576\n"
        "v_cvt_pk_bf16_f32 v83, 0, v83\n"
        "v_mfma_f32_16x16x32_bf16 v[24:27], a[152:155], a[220:223], v[24:27]\n"
        "ds_read_b128 a[12:15], v254 offset:28672\n"
        "v_cvt_pk_bf16_f32 v104, 0, v104\n"
        "v_mfma_f32_16x16x32_bf16 v[24:27], a[144:147], a[216:219], v[24:27]\n"
        "ds_read_b128 a[16:19], v254 offset:17408\n"
        "global_load_lds_dwordx4 v146, s[60:61] offset:2048\n"
        "v_mfma_f32_16x16x32_bf16 v[24:27], a[136:139], a[212:215], v[24:27]\n"
        "ds_read_b128 a[20:23], v254 offset:21504\n"
        "v_cvt_pk_bf16_f32 v105, 0, v105\n"
        "v_mfma_f32_16x16x32_bf16 v[24:27], a[128:131], a[208:211], v[24:27]\n"
        "ds_read_b128 a[24:27], v254 offset:25600\n"
        "ds_read_b128 a[28:31], v254 offset:29696\n"
        "global_load_lds_dwordx4 v147, s[60:61] offset:3072\n"
        "s_add_u32 m0, s73, 0x0\n"
        "s_waitcnt lgkmcnt(7)\n"
        "v_mfma_f32_16x16x32_bf16 v[0:3], v[160:163], a[0:3], v[0:3]\n"
        "ds_read_b128 a[192:195], v254 offset:18432\n"
        "s_waitcnt lgkmcnt(7)\n"
        "v_mfma_f32_16x16x32_bf16 v[0:3], v[168:171], a[4:7], v[0:3]\n"
        "ds_read_b128 a[196:199], v254 offset:22528\n"
        "s_waitcnt lgkmcnt(7)\n"
        "v_mfma_f32_16x16x32_bf16 v[0:3], v[176:179], a[8:11], v[0:3]\n"
        "ds_read_b128 a[200:203], v254 offset:26624\n"
        "s_waitcnt lgkmcnt(7)\n"
        "v_mfma_f32_16x16x32_bf16 v[0:3], v[184:187], a[12:15], v[0:3]\n"
        "ds_read_b128 a[204:207], v254 offset:30720\n"
        "s_add_u32 s60, s68, s66\n"
        "s_addc_u32 s61, s69, 0\n"
        "v_mfma_f32_16x16x32_bf16 v[4:7], v[188:191], a[12:15], v[4:7]\n"
        "ds_read_b128 a[208:211], v254 offset:19456\n"
        "global_load_lds_dwordx4 v254, s[60:61] offset:0 sc1\n"
        "s_xor_b32 s66, s29, 0x1e0000\n"
        "v_mfma_f32_16x16x32_bf16 v[4:7], v[180:183], a[8:11], v[4:7]\n"
        "ds_read_b128 a[212:215], v254 offset:23552\n"
        "s_add_u32 s28, s38, s16\n"
        "s_addc_u32 s29, s39, 0\n"
        "v_mfma_f32_16x16x32_bf16 v[4:7], v[172:175], a[4:7], v[4:7]\n"
        "ds_read_b128 a[216:219], v254 offset:27648\n"
        "v_cvt_pk_bf16_f32 v106, 0, v106\n"
        "v_mfma_f32_16x16x32_bf16 v[4:7], v[164:167], a[0:3], v[4:7]\n"
        "ds_read_b128 a[220:223], v254 offset:31744\n"
        "s_waitcnt lgkmcnt(8)\n"
        "v_mfma_f32_16x16x32_bf16 v[12:15], v[164:167], a[16:19], v[12:15]\n"
        "global_load_lds_dwordx4 v254, s[60:61] offset:1024 sc1\n"
        "v_cvt_pk_bf16_f32 v107, 0, v107\n"
        "v_mfma_f32_16x16x32_bf16 v[12:15], v[172:175], a[20:23], v[12:15]\n"
        "v_add_f32 v80, v80, v104\n"
        "v_add_f32 v81, v81, v105\n"
        "v_mfma_f32_16x16x32_bf16 v[12:15], v[180:183], a[24:27], v[12:15]\n"
        "v_add_f32 v82, v82, v106\n"
        "v_add_f32 v83, v83, v107\n"
        "v_mfma_f32_16x16x32_bf16 v[12:15], v[188:191], a[28:31], v[12:15]\n"
        "global_load_lds_dwordx4 v254, s[60:61] offset:2048 sc1\n"
        "v_cvt_pk_bf16_f32 v92, v80, v81\n"
        "v_mfma_f32_16x16x32_bf16 v[8:11], v[184:187], a[28:31], v[8:11]\n"
        "s_waitcnt lgkmcnt(3)\n"
        "v_cvt_pk_bf16_f32 v93, v82, v83\n"
        "v_and_b32 v84, s35, v71\n"
        "v_mfma_f32_16x16x32_bf16 v[8:11], v[176:179], a[24:27], v[8:11]\n"
        "v_lshlrev_b32 v85, 16, v71\n"
        "v_and_b32 v86, s35, v70\n"
        "v_mfma_f32_16x16x32_bf16 v[8:11], v[168:171], a[20:23], v[8:11]\n"
        "global_load_lds_dwordx4 v254, s[60:61] offset:3072 sc1\n"
        "s_add_u32 m0, s73, 0x14000\n"
        "v_lshlrev_b32 v87, 16, v70\n"
        "v_mfma_f32_16x16x32_bf16 v[8:11], v[160:163], a[16:19], v[8:11]\n"
        "s_waitcnt vmcnt(20)\n"
        "s_add_u32 s60, s22, s66\n"
        "s_addc_u32 s61, s23, 0\n"
        "v_mfma_f32_16x16x32_bf16 v[16:19], v[160:163], a[192:195], v[16:19]\n"
        "s_barrier\n"
        "v_mfma_f32_16x16x32_bf16 v[16:19], v[168:171], a[196:199], v[16:19]\n"
        "ds_read_b64_tr_b16 a[176:177], v155 offset:32768\n"
        "global_load_lds_dwordx4 v144, s[60:61] offset:0\n"
        "s_xor_b32 s66, s17, 7\n"
        "v_mfma_f32_16x16x32_bf16 v[16:19], v[176:179], a[200:203], v[16:19]\n"
        "ds_read_b64_tr_b16 a[178:179], v155 offset:36864\n"
        "s_lshl_b32 s66, s66, 15\n"
        "v_mul_f32 v84, s54, v84\n"
        "v_mfma_f32_16x16x32_bf16 v[16:19], v[184:187], a[204:207], v[16:19]\n"
        "ds_read_b64_tr_b16 a[180:181], v156 offset:32768\n"
        "v_mul_f32 v85, s54, v85\n"
        "v_mfma_f32_16x16x32_bf16 v[20:23], v[188:191], a[204:207], v[20:23]\n"
        "ds_read_b64_tr_b16 a[182:183], v156 offset:36864\n"
        "global_load_lds_dwordx4 v145, s[60:61] offset:1024\n"
        "v_mfma_f32_16x16x32_bf16 v[20:23], v[180:183], a[200:203], v[20:23]\n"
        "ds_read_b64_tr_b16 a[184:185], v155 offset:40960\n"
        "v_mul_f32 v86, s54, v86\n"
        "v_mfma_f32_16x16x32_bf16 v[20:23], v[172:175], a[196:199], v[20:23]\n"
        "ds_read_b64_tr_b16 a[186:187], v155 offset:45056\n"
        "v_mul_f32 v87, s54, v87\n"
        "v_mfma_f32_16x16x32_bf16 v[20:23], v[164:167], a[192:195], v[20:23]\n"
        "ds_read_b64_tr_b16 a[188:189], v156 offset:40960\n"
        "global_load_lds_dwordx4 v146, s[60:61] offset:2048\n"
        "v_mfma_f32_16x16x32_bf16 v[28:31], v[164:167], a[208:211], v[28:31]\n"
        "ds_read_b64_tr_b16 a[190:191], v156 offset:45056\n"
        "s_waitcnt lgkmcnt(8)\n"
        "v_mfma_f32_16x16x32_bf16 v[28:31], v[172:175], a[212:215], v[28:31]\n"
        "ds_read_b128 a[0:3], v254 offset:49152\n"
        "v_cvt_pk_bf16_f32 v84, 0, v84\n"
        "v_mfma_f32_16x16x32_bf16 v[28:31], v[180:183], a[216:219], v[28:31]\n"
        "ds_read_b128 a[4:7], v254 offset:53248\n"
        "global_load_lds_dwordx4 v147, s[60:61] offset:3072\n"
        "s_add_u32 m0, s73, 0x8000\n"
        "v_mfma_f32_16x16x32_bf16 v[28:31], v[188:191], a[220:223], v[28:31]\n"
        "ds_read_b128 a[8:11], v254 offset:57344\n"
        "s_add_u32 s60, s68, s66\n"
        "s_addc_u32 s61, s69, 0\n"
        "v_mfma_f32_16x16x32_bf16 v[24:27], v[184:187], a[220:223], v[24:27]\n"
        "ds_read_b128 a[12:15], v254 offset:61440\n"
        "s_sub_u32 s66, 15, s12\n"
        "v_cvt_pk_bf16_f32 v85, 0, v85\n"
        "v_mfma_f32_16x16x32_bf16 v[24:27], v[176:179], a[216:219], v[24:27]\n"
        "ds_read_b128 a[16:19], v254 offset:50176\n"
        "global_load_lds_dwordx4 v254, s[60:61] offset:0 sc1\n"
        "v_mfma_f32_16x16x32_bf16 v[24:27], v[168:171], a[212:215], v[24:27]\n"
        "ds_read_b128 a[20:23], v254 offset:54272\n"
        "s_cmp_lt_u32 s75, 2\n"
        "s_cselect_b32 s66, s12, s66\n"
        "v_mfma_f32_16x16x32_bf16 v[24:27], v[160:163], a[208:211], v[24:27]\n"
        "ds_read_b128 a[24:27], v254 offset:58368\n"
        "ds_read_b128 a[28:31], v254 offset:62464\n"
        "global_load_lds_dwordx4 v254, s[60:61] offset:1024 sc1\n"
        "s_mul_i32 s66, s66, 1052672\n"
        "s_waitcnt lgkmcnt(7)\n"
        "v_mfma_f32_16x16x32_bf16 v[0:3], v[192:195], a[0:3], v[0:3]\n"
        "ds_read_b128 a[192:195], v254 offset:51200\n"
        "s_add_u32 s66, s66, s67\n"
        "s_waitcnt lgkmcnt(7)\n"
        "v_mfma_f32_16x16x32_bf16 v[0:3], v[200:203], a[4:7], v[0:3]\n"
        "ds_read_b128 a[196:199], v254 offset:55296\n"
        "s_and_b32 s67, s76, 1\n"
        "s_waitcnt lgkmcnt(7)\n"
        "v_mfma_f32_16x16x32_bf16 v[0:3], v[208:211], a[8:11], v[0:3]\n"
        "ds_read_b128 a[200:203], v254 offset:59392\n"
        "s_lshl_b32 s67, s67, 12\n"
        "s_waitcnt lgkmcnt(7)\n"
        "v_mfma_f32_16x16x32_bf16 v[0:3], v[216:219], a[12:15], v[0:3]\n"
        "ds_read_b128 a[204:207], v254 offset:63488\n"
        "global_load_lds_dwordx4 v254, s[60:61] offset:2048 sc1\n"
        "v_mfma_f32_16x16x32_bf16 v[4:7], v[220:223], a[12:15], v[4:7]\n"
        "ds_read_b128 a[208:211], v254 offset:52224\n"
        "s_add_u32 s68, s20, s66\n"
        "s_addc_u32 s69, s21, 0\n"
        "v_mfma_f32_16x16x32_bf16 v[4:7], v[212:215], a[8:11], v[4:7]\n"
        "ds_read_b128 a[212:215], v254 offset:56320\n"
        "s_xor_b32 s66, s17, 0\n"
        "s_lshl_b32 s66, s66, 15\n"
        "v_mfma_f32_16x16x32_bf16 v[4:7], v[204:207], a[4:7], v[4:7]\n"
        "ds_read_b128 a[216:219], v254 offset:60416\n"
        "global_load_lds_dwordx4 v254, s[60:61] offset:3072 sc1\n"
        "s_add_u32 m0, s73, 0x4000\n"
        "v_mfma_f32_16x16x32_bf16 v[4:7], v[196:199], a[0:3], v[4:7]\n"
        "ds_read_b128 a[220:223], v254 offset:64512\n"
        "s_waitcnt lgkmcnt(8)\n"
        "v_mfma_f32_16x16x32_bf16 v[12:15], v[196:199], a[16:19], v[12:15]\n"
        "s_add_u32 s60, s68, s66\n"
        "s_addc_u32 s61, s69, 0\n"
        "s_xor_b32 s66, s17, 1\n"
        "v_mfma_f32_16x16x32_bf16 v[12:15], v[204:207], a[20:23], v[12:15]\n"
        "s_lshl_b32 s66, s66, 15\n"
        "v_cvt_pk_bf16_f32 v86, 0, v86\n"
        "v_cvt_pk_bf16_f32 v87, 0, v87\n"
        "v_mfma_f32_16x16x32_bf16 v[12:15], v[212:215], a[24:27], v[12:15]\n"
        "v_cvt_pk_bf16_f32 v108, 0, v108\n"
        "v_cvt_pk_bf16_f32 v109, 0, v109\n"
        "v_mfma_f32_16x16x32_bf16 v[12:15], v[220:223], a[28:31], v[12:15]\n"
        "v_cvt_pk_bf16_f32 v110, 0, v110\n"
        "v_cvt_pk_bf16_f32 v111, 0, v111\n"
        "v_mfma_f32_16x16x32_bf16 v[8:11], v[216:219], a[28:31], v[8:11]\n"
        "s_waitcnt lgkmcnt(3)\n"
        "v_add_f32 v84, v84, v108\n"
        "v_add_f32 v85, v85, v109\n"
        "v_mfma_f32_16x16x32_bf16 v[8:11], v[208:211], a[24:27], v[8:11]\n"
        "v_add_f32 v86, v86, v110\n"
        "v_add_f32 v87, v87, v111\n"
        "v_mfma_f32_16x16x32_bf16 v[8:11], v[200:203], a[20:23], v[8:11]\n"
        "v_cvt_pk_bf16_f32 v94, v84, v85\n"
        "v_cvt_pk_bf16_f32 v95, v86, v87\n"
        "v_mfma_f32_16x16x32_bf16 v[8:11], v[192:195], a[16:19], v[8:11]\n"
        "s_waitcnt vmcnt(20)\n"
        "v_permlane16_swap_b32 v92, v94\n"
        "v_mfma_f32_16x16x32_bf16 v[16:19], v[192:195], a[192:195], v[16:19]\n"
        "s_barrier\n"
        "v_mfma_f32_16x16x32_bf16 v[16:19], v[200:203], a[196:199], v[16:19]\n"
        "ds_read_b64_tr_b16 a[32:33], v157 offset:0\n"
        "global_load_lds_dwordx4 v254, s[60:61] offset:0 sc1\n"
        "v_mfma_f32_16x16x32_bf16 v[16:19], v[208:211], a[200:203], v[16:19]\n"
        "ds_read_b64_tr_b16 a[34:35], v157 offset:4096\n"
        "v_and_b32 v80, s35, v73\n"
        "v_mfma_f32_16x16x32_bf16 v[16:19], v[216:219], a[204:207], v[16:19]\n"
        "ds_read_b64_tr_b16 a[36:37], v158 offset:0\n"
        "v_lshlrev_b32 v81, 16, v73\n"
        "v_mfma_f32_16x16x32_bf16 v[20:23], v[220:223], a[204:207], v[20:23]\n"
        "ds_read_b64_tr_b16 a[38:39], v158 offset:4096\n"
        "global_load_lds_dwordx4 v254, s[60:61] offset:1024 sc1\n"
        "v_mfma_f32_16x16x32_bf16 v[20:23], v[212:215], a[200:203], v[20:23]\n"
        "s_waitcnt lgkmcnt(4)\n"
        "ds_read_b64_tr_b16 a[40:41], v157 offset:8192\n"
        "v_mfma_f32_16x16x32_bf16 v[20:23], v[204:207], a[196:199], v[20:23]\n"
        "ds_read_b64_tr_b16 a[42:43], v157 offset:12288\n"
        "v_and_b32 v82, s35, v72\n"
        "v_mfma_f32_16x16x32_bf16 v[20:23], v[196:199], a[192:195], v[20:23]\n"
        "ds_read_b64_tr_b16 a[44:45], v158 offset:8192\n"
        "global_load_lds_dwordx4 v254, s[60:61] offset:2048 sc1\n"
        "v_mfma_f32_16x16x32_bf16 v[28:31], v[196:199], a[208:211], v[28:31]\n"
        "ds_read_b64_tr_b16 a[46:47], v158 offset:12288\n"
        "v_lshlrev_b32 v83, 16, v72\n"
        "v_mfma_f32_16x16x32_bf16 v[28:31], v[204:207], a[212:215], v[28:31]\n"
        "ds_read_b128 a[0:3], v244 offset:49152\n"
        "v_mul_f32 v80, s54, v80\n"
        "v_mfma_f32_16x16x32_bf16 v[28:31], v[212:215], a[216:219], v[28:31]\n"
        "ds_read_b128 a[4:7], v244 offset:53248\n"
        "global_load_lds_dwordx4 v254, s[60:61] offset:3072 sc1\n"
        "s_add_u32 m0, s73, 0x18000\n"
        "v_mfma_f32_16x16x32_bf16 v[28:31], v[220:223], a[220:223], v[28:31]\n"
        "ds_read_b128 a[8:11], v244 offset:57344\n"
        "s_add_u32 s60, s68, s66\n"
        "s_addc_u32 s61, s69, 0\n"
        "v_mfma_f32_16x16x32_bf16 v[24:27], v[216:219], a[220:223], v[24:27]\n"
        "ds_read_b128 a[12:15], v244 offset:61440\n"
        "s_xor_b32 s66, s17, 2\n"
        "s_lshl_b32 s66, s66, 15\n"
        "v_mfma_f32_16x16x32_bf16 v[24:27], v[208:211], a[216:219], v[24:27]\n"
        "ds_read_b128 a[16:19], v244 offset:50176\n"
        "global_load_lds_dwordx4 v254, s[60:61] offset:0 sc1\n"
        "v_mfma_f32_16x16x32_bf16 v[24:27], v[200:203], a[212:215], v[24:27]\n"
        "ds_read_b128 a[20:23], v244 offset:54272\n"
        "v_mul_f32 v81, s54, v81\n"
        "v_mfma_f32_16x16x32_bf16 v[24:27], v[192:195], a[208:211], v[24:27]\n"
        "ds_read_b128 a[24:27], v244 offset:58368\n"
        "ds_read_b128 a[28:31], v244 offset:62464\n"
        "global_load_lds_dwordx4 v254, s[60:61] offset:1024 sc1\n"
        "s_waitcnt lgkmcnt(7)\n"
        "v_mfma_f32_16x16x32_bf16 v[0:3], v[112:115], a[0:3], v[0:3]\n"
        "ds_read_b128 a[192:195], v244 offset:51200\n"
        "s_waitcnt lgkmcnt(7)\n"
        "v_mfma_f32_16x16x32_bf16 v[0:3], v[120:123], a[4:7], v[0:3]\n"
        "ds_read_b128 a[196:199], v244 offset:55296\n"
        "s_waitcnt lgkmcnt(7)\n"
        "v_mfma_f32_16x16x32_bf16 v[0:3], a[64:67], a[8:11], v[0:3]\n"
        "ds_read_b128 a[200:203], v244 offset:59392\n"
        "s_waitcnt lgkmcnt(7)\n"
        "v_mfma_f32_16x16x32_bf16 v[0:3], a[72:75], a[12:15], v[0:3]\n"
        "ds_read_b128 a[204:207], v244 offset:63488\n"
        "global_load_lds_dwordx4 v254, s[60:61] offset:2048 sc1\n"
        "v_mfma_f32_16x16x32_bf16 v[4:7], a[76:79], a[12:15], v[4:7]\n"
        "s_waitcnt lgkmcnt(7)\n"
        "ds_read_b128 a[208:211], v244 offset:52224\n"
        "v_mfma_f32_16x16x32_bf16 v[4:7], a[68:71], a[8:11], v[4:7]\n"
        "s_waitcnt lgkmcnt(7)\n"
        "ds_read_b128 a[212:215], v244 offset:56320\n"
        "v_mfma_f32_16x16x32_bf16 v[4:7], v[124:127], a[4:7], v[4:7]\n"
        "s_waitcnt lgkmcnt(7)\n"
        "ds_read_b128 a[216:219], v244 offset:60416\n"
        "v_mfma_f32_16x16x32_bf16 v[4:7], v[116:119], a[0:3], v[4:7]\n"
        "s_waitcnt lgkmcnt(7)\n"
        "ds_read_b128 a[220:223], v244 offset:64512\n"
        "v_mfma_f32_16x16x32_bf16 v[12:15], v[116:119], a[16:19], v[12:15]\n"
        "v_permlane16_swap_b32 v93, v95\n"
        "v_mfma_f32_16x16x32_bf16 v[12:15], v[124:127], a[20:23], v[12:15]\n"
        "global_load_lds_dwordx4 v254, s[60:61] offset:3072 sc1\n"
        "s_add_u32 m0, s73, 0xc000\n"
        "v_mul_f32 v82, s54, v82\n"
        "v_mfma_f32_16x16x32_bf16 v[12:15], a[68:71], a[24:27], v[12:15]\n"
        "s_add_u32 s60, s68, s66\n"
        "s_addc_u32 s61, s69, 0\n"
        "s_xor_b32 s66, s17, 3\n"
        "v_mfma_f32_16x16x32_bf16 v[12:15], a[76:79], a[28:31], v[12:15]\n"
        "s_lshl_b32 s66, s66, 15\n"
        "v_mul_f32 v83, s54, v83\n"
        "v_cvt_pk_bf16_f32 v80, 0, v80\n"
        "v_mfma_f32_16x16x32_bf16 v[8:11], a[72:75], a[28:31], v[8:11]\n"
        "s_waitcnt lgkmcnt(4)\n"
        "v_cvt_pk_bf16_f32 v81, 0, v81\n"
        "v_cvt_pk_bf16_f32 v82, 0, v82\n"
        "v_mfma_f32_16x16x32_bf16 v[8:11], a[64:67], a[24:27], v[8:11]\n"
        "v_cvt_pk_bf16_f32 v83, 0, v83\n"
        "v_cvt_pk_bf16_f32 v128, 0, v128\n"
        "v_mfma_f32_16x16x32_bf16 v[8:11], v[120:123], a[20:23], v[8:11]\n"
        "v_cvt_pk_bf16_f32 v129, 0, v129\n"
        "v_cvt_pk_bf16_f32 v130, 0, v130\n"
        "v_mfma_f32_16x16x32_bf16 v[8:11], v[112:115], a[16:19], v[8:11]\n"
        "s_waitcnt vmcnt(20)\n"
        "v_cvt_pk_bf16_f32 v131, 0, v131\n"
        "v_add_f32 v80, v80, v128\n"
        "v_mfma_f32_16x16x32_bf16 v[16:19], v[112:115], a[192:195], v[16:19]\n"
        "s_barrier\n"
        "v_mfma_f32_16x16x32_bf16 v[16:19], v[120:123], a[196:199], v[16:19]\n"
        "ds_read_b64_tr_b16 a[48:49], v155 offset:0\n"
        "global_load_lds_dwordx4 v254, s[60:61] offset:0 sc1\n"
        "v_mfma_f32_16x16x32_bf16 v[16:19], a[64:67], a[200:203], v[16:19]\n"
        "ds_read_b64_tr_b16 a[50:51], v155 offset:4096\n"
        "v_add_f32 v81, v81, v129\n"
        "v_mfma_f32_16x16x32_bf16 v[16:19], a[72:75], a[204:207], v[16:19]\n"
        "ds_read_b64_tr_b16 a[52:53], v156 offset:0\n"
        "v_add_f32 v82, v82, v130\n"
        "v_mfma_f32_16x16x32_bf16 v[20:23], a[76:79], a[204:207], v[20:23]\n"
        "ds_read_b64_tr_b16 a[54:55], v156 offset:4096\n"
        "global_load_lds_dwordx4 v254, s[60:61] offset:1024 sc1\n"
        "v_mfma_f32_16x16x32_bf16 v[20:23], a[68:71], a[200:203], v[20:23]\n"
        "ds_read_b64_tr_b16 a[56:57], v155 offset:8192\n"
        "v_add_f32 v83, v83, v131\n"
        "v_mfma_f32_16x16x32_bf16 v[20:23], v[124:127], a[196:199], v[20:23]\n"
        "ds_read_b64_tr_b16 a[58:59], v155 offset:12288\n"
        "v_and_b32 v84, s35, v75\n"
        "v_mfma_f32_16x16x32_bf16 v[20:23], v[116:119], a[192:195], v[20:23]\n"
        "ds_read_b64_tr_b16 a[60:61], v156 offset:8192\n"
        "s_waitcnt lgkmcnt(7)\n"
        "v_mfma_f32_16x16x32_bf16 v[28:31], v[116:119], a[208:211], v[28:31]\n"
        "ds_read_b64_tr_b16 a[62:63], v156 offset:12288\n"
        "global_load_lds_dwordx4 v254, s[60:61] offset:2048 sc1\n"
        "v_mfma_f32_16x16x32_bf16 v[28:31], v[124:127], a[212:215], v[28:31]\n"
        "ds_read_b128 a[0:3], v245 offset:16384\n"
        "v_lshlrev_b32 v85, 16, v75\n"
        "v_mfma_f32_16x16x32_bf16 v[28:31], a[68:71], a[216:219], v[28:31]\n"
        "ds_read_b128 a[4:7], v245 offset:20480\n"
        "v_and_b32 v86, s35, v74\n"
        "v_mfma_f32_16x16x32_bf16 v[28:31], a[76:79], a[220:223], v[28:31]\n"
        "ds_read_b128 a[8:11], v245 offset:24576\n"
        "global_load_lds_dwordx4 v254, s[60:61] offset:3072 sc1\n"
        "s_add_u32 m0, s73, 0x20000\n"
        "v_mfma_f32_16x16x32_bf16 v[24:27], a[72:75], a[220:223], v[24:27]\n"
        "ds_read_b128 a[12:15], v245 offset:28672\n"
        "s_add_u32 s60, s68, s66\n"
        "s_addc_u32 s61, s69, 0\n"
        "v_mfma_f32_16x16x32_bf16 v[24:27], a[64:67], a[216:219], v[24:27]\n"
        "ds_read_b128 a[16:19], v245 offset:17408\n"
        "s_xor_b32 s66, s17, 4\n"
        "s_lshl_b32 s66, s66, 15\n"
        "v_mfma_f32_16x16x32_bf16 v[24:27], v[120:123], a[212:215], v[24:27]\n"
        "ds_read_b128 a[20:23], v245 offset:21504\n"
        "global_load_lds_dwordx4 v254, s[60:61] offset:0 sc1\n"
        "v_mfma_f32_16x16x32_bf16 v[24:27], v[112:115], a[208:211], v[24:27]\n"
        "ds_read_b128 a[24:27], v245 offset:25600\n"
        "ds_read_b128 a[28:31], v245 offset:29696\n"
        "ds_read_b128 a[192:195], v245 offset:18432\n"
        "v_lshlrev_b32 v87, 16, v74\n"
        "s_waitcnt lgkmcnt(8)\n"
        "v_mfma_f32_16x16x32_bf16 v[0:3], a[80:83], a[0:3], v[0:3]\n"
        "ds_read_b128 a[196:199], v245 offset:22528\n"
        "s_waitcnt lgkmcnt(8)\n"
        "v_mfma_f32_16x16x32_bf16 v[0:3], a[88:91], a[4:7], v[0:3]\n"
        "ds_read_b128 a[200:203], v245 offset:26624\n"
        "s_waitcnt lgkmcnt(8)\n"
        "v_mfma_f32_16x16x32_bf16 v[0:3], a[160:163], a[8:11], v[0:3]\n"
        "ds_read_b128 a[204:207], v245 offset:30720\n"
        "s_waitcnt lgkmcnt(8)\n"
        "v_mfma_f32_16x16x32_bf16 v[0:3], a[168:171], a[12:15], v[0:3]\n"
        "ds_read_b128 a[208:211], v245 offset:19456\n"
        "global_load_lds_dwordx4 v254, s[60:61] offset:1024 sc1\n"
        "v_mfma_f32_16x16x32_bf16 v[4:7], a[172:175], a[12:15], v[4:7]\n"
        "ds_read_b128 a[212:215], v245 offset:23552\n"
        "v_mul_f32 v84, s54, v84\n"
        "v_mfma_f32_16x16x32_bf16 v[4:7], a[164:167], a[8:11], v[4:7]\n"
        "ds_read_b128 a[216:219], v245 offset:27648\n"
        "v_mul_f32 v85, s54, v85\n"
        "v_mfma_f32_16x16x32_bf16 v[4:7], a[92:95], a[4:7], v[4:7]\n"
        "ds_read_b128 a[220:223], v245 offset:31744\n"
        "global_load_lds_dwordx4 v254, s[60:61] offset:2048 sc1\n"
        "v_mfma_f32_16x16x32_bf16 v[4:7], a[84:87], a[0:3], v[4:7]\n"
        "s_waitcnt lgkmcnt(7)\n"
        "v_mul_f32 v86, s54, v86\n"
        "v_mul_f32 v87, s54, v87\n"
        "v_mfma_f32_16x16x32_bf16 v[12:15], a[84:87], a[16:19], v[12:15]\n"
        "v_cvt_pk_bf16_f32 v84, 0, v84\n"
        "v_cvt_pk_bf16_f32 v85, 0, v85\n"
        "v_mfma_f32_16x16x32_bf16 v[12:15], a[92:95], a[20:23], v[12:15]\n"
        "global_load_lds_dwordx4 v254, s[60:61] offset:3072 sc1\n"
        "s_add_u32 m0, s73, 0x1c000\n"
        "v_mfma_f32_16x16x32_bf16 v[12:15], a[164:167], a[24:27], v[12:15]\n"
        "s_add_u32 s60, s68, s66\n"
        "s_addc_u32 s61, s69, 0\n"
        "s_xor_b32 s66, s17, 5\n"
        "v_mfma_f32_16x16x32_bf16 v[12:15], a[172:175], a[28:31], v[12:15]\n"
        "s_lshl_b32 s66, s66, 15\n"
        "v_cvt_pk_bf16_f32 v86, 0, v86\n"
        "v_cvt_pk_bf16_f32 v87, 0, v87\n"
        "v_mfma_f32_16x16x32_bf16 v[8:11], a[168:171], a[28:31], v[8:11]\n"
        "v_cvt_pk_bf16_f32 v132, 0, v132\n"
        "v_cvt_pk_bf16_f32 v133, 0, v133\n"
        "v_mfma_f32_16x16x32_bf16 v[8:11], a[160:163], a[24:27], v[8:11]\n"
        "s_waitcnt lgkmcnt(1)\n"
        "v_cvt_pk_bf16_f32 v134, 0, v134\n"
        "v_cvt_pk_bf16_f32 v135, 0, v135\n"
        "v_mfma_f32_16x16x32_bf16 v[8:11], a[88:91], a[20:23], v[8:11]\n"
        "v_add_f32 v84, v84, v132\n"
        "v_add_f32 v85, v85, v133\n"
        "v_mfma_f32_16x16x32_bf16 v[8:11], a[80:83], a[16:19], v[8:11]\n"
        "s_waitcnt vmcnt(20)\n"
        "v_add_f32 v86, v86, v134\n"
        "v_add_f32 v87, v87, v135\n"
        "v_mfma_f32_16x16x32_bf16 v[16:19], a[80:83], a[192:195], v[16:19]\n"
        "s_barrier\n"
        "v_mfma_f32_16x16x32_bf16 v[16:19], a[88:91], a[196:199], v[16:19]\n"
        "ds_read_b64_tr_b16 a[224:225], v155 offset:16384\n"
        "global_load_lds_dwordx4 v254, s[60:61] offset:0 sc1\n"
        "v_mfma_f32_16x16x32_bf16 v[16:19], a[160:163], a[200:203], v[16:19]\n"
        "ds_read_b64_tr_b16 a[226:227], v155 offset:20480\n"
        "v_cvt_pk_bf16_f32 v136, 0, v136\n"
        "v_mfma_f32_16x16x32_bf16 v[16:19], a[168:171], a[204:207], v[16:19]\n"
        "ds_read_b64_tr_b16 a[228:229], v156 offset:16384\n"
        "v_cvt_pk_bf16_f32 v137, 0, v137\n"
        "v_mfma_f32_16x16x32_bf16 v[20:23], a[172:175], a[204:207], v[20:23]\n"
        "ds_read_b64_tr_b16 a[230:231], v156 offset:20480\n"
        "global_load_lds_dwordx4 v254, s[60:61] offset:1024 sc1\n"
        "v_mfma_f32_16x16x32_bf16 v[20:23], a[164:167], a[200:203], v[20:23]\n"
        "ds_read_b64_tr_b16 a[232:233], v155 offset:24576\n"
        "v_cvt_pk_bf16_f32 v138, 0, v138\n"
        "v_mfma_f32_16x16x32_bf16 v[20:23], a[92:95], a[196:199], v[20:23]\n"
        "ds_read_b64_tr_b16 a[234:235], v155 offset:28672\n"
        "v_cvt_pk_bf16_f32 v139, 0, v139\n"
        "v_mfma_f32_16x16x32_bf16 v[20:23], a[84:87], a[192:195], v[20:23]\n"
        "ds_read_b64_tr_b16 a[236:237], v156 offset:24576\n"
        "global_load_lds_dwordx4 v254, s[60:61] offset:2048 sc1\n"
        "v_mfma_f32_16x16x32_bf16 v[28:31], a[84:87], a[208:211], v[28:31]\n"
        "ds_read_b64_tr_b16 a[238:239], v156 offset:28672\n"
        "v_cvt_pk_bf16_f32 v140, 0, v140\n"
        "v_mfma_f32_16x16x32_bf16 v[28:31], a[92:95], a[212:215], v[28:31]\n"
        "ds_read_b128 a[0:3], v254 offset:0\n"
        "v_cvt_pk_bf16_f32 v141, 0, v141\n"
        "v_mfma_f32_16x16x32_bf16 v[28:31], a[164:167], a[216:219], v[28:31]\n"
        "ds_read_b128 a[4:7], v254 offset:4096\n"
        "s_waitcnt lgkmcnt(8)\n"
        "v_mfma_f32_16x16x32_bf16 v[28:31], a[172:175], a[220:223], v[28:31]\n"
        "ds_read_b128 a[8:11], v254 offset:8192\n"
        "global_load_lds_dwordx4 v254, s[60:61] offset:3072 sc1\n"
        "s_add_u32 m0, s73, 0x10000\n"
        "v_mfma_f32_16x16x32_bf16 v[24:27], a[168:171], a[220:223], v[24:27]\n"
        "ds_read_b128 a[12:15], v254 offset:12288\n"
        "s_add_u32 s60, s68, s66\n"
        "s_addc_u32 s61, s69, 0\n"
        "v_mfma_f32_16x16x32_bf16 v[24:27], a[160:163], a[216:219], v[24:27]\n"
        "ds_read_b128 a[16:19], v254 offset:1024\n"
        "s_xor_b32 s66, s17, 6\n"
        "s_lshl_b32 s66, s66, 15\n"
        "v_mfma_f32_16x16x32_bf16 v[24:27], a[88:91], a[212:215], v[24:27]\n"
        "ds_read_b128 a[20:23], v254 offset:5120\n"
        "global_load_lds_dwordx4 v254, s[60:61] offset:0 sc1\n"
        "v_mfma_f32_16x16x32_bf16 v[24:27], a[80:83], a[208:211], v[24:27]\n"
        "ds_read_b128 a[24:27], v254 offset:9216\n"
        "ds_read_b128 a[28:31], v254 offset:13312\n"
        "ds_read_b128 a[192:195], v254 offset:2048\n"
        "v_cvt_pk_bf16_f32 v142, 0, v142\n"
        "s_waitcnt lgkmcnt(8)\n"
        "v_mfma_f32_16x16x32_bf16 v[0:3], a[176:179], a[0:3], v[0:3]\n"
        "ds_read_b128 a[196:199], v254 offset:6144\n"
        "s_waitcnt lgkmcnt(8)\n"
        "v_mfma_f32_16x16x32_bf16 v[0:3], a[184:187], a[4:7], v[0:3]\n"
        "ds_read_b128 a[200:203], v254 offset:10240\n"
        "s_waitcnt lgkmcnt(8)\n"
        "v_mfma_f32_16x16x32_bf16 v[0:3], a[32:35], a[8:11], v[0:3]\n"
        "ds_read_b128 a[204:207], v254 offset:14336\n"
        "s_waitcnt lgkmcnt(8)\n"
        "v_mfma_f32_16x16x32_bf16 v[0:3], a[40:43], a[12:15], v[0:3]\n"
        "ds_read_b128 a[208:211], v254 offset:3072\n"
        "global_load_lds_dwordx4 v254, s[60:61] offset:1024 sc1\n"
        "v_mfma_f32_16x16x32_bf16 v[4:7], a[44:47], a[12:15], v[4:7]\n"
        "ds_read_b128 a[212:215], v254 offset:7168\n"
        "v_cvt_pk_bf16_f32 v143, 0, v143\n"
        "v_mfma_f32_16x16x32_bf16 v[4:7], a[36:39], a[8:11], v[4:7]\n"
        "ds_read_b128 a[216:219], v254 offset:11264\n"
        "s_waitcnt lgkmcnt(9)\n"
        "v_mfma_f32_16x16x32_bf16 v[4:7], a[188:191], a[4:7], v[4:7]\n"
        "ds_read_b128 a[220:223], v254 offset:15360\n"
        "global_load_lds_dwordx4 v254, s[60:61] offset:2048 sc1\n"
        "v_mfma_f32_16x16x32_bf16 v[4:7], a[180:183], a[0:3], v[4:7]\n"
        "s_waitcnt lgkmcnt(7)\n"
        "v_mfma_f32_16x16x32_bf16 v[12:15], a[180:183], a[16:19], v[12:15]\n"
        "v_mfma_f32_16x16x32_bf16 v[12:15], a[188:191], a[20:23], v[12:15]\n"
        "global_load_lds_dwordx4 v254, s[60:61] offset:3072 sc1\n"
        "s_add_u32 m0, s73, 0x24000\n"
        "v_mfma_f32_16x16x32_bf16 v[12:15], a[36:39], a[24:27], v[12:15]\n"
        "s_add_u32 s60, s68, s66\n"
        "s_addc_u32 s61, s69, 0\n"
        "s_xor_b32 s66, s17, 7\n"
        "v_mfma_f32_16x16x32_bf16 v[12:15], a[44:47], a[28:31], v[12:15]\n"
        "s_lshl_b32 s66, s66, 15\n"
        "v_mfma_f32_16x16x32_bf16 v[8:11], a[40:43], a[28:31], v[8:11]\n"
        "v_mfma_f32_16x16x32_bf16 v[8:11], a[32:35], a[24:27], v[8:11]\n"
        "s_waitcnt lgkmcnt(1)\n"
        "v_mfma_f32_16x16x32_bf16 v[8:11], a[184:187], a[20:23], v[8:11]\n"
        "v_mfma_f32_16x16x32_bf16 v[8:11], a[176:179], a[16:19], v[8:11]\n"
        "s_waitcnt vmcnt(24)\n"
        "v_mfma_f32_16x16x32_bf16 v[16:19], a[176:179], a[192:195], v[16:19]\n"
        "s_barrier\n"
        "v_mfma_f32_16x16x32_bf16 v[16:19], a[184:187], a[196:199], v[16:19]\n"
        "ds_read_b128 a[0:3], v254 offset:32768\n"
        "global_load_lds_dwordx4 v254, s[60:61] offset:0 sc1\n"
        "v_mfma_f32_16x16x32_bf16 v[16:19], a[32:35], a[200:203], v[16:19]\n"
        "ds_read_b128 a[4:7], v254 offset:36864\n"
        "v_mfma_f32_16x16x32_bf16 v[16:19], a[40:43], a[204:207], v[16:19]\n"
        "ds_read_b128 a[8:11], v254 offset:40960\n"
        "v_mfma_f32_16x16x32_bf16 v[20:23], a[44:47], a[204:207], v[20:23]\n"
        "ds_read_b128 a[12:15], v254 offset:45056\n"
        "global_load_lds_dwordx4 v254, s[60:61] offset:1024 sc1\n"
        "v_mfma_f32_16x16x32_bf16 v[20:23], a[36:39], a[200:203], v[20:23]\n"
        "ds_read_b128 a[28:31], v254 offset:46080\n"
        "v_mfma_f32_16x16x32_bf16 v[20:23], a[188:191], a[196:199], v[20:23]\n"
        "ds_read_b128 a[24:27], v254 offset:41984\n"
        "v_mfma_f32_16x16x32_bf16 v[20:23], a[180:183], a[192:195], v[20:23]\n"
        "ds_read_b128 a[20:23], v254 offset:37888\n"
        "global_load_lds_dwordx4 v254, s[60:61] offset:2048 sc1\n"
        "v_mfma_f32_16x16x32_bf16 v[28:31], a[180:183], a[208:211], v[28:31]\n"
        "LHB0_%=:\n"
        "s_cbranch_execz LHRB_%=\n"
        "ds_read_b128 a[16:19], v254 offset:33792\n"
        "v_mfma_f32_16x16x32_bf16 v[28:31], a[188:191], a[212:215], v[28:31]\n"
        "ds_read_b128 a[192:195], v254 offset:34816\n"
        "s_waitcnt lgkmcnt(8)\n"
        "v_mfma_f32_16x16x32_bf16 v[0:3], a[48:51], a[0:3], v[0:3]\n"
        "ds_read_b128 a[196:199], v254 offset:38912\n"
        "s_waitcnt lgkmcnt(8)\n"
        "v_mfma_f32_16x16x32_bf16 v[0:3], a[56:59], a[4:7], v[0:3]\n"
        "ds_read_b128 a[200:203], v254 offset:43008\n"
        "s_waitcnt lgkmcnt(8)\n"
        "v_mfma_f32_16x16x32_bf16 v[0:3], a[224:227], a[8:11], v[0:3]\n"
        "ds_read_b128 a[204:207], v254 offset:47104\n"
        "s_waitcnt lgkmcnt(8)\n"
        "v_mfma_f32_16x16x32_bf16 v[0:3], a[232:235], a[12:15], v[0:3]\n"
        "global_load_lds_dwordx4 v254, s[60:61] offset:3072 sc1\n"
        "s_add_u32 m0, s73, 0x14000\n"
        "v_mfma_f32_16x16x32_bf16 v[4:7], a[236:239], a[12:15], v[4:7]\n"
        "s_add_u32 s60, s68, s66\n"
        "s_addc_u32 s61, s69, 0\n"
        "s_sub_u32 s66, 15, s12\n"
        "v_mfma_f32_16x16x32_bf16 v[4:7], a[228:231], a[8:11], v[4:7]\n"
        "s_cmp_lt_u32 s76, 2\n"
        "s_cselect_b32 s66, s12, s66\n"
        "s_mul_i32 s66, s66, 1052672\n"
        "v_mfma_f32_16x16x32_bf16 v[4:7], a[60:63], a[4:7], v[4:7]\n"
        "global_load_lds_dwordx4 v254, s[60:61] offset:0 sc1\n"
        "s_add_u32 s66, s66, s67\n"
        "v_cvt_pk_bf16_f32 v0, v0, v1\n"
        "v_mfma_f32_16x16x32_bf16 v[4:7], a[52:55], a[0:3], v[4:7]\n"
        "s_waitcnt lgkmcnt(3)\n"
        "v_cvt_pk_bf16_f32 v1, v2, v3\n"
        "v_mfma_f32_16x16x32_bf16 v[28:31], a[36:39], a[216:219], v[28:31]\n"
        "s_add_u32 s68, s20, s66\n"
        "s_addc_u32 s69, s21, 0\n"
        "s_xor_b32 s66, s17, 0\n"
        "v_mfma_f32_16x16x32_bf16 v[28:31], a[44:47], a[220:223], v[28:31]\n"
        "global_load_lds_dwordx4 v254, s[60:61] offset:1024 sc1\n"
        "s_lshl_b32 s66, s66, 15\n"
        "v_cvt_pk_bf16_f32 v2, v4, v5\n"
        "v_mfma_f32_16x16x32_bf16 v[8:11], a[232:235], a[28:31], v[8:11]\n"
        "v_cvt_pk_bf16_f32 v3, v6, v7\n"
        "v_mfma_f32_16x16x32_bf16 v[8:11], a[224:227], a[24:27], v[8:11]\n"
        "v_mfma_f32_16x16x32_bf16 v[8:11], a[56:59], a[20:23], v[8:11]\n"
        "global_store_dwordx4 v254, v[0:3], s[28:29] offset:0 sc1\n"
        "v_mfma_f32_16x16x32_bf16 v[8:11], a[48:51], a[16:19], v[8:11]\n"
        "s_waitcnt vmcnt(27)\n"
        "v_mfma_f32_16x16x32_bf16 v[12:15], a[52:55], a[16:19], v[12:15]\n"
        "s_barrier\n"
        "v_mfma_f32_16x16x32_bf16 v[12:15], a[60:63], a[20:23], v[12:15]\n"
        "ds_read_b128 a[0:3], v254 offset:16384\n"
        "global_load_lds_dwordx4 v254, s[60:61] offset:2048 sc1\n"
        "v_mfma_f32_16x16x32_bf16 v[12:15], a[228:231], a[24:27], v[12:15]\n"
        "ds_read_b128 a[4:7], v254 offset:20480\n"
        "v_cvt_pk_bf16_f32 v8, v8, v9\n"
        "v_mfma_f32_16x16x32_bf16 v[12:15], a[236:239], a[28:31], v[12:15]\n"
        "ds_read_b128 a[8:11], v254 offset:24576\n"
        "v_cvt_pk_bf16_f32 v9, v10, v11\n"
        "v_mfma_f32_16x16x32_bf16 v[24:27], a[40:43], a[220:223], v[24:27]\n"
        "ds_read_b128 a[220:223], v254 offset:48128\n"
        "global_load_lds_dwordx4 v254, s[60:61] offset:3072 sc1\n"
        "s_add_u32 m0, s73, 0x0\n"
        "v_mfma_f32_16x16x32_bf16 v[24:27], a[32:35], a[216:219], v[24:27]\n"
        "ds_read_b128 a[216:219], v254 offset:44032\n"
        "v_cvt_pk_bf16_f32 v10, v12, v13\n"
        "v_mfma_f32_16x16x32_bf16 v[24:27], a[184:187], a[212:215], v[24:27]\n"
        "ds_read_b128 a[212:215], v254 offset:39936\n"
        "v_cvt_pk_bf16_f32 v11, v14, v15\n"
        "v_mfma_f32_16x16x32_bf16 v[24:27], a[176:179], a[208:211], v[24:27]\n"
        "ds_read_b128 a[208:211], v254 offset:35840\n"
        "s_add_u32 s60, s68, s66\n"
        "s_addc_u32 s61, s69, 0\n"
        "v_mfma_f32_16x16x32_bf16 v[16:19], a[48:51], a[192:195], v[16:19]\n"
        "ds_read_b128 a[12:15], v254 offset:28672\n"
        "s_xor_b32 s66, s17, 1\n"
        "s_waitcnt lgkmcnt(8)\n"
        "v_mfma_f32_16x16x32_bf16 v[16:19], a[56:59], a[196:199], v[16:19]\n"
        "global_store_dwordx4 v254, v[8:11], s[28:29] offset:1024 sc1\n"
        "ds_read_b128 a[16:19], v254 offset:17408\n"
        "ds_read_b128 a[20:23], v254 offset:21504\n"
        "s_lshl_b32 s66, s66, 15\n"
        "v_mfma_f32_16x16x32_bf16 v[16:19], a[224:227], a[200:203], v[16:19]\n"
        "ds_read_b128 a[24:27], v254 offset:25600\n"
        "v_mfma_f32_16x16x32_bf16 v[16:19], a[232:235], a[204:207], v[16:19]\n"
        "ds_read_b128 a[28:31], v254 offset:29696\n"
        "global_load_lds_dwordx4 v254, s[60:61] offset:0 sc1\n"
        "v_mfma_f32_16x16x32_bf16 v[20:23], a[236:239], a[204:207], v[20:23]\n"
        "ds_read_b128 a[204:207], v254 offset:30720\n"
        "s_waitcnt lgkmcnt(8)\n"
        "v_mfma_f32_16x16x32_bf16 v[20:23], a[228:231], a[200:203], v[20:23]\n"
        "ds_read_b128 a[200:203], v254 offset:26624\n"
        "v_mfma_f32_16x16x32_bf16 v[20:23], a[60:63], a[196:199], v[20:23]\n"
        "ds_read_b128 a[196:199], v254 offset:22528\n"
        "v_cvt_pk_bf16_f32 v16, v16, v17\n"
        "v_mfma_f32_16x16x32_bf16 v[20:23], a[52:55], a[192:195], v[20:23]\n"
        "ds_read_b128 a[192:195], v254 offset:18432\n"
        "v_cvt_pk_bf16_f32 v17, v18, v19\n"
        "v_mfma_f32_16x16x32_bf16 v[24:27], a[232:235], a[220:223], v[24:27]\n"
        "global_load_lds_dwordx4 v254, s[60:61] offset:1024 sc1\n"
        "s_waitcnt lgkmcnt(8)\n"
        "v_mfma_f32_16x16x32_bf16 v[24:27], a[224:227], a[216:219], v[24:27]\n"
        "v_mfma_f32_16x16x32_bf16 v[24:27], a[56:59], a[212:215], v[24:27]\n"
        "v_mfma_f32_16x16x32_bf16 v[24:27], a[48:51], a[208:211], v[24:27]\n"
        "global_load_lds_dwordx4 v254, s[60:61] offset:2048 sc1\n"
        "v_cvt_pk_bf16_f32 v18, v20, v21\n"
        "v_mfma_f32_16x16x32_bf16 v[28:31], a[52:55], a[208:211], v[28:31]\n"
        "ds_read_b128 a[208:211], v254 offset:19456\n"
        "v_cvt_pk_bf16_f32 v19, v22, v23\n"
        "v_mfma_f32_16x16x32_bf16 v[28:31], a[60:63], a[212:215], v[28:31]\n"
        "ds_read_b128 a[212:215], v254 offset:23552\n"
        "v_mfma_f32_16x16x32_bf16 v[28:31], a[228:231], a[216:219], v[28:31]\n"
        "global_store_dwordx4 v254, v[16:19], s[28:29] offset:2048 sc1\n"
        "ds_read_b128 a[216:219], v254 offset:27648\n"
        "v_cvt_pk_bf16_f32 v24, v24, v25\n"
        "v_cvt_pk_bf16_f32 v25, v26, v27\n"
        "v_mfma_f32_16x16x32_bf16 v[28:31], a[236:239], a[220:223], v[28:31]\n"
        "ds_read_b128 a[220:223], v254 offset:31744\n"
        "v_mfma_f32_16x16x32_bf16 v[32:35], a[96:99], a[0:3], 0\n"
        "global_load_lds_dwordx4 v254, s[60:61] offset:3072 sc1\n"
        "s_add_u32 m0, s73, 0x8000\n"
        "v_mfma_f32_16x16x32_bf16 v[32:35], a[104:107], a[4:7], v[32:35]\n"
        "s_add_u32 s60, s68, s66\n"
        "s_addc_u32 s61, s69, 0\n"
        "s_xor_b32 s66, s17, 2\n"
        "v_mfma_f32_16x16x32_bf16 v[32:35], a[112:115], a[8:11], v[32:35]\n"
        "s_lshl_b32 s66, s66, 15\n"
        "v_cvt_pk_bf16_f32 v26, v28, v29\n"
        "v_cvt_pk_bf16_f32 v27, v30, v31\n"
        "v_mfma_f32_16x16x32_bf16 v[32:35], a[120:123], a[12:15], v[32:35]\n"
        "v_mfma_f32_16x16x32_bf16 v[36:39], a[124:127], a[12:15], 0\n"
        "global_store_dwordx4 v254, v[24:27], s[28:29] offset:3072 sc1\n"
        "s_waitcnt lgkmcnt(2)\n"
        "v_mfma_f32_16x16x32_bf16 v[36:39], a[116:119], a[8:11], v[36:39]\n"
        "v_mfma_f32_16x16x32_bf16 v[36:39], a[108:111], a[4:7], v[36:39]\n"
        "v_mfma_f32_16x16x32_bf16 v[36:39], a[100:103], a[0:3], v[36:39]\n"
        "global_store_dwordx4 v229, v[88:91], s[24:25] offset:0 sc1\n"
        "v_mfma_f32_16x16x32_bf16 v[44:47], a[100:103], a[16:19], 0\n"
        "v_mfma_f32_16x16x32_bf16 v[44:47], a[108:111], a[20:23], v[44:47]\n"
        "v_cvt_pk_bf16_f32 v88, v80, v81\n"
        "v_cvt_pk_bf16_f32 v89, v82, v83\n"
        "v_mfma_f32_16x16x32_bf16 v[44:47], a[116:119], a[24:27], v[44:47]\n"
        "global_store_dwordx4 v229, v[92:95], s[24:25] offset:1024 sc1\n"
        "v_cvt_pk_bf16_f32 v90, v84, v85\n"
        "v_cvt_pk_bf16_f32 v91, v86, v87\n"
        "v_and_b32 v80, s35, v77\n"
        "v_lshlrev_b32 v81, 16, v77\n"
        "v_mfma_f32_16x16x32_bf16 v[44:47], a[124:127], a[28:31], v[44:47]\n"
        "s_lshl2_add_u32 s15, s75, s10\n"
        "v_permlane16_swap_b32 v88, v90\n"
        "v_mfma_f32_16x16x32_bf16 v[40:43], a[120:123], a[28:31], 0\n"
        "s_mul_i32 s16, s13, 263168\n"
        "v_permlane16_swap_b32 v89, v91\n"
        "v_mfma_f32_16x16x32_bf16 v[40:43], a[112:115], a[24:27], v[40:43]\n"
        "s_lshl_b32 s15, s15, 14\n"
        "v_and_b32 v82, s35, v76\n"
        "v_lshlrev_b32 v83, 16, v76\n"
        "v_mfma_f32_16x16x32_bf16 v[40:43], a[104:107], a[20:23], v[40:43]\n"
        "s_add_u32 s16, s16, s15\n"
        "v_mul_f32 v80, s54, v80\n"
        "v_mul_f32 v81, s54, v81\n"
        "v_mfma_f32_16x16x32_bf16 v[40:43], a[96:99], a[16:19], v[40:43]\n"
        "s_waitcnt vmcnt(34)\n"
        "v_mul_f32 v82, s54, v82\n"
        "v_mul_f32 v83, s54, v83\n"
        "v_mfma_f32_16x16x32_bf16 v[48:51], a[96:99], a[192:195], 0\n"
        "s_barrier\n"
        "v_mfma_f32_16x16x32_bf16 v[48:51], a[104:107], a[196:199], v[48:51]\n"
        "ds_read_b128 a[0:3], v244 offset:32768\n"
        "global_load_lds_dwordx4 v254, s[60:61] offset:0 sc1\n"
        "s_add_u32 s16, s16, s73\n"
        "v_mfma_f32_16x16x32_bf16 v[48:51], a[112:115], a[200:203], v[48:51]\n"
        "ds_read_b128 a[4:7], v244 offset:36864\n"
        "s_add_u32 s16, s16, 0x4000000\n"
        "v_cvt_pk_bf16_f32 v80, 0, v80\n"
        "v_mfma_f32_16x16x32_bf16 v[48:51], a[120:123], a[204:207], v[48:51]\n"
        "ds_read_b128 a[8:11], v244 offset:40960\n"
        "v_cvt_pk_bf16_f32 v81, 0, v81\n"
        "v_mfma_f32_16x16x32_bf16 v[52:55], a[124:127], a[204:207], 0\n"
        "ds_read_b128 a[12:15], v244 offset:45056\n"
        "global_load_lds_dwordx4 v254, s[60:61] offset:1024 sc1\n"
        "v_mfma_f32_16x16x32_bf16 v[52:55], a[116:119], a[200:203], v[52:55]\n"
        "ds_read_b128 a[16:19], v244 offset:33792\n"
        "v_cvt_pk_bf16_f32 v82, 0, v82\n"
        "v_mfma_f32_16x16x32_bf16 v[52:55], a[108:111], a[196:199], v[52:55]\n"
        "ds_read_b128 a[20:23], v244 offset:37888\n"
        "v_cvt_pk_bf16_f32 v83, 0, v83\n"
        "v_mfma_f32_16x16x32_bf16 v[52:55], a[100:103], a[192:195], v[52:55]\n"
        "ds_read_b128 a[24:27], v244 offset:41984\n"
        "global_load_lds_dwordx4 v254, s[60:61] offset:2048 sc1\n"
        "v_mfma_f32_16x16x32_bf16 v[60:63], a[100:103], a[208:211], 0\n"
        "ds_read_b128 a[28:31], v244 offset:46080\n"
        "v_add_f32 v80, v80, v136\n"
        "v_mfma_f32_16x16x32_bf16 v[60:63], a[108:111], a[212:215], v[60:63]\n"
        "ds_read_b128 a[192:195], v244 offset:34816\n"
        "s_waitcnt lgkmcnt(8)\n"
        "v_mfma_f32_16x16x32_bf16 v[60:63], a[116:119], a[216:219], v[60:63]\n"
        "ds_read_b128 a[196:199], v244 offset:38912\n"
        "global_load_lds_dwordx4 v254, s[60:61] offset:3072 sc1\n"
        "s_add_u32 m0, s73, 0x4000\n"
        "v_mfma_f32_16x16x32_bf16 v[60:63], a[124:127], a[220:223], v[60:63]\n"
        "ds_read_b128 a[200:203], v244 offset:43008\n"
        "s_add_u32 s60, s68, s66\n"
        "s_addc_u32 s61, s69, 0\n"
        "v_mfma_f32_16x16x32_bf16 v[56:59], a[120:123], a[220:223], 0\n"
        "ds_read_b128 a[204:207], v244 offset:47104\n"
        "s_xor_b32 s66, s17, 3\n"
        "s_lshl_b32 s66, s66, 15\n"
        "v_mfma_f32_16x16x32_bf16 v[56:59], a[112:115], a[216:219], v[56:59]\n"
        "ds_read_b128 a[216:219], v244 offset:44032\n"
        "v_add_f32 v81, v81, v137\n"
        "v_mfma_f32_16x16x32_bf16 v[56:59], a[104:107], a[212:215], v[56:59]\n"
        "ds_read_b128 a[212:215], v244 offset:39936\n"
        "v_add_f32 v82, v82, v138\n"
        "v_mfma_f32_16x16x32_bf16 v[56:59], a[96:99], a[208:211], v[56:59]\n"
        "ds_read_b128 a[208:211], v244 offset:35840\n"
        "v_add_f32 v83, v83, v139\n"
        "v_mfma_f32_16x16x32_bf16 v[32:35], a[128:131], a[0:3], v[32:35]\n"
        "ds_read_b128 a[220:223], v244 offset:48128\n"
        "s_waitcnt lgkmcnt(8)\n"
        "v_mfma_f32_16x16x32_bf16 v[32:35], a[136:139], a[4:7], v[32:35]\n"
        "s_add_u32 s28, s38, s16\n"
        "s_addc_u32 s29, s39, 0\n"
        "v_cvt_pk_bf16_f32 v92, v80, v81\n"
        "v_mfma_f32_16x16x32_bf16 v[32:35], a[144:147], a[8:11], v[32:35]\n"
        "v_cvt_pk_bf16_f32 v93, v82, v83\n"
        "v_and_b32 v84, s35, v79\n"
        "v_mfma_f32_16x16x32_bf16 v[32:35], a[152:155], a[12:15], v[32:35]\n"
        "v_lshlrev_b32 v85, 16, v79\n"
        "v_and_b32 v86, s35, v78\n"
        "v_mfma_f32_16x16x32_bf16 v[36:39], a[156:159], a[12:15], v[36:39]\n"
        "v_lshlrev_b32 v87, 16, v78\n"
        "v_mul_f32 v84, s54, v84\n"
        "v_mfma_f32_16x16x32_bf16 v[36:39], a[148:151], a[8:11], v[36:39]\n"
        "v_mul_f32 v85, s54, v85\n"
        "v_mul_f32 v86, s54, v86\n"
        "v_mfma_f32_16x16x32_bf16 v[36:39], a[140:143], a[4:7], v[36:39]\n"
        "v_mul_f32 v87, s54, v87\n"
        "v_cvt_pk_bf16_f32 v84, 0, v84\n"
        "v_mfma_f32_16x16x32_bf16 v[36:39], a[132:135], a[0:3], v[36:39]\n"
        "global_store_dwordx4 v229, v[88:91], s[24:25] offset:2048 sc1\n"
        "s_and_b32 s67, s10, 1\n"
        "v_cvt_pk_bf16_f32 v85, 0, v85\n"
        "v_cvt_pk_bf16_f32 v86, 0, v86\n"
        "v_cvt_pk_bf16_f32 v87, 0, v87\n"
        "v_add_f32 v84, v84, v140\n"
        "v_mfma_f32_16x16x32_bf16 v[44:47], a[132:135], a[16:19], v[44:47]\n"
        "s_lshl_b32 s67, s67, 12\n"
        "v_add_f32 v85, v85, v141\n"
        "v_add_f32 v86, v86, v142\n"
        "v_mfma_f32_16x16x32_bf16 v[44:47], a[140:143], a[20:23], v[44:47]\n"
        "v_add_f32 v87, v87, v143\n"
        "v_cvt_pk_bf16_f32 v94, v84, v85\n"
        "v_mfma_f32_16x16x32_bf16 v[44:47], a[148:151], a[24:27], v[44:47]\n"
        "v_cvt_pk_bf16_f32 v95, v86, v87\n"
        "v_mfma_f32_16x16x32_bf16 v[44:47], a[156:159], a[28:31], v[44:47]\n"
        "v_permlane16_swap_b32 v92, v94\n"
        "v_mfma_f32_16x16x32_bf16 v[40:43], a[152:155], a[28:31], v[40:43]\n"
        "s_waitcnt lgkmcnt(0)\n"
        "v_permlane16_swap_b32 v93, v95\n"
        "v_mfma_f32_16x16x32_bf16 v[40:43], a[144:147], a[24:27], v[40:43]\n"
        "v_mfma_f32_16x16x32_bf16 v[40:43], a[136:139], a[20:23], v[40:43]\n"
        "v_mfma_f32_16x16x32_bf16 v[40:43], a[128:131], a[16:19], v[40:43]\n"
        "s_waitcnt vmcnt(35)\n"
        "v_mfma_f32_16x16x32_bf16 v[48:51], a[128:131], a[192:195], v[48:51]\n"
        "s_barrier\n"
        "v_mfma_f32_16x16x32_bf16 v[48:51], a[136:139], a[196:199], v[48:51]\n"
        "ds_read_b128 a[0:3], v254 offset:49152\n"
        "global_load_lds_dwordx4 v254, s[60:61] offset:0 sc1\n"
        "v_mfma_f32_16x16x32_bf16 v[48:51], a[144:147], a[200:203], v[48:51]\n"
        "ds_read_b128 a[4:7], v254 offset:53248\n"
        "v_mfma_f32_16x16x32_bf16 v[48:51], a[152:155], a[204:207], v[48:51]\n"
        "ds_read_b128 a[8:11], v254 offset:57344\n"
        "v_mfma_f32_16x16x32_bf16 v[52:55], a[156:159], a[204:207], v[52:55]\n"
        "ds_read_b128 a[12:15], v254 offset:61440\n"
        "global_load_lds_dwordx4 v254, s[60:61] offset:1024 sc1\n"
        "v_mfma_f32_16x16x32_bf16 v[52:55], a[148:151], a[200:203], v[52:55]\n"
        "ds_read_b128 a[16:19], v254 offset:50176\n"
        "v_mfma_f32_16x16x32_bf16 v[52:55], a[140:143], a[196:199], v[52:55]\n"
        "ds_read_b128 a[20:23], v254 offset:54272\n"
        "v_mfma_f32_16x16x32_bf16 v[52:55], a[132:135], a[192:195], v[52:55]\n"
        "ds_read_b128 a[24:27], v254 offset:58368\n"
        "global_load_lds_dwordx4 v254, s[60:61] offset:2048 sc1\n"
        "v_mfma_f32_16x16x32_bf16 v[60:63], a[132:135], a[208:211], v[60:63]\n"
        "ds_read_b128 a[28:31], v254 offset:62464\n"
        "v_mfma_f32_16x16x32_bf16 v[60:63], a[140:143], a[212:215], v[60:63]\n"
        "ds_read_b128 a[192:195], v254 offset:51200\n"
        "v_mfma_f32_16x16x32_bf16 v[60:63], a[148:151], a[216:219], v[60:63]\n"
        "ds_read_b128 a[196:199], v254 offset:55296\n"
        "global_load_lds_dwordx4 v254, s[60:61] offset:3072 sc1\n"
        "s_add_u32 m0, s73, 0x18000\n"
        "v_mfma_f32_16x16x32_bf16 v[60:63], a[156:159], a[220:223], v[60:63]\n"
        "ds_read_b128 a[200:203], v254 offset:59392\n"
        "s_add_u32 s60, s68, s66\n"
        "s_addc_u32 s61, s69, 0\n"
        "v_mfma_f32_16x16x32_bf16 v[56:59], a[152:155], a[220:223], v[56:59]\n"
        "ds_read_b128 a[204:207], v254 offset:63488\n"
        "s_xor_b32 s66, s17, 4\n"
        "s_lshl_b32 s66, s66, 15\n"
        "v_mfma_f32_16x16x32_bf16 v[56:59], a[144:147], a[216:219], v[56:59]\n"
        "ds_read_b128 a[216:219], v254 offset:60416\n"
        "s_waitcnt lgkmcnt(8)\n"
        "v_mfma_f32_16x16x32_bf16 v[56:59], a[136:139], a[212:215], v[56:59]\n"
        "ds_read_b128 a[212:215], v254 offset:56320\n"
        "v_mfma_f32_16x16x32_bf16 v[56:59], a[128:131], a[208:211], v[56:59]\n"
        "ds_read_b128 a[208:211], v254 offset:52224\n"
        "v_mfma_f32_16x16x32_bf16 v[32:35], v[160:163], a[0:3], v[32:35]\n"
        "ds_read_b128 a[220:223], v254 offset:64512\n"
        "v_mfma_f32_16x16x32_bf16 v[32:35], v[168:171], a[4:7], v[32:35]\n"
        "v_mfma_f32_16x16x32_bf16 v[32:35], v[176:179], a[8:11], v[32:35]\n"
        "v_mfma_f32_16x16x32_bf16 v[32:35], v[184:187], a[12:15], v[32:35]\n"
        "v_mfma_f32_16x16x32_bf16 v[36:39], v[188:191], a[12:15], v[36:39]\n"
        "v_mfma_f32_16x16x32_bf16 v[36:39], v[180:183], a[8:11], v[36:39]\n"
        "s_waitcnt lgkmcnt(3)\n"
        "v_mfma_f32_16x16x32_bf16 v[36:39], v[172:175], a[4:7], v[36:39]\n"
        "v_mfma_f32_16x16x32_bf16 v[36:39], v[164:167], a[0:3], v[36:39]\n"
        "global_store_dwordx4 v229, v[92:95], s[24:25] offset:3072 sc1\n"
        "v_mfma_f32_16x16x32_bf16 v[44:47], v[164:167], a[16:19], v[44:47]\n"
        "v_mfma_f32_16x16x32_bf16 v[44:47], v[172:175], a[20:23], v[44:47]\n"
        "v_mfma_f32_16x16x32_bf16 v[44:47], v[180:183], a[24:27], v[44:47]\n"
        "v_mfma_f32_16x16x32_bf16 v[44:47], v[188:191], a[28:31], v[44:47]\n"
        "v_mfma_f32_16x16x32_bf16 v[40:43], v[184:187], a[28:31], v[40:43]\n"
        "v_mfma_f32_16x16x32_bf16 v[40:43], v[176:179], a[24:27], v[40:43]\n"
        "v_mfma_f32_16x16x32_bf16 v[40:43], v[168:171], a[20:23], v[40:43]\n"
        "v_mfma_f32_16x16x32_bf16 v[40:43], v[160:163], a[16:19], v[40:43]\n"
        "s_waitcnt vmcnt(36)\n"
        "v_mfma_f32_16x16x32_bf16 v[48:51], v[160:163], a[192:195], v[48:51]\n"
        "s_barrier\n"
        "v_mfma_f32_16x16x32_bf16 v[48:51], v[168:171], a[196:199], v[48:51]\n"
        "ds_read_b128 a[0:3], v245 offset:0\n"
        "global_load_lds_dwordx4 v254, s[60:61] offset:0 sc1\n"
        "v_mfma_f32_16x16x32_bf16 v[48:51], v[176:179], a[200:203], v[48:51]\n"
        "ds_read_b128 a[4:7], v245 offset:4096\n"
        "v_mfma_f32_16x16x32_bf16 v[48:51], v[184:187], a[204:207], v[48:51]\n"
        "ds_read_b128 a[8:11], v245 offset:8192\n"
        "v_mfma_f32_16x16x32_bf16 v[52:55], v[188:191], a[204:207], v[52:55]\n"
        "ds_read_b128 a[12:15], v245 offset:12288\n"
        "global_load_lds_dwordx4 v254, s[60:61] offset:1024 sc1\n"
        "v_mfma_f32_16x16x32_bf16 v[52:55], v[180:183], a[200:203], v[52:55]\n"
        "ds_read_b128 a[16:19], v245 offset:1024\n"
        "s_waitcnt lgkmcnt(5)\n"
        "v_mfma_f32_16x16x32_bf16 v[52:55], v[172:175], a[196:199], v[52:55]\n"
        "ds_read_b128 a[20:23], v245 offset:5120\n"
        "v_mfma_f32_16x16x32_bf16 v[52:55], v[164:167], a[192:195], v[52:55]\n"
        "ds_read_b128 a[24:27], v245 offset:9216\n"
        "global_load_lds_dwordx4 v254, s[60:61] offset:2048 sc1\n"
        "v_mfma_f32_16x16x32_bf16 v[60:63], v[164:167], a[208:211], v[60:63]\n"
        "ds_read_b128 a[28:31], v245 offset:13312\n"
        "v_mfma_f32_16x16x32_bf16 v[60:63], v[172:175], a[212:215], v[60:63]\n"
        "ds_read_b128 a[192:195], v245 offset:2048\n"
        "v_mfma_f32_16x16x32_bf16 v[60:63], v[180:183], a[216:219], v[60:63]\n"
        "ds_read_b128 a[196:199], v245 offset:6144\n"
        "global_load_lds_dwordx4 v254, s[60:61] offset:3072 sc1\n"
        "s_add_u32 m0, s73, 0xc000\n"
        "v_mfma_f32_16x16x32_bf16 v[60:63], v[188:191], a[220:223], v[60:63]\n"
        "ds_read_b128 a[200:203], v245 offset:10240\n"
        "s_add_u32 s60, s68, s66\n"
        "s_addc_u32 s61, s69, 0\n"
        "v_mfma_f32_16x16x32_bf16 v[56:59], v[184:187], a[220:223], v[56:59]\n"
        "ds_read_b128 a[204:207], v245 offset:14336\n"
        "s_xor_b32 s66, s17, 5\n"
        "s_lshl_b32 s66, s66, 15\n"
        "v_mfma_f32_16x16x32_bf16 v[56:59], v[176:179], a[216:219], v[56:59]\n"
        "ds_read_b128 a[216:219], v245 offset:11264\n"
        "s_waitcnt lgkmcnt(8)\n"
        "v_mfma_f32_16x16x32_bf16 v[56:59], v[168:171], a[212:215], v[56:59]\n"
        "ds_read_b128 a[212:215], v245 offset:7168\n"
        "v_mfma_f32_16x16x32_bf16 v[56:59], v[160:163], a[208:211], v[56:59]\n"
        "ds_read_b128 a[208:211], v245 offset:3072\n"
        "v_mfma_f32_16x16x32_bf16 v[32:35], v[192:195], a[0:3], v[32:35]\n"
        "ds_read_b128 a[220:223], v245 offset:15360\n"
        "v_mfma_f32_16x16x32_bf16 v[32:35], v[200:203], a[4:7], v[32:35]\n"
        "v_mfma_f32_16x16x32_bf16 v[32:35], v[208:211], a[8:11], v[32:35]\n"
        "v_mfma_f32_16x16x32_bf16 v[32:35], v[216:219], a[12:15], v[32:35]\n"
        "v_mfma_f32_16x16x32_bf16 v[36:39], v[220:223], a[12:15], v[36:39]\n"
        "v_mfma_f32_16x16x32_bf16 v[36:39], v[212:215], a[8:11], v[36:39]\n"
        "s_waitcnt lgkmcnt(3)\n"
        "v_mfma_f32_16x16x32_bf16 v[36:39], v[204:207], a[4:7], v[36:39]\n"
        "v_mfma_f32_16x16x32_bf16 v[36:39], v[196:199], a[0:3], v[36:39]\n"
        "v_mfma_f32_16x16x32_bf16 v[44:47], v[196:199], a[16:19], v[44:47]\n"
        "v_mfma_f32_16x16x32_bf16 v[44:47], v[204:207], a[20:23], v[44:47]\n"
        "v_mfma_f32_16x16x32_bf16 v[44:47], v[212:215], a[24:27], v[44:47]\n"
        "v_mfma_f32_16x16x32_bf16 v[44:47], v[220:223], a[28:31], v[44:47]\n"
        "v_mfma_f32_16x16x32_bf16 v[40:43], v[216:219], a[28:31], v[40:43]\n"
        "v_mfma_f32_16x16x32_bf16 v[40:43], v[208:211], a[24:27], v[40:43]\n"
        "v_mfma_f32_16x16x32_bf16 v[40:43], v[200:203], a[20:23], v[40:43]\n"
        "v_mfma_f32_16x16x32_bf16 v[40:43], v[192:195], a[16:19], v[40:43]\n"
        "s_waitcnt vmcnt(36)\n"
        "v_mfma_f32_16x16x32_bf16 v[48:51], v[192:195], a[192:195], v[48:51]\n"
        "s_barrier\n"
        "v_mfma_f32_16x16x32_bf16 v[48:51], v[200:203], a[196:199], v[48:51]\n"
        "ds_read_b128 a[0:3], v244 offset:49152\n"
        "global_load_lds_dwordx4 v254, s[60:61] offset:0 sc1\n"
        "v_mfma_f32_16x16x32_bf16 v[48:51], v[208:211], a[200:203], v[48:51]\n"
        "ds_read_b128 a[4:7], v244 offset:53248\n"
        "v_mfma_f32_16x16x32_bf16 v[48:51], v[216:219], a[204:207], v[48:51]\n"
        "ds_read_b128 a[8:11], v244 offset:57344\n"
        "v_mfma_f32_16x16x32_bf16 v[52:55], v[220:223], a[204:207], v[52:55]\n"
        "ds_read_b128 a[12:15], v244 offset:61440\n"
        "global_load_lds_dwordx4 v254, s[60:61] offset:1024 sc1\n"
        "v_mfma_f32_16x16x32_bf16 v[52:55], v[212:215], a[200:203], v[52:55]\n"
        "ds_read_b128 a[16:19], v244 offset:50176\n"
        "s_waitcnt lgkmcnt(5)\n"
        "v_mfma_f32_16x16x32_bf16 v[52:55], v[204:207], a[196:199], v[52:55]\n"
        "ds_read_b128 a[20:23], v244 offset:54272\n"
        "v_mfma_f32_16x16x32_bf16 v[52:55], v[196:199], a[192:195], v[52:55]\n"
        "ds_read_b128 a[24:27], v244 offset:58368\n"
        "global_load_lds_dwordx4 v254, s[60:61] offset:2048 sc1\n"
        "v_mfma_f32_16x16x32_bf16 v[60:63], v[196:199], a[208:211], v[60:63]\n"
        "ds_read_b128 a[28:31], v244 offset:62464\n"
        "v_mfma_f32_16x16x32_bf16 v[60:63], v[204:207], a[212:215], v[60:63]\n"
        "ds_read_b128 a[192:195], v244 offset:51200\n"
        "v_mfma_f32_16x16x32_bf16 v[60:63], v[212:215], a[216:219], v[60:63]\n"
        "ds_read_b128 a[196:199], v244 offset:55296\n"
        "global_load_lds_dwordx4 v254, s[60:61] offset:3072 sc1\n"
        "s_add_u32 m0, s73, 0x20000\n"
        "v_mfma_f32_16x16x32_bf16 v[60:63], v[220:223], a[220:223], v[60:63]\n"
        "ds_read_b128 a[200:203], v244 offset:59392\n"
        "s_add_u32 s60, s68, s66\n"
        "s_addc_u32 s61, s69, 0\n"
        "v_mfma_f32_16x16x32_bf16 v[56:59], v[216:219], a[220:223], v[56:59]\n"
        "ds_read_b128 a[204:207], v244 offset:63488\n"
        "s_xor_b32 s66, s17, 6\n"
        "s_lshl_b32 s66, s66, 15\n"
        "v_mfma_f32_16x16x32_bf16 v[56:59], v[208:211], a[216:219], v[56:59]\n"
        "ds_read_b128 a[216:219], v244 offset:60416\n"
        "s_waitcnt lgkmcnt(8)\n"
        "v_mfma_f32_16x16x32_bf16 v[56:59], v[200:203], a[212:215], v[56:59]\n"
        "ds_read_b128 a[212:215], v244 offset:56320\n"
        "v_mfma_f32_16x16x32_bf16 v[56:59], v[192:195], a[208:211], v[56:59]\n"
        "ds_read_b128 a[208:211], v244 offset:52224\n"
        "v_mfma_f32_16x16x32_bf16 v[32:35], v[112:115], a[0:3], v[32:35]\n"
        "ds_read_b128 a[220:223], v244 offset:64512\n"
        "v_mfma_f32_16x16x32_bf16 v[32:35], v[120:123], a[4:7], v[32:35]\n"
        "v_mfma_f32_16x16x32_bf16 v[32:35], a[64:67], a[8:11], v[32:35]\n"
        "v_mfma_f32_16x16x32_bf16 v[32:35], a[72:75], a[12:15], v[32:35]\n"
        "v_mfma_f32_16x16x32_bf16 v[36:39], a[76:79], a[12:15], v[36:39]\n"
        "v_mfma_f32_16x16x32_bf16 v[36:39], a[68:71], a[8:11], v[36:39]\n"
        "s_waitcnt lgkmcnt(3)\n"
        "v_mfma_f32_16x16x32_bf16 v[36:39], v[124:127], a[4:7], v[36:39]\n"
        "v_mfma_f32_16x16x32_bf16 v[36:39], v[116:119], a[0:3], v[36:39]\n"
        "v_lshlrev_b32 v159, 2, v232\n"
        "v_add_u32 v159, 0x8000, v159\n"
        "v_mfma_f32_16x16x32_bf16 v[44:47], v[116:119], a[16:19], v[44:47]\n"
        "v_mfma_f32_16x16x32_bf16 v[44:47], v[124:127], a[20:23], v[44:47]\n"
        "v_mfma_f32_16x16x32_bf16 v[44:47], a[68:71], a[24:27], v[44:47]\n"
        "v_mfma_f32_16x16x32_bf16 v[44:47], a[76:79], a[28:31], v[44:47]\n"
        "v_mfma_f32_16x16x32_bf16 v[40:43], a[72:75], a[28:31], v[40:43]\n"
        "v_mfma_f32_16x16x32_bf16 v[40:43], a[64:67], a[24:27], v[40:43]\n"
        "v_mfma_f32_16x16x32_bf16 v[40:43], v[120:123], a[20:23], v[40:43]\n"
        "v_mfma_f32_16x16x32_bf16 v[40:43], v[112:115], a[16:19], v[40:43]\n"
        "s_waitcnt vmcnt(36)\n"
        "v_mfma_f32_16x16x32_bf16 v[48:51], v[112:115], a[192:195], v[48:51]\n"
        "s_barrier\n"
        "v_mfma_f32_16x16x32_bf16 v[48:51], v[120:123], a[196:199], v[48:51]\n"
        "ds_read_b128 a[0:3], v244 offset:0\n"
        "global_load_lds_dwordx4 v254, s[60:61] offset:0 sc1\n"
        "v_mfma_f32_16x16x32_bf16 v[48:51], a[64:67], a[200:203], v[48:51]\n"
        "ds_read_b128 a[4:7], v244 offset:4096\n"
        "v_mfma_f32_16x16x32_bf16 v[48:51], a[72:75], a[204:207], v[48:51]\n"
        "ds_read_b128 a[8:11], v244 offset:8192\n"
        "v_mfma_f32_16x16x32_bf16 v[52:55], a[76:79], a[204:207], v[52:55]\n"
        "ds_read_b128 a[12:15], v244 offset:12288\n"
        "global_load_lds_dwordx4 v254, s[60:61] offset:1024 sc1\n"
        "v_mfma_f32_16x16x32_bf16 v[52:55], a[68:71], a[200:203], v[52:55]\n"
        "ds_read_b128 a[16:19], v244 offset:1024\n"
        "s_waitcnt lgkmcnt(5)\n"
        "v_mfma_f32_16x16x32_bf16 v[52:55], v[124:127], a[196:199], v[52:55]\n"
        "ds_read_b128 a[20:23], v244 offset:5120\n"
        "v_mfma_f32_16x16x32_bf16 v[52:55], v[116:119], a[192:195], v[52:55]\n"
        "ds_read_b128 a[24:27], v244 offset:9216\n"
        "global_load_lds_dwordx4 v254, s[60:61] offset:2048 sc1\n"
        "v_mfma_f32_16x16x32_bf16 v[60:63], v[116:119], a[208:211], v[60:63]\n"
        "ds_read_b128 a[28:31], v244 offset:13312\n"
        "v_mfma_f32_16x16x32_bf16 v[60:63], v[124:127], a[212:215], v[60:63]\n"
        "ds_read_b128 a[192:195], v244 offset:2048\n"
        "v_mfma_f32_16x16x32_bf16 v[60:63], a[68:71], a[216:219], v[60:63]\n"
        "ds_read_b128 a[196:199], v244 offset:6144\n"
        "global_load_lds_dwordx4 v254, s[60:61] offset:3072 sc1\n"
        "s_add_u32 m0, s73, 0x1c000\n"
        "v_mfma_f32_16x16x32_bf16 v[60:63], a[76:79], a[220:223], v[60:63]\n"
        "ds_read_b128 a[200:203], v244 offset:10240\n"
        "s_add_u32 s60, s68, s66\n"
        "s_addc_u32 s61, s69, 0\n"
        "v_mfma_f32_16x16x32_bf16 v[56:59], a[72:75], a[220:223], v[56:59]\n"
        "ds_read_b128 a[204:207], v244 offset:14336\n"
        "s_xor_b32 s66, s17, 7\n"
        "s_lshl_b32 s66, s66, 15\n"
        "v_mfma_f32_16x16x32_bf16 v[56:59], a[64:67], a[216:219], v[56:59]\n"
        "ds_read_b128 a[216:219], v244 offset:11264\n"
        "s_waitcnt lgkmcnt(8)\n"
        "v_mfma_f32_16x16x32_bf16 v[56:59], v[120:123], a[212:215], v[56:59]\n"
        "ds_read_b128 a[212:215], v244 offset:7168\n"
        "v_mfma_f32_16x16x32_bf16 v[56:59], v[112:115], a[208:211], v[56:59]\n"
        "ds_read_b128 a[208:211], v244 offset:3072\n"
        "v_mfma_f32_16x16x32_bf16 v[32:35], a[80:83], a[0:3], v[32:35]\n"
        "ds_read_b128 a[220:223], v244 offset:15360\n"
        "v_mfma_f32_16x16x32_bf16 v[32:35], a[88:91], a[4:7], v[32:35]\n"
        "v_mfma_f32_16x16x32_bf16 v[32:35], a[160:163], a[8:11], v[32:35]\n"
        "v_mfma_f32_16x16x32_bf16 v[32:35], a[168:171], a[12:15], v[32:35]\n"
        "v_mfma_f32_16x16x32_bf16 v[36:39], a[172:175], a[12:15], v[36:39]\n"
        "v_mfma_f32_16x16x32_bf16 v[36:39], a[164:167], a[8:11], v[36:39]\n"
        "s_waitcnt lgkmcnt(3)\n"
        "v_mfma_f32_16x16x32_bf16 v[36:39], a[92:95], a[4:7], v[36:39]\n"
        "v_mfma_f32_16x16x32_bf16 v[36:39], a[84:87], a[0:3], v[36:39]\n"
        "s_lshl_b32 s16, s63, 12\n"
        "v_and_b32 v150, 1, v233\n"
        "v_lshlrev_b32 v151, 11, v232\n"
        "v_mfma_f32_16x16x32_bf16 v[44:47], a[84:87], a[16:19], v[44:47]\n"
        "s_lshl1_add_u32 s16, s62, s16\n"
        "v_lshl_add_u32 v151, v150, 5, v151\n"
        "v_lshrrev_b32 v150, 1, v233\n"
        "v_mfma_f32_16x16x32_bf16 v[44:47], a[92:95], a[20:23], v[44:47]\n"
        "s_add_u32 s16, s16, 0x3700000\n"
        "v_lshl_add_u32 v151, v150, 4, v151\n"
        "v_lshrrev_b32 v148, 7, v254\n"
        "v_mfma_f32_16x16x32_bf16 v[44:47], a[164:167], a[24:27], v[44:47]\n"
        "s_add_u32 s58, s38, s16\n"
        "s_addc_u32 s59, s39, 0\n"
        "v_lshlrev_b32 v148, 12, v148\n"
        "v_mfma_f32_16x16x32_bf16 v[44:47], a[172:175], a[28:31], v[44:47]\n"
        "s_add_u32 s16, s73, 0x4000\n"
        "v_and_b32 v150, 0x70, v254\n"
        "v_add_u32 v148, v148, v150\n"
        "v_mfma_f32_16x16x32_bf16 v[40:43], a[168:171], a[28:31], v[40:43]\n"
        "v_lshrrev_b32 v149, 6, v254\n"
        "v_lshlrev_b32 v149, 7, v149\n"
        "v_mfma_f32_16x16x32_bf16 v[40:43], a[160:163], a[24:27], v[40:43]\n"
        "v_bfe_u32 v150, v254, 4, 2\n"
        "v_lshl_add_u32 v149, v150, 3, v149\n"
        "v_mfma_f32_16x16x32_bf16 v[40:43], a[88:91], a[20:23], v[40:43]\n"
        "v_mov_b32 v238, 0\n"
        "v_mov_b32 v239, 0\n"
        "v_mfma_f32_16x16x32_bf16 v[40:43], a[80:83], a[16:19], v[40:43]\n"
        "s_waitcnt vmcnt(12)\n"
        "v_mov_b32 v242, 0\n"
        "v_mov_b32 v243, 0\n"
        "v_mfma_f32_16x16x32_bf16 v[48:51], a[80:83], a[192:195], v[48:51]\n"
        "s_barrier\n"
        "v_mfma_f32_16x16x32_bf16 v[48:51], a[88:91], a[196:199], v[48:51]\n"
        "ds_read_b128 a[0:3], v245 offset:16384\n"
        "global_load_lds_dwordx4 v254, s[60:61] offset:0 sc1\n"
        "v_mfma_f32_16x16x32_bf16 v[48:51], a[160:163], a[200:203], v[48:51]\n"
        "ds_read_b128 a[4:7], v245 offset:20480\n"
        "v_and_b32 v246, 1, v232\n"
        "v_mfma_f32_16x16x32_bf16 v[48:51], a[168:171], a[204:207], v[48:51]\n"
        "ds_read_b128 a[8:11], v245 offset:24576\n"
        "v_lshlrev_b32 v246, 4, v246\n"
        "v_mfma_f32_16x16x32_bf16 v[52:55], a[172:175], a[204:207], v[52:55]\n"
        "ds_read_b128 a[12:15], v245 offset:28672\n"
        "global_load_lds_dwordx4 v254, s[60:61] offset:1024 sc1\n"
        "v_mfma_f32_16x16x32_bf16 v[52:55], a[164:167], a[200:203], v[52:55]\n"
        "ds_read_b128 a[16:19], v245 offset:17408\n"
        "v_mov_b32 v247, 0x3f80\n"
        "v_mfma_f32_16x16x32_bf16 v[52:55], a[92:95], a[196:199], v[52:55]\n"
        "ds_read_b128 a[20:23], v245 offset:21504\n"
        "v_lshlrev_b32 v246, v246, v247\n"
        "v_mfma_f32_16x16x32_bf16 v[52:55], a[84:87], a[192:195], v[52:55]\n"
        "ds_read_b128 a[24:27], v245 offset:25600\n"
        "s_waitcnt lgkmcnt(7)\n"
        "v_mfma_f32_16x16x32_bf16 v[60:63], a[84:87], a[208:211], v[60:63]\n"
        "ds_read_b128 a[28:31], v245 offset:29696\n"
        "global_load_lds_dwordx4 v254, s[60:61] offset:2048 sc1\n"
        "v_mfma_f32_16x16x32_bf16 v[60:63], a[92:95], a[212:215], v[60:63]\n"
        "ds_read_b128 a[192:195], v245 offset:18432\n"
        "v_lshrrev_b32 v247, 1, v232\n"
        "v_mfma_f32_16x16x32_bf16 v[60:63], a[164:167], a[216:219], v[60:63]\n"
        "ds_read_b128 a[196:199], v245 offset:22528\n"
        "v_lshlrev_b32 v248, 1, v233\n"
        "v_mfma_f32_16x16x32_bf16 v[60:63], a[172:175], a[220:223], v[60:63]\n"
        "ds_read_b128 a[200:203], v245 offset:26624\n"
        "global_load_lds_dwordx4 v254, s[60:61] offset:3072 sc1\n"
        "s_add_u32 m0, s73, 0x10000\n"
        "v_mfma_f32_16x16x32_bf16 v[56:59], a[168:171], a[220:223], v[56:59]\n"
        "ds_read_b128 a[204:207], v245 offset:30720\n"
        "s_add_u32 s60, s68, s66\n"
        "s_addc_u32 s61, s69, 0\n"
        "v_mfma_f32_16x16x32_bf16 v[56:59], a[160:163], a[216:219], v[56:59]\n"
        "ds_read_b128 a[216:219], v245 offset:27648\n"
        "s_sub_u32 s66, 15, s12\n"
        "v_or_b32 v249, 1, v248\n"
        "LHB1_%=:\n"
        "s_cbranch_execz LHRB_%=\n"
        "v_mfma_f32_16x16x32_bf16 v[56:59], a[88:91], a[212:215], v[56:59]\n"
        "ds_read_b128 a[212:215], v245 offset:23552\n"
        "s_cmp_lt_u32 s10, 2\n"
        "s_cselect_b32 s66, s12, s66\n"
        "v_mfma_f32_16x16x32_bf16 v[56:59], a[80:83], a[208:211], v[56:59]\n"
        "ds_read_b128 a[208:211], v245 offset:19456\n"
        "s_mul_i32 s66, s66, 1052672\n"
        "s_waitcnt lgkmcnt(8)\n"
        "v_mfma_f32_16x16x32_bf16 v[32:35], a[176:179], a[0:3], v[32:35]\n"
        "s_cmp_lg_u32 s40, 0\n"
        "s_cbranch_scc1 LW2_%=\n"
        "s_mov_b64 exec, 0xffff\n"
        "global_store_dwordx2 v234, v[250:251], s[48:49] sc1\n"
        "s_mov_b64 exec, -1\n"
        "LW2_%=:\n"
        "s_add_u32 s66, s66, s67\n"
        "v_mfma_f32_16x16x32_bf16 v[32:35], a[184:187], a[4:7], v[32:35]\n"
        "ds_read_b128 a[220:223], v245 offset:31744\n"
        "s_add_u32 s68, s20, s66\n"
        "s_addc_u32 s69, s21, 0\n"
        "v_mfma_f32_16x16x32_bf16 v[32:35], a[32:35], a[8:11], v[32:35]\n"
        "s_xor_b32 s66, s17, 0\n"
        "s_lshl_b32 s66, s66, 15\n"
        "v_bfe_u32 v234, v254, 4, 3\n"
        "v_mfma_f32_16x16x32_bf16 v[32:35], a[40:43], a[12:15], v[32:35]\n"
        "v_lshl_add_u32 v234, v234, 7, s14\n"
        "s_lshl_b32 s14, s40, 6\n"
        "v_mfma_f32_16x16x32_bf16 v[36:39], a[44:47], a[12:15], v[36:39]\n"
        "v_add_u32 v225, s14, v159\n"
        "v_cmp_eq_u32 s[14:15], v247, v248\n"
        "v_mfma_f32_16x16x32_bf16 v[36:39], a[36:39], a[8:11], v[36:39]\n"
        "v_add_u32 v149, s16, v149\n"
        "v_cndmask_b32 v240, 0, v246, s[14:15]\n"
        "v_mfma_f32_16x16x32_bf16 v[36:39], a[188:191], a[4:7], v[36:39]\n"
        "v_cmp_eq_u32 s[14:15], v247, v249\n"
        "v_mfma_f32_16x16x32_bf16 v[36:39], a[180:183], a[0:3], v[36:39]\n"
        "s_waitcnt lgkmcnt(1)\n"
        "v_cndmask_b32 v241, 0, v246, s[14:15]\n"
        "v_mfma_f32_16x16x32_bf16 v[44:47], a[180:183], a[16:19], v[44:47]\n"
        "v_mfma_f32_16x16x32_bf16 v[44:47], a[188:191], a[20:23], v[44:47]\n"
        "v_mfma_f32_16x16x32_bf16 v[44:47], a[36:39], a[24:27], v[44:47]\n"
        "v_mfma_f32_16x16x32_bf16 v[44:47], a[44:47], a[28:31], v[44:47]\n"
        "v_mfma_f32_16x16x32_bf16 v[40:43], a[40:43], a[28:31], v[40:43]\n"
        "v_mfma_f32_16x16x32_bf16 v[40:43], a[32:35], a[24:27], v[40:43]\n"
        "v_mfma_f32_16x16x32_bf16 v[40:43], a[184:187], a[20:23], v[40:43]\n"
        "v_mfma_f32_16x16x32_bf16 v[40:43], a[176:179], a[16:19], v[40:43]\n"
        "v_mfma_f32_16x16x32_bf16 v[48:51], a[176:179], a[192:195], v[48:51]\n"
        "s_barrier\n"
        "v_mfma_f32_16x16x32_bf16 v[48:51], a[184:187], a[196:199], v[48:51]\n"
        "ds_read_b128 a[0:3], v244 offset:16384\n"
        "global_load_lds_dwordx4 v254, s[60:61] offset:0 sc1\n"
        "v_mfma_f32_16x16x32_bf16 v[48:51], a[32:35], a[200:203], v[48:51]\n"
        "ds_read_b128 a[4:7], v244 offset:20480\n"
        "v_mfma_f32_16x16x32_bf16 v[48:51], a[40:43], a[204:207], v[48:51]\n"
        "ds_read_b128 a[8:11], v244 offset:24576\n"
        "v_mfma_f32_16x16x32_bf16 v[52:55], a[44:47], a[204:207], v[52:55]\n"
        "ds_read_b128 a[12:15], v244 offset:28672\n"
        "global_load_lds_dwordx4 v254, s[60:61] offset:1024 sc1\n"
        "v_mfma_f32_16x16x32_bf16 v[52:55], a[36:39], a[200:203], v[52:55]\n"
        "ds_read_b128 a[28:31], v244 offset:29696\n"
        "v_mfma_f32_16x16x32_bf16 v[52:55], a[188:191], a[196:199], v[52:55]\n"
        "ds_read_b128 a[24:27], v244 offset:25600\n"
        "v_mfma_f32_16x16x32_bf16 v[52:55], a[180:183], a[192:195], v[52:55]\n"
        "ds_read_b128 a[20:23], v244 offset:21504\n"
        "global_load_lds_dwordx4 v254, s[60:61] offset:2048 sc1\n"
        "v_mfma_f32_16x16x32_bf16 v[60:63], a[180:183], a[208:211], v[60:63]\n"
        "ds_read_b128 a[16:19], v244 offset:17408\n"
        "v_mfma_f32_16x16x32_bf16 v[60:63], a[188:191], a[212:215], v[60:63]\n"
        "ds_read_b128 a[192:195], v244 offset:18432\n"
        "s_waitcnt lgkmcnt(8)\n"
        "v_mfma_f32_16x16x32_bf16 v[32:35], a[48:51], a[0:3], v[32:35]\n"
        "ds_read_b128 a[196:199], v244 offset:22528\n"
        "s_waitcnt lgkmcnt(8)\n"
        "v_mfma_f32_16x16x32_bf16 v[32:35], a[56:59], a[4:7], v[32:35]\n"
        "ds_read_b128 a[200:203], v244 offset:26624\n"
        "s_waitcnt lgkmcnt(8)\n"
        "v_mfma_f32_16x16x32_bf16 v[32:35], a[224:227], a[8:11], v[32:35]\n"
        "ds_read_b128 a[204:207], v244 offset:30720\n"
        "s_waitcnt lgkmcnt(8)\n"
        "v_mfma_f32_16x16x32_bf16 v[32:35], a[232:235], a[12:15], v[32:35]\n"
        "global_load_lds_dwordx4 v254, s[60:61] offset:3072 sc1\n"
        "s_add_u32 m0, s73, 0x24000\n"
        "v_mfma_f32_16x16x32_bf16 v[36:39], a[236:239], a[12:15], v[36:39]\n"
        "s_add_u32 s60, s68, s66\n"
        "s_addc_u32 s61, s69, 0\n"
        "s_xor_b32 s66, s17, 1\n"
        "v_mfma_f32_16x16x32_bf16 v[36:39], a[228:231], a[8:11], v[36:39]\n"
        "s_lshl_b32 s66, s66, 15\n"
        "v_mfma_f32_16x16x32_bf16 v[36:39], a[60:63], a[4:7], v[36:39]\n"
        "v_cvt_pk_bf16_f32 v32, v32, v33\n"
        "v_cvt_pk_bf16_f32 v33, v34, v35\n"
        "v_mfma_f32_16x16x32_bf16 v[36:39], a[52:55], a[0:3], v[36:39]\n"
        "s_waitcnt lgkmcnt(3)\n"
        "v_mfma_f32_16x16x32_bf16 v[60:63], a[36:39], a[216:219], v[60:63]\n"
        "v_mfma_f32_16x16x32_bf16 v[60:63], a[44:47], a[220:223], v[60:63]\n"
        "v_mfma_f32_16x16x32_bf16 v[40:43], a[232:235], a[28:31], v[40:43]\n"
        "v_mfma_f32_16x16x32_bf16 v[40:43], a[224:227], a[24:27], v[40:43]\n"
        "v_mfma_f32_16x16x32_bf16 v[40:43], a[56:59], a[20:23], v[40:43]\n"
        "v_mfma_f32_16x16x32_bf16 v[40:43], a[48:51], a[16:19], v[40:43]\n"
        "s_nop 0\n"
        "v_cvt_pk_bf16_f32 v34, v36, v37\n"
        "v_cvt_pk_bf16_f32 v35, v38, v39\n"
        "v_mfma_f32_16x16x32_bf16 v[44:47], a[52:55], a[16:19], v[44:47]\n"
        "global_store_dwordx4 v254, v[32:35], s[28:29] offset:0 sc1\n"
        "s_barrier\n"
        "ds_read_b128 a[0:3], v254 offset:0\n"
        "v_mfma_f32_16x16x32_bf16 v[44:47], a[60:63], a[20:23], v[44:47]\n"
        "ds_read_b128 a[4:7], v254 offset:4096\n"
        "v_cvt_pk_bf16_f32 v40, v40, v41\n"
        "v_mfma_f32_16x16x32_bf16 v[44:47], a[228:231], a[24:27], v[44:47]\n"
        "ds_read_b128 a[8:11], v254 offset:8192\n"
        "v_cvt_pk_bf16_f32 v41, v42, v43\n"
        "v_mfma_f32_16x16x32_bf16 v[44:47], a[236:239], a[28:31], v[44:47]\n"
        "ds_read_b128 a[12:15], v254 offset:12288\n"
        "global_load_lds_dwordx4 v254, s[60:61] offset:0 sc1\n"
        "v_mfma_f32_16x16x32_bf16 v[56:59], a[40:43], a[220:223], v[56:59]\n"
        "ds_read_b128 a[220:223], v244 offset:31744\n"
        "v_mfma_f32_16x16x32_bf16 v[56:59], a[32:35], a[216:219], v[56:59]\n"
        "ds_read_b128 a[216:219], v244 offset:27648\n"
        "s_waitcnt lgkmcnt(6)\n"
        "v_mfma_f32_16x16x32_bf16 v[56:59], a[184:187], a[212:215], v[56:59]\n"
        "ds_read_b128 a[212:215], v244 offset:23552\n"
        "v_cvt_pk_bf16_f32 v42, v44, v45\n"
        "v_mfma_f32_16x16x32_bf16 v[56:59], a[176:179], a[208:211], v[56:59]\n"
        "ds_read_b128 a[208:211], v244 offset:19456\n"
        "v_cvt_pk_bf16_f32 v43, v46, v47\n"
        "v_mfma_f32_16x16x32_bf16 v[48:51], a[48:51], a[192:195], v[48:51]\n"
        "global_store_dwordx4 v254, v[40:43], s[28:29] offset:1024 sc1\n"
        "ds_read_b128 a[16:19], v254 offset:1024\n"
        "ds_read_b128 a[20:23], v254 offset:5120\n"
        "v_mfma_f32_16x16x32_bf16 v[48:51], a[56:59], a[196:199], v[48:51]\n"
        "ds_read_b128 a[24:27], v254 offset:9216\n"
        "v_mfma_f32_16x16x32_bf16 v[48:51], a[224:227], a[200:203], v[48:51]\n"
        "ds_read_b128 a[28:31], v254 offset:13312\n"
        "global_load_lds_dwordx4 v254, s[60:61] offset:1024 sc1\n"
        "v_mfma_f32_16x16x32_bf16 v[48:51], a[232:235], a[204:207], v[48:51]\n"
        "v_mfma_f32_16x16x32_bf16 v[52:55], a[236:239], a[204:207], v[52:55]\n"
        "ds_read_b128 a[204:207], v254 offset:14336\n"
        "s_waitcnt lgkmcnt(7)\n"
        "v_mfma_f32_16x16x32_bf16 v[52:55], a[228:231], a[200:203], v[52:55]\n"
        "ds_read_b128 a[200:203], v254 offset:10240\n"
        "global_load_lds_dwordx4 v254, s[60:61] offset:2048 sc1\n"
        "v_mfma_f32_16x16x32_bf16 v[52:55], a[60:63], a[196:199], v[52:55]\n"
        "ds_read_b128 a[196:199], v254 offset:6144\n"
        "v_cvt_pk_bf16_f32 v48, v48, v49\n"
        "v_mfma_f32_16x16x32_bf16 v[52:55], a[52:55], a[192:195], v[52:55]\n"
        "ds_read_b128 a[192:195], v254 offset:2048\n"
        "v_cvt_pk_bf16_f32 v49, v50, v51\n"
        "v_mfma_f32_16x16x32_bf16 v[56:59], a[232:235], a[220:223], v[56:59]\n"
        "global_load_lds_dwordx4 v254, s[60:61] offset:3072 sc1\n"
        "s_add_u32 m0, s73, 0x14000\n"
        "v_mfma_f32_16x16x32_bf16 v[56:59], a[224:227], a[216:219], v[56:59]\n"
        "s_add_u32 s60, s68, s66\n"
        "s_addc_u32 s61, s69, 0\n"
        "s_waitcnt lgkmcnt(5)\n"
        "v_mfma_f32_16x16x32_bf16 v[56:59], a[56:59], a[212:215], v[56:59]\n"
        "s_xor_b32 s66, s17, 2\n"
        "v_cvt_pk_bf16_f32 v50, v52, v53\n"
        "v_cvt_pk_bf16_f32 v51, v54, v55\n"
        "v_mfma_f32_16x16x32_bf16 v[56:59], a[48:51], a[208:211], v[56:59]\n"
        "s_lshl_b32 s66, s66, 15\n"
        "v_mfma_f32_16x16x32_bf16 v[60:63], a[52:55], a[208:211], v[60:63]\n"
        "global_store_dwordx4 v254, v[48:51], s[28:29] offset:2048 sc1\n"
        "ds_read_b128 a[208:211], v254 offset:3072\n"
        "v_mfma_f32_16x16x32_bf16 v[60:63], a[60:63], a[212:215], v[60:63]\n"
        "ds_read_b128 a[212:215], v254 offset:7168\n"
        "v_mfma_f32_16x16x32_bf16 v[60:63], a[228:231], a[216:219], v[60:63]\n"
        "ds_read_b128 a[216:219], v254 offset:11264\n"
        "v_cvt_pk_bf16_f32 v56, v56, v57\n"
        "v_mfma_f32_16x16x32_bf16 v[60:63], a[236:239], a[220:223], v[60:63]\n"
        "ds_read_b128 a[220:223], v254 offset:15360\n"
        "v_cvt_pk_bf16_f32 v57, v58, v59\n"
        "v_mfma_f32_16x16x32_bf16 v[0:3], a[96:99], a[0:3], 0\n"
        "v_mfma_f32_16x16x32_bf16 v[0:3], a[104:107], a[4:7], v[0:3]\n"
        "v_mfma_f32_16x16x32_bf16 v[0:3], a[112:115], a[8:11], v[0:3]\n"
        "v_mfma_f32_16x16x32_bf16 v[0:3], a[120:123], a[12:15], v[0:3]\n"
        "v_mfma_f32_16x16x32_bf16 v[4:7], a[124:127], a[12:15], 0\n"
        "v_mfma_f32_16x16x32_bf16 v[4:7], a[116:119], a[8:11], v[4:7]\n"
        "v_cvt_pk_bf16_f32 v58, v60, v61\n"
        "v_cvt_pk_bf16_f32 v59, v62, v63\n"
        "v_mfma_f32_16x16x32_bf16 v[4:7], a[108:111], a[4:7], v[4:7]\n"
        "global_store_dwordx4 v254, v[56:59], s[28:29] offset:3072 sc1\n"
        "v_mfma_f32_16x16x32_bf16 v[4:7], a[100:103], a[0:3], v[4:7]\n"
        "s_waitcnt lgkmcnt(0)\n"
        "v_mfma_f32_16x16x32_bf16 v[12:15], a[100:103], a[16:19], 0\n"
        "v_mfma_f32_16x16x32_bf16 v[12:15], a[108:111], a[20:23], v[12:15]\n"
        "v_mfma_f32_16x16x32_bf16 v[12:15], a[116:119], a[24:27], v[12:15]\n"
        "v_mfma_f32_16x16x32_bf16 v[12:15], a[124:127], a[28:31], v[12:15]\n"
        "s_lshl2_add_u32 s15, s76, s10\n"
        "s_mul_i32 s16, s13, 263168\n"
        "s_lshl_b32 s15, s15, 14\n"
        "v_mfma_f32_16x16x32_bf16 v[8:11], a[120:123], a[28:31], 0\n"
        "s_add_u32 s16, s16, s15\n"
        "s_add_u32 s16, s16, s73\n"
        "s_add_u32 s16, s16, 0x4000000\n"
        "v_mfma_f32_16x16x32_bf16 v[8:11], a[112:115], a[24:27], v[8:11]\n"
        "s_add_u32 s28, s38, s16\n"
        "s_addc_u32 s29, s39, 0\n"
        "v_mfma_f32_16x16x32_bf16 v[8:11], a[104:107], a[20:23], v[8:11]\n"
        "v_mfma_f32_16x16x32_bf16 v[8:11], a[96:99], a[16:19], v[8:11]\n"
        "global_load_dwordx2 v[236:237], v234, s[48:49] sc1\n"
        "v_mfma_f32_16x16x32_bf16 v[16:19], a[96:99], a[192:195], 0\n"
        "s_barrier\n"
        "v_mfma_f32_16x16x32_bf16 v[16:19], a[104:107], a[196:199], v[16:19]\n"
        "ds_read_b128 a[0:3], v254 offset:32768\n"
        "global_load_lds_dwordx4 v254, s[60:61] offset:0 sc1\n"
        "v_mfma_f32_16x16x32_bf16 v[16:19], a[112:115], a[200:203], v[16:19]\n"
        "ds_read_b128 a[4:7], v254 offset:36864\n"
        "v_mfma_f32_16x16x32_bf16 v[16:19], a[120:123], a[204:207], v[16:19]\n"
        "ds_read_b128 a[8:11], v254 offset:40960\n"
        "v_mfma_f32_16x16x32_bf16 v[20:23], a[124:127], a[204:207], 0\n"
        "ds_read_b128 a[12:15], v254 offset:45056\n"
        "global_load_lds_dwordx4 v254, s[60:61] offset:1024 sc1\n"
        "v_mfma_f32_16x16x32_bf16 v[20:23], a[116:119], a[200:203], v[20:23]\n"
        "ds_read_b128 a[16:19], v254 offset:33792\n"
        "v_mfma_f32_16x16x32_bf16 v[20:23], a[108:111], a[196:199], v[20:23]\n"
        "ds_read_b128 a[20:23], v254 offset:37888\n"
        "v_mfma_f32_16x16x32_bf16 v[20:23], a[100:103], a[192:195], v[20:23]\n"
        "ds_read_b128 a[24:27], v254 offset:41984\n"
        "global_load_lds_dwordx4 v254, s[60:61] offset:2048 sc1\n"
        "v_mfma_f32_16x16x32_bf16 v[28:31], a[100:103], a[208:211], 0\n"
        "ds_read_b128 a[28:31], v254 offset:46080\n"
        "v_mfma_f32_16x16x32_bf16 v[28:31], a[108:111], a[212:215], v[28:31]\n"
        "ds_read_b128 a[192:195], v254 offset:34816\n"
        "v_mfma_f32_16x16x32_bf16 v[28:31], a[116:119], a[216:219], v[28:31]\n"
        "ds_read_b128 a[196:199], v254 offset:38912\n"
        "global_load_lds_dwordx4 v254, s[60:61] offset:3072 sc1\n"
        "s_add_u32 m0, s73, 0x0\n"
        "v_mfma_f32_16x16x32_bf16 v[28:31], a[124:127], a[220:223], v[28:31]\n"
        "ds_read_b128 a[200:203], v254 offset:43008\n"
        "s_add_u32 s60, s68, s66\n"
        "s_addc_u32 s61, s69, 0\n"
        "v_mfma_f32_16x16x32_bf16 v[24:27], a[120:123], a[220:223], 0\n"
        "ds_read_b128 a[204:207], v254 offset:47104\n"
        "s_xor_b32 s66, s17, 3\n"
        "s_lshl_b32 s66, s66, 15\n"
        "v_mfma_f32_16x16x32_bf16 v[24:27], a[112:115], a[216:219], v[24:27]\n"
        "ds_read_b128 a[216:219], v254 offset:44032\n"
        "s_waitcnt lgkmcnt(8)\n"
        "v_mfma_f32_16x16x32_bf16 v[24:27], a[104:107], a[212:215], v[24:27]\n"
        "ds_read_b128 a[212:215], v254 offset:39936\n"
        "v_mfma_f32_16x16x32_bf16 v[24:27], a[96:99], a[208:211], v[24:27]\n"
        "ds_read_b128 a[208:211], v254 offset:35840\n"
        "v_mfma_f32_16x16x32_bf16 v[0:3], a[128:131], a[0:3], v[0:3]\n"
        "ds_read_b128 a[220:223], v254 offset:48128\n"
        "v_mfma_f32_16x16x32_bf16 v[0:3], a[136:139], a[4:7], v[0:3]\n"
        "v_mfma_f32_16x16x32_bf16 v[0:3], a[144:147], a[8:11], v[0:3]\n"
        "v_mfma_f32_16x16x32_bf16 v[0:3], a[152:155], a[12:15], v[0:3]\n"
        "v_mfma_f32_16x16x32_bf16 v[4:7], a[156:159], a[12:15], v[4:7]\n"
        "v_mfma_f32_16x16x32_bf16 v[4:7], a[148:151], a[8:11], v[4:7]\n"
        "s_waitcnt lgkmcnt(3)\n"
        "v_mfma_f32_16x16x32_bf16 v[4:7], a[140:143], a[4:7], v[4:7]\n"
        "v_mfma_f32_16x16x32_bf16 v[4:7], a[132:135], a[0:3], v[4:7]\n"
        "v_mfma_f32_16x16x32_bf16 v[12:15], a[132:135], a[16:19], v[12:15]\n"
        "v_mfma_f32_16x16x32_bf16 v[12:15], a[140:143], a[20:23], v[12:15]\n"
        "v_mfma_f32_16x16x32_bf16 v[12:15], a[148:151], a[24:27], v[12:15]\n"
        "v_mfma_f32_16x16x32_bf16 v[12:15], a[156:159], a[28:31], v[12:15]\n"
        "v_mfma_f32_16x16x32_bf16 v[8:11], a[152:155], a[28:31], v[8:11]\n"
        "v_mfma_f32_16x16x32_bf16 v[8:11], a[144:147], a[24:27], v[8:11]\n"
        "v_mfma_f32_16x16x32_bf16 v[8:11], a[136:139], a[20:23], v[8:11]\n"
        "v_mfma_f32_16x16x32_bf16 v[8:11], a[128:131], a[16:19], v[8:11]\n"
        "v_mfma_f32_16x16x32_bf16 v[16:19], a[128:131], a[192:195], v[16:19]\n"
        "s_barrier\n"
        "v_mfma_f32_16x16x32_bf16 v[16:19], a[136:139], a[196:199], v[16:19]\n"
        "ds_read_b128 a[0:3], v254 offset:16384\n"
        "global_load_lds_dwordx4 v254, s[60:61] offset:0 sc1\n"
        "v_mfma_f32_16x16x32_bf16 v[16:19], a[144:147], a[200:203], v[16:19]\n"
        "ds_read_b128 a[4:7], v254 offset:20480\n"
        "v_mfma_f32_16x16x32_bf16 v[16:19], a[152:155], a[204:207], v[16:19]\n"
        "ds_read_b128 a[8:11], v254 offset:24576\n"
        "v_mfma_f32_16x16x32_bf16 v[20:23], a[156:159], a[204:207], v[20:23]\n"
        "ds_read_b128 a[12:15], v254 offset:28672\n"
        "global_load_lds_dwordx4 v254, s[60:61] offset:1024 sc1\n"
        "v_mfma_f32_16x16x32_bf16 v[20:23], a[148:151], a[200:203], v[20:23]\n"
        "ds_read_b128 a[16:19], v254 offset:17408\n"
        "s_waitcnt lgkmcnt(5)\n"
        "v_mfma_f32_16x16x32_bf16 v[20:23], a[140:143], a[196:199], v[20:23]\n"
        "ds_read_b128 a[20:23], v254 offset:21504\n"
        "v_mfma_f32_16x16x32_bf16 v[20:23], a[132:135], a[192:195], v[20:23]\n"
        "ds_read_b128 a[24:27], v254 offset:25600\n"
        "global_load_lds_dwordx4 v254, s[60:61] offset:2048 sc1\n"
        "v_mfma_f32_16x16x32_bf16 v[28:31], a[132:135], a[208:211], v[28:31]\n"
        "ds_read_b128 a[28:31], v254 offset:29696\n"
        "v_mfma_f32_16x16x32_bf16 v[28:31], a[140:143], a[212:215], v[28:31]\n"
        "ds_read_b128 a[192:195], v254 offset:18432\n"
        "v_mfma_f32_16x16x32_bf16 v[28:31], a[148:151], a[216:219], v[28:31]\n"
        "ds_read_b128 a[196:199], v254 offset:22528\n"
        "global_load_lds_dwordx4 v254, s[60:61] offset:3072 sc1\n"
        "s_add_u32 m0, s73, 0x4000\n"
        "v_mfma_f32_16x16x32_bf16 v[28:31], a[156:159], a[220:223], v[28:31]\n"
        "ds_read_b128 a[200:203], v254 offset:26624\n"
        "s_add_u32 s60, s68, s66\n"
        "s_addc_u32 s61, s69, 0\n"
        "v_mfma_f32_16x16x32_bf16 v[24:27], a[152:155], a[220:223], v[24:27]\n"
        "ds_read_b128 a[204:207], v254 offset:30720\n"
        "s_xor_b32 s66, s17, 4\n"
        "s_lshl_b32 s66, s66, 15\n"
        "v_mfma_f32_16x16x32_bf16 v[24:27], a[144:147], a[216:219], v[24:27]\n"
        "ds_read_b128 a[216:219], v254 offset:27648\n"
        "s_waitcnt lgkmcnt(8)\n"
        "v_mfma_f32_16x16x32_bf16 v[24:27], a[136:139], a[212:215], v[24:27]\n"
        "ds_read_b128 a[212:215], v254 offset:23552\n"
        "v_mfma_f32_16x16x32_bf16 v[24:27], a[128:131], a[208:211], v[24:27]\n"
        "ds_read_b128 a[208:211], v254 offset:19456\n"
        "v_mfma_f32_16x16x32_bf16 v[0:3], v[160:163], a[0:3], v[0:3]\n"
        "ds_read_b128 a[220:223], v254 offset:31744\n"
        "v_mfma_f32_16x16x32_bf16 v[0:3], v[168:171], a[4:7], v[0:3]\n"
        "v_mfma_f32_16x16x32_bf16 v[0:3], v[176:179], a[8:11], v[0:3]\n"
        "v_mfma_f32_16x16x32_bf16 v[0:3], v[184:187], a[12:15], v[0:3]\n"
        "v_mfma_f32_16x16x32_bf16 v[4:7], v[188:191], a[12:15], v[4:7]\n"
        "v_mfma_f32_16x16x32_bf16 v[4:7], v[180:183], a[8:11], v[4:7]\n"
        "s_waitcnt lgkmcnt(3)\n"
        "v_mfma_f32_16x16x32_bf16 v[4:7], v[172:175], a[4:7], v[4:7]\n"
        "v_mfma_f32_16x16x32_bf16 v[4:7], v[164:167], a[0:3], v[4:7]\n"
        "v_mfma_f32_16x16x32_bf16 v[12:15], v[164:167], a[16:19], v[12:15]\n"
        "v_mfma_f32_16x16x32_bf16 v[12:15], v[172:175], a[20:23], v[12:15]\n"
        "v_mfma_f32_16x16x32_bf16 v[12:15], v[180:183], a[24:27], v[12:15]\n"
        "v_mfma_f32_16x16x32_bf16 v[12:15], v[188:191], a[28:31], v[12:15]\n"
        "v_mfma_f32_16x16x32_bf16 v[8:11], v[184:187], a[28:31], v[8:11]\n"
        "v_mfma_f32_16x16x32_bf16 v[8:11], v[176:179], a[24:27], v[8:11]\n"
        "v_mfma_f32_16x16x32_bf16 v[8:11], v[168:171], a[20:23], v[8:11]\n"
        "v_mfma_f32_16x16x32_bf16 v[8:11], v[160:163], a[16:19], v[8:11]\n"
        "s_waitcnt vmcnt(33)\n"
        "v_mfma_f32_16x16x32_bf16 v[16:19], v[160:163], a[192:195], v[16:19]\n"
        "s_barrier\n"
        "v_mfma_f32_16x16x32_bf16 v[16:19], v[168:171], a[196:199], v[16:19]\n"
        "ds_write_b32 v225, v224\n"
        "v_mfma_f32_16x16x32_bf16 v[16:19], v[176:179], a[200:203], v[16:19]\n"
        "ds_read_b128 a[0:3], v244 offset:32768\n"
        "v_mfma_f32_16x16x32_bf16 v[16:19], v[184:187], a[204:207], v[16:19]\n"
        "ds_read_b128 a[4:7], v244 offset:36864\n"
        "v_mfma_f32_16x16x32_bf16 v[20:23], v[188:191], a[204:207], v[20:23]\n"
        "ds_read_b128 a[8:11], v244 offset:40960\n"
        "s_waitcnt lgkmcnt(4)\n"
        "v_mfma_f32_16x16x32_bf16 v[20:23], v[180:183], a[200:203], v[20:23]\n"
        "ds_read_b128 a[12:15], v244 offset:45056\n"
        "v_mfma_f32_16x16x32_bf16 v[20:23], v[172:175], a[196:199], v[20:23]\n"
        "ds_read_b128 a[16:19], v244 offset:33792\n"
        "v_mfma_f32_16x16x32_bf16 v[20:23], v[164:167], a[192:195], v[20:23]\n"
        "ds_read_b128 a[20:23], v244 offset:37888\n"
        "v_mfma_f32_16x16x32_bf16 v[28:31], v[164:167], a[208:211], v[28:31]\n"
        "ds_read_b128 a[24:27], v244 offset:41984\n"
        "v_mfma_f32_16x16x32_bf16 v[28:31], v[172:175], a[212:215], v[28:31]\n"
        "ds_read_b128 a[28:31], v244 offset:46080\n"
        "s_lshl2_add_u32 s14, s13, s10\n"
        "s_lshl2_add_u32 s14, s14, s40\n"
        "v_mfma_f32_16x16x32_bf16 v[28:31], v[180:183], a[216:219], v[28:31]\n"
        "ds_read_b128 a[192:195], v244 offset:34816\n"
        "s_add_u32 s14, s14, 3072\n"
        "v_lshrrev_b32 v154, 1, v254\n"
        "v_mfma_f32_16x16x32_bf16 v[28:31], v[188:191], a[220:223], v[28:31]\n"
        "ds_read_b128 a[196:199], v244 offset:38912\n"
        "s_lshl_b32 s14, s14, 7\n"
        "v_add_u32 v154, s14, v154\n"
        "v_mfma_f32_16x16x32_bf16 v[24:27], v[184:187], a[220:223], v[24:27]\n"
        "ds_read_b128 a[200:203], v244 offset:43008\n"
        "s_waitcnt lgkmcnt(8)\n"
        "v_mfma_f32_16x16x32_bf16 v[24:27], v[176:179], a[216:219], v[24:27]\n"
        "ds_read_b128 a[204:207], v244 offset:47104\n"
        "v_mfma_f32_16x16x32_bf16 v[24:27], v[168:171], a[212:215], v[24:27]\n"
        "ds_read_b128 a[212:215], v244 offset:39936\n"
        "v_mfma_f32_16x16x32_bf16 v[24:27], v[160:163], a[208:211], v[24:27]\n"
        "ds_read_b128 a[208:211], v244 offset:35840\n"
        "s_waitcnt lgkmcnt(8)\n"
        "v_mfma_f32_16x16x32_bf16 v[0:3], v[192:195], a[0:3], v[0:3]\n"
        "ds_read_b128 a[216:219], v244 offset:44032\n"
        "v_mfma_f32_16x16x32_bf16 v[0:3], v[200:203], a[4:7], v[0:3]\n"
        "ds_read_b128 a[220:223], v244 offset:48128\n"
        "v_mfma_f32_16x16x32_bf16 v[0:3], v[208:211], a[8:11], v[0:3]\n"
        "v_mfma_f32_16x16x32_bf16 v[0:3], v[216:219], a[12:15], v[0:3]\n"
        "v_mfma_f32_16x16x32_bf16 v[4:7], v[220:223], a[12:15], v[4:7]\n"
        "v_mfma_f32_16x16x32_bf16 v[4:7], v[212:215], a[8:11], v[4:7]\n"
        "v_mfma_f32_16x16x32_bf16 v[4:7], v[204:207], a[4:7], v[4:7]\n"
        "s_waitcnt lgkmcnt(3)\n"
        "v_mfma_f32_16x16x32_bf16 v[4:7], v[196:199], a[0:3], v[4:7]\n"
        "v_mfma_f32_16x16x32_bf16 v[12:15], v[196:199], a[16:19], v[12:15]\n"
        "v_mfma_f32_16x16x32_bf16 v[12:15], v[204:207], a[20:23], v[12:15]\n"
        "v_mfma_f32_16x16x32_bf16 v[12:15], v[212:215], a[24:27], v[12:15]\n"
        "v_mfma_f32_16x16x32_bf16 v[12:15], v[220:223], a[28:31], v[12:15]\n"
        "v_mfma_f32_16x16x32_bf16 v[8:11], v[216:219], a[28:31], v[8:11]\n"
        "v_mfma_f32_16x16x32_bf16 v[8:11], v[208:211], a[24:27], v[8:11]\n"
        "s_waitcnt vmcnt(8) lgkmcnt(0)\n"
        "v_cmp_ne_u64 vcc, v[236:237], v[250:251]\n"
        "v_mfma_f32_16x16x32_bf16 v[8:11], v[200:203], a[20:23], v[8:11]\n"
        "s_cbranch_vccnz LPZZ_%=\n"
        "LBZZ_%=:\n"
        "v_mfma_f32_16x16x32_bf16 v[8:11], v[192:195], a[16:19], v[8:11]\n"
        "v_mfma_f32_16x16x32_bf16 v[16:19], v[192:195], a[192:195], v[16:19]\n"
        "s_barrier\n"
        "v_mfma_f32_16x16x32_bf16 v[16:19], v[200:203], a[196:199], v[16:19]\n"
        "ds_read_b128 a[0:3], v254 offset:49152\n"
        "global_load_lds_dwordx4 v148, s[58:59] offset:0 sc1\n"
        "s_add_u32 m0, s73, 0x18000\n"
        "v_mfma_f32_16x16x32_bf16 v[16:19], v[208:211], a[200:203], v[16:19]\n"
        "ds_read_b128 a[4:7], v254 offset:53248\n"
        "s_add_u32 s58, s58, 0x8000\n"
        "s_addc_u32 s59, s59, 0\n"
        "v_mfma_f32_16x16x32_bf16 v[16:19], v[216:219], a[204:207], v[16:19]\n"
        "ds_read_b128 a[8:11], v254 offset:57344\n"
        "v_mfma_f32_16x16x32_bf16 v[20:23], v[220:223], a[204:207], v[20:23]\n"
        "ds_read_b128 a[12:15], v254 offset:61440\n"
        "global_load_dwordx4 v[96:99], v151, s[64:65]\n"
        "v_mfma_f32_16x16x32_bf16 v[20:23], v[212:215], a[200:203], v[20:23]\n"
        "ds_read_b128 a[16:19], v254 offset:50176\n"
        "s_add_u32 s64, s64, 0x8000\n"
        "s_addc_u32 s65, s65, 0\n"
        "v_mfma_f32_16x16x32_bf16 v[20:23], v[204:207], a[196:199], v[20:23]\n"
        "ds_read_b128 a[20:23], v254 offset:54272\n"
        "v_mfma_f32_16x16x32_bf16 v[20:23], v[196:199], a[192:195], v[20:23]\n"
        "ds_read_b128 a[24:27], v254 offset:58368\n"
        "v_mfma_f32_16x16x32_bf16 v[28:31], v[196:199], a[208:211], v[28:31]\n"
        "ds_read_b128 a[28:31], v254 offset:62464\n"
        "v_mfma_f32_16x16x32_bf16 v[28:31], v[204:207], a[212:215], v[28:31]\n"
        "ds_read_b128 a[192:195], v254 offset:51200\n"
        "v_mfma_f32_16x16x32_bf16 v[28:31], v[212:215], a[216:219], v[28:31]\n"
        "ds_read_b128 a[196:199], v254 offset:55296\n"
        "v_mfma_f32_16x16x32_bf16 v[28:31], v[220:223], a[220:223], v[28:31]\n"
        "ds_read_b128 a[200:203], v254 offset:59392\n"
        "v_mfma_f32_16x16x32_bf16 v[24:27], v[216:219], a[220:223], v[24:27]\n"
        "ds_read_b128 a[204:207], v254 offset:63488\n"
        "s_waitcnt lgkmcnt(8)\n"
        "v_mfma_f32_16x16x32_bf16 v[24:27], v[208:211], a[216:219], v[24:27]\n"
        "ds_read_b128 a[216:219], v254 offset:60416\n"
        "v_mfma_f32_16x16x32_bf16 v[24:27], v[200:203], a[212:215], v[24:27]\n"
        "ds_read_b128 a[212:215], v254 offset:56320\n"
        "v_mfma_f32_16x16x32_bf16 v[24:27], v[192:195], a[208:211], v[24:27]\n"
        "ds_read_b128 a[208:211], v254 offset:52224\n"
        "s_and_b32 s14, s14, 0xfffff9ff\n"
        "v_bfe_u32 v155, v254, 4, 2\n"
        "v_mfma_f32_16x16x32_bf16 v[0:3], v[112:115], a[0:3], v[0:3]\n"
        "ds_read_b128 a[220:223], v254 offset:64512\n"
        "v_lshl_add_u32 v155, v155, 9, s14\n"
        "v_mfma_f32_16x16x32_bf16 v[0:3], v[120:123], a[4:7], v[0:3]\n"
        "v_mfma_f32_16x16x32_bf16 v[0:3], a[64:67], a[8:11], v[0:3]\n"
        "v_mfma_f32_16x16x32_bf16 v[0:3], a[72:75], a[12:15], v[0:3]\n"
        "v_mfma_f32_16x16x32_bf16 v[4:7], a[76:79], a[12:15], v[4:7]\n"
        "s_waitcnt lgkmcnt(4)\n"
        "v_mfma_f32_16x16x32_bf16 v[4:7], a[68:71], a[8:11], v[4:7]\n"
        "v_mfma_f32_16x16x32_bf16 v[4:7], v[124:127], a[4:7], v[4:7]\n"
        "v_mfma_f32_16x16x32_bf16 v[4:7], v[116:119], a[0:3], v[4:7]\n"
        "v_mfma_f32_16x16x32_bf16 v[12:15], v[116:119], a[16:19], v[12:15]\n"
        "v_mfma_f32_16x16x32_bf16 v[12:15], v[124:127], a[20:23], v[12:15]\n"
        "v_mfma_f32_16x16x32_bf16 v[12:15], a[68:71], a[24:27], v[12:15]\n"
        "v_mfma_f32_16x16x32_bf16 v[12:15], a[76:79], a[28:31], v[12:15]\n"
        "v_mfma_f32_16x16x32_bf16 v[8:11], a[72:75], a[28:31], v[8:11]\n"
        "v_mfma_f32_16x16x32_bf16 v[8:11], a[64:67], a[24:27], v[8:11]\n"
        "v_mfma_f32_16x16x32_bf16 v[8:11], v[120:123], a[20:23], v[8:11]\n"
        "v_mfma_f32_16x16x32_bf16 v[8:11], v[112:115], a[16:19], v[8:11]\n"
        "v_mfma_f32_16x16x32_bf16 v[16:19], v[112:115], a[192:195], v[16:19]\n"
        "s_barrier\n"
        "v_mfma_f32_16x16x32_bf16 v[16:19], v[120:123], a[196:199], v[16:19]\n"
        "ds_read_b128 a[0:3], v245 offset:0\n"
        "global_load_lds_dwordx4 v254, s[60:61] offset:0 sc1\n"
        "v_mfma_f32_16x16x32_bf16 v[16:19], a[64:67], a[200:203], v[16:19]\n"
        "ds_read_b128 a[4:7], v245 offset:4096\n"
        "v_mfma_f32_16x16x32_bf16 v[16:19], a[72:75], a[204:207], v[16:19]\n"
        "ds_read_b128 a[8:11], v245 offset:8192\n"
        "v_mfma_f32_16x16x32_bf16 v[20:23], a[76:79], a[204:207], v[20:23]\n"
        "ds_read_b128 a[12:15], v245 offset:12288\n"
        "global_load_lds_dwordx4 v254, s[60:61] offset:1024 sc1\n"
        "v_mfma_f32_16x16x32_bf16 v[20:23], a[68:71], a[200:203], v[20:23]\n"
        "ds_read_b128 a[16:19], v245 offset:1024\n"
        "s_waitcnt lgkmcnt(5)\n"
        "v_mfma_f32_16x16x32_bf16 v[20:23], v[124:127], a[196:199], v[20:23]\n"
        "ds_read_b128 a[20:23], v245 offset:5120\n"
        "v_mfma_f32_16x16x32_bf16 v[20:23], v[116:119], a[192:195], v[20:23]\n"
        "ds_read_b128 a[24:27], v245 offset:9216\n"
        "global_load_lds_dwordx4 v254, s[60:61] offset:2048 sc1\n"
        "v_mfma_f32_16x16x32_bf16 v[28:31], v[116:119], a[208:211], v[28:31]\n"
        "ds_read_b128 a[28:31], v245 offset:13312\n"
        "v_mfma_f32_16x16x32_bf16 v[28:31], v[124:127], a[212:215], v[28:31]\n"
        "ds_read_b128 a[192:195], v245 offset:2048\n"
        "v_mfma_f32_16x16x32_bf16 v[28:31], a[68:71], a[216:219], v[28:31]\n"
        "ds_read_b128 a[196:199], v245 offset:6144\n"
        "global_load_lds_dwordx4 v254, s[60:61] offset:3072 sc1\n"
        "s_add_u32 m0, s73, 0x4400\n"
        "v_mfma_f32_16x16x32_bf16 v[28:31], a[76:79], a[220:223], v[28:31]\n"
        "ds_read_b128 a[200:203], v245 offset:10240\n"
        "s_add_u32 s60, s68, s66\n"
        "s_addc_u32 s61, s69, 0\n"
        "v_mfma_f32_16x16x32_bf16 v[24:27], a[72:75], a[220:223], v[24:27]\n"
        "ds_read_b128 a[204:207], v245 offset:14336\n"
        "s_xor_b32 s66, s17, 5\n"
        "s_lshl_b32 s66, s66, 15\n"
        "v_mfma_f32_16x16x32_bf16 v[24:27], a[64:67], a[216:219], v[24:27]\n"
        "ds_read_b128 a[216:219], v245 offset:11264\n"
        "global_load_lds_dwordx4 v148, s[58:59] offset:0 sc1\n"
        "s_add_u32 m0, s73, 0xc000\n"
        "v_mfma_f32_16x16x32_bf16 v[24:27], v[120:123], a[212:215], v[24:27]\n"
        "ds_read_b128 a[212:215], v245 offset:7168\n"
        "s_add_u32 s58, s58, 0x8000\n"
        "s_addc_u32 s59, s59, 0\n"
        "v_mfma_f32_16x16x32_bf16 v[24:27], v[112:115], a[208:211], v[24:27]\n"
        "ds_read_b128 a[208:211], v245 offset:3072\n"
        "s_waitcnt lgkmcnt(8)\n"
        "v_mfma_f32_16x16x32_bf16 v[0:3], a[80:83], a[0:3], v[0:3]\n"
        "ds_read_b128 a[220:223], v245 offset:15360\n"
        "global_load_dwordx4 v[100:103], v151, s[64:65]\n"
        "v_mfma_f32_16x16x32_bf16 v[0:3], a[88:91], a[4:7], v[0:3]\n"
        "ds_read_b32 v224, v159 offset:0\n"
        "s_add_u32 s64, s64, 0x8000\n"
        "s_addc_u32 s65, s65, 0\n"
        "v_mfma_f32_16x16x32_bf16 v[0:3], a[160:163], a[8:11], v[0:3]\n"
        "ds_read_b32 v225, v159 offset:64\n"
        "v_mfma_f32_16x16x32_bf16 v[0:3], a[168:171], a[12:15], v[0:3]\n"
        "ds_read_b32 v226, v159 offset:128\n"
        "v_mfma_f32_16x16x32_bf16 v[4:7], a[172:175], a[12:15], v[4:7]\n"
        "ds_read_b32 v227, v159 offset:192\n"
        "v_mfma_f32_16x16x32_bf16 v[4:7], a[164:167], a[8:11], v[4:7]\n"
        "v_mfma_f32_16x16x32_bf16 v[4:7], a[92:95], a[4:7], v[4:7]\n"
        "v_mfma_f32_16x16x32_bf16 v[4:7], a[84:87], a[0:3], v[4:7]\n"
        "s_waitcnt lgkmcnt(5)\n"
        "v_mfma_f32_16x16x32_bf16 v[12:15], a[84:87], a[16:19], v[12:15]\n"
        "v_mfma_f32_16x16x32_bf16 v[12:15], a[92:95], a[20:23], v[12:15]\n"
        "s_waitcnt lgkmcnt(3)\n"
        "v_sub_f32 v228, 1.0, v224\n"
        "v_cvt_pk_bf16_f32 v228, 0, v228\n"
        "v_mfma_f32_16x16x32_bf16 v[12:15], a[164:167], a[24:27], v[12:15]\n"
        "s_waitcnt lgkmcnt(2)\n"
        "v_sub_f32 v229, 1.0, v225\n"
        "v_cvt_pk_bf16_f32 v229, 0, v229\n"
        "v_mfma_f32_16x16x32_bf16 v[12:15], a[172:175], a[28:31], v[12:15]\n"
        "s_waitcnt lgkmcnt(1)\n"
        "v_sub_f32 v230, 1.0, v226\n"
        "v_cvt_pk_bf16_f32 v230, 0, v230\n"
        "v_mfma_f32_16x16x32_bf16 v[8:11], a[168:171], a[28:31], v[8:11]\n"
        "s_waitcnt lgkmcnt(0)\n"
        "v_sub_f32 v231, 1.0, v227\n"
        "v_cvt_pk_bf16_f32 v231, 0, v231\n"
        "v_mfma_f32_16x16x32_bf16 v[8:11], a[160:163], a[24:27], v[8:11]\n"
        "v_mfma_f32_16x16x32_bf16 v[8:11], a[88:91], a[20:23], v[8:11]\n"
        "v_mfma_f32_16x16x32_bf16 v[8:11], a[80:83], a[16:19], v[8:11]\n"
        "v_mfma_f32_16x16x32_bf16 v[16:19], a[80:83], a[192:195], v[16:19]\n"
        "s_barrier\n"
        "v_mfma_f32_16x16x32_bf16 v[16:19], a[88:91], a[196:199], v[16:19]\n"
        "ds_read_b128 a[0:3], v244 offset:49152\n"
        "global_load_lds_dwordx4 v254, s[60:61] offset:0 sc1\n"
        "s_mul_i32 s16, s13, 263168\n"
        "v_mfma_f32_16x16x32_bf16 v[16:19], a[160:163], a[200:203], v[16:19]\n"
        "ds_read_b128 a[4:7], v244 offset:53248\n"
        "s_lshl2_add_u32 s15, s10, s74\n"
        "s_lshl_b32 s15, s15, 14\n"
        "v_mfma_f32_16x16x32_bf16 v[16:19], a[168:171], a[204:207], v[16:19]\n"
        "ds_read_b128 a[8:11], v244 offset:57344\n"
        "s_add_u32 s16, s16, s15\n"
        "s_add_u32 s16, s16, s73\n"
        "v_mfma_f32_16x16x32_bf16 v[20:23], a[172:175], a[204:207], v[20:23]\n"
        "ds_read_b128 a[12:15], v244 offset:61440\n"
        "global_load_lds_dwordx4 v254, s[60:61] offset:1024 sc1\n"
        "s_add_u32 s16, s16, 0x4000000\n"
        "v_mfma_f32_16x16x32_bf16 v[20:23], a[164:167], a[200:203], v[20:23]\n"
        "ds_read_b128 a[16:19], v244 offset:50176\n"
        "s_add_u32 s30, s38, s16\n"
        "s_addc_u32 s31, s39, 0\n"
        "v_mfma_f32_16x16x32_bf16 v[20:23], a[92:95], a[196:199], v[20:23]\n"
        "ds_read_b128 a[20:23], v244 offset:54272\n"
        "s_mul_i32 s16, s13, 263168\n"
        "s_lshl2_add_u32 s15, s10, s75\n"
        "v_mfma_f32_16x16x32_bf16 v[20:23], a[84:87], a[192:195], v[20:23]\n"
        "ds_read_b128 a[24:27], v244 offset:58368\n"
        "global_load_lds_dwordx4 v254, s[60:61] offset:2048 sc1\n"
        "s_lshl_b32 s15, s15, 14\n"
        "v_mfma_f32_16x16x32_bf16 v[28:31], a[84:87], a[208:211], v[28:31]\n"
        "ds_read_b128 a[28:31], v244 offset:62464\n"
        "s_add_u32 s16, s16, s15\n"
        "s_add_u32 s16, s16, s73\n"
        "LHB2_%=:\n"
        "s_cbranch_execz LHRB_%=\n"
        "v_mfma_f32_16x16x32_bf16 v[28:31], a[92:95], a[212:215], v[28:31]\n"
        "ds_read_b128 a[192:195], v244 offset:51200\n"
        "s_add_u32 s16, s16, 0x4000000\n"
        "s_lshl2_add_u32 s15, s10, s76\n"
        "v_mfma_f32_16x16x32_bf16 v[28:31], a[164:167], a[216:219], v[28:31]\n"
        "ds_read_b128 a[196:199], v244 offset:55296\n"
        "global_load_lds_dwordx4 v254, s[60:61] offset:3072 sc1\n"
        "s_add_u32 m0, s73, 0x4800\n"
        "v_mfma_f32_16x16x32_bf16 v[28:31], a[172:175], a[220:223], v[28:31]\n"
        "ds_read_b128 a[200:203], v244 offset:59392\n"
        "s_add_u32 s60, s68, s66\n"
        "s_addc_u32 s61, s69, 0\n"
        "v_mfma_f32_16x16x32_bf16 v[24:27], a[168:171], a[220:223], v[24:27]\n"
        "ds_read_b128 a[204:207], v244 offset:63488\n"
        "s_xor_b32 s66, s17, 6\n"
        "s_lshl_b32 s66, s66, 15\n"
        "v_mfma_f32_16x16x32_bf16 v[24:27], a[160:163], a[216:219], v[24:27]\n"
        "ds_read_b128 a[216:219], v244 offset:60416\n"
        "global_load_lds_dwordx4 v148, s[58:59] offset:0 sc1\n"
        "s_add_u32 m0, s73, 0x20000\n"
        "v_mfma_f32_16x16x32_bf16 v[24:27], a[88:91], a[212:215], v[24:27]\n"
        "ds_read_b128 a[212:215], v244 offset:56320\n"
        "s_add_u32 s58, s58, 0x8000\n"
        "s_addc_u32 s59, s59, 0\n"
        "v_mfma_f32_16x16x32_bf16 v[24:27], a[80:83], a[208:211], v[24:27]\n"
        "ds_read_b128 a[208:211], v244 offset:52224\n"
        "s_lshl_b32 s15, s15, 14\n"
        "s_waitcnt lgkmcnt(8)\n"
        "v_mfma_f32_16x16x32_bf16 v[0:3], a[176:179], a[0:3], v[0:3]\n"
        "ds_read_b128 a[220:223], v244 offset:64512\n"
        "global_load_dwordx4 v[104:107], v151, s[64:65]\n"
        "v_mfma_f32_16x16x32_bf16 v[0:3], a[184:187], a[4:7], v[0:3]\n"
        "s_add_u32 s64, s64, 0x8000\n"
        "s_addc_u32 s65, s65, 0\n"
        "v_mfma_f32_16x16x32_bf16 v[0:3], a[32:35], a[8:11], v[0:3]\n"
        "v_mfma_f32_16x16x32_bf16 v[0:3], a[40:43], a[12:15], v[0:3]\n"
        "v_mfma_f32_16x16x32_bf16 v[4:7], a[44:47], a[12:15], v[4:7]\n"
        "v_mfma_f32_16x16x32_bf16 v[4:7], a[36:39], a[8:11], v[4:7]\n"
        "v_mfma_f32_16x16x32_bf16 v[4:7], a[188:191], a[4:7], v[4:7]\n"
        "v_mfma_f32_16x16x32_bf16 v[4:7], a[180:183], a[0:3], v[4:7]\n"
        "s_waitcnt lgkmcnt(1)\n"
        "v_mfma_f32_16x16x32_bf16 v[12:15], a[180:183], a[16:19], v[12:15]\n"
        "v_mfma_f32_16x16x32_bf16 v[12:15], a[188:191], a[20:23], v[12:15]\n"
        "v_mfma_f32_16x16x32_bf16 v[12:15], a[36:39], a[24:27], v[12:15]\n"
        "v_mfma_f32_16x16x32_bf16 v[12:15], a[44:47], a[28:31], v[12:15]\n"
        "v_mfma_f32_16x16x32_bf16 v[8:11], a[40:43], a[28:31], v[8:11]\n"
        "v_mfma_f32_16x16x32_bf16 v[8:11], a[32:35], a[24:27], v[8:11]\n"
        "v_mfma_f32_16x16x32_bf16 v[8:11], a[184:187], a[20:23], v[8:11]\n"
        "v_mfma_f32_16x16x32_bf16 v[8:11], a[176:179], a[16:19], v[8:11]\n"
        "v_mfma_f32_16x16x32_bf16 v[16:19], a[176:179], a[192:195], v[16:19]\n"
        "s_barrier\n"
        "v_mfma_f32_16x16x32_bf16 v[16:19], a[184:187], a[196:199], v[16:19]\n"
        "ds_read_b128 a[0:3], v244 offset:0\n"
        "global_load_lds_dwordx4 v254, s[60:61] offset:0 sc1\n"
        "v_mfma_f32_16x16x32_bf16 v[16:19], a[32:35], a[200:203], v[16:19]\n"
        "ds_read_b128 a[4:7], v244 offset:4096\n"
        "s_waitcnt vmcnt(13) lgkmcnt(2)\n"
        "v_mfma_f32_16x16x32_bf16 v[16:19], a[40:43], a[204:207], v[16:19]\n"
        "ds_read_b128 a[8:11], v244 offset:8192\n"
        "v_mfma_f32_16x16x32_bf16 v[20:23], a[44:47], a[204:207], v[20:23]\n"
        "ds_read_b128 a[12:15], v244 offset:12288\n"
        "global_load_lds_dwordx4 v254, s[60:61] offset:1024 sc1\n"
        "v_mfma_f32_16x16x32_bf16 v[20:23], a[36:39], a[200:203], v[20:23]\n"
        "ds_read_b128 a[28:31], v244 offset:13312\n"
        "v_mfma_f32_16x16x32_bf16 v[20:23], a[188:191], a[196:199], v[20:23]\n"
        "ds_read_b128 a[24:27], v244 offset:9216\n"
        "v_mfma_f32_16x16x32_bf16 v[20:23], a[180:183], a[192:195], v[20:23]\n"
        "ds_read_b128 a[20:23], v244 offset:5120\n"
        "global_load_lds_dwordx4 v254, s[60:61] offset:2048 sc1\n"
        "v_mfma_f32_16x16x32_bf16 v[28:31], a[180:183], a[208:211], v[28:31]\n"
        "ds_read_b128 a[16:19], v244 offset:1024\n"
        "v_mfma_f32_16x16x32_bf16 v[28:31], a[188:191], a[212:215], v[28:31]\n"
        "s_waitcnt lgkmcnt(7)\n"
        "ds_read_b128 a[192:195], v244 offset:2048\n"
        "v_mfma_f32_16x16x32_bf16 v[0:3], a[48:51], a[0:3], v[0:3]\n"
        "s_waitcnt lgkmcnt(7)\n"
        "ds_read_b128 a[196:199], v244 offset:6144\n"
        "v_mfma_f32_16x16x32_bf16 v[0:3], a[56:59], a[4:7], v[0:3]\n"
        "s_waitcnt lgkmcnt(7)\n"
        "ds_read_b128 a[200:203], v244 offset:10240\n"
        "v_mfma_f32_16x16x32_bf16 v[0:3], a[224:227], a[8:11], v[0:3]\n"
        "s_waitcnt lgkmcnt(7)\n"
        "ds_read_b128 a[204:207], v244 offset:14336\n"
        "v_mfma_f32_16x16x32_bf16 v[0:3], a[232:235], a[12:15], v[0:3]\n"
        "v_permlane16_swap_b32 v96, v98\n"
        "v_mfma_f32_16x16x32_bf16 v[4:7], a[236:239], a[12:15], v[4:7]\n"
        "v_permlane16_swap_b32 v97, v99\n"
        "v_mfma_f32_16x16x32_bf16 v[4:7], a[228:231], a[8:11], v[4:7]\n"
        "global_load_lds_dwordx4 v254, s[60:61] offset:3072 sc1\n"
        "s_add_u32 m0, s73, 0x4c00\n"
        "v_lshlrev_b32 v64, 16, v96\n"
        "v_mfma_f32_16x16x32_bf16 v[4:7], a[60:63], a[4:7], v[4:7]\n"
        "s_add_u32 s60, s68, s66\n"
        "s_addc_u32 s61, s69, 0\n"
        "v_cvt_pk_bf16_f32 v0, v0, v1\n"
        "v_mfma_f32_16x16x32_bf16 v[4:7], a[52:55], a[0:3], v[4:7]\n"
        "s_waitcnt lgkmcnt(4)\n"
        "v_cvt_pk_bf16_f32 v1, v2, v3\n"
        "s_xor_b32 s66, s17, 7\n"
        "v_mfma_f32_16x16x32_bf16 v[28:31], a[36:39], a[216:219], v[28:31]\n"
        "global_load_lds_dwordx4 v148, s[58:59] offset:0 sc1\n"
        "s_add_u32 m0, s73, 0x1c000\n"
        "s_lshl_b32 s66, s66, 15\n"
        "v_mfma_f32_16x16x32_bf16 v[28:31], a[44:47], a[220:223], v[28:31]\n"
        "v_cvt_pk_bf16_f32 v2, v4, v5\n"
        "v_cvt_pk_bf16_f32 v3, v6, v7\n"
        "v_mfma_f32_16x16x32_bf16 v[8:11], a[232:235], a[28:31], v[8:11]\n"
        "v_and_b32 v65, s35, v96\n"
        "v_lshlrev_b32 v66, 16, v97\n"
        "v_mfma_f32_16x16x32_bf16 v[8:11], a[224:227], a[24:27], v[8:11]\n"
        "global_store_dwordx4 v254, v[0:3], s[28:29] offset:0 sc1\n"
        "v_and_b32 v67, s35, v97\n"
        "v_lshlrev_b32 v68, 16, v98\n"
        "v_and_b32 v69, s35, v98\n"
        "v_lshlrev_b32 v70, 16, v99\n"
        "v_mfma_f32_16x16x32_bf16 v[8:11], a[56:59], a[20:23], v[8:11]\n"
        "v_and_b32 v71, s35, v99\n"
        "v_mfma_f32_16x16x32_bf16 v[8:11], a[48:51], a[16:19], v[8:11]\n"
        "global_load_dwordx4 v[108:111], v151, s[64:65]\n"
        "v_mfma_f32_16x16x32_bf16 v[12:15], a[52:55], a[16:19], v[12:15]\n"
        "v_mfma_f32_16x16x32_bf16 v[12:15], a[60:63], a[20:23], v[12:15]\n"
        "s_barrier\n"
        "v_mfma_f32_16x16x32_bf16 v[12:15], a[228:231], a[24:27], v[12:15]\n"
        "ds_read_b128 a[0:3], v245 offset:16384\n"
        "global_load_lds_dwordx4 v254, s[60:61] offset:0 sc1\n"
        "v_mfma_f32_16x16x32_bf16 v[12:15], a[236:239], a[28:31], v[12:15]\n"
        "ds_read_b128 a[4:7], v245 offset:20480\n"
        "v_cvt_pk_bf16_f32 v8, v8, v9\n"
        "v_mfma_f32_16x16x32_bf16 v[24:27], a[40:43], a[220:223], v[24:27]\n"
        "ds_read_b128 a[220:223], v244 offset:15360\n"
        "v_cvt_pk_bf16_f32 v9, v10, v11\n"
        "v_mfma_f32_16x16x32_bf16 v[24:27], a[32:35], a[216:219], v[24:27]\n"
        "ds_read_b128 a[216:219], v244 offset:11264\n"
        "global_load_lds_dwordx4 v254, s[60:61] offset:1024 sc1\n"
        "v_mfma_f32_16x16x32_bf16 v[24:27], a[184:187], a[212:215], v[24:27]\n"
        "ds_read_b128 a[212:215], v244 offset:7168\n"
        "v_cvt_pk_bf16_f32 v10, v12, v13\n"
        "v_mfma_f32_16x16x32_bf16 v[24:27], a[176:179], a[208:211], v[24:27]\n"
        "ds_read_b128 a[208:211], v244 offset:3072\n"
        "s_waitcnt lgkmcnt(6)\n"
        "v_mfma_f32_16x16x32_bf16 v[16:19], a[48:51], a[192:195], v[16:19]\n"
        "ds_read_b128 a[8:11], v245 offset:24576\n"
        "v_cvt_pk_bf16_f32 v11, v14, v15\n"
        "v_mfma_f32_16x16x32_bf16 v[16:19], a[56:59], a[196:199], v[16:19]\n"
        "global_store_dwordx4 v254, v[8:11], s[28:29] offset:1024 sc1\n"
        "ds_read_b128 a[12:15], v245 offset:28672\n"
        "ds_read_b128 a[16:19], v245 offset:17408\n"
        "v_mfma_f32_16x16x32_bf16 v[16:19], a[224:227], a[200:203], v[16:19]\n"
        "ds_read_b128 a[20:23], v245 offset:21504\n"
        "v_mfma_f32_16x16x32_bf16 v[16:19], a[232:235], a[204:207], v[16:19]\n"
        "ds_read_b128 a[24:27], v245 offset:25600\n"
        "global_load_lds_dwordx4 v254, s[60:61] offset:2048 sc1\n"
        "v_mfma_f32_16x16x32_bf16 v[20:23], a[236:239], a[204:207], v[20:23]\n"
        "ds_read_b128 a[28:31], v245 offset:29696\n"
        "s_waitcnt lgkmcnt(8)\n"
        "v_mfma_f32_16x16x32_bf16 v[20:23], a[228:231], a[200:203], v[20:23]\n"
        "ds_read_b128 a[200:203], v245 offset:26624\n"
        "v_mfma_f32_16x16x32_bf16 v[20:23], a[60:63], a[196:199], v[20:23]\n"
        "ds_read_b128 a[196:199], v245 offset:22528\n"
        "v_cvt_pk_bf16_f32 v16, v16, v17\n"
        "v_mfma_f32_16x16x32_bf16 v[20:23], a[52:55], a[192:195], v[20:23]\n"
        "ds_read_b128 a[192:195], v245 offset:18432\n"
        "v_cvt_pk_bf16_f32 v17, v18, v19\n"
        "v_mfma_f32_16x16x32_bf16 v[24:27], a[232:235], a[220:223], v[24:27]\n"
        "ds_read_b128 a[204:207], v245 offset:30720\n"
        "global_load_lds_dwordx4 v254, s[60:61] offset:3072 sc1\n"
        "s_add_u32 m0, s73, 0x10000\n"
        "v_mfma_f32_16x16x32_bf16 v[24:27], a[224:227], a[216:219], v[24:27]\n"
        "s_waitcnt lgkmcnt(7)\n"
        "v_cvt_pk_bf16_f32 v18, v20, v21\n"
        "v_cvt_pk_bf16_f32 v19, v22, v23\n"
        "v_mfma_f32_16x16x32_bf16 v[28:31], a[52:55], a[208:211], v[28:31]\n"
        "s_add_u32 s60, s68, s66\n"
        "s_addc_u32 s61, s69, 0\n"
        "v_mfma_f32_16x16x32_bf16 v[24:27], a[56:59], a[212:215], v[24:27]\n"
        "v_mfma_f32_16x16x32_bf16 v[28:31], a[60:63], a[212:215], v[28:31]\n"
        "global_store_dwordx4 v254, v[16:19], s[28:29] offset:2048 sc1\n"
        "ds_read_b128 a[212:215], v245 offset:23552\n"
        "s_waitcnt vmcnt(19) lgkmcnt(5)\n"
        "v_permlane16_swap_b32 v100, v102\n"
        "v_mfma_f32_16x16x32_bf16 v[24:27], a[48:51], a[208:211], v[24:27]\n"
        "ds_read_b128 a[208:211], v245 offset:19456\n"
        "v_lshlrev_b32 v72, 16, v100\n"
        "v_mfma_f32_16x16x32_bf16 v[28:31], a[228:231], a[216:219], v[28:31]\n"
        "ds_read_b128 a[216:219], v245 offset:27648\n"
        "v_and_b32 v73, s35, v100\n"
        "v_mfma_f32_16x16x32_bf16 v[28:31], a[236:239], a[220:223], v[28:31]\n"
        "ds_read_b128 a[220:223], v245 offset:31744\n"
        "v_lshlrev_b32 v76, 16, v102\n"
        "v_mfma_f32_16x16x32_bf16 v[32:35], a[96:99], a[0:3], 0\n"
        "v_cvt_pk_bf16_f32 v24, v24, v25\n"
        "v_cvt_pk_bf16_f32 v25, v26, v27\n"
        "v_mfma_f32_16x16x32_bf16 v[32:35], a[104:107], a[4:7], v[32:35]\n"
        "v_permlane16_swap_b32 v101, v103\n"
        "v_mfma_f32_16x16x32_bf16 v[32:35], a[112:115], a[8:11], v[32:35]\n"
        "v_cvt_pk_bf16_f32 v26, v28, v29\n"
        "v_cvt_pk_bf16_f32 v27, v30, v31\n"
        "v_mfma_f32_16x16x32_bf16 v[32:35], a[120:123], a[12:15], v[32:35]\n"
        "global_store_dwordx4 v254, v[24:27], s[28:29] offset:3072 sc1\n"
        "v_lshlrev_b32 v74, 16, v101\n"
        "v_and_b32 v75, s35, v101\n"
        "v_and_b32 v77, s35, v102\n"
        "v_lshlrev_b32 v78, 16, v103\n"
        "v_mfma_f32_16x16x32_bf16 v[36:39], a[124:127], a[12:15], 0\n"
        "v_and_b32 v79, s35, v103\n"
        "v_mfma_f32_16x16x32_bf16 v[36:39], a[116:119], a[8:11], v[36:39]\n"
        "v_mfma_f32_16x16x32_bf16 v[36:39], a[108:111], a[4:7], v[36:39]\n"
        "v_mfma_f32_16x16x32_bf16 v[36:39], a[100:103], a[0:3], v[36:39]\n"
        "v_mfma_f32_16x16x32_bf16 v[44:47], a[100:103], a[16:19], 0\n"
        "v_mfma_f32_16x16x32_bf16 v[44:47], a[108:111], a[20:23], v[44:47]\n"
        "v_mfma_f32_16x16x32_bf16 v[44:47], a[116:119], a[24:27], v[44:47]\n"
        "v_mfma_f32_16x16x32_bf16 v[44:47], a[124:127], a[28:31], v[44:47]\n"
        "v_mfma_f32_16x16x32_bf16 v[40:43], a[120:123], a[28:31], 0\n"
        "s_waitcnt lgkmcnt(0)\n"
        "v_mfma_f32_16x16x32_bf16 v[40:43], a[112:115], a[24:27], v[40:43]\n"
        "v_mfma_f32_16x16x32_bf16 v[40:43], a[104:107], a[20:23], v[40:43]\n"
        "v_mfma_f32_16x16x32_bf16 v[40:43], a[96:99], a[16:19], v[40:43]\n"
        "v_mfma_f32_16x16x32_bf16 v[48:51], a[96:99], a[192:195], 0\n"
        "s_barrier\n"
        "v_mfma_f32_16x16x32_bf16 v[48:51], a[104:107], a[196:199], v[48:51]\n"
        "ds_read_b128 a[0:3], v244 offset:16384\n"
        "global_load_lds_dwordx4 v254, s[60:61] offset:0 sc1\n"
        "v_mfma_f32_16x16x32_bf16 v[48:51], a[112:115], a[200:203], v[48:51]\n"
        "ds_read_b128 a[4:7], v244 offset:20480\n"
        "v_mfma_f32_16x16x32_bf16 v[48:51], a[120:123], a[204:207], v[48:51]\n"
        "ds_read_b128 a[8:11], v244 offset:24576\n"
        "v_mfma_f32_16x16x32_bf16 v[52:55], a[124:127], a[204:207], 0\n"
        "ds_read_b128 a[12:15], v244 offset:28672\n"
        "global_load_lds_dwordx4 v254, s[60:61] offset:1024 sc1\n"
        "v_mfma_f32_16x16x32_bf16 v[52:55], a[116:119], a[200:203], v[52:55]\n"
        "ds_read_b128 a[16:19], v244 offset:17408\n"
        "v_mfma_f32_16x16x32_bf16 v[52:55], a[108:111], a[196:199], v[52:55]\n"
        "ds_read_b128 a[20:23], v244 offset:21504\n"
        "v_mfma_f32_16x16x32_bf16 v[52:55], a[100:103], a[192:195], v[52:55]\n"
        "ds_read_b128 a[24:27], v244 offset:25600\n"
        "global_load_lds_dwordx4 v254, s[60:61] offset:2048 sc1\n"
        "v_mfma_f32_16x16x32_bf16 v[60:63], a[100:103], a[208:211], 0\n"
        "ds_read_b128 a[28:31], v244 offset:29696\n"
        "v_mfma_f32_16x16x32_bf16 v[60:63], a[108:111], a[212:215], v[60:63]\n"
        "ds_read_b128 a[192:195], v244 offset:18432\n"
        "v_mfma_f32_16x16x32_bf16 v[60:63], a[116:119], a[216:219], v[60:63]\n"
        "ds_read_b128 a[196:199], v244 offset:22528\n"
        "global_load_lds_dwordx4 v254, s[60:61] offset:3072 sc1\n"
        "v_mfma_f32_16x16x32_bf16 v[60:63], a[124:127], a[220:223], v[60:63]\n"
        "ds_read_b128 a[200:203], v244 offset:26624\n"
        "s_waitcnt vmcnt(18) lgkmcnt(8)\n"
        "v_mfma_f32_16x16x32_bf16 v[56:59], a[120:123], a[220:223], 0\n"
        "ds_read_b128 a[204:207], v244 offset:30720\n"
        "v_mfma_f32_16x16x32_bf16 v[56:59], a[112:115], a[216:219], v[56:59]\n"
        "ds_read_b128 a[216:219], v244 offset:27648\n"
        "v_mfma_f32_16x16x32_bf16 v[56:59], a[104:107], a[212:215], v[56:59]\n"
        "ds_read_b128 a[212:215], v244 offset:23552\n"
        "v_mfma_f32_16x16x32_bf16 v[56:59], a[96:99], a[208:211], v[56:59]\n"
        "s_waitcnt lgkmcnt(7)\n"
        "ds_read_b128 a[208:211], v244 offset:19456\n"
        "v_mfma_f32_16x16x32_bf16 v[32:35], a[128:131], a[0:3], v[32:35]\n"
        "ds_read_b128 a[220:223], v244 offset:31744\n"
        "v_mfma_f32_16x16x32_bf16 v[32:35], a[136:139], a[4:7], v[32:35]\n"
        "v_permlane16_swap_b32 v104, v106\n"
        "v_mfma_f32_16x16x32_bf16 v[32:35], a[144:147], a[8:11], v[32:35]\n"
        "v_permlane16_swap_b32 v105, v107\n"
        "v_mfma_f32_16x16x32_bf16 v[32:35], a[152:155], a[12:15], v[32:35]\n"
        "v_lshlrev_b32 v80, 16, v104\n"
        "v_and_b32 v81, s35, v104\n"
        "v_mfma_f32_16x16x32_bf16 v[36:39], a[156:159], a[12:15], v[36:39]\n"
        "v_lshlrev_b32 v82, 16, v105\n"
        "v_and_b32 v83, s35, v105\n"
        "v_mfma_f32_16x16x32_bf16 v[36:39], a[148:151], a[8:11], v[36:39]\n"
        "v_lshlrev_b32 v84, 16, v106\n"
        "v_and_b32 v85, s35, v106\n"
        "v_mfma_f32_16x16x32_bf16 v[36:39], a[140:143], a[4:7], v[36:39]\n"
        "v_lshlrev_b32 v86, 16, v107\n"
        "v_and_b32 v87, s35, v107\n"
        "v_mfma_f32_16x16x32_bf16 v[36:39], a[132:135], a[0:3], v[36:39]\n"
        "s_waitcnt lgkmcnt(2)\n"
        "v_mfma_f32_16x16x32_bf16 v[44:47], a[132:135], a[16:19], v[44:47]\n"
        "v_mfma_f32_16x16x32_bf16 v[44:47], a[140:143], a[20:23], v[44:47]\n"
        "v_mfma_f32_16x16x32_bf16 v[44:47], a[148:151], a[24:27], v[44:47]\n"
        "v_mfma_f32_16x16x32_bf16 v[44:47], a[156:159], a[28:31], v[44:47]\n"
        "v_mfma_f32_16x16x32_bf16 v[40:43], a[152:155], a[28:31], v[40:43]\n"
        "v_mfma_f32_16x16x32_bf16 v[40:43], a[144:147], a[24:27], v[40:43]\n"
        "v_mfma_f32_16x16x32_bf16 v[40:43], a[136:139], a[20:23], v[40:43]\n"
        "v_mfma_f32_16x16x32_bf16 v[40:43], a[128:131], a[16:19], v[40:43]\n"
        "v_mfma_f32_16x16x32_bf16 v[48:51], a[128:131], a[192:195], v[48:51]\n"
        "s_barrier\n"
        "v_mfma_f32_16x16x32_bf16 v[48:51], a[136:139], a[196:199], v[48:51]\n"
        "ds_read_b128 a[0:3], v254 offset:0\n"
        "v_mfma_f32_16x16x32_bf16 v[48:51], a[144:147], a[200:203], v[48:51]\n"
        "ds_read_b128 a[4:7], v254 offset:4096\n"
        "v_mfma_f32_16x16x32_bf16 v[48:51], a[152:155], a[204:207], v[48:51]\n"
        "ds_read_b128 a[8:11], v254 offset:8192\n"
        "v_mfma_f32_16x16x32_bf16 v[52:55], a[156:159], a[204:207], v[52:55]\n"
        "ds_read_b128 a[12:15], v254 offset:12288\n"
        "s_waitcnt lgkmcnt(4)\n"
        "v_mfma_f32_16x16x32_bf16 v[52:55], a[148:151], a[200:203], v[52:55]\n"
        "ds_read_b128 a[16:19], v254 offset:1024\n"
        "v_mfma_f32_16x16x32_bf16 v[52:55], a[140:143], a[196:199], v[52:55]\n"
        "ds_read_b128 a[20:23], v254 offset:5120\n"
        "v_mfma_f32_16x16x32_bf16 v[52:55], a[132:135], a[192:195], v[52:55]\n"
        "ds_read_b128 a[24:27], v254 offset:9216\n"
        "v_mfma_f32_16x16x32_bf16 v[60:63], a[132:135], a[208:211], v[60:63]\n"
        "ds_read_b128 a[28:31], v254 offset:13312\n"
        "v_mfma_f32_16x16x32_bf16 v[60:63], a[140:143], a[212:215], v[60:63]\n"
        "ds_read_b128 a[192:195], v254 offset:2048\n"
        "v_mfma_f32_16x16x32_bf16 v[60:63], a[148:151], a[216:219], v[60:63]\n"
        "ds_read_b128 a[196:199], v254 offset:6144\n"
        "s_waitcnt vmcnt(4)\n"
        "v_mfma_f32_16x16x32_bf16 v[60:63], a[156:159], a[220:223], v[60:63]\n"
        "s_mov_b64 exec, 0xffff\n"
        "global_store_dwordx2 v154, v[250:251], s[48:49] sc1\n"
        "s_mov_b64 exec, -1\n"
        "v_mfma_f32_16x16x32_bf16 v[56:59], a[152:155], a[220:223], v[56:59]\n"
        "ds_read_b128 a[200:203], v254 offset:10240\n"
        "v_mfma_f32_16x16x32_bf16 v[56:59], a[144:147], a[216:219], v[56:59]\n"
        "ds_read_b128 a[204:207], v254 offset:14336\n"
        "s_waitcnt lgkmcnt(7)\n"
        "v_mfma_f32_16x16x32_bf16 v[56:59], a[136:139], a[212:215], v[56:59]\n"
        "ds_read_b128 a[212:215], v254 offset:7168\n"
        "v_mfma_f32_16x16x32_bf16 v[56:59], a[128:131], a[208:211], v[56:59]\n"
        "ds_read_b128 a[208:211], v254 offset:3072\n"
        "v_mfma_f32_16x16x32_bf16 v[32:35], v[160:163], a[0:3], v[32:35]\n"
        "ds_read_b128 a[216:219], v254 offset:11264\n"
        "v_mfma_f32_16x16x32_bf16 v[32:35], v[168:171], a[4:7], v[32:35]\n"
        "ds_read_b128 a[220:223], v254 offset:15360\n"
        "v_mfma_f32_16x16x32_bf16 v[32:35], v[176:179], a[8:11], v[32:35]\n"
        "ds_read_b64_tr_b16 v[128:129], v149 offset:0\n"
        "v_mfma_f32_16x16x32_bf16 v[32:35], v[184:187], a[12:15], v[32:35]\n"
        "ds_read_b64_tr_b16 v[130:131], v149 offset:2048\n"
        "v_mfma_f32_16x16x32_bf16 v[36:39], v[188:191], a[12:15], v[36:39]\n"
        "ds_read_b64_tr_b16 v[132:133], v149 offset:32\n"
        "v_mfma_f32_16x16x32_bf16 v[36:39], v[180:183], a[8:11], v[36:39]\n"
        "s_waitcnt lgkmcnt(7)\n"
        "ds_read_b64_tr_b16 v[134:135], v149 offset:2080\n"
        "v_mfma_f32_16x16x32_bf16 v[36:39], v[172:175], a[4:7], v[36:39]\n"
        "ds_read_b64_tr_b16 v[136:137], v149 offset:64\n"
        "v_mfma_f32_16x16x32_bf16 v[36:39], v[164:167], a[0:3], v[36:39]\n"
        "ds_read_b64_tr_b16 v[138:139], v149 offset:2112\n"
        "v_mfma_f32_16x16x32_bf16 v[44:47], v[164:167], a[16:19], v[44:47]\n"
        "ds_read_b64_tr_b16 v[140:141], v149 offset:96\n"
        "v_mfma_f32_16x16x32_bf16 v[44:47], v[172:175], a[20:23], v[44:47]\n"
        "ds_read_b64_tr_b16 v[142:143], v149 offset:2144\n"
        "v_mfma_f32_16x16x32_bf16 v[40:43], v[184:187], a[28:31], v[40:43]\n"
        "s_waitcnt lgkmcnt(7)\n"
        "v_permlane16_swap_b32 v108, v110\n"
        "v_mfma_f32_16x16x32_bf16 v[40:43], v[176:179], a[24:27], v[40:43]\n"
        "s_waitcnt lgkmcnt(6)\n"
        "v_permlane16_swap_b32 v109, v111\n"
        "v_mfma_f32_16x16x32_bf16 v[48:51], v[160:163], a[192:195], v[48:51]\n"
        "v_lshlrev_b32 v246, 16, v128\n"
        "v_mul_f32 v64, v64, v246\n"
        "v_mfma_f32_16x16x32_bf16 v[40:43], v[168:171], a[20:23], v[40:43]\n"
        "s_waitcnt lgkmcnt(5)\n"
        "v_cvt_pk_bf16_f32 v64, 0, v64\n"
        "v_mul_f32 v64, v64, v228\n"
        "v_mfma_f32_16x16x32_bf16 v[48:51], v[168:171], a[196:199], v[48:51]\n"
        "v_cvt_pk_bf16_f32 v64, 0, v64\n"
        "v_and_b32 v247, s35, v128\n"
        "v_mfma_f32_16x16x32_bf16 v[40:43], v[160:163], a[16:19], v[40:43]\n"
        "v_mul_f32 v65, v65, v247\n"
        "v_cvt_pk_bf16_f32 v65, 0, v65\n"
        "v_mfma_f32_16x16x32_bf16 v[48:51], v[176:179], a[200:203], v[48:51]\n"
        "s_barrier\n"
        "v_mfma_f32_16x16x32_bf16 v[52:55], v[188:191], a[204:207], v[52:55]\n"
        "ds_read_b128 a[0:3], v244 offset:32768\n"
        "v_mul_f32 v65, v65, v228\n"
        "v_mfma_f32_16x16x32_bf16 v[48:51], v[184:187], a[204:207], v[48:51]\n"
        "ds_read_b128 a[204:207], v244 offset:47104\n"
        "v_cvt_pk_bf16_f32 v65, 0, v65\n"
        "v_mfma_f32_16x16x32_bf16 v[52:55], v[180:183], a[200:203], v[52:55]\n"
        "ds_read_b128 a[200:203], v244 offset:43008\n"
        "v_lshlrev_b32 v248, 16, v129\n"
        "v_mfma_f32_16x16x32_bf16 v[52:55], v[172:175], a[196:199], v[52:55]\n"
        "ds_read_b128 a[196:199], v244 offset:38912\n"
        "v_mul_f32 v66, v66, v248\n"
        "v_mfma_f32_16x16x32_bf16 v[52:55], v[164:167], a[192:195], v[52:55]\n"
        "ds_read_b128 a[192:195], v244 offset:34816\n"
        "v_cvt_pk_bf16_f32 v66, 0, v66\n"
        "v_mfma_f32_16x16x32_bf16 v[44:47], v[180:183], a[24:27], v[44:47]\n"
        "ds_read_b128 a[4:7], v244 offset:36864\n"
        "v_mul_f32 v66, v66, v228\n"
        "v_mfma_f32_16x16x32_bf16 v[44:47], v[188:191], a[28:31], v[44:47]\n"
        "ds_read_b128 a[8:11], v244 offset:40960\n"
        "v_cvt_pk_bf16_f32 v66, 0, v66\n"
        "v_mfma_f32_16x16x32_bf16 v[60:63], v[164:167], a[208:211], v[60:63]\n"
        "ds_read_b128 a[12:15], v244 offset:45056\n"
        "v_and_b32 v249, s35, v129\n"
        "v_mfma_f32_16x16x32_bf16 v[60:63], v[172:175], a[212:215], v[60:63]\n"
        "ds_read_b128 a[16:19], v244 offset:33792\n"
        "v_mul_f32 v67, v67, v249\n"
        "v_mfma_f32_16x16x32_bf16 v[60:63], v[180:183], a[216:219], v[60:63]\n"
        "ds_read_b128 a[20:23], v244 offset:37888\n"
        "s_waitcnt lgkmcnt(8)\n"
        "v_mfma_f32_16x16x32_bf16 v[52:55], v[220:223], a[204:207], v[52:55]\n"
        "ds_read_b128 a[24:27], v244 offset:41984\n"
        "s_waitcnt lgkmcnt(8)\n"
        "v_mfma_f32_16x16x32_bf16 v[52:55], v[212:215], a[200:203], v[52:55]\n"
        "ds_read_b128 a[28:31], v244 offset:46080\n"
        "s_waitcnt lgkmcnt(8)\n"
        "v_mfma_f32_16x16x32_bf16 v[52:55], v[204:207], a[196:199], v[52:55]\n"
        "s_waitcnt lgkmcnt(7)\n"
        "v_cvt_pk_bf16_f32 v67, 0, v67\n"
        "v_mul_f32 v67, v67, v228\n"
        "v_mfma_f32_16x16x32_bf16 v[52:55], v[196:199], a[192:195], v[52:55]\n"
        "global_load_dwordx2 v[156:157], v155, s[48:49] sc1\n"
        "v_cvt_pk_bf16_f32 v67, 0, v67\n"
        "v_mfma_f32_16x16x32_bf16 v[60:63], v[188:191], a[220:223], v[60:63]\n"
        "v_lshlrev_b32 v144, 16, v130\n"
        "v_mul_f32 v68, v68, v144\n"
        "v_mfma_f32_16x16x32_bf16 v[56:59], v[184:187], a[220:223], v[56:59]\n"
        "ds_read_b128 a[220:223], v244 offset:48128\n"
        "v_cvt_pk_bf16_f32 v68, 0, v68\n"
        "v_mfma_f32_16x16x32_bf16 v[56:59], v[176:179], a[216:219], v[56:59]\n"
        "ds_read_b128 a[216:219], v244 offset:44032\n"
        "v_mul_f32 v68, v68, v228\n"
        "v_mfma_f32_16x16x32_bf16 v[56:59], v[168:171], a[212:215], v[56:59]\n"
        "ds_read_b128 a[212:215], v244 offset:39936\n"
        "v_cvt_pk_bf16_f32 v68, 0, v68\n"
        "v_mfma_f32_16x16x32_bf16 v[56:59], v[160:163], a[208:211], v[56:59]\n"
        "ds_read_b128 a[208:211], v244 offset:35840\n"
        "v_and_b32 v145, s35, v130\n"
        "v_mfma_f32_16x16x32_bf16 v[32:35], v[192:195], a[0:3], v[32:35]\n"
        "s_waitcnt lgkmcnt(4)\n"
        "v_mul_f32 v69, v69, v145\n"
        "v_cvt_pk_bf16_f32 v69, 0, v69\n"
        "v_mfma_f32_16x16x32_bf16 v[32:35], v[200:203], a[4:7], v[32:35]\n"
        "v_mul_f32 v69, v69, v228\n"
        "v_cvt_pk_bf16_f32 v69, 0, v69\n"
        "v_mfma_f32_16x16x32_bf16 v[32:35], v[208:211], a[8:11], v[32:35]\n"
        "v_lshlrev_b32 v146, 16, v131\n"
        "v_mul_f32 v70, v70, v146\n"
        "v_mfma_f32_16x16x32_bf16 v[32:35], v[216:219], a[12:15], v[32:35]\n"
        "v_cvt_pk_bf16_f32 v70, 0, v70\n"
        "v_mul_f32 v70, v70, v228\n"
        "v_mfma_f32_16x16x32_bf16 v[36:39], v[220:223], a[12:15], v[36:39]\n"
        "v_cvt_pk_bf16_f32 v70, 0, v70\n"
        "v_and_b32 v147, s35, v131\n"
        "v_mfma_f32_16x16x32_bf16 v[36:39], v[212:215], a[8:11], v[36:39]\n"
        "v_mul_f32 v71, v71, v147\n"
        "v_cvt_pk_bf16_f32 v71, 0, v71\n"
        "v_mfma_f32_16x16x32_bf16 v[36:39], v[204:207], a[4:7], v[36:39]\n"
        "v_mul_f32 v71, v71, v228\n"
        "v_cvt_pk_bf16_f32 v71, 0, v71\n"
        "v_mfma_f32_16x16x32_bf16 v[36:39], v[196:199], a[0:3], v[36:39]\n"
        "v_lshlrev_b32 v246, 16, v132\n"
        "v_mul_f32 v72, v72, v246\n"
        "v_mfma_f32_16x16x32_bf16 v[44:47], v[196:199], a[16:19], v[44:47]\n"
        "v_cvt_pk_bf16_f32 v72, 0, v72\n"
        "v_mul_f32 v72, v72, v229\n"
        "v_mfma_f32_16x16x32_bf16 v[44:47], v[204:207], a[20:23], v[44:47]\n"
        "v_cvt_pk_bf16_f32 v72, 0, v72\n"
        "v_and_b32 v247, s35, v132\n"
        "v_mfma_f32_16x16x32_bf16 v[44:47], v[212:215], a[24:27], v[44:47]\n"
        "v_mul_f32 v73, v73, v247\n"
        "v_cvt_pk_bf16_f32 v73, 0, v73\n"
        "v_mfma_f32_16x16x32_bf16 v[44:47], v[220:223], a[28:31], v[44:47]\n"
        "v_mul_f32 v73, v73, v229\n"
        "v_cvt_pk_bf16_f32 v73, 0, v73\n"
        "v_mfma_f32_16x16x32_bf16 v[40:43], v[216:219], a[28:31], v[40:43]\n"
        "v_lshlrev_b32 v248, 16, v133\n"
        "v_mul_f32 v74, v74, v248\n"
        "v_mfma_f32_16x16x32_bf16 v[40:43], v[208:211], a[24:27], v[40:43]\n"
        "v_cvt_pk_bf16_f32 v74, 0, v74\n"
        "v_mul_f32 v74, v74, v229\n"
        "v_mfma_f32_16x16x32_bf16 v[40:43], v[200:203], a[20:23], v[40:43]\n"
        "v_cvt_pk_bf16_f32 v74, 0, v74\n"
        "v_and_b32 v249, s35, v133\n"
        "v_mfma_f32_16x16x32_bf16 v[40:43], v[192:195], a[16:19], v[40:43]\n"
        "v_mul_f32 v75, v75, v249\n"
        "v_cvt_pk_bf16_f32 v75, 0, v75\n"
        "v_mfma_f32_16x16x32_bf16 v[48:51], v[192:195], a[192:195], v[48:51]\n"
        "s_barrier\n"
        "v_mfma_f32_16x16x32_bf16 v[48:51], v[200:203], a[196:199], v[48:51]\n"
        "ds_read_b128 a[0:3], v254 offset:49152\n"
        "v_mul_f32 v75, v75, v229\n"
        "v_mfma_f32_16x16x32_bf16 v[48:51], v[208:211], a[200:203], v[48:51]\n"
        "ds_read_b128 a[4:7], v254 offset:53248\n"
        "v_cvt_pk_bf16_f32 v75, 0, v75\n"
        "v_mfma_f32_16x16x32_bf16 v[48:51], v[216:219], a[204:207], v[48:51]\n"
        "ds_read_b128 a[8:11], v254 offset:57344\n"
        "s_waitcnt lgkmcnt(3)\n"
        "v_mfma_f32_16x16x32_bf16 v[60:63], v[196:199], a[208:211], v[60:63]\n"
        "ds_read_b128 a[12:15], v254 offset:61440\n"
        "v_lshlrev_b32 v144, 16, v134\n"
        "v_mfma_f32_16x16x32_bf16 v[60:63], v[204:207], a[212:215], v[60:63]\n"
        "ds_read_b128 a[16:19], v254 offset:50176\n"
        "v_mul_f32 v76, v76, v144\n"
        "v_mfma_f32_16x16x32_bf16 v[60:63], v[212:215], a[216:219], v[60:63]\n"
        "ds_read_b128 a[20:23], v254 offset:54272\n"
        "v_cvt_pk_bf16_f32 v76, 0, v76\n"
        "v_mfma_f32_16x16x32_bf16 v[60:63], v[220:223], a[220:223], v[60:63]\n"
        "ds_read_b128 a[24:27], v254 offset:58368\n"
        "v_mul_f32 v76, v76, v229\n"
        "v_mfma_f32_16x16x32_bf16 v[56:59], v[216:219], a[220:223], v[56:59]\n"
        "ds_read_b128 a[28:31], v254 offset:62464\n"
        "v_cvt_pk_bf16_f32 v76, 0, v76\n"
        "v_mfma_f32_16x16x32_bf16 v[56:59], v[208:211], a[216:219], v[56:59]\n"
        "ds_read_b128 a[192:195], v254 offset:51200\n"
        "v_and_b32 v145, s35, v134\n"
        "v_mfma_f32_16x16x32_bf16 v[56:59], v[200:203], a[212:215], v[56:59]\n"
        "ds_read_b128 a[196:199], v254 offset:55296\n"
        "v_mul_f32 v77, v77, v145\n"
        "v_mfma_f32_16x16x32_bf16 v[56:59], v[192:195], a[208:211], v[56:59]\n"
        "ds_read_b128 a[200:203], v254 offset:59392\n"
        "s_waitcnt lgkmcnt(8)\n"
        "v_mfma_f32_16x16x32_bf16 v[32:35], v[112:115], a[0:3], v[32:35]\n"
        "ds_read_b128 a[204:207], v254 offset:63488\n"
        "v_cvt_pk_bf16_f32 v77, 0, v77\n"
        "v_mfma_f32_16x16x32_bf16 v[32:35], v[120:123], a[4:7], v[32:35]\n"
        "ds_read_b128 a[208:211], v254 offset:52224\n"
        "v_mul_f32 v77, v77, v229\n"
        "v_mfma_f32_16x16x32_bf16 v[32:35], a[64:67], a[8:11], v[32:35]\n"
        "ds_read_b128 a[212:215], v254 offset:56320\n"
        "s_waitcnt lgkmcnt(8)\n"
        "v_mfma_f32_16x16x32_bf16 v[32:35], a[72:75], a[12:15], v[32:35]\n"
        "ds_read_b128 a[216:219], v254 offset:60416\n"
        "v_cvt_pk_bf16_f32 v77, 0, v77\n"
        "v_mfma_f32_16x16x32_bf16 v[36:39], a[76:79], a[12:15], v[36:39]\n"
        "ds_read_b128 a[220:223], v254 offset:64512\n"
        "v_lshlrev_b32 v146, 16, v135\n"
        "v_mfma_f32_16x16x32_bf16 v[36:39], a[68:71], a[8:11], v[36:39]\n"
        "s_waitcnt vmcnt(0) lgkmcnt(7)\n"
        "v_cmp_ne_u64 vcc, v[156:157], v[250:251]\n"
        "v_mul_f32 v78, v78, v146\n"
        "v_mfma_f32_16x16x32_bf16 v[36:39], v[124:127], a[4:7], v[36:39]\n"
        "s_cbranch_vccnz LPPR_%=\n"
        "LBPR_%=:\n"
        "global_load_dwordx4 a[240:243], v254, s[30:31] offset:0 sc1\n"
        "v_mfma_f32_16x16x32_bf16 v[36:39], v[116:119], a[0:3], v[36:39]\n"
        "v_cvt_pk_bf16_f32 v78, 0, v78\n"
        "v_mul_f32 v78, v78, v229\n"
        "v_mfma_f32_16x16x32_bf16 v[44:47], v[116:119], a[16:19], v[44:47]\n"
        "global_load_dwordx4 a[244:247], v254, s[30:31] offset:1024 sc1\n"
        "v_mfma_f32_16x16x32_bf16 v[44:47], v[124:127], a[20:23], v[44:47]\n"
        "v_cvt_pk_bf16_f32 v78, 0, v78\n"
        "v_and_b32 v147, s35, v135\n"
        "v_mfma_f32_16x16x32_bf16 v[44:47], a[68:71], a[24:27], v[44:47]\n"
        "global_load_dwordx4 a[248:251], v254, s[30:31] offset:2048 sc1\n"
        "v_mfma_f32_16x16x32_bf16 v[44:47], a[76:79], a[28:31], v[44:47]\n"
        "v_mul_f32 v79, v79, v147\n"
        "v_cvt_pk_bf16_f32 v79, 0, v79\n"
        "v_mfma_f32_16x16x32_bf16 v[40:43], a[72:75], a[28:31], v[40:43]\n"
        "global_load_dwordx4 a[252:255], v254, s[30:31] offset:3072 sc1\n"
        "v_mfma_f32_16x16x32_bf16 v[40:43], a[64:67], a[24:27], v[40:43]\n"
        "s_add_u32 s30, s38, s16\n"
        "s_addc_u32 s31, s39, 0\n"
        "s_mul_i32 s16, s13, 263168\n"
        "v_mfma_f32_16x16x32_bf16 v[40:43], v[120:123], a[20:23], v[40:43]\n"
        "global_load_dwordx4 v[0:3], v254, s[30:31] offset:0 sc1\n"
        "s_add_u32 s16, s16, s15\n"
        "v_mfma_f32_16x16x32_bf16 v[40:43], v[112:115], a[16:19], v[40:43]\n"
        "s_add_u32 s16, s16, s73\n"
        "s_add_u32 s16, s16, 0x4000000\n"
        "v_mfma_f32_16x16x32_bf16 v[48:51], v[112:115], a[192:195], v[48:51]\n"
        "s_waitcnt lgkmcnt(0)\n"
        "global_load_dwordx4 v[4:7], v254, s[30:31] offset:1024 sc1\n"
        "v_mfma_f32_16x16x32_bf16 v[48:51], v[120:123], a[196:199], v[48:51]\n"
        "s_barrier\n"
        "v_mfma_f32_16x16x32_bf16 v[48:51], a[64:67], a[200:203], v[48:51]\n"
        "ds_read_b128 a[0:3], v245 offset:0\n"
        "global_load_dwordx4 v[8:11], v254, s[30:31] offset:2048 sc1\n"
        "v_mfma_f32_16x16x32_bf16 v[48:51], a[72:75], a[204:207], v[48:51]\n"
        "ds_read_b128 a[4:7], v245 offset:4096\n"
        "v_mul_f32 v79, v79, v229\n"
        "v_mfma_f32_16x16x32_bf16 v[52:55], a[76:79], a[204:207], v[52:55]\n"
        "ds_read_b128 a[8:11], v245 offset:8192\n"
        "global_load_dwordx4 v[12:15], v254, s[30:31] offset:3072 sc1\n"
        "v_mfma_f32_16x16x32_bf16 v[52:55], a[68:71], a[200:203], v[52:55]\n"
        "ds_read_b128 a[12:15], v245 offset:12288\n"
        "s_add_u32 s30, s38, s16\n"
        "s_addc_u32 s31, s39, 0\n"
        "v_mfma_f32_16x16x32_bf16 v[52:55], v[124:127], a[196:199], v[52:55]\n"
        "ds_read_b128 a[16:19], v245 offset:1024\n"
        "global_load_dwordx4 v[16:19], v254, s[30:31] offset:0 sc1\n"
        "v_mfma_f32_16x16x32_bf16 v[52:55], v[116:119], a[192:195], v[52:55]\n"
        "ds_read_b128 a[20:23], v245 offset:5120\n"
        "v_cvt_pk_bf16_f32 v79, 0, v79\n"
        "v_mfma_f32_16x16x32_bf16 v[60:63], v[116:119], a[208:211], v[60:63]\n"
        "ds_read_b128 a[24:27], v245 offset:9216\n"
        "global_load_dwordx4 v[20:23], v254, s[30:31] offset:1024 sc1\n"
        "v_mfma_f32_16x16x32_bf16 v[60:63], v[124:127], a[212:215], v[60:63]\n"
        "ds_read_b128 a[28:31], v245 offset:13312\n"
        "v_lshlrev_b32 v246, 16, v136\n"
        "v_mfma_f32_16x16x32_bf16 v[60:63], a[68:71], a[216:219], v[60:63]\n"
        "ds_read_b128 a[192:195], v245 offset:2048\n"
        "global_load_dwordx4 v[24:27], v254, s[30:31] offset:2048 sc1\n"
        "LHB3_%=:\n"
        "s_cbranch_execz LHRB_%=\n"
        "v_mfma_f32_16x16x32_bf16 v[60:63], a[76:79], a[220:223], v[60:63]\n"
        "ds_read_b128 a[196:199], v245 offset:6144\n"
        "v_mul_f32 v80, v80, v246\n"
        "v_mfma_f32_16x16x32_bf16 v[56:59], a[72:75], a[220:223], v[56:59]\n"
        "ds_read_b128 a[200:203], v245 offset:10240\n"
        "global_load_dwordx4 v[28:31], v254, s[30:31] offset:3072 sc1\n"
        "v_mfma_f32_16x16x32_bf16 v[56:59], a[64:67], a[216:219], v[56:59]\n"
        "ds_read_b128 a[204:207], v245 offset:14336\n"
        "v_cvt_pk_bf16_f32 v80, 0, v80\n"
        "v_mfma_f32_16x16x32_bf16 v[56:59], v[120:123], a[212:215], v[56:59]\n"
        "ds_read_b128 a[212:215], v245 offset:7168\n"
        "v_mul_f32 v80, v80, v230\n"
        "v_mfma_f32_16x16x32_bf16 v[56:59], v[112:115], a[208:211], v[56:59]\n"
        "ds_read_b128 a[208:211], v245 offset:3072\n"
        "s_waitcnt lgkmcnt(8)\n"
        "v_mfma_f32_16x16x32_bf16 v[32:35], a[80:83], a[0:3], v[32:35]\n"
        "ds_read_b128 a[216:219], v245 offset:11264\n"
        "v_cvt_pk_bf16_f32 v80, 0, v80\n"
        "v_mfma_f32_16x16x32_bf16 v[32:35], a[88:91], a[4:7], v[32:35]\n"
        "ds_read_b128 a[220:223], v245 offset:15360\n"
        "v_and_b32 v247, s35, v136\n"
        "v_mfma_f32_16x16x32_bf16 v[32:35], a[160:163], a[8:11], v[32:35]\n"
        "v_mul_f32 v81, v81, v247\n"
        "v_cvt_pk_bf16_f32 v81, 0, v81\n"
        "v_mfma_f32_16x16x32_bf16 v[32:35], a[168:171], a[12:15], v[32:35]\n"
        "v_mul_f32 v81, v81, v230\n"
        "v_cvt_pk_bf16_f32 v81, 0, v81\n"
        "v_mfma_f32_16x16x32_bf16 v[36:39], a[172:175], a[12:15], v[36:39]\n"
        "v_lshlrev_b32 v248, 16, v137\n"
        "v_mul_f32 v82, v82, v248\n"
        "v_mfma_f32_16x16x32_bf16 v[36:39], a[164:167], a[8:11], v[36:39]\n"
        "v_cvt_pk_bf16_f32 v82, 0, v82\n"
        "v_mul_f32 v82, v82, v230\n"
        "v_mfma_f32_16x16x32_bf16 v[36:39], a[92:95], a[4:7], v[36:39]\n"
        "s_waitcnt lgkmcnt(3)\n"
        "v_cvt_pk_bf16_f32 v82, 0, v82\n"
        "v_and_b32 v249, s35, v137\n"
        "v_mfma_f32_16x16x32_bf16 v[36:39], a[84:87], a[0:3], v[36:39]\n"
        "v_mul_f32 v83, v83, v249\n"
        "v_cvt_pk_bf16_f32 v83, 0, v83\n"
        "v_mfma_f32_16x16x32_bf16 v[44:47], a[84:87], a[16:19], v[44:47]\n"
        "v_mul_f32 v83, v83, v230\n"
        "v_cvt_pk_bf16_f32 v83, 0, v83\n"
        "v_mfma_f32_16x16x32_bf16 v[44:47], a[92:95], a[20:23], v[44:47]\n"
        "v_lshlrev_b32 v144, 16, v138\n"
        "v_mul_f32 v84, v84, v144\n"
        "v_mfma_f32_16x16x32_bf16 v[44:47], a[164:167], a[24:27], v[44:47]\n"
        "v_cvt_pk_bf16_f32 v84, 0, v84\n"
        "v_mul_f32 v84, v84, v230\n"
        "v_mfma_f32_16x16x32_bf16 v[44:47], a[172:175], a[28:31], v[44:47]\n"
        "v_cvt_pk_bf16_f32 v84, 0, v84\n"
        "v_and_b32 v145, s35, v138\n"
        "v_mfma_f32_16x16x32_bf16 v[40:43], a[168:171], a[28:31], v[40:43]\n"
        "v_mul_f32 v85, v85, v145\n"
        "v_cvt_pk_bf16_f32 v85, 0, v85\n"
        "v_mfma_f32_16x16x32_bf16 v[40:43], a[160:163], a[24:27], v[40:43]\n"
        "v_mul_f32 v85, v85, v230\n"
        "v_cvt_pk_bf16_f32 v85, 0, v85\n"
        "v_mfma_f32_16x16x32_bf16 v[40:43], a[88:91], a[20:23], v[40:43]\n"
        "v_lshlrev_b32 v146, 16, v139\n"
        "v_mul_f32 v86, v86, v146\n"
        "v_mfma_f32_16x16x32_bf16 v[40:43], a[80:83], a[16:19], v[40:43]\n"
        "v_cvt_pk_bf16_f32 v86, 0, v86\n"
        "v_mul_f32 v86, v86, v230\n"
        "v_mfma_f32_16x16x32_bf16 v[48:51], a[80:83], a[192:195], v[48:51]\n"
        "s_barrier\n"
        "v_mfma_f32_16x16x32_bf16 v[48:51], a[88:91], a[196:199], v[48:51]\n"
        "ds_read_b128 a[0:3], v244 offset:49152\n"
        "v_cvt_pk_bf16_f32 v86, 0, v86\n"
        "v_mfma_f32_16x16x32_bf16 v[48:51], a[160:163], a[200:203], v[48:51]\n"
        "ds_read_b128 a[4:7], v244 offset:53248\n"
        "v_and_b32 v147, s35, v139\n"
        "v_mfma_f32_16x16x32_bf16 v[48:51], a[168:171], a[204:207], v[48:51]\n"
        "ds_read_b128 a[8:11], v244 offset:57344\n"
        "v_mul_f32 v87, v87, v147\n"
        "v_mfma_f32_16x16x32_bf16 v[52:55], a[172:175], a[204:207], v[52:55]\n"
        "ds_read_b128 a[12:15], v244 offset:61440\n"
        "v_cvt_pk_bf16_f32 v87, 0, v87\n"
        "v_mfma_f32_16x16x32_bf16 v[52:55], a[164:167], a[200:203], v[52:55]\n"
        "ds_read_b128 a[200:203], v244 offset:58368\n"
        "v_mul_f32 v87, v87, v230\n"
        "v_mfma_f32_16x16x32_bf16 v[52:55], a[92:95], a[196:199], v[52:55]\n"
        "ds_read_b128 a[196:199], v244 offset:54272\n"
        "v_cvt_pk_bf16_f32 v87, 0, v87\n"
        "v_mfma_f32_16x16x32_bf16 v[52:55], a[84:87], a[192:195], v[52:55]\n"
        "ds_read_b128 a[192:195], v244 offset:50176\n"
        "s_waitcnt lgkmcnt(7)\n"
        "v_mfma_f32_16x16x32_bf16 v[60:63], a[84:87], a[208:211], v[60:63]\n"
        "ds_read_b128 a[204:207], v244 offset:62464\n"
        "v_mfma_f32_16x16x32_bf16 v[60:63], a[92:95], a[212:215], v[60:63]\n"
        "s_barrier\n"
        "v_mfma_f32_16x16x32_bf16 v[60:63], a[164:167], a[216:219], v[60:63]\n"
        "ds_read_b128 a[16:19], v244 offset:0\n"
        "v_lshlrev_b32 v88, 16, v108\n"
        "v_mfma_f32_16x16x32_bf16 v[60:63], a[172:175], a[220:223], v[60:63]\n"
        "ds_read_b128 a[20:23], v244 offset:4096\n"
        "v_and_b32 v89, s35, v108\n"
        "v_mfma_f32_16x16x32_bf16 v[56:59], a[168:171], a[220:223], v[56:59]\n"
        "ds_read_b128 a[24:27], v244 offset:8192\n"
        "v_lshlrev_b32 v90, 16, v109\n"
        "v_mfma_f32_16x16x32_bf16 v[56:59], a[160:163], a[216:219], v[56:59]\n"
        "ds_read_b128 a[28:31], v244 offset:12288\n"
        "v_and_b32 v91, s35, v109\n"
        "v_mfma_f32_16x16x32_bf16 v[56:59], a[88:91], a[212:215], v[56:59]\n"
        "ds_read_b128 a[212:215], v244 offset:5120\n"
        "v_lshlrev_b32 v246, 16, v140\n"
        "v_mfma_f32_16x16x32_bf16 v[56:59], a[80:83], a[208:211], v[56:59]\n"
        "ds_read_b128 a[208:211], v244 offset:1024\n"
        "s_waitcnt lgkmcnt(7)\n"
        "v_mfma_f32_16x16x32_bf16 v[32:35], a[176:179], a[0:3], v[32:35]\n"
        "ds_read_b128 a[216:219], v244 offset:9216\n"
        "v_and_b32 v247, s35, v140\n"
        "v_mfma_f32_16x16x32_bf16 v[32:35], a[184:187], a[4:7], v[32:35]\n"
        "ds_read_b128 a[220:223], v244 offset:13312\n"
        "v_lshlrev_b32 v248, 16, v141\n"
        "v_mfma_f32_16x16x32_bf16 v[32:35], a[32:35], a[8:11], v[32:35]\n"
        "v_and_b32 v249, s35, v141\n"
        "v_mul_f32 v88, v88, v246\n"
        "v_mfma_f32_16x16x32_bf16 v[32:35], a[40:43], a[12:15], v[32:35]\n"
        "v_mul_f32 v89, v89, v247\n"
        "v_mul_f32 v90, v90, v248\n"
        "v_mfma_f32_16x16x32_bf16 v[36:39], a[44:47], a[12:15], v[36:39]\n"
        "ds_read_b128 a[12:15], v244 offset:63488\n"
        "v_mul_f32 v91, v91, v249\n"
        "v_mfma_f32_16x16x32_bf16 v[36:39], a[36:39], a[8:11], v[36:39]\n"
        "ds_read_b128 a[8:11], v244 offset:59392\n"
        "v_cvt_pk_bf16_f32 v88, 0, v88\n"
        "v_mfma_f32_16x16x32_bf16 v[36:39], a[188:191], a[4:7], v[36:39]\n"
        "ds_read_b128 a[4:7], v244 offset:55296\n"
        "v_cvt_pk_bf16_f32 v89, 0, v89\n"
        "v_mfma_f32_16x16x32_bf16 v[36:39], a[180:183], a[0:3], v[36:39]\n"
        "ds_read_b128 a[0:3], v244 offset:51200\n"
        "s_waitcnt lgkmcnt(6)\n"
        "v_mfma_f32_16x16x32_bf16 v[32:35], a[48:51], a[16:19], v[32:35]\n"
        "v_cvt_pk_bf16_f32 v90, 0, v90\n"
        "v_cvt_pk_bf16_f32 v91, 0, v91\n"
        "v_mfma_f32_16x16x32_bf16 v[32:35], a[56:59], a[20:23], v[32:35]\n"
        "v_mul_f32 v88, v88, v231\n"
        "v_mul_f32 v89, v89, v231\n"
        "v_mfma_f32_16x16x32_bf16 v[32:35], a[224:227], a[24:27], v[32:35]\n"
        "v_mul_f32 v90, v90, v231\n"
        "v_mul_f32 v91, v91, v231\n"
        "v_mfma_f32_16x16x32_bf16 v[32:35], a[232:235], a[28:31], v[32:35]\n"
        "v_cvt_pk_bf16_f32 v88, 0, v88\n"
        "v_cvt_pk_bf16_f32 v89, 0, v89\n"
        "v_mfma_f32_16x16x32_bf16 v[36:39], a[236:239], a[28:31], v[36:39]\n"
        "ds_read_b128 a[28:31], v244 offset:14336\n"
        "v_cvt_pk_bf16_f32 v90, 0, v90\n"
        "v_mfma_f32_16x16x32_bf16 v[36:39], a[228:231], a[24:27], v[36:39]\n"
        "ds_read_b128 a[24:27], v244 offset:10240\n"
        "v_cvt_pk_bf16_f32 v91, 0, v91\n"
        "v_mfma_f32_16x16x32_bf16 v[36:39], a[60:63], a[20:23], v[36:39]\n"
        "ds_read_b128 a[20:23], v244 offset:6144\n"
        "v_lshlrev_b32 v92, 16, v110\n"
        "v_mfma_f32_16x16x32_bf16 v[36:39], a[52:55], a[16:19], v[36:39]\n"
        "ds_read_b128 a[16:19], v244 offset:2048\n"
        "s_waitcnt vmcnt(11) lgkmcnt(4)\n"
        "v_mfma_f32_16x16x32_bf16 v[32:35], v[240:243], a[240:243], v[32:35]\n"
        "v_and_b32 v93, s35, v110\n"
        "v_lshlrev_b32 v94, 16, v111\n"
        "v_mfma_f32_16x16x32_bf16 v[36:39], v[238:241], a[240:243], v[36:39]\n"
        "s_waitcnt vmcnt(7)\n"
        "v_and_b32 v95, s35, v111\n"
        "v_lshlrev_b32 v144, 16, v142\n"
        "v_mfma_f32_16x16x32_bf16 v[32:35], v[240:243], v[0:3], v[32:35]\n"
        "v_and_b32 v145, s35, v142\n"
        "v_lshlrev_b32 v146, 16, v143\n"
        "v_mfma_f32_16x16x32_bf16 v[36:39], v[238:241], v[0:3], v[36:39]\n"
        "v_and_b32 v147, s35, v143\n"
        "v_mul_f32 v92, v92, v144\n"
        "v_mfma_f32_16x16x32_bf16 v[40:43], a[176:179], a[192:195], v[40:43]\n"
        "v_mul_f32 v93, v93, v145\n"
        "s_waitcnt vmcnt(3) lgkmcnt(3)\n"
        "v_mfma_f32_16x16x32_bf16 v[32:35], v[240:243], v[16:19], v[32:35]\n"
        "v_mul_f32 v94, v94, v146\n"
        "v_mul_f32 v95, v95, v147\n"
        "v_mfma_f32_16x16x32_bf16 v[36:39], v[238:241], v[16:19], v[36:39]\n"
        "v_cvt_pk_bf16_f32 v92, 0, v92\n"
        "v_cvt_pk_bf16_f32 v93, 0, v93\n"
        "v_mfma_f32_16x16x32_bf16 v[40:43], a[184:187], a[196:199], v[40:43]\n"
        "v_cvt_pk_bf16_f32 v94, 0, v94\n"
        "v_cvt_pk_bf16_f32 v95, 0, v95\n"
        "v_mfma_f32_16x16x32_bf16 v[40:43], a[32:35], a[200:203], v[40:43]\n"
        "v_fmac_f32 v64, v32, v224\n"
        "v_fmac_f32 v65, v33, v224\n"
        "v_mfma_f32_16x16x32_bf16 v[40:43], a[40:43], a[204:207], v[40:43]\n"
        "v_fmac_f32 v66, v34, v224\n"
        "v_fmac_f32 v67, v35, v224\n"
        "v_mfma_f32_16x16x32_bf16 v[44:47], a[44:47], a[204:207], v[44:47]\n"
        "ds_read_b128 a[204:207], v244 offset:64512\n"
        "v_fmac_f32 v68, v36, v224\n"
        "v_mfma_f32_16x16x32_bf16 v[44:47], a[36:39], a[200:203], v[44:47]\n"
        "ds_read_b128 a[200:203], v244 offset:60416\n"
        "v_fmac_f32 v69, v37, v224\n"
        "v_mfma_f32_16x16x32_bf16 v[44:47], a[188:191], a[196:199], v[44:47]\n"
        "ds_read_b128 a[196:199], v244 offset:56320\n"
        "v_fmac_f32 v70, v38, v224\n"
        "v_mfma_f32_16x16x32_bf16 v[44:47], a[180:183], a[192:195], v[44:47]\n"
        "ds_read_b128 a[192:195], v244 offset:52224\n"
        "v_fmac_f32 v71, v39, v224\n"
        "v_mfma_f32_16x16x32_bf16 v[40:43], a[48:51], a[208:211], v[40:43]\n"
        "v_cvt_pk_bf16_f32 v64, v64, v65\n"
        "v_cvt_pk_bf16_f32 v65, v66, v67\n"
        "v_mfma_f32_16x16x32_bf16 v[40:43], a[56:59], a[212:215], v[40:43]\n"
        "v_cvt_pk_bf16_f32 v66, v68, v69\n"
        "v_cvt_pk_bf16_f32 v67, v70, v71\n"
        "v_mfma_f32_16x16x32_bf16 v[40:43], a[224:227], a[216:219], v[40:43]\n"
        "v_permlane16_swap_b32 v64, v66\n"
        "v_mfma_f32_16x16x32_bf16 v[40:43], a[232:235], a[220:223], v[40:43]\n"
        "v_permlane16_swap_b32 v65, v67\n"
        "v_mfma_f32_16x16x32_bf16 v[44:47], a[236:239], a[220:223], v[44:47]\n"
        "global_store_dwordx4 v151, v[64:67], s[56:57] sc0 sc1\n"
        "ds_read_b128 a[220:223], v244 offset:15360\n"
        "s_add_u32 s56, s56, 0x8000\n"
        "s_addc_u32 s57, s57, 0\n"
        "v_mul_f32 v92, v92, v231\n"
        "v_mul_f32 v93, v93, v231\n"
        "v_mfma_f32_16x16x32_bf16 v[44:47], a[228:231], a[216:219], v[44:47]\n"
        "ds_read_b128 a[216:219], v244 offset:11264\n"
        "v_mul_f32 v94, v94, v231\n"
        "v_mfma_f32_16x16x32_bf16 v[44:47], a[60:63], a[212:215], v[44:47]\n"
        "ds_read_b128 a[212:215], v244 offset:7168\n"
        "v_mul_f32 v95, v95, v231\n"
        "v_mfma_f32_16x16x32_bf16 v[44:47], a[52:55], a[208:211], v[44:47]\n"
        "ds_read_b128 a[208:211], v244 offset:3072\n"
        "v_cvt_pk_bf16_f32 v92, 0, v92\n"
        "v_mfma_f32_16x16x32_bf16 v[40:43], v[240:243], a[244:247], v[40:43]\n"
        "v_cvt_pk_bf16_f32 v93, 0, v93\n"
        "v_cvt_pk_bf16_f32 v94, 0, v94\n"
        "v_mfma_f32_16x16x32_bf16 v[44:47], v[238:241], a[244:247], v[44:47]\n"
        "v_cvt_pk_bf16_f32 v95, 0, v95\n"
        "v_mov_b32 v144, 0\n"
        "v_mfma_f32_16x16x32_bf16 v[40:43], v[240:243], v[4:7], v[40:43]\n"
        "v_mfma_f32_16x16x32_bf16 v[44:47], v[238:241], v[4:7], v[44:47]\n"
        "s_waitcnt vmcnt(3) lgkmcnt(4)\n"
        "v_mfma_f32_16x16x32_bf16 v[40:43], v[240:243], v[20:23], v[40:43]\n"
        "v_mfma_f32_16x16x32_bf16 v[44:47], v[238:241], v[20:23], v[44:47]\n"
        "v_mfma_f32_16x16x32_bf16 v[48:51], a[176:179], a[0:3], v[48:51]\n"
        "v_mfma_f32_16x16x32_bf16 v[48:51], a[184:187], a[4:7], v[48:51]\n"
        "v_mfma_f32_16x16x32_bf16 v[48:51], a[32:35], a[8:11], v[48:51]\n"
        "v_mfma_f32_16x16x32_bf16 v[48:51], a[40:43], a[12:15], v[48:51]\n"
        "v_mfma_f32_16x16x32_bf16 v[52:55], a[44:47], a[12:15], v[52:55]\n"
        "v_mfma_f32_16x16x32_bf16 v[52:55], a[36:39], a[8:11], v[52:55]\n"
        "v_mfma_f32_16x16x32_bf16 v[52:55], a[188:191], a[4:7], v[52:55]\n"
        "v_fmac_f32 v72, v40, v225\n"
        "v_fmac_f32 v73, v41, v225\n"
        "v_mfma_f32_16x16x32_bf16 v[52:55], a[180:183], a[0:3], v[52:55]\n"
        "v_fmac_f32 v74, v42, v225\n"
        "v_fmac_f32 v75, v43, v225\n"
        "v_mfma_f32_16x16x32_bf16 v[48:51], a[48:51], a[16:19], v[48:51]\n"
        "v_fmac_f32 v76, v44, v225\n"
        "v_fmac_f32 v77, v45, v225\n"
        "v_mfma_f32_16x16x32_bf16 v[48:51], a[56:59], a[20:23], v[48:51]\n"
        "v_fmac_f32 v78, v46, v225\n"
        "v_fmac_f32 v79, v47, v225\n"
        "v_mfma_f32_16x16x32_bf16 v[48:51], a[224:227], a[24:27], v[48:51]\n"
        "v_cvt_pk_bf16_f32 v72, v72, v73\n"
        "v_cvt_pk_bf16_f32 v73, v74, v75\n"
        "v_mfma_f32_16x16x32_bf16 v[48:51], a[232:235], a[28:31], v[48:51]\n"
        "v_cvt_pk_bf16_f32 v74, v76, v77\n"
        "v_cvt_pk_bf16_f32 v75, v78, v79\n"
        "v_mfma_f32_16x16x32_bf16 v[52:55], a[236:239], a[28:31], v[52:55]\n"
        "v_permlane16_swap_b32 v72, v74\n"
        "v_mfma_f32_16x16x32_bf16 v[52:55], a[228:231], a[24:27], v[52:55]\n"
        "v_permlane16_swap_b32 v73, v75\n"
        "v_mfma_f32_16x16x32_bf16 v[52:55], a[60:63], a[20:23], v[52:55]\n"
        "global_store_dwordx4 v151, v[72:75], s[56:57] sc0 sc1\n"
        "s_add_u32 s56, s56, 0x8000\n"
        "s_addc_u32 s57, s57, 0\n"
        "v_mfma_f32_16x16x32_bf16 v[52:55], a[52:55], a[16:19], v[52:55]\n"
        "v_mfma_f32_16x16x32_bf16 v[48:51], v[240:243], a[248:251], v[48:51]\n"
        "v_mfma_f32_16x16x32_bf16 v[52:55], v[238:241], a[248:251], v[52:55]\n"
        "v_mfma_f32_16x16x32_bf16 v[48:51], v[240:243], v[8:11], v[48:51]\n"
        "v_mfma_f32_16x16x32_bf16 v[52:55], v[238:241], v[8:11], v[52:55]\n"
        "s_waitcnt vmcnt(3) lgkmcnt(0)\n"
        "v_mfma_f32_16x16x32_bf16 v[48:51], v[240:243], v[24:27], v[48:51]\n"
        "v_mfma_f32_16x16x32_bf16 v[52:55], v[238:241], v[24:27], v[52:55]\n"
        "v_mfma_f32_16x16x32_bf16 v[56:59], a[176:179], a[192:195], v[56:59]\n"
        "v_mfma_f32_16x16x32_bf16 v[56:59], a[184:187], a[196:199], v[56:59]\n"
        "v_mfma_f32_16x16x32_bf16 v[56:59], a[32:35], a[200:203], v[56:59]\n"
        "v_mfma_f32_16x16x32_bf16 v[56:59], a[40:43], a[204:207], v[56:59]\n"
        "v_mfma_f32_16x16x32_bf16 v[60:63], a[44:47], a[204:207], v[60:63]\n"
        "v_mfma_f32_16x16x32_bf16 v[60:63], a[36:39], a[200:203], v[60:63]\n"
        "v_mfma_f32_16x16x32_bf16 v[60:63], a[188:191], a[196:199], v[60:63]\n"
        "v_fmac_f32 v80, v48, v226\n"
        "v_fmac_f32 v81, v49, v226\n"
        "v_mfma_f32_16x16x32_bf16 v[60:63], a[180:183], a[192:195], v[60:63]\n"
        "v_fmac_f32 v82, v50, v226\n"
        "v_fmac_f32 v83, v51, v226\n"
        "v_mfma_f32_16x16x32_bf16 v[56:59], a[48:51], a[208:211], v[56:59]\n"
        "v_fmac_f32 v84, v52, v226\n"
        "v_fmac_f32 v85, v53, v226\n"
        "v_mfma_f32_16x16x32_bf16 v[56:59], a[56:59], a[212:215], v[56:59]\n"
        "v_fmac_f32 v86, v54, v226\n"
        "v_fmac_f32 v87, v55, v226\n"
        "v_mfma_f32_16x16x32_bf16 v[56:59], a[224:227], a[216:219], v[56:59]\n"
        "v_cvt_pk_bf16_f32 v80, v80, v81\n"
        "v_cvt_pk_bf16_f32 v81, v82, v83\n"
        "v_mfma_f32_16x16x32_bf16 v[56:59], a[232:235], a[220:223], v[56:59]\n"
        "v_cvt_pk_bf16_f32 v82, v84, v85\n"
        "v_cvt_pk_bf16_f32 v83, v86, v87\n"
        "v_mfma_f32_16x16x32_bf16 v[60:63], a[236:239], a[220:223], v[60:63]\n"
        "v_permlane16_swap_b32 v80, v82\n"
        "v_mfma_f32_16x16x32_bf16 v[60:63], a[228:231], a[216:219], v[60:63]\n"
        "v_permlane16_swap_b32 v81, v83\n"
        "v_mfma_f32_16x16x32_bf16 v[60:63], a[60:63], a[212:215], v[60:63]\n"
        "global_store_dwordx4 v151, v[80:83], s[56:57] sc0 sc1\n"
        "s_add_u32 s56, s56, 0x8000\n"
        "s_addc_u32 s57, s57, 0\n"
        "v_mfma_f32_16x16x32_bf16 v[60:63], a[52:55], a[208:211], v[60:63]\n"
        "v_mfma_f32_16x16x32_bf16 v[56:59], v[240:243], a[252:255], v[56:59]\n"
        "v_mfma_f32_16x16x32_bf16 v[60:63], v[238:241], a[252:255], v[60:63]\n"
        "v_mfma_f32_16x16x32_bf16 v[56:59], v[240:243], v[12:15], v[56:59]\n"
        "v_mfma_f32_16x16x32_bf16 v[60:63], v[238:241], v[12:15], v[60:63]\n"
        "s_waitcnt vmcnt(3)\n"
        "v_mfma_f32_16x16x32_bf16 v[56:59], v[240:243], v[28:31], v[56:59]\n"
        "v_mfma_f32_16x16x32_bf16 v[60:63], v[238:241], v[28:31], v[60:63]\n"
        "s_nop 6\n"
        "v_fmac_f32 v88, v56, v227\n"
        "v_fmac_f32 v89, v57, v227\n"
        "v_fmac_f32 v90, v58, v227\n"
        "v_fmac_f32 v91, v59, v227\n"
        "v_fmac_f32 v92, v60, v227\n"
        "v_fmac_f32 v93, v61, v227\n"
        "v_fmac_f32 v94, v62, v227\n"
        "v_fmac_f32 v95, v63, v227\n"
        "v_cvt_pk_bf16_f32 v88, v88, v89\n"
        "v_cvt_pk_bf16_f32 v89, v90, v91\n"
        "v_cvt_pk_bf16_f32 v90, v92, v93\n"
        "v_cvt_pk_bf16_f32 v91, v94, v95\n"
        "s_nop 0\n"
        "v_permlane16_swap_b32 v88, v90\n"
        "v_permlane16_swap_b32 v89, v91\n"
        "global_store_dwordx4 v151, v[88:91], s[56:57] sc0 sc1\n"
        "s_or_b32 s97, s55, s40\n"
        "s_cbranch_scc1 LEB_%=\n"
        "s_mov_b64 exec, 1\n"
        "global_store_dwordx2 v144, v[250:251], s[48:49] sc1\n"
        "s_mov_b64 exec, -1\n"
        "LEB_%=:\n"
        "; ---- END\n"
        "s_endpgm\n"
        "LPKV_%=:\n"
        "s_mov_b32 s97, 0x200000\n"
        "LQKV_%=:\n"
        "global_load_dwordx2 v[252:253], v245, s[48:49] sc1\n"
        "s_waitcnt vmcnt(0)\n"
        "v_cmp_ne_u64 vcc, v[252:253], v[250:251]\n"
        "s_nop 1\n"
        "s_cbranch_vccz LBKV_%=\n"
        "s_sleep 2\n"
        "s_sub_u32 s97, s97, 1\n"
        "s_cmp_lg_u32 s97, 0\n"
        "s_cbranch_scc1 LQKV_%=\n"
        "s_branch LBKV_%=\n"
        "LRL_%=:\n"
        "s_waitcnt vmcnt(0) lgkmcnt(0)\n"
        "s_nop 7\n"
        "v_accvgpr_write_b32 a176, v96\n"
        "v_accvgpr_write_b32 a177, v97\n"
        "v_accvgpr_write_b32 a178, v98\n"
        "v_accvgpr_write_b32 a179, v99\n"
        "v_accvgpr_write_b32 a180, v100\n"
        "v_accvgpr_write_b32 a181, v101\n"
        "v_accvgpr_write_b32 a182, v102\n"
        "v_accvgpr_write_b32 a183, v103\n"
        "v_accvgpr_write_b32 a184, v104\n"
        "v_accvgpr_write_b32 a185, v105\n"
        "v_accvgpr_write_b32 a186, v106\n"
        "v_accvgpr_write_b32 a187, v107\n"
        "v_accvgpr_write_b32 a188, v108\n"
        "v_accvgpr_write_b32 a189, v109\n"
        "v_accvgpr_write_b32 a190, v110\n"
        "v_accvgpr_write_b32 a191, v111\n"
        "s_mov_b32 s53, s44\n"
        "s_mov_b32 s96, 64\n"
        "s_mov_b32 s52, 0\n"
        "s_branch LREDO_%=\n"
        "LRET0_%=:\n"
        "v_accvgpr_write_b32 a176, v128\n"
        "v_accvgpr_write_b32 a177, v129\n"
        "v_accvgpr_write_b32 a178, v130\n"
        "v_accvgpr_write_b32 a179, v131\n"
        "v_accvgpr_write_b32 a180, v132\n"
        "v_accvgpr_write_b32 a181, v133\n"
        "v_accvgpr_write_b32 a182, v134\n"
        "v_accvgpr_write_b32 a183, v135\n"
        "v_accvgpr_write_b32 a184, v136\n"
        "v_accvgpr_write_b32 a185, v137\n"
        "v_accvgpr_write_b32 a186, v138\n"
        "v_accvgpr_write_b32 a187, v139\n"
        "v_accvgpr_write_b32 a188, v140\n"
        "v_accvgpr_write_b32 a189, v141\n"
        "v_accvgpr_write_b32 a190, v142\n"
        "v_accvgpr_write_b32 a191, v143\n"
        "s_mov_b32 s53, s44\n"
        "s_mov_b32 s96, 0\n"
        "s_mov_b32 s52, 1\n"
        "s_branch LREDO_%=\n"
        "LRET1_%=:\n"
        "s_branch LSL_%=\n"
        "LRH_%=:\n"
        "s_waitcnt vmcnt(0) lgkmcnt(0)\n"
        "s_nop 7\n"
        "v_accvgpr_write_b32 a176, v0\n"
        "v_accvgpr_write_b32 a177, v1\n"
        "v_accvgpr_write_b32 a178, v2\n"
        "v_accvgpr_write_b32 a179, v3\n"
        "v_accvgpr_write_b32 a180, v4\n"
        "v_accvgpr_write_b32 a181, v5\n"
        "v_accvgpr_write_b32 a182, v6\n"
        "v_accvgpr_write_b32 a183, v7\n"
        "v_accvgpr_write_b32 a184, v8\n"
        "v_accvgpr_write_b32 a185, v9\n"
        "v_accvgpr_write_b32 a186, v10\n"
        "v_accvgpr_write_b32 a187, v11\n"
        "v_accvgpr_write_b32 a188, v12\n"
        "v_accvgpr_write_b32 a189, v13\n"
        "v_accvgpr_write_b32 a190, v14\n"
        "v_accvgpr_write_b32 a191, v15\n"
        "s_mov_b32 s53, s45\n"
        "s_mov_b32 s96, 64\n"
        "s_mov_b32 s52, 2\n"
        "s_branch LREDO_%=\n"
        "LRET2_%=:\n"
        "v_accvgpr_write_b32 a176, v64\n"
        "v_accvgpr_write_b32 a177, v65\n"
        "v_accvgpr_write_b32 a178, v66\n"
        "v_accvgpr_write_b32 a179, v67\n"
        "v_accvgpr_write_b32 a180, v68\n"
        "v_accvgpr_write_b32 a181, v69\n"
        "v_accvgpr_write_b32 a182, v70\n"
        "v_accvgpr_write_b32 a183, v71\n"
        "v_accvgpr_write_b32 a184, v72\n"
        "v_accvgpr_write_b32 a185, v73\n"
        "v_accvgpr_write_b32 a186, v74\n"
        "v_accvgpr_write_b32 a187, v75\n"
        "v_accvgpr_write_b32 a188, v76\n"
        "v_accvgpr_write_b32 a189, v77\n"
        "v_accvgpr_write_b32 a190, v78\n"
        "v_accvgpr_write_b32 a191, v79\n"
        "s_mov_b32 s53, s45\n"
        "s_mov_b32 s96, 0\n"
        "s_mov_b32 s52, 3\n"
        "s_branch LREDO_%=\n"
        "LRET3_%=:\n"
        "s_branch LSH_%=\n"
        "LPCX_%=:\n"
        "s_mov_b32 s97, 0x200000\n"
        "LQCX_%=:\n"
        "global_load_dwordx2 v[236:237], v159, s[48:49] sc1\n"
        "s_waitcnt vmcnt(0)\n"
        "v_cmp_ne_u64 vcc, v[236:237], v[250:251]\n"
        "s_nop 1\n"
        "s_cbranch_vccz LBCX_%=\n"
        "s_sleep 2\n"
        "s_sub_u32 s97, s97, 1\n"
        "s_cmp_lg_u32 s97, 0\n"
        "s_cbranch_scc1 LQCX_%=\n"
        "s_branch LBCX_%=\n"
        "LPZZ_%=:\n"
        "s_mov_b32 s97, 0x200000\n"
        "LQZZ_%=:\n"
        "global_load_dwordx2 v[236:237], v234, s[48:49] sc1\n"
        "s_waitcnt vmcnt(0)\n"
        "v_cmp_ne_u64 vcc, v[236:237], v[250:251]\n"
        "s_nop 1\n"
        "s_cbranch_vccz LBZZ_%=\n"
        "s_sleep 2\n"
        "s_sub_u32 s97, s97, 1\n"
        "s_cmp_lg_u32 s97, 0\n"
        "s_cbranch_scc1 LQZZ_%=\n"
        "s_branch LBZZ_%=\n"
        "LPPR_%=:\n"
        "s_mov_b32 s97, 0x200000\n"
        "LQPR_%=:\n"
        "global_load_dwordx2 v[156:157], v155, s[48:49] sc1\n"
        "s_waitcnt vmcnt(0)\n"
        "v_cmp_ne_u64 vcc, v[156:157], v[250:251]\n"
        "s_nop 1\n"
        "s_cbranch_vccz LBPR_%=\n"
        "s_sleep 2\n"
        "s_sub_u32 s97, s97, 1\n"
        "s_cmp_lg_u32 s97, 0\n"
        "s_cbranch_scc1 LQPR_%=\n"
        "s_branch LBPR_%=\n"
        "LREDO_%=:\n"
        "s_lshl2_add_u32 s97, s55, s40\n"
        "s_lshl_b32 s97, s97, 12\n"
        "s_add_u32 s97, s97, 0x5100000\n"
        "s_add_u32 s88, s38, s97\n"
        "s_addc_u32 s89, s39, 0\n"
        "global_store_dwordx4 v254, v[144:147], s[88:89] offset:0\n"
        "global_store_dwordx4 v254, v[148:151], s[88:89] offset:1024\n"
        "global_store_dwordx4 v254, v[152:155], s[88:89] offset:2048\n"
        "global_store_dwordx4 v254, v[156:159], s[88:89] offset:3072\n"
        "s_waitcnt vmcnt(0)\n"
        "v_mov_b32 v112, 0\n"
        "v_mov_b32 v113, 0\n"
        "v_mov_b32 v114, 0\n"
        "v_mov_b32 v115, 0\n"
        "v_mov_b32 v116, 0\n"
        "v_mov_b32 v117, 0\n"
        "v_mov_b32 v118, 0\n"
        "v_mov_b32 v119, 0\n"
        "v_mov_b32 v120, 0\n"
        "v_mov_b32 v121, 0\n"
        "v_mov_b32 v122, 0\n"
        "v_mov_b32 v123, 0\n"
        "v_mov_b32 v124, 0\n"
        "v_mov_b32 v125, 0\n"
        "v_mov_b32 v126, 0\n"
        "v_mov_b32 v127, 0\n"
        "v_mov_b32 v144, 0\n"
        "v_mov_b32 v145, 0\n"
        "v_mov_b32 v146, 0\n"
        "v_mov_b32 v147, 0\n"
        "v_mov_b32 v148, 0\n"
        "v_mov_b32 v149, 0\n"
        "v_mov_b32 v150, 0\n"
        "v_mov_b32 v151, 0\n"
        "v_mov_b32 v152, 0\n"
        "v_mov_b32 v153, 0\n"
        "v_mov_b32 v154, 0\n"
        "v_mov_b32 v155, 0\n"
        "v_mov_b32 v156, 0\n"
        "v_mov_b32 v157, 0\n"
        "v_mov_b32 v158, 0\n"
        "v_mov_b32 v159, 0\n"
        "v_mov_b32 v60, 0\n"
        "v_mov_b32 v61, 0\n"
        "v_mov_b32 v62, 0\n"
        "v_mov_b32 v63, 0\n"
        "v_mov_b32 v24, 0xf149f2ca\n"
        "s_lshl_b32 s97, s53, 7\n"
        "s_add_u32 s97, s97, s96\n"
        "v_add_u32 v27, s97, v243\n"
        "s_mul_i32 s97, s43, 529408\n"
        "s_add_u32 s84, s38, s97\n"
        "s_addc_u32 s85, s39, 0\n"
        "s_add_u32 s84, s84, 0x1000\n"
        "s_addc_u32 s85, s85, 0\n"
        "s_add_u32 s86, s84, 0x1100000\n"
        "s_addc_u32 s87, s85, 0\n"
        "s_lshl_b32 s97, s53, 2\n"
        "s_add_u32 s97, s97, 4\n"
        "LRLOOP_%=:\n"
        "global_load_dwordx4 a[160:163], v254, s[84:85] offset:-4096 sc1\n"
        "global_load_dwordx4 a[164:167], v254, s[84:85] offset:-3072 sc1\n"
        "global_load_dwordx4 a[168:171], v254, s[84:85] offset:-2048 sc1\n"
        "global_load_dwordx4 a[172:175], v254, s[84:85] offset:-1024 sc1\n"
        "s_waitcnt vmcnt(0)\n"
        "v_mfma_f32_16x16x32_bf16 v[40:43], a[160:163], a[176:179], 0\n"
        "v_mfma_f32_16x16x32_bf16 v[40:43], a[164:167], a[180:183], v[40:43]\n"
        "v_mfma_f32_16x16x32_bf16 v[40:43], a[168:171], a[184:187], v[40:43]\n"
        "v_mfma_f32_16x16x32_bf16 v[40:43], a[172:175], a[188:191], v[40:43]\n"
        "s_nop 7\n"
        "global_load_dwordx4 a[160:163], v254, s[84:85] offset:0 sc1\n"
        "global_load_dwordx4 a[164:167], v254, s[84:85] offset:1024 sc1\n"
        "global_load_dwordx4 a[168:171], v254, s[84:85] offset:2048 sc1\n"
        "global_load_dwordx4 a[172:175], v254, s[84:85] offset:3072 sc1\n"
        "s_waitcnt vmcnt(0)\n"
        "v_mfma_f32_16x16x32_bf16 v[44:47], a[160:163], a[176:179], 0\n"
        "v_mfma_f32_16x16x32_bf16 v[44:47], a[164:167], a[180:183], v[44:47]\n"
        "v_mfma_f32_16x16x32_bf16 v[44:47], a[168:171], a[184:187], v[44:47]\n"
        "v_mfma_f32_16x16x32_bf16 v[44:47], a[172:175], a[188:191], v[44:47]\n"
        "s_nop 7\n"
        "v_cmp_gt_i32 vcc, 0, v27\n"
        "s_nop 1\n"
        "v_cndmask_b32 v40, v40, v242, vcc\n"
        "v_cmp_gt_i32 vcc, 1, v27\n"
        "s_nop 1\n"
        "v_cndmask_b32 v41, v41, v242, vcc\n"
        "v_cmp_gt_i32 vcc, 2, v27\n"
        "s_nop 1\n"
        "v_cndmask_b32 v42, v42, v242, vcc\n"
        "v_cmp_gt_i32 vcc, 3, v27\n"
        "s_nop 1\n"
        "v_cndmask_b32 v43, v43, v242, vcc\n"
        "v_cmp_gt_i32 vcc, 16, v27\n"
        "s_nop 1\n"
        "v_cndmask_b32 v44, v44, v242, vcc\n"
        "v_cmp_gt_i32 vcc, 17, v27\n"
        "s_nop 1\n"
        "v_cndmask_b32 v45, v45, v242, vcc\n"
        "v_cmp_gt_i32 vcc, 18, v27\n"
        "s_nop 1\n"
        "v_cndmask_b32 v46, v46, v242, vcc\n"
        "v_cmp_gt_i32 vcc, 19, v27\n"
        "s_nop 1\n"
        "v_cndmask_b32 v47, v47, v242, vcc\n"
        "v_max3_f32 v25, v40, v41, v24\n"
        "v_max3_f32 v25, v42, v43, v25\n"
        "v_max3_f32 v25, v44, v45, v25\n"
        "v_max3_f32 v25, v46, v47, v25\n"
        "v_mov_b32 v28, v25\n"
        "s_nop 1\n"
        "v_permlane16_swap_b32 v25, v28\n"
        "v_max_f32 v25, v25, v28\n"
        "v_mov_b32 v28, v25\n"
        "s_nop 1\n"
        "v_permlane32_swap_b32 v25, v28\n"
        "v_max_f32 v25, v25, v28\n"
        "v_sub_f32 v26, v24, v25\n"
        "v_exp_f32 v26, v26\n"
        "v_mov_b32 v24, v25\n"
        "v_sub_f32 v40, v40, v25\n"
        "v_exp_f32 v40, v40\n"
        "v_sub_f32 v41, v41, v25\n"
        "v_exp_f32 v41, v41\n"
        "v_sub_f32 v42, v42, v25\n"
        "v_exp_f32 v42, v42\n"
        "v_sub_f32 v43, v43, v25\n"
        "v_exp_f32 v43, v43\n"
        "v_sub_f32 v44, v44, v25\n"
        "v_exp_f32 v44, v44\n"
        "v_sub_f32 v45, v45, v25\n"
        "v_exp_f32 v45, v45\n"
        "v_sub_f32 v46, v46, v25\n"
        "v_exp_f32 v46, v46\n"
        "v_sub_f32 v47, v47, v25\n"
        "v_exp_f32 v47, v47\n"
        "v_cvt_pk_bf16_f32 v56, v40, v41\n"
        "v_cvt_pk_bf16_f32 v57, v42, v43\n"
        "v_cvt_pk_bf16_f32 v58, v44, v45\n"
        "v_cvt_pk_bf16_f32 v59, v46, v47\n"
        "v_mul_f32 v112, v112, v26\n"
        "v_mul_f32 v113, v113, v26\n"
        "v_mul_f32 v114, v114, v26\n"
        "v_mul_f32 v115, v115, v26\n"
        "v_mul_f32 v116, v116, v26\n"
        "v_mul_f32 v117, v117, v26\n"
        "v_mul_f32 v118, v118, v26\n"
        "v_mul_f32 v119, v119, v26\n"
        "v_mul_f32 v120, v120, v26\n"
        "v_mul_f32 v121, v121, v26\n"
        "v_mul_f32 v122, v122, v26\n"
        "v_mul_f32 v123, v123, v26\n"
        "v_mul_f32 v124, v124, v26\n"
        "v_mul_f32 v125, v125, v26\n"
        "v_mul_f32 v126, v126, v26\n"
        "v_mul_f32 v127, v127, v26\n"
        "v_mul_f32 v144, v144, v26\n"
        "v_mul_f32 v145, v145, v26\n"
        "v_mul_f32 v146, v146, v26\n"
        "v_mul_f32 v147, v147, v26\n"
        "v_mul_f32 v148, v148, v26\n"
        "v_mul_f32 v149, v149, v26\n"
        "v_mul_f32 v150, v150, v26\n"
        "v_mul_f32 v151, v151, v26\n"
        "v_mul_f32 v152, v152, v26\n"
        "v_mul_f32 v153, v153, v26\n"
        "v_mul_f32 v154, v154, v26\n"
        "v_mul_f32 v155, v155, v26\n"
        "v_mul_f32 v156, v156, v26\n"
        "v_mul_f32 v157, v157, v26\n"
        "v_mul_f32 v158, v158, v26\n"
        "v_mul_f32 v159, v159, v26\n"
        "v_mul_f32 v60, v60, v26\n"
        "v_mul_f32 v61, v61, v26\n"
        "v_mul_f32 v62, v62, v26\n"
        "v_mul_f32 v63, v63, v26\n"
        "s_nop 1\n"
        "v_mfma_f32_16x16x32_bf16 v[60:63], a[80:83], v[56:59], v[60:63]\n"
        "global_load_dwordx4 a[160:163], v254, s[86:87] offset:-4096 sc1\n"
        "global_load_dwordx4 a[164:167], v254, s[86:87] offset:-3072 sc1\n"
        "global_load_dwordx4 a[168:171], v254, s[86:87] offset:-2048 sc1\n"
        "global_load_dwordx4 a[172:175], v254, s[86:87] offset:-1024 sc1\n"
        "s_waitcnt vmcnt(0)\n"
        "v_mfma_f32_16x16x32_bf16 v[112:115], a[160:163], v[56:59], v[112:115]\n"
        "v_mfma_f32_16x16x32_bf16 v[116:119], a[164:167], v[56:59], v[116:119]\n"
        "v_mfma_f32_16x16x32_bf16 v[120:123], a[168:171], v[56:59], v[120:123]\n"
        "v_mfma_f32_16x16x32_bf16 v[124:127], a[172:175], v[56:59], v[124:127]\n"
        "s_nop 7\n"
        "global_load_dwordx4 a[160:163], v254, s[86:87] offset:0 sc1\n"
        "global_load_dwordx4 a[164:167], v254, s[86:87] offset:1024 sc1\n"
        "global_load_dwordx4 a[168:171], v254, s[86:87] offset:2048 sc1\n"
        "global_load_dwordx4 a[172:175], v254, s[86:87] offset:3072 sc1\n"
        "s_waitcnt vmcnt(0)\n"
        "v_mfma_f32_16x16x32_bf16 v[144:147], a[160:163], v[56:59], v[144:147]\n"
        "v_mfma_f32_16x16x32_bf16 v[148:151], a[164:167], v[56:59], v[148:151]\n"
        "v_mfma_f32_16x16x32_bf16 v[152:155], a[168:171], v[56:59], v[152:155]\n"
        "v_mfma_f32_16x16x32_bf16 v[156:159], a[172:175], v[56:59], v[156:159]\n"
        "s_nop 7\n"
        "s_add_u32 s84, s84, 0x2000\n"
        "s_addc_u32 s85, s85, 0\n"
        "s_add_u32 s86, s86, 0x2000\n"
        "s_addc_u32 s87, s87, 0\n"
        "v_subrev_u32 v27, 32, v27\n"
        "s_sub_u32 s97, s97, 1\n"
        "s_cmp_lg_u32 s97, 0\n"
        "s_cbranch_scc1 LRLOOP_%=\n"
        "s_mul_i32 s97, s53, 1052672\n"
        "s_lshl_b32 s86, s43, 15\n"
        "s_add_u32 s97, s97, s86\n"
        "s_lshl_b32 s86, s40, 10\n"
        "s_add_u32 s97, s97, s86\n"
        "s_lshl_b32 s86, s96, 6\n"
        "s_add_u32 s97, s97, s86\n"
        "s_add_u32 s97, s97, 0x2200000\n"
        "s_add_u32 s84, s38, s97\n"
        "s_addc_u32 s85, s39, 0\n"
        "v_rcp_f32 v26, v60\n"
        "s_nop 0\n"
        "v_mul_f32 v112, v112, v26\n"
        "v_mul_f32 v113, v113, v26\n"
        "v_mul_f32 v114, v114, v26\n"
        "v_mul_f32 v115, v115, v26\n"
        "v_mul_f32 v116, v116, v26\n"
        "v_mul_f32 v117, v117, v26\n"
        "v_mul_f32 v118, v118, v26\n"
        "v_mul_f32 v119, v119, v26\n"
        "v_cvt_pk_bf16_f32 v112, v112, v113\n"
        "v_cvt_pk_bf16_f32 v113, v114, v115\n"
        "v_cvt_pk_bf16_f32 v114, v116, v117\n"
        "v_cvt_pk_bf16_f32 v115, v118, v119\n"
        "global_store_dwordx4 v254, v[112:115], s[84:85] sc1\n"
        "s_add_u32 s84, s84, 0x2000\n"
        "s_addc_u32 s85, s85, 0\n"
        "v_mul_f32 v120, v120, v26\n"
        "v_mul_f32 v121, v121, v26\n"
        "v_mul_f32 v122, v122, v26\n"
        "v_mul_f32 v123, v123, v26\n"
        "v_mul_f32 v124, v124, v26\n"
        "v_mul_f32 v125, v125, v26\n"
        "v_mul_f32 v126, v126, v26\n"
        "v_mul_f32 v127, v127, v26\n"
        "v_cvt_pk_bf16_f32 v120, v120, v121\n"
        "v_cvt_pk_bf16_f32 v121, v122, v123\n"
        "v_cvt_pk_bf16_f32 v122, v124, v125\n"
        "v_cvt_pk_bf16_f32 v123, v126, v127\n"
        "global_store_dwordx4 v254, v[120:123], s[84:85] sc1\n"
        "s_add_u32 s84, s84, 0x2000\n"
        "s_addc_u32 s85, s85, 0\n"
        "v_mul_f32 v144, v144, v26\n"
        "v_mul_f32 v145, v145, v26\n"
        "v_mul_f32 v146, v146, v26\n"
        "v_mul_f32 v147, v147, v26\n"
        "v_mul_f32 v148, v148, v26\n"
        "v_mul_f32 v149, v149, v26\n"
        "v_mul_f32 v150, v150, v26\n"
        "v_mul_f32 v151, v151, v26\n"
        "v_cvt_pk_bf16_f32 v144, v144, v145\n"
        "v_cvt_pk_bf16_f32 v145, v146, v147\n"
        "v_cvt_pk_bf16_f32 v146, v148, v149\n"
        "v_cvt_pk_bf16_f32 v147, v150, v151\n"
        "global_store_dwordx4 v254, v[144:147], s[84:85] sc1\n"
        "s_add_u32 s84, s84, 0x2000\n"
        "s_addc_u32 s85, s85, 0\n"
        "v_mul_f32 v152, v152, v26\n"
        "v_mul_f32 v153, v153, v26\n"
        "v_mul_f32 v154, v154, v26\n"
        "v_mul_f32 v155, v155, v26\n"
        "v_mul_f32 v156, v156, v26\n"
        "v_mul_f32 v157, v157, v26\n"
        "v_mul_f32 v158, v158, v26\n"
        "v_mul_f32 v159, v159, v26\n"
        "v_cvt_pk_bf16_f32 v152, v152, v153\n"
        "v_cvt_pk_bf16_f32 v153, v154, v155\n"
        "v_cvt_pk_bf16_f32 v154, v156, v157\n"
        "v_cvt_pk_bf16_f32 v155, v158, v159\n"
        "global_store_dwordx4 v254, v[152:155], s[84:85] sc1\n"
        "s_add_u32 s84, s84, 0x2000\n"
        "s_addc_u32 s85, s85, 0\n"
        "s_waitcnt vmcnt(0)\n"
        "global_load_dwordx4 v[144:147], v254, s[88:89] offset:0 sc1\n"
        "global_load_dwordx4 v[148:151], v254, s[88:89] offset:1024 sc1\n"
        "global_load_dwordx4 v[152:155], v254, s[88:89] offset:2048 sc1\n"
        "global_load_dwordx4 v[156:159], v254, s[88:89] offset:3072 sc1\n"
        "s_waitcnt vmcnt(0)\n"
        "s_cmp_eq_u32 s52, 0\n"
        "s_cbranch_scc1 LRET0_%=\n"
        "s_cmp_eq_u32 s52, 1\n"
        "s_cbranch_scc1 LRET1_%=\n"
        "s_cmp_eq_u32 s52, 2\n"
        "s_cbranch_scc1 LRET2_%=\n"
        "s_branch LRET3_%=\n"
        "LEND_%=:\n"
        ".p2align 12\n"
        :
        : "s"(__builtin_amdgcn_kernarg_segment_ptr()), "s"(blockIdx.x), "s"(__builtin_amdgcn_readfirstlane(threadIdx.x >> 6))
        : "v0", "v1", "v2", "v3", "v4", "v5", "v6", "v7", "v8", "v9", "v10", "v11", "v12", "v13", "v14", "v15", "v16", "v17", "v18", "v19", "v20", "v21", "v22", "v23", "v24", "v25", "v26", "v27", "v28", "v29", "v30", "v31",
        "v32", "v33", "v34", "v35", "v36", "v37", "v38", "v39", "v40", "v41", "v42", "v43", "v44", "v45", "v46", "v47", "v48", "v49", "v50", "v51", "v52", "v53", "v54", "v55", "v56", "v57", "v58", "v59", "v60", "v61", "v62", "v63",
        "v64", "v65", "v66", "v67", "v68", "v69", "v70", "v71", "v72", "v73", "v74", "v75", "v76", "v77", "v78", "v79", "v80", "v81", "v82", "v83", "v84", "v85", "v86", "v87", "v88", "v89", "v90", "v91", "v92", "v93", "v94", "v95",
        "v96", "v97", "v98", "v99", "v100", "v101", "v102", "v103", "v104", "v105", "v106", "v107", "v108", "v109", "v110", "v111", "v112", "v113", "v114", "v115", "v116", "v117", "v118", "v119", "v120", "v121", "v122", "v123", "v124", "v125", "v126", "v127",
        "v128", "v129", "v130", "v131", "v132", "v133", "v134", "v135", "v136", "v137", "v138", "v139", "v140", "v141", "v142", "v143", "v144", "v145", "v146", "v147", "v148", "v149", "v150", "v151", "v152", "v153", "v154", "v155", "v156", "v157", "v158", "v159",
        "v160", "v161", "v162", "v163", "v164", "v165", "v166", "v167", "v168", "v169", "v170", "v171", "v172", "v173", "v174", "v175", "v176", "v177", "v178", "v179", "v180", "v181", "v182", "v183", "v184", "v185", "v186", "v187", "v188", "v189", "v190", "v191",
        "v192", "v193", "v194", "v195", "v196", "v197", "v198", "v199", "v200", "v201", "v202", "v203", "v204", "v205", "v206", "v207", "v208", "v209", "v210", "v211", "v212", "v213", "v214", "v215", "v216", "v217", "v218", "v219", "v220", "v221", "v222", "v223",
        "v224", "v225", "v226", "v227", "v228", "v229", "v230", "v231", "v232", "v233", "v234", "v235", "v236", "v237", "v238", "v239", "v240", "v241", "v242", "v243", "v244", "v245", "v246", "v247", "v248", "v249", "v250", "v251", "v252", "v253", "v254", "v255",
        "a0", "a1", "a2", "a3", "a4", "a5", "a6", "a7", "a8", "a9", "a10", "a11", "a12", "a13", "a14", "a15", "a16", "a17", "a18", "a19", "a20", "a21", "a22", "a23", "a24", "a25", "a26", "a27", "a28", "a29", "a30", "a31",
        "a32", "a33", "a34", "a35", "a36", "a37", "a38", "a39", "a40", "a41", "a42", "a43", "a44", "a45", "a46", "a47", "a48", "a49", "a50", "a51", "a52", "a53", "a54", "a55", "a56", "a57", "a58", "a59", "a60", "a61", "a62", "a63",
        "a64", "a65", "a66", "a67", "a68", "a69", "a70", "a71", "a72", "a73", "a74", "a75", "a76", "a77", "a78", "a79", "a80", "a81", "a82", "a83", "a84", "a85", "a86", "a87", "a88", "a89", "a90", "a91", "a92", "a93", "a94", "a95",
        "a96", "a97", "a98", "a99", "a100", "a101", "a102", "a103", "a104", "a105", "a106", "a107", "a108", "a109", "a110", "a111", "a112", "a113", "a114", "a115", "a116", "a117", "a118", "a119", "a120", "a121", "a122", "a123", "a124", "a125", "a126", "a127",
        "a128", "a129", "a130", "a131", "a132", "a133", "a134", "a135", "a136", "a137", "a138", "a139", "a140", "a141", "a142", "a143", "a144", "a145", "a146", "a147", "a148", "a149", "a150", "a151", "a152", "a153", "a154", "a155", "a156", "a157", "a158", "a159",
        "a160", "a161", "a162", "a163", "a164", "a165", "a166", "a167", "a168", "a169", "a170", "a171", "a172", "a173", "a174", "a175", "a176", "a177", "a178", "a179", "a180", "a181", "a182", "a183", "a184", "a185", "a186", "a187", "a188", "a189", "a190", "a191",
        "a192", "a193", "a194", "a195", "a196", "a197", "a198", "a199", "a200", "a201", "a202", "a203", "a204", "a205", "a206", "a207", "a208", "a209", "a210", "a211", "a212", "a213", "a214", "a215", "a216", "a217", "a218", "a219", "a220", "a221", "a222", "a223",
        "a224", "a225", "a226", "a227", "a228", "a229", "a230", "a231", "a232", "a233", "a234", "a235", "a236", "a237", "a238", "a239", "a240", "a241", "a242", "a243", "a244", "a245", "a246", "a247", "a248", "a249", "a250", "a251", "a252", "a253", "a254", "a255",
        "s8", "s9", "s10", "s11", "s12", "s13", "s14", "s15", "s16", "s17", "s18", "s19", "s20", "s21", "s22", "s23", "s24", "s25", "s26", "s27", "s28", "s29", "s30", "s31", "s34", "s35", "s36", "s37", "s38", "s39", "s40", "s41",
        "s42", "s43", "s44", "s45", "s46", "s47", "s48", "s49", "s50", "s51", "s52", "s53", "s54", "s55", "s56", "s57", "s58", "s59", "s60", "s61", "s62", "s63", "s64", "s65", "s66", "s67", "s68", "s69", "s70", "s71", "s72", "s73",
        "s74", "s75", "s76", "s77", "s78", "s79", "s80", "s81", "s82", "s83", "s84", "s85", "s86", "s87", "s88", "s89", "s90", "s91", "s92", "s93", "s94", "s95", "s96", "s97", "s98", "s99", "vcc", "scc", "memory");
}

extern "C" void kernel_launch(void* const* d_in, const int* in_sizes, int n_in, void* d_out, int out_size, void* d_ws, size_t ws_size, hipStream_t stream) {
    (void)in_sizes; (void)n_in; (void)out_size;
    constexpr size_t WS_USED = (size_t)85 << 20;
    Params P;
    P.x = d_in[0]; P.ckv = d_in[1]; P.wq = d_in[2]; P.wuk = d_in[3]; P.wuv = d_in[4]; P.wo = d_in[5];
    P.bk = d_in[7]; P.bv = d_in[8]; P.shk = d_in[9]; P.ssk = d_in[10]; P.ssd = d_in[11]; P.gw = d_in[12];
    P.out = d_out; P.ws = (char*)d_ws;
    if (ws_size > WS_USED) P.ws += (ws_size - WS_USED) & ~(size_t)0xfffff;
    constexpr int LDS_MAX = 163840;
    (void)hipFuncSetAttribute((const void*)k_main, hipFuncAttributeMaxDynamicSharedMemorySize, LDS_MAX);
    k_main<<<256, 256, LDS_MAX, stream>>>(P);
}
```

```cpp
#include <hip/hip_runtime.h>
#include <stdint.h>

struct Params {
    const void *x, *ckv, *wq, *wuk, *wuv, *wo, *bk, *bv, *shk, *ssk, *ssd, *gw;
    void* out; char* ws;
};

__global__ __launch_bounds__(256, 1) void k_main(Params P) {
    asm volatile(
        "; ---- PRO\n"
        "s_load_dwordx16 s[8:23], %0, 0\n"
        "s_load_dwordx8 s[24:31], %0, 64\n"
        "s_load_dwordx4 s[36:39], %0, 96\n"
        "s_mov_b32 s55, %1\n"
        "s_mov_b32 s40, %2\n"
        "v_mbcnt_lo_u32_b32 v0, -1, 0\n"
        "v_mbcnt_hi_u32_b32 v0, -1, v0\n"
        "s_getpc_b64 s[46:47]\n"
        "v_min_u32 v4, 12, v0\n"
        "v_lshlrev_b32 v4, 12, v4\n"
        "global_load_dword v253, v4, s[46:47] offset:0\n"
        "s_and_b32 s41, s55, 7\n"
        "s_lshr_b32 s42, s55, 3\n"
        "s_lshr_b32 s50, s42, 3\n"
        "s_lshl2_add_u32 s43, s41, s50\n"
        "s_and_b32 s44, s42, 7\n"
        "s_sub_u32 s45, 15, s44\n"
        "s_lshl_b32 s72, s40, 13\n"
        "s_lshl_b32 s73, s40, 12\n"
        "v_lshlrev_b32 v254, 4, v0\n"
        "v_lshrrev_b32 v2, 4, v0\n"
        "v_and_b32 v1, 15, v0\n"
        "v_lshrrev_b32 v4, 3, v0\n"
        "v_and_b32 v5, 7, v0\n"
        "v_and_b32 v8, 6, v4\n"
        "v_xor_b32 v8, v8, v5\n"
        "v_lshlrev_b32 v8, 4, v8\n"
        "v_lshl_add_u32 v9, v4, 13, v8\n"
        "v_xor_b32 v10, 4, v4\n"
        "v_lshl_add_u32 v10, v10, 13, v8\n"
        "v_add_u32 v238, 4096, v9\n"
        "v_add_u32 v239, 3200, v9\n"
        "v_add_u32 v240, 67584, v10\n"
        "v_add_u32 v241, 66688, v10\n"
        "s_cmp_ge_u32 s40, 2\n"
        "s_cselect_b32 s50, s45, s44\n"
        "s_and_b32 s51, s40, 1\n"
        "s_lshl1_add_u32 s50, s50, s51\n"
        "s_lshl_b32 s50, s50, 16\n"
        "s_lshl_b32 s51, s40, 17\n"
        "s_lshl_b32 s52, s43, 8\n"
        "s_add_u32 s51, s51, s52\n"
        "s_waitcnt lgkmcnt(0)\n"
        "s_add_u32 s60, s14, s51\n"
        "s_addc_u32 s61, s15, 0\n"
        "s_sub_u32 s60, s60, 4096\n"
        "s_subb_u32 s61, s61, 0\n"
        "s_add_u32 m0, s73, 0x10000\n"
        "s_nop 0\n"
        "global_load_lds_dwordx4 v238, s[60:61] offset:0\n"
        "global_load_lds_dwordx4 v239, s[60:61] offset:1024\n"
        "global_load_lds_dwordx4 v240, s[60:61] offset:2048\n"
        "global_load_lds_dwordx4 v241, s[60:61] offset:3072\n"
        "v_xor_b32 v6, v5, v2\n"
        "v_lshlrev_b32 v6, 4, v6\n"
        "v_xor_b32 v7, 64, v6\n"
        "v_lshl_add_u32 v234, v4, 10, v6\n"
        "v_lshl_add_u32 v8, v4, 10, v7\n"
        "v_add_u32 v235, 7168, v8\n"
        "v_add_u32 v236, 14336, v234\n"
        "v_add_u32 v237, 21504, v8\n"
        "s_add_u32 s56, s10, s50\n"
        "s_addc_u32 s57, s11, 0\n"
        "s_add_u32 m0, s72, 0x0\n"
        "s_nop 0\n"
        "global_load_lds_dwordx4 v234, s[56:57] offset:0\n"
        "global_load_lds_dwordx4 v235, s[56:57] offset:1024\n"
        "global_load_lds_dwordx4 v236, s[56:57] offset:2048\n"
        "global_load_lds_dwordx4 v237, s[56:57] offset:3072\n"
        "s_getpc_b64 s[46:47]\n"
        "LPC_%=:\n"
        "s_lshl2_add_u32 s53, s42, s40\n"
        "s_lshl_b32 s53, s53, 10\n"
        "v_lshlrev_b32 v12, 6, v1\n"
        "v_add_u32 v12, s53, v12\n"
        "v_min_u32 v252, LEND_%=-LPC_%=-4, v12\n"
        "global_load_dword v253, v252, s[46:47] offset:0\n"
        "s_add_u32 s62, s16, s51\n"
        "s_addc_u32 s63, s17, 0\n"
        "s_sub_u32 s62, s62, 4096\n"
        "s_subb_u32 s63, s63, 0\n"
        "s_add_u32 m0, s73, 0x14000\n"
        "s_nop 0\n"
        "global_load_lds_dwordx4 v238, s[62:63] offset:0\n"
        "global_load_lds_dwordx4 v239, s[62:63] offset:1024\n"
        "global_load_lds_dwordx4 v240, s[62:63] offset:2048\n"
        "global_load_lds_dwordx4 v241, s[62:63] offset:3072\n"
        "s_add_u32 s58, s56, 0x8000\n"
        "s_addc_u32 s59, s57, 0\n"
        "s_add_u32 s68, s12, s51\n"
        "s_addc_u32 s69, s13, 0\n"
        "s_add_u32 m0, s72, 0x1000\n"
        "s_nop 0\n"
        "global_load_lds_dwordx4 v234, s[58:59] offset:0\n"
        "global_load_lds_dwordx4 v235, s[58:59] offset:1024\n"
        "global_load_lds_dwordx4 v236, s[58:59] offset:2048\n"
        "global_load_lds_dwordx4 v237, s[58:59] offset:3072\n"
        "s_sub_u32 s68, s68, 4096\n"
        "s_subb_u32 s69, s69, 0\n"
        "v_lshrrev_b32 v8, 1, v1\n"
        "v_xor_b32 v8, v8, v2\n"
        "v_lshlrev_b32 v8, 4, v8\n"
        "v_lshl_add_u32 v8, v1, 7, v8\n"
        "v_xor_b32 v9, 64, v8\n"
        "s_lshr_b32 s50, s40, 1\n"
        "s_lshl_b32 s50, s50, 13\n"
        "v_add_u32 v224, s50, v8\n"
        "v_add_u32 v246, s72, v8\n"
        "v_add_u32 v248, 0x10000, v246\n"
        "v_add_u32 v225, s50, v9\n"
        "v_add_u32 v247, s72, v9\n"
        "v_add_u32 v249, 0x10000, v247\n"
        "s_and_b32 s50, s40, 1\n"
        "s_lshl_b32 s50, s50, 10\n"
        "s_add_u32 s50, s50, 0x10000\n"
        "v_lshlrev_b32 v8, 11, v2\n"
        "v_add_u32 v8, s50, v8\n"
        "v_lshrrev_b32 v9, 2, v1\n"
        "v_or_b32 v10, 4, v9\n"
        "v_and_b32 v11, 1, v2\n"
        "v_and_b32 v13, 3, v0\n"
        "v_lshlrev_b32 v14, 2, v11\n"
        "v_xor_b32 v14, v14, v9\n"
        "v_bfe_u32 v15, v14, 1, 2\n"
        "v_xor_b32 v15, 0, v15\n"
        "v_lshlrev_b32 v15, 5, v15\n"
        "v_lshl_add_u32 v14, v14, 7, v15\n"
        "v_lshl_add_u32 v14, v13, 3, v14\n"
        "v_add_u32 v226, v14, v8\n"
        "v_xor_b32 v228, 32, v226\n"
        "v_xor_b32 v230, 64, v226\n"
        "v_xor_b32 v232, 96, v226\n"
        "v_lshlrev_b32 v14, 2, v11\n"
        "v_xor_b32 v14, v14, v10\n"
        "v_bfe_u32 v15, v14, 1, 2\n"
        "v_xor_b32 v15, 0, v15\n"
        "v_lshlrev_b32 v15, 5, v15\n"
        "v_lshl_add_u32 v14, v14, 7, v15\n"
        "v_lshl_add_u32 v14, v13, 3, v14\n"
        "v_add_u32 v227, v14, v8\n"
        "v_xor_b32 v229, 32, v227\n"
        "v_xor_b32 v231, 64, v227\n"
        "v_xor_b32 v233, 96, v227\n"
        "v_add_u32 v216, 0x10000, v226\n"
        "v_add_u32 v217, 0x10000, v227\n"
        "v_add_u32 v218, 0x10000, v228\n"
        "v_add_u32 v219, 0x10000, v229\n"
        "v_add_u32 v220, 0x10000, v230\n"
        "v_add_u32 v221, 0x10000, v231\n"
        "v_add_u32 v222, 0x10000, v232\n"
        "v_add_u32 v223, 0x10000, v233\n"
        "v_lshl_add_u32 v242, v4, 11, v6\n"
        "v_lshl_add_u32 v8, v4, 11, v7\n"
        "v_add_u32 v243, 15360, v8\n"
        "v_add_u32 v244, 129024, v242\n"
        "v_add_u32 v245, 144384, v8\n"
        "s_lshl_b32 s50, s45, 18\n"
        "s_lshl_b32 s51, s40, 15\n"
        "s_add_u32 s50, s50, s51\n"
        "s_add_u32 s64, s8, s50\n"
        "s_addc_u32 s65, s9, 0\n"
        "s_lshl_b32 s50, s44, 18\n"
        "s_add_u32 s50, s50, s51\n"
        "s_add_u32 s66, s8, s50\n"
        "s_addc_u32 s67, s9, 0\n"
        "s_add_u32 s48, s38, 0x3b00000\n"
        "s_addc_u32 s49, s39, 0\n"
        "s_mov_b32 s35, 0xffff0000\n"
        "s_add_u32 s34, s43, s44\n"
        "v_mov_b32 v11, 0\n"
        "global_load_dwordx2 v[250:251], v11, s[48:49] sc1\n"
        "s_lshl2_add_u32 s82, s55, s40\n"
        "s_lshl_b32 s82, s82, 11\n"
        "v_and_b32 v20, 0xf0, v254\n"
        "v_lshl_add_u32 v20, v20, 3, s82\n"
        "global_load_dword v253, v20, s[10:11] offset:0\n"
        "s_lshl2_add_u32 s82, s42, s40\n"
        "s_lshl3_add_u32 s82, s82, s41\n"
        "s_lshl_b32 s82, s82, 10\n"
        "v_and_b32 v21, 0x70, v254\n"
        "v_lshl_add_u32 v173, v21, 3, s82\n"
        "s_lshl2_add_u32 s82, s55, s40\n"
        "s_lshl_b32 s82, s82, 12\n"
        "v_bfe_u32 v20, v254, 4, 1\n"
        "v_bfe_u32 v21, v254, 5, 1\n"
        "v_lshlrev_b32 v20, 7, v20\n"
        "v_lshl_add_u32 v20, v21, 11, v20\n"
        "v_add_u32 v199, s82, v20\n"
        "s_mov_b64 exec, 0\n"
        "s_cmp_eq_u32 s40, 0\n"
        "s_cbranch_scc1 LHA0_%=\n"
        "s_cmp_eq_u32 s40, 1\n"
        "s_cbranch_scc1 LHA1_%=\n"
        "s_cmp_eq_u32 s40, 2\n"
        "s_cbranch_scc1 LHA2_%=\n"
        "s_branch LHA3_%=\n"
        "LHRA_%=:\n"
        "s_mov_b64 exec, -1\n"
        "s_add_u32 m0, s73, 0x18000\n"
        "s_add_u32 s86, s60, 0x80000\n"
        "s_addc_u32 s87, s61, 0\n"
        "global_load_lds_dwordx4 v238, s[86:87] offset:0\n"
        "global_load_lds_dwordx4 v239, s[86:87] offset:1024\n"
        "global_load_lds_dwordx4 v240, s[86:87] offset:2048\n"
        "global_load_lds_dwordx4 v241, s[86:87] offset:3072\n"
        "s_add_u32 m0, s73, 0x1c000\n"
        "s_add_u32 s86, s62, 0x80000\n"
        "s_addc_u32 s87, s63, 0\n"
        "global_load_lds_dwordx4 v238, s[86:87] offset:0\n"
        "global_load_lds_dwordx4 v239, s[86:87] offset:1024\n"
        "global_load_lds_dwordx4 v240, s[86:87] offset:2048\n"
        "global_load_lds_dwordx4 v241, s[86:87] offset:3072\n"
        "; ---- FRONT\n"
        "s_add_u32 m0, s72, 0x8000\n"
        "s_add_u32 s86, s56, 0x80\n"
        "s_addc_u32 s87, s57, 0\n"
        "global_load_lds_dwordx4 v234, s[86:87] offset:0\n"
        "s_waitcnt vmcnt(20)\n"
        "s_barrier\n"
        "ds_read_b64_tr_b16 v[32:33], v226 offset:0\n"
        "ds_read_b64_tr_b16 v[34:35], v227 offset:0\n"
        "ds_read_b128 v[0:3], v224 offset:16384\n"
        "global_load_lds_dwordx4 v235, s[86:87] offset:1024\n"
        "global_load_lds_dwordx4 v236, s[86:87] offset:2048\n"
        "ds_read_b128 v[4:7], v225 offset:16384\n"
        "ds_read_b64_tr_b16 v[64:65], v226 offset:8192\n"
        "ds_read_b64_tr_b16 v[66:67], v227 offset:8192\n"
        "ds_read_b128 v[12:15], v225 offset:18432\n"
        "s_lshl_b32 s94, s41, 7\n"
        "s_waitcnt lgkmcnt(4)\n"
        "v_mfma_f32_16x16x32_bf16 a[0:3], v[32:35], v[0:3], 0\n"
        "global_load_lds_dwordx4 v237, s[86:87] offset:3072\n"
        "ds_read_b128 v[8:11], v224 offset:18432\n"
        "s_add_u32 m0, s72, 0x9000\n"
        "s_add_u32 s86, s58, 0x80\n"
        "s_addc_u32 s87, s59, 0\n"
        "global_load_lds_dwordx4 v234, s[86:87] offset:0\n"
        "ds_read_b64_tr_b16 v[36:37], v228 offset:0\n"
        "s_lshl2_add_u32 s94, s42, s94\n"
        "s_add_u32 s94, s94, s40\n"
        "s_waitcnt lgkmcnt(3)\n"
        "v_mfma_f32_16x16x32_bf16 a[0:3], v[64:67], v[4:7], a[0:3]\n"
        "ds_read_b64_tr_b16 v[38:39], v229 offset:0\n"
        "s_lshl_b32 s94, s94, 1\n"
        "s_waitcnt lgkmcnt(3)\n"
        "v_mfma_f32_16x16x32_bf16 a[32:35], v[64:67], v[12:15], 0\n"
        "ds_read_b64_tr_b16 v[68:69], v228 offset:8192\n"
        "ds_read_b64_tr_b16 v[70:71], v229 offset:8192\n"
        "ds_read_b64_tr_b16 v[40:41], v230 offset:0\n"
        "s_lshl_b32 s50, s42, 6\n"
        "s_waitcnt lgkmcnt(5)\n"
        "v_mfma_f32_16x16x32_bf16 a[32:35], v[32:35], v[8:11], a[32:35]\n"
        "ds_read_b64_tr_b16 v[42:43], v231 offset:0\n"
        "ds_read_b64_tr_b16 v[72:73], v230 offset:8192\n"
        "ds_read_b64_tr_b16 v[74:75], v231 offset:8192\n"
        "ds_read_b64_tr_b16 v[44:45], v232 offset:0\n"
        "ds_read_b64_tr_b16 v[46:47], v233 offset:0\n"
        "ds_read_b64_tr_b16 v[76:77], v232 offset:8192\n"
        "ds_read_b64_tr_b16 v[78:79], v233 offset:8192\n"
        "ds_read_b128 v[16:19], v224 offset:0\n"
        "ds_read_b128 v[20:23], v225 offset:0\n"
        "global_load_lds_dwordx4 v235, s[86:87] offset:1024\n"
        "s_lshl_b32 s51, s41, 7\n"
        "s_waitcnt lgkmcnt(12)\n"
        "v_mfma_f32_16x16x32_bf16 a[36:39], v[36:39], v[8:11], 0\n"
        "ds_read_b128 v[24:27], v224 offset:2048\n"
        "ds_read_b128 v[28:31], v225 offset:2048\n"
        "s_lshl_b32 s52, s40, 5\n"
        "s_waitcnt lgkmcnt(12)\n"
        "v_mfma_f32_16x16x32_bf16 a[36:39], v[68:71], v[12:15], a[36:39]\n"
        "s_add_u32 s51, s51, s52\n"
        "v_mov_b32 v172, s94\n"
        "v_lshlrev_b32 v198, 11, v172\n"
        "v_mfma_f32_16x16x32_bf16 a[4:7], v[68:71], v[4:7], 0\n"
        "s_waitcnt lgkmcnt(10)\n"
        "v_lshrrev_b32 v166, 4, v254\n"
        "v_lshrrev_b32 v167, 3, v166\n"
        "v_mfma_f32_16x16x32_bf16 a[4:7], v[36:39], v[0:3], a[4:7]\n"
        "global_load_lds_dwordx4 v236, s[86:87] offset:2048\n"
        "s_and_b32 s91, s43, 15\n"
        "v_and_b32 v168, 7, v166\n"
        "v_mfma_f32_16x16x32_bf16 a[8:11], v[40:43], v[0:3], 0\n"
        "s_waitcnt lgkmcnt(7)\n"
        "v_lshl_add_u32 v167, v167, 3, s50\n"
        "v_lshl_add_u32 v168, v168, 2, s51\n"
        "v_mfma_f32_16x16x32_bf16 a[8:11], v[72:75], v[4:7], a[8:11]\n"
        "s_waitcnt lgkmcnt(5)\n"
        "v_lshlrev_b32 v169, 11, v167\n"
        "v_lshl_add_u32 v169, v168, 1, v169\n"
        "v_mfma_f32_16x16x32_bf16 a[40:43], v[72:75], v[12:15], 0\n"
        "s_waitcnt lgkmcnt(3)\n"
        "v_cmp_eq_u32 s[78:79], 0, v167\n"
        "v_mov_b32 v171, 6144\n"
        "v_mfma_f32_16x16x32_bf16 a[40:43], v[40:43], v[8:11], a[40:43]\n"
        "global_load_lds_dwordx4 v237, s[86:87] offset:3072\n"
        "s_add_u32 m0, s73, 0x20000\n"
        "s_waitcnt vmcnt(22)\n"
        "v_mfma_f32_16x16x32_bf16 a[44:47], v[44:47], v[8:11], 0\n"
        "s_add_u32 s86, s60, 0x100000\n"
        "s_addc_u32 s87, s61, 0\n"
        "v_cndmask_b32 v170, 0, v171, s[78:79]\n"
        "v_mfma_f32_16x16x32_bf16 a[44:47], v[76:79], v[12:15], a[44:47]\n"
        "s_barrier\n"
        "v_mfma_f32_16x16x32_bf16 a[12:15], v[76:79], v[4:7], 0\n"
        "ds_read_b64_tr_b16 v[48:49], v226 offset:16384\n"
        "s_mul_i32 s96, s40, 4224\n"
        "v_add_u32 v170, v170, v169\n"
        "v_mfma_f32_16x16x32_bf16 a[12:15], v[44:47], v[0:3], a[12:15]\n"
        "global_load_lds_dwordx4 v238, s[86:87] offset:0\n"
        "ds_read_b64_tr_b16 v[50:51], v227 offset:16384\n"
        "s_add_u32 s96, s96, 0x20240\n"
        "v_mfma_f32_16x16x32_bf16 a[128:131], v[32:35], v[16:19], 0\n"
        "ds_read_b64_tr_b16 v[80:81], v226 offset:24576\n"
        "s_waitcnt lgkmcnt(3)\n"
        "v_mfma_f32_16x16x32_bf16 a[128:131], v[64:67], v[20:23], a[128:131]\n"
        "ds_read_b64_tr_b16 v[82:83], v227 offset:24576\n"
        "v_sub_u32 v171, 2040, v167\n"
        "v_mfma_f32_16x16x32_bf16 a[160:163], v[64:67], v[28:31], 0\n"
        "ds_read_b64_tr_b16 v[52:53], v228 offset:16384\n"
        "v_lshlrev_b32 v171, 1, v171\n"
        "v_mfma_f32_16x16x32_bf16 a[160:163], v[32:35], v[24:27], a[160:163]\n"
        "ds_read_b64_tr_b16 v[54:55], v229 offset:16384\n"
        "global_load_lds_dwordx4 v239, s[86:87] offset:1024\n"
        "v_mfma_f32_16x16x32_bf16 a[164:167], v[36:39], v[24:27], 0\n"
        "ds_read_b64_tr_b16 v[84:85], v228 offset:24576\n"
        "v_lshl_add_u32 v171, v168, 12, v171\n"
        "v_mfma_f32_16x16x32_bf16 a[164:167], v[68:71], v[28:31], a[164:167]\n"
        "ds_read_b64_tr_b16 v[86:87], v229 offset:24576\n"
        "v_lshlrev_b32 v168, 3, v168\n"
        "v_mfma_f32_16x16x32_bf16 a[132:135], v[68:71], v[20:23], 0\n"
        "ds_read_b64_tr_b16 v[56:57], v230 offset:16384\n"
        "s_sub_u32 s74, s20, 2048\n"
        "s_subb_u32 s75, s21, 0\n"
        "v_mfma_f32_16x16x32_bf16 a[132:135], v[36:39], v[16:19], a[132:135]\n"
        "ds_read_b64_tr_b16 v[58:59], v231 offset:16384\n"
        "global_load_lds_dwordx4 v240, s[86:87] offset:2048\n"
        "v_mfma_f32_16x16x32_bf16 a[136:139], v[40:43], v[16:19], 0\n"
        "ds_read_b64_tr_b16 v[88:89], v230 offset:24576\n"
        "s_cmp_lt_u32 s43, 16\n"
        "s_cselect_b32 s90, s44, s45\n"
        "v_mfma_f32_16x16x32_bf16 a[136:139], v[72:75], v[20:23], a[136:139]\n"
        "ds_read_b64_tr_b16 v[90:91], v231 offset:24576\n"
        "s_lshl4_add_u32 s90, s90, s91\n"
        "s_lshl2_add_u32 s90, s90, s40\n"
        "v_mfma_f32_16x16x32_bf16 a[168:171], v[72:75], v[28:31], 0\n"
        "ds_read_b64_tr_b16 v[60:61], v232 offset:16384\n"
        "s_lshl_b32 s90, s90, 12\n"
        "s_lshl_b32 s91, s44, 5\n"
        "v_mfma_f32_16x16x32_bf16 a[168:171], v[40:43], v[24:27], a[168:171]\n"
        "ds_read_b64_tr_b16 v[62:63], v233 offset:16384\n"
        "global_load_dwordx4 v[158:161], v254, s[30:31] offset:0\n"
        "s_add_u32 s91, s91, s43\n"
        "v_mfma_f32_16x16x32_bf16 a[172:175], v[44:47], v[24:27], 0\n"
        "ds_read_b64_tr_b16 v[92:93], v232 offset:24576\n"
        "s_add_u32 s92, s8, s90\n"
        "s_addc_u32 s93, s9, 0\n"
        "v_mfma_f32_16x16x32_bf16 a[172:175], v[76:79], v[28:31], a[172:175]\n"
        "ds_read_b64_tr_b16 v[94:95], v233 offset:24576\n"
        "s_lshl2_add_u32 s91, s91, s40\n"
        "s_lshl_b32 s80, s91, 1\n"
        "v_mfma_f32_16x16x32_bf16 a[140:143], v[76:79], v[20:23], 0\n"
        "s_add_u32 s81, s80, 1\n"
        "s_add_u32 s76, s38, 0x3301000\n"
        "s_addc_u32 s77, s39, 0\n"
        "v_mfma_f32_16x16x32_bf16 a[140:143], v[44:47], v[16:19], a[140:143]\n"
        "s_waitcnt lgkmcnt(6)\n"
        "global_load_lds_dwordx4 v241, s[86:87] offset:3072\n"
        "s_add_u32 m0, s73, 0x24000\n"
        "v_mfma_f32_16x16x32_bf16 a[16:19], v[0:3], v[48:51], 0\n"
        "s_add_u32 s86, s62, 0x100000\n"
        "s_addc_u32 s87, s63, 0\n"
        "s_lshl_b32 s80, s80, 7\n"
        "v_mfma_f32_16x16x32_bf16 a[16:19], v[4:7], v[80:83], a[16:19]\n"
        "s_lshl_b32 s81, s81, 7\n"
        "v_mfma_f32_16x16x32_bf16 a[48:51], v[12:15], v[80:83], 0\n"
        "v_mfma_f32_16x16x32_bf16 a[48:51], v[8:11], v[48:51], a[48:51]\n"
        "global_load_lds_dwordx4 v238, s[86:87] offset:0\n"
        "v_mfma_f32_16x16x32_bf16 a[52:55], v[8:11], v[52:55], 0\n"
        "v_mfma_f32_16x16x32_bf16 a[52:55], v[12:15], v[84:87], a[52:55]\n"
        "s_waitcnt lgkmcnt(0)\n"
        "v_mfma_f32_16x16x32_bf16 a[20:23], v[4:7], v[84:87], 0\n"
        "v_mfma_f32_16x16x32_bf16 a[20:23], v[0:3], v[52:55], a[20:23]\n"
        "global_load_dwordx4 v[162:165], v254, s[30:31] offset:1024\n"
        "v_mfma_f32_16x16x32_bf16 a[24:27], v[0:3], v[56:59], 0\n"
        "v_mfma_f32_16x16x32_bf16 a[24:27], v[4:7], v[88:91], a[24:27]\n"
        "v_mfma_f32_16x16x32_bf16 a[56:59], v[12:15], v[88:91], 0\n"
        "v_mfma_f32_16x16x32_bf16 a[56:59], v[8:11], v[56:59], a[56:59]\n"
        "global_load_lds_dwordx4 v239, s[86:87] offset:1024\n"
        "v_mfma_f32_16x16x32_bf16 a[60:63], v[8:11], v[60:63], 0\n"
        "v_mfma_f32_16x16x32_bf16 a[60:63], v[12:15], v[92:95], a[60:63]\n"
        "v_mfma_f32_16x16x32_bf16 a[28:31], v[4:7], v[92:95], 0\n"
        "v_mfma_f32_16x16x32_bf16 a[28:31], v[0:3], v[60:63], a[28:31]\n"
        "global_load_lds_dwordx4 v240, s[86:87] offset:2048\n"
        "v_mfma_f32_16x16x32_bf16 a[144:147], v[16:19], v[48:51], 0\n"
        "s_waitcnt vmcnt(27)\n"
        "v_mfma_f32_16x16x32_bf16 a[144:147], v[20:23], v[80:83], a[144:147]\n"
        "s_barrier\n"
        "v_mfma_f32_16x16x32_bf16 a[176:179], v[28:31], v[80:83], 0\n"
        "ds_read_b128 v[0:3], v224 offset:20480\n"
        "v_mfma_f32_16x16x32_bf16 a[176:179], v[24:27], v[48:51], a[176:179]\n"
        "global_load_ushort v172, v172, s[28:29]\n"
        "v_mfma_f32_16x16x32_bf16 a[180:183], v[24:27], v[52:55], 0\n"
        "global_load_lds_dwordx4 v241, s[86:87] offset:3072\n"
        "ds_read_b128 v[4:7], v225 offset:20480\n"
        "s_add_u32 m0, s72, 0x0\n"
        "v_mfma_f32_16x16x32_bf16 a[180:183], v[28:31], v[84:87], a[180:183]\n"
        "ds_read_b128 v[8:11], v224 offset:22528\n"
        "s_add_u32 s86, s56, 0x100\n"
        "s_addc_u32 s87, s57, 0\n"
        "v_mfma_f32_16x16x32_bf16 a[148:151], v[20:23], v[84:87], 0\n"
        "ds_read_b128 v[12:15], v225 offset:22528\n"
        "v_mfma_f32_16x16x32_bf16 a[148:151], v[16:19], v[52:55], a[148:151]\n"
        "v_mfma_f32_16x16x32_bf16 a[152:155], v[16:19], v[56:59], 0\n"
        "global_load_ushort v198, v198, s[26:27]\n"
        "v_mfma_f32_16x16x32_bf16 a[152:155], v[20:23], v[88:91], a[152:155]\n"
        "global_load_dwordx4 v[134:137], v168, s[24:25] offset:0\n"
        "v_mfma_f32_16x16x32_bf16 a[184:187], v[28:31], v[88:91], 0\n"
        "v_mfma_f32_16x16x32_bf16 a[184:187], v[24:27], v[56:59], a[184:187]\n"
        "v_mfma_f32_16x16x32_bf16 a[188:191], v[24:27], v[60:63], 0\n"
        "ds_read_b128 v[24:27], v224 offset:6144\n"
        "s_waitcnt lgkmcnt(3)\n"
        "v_mfma_f32_16x16x32_bf16 a[188:191], v[28:31], v[92:95], a[188:191]\n"
        "ds_read_b128 v[28:31], v225 offset:6144\n"
        "v_mfma_f32_16x16x32_bf16 a[156:159], v[20:23], v[92:95], 0\n"
        "ds_read_b128 v[20:23], v225 offset:4096\n"
        "s_waitcnt lgkmcnt(3)\n"
        "v_mfma_f32_16x16x32_bf16 a[156:159], v[16:19], v[60:63], a[156:159]\n"
        "ds_read_b128 v[16:19], v224 offset:4096\n"
        "v_mfma_f32_16x16x32_bf16 a[64:67], v[32:35], v[0:3], 0\n"
        "v_mfma_f32_16x16x32_bf16 a[64:67], v[64:67], v[4:7], a[64:67]\n"
        "v_mfma_f32_16x16x32_bf16 a[96:99], v[64:67], v[12:15], 0\n"
        "v_mfma_f32_16x16x32_bf16 a[96:99], v[32:35], v[8:11], a[96:99]\n"
        "v_mfma_f32_16x16x32_bf16 a[100:103], v[36:39], v[8:11], 0\n"
        "v_mfma_f32_16x16x32_bf16 a[100:103], v[68:71], v[12:15], a[100:103]\n"
        "v_mfma_f32_16x16x32_bf16 a[68:71], v[68:71], v[4:7], 0\n"
        "v_mfma_f32_16x16x32_bf16 a[68:71], v[36:39], v[0:3], a[68:71]\n"
        "global_load_dwordx4 v[138:141], v168, s[24:25] offset:16\n"
        "v_mfma_f32_16x16x32_bf16 a[72:75], v[40:43], v[0:3], 0\n"
        "v_mfma_f32_16x16x32_bf16 a[72:75], v[72:75], v[4:7], a[72:75]\n"
        "v_mfma_f32_16x16x32_bf16 a[104:107], v[72:75], v[12:15], 0\n"
        "v_mfma_f32_16x16x32_bf16 a[104:107], v[40:43], v[8:11], a[104:107]\n"
        "v_mfma_f32_16x16x32_bf16 a[108:111], v[44:47], v[8:11], 0\n"
        "v_mfma_f32_16x16x32_bf16 a[108:111], v[76:79], v[12:15], a[108:111]\n"
        "v_mfma_f32_16x16x32_bf16 a[76:79], v[76:79], v[4:7], 0\n"
        "v_mfma_f32_16x16x32_bf16 a[76:79], v[44:47], v[0:3], a[76:79]\n"
        "v_mfma_f32_16x16x32_bf16 a[80:83], v[0:3], v[48:51], 0\n"
        "v_mfma_f32_16x16x32_bf16 a[80:83], v[4:7], v[80:83], a[80:83]\n"
        "v_mfma_f32_16x16x32_bf16 a[112:115], v[12:15], v[80:83], 0\n"
        "v_mfma_f32_16x16x32_bf16 a[112:115], v[8:11], v[48:51], a[112:115]\n"
        "v_mfma_f32_16x16x32_bf16 a[116:119], v[8:11], v[52:55], 0\n"
        "v_mfma_f32_16x16x32_bf16 a[116:119], v[12:15], v[84:87], a[116:119]\n"
        "v_mfma_f32_16x16x32_bf16 a[84:87], v[4:7], v[84:87], 0\n"
        "v_mfma_f32_16x16x32_bf16 a[84:87], v[0:3], v[52:55], a[84:87]\n"
        "global_load_dwordx2 v[96:97], v170, s[74:75] offset:-4096\n"
        "v_mfma_f32_16x16x32_bf16 a[88:91], v[0:3], v[56:59], 0\n"
        "v_mfma_f32_16x16x32_bf16 a[88:91], v[4:7], v[88:91], a[88:91]\n"
        "v_mfma_f32_16x16x32_bf16 a[120:123], v[12:15], v[88:91], 0\n"
        "v_mfma_f32_16x16x32_bf16 a[120:123], v[8:11], v[56:59], a[120:123]\n"
        "s_waitcnt vmcnt(15) lgkmcnt(0)\n"
        "v_mfma_f32_16x16x32_bf16 a[124:127], v[8:11], v[60:63], 0\n"
        "s_barrier\n"
        "v_mfma_f32_16x16x32_bf16 a[124:127], v[12:15], v[92:95], a[124:127]\n"
        "global_load_lds_dwordx4 v234, s[86:87] offset:0\n"
        "ds_read_b128 v[8:11], v224 offset:51200\n"
        "v_mfma_f32_16x16x32_bf16 a[92:95], v[4:7], v[92:95], 0\n"
        "ds_read_b128 v[4:7], v225 offset:49152\n"
        "v_mfma_f32_16x16x32_bf16 a[92:95], v[0:3], v[60:63], a[92:95]\n"
        "ds_read_b128 v[0:3], v224 offset:49152\n"
        "s_add_u32 s82, s12, 0x0\n"
        "s_addc_u32 s83, s13, 0\n"
        "v_mfma_f32_16x16x32_bf16 a[192:195], v[32:35], v[16:19], 0\n"
        "ds_read_b128 v[12:15], v225 offset:51200\n"
        "v_mfma_f32_16x16x32_bf16 a[192:195], v[64:67], v[20:23], a[192:195]\n"
        "global_load_lds_dwordx4 v235, s[86:87] offset:1024\n"
        "v_mfma_f32_16x16x32_bf16 a[224:227], v[64:67], v[28:31], 0\n"
        "ds_read_b64_tr_b16 v[64:65], v226 offset:40960\n"
        "v_mfma_f32_16x16x32_bf16 a[224:227], v[32:35], v[24:27], a[224:227]\n"
        "ds_read_b64_tr_b16 v[32:33], v226 offset:32768\n"
        "v_mfma_f32_16x16x32_bf16 a[228:231], v[36:39], v[24:27], 0\n"
        "ds_read_b64_tr_b16 v[34:35], v227 offset:32768\n"
        "v_mfma_f32_16x16x32_bf16 a[228:231], v[68:71], v[28:31], a[228:231]\n"
        "ds_read_b64_tr_b16 v[66:67], v227 offset:40960\n"
        "global_load_lds_dwordx4 v236, s[86:87] offset:2048\n"
        "v_mfma_f32_16x16x32_bf16 a[196:199], v[68:71], v[20:23], 0\n"
        "ds_read_b64_tr_b16 v[68:69], v228 offset:40960\n"
        "v_mfma_f32_16x16x32_bf16 a[196:199], v[36:39], v[16:19], a[196:199]\n"
        "ds_read_b64_tr_b16 v[36:37], v228 offset:32768\n"
        "v_mfma_f32_16x16x32_bf16 a[200:203], v[40:43], v[16:19], 0\n"
        "ds_read_b64_tr_b16 v[38:39], v229 offset:32768\n"
        "v_mfma_f32_16x16x32_bf16 a[200:203], v[72:75], v[20:23], a[200:203]\n"
        "ds_read_b64_tr_b16 v[70:71], v229 offset:40960\n"
        "global_load_lds_dwordx4 v237, s[86:87] offset:3072\n"
        "s_add_u32 m0, s72, 0x1000\n"
        "v_mfma_f32_16x16x32_bf16 a[232:235], v[72:75], v[28:31], 0\n"
        "ds_read_b64_tr_b16 v[72:73], v230 offset:40960\n"
        "s_add_u32 s86, s58, 0x100\n"
        "s_addc_u32 s87, s59, 0\n"
        "v_mfma_f32_16x16x32_bf16 a[232:235], v[40:43], v[24:27], a[232:235]\n"
        "ds_read_b64_tr_b16 v[40:41], v230 offset:32768\n"
        "v_mfma_f32_16x16x32_bf16 a[236:239], v[44:47], v[24:27], 0\n"
        "ds_read_b64_tr_b16 v[42:43], v231 offset:32768\n"
        "v_mfma_f32_16x16x32_bf16 a[236:239], v[76:79], v[28:31], a[236:239]\n"
        "ds_read_b64_tr_b16 v[74:75], v231 offset:40960\n"
        "global_load_dwordx2 v[98:99], v170, s[74:75] offset:-2048\n"
        "v_mfma_f32_16x16x32_bf16 a[204:207], v[76:79], v[20:23], 0\n"
        "ds_read_b64_tr_b16 v[76:77], v232 offset:40960\n"
        "global_load_lds_dwordx4 v234, s[86:87] offset:0\n"
        "v_mfma_f32_16x16x32_bf16 a[204:207], v[44:47], v[16:19], a[204:207]\n"
        "ds_read_b64_tr_b16 v[44:45], v232 offset:32768\n"
        "v_mfma_f32_16x16x32_bf16 a[208:211], v[16:19], v[48:51], 0\n"
        "ds_read_b64_tr_b16 v[46:47], v233 offset:32768\n"
        "v_mfma_f32_16x16x32_bf16 a[208:211], v[20:23], v[80:83], a[208:211]\n"
        "ds_read_b64_tr_b16 v[78:79], v233 offset:40960\n"
        "v_mfma_f32_16x16x32_bf16 a[240:243], v[28:31], v[80:83], 0\n"
        "ds_read_b64_tr_b16 v[80:81], v226 offset:57344\n"
        "global_load_lds_dwordx4 v235, s[86:87] offset:1024\n"
        "v_mfma_f32_16x16x32_bf16 a[240:243], v[24:27], v[48:51], a[240:243]\n"
        "ds_read_b64_tr_b16 v[48:49], v226 offset:49152\n"
        "v_mfma_f32_16x16x32_bf16 a[244:247], v[24:27], v[52:55], 0\n"
        "ds_read_b64_tr_b16 v[50:51], v227 offset:49152\n"
        "v_mfma_f32_16x16x32_bf16 a[244:247], v[28:31], v[84:87], a[244:247]\n"
        "ds_read_b64_tr_b16 v[82:83], v227 offset:57344\n"
        "v_mfma_f32_16x16x32_bf16 a[212:215], v[20:23], v[84:87], 0\n"
        "ds_read_b64_tr_b16 v[84:85], v228 offset:57344\n"
        "global_load_lds_dwordx4 v236, s[86:87] offset:2048\n"
        "v_mfma_f32_16x16x32_bf16 a[212:215], v[16:19], v[52:55], a[212:215]\n"
        "ds_read_b64_tr_b16 v[52:53], v228 offset:49152\n"
        "v_mfma_f32_16x16x32_bf16 a[216:219], v[16:19], v[56:59], 0\n"
        "ds_read_b64_tr_b16 v[54:55], v229 offset:49152\n"
        "v_mfma_f32_16x16x32_bf16 a[216:219], v[20:23], v[88:91], a[216:219]\n"
        "ds_read_b64_tr_b16 v[86:87], v229 offset:57344\n"
        "v_mfma_f32_16x16x32_bf16 a[248:251], v[28:31], v[88:91], 0\n"
        "ds_read_b64_tr_b16 v[88:89], v230 offset:57344\n"
        "global_load_lds_dwordx4 v237, s[86:87] offset:3072\n"
        "s_add_u32 m0, s73, 0x10000\n"
        "v_mfma_f32_16x16x32_bf16 a[248:251], v[24:27], v[56:59], a[248:251]\n"
        "ds_read_b64_tr_b16 v[56:57], v230 offset:49152\n"
        "s_add_u32 s86, s60, 0x180000\n"
        "s_addc_u32 s87, s61, 0\n"
        "v_mfma_f32_16x16x32_bf16 a[252:255], v[24:27], v[60:63], 0\n"
        "ds_read_b64_tr_b16 v[58:59], v231 offset:49152\n"
        "s_waitcnt lgkmcnt(8)\n"
        "v_mfma_f32_16x16x32_bf16 a[252:255], v[28:31], v[92:95], a[252:255]\n"
        "ds_read_b64_tr_b16 v[90:91], v231 offset:57344\n"
        "v_mfma_f32_16x16x32_bf16 a[220:223], v[20:23], v[92:95], 0\n"
        "ds_read_b64_tr_b16 v[92:93], v232 offset:57344\n"
        "global_load_dwordx2 v[100:101], v170, s[74:75] offset:0\n"
        "v_mfma_f32_16x16x32_bf16 a[220:223], v[16:19], v[60:63], a[220:223]\n"
        "ds_read_b64_tr_b16 v[60:61], v232 offset:49152\n"
        "global_load_lds_dwordx4 v238, s[86:87] offset:0\n"
        "v_mfma_f32_16x16x32_bf16 a[0:3], v[32:35], v[0:3], a[0:3]\n"
        "ds_read_b64_tr_b16 v[62:63], v233 offset:49152\n"
        "v_mfma_f32_16x16x32_bf16 a[0:3], v[64:67], v[4:7], a[0:3]\n"
        "ds_read_b64_tr_b16 v[94:95], v233 offset:57344\n"
        "v_mfma_f32_16x16x32_bf16 a[32:35], v[64:67], v[12:15], a[32:35]\n"
        "ds_read_b128 v[16:19], v224 offset:53248\n"
        "v_mfma_f32_16x16x32_bf16 a[32:35], v[32:35], v[8:11], a[32:35]\n"
        "ds_read_b128 v[20:23], v225 offset:53248\n"
        "global_load_lds_dwordx4 v239, s[86:87] offset:1024\n"
        "v_mfma_f32_16x16x32_bf16 a[36:39], v[36:39], v[8:11], a[36:39]\n"
        "ds_read_b128 v[24:27], v224 offset:55296\n"
        "v_mfma_f32_16x16x32_bf16 a[36:39], v[68:71], v[12:15], a[36:39]\n"
        "ds_read_b128 v[28:31], v225 offset:55296\n"
        "v_mfma_f32_16x16x32_bf16 a[4:7], v[68:71], v[4:7], a[4:7]\n"
        "v_mfma_f32_16x16x32_bf16 a[4:7], v[36:39], v[0:3], a[4:7]\n"
        "global_load_lds_dwordx4 v240, s[86:87] offset:2048\n"
        "v_mfma_f32_16x16x32_bf16 a[8:11], v[40:43], v[0:3], a[8:11]\n"
        "v_mfma_f32_16x16x32_bf16 a[8:11], v[72:75], v[4:7], a[8:11]\n"
        "v_mfma_f32_16x16x32_bf16 a[40:43], v[72:75], v[12:15], a[40:43]\n"
        "v_mfma_f32_16x16x32_bf16 a[40:43], v[40:43], v[8:11], a[40:43]\n"
        "global_load_lds_dwordx4 v241, s[86:87] offset:3072\n"
        "s_add_u32 m0, s73, 0x14000\n"
        "v_mfma_f32_16x16x32_bf16 a[44:47], v[44:47], v[8:11], a[44:47]\n"
        "s_add_u32 s86, s62, 0x180000\n"
        "s_addc_u32 s87, s63, 0\n"
        "v_mfma_f32_16x16x32_bf16 a[44:47], v[76:79], v[12:15], a[44:47]\n"
        "s_waitcnt lgkmcnt(0)\n"
        "v_mfma_f32_16x16x32_bf16 a[12:15], v[76:79], v[4:7], a[12:15]\n"
        "v_mfma_f32_16x16x32_bf16 a[12:15], v[44:47], v[0:3], a[12:15]\n"
        "global_load_dwordx2 v[102:103], v169, s[74:75] offset:2048\n"
        "v_mfma_f32_16x16x32_bf16 a[16:19], v[0:3], v[48:51], a[16:19]\n"
        "global_load_lds_dwordx4 v238, s[86:87] offset:0\n"
        "s_add_u32 s74, s20, 0x1800\n"
        "s_addc_u32 s75, s21, 0\n"
        "v_mfma_f32_16x16x32_bf16 a[16:19], v[4:7], v[80:83], a[16:19]\n"
        "v_mfma_f32_16x16x32_bf16 a[48:51], v[12:15], v[80:83], a[48:51]\n"
        "v_mfma_f32_16x16x32_bf16 a[48:51], v[8:11], v[48:51], a[48:51]\n"
        "v_mfma_f32_16x16x32_bf16 a[52:55], v[8:11], v[52:55], a[52:55]\n"
        "global_load_lds_dwordx4 v239, s[86:87] offset:1024\n"
        "v_mfma_f32_16x16x32_bf16 a[52:55], v[12:15], v[84:87], a[52:55]\n"
        "v_mfma_f32_16x16x32_bf16 a[20:23], v[4:7], v[84:87], a[20:23]\n"
        "v_mfma_f32_16x16x32_bf16 a[20:23], v[0:3], v[52:55], a[20:23]\n"
        "v_mfma_f32_16x16x32_bf16 a[24:27], v[0:3], v[56:59], a[24:27]\n"
        "global_load_lds_dwordx4 v240, s[86:87] offset:2048\n"
        "v_mfma_f32_16x16x32_bf16 a[24:27], v[4:7], v[88:91], a[24:27]\n"
        "v_mfma_f32_16x16x32_bf16 a[56:59], v[12:15], v[88:91], a[56:59]\n"
        "v_mfma_f32_16x16x32_bf16 a[56:59], v[8:11], v[56:59], a[56:59]\n"
        "v_mfma_f32_16x16x32_bf16 a[60:63], v[8:11], v[60:63], a[60:63]\n"
        "global_load_lds_dwordx4 v241, s[86:87] offset:3072\n"
        "ds_read_b128 v[8:11], v224 offset:34816\n"
        "s_add_u32 m0, s72, 0x8000\n"
        "v_mfma_f32_16x16x32_bf16 a[60:63], v[12:15], v[92:95], a[60:63]\n"
        "ds_read_b128 v[12:15], v225 offset:34816\n"
        "s_add_u32 s86, s56, 0x180\n"
        "s_addc_u32 s87, s57, 0\n"
        "v_mfma_f32_16x16x32_bf16 a[28:31], v[4:7], v[92:95], a[28:31]\n"
        "ds_read_b128 v[4:7], v225 offset:32768\n"
        "v_mfma_f32_16x16x32_bf16 a[28:31], v[0:3], v[60:63], a[28:31]\n"
        "ds_read_b128 v[0:3], v224 offset:32768\n"
        "v_mfma_f32_16x16x32_bf16 a[64:67], v[32:35], v[16:19], a[64:67]\n"
        "global_load_dwordx2 v[104:105], v169, s[74:75] offset:-4096\n"
        "v_mfma_f32_16x16x32_bf16 a[64:67], v[64:67], v[20:23], a[64:67]\n"
        "LHA0_%=:\n"
        "s_cbranch_execz LHA4_%=\n"
        "v_mfma_f32_16x16x32_bf16 a[96:99], v[64:67], v[28:31], a[96:99]\n"
        "v_mfma_f32_16x16x32_bf16 a[96:99], v[32:35], v[24:27], a[96:99]\n"
        "v_mfma_f32_16x16x32_bf16 a[100:103], v[36:39], v[24:27], a[100:103]\n"
        "v_mfma_f32_16x16x32_bf16 a[100:103], v[68:71], v[28:31], a[100:103]\n"
        "v_mfma_f32_16x16x32_bf16 a[68:71], v[68:71], v[20:23], a[68:71]\n"
        "v_mfma_f32_16x16x32_bf16 a[68:71], v[36:39], v[16:19], a[68:71]\n"
        "global_load_dwordx2 v[106:107], v169, s[74:75] offset:-2048\n"
        "v_mfma_f32_16x16x32_bf16 a[72:75], v[40:43], v[16:19], a[72:75]\n"
        "v_mfma_f32_16x16x32_bf16 a[72:75], v[72:75], v[20:23], a[72:75]\n"
        "v_mfma_f32_16x16x32_bf16 a[104:107], v[72:75], v[28:31], a[104:107]\n"
        "v_mfma_f32_16x16x32_bf16 a[104:107], v[40:43], v[24:27], a[104:107]\n"
        "v_mfma_f32_16x16x32_bf16 a[108:111], v[44:47], v[24:27], a[108:111]\n"
        "v_mfma_f32_16x16x32_bf16 a[108:111], v[76:79], v[28:31], a[108:111]\n"
        "v_mfma_f32_16x16x32_bf16 a[76:79], v[76:79], v[20:23], a[76:79]\n"
        "v_mfma_f32_16x16x32_bf16 a[76:79], v[44:47], v[16:19], a[76:79]\n"
        "v_mfma_f32_16x16x32_bf16 a[80:83], v[16:19], v[48:51], a[80:83]\n"
        "v_mfma_f32_16x16x32_bf16 a[80:83], v[20:23], v[80:83], a[80:83]\n"
        "v_mfma_f32_16x16x32_bf16 a[112:115], v[28:31], v[80:83], a[112:115]\n"
        "v_mfma_f32_16x16x32_bf16 a[112:115], v[24:27], v[48:51], a[112:115]\n"
        "v_mfma_f32_16x16x32_bf16 a[116:119], v[24:27], v[52:55], a[116:119]\n"
        "v_mfma_f32_16x16x32_bf16 a[116:119], v[28:31], v[84:87], a[116:119]\n"
        "s_waitcnt vmcnt(24) lgkmcnt(0)\n"
        "v_lshl_or_b32 v172, v172, 16, v198\n"
        "v_mfma_f32_16x16x32_bf16 a[84:87], v[20:23], v[84:87], a[84:87]\n"
        "s_waitcnt vmcnt(23)\n"
        "v_lshlrev_b32 v185, 16, v134\n"
        "v_and_b32 v186, s35, v134\n"
        "v_mfma_f32_16x16x32_bf16 a[84:87], v[16:19], v[52:55], a[84:87]\n"
        "global_load_dwordx2 v[108:109], v169, s[74:75] offset:0\n"
        "v_lshlrev_b32 v187, 16, v135\n"
        "v_mfma_f32_16x16x32_bf16 a[88:91], v[16:19], v[56:59], a[88:91]\n"
        "v_and_b32 v188, s35, v135\n"
        "v_readfirstlane_b32 s54, v172\n"
        "v_mfma_f32_16x16x32_bf16 a[88:91], v[20:23], v[88:91], a[88:91]\n"
        "v_mfma_f32_16x16x32_bf16 a[120:123], v[28:31], v[88:91], a[120:123]\n"
        "v_mfma_f32_16x16x32_bf16 a[120:123], v[24:27], v[56:59], a[120:123]\n"
        "v_mfma_f32_16x16x32_bf16 a[124:127], v[24:27], v[60:63], a[124:127]\n"
        "ds_read_b128 v[24:27], v224 offset:38912\n"
        "v_mfma_f32_16x16x32_bf16 a[124:127], v[28:31], v[92:95], a[124:127]\n"
        "ds_read_b128 v[28:31], v225 offset:38912\n"
        "v_mfma_f32_16x16x32_bf16 a[92:95], v[20:23], v[92:95], a[92:95]\n"
        "ds_read_b128 v[20:23], v225 offset:36864\n"
        "v_mfma_f32_16x16x32_bf16 a[92:95], v[16:19], v[60:63], a[92:95]\n"
        "ds_read_b128 v[16:19], v224 offset:36864\n"
        "v_mfma_f32_16x16x32_bf16 a[128:131], v[32:35], v[0:3], a[128:131]\n"
        "v_mfma_f32_16x16x32_bf16 a[128:131], v[64:67], v[4:7], a[128:131]\n"
        "v_mfma_f32_16x16x32_bf16 a[160:163], v[64:67], v[12:15], a[160:163]\n"
        "v_mfma_f32_16x16x32_bf16 a[160:163], v[32:35], v[8:11], a[160:163]\n"
        "v_mfma_f32_16x16x32_bf16 a[164:167], v[36:39], v[8:11], a[164:167]\n"
        "v_mfma_f32_16x16x32_bf16 a[164:167], v[68:71], v[12:15], a[164:167]\n"
        "v_mfma_f32_16x16x32_bf16 a[132:135], v[68:71], v[4:7], a[132:135]\n"
        "v_mfma_f32_16x16x32_bf16 a[132:135], v[36:39], v[0:3], a[132:135]\n"
        "global_load_dwordx2 v[110:111], v169, s[74:75] offset:2048\n"
        "s_add_u32 s74, s20, 0x3800\n"
        "s_addc_u32 s75, s21, 0\n"
        "v_mfma_f32_16x16x32_bf16 a[136:139], v[40:43], v[0:3], a[136:139]\n"
        "v_mfma_f32_16x16x32_bf16 a[136:139], v[72:75], v[4:7], a[136:139]\n"
        "v_mfma_f32_16x16x32_bf16 a[168:171], v[72:75], v[12:15], a[168:171]\n"
        "v_mfma_f32_16x16x32_bf16 a[168:171], v[40:43], v[8:11], a[168:171]\n"
        "v_mfma_f32_16x16x32_bf16 a[172:175], v[44:47], v[8:11], a[172:175]\n"
        "v_mfma_f32_16x16x32_bf16 a[172:175], v[76:79], v[12:15], a[172:175]\n"
        "v_mfma_f32_16x16x32_bf16 a[140:143], v[76:79], v[4:7], a[140:143]\n"
        "v_mfma_f32_16x16x32_bf16 a[140:143], v[44:47], v[0:3], a[140:143]\n"
        "v_mfma_f32_16x16x32_bf16 a[144:147], v[0:3], v[48:51], a[144:147]\n"
        "v_mfma_f32_16x16x32_bf16 a[144:147], v[4:7], v[80:83], a[144:147]\n"
        "v_mfma_f32_16x16x32_bf16 a[176:179], v[12:15], v[80:83], a[176:179]\n"
        "v_mfma_f32_16x16x32_bf16 a[176:179], v[8:11], v[48:51], a[176:179]\n"
        "v_mfma_f32_16x16x32_bf16 a[180:183], v[8:11], v[52:55], a[180:183]\n"
        "s_waitcnt vmcnt(23) lgkmcnt(0)\n"
        "v_cndmask_b32 v96, v96, 0, s[78:79]\n"
        "v_cndmask_b32 v97, v97, 0, s[78:79]\n"
        "v_mfma_f32_16x16x32_bf16 a[180:183], v[12:15], v[84:87], a[180:183]\n"
        "v_lshlrev_b32 v174, 16, v96\n"
        "v_mul_f32 v189, v174, v188\n"
        "v_mfma_f32_16x16x32_bf16 a[148:151], v[4:7], v[84:87], a[148:151]\n"
        "v_cvt_pk_bf16_f32 v189, 0, v189\n"
        "v_and_b32 v174, s35, v96\n"
        "v_mfma_f32_16x16x32_bf16 a[148:151], v[0:3], v[52:55], a[148:151]\n"
        "global_load_dwordx2 v[112:113], v169, s[74:75] offset:-4096\n"
        "v_mfma_f32_16x16x32_bf16 a[152:155], v[0:3], v[56:59], a[152:155]\n"
        "v_mfma_f32_16x16x32_bf16 a[152:155], v[4:7], v[88:91], a[152:155]\n"
        "v_mfma_f32_16x16x32_bf16 a[184:187], v[12:15], v[88:91], a[184:187]\n"
        "v_mfma_f32_16x16x32_bf16 a[184:187], v[8:11], v[56:59], a[184:187]\n"
        "s_waitcnt vmcnt(15)\n"
        "v_mfma_f32_16x16x32_bf16 a[188:191], v[8:11], v[60:63], a[188:191]\n"
        "s_barrier\n"
        "v_mfma_f32_16x16x32_bf16 a[188:191], v[12:15], v[92:95], a[188:191]\n"
        "global_load_lds_dwordx4 v234, s[86:87] offset:0\n"
        "ds_read_b128 v[8:11], v224 offset:18432\n"
        "v_mfma_f32_16x16x32_bf16 a[156:159], v[4:7], v[92:95], a[156:159]\n"
        "ds_read_b128 v[4:7], v225 offset:16384\n"
        "v_mfma_f32_16x16x32_bf16 a[156:159], v[0:3], v[60:63], a[156:159]\n"
        "ds_read_b128 v[0:3], v224 offset:16384\n"
        "v_mfma_f32_16x16x32_bf16 a[192:195], v[32:35], v[16:19], a[192:195]\n"
        "ds_read_b128 v[12:15], v225 offset:18432\n"
        "v_mfma_f32_16x16x32_bf16 a[192:195], v[64:67], v[20:23], a[192:195]\n"
        "global_load_lds_dwordx4 v235, s[86:87] offset:1024\n"
        "v_mfma_f32_16x16x32_bf16 a[224:227], v[64:67], v[28:31], a[224:227]\n"
        "ds_read_b64_tr_b16 v[64:65], v216 offset:8192\n"
        "v_mfma_f32_16x16x32_bf16 a[224:227], v[32:35], v[24:27], a[224:227]\n"
        "ds_read_b64_tr_b16 v[32:33], v216 offset:0\n"
        "v_mfma_f32_16x16x32_bf16 a[228:231], v[36:39], v[24:27], a[228:231]\n"
        "ds_read_b64_tr_b16 v[34:35], v217 offset:0\n"
        "v_mfma_f32_16x16x32_bf16 a[228:231], v[68:71], v[28:31], a[228:231]\n"
        "ds_read_b64_tr_b16 v[66:67], v217 offset:8192\n"
        "global_load_lds_dwordx4 v236, s[86:87] offset:2048\n"
        "v_mfma_f32_16x16x32_bf16 a[196:199], v[68:71], v[20:23], a[196:199]\n"
        "ds_read_b64_tr_b16 v[68:69], v218 offset:8192\n"
        "v_mfma_f32_16x16x32_bf16 a[196:199], v[36:39], v[16:19], a[196:199]\n"
        "ds_read_b64_tr_b16 v[36:37], v218 offset:0\n"
        "v_mfma_f32_16x16x32_bf16 a[200:203], v[40:43], v[16:19], a[200:203]\n"
        "ds_read_b64_tr_b16 v[38:39], v219 offset:0\n"
        "v_mfma_f32_16x16x32_bf16 a[200:203], v[72:75], v[20:23], a[200:203]\n"
        "ds_read_b64_tr_b16 v[70:71], v219 offset:8192\n"
        "global_load_lds_dwordx4 v237, s[86:87] offset:3072\n"
        "s_add_u32 m0, s72, 0x9000\n"
        "v_mfma_f32_16x16x32_bf16 a[232:235], v[72:75], v[28:31], a[232:235]\n"
        "ds_read_b64_tr_b16 v[72:73], v220 offset:8192\n"
        "s_add_u32 s86, s58, 0x180\n"
        "s_addc_u32 s87, s59, 0\n"
        "v_mfma_f32_16x16x32_bf16 a[232:235], v[40:43], v[24:27], a[232:235]\n"
        "ds_read_b64_tr_b16 v[40:41], v220 offset:0\n"
        "v_cndmask_b32 v98, v98, 0, s[78:79]\n"
        "v_mfma_f32_16x16x32_bf16 a[236:239], v[44:47], v[24:27], a[236:239]\n"
        "ds_read_b64_tr_b16 v[42:43], v221 offset:0\n"
        "v_cndmask_b32 v99, v99, 0, s[78:79]\n"
        "v_mfma_f32_16x16x32_bf16 a[236:239], v[76:79], v[28:31], a[236:239]\n"
        "ds_read_b64_tr_b16 v[74:75], v221 offset:8192\n"
        "global_load_dwordx2 v[114:115], v169, s[74:75] offset:-2048\n"
        "v_mfma_f32_16x16x32_bf16 a[204:207], v[76:79], v[20:23], a[204:207]\n"
        "ds_read_b64_tr_b16 v[76:77], v222 offset:8192\n"
        "global_load_lds_dwordx4 v234, s[86:87] offset:0\n"
        "v_mfma_f32_16x16x32_bf16 a[204:207], v[44:47], v[16:19], a[204:207]\n"
        "ds_read_b64_tr_b16 v[44:45], v222 offset:0\n"
        "v_lshlrev_b32 v175, 16, v98\n"
        "v_mfma_f32_16x16x32_bf16 a[208:211], v[16:19], v[48:51], a[208:211]\n"
        "ds_read_b64_tr_b16 v[46:47], v223 offset:0\n"
        "v_mul_f32 v191, v175, v188\n"
        "v_mfma_f32_16x16x32_bf16 a[208:211], v[20:23], v[80:83], a[208:211]\n"
        "ds_read_b64_tr_b16 v[78:79], v223 offset:8192\n"
        "v_cvt_pk_bf16_f32 v191, 0, v191\n"
        "v_mfma_f32_16x16x32_bf16 a[240:243], v[28:31], v[80:83], a[240:243]\n"
        "ds_read_b64_tr_b16 v[80:81], v216 offset:24576\n"
        "global_load_lds_dwordx4 v235, s[86:87] offset:1024\n"
        "v_mfma_f32_16x16x32_bf16 a[240:243], v[24:27], v[48:51], a[240:243]\n"
        "ds_read_b64_tr_b16 v[48:49], v216 offset:16384\n"
        "v_mul_f32 v190, v175, v187\n"
        "v_mfma_f32_16x16x32_bf16 a[244:247], v[24:27], v[52:55], a[244:247]\n"
        "ds_read_b64_tr_b16 v[50:51], v217 offset:16384\n"
        "v_cvt_pk_bf16_f32 v190, 0, v190\n"
        "v_mfma_f32_16x16x32_bf16 a[244:247], v[28:31], v[84:87], a[244:247]\n"
        "ds_read_b64_tr_b16 v[82:83], v217 offset:24576\n"
        "v_add_f32 v189, v189, v190\n"
        "v_mfma_f32_16x16x32_bf16 a[212:215], v[20:23], v[84:87], a[212:215]\n"
        "ds_read_b64_tr_b16 v[84:85], v218 offset:24576\n"
        "global_load_lds_dwordx4 v236, s[86:87] offset:2048\n"
        "v_mfma_f32_16x16x32_bf16 a[212:215], v[16:19], v[52:55], a[212:215]\n"
        "ds_read_b64_tr_b16 v[52:53], v218 offset:16384\n"
        "v_cvt_pk_bf16_f32 v189, 0, v189\n"
        "v_mfma_f32_16x16x32_bf16 a[216:219], v[16:19], v[56:59], a[216:219]\n"
        "ds_read_b64_tr_b16 v[54:55], v219 offset:16384\n"
        "v_and_b32 v175, s35, v98\n"
        "v_mfma_f32_16x16x32_bf16 a[216:219], v[20:23], v[88:91], a[216:219]\n"
        "ds_read_b64_tr_b16 v[86:87], v219 offset:24576\n"
        "v_mfma_f32_16x16x32_bf16 a[248:251], v[28:31], v[88:91], a[248:251]\n"
        "ds_read_b64_tr_b16 v[88:89], v220 offset:24576\n"
        "global_load_lds_dwordx4 v237, s[86:87] offset:3072\n"
        "s_add_u32 m0, s73, 0x18000\n"
        "v_mfma_f32_16x16x32_bf16 a[248:251], v[24:27], v[56:59], a[248:251]\n"
        "ds_read_b64_tr_b16 v[56:57], v220 offset:16384\n"
        "s_add_u32 s86, s60, 0x200000\n"
        "s_addc_u32 s87, s61, 0\n"
        "v_mfma_f32_16x16x32_bf16 a[252:255], v[24:27], v[60:63], a[252:255]\n"
        "ds_read_b64_tr_b16 v[58:59], v221 offset:16384\n"
        "s_waitcnt lgkmcnt(8)\n"
        "v_mfma_f32_16x16x32_bf16 a[252:255], v[28:31], v[92:95], a[252:255]\n"
        "ds_read_b64_tr_b16 v[90:91], v221 offset:24576\n"
        "s_waitcnt vmcnt(23) lgkmcnt(8)\n"
        "v_mfma_f32_16x16x32_bf16 a[220:223], v[20:23], v[92:95], a[220:223]\n"
        "ds_read_b64_tr_b16 v[92:93], v222 offset:24576\n"
        "global_load_dwordx2 v[116:117], v169, s[74:75] offset:0\n"
        "v_mfma_f32_16x16x32_bf16 a[220:223], v[16:19], v[60:63], a[220:223]\n"
        "ds_read_b64_tr_b16 v[60:61], v222 offset:16384\n"
        "global_load_lds_dwordx4 v238, s[86:87] offset:0\n"
        "v_mfma_f32_16x16x32_bf16 a[0:3], v[32:35], v[0:3], a[0:3]\n"
        "ds_read_b64_tr_b16 v[62:63], v223 offset:16384\n"
        "s_add_u32 s74, s22, 0x1000\n"
        "s_addc_u32 s75, s23, 0\n"
        "v_mfma_f32_16x16x32_bf16 a[0:3], v[64:67], v[4:7], a[0:3]\n"
        "ds_read_b64_tr_b16 v[94:95], v223 offset:24576\n"
        "v_cndmask_b32 v100, v100, 0, s[78:79]\n"
        "v_mfma_f32_16x16x32_bf16 a[32:35], v[64:67], v[12:15], a[32:35]\n"
        "ds_read_b128 v[16:19], v224 offset:20480\n"
        "v_cndmask_b32 v101, v101, 0, s[78:79]\n"
        "v_mfma_f32_16x16x32_bf16 a[32:35], v[32:35], v[8:11], a[32:35]\n"
        "ds_read_b128 v[20:23], v225 offset:20480\n"
        "global_load_lds_dwordx4 v239, s[86:87] offset:1024\n"
        "v_mfma_f32_16x16x32_bf16 a[36:39], v[36:39], v[8:11], a[36:39]\n"
        "ds_read_b128 v[24:27], v224 offset:22528\n"
        "v_lshlrev_b32 v176, 16, v100\n"
        "v_mfma_f32_16x16x32_bf16 a[36:39], v[68:71], v[12:15], a[36:39]\n"
        "ds_read_b128 v[28:31], v225 offset:22528\n"
        "v_mul_f32 v192, v176, v187\n"
        "v_mfma_f32_16x16x32_bf16 a[4:7], v[68:71], v[4:7], a[4:7]\n"
        "v_cvt_pk_bf16_f32 v192, 0, v192\n"
        "v_add_f32 v191, v191, v192\n"
        "v_mfma_f32_16x16x32_bf16 a[4:7], v[36:39], v[0:3], a[4:7]\n"
        "global_load_lds_dwordx4 v240, s[86:87] offset:2048\n"
        "v_mfma_f32_16x16x32_bf16 a[8:11], v[40:43], v[0:3], a[8:11]\n"
        "v_cvt_pk_bf16_f32 v191, 0, v191\n"
        "v_mul_f32 v190, v176, v186\n"
        "v_mfma_f32_16x16x32_bf16 a[8:11], v[72:75], v[4:7], a[8:11]\n"
        "v_cvt_pk_bf16_f32 v190, 0, v190\n"
        "v_add_f32 v189, v189, v190\n"
        "v_mfma_f32_16x16x32_bf16 a[40:43], v[72:75], v[12:15], a[40:43]\n"
        "v_cvt_pk_bf16_f32 v189, 0, v189\n"
        "v_mul_f32 v193, v176, v188\n"
        "v_mfma_f32_16x16x32_bf16 a[40:43], v[40:43], v[8:11], a[40:43]\n"
        "global_load_lds_dwordx4 v241, s[86:87] offset:3072\n"
        "s_add_u32 m0, s73, 0x1c000\n"
        "v_mfma_f32_16x16x32_bf16 a[44:47], v[44:47], v[8:11], a[44:47]\n"
        "s_add_u32 s86, s62, 0x200000\n"
        "s_addc_u32 s87, s63, 0\n"
        "v_cvt_pk_bf16_f32 v193, 0, v193\n"
        "v_mfma_f32_16x16x32_bf16 a[44:47], v[76:79], v[12:15], a[44:47]\n"
        "s_waitcnt vmcnt(23) lgkmcnt(0)\n"
        "v_lshlrev_b32 v177, 16, v102\n"
        "v_mul_f32 v192, v177, v186\n"
        "v_mfma_f32_16x16x32_bf16 a[12:15], v[76:79], v[4:7], a[12:15]\n"
        "v_cvt_pk_bf16_f32 v192, 0, v192\n"
        "v_add_f32 v191, v191, v192\n"
        "v_mfma_f32_16x16x32_bf16 a[12:15], v[44:47], v[0:3], a[12:15]\n"
        "global_load_dwordx2 v[118:119], v169, s[74:75] offset:-4096\n"
        "v_mfma_f32_16x16x32_bf16 a[16:19], v[0:3], v[48:51], a[16:19]\n"
        "global_load_lds_dwordx4 v238, s[86:87] offset:0\n"
        "v_cvt_pk_bf16_f32 v191, 0, v191\n"
        "v_mfma_f32_16x16x32_bf16 a[16:19], v[4:7], v[80:83], a[16:19]\n"
        "v_mul_f32 v190, v177, v185\n"
        "v_cvt_pk_bf16_f32 v190, 0, v190\n"
        "v_mfma_f32_16x16x32_bf16 a[48:51], v[12:15], v[80:83], a[48:51]\n"
        "v_add_f32 v189, v189, v190\n"
        "v_cvt_pk_bf16_f32 v189, 0, v189\n"
        "v_mfma_f32_16x16x32_bf16 a[48:51], v[8:11], v[48:51], a[48:51]\n"
        "v_mul_f32 v195, v177, v188\n"
        "v_cvt_pk_bf16_f32 v195, 0, v195\n"
        "v_mfma_f32_16x16x32_bf16 a[52:55], v[8:11], v[52:55], a[52:55]\n"
        "global_load_lds_dwordx4 v239, s[86:87] offset:1024\n"
        "v_mul_f32 v194, v177, v187\n"
        "v_mfma_f32_16x16x32_bf16 a[52:55], v[12:15], v[84:87], a[52:55]\n"
        "v_cvt_pk_bf16_f32 v194, 0, v194\n"
        "v_add_f32 v193, v193, v194\n"
        "v_mfma_f32_16x16x32_bf16 a[20:23], v[4:7], v[84:87], a[20:23]\n"
        "v_cvt_pk_bf16_f32 v193, 0, v193\n"
        "v_and_b32 v176, s35, v100\n"
        "v_mfma_f32_16x16x32_bf16 a[20:23], v[0:3], v[52:55], a[20:23]\n"
        "v_and_b32 v177, s35, v102\n"
        "v_mfma_f32_16x16x32_bf16 a[24:27], v[0:3], v[56:59], a[24:27]\n"
        "global_load_lds_dwordx4 v240, s[86:87] offset:2048\n"
        "v_mfma_f32_16x16x32_bf16 a[24:27], v[4:7], v[88:91], a[24:27]\n"
        "v_mfma_f32_16x16x32_bf16 a[56:59], v[12:15], v[88:91], a[56:59]\n"
        "v_mfma_f32_16x16x32_bf16 a[56:59], v[8:11], v[56:59], a[56:59]\n"
        "v_mfma_f32_16x16x32_bf16 a[60:63], v[8:11], v[60:63], a[60:63]\n"
        "global_load_lds_dwordx4 v241, s[86:87] offset:3072\n"
        "ds_read_b128 v[8:11], v224 offset:2048\n"
        "s_add_u32 m0, s72, 0x0\n"
        "v_mfma_f32_16x16x32_bf16 a[60:63], v[12:15], v[92:95], a[60:63]\n"
        "ds_read_b128 v[12:15], v225 offset:2048\n"
        "s_add_u32 s86, s56, 0x200\n"
        "s_addc_u32 s87, s57, 0\n"
        "v_mfma_f32_16x16x32_bf16 a[28:31], v[4:7], v[92:95], a[28:31]\n"
        "ds_read_b128 v[4:7], v225 offset:0\n"
        "s_waitcnt vmcnt(23)\n"
        "v_mfma_f32_16x16x32_bf16 a[28:31], v[0:3], v[60:63], a[28:31]\n"
        "ds_read_b128 v[0:3], v224 offset:0\n"
        "v_lshlrev_b32 v178, 16, v104\n"
        "v_mfma_f32_16x16x32_bf16 a[64:67], v[32:35], v[16:19], a[64:67]\n"
        "global_load_dwordx2 v[120:121], v169, s[74:75] offset:-2048\n"
        "v_mul_f32 v192, v178, v185\n"
        "v_mfma_f32_16x16x32_bf16 a[64:67], v[64:67], v[20:23], a[64:67]\n"
        "v_cvt_pk_bf16_f32 v192, 0, v192\n"
        "v_add_f32 v191, v191, v192\n"
        "v_mfma_f32_16x16x32_bf16 a[96:99], v[64:67], v[28:31], a[96:99]\n"
        "v_cvt_pk_bf16_f32 v191, 0, v191\n"
        "v_mul_f32 v196, v178, v187\n"
        "v_mfma_f32_16x16x32_bf16 a[96:99], v[32:35], v[24:27], a[96:99]\n"
        "v_cvt_pk_bf16_f32 v196, 0, v196\n"
        "v_add_f32 v195, v195, v196\n"
        "v_mfma_f32_16x16x32_bf16 a[100:103], v[36:39], v[24:27], a[100:103]\n"
        "s_waitcnt vmcnt(23)\n"
        "v_lshlrev_b32 v179, 16, v106\n"
        "v_cvt_pk_bf16_f32 v195, 0, v195\n"
        "v_mfma_f32_16x16x32_bf16 a[100:103], v[68:71], v[28:31], a[100:103]\n"
        "v_mul_f32 v194, v178, v186\n"
        "v_mul_f32 v196, v179, v186\n"
        "v_mfma_f32_16x16x32_bf16 a[68:71], v[68:71], v[20:23], a[68:71]\n"
        "v_cvt_pk_bf16_f32 v194, 0, v194\n"
        "v_cvt_pk_bf16_f32 v196, 0, v196\n"
        "v_mfma_f32_16x16x32_bf16 a[68:71], v[36:39], v[16:19], a[68:71]\n"
        "global_load_dword v253, v173, s[82:83] offset:0\n"
        "s_add_u32 s82, s12, 0x100000\n"
        "s_addc_u32 s83, s13, 0\n"
        "v_mfma_f32_16x16x32_bf16 a[72:75], v[40:43], v[16:19], a[72:75]\n"
        "global_load_dwordx2 v[122:123], v169, s[74:75] offset:0\n"
        "v_add_f32 v193, v193, v194\n"
        "v_mfma_f32_16x16x32_bf16 a[72:75], v[72:75], v[20:23], a[72:75]\n"
        "v_add_f32 v195, v195, v196\n"
        "v_cvt_pk_bf16_f32 v193, 0, v193\n"
        "v_mfma_f32_16x16x32_bf16 a[104:107], v[72:75], v[28:31], a[104:107]\n"
        "v_cvt_pk_bf16_f32 v195, 0, v195\n"
        "v_mul_f32 v194, v179, v185\n"
        "v_mfma_f32_16x16x32_bf16 a[104:107], v[40:43], v[24:27], a[104:107]\n"
        "v_cvt_pk_bf16_f32 v194, 0, v194\n"
        "v_add_f32 v193, v193, v194\n"
        "v_mfma_f32_16x16x32_bf16 a[108:111], v[44:47], v[24:27], a[108:111]\n"
        "v_cvt_pk_bf16_f32 v193, 0, v193\n"
        "v_mfma_f32_16x16x32_bf16 a[108:111], v[76:79], v[28:31], a[108:111]\n"
        "v_mfma_f32_16x16x32_bf16 a[76:79], v[76:79], v[20:23], a[76:79]\n"
        "v_mfma_f32_16x16x32_bf16 a[76:79], v[44:47], v[16:19], a[76:79]\n"
        "v_mfma_f32_16x16x32_bf16 a[80:83], v[16:19], v[48:51], a[80:83]\n"
        "v_mfma_f32_16x16x32_bf16 a[80:83], v[20:23], v[80:83], a[80:83]\n"
        "v_mfma_f32_16x16x32_bf16 a[112:115], v[28:31], v[80:83], a[112:115]\n"
        "v_mfma_f32_16x16x32_bf16 a[112:115], v[24:27], v[48:51], a[112:115]\n"
        "v_mfma_f32_16x16x32_bf16 a[116:119], v[24:27], v[52:55], a[116:119]\n"
        "s_waitcnt vmcnt(24) lgkmcnt(0)\n"
        "v_lshlrev_b32 v180, 16, v108\n"
        "v_mul_f32 v196, v180, v185\n"
        "v_mfma_f32_16x16x32_bf16 a[116:119], v[28:31], v[84:87], a[116:119]\n"
        "v_cvt_pk_bf16_f32 v196, 0, v196\n"
        "v_add_f32 v195, v195, v196\n"
        "v_mfma_f32_16x16x32_bf16 a[84:87], v[20:23], v[84:87], a[84:87]\n"
        "v_cvt_pk_bf16_f32 v195, 0, v195\n"
        "v_mfma_f32_16x16x32_bf16 a[84:87], v[16:19], v[52:55], a[84:87]\n"
        "global_load_dwordx2 v[124:125], v169, s[74:75] offset:2048\n"
        "s_add_u32 s74, s22, 0x3000\n"
        "s_addc_u32 s75, s23, 0\n"
        "v_mfma_f32_16x16x32_bf16 a[88:91], v[16:19], v[56:59], a[88:91]\n"
        "v_mfma_f32_16x16x32_bf16 a[88:91], v[20:23], v[88:91], a[88:91]\n"
        "v_mfma_f32_16x16x32_bf16 a[120:123], v[28:31], v[88:91], a[120:123]\n"
        "v_mfma_f32_16x16x32_bf16 a[120:123], v[24:27], v[56:59], a[120:123]\n"
        "v_mfma_f32_16x16x32_bf16 a[124:127], v[24:27], v[60:63], a[124:127]\n"
        "ds_read_b128 v[24:27], v224 offset:6144\n"
        "v_mfma_f32_16x16x32_bf16 a[124:127], v[28:31], v[92:95], a[124:127]\n"
        "ds_read_b128 v[28:31], v225 offset:6144\n"
        "v_mfma_f32_16x16x32_bf16 a[92:95], v[20:23], v[92:95], a[92:95]\n"
        "ds_read_b128 v[20:23], v225 offset:4096\n"
        "v_mfma_f32_16x16x32_bf16 a[92:95], v[16:19], v[60:63], a[92:95]\n"
        "ds_read_b128 v[16:19], v224 offset:4096\n"
        "v_mfma_f32_16x16x32_bf16 a[128:131], v[32:35], v[0:3], a[128:131]\n"
        "v_mfma_f32_16x16x32_bf16 a[128:131], v[64:67], v[4:7], a[128:131]\n"
        "v_mfma_f32_16x16x32_bf16 a[160:163], v[64:67], v[12:15], a[160:163]\n"
        "v_mfma_f32_16x16x32_bf16 a[160:163], v[32:35], v[8:11], a[160:163]\n"
        "v_mfma_f32_16x16x32_bf16 a[164:167], v[36:39], v[8:11], a[164:167]\n"
        "s_waitcnt vmcnt(24) lgkmcnt(3)\n"
        "v_lshlrev_b32 v181, 16, v110\n"
        "v_mfma_f32_16x16x32_bf16 a[164:167], v[68:71], v[12:15], a[164:167]\n"
        "v_mfma_f32_16x16x32_bf16 a[132:135], v[68:71], v[4:7], a[132:135]\n"
        "v_mfma_f32_16x16x32_bf16 a[132:135], v[36:39], v[0:3], a[132:135]\n"
        "global_load_dwordx2 v[126:127], v169, s[74:75] offset:-4096\n"
        "v_mfma_f32_16x16x32_bf16 a[136:139], v[40:43], v[0:3], a[136:139]\n"
        "v_mfma_f32_16x16x32_bf16 a[136:139], v[72:75], v[4:7], a[136:139]\n"
        "v_mfma_f32_16x16x32_bf16 a[168:171], v[72:75], v[12:15], a[168:171]\n"
        "v_mfma_f32_16x16x32_bf16 a[168:171], v[40:43], v[8:11], a[168:171]\n"
        "v_mfma_f32_16x16x32_bf16 a[172:175], v[44:47], v[8:11], a[172:175]\n"
        "v_mfma_f32_16x16x32_bf16 a[172:175], v[76:79], v[12:15], a[172:175]\n"
        "v_mfma_f32_16x16x32_bf16 a[140:143], v[76:79], v[4:7], a[140:143]\n"
        "v_mfma_f32_16x16x32_bf16 a[140:143], v[44:47], v[0:3], a[140:143]\n"
        "v_mfma_f32_16x16x32_bf16 a[144:147], v[0:3], v[48:51], a[144:147]\n"
        "v_mfma_f32_16x16x32_bf16 a[144:147], v[4:7], v[80:83], a[144:147]\n"
        "v_mfma_f32_16x16x32_bf16 a[176:179], v[12:15], v[80:83], a[176:179]\n"
        "v_mfma_f32_16x16x32_bf16 a[176:179], v[8:11], v[48:51], a[176:179]\n"
        "v_mfma_f32_16x16x32_bf16 a[180:183], v[8:11], v[52:55], a[180:183]\n"
        "s_waitcnt vmcnt(24) lgkmcnt(0)\n"
        "v_lshlrev_b32 v182, 16, v112\n"
        "v_mfma_f32_16x16x32_bf16 a[180:183], v[12:15], v[84:87], a[180:183]\n"
        "v_mfma_f32_16x16x32_bf16 a[148:151], v[4:7], v[84:87], a[148:151]\n"
        "v_mfma_f32_16x16x32_bf16 a[148:151], v[0:3], v[52:55], a[148:151]\n"
        "global_load_dwordx2 v[128:129], v169, s[74:75] offset:-2048\n"
        "v_mfma_f32_16x16x32_bf16 a[152:155], v[0:3], v[56:59], a[152:155]\n"
        "v_mfma_f32_16x16x32_bf16 a[152:155], v[4:7], v[88:91], a[152:155]\n"
        "v_mfma_f32_16x16x32_bf16 a[184:187], v[12:15], v[88:91], a[184:187]\n"
        "v_mfma_f32_16x16x32_bf16 a[184:187], v[8:11], v[56:59], a[184:187]\n"
        "s_waitcnt vmcnt(16)\n"
        "v_mfma_f32_16x16x32_bf16 a[188:191], v[8:11], v[60:63], a[188:191]\n"
        "s_barrier\n"
        "v_mfma_f32_16x16x32_bf16 a[188:191], v[12:15], v[92:95], a[188:191]\n"
        "global_load_lds_dwordx4 v234, s[86:87] offset:0\n"
        "ds_read_b128 v[8:11], v224 offset:51200\n"
        "v_mfma_f32_16x16x32_bf16 a[156:159], v[4:7], v[92:95], a[156:159]\n"
        "ds_read_b128 v[4:7], v225 offset:49152\n"
        "v_mfma_f32_16x16x32_bf16 a[156:159], v[0:3], v[60:63], a[156:159]\n"
        "ds_read_b128 v[0:3], v224 offset:49152\n"
        "v_mfma_f32_16x16x32_bf16 a[192:195], v[32:35], v[16:19], a[192:195]\n"
        "ds_read_b128 v[12:15], v225 offset:51200\n"
        "v_mfma_f32_16x16x32_bf16 a[192:195], v[64:67], v[20:23], a[192:195]\n"
        "global_load_lds_dwordx4 v235, s[86:87] offset:1024\n"
        "v_mfma_f32_16x16x32_bf16 a[224:227], v[64:67], v[28:31], a[224:227]\n"
        "ds_read_b64_tr_b16 v[64:65], v226 offset:8192\n"
        "v_mfma_f32_16x16x32_bf16 a[224:227], v[32:35], v[24:27], a[224:227]\n"
        "ds_read_b64_tr_b16 v[32:33], v226 offset:0\n"
        "v_mfma_f32_16x16x32_bf16 a[228:231], v[36:39], v[24:27], a[228:231]\n"
        "ds_read_b64_tr_b16 v[34:35], v227 offset:0\n"
        "v_mfma_f32_16x16x32_bf16 a[228:231], v[68:71], v[28:31], a[228:231]\n"
        "ds_read_b64_tr_b16 v[66:67], v227 offset:8192\n"
        "global_load_lds_dwordx4 v236, s[86:87] offset:2048\n"
        "v_mfma_f32_16x16x32_bf16 a[196:199], v[68:71], v[20:23], a[196:199]\n"
        "ds_read_b64_tr_b16 v[68:69], v228 offset:8192\n"
        "v_mfma_f32_16x16x32_bf16 a[196:199], v[36:39], v[16:19], a[196:199]\n"
        "ds_read_b64_tr_b16 v[36:37], v228 offset:0\n"
        "v_mfma_f32_16x16x32_bf16 a[200:203], v[40:43], v[16:19], a[200:203]\n"
        "ds_read_b64_tr_b16 v[38:39], v229 offset:0\n"
        "v_mfma_f32_16x16x32_bf16 a[200:203], v[72:75], v[20:23], a[200:203]\n"
        "ds_read_b64_tr_b16 v[70:71], v229 offset:8192\n"
        "global_load_lds_dwordx4 v237, s[86:87] offset:3072\n"
        "s_add_u32 m0, s72, 0x1000\n"
        "v_mfma_f32_16x16x32_bf16 a[232:235], v[72:75], v[28:31], a[232:235]\n"
        "ds_read_b64_tr_b16 v[72:73], v230 offset:8192\n"
        "s_add_u32 s86, s58, 0x200\n"
        "s_addc_u32 s87, s59, 0\n"
        "v_mfma_f32_16x16x32_bf16 a[232:235], v[40:43], v[24:27], a[232:235]\n"
        "ds_read_b64_tr_b16 v[40:41], v230 offset:0\n"
        "v_lshlrev_b32 v183, 16, v114\n"
        "v_mfma_f32_16x16x32_bf16 a[236:239], v[44:47], v[24:27], a[236:239]\n"
        "ds_read_b64_tr_b16 v[42:43], v231 offset:0\n"
        "v_mfma_f32_16x16x32_bf16 a[236:239], v[76:79], v[28:31], a[236:239]\n"
        "ds_read_b64_tr_b16 v[74:75], v231 offset:8192\n"
        "global_load_dwordx2 v[130:131], v169, s[74:75] offset:0\n"
        "v_mfma_f32_16x16x32_bf16 a[204:207], v[76:79], v[20:23], a[204:207]\n"
        "ds_read_b64_tr_b16 v[76:77], v232 offset:8192\n"
        "global_load_lds_dwordx4 v234, s[86:87] offset:0\n"
        "v_mfma_f32_16x16x32_bf16 a[204:207], v[44:47], v[16:19], a[204:207]\n"
        "ds_read_b64_tr_b16 v[44:45], v232 offset:0\n"
        "v_mfma_f32_16x16x32_bf16 a[208:211], v[16:19], v[48:51], a[208:211]\n"
        "ds_read_b64_tr_b16 v[46:47], v233 offset:0\n"
        "v_mfma_f32_16x16x32_bf16 a[208:211], v[20:23], v[80:83], a[208:211]\n"
        "ds_read_b64_tr_b16 v[78:79], v233 offset:8192\n"
        "v_mfma_f32_16x16x32_bf16 a[240:243], v[28:31], v[80:83], a[240:243]\n"
        "ds_read_b64_tr_b16 v[80:81], v226 offset:24576\n"
        "global_load_lds_dwordx4 v235, s[86:87] offset:1024\n"
        "v_mfma_f32_16x16x32_bf16 a[240:243], v[24:27], v[48:51], a[240:243]\n"
        "ds_read_b64_tr_b16 v[48:49], v226 offset:16384\n"
        "v_mfma_f32_16x16x32_bf16 a[244:247], v[24:27], v[52:55], a[244:247]\n"
        "ds_read_b64_tr_b16 v[50:51], v227 offset:16384\n"
        "v_mfma_f32_16x16x32_bf16 a[244:247], v[28:31], v[84:87], a[244:247]\n"
        "ds_read_b64_tr_b16 v[82:83], v227 offset:24576\n"
        "v_mfma_f32_16x16x32_bf16 a[212:215], v[20:23], v[84:87], a[212:215]\n"
        "ds_read_b64_tr_b16 v[84:85], v228 offset:24576\n"
        "global_load_lds_dwordx4 v236, s[86:87] offset:2048\n"
        "v_mfma_f32_16x16x32_bf16 a[212:215], v[16:19], v[52:55], a[212:215]\n"
        "ds_read_b64_tr_b16 v[52:53], v228 offset:16384\n"
        "v_mfma_f32_16x16x32_bf16 a[216:219], v[16:19], v[56:59], a[216:219]\n"
        "ds_read_b64_tr_b16 v[54:55], v229 offset:16384\n"
        "v_mfma_f32_16x16x32_bf16 a[216:219], v[20:23], v[88:91], a[216:219]\n"
        "ds_read_b64_tr_b16 v[86:87], v229 offset:24576\n"
        "v_mfma_f32_16x16x32_bf16 a[248:251], v[28:31], v[88:91], a[248:251]\n"
        "global_load_dword v253, v173, s[82:83] offset:0\n"
        "v_mfma_f32_16x16x32_bf16 a[248:251], v[24:27], v[56:59], a[248:251]\n"
        "ds_read_b64_tr_b16 v[56:57], v230 offset:16384\n"
        "global_load_lds_dwordx4 v237, s[86:87] offset:3072\n"
        "s_add_u32 m0, s73, 0x20000\n"
        "v_mfma_f32_16x16x32_bf16 a[252:255], v[24:27], v[60:63], a[252:255]\n"
        "ds_read_b64_tr_b16 v[58:59], v231 offset:16384\n"
        "s_add_u32 s86, s60, 0x280000\n"
        "s_addc_u32 s87, s61, 0\n"
        "v_mfma_f32_16x16x32_bf16 a[252:255], v[28:31], v[92:95], a[252:255]\n"
        "ds_read_b64_tr_b16 v[88:89], v230 offset:24576\n"
        "s_add_u32 s82, s12, 0x200000\n"
        "s_addc_u32 s83, s13, 0\n"
        "v_mfma_f32_16x16x32_bf16 a[220:223], v[20:23], v[92:95], a[220:223]\n"
        "ds_read_b64_tr_b16 v[90:91], v231 offset:24576\n"
        "s_waitcnt lgkmcnt(7)\n"
        "v_mfma_f32_16x16x32_bf16 a[220:223], v[16:19], v[60:63], a[220:223]\n"
        "ds_read_b64_tr_b16 v[60:61], v232 offset:16384\n"
        "global_load_dwordx2 v[132:133], v169, s[74:75] offset:2048\n"
        "v_mfma_f32_16x16x32_bf16 a[0:3], v[32:35], v[0:3], a[0:3]\n"
        "ds_read_b64_tr_b16 v[62:63], v233 offset:16384\n"
        "global_load_lds_dwordx4 v238, s[86:87] offset:0\n"
        "v_mfma_f32_16x16x32_bf16 a[0:3], v[64:67], v[4:7], a[0:3]\n"
        "ds_read_b64_tr_b16 v[92:93], v232 offset:24576\n"
        "s_waitcnt vmcnt(27) lgkmcnt(7)\n"
        "v_mfma_f32_16x16x32_bf16 a[32:35], v[64:67], v[12:15], a[32:35]\n"
        "ds_read_b64_tr_b16 v[94:95], v233 offset:24576\n"
        "v_lshlrev_b32 v184, 16, v116\n"
        "v_mfma_f32_16x16x32_bf16 a[32:35], v[32:35], v[8:11], a[32:35]\n"
        "ds_read_b128 v[16:19], v224 offset:53248\n"
        "v_mfma_f32_16x16x32_bf16 a[36:39], v[36:39], v[8:11], a[36:39]\n"
        "ds_read_b128 v[20:23], v225 offset:53248\n"
        "global_load_lds_dwordx4 v239, s[86:87] offset:1024\n"
        "v_mfma_f32_16x16x32_bf16 a[36:39], v[68:71], v[12:15], a[36:39]\n"
        "ds_read_b128 v[24:27], v224 offset:55296\n"
        "v_mfma_f32_16x16x32_bf16 a[4:7], v[68:71], v[4:7], a[4:7]\n"
        "ds_read_b128 v[28:31], v225 offset:55296\n"
        "v_mfma_f32_16x16x32_bf16 a[4:7], v[36:39], v[0:3], a[4:7]\n"
        "v_mfma_f32_16x16x32_bf16 a[8:11], v[40:43], v[0:3], a[8:11]\n"
        "global_load_lds_dwordx4 v240, s[86:87] offset:2048\n"
        "v_mfma_f32_16x16x32_bf16 a[8:11], v[72:75], v[4:7], a[8:11]\n"
        "v_mfma_f32_16x16x32_bf16 a[40:43], v[72:75], v[12:15], a[40:43]\n"
        "v_mfma_f32_16x16x32_bf16 a[40:43], v[40:43], v[8:11], a[40:43]\n"
        "v_mfma_f32_16x16x32_bf16 a[44:47], v[44:47], v[8:11], a[44:47]\n"
        "global_load_lds_dwordx4 v241, s[86:87] offset:3072\n"
        "s_add_u32 m0, s73, 0x24000\n"
        "v_mfma_f32_16x16x32_bf16 a[44:47], v[76:79], v[12:15], a[44:47]\n"
        "s_add_u32 s86, s62, 0x280000\n"
        "s_addc_u32 s87, s63, 0\n"
        "s_waitcnt vmcnt(25) lgkmcnt(1)\n"
        "v_mfma_f32_16x16x32_bf16 a[12:15], v[76:79], v[4:7], a[12:15]\n"
        "v_lshlrev_b32 v190, 16, v118\n"
        "v_mul_f32 v189, v189, v190\n"
        "v_mfma_f32_16x16x32_bf16 a[12:15], v[44:47], v[0:3], a[12:15]\n"
        "v_mul_f32 v190, v179, v187\n"
        "v_cvt_pk_bf16_f32 v190, 0, v190\n"
        "v_mfma_f32_16x16x32_bf16 a[16:19], v[0:3], v[48:51], a[16:19]\n"
        "global_load_lds_dwordx4 v238, s[86:87] offset:0\n"
        "v_mfma_f32_16x16x32_bf16 a[16:19], v[4:7], v[80:83], a[16:19]\n"
        "v_mfma_f32_16x16x32_bf16 a[48:51], v[12:15], v[80:83], a[48:51]\n"
        "v_mfma_f32_16x16x32_bf16 a[48:51], v[8:11], v[48:51], a[48:51]\n"
        "v_mfma_f32_16x16x32_bf16 a[52:55], v[8:11], v[52:55], a[52:55]\n"
        "global_load_lds_dwordx4 v239, s[86:87] offset:1024\n"
        "v_mfma_f32_16x16x32_bf16 a[52:55], v[12:15], v[84:87], a[52:55]\n"
        "v_mfma_f32_16x16x32_bf16 a[20:23], v[4:7], v[84:87], a[20:23]\n"
        "v_mfma_f32_16x16x32_bf16 a[20:23], v[0:3], v[52:55], a[20:23]\n"
        "v_mfma_f32_16x16x32_bf16 a[24:27], v[0:3], v[56:59], a[24:27]\n"
        "global_load_lds_dwordx4 v240, s[86:87] offset:2048\n"
        "v_mfma_f32_16x16x32_bf16 a[24:27], v[4:7], v[88:91], a[24:27]\n"
        "v_mfma_f32_16x16x32_bf16 a[56:59], v[12:15], v[88:91], a[56:59]\n"
        "v_mfma_f32_16x16x32_bf16 a[56:59], v[8:11], v[56:59], a[56:59]\n"
        "v_mfma_f32_16x16x32_bf16 a[60:63], v[8:11], v[60:63], a[60:63]\n"
        "global_load_lds_dwordx4 v241, s[86:87] offset:3072\n"
        "s_add_u32 m0, s72, 0x8000\n"
        "v_mfma_f32_16x16x32_bf16 a[60:63], v[12:15], v[92:95], a[60:63]\n"
        "ds_read_b128 v[8:11], v224 offset:34816\n"
        "s_add_u32 s86, s56, 0x280\n"
        "s_addc_u32 s87, s57, 0\n"
        "v_mfma_f32_16x16x32_bf16 a[28:31], v[4:7], v[92:95], a[28:31]\n"
        "ds_read_b128 v[4:7], v225 offset:32768\n"
        "s_waitcnt lgkmcnt(2)\n"
        "v_mfma_f32_16x16x32_bf16 a[28:31], v[0:3], v[60:63], a[28:31]\n"
        "ds_read_b128 v[0:3], v224 offset:32768\n"
        "s_waitcnt vmcnt(24)\n"
        "v_mfma_f32_16x16x32_bf16 a[64:67], v[32:35], v[16:19], a[64:67]\n"
        "ds_read_b128 v[12:15], v225 offset:34816\n"
        "global_load_dwordx4 v[142:145], v254, s[92:93] offset:0\n"
        "v_mfma_f32_16x16x32_bf16 a[64:67], v[64:67], v[20:23], a[64:67]\n"
        "v_lshlrev_b32 v192, 16, v120\n"
        "v_mul_f32 v191, v191, v192\n"
        "v_mfma_f32_16x16x32_bf16 a[96:99], v[64:67], v[28:31], a[96:99]\n"
        "v_cvt_pk_bf16_f32 v203, v191, v189\n"
        "v_mul_f32 v189, v178, v188\n"
        "v_mfma_f32_16x16x32_bf16 a[96:99], v[32:35], v[24:27], a[96:99]\n"
        "v_mul_f32 v191, v179, v188\n"
        "v_cvt_pk_bf16_f32 v189, 0, v189\n"
        "v_mfma_f32_16x16x32_bf16 a[100:103], v[36:39], v[24:27], a[100:103]\n"
        "v_cvt_pk_bf16_f32 v191, 0, v191\n"
        "v_mul_f32 v192, v180, v187\n"
        "v_mfma_f32_16x16x32_bf16 a[100:103], v[68:71], v[28:31], a[100:103]\n"
        "s_waitcnt vmcnt(23) lgkmcnt(3)\n"
        "v_lshlrev_b32 v194, 16, v122\n"
        "v_mul_f32 v193, v193, v194\n"
        "v_mfma_f32_16x16x32_bf16 a[68:71], v[68:71], v[20:23], a[68:71]\n"
        "LHA1_%=:\n"
        "s_cbranch_execz LHA5_%=\n"
        "v_cvt_pk_bf16_f32 v192, 0, v192\n"
        "v_add_f32 v189, v189, v190\n"
        "v_mfma_f32_16x16x32_bf16 a[68:71], v[36:39], v[16:19], a[68:71]\n"
        "v_add_f32 v191, v191, v192\n"
        "v_cvt_pk_bf16_f32 v189, 0, v189\n"
        "v_mfma_f32_16x16x32_bf16 a[72:75], v[40:43], v[16:19], a[72:75]\n"
        "v_cvt_pk_bf16_f32 v191, 0, v191\n"
        "v_mul_f32 v190, v180, v186\n"
        "v_mfma_f32_16x16x32_bf16 a[72:75], v[72:75], v[20:23], a[72:75]\n"
        "v_mul_f32 v192, v181, v186\n"
        "v_cvt_pk_bf16_f32 v190, 0, v190\n"
        "v_mfma_f32_16x16x32_bf16 a[104:107], v[72:75], v[28:31], a[104:107]\n"
        "v_cvt_pk_bf16_f32 v192, 0, v192\n"
        "v_add_f32 v189, v189, v190\n"
        "v_mfma_f32_16x16x32_bf16 a[104:107], v[40:43], v[24:27], a[104:107]\n"
        "v_add_f32 v191, v191, v192\n"
        "v_cvt_pk_bf16_f32 v189, 0, v189\n"
        "v_mfma_f32_16x16x32_bf16 a[108:111], v[44:47], v[24:27], a[108:111]\n"
        "v_cvt_pk_bf16_f32 v191, 0, v191\n"
        "v_mul_f32 v190, v181, v185\n"
        "v_mfma_f32_16x16x32_bf16 a[108:111], v[76:79], v[28:31], a[108:111]\n"
        "v_mul_f32 v192, v182, v185\n"
        "v_cvt_pk_bf16_f32 v190, 0, v190\n"
        "v_mfma_f32_16x16x32_bf16 a[76:79], v[76:79], v[20:23], a[76:79]\n"
        "v_cvt_pk_bf16_f32 v192, 0, v192\n"
        "v_add_f32 v189, v189, v190\n"
        "v_mfma_f32_16x16x32_bf16 a[76:79], v[44:47], v[16:19], a[76:79]\n"
        "v_add_f32 v191, v191, v192\n"
        "v_cvt_pk_bf16_f32 v189, 0, v189\n"
        "v_mfma_f32_16x16x32_bf16 a[80:83], v[16:19], v[48:51], a[80:83]\n"
        "v_cvt_pk_bf16_f32 v191, 0, v191\n"
        "v_mul_f32 v194, v181, v187\n"
        "v_mfma_f32_16x16x32_bf16 a[80:83], v[20:23], v[80:83], a[80:83]\n"
        "v_cvt_pk_bf16_f32 v194, 0, v194\n"
        "v_and_b32 v178, s35, v104\n"
        "v_mfma_f32_16x16x32_bf16 a[112:115], v[28:31], v[80:83], a[112:115]\n"
        "v_and_b32 v179, s35, v106\n"
        "v_mfma_f32_16x16x32_bf16 a[112:115], v[24:27], v[48:51], a[112:115]\n"
        "v_mfma_f32_16x16x32_bf16 a[116:119], v[24:27], v[52:55], a[116:119]\n"
        "s_waitcnt vmcnt(22) lgkmcnt(0)\n"
        "v_lshlrev_b32 v196, 16, v124\n"
        "v_mul_f32 v195, v195, v196\n"
        "v_mfma_f32_16x16x32_bf16 a[116:119], v[28:31], v[84:87], a[116:119]\n"
        "v_cvt_pk_bf16_f32 v202, v195, v193\n"
        "v_mul_f32 v193, v180, v188\n"
        "v_mfma_f32_16x16x32_bf16 a[84:87], v[20:23], v[84:87], a[84:87]\n"
        "v_mul_f32 v195, v181, v188\n"
        "v_cvt_pk_bf16_f32 v193, 0, v193\n"
        "v_mfma_f32_16x16x32_bf16 a[84:87], v[16:19], v[52:55], a[84:87]\n"
        "v_cvt_pk_bf16_f32 v195, 0, v195\n"
        "v_mul_f32 v196, v182, v187\n"
        "v_mfma_f32_16x16x32_bf16 a[88:91], v[16:19], v[56:59], a[88:91]\n"
        "v_cvt_pk_bf16_f32 v196, 0, v196\n"
        "v_add_f32 v193, v193, v194\n"
        "v_mfma_f32_16x16x32_bf16 a[88:91], v[20:23], v[88:91], a[88:91]\n"
        "v_add_f32 v195, v195, v196\n"
        "v_cvt_pk_bf16_f32 v193, 0, v193\n"
        "v_mfma_f32_16x16x32_bf16 a[120:123], v[28:31], v[88:91], a[120:123]\n"
        "v_cvt_pk_bf16_f32 v195, 0, v195\n"
        "v_mul_f32 v194, v182, v186\n"
        "v_mfma_f32_16x16x32_bf16 a[120:123], v[24:27], v[56:59], a[120:123]\n"
        "v_mul_f32 v196, v183, v186\n"
        "v_cvt_pk_bf16_f32 v194, 0, v194\n"
        "v_mfma_f32_16x16x32_bf16 a[124:127], v[24:27], v[60:63], a[124:127]\n"
        "ds_read_b128 v[24:27], v224 offset:38912\n"
        "v_cvt_pk_bf16_f32 v196, 0, v196\n"
        "v_mfma_f32_16x16x32_bf16 a[124:127], v[28:31], v[92:95], a[124:127]\n"
        "ds_read_b128 v[28:31], v225 offset:38912\n"
        "v_add_f32 v193, v193, v194\n"
        "v_mfma_f32_16x16x32_bf16 a[92:95], v[20:23], v[92:95], a[92:95]\n"
        "ds_read_b128 v[20:23], v225 offset:36864\n"
        "v_add_f32 v195, v195, v196\n"
        "v_mfma_f32_16x16x32_bf16 a[92:95], v[16:19], v[60:63], a[92:95]\n"
        "ds_read_b128 v[16:19], v224 offset:36864\n"
        "v_cvt_pk_bf16_f32 v193, 0, v193\n"
        "v_mfma_f32_16x16x32_bf16 a[128:131], v[32:35], v[0:3], a[128:131]\n"
        "v_cvt_pk_bf16_f32 v195, 0, v195\n"
        "v_mul_f32 v194, v183, v185\n"
        "v_mfma_f32_16x16x32_bf16 a[128:131], v[64:67], v[4:7], a[128:131]\n"
        "v_mul_f32 v196, v184, v185\n"
        "v_cvt_pk_bf16_f32 v194, 0, v194\n"
        "v_mfma_f32_16x16x32_bf16 a[160:163], v[64:67], v[12:15], a[160:163]\n"
        "v_cvt_pk_bf16_f32 v196, 0, v196\n"
        "v_add_f32 v193, v193, v194\n"
        "v_mfma_f32_16x16x32_bf16 a[160:163], v[32:35], v[8:11], a[160:163]\n"
        "v_add_f32 v195, v195, v196\n"
        "v_cvt_pk_bf16_f32 v193, 0, v193\n"
        "v_mfma_f32_16x16x32_bf16 a[164:167], v[36:39], v[8:11], a[164:167]\n"
        "s_waitcnt vmcnt(21) lgkmcnt(3)\n"
        "v_lshlrev_b32 v190, 16, v126\n"
        "v_mul_f32 v189, v189, v190\n"
        "v_mfma_f32_16x16x32_bf16 a[164:167], v[68:71], v[12:15], a[164:167]\n"
        "v_cvt_pk_bf16_f32 v195, 0, v195\n"
        "v_lshlrev_b32 v185, 16, v136\n"
        "v_mfma_f32_16x16x32_bf16 a[132:135], v[68:71], v[4:7], a[132:135]\n"
        "v_and_b32 v186, s35, v136\n"
        "v_lshlrev_b32 v187, 16, v137\n"
        "v_mfma_f32_16x16x32_bf16 a[132:135], v[36:39], v[0:3], a[132:135]\n"
        "global_load_dword v253, v173, s[82:83] offset:0\n"
        "s_add_u32 s82, s12, 0x300000\n"
        "s_addc_u32 s83, s13, 0\n"
        "v_mfma_f32_16x16x32_bf16 a[136:139], v[40:43], v[0:3], a[136:139]\n"
        "global_load_dwordx4 v[146:149], v254, s[92:93] offset:1024\n"
        "v_and_b32 v188, s35, v137\n"
        "v_mfma_f32_16x16x32_bf16 a[136:139], v[72:75], v[4:7], a[136:139]\n"
        "v_and_b32 v180, s35, v108\n"
        "v_and_b32 v181, s35, v110\n"
        "v_mfma_f32_16x16x32_bf16 a[168:171], v[72:75], v[12:15], a[168:171]\n"
        "v_and_b32 v182, s35, v112\n"
        "v_and_b32 v183, s35, v114\n"
        "v_mfma_f32_16x16x32_bf16 a[168:171], v[40:43], v[8:11], a[168:171]\n"
        "v_and_b32 v184, s35, v116\n"
        "v_mul_f32 v190, v175, v187\n"
        "v_mfma_f32_16x16x32_bf16 a[172:175], v[44:47], v[8:11], a[172:175]\n"
        "v_cvt_pk_bf16_f32 v190, 0, v190\n"
        "v_mfma_f32_16x16x32_bf16 a[172:175], v[76:79], v[12:15], a[172:175]\n"
        "v_mfma_f32_16x16x32_bf16 a[140:143], v[76:79], v[4:7], a[140:143]\n"
        "v_mfma_f32_16x16x32_bf16 a[140:143], v[44:47], v[0:3], a[140:143]\n"
        "v_mfma_f32_16x16x32_bf16 a[144:147], v[0:3], v[48:51], a[144:147]\n"
        "v_mfma_f32_16x16x32_bf16 a[144:147], v[4:7], v[80:83], a[144:147]\n"
        "v_mfma_f32_16x16x32_bf16 a[176:179], v[12:15], v[80:83], a[176:179]\n"
        "v_mfma_f32_16x16x32_bf16 a[176:179], v[8:11], v[48:51], a[176:179]\n"
        "v_mfma_f32_16x16x32_bf16 a[180:183], v[8:11], v[52:55], a[180:183]\n"
        "s_waitcnt vmcnt(22) lgkmcnt(0)\n"
        "v_lshlrev_b32 v192, 16, v128\n"
        "v_mul_f32 v191, v191, v192\n"
        "v_mfma_f32_16x16x32_bf16 a[180:183], v[12:15], v[84:87], a[180:183]\n"
        "v_cvt_pk_bf16_f32 v201, v191, v189\n"
        "v_mul_f32 v189, v174, v188\n"
        "v_mfma_f32_16x16x32_bf16 a[148:151], v[4:7], v[84:87], a[148:151]\n"
        "v_mul_f32 v191, v175, v188\n"
        "v_cvt_pk_bf16_f32 v189, 0, v189\n"
        "v_mfma_f32_16x16x32_bf16 a[148:151], v[0:3], v[52:55], a[148:151]\n"
        "v_cvt_pk_bf16_f32 v191, 0, v191\n"
        "v_mul_f32 v192, v176, v187\n"
        "v_mfma_f32_16x16x32_bf16 a[152:155], v[0:3], v[56:59], a[152:155]\n"
        "v_cvt_pk_bf16_f32 v192, 0, v192\n"
        "v_add_f32 v189, v189, v190\n"
        "v_mfma_f32_16x16x32_bf16 a[152:155], v[4:7], v[88:91], a[152:155]\n"
        "v_add_f32 v191, v191, v192\n"
        "v_cvt_pk_bf16_f32 v189, 0, v189\n"
        "v_mfma_f32_16x16x32_bf16 a[184:187], v[12:15], v[88:91], a[184:187]\n"
        "v_cvt_pk_bf16_f32 v191, 0, v191\n"
        "v_mul_f32 v190, v176, v186\n"
        "v_mfma_f32_16x16x32_bf16 a[184:187], v[8:11], v[56:59], a[184:187]\n"
        "s_waitcnt vmcnt(12)\n"
        "v_mul_f32 v192, v177, v186\n"
        "v_cvt_pk_bf16_f32 v190, 0, v190\n"
        "v_mfma_f32_16x16x32_bf16 a[188:191], v[8:11], v[60:63], a[188:191]\n"
        "s_barrier\n"
        "v_mfma_f32_16x16x32_bf16 a[188:191], v[12:15], v[92:95], a[188:191]\n"
        "global_load_lds_dwordx4 v234, s[86:87] offset:0\n"
        "ds_read_b128 v[8:11], v224 offset:18432\n"
        "v_mfma_f32_16x16x32_bf16 a[156:159], v[4:7], v[92:95], a[156:159]\n"
        "ds_read_b128 v[4:7], v225 offset:16384\n"
        "v_cvt_pk_bf16_f32 v192, 0, v192\n"
        "v_mfma_f32_16x16x32_bf16 a[156:159], v[0:3], v[60:63], a[156:159]\n"
        "ds_read_b128 v[0:3], v224 offset:16384\n"
        "v_add_f32 v189, v189, v190\n"
        "v_mfma_f32_16x16x32_bf16 a[192:195], v[32:35], v[16:19], a[192:195]\n"
        "ds_read_b128 v[12:15], v225 offset:18432\n"
        "v_add_f32 v191, v191, v192\n"
        "v_mfma_f32_16x16x32_bf16 a[192:195], v[64:67], v[20:23], a[192:195]\n"
        "global_load_lds_dwordx4 v235, s[86:87] offset:1024\n"
        "v_cvt_pk_bf16_f32 v189, 0, v189\n"
        "v_mfma_f32_16x16x32_bf16 a[224:227], v[64:67], v[28:31], a[224:227]\n"
        "ds_read_b64_tr_b16 v[64:65], v226 offset:40960\n"
        "v_cvt_pk_bf16_f32 v191, 0, v191\n"
        "v_mfma_f32_16x16x32_bf16 a[224:227], v[32:35], v[24:27], a[224:227]\n"
        "ds_read_b64_tr_b16 v[32:33], v226 offset:32768\n"
        "v_mul_f32 v190, v177, v185\n"
        "v_mfma_f32_16x16x32_bf16 a[228:231], v[36:39], v[24:27], a[228:231]\n"
        "ds_read_b64_tr_b16 v[34:35], v227 offset:32768\n"
        "v_mul_f32 v192, v178, v185\n"
        "v_mfma_f32_16x16x32_bf16 a[228:231], v[68:71], v[28:31], a[228:231]\n"
        "ds_read_b64_tr_b16 v[66:67], v227 offset:40960\n"
        "global_load_lds_dwordx4 v236, s[86:87] offset:2048\n"
        "v_mfma_f32_16x16x32_bf16 a[196:199], v[68:71], v[20:23], a[196:199]\n"
        "ds_read_b64_tr_b16 v[68:69], v228 offset:40960\n"
        "v_cvt_pk_bf16_f32 v190, 0, v190\n"
        "v_mfma_f32_16x16x32_bf16 a[196:199], v[36:39], v[16:19], a[196:199]\n"
        "ds_read_b64_tr_b16 v[36:37], v228 offset:32768\n"
        "v_cvt_pk_bf16_f32 v192, 0, v192\n"
        "v_mfma_f32_16x16x32_bf16 a[200:203], v[40:43], v[16:19], a[200:203]\n"
        "ds_read_b64_tr_b16 v[38:39], v229 offset:32768\n"
        "v_add_f32 v189, v189, v190\n"
        "v_mfma_f32_16x16x32_bf16 a[200:203], v[72:75], v[20:23], a[200:203]\n"
        "ds_read_b64_tr_b16 v[70:71], v229 offset:40960\n"
        "global_load_lds_dwordx4 v237, s[86:87] offset:3072\n"
        "s_add_u32 m0, s72, 0x9000\n"
        "v_mfma_f32_16x16x32_bf16 a[232:235], v[72:75], v[28:31], a[232:235]\n"
        "ds_read_b64_tr_b16 v[72:73], v230 offset:40960\n"
        "s_add_u32 s86, s58, 0x280\n"
        "s_addc_u32 s87, s59, 0\n"
        "v_mfma_f32_16x16x32_bf16 a[232:235], v[40:43], v[24:27], a[232:235]\n"
        "ds_read_b64_tr_b16 v[40:41], v230 offset:32768\n"
        "v_lshlrev_b32 v194, 16, v130\n"
        "v_mfma_f32_16x16x32_bf16 a[236:239], v[44:47], v[24:27], a[236:239]\n"
        "ds_read_b64_tr_b16 v[42:43], v231 offset:32768\n"
        "v_mul_f32 v193, v193, v194\n"
        "v_mfma_f32_16x16x32_bf16 a[236:239], v[76:79], v[28:31], a[236:239]\n"
        "ds_read_b64_tr_b16 v[74:75], v231 offset:40960\n"
        "global_load_lds_dwordx4 v234, s[86:87] offset:0\n"
        "v_mfma_f32_16x16x32_bf16 a[204:207], v[76:79], v[20:23], a[204:207]\n"
        "ds_read_b64_tr_b16 v[76:77], v232 offset:40960\n"
        "v_add_f32 v191, v191, v192\n"
        "v_mfma_f32_16x16x32_bf16 a[204:207], v[44:47], v[16:19], a[204:207]\n"
        "ds_read_b64_tr_b16 v[44:45], v232 offset:32768\n"
        "v_cvt_pk_bf16_f32 v189, 0, v189\n"
        "v_mfma_f32_16x16x32_bf16 a[208:211], v[16:19], v[48:51], a[208:211]\n"
        "ds_read_b64_tr_b16 v[46:47], v233 offset:32768\n"
        "v_cvt_pk_bf16_f32 v191, 0, v191\n"
        "v_mfma_f32_16x16x32_bf16 a[208:211], v[20:23], v[80:83], a[208:211]\n"
        "ds_read_b64_tr_b16 v[78:79], v233 offset:40960\n"
        "global_load_lds_dwordx4 v235, s[86:87] offset:1024\n"
        "v_mfma_f32_16x16x32_bf16 a[240:243], v[28:31], v[80:83], a[240:243]\n"
        "ds_read_b64_tr_b16 v[80:81], v226 offset:57344\n"
        "v_and_b32 v190, s35, v118\n"
        "v_mfma_f32_16x16x32_bf16 a[240:243], v[24:27], v[48:51], a[240:243]\n"
        "ds_read_b64_tr_b16 v[48:49], v226 offset:49152\n"
        "v_and_b32 v192, s35, v120\n"
        "v_mfma_f32_16x16x32_bf16 a[244:247], v[24:27], v[52:55], a[244:247]\n"
        "ds_read_b64_tr_b16 v[50:51], v227 offset:49152\n"
        "v_mul_f32 v189, v189, v190\n"
        "v_mfma_f32_16x16x32_bf16 a[244:247], v[28:31], v[84:87], a[244:247]\n"
        "ds_read_b64_tr_b16 v[82:83], v227 offset:57344\n"
        "global_load_lds_dwordx4 v236, s[86:87] offset:2048\n"
        "v_mfma_f32_16x16x32_bf16 a[212:215], v[20:23], v[84:87], a[212:215]\n"
        "ds_read_b64_tr_b16 v[84:85], v228 offset:57344\n"
        "v_mul_f32 v191, v191, v192\n"
        "v_mfma_f32_16x16x32_bf16 a[212:215], v[16:19], v[52:55], a[212:215]\n"
        "ds_read_b64_tr_b16 v[52:53], v228 offset:49152\n"
        "v_cvt_pk_bf16_f32 v211, v191, v189\n"
        "v_mfma_f32_16x16x32_bf16 a[216:219], v[16:19], v[56:59], a[216:219]\n"
        "ds_read_b64_tr_b16 v[54:55], v229 offset:49152\n"
        "v_mul_f32 v194, v177, v187\n"
        "v_mfma_f32_16x16x32_bf16 a[216:219], v[20:23], v[88:91], a[216:219]\n"
        "ds_read_b64_tr_b16 v[86:87], v229 offset:57344\n"
        "global_load_lds_dwordx4 v237, s[86:87] offset:3072\n"
        "s_add_u32 m0, s73, 0x10000\n"
        "v_mfma_f32_16x16x32_bf16 a[248:251], v[28:31], v[88:91], a[248:251]\n"
        "ds_read_b64_tr_b16 v[88:89], v230 offset:57344\n"
        "s_add_u32 s86, s60, 0x300000\n"
        "s_addc_u32 s87, s61, 0\n"
        "v_mfma_f32_16x16x32_bf16 a[248:251], v[24:27], v[56:59], a[248:251]\n"
        "ds_read_b64_tr_b16 v[56:57], v230 offset:49152\n"
        "v_cvt_pk_bf16_f32 v194, 0, v194\n"
        "v_mfma_f32_16x16x32_bf16 a[252:255], v[24:27], v[60:63], a[252:255]\n"
        "ds_read_b64_tr_b16 v[58:59], v231 offset:49152\n"
        "v_mul_f32 v189, v178, v188\n"
        "v_mfma_f32_16x16x32_bf16 a[252:255], v[28:31], v[92:95], a[252:255]\n"
        "ds_read_b64_tr_b16 v[90:91], v231 offset:57344\n"
        "global_load_dwordx4 v[150:153], v254, s[92:93] offset:2048\n"
        "v_mfma_f32_16x16x32_bf16 a[220:223], v[20:23], v[92:95], a[220:223]\n"
        "ds_read_b64_tr_b16 v[92:93], v232 offset:57344\n"
        "v_mul_f32 v191, v179, v188\n"
        "v_mfma_f32_16x16x32_bf16 a[220:223], v[16:19], v[60:63], a[220:223]\n"
        "ds_read_b64_tr_b16 v[60:61], v232 offset:49152\n"
        "s_waitcnt lgkmcnt(8)\n"
        "v_mfma_f32_16x16x32_bf16 a[0:3], v[32:35], v[0:3], a[0:3]\n"
        "ds_read_b64_tr_b16 v[62:63], v233 offset:49152\n"
        "v_cvt_pk_bf16_f32 v189, 0, v189\n"
        "v_mfma_f32_16x16x32_bf16 a[0:3], v[64:67], v[4:7], a[0:3]\n"
        "ds_read_b64_tr_b16 v[94:95], v233 offset:57344\n"
        "global_load_lds_dwordx4 v238, s[86:87] offset:0\n"
        "v_mfma_f32_16x16x32_bf16 a[32:35], v[64:67], v[12:15], a[32:35]\n"
        "ds_read_b128 v[16:19], v224 offset:20480\n"
        "v_cvt_pk_bf16_f32 v191, 0, v191\n"
        "v_mfma_f32_16x16x32_bf16 a[32:35], v[32:35], v[8:11], a[32:35]\n"
        "ds_read_b128 v[20:23], v225 offset:20480\n"
        "v_mul_f32 v190, v179, v187\n"
        "v_mfma_f32_16x16x32_bf16 a[36:39], v[36:39], v[8:11], a[36:39]\n"
        "ds_read_b128 v[24:27], v224 offset:22528\n"
        "v_mul_f32 v192, v180, v187\n"
        "v_mfma_f32_16x16x32_bf16 a[36:39], v[68:71], v[12:15], a[36:39]\n"
        "ds_read_b128 v[28:31], v225 offset:22528\n"
        "global_load_lds_dwordx4 v239, s[86:87] offset:1024\n"
        "v_mfma_f32_16x16x32_bf16 a[4:7], v[68:71], v[4:7], a[4:7]\n"
        "s_waitcnt vmcnt(22) lgkmcnt(7)\n"
        "v_lshlrev_b32 v196, 16, v132\n"
        "v_mul_f32 v195, v195, v196\n"
        "v_mfma_f32_16x16x32_bf16 a[4:7], v[36:39], v[0:3], a[4:7]\n"
        "v_cvt_pk_bf16_f32 v200, v195, v193\n"
        "v_mul_f32 v193, v176, v188\n"
        "v_mfma_f32_16x16x32_bf16 a[8:11], v[40:43], v[0:3], a[8:11]\n"
        "v_mul_f32 v195, v177, v188\n"
        "v_cvt_pk_bf16_f32 v193, 0, v193\n"
        "v_mfma_f32_16x16x32_bf16 a[8:11], v[72:75], v[4:7], a[8:11]\n"
        "global_load_lds_dwordx4 v240, s[86:87] offset:2048\n"
        "v_mfma_f32_16x16x32_bf16 a[40:43], v[72:75], v[12:15], a[40:43]\n"
        "v_cvt_pk_bf16_f32 v195, 0, v195\n"
        "v_mul_f32 v196, v178, v187\n"
        "v_mfma_f32_16x16x32_bf16 a[40:43], v[40:43], v[8:11], a[40:43]\n"
        "v_cvt_pk_bf16_f32 v196, 0, v196\n"
        "v_add_f32 v193, v193, v194\n"
        "v_mfma_f32_16x16x32_bf16 a[44:47], v[44:47], v[8:11], a[44:47]\n"
        "v_add_f32 v195, v195, v196\n"
        "v_cvt_pk_bf16_f32 v193, 0, v193\n"
        "v_mfma_f32_16x16x32_bf16 a[44:47], v[76:79], v[12:15], a[44:47]\n"
        "global_load_lds_dwordx4 v241, s[86:87] offset:3072\n"
        "s_add_u32 m0, s73, 0x14000\n"
        "v_mfma_f32_16x16x32_bf16 a[12:15], v[76:79], v[4:7], a[12:15]\n"
        "s_add_u32 s86, s62, 0x300000\n"
        "s_addc_u32 s87, s63, 0\n"
        "v_cvt_pk_bf16_f32 v195, 0, v195\n"
        "v_mfma_f32_16x16x32_bf16 a[12:15], v[44:47], v[0:3], a[12:15]\n"
        "v_mul_f32 v194, v178, v186\n"
        "v_mul_f32 v196, v179, v186\n"
        "v_mfma_f32_16x16x32_bf16 a[16:19], v[0:3], v[48:51], a[16:19]\n"
        "v_cvt_pk_bf16_f32 v194, 0, v194\n"
        "v_cvt_pk_bf16_f32 v196, 0, v196\n"
        "v_mfma_f32_16x16x32_bf16 a[16:19], v[4:7], v[80:83], a[16:19]\n"
        "global_load_lds_dwordx4 v238, s[86:87] offset:0\n"
        "v_mfma_f32_16x16x32_bf16 a[48:51], v[12:15], v[80:83], a[48:51]\n"
        "v_add_f32 v193, v193, v194\n"
        "v_add_f32 v195, v195, v196\n"
        "v_mfma_f32_16x16x32_bf16 a[48:51], v[8:11], v[48:51], a[48:51]\n"
        "v_cvt_pk_bf16_f32 v193, 0, v193\n"
        "v_cvt_pk_bf16_f32 v195, 0, v195\n"
        "v_mfma_f32_16x16x32_bf16 a[52:55], v[8:11], v[52:55], a[52:55]\n"
        "v_mul_f32 v194, v179, v185\n"
        "v_mul_f32 v196, v180, v185\n"
        "v_mfma_f32_16x16x32_bf16 a[52:55], v[12:15], v[84:87], a[52:55]\n"
        "global_load_lds_dwordx4 v239, s[86:87] offset:1024\n"
        "v_mfma_f32_16x16x32_bf16 a[20:23], v[4:7], v[84:87], a[20:23]\n"
        "v_cvt_pk_bf16_f32 v194, 0, v194\n"
        "v_cvt_pk_bf16_f32 v196, 0, v196\n"
        "v_mfma_f32_16x16x32_bf16 a[20:23], v[0:3], v[52:55], a[20:23]\n"
        "v_add_f32 v193, v193, v194\n"
        "v_add_f32 v195, v195, v196\n"
        "v_mfma_f32_16x16x32_bf16 a[24:27], v[0:3], v[56:59], a[24:27]\n"
        "s_waitcnt lgkmcnt(0)\n"
        "v_cvt_pk_bf16_f32 v193, 0, v193\n"
        "v_cvt_pk_bf16_f32 v195, 0, v195\n"
        "v_mfma_f32_16x16x32_bf16 a[24:27], v[4:7], v[88:91], a[24:27]\n"
        "global_load_dword v253, v173, s[82:83] offset:0\n"
        "v_mfma_f32_16x16x32_bf16 a[56:59], v[12:15], v[88:91], a[56:59]\n"
        "global_load_lds_dwordx4 v240, s[86:87] offset:2048\n"
        "s_add_u32 s82, s12, 0x400000\n"
        "s_addc_u32 s83, s13, 0\n"
        "v_mfma_f32_16x16x32_bf16 a[56:59], v[8:11], v[56:59], a[56:59]\n"
        "v_and_b32 v194, s35, v122\n"
        "v_and_b32 v196, s35, v124\n"
        "v_mfma_f32_16x16x32_bf16 a[60:63], v[8:11], v[60:63], a[60:63]\n"
        "ds_read_b128 v[8:11], v224 offset:2048\n"
        "v_mul_f32 v193, v193, v194\n"
        "v_mfma_f32_16x16x32_bf16 a[60:63], v[12:15], v[92:95], a[60:63]\n"
        "ds_read_b128 v[12:15], v225 offset:2048\n"
        "v_mul_f32 v195, v195, v196\n"
        "v_mfma_f32_16x16x32_bf16 a[28:31], v[4:7], v[92:95], a[28:31]\n"
        "global_load_lds_dwordx4 v241, s[86:87] offset:3072\n"
        "ds_read_b128 v[4:7], v225 offset:0\n"
        "s_add_u32 m0, s72, 0x0\n"
        "v_mfma_f32_16x16x32_bf16 a[28:31], v[0:3], v[60:63], a[28:31]\n"
        "ds_read_b128 v[0:3], v224 offset:0\n"
        "s_add_u32 s86, s56, 0x300\n"
        "s_addc_u32 s87, s57, 0\n"
        "v_mfma_f32_16x16x32_bf16 a[64:67], v[32:35], v[16:19], a[64:67]\n"
        "v_cvt_pk_bf16_f32 v210, v195, v193\n"
        "v_cvt_pk_bf16_f32 v190, 0, v190\n"
        "v_mfma_f32_16x16x32_bf16 a[64:67], v[64:67], v[20:23], a[64:67]\n"
        "v_cvt_pk_bf16_f32 v192, 0, v192\n"
        "v_add_f32 v189, v189, v190\n"
        "v_mfma_f32_16x16x32_bf16 a[96:99], v[64:67], v[28:31], a[96:99]\n"
        "global_store_dwordx4 v171, v[200:203], s[76:77] offset:-4096 sc1\n"
        "v_add_f32 v191, v191, v192\n"
        "v_cvt_pk_bf16_f32 v189, 0, v189\n"
        "v_cvt_pk_bf16_f32 v191, 0, v191\n"
        "v_mul_f32 v190, v180, v186\n"
        "v_mfma_f32_16x16x32_bf16 a[96:99], v[32:35], v[24:27], a[96:99]\n"
        "v_mul_f32 v192, v181, v186\n"
        "v_cvt_pk_bf16_f32 v190, 0, v190\n"
        "v_mfma_f32_16x16x32_bf16 a[100:103], v[36:39], v[24:27], a[100:103]\n"
        "v_cvt_pk_bf16_f32 v192, 0, v192\n"
        "v_add_f32 v189, v189, v190\n"
        "v_mfma_f32_16x16x32_bf16 a[100:103], v[68:71], v[28:31], a[100:103]\n"
        "v_add_f32 v191, v191, v192\n"
        "v_cvt_pk_bf16_f32 v189, 0, v189\n"
        "v_mfma_f32_16x16x32_bf16 a[68:71], v[68:71], v[20:23], a[68:71]\n"
        "v_cvt_pk_bf16_f32 v191, 0, v191\n"
        "v_mul_f32 v190, v181, v185\n"
        "v_mfma_f32_16x16x32_bf16 a[68:71], v[36:39], v[16:19], a[68:71]\n"
        "global_load_dwordx4 v[154:157], v254, s[92:93] offset:3072\n"
        "v_mul_f32 v192, v182, v185\n"
        "v_mfma_f32_16x16x32_bf16 a[72:75], v[40:43], v[16:19], a[72:75]\n"
        "v_cvt_pk_bf16_f32 v190, 0, v190\n"
        "v_cvt_pk_bf16_f32 v192, 0, v192\n"
        "v_mfma_f32_16x16x32_bf16 a[72:75], v[72:75], v[20:23], a[72:75]\n"
        "v_add_f32 v189, v189, v190\n"
        "v_add_f32 v191, v191, v192\n"
        "v_mfma_f32_16x16x32_bf16 a[104:107], v[72:75], v[28:31], a[104:107]\n"
        "v_cvt_pk_bf16_f32 v189, 0, v189\n"
        "v_cvt_pk_bf16_f32 v191, 0, v191\n"
        "v_mfma_f32_16x16x32_bf16 a[104:107], v[40:43], v[24:27], a[104:107]\n"
        "v_and_b32 v190, s35, v126\n"
        "v_and_b32 v192, s35, v128\n"
        "v_mfma_f32_16x16x32_bf16 a[108:111], v[44:47], v[24:27], a[108:111]\n"
        "v_mul_f32 v189, v189, v190\n"
        "v_mul_f32 v191, v191, v192\n"
        "v_mfma_f32_16x16x32_bf16 a[108:111], v[76:79], v[28:31], a[108:111]\n"
        "v_cvt_pk_bf16_f32 v209, v191, v189\n"
        "v_mul_f32 v193, v180, v188\n"
        "v_mfma_f32_16x16x32_bf16 a[76:79], v[76:79], v[20:23], a[76:79]\n"
        "v_mul_f32 v195, v181, v188\n"
        "v_cvt_pk_bf16_f32 v193, 0, v193\n"
        "v_mfma_f32_16x16x32_bf16 a[76:79], v[44:47], v[16:19], a[76:79]\n"
        "v_cvt_pk_bf16_f32 v195, 0, v195\n"
        "v_mul_f32 v194, v181, v187\n"
        "v_mfma_f32_16x16x32_bf16 a[80:83], v[16:19], v[48:51], a[80:83]\n"
        "v_mul_f32 v196, v182, v187\n"
        "v_cvt_pk_bf16_f32 v194, 0, v194\n"
        "v_mfma_f32_16x16x32_bf16 a[80:83], v[20:23], v[80:83], a[80:83]\n"
        "v_cvt_pk_bf16_f32 v196, 0, v196\n"
        "v_add_f32 v193, v193, v194\n"
        "v_mfma_f32_16x16x32_bf16 a[112:115], v[28:31], v[80:83], a[112:115]\n"
        "v_add_f32 v195, v195, v196\n"
        "v_cvt_pk_bf16_f32 v193, 0, v193\n"
        "v_mfma_f32_16x16x32_bf16 a[112:115], v[24:27], v[48:51], a[112:115]\n"
        "v_cvt_pk_bf16_f32 v195, 0, v195\n"
        "v_mul_f32 v194, v182, v186\n"
        "v_mfma_f32_16x16x32_bf16 a[116:119], v[24:27], v[52:55], a[116:119]\n"
        "v_mul_f32 v196, v183, v186\n"
        "v_cvt_pk_bf16_f32 v194, 0, v194\n"
        "v_mfma_f32_16x16x32_bf16 a[116:119], v[28:31], v[84:87], a[116:119]\n"
        "v_cvt_pk_bf16_f32 v196, 0, v196\n"
        "v_add_f32 v193, v193, v194\n"
        "v_mfma_f32_16x16x32_bf16 a[84:87], v[20:23], v[84:87], a[84:87]\n"
        "v_add_f32 v195, v195, v196\n"
        "v_cvt_pk_bf16_f32 v193, 0, v193\n"
        "v_mfma_f32_16x16x32_bf16 a[84:87], v[16:19], v[52:55], a[84:87]\n"
        "v_cvt_pk_bf16_f32 v195, 0, v195\n"
        "v_mul_f32 v194, v183, v185\n"
        "v_mfma_f32_16x16x32_bf16 a[88:91], v[16:19], v[56:59], a[88:91]\n"
        "v_mul_f32 v196, v184, v185\n"
        "v_cvt_pk_bf16_f32 v194, 0, v194\n"
        "v_mfma_f32_16x16x32_bf16 a[88:91], v[20:23], v[88:91], a[88:91]\n"
        "v_cvt_pk_bf16_f32 v196, 0, v196\n"
        "v_add_f32 v193, v193, v194\n"
        "v_mfma_f32_16x16x32_bf16 a[120:123], v[28:31], v[88:91], a[120:123]\n"
        "v_add_f32 v195, v195, v196\n"
        "v_cvt_pk_bf16_f32 v193, 0, v193\n"
        "v_mfma_f32_16x16x32_bf16 a[120:123], v[24:27], v[56:59], a[120:123]\n"
        "v_cvt_pk_bf16_f32 v195, 0, v195\n"
        "v_and_b32 v194, s35, v130\n"
        "v_mfma_f32_16x16x32_bf16 a[124:127], v[24:27], v[60:63], a[124:127]\n"
        "ds_read_b128 v[24:27], v224 offset:6144\n"
        "v_and_b32 v196, s35, v132\n"
        "v_mfma_f32_16x16x32_bf16 a[124:127], v[28:31], v[92:95], a[124:127]\n"
        "ds_read_b128 v[28:31], v225 offset:6144\n"
        "v_mul_f32 v193, v193, v194\n"
        "v_mfma_f32_16x16x32_bf16 a[92:95], v[20:23], v[92:95], a[92:95]\n"
        "ds_read_b128 v[20:23], v225 offset:4096\n"
        "v_mul_f32 v195, v195, v196\n"
        "v_mfma_f32_16x16x32_bf16 a[92:95], v[16:19], v[60:63], a[92:95]\n"
        "ds_read_b128 v[16:19], v224 offset:4096\n"
        "s_waitcnt lgkmcnt(4)\n"
        "v_mfma_f32_16x16x32_bf16 a[128:131], v[32:35], v[0:3], a[128:131]\n"
        "v_cvt_pk_bf16_f32 v208, v195, v193\n"
        "v_lshlrev_b32 v185, 16, v138\n"
        "v_mfma_f32_16x16x32_bf16 a[128:131], v[64:67], v[4:7], a[128:131]\n"
        "global_store_dwordx4 v171, v[208:211], s[76:77] offset:0 sc1\n"
        "s_add_u32 s76, s76, 0x2000\n"
        "s_addc_u32 s77, s77, 0\n"
        "v_and_b32 v186, s35, v138\n"
        "v_lshlrev_b32 v187, 16, v139\n"
        "v_and_b32 v188, s35, v139\n"
        "v_mfma_f32_16x16x32_bf16 a[160:163], v[64:67], v[12:15], a[160:163]\n"
        "v_lshlrev_b32 v174, 16, v97\n"
        "v_lshlrev_b32 v175, 16, v99\n"
        "v_mfma_f32_16x16x32_bf16 a[160:163], v[32:35], v[8:11], a[160:163]\n"
        "v_lshlrev_b32 v176, 16, v101\n"
        "v_lshlrev_b32 v177, 16, v103\n"
        "v_mfma_f32_16x16x32_bf16 a[164:167], v[36:39], v[8:11], a[164:167]\n"
        "v_lshlrev_b32 v178, 16, v105\n"
        "v_lshlrev_b32 v179, 16, v107\n"
        "v_mfma_f32_16x16x32_bf16 a[164:167], v[68:71], v[12:15], a[164:167]\n"
        "v_lshlrev_b32 v180, 16, v109\n"
        "v_lshlrev_b32 v181, 16, v111\n"
        "v_mfma_f32_16x16x32_bf16 a[132:135], v[68:71], v[4:7], a[132:135]\n"
        "v_lshlrev_b32 v182, 16, v113\n"
        "v_lshlrev_b32 v183, 16, v115\n"
        "v_mfma_f32_16x16x32_bf16 a[132:135], v[36:39], v[0:3], a[132:135]\n"
        "v_lshlrev_b32 v184, 16, v117\n"
        "v_mul_f32 v189, v174, v188\n"
        "v_mfma_f32_16x16x32_bf16 a[136:139], v[40:43], v[0:3], a[136:139]\n"
        "v_mul_f32 v191, v175, v188\n"
        "v_cvt_pk_bf16_f32 v189, 0, v189\n"
        "v_mfma_f32_16x16x32_bf16 a[136:139], v[72:75], v[4:7], a[136:139]\n"
        "v_cvt_pk_bf16_f32 v191, 0, v191\n"
        "v_mul_f32 v190, v175, v187\n"
        "v_mfma_f32_16x16x32_bf16 a[168:171], v[72:75], v[12:15], a[168:171]\n"
        "v_mul_f32 v192, v176, v187\n"
        "v_cvt_pk_bf16_f32 v190, 0, v190\n"
        "v_mfma_f32_16x16x32_bf16 a[168:171], v[40:43], v[8:11], a[168:171]\n"
        "v_cvt_pk_bf16_f32 v192, 0, v192\n"
        "v_add_f32 v189, v189, v190\n"
        "v_mfma_f32_16x16x32_bf16 a[172:175], v[44:47], v[8:11], a[172:175]\n"
        "v_add_f32 v191, v191, v192\n"
        "v_cvt_pk_bf16_f32 v189, 0, v189\n"
        "v_mfma_f32_16x16x32_bf16 a[172:175], v[76:79], v[12:15], a[172:175]\n"
        "v_cvt_pk_bf16_f32 v191, 0, v191\n"
        "v_mul_f32 v190, v176, v186\n"
        "v_mfma_f32_16x16x32_bf16 a[140:143], v[76:79], v[4:7], a[140:143]\n"
        "v_mul_f32 v192, v177, v186\n"
        "v_cvt_pk_bf16_f32 v190, 0, v190\n"
        "v_mfma_f32_16x16x32_bf16 a[140:143], v[44:47], v[0:3], a[140:143]\n"
        "v_cvt_pk_bf16_f32 v192, 0, v192\n"
        "v_add_f32 v189, v189, v190\n"
        "v_mfma_f32_16x16x32_bf16 a[144:147], v[0:3], v[48:51], a[144:147]\n"
        "v_add_f32 v191, v191, v192\n"
        "v_cvt_pk_bf16_f32 v189, 0, v189\n"
        "v_mfma_f32_16x16x32_bf16 a[144:147], v[4:7], v[80:83], a[144:147]\n"
        "v_cvt_pk_bf16_f32 v191, 0, v191\n"
        "v_mul_f32 v190, v177, v185\n"
        "v_mfma_f32_16x16x32_bf16 a[176:179], v[12:15], v[80:83], a[176:179]\n"
        "v_mul_f32 v192, v178, v185\n"
        "v_cvt_pk_bf16_f32 v190, 0, v190\n"
        "v_mfma_f32_16x16x32_bf16 a[176:179], v[8:11], v[48:51], a[176:179]\n"
        "v_cvt_pk_bf16_f32 v192, 0, v192\n"
        "v_add_f32 v189, v189, v190\n"
        "v_mfma_f32_16x16x32_bf16 a[180:183], v[8:11], v[52:55], a[180:183]\n"
        "v_add_f32 v191, v191, v192\n"
        "v_cvt_pk_bf16_f32 v189, 0, v189\n"
        "v_mfma_f32_16x16x32_bf16 a[180:183], v[12:15], v[84:87], a[180:183]\n"
        "v_cvt_pk_bf16_f32 v191, 0, v191\n"
        "v_lshlrev_b32 v190, 16, v119\n"
        "v_mfma_f32_16x16x32_bf16 a[148:151], v[4:7], v[84:87], a[148:151]\n"
        "v_lshlrev_b32 v192, 16, v121\n"
        "v_mul_f32 v189, v189, v190\n"
        "v_mfma_f32_16x16x32_bf16 a[148:151], v[0:3], v[52:55], a[148:151]\n"
        "v_mul_f32 v191, v191, v192\n"
        "v_cvt_pk_bf16_f32 v203, v191, v189\n"
        "v_mfma_f32_16x16x32_bf16 a[152:155], v[0:3], v[56:59], a[152:155]\n"
        "v_mul_f32 v193, v176, v188\n"
        "v_mul_f32 v195, v177, v188\n"
        "v_mfma_f32_16x16x32_bf16 a[152:155], v[4:7], v[88:91], a[152:155]\n"
        "v_cvt_pk_bf16_f32 v193, 0, v193\n"
        "v_cvt_pk_bf16_f32 v195, 0, v195\n"
        "v_mfma_f32_16x16x32_bf16 a[184:187], v[12:15], v[88:91], a[184:187]\n"
        "v_mul_f32 v194, v177, v187\n"
        "v_mul_f32 v196, v178, v187\n"
        "v_mfma_f32_16x16x32_bf16 a[184:187], v[8:11], v[56:59], a[184:187]\n"
        "s_waitcnt vmcnt(13) lgkmcnt(0)\n"
        "v_cvt_pk_bf16_f32 v194, 0, v194\n"
        "v_cvt_pk_bf16_f32 v196, 0, v196\n"
        "v_mfma_f32_16x16x32_bf16 a[188:191], v[8:11], v[60:63], a[188:191]\n"
        "s_barrier\n"
        "v_mfma_f32_16x16x32_bf16 a[188:191], v[12:15], v[92:95], a[188:191]\n"
        "global_load_lds_dwordx4 v234, s[86:87] offset:0\n"
        "ds_read_b128 v[8:11], v224 offset:51200\n"
        "v_mfma_f32_16x16x32_bf16 a[156:159], v[4:7], v[92:95], a[156:159]\n"
        "ds_read_b128 v[4:7], v225 offset:49152\n"
        "v_add_f32 v193, v193, v194\n"
        "v_mfma_f32_16x16x32_bf16 a[156:159], v[0:3], v[60:63], a[156:159]\n"
        "ds_read_b128 v[0:3], v224 offset:49152\n"
        "v_add_f32 v195, v195, v196\n"
        "v_mfma_f32_16x16x32_bf16 a[192:195], v[32:35], v[16:19], a[192:195]\n"
        "ds_read_b128 v[12:15], v225 offset:51200\n"
        "v_cvt_pk_bf16_f32 v193, 0, v193\n"
        "v_mfma_f32_16x16x32_bf16 a[192:195], v[64:67], v[20:23], a[192:195]\n"
        "global_load_lds_dwordx4 v235, s[86:87] offset:1024\n"
        "v_cvt_pk_bf16_f32 v195, 0, v195\n"
        "v_mfma_f32_16x16x32_bf16 a[224:227], v[64:67], v[28:31], a[224:227]\n"
        "ds_read_b64_tr_b16 v[64:65], v216 offset:8192\n"
        "v_mul_f32 v194, v178, v186\n"
        "v_mfma_f32_16x16x32_bf16 a[224:227], v[32:35], v[24:27], a[224:227]\n"
        "ds_read_b64_tr_b16 v[32:33], v216 offset:0\n"
        "v_mul_f32 v196, v179, v186\n"
        "v_mfma_f32_16x16x32_bf16 a[228:231], v[36:39], v[24:27], a[228:231]\n"
        "ds_read_b64_tr_b16 v[34:35], v217 offset:0\n"
        "v_cvt_pk_bf16_f32 v194, 0, v194\n"
        "v_mfma_f32_16x16x32_bf16 a[228:231], v[68:71], v[28:31], a[228:231]\n"
        "ds_read_b64_tr_b16 v[66:67], v217 offset:8192\n"
        "global_load_lds_dwordx4 v236, s[86:87] offset:2048\n"
        "v_mfma_f32_16x16x32_bf16 a[196:199], v[68:71], v[20:23], a[196:199]\n"
        "ds_read_b64_tr_b16 v[68:69], v218 offset:8192\n"
        "v_cvt_pk_bf16_f32 v196, 0, v196\n"
        "v_mfma_f32_16x16x32_bf16 a[196:199], v[36:39], v[16:19], a[196:199]\n"
        "ds_read_b64_tr_b16 v[36:37], v218 offset:0\n"
        "v_add_f32 v193, v193, v194\n"
        "v_mfma_f32_16x16x32_bf16 a[200:203], v[40:43], v[16:19], a[200:203]\n"
        "ds_read_b64_tr_b16 v[38:39], v219 offset:0\n"
        "v_add_f32 v195, v195, v196\n"
        "v_mfma_f32_16x16x32_bf16 a[200:203], v[72:75], v[20:23], a[200:203]\n"
        "ds_read_b64_tr_b16 v[70:71], v219 offset:8192\n"
        "global_load_dword v253, v173, s[82:83] offset:0\n"
        "v_mfma_f32_16x16x32_bf16 a[232:235], v[72:75], v[28:31], a[232:235]\n"
        "ds_read_b64_tr_b16 v[72:73], v220 offset:8192\n"
        "global_load_lds_dwordx4 v237, s[86:87] offset:3072\n"
        "s_add_u32 m0, s72, 0x1000\n"
        "v_mfma_f32_16x16x32_bf16 a[232:235], v[40:43], v[24:27], a[232:235]\n"
        "ds_read_b64_tr_b16 v[40:41], v220 offset:0\n"
        "s_add_u32 s86, s58, 0x300\n"
        "s_addc_u32 s87, s59, 0\n"
        "v_mfma_f32_16x16x32_bf16 a[236:239], v[44:47], v[24:27], a[236:239]\n"
        "ds_read_b64_tr_b16 v[42:43], v221 offset:0\n"
        "s_add_u32 s82, s12, 0x500000\n"
        "s_addc_u32 s83, s13, 0\n"
        "v_mfma_f32_16x16x32_bf16 a[236:239], v[76:79], v[28:31], a[236:239]\n"
        "ds_read_b64_tr_b16 v[74:75], v221 offset:8192\n"
        "LHA2_%=:\n"
        "s_cbranch_execz LHA6_%=\n"
        "v_cvt_pk_bf16_f32 v193, 0, v193\n"
        "v_mfma_f32_16x16x32_bf16 a[204:207], v[76:79], v[20:23], a[204:207]\n"
        "ds_read_b64_tr_b16 v[76:77], v222 offset:8192\n"
        "global_load_lds_dwordx4 v234, s[86:87] offset:0\n"
        "v_mfma_f32_16x16x32_bf16 a[204:207], v[44:47], v[16:19], a[204:207]\n"
        "ds_read_b64_tr_b16 v[44:45], v222 offset:0\n"
        "v_cvt_pk_bf16_f32 v195, 0, v195\n"
        "v_mfma_f32_16x16x32_bf16 a[208:211], v[16:19], v[48:51], a[208:211]\n"
        "ds_read_b64_tr_b16 v[46:47], v223 offset:0\n"
        "v_mul_f32 v194, v179, v185\n"
        "v_mfma_f32_16x16x32_bf16 a[208:211], v[20:23], v[80:83], a[208:211]\n"
        "ds_read_b64_tr_b16 v[78:79], v223 offset:8192\n"
        "v_mul_f32 v196, v180, v185\n"
        "v_mfma_f32_16x16x32_bf16 a[240:243], v[28:31], v[80:83], a[240:243]\n"
        "ds_read_b64_tr_b16 v[80:81], v216 offset:24576\n"
        "global_load_lds_dwordx4 v235, s[86:87] offset:1024\n"
        "v_mfma_f32_16x16x32_bf16 a[240:243], v[24:27], v[48:51], a[240:243]\n"
        "ds_read_b64_tr_b16 v[48:49], v216 offset:16384\n"
        "v_cvt_pk_bf16_f32 v194, 0, v194\n"
        "v_mfma_f32_16x16x32_bf16 a[244:247], v[24:27], v[52:55], a[244:247]\n"
        "ds_read_b64_tr_b16 v[50:51], v217 offset:16384\n"
        "v_cvt_pk_bf16_f32 v196, 0, v196\n"
        "v_mfma_f32_16x16x32_bf16 a[244:247], v[28:31], v[84:87], a[244:247]\n"
        "ds_read_b64_tr_b16 v[82:83], v217 offset:24576\n"
        "v_add_f32 v193, v193, v194\n"
        "v_mfma_f32_16x16x32_bf16 a[212:215], v[20:23], v[84:87], a[212:215]\n"
        "ds_read_b64_tr_b16 v[84:85], v218 offset:24576\n"
        "global_load_lds_dwordx4 v236, s[86:87] offset:2048\n"
        "v_mfma_f32_16x16x32_bf16 a[212:215], v[16:19], v[52:55], a[212:215]\n"
        "ds_read_b64_tr_b16 v[52:53], v218 offset:16384\n"
        "v_add_f32 v195, v195, v196\n"
        "v_mfma_f32_16x16x32_bf16 a[216:219], v[16:19], v[56:59], a[216:219]\n"
        "ds_read_b64_tr_b16 v[54:55], v219 offset:16384\n"
        "v_cvt_pk_bf16_f32 v193, 0, v193\n"
        "v_mfma_f32_16x16x32_bf16 a[216:219], v[20:23], v[88:91], a[216:219]\n"
        "ds_read_b64_tr_b16 v[86:87], v219 offset:24576\n"
        "v_cvt_pk_bf16_f32 v195, 0, v195\n"
        "v_mfma_f32_16x16x32_bf16 a[248:251], v[28:31], v[88:91], a[248:251]\n"
        "ds_read_b64_tr_b16 v[88:89], v220 offset:24576\n"
        "global_load_lds_dwordx4 v237, s[86:87] offset:3072\n"
        "s_add_u32 m0, s73, 0x18000\n"
        "v_mfma_f32_16x16x32_bf16 a[248:251], v[24:27], v[56:59], a[248:251]\n"
        "ds_read_b64_tr_b16 v[56:57], v220 offset:16384\n"
        "s_add_u32 s86, s60, 0x380000\n"
        "s_addc_u32 s87, s61, 0\n"
        "v_mfma_f32_16x16x32_bf16 a[252:255], v[24:27], v[60:63], a[252:255]\n"
        "ds_read_b64_tr_b16 v[58:59], v221 offset:16384\n"
        "v_lshlrev_b32 v194, 16, v123\n"
        "v_mfma_f32_16x16x32_bf16 a[252:255], v[28:31], v[92:95], a[252:255]\n"
        "ds_read_b64_tr_b16 v[90:91], v221 offset:24576\n"
        "v_lshlrev_b32 v196, 16, v125\n"
        "v_mfma_f32_16x16x32_bf16 a[220:223], v[20:23], v[92:95], a[220:223]\n"
        "ds_read_b64_tr_b16 v[92:93], v222 offset:24576\n"
        "global_load_lds_dwordx4 v238, s[86:87] offset:0\n"
        "v_mfma_f32_16x16x32_bf16 a[220:223], v[16:19], v[60:63], a[220:223]\n"
        "ds_read_b64_tr_b16 v[60:61], v222 offset:16384\n"
        "s_waitcnt lgkmcnt(8)\n"
        "v_mfma_f32_16x16x32_bf16 a[0:3], v[32:35], v[0:3], a[0:3]\n"
        "ds_read_b64_tr_b16 v[62:63], v223 offset:16384\n"
        "v_mul_f32 v193, v193, v194\n"
        "v_mfma_f32_16x16x32_bf16 a[0:3], v[64:67], v[4:7], a[0:3]\n"
        "ds_read_b64_tr_b16 v[94:95], v223 offset:24576\n"
        "v_mul_f32 v195, v195, v196\n"
        "v_mfma_f32_16x16x32_bf16 a[32:35], v[64:67], v[12:15], a[32:35]\n"
        "ds_read_b128 v[16:19], v224 offset:53248\n"
        "global_load_lds_dwordx4 v239, s[86:87] offset:1024\n"
        "v_mfma_f32_16x16x32_bf16 a[32:35], v[32:35], v[8:11], a[32:35]\n"
        "ds_read_b128 v[20:23], v225 offset:53248\n"
        "v_cvt_pk_bf16_f32 v202, v195, v193\n"
        "v_mfma_f32_16x16x32_bf16 a[36:39], v[36:39], v[8:11], a[36:39]\n"
        "ds_read_b128 v[24:27], v224 offset:55296\n"
        "v_mul_f32 v189, v178, v188\n"
        "v_mfma_f32_16x16x32_bf16 a[36:39], v[68:71], v[12:15], a[36:39]\n"
        "ds_read_b128 v[28:31], v225 offset:55296\n"
        "v_mul_f32 v191, v179, v188\n"
        "v_mfma_f32_16x16x32_bf16 a[4:7], v[68:71], v[4:7], a[4:7]\n"
        "global_load_lds_dwordx4 v240, s[86:87] offset:2048\n"
        "v_mfma_f32_16x16x32_bf16 a[4:7], v[36:39], v[0:3], a[4:7]\n"
        "v_cvt_pk_bf16_f32 v189, 0, v189\n"
        "v_cvt_pk_bf16_f32 v191, 0, v191\n"
        "v_mfma_f32_16x16x32_bf16 a[8:11], v[40:43], v[0:3], a[8:11]\n"
        "v_mul_f32 v190, v179, v187\n"
        "v_mul_f32 v192, v180, v187\n"
        "v_mfma_f32_16x16x32_bf16 a[8:11], v[72:75], v[4:7], a[8:11]\n"
        "v_cvt_pk_bf16_f32 v190, 0, v190\n"
        "v_cvt_pk_bf16_f32 v192, 0, v192\n"
        "v_mfma_f32_16x16x32_bf16 a[40:43], v[72:75], v[12:15], a[40:43]\n"
        "global_load_lds_dwordx4 v241, s[86:87] offset:3072\n"
        "s_add_u32 m0, s73, 0x1c000\n"
        "v_mfma_f32_16x16x32_bf16 a[40:43], v[40:43], v[8:11], a[40:43]\n"
        "s_add_u32 s86, s62, 0x380000\n"
        "s_addc_u32 s87, s63, 0\n"
        "v_add_f32 v189, v189, v190\n"
        "v_mfma_f32_16x16x32_bf16 a[44:47], v[44:47], v[8:11], a[44:47]\n"
        "v_add_f32 v191, v191, v192\n"
        "v_cvt_pk_bf16_f32 v189, 0, v189\n"
        "v_mfma_f32_16x16x32_bf16 a[44:47], v[76:79], v[12:15], a[44:47]\n"
        "v_cvt_pk_bf16_f32 v191, 0, v191\n"
        "v_mul_f32 v190, v180, v186\n"
        "v_mfma_f32_16x16x32_bf16 a[12:15], v[76:79], v[4:7], a[12:15]\n"
        "global_load_lds_dwordx4 v238, s[86:87] offset:0\n"
        "v_mfma_f32_16x16x32_bf16 a[12:15], v[44:47], v[0:3], a[12:15]\n"
        "v_mul_f32 v192, v181, v186\n"
        "v_cvt_pk_bf16_f32 v190, 0, v190\n"
        "v_mfma_f32_16x16x32_bf16 a[16:19], v[0:3], v[48:51], a[16:19]\n"
        "s_waitcnt lgkmcnt(0)\n"
        "v_cvt_pk_bf16_f32 v192, 0, v192\n"
        "v_add_f32 v189, v189, v190\n"
        "v_mfma_f32_16x16x32_bf16 a[16:19], v[4:7], v[80:83], a[16:19]\n"
        "v_add_f32 v191, v191, v192\n"
        "v_cvt_pk_bf16_f32 v189, 0, v189\n"
        "v_mfma_f32_16x16x32_bf16 a[48:51], v[12:15], v[80:83], a[48:51]\n"
        "global_load_lds_dwordx4 v239, s[86:87] offset:1024\n"
        "v_mfma_f32_16x16x32_bf16 a[48:51], v[8:11], v[48:51], a[48:51]\n"
        "v_cvt_pk_bf16_f32 v191, 0, v191\n"
        "v_mul_f32 v190, v181, v185\n"
        "v_mfma_f32_16x16x32_bf16 a[52:55], v[8:11], v[52:55], a[52:55]\n"
        "v_mul_f32 v192, v182, v185\n"
        "v_cvt_pk_bf16_f32 v190, 0, v190\n"
        "v_mfma_f32_16x16x32_bf16 a[52:55], v[12:15], v[84:87], a[52:55]\n"
        "v_cvt_pk_bf16_f32 v192, 0, v192\n"
        "v_add_f32 v189, v189, v190\n"
        "v_mfma_f32_16x16x32_bf16 a[20:23], v[4:7], v[84:87], a[20:23]\n"
        "global_load_lds_dwordx4 v240, s[86:87] offset:2048\n"
        "v_mfma_f32_16x16x32_bf16 a[20:23], v[0:3], v[52:55], a[20:23]\n"
        "v_add_f32 v191, v191, v192\n"
        "v_cvt_pk_bf16_f32 v189, 0, v189\n"
        "v_mfma_f32_16x16x32_bf16 a[24:27], v[0:3], v[56:59], a[24:27]\n"
        "v_cvt_pk_bf16_f32 v191, 0, v191\n"
        "v_lshlrev_b32 v190, 16, v127\n"
        "v_mfma_f32_16x16x32_bf16 a[24:27], v[4:7], v[88:91], a[24:27]\n"
        "v_lshlrev_b32 v192, 16, v129\n"
        "v_mul_f32 v189, v189, v190\n"
        "v_mfma_f32_16x16x32_bf16 a[56:59], v[12:15], v[88:91], a[56:59]\n"
        "global_load_lds_dwordx4 v241, s[86:87] offset:3072\n"
        "s_add_u32 m0, s72, 0x8000\n"
        "v_mfma_f32_16x16x32_bf16 a[56:59], v[8:11], v[56:59], a[56:59]\n"
        "s_add_u32 s86, s56, 0x380\n"
        "s_addc_u32 s87, s57, 0\n"
        "v_mul_f32 v191, v191, v192\n"
        "v_mfma_f32_16x16x32_bf16 a[60:63], v[8:11], v[60:63], a[60:63]\n"
        "ds_read_b128 v[8:11], v224 offset:34816\n"
        "v_cvt_pk_bf16_f32 v201, v191, v189\n"
        "v_mfma_f32_16x16x32_bf16 a[60:63], v[12:15], v[92:95], a[60:63]\n"
        "ds_read_b128 v[12:15], v225 offset:34816\n"
        "v_mul_f32 v193, v180, v188\n"
        "v_mfma_f32_16x16x32_bf16 a[28:31], v[4:7], v[92:95], a[28:31]\n"
        "ds_read_b128 v[4:7], v225 offset:32768\n"
        "v_mul_f32 v195, v181, v188\n"
        "v_mfma_f32_16x16x32_bf16 a[28:31], v[0:3], v[60:63], a[28:31]\n"
        "ds_read_b128 v[0:3], v224 offset:32768\n"
        "v_cvt_pk_bf16_f32 v193, 0, v193\n"
        "v_mfma_f32_16x16x32_bf16 a[64:67], v[32:35], v[16:19], a[64:67]\n"
        "v_cvt_pk_bf16_f32 v195, 0, v195\n"
        "v_mul_f32 v194, v181, v187\n"
        "v_mfma_f32_16x16x32_bf16 a[64:67], v[64:67], v[20:23], a[64:67]\n"
        "v_mul_f32 v196, v182, v187\n"
        "v_cvt_pk_bf16_f32 v194, 0, v194\n"
        "v_mfma_f32_16x16x32_bf16 a[96:99], v[64:67], v[28:31], a[96:99]\n"
        "v_cvt_pk_bf16_f32 v196, 0, v196\n"
        "v_add_f32 v193, v193, v194\n"
        "v_mfma_f32_16x16x32_bf16 a[96:99], v[32:35], v[24:27], a[96:99]\n"
        "v_add_f32 v195, v195, v196\n"
        "v_cvt_pk_bf16_f32 v193, 0, v193\n"
        "v_mfma_f32_16x16x32_bf16 a[100:103], v[36:39], v[24:27], a[100:103]\n"
        "v_cvt_pk_bf16_f32 v195, 0, v195\n"
        "v_mul_f32 v194, v182, v186\n"
        "v_mfma_f32_16x16x32_bf16 a[100:103], v[68:71], v[28:31], a[100:103]\n"
        "v_mul_f32 v196, v183, v186\n"
        "v_cvt_pk_bf16_f32 v194, 0, v194\n"
        "v_mfma_f32_16x16x32_bf16 a[68:71], v[68:71], v[20:23], a[68:71]\n"
        "v_cvt_pk_bf16_f32 v196, 0, v196\n"
        "v_add_f32 v193, v193, v194\n"
        "v_mfma_f32_16x16x32_bf16 a[68:71], v[36:39], v[16:19], a[68:71]\n"
        "v_add_f32 v195, v195, v196\n"
        "v_cvt_pk_bf16_f32 v193, 0, v193\n"
        "v_mfma_f32_16x16x32_bf16 a[72:75], v[40:43], v[16:19], a[72:75]\n"
        "v_cvt_pk_bf16_f32 v195, 0, v195\n"
        "v_mul_f32 v194, v183, v185\n"
        "v_mfma_f32_16x16x32_bf16 a[72:75], v[72:75], v[20:23], a[72:75]\n"
        "v_mul_f32 v196, v184, v185\n"
        "v_cvt_pk_bf16_f32 v194, 0, v194\n"
        "v_mfma_f32_16x16x32_bf16 a[104:107], v[72:75], v[28:31], a[104:107]\n"
        "v_cvt_pk_bf16_f32 v196, 0, v196\n"
        "v_add_f32 v193, v193, v194\n"
        "v_mfma_f32_16x16x32_bf16 a[104:107], v[40:43], v[24:27], a[104:107]\n"
        "v_add_f32 v195, v195, v196\n"
        "v_cvt_pk_bf16_f32 v193, 0, v193\n"
        "v_mfma_f32_16x16x32_bf16 a[108:111], v[44:47], v[24:27], a[108:111]\n"
        "v_cvt_pk_bf16_f32 v195, 0, v195\n"
        "v_lshlrev_b32 v194, 16, v131\n"
        "v_mfma_f32_16x16x32_bf16 a[108:111], v[76:79], v[28:31], a[108:111]\n"
        "v_lshlrev_b32 v196, 16, v133\n"
        "v_mul_f32 v193, v193, v194\n"
        "v_mfma_f32_16x16x32_bf16 a[76:79], v[76:79], v[20:23], a[76:79]\n"
        "v_mul_f32 v195, v195, v196\n"
        "v_cvt_pk_bf16_f32 v200, v195, v193\n"
        "v_mfma_f32_16x16x32_bf16 a[76:79], v[44:47], v[16:19], a[76:79]\n"
        "global_store_dwordx4 v171, v[200:203], s[76:77] offset:-4096 sc1\n"
        "v_lshlrev_b32 v185, 16, v140\n"
        "v_and_b32 v186, s35, v140\n"
        "v_lshlrev_b32 v187, 16, v141\n"
        "v_and_b32 v188, s35, v141\n"
        "v_mfma_f32_16x16x32_bf16 a[80:83], v[16:19], v[48:51], a[80:83]\n"
        "v_and_b32 v174, s35, v97\n"
        "v_and_b32 v175, s35, v99\n"
        "v_mfma_f32_16x16x32_bf16 a[80:83], v[20:23], v[80:83], a[80:83]\n"
        "v_and_b32 v176, s35, v101\n"
        "v_and_b32 v177, s35, v103\n"
        "v_mfma_f32_16x16x32_bf16 a[112:115], v[28:31], v[80:83], a[112:115]\n"
        "v_and_b32 v178, s35, v105\n"
        "v_and_b32 v179, s35, v107\n"
        "v_mfma_f32_16x16x32_bf16 a[112:115], v[24:27], v[48:51], a[112:115]\n"
        "v_and_b32 v180, s35, v109\n"
        "v_and_b32 v181, s35, v111\n"
        "v_mfma_f32_16x16x32_bf16 a[116:119], v[24:27], v[52:55], a[116:119]\n"
        "v_and_b32 v182, s35, v113\n"
        "v_and_b32 v183, s35, v115\n"
        "v_mfma_f32_16x16x32_bf16 a[116:119], v[28:31], v[84:87], a[116:119]\n"
        "v_and_b32 v184, s35, v117\n"
        "v_mul_f32 v189, v174, v188\n"
        "v_mfma_f32_16x16x32_bf16 a[84:87], v[20:23], v[84:87], a[84:87]\n"
        "v_mul_f32 v191, v175, v188\n"
        "v_cvt_pk_bf16_f32 v189, 0, v189\n"
        "v_mfma_f32_16x16x32_bf16 a[84:87], v[16:19], v[52:55], a[84:87]\n"
        "global_load_dword v253, v173, s[82:83] offset:0\n"
        "s_add_u32 s82, s12, 0x600000\n"
        "s_addc_u32 s83, s13, 0\n"
        "v_mfma_f32_16x16x32_bf16 a[88:91], v[16:19], v[56:59], a[88:91]\n"
        "v_cvt_pk_bf16_f32 v191, 0, v191\n"
        "v_mul_f32 v190, v175, v187\n"
        "v_mfma_f32_16x16x32_bf16 a[88:91], v[20:23], v[88:91], a[88:91]\n"
        "v_mul_f32 v192, v176, v187\n"
        "v_cvt_pk_bf16_f32 v190, 0, v190\n"
        "v_mfma_f32_16x16x32_bf16 a[120:123], v[28:31], v[88:91], a[120:123]\n"
        "v_cvt_pk_bf16_f32 v192, 0, v192\n"
        "v_add_f32 v189, v189, v190\n"
        "v_mfma_f32_16x16x32_bf16 a[120:123], v[24:27], v[56:59], a[120:123]\n"
        "v_add_f32 v191, v191, v192\n"
        "v_cvt_pk_bf16_f32 v189, 0, v189\n"
        "v_mfma_f32_16x16x32_bf16 a[124:127], v[24:27], v[60:63], a[124:127]\n"
        "ds_read_b128 v[24:27], v224 offset:38912\n"
        "v_cvt_pk_bf16_f32 v191, 0, v191\n"
        "v_mfma_f32_16x16x32_bf16 a[124:127], v[28:31], v[92:95], a[124:127]\n"
        "ds_read_b128 v[28:31], v225 offset:38912\n"
        "v_mul_f32 v190, v176, v186\n"
        "v_mfma_f32_16x16x32_bf16 a[92:95], v[20:23], v[92:95], a[92:95]\n"
        "ds_read_b128 v[20:23], v225 offset:36864\n"
        "v_mul_f32 v192, v177, v186\n"
        "v_mfma_f32_16x16x32_bf16 a[92:95], v[16:19], v[60:63], a[92:95]\n"
        "ds_read_b128 v[16:19], v224 offset:36864\n"
        "s_waitcnt lgkmcnt(4)\n"
        "v_mfma_f32_16x16x32_bf16 a[128:131], v[32:35], v[0:3], a[128:131]\n"
        "v_cvt_pk_bf16_f32 v190, 0, v190\n"
        "v_cvt_pk_bf16_f32 v192, 0, v192\n"
        "v_mfma_f32_16x16x32_bf16 a[128:131], v[64:67], v[4:7], a[128:131]\n"
        "v_add_f32 v189, v189, v190\n"
        "v_add_f32 v191, v191, v192\n"
        "v_mfma_f32_16x16x32_bf16 a[160:163], v[64:67], v[12:15], a[160:163]\n"
        "v_cvt_pk_bf16_f32 v189, 0, v189\n"
        "v_cvt_pk_bf16_f32 v191, 0, v191\n"
        "v_mfma_f32_16x16x32_bf16 a[160:163], v[32:35], v[8:11], a[160:163]\n"
        "v_mul_f32 v190, v177, v185\n"
        "v_mul_f32 v192, v178, v185\n"
        "v_mfma_f32_16x16x32_bf16 a[164:167], v[36:39], v[8:11], a[164:167]\n"
        "v_cvt_pk_bf16_f32 v190, 0, v190\n"
        "v_cvt_pk_bf16_f32 v192, 0, v192\n"
        "v_mfma_f32_16x16x32_bf16 a[164:167], v[68:71], v[12:15], a[164:167]\n"
        "v_add_f32 v189, v189, v190\n"
        "v_add_f32 v191, v191, v192\n"
        "v_mfma_f32_16x16x32_bf16 a[132:135], v[68:71], v[4:7], a[132:135]\n"
        "v_cvt_pk_bf16_f32 v189, 0, v189\n"
        "v_cvt_pk_bf16_f32 v191, 0, v191\n"
        "v_mfma_f32_16x16x32_bf16 a[132:135], v[36:39], v[0:3], a[132:135]\n"
        "v_and_b32 v190, s35, v119\n"
        "v_and_b32 v192, s35, v121\n"
        "v_mfma_f32_16x16x32_bf16 a[136:139], v[40:43], v[0:3], a[136:139]\n"
        "v_mul_f32 v189, v189, v190\n"
        "v_mul_f32 v191, v191, v192\n"
        "v_mfma_f32_16x16x32_bf16 a[136:139], v[72:75], v[4:7], a[136:139]\n"
        "v_cvt_pk_bf16_f32 v211, v191, v189\n"
        "v_mul_f32 v193, v176, v188\n"
        "v_mfma_f32_16x16x32_bf16 a[168:171], v[72:75], v[12:15], a[168:171]\n"
        "v_mul_f32 v195, v177, v188\n"
        "v_cvt_pk_bf16_f32 v193, 0, v193\n"
        "v_mfma_f32_16x16x32_bf16 a[168:171], v[40:43], v[8:11], a[168:171]\n"
        "v_cvt_pk_bf16_f32 v195, 0, v195\n"
        "v_mul_f32 v194, v177, v187\n"
        "v_mfma_f32_16x16x32_bf16 a[172:175], v[44:47], v[8:11], a[172:175]\n"
        "v_mul_f32 v196, v178, v187\n"
        "v_cvt_pk_bf16_f32 v194, 0, v194\n"
        "v_mfma_f32_16x16x32_bf16 a[172:175], v[76:79], v[12:15], a[172:175]\n"
        "v_cvt_pk_bf16_f32 v196, 0, v196\n"
        "v_add_f32 v193, v193, v194\n"
        "v_mfma_f32_16x16x32_bf16 a[140:143], v[76:79], v[4:7], a[140:143]\n"
        "v_add_f32 v195, v195, v196\n"
        "v_cvt_pk_bf16_f32 v193, 0, v193\n"
        "v_mfma_f32_16x16x32_bf16 a[140:143], v[44:47], v[0:3], a[140:143]\n"
        "v_cvt_pk_bf16_f32 v195, 0, v195\n"
        "v_mul_f32 v194, v178, v186\n"
        "v_mfma_f32_16x16x32_bf16 a[144:147], v[0:3], v[48:51], a[144:147]\n"
        "v_mul_f32 v196, v179, v186\n"
        "v_cvt_pk_bf16_f32 v194, 0, v194\n"
        "v_mfma_f32_16x16x32_bf16 a[144:147], v[4:7], v[80:83], a[144:147]\n"
        "v_cvt_pk_bf16_f32 v196, 0, v196\n"
        "v_add_f32 v193, v193, v194\n"
        "v_mfma_f32_16x16x32_bf16 a[176:179], v[12:15], v[80:83], a[176:179]\n"
        "v_add_f32 v195, v195, v196\n"
        "v_cvt_pk_bf16_f32 v193, 0, v193\n"
        "v_mfma_f32_16x16x32_bf16 a[176:179], v[8:11], v[48:51], a[176:179]\n"
        "v_cvt_pk_bf16_f32 v195, 0, v195\n"
        "v_mul_f32 v194, v179, v185\n"
        "v_mfma_f32_16x16x32_bf16 a[180:183], v[8:11], v[52:55], a[180:183]\n"
        "v_mul_f32 v196, v180, v185\n"
        "v_cvt_pk_bf16_f32 v194, 0, v194\n"
        "v_mfma_f32_16x16x32_bf16 a[180:183], v[12:15], v[84:87], a[180:183]\n"
        "v_cvt_pk_bf16_f32 v196, 0, v196\n"
        "v_add_f32 v193, v193, v194\n"
        "v_mfma_f32_16x16x32_bf16 a[148:151], v[4:7], v[84:87], a[148:151]\n"
        "v_add_f32 v195, v195, v196\n"
        "v_cvt_pk_bf16_f32 v193, 0, v193\n"
        "v_mfma_f32_16x16x32_bf16 a[148:151], v[0:3], v[52:55], a[148:151]\n"
        "v_cvt_pk_bf16_f32 v195, 0, v195\n"
        "v_and_b32 v194, s35, v123\n"
        "v_mfma_f32_16x16x32_bf16 a[152:155], v[0:3], v[56:59], a[152:155]\n"
        "v_and_b32 v196, s35, v125\n"
        "v_mul_f32 v193, v193, v194\n"
        "v_mfma_f32_16x16x32_bf16 a[152:155], v[4:7], v[88:91], a[152:155]\n"
        "v_mul_f32 v195, v195, v196\n"
        "v_cvt_pk_bf16_f32 v210, v195, v193\n"
        "v_mfma_f32_16x16x32_bf16 a[184:187], v[12:15], v[88:91], a[184:187]\n"
        "v_mul_f32 v189, v178, v188\n"
        "v_mul_f32 v191, v179, v188\n"
        "v_mfma_f32_16x16x32_bf16 a[184:187], v[8:11], v[56:59], a[184:187]\n"
        "s_waitcnt vmcnt(10) lgkmcnt(0)\n"
        "v_cvt_pk_bf16_f32 v189, 0, v189\n"
        "v_cvt_pk_bf16_f32 v191, 0, v191\n"
        "v_mfma_f32_16x16x32_bf16 a[188:191], v[8:11], v[60:63], a[188:191]\n"
        "s_barrier\n"
        "v_mfma_f32_16x16x32_bf16 a[188:191], v[12:15], v[92:95], a[188:191]\n"
        "global_load_lds_dwordx4 v234, s[86:87] offset:0\n"
        "ds_read_b128 v[8:11], v224 offset:18432\n"
        "v_mfma_f32_16x16x32_bf16 a[156:159], v[4:7], v[92:95], a[156:159]\n"
        "ds_read_b128 v[4:7], v225 offset:16384\n"
        "v_mul_f32 v190, v179, v187\n"
        "v_mfma_f32_16x16x32_bf16 a[156:159], v[0:3], v[60:63], a[156:159]\n"
        "ds_read_b128 v[0:3], v224 offset:16384\n"
        "v_mul_f32 v192, v180, v187\n"
        "v_mfma_f32_16x16x32_bf16 a[192:195], v[32:35], v[16:19], a[192:195]\n"
        "ds_read_b128 v[12:15], v225 offset:18432\n"
        "v_cvt_pk_bf16_f32 v190, 0, v190\n"
        "v_mfma_f32_16x16x32_bf16 a[192:195], v[64:67], v[20:23], a[192:195]\n"
        "global_load_lds_dwordx4 v235, s[86:87] offset:1024\n"
        "v_cvt_pk_bf16_f32 v192, 0, v192\n"
        "v_mfma_f32_16x16x32_bf16 a[224:227], v[64:67], v[28:31], a[224:227]\n"
        "ds_read_b64_tr_b16 v[64:65], v226 offset:8192\n"
        "v_add_f32 v189, v189, v190\n"
        "v_mfma_f32_16x16x32_bf16 a[224:227], v[32:35], v[24:27], a[224:227]\n"
        "ds_read_b64_tr_b16 v[32:33], v226 offset:0\n"
        "v_add_f32 v191, v191, v192\n"
        "v_mfma_f32_16x16x32_bf16 a[228:231], v[36:39], v[24:27], a[228:231]\n"
        "ds_read_b64_tr_b16 v[34:35], v227 offset:0\n"
        "v_cvt_pk_bf16_f32 v189, 0, v189\n"
        "v_mfma_f32_16x16x32_bf16 a[228:231], v[68:71], v[28:31], a[228:231]\n"
        "ds_read_b64_tr_b16 v[66:67], v227 offset:8192\n"
        "global_load_lds_dwordx4 v236, s[86:87] offset:2048\n"
        "v_mfma_f32_16x16x32_bf16 a[196:199], v[68:71], v[20:23], a[196:199]\n"
        "ds_read_b64_tr_b16 v[68:69], v228 offset:8192\n"
        "v_cvt_pk_bf16_f32 v191, 0, v191\n"
        "v_mfma_f32_16x16x32_bf16 a[196:199], v[36:39], v[16:19], a[196:199]\n"
        "ds_read_b64_tr_b16 v[36:37], v228 offset:0\n"
        "v_mul_f32 v190, v180, v186\n"
        "v_mfma_f32_16x16x32_bf16 a[200:203], v[40:43], v[16:19], a[200:203]\n"
        "ds_read_b64_tr_b16 v[38:39], v229 offset:0\n"
        "v_mul_f32 v192, v181, v186\n"
        "v_mfma_f32_16x16x32_bf16 a[200:203], v[72:75], v[20:23], a[200:203]\n"
        "ds_read_b64_tr_b16 v[70:71], v229 offset:8192\n"
        "global_load_lds_dwordx4 v237, s[86:87] offset:3072\n"
        "s_add_u32 m0, s72, 0x9000\n"
        "v_mfma_f32_16x16x32_bf16 a[232:235], v[72:75], v[28:31], a[232:235]\n"
        "ds_read_b64_tr_b16 v[72:73], v230 offset:8192\n"
        "s_add_u32 s86, s58, 0x380\n"
        "s_addc_u32 s87, s59, 0\n"
        "v_mfma_f32_16x16x32_bf16 a[232:235], v[40:43], v[24:27], a[232:235]\n"
        "ds_read_b64_tr_b16 v[40:41], v230 offset:0\n"
        "v_cvt_pk_bf16_f32 v190, 0, v190\n"
        "v_mfma_f32_16x16x32_bf16 a[236:239], v[44:47], v[24:27], a[236:239]\n"
        "ds_read_b64_tr_b16 v[42:43], v231 offset:0\n"
        "v_cvt_pk_bf16_f32 v192, 0, v192\n"
        "v_mfma_f32_16x16x32_bf16 a[236:239], v[76:79], v[28:31], a[236:239]\n"
        "ds_read_b64_tr_b16 v[74:75], v231 offset:8192\n"
        "global_load_lds_dwordx4 v234, s[86:87] offset:0\n"
        "v_mfma_f32_16x16x32_bf16 a[204:207], v[76:79], v[20:23], a[204:207]\n"
        "ds_read_b64_tr_b16 v[76:77], v232 offset:8192\n"
        "v_add_f32 v189, v189, v190\n"
        "v_mfma_f32_16x16x32_bf16 a[204:207], v[44:47], v[16:19], a[204:207]\n"
        "ds_read_b64_tr_b16 v[44:45], v232 offset:0\n"
        "v_add_f32 v191, v191, v192\n"
        "v_mfma_f32_16x16x32_bf16 a[208:211], v[16:19], v[48:51], a[208:211]\n"
        "ds_read_b64_tr_b16 v[46:47], v233 offset:0\n"
        "v_cvt_pk_bf16_f32 v189, 0, v189\n"
        "v_mfma_f32_16x16x32_bf16 a[208:211], v[20:23], v[80:83], a[208:211]\n"
        "ds_read_b64_tr_b16 v[78:79], v233 offset:8192\n"
        "global_load_lds_dwordx4 v235, s[86:87] offset:1024\n"
        "v_mfma_f32_16x16x32_bf16 a[240:243], v[28:31], v[80:83], a[240:243]\n"
        "ds_read_b64_tr_b16 v[80:81], v226 offset:24576\n"
        "v_cvt_pk_bf16_f32 v191, 0, v191\n"
        "v_mfma_f32_16x16x32_bf16 a[240:243], v[24:27], v[48:51], a[240:243]\n"
        "ds_read_b64_tr_b16 v[48:49], v226 offset:16384\n"
        "v_mul_f32 v190, v181, v185\n"
        "v_mfma_f32_16x16x32_bf16 a[244:247], v[24:27], v[52:55], a[244:247]\n"
        "ds_read_b64_tr_b16 v[50:51], v227 offset:16384\n"
        "v_mul_f32 v192, v182, v185\n"
        "v_mfma_f32_16x16x32_bf16 a[244:247], v[28:31], v[84:87], a[244:247]\n"
        "ds_read_b64_tr_b16 v[82:83], v227 offset:24576\n"
        "global_load_lds_dwordx4 v236, s[86:87] offset:2048\n"
        "v_mfma_f32_16x16x32_bf16 a[212:215], v[20:23], v[84:87], a[212:215]\n"
        "ds_read_b64_tr_b16 v[84:85], v228 offset:24576\n"
        "v_cvt_pk_bf16_f32 v190, 0, v190\n"
        "v_mfma_f32_16x16x32_bf16 a[212:215], v[16:19], v[52:55], a[212:215]\n"
        "ds_read_b64_tr_b16 v[52:53], v228 offset:16384\n"
        "v_cvt_pk_bf16_f32 v192, 0, v192\n"
        "v_mfma_f32_16x16x32_bf16 a[216:219], v[16:19], v[56:59], a[216:219]\n"
        "ds_read_b64_tr_b16 v[54:55], v229 offset:16384\n"
        "v_add_f32 v189, v189, v190\n"
        "v_mfma_f32_16x16x32_bf16 a[216:219], v[20:23], v[88:91], a[216:219]\n"
        "ds_read_b64_tr_b16 v[86:87], v229 offset:24576\n"
        "global_load_lds_dwordx4 v237, s[86:87] offset:3072\n"
        "s_add_u32 m0, s73, 0x20000\n"
        "v_mfma_f32_16x16x32_bf16 a[248:251], v[28:31], v[88:91], a[248:251]\n"
        "ds_read_b64_tr_b16 v[88:89], v230 offset:24576\n"
        "s_add_u32 s86, s68, 0x80000\n"
        "s_addc_u32 s87, s69, 0\n"
        "v_mfma_f32_16x16x32_bf16 a[248:251], v[24:27], v[56:59], a[248:251]\n"
        "ds_read_b64_tr_b16 v[56:57], v230 offset:16384\n"
        "v_add_f32 v191, v191, v192\n"
        "v_mfma_f32_16x16x32_bf16 a[252:255], v[24:27], v[60:63], a[252:255]\n"
        "ds_read_b64_tr_b16 v[58:59], v231 offset:16384\n"
        "v_cvt_pk_bf16_f32 v189, 0, v189\n"
        "v_mfma_f32_16x16x32_bf16 a[252:255], v[28:31], v[92:95], a[252:255]\n"
        "ds_read_b64_tr_b16 v[90:91], v231 offset:24576\n"
        "global_load_lds_dwordx4 v238, s[68:69] offset:0\n"
        "v_mfma_f32_16x16x32_bf16 a[220:223], v[20:23], v[92:95], a[220:223]\n"
        "ds_read_b64_tr_b16 v[92:93], v232 offset:24576\n"
        "v_cvt_pk_bf16_f32 v191, 0, v191\n"
        "v_mfma_f32_16x16x32_bf16 a[220:223], v[16:19], v[60:63], a[220:223]\n"
        "ds_read_b64_tr_b16 v[60:61], v232 offset:16384\n"
        "s_waitcnt lgkmcnt(8)\n"
        "v_mfma_f32_16x16x32_bf16 a[0:3], v[32:35], v[0:3], a[0:3]\n"
        "ds_read_b64_tr_b16 v[62:63], v233 offset:16384\n"
        "v_and_b32 v190, s35, v127\n"
        "v_mfma_f32_16x16x32_bf16 a[0:3], v[64:67], v[4:7], a[0:3]\n"
        "ds_read_b64_tr_b16 v[94:95], v233 offset:24576\n"
        "global_load_lds_dwordx4 v239, s[68:69] offset:1024\n"
        "v_mfma_f32_16x16x32_bf16 a[32:35], v[64:67], v[12:15], a[32:35]\n"
        "ds_read_b128 v[16:19], v224 offset:20480\n"
        "v_and_b32 v192, s35, v129\n"
        "v_mfma_f32_16x16x32_bf16 a[32:35], v[32:35], v[8:11], a[32:35]\n"
        "ds_read_b128 v[20:23], v225 offset:20480\n"
        "v_mul_f32 v189, v189, v190\n"
        "v_mfma_f32_16x16x32_bf16 a[36:39], v[36:39], v[8:11], a[36:39]\n"
        "ds_read_b128 v[24:27], v224 offset:22528\n"
        "v_mul_f32 v191, v191, v192\n"
        "v_mfma_f32_16x16x32_bf16 a[36:39], v[68:71], v[12:15], a[36:39]\n"
        "ds_read_b128 v[28:31], v225 offset:22528\n"
        "global_load_lds_dwordx4 v240, s[68:69] offset:2048\n"
        "v_mfma_f32_16x16x32_bf16 a[4:7], v[68:71], v[4:7], a[4:7]\n"
        "v_cvt_pk_bf16_f32 v209, v191, v189\n"
        "v_mul_f32 v193, v180, v188\n"
        "v_mfma_f32_16x16x32_bf16 a[4:7], v[36:39], v[0:3], a[4:7]\n"
        "v_mul_f32 v195, v181, v188\n"
        "v_cvt_pk_bf16_f32 v193, 0, v193\n"
        "v_mfma_f32_16x16x32_bf16 a[8:11], v[40:43], v[0:3], a[8:11]\n"
        "v_cvt_pk_bf16_f32 v195, 0, v195\n"
        "v_mul_f32 v194, v181, v187\n"
        "v_mfma_f32_16x16x32_bf16 a[8:11], v[72:75], v[4:7], a[8:11]\n"
        "global_load_dword v253, v173, s[82:83] offset:0\n"
        "v_mfma_f32_16x16x32_bf16 a[40:43], v[72:75], v[12:15], a[40:43]\n"
        "global_load_lds_dwordx4 v241, s[68:69] offset:3072\n"
        "s_add_u32 m0, s73, 0x24000\n"
        "v_mul_f32 v196, v182, v187\n"
        "v_mfma_f32_16x16x32_bf16 a[40:43], v[40:43], v[8:11], a[40:43]\n"
        "s_add_u32 s82, s12, 0x700000\n"
        "s_addc_u32 s83, s13, 0\n"
        "v_cvt_pk_bf16_f32 v194, 0, v194\n"
        "v_mfma_f32_16x16x32_bf16 a[44:47], v[44:47], v[8:11], a[44:47]\n"
        "v_cvt_pk_bf16_f32 v196, 0, v196\n"
        "v_add_f32 v193, v193, v194\n"
        "v_mfma_f32_16x16x32_bf16 a[44:47], v[76:79], v[12:15], a[44:47]\n"
        "v_add_f32 v195, v195, v196\n"
        "v_cvt_pk_bf16_f32 v193, 0, v193\n"
        "v_mfma_f32_16x16x32_bf16 a[12:15], v[76:79], v[4:7], a[12:15]\n"
        "global_load_lds_dwordx4 v238, s[86:87] offset:0\n"
        "v_cvt_pk_bf16_f32 v195, 0, v195\n"
        "v_mfma_f32_16x16x32_bf16 a[12:15], v[44:47], v[0:3], a[12:15]\n"
        "v_mul_f32 v194, v182, v186\n"
        "v_mul_f32 v196, v183, v186\n"
        "v_mfma_f32_16x16x32_bf16 a[16:19], v[0:3], v[48:51], a[16:19]\n"
        "s_waitcnt lgkmcnt(0)\n"
        "v_cvt_pk_bf16_f32 v194, 0, v194\n"
        "v_cvt_pk_bf16_f32 v196, 0, v196\n"
        "v_mfma_f32_16x16x32_bf16 a[16:19], v[4:7], v[80:83], a[16:19]\n"
        "v_add_f32 v193, v193, v194\n"
        "v_add_f32 v195, v195, v196\n"
        "v_mfma_f32_16x16x32_bf16 a[48:51], v[12:15], v[80:83], a[48:51]\n"
        "global_load_lds_dwordx4 v239, s[86:87] offset:1024\n"
        "v_cvt_pk_bf16_f32 v193, 0, v193\n"
        "v_mfma_f32_16x16x32_bf16 a[48:51], v[8:11], v[48:51], a[48:51]\n"
        "v_cvt_pk_bf16_f32 v195, 0, v195\n"
        "v_mul_f32 v194, v183, v185\n"
        "v_mfma_f32_16x16x32_bf16 a[52:55], v[8:11], v[52:55], a[52:55]\n"
        "v_mul_f32 v196, v184, v185\n"
        "v_cvt_pk_bf16_f32 v194, 0, v194\n"
        "v_mfma_f32_16x16x32_bf16 a[52:55], v[12:15], v[84:87], a[52:55]\n"
        "v_cvt_pk_bf16_f32 v196, 0, v196\n"
        "v_add_f32 v193, v193, v194\n"
        "v_mfma_f32_16x16x32_bf16 a[20:23], v[4:7], v[84:87], a[20:23]\n"
        "global_load_lds_dwordx4 v240, s[86:87] offset:2048\n"
        "v_add_f32 v195, v195, v196\n"
        "v_mfma_f32_16x16x32_bf16 a[20:23], v[0:3], v[52:55], a[20:23]\n"
        "s_and_b32 s53, s40, 1\n"
        "v_cvt_pk_bf16_f32 v193, 0, v193\n"
        "v_cvt_pk_bf16_f32 v195, 0, v195\n"
        "v_mfma_f32_16x16x32_bf16 a[24:27], v[0:3], v[56:59], a[24:27]\n"
        "s_lshl_b32 s53, s53, 10\n"
        "v_and_b32 v194, s35, v131\n"
        "v_and_b32 v196, s35, v133\n"
        "v_mfma_f32_16x16x32_bf16 a[24:27], v[4:7], v[88:91], a[24:27]\n"
        "s_sub_u32 s53, 0x8000, s53\n"
        "v_mul_f32 v193, v193, v194\n"
        "v_mul_f32 v195, v195, v196\n"
        "v_mfma_f32_16x16x32_bf16 a[56:59], v[12:15], v[88:91], a[56:59]\n"
        "global_load_lds_dwordx4 v241, s[86:87] offset:3072\n"
        "s_add_u32 m0, s72, 0x0\n"
        "v_cvt_pk_bf16_f32 v208, v195, v193\n"
        "v_mfma_f32_16x16x32_bf16 a[56:59], v[8:11], v[56:59], a[56:59]\n"
        "s_add_u32 s86, s64, 0x80\n"
        "s_addc_u32 s87, s65, 0\n"
        "v_readfirstlane_b32 s70, v250\n"
        "v_mfma_f32_16x16x32_bf16 a[60:63], v[8:11], v[60:63], a[60:63]\n"
        "ds_read_b128 v[8:11], v224 offset:2048\n"
        "v_readfirstlane_b32 s71, v251\n"
        "v_mfma_f32_16x16x32_bf16 a[60:63], v[12:15], v[92:95], a[60:63]\n"
        "ds_read_b128 v[12:15], v225 offset:2048\n"
        "s_add_u32 s70, s70, 0x1\n"
        "s_addc_u32 s71, s71, 0\n"
        "v_mfma_f32_16x16x32_bf16 a[28:31], v[4:7], v[92:95], a[28:31]\n"
        "ds_read_b128 v[4:7], v225 offset:0\n"
        "v_mov_b32 v250, s70\n"
        "v_mfma_f32_16x16x32_bf16 a[28:31], v[0:3], v[60:63], a[28:31]\n"
        "ds_read_b128 v[0:3], v224 offset:0\n"
        "v_mov_b32 v251, s71\n"
        "v_mfma_f32_16x16x32_bf16 a[64:67], v[32:35], v[16:19], a[64:67]\n"
        "global_store_dwordx4 v171, v[208:211], s[76:77] offset:0 sc1\n"
        "s_add_u32 s76, s38, 0x3b20040\n"
        "s_addc_u32 s77, s39, 0\n"
        "v_lshlrev_b32 v174, 16, v158\n"
        "v_and_b32 v175, s35, v158\n"
        "v_lshlrev_b32 v176, 16, v159\n"
        "v_mfma_f32_16x16x32_bf16 a[64:67], v[64:67], v[20:23], a[64:67]\n"
        "v_and_b32 v177, s35, v159\n"
        "v_lshlrev_b32 v178, 16, v160\n"
        "v_mfma_f32_16x16x32_bf16 a[96:99], v[64:67], v[28:31], a[96:99]\n"
        "v_and_b32 v179, s35, v160\n"
        "v_lshlrev_b32 v180, 16, v161\n"
        "v_mfma_f32_16x16x32_bf16 a[96:99], v[32:35], v[24:27], a[96:99]\n"
        "v_and_b32 v181, s35, v161\n"
        "v_lshlrev_b32 v182, 16, v162\n"
        "v_mfma_f32_16x16x32_bf16 a[100:103], v[36:39], v[24:27], a[100:103]\n"
        "v_and_b32 v183, s35, v162\n"
        "v_lshlrev_b32 v184, 16, v163\n"
        "v_mfma_f32_16x16x32_bf16 a[100:103], v[68:71], v[28:31], a[100:103]\n"
        "v_and_b32 v185, s35, v163\n"
        "v_lshlrev_b32 v186, 16, v164\n"
        "v_mfma_f32_16x16x32_bf16 a[68:71], v[68:71], v[20:23], a[68:71]\n"
        "v_and_b32 v187, s35, v164\n"
        "v_lshlrev_b32 v188, 16, v165\n"
        "v_mfma_f32_16x16x32_bf16 a[68:71], v[36:39], v[16:19], a[68:71]\n"
        "v_and_b32 v189, s35, v165\n"
        "v_lshlrev_b32 v192, 16, v142\n"
        "v_mfma_f32_16x16x32_bf16 a[72:75], v[40:43], v[16:19], a[72:75]\n"
        "v_and_b32 v193, s35, v142\n"
        "v_mul_f32 v192, v192, v174\n"
        "v_mfma_f32_16x16x32_bf16 a[72:75], v[72:75], v[20:23], a[72:75]\n"
        "v_mul_f32 v193, v193, v175\n"
        "v_cvt_pk_bf16_f32 v192, 0, v192\n"
        "v_mfma_f32_16x16x32_bf16 a[104:107], v[72:75], v[28:31], a[104:107]\n"
        "v_cvt_pk_bf16_f32 v193, 0, v193\n"
        "v_add_f32 v190, v192, v193\n"
        "v_mfma_f32_16x16x32_bf16 a[104:107], v[40:43], v[24:27], a[104:107]\n"
        "v_lshlrev_b32 v194, 16, v143\n"
        "v_and_b32 v195, s35, v143\n"
        "v_mfma_f32_16x16x32_bf16 a[108:111], v[44:47], v[24:27], a[108:111]\n"
        "v_mul_f32 v194, v194, v176\n"
        "v_mul_f32 v195, v195, v177\n"
        "v_mfma_f32_16x16x32_bf16 a[108:111], v[76:79], v[28:31], a[108:111]\n"
        "v_cvt_pk_bf16_f32 v194, 0, v194\n"
        "v_cvt_pk_bf16_f32 v195, 0, v195\n"
        "v_mfma_f32_16x16x32_bf16 a[76:79], v[76:79], v[20:23], a[76:79]\n"
        "v_add_f32 v194, v194, v195\n"
        "v_add_f32 v190, v190, v194\n"
        "v_mfma_f32_16x16x32_bf16 a[76:79], v[44:47], v[16:19], a[76:79]\n"
        "v_lshlrev_b32 v192, 16, v144\n"
        "v_and_b32 v193, s35, v144\n"
        "v_mfma_f32_16x16x32_bf16 a[80:83], v[16:19], v[48:51], a[80:83]\n"
        "LHA3_%=:\n"
        "s_cbranch_execz LHA7_%=\n"
        "v_mul_f32 v192, v192, v178\n"
        "v_mul_f32 v193, v193, v179\n"
        "v_mfma_f32_16x16x32_bf16 a[80:83], v[20:23], v[80:83], a[80:83]\n"
        "v_cvt_pk_bf16_f32 v192, 0, v192\n"
        "v_cvt_pk_bf16_f32 v193, 0, v193\n"
        "v_mfma_f32_16x16x32_bf16 a[112:115], v[28:31], v[80:83], a[112:115]\n"
        "v_add_f32 v192, v192, v193\n"
        "v_add_f32 v190, v190, v192\n"
        "v_mfma_f32_16x16x32_bf16 a[112:115], v[24:27], v[48:51], a[112:115]\n"
        "v_lshlrev_b32 v194, 16, v145\n"
        "v_and_b32 v195, s35, v145\n"
        "v_mfma_f32_16x16x32_bf16 a[116:119], v[24:27], v[52:55], a[116:119]\n"
        "v_mul_f32 v194, v194, v180\n"
        "v_mul_f32 v195, v195, v181\n"
        "v_mfma_f32_16x16x32_bf16 a[116:119], v[28:31], v[84:87], a[116:119]\n"
        "v_cvt_pk_bf16_f32 v194, 0, v194\n"
        "v_cvt_pk_bf16_f32 v195, 0, v195\n"
        "v_mfma_f32_16x16x32_bf16 a[84:87], v[20:23], v[84:87], a[84:87]\n"
        "v_add_f32 v194, v194, v195\n"
        "v_add_f32 v190, v190, v194\n"
        "v_mfma_f32_16x16x32_bf16 a[84:87], v[16:19], v[52:55], a[84:87]\n"
        "s_mov_b32 s88, 0x3e0293ee\n"
        "v_lshlrev_b32 v192, 16, v146\n"
        "v_and_b32 v193, s35, v146\n"
        "v_mfma_f32_16x16x32_bf16 a[88:91], v[16:19], v[56:59], a[88:91]\n"
        "s_sub_u32 s75, s45, s44\n"
        "v_mul_f32 v192, v192, v182\n"
        "v_mul_f32 v193, v193, v183\n"
        "v_mfma_f32_16x16x32_bf16 a[88:91], v[20:23], v[88:91], a[88:91]\n"
        "s_lshl_b32 s75, s75, 15\n"
        "v_cvt_pk_bf16_f32 v192, 0, v192\n"
        "v_cvt_pk_bf16_f32 v193, 0, v193\n"
        "v_mfma_f32_16x16x32_bf16 a[120:123], v[28:31], v[88:91], a[120:123]\n"
        "s_lshr_b32 s50, s40, 1\n"
        "v_add_f32 v192, v192, v193\n"
        "v_add_f32 v190, v190, v192\n"
        "v_mfma_f32_16x16x32_bf16 a[120:123], v[24:27], v[56:59], a[120:123]\n"
        "s_lshl1_add_u32 s50, s44, s50\n"
        "v_lshlrev_b32 v194, 16, v147\n"
        "v_and_b32 v195, s35, v147\n"
        "v_mfma_f32_16x16x32_bf16 a[124:127], v[24:27], v[60:63], a[124:127]\n"
        "ds_read_b128 v[24:27], v224 offset:6144\n"
        "s_lshl_b32 s50, s50, 14\n"
        "v_mul_f32 v194, v194, v184\n"
        "v_mfma_f32_16x16x32_bf16 a[124:127], v[28:31], v[92:95], a[124:127]\n"
        "ds_read_b128 v[28:31], v225 offset:6144\n"
        "s_mul_i32 s51, s43, 529408\n"
        "v_mul_f32 v195, v195, v185\n"
        "v_mfma_f32_16x16x32_bf16 a[92:95], v[20:23], v[92:95], a[92:95]\n"
        "ds_read_b128 v[20:23], v225 offset:4096\n"
        "s_add_u32 s50, s50, s51\n"
        "v_cvt_pk_bf16_f32 v194, 0, v194\n"
        "v_mfma_f32_16x16x32_bf16 a[92:95], v[16:19], v[60:63], a[92:95]\n"
        "ds_read_b128 v[16:19], v224 offset:4096\n"
        "s_and_b32 s51, s40, 1\n"
        "s_waitcnt lgkmcnt(4)\n"
        "v_mfma_f32_16x16x32_bf16 a[128:131], v[32:35], v[0:3], a[128:131]\n"
        "s_lshl_b32 s51, s51, 11\n"
        "v_cvt_pk_bf16_f32 v195, 0, v195\n"
        "v_add_f32 v194, v194, v195\n"
        "v_mfma_f32_16x16x32_bf16 a[128:131], v[64:67], v[4:7], a[128:131]\n"
        "s_add_u32 s52, s50, s51\n"
        "v_add_f32 v190, v190, v194\n"
        "v_lshlrev_b32 v192, 16, v148\n"
        "v_mfma_f32_16x16x32_bf16 a[160:163], v[64:67], v[12:15], a[160:163]\n"
        "v_and_b32 v193, s35, v148\n"
        "v_mul_f32 v192, v192, v186\n"
        "v_mfma_f32_16x16x32_bf16 a[160:163], v[32:35], v[8:11], a[160:163]\n"
        "v_mul_f32 v193, v193, v187\n"
        "v_cvt_pk_bf16_f32 v192, 0, v192\n"
        "v_mfma_f32_16x16x32_bf16 a[164:167], v[36:39], v[8:11], a[164:167]\n"
        "v_cvt_pk_bf16_f32 v193, 0, v193\n"
        "v_add_f32 v192, v192, v193\n"
        "v_mfma_f32_16x16x32_bf16 a[164:167], v[68:71], v[12:15], a[164:167]\n"
        "v_add_f32 v190, v190, v192\n"
        "v_lshlrev_b32 v194, 16, v149\n"
        "v_mfma_f32_16x16x32_bf16 a[132:135], v[68:71], v[4:7], a[132:135]\n"
        "v_and_b32 v195, s35, v149\n"
        "v_mul_f32 v194, v194, v188\n"
        "v_mfma_f32_16x16x32_bf16 a[132:135], v[36:39], v[0:3], a[132:135]\n"
        "v_mul_f32 v195, v195, v189\n"
        "v_cvt_pk_bf16_f32 v194, 0, v194\n"
        "v_mfma_f32_16x16x32_bf16 a[136:139], v[40:43], v[0:3], a[136:139]\n"
        "v_cvt_pk_bf16_f32 v195, 0, v195\n"
        "v_add_f32 v194, v194, v195\n"
        "v_mfma_f32_16x16x32_bf16 a[136:139], v[72:75], v[4:7], a[136:139]\n"
        "v_add_f32 v190, v190, v194\n"
        "v_lshlrev_b32 v192, 16, v150\n"
        "v_mfma_f32_16x16x32_bf16 a[168:171], v[72:75], v[12:15], a[168:171]\n"
        "v_and_b32 v193, s35, v150\n"
        "v_mul_f32 v192, v192, v174\n"
        "v_mfma_f32_16x16x32_bf16 a[168:171], v[40:43], v[8:11], a[168:171]\n"
        "v_mul_f32 v193, v193, v175\n"
        "v_cvt_pk_bf16_f32 v192, 0, v192\n"
        "v_mfma_f32_16x16x32_bf16 a[172:175], v[44:47], v[8:11], a[172:175]\n"
        "v_cvt_pk_bf16_f32 v193, 0, v193\n"
        "v_add_f32 v191, v192, v193\n"
        "v_mfma_f32_16x16x32_bf16 a[172:175], v[76:79], v[12:15], a[172:175]\n"
        "v_lshlrev_b32 v194, 16, v151\n"
        "v_and_b32 v195, s35, v151\n"
        "v_mfma_f32_16x16x32_bf16 a[140:143], v[76:79], v[4:7], a[140:143]\n"
        "v_mul_f32 v194, v194, v176\n"
        "v_mul_f32 v195, v195, v177\n"
        "v_mfma_f32_16x16x32_bf16 a[140:143], v[44:47], v[0:3], a[140:143]\n"
        "v_cvt_pk_bf16_f32 v194, 0, v194\n"
        "v_cvt_pk_bf16_f32 v195, 0, v195\n"
        "v_mfma_f32_16x16x32_bf16 a[144:147], v[0:3], v[48:51], a[144:147]\n"
        "v_add_f32 v194, v194, v195\n"
        "v_add_f32 v191, v191, v194\n"
        "v_mfma_f32_16x16x32_bf16 a[144:147], v[4:7], v[80:83], a[144:147]\n"
        "v_lshlrev_b32 v192, 16, v152\n"
        "v_and_b32 v193, s35, v152\n"
        "v_mfma_f32_16x16x32_bf16 a[176:179], v[12:15], v[80:83], a[176:179]\n"
        "v_mul_f32 v192, v192, v178\n"
        "v_mul_f32 v193, v193, v179\n"
        "v_mfma_f32_16x16x32_bf16 a[176:179], v[8:11], v[48:51], a[176:179]\n"
        "v_cvt_pk_bf16_f32 v192, 0, v192\n"
        "v_cvt_pk_bf16_f32 v193, 0, v193\n"
        "v_mfma_f32_16x16x32_bf16 a[180:183], v[8:11], v[52:55], a[180:183]\n"
        "v_add_f32 v192, v192, v193\n"
        "v_add_f32 v191, v191, v192\n"
        "v_mfma_f32_16x16x32_bf16 a[180:183], v[12:15], v[84:87], a[180:183]\n"
        "v_lshlrev_b32 v194, 16, v153\n"
        "v_and_b32 v195, s35, v153\n"
        "v_mfma_f32_16x16x32_bf16 a[148:151], v[4:7], v[84:87], a[148:151]\n"
        "v_mul_f32 v194, v194, v180\n"
        "v_mul_f32 v195, v195, v181\n"
        "v_mfma_f32_16x16x32_bf16 a[148:151], v[0:3], v[52:55], a[148:151]\n"
        "global_load_dword v253, v173, s[82:83] offset:0\n"
        "v_cvt_pk_bf16_f32 v194, 0, v194\n"
        "v_mfma_f32_16x16x32_bf16 a[152:155], v[0:3], v[56:59], a[152:155]\n"
        "v_cvt_pk_bf16_f32 v195, 0, v195\n"
        "v_add_f32 v194, v194, v195\n"
        "v_mfma_f32_16x16x32_bf16 a[152:155], v[4:7], v[88:91], a[152:155]\n"
        "v_add_f32 v191, v191, v194\n"
        "v_lshlrev_b32 v192, 16, v154\n"
        "v_mfma_f32_16x16x32_bf16 a[184:187], v[12:15], v[88:91], a[184:187]\n"
        "v_and_b32 v193, s35, v154\n"
        "v_mul_f32 v192, v192, v182\n"
        "v_mfma_f32_16x16x32_bf16 a[184:187], v[8:11], v[56:59], a[184:187]\n"
        "s_waitcnt vmcnt(11) lgkmcnt(0)\n"
        "v_mul_f32 v193, v193, v183\n"
        "v_cvt_pk_bf16_f32 v192, 0, v192\n"
        "v_mfma_f32_16x16x32_bf16 a[188:191], v[8:11], v[60:63], a[188:191]\n"
        "s_barrier\n"
        "v_mfma_f32_16x16x32_bf16 a[188:191], v[12:15], v[92:95], a[188:191]\n"
        "global_load_lds_dwordx4 v242, s[64:65] offset:0\n"
        "ds_read_b128 v[8:11], v224 offset:51200\n"
        "v_mfma_f32_16x16x32_bf16 a[156:159], v[4:7], v[92:95], a[156:159]\n"
        "ds_read_b128 v[4:7], v225 offset:49152\n"
        "v_cvt_pk_bf16_f32 v193, 0, v193\n"
        "v_mfma_f32_16x16x32_bf16 a[156:159], v[0:3], v[60:63], a[156:159]\n"
        "ds_read_b128 v[0:3], v224 offset:49152\n"
        "v_add_f32 v192, v192, v193\n"
        "v_mfma_f32_16x16x32_bf16 a[192:195], v[32:35], v[16:19], a[192:195]\n"
        "ds_read_b128 v[12:15], v225 offset:51200\n"
        "v_add_f32 v191, v191, v192\n"
        "v_mfma_f32_16x16x32_bf16 a[192:195], v[64:67], v[20:23], a[192:195]\n"
        "global_load_lds_dwordx4 v243, s[64:65] offset:1024\n"
        "v_lshlrev_b32 v194, 16, v155\n"
        "v_mfma_f32_16x16x32_bf16 a[224:227], v[64:67], v[28:31], a[224:227]\n"
        "ds_read_b64_tr_b16 v[64:65], v226 offset:40960\n"
        "v_and_b32 v195, s35, v155\n"
        "v_mfma_f32_16x16x32_bf16 a[224:227], v[32:35], v[24:27], a[224:227]\n"
        "ds_read_b64_tr_b16 v[32:33], v226 offset:32768\n"
        "s_add_u32 s97, s52, 0x3000\n"
        "v_mul_f32 v194, v194, v184\n"
        "v_mfma_f32_16x16x32_bf16 a[228:231], v[36:39], v[24:27], a[228:231]\n"
        "ds_read_b64_tr_b16 v[34:35], v227 offset:32768\n"
        "v_mul_f32 v195, v195, v185\n"
        "v_mfma_f32_16x16x32_bf16 a[228:231], v[68:71], v[28:31], a[228:231]\n"
        "ds_read_b64_tr_b16 v[66:67], v227 offset:40960\n"
        "global_load_lds_dwordx4 v244, s[64:65] offset:2048\n"
        "v_mfma_f32_16x16x32_bf16 a[196:199], v[68:71], v[20:23], a[196:199]\n"
        "ds_read_b64_tr_b16 v[68:69], v228 offset:40960\n"
        "v_cvt_pk_bf16_f32 v194, 0, v194\n"
        "v_mfma_f32_16x16x32_bf16 a[196:199], v[36:39], v[16:19], a[196:199]\n"
        "ds_read_b64_tr_b16 v[36:37], v228 offset:32768\n"
        "v_cvt_pk_bf16_f32 v195, 0, v195\n"
        "v_mfma_f32_16x16x32_bf16 a[200:203], v[40:43], v[16:19], a[200:203]\n"
        "ds_read_b64_tr_b16 v[38:39], v229 offset:32768\n"
        "v_add_f32 v194, v194, v195\n"
        "v_mfma_f32_16x16x32_bf16 a[200:203], v[72:75], v[20:23], a[200:203]\n"
        "ds_read_b64_tr_b16 v[70:71], v229 offset:40960\n"
        "global_load_lds_dwordx4 v245, s[64:65] offset:3072\n"
        "s_add_u32 m0, s72, 0x1000\n"
        "v_mfma_f32_16x16x32_bf16 a[232:235], v[72:75], v[28:31], a[232:235]\n"
        "ds_read_b64_tr_b16 v[72:73], v230 offset:40960\n"
        "v_add_f32 v191, v191, v194\n"
        "v_mfma_f32_16x16x32_bf16 a[232:235], v[40:43], v[24:27], a[232:235]\n"
        "ds_read_b64_tr_b16 v[40:41], v230 offset:32768\n"
        "v_lshlrev_b32 v192, 16, v156\n"
        "v_mfma_f32_16x16x32_bf16 a[236:239], v[44:47], v[24:27], a[236:239]\n"
        "ds_read_b64_tr_b16 v[42:43], v231 offset:32768\n"
        "v_and_b32 v193, s35, v156\n"
        "v_mfma_f32_16x16x32_bf16 a[236:239], v[76:79], v[28:31], a[236:239]\n"
        "ds_read_b64_tr_b16 v[74:75], v231 offset:40960\n"
        "global_load_lds_dwordx4 v242, s[66:67] offset:0\n"
        "v_mfma_f32_16x16x32_bf16 a[204:207], v[76:79], v[20:23], a[204:207]\n"
        "ds_read_b64_tr_b16 v[76:77], v232 offset:40960\n"
        "v_mul_f32 v192, v192, v186\n"
        "v_mfma_f32_16x16x32_bf16 a[204:207], v[44:47], v[16:19], a[204:207]\n"
        "ds_read_b64_tr_b16 v[44:45], v232 offset:32768\n"
        "v_mul_f32 v193, v193, v187\n"
        "v_mfma_f32_16x16x32_bf16 a[208:211], v[16:19], v[48:51], a[208:211]\n"
        "ds_read_b64_tr_b16 v[46:47], v233 offset:32768\n"
        "v_cvt_pk_bf16_f32 v192, 0, v192\n"
        "v_mfma_f32_16x16x32_bf16 a[208:211], v[20:23], v[80:83], a[208:211]\n"
        "ds_read_b64_tr_b16 v[78:79], v233 offset:40960\n"
        "global_load_lds_dwordx4 v243, s[66:67] offset:1024\n"
        "v_mfma_f32_16x16x32_bf16 a[240:243], v[28:31], v[80:83], a[240:243]\n"
        "ds_read_b64_tr_b16 v[80:81], v226 offset:57344\n"
        "v_cvt_pk_bf16_f32 v193, 0, v193\n"
        "v_mfma_f32_16x16x32_bf16 a[240:243], v[24:27], v[48:51], a[240:243]\n"
        "ds_read_b64_tr_b16 v[48:49], v226 offset:49152\n"
        "v_add_f32 v192, v192, v193\n"
        "v_mfma_f32_16x16x32_bf16 a[244:247], v[24:27], v[52:55], a[244:247]\n"
        "ds_read_b64_tr_b16 v[50:51], v227 offset:49152\n"
        "v_add_f32 v191, v191, v192\n"
        "v_mfma_f32_16x16x32_bf16 a[244:247], v[28:31], v[84:87], a[244:247]\n"
        "ds_read_b64_tr_b16 v[82:83], v227 offset:57344\n"
        "global_load_lds_dwordx4 v244, s[66:67] offset:2048\n"
        "v_mfma_f32_16x16x32_bf16 a[212:215], v[20:23], v[84:87], a[212:215]\n"
        "ds_read_b64_tr_b16 v[84:85], v228 offset:57344\n"
        "v_lshlrev_b32 v194, 16, v157\n"
        "v_mfma_f32_16x16x32_bf16 a[212:215], v[16:19], v[52:55], a[212:215]\n"
        "ds_read_b64_tr_b16 v[52:53], v228 offset:49152\n"
        "v_and_b32 v195, s35, v157\n"
        "v_mfma_f32_16x16x32_bf16 a[216:219], v[16:19], v[56:59], a[216:219]\n"
        "ds_read_b64_tr_b16 v[54:55], v229 offset:49152\n"
        "v_mul_f32 v194, v194, v188\n"
        "v_mfma_f32_16x16x32_bf16 a[216:219], v[20:23], v[88:91], a[216:219]\n"
        "ds_read_b64_tr_b16 v[86:87], v229 offset:57344\n"
        "global_load_lds_dwordx4 v245, s[66:67] offset:3072\n"
        "s_add_u32 m0, s72, 0x10000\n"
        "v_mfma_f32_16x16x32_bf16 a[248:251], v[28:31], v[88:91], a[248:251]\n"
        "ds_read_b64_tr_b16 v[88:89], v230 offset:57344\n"
        "v_mul_f32 v195, v195, v189\n"
        "v_mfma_f32_16x16x32_bf16 a[248:251], v[24:27], v[56:59], a[248:251]\n"
        "ds_read_b64_tr_b16 v[56:57], v230 offset:49152\n"
        "v_cvt_pk_bf16_f32 v194, 0, v194\n"
        "v_mfma_f32_16x16x32_bf16 a[252:255], v[24:27], v[60:63], a[252:255]\n"
        "ds_read_b64_tr_b16 v[58:59], v231 offset:49152\n"
        "v_cvt_pk_bf16_f32 v195, 0, v195\n"
        "v_mfma_f32_16x16x32_bf16 a[252:255], v[28:31], v[92:95], a[252:255]\n"
        "ds_read_b64_tr_b16 v[90:91], v231 offset:57344\n"
        "global_load_lds_dwordx4 v242, s[86:87] offset:0\n"
        "v_mfma_f32_16x16x32_bf16 a[220:223], v[20:23], v[92:95], a[220:223]\n"
        "ds_read_b64_tr_b16 v[92:93], v232 offset:57344\n"
        "v_add_f32 v194, v194, v195\n"
        "v_mfma_f32_16x16x32_bf16 a[220:223], v[16:19], v[60:63], a[220:223]\n"
        "ds_read_b64_tr_b16 v[60:61], v232 offset:49152\n"
        "s_waitcnt lgkmcnt(8)\n"
        "v_mfma_f32_16x16x32_bf16 a[0:3], v[32:35], v[0:3], a[0:3]\n"
        "ds_read_b64_tr_b16 v[62:63], v233 offset:49152\n"
        "v_add_f32 v191, v191, v194\n"
        "v_mfma_f32_16x16x32_bf16 a[0:3], v[64:67], v[4:7], a[0:3]\n"
        "ds_read_b64_tr_b16 v[94:95], v233 offset:57344\n"
        "global_load_lds_dwordx4 v243, s[86:87] offset:1024\n"
        "v_mfma_f32_16x16x32_bf16 a[32:35], v[64:67], v[12:15], a[32:35]\n"
        "ds_read_b128 v[16:19], v224 offset:53248\n"
        "v_add_f32_dpp v190, v190, v190 row_shr:1 row_mask:0xf bank_mask:0xf bound_ctrl:1\n"
        "v_mfma_f32_16x16x32_bf16 a[32:35], v[32:35], v[8:11], a[32:35]\n"
        "ds_read_b128 v[20:23], v225 offset:53248\n"
        "v_add_f32_dpp v191, v191, v191 row_shr:1 row_mask:0xf bank_mask:0xf bound_ctrl:1\n"
        "v_mfma_f32_16x16x32_bf16 a[36:39], v[36:39], v[8:11], a[36:39]\n"
        "ds_read_b128 v[24:27], v224 offset:55296\n"
        "v_add_f32_dpp v190, v190, v190 row_shr:2 row_mask:0xf bank_mask:0xf bound_ctrl:1\n"
        "v_mfma_f32_16x16x32_bf16 a[36:39], v[68:71], v[12:15], a[36:39]\n"
        "ds_read_b128 v[28:31], v225 offset:55296\n"
        "global_load_lds_dwordx4 v244, s[86:87] offset:2048\n"
        "v_mfma_f32_16x16x32_bf16 a[4:7], v[68:71], v[4:7], a[4:7]\n"
        "s_add_u32 s70, s38, s97\n"
        "s_addc_u32 s71, s39, 0\n"
        "v_add_f32_dpp v191, v191, v191 row_shr:2 row_mask:0xf bank_mask:0xf bound_ctrl:1\n"
        "v_mfma_f32_16x16x32_bf16 a[4:7], v[36:39], v[0:3], a[4:7]\n"
        "v_add_f32_dpp v190, v190, v190 row_shr:4 row_mask:0xf bank_mask:0xf bound_ctrl:1\n"
        "v_add_f32_dpp v191, v191, v191 row_shr:4 row_mask:0xf bank_mask:0xf bound_ctrl:1\n"
        "v_mfma_f32_16x16x32_bf16 a[8:11], v[40:43], v[0:3], a[8:11]\n"
        "v_add_f32_dpp v190, v190, v190 row_shr:8 row_mask:0xf bank_mask:0xf bound_ctrl:1\n"
        "v_add_f32_dpp v191, v191, v191 row_shr:8 row_mask:0xf bank_mask:0xf bound_ctrl:1\n"
        "v_mfma_f32_16x16x32_bf16 a[8:11], v[72:75], v[4:7], a[8:11]\n"
        "global_load_lds_dwordx4 v245, s[86:87] offset:3072\n"
        "s_add_u32 m0, s72, 0x11000\n"
        "v_mfma_f32_16x16x32_bf16 a[40:43], v[72:75], v[12:15], a[40:43]\n"
        "s_add_u32 s86, s66, 0x80\n"
        "s_addc_u32 s87, s67, 0\n"
        "v_add_f32_dpp v190, v190, v190 row_bcast:15 row_mask:0xa bank_mask:0xf bound_ctrl:1\n"
        "v_mfma_f32_16x16x32_bf16 a[40:43], v[40:43], v[8:11], a[40:43]\n"
        "v_add_f32_dpp v191, v191, v191 row_bcast:15 row_mask:0xa bank_mask:0xf bound_ctrl:1\n"
        "v_add_f32_dpp v190, v190, v190 row_bcast:31 row_mask:0xc bank_mask:0xf bound_ctrl:1\n"
        "v_mfma_f32_16x16x32_bf16 a[44:47], v[44:47], v[8:11], a[44:47]\n"
        "v_add_f32_dpp v191, v191, v191 row_bcast:31 row_mask:0xc bank_mask:0xf bound_ctrl:1\n"
        "v_cvt_pk_bf16_f32 v190, 0, v190\n"
        "v_mfma_f32_16x16x32_bf16 a[44:47], v[76:79], v[12:15], a[44:47]\n"
        "global_load_lds_dwordx4 v242, s[86:87] offset:0\n"
        "v_mfma_f32_16x16x32_bf16 a[12:15], v[76:79], v[4:7], a[12:15]\n"
        "v_mul_f32 v190, 0xbfb8aa3b, v190\n"
        "v_lshrrev_b32 v192, 2, v254\n"
        "v_mfma_f32_16x16x32_bf16 a[12:15], v[44:47], v[0:3], a[12:15]\n"
        "v_exp_f32 v190, v190\n"
        "v_mfma_f32_16x16x32_bf16 a[16:19], v[0:3], v[48:51], a[16:19]\n"
        "s_waitcnt lgkmcnt(0)\n"
        "v_add_f32 v190, 1.0, v190\n"
        "v_add_u32 v192, s80, v192\n"
        "v_mfma_f32_16x16x32_bf16 a[16:19], v[4:7], v[80:83], a[16:19]\n"
        "global_load_lds_dwordx4 v243, s[86:87] offset:1024\n"
        "v_mfma_f32_16x16x32_bf16 a[48:51], v[12:15], v[80:83], a[48:51]\n"
        "v_rcp_f32 v190, v190\n"
        "v_mfma_f32_16x16x32_bf16 a[48:51], v[8:11], v[48:51], a[48:51]\n"
        "v_cvt_pk_bf16_f32 v190, 0, v190\n"
        "v_cvt_pk_bf16_f32 v191, 0, v191\n"
        "v_mfma_f32_16x16x32_bf16 a[52:55], v[8:11], v[52:55], a[52:55]\n"
        "v_readlane_b32 s84, v190, 63\n"
        "v_mul_f32 v191, 0xbfb8aa3b, v191\n"
        "v_mfma_f32_16x16x32_bf16 a[52:55], v[12:15], v[84:87], a[52:55]\n"
        "global_load_lds_dwordx4 v244, s[86:87] offset:2048\n"
        "v_mfma_f32_16x16x32_bf16 a[20:23], v[4:7], v[84:87], a[20:23]\n"
        "v_mov_b32 v190, s84\n"
        "v_lshrrev_b32 v193, 2, v254\n"
        "v_mfma_f32_16x16x32_bf16 a[20:23], v[0:3], v[52:55], a[20:23]\n"
        "v_exp_f32 v191, v191\n"
        "v_mfma_f32_16x16x32_bf16 a[24:27], v[0:3], v[56:59], a[24:27]\n"
        "v_add_f32 v191, 1.0, v191\n"
        "v_add_u32 v193, s81, v193\n"
        "v_mfma_f32_16x16x32_bf16 a[24:27], v[4:7], v[88:91], a[24:27]\n"
        "global_load_lds_dwordx4 v245, s[86:87] offset:3072\n"
        "s_add_u32 m0, s73, 0x1c000\n"
        "v_mfma_f32_16x16x32_bf16 a[56:59], v[12:15], v[88:91], a[56:59]\n"
        "v_rcp_f32 v191, v191\n"
        "v_mfma_f32_16x16x32_bf16 a[56:59], v[8:11], v[56:59], a[56:59]\n"
        "s_add_u32 s86, s68, 0x100000\n"
        "s_addc_u32 s87, s69, 0\n"
        "v_cvt_pk_bf16_f32 v191, 0, v191\n"
        "v_mfma_f32_16x16x32_bf16 a[60:63], v[8:11], v[60:63], a[60:63]\n"
        "ds_read_b128 v[8:11], v224 offset:34816\n"
        "v_readlane_b32 s85, v191, 63\n"
        "v_mfma_f32_16x16x32_bf16 a[60:63], v[12:15], v[92:95], a[60:63]\n"
        "ds_read_b128 v[12:15], v225 offset:34816\n"
        "v_mov_b32 v191, s85\n"
        "v_mfma_f32_16x16x32_bf16 a[28:31], v[4:7], v[92:95], a[28:31]\n"
        "ds_read_b128 v[4:7], v225 offset:32768\n"
        "v_add_u32 v226, s53, v226\n"
        "v_mfma_f32_16x16x32_bf16 a[28:31], v[0:3], v[60:63], a[28:31]\n"
        "ds_read_b128 v[0:3], v224 offset:32768\n"
        "v_add_u32 v227, s53, v227\n"
        "v_mfma_f32_16x16x32_bf16 a[64:67], v[32:35], v[16:19], a[64:67]\n"
        "s_mov_b64 exec, 0xffff\n"
        "global_store_dword v192, v190, s[76:77] sc1\n"
        "s_mov_b64 exec, -1\n"
        "v_mfma_f32_16x16x32_bf16 a[64:67], v[64:67], v[20:23], a[64:67]\n"
        "s_mov_b64 exec, 0xffff\n"
        "global_store_dword v193, v191, s[76:77] sc1\n"
        "s_mov_b64 exec, -1\n"
        "v_mfma_f32_16x16x32_bf16 a[96:99], v[64:67], v[28:31], a[96:99]\n"
        "v_add_u32 v228, s53, v228\n"
        "v_add_u32 v229, s53, v229\n"
        "v_mfma_f32_16x16x32_bf16 a[96:99], v[32:35], v[24:27], a[96:99]\n"
        "v_add_u32 v230, s53, v230\n"
        "v_add_u32 v231, s53, v231\n"
        "v_mfma_f32_16x16x32_bf16 a[100:103], v[36:39], v[24:27], a[100:103]\n"
        "v_add_u32 v232, s53, v232\n"
        "v_add_u32 v233, s53, v233\n"
        "v_mfma_f32_16x16x32_bf16 a[100:103], v[68:71], v[28:31], a[100:103]\n"
        "s_add_u32 s53, s52, s51\n"
        "s_add_u32 s53, s53, 0x1100000\n"
        "v_mfma_f32_16x16x32_bf16 a[68:71], v[68:71], v[20:23], a[68:71]\n"
        "v_mfma_f32_16x16x32_bf16 a[68:71], v[36:39], v[16:19], a[68:71]\n"
        "v_mfma_f32_16x16x32_bf16 a[72:75], v[40:43], v[16:19], a[72:75]\n"
        "v_mfma_f32_16x16x32_bf16 a[72:75], v[72:75], v[20:23], a[72:75]\n"
        "v_mfma_f32_16x16x32_bf16 a[104:107], v[72:75], v[28:31], a[104:107]\n"
        "v_mfma_f32_16x16x32_bf16 a[104:107], v[40:43], v[24:27], a[104:107]\n"
        "v_mfma_f32_16x16x32_bf16 a[108:111], v[44:47], v[24:27], a[108:111]\n"
        "v_mfma_f32_16x16x32_bf16 a[108:111], v[76:79], v[28:31], a[108:111]\n"
        "v_mfma_f32_16x16x32_bf16 a[76:79], v[76:79], v[20:23], a[76:79]\n"
        "v_mfma_f32_16x16x32_bf16 a[76:79], v[44:47], v[16:19], a[76:79]\n"
        "v_mfma_f32_16x16x32_bf16 a[80:83], v[16:19], v[48:51], a[80:83]\n"
        "v_mfma_f32_16x16x32_bf16 a[80:83], v[20:23], v[80:83], a[80:83]\n"
        "v_mfma_f32_16x16x32_bf16 a[112:115], v[28:31], v[80:83], a[112:115]\n"
        "v_mfma_f32_16x16x32_bf16 a[112:115], v[24:27], v[48:51], a[112:115]\n"
        "v_mfma_f32_16x16x32_bf16 a[116:119], v[24:27], v[52:55], a[116:119]\n"
        "v_mfma_f32_16x16x32_bf16 a[116:119], v[28:31], v[84:87], a[116:119]\n"
        "v_mfma_f32_16x16x32_bf16 a[84:87], v[20:23], v[84:87], a[84:87]\n"
        "v_mfma_f32_16x16x32_bf16 a[84:87], v[16:19], v[52:55], a[84:87]\n"
        "v_mfma_f32_16x16x32_bf16 a[88:91], v[16:19], v[56:59], a[88:91]\n"
        "v_mfma_f32_16x16x32_bf16 a[88:91], v[20:23], v[88:91], a[88:91]\n"
        "v_mfma_f32_16x16x32_bf16 a[120:123], v[28:31], v[88:91], a[120:123]\n"
        "v_mfma_f32_16x16x32_bf16 a[120:123], v[24:27], v[56:59], a[120:123]\n"
        "v_mfma_f32_16x16x32_bf16 a[124:127], v[24:27], v[60:63], a[124:127]\n"
        "ds_read_b128 v[24:27], v224 offset:38912\n"
        "s_waitcnt lgkmcnt(1)\n"
        "v_mfma_f32_16x16x32_bf16 a[124:127], v[28:31], v[92:95], a[124:127]\n"
        "ds_read_b128 v[28:31], v225 offset:38912\n"
        "v_mfma_f32_16x16x32_bf16 a[92:95], v[20:23], v[92:95], a[92:95]\n"
        "ds_read_b128 v[20:23], v225 offset:36864\n"
        "v_mfma_f32_16x16x32_bf16 a[92:95], v[16:19], v[60:63], a[92:95]\n"
        "ds_read_b128 v[16:19], v224 offset:36864\n"
        "v_mfma_f32_16x16x32_bf16 a[128:131], v[32:35], v[0:3], a[128:131]\n"
        "v_mfma_f32_16x16x32_bf16 a[128:131], v[64:67], v[4:7], a[128:131]\n"
        "v_mfma_f32_16x16x32_bf16 a[160:163], v[64:67], v[12:15], a[160:163]\n"
        "v_mfma_f32_16x16x32_bf16 a[160:163], v[32:35], v[8:11], a[160:163]\n"
        "v_mfma_f32_16x16x32_bf16 a[164:167], v[36:39], v[8:11], a[164:167]\n"
        "v_mfma_f32_16x16x32_bf16 a[164:167], v[68:71], v[12:15], a[164:167]\n"
        "v_mfma_f32_16x16x32_bf16 a[132:135], v[68:71], v[4:7], a[132:135]\n"
        "v_mfma_f32_16x16x32_bf16 a[132:135], v[36:39], v[0:3], a[132:135]\n"
        "v_mfma_f32_16x16x32_bf16 a[136:139], v[40:43], v[0:3], a[136:139]\n"
        "v_mfma_f32_16x16x32_bf16 a[136:139], v[72:75], v[4:7], a[136:139]\n"
        "v_mfma_f32_16x16x32_bf16 a[168:171], v[72:75], v[12:15], a[168:171]\n"
        "v_mfma_f32_16x16x32_bf16 a[168:171], v[40:43], v[8:11], a[168:171]\n"
        "v_mfma_f32_16x16x32_bf16 a[172:175], v[44:47], v[8:11], a[172:175]\n"
        "v_mfma_f32_16x16x32_bf16 a[172:175], v[76:79], v[12:15], a[172:175]\n"
        "v_mfma_f32_16x16x32_bf16 a[140:143], v[76:79], v[4:7], a[140:143]\n"
        "v_mfma_f32_16x16x32_bf16 a[140:143], v[44:47], v[0:3], a[140:143]\n"
        "v_mfma_f32_16x16x32_bf16 a[144:147], v[0:3], v[48:51], a[144:147]\n"
        "v_mfma_f32_16x16x32_bf16 a[144:147], v[4:7], v[80:83], a[144:147]\n"
        "v_mfma_f32_16x16x32_bf16 a[176:179], v[12:15], v[80:83], a[176:179]\n"
        "v_mfma_f32_16x16x32_bf16 a[176:179], v[8:11], v[48:51], a[176:179]\n"
        "v_mfma_f32_16x16x32_bf16 a[180:183], v[8:11], v[52:55], a[180:183]\n"
        "v_mfma_f32_16x16x32_bf16 a[180:183], v[12:15], v[84:87], a[180:183]\n"
        "v_mfma_f32_16x16x32_bf16 a[148:151], v[4:7], v[84:87], a[148:151]\n"
        "v_mfma_f32_16x16x32_bf16 a[148:151], v[0:3], v[52:55], a[148:151]\n"
        "v_mfma_f32_16x16x32_bf16 a[152:155], v[0:3], v[56:59], a[152:155]\n"
        "v_mfma_f32_16x16x32_bf16 a[152:155], v[4:7], v[88:91], a[152:155]\n"
        "v_mfma_f32_16x16x32_bf16 a[184:187], v[12:15], v[88:91], a[184:187]\n"
        "v_mfma_f32_16x16x32_bf16 a[184:187], v[8:11], v[56:59], a[184:187]\n"
        "s_waitcnt vmcnt(10) lgkmcnt(0)\n"
        "v_mfma_f32_16x16x32_bf16 a[188:191], v[8:11], v[60:63], a[188:191]\n"
        "s_barrier\n"
        "v_mfma_f32_16x16x32_bf16 a[188:191], v[12:15], v[92:95], a[188:191]\n"
        "global_load_lds_dwordx4 v238, s[86:87] offset:0\n"
        "ds_read_b64_tr_b16 v[160:161], v226 offset:32768\n"
        "v_mfma_f32_16x16x32_bf16 a[156:159], v[4:7], v[92:95], a[156:159]\n"
        "ds_read_b64_tr_b16 v[162:163], v227 offset:32768\n"
        "v_mfma_f32_16x16x32_bf16 a[156:159], v[0:3], v[60:63], a[156:159]\n"
        "ds_read_b64_tr_b16 v[164:165], v226 offset:40960\n"
        "v_mfma_f32_16x16x32_bf16 a[192:195], v[32:35], v[16:19], a[192:195]\n"
        "ds_read_b64_tr_b16 v[166:167], v227 offset:40960\n"
        "v_mfma_f32_16x16x32_bf16 a[192:195], v[64:67], v[20:23], a[192:195]\n"
        "ds_read_b64_tr_b16 v[172:173], v228 offset:40960\n"
        "global_load_lds_dwordx4 v239, s[86:87] offset:1024\n"
        "v_mfma_f32_16x16x32_bf16 a[224:227], v[64:67], v[28:31], a[224:227]\n"
        "ds_read_b64_tr_b16 v[174:175], v229 offset:40960\n"
        "v_mfma_f32_16x16x32_bf16 a[224:227], v[32:35], v[24:27], a[224:227]\n"
        "ds_read_b64_tr_b16 v[168:169], v228 offset:32768\n"
        "v_mfma_f32_16x16x32_bf16 a[228:231], v[36:39], v[24:27], a[228:231]\n"
        "ds_read_b64_tr_b16 v[170:171], v229 offset:32768\n"
        "v_mfma_f32_16x16x32_bf16 a[228:231], v[68:71], v[28:31], a[228:231]\n"
        "ds_read_b128 v[192:195], v246 offset:2048\n"
        "global_load_lds_dwordx4 v240, s[86:87] offset:2048\n"
        "v_mfma_f32_16x16x32_bf16 a[196:199], v[68:71], v[20:23], a[196:199]\n"
        "ds_read_b128 v[208:211], v247 offset:2048\n"
        "v_accvgpr_read_b32 v177, a225\n"
        "v_mfma_f32_16x16x32_bf16 a[196:199], v[36:39], v[16:19], a[196:199]\n"
        "ds_read_b128 v[196:199], v246 offset:0\n"
        "v_accvgpr_read_b32 v178, a226\n"
        "v_mfma_f32_16x16x32_bf16 a[200:203], v[40:43], v[16:19], a[200:203]\n"
        "ds_read_b128 v[212:215], v247 offset:0\n"
        "v_accvgpr_read_b32 v179, a227\n"
        "v_mfma_f32_16x16x32_bf16 a[200:203], v[72:75], v[20:23], a[200:203]\n"
        "ds_read_b128 v[200:203], v246 offset:6144\n"
        "global_load_lds_dwordx4 v241, s[86:87] offset:3072\n"
        "s_add_u32 m0, s72, 0x8000\n"
        "v_mfma_f32_16x16x32_bf16 a[232:235], v[72:75], v[28:31], a[232:235]\n"
        "ds_read_b128 v[216:219], v247 offset:6144\n"
        "s_add_u32 s86, s64, 0x100\n"
        "s_addc_u32 s87, s65, 0\n"
        "v_mfma_f32_16x16x32_bf16 a[232:235], v[40:43], v[24:27], a[232:235]\n"
        "ds_read_b128 v[204:207], v246 offset:4096\n"
        "v_accvgpr_read_b32 v180, a228\n"
        "v_mfma_f32_16x16x32_bf16 a[236:239], v[44:47], v[24:27], a[236:239]\n"
        "ds_read_b128 v[220:223], v247 offset:4096\n"
        "v_accvgpr_read_b32 v181, a229\n"
        "v_mfma_f32_16x16x32_bf16 a[236:239], v[76:79], v[28:31], a[236:239]\n"
        "ds_read_b64_tr_b16 v[32:33], v230 offset:32768\n"
        "global_load_lds_dwordx4 v242, s[86:87] offset:0\n"
        "v_mfma_f32_16x16x32_bf16 a[204:207], v[76:79], v[20:23], a[204:207]\n"
        "ds_read_b64_tr_b16 v[34:35], v231 offset:32768\n"
        "v_accvgpr_read_b32 v182, a230\n"
        "v_mfma_f32_16x16x32_bf16 a[204:207], v[44:47], v[16:19], a[204:207]\n"
        "ds_read_b64_tr_b16 v[36:37], v230 offset:40960\n"
        "s_add_u32 s97, s52, 0x2000\n"
        "v_accvgpr_read_b32 v183, a231\n"
        "v_mfma_f32_16x16x32_bf16 a[208:211], v[16:19], v[48:51], a[208:211]\n"
        "ds_read_b64_tr_b16 v[38:39], v231 offset:40960\n"
        "v_mul_f32 v177, s88, v177\n"
        "v_mfma_f32_16x16x32_bf16 a[208:211], v[20:23], v[80:83], a[208:211]\n"
        "ds_read_b64_tr_b16 v[44:45], v232 offset:40960\n"
        "global_load_lds_dwordx4 v243, s[86:87] offset:1024\n"
        "v_mfma_f32_16x16x32_bf16 a[240:243], v[28:31], v[80:83], a[240:243]\n"
        "ds_read_b64_tr_b16 v[46:47], v233 offset:40960\n"
        "v_mul_f32 v178, s88, v178\n"
        "v_mfma_f32_16x16x32_bf16 a[240:243], v[24:27], v[48:51], a[240:243]\n"
        "ds_read_b64_tr_b16 v[40:41], v232 offset:32768\n"
        "v_mul_f32 v179, s88, v179\n"
        "v_mfma_f32_16x16x32_bf16 a[244:247], v[24:27], v[52:55], a[244:247]\n"
        "ds_read_b64_tr_b16 v[42:43], v233 offset:32768\n"
        "v_mul_f32 v180, s88, v180\n"
        "v_mfma_f32_16x16x32_bf16 a[244:247], v[28:31], v[84:87], a[244:247]\n"
        "global_load_lds_dwordx4 v244, s[86:87] offset:2048\n"
        "v_mfma_f32_16x16x32_bf16 a[212:215], v[20:23], v[84:87], a[212:215]\n"
        "ds_read_b64_tr_b16 v[48:49], v226 offset:33792\n"
        "v_mul_f32 v181, s88, v181\n"
        "v_mfma_f32_16x16x32_bf16 a[212:215], v[16:19], v[52:55], a[212:215]\n"
        "ds_read_b64_tr_b16 v[50:51], v227 offset:33792\n"
        "v_mul_f32 v182, s88, v182\n"
        "v_mfma_f32_16x16x32_bf16 a[216:219], v[16:19], v[56:59], a[216:219]\n"
        "ds_read_b64_tr_b16 v[52:53], v226 offset:41984\n"
        "v_mul_f32 v183, s88, v183\n"
        "v_mfma_f32_16x16x32_bf16 a[216:219], v[20:23], v[88:91], a[216:219]\n"
        "global_load_lds_dwordx4 v245, s[86:87] offset:3072\n"
        "s_add_u32 m0, s72, 0x9000\n"
        "v_mfma_f32_16x16x32_bf16 a[248:251], v[28:31], v[88:91], a[248:251]\n"
        "s_add_u32 s86, s66, 0x100\n"
        "s_addc_u32 s87, s67, 0\n"
        "v_accvgpr_read_b32 v185, a233\n"
        "v_mfma_f32_16x16x32_bf16 a[248:251], v[24:27], v[56:59], a[248:251]\n"
        "ds_read_b64_tr_b16 v[54:55], v227 offset:41984\n"
        "v_accvgpr_read_b32 v186, a234\n"
        "v_mfma_f32_16x16x32_bf16 a[252:255], v[24:27], v[60:63], a[252:255]\n"
        "ds_read_b64_tr_b16 v[56:57], v228 offset:33792\n"
        "v_accvgpr_read_b32 v187, a235\n"
        "v_mfma_f32_16x16x32_bf16 a[252:255], v[28:31], v[92:95], a[252:255]\n"
        "global_load_lds_dwordx4 v242, s[86:87] offset:0\n"
        "v_mfma_f32_16x16x32_bf16 a[220:223], v[20:23], v[92:95], a[220:223]\n"
        "ds_read_b64_tr_b16 v[58:59], v229 offset:33792\n"
        "v_accvgpr_read_b32 v188, a236\n"
        "v_mfma_f32_16x16x32_bf16 a[220:223], v[16:19], v[60:63], a[220:223]\n"
        "ds_read_b64_tr_b16 v[60:61], v228 offset:41984\n"
        "s_waitcnt lgkmcnt(5)\n"
        "v_mfma_f32_16x16x32_bf16 v[0:3], v[160:163], v[192:195], 0\n"
        "ds_read_b64_tr_b16 v[62:63], v229 offset:41984\n"
        "v_accvgpr_read_b32 v189, a237\n"
        "v_mfma_f32_16x16x32_bf16 v[0:3], v[164:167], v[208:211], v[0:3]\n"
        "global_load_lds_dwordx4 v243, s[86:87] offset:1024\n"
        "v_mfma_f32_16x16x32_bf16 v[4:7], v[172:175], v[208:211], 0\n"
        "v_accvgpr_read_b32 v190, a238\n"
        "v_accvgpr_read_b32 v191, a239\n"
        "v_mfma_f32_16x16x32_bf16 v[4:7], v[168:171], v[192:195], v[4:7]\n"
        "v_mul_f32 v185, s88, v185\n"
        "v_mul_f32 v186, s88, v186\n"
        "v_mfma_f32_16x16x32_bf16 v[68:71], v[168:171], v[196:199], 0\n"
        "v_mul_f32 v187, s88, v187\n"
        "v_mul_f32 v188, s88, v188\n"
        "v_mfma_f32_16x16x32_bf16 v[68:71], v[172:175], v[212:215], v[68:71]\n"
        "global_load_lds_dwordx4 v244, s[86:87] offset:2048\n"
        "v_mfma_f32_16x16x32_bf16 v[64:67], v[164:167], v[212:215], 0\n"
        "v_mul_f32 v189, s88, v189\n"
        "v_mul_f32 v190, s88, v190\n"
        "v_mfma_f32_16x16x32_bf16 v[64:67], v[160:163], v[196:199], v[64:67]\n"
        "v_accvgpr_read_b32 v176, a224\n"
        "v_mul_f32 v176, s88, v176\n"
        "v_mfma_f32_16x16x32_bf16 v[96:99], v[160:163], v[200:203], 0\n"
        "v_cvt_pk_bf16_f32 v176, v176, v177\n"
        "v_cvt_pk_bf16_f32 v177, v178, v179\n"
        "v_mfma_f32_16x16x32_bf16 v[96:99], v[164:167], v[216:219], v[96:99]\n"
        "v_cvt_pk_bf16_f32 v178, v180, v181\n"
        "v_cvt_pk_bf16_f32 v179, v182, v183\n"
        "v_mfma_f32_16x16x32_bf16 v[100:103], v[172:175], v[216:219], 0\n"
        "global_load_lds_dwordx4 v245, s[86:87] offset:3072\n"
        "s_add_u32 m0, s73, 0x20000\n"
        "v_accvgpr_write_b32 a224, v176\n"
        "v_mfma_f32_16x16x32_bf16 v[100:103], v[168:171], v[200:203], v[100:103]\n"
        "s_add_u32 s86, s68, 0x180000\n"
        "s_addc_u32 s87, s69, 0\n"
        "v_accvgpr_write_b32 a225, v177\n"
        "v_mfma_f32_16x16x32_bf16 v[132:135], v[168:171], v[204:207], 0\n"
        "ds_read_b64_tr_b16 v[168:169], v232 offset:33792\n"
        "v_accvgpr_write_b32 a226, v178\n"
        "v_mfma_f32_16x16x32_bf16 v[132:135], v[172:175], v[220:223], v[132:135]\n"
        "ds_read_b64_tr_b16 v[172:173], v232 offset:41984\n"
        "v_accvgpr_write_b32 a227, v179\n"
        "v_mfma_f32_16x16x32_bf16 v[128:131], v[164:167], v[220:223], 0\n"
        "ds_read_b64_tr_b16 v[164:165], v230 offset:41984\n"
        "v_mul_f32 v191, s88, v191\n"
        "v_mfma_f32_16x16x32_bf16 v[128:131], v[160:163], v[204:207], v[128:131]\n"
        "ds_read_b64_tr_b16 v[160:161], v230 offset:33792\n"
        "v_accvgpr_read_b32 v177, a193\n"
        "v_mfma_f32_16x16x32_bf16 v[8:11], v[32:35], v[192:195], 0\n"
        "ds_read_b64_tr_b16 v[162:163], v231 offset:33792\n"
        "v_accvgpr_read_b32 v178, a194\n"
        "v_mfma_f32_16x16x32_bf16 v[8:11], v[36:39], v[208:211], v[8:11]\n"
        "ds_read_b64_tr_b16 v[166:167], v231 offset:41984\n"
        "v_accvgpr_read_b32 v179, a195\n"
        "LHA4_%=:\n"
        "s_cbranch_execz LHA8_%=\n"
        "v_mfma_f32_16x16x32_bf16 v[12:15], v[44:47], v[208:211], 0\n"
        "ds_read_b64_tr_b16 v[174:175], v233 offset:41984\n"
        "v_accvgpr_read_b32 v180, a196\n"
        "v_mfma_f32_16x16x32_bf16 v[12:15], v[40:43], v[192:195], v[12:15]\n"
        "ds_read_b64_tr_b16 v[170:171], v233 offset:33792\n"
        "v_accvgpr_read_b32 v181, a197\n"
        "v_mfma_f32_16x16x32_bf16 v[76:79], v[40:43], v[196:199], 0\n"
        "v_accvgpr_read_b32 v182, a198\n"
        "v_accvgpr_read_b32 v183, a199\n"
        "v_mfma_f32_16x16x32_bf16 v[76:79], v[44:47], v[212:215], v[76:79]\n"
        "v_mul_f32 v177, s88, v177\n"
        "v_mul_f32 v178, s88, v178\n"
        "v_mfma_f32_16x16x32_bf16 v[72:75], v[36:39], v[212:215], 0\n"
        "v_mul_f32 v179, s88, v179\n"
        "v_mul_f32 v180, s88, v180\n"
        "v_mfma_f32_16x16x32_bf16 v[72:75], v[32:35], v[196:199], v[72:75]\n"
        "v_mul_f32 v181, s88, v181\n"
        "v_mul_f32 v182, s88, v182\n"
        "v_mfma_f32_16x16x32_bf16 v[104:107], v[32:35], v[200:203], 0\n"
        "v_mul_f32 v183, s88, v183\n"
        "v_mfma_f32_16x16x32_bf16 v[104:107], v[36:39], v[216:219], v[104:107]\n"
        "v_mfma_f32_16x16x32_bf16 v[108:111], v[44:47], v[216:219], 0\n"
        "v_mfma_f32_16x16x32_bf16 v[108:111], v[40:43], v[200:203], v[108:111]\n"
        "s_waitcnt vmcnt(14) lgkmcnt(0)\n"
        "v_mfma_f32_16x16x32_bf16 v[140:143], v[40:43], v[204:207], 0\n"
        "s_barrier\n"
        "v_mfma_f32_16x16x32_bf16 v[140:143], v[44:47], v[220:223], v[140:143]\n"
        "global_load_lds_dwordx4 v238, s[86:87] offset:0\n"
        "ds_read_b64_tr_b16 v[44:45], v228 offset:57344\n"
        "v_mfma_f32_16x16x32_bf16 v[136:139], v[36:39], v[220:223], 0\n"
        "ds_read_b64_tr_b16 v[36:37], v226 offset:57344\n"
        "v_mfma_f32_16x16x32_bf16 v[136:139], v[32:35], v[204:207], v[136:139]\n"
        "ds_read_b64_tr_b16 v[32:33], v226 offset:49152\n"
        "v_accvgpr_read_b32 v184, a232\n"
        "v_mfma_f32_16x16x32_bf16 v[16:19], v[48:51], v[192:195], 0\n"
        "ds_read_b64_tr_b16 v[34:35], v227 offset:49152\n"
        "v_mul_f32 v184, s88, v184\n"
        "v_mfma_f32_16x16x32_bf16 v[16:19], v[52:55], v[208:211], v[16:19]\n"
        "ds_read_b64_tr_b16 v[38:39], v227 offset:57344\n"
        "v_cvt_pk_bf16_f32 v184, v184, v185\n"
        "v_mfma_f32_16x16x32_bf16 v[20:23], v[60:63], v[208:211], 0\n"
        "ds_read_b64_tr_b16 v[46:47], v229 offset:57344\n"
        "v_cvt_pk_bf16_f32 v185, v186, v187\n"
        "v_mfma_f32_16x16x32_bf16 v[20:23], v[56:59], v[192:195], v[20:23]\n"
        "ds_read_b64_tr_b16 v[40:41], v228 offset:49152\n"
        "v_cvt_pk_bf16_f32 v186, v188, v189\n"
        "v_mfma_f32_16x16x32_bf16 v[84:87], v[56:59], v[196:199], 0\n"
        "ds_read_b64_tr_b16 v[42:43], v229 offset:49152\n"
        "v_cvt_pk_bf16_f32 v187, v190, v191\n"
        "v_mfma_f32_16x16x32_bf16 v[84:87], v[60:63], v[212:215], v[84:87]\n"
        "global_load_lds_dwordx4 v239, s[86:87] offset:1024\n"
        "v_accvgpr_write_b32 a228, v184\n"
        "v_mfma_f32_16x16x32_bf16 v[80:83], v[52:55], v[212:215], 0\n"
        "v_accvgpr_write_b32 a229, v185\n"
        "v_accvgpr_write_b32 a230, v186\n"
        "v_mfma_f32_16x16x32_bf16 v[80:83], v[48:51], v[196:199], v[80:83]\n"
        "v_accvgpr_write_b32 a231, v187\n"
        "v_accvgpr_read_b32 v185, a201\n"
        "v_mfma_f32_16x16x32_bf16 v[112:115], v[48:51], v[200:203], 0\n"
        "v_accvgpr_read_b32 v186, a202\n"
        "v_accvgpr_read_b32 v187, a203\n"
        "v_mfma_f32_16x16x32_bf16 v[112:115], v[52:55], v[216:219], v[112:115]\n"
        "global_load_lds_dwordx4 v240, s[86:87] offset:2048\n"
        "v_accvgpr_read_b32 v188, a204\n"
        "v_mfma_f32_16x16x32_bf16 v[116:119], v[60:63], v[216:219], 0\n"
        "v_accvgpr_read_b32 v189, a205\n"
        "v_accvgpr_read_b32 v190, a206\n"
        "v_mfma_f32_16x16x32_bf16 v[116:119], v[56:59], v[200:203], v[116:119]\n"
        "v_accvgpr_read_b32 v191, a207\n"
        "v_mul_f32 v185, s88, v185\n"
        "v_mfma_f32_16x16x32_bf16 v[148:151], v[56:59], v[204:207], 0\n"
        "ds_read_b64_tr_b16 v[56:57], v232 offset:49152\n"
        "v_mul_f32 v186, s88, v186\n"
        "v_mfma_f32_16x16x32_bf16 v[148:151], v[60:63], v[220:223], v[148:151]\n"
        "global_store_dwordx4 v254, a[224:227], s[70:71] offset:0 sc1\n"
        "ds_read_b64_tr_b16 v[60:61], v232 offset:57344\n"
        "ds_read_b64_tr_b16 v[62:63], v233 offset:57344\n"
        "v_mul_f32 v187, s88, v187\n"
        "v_mfma_f32_16x16x32_bf16 v[144:147], v[52:55], v[220:223], 0\n"
        "ds_read_b64_tr_b16 v[52:53], v230 offset:57344\n"
        "v_mul_f32 v188, s88, v188\n"
        "v_mfma_f32_16x16x32_bf16 v[144:147], v[48:51], v[204:207], v[144:147]\n"
        "ds_read_b64_tr_b16 v[48:49], v230 offset:49152\n"
        "v_mul_f32 v189, s88, v189\n"
        "v_mfma_f32_16x16x32_bf16 v[24:27], v[160:163], v[192:195], 0\n"
        "global_load_lds_dwordx4 v241, s[86:87] offset:3072\n"
        "s_add_u32 m0, s72, 0x0\n"
        "v_mfma_f32_16x16x32_bf16 v[24:27], v[164:167], v[208:211], v[24:27]\n"
        "ds_read_b64_tr_b16 v[50:51], v231 offset:49152\n"
        "s_add_u32 s86, s64, 0x180\n"
        "s_addc_u32 s87, s65, 0\n"
        "v_mfma_f32_16x16x32_bf16 v[28:31], v[172:175], v[208:211], 0\n"
        "ds_read_b64_tr_b16 v[54:55], v231 offset:57344\n"
        "v_mul_f32 v190, s88, v190\n"
        "v_mfma_f32_16x16x32_bf16 v[28:31], v[168:171], v[192:195], v[28:31]\n"
        "ds_read_b64_tr_b16 v[58:59], v233 offset:49152\n"
        "v_mul_f32 v191, s88, v191\n"
        "v_mfma_f32_16x16x32_bf16 v[92:95], v[168:171], v[196:199], 0\n"
        "global_load_lds_dwordx4 v242, s[86:87] offset:0\n"
        "v_mfma_f32_16x16x32_bf16 v[92:95], v[172:175], v[212:215], v[92:95]\n"
        "ds_read_b128 v[192:195], v248 offset:2048\n"
        "v_mfma_f32_16x16x32_bf16 v[88:91], v[164:167], v[212:215], 0\n"
        "ds_read_b128 v[208:211], v249 offset:2048\n"
        "v_mfma_f32_16x16x32_bf16 v[88:91], v[160:163], v[196:199], v[88:91]\n"
        "ds_read_b128 v[196:199], v248 offset:0\n"
        "v_accvgpr_read_b32 v176, a192\n"
        "v_mfma_f32_16x16x32_bf16 v[120:123], v[160:163], v[200:203], 0\n"
        "global_load_lds_dwordx4 v243, s[86:87] offset:1024\n"
        "v_mfma_f32_16x16x32_bf16 v[120:123], v[164:167], v[216:219], v[120:123]\n"
        "ds_read_b128 v[212:215], v249 offset:0\n"
        "v_mul_f32 v176, s88, v176\n"
        "v_mfma_f32_16x16x32_bf16 v[124:127], v[172:175], v[216:219], 0\n"
        "ds_read_b128 v[216:219], v249 offset:6144\n"
        "v_cvt_pk_bf16_f32 v176, v176, v177\n"
        "v_mfma_f32_16x16x32_bf16 v[124:127], v[168:171], v[200:203], v[124:127]\n"
        "ds_read_b128 v[200:203], v248 offset:6144\n"
        "v_cvt_pk_bf16_f32 v177, v178, v179\n"
        "v_mfma_f32_16x16x32_bf16 v[156:159], v[168:171], v[204:207], 0\n"
        "ds_read_b64_tr_b16 v[168:169], v228 offset:50176\n"
        "v_cvt_pk_bf16_f32 v178, v180, v181\n"
        "v_mfma_f32_16x16x32_bf16 v[156:159], v[172:175], v[220:223], v[156:159]\n"
        "s_waitcnt lgkmcnt(6)\n"
        "v_cvt_pk_bf16_f32 v179, v182, v183\n"
        "v_accvgpr_write_b32 a232, v176\n"
        "v_mfma_f32_16x16x32_bf16 v[152:155], v[164:167], v[220:223], 0\n"
        "s_waitcnt lgkmcnt(5)\n"
        "v_accvgpr_write_b32 a233, v177\n"
        "v_accvgpr_write_b32 a234, v178\n"
        "v_mfma_f32_16x16x32_bf16 v[152:155], v[160:163], v[204:207], v[152:155]\n"
        "global_load_lds_dwordx4 v244, s[86:87] offset:2048\n"
        "v_accvgpr_write_b32 a235, v179\n"
        "v_mfma_f32_16x16x32_bf16 v[0:3], v[32:35], v[192:195], v[0:3]\n"
        "ds_read_b128 v[204:207], v248 offset:4096\n"
        "v_accvgpr_read_b32 v177, a161\n"
        "v_mfma_f32_16x16x32_bf16 v[0:3], v[36:39], v[208:211], v[0:3]\n"
        "ds_read_b128 v[220:223], v249 offset:4096\n"
        "v_accvgpr_read_b32 v178, a162\n"
        "v_mfma_f32_16x16x32_bf16 v[4:7], v[44:47], v[208:211], v[4:7]\n"
        "ds_read_b64_tr_b16 v[160:161], v226 offset:50176\n"
        "v_accvgpr_read_b32 v179, a163\n"
        "v_mfma_f32_16x16x32_bf16 v[4:7], v[40:43], v[192:195], v[4:7]\n"
        "global_load_lds_dwordx4 v245, s[86:87] offset:3072\n"
        "s_add_u32 m0, s72, 0x1000\n"
        "s_waitcnt lgkmcnt(4)\n"
        "v_mfma_f32_16x16x32_bf16 v[68:71], v[40:43], v[196:199], v[68:71]\n"
        "ds_read_b64_tr_b16 v[162:163], v227 offset:50176\n"
        "s_add_u32 s86, s66, 0x180\n"
        "s_addc_u32 s87, s67, 0\n"
        "v_mfma_f32_16x16x32_bf16 v[68:71], v[44:47], v[212:215], v[68:71]\n"
        "ds_read_b64_tr_b16 v[164:165], v226 offset:58368\n"
        "v_accvgpr_read_b32 v180, a164\n"
        "v_mfma_f32_16x16x32_bf16 v[64:67], v[36:39], v[212:215], v[64:67]\n"
        "ds_read_b64_tr_b16 v[166:167], v227 offset:58368\n"
        "v_accvgpr_read_b32 v181, a165\n"
        "v_mfma_f32_16x16x32_bf16 v[64:67], v[32:35], v[196:199], v[64:67]\n"
        "ds_read_b64_tr_b16 v[172:173], v228 offset:58368\n"
        "global_load_lds_dwordx4 v242, s[86:87] offset:0\n"
        "v_mfma_f32_16x16x32_bf16 v[96:99], v[32:35], v[200:203], v[96:99]\n"
        "ds_read_b64_tr_b16 v[174:175], v229 offset:58368\n"
        "v_accvgpr_read_b32 v182, a166\n"
        "v_mfma_f32_16x16x32_bf16 v[96:99], v[36:39], v[216:219], v[96:99]\n"
        "ds_read_b64_tr_b16 v[170:171], v229 offset:50176\n"
        "v_accvgpr_read_b32 v183, a167\n"
        "v_mfma_f32_16x16x32_bf16 v[100:103], v[44:47], v[216:219], v[100:103]\n"
        "s_waitcnt lgkmcnt(6)\n"
        "v_mul_f32 v177, s88, v177\n"
        "v_mul_f32 v178, s88, v178\n"
        "v_mfma_f32_16x16x32_bf16 v[100:103], v[40:43], v[200:203], v[100:103]\n"
        "global_load_lds_dwordx4 v243, s[86:87] offset:1024\n"
        "v_mfma_f32_16x16x32_bf16 v[132:135], v[40:43], v[204:207], v[132:135]\n"
        "ds_read_b64_tr_b16 v[40:41], v232 offset:50176\n"
        "v_mul_f32 v179, s88, v179\n"
        "v_mfma_f32_16x16x32_bf16 v[132:135], v[44:47], v[220:223], v[132:135]\n"
        "ds_read_b64_tr_b16 v[44:45], v232 offset:58368\n"
        "v_mul_f32 v180, s88, v180\n"
        "v_mfma_f32_16x16x32_bf16 v[128:131], v[36:39], v[220:223], v[128:131]\n"
        "ds_read_b64_tr_b16 v[36:37], v230 offset:58368\n"
        "v_mul_f32 v181, s88, v181\n"
        "v_mfma_f32_16x16x32_bf16 v[128:131], v[32:35], v[204:207], v[128:131]\n"
        "global_load_lds_dwordx4 v244, s[86:87] offset:2048\n"
        "v_mfma_f32_16x16x32_bf16 v[8:11], v[48:51], v[192:195], v[8:11]\n"
        "ds_read_b64_tr_b16 v[32:33], v230 offset:50176\n"
        "v_accvgpr_read_b32 v184, a200\n"
        "v_mfma_f32_16x16x32_bf16 v[8:11], v[52:55], v[208:211], v[8:11]\n"
        "ds_read_b64_tr_b16 v[34:35], v231 offset:50176\n"
        "v_mul_f32 v184, s88, v184\n"
        "v_mfma_f32_16x16x32_bf16 v[12:15], v[60:63], v[208:211], v[12:15]\n"
        "ds_read_b64_tr_b16 v[38:39], v231 offset:58368\n"
        "v_cvt_pk_bf16_f32 v184, v184, v185\n"
        "v_mfma_f32_16x16x32_bf16 v[12:15], v[56:59], v[192:195], v[12:15]\n"
        "global_load_lds_dwordx4 v245, s[86:87] offset:3072\n"
        "s_add_u32 m0, s73, 0x24000\n"
        "v_mfma_f32_16x16x32_bf16 v[76:79], v[56:59], v[196:199], v[76:79]\n"
        "ds_read_b64_tr_b16 v[46:47], v233 offset:58368\n"
        "v_cvt_pk_bf16_f32 v185, v186, v187\n"
        "v_mfma_f32_16x16x32_bf16 v[76:79], v[60:63], v[212:215], v[76:79]\n"
        "ds_read_b64_tr_b16 v[42:43], v233 offset:50176\n"
        "v_cvt_pk_bf16_f32 v186, v188, v189\n"
        "v_mfma_f32_16x16x32_bf16 v[72:75], v[52:55], v[212:215], v[72:75]\n"
        "s_add_u32 s86, s68, 0x200000\n"
        "s_addc_u32 s87, s69, 0\n"
        "v_cvt_pk_bf16_f32 v187, v190, v191\n"
        "v_mfma_f32_16x16x32_bf16 v[72:75], v[48:51], v[196:199], v[72:75]\n"
        "v_accvgpr_write_b32 a236, v184\n"
        "v_accvgpr_write_b32 a237, v185\n"
        "v_mfma_f32_16x16x32_bf16 v[104:107], v[48:51], v[200:203], v[104:107]\n"
        "v_accvgpr_write_b32 a238, v186\n"
        "v_accvgpr_write_b32 a239, v187\n"
        "v_mfma_f32_16x16x32_bf16 v[104:107], v[52:55], v[216:219], v[104:107]\n"
        "v_mul_f32 v182, s88, v182\n"
        "v_mul_f32 v183, s88, v183\n"
        "v_mfma_f32_16x16x32_bf16 v[108:111], v[60:63], v[216:219], v[108:111]\n"
        "v_accvgpr_read_b32 v185, a169\n"
        "v_accvgpr_read_b32 v186, a170\n"
        "v_mfma_f32_16x16x32_bf16 v[108:111], v[56:59], v[200:203], v[108:111]\n"
        "v_accvgpr_read_b32 v187, a171\n"
        "v_accvgpr_read_b32 v188, a172\n"
        "v_mfma_f32_16x16x32_bf16 v[140:143], v[56:59], v[204:207], v[140:143]\n"
        "s_waitcnt lgkmcnt(1)\n"
        "v_accvgpr_read_b32 v189, a173\n"
        "v_accvgpr_read_b32 v190, a174\n"
        "v_mfma_f32_16x16x32_bf16 v[140:143], v[60:63], v[220:223], v[140:143]\n"
        "s_waitcnt vmcnt(13) lgkmcnt(0)\n"
        "v_accvgpr_read_b32 v191, a175\n"
        "v_mul_f32 v185, s88, v185\n"
        "v_mfma_f32_16x16x32_bf16 v[136:139], v[52:55], v[220:223], v[136:139]\n"
        "s_barrier\n"
        "v_mfma_f32_16x16x32_bf16 v[136:139], v[48:51], v[204:207], v[136:139]\n"
        "global_load_lds_dwordx4 v238, s[86:87] offset:0\n"
        "ds_read_b64_tr_b16 v[48:49], v226 offset:16384\n"
        "v_mfma_f32_16x16x32_bf16 v[16:19], v[160:163], v[192:195], v[16:19]\n"
        "ds_read_b64_tr_b16 v[50:51], v227 offset:16384\n"
        "v_mul_f32 v186, s88, v186\n"
        "v_mfma_f32_16x16x32_bf16 v[16:19], v[164:167], v[208:211], v[16:19]\n"
        "ds_read_b64_tr_b16 v[52:53], v226 offset:24576\n"
        "v_mul_f32 v187, s88, v187\n"
        "v_mfma_f32_16x16x32_bf16 v[20:23], v[172:175], v[208:211], v[20:23]\n"
        "ds_read_b64_tr_b16 v[54:55], v227 offset:24576\n"
        "v_mul_f32 v188, s88, v188\n"
        "v_mfma_f32_16x16x32_bf16 v[20:23], v[168:171], v[192:195], v[20:23]\n"
        "ds_read_b64_tr_b16 v[60:61], v228 offset:24576\n"
        "global_load_lds_dwordx4 v239, s[86:87] offset:1024\n"
        "v_mfma_f32_16x16x32_bf16 v[84:87], v[168:171], v[196:199], v[84:87]\n"
        "ds_read_b64_tr_b16 v[62:63], v229 offset:24576\n"
        "v_mul_f32 v189, s88, v189\n"
        "v_mfma_f32_16x16x32_bf16 v[84:87], v[172:175], v[212:215], v[84:87]\n"
        "ds_read_b64_tr_b16 v[56:57], v228 offset:16384\n"
        "v_mul_f32 v190, s88, v190\n"
        "v_mfma_f32_16x16x32_bf16 v[80:83], v[164:167], v[212:215], v[80:83]\n"
        "ds_read_b64_tr_b16 v[58:59], v229 offset:16384\n"
        "v_mul_f32 v191, s88, v191\n"
        "v_mfma_f32_16x16x32_bf16 v[80:83], v[160:163], v[196:199], v[80:83]\n"
        "v_accvgpr_read_b32 v176, a160\n"
        "v_mul_f32 v176, s88, v176\n"
        "v_mfma_f32_16x16x32_bf16 v[112:115], v[160:163], v[200:203], v[112:115]\n"
        "global_load_lds_dwordx4 v240, s[86:87] offset:2048\n"
        "v_cvt_pk_bf16_f32 v176, v176, v177\n"
        "v_mfma_f32_16x16x32_bf16 v[112:115], v[164:167], v[216:219], v[112:115]\n"
        "v_cvt_pk_bf16_f32 v177, v178, v179\n"
        "v_cvt_pk_bf16_f32 v178, v180, v181\n"
        "v_mfma_f32_16x16x32_bf16 v[116:119], v[172:175], v[216:219], v[116:119]\n"
        "v_cvt_pk_bf16_f32 v179, v182, v183\n"
        "v_accvgpr_write_b32 a192, v176\n"
        "v_mfma_f32_16x16x32_bf16 v[116:119], v[168:171], v[200:203], v[116:119]\n"
        "v_accvgpr_write_b32 a193, v177\n"
        "v_accvgpr_write_b32 a194, v178\n"
        "v_mfma_f32_16x16x32_bf16 v[148:151], v[168:171], v[204:207], v[148:151]\n"
        "global_load_lds_dwordx4 v241, s[86:87] offset:3072\n"
        "s_add_u32 m0, s72, 0x10000\n"
        "v_accvgpr_write_b32 a195, v179\n"
        "v_mfma_f32_16x16x32_bf16 v[148:151], v[172:175], v[220:223], v[148:151]\n"
        "ds_read_b64_tr_b16 v[172:173], v232 offset:24576\n"
        "s_add_u32 s86, s64, 0x200\n"
        "s_addc_u32 s87, s65, 0\n"
        "v_mfma_f32_16x16x32_bf16 v[144:147], v[164:167], v[220:223], v[144:147]\n"
        "ds_read_b64_tr_b16 v[164:165], v230 offset:24576\n"
        "v_accvgpr_read_b32 v177, a1\n"
        "v_mfma_f32_16x16x32_bf16 v[144:147], v[160:163], v[204:207], v[144:147]\n"
        "ds_read_b64_tr_b16 v[160:161], v230 offset:16384\n"
        "v_accvgpr_read_b32 v178, a2\n"
        "v_mfma_f32_16x16x32_bf16 v[24:27], v[32:35], v[192:195], v[24:27]\n"
        "global_store_dwordx4 v254, a[228:231], s[70:71] offset:1024 sc1\n"
        "ds_read_b64_tr_b16 v[162:163], v231 offset:16384\n"
        "ds_read_b64_tr_b16 v[166:167], v231 offset:24576\n"
        "v_accvgpr_read_b32 v179, a3\n"
        "v_mfma_f32_16x16x32_bf16 v[24:27], v[36:39], v[208:211], v[24:27]\n"
        "ds_read_b64_tr_b16 v[174:175], v233 offset:24576\n"
        "v_accvgpr_read_b32 v180, a4\n"
        "v_mfma_f32_16x16x32_bf16 v[28:31], v[44:47], v[208:211], v[28:31]\n"
        "ds_read_b64_tr_b16 v[168:169], v232 offset:16384\n"
        "v_accvgpr_read_b32 v181, a5\n"
        "v_mfma_f32_16x16x32_bf16 v[28:31], v[40:43], v[192:195], v[28:31]\n"
        "ds_read_b64_tr_b16 v[170:171], v233 offset:16384\n"
        "global_load_lds_dwordx4 v242, s[86:87] offset:0\n"
        "v_mfma_f32_16x16x32_bf16 v[92:95], v[40:43], v[196:199], v[92:95]\n"
        "ds_read_b128 v[192:195], v246 offset:34816\n"
        "v_accvgpr_read_b32 v182, a6\n"
        "v_mfma_f32_16x16x32_bf16 v[92:95], v[44:47], v[212:215], v[92:95]\n"
        "ds_read_b128 v[208:211], v247 offset:34816\n"
        "v_accvgpr_read_b32 v183, a7\n"
        "v_mfma_f32_16x16x32_bf16 v[88:91], v[36:39], v[212:215], v[88:91]\n"
        "ds_read_b128 v[212:215], v247 offset:32768\n"
        "v_mul_f32 v177, s88, v177\n"
        "v_mfma_f32_16x16x32_bf16 v[88:91], v[32:35], v[196:199], v[88:91]\n"
        "global_load_lds_dwordx4 v243, s[86:87] offset:1024\n"
        "v_mfma_f32_16x16x32_bf16 v[120:123], v[32:35], v[200:203], v[120:123]\n"
        "ds_read_b128 v[196:199], v246 offset:32768\n"
        "v_mul_f32 v178, s88, v178\n"
        "v_mfma_f32_16x16x32_bf16 v[120:123], v[36:39], v[216:219], v[120:123]\n"
        "s_add_u32 s70, s38, s97\n"
        "s_addc_u32 s71, s39, 0\n"
        "v_mul_f32 v179, s88, v179\n"
        "v_mfma_f32_16x16x32_bf16 v[124:127], v[44:47], v[216:219], v[124:127]\n"
        "ds_read_b128 v[216:219], v247 offset:38912\n"
        "v_mul_f32 v180, s88, v180\n"
        "v_mfma_f32_16x16x32_bf16 v[124:127], v[40:43], v[200:203], v[124:127]\n"
        "global_load_lds_dwordx4 v244, s[86:87] offset:2048\n"
        "s_add_u32 s97, s52, 0x1000\n"
        "v_mfma_f32_16x16x32_bf16 v[156:159], v[40:43], v[204:207], v[156:159]\n"
        "ds_read_b128 v[200:203], v246 offset:38912\n"
        "v_mul_f32 v181, s88, v181\n"
        "v_mfma_f32_16x16x32_bf16 v[156:159], v[44:47], v[220:223], v[156:159]\n"
        "ds_read_b64_tr_b16 v[44:45], v228 offset:25600\n"
        "v_mul_f32 v182, s88, v182\n"
        "v_mfma_f32_16x16x32_bf16 v[152:155], v[36:39], v[220:223], v[152:155]\n"
        "ds_read_b128 v[220:223], v247 offset:36864\n"
        "v_mul_f32 v183, s88, v183\n"
        "v_mfma_f32_16x16x32_bf16 v[152:155], v[32:35], v[204:207], v[152:155]\n"
        "s_waitcnt lgkmcnt(5)\n"
        "global_load_lds_dwordx4 v245, s[86:87] offset:3072\n"
        "s_add_u32 m0, s72, 0x11000\n"
        "v_mfma_f32_16x16x32_bf16 v[0:3], v[48:51], v[192:195], v[0:3]\n"
        "ds_read_b128 v[204:207], v246 offset:36864\n"
        "v_accvgpr_read_b32 v184, a168\n"
        "v_mfma_f32_16x16x32_bf16 v[0:3], v[52:55], v[208:211], v[0:3]\n"
        "s_waitcnt lgkmcnt(5)\n"
        "v_mul_f32 v184, s88, v184\n"
        "v_cvt_pk_bf16_f32 v184, v184, v185\n"
        "v_mfma_f32_16x16x32_bf16 v[4:7], v[60:63], v[208:211], v[4:7]\n"
        "s_add_u32 s86, s66, 0x200\n"
        "s_addc_u32 s87, s67, 0\n"
        "v_cvt_pk_bf16_f32 v185, v186, v187\n"
        "v_mfma_f32_16x16x32_bf16 v[4:7], v[56:59], v[192:195], v[4:7]\n"
        "global_load_lds_dwordx4 v242, s[86:87] offset:0\n"
        "v_mfma_f32_16x16x32_bf16 v[68:71], v[56:59], v[196:199], v[68:71]\n"
        "s_waitcnt lgkmcnt(3)\n"
        "v_cvt_pk_bf16_f32 v186, v188, v189\n"
        "v_cvt_pk_bf16_f32 v187, v190, v191\n"
        "v_mfma_f32_16x16x32_bf16 v[68:71], v[60:63], v[212:215], v[68:71]\n"
        "ds_read_b64_tr_b16 v[32:33], v226 offset:17408\n"
        "v_accvgpr_write_b32 a196, v184\n"
        "v_mfma_f32_16x16x32_bf16 v[64:67], v[52:55], v[212:215], v[64:67]\n"
        "ds_read_b64_tr_b16 v[34:35], v227 offset:17408\n"
        "v_accvgpr_write_b32 a197, v185\n"
        "v_mfma_f32_16x16x32_bf16 v[64:67], v[48:51], v[196:199], v[64:67]\n"
        "ds_read_b64_tr_b16 v[36:37], v226 offset:25600\n"
        "v_accvgpr_write_b32 a198, v186\n"
        "v_mfma_f32_16x16x32_bf16 v[96:99], v[48:51], v[200:203], v[96:99]\n"
        "s_waitcnt lgkmcnt(3)\n"
        "v_accvgpr_write_b32 a199, v187\n"
        "v_accvgpr_read_b32 v185, a9\n"
        "v_mfma_f32_16x16x32_bf16 v[96:99], v[52:55], v[216:219], v[96:99]\n"
        "ds_read_b64_tr_b16 v[38:39], v227 offset:25600\n"
        "global_load_lds_dwordx4 v243, s[86:87] offset:1024\n"
        "v_mfma_f32_16x16x32_bf16 v[100:103], v[60:63], v[216:219], v[100:103]\n"
        "ds_read_b64_tr_b16 v[46:47], v229 offset:25600\n"
        "v_accvgpr_read_b32 v186, a10\n"
        "v_mfma_f32_16x16x32_bf16 v[100:103], v[56:59], v[200:203], v[100:103]\n"
        "ds_read_b64_tr_b16 v[40:41], v228 offset:17408\n"
        "v_accvgpr_read_b32 v187, a11\n"
        "v_mfma_f32_16x16x32_bf16 v[132:135], v[56:59], v[204:207], v[132:135]\n"
        "ds_read_b64_tr_b16 v[42:43], v229 offset:17408\n"
        "v_accvgpr_read_b32 v188, a12\n"
        "v_mfma_f32_16x16x32_bf16 v[132:135], v[60:63], v[220:223], v[132:135]\n"
        "global_load_lds_dwordx4 v244, s[86:87] offset:2048\n"
        "v_mfma_f32_16x16x32_bf16 v[128:131], v[52:55], v[220:223], v[128:131]\n"
        "ds_read_b64_tr_b16 v[52:53], v230 offset:25600\n"
        "v_accvgpr_read_b32 v189, a13\n"
        "v_mfma_f32_16x16x32_bf16 v[128:131], v[48:51], v[204:207], v[128:131]\n"
        "ds_read_b64_tr_b16 v[48:49], v230 offset:17408\n"
        "v_accvgpr_read_b32 v190, a14\n"
        "v_mfma_f32_16x16x32_bf16 v[8:11], v[160:163], v[192:195], v[8:11]\n"
        "ds_read_b64_tr_b16 v[50:51], v231 offset:17408\n"
        "v_accvgpr_read_b32 v191, a15\n"
        "v_mfma_f32_16x16x32_bf16 v[8:11], v[164:167], v[208:211], v[8:11]\n"
        "global_load_lds_dwordx4 v245, s[86:87] offset:3072\n"
        "s_add_u32 m0, s73, 0x1c000\n"
        "v_mfma_f32_16x16x32_bf16 v[12:15], v[172:175], v[208:211], v[12:15]\n"
        "ds_read_b64_tr_b16 v[54:55], v231 offset:25600\n"
        "s_add_u32 s86, s68, 0x280000\n"
        "s_addc_u32 s87, s69, 0\n"
        "v_mfma_f32_16x16x32_bf16 v[12:15], v[168:171], v[192:195], v[12:15]\n"
        "ds_read_b64_tr_b16 v[60:61], v232 offset:25600\n"
        "v_mul_f32 v185, s88, v185\n"
        "v_mfma_f32_16x16x32_bf16 v[76:79], v[168:171], v[196:199], v[76:79]\n"
        "ds_read_b64_tr_b16 v[62:63], v233 offset:25600\n"
        "v_mul_f32 v186, s88, v186\n"
        "v_mfma_f32_16x16x32_bf16 v[76:79], v[172:175], v[212:215], v[76:79]\n"
        "ds_read_b64_tr_b16 v[56:57], v232 offset:17408\n"
        "v_mul_f32 v187, s88, v187\n"
        "v_mfma_f32_16x16x32_bf16 v[72:75], v[164:167], v[212:215], v[72:75]\n"
        "ds_read_b64_tr_b16 v[58:59], v233 offset:17408\n"
        "v_mul_f32 v188, s88, v188\n"
        "v_mfma_f32_16x16x32_bf16 v[72:75], v[160:163], v[196:199], v[72:75]\n"
        "v_accvgpr_read_b32 v176, a0\n"
        "v_mul_f32 v176, s88, v176\n"
        "v_mfma_f32_16x16x32_bf16 v[104:107], v[160:163], v[200:203], v[104:107]\n"
        "v_cvt_pk_bf16_f32 v176, v176, v177\n"
        "v_cvt_pk_bf16_f32 v177, v178, v179\n"
        "v_mfma_f32_16x16x32_bf16 v[104:107], v[164:167], v[216:219], v[104:107]\n"
        "v_cvt_pk_bf16_f32 v178, v180, v181\n"
        "v_cvt_pk_bf16_f32 v179, v182, v183\n"
        "v_mfma_f32_16x16x32_bf16 v[108:111], v[172:175], v[216:219], v[108:111]\n"
        "v_accvgpr_write_b32 a200, v176\n"
        "v_accvgpr_write_b32 a201, v177\n"
        "v_mfma_f32_16x16x32_bf16 v[108:111], v[168:171], v[200:203], v[108:111]\n"
        "v_accvgpr_write_b32 a202, v178\n"
        "v_accvgpr_write_b32 a203, v179\n"
        "v_mfma_f32_16x16x32_bf16 v[140:143], v[168:171], v[204:207], v[140:143]\n"
        "s_waitcnt lgkmcnt(2)\n"
        "v_mul_f32 v189, s88, v189\n"
        "v_mul_f32 v190, s88, v190\n"
        "v_mfma_f32_16x16x32_bf16 v[140:143], v[172:175], v[220:223], v[140:143]\n"
        "v_mul_f32 v191, s88, v191\n"
        "v_accvgpr_read_b32 v177, a33\n"
        "v_mfma_f32_16x16x32_bf16 v[136:139], v[164:167], v[220:223], v[136:139]\n"
        "s_waitcnt vmcnt(13) lgkmcnt(0)\n"
        "v_accvgpr_read_b32 v178, a34\n"
        "v_accvgpr_read_b32 v179, a35\n"
        "v_mfma_f32_16x16x32_bf16 v[136:139], v[160:163], v[204:207], v[136:139]\n"
        "s_barrier\n"
        "v_mfma_f32_16x16x32_bf16 v[16:19], v[32:35], v[192:195], v[16:19]\n"
        "global_load_lds_dwordx4 v238, s[86:87] offset:0\n"
        "ds_read_b64_tr_b16 v[160:161], v226 offset:32768\n"
        "v_mfma_f32_16x16x32_bf16 v[16:19], v[36:39], v[208:211], v[16:19]\n"
        "ds_read_b64_tr_b16 v[162:163], v227 offset:32768\n"
        "v_accvgpr_read_b32 v180, a36\n"
        "v_mfma_f32_16x16x32_bf16 v[20:23], v[44:47], v[208:211], v[20:23]\n"
        "ds_read_b64_tr_b16 v[164:165], v226 offset:40960\n"
        "v_accvgpr_read_b32 v181, a37\n"
        "v_mfma_f32_16x16x32_bf16 v[20:23], v[40:43], v[192:195], v[20:23]\n"
        "ds_read_b64_tr_b16 v[166:167], v227 offset:40960\n"
        "v_accvgpr_read_b32 v182, a38\n"
        "v_mfma_f32_16x16x32_bf16 v[84:87], v[40:43], v[196:199], v[84:87]\n"
        "ds_read_b64_tr_b16 v[172:173], v228 offset:40960\n"
        "global_load_lds_dwordx4 v239, s[86:87] offset:1024\n"
        "v_mfma_f32_16x16x32_bf16 v[84:87], v[44:47], v[212:215], v[84:87]\n"
        "ds_read_b64_tr_b16 v[174:175], v229 offset:40960\n"
        "v_accvgpr_read_b32 v183, a39\n"
        "v_mfma_f32_16x16x32_bf16 v[80:83], v[36:39], v[212:215], v[80:83]\n"
        "ds_read_b64_tr_b16 v[168:169], v228 offset:32768\n"
        "v_mul_f32 v177, s88, v177\n"
        "v_mfma_f32_16x16x32_bf16 v[80:83], v[32:35], v[196:199], v[80:83]\n"
        "ds_read_b64_tr_b16 v[170:171], v229 offset:32768\n"
        "v_mul_f32 v178, s88, v178\n"
        "v_mfma_f32_16x16x32_bf16 v[112:115], v[32:35], v[200:203], v[112:115]\n"
        "global_load_lds_dwordx4 v240, s[86:87] offset:2048\n"
        "v_mfma_f32_16x16x32_bf16 v[112:115], v[36:39], v[216:219], v[112:115]\n"
        "v_mul_f32 v179, s88, v179\n"
        "v_mul_f32 v180, s88, v180\n"
        "v_mfma_f32_16x16x32_bf16 v[116:119], v[44:47], v[216:219], v[116:119]\n"
        "v_mul_f32 v181, s88, v181\n"
        "v_mul_f32 v182, s88, v182\n"
        "v_mfma_f32_16x16x32_bf16 v[116:119], v[40:43], v[200:203], v[116:119]\n"
        "v_mul_f32 v183, s88, v183\n"
        "v_mfma_f32_16x16x32_bf16 v[148:151], v[40:43], v[204:207], v[148:151]\n"
        "global_load_lds_dwordx4 v241, s[86:87] offset:3072\n"
        "s_add_u32 m0, s72, 0x8000\n"
        "v_mfma_f32_16x16x32_bf16 v[148:151], v[44:47], v[220:223], v[148:151]\n"
        "s_add_u32 s86, s64, 0x280\n"
        "s_addc_u32 s87, s65, 0\n"
        "v_mfma_f32_16x16x32_bf16 v[144:147], v[36:39], v[220:223], v[144:147]\n"
        "ds_read_b64_tr_b16 v[36:37], v230 offset:40960\n"
        "v_mfma_f32_16x16x32_bf16 v[144:147], v[32:35], v[204:207], v[144:147]\n"
        "ds_read_b64_tr_b16 v[32:33], v230 offset:32768\n"
        "v_accvgpr_read_b32 v184, a8\n"
        "v_mfma_f32_16x16x32_bf16 v[24:27], v[48:51], v[192:195], v[24:27]\n"
        "ds_read_b64_tr_b16 v[34:35], v231 offset:32768\n"
        "v_mul_f32 v184, s88, v184\n"
        "v_mfma_f32_16x16x32_bf16 v[24:27], v[52:55], v[208:211], v[24:27]\n"
        "global_load_lds_dwordx4 v242, s[86:87] offset:0\n"
        "v_cvt_pk_bf16_f32 v184, v184, v185\n"
        "v_mfma_f32_16x16x32_bf16 v[28:31], v[60:63], v[208:211], v[28:31]\n"
        "ds_read_b64_tr_b16 v[38:39], v231 offset:40960\n"
        "v_cvt_pk_bf16_f32 v185, v186, v187\n"
        "v_mfma_f32_16x16x32_bf16 v[28:31], v[56:59], v[192:195], v[28:31]\n"
        "ds_read_b64_tr_b16 v[44:45], v232 offset:40960\n"
        "v_cvt_pk_bf16_f32 v186, v188, v189\n"
        "v_mfma_f32_16x16x32_bf16 v[92:95], v[56:59], v[196:199], v[92:95]\n"
        "ds_read_b64_tr_b16 v[46:47], v233 offset:40960\n"
        "v_cvt_pk_bf16_f32 v187, v190, v191\n"
        "v_mfma_f32_16x16x32_bf16 v[92:95], v[60:63], v[212:215], v[92:95]\n"
        "ds_read_b64_tr_b16 v[40:41], v232 offset:32768\n"
        "v_accvgpr_write_b32 a204, v184\n"
        "v_mfma_f32_16x16x32_bf16 v[88:91], v[52:55], v[212:215], v[88:91]\n"
        "ds_read_b64_tr_b16 v[42:43], v233 offset:32768\n"
        "v_accvgpr_write_b32 a205, v185\n"
        "v_mfma_f32_16x16x32_bf16 v[88:91], v[48:51], v[196:199], v[88:91]\n"
        "ds_read_b128 v[192:195], v246 offset:2048\n"
        "v_accvgpr_write_b32 a206, v186\n"
        "v_mfma_f32_16x16x32_bf16 v[120:123], v[48:51], v[200:203], v[120:123]\n"
        "ds_read_b128 v[208:211], v247 offset:2048\n"
        "v_accvgpr_write_b32 a207, v187\n"
        "v_mfma_f32_16x16x32_bf16 v[120:123], v[52:55], v[216:219], v[120:123]\n"
        "global_load_lds_dwordx4 v243, s[86:87] offset:1024\n"
        "ds_read_b128 v[196:199], v246 offset:0\n"
        "v_mfma_f32_16x16x32_bf16 v[124:127], v[60:63], v[216:219], v[124:127]\n"
        "ds_read_b128 v[212:215], v247 offset:0\n"
        "v_accvgpr_read_b32 v185, a41\n"
        "v_mfma_f32_16x16x32_bf16 v[124:127], v[56:59], v[200:203], v[124:127]\n"
        "ds_read_b128 v[200:203], v246 offset:6144\n"
        "v_accvgpr_read_b32 v186, a42\n"
        "v_mfma_f32_16x16x32_bf16 v[156:159], v[56:59], v[204:207], v[156:159]\n"
        "ds_read_b128 v[216:219], v247 offset:6144\n"
        "v_accvgpr_read_b32 v187, a43\n"
        "v_mfma_f32_16x16x32_bf16 v[156:159], v[60:63], v[220:223], v[156:159]\n"
        "global_store_dwordx4 v254, a[232:235], s[70:71] offset:0 sc1\n"
        "ds_read_b64_tr_b16 v[60:61], v228 offset:41984\n"
        "ds_read_b64_tr_b16 v[62:63], v229 offset:41984\n"
        "v_accvgpr_read_b32 v188, a44\n"
        "v_mfma_f32_16x16x32_bf16 v[152:155], v[52:55], v[220:223], v[152:155]\n"
        "ds_read_b128 v[220:223], v247 offset:4096\n"
        "v_accvgpr_read_b32 v189, a45\n"
        "v_mfma_f32_16x16x32_bf16 v[152:155], v[48:51], v[204:207], v[152:155]\n"
        "ds_read_b128 v[204:207], v246 offset:4096\n"
        "s_waitcnt lgkmcnt(8)\n"
        "v_mfma_f32_16x16x32_bf16 v[0:3], v[160:163], v[192:195], v[0:3]\n"
        "global_load_lds_dwordx4 v244, s[86:87] offset:2048\n"
        "v_mfma_f32_16x16x32_bf16 v[0:3], v[164:167], v[208:211], v[0:3]\n"
        "ds_read_b64_tr_b16 v[48:49], v226 offset:33792\n"
        "v_accvgpr_read_b32 v190, a46\n"
        "v_mfma_f32_16x16x32_bf16 v[4:7], v[172:175], v[208:211], v[4:7]\n"
        "ds_read_b64_tr_b16 v[50:51], v227 offset:33792\n"
        "v_accvgpr_read_b32 v191, a47\n"
        "v_mfma_f32_16x16x32_bf16 v[4:7], v[168:171], v[192:195], v[4:7]\n"
        "ds_read_b64_tr_b16 v[52:53], v226 offset:41984\n"
        "s_waitcnt lgkmcnt(7)\n"
        "v_mfma_f32_16x16x32_bf16 v[68:71], v[168:171], v[196:199], v[68:71]\n"
        "global_load_lds_dwordx4 v245, s[86:87] offset:3072\n"
        "s_add_u32 m0, s72, 0x9000\n"
        "v_mfma_f32_16x16x32_bf16 v[68:71], v[172:175], v[212:215], v[68:71]\n"
        "ds_read_b64_tr_b16 v[54:55], v227 offset:41984\n"
        "s_add_u32 s86, s66, 0x280\n"
        "s_addc_u32 s87, s67, 0\n"
        "v_mfma_f32_16x16x32_bf16 v[64:67], v[164:167], v[212:215], v[64:67]\n"
        "ds_read_b64_tr_b16 v[56:57], v228 offset:33792\n"
        "v_mul_f32 v185, s88, v185\n"
        "v_mfma_f32_16x16x32_bf16 v[64:67], v[160:163], v[196:199], v[64:67]\n"
        "ds_read_b64_tr_b16 v[58:59], v229 offset:33792\n"
        "v_accvgpr_read_b32 v176, a32\n"
        "v_mfma_f32_16x16x32_bf16 v[96:99], v[160:163], v[200:203], v[96:99]\n"
        "global_load_lds_dwordx4 v242, s[86:87] offset:0\n"
        "v_mfma_f32_16x16x32_bf16 v[96:99], v[164:167], v[216:219], v[96:99]\n"
        "s_waitcnt lgkmcnt(5)\n"
        "v_mul_f32 v176, s88, v176\n"
        "v_cvt_pk_bf16_f32 v176, v176, v177\n"
        "v_mfma_f32_16x16x32_bf16 v[100:103], v[172:175], v[216:219], v[100:103]\n"
        "LHA5_%=:\n"
        "s_cbranch_execz LHA9_%=\n"
        "v_cvt_pk_bf16_f32 v177, v178, v179\n"
        "v_cvt_pk_bf16_f32 v178, v180, v181\n"
        "v_mfma_f32_16x16x32_bf16 v[100:103], v[168:171], v[200:203], v[100:103]\n"
        "v_cvt_pk_bf16_f32 v179, v182, v183\n"
        "v_accvgpr_write_b32 a160, v176\n"
        "v_mfma_f32_16x16x32_bf16 v[132:135], v[168:171], v[204:207], v[132:135]\n"
        "global_load_lds_dwordx4 v243, s[86:87] offset:1024\n"
        "v_mfma_f32_16x16x32_bf16 v[132:135], v[172:175], v[220:223], v[132:135]\n"
        "ds_read_b64_tr_b16 v[172:173], v232 offset:41984\n"
        "v_accvgpr_write_b32 a161, v177\n"
        "v_mfma_f32_16x16x32_bf16 v[128:131], v[164:167], v[220:223], v[128:131]\n"
        "ds_read_b64_tr_b16 v[164:165], v230 offset:41984\n"
        "v_accvgpr_write_b32 a162, v178\n"
        "v_mfma_f32_16x16x32_bf16 v[128:131], v[160:163], v[204:207], v[128:131]\n"
        "ds_read_b64_tr_b16 v[160:161], v230 offset:33792\n"
        "v_accvgpr_write_b32 a163, v179\n"
        "v_mfma_f32_16x16x32_bf16 v[8:11], v[32:35], v[192:195], v[8:11]\n"
        "global_load_lds_dwordx4 v244, s[86:87] offset:2048\n"
        "v_mfma_f32_16x16x32_bf16 v[8:11], v[36:39], v[208:211], v[8:11]\n"
        "ds_read_b64_tr_b16 v[162:163], v231 offset:33792\n"
        "v_mul_f32 v186, s88, v186\n"
        "v_mfma_f32_16x16x32_bf16 v[12:15], v[44:47], v[208:211], v[12:15]\n"
        "ds_read_b64_tr_b16 v[166:167], v231 offset:41984\n"
        "v_mul_f32 v187, s88, v187\n"
        "v_mfma_f32_16x16x32_bf16 v[12:15], v[40:43], v[192:195], v[12:15]\n"
        "ds_read_b64_tr_b16 v[174:175], v233 offset:41984\n"
        "v_mul_f32 v188, s88, v188\n"
        "v_mfma_f32_16x16x32_bf16 v[76:79], v[40:43], v[196:199], v[76:79]\n"
        "global_load_lds_dwordx4 v245, s[86:87] offset:3072\n"
        "s_add_u32 m0, s73, 0x20000\n"
        "v_mfma_f32_16x16x32_bf16 v[76:79], v[44:47], v[212:215], v[76:79]\n"
        "ds_read_b64_tr_b16 v[168:169], v232 offset:33792\n"
        "v_mul_f32 v189, s88, v189\n"
        "v_mfma_f32_16x16x32_bf16 v[72:75], v[36:39], v[212:215], v[72:75]\n"
        "ds_read_b64_tr_b16 v[170:171], v233 offset:33792\n"
        "v_mul_f32 v190, s88, v190\n"
        "v_mfma_f32_16x16x32_bf16 v[72:75], v[32:35], v[196:199], v[72:75]\n"
        "s_add_u32 s86, s68, 0x300000\n"
        "s_addc_u32 s87, s69, 0\n"
        "v_mul_f32 v191, s88, v191\n"
        "v_mfma_f32_16x16x32_bf16 v[104:107], v[32:35], v[200:203], v[104:107]\n"
        "v_accvgpr_read_b32 v177, a65\n"
        "v_accvgpr_read_b32 v178, a66\n"
        "v_mfma_f32_16x16x32_bf16 v[104:107], v[36:39], v[216:219], v[104:107]\n"
        "v_accvgpr_read_b32 v179, a67\n"
        "v_accvgpr_read_b32 v180, a68\n"
        "v_mfma_f32_16x16x32_bf16 v[108:111], v[44:47], v[216:219], v[108:111]\n"
        "v_accvgpr_read_b32 v181, a69\n"
        "v_accvgpr_read_b32 v182, a70\n"
        "v_mfma_f32_16x16x32_bf16 v[108:111], v[40:43], v[200:203], v[108:111]\n"
        "v_accvgpr_read_b32 v183, a71\n"
        "v_mul_f32 v177, s88, v177\n"
        "v_mfma_f32_16x16x32_bf16 v[140:143], v[40:43], v[204:207], v[140:143]\n"
        "s_waitcnt lgkmcnt(2)\n"
        "v_mul_f32 v178, s88, v178\n"
        "v_mul_f32 v179, s88, v179\n"
        "v_mfma_f32_16x16x32_bf16 v[140:143], v[44:47], v[220:223], v[140:143]\n"
        "v_mul_f32 v180, s88, v180\n"
        "v_mul_f32 v181, s88, v181\n"
        "v_mfma_f32_16x16x32_bf16 v[136:139], v[36:39], v[220:223], v[136:139]\n"
        "s_waitcnt vmcnt(13) lgkmcnt(0)\n"
        "v_mul_f32 v182, s88, v182\n"
        "v_mul_f32 v183, s88, v183\n"
        "v_mfma_f32_16x16x32_bf16 v[136:139], v[32:35], v[204:207], v[136:139]\n"
        "v_accvgpr_read_b32 v184, a40\n"
        "v_mul_f32 v184, s88, v184\n"
        "v_mfma_f32_16x16x32_bf16 v[16:19], v[48:51], v[192:195], v[16:19]\n"
        "v_cvt_pk_bf16_f32 v184, v184, v185\n"
        "v_cvt_pk_bf16_f32 v185, v186, v187\n"
        "v_mfma_f32_16x16x32_bf16 v[16:19], v[52:55], v[208:211], v[16:19]\n"
        "v_cvt_pk_bf16_f32 v186, v188, v189\n"
        "v_cvt_pk_bf16_f32 v187, v190, v191\n"
        "v_mfma_f32_16x16x32_bf16 v[20:23], v[60:63], v[208:211], v[20:23]\n"
        "v_accvgpr_write_b32 a164, v184\n"
        "v_accvgpr_write_b32 a165, v185\n"
        "v_mfma_f32_16x16x32_bf16 v[20:23], v[56:59], v[192:195], v[20:23]\n"
        "v_accvgpr_write_b32 a166, v186\n"
        "v_accvgpr_write_b32 a167, v187\n"
        "v_mfma_f32_16x16x32_bf16 v[84:87], v[56:59], v[196:199], v[84:87]\n"
        "s_barrier\n"
        "v_mfma_f32_16x16x32_bf16 v[84:87], v[60:63], v[212:215], v[84:87]\n"
        "global_load_lds_dwordx4 v238, s[86:87] offset:0\n"
        "ds_read_b64_tr_b16 v[32:33], v226 offset:49152\n"
        "v_mfma_f32_16x16x32_bf16 v[80:83], v[52:55], v[212:215], v[80:83]\n"
        "ds_read_b64_tr_b16 v[34:35], v227 offset:49152\n"
        "v_accvgpr_read_b32 v185, a73\n"
        "v_mfma_f32_16x16x32_bf16 v[80:83], v[48:51], v[196:199], v[80:83]\n"
        "ds_read_b64_tr_b16 v[36:37], v226 offset:57344\n"
        "v_accvgpr_read_b32 v186, a74\n"
        "v_mfma_f32_16x16x32_bf16 v[112:115], v[48:51], v[200:203], v[112:115]\n"
        "ds_read_b64_tr_b16 v[38:39], v227 offset:57344\n"
        "v_accvgpr_read_b32 v187, a75\n"
        "v_mfma_f32_16x16x32_bf16 v[112:115], v[52:55], v[216:219], v[112:115]\n"
        "ds_read_b64_tr_b16 v[44:45], v228 offset:57344\n"
        "global_load_lds_dwordx4 v239, s[86:87] offset:1024\n"
        "v_mfma_f32_16x16x32_bf16 v[116:119], v[60:63], v[216:219], v[116:119]\n"
        "ds_read_b64_tr_b16 v[46:47], v229 offset:57344\n"
        "v_accvgpr_read_b32 v188, a76\n"
        "v_mfma_f32_16x16x32_bf16 v[116:119], v[56:59], v[200:203], v[116:119]\n"
        "ds_read_b64_tr_b16 v[40:41], v228 offset:49152\n"
        "v_accvgpr_read_b32 v189, a77\n"
        "v_mfma_f32_16x16x32_bf16 v[148:151], v[56:59], v[204:207], v[148:151]\n"
        "ds_read_b64_tr_b16 v[42:43], v229 offset:49152\n"
        "v_accvgpr_read_b32 v190, a78\n"
        "v_mfma_f32_16x16x32_bf16 v[148:151], v[60:63], v[220:223], v[148:151]\n"
        "global_load_lds_dwordx4 v240, s[86:87] offset:2048\n"
        "v_mfma_f32_16x16x32_bf16 v[144:147], v[52:55], v[220:223], v[144:147]\n"
        "ds_read_b64_tr_b16 v[52:53], v230 offset:57344\n"
        "v_accvgpr_read_b32 v191, a79\n"
        "v_mfma_f32_16x16x32_bf16 v[144:147], v[48:51], v[204:207], v[144:147]\n"
        "ds_read_b64_tr_b16 v[48:49], v230 offset:49152\n"
        "v_mul_f32 v185, s88, v185\n"
        "v_mfma_f32_16x16x32_bf16 v[24:27], v[160:163], v[192:195], v[24:27]\n"
        "ds_read_b64_tr_b16 v[50:51], v231 offset:49152\n"
        "v_mul_f32 v186, s88, v186\n"
        "v_mfma_f32_16x16x32_bf16 v[24:27], v[164:167], v[208:211], v[24:27]\n"
        "global_load_lds_dwordx4 v241, s[86:87] offset:3072\n"
        "s_add_u32 m0, s72, 0x0\n"
        "v_mfma_f32_16x16x32_bf16 v[28:31], v[172:175], v[208:211], v[28:31]\n"
        "ds_read_b64_tr_b16 v[54:55], v231 offset:57344\n"
        "s_add_u32 s86, s64, 0x300\n"
        "s_addc_u32 s87, s65, 0\n"
        "v_mfma_f32_16x16x32_bf16 v[28:31], v[168:171], v[192:195], v[28:31]\n"
        "ds_read_b64_tr_b16 v[60:61], v232 offset:57344\n"
        "v_mul_f32 v187, s88, v187\n"
        "v_mfma_f32_16x16x32_bf16 v[92:95], v[168:171], v[196:199], v[92:95]\n"
        "ds_read_b64_tr_b16 v[62:63], v233 offset:57344\n"
        "v_mul_f32 v188, s88, v188\n"
        "v_mfma_f32_16x16x32_bf16 v[92:95], v[172:175], v[212:215], v[92:95]\n"
        "ds_read_b64_tr_b16 v[56:57], v232 offset:49152\n"
        "global_load_lds_dwordx4 v242, s[86:87] offset:0\n"
        "v_mfma_f32_16x16x32_bf16 v[88:91], v[164:167], v[212:215], v[88:91]\n"
        "ds_read_b64_tr_b16 v[58:59], v233 offset:49152\n"
        "v_mul_f32 v189, s88, v189\n"
        "v_mfma_f32_16x16x32_bf16 v[88:91], v[160:163], v[196:199], v[88:91]\n"
        "ds_read_b128 v[192:195], v248 offset:2048\n"
        "v_accvgpr_read_b32 v176, a64\n"
        "v_mfma_f32_16x16x32_bf16 v[120:123], v[160:163], v[200:203], v[120:123]\n"
        "ds_read_b128 v[208:211], v249 offset:2048\n"
        "v_mul_f32 v176, s88, v176\n"
        "v_mfma_f32_16x16x32_bf16 v[120:123], v[164:167], v[216:219], v[120:123]\n"
        "global_load_lds_dwordx4 v243, s[86:87] offset:1024\n"
        "v_mfma_f32_16x16x32_bf16 v[124:127], v[172:175], v[216:219], v[124:127]\n"
        "ds_read_b128 v[196:199], v248 offset:0\n"
        "v_cvt_pk_bf16_f32 v176, v176, v177\n"
        "v_mfma_f32_16x16x32_bf16 v[124:127], v[168:171], v[200:203], v[124:127]\n"
        "ds_read_b128 v[212:215], v249 offset:0\n"
        "v_cvt_pk_bf16_f32 v177, v178, v179\n"
        "v_mfma_f32_16x16x32_bf16 v[156:159], v[168:171], v[204:207], v[156:159]\n"
        "ds_read_b128 v[200:203], v248 offset:6144\n"
        "v_cvt_pk_bf16_f32 v178, v180, v181\n"
        "v_mfma_f32_16x16x32_bf16 v[156:159], v[172:175], v[220:223], v[156:159]\n"
        "ds_read_b128 v[216:219], v249 offset:6144\n"
        "v_cvt_pk_bf16_f32 v179, v182, v183\n"
        "v_mfma_f32_16x16x32_bf16 v[152:155], v[164:167], v[220:223], v[152:155]\n"
        "global_load_lds_dwordx4 v244, s[86:87] offset:2048\n"
        "v_accvgpr_write_b32 a168, v176\n"
        "v_mfma_f32_16x16x32_bf16 v[152:155], v[160:163], v[204:207], v[152:155]\n"
        "s_waitcnt lgkmcnt(5)\n"
        "v_accvgpr_write_b32 a169, v177\n"
        "v_accvgpr_write_b32 a170, v178\n"
        "v_mfma_f32_16x16x32_bf16 v[0:3], v[32:35], v[192:195], v[0:3]\n"
        "s_waitcnt lgkmcnt(4)\n"
        "v_accvgpr_write_b32 a171, v179\n"
        "v_mul_f32 v190, s88, v190\n"
        "v_mfma_f32_16x16x32_bf16 v[0:3], v[36:39], v[208:211], v[0:3]\n"
        "ds_read_b128 v[204:207], v248 offset:4096\n"
        "v_mul_f32 v191, s88, v191\n"
        "v_mfma_f32_16x16x32_bf16 v[4:7], v[44:47], v[208:211], v[4:7]\n"
        "global_store_dwordx4 v254, a[236:239], s[70:71] offset:1024 sc1\n"
        "ds_read_b128 v[220:223], v249 offset:4096\n"
        "ds_read_b64_tr_b16 v[160:161], v226 offset:50176\n"
        "v_accvgpr_read_b32 v177, a97\n"
        "v_mfma_f32_16x16x32_bf16 v[4:7], v[40:43], v[192:195], v[4:7]\n"
        "ds_read_b64_tr_b16 v[162:163], v227 offset:50176\n"
        "s_waitcnt lgkmcnt(5)\n"
        "v_mfma_f32_16x16x32_bf16 v[68:71], v[40:43], v[196:199], v[68:71]\n"
        "ds_read_b64_tr_b16 v[164:165], v226 offset:58368\n"
        "v_accvgpr_read_b32 v178, a98\n"
        "v_mfma_f32_16x16x32_bf16 v[68:71], v[44:47], v[212:215], v[68:71]\n"
        "global_load_lds_dwordx4 v245, s[86:87] offset:3072\n"
        "s_add_u32 m0, s72, 0x1000\n"
        "v_mfma_f32_16x16x32_bf16 v[64:67], v[36:39], v[212:215], v[64:67]\n"
        "ds_read_b64_tr_b16 v[166:167], v227 offset:58368\n"
        "s_add_u32 s86, s66, 0x300\n"
        "s_addc_u32 s87, s67, 0\n"
        "v_mfma_f32_16x16x32_bf16 v[64:67], v[32:35], v[196:199], v[64:67]\n"
        "ds_read_b64_tr_b16 v[172:173], v228 offset:58368\n"
        "v_accvgpr_read_b32 v179, a99\n"
        "v_mfma_f32_16x16x32_bf16 v[96:99], v[32:35], v[200:203], v[96:99]\n"
        "ds_read_b64_tr_b16 v[174:175], v229 offset:58368\n"
        "s_waitcnt lgkmcnt(7)\n"
        "v_mfma_f32_16x16x32_bf16 v[96:99], v[36:39], v[216:219], v[96:99]\n"
        "ds_read_b64_tr_b16 v[168:169], v228 offset:50176\n"
        "global_load_lds_dwordx4 v242, s[86:87] offset:0\n"
        "v_mfma_f32_16x16x32_bf16 v[100:103], v[44:47], v[216:219], v[100:103]\n"
        "ds_read_b64_tr_b16 v[170:171], v229 offset:50176\n"
        "v_accvgpr_read_b32 v180, a100\n"
        "v_mfma_f32_16x16x32_bf16 v[100:103], v[40:43], v[200:203], v[100:103]\n"
        "s_waitcnt lgkmcnt(6)\n"
        "v_accvgpr_read_b32 v181, a101\n"
        "v_accvgpr_read_b32 v182, a102\n"
        "v_mfma_f32_16x16x32_bf16 v[132:135], v[40:43], v[204:207], v[132:135]\n"
        "ds_read_b64_tr_b16 v[40:41], v232 offset:50176\n"
        "v_accvgpr_read_b32 v183, a103\n"
        "v_mfma_f32_16x16x32_bf16 v[132:135], v[44:47], v[220:223], v[132:135]\n"
        "global_load_lds_dwordx4 v243, s[86:87] offset:1024\n"
        "v_mfma_f32_16x16x32_bf16 v[128:131], v[36:39], v[220:223], v[128:131]\n"
        "ds_read_b64_tr_b16 v[36:37], v230 offset:58368\n"
        "v_mul_f32 v177, s88, v177\n"
        "v_mfma_f32_16x16x32_bf16 v[128:131], v[32:35], v[204:207], v[128:131]\n"
        "ds_read_b64_tr_b16 v[32:33], v230 offset:50176\n"
        "v_accvgpr_read_b32 v184, a72\n"
        "v_mfma_f32_16x16x32_bf16 v[8:11], v[48:51], v[192:195], v[8:11]\n"
        "ds_read_b64_tr_b16 v[34:35], v231 offset:50176\n"
        "v_mul_f32 v184, s88, v184\n"
        "v_mfma_f32_16x16x32_bf16 v[8:11], v[52:55], v[208:211], v[8:11]\n"
        "global_load_lds_dwordx4 v244, s[86:87] offset:2048\n"
        "v_mfma_f32_16x16x32_bf16 v[12:15], v[60:63], v[208:211], v[12:15]\n"
        "ds_read_b64_tr_b16 v[38:39], v231 offset:58368\n"
        "v_cvt_pk_bf16_f32 v184, v184, v185\n"
        "v_mfma_f32_16x16x32_bf16 v[12:15], v[56:59], v[192:195], v[12:15]\n"
        "ds_read_b64_tr_b16 v[44:45], v232 offset:58368\n"
        "v_cvt_pk_bf16_f32 v185, v186, v187\n"
        "v_mfma_f32_16x16x32_bf16 v[76:79], v[56:59], v[196:199], v[76:79]\n"
        "ds_read_b64_tr_b16 v[46:47], v233 offset:58368\n"
        "v_cvt_pk_bf16_f32 v186, v188, v189\n"
        "v_mfma_f32_16x16x32_bf16 v[76:79], v[60:63], v[212:215], v[76:79]\n"
        "global_load_lds_dwordx4 v245, s[86:87] offset:3072\n"
        "s_add_u32 m0, s73, 0x24000\n"
        "v_mfma_f32_16x16x32_bf16 v[72:75], v[52:55], v[212:215], v[72:75]\n"
        "ds_read_b64_tr_b16 v[42:43], v233 offset:50176\n"
        "v_cvt_pk_bf16_f32 v187, v190, v191\n"
        "v_mfma_f32_16x16x32_bf16 v[72:75], v[48:51], v[196:199], v[72:75]\n"
        "s_add_u32 s86, s68, 0x380000\n"
        "s_addc_u32 s87, s69, 0\n"
        "v_accvgpr_write_b32 a172, v184\n"
        "v_mfma_f32_16x16x32_bf16 v[104:107], v[48:51], v[200:203], v[104:107]\n"
        "s_add_u32 s70, s38, s97\n"
        "s_addc_u32 s71, s39, 0\n"
        "v_accvgpr_write_b32 a173, v185\n"
        "v_mfma_f32_16x16x32_bf16 v[104:107], v[52:55], v[216:219], v[104:107]\n"
        "s_add_u32 s97, s52, 0x0\n"
        "v_accvgpr_write_b32 a174, v186\n"
        "v_accvgpr_write_b32 a175, v187\n"
        "v_mfma_f32_16x16x32_bf16 v[108:111], v[60:63], v[216:219], v[108:111]\n"
        "s_add_u32 s97, s97, s75\n"
        "v_mul_f32 v178, s88, v178\n"
        "v_mul_f32 v179, s88, v179\n"
        "v_mfma_f32_16x16x32_bf16 v[108:111], v[56:59], v[200:203], v[108:111]\n"
        "v_mul_f32 v180, s88, v180\n"
        "v_mul_f32 v181, s88, v181\n"
        "v_mfma_f32_16x16x32_bf16 v[140:143], v[56:59], v[204:207], v[140:143]\n"
        "v_mul_f32 v182, s88, v182\n"
        "v_mul_f32 v183, s88, v183\n"
        "v_mfma_f32_16x16x32_bf16 v[140:143], v[60:63], v[220:223], v[140:143]\n"
        "s_waitcnt lgkmcnt(1)\n"
        "v_accvgpr_read_b32 v185, a105\n"
        "v_accvgpr_read_b32 v186, a106\n"
        "v_mfma_f32_16x16x32_bf16 v[136:139], v[52:55], v[220:223], v[136:139]\n"
        "s_waitcnt vmcnt(13) lgkmcnt(0)\n"
        "v_accvgpr_read_b32 v187, a107\n"
        "v_accvgpr_read_b32 v188, a108\n"
        "v_mfma_f32_16x16x32_bf16 v[136:139], v[48:51], v[204:207], v[136:139]\n"
        "s_barrier\n"
        "v_mfma_f32_16x16x32_bf16 v[16:19], v[160:163], v[192:195], v[16:19]\n"
        "global_load_lds_dwordx4 v238, s[86:87] offset:0\n"
        "ds_read_b64_tr_b16 v[48:49], v226 offset:16384\n"
        "v_mfma_f32_16x16x32_bf16 v[16:19], v[164:167], v[208:211], v[16:19]\n"
        "ds_read_b64_tr_b16 v[50:51], v227 offset:16384\n"
        "v_accvgpr_read_b32 v189, a109\n"
        "v_mfma_f32_16x16x32_bf16 v[20:23], v[172:175], v[208:211], v[20:23]\n"
        "ds_read_b64_tr_b16 v[52:53], v226 offset:24576\n"
        "v_accvgpr_read_b32 v190, a110\n"
        "v_mfma_f32_16x16x32_bf16 v[20:23], v[168:171], v[192:195], v[20:23]\n"
        "ds_read_b64_tr_b16 v[54:55], v227 offset:24576\n"
        "v_accvgpr_read_b32 v191, a111\n"
        "v_mfma_f32_16x16x32_bf16 v[84:87], v[168:171], v[196:199], v[84:87]\n"
        "ds_read_b64_tr_b16 v[60:61], v228 offset:24576\n"
        "global_load_lds_dwordx4 v239, s[86:87] offset:1024\n"
        "v_mfma_f32_16x16x32_bf16 v[84:87], v[172:175], v[212:215], v[84:87]\n"
        "ds_read_b64_tr_b16 v[62:63], v229 offset:24576\n"
        "v_mul_f32 v185, s88, v185\n"
        "v_mfma_f32_16x16x32_bf16 v[80:83], v[164:167], v[212:215], v[80:83]\n"
        "ds_read_b64_tr_b16 v[56:57], v228 offset:16384\n"
        "v_mul_f32 v186, s88, v186\n"
        "v_mfma_f32_16x16x32_bf16 v[80:83], v[160:163], v[196:199], v[80:83]\n"
        "ds_read_b64_tr_b16 v[58:59], v229 offset:16384\n"
        "v_accvgpr_read_b32 v176, a96\n"
        "v_mfma_f32_16x16x32_bf16 v[112:115], v[160:163], v[200:203], v[112:115]\n"
        "global_load_lds_dwordx4 v240, s[86:87] offset:2048\n"
        "v_mfma_f32_16x16x32_bf16 v[112:115], v[164:167], v[216:219], v[112:115]\n"
        "v_mul_f32 v176, s88, v176\n"
        "v_cvt_pk_bf16_f32 v176, v176, v177\n"
        "v_mfma_f32_16x16x32_bf16 v[116:119], v[172:175], v[216:219], v[116:119]\n"
        "v_cvt_pk_bf16_f32 v177, v178, v179\n"
        "v_cvt_pk_bf16_f32 v178, v180, v181\n"
        "v_mfma_f32_16x16x32_bf16 v[116:119], v[168:171], v[200:203], v[116:119]\n"
        "v_cvt_pk_bf16_f32 v179, v182, v183\n"
        "v_accvgpr_write_b32 a0, v176\n"
        "v_mfma_f32_16x16x32_bf16 v[148:151], v[168:171], v[204:207], v[148:151]\n"
        "ds_read_b64_tr_b16 v[168:169], v232 offset:16384\n"
        "v_accvgpr_write_b32 a1, v177\n"
        "v_mfma_f32_16x16x32_bf16 v[148:151], v[172:175], v[220:223], v[148:151]\n"
        "global_load_lds_dwordx4 v241, s[86:87] offset:3072\n"
        "s_add_u32 m0, s72, 0x10000\n"
        "v_accvgpr_write_b32 a2, v178\n"
        "v_mfma_f32_16x16x32_bf16 v[144:147], v[164:167], v[220:223], v[144:147]\n"
        "ds_read_b64_tr_b16 v[164:165], v230 offset:24576\n"
        "v_accvgpr_write_b32 a3, v179\n"
        "v_mfma_f32_16x16x32_bf16 v[144:147], v[160:163], v[204:207], v[144:147]\n"
        "ds_read_b64_tr_b16 v[160:161], v230 offset:16384\n"
        "s_add_u32 s86, s64, 0x380\n"
        "s_addc_u32 s87, s65, 0\n"
        "v_mfma_f32_16x16x32_bf16 v[24:27], v[32:35], v[192:195], v[24:27]\n"
        "ds_read_b64_tr_b16 v[162:163], v231 offset:16384\n"
        "v_mul_f32 v187, s88, v187\n"
        "v_mfma_f32_16x16x32_bf16 v[24:27], v[36:39], v[208:211], v[24:27]\n"
        "ds_read_b64_tr_b16 v[166:167], v231 offset:24576\n"
        "global_load_lds_dwordx4 v242, s[86:87] offset:0\n"
        "v_mfma_f32_16x16x32_bf16 v[28:31], v[44:47], v[208:211], v[28:31]\n"
        "ds_read_b64_tr_b16 v[172:173], v232 offset:24576\n"
        "v_mul_f32 v188, s88, v188\n"
        "v_mfma_f32_16x16x32_bf16 v[28:31], v[40:43], v[192:195], v[28:31]\n"
        "ds_read_b64_tr_b16 v[174:175], v233 offset:24576\n"
        "v_mul_f32 v189, s88, v189\n"
        "v_mfma_f32_16x16x32_bf16 v[92:95], v[40:43], v[196:199], v[92:95]\n"
        "ds_read_b64_tr_b16 v[170:171], v233 offset:16384\n"
        "v_mul_f32 v190, s88, v190\n"
        "v_mfma_f32_16x16x32_bf16 v[92:95], v[44:47], v[212:215], v[92:95]\n"
        "ds_read_b128 v[192:195], v246 offset:34816\n"
        "global_load_lds_dwordx4 v243, s[86:87] offset:1024\n"
        "v_mfma_f32_16x16x32_bf16 v[88:91], v[36:39], v[212:215], v[88:91]\n"
        "ds_read_b128 v[208:211], v247 offset:34816\n"
        "v_mul_f32 v191, s88, v191\n"
        "v_mfma_f32_16x16x32_bf16 v[88:91], v[32:35], v[196:199], v[88:91]\n"
        "ds_read_b128 v[196:199], v246 offset:32768\n"
        "v_accvgpr_read_b32 v177, a129\n"
        "v_mfma_f32_16x16x32_bf16 v[120:123], v[32:35], v[200:203], v[120:123]\n"
        "ds_read_b128 v[212:215], v247 offset:32768\n"
        "v_accvgpr_read_b32 v178, a130\n"
        "v_mfma_f32_16x16x32_bf16 v[120:123], v[36:39], v[216:219], v[120:123]\n"
        "global_load_lds_dwordx4 v244, s[86:87] offset:2048\n"
        "v_mfma_f32_16x16x32_bf16 v[124:127], v[44:47], v[216:219], v[124:127]\n"
        "ds_read_b128 v[216:219], v247 offset:38912\n"
        "v_accvgpr_read_b32 v179, a131\n"
        "v_mfma_f32_16x16x32_bf16 v[124:127], v[40:43], v[200:203], v[124:127]\n"
        "ds_read_b128 v[200:203], v246 offset:38912\n"
        "v_accvgpr_read_b32 v180, a132\n"
        "v_mfma_f32_16x16x32_bf16 v[156:159], v[40:43], v[204:207], v[156:159]\n"
        "ds_read_b64_tr_b16 v[40:41], v228 offset:17408\n"
        "v_accvgpr_read_b32 v181, a133\n"
        "v_mfma_f32_16x16x32_bf16 v[156:159], v[44:47], v[220:223], v[156:159]\n"
        "global_load_lds_dwordx4 v245, s[86:87] offset:3072\n"
        "s_add_u32 m0, s72, 0x11000\n"
        "v_mfma_f32_16x16x32_bf16 v[152:155], v[36:39], v[220:223], v[152:155]\n"
        "ds_read_b128 v[220:223], v247 offset:36864\n"
        "s_add_u32 s86, s66, 0x380\n"
        "s_addc_u32 s87, s67, 0\n"
        "v_mfma_f32_16x16x32_bf16 v[152:155], v[32:35], v[204:207], v[152:155]\n"
        "ds_read_b128 v[204:207], v246 offset:36864\n"
        "s_waitcnt lgkmcnt(6)\n"
        "v_mfma_f32_16x16x32_bf16 v[0:3], v[48:51], v[192:195], v[0:3]\n"
        "ds_read_b64_tr_b16 v[32:33], v226 offset:17408\n"
        "v_accvgpr_read_b32 v184, a104\n"
        "v_mfma_f32_16x16x32_bf16 v[0:3], v[52:55], v[208:211], v[0:3]\n"
        "s_waitcnt lgkmcnt(6)\n"
        "v_mul_f32 v184, s88, v184\n"
        "v_cvt_pk_bf16_f32 v184, v184, v185\n"
        "v_mfma_f32_16x16x32_bf16 v[4:7], v[60:63], v[208:211], v[4:7]\n"
        "global_load_lds_dwordx4 v242, s[86:87] offset:0\n"
        "v_cvt_pk_bf16_f32 v185, v186, v187\n"
        "v_mfma_f32_16x16x32_bf16 v[4:7], v[56:59], v[192:195], v[4:7]\n"
        "ds_read_b64_tr_b16 v[34:35], v227 offset:17408\n"
        "v_cvt_pk_bf16_f32 v186, v188, v189\n"
        "v_mfma_f32_16x16x32_bf16 v[68:71], v[56:59], v[196:199], v[68:71]\n"
        "s_waitcnt lgkmcnt(4)\n"
        "v_cvt_pk_bf16_f32 v187, v190, v191\n"
        "v_accvgpr_write_b32 a4, v184\n"
        "v_mfma_f32_16x16x32_bf16 v[68:71], v[60:63], v[212:215], v[68:71]\n"
        "ds_read_b64_tr_b16 v[36:37], v226 offset:25600\n"
        "v_accvgpr_write_b32 a5, v185\n"
        "v_mfma_f32_16x16x32_bf16 v[64:67], v[52:55], v[212:215], v[64:67]\n"
        "ds_read_b64_tr_b16 v[38:39], v227 offset:25600\n"
        "v_accvgpr_write_b32 a6, v186\n"
        "v_mfma_f32_16x16x32_bf16 v[64:67], v[48:51], v[196:199], v[64:67]\n"
        "ds_read_b64_tr_b16 v[44:45], v228 offset:25600\n"
        "v_accvgpr_write_b32 a7, v187\n"
        "v_mfma_f32_16x16x32_bf16 v[96:99], v[48:51], v[200:203], v[96:99]\n"
        "ds_read_b64_tr_b16 v[46:47], v229 offset:25600\n"
        "global_load_lds_dwordx4 v243, s[86:87] offset:1024\n"
        "v_mfma_f32_16x16x32_bf16 v[96:99], v[52:55], v[216:219], v[96:99]\n"
        "ds_read_b64_tr_b16 v[42:43], v229 offset:17408\n"
        "v_accvgpr_read_b32 v182, a134\n"
        "v_mfma_f32_16x16x32_bf16 v[100:103], v[60:63], v[216:219], v[100:103]\n"
        "s_waitcnt lgkmcnt(6)\n"
        "v_accvgpr_read_b32 v183, a135\n"
        "v_mul_f32 v177, s88, v177\n"
        "v_mfma_f32_16x16x32_bf16 v[100:103], v[56:59], v[200:203], v[100:103]\n"
        "v_mul_f32 v178, s88, v178\n"
        "v_mul_f32 v179, s88, v179\n"
        "v_mfma_f32_16x16x32_bf16 v[132:135], v[56:59], v[204:207], v[132:135]\n"
        "global_load_lds_dwordx4 v244, s[86:87] offset:2048\n"
        "v_mfma_f32_16x16x32_bf16 v[132:135], v[60:63], v[220:223], v[132:135]\n"
        "ds_read_b64_tr_b16 v[60:61], v232 offset:25600\n"
        "v_mul_f32 v180, s88, v180\n"
        "v_mfma_f32_16x16x32_bf16 v[128:131], v[52:55], v[220:223], v[128:131]\n"
        "ds_read_b64_tr_b16 v[52:53], v230 offset:25600\n"
        "v_mul_f32 v181, s88, v181\n"
        "v_mfma_f32_16x16x32_bf16 v[128:131], v[48:51], v[204:207], v[128:131]\n"
        "ds_read_b64_tr_b16 v[48:49], v230 offset:17408\n"
        "v_mul_f32 v182, s88, v182\n"
        "v_mfma_f32_16x16x32_bf16 v[8:11], v[160:163], v[192:195], v[8:11]\n"
        "global_load_lds_dwordx4 v245, s[86:87] offset:3072\n"
        "s_add_u32 m0, s73, 0x1c000\n"
        "v_mfma_f32_16x16x32_bf16 v[8:11], v[164:167], v[208:211], v[8:11]\n"
        "ds_read_b64_tr_b16 v[50:51], v231 offset:17408\n"
        "v_mul_f32 v183, s88, v183\n"
        "v_mfma_f32_16x16x32_bf16 v[12:15], v[172:175], v[208:211], v[12:15]\n"
        "ds_read_b64_tr_b16 v[54:55], v231 offset:25600\n"
        "s_add_u32 s86, s68, 0x400000\n"
        "s_addc_u32 s87, s69, 0\n"
        "v_mfma_f32_16x16x32_bf16 v[12:15], v[168:171], v[192:195], v[12:15]\n"
        "ds_read_b64_tr_b16 v[62:63], v233 offset:25600\n"
        "v_accvgpr_read_b32 v185, a137\n"
        "v_mfma_f32_16x16x32_bf16 v[76:79], v[168:171], v[196:199], v[76:79]\n"
        "ds_read_b64_tr_b16 v[56:57], v232 offset:17408\n"
        "v_accvgpr_read_b32 v186, a138\n"
        "v_mfma_f32_16x16x32_bf16 v[76:79], v[172:175], v[212:215], v[76:79]\n"
        "global_store_dwordx4 v254, a[192:195], s[70:71] offset:0 sc1\n"
        "ds_read_b64_tr_b16 v[58:59], v233 offset:17408\n"
        "v_accvgpr_read_b32 v187, a139\n"
        "v_accvgpr_read_b32 v188, a140\n"
        "v_accvgpr_read_b32 v189, a141\n"
        "v_mfma_f32_16x16x32_bf16 v[72:75], v[164:167], v[212:215], v[72:75]\n"
        "v_accvgpr_read_b32 v190, a142\n"
        "v_accvgpr_read_b32 v191, a143\n"
        "v_mfma_f32_16x16x32_bf16 v[72:75], v[160:163], v[196:199], v[72:75]\n"
        "v_accvgpr_read_b32 v176, a128\n"
        "v_mul_f32 v176, s88, v176\n"
        "v_mfma_f32_16x16x32_bf16 v[104:107], v[160:163], v[200:203], v[104:107]\n"
        "v_cvt_pk_bf16_f32 v176, v176, v177\n"
        "v_cvt_pk_bf16_f32 v177, v178, v179\n"
        "v_mfma_f32_16x16x32_bf16 v[104:107], v[164:167], v[216:219], v[104:107]\n"
        "v_cvt_pk_bf16_f32 v178, v180, v181\n"
        "v_cvt_pk_bf16_f32 v179, v182, v183\n"
        "v_mfma_f32_16x16x32_bf16 v[108:111], v[172:175], v[216:219], v[108:111]\n"
        "v_accvgpr_write_b32 a8, v176\n"
        "v_accvgpr_write_b32 a9, v177\n"
        "v_mfma_f32_16x16x32_bf16 v[108:111], v[168:171], v[200:203], v[108:111]\n"
        "v_accvgpr_write_b32 a10, v178\n"
        "v_accvgpr_write_b32 a11, v179\n"
        "v_mfma_f32_16x16x32_bf16 v[140:143], v[168:171], v[204:207], v[140:143]\n"
        "s_waitcnt vmcnt(13) lgkmcnt(0)\n"
        "v_mfma_f32_16x16x32_bf16 v[140:143], v[172:175], v[220:223], v[140:143]\n"
        "s_barrier\n"
        "v_mfma_f32_16x16x32_bf16 v[136:139], v[164:167], v[220:223], v[136:139]\n"
        "global_load_lds_dwordx4 v238, s[86:87] offset:0\n"
        "ds_read_b64_tr_b16 v[164:165], v226 offset:40960\n"
        "v_mfma_f32_16x16x32_bf16 v[136:139], v[160:163], v[204:207], v[136:139]\n"
        "ds_read_b64_tr_b16 v[160:161], v226 offset:32768\n"
        "v_mul_f32 v185, s88, v185\n"
        "v_mfma_f32_16x16x32_bf16 v[16:19], v[32:35], v[192:195], v[16:19]\n"
        "ds_read_b64_tr_b16 v[162:163], v227 offset:32768\n"
        "v_mul_f32 v186, s88, v186\n"
        "v_mfma_f32_16x16x32_bf16 v[16:19], v[36:39], v[208:211], v[16:19]\n"
        "ds_read_b64_tr_b16 v[166:167], v227 offset:40960\n"
        "v_mul_f32 v187, s88, v187\n"
        "v_mfma_f32_16x16x32_bf16 v[20:23], v[44:47], v[208:211], v[20:23]\n"
        "ds_read_b64_tr_b16 v[172:173], v228 offset:40960\n"
        "global_load_lds_dwordx4 v239, s[86:87] offset:1024\n"
        "v_mfma_f32_16x16x32_bf16 v[20:23], v[40:43], v[192:195], v[20:23]\n"
        "ds_read_b64_tr_b16 v[174:175], v229 offset:40960\n"
        "v_mul_f32 v188, s88, v188\n"
        "v_mfma_f32_16x16x32_bf16 v[84:87], v[40:43], v[196:199], v[84:87]\n"
        "ds_read_b64_tr_b16 v[168:169], v228 offset:32768\n"
        "v_mul_f32 v189, s88, v189\n"
        "v_mfma_f32_16x16x32_bf16 v[84:87], v[44:47], v[212:215], v[84:87]\n"
        "ds_read_b64_tr_b16 v[170:171], v229 offset:32768\n"
        "v_mul_f32 v190, s88, v190\n"
        "v_mfma_f32_16x16x32_bf16 v[80:83], v[36:39], v[212:215], v[80:83]\n"
        "global_load_lds_dwordx4 v240, s[86:87] offset:2048\n"
        "v_mfma_f32_16x16x32_bf16 v[80:83], v[32:35], v[196:199], v[80:83]\n"
        "v_mul_f32 v191, s88, v191\n"
        "v_accvgpr_read_b32 v177, a209\n"
        "v_mfma_f32_16x16x32_bf16 v[112:115], v[32:35], v[200:203], v[112:115]\n"
        "v_accvgpr_read_b32 v178, a210\n"
        "v_accvgpr_read_b32 v179, a211\n"
        "v_mfma_f32_16x16x32_bf16 v[112:115], v[36:39], v[216:219], v[112:115]\n"
        "v_accvgpr_read_b32 v180, a240\n"
        "v_accvgpr_read_b32 v181, a241\n"
        "v_mfma_f32_16x16x32_bf16 v[116:119], v[44:47], v[216:219], v[116:119]\n"
        "global_load_lds_dwordx4 v241, s[86:87] offset:3072\n"
        "s_add_u32 m0, s72, 0x8000\n"
        "v_mfma_f32_16x16x32_bf16 v[116:119], v[40:43], v[200:203], v[116:119]\n"
        "s_add_u32 s86, s64, 0x400\n"
        "s_addc_u32 s87, s65, 0\n"
        "v_accvgpr_read_b32 v182, a242\n"
        "v_mfma_f32_16x16x32_bf16 v[148:151], v[40:43], v[204:207], v[148:151]\n"
        "ds_read_b64_tr_b16 v[40:41], v232 offset:32768\n"
        "v_accvgpr_read_b32 v183, a243\n"
        "v_mfma_f32_16x16x32_bf16 v[148:151], v[44:47], v[220:223], v[148:151]\n"
        "ds_read_b64_tr_b16 v[44:45], v232 offset:40960\n"
        "v_mfma_f32_16x16x32_bf16 v[144:147], v[36:39], v[220:223], v[144:147]\n"
        "global_load_lds_dwordx4 v242, s[86:87] offset:0\n"
        "v_mfma_f32_16x16x32_bf16 v[144:147], v[32:35], v[204:207], v[144:147]\n"
        "ds_read_b64_tr_b16 v[32:33], v230 offset:32768\n"
        "v_accvgpr_read_b32 v184, a136\n"
        "v_mfma_f32_16x16x32_bf16 v[24:27], v[48:51], v[192:195], v[24:27]\n"
        "ds_read_b64_tr_b16 v[34:35], v231 offset:32768\n"
        "v_mul_f32 v184, s88, v184\n"
        "v_mfma_f32_16x16x32_bf16 v[24:27], v[52:55], v[208:211], v[24:27]\n"
        "ds_read_b64_tr_b16 v[36:37], v230 offset:40960\n"
        "v_cvt_pk_bf16_f32 v184, v184, v185\n"
        "v_mfma_f32_16x16x32_bf16 v[28:31], v[60:63], v[208:211], v[28:31]\n"
        "ds_read_b64_tr_b16 v[38:39], v231 offset:40960\n"
        "v_cvt_pk_bf16_f32 v185, v186, v187\n"
        "v_mfma_f32_16x16x32_bf16 v[28:31], v[56:59], v[192:195], v[28:31]\n"
        "ds_read_b64_tr_b16 v[46:47], v233 offset:40960\n"
        "v_cvt_pk_bf16_f32 v186, v188, v189\n"
        "v_mfma_f32_16x16x32_bf16 v[92:95], v[56:59], v[196:199], v[92:95]\n"
        "ds_read_b64_tr_b16 v[42:43], v233 offset:32768\n"
        "v_cvt_pk_bf16_f32 v187, v190, v191\n"
        "v_mfma_f32_16x16x32_bf16 v[92:95], v[60:63], v[212:215], v[92:95]\n"
        "ds_read_b128 v[192:195], v246 offset:2048\n"
        "v_accvgpr_write_b32 a12, v184\n"
        "v_mfma_f32_16x16x32_bf16 v[88:91], v[52:55], v[212:215], v[88:91]\n"
        "ds_read_b128 v[208:211], v247 offset:2048\n"
        "v_accvgpr_write_b32 a13, v185\n"
        "v_mfma_f32_16x16x32_bf16 v[88:91], v[48:51], v[196:199], v[88:91]\n"
        "global_load_lds_dwordx4 v243, s[86:87] offset:1024\n"
        "v_accvgpr_write_b32 a14, v186\n"
        "v_mfma_f32_16x16x32_bf16 v[120:123], v[48:51], v[200:203], v[120:123]\n"
        "ds_read_b128 v[196:199], v246 offset:0\n"
        "v_accvgpr_write_b32 a15, v187\n"
        "v_mfma_f32_16x16x32_bf16 v[120:123], v[52:55], v[216:219], v[120:123]\n"
        "ds_read_b128 v[212:215], v247 offset:0\n"
        "v_accvgpr_read_b32 v185, a213\n"
        "v_mfma_f32_16x16x32_bf16 v[124:127], v[60:63], v[216:219], v[124:127]\n"
        "ds_read_b128 v[216:219], v247 offset:6144\n"
        "v_accvgpr_read_b32 v186, a214\n"
        "v_mfma_f32_16x16x32_bf16 v[124:127], v[56:59], v[200:203], v[124:127]\n"
        "global_load_lds_dwordx4 v244, s[86:87] offset:2048\n"
        "ds_read_b128 v[200:203], v246 offset:6144\n"
        "v_mfma_f32_16x16x32_bf16 v[156:159], v[56:59], v[204:207], v[156:159]\n"
        "ds_read_b64_tr_b16 v[56:57], v228 offset:33792\n"
        "v_accvgpr_read_b32 v187, a215\n"
        "v_mfma_f32_16x16x32_bf16 v[156:159], v[60:63], v[220:223], v[156:159]\n"
        "ds_read_b64_tr_b16 v[60:61], v228 offset:41984\n"
        "v_accvgpr_read_b32 v188, a244\n"
        "v_mfma_f32_16x16x32_bf16 v[152:155], v[52:55], v[220:223], v[152:155]\n"
        "ds_read_b128 v[220:223], v247 offset:4096\n"
        "LHA6_%=:\n"
        "s_cbranch_execz LHA10_%=\n"
        "v_accvgpr_read_b32 v189, a245\n"
        "v_mfma_f32_16x16x32_bf16 v[152:155], v[48:51], v[204:207], v[152:155]\n"
        "global_load_lds_dwordx4 v245, s[86:87] offset:3072\n"
        "s_add_u32 m0, s72, 0x9000\n"
        "s_waitcnt lgkmcnt(7)\n"
        "v_mfma_f32_16x16x32_bf16 v[0:3], v[160:163], v[192:195], v[0:3]\n"
        "ds_read_b128 v[204:207], v246 offset:4096\n"
        "s_add_u32 s86, s66, 0x400\n"
        "s_addc_u32 s87, s67, 0\n"
        "v_mfma_f32_16x16x32_bf16 v[0:3], v[164:167], v[208:211], v[0:3]\n"
        "ds_read_b64_tr_b16 v[48:49], v226 offset:33792\n"
        "v_accvgpr_read_b32 v190, a246\n"
        "v_mfma_f32_16x16x32_bf16 v[4:7], v[172:175], v[208:211], v[4:7]\n"
        "ds_read_b64_tr_b16 v[50:51], v227 offset:33792\n"
        "v_accvgpr_read_b32 v191, a247\n"
        "v_mfma_f32_16x16x32_bf16 v[4:7], v[168:171], v[192:195], v[4:7]\n"
        "ds_read_b64_tr_b16 v[52:53], v226 offset:41984\n"
        "s_waitcnt lgkmcnt(7)\n"
        "v_mfma_f32_16x16x32_bf16 v[68:71], v[168:171], v[196:199], v[68:71]\n"
        "ds_read_b64_tr_b16 v[54:55], v227 offset:41984\n"
        "global_load_lds_dwordx4 v242, s[86:87] offset:0\n"
        "v_mfma_f32_16x16x32_bf16 v[68:71], v[172:175], v[212:215], v[68:71]\n"
        "ds_read_b64_tr_b16 v[62:63], v229 offset:41984\n"
        "v_mfma_f32_16x16x32_bf16 v[64:67], v[164:167], v[212:215], v[64:67]\n"
        "ds_read_b64_tr_b16 v[58:59], v229 offset:33792\n"
        "v_mfma_f32_16x16x32_bf16 v[64:67], v[160:163], v[196:199], v[64:67]\n"
        "v_accvgpr_read_b32 v176, a208\n"
        "v_cvt_pk_bf16_f32 v176, v176, v177\n"
        "v_mfma_f32_16x16x32_bf16 v[96:99], v[160:163], v[200:203], v[96:99]\n"
        "global_load_lds_dwordx4 v243, s[86:87] offset:1024\n"
        "v_mfma_f32_16x16x32_bf16 v[96:99], v[164:167], v[216:219], v[96:99]\n"
        "s_waitcnt lgkmcnt(5)\n"
        "v_cvt_pk_bf16_f32 v177, v178, v179\n"
        "v_cvt_pk_bf16_f32 v178, v180, v181\n"
        "v_mfma_f32_16x16x32_bf16 v[100:103], v[172:175], v[216:219], v[100:103]\n"
        "v_cvt_pk_bf16_f32 v179, v182, v183\n"
        "v_accvgpr_write_b32 a208, v176\n"
        "v_mfma_f32_16x16x32_bf16 v[100:103], v[168:171], v[200:203], v[100:103]\n"
        "v_accvgpr_write_b32 a209, v177\n"
        "v_accvgpr_write_b32 a210, v178\n"
        "v_mfma_f32_16x16x32_bf16 v[132:135], v[168:171], v[204:207], v[132:135]\n"
        "ds_read_b64_tr_b16 v[168:169], v232 offset:33792\n"
        "v_accvgpr_write_b32 a211, v179\n"
        "v_mfma_f32_16x16x32_bf16 v[132:135], v[172:175], v[220:223], v[132:135]\n"
        "global_store_dwordx4 v254, a[196:199], s[70:71] offset:1024 sc1\n"
        "ds_read_b64_tr_b16 v[172:173], v232 offset:41984\n"
        "ds_read_b64_tr_b16 v[174:175], v233 offset:41984\n"
        "v_accvgpr_read_b32 v177, a217\n"
        "v_mfma_f32_16x16x32_bf16 v[128:131], v[164:167], v[220:223], v[128:131]\n"
        "ds_read_b64_tr_b16 v[164:165], v230 offset:41984\n"
        "v_accvgpr_read_b32 v178, a218\n"
        "v_mfma_f32_16x16x32_bf16 v[128:131], v[160:163], v[204:207], v[128:131]\n"
        "ds_read_b64_tr_b16 v[160:161], v230 offset:33792\n"
        "v_accvgpr_read_b32 v179, a219\n"
        "v_mfma_f32_16x16x32_bf16 v[8:11], v[32:35], v[192:195], v[8:11]\n"
        "global_load_lds_dwordx4 v244, s[86:87] offset:2048\n"
        "v_mfma_f32_16x16x32_bf16 v[8:11], v[36:39], v[208:211], v[8:11]\n"
        "ds_read_b64_tr_b16 v[162:163], v231 offset:33792\n"
        "v_accvgpr_read_b32 v180, a248\n"
        "v_mfma_f32_16x16x32_bf16 v[12:15], v[44:47], v[208:211], v[12:15]\n"
        "ds_read_b64_tr_b16 v[166:167], v231 offset:41984\n"
        "v_accvgpr_read_b32 v181, a249\n"
        "v_mfma_f32_16x16x32_bf16 v[12:15], v[40:43], v[192:195], v[12:15]\n"
        "ds_read_b64_tr_b16 v[170:171], v233 offset:33792\n"
        "v_accvgpr_read_b32 v182, a250\n"
        "v_mfma_f32_16x16x32_bf16 v[76:79], v[40:43], v[196:199], v[76:79]\n"
        "global_load_lds_dwordx4 v245, s[86:87] offset:3072\n"
        "s_add_u32 m0, s73, 0x20000\n"
        "v_mfma_f32_16x16x32_bf16 v[76:79], v[44:47], v[212:215], v[76:79]\n"
        "s_add_u32 s86, s68, 0x480000\n"
        "s_addc_u32 s87, s69, 0\n"
        "v_accvgpr_read_b32 v183, a251\n"
        "v_mfma_f32_16x16x32_bf16 v[72:75], v[36:39], v[212:215], v[72:75]\n"
        "s_add_u32 s70, s38, s97\n"
        "s_addc_u32 s71, s39, 0\n"
        "s_add_u32 s97, s52, 0x1000\n"
        "v_mfma_f32_16x16x32_bf16 v[72:75], v[32:35], v[196:199], v[72:75]\n"
        "s_add_u32 s97, s97, s75\n"
        "v_mfma_f32_16x16x32_bf16 v[104:107], v[32:35], v[200:203], v[104:107]\n"
        "v_mfma_f32_16x16x32_bf16 v[104:107], v[36:39], v[216:219], v[104:107]\n"
        "v_mfma_f32_16x16x32_bf16 v[108:111], v[44:47], v[216:219], v[108:111]\n"
        "v_mfma_f32_16x16x32_bf16 v[108:111], v[40:43], v[200:203], v[108:111]\n"
        "s_waitcnt vmcnt(14) lgkmcnt(0)\n"
        "v_mfma_f32_16x16x32_bf16 v[140:143], v[40:43], v[204:207], v[140:143]\n"
        "s_barrier\n"
        "v_mfma_f32_16x16x32_bf16 v[140:143], v[44:47], v[220:223], v[140:143]\n"
        "global_load_lds_dwordx4 v238, s[86:87] offset:0\n"
        "ds_read_b64_tr_b16 v[44:45], v228 offset:57344\n"
        "v_mfma_f32_16x16x32_bf16 v[136:139], v[36:39], v[220:223], v[136:139]\n"
        "ds_read_b64_tr_b16 v[36:37], v226 offset:57344\n"
        "v_mfma_f32_16x16x32_bf16 v[136:139], v[32:35], v[204:207], v[136:139]\n"
        "ds_read_b64_tr_b16 v[32:33], v226 offset:49152\n"
        "v_accvgpr_read_b32 v184, a212\n"
        "v_mfma_f32_16x16x32_bf16 v[16:19], v[48:51], v[192:195], v[16:19]\n"
        "ds_read_b64_tr_b16 v[34:35], v227 offset:49152\n"
        "v_cvt_pk_bf16_f32 v184, v184, v185\n"
        "v_mfma_f32_16x16x32_bf16 v[16:19], v[52:55], v[208:211], v[16:19]\n"
        "ds_read_b64_tr_b16 v[38:39], v227 offset:57344\n"
        "v_cvt_pk_bf16_f32 v185, v186, v187\n"
        "v_mfma_f32_16x16x32_bf16 v[20:23], v[60:63], v[208:211], v[20:23]\n"
        "ds_read_b64_tr_b16 v[46:47], v229 offset:57344\n"
        "v_cvt_pk_bf16_f32 v186, v188, v189\n"
        "v_mfma_f32_16x16x32_bf16 v[20:23], v[56:59], v[192:195], v[20:23]\n"
        "ds_read_b64_tr_b16 v[40:41], v228 offset:49152\n"
        "v_cvt_pk_bf16_f32 v187, v190, v191\n"
        "v_mfma_f32_16x16x32_bf16 v[84:87], v[56:59], v[196:199], v[84:87]\n"
        "ds_read_b64_tr_b16 v[42:43], v229 offset:49152\n"
        "v_accvgpr_write_b32 a240, v184\n"
        "v_mfma_f32_16x16x32_bf16 v[84:87], v[60:63], v[212:215], v[84:87]\n"
        "global_load_lds_dwordx4 v239, s[86:87] offset:1024\n"
        "v_accvgpr_write_b32 a241, v185\n"
        "v_mfma_f32_16x16x32_bf16 v[80:83], v[52:55], v[212:215], v[80:83]\n"
        "v_accvgpr_write_b32 a242, v186\n"
        "v_accvgpr_write_b32 a243, v187\n"
        "v_mfma_f32_16x16x32_bf16 v[80:83], v[48:51], v[196:199], v[80:83]\n"
        "v_accvgpr_read_b32 v185, a221\n"
        "v_accvgpr_read_b32 v186, a222\n"
        "v_mfma_f32_16x16x32_bf16 v[112:115], v[48:51], v[200:203], v[112:115]\n"
        "v_accvgpr_read_b32 v187, a223\n"
        "v_accvgpr_read_b32 v188, a252\n"
        "v_mfma_f32_16x16x32_bf16 v[112:115], v[52:55], v[216:219], v[112:115]\n"
        "global_load_lds_dwordx4 v240, s[86:87] offset:2048\n"
        "v_accvgpr_read_b32 v189, a253\n"
        "v_mfma_f32_16x16x32_bf16 v[116:119], v[60:63], v[216:219], v[116:119]\n"
        "v_accvgpr_read_b32 v190, a254\n"
        "v_accvgpr_read_b32 v191, a255\n"
        "v_mfma_f32_16x16x32_bf16 v[116:119], v[56:59], v[200:203], v[116:119]\n"
        "v_mfma_f32_16x16x32_bf16 v[148:151], v[56:59], v[204:207], v[148:151]\n"
        "ds_read_b64_tr_b16 v[56:57], v232 offset:49152\n"
        "v_mfma_f32_16x16x32_bf16 v[148:151], v[60:63], v[220:223], v[148:151]\n"
        "global_load_lds_dwordx4 v241, s[86:87] offset:3072\n"
        "ds_read_b64_tr_b16 v[60:61], v232 offset:57344\n"
        "s_add_u32 m0, s72, 0x0\n"
        "v_mfma_f32_16x16x32_bf16 v[144:147], v[52:55], v[220:223], v[144:147]\n"
        "ds_read_b64_tr_b16 v[52:53], v230 offset:57344\n"
        "s_add_u32 s86, s64, 0x480\n"
        "s_addc_u32 s87, s65, 0\n"
        "v_mfma_f32_16x16x32_bf16 v[144:147], v[48:51], v[204:207], v[144:147]\n"
        "ds_read_b64_tr_b16 v[48:49], v230 offset:49152\n"
        "v_mfma_f32_16x16x32_bf16 v[24:27], v[160:163], v[192:195], v[24:27]\n"
        "ds_read_b64_tr_b16 v[50:51], v231 offset:49152\n"
        "v_mfma_f32_16x16x32_bf16 v[24:27], v[164:167], v[208:211], v[24:27]\n"
        "ds_read_b64_tr_b16 v[54:55], v231 offset:57344\n"
        "global_load_lds_dwordx4 v242, s[86:87] offset:0\n"
        "v_mfma_f32_16x16x32_bf16 v[28:31], v[172:175], v[208:211], v[28:31]\n"
        "ds_read_b64_tr_b16 v[62:63], v233 offset:57344\n"
        "v_mfma_f32_16x16x32_bf16 v[28:31], v[168:171], v[192:195], v[28:31]\n"
        "ds_read_b64_tr_b16 v[58:59], v233 offset:49152\n"
        "v_mfma_f32_16x16x32_bf16 v[92:95], v[168:171], v[196:199], v[92:95]\n"
        "ds_read_b128 v[192:195], v248 offset:2048\n"
        "v_mfma_f32_16x16x32_bf16 v[92:95], v[172:175], v[212:215], v[92:95]\n"
        "ds_read_b128 v[208:211], v249 offset:2048\n"
        "global_load_lds_dwordx4 v243, s[86:87] offset:1024\n"
        "v_mfma_f32_16x16x32_bf16 v[88:91], v[164:167], v[212:215], v[88:91]\n"
        "ds_read_b128 v[212:215], v249 offset:0\n"
        "v_mfma_f32_16x16x32_bf16 v[88:91], v[160:163], v[196:199], v[88:91]\n"
        "ds_read_b128 v[196:199], v248 offset:0\n"
        "v_accvgpr_read_b32 v176, a216\n"
        "v_mfma_f32_16x16x32_bf16 v[120:123], v[160:163], v[200:203], v[120:123]\n"
        "v_cvt_pk_bf16_f32 v176, v176, v177\n"
        "v_cvt_pk_bf16_f32 v177, v178, v179\n"
        "v_mfma_f32_16x16x32_bf16 v[120:123], v[164:167], v[216:219], v[120:123]\n"
        "v_cvt_pk_bf16_f32 v178, v180, v181\n"
        "v_cvt_pk_bf16_f32 v179, v182, v183\n"
        "v_mfma_f32_16x16x32_bf16 v[124:127], v[172:175], v[216:219], v[124:127]\n"
        "global_load_lds_dwordx4 v244, s[86:87] offset:2048\n"
        "v_accvgpr_write_b32 a212, v176\n"
        "v_mfma_f32_16x16x32_bf16 v[124:127], v[168:171], v[200:203], v[124:127]\n"
        "ds_read_b128 v[200:203], v248 offset:6144\n"
        "v_accvgpr_write_b32 a213, v177\n"
        "v_mfma_f32_16x16x32_bf16 v[156:159], v[168:171], v[204:207], v[156:159]\n"
        "s_waitcnt lgkmcnt(4)\n"
        "v_accvgpr_write_b32 a214, v178\n"
        "v_accvgpr_write_b32 a215, v179\n"
        "v_mfma_f32_16x16x32_bf16 v[156:159], v[172:175], v[220:223], v[156:159]\n"
        "ds_read_b128 v[216:219], v249 offset:6144\n"
        "v_accvgpr_read_b32 v177, a145\n"
        "v_mfma_f32_16x16x32_bf16 v[152:155], v[164:167], v[220:223], v[152:155]\n"
        "global_load_lds_dwordx4 v245, s[86:87] offset:3072\n"
        "ds_read_b128 v[220:223], v249 offset:4096\n"
        "s_add_u32 m0, s72, 0x1000\n"
        "v_mfma_f32_16x16x32_bf16 v[152:155], v[160:163], v[204:207], v[152:155]\n"
        "ds_read_b128 v[204:207], v248 offset:4096\n"
        "s_add_u32 s86, s66, 0x480\n"
        "s_addc_u32 s87, s67, 0\n"
        "v_mfma_f32_16x16x32_bf16 v[0:3], v[32:35], v[192:195], v[0:3]\n"
        "ds_read_b64_tr_b16 v[160:161], v226 offset:50176\n"
        "s_waitcnt lgkmcnt(5)\n"
        "v_mfma_f32_16x16x32_bf16 v[0:3], v[36:39], v[208:211], v[0:3]\n"
        "ds_read_b64_tr_b16 v[162:163], v227 offset:50176\n"
        "v_accvgpr_read_b32 v178, a146\n"
        "v_mfma_f32_16x16x32_bf16 v[4:7], v[44:47], v[208:211], v[4:7]\n"
        "ds_read_b64_tr_b16 v[164:165], v226 offset:58368\n"
        "global_load_lds_dwordx4 v242, s[86:87] offset:0\n"
        "v_mfma_f32_16x16x32_bf16 v[4:7], v[40:43], v[192:195], v[4:7]\n"
        "ds_read_b64_tr_b16 v[166:167], v227 offset:58368\n"
        "v_accvgpr_read_b32 v179, a147\n"
        "v_mfma_f32_16x16x32_bf16 v[68:71], v[40:43], v[196:199], v[68:71]\n"
        "ds_read_b64_tr_b16 v[172:173], v228 offset:58368\n"
        "v_accvgpr_read_b32 v180, a176\n"
        "v_mfma_f32_16x16x32_bf16 v[68:71], v[44:47], v[212:215], v[68:71]\n"
        "ds_read_b64_tr_b16 v[174:175], v229 offset:58368\n"
        "v_accvgpr_read_b32 v181, a177\n"
        "v_mfma_f32_16x16x32_bf16 v[64:67], v[36:39], v[212:215], v[64:67]\n"
        "ds_read_b64_tr_b16 v[168:169], v228 offset:50176\n"
        "global_load_lds_dwordx4 v243, s[86:87] offset:1024\n"
        "v_mfma_f32_16x16x32_bf16 v[64:67], v[32:35], v[196:199], v[64:67]\n"
        "ds_read_b64_tr_b16 v[170:171], v229 offset:50176\n"
        "s_waitcnt lgkmcnt(8)\n"
        "v_mfma_f32_16x16x32_bf16 v[96:99], v[32:35], v[200:203], v[96:99]\n"
        "v_accvgpr_read_b32 v182, a178\n"
        "v_accvgpr_read_b32 v183, a179\n"
        "v_mfma_f32_16x16x32_bf16 v[96:99], v[36:39], v[216:219], v[96:99]\n"
        "v_mfma_f32_16x16x32_bf16 v[100:103], v[44:47], v[216:219], v[100:103]\n"
        "global_load_lds_dwordx4 v244, s[86:87] offset:2048\n"
        "v_mfma_f32_16x16x32_bf16 v[100:103], v[40:43], v[200:203], v[100:103]\n"
        "v_mfma_f32_16x16x32_bf16 v[132:135], v[40:43], v[204:207], v[132:135]\n"
        "ds_read_b64_tr_b16 v[40:41], v232 offset:50176\n"
        "v_mfma_f32_16x16x32_bf16 v[132:135], v[44:47], v[220:223], v[132:135]\n"
        "ds_read_b64_tr_b16 v[44:45], v232 offset:58368\n"
        "v_mfma_f32_16x16x32_bf16 v[128:131], v[36:39], v[220:223], v[128:131]\n"
        "global_load_lds_dwordx4 v245, s[86:87] offset:3072\n"
        "s_add_u32 m0, s73, 0x24000\n"
        "v_mfma_f32_16x16x32_bf16 v[128:131], v[32:35], v[204:207], v[128:131]\n"
        "ds_read_b64_tr_b16 v[32:33], v230 offset:50176\n"
        "v_accvgpr_read_b32 v184, a220\n"
        "v_mfma_f32_16x16x32_bf16 v[8:11], v[48:51], v[192:195], v[8:11]\n"
        "ds_read_b64_tr_b16 v[34:35], v231 offset:50176\n"
        "v_cvt_pk_bf16_f32 v184, v184, v185\n"
        "v_mfma_f32_16x16x32_bf16 v[8:11], v[52:55], v[208:211], v[8:11]\n"
        "ds_read_b64_tr_b16 v[36:37], v230 offset:58368\n"
        "v_cvt_pk_bf16_f32 v185, v186, v187\n"
        "v_mfma_f32_16x16x32_bf16 v[12:15], v[60:63], v[208:211], v[12:15]\n"
        "ds_read_b64_tr_b16 v[38:39], v231 offset:58368\n"
        "v_cvt_pk_bf16_f32 v186, v188, v189\n"
        "v_mfma_f32_16x16x32_bf16 v[12:15], v[56:59], v[192:195], v[12:15]\n"
        "ds_read_b64_tr_b16 v[46:47], v233 offset:58368\n"
        "v_cvt_pk_bf16_f32 v187, v190, v191\n"
        "v_mfma_f32_16x16x32_bf16 v[76:79], v[56:59], v[196:199], v[76:79]\n"
        "ds_read_b64_tr_b16 v[42:43], v233 offset:50176\n"
        "v_accvgpr_write_b32 a244, v184\n"
        "v_mfma_f32_16x16x32_bf16 v[76:79], v[60:63], v[212:215], v[76:79]\n"
        "s_add_u32 s86, s68, 0x500000\n"
        "s_addc_u32 s87, s69, 0\n"
        "v_accvgpr_write_b32 a245, v185\n"
        "v_mfma_f32_16x16x32_bf16 v[72:75], v[52:55], v[212:215], v[72:75]\n"
        "v_accvgpr_write_b32 a246, v186\n"
        "v_accvgpr_write_b32 a247, v187\n"
        "v_mfma_f32_16x16x32_bf16 v[72:75], v[48:51], v[196:199], v[72:75]\n"
        "global_store_dwordx4 v254, a[200:203], s[70:71] offset:0 sc1\n"
        "v_accvgpr_read_b32 v185, a149\n"
        "v_accvgpr_read_b32 v186, a150\n"
        "v_accvgpr_read_b32 v187, a151\n"
        "v_accvgpr_read_b32 v188, a180\n"
        "v_mfma_f32_16x16x32_bf16 v[104:107], v[48:51], v[200:203], v[104:107]\n"
        "v_accvgpr_read_b32 v189, a181\n"
        "v_accvgpr_read_b32 v190, a182\n"
        "v_mfma_f32_16x16x32_bf16 v[104:107], v[52:55], v[216:219], v[104:107]\n"
        "v_accvgpr_read_b32 v191, a183\n"
        "v_mfma_f32_16x16x32_bf16 v[108:111], v[60:63], v[216:219], v[108:111]\n"
        "v_mfma_f32_16x16x32_bf16 v[108:111], v[56:59], v[200:203], v[108:111]\n"
        "s_waitcnt vmcnt(13) lgkmcnt(0)\n"
        "v_mfma_f32_16x16x32_bf16 v[140:143], v[56:59], v[204:207], v[140:143]\n"
        "s_barrier\n"
        "v_mfma_f32_16x16x32_bf16 v[140:143], v[60:63], v[220:223], v[140:143]\n"
        "global_load_lds_dwordx4 v238, s[86:87] offset:0\n"
        "ds_read_b64_tr_b16 v[60:61], v228 offset:24576\n"
        "v_mfma_f32_16x16x32_bf16 v[136:139], v[52:55], v[220:223], v[136:139]\n"
        "ds_read_b64_tr_b16 v[52:53], v226 offset:24576\n"
        "v_mfma_f32_16x16x32_bf16 v[136:139], v[48:51], v[204:207], v[136:139]\n"
        "ds_read_b64_tr_b16 v[48:49], v226 offset:16384\n"
        "v_mfma_f32_16x16x32_bf16 v[16:19], v[160:163], v[192:195], v[16:19]\n"
        "ds_read_b64_tr_b16 v[50:51], v227 offset:16384\n"
        "v_mfma_f32_16x16x32_bf16 v[16:19], v[164:167], v[208:211], v[16:19]\n"
        "ds_read_b64_tr_b16 v[54:55], v227 offset:24576\n"
        "global_load_lds_dwordx4 v239, s[86:87] offset:1024\n"
        "v_mfma_f32_16x16x32_bf16 v[20:23], v[172:175], v[208:211], v[20:23]\n"
        "ds_read_b64_tr_b16 v[62:63], v229 offset:24576\n"
        "v_mfma_f32_16x16x32_bf16 v[20:23], v[168:171], v[192:195], v[20:23]\n"
        "ds_read_b64_tr_b16 v[56:57], v228 offset:16384\n"
        "v_mfma_f32_16x16x32_bf16 v[84:87], v[168:171], v[196:199], v[84:87]\n"
        "ds_read_b64_tr_b16 v[58:59], v229 offset:16384\n"
        "v_mfma_f32_16x16x32_bf16 v[84:87], v[172:175], v[212:215], v[84:87]\n"
        "global_load_lds_dwordx4 v240, s[86:87] offset:2048\n"
        "v_mfma_f32_16x16x32_bf16 v[80:83], v[164:167], v[212:215], v[80:83]\n"
        "v_mfma_f32_16x16x32_bf16 v[80:83], v[160:163], v[196:199], v[80:83]\n"
        "v_accvgpr_read_b32 v176, a144\n"
        "v_cvt_pk_bf16_f32 v176, v176, v177\n"
        "v_mfma_f32_16x16x32_bf16 v[112:115], v[160:163], v[200:203], v[112:115]\n"
        "v_cvt_pk_bf16_f32 v177, v178, v179\n"
        "v_cvt_pk_bf16_f32 v178, v180, v181\n"
        "v_mfma_f32_16x16x32_bf16 v[112:115], v[164:167], v[216:219], v[112:115]\n"
        "v_cvt_pk_bf16_f32 v179, v182, v183\n"
        "v_accvgpr_write_b32 a216, v176\n"
        "v_mfma_f32_16x16x32_bf16 v[116:119], v[172:175], v[216:219], v[116:119]\n"
        "global_load_lds_dwordx4 v241, s[86:87] offset:3072\n"
        "s_add_u32 m0, s72, 0x10000\n"
        "v_accvgpr_write_b32 a217, v177\n"
        "v_mfma_f32_16x16x32_bf16 v[116:119], v[168:171], v[200:203], v[116:119]\n"
        "s_add_u32 s86, s64, 0x500\n"
        "s_addc_u32 s87, s65, 0\n"
        "v_accvgpr_write_b32 a218, v178\n"
        "v_mfma_f32_16x16x32_bf16 v[148:151], v[168:171], v[204:207], v[148:151]\n"
        "ds_read_b64_tr_b16 v[168:169], v232 offset:16384\n"
        "v_accvgpr_write_b32 a219, v179\n"
        "v_mfma_f32_16x16x32_bf16 v[148:151], v[172:175], v[220:223], v[148:151]\n"
        "ds_read_b64_tr_b16 v[172:173], v232 offset:24576\n"
        "v_accvgpr_read_b32 v177, a153\n"
        "v_mfma_f32_16x16x32_bf16 v[144:147], v[164:167], v[220:223], v[144:147]\n"
        "ds_read_b64_tr_b16 v[164:165], v230 offset:24576\n"
        "global_load_lds_dwordx4 v242, s[86:87] offset:0\n"
        "v_mfma_f32_16x16x32_bf16 v[144:147], v[160:163], v[204:207], v[144:147]\n"
        "ds_read_b64_tr_b16 v[160:161], v230 offset:16384\n"
        "v_accvgpr_read_b32 v178, a154\n"
        "v_mfma_f32_16x16x32_bf16 v[24:27], v[32:35], v[192:195], v[24:27]\n"
        "ds_read_b64_tr_b16 v[162:163], v231 offset:16384\n"
        "v_accvgpr_read_b32 v179, a155\n"
        "v_mfma_f32_16x16x32_bf16 v[24:27], v[36:39], v[208:211], v[24:27]\n"
        "ds_read_b64_tr_b16 v[166:167], v231 offset:24576\n"
        "v_accvgpr_read_b32 v180, a184\n"
        "v_mfma_f32_16x16x32_bf16 v[28:31], v[44:47], v[208:211], v[28:31]\n"
        "ds_read_b64_tr_b16 v[174:175], v233 offset:24576\n"
        "global_load_lds_dwordx4 v243, s[86:87] offset:1024\n"
        "v_mfma_f32_16x16x32_bf16 v[28:31], v[40:43], v[192:195], v[28:31]\n"
        "ds_read_b64_tr_b16 v[170:171], v233 offset:16384\n"
        "v_accvgpr_read_b32 v181, a185\n"
        "v_mfma_f32_16x16x32_bf16 v[92:95], v[40:43], v[196:199], v[92:95]\n"
        "ds_read_b128 v[192:195], v246 offset:34816\n"
        "v_accvgpr_read_b32 v182, a186\n"
        "v_mfma_f32_16x16x32_bf16 v[92:95], v[44:47], v[212:215], v[92:95]\n"
        "ds_read_b128 v[208:211], v247 offset:34816\n"
        "v_accvgpr_read_b32 v183, a187\n"
        "v_mfma_f32_16x16x32_bf16 v[88:91], v[36:39], v[212:215], v[88:91]\n"
        "ds_read_b128 v[212:215], v247 offset:32768\n"
        "global_load_lds_dwordx4 v244, s[86:87] offset:2048\n"
        "v_mfma_f32_16x16x32_bf16 v[88:91], v[32:35], v[196:199], v[88:91]\n"
        "ds_read_b128 v[196:199], v246 offset:32768\n"
        "v_mfma_f32_16x16x32_bf16 v[120:123], v[32:35], v[200:203], v[120:123]\n"
        "v_mfma_f32_16x16x32_bf16 v[120:123], v[36:39], v[216:219], v[120:123]\n"
        "v_mfma_f32_16x16x32_bf16 v[124:127], v[44:47], v[216:219], v[124:127]\n"
        "ds_read_b128 v[216:219], v247 offset:38912\n"
        "global_load_lds_dwordx4 v245, s[86:87] offset:3072\n"
        "s_add_u32 m0, s72, 0x11000\n"
        "v_mfma_f32_16x16x32_bf16 v[124:127], v[40:43], v[200:203], v[124:127]\n"
        "ds_read_b128 v[200:203], v246 offset:38912\n"
        "s_add_u32 s86, s66, 0x500\n"
        "s_addc_u32 s87, s67, 0\n"
        "v_mfma_f32_16x16x32_bf16 v[156:159], v[40:43], v[204:207], v[156:159]\n"
        "ds_read_b64_tr_b16 v[40:41], v228 offset:17408\n"
        "s_waitcnt lgkmcnt(6)\n"
        "v_mfma_f32_16x16x32_bf16 v[156:159], v[44:47], v[220:223], v[156:159]\n"
        "ds_read_b64_tr_b16 v[44:45], v228 offset:25600\n"
        "s_waitcnt lgkmcnt(6)\n"
        "v_mfma_f32_16x16x32_bf16 v[152:155], v[36:39], v[220:223], v[152:155]\n"
        "ds_read_b128 v[220:223], v247 offset:36864\n"
        "global_load_lds_dwordx4 v242, s[86:87] offset:0\n"
        "v_mfma_f32_16x16x32_bf16 v[152:155], v[32:35], v[204:207], v[152:155]\n"
        "ds_read_b128 v[204:207], v246 offset:36864\n"
        "v_accvgpr_read_b32 v184, a148\n"
        "v_mfma_f32_16x16x32_bf16 v[0:3], v[48:51], v[192:195], v[0:3]\n"
        "s_waitcnt lgkmcnt(6)\n"
        "v_cvt_pk_bf16_f32 v184, v184, v185\n"
        "v_cvt_pk_bf16_f32 v185, v186, v187\n"
        "v_mfma_f32_16x16x32_bf16 v[0:3], v[52:55], v[208:211], v[0:3]\n"
        "ds_read_b64_tr_b16 v[32:33], v226 offset:17408\n"
        "v_cvt_pk_bf16_f32 v186, v188, v189\n"
        "v_mfma_f32_16x16x32_bf16 v[4:7], v[60:63], v[208:211], v[4:7]\n"
        "ds_read_b64_tr_b16 v[34:35], v227 offset:17408\n"
        "v_cvt_pk_bf16_f32 v187, v190, v191\n"
        "v_mfma_f32_16x16x32_bf16 v[4:7], v[56:59], v[192:195], v[4:7]\n"
        "ds_read_b64_tr_b16 v[36:37], v226 offset:25600\n"
        "v_accvgpr_write_b32 a248, v184\n"
        "v_mfma_f32_16x16x32_bf16 v[68:71], v[56:59], v[196:199], v[68:71]\n"
        "s_waitcnt lgkmcnt(6)\n"
        "v_accvgpr_write_b32 a249, v185\n"
        "v_accvgpr_write_b32 a250, v186\n"
        "v_mfma_f32_16x16x32_bf16 v[68:71], v[60:63], v[212:215], v[68:71]\n"
        "ds_read_b64_tr_b16 v[38:39], v227 offset:25600\n"
        "v_accvgpr_write_b32 a251, v187\n"
        "v_mfma_f32_16x16x32_bf16 v[64:67], v[52:55], v[212:215], v[64:67]\n"
        "ds_read_b64_tr_b16 v[46:47], v229 offset:25600\n"
        "global_load_lds_dwordx4 v243, s[86:87] offset:1024\n"
        "v_mfma_f32_16x16x32_bf16 v[64:67], v[48:51], v[196:199], v[64:67]\n"
        "ds_read_b64_tr_b16 v[42:43], v229 offset:17408\n"
        "v_accvgpr_read_b32 v185, a157\n"
        "v_mfma_f32_16x16x32_bf16 v[96:99], v[48:51], v[200:203], v[96:99]\n"
        "s_waitcnt lgkmcnt(6)\n"
        "v_accvgpr_read_b32 v186, a158\n"
        "v_accvgpr_read_b32 v187, a159\n"
        "v_mfma_f32_16x16x32_bf16 v[96:99], v[52:55], v[216:219], v[96:99]\n"
        "v_accvgpr_read_b32 v188, a188\n"
        "v_accvgpr_read_b32 v189, a189\n"
        "v_mfma_f32_16x16x32_bf16 v[100:103], v[60:63], v[216:219], v[100:103]\n"
        "global_load_lds_dwordx4 v244, s[86:87] offset:2048\n"
        "v_mfma_f32_16x16x32_bf16 v[100:103], v[56:59], v[200:203], v[100:103]\n"
        "v_accvgpr_read_b32 v190, a190\n"
        "v_accvgpr_read_b32 v191, a191\n"
        "v_mfma_f32_16x16x32_bf16 v[132:135], v[56:59], v[204:207], v[132:135]\n"
        "ds_read_b64_tr_b16 v[56:57], v232 offset:17408\n"
        "v_mfma_f32_16x16x32_bf16 v[132:135], v[60:63], v[220:223], v[132:135]\n"
        "ds_read_b64_tr_b16 v[60:61], v232 offset:25600\n"
        "v_mfma_f32_16x16x32_bf16 v[128:131], v[52:55], v[220:223], v[128:131]\n"
        "global_load_lds_dwordx4 v245, s[86:87] offset:3072\n"
        "s_add_u32 m0, s73, 0x1c000\n"
        "v_mfma_f32_16x16x32_bf16 v[128:131], v[48:51], v[204:207], v[128:131]\n"
        "ds_read_b64_tr_b16 v[48:49], v230 offset:17408\n"
        "s_add_u32 s86, s68, 0x580000\n"
        "s_addc_u32 s87, s69, 0\n"
        "v_mfma_f32_16x16x32_bf16 v[8:11], v[160:163], v[192:195], v[8:11]\n"
        "ds_read_b64_tr_b16 v[50:51], v231 offset:17408\n"
        "v_mfma_f32_16x16x32_bf16 v[8:11], v[164:167], v[208:211], v[8:11]\n"
        "ds_read_b64_tr_b16 v[52:53], v230 offset:25600\n"
        "v_mfma_f32_16x16x32_bf16 v[12:15], v[172:175], v[208:211], v[12:15]\n"
        "ds_read_b64_tr_b16 v[54:55], v231 offset:25600\n"
        "v_mfma_f32_16x16x32_bf16 v[12:15], v[168:171], v[192:195], v[12:15]\n"
        "ds_read_b64_tr_b16 v[62:63], v233 offset:25600\n"
        "v_mfma_f32_16x16x32_bf16 v[76:79], v[168:171], v[196:199], v[76:79]\n"
        "ds_read_b64_tr_b16 v[58:59], v233 offset:17408\n"
        "v_mfma_f32_16x16x32_bf16 v[76:79], v[172:175], v[212:215], v[76:79]\n"
        "global_store_dwordx4 v254, a[204:207], s[70:71] offset:1024 sc1\n"
        "s_add_u32 s70, s38, s97\n"
        "s_addc_u32 s71, s39, 0\n"
        "v_mfma_f32_16x16x32_bf16 v[72:75], v[164:167], v[212:215], v[72:75]\n"
        "v_mfma_f32_16x16x32_bf16 v[72:75], v[160:163], v[196:199], v[72:75]\n"
        "v_accvgpr_read_b32 v176, a152\n"
        "v_cvt_pk_bf16_f32 v176, v176, v177\n"
        "v_mfma_f32_16x16x32_bf16 v[104:107], v[160:163], v[200:203], v[104:107]\n"
        "v_cvt_pk_bf16_f32 v177, v178, v179\n"
        "v_cvt_pk_bf16_f32 v178, v180, v181\n"
        "v_mfma_f32_16x16x32_bf16 v[104:107], v[164:167], v[216:219], v[104:107]\n"
        "v_cvt_pk_bf16_f32 v179, v182, v183\n"
        "v_accvgpr_write_b32 a220, v176\n"
        "v_mfma_f32_16x16x32_bf16 v[108:111], v[172:175], v[216:219], v[108:111]\n"
        "v_accvgpr_write_b32 a221, v177\n"
        "v_accvgpr_write_b32 a222, v178\n"
        "v_mfma_f32_16x16x32_bf16 v[108:111], v[168:171], v[200:203], v[108:111]\n"
        "s_waitcnt vmcnt(14) lgkmcnt(0)\n"
        "v_accvgpr_write_b32 a223, v179\n"
        "v_mfma_f32_16x16x32_bf16 v[140:143], v[168:171], v[204:207], v[140:143]\n"
        "s_barrier\n"
        "v_mfma_f32_16x16x32_bf16 v[140:143], v[172:175], v[220:223], v[140:143]\n"
        "global_load_lds_dwordx4 v238, s[86:87] offset:0\n"
        "ds_read_b64_tr_b16 v[172:173], v228 offset:40960\n"
        "v_mfma_f32_16x16x32_bf16 v[136:139], v[164:167], v[220:223], v[136:139]\n"
        "ds_read_b64_tr_b16 v[164:165], v226 offset:40960\n"
        "v_accvgpr_read_b32 v177, a17\n"
        "v_mfma_f32_16x16x32_bf16 v[136:139], v[160:163], v[204:207], v[136:139]\n"
        "ds_read_b64_tr_b16 v[160:161], v226 offset:32768\n"
        "s_add_u32 s97, s52, 0x2000\n"
        "v_accvgpr_read_b32 v178, a18\n"
        "v_mfma_f32_16x16x32_bf16 v[16:19], v[32:35], v[192:195], v[16:19]\n"
        "ds_read_b64_tr_b16 v[162:163], v227 offset:32768\n"
        "s_add_u32 s97, s97, s75\n"
        "v_accvgpr_read_b32 v179, a19\n"
        "v_mfma_f32_16x16x32_bf16 v[16:19], v[36:39], v[208:211], v[16:19]\n"
        "ds_read_b64_tr_b16 v[166:167], v227 offset:40960\n"
        "global_load_lds_dwordx4 v239, s[86:87] offset:1024\n"
        "v_mfma_f32_16x16x32_bf16 v[20:23], v[44:47], v[208:211], v[20:23]\n"
        "ds_read_b64_tr_b16 v[174:175], v229 offset:40960\n"
        "v_accvgpr_read_b32 v180, a48\n"
        "v_mfma_f32_16x16x32_bf16 v[20:23], v[40:43], v[192:195], v[20:23]\n"
        "ds_read_b64_tr_b16 v[168:169], v228 offset:32768\n"
        "v_accvgpr_read_b32 v181, a49\n"
        "v_mfma_f32_16x16x32_bf16 v[84:87], v[40:43], v[196:199], v[84:87]\n"
        "ds_read_b64_tr_b16 v[170:171], v229 offset:32768\n"
        "v_accvgpr_read_b32 v182, a50\n"
        "v_mfma_f32_16x16x32_bf16 v[84:87], v[44:47], v[212:215], v[84:87]\n"
        "global_load_lds_dwordx4 v240, s[86:87] offset:2048\n"
        "v_mfma_f32_16x16x32_bf16 v[80:83], v[36:39], v[212:215], v[80:83]\n"
        "v_accvgpr_read_b32 v183, a51\n"
        "v_accvgpr_write_b32 a48, 0\n"
        "v_mfma_f32_16x16x32_bf16 v[80:83], v[32:35], v[196:199], v[80:83]\n"
        "v_accvgpr_write_b32 a49, 0\n"
        "v_accvgpr_write_b32 a50, 0\n"
        "v_mfma_f32_16x16x32_bf16 v[112:115], v[32:35], v[200:203], v[112:115]\n"
        "v_accvgpr_write_b32 a51, 0\n"
        "v_mfma_f32_16x16x32_bf16 v[112:115], v[36:39], v[216:219], v[112:115]\n"
        "global_load_lds_dwordx4 v241, s[86:87] offset:3072\n"
        "s_add_u32 m0, s72, 0x8000\n"
        "v_mfma_f32_16x16x32_bf16 v[116:119], v[44:47], v[216:219], v[116:119]\n"
        "s_add_u32 s86, s64, 0x580\n"
        "s_addc_u32 s87, s65, 0\n"
        "v_mfma_f32_16x16x32_bf16 v[116:119], v[40:43], v[200:203], v[116:119]\n"
        "v_mfma_f32_16x16x32_bf16 v[148:151], v[40:43], v[204:207], v[148:151]\n"
        "ds_read_b64_tr_b16 v[40:41], v232 offset:32768\n"
        "v_mfma_f32_16x16x32_bf16 v[148:151], v[44:47], v[220:223], v[148:151]\n"
        "global_load_lds_dwordx4 v242, s[86:87] offset:0\n"
        "v_mfma_f32_16x16x32_bf16 v[144:147], v[36:39], v[220:223], v[144:147]\n"
        "ds_read_b64_tr_b16 v[36:37], v230 offset:40960\n"
        "v_mfma_f32_16x16x32_bf16 v[144:147], v[32:35], v[204:207], v[144:147]\n"
        "ds_read_b64_tr_b16 v[32:33], v230 offset:32768\n"
        "v_accvgpr_read_b32 v184, a156\n"
        "v_mfma_f32_16x16x32_bf16 v[24:27], v[48:51], v[192:195], v[24:27]\n"
        "ds_read_b64_tr_b16 v[34:35], v231 offset:32768\n"
        "v_cvt_pk_bf16_f32 v184, v184, v185\n"
        "v_mfma_f32_16x16x32_bf16 v[24:27], v[52:55], v[208:211], v[24:27]\n"
        "ds_read_b64_tr_b16 v[38:39], v231 offset:40960\n"
        "v_cvt_pk_bf16_f32 v185, v186, v187\n"
        "v_mfma_f32_16x16x32_bf16 v[28:31], v[60:63], v[208:211], v[28:31]\n"
        "ds_read_b64_tr_b16 v[44:45], v232 offset:40960\n"
        "v_cvt_pk_bf16_f32 v186, v188, v189\n"
        "v_mfma_f32_16x16x32_bf16 v[28:31], v[56:59], v[192:195], v[28:31]\n"
        "ds_read_b64_tr_b16 v[46:47], v233 offset:40960\n"
        "v_cvt_pk_bf16_f32 v187, v190, v191\n"
        "v_mfma_f32_16x16x32_bf16 v[92:95], v[56:59], v[196:199], v[92:95]\n"
        "ds_read_b64_tr_b16 v[42:43], v233 offset:32768\n"
        "v_accvgpr_write_b32 a252, v184\n"
        "v_mfma_f32_16x16x32_bf16 v[92:95], v[60:63], v[212:215], v[92:95]\n"
        "ds_read_b128 v[192:195], v246 offset:2048\n"
        "v_accvgpr_write_b32 a253, v185\n"
        "v_mfma_f32_16x16x32_bf16 v[88:91], v[52:55], v[212:215], v[88:91]\n"
        "ds_read_b128 v[208:211], v247 offset:2048\n"
        "v_accvgpr_write_b32 a254, v186\n"
        "v_mfma_f32_16x16x32_bf16 v[88:91], v[48:51], v[196:199], v[88:91]\n"
        "global_load_lds_dwordx4 v243, s[86:87] offset:1024\n"
        "v_accvgpr_write_b32 a255, v187\n"
        "v_mfma_f32_16x16x32_bf16 v[120:123], v[48:51], v[200:203], v[120:123]\n"
        "ds_read_b128 v[196:199], v246 offset:0\n"
        "v_accvgpr_read_b32 v185, a21\n"
        "v_mfma_f32_16x16x32_bf16 v[120:123], v[52:55], v[216:219], v[120:123]\n"
        "ds_read_b128 v[212:215], v247 offset:0\n"
        "v_accvgpr_read_b32 v186, a22\n"
        "v_mfma_f32_16x16x32_bf16 v[124:127], v[60:63], v[216:219], v[124:127]\n"
        "ds_read_b128 v[216:219], v247 offset:6144\n"
        "v_accvgpr_read_b32 v187, a23\n"
        "v_mfma_f32_16x16x32_bf16 v[124:127], v[56:59], v[200:203], v[124:127]\n"
        "global_load_lds_dwordx4 v244, s[86:87] offset:2048\n"
        "ds_read_b128 v[200:203], v246 offset:6144\n"
        "LHA7_%=:\n"
        "s_cbranch_execz LHA11_%=\n"
        "v_mfma_f32_16x16x32_bf16 v[156:159], v[56:59], v[204:207], v[156:159]\n"
        "ds_read_b64_tr_b16 v[56:57], v228 offset:33792\n"
        "v_accvgpr_read_b32 v188, a52\n"
        "v_mfma_f32_16x16x32_bf16 v[156:159], v[60:63], v[220:223], v[156:159]\n"
        "ds_read_b64_tr_b16 v[60:61], v228 offset:41984\n"
        "v_accvgpr_read_b32 v189, a53\n"
        "v_mfma_f32_16x16x32_bf16 v[152:155], v[52:55], v[220:223], v[152:155]\n"
        "ds_read_b128 v[220:223], v247 offset:4096\n"
        "v_accvgpr_read_b32 v190, a54\n"
        "v_mfma_f32_16x16x32_bf16 v[152:155], v[48:51], v[204:207], v[152:155]\n"
        "global_load_lds_dwordx4 v245, s[86:87] offset:3072\n"
        "s_add_u32 m0, s72, 0x9000\n"
        "s_waitcnt lgkmcnt(7)\n"
        "v_mfma_f32_16x16x32_bf16 v[0:3], v[160:163], v[192:195], v[0:3]\n"
        "ds_read_b128 v[204:207], v246 offset:4096\n"
        "s_add_u32 s86, s66, 0x580\n"
        "s_addc_u32 s87, s67, 0\n"
        "v_mfma_f32_16x16x32_bf16 v[0:3], v[164:167], v[208:211], v[0:3]\n"
        "ds_read_b64_tr_b16 v[48:49], v226 offset:33792\n"
        "v_accvgpr_read_b32 v191, a55\n"
        "v_mfma_f32_16x16x32_bf16 v[4:7], v[172:175], v[208:211], v[4:7]\n"
        "ds_read_b64_tr_b16 v[50:51], v227 offset:33792\n"
        "v_accvgpr_write_b32 a52, 0\n"
        "v_mfma_f32_16x16x32_bf16 v[4:7], v[168:171], v[192:195], v[4:7]\n"
        "ds_read_b64_tr_b16 v[52:53], v226 offset:41984\n"
        "s_waitcnt lgkmcnt(7)\n"
        "v_mfma_f32_16x16x32_bf16 v[68:71], v[168:171], v[196:199], v[68:71]\n"
        "ds_read_b64_tr_b16 v[54:55], v227 offset:41984\n"
        "global_load_lds_dwordx4 v242, s[86:87] offset:0\n"
        "v_mfma_f32_16x16x32_bf16 v[68:71], v[172:175], v[212:215], v[68:71]\n"
        "ds_read_b64_tr_b16 v[62:63], v229 offset:41984\n"
        "v_accvgpr_write_b32 a53, 0\n"
        "v_mfma_f32_16x16x32_bf16 v[64:67], v[164:167], v[212:215], v[64:67]\n"
        "ds_read_b64_tr_b16 v[58:59], v229 offset:33792\n"
        "v_accvgpr_write_b32 a54, 0\n"
        "v_mfma_f32_16x16x32_bf16 v[64:67], v[160:163], v[196:199], v[64:67]\n"
        "v_accvgpr_read_b32 v176, a16\n"
        "v_cvt_pk_bf16_f32 v176, v176, v177\n"
        "v_mfma_f32_16x16x32_bf16 v[96:99], v[160:163], v[200:203], v[96:99]\n"
        "global_load_lds_dwordx4 v243, s[86:87] offset:1024\n"
        "v_mfma_f32_16x16x32_bf16 v[96:99], v[164:167], v[216:219], v[96:99]\n"
        "s_waitcnt lgkmcnt(5)\n"
        "v_cvt_pk_bf16_f32 v177, v178, v179\n"
        "v_cvt_pk_bf16_f32 v178, v180, v181\n"
        "v_mfma_f32_16x16x32_bf16 v[100:103], v[172:175], v[216:219], v[100:103]\n"
        "v_cvt_pk_bf16_f32 v179, v182, v183\n"
        "v_accvgpr_write_b32 a176, v176\n"
        "v_mfma_f32_16x16x32_bf16 v[100:103], v[168:171], v[200:203], v[100:103]\n"
        "v_accvgpr_write_b32 a177, v177\n"
        "v_accvgpr_write_b32 a178, v178\n"
        "v_mfma_f32_16x16x32_bf16 v[132:135], v[168:171], v[204:207], v[132:135]\n"
        "ds_read_b64_tr_b16 v[168:169], v232 offset:33792\n"
        "v_accvgpr_write_b32 a179, v179\n"
        "v_mfma_f32_16x16x32_bf16 v[132:135], v[172:175], v[220:223], v[132:135]\n"
        "global_load_lds_dwordx4 v244, s[86:87] offset:2048\n"
        "ds_read_b64_tr_b16 v[172:173], v232 offset:41984\n"
        "v_mfma_f32_16x16x32_bf16 v[128:131], v[164:167], v[220:223], v[128:131]\n"
        "ds_read_b64_tr_b16 v[164:165], v230 offset:41984\n"
        "v_accvgpr_read_b32 v177, a25\n"
        "v_mfma_f32_16x16x32_bf16 v[128:131], v[160:163], v[204:207], v[128:131]\n"
        "ds_read_b64_tr_b16 v[160:161], v230 offset:33792\n"
        "v_accvgpr_read_b32 v178, a26\n"
        "v_mfma_f32_16x16x32_bf16 v[8:11], v[32:35], v[192:195], v[8:11]\n"
        "ds_read_b64_tr_b16 v[162:163], v231 offset:33792\n"
        "v_accvgpr_read_b32 v179, a27\n"
        "v_mfma_f32_16x16x32_bf16 v[8:11], v[36:39], v[208:211], v[8:11]\n"
        "global_load_lds_dwordx4 v245, s[86:87] offset:3072\n"
        "ds_read_b64_tr_b16 v[166:167], v231 offset:41984\n"
        "s_add_u32 m0, s73, 0x20000\n"
        "v_mfma_f32_16x16x32_bf16 v[12:15], v[44:47], v[208:211], v[12:15]\n"
        "ds_read_b64_tr_b16 v[174:175], v233 offset:41984\n"
        "s_add_u32 s86, s68, 0x600000\n"
        "s_addc_u32 s87, s69, 0\n"
        "v_mfma_f32_16x16x32_bf16 v[12:15], v[40:43], v[192:195], v[12:15]\n"
        "ds_read_b64_tr_b16 v[170:171], v233 offset:33792\n"
        "v_accvgpr_read_b32 v180, a56\n"
        "v_mfma_f32_16x16x32_bf16 v[76:79], v[40:43], v[196:199], v[76:79]\n"
        "v_accvgpr_read_b32 v181, a57\n"
        "v_accvgpr_read_b32 v182, a58\n"
        "v_mfma_f32_16x16x32_bf16 v[76:79], v[44:47], v[212:215], v[76:79]\n"
        "v_accvgpr_read_b32 v183, a59\n"
        "v_accvgpr_write_b32 a55, 0\n"
        "v_mfma_f32_16x16x32_bf16 v[72:75], v[36:39], v[212:215], v[72:75]\n"
        "v_accvgpr_write_b32 a56, 0\n"
        "v_accvgpr_write_b32 a57, 0\n"
        "v_mfma_f32_16x16x32_bf16 v[72:75], v[32:35], v[196:199], v[72:75]\n"
        "v_accvgpr_write_b32 a58, 0\n"
        "v_accvgpr_write_b32 a59, 0\n"
        "v_mfma_f32_16x16x32_bf16 v[104:107], v[32:35], v[200:203], v[104:107]\n"
        "v_mfma_f32_16x16x32_bf16 v[104:107], v[36:39], v[216:219], v[104:107]\n"
        "v_mfma_f32_16x16x32_bf16 v[108:111], v[44:47], v[216:219], v[108:111]\n"
        "v_mfma_f32_16x16x32_bf16 v[108:111], v[40:43], v[200:203], v[108:111]\n"
        "global_store_dwordx4 v254, a[160:163], s[70:71] offset:0 sc1\n"
        "s_waitcnt vmcnt(14) lgkmcnt(0)\n"
        "s_barrier\n"
        "v_mfma_f32_16x16x32_bf16 v[140:143], v[40:43], v[204:207], v[140:143]\n"
        "ds_read_b64_tr_b16 v[40:41], v228 offset:49152\n"
        "v_mfma_f32_16x16x32_bf16 v[140:143], v[44:47], v[220:223], v[140:143]\n"
        "ds_read_b64_tr_b16 v[44:45], v228 offset:57344\n"
        "v_mfma_f32_16x16x32_bf16 v[136:139], v[36:39], v[220:223], v[136:139]\n"
        "global_load_lds_dwordx4 v238, s[86:87] offset:0\n"
        "v_mfma_f32_16x16x32_bf16 v[136:139], v[32:35], v[204:207], v[136:139]\n"
        "ds_read_b64_tr_b16 v[32:33], v226 offset:49152\n"
        "v_accvgpr_read_b32 v184, a20\n"
        "v_mfma_f32_16x16x32_bf16 v[16:19], v[48:51], v[192:195], v[16:19]\n"
        "ds_read_b64_tr_b16 v[34:35], v227 offset:49152\n"
        "v_cvt_pk_bf16_f32 v184, v184, v185\n"
        "v_mfma_f32_16x16x32_bf16 v[16:19], v[52:55], v[208:211], v[16:19]\n"
        "ds_read_b64_tr_b16 v[36:37], v226 offset:57344\n"
        "v_cvt_pk_bf16_f32 v185, v186, v187\n"
        "v_mfma_f32_16x16x32_bf16 v[20:23], v[60:63], v[208:211], v[20:23]\n"
        "ds_read_b64_tr_b16 v[38:39], v227 offset:57344\n"
        "v_cvt_pk_bf16_f32 v186, v188, v189\n"
        "v_mfma_f32_16x16x32_bf16 v[20:23], v[56:59], v[192:195], v[20:23]\n"
        "ds_read_b64_tr_b16 v[46:47], v229 offset:57344\n"
        "v_cvt_pk_bf16_f32 v187, v190, v191\n"
        "v_mfma_f32_16x16x32_bf16 v[84:87], v[56:59], v[196:199], v[84:87]\n"
        "ds_read_b64_tr_b16 v[42:43], v229 offset:49152\n"
        "v_accvgpr_write_b32 a180, v184\n"
        "v_mfma_f32_16x16x32_bf16 v[84:87], v[60:63], v[212:215], v[84:87]\n"
        "global_load_lds_dwordx4 v239, s[86:87] offset:1024\n"
        "v_accvgpr_write_b32 a181, v185\n"
        "v_mfma_f32_16x16x32_bf16 v[80:83], v[52:55], v[212:215], v[80:83]\n"
        "v_accvgpr_write_b32 a182, v186\n"
        "v_accvgpr_write_b32 a183, v187\n"
        "v_mfma_f32_16x16x32_bf16 v[80:83], v[48:51], v[196:199], v[80:83]\n"
        "v_accvgpr_read_b32 v185, a29\n"
        "v_accvgpr_read_b32 v186, a30\n"
        "v_mfma_f32_16x16x32_bf16 v[112:115], v[48:51], v[200:203], v[112:115]\n"
        "v_accvgpr_read_b32 v187, a31\n"
        "v_accvgpr_read_b32 v188, a60\n"
        "v_mfma_f32_16x16x32_bf16 v[112:115], v[52:55], v[216:219], v[112:115]\n"
        "global_load_lds_dwordx4 v240, s[86:87] offset:2048\n"
        "v_accvgpr_read_b32 v189, a61\n"
        "v_mfma_f32_16x16x32_bf16 v[116:119], v[60:63], v[216:219], v[116:119]\n"
        "v_accvgpr_read_b32 v190, a62\n"
        "v_accvgpr_read_b32 v191, a63\n"
        "v_mfma_f32_16x16x32_bf16 v[116:119], v[56:59], v[200:203], v[116:119]\n"
        "v_accvgpr_write_b32 a60, 0\n"
        "v_accvgpr_write_b32 a61, 0\n"
        "v_mfma_f32_16x16x32_bf16 v[148:151], v[56:59], v[204:207], v[148:151]\n"
        "ds_read_b64_tr_b16 v[56:57], v232 offset:49152\n"
        "v_accvgpr_write_b32 a62, 0\n"
        "v_mfma_f32_16x16x32_bf16 v[148:151], v[60:63], v[220:223], v[148:151]\n"
        "global_load_lds_dwordx4 v241, s[86:87] offset:3072\n"
        "ds_read_b64_tr_b16 v[60:61], v232 offset:57344\n"
        "s_add_u32 m0, s72, 0x0\n"
        "v_mfma_f32_16x16x32_bf16 v[144:147], v[52:55], v[220:223], v[144:147]\n"
        "ds_read_b64_tr_b16 v[52:53], v230 offset:57344\n"
        "s_add_u32 s86, s64, 0x600\n"
        "s_addc_u32 s87, s65, 0\n"
        "v_mfma_f32_16x16x32_bf16 v[144:147], v[48:51], v[204:207], v[144:147]\n"
        "ds_read_b64_tr_b16 v[48:49], v230 offset:49152\n"
        "v_accvgpr_write_b32 a63, 0\n"
        "v_mfma_f32_16x16x32_bf16 v[24:27], v[160:163], v[192:195], v[24:27]\n"
        "ds_read_b64_tr_b16 v[50:51], v231 offset:49152\n"
        "v_mfma_f32_16x16x32_bf16 v[24:27], v[164:167], v[208:211], v[24:27]\n"
        "ds_read_b64_tr_b16 v[54:55], v231 offset:57344\n"
        "global_load_lds_dwordx4 v242, s[86:87] offset:0\n"
        "v_mfma_f32_16x16x32_bf16 v[28:31], v[172:175], v[208:211], v[28:31]\n"
        "ds_read_b64_tr_b16 v[62:63], v233 offset:57344\n"
        "v_mfma_f32_16x16x32_bf16 v[28:31], v[168:171], v[192:195], v[28:31]\n"
        "ds_read_b64_tr_b16 v[58:59], v233 offset:49152\n"
        "v_mfma_f32_16x16x32_bf16 v[92:95], v[168:171], v[196:199], v[92:95]\n"
        "ds_read_b128 v[192:195], v248 offset:2048\n"
        "v_mfma_f32_16x16x32_bf16 v[92:95], v[172:175], v[212:215], v[92:95]\n"
        "ds_read_b128 v[208:211], v249 offset:2048\n"
        "global_load_lds_dwordx4 v243, s[86:87] offset:1024\n"
        "v_mfma_f32_16x16x32_bf16 v[88:91], v[164:167], v[212:215], v[88:91]\n"
        "ds_read_b128 v[212:215], v249 offset:0\n"
        "v_mfma_f32_16x16x32_bf16 v[88:91], v[160:163], v[196:199], v[88:91]\n"
        "ds_read_b128 v[196:199], v248 offset:0\n"
        "v_accvgpr_read_b32 v176, a24\n"
        "v_mfma_f32_16x16x32_bf16 v[120:123], v[160:163], v[200:203], v[120:123]\n"
        "v_cvt_pk_bf16_f32 v176, v176, v177\n"
        "v_cvt_pk_bf16_f32 v177, v178, v179\n"
        "v_mfma_f32_16x16x32_bf16 v[120:123], v[164:167], v[216:219], v[120:123]\n"
        "v_cvt_pk_bf16_f32 v178, v180, v181\n"
        "v_cvt_pk_bf16_f32 v179, v182, v183\n"
        "v_mfma_f32_16x16x32_bf16 v[124:127], v[172:175], v[216:219], v[124:127]\n"
        "global_load_lds_dwordx4 v244, s[86:87] offset:2048\n"
        "v_accvgpr_write_b32 a184, v176\n"
        "v_mfma_f32_16x16x32_bf16 v[124:127], v[168:171], v[200:203], v[124:127]\n"
        "ds_read_b128 v[200:203], v248 offset:6144\n"
        "v_accvgpr_write_b32 a185, v177\n"
        "v_mfma_f32_16x16x32_bf16 v[156:159], v[168:171], v[204:207], v[156:159]\n"
        "s_waitcnt lgkmcnt(4)\n"
        "v_accvgpr_write_b32 a186, v178\n"
        "v_accvgpr_write_b32 a187, v179\n"
        "v_mfma_f32_16x16x32_bf16 v[156:159], v[172:175], v[220:223], v[156:159]\n"
        "ds_read_b128 v[216:219], v249 offset:6144\n"
        "v_accvgpr_read_b32 v177, a81\n"
        "v_mfma_f32_16x16x32_bf16 v[152:155], v[164:167], v[220:223], v[152:155]\n"
        "global_load_lds_dwordx4 v245, s[86:87] offset:3072\n"
        "ds_read_b128 v[220:223], v249 offset:4096\n"
        "s_add_u32 m0, s72, 0x1000\n"
        "v_mfma_f32_16x16x32_bf16 v[152:155], v[160:163], v[204:207], v[152:155]\n"
        "ds_read_b128 v[204:207], v248 offset:4096\n"
        "s_add_u32 s86, s66, 0x600\n"
        "s_addc_u32 s87, s67, 0\n"
        "v_mfma_f32_16x16x32_bf16 v[0:3], v[32:35], v[192:195], v[0:3]\n"
        "ds_read_b64_tr_b16 v[160:161], v226 offset:50176\n"
        "s_waitcnt lgkmcnt(5)\n"
        "v_mfma_f32_16x16x32_bf16 v[0:3], v[36:39], v[208:211], v[0:3]\n"
        "ds_read_b64_tr_b16 v[162:163], v227 offset:50176\n"
        "v_accvgpr_read_b32 v178, a82\n"
        "v_mfma_f32_16x16x32_bf16 v[4:7], v[44:47], v[208:211], v[4:7]\n"
        "ds_read_b64_tr_b16 v[164:165], v226 offset:58368\n"
        "global_load_lds_dwordx4 v242, s[86:87] offset:0\n"
        "v_mfma_f32_16x16x32_bf16 v[4:7], v[40:43], v[192:195], v[4:7]\n"
        "ds_read_b64_tr_b16 v[166:167], v227 offset:58368\n"
        "v_accvgpr_read_b32 v179, a83\n"
        "v_mfma_f32_16x16x32_bf16 v[68:71], v[40:43], v[196:199], v[68:71]\n"
        "ds_read_b64_tr_b16 v[172:173], v228 offset:58368\n"
        "v_accvgpr_read_b32 v180, a112\n"
        "v_mfma_f32_16x16x32_bf16 v[68:71], v[44:47], v[212:215], v[68:71]\n"
        "ds_read_b64_tr_b16 v[174:175], v229 offset:58368\n"
        "v_accvgpr_read_b32 v181, a113\n"
        "v_mfma_f32_16x16x32_bf16 v[64:67], v[36:39], v[212:215], v[64:67]\n"
        "ds_read_b64_tr_b16 v[168:169], v228 offset:50176\n"
        "global_load_lds_dwordx4 v243, s[86:87] offset:1024\n"
        "v_mfma_f32_16x16x32_bf16 v[64:67], v[32:35], v[196:199], v[64:67]\n"
        "ds_read_b64_tr_b16 v[170:171], v229 offset:50176\n"
        "s_waitcnt lgkmcnt(8)\n"
        "v_mfma_f32_16x16x32_bf16 v[96:99], v[32:35], v[200:203], v[96:99]\n"
        "v_accvgpr_read_b32 v182, a114\n"
        "v_accvgpr_read_b32 v183, a115\n"
        "v_mfma_f32_16x16x32_bf16 v[96:99], v[36:39], v[216:219], v[96:99]\n"
        "v_mfma_f32_16x16x32_bf16 v[100:103], v[44:47], v[216:219], v[100:103]\n"
        "global_load_lds_dwordx4 v244, s[86:87] offset:2048\n"
        "v_mfma_f32_16x16x32_bf16 v[100:103], v[40:43], v[200:203], v[100:103]\n"
        "v_mfma_f32_16x16x32_bf16 v[132:135], v[40:43], v[204:207], v[132:135]\n"
        "ds_read_b64_tr_b16 v[40:41], v232 offset:50176\n"
        "v_mfma_f32_16x16x32_bf16 v[132:135], v[44:47], v[220:223], v[132:135]\n"
        "ds_read_b64_tr_b16 v[44:45], v232 offset:58368\n"
        "v_mfma_f32_16x16x32_bf16 v[128:131], v[36:39], v[220:223], v[128:131]\n"
        "global_load_lds_dwordx4 v245, s[86:87] offset:3072\n"
        "s_add_u32 m0, s73, 0x24000\n"
        "v_mfma_f32_16x16x32_bf16 v[128:131], v[32:35], v[204:207], v[128:131]\n"
        "ds_read_b64_tr_b16 v[32:33], v230 offset:50176\n"
        "v_accvgpr_read_b32 v184, a28\n"
        "v_mfma_f32_16x16x32_bf16 v[8:11], v[48:51], v[192:195], v[8:11]\n"
        "ds_read_b64_tr_b16 v[34:35], v231 offset:50176\n"
        "v_cvt_pk_bf16_f32 v184, v184, v185\n"
        "v_mfma_f32_16x16x32_bf16 v[8:11], v[52:55], v[208:211], v[8:11]\n"
        "ds_read_b64_tr_b16 v[36:37], v230 offset:58368\n"
        "v_cvt_pk_bf16_f32 v185, v186, v187\n"
        "v_mfma_f32_16x16x32_bf16 v[12:15], v[60:63], v[208:211], v[12:15]\n"
        "ds_read_b64_tr_b16 v[38:39], v231 offset:58368\n"
        "v_cvt_pk_bf16_f32 v186, v188, v189\n"
        "v_mfma_f32_16x16x32_bf16 v[12:15], v[56:59], v[192:195], v[12:15]\n"
        "ds_read_b64_tr_b16 v[46:47], v233 offset:58368\n"
        "v_cvt_pk_bf16_f32 v187, v190, v191\n"
        "v_mfma_f32_16x16x32_bf16 v[76:79], v[56:59], v[196:199], v[76:79]\n"
        "ds_read_b64_tr_b16 v[42:43], v233 offset:50176\n"
        "v_accvgpr_write_b32 a188, v184\n"
        "v_mfma_f32_16x16x32_bf16 v[76:79], v[60:63], v[212:215], v[76:79]\n"
        "s_add_u32 s86, s68, 0x680000\n"
        "s_addc_u32 s87, s69, 0\n"
        "v_accvgpr_write_b32 a189, v185\n"
        "v_mfma_f32_16x16x32_bf16 v[72:75], v[52:55], v[212:215], v[72:75]\n"
        "v_accvgpr_write_b32 a190, v186\n"
        "v_accvgpr_write_b32 a191, v187\n"
        "v_mfma_f32_16x16x32_bf16 v[72:75], v[48:51], v[196:199], v[72:75]\n"
        "v_accvgpr_read_b32 v185, a85\n"
        "v_accvgpr_read_b32 v186, a86\n"
        "v_mfma_f32_16x16x32_bf16 v[104:107], v[48:51], v[200:203], v[104:107]\n"
        "v_accvgpr_read_b32 v187, a87\n"
        "v_accvgpr_read_b32 v188, a116\n"
        "v_mfma_f32_16x16x32_bf16 v[104:107], v[52:55], v[216:219], v[104:107]\n"
        "v_accvgpr_read_b32 v189, a117\n"
        "v_accvgpr_read_b32 v190, a118\n"
        "v_mfma_f32_16x16x32_bf16 v[108:111], v[60:63], v[216:219], v[108:111]\n"
        "v_accvgpr_read_b32 v191, a119\n"
        "v_mfma_f32_16x16x32_bf16 v[108:111], v[56:59], v[200:203], v[108:111]\n"
        "v_mfma_f32_16x16x32_bf16 v[140:143], v[56:59], v[204:207], v[140:143]\n"
        "s_waitcnt vmcnt(13) lgkmcnt(0)\n"
        "v_mfma_f32_16x16x32_bf16 v[140:143], v[60:63], v[220:223], v[140:143]\n"
        "s_barrier\n"
        "v_mfma_f32_16x16x32_bf16 v[136:139], v[52:55], v[220:223], v[136:139]\n"
        "global_load_lds_dwordx4 v238, s[86:87] offset:0\n"
        "ds_read_b64_tr_b16 v[52:53], v226 offset:24576\n"
        "v_mfma_f32_16x16x32_bf16 v[136:139], v[48:51], v[204:207], v[136:139]\n"
        "ds_read_b64_tr_b16 v[48:49], v226 offset:16384\n"
        "v_mfma_f32_16x16x32_bf16 v[16:19], v[160:163], v[192:195], v[16:19]\n"
        "ds_read_b64_tr_b16 v[50:51], v227 offset:16384\n"
        "v_mfma_f32_16x16x32_bf16 v[16:19], v[164:167], v[208:211], v[16:19]\n"
        "ds_read_b64_tr_b16 v[54:55], v227 offset:24576\n"
        "v_mfma_f32_16x16x32_bf16 v[20:23], v[172:175], v[208:211], v[20:23]\n"
        "global_store_dwordx4 v254, a[164:167], s[70:71] offset:1024 sc1\n"
        "ds_read_b64_tr_b16 v[60:61], v228 offset:24576\n"
        "ds_read_b64_tr_b16 v[62:63], v229 offset:24576\n"
        "s_add_u32 s70, s38, s97\n"
        "s_addc_u32 s71, s39, 0\n"
        "v_mfma_f32_16x16x32_bf16 v[20:23], v[168:171], v[192:195], v[20:23]\n"
        "ds_read_b64_tr_b16 v[56:57], v228 offset:16384\n"
        "s_add_u32 s97, s52, 0x3000\n"
        "s_add_u32 s97, s97, s75\n"
        "v_mfma_f32_16x16x32_bf16 v[84:87], v[168:171], v[196:199], v[84:87]\n"
        "ds_read_b64_tr_b16 v[58:59], v229 offset:16384\n"
        "v_mfma_f32_16x16x32_bf16 v[84:87], v[172:175], v[212:215], v[84:87]\n"
        "global_load_lds_dwordx4 v239, s[86:87] offset:1024\n"
        "v_mfma_f32_16x16x32_bf16 v[80:83], v[164:167], v[212:215], v[80:83]\n"
        "v_mfma_f32_16x16x32_bf16 v[80:83], v[160:163], v[196:199], v[80:83]\n"
        "v_accvgpr_read_b32 v176, a80\n"
        "v_cvt_pk_bf16_f32 v176, v176, v177\n"
        "v_mfma_f32_16x16x32_bf16 v[112:115], v[160:163], v[200:203], v[112:115]\n"
        "v_cvt_pk_bf16_f32 v177, v178, v179\n"
        "v_cvt_pk_bf16_f32 v178, v180, v181\n"
        "v_mfma_f32_16x16x32_bf16 v[112:115], v[164:167], v[216:219], v[112:115]\n"
        "global_load_lds_dwordx4 v240, s[86:87] offset:2048\n"
        "v_mfma_f32_16x16x32_bf16 v[116:119], v[172:175], v[216:219], v[116:119]\n"
        "v_cvt_pk_bf16_f32 v179, v182, v183\n"
        "v_accvgpr_write_b32 a16, v176\n"
        "v_mfma_f32_16x16x32_bf16 v[116:119], v[168:171], v[200:203], v[116:119]\n"
        "v_accvgpr_write_b32 a17, v177\n"
        "v_accvgpr_write_b32 a18, v178\n"
        "v_mfma_f32_16x16x32_bf16 v[148:151], v[168:171], v[204:207], v[148:151]\n"
        "ds_read_b64_tr_b16 v[168:169], v232 offset:16384\n"
        "v_accvgpr_write_b32 a19, v179\n"
        "v_mfma_f32_16x16x32_bf16 v[148:151], v[172:175], v[220:223], v[148:151]\n"
        "global_load_lds_dwordx4 v241, s[86:87] offset:3072\n"
        "s_add_u32 m0, s72, 0x10000\n"
        "v_mfma_f32_16x16x32_bf16 v[144:147], v[164:167], v[220:223], v[144:147]\n"
        "ds_read_b64_tr_b16 v[164:165], v230 offset:24576\n"
        "s_add_u32 s86, s64, 0x680\n"
        "s_addc_u32 s87, s65, 0\n"
        "v_mfma_f32_16x16x32_bf16 v[144:147], v[160:163], v[204:207], v[144:147]\n"
        "ds_read_b64_tr_b16 v[160:161], v230 offset:16384\n"
        "v_accvgpr_read_b32 v177, a89\n"
        "v_mfma_f32_16x16x32_bf16 v[24:27], v[32:35], v[192:195], v[24:27]\n"
        "ds_read_b64_tr_b16 v[162:163], v231 offset:16384\n"
        "v_accvgpr_read_b32 v178, a90\n"
        "v_mfma_f32_16x16x32_bf16 v[24:27], v[36:39], v[208:211], v[24:27]\n"
        "ds_read_b64_tr_b16 v[166:167], v231 offset:24576\n"
        "global_load_lds_dwordx4 v242, s[86:87] offset:0\n"
        "v_mfma_f32_16x16x32_bf16 v[28:31], v[44:47], v[208:211], v[28:31]\n"
        "ds_read_b64_tr_b16 v[172:173], v232 offset:24576\n"
        "v_accvgpr_read_b32 v179, a91\n"
        "v_mfma_f32_16x16x32_bf16 v[28:31], v[40:43], v[192:195], v[28:31]\n"
        "ds_read_b64_tr_b16 v[174:175], v233 offset:24576\n"
        "v_accvgpr_read_b32 v180, a120\n"
        "v_mfma_f32_16x16x32_bf16 v[92:95], v[40:43], v[196:199], v[92:95]\n"
        "ds_read_b64_tr_b16 v[170:171], v233 offset:16384\n"
        "v_accvgpr_read_b32 v181, a121\n"
        "v_mfma_f32_16x16x32_bf16 v[92:95], v[44:47], v[212:215], v[92:95]\n"
        "ds_read_b128 v[192:195], v246 offset:34816\n"
        "global_load_lds_dwordx4 v243, s[86:87] offset:1024\n"
        "v_mfma_f32_16x16x32_bf16 v[88:91], v[36:39], v[212:215], v[88:91]\n"
        "ds_read_b128 v[208:211], v247 offset:34816\n"
        "v_accvgpr_read_b32 v182, a122\n"
        "v_mfma_f32_16x16x32_bf16 v[88:91], v[32:35], v[196:199], v[88:91]\n"
        "ds_read_b128 v[196:199], v246 offset:32768\n"
        "v_accvgpr_read_b32 v183, a123\n"
        "v_mfma_f32_16x16x32_bf16 v[120:123], v[32:35], v[200:203], v[120:123]\n"
        "ds_read_b128 v[212:215], v247 offset:32768\n"
        "v_mfma_f32_16x16x32_bf16 v[120:123], v[36:39], v[216:219], v[120:123]\n"
        "global_load_lds_dwordx4 v244, s[86:87] offset:2048\n"
        "v_mfma_f32_16x16x32_bf16 v[124:127], v[44:47], v[216:219], v[124:127]\n"
        "ds_read_b128 v[216:219], v247 offset:38912\n"
        "v_mfma_f32_16x16x32_bf16 v[124:127], v[40:43], v[200:203], v[124:127]\n"
        "ds_read_b128 v[200:203], v246 offset:38912\n"
        "v_mfma_f32_16x16x32_bf16 v[156:159], v[40:43], v[204:207], v[156:159]\n"
        "ds_read_b64_tr_b16 v[40:41], v228 offset:17408\n"
        "v_mfma_f32_16x16x32_bf16 v[156:159], v[44:47], v[220:223], v[156:159]\n"
        "global_load_lds_dwordx4 v245, s[86:87] offset:3072\n"
        "s_add_u32 m0, s72, 0x11000\n"
        "v_mfma_f32_16x16x32_bf16 v[152:155], v[36:39], v[220:223], v[152:155]\n"
        "ds_read_b128 v[220:223], v247 offset:36864\n"
        "s_add_u32 s86, s66, 0x680\n"
        "s_addc_u32 s87, s67, 0\n"
        "v_mfma_f32_16x16x32_bf16 v[152:155], v[32:35], v[204:207], v[152:155]\n"
        "ds_read_b128 v[204:207], v246 offset:36864\n"
        "s_waitcnt lgkmcnt(6)\n"
        "v_mfma_f32_16x16x32_bf16 v[0:3], v[48:51], v[192:195], v[0:3]\n"
        "ds_read_b64_tr_b16 v[32:33], v226 offset:17408\n"
        "v_accvgpr_read_b32 v184, a84\n"
        "v_mfma_f32_16x16x32_bf16 v[0:3], v[52:55], v[208:211], v[0:3]\n"
        "s_waitcnt lgkmcnt(6)\n"
        "v_cvt_pk_bf16_f32 v184, v184, v185\n"
        "v_cvt_pk_bf16_f32 v185, v186, v187\n"
        "v_mfma_f32_16x16x32_bf16 v[4:7], v[60:63], v[208:211], v[4:7]\n"
        "global_load_lds_dwordx4 v242, s[86:87] offset:0\n"
        "v_cvt_pk_bf16_f32 v186, v188, v189\n"
        "v_mfma_f32_16x16x32_bf16 v[4:7], v[56:59], v[192:195], v[4:7]\n"
        "ds_read_b64_tr_b16 v[34:35], v227 offset:17408\n"
        "v_cvt_pk_bf16_f32 v187, v190, v191\n"
        "v_mfma_f32_16x16x32_bf16 v[68:71], v[56:59], v[196:199], v[68:71]\n"
        "s_waitcnt lgkmcnt(4)\n"
        "v_accvgpr_write_b32 a20, v184\n"
        "v_accvgpr_write_b32 a21, v185\n"
        "v_mfma_f32_16x16x32_bf16 v[68:71], v[60:63], v[212:215], v[68:71]\n"
        "ds_read_b64_tr_b16 v[36:37], v226 offset:25600\n"
        "v_accvgpr_write_b32 a22, v186\n"
        "v_mfma_f32_16x16x32_bf16 v[64:67], v[52:55], v[212:215], v[64:67]\n"
        "ds_read_b64_tr_b16 v[38:39], v227 offset:25600\n"
        "v_accvgpr_write_b32 a23, v187\n"
        "v_mfma_f32_16x16x32_bf16 v[64:67], v[48:51], v[196:199], v[64:67]\n"
        "ds_read_b64_tr_b16 v[44:45], v228 offset:25600\n"
        "global_load_lds_dwordx4 v243, s[86:87] offset:1024\n"
        "v_mfma_f32_16x16x32_bf16 v[96:99], v[48:51], v[200:203], v[96:99]\n"
        "ds_read_b64_tr_b16 v[46:47], v229 offset:25600\n"
        "v_accvgpr_read_b32 v185, a93\n"
        "v_mfma_f32_16x16x32_bf16 v[96:99], v[52:55], v[216:219], v[96:99]\n"
        "ds_read_b64_tr_b16 v[42:43], v229 offset:17408\n"
        "v_accvgpr_read_b32 v186, a94\n"
        "v_mfma_f32_16x16x32_bf16 v[100:103], v[60:63], v[216:219], v[100:103]\n"
        "s_waitcnt lgkmcnt(6)\n"
        "v_accvgpr_read_b32 v187, a95\n"
        "v_accvgpr_read_b32 v188, a124\n"
        "v_mfma_f32_16x16x32_bf16 v[100:103], v[56:59], v[200:203], v[100:103]\n"
        "global_load_lds_dwordx4 v244, s[86:87] offset:2048\n"
        "v_mfma_f32_16x16x32_bf16 v[132:135], v[56:59], v[204:207], v[132:135]\n"
        "ds_read_b64_tr_b16 v[56:57], v232 offset:17408\n"
        "v_accvgpr_read_b32 v189, a125\n"
        "v_mfma_f32_16x16x32_bf16 v[132:135], v[60:63], v[220:223], v[132:135]\n"
        "ds_read_b64_tr_b16 v[60:61], v232 offset:25600\n"
        "v_accvgpr_read_b32 v190, a126\n"
        "v_mfma_f32_16x16x32_bf16 v[128:131], v[52:55], v[220:223], v[128:131]\n"
        "ds_read_b64_tr_b16 v[52:53], v230 offset:25600\n"
        "v_accvgpr_read_b32 v191, a127\n"
        "v_mfma_f32_16x16x32_bf16 v[128:131], v[48:51], v[204:207], v[128:131]\n"
        "global_load_lds_dwordx4 v245, s[86:87] offset:3072\n"
        "s_add_u32 m0, s73, 0x1c000\n"
        "v_mfma_f32_16x16x32_bf16 v[8:11], v[160:163], v[192:195], v[8:11]\n"
        "ds_read_b64_tr_b16 v[48:49], v230 offset:17408\n"
        "s_add_u32 s86, s68, 0x700000\n"
        "s_addc_u32 s87, s69, 0\n"
        "v_mfma_f32_16x16x32_bf16 v[8:11], v[164:167], v[208:211], v[8:11]\n"
        "ds_read_b64_tr_b16 v[50:51], v231 offset:17408\n"
        "v_accvgpr_write_b32 a93, v252\n"
        "v_mfma_f32_16x16x32_bf16 v[12:15], v[172:175], v[208:211], v[12:15]\n"
        "ds_read_b64_tr_b16 v[54:55], v231 offset:25600\n"
        "v_mfma_f32_16x16x32_bf16 v[12:15], v[168:171], v[192:195], v[12:15]\n"
        "ds_read_b64_tr_b16 v[62:63], v233 offset:25600\n"
        "v_mfma_f32_16x16x32_bf16 v[76:79], v[168:171], v[196:199], v[76:79]\n"
        "ds_read_b64_tr_b16 v[58:59], v233 offset:17408\n"
        "v_mfma_f32_16x16x32_bf16 v[76:79], v[172:175], v[212:215], v[76:79]\n"
        "v_mfma_f32_16x16x32_bf16 v[72:75], v[164:167], v[212:215], v[72:75]\n"
        "v_mfma_f32_16x16x32_bf16 v[72:75], v[160:163], v[196:199], v[72:75]\n"
        "v_accvgpr_read_b32 v176, a88\n"
        "v_cvt_pk_bf16_f32 v176, v176, v177\n"
        "v_mfma_f32_16x16x32_bf16 v[104:107], v[160:163], v[200:203], v[104:107]\n"
        "s_mul_i32 s90, s43, 529408\n"
        "v_cvt_pk_bf16_f32 v177, v178, v179\n"
        "v_cvt_pk_bf16_f32 v178, v180, v181\n"
        "v_mfma_f32_16x16x32_bf16 v[104:107], v[164:167], v[216:219], v[104:107]\n"
        "s_add_u32 s90, s90, s72\n"
        "v_cvt_pk_bf16_f32 v179, v182, v183\n"
        "v_accvgpr_write_b32 a24, v176\n"
        "v_mfma_f32_16x16x32_bf16 v[108:111], v[172:175], v[216:219], v[108:111]\n"
        "s_and_b32 s78, s45, 1\n"
        "v_accvgpr_write_b32 a25, v177\n"
        "v_accvgpr_write_b32 a26, v178\n"
        "v_mfma_f32_16x16x32_bf16 v[108:111], v[168:171], v[200:203], v[108:111]\n"
        "s_lshl_b32 s78, s78, 15\n"
        "v_accvgpr_write_b32 a27, v179\n"
        "s_add_u32 s78, s78, 0x10000\n"
        "v_mfma_f32_16x16x32_bf16 v[140:143], v[168:171], v[204:207], v[140:143]\n"
        "s_waitcnt vmcnt(13) lgkmcnt(0)\n"
        "s_and_b32 s80, s40, 1\n"
        "s_lshl_b32 s80, s80, 11\n"
        "v_mfma_f32_16x16x32_bf16 v[140:143], v[172:175], v[220:223], v[140:143]\n"
        "s_barrier\n"
        "v_mfma_f32_16x16x32_bf16 v[136:139], v[164:167], v[220:223], v[136:139]\n"
        "global_load_lds_dwordx4 v238, s[86:87] offset:0\n"
        "ds_read_b64_tr_b16 v[164:165], v226 offset:40960\n"
        "s_and_b32 s79, s45, 1\n"
        "v_mfma_f32_16x16x32_bf16 v[136:139], v[160:163], v[204:207], v[136:139]\n"
        "ds_read_b64_tr_b16 v[160:161], v226 offset:32768\n"
        "s_lshl_b32 s79, s79, 15\n"
        "s_lshl_b32 s82, s40, 4\n"
        "v_mfma_f32_16x16x32_bf16 v[16:19], v[32:35], v[192:195], v[16:19]\n"
        "ds_read_b64_tr_b16 v[162:163], v227 offset:32768\n"
        "s_sub_u32 s83, 14, s44\n"
        "v_accvgpr_write_b32 a64, 0\n"
        "v_mfma_f32_16x16x32_bf16 v[16:19], v[36:39], v[208:211], v[16:19]\n"
        "ds_read_b64_tr_b16 v[166:167], v227 offset:40960\n"
        "s_lshl_b32 s50, s41, 7\n"
        "v_accvgpr_write_b32 a65, 0\n"
        "v_mfma_f32_16x16x32_bf16 v[20:23], v[44:47], v[208:211], v[20:23]\n"
        "ds_read_b64_tr_b16 v[172:173], v228 offset:40960\n"
        "global_load_lds_dwordx4 v239, s[86:87] offset:1024\n"
        "s_lshl2_add_u32 s50, s42, s50\n"
        "v_mfma_f32_16x16x32_bf16 v[20:23], v[40:43], v[192:195], v[20:23]\n"
        "ds_read_b64_tr_b16 v[174:175], v229 offset:40960\n"
        "s_add_u32 s50, s50, s40\n"
        "v_accvgpr_write_b32 a66, 0\n"
        "v_mfma_f32_16x16x32_bf16 v[84:87], v[40:43], v[196:199], v[84:87]\n"
        "ds_read_b64_tr_b16 v[168:169], v228 offset:32768\n"
        "s_lshl_b32 s50, s50, 12\n"
        "v_accvgpr_write_b32 a67, 0\n"
        "v_mfma_f32_16x16x32_bf16 v[84:87], v[44:47], v[212:215], v[84:87]\n"
        "ds_read_b64_tr_b16 v[170:171], v229 offset:32768\n"
        "s_mul_i32 s51, s40, 8320\n"
        "v_accvgpr_write_b32 a68, 0\n"
        "v_mfma_f32_16x16x32_bf16 v[80:83], v[36:39], v[212:215], v[80:83]\n"
        "global_load_lds_dwordx4 v240, s[86:87] offset:2048\n"
        "s_add_u32 s51, s51, 0x18000\n"
        "v_mfma_f32_16x16x32_bf16 v[80:83], v[32:35], v[196:199], v[80:83]\n"
        "s_add_u32 s92, s26, s50\n"
        "s_addc_u32 s93, s27, 0\n"
        "v_accvgpr_write_b32 a69, 0\n"
        "v_mfma_f32_16x16x32_bf16 v[112:115], v[32:35], v[200:203], v[112:115]\n"
        "s_add_u32 s50, s50, 0x3300000\n"
        "v_accvgpr_write_b32 a70, 0\n"
        "v_accvgpr_write_b32 a71, 0\n"
        "v_mfma_f32_16x16x32_bf16 v[112:115], v[36:39], v[216:219], v[112:115]\n"
        "s_add_u32 s94, s38, s50\n"
        "s_addc_u32 s95, s39, 0\n"
        "s_add_u32 s50, s51, 4128\n"
        "v_mfma_f32_16x16x32_bf16 v[116:119], v[44:47], v[216:219], v[116:119]\n"
        "global_load_lds_dwordx4 v241, s[86:87] offset:3072\n"
        "s_add_u32 m0, s72, 0x8000\n"
        "v_mfma_f32_16x16x32_bf16 v[116:119], v[40:43], v[200:203], v[116:119]\n"
        "s_add_u32 s86, s64, 0x700\n"
        "s_addc_u32 s87, s65, 0\n"
        "v_bfe_u32 v176, v254, 4, 4\n"
        "v_mfma_f32_16x16x32_bf16 v[148:151], v[40:43], v[204:207], v[148:151]\n"
        "ds_read_b64_tr_b16 v[40:41], v232 offset:32768\n"
        "v_lshrrev_b32 v177, 8, v254\n"
        "v_mfma_f32_16x16x32_bf16 v[148:151], v[44:47], v[220:223], v[148:151]\n"
        "ds_read_b64_tr_b16 v[44:45], v232 offset:40960\n"
        "v_lshrrev_b32 v178, 7, v254\n"
        "v_mfma_f32_16x16x32_bf16 v[144:147], v[36:39], v[220:223], v[144:147]\n"
        "global_load_lds_dwordx4 v242, s[86:87] offset:0\n"
        "v_mfma_f32_16x16x32_bf16 v[144:147], v[32:35], v[204:207], v[144:147]\n"
        "ds_read_b64_tr_b16 v[32:33], v230 offset:32768\n"
        "v_accvgpr_read_b32 v184, a92\n"
        "v_mfma_f32_16x16x32_bf16 v[24:27], v[48:51], v[192:195], v[24:27]\n"
        "ds_read_b64_tr_b16 v[34:35], v231 offset:32768\n"
        "v_cvt_pk_bf16_f32 v184, v184, v185\n"
        "v_mfma_f32_16x16x32_bf16 v[24:27], v[52:55], v[208:211], v[24:27]\n"
        "ds_read_b64_tr_b16 v[36:37], v230 offset:40960\n"
        "v_cvt_pk_bf16_f32 v185, v186, v187\n"
        "v_mfma_f32_16x16x32_bf16 v[28:31], v[60:63], v[208:211], v[28:31]\n"
        "LHA8_%=:\n"
        "s_cbranch_execz LHRA_%=\n"
        "ds_read_b64_tr_b16 v[38:39], v231 offset:40960\n"
        "v_cvt_pk_bf16_f32 v186, v188, v189\n"
        "v_mfma_f32_16x16x32_bf16 v[28:31], v[56:59], v[192:195], v[28:31]\n"
        "global_store_dwordx4 v254, a[168:171], s[70:71] offset:0 sc1\n"
        "ds_read_b64_tr_b16 v[46:47], v233 offset:40960\n"
        "ds_read_b64_tr_b16 v[42:43], v233 offset:32768\n"
        "v_cvt_pk_bf16_f32 v187, v190, v191\n"
        "v_mfma_f32_16x16x32_bf16 v[92:95], v[56:59], v[196:199], v[92:95]\n"
        "ds_read_b128 v[192:195], v246 offset:2048\n"
        "v_accvgpr_write_b32 a28, v184\n"
        "v_mfma_f32_16x16x32_bf16 v[92:95], v[60:63], v[212:215], v[92:95]\n"
        "ds_read_b128 v[208:211], v247 offset:2048\n"
        "v_accvgpr_write_b32 a29, v185\n"
        "v_mfma_f32_16x16x32_bf16 v[88:91], v[52:55], v[212:215], v[88:91]\n"
        "ds_read_b128 v[212:215], v247 offset:0\n"
        "v_accvgpr_write_b32 a30, v186\n"
        "v_mfma_f32_16x16x32_bf16 v[88:91], v[48:51], v[196:199], v[88:91]\n"
        "global_load_lds_dwordx4 v243, s[86:87] offset:1024\n"
        "v_accvgpr_write_b32 a31, v187\n"
        "v_mfma_f32_16x16x32_bf16 v[120:123], v[48:51], v[200:203], v[120:123]\n"
        "ds_read_b128 v[196:199], v246 offset:0\n"
        "v_lshrrev_b32 v186, 5, v254\n"
        "v_mfma_f32_16x16x32_bf16 v[120:123], v[52:55], v[216:219], v[120:123]\n"
        "v_lshrrev_b32 v187, 9, v254\n"
        "v_xor_b32 v186, v186, v187\n"
        "v_mfma_f32_16x16x32_bf16 v[124:127], v[60:63], v[216:219], v[124:127]\n"
        "ds_read_b128 v[216:219], v247 offset:6144\n"
        "v_and_b32 v186, 1, v186\n"
        "v_mfma_f32_16x16x32_bf16 v[124:127], v[56:59], v[200:203], v[124:127]\n"
        "global_load_lds_dwordx4 v244, s[86:87] offset:2048\n"
        "ds_read_b128 v[200:203], v246 offset:6144\n"
        "v_mfma_f32_16x16x32_bf16 v[156:159], v[56:59], v[204:207], v[156:159]\n"
        "ds_read_b64_tr_b16 v[56:57], v228 offset:33792\n"
        "v_cmp_eq_u32 vcc, 0, v186\n"
        "v_mfma_f32_16x16x32_bf16 v[156:159], v[60:63], v[220:223], v[156:159]\n"
        "ds_read_b64_tr_b16 v[60:61], v228 offset:41984\n"
        "v_and_b32 v187, 0x30, v254\n"
        "v_mfma_f32_16x16x32_bf16 v[152:155], v[52:55], v[220:223], v[152:155]\n"
        "ds_read_b128 v[220:223], v247 offset:4096\n"
        "v_bfe_u32 v179, v254, 4, 3\n"
        "v_mfma_f32_16x16x32_bf16 v[152:155], v[48:51], v[204:207], v[152:155]\n"
        "global_load_lds_dwordx4 v245, s[86:87] offset:3072\n"
        "s_add_u32 m0, s72, 0x9000\n"
        "s_waitcnt lgkmcnt(6)\n"
        "v_mfma_f32_16x16x32_bf16 v[0:3], v[160:163], v[192:195], v[0:3]\n"
        "ds_read_b128 v[204:207], v246 offset:4096\n"
        "s_add_u32 s86, s66, 0x700\n"
        "s_addc_u32 s87, s67, 0\n"
        "v_mfma_f32_16x16x32_bf16 v[0:3], v[164:167], v[208:211], v[0:3]\n"
        "ds_read_b64_tr_b16 v[48:49], v226 offset:33792\n"
        "v_and_b32 v180, 6, v178\n"
        "v_mfma_f32_16x16x32_bf16 v[4:7], v[172:175], v[208:211], v[4:7]\n"
        "ds_read_b64_tr_b16 v[50:51], v227 offset:33792\n"
        "v_xor_b32 v180, v180, v179\n"
        "v_mfma_f32_16x16x32_bf16 v[4:7], v[168:171], v[192:195], v[4:7]\n"
        "ds_read_b64_tr_b16 v[52:53], v226 offset:41984\n"
        "s_waitcnt lgkmcnt(7)\n"
        "v_mfma_f32_16x16x32_bf16 v[68:71], v[168:171], v[196:199], v[68:71]\n"
        "ds_read_b64_tr_b16 v[54:55], v227 offset:41984\n"
        "global_load_lds_dwordx4 v242, s[86:87] offset:0\n"
        "v_mfma_f32_16x16x32_bf16 v[68:71], v[172:175], v[212:215], v[68:71]\n"
        "ds_read_b64_tr_b16 v[62:63], v229 offset:41984\n"
        "v_lshlrev_b32 v180, 4, v180\n"
        "v_mfma_f32_16x16x32_bf16 v[64:67], v[164:167], v[212:215], v[64:67]\n"
        "ds_read_b64_tr_b16 v[58:59], v229 offset:33792\n"
        "v_lshl_add_u32 v181, v178, 11, v180\n"
        "v_mfma_f32_16x16x32_bf16 v[64:67], v[160:163], v[196:199], v[64:67]\n"
        "v_xor_b32 v178, 4, v178\n"
        "v_lshl_add_u32 v178, v178, 11, v180\n"
        "v_mfma_f32_16x16x32_bf16 v[96:99], v[160:163], v[200:203], v[96:99]\n"
        "global_load_lds_dwordx4 v243, s[86:87] offset:1024\n"
        "v_mfma_f32_16x16x32_bf16 v[96:99], v[164:167], v[216:219], v[96:99]\n"
        "s_waitcnt lgkmcnt(5)\n"
        "v_add_u32 v182, 4096, v181\n"
        "v_accvgpr_write_b32 a84, v182\n"
        "v_mfma_f32_16x16x32_bf16 v[100:103], v[172:175], v[216:219], v[100:103]\n"
        "v_add_u32 v182, 3200, v181\n"
        "v_accvgpr_write_b32 a85, v182\n"
        "v_mfma_f32_16x16x32_bf16 v[100:103], v[168:171], v[200:203], v[100:103]\n"
        "v_add_u32 v182, 18432, v178\n"
        "v_accvgpr_write_b32 a86, v182\n"
        "v_mfma_f32_16x16x32_bf16 v[132:135], v[168:171], v[204:207], v[132:135]\n"
        "global_load_lds_dwordx4 v244, s[86:87] offset:2048\n"
        "v_mfma_f32_16x16x32_bf16 v[132:135], v[172:175], v[220:223], v[132:135]\n"
        "ds_read_b64_tr_b16 v[172:173], v232 offset:41984\n"
        "v_add_u32 v182, 17536, v178\n"
        "v_mfma_f32_16x16x32_bf16 v[128:131], v[164:167], v[220:223], v[128:131]\n"
        "ds_read_b64_tr_b16 v[164:165], v230 offset:41984\n"
        "v_accvgpr_write_b32 a87, v182\n"
        "v_mfma_f32_16x16x32_bf16 v[128:131], v[160:163], v[204:207], v[128:131]\n"
        "ds_read_b64_tr_b16 v[160:161], v230 offset:33792\n"
        "v_mov_b32 v178, s96\n"
        "v_mfma_f32_16x16x32_bf16 v[8:11], v[32:35], v[192:195], v[8:11]\n"
        "global_load_lds_dwordx4 v245, s[86:87] offset:3072\n"
        "s_add_u32 m0, s73, 0x20000\n"
        "v_mfma_f32_16x16x32_bf16 v[8:11], v[36:39], v[208:211], v[8:11]\n"
        "ds_read_b64_tr_b16 v[162:163], v231 offset:33792\n"
        "s_add_u32 s86, s68, 0x780000\n"
        "s_addc_u32 s87, s69, 0\n"
        "v_mfma_f32_16x16x32_bf16 v[12:15], v[44:47], v[208:211], v[12:15]\n"
        "ds_read_b64_tr_b16 v[166:167], v231 offset:41984\n"
        "v_add_u32 v180, s51, v254\n"
        "v_mfma_f32_16x16x32_bf16 v[12:15], v[40:43], v[192:195], v[12:15]\n"
        "ds_read_b64_tr_b16 v[174:175], v233 offset:41984\n"
        "v_add_u32 v180, 4096, v180\n"
        "v_mfma_f32_16x16x32_bf16 v[76:79], v[40:43], v[196:199], v[76:79]\n"
        "ds_read_b64_tr_b16 v[168:169], v232 offset:33792\n"
        "v_writelane_b32 v180, s96, 2\n"
        "v_mfma_f32_16x16x32_bf16 v[76:79], v[44:47], v[212:215], v[76:79]\n"
        "ds_read_b64_tr_b16 v[170:171], v233 offset:33792\n"
        "s_add_u32 s96, s96, 16\n"
        "v_writelane_b32 v180, s96, 3\n"
        "v_mfma_f32_16x16x32_bf16 v[72:75], v[36:39], v[212:215], v[72:75]\n"
        "s_add_u32 s96, s51, 4098\n"
        "v_accvgpr_write_b32 a90, v180\n"
        "v_sub_u32 v179, s96, v178\n"
        "v_mfma_f32_16x16x32_bf16 v[72:75], v[32:35], v[196:199], v[72:75]\n"
        "s_add_u32 s96, s51, 480\n"
        "v_and_b32 v181, 1, v176\n"
        "v_mul_lo_u32 v179, v179, v181\n"
        "v_mfma_f32_16x16x32_bf16 v[104:107], v[32:35], v[200:203], v[104:107]\n"
        "v_add_u32 v178, v178, v179\n"
        "v_lshl_add_u32 v178, v176, 1, v178\n"
        "v_mfma_f32_16x16x32_bf16 v[104:107], v[36:39], v[216:219], v[104:107]\n"
        "v_lshl_add_u32 v178, v177, 4, v178\n"
        "v_accvgpr_write_b32 a88, v178\n"
        "v_mfma_f32_16x16x32_bf16 v[108:111], v[44:47], v[216:219], v[108:111]\n"
        "v_lshlrev_b32 v179, 5, v176\n"
        "v_sub_u32 v179, s96, v179\n"
        "v_mfma_f32_16x16x32_bf16 v[108:111], v[40:43], v[200:203], v[108:111]\n"
        "s_lshl_b32 s96, s54, 16\n"
        "v_lshl_add_u32 v179, v177, 4, v179\n"
        "v_accvgpr_write_b32 a89, v179\n"
        "v_mfma_f32_16x16x32_bf16 v[140:143], v[40:43], v[204:207], v[140:143]\n"
        "s_waitcnt lgkmcnt(1)\n"
        "v_mov_b32 v181, 0\n"
        "v_writelane_b32 v181, s96, 3\n"
        "v_mfma_f32_16x16x32_bf16 v[140:143], v[44:47], v[220:223], v[140:143]\n"
        "s_waitcnt vmcnt(13) lgkmcnt(0)\n"
        "v_accvgpr_write_b32 a91, v181\n"
        "s_and_b32 s54, s54, 0xffff0000\n"
        "v_mfma_f32_16x16x32_bf16 v[136:139], v[36:39], v[220:223], v[136:139]\n"
        "s_barrier\n"
        "v_mfma_f32_16x16x32_bf16 v[136:139], v[32:35], v[204:207], v[136:139]\n"
        "global_load_lds_dwordx4 v238, s[86:87] offset:0\n"
        "ds_read_b64_tr_b16 v[32:33], v226 offset:49152\n"
        "s_lshr_b32 s96, s40, 1\n"
        "v_mfma_f32_16x16x32_bf16 v[16:19], v[48:51], v[192:195], v[16:19]\n"
        "ds_read_b64_tr_b16 v[34:35], v227 offset:49152\n"
        "s_lshl_b32 s96, s96, 10\n"
        "v_lshrrev_b32 v178, 1, v177\n"
        "v_mfma_f32_16x16x32_bf16 v[16:19], v[52:55], v[208:211], v[16:19]\n"
        "ds_read_b64_tr_b16 v[36:37], v226 offset:57344\n"
        "v_lshlrev_b32 v179, 11, v178\n"
        "v_mfma_f32_16x16x32_bf16 v[20:23], v[60:63], v[208:211], v[20:23]\n"
        "ds_read_b64_tr_b16 v[38:39], v227 offset:57344\n"
        "v_add_u32 v179, s96, v179\n"
        "v_mfma_f32_16x16x32_bf16 v[20:23], v[56:59], v[192:195], v[20:23]\n"
        "ds_read_b64_tr_b16 v[44:45], v228 offset:57344\n"
        "global_load_lds_dwordx4 v239, s[86:87] offset:1024\n"
        "s_and_b32 s96, s40, 1\n"
        "v_mfma_f32_16x16x32_bf16 v[84:87], v[56:59], v[196:199], v[84:87]\n"
        "ds_read_b64_tr_b16 v[46:47], v229 offset:57344\n"
        "s_lshl_b32 s96, s96, 6\n"
        "v_bfe_u32 v180, v254, 6, 3\n"
        "v_mfma_f32_16x16x32_bf16 v[84:87], v[60:63], v[212:215], v[84:87]\n"
        "ds_read_b64_tr_b16 v[40:41], v228 offset:49152\n"
        "v_bfe_u32 v181, v254, 4, 2\n"
        "v_mfma_f32_16x16x32_bf16 v[80:83], v[52:55], v[212:215], v[80:83]\n"
        "ds_read_b64_tr_b16 v[42:43], v229 offset:49152\n"
        "v_lshlrev_b32 v183, 2, v178\n"
        "v_mfma_f32_16x16x32_bf16 v[80:83], v[48:51], v[196:199], v[80:83]\n"
        "global_load_lds_dwordx4 v240, s[86:87] offset:2048\n"
        "v_mfma_f32_16x16x32_bf16 v[112:115], v[48:51], v[200:203], v[112:115]\n"
        "v_add_u32 v240, s78, v254\n"
        "v_xor_b32 v183, v183, v180\n"
        "v_mfma_f32_16x16x32_bf16 v[112:115], v[52:55], v[216:219], v[112:115]\n"
        "v_bfe_u32 v184, v183, 1, 2\n"
        "v_xor_b32 v184, 0, v184\n"
        "v_mfma_f32_16x16x32_bf16 v[116:119], v[60:63], v[216:219], v[116:119]\n"
        "v_lshlrev_b32 v184, 5, v184\n"
        "v_lshl_add_u32 v183, v183, 7, v184\n"
        "v_mfma_f32_16x16x32_bf16 v[116:119], v[56:59], v[200:203], v[116:119]\n"
        "global_load_lds_dwordx4 v241, s[86:87] offset:3072\n"
        "s_add_u32 m0, s72, 0x0\n"
        "v_mfma_f32_16x16x32_bf16 v[148:151], v[56:59], v[204:207], v[148:151]\n"
        "ds_read_b64_tr_b16 v[56:57], v232 offset:49152\n"
        "s_add_u32 s86, s64, 0x780\n"
        "s_addc_u32 s87, s65, 0\n"
        "v_mfma_f32_16x16x32_bf16 v[148:151], v[60:63], v[220:223], v[148:151]\n"
        "ds_read_b64_tr_b16 v[60:61], v232 offset:57344\n"
        "s_add_u32 s64, s38, s90\n"
        "s_addc_u32 s65, s39, 0\n"
        "v_mfma_f32_16x16x32_bf16 v[144:147], v[52:55], v[220:223], v[144:147]\n"
        "ds_read_b64_tr_b16 v[52:53], v230 offset:57344\n"
        "s_lshl_b32 s90, s40, 13\n"
        "s_and_b32 s90, s90, 0x4000\n"
        "v_mfma_f32_16x16x32_bf16 v[144:147], v[48:51], v[204:207], v[144:147]\n"
        "ds_read_b64_tr_b16 v[48:49], v230 offset:49152\n"
        "global_load_lds_dwordx4 v242, s[86:87] offset:0\n"
        "s_add_u32 s91, s78, s90\n"
        "v_mfma_f32_16x16x32_bf16 v[24:27], v[160:163], v[192:195], v[24:27]\n"
        "ds_read_b64_tr_b16 v[50:51], v231 offset:49152\n"
        "s_add_u32 s91, s91, s80\n"
        "s_xor_b32 s78, s78, 0x8000\n"
        "v_mfma_f32_16x16x32_bf16 v[24:27], v[164:167], v[208:211], v[24:27]\n"
        "ds_read_b64_tr_b16 v[54:55], v231 offset:57344\n"
        "s_add_u32 s78, s78, s72\n"
        "v_lshl_add_u32 v183, v181, 3, v183\n"
        "v_mfma_f32_16x16x32_bf16 v[28:31], v[172:175], v[208:211], v[28:31]\n"
        "ds_read_b64_tr_b16 v[62:63], v233 offset:57344\n"
        "v_add_u32 v182, v183, v179\n"
        "v_mfma_f32_16x16x32_bf16 v[28:31], v[168:171], v[192:195], v[28:31]\n"
        "ds_read_b64_tr_b16 v[58:59], v233 offset:49152\n"
        "global_load_lds_dwordx4 v243, s[86:87] offset:1024\n"
        "v_mfma_f32_16x16x32_bf16 v[92:95], v[168:171], v[196:199], v[92:95]\n"
        "ds_read_b128 v[192:195], v248 offset:2048\n"
        "v_xor_b32 v182, s96, v182\n"
        "v_mfma_f32_16x16x32_bf16 v[92:95], v[172:175], v[212:215], v[92:95]\n"
        "ds_read_b128 v[208:211], v249 offset:2048\n"
        "s_lshl_b32 s96, s41, 12\n"
        "v_accvgpr_write_b32 a92, v182\n"
        "v_mfma_f32_16x16x32_bf16 v[88:91], v[164:167], v[212:215], v[88:91]\n"
        "ds_read_b128 v[212:215], v249 offset:0\n"
        "s_add_u32 s96, s96, 32768\n"
        "v_bfe_u32 v188, v254, 4, 5\n"
        "v_mfma_f32_16x16x32_bf16 v[88:91], v[160:163], v[196:199], v[88:91]\n"
        "ds_read_b128 v[196:199], v248 offset:0\n"
        "global_load_lds_dwordx4 v244, s[86:87] offset:2048\n"
        "v_mfma_f32_16x16x32_bf16 v[120:123], v[160:163], v[200:203], v[120:123]\n"
        "v_lshl_add_u32 v188, v188, 7, s96\n"
        "v_accvgpr_write_b32 a94, v188\n"
        "v_mfma_f32_16x16x32_bf16 v[120:123], v[164:167], v[216:219], v[120:123]\n"
        "v_add_u32 v241, s79, v254\n"
        "v_mfma_f32_16x16x32_bf16 v[124:127], v[172:175], v[216:219], v[124:127]\n"
        "ds_read_b128 v[216:219], v249 offset:6144\n"
        "v_mfma_f32_16x16x32_bf16 v[124:127], v[168:171], v[200:203], v[124:127]\n"
        "ds_read_b128 v[200:203], v248 offset:6144\n"
        "global_load_lds_dwordx4 v245, s[86:87] offset:3072\n"
        "s_add_u32 m0, s72, 0x1000\n"
        "v_mfma_f32_16x16x32_bf16 v[156:159], v[168:171], v[204:207], v[156:159]\n"
        "ds_read_b64_tr_b16 v[168:169], v228 offset:50176\n"
        "s_add_u32 s86, s66, 0x780\n"
        "s_addc_u32 s87, s67, 0\n"
        "v_mfma_f32_16x16x32_bf16 v[156:159], v[172:175], v[220:223], v[156:159]\n"
        "ds_read_b64_tr_b16 v[172:173], v228 offset:58368\n"
        "s_add_u32 s66, s64, 0x1100000\n"
        "s_addc_u32 s67, s65, 0\n"
        "v_mfma_f32_16x16x32_bf16 v[152:155], v[164:167], v[220:223], v[152:155]\n"
        "ds_read_b128 v[220:223], v249 offset:4096\n"
        "s_waitcnt lgkmcnt(6)\n"
        "v_mfma_f32_16x16x32_bf16 v[152:155], v[160:163], v[204:207], v[152:155]\n"
        "ds_read_b128 v[204:207], v248 offset:4096\n"
        "global_load_lds_dwordx4 v242, s[86:87] offset:0\n"
        "v_mfma_f32_16x16x32_bf16 v[0:3], v[32:35], v[192:195], v[0:3]\n"
        "ds_read_b64_tr_b16 v[160:161], v226 offset:50176\n"
        "v_lshrrev_b32 v242, 6, v254\n"
        "v_mfma_f32_16x16x32_bf16 v[0:3], v[36:39], v[208:211], v[0:3]\n"
        "ds_read_b64_tr_b16 v[162:163], v227 offset:50176\n"
        "v_and_b32 v242, 12, v242\n"
        "v_mfma_f32_16x16x32_bf16 v[4:7], v[44:47], v[208:211], v[4:7]\n"
        "ds_read_b64_tr_b16 v[164:165], v226 offset:58368\n"
        "s_waitcnt lgkmcnt(8)\n"
        "v_mfma_f32_16x16x32_bf16 v[4:7], v[40:43], v[192:195], v[4:7]\n"
        "ds_read_b64_tr_b16 v[166:167], v227 offset:58368\n"
        "global_load_lds_dwordx4 v243, s[86:87] offset:1024\n"
        "v_mfma_f32_16x16x32_bf16 v[68:71], v[40:43], v[196:199], v[68:71]\n"
        "ds_read_b64_tr_b16 v[174:175], v229 offset:58368\n"
        "v_bfe_u32 v243, v254, 4, 4\n"
        "v_mfma_f32_16x16x32_bf16 v[68:71], v[44:47], v[212:215], v[68:71]\n"
        "ds_read_b64_tr_b16 v[170:171], v229 offset:50176\n"
        "v_sub_u32 v243, v243, v242\n"
        "v_mfma_f32_16x16x32_bf16 v[64:67], v[36:39], v[212:215], v[64:67]\n"
        "s_waitcnt lgkmcnt(7)\n"
        "v_add_u32 v243, s82, v243\n"
        "v_cmp_gt_i32 s[10:11], 0, v243\n"
        "v_mfma_f32_16x16x32_bf16 v[64:67], v[32:35], v[196:199], v[64:67]\n"
        "global_load_lds_dwordx4 v244, s[86:87] offset:2048\n"
        "s_mov_b32 s82, -1\n"
        "v_mfma_f32_16x16x32_bf16 v[96:99], v[32:35], v[200:203], v[96:99]\n"
        "s_waitcnt lgkmcnt(5)\n"
        "v_add_u32 v244, s91, v254\n"
        "s_xor_b32 s91, s79, 0x8000\n"
        "v_mfma_f32_16x16x32_bf16 v[96:99], v[36:39], v[216:219], v[96:99]\n"
        "s_add_u32 s91, s91, s90\n"
        "s_lshl1_add_u32 s91, s80, s91\n"
        "v_cmp_gt_i32 s[12:13], 1, v243\n"
        "v_mfma_f32_16x16x32_bf16 v[100:103], v[44:47], v[216:219], v[100:103]\n"
        "s_add_u32 s79, s79, s72\n"
        "v_cmp_gt_i32 s[14:15], 2, v243\n"
        "v_cmp_gt_i32 s[16:17], 3, v243\n"
        "v_mfma_f32_16x16x32_bf16 v[100:103], v[40:43], v[200:203], v[100:103]\n"
        "global_load_lds_dwordx4 v245, s[86:87] offset:3072\n"
        "s_lshl_b32 s80, s41, 5\n"
        "v_mfma_f32_16x16x32_bf16 v[132:135], v[40:43], v[204:207], v[132:135]\n"
        "ds_read_b64_tr_b16 v[40:41], v232 offset:50176\n"
        "s_add_u32 s80, s80, 256\n"
        "v_add_u32 v245, s91, v254\n"
        "v_mfma_f32_16x16x32_bf16 v[132:135], v[44:47], v[220:223], v[132:135]\n"
        "ds_read_b64_tr_b16 v[44:45], v232 offset:58368\n"
        "s_add_u32 s81, s80, s42\n"
        "v_cmp_gt_i32 s[20:21], 16, v243\n"
        "v_mfma_f32_16x16x32_bf16 v[128:131], v[36:39], v[220:223], v[128:131]\n"
        "ds_read_b64_tr_b16 v[36:37], v230 offset:58368\n"
        "s_lshl_b32 s81, s81, 7\n"
        "v_cmp_gt_i32 s[22:23], 17, v243\n"
        "v_mfma_f32_16x16x32_bf16 v[128:131], v[32:35], v[204:207], v[128:131]\n"
        "ds_read_b64_tr_b16 v[32:33], v230 offset:50176\n"
        "s_and_b32 s80, s81, 0xfffffc7f\n"
        "v_cmp_gt_i32 s[24:25], 18, v243\n"
        "v_mfma_f32_16x16x32_bf16 v[8:11], v[48:51], v[192:195], v[8:11]\n"
        "global_store_dwordx4 v254, a[172:175], s[70:71] offset:1024 sc1\n"
        "ds_read_b64_tr_b16 v[34:35], v231 offset:50176\n"
        "ds_read_b64_tr_b16 v[38:39], v231 offset:58368\n"
        "s_add_u32 s70, s38, s97\n"
        "s_addc_u32 s71, s39, 0\n"
        "v_mfma_f32_16x16x32_bf16 v[8:11], v[52:55], v[208:211], v[8:11]\n"
        "ds_read_b64_tr_b16 v[46:47], v233 offset:58368\n"
        "s_add_u32 s97, s52, 0x0\n"
        "v_cmp_gt_i32 s[28:29], 19, v243\n"
        "v_mfma_f32_16x16x32_bf16 v[12:15], v[60:63], v[208:211], v[12:15]\n"
        "ds_read_b64_tr_b16 v[42:43], v233 offset:50176\n"
        "s_xor_b32 s79, s79, 0x8000\n"
        "v_cmp_gt_i32 s[30:31], 32, v243\n"
        "v_mfma_f32_16x16x32_bf16 v[12:15], v[56:59], v[192:195], v[12:15]\n"
        "v_cmp_gt_i32 s[56:57], 33, v243\n"
        "v_cmp_gt_i32 s[58:59], 34, v243\n"
        "v_mfma_f32_16x16x32_bf16 v[76:79], v[56:59], v[196:199], v[76:79]\n"
        "global_store_dwordx4 v254, a[0:3], s[70:71] offset:0 sc1\n"
        "v_cmp_gt_i32 s[60:61], 35, v243\n"
        "v_cmp_gt_i32 s[62:63], 48, v243\n"
        "v_cmp_gt_i32 s[84:85], 49, v243\n"
        "v_cmp_gt_i32 s[86:87], 50, v243\n"
        "v_mfma_f32_16x16x32_bf16 v[76:79], v[60:63], v[212:215], v[76:79]\n"
        "v_cmp_gt_i32 s[88:89], 51, v243\n"
        "v_mov_b32 v242, 0x3f803f80\n"
        "v_mfma_f32_16x16x32_bf16 v[72:75], v[52:55], v[212:215], v[72:75]\n"
        "v_accvgpr_write_b32 a80, v242\n"
        "v_accvgpr_write_b32 a81, v242\n"
        "v_mfma_f32_16x16x32_bf16 v[72:75], v[48:51], v[196:199], v[72:75]\n"
        "v_accvgpr_write_b32 a82, v242\n"
        "v_accvgpr_write_b32 a83, v242\n"
        "v_mfma_f32_16x16x32_bf16 v[104:107], v[48:51], v[200:203], v[104:107]\n"
        "v_cndmask_b32 v186, 0, v242, vcc\n"
        "v_accvgpr_write_b32 a72, v186\n"
        "v_mfma_f32_16x16x32_bf16 v[104:107], v[52:55], v[216:219], v[104:107]\n"
        "s_waitcnt vmcnt(14) lgkmcnt(0)\n"
        "v_accvgpr_write_b32 a73, v186\n"
        "v_mfma_f32_16x16x32_bf16 v[108:111], v[60:63], v[216:219], v[108:111]\n"
        "s_barrier\n"
        "v_mfma_f32_16x16x32_bf16 v[108:111], v[56:59], v[200:203], v[108:111]\n"
        "v_accvgpr_write_b32 a74, v186\n"
        "v_accvgpr_write_b32 a75, v186\n"
        "v_mfma_f32_16x16x32_bf16 v[140:143], v[56:59], v[204:207], v[140:143]\n"
        "ds_read_b64_tr_b16 v[56:57], v228 offset:16384\n"
        "v_cmp_eq_u32 vcc, 0, v187\n"
        "v_mfma_f32_16x16x32_bf16 v[140:143], v[60:63], v[220:223], v[140:143]\n"
        "ds_read_b64_tr_b16 v[60:61], v228 offset:24576\n"
        "v_cndmask_b32 v187, 0, v242, vcc\n"
        "v_mfma_f32_16x16x32_bf16 v[136:139], v[52:55], v[220:223], v[136:139]\n"
        "ds_read_b64_tr_b16 v[52:53], v226 offset:24576\n"
        "v_accvgpr_write_b32 a76, v187\n"
        "v_mfma_f32_16x16x32_bf16 v[136:139], v[48:51], v[204:207], v[136:139]\n"
        "ds_read_b64_tr_b16 v[48:49], v226 offset:16384\n"
        "v_accvgpr_write_b32 a77, v187\n"
        "v_mfma_f32_16x16x32_bf16 v[16:19], v[160:163], v[192:195], v[16:19]\n"
        "ds_read_b64_tr_b16 v[50:51], v227 offset:16384\n"
        "v_accvgpr_write_b32 a78, v187\n"
        "v_mfma_f32_16x16x32_bf16 v[16:19], v[164:167], v[208:211], v[16:19]\n"
        "ds_read_b64_tr_b16 v[54:55], v227 offset:24576\n"
        "v_accvgpr_write_b32 a79, v187\n"
        "v_mfma_f32_16x16x32_bf16 v[20:23], v[172:175], v[208:211], v[20:23]\n"
        "ds_read_b64_tr_b16 v[62:63], v229 offset:24576\n"
        "v_mov_b32 v242, 0xff800000\n"
        "v_mfma_f32_16x16x32_bf16 v[20:23], v[168:171], v[192:195], v[20:23]\n"
        "ds_read_b64_tr_b16 v[58:59], v229 offset:16384\n"
        "v_xor_b32 v241, 0x8000, v241\n"
        "v_mfma_f32_16x16x32_bf16 v[84:87], v[168:171], v[196:199], v[84:87]\n"
        "global_store_dwordx4 v254, a[4:7], s[70:71] offset:1024 sc1\n"
        "s_add_u32 s70, s38, s97\n"
        "s_addc_u32 s71, s39, 0\n"
        "s_add_u32 s97, s53, 0x2000\n"
        "v_mfma_f32_16x16x32_bf16 v[84:87], v[172:175], v[212:215], v[84:87]\n"
        "v_mfma_f32_16x16x32_bf16 v[80:83], v[164:167], v[212:215], v[80:83]\n"
        "v_mfma_f32_16x16x32_bf16 v[80:83], v[160:163], v[196:199], v[80:83]\n"
        "v_mfma_f32_16x16x32_bf16 v[112:115], v[160:163], v[200:203], v[112:115]\n"
        "v_mfma_f32_16x16x32_bf16 v[112:115], v[164:167], v[216:219], v[112:115]\n"
        "v_mfma_f32_16x16x32_bf16 v[116:119], v[172:175], v[216:219], v[116:119]\n"
        "v_mfma_f32_16x16x32_bf16 v[116:119], v[168:171], v[200:203], v[116:119]\n"
        "v_mfma_f32_16x16x32_bf16 v[148:151], v[168:171], v[204:207], v[148:151]\n"
        "ds_read_b64_tr_b16 v[168:169], v232 offset:16384\n"
        "v_mfma_f32_16x16x32_bf16 v[148:151], v[172:175], v[220:223], v[148:151]\n"
        "ds_read_b64_tr_b16 v[172:173], v232 offset:24576\n"
        "v_mfma_f32_16x16x32_bf16 v[144:147], v[164:167], v[220:223], v[144:147]\n"
        "ds_read_b64_tr_b16 v[164:165], v230 offset:24576\n"
        "v_mfma_f32_16x16x32_bf16 v[144:147], v[160:163], v[204:207], v[144:147]\n"
        "global_store_dwordx4 v254, a[8:11], s[70:71] offset:0 sc1\n"
        "ds_read_b64_tr_b16 v[160:161], v230 offset:16384\n"
        "ds_read_b64_tr_b16 v[162:163], v231 offset:16384\n"
        "v_mfma_f32_16x16x32_bf16 v[24:27], v[32:35], v[192:195], v[24:27]\n"
        "ds_read_b64_tr_b16 v[166:167], v231 offset:24576\n"
        "v_mfma_f32_16x16x32_bf16 v[24:27], v[36:39], v[208:211], v[24:27]\n"
        "ds_read_b64_tr_b16 v[174:175], v233 offset:24576\n"
        "v_mfma_f32_16x16x32_bf16 v[28:31], v[44:47], v[208:211], v[28:31]\n"
        "ds_read_b64_tr_b16 v[170:171], v233 offset:16384\n"
        "v_mfma_f32_16x16x32_bf16 v[28:31], v[40:43], v[192:195], v[28:31]\n"
        "ds_read_b128 v[192:195], v246 offset:34816\n"
        "v_mfma_f32_16x16x32_bf16 v[92:95], v[40:43], v[196:199], v[92:95]\n"
        "ds_read_b128 v[208:211], v247 offset:34816\n"
        "v_mfma_f32_16x16x32_bf16 v[92:95], v[44:47], v[212:215], v[92:95]\n"
        "v_mfma_f32_16x16x32_bf16 v[88:91], v[36:39], v[212:215], v[88:91]\n"
        "ds_read_b128 v[212:215], v247 offset:32768\n"
        "v_mfma_f32_16x16x32_bf16 v[88:91], v[32:35], v[196:199], v[88:91]\n"
        "ds_read_b128 v[196:199], v246 offset:32768\n"
        "v_mfma_f32_16x16x32_bf16 v[120:123], v[32:35], v[200:203], v[120:123]\n"
        "v_mfma_f32_16x16x32_bf16 v[120:123], v[36:39], v[216:219], v[120:123]\n"
        "v_mfma_f32_16x16x32_bf16 v[124:127], v[44:47], v[216:219], v[124:127]\n"
        "ds_read_b128 v[216:219], v247 offset:38912\n"
        "v_mfma_f32_16x16x32_bf16 v[124:127], v[40:43], v[200:203], v[124:127]\n"
        "ds_read_b128 v[200:203], v246 offset:38912\n"
        "v_mfma_f32_16x16x32_bf16 v[156:159], v[40:43], v[204:207], v[156:159]\n"
        "ds_read_b64_tr_b16 v[40:41], v228 offset:17408\n"
        "s_waitcnt lgkmcnt(5)\n"
        "v_mfma_f32_16x16x32_bf16 v[156:159], v[44:47], v[220:223], v[156:159]\n"
        "ds_read_b64_tr_b16 v[44:45], v228 offset:25600\n"
        "v_mfma_f32_16x16x32_bf16 v[152:155], v[36:39], v[220:223], v[152:155]\n"
        "ds_read_b128 v[220:223], v247 offset:36864\n"
        "v_mfma_f32_16x16x32_bf16 v[152:155], v[32:35], v[204:207], v[152:155]\n"
        "ds_read_b128 v[204:207], v246 offset:36864\n"
        "v_mfma_f32_16x16x32_bf16 v[0:3], v[48:51], v[192:195], v[0:3]\n"
        "global_store_dwordx4 v254, a[12:15], s[70:71] offset:1024 sc1\n"
        "ds_read_b64_tr_b16 v[32:33], v226 offset:17408\n"
        "ds_read_b64_tr_b16 v[34:35], v227 offset:17408\n"
        "s_add_u32 s70, s38, s97\n"
        "s_addc_u32 s71, s39, 0\n"
        "v_mfma_f32_16x16x32_bf16 v[0:3], v[52:55], v[208:211], v[0:3]\n"
        "ds_read_b64_tr_b16 v[36:37], v226 offset:25600\n"
        "s_add_u32 s97, s53, 0x0\n"
        "s_waitcnt lgkmcnt(8)\n"
        "v_mfma_f32_16x16x32_bf16 v[4:7], v[60:63], v[208:211], v[4:7]\n"
        "ds_read_b64_tr_b16 v[38:39], v227 offset:25600\n"
        "v_mfma_f32_16x16x32_bf16 v[4:7], v[56:59], v[192:195], v[4:7]\n"
        "ds_read_b64_tr_b16 v[46:47], v229 offset:25600\n"
        "v_mfma_f32_16x16x32_bf16 v[68:71], v[56:59], v[196:199], v[68:71]\n"
        "ds_read_b64_tr_b16 v[42:43], v229 offset:17408\n"
        "s_waitcnt lgkmcnt(8)\n"
        "v_mfma_f32_16x16x32_bf16 v[68:71], v[60:63], v[212:215], v[68:71]\n"
        "v_mfma_f32_16x16x32_bf16 v[64:67], v[52:55], v[212:215], v[64:67]\n"
        "v_mfma_f32_16x16x32_bf16 v[64:67], v[48:51], v[196:199], v[64:67]\n"
        "v_mfma_f32_16x16x32_bf16 v[96:99], v[48:51], v[200:203], v[96:99]\n"
        "s_waitcnt lgkmcnt(4)\n"
        "v_mfma_f32_16x16x32_bf16 v[96:99], v[52:55], v[216:219], v[96:99]\n"
        "v_mfma_f32_16x16x32_bf16 v[100:103], v[60:63], v[216:219], v[100:103]\n"
        "v_mfma_f32_16x16x32_bf16 v[100:103], v[56:59], v[200:203], v[100:103]\n"
        "global_store_dwordx4 v254, a[208:211], s[70:71] offset:0 sc1\n"
        "v_mfma_f32_16x16x32_bf16 v[132:135], v[56:59], v[204:207], v[132:135]\n"
        "ds_read_b64_tr_b16 v[56:57], v232 offset:17408\n"
        "v_mfma_f32_16x16x32_bf16 v[132:135], v[60:63], v[220:223], v[132:135]\n"
        "ds_read_b64_tr_b16 v[60:61], v232 offset:25600\n"
        "v_mfma_f32_16x16x32_bf16 v[128:131], v[52:55], v[220:223], v[128:131]\n"
        "ds_read_b64_tr_b16 v[52:53], v230 offset:25600\n"
        "v_mfma_f32_16x16x32_bf16 v[128:131], v[48:51], v[204:207], v[128:131]\n"
        "ds_read_b64_tr_b16 v[48:49], v230 offset:17408\n"
        "v_mfma_f32_16x16x32_bf16 v[8:11], v[160:163], v[192:195], v[8:11]\n"
        "ds_read_b64_tr_b16 v[50:51], v231 offset:17408\n"
        "v_mfma_f32_16x16x32_bf16 v[8:11], v[164:167], v[208:211], v[8:11]\n"
        "ds_read_b64_tr_b16 v[54:55], v231 offset:25600\n"
        "v_mfma_f32_16x16x32_bf16 v[12:15], v[172:175], v[208:211], v[12:15]\n"
        "ds_read_b64_tr_b16 v[62:63], v233 offset:25600\n"
        "v_mfma_f32_16x16x32_bf16 v[12:15], v[168:171], v[192:195], v[12:15]\n"
        "ds_read_b64_tr_b16 v[58:59], v233 offset:17408\n"
        "v_mfma_f32_16x16x32_bf16 v[76:79], v[168:171], v[196:199], v[76:79]\n"
        "v_mfma_f32_16x16x32_bf16 v[76:79], v[172:175], v[212:215], v[76:79]\n"
        "v_mfma_f32_16x16x32_bf16 v[72:75], v[164:167], v[212:215], v[72:75]\n"
        "v_mfma_f32_16x16x32_bf16 v[72:75], v[160:163], v[196:199], v[72:75]\n"
        "v_mfma_f32_16x16x32_bf16 v[104:107], v[160:163], v[200:203], v[104:107]\n"
        "v_mfma_f32_16x16x32_bf16 v[104:107], v[164:167], v[216:219], v[104:107]\n"
        "v_mfma_f32_16x16x32_bf16 v[108:111], v[172:175], v[216:219], v[108:111]\n"
        "v_mfma_f32_16x16x32_bf16 v[108:111], v[168:171], v[200:203], v[108:111]\n"
        "global_store_dwordx4 v254, a[240:243], s[70:71] offset:1024 sc1\n"
        "s_waitcnt vmcnt(7) lgkmcnt(0)\n"
        "s_barrier\n"
        "v_mfma_f32_16x16x32_bf16 v[140:143], v[168:171], v[204:207], v[140:143]\n"
        "ds_write_b128 v244, a[200:203] offset:0\n"
        "v_mfma_f32_16x16x32_bf16 v[140:143], v[172:175], v[220:223], v[140:143]\n"
        "ds_read_b64_tr_b16 v[172:173], v228 offset:40960\n"
        "v_mfma_f32_16x16x32_bf16 v[136:139], v[164:167], v[220:223], v[136:139]\n"
        "ds_read_b64_tr_b16 v[164:165], v226 offset:40960\n"
        "v_mfma_f32_16x16x32_bf16 v[136:139], v[160:163], v[204:207], v[136:139]\n"
        "ds_write_b128 v244, a[204:207] offset:1024\n"
        "v_mfma_f32_16x16x32_bf16 v[16:19], v[32:35], v[192:195], v[16:19]\n"
        "ds_read_b64_tr_b16 v[160:161], v226 offset:32768\n"
        "global_load_dword v253, v252, s[46:47] offset:0\n"
        "v_mfma_f32_16x16x32_bf16 v[16:19], v[36:39], v[208:211], v[16:19]\n"
        "ds_read_b64_tr_b16 v[162:163], v227 offset:32768\n"
        "v_mfma_f32_16x16x32_bf16 v[20:23], v[44:47], v[208:211], v[20:23]\n"
        "ds_write_b128 v244, a[160:163] offset:4096\n"
        "v_mfma_f32_16x16x32_bf16 v[20:23], v[40:43], v[192:195], v[20:23]\n"
        "ds_read_b64_tr_b16 v[166:167], v227 offset:40960\n"
        "v_mfma_f32_16x16x32_bf16 v[84:87], v[40:43], v[196:199], v[84:87]\n"
        "ds_read_b64_tr_b16 v[174:175], v229 offset:40960\n"
        "v_mfma_f32_16x16x32_bf16 v[84:87], v[44:47], v[212:215], v[84:87]\n"
        "ds_write_b128 v244, a[164:167] offset:5120\n"
        "v_mfma_f32_16x16x32_bf16 v[80:83], v[36:39], v[212:215], v[80:83]\n"
        "ds_read_b64_tr_b16 v[168:169], v228 offset:32768\n"
        "v_mfma_f32_16x16x32_bf16 v[80:83], v[32:35], v[196:199], v[80:83]\n"
        "ds_read_b64_tr_b16 v[170:171], v229 offset:32768\n"
        "v_mfma_f32_16x16x32_bf16 v[112:115], v[32:35], v[200:203], v[112:115]\n"
        "ds_write_b128 v244, a[168:171] offset:8192\n"
        "v_mfma_f32_16x16x32_bf16 v[112:115], v[36:39], v[216:219], v[112:115]\n"
        "global_store_dwordx4 v254, a[212:215], s[70:71] offset:2048 sc1\n"
        "v_mfma_f32_16x16x32_bf16 v[116:119], v[44:47], v[216:219], v[116:119]\n"
        "v_mfma_f32_16x16x32_bf16 v[116:119], v[40:43], v[200:203], v[116:119]\n"
        "ds_write_b128 v244, a[172:175] offset:9216\n"
        "v_mfma_f32_16x16x32_bf16 v[148:151], v[40:43], v[204:207], v[148:151]\n"
        "ds_read_b64_tr_b16 v[40:41], v232 offset:32768\n"
        "v_mfma_f32_16x16x32_bf16 v[148:151], v[44:47], v[220:223], v[148:151]\n"
        "ds_read_b64_tr_b16 v[44:45], v232 offset:40960\n"
        "v_mfma_f32_16x16x32_bf16 v[144:147], v[36:39], v[220:223], v[144:147]\n"
        "ds_write_b128 v244, a[0:3] offset:12288\n"
        "v_mfma_f32_16x16x32_bf16 v[144:147], v[32:35], v[204:207], v[144:147]\n"
        "ds_read_b64_tr_b16 v[32:33], v230 offset:32768\n"
        "v_mfma_f32_16x16x32_bf16 v[24:27], v[48:51], v[192:195], v[24:27]\n"
        "ds_read_b64_tr_b16 v[34:35], v231 offset:32768\n"
        "v_mfma_f32_16x16x32_bf16 v[24:27], v[52:55], v[208:211], v[24:27]\n"
        "ds_write_b128 v244, a[4:7] offset:13312\n"
        "v_mfma_f32_16x16x32_bf16 v[28:31], v[60:63], v[208:211], v[28:31]\n"
        "ds_read_b64_tr_b16 v[36:37], v230 offset:40960\n"
        "v_xor_b32 v244, 0xc000, v244\n"
        "v_mfma_f32_16x16x32_bf16 v[28:31], v[56:59], v[192:195], v[28:31]\n"
        "ds_read_b64_tr_b16 v[38:39], v231 offset:40960\n"
        "v_mfma_f32_16x16x32_bf16 v[92:95], v[56:59], v[196:199], v[92:95]\n"
        "ds_read_b64_tr_b16 v[46:47], v233 offset:40960\n"
        "v_mfma_f32_16x16x32_bf16 v[92:95], v[60:63], v[212:215], v[92:95]\n"
        "ds_read_b64_tr_b16 v[42:43], v233 offset:32768\n"
        "v_mfma_f32_16x16x32_bf16 v[88:91], v[52:55], v[212:215], v[88:91]\n"
        "ds_read_b128 v[192:195], v246 offset:2048\n"
        "v_mfma_f32_16x16x32_bf16 v[88:91], v[48:51], v[196:199], v[88:91]\n"
        "ds_read_b128 v[208:211], v247 offset:2048\n"
        "v_mfma_f32_16x16x32_bf16 v[120:123], v[48:51], v[200:203], v[120:123]\n"
        "ds_read_b128 v[196:199], v246 offset:0\n"
        "v_mfma_f32_16x16x32_bf16 v[120:123], v[52:55], v[216:219], v[120:123]\n"
        "ds_read_b128 v[212:215], v247 offset:0\n"
        "v_mfma_f32_16x16x32_bf16 v[124:127], v[60:63], v[216:219], v[124:127]\n"
        "global_store_dwordx4 v254, a[244:247], s[70:71] offset:3072 sc1\n"
        "ds_read_b128 v[216:219], v247 offset:6144\n"
        "s_add_u32 s70, s38, s97\n"
        "s_addc_u32 s71, s39, 0\n"
        "s_add_u32 s97, s53, 0x0\n"
        "s_add_u32 s97, s97, s75\n"
        "v_mfma_f32_16x16x32_bf16 v[124:127], v[56:59], v[200:203], v[124:127]\n"
        "ds_read_b128 v[200:203], v246 offset:6144\n"
        "v_mfma_f32_16x16x32_bf16 v[156:159], v[56:59], v[204:207], v[156:159]\n"
        "ds_read_b64_tr_b16 v[56:57], v228 offset:33792\n"
        "v_mfma_f32_16x16x32_bf16 v[156:159], v[60:63], v[220:223], v[156:159]\n"
        "ds_read_b64_tr_b16 v[60:61], v228 offset:41984\n"
        "s_waitcnt lgkmcnt(7)\n"
        "v_mfma_f32_16x16x32_bf16 v[152:155], v[52:55], v[220:223], v[152:155]\n"
        "ds_read_b128 v[220:223], v247 offset:4096\n"
        "s_waitcnt lgkmcnt(7)\n"
        "v_mfma_f32_16x16x32_bf16 v[152:155], v[48:51], v[204:207], v[152:155]\n"
        "LHA9_%=:\n"
        "s_cbranch_execz LHRA_%=\n"
        "ds_read_b128 v[204:207], v246 offset:4096\n"
        "v_mfma_f32_16x16x32_bf16 v[0:3], v[160:163], v[192:195], v[0:3]\n"
        "ds_read_b64_tr_b16 v[48:49], v226 offset:33792\n"
        "s_waitcnt lgkmcnt(7)\n"
        "v_mfma_f32_16x16x32_bf16 v[0:3], v[164:167], v[208:211], v[0:3]\n"
        "ds_read_b64_tr_b16 v[50:51], v227 offset:33792\n"
        "v_mfma_f32_16x16x32_bf16 v[4:7], v[172:175], v[208:211], v[4:7]\n"
        "ds_read_b64_tr_b16 v[52:53], v226 offset:41984\n"
        "v_mfma_f32_16x16x32_bf16 v[4:7], v[168:171], v[192:195], v[4:7]\n"
        "ds_read_b64_tr_b16 v[54:55], v227 offset:41984\n"
        "v_mfma_f32_16x16x32_bf16 v[68:71], v[168:171], v[196:199], v[68:71]\n"
        "ds_read_b64_tr_b16 v[62:63], v229 offset:41984\n"
        "s_waitcnt lgkmcnt(8)\n"
        "v_mfma_f32_16x16x32_bf16 v[68:71], v[172:175], v[212:215], v[68:71]\n"
        "ds_read_b64_tr_b16 v[58:59], v229 offset:33792\n"
        "v_cvt_pk_bf16_f32 v0, v0, v1\n"
        "v_mfma_f32_16x16x32_bf16 v[64:67], v[164:167], v[212:215], v[64:67]\n"
        "ds_write_b128 v244, a[8:11] offset:0\n"
        "v_mfma_f32_16x16x32_bf16 v[64:67], v[160:163], v[196:199], v[64:67]\n"
        "global_store_dwordx4 v254, a[216:219], s[70:71] offset:0 sc1\n"
        "v_cvt_pk_bf16_f32 v1, v2, v3\n"
        "v_cvt_pk_bf16_f32 v2, v4, v5\n"
        "v_cvt_pk_bf16_f32 v3, v6, v7\n"
        "v_mfma_f32_16x16x32_bf16 v[96:99], v[160:163], v[200:203], v[96:99]\n"
        "s_waitcnt lgkmcnt(5)\n"
        "v_mfma_f32_16x16x32_bf16 v[96:99], v[164:167], v[216:219], v[96:99]\n"
        "ds_write_b128 v244, a[12:15] offset:1024\n"
        "v_mfma_f32_16x16x32_bf16 v[100:103], v[172:175], v[216:219], v[100:103]\n"
        "v_cvt_pk_bf16_f32 v64, v64, v65\n"
        "v_cvt_pk_bf16_f32 v65, v66, v67\n"
        "v_mfma_f32_16x16x32_bf16 v[100:103], v[168:171], v[200:203], v[100:103]\n"
        "v_cvt_pk_bf16_f32 v66, v68, v69\n"
        "v_cvt_pk_bf16_f32 v67, v70, v71\n"
        "v_mfma_f32_16x16x32_bf16 v[132:135], v[168:171], v[204:207], v[132:135]\n"
        "ds_write_b128 v244, a[192:195] offset:4096\n"
        "v_mfma_f32_16x16x32_bf16 v[132:135], v[172:175], v[220:223], v[132:135]\n"
        "ds_read_b64_tr_b16 v[172:173], v232 offset:41984\n"
        "v_cvt_pk_bf16_f32 v96, v96, v97\n"
        "v_mfma_f32_16x16x32_bf16 v[128:131], v[164:167], v[220:223], v[128:131]\n"
        "ds_read_b64_tr_b16 v[164:165], v230 offset:41984\n"
        "v_cvt_pk_bf16_f32 v97, v98, v99\n"
        "v_mfma_f32_16x16x32_bf16 v[128:131], v[160:163], v[204:207], v[128:131]\n"
        "ds_write_b128 v244, a[196:199] offset:5120\n"
        "v_mfma_f32_16x16x32_bf16 v[8:11], v[32:35], v[192:195], v[8:11]\n"
        "ds_read_b64_tr_b16 v[160:161], v230 offset:33792\n"
        "v_cvt_pk_bf16_f32 v98, v100, v101\n"
        "v_mfma_f32_16x16x32_bf16 v[8:11], v[36:39], v[208:211], v[8:11]\n"
        "ds_read_b64_tr_b16 v[162:163], v231 offset:33792\n"
        "v_cvt_pk_bf16_f32 v99, v102, v103\n"
        "v_mfma_f32_16x16x32_bf16 v[12:15], v[44:47], v[208:211], v[12:15]\n"
        "ds_read_b64_tr_b16 v[166:167], v231 offset:41984\n"
        "v_cvt_pk_bf16_f32 v128, v128, v129\n"
        "v_mfma_f32_16x16x32_bf16 v[12:15], v[40:43], v[192:195], v[12:15]\n"
        "ds_read_b64_tr_b16 v[174:175], v233 offset:41984\n"
        "v_cvt_pk_bf16_f32 v129, v130, v131\n"
        "v_mfma_f32_16x16x32_bf16 v[76:79], v[40:43], v[196:199], v[76:79]\n"
        "ds_read_b64_tr_b16 v[168:169], v232 offset:33792\n"
        "v_cvt_pk_bf16_f32 v4, v8, v9\n"
        "v_mfma_f32_16x16x32_bf16 v[76:79], v[44:47], v[212:215], v[76:79]\n"
        "ds_read_b64_tr_b16 v[170:171], v233 offset:33792\n"
        "v_cvt_pk_bf16_f32 v5, v10, v11\n"
        "v_mfma_f32_16x16x32_bf16 v[72:75], v[36:39], v[212:215], v[72:75]\n"
        "ds_write_b128 v244, a[232:235] offset:8192\n"
        "v_mfma_f32_16x16x32_bf16 v[72:75], v[32:35], v[196:199], v[72:75]\n"
        "global_store_dwordx4 v254, a[248:251], s[70:71] offset:1024 sc1\n"
        "s_add_u32 s82, s82, 1\n"
        "v_cvt_pk_bf16_f32 v6, v12, v13\n"
        "v_cvt_pk_bf16_f32 v7, v14, v15\n"
        "v_cvt_pk_bf16_f32 v70, v76, v77\n"
        "v_cvt_pk_bf16_f32 v71, v78, v79\n"
        "v_mfma_f32_16x16x32_bf16 v[104:107], v[32:35], v[200:203], v[104:107]\n"
        "s_cmp_ge_u32 s82, s44\n"
        "s_addc_u32 s90, s82, 0\n"
        "v_cvt_pk_bf16_f32 v68, v72, v73\n"
        "v_mfma_f32_16x16x32_bf16 v[104:107], v[36:39], v[216:219], v[104:107]\n"
        "ds_write_b128 v244, a[236:239] offset:9216\n"
        "v_mfma_f32_16x16x32_bf16 v[108:111], v[44:47], v[216:219], v[108:111]\n"
        "s_lshl_b32 s90, s90, 15\n"
        "v_cvt_pk_bf16_f32 v69, v74, v75\n"
        "v_cvt_pk_bf16_f32 v131, v134, v135\n"
        "v_mfma_f32_16x16x32_bf16 v[108:111], v[40:43], v[200:203], v[108:111]\n"
        "s_waitcnt lgkmcnt(2)\n"
        "v_cvt_pk_bf16_f32 v130, v132, v133\n"
        "v_cvt_pk_bf16_f32 v100, v104, v105\n"
        "v_mfma_f32_16x16x32_bf16 v[140:143], v[40:43], v[204:207], v[140:143]\n"
        "ds_write_b128 v244, a[224:227] offset:12288\n"
        "v_mfma_f32_16x16x32_bf16 v[140:143], v[44:47], v[220:223], v[140:143]\n"
        "s_barrier\n"
        "v_mfma_f32_16x16x32_bf16 v[136:139], v[36:39], v[220:223], v[136:139]\n"
        "s_add_u32 s68, s64, s90\n"
        "s_addc_u32 s69, s65, 0\n"
        "v_cndmask_b32 v36, 0, v242, s[10:11]\n"
        "v_mfma_f32_16x16x32_bf16 v[136:139], v[32:35], v[204:207], v[136:139]\n"
        "ds_write_b128 v244, a[228:231] offset:13312\n"
        "v_mfma_f32_16x16x32_bf16 v[16:19], v[48:51], v[192:195], v[16:19]\n"
        "global_store_dwordx4 v254, a[220:223], s[70:71] offset:2048 sc1\n"
        "s_xor_b32 s78, s78, 0x8000\n"
        "v_cndmask_b32 v37, 0, v242, s[12:13]\n"
        "v_cndmask_b32 v38, 0, v242, s[14:15]\n"
        "v_cndmask_b32 v39, 0, v242, s[16:17]\n"
        "v_cvt_pk_bf16_f32 v134, v140, v141\n"
        "v_mfma_f32_16x16x32_bf16 v[16:19], v[52:55], v[208:211], v[16:19]\n"
        "s_lshr_b32 s52, s41, 1\n"
        "v_cvt_pk_bf16_f32 v101, v106, v107\n"
        "v_cvt_pk_bf16_f32 v102, v108, v109\n"
        "v_mfma_f32_16x16x32_bf16 v[20:23], v[60:63], v[208:211], v[20:23]\n"
        "ds_write_b128 v245, a[176:179] offset:0\n"
        "v_mfma_f32_16x16x32_bf16 v[20:23], v[56:59], v[192:195], v[20:23]\n"
        "v_cvt_pk_bf16_f32 v103, v110, v111\n"
        "s_nop 0\n"
        "v_cvt_pk_bf16_f32 v8, v16, v17\n"
        "v_mfma_f32_16x16x32_bf16 v[84:87], v[56:59], v[196:199], v[84:87]\n"
        "s_barrier\n"
        "v_mfma_f32_16x16x32_bf16 v[84:87], v[60:63], v[212:215], v[84:87]\n"
        "ds_read_b128 a[96:99], v240 offset:0\n"
        "s_lshl_b32 s52, s52, 11\n"
        "v_cvt_pk_bf16_f32 v9, v18, v19\n"
        "v_mfma_f32_16x16x32_bf16 v[80:83], v[52:55], v[212:215], v[80:83]\n"
        "ds_read_b128 a[100:103], v240 offset:1024\n"
        "v_cvt_pk_bf16_f32 v10, v20, v21\n"
        "v_mfma_f32_16x16x32_bf16 v[80:83], v[48:51], v[196:199], v[80:83]\n"
        "ds_read_b128 a[104:107], v240 offset:2048\n"
        "v_cvt_pk_bf16_f32 v11, v22, v23\n"
        "v_mfma_f32_16x16x32_bf16 v[112:115], v[48:51], v[200:203], v[112:115]\n"
        "ds_read_b128 a[108:111], v240 offset:3072\n"
        "v_cvt_pk_bf16_f32 v74, v84, v85\n"
        "v_mfma_f32_16x16x32_bf16 v[112:115], v[52:55], v[216:219], v[112:115]\n"
        "ds_write_b128 v245, a[180:183] offset:1024\n"
        "v_mfma_f32_16x16x32_bf16 v[116:119], v[60:63], v[216:219], v[116:119]\n"
        "ds_read_b128 a[112:115], v240 offset:4096\n"
        "v_cvt_pk_bf16_f32 v72, v80, v81\n"
        "v_mfma_f32_16x16x32_bf16 v[116:119], v[56:59], v[200:203], v[116:119]\n"
        "ds_read_b128 a[116:119], v240 offset:5120\n"
        "v_cvt_pk_bf16_f32 v73, v82, v83\n"
        "v_mfma_f32_16x16x32_bf16 v[148:151], v[56:59], v[204:207], v[148:151]\n"
        "ds_read_b128 a[120:123], v240 offset:6144\n"
        "v_cvt_pk_bf16_f32 v75, v86, v87\n"
        "v_mfma_f32_16x16x32_bf16 v[148:151], v[60:63], v[220:223], v[148:151]\n"
        "ds_write_b128 v245, a[184:187] offset:2048\n"
        "v_mfma_f32_16x16x32_bf16 v[144:147], v[52:55], v[220:223], v[144:147]\n"
        "ds_read_b128 a[124:127], v240 offset:7168\n"
        "v_cndmask_b32 v52, 0, v242, s[20:21]\n"
        "v_mfma_f32_16x16x32_bf16 v[144:147], v[48:51], v[204:207], v[144:147]\n"
        "global_store_dwordx4 v254, a[252:255], s[70:71] offset:3072 sc1\n"
        "s_add_u32 s70, s38, s97\n"
        "s_addc_u32 s71, s39, 0\n"
        "s_add_u32 s97, s53, 0x2000\n"
        "v_cndmask_b32 v53, 0, v242, s[22:23]\n"
        "v_cndmask_b32 v54, 0, v242, s[24:25]\n"
        "v_mfma_f32_16x16x32_bf16 v[24:27], v[160:163], v[192:195], v[24:27]\n"
        "ds_write_b128 v245, a[188:191] offset:3072\n"
        "v_mfma_f32_16x16x32_bf16 v[24:27], v[164:167], v[208:211], v[24:27]\n"
        "s_add_u32 s97, s97, s75\n"
        "v_cndmask_b32 v55, 0, v242, s[28:29]\n"
        "v_cvt_pk_bf16_f32 v104, v112, v113\n"
        "v_mfma_f32_16x16x32_bf16 v[28:31], v[172:175], v[208:211], v[28:31]\n"
        "global_store_dwordx4 v254, a[180:183], s[70:71] offset:1024 sc1\n"
        "s_add_u32 s74, s68, 0x1000\n"
        "s_addc_u32 s75, s69, 0\n"
        "s_lshl2_add_u32 s53, s42, s40\n"
        "v_cvt_pk_bf16_f32 v105, v114, v115\n"
        "v_cvt_pk_bf16_f32 v12, v24, v25\n"
        "v_mfma_f32_16x16x32_bf16 v[28:31], v[168:171], v[192:195], v[28:31]\n"
        "ds_write_b128 v245, a[16:19] offset:8192\n"
        "v_mfma_f32_16x16x32_bf16 v[92:95], v[168:171], v[196:199], v[92:95]\n"
        "s_add_u32 s52, s52, s53\n"
        "v_cvt_pk_bf16_f32 v13, v26, v27\n"
        "v_cvt_pk_bf16_f32 v106, v116, v117\n"
        "v_mfma_f32_16x16x32_bf16 v[92:95], v[172:175], v[212:215], v[92:95]\n"
        "s_waitcnt lgkmcnt(2)\n"
        "v_cvt_pk_bf16_f32 v107, v118, v119\n"
        "v_cvt_pk_bf16_f32 v14, v28, v29\n"
        "v_mfma_f32_16x16x32_bf16 v[88:91], v[164:167], v[212:215], v[88:91]\n"
        "ds_write_b128 v245, a[20:23] offset:9216\n"
        "v_mfma_f32_16x16x32_bf16 v[88:91], v[160:163], v[196:199], v[88:91]\n"
        "s_add_u32 s52, s52, 0\n"
        "v_cvt_pk_bf16_f32 v15, v30, v31\n"
        "v_cvt_pk_bf16_f32 v78, v92, v93\n"
        "v_mfma_f32_16x16x32_bf16 v[120:123], v[160:163], v[200:203], v[120:123]\n"
        "s_lshl_b32 s52, s52, 10\n"
        "v_cvt_pk_bf16_f32 v79, v94, v95\n"
        "v_cvt_pk_bf16_f32 v132, v136, v137\n"
        "v_mfma_f32_16x16x32_bf16 v[32:35], a[96:99], v[0:3], 0\n"
        "ds_write_b128 v245, a[24:27] offset:10240\n"
        "v_mfma_f32_16x16x32_bf16 v[32:35], a[100:103], v[4:7], v[32:35]\n"
        "s_add_u32 s26, s18, s52\n"
        "s_addc_u32 s27, s19, 0\n"
        "v_cvt_pk_bf16_f32 v76, v88, v89\n"
        "v_mfma_f32_16x16x32_bf16 v[32:35], a[104:107], v[8:11], v[32:35]\n"
        "s_add_u32 s76, s66, s90\n"
        "s_addc_u32 s77, s67, 0\n"
        "v_cvt_pk_bf16_f32 v77, v90, v91\n"
        "v_mfma_f32_16x16x32_bf16 v[32:35], a[108:111], v[12:15], v[32:35]\n"
        "ds_write_b128 v245, a[28:31] offset:11264\n"
        "v_mfma_f32_16x16x32_bf16 v[120:123], v[164:167], v[216:219], v[120:123]\n"
        "v_xor_b32 v245, 0xc000, v245\n"
        "v_cvt_pk_bf16_f32 v133, v138, v139\n"
        "v_mfma_f32_16x16x32_bf16 v[124:127], v[172:175], v[216:219], v[124:127]\n"
        "v_cvt_pk_bf16_f32 v135, v142, v143\n"
        "v_cvt_pk_bf16_f32 v136, v144, v145\n"
        "v_mfma_f32_16x16x32_bf16 v[124:127], v[168:171], v[200:203], v[124:127]\n"
        "v_max_f32 v246, v32, v33\n"
        "v_max3_f32 v246, v34, v35, v246\n"
        "v_mfma_f32_16x16x32_bf16 v[156:159], v[168:171], v[204:207], v[156:159]\n"
        "v_mov_b32 v247, v246\n"
        "v_cvt_pk_bf16_f32 v108, v120, v121\n"
        "v_mfma_f32_16x16x32_bf16 v[36:39], a[108:111], v[76:79], v[36:39]\n"
        "v_permlane16_swap_b32 v246, v247\n"
        "v_mfma_f32_16x16x32_bf16 v[36:39], a[104:107], v[72:75], v[36:39]\n"
        "ds_read_b128 a[104:107], v240 offset:10240\n"
        "v_max_f32 v246, v246, v247\n"
        "v_mfma_f32_16x16x32_bf16 v[36:39], a[100:103], v[68:71], v[36:39]\n"
        "ds_read_b128 a[100:103], v240 offset:9216\n"
        "v_mov_b32 v247, v246\n"
        "v_mfma_f32_16x16x32_bf16 v[36:39], a[96:99], v[64:67], v[36:39]\n"
        "global_store_dwordx4 v254, a[176:179], s[70:71] offset:0 sc1\n"
        "ds_read_b128 a[96:99], v240 offset:8192\n"
        "s_waitcnt lgkmcnt(3)\n"
        "v_permlane32_swap_b32 v246, v247\n"
        "v_mfma_f32_16x16x32_bf16 v[156:159], v[172:175], v[220:223], v[156:159]\n"
        "ds_read_b128 a[108:111], v240 offset:11264\n"
        "v_max_f32 v246, v246, v247\n"
        "v_mfma_f32_16x16x32_bf16 v[152:155], v[164:167], v[220:223], v[152:155]\n"
        "v_sub_f32 v224, 0, v246\n"
        "v_sub_f32 v225, 0, v246\n"
        "v_mfma_f32_16x16x32_bf16 v[152:155], v[160:163], v[204:207], v[152:155]\n"
        "v_sub_f32 v226, 0, v246\n"
        "v_sub_f32 v227, 0, v246\n"
        "v_mfma_f32_16x16x32_bf16 v[48:51], a[112:115], v[0:3], 0\n"
        "v_max_f32 v248, v36, v37\n"
        "v_max3_f32 v248, v38, v39, v248\n"
        "v_mfma_f32_16x16x32_bf16 v[48:51], a[116:119], v[4:7], v[48:51]\n"
        "v_mov_b32 v249, v248\n"
        "v_add_f32 v32, v32, v224\n"
        "v_mfma_f32_16x16x32_bf16 v[48:51], a[120:123], v[8:11], v[48:51]\n"
        "v_permlane16_swap_b32 v248, v249\n"
        "v_mfma_f32_16x16x32_bf16 v[48:51], a[124:127], v[12:15], v[48:51]\n"
        "v_max_f32 v248, v248, v249\n"
        "v_mov_b32 v249, v248\n"
        "v_mfma_f32_16x16x32_bf16 v[52:55], a[124:127], v[76:79], v[52:55]\n"
        "global_store_dwordx4 v254, a[184:187], s[70:71] offset:2048 sc1\n"
        "s_waitcnt lgkmcnt(1)\n"
        "v_permlane32_swap_b32 v248, v249\n"
        "v_max_f32 v248, v248, v249\n"
        "v_mfma_f32_16x16x32_bf16 v[52:55], a[120:123], v[72:75], v[52:55]\n"
        "ds_read_b128 a[120:123], v240 offset:14336\n"
        "v_sub_f32 v228, 0, v248\n"
        "v_mfma_f32_16x16x32_bf16 v[52:55], a[116:119], v[68:71], v[52:55]\n"
        "ds_read_b128 a[116:119], v240 offset:13312\n"
        "v_sub_f32 v229, 0, v248\n"
        "v_mfma_f32_16x16x32_bf16 v[52:55], a[112:115], v[64:67], v[52:55]\n"
        "v_sub_f32 v230, 0, v248\n"
        "v_sub_f32 v231, 0, v248\n"
        "s_barrier\n"
        "ds_read_b128 a[144:147], v241 offset:0\n"
        "v_exp_f32 v32, v32\n"
        "v_add_f32 v33, v33, v224\n"
        "v_exp_f32 v33, v33\n"
        "ds_read_b128 a[148:151], v241 offset:1024\n"
        "v_cvt_pk_bf16_f32 v16, v32, v33\n"
        "v_add_f32 v34, v34, v224\n"
        "v_exp_f32 v34, v34\n"
        "v_add_f32 v35, v35, v224\n"
        "v_exp_f32 v35, v35\n"
        "ds_read_b128 a[152:155], v241 offset:2048\n"
        "v_cvt_pk_bf16_f32 v17, v34, v35\n"
        "v_mfma_f32_16x16x32_bf16 v[32:35], a[96:99], v[0:3], v[224:227]\n"
        "global_store_dwordx4 v254, a[188:191], s[70:71] offset:3072 sc1\n"
        "s_waitcnt lgkmcnt(4)\n"
        "v_add_f32 v36, v36, v228\n"
        "v_exp_f32 v36, v36\n"
        "v_mfma_f32_16x16x32_bf16 v[32:35], a[100:103], v[4:7], v[32:35]\n"
        "ds_read_b128 a[112:115], v240 offset:12288\n"
        "v_add_f32 v37, v37, v228\n"
        "v_mfma_f32_16x16x32_bf16 v[32:35], a[104:107], v[8:11], v[32:35]\n"
        "v_exp_f32 v37, v37\n"
        "v_mfma_f32_16x16x32_bf16 v[32:35], a[108:111], v[12:15], v[32:35]\n"
        "v_cvt_pk_bf16_f32 v20, v36, v37\n"
        "v_add_f32 v38, v38, v228\n"
        "v_exp_f32 v38, v38\n"
        "v_add_f32 v39, v39, v228\n"
        "v_exp_f32 v39, v39\n"
        "ds_read_b128 a[156:159], v241 offset:3072\n"
        "v_cvt_pk_bf16_f32 v21, v38, v39\n"
        "ds_read_b128 a[128:131], v241 offset:4096\n"
        "v_add_f32 v48, v48, v224\n"
        "v_exp_f32 v48, v48\n"
        "v_add_f32 v49, v49, v224\n"
        "v_exp_f32 v49, v49\n"
        "ds_read_b128 a[124:127], v240 offset:15360\n"
        "v_cvt_pk_bf16_f32 v18, v48, v49\n"
        "ds_read_b128 a[132:135], v241 offset:5120\n"
        "s_add_u32 s70, s38, s97\n"
        "s_addc_u32 s71, s39, 0\n"
        "global_store_dwordx4 v254, a[16:19], s[70:71] offset:0 sc1\n"
        "v_add_f32 v50, v50, v224\n"
        "v_exp_f32 v50, v50\n"
        "v_add_f32 v51, v51, v224\n"
        "v_exp_f32 v51, v51\n"
        "ds_read_b128 a[136:139], v241 offset:6144\n"
        "v_cvt_pk_bf16_f32 v19, v50, v51\n"
        "s_waitcnt lgkmcnt(5)\n"
        "v_mfma_f32_16x16x32_bf16 v[48:51], a[112:115], v[0:3], v[224:227]\n"
        "ds_read_b128 a[140:143], v241 offset:7168\n"
        "s_mul_i32 s96, s40, 4224\n"
        "v_cndmask_b32 v36, v228, v242, s[30:31]\n"
        "v_mfma_f32_16x16x32_bf16 v[48:51], a[116:119], v[4:7], v[48:51]\n"
        "v_cndmask_b32 v37, v228, v242, s[56:57]\n"
        "v_cndmask_b32 v38, v228, v242, s[58:59]\n"
        "v_mfma_f32_16x16x32_bf16 v[48:51], a[120:123], v[8:11], v[48:51]\n"
        "v_cvt_pk_bf16_f32 v141, v154, v155\n"
        "v_cndmask_b32 v39, v228, v242, s[60:61]\n"
        "s_nop 1\n"
        "v_mfma_f32_16x16x32_bf16 v[36:39], a[108:111], v[76:79], v[36:39]\n"
        "ds_read_b128 a[108:111], v240 offset:19456\n"
        "v_bfe_u32 v154, v254, 4, 1\n"
        "v_mfma_f32_16x16x32_bf16 v[36:39], a[104:107], v[72:75], v[36:39]\n"
        "ds_read_b128 a[104:107], v240 offset:18432\n"
        "v_mul_u32_u24 v154, 0xaaaa, v154\n"
        "v_mfma_f32_16x16x32_bf16 v[36:39], a[100:103], v[68:71], v[36:39]\n"
        "s_waitcnt lgkmcnt(4)\n"
        "v_add_u32 v154, 0x4444, v154\n"
        "v_add_f32 v52, v52, v228\n"
        "v_mfma_f32_16x16x32_bf16 v[36:39], a[96:99], v[64:67], v[36:39]\n"
        "v_exp_f32 v52, v52\n"
        "v_mfma_f32_16x16x32_bf16 v[48:51], a[124:127], v[12:15], v[48:51]\n"
        "ds_read_b128 a[96:99], v240 offset:16384\n"
        "v_add_f32 v53, v53, v228\n"
        "v_mfma_f32_16x16x32_bf16 v[160:163], a[144:147], v[16:19], 0\n"
        "v_exp_f32 v53, v53\n"
        "v_mfma_f32_16x16x32_bf16 v[164:167], a[148:151], v[16:19], 0\n"
        "ds_read_b128 a[100:103], v240 offset:17408\n"
        "v_cvt_pk_bf16_f32 v22, v52, v53\n"
        "v_mfma_f32_16x16x32_bf16 v[168:171], a[152:155], v[16:19], 0\n"
        "v_add_f32 v54, v54, v228\n"
        "v_add_f32 v55, v55, v228\n"
        "v_mfma_f32_16x16x32_bf16 v[172:175], a[156:159], v[16:19], 0\n"
        "v_exp_f32 v54, v54\n"
        "v_mfma_f32_16x16x32_bf16 v[176:179], a[128:131], v[16:19], 0\n"
        "v_exp_f32 v55, v55\n"
        "v_mfma_f32_16x16x32_bf16 v[180:183], a[132:135], v[16:19], 0\n"
        "s_waitcnt lgkmcnt(2)\n"
        "v_cvt_pk_bf16_f32 v23, v54, v55\n"
        "s_nop 1\n"
        "v_smfmac_f32_16x16x64_bf16 a[64:67], a[72:75], v[16:23], v154\n"
        "global_store_dwordx4 v254, a[20:23], s[70:71] offset:1024 sc1\n"
        "v_exp_f32 v32, v32\n"
        "v_exp_f32 v33, v33\n"
        "v_mfma_f32_16x16x32_bf16 v[192:195], a[144:147], v[20:23], 0\n"
        "ds_read_b128 a[144:147], v241 offset:8192\n"
        "v_cvt_pk_bf16_f32 v80, v32, v33\n"
        "v_mfma_f32_16x16x32_bf16 v[196:199], a[148:151], v[20:23], 0\n"
        "ds_read_b128 a[148:151], v241 offset:9216\n"
        "v_cndmask_b32 v52, v228, v242, s[62:63]\n"
        "v_mfma_f32_16x16x32_bf16 v[200:203], a[152:155], v[20:23], 0\n"
        "ds_read_b128 a[152:155], v241 offset:10240\n"
        "v_cndmask_b32 v53, v228, v242, s[84:85]\n"
        "v_mfma_f32_16x16x32_bf16 v[204:207], a[156:159], v[20:23], 0\n"
        "ds_read_b128 a[156:159], v241 offset:11264\n"
        "v_cndmask_b32 v54, v228, v242, s[86:87]\n"
        "v_mfma_f32_16x16x32_bf16 v[208:211], a[128:131], v[20:23], 0\n"
        "ds_read_b128 a[128:131], v241 offset:12288\n"
        "v_cndmask_b32 v55, v228, v242, s[88:89]\n"
        "s_nop 1\n"
        "v_mfma_f32_16x16x32_bf16 v[52:55], a[124:127], v[76:79], v[52:55]\n"
        "v_exp_f32 v34, v34\n"
        "v_mfma_f32_16x16x32_bf16 v[52:55], a[120:123], v[72:75], v[52:55]\n"
        "v_exp_f32 v35, v35\n"
        "v_mfma_f32_16x16x32_bf16 v[52:55], a[116:119], v[68:71], v[52:55]\n"
        "ds_read_b128 a[116:119], v240 offset:21504\n"
        "v_cvt_pk_bf16_f32 v81, v34, v35\n"
        "v_mfma_f32_16x16x32_bf16 v[52:55], a[112:115], v[64:67], v[52:55]\n"
        "ds_read_b128 a[112:115], v240 offset:20480\n"
        "v_cndmask_b32 v32, v224, v242, s[10:11]\n"
        "v_mfma_f32_16x16x32_bf16 v[212:215], a[132:135], v[20:23], 0\n"
        "s_waitcnt lgkmcnt(5)\n"
        "v_exp_f32 v36, v36\n"
        "v_mfma_f32_16x16x32_bf16 v[184:187], a[136:139], v[16:19], 0\n"
        "v_exp_f32 v37, v37\n"
        "v_mfma_f32_16x16x32_bf16 v[216:219], a[136:139], v[20:23], 0\n"
        "ds_read_b128 a[120:123], v240 offset:22528\n"
        "v_cvt_pk_bf16_f32 v84, v36, v37\n"
        "v_mfma_f32_16x16x32_bf16 v[220:223], a[140:143], v[20:23], 0\n"
        "global_store_dwordx4 v254, a[24:27], s[70:71] offset:2048 sc1\n"
        "ds_read_b128 a[124:127], v240 offset:23552\n"
        "s_add_u32 s82, s82, 1\n"
        "v_exp_f32 v38, v38\n"
        "v_cndmask_b32 v33, v224, v242, s[12:13]\n"
        "v_mfma_f32_16x16x32_bf16 v[188:191], a[140:143], v[16:19], 0\n"
        "v_exp_f32 v39, v39\n"
        "ds_write_b128 v245, a[216:219] offset:0\n"
        "v_cvt_pk_bf16_f32 v85, v38, v39\n"
        "v_cndmask_b32 v34, v224, v242, s[14:15]\n"
        "v_cndmask_b32 v35, v224, v242, s[16:17]\n"
        "s_nop 1\n"
        "v_mfma_f32_16x16x32_bf16 v[32:35], a[96:99], v[0:3], v[32:35]\n"
        "v_exp_f32 v48, v48\n"
        "v_mfma_f32_16x16x32_bf16 v[32:35], a[100:103], v[4:7], v[32:35]\n"
        "v_exp_f32 v49, v49\n"
        "v_mfma_f32_16x16x32_bf16 v[32:35], a[104:107], v[8:11], v[32:35]\n"
        "ds_read_b128 a[96:99], v240 offset:24576\n"
        "v_cvt_pk_bf16_f32 v82, v48, v49\n"
        "v_mfma_f32_16x16x32_bf16 v[32:35], a[108:111], v[12:15], v[32:35]\n"
        "v_exp_f32 v50, v50\n"
        "v_exp_f32 v51, v51\n"
        "global_store_dwordx4 v254, a[28:31], s[70:71] offset:3072 sc1\n"
        "v_cvt_pk_bf16_f32 v83, v50, v51\n"
        "s_nop 1\n"
        "v_mfma_f32_16x16x32_bf16 v[160:163], a[144:147], v[80:83], v[160:163]\n"
        "ds_write_b128 v245, a[248:251] offset:1024\n"
        "v_mfma_f32_16x16x32_bf16 v[164:167], a[148:151], v[80:83], v[164:167]\n"
        "s_waitcnt lgkmcnt(2)\n"
        "v_exp_f32 v52, v52\n"
        "v_mfma_f32_16x16x32_bf16 v[168:171], a[152:155], v[80:83], v[168:171]\n"
        "v_exp_f32 v53, v53\n"
        "v_mfma_f32_16x16x32_bf16 v[172:175], a[156:159], v[80:83], v[172:175]\n"
        "ds_read_b128 a[132:135], v241 offset:13312\n"
        "v_cvt_pk_bf16_f32 v86, v52, v53\n"
        "v_mfma_f32_16x16x32_bf16 v[176:179], a[128:131], v[80:83], v[176:179]\n"
        "ds_read_b128 a[100:103], v240 offset:25600\n"
        "ds_read_b128 a[136:139], v241 offset:14336\n"
        "ds_read_b128 a[104:107], v240 offset:26624\n"
        "ds_read_b128 a[140:143], v241 offset:15360\n"
        "ds_write_b128 v245, a[220:223] offset:2048\n"
        "ds_read_b128 a[108:111], v240 offset:27648\n"
        "s_add_u32 s70, s92, 0x2\n"
        "s_addc_u32 s71, s93, 0\n"
        "s_cmp_ge_u32 s82, s44\n"
        "s_addc_u32 s90, s82, 0\n"
        "s_lshl_b32 s90, s90, 15\n"
        "v_exp_f32 v54, v54\n"
        "v_exp_f32 v55, v55\n"
        "s_waitcnt lgkmcnt(6)\n"
        "v_cvt_pk_bf16_f32 v87, v54, v55\n"
        "s_nop 1\n"
        "v_smfmac_f32_16x16x64_bf16 a[64:67], a[72:75], v[80:87], v154\n"
        "v_cndmask_b32 v48, v224, v242, s[20:21]\n"
        "v_mfma_f32_16x16x32_bf16 v[192:195], a[144:147], v[84:87], v[192:195]\n"
        "ds_read_b128 a[144:147], v241 offset:16384\n"
        "v_cndmask_b32 v49, v224, v242, s[22:23]\n"
        "v_mfma_f32_16x16x32_bf16 v[196:199], a[148:151], v[84:87], v[196:199]\n"
        "ds_read_b128 a[148:151], v241 offset:17408\n"
        "v_cndmask_b32 v50, v224, v242, s[24:25]\n"
        "v_mfma_f32_16x16x32_bf16 v[200:203], a[152:155], v[84:87], v[200:203]\n"
        "ds_read_b128 a[152:155], v241 offset:18432\n"
        "v_cndmask_b32 v51, v224, v242, s[28:29]\n"
        "s_nop 1\n"
        "v_mfma_f32_16x16x32_bf16 v[48:51], a[112:115], v[0:3], v[48:51]\n"
        "v_exp_f32 v32, v32\n"
        "v_mfma_f32_16x16x32_bf16 v[48:51], a[116:119], v[4:7], v[48:51]\n"
        "v_exp_f32 v33, v33\n"
        "v_mfma_f32_16x16x32_bf16 v[48:51], a[120:123], v[8:11], v[48:51]\n"
        "ds_read_b128 a[112:115], v240 offset:28672\n"
        "v_cvt_pk_bf16_f32 v16, v32, v33\n"
        "v_mfma_f32_16x16x32_bf16 v[48:51], a[124:127], v[12:15], v[48:51]\n"
        "s_waitcnt lgkmcnt(4)\n"
        "v_cvt_pk_bf16_f32 v109, v122, v123\n"
        "v_cvt_pk_bf16_f32 v110, v124, v125\n"
        "v_mfma_f32_16x16x32_bf16 v[204:207], a[156:159], v[84:87], v[204:207]\n"
        "ds_read_b128 a[116:119], v240 offset:29696\n"
        "v_cvt_pk_bf16_f32 v111, v126, v127\n"
        "v_mfma_f32_16x16x32_bf16 v[208:211], a[128:131], v[84:87], v[208:211]\n"
        "v_exp_f32 v34, v34\n"
        "v_mfma_f32_16x16x32_bf16 v[212:215], a[132:135], v[84:87], v[212:215]\n"
        "v_exp_f32 v35, v35\n"
        "v_mfma_f32_16x16x32_bf16 v[180:183], a[132:135], v[80:83], v[180:183]\n"
        "ds_read_b128 a[120:123], v240 offset:30720\n"
        "v_cvt_pk_bf16_f32 v17, v34, v35\n"
        "v_mfma_f32_16x16x32_bf16 v[184:187], a[136:139], v[80:83], v[184:187]\n"
        "ds_write_b128 v245, a[252:255] offset:3072\n"
        "v_mfma_f32_16x16x32_bf16 v[216:219], a[136:139], v[84:87], v[216:219]\n"
        "ds_read_b128 a[124:127], v240 offset:31744\n"
        "v_cndmask_b32 v32, v224, v242, s[30:31]\n"
        "v_mfma_f32_16x16x32_bf16 v[220:223], a[140:143], v[84:87], v[220:223]\n"
        "ds_read_b128 a[156:159], v241 offset:19456\n"
        "v_cndmask_b32 v33, v224, v242, s[56:57]\n"
        "v_mfma_f32_16x16x32_bf16 v[188:191], a[140:143], v[80:83], v[188:191]\n"
        "v_cndmask_b32 v34, v224, v242, s[58:59]\n"
        "v_cndmask_b32 v35, v224, v242, s[60:61]\n"
        "s_nop 1\n"
        "v_mfma_f32_16x16x32_bf16 v[32:35], a[96:99], v[0:3], v[32:35]\n"
        "v_exp_f32 v48, v48\n"
        "v_mfma_f32_16x16x32_bf16 v[32:35], a[100:103], v[4:7], v[32:35]\n"
        "v_exp_f32 v49, v49\n"
        "v_mfma_f32_16x16x32_bf16 v[32:35], a[104:107], v[8:11], v[32:35]\n"
        "s_waitcnt lgkmcnt(3)\n"
        "v_cvt_pk_bf16_f32 v18, v48, v49\n"
        "v_xor_b32 v240, 0x8000, v240\n"
        "v_mfma_f32_16x16x32_bf16 v[32:35], a[108:111], v[12:15], v[32:35]\n"
        "ds_write_b128 v245, a[208:211] offset:8192\n"
        "s_waitcnt lgkmcnt(2)\n"
        "s_barrier\n"
        "ds_read_b128 a[96:99], v240 offset:0\n"
        "ds_read_b128 a[128:131], v241 offset:20480\n"
        "v_exp_f32 v50, v50\n"
        "v_exp_f32 v51, v51\n"
        "v_cndmask_b32 v48, v224, v242, s[62:63]\n"
        "v_cvt_pk_bf16_f32 v19, v50, v51\n"
        "s_nop 1\n"
        "v_mfma_f32_16x16x32_bf16 a[64:67], a[76:79], v[16:19], a[64:67]\n"
        "ds_read_b128 a[132:135], v241 offset:21504\n"
        "v_cndmask_b32 v49, v224, v242, s[84:85]\n"
        "v_mfma_f32_16x16x32_bf16 v[160:163], a[144:147], v[16:19], v[160:163]\n"
        "ds_read_b128 a[100:103], v240 offset:1024\n"
        "v_cndmask_b32 v50, v224, v242, s[86:87]\n"
        "v_mfma_f32_16x16x32_bf16 v[164:167], a[148:151], v[16:19], v[164:167]\n"
        "ds_read_b128 a[136:139], v241 offset:22528\n"
        "v_cndmask_b32 v51, v224, v242, s[88:89]\n"
        "s_nop 1\n"
        "v_mfma_f32_16x16x32_bf16 v[48:51], a[112:115], v[0:3], v[48:51]\n"
        "ds_read_b128 a[112:115], v240 offset:4096\n"
        "v_cvt_pk_bf16_f32 v137, v146, v147\n"
        "v_mfma_f32_16x16x32_bf16 v[48:51], a[116:119], v[4:7], v[48:51]\n"
        "ds_read_b128 a[116:119], v240 offset:5120\n"
        "v_cvt_pk_bf16_f32 v138, v148, v149\n"
        "v_mfma_f32_16x16x32_bf16 v[48:51], a[120:123], v[8:11], v[48:51]\n"
        "ds_read_b128 a[120:123], v240 offset:6144\n"
        "v_cvt_pk_bf16_f32 v139, v150, v151\n"
        "v_mfma_f32_16x16x32_bf16 v[48:51], a[124:127], v[12:15], v[48:51]\n"
        "ds_read_b128 a[124:127], v240 offset:7168\n"
        "v_lshrrev_b32 v244, 1, v254\n"
        "v_mfma_f32_16x16x32_bf16 v[168:171], a[152:155], v[16:19], v[168:171]\n"
        "ds_read_b128 a[140:143], v241 offset:23552\n"
        "s_waitcnt lgkmcnt(8)\n"
        "v_mfma_f32_16x16x32_bf16 v[172:175], a[156:159], v[16:19], v[172:175]\n"
        "ds_read_b128 a[104:107], v240 offset:2048\n"
        "v_add_u32 v244, s81, v244\n"
        "v_mfma_f32_16x16x32_bf16 v[176:179], a[128:131], v[16:19], v[176:179]\n"
        "s_waitcnt lgkmcnt(7)\n"
        "v_exp_f32 v32, v32\n"
        "v_mfma_f32_16x16x32_bf16 v[180:183], a[132:135], v[16:19], v[180:183]\n"
        "s_waitcnt lgkmcnt(6)\n"
        "v_exp_f32 v33, v33\n"
        "v_mfma_f32_16x16x32_bf16 v[184:187], a[136:139], v[16:19], v[184:187]\n"
        "ds_write_b128 v245, a[240:243] offset:9216\n"
        "ds_read_b128 a[108:111], v240 offset:3072\n"
        "ds_read_b128 a[144:147], v241 offset:24576\n"
        "s_add_u32 s81, s78, 4096\n"
        "v_cvt_pk_bf16_f32 v80, v32, v33\n"
        "s_waitcnt lgkmcnt(5)\n"
        "v_mfma_f32_16x16x32_bf16 v[52:55], a[124:127], v[76:79], v[228:231]\n"
        "v_exp_f32 v34, v34\n"
        "v_mfma_f32_16x16x32_bf16 v[52:55], a[120:123], v[72:75], v[52:55]\n"
        "ds_write_b128 v245, a[212:215] offset:10240\n"
        "v_mfma_f32_16x16x32_bf16 v[52:55], a[116:119], v[68:71], v[52:55]\n"
        "v_exp_f32 v35, v35\n"
        "v_mfma_f32_16x16x32_bf16 v[52:55], a[112:115], v[64:67], v[52:55]\n"
        "s_waitcnt vmcnt(0)\n"
        "v_cvt_pk_bf16_f32 v81, v34, v35\n"
        "s_waitcnt lgkmcnt(4)\n"
        "v_mfma_f32_16x16x32_bf16 v[188:191], a[140:143], v[16:19], v[188:191]\n"
        "s_barrier\n"
        "v_mfma_f32_16x16x32_bf16 v[32:35], a[96:99], v[0:3], v[224:227]\n"
        "s_cmp_lg_u32 s40, 0\n"
        "s_cbranch_scc1 LW1_%=\n"
        "s_mov_b64 exec, 0xffff\n"
        "global_store_dwordx2 v244, v[250:251], s[48:49] sc1\n"
        "s_mov_b64 exec, -1\n"
        "LW1_%=:\n"
        "v_mfma_f32_16x16x32_bf16 v[32:35], a[100:103], v[4:7], v[32:35]\n"
        "ds_write_b128 v245, a[244:247] offset:11264\n"
        "v_mfma_f32_16x16x32_bf16 v[32:35], a[104:107], v[8:11], v[32:35]\n"
        "s_waitcnt lgkmcnt(1)\n"
        "v_bfe_u32 v245, v254, 4, 3\n"
        "v_lshl_add_u32 v245, v245, 7, s80\n"
        "v_mfma_f32_16x16x32_bf16 v[32:35], a[108:111], v[12:15], v[32:35]\n"
        "ds_read_b128 a[148:151], v241 offset:25600\n"
        "s_mov_b32 s80, s78\n"
        "v_cvt_pk_bf16_f32 v140, v152, v153\n"
        "v_mfma_f32_16x16x32_bf16 v[36:39], a[108:111], v[76:79], v[228:231]\n"
        "ds_read_b128 a[152:155], v241 offset:26624\n"
        "s_add_u32 m0, s80, 0x0\n"
        "v_cvt_pk_bf16_f32 v142, v156, v157\n"
        "v_mfma_f32_16x16x32_bf16 v[36:39], a[104:107], v[72:75], v[36:39]\n"
        "ds_read_b128 a[156:159], v241 offset:27648\n"
        "s_xor_b32 s78, s78, 0x8000\n"
        "v_cvt_pk_bf16_f32 v143, v158, v159\n"
        "v_mfma_f32_16x16x32_bf16 v[36:39], a[100:103], v[68:71], v[36:39]\n"
        "ds_read_b128 a[128:131], v241 offset:28672\n"
        "s_mov_b32 s80, s78\n"
        "v_cndmask_b32 v40, 0, v242, s[10:11]\n"
        "v_mfma_f32_16x16x32_bf16 v[36:39], a[96:99], v[64:67], v[36:39]\n"
        "ds_read_b128 a[132:135], v241 offset:29696\n"
        "ds_read_b128 a[136:139], v241 offset:30720\n"
        "ds_read_b128 a[140:143], v241 offset:31744\n"
        "v_exp_f32 v48, v48\n"
        "v_exp_f32 v49, v49\n"
        "v_exp_f32 v50, v50\n"
        "v_cvt_pk_bf16_f32 v82, v48, v49\n"
        "v_exp_f32 v51, v51\n"
        "v_xor_b32 v241, 0x8000, v241\n"
        "v_cvt_pk_bf16_f32 v83, v50, v51\n"
        "s_waitcnt lgkmcnt(6)\n"
        "s_nop 0\n"
        "v_mfma_f32_16x16x32_bf16 v[164:167], a[148:151], v[80:83], v[164:167]\n"
        "s_add_u32 s91, s44, 1\n"
        "v_cndmask_b32 v41, 0, v242, s[12:13]\n"
        "v_cndmask_b32 v42, 0, v242, s[14:15]\n"
        "v_mfma_f32_16x16x32_bf16 v[48:51], a[112:115], v[0:3], v[224:227]\n"
        "LHA10_%=:\n"
        "s_cbranch_execz LHRA_%=\n"
        "v_cndmask_b32 v43, 0, v242, s[16:17]\n"
        "s_nop 1\n"
        "v_mfma_f32_16x16x32_bf16 v[40:43], a[96:99], v[96:99], v[40:43]\n"
        "ds_read_b128 a[96:99], v240 offset:8192\n"
        "v_cndmask_b32 v56, 0, v242, s[20:21]\n"
        "v_mfma_f32_16x16x32_bf16 v[40:43], a[100:103], v[100:103], v[40:43]\n"
        "ds_read_b128 a[100:103], v240 offset:9216\n"
        "v_cndmask_b32 v57, 0, v242, s[22:23]\n"
        "v_mfma_f32_16x16x32_bf16 v[40:43], a[104:107], v[104:107], v[40:43]\n"
        "s_waitcnt lgkmcnt(2)\n"
        "v_exp_f32 v32, v32\n"
        "v_mfma_f32_16x16x32_bf16 v[40:43], a[108:111], v[108:111], v[40:43]\n"
        "s_barrier\n"
        "v_mfma_f32_16x16x32_bf16 v[48:51], a[116:119], v[4:7], v[48:51]\n"
        "ds_read_b128 a[148:151], v241 offset:1024\n"
        "v_cndmask_b32 v58, 0, v242, s[24:25]\n"
        "v_mfma_f32_16x16x32_bf16 v[48:51], a[120:123], v[8:11], v[48:51]\n"
        "v_exp_f32 v33, v33\n"
        "v_mfma_f32_16x16x32_bf16 v[48:51], a[124:127], v[12:15], v[48:51]\n"
        "v_exp_f32 v34, v34\n"
        "v_mfma_f32_16x16x32_bf16 a[64:67], a[76:79], v[80:83], a[64:67]\n"
        "ds_read_b128 a[104:107], v240 offset:10240\n"
        "v_max_f32 v246, v40, v41\n"
        "v_mfma_f32_16x16x32_bf16 v[160:163], a[144:147], v[80:83], v[160:163]\n"
        "ds_read_b128 a[108:111], v240 offset:11264\n"
        "v_max3_f32 v246, v42, v43, v246\n"
        "v_mfma_f32_16x16x32_bf16 v[168:171], a[152:155], v[80:83], v[168:171]\n"
        "ds_read_b128 a[144:147], v241 offset:0\n"
        "v_mov_b32 v247, v246\n"
        "v_mfma_f32_16x16x32_bf16 v[172:175], a[156:159], v[80:83], v[172:175]\n"
        "v_exp_f32 v35, v35\n"
        "v_mfma_f32_16x16x32_bf16 v[176:179], a[128:131], v[80:83], v[176:179]\n"
        "v_permlane16_swap_b32 v246, v247\n"
        "v_mfma_f32_16x16x32_bf16 v[180:183], a[132:135], v[80:83], v[180:183]\n"
        "s_waitcnt lgkmcnt(3)\n"
        "v_max_f32 v246, v246, v247\n"
        "v_mov_b32 v247, v246\n"
        "v_mfma_f32_16x16x32_bf16 v[184:187], a[136:139], v[80:83], v[184:187]\n"
        "v_exp_f32 v48, v48\n"
        "v_mfma_f32_16x16x32_bf16 v[188:191], a[140:143], v[80:83], v[188:191]\n"
        "v_permlane32_swap_b32 v246, v247\n"
        "v_max_f32 v246, v246, v247\n"
        "v_sub_f32 v232, 0, v246\n"
        "v_sub_f32 v233, 0, v246\n"
        "v_sub_f32 v234, 0, v246\n"
        "v_sub_f32 v235, 0, v246\n"
        "v_exp_f32 v49, v49\n"
        "v_exp_f32 v50, v50\n"
        "v_exp_f32 v51, v51\n"
        "v_cvt_pk_bf16_f32 v16, v32, v33\n"
        "v_cvt_pk_bf16_f32 v17, v34, v35\n"
        "v_mfma_f32_16x16x32_bf16 v[32:35], a[96:99], v[0:3], v[224:227]\n"
        "ds_read_b128 a[152:155], v241 offset:2048\n"
        "v_cvt_pk_bf16_f32 v18, v48, v49\n"
        "v_mfma_f32_16x16x32_bf16 v[32:35], a[100:103], v[4:7], v[32:35]\n"
        "s_waitcnt lgkmcnt(1)\n"
        "v_cvt_pk_bf16_f32 v19, v50, v51\n"
        "s_nop 1\n"
        "v_mfma_f32_16x16x32_bf16 v[164:167], a[148:151], v[16:19], v[164:167]\n"
        "global_load_dwordx2 v[252:253], v245, s[48:49] sc1\n"
        "v_cndmask_b32 v59, 0, v242, s[28:29]\n"
        "s_nop 1\n"
        "v_mfma_f32_16x16x32_bf16 v[56:59], a[112:115], v[96:99], v[56:59]\n"
        "v_exp_f32 v36, v36\n"
        "v_mfma_f32_16x16x32_bf16 v[56:59], a[116:119], v[100:103], v[56:59]\n"
        "v_exp_f32 v37, v37\n"
        "v_mfma_f32_16x16x32_bf16 v[56:59], a[120:123], v[104:107], v[56:59]\n"
        "ds_read_b128 a[112:115], v240 offset:12288\n"
        "v_cvt_pk_bf16_f32 v20, v36, v37\n"
        "v_mfma_f32_16x16x32_bf16 v[56:59], a[124:127], v[108:111], v[56:59]\n"
        "v_exp_f32 v38, v38\n"
        "v_mfma_f32_16x16x32_bf16 v[32:35], a[104:107], v[8:11], v[32:35]\n"
        "v_exp_f32 v39, v39\n"
        "v_mfma_f32_16x16x32_bf16 v[32:35], a[108:111], v[12:15], v[32:35]\n"
        "ds_read_b128 a[116:119], v240 offset:13312\n"
        "v_cvt_pk_bf16_f32 v21, v38, v39\n"
        "v_mfma_f32_16x16x32_bf16 v[36:39], a[108:111], v[76:79], v[228:231]\n"
        "ds_read_b128 a[120:123], v240 offset:14336\n"
        "v_add_f32 v40, v40, v232\n"
        "v_mfma_f32_16x16x32_bf16 v[36:39], a[104:107], v[72:75], v[36:39]\n"
        "v_exp_f32 v40, v40\n"
        "v_mfma_f32_16x16x32_bf16 v[36:39], a[100:103], v[68:71], v[36:39]\n"
        "ds_read_b128 a[156:159], v241 offset:3072\n"
        "v_add_f32 v41, v41, v232\n"
        "v_mfma_f32_16x16x32_bf16 v[36:39], a[96:99], v[64:67], v[36:39]\n"
        "v_exp_f32 v41, v41\n"
        "v_mfma_f32_16x16x32_bf16 v[160:163], a[144:147], v[16:19], v[160:163]\n"
        "s_waitcnt lgkmcnt(3)\n"
        "v_cvt_pk_bf16_f32 v24, v40, v41\n"
        "v_add_f32 v42, v42, v232\n"
        "v_mfma_f32_16x16x32_bf16 v[48:51], a[112:115], v[0:3], v[224:227]\n"
        "v_exp_f32 v42, v42\n"
        "v_mfma_f32_16x16x32_bf16 v[168:171], a[152:155], v[16:19], v[168:171]\n"
        "ds_read_b128 a[124:127], v240 offset:15360\n"
        "v_add_f32 v43, v43, v232\n"
        "s_waitcnt lgkmcnt(3)\n"
        "v_mfma_f32_16x16x32_bf16 v[48:51], a[116:119], v[4:7], v[48:51]\n"
        "s_waitcnt lgkmcnt(2)\n"
        "v_exp_f32 v43, v43\n"
        "v_mfma_f32_16x16x32_bf16 v[48:51], a[120:123], v[8:11], v[48:51]\n"
        "ds_read_b128 a[128:131], v241 offset:4096\n"
        "ds_read_b128 a[132:135], v241 offset:5120\n"
        "v_cvt_pk_bf16_f32 v25, v42, v43\n"
        "v_cndmask_b32 v40, v232, v242, s[30:31]\n"
        "s_waitcnt lgkmcnt(3)\n"
        "v_mfma_f32_16x16x32_bf16 v[172:175], a[156:159], v[16:19], v[172:175]\n"
        "s_mov_b64 exec, 0\n"
        "s_cmp_eq_u32 s40, 0\n"
        "s_cbranch_scc1 LHC0_%=\n"
        "s_cmp_eq_u32 s40, 1\n"
        "s_cbranch_scc1 LHC1_%=\n"
        "s_cmp_eq_u32 s40, 2\n"
        "s_cbranch_scc1 LHC2_%=\n"
        "s_branch LHC3_%=\n"
        "LHRC_%=:\n"
        "s_mov_b64 exec, -1\n"
        "ds_read_b128 a[136:139], v241 offset:6144\n"
        "ds_read_b128 a[140:143], v241 offset:7168\n"
        "v_exp_f32 v52, v52\n"
        "v_exp_f32 v53, v53\n"
        "s_waitcnt lgkmcnt(4)\n"
        "v_cvt_pk_bf16_f32 v22, v52, v53\n"
        "v_mfma_f32_16x16x32_bf16 v[48:51], a[124:127], v[12:15], v[48:51]\n"
        "v_cndmask_b32 v41, v232, v242, s[56:57]\n"
        "v_cndmask_b32 v42, v232, v242, s[58:59]\n"
        "v_cndmask_b32 v43, v232, v242, s[60:61]\n"
        "s_nop 1\n"
        "v_mfma_f32_16x16x32_bf16 v[40:43], a[96:99], v[96:99], v[40:43]\n"
        "ds_read_b128 a[96:99], v240 offset:16384\n"
        "v_add_f32 v56, v56, v232\n"
        "v_mfma_f32_16x16x32_bf16 v[40:43], a[100:103], v[100:103], v[40:43]\n"
        "v_exp_f32 v54, v54\n"
        "v_mfma_f32_16x16x32_bf16 v[40:43], a[104:107], v[104:107], v[40:43]\n"
        "s_waitcnt vmcnt(0) lgkmcnt(3)\n"
        "v_cmp_ne_u64 vcc, v[252:253], v[250:251]\n"
        "v_add_f32 v57, v57, v232\n"
        "v_mfma_f32_16x16x32_bf16 v[40:43], a[108:111], v[108:111], v[40:43]\n"
        "s_cbranch_vccnz LPKV_%=\n"
        "LBKV_%=:\n"
        "v_exp_f32 v55, v55\n"
        "v_mfma_f32_16x16x32_bf16 v[176:179], a[128:131], v[16:19], v[176:179]\n"
        "ds_read_b128 a[100:103], v240 offset:17408\n"
        "v_cvt_pk_bf16_f32 v23, v54, v55\n"
        "v_mfma_f32_16x16x32_bf16 v[52:55], a[124:127], v[76:79], v[228:231]\n"
        "v_exp_f32 v56, v56\n"
        "v_mfma_f32_16x16x32_bf16 v[52:55], a[120:123], v[72:75], v[52:55]\n"
        "ds_read_b128 a[104:107], v240 offset:18432\n"
        "v_mov_b32 v144, v254\n"
        "v_mfma_f32_16x16x32_bf16 v[52:55], a[116:119], v[68:71], v[52:55]\n"
        "v_exp_f32 v57, v57\n"
        "v_mfma_f32_16x16x32_bf16 v[52:55], a[112:115], v[64:67], v[52:55]\n"
        "ds_read_b128 a[108:111], v240 offset:19456\n"
        "v_mov_b32 v145, v254\n"
        "v_smfmac_f32_16x16x64_bf16 a[64:67], a[72:75], v[16:23], v154\n"
        "global_load_lds_dwordx4 v254, s[68:69] offset:0 sc1\n"
        "v_cvt_pk_bf16_f32 v26, v56, v57\n"
        "v_mfma_f32_16x16x32_bf16 v[192:195], a[144:147], v[20:23], v[192:195]\n"
        "v_mov_b32 v146, v254\n"
        "v_mov_b32 v147, v254\n"
        "v_mfma_f32_16x16x32_bf16 v[196:199], a[148:151], v[20:23], v[196:199]\n"
        "v_accvgpr_read_b32 v148, a88\n"
        "v_accvgpr_read_b32 v152, a89\n"
        "v_mfma_f32_16x16x32_bf16 v[200:203], a[152:155], v[20:23], v[200:203]\n"
        "v_accvgpr_read_b32 v156, a90\n"
        "v_accvgpr_read_b32 v153, a92\n"
        "v_mfma_f32_16x16x32_bf16 v[204:207], a[156:159], v[20:23], v[204:207]\n"
        "global_load_lds_dwordx4 v254, s[68:69] offset:1024 sc1\n"
        "v_accvgpr_read_b32 v155, a94\n"
        "v_mfma_f32_16x16x32_bf16 v[208:211], a[128:131], v[20:23], v[208:211]\n"
        "v_add_u32 v149, 1024, v148\n"
        "v_accvgpr_write_b32 a88, 0\n"
        "v_mfma_f32_16x16x32_bf16 v[212:215], a[132:135], v[20:23], v[212:215]\n"
        "v_add_u32 v150, 2048, v148\n"
        "v_accvgpr_write_b32 a89, 0\n"
        "v_mfma_f32_16x16x32_bf16 v[180:183], a[132:135], v[16:19], v[180:183]\n"
        "s_waitcnt lgkmcnt(0)\n"
        "v_add_u32 v151, 3072, v148\n"
        "v_accvgpr_write_b32 a90, 0\n"
        "v_mfma_f32_16x16x32_bf16 v[184:187], a[136:139], v[16:19], v[184:187]\n"
        "global_load_lds_dwordx4 v254, s[68:69] offset:2048 sc1\n"
        "v_add_f32 v58, v58, v232\n"
        "v_mfma_f32_16x16x32_bf16 v[216:219], a[136:139], v[20:23], v[216:219]\n"
        "v_exp_f32 v58, v58\n"
        "v_mfma_f32_16x16x32_bf16 v[220:223], a[140:143], v[20:23], v[220:223]\n"
        "v_add_f32 v59, v59, v232\n"
        "v_cndmask_b32 v56, v232, v242, s[62:63]\n"
        "v_mfma_f32_16x16x32_bf16 v[188:191], a[140:143], v[16:19], v[188:191]\n"
        "v_exp_f32 v59, v59\n"
        "v_exp_f32 v32, v32\n"
        "v_cvt_pk_bf16_f32 v27, v58, v59\n"
        "s_nop 1\n"
        "v_mfma_f32_16x16x32_bf16 a[68:71], a[76:79], v[24:27], a[68:71]\n"
        "v_exp_f32 v33, v33\n"
        "v_mfma_f32_16x16x32_bf16 a[0:3], a[144:147], v[24:27], 0\n"
        "ds_read_b128 a[144:147], v241 offset:8192\n"
        "v_cvt_pk_bf16_f32 v80, v32, v33\n"
        "v_mfma_f32_16x16x32_bf16 a[4:7], a[148:151], v[24:27], 0\n"
        "v_exp_f32 v34, v34\n"
        "v_mfma_f32_16x16x32_bf16 a[8:11], a[152:155], v[24:27], 0\n"
        "v_exp_f32 v35, v35\n"
        "v_mfma_f32_16x16x32_bf16 a[12:15], a[156:159], v[24:27], 0\n"
        "ds_read_b128 a[148:151], v241 offset:9216\n"
        "v_cvt_pk_bf16_f32 v81, v34, v35\n"
        "v_mfma_f32_16x16x32_bf16 v[32:35], a[96:99], v[0:3], v[224:227]\n"
        "ds_read_b128 a[152:155], v241 offset:10240\n"
        "v_cndmask_b32 v57, v232, v242, s[84:85]\n"
        "v_mfma_f32_16x16x32_bf16 v[32:35], a[100:103], v[4:7], v[32:35]\n"
        "ds_read_b128 a[156:159], v241 offset:11264\n"
        "v_cndmask_b32 v58, v232, v242, s[86:87]\n"
        "v_mfma_f32_16x16x32_bf16 v[32:35], a[104:107], v[8:11], v[32:35]\n"
        "global_load_lds_dwordx4 v254, s[68:69] offset:3072 sc1\n"
        "s_add_u32 m0, s81, 0x0\n"
        "v_cndmask_b32 v59, v232, v242, s[88:89]\n"
        "s_nop 1\n"
        "v_mfma_f32_16x16x32_bf16 v[56:59], a[112:115], v[96:99], v[56:59]\n"
        "s_add_u32 s81, s78, 4096\n"
        "v_exp_f32 v36, v36\n"
        "v_mfma_f32_16x16x32_bf16 v[56:59], a[116:119], v[100:103], v[56:59]\n"
        "v_exp_f32 v37, v37\n"
        "v_mfma_f32_16x16x32_bf16 v[56:59], a[120:123], v[104:107], v[56:59]\n"
        "ds_read_b128 a[112:115], v240 offset:20480\n"
        "v_cvt_pk_bf16_f32 v84, v36, v37\n"
        "v_mfma_f32_16x16x32_bf16 v[56:59], a[124:127], v[108:111], v[56:59]\n"
        "v_exp_f32 v38, v38\n"
        "v_mfma_f32_16x16x32_bf16 v[32:35], a[108:111], v[12:15], v[32:35]\n"
        "v_exp_f32 v39, v39\n"
        "v_mfma_f32_16x16x32_bf16 a[16:19], a[128:131], v[24:27], 0\n"
        "ds_read_b128 a[116:119], v240 offset:21504\n"
        "v_cvt_pk_bf16_f32 v85, v38, v39\n"
        "v_mfma_f32_16x16x32_bf16 v[36:39], a[108:111], v[76:79], v[228:231]\n"
        "v_exp_f32 v40, v40\n"
        "v_mfma_f32_16x16x32_bf16 v[36:39], a[104:107], v[72:75], v[36:39]\n"
        "v_exp_f32 v41, v41\n"
        "v_mfma_f32_16x16x32_bf16 v[36:39], a[100:103], v[68:71], v[36:39]\n"
        "ds_read_b128 a[120:123], v240 offset:22528\n"
        "v_cvt_pk_bf16_f32 v88, v40, v41\n"
        "v_mfma_f32_16x16x32_bf16 v[36:39], a[96:99], v[64:67], v[36:39]\n"
        "ds_read_b128 a[124:127], v240 offset:23552\n"
        "v_and_b32 v245, 0x70, v254\n"
        "v_mfma_f32_16x16x32_bf16 a[20:23], a[132:135], v[24:27], 0\n"
        "global_load_lds_dwordx4 v254, s[74:75] offset:0 sc1\n"
        "v_lshlrev_b32 v245, 3, v245\n"
        "v_mfma_f32_16x16x32_bf16 a[24:27], a[136:139], v[24:27], 0\n"
        "v_exp_f32 v42, v42\n"
        "v_mfma_f32_16x16x32_bf16 a[28:31], a[140:143], v[24:27], 0\n"
        "v_exp_f32 v43, v43\n"
        "v_exp_f32 v48, v48\n"
        "v_cvt_pk_bf16_f32 v89, v42, v43\n"
        "v_mfma_f32_16x16x32_bf16 v[40:43], a[96:99], v[96:99], v[232:235]\n"
        "v_exp_f32 v49, v49\n"
        "v_mfma_f32_16x16x32_bf16 v[40:43], a[100:103], v[100:103], v[40:43]\n"
        "global_load_lds_dwordx4 v254, s[74:75] offset:1024 sc1\n"
        "v_cvt_pk_bf16_f32 v82, v48, v49\n"
        "v_mfma_f32_16x16x32_bf16 v[40:43], a[104:107], v[104:107], v[40:43]\n"
        "v_exp_f32 v50, v50\n"
        "v_mfma_f32_16x16x32_bf16 v[40:43], a[108:111], v[108:111], v[40:43]\n"
        "v_exp_f32 v51, v51\n"
        "v_exp_f32 v52, v52\n"
        "v_cvt_pk_bf16_f32 v83, v50, v51\n"
        "s_waitcnt lgkmcnt(0)\n"
        "v_mfma_f32_16x16x32_bf16 v[48:51], a[112:115], v[0:3], v[224:227]\n"
        "v_exp_f32 v53, v53\n"
        "v_mfma_f32_16x16x32_bf16 v[48:51], a[116:119], v[4:7], v[48:51]\n"
        "global_load_lds_dwordx4 v254, s[74:75] offset:2048 sc1\n"
        "v_cvt_pk_bf16_f32 v86, v52, v53\n"
        "v_mfma_f32_16x16x32_bf16 v[48:51], a[120:123], v[8:11], v[48:51]\n"
        "ds_read_b128 a[128:131], v241 offset:12288\n"
        "v_cndmask_b32 v44, 0, v242, s[10:11]\n"
        "v_mfma_f32_16x16x32_bf16 v[48:51], a[124:127], v[12:15], v[48:51]\n"
        "ds_read_b128 a[132:135], v241 offset:13312\n"
        "s_lshr_b32 s10, s41, 1\n"
        "v_cndmask_b32 v45, 0, v242, s[12:13]\n"
        "v_mfma_f32_16x16x32_bf16 v[160:163], a[144:147], v[80:83], v[160:163]\n"
        "ds_read_b128 a[136:139], v241 offset:14336\n"
        "s_and_b32 s11, s42, 7\n"
        "v_cndmask_b32 v46, 0, v242, s[14:15]\n"
        "v_mfma_f32_16x16x32_bf16 v[164:167], a[148:151], v[80:83], v[164:167]\n"
        "global_load_lds_dwordx4 v254, s[74:75] offset:3072 sc1\n"
        "s_add_u32 m0, s79, 0x0\n"
        "v_cndmask_b32 v47, 0, v242, s[16:17]\n"
        "s_nop 1\n"
        "v_mfma_f32_16x16x32_bf16 v[44:47], a[108:111], v[140:143], v[44:47]\n"
        "s_and_b32 s12, s41, 1\n"
        "v_exp_f32 v54, v54\n"
        "v_mfma_f32_16x16x32_bf16 v[44:47], a[104:107], v[136:139], v[44:47]\n"
        "s_lshr_b32 s14, s42, 3\n"
        "v_exp_f32 v55, v55\n"
        "v_mfma_f32_16x16x32_bf16 v[44:47], a[100:103], v[132:135], v[44:47]\n"
        "ds_read_b128 a[100:103], v240 offset:25600\n"
        "s_lshl2_add_u32 s12, s12, s14\n"
        "v_cvt_pk_bf16_f32 v87, v54, v55\n"
        "v_mfma_f32_16x16x32_bf16 v[44:47], a[96:99], v[128:131], v[44:47]\n"
        "ds_read_b128 a[96:99], v240 offset:24576\n"
        "global_load_lds_dwordx4 v254, s[76:77] offset:0 sc1\n"
        "s_lshl3_add_u32 s13, s12, s11\n"
        "v_mfma_f32_16x16x32_bf16 v[52:55], a[124:127], v[76:79], v[228:231]\n"
        "s_lshl_b32 s14, s10, 21\n"
        "v_exp_f32 v56, v56\n"
        "v_mfma_f32_16x16x32_bf16 v[52:55], a[120:123], v[72:75], v[52:55]\n"
        "s_lshl_b32 s15, s40, 15\n"
        "v_exp_f32 v57, v57\n"
        "v_mfma_f32_16x16x32_bf16 v[52:55], a[116:119], v[68:71], v[52:55]\n"
        "ds_read_b128 a[104:107], v240 offset:26624\n"
        "s_add_u32 s14, s14, s15\n"
        "v_cvt_pk_bf16_f32 v90, v56, v57\n"
        "v_mfma_f32_16x16x32_bf16 v[52:55], a[112:115], v[64:67], v[52:55]\n"
        "s_lshl_b32 s15, s11, 8\n"
        "v_exp_f32 v58, v58\n"
        "v_smfmac_f32_16x16x64_bf16 a[64:67], a[72:75], v[80:87], v154\n"
        "ds_read_b128 a[140:143], v241 offset:15360\n"
        "global_load_lds_dwordx4 v254, s[76:77] offset:1024 sc1\n"
        "s_add_u32 s14, s14, s15\n"
        "v_mfma_f32_16x16x32_bf16 v[192:195], a[144:147], v[84:87], v[192:195]\n"
        "s_add_u32 s17, s11, s12\n"
        "v_exp_f32 v59, v59\n"
        "v_mfma_f32_16x16x32_bf16 v[196:199], a[148:151], v[84:87], v[196:199]\n"
        "ds_read_b128 a[108:111], v240 offset:27648\n"
        "s_and_b32 s17, s17, 7\n"
        "v_cvt_pk_bf16_f32 v91, v58, v59\n"
        "v_mfma_f32_16x16x32_bf16 v[56:59], a[112:115], v[96:99], v[232:235]\n"
        "v_exp_f32 v32, v32\n"
        "v_mfma_f32_16x16x32_bf16 v[56:59], a[116:119], v[100:103], v[56:59]\n"
        "v_exp_f32 v33, v33\n"
        "v_mfma_f32_16x16x32_bf16 v[56:59], a[120:123], v[104:107], v[56:59]\n"
        "global_load_lds_dwordx4 v254, s[76:77] offset:2048 sc1\n"
        "v_cvt_pk_bf16_f32 v16, v32, v33\n"
        "v_mfma_f32_16x16x32_bf16 v[56:59], a[124:127], v[108:111], v[56:59]\n"
        "v_exp_f32 v34, v34\n"
        "v_mfma_f32_16x16x32_bf16 a[68:71], a[76:79], v[88:91], a[68:71]\n"
        "v_exp_f32 v35, v35\n"
        "v_mfma_f32_16x16x32_bf16 a[0:3], a[144:147], v[88:91], a[0:3]\n"
        "ds_read_b128 a[144:147], v241 offset:16384\n"
        "v_cvt_pk_bf16_f32 v17, v34, v35\n"
        "v_mfma_f32_16x16x32_bf16 a[4:7], a[148:151], v[88:91], a[4:7]\n"
        "v_exp_f32 v36, v36\n"
        "v_mfma_f32_16x16x32_bf16 v[168:171], a[152:155], v[80:83], v[168:171]\n"
        "v_exp_f32 v37, v37\n"
        "v_mfma_f32_16x16x32_bf16 v[200:203], a[152:155], v[84:87], v[200:203]\n"
        "global_load_lds_dwordx4 v254, s[76:77] offset:3072 sc1\n"
        "s_add_u32 m0, s79, 0x1000\n"
        "v_max_f32 v248, v44, v45\n"
        "v_mfma_f32_16x16x32_bf16 a[8:11], a[152:155], v[88:91], a[8:11]\n"
        "ds_read_b128 a[148:151], v241 offset:17408\n"
        "v_cvt_pk_bf16_f32 v20, v36, v37\n"
        "v_mfma_f32_16x16x32_bf16 a[12:15], a[156:159], v[88:91], a[12:15]\n"
        "ds_read_b128 a[152:155], v241 offset:18432\n"
        "v_max3_f32 v248, v46, v47, v248\n"
        "v_mfma_f32_16x16x32_bf16 v[204:207], a[156:159], v[84:87], v[204:207]\n"
        "s_add_u32 s76, s76, 0x1000\n"
        "s_addc_u32 s77, s77, 0\n"
        "v_mov_b32 v249, v248\n"
        "v_mfma_f32_16x16x32_bf16 v[172:175], a[156:159], v[80:83], v[172:175]\n"
        "s_waitcnt lgkmcnt(3)\n"
        "v_permlane16_swap_b32 v248, v249\n"
        "v_mfma_f32_16x16x32_bf16 v[32:35], a[96:99], v[0:3], v[224:227]\n"
        "global_load_lds_dwordx4 v254, s[76:77] offset:0 sc1\n"
        "v_max_f32 v248, v248, v249\n"
        "v_mfma_f32_16x16x32_bf16 v[32:35], a[100:103], v[4:7], v[32:35]\n"
        "ds_read_b128 a[156:159], v241 offset:19456\n"
        "v_mov_b32 v249, v248\n"
        "v_mfma_f32_16x16x32_bf16 v[32:35], a[104:107], v[8:11], v[32:35]\n"
        "v_exp_f32 v38, v38\n"
        "v_mfma_f32_16x16x32_bf16 v[32:35], a[108:111], v[12:15], v[32:35]\n"
        "v_permlane32_swap_b32 v248, v249\n"
        "v_mfma_f32_16x16x32_bf16 v[176:179], a[128:131], v[80:83], v[176:179]\n"
        "global_load_lds_dwordx4 v254, s[76:77] offset:1024 sc1\n"
        "v_max_f32 v248, v248, v249\n"
        "v_mfma_f32_16x16x32_bf16 v[208:211], a[128:131], v[84:87], v[208:211]\n"
        "s_add_u32 s68, s64, s90\n"
        "s_addc_u32 s69, s65, 0\n"
        "v_sub_f32 v236, 0, v248\n"
        "v_mfma_f32_16x16x32_bf16 a[16:19], a[128:131], v[88:91], a[16:19]\n"
        "ds_read_b128 a[128:131], v241 offset:20480\n"
        "v_sub_f32 v237, 0, v248\n"
        "v_mfma_f32_16x16x32_bf16 a[20:23], a[132:135], v[88:91], a[20:23]\n"
        "s_add_u32 s74, s68, 0x1000\n"
        "s_addc_u32 s75, s69, 0\n"
        "v_sub_f32 v238, 0, v248\n"
        "v_mfma_f32_16x16x32_bf16 v[212:215], a[132:135], v[84:87], v[212:215]\n"
        "global_load_lds_dwordx4 v254, s[76:77] offset:2048 sc1\n"
        "v_sub_f32 v239, 0, v248\n"
        "v_mfma_f32_16x16x32_bf16 v[180:183], a[132:135], v[80:83], v[180:183]\n"
        "v_exp_f32 v39, v39\n"
        "v_mfma_f32_16x16x32_bf16 v[184:187], a[136:139], v[80:83], v[184:187]\n"
        "ds_read_b128 a[132:135], v241 offset:21504\n"
        "v_cvt_pk_bf16_f32 v21, v38, v39\n"
        "v_mfma_f32_16x16x32_bf16 v[36:39], a[108:111], v[76:79], v[228:231]\n"
        "v_cndmask_b32 v60, 0, v242, s[20:21]\n"
        "v_cndmask_b32 v61, 0, v242, s[22:23]\n"
        "v_mfma_f32_16x16x32_bf16 v[36:39], a[104:107], v[72:75], v[36:39]\n"
        "global_load_lds_dwordx4 v254, s[76:77] offset:3072 sc1\n"
        "s_add_u32 m0, s96, 0x20260\n"
        "v_cndmask_b32 v62, 0, v242, s[24:25]\n"
        "v_mfma_f32_16x16x32_bf16 v[36:39], a[100:103], v[68:71], v[36:39]\n"
        "s_sub_u32 s24, s83, 1\n"
        "v_cndmask_b32 v63, 0, v242, s[28:29]\n"
        "s_nop 1\n"
        "v_mfma_f32_16x16x32_bf16 v[60:63], a[124:127], v[140:143], v[60:63]\n"
        "s_min_u32 s24, s24, s91\n"
        "v_exp_f32 v40, v40\n"
        "v_mfma_f32_16x16x32_bf16 v[60:63], a[120:123], v[136:139], v[60:63]\n"
        "s_mov_b32 s25, 4096\n"
        "v_exp_f32 v41, v41\n"
        "v_mfma_f32_16x16x32_bf16 v[60:63], a[116:119], v[132:135], v[60:63]\n"
        "ds_read_b128 a[116:119], v240 offset:29696\n"
        "s_mov_b32 s28, 4096\n"
        "v_cvt_pk_bf16_f32 v24, v40, v41\n"
        "v_mfma_f32_16x16x32_bf16 v[60:63], a[112:115], v[128:131], v[60:63]\n"
        "ds_read_b128 a[112:115], v240 offset:28672\n"
        "s_lshl_b32 s29, s17, 18\n"
        "v_add_f32 v44, v44, v236\n"
        "v_mfma_f32_16x16x32_bf16 v[36:39], a[96:99], v[64:67], v[36:39]\n"
        "v_exp_f32 v42, v42\n"
        "v_mfma_f32_16x16x32_bf16 v[216:219], a[136:139], v[84:87], v[216:219]\n"
        "v_exp_f32 v43, v43\n"
        "v_mfma_f32_16x16x32_bf16 a[24:27], a[136:139], v[88:91], a[24:27]\n"
        "ds_read_b128 a[120:123], v240 offset:30720\n"
        "v_cvt_pk_bf16_f32 v25, v42, v43\n"
        "v_mfma_f32_16x16x32_bf16 v[40:43], a[96:99], v[96:99], v[232:235]\n"
        "v_exp_f32 v44, v44\n"
        "v_mfma_f32_16x16x32_bf16 v[40:43], a[100:103], v[100:103], v[40:43]\n"
        "global_load_lds_dwordx4 v254, s[70:71] offset:0\n"
        "v_add_f32 v45, v45, v236\n"
        "v_mfma_f32_16x16x32_bf16 v[40:43], a[104:107], v[104:107], v[40:43]\n"
        "v_exp_f32 v45, v45\n"
        "v_mfma_f32_16x16x32_bf16 v[40:43], a[108:111], v[108:111], v[40:43]\n"
        "ds_read_b128 a[124:127], v240 offset:31744\n"
        "v_cvt_pk_bf16_f32 v28, v44, v45\n"
        "v_mfma_f32_16x16x32_bf16 a[28:31], a[140:143], v[88:91], a[28:31]\n"
        "ds_read_b128 a[136:139], v241 offset:22528\n"
        "v_add_f32 v46, v46, v236\n"
        "v_mfma_f32_16x16x32_bf16 v[220:223], a[140:143], v[84:87], v[220:223]\n"
        "v_exp_f32 v46, v46\n"
        "v_mfma_f32_16x16x32_bf16 v[188:191], a[140:143], v[80:83], v[188:191]\n"
        "v_add_f32 v47, v47, v236\n"
        "v_exp_f32 v47, v47\n"
        "v_exp_f32 v48, v48\n"
        "v_cvt_pk_bf16_f32 v29, v46, v47\n"
        "v_exp_f32 v49, v49\n"
        "global_load_lds_dwordx4 v254, s[70:71] offset:1024\n"
        "v_cvt_pk_bf16_f32 v18, v48, v49\n"
        "v_exp_f32 v50, v50\n"
        "v_exp_f32 v51, v51\n"
        "v_exp_f32 v52, v52\n"
        "v_cvt_pk_bf16_f32 v19, v50, v51\n"
        "s_waitcnt lgkmcnt(2)\n"
        "v_mfma_f32_16x16x32_bf16 v[48:51], a[112:115], v[0:3], v[224:227]\n"
        "s_waitcnt vmcnt(10) lgkmcnt(1)\n"
        "v_exp_f32 v53, v53\n"
        "v_mfma_f32_16x16x32_bf16 v[48:51], a[116:119], v[4:7], v[48:51]\n"
        "global_load_lds_dwordx4 v254, s[70:71] offset:2048\n"
        "v_cvt_pk_bf16_f32 v22, v52, v53\n"
        "v_mfma_f32_16x16x32_bf16 v[48:51], a[120:123], v[8:11], v[48:51]\n"
        "v_exp_f32 v54, v54\n"
        "v_mfma_f32_16x16x32_bf16 v[48:51], a[124:127], v[12:15], v[48:51]\n"
        "v_exp_f32 v55, v55\n"
        "v_mfma_f32_16x16x32_bf16 v[160:163], a[144:147], v[16:19], v[160:163]\n"
        "ds_read_b128 a[140:143], v241 offset:23552\n"
        "v_cvt_pk_bf16_f32 v23, v54, v55\n"
        "v_mfma_f32_16x16x32_bf16 v[52:55], a[124:127], v[76:79], v[228:231]\n"
        "global_load_dword v244, v245, s[26:27] offset:0\n"
        "v_cndmask_b32 v44, v236, v242, s[30:31]\n"
        "v_mfma_f32_16x16x32_bf16 v[52:55], a[120:123], v[72:75], v[52:55]\n"
        "s_add_u32 s26, s26, 0x20000\n"
        "s_addc_u32 s27, s27, 0\n"
        "v_cndmask_b32 v45, v236, v242, s[56:57]\n"
        "v_mfma_f32_16x16x32_bf16 v[52:55], a[116:119], v[68:71], v[52:55]\n"
        "global_load_dword v244, v245, s[26:27] offset:0\n"
        "s_mov_b32 s30, 131072\n"
        "v_cndmask_b32 v46, v236, v242, s[58:59]\n"
        "v_mfma_f32_16x16x32_bf16 v[52:55], a[112:115], v[64:67], v[52:55]\n"
        "s_add_u32 s26, s26, 0x20000\n"
        "s_addc_u32 s27, s27, 0\n"
        "v_cndmask_b32 v47, v236, v242, s[60:61]\n"
        "s_nop 1\n"
        "v_mfma_f32_16x16x32_bf16 v[44:47], a[108:111], v[140:143], v[44:47]\n"
        "v_exp_f32 v56, v56\n"
        "v_mfma_f32_16x16x32_bf16 v[44:47], a[104:107], v[136:139], v[44:47]\n"
        "v_exp_f32 v57, v57\n"
        "v_mfma_f32_16x16x32_bf16 v[44:47], a[100:103], v[132:135], v[44:47]\n"
        "s_add_u32 s22, s18, s14\n"
        "s_addc_u32 s23, s19, 0\n"
        "v_cvt_pk_bf16_f32 v26, v56, v57\n"
        "v_mfma_f32_16x16x32_bf16 v[44:47], a[96:99], v[128:131], v[44:47]\n"
        "s_barrier\n"
        "v_smfmac_f32_16x16x64_bf16 a[64:67], a[72:75], v[16:23], v154\n"
        "s_sub_u32 s22, s22, 4096\n"
        "s_subb_u32 s23, s23, 0\n"
        "v_xor_b32 v240, 0x8000, v240\n"
        "v_mfma_f32_16x16x32_bf16 v[192:195], a[144:147], v[20:23], v[192:195]\n"
        "ds_read_b128 a[96:99], v240 offset:0\n"
        "s_lshl_b32 s14, s10, 18\n"
        "v_lshrrev_b32 v252, 2, v254\n"
        "v_mfma_f32_16x16x32_bf16 v[196:199], a[148:151], v[20:23], v[196:199]\n"
        "s_add_u32 s14, s14, s72\n"
        "v_exp_f32 v58, v58\n"
        "v_mfma_f32_16x16x32_bf16 v[164:167], a[148:151], v[16:19], v[164:167]\n"
        "s_add_u32 s14, s14, 0x2200000\n"
        "v_exp_f32 v59, v59\n"
        "v_mfma_f32_16x16x32_bf16 v[168:171], a[152:155], v[16:19], v[168:171]\n"
        "ds_read_b128 a[100:103], v240 offset:1024\n"
        "v_cvt_pk_bf16_f32 v27, v58, v59\n"
        "v_mfma_f32_16x16x32_bf16 v[56:59], a[112:115], v[96:99], v[232:235]\n"
        "ds_read_b128 a[104:107], v240 offset:2048\n"
        "v_add_f32 v60, v60, v236\n"
        "v_mfma_f32_16x16x32_bf16 v[56:59], a[116:119], v[100:103], v[56:59]\n"
        "v_exp_f32 v60, v60\n"
        "v_mfma_f32_16x16x32_bf16 v[56:59], a[120:123], v[104:107], v[56:59]\n"
        "ds_read_b128 a[108:111], v240 offset:3072\n"
        "v_add_f32 v61, v61, v236\n"
        "v_mfma_f32_16x16x32_bf16 v[56:59], a[124:127], v[108:111], v[56:59]\n"
        "v_exp_f32 v61, v61\n"
        "v_mfma_f32_16x16x32_bf16 a[0:3], a[144:147], v[24:27], a[0:3]\n"
        "s_add_u32 s20, s38, s14\n"
        "s_addc_u32 s21, s39, 0\n"
        "v_cvt_pk_bf16_f32 v30, v60, v61\n"
        "v_mfma_f32_16x16x32_bf16 a[4:7], a[148:151], v[24:27], a[4:7]\n"
        "s_waitcnt lgkmcnt(3)\n"
        "v_add_f32 v62, v62, v236\n"
        "v_add_f32 v63, v63, v236\n"
        "v_mfma_f32_16x16x32_bf16 v[200:203], a[152:155], v[20:23], v[200:203]\n"
        "v_exp_f32 v62, v62\n"
        "v_mfma_f32_16x16x32_bf16 a[8:11], a[152:155], v[24:27], a[8:11]\n"
        "v_exp_f32 v63, v63\n"
        "v_mfma_f32_16x16x32_bf16 a[12:15], a[156:159], v[24:27], a[12:15]\n"
        "v_cvt_pk_bf16_f32 v31, v62, v63\n"
        "s_nop 1\n"
        "v_smfmac_f32_16x16x64_bf16 a[68:71], a[72:75], v[24:31], v154\n"
        "v_cndmask_b32 v60, v236, v242, s[62:63]\n"
        "v_mfma_f32_16x16x32_bf16 a[32:35], a[144:147], v[28:31], 0\n"
        "v_exp_f32 v32, v32\n"
        "v_mfma_f32_16x16x32_bf16 a[36:39], a[148:151], v[28:31], 0\n"
        "v_exp_f32 v33, v33\n"
        "v_mfma_f32_16x16x32_bf16 a[40:43], a[152:155], v[28:31], 0\n"
        "ds_read_b128 a[144:147], v241 offset:24576\n"
        "v_cvt_pk_bf16_f32 v80, v32, v33\n"
        "v_mfma_f32_16x16x32_bf16 a[44:47], a[156:159], v[28:31], 0\n"
        "v_exp_f32 v34, v34\n"
        "v_mfma_f32_16x16x32_bf16 v[204:207], a[156:159], v[20:23], v[204:207]\n"
        "v_exp_f32 v35, v35\n"
        "v_mfma_f32_16x16x32_bf16 v[172:175], a[156:159], v[16:19], v[172:175]\n"
        "s_mov_b32 exec_hi, 0x7fffffff\n"
        "global_load_lds_dwordx4 v254, s[70:71] offset:3072\n"
        "s_mov_b64 exec, -1\n"
        "v_cvt_pk_bf16_f32 v81, v34, v35\n"
        "v_exp_f32 v36, v36\n"
        "v_exp_f32 v37, v37\n"
        "v_cndmask_b32 v61, v236, v242, s[84:85]\n"
        "v_cvt_pk_bf16_f32 v84, v36, v37\n"
        "v_cndmask_b32 v62, v236, v242, s[86:87]\n"
        "v_cndmask_b32 v63, v236, v242, s[88:89]\n"
        "s_nop 1\n"
        "v_mfma_f32_16x16x32_bf16 v[60:63], a[124:127], v[140:143], v[60:63]\n"
        "v_exp_f32 v38, v38\n"
        "v_mfma_f32_16x16x32_bf16 v[60:63], a[120:123], v[136:139], v[60:63]\n"
        "v_exp_f32 v39, v39\n"
        "v_mfma_f32_16x16x32_bf16 v[60:63], a[116:119], v[132:135], v[60:63]\n"
        "ds_read_b128 a[116:119], v240 offset:5120\n"
        "v_cvt_pk_bf16_f32 v85, v38, v39\n"
        "v_mfma_f32_16x16x32_bf16 v[60:63], a[112:115], v[128:131], v[60:63]\n"
        "s_mov_b64 exec, 7\n"
        "global_load_lds_dword v252, s[70:71] offset:4080\n"
        "s_mov_b64 exec, -1\n"
        "ds_read_b128 a[112:115], v240 offset:4096\n"
        "v_exp_f32 v40, v40\n"
        "v_exp_f32 v41, v41\n"
        "v_exp_f32 v42, v42\n"
        "v_cvt_pk_bf16_f32 v88, v40, v41\n"
        "v_exp_f32 v43, v43\n"
        "ds_read_b128 a[148:151], v241 offset:25600\n"
        "v_cvt_pk_bf16_f32 v89, v42, v43\n"
        "v_exp_f32 v44, v44\n"
        "v_exp_f32 v45, v45\n"
        "ds_read_b128 a[152:155], v241 offset:26624\n"
        "v_cvt_pk_bf16_f32 v92, v44, v45\n"
        "v_exp_f32 v46, v46\n"
        "v_exp_f32 v47, v47\n"
        "ds_read_b128 a[120:123], v240 offset:6144\n"
        "v_cvt_pk_bf16_f32 v93, v46, v47\n"
        "s_cmp_eq_u32 s44, 0\n"
        "s_cbranch_scc1 LLD_%=\n"
        "; ---- DUAL\n"
        "s_branch LDU_%=\n"
        "s_nop 0\n"
        "s_nop 0\n"
        "s_nop 0\n"
        "s_nop 0\n"
        "s_nop 0\n"
        "s_nop 0\n"
        "s_nop 0\n"
        "s_nop 0\n"
        "s_nop 0\n"
        "s_nop 0\n"
        "s_nop 0\n"
        "s_nop 0\n"
        "s_nop 0\n"
        "s_nop 0\n"
        "s_nop 0\n"
        "s_nop 0\n"
        "s_nop 0\n"
        "s_nop 0\n"
        "s_nop 0\n"
        "s_nop 0\n"
        "s_nop 0\n"
        "s_nop 0\n"
        "s_nop 0\n"
        "s_nop 0\n"
        "s_nop 0\n"
        "s_nop 0\n"
        "s_nop 0\n"
        "s_nop 0\n"
        "s_nop 0\n"
        "s_nop 0\n"
        "s_nop 0\n"
        "s_nop 0\n"
        "s_nop 0\n"
        "s_nop 0\n"
        "s_nop 0\n"
        "s_nop 0\n"
        "s_nop 0\n"
        "s_nop 0\n"
        "s_nop 0\n"
        "s_nop 0\n"
        "s_nop 0\n"
        "s_nop 0\n"
        "s_nop 0\n"
        "s_nop 0\n"
        "s_nop 0\n"
        "s_nop 0\n"
        "s_nop 0\n"
        "s_nop 0\n"
        "s_nop 0\n"
        "s_nop 0\n"
        "s_nop 0\n"
        "s_nop 0\n"
        "s_nop 0\n"
        "s_nop 0\n"
        "s_nop 0\n"
        "s_nop 0\n"
        "s_nop 0\n"
        "s_nop 0\n"
        "s_nop 0\n"
        "s_nop 0\n"
        "s_nop 0\n"
        "s_nop 0\n"
        "s_nop 0\n"
        "s_nop 0\n"
        "s_nop 0\n"
        "s_nop 0\n"
        "s_nop 0\n"
        "LHA11_%=:\n"
        "s_cbranch_execz LHRA_%=\n"
        "s_nop 0\n"
        "s_nop 0\n"
        "s_nop 0\n"
        "s_nop 0\n"
        "s_nop 0\n"
        "s_nop 0\n"
        "s_nop 0\n"
        "s_nop 0\n"
        "s_nop 0\n"
        "s_nop 0\n"
        "s_nop 0\n"
        "s_nop 0\n"
        "s_nop 0\n"
        "s_nop 0\n"
        "s_nop 0\n"
        "LDU_%=:\n"
        "v_mfma_f32_16x16x32_bf16 v[32:35], a[96:99], v[0:3], v[224:227]\n"
        "s_waitcnt lgkmcnt(4)\n"
        "ds_read_b128 a[156:159], v241 offset:27648\n"
        "s_add_u32 m0, s80, 0x0\n"
        "v_mfma_f32_16x16x32_bf16 v[32:35], a[100:103], v[4:7], v[32:35]\n"
        "ds_read_b128 a[124:127], v240 offset:7168\n"
        "global_load_lds_dwordx4 v254, s[68:69] offset:0 sc1\n"
        "s_xor_b32 s79, s79, 0x8000\n"
        "v_mfma_f32_16x16x32_bf16 v[32:35], a[104:107], v[8:11], v[32:35]\n"
        "v_exp_f32 v48, v48\n"
        "v_mfma_f32_16x16x32_bf16 v[32:35], a[108:111], v[12:15], v[32:35]\n"
        "v_exp_f32 v49, v49\n"
        "v_mfma_f32_16x16x32_bf16 v[36:39], a[108:111], v[76:79], v[228:231]\n"
        "v_exp_f32 v50, v50\n"
        "v_mfma_f32_16x16x32_bf16 v[36:39], a[104:107], v[72:75], v[36:39]\n"
        "v_exp_f32 v51, v51\n"
        "v_mfma_f32_16x16x32_bf16 v[36:39], a[100:103], v[68:71], v[36:39]\n"
        "v_exp_f32 v52, v52\n"
        "v_mfma_f32_16x16x32_bf16 v[36:39], a[96:99], v[64:67], v[36:39]\n"
        "v_exp_f32 v53, v53\n"
        "v_mfma_f32_16x16x32_bf16 v[40:43], a[96:99], v[96:99], v[232:235]\n"
        "v_exp_f32 v56, v56\n"
        "v_mfma_f32_16x16x32_bf16 v[40:43], a[100:103], v[100:103], v[40:43]\n"
        "v_exp_f32 v57, v57\n"
        "v_mfma_f32_16x16x32_bf16 v[40:43], a[104:107], v[104:107], v[40:43]\n"
        "v_exp_f32 v54, v54\n"
        "v_mfma_f32_16x16x32_bf16 v[40:43], a[108:111], v[108:111], v[40:43]\n"
        "v_exp_f32 v55, v55\n"
        "v_mfma_f32_16x16x32_bf16 v[44:47], a[108:111], v[140:143], v[236:239]\n"
        "ds_read_b128 a[108:111], v240 offset:11264\n"
        "v_cvt_pk_bf16_f32 v82, v48, v49\n"
        "v_mfma_f32_16x16x32_bf16 v[44:47], a[104:107], v[136:139], v[44:47]\n"
        "ds_read_b128 a[104:107], v240 offset:10240\n"
        "global_load_lds_dwordx4 v254, s[68:69] offset:1024 sc1\n"
        "v_mfma_f32_16x16x32_bf16 v[44:47], a[100:103], v[132:135], v[44:47]\n"
        "ds_read_b128 a[100:103], v240 offset:9216\n"
        "v_cvt_pk_bf16_f32 v83, v50, v51\n"
        "v_mfma_f32_16x16x32_bf16 v[44:47], a[96:99], v[128:131], v[44:47]\n"
        "ds_read_b128 a[96:99], v240 offset:8192\n"
        "v_cvt_pk_bf16_f32 v86, v52, v53\n"
        "v_mfma_f32_16x16x32_bf16 v[176:179], a[128:131], v[16:19], v[176:179]\n"
        "v_exp_f32 v58, v58\n"
        "v_mfma_f32_16x16x32_bf16 v[208:211], a[128:131], v[20:23], v[208:211]\n"
        "v_exp_f32 v59, v59\n"
        "v_mfma_f32_16x16x32_bf16 a[16:19], a[128:131], v[24:27], a[16:19]\n"
        "v_exp_f32 v60, v60\n"
        "v_mfma_f32_16x16x32_bf16 a[48:51], a[128:131], v[28:31], a[48:51]\n"
        "ds_read_b128 a[128:131], v241 offset:28672\n"
        "global_load_lds_dwordx4 v254, s[68:69] offset:2048 sc1\n"
        "v_mfma_f32_16x16x32_bf16 a[52:55], a[132:135], v[28:31], a[52:55]\n"
        "v_exp_f32 v61, v61\n"
        "v_mfma_f32_16x16x32_bf16 a[20:23], a[132:135], v[24:27], a[20:23]\n"
        "v_exp_f32 v62, v62\n"
        "v_mfma_f32_16x16x32_bf16 v[212:215], a[132:135], v[20:23], v[212:215]\n"
        "v_exp_f32 v63, v63\n"
        "v_mfma_f32_16x16x32_bf16 v[180:183], a[132:135], v[16:19], v[180:183]\n"
        "ds_read_b128 a[132:135], v241 offset:29696\n"
        "global_load_lds_dwordx4 v254, s[68:69] offset:3072 sc1\n"
        "s_add_u32 m0, s81, 0x0\n"
        "v_mfma_f32_16x16x32_bf16 v[184:187], a[136:139], v[16:19], v[184:187]\n"
        "v_exp_f32 v32, v32\n"
        "v_mfma_f32_16x16x32_bf16 v[216:219], a[136:139], v[20:23], v[216:219]\n"
        "v_exp_f32 v33, v33\n"
        "v_mfma_f32_16x16x32_bf16 a[24:27], a[136:139], v[24:27], a[24:27]\n"
        "v_exp_f32 v34, v34\n"
        "v_mfma_f32_16x16x32_bf16 a[56:59], a[136:139], v[28:31], a[56:59]\n"
        "ds_read_b128 a[136:139], v241 offset:30720\n"
        "global_load_lds_dwordx4 v254, s[74:75] offset:0 sc1\n"
        "v_mfma_f32_16x16x32_bf16 a[60:63], a[140:143], v[28:31], a[60:63]\n"
        "v_exp_f32 v35, v35\n"
        "v_mfma_f32_16x16x32_bf16 a[28:31], a[140:143], v[24:27], a[28:31]\n"
        "v_exp_f32 v36, v36\n"
        "v_mfma_f32_16x16x32_bf16 v[220:223], a[140:143], v[20:23], v[220:223]\n"
        "s_waitcnt lgkmcnt(2)\n"
        "v_exp_f32 v37, v37\n"
        "v_mfma_f32_16x16x32_bf16 v[188:191], a[140:143], v[16:19], v[188:191]\n"
        "ds_read_b128 a[140:143], v241 offset:31744\n"
        "v_cvt_pk_bf16_f32 v90, v56, v57\n"
        "v_mfma_f32_16x16x32_bf16 v[48:51], a[112:115], v[0:3], v[224:227]\n"
        "v_exp_f32 v38, v38\n"
        "v_mfma_f32_16x16x32_bf16 v[48:51], a[116:119], v[4:7], v[48:51]\n"
        "v_exp_f32 v39, v39\n"
        "v_mfma_f32_16x16x32_bf16 v[48:51], a[120:123], v[8:11], v[48:51]\n"
        "v_exp_f32 v40, v40\n"
        "v_mfma_f32_16x16x32_bf16 v[48:51], a[124:127], v[12:15], v[48:51]\n"
        "v_exp_f32 v41, v41\n"
        "v_mfma_f32_16x16x32_bf16 v[160:163], a[144:147], v[80:83], v[160:163]\n"
        "v_exp_f32 v42, v42\n"
        "v_mfma_f32_16x16x32_bf16 v[164:167], a[148:151], v[80:83], v[164:167]\n"
        "v_exp_f32 v43, v43\n"
        "v_mfma_f32_16x16x32_bf16 v[168:171], a[152:155], v[80:83], v[168:171]\n"
        "v_exp_f32 v44, v44\n"
        "v_mfma_f32_16x16x32_bf16 v[172:175], a[156:159], v[80:83], v[172:175]\n"
        "s_waitcnt vmcnt(7) lgkmcnt(0)\n"
        "v_exp_f32 v45, v45\n"
        "v_cvt_pk_bf16_f32 v87, v54, v55\n"
        "v_mfma_f32_16x16x32_bf16 v[52:55], a[124:127], v[76:79], v[228:231]\n"
        "s_barrier\n"
        "v_mfma_f32_16x16x32_bf16 v[52:55], a[120:123], v[72:75], v[52:55]\n"
        "s_add_u32 s82, s82, 1\n"
        "v_exp_f32 v46, v46\n"
        "v_mfma_f32_16x16x32_bf16 v[52:55], a[116:119], v[68:71], v[52:55]\n"
        "s_xor_b32 s78, s78, 0x8000\n"
        "v_exp_f32 v47, v47\n"
        "v_mfma_f32_16x16x32_bf16 v[52:55], a[112:115], v[64:67], v[52:55]\n"
        "s_add_u32 s81, s78, 4096\n"
        "v_exp_f32 v48, v48\n"
        "v_smfmac_f32_16x16x64_bf16 a[64:67], a[72:75], v[80:87], v154\n"
        "global_load_lds_dwordx4 v254, s[74:75] offset:1024 sc1\n"
        "s_mov_b32 s80, s78\n"
        "v_cvt_pk_bf16_f32 v91, v58, v59\n"
        "v_mfma_f32_16x16x32_bf16 v[56:59], a[112:115], v[96:99], v[232:235]\n"
        "v_exp_f32 v49, v49\n"
        "v_mfma_f32_16x16x32_bf16 v[56:59], a[116:119], v[100:103], v[56:59]\n"
        "v_exp_f32 v50, v50\n"
        "v_mfma_f32_16x16x32_bf16 v[56:59], a[120:123], v[104:107], v[56:59]\n"
        "v_exp_f32 v51, v51\n"
        "v_mfma_f32_16x16x32_bf16 v[56:59], a[124:127], v[108:111], v[56:59]\n"
        "v_exp_f32 v52, v52\n"
        "v_mfma_f32_16x16x32_bf16 v[192:195], a[144:147], v[84:87], v[192:195]\n"
        "v_exp_f32 v53, v53\n"
        "v_mfma_f32_16x16x32_bf16 a[0:3], a[144:147], v[88:91], a[0:3]\n"
        "v_exp_f32 v54, v54\n"
        "v_mfma_f32_16x16x32_bf16 a[4:7], a[148:151], v[88:91], a[4:7]\n"
        "v_exp_f32 v55, v55\n"
        "v_mfma_f32_16x16x32_bf16 v[196:199], a[148:151], v[84:87], v[196:199]\n"
        "v_exp_f32 v56, v56\n"
        "v_mfma_f32_16x16x32_bf16 v[200:203], a[152:155], v[84:87], v[200:203]\n"
        "v_exp_f32 v57, v57\n"
        "v_mfma_f32_16x16x32_bf16 a[8:11], a[152:155], v[88:91], a[8:11]\n"
        "v_exp_f32 v58, v58\n"
        "v_mfma_f32_16x16x32_bf16 a[12:15], a[156:159], v[88:91], a[12:15]\n"
        "v_exp_f32 v59, v59\n"
        "v_mfma_f32_16x16x32_bf16 v[204:207], a[156:159], v[84:87], v[204:207]\n"
        "global_load_lds_dwordx4 v254, s[74:75] offset:2048 sc1\n"
        "v_cvt_pk_bf16_f32 v94, v60, v61\n"
        "v_mfma_f32_16x16x32_bf16 v[176:179], a[128:131], v[80:83], v[176:179]\n"
        "s_add_u32 s76, s66, s90\n"
        "s_addc_u32 s77, s67, 0\n"
        "v_cvt_pk_bf16_f32 v95, v62, v63\n"
        "v_mfma_f32_16x16x32_bf16 v[60:63], a[124:127], v[140:143], v[236:239]\n"
        "ds_read_b128 a[124:127], v240 offset:15360\n"
        "v_cvt_pk_bf16_f32 v16, v32, v33\n"
        "v_mfma_f32_16x16x32_bf16 v[60:63], a[120:123], v[136:139], v[60:63]\n"
        "ds_read_b128 a[120:123], v240 offset:14336\n"
        "v_cvt_pk_bf16_f32 v17, v34, v35\n"
        "v_mfma_f32_16x16x32_bf16 v[60:63], a[116:119], v[132:135], v[60:63]\n"
        "ds_read_b128 a[116:119], v240 offset:13312\n"
        "global_load_lds_dwordx4 v254, s[74:75] offset:3072 sc1\n"
        "s_add_u32 m0, s79, 0x0\n"
        "v_mfma_f32_16x16x32_bf16 v[60:63], a[112:115], v[128:131], v[60:63]\n"
        "ds_read_b128 a[112:115], v240 offset:12288\n"
        "v_cvt_pk_bf16_f32 v20, v36, v37\n"
        "v_smfmac_f32_16x16x64_bf16 a[68:71], a[72:75], v[88:95], v154\n"
        "s_cmp_ge_u32 s82, s44\n"
        "s_addc_u32 s90, s82, 0\n"
        "v_cvt_pk_bf16_f32 v21, v38, v39\n"
        "v_mfma_f32_16x16x32_bf16 a[32:35], a[144:147], v[92:95], a[32:35]\n"
        "s_lshl_b32 s90, s90, 15\n"
        "v_cvt_pk_bf16_f32 v24, v40, v41\n"
        "v_xor_b32 v241, 0x8000, v241\n"
        "v_mfma_f32_16x16x32_bf16 a[36:39], a[148:151], v[92:95], a[36:39]\n"
        "ds_read_b128 a[144:147], v241 offset:0\n"
        "global_load_lds_dwordx4 v144, s[76:77] offset:0 sc1\n"
        "v_mfma_f32_16x16x32_bf16 a[40:43], a[152:155], v[92:95], a[40:43]\n"
        "ds_read_b128 a[148:151], v241 offset:1024\n"
        "v_cvt_pk_bf16_f32 v25, v42, v43\n"
        "v_mfma_f32_16x16x32_bf16 a[44:47], a[156:159], v[92:95], a[44:47]\n"
        "ds_read_b128 a[152:155], v241 offset:2048\n"
        "v_cvt_pk_bf16_f32 v28, v44, v45\n"
        "v_mfma_f32_16x16x32_bf16 v[32:35], a[96:99], v[0:3], v[224:227]\n"
        "ds_read_b128 a[156:159], v241 offset:3072\n"
        "v_cvt_pk_bf16_f32 v29, v46, v47\n"
        "v_mfma_f32_16x16x32_bf16 v[32:35], a[100:103], v[4:7], v[32:35]\n"
        "v_exp_f32 v60, v60\n"
        "v_mfma_f32_16x16x32_bf16 v[32:35], a[104:107], v[8:11], v[32:35]\n"
        "v_exp_f32 v61, v61\n"
        "v_mfma_f32_16x16x32_bf16 v[32:35], a[108:111], v[12:15], v[32:35]\n"
        "v_exp_f32 v62, v62\n"
        "v_mfma_f32_16x16x32_bf16 v[36:39], a[108:111], v[76:79], v[228:231]\n"
        "v_exp_f32 v63, v63\n"
        "v_mfma_f32_16x16x32_bf16 v[36:39], a[104:107], v[72:75], v[36:39]\n"
        "global_load_lds_dwordx4 v145, s[76:77] offset:1024 sc1\n"
        "v_cvt_pk_bf16_f32 v18, v48, v49\n"
        "v_mfma_f32_16x16x32_bf16 v[36:39], a[100:103], v[68:71], v[36:39]\n"
        "s_add_u32 s68, s64, s90\n"
        "s_addc_u32 s69, s65, 0\n"
        "v_cvt_pk_bf16_f32 v19, v50, v51\n"
        "v_mfma_f32_16x16x32_bf16 v[36:39], a[96:99], v[64:67], v[36:39]\n"
        "v_exp_f32 v32, v32\n"
        "v_mfma_f32_16x16x32_bf16 v[40:43], a[96:99], v[96:99], v[232:235]\n"
        "v_exp_f32 v33, v33\n"
        "v_mfma_f32_16x16x32_bf16 v[40:43], a[100:103], v[100:103], v[40:43]\n"
        "v_exp_f32 v34, v34\n"
        "v_mfma_f32_16x16x32_bf16 v[40:43], a[104:107], v[104:107], v[40:43]\n"
        "v_exp_f32 v35, v35\n"
        "v_mfma_f32_16x16x32_bf16 v[40:43], a[108:111], v[108:111], v[40:43]\n"
        "v_exp_f32 v36, v36\n"
        "v_mfma_f32_16x16x32_bf16 v[44:47], a[108:111], v[140:143], v[236:239]\n"
        "ds_read_b128 a[108:111], v240 offset:19456\n"
        "global_load_lds_dwordx4 v146, s[76:77] offset:2048 sc1\n"
        "v_mfma_f32_16x16x32_bf16 v[44:47], a[104:107], v[136:139], v[44:47]\n"
        "ds_read_b128 a[104:107], v240 offset:18432\n"
        "v_cvt_pk_bf16_f32 v22, v52, v53\n"
        "v_mfma_f32_16x16x32_bf16 v[44:47], a[100:103], v[132:135], v[44:47]\n"
        "ds_read_b128 a[100:103], v240 offset:17408\n"
        "v_cvt_pk_bf16_f32 v23, v54, v55\n"
        "v_mfma_f32_16x16x32_bf16 v[44:47], a[96:99], v[128:131], v[44:47]\n"
        "ds_read_b128 a[96:99], v240 offset:16384\n"
        "v_cvt_pk_bf16_f32 v26, v56, v57\n"
        "v_mfma_f32_16x16x32_bf16 v[208:211], a[128:131], v[84:87], v[208:211]\n"
        "v_exp_f32 v37, v37\n"
        "v_mfma_f32_16x16x32_bf16 a[16:19], a[128:131], v[88:91], a[16:19]\n"
        "v_exp_f32 v38, v38\n"
        "v_mfma_f32_16x16x32_bf16 a[48:51], a[128:131], v[92:95], a[48:51]\n"
        "ds_read_b128 a[128:131], v241 offset:4096\n"
        "global_load_lds_dwordx4 v147, s[76:77] offset:3072 sc1\n"
        "s_add_u32 m0, s79, s28\n"
        "v_mfma_f32_16x16x32_bf16 a[52:55], a[132:135], v[92:95], a[52:55]\n"
        "v_exp_f32 v39, v39\n"
        "v_mfma_f32_16x16x32_bf16 a[20:23], a[132:135], v[88:91], a[20:23]\n"
        "v_exp_f32 v40, v40\n"
        "v_mfma_f32_16x16x32_bf16 v[212:215], a[132:135], v[84:87], v[212:215]\n"
        "v_exp_f32 v41, v41\n"
        "v_mfma_f32_16x16x32_bf16 v[180:183], a[132:135], v[80:83], v[180:183]\n"
        "ds_read_b128 a[132:135], v241 offset:5120\n"
        "s_add_u32 s76, s76, s25\n"
        "s_addc_u32 s77, s77, 0\n"
        "v_mfma_f32_16x16x32_bf16 v[184:187], a[136:139], v[80:83], v[184:187]\n"
        "v_exp_f32 v42, v42\n"
        "v_mfma_f32_16x16x32_bf16 v[216:219], a[136:139], v[84:87], v[216:219]\n"
        "v_exp_f32 v43, v43\n"
        "v_mfma_f32_16x16x32_bf16 a[24:27], a[136:139], v[88:91], a[24:27]\n"
        "v_exp_f32 v44, v44\n"
        "v_mfma_f32_16x16x32_bf16 a[56:59], a[136:139], v[92:95], a[56:59]\n"
        "ds_read_b128 a[136:139], v241 offset:6144\n"
        "global_load_lds_dwordx4 v144, s[76:77] offset:0 sc1\n"
        "v_mfma_f32_16x16x32_bf16 a[60:63], a[140:143], v[92:95], a[60:63]\n"
        "s_waitcnt lgkmcnt(2)\n"
        "v_exp_f32 v45, v45\n"
        "v_mfma_f32_16x16x32_bf16 a[28:31], a[140:143], v[88:91], a[28:31]\n"
        "v_exp_f32 v46, v46\n"
        "v_mfma_f32_16x16x32_bf16 v[220:223], a[140:143], v[84:87], v[220:223]\n"
        "v_exp_f32 v47, v47\n"
        "v_mfma_f32_16x16x32_bf16 v[188:191], a[140:143], v[80:83], v[188:191]\n"
        "ds_read_b128 a[140:143], v241 offset:7168\n"
        "global_load_lds_dwordx4 v145, s[76:77] offset:1024 sc1\n"
        "v_mfma_f32_16x16x32_bf16 v[48:51], a[112:115], v[0:3], v[224:227]\n"
        "s_add_u32 s74, s68, 0x1000\n"
        "s_addc_u32 s75, s69, 0\n"
        "v_cvt_pk_bf16_f32 v27, v58, v59\n"
        "v_mfma_f32_16x16x32_bf16 v[48:51], a[116:119], v[4:7], v[48:51]\n"
        "v_cvt_pk_bf16_f32 v30, v60, v61\n"
        "v_cvt_pk_bf16_f32 v31, v62, v63\n"
        "v_mfma_f32_16x16x32_bf16 v[48:51], a[120:123], v[8:11], v[48:51]\n"
        "v_cvt_pk_bf16_f32 v80, v32, v33\n"
        "v_cvt_pk_bf16_f32 v81, v34, v35\n"
        "v_mfma_f32_16x16x32_bf16 v[48:51], a[124:127], v[12:15], v[48:51]\n"
        "global_load_lds_dwordx4 v146, s[76:77] offset:2048 sc1\n"
        "v_mfma_f32_16x16x32_bf16 v[52:55], a[124:127], v[76:79], v[228:231]\n"
        "v_cvt_pk_bf16_f32 v84, v36, v37\n"
        "v_cvt_pk_bf16_f32 v85, v38, v39\n"
        "v_mfma_f32_16x16x32_bf16 v[52:55], a[120:123], v[72:75], v[52:55]\n"
        "v_cvt_pk_bf16_f32 v88, v40, v41\n"
        "v_cvt_pk_bf16_f32 v89, v42, v43\n"
        "v_mfma_f32_16x16x32_bf16 v[52:55], a[116:119], v[68:71], v[52:55]\n"
        "v_exp_f32 v48, v48\n"
        "v_mfma_f32_16x16x32_bf16 v[52:55], a[112:115], v[64:67], v[52:55]\n"
        "v_exp_f32 v49, v49\n"
        "v_mfma_f32_16x16x32_bf16 v[56:59], a[112:115], v[96:99], v[232:235]\n"
        "v_exp_f32 v50, v50\n"
        "v_mfma_f32_16x16x32_bf16 v[56:59], a[116:119], v[100:103], v[56:59]\n"
        "v_exp_f32 v51, v51\n"
        "v_mfma_f32_16x16x32_bf16 v[56:59], a[120:123], v[104:107], v[56:59]\n"
        "global_load_lds_dwordx4 v147, s[76:77] offset:3072 sc1\n"
        "v_cvt_pk_bf16_f32 v92, v44, v45\n"
        "v_mfma_f32_16x16x32_bf16 v[56:59], a[124:127], v[108:111], v[56:59]\n"
        "v_exp_f32 v52, v52\n"
        "v_mfma_f32_16x16x32_bf16 v[60:63], a[124:127], v[140:143], v[236:239]\n"
        "ds_read_b128 a[124:127], v240 offset:23552\n"
        "v_cvt_pk_bf16_f32 v93, v46, v47\n"
        "v_mfma_f32_16x16x32_bf16 v[60:63], a[120:123], v[136:139], v[60:63]\n"
        "ds_read_b128 a[120:123], v240 offset:22528\n"
        "v_cvt_pk_bf16_f32 v82, v48, v49\n"
        "v_mfma_f32_16x16x32_bf16 v[60:63], a[116:119], v[132:135], v[60:63]\n"
        "ds_read_b128 a[116:119], v240 offset:21504\n"
        "global_load_dword v244, v245, s[26:27] offset:0\n"
        "v_mfma_f32_16x16x32_bf16 v[60:63], a[112:115], v[128:131], v[60:63]\n"
        "ds_read_b128 a[112:115], v240 offset:20480\n"
        "s_add_u32 s26, s26, 0x20000\n"
        "s_addc_u32 s27, s27, 0\n"
        "v_smfmac_f32_16x16x64_bf16 a[64:67], a[72:75], v[16:23], v154\n"
        "v_cvt_pk_bf16_f32 v83, v50, v51\n"
        "v_smfmac_f32_16x16x64_bf16 a[68:71], a[72:75], v[24:31], v154\n"
        "v_mfma_f32_16x16x32_bf16 v[160:163], a[144:147], v[16:19], v[160:163]\n"
        "v_exp_f32 v53, v53\n"
        "v_mfma_f32_16x16x32_bf16 v[192:195], a[144:147], v[20:23], v[192:195]\n"
        "v_exp_f32 v54, v54\n"
        "v_mfma_f32_16x16x32_bf16 a[0:3], a[144:147], v[24:27], a[0:3]\n"
        "v_exp_f32 v55, v55\n"
        "v_mfma_f32_16x16x32_bf16 a[32:35], a[144:147], v[28:31], a[32:35]\n"
        "ds_read_b128 a[144:147], v241 offset:8192\n"
        "v_cvt_pk_bf16_f32 v86, v52, v53\n"
        "v_mfma_f32_16x16x32_bf16 a[36:39], a[148:151], v[28:31], a[36:39]\n"
        "v_exp_f32 v56, v56\n"
        "v_mfma_f32_16x16x32_bf16 a[4:7], a[148:151], v[24:27], a[4:7]\n"
        "v_exp_f32 v57, v57\n"
        "v_mfma_f32_16x16x32_bf16 v[196:199], a[148:151], v[20:23], v[196:199]\n"
        "v_exp_f32 v58, v58\n"
        "v_mfma_f32_16x16x32_bf16 v[164:167], a[148:151], v[16:19], v[164:167]\n"
        "ds_read_b128 a[148:151], v241 offset:9216\n"
        "v_cvt_pk_bf16_f32 v87, v54, v55\n"
        "v_mfma_f32_16x16x32_bf16 v[168:171], a[152:155], v[16:19], v[168:171]\n"
        "v_exp_f32 v59, v59\n"
        "v_mfma_f32_16x16x32_bf16 v[200:203], a[152:155], v[20:23], v[200:203]\n"
        "v_exp_f32 v60, v60\n"
        "v_mfma_f32_16x16x32_bf16 a[8:11], a[152:155], v[24:27], a[8:11]\n"
        "v_exp_f32 v61, v61\n"
        "v_mfma_f32_16x16x32_bf16 a[40:43], a[152:155], v[28:31], a[40:43]\n"
        "ds_read_b128 a[152:155], v241 offset:10240\n"
        "v_cvt_pk_bf16_f32 v90, v56, v57\n"
        "v_mfma_f32_16x16x32_bf16 a[44:47], a[156:159], v[28:31], a[44:47]\n"
        "v_exp_f32 v62, v62\n"
        "v_mfma_f32_16x16x32_bf16 a[12:15], a[156:159], v[24:27], a[12:15]\n"
        "v_exp_f32 v63, v63\n"
        "v_mfma_f32_16x16x32_bf16 v[204:207], a[156:159], v[20:23], v[204:207]\n"
        "v_cvt_pk_bf16_f32 v91, v58, v59\n"
        "v_cvt_pk_bf16_f32 v94, v60, v61\n"
        "v_mfma_f32_16x16x32_bf16 v[172:175], a[156:159], v[16:19], v[172:175]\n"
        "ds_read_b128 a[156:159], v241 offset:11264\n"
        "v_cvt_pk_bf16_f32 v95, v62, v63\n"
        "v_mfma_f32_16x16x32_bf16 v[32:35], a[96:99], v[0:3], v[224:227]\n"
        "v_mfma_f32_16x16x32_bf16 v[32:35], a[100:103], v[4:7], v[32:35]\n"
        "v_mfma_f32_16x16x32_bf16 v[32:35], a[104:107], v[8:11], v[32:35]\n"
        "v_mfma_f32_16x16x32_bf16 v[32:35], a[108:111], v[12:15], v[32:35]\n"
        "v_mfma_f32_16x16x32_bf16 v[36:39], a[108:111], v[76:79], v[228:231]\n"
        "v_mfma_f32_16x16x32_bf16 v[36:39], a[104:107], v[72:75], v[36:39]\n"
        "v_mfma_f32_16x16x32_bf16 v[36:39], a[100:103], v[68:71], v[36:39]\n"
        "v_mfma_f32_16x16x32_bf16 v[36:39], a[96:99], v[64:67], v[36:39]\n"
        "v_mfma_f32_16x16x32_bf16 v[40:43], a[96:99], v[96:99], v[232:235]\n"
        "v_mfma_f32_16x16x32_bf16 v[40:43], a[100:103], v[100:103], v[40:43]\n"
        "v_mfma_f32_16x16x32_bf16 v[40:43], a[104:107], v[104:107], v[40:43]\n"
        "v_mfma_f32_16x16x32_bf16 v[40:43], a[108:111], v[108:111], v[40:43]\n"
        "v_exp_f32 v32, v32\n"
        "v_mfma_f32_16x16x32_bf16 v[44:47], a[108:111], v[140:143], v[236:239]\n"
        "v_exp_f32 v33, v33\n"
        "v_mfma_f32_16x16x32_bf16 v[44:47], a[104:107], v[136:139], v[44:47]\n"
        "ds_read_b128 a[104:107], v240 offset:26624\n"
        "v_mfma_f32_16x16x32_bf16 v[44:47], a[100:103], v[132:135], v[44:47]\n"
        "ds_read_b128 a[100:103], v240 offset:25600\n"
        "v_mfma_f32_16x16x32_bf16 v[44:47], a[96:99], v[128:131], v[44:47]\n"
        "ds_read_b128 a[96:99], v240 offset:24576\n"
        "v_mfma_f32_16x16x32_bf16 v[176:179], a[128:131], v[16:19], v[176:179]\n"
        "s_waitcnt lgkmcnt(3)\n"
        "v_exp_f32 v34, v34\n"
        "v_mfma_f32_16x16x32_bf16 v[208:211], a[128:131], v[20:23], v[208:211]\n"
        "v_exp_f32 v35, v35\n"
        "v_mfma_f32_16x16x32_bf16 a[16:19], a[128:131], v[24:27], a[16:19]\n"
        "ds_read_b128 a[108:111], v240 offset:27648\n"
        "v_mfma_f32_16x16x32_bf16 a[48:51], a[128:131], v[28:31], a[48:51]\n"
        "ds_read_b128 a[128:131], v241 offset:12288\n"
        "v_mfma_f32_16x16x32_bf16 a[52:55], a[132:135], v[28:31], a[52:55]\n"
        "v_exp_f32 v36, v36\n"
        "v_mfma_f32_16x16x32_bf16 a[20:23], a[132:135], v[24:27], a[20:23]\n"
        "v_exp_f32 v37, v37\n"
        "v_mfma_f32_16x16x32_bf16 v[212:215], a[132:135], v[20:23], v[212:215]\n"
        "v_exp_f32 v38, v38\n"
        "v_mfma_f32_16x16x32_bf16 v[180:183], a[132:135], v[16:19], v[180:183]\n"
        "ds_read_b128 a[132:135], v241 offset:13312\n"
        "v_mfma_f32_16x16x32_bf16 v[184:187], a[136:139], v[16:19], v[184:187]\n"
        "v_exp_f32 v39, v39\n"
        "v_mfma_f32_16x16x32_bf16 v[216:219], a[136:139], v[20:23], v[216:219]\n"
        "v_exp_f32 v40, v40\n"
        "v_mfma_f32_16x16x32_bf16 a[24:27], a[136:139], v[24:27], a[24:27]\n"
        "v_exp_f32 v41, v41\n"
        "v_mfma_f32_16x16x32_bf16 a[56:59], a[136:139], v[28:31], a[56:59]\n"
        "ds_read_b128 a[136:139], v241 offset:14336\n"
        "v_mfma_f32_16x16x32_bf16 a[60:63], a[140:143], v[28:31], a[60:63]\n"
        "v_exp_f32 v42, v42\n"
        "v_mfma_f32_16x16x32_bf16 a[28:31], a[140:143], v[24:27], a[28:31]\n"
        "v_exp_f32 v43, v43\n"
        "v_mfma_f32_16x16x32_bf16 v[220:223], a[140:143], v[20:23], v[220:223]\n"
        "v_exp_f32 v44, v44\n"
        "v_mfma_f32_16x16x32_bf16 v[188:191], a[140:143], v[16:19], v[188:191]\n"
        "ds_read_b128 a[140:143], v241 offset:15360\n"
        "global_load_dword v244, v245, s[26:27] offset:0\n"
        "v_mfma_f32_16x16x32_bf16 v[48:51], a[112:115], v[0:3], v[224:227]\n"
        "v_exp_f32 v45, v45\n"
        "v_mfma_f32_16x16x32_bf16 v[48:51], a[116:119], v[4:7], v[48:51]\n"
        "v_exp_f32 v46, v46\n"
        "v_mfma_f32_16x16x32_bf16 v[48:51], a[120:123], v[8:11], v[48:51]\n"
        "v_exp_f32 v47, v47\n"
        "v_mfma_f32_16x16x32_bf16 v[48:51], a[124:127], v[12:15], v[48:51]\n"
        "s_add_u32 s26, s26, 0x20000\n"
        "s_addc_u32 s27, s27, 0\n"
        "v_cvt_pk_bf16_f32 v16, v32, v33\n"
        "v_mfma_f32_16x16x32_bf16 v[52:55], a[124:127], v[76:79], v[228:231]\n"
        "v_cvt_pk_bf16_f32 v17, v34, v35\n"
        "v_cvt_pk_bf16_f32 v20, v36, v37\n"
        "v_mfma_f32_16x16x32_bf16 v[52:55], a[120:123], v[72:75], v[52:55]\n"
        "v_cvt_pk_bf16_f32 v21, v38, v39\n"
        "v_cvt_pk_bf16_f32 v24, v40, v41\n"
        "v_mfma_f32_16x16x32_bf16 v[52:55], a[116:119], v[68:71], v[52:55]\n"
        "v_exp_f32 v48, v48\n"
        "v_mfma_f32_16x16x32_bf16 v[52:55], a[112:115], v[64:67], v[52:55]\n"
        "v_exp_f32 v49, v49\n"
        "v_mfma_f32_16x16x32_bf16 v[56:59], a[112:115], v[96:99], v[232:235]\n"
        "v_exp_f32 v50, v50\n"
        "v_mfma_f32_16x16x32_bf16 v[56:59], a[116:119], v[100:103], v[56:59]\n"
        "v_exp_f32 v51, v51\n"
        "v_mfma_f32_16x16x32_bf16 v[56:59], a[120:123], v[104:107], v[56:59]\n"
        "v_cvt_pk_bf16_f32 v25, v42, v43\n"
        "v_cvt_pk_bf16_f32 v28, v44, v45\n"
        "v_mfma_f32_16x16x32_bf16 v[56:59], a[124:127], v[108:111], v[56:59]\n"
        "v_exp_f32 v52, v52\n"
        "v_mfma_f32_16x16x32_bf16 v[60:63], a[124:127], v[140:143], v[236:239]\n"
        "ds_read_b128 a[124:127], v240 offset:31744\n"
        "v_cvt_pk_bf16_f32 v29, v46, v47\n"
        "v_mfma_f32_16x16x32_bf16 v[60:63], a[120:123], v[136:139], v[60:63]\n"
        "ds_read_b128 a[120:123], v240 offset:30720\n"
        "v_cvt_pk_bf16_f32 v18, v48, v49\n"
        "v_mfma_f32_16x16x32_bf16 v[60:63], a[116:119], v[132:135], v[60:63]\n"
        "ds_read_b128 a[116:119], v240 offset:29696\n"
        "v_cvt_pk_bf16_f32 v19, v50, v51\n"
        "v_mfma_f32_16x16x32_bf16 v[60:63], a[112:115], v[128:131], v[60:63]\n"
        "ds_read_b128 a[112:115], v240 offset:28672\n"
        "v_xor_b32 v240, 0x8000, v240\n"
        "v_smfmac_f32_16x16x64_bf16 a[64:67], a[72:75], v[80:87], v154\n"
        "v_smfmac_f32_16x16x64_bf16 a[68:71], a[72:75], v[88:95], v154\n"
        "v_mfma_f32_16x16x32_bf16 v[160:163], a[144:147], v[80:83], v[160:163]\n"
        "v_exp_f32 v53, v53\n"
        "v_mfma_f32_16x16x32_bf16 v[192:195], a[144:147], v[84:87], v[192:195]\n"
        "v_exp_f32 v54, v54\n"
        "v_mfma_f32_16x16x32_bf16 a[0:3], a[144:147], v[88:91], a[0:3]\n"
        "v_exp_f32 v55, v55\n"
        "v_mfma_f32_16x16x32_bf16 a[32:35], a[144:147], v[92:95], a[32:35]\n"
        "ds_read_b128 a[144:147], v241 offset:16384\n"
        "v_cvt_pk_bf16_f32 v22, v52, v53\n"
        "v_mfma_f32_16x16x32_bf16 a[36:39], a[148:151], v[92:95], a[36:39]\n"
        "v_exp_f32 v56, v56\n"
        "v_mfma_f32_16x16x32_bf16 a[4:7], a[148:151], v[88:91], a[4:7]\n"
        "s_waitcnt vmcnt(10) lgkmcnt(1)\n"
        "v_exp_f32 v57, v57\n"
        "v_mfma_f32_16x16x32_bf16 v[196:199], a[148:151], v[84:87], v[196:199]\n"
        "s_barrier\n"
        "v_mfma_f32_16x16x32_bf16 v[164:167], a[148:151], v[80:83], v[164:167]\n"
        "ds_read_b128 a[148:151], v241 offset:17408\n"
        "v_cvt_pk_bf16_f32 v23, v54, v55\n"
        "v_mfma_f32_16x16x32_bf16 v[168:171], a[152:155], v[80:83], v[168:171]\n"
        "v_exp_f32 v58, v58\n"
        "v_mfma_f32_16x16x32_bf16 v[200:203], a[152:155], v[84:87], v[200:203]\n"
        "v_exp_f32 v59, v59\n"
        "v_mfma_f32_16x16x32_bf16 a[8:11], a[152:155], v[88:91], a[8:11]\n"
        "v_exp_f32 v60, v60\n"
        "v_mfma_f32_16x16x32_bf16 a[40:43], a[152:155], v[92:95], a[40:43]\n"
        "ds_read_b128 a[152:155], v241 offset:18432\n"
        "v_cvt_pk_bf16_f32 v26, v56, v57\n"
        "v_mfma_f32_16x16x32_bf16 a[44:47], a[156:159], v[92:95], a[44:47]\n"
        "v_exp_f32 v61, v61\n"
        "v_mfma_f32_16x16x32_bf16 a[12:15], a[156:159], v[88:91], a[12:15]\n"
        "v_exp_f32 v62, v62\n"
        "v_mfma_f32_16x16x32_bf16 v[204:207], a[156:159], v[84:87], v[204:207]\n"
        "v_exp_f32 v63, v63\n"
        "v_mfma_f32_16x16x32_bf16 v[172:175], a[156:159], v[80:83], v[172:175]\n"
        "ds_read_b128 a[156:159], v241 offset:19456\n"
        "v_cvt_pk_bf16_f32 v27, v58, v59\n"
        "v_mfma_f32_16x16x32_bf16 v[32:35], a[96:99], v[0:3], v[224:227]\n"
        "v_cvt_pk_bf16_f32 v30, v60, v61\n"
        "v_cvt_pk_bf16_f32 v31, v62, v63\n"
        "v_mfma_f32_16x16x32_bf16 v[32:35], a[100:103], v[4:7], v[32:35]\n"
        "v_mfma_f32_16x16x32_bf16 v[32:35], a[104:107], v[8:11], v[32:35]\n"
        "v_mfma_f32_16x16x32_bf16 v[32:35], a[108:111], v[12:15], v[32:35]\n"
        "v_mfma_f32_16x16x32_bf16 v[36:39], a[108:111], v[76:79], v[228:231]\n"
        "v_mfma_f32_16x16x32_bf16 v[36:39], a[104:107], v[72:75], v[36:39]\n"
        "v_mfma_f32_16x16x32_bf16 v[36:39], a[100:103], v[68:71], v[36:39]\n"
        "v_mfma_f32_16x16x32_bf16 v[36:39], a[96:99], v[64:67], v[36:39]\n"
        "v_mfma_f32_16x16x32_bf16 v[40:43], a[96:99], v[96:99], v[232:235]\n"
        "v_mfma_f32_16x16x32_bf16 v[40:43], a[100:103], v[100:103], v[40:43]\n"
        "v_mfma_f32_16x16x32_bf16 v[40:43], a[104:107], v[104:107], v[40:43]\n"
        "v_mfma_f32_16x16x32_bf16 v[40:43], a[108:111], v[108:111], v[40:43]\n"
        "v_exp_f32 v32, v32\n"
        "v_mfma_f32_16x16x32_bf16 v[44:47], a[108:111], v[140:143], v[236:239]\n"
        "v_exp_f32 v33, v33\n"
        "v_mfma_f32_16x16x32_bf16 v[44:47], a[104:107], v[136:139], v[44:47]\n"
        "ds_read_b128 a[104:107], v240 offset:2048\n"
        "v_mfma_f32_16x16x32_bf16 v[44:47], a[100:103], v[132:135], v[44:47]\n"
        "ds_read_b128 a[100:103], v240 offset:1024\n"
        "v_mfma_f32_16x16x32_bf16 v[44:47], a[96:99], v[128:131], v[44:47]\n"
        "ds_read_b128 a[96:99], v240 offset:0\n"
        "v_mfma_f32_16x16x32_bf16 v[176:179], a[128:131], v[80:83], v[176:179]\n"
        "v_exp_f32 v34, v34\n"
        "v_mfma_f32_16x16x32_bf16 v[208:211], a[128:131], v[84:87], v[208:211]\n"
        "v_exp_f32 v35, v35\n"
        "v_mfma_f32_16x16x32_bf16 a[16:19], a[128:131], v[88:91], a[16:19]\n"
        "ds_read_b128 a[108:111], v240 offset:3072\n"
        "v_mfma_f32_16x16x32_bf16 a[48:51], a[128:131], v[92:95], a[48:51]\n"
        "ds_read_b128 a[128:131], v241 offset:20480\n"
        "v_mfma_f32_16x16x32_bf16 a[52:55], a[132:135], v[92:95], a[52:55]\n"
        "v_exp_f32 v36, v36\n"
        "v_mfma_f32_16x16x32_bf16 a[20:23], a[132:135], v[88:91], a[20:23]\n"
        "v_exp_f32 v37, v37\n"
        "v_mfma_f32_16x16x32_bf16 v[212:215], a[132:135], v[84:87], v[212:215]\n"
        "v_exp_f32 v38, v38\n"
        "v_mfma_f32_16x16x32_bf16 v[180:183], a[132:135], v[80:83], v[180:183]\n"
        "ds_read_b128 a[132:135], v241 offset:21504\n"
        "v_mfma_f32_16x16x32_bf16 v[184:187], a[136:139], v[80:83], v[184:187]\n"
        "v_exp_f32 v39, v39\n"
        "v_mfma_f32_16x16x32_bf16 v[216:219], a[136:139], v[84:87], v[216:219]\n"
        "v_exp_f32 v40, v40\n"
        "v_mfma_f32_16x16x32_bf16 a[24:27], a[136:139], v[88:91], a[24:27]\n"
        "v_exp_f32 v41, v41\n"
        "v_mfma_f32_16x16x32_bf16 a[56:59], a[136:139], v[92:95], a[56:59]\n"
        "ds_read_b128 a[136:139], v241 offset:22528\n"
        "s_waitcnt lgkmcnt(2)\n"
        "v_mfma_f32_16x16x32_bf16 a[60:63], a[140:143], v[92:95], a[60:63]\n"
        "v_exp_f32 v42, v42\n"
        "v_mfma_f32_16x16x32_bf16 a[28:31], a[140:143], v[88:91], a[28:31]\n"
        "v_exp_f32 v43, v43\n"
        "v_mfma_f32_16x16x32_bf16 v[220:223], a[140:143], v[84:87], v[220:223]\n"
        "v_exp_f32 v44, v44\n"
        "v_mfma_f32_16x16x32_bf16 v[188:191], a[140:143], v[80:83], v[188:191]\n"
        "ds_read_b128 a[140:143], v241 offset:23552\n"
        "v_cvt_pk_bf16_f32 v80, v32, v33\n"
        "v_mfma_f32_16x16x32_bf16 v[48:51], a[112:115], v[0:3], v[224:227]\n"
        "v_exp_f32 v45, v45\n"
        "v_mfma_f32_16x16x32_bf16 v[48:51], a[116:119], v[4:7], v[48:51]\n"
        "v_exp_f32 v46, v46\n"
        "v_mfma_f32_16x16x32_bf16 v[48:51], a[120:123], v[8:11], v[48:51]\n"
        "v_exp_f32 v47, v47\n"
        "v_mfma_f32_16x16x32_bf16 v[48:51], a[124:127], v[12:15], v[48:51]\n"
        "v_cvt_pk_bf16_f32 v81, v34, v35\n"
        "v_cvt_pk_bf16_f32 v84, v36, v37\n"
        "v_mfma_f32_16x16x32_bf16 v[52:55], a[124:127], v[76:79], v[228:231]\n"
        "v_cvt_pk_bf16_f32 v85, v38, v39\n"
        "v_cvt_pk_bf16_f32 v88, v40, v41\n"
        "v_mfma_f32_16x16x32_bf16 v[52:55], a[120:123], v[72:75], v[52:55]\n"
        "v_cvt_pk_bf16_f32 v89, v42, v43\n"
        "v_cvt_pk_bf16_f32 v92, v44, v45\n"
        "v_mfma_f32_16x16x32_bf16 v[52:55], a[116:119], v[68:71], v[52:55]\n"
        "v_cvt_pk_bf16_f32 v93, v46, v47\n"
        "v_mfma_f32_16x16x32_bf16 v[52:55], a[112:115], v[64:67], v[52:55]\n"
        "v_mfma_f32_16x16x32_bf16 v[56:59], a[112:115], v[96:99], v[232:235]\n"
        "v_mfma_f32_16x16x32_bf16 v[56:59], a[116:119], v[100:103], v[56:59]\n"
        "v_mfma_f32_16x16x32_bf16 v[56:59], a[120:123], v[104:107], v[56:59]\n"
        "v_mfma_f32_16x16x32_bf16 v[56:59], a[124:127], v[108:111], v[56:59]\n"
        "v_mfma_f32_16x16x32_bf16 v[60:63], a[124:127], v[140:143], v[236:239]\n"
        "v_mfma_f32_16x16x32_bf16 v[60:63], a[120:123], v[136:139], v[60:63]\n"
        "ds_read_b128 a[120:123], v240 offset:6144\n"
        "v_mfma_f32_16x16x32_bf16 v[60:63], a[116:119], v[132:135], v[60:63]\n"
        "ds_read_b128 a[116:119], v240 offset:5120\n"
        "v_mfma_f32_16x16x32_bf16 v[60:63], a[112:115], v[128:131], v[60:63]\n"
        "ds_read_b128 a[112:115], v240 offset:4096\n"
        "v_smfmac_f32_16x16x64_bf16 a[64:67], a[72:75], v[16:23], v154\n"
        "v_smfmac_f32_16x16x64_bf16 a[68:71], a[72:75], v[24:31], v154\n"
        "v_mfma_f32_16x16x32_bf16 v[160:163], a[144:147], v[16:19], v[160:163]\n"
        "v_mfma_f32_16x16x32_bf16 v[192:195], a[144:147], v[20:23], v[192:195]\n"
        "v_mfma_f32_16x16x32_bf16 a[0:3], a[144:147], v[24:27], a[0:3]\n"
        "v_mfma_f32_16x16x32_bf16 a[32:35], a[144:147], v[28:31], a[32:35]\n"
        "ds_read_b128 a[144:147], v241 offset:24576\n"
        "v_mfma_f32_16x16x32_bf16 a[36:39], a[148:151], v[28:31], a[36:39]\n"
        "v_mfma_f32_16x16x32_bf16 a[4:7], a[148:151], v[24:27], a[4:7]\n"
        "v_mfma_f32_16x16x32_bf16 v[196:199], a[148:151], v[20:23], v[196:199]\n"
        "v_mfma_f32_16x16x32_bf16 v[164:167], a[148:151], v[16:19], v[164:167]\n"
        "ds_read_b128 a[148:151], v241 offset:25600\n"
        "v_mfma_f32_16x16x32_bf16 v[168:171], a[152:155], v[16:19], v[168:171]\n"
        "v_mfma_f32_16x16x32_bf16 v[200:203], a[152:155], v[20:23], v[200:203]\n"
        "v_mfma_f32_16x16x32_bf16 a[8:11], a[152:155], v[24:27], a[8:11]\n"
        "v_mfma_f32_16x16x32_bf16 a[40:43], a[152:155], v[28:31], a[40:43]\n"
        "ds_read_b128 a[152:155], v241 offset:26624\n"
        "v_mfma_f32_16x16x32_bf16 a[44:47], a[156:159], v[28:31], a[44:47]\n"
        "v_mfma_f32_16x16x32_bf16 a[12:15], a[156:159], v[24:27], a[12:15]\n"
        "v_mfma_f32_16x16x32_bf16 v[204:207], a[156:159], v[20:23], v[204:207]\n"
        "s_cmp_lg_u32 s82, s24\n"
        "v_mfma_f32_16x16x32_bf16 v[172:175], a[156:159], v[16:19], v[172:175]\n"
        "s_cbranch_scc1 LDU_%=\n"
        "s_sub_u32 s97, s83, s82\n"
        "s_cmp_gt_u32 s97, 1\n"
        "s_cbranch_scc1 LLD_%=\n"
        "s_cmp_eq_u32 s97, 0\n"
        "s_cbranch_scc1 LE3D_%=\n"
        "global_load_dwordx2 v[252:253], v155, s[48:49] sc1\n"
        "v_accvgpr_read_b32 v246, a93\n"
        "global_load_dword v244, v246, s[46:47]\n"
        "s_branch LEWD_%=\n"
        "LE3D_%=:\n"
        "v_cmp_ne_u64 vcc, v[252:253], v[250:251]\n"
        "s_mov_b64 s[68:69], s[92:93]\n"
        "s_mov_b64 s[74:75], s[94:95]\n"
        "LHC0_%=:\n"
        "s_cbranch_execz LHRC_%=\n"
        "s_mov_b32 s80, s50\n"
        "s_mov_b32 s81, s51\n"
        "s_mov_b64 s[66:67], s[22:23]\n"
        "s_mov_b32 s90, s29\n"
        "s_mov_b32 s25, s30\n"
        "s_lshl_b32 s28, s28, 2\n"
        "s_xor_b32 s79, s73, 0x8000\n"
        "v_accvgpr_read_b32 v144, a84\n"
        "v_accvgpr_read_b32 v145, a85\n"
        "v_accvgpr_read_b32 v146, a86\n"
        "v_accvgpr_read_b32 v147, a87\n"
        "s_mov_b64 exec, 15\n"
        "ds_write_b128 v156, a[88:91]\n"
        "s_mov_b64 exec, -1\n"
        "s_cbranch_vccnz LE4D_%=\n"
        "LEWD_%=:\n"
        "s_add_u32 s24, s82, 1\n"
        "s_cmp_lg_u32 s82, s91\n"
        "s_cbranch_scc1 LDU_%=\n"
        "; ---- LDRAIN\n"
        "LLD_%=:\n"
        "v_mfma_f32_16x16x32_bf16 a[16:19], a[128:131], v[24:27], a[16:19]\n"
        "s_waitcnt lgkmcnt(4)\n"
        "v_exp_f32 v62, v62\n"
        "v_mfma_f32_16x16x32_bf16 a[48:51], a[128:131], v[28:31], a[48:51]\n"
        "s_mul_i32 s53, s44, 1052672\n"
        "v_exp_f32 v63, v63\n"
        "v_mfma_f32_16x16x32_bf16 a[52:55], a[132:135], v[28:31], a[52:55]\n"
        "ds_read_b128 a[172:175], v241 offset:27648\n"
        "s_lshl_b32 s96, s43, 15\n"
        "v_cvt_pk_bf16_f32 v95, v62, v63\n"
        "v_mfma_f32_16x16x32_bf16 a[20:23], a[132:135], v[24:27], a[20:23]\n"
        "s_add_u32 s53, s53, s96\n"
        "v_exp_f32 v59, v59\n"
        "v_mfma_f32_16x16x32_bf16 a[24:27], a[136:139], v[24:27], a[24:27]\n"
        "s_lshl_b32 s96, s40, 10\n"
        "v_exp_f32 v58, v58\n"
        "v_mfma_f32_16x16x32_bf16 a[56:59], a[136:139], v[28:31], a[56:59]\n"
        "ds_read_b128 a[176:179], v241 offset:28672\n"
        "s_add_u32 s53, s53, s96\n"
        "v_cvt_pk_bf16_f32 v91, v58, v59\n"
        "v_mfma_f32_16x16x32_bf16 a[60:63], a[140:143], v[28:31], a[60:63]\n"
        "s_add_u32 s53, s53, 0x2201000\n"
        "v_exp_f32 v57, v57\n"
        "v_mfma_f32_16x16x32_bf16 a[28:31], a[140:143], v[24:27], a[28:31]\n"
        "v_exp_f32 v56, v56\n"
        "v_exp_f32 v61, v61\n"
        "v_cvt_pk_bf16_f32 v90, v56, v57\n"
        "s_nop 1\n"
        "s_waitcnt lgkmcnt(4)\n"
        "v_mfma_f32_16x16x32_bf16 a[0:3], a[144:147], v[88:91], a[0:3]\n"
        "s_waitcnt lgkmcnt(2)\n"
        "v_exp_f32 v60, v60\n"
        "v_mfma_f32_16x16x32_bf16 a[4:7], a[148:151], v[88:91], a[4:7]\n"
        "ds_read_b128 a[180:183], v241 offset:29696\n"
        "s_sub_u32 s24, s83, 1\n"
        "v_cvt_pk_bf16_f32 v94, v60, v61\n"
        "s_nop 1\n"
        "v_smfmac_f32_16x16x64_bf16 a[68:71], a[72:75], v[88:95], v154\n"
        "ds_read_b128 a[184:187], v241 offset:30720\n"
        "s_add_u32 s70, s38, s53\n"
        "s_addc_u32 s71, s39, 0\n"
        "v_mfma_f32_16x16x32_bf16 a[32:35], a[144:147], v[92:95], a[32:35]\n"
        "ds_read_b128 a[188:191], v241 offset:31744\n"
        "s_add_u32 s91, s82, 1\n"
        "v_accvgpr_read_b32 v120, a0\n"
        "v_mfma_f32_16x16x32_bf16 a[36:39], a[148:151], v[92:95], a[36:39]\n"
        "s_waitcnt lgkmcnt(3)\n"
        "v_accvgpr_read_b32 v248, a68\n"
        "v_accvgpr_read_b32 v246, a69\n"
        "v_mfma_f32_16x16x32_bf16 a[8:11], a[152:155], v[88:91], a[8:11]\n"
        "s_max_u32 s24, s24, s91\n"
        "v_add_f32 v248, v248, v246\n"
        "v_accvgpr_read_b32 v249, a70\n"
        "v_mfma_f32_16x16x32_bf16 a[40:43], a[152:155], v[92:95], a[40:43]\n"
        "s_add_u32 s91, s83, 1\n"
        "v_rcp_f32 v246, v248\n"
        "v_mfma_f32_16x16x32_bf16 a[44:47], a[172:175], v[92:95], a[44:47]\n"
        "v_accvgpr_read_b32 v247, a71\n"
        "v_add_f32 v249, v249, v247\n"
        "v_mfma_f32_16x16x32_bf16 a[12:15], a[172:175], v[88:91], a[12:15]\n"
        "v_rcp_f32 v247, v249\n"
        "v_mfma_f32_16x16x32_bf16 a[16:19], a[176:179], v[88:91], a[16:19]\n"
        "s_waitcnt lgkmcnt(2)\n"
        "v_accvgpr_read_b32 v112, a32\n"
        "v_accvgpr_read_b32 v113, a33\n"
        "v_mfma_f32_16x16x32_bf16 a[48:51], a[176:179], v[92:95], a[48:51]\n"
        "s_waitcnt lgkmcnt(1)\n"
        "v_accvgpr_read_b32 v114, a34\n"
        "v_accvgpr_read_b32 v115, a35\n"
        "v_mfma_f32_16x16x32_bf16 a[52:55], a[180:183], v[92:95], a[52:55]\n"
        "s_waitcnt lgkmcnt(0)\n"
        "v_accvgpr_read_b32 v116, a36\n"
        "v_accvgpr_read_b32 v117, a37\n"
        "v_mfma_f32_16x16x32_bf16 a[20:23], a[180:183], v[88:91], a[20:23]\n"
        "v_accvgpr_read_b32 v118, a38\n"
        "v_accvgpr_read_b32 v119, a39\n"
        "v_mfma_f32_16x16x32_bf16 a[24:27], a[184:187], v[88:91], a[24:27]\n"
        "v_accvgpr_read_b32 v121, a1\n"
        "v_accvgpr_read_b32 v122, a2\n"
        "v_mfma_f32_16x16x32_bf16 a[56:59], a[184:187], v[92:95], a[56:59]\n"
        "v_accvgpr_read_b32 v123, a3\n"
        "v_accvgpr_read_b32 v124, a4\n"
        "v_mfma_f32_16x16x32_bf16 a[60:63], a[188:191], v[92:95], a[60:63]\n"
        "v_accvgpr_read_b32 v125, a5\n"
        "v_accvgpr_read_b32 v126, a6\n"
        "v_mfma_f32_16x16x32_bf16 a[28:31], a[188:191], v[88:91], a[28:31]\n"
        "v_pk_mul_f32 v[112:113], v[112:113], v[246:247] op_sel:[0,1] op_sel_hi:[1,1]\n"
        "v_pk_mul_f32 v[114:115], v[114:115], v[246:247] op_sel:[0,1] op_sel_hi:[1,1]\n"
        "v_pk_mul_f32 v[116:117], v[116:117], v[246:247] op_sel:[0,1] op_sel_hi:[1,1]\n"
        "v_pk_mul_f32 v[118:119], v[118:119], v[246:247] op_sel:[0,1] op_sel_hi:[1,1]\n"
        "v_cvt_pk_bf16_f32 v112, v112, v113\n"
        "v_cvt_pk_bf16_f32 v113, v114, v115\n"
        "v_cvt_pk_bf16_f32 v114, v116, v117\n"
        "v_cvt_pk_bf16_f32 v115, v118, v119\n"
        "global_store_dwordx4 v254, v[112:115], s[70:71] offset:-4096 sc1\n"
        "v_accvgpr_read_b32 v127, a7\n"
        "v_pk_mul_f32 v[120:121], v[120:121], v[246:247] op_sel:[0,0] op_sel_hi:[1,0]\n"
        "v_pk_mul_f32 v[122:123], v[122:123], v[246:247] op_sel:[0,0] op_sel_hi:[1,0]\n"
        "v_pk_mul_f32 v[124:125], v[124:125], v[246:247] op_sel:[0,0] op_sel_hi:[1,0]\n"
        "v_pk_mul_f32 v[126:127], v[126:127], v[246:247] op_sel:[0,0] op_sel_hi:[1,0]\n"
        "v_cvt_pk_bf16_f32 v120, v120, v121\n"
        "v_cvt_pk_bf16_f32 v121, v122, v123\n"
        "v_cvt_pk_bf16_f32 v122, v124, v125\n"
        "v_cvt_pk_bf16_f32 v123, v126, v127\n"
        "global_store_dwordx4 v254, v[120:123], s[70:71] offset:0 sc1\n"
        "s_add_u32 s70, s70, 0x2000\n"
        "s_addc_u32 s71, s71, 0\n"
        "v_accvgpr_read_b32 v40, a40\n"
        "v_accvgpr_read_b32 v41, a41\n"
        "v_accvgpr_read_b32 v42, a42\n"
        "v_accvgpr_read_b32 v43, a43\n"
        "v_accvgpr_read_b32 v44, a44\n"
        "v_accvgpr_read_b32 v45, a45\n"
        "v_accvgpr_read_b32 v46, a46\n"
        "v_accvgpr_read_b32 v47, a47\n"
        "v_pk_mul_f32 v[40:41], v[40:41], v[246:247] op_sel:[0,1] op_sel_hi:[1,1]\n"
        "v_pk_mul_f32 v[42:43], v[42:43], v[246:247] op_sel:[0,1] op_sel_hi:[1,1]\n"
        "v_pk_mul_f32 v[44:45], v[44:45], v[246:247] op_sel:[0,1] op_sel_hi:[1,1]\n"
        "v_pk_mul_f32 v[46:47], v[46:47], v[246:247] op_sel:[0,1] op_sel_hi:[1,1]\n"
        "v_cvt_pk_bf16_f32 v40, v40, v41\n"
        "v_cvt_pk_bf16_f32 v41, v42, v43\n"
        "v_cvt_pk_bf16_f32 v42, v44, v45\n"
        "v_cvt_pk_bf16_f32 v43, v46, v47\n"
        "global_store_dwordx4 v254, v[40:43], s[70:71] offset:-4096 sc1\n"
        "v_accvgpr_read_b32 v56, a8\n"
        "v_accvgpr_read_b32 v57, a9\n"
        "v_accvgpr_read_b32 v58, a10\n"
        "v_accvgpr_read_b32 v59, a11\n"
        "v_accvgpr_read_b32 v60, a12\n"
        "v_accvgpr_read_b32 v61, a13\n"
        "v_accvgpr_read_b32 v62, a14\n"
        "v_accvgpr_read_b32 v63, a15\n"
        "v_pk_mul_f32 v[56:57], v[56:57], v[246:247] op_sel:[0,0] op_sel_hi:[1,0]\n"
        "v_pk_mul_f32 v[58:59], v[58:59], v[246:247] op_sel:[0,0] op_sel_hi:[1,0]\n"
        "v_pk_mul_f32 v[60:61], v[60:61], v[246:247] op_sel:[0,0] op_sel_hi:[1,0]\n"
        "v_pk_mul_f32 v[62:63], v[62:63], v[246:247] op_sel:[0,0] op_sel_hi:[1,0]\n"
        "v_cvt_pk_bf16_f32 v56, v56, v57\n"
        "v_cvt_pk_bf16_f32 v57, v58, v59\n"
        "v_cvt_pk_bf16_f32 v58, v60, v61\n"
        "v_cvt_pk_bf16_f32 v59, v62, v63\n"
        "global_store_dwordx4 v254, v[56:59], s[70:71] offset:0 sc1\n"
        "s_add_u32 s70, s70, 0x2000\n"
        "s_addc_u32 s71, s71, 0\n"
        "v_accvgpr_read_b32 v112, a48\n"
        "v_accvgpr_read_b32 v113, a49\n"
        "v_accvgpr_read_b32 v114, a50\n"
        "v_accvgpr_read_b32 v115, a51\n"
        "v_accvgpr_read_b32 v116, a52\n"
        "v_accvgpr_read_b32 v117, a53\n"
        "v_accvgpr_read_b32 v118, a54\n"
        "v_accvgpr_read_b32 v119, a55\n"
        "v_pk_mul_f32 v[112:113], v[112:113], v[246:247] op_sel:[0,1] op_sel_hi:[1,1]\n"
        "v_pk_mul_f32 v[114:115], v[114:115], v[246:247] op_sel:[0,1] op_sel_hi:[1,1]\n"
        "v_pk_mul_f32 v[116:117], v[116:117], v[246:247] op_sel:[0,1] op_sel_hi:[1,1]\n"
        "v_pk_mul_f32 v[118:119], v[118:119], v[246:247] op_sel:[0,1] op_sel_hi:[1,1]\n"
        "v_cvt_pk_bf16_f32 v112, v112, v113\n"
        "v_cvt_pk_bf16_f32 v113, v114, v115\n"
        "v_cvt_pk_bf16_f32 v114, v116, v117\n"
        "v_cvt_pk_bf16_f32 v115, v118, v119\n"
        "global_store_dwordx4 v254, v[112:115], s[70:71] offset:-4096 sc1\n"
        "v_accvgpr_read_b32 v120, a16\n"
        "v_accvgpr_read_b32 v121, a17\n"
        "v_accvgpr_read_b32 v122, a18\n"
        "v_accvgpr_read_b32 v123, a19\n"
        "v_accvgpr_read_b32 v124, a20\n"
        "v_accvgpr_read_b32 v125, a21\n"
        "v_accvgpr_read_b32 v126, a22\n"
        "v_accvgpr_read_b32 v127, a23\n"
        "v_pk_mul_f32 v[120:121], v[120:121], v[246:247] op_sel:[0,0] op_sel_hi:[1,0]\n"
        "v_pk_mul_f32 v[122:123], v[122:123], v[246:247] op_sel:[0,0] op_sel_hi:[1,0]\n"
        "v_pk_mul_f32 v[124:125], v[124:125], v[246:247] op_sel:[0,0] op_sel_hi:[1,0]\n"
        "v_pk_mul_f32 v[126:127], v[126:127], v[246:247] op_sel:[0,0] op_sel_hi:[1,0]\n"
        "v_cvt_pk_bf16_f32 v120, v120, v121\n"
        "v_cvt_pk_bf16_f32 v121, v122, v123\n"
        "v_cvt_pk_bf16_f32 v122, v124, v125\n"
        "v_cvt_pk_bf16_f32 v123, v126, v127\n"
        "global_store_dwordx4 v254, v[120:123], s[70:71] offset:0 sc1\n"
        "s_add_u32 s70, s70, 0x2000\n"
        "s_addc_u32 s71, s71, 0\n"
        "v_accvgpr_read_b32 v40, a56\n"
        "v_accvgpr_read_b32 v41, a57\n"
        "v_accvgpr_read_b32 v42, a58\n"
        "v_accvgpr_read_b32 v43, a59\n"
        "v_accvgpr_read_b32 v44, a60\n"
        "v_accvgpr_read_b32 v45, a61\n"
        "v_accvgpr_read_b32 v46, a62\n"
        "v_accvgpr_read_b32 v47, a63\n"
        "v_pk_mul_f32 v[40:41], v[40:41], v[246:247] op_sel:[0,1] op_sel_hi:[1,1]\n"
        "v_pk_mul_f32 v[42:43], v[42:43], v[246:247] op_sel:[0,1] op_sel_hi:[1,1]\n"
        "v_pk_mul_f32 v[44:45], v[44:45], v[246:247] op_sel:[0,1] op_sel_hi:[1,1]\n"
        "v_pk_mul_f32 v[46:47], v[46:47], v[246:247] op_sel:[0,1] op_sel_hi:[1,1]\n"
        "v_cvt_pk_bf16_f32 v40, v40, v41\n"
        "v_cvt_pk_bf16_f32 v41, v42, v43\n"
        "v_cvt_pk_bf16_f32 v42, v44, v45\n"
        "v_cvt_pk_bf16_f32 v43, v46, v47\n"
        "global_store_dwordx4 v254, v[40:43], s[70:71] offset:-4096 sc1\n"
        "v_accvgpr_read_b32 v56, a24\n"
        "v_accvgpr_read_b32 v57, a25\n"
        "v_accvgpr_read_b32 v58, a26\n"
        "v_accvgpr_read_b32 v59, a27\n"
        "v_accvgpr_read_b32 v60, a28\n"
        "v_accvgpr_read_b32 v61, a29\n"
        "v_accvgpr_read_b32 v62, a30\n"
        "v_accvgpr_read_b32 v63, a31\n"
        "v_pk_mul_f32 v[56:57], v[56:57], v[246:247] op_sel:[0,0] op_sel_hi:[1,0]\n"
        "v_pk_mul_f32 v[58:59], v[58:59], v[246:247] op_sel:[0,0] op_sel_hi:[1,0]\n"
        "v_pk_mul_f32 v[60:61], v[60:61], v[246:247] op_sel:[0,0] op_sel_hi:[1,0]\n"
        "v_pk_mul_f32 v[62:63], v[62:63], v[246:247] op_sel:[0,0] op_sel_hi:[1,0]\n"
        "v_cvt_pk_bf16_f32 v56, v56, v57\n"
        "v_cvt_pk_bf16_f32 v57, v58, v59\n"
        "v_cvt_pk_bf16_f32 v58, v60, v61\n"
        "v_cvt_pk_bf16_f32 v59, v62, v63\n"
        "global_store_dwordx4 v254, v[56:59], s[70:71] offset:0 sc1\n"
        "v_add_f32 v247, v248, v249\n"
        "v_cmp_nge_f32 vcc, 0x4e6e6b28, v247\n"
        "s_nop 0\n"
        "s_cbranch_vccnz LRL_%=\n"
        "LSL_%=:\n"
        "s_cmp_eq_u32 s44, 7\n"
        "s_cbranch_scc1 LTL_%=\n"
        "; ---- SINGLE\n"
        "s_branch LSI_%=\n"
        "s_nop 0\n"
        "s_nop 0\n"
        "s_nop 0\n"
        "s_nop 0\n"
        "s_nop 0\n"
        "s_nop 0\n"
        "s_nop 0\n"
        "LSI_%=:\n"
        "v_mfma_f32_16x16x32_bf16 v[32:35], a[96:99], v[0:3], v[224:227]\n"
        "s_waitcnt lgkmcnt(4)\n"
        "ds_read_b128 a[156:159], v241 offset:27648\n"
        "s_add_u32 m0, s80, 0x0\n"
        "v_mfma_f32_16x16x32_bf16 v[32:35], a[100:103], v[4:7], v[32:35]\n"
        "ds_read_b128 a[124:127], v240 offset:7168\n"
        "global_load_lds_dwordx4 v254, s[68:69] offset:0 sc1\n"
        "s_xor_b32 s79, s79, 0x8000\n"
        "v_mfma_f32_16x16x32_bf16 v[32:35], a[104:107], v[8:11], v[32:35]\n"
        "s_add_u32 s82, s82, 1\n"
        "v_exp_f32 v48, v48\n"
        "v_mfma_f32_16x16x32_bf16 v[32:35], a[108:111], v[12:15], v[32:35]\n"
        "s_xor_b32 s78, s78, 0x8000\n"
        "v_exp_f32 v49, v49\n"
        "v_mfma_f32_16x16x32_bf16 v[36:39], a[108:111], v[76:79], v[228:231]\n"
        "ds_read_b128 a[108:111], v240 offset:11264\n"
        "s_mov_b32 s80, s78\n"
        "v_cvt_pk_bf16_f32 v82, v48, v49\n"
        "v_mfma_f32_16x16x32_bf16 v[36:39], a[104:107], v[72:75], v[36:39]\n"
        "ds_read_b128 a[104:107], v240 offset:10240\n"
        "global_load_lds_dwordx4 v254, s[68:69] offset:1024 sc1\n"
        "v_mfma_f32_16x16x32_bf16 v[36:39], a[100:103], v[68:71], v[36:39]\n"
        "v_exp_f32 v50, v50\n"
        "v_mfma_f32_16x16x32_bf16 v[36:39], a[96:99], v[64:67], v[36:39]\n"
        "v_exp_f32 v51, v51\n"
        "v_mfma_f32_16x16x32_bf16 v[176:179], a[128:131], v[16:19], v[176:179]\n"
        "ds_read_b128 a[96:99], v240 offset:8192\n"
        "v_cvt_pk_bf16_f32 v83, v50, v51\n"
        "v_mfma_f32_16x16x32_bf16 v[208:211], a[128:131], v[20:23], v[208:211]\n"
        "ds_read_b128 a[128:131], v241 offset:28672\n"
        "global_load_lds_dwordx4 v254, s[68:69] offset:2048 sc1\n"
        "v_mfma_f32_16x16x32_bf16 v[212:215], a[132:135], v[20:23], v[212:215]\n"
        "v_exp_f32 v52, v52\n"
        "v_mfma_f32_16x16x32_bf16 v[180:183], a[132:135], v[16:19], v[180:183]\n"
        "v_exp_f32 v53, v53\n"
        "v_mfma_f32_16x16x32_bf16 v[184:187], a[136:139], v[16:19], v[184:187]\n"
        "ds_read_b128 a[100:103], v240 offset:9216\n"
        "v_cvt_pk_bf16_f32 v86, v52, v53\n"
        "v_mfma_f32_16x16x32_bf16 v[216:219], a[136:139], v[20:23], v[216:219]\n"
        "ds_read_b128 a[132:135], v241 offset:29696\n"
        "global_load_lds_dwordx4 v254, s[68:69] offset:3072 sc1\n"
        "s_add_u32 m0, s81, 0x0\n"
        "v_mfma_f32_16x16x32_bf16 v[220:223], a[140:143], v[20:23], v[220:223]\n"
        "s_waitcnt lgkmcnt(4)\n"
        "v_exp_f32 v54, v54\n"
        "v_mfma_f32_16x16x32_bf16 v[188:191], a[140:143], v[16:19], v[188:191]\n"
        "s_add_u32 s81, s78, 4096\n"
        "v_exp_f32 v55, v55\n"
        "v_mfma_f32_16x16x32_bf16 v[48:51], a[112:115], v[0:3], v[224:227]\n"
        "ds_read_b128 a[136:139], v241 offset:30720\n"
        "v_cvt_pk_bf16_f32 v87, v54, v55\n"
        "v_mfma_f32_16x16x32_bf16 v[48:51], a[116:119], v[4:7], v[48:51]\n"
        "ds_read_b128 a[140:143], v241 offset:31744\n"
        "global_load_lds_dwordx4 v254, s[74:75] offset:0 sc1\n"
        "v_mfma_f32_16x16x32_bf16 v[48:51], a[120:123], v[8:11], v[48:51]\n"
        "v_exp_f32 v32, v32\n"
        "v_mfma_f32_16x16x32_bf16 v[48:51], a[124:127], v[12:15], v[48:51]\n"
        "v_exp_f32 v33, v33\n"
        "v_mfma_f32_16x16x32_bf16 v[52:55], a[124:127], v[76:79], v[228:231]\n"
        "ds_read_b128 a[124:127], v240 offset:15360\n"
        "v_cvt_pk_bf16_f32 v16, v32, v33\n"
        "v_mfma_f32_16x16x32_bf16 v[52:55], a[120:123], v[72:75], v[52:55]\n"
        "ds_read_b128 a[120:123], v240 offset:14336\n"
        "global_load_lds_dwordx4 v254, s[74:75] offset:1024 sc1\n"
        "v_mfma_f32_16x16x32_bf16 v[52:55], a[116:119], v[68:71], v[52:55]\n"
        "v_exp_f32 v34, v34\n"
        "v_mfma_f32_16x16x32_bf16 v[52:55], a[112:115], v[64:67], v[52:55]\n"
        "v_exp_f32 v35, v35\n"
        "v_smfmac_f32_16x16x64_bf16 a[64:67], a[72:75], v[80:87], v154\n"
        "ds_read_b128 a[112:115], v240 offset:12288\n"
        "v_cvt_pk_bf16_f32 v17, v34, v35\n"
        "v_mfma_f32_16x16x32_bf16 v[160:163], a[144:147], v[80:83], v[160:163]\n"
        "ds_read_b128 a[116:119], v240 offset:13312\n"
        "global_load_lds_dwordx4 v254, s[74:75] offset:2048 sc1\n"
        "v_mfma_f32_16x16x32_bf16 v[192:195], a[144:147], v[84:87], v[192:195]\n"
        "s_waitcnt vmcnt(8) lgkmcnt(4)\n"
        "v_exp_f32 v38, v38\n"
        "v_mfma_f32_16x16x32_bf16 v[196:199], a[148:151], v[84:87], v[196:199]\n"
        "s_barrier\n"
        "v_mfma_f32_16x16x32_bf16 v[164:167], a[148:151], v[80:83], v[164:167]\n"
        "v_exp_f32 v39, v39\n"
        "v_mfma_f32_16x16x32_bf16 v[168:171], a[152:155], v[80:83], v[168:171]\n"
        "v_exp_f32 v36, v36\n"
        "v_mfma_f32_16x16x32_bf16 v[200:203], a[152:155], v[84:87], v[200:203]\n"
        "v_exp_f32 v37, v37\n"
        "v_mfma_f32_16x16x32_bf16 v[204:207], a[156:159], v[84:87], v[204:207]\n"
        "v_exp_f32 v48, v48\n"
        "v_mfma_f32_16x16x32_bf16 v[172:175], a[156:159], v[80:83], v[172:175]\n"
        "v_exp_f32 v49, v49\n"
        "v_mfma_f32_16x16x32_bf16 v[32:35], a[96:99], v[0:3], v[224:227]\n"
        "v_exp_f32 v50, v50\n"
        "v_mfma_f32_16x16x32_bf16 v[32:35], a[100:103], v[4:7], v[32:35]\n"
        "v_exp_f32 v51, v51\n"
        "v_mfma_f32_16x16x32_bf16 v[32:35], a[104:107], v[8:11], v[32:35]\n"
        "v_exp_f32 v52, v52\n"
        "v_mfma_f32_16x16x32_bf16 v[32:35], a[108:111], v[12:15], v[32:35]\n"
        "v_exp_f32 v53, v53\n"
        "v_mfma_f32_16x16x32_bf16 v[176:179], a[128:131], v[80:83], v[176:179]\n"
        "v_exp_f32 v54, v54\n"
        "v_mfma_f32_16x16x32_bf16 v[208:211], a[128:131], v[84:87], v[208:211]\n"
        "v_exp_f32 v55, v55\n"
        "v_mfma_f32_16x16x32_bf16 v[212:215], a[132:135], v[84:87], v[212:215]\n"
        "global_load_lds_dwordx4 v254, s[74:75] offset:3072 sc1\n"
        "s_add_u32 m0, s79, 0x0\n"
        "v_cvt_pk_bf16_f32 v21, v38, v39\n"
        "v_mfma_f32_16x16x32_bf16 v[180:183], a[132:135], v[80:83], v[180:183]\n"
        "v_exp_f32 v32, v32\n"
        "v_mfma_f32_16x16x32_bf16 v[184:187], a[136:139], v[80:83], v[184:187]\n"
        "v_exp_f32 v33, v33\n"
        "v_mfma_f32_16x16x32_bf16 v[216:219], a[136:139], v[84:87], v[216:219]\n"
        "v_exp_f32 v34, v34\n"
        "v_mfma_f32_16x16x32_bf16 v[220:223], a[140:143], v[84:87], v[220:223]\n"
        "v_exp_f32 v35, v35\n"
        "v_mfma_f32_16x16x32_bf16 v[188:191], a[140:143], v[80:83], v[188:191]\n"
        "v_cvt_pk_bf16_f32 v20, v36, v37\n"
        "v_xor_b32 v241, 0x8000, v241\n"
        "v_mfma_f32_16x16x32_bf16 v[36:39], a[108:111], v[76:79], v[228:231]\n"
        "ds_read_b128 a[144:147], v241 offset:0\n"
        "s_add_u32 s76, s66, s90\n"
        "s_addc_u32 s77, s67, 0\n"
        "v_mfma_f32_16x16x32_bf16 v[36:39], a[104:107], v[72:75], v[36:39]\n"
        "ds_read_b128 a[148:151], v241 offset:1024\n"
        "global_load_lds_dwordx4 v144, s[76:77] offset:0 sc1\n"
        "v_mfma_f32_16x16x32_bf16 v[36:39], a[100:103], v[68:71], v[36:39]\n"
        "ds_read_b128 a[152:155], v241 offset:2048\n"
        "v_cvt_pk_bf16_f32 v18, v48, v49\n"
        "v_mfma_f32_16x16x32_bf16 v[36:39], a[96:99], v[64:67], v[36:39]\n"
        "ds_read_b128 a[156:159], v241 offset:3072\n"
        "v_cvt_pk_bf16_f32 v19, v50, v51\n"
        "s_waitcnt lgkmcnt(4)\n"
        "v_mfma_f32_16x16x32_bf16 v[48:51], a[112:115], v[0:3], v[224:227]\n"
        "ds_read_b128 a[96:99], v240 offset:16384\n"
        "v_cvt_pk_bf16_f32 v22, v52, v53\n"
        "v_mfma_f32_16x16x32_bf16 v[48:51], a[116:119], v[4:7], v[48:51]\n"
        "ds_read_b128 a[128:131], v241 offset:4096\n"
        "global_load_lds_dwordx4 v145, s[76:77] offset:1024 sc1\n"
        "v_mfma_f32_16x16x32_bf16 v[48:51], a[120:123], v[8:11], v[48:51]\n"
        "ds_read_b128 a[100:103], v240 offset:17408\n"
        "v_cvt_pk_bf16_f32 v23, v54, v55\n"
        "v_mfma_f32_16x16x32_bf16 v[48:51], a[124:127], v[12:15], v[48:51]\n"
        "ds_read_b128 a[132:135], v241 offset:5120\n"
        "v_cvt_pk_bf16_f32 v80, v32, v33\n"
        "v_mfma_f32_16x16x32_bf16 v[52:55], a[124:127], v[76:79], v[228:231]\n"
        "ds_read_b128 a[136:139], v241 offset:6144\n"
        "v_cvt_pk_bf16_f32 v81, v34, v35\n"
        "v_mfma_f32_16x16x32_bf16 v[52:55], a[120:123], v[72:75], v[52:55]\n"
        "s_waitcnt lgkmcnt(7)\n"
        "ds_read_b128 a[104:107], v240 offset:18432\n"
        "v_mfma_f32_16x16x32_bf16 v[52:55], a[116:119], v[68:71], v[52:55]\n"
        "ds_read_b128 a[140:143], v241 offset:7168\n"
        "global_load_lds_dwordx4 v146, s[76:77] offset:2048 sc1\n"
        "v_mfma_f32_16x16x32_bf16 v[52:55], a[112:115], v[64:67], v[52:55]\n"
        "ds_read_b128 a[108:111], v240 offset:19456\n"
        "s_cmp_ge_u32 s82, s44\n"
        "s_addc_u32 s90, s82, 0\n"
        "v_smfmac_f32_16x16x64_bf16 a[64:67], a[72:75], v[16:23], v154\n"
        "ds_read_b128 a[112:115], v240 offset:20480\n"
        "s_lshl_b32 s90, s90, 15\n"
        "v_mfma_f32_16x16x32_bf16 v[160:163], a[144:147], v[16:19], v[160:163]\n"
        "s_waitcnt lgkmcnt(7)\n"
        "v_exp_f32 v36, v36\n"
        "v_mfma_f32_16x16x32_bf16 v[192:195], a[144:147], v[20:23], v[192:195]\n"
        "ds_read_b128 a[144:147], v241 offset:8192\n"
        "global_load_lds_dwordx4 v147, s[76:77] offset:3072 sc1\n"
        "s_add_u32 m0, s79, s28\n"
        "v_mfma_f32_16x16x32_bf16 v[196:199], a[148:151], v[20:23], v[196:199]\n"
        "ds_read_b128 a[116:119], v240 offset:21504\n"
        "s_add_u32 s76, s76, s25\n"
        "s_addc_u32 s77, s77, 0\n"
        "v_mfma_f32_16x16x32_bf16 v[164:167], a[148:151], v[16:19], v[164:167]\n"
        "v_exp_f32 v37, v37\n"
        "v_mfma_f32_16x16x32_bf16 v[168:171], a[152:155], v[16:19], v[168:171]\n"
        "ds_read_b128 a[148:151], v241 offset:9216\n"
        "v_cvt_pk_bf16_f32 v84, v36, v37\n"
        "v_mfma_f32_16x16x32_bf16 v[200:203], a[152:155], v[20:23], v[200:203]\n"
        "s_waitcnt lgkmcnt(5)\n"
        "ds_read_b128 a[152:155], v241 offset:10240\n"
        "v_mfma_f32_16x16x32_bf16 v[204:207], a[156:159], v[20:23], v[204:207]\n"
        "ds_read_b128 a[120:123], v240 offset:22528\n"
        "global_load_lds_dwordx4 v144, s[76:77] offset:0 sc1\n"
        "v_mfma_f32_16x16x32_bf16 v[172:175], a[156:159], v[16:19], v[172:175]\n"
        "s_waitcnt lgkmcnt(5)\n"
        "v_exp_f32 v38, v38\n"
        "v_mfma_f32_16x16x32_bf16 v[32:35], a[96:99], v[0:3], v[224:227]\n"
        "v_exp_f32 v39, v39\n"
        "v_mfma_f32_16x16x32_bf16 v[32:35], a[100:103], v[4:7], v[32:35]\n"
        "ds_read_b128 a[156:159], v241 offset:11264\n"
        "v_cvt_pk_bf16_f32 v85, v38, v39\n"
        "v_mfma_f32_16x16x32_bf16 v[32:35], a[104:107], v[8:11], v[32:35]\n"
        "ds_read_b128 a[124:127], v240 offset:23552\n"
        "global_load_lds_dwordx4 v145, s[76:77] offset:1024 sc1\n"
        "v_mfma_f32_16x16x32_bf16 v[32:35], a[108:111], v[12:15], v[32:35]\n"
        "v_exp_f32 v48, v48\n"
        "v_mfma_f32_16x16x32_bf16 v[36:39], a[108:111], v[76:79], v[228:231]\n"
        "v_exp_f32 v49, v49\n"
        "v_mfma_f32_16x16x32_bf16 v[36:39], a[104:107], v[72:75], v[36:39]\n"
        "ds_read_b128 a[104:107], v240 offset:26624\n"
        "v_cvt_pk_bf16_f32 v82, v48, v49\n"
        "v_mfma_f32_16x16x32_bf16 v[36:39], a[100:103], v[68:71], v[36:39]\n"
        "ds_read_b128 a[100:103], v240 offset:25600\n"
        "global_load_lds_dwordx4 v146, s[76:77] offset:2048 sc1\n"
        "v_mfma_f32_16x16x32_bf16 v[36:39], a[96:99], v[64:67], v[36:39]\n"
        "v_exp_f32 v50, v50\n"
        "v_mfma_f32_16x16x32_bf16 v[176:179], a[128:131], v[16:19], v[176:179]\n"
        "v_exp_f32 v51, v51\n"
        "v_mfma_f32_16x16x32_bf16 v[208:211], a[128:131], v[20:23], v[208:211]\n"
        "ds_read_b128 a[96:99], v240 offset:24576\n"
        "v_cvt_pk_bf16_f32 v83, v50, v51\n"
        "v_mfma_f32_16x16x32_bf16 v[212:215], a[132:135], v[20:23], v[212:215]\n"
        "ds_read_b128 a[128:131], v241 offset:12288\n"
        "global_load_lds_dwordx4 v147, s[76:77] offset:3072 sc1\n"
        "v_mfma_f32_16x16x32_bf16 v[180:183], a[132:135], v[16:19], v[180:183]\n"
        "v_exp_f32 v52, v52\n"
        "v_mfma_f32_16x16x32_bf16 v[184:187], a[136:139], v[16:19], v[184:187]\n"
        "v_exp_f32 v53, v53\n"
        "v_mfma_f32_16x16x32_bf16 v[216:219], a[136:139], v[20:23], v[216:219]\n"
        "s_waitcnt lgkmcnt(3)\n"
        "ds_read_b128 a[132:135], v241 offset:13312\n"
        "v_mfma_f32_16x16x32_bf16 v[220:223], a[140:143], v[20:23], v[220:223]\n"
        "ds_read_b128 a[136:139], v241 offset:14336\n"
        "v_cvt_pk_bf16_f32 v86, v52, v53\n"
        "v_mfma_f32_16x16x32_bf16 v[188:191], a[140:143], v[16:19], v[188:191]\n"
        "v_exp_f32 v54, v54\n"
        "v_mfma_f32_16x16x32_bf16 v[48:51], a[112:115], v[0:3], v[224:227]\n"
        "v_exp_f32 v55, v55\n"
        "v_mfma_f32_16x16x32_bf16 v[48:51], a[116:119], v[4:7], v[48:51]\n"
        "ds_read_b128 a[140:143], v241 offset:15360\n"
        "v_cvt_pk_bf16_f32 v87, v54, v55\n"
        "v_mfma_f32_16x16x32_bf16 v[48:51], a[120:123], v[8:11], v[48:51]\n"
        "ds_read_b128 a[108:111], v240 offset:27648\n"
        "global_load_dword v244, v245, s[26:27] offset:0\n"
        "v_mfma_f32_16x16x32_bf16 v[48:51], a[124:127], v[12:15], v[48:51]\n"
        "v_exp_f32 v32, v32\n"
        "v_mfma_f32_16x16x32_bf16 v[52:55], a[124:127], v[76:79], v[228:231]\n"
        "ds_read_b128 a[124:127], v240 offset:31744\n"
        "s_add_u32 s26, s26, 0x20000\n"
        "s_addc_u32 s27, s27, 0\n"
        "v_mfma_f32_16x16x32_bf16 v[52:55], a[120:123], v[72:75], v[52:55]\n"
        "v_exp_f32 v33, v33\n"
        "v_mfma_f32_16x16x32_bf16 v[52:55], a[116:119], v[68:71], v[52:55]\n"
        "ds_read_b128 a[116:119], v240 offset:29696\n"
        "v_cvt_pk_bf16_f32 v16, v32, v33\n"
        "v_mfma_f32_16x16x32_bf16 v[52:55], a[112:115], v[64:67], v[52:55]\n"
        "v_exp_f32 v34, v34\n"
        "v_smfmac_f32_16x16x64_bf16 a[64:67], a[72:75], v[80:87], v154\n"
        "ds_read_b128 a[112:115], v240 offset:28672\n"
        "s_add_u32 s68, s64, s90\n"
        "s_addc_u32 s69, s65, 0\n"
        "v_mfma_f32_16x16x32_bf16 v[160:163], a[144:147], v[80:83], v[160:163]\n"
        "v_exp_f32 v35, v35\n"
        "v_mfma_f32_16x16x32_bf16 v[192:195], a[144:147], v[84:87], v[192:195]\n"
        "ds_read_b128 a[144:147], v241 offset:16384\n"
        "v_cvt_pk_bf16_f32 v17, v34, v35\n"
        "v_mfma_f32_16x16x32_bf16 v[196:199], a[148:151], v[84:87], v[196:199]\n"
        "v_exp_f32 v36, v36\n"
        "v_mfma_f32_16x16x32_bf16 v[164:167], a[148:151], v[80:83], v[164:167]\n"
        "v_exp_f32 v37, v37\n"
        "v_mfma_f32_16x16x32_bf16 v[168:171], a[152:155], v[80:83], v[168:171]\n"
        "s_waitcnt lgkmcnt(3)\n"
        "ds_read_b128 a[148:151], v241 offset:17408\n"
        "v_mfma_f32_16x16x32_bf16 v[200:203], a[152:155], v[84:87], v[200:203]\n"
        "ds_read_b128 a[152:155], v241 offset:18432\n"
        "v_cvt_pk_bf16_f32 v20, v36, v37\n"
        "v_mfma_f32_16x16x32_bf16 v[204:207], a[156:159], v[84:87], v[204:207]\n"
        "v_exp_f32 v38, v38\n"
        "v_mfma_f32_16x16x32_bf16 v[172:175], a[156:159], v[80:83], v[172:175]\n"
        "v_exp_f32 v39, v39\n"
        "v_mfma_f32_16x16x32_bf16 v[32:35], a[96:99], v[0:3], v[224:227]\n"
        "ds_read_b128 a[120:123], v240 offset:30720\n"
        "v_cvt_pk_bf16_f32 v21, v38, v39\n"
        "v_mfma_f32_16x16x32_bf16 v[32:35], a[100:103], v[4:7], v[32:35]\n"
        "ds_read_b128 a[156:159], v241 offset:19456\n"
        "v_xor_b32 v240, 0x8000, v240\n"
        "v_mfma_f32_16x16x32_bf16 v[32:35], a[104:107], v[8:11], v[32:35]\n"
        "v_exp_f32 v48, v48\n"
        "v_mfma_f32_16x16x32_bf16 v[32:35], a[108:111], v[12:15], v[32:35]\n"
        "v_exp_f32 v49, v49\n"
        "v_mfma_f32_16x16x32_bf16 v[36:39], a[108:111], v[76:79], v[228:231]\n"
        "v_exp_f32 v50, v50\n"
        "v_mfma_f32_16x16x32_bf16 v[36:39], a[104:107], v[72:75], v[36:39]\n"
        "v_exp_f32 v51, v51\n"
        "v_mfma_f32_16x16x32_bf16 v[36:39], a[100:103], v[68:71], v[36:39]\n"
        "v_exp_f32 v52, v52\n"
        "v_mfma_f32_16x16x32_bf16 v[36:39], a[96:99], v[64:67], v[36:39]\n"
        "v_exp_f32 v53, v53\n"
        "v_mfma_f32_16x16x32_bf16 v[176:179], a[128:131], v[80:83], v[176:179]\n"
        "s_waitcnt vmcnt(9) lgkmcnt(1)\n"
        "v_exp_f32 v54, v54\n"
        "v_mfma_f32_16x16x32_bf16 v[208:211], a[128:131], v[84:87], v[208:211]\n"
        "s_barrier\n"
        "v_mfma_f32_16x16x32_bf16 v[212:215], a[132:135], v[84:87], v[212:215]\n"
        "ds_read_b128 a[96:99], v240 offset:0\n"
        "v_cvt_pk_bf16_f32 v18, v48, v49\n"
        "v_mfma_f32_16x16x32_bf16 v[180:183], a[132:135], v[80:83], v[180:183]\n"
        "ds_read_b128 a[128:131], v241 offset:20480\n"
        "v_cvt_pk_bf16_f32 v19, v50, v51\n"
        "v_mfma_f32_16x16x32_bf16 v[184:187], a[136:139], v[80:83], v[184:187]\n"
        "ds_read_b128 a[100:103], v240 offset:1024\n"
        "s_add_u32 s74, s68, 0x1000\n"
        "s_addc_u32 s75, s69, 0\n"
        "v_mfma_f32_16x16x32_bf16 v[216:219], a[136:139], v[84:87], v[216:219]\n"
        "ds_read_b128 a[132:135], v241 offset:21504\n"
        "v_cvt_pk_bf16_f32 v22, v52, v53\n"
        "v_mfma_f32_16x16x32_bf16 v[220:223], a[140:143], v[84:87], v[220:223]\n"
        "v_exp_f32 v55, v55\n"
        "v_mfma_f32_16x16x32_bf16 v[188:191], a[140:143], v[80:83], v[188:191]\n"
        "ds_read_b128 a[136:139], v241 offset:22528\n"
        "v_cvt_pk_bf16_f32 v23, v54, v55\n"
        "v_mfma_f32_16x16x32_bf16 v[48:51], a[112:115], v[0:3], v[224:227]\n"
        "ds_read_b128 a[104:107], v240 offset:2048\n"
        "v_mfma_f32_16x16x32_bf16 v[48:51], a[116:119], v[4:7], v[48:51]\n"
        "ds_read_b128 a[140:143], v241 offset:23552\n"
        "v_mfma_f32_16x16x32_bf16 v[48:51], a[120:123], v[8:11], v[48:51]\n"
        "v_exp_f32 v32, v32\n"
        "v_mfma_f32_16x16x32_bf16 v[48:51], a[124:127], v[12:15], v[48:51]\n"
        "s_waitcnt lgkmcnt(5)\n"
        "v_exp_f32 v33, v33\n"
        "v_mfma_f32_16x16x32_bf16 v[52:55], a[124:127], v[76:79], v[228:231]\n"
        "ds_read_b128 a[108:111], v240 offset:3072\n"
        "v_cvt_pk_bf16_f32 v80, v32, v33\n"
        "v_mfma_f32_16x16x32_bf16 v[52:55], a[120:123], v[72:75], v[52:55]\n"
        "v_exp_f32 v34, v34\n"
        "v_mfma_f32_16x16x32_bf16 v[52:55], a[116:119], v[68:71], v[52:55]\n"
        "v_exp_f32 v35, v35\n"
        "v_mfma_f32_16x16x32_bf16 v[52:55], a[112:115], v[64:67], v[52:55]\n"
        "ds_read_b128 a[112:115], v240 offset:4096\n"
        "v_cvt_pk_bf16_f32 v81, v34, v35\n"
        "v_smfmac_f32_16x16x64_bf16 a[64:67], a[72:75], v[16:23], v154\n"
        "ds_read_b128 a[116:119], v240 offset:5120\n"
        "v_mfma_f32_16x16x32_bf16 v[160:163], a[144:147], v[16:19], v[160:163]\n"
        "v_exp_f32 v36, v36\n"
        "v_mfma_f32_16x16x32_bf16 v[192:195], a[144:147], v[20:23], v[192:195]\n"
        "v_exp_f32 v37, v37\n"
        "v_mfma_f32_16x16x32_bf16 v[196:199], a[148:151], v[20:23], v[196:199]\n"
        "ds_read_b128 a[144:147], v241 offset:24576\n"
        "v_cvt_pk_bf16_f32 v84, v36, v37\n"
        "v_mfma_f32_16x16x32_bf16 v[164:167], a[148:151], v[16:19], v[164:167]\n"
        "ds_read_b128 a[148:151], v241 offset:25600\n"
        "v_mfma_f32_16x16x32_bf16 v[168:171], a[152:155], v[16:19], v[168:171]\n"
        "v_exp_f32 v38, v38\n"
        "v_mfma_f32_16x16x32_bf16 v[200:203], a[152:155], v[20:23], v[200:203]\n"
        "s_cmp_lg_u32 s82, s24\n"
        "v_exp_f32 v39, v39\n"
        "v_mfma_f32_16x16x32_bf16 v[204:207], a[156:159], v[20:23], v[204:207]\n"
        "ds_read_b128 a[152:155], v241 offset:26624\n"
        "v_cvt_pk_bf16_f32 v85, v38, v39\n"
        "v_mfma_f32_16x16x32_bf16 v[172:175], a[156:159], v[16:19], v[172:175]\n"
        "ds_read_b128 a[120:123], v240 offset:6144\n"
        "s_cbranch_scc1 LSI_%=\n"
        "LHC1_%=:\n"
        "s_cbranch_execz LHRC_%=\n"
        "s_sub_u32 s97, s83, s82\n"
        "s_cmp_gt_u32 s97, 1\n"
        "s_cbranch_scc1 LTL_%=\n"
        "s_cmp_eq_u32 s97, 0\n"
        "s_cbranch_scc1 LE3S_%=\n"
        "global_load_dwordx2 v[252:253], v155, s[48:49] sc1\n"
        "v_accvgpr_read_b32 v246, a93\n"
        "global_load_dword v244, v246, s[46:47]\n"
        "s_branch LEWS_%=\n"
        "LE3S_%=:\n"
        "v_cmp_ne_u64 vcc, v[252:253], v[250:251]\n"
        "s_mov_b64 s[68:69], s[92:93]\n"
        "s_mov_b64 s[74:75], s[94:95]\n"
        "s_mov_b32 s80, s50\n"
        "s_mov_b32 s81, s51\n"
        "s_mov_b64 s[66:67], s[22:23]\n"
        "s_mov_b32 s90, s29\n"
        "s_mov_b32 s25, s30\n"
        "s_lshl_b32 s28, s28, 2\n"
        "s_xor_b32 s79, s73, 0x8000\n"
        "v_accvgpr_read_b32 v144, a84\n"
        "v_accvgpr_read_b32 v145, a85\n"
        "v_accvgpr_read_b32 v146, a86\n"
        "v_accvgpr_read_b32 v147, a87\n"
        "s_mov_b64 exec, 15\n"
        "ds_write_b128 v156, a[88:91]\n"
        "s_mov_b64 exec, -1\n"
        "s_cbranch_vccnz LE4S_%=\n"
        "LEWS_%=:\n"
        "s_add_u32 s24, s82, 1\n"
        "s_cmp_lg_u32 s82, s91\n"
        "s_cbranch_scc1 LSI_%=\n"
        "; ---- BACK\n"
        "LTL_%=:\n"
        "v_mfma_f32_16x16x32_bf16 v[176:179], a[128:131], v[16:19], v[176:179]\n"
        "s_waitcnt lgkmcnt(4)\n"
        "ds_read_b128 a[156:159], v241 offset:27648\n"
        "s_mul_i32 s53, s45, 1052672\n"
        "v_mfma_f32_16x16x32_bf16 v[208:211], a[128:131], v[20:23], v[208:211]\n"
        "s_lshl_b32 s96, s43, 15\n"
        "v_exp_f32 v48, v48\n"
        "v_mfma_f32_16x16x32_bf16 v[212:215], a[132:135], v[20:23], v[212:215]\n"
        "s_add_u32 s53, s53, s96\n"
        "v_exp_f32 v49, v49\n"
        "v_mfma_f32_16x16x32_bf16 v[180:183], a[132:135], v[16:19], v[180:183]\n"
        "ds_read_b128 a[128:131], v241 offset:28672\n"
        "s_lshl_b32 s96, s40, 10\n"
        "v_cvt_pk_bf16_f32 v82, v48, v49\n"
        "v_mfma_f32_16x16x32_bf16 v[184:187], a[136:139], v[16:19], v[184:187]\n"
        "s_add_u32 s53, s53, s96\n"
        "v_exp_f32 v50, v50\n"
        "v_mfma_f32_16x16x32_bf16 v[216:219], a[136:139], v[20:23], v[216:219]\n"
        "s_add_u32 s53, s53, 0x2201000\n"
        "v_exp_f32 v51, v51\n"
        "v_mfma_f32_16x16x32_bf16 v[220:223], a[140:143], v[20:23], v[220:223]\n"
        "s_waitcnt lgkmcnt(3)\n"
        "ds_read_b128 a[132:135], v241 offset:29696\n"
        "s_add_u32 s16, s51, 4096\n"
        "v_mfma_f32_16x16x32_bf16 v[188:191], a[140:143], v[16:19], v[188:191]\n"
        "v_cvt_pk_bf16_f32 v83, v50, v51\n"
        "s_nop 1\n"
        "v_mfma_f32_16x16x32_bf16 v[160:163], a[144:147], v[80:83], v[160:163]\n"
        "s_lshl_b32 s14, s44, 5\n"
        "v_exp_f32 v52, v52\n"
        "v_mfma_f32_16x16x32_bf16 v[164:167], a[148:151], v[80:83], v[164:167]\n"
        "s_add_u32 s14, s14, s43\n"
        "v_exp_f32 v53, v53\n"
        "v_mfma_f32_16x16x32_bf16 v[168:171], a[152:155], v[80:83], v[168:171]\n"
        "s_waitcnt lgkmcnt(2)\n"
        "ds_read_b128 a[136:139], v241 offset:30720\n"
        "s_lshl2_add_u32 s14, s14, s40\n"
        "v_mfma_f32_16x16x32_bf16 v[172:175], a[156:159], v[80:83], v[172:175]\n"
        "s_waitcnt lgkmcnt(2)\n"
        "ds_read_b128 a[140:143], v241 offset:31744\n"
        "s_lshl_b32 s14, s14, 8\n"
        "v_mfma_f32_16x16x32_bf16 v[176:179], a[128:131], v[80:83], v[176:179]\n"
        "s_add_u32 s70, s38, s53\n"
        "s_addc_u32 s71, s39, 0\n"
        "s_add_u32 s14, s14, 131072\n"
        "s_add_u32 m0, s73, 0x8000\n"
        "s_xor_b32 s66, s29, 0x40000\n"
        "v_cvt_pk_bf16_f32 v86, v52, v53\n"
        "v_add_u32 v230, 3584, v152\n"
        "s_waitcnt lgkmcnt(2)\n"
        "v_mfma_f32_16x16x32_bf16 v[180:183], a[132:135], v[80:83], v[180:183]\n"
        "s_add_u32 s60, s22, s66\n"
        "s_addc_u32 s61, s23, 0\n"
        "s_xor_b32 s66, s29, 0x60000\n"
        "s_cmp_le_u32 s10, 0\n"
        "s_addc_u32 s74, 0, 0\n"
        "s_cmp_le_u32 s10, 1\n"
        "s_addc_u32 s75, 1, 0\n"
        "s_cmp_le_u32 s10, 2\n"
        "s_addc_u32 s76, 2, 0\n"
        "s_sub_u32 s62, 15, s12\n"
        "s_cmp_lt_u32 s10, 2\n"
        "s_cselect_b32 s62, s12, s62\n"
        "s_lshl2_add_u32 s63, s11, s40\n"
        "v_exp_f32 v54, v54\n"
        "s_waitcnt lgkmcnt(1)\n"
        "v_exp_f32 v55, v55\n"
        "v_mfma_f32_16x16x32_bf16 v[184:187], a[136:139], v[80:83], v[184:187]\n"
        "s_waitcnt lgkmcnt(0)\n"
        "v_cvt_pk_bf16_f32 v87, v54, v55\n"
        "s_nop 1\n"
        "v_smfmac_f32_16x16x64_bf16 a[64:67], a[72:75], v[80:87], v154\n"
        "ds_read2_b32 a[0:1], v148 offset0:0 offset1:1\n"
        "s_lshl_b32 s63, s63, 5\n"
        "v_min_u32 v230, s16, v230\n"
        "v_mfma_f32_16x16x32_bf16 v[192:195], a[144:147], v[84:87], v[192:195]\n"
        "ds_read2_b32 a[2:3], v148 offset0:2 offset1:3\n"
        "v_add_u32 v231, 3648, v152\n"
        "v_mfma_f32_16x16x32_bf16 v[196:199], a[148:151], v[84:87], v[196:199]\n"
        "ds_read_b128 a[192:195], v230 offset:0\n"
        "v_accvgpr_read_b32 v248, a64\n"
        "v_mfma_f32_16x16x32_bf16 v[200:203], a[152:155], v[84:87], v[200:203]\n"
        "ds_read2_b32 a[4:5], v148 offset0:128 offset1:129\n"
        "v_accvgpr_read_b32 v246, a65\n"
        "v_mfma_f32_16x16x32_bf16 v[204:207], a[156:159], v[84:87], v[204:207]\n"
        "ds_read2_b32 a[6:7], v148 offset0:130 offset1:131\n"
        "v_add_f32 v248, v248, v246\n"
        "v_mfma_f32_16x16x32_bf16 v[208:211], a[128:131], v[84:87], v[208:211]\n"
        "v_rcp_f32 v246, v248\n"
        "v_mfma_f32_16x16x32_bf16 v[212:215], a[132:135], v[84:87], v[212:215]\n"
        "ds_read_b128 a[196:199], v152 offset:3072\n"
        "v_accvgpr_read_b32 v249, a66\n"
        "v_mfma_f32_16x16x32_bf16 v[216:219], a[136:139], v[84:87], v[216:219]\n"
        "ds_read2_b32 a[8:9], v149 offset0:0 offset1:1\n"
        "v_accvgpr_read_b32 v247, a67\n"
        "v_mfma_f32_16x16x32_bf16 v[220:223], a[140:143], v[84:87], v[220:223]\n"
        "ds_read2_b32 a[10:11], v149 offset0:2 offset1:3\n"
        "v_add_f32 v249, v249, v247\n"
        "v_mfma_f32_16x16x32_bf16 v[188:191], a[140:143], v[80:83], v[188:191]\n"
        "v_rcp_f32 v247, v249\n"
        "s_waitcnt lgkmcnt(5)\n"
        "v_mul_f32 v192, v192, v247\n"
        "v_mul_f32 v193, v193, v247\n"
        "v_mul_f32 v194, v194, v247\n"
        "v_mul_f32 v195, v195, v247\n"
        "v_mul_f32 v196, v196, v247\n"
        "v_mul_f32 v197, v197, v247\n"
        "v_mul_f32 v198, v198, v247\n"
        "v_mul_f32 v199, v199, v247\n"
        "v_cvt_pk_bf16_f32 v192, v192, v193\n"
        "v_cvt_pk_bf16_f32 v193, v194, v195\n"
        "v_cvt_pk_bf16_f32 v194, v196, v197\n"
        "v_cvt_pk_bf16_f32 v195, v198, v199\n"
        "global_store_dwordx4 v254, v[192:195], s[70:71] offset:-4096 sc1\n"
        "ds_read_b128 a[200:203], v152 offset:2560\n"
        "s_waitcnt lgkmcnt(3)\n"
        "v_mul_f32 v160, v160, v246\n"
        "v_mul_f32 v161, v161, v246\n"
        "v_mul_f32 v162, v162, v246\n"
        "v_mul_f32 v163, v163, v246\n"
        "v_mul_f32 v164, v164, v246\n"
        "v_mul_f32 v165, v165, v246\n"
        "v_mul_f32 v166, v166, v246\n"
        "v_mul_f32 v167, v167, v246\n"
        "v_cvt_pk_bf16_f32 v160, v160, v161\n"
        "v_cvt_pk_bf16_f32 v161, v162, v163\n"
        "v_cvt_pk_bf16_f32 v162, v164, v165\n"
        "v_cvt_pk_bf16_f32 v163, v166, v167\n"
        "global_store_dwordx4 v254, v[160:163], s[70:71] offset:0 sc1\n"
        "s_add_u32 s70, s70, 0x2000\n"
        "s_addc_u32 s71, s71, 0\n"
        "s_waitcnt lgkmcnt(1)\n"
        "v_mul_f32 v200, v200, v247\n"
        "v_mul_f32 v201, v201, v247\n"
        "v_mul_f32 v202, v202, v247\n"
        "v_mul_f32 v203, v203, v247\n"
        "v_mul_f32 v204, v204, v247\n"
        "v_mul_f32 v205, v205, v247\n"
        "v_mul_f32 v206, v206, v247\n"
        "v_mul_f32 v207, v207, v247\n"
        "v_cvt_pk_bf16_f32 v200, v200, v201\n"
        "v_cvt_pk_bf16_f32 v201, v202, v203\n"
        "v_cvt_pk_bf16_f32 v202, v204, v205\n"
        "v_cvt_pk_bf16_f32 v203, v206, v207\n"
        "global_store_dwordx4 v254, v[200:203], s[70:71] offset:-4096 sc1\n"
        "ds_read2_b32 a[12:13], v149 offset0:128 offset1:129\n"
        "ds_read2_b32 a[14:15], v149 offset0:130 offset1:131\n"
        "v_mul_f32 v168, v168, v246\n"
        "v_mul_f32 v169, v169, v246\n"
        "v_mul_f32 v170, v170, v246\n"
        "v_mul_f32 v171, v171, v246\n"
        "v_mul_f32 v172, v172, v246\n"
        "v_mul_f32 v173, v173, v246\n"
        "v_mul_f32 v174, v174, v246\n"
        "v_mul_f32 v175, v175, v246\n"
        "v_cvt_pk_bf16_f32 v168, v168, v169\n"
        "v_cvt_pk_bf16_f32 v169, v170, v171\n"
        "v_cvt_pk_bf16_f32 v170, v172, v173\n"
        "v_cvt_pk_bf16_f32 v171, v174, v175\n"
        "global_store_dwordx4 v254, v[168:171], s[70:71] offset:0 sc1\n"
        "s_add_u32 s70, s70, 0x2000\n"
        "s_addc_u32 s71, s71, 0\n"
        "ds_read_b128 a[204:207], v152 offset:2048\n"
        "v_mul_f32 v208, v208, v247\n"
        "v_mul_f32 v209, v209, v247\n"
        "v_mul_f32 v210, v210, v247\n"
        "v_mul_f32 v211, v211, v247\n"
        "v_mul_f32 v212, v212, v247\n"
        "v_mul_f32 v213, v213, v247\n"
        "v_mul_f32 v214, v214, v247\n"
        "v_mul_f32 v215, v215, v247\n"
        "v_cvt_pk_bf16_f32 v208, v208, v209\n"
        "v_cvt_pk_bf16_f32 v209, v210, v211\n"
        "v_cvt_pk_bf16_f32 v210, v212, v213\n"
        "v_cvt_pk_bf16_f32 v211, v214, v215\n"
        "global_store_dwordx4 v254, v[208:211], s[70:71] offset:-4096 sc1\n"
        "ds_read2_b32 a[16:17], v150 offset0:0 offset1:1\n"
        "ds_read2_b32 a[18:19], v150 offset0:2 offset1:3\n"
        "v_mul_f32 v176, v176, v246\n"
        "v_mul_f32 v177, v177, v246\n"
        "v_mul_f32 v178, v178, v246\n"
        "v_mul_f32 v179, v179, v246\n"
        "v_mul_f32 v180, v180, v246\n"
        "v_mul_f32 v181, v181, v246\n"
        "v_mul_f32 v182, v182, v246\n"
        "v_mul_f32 v183, v183, v246\n"
        "v_cvt_pk_bf16_f32 v176, v176, v177\n"
        "v_cvt_pk_bf16_f32 v177, v178, v179\n"
        "v_cvt_pk_bf16_f32 v178, v180, v181\n"
        "v_cvt_pk_bf16_f32 v179, v182, v183\n"
        "global_store_dwordx4 v254, v[176:179], s[70:71] offset:0 sc1\n"
        "s_add_u32 s70, s70, 0x2000\n"
        "s_addc_u32 s71, s71, 0\n"
        "ds_read_b128 a[208:211], v152 offset:1536\n"
        "v_mul_f32 v216, v216, v247\n"
        "v_mfma_f32_16x16x32_bf16 v[96:99], a[0:3], a[192:195], 0\n"
        "ds_read2_b32 a[20:21], v150 offset0:128 offset1:129\n"
        "v_mul_f32 v217, v217, v247\n"
        "v_mfma_f32_16x16x32_bf16 v[100:103], a[0:3], a[196:199], 0\n"
        "ds_read2_b32 a[22:23], v150 offset0:130 offset1:131\n"
        "v_mul_f32 v218, v218, v247\n"
        "v_mfma_f32_16x16x32_bf16 v[100:103], a[4:7], a[192:195], v[100:103]\n"
        "ds_read_b128 a[212:215], v152 offset:1024\n"
        "v_mul_f32 v219, v219, v247\n"
        "v_mul_f32 v220, v220, v247\n"
        "v_mul_f32 v221, v221, v247\n"
        "v_mul_f32 v222, v222, v247\n"
        "v_mul_f32 v223, v223, v247\n"
        "v_cvt_pk_bf16_f32 v216, v216, v217\n"
        "v_cvt_pk_bf16_f32 v217, v218, v219\n"
        "v_cvt_pk_bf16_f32 v218, v220, v221\n"
        "v_cvt_pk_bf16_f32 v219, v222, v223\n"
        "global_store_dwordx4 v254, v[216:219], s[70:71] offset:-4096 sc1\n"
        "s_waitcnt lgkmcnt(4)\n"
        "v_mul_f32 v184, v184, v246\n"
        "v_mul_f32 v185, v185, v246\n"
        "v_mul_f32 v186, v186, v246\n"
        "v_mfma_f32_16x16x32_bf16 v[104:107], a[8:11], a[192:195], 0\n"
        "ds_read2_b32 a[24:25], v151 offset0:0 offset1:1\n"
        "v_mul_f32 v187, v187, v246\n"
        "v_mul_f32 v188, v188, v246\n"
        "v_mul_f32 v189, v189, v246\n"
        "v_mul_f32 v190, v190, v246\n"
        "v_mul_f32 v191, v191, v246\n"
        "v_cvt_pk_bf16_f32 v184, v184, v185\n"
        "v_cvt_pk_bf16_f32 v185, v186, v187\n"
        "v_cvt_pk_bf16_f32 v186, v188, v189\n"
        "v_cvt_pk_bf16_f32 v187, v190, v191\n"
        "global_store_dwordx4 v254, v[184:187], s[70:71] offset:0 sc1\n"
        "ds_read2_b32 a[26:27], v151 offset0:2 offset1:3\n"
        "ds_read_b128 a[216:219], v152 offset:512\n"
        "v_add_f32 v247, v248, v249\n"
        "v_mfma_f32_16x16x32_bf16 v[104:107], a[4:7], a[196:199], v[104:107]\n"
        "ds_read2_b32 a[28:29], v151 offset0:128 offset1:129\n"
        "v_cmp_nge_f32 vcc, 0x4e6e6b28, v247\n"
        "v_mfma_f32_16x16x32_bf16 v[104:107], a[0:3], a[200:203], v[104:107]\n"
        "ds_read2_b32 a[30:31], v151 offset0:130 offset1:131\n"
        "s_cbranch_vccnz LRH_%=\n"
        "LSH_%=:\n"
        "v_min_u32 v231, s16, v231\n"
        "v_mfma_f32_16x16x32_bf16 v[108:111], a[0:3], a[204:207], 0\n"
        "ds_read_b128 a[220:223], v152 offset:0\n"
        "v_and_b32 v234, 0x70, v254\n"
        "v_mfma_f32_16x16x32_bf16 v[108:111], a[4:7], a[200:203], v[108:111]\n"
        "ds_read2_b32 a[32:33], v148 offset0:16 offset1:17\n"
        "v_lshrrev_b32 v234, 1, v234\n"
        "v_mfma_f32_16x16x32_bf16 v[108:111], a[8:11], a[196:199], v[108:111]\n"
        "ds_read2_b32 a[34:35], v148 offset0:18 offset1:19\n"
        "v_and_b32 v235, 0x80, v254\n"
        "v_mfma_f32_16x16x32_bf16 v[108:111], a[12:15], a[192:195], v[108:111]\n"
        "ds_read_b128 a[224:227], v231 offset:0\n"
        "v_add3_u32 v234, v234, v235, s14\n"
        "v_mfma_f32_16x16x32_bf16 v[128:131], a[16:19], a[192:195], 0\n"
        "ds_read2_b32 a[36:37], v148 offset0:144 offset1:145\n"
        "s_and_b32 s14, s10, 1\n"
        "v_xor_b32 v154, 32, v153\n"
        "v_mfma_f32_16x16x32_bf16 v[128:131], a[12:15], a[196:199], v[128:131]\n"
        "ds_read2_b32 a[38:39], v148 offset0:146 offset1:147\n"
        "s_lshl1_add_u32 s62, s62, s14\n"
        "v_add_u32 v155, 0x10000, v153\n"
        "v_mfma_f32_16x16x32_bf16 v[128:131], a[8:11], a[200:203], v[128:131]\n"
        "ds_read_b128 a[228:231], v152 offset:3136\n"
        "s_lshl_b32 s62, s62, 6\n"
        "v_add_u32 v156, 0x10000, v154\n"
        "v_mfma_f32_16x16x32_bf16 v[128:131], a[4:7], a[204:207], v[128:131]\n"
        "ds_read2_b32 a[40:41], v149 offset0:16 offset1:17\n"
        "s_lshl_b32 s14, s62, 11\n"
        "s_waitcnt lgkmcnt(8)\n"
        "v_mfma_f32_16x16x32_bf16 v[128:131], a[0:3], a[208:211], v[128:131]\n"
        "ds_read2_b32 a[42:43], v149 offset0:18 offset1:19\n"
        "s_lshl1_add_u32 s14, s63, s14\n"
        "v_add_u32 v157, 0x20000, v153\n"
        "v_mfma_f32_16x16x32_bf16 v[132:135], a[0:3], a[212:215], 0\n"
        "ds_read_b128 a[232:235], v152 offset:2624\n"
        "v_add_u32 v158, 0x20000, v154\n"
        "v_mfma_f32_16x16x32_bf16 v[132:135], a[4:7], a[208:211], v[132:135]\n"
        "ds_read2_b32 a[44:45], v149 offset0:144 offset1:145\n"
        "v_add_u32 v230, 3712, v152\n"
        "v_mfma_f32_16x16x32_bf16 v[132:135], a[8:11], a[204:207], v[132:135]\n"
        "ds_read2_b32 a[46:47], v149 offset0:146 offset1:147\n"
        "v_min_u32 v230, s16, v230\n"
        "v_mfma_f32_16x16x32_bf16 v[132:135], a[12:15], a[200:203], v[132:135]\n"
        "ds_read_b128 a[236:239], v152 offset:2112\n"
        "s_and_b32 s67, s74, 1\n"
        "v_bfe_u32 v232, v254, 4, 4\n"
        "v_mfma_f32_16x16x32_bf16 v[132:135], a[16:19], a[196:199], v[132:135]\n"
        "ds_read2_b32 a[48:49], v150 offset0:16 offset1:17\n"
        "s_waitcnt vmcnt(9)\n"
        "v_mfma_f32_16x16x32_bf16 v[132:135], a[20:23], a[192:195], v[132:135]\n"
        "s_barrier\n"
        "v_mfma_f32_16x16x32_bf16 v[136:139], a[24:27], a[192:195], 0\n"
        "ds_read2_b32 a[50:51], v150 offset0:18 offset1:19\n"
        "s_lshl_b32 s67, s67, 12\n"
        "v_lshrrev_b32 v233, 8, v254\n"
        "v_mfma_f32_16x16x32_bf16 v[136:139], a[20:23], a[196:199], v[136:139]\n"
        "ds_read_b128 a[240:243], v152 offset:1600\n"
        "v_add_u32 v231, 3776, v152\n"
        "v_mfma_f32_16x16x32_bf16 v[136:139], a[16:19], a[200:203], v[136:139]\n"
        "ds_read2_b32 a[52:53], v150 offset0:144 offset1:145\n"
        "v_min_u32 v231, s16, v231\n"
        "v_mfma_f32_16x16x32_bf16 v[136:139], a[12:15], a[204:207], v[136:139]\n"
        "ds_read2_b32 a[54:55], v150 offset0:146 offset1:147\n"
        "s_add_u32 s64, s8, s14\n"
        "s_addc_u32 s65, s9, 0\n"
        "v_mfma_f32_16x16x32_bf16 v[136:139], a[8:11], a[208:211], v[136:139]\n"
        "ds_read_b128 a[244:247], v152 offset:1088\n"
        "s_add_u32 s56, s36, s14\n"
        "s_addc_u32 s57, s37, 0\n"
        "v_mfma_f32_16x16x32_bf16 v[136:139], a[4:7], a[212:215], v[136:139]\n"
        "ds_read2_b32 a[56:57], v151 offset0:16 offset1:17\n"
        "s_lshl2_add_u32 s14, s12, s10\n"
        "s_lshl_b32 s14, s14, 6\n"
        "v_mfma_f32_16x16x32_bf16 v[136:139], a[0:3], a[216:219], v[136:139]\n"
        "ds_read2_b32 a[58:59], v151 offset0:18 offset1:19\n"
        "s_lshl4_add_u32 s14, s40, s14\n"
        "s_waitcnt lgkmcnt(7)\n"
        "v_mfma_f32_16x16x32_bf16 v[140:143], a[0:3], a[220:223], 0\n"
        "ds_read_b128 a[248:251], v152 offset:576\n"
        "s_add_u32 s14, s14, 1024\n"
        "s_lshl_b32 s14, s14, 7\n"
        "v_mfma_f32_16x16x32_bf16 v[140:143], a[4:7], a[216:219], v[140:143]\n"
        "ds_read2_b32 a[60:61], v151 offset0:144 offset1:145\n"
        "v_lshl_add_u32 v159, v232, 7, s14\n"
        "v_mfma_f32_16x16x32_bf16 v[140:143], a[8:11], a[212:215], v[140:143]\n"
        "ds_read2_b32 a[62:63], v151 offset0:146 offset1:147\n"
        "s_add_u32 s24, s94, 0x400000\n"
        "s_addc_u32 s25, s95, 0\n"
        "v_mfma_f32_16x16x32_bf16 v[140:143], a[12:15], a[208:211], v[140:143]\n"
        "ds_read_b128 a[252:255], v152 offset:64\n"
        "v_mfma_f32_16x16x32_bf16 v[140:143], a[16:19], a[204:207], v[140:143]\n"
        "ds_read_b64_tr_b16 a[96:97], v153 offset:0\n"
        "v_mfma_f32_16x16x32_bf16 v[140:143], a[20:23], a[200:203], v[140:143]\n"
        "ds_read_b64_tr_b16 a[98:99], v153 offset:4096\n"
        "v_mfma_f32_16x16x32_bf16 v[140:143], a[24:27], a[196:199], v[140:143]\n"
        "ds_read2_b32 a[0:1], v148 offset0:32 offset1:33\n"
        "v_mfma_f32_16x16x32_bf16 v[140:143], a[28:31], a[192:195], v[140:143]\n"
        "s_mov_b64 exec, 0\n"
        "s_cmp_eq_u32 s40, 0\n"
        "s_cbranch_scc1 LHB0_%=\n"
        "s_cmp_eq_u32 s40, 1\n"
        "s_cbranch_scc1 LHB1_%=\n"
        "s_cmp_eq_u32 s40, 2\n"
        "s_cbranch_scc1 LHB2_%=\n"
        "s_branch LHB3_%=\n"
        "LHRB_%=:\n"
        "s_mov_b64 exec, -1\n"
        "v_mfma_f32_16x16x32_bf16 v[96:99], a[32:35], a[224:227], v[96:99]\n"
        "ds_read2_b32 a[2:3], v148 offset0:34 offset1:35\n"
        "s_waitcnt vmcnt(0)\n"
        "v_mfma_f32_16x16x32_bf16 v[100:103], a[32:35], a[228:231], v[100:103]\n"
        "s_mov_b64 exec, 0xffff\n"
        "global_store_dwordx2 v234, v[250:251], s[48:49] sc1\n"
        "s_mov_b64 exec, -1\n"
        "v_mfma_f32_16x16x32_bf16 v[100:103], a[36:39], a[224:227], v[100:103]\n"
        "ds_read_b128 a[192:195], v230 offset:0\n"
        "global_load_lds_dwordx4 v144, s[60:61] offset:0\n"
        "v_mfma_f32_16x16x32_bf16 v[104:107], a[40:43], a[224:227], v[104:107]\n"
        "ds_read_b64_tr_b16 a[100:101], v154 offset:0\n"
        "v_add_u32 v244, 0x10000, v254\n"
        "v_mfma_f32_16x16x32_bf16 v[104:107], a[36:39], a[228:231], v[104:107]\n"
        "ds_read_b64_tr_b16 a[102:103], v154 offset:4096\n"
        "v_add_u32 v245, 0x20000, v254\n"
        "v_mfma_f32_16x16x32_bf16 v[104:107], a[32:35], a[232:235], v[104:107]\n"
        "ds_read2_b32 a[4:5], v148 offset0:160 offset1:161\n"
        "v_add_u32 v230, 3840, v152\n"
        "v_mfma_f32_16x16x32_bf16 v[108:111], a[32:35], a[236:239], v[108:111]\n"
        "ds_read2_b32 a[6:7], v148 offset0:162 offset1:163\n"
        "global_load_lds_dwordx4 v145, s[60:61] offset:1024\n"
        "v_mfma_f32_16x16x32_bf16 v[108:111], a[36:39], a[232:235], v[108:111]\n"
        "ds_read_b128 a[196:199], v152 offset:3200\n"
        "s_add_u32 s14, s51, 504\n"
        "v_min_u32 v230, s16, v230\n"
        "v_mfma_f32_16x16x32_bf16 v[108:111], a[40:43], a[228:231], v[108:111]\n"
        "ds_read_b64_tr_b16 a[104:105], v153 offset:8192\n"
        "v_lshlrev_b32 v229, 5, v232\n"
        "v_mfma_f32_16x16x32_bf16 v[108:111], a[44:47], a[224:227], v[108:111]\n"
        "ds_read_b64_tr_b16 a[106:107], v153 offset:12288\n"
        "s_waitcnt lgkmcnt(8)\n"
        "v_mfma_f32_16x16x32_bf16 v[128:131], a[48:51], a[224:227], v[128:131]\n"
        "ds_read2_b32 a[8:9], v149 offset0:32 offset1:33\n"
        "global_load_lds_dwordx4 v146, s[60:61] offset:2048\n"
        "v_mfma_f32_16x16x32_bf16 v[128:131], a[44:47], a[228:231], v[128:131]\n"
        "ds_read2_b32 a[10:11], v149 offset0:34 offset1:35\n"
        "v_lshl_add_u32 v229, v233, 3, v229\n"
        "v_mfma_f32_16x16x32_bf16 v[128:131], a[40:43], a[232:235], v[128:131]\n"
        "ds_read_b128 a[200:203], v152 offset:2688\n"
        "v_sub_u32 v229, s14, v229\n"
        "v_mfma_f32_16x16x32_bf16 v[128:131], a[36:39], a[236:239], v[128:131]\n"
        "ds_read_b64_tr_b16 a[108:109], v154 offset:8192\n"
        "s_lshl_b32 s14, s41, 5\n"
        "s_add_u32 s14, s14, s42\n"
        "v_mfma_f32_16x16x32_bf16 v[128:131], a[32:35], a[240:243], v[128:131]\n"
        "ds_read_b64_tr_b16 a[110:111], v154 offset:12288\n"
        "global_load_lds_dwordx4 v147, s[60:61] offset:3072\n"
        "s_add_u32 m0, s73, 0xc000\n"
        "v_mfma_f32_16x16x32_bf16 v[132:135], a[32:35], a[244:247], v[132:135]\n"
        "ds_read2_b32 a[12:13], v149 offset0:160 offset1:161\n"
        "s_add_u32 s60, s22, s66\n"
        "s_addc_u32 s61, s23, 0\n"
        "v_mfma_f32_16x16x32_bf16 v[132:135], a[36:39], a[240:243], v[132:135]\n"
        "ds_read2_b32 a[14:15], v149 offset0:162 offset1:163\n"
        "s_xor_b32 s66, s29, 0x80000\n"
        "s_add_u32 s14, s14, 512\n"
        "v_mfma_f32_16x16x32_bf16 v[132:135], a[40:43], a[236:239], v[132:135]\n"
        "ds_read_b128 a[204:207], v152 offset:2176\n"
        "s_lshl_b32 s14, s14, 7\n"
        "v_lshrrev_b32 v234, 1, v254\n"
        "v_mfma_f32_16x16x32_bf16 v[132:135], a[44:47], a[232:235], v[132:135]\n"
        "ds_read_b64_tr_b16 a[112:113], v153 offset:16384\n"
        "global_load_lds_dwordx4 v144, s[60:61] offset:0\n"
        "v_mfma_f32_16x16x32_bf16 v[132:135], a[48:51], a[228:231], v[132:135]\n"
        "ds_read_b64_tr_b16 a[114:115], v153 offset:20480\n"
        "v_add_u32 v234, s14, v234\n"
        "v_mfma_f32_16x16x32_bf16 v[132:135], a[52:55], a[224:227], v[132:135]\n"
        "ds_read2_b32 a[16:17], v150 offset0:32 offset1:33\n"
        "s_lshr_b32 s14, s63, 2\n"
        "s_add_u32 s14, s14, 512\n"
        "v_mfma_f32_16x16x32_bf16 v[136:139], a[56:59], a[224:227], v[136:139]\n"
        "ds_read2_b32 a[18:19], v150 offset0:34 offset1:35\n"
        "s_lshl_b32 s14, s14, 7\n"
        "v_mfma_f32_16x16x32_bf16 v[136:139], a[52:55], a[228:231], v[136:139]\n"
        "ds_read_b128 a[208:211], v152 offset:1664\n"
        "global_load_lds_dwordx4 v145, s[60:61] offset:1024\n"
        "v_mfma_f32_16x16x32_bf16 v[136:139], a[48:51], a[232:235], v[136:139]\n"
        "ds_read_b64_tr_b16 a[116:117], v154 offset:16384\n"
        "v_mfma_f32_16x16x32_bf16 v[136:139], a[44:47], a[236:239], v[136:139]\n"
        "ds_read_b64_tr_b16 a[118:119], v154 offset:20480\n"
        "v_mfma_f32_16x16x32_bf16 v[136:139], a[40:43], a[240:243], v[136:139]\n"
        "ds_read2_b32 a[20:21], v150 offset0:160 offset1:161\n"
        "v_mfma_f32_16x16x32_bf16 v[136:139], a[36:39], a[244:247], v[136:139]\n"
        "ds_read2_b32 a[22:23], v150 offset0:162 offset1:163\n"
        "global_load_lds_dwordx4 v146, s[60:61] offset:2048\n"
        "v_mfma_f32_16x16x32_bf16 v[136:139], a[32:35], a[248:251], v[136:139]\n"
        "ds_read_b128 a[212:215], v152 offset:1152\n"
        "v_mfma_f32_16x16x32_bf16 v[140:143], a[32:35], a[252:255], v[140:143]\n"
        "ds_read_b64_tr_b16 a[120:121], v153 offset:24576\n"
        "v_mfma_f32_16x16x32_bf16 v[140:143], a[36:39], a[248:251], v[140:143]\n"
        "ds_read_b64_tr_b16 a[122:123], v153 offset:28672\n"
        "v_mfma_f32_16x16x32_bf16 v[140:143], a[40:43], a[244:247], v[140:143]\n"
        "ds_read_b64_tr_b16 a[124:125], v154 offset:24576\n"
        "global_load_lds_dwordx4 v147, s[60:61] offset:3072\n"
        "s_add_u32 m0, s73, 0x10000\n"
        "v_mfma_f32_16x16x32_bf16 v[140:143], a[44:47], a[240:243], v[140:143]\n"
        "ds_read_b64_tr_b16 a[126:127], v154 offset:28672\n"
        "s_add_u32 s60, s22, s66\n"
        "s_addc_u32 s61, s23, 0\n"
        "v_mfma_f32_16x16x32_bf16 v[140:143], a[48:51], a[236:239], v[140:143]\n"
        "ds_read2_b32 a[24:25], v151 offset0:32 offset1:33\n"
        "s_xor_b32 s66, s29, 0xa0000\n"
        "s_waitcnt lgkmcnt(8)\n"
        "v_mfma_f32_16x16x32_bf16 v[140:143], a[52:55], a[232:235], v[140:143]\n"
        "ds_read2_b32 a[26:27], v151 offset0:34 offset1:35\n"
        "v_mfma_f32_16x16x32_bf16 v[140:143], a[56:59], a[228:231], v[140:143]\n"
        "ds_read_b128 a[216:219], v152 offset:640\n"
        "global_load_lds_dwordx4 v144, s[60:61] offset:0\n"
        "v_mfma_f32_16x16x32_bf16 v[140:143], a[60:63], a[224:227], v[140:143]\n"
        "ds_read2_b32 a[28:29], v151 offset0:160 offset1:161\n"
        "v_mfma_f32_16x16x32_bf16 v[96:99], a[0:3], a[192:195], v[96:99]\n"
        "ds_read2_b32 a[30:31], v151 offset0:162 offset1:163\n"
        "v_mfma_f32_16x16x32_bf16 v[100:103], a[0:3], a[196:199], v[100:103]\n"
        "ds_read_b128 a[220:223], v152 offset:128\n"
        "v_mfma_f32_16x16x32_bf16 v[100:103], a[4:7], a[192:195], v[100:103]\n"
        "ds_read2_b32 a[32:33], v148 offset0:48 offset1:49\n"
        "global_load_lds_dwordx4 v145, s[60:61] offset:1024\n"
        "v_mfma_f32_16x16x32_bf16 v[104:107], a[8:11], a[192:195], v[104:107]\n"
        "ds_read2_b32 a[34:35], v148 offset0:50 offset1:51\n"
        "s_waitcnt lgkmcnt(8)\n"
        "v_mfma_f32_16x16x32_bf16 v[104:107], a[4:7], a[196:199], v[104:107]\n"
        "s_barrier\n"
        "v_mfma_f32_16x16x32_bf16 v[104:107], a[0:3], a[200:203], v[104:107]\n"
        "ds_read_b128 a[224:227], v231 offset:0\n"
        "v_add_u32 v231, 3904, v152\n"
        "v_mfma_f32_16x16x32_bf16 v[108:111], a[0:3], a[204:207], v[108:111]\n"
        "ds_read2_b32 a[36:37], v148 offset0:176 offset1:177\n"
        "global_load_lds_dwordx4 v146, s[60:61] offset:2048\n"
        "v_mfma_f32_16x16x32_bf16 v[108:111], a[4:7], a[200:203], v[108:111]\n"
        "ds_read2_b32 a[38:39], v148 offset0:178 offset1:179\n"
        "v_min_u32 v231, s16, v231\n"
        "v_mfma_f32_16x16x32_bf16 v[108:111], a[8:11], a[196:199], v[108:111]\n"
        "ds_read_b128 a[228:231], v152 offset:3264\n"
        "v_mfma_f32_16x16x32_bf16 v[108:111], a[12:15], a[192:195], v[108:111]\n"
        "ds_read2_b32 a[40:41], v149 offset0:48 offset1:49\n"
        "v_mfma_f32_16x16x32_bf16 v[128:131], a[16:19], a[192:195], v[128:131]\n"
        "ds_read2_b32 a[42:43], v149 offset0:50 offset1:51\n"
        "global_load_lds_dwordx4 v147, s[60:61] offset:3072\n"
        "s_add_u32 m0, s73, 0x14000\n"
        "v_mfma_f32_16x16x32_bf16 v[128:131], a[12:15], a[196:199], v[128:131]\n"
        "ds_read_b128 a[232:235], v152 offset:2752\n"
        "s_add_u32 s60, s22, s66\n"
        "s_addc_u32 s61, s23, 0\n"
        "v_mfma_f32_16x16x32_bf16 v[128:131], a[8:11], a[200:203], v[128:131]\n"
        "ds_read2_b32 a[44:45], v149 offset0:176 offset1:177\n"
        "s_xor_b32 s66, s29, 0xc0000\n"
        "v_mfma_f32_16x16x32_bf16 v[128:131], a[4:7], a[204:207], v[128:131]\n"
        "ds_read2_b32 a[46:47], v149 offset0:178 offset1:179\n"
        "v_mfma_f32_16x16x32_bf16 v[128:131], a[0:3], a[208:211], v[128:131]\n"
        "ds_read_b128 a[236:239], v152 offset:2240\n"
        "global_load_lds_dwordx4 v144, s[60:61] offset:0\n"
        "v_mfma_f32_16x16x32_bf16 v[132:135], a[0:3], a[212:215], v[132:135]\n"
        "ds_read2_b32 a[48:49], v150 offset0:48 offset1:49\n"
        "v_mfma_f32_16x16x32_bf16 v[132:135], a[4:7], a[208:211], v[132:135]\n"
        "ds_read2_b32 a[50:51], v150 offset0:50 offset1:51\n"
        "v_mfma_f32_16x16x32_bf16 v[132:135], a[8:11], a[204:207], v[132:135]\n"
        "ds_read_b128 a[240:243], v152 offset:1728\n"
        "s_waitcnt lgkmcnt(8)\n"
        "v_mfma_f32_16x16x32_bf16 v[132:135], a[12:15], a[200:203], v[132:135]\n"
        "ds_read2_b32 a[52:53], v150 offset0:176 offset1:177\n"
        "global_load_lds_dwordx4 v145, s[60:61] offset:1024\n"
        "v_mfma_f32_16x16x32_bf16 v[132:135], a[16:19], a[196:199], v[132:135]\n"
        "ds_read2_b32 a[54:55], v150 offset0:178 offset1:179\n"
        "v_mfma_f32_16x16x32_bf16 v[132:135], a[20:23], a[192:195], v[132:135]\n"
        "ds_read_b128 a[244:247], v152 offset:1216\n"
        "v_mfma_f32_16x16x32_bf16 v[136:139], a[24:27], a[192:195], v[136:139]\n"
        "ds_read2_b32 a[56:57], v151 offset0:48 offset1:49\n"
        "v_mfma_f32_16x16x32_bf16 v[136:139], a[20:23], a[196:199], v[136:139]\n"
        "ds_read2_b32 a[58:59], v151 offset0:50 offset1:51\n"
        "global_load_lds_dwordx4 v146, s[60:61] offset:2048\n"
        "v_mfma_f32_16x16x32_bf16 v[136:139], a[16:19], a[200:203], v[136:139]\n"
        "ds_read_b128 a[248:251], v152 offset:704\n"
        "v_mfma_f32_16x16x32_bf16 v[136:139], a[12:15], a[204:207], v[136:139]\n"
        "ds_read2_b32 a[60:61], v151 offset0:176 offset1:177\n"
        "v_mfma_f32_16x16x32_bf16 v[136:139], a[8:11], a[208:211], v[136:139]\n"
        "ds_read2_b32 a[62:63], v151 offset0:178 offset1:179\n"
        "v_mfma_f32_16x16x32_bf16 v[136:139], a[4:7], a[212:215], v[136:139]\n"
        "ds_read_b128 a[252:255], v152 offset:192\n"
        "global_load_lds_dwordx4 v147, s[60:61] offset:3072\n"
        "s_add_u32 m0, s73, 0x0\n"
        "v_mfma_f32_16x16x32_bf16 v[136:139], a[0:3], a[216:219], v[136:139]\n"
        "ds_read_b64 v[64:65], v229 offset:3584\n"
        "s_add_u32 s60, s22, s66\n"
        "s_addc_u32 s61, s23, 0\n"
        "v_mfma_f32_16x16x32_bf16 v[140:143], a[0:3], a[220:223], v[140:143]\n"
        "ds_read2_b32 a[0:1], v148 offset0:64 offset1:65\n"
        "s_xor_b32 s66, s29, 0xe0000\n"
        "v_mfma_f32_16x16x32_bf16 v[140:143], a[4:7], a[216:219], v[140:143]\n"
        "ds_read2_b32 a[2:3], v148 offset0:66 offset1:67\n"
        "v_mfma_f32_16x16x32_bf16 v[140:143], a[8:11], a[212:215], v[140:143]\n"
        "ds_read2_b32 a[4:5], v148 offset0:192 offset1:193\n"
        "global_load_lds_dwordx4 v144, s[60:61] offset:0\n"
        "v_mfma_f32_16x16x32_bf16 v[140:143], a[12:15], a[208:211], v[140:143]\n"
        "ds_read2_b32 a[6:7], v148 offset0:194 offset1:195\n"
        "v_mfma_f32_16x16x32_bf16 v[140:143], a[16:19], a[204:207], v[140:143]\n"
        "ds_read2_b32 a[8:9], v149 offset0:64 offset1:65\n"
        "v_mfma_f32_16x16x32_bf16 v[140:143], a[20:23], a[200:203], v[140:143]\n"
        "ds_read2_b32 a[10:11], v149 offset0:66 offset1:67\n"
        "v_mfma_f32_16x16x32_bf16 v[140:143], a[24:27], a[196:199], v[140:143]\n"
        "ds_read_b128 a[196:199], v152 offset:3328\n"
        "global_load_lds_dwordx4 v145, s[60:61] offset:1024\n"
        "v_mfma_f32_16x16x32_bf16 v[140:143], a[28:31], a[192:195], v[140:143]\n"
        "ds_read_b128 a[192:195], v230 offset:0\n"
        "v_add_u32 v230, 3968, v152\n"
        "v_mfma_f32_16x16x32_bf16 v[96:99], a[32:35], a[224:227], v[96:99]\n"
        "ds_read_b128 a[200:203], v152 offset:2816\n"
        "v_min_u32 v230, s16, v230\n"
        "v_mfma_f32_16x16x32_bf16 v[100:103], a[32:35], a[228:231], v[100:103]\n"
        "ds_read2_b32 a[12:13], v149 offset0:192 offset1:193\n"
        "s_waitcnt lgkmcnt(8)\n"
        "v_mfma_f32_16x16x32_bf16 v[100:103], a[36:39], a[224:227], v[100:103]\n"
        "ds_read2_b32 a[14:15], v149 offset0:194 offset1:195\n"
        "global_load_lds_dwordx4 v146, s[60:61] offset:2048\n"
        "v_mfma_f32_16x16x32_bf16 v[104:107], a[40:43], a[224:227], v[104:107]\n"
        "ds_read_b128 a[204:207], v152 offset:2304\n"
        "v_and_b32 v80, s35, v65\n"
        "v_mfma_f32_16x16x32_bf16 v[104:107], a[36:39], a[228:231], v[104:107]\n"
        "ds_read2_b32 a[16:17], v150 offset0:64 offset1:65\n"
        "v_lshlrev_b32 v81, 16, v65\n"
        "v_mfma_f32_16x16x32_bf16 v[104:107], a[32:35], a[232:235], v[104:107]\n"
        "ds_read2_b32 a[18:19], v150 offset0:66 offset1:67\n"
        "v_and_b32 v82, s35, v64\n"
        "v_mfma_f32_16x16x32_bf16 v[108:111], a[32:35], a[236:239], v[108:111]\n"
        "ds_read_b128 a[208:211], v152 offset:1792\n"
        "global_load_lds_dwordx4 v147, s[60:61] offset:3072\n"
        "s_add_u32 m0, s73, 0x4000\n"
        "v_mfma_f32_16x16x32_bf16 v[108:111], a[36:39], a[232:235], v[108:111]\n"
        "ds_read2_b32 a[20:21], v150 offset0:192 offset1:193\n"
        "s_add_u32 s60, s22, s66\n"
        "s_addc_u32 s61, s23, 0\n"
        "v_mfma_f32_16x16x32_bf16 v[108:111], a[40:43], a[228:231], v[108:111]\n"
        "ds_read2_b32 a[22:23], v150 offset0:194 offset1:195\n"
        "s_waitcnt vmcnt(12)\n"
        "v_mfma_f32_16x16x32_bf16 v[108:111], a[44:47], a[224:227], v[108:111]\n"
        "s_barrier\n"
        "v_mfma_f32_16x16x32_bf16 v[128:131], a[48:51], a[224:227], v[128:131]\n"
        "ds_read_b64_tr_b16 a[128:129], v153 offset:32768\n"
        "LHC2_%=:\n"
        "s_cbranch_execz LHRC_%=\n"
        "global_load_lds_dwordx4 v144, s[60:61] offset:0\n"
        "s_xor_b32 s66, s29, 0x100000\n"
        "v_mfma_f32_16x16x32_bf16 v[128:131], a[44:47], a[228:231], v[128:131]\n"
        "ds_read_b64_tr_b16 a[130:131], v153 offset:36864\n"
        "v_lshlrev_b32 v83, 16, v64\n"
        "v_mfma_f32_16x16x32_bf16 v[128:131], a[40:43], a[232:235], v[128:131]\n"
        "ds_read_b64_tr_b16 a[132:133], v154 offset:32768\n"
        "v_mul_f32 v80, s54, v80\n"
        "v_mfma_f32_16x16x32_bf16 v[128:131], a[36:39], a[236:239], v[128:131]\n"
        "ds_read_b64_tr_b16 a[134:135], v154 offset:36864\n"
        "v_mul_f32 v81, s54, v81\n"
        "v_mfma_f32_16x16x32_bf16 v[128:131], a[32:35], a[240:243], v[128:131]\n"
        "ds_read_b64_tr_b16 a[136:137], v153 offset:40960\n"
        "global_load_lds_dwordx4 v145, s[60:61] offset:1024\n"
        "v_mfma_f32_16x16x32_bf16 v[132:135], a[32:35], a[244:247], v[132:135]\n"
        "ds_read_b64_tr_b16 a[138:139], v153 offset:45056\n"
        "v_mul_f32 v82, s54, v82\n"
        "v_mfma_f32_16x16x32_bf16 v[132:135], a[36:39], a[240:243], v[132:135]\n"
        "ds_read_b64_tr_b16 a[140:141], v154 offset:40960\n"
        "v_mul_f32 v83, s54, v83\n"
        "v_mfma_f32_16x16x32_bf16 v[132:135], a[40:43], a[236:239], v[132:135]\n"
        "ds_read_b64_tr_b16 a[142:143], v154 offset:45056\n"
        "v_cvt_pk_bf16_f32 v80, 0, v80\n"
        "v_mfma_f32_16x16x32_bf16 v[132:135], a[44:47], a[232:235], v[132:135]\n"
        "ds_read_b64_tr_b16 a[144:145], v153 offset:49152\n"
        "global_load_lds_dwordx4 v146, s[60:61] offset:2048\n"
        "v_mfma_f32_16x16x32_bf16 v[132:135], a[48:51], a[228:231], v[132:135]\n"
        "ds_read_b64_tr_b16 a[146:147], v153 offset:53248\n"
        "v_cvt_pk_bf16_f32 v81, 0, v81\n"
        "v_mfma_f32_16x16x32_bf16 v[132:135], a[52:55], a[224:227], v[132:135]\n"
        "ds_read_b64_tr_b16 a[148:149], v154 offset:49152\n"
        "v_cvt_pk_bf16_f32 v82, 0, v82\n"
        "v_mfma_f32_16x16x32_bf16 v[136:139], a[56:59], a[224:227], v[136:139]\n"
        "ds_read_b64_tr_b16 a[150:151], v154 offset:53248\n"
        "v_cvt_pk_bf16_f32 v83, 0, v83\n"
        "v_mfma_f32_16x16x32_bf16 v[136:139], a[52:55], a[228:231], v[136:139]\n"
        "ds_read_b128 a[212:215], v152 offset:1280\n"
        "global_load_lds_dwordx4 v147, s[60:61] offset:3072\n"
        "s_add_u32 m0, s73, 0x8000\n"
        "v_mfma_f32_16x16x32_bf16 v[136:139], a[48:51], a[232:235], v[136:139]\n"
        "ds_read_b64_tr_b16 a[152:153], v153 offset:57344\n"
        "s_add_u32 s60, s22, s66\n"
        "s_addc_u32 s61, s23, 0\n"
        "v_mfma_f32_16x16x32_bf16 v[136:139], a[44:47], a[236:239], v[136:139]\n"
        "ds_read_b64_tr_b16 a[154:155], v153 offset:61440\n"
        "s_xor_b32 s66, s29, 0x120000\n"
        "v_mfma_f32_16x16x32_bf16 v[136:139], a[40:43], a[240:243], v[136:139]\n"
        "ds_read_b64_tr_b16 a[156:157], v154 offset:57344\n"
        "v_mfma_f32_16x16x32_bf16 v[136:139], a[36:39], a[244:247], v[136:139]\n"
        "ds_read_b64_tr_b16 a[158:159], v154 offset:61440\n"
        "v_mfma_f32_16x16x32_bf16 v[136:139], a[32:35], a[248:251], v[136:139]\n"
        "ds_read2_b32 a[24:25], v151 offset0:64 offset1:65\n"
        "v_mfma_f32_16x16x32_bf16 v[140:143], a[32:35], a[252:255], v[140:143]\n"
        "ds_read2_b32 a[26:27], v151 offset0:66 offset1:67\n"
        "v_mfma_f32_16x16x32_bf16 v[140:143], a[36:39], a[248:251], v[140:143]\n"
        "ds_read_b128 a[216:219], v152 offset:768\n"
        "v_mfma_f32_16x16x32_bf16 v[140:143], a[40:43], a[244:247], v[140:143]\n"
        "ds_read2_b32 a[28:29], v151 offset0:192 offset1:193\n"
        "v_mfma_f32_16x16x32_bf16 v[140:143], a[44:47], a[240:243], v[140:143]\n"
        "ds_read2_b32 a[30:31], v151 offset0:194 offset1:195\n"
        "v_mfma_f32_16x16x32_bf16 v[140:143], a[48:51], a[236:239], v[140:143]\n"
        "ds_read_b128 a[220:223], v152 offset:256\n"
        "s_waitcnt lgkmcnt(8)\n"
        "v_mfma_f32_16x16x32_bf16 v[140:143], a[52:55], a[232:235], v[140:143]\n"
        "ds_read2_b32 a[32:33], v148 offset0:80 offset1:81\n"
        "v_mfma_f32_16x16x32_bf16 v[140:143], a[56:59], a[228:231], v[140:143]\n"
        "ds_read2_b32 a[34:35], v148 offset0:82 offset1:83\n"
        "v_mfma_f32_16x16x32_bf16 v[140:143], a[60:63], a[224:227], v[140:143]\n"
        "ds_read_b128 a[224:227], v231 offset:0\n"
        "v_add_u32 v231, 4032, v152\n"
        "v_mfma_f32_16x16x32_bf16 v[96:99], a[0:3], a[192:195], v[96:99]\n"
        "ds_read2_b32 a[36:37], v148 offset0:208 offset1:209\n"
        "v_min_u32 v231, s16, v231\n"
        "v_mfma_f32_16x16x32_bf16 v[100:103], a[0:3], a[196:199], v[100:103]\n"
        "ds_read2_b32 a[38:39], v148 offset0:210 offset1:211\n"
        "v_mfma_f32_16x16x32_bf16 v[100:103], a[4:7], a[192:195], v[100:103]\n"
        "ds_read_b128 a[228:231], v152 offset:3392\n"
        "v_mfma_f32_16x16x32_bf16 v[104:107], a[8:11], a[192:195], v[104:107]\n"
        "ds_read2_b32 a[40:41], v149 offset0:80 offset1:81\n"
        "v_mfma_f32_16x16x32_bf16 v[104:107], a[4:7], a[196:199], v[104:107]\n"
        "ds_read2_b32 a[42:43], v149 offset0:82 offset1:83\n"
        "s_waitcnt vmcnt(8) lgkmcnt(14)\n"
        "v_mfma_f32_16x16x32_bf16 v[104:107], a[0:3], a[200:203], v[104:107]\n"
        "s_barrier\n"
        "v_mfma_f32_16x16x32_bf16 v[108:111], a[0:3], a[204:207], v[108:111]\n"
        "ds_read_b64_tr_b16 v[160:161], v155 offset:0\n"
        "global_load_lds_dwordx4 v144, s[60:61] offset:0\n"
        "v_mfma_f32_16x16x32_bf16 v[108:111], a[4:7], a[200:203], v[108:111]\n"
        "ds_read_b64_tr_b16 v[162:163], v155 offset:4096\n"
        "v_mfma_f32_16x16x32_bf16 v[108:111], a[8:11], a[196:199], v[108:111]\n"
        "ds_read_b64_tr_b16 v[164:165], v156 offset:0\n"
        "v_mfma_f32_16x16x32_bf16 v[108:111], a[12:15], a[192:195], v[108:111]\n"
        "ds_read_b64_tr_b16 v[166:167], v156 offset:4096\n"
        "v_mfma_f32_16x16x32_bf16 v[128:131], a[16:19], a[192:195], v[128:131]\n"
        "ds_read_b64_tr_b16 v[168:169], v155 offset:8192\n"
        "global_load_lds_dwordx4 v145, s[60:61] offset:1024\n"
        "v_mfma_f32_16x16x32_bf16 v[128:131], a[12:15], a[196:199], v[128:131]\n"
        "ds_read_b64_tr_b16 v[170:171], v155 offset:12288\n"
        "v_mfma_f32_16x16x32_bf16 v[128:131], a[8:11], a[200:203], v[128:131]\n"
        "ds_read_b128 a[232:235], v152 offset:2880\n"
        "v_mfma_f32_16x16x32_bf16 v[128:131], a[4:7], a[204:207], v[128:131]\n"
        "ds_read_b64_tr_b16 v[172:173], v156 offset:8192\n"
        "v_mfma_f32_16x16x32_bf16 v[128:131], a[0:3], a[208:211], v[128:131]\n"
        "ds_read_b64_tr_b16 v[174:175], v156 offset:12288\n"
        "global_load_lds_dwordx4 v146, s[60:61] offset:2048\n"
        "v_mfma_f32_16x16x32_bf16 v[132:135], a[0:3], a[212:215], v[132:135]\n"
        "ds_read2_b32 a[44:45], v149 offset0:208 offset1:209\n"
        "v_mfma_f32_16x16x32_bf16 v[132:135], a[4:7], a[208:211], v[132:135]\n"
        "ds_read2_b32 a[46:47], v149 offset0:210 offset1:211\n"
        "v_mfma_f32_16x16x32_bf16 v[132:135], a[8:11], a[204:207], v[132:135]\n"
        "ds_read_b128 a[236:239], v152 offset:2368\n"
        "s_waitcnt lgkmcnt(8)\n"
        "v_mfma_f32_16x16x32_bf16 v[132:135], a[12:15], a[200:203], v[132:135]\n"
        "ds_read_b64_tr_b16 v[176:177], v155 offset:16384\n"
        "global_load_lds_dwordx4 v147, s[60:61] offset:3072\n"
        "s_add_u32 m0, s73, 0xc000\n"
        "v_mfma_f32_16x16x32_bf16 v[132:135], a[16:19], a[196:199], v[132:135]\n"
        "ds_read_b64_tr_b16 v[178:179], v155 offset:20480\n"
        "s_add_u32 s60, s22, s66\n"
        "s_addc_u32 s61, s23, 0\n"
        "v_mfma_f32_16x16x32_bf16 v[132:135], a[20:23], a[192:195], v[132:135]\n"
        "ds_read2_b32 a[48:49], v150 offset0:80 offset1:81\n"
        "s_xor_b32 s66, s29, 0x140000\n"
        "v_mfma_f32_16x16x32_bf16 v[136:139], a[24:27], a[192:195], v[136:139]\n"
        "ds_read2_b32 a[50:51], v150 offset0:82 offset1:83\n"
        "v_mfma_f32_16x16x32_bf16 v[136:139], a[20:23], a[196:199], v[136:139]\n"
        "ds_read_b128 a[240:243], v152 offset:1856\n"
        "global_load_lds_dwordx4 v144, s[60:61] offset:0\n"
        "v_mfma_f32_16x16x32_bf16 v[136:139], a[16:19], a[200:203], v[136:139]\n"
        "ds_read_b64_tr_b16 v[180:181], v156 offset:16384\n"
        "v_mfma_f32_16x16x32_bf16 v[136:139], a[12:15], a[204:207], v[136:139]\n"
        "ds_read_b64_tr_b16 v[182:183], v156 offset:20480\n"
        "v_mfma_f32_16x16x32_bf16 v[136:139], a[8:11], a[208:211], v[136:139]\n"
        "ds_read2_b32 a[52:53], v150 offset0:208 offset1:209\n"
        "v_mfma_f32_16x16x32_bf16 v[136:139], a[4:7], a[212:215], v[136:139]\n"
        "ds_read2_b32 a[54:55], v150 offset0:210 offset1:211\n"
        "global_load_lds_dwordx4 v145, s[60:61] offset:1024\n"
        "v_mfma_f32_16x16x32_bf16 v[136:139], a[0:3], a[216:219], v[136:139]\n"
        "ds_read_b128 a[244:247], v152 offset:1344\n"
        "v_mfma_f32_16x16x32_bf16 v[140:143], a[0:3], a[220:223], v[140:143]\n"
        "ds_read_b64_tr_b16 v[184:185], v155 offset:24576\n"
        "v_mfma_f32_16x16x32_bf16 v[140:143], a[4:7], a[216:219], v[140:143]\n"
        "ds_read_b64_tr_b16 v[186:187], v155 offset:28672\n"
        "v_mfma_f32_16x16x32_bf16 v[140:143], a[8:11], a[212:215], v[140:143]\n"
        "ds_read2_b32 a[56:57], v151 offset0:80 offset1:81\n"
        "global_load_lds_dwordx4 v146, s[60:61] offset:2048\n"
        "v_mfma_f32_16x16x32_bf16 v[140:143], a[12:15], a[208:211], v[140:143]\n"
        "ds_read2_b32 a[58:59], v151 offset0:82 offset1:83\n"
        "v_mfma_f32_16x16x32_bf16 v[140:143], a[16:19], a[204:207], v[140:143]\n"
        "ds_read_b128 a[248:251], v152 offset:832\n"
        "v_mfma_f32_16x16x32_bf16 v[140:143], a[20:23], a[200:203], v[140:143]\n"
        "ds_read_b64_tr_b16 v[188:189], v156 offset:24576\n"
        "v_mfma_f32_16x16x32_bf16 v[140:143], a[24:27], a[196:199], v[140:143]\n"
        "ds_read_b64_tr_b16 v[190:191], v156 offset:28672\n"
        "global_load_lds_dwordx4 v147, s[60:61] offset:3072\n"
        "s_add_u32 m0, s73, 0x10000\n"
        "v_mfma_f32_16x16x32_bf16 v[140:143], a[28:31], a[192:195], v[140:143]\n"
        "ds_read2_b32 a[60:61], v151 offset0:208 offset1:209\n"
        "s_add_u32 s60, s22, s66\n"
        "s_addc_u32 s61, s23, 0\n"
        "v_mfma_f32_16x16x32_bf16 v[96:99], a[32:35], a[224:227], v[96:99]\n"
        "ds_read2_b32 a[62:63], v151 offset0:210 offset1:211\n"
        "s_xor_b32 s66, s29, 0x160000\n"
        "v_mfma_f32_16x16x32_bf16 v[100:103], a[32:35], a[228:231], v[100:103]\n"
        "ds_read_b128 a[252:255], v152 offset:320\n"
        "s_waitcnt lgkmcnt(8)\n"
        "v_mfma_f32_16x16x32_bf16 v[100:103], a[36:39], a[224:227], v[100:103]\n"
        "ds_read2_b32 a[0:1], v148 offset0:96 offset1:97\n"
        "global_load_dwordx2 v[236:237], v159, s[48:49] sc1\n"
        "v_mfma_f32_16x16x32_bf16 v[104:107], a[40:43], a[224:227], v[104:107]\n"
        "ds_read2_b32 a[2:3], v148 offset0:98 offset1:99\n"
        "v_mfma_f32_16x16x32_bf16 v[104:107], a[36:39], a[228:231], v[104:107]\n"
        "ds_read_b128 a[192:195], v230 offset:0\n"
        "v_lshrrev_b32 v230, 1, v233\n"
        "v_mfma_f32_16x16x32_bf16 v[104:107], a[32:35], a[232:235], v[104:107]\n"
        "ds_read2_b32 a[4:5], v148 offset0:224 offset1:225\n"
        "v_mfma_f32_16x16x32_bf16 v[108:111], a[32:35], a[236:239], v[108:111]\n"
        "ds_read2_b32 a[6:7], v148 offset0:226 offset1:227\n"
        "s_waitcnt vmcnt(9) lgkmcnt(8)\n"
        "v_mfma_f32_16x16x32_bf16 v[108:111], a[36:39], a[232:235], v[108:111]\n"
        "s_barrier\n"
        "v_mfma_f32_16x16x32_bf16 v[108:111], a[40:43], a[228:231], v[108:111]\n"
        "ds_read_b64_tr_b16 v[192:193], v153 offset:0\n"
        "global_load_lds_dwordx4 v144, s[60:61] offset:0\n"
        "v_mfma_f32_16x16x32_bf16 v[108:111], a[44:47], a[224:227], v[108:111]\n"
        "ds_read_b64_tr_b16 v[194:195], v153 offset:4096\n"
        "v_mfma_f32_16x16x32_bf16 v[128:131], a[48:51], a[224:227], v[128:131]\n"
        "ds_read_b64_tr_b16 v[196:197], v154 offset:0\n"
        "v_mfma_f32_16x16x32_bf16 v[128:131], a[44:47], a[228:231], v[128:131]\n"
        "ds_read_b64_tr_b16 v[198:199], v154 offset:4096\n"
        "v_mfma_f32_16x16x32_bf16 v[128:131], a[40:43], a[232:235], v[128:131]\n"
        "ds_read_b128 a[196:199], v152 offset:3456\n"
        "global_load_lds_dwordx4 v145, s[60:61] offset:1024\n"
        "v_mfma_f32_16x16x32_bf16 v[128:131], a[36:39], a[236:239], v[128:131]\n"
        "ds_read_b64_tr_b16 v[200:201], v153 offset:8192\n"
        "v_mfma_f32_16x16x32_bf16 v[128:131], a[32:35], a[240:243], v[128:131]\n"
        "ds_read_b64_tr_b16 v[202:203], v153 offset:12288\n"
        "v_mfma_f32_16x16x32_bf16 v[132:135], a[32:35], a[244:247], v[132:135]\n"
        "ds_read2_b32 a[8:9], v149 offset0:96 offset1:97\n"
        "v_mfma_f32_16x16x32_bf16 v[132:135], a[36:39], a[240:243], v[132:135]\n"
        "ds_read2_b32 a[10:11], v149 offset0:98 offset1:99\n"
        "global_load_lds_dwordx4 v146, s[60:61] offset:2048\n"
        "v_mfma_f32_16x16x32_bf16 v[132:135], a[40:43], a[236:239], v[132:135]\n"
        "ds_read_b128 a[200:203], v152 offset:2944\n"
        "v_mfma_f32_16x16x32_bf16 v[132:135], a[44:47], a[232:235], v[132:135]\n"
        "ds_read_b64_tr_b16 v[204:205], v154 offset:8192\n"
        "v_mfma_f32_16x16x32_bf16 v[132:135], a[48:51], a[228:231], v[132:135]\n"
        "ds_read_b64_tr_b16 v[206:207], v154 offset:12288\n"
        "v_mfma_f32_16x16x32_bf16 v[132:135], a[52:55], a[224:227], v[132:135]\n"
        "ds_read2_b32 a[12:13], v149 offset0:224 offset1:225\n"
        "global_load_lds_dwordx4 v147, s[60:61] offset:3072\n"
        "s_add_u32 m0, s73, 0x14000\n"
        "v_mfma_f32_16x16x32_bf16 v[136:139], a[56:59], a[224:227], v[136:139]\n"
        "ds_read2_b32 a[14:15], v149 offset0:226 offset1:227\n"
        "s_add_u32 s60, s22, s66\n"
        "s_addc_u32 s61, s23, 0\n"
        "v_mfma_f32_16x16x32_bf16 v[136:139], a[52:55], a[228:231], v[136:139]\n"
        "ds_read_b128 a[204:207], v152 offset:2432\n"
        "s_sub_u32 s66, 15, s12\n"
        "v_mfma_f32_16x16x32_bf16 v[136:139], a[48:51], a[232:235], v[136:139]\n"
        "ds_read_b64_tr_b16 v[208:209], v153 offset:16384\n"
        "s_cmp_lt_u32 s74, 2\n"
        "s_cselect_b32 s66, s12, s66\n"
        "v_mfma_f32_16x16x32_bf16 v[136:139], a[44:47], a[236:239], v[136:139]\n"
        "ds_read_b64_tr_b16 v[210:211], v153 offset:20480\n"
        "global_load_lds_dwordx4 v144, s[60:61] offset:0\n"
        "s_mul_i32 s66, s66, 1052672\n"
        "v_mfma_f32_16x16x32_bf16 v[136:139], a[40:43], a[240:243], v[136:139]\n"
        "ds_read2_b32 a[16:17], v150 offset0:96 offset1:97\n"
        "s_add_u32 s66, s66, s67\n"
        "s_and_b32 s67, s75, 1\n"
        "v_mfma_f32_16x16x32_bf16 v[136:139], a[36:39], a[244:247], v[136:139]\n"
        "ds_read2_b32 a[18:19], v150 offset0:98 offset1:99\n"
        "s_add_u32 s68, s20, s66\n"
        "s_addc_u32 s69, s21, 0\n"
        "v_mfma_f32_16x16x32_bf16 v[136:139], a[32:35], a[248:251], v[136:139]\n"
        "ds_read_b128 a[208:211], v152 offset:1920\n"
        "s_xor_b32 s66, s17, 0\n"
        "s_waitcnt lgkmcnt(8)\n"
        "v_mfma_f32_16x16x32_bf16 v[140:143], a[32:35], a[252:255], v[140:143]\n"
        "ds_read_b64_tr_b16 v[212:213], v154 offset:16384\n"
        "global_load_lds_dwordx4 v145, s[60:61] offset:1024\n"
        "s_lshl_b32 s66, s66, 15\n"
        "v_mfma_f32_16x16x32_bf16 v[140:143], a[36:39], a[248:251], v[140:143]\n"
        "ds_read_b64_tr_b16 v[214:215], v154 offset:20480\n"
        "s_lshl_b32 s67, s67, 12\n"
        "v_mfma_f32_16x16x32_bf16 v[140:143], a[40:43], a[244:247], v[140:143]\n"
        "ds_read2_b32 a[20:21], v150 offset0:224 offset1:225\n"
        "v_mfma_f32_16x16x32_bf16 v[140:143], a[44:47], a[240:243], v[140:143]\n"
        "ds_read2_b32 a[22:23], v150 offset0:226 offset1:227\n"
        "v_mfma_f32_16x16x32_bf16 v[140:143], a[48:51], a[236:239], v[140:143]\n"
        "ds_read_b128 a[212:215], v152 offset:1408\n"
        "global_load_lds_dwordx4 v146, s[60:61] offset:2048\n"
        "v_mfma_f32_16x16x32_bf16 v[140:143], a[52:55], a[232:235], v[140:143]\n"
        "ds_read_b64_tr_b16 v[216:217], v153 offset:24576\n"
        "s_waitcnt vmcnt(7) lgkmcnt(8)\n"
        "v_mfma_f32_16x16x32_bf16 v[140:143], a[56:59], a[228:231], v[140:143]\n"
        "ds_read_b64_tr_b16 v[218:219], v153 offset:28672\n"
        "v_cmp_ne_u64 vcc, v[236:237], v[250:251]\n"
        "v_mfma_f32_16x16x32_bf16 v[140:143], a[60:63], a[224:227], v[140:143]\n"
        "ds_read2_b32 a[24:25], v151 offset0:96 offset1:97\n"
        "s_cbranch_vccnz LPCX_%=\n"
        "LBCX_%=:\n"
        "v_mfma_f32_16x16x32_bf16 v[96:99], a[0:3], a[192:195], v[96:99]\n"
        "ds_read2_b32 a[26:27], v151 offset0:98 offset1:99\n"
        "global_load_lds_dwordx4 v147, s[60:61] offset:3072\n"
        "s_add_u32 m0, s73, 0x0\n"
        "v_mfma_f32_16x16x32_bf16 v[100:103], a[0:3], a[196:199], v[100:103]\n"
        "ds_read_b128 a[216:219], v152 offset:896\n"
        "s_add_u32 s60, s68, s66\n"
        "s_addc_u32 s61, s69, 0\n"
        "v_mfma_f32_16x16x32_bf16 v[100:103], a[4:7], a[192:195], v[100:103]\n"
        "ds_read_b64_tr_b16 v[220:221], v154 offset:24576\n"
        "s_xor_b32 s66, s17, 1\n"
        "s_lshl_b32 s66, s66, 15\n"
        "v_mfma_f32_16x16x32_bf16 v[104:107], a[8:11], a[192:195], v[104:107]\n"
        "ds_read_b64_tr_b16 v[222:223], v154 offset:28672\n"
        "v_mfma_f32_16x16x32_bf16 v[104:107], a[4:7], a[196:199], v[104:107]\n"
        "ds_read2_b32 a[28:29], v151 offset0:224 offset1:225\n"
        "global_load_dword v224, v159, s[48:49] offset:64 sc1\n"
        "v_mfma_f32_16x16x32_bf16 v[104:107], a[0:3], a[200:203], v[104:107]\n"
        "ds_read2_b32 a[30:31], v151 offset0:226 offset1:227\n"
        "v_mfma_f32_16x16x32_bf16 v[108:111], a[0:3], a[204:207], v[108:111]\n"
        "ds_read_b128 a[220:223], v152 offset:384\n"
        "s_waitcnt lgkmcnt(8)\n"
        "v_mfma_f32_16x16x32_bf16 v[108:111], a[4:7], a[200:203], v[108:111]\n"
        "ds_read2_b32 a[32:33], v148 offset0:112 offset1:113\n"
        "v_mfma_f32_16x16x32_bf16 v[108:111], a[8:11], a[196:199], v[108:111]\n"
        "ds_read2_b32 a[34:35], v148 offset0:114 offset1:115\n"
        "v_mfma_f32_16x16x32_bf16 v[108:111], a[12:15], a[192:195], v[108:111]\n"
        "ds_read_b128 a[224:227], v231 offset:0\n"
        "v_mfma_f32_16x16x32_bf16 v[128:131], a[16:19], a[192:195], v[128:131]\n"
        "ds_read2_b32 a[36:37], v148 offset0:240 offset1:241\n"
        "v_mfma_f32_16x16x32_bf16 v[128:131], a[12:15], a[196:199], v[128:131]\n"
        "ds_read2_b32 a[38:39], v148 offset0:242 offset1:243\n"
        "v_mfma_f32_16x16x32_bf16 v[128:131], a[8:11], a[200:203], v[128:131]\n"
        "ds_read_b128 a[228:231], v152 offset:3520\n"
        "s_waitcnt lgkmcnt(9)\n"
        "v_mfma_f32_16x16x32_bf16 v[128:131], a[4:7], a[204:207], v[128:131]\n"
        "s_barrier\n"
        "v_mfma_f32_16x16x32_bf16 v[128:131], a[0:3], a[208:211], v[128:131]\n"
        "ds_read_b64_tr_b16 v[112:113], v153 offset:32768\n"
        "global_load_lds_dwordx4 v254, s[60:61] offset:0 sc1\n"
        "v_mfma_f32_16x16x32_bf16 v[132:135], a[0:3], a[212:215], v[132:135]\n"
        "ds_read_b64_tr_b16 v[114:115], v153 offset:36864\n"
        "v_mfma_f32_16x16x32_bf16 v[132:135], a[4:7], a[208:211], v[132:135]\n"
        "ds_read_b64_tr_b16 v[116:117], v154 offset:32768\n"
        "v_mfma_f32_16x16x32_bf16 v[132:135], a[8:11], a[204:207], v[132:135]\n"
        "ds_read_b64_tr_b16 v[118:119], v154 offset:36864\n"
        "v_mfma_f32_16x16x32_bf16 v[132:135], a[12:15], a[200:203], v[132:135]\n"
        "ds_read_b64_tr_b16 v[120:121], v153 offset:40960\n"
        "global_load_lds_dwordx4 v254, s[60:61] offset:1024 sc1\n"
        "v_mfma_f32_16x16x32_bf16 v[132:135], a[16:19], a[196:199], v[132:135]\n"
        "ds_read_b64_tr_b16 v[122:123], v153 offset:45056\n"
        "v_mfma_f32_16x16x32_bf16 v[132:135], a[20:23], a[192:195], v[132:135]\n"
        "ds_read2_b32 a[40:41], v149 offset0:112 offset1:113\n"
        "v_mfma_f32_16x16x32_bf16 v[136:139], a[24:27], a[192:195], v[136:139]\n"
        "ds_read2_b32 a[42:43], v149 offset0:114 offset1:115\n"
        "v_mfma_f32_16x16x32_bf16 v[136:139], a[20:23], a[196:199], v[136:139]\n"
        "ds_read_b128 a[232:235], v152 offset:3008\n"
        "global_load_lds_dwordx4 v254, s[60:61] offset:2048 sc1\n"
        "v_mfma_f32_16x16x32_bf16 v[136:139], a[16:19], a[200:203], v[136:139]\n"
        "ds_read_b64_tr_b16 v[124:125], v154 offset:40960\n"
        "v_mfma_f32_16x16x32_bf16 v[136:139], a[12:15], a[204:207], v[136:139]\n"
        "ds_read_b64_tr_b16 v[126:127], v154 offset:45056\n"
        "s_waitcnt lgkmcnt(8)\n"
        "v_mfma_f32_16x16x32_bf16 v[136:139], a[8:11], a[208:211], v[136:139]\n"
        "ds_read2_b32 a[44:45], v149 offset0:240 offset1:241\n"
        "v_mfma_f32_16x16x32_bf16 v[136:139], a[4:7], a[212:215], v[136:139]\n"
        "ds_read2_b32 a[46:47], v149 offset0:242 offset1:243\n"
        "global_load_lds_dwordx4 v254, s[60:61] offset:3072 sc1\n"
        "s_add_u32 m0, s73, 0x4000\n"
        "v_mfma_f32_16x16x32_bf16 v[136:139], a[0:3], a[216:219], v[136:139]\n"
        "ds_read_b128 a[236:239], v152 offset:2496\n"
        "s_add_u32 s60, s68, s66\n"
        "s_addc_u32 s61, s69, 0\n"
        "v_mfma_f32_16x16x32_bf16 v[140:143], a[0:3], a[220:223], v[140:143]\n"
        "ds_read_b64_tr_b16 a[64:65], v153 offset:49152\n"
        "s_xor_b32 s66, s29, 0x180000\n"
        "v_mfma_f32_16x16x32_bf16 v[140:143], a[4:7], a[216:219], v[140:143]\n"
        "ds_read_b64_tr_b16 a[66:67], v153 offset:53248\n"
        "v_mfma_f32_16x16x32_bf16 v[140:143], a[8:11], a[212:215], v[140:143]\n"
        "ds_read2_b32 a[48:49], v150 offset0:112 offset1:113\n"
        "global_load_lds_dwordx4 v254, s[60:61] offset:0 sc1\n"
        "v_mfma_f32_16x16x32_bf16 v[140:143], a[12:15], a[208:211], v[140:143]\n"
        "ds_read2_b32 a[50:51], v150 offset0:114 offset1:115\n"
        "v_mfma_f32_16x16x32_bf16 v[140:143], a[16:19], a[204:207], v[140:143]\n"
        "ds_read_b128 a[240:243], v152 offset:1984\n"
        "v_mfma_f32_16x16x32_bf16 v[140:143], a[20:23], a[200:203], v[140:143]\n"
        "ds_read_b64_tr_b16 a[68:69], v154 offset:49152\n"
        "v_mfma_f32_16x16x32_bf16 v[140:143], a[24:27], a[196:199], v[140:143]\n"
        "ds_read_b64_tr_b16 a[70:71], v154 offset:53248\n"
        "global_load_lds_dwordx4 v254, s[60:61] offset:1024 sc1\n"
        "v_mfma_f32_16x16x32_bf16 v[140:143], a[28:31], a[192:195], v[140:143]\n"
        "ds_read2_b32 a[52:53], v150 offset0:240 offset1:241\n"
        "s_waitcnt lgkmcnt(8)\n"
        "v_mfma_f32_16x16x32_bf16 v[96:99], a[32:35], a[224:227], v[96:99]\n"
        "ds_read2_b32 a[54:55], v150 offset0:242 offset1:243\n"
        "v_mfma_f32_16x16x32_bf16 v[100:103], a[32:35], a[228:231], v[100:103]\n"
        "ds_read_b128 a[244:247], v152 offset:1472\n"
        "v_mfma_f32_16x16x32_bf16 v[100:103], a[36:39], a[224:227], v[100:103]\n"
        "ds_read_b64_tr_b16 a[72:73], v153 offset:57344\n"
        "global_load_lds_dwordx4 v254, s[60:61] offset:2048 sc1\n"
        "v_mfma_f32_16x16x32_bf16 v[104:107], a[40:43], a[224:227], v[104:107]\n"
        "ds_read_b64_tr_b16 a[74:75], v153 offset:61440\n"
        "v_cvt_pk_bf16_f32 v96, 0, v96\n"
        "v_mfma_f32_16x16x32_bf16 v[104:107], a[36:39], a[228:231], v[104:107]\n"
        "ds_read_b64_tr_b16 a[76:77], v154 offset:57344\n"
        "v_cvt_pk_bf16_f32 v97, 0, v97\n"
        "v_mfma_f32_16x16x32_bf16 v[104:107], a[32:35], a[232:235], v[104:107]\n"
        "ds_read_b64_tr_b16 a[78:79], v154 offset:61440\n"
        "v_cvt_pk_bf16_f32 v98, 0, v98\n"
        "v_mfma_f32_16x16x32_bf16 v[108:111], a[32:35], a[236:239], v[108:111]\n"
        "ds_read2_b32 a[56:57], v151 offset0:112 offset1:113\n"
        "global_load_lds_dwordx4 v254, s[60:61] offset:3072 sc1\n"
        "s_add_u32 m0, s73, 0x8000\n"
        "v_mfma_f32_16x16x32_bf16 v[108:111], a[36:39], a[232:235], v[108:111]\n"
        "ds_read2_b32 a[58:59], v151 offset0:114 offset1:115\n"
        "s_add_u32 s60, s22, s66\n"
        "s_addc_u32 s61, s23, 0\n"
        "v_mfma_f32_16x16x32_bf16 v[108:111], a[40:43], a[228:231], v[108:111]\n"
        "ds_read_b128 a[248:251], v152 offset:960\n"
        "s_xor_b32 s66, s17, 2\n"
        "s_lshl_b32 s66, s66, 15\n"
        "v_mfma_f32_16x16x32_bf16 v[108:111], a[44:47], a[224:227], v[108:111]\n"
        "ds_read_b64 v[66:67], v229 offset:3072\n"
        "s_waitcnt lgkmcnt(8)\n"
        "v_mfma_f32_16x16x32_bf16 v[128:131], a[48:51], a[224:227], v[128:131]\n"
        "ds_read_b64 v[68:69], v229 offset:2560\n"
        "v_cvt_pk_bf16_f32 v99, 0, v99\n"
        "v_mfma_f32_16x16x32_bf16 v[128:131], a[44:47], a[228:231], v[128:131]\n"
        "ds_read_b64 v[70:71], v229 offset:2048\n"
        "v_cvt_pk_bf16_f32 v100, 0, v100\n"
        "v_mfma_f32_16x16x32_bf16 v[128:131], a[40:43], a[232:235], v[128:131]\n"
        "ds_read_b64 v[72:73], v229 offset:1536\n"
        "v_cvt_pk_bf16_f32 v101, 0, v101\n"
        "v_mfma_f32_16x16x32_bf16 v[128:131], a[36:39], a[236:239], v[128:131]\n"
        "ds_read_b64 v[74:75], v229 offset:1024\n"
        "v_cvt_pk_bf16_f32 v102, 0, v102\n"
        "v_mfma_f32_16x16x32_bf16 v[128:131], a[32:35], a[240:243], v[128:131]\n"
        "ds_read_b64 v[76:77], v229 offset:512\n"
        "s_waitcnt vmcnt(9) lgkmcnt(9)\n"
        "v_mfma_f32_16x16x32_bf16 v[132:135], a[32:35], a[244:247], v[132:135]\n"
        "s_barrier\n"
        "v_mfma_f32_16x16x32_bf16 v[132:135], a[36:39], a[240:243], v[132:135]\n"
        "ds_read_b64 v[78:79], v229 offset:0\n"
        "global_load_lds_dwordx4 v144, s[60:61] offset:0\n"
        "v_mfma_f32_16x16x32_bf16 v[132:135], a[40:43], a[236:239], v[132:135]\n"
        "ds_read_b128 a[252:255], v152 offset:448\n"
        "v_cvt_pk_bf16_f32 v103, 0, v103\n"
        "v_mfma_f32_16x16x32_bf16 v[132:135], a[44:47], a[232:235], v[132:135]\n"
        "ds_read2_b32 a[60:61], v151 offset0:240 offset1:241\n"
        "v_add_f32 v80, v80, v96\n"
        "v_mfma_f32_16x16x32_bf16 v[132:135], a[48:51], a[228:231], v[132:135]\n"
        "ds_read2_b32 a[62:63], v151 offset0:242 offset1:243\n"
        "v_add_f32 v81, v81, v97\n"
        "v_mfma_f32_16x16x32_bf16 v[132:135], a[52:55], a[224:227], v[132:135]\n"
        "ds_read_b64_tr_b16 a[80:81], v155 offset:0\n"
        "s_waitcnt lgkmcnt(7)\n"
        "v_mfma_f32_16x16x32_bf16 v[136:139], a[56:59], a[224:227], v[136:139]\n"
        "ds_read_b64_tr_b16 a[82:83], v155 offset:4096\n"
        "global_load_lds_dwordx4 v145, s[60:61] offset:1024\n"
        "v_mfma_f32_16x16x32_bf16 v[136:139], a[52:55], a[228:231], v[136:139]\n"
        "ds_read_b64_tr_b16 a[84:85], v156 offset:0\n"
        "v_and_b32 v84, s35, v67\n"
        "v_mfma_f32_16x16x32_bf16 v[136:139], a[48:51], a[232:235], v[136:139]\n"
        "ds_read_b64_tr_b16 a[86:87], v156 offset:4096\n"
        "v_lshlrev_b32 v85, 16, v67\n"
        "v_mfma_f32_16x16x32_bf16 v[136:139], a[44:47], a[236:239], v[136:139]\n"
        "ds_read_b64_tr_b16 a[88:89], v155 offset:8192\n"
        "v_and_b32 v86, s35, v66\n"
        "v_mfma_f32_16x16x32_bf16 v[136:139], a[40:43], a[240:243], v[136:139]\n"
        "ds_read_b64_tr_b16 a[90:91], v155 offset:12288\n"
        "global_load_lds_dwordx4 v146, s[60:61] offset:2048\n"
        "v_mfma_f32_16x16x32_bf16 v[136:139], a[36:39], a[244:247], v[136:139]\n"
        "ds_read_b64_tr_b16 a[92:93], v156 offset:8192\n"
        "v_lshlrev_b32 v87, 16, v66\n"
        "v_mfma_f32_16x16x32_bf16 v[136:139], a[32:35], a[248:251], v[136:139]\n"
        "ds_read_b64_tr_b16 a[94:95], v156 offset:12288\n"
        "s_waitcnt lgkmcnt(8)\n"
        "v_mfma_f32_16x16x32_bf16 v[140:143], a[32:35], a[252:255], v[140:143]\n"
        "ds_read_b64_tr_b16 a[160:161], v155 offset:16384\n"
        "v_mfma_f32_16x16x32_bf16 v[140:143], a[36:39], a[248:251], v[140:143]\n"
        "s_barrier\n"
        "v_mfma_f32_16x16x32_bf16 v[140:143], a[40:43], a[244:247], v[140:143]\n"
        "ds_read_b64_tr_b16 a[162:163], v155 offset:20480\n"
        "global_load_lds_dwordx4 v147, s[60:61] offset:3072\n"
        "s_add_u32 m0, s73, 0xc000\n"
        "v_mfma_f32_16x16x32_bf16 v[140:143], a[44:47], a[240:243], v[140:143]\n"
        "ds_read_b64_tr_b16 a[164:165], v156 offset:16384\n"
        "s_add_u32 s60, s68, s66\n"
        "s_addc_u32 s61, s69, 0\n"
        "v_mfma_f32_16x16x32_bf16 v[140:143], a[48:51], a[236:239], v[140:143]\n"
        "ds_read_b64_tr_b16 a[166:167], v156 offset:20480\n"
        "s_xor_b32 s66, s29, 0x1a0000\n"
        "v_mul_f32 v84, s54, v84\n"
        "v_mfma_f32_16x16x32_bf16 v[140:143], a[52:55], a[232:235], v[140:143]\n"
        "ds_read_b64_tr_b16 a[168:169], v155 offset:24576\n"
        "v_mul_f32 v85, s54, v85\n"
        "v_mfma_f32_16x16x32_bf16 v[140:143], a[56:59], a[228:231], v[140:143]\n"
        "ds_read_b64_tr_b16 a[170:171], v155 offset:28672\n"
        "global_load_lds_dwordx4 v254, s[60:61] offset:0 sc1\n"
        "v_mfma_f32_16x16x32_bf16 v[140:143], a[60:63], a[224:227], v[140:143]\n"
        "ds_read_b64_tr_b16 a[172:173], v156 offset:24576\n"
        "ds_read_b64_tr_b16 a[174:175], v156 offset:28672\n"
        "v_mul_f32 v86, s54, v86\n"
        "v_mul_f32 v87, s54, v87\n"
        "v_cvt_pk_bf16_f32 v84, 0, v84\n"
        "v_cvt_pk_bf16_f32 v85, 0, v85\n"
        "v_cvt_pk_bf16_f32 v86, 0, v86\n"
        "global_load_lds_dwordx4 v254, s[60:61] offset:1024 sc1\n"
        "v_cvt_pk_bf16_f32 v87, 0, v87\n"
        "v_add_f32 v82, v82, v98\n"
        "v_add_f32 v83, v83, v99\n"
        "v_add_f32 v84, v84, v100\n"
        "v_add_f32 v85, v85, v101\n"
        "v_add_f32 v86, v86, v102\n"
        "v_add_f32 v87, v87, v103\n"
        "v_cvt_pk_bf16_f32 v88, v80, v81\n"
        "v_cvt_pk_bf16_f32 v89, v82, v83\n"
        "v_cvt_pk_bf16_f32 v90, v84, v85\n"
        "global_load_lds_dwordx4 v254, s[60:61] offset:2048 sc1\n"
        "v_cvt_pk_bf16_f32 v91, v86, v87\n"
        "v_and_b32 v80, s35, v69\n"
        "v_lshlrev_b32 v81, 16, v69\n"
        "v_and_b32 v82, s35, v68\n"
        "v_lshlrev_b32 v83, 16, v68\n"
        "v_and_b32 v84, s35, v71\n"
        "v_lshlrev_b32 v85, 16, v71\n"
        "v_and_b32 v86, s35, v70\n"
        "v_lshlrev_b32 v87, 16, v70\n"
        "v_mul_f32 v80, s54, v80\n"
        "global_load_lds_dwordx4 v254, s[60:61] offset:3072 sc1\n"
        "s_add_u32 m0, s73, 0x18000\n"
        "s_add_u32 s60, s22, s66\n"
        "s_addc_u32 s61, s23, 0\n"
        "s_xor_b32 s66, s17, 3\n"
        "s_lshl_b32 s66, s66, 15\n"
        "v_mul_f32 v81, s54, v81\n"
        "v_mul_f32 v82, s54, v82\n"
        "v_mul_f32 v83, s54, v83\n"
        "v_mul_f32 v84, s54, v84\n"
        "v_mul_f32 v85, s54, v85\n"
        "v_mul_f32 v86, s54, v86\n"
        "global_load_lds_dwordx4 v144, s[60:61] offset:0\n"
        "v_mul_f32 v87, s54, v87\n"
        "v_cvt_pk_bf16_f32 v104, 0, v104\n"
        "v_cvt_pk_bf16_f32 v105, 0, v105\n"
        "v_cvt_pk_bf16_f32 v106, 0, v106\n"
        "v_cvt_pk_bf16_f32 v107, 0, v107\n"
        "v_cvt_pk_bf16_f32 v108, 0, v108\n"
        "v_cvt_pk_bf16_f32 v109, 0, v109\n"
        "v_cvt_pk_bf16_f32 v110, 0, v110\n"
        "v_cvt_pk_bf16_f32 v111, 0, v111\n"
        "v_cvt_pk_bf16_f32 v80, 0, v80\n"
        "global_load_lds_dwordx4 v145, s[60:61] offset:1024\n"
        "v_cvt_pk_bf16_f32 v81, 0, v81\n"
        "v_cvt_pk_bf16_f32 v82, 0, v82\n"
        "v_cvt_pk_bf16_f32 v83, 0, v83\n"
        "v_cvt_pk_bf16_f32 v84, 0, v84\n"
        "v_cvt_pk_bf16_f32 v85, 0, v85\n"
        "v_cvt_pk_bf16_f32 v86, 0, v86\n"
        "v_cvt_pk_bf16_f32 v87, 0, v87\n"
        "s_waitcnt vmcnt(14) lgkmcnt(0)\n"
        "s_barrier\n"
        "ds_read_b128 a[0:3], v254 offset:0\n"
        "ds_read_b128 a[4:7], v254 offset:4096\n"
        "ds_read_b128 a[8:11], v254 offset:8192\n"
        "ds_read_b128 a[12:15], v254 offset:12288\n"
        "global_load_lds_dwordx4 v146, s[60:61] offset:2048\n"
        "global_load_lds_dwordx4 v147, s[60:61] offset:3072\n"
        "s_add_u32 m0, s73, 0x1c000\n"
        "s_add_u32 s60, s68, s66\n"
        "s_addc_u32 s61, s69, 0\n"
        "s_xor_b32 s66, s29, 0x1c0000\n"
        "ds_read_b128 a[16:19], v254 offset:1024\n"
        "ds_read_b128 a[20:23], v254 offset:5120\n"
        "ds_read_b128 a[24:27], v254 offset:9216\n"
        "s_waitcnt lgkmcnt(6)\n"
        "v_mfma_f32_16x16x32_bf16 v[0:3], a[96:99], a[0:3], 0\n"
        "s_waitcnt lgkmcnt(5)\n"
        "ds_read_b128 a[28:31], v254 offset:13312\n"
        "v_mfma_f32_16x16x32_bf16 v[0:3], a[104:107], a[4:7], v[0:3]\n"
        "s_waitcnt lgkmcnt(5)\n"
        "v_permlane16_swap_b32 v88, v90\n"
        "v_mfma_f32_16x16x32_bf16 v[0:3], a[112:115], a[8:11], v[0:3]\n"
        "s_waitcnt lgkmcnt(4)\n"
        "ds_read_b128 a[192:195], v254 offset:2048\n"
        "v_mfma_f32_16x16x32_bf16 v[0:3], a[120:123], a[12:15], v[0:3]\n"
        "ds_read_b128 a[196:199], v254 offset:6144\n"
        "global_load_lds_dwordx4 v254, s[60:61] offset:0 sc1\n"
        "v_mfma_f32_16x16x32_bf16 v[4:7], a[124:127], a[12:15], 0\n"
        "ds_read_b128 a[200:203], v254 offset:10240\n"
        "v_add_f32 v80, v80, v104\n"
        "v_mfma_f32_16x16x32_bf16 v[4:7], a[116:119], a[8:11], v[4:7]\n"
        "ds_read_b128 a[204:207], v254 offset:14336\n"
        "v_add_f32 v81, v81, v105\n"
        "v_mfma_f32_16x16x32_bf16 v[4:7], a[108:111], a[4:7], v[4:7]\n"
        "s_waitcnt lgkmcnt(7)\n"
        "v_permlane16_swap_b32 v89, v91\n"
        "v_mfma_f32_16x16x32_bf16 v[4:7], a[100:103], a[0:3], v[4:7]\n"
        "ds_read_b128 a[208:211], v254 offset:3072\n"
        "global_load_lds_dwordx4 v254, s[60:61] offset:1024 sc1\n"
        "v_mfma_f32_16x16x32_bf16 v[12:15], a[100:103], a[16:19], 0\n"
        "ds_read_b128 a[212:215], v254 offset:7168\n"
        "s_waitcnt lgkmcnt(6)\n"
        "v_mfma_f32_16x16x32_bf16 v[12:15], a[108:111], a[20:23], v[12:15]\n"
        "ds_read_b128 a[216:219], v254 offset:11264\n"
        "LHC3_%=:\n"
        "s_cbranch_execz LHRC_%=\n"
        "v_add_f32 v82, v82, v106\n"
        "v_mfma_f32_16x16x32_bf16 v[12:15], a[116:119], a[24:27], v[12:15]\n"
        "ds_read_b128 a[220:223], v254 offset:15360\n"
        "v_add_f32 v83, v83, v107\n"
        "v_mfma_f32_16x16x32_bf16 v[12:15], a[124:127], a[28:31], v[12:15]\n"
        "global_load_lds_dwordx4 v254, s[60:61] offset:2048 sc1\n"
        "v_add_f32 v84, v84, v108\n"
        "v_mfma_f32_16x16x32_bf16 v[8:11], a[120:123], a[28:31], 0\n"
        "s_waitcnt lgkmcnt(5)\n"
        "v_add_f32 v85, v85, v109\n"
        "v_add_f32 v86, v86, v110\n"
        "v_mfma_f32_16x16x32_bf16 v[8:11], a[112:115], a[24:27], v[8:11]\n"
        "v_add_f32 v87, v87, v111\n"
        "v_and_b32 v229, 1, v233\n"
        "v_mfma_f32_16x16x32_bf16 v[8:11], a[104:107], a[20:23], v[8:11]\n"
        "global_load_lds_dwordx4 v254, s[60:61] offset:3072 sc1\n"
        "s_add_u32 m0, s73, 0x20000\n"
        "v_mfma_f32_16x16x32_bf16 v[8:11], a[96:99], a[16:19], v[8:11]\n"
        "s_add_u32 s60, s22, s66\n"
        "s_addc_u32 s61, s23, 0\n"
        "s_sub_u32 s66, 15, s12\n"
        "v_mfma_f32_16x16x32_bf16 v[16:19], a[96:99], a[192:195], 0\n"
        "s_cmp_lt_u32 s75, 2\n"
        "s_cselect_b32 s66, s12, s66\n"
        "s_mul_i32 s66, s66, 1052672\n"
        "v_mfma_f32_16x16x32_bf16 v[16:19], a[104:107], a[196:199], v[16:19]\n"
        "s_add_u32 s66, s66, s67\n"
        "s_add_u32 s68, s20, s66\n"
        "s_addc_u32 s69, s21, 0\n"
        "v_mfma_f32_16x16x32_bf16 v[16:19], a[112:115], a[200:203], v[16:19]\n"
        "s_xor_b32 s66, s17, 0\n"
        "s_lshl_b32 s66, s66, 15\n"
        "s_waitcnt lgkmcnt(0)\n"
        "v_mfma_f32_16x16x32_bf16 v[16:19], a[120:123], a[204:207], v[16:19]\n"
        "global_load_lds_dwordx4 v144, s[60:61] offset:0\n"
        "s_waitcnt vmcnt(17)\n"
        "v_mfma_f32_16x16x32_bf16 v[20:23], a[124:127], a[204:207], 0\n"
        "s_barrier\n"
        "v_mfma_f32_16x16x32_bf16 v[20:23], a[116:119], a[200:203], v[20:23]\n"
        "ds_read_b128 a[0:3], v254 offset:16384\n"
        "s_and_b32 s67, s74, 1\n"
        "s_lshl_b32 s67, s67, 12\n"
        "v_mfma_f32_16x16x32_bf16 v[20:23], a[108:111], a[196:199], v[20:23]\n"
        "ds_read_b128 a[4:7], v254 offset:20480\n"
        "global_load_lds_dwordx4 v145, s[60:61] offset:1024\n"
        "v_mfma_f32_16x16x32_bf16 v[20:23], a[100:103], a[192:195], v[20:23]\n"
        "ds_read_b128 a[8:11], v254 offset:24576\n"
        "v_cvt_pk_bf16_f32 v92, v80, v81\n"
        "v_mfma_f32_16x16x32_bf16 v[28:31], a[100:103], a[208:211], 0\n"
        "ds_read_b128 a[12:15], v254 offset:28672\n"
        "v_cvt_pk_bf16_f32 v93, v82, v83\n"
        "v_mfma_f32_16x16x32_bf16 v[28:31], a[108:111], a[212:215], v[28:31]\n"
        "ds_read_b128 a[16:19], v254 offset:17408\n"
        "v_cvt_pk_bf16_f32 v94, v84, v85\n"
        "v_mfma_f32_16x16x32_bf16 v[28:31], a[116:119], a[216:219], v[28:31]\n"
        "ds_read_b128 a[20:23], v254 offset:21504\n"
        "global_load_lds_dwordx4 v146, s[60:61] offset:2048\n"
        "v_mfma_f32_16x16x32_bf16 v[28:31], a[124:127], a[220:223], v[28:31]\n"
        "ds_read_b128 a[24:27], v254 offset:25600\n"
        "v_cvt_pk_bf16_f32 v95, v86, v87\n"
        "v_mfma_f32_16x16x32_bf16 v[24:27], a[120:123], a[220:223], 0\n"
        "ds_read_b128 a[28:31], v254 offset:29696\n"
        "v_lshlrev_b32 v229, 9, v229\n"
        "v_mfma_f32_16x16x32_bf16 v[24:27], a[112:115], a[216:219], v[24:27]\n"
        "s_waitcnt lgkmcnt(7)\n"
        "ds_read_b128 a[192:195], v254 offset:18432\n"
        "v_mfma_f32_16x16x32_bf16 v[24:27], a[104:107], a[212:215], v[24:27]\n"
        "s_waitcnt lgkmcnt(7)\n"
        "ds_read_b128 a[196:199], v254 offset:22528\n"
        "v_mfma_f32_16x16x32_bf16 v[24:27], a[96:99], a[208:211], v[24:27]\n"
        "s_waitcnt lgkmcnt(7)\n"
        "ds_read_b128 a[200:203], v254 offset:26624\n"
        "v_mfma_f32_16x16x32_bf16 v[0:3], a[128:131], a[0:3], v[0:3]\n"
        "s_waitcnt lgkmcnt(7)\n"
        "ds_read_b128 a[204:207], v254 offset:30720\n"
        "v_mfma_f32_16x16x32_bf16 v[0:3], a[136:139], a[4:7], v[0:3]\n"
        "ds_read_b128 a[208:211], v254 offset:19456\n"
        "global_load_lds_dwordx4 v147, s[60:61] offset:3072\n"
        "s_add_u32 m0, s73, 0x24000\n"
        "v_mfma_f32_16x16x32_bf16 v[0:3], a[144:147], a[8:11], v[0:3]\n"
        "ds_read_b128 a[212:215], v254 offset:23552\n"
        "s_add_u32 s60, s68, s66\n"
        "s_addc_u32 s61, s69, 0\n"
        "v_mfma_f32_16x16x32_bf16 v[0:3], a[152:155], a[12:15], v[0:3]\n"
        "ds_read_b128 a[216:219], v254 offset:27648\n"
        "s_xor_b32 s66, s29, 0x1e0000\n"
        "v_and_b32 v80, s35, v73\n"
        "v_mfma_f32_16x16x32_bf16 v[4:7], a[156:159], a[12:15], v[4:7]\n"
        "s_waitcnt lgkmcnt(7)\n"
        "ds_read_b128 a[220:223], v254 offset:31744\n"
        "v_mfma_f32_16x16x32_bf16 v[4:7], a[148:151], a[8:11], v[4:7]\n"
        "v_permlane16_swap_b32 v92, v94\n"
        "v_mfma_f32_16x16x32_bf16 v[4:7], a[140:143], a[4:7], v[4:7]\n"
        "v_permlane16_swap_b32 v93, v95\n"
        "v_mfma_f32_16x16x32_bf16 v[4:7], a[132:135], a[0:3], v[4:7]\n"
        "global_load_lds_dwordx4 v254, s[60:61] offset:0 sc1\n"
        "v_lshlrev_b32 v81, 16, v73\n"
        "v_mfma_f32_16x16x32_bf16 v[12:15], a[132:135], a[16:19], v[12:15]\n"
        "v_and_b32 v82, s35, v72\n"
        "v_lshlrev_b32 v83, 16, v72\n"
        "v_mfma_f32_16x16x32_bf16 v[12:15], a[140:143], a[20:23], v[12:15]\n"
        "v_and_b32 v84, s35, v75\n"
        "v_lshlrev_b32 v85, 16, v75\n"
        "v_mfma_f32_16x16x32_bf16 v[12:15], a[148:151], a[24:27], v[12:15]\n"
        "global_load_lds_dwordx4 v254, s[60:61] offset:1024 sc1\n"
        "v_mfma_f32_16x16x32_bf16 v[12:15], a[156:159], a[28:31], v[12:15]\n"
        "v_and_b32 v86, s35, v74\n"
        "v_lshlrev_b32 v87, 16, v74\n"
        "v_mfma_f32_16x16x32_bf16 v[8:11], a[152:155], a[28:31], v[8:11]\n"
        "s_waitcnt lgkmcnt(1)\n"
        "v_lshl_add_u32 v229, v232, 5, v229\n"
        "v_mul_f32 v80, s54, v80\n"
        "v_mfma_f32_16x16x32_bf16 v[8:11], a[144:147], a[24:27], v[8:11]\n"
        "v_mul_f32 v81, s54, v81\n"
        "v_mul_f32 v82, s54, v82\n"
        "v_mfma_f32_16x16x32_bf16 v[8:11], a[136:139], a[20:23], v[8:11]\n"
        "global_load_lds_dwordx4 v254, s[60:61] offset:2048 sc1\n"
        "v_mul_f32 v83, s54, v83\n"
        "v_mfma_f32_16x16x32_bf16 v[8:11], a[128:131], a[16:19], v[8:11]\n"
        "s_waitcnt vmcnt(15)\n"
        "v_mul_f32 v84, s54, v84\n"
        "v_mul_f32 v85, s54, v85\n"
        "v_mfma_f32_16x16x32_bf16 v[16:19], a[128:131], a[192:195], v[16:19]\n"
        "s_barrier\n"
        "v_mfma_f32_16x16x32_bf16 v[16:19], a[136:139], a[196:199], v[16:19]\n"
        "ds_read_b64_tr_b16 a[176:177], v153 offset:32768\n"
        "global_load_lds_dwordx4 v254, s[60:61] offset:3072 sc1\n"
        "s_add_u32 m0, s73, 0x10000\n"
        "v_mfma_f32_16x16x32_bf16 v[16:19], a[144:147], a[200:203], v[16:19]\n"
        "ds_read_b64_tr_b16 a[178:179], v153 offset:36864\n"
        "s_add_u32 s60, s22, s66\n"
        "s_addc_u32 s61, s23, 0\n"
        "v_mfma_f32_16x16x32_bf16 v[16:19], a[152:155], a[204:207], v[16:19]\n"
        "ds_read_b64_tr_b16 a[180:181], v154 offset:32768\n"
        "s_xor_b32 s66, s17, 1\n"
        "s_lshl_b32 s66, s66, 15\n"
        "v_mfma_f32_16x16x32_bf16 v[20:23], a[156:159], a[204:207], v[20:23]\n"
        "ds_read_b64_tr_b16 a[182:183], v154 offset:36864\n"
        "v_mul_f32 v86, s54, v86\n"
        "v_mfma_f32_16x16x32_bf16 v[20:23], a[148:151], a[200:203], v[20:23]\n"
        "ds_read_b64_tr_b16 a[184:185], v153 offset:40960\n"
        "global_load_lds_dwordx4 v144, s[60:61] offset:0\n"
        "v_mfma_f32_16x16x32_bf16 v[20:23], a[140:143], a[196:199], v[20:23]\n"
        "ds_read_b64_tr_b16 a[186:187], v153 offset:45056\n"
        "v_mul_f32 v87, s54, v87\n"
        "v_mfma_f32_16x16x32_bf16 v[20:23], a[132:135], a[192:195], v[20:23]\n"
        "ds_read_b64_tr_b16 a[188:189], v154 offset:40960\n"
        "v_cvt_pk_bf16_f32 v128, 0, v128\n"
        "v_mfma_f32_16x16x32_bf16 v[28:31], a[132:135], a[208:211], v[28:31]\n"
        "ds_read_b64_tr_b16 a[190:191], v154 offset:45056\n"
        "v_cvt_pk_bf16_f32 v129, 0, v129\n"
        "v_mfma_f32_16x16x32_bf16 v[28:31], a[140:143], a[212:215], v[28:31]\n"
        "ds_read_b128 a[0:3], v254 offset:49152\n"
        "global_load_lds_dwordx4 v145, s[60:61] offset:1024\n"
        "v_mfma_f32_16x16x32_bf16 v[28:31], a[148:151], a[216:219], v[28:31]\n"
        "ds_read_b128 a[4:7], v254 offset:53248\n"
        "s_waitcnt lgkmcnt(8)\n"
        "v_mfma_f32_16x16x32_bf16 v[28:31], a[156:159], a[220:223], v[28:31]\n"
        "ds_read_b128 a[8:11], v254 offset:57344\n"
        "v_cvt_pk_bf16_f32 v130, 0, v130\n"
        "v_mfma_f32_16x16x32_bf16 v[24:27], a[152:155], a[220:223], v[24:27]\n"
        "ds_read_b128 a[12:15], v254 offset:61440\n"
        "v_cvt_pk_bf16_f32 v131, 0, v131\n"
        "v_mfma_f32_16x16x32_bf16 v[24:27], a[144:147], a[216:219], v[24:27]\n"
        "ds_read_b128 a[16:19], v254 offset:50176\n"
        "global_load_lds_dwordx4 v146, s[60:61] offset:2048\n"
        "v_mfma_f32_16x16x32_bf16 v[24:27], a[136:139], a[212:215], v[24:27]\n"
        "ds_read_b128 a[20:23], v254 offset:54272\n"
        "v_cvt_pk_bf16_f32 v132, 0, v132\n"
        "v_mfma_f32_16x16x32_bf16 v[24:27], a[128:131], a[208:211], v[24:27]\n"
        "ds_read_b128 a[24:27], v254 offset:58368\n"
        "s_waitcnt lgkmcnt(6)\n"
        "global_load_lds_dwordx4 v147, s[60:61] offset:3072\n"
        "s_add_u32 m0, s73, 0x14000\n"
        "v_mfma_f32_16x16x32_bf16 v[0:3], v[160:163], a[0:3], v[0:3]\n"
        "ds_read_b128 a[28:31], v254 offset:62464\n"
        "s_waitcnt lgkmcnt(6)\n"
        "v_mfma_f32_16x16x32_bf16 v[0:3], v[168:171], a[4:7], v[0:3]\n"
        "ds_read_b128 a[192:195], v254 offset:51200\n"
        "s_waitcnt lgkmcnt(6)\n"
        "v_mfma_f32_16x16x32_bf16 v[0:3], v[176:179], a[8:11], v[0:3]\n"
        "ds_read_b128 a[196:199], v254 offset:55296\n"
        "s_waitcnt lgkmcnt(6)\n"
        "v_mfma_f32_16x16x32_bf16 v[0:3], v[184:187], a[12:15], v[0:3]\n"
        "ds_read_b128 a[200:203], v254 offset:59392\n"
        "s_add_u32 s60, s68, s66\n"
        "s_addc_u32 s61, s69, 0\n"
        "v_mfma_f32_16x16x32_bf16 v[4:7], v[188:191], a[12:15], v[4:7]\n"
        "ds_read_b128 a[204:207], v254 offset:63488\n"
        "global_load_lds_dwordx4 v254, s[60:61] offset:0 sc1\n"
        "s_xor_b32 s66, s17, 2\n"
        "v_mfma_f32_16x16x32_bf16 v[4:7], v[180:183], a[8:11], v[4:7]\n"
        "ds_read_b128 a[208:211], v254 offset:52224\n"
        "s_lshl_b32 s66, s66, 15\n"
        "v_cvt_pk_bf16_f32 v133, 0, v133\n"
        "v_mfma_f32_16x16x32_bf16 v[4:7], v[172:175], a[4:7], v[4:7]\n"
        "ds_read_b128 a[212:215], v254 offset:56320\n"
        "v_cvt_pk_bf16_f32 v134, 0, v134\n"
        "v_mfma_f32_16x16x32_bf16 v[4:7], v[164:167], a[0:3], v[4:7]\n"
        "ds_read_b128 a[216:219], v254 offset:60416\n"
        "s_waitcnt lgkmcnt(8)\n"
        "v_mfma_f32_16x16x32_bf16 v[12:15], v[164:167], a[16:19], v[12:15]\n"
        "ds_read_b128 a[220:223], v254 offset:64512\n"
        "global_load_lds_dwordx4 v254, s[60:61] offset:1024 sc1\n"
        "v_mfma_f32_16x16x32_bf16 v[12:15], v[172:175], a[20:23], v[12:15]\n"
        "s_waitcnt lgkmcnt(7)\n"
        "v_cvt_pk_bf16_f32 v135, 0, v135\n"
        "v_lshl_add_u32 v229, v230, 4, v229\n"
        "v_mfma_f32_16x16x32_bf16 v[12:15], v[180:183], a[24:27], v[12:15]\n"
        "v_cvt_pk_bf16_f32 v80, 0, v80\n"
        "v_cvt_pk_bf16_f32 v81, 0, v81\n"
        "v_mfma_f32_16x16x32_bf16 v[12:15], v[188:191], a[28:31], v[12:15]\n"
        "v_cvt_pk_bf16_f32 v82, 0, v82\n"
        "v_cvt_pk_bf16_f32 v83, 0, v83\n"
        "v_mfma_f32_16x16x32_bf16 v[8:11], v[184:187], a[28:31], v[8:11]\n"
        "global_load_lds_dwordx4 v254, s[60:61] offset:2048 sc1\n"
        "v_cvt_pk_bf16_f32 v84, 0, v84\n"
        "v_mfma_f32_16x16x32_bf16 v[8:11], v[176:179], a[24:27], v[8:11]\n"
        "s_waitcnt lgkmcnt(3)\n"
        "v_cvt_pk_bf16_f32 v85, 0, v85\n"
        "v_cvt_pk_bf16_f32 v86, 0, v86\n"
        "v_mfma_f32_16x16x32_bf16 v[8:11], v[168:171], a[20:23], v[8:11]\n"
        "v_cvt_pk_bf16_f32 v87, 0, v87\n"
        "v_add_f32 v80, v80, v128\n"
        "v_mfma_f32_16x16x32_bf16 v[8:11], v[160:163], a[16:19], v[8:11]\n"
        "global_load_lds_dwordx4 v254, s[60:61] offset:3072 sc1\n"
        "s_add_u32 m0, s73, 0x0\n"
        "v_mfma_f32_16x16x32_bf16 v[16:19], v[160:163], a[192:195], v[16:19]\n"
        "s_add_u32 s60, s68, s66\n"
        "s_addc_u32 s61, s69, 0\n"
        "s_xor_b32 s66, s17, 3\n"
        "v_mfma_f32_16x16x32_bf16 v[16:19], v[168:171], a[196:199], v[16:19]\n"
        "s_lshl_b32 s66, s66, 15\n"
        "v_add_f32 v81, v81, v129\n"
        "v_add_f32 v82, v82, v130\n"
        "v_mfma_f32_16x16x32_bf16 v[16:19], v[176:179], a[200:203], v[16:19]\n"
        "v_add_f32 v83, v83, v131\n"
        "v_add_f32 v84, v84, v132\n"
        "v_mfma_f32_16x16x32_bf16 v[16:19], v[184:187], a[204:207], v[16:19]\n"
        "global_load_lds_dwordx4 v254, s[60:61] offset:0 sc1\n"
        "v_add_f32 v85, v85, v133\n"
        "v_mfma_f32_16x16x32_bf16 v[20:23], v[188:191], a[204:207], v[20:23]\n"
        "v_add_f32 v86, v86, v134\n"
        "v_add_f32 v87, v87, v135\n"
        "v_mfma_f32_16x16x32_bf16 v[20:23], v[180:183], a[200:203], v[20:23]\n"
        "s_waitcnt lgkmcnt(0)\n"
        "v_cvt_pk_bf16_f32 v136, 0, v136\n"
        "v_cvt_pk_bf16_f32 v137, 0, v137\n"
        "v_mfma_f32_16x16x32_bf16 v[20:23], v[172:175], a[196:199], v[20:23]\n"
        "global_load_lds_dwordx4 v254, s[60:61] offset:1024 sc1\n"
        "v_mfma_f32_16x16x32_bf16 v[20:23], v[164:167], a[192:195], v[20:23]\n"
        "v_cvt_pk_bf16_f32 v138, 0, v138\n"
        "v_cvt_pk_bf16_f32 v139, 0, v139\n"
        "v_mfma_f32_16x16x32_bf16 v[28:31], v[164:167], a[208:211], v[28:31]\n"
        "v_cvt_pk_bf16_f32 v140, 0, v140\n"
        "v_cvt_pk_bf16_f32 v141, 0, v141\n"
        "v_mfma_f32_16x16x32_bf16 v[28:31], v[172:175], a[212:215], v[28:31]\n"
        "v_cvt_pk_bf16_f32 v142, 0, v142\n"
        "v_cvt_pk_bf16_f32 v143, 0, v143\n"
        "v_mfma_f32_16x16x32_bf16 v[28:31], v[180:183], a[216:219], v[28:31]\n"
        "global_load_lds_dwordx4 v254, s[60:61] offset:2048 sc1\n"
        "v_mfma_f32_16x16x32_bf16 v[28:31], v[188:191], a[220:223], v[28:31]\n"
        "v_mfma_f32_16x16x32_bf16 v[24:27], v[184:187], a[220:223], v[24:27]\n"
        "v_mfma_f32_16x16x32_bf16 v[24:27], v[176:179], a[216:219], v[24:27]\n"
        "global_load_lds_dwordx4 v254, s[60:61] offset:3072 sc1\n"
        "s_add_u32 m0, s73, 0x4000\n"
        "v_mfma_f32_16x16x32_bf16 v[24:27], v[168:171], a[212:215], v[24:27]\n"
        "s_add_u32 s60, s68, s66\n"
        "s_addc_u32 s61, s69, 0\n"
        "s_sub_u32 s66, 15, s12\n"
        "v_mfma_f32_16x16x32_bf16 v[24:27], v[160:163], a[208:211], v[24:27]\n"
        "s_cmp_lt_u32 s74, 2\n"
        "s_cselect_b32 s66, s12, s66\n"
        "s_mul_i32 s66, s66, 1052672\n"
        "s_add_u32 s66, s66, s67\n"
        "s_add_u32 s68, s20, s66\n"
        "s_addc_u32 s69, s21, 0\n"
        "s_xor_b32 s66, s17, 4\n"
        "s_lshl_b32 s66, s66, 15\n"
        "s_and_b32 s67, s75, 1\n"
        "s_lshl_b32 s67, s67, 12\n"
        "s_waitcnt vmcnt(8)\n"
        "s_barrier\n"
        "ds_read_b64_tr_b16 a[32:33], v155 offset:32768\n"
        "ds_read_b64_tr_b16 a[34:35], v155 offset:36864\n"
        "ds_read_b64_tr_b16 a[36:37], v156 offset:32768\n"
        "ds_read_b64_tr_b16 a[38:39], v156 offset:36864\n"
        "ds_read_b64_tr_b16 a[40:41], v155 offset:40960\n"
        "ds_read_b64_tr_b16 a[42:43], v155 offset:45056\n"
        "ds_read_b64_tr_b16 a[44:45], v156 offset:40960\n"
        "ds_read_b64_tr_b16 a[46:47], v156 offset:45056\n"
        "ds_read_b64_tr_b16 a[224:225], v155 offset:0\n"
        "ds_read_b64_tr_b16 a[226:227], v155 offset:4096\n"
        "ds_read_b64_tr_b16 a[228:229], v156 offset:0\n"
        "ds_read_b64_tr_b16 a[230:231], v156 offset:4096\n"
        "ds_read_b64_tr_b16 a[232:233], v155 offset:8192\n"
        "ds_read_b64_tr_b16 a[234:235], v155 offset:12288\n"
        "ds_read_b64_tr_b16 a[236:237], v156 offset:8192\n"
        "ds_read_b64_tr_b16 a[238:239], v156 offset:12288\n"
        "global_load_lds_dwordx4 v254, s[60:61] offset:0 sc1\n"
        "ds_read_b128 a[0:3], v244 offset:49152\n"
        "ds_read_b128 a[4:7], v244 offset:53248\n"
        "ds_read_b128 a[8:11], v244 offset:57344\n"
        "ds_read_b128 a[12:15], v244 offset:61440\n"
        "global_load_lds_dwordx4 v254, s[60:61] offset:1024 sc1\n"
        "global_load_lds_dwordx4 v254, s[60:61] offset:2048 sc1\n"
        "ds_read_b128 a[16:19], v244 offset:50176\n"
        "ds_read_b128 a[20:23], v244 offset:54272\n"
        "s_waitcnt lgkmcnt(5)\n"
        "v_mfma_f32_16x16x32_bf16 v[0:3], v[192:195], a[0:3], v[0:3]\n"
        "ds_read_b128 a[24:27], v244 offset:58368\n"
        "s_waitcnt lgkmcnt(5)\n"
        "v_mfma_f32_16x16x32_bf16 v[0:3], v[200:203], a[4:7], v[0:3]\n"
        "ds_read_b128 a[28:31], v244 offset:62464\n"
        "s_waitcnt lgkmcnt(4)\n"
        "v_mfma_f32_16x16x32_bf16 v[0:3], v[208:211], a[8:11], v[0:3]\n"
        "ds_read_b128 a[192:195], v244 offset:51200\n"
        "global_load_lds_dwordx4 v254, s[60:61] offset:3072 sc1\n"
        "s_add_u32 m0, s73, 0x8000\n"
        "v_mfma_f32_16x16x32_bf16 v[0:3], v[216:219], a[12:15], v[0:3]\n"
        "ds_read_b128 a[196:199], v244 offset:55296\n"
        "s_add_u32 s60, s68, s66\n"
        "s_addc_u32 s61, s69, 0\n"
        "v_mfma_f32_16x16x32_bf16 v[4:7], v[220:223], a[12:15], v[4:7]\n"
        "ds_read_b128 a[200:203], v244 offset:59392\n"
        "s_xor_b32 s66, s17, 5\n"
        "s_lshl_b32 s66, s66, 15\n"
        "v_mfma_f32_16x16x32_bf16 v[4:7], v[212:215], a[8:11], v[4:7]\n"
        "ds_read_b128 a[204:207], v244 offset:63488\n"
        "v_mfma_f32_16x16x32_bf16 v[4:7], v[204:207], a[4:7], v[4:7]\n"
        "ds_read_b128 a[208:211], v244 offset:52224\n"
        "global_load_lds_dwordx4 v254, s[60:61] offset:0 sc1\n"
        "v_mfma_f32_16x16x32_bf16 v[4:7], v[196:199], a[0:3], v[4:7]\n"
        "ds_read_b128 a[212:215], v244 offset:56320\n"
        "s_waitcnt lgkmcnt(8)\n"
        "v_mfma_f32_16x16x32_bf16 v[12:15], v[196:199], a[16:19], v[12:15]\n"
        "ds_read_b128 a[216:219], v244 offset:60416\n"
        "s_waitcnt lgkmcnt(8)\n"
        "v_mfma_f32_16x16x32_bf16 v[12:15], v[204:207], a[20:23], v[12:15]\n"
        "ds_read_b128 a[220:223], v244 offset:64512\n"
        "s_waitcnt lgkmcnt(8)\n"
        "v_mfma_f32_16x16x32_bf16 v[12:15], v[212:215], a[24:27], v[12:15]\n"
        "global_load_lds_dwordx4 v254, s[60:61] offset:1024 sc1\n"
        "v_mfma_f32_16x16x32_bf16 v[12:15], v[220:223], a[28:31], v[12:15]\n"
        "v_mfma_f32_16x16x32_bf16 v[8:11], v[216:219], a[28:31], v[8:11]\n"
        "s_waitcnt lgkmcnt(5)\n"
        "v_mfma_f32_16x16x32_bf16 v[8:11], v[208:211], a[24:27], v[8:11]\n"
        "global_load_lds_dwordx4 v254, s[60:61] offset:2048 sc1\n"
        "v_mfma_f32_16x16x32_bf16 v[8:11], v[200:203], a[20:23], v[8:11]\n"
        "v_mfma_f32_16x16x32_bf16 v[8:11], v[192:195], a[16:19], v[8:11]\n"
        "s_waitcnt lgkmcnt(2)\n"
        "v_mfma_f32_16x16x32_bf16 v[16:19], v[192:195], a[192:195], v[16:19]\n"
        "s_barrier\n"
        "v_mfma_f32_16x16x32_bf16 v[16:19], v[200:203], a[196:199], v[16:19]\n"
        "ds_read_b64_tr_b16 a[48:49], v157 offset:0\n"
        "global_load_lds_dwordx4 v254, s[60:61] offset:3072 sc1\n"
        "s_add_u32 m0, s73, 0xc000\n"
        "v_mfma_f32_16x16x32_bf16 v[16:19], v[208:211], a[200:203], v[16:19]\n"
        "ds_read_b64_tr_b16 a[50:51], v157 offset:4096\n"
        "s_add_u32 s60, s68, s66\n"
        "s_addc_u32 s61, s69, 0\n"
        "v_mfma_f32_16x16x32_bf16 v[16:19], v[216:219], a[204:207], v[16:19]\n"
        "ds_read_b64_tr_b16 a[52:53], v158 offset:0\n"
        "s_xor_b32 s66, s17, 6\n"
        "s_lshl_b32 s66, s66, 15\n"
        "v_mfma_f32_16x16x32_bf16 v[20:23], v[220:223], a[204:207], v[20:23]\n"
        "ds_read_b64_tr_b16 a[54:55], v158 offset:4096\n"
        "v_mfma_f32_16x16x32_bf16 v[20:23], v[212:215], a[200:203], v[20:23]\n"
        "ds_read_b64_tr_b16 a[56:57], v157 offset:8192\n"
        "global_load_lds_dwordx4 v254, s[60:61] offset:0 sc1\n"
        "v_mfma_f32_16x16x32_bf16 v[20:23], v[204:207], a[196:199], v[20:23]\n"
        "ds_read_b64_tr_b16 a[58:59], v157 offset:12288\n"
        "s_waitcnt lgkmcnt(6)\n"
        "v_mfma_f32_16x16x32_bf16 v[20:23], v[196:199], a[192:195], v[20:23]\n"
        "ds_read_b64_tr_b16 a[60:61], v158 offset:8192\n"
        "v_mfma_f32_16x16x32_bf16 v[28:31], v[196:199], a[208:211], v[28:31]\n"
        "ds_read_b64_tr_b16 a[62:63], v158 offset:12288\n"
        "v_mfma_f32_16x16x32_bf16 v[28:31], v[204:207], a[212:215], v[28:31]\n"
        "ds_read_b128 a[0:3], v245 offset:16384\n"
        "global_load_lds_dwordx4 v254, s[60:61] offset:1024 sc1\n"
        "v_mfma_f32_16x16x32_bf16 v[28:31], v[212:215], a[216:219], v[28:31]\n"
        "ds_read_b128 a[4:7], v245 offset:20480\n"
        "v_mfma_f32_16x16x32_bf16 v[28:31], v[220:223], a[220:223], v[28:31]\n"
        "ds_read_b128 a[8:11], v245 offset:24576\n"
        "v_mfma_f32_16x16x32_bf16 v[24:27], v[216:219], a[220:223], v[24:27]\n"
        "ds_read_b128 a[12:15], v245 offset:28672\n"
        "v_mfma_f32_16x16x32_bf16 v[24:27], v[208:211], a[216:219], v[24:27]\n"
        "ds_read_b128 a[16:19], v245 offset:17408\n"
        "global_load_lds_dwordx4 v254, s[60:61] offset:2048 sc1\n"
        "v_mfma_f32_16x16x32_bf16 v[24:27], v[200:203], a[212:215], v[24:27]\n"
        "ds_read_b128 a[20:23], v245 offset:21504\n"
        "v_mfma_f32_16x16x32_bf16 v[24:27], v[192:195], a[208:211], v[24:27]\n"
        "ds_read_b128 a[24:27], v245 offset:25600\n"
        "ds_read_b128 a[28:31], v245 offset:29696\n"
        "s_waitcnt lgkmcnt(7)\n"
        "global_load_lds_dwordx4 v254, s[60:61] offset:3072 sc1\n"
        "s_add_u32 m0, s73, 0x18000\n"
        "v_mfma_f32_16x16x32_bf16 v[32:35], a[96:99], a[0:3], 0\n"
        "ds_read_b128 a[192:195], v245 offset:18432\n"
        "s_waitcnt lgkmcnt(7)\n"
        "v_mfma_f32_16x16x32_bf16 v[32:35], a[104:107], a[4:7], v[32:35]\n"
        "ds_read_b128 a[196:199], v245 offset:22528\n"
        "s_waitcnt lgkmcnt(7)\n"
        "v_mfma_f32_16x16x32_bf16 v[32:35], a[112:115], a[8:11], v[32:35]\n"
        "ds_read_b128 a[200:203], v245 offset:26624\n"
        "s_waitcnt lgkmcnt(7)\n"
        "v_mfma_f32_16x16x32_bf16 v[32:35], a[120:123], a[12:15], v[32:35]\n"
        "ds_read_b128 a[204:207], v245 offset:30720\n"
        "s_add_u32 s60, s68, s66\n"
        "s_addc_u32 s61, s69, 0\n"
        "v_mfma_f32_16x16x32_bf16 v[36:39], a[124:127], a[12:15], 0\n"
        "ds_read_b128 a[208:211], v245 offset:19456\n"
        "global_load_lds_dwordx4 v254, s[60:61] offset:0 sc1\n"
        "s_xor_b32 s66, s17, 7\n"
        "v_mfma_f32_16x16x32_bf16 v[36:39], a[116:119], a[8:11], v[36:39]\n"
        "ds_read_b128 a[212:215], v245 offset:23552\n"
        "s_lshl_b32 s66, s66, 15\n"
        "s_waitcnt lgkmcnt(8)\n"
        "v_mfma_f32_16x16x32_bf16 v[36:39], a[108:111], a[4:7], v[36:39]\n"
        "ds_read_b128 a[216:219], v245 offset:27648\n"
        "s_waitcnt lgkmcnt(7)\n"
        "v_mfma_f32_16x16x32_bf16 v[36:39], a[100:103], a[0:3], v[36:39]\n"
        "ds_read_b128 a[220:223], v245 offset:31744\n"
        "v_mfma_f32_16x16x32_bf16 v[44:47], a[100:103], a[16:19], 0\n"
        "global_load_lds_dwordx4 v254, s[60:61] offset:1024 sc1\n"
        "v_mfma_f32_16x16x32_bf16 v[44:47], a[108:111], a[20:23], v[44:47]\n"
        "v_mfma_f32_16x16x32_bf16 v[44:47], a[116:119], a[24:27], v[44:47]\n"
        "v_mfma_f32_16x16x32_bf16 v[44:47], a[124:127], a[28:31], v[44:47]\n"
        "global_load_lds_dwordx4 v254, s[60:61] offset:2048 sc1\n"
        "v_mfma_f32_16x16x32_bf16 v[40:43], a[120:123], a[28:31], 0\n"
        "s_waitcnt lgkmcnt(3)\n"
        "v_mfma_f32_16x16x32_bf16 v[40:43], a[112:115], a[24:27], v[40:43]\n"
        "v_mfma_f32_16x16x32_bf16 v[40:43], a[104:107], a[20:23], v[40:43]\n"
        "v_mfma_f32_16x16x32_bf16 v[40:43], a[96:99], a[16:19], v[40:43]\n"
        "global_load_lds_dwordx4 v254, s[60:61] offset:3072 sc1\n"
        "s_add_u32 m0, s73, 0x10000\n"
        "s_waitcnt vmcnt(20)\n"
        "v_mfma_f32_16x16x32_bf16 v[48:51], a[96:99], a[192:195], 0\n"
        "s_barrier\n"
        "v_mfma_f32_16x16x32_bf16 v[48:51], a[104:107], a[196:199], v[48:51]\n"
        "ds_read_b128 a[0:3], v244 offset:16384\n"
        "s_add_u32 s60, s68, s66\n"
        "s_addc_u32 s61, s69, 0\n"
        "v_mfma_f32_16x16x32_bf16 v[48:51], a[112:115], a[200:203], v[48:51]\n"
        "ds_read_b128 a[4:7], v244 offset:20480\n"
        "global_load_lds_dwordx4 v254, s[60:61] offset:0 sc1\n"
        "s_sub_u32 s66, 15, s12\n"
        "v_mfma_f32_16x16x32_bf16 v[48:51], a[120:123], a[204:207], v[48:51]\n"
        "ds_read_b128 a[8:11], v244 offset:24576\n"
        "s_cmp_lt_u32 s75, 2\n"
        "s_cselect_b32 s66, s12, s66\n"
        "v_mfma_f32_16x16x32_bf16 v[52:55], a[124:127], a[204:207], 0\n"
        "ds_read_b128 a[12:15], v244 offset:28672\n"
        "s_mul_i32 s66, s66, 1052672\n"
        "s_add_u32 s66, s66, s67\n"
        "v_mfma_f32_16x16x32_bf16 v[52:55], a[116:119], a[200:203], v[52:55]\n"
        "ds_read_b128 a[16:19], v244 offset:17408\n"
        "s_add_u32 s68, s20, s66\n"
        "s_addc_u32 s69, s21, 0\n"
        "v_mfma_f32_16x16x32_bf16 v[52:55], a[108:111], a[196:199], v[52:55]\n"
        "ds_read_b128 a[20:23], v244 offset:21504\n"
        "global_load_lds_dwordx4 v254, s[60:61] offset:1024 sc1\n"
        "s_xor_b32 s66, s17, 4\n"
        "v_mfma_f32_16x16x32_bf16 v[52:55], a[100:103], a[192:195], v[52:55]\n"
        "ds_read_b128 a[24:27], v244 offset:25600\n"
        "s_lshl_b32 s66, s66, 15\n"
        "s_and_b32 s67, s76, 1\n"
        "v_mfma_f32_16x16x32_bf16 v[60:63], a[100:103], a[208:211], 0\n"
        "ds_read_b128 a[28:31], v244 offset:29696\n"
        "s_lshl_b32 s67, s67, 12\n"
        "s_waitcnt lgkmcnt(8)\n"
        "v_mfma_f32_16x16x32_bf16 v[60:63], a[108:111], a[212:215], v[60:63]\n"
        "ds_read_b128 a[192:195], v244 offset:18432\n"
        "s_lshl2_add_u32 s15, s74, s10\n"
        "s_mul_i32 s16, s13, 263168\n"
        "v_mfma_f32_16x16x32_bf16 v[60:63], a[116:119], a[216:219], v[60:63]\n"
        "ds_read_b128 a[196:199], v244 offset:22528\n"
        "global_load_lds_dwordx4 v254, s[60:61] offset:2048 sc1\n"
        "s_lshl_b32 s15, s15, 14\n"
        "v_mfma_f32_16x16x32_bf16 v[60:63], a[124:127], a[220:223], v[60:63]\n"
        "ds_read_b128 a[200:203], v244 offset:26624\n"
        "s_add_u32 s16, s16, s15\n"
        "s_add_u32 s16, s16, s73\n"
        "v_mfma_f32_16x16x32_bf16 v[56:59], a[120:123], a[220:223], 0\n"
        "ds_read_b128 a[204:207], v244 offset:30720\n"
        "s_add_u32 s16, s16, 0x4000000\n"
        "s_waitcnt lgkmcnt(8)\n"
        "v_mfma_f32_16x16x32_bf16 v[56:59], a[112:115], a[216:219], v[56:59]\n"
        "ds_read_b128 a[216:219], v244 offset:27648\n"
        "s_add_u32 s28, s38, s16\n"
        "s_addc_u32 s29, s39, 0\n"
        "v_mfma_f32_16x16x32_bf16 v[56:59], a[104:107], a[212:215], v[56:59]\n"
        "ds_read_b128 a[212:215], v244 offset:23552\n"
        "global_load_lds_dwordx4 v254, s[60:61] offset:3072 sc1\n"
        "s_add_u32 m0, s73, 0x1c000\n"
        "v_mfma_f32_16x16x32_bf16 v[56:59], a[96:99], a[208:211], v[56:59]\n"
        "ds_read_b128 a[208:211], v244 offset:19456\n"
        "s_add_u32 s60, s68, s66\n"
        "s_addc_u32 s61, s69, 0\n"
        "v_mfma_f32_16x16x32_bf16 v[32:35], a[128:131], a[0:3], v[32:35]\n"
        "ds_read_b128 a[220:223], v244 offset:31744\n"
        "s_xor_b32 s66, s17, 5\n"
        "s_lshl_b32 s66, s66, 15\n"
        "v_mfma_f32_16x16x32_bf16 v[32:35], a[136:139], a[4:7], v[32:35]\n"
        "v_mfma_f32_16x16x32_bf16 v[32:35], a[144:147], a[8:11], v[32:35]\n"
        "global_load_lds_dwordx4 v254, s[60:61] offset:0 sc1\n"
        "v_mfma_f32_16x16x32_bf16 v[32:35], a[152:155], a[12:15], v[32:35]\n"
        "v_mfma_f32_16x16x32_bf16 v[36:39], a[156:159], a[12:15], v[36:39]\n"
        "s_waitcnt lgkmcnt(4)\n"
        "v_mfma_f32_16x16x32_bf16 v[36:39], a[148:151], a[8:11], v[36:39]\n"
        "global_load_lds_dwordx4 v254, s[60:61] offset:1024 sc1\n"
        "v_mfma_f32_16x16x32_bf16 v[36:39], a[140:143], a[4:7], v[36:39]\n"
        "v_mfma_f32_16x16x32_bf16 v[36:39], a[132:135], a[0:3], v[36:39]\n"
        "v_mfma_f32_16x16x32_bf16 v[44:47], a[132:135], a[16:19], v[44:47]\n"
        "v_mfma_f32_16x16x32_bf16 v[44:47], a[140:143], a[20:23], v[44:47]\n"
        "global_load_lds_dwordx4 v254, s[60:61] offset:2048 sc1\n"
        "v_mfma_f32_16x16x32_bf16 v[44:47], a[148:151], a[24:27], v[44:47]\n"
        "v_mfma_f32_16x16x32_bf16 v[44:47], a[156:159], a[28:31], v[44:47]\n"
        "v_mfma_f32_16x16x32_bf16 v[40:43], a[152:155], a[28:31], v[40:43]\n"
        "global_load_lds_dwordx4 v254, s[60:61] offset:3072 sc1\n"
        "s_add_u32 m0, s73, 0x20000\n"
        "v_mfma_f32_16x16x32_bf16 v[40:43], a[144:147], a[24:27], v[40:43]\n"
        "s_add_u32 s60, s68, s66\n"
        "s_addc_u32 s61, s69, 0\n"
        "s_xor_b32 s66, s17, 6\n"
        "v_mfma_f32_16x16x32_bf16 v[40:43], a[136:139], a[20:23], v[40:43]\n"
        "s_lshl_b32 s66, s66, 15\n"
        "v_mfma_f32_16x16x32_bf16 v[40:43], a[128:131], a[16:19], v[40:43]\n"
        "s_waitcnt vmcnt(24)\n"
        "v_mfma_f32_16x16x32_bf16 v[48:51], a[128:131], a[192:195], v[48:51]\n"
        "s_barrier\n"
        "v_mfma_f32_16x16x32_bf16 v[48:51], a[136:139], a[196:199], v[48:51]\n"
        "ds_read_b128 a[0:3], v254 offset:0\n"
        "global_load_lds_dwordx4 v254, s[60:61] offset:0 sc1\n"
        "v_mfma_f32_16x16x32_bf16 v[48:51], a[144:147], a[200:203], v[48:51]\n"
        "ds_read_b128 a[4:7], v254 offset:4096\n"
        "v_mfma_f32_16x16x32_bf16 v[48:51], a[152:155], a[204:207], v[48:51]\n"
        "ds_read_b128 a[8:11], v254 offset:8192\n"
        "v_mfma_f32_16x16x32_bf16 v[52:55], a[156:159], a[204:207], v[52:55]\n"
        "ds_read_b128 a[12:15], v254 offset:12288\n"
        "s_waitcnt lgkmcnt(4)\n"
        "v_mfma_f32_16x16x32_bf16 v[52:55], a[148:151], a[200:203], v[52:55]\n"
        "ds_read_b128 a[16:19], v254 offset:1024\n"
        "global_load_lds_dwordx4 v254, s[60:61] offset:1024 sc1\n"
        "v_mfma_f32_16x16x32_bf16 v[52:55], a[140:143], a[196:199], v[52:55]\n"
        "ds_read_b128 a[20:23], v254 offset:5120\n"
        "v_mfma_f32_16x16x32_bf16 v[52:55], a[132:135], a[192:195], v[52:55]\n"
        "ds_read_b128 a[24:27], v254 offset:9216\n"
        "v_mfma_f32_16x16x32_bf16 v[60:63], a[132:135], a[208:211], v[60:63]\n"
        "ds_read_b128 a[28:31], v254 offset:13312\n"
        "v_mfma_f32_16x16x32_bf16 v[60:63], a[140:143], a[212:215], v[60:63]\n"
        "ds_read_b128 a[192:195], v254 offset:2048\n"
        "global_load_lds_dwordx4 v254, s[60:61] offset:2048 sc1\n"
        "v_mfma_f32_16x16x32_bf16 v[60:63], a[148:151], a[216:219], v[60:63]\n"
        "ds_read_b128 a[196:199], v254 offset:6144\n"
        "v_mfma_f32_16x16x32_bf16 v[60:63], a[156:159], a[220:223], v[60:63]\n"
        "ds_read_b128 a[200:203], v254 offset:10240\n"
        "v_mfma_f32_16x16x32_bf16 v[56:59], a[152:155], a[220:223], v[56:59]\n"
        "ds_read_b128 a[204:207], v254 offset:14336\n"
        "s_waitcnt lgkmcnt(8)\n"
        "v_mfma_f32_16x16x32_bf16 v[56:59], a[144:147], a[216:219], v[56:59]\n"
        "ds_read_b128 a[216:219], v254 offset:11264\n"
        "global_load_lds_dwordx4 v254, s[60:61] offset:3072 sc1\n"
        "s_add_u32 m0, s73, 0x24000\n"
        "v_mfma_f32_16x16x32_bf16 v[56:59], a[136:139], a[212:215], v[56:59]\n"
        "ds_read_b128 a[212:215], v254 offset:7168\n"
        "s_add_u32 s60, s68, s66\n"
        "s_addc_u32 s61, s69, 0\n"
        "v_mfma_f32_16x16x32_bf16 v[56:59], a[128:131], a[208:211], v[56:59]\n"
        "ds_read_b128 a[208:211], v254 offset:3072\n"
        "s_xor_b32 s66, s17, 7\n"
        "s_lshl_b32 s66, s66, 15\n"
        "v_mfma_f32_16x16x32_bf16 v[32:35], v[160:163], a[0:3], v[32:35]\n"
        "ds_read_b128 a[220:223], v254 offset:15360\n"
        "v_mfma_f32_16x16x32_bf16 v[32:35], v[168:171], a[4:7], v[32:35]\n"
        "global_load_lds_dwordx4 v254, s[60:61] offset:0 sc1\n"
        "v_mfma_f32_16x16x32_bf16 v[32:35], v[176:179], a[8:11], v[32:35]\n"
        "v_mfma_f32_16x16x32_bf16 v[32:35], v[184:187], a[12:15], v[32:35]\n"
        "v_mfma_f32_16x16x32_bf16 v[36:39], v[188:191], a[12:15], v[36:39]\n"
        "global_load_lds_dwordx4 v254, s[60:61] offset:1024 sc1\n"
        "v_mfma_f32_16x16x32_bf16 v[36:39], v[180:183], a[8:11], v[36:39]\n"
        "s_waitcnt lgkmcnt(3)\n"
        "v_mfma_f32_16x16x32_bf16 v[36:39], v[172:175], a[4:7], v[36:39]\n"
        "v_mfma_f32_16x16x32_bf16 v[36:39], v[164:167], a[0:3], v[36:39]\n"
        "v_mfma_f32_16x16x32_bf16 v[44:47], v[164:167], a[16:19], v[44:47]\n"
        "global_load_lds_dwordx4 v254, s[60:61] offset:2048 sc1\n"
        "v_mfma_f32_16x16x32_bf16 v[44:47], v[172:175], a[20:23], v[44:47]\n"
        "v_mfma_f32_16x16x32_bf16 v[44:47], v[180:183], a[24:27], v[44:47]\n"
        "v_mfma_f32_16x16x32_bf16 v[44:47], v[188:191], a[28:31], v[44:47]\n"
        "global_load_lds_dwordx4 v254, s[60:61] offset:3072 sc1\n"
        "s_add_u32 m0, s73, 0x14000\n"
        "v_mfma_f32_16x16x32_bf16 v[40:43], v[184:187], a[28:31], v[40:43]\n"
        "s_add_u32 s60, s68, s66\n"
        "s_addc_u32 s61, s69, 0\n"
        "s_sub_u32 s66, 15, s12\n"
        "LHB0_%=:\n"
        "s_cbranch_execz LHRB_%=\n"
        "v_mfma_f32_16x16x32_bf16 v[40:43], v[176:179], a[24:27], v[40:43]\n"
        "s_cmp_lt_u32 s76, 2\n"
        "s_cselect_b32 s66, s12, s66\n"
        "s_mul_i32 s66, s66, 1052672\n"
        "v_mfma_f32_16x16x32_bf16 v[40:43], v[168:171], a[20:23], v[40:43]\n"
        "s_add_u32 s66, s66, s67\n"
        "s_add_u32 s68, s20, s66\n"
        "s_addc_u32 s69, s21, 0\n"
        "v_mfma_f32_16x16x32_bf16 v[40:43], v[160:163], a[16:19], v[40:43]\n"
        "s_waitcnt vmcnt(28)\n"
        "s_xor_b32 s66, s17, 0\n"
        "s_lshl_b32 s66, s66, 15\n"
        "v_mfma_f32_16x16x32_bf16 v[48:51], v[160:163], a[192:195], v[48:51]\n"
        "s_barrier\n"
        "v_mfma_f32_16x16x32_bf16 v[48:51], v[168:171], a[196:199], v[48:51]\n"
        "ds_read_b128 a[0:3], v254 offset:16384\n"
        "global_load_lds_dwordx4 v254, s[60:61] offset:0 sc1\n"
        "v_mfma_f32_16x16x32_bf16 v[48:51], v[176:179], a[200:203], v[48:51]\n"
        "ds_read_b128 a[4:7], v254 offset:20480\n"
        "v_mfma_f32_16x16x32_bf16 v[48:51], v[184:187], a[204:207], v[48:51]\n"
        "ds_read_b128 a[8:11], v254 offset:24576\n"
        "v_mfma_f32_16x16x32_bf16 v[52:55], v[188:191], a[204:207], v[52:55]\n"
        "ds_read_b128 a[12:15], v254 offset:28672\n"
        "s_waitcnt lgkmcnt(4)\n"
        "v_mfma_f32_16x16x32_bf16 v[52:55], v[180:183], a[200:203], v[52:55]\n"
        "ds_read_b128 a[16:19], v254 offset:17408\n"
        "global_load_lds_dwordx4 v254, s[60:61] offset:1024 sc1\n"
        "v_mfma_f32_16x16x32_bf16 v[52:55], v[172:175], a[196:199], v[52:55]\n"
        "ds_read_b128 a[20:23], v254 offset:21504\n"
        "v_mfma_f32_16x16x32_bf16 v[52:55], v[164:167], a[192:195], v[52:55]\n"
        "ds_read_b128 a[24:27], v254 offset:25600\n"
        "v_mfma_f32_16x16x32_bf16 v[60:63], v[164:167], a[208:211], v[60:63]\n"
        "ds_read_b128 a[28:31], v254 offset:29696\n"
        "v_mfma_f32_16x16x32_bf16 v[60:63], v[172:175], a[212:215], v[60:63]\n"
        "ds_read_b128 a[192:195], v254 offset:18432\n"
        "global_load_lds_dwordx4 v254, s[60:61] offset:2048 sc1\n"
        "v_mfma_f32_16x16x32_bf16 v[60:63], v[180:183], a[216:219], v[60:63]\n"
        "ds_read_b128 a[196:199], v254 offset:22528\n"
        "v_mfma_f32_16x16x32_bf16 v[60:63], v[188:191], a[220:223], v[60:63]\n"
        "ds_read_b128 a[200:203], v254 offset:26624\n"
        "v_mfma_f32_16x16x32_bf16 v[56:59], v[184:187], a[220:223], v[56:59]\n"
        "ds_read_b128 a[204:207], v254 offset:30720\n"
        "s_waitcnt lgkmcnt(8)\n"
        "v_mfma_f32_16x16x32_bf16 v[56:59], v[176:179], a[216:219], v[56:59]\n"
        "ds_read_b128 a[216:219], v254 offset:27648\n"
        "global_load_lds_dwordx4 v254, s[60:61] offset:3072 sc1\n"
        "s_add_u32 m0, s73, 0x0\n"
        "v_mfma_f32_16x16x32_bf16 v[56:59], v[168:171], a[212:215], v[56:59]\n"
        "ds_read_b128 a[212:215], v254 offset:23552\n"
        "s_add_u32 s60, s68, s66\n"
        "s_addc_u32 s61, s69, 0\n"
        "v_mfma_f32_16x16x32_bf16 v[56:59], v[160:163], a[208:211], v[56:59]\n"
        "ds_read_b128 a[208:211], v254 offset:19456\n"
        "s_xor_b32 s66, s17, 1\n"
        "s_lshl_b32 s66, s66, 15\n"
        "v_mfma_f32_16x16x32_bf16 v[32:35], v[192:195], a[0:3], v[32:35]\n"
        "ds_read_b128 a[220:223], v254 offset:31744\n"
        "v_mfma_f32_16x16x32_bf16 v[32:35], v[200:203], a[4:7], v[32:35]\n"
        "global_store_dwordx4 v229, v[88:91], s[24:25] offset:0 sc1\n"
        "v_mfma_f32_16x16x32_bf16 v[32:35], v[208:211], a[8:11], v[32:35]\n"
        "v_mfma_f32_16x16x32_bf16 v[32:35], v[216:219], a[12:15], v[32:35]\n"
        "v_cvt_pk_bf16_f32 v88, v80, v81\n"
        "v_cvt_pk_bf16_f32 v89, v82, v83\n"
        "v_mfma_f32_16x16x32_bf16 v[36:39], v[220:223], a[12:15], v[36:39]\n"
        "s_waitcnt lgkmcnt(4)\n"
        "v_cvt_pk_bf16_f32 v90, v84, v85\n"
        "v_cvt_pk_bf16_f32 v91, v86, v87\n"
        "v_mfma_f32_16x16x32_bf16 v[36:39], v[212:215], a[8:11], v[36:39]\n"
        "v_permlane16_swap_b32 v88, v90\n"
        "v_mfma_f32_16x16x32_bf16 v[36:39], v[204:207], a[4:7], v[36:39]\n"
        "v_permlane16_swap_b32 v89, v91\n"
        "v_mfma_f32_16x16x32_bf16 v[36:39], v[196:199], a[0:3], v[36:39]\n"
        "v_and_b32 v80, s35, v77\n"
        "v_lshlrev_b32 v81, 16, v77\n"
        "v_mfma_f32_16x16x32_bf16 v[44:47], v[196:199], a[16:19], v[44:47]\n"
        "v_and_b32 v82, s35, v76\n"
        "v_lshlrev_b32 v83, 16, v76\n"
        "v_mfma_f32_16x16x32_bf16 v[44:47], v[204:207], a[20:23], v[44:47]\n"
        "v_and_b32 v84, s35, v79\n"
        "v_lshlrev_b32 v85, 16, v79\n"
        "v_mfma_f32_16x16x32_bf16 v[44:47], v[212:215], a[24:27], v[44:47]\n"
        "v_and_b32 v86, s35, v78\n"
        "v_lshlrev_b32 v87, 16, v78\n"
        "v_mfma_f32_16x16x32_bf16 v[44:47], v[220:223], a[28:31], v[44:47]\n"
        "v_mul_f32 v80, s54, v80\n"
        "v_mul_f32 v81, s54, v81\n"
        "v_mfma_f32_16x16x32_bf16 v[40:43], v[216:219], a[28:31], v[40:43]\n"
        "v_mul_f32 v82, s54, v82\n"
        "v_mul_f32 v83, s54, v83\n"
        "v_mfma_f32_16x16x32_bf16 v[40:43], v[208:211], a[24:27], v[40:43]\n"
        "v_mul_f32 v84, s54, v84\n"
        "v_mul_f32 v85, s54, v85\n"
        "v_mfma_f32_16x16x32_bf16 v[40:43], v[200:203], a[20:23], v[40:43]\n"
        "v_mul_f32 v86, s54, v86\n"
        "v_mul_f32 v87, s54, v87\n"
        "v_mfma_f32_16x16x32_bf16 v[40:43], v[192:195], a[16:19], v[40:43]\n"
        "s_waitcnt vmcnt(29)\n"
        "v_cvt_pk_bf16_f32 v80, 0, v80\n"
        "v_cvt_pk_bf16_f32 v81, 0, v81\n"
        "v_mfma_f32_16x16x32_bf16 v[48:51], v[192:195], a[192:195], v[48:51]\n"
        "s_barrier\n"
        "v_mfma_f32_16x16x32_bf16 v[48:51], v[200:203], a[196:199], v[48:51]\n"
        "ds_read_b128 a[0:3], v254 offset:32768\n"
        "global_load_lds_dwordx4 v254, s[60:61] offset:0 sc1\n"
        "v_mfma_f32_16x16x32_bf16 v[48:51], v[208:211], a[200:203], v[48:51]\n"
        "ds_read_b128 a[4:7], v254 offset:36864\n"
        "v_cvt_pk_bf16_f32 v82, 0, v82\n"
        "v_mfma_f32_16x16x32_bf16 v[48:51], v[216:219], a[204:207], v[48:51]\n"
        "ds_read_b128 a[8:11], v254 offset:40960\n"
        "v_cvt_pk_bf16_f32 v83, 0, v83\n"
        "v_mfma_f32_16x16x32_bf16 v[52:55], v[220:223], a[204:207], v[52:55]\n"
        "ds_read_b128 a[12:15], v254 offset:45056\n"
        "v_cvt_pk_bf16_f32 v84, 0, v84\n"
        "v_mfma_f32_16x16x32_bf16 v[52:55], v[212:215], a[200:203], v[52:55]\n"
        "ds_read_b128 a[16:19], v254 offset:33792\n"
        "global_load_lds_dwordx4 v254, s[60:61] offset:1024 sc1\n"
        "v_mfma_f32_16x16x32_bf16 v[52:55], v[204:207], a[196:199], v[52:55]\n"
        "ds_read_b128 a[20:23], v254 offset:37888\n"
        "v_cvt_pk_bf16_f32 v85, 0, v85\n"
        "v_mfma_f32_16x16x32_bf16 v[52:55], v[196:199], a[192:195], v[52:55]\n"
        "ds_read_b128 a[24:27], v254 offset:41984\n"
        "s_waitcnt lgkmcnt(7)\n"
        "v_mfma_f32_16x16x32_bf16 v[60:63], v[196:199], a[208:211], v[60:63]\n"
        "ds_read_b128 a[28:31], v254 offset:46080\n"
        "v_cvt_pk_bf16_f32 v86, 0, v86\n"
        "v_mfma_f32_16x16x32_bf16 v[60:63], v[204:207], a[212:215], v[60:63]\n"
        "ds_read_b128 a[192:195], v254 offset:34816\n"
        "global_load_lds_dwordx4 v254, s[60:61] offset:2048 sc1\n"
        "v_mfma_f32_16x16x32_bf16 v[60:63], v[212:215], a[216:219], v[60:63]\n"
        "ds_read_b128 a[196:199], v254 offset:38912\n"
        "v_cvt_pk_bf16_f32 v87, 0, v87\n"
        "v_mfma_f32_16x16x32_bf16 v[60:63], v[220:223], a[220:223], v[60:63]\n"
        "ds_read_b128 a[200:203], v254 offset:43008\n"
        "v_add_f32 v80, v80, v136\n"
        "v_mfma_f32_16x16x32_bf16 v[56:59], v[216:219], a[220:223], v[56:59]\n"
        "ds_read_b128 a[204:207], v254 offset:47104\n"
        "v_add_f32 v81, v81, v137\n"
        "v_mfma_f32_16x16x32_bf16 v[56:59], v[208:211], a[216:219], v[56:59]\n"
        "ds_read_b128 a[216:219], v254 offset:44032\n"
        "global_load_lds_dwordx4 v254, s[60:61] offset:3072 sc1\n"
        "s_add_u32 m0, s73, 0x4000\n"
        "v_mfma_f32_16x16x32_bf16 v[56:59], v[200:203], a[212:215], v[56:59]\n"
        "ds_read_b128 a[212:215], v254 offset:39936\n"
        "s_add_u32 s60, s68, s66\n"
        "s_addc_u32 s61, s69, 0\n"
        "v_mfma_f32_16x16x32_bf16 v[56:59], v[192:195], a[208:211], v[56:59]\n"
        "ds_read_b128 a[208:211], v254 offset:35840\n"
        "s_xor_b32 s66, s17, 2\n"
        "s_waitcnt lgkmcnt(8)\n"
        "v_mfma_f32_16x16x32_bf16 v[0:3], v[112:115], a[0:3], v[0:3]\n"
        "ds_read_b128 a[220:223], v254 offset:48128\n"
        "s_lshl_b32 s66, s66, 15\n"
        "v_add_f32 v82, v82, v138\n"
        "v_mfma_f32_16x16x32_bf16 v[0:3], v[120:123], a[4:7], v[0:3]\n"
        "global_store_dwordx4 v229, v[92:95], s[24:25] offset:1024 sc1\n"
        "v_add_f32 v83, v83, v139\n"
        "v_add_f32 v84, v84, v140\n"
        "v_add_f32 v85, v85, v141\n"
        "v_add_f32 v86, v86, v142\n"
        "v_mfma_f32_16x16x32_bf16 v[0:3], a[64:67], a[8:11], v[0:3]\n"
        "v_add_f32 v87, v87, v143\n"
        "v_cvt_pk_bf16_f32 v92, v80, v81\n"
        "v_mfma_f32_16x16x32_bf16 v[0:3], a[72:75], a[12:15], v[0:3]\n"
        "v_cvt_pk_bf16_f32 v93, v82, v83\n"
        "v_cvt_pk_bf16_f32 v94, v84, v85\n"
        "v_mfma_f32_16x16x32_bf16 v[4:7], a[76:79], a[12:15], v[4:7]\n"
        "v_cvt_pk_bf16_f32 v95, v86, v87\n"
        "v_mfma_f32_16x16x32_bf16 v[4:7], a[68:71], a[8:11], v[4:7]\n"
        "v_permlane16_swap_b32 v92, v94\n"
        "v_mfma_f32_16x16x32_bf16 v[4:7], v[124:127], a[4:7], v[4:7]\n"
        "v_permlane16_swap_b32 v93, v95\n"
        "v_mfma_f32_16x16x32_bf16 v[4:7], v[116:119], a[0:3], v[4:7]\n"
        "global_store_dwordx4 v229, v[88:91], s[24:25] offset:2048 sc1\n"
        "s_waitcnt lgkmcnt(0)\n"
        "v_mfma_f32_16x16x32_bf16 v[12:15], v[116:119], a[16:19], v[12:15]\n"
        "v_mfma_f32_16x16x32_bf16 v[12:15], v[124:127], a[20:23], v[12:15]\n"
        "v_mfma_f32_16x16x32_bf16 v[12:15], a[68:71], a[24:27], v[12:15]\n"
        "global_store_dwordx4 v229, v[92:95], s[24:25] offset:3072 sc1\n"
        "v_mfma_f32_16x16x32_bf16 v[12:15], a[76:79], a[28:31], v[12:15]\n"
        "s_lshl2_add_u32 s15, s75, s10\n"
        "s_mul_i32 s16, s13, 263168\n"
        "s_lshl_b32 s15, s15, 14\n"
        "v_mfma_f32_16x16x32_bf16 v[8:11], a[72:75], a[28:31], v[8:11]\n"
        "s_add_u32 s16, s16, s15\n"
        "s_add_u32 s16, s16, s73\n"
        "s_add_u32 s16, s16, 0x4000000\n"
        "v_mfma_f32_16x16x32_bf16 v[8:11], a[64:67], a[24:27], v[8:11]\n"
        "v_mfma_f32_16x16x32_bf16 v[8:11], v[120:123], a[20:23], v[8:11]\n"
        "v_mfma_f32_16x16x32_bf16 v[8:11], v[112:115], a[16:19], v[8:11]\n"
        "s_waitcnt vmcnt(32)\n"
        "v_mfma_f32_16x16x32_bf16 v[16:19], v[112:115], a[192:195], v[16:19]\n"
        "s_barrier\n"
        "v_mfma_f32_16x16x32_bf16 v[16:19], v[120:123], a[196:199], v[16:19]\n"
        "ds_read_b128 a[0:3], v254 offset:49152\n"
        "global_load_lds_dwordx4 v254, s[60:61] offset:0 sc1\n"
        "v_mfma_f32_16x16x32_bf16 v[16:19], a[64:67], a[200:203], v[16:19]\n"
        "ds_read_b128 a[4:7], v254 offset:53248\n"
        "v_mfma_f32_16x16x32_bf16 v[16:19], a[72:75], a[204:207], v[16:19]\n"
        "ds_read_b128 a[8:11], v254 offset:57344\n"
        "v_mfma_f32_16x16x32_bf16 v[20:23], a[76:79], a[204:207], v[20:23]\n"
        "ds_read_b128 a[12:15], v254 offset:61440\n"
        "v_mfma_f32_16x16x32_bf16 v[20:23], a[68:71], a[200:203], v[20:23]\n"
        "ds_read_b128 a[16:19], v254 offset:50176\n"
        "global_load_lds_dwordx4 v254, s[60:61] offset:1024 sc1\n"
        "v_mfma_f32_16x16x32_bf16 v[20:23], v[124:127], a[196:199], v[20:23]\n"
        "ds_read_b128 a[20:23], v254 offset:54272\n"
        "v_mfma_f32_16x16x32_bf16 v[20:23], v[116:119], a[192:195], v[20:23]\n"
        "ds_read_b128 a[24:27], v254 offset:58368\n"
        "v_mfma_f32_16x16x32_bf16 v[28:31], v[116:119], a[208:211], v[28:31]\n"
        "ds_read_b128 a[28:31], v254 offset:62464\n"
        "v_mfma_f32_16x16x32_bf16 v[28:31], v[124:127], a[212:215], v[28:31]\n"
        "ds_read_b128 a[192:195], v254 offset:51200\n"
        "global_load_lds_dwordx4 v254, s[60:61] offset:2048 sc1\n"
        "v_mfma_f32_16x16x32_bf16 v[28:31], a[68:71], a[216:219], v[28:31]\n"
        "ds_read_b128 a[196:199], v254 offset:55296\n"
        "v_mfma_f32_16x16x32_bf16 v[28:31], a[76:79], a[220:223], v[28:31]\n"
        "ds_read_b128 a[200:203], v254 offset:59392\n"
        "v_mfma_f32_16x16x32_bf16 v[24:27], a[72:75], a[220:223], v[24:27]\n"
        "ds_read_b128 a[204:207], v254 offset:63488\n"
        "s_waitcnt lgkmcnt(8)\n"
        "v_mfma_f32_16x16x32_bf16 v[24:27], a[64:67], a[216:219], v[24:27]\n"
        "ds_read_b128 a[216:219], v254 offset:60416\n"
        "global_load_lds_dwordx4 v254, s[60:61] offset:3072 sc1\n"
        "s_add_u32 m0, s73, 0x8000\n"
        "v_mfma_f32_16x16x32_bf16 v[24:27], v[120:123], a[212:215], v[24:27]\n"
        "ds_read_b128 a[212:215], v254 offset:56320\n"
        "s_add_u32 s60, s68, s66\n"
        "s_addc_u32 s61, s69, 0\n"
        "v_mfma_f32_16x16x32_bf16 v[24:27], v[112:115], a[208:211], v[24:27]\n"
        "ds_read_b128 a[208:211], v254 offset:52224\n"
        "s_xor_b32 s66, s17, 3\n"
        "s_lshl_b32 s66, s66, 15\n"
        "v_mfma_f32_16x16x32_bf16 v[0:3], a[80:83], a[0:3], v[0:3]\n"
        "ds_read_b128 a[220:223], v254 offset:64512\n"
        "v_mfma_f32_16x16x32_bf16 v[0:3], a[88:91], a[4:7], v[0:3]\n"
        "v_mfma_f32_16x16x32_bf16 v[0:3], a[160:163], a[8:11], v[0:3]\n"
        "v_mfma_f32_16x16x32_bf16 v[0:3], a[168:171], a[12:15], v[0:3]\n"
        "v_mfma_f32_16x16x32_bf16 v[4:7], a[172:175], a[12:15], v[4:7]\n"
        "s_waitcnt lgkmcnt(4)\n"
        "v_mfma_f32_16x16x32_bf16 v[4:7], a[164:167], a[8:11], v[4:7]\n"
        "v_mfma_f32_16x16x32_bf16 v[4:7], a[92:95], a[4:7], v[4:7]\n"
        "v_mfma_f32_16x16x32_bf16 v[4:7], a[84:87], a[0:3], v[4:7]\n"
        "s_and_b32 s67, s10, 1\n"
        "s_lshl_b32 s67, s67, 12\n"
        "v_mfma_f32_16x16x32_bf16 v[12:15], a[84:87], a[16:19], v[12:15]\n"
        "v_mfma_f32_16x16x32_bf16 v[12:15], a[92:95], a[20:23], v[12:15]\n"
        "v_mfma_f32_16x16x32_bf16 v[12:15], a[164:167], a[24:27], v[12:15]\n"
        "v_mfma_f32_16x16x32_bf16 v[12:15], a[172:175], a[28:31], v[12:15]\n"
        "v_mfma_f32_16x16x32_bf16 v[8:11], a[168:171], a[28:31], v[8:11]\n"
        "v_mfma_f32_16x16x32_bf16 v[8:11], a[160:163], a[24:27], v[8:11]\n"
        "v_mfma_f32_16x16x32_bf16 v[8:11], a[88:91], a[20:23], v[8:11]\n"
        "v_mfma_f32_16x16x32_bf16 v[8:11], a[80:83], a[16:19], v[8:11]\n"
        "s_waitcnt vmcnt(32)\n"
        "v_mfma_f32_16x16x32_bf16 v[16:19], a[80:83], a[192:195], v[16:19]\n"
        "s_barrier\n"
        "v_mfma_f32_16x16x32_bf16 v[16:19], a[88:91], a[196:199], v[16:19]\n"
        "ds_read_b128 a[0:3], v244 offset:32768\n"
        "global_load_lds_dwordx4 v254, s[60:61] offset:0 sc1\n"
        "v_mfma_f32_16x16x32_bf16 v[16:19], a[160:163], a[200:203], v[16:19]\n"
        "ds_read_b128 a[4:7], v244 offset:36864\n"
        "v_mfma_f32_16x16x32_bf16 v[16:19], a[168:171], a[204:207], v[16:19]\n"
        "ds_read_b128 a[8:11], v244 offset:40960\n"
        "v_mfma_f32_16x16x32_bf16 v[20:23], a[172:175], a[204:207], v[20:23]\n"
        "ds_read_b128 a[12:15], v244 offset:45056\n"
        "s_waitcnt lgkmcnt(4)\n"
        "v_mfma_f32_16x16x32_bf16 v[20:23], a[164:167], a[200:203], v[20:23]\n"
        "ds_read_b128 a[16:19], v244 offset:33792\n"
        "global_load_lds_dwordx4 v254, s[60:61] offset:1024 sc1\n"
        "v_mfma_f32_16x16x32_bf16 v[20:23], a[92:95], a[196:199], v[20:23]\n"
        "ds_read_b128 a[20:23], v244 offset:37888\n"
        "v_mfma_f32_16x16x32_bf16 v[20:23], a[84:87], a[192:195], v[20:23]\n"
        "ds_read_b128 a[24:27], v244 offset:41984\n"
        "v_mfma_f32_16x16x32_bf16 v[28:31], a[84:87], a[208:211], v[28:31]\n"
        "ds_read_b128 a[28:31], v244 offset:46080\n"
        "v_mfma_f32_16x16x32_bf16 v[28:31], a[92:95], a[212:215], v[28:31]\n"
        "ds_read_b128 a[192:195], v244 offset:34816\n"
        "global_load_lds_dwordx4 v254, s[60:61] offset:2048 sc1\n"
        "v_mfma_f32_16x16x32_bf16 v[28:31], a[164:167], a[216:219], v[28:31]\n"
        "ds_read_b128 a[196:199], v244 offset:38912\n"
        "v_mfma_f32_16x16x32_bf16 v[28:31], a[172:175], a[220:223], v[28:31]\n"
        "ds_read_b128 a[200:203], v244 offset:43008\n"
        "v_mfma_f32_16x16x32_bf16 v[24:27], a[168:171], a[220:223], v[24:27]\n"
        "ds_read_b128 a[204:207], v244 offset:47104\n"
        "s_waitcnt lgkmcnt(8)\n"
        "v_mfma_f32_16x16x32_bf16 v[24:27], a[160:163], a[216:219], v[24:27]\n"
        "ds_read_b128 a[216:219], v244 offset:44032\n"
        "global_load_lds_dwordx4 v254, s[60:61] offset:3072 sc1\n"
        "s_add_u32 m0, s73, 0xc000\n"
        "v_mfma_f32_16x16x32_bf16 v[24:27], a[88:91], a[212:215], v[24:27]\n"
        "ds_read_b128 a[212:215], v244 offset:39936\n"
        "s_add_u32 s60, s68, s66\n"
        "s_addc_u32 s61, s69, 0\n"
        "v_mfma_f32_16x16x32_bf16 v[24:27], a[80:83], a[208:211], v[24:27]\n"
        "ds_read_b128 a[208:211], v244 offset:35840\n"
        "s_xor_b32 s66, s17, 4\n"
        "s_lshl_b32 s66, s66, 15\n"
        "v_mfma_f32_16x16x32_bf16 v[0:3], a[176:179], a[0:3], v[0:3]\n"
        "ds_read_b128 a[220:223], v244 offset:48128\n"
        "v_mfma_f32_16x16x32_bf16 v[0:3], a[184:187], a[4:7], v[0:3]\n"
        "v_mfma_f32_16x16x32_bf16 v[0:3], a[32:35], a[8:11], v[0:3]\n"
        "v_mfma_f32_16x16x32_bf16 v[0:3], a[40:43], a[12:15], v[0:3]\n"
        "v_mfma_f32_16x16x32_bf16 v[4:7], a[44:47], a[12:15], v[4:7]\n"
        "s_waitcnt lgkmcnt(4)\n"
        "v_mfma_f32_16x16x32_bf16 v[4:7], a[36:39], a[8:11], v[4:7]\n"
        "v_mfma_f32_16x16x32_bf16 v[4:7], a[188:191], a[4:7], v[4:7]\n"
        "v_mfma_f32_16x16x32_bf16 v[4:7], a[180:183], a[0:3], v[4:7]\n"
        "v_mfma_f32_16x16x32_bf16 v[12:15], a[180:183], a[16:19], v[12:15]\n"
        "v_mfma_f32_16x16x32_bf16 v[12:15], a[188:191], a[20:23], v[12:15]\n"
        "v_mfma_f32_16x16x32_bf16 v[12:15], a[36:39], a[24:27], v[12:15]\n"
        "v_mfma_f32_16x16x32_bf16 v[12:15], a[44:47], a[28:31], v[12:15]\n"
        "v_mfma_f32_16x16x32_bf16 v[8:11], a[40:43], a[28:31], v[8:11]\n"
        "v_mfma_f32_16x16x32_bf16 v[8:11], a[32:35], a[24:27], v[8:11]\n"
        "v_mfma_f32_16x16x32_bf16 v[8:11], a[184:187], a[20:23], v[8:11]\n"
        "v_mfma_f32_16x16x32_bf16 v[8:11], a[176:179], a[16:19], v[8:11]\n"
        "s_waitcnt vmcnt(32)\n"
        "v_mfma_f32_16x16x32_bf16 v[16:19], a[176:179], a[192:195], v[16:19]\n"
        "s_barrier\n"
        "v_mfma_f32_16x16x32_bf16 v[16:19], a[184:187], a[196:199], v[16:19]\n"
        "ds_read_b128 a[0:3], v244 offset:0\n"
        "global_load_lds_dwordx4 v254, s[60:61] offset:0 sc1\n"
        "v_mfma_f32_16x16x32_bf16 v[16:19], a[32:35], a[200:203], v[16:19]\n"
        "ds_read_b128 a[4:7], v244 offset:4096\n"
        "v_mfma_f32_16x16x32_bf16 v[16:19], a[40:43], a[204:207], v[16:19]\n"
        "ds_read_b128 a[8:11], v244 offset:8192\n"
        "v_mfma_f32_16x16x32_bf16 v[20:23], a[44:47], a[204:207], v[20:23]\n"
        "ds_read_b128 a[12:15], v244 offset:12288\n"
        "v_mfma_f32_16x16x32_bf16 v[20:23], a[36:39], a[200:203], v[20:23]\n"
        "ds_read_b128 a[28:31], v244 offset:13312\n"
        "global_load_lds_dwordx4 v254, s[60:61] offset:1024 sc1\n"
        "v_mfma_f32_16x16x32_bf16 v[20:23], a[188:191], a[196:199], v[20:23]\n"
        "ds_read_b128 a[24:27], v244 offset:9216\n"
        "v_mfma_f32_16x16x32_bf16 v[20:23], a[180:183], a[192:195], v[20:23]\n"
        "ds_read_b128 a[20:23], v244 offset:5120\n"
        "s_waitcnt lgkmcnt(7)\n"
        "v_mfma_f32_16x16x32_bf16 v[28:31], a[180:183], a[208:211], v[28:31]\n"
        "ds_read_b128 a[16:19], v244 offset:1024\n"
        "v_mfma_f32_16x16x32_bf16 v[28:31], a[188:191], a[212:215], v[28:31]\n"
        "ds_read_b128 a[192:195], v244 offset:2048\n"
        "s_waitcnt lgkmcnt(8)\n"
        "v_mfma_f32_16x16x32_bf16 v[0:3], a[48:51], a[0:3], v[0:3]\n"
        "ds_read_b128 a[196:199], v244 offset:6144\n"
        "s_waitcnt lgkmcnt(8)\n"
        "v_mfma_f32_16x16x32_bf16 v[0:3], a[56:59], a[4:7], v[0:3]\n"
        "ds_read_b128 a[200:203], v244 offset:10240\n"
        "s_waitcnt lgkmcnt(8)\n"
        "v_mfma_f32_16x16x32_bf16 v[0:3], a[224:227], a[8:11], v[0:3]\n"
        "ds_read_b128 a[204:207], v244 offset:14336\n"
        "s_waitcnt lgkmcnt(8)\n"
        "v_mfma_f32_16x16x32_bf16 v[0:3], a[232:235], a[12:15], v[0:3]\n"
        "global_load_lds_dwordx4 v254, s[60:61] offset:2048 sc1\n"
        "v_mfma_f32_16x16x32_bf16 v[4:7], a[236:239], a[12:15], v[4:7]\n"
        "v_mfma_f32_16x16x32_bf16 v[4:7], a[228:231], a[8:11], v[4:7]\n"
        "v_mfma_f32_16x16x32_bf16 v[4:7], a[60:63], a[4:7], v[4:7]\n"
        "global_load_lds_dwordx4 v254, s[60:61] offset:3072 sc1\n"
        "s_add_u32 m0, s73, 0x18000\n"
        "v_mfma_f32_16x16x32_bf16 v[4:7], a[52:55], a[0:3], v[4:7]\n"
        "s_add_u32 s60, s68, s66\n"
        "s_addc_u32 s61, s69, 0\n"
        "v_cvt_pk_bf16_f32 v0, v0, v1\n"
        "v_mfma_f32_16x16x32_bf16 v[28:31], a[36:39], a[216:219], v[28:31]\n"
        "s_waitcnt lgkmcnt(2)\n"
        "v_cvt_pk_bf16_f32 v1, v2, v3\n"
        "s_xor_b32 s66, s17, 5\n"
        "v_mfma_f32_16x16x32_bf16 v[28:31], a[44:47], a[220:223], v[28:31]\n"
        "s_lshl_b32 s66, s66, 15\n"
        "v_cvt_pk_bf16_f32 v2, v4, v5\n"
        "v_cvt_pk_bf16_f32 v3, v6, v7\n"
        "v_mfma_f32_16x16x32_bf16 v[8:11], a[232:235], a[28:31], v[8:11]\n"
        "global_store_dwordx4 v254, v[0:3], s[28:29] offset:0 sc1\n"
        "v_mfma_f32_16x16x32_bf16 v[8:11], a[224:227], a[24:27], v[8:11]\n"
        "v_mfma_f32_16x16x32_bf16 v[8:11], a[56:59], a[20:23], v[8:11]\n"
        "v_mfma_f32_16x16x32_bf16 v[8:11], a[48:51], a[16:19], v[8:11]\n"
        "s_waitcnt vmcnt(13)\n"
        "v_mfma_f32_16x16x32_bf16 v[12:15], a[52:55], a[16:19], v[12:15]\n"
        "s_barrier\n"
        "v_mfma_f32_16x16x32_bf16 v[12:15], a[60:63], a[20:23], v[12:15]\n"
        "ds_read_b128 a[0:3], v244 offset:49152\n"
        "global_load_lds_dwordx4 v254, s[60:61] offset:0 sc1\n"
        "v_mfma_f32_16x16x32_bf16 v[12:15], a[228:231], a[24:27], v[12:15]\n"
        "ds_read_b128 a[4:7], v244 offset:53248\n"
        "v_cvt_pk_bf16_f32 v8, v8, v9\n"
        "v_mfma_f32_16x16x32_bf16 v[12:15], a[236:239], a[28:31], v[12:15]\n"
        "ds_read_b128 a[8:11], v244 offset:57344\n"
        "v_cvt_pk_bf16_f32 v9, v10, v11\n"
        "v_mfma_f32_16x16x32_bf16 v[24:27], a[40:43], a[220:223], v[24:27]\n"
        "ds_read_b128 a[220:223], v244 offset:15360\n"
        "v_mfma_f32_16x16x32_bf16 v[24:27], a[32:35], a[216:219], v[24:27]\n"
        "ds_read_b128 a[216:219], v244 offset:11264\n"
        "global_load_lds_dwordx4 v254, s[60:61] offset:1024 sc1\n"
        "v_mfma_f32_16x16x32_bf16 v[24:27], a[184:187], a[212:215], v[24:27]\n"
        "ds_read_b128 a[212:215], v244 offset:7168\n"
        "v_cvt_pk_bf16_f32 v10, v12, v13\n"
        "v_mfma_f32_16x16x32_bf16 v[24:27], a[176:179], a[208:211], v[24:27]\n"
        "ds_read_b128 a[208:211], v244 offset:3072\n"
        "v_cvt_pk_bf16_f32 v11, v14, v15\n"
        "v_mfma_f32_16x16x32_bf16 v[16:19], a[48:51], a[192:195], v[16:19]\n"
        "ds_read_b128 a[12:15], v244 offset:61440\n"
        "s_waitcnt lgkmcnt(8)\n"
        "v_mfma_f32_16x16x32_bf16 v[16:19], a[56:59], a[196:199], v[16:19]\n"
        "global_store_dwordx4 v254, v[8:11], s[28:29] offset:1024 sc1\n"
        "ds_read_b128 a[16:19], v244 offset:50176\n"
        "ds_read_b128 a[20:23], v244 offset:54272\n"
        "v_mfma_f32_16x16x32_bf16 v[16:19], a[224:227], a[200:203], v[16:19]\n"
        "ds_read_b128 a[24:27], v244 offset:58368\n"
        "v_mfma_f32_16x16x32_bf16 v[16:19], a[232:235], a[204:207], v[16:19]\n"
        "ds_read_b128 a[28:31], v244 offset:62464\n"
        "v_mfma_f32_16x16x32_bf16 v[20:23], a[236:239], a[204:207], v[20:23]\n"
        "ds_read_b128 a[204:207], v244 offset:63488\n"
        "global_load_lds_dwordx4 v254, s[60:61] offset:2048 sc1\n"
        "v_mfma_f32_16x16x32_bf16 v[20:23], a[228:231], a[200:203], v[20:23]\n"
        "ds_read_b128 a[200:203], v244 offset:59392\n"
        "s_waitcnt lgkmcnt(8)\n"
        "v_mfma_f32_16x16x32_bf16 v[20:23], a[60:63], a[196:199], v[20:23]\n"
        "ds_read_b128 a[196:199], v244 offset:55296\n"
        "v_cvt_pk_bf16_f32 v16, v16, v17\n"
        "v_mfma_f32_16x16x32_bf16 v[20:23], a[52:55], a[192:195], v[20:23]\n"
        "ds_read_b128 a[192:195], v244 offset:51200\n"
        "v_cvt_pk_bf16_f32 v17, v18, v19\n"
        "v_mfma_f32_16x16x32_bf16 v[24:27], a[232:235], a[220:223], v[24:27]\n"
        "global_load_lds_dwordx4 v254, s[60:61] offset:3072 sc1\n"
        "s_add_u32 m0, s73, 0x10000\n"
        "s_waitcnt lgkmcnt(7)\n"
        "v_mfma_f32_16x16x32_bf16 v[24:27], a[224:227], a[216:219], v[24:27]\n"
        "s_add_u32 s60, s68, s66\n"
        "s_addc_u32 s61, s69, 0\n"
        "v_cvt_pk_bf16_f32 v18, v20, v21\n"
        "v_mfma_f32_16x16x32_bf16 v[24:27], a[56:59], a[212:215], v[24:27]\n"
        "s_xor_b32 s66, s17, 6\n"
        "v_cvt_pk_bf16_f32 v19, v22, v23\n"
        "s_lshl_b32 s66, s66, 15\n"
        "v_mfma_f32_16x16x32_bf16 v[24:27], a[48:51], a[208:211], v[24:27]\n"
        "v_mfma_f32_16x16x32_bf16 v[28:31], a[52:55], a[208:211], v[28:31]\n"
        "global_store_dwordx4 v254, v[16:19], s[28:29] offset:2048 sc1\n"
        "ds_read_b128 a[208:211], v244 offset:52224\n"
        "v_lshlrev_b32 v159, 2, v232\n"
        "v_add_u32 v159, 0x0, v159\n"
        "v_mfma_f32_16x16x32_bf16 v[28:31], a[60:63], a[212:215], v[28:31]\n"
        "ds_read_b128 a[212:215], v244 offset:56320\n"
        "v_mfma_f32_16x16x32_bf16 v[28:31], a[228:231], a[216:219], v[28:31]\n"
        "ds_read_b128 a[216:219], v244 offset:60416\n"
        "v_cvt_pk_bf16_f32 v24, v24, v25\n"
        "v_mfma_f32_16x16x32_bf16 v[28:31], a[236:239], a[220:223], v[28:31]\n"
        "ds_read_b128 a[220:223], v244 offset:64512\n"
        "v_cvt_pk_bf16_f32 v25, v26, v27\n"
        "v_mfma_f32_16x16x32_bf16 v[32:35], v[112:115], a[0:3], v[32:35]\n"
        "s_cmp_lg_u32 s40, 0\n"
        "s_cbranch_scc1 LW2_%=\n"
        "s_mov_b64 exec, 0xffff\n"
        "global_store_dwordx2 v234, v[250:251], s[48:49] sc1\n"
        "s_mov_b64 exec, -1\n"
        "LW2_%=:\n"
        "v_mfma_f32_16x16x32_bf16 v[32:35], v[120:123], a[4:7], v[32:35]\n"
        "v_bfe_u32 v234, v254, 4, 3\n"
        "v_lshl_add_u32 v234, v234, 7, s14\n"
        "v_mfma_f32_16x16x32_bf16 v[32:35], a[64:67], a[8:11], v[32:35]\n"
        "s_lshl_b32 s14, s40, 6\n"
        "v_cvt_pk_bf16_f32 v26, v28, v29\n"
        "v_cvt_pk_bf16_f32 v27, v30, v31\n"
        "v_mfma_f32_16x16x32_bf16 v[32:35], a[72:75], a[12:15], v[32:35]\n"
        "global_store_dwordx4 v254, v[24:27], s[28:29] offset:3072 sc1\n"
        "s_add_u32 s28, s38, s16\n"
        "s_addc_u32 s29, s39, 0\n"
        "v_add_u32 v225, s14, v159\n"
        "v_mfma_f32_16x16x32_bf16 v[36:39], a[76:79], a[12:15], v[36:39]\n"
        "v_mfma_f32_16x16x32_bf16 v[36:39], a[68:71], a[8:11], v[36:39]\n"
        "s_waitcnt lgkmcnt(1)\n"
        "v_mfma_f32_16x16x32_bf16 v[36:39], v[124:127], a[4:7], v[36:39]\n"
        "v_mfma_f32_16x16x32_bf16 v[36:39], v[116:119], a[0:3], v[36:39]\n"
        "s_lshl_b32 s16, s63, 12\n"
        "v_and_b32 v150, 1, v233\n"
        "v_lshlrev_b32 v151, 11, v232\n"
        "v_mfma_f32_16x16x32_bf16 v[44:47], v[116:119], a[16:19], v[44:47]\n"
        "s_lshl1_add_u32 s16, s62, s16\n"
        "v_lshl_add_u32 v151, v150, 5, v151\n"
        "v_lshrrev_b32 v150, 1, v233\n"
        "v_mfma_f32_16x16x32_bf16 v[44:47], v[124:127], a[20:23], v[44:47]\n"
        "s_add_u32 s16, s16, 0x3700000\n"
        "v_lshl_add_u32 v151, v150, 4, v151\n"
        "v_lshrrev_b32 v148, 7, v254\n"
        "v_mfma_f32_16x16x32_bf16 v[44:47], a[68:71], a[24:27], v[44:47]\n"
        "s_add_u32 s58, s38, s16\n"
        "s_addc_u32 s59, s39, 0\n"
        "v_lshlrev_b32 v148, 12, v148\n"
        "v_mfma_f32_16x16x32_bf16 v[44:47], a[76:79], a[28:31], v[44:47]\n"
        "s_add_u32 s16, s73, 0x4000\n"
        "v_and_b32 v150, 0x70, v254\n"
        "v_add_u32 v148, v148, v150\n"
        "v_mfma_f32_16x16x32_bf16 v[40:43], a[72:75], a[28:31], v[40:43]\n"
        "v_lshrrev_b32 v149, 6, v254\n"
        "v_lshlrev_b32 v149, 7, v149\n"
        "v_mfma_f32_16x16x32_bf16 v[40:43], a[64:67], a[24:27], v[40:43]\n"
        "v_bfe_u32 v150, v254, 4, 2\n"
        "v_lshl_add_u32 v149, v150, 3, v149\n"
        "v_mfma_f32_16x16x32_bf16 v[40:43], v[120:123], a[20:23], v[40:43]\n"
        "v_mov_b32 v238, 0\n"
        "v_mov_b32 v239, 0\n"
        "v_mfma_f32_16x16x32_bf16 v[40:43], v[112:115], a[16:19], v[40:43]\n"
        "v_mov_b32 v242, 0\n"
        "v_mov_b32 v243, 0\n"
        "v_mfma_f32_16x16x32_bf16 v[48:51], v[112:115], a[192:195], v[48:51]\n"
        "s_barrier\n"
        "v_mfma_f32_16x16x32_bf16 v[48:51], v[120:123], a[196:199], v[48:51]\n"
        "ds_read_b128 a[0:3], v245 offset:0\n"
        "global_load_lds_dwordx4 v254, s[60:61] offset:0 sc1\n"
        "v_mfma_f32_16x16x32_bf16 v[48:51], a[64:67], a[200:203], v[48:51]\n"
        "ds_read_b128 a[4:7], v245 offset:4096\n"
        "v_and_b32 v246, 1, v232\n"
        "v_mfma_f32_16x16x32_bf16 v[48:51], a[72:75], a[204:207], v[48:51]\n"
        "ds_read_b128 a[8:11], v245 offset:8192\n"
        "v_lshlrev_b32 v246, 4, v246\n"
        "v_mfma_f32_16x16x32_bf16 v[52:55], a[76:79], a[204:207], v[52:55]\n"
        "ds_read_b128 a[12:15], v245 offset:12288\n"
        "v_mov_b32 v247, 0x3f80\n"
        "v_mfma_f32_16x16x32_bf16 v[52:55], a[68:71], a[200:203], v[52:55]\n"
        "ds_read_b128 a[16:19], v245 offset:1024\n"
        "global_load_lds_dwordx4 v254, s[60:61] offset:1024 sc1\n"
        "v_mfma_f32_16x16x32_bf16 v[52:55], v[124:127], a[196:199], v[52:55]\n"
        "ds_read_b128 a[20:23], v245 offset:5120\n"
        "v_lshlrev_b32 v246, v246, v247\n"
        "v_mfma_f32_16x16x32_bf16 v[52:55], v[116:119], a[192:195], v[52:55]\n"
        "ds_read_b128 a[24:27], v245 offset:9216\n"
        "v_lshrrev_b32 v247, 1, v232\n"
        "v_mfma_f32_16x16x32_bf16 v[60:63], v[116:119], a[208:211], v[60:63]\n"
        "ds_read_b128 a[28:31], v245 offset:13312\n"
        "v_lshlrev_b32 v248, 1, v233\n"
        "v_mfma_f32_16x16x32_bf16 v[60:63], v[124:127], a[212:215], v[60:63]\n"
        "ds_read_b128 a[192:195], v245 offset:2048\n"
        "global_load_lds_dwordx4 v254, s[60:61] offset:2048 sc1\n"
        "v_mfma_f32_16x16x32_bf16 v[60:63], a[68:71], a[216:219], v[60:63]\n"
        "ds_read_b128 a[196:199], v245 offset:6144\n"
        "s_waitcnt lgkmcnt(8)\n"
        "v_mfma_f32_16x16x32_bf16 v[60:63], a[76:79], a[220:223], v[60:63]\n"
        "ds_read_b128 a[200:203], v245 offset:10240\n"
        "v_or_b32 v249, 1, v248\n"
        "v_mfma_f32_16x16x32_bf16 v[56:59], a[72:75], a[220:223], v[56:59]\n"
        "ds_read_b128 a[204:207], v245 offset:14336\n"
        "v_cmp_eq_u32 s[14:15], v247, v248\n"
        "v_mfma_f32_16x16x32_bf16 v[56:59], a[64:67], a[216:219], v[56:59]\n"
        "ds_read_b128 a[216:219], v245 offset:11264\n"
        "global_load_lds_dwordx4 v254, s[60:61] offset:3072 sc1\n"
        "s_add_u32 m0, s73, 0x1c000\n"
        "v_mfma_f32_16x16x32_bf16 v[56:59], v[120:123], a[212:215], v[56:59]\n"
        "ds_read_b128 a[212:215], v245 offset:7168\n"
        "s_add_u32 s60, s68, s66\n"
        "s_addc_u32 s61, s69, 0\n"
        "v_mfma_f32_16x16x32_bf16 v[56:59], v[112:115], a[208:211], v[56:59]\n"
        "ds_read_b128 a[208:211], v245 offset:3072\n"
        "s_xor_b32 s66, s17, 7\n"
        "s_lshl_b32 s66, s66, 15\n"
        "v_mfma_f32_16x16x32_bf16 v[32:35], a[80:83], a[0:3], v[32:35]\n"
        "ds_read_b128 a[220:223], v245 offset:15360\n"
        "v_cndmask_b32 v240, 0, v246, s[14:15]\n"
        "v_mfma_f32_16x16x32_bf16 v[32:35], a[88:91], a[4:7], v[32:35]\n"
        "s_waitcnt lgkmcnt(7)\n"
        "v_cmp_eq_u32 s[14:15], v247, v249\n"
        "v_add_u32 v149, s16, v149\n"
        "v_mfma_f32_16x16x32_bf16 v[32:35], a[160:163], a[8:11], v[32:35]\n"
        "v_cndmask_b32 v241, 0, v246, s[14:15]\n"
        "v_mfma_f32_16x16x32_bf16 v[32:35], a[168:171], a[12:15], v[32:35]\n"
        "v_mfma_f32_16x16x32_bf16 v[36:39], a[172:175], a[12:15], v[36:39]\n"
        "v_mfma_f32_16x16x32_bf16 v[36:39], a[164:167], a[8:11], v[36:39]\n"
        "v_mfma_f32_16x16x32_bf16 v[36:39], a[92:95], a[4:7], v[36:39]\n"
        "v_mfma_f32_16x16x32_bf16 v[36:39], a[84:87], a[0:3], v[36:39]\n"
        "v_mfma_f32_16x16x32_bf16 v[44:47], a[84:87], a[16:19], v[44:47]\n"
        "v_mfma_f32_16x16x32_bf16 v[44:47], a[92:95], a[20:23], v[44:47]\n"
        "v_mfma_f32_16x16x32_bf16 v[44:47], a[164:167], a[24:27], v[44:47]\n"
        "v_mfma_f32_16x16x32_bf16 v[44:47], a[172:175], a[28:31], v[44:47]\n"
        "v_mfma_f32_16x16x32_bf16 v[40:43], a[168:171], a[28:31], v[40:43]\n"
        "v_mfma_f32_16x16x32_bf16 v[40:43], a[160:163], a[24:27], v[40:43]\n"
        "s_waitcnt lgkmcnt(0)\n"
        "v_mfma_f32_16x16x32_bf16 v[40:43], a[88:91], a[20:23], v[40:43]\n"
        "v_mfma_f32_16x16x32_bf16 v[40:43], a[80:83], a[16:19], v[40:43]\n"
        "v_mfma_f32_16x16x32_bf16 v[48:51], a[80:83], a[192:195], v[48:51]\n"
        "s_barrier\n"
        "v_mfma_f32_16x16x32_bf16 v[48:51], a[88:91], a[196:199], v[48:51]\n"
        "ds_read_b128 a[0:3], v245 offset:16384\n"
        "global_load_lds_dwordx4 v254, s[60:61] offset:0 sc1\n"
        "v_mfma_f32_16x16x32_bf16 v[48:51], a[160:163], a[200:203], v[48:51]\n"
        "ds_read_b128 a[4:7], v245 offset:20480\n"
        "v_mfma_f32_16x16x32_bf16 v[48:51], a[168:171], a[204:207], v[48:51]\n"
        "ds_read_b128 a[8:11], v245 offset:24576\n"
        "v_mfma_f32_16x16x32_bf16 v[52:55], a[172:175], a[204:207], v[52:55]\n"
        "ds_read_b128 a[12:15], v245 offset:28672\n"
        "v_mfma_f32_16x16x32_bf16 v[52:55], a[164:167], a[200:203], v[52:55]\n"
        "ds_read_b128 a[16:19], v245 offset:17408\n"
        "global_load_lds_dwordx4 v254, s[60:61] offset:1024 sc1\n"
        "v_mfma_f32_16x16x32_bf16 v[52:55], a[92:95], a[196:199], v[52:55]\n"
        "LHB1_%=:\n"
        "s_cbranch_execz LHRB_%=\n"
        "ds_read_b128 a[20:23], v245 offset:21504\n"
        "v_mfma_f32_16x16x32_bf16 v[52:55], a[84:87], a[192:195], v[52:55]\n"
        "ds_read_b128 a[24:27], v245 offset:25600\n"
        "v_mfma_f32_16x16x32_bf16 v[60:63], a[84:87], a[208:211], v[60:63]\n"
        "ds_read_b128 a[28:31], v245 offset:29696\n"
        "v_mfma_f32_16x16x32_bf16 v[60:63], a[92:95], a[212:215], v[60:63]\n"
        "ds_read_b128 a[192:195], v245 offset:18432\n"
        "global_load_lds_dwordx4 v254, s[60:61] offset:2048 sc1\n"
        "v_mfma_f32_16x16x32_bf16 v[60:63], a[164:167], a[216:219], v[60:63]\n"
        "ds_read_b128 a[196:199], v245 offset:22528\n"
        "v_mfma_f32_16x16x32_bf16 v[60:63], a[172:175], a[220:223], v[60:63]\n"
        "ds_read_b128 a[200:203], v245 offset:26624\n"
        "v_mfma_f32_16x16x32_bf16 v[56:59], a[168:171], a[220:223], v[56:59]\n"
        "ds_read_b128 a[204:207], v245 offset:30720\n"
        "s_waitcnt lgkmcnt(8)\n"
        "v_mfma_f32_16x16x32_bf16 v[56:59], a[160:163], a[216:219], v[56:59]\n"
        "ds_read_b128 a[216:219], v245 offset:27648\n"
        "global_load_lds_dwordx4 v254, s[60:61] offset:3072 sc1\n"
        "s_add_u32 m0, s73, 0x20000\n"
        "v_mfma_f32_16x16x32_bf16 v[56:59], a[88:91], a[212:215], v[56:59]\n"
        "ds_read_b128 a[212:215], v245 offset:23552\n"
        "s_add_u32 s60, s68, s66\n"
        "s_addc_u32 s61, s69, 0\n"
        "v_mfma_f32_16x16x32_bf16 v[56:59], a[80:83], a[208:211], v[56:59]\n"
        "ds_read_b128 a[208:211], v245 offset:19456\n"
        "s_sub_u32 s66, 15, s12\n"
        "v_mfma_f32_16x16x32_bf16 v[32:35], a[176:179], a[0:3], v[32:35]\n"
        "ds_read_b128 a[220:223], v245 offset:31744\n"
        "s_cmp_lt_u32 s10, 2\n"
        "s_cselect_b32 s66, s12, s66\n"
        "v_mfma_f32_16x16x32_bf16 v[32:35], a[184:187], a[4:7], v[32:35]\n"
        "s_mul_i32 s66, s66, 1052672\n"
        "s_add_u32 s66, s66, s67\n"
        "v_mfma_f32_16x16x32_bf16 v[32:35], a[32:35], a[8:11], v[32:35]\n"
        "s_add_u32 s68, s20, s66\n"
        "s_addc_u32 s69, s21, 0\n"
        "s_xor_b32 s66, s17, 0\n"
        "v_mfma_f32_16x16x32_bf16 v[32:35], a[40:43], a[12:15], v[32:35]\n"
        "s_lshl_b32 s66, s66, 15\n"
        "v_mfma_f32_16x16x32_bf16 v[36:39], a[44:47], a[12:15], v[36:39]\n"
        "s_waitcnt lgkmcnt(4)\n"
        "v_mfma_f32_16x16x32_bf16 v[36:39], a[36:39], a[8:11], v[36:39]\n"
        "v_mfma_f32_16x16x32_bf16 v[36:39], a[188:191], a[4:7], v[36:39]\n"
        "v_mfma_f32_16x16x32_bf16 v[36:39], a[180:183], a[0:3], v[36:39]\n"
        "v_mfma_f32_16x16x32_bf16 v[44:47], a[180:183], a[16:19], v[44:47]\n"
        "v_mfma_f32_16x16x32_bf16 v[44:47], a[188:191], a[20:23], v[44:47]\n"
        "v_mfma_f32_16x16x32_bf16 v[44:47], a[36:39], a[24:27], v[44:47]\n"
        "v_mfma_f32_16x16x32_bf16 v[44:47], a[44:47], a[28:31], v[44:47]\n"
        "v_mfma_f32_16x16x32_bf16 v[40:43], a[40:43], a[28:31], v[40:43]\n"
        "v_mfma_f32_16x16x32_bf16 v[40:43], a[32:35], a[24:27], v[40:43]\n"
        "v_mfma_f32_16x16x32_bf16 v[40:43], a[184:187], a[20:23], v[40:43]\n"
        "v_mfma_f32_16x16x32_bf16 v[40:43], a[176:179], a[16:19], v[40:43]\n"
        "v_mfma_f32_16x16x32_bf16 v[48:51], a[176:179], a[192:195], v[48:51]\n"
        "s_barrier\n"
        "v_mfma_f32_16x16x32_bf16 v[48:51], a[184:187], a[196:199], v[48:51]\n"
        "ds_read_b128 a[0:3], v244 offset:16384\n"
        "global_load_lds_dwordx4 v254, s[60:61] offset:0 sc1\n"
        "v_mfma_f32_16x16x32_bf16 v[48:51], a[32:35], a[200:203], v[48:51]\n"
        "ds_read_b128 a[4:7], v244 offset:20480\n"
        "v_mfma_f32_16x16x32_bf16 v[48:51], a[40:43], a[204:207], v[48:51]\n"
        "ds_read_b128 a[8:11], v244 offset:24576\n"
        "v_mfma_f32_16x16x32_bf16 v[52:55], a[44:47], a[204:207], v[52:55]\n"
        "ds_read_b128 a[12:15], v244 offset:28672\n"
        "v_mfma_f32_16x16x32_bf16 v[52:55], a[36:39], a[200:203], v[52:55]\n"
        "ds_read_b128 a[28:31], v244 offset:29696\n"
        "global_load_lds_dwordx4 v254, s[60:61] offset:1024 sc1\n"
        "v_mfma_f32_16x16x32_bf16 v[52:55], a[188:191], a[196:199], v[52:55]\n"
        "ds_read_b128 a[24:27], v244 offset:25600\n"
        "v_mfma_f32_16x16x32_bf16 v[52:55], a[180:183], a[192:195], v[52:55]\n"
        "ds_read_b128 a[20:23], v244 offset:21504\n"
        "s_waitcnt lgkmcnt(7)\n"
        "v_mfma_f32_16x16x32_bf16 v[60:63], a[180:183], a[208:211], v[60:63]\n"
        "ds_read_b128 a[16:19], v244 offset:17408\n"
        "v_mfma_f32_16x16x32_bf16 v[60:63], a[188:191], a[212:215], v[60:63]\n"
        "ds_read_b128 a[192:195], v244 offset:18432\n"
        "s_waitcnt lgkmcnt(8)\n"
        "v_mfma_f32_16x16x32_bf16 v[32:35], a[48:51], a[0:3], v[32:35]\n"
        "ds_read_b128 a[196:199], v244 offset:22528\n"
        "s_waitcnt lgkmcnt(8)\n"
        "v_mfma_f32_16x16x32_bf16 v[32:35], a[56:59], a[4:7], v[32:35]\n"
        "ds_read_b128 a[200:203], v244 offset:26624\n"
        "s_waitcnt lgkmcnt(8)\n"
        "v_mfma_f32_16x16x32_bf16 v[32:35], a[224:227], a[8:11], v[32:35]\n"
        "ds_read_b128 a[204:207], v244 offset:30720\n"
        "s_waitcnt lgkmcnt(8)\n"
        "v_mfma_f32_16x16x32_bf16 v[32:35], a[232:235], a[12:15], v[32:35]\n"
        "global_load_lds_dwordx4 v254, s[60:61] offset:2048 sc1\n"
        "v_mfma_f32_16x16x32_bf16 v[36:39], a[236:239], a[12:15], v[36:39]\n"
        "v_mfma_f32_16x16x32_bf16 v[36:39], a[228:231], a[8:11], v[36:39]\n"
        "v_mfma_f32_16x16x32_bf16 v[36:39], a[60:63], a[4:7], v[36:39]\n"
        "global_load_lds_dwordx4 v254, s[60:61] offset:3072 sc1\n"
        "s_add_u32 m0, s73, 0x24000\n"
        "v_mfma_f32_16x16x32_bf16 v[36:39], a[52:55], a[0:3], v[36:39]\n"
        "s_add_u32 s60, s68, s66\n"
        "s_addc_u32 s61, s69, 0\n"
        "v_cvt_pk_bf16_f32 v32, v32, v33\n"
        "v_mfma_f32_16x16x32_bf16 v[60:63], a[36:39], a[216:219], v[60:63]\n"
        "s_waitcnt lgkmcnt(2)\n"
        "v_cvt_pk_bf16_f32 v33, v34, v35\n"
        "s_xor_b32 s66, s17, 1\n"
        "v_mfma_f32_16x16x32_bf16 v[60:63], a[44:47], a[220:223], v[60:63]\n"
        "s_lshl_b32 s66, s66, 15\n"
        "v_cvt_pk_bf16_f32 v34, v36, v37\n"
        "v_cvt_pk_bf16_f32 v35, v38, v39\n"
        "v_mfma_f32_16x16x32_bf16 v[40:43], a[232:235], a[28:31], v[40:43]\n"
        "global_store_dwordx4 v254, v[32:35], s[28:29] offset:0 sc1\n"
        "v_mfma_f32_16x16x32_bf16 v[40:43], a[224:227], a[24:27], v[40:43]\n"
        "v_mfma_f32_16x16x32_bf16 v[40:43], a[56:59], a[20:23], v[40:43]\n"
        "v_mfma_f32_16x16x32_bf16 v[40:43], a[48:51], a[16:19], v[40:43]\n"
        "global_load_dwordx2 v[236:237], v234, s[48:49] sc1\n"
        "v_mfma_f32_16x16x32_bf16 v[44:47], a[52:55], a[16:19], v[44:47]\n"
        "s_barrier\n"
        "v_mfma_f32_16x16x32_bf16 v[44:47], a[60:63], a[20:23], v[44:47]\n"
        "ds_read_b128 a[0:3], v254 offset:0\n"
        "global_load_lds_dwordx4 v254, s[60:61] offset:0 sc1\n"
        "v_mfma_f32_16x16x32_bf16 v[44:47], a[228:231], a[24:27], v[44:47]\n"
        "ds_read_b128 a[4:7], v254 offset:4096\n"
        "v_cvt_pk_bf16_f32 v40, v40, v41\n"
        "v_mfma_f32_16x16x32_bf16 v[44:47], a[236:239], a[28:31], v[44:47]\n"
        "ds_read_b128 a[8:11], v254 offset:8192\n"
        "v_cvt_pk_bf16_f32 v41, v42, v43\n"
        "v_mfma_f32_16x16x32_bf16 v[56:59], a[40:43], a[220:223], v[56:59]\n"
        "ds_read_b128 a[220:223], v244 offset:31744\n"
        "v_mfma_f32_16x16x32_bf16 v[56:59], a[32:35], a[216:219], v[56:59]\n"
        "ds_read_b128 a[216:219], v244 offset:27648\n"
        "global_load_lds_dwordx4 v254, s[60:61] offset:1024 sc1\n"
        "v_mfma_f32_16x16x32_bf16 v[56:59], a[184:187], a[212:215], v[56:59]\n"
        "ds_read_b128 a[212:215], v244 offset:23552\n"
        "v_cvt_pk_bf16_f32 v42, v44, v45\n"
        "v_mfma_f32_16x16x32_bf16 v[56:59], a[176:179], a[208:211], v[56:59]\n"
        "ds_read_b128 a[208:211], v244 offset:19456\n"
        "v_cvt_pk_bf16_f32 v43, v46, v47\n"
        "v_mfma_f32_16x16x32_bf16 v[48:51], a[48:51], a[192:195], v[48:51]\n"
        "ds_read_b128 a[12:15], v254 offset:12288\n"
        "s_waitcnt lgkmcnt(8)\n"
        "v_mfma_f32_16x16x32_bf16 v[48:51], a[56:59], a[196:199], v[48:51]\n"
        "global_store_dwordx4 v254, v[40:43], s[28:29] offset:1024 sc1\n"
        "ds_read_b128 a[16:19], v254 offset:1024\n"
        "ds_read_b128 a[20:23], v254 offset:5120\n"
        "v_mfma_f32_16x16x32_bf16 v[48:51], a[224:227], a[200:203], v[48:51]\n"
        "ds_read_b128 a[24:27], v254 offset:9216\n"
        "v_mfma_f32_16x16x32_bf16 v[48:51], a[232:235], a[204:207], v[48:51]\n"
        "ds_read_b128 a[28:31], v254 offset:13312\n"
        "v_mfma_f32_16x16x32_bf16 v[52:55], a[236:239], a[204:207], v[52:55]\n"
        "ds_read_b128 a[204:207], v254 offset:14336\n"
        "global_load_lds_dwordx4 v254, s[60:61] offset:2048 sc1\n"
        "v_mfma_f32_16x16x32_bf16 v[52:55], a[228:231], a[200:203], v[52:55]\n"
        "ds_read_b128 a[200:203], v254 offset:10240\n"
        "s_waitcnt lgkmcnt(8)\n"
        "v_mfma_f32_16x16x32_bf16 v[52:55], a[60:63], a[196:199], v[52:55]\n"
        "ds_read_b128 a[196:199], v254 offset:6144\n"
        "v_cvt_pk_bf16_f32 v48, v48, v49\n"
        "v_mfma_f32_16x16x32_bf16 v[52:55], a[52:55], a[192:195], v[52:55]\n"
        "ds_read_b128 a[192:195], v254 offset:2048\n"
        "v_cvt_pk_bf16_f32 v49, v50, v51\n"
        "v_mfma_f32_16x16x32_bf16 v[56:59], a[232:235], a[220:223], v[56:59]\n"
        "global_load_lds_dwordx4 v254, s[60:61] offset:3072 sc1\n"
        "s_add_u32 m0, s73, 0x14000\n"
        "s_waitcnt lgkmcnt(7)\n"
        "v_mfma_f32_16x16x32_bf16 v[56:59], a[224:227], a[216:219], v[56:59]\n"
        "s_add_u32 s60, s68, s66\n"
        "s_addc_u32 s61, s69, 0\n"
        "v_cvt_pk_bf16_f32 v50, v52, v53\n"
        "v_mfma_f32_16x16x32_bf16 v[56:59], a[56:59], a[212:215], v[56:59]\n"
        "s_xor_b32 s66, s17, 2\n"
        "v_cvt_pk_bf16_f32 v51, v54, v55\n"
        "s_lshl_b32 s66, s66, 15\n"
        "v_mfma_f32_16x16x32_bf16 v[56:59], a[48:51], a[208:211], v[56:59]\n"
        "v_mfma_f32_16x16x32_bf16 v[60:63], a[52:55], a[208:211], v[60:63]\n"
        "global_store_dwordx4 v254, v[48:51], s[28:29] offset:2048 sc1\n"
        "ds_read_b128 a[208:211], v254 offset:3072\n"
        "v_mfma_f32_16x16x32_bf16 v[60:63], a[60:63], a[212:215], v[60:63]\n"
        "ds_read_b128 a[212:215], v254 offset:7168\n"
        "v_mfma_f32_16x16x32_bf16 v[60:63], a[228:231], a[216:219], v[60:63]\n"
        "ds_read_b128 a[216:219], v254 offset:11264\n"
        "v_mfma_f32_16x16x32_bf16 v[60:63], a[236:239], a[220:223], v[60:63]\n"
        "ds_read_b128 a[220:223], v254 offset:15360\n"
        "v_cvt_pk_bf16_f32 v56, v56, v57\n"
        "v_mfma_f32_16x16x32_bf16 v[0:3], a[96:99], a[0:3], 0\n"
        "v_cvt_pk_bf16_f32 v57, v58, v59\n"
        "v_mfma_f32_16x16x32_bf16 v[0:3], a[104:107], a[4:7], v[0:3]\n"
        "v_mfma_f32_16x16x32_bf16 v[0:3], a[112:115], a[8:11], v[0:3]\n"
        "v_mfma_f32_16x16x32_bf16 v[0:3], a[120:123], a[12:15], v[0:3]\n"
        "v_mfma_f32_16x16x32_bf16 v[4:7], a[124:127], a[12:15], 0\n"
        "v_cvt_pk_bf16_f32 v58, v60, v61\n"
        "v_cvt_pk_bf16_f32 v59, v62, v63\n"
        "v_mfma_f32_16x16x32_bf16 v[4:7], a[116:119], a[8:11], v[4:7]\n"
        "global_store_dwordx4 v254, v[56:59], s[28:29] offset:3072 sc1\n"
        "s_waitcnt lgkmcnt(2)\n"
        "v_mfma_f32_16x16x32_bf16 v[4:7], a[108:111], a[4:7], v[4:7]\n"
        "v_mfma_f32_16x16x32_bf16 v[4:7], a[100:103], a[0:3], v[4:7]\n"
        "v_mfma_f32_16x16x32_bf16 v[12:15], a[100:103], a[16:19], 0\n"
        "v_mfma_f32_16x16x32_bf16 v[12:15], a[108:111], a[20:23], v[12:15]\n"
        "v_mfma_f32_16x16x32_bf16 v[12:15], a[116:119], a[24:27], v[12:15]\n"
        "v_mfma_f32_16x16x32_bf16 v[12:15], a[124:127], a[28:31], v[12:15]\n"
        "s_lshl2_add_u32 s15, s76, s10\n"
        "s_mul_i32 s16, s13, 263168\n"
        "s_lshl_b32 s15, s15, 14\n"
        "v_mfma_f32_16x16x32_bf16 v[8:11], a[120:123], a[28:31], 0\n"
        "s_add_u32 s16, s16, s15\n"
        "s_add_u32 s16, s16, s73\n"
        "s_add_u32 s16, s16, 0x4000000\n"
        "v_mfma_f32_16x16x32_bf16 v[8:11], a[112:115], a[24:27], v[8:11]\n"
        "s_add_u32 s28, s38, s16\n"
        "s_addc_u32 s29, s39, 0\n"
        "v_mfma_f32_16x16x32_bf16 v[8:11], a[104:107], a[20:23], v[8:11]\n"
        "v_mfma_f32_16x16x32_bf16 v[8:11], a[96:99], a[16:19], v[8:11]\n"
        "s_waitcnt vmcnt(37)\n"
        "v_mfma_f32_16x16x32_bf16 v[16:19], a[96:99], a[192:195], 0\n"
        "s_barrier\n"
        "v_mfma_f32_16x16x32_bf16 v[16:19], a[104:107], a[196:199], v[16:19]\n"
        "ds_read_b128 a[0:3], v254 offset:16384\n"
        "global_load_lds_dwordx4 v254, s[60:61] offset:0 sc1\n"
        "v_mfma_f32_16x16x32_bf16 v[16:19], a[112:115], a[200:203], v[16:19]\n"
        "ds_read_b128 a[4:7], v254 offset:20480\n"
        "v_mfma_f32_16x16x32_bf16 v[16:19], a[120:123], a[204:207], v[16:19]\n"
        "ds_read_b128 a[8:11], v254 offset:24576\n"
        "v_mfma_f32_16x16x32_bf16 v[20:23], a[124:127], a[204:207], 0\n"
        "ds_read_b128 a[12:15], v254 offset:28672\n"
        "v_mfma_f32_16x16x32_bf16 v[20:23], a[116:119], a[200:203], v[20:23]\n"
        "ds_read_b128 a[16:19], v254 offset:17408\n"
        "global_load_lds_dwordx4 v254, s[60:61] offset:1024 sc1\n"
        "v_mfma_f32_16x16x32_bf16 v[20:23], a[108:111], a[196:199], v[20:23]\n"
        "ds_read_b128 a[20:23], v254 offset:21504\n"
        "s_waitcnt lgkmcnt(6)\n"
        "v_mfma_f32_16x16x32_bf16 v[20:23], a[100:103], a[192:195], v[20:23]\n"
        "ds_read_b128 a[24:27], v254 offset:25600\n"
        "v_mfma_f32_16x16x32_bf16 v[28:31], a[100:103], a[208:211], 0\n"
        "ds_read_b128 a[28:31], v254 offset:29696\n"
        "v_mfma_f32_16x16x32_bf16 v[28:31], a[108:111], a[212:215], v[28:31]\n"
        "ds_read_b128 a[192:195], v254 offset:18432\n"
        "global_load_lds_dwordx4 v254, s[60:61] offset:2048 sc1\n"
        "v_mfma_f32_16x16x32_bf16 v[28:31], a[116:119], a[216:219], v[28:31]\n"
        "ds_read_b128 a[196:199], v254 offset:22528\n"
        "v_mfma_f32_16x16x32_bf16 v[28:31], a[124:127], a[220:223], v[28:31]\n"
        "ds_read_b128 a[200:203], v254 offset:26624\n"
        "v_mfma_f32_16x16x32_bf16 v[24:27], a[120:123], a[220:223], 0\n"
        "ds_read_b128 a[204:207], v254 offset:30720\n"
        "s_waitcnt lgkmcnt(8)\n"
        "v_mfma_f32_16x16x32_bf16 v[24:27], a[112:115], a[216:219], v[24:27]\n"
        "ds_read_b128 a[216:219], v254 offset:27648\n"
        "global_load_lds_dwordx4 v254, s[60:61] offset:3072 sc1\n"
        "s_add_u32 m0, s73, 0x4000\n"
        "v_mfma_f32_16x16x32_bf16 v[24:27], a[104:107], a[212:215], v[24:27]\n"
        "ds_read_b128 a[212:215], v254 offset:23552\n"
        "s_add_u32 s60, s68, s66\n"
        "s_addc_u32 s61, s69, 0\n"
        "v_mfma_f32_16x16x32_bf16 v[24:27], a[96:99], a[208:211], v[24:27]\n"
        "ds_read_b128 a[208:211], v254 offset:19456\n"
        "s_xor_b32 s66, s17, 3\n"
        "s_lshl_b32 s66, s66, 15\n"
        "v_mfma_f32_16x16x32_bf16 v[0:3], a[128:131], a[0:3], v[0:3]\n"
        "ds_read_b128 a[220:223], v254 offset:31744\n"
        "v_mfma_f32_16x16x32_bf16 v[0:3], a[136:139], a[4:7], v[0:3]\n"
        "v_mfma_f32_16x16x32_bf16 v[0:3], a[144:147], a[8:11], v[0:3]\n"
        "v_mfma_f32_16x16x32_bf16 v[0:3], a[152:155], a[12:15], v[0:3]\n"
        "v_mfma_f32_16x16x32_bf16 v[4:7], a[156:159], a[12:15], v[4:7]\n"
        "s_waitcnt lgkmcnt(4)\n"
        "v_mfma_f32_16x16x32_bf16 v[4:7], a[148:151], a[8:11], v[4:7]\n"
        "v_mfma_f32_16x16x32_bf16 v[4:7], a[140:143], a[4:7], v[4:7]\n"
        "v_mfma_f32_16x16x32_bf16 v[4:7], a[132:135], a[0:3], v[4:7]\n"
        "v_mfma_f32_16x16x32_bf16 v[12:15], a[132:135], a[16:19], v[12:15]\n"
        "v_mfma_f32_16x16x32_bf16 v[12:15], a[140:143], a[20:23], v[12:15]\n"
        "v_mfma_f32_16x16x32_bf16 v[12:15], a[148:151], a[24:27], v[12:15]\n"
        "v_mfma_f32_16x16x32_bf16 v[12:15], a[156:159], a[28:31], v[12:15]\n"
        "v_mfma_f32_16x16x32_bf16 v[8:11], a[152:155], a[28:31], v[8:11]\n"
        "v_mfma_f32_16x16x32_bf16 v[8:11], a[144:147], a[24:27], v[8:11]\n"
        "v_mfma_f32_16x16x32_bf16 v[8:11], a[136:139], a[20:23], v[8:11]\n"
        "v_mfma_f32_16x16x32_bf16 v[8:11], a[128:131], a[16:19], v[8:11]\n"
        "s_waitcnt vmcnt(37)\n"
        "v_mfma_f32_16x16x32_bf16 v[16:19], a[128:131], a[192:195], v[16:19]\n"
        "s_barrier\n"
        "v_mfma_f32_16x16x32_bf16 v[16:19], a[136:139], a[196:199], v[16:19]\n"
        "ds_write_b32 v225, v224\n"
        "v_mfma_f32_16x16x32_bf16 v[16:19], a[144:147], a[200:203], v[16:19]\n"
        "ds_read_b128 a[0:3], v254 offset:32768\n"
        "v_mfma_f32_16x16x32_bf16 v[16:19], a[152:155], a[204:207], v[16:19]\n"
        "ds_read_b128 a[4:7], v254 offset:36864\n"
        "v_mfma_f32_16x16x32_bf16 v[20:23], a[156:159], a[204:207], v[20:23]\n"
        "ds_read_b128 a[8:11], v254 offset:40960\n"
        "s_waitcnt lgkmcnt(4)\n"
        "v_mfma_f32_16x16x32_bf16 v[20:23], a[148:151], a[200:203], v[20:23]\n"
        "ds_read_b128 a[12:15], v254 offset:45056\n"
        "v_mfma_f32_16x16x32_bf16 v[20:23], a[140:143], a[196:199], v[20:23]\n"
        "ds_read_b128 a[16:19], v254 offset:33792\n"
        "v_mfma_f32_16x16x32_bf16 v[20:23], a[132:135], a[192:195], v[20:23]\n"
        "ds_read_b128 a[20:23], v254 offset:37888\n"
        "v_mfma_f32_16x16x32_bf16 v[28:31], a[132:135], a[208:211], v[28:31]\n"
        "ds_read_b128 a[24:27], v254 offset:41984\n"
        "v_mfma_f32_16x16x32_bf16 v[28:31], a[140:143], a[212:215], v[28:31]\n"
        "ds_read_b128 a[28:31], v254 offset:46080\n"
        "v_mfma_f32_16x16x32_bf16 v[28:31], a[148:151], a[216:219], v[28:31]\n"
        "ds_read_b128 a[192:195], v254 offset:34816\n"
        "v_mfma_f32_16x16x32_bf16 v[28:31], a[156:159], a[220:223], v[28:31]\n"
        "ds_read_b128 a[196:199], v254 offset:38912\n"
        "v_mfma_f32_16x16x32_bf16 v[24:27], a[152:155], a[220:223], v[24:27]\n"
        "ds_read_b128 a[200:203], v254 offset:43008\n"
        "s_waitcnt lgkmcnt(8)\n"
        "v_mfma_f32_16x16x32_bf16 v[24:27], a[144:147], a[216:219], v[24:27]\n"
        "ds_read_b128 a[204:207], v254 offset:47104\n"
        "v_mfma_f32_16x16x32_bf16 v[24:27], a[136:139], a[212:215], v[24:27]\n"
        "ds_read_b128 a[212:215], v254 offset:39936\n"
        "v_mfma_f32_16x16x32_bf16 v[24:27], a[128:131], a[208:211], v[24:27]\n"
        "ds_read_b128 a[208:211], v254 offset:35840\n"
        "s_waitcnt lgkmcnt(8)\n"
        "v_mfma_f32_16x16x32_bf16 v[0:3], v[160:163], a[0:3], v[0:3]\n"
        "ds_read_b128 a[216:219], v254 offset:44032\n"
        "v_mfma_f32_16x16x32_bf16 v[0:3], v[168:171], a[4:7], v[0:3]\n"
        "ds_read_b128 a[220:223], v254 offset:48128\n"
        "v_mfma_f32_16x16x32_bf16 v[0:3], v[176:179], a[8:11], v[0:3]\n"
        "v_mfma_f32_16x16x32_bf16 v[0:3], v[184:187], a[12:15], v[0:3]\n"
        "s_waitcnt vmcnt(11) lgkmcnt(6)\n"
        "v_cmp_ne_u64 vcc, v[236:237], v[250:251]\n"
        "v_mfma_f32_16x16x32_bf16 v[4:7], v[188:191], a[12:15], v[4:7]\n"
        "s_cbranch_vccnz LPZZ_%=\n"
        "LBZZ_%=:\n"
        "v_mfma_f32_16x16x32_bf16 v[4:7], v[180:183], a[8:11], v[4:7]\n"
        "v_mfma_f32_16x16x32_bf16 v[4:7], v[172:175], a[4:7], v[4:7]\n"
        "v_mfma_f32_16x16x32_bf16 v[4:7], v[164:167], a[0:3], v[4:7]\n"
        "v_mfma_f32_16x16x32_bf16 v[12:15], v[164:167], a[16:19], v[12:15]\n"
        "v_mfma_f32_16x16x32_bf16 v[12:15], v[172:175], a[20:23], v[12:15]\n"
        "v_mfma_f32_16x16x32_bf16 v[12:15], v[180:183], a[24:27], v[12:15]\n"
        "v_mfma_f32_16x16x32_bf16 v[12:15], v[188:191], a[28:31], v[12:15]\n"
        "v_mfma_f32_16x16x32_bf16 v[8:11], v[184:187], a[28:31], v[8:11]\n"
        "v_mfma_f32_16x16x32_bf16 v[8:11], v[176:179], a[24:27], v[8:11]\n"
        "v_mfma_f32_16x16x32_bf16 v[8:11], v[168:171], a[20:23], v[8:11]\n"
        "s_waitcnt lgkmcnt(0)\n"
        "v_mfma_f32_16x16x32_bf16 v[8:11], v[160:163], a[16:19], v[8:11]\n"
        "v_mfma_f32_16x16x32_bf16 v[16:19], v[160:163], a[192:195], v[16:19]\n"
        "s_barrier\n"
        "v_mfma_f32_16x16x32_bf16 v[16:19], v[168:171], a[196:199], v[16:19]\n"
        "ds_read_b128 a[0:3], v254 offset:49152\n"
        "global_load_lds_dwordx4 v148, s[58:59] offset:0 sc1\n"
        "s_add_u32 m0, s73, 0x8000\n"
        "v_mfma_f32_16x16x32_bf16 v[16:19], v[176:179], a[200:203], v[16:19]\n"
        "ds_read_b128 a[4:7], v254 offset:53248\n"
        "s_add_u32 s58, s58, 0x8000\n"
        "s_addc_u32 s59, s59, 0\n"
        "v_mfma_f32_16x16x32_bf16 v[16:19], v[184:187], a[204:207], v[16:19]\n"
        "ds_read_b128 a[8:11], v254 offset:57344\n"
        "v_mfma_f32_16x16x32_bf16 v[20:23], v[188:191], a[204:207], v[20:23]\n"
        "ds_read_b128 a[12:15], v254 offset:61440\n"
        "v_mfma_f32_16x16x32_bf16 v[20:23], v[180:183], a[200:203], v[20:23]\n"
        "ds_read_b128 a[16:19], v254 offset:50176\n"
        "global_load_dwordx4 v[96:99], v151, s[64:65]\n"
        "v_mfma_f32_16x16x32_bf16 v[20:23], v[172:175], a[196:199], v[20:23]\n"
        "ds_read_b128 a[20:23], v254 offset:54272\n"
        "s_add_u32 s64, s64, 0x8000\n"
        "s_addc_u32 s65, s65, 0\n"
        "v_mfma_f32_16x16x32_bf16 v[20:23], v[164:167], a[192:195], v[20:23]\n"
        "ds_read_b128 a[24:27], v254 offset:58368\n"
        "v_mfma_f32_16x16x32_bf16 v[28:31], v[164:167], a[208:211], v[28:31]\n"
        "ds_read_b128 a[28:31], v254 offset:62464\n"
        "s_lshl2_add_u32 s14, s13, s10\n"
        "s_lshl2_add_u32 s14, s14, s40\n"
        "v_mfma_f32_16x16x32_bf16 v[28:31], v[172:175], a[212:215], v[28:31]\n"
        "ds_read_b128 a[192:195], v254 offset:51200\n"
        "s_add_u32 s14, s14, 3072\n"
        "v_lshrrev_b32 v154, 1, v254\n"
        "v_mfma_f32_16x16x32_bf16 v[28:31], v[180:183], a[216:219], v[28:31]\n"
        "ds_read_b128 a[196:199], v254 offset:55296\n"
        "s_lshl_b32 s14, s14, 7\n"
        "v_add_u32 v154, s14, v154\n"
        "v_mfma_f32_16x16x32_bf16 v[28:31], v[188:191], a[220:223], v[28:31]\n"
        "ds_read_b128 a[200:203], v254 offset:59392\n"
        "v_mfma_f32_16x16x32_bf16 v[24:27], v[184:187], a[220:223], v[24:27]\n"
        "ds_read_b128 a[204:207], v254 offset:63488\n"
        "s_waitcnt lgkmcnt(8)\n"
        "v_mfma_f32_16x16x32_bf16 v[24:27], v[176:179], a[216:219], v[24:27]\n"
        "ds_read_b128 a[216:219], v254 offset:60416\n"
        "v_mfma_f32_16x16x32_bf16 v[24:27], v[168:171], a[212:215], v[24:27]\n"
        "ds_read_b128 a[212:215], v254 offset:56320\n"
        "v_mfma_f32_16x16x32_bf16 v[24:27], v[160:163], a[208:211], v[24:27]\n"
        "ds_read_b128 a[208:211], v254 offset:52224\n"
        "v_mfma_f32_16x16x32_bf16 v[0:3], v[192:195], a[0:3], v[0:3]\n"
        "ds_read_b128 a[220:223], v254 offset:64512\n"
        "v_mfma_f32_16x16x32_bf16 v[0:3], v[200:203], a[4:7], v[0:3]\n"
        "v_mfma_f32_16x16x32_bf16 v[0:3], v[208:211], a[8:11], v[0:3]\n"
        "v_mfma_f32_16x16x32_bf16 v[0:3], v[216:219], a[12:15], v[0:3]\n"
        "v_mfma_f32_16x16x32_bf16 v[4:7], v[220:223], a[12:15], v[4:7]\n"
        "s_waitcnt lgkmcnt(4)\n"
        "v_mfma_f32_16x16x32_bf16 v[4:7], v[212:215], a[8:11], v[4:7]\n"
        "v_mfma_f32_16x16x32_bf16 v[4:7], v[204:207], a[4:7], v[4:7]\n"
        "v_mfma_f32_16x16x32_bf16 v[4:7], v[196:199], a[0:3], v[4:7]\n"
        "v_mfma_f32_16x16x32_bf16 v[12:15], v[196:199], a[16:19], v[12:15]\n"
        "v_mfma_f32_16x16x32_bf16 v[12:15], v[204:207], a[20:23], v[12:15]\n"
        "v_mfma_f32_16x16x32_bf16 v[12:15], v[212:215], a[24:27], v[12:15]\n"
        "v_mfma_f32_16x16x32_bf16 v[12:15], v[220:223], a[28:31], v[12:15]\n"
        "v_mfma_f32_16x16x32_bf16 v[8:11], v[216:219], a[28:31], v[8:11]\n"
        "v_mfma_f32_16x16x32_bf16 v[8:11], v[208:211], a[24:27], v[8:11]\n"
        "v_mfma_f32_16x16x32_bf16 v[8:11], v[200:203], a[20:23], v[8:11]\n"
        "v_mfma_f32_16x16x32_bf16 v[8:11], v[192:195], a[16:19], v[8:11]\n"
        "v_mfma_f32_16x16x32_bf16 v[16:19], v[192:195], a[192:195], v[16:19]\n"
        "s_barrier\n"
        "v_mfma_f32_16x16x32_bf16 v[16:19], v[200:203], a[196:199], v[16:19]\n"
        "ds_read_b128 a[0:3], v244 offset:32768\n"
        "global_load_lds_dwordx4 v254, s[60:61] offset:0 sc1\n"
        "v_mfma_f32_16x16x32_bf16 v[16:19], v[208:211], a[200:203], v[16:19]\n"
        "ds_read_b128 a[4:7], v244 offset:36864\n"
        "v_mfma_f32_16x16x32_bf16 v[16:19], v[216:219], a[204:207], v[16:19]\n"
        "ds_read_b128 a[8:11], v244 offset:40960\n"
        "v_mfma_f32_16x16x32_bf16 v[20:23], v[220:223], a[204:207], v[20:23]\n"
        "ds_read_b128 a[12:15], v244 offset:45056\n"
        "s_waitcnt lgkmcnt(4)\n"
        "v_mfma_f32_16x16x32_bf16 v[20:23], v[212:215], a[200:203], v[20:23]\n"
        "ds_read_b128 a[16:19], v244 offset:33792\n"
        "global_load_lds_dwordx4 v254, s[60:61] offset:1024 sc1\n"
        "v_mfma_f32_16x16x32_bf16 v[20:23], v[204:207], a[196:199], v[20:23]\n"
        "ds_read_b128 a[20:23], v244 offset:37888\n"
        "v_mfma_f32_16x16x32_bf16 v[20:23], v[196:199], a[192:195], v[20:23]\n"
        "ds_read_b128 a[24:27], v244 offset:41984\n"
        "v_mfma_f32_16x16x32_bf16 v[28:31], v[196:199], a[208:211], v[28:31]\n"
        "ds_read_b128 a[28:31], v244 offset:46080\n"
        "v_mfma_f32_16x16x32_bf16 v[28:31], v[204:207], a[212:215], v[28:31]\n"
        "ds_read_b128 a[192:195], v244 offset:34816\n"
        "global_load_lds_dwordx4 v254, s[60:61] offset:2048 sc1\n"
        "v_mfma_f32_16x16x32_bf16 v[28:31], v[212:215], a[216:219], v[28:31]\n"
        "ds_read_b128 a[196:199], v244 offset:38912\n"
        "v_mfma_f32_16x16x32_bf16 v[28:31], v[220:223], a[220:223], v[28:31]\n"
        "ds_read_b128 a[200:203], v244 offset:43008\n"
        "v_mfma_f32_16x16x32_bf16 v[24:27], v[216:219], a[220:223], v[24:27]\n"
        "ds_read_b128 a[204:207], v244 offset:47104\n"
        "s_waitcnt lgkmcnt(8)\n"
        "v_mfma_f32_16x16x32_bf16 v[24:27], v[208:211], a[216:219], v[24:27]\n"
        "ds_read_b128 a[216:219], v244 offset:44032\n"
        "global_load_lds_dwordx4 v254, s[60:61] offset:3072 sc1\n"
        "s_add_u32 m0, s73, 0x4400\n"
        "v_mfma_f32_16x16x32_bf16 v[24:27], v[200:203], a[212:215], v[24:27]\n"
        "ds_read_b128 a[212:215], v244 offset:39936\n"
        "s_add_u32 s60, s68, s66\n"
        "s_addc_u32 s61, s69, 0\n"
        "v_mfma_f32_16x16x32_bf16 v[24:27], v[192:195], a[208:211], v[24:27]\n"
        "ds_read_b128 a[208:211], v244 offset:35840\n"
        "s_xor_b32 s66, s17, 4\n"
        "s_lshl_b32 s66, s66, 15\n"
        "v_mfma_f32_16x16x32_bf16 v[0:3], v[112:115], a[0:3], v[0:3]\n"
        "ds_read_b128 a[220:223], v244 offset:48128\n"
        "v_mfma_f32_16x16x32_bf16 v[0:3], v[120:123], a[4:7], v[0:3]\n"
        "ds_read_b32 v224, v159 offset:0\n"
        "global_load_lds_dwordx4 v148, s[58:59] offset:0 sc1\n"
        "s_add_u32 m0, s73, 0xc000\n"
        "v_mfma_f32_16x16x32_bf16 v[0:3], a[64:67], a[8:11], v[0:3]\n"
        "ds_read_b32 v225, v159 offset:64\n"
        "s_add_u32 s58, s58, 0x8000\n"
        "s_addc_u32 s59, s59, 0\n"
        "v_mfma_f32_16x16x32_bf16 v[0:3], a[72:75], a[12:15], v[0:3]\n"
        "ds_read_b32 v226, v159 offset:128\n"
        "s_and_b32 s14, s14, 0xfffff9ff\n"
        "v_bfe_u32 v155, v254, 4, 2\n"
        "v_mfma_f32_16x16x32_bf16 v[4:7], a[76:79], a[12:15], v[4:7]\n"
        "ds_read_b32 v227, v159 offset:192\n"
        "v_lshl_add_u32 v155, v155, 9, s14\n"
        "v_mfma_f32_16x16x32_bf16 v[4:7], a[68:71], a[8:11], v[4:7]\n"
        "global_load_dwordx4 v[100:103], v151, s[64:65]\n"
        "s_add_u32 s64, s64, 0x8000\n"
        "s_addc_u32 s65, s65, 0\n"
        "v_mfma_f32_16x16x32_bf16 v[4:7], v[124:127], a[4:7], v[4:7]\n"
        "s_waitcnt lgkmcnt(6)\n"
        "v_mfma_f32_16x16x32_bf16 v[4:7], v[116:119], a[0:3], v[4:7]\n"
        "v_mfma_f32_16x16x32_bf16 v[12:15], v[116:119], a[16:19], v[12:15]\n"
        "v_mfma_f32_16x16x32_bf16 v[12:15], v[124:127], a[20:23], v[12:15]\n"
        "s_waitcnt lgkmcnt(3)\n"
        "v_sub_f32 v228, 1.0, v224\n"
        "v_cvt_pk_bf16_f32 v228, 0, v228\n"
        "v_mfma_f32_16x16x32_bf16 v[12:15], a[68:71], a[24:27], v[12:15]\n"
        "s_waitcnt lgkmcnt(2)\n"
        "v_sub_f32 v229, 1.0, v225\n"
        "v_cvt_pk_bf16_f32 v229, 0, v229\n"
        "v_mfma_f32_16x16x32_bf16 v[12:15], a[76:79], a[28:31], v[12:15]\n"
        "s_waitcnt lgkmcnt(1)\n"
        "v_sub_f32 v230, 1.0, v226\n"
        "v_cvt_pk_bf16_f32 v230, 0, v230\n"
        "v_mfma_f32_16x16x32_bf16 v[8:11], a[72:75], a[28:31], v[8:11]\n"
        "s_waitcnt lgkmcnt(0)\n"
        "v_sub_f32 v231, 1.0, v227\n"
        "v_cvt_pk_bf16_f32 v231, 0, v231\n"
        "v_mfma_f32_16x16x32_bf16 v[8:11], a[64:67], a[24:27], v[8:11]\n"
        "v_mfma_f32_16x16x32_bf16 v[8:11], v[120:123], a[20:23], v[8:11]\n"
        "v_mfma_f32_16x16x32_bf16 v[8:11], v[112:115], a[16:19], v[8:11]\n"
        "v_mfma_f32_16x16x32_bf16 v[16:19], v[112:115], a[192:195], v[16:19]\n"
        "s_barrier\n"
        "v_mfma_f32_16x16x32_bf16 v[16:19], v[120:123], a[196:199], v[16:19]\n"
        "ds_read_b128 a[0:3], v244 offset:0\n"
        "global_load_lds_dwordx4 v254, s[60:61] offset:0 sc1\n"
        "v_mfma_f32_16x16x32_bf16 v[16:19], a[64:67], a[200:203], v[16:19]\n"
        "ds_read_b128 a[4:7], v244 offset:4096\n"
        "v_mfma_f32_16x16x32_bf16 v[16:19], a[72:75], a[204:207], v[16:19]\n"
        "ds_read_b128 a[8:11], v244 offset:8192\n"
        "v_mfma_f32_16x16x32_bf16 v[20:23], a[76:79], a[204:207], v[20:23]\n"
        "ds_read_b128 a[12:15], v244 offset:12288\n"
        "v_mfma_f32_16x16x32_bf16 v[20:23], a[68:71], a[200:203], v[20:23]\n"
        "ds_read_b128 a[16:19], v244 offset:1024\n"
        "global_load_lds_dwordx4 v254, s[60:61] offset:1024 sc1\n"
        "v_mfma_f32_16x16x32_bf16 v[20:23], v[124:127], a[196:199], v[20:23]\n"
        "ds_read_b128 a[20:23], v244 offset:5120\n"
        "v_mfma_f32_16x16x32_bf16 v[20:23], v[116:119], a[192:195], v[20:23]\n"
        "ds_read_b128 a[24:27], v244 offset:9216\n"
        "v_mfma_f32_16x16x32_bf16 v[28:31], v[116:119], a[208:211], v[28:31]\n"
        "ds_read_b128 a[28:31], v244 offset:13312\n"
        "v_mfma_f32_16x16x32_bf16 v[28:31], v[124:127], a[212:215], v[28:31]\n"
        "ds_read_b128 a[192:195], v244 offset:2048\n"
        "global_load_lds_dwordx4 v254, s[60:61] offset:2048 sc1\n"
        "v_mfma_f32_16x16x32_bf16 v[28:31], a[68:71], a[216:219], v[28:31]\n"
        "ds_read_b128 a[196:199], v244 offset:6144\n"
        "v_mfma_f32_16x16x32_bf16 v[28:31], a[76:79], a[220:223], v[28:31]\n"
        "ds_read_b128 a[200:203], v244 offset:10240\n"
        "v_mfma_f32_16x16x32_bf16 v[24:27], a[72:75], a[220:223], v[24:27]\n"
        "ds_read_b128 a[204:207], v244 offset:14336\n"
        "s_waitcnt lgkmcnt(8)\n"
        "v_mfma_f32_16x16x32_bf16 v[24:27], a[64:67], a[216:219], v[24:27]\n"
        "ds_read_b128 a[216:219], v244 offset:11264\n"
        "global_load_lds_dwordx4 v254, s[60:61] offset:3072 sc1\n"
        "s_add_u32 m0, s73, 0x4800\n"
        "v_mfma_f32_16x16x32_bf16 v[24:27], v[120:123], a[212:215], v[24:27]\n"
        "ds_read_b128 a[212:215], v244 offset:7168\n"
        "s_add_u32 s60, s68, s66\n"
        "s_addc_u32 s61, s69, 0\n"
        "v_mfma_f32_16x16x32_bf16 v[24:27], v[112:115], a[208:211], v[24:27]\n"
        "ds_read_b128 a[208:211], v244 offset:3072\n"
        "s_xor_b32 s66, s17, 5\n"
        "s_lshl_b32 s66, s66, 15\n"
        "v_mfma_f32_16x16x32_bf16 v[0:3], a[80:83], a[0:3], v[0:3]\n"
        "ds_read_b128 a[220:223], v244 offset:15360\n"
        "v_mfma_f32_16x16x32_bf16 v[0:3], a[88:91], a[4:7], v[0:3]\n"
        "global_load_lds_dwordx4 v148, s[58:59] offset:0 sc1\n"
        "s_add_u32 s58, s58, 0x8000\n"
        "s_addc_u32 s59, s59, 0\n"
        "v_mfma_f32_16x16x32_bf16 v[0:3], a[160:163], a[8:11], v[0:3]\n"
        "s_add_u32 m0, s73, 0x18000\n"
        "v_mfma_f32_16x16x32_bf16 v[0:3], a[168:171], a[12:15], v[0:3]\n"
        "v_mfma_f32_16x16x32_bf16 v[4:7], a[172:175], a[12:15], v[4:7]\n"
        "global_load_dwordx4 v[104:107], v151, s[64:65]\n"
        "v_mfma_f32_16x16x32_bf16 v[4:7], a[164:167], a[8:11], v[4:7]\n"
        "s_add_u32 s64, s64, 0x8000\n"
        "s_addc_u32 s65, s65, 0\n"
        "s_waitcnt lgkmcnt(3)\n"
        "v_mfma_f32_16x16x32_bf16 v[4:7], a[92:95], a[4:7], v[4:7]\n"
        "v_mfma_f32_16x16x32_bf16 v[4:7], a[84:87], a[0:3], v[4:7]\n"
        "v_mfma_f32_16x16x32_bf16 v[12:15], a[84:87], a[16:19], v[12:15]\n"
        "v_mfma_f32_16x16x32_bf16 v[12:15], a[92:95], a[20:23], v[12:15]\n"
        "v_mfma_f32_16x16x32_bf16 v[12:15], a[164:167], a[24:27], v[12:15]\n"
        "v_mfma_f32_16x16x32_bf16 v[12:15], a[172:175], a[28:31], v[12:15]\n"
        "v_mfma_f32_16x16x32_bf16 v[8:11], a[168:171], a[28:31], v[8:11]\n"
        "v_mfma_f32_16x16x32_bf16 v[8:11], a[160:163], a[24:27], v[8:11]\n"
        "v_mfma_f32_16x16x32_bf16 v[8:11], a[88:91], a[20:23], v[8:11]\n"
        "v_mfma_f32_16x16x32_bf16 v[8:11], a[80:83], a[16:19], v[8:11]\n"
        "v_mfma_f32_16x16x32_bf16 v[16:19], a[80:83], a[192:195], v[16:19]\n"
        "s_barrier\n"
        "v_mfma_f32_16x16x32_bf16 v[16:19], a[88:91], a[196:199], v[16:19]\n"
        "ds_read_b128 a[0:3], v244 offset:49152\n"
        "global_load_lds_dwordx4 v254, s[60:61] offset:0 sc1\n"
        "s_mul_i32 s16, s13, 263168\n"
        "v_mfma_f32_16x16x32_bf16 v[16:19], a[160:163], a[200:203], v[16:19]\n"
        "ds_read_b128 a[4:7], v244 offset:53248\n"
        "s_lshl2_add_u32 s15, s10, s74\n"
        "s_lshl_b32 s15, s15, 14\n"
        "LHB2_%=:\n"
        "s_cbranch_execz LHRB_%=\n"
        "v_mfma_f32_16x16x32_bf16 v[16:19], a[168:171], a[204:207], v[16:19]\n"
        "ds_read_b128 a[8:11], v244 offset:57344\n"
        "s_add_u32 s16, s16, s15\n"
        "s_add_u32 s16, s16, s73\n"
        "v_mfma_f32_16x16x32_bf16 v[20:23], a[172:175], a[204:207], v[20:23]\n"
        "ds_read_b128 a[12:15], v244 offset:61440\n"
        "s_add_u32 s16, s16, 0x4000000\n"
        "s_lshl2_add_u32 s15, s10, s75\n"
        "v_mfma_f32_16x16x32_bf16 v[20:23], a[164:167], a[200:203], v[20:23]\n"
        "ds_read_b128 a[16:19], v244 offset:50176\n"
        "global_load_lds_dwordx4 v254, s[60:61] offset:1024 sc1\n"
        "s_lshl_b32 s15, s15, 14\n"
        "v_mfma_f32_16x16x32_bf16 v[20:23], a[92:95], a[196:199], v[20:23]\n"
        "ds_read_b128 a[20:23], v244 offset:54272\n"
        "s_add_u32 s30, s38, s16\n"
        "s_addc_u32 s31, s39, 0\n"
        "v_mfma_f32_16x16x32_bf16 v[20:23], a[84:87], a[192:195], v[20:23]\n"
        "ds_read_b128 a[24:27], v244 offset:58368\n"
        "s_mul_i32 s16, s13, 263168\n"
        "s_waitcnt lgkmcnt(7)\n"
        "v_mfma_f32_16x16x32_bf16 v[28:31], a[84:87], a[208:211], v[28:31]\n"
        "ds_read_b128 a[28:31], v244 offset:62464\n"
        "s_add_u32 s16, s16, s15\n"
        "s_add_u32 s16, s16, s73\n"
        "v_mfma_f32_16x16x32_bf16 v[28:31], a[92:95], a[212:215], v[28:31]\n"
        "ds_read_b128 a[192:195], v244 offset:51200\n"
        "global_load_lds_dwordx4 v254, s[60:61] offset:2048 sc1\n"
        "s_add_u32 s16, s16, 0x4000000\n"
        "v_mfma_f32_16x16x32_bf16 v[28:31], a[164:167], a[216:219], v[28:31]\n"
        "ds_read_b128 a[196:199], v244 offset:55296\n"
        "s_lshl2_add_u32 s15, s10, s76\n"
        "s_lshl_b32 s15, s15, 14\n"
        "v_mfma_f32_16x16x32_bf16 v[28:31], a[172:175], a[220:223], v[28:31]\n"
        "ds_read_b128 a[200:203], v244 offset:59392\n"
        "s_waitcnt vmcnt(15) lgkmcnt(8)\n"
        "v_mfma_f32_16x16x32_bf16 v[24:27], a[168:171], a[220:223], v[24:27]\n"
        "ds_read_b128 a[204:207], v244 offset:63488\n"
        "v_mfma_f32_16x16x32_bf16 v[24:27], a[160:163], a[216:219], v[24:27]\n"
        "ds_read_b128 a[216:219], v244 offset:60416\n"
        "global_load_lds_dwordx4 v254, s[60:61] offset:3072 sc1\n"
        "s_add_u32 m0, s73, 0x4c00\n"
        "v_mfma_f32_16x16x32_bf16 v[24:27], a[88:91], a[212:215], v[24:27]\n"
        "ds_read_b128 a[212:215], v244 offset:56320\n"
        "s_add_u32 s60, s68, s66\n"
        "s_addc_u32 s61, s69, 0\n"
        "v_mfma_f32_16x16x32_bf16 v[24:27], a[80:83], a[208:211], v[24:27]\n"
        "s_waitcnt lgkmcnt(7)\n"
        "ds_read_b128 a[208:211], v244 offset:52224\n"
        "s_xor_b32 s66, s17, 6\n"
        "v_mfma_f32_16x16x32_bf16 v[0:3], a[176:179], a[0:3], v[0:3]\n"
        "ds_read_b128 a[220:223], v244 offset:64512\n"
        "s_lshl_b32 s66, s66, 15\n"
        "v_mfma_f32_16x16x32_bf16 v[0:3], a[184:187], a[4:7], v[0:3]\n"
        "v_permlane16_swap_b32 v96, v98\n"
        "v_mfma_f32_16x16x32_bf16 v[0:3], a[32:35], a[8:11], v[0:3]\n"
        "v_permlane16_swap_b32 v97, v99\n"
        "v_mfma_f32_16x16x32_bf16 v[0:3], a[40:43], a[12:15], v[0:3]\n"
        "global_load_lds_dwordx4 v148, s[58:59] offset:0 sc1\n"
        "s_add_u32 m0, s73, 0x10000\n"
        "v_lshlrev_b32 v64, 16, v96\n"
        "v_mfma_f32_16x16x32_bf16 v[4:7], a[44:47], a[12:15], v[4:7]\n"
        "v_and_b32 v65, s35, v96\n"
        "v_lshlrev_b32 v66, 16, v97\n"
        "v_mfma_f32_16x16x32_bf16 v[4:7], a[36:39], a[8:11], v[4:7]\n"
        "v_and_b32 v67, s35, v97\n"
        "v_lshlrev_b32 v68, 16, v98\n"
        "v_mfma_f32_16x16x32_bf16 v[4:7], a[188:191], a[4:7], v[4:7]\n"
        "global_load_dwordx4 v[108:111], v151, s[64:65]\n"
        "v_mfma_f32_16x16x32_bf16 v[4:7], a[180:183], a[0:3], v[4:7]\n"
        "s_waitcnt lgkmcnt(2)\n"
        "v_and_b32 v69, s35, v98\n"
        "v_lshlrev_b32 v70, 16, v99\n"
        "v_mfma_f32_16x16x32_bf16 v[12:15], a[180:183], a[16:19], v[12:15]\n"
        "v_and_b32 v71, s35, v99\n"
        "v_mfma_f32_16x16x32_bf16 v[12:15], a[188:191], a[20:23], v[12:15]\n"
        "v_mfma_f32_16x16x32_bf16 v[12:15], a[36:39], a[24:27], v[12:15]\n"
        "v_mfma_f32_16x16x32_bf16 v[12:15], a[44:47], a[28:31], v[12:15]\n"
        "v_mfma_f32_16x16x32_bf16 v[8:11], a[40:43], a[28:31], v[8:11]\n"
        "v_mfma_f32_16x16x32_bf16 v[8:11], a[32:35], a[24:27], v[8:11]\n"
        "v_mfma_f32_16x16x32_bf16 v[8:11], a[184:187], a[20:23], v[8:11]\n"
        "v_mfma_f32_16x16x32_bf16 v[8:11], a[176:179], a[16:19], v[8:11]\n"
        "v_mfma_f32_16x16x32_bf16 v[16:19], a[176:179], a[192:195], v[16:19]\n"
        "s_barrier\n"
        "v_mfma_f32_16x16x32_bf16 v[16:19], a[184:187], a[196:199], v[16:19]\n"
        "ds_read_b128 a[0:3], v245 offset:0\n"
        "global_load_lds_dwordx4 v254, s[60:61] offset:0 sc1\n"
        "v_mfma_f32_16x16x32_bf16 v[16:19], a[32:35], a[200:203], v[16:19]\n"
        "ds_read_b128 a[4:7], v245 offset:4096\n"
        "v_mfma_f32_16x16x32_bf16 v[16:19], a[40:43], a[204:207], v[16:19]\n"
        "ds_read_b128 a[8:11], v245 offset:8192\n"
        "v_mfma_f32_16x16x32_bf16 v[20:23], a[44:47], a[204:207], v[20:23]\n"
        "ds_read_b128 a[12:15], v245 offset:12288\n"
        "v_mfma_f32_16x16x32_bf16 v[20:23], a[36:39], a[200:203], v[20:23]\n"
        "ds_read_b128 a[28:31], v245 offset:13312\n"
        "global_load_lds_dwordx4 v254, s[60:61] offset:1024 sc1\n"
        "v_mfma_f32_16x16x32_bf16 v[20:23], a[188:191], a[196:199], v[20:23]\n"
        "ds_read_b128 a[24:27], v245 offset:9216\n"
        "v_mfma_f32_16x16x32_bf16 v[20:23], a[180:183], a[192:195], v[20:23]\n"
        "ds_read_b128 a[20:23], v245 offset:5120\n"
        "s_waitcnt lgkmcnt(7)\n"
        "v_mfma_f32_16x16x32_bf16 v[28:31], a[180:183], a[208:211], v[28:31]\n"
        "ds_read_b128 a[16:19], v245 offset:1024\n"
        "v_mfma_f32_16x16x32_bf16 v[28:31], a[188:191], a[212:215], v[28:31]\n"
        "ds_read_b128 a[192:195], v245 offset:2048\n"
        "s_waitcnt lgkmcnt(8)\n"
        "v_mfma_f32_16x16x32_bf16 v[0:3], a[48:51], a[0:3], v[0:3]\n"
        "ds_read_b128 a[196:199], v245 offset:6144\n"
        "s_waitcnt lgkmcnt(8)\n"
        "v_mfma_f32_16x16x32_bf16 v[0:3], a[56:59], a[4:7], v[0:3]\n"
        "ds_read_b128 a[200:203], v245 offset:10240\n"
        "s_waitcnt lgkmcnt(8)\n"
        "v_mfma_f32_16x16x32_bf16 v[0:3], a[224:227], a[8:11], v[0:3]\n"
        "ds_read_b128 a[204:207], v245 offset:14336\n"
        "s_waitcnt lgkmcnt(8)\n"
        "v_mfma_f32_16x16x32_bf16 v[0:3], a[232:235], a[12:15], v[0:3]\n"
        "global_load_lds_dwordx4 v254, s[60:61] offset:2048 sc1\n"
        "v_mfma_f32_16x16x32_bf16 v[4:7], a[236:239], a[12:15], v[4:7]\n"
        "v_mfma_f32_16x16x32_bf16 v[4:7], a[228:231], a[8:11], v[4:7]\n"
        "v_mfma_f32_16x16x32_bf16 v[4:7], a[60:63], a[4:7], v[4:7]\n"
        "global_load_lds_dwordx4 v254, s[60:61] offset:3072 sc1\n"
        "s_add_u32 m0, s73, 0x1c000\n"
        "v_mfma_f32_16x16x32_bf16 v[4:7], a[52:55], a[0:3], v[4:7]\n"
        "s_add_u32 s60, s68, s66\n"
        "s_addc_u32 s61, s69, 0\n"
        "v_cvt_pk_bf16_f32 v0, v0, v1\n"
        "v_mfma_f32_16x16x32_bf16 v[28:31], a[36:39], a[216:219], v[28:31]\n"
        "s_waitcnt lgkmcnt(2)\n"
        "v_cvt_pk_bf16_f32 v1, v2, v3\n"
        "s_xor_b32 s66, s17, 7\n"
        "v_mfma_f32_16x16x32_bf16 v[28:31], a[44:47], a[220:223], v[28:31]\n"
        "s_lshl_b32 s66, s66, 15\n"
        "v_cvt_pk_bf16_f32 v2, v4, v5\n"
        "v_cvt_pk_bf16_f32 v3, v6, v7\n"
        "v_mfma_f32_16x16x32_bf16 v[8:11], a[232:235], a[28:31], v[8:11]\n"
        "global_store_dwordx4 v254, v[0:3], s[28:29] offset:0 sc1\n"
        "s_waitcnt vmcnt(17) lgkmcnt(0)\n"
        "v_permlane16_swap_b32 v100, v102\n"
        "v_lshlrev_b32 v72, 16, v100\n"
        "v_mfma_f32_16x16x32_bf16 v[8:11], a[224:227], a[24:27], v[8:11]\n"
        "v_permlane16_swap_b32 v101, v103\n"
        "v_mfma_f32_16x16x32_bf16 v[8:11], a[56:59], a[20:23], v[8:11]\n"
        "v_and_b32 v73, s35, v100\n"
        "v_lshlrev_b32 v74, 16, v101\n"
        "v_mfma_f32_16x16x32_bf16 v[8:11], a[48:51], a[16:19], v[8:11]\n"
        "v_and_b32 v75, s35, v101\n"
        "v_lshlrev_b32 v76, 16, v102\n"
        "v_mfma_f32_16x16x32_bf16 v[12:15], a[52:55], a[16:19], v[12:15]\n"
        "s_barrier\n"
        "v_mfma_f32_16x16x32_bf16 v[12:15], a[60:63], a[20:23], v[12:15]\n"
        "ds_read_b128 a[0:3], v245 offset:16384\n"
        "global_load_lds_dwordx4 v254, s[60:61] offset:0 sc1\n"
        "v_mfma_f32_16x16x32_bf16 v[12:15], a[228:231], a[24:27], v[12:15]\n"
        "ds_read_b128 a[4:7], v245 offset:20480\n"
        "v_cvt_pk_bf16_f32 v8, v8, v9\n"
        "v_mfma_f32_16x16x32_bf16 v[12:15], a[236:239], a[28:31], v[12:15]\n"
        "ds_read_b128 a[8:11], v245 offset:24576\n"
        "v_cvt_pk_bf16_f32 v9, v10, v11\n"
        "v_mfma_f32_16x16x32_bf16 v[24:27], a[40:43], a[220:223], v[24:27]\n"
        "ds_read_b128 a[220:223], v245 offset:15360\n"
        "v_and_b32 v77, s35, v102\n"
        "v_mfma_f32_16x16x32_bf16 v[24:27], a[32:35], a[216:219], v[24:27]\n"
        "ds_read_b128 a[216:219], v245 offset:11264\n"
        "global_load_lds_dwordx4 v254, s[60:61] offset:1024 sc1\n"
        "v_mfma_f32_16x16x32_bf16 v[24:27], a[184:187], a[212:215], v[24:27]\n"
        "ds_read_b128 a[212:215], v245 offset:7168\n"
        "v_cvt_pk_bf16_f32 v10, v12, v13\n"
        "v_mfma_f32_16x16x32_bf16 v[24:27], a[176:179], a[208:211], v[24:27]\n"
        "ds_read_b128 a[208:211], v245 offset:3072\n"
        "v_cvt_pk_bf16_f32 v11, v14, v15\n"
        "v_mfma_f32_16x16x32_bf16 v[16:19], a[48:51], a[192:195], v[16:19]\n"
        "ds_read_b128 a[12:15], v245 offset:28672\n"
        "v_lshlrev_b32 v78, 16, v103\n"
        "v_mfma_f32_16x16x32_bf16 v[16:19], a[56:59], a[196:199], v[16:19]\n"
        "global_store_dwordx4 v254, v[8:11], s[28:29] offset:1024 sc1\n"
        "ds_read_b128 a[16:19], v245 offset:17408\n"
        "ds_read_b128 a[20:23], v245 offset:21504\n"
        "v_and_b32 v79, s35, v103\n"
        "v_mfma_f32_16x16x32_bf16 v[16:19], a[224:227], a[200:203], v[16:19]\n"
        "ds_read_b128 a[24:27], v245 offset:25600\n"
        "v_mfma_f32_16x16x32_bf16 v[16:19], a[232:235], a[204:207], v[16:19]\n"
        "ds_read_b128 a[28:31], v245 offset:29696\n"
        "v_mfma_f32_16x16x32_bf16 v[20:23], a[236:239], a[204:207], v[20:23]\n"
        "ds_read_b128 a[204:207], v245 offset:30720\n"
        "global_load_lds_dwordx4 v254, s[60:61] offset:2048 sc1\n"
        "v_mfma_f32_16x16x32_bf16 v[20:23], a[228:231], a[200:203], v[20:23]\n"
        "ds_read_b128 a[200:203], v245 offset:26624\n"
        "s_waitcnt lgkmcnt(8)\n"
        "v_mfma_f32_16x16x32_bf16 v[20:23], a[60:63], a[196:199], v[20:23]\n"
        "ds_read_b128 a[196:199], v245 offset:22528\n"
        "v_cvt_pk_bf16_f32 v16, v16, v17\n"
        "v_mfma_f32_16x16x32_bf16 v[20:23], a[52:55], a[192:195], v[20:23]\n"
        "ds_read_b128 a[192:195], v245 offset:18432\n"
        "v_cvt_pk_bf16_f32 v17, v18, v19\n"
        "v_mfma_f32_16x16x32_bf16 v[24:27], a[232:235], a[220:223], v[24:27]\n"
        "global_load_lds_dwordx4 v254, s[60:61] offset:3072 sc1\n"
        "s_add_u32 m0, s73, 0x20000\n"
        "s_waitcnt lgkmcnt(7)\n"
        "v_mfma_f32_16x16x32_bf16 v[24:27], a[224:227], a[216:219], v[24:27]\n"
        "s_add_u32 s60, s68, s66\n"
        "s_addc_u32 s61, s69, 0\n"
        "v_cvt_pk_bf16_f32 v18, v20, v21\n"
        "v_mfma_f32_16x16x32_bf16 v[28:31], a[52:55], a[208:211], v[28:31]\n"
        "v_cvt_pk_bf16_f32 v19, v22, v23\n"
        "v_mfma_f32_16x16x32_bf16 v[24:27], a[56:59], a[212:215], v[24:27]\n"
        "v_mfma_f32_16x16x32_bf16 v[28:31], a[60:63], a[212:215], v[28:31]\n"
        "global_store_dwordx4 v254, v[16:19], s[28:29] offset:2048 sc1\n"
        "ds_read_b128 a[212:215], v245 offset:23552\n"
        "v_mfma_f32_16x16x32_bf16 v[24:27], a[48:51], a[208:211], v[24:27]\n"
        "ds_read_b128 a[208:211], v245 offset:19456\n"
        "v_mfma_f32_16x16x32_bf16 v[28:31], a[228:231], a[216:219], v[28:31]\n"
        "ds_read_b128 a[216:219], v245 offset:27648\n"
        "v_mfma_f32_16x16x32_bf16 v[28:31], a[236:239], a[220:223], v[28:31]\n"
        "ds_read_b128 a[220:223], v245 offset:31744\n"
        "s_waitcnt vmcnt(17) lgkmcnt(4)\n"
        "v_mfma_f32_16x16x32_bf16 v[32:35], a[96:99], a[0:3], 0\n"
        "v_permlane16_swap_b32 v104, v106\n"
        "v_mfma_f32_16x16x32_bf16 v[32:35], a[104:107], a[4:7], v[32:35]\n"
        "v_cvt_pk_bf16_f32 v24, v24, v25\n"
        "v_cvt_pk_bf16_f32 v25, v26, v27\n"
        "v_mfma_f32_16x16x32_bf16 v[32:35], a[112:115], a[8:11], v[32:35]\n"
        "v_cvt_pk_bf16_f32 v26, v28, v29\n"
        "v_cvt_pk_bf16_f32 v27, v30, v31\n"
        "v_mfma_f32_16x16x32_bf16 v[32:35], a[120:123], a[12:15], v[32:35]\n"
        "global_store_dwordx4 v254, v[24:27], s[28:29] offset:3072 sc1\n"
        "v_permlane16_swap_b32 v105, v107\n"
        "v_lshlrev_b32 v80, 16, v104\n"
        "v_and_b32 v81, s35, v104\n"
        "v_mfma_f32_16x16x32_bf16 v[36:39], a[124:127], a[12:15], 0\n"
        "v_lshlrev_b32 v82, 16, v105\n"
        "v_and_b32 v83, s35, v105\n"
        "v_mfma_f32_16x16x32_bf16 v[36:39], a[116:119], a[8:11], v[36:39]\n"
        "v_lshlrev_b32 v84, 16, v106\n"
        "v_and_b32 v85, s35, v106\n"
        "v_mfma_f32_16x16x32_bf16 v[36:39], a[108:111], a[4:7], v[36:39]\n"
        "v_lshlrev_b32 v86, 16, v107\n"
        "v_and_b32 v87, s35, v107\n"
        "v_mfma_f32_16x16x32_bf16 v[36:39], a[100:103], a[0:3], v[36:39]\n"
        "v_mfma_f32_16x16x32_bf16 v[44:47], a[100:103], a[16:19], 0\n"
        "v_mfma_f32_16x16x32_bf16 v[44:47], a[108:111], a[20:23], v[44:47]\n"
        "v_mfma_f32_16x16x32_bf16 v[44:47], a[116:119], a[24:27], v[44:47]\n"
        "v_mfma_f32_16x16x32_bf16 v[44:47], a[124:127], a[28:31], v[44:47]\n"
        "v_mfma_f32_16x16x32_bf16 v[40:43], a[120:123], a[28:31], 0\n"
        "v_mfma_f32_16x16x32_bf16 v[40:43], a[112:115], a[24:27], v[40:43]\n"
        "v_mfma_f32_16x16x32_bf16 v[40:43], a[104:107], a[20:23], v[40:43]\n"
        "v_mfma_f32_16x16x32_bf16 v[40:43], a[96:99], a[16:19], v[40:43]\n"
        "v_mfma_f32_16x16x32_bf16 v[48:51], a[96:99], a[192:195], 0\n"
        "s_barrier\n"
        "v_mfma_f32_16x16x32_bf16 v[48:51], a[104:107], a[196:199], v[48:51]\n"
        "ds_read_b128 a[0:3], v244 offset:16384\n"
        "global_load_lds_dwordx4 v254, s[60:61] offset:0 sc1\n"
        "v_mfma_f32_16x16x32_bf16 v[48:51], a[112:115], a[200:203], v[48:51]\n"
        "ds_read_b128 a[4:7], v244 offset:20480\n"
        "v_mfma_f32_16x16x32_bf16 v[48:51], a[120:123], a[204:207], v[48:51]\n"
        "ds_read_b128 a[8:11], v244 offset:24576\n"
        "v_mfma_f32_16x16x32_bf16 v[52:55], a[124:127], a[204:207], 0\n"
        "ds_read_b128 a[12:15], v244 offset:28672\n"
        "s_waitcnt lgkmcnt(4)\n"
        "v_mfma_f32_16x16x32_bf16 v[52:55], a[116:119], a[200:203], v[52:55]\n"
        "ds_read_b128 a[16:19], v244 offset:17408\n"
        "global_load_lds_dwordx4 v254, s[60:61] offset:1024 sc1\n"
        "v_mfma_f32_16x16x32_bf16 v[52:55], a[108:111], a[196:199], v[52:55]\n"
        "ds_read_b128 a[20:23], v244 offset:21504\n"
        "v_mfma_f32_16x16x32_bf16 v[52:55], a[100:103], a[192:195], v[52:55]\n"
        "ds_read_b128 a[24:27], v244 offset:25600\n"
        "v_mfma_f32_16x16x32_bf16 v[60:63], a[100:103], a[208:211], 0\n"
        "ds_read_b128 a[28:31], v244 offset:29696\n"
        "v_mfma_f32_16x16x32_bf16 v[60:63], a[108:111], a[212:215], v[60:63]\n"
        "ds_read_b128 a[192:195], v244 offset:18432\n"
        "global_load_lds_dwordx4 v254, s[60:61] offset:2048 sc1\n"
        "v_mfma_f32_16x16x32_bf16 v[60:63], a[116:119], a[216:219], v[60:63]\n"
        "ds_read_b128 a[196:199], v244 offset:22528\n"
        "v_mfma_f32_16x16x32_bf16 v[60:63], a[124:127], a[220:223], v[60:63]\n"
        "ds_read_b128 a[200:203], v244 offset:26624\n"
        "v_mfma_f32_16x16x32_bf16 v[56:59], a[120:123], a[220:223], 0\n"
        "ds_read_b128 a[204:207], v244 offset:30720\n"
        "s_waitcnt lgkmcnt(8)\n"
        "v_mfma_f32_16x16x32_bf16 v[56:59], a[112:115], a[216:219], v[56:59]\n"
        "ds_read_b128 a[216:219], v244 offset:27648\n"
        "global_load_lds_dwordx4 v254, s[60:61] offset:3072 sc1\n"
        "v_mfma_f32_16x16x32_bf16 v[56:59], a[104:107], a[212:215], v[56:59]\n"
        "ds_read_b128 a[212:215], v244 offset:23552\n"
        "s_waitcnt vmcnt(17)\n"
        "v_mfma_f32_16x16x32_bf16 v[56:59], a[96:99], a[208:211], v[56:59]\n"
        "ds_read_b128 a[208:211], v244 offset:19456\n"
        "v_mfma_f32_16x16x32_bf16 v[32:35], a[128:131], a[0:3], v[32:35]\n"
        "ds_read_b128 a[220:223], v244 offset:31744\n"
        "s_waitcnt vmcnt(16) lgkmcnt(8)\n"
        "v_mfma_f32_16x16x32_bf16 v[32:35], a[136:139], a[4:7], v[32:35]\n"
        "ds_read_b64_tr_b16 v[128:129], v149 offset:0\n"
        "v_mfma_f32_16x16x32_bf16 v[32:35], a[144:147], a[8:11], v[32:35]\n"
        "ds_read_b64_tr_b16 v[130:131], v149 offset:2048\n"
        "v_mfma_f32_16x16x32_bf16 v[32:35], a[152:155], a[12:15], v[32:35]\n"
        "ds_read_b64_tr_b16 v[132:133], v149 offset:32\n"
        "v_mfma_f32_16x16x32_bf16 v[36:39], a[156:159], a[12:15], v[36:39]\n"
        "ds_read_b64_tr_b16 v[134:135], v149 offset:2080\n"
        "v_mfma_f32_16x16x32_bf16 v[36:39], a[148:151], a[8:11], v[36:39]\n"
        "ds_read_b64_tr_b16 v[136:137], v149 offset:64\n"
        "v_mfma_f32_16x16x32_bf16 v[36:39], a[140:143], a[4:7], v[36:39]\n"
        "ds_read_b64_tr_b16 v[138:139], v149 offset:2112\n"
        "v_mfma_f32_16x16x32_bf16 v[36:39], a[132:135], a[0:3], v[36:39]\n"
        "ds_read_b64_tr_b16 v[140:141], v149 offset:96\n"
        "v_mfma_f32_16x16x32_bf16 v[44:47], a[132:135], a[16:19], v[44:47]\n"
        "ds_read_b64_tr_b16 v[142:143], v149 offset:2144\n"
        "v_mfma_f32_16x16x32_bf16 v[44:47], a[140:143], a[20:23], v[44:47]\n"
        "s_waitcnt lgkmcnt(7)\n"
        "v_permlane16_swap_b32 v108, v110\n"
        "v_mfma_f32_16x16x32_bf16 v[44:47], a[148:151], a[24:27], v[44:47]\n"
        "s_waitcnt lgkmcnt(6)\n"
        "v_permlane16_swap_b32 v109, v111\n"
        "v_mfma_f32_16x16x32_bf16 v[44:47], a[156:159], a[28:31], v[44:47]\n"
        "v_lshlrev_b32 v246, 16, v128\n"
        "v_mul_f32 v64, v64, v246\n"
        "v_mfma_f32_16x16x32_bf16 v[40:43], a[152:155], a[28:31], v[40:43]\n"
        "s_waitcnt lgkmcnt(4)\n"
        "v_cvt_pk_bf16_f32 v64, 0, v64\n"
        "v_mul_f32 v64, v64, v228\n"
        "v_mfma_f32_16x16x32_bf16 v[40:43], a[144:147], a[24:27], v[40:43]\n"
        "v_cvt_pk_bf16_f32 v64, 0, v64\n"
        "v_and_b32 v247, s35, v128\n"
        "v_mfma_f32_16x16x32_bf16 v[40:43], a[136:139], a[20:23], v[40:43]\n"
        "v_mul_f32 v65, v65, v247\n"
        "v_cvt_pk_bf16_f32 v65, 0, v65\n"
        "v_mfma_f32_16x16x32_bf16 v[40:43], a[128:131], a[16:19], v[40:43]\n"
        "v_mul_f32 v65, v65, v228\n"
        "v_cvt_pk_bf16_f32 v65, 0, v65\n"
        "v_mfma_f32_16x16x32_bf16 v[48:51], a[128:131], a[192:195], v[48:51]\n"
        "s_barrier\n"
        "v_mfma_f32_16x16x32_bf16 v[48:51], a[136:139], a[196:199], v[48:51]\n"
        "ds_read_b128 a[0:3], v254 offset:32768\n"
        "v_lshlrev_b32 v248, 16, v129\n"
        "v_mfma_f32_16x16x32_bf16 v[48:51], a[144:147], a[200:203], v[48:51]\n"
        "ds_read_b128 a[4:7], v254 offset:36864\n"
        "v_mul_f32 v66, v66, v248\n"
        "v_mfma_f32_16x16x32_bf16 v[48:51], a[152:155], a[204:207], v[48:51]\n"
        "ds_read_b128 a[8:11], v254 offset:40960\n"
        "v_cvt_pk_bf16_f32 v66, 0, v66\n"
        "v_mfma_f32_16x16x32_bf16 v[52:55], a[156:159], a[204:207], v[52:55]\n"
        "ds_read_b128 a[12:15], v254 offset:45056\n"
        "v_mul_f32 v66, v66, v228\n"
        "v_mfma_f32_16x16x32_bf16 v[52:55], a[148:151], a[200:203], v[52:55]\n"
        "ds_read_b128 a[16:19], v254 offset:33792\n"
        "v_cvt_pk_bf16_f32 v66, 0, v66\n"
        "v_mfma_f32_16x16x32_bf16 v[52:55], a[140:143], a[196:199], v[52:55]\n"
        "ds_read_b128 a[20:23], v254 offset:37888\n"
        "v_and_b32 v249, s35, v129\n"
        "v_mfma_f32_16x16x32_bf16 v[52:55], a[132:135], a[192:195], v[52:55]\n"
        "ds_read_b128 a[24:27], v254 offset:41984\n"
        "v_mul_f32 v67, v67, v249\n"
        "v_mfma_f32_16x16x32_bf16 v[60:63], a[132:135], a[208:211], v[60:63]\n"
        "ds_read_b128 a[28:31], v254 offset:46080\n"
        "v_cvt_pk_bf16_f32 v67, 0, v67\n"
        "v_mfma_f32_16x16x32_bf16 v[60:63], a[140:143], a[212:215], v[60:63]\n"
        "ds_read_b128 a[192:195], v254 offset:34816\n"
        "v_mul_f32 v67, v67, v228\n"
        "v_mfma_f32_16x16x32_bf16 v[60:63], a[148:151], a[216:219], v[60:63]\n"
        "ds_read_b128 a[196:199], v254 offset:38912\n"
        "s_waitcnt vmcnt(4)\n"
        "v_mfma_f32_16x16x32_bf16 v[60:63], a[156:159], a[220:223], v[60:63]\n"
        "s_mov_b64 exec, 0xffff\n"
        "global_store_dwordx2 v154, v[250:251], s[48:49] sc1\n"
        "s_mov_b64 exec, -1\n"
        "v_mfma_f32_16x16x32_bf16 v[56:59], a[152:155], a[220:223], v[56:59]\n"
        "ds_read_b128 a[200:203], v254 offset:43008\n"
        "v_cvt_pk_bf16_f32 v67, 0, v67\n"
        "v_mfma_f32_16x16x32_bf16 v[56:59], a[144:147], a[216:219], v[56:59]\n"
        "ds_read_b128 a[204:207], v254 offset:47104\n"
        "v_lshlrev_b32 v144, 16, v130\n"
        "v_mfma_f32_16x16x32_bf16 v[56:59], a[136:139], a[212:215], v[56:59]\n"
        "ds_read_b128 a[212:215], v254 offset:39936\n"
        "v_mul_f32 v68, v68, v144\n"
        "v_mfma_f32_16x16x32_bf16 v[56:59], a[128:131], a[208:211], v[56:59]\n"
        "ds_read_b128 a[208:211], v254 offset:35840\n"
        "s_waitcnt lgkmcnt(7)\n"
        "v_mfma_f32_16x16x32_bf16 v[32:35], v[160:163], a[0:3], v[32:35]\n"
        "ds_read_b128 a[216:219], v254 offset:44032\n"
        "v_cvt_pk_bf16_f32 v68, 0, v68\n"
        "v_mfma_f32_16x16x32_bf16 v[32:35], v[168:171], a[4:7], v[32:35]\n"
        "ds_read_b128 a[220:223], v254 offset:48128\n"
        "v_mul_f32 v68, v68, v228\n"
        "v_mfma_f32_16x16x32_bf16 v[32:35], v[176:179], a[8:11], v[32:35]\n"
        "v_cvt_pk_bf16_f32 v68, 0, v68\n"
        "v_and_b32 v145, s35, v130\n"
        "v_mfma_f32_16x16x32_bf16 v[32:35], v[184:187], a[12:15], v[32:35]\n"
        "v_mul_f32 v69, v69, v145\n"
        "v_cvt_pk_bf16_f32 v69, 0, v69\n"
        "v_mfma_f32_16x16x32_bf16 v[36:39], v[188:191], a[12:15], v[36:39]\n"
        "v_mul_f32 v69, v69, v228\n"
        "v_cvt_pk_bf16_f32 v69, 0, v69\n"
        "v_mfma_f32_16x16x32_bf16 v[36:39], v[180:183], a[8:11], v[36:39]\n"
        "v_lshlrev_b32 v146, 16, v131\n"
        "v_mul_f32 v70, v70, v146\n"
        "v_mfma_f32_16x16x32_bf16 v[36:39], v[172:175], a[4:7], v[36:39]\n"
        "s_waitcnt lgkmcnt(3)\n"
        "v_cvt_pk_bf16_f32 v70, 0, v70\n"
        "v_mul_f32 v70, v70, v228\n"
        "v_mfma_f32_16x16x32_bf16 v[36:39], v[164:167], a[0:3], v[36:39]\n"
        "v_cvt_pk_bf16_f32 v70, 0, v70\n"
        "v_and_b32 v147, s35, v131\n"
        "v_mfma_f32_16x16x32_bf16 v[44:47], v[164:167], a[16:19], v[44:47]\n"
        "v_mul_f32 v71, v71, v147\n"
        "v_cvt_pk_bf16_f32 v71, 0, v71\n"
        "v_mfma_f32_16x16x32_bf16 v[44:47], v[172:175], a[20:23], v[44:47]\n"
        "v_mul_f32 v71, v71, v228\n"
        "v_cvt_pk_bf16_f32 v71, 0, v71\n"
        "v_mfma_f32_16x16x32_bf16 v[40:43], v[184:187], a[28:31], v[40:43]\n"
        "v_lshlrev_b32 v246, 16, v132\n"
        "v_and_b32 v247, s35, v132\n"
        "v_mfma_f32_16x16x32_bf16 v[40:43], v[176:179], a[24:27], v[40:43]\n"
        "v_lshlrev_b32 v248, 16, v133\n"
        "v_and_b32 v249, s35, v133\n"
        "v_mfma_f32_16x16x32_bf16 v[48:51], v[160:163], a[192:195], v[48:51]\n"
        "v_mul_f32 v72, v72, v246\n"
        "v_mul_f32 v73, v73, v247\n"
        "v_mfma_f32_16x16x32_bf16 v[40:43], v[168:171], a[20:23], v[40:43]\n"
        "v_mul_f32 v74, v74, v248\n"
        "v_mul_f32 v75, v75, v249\n"
        "v_mfma_f32_16x16x32_bf16 v[48:51], v[168:171], a[196:199], v[48:51]\n"
        "v_cvt_pk_bf16_f32 v72, 0, v72\n"
        "v_cvt_pk_bf16_f32 v73, 0, v73\n"
        "v_mfma_f32_16x16x32_bf16 v[40:43], v[160:163], a[16:19], v[40:43]\n"
        "v_cvt_pk_bf16_f32 v74, 0, v74\n"
        "v_cvt_pk_bf16_f32 v75, 0, v75\n"
        "v_mfma_f32_16x16x32_bf16 v[48:51], v[176:179], a[200:203], v[48:51]\n"
        "s_barrier\n"
        "v_mfma_f32_16x16x32_bf16 v[52:55], v[188:191], a[204:207], v[52:55]\n"
        "ds_read_b128 a[0:3], v254 offset:49152\n"
        "v_mul_f32 v72, v72, v229\n"
        "v_mfma_f32_16x16x32_bf16 v[48:51], v[184:187], a[204:207], v[48:51]\n"
        "ds_read_b128 a[204:207], v254 offset:63488\n"
        "v_mul_f32 v73, v73, v229\n"
        "v_mfma_f32_16x16x32_bf16 v[52:55], v[180:183], a[200:203], v[52:55]\n"
        "ds_read_b128 a[200:203], v254 offset:59392\n"
        "v_mul_f32 v74, v74, v229\n"
        "v_mfma_f32_16x16x32_bf16 v[52:55], v[172:175], a[196:199], v[52:55]\n"
        "ds_read_b128 a[196:199], v254 offset:55296\n"
        "v_mul_f32 v75, v75, v229\n"
        "v_mfma_f32_16x16x32_bf16 v[52:55], v[164:167], a[192:195], v[52:55]\n"
        "ds_read_b128 a[192:195], v254 offset:51200\n"
        "v_cvt_pk_bf16_f32 v72, 0, v72\n"
        "v_mfma_f32_16x16x32_bf16 v[44:47], v[180:183], a[24:27], v[44:47]\n"
        "ds_read_b128 a[4:7], v254 offset:53248\n"
        "v_cvt_pk_bf16_f32 v73, 0, v73\n"
        "v_mfma_f32_16x16x32_bf16 v[44:47], v[188:191], a[28:31], v[44:47]\n"
        "ds_read_b128 a[8:11], v254 offset:57344\n"
        "s_waitcnt lgkmcnt(7)\n"
        "v_mfma_f32_16x16x32_bf16 v[60:63], v[164:167], a[208:211], v[60:63]\n"
        "ds_read_b128 a[12:15], v254 offset:61440\n"
        "v_cvt_pk_bf16_f32 v74, 0, v74\n"
        "v_mfma_f32_16x16x32_bf16 v[60:63], v[172:175], a[212:215], v[60:63]\n"
        "ds_read_b128 a[16:19], v254 offset:50176\n"
        "v_cvt_pk_bf16_f32 v75, 0, v75\n"
        "v_mfma_f32_16x16x32_bf16 v[60:63], v[180:183], a[216:219], v[60:63]\n"
        "ds_read_b128 a[20:23], v254 offset:54272\n"
        "s_waitcnt lgkmcnt(8)\n"
        "v_mfma_f32_16x16x32_bf16 v[52:55], v[220:223], a[204:207], v[52:55]\n"
        "ds_read_b128 a[24:27], v254 offset:58368\n"
        "s_waitcnt lgkmcnt(8)\n"
        "v_mfma_f32_16x16x32_bf16 v[52:55], v[212:215], a[200:203], v[52:55]\n"
        "ds_read_b128 a[28:31], v254 offset:62464\n"
        "s_waitcnt lgkmcnt(8)\n"
        "v_mfma_f32_16x16x32_bf16 v[52:55], v[204:207], a[196:199], v[52:55]\n"
        "s_waitcnt lgkmcnt(7)\n"
        "v_lshlrev_b32 v144, 16, v134\n"
        "v_and_b32 v145, s35, v134\n"
        "v_mfma_f32_16x16x32_bf16 v[52:55], v[196:199], a[192:195], v[52:55]\n"
        "global_load_dwordx2 v[156:157], v155, s[48:49] sc1\n"
        "v_lshlrev_b32 v146, 16, v135\n"
        "v_mfma_f32_16x16x32_bf16 v[60:63], v[188:191], a[220:223], v[60:63]\n"
        "v_and_b32 v147, s35, v135\n"
        "v_mul_f32 v76, v76, v144\n"
        "v_mfma_f32_16x16x32_bf16 v[56:59], v[184:187], a[220:223], v[56:59]\n"
        "ds_read_b128 a[220:223], v254 offset:64512\n"
        "v_mul_f32 v77, v77, v145\n"
        "v_mfma_f32_16x16x32_bf16 v[56:59], v[176:179], a[216:219], v[56:59]\n"
        "ds_read_b128 a[216:219], v254 offset:60416\n"
        "v_mul_f32 v78, v78, v146\n"
        "v_mfma_f32_16x16x32_bf16 v[56:59], v[168:171], a[212:215], v[56:59]\n"
        "ds_read_b128 a[212:215], v254 offset:56320\n"
        "v_mul_f32 v79, v79, v147\n"
        "v_mfma_f32_16x16x32_bf16 v[56:59], v[160:163], a[208:211], v[56:59]\n"
        "ds_read_b128 a[208:211], v254 offset:52224\n"
        "v_cvt_pk_bf16_f32 v76, 0, v76\n"
        "v_mfma_f32_16x16x32_bf16 v[32:35], v[192:195], a[0:3], v[32:35]\n"
        "s_waitcnt lgkmcnt(4)\n"
        "v_cvt_pk_bf16_f32 v77, 0, v77\n"
        "v_cvt_pk_bf16_f32 v78, 0, v78\n"
        "v_mfma_f32_16x16x32_bf16 v[32:35], v[200:203], a[4:7], v[32:35]\n"
        "v_cvt_pk_bf16_f32 v79, 0, v79\n"
        "v_mul_f32 v76, v76, v229\n"
        "v_mfma_f32_16x16x32_bf16 v[32:35], v[208:211], a[8:11], v[32:35]\n"
        "v_mul_f32 v77, v77, v229\n"
        "v_mul_f32 v78, v78, v229\n"
        "v_mfma_f32_16x16x32_bf16 v[32:35], v[216:219], a[12:15], v[32:35]\n"
        "v_mul_f32 v79, v79, v229\n"
        "v_cvt_pk_bf16_f32 v76, 0, v76\n"
        "v_mfma_f32_16x16x32_bf16 v[36:39], v[220:223], a[12:15], v[36:39]\n"
        "v_cvt_pk_bf16_f32 v77, 0, v77\n"
        "v_cvt_pk_bf16_f32 v78, 0, v78\n"
        "v_mfma_f32_16x16x32_bf16 v[36:39], v[212:215], a[8:11], v[36:39]\n"
        "v_cvt_pk_bf16_f32 v79, 0, v79\n"
        "v_lshlrev_b32 v246, 16, v136\n"
        "v_mfma_f32_16x16x32_bf16 v[36:39], v[204:207], a[4:7], v[36:39]\n"
        "v_and_b32 v247, s35, v136\n"
        "v_lshlrev_b32 v248, 16, v137\n"
        "v_mfma_f32_16x16x32_bf16 v[36:39], v[196:199], a[0:3], v[36:39]\n"
        "v_and_b32 v249, s35, v137\n"
        "v_mul_f32 v80, v80, v246\n"
        "v_mfma_f32_16x16x32_bf16 v[44:47], v[196:199], a[16:19], v[44:47]\n"
        "v_mul_f32 v81, v81, v247\n"
        "v_mul_f32 v82, v82, v248\n"
        "v_mfma_f32_16x16x32_bf16 v[44:47], v[204:207], a[20:23], v[44:47]\n"
        "v_mul_f32 v83, v83, v249\n"
        "v_cvt_pk_bf16_f32 v80, 0, v80\n"
        "v_mfma_f32_16x16x32_bf16 v[44:47], v[212:215], a[24:27], v[44:47]\n"
        "v_cvt_pk_bf16_f32 v81, 0, v81\n"
        "v_cvt_pk_bf16_f32 v82, 0, v82\n"
        "v_mfma_f32_16x16x32_bf16 v[44:47], v[220:223], a[28:31], v[44:47]\n"
        "v_cvt_pk_bf16_f32 v83, 0, v83\n"
        "v_mul_f32 v80, v80, v230\n"
        "v_mfma_f32_16x16x32_bf16 v[40:43], v[216:219], a[28:31], v[40:43]\n"
        "v_mul_f32 v81, v81, v230\n"
        "v_mul_f32 v82, v82, v230\n"
        "v_mfma_f32_16x16x32_bf16 v[40:43], v[208:211], a[24:27], v[40:43]\n"
        "v_mul_f32 v83, v83, v230\n"
        "v_cvt_pk_bf16_f32 v80, 0, v80\n"
        "v_mfma_f32_16x16x32_bf16 v[40:43], v[200:203], a[20:23], v[40:43]\n"
        "v_cvt_pk_bf16_f32 v81, 0, v81\n"
        "v_cvt_pk_bf16_f32 v82, 0, v82\n"
        "v_mfma_f32_16x16x32_bf16 v[40:43], v[192:195], a[16:19], v[40:43]\n"
        "v_cvt_pk_bf16_f32 v83, 0, v83\n"
        "v_lshlrev_b32 v144, 16, v138\n"
        "v_mfma_f32_16x16x32_bf16 v[48:51], v[192:195], a[192:195], v[48:51]\n"
        "s_barrier\n"
        "v_mfma_f32_16x16x32_bf16 v[48:51], v[200:203], a[196:199], v[48:51]\n"
        "ds_read_b128 a[0:3], v244 offset:32768\n"
        "v_and_b32 v145, s35, v138\n"
        "v_mfma_f32_16x16x32_bf16 v[48:51], v[208:211], a[200:203], v[48:51]\n"
        "ds_read_b128 a[4:7], v244 offset:36864\n"
        "v_lshlrev_b32 v146, 16, v139\n"
        "v_mfma_f32_16x16x32_bf16 v[48:51], v[216:219], a[204:207], v[48:51]\n"
        "ds_read_b128 a[8:11], v244 offset:40960\n"
        "s_waitcnt lgkmcnt(3)\n"
        "v_mfma_f32_16x16x32_bf16 v[60:63], v[196:199], a[208:211], v[60:63]\n"
        "ds_read_b128 a[12:15], v244 offset:45056\n"
        "v_and_b32 v147, s35, v139\n"
        "v_mfma_f32_16x16x32_bf16 v[60:63], v[204:207], a[212:215], v[60:63]\n"
        "ds_read_b128 a[16:19], v244 offset:33792\n"
        "v_mul_f32 v84, v84, v144\n"
        "v_mfma_f32_16x16x32_bf16 v[60:63], v[212:215], a[216:219], v[60:63]\n"
        "ds_read_b128 a[20:23], v244 offset:37888\n"
        "v_mul_f32 v85, v85, v145\n"
        "v_mfma_f32_16x16x32_bf16 v[60:63], v[220:223], a[220:223], v[60:63]\n"
        "ds_read_b128 a[24:27], v244 offset:41984\n"
        "v_mul_f32 v86, v86, v146\n"
        "v_mfma_f32_16x16x32_bf16 v[56:59], v[216:219], a[220:223], v[56:59]\n"
        "ds_read_b128 a[28:31], v244 offset:46080\n"
        "v_mul_f32 v87, v87, v147\n"
        "v_mfma_f32_16x16x32_bf16 v[56:59], v[208:211], a[216:219], v[56:59]\n"
        "ds_read_b128 a[192:195], v244 offset:34816\n"
        "v_cvt_pk_bf16_f32 v84, 0, v84\n"
        "v_mfma_f32_16x16x32_bf16 v[56:59], v[200:203], a[212:215], v[56:59]\n"
        "ds_read_b128 a[196:199], v244 offset:38912\n"
        "v_cvt_pk_bf16_f32 v85, 0, v85\n"
        "v_mfma_f32_16x16x32_bf16 v[56:59], v[192:195], a[208:211], v[56:59]\n"
        "ds_read_b128 a[200:203], v244 offset:43008\n"
        "s_waitcnt lgkmcnt(8)\n"
        "v_mfma_f32_16x16x32_bf16 v[32:35], v[112:115], a[0:3], v[32:35]\n"
        "ds_read_b128 a[204:207], v244 offset:47104\n"
        "v_cvt_pk_bf16_f32 v86, 0, v86\n"
        "v_mfma_f32_16x16x32_bf16 v[32:35], v[120:123], a[4:7], v[32:35]\n"
        "ds_read_b128 a[208:211], v244 offset:35840\n"
        "v_cvt_pk_bf16_f32 v87, 0, v87\n"
        "v_mfma_f32_16x16x32_bf16 v[32:35], a[64:67], a[8:11], v[32:35]\n"
        "ds_read_b128 a[212:215], v244 offset:39936\n"
        "s_waitcnt lgkmcnt(8)\n"
        "v_mfma_f32_16x16x32_bf16 v[32:35], a[72:75], a[12:15], v[32:35]\n"
        "ds_read_b128 a[216:219], v244 offset:44032\n"
        "v_mul_f32 v84, v84, v230\n"
        "v_mfma_f32_16x16x32_bf16 v[36:39], a[76:79], a[12:15], v[36:39]\n"
        "ds_read_b128 a[220:223], v244 offset:48128\n"
        "v_mul_f32 v85, v85, v230\n"
        "v_mfma_f32_16x16x32_bf16 v[36:39], a[68:71], a[8:11], v[36:39]\n"
        "s_waitcnt vmcnt(0) lgkmcnt(7)\n"
        "v_cmp_ne_u64 vcc, v[156:157], v[250:251]\n"
        "v_mul_f32 v86, v86, v230\n"
        "v_mfma_f32_16x16x32_bf16 v[36:39], v[124:127], a[4:7], v[36:39]\n"
        "s_cbranch_vccnz LPPR_%=\n"
        "LBPR_%=:\n"
        "global_load_dwordx4 a[240:243], v254, s[30:31] offset:0 sc1\n"
        "v_mfma_f32_16x16x32_bf16 v[36:39], v[116:119], a[0:3], v[36:39]\n"
        "v_mul_f32 v87, v87, v230\n"
        "v_cvt_pk_bf16_f32 v84, 0, v84\n"
        "v_mfma_f32_16x16x32_bf16 v[44:47], v[116:119], a[16:19], v[44:47]\n"
        "global_load_dwordx4 a[244:247], v254, s[30:31] offset:1024 sc1\n"
        "v_mfma_f32_16x16x32_bf16 v[44:47], v[124:127], a[20:23], v[44:47]\n"
        "v_cvt_pk_bf16_f32 v85, 0, v85\n"
        "v_cvt_pk_bf16_f32 v86, 0, v86\n"
        "v_mfma_f32_16x16x32_bf16 v[44:47], a[68:71], a[24:27], v[44:47]\n"
        "global_load_dwordx4 a[248:251], v254, s[30:31] offset:2048 sc1\n"
        "v_mfma_f32_16x16x32_bf16 v[44:47], a[76:79], a[28:31], v[44:47]\n"
        "LHB3_%=:\n"
        "s_cbranch_execz LHRB_%=\n"
        "v_cvt_pk_bf16_f32 v87, 0, v87\n"
        "v_lshlrev_b32 v88, 16, v108\n"
        "v_mfma_f32_16x16x32_bf16 v[40:43], a[72:75], a[28:31], v[40:43]\n"
        "global_load_dwordx4 a[252:255], v254, s[30:31] offset:3072 sc1\n"
        "v_mfma_f32_16x16x32_bf16 v[40:43], a[64:67], a[24:27], v[40:43]\n"
        "s_add_u32 s30, s38, s16\n"
        "s_addc_u32 s31, s39, 0\n"
        "s_mul_i32 s16, s13, 263168\n"
        "v_mfma_f32_16x16x32_bf16 v[40:43], v[120:123], a[20:23], v[40:43]\n"
        "global_load_dwordx4 v[0:3], v254, s[30:31] offset:0 sc1\n"
        "s_add_u32 s16, s16, s15\n"
        "v_mfma_f32_16x16x32_bf16 v[40:43], v[112:115], a[16:19], v[40:43]\n"
        "s_add_u32 s16, s16, s73\n"
        "s_add_u32 s16, s16, 0x4000000\n"
        "v_mfma_f32_16x16x32_bf16 v[48:51], v[112:115], a[192:195], v[48:51]\n"
        "s_waitcnt lgkmcnt(0)\n"
        "global_load_dwordx4 v[4:7], v254, s[30:31] offset:1024 sc1\n"
        "v_mfma_f32_16x16x32_bf16 v[48:51], v[120:123], a[196:199], v[48:51]\n"
        "s_barrier\n"
        "v_mfma_f32_16x16x32_bf16 v[48:51], a[64:67], a[200:203], v[48:51]\n"
        "ds_read_b128 a[0:3], v244 offset:0\n"
        "global_load_dwordx4 v[8:11], v254, s[30:31] offset:2048 sc1\n"
        "v_mfma_f32_16x16x32_bf16 v[48:51], a[72:75], a[204:207], v[48:51]\n"
        "ds_read_b128 a[4:7], v244 offset:4096\n"
        "v_and_b32 v89, s35, v108\n"
        "v_mfma_f32_16x16x32_bf16 v[52:55], a[76:79], a[204:207], v[52:55]\n"
        "ds_read_b128 a[8:11], v244 offset:8192\n"
        "global_load_dwordx4 v[12:15], v254, s[30:31] offset:3072 sc1\n"
        "v_mfma_f32_16x16x32_bf16 v[52:55], a[68:71], a[200:203], v[52:55]\n"
        "ds_read_b128 a[12:15], v244 offset:12288\n"
        "s_add_u32 s30, s38, s16\n"
        "s_addc_u32 s31, s39, 0\n"
        "v_mfma_f32_16x16x32_bf16 v[52:55], v[124:127], a[196:199], v[52:55]\n"
        "ds_read_b128 a[16:19], v244 offset:1024\n"
        "global_load_dwordx4 v[16:19], v254, s[30:31] offset:0 sc1\n"
        "v_mfma_f32_16x16x32_bf16 v[52:55], v[116:119], a[192:195], v[52:55]\n"
        "ds_read_b128 a[20:23], v244 offset:5120\n"
        "v_lshlrev_b32 v90, 16, v109\n"
        "v_mfma_f32_16x16x32_bf16 v[60:63], v[116:119], a[208:211], v[60:63]\n"
        "ds_read_b128 a[24:27], v244 offset:9216\n"
        "global_load_dwordx4 v[20:23], v254, s[30:31] offset:1024 sc1\n"
        "v_mfma_f32_16x16x32_bf16 v[60:63], v[124:127], a[212:215], v[60:63]\n"
        "ds_read_b128 a[28:31], v244 offset:13312\n"
        "v_and_b32 v91, s35, v109\n"
        "v_mfma_f32_16x16x32_bf16 v[60:63], a[68:71], a[216:219], v[60:63]\n"
        "ds_read_b128 a[192:195], v244 offset:2048\n"
        "global_load_dwordx4 v[24:27], v254, s[30:31] offset:2048 sc1\n"
        "v_mfma_f32_16x16x32_bf16 v[60:63], a[76:79], a[220:223], v[60:63]\n"
        "ds_read_b128 a[196:199], v244 offset:6144\n"
        "v_lshlrev_b32 v246, 16, v140\n"
        "v_mfma_f32_16x16x32_bf16 v[56:59], a[72:75], a[220:223], v[56:59]\n"
        "ds_read_b128 a[200:203], v244 offset:10240\n"
        "global_load_dwordx4 v[28:31], v254, s[30:31] offset:3072 sc1\n"
        "v_mfma_f32_16x16x32_bf16 v[56:59], a[64:67], a[216:219], v[56:59]\n"
        "ds_read_b128 a[204:207], v244 offset:14336\n"
        "v_and_b32 v247, s35, v140\n"
        "v_mfma_f32_16x16x32_bf16 v[56:59], v[120:123], a[212:215], v[56:59]\n"
        "ds_read_b128 a[212:215], v244 offset:7168\n"
        "v_lshlrev_b32 v248, 16, v141\n"
        "v_mfma_f32_16x16x32_bf16 v[56:59], v[112:115], a[208:211], v[56:59]\n"
        "ds_read_b128 a[208:211], v244 offset:3072\n"
        "s_waitcnt lgkmcnt(8)\n"
        "v_mfma_f32_16x16x32_bf16 v[32:35], a[80:83], a[0:3], v[32:35]\n"
        "ds_read_b128 a[216:219], v244 offset:11264\n"
        "v_and_b32 v249, s35, v141\n"
        "v_mfma_f32_16x16x32_bf16 v[32:35], a[88:91], a[4:7], v[32:35]\n"
        "ds_read_b128 a[220:223], v244 offset:15360\n"
        "v_mul_f32 v88, v88, v246\n"
        "v_mfma_f32_16x16x32_bf16 v[32:35], a[160:163], a[8:11], v[32:35]\n"
        "v_mul_f32 v89, v89, v247\n"
        "v_mul_f32 v90, v90, v248\n"
        "v_mfma_f32_16x16x32_bf16 v[32:35], a[168:171], a[12:15], v[32:35]\n"
        "v_mul_f32 v91, v91, v249\n"
        "v_cvt_pk_bf16_f32 v88, 0, v88\n"
        "v_mfma_f32_16x16x32_bf16 v[36:39], a[172:175], a[12:15], v[36:39]\n"
        "v_cvt_pk_bf16_f32 v89, 0, v89\n"
        "v_cvt_pk_bf16_f32 v90, 0, v90\n"
        "v_mfma_f32_16x16x32_bf16 v[36:39], a[164:167], a[8:11], v[36:39]\n"
        "v_cvt_pk_bf16_f32 v91, 0, v91\n"
        "v_mul_f32 v88, v88, v231\n"
        "v_mfma_f32_16x16x32_bf16 v[36:39], a[92:95], a[4:7], v[36:39]\n"
        "s_waitcnt lgkmcnt(3)\n"
        "v_mul_f32 v89, v89, v231\n"
        "v_mul_f32 v90, v90, v231\n"
        "v_mfma_f32_16x16x32_bf16 v[36:39], a[84:87], a[0:3], v[36:39]\n"
        "v_mul_f32 v91, v91, v231\n"
        "v_cvt_pk_bf16_f32 v88, 0, v88\n"
        "v_mfma_f32_16x16x32_bf16 v[44:47], a[84:87], a[16:19], v[44:47]\n"
        "v_cvt_pk_bf16_f32 v89, 0, v89\n"
        "v_cvt_pk_bf16_f32 v90, 0, v90\n"
        "v_mfma_f32_16x16x32_bf16 v[44:47], a[92:95], a[20:23], v[44:47]\n"
        "v_cvt_pk_bf16_f32 v91, 0, v91\n"
        "v_lshlrev_b32 v92, 16, v110\n"
        "v_mfma_f32_16x16x32_bf16 v[44:47], a[164:167], a[24:27], v[44:47]\n"
        "v_and_b32 v93, s35, v110\n"
        "v_lshlrev_b32 v94, 16, v111\n"
        "v_mfma_f32_16x16x32_bf16 v[44:47], a[172:175], a[28:31], v[44:47]\n"
        "v_and_b32 v95, s35, v111\n"
        "v_lshlrev_b32 v144, 16, v142\n"
        "v_mfma_f32_16x16x32_bf16 v[40:43], a[168:171], a[28:31], v[40:43]\n"
        "v_and_b32 v145, s35, v142\n"
        "v_lshlrev_b32 v146, 16, v143\n"
        "v_mfma_f32_16x16x32_bf16 v[40:43], a[160:163], a[24:27], v[40:43]\n"
        "v_and_b32 v147, s35, v143\n"
        "v_mul_f32 v92, v92, v144\n"
        "v_mfma_f32_16x16x32_bf16 v[40:43], a[88:91], a[20:23], v[40:43]\n"
        "v_mul_f32 v93, v93, v145\n"
        "v_mul_f32 v94, v94, v146\n"
        "v_mfma_f32_16x16x32_bf16 v[40:43], a[80:83], a[16:19], v[40:43]\n"
        "v_mul_f32 v95, v95, v147\n"
        "v_cvt_pk_bf16_f32 v92, 0, v92\n"
        "v_mfma_f32_16x16x32_bf16 v[48:51], a[80:83], a[192:195], v[48:51]\n"
        "s_barrier\n"
        "v_mfma_f32_16x16x32_bf16 v[48:51], a[88:91], a[196:199], v[48:51]\n"
        "ds_read_b128 a[0:3], v244 offset:49152\n"
        "v_cvt_pk_bf16_f32 v93, 0, v93\n"
        "v_mfma_f32_16x16x32_bf16 v[48:51], a[160:163], a[200:203], v[48:51]\n"
        "ds_read_b128 a[4:7], v244 offset:53248\n"
        "v_cvt_pk_bf16_f32 v94, 0, v94\n"
        "v_mfma_f32_16x16x32_bf16 v[48:51], a[168:171], a[204:207], v[48:51]\n"
        "ds_read_b128 a[8:11], v244 offset:57344\n"
        "v_cvt_pk_bf16_f32 v95, 0, v95\n"
        "v_mfma_f32_16x16x32_bf16 v[52:55], a[172:175], a[204:207], v[52:55]\n"
        "ds_read_b128 a[12:15], v244 offset:61440\n"
        "v_mul_f32 v92, v92, v231\n"
        "v_mfma_f32_16x16x32_bf16 v[52:55], a[164:167], a[200:203], v[52:55]\n"
        "ds_read_b128 a[200:203], v244 offset:58368\n"
        "v_mul_f32 v93, v93, v231\n"
        "v_mfma_f32_16x16x32_bf16 v[52:55], a[92:95], a[196:199], v[52:55]\n"
        "ds_read_b128 a[196:199], v244 offset:54272\n"
        "v_mul_f32 v94, v94, v231\n"
        "v_mfma_f32_16x16x32_bf16 v[52:55], a[84:87], a[192:195], v[52:55]\n"
        "ds_read_b128 a[192:195], v244 offset:50176\n"
        "s_waitcnt lgkmcnt(7)\n"
        "v_mfma_f32_16x16x32_bf16 v[60:63], a[84:87], a[208:211], v[60:63]\n"
        "ds_read_b128 a[204:207], v244 offset:62464\n"
        "v_mfma_f32_16x16x32_bf16 v[60:63], a[92:95], a[212:215], v[60:63]\n"
        "s_barrier\n"
        "v_mfma_f32_16x16x32_bf16 v[60:63], a[164:167], a[216:219], v[60:63]\n"
        "ds_read_b128 a[16:19], v245 offset:0\n"
        "v_mul_f32 v95, v95, v231\n"
        "v_mfma_f32_16x16x32_bf16 v[60:63], a[172:175], a[220:223], v[60:63]\n"
        "ds_read_b128 a[20:23], v245 offset:4096\n"
        "v_cvt_pk_bf16_f32 v92, 0, v92\n"
        "v_mfma_f32_16x16x32_bf16 v[56:59], a[168:171], a[220:223], v[56:59]\n"
        "ds_read_b128 a[24:27], v245 offset:8192\n"
        "v_cvt_pk_bf16_f32 v93, 0, v93\n"
        "v_mfma_f32_16x16x32_bf16 v[56:59], a[160:163], a[216:219], v[56:59]\n"
        "ds_read_b128 a[28:31], v245 offset:12288\n"
        "v_cvt_pk_bf16_f32 v94, 0, v94\n"
        "v_mfma_f32_16x16x32_bf16 v[56:59], a[88:91], a[212:215], v[56:59]\n"
        "ds_read_b128 a[212:215], v245 offset:5120\n"
        "v_cvt_pk_bf16_f32 v95, 0, v95\n"
        "v_mfma_f32_16x16x32_bf16 v[56:59], a[80:83], a[208:211], v[56:59]\n"
        "ds_read_b128 a[208:211], v245 offset:1024\n"
        "s_waitcnt lgkmcnt(7)\n"
        "v_mfma_f32_16x16x32_bf16 v[32:35], a[176:179], a[0:3], v[32:35]\n"
        "ds_read_b128 a[216:219], v245 offset:9216\n"
        "v_mov_b32 v144, 0\n"
        "v_mfma_f32_16x16x32_bf16 v[32:35], a[184:187], a[4:7], v[32:35]\n"
        "ds_read_b128 a[220:223], v245 offset:13312\n"
        "v_mfma_f32_16x16x32_bf16 v[32:35], a[32:35], a[8:11], v[32:35]\n"
        "v_mfma_f32_16x16x32_bf16 v[32:35], a[40:43], a[12:15], v[32:35]\n"
        "v_mfma_f32_16x16x32_bf16 v[36:39], a[44:47], a[12:15], v[36:39]\n"
        "ds_read_b128 a[12:15], v244 offset:63488\n"
        "s_waitcnt lgkmcnt(6)\n"
        "v_mfma_f32_16x16x32_bf16 v[36:39], a[36:39], a[8:11], v[36:39]\n"
        "ds_read_b128 a[8:11], v244 offset:59392\n"
        "v_mfma_f32_16x16x32_bf16 v[36:39], a[188:191], a[4:7], v[36:39]\n"
        "ds_read_b128 a[4:7], v244 offset:55296\n"
        "v_mfma_f32_16x16x32_bf16 v[36:39], a[180:183], a[0:3], v[36:39]\n"
        "ds_read_b128 a[0:3], v244 offset:51200\n"
        "s_waitcnt lgkmcnt(6)\n"
        "v_mfma_f32_16x16x32_bf16 v[32:35], a[48:51], a[16:19], v[32:35]\n"
        "v_mfma_f32_16x16x32_bf16 v[32:35], a[56:59], a[20:23], v[32:35]\n"
        "v_mfma_f32_16x16x32_bf16 v[32:35], a[224:227], a[24:27], v[32:35]\n"
        "v_mfma_f32_16x16x32_bf16 v[32:35], a[232:235], a[28:31], v[32:35]\n"
        "v_mfma_f32_16x16x32_bf16 v[36:39], a[236:239], a[28:31], v[36:39]\n"
        "ds_read_b128 a[28:31], v245 offset:14336\n"
        "v_mfma_f32_16x16x32_bf16 v[36:39], a[228:231], a[24:27], v[36:39]\n"
        "ds_read_b128 a[24:27], v245 offset:10240\n"
        "v_mfma_f32_16x16x32_bf16 v[36:39], a[60:63], a[20:23], v[36:39]\n"
        "ds_read_b128 a[20:23], v245 offset:6144\n"
        "v_mfma_f32_16x16x32_bf16 v[36:39], a[52:55], a[16:19], v[36:39]\n"
        "ds_read_b128 a[16:19], v245 offset:2048\n"
        "s_waitcnt vmcnt(11) lgkmcnt(4)\n"
        "v_mfma_f32_16x16x32_bf16 v[32:35], v[240:243], a[240:243], v[32:35]\n"
        "v_mfma_f32_16x16x32_bf16 v[36:39], v[238:241], a[240:243], v[36:39]\n"
        "s_waitcnt vmcnt(7)\n"
        "v_mfma_f32_16x16x32_bf16 v[32:35], v[240:243], v[0:3], v[32:35]\n"
        "v_mfma_f32_16x16x32_bf16 v[36:39], v[238:241], v[0:3], v[36:39]\n"
        "v_mfma_f32_16x16x32_bf16 v[40:43], a[176:179], a[192:195], v[40:43]\n"
        "s_waitcnt vmcnt(3) lgkmcnt(3)\n"
        "v_mfma_f32_16x16x32_bf16 v[32:35], v[240:243], v[16:19], v[32:35]\n"
        "v_mfma_f32_16x16x32_bf16 v[36:39], v[238:241], v[16:19], v[36:39]\n"
        "v_mfma_f32_16x16x32_bf16 v[40:43], a[184:187], a[196:199], v[40:43]\n"
        "v_mfma_f32_16x16x32_bf16 v[40:43], a[32:35], a[200:203], v[40:43]\n"
        "v_mfma_f32_16x16x32_bf16 v[40:43], a[40:43], a[204:207], v[40:43]\n"
        "v_mfma_f32_16x16x32_bf16 v[44:47], a[44:47], a[204:207], v[44:47]\n"
        "ds_read_b128 a[204:207], v244 offset:64512\n"
        "v_mfma_f32_16x16x32_bf16 v[44:47], a[36:39], a[200:203], v[44:47]\n"
        "ds_read_b128 a[200:203], v244 offset:60416\n"
        "v_fmac_f32 v64, v32, v224\n"
        "v_mfma_f32_16x16x32_bf16 v[44:47], a[188:191], a[196:199], v[44:47]\n"
        "ds_read_b128 a[196:199], v244 offset:56320\n"
        "v_fmac_f32 v65, v33, v224\n"
        "v_mfma_f32_16x16x32_bf16 v[44:47], a[180:183], a[192:195], v[44:47]\n"
        "ds_read_b128 a[192:195], v244 offset:52224\n"
        "v_fmac_f32 v66, v34, v224\n"
        "v_mfma_f32_16x16x32_bf16 v[40:43], a[48:51], a[208:211], v[40:43]\n"
        "v_fmac_f32 v67, v35, v224\n"
        "v_fmac_f32 v68, v36, v224\n"
        "v_mfma_f32_16x16x32_bf16 v[40:43], a[56:59], a[212:215], v[40:43]\n"
        "v_fmac_f32 v69, v37, v224\n"
        "v_fmac_f32 v70, v38, v224\n"
        "v_mfma_f32_16x16x32_bf16 v[40:43], a[224:227], a[216:219], v[40:43]\n"
        "v_fmac_f32 v71, v39, v224\n"
        "v_cvt_pk_bf16_f32 v64, v64, v65\n"
        "v_mfma_f32_16x16x32_bf16 v[40:43], a[232:235], a[220:223], v[40:43]\n"
        "v_cvt_pk_bf16_f32 v65, v66, v67\n"
        "v_cvt_pk_bf16_f32 v66, v68, v69\n"
        "v_mfma_f32_16x16x32_bf16 v[44:47], a[236:239], a[220:223], v[44:47]\n"
        "ds_read_b128 a[220:223], v245 offset:15360\n"
        "v_cvt_pk_bf16_f32 v67, v70, v71\n"
        "v_mfma_f32_16x16x32_bf16 v[44:47], a[228:231], a[216:219], v[44:47]\n"
        "v_permlane16_swap_b32 v64, v66\n"
        "v_mfma_f32_16x16x32_bf16 v[44:47], a[60:63], a[212:215], v[44:47]\n"
        "v_permlane16_swap_b32 v65, v67\n"
        "v_mfma_f32_16x16x32_bf16 v[44:47], a[52:55], a[208:211], v[44:47]\n"
        "global_store_dwordx4 v151, v[64:67], s[56:57] sc0 sc1\n"
        "ds_read_b128 a[208:211], v245 offset:3072\n"
        "s_add_u32 s56, s56, 0x8000\n"
        "s_addc_u32 s57, s57, 0\n"
        "ds_read_b128 a[212:215], v245 offset:7168\n"
        "v_mfma_f32_16x16x32_bf16 v[40:43], v[240:243], a[244:247], v[40:43]\n"
        "ds_read_b128 a[216:219], v245 offset:11264\n"
        "v_mfma_f32_16x16x32_bf16 v[44:47], v[238:241], a[244:247], v[44:47]\n"
        "v_mfma_f32_16x16x32_bf16 v[40:43], v[240:243], v[4:7], v[40:43]\n"
        "v_mfma_f32_16x16x32_bf16 v[44:47], v[238:241], v[4:7], v[44:47]\n"
        "s_waitcnt vmcnt(3) lgkmcnt(4)\n"
        "v_mfma_f32_16x16x32_bf16 v[40:43], v[240:243], v[20:23], v[40:43]\n"
        "v_mfma_f32_16x16x32_bf16 v[44:47], v[238:241], v[20:23], v[44:47]\n"
        "v_mfma_f32_16x16x32_bf16 v[48:51], a[176:179], a[0:3], v[48:51]\n"
        "v_mfma_f32_16x16x32_bf16 v[48:51], a[184:187], a[4:7], v[48:51]\n"
        "v_mfma_f32_16x16x32_bf16 v[48:51], a[32:35], a[8:11], v[48:51]\n"
        "v_mfma_f32_16x16x32_bf16 v[48:51], a[40:43], a[12:15], v[48:51]\n"
        "v_mfma_f32_16x16x32_bf16 v[52:55], a[44:47], a[12:15], v[52:55]\n"
        "v_mfma_f32_16x16x32_bf16 v[52:55], a[36:39], a[8:11], v[52:55]\n"
        "v_mfma_f32_16x16x32_bf16 v[52:55], a[188:191], a[4:7], v[52:55]\n"
        "v_fmac_f32 v72, v40, v225\n"
        "v_fmac_f32 v73, v41, v225\n"
        "v_mfma_f32_16x16x32_bf16 v[52:55], a[180:183], a[0:3], v[52:55]\n"
        "v_fmac_f32 v74, v42, v225\n"
        "v_fmac_f32 v75, v43, v225\n"
        "v_mfma_f32_16x16x32_bf16 v[48:51], a[48:51], a[16:19], v[48:51]\n"
        "v_fmac_f32 v76, v44, v225\n"
        "v_fmac_f32 v77, v45, v225\n"
        "v_mfma_f32_16x16x32_bf16 v[48:51], a[56:59], a[20:23], v[48:51]\n"
        "v_fmac_f32 v78, v46, v225\n"
        "v_fmac_f32 v79, v47, v225\n"
        "v_mfma_f32_16x16x32_bf16 v[48:51], a[224:227], a[24:27], v[48:51]\n"
        "v_cvt_pk_bf16_f32 v72, v72, v73\n"
        "v_cvt_pk_bf16_f32 v73, v74, v75\n"
        "v_mfma_f32_16x16x32_bf16 v[48:51], a[232:235], a[28:31], v[48:51]\n"
        "v_cvt_pk_bf16_f32 v74, v76, v77\n"
        "v_cvt_pk_bf16_f32 v75, v78, v79\n"
        "v_mfma_f32_16x16x32_bf16 v[52:55], a[236:239], a[28:31], v[52:55]\n"
        "v_permlane16_swap_b32 v72, v74\n"
        "v_mfma_f32_16x16x32_bf16 v[52:55], a[228:231], a[24:27], v[52:55]\n"
        "v_permlane16_swap_b32 v73, v75\n"
        "v_mfma_f32_16x16x32_bf16 v[52:55], a[60:63], a[20:23], v[52:55]\n"
        "global_store_dwordx4 v151, v[72:75], s[56:57] sc0 sc1\n"
        "s_add_u32 s56, s56, 0x8000\n"
        "s_addc_u32 s57, s57, 0\n"
        "v_mfma_f32_16x16x32_bf16 v[52:55], a[52:55], a[16:19], v[52:55]\n"
        "v_mfma_f32_16x16x32_bf16 v[48:51], v[240:243], a[248:251], v[48:51]\n"
        "v_mfma_f32_16x16x32_bf16 v[52:55], v[238:241], a[248:251], v[52:55]\n"
        "v_mfma_f32_16x16x32_bf16 v[48:51], v[240:243], v[8:11], v[48:51]\n"
        "v_mfma_f32_16x16x32_bf16 v[52:55], v[238:241], v[8:11], v[52:55]\n"
        "s_waitcnt vmcnt(3) lgkmcnt(0)\n"
        "v_mfma_f32_16x16x32_bf16 v[48:51], v[240:243], v[24:27], v[48:51]\n"
        "v_mfma_f32_16x16x32_bf16 v[52:55], v[238:241], v[24:27], v[52:55]\n"
        "v_mfma_f32_16x16x32_bf16 v[56:59], a[176:179], a[192:195], v[56:59]\n"
        "v_mfma_f32_16x16x32_bf16 v[56:59], a[184:187], a[196:199], v[56:59]\n"
        "v_mfma_f32_16x16x32_bf16 v[56:59], a[32:35], a[200:203], v[56:59]\n"
        "v_mfma_f32_16x16x32_bf16 v[56:59], a[40:43], a[204:207], v[56:59]\n"
        "v_mfma_f32_16x16x32_bf16 v[60:63], a[44:47], a[204:207], v[60:63]\n"
        "v_mfma_f32_16x16x32_bf16 v[60:63], a[36:39], a[200:203], v[60:63]\n"
        "v_mfma_f32_16x16x32_bf16 v[60:63], a[188:191], a[196:199], v[60:63]\n"
        "v_fmac_f32 v80, v48, v226\n"
        "v_fmac_f32 v81, v49, v226\n"
        "v_mfma_f32_16x16x32_bf16 v[60:63], a[180:183], a[192:195], v[60:63]\n"
        "v_fmac_f32 v82, v50, v226\n"
        "v_fmac_f32 v83, v51, v226\n"
        "v_mfma_f32_16x16x32_bf16 v[56:59], a[48:51], a[208:211], v[56:59]\n"
        "v_fmac_f32 v84, v52, v226\n"
        "v_fmac_f32 v85, v53, v226\n"
        "v_mfma_f32_16x16x32_bf16 v[56:59], a[56:59], a[212:215], v[56:59]\n"
        "v_fmac_f32 v86, v54, v226\n"
        "v_fmac_f32 v87, v55, v226\n"
        "v_mfma_f32_16x16x32_bf16 v[56:59], a[224:227], a[216:219], v[56:59]\n"
        "v_cvt_pk_bf16_f32 v80, v80, v81\n"
        "v_cvt_pk_bf16_f32 v81, v82, v83\n"
        "v_mfma_f32_16x16x32_bf16 v[56:59], a[232:235], a[220:223], v[56:59]\n"
        "v_cvt_pk_bf16_f32 v82, v84, v85\n"
        "v_cvt_pk_bf16_f32 v83, v86, v87\n"
        "v_mfma_f32_16x16x32_bf16 v[60:63], a[236:239], a[220:223], v[60:63]\n"
        "v_permlane16_swap_b32 v80, v82\n"
        "v_mfma_f32_16x16x32_bf16 v[60:63], a[228:231], a[216:219], v[60:63]\n"
        "v_permlane16_swap_b32 v81, v83\n"
        "v_mfma_f32_16x16x32_bf16 v[60:63], a[60:63], a[212:215], v[60:63]\n"
        "global_store_dwordx4 v151, v[80:83], s[56:57] sc0 sc1\n"
        "s_add_u32 s56, s56, 0x8000\n"
        "s_addc_u32 s57, s57, 0\n"
        "v_mfma_f32_16x16x32_bf16 v[60:63], a[52:55], a[208:211], v[60:63]\n"
        "v_mfma_f32_16x16x32_bf16 v[56:59], v[240:243], a[252:255], v[56:59]\n"
        "v_mfma_f32_16x16x32_bf16 v[60:63], v[238:241], a[252:255], v[60:63]\n"
        "v_mfma_f32_16x16x32_bf16 v[56:59], v[240:243], v[12:15], v[56:59]\n"
        "v_mfma_f32_16x16x32_bf16 v[60:63], v[238:241], v[12:15], v[60:63]\n"
        "s_waitcnt vmcnt(3)\n"
        "v_mfma_f32_16x16x32_bf16 v[56:59], v[240:243], v[28:31], v[56:59]\n"
        "v_mfma_f32_16x16x32_bf16 v[60:63], v[238:241], v[28:31], v[60:63]\n"
        "s_nop 6\n"
        "v_fmac_f32 v88, v56, v227\n"
        "v_fmac_f32 v89, v57, v227\n"
        "v_fmac_f32 v90, v58, v227\n"
        "v_fmac_f32 v91, v59, v227\n"
        "v_fmac_f32 v92, v60, v227\n"
        "v_fmac_f32 v93, v61, v227\n"
        "v_fmac_f32 v94, v62, v227\n"
        "v_fmac_f32 v95, v63, v227\n"
        "v_cvt_pk_bf16_f32 v88, v88, v89\n"
        "v_cvt_pk_bf16_f32 v89, v90, v91\n"
        "v_cvt_pk_bf16_f32 v90, v92, v93\n"
        "v_cvt_pk_bf16_f32 v91, v94, v95\n"
        "s_nop 0\n"
        "v_permlane16_swap_b32 v88, v90\n"
        "v_permlane16_swap_b32 v89, v91\n"
        "global_store_dwordx4 v151, v[88:91], s[56:57] sc0 sc1\n"
        "s_or_b32 s97, s55, s40\n"
        "s_cbranch_scc1 LEB_%=\n"
        "s_mov_b64 exec, 1\n"
        "global_store_dwordx2 v144, v[250:251], s[48:49] sc1\n"
        "s_mov_b64 exec, -1\n"
        "LEB_%=:\n"
        "; ---- END\n"
        "s_endpgm\n"
        "LPKV_%=:\n"
        "s_mov_b32 s97, 0x200000\n"
        "LQKV_%=:\n"
        "global_load_dwordx2 v[252:253], v245, s[48:49] sc1\n"
        "s_waitcnt vmcnt(0)\n"
        "v_cmp_ne_u64 vcc, v[252:253], v[250:251]\n"
        "s_nop 1\n"
        "s_cbranch_vccz LBKV_%=\n"
        "s_sleep 2\n"
        "s_sub_u32 s97, s97, 1\n"
        "s_cmp_lg_u32 s97, 0\n"
        "s_cbranch_scc1 LQKV_%=\n"
        "s_branch LBKV_%=\n"
        "LE4D_%=:\n"
        "s_mov_b32 s97, 0x200000\n"
        "LE5D_%=:\n"
        "s_sleep 2\n"
        "global_load_dwordx2 v[252:253], v155, s[48:49] sc1\n"
        "s_waitcnt vmcnt(0)\n"
        "v_cmp_ne_u64 vcc, v[252:253], v[250:251]\n"
        "s_nop 1\n"
        "s_cbranch_vccz LEWD_%=\n"
        "s_sub_u32 s97, s97, 1\n"
        "s_cmp_lg_u32 s97, 0\n"
        "s_cbranch_scc1 LE5D_%=\n"
        "s_branch LEWD_%=\n"
        "LRL_%=:\n"
        "s_waitcnt vmcnt(0) lgkmcnt(0)\n"
        "s_nop 7\n"
        "v_accvgpr_write_b32 a176, v96\n"
        "v_accvgpr_write_b32 a177, v97\n"
        "v_accvgpr_write_b32 a178, v98\n"
        "v_accvgpr_write_b32 a179, v99\n"
        "v_accvgpr_write_b32 a180, v100\n"
        "v_accvgpr_write_b32 a181, v101\n"
        "v_accvgpr_write_b32 a182, v102\n"
        "v_accvgpr_write_b32 a183, v103\n"
        "v_accvgpr_write_b32 a184, v104\n"
        "v_accvgpr_write_b32 a185, v105\n"
        "v_accvgpr_write_b32 a186, v106\n"
        "v_accvgpr_write_b32 a187, v107\n"
        "v_accvgpr_write_b32 a188, v108\n"
        "v_accvgpr_write_b32 a189, v109\n"
        "v_accvgpr_write_b32 a190, v110\n"
        "v_accvgpr_write_b32 a191, v111\n"
        "s_mov_b32 s53, s44\n"
        "s_mov_b32 s96, 64\n"
        "s_mov_b32 s52, 0\n"
        "s_branch LREDO_%=\n"
        "LRET0_%=:\n"
        "v_accvgpr_write_b32 a176, v128\n"
        "v_accvgpr_write_b32 a177, v129\n"
        "v_accvgpr_write_b32 a178, v130\n"
        "v_accvgpr_write_b32 a179, v131\n"
        "v_accvgpr_write_b32 a180, v132\n"
        "v_accvgpr_write_b32 a181, v133\n"
        "v_accvgpr_write_b32 a182, v134\n"
        "v_accvgpr_write_b32 a183, v135\n"
        "v_accvgpr_write_b32 a184, v136\n"
        "v_accvgpr_write_b32 a185, v137\n"
        "v_accvgpr_write_b32 a186, v138\n"
        "v_accvgpr_write_b32 a187, v139\n"
        "v_accvgpr_write_b32 a188, v140\n"
        "v_accvgpr_write_b32 a189, v141\n"
        "v_accvgpr_write_b32 a190, v142\n"
        "v_accvgpr_write_b32 a191, v143\n"
        "s_mov_b32 s53, s44\n"
        "s_mov_b32 s96, 0\n"
        "s_mov_b32 s52, 1\n"
        "s_branch LREDO_%=\n"
        "LRET1_%=:\n"
        "s_branch LSL_%=\n"
        "LE4S_%=:\n"
        "s_mov_b32 s97, 0x200000\n"
        "LE5S_%=:\n"
        "s_sleep 2\n"
        "global_load_dwordx2 v[252:253], v155, s[48:49] sc1\n"
        "s_waitcnt vmcnt(0)\n"
        "v_cmp_ne_u64 vcc, v[252:253], v[250:251]\n"
        "s_nop 1\n"
        "s_cbranch_vccz LEWS_%=\n"
        "s_sub_u32 s97, s97, 1\n"
        "s_cmp_lg_u32 s97, 0\n"
        "s_cbranch_scc1 LE5S_%=\n"
        "s_branch LEWS_%=\n"
        "LRH_%=:\n"
        "s_waitcnt vmcnt(0) lgkmcnt(0)\n"
        "s_nop 7\n"
        "v_accvgpr_write_b32 a176, v0\n"
        "v_accvgpr_write_b32 a177, v1\n"
        "v_accvgpr_write_b32 a178, v2\n"
        "v_accvgpr_write_b32 a179, v3\n"
        "v_accvgpr_write_b32 a180, v4\n"
        "v_accvgpr_write_b32 a181, v5\n"
        "v_accvgpr_write_b32 a182, v6\n"
        "v_accvgpr_write_b32 a183, v7\n"
        "v_accvgpr_write_b32 a184, v8\n"
        "v_accvgpr_write_b32 a185, v9\n"
        "v_accvgpr_write_b32 a186, v10\n"
        "v_accvgpr_write_b32 a187, v11\n"
        "v_accvgpr_write_b32 a188, v12\n"
        "v_accvgpr_write_b32 a189, v13\n"
        "v_accvgpr_write_b32 a190, v14\n"
        "v_accvgpr_write_b32 a191, v15\n"
        "s_mov_b32 s53, s45\n"
        "s_mov_b32 s96, 64\n"
        "s_mov_b32 s52, 2\n"
        "s_branch LREDO_%=\n"
        "LRET2_%=:\n"
        "v_accvgpr_write_b32 a176, v64\n"
        "v_accvgpr_write_b32 a177, v65\n"
        "v_accvgpr_write_b32 a178, v66\n"
        "v_accvgpr_write_b32 a179, v67\n"
        "v_accvgpr_write_b32 a180, v68\n"
        "v_accvgpr_write_b32 a181, v69\n"
        "v_accvgpr_write_b32 a182, v70\n"
        "v_accvgpr_write_b32 a183, v71\n"
        "v_accvgpr_write_b32 a184, v72\n"
        "v_accvgpr_write_b32 a185, v73\n"
        "v_accvgpr_write_b32 a186, v74\n"
        "v_accvgpr_write_b32 a187, v75\n"
        "v_accvgpr_write_b32 a188, v76\n"
        "v_accvgpr_write_b32 a189, v77\n"
        "v_accvgpr_write_b32 a190, v78\n"
        "v_accvgpr_write_b32 a191, v79\n"
        "s_mov_b32 s53, s45\n"
        "s_mov_b32 s96, 0\n"
        "s_mov_b32 s52, 3\n"
        "s_branch LREDO_%=\n"
        "LRET3_%=:\n"
        "s_branch LSH_%=\n"
        "LPCX_%=:\n"
        "s_mov_b32 s97, 0x200000\n"
        "LQCX_%=:\n"
        "global_load_dwordx2 v[236:237], v159, s[48:49] sc1\n"
        "s_waitcnt vmcnt(0)\n"
        "v_cmp_ne_u64 vcc, v[236:237], v[250:251]\n"
        "s_nop 1\n"
        "s_cbranch_vccz LBCX_%=\n"
        "s_sleep 2\n"
        "s_sub_u32 s97, s97, 1\n"
        "s_cmp_lg_u32 s97, 0\n"
        "s_cbranch_scc1 LQCX_%=\n"
        "s_branch LBCX_%=\n"
        "LPZZ_%=:\n"
        "s_mov_b32 s97, 0x200000\n"
        "LQZZ_%=:\n"
        "global_load_dwordx2 v[236:237], v234, s[48:49] sc1\n"
        "s_waitcnt vmcnt(0)\n"
        "v_cmp_ne_u64 vcc, v[236:237], v[250:251]\n"
        "s_nop 1\n"
        "s_cbranch_vccz LBZZ_%=\n"
        "s_sleep 2\n"
        "s_sub_u32 s97, s97, 1\n"
        "s_cmp_lg_u32 s97, 0\n"
        "s_cbranch_scc1 LQZZ_%=\n"
        "s_branch LBZZ_%=\n"
        "LPPR_%=:\n"
        "s_mov_b32 s97, 0x200000\n"
        "LQPR_%=:\n"
        "global_load_dwordx2 v[156:157], v155, s[48:49] sc1\n"
        "s_waitcnt vmcnt(0)\n"
        "v_cmp_ne_u64 vcc, v[156:157], v[250:251]\n"
        "s_nop 1\n"
        "s_cbranch_vccz LBPR_%=\n"
        "s_sleep 2\n"
        "s_sub_u32 s97, s97, 1\n"
        "s_cmp_lg_u32 s97, 0\n"
        "s_cbranch_scc1 LQPR_%=\n"
        "s_branch LBPR_%=\n"
        "LREDO_%=:\n"
        "s_lshl2_add_u32 s97, s55, s40\n"
        "s_lshl_b32 s97, s97, 12\n"
        "s_add_u32 s97, s97, 0x5100000\n"
        "s_add_u32 s88, s38, s97\n"
        "s_addc_u32 s89, s39, 0\n"
        "global_store_dwordx4 v254, v[144:147], s[88:89] offset:0\n"
        "global_store_dwordx4 v254, v[148:151], s[88:89] offset:1024\n"
        "global_store_dwordx4 v254, v[152:155], s[88:89] offset:2048\n"
        "global_store_dwordx4 v254, v[156:159], s[88:89] offset:3072\n"
        "s_waitcnt vmcnt(0)\n"
        "v_mov_b32 v112, 0\n"
        "v_mov_b32 v113, 0\n"
        "v_mov_b32 v114, 0\n"
        "v_mov_b32 v115, 0\n"
        "v_mov_b32 v116, 0\n"
        "v_mov_b32 v117, 0\n"
        "v_mov_b32 v118, 0\n"
        "v_mov_b32 v119, 0\n"
        "v_mov_b32 v120, 0\n"
        "v_mov_b32 v121, 0\n"
        "v_mov_b32 v122, 0\n"
        "v_mov_b32 v123, 0\n"
        "v_mov_b32 v124, 0\n"
        "v_mov_b32 v125, 0\n"
        "v_mov_b32 v126, 0\n"
        "v_mov_b32 v127, 0\n"
        "v_mov_b32 v144, 0\n"
        "v_mov_b32 v145, 0\n"
        "v_mov_b32 v146, 0\n"
        "v_mov_b32 v147, 0\n"
        "v_mov_b32 v148, 0\n"
        "v_mov_b32 v149, 0\n"
        "v_mov_b32 v150, 0\n"
        "v_mov_b32 v151, 0\n"
        "v_mov_b32 v152, 0\n"
        "v_mov_b32 v153, 0\n"
        "v_mov_b32 v154, 0\n"
        "v_mov_b32 v155, 0\n"
        "v_mov_b32 v156, 0\n"
        "v_mov_b32 v157, 0\n"
        "v_mov_b32 v158, 0\n"
        "v_mov_b32 v159, 0\n"
        "v_mov_b32 v60, 0\n"
        "v_mov_b32 v61, 0\n"
        "v_mov_b32 v62, 0\n"
        "v_mov_b32 v63, 0\n"
        "v_mov_b32 v24, 0xf149f2ca\n"
        "s_lshl_b32 s97, s53, 7\n"
        "s_add_u32 s97, s97, s96\n"
        "v_add_u32 v27, s97, v243\n"
        "s_mul_i32 s97, s43, 529408\n"
        "s_add_u32 s84, s38, s97\n"
        "s_addc_u32 s85, s39, 0\n"
        "s_add_u32 s84, s84, 0x1000\n"
        "s_addc_u32 s85, s85, 0\n"
        "s_add_u32 s86, s84, 0x1100000\n"
        "s_addc_u32 s87, s85, 0\n"
        "s_lshl_b32 s97, s53, 2\n"
        "s_add_u32 s97, s97, 4\n"
        "LRLOOP_%=:\n"
        "global_load_dwordx4 a[160:163], v254, s[84:85] offset:-4096 sc1\n"
        "global_load_dwordx4 a[164:167], v254, s[84:85] offset:-3072 sc1\n"
        "global_load_dwordx4 a[168:171], v254, s[84:85] offset:-2048 sc1\n"
        "global_load_dwordx4 a[172:175], v254, s[84:85] offset:-1024 sc1\n"
        "s_waitcnt vmcnt(0)\n"
        "v_mfma_f32_16x16x32_bf16 v[40:43], a[160:163], a[176:179], 0\n"
        "v_mfma_f32_16x16x32_bf16 v[40:43], a[164:167], a[180:183], v[40:43]\n"
        "v_mfma_f32_16x16x32_bf16 v[40:43], a[168:171], a[184:187], v[40:43]\n"
        "v_mfma_f32_16x16x32_bf16 v[40:43], a[172:175], a[188:191], v[40:43]\n"
        "s_nop 7\n"
        "global_load_dwordx4 a[160:163], v254, s[84:85] offset:0 sc1\n"
        "global_load_dwordx4 a[164:167], v254, s[84:85] offset:1024 sc1\n"
        "global_load_dwordx4 a[168:171], v254, s[84:85] offset:2048 sc1\n"
        "global_load_dwordx4 a[172:175], v254, s[84:85] offset:3072 sc1\n"
        "s_waitcnt vmcnt(0)\n"
        "v_mfma_f32_16x16x32_bf16 v[44:47], a[160:163], a[176:179], 0\n"
        "v_mfma_f32_16x16x32_bf16 v[44:47], a[164:167], a[180:183], v[44:47]\n"
        "v_mfma_f32_16x16x32_bf16 v[44:47], a[168:171], a[184:187], v[44:47]\n"
        "v_mfma_f32_16x16x32_bf16 v[44:47], a[172:175], a[188:191], v[44:47]\n"
        "s_nop 7\n"
        "v_cmp_gt_i32 vcc, 0, v27\n"
        "s_nop 1\n"
        "v_cndmask_b32 v40, v40, v242, vcc\n"
        "v_cmp_gt_i32 vcc, 1, v27\n"
        "s_nop 1\n"
        "v_cndmask_b32 v41, v41, v242, vcc\n"
        "v_cmp_gt_i32 vcc, 2, v27\n"
        "s_nop 1\n"
        "v_cndmask_b32 v42, v42, v242, vcc\n"
        "v_cmp_gt_i32 vcc, 3, v27\n"
        "s_nop 1\n"
        "v_cndmask_b32 v43, v43, v242, vcc\n"
        "v_cmp_gt_i32 vcc, 16, v27\n"
        "s_nop 1\n"
        "v_cndmask_b32 v44, v44, v242, vcc\n"
        "v_cmp_gt_i32 vcc, 17, v27\n"
        "s_nop 1\n"
        "v_cndmask_b32 v45, v45, v242, vcc\n"
        "v_cmp_gt_i32 vcc, 18, v27\n"
        "s_nop 1\n"
        "v_cndmask_b32 v46, v46, v242, vcc\n"
        "v_cmp_gt_i32 vcc, 19, v27\n"
        "s_nop 1\n"
        "v_cndmask_b32 v47, v47, v242, vcc\n"
        "v_max3_f32 v25, v40, v41, v24\n"
        "v_max3_f32 v25, v42, v43, v25\n"
        "v_max3_f32 v25, v44, v45, v25\n"
        "v_max3_f32 v25, v46, v47, v25\n"
        "v_mov_b32 v28, v25\n"
        "s_nop 1\n"
        "v_permlane16_swap_b32 v25, v28\n"
        "v_max_f32 v25, v25, v28\n"
        "v_mov_b32 v28, v25\n"
        "s_nop 1\n"
        "v_permlane32_swap_b32 v25, v28\n"
        "v_max_f32 v25, v25, v28\n"
        "v_sub_f32 v26, v24, v25\n"
        "v_exp_f32 v26, v26\n"
        "v_mov_b32 v24, v25\n"
        "v_sub_f32 v40, v40, v25\n"
        "v_exp_f32 v40, v40\n"
        "v_sub_f32 v41, v41, v25\n"
        "v_exp_f32 v41, v41\n"
        "v_sub_f32 v42, v42, v25\n"
        "v_exp_f32 v42, v42\n"
        "v_sub_f32 v43, v43, v25\n"
        "v_exp_f32 v43, v43\n"
        "v_sub_f32 v44, v44, v25\n"
        "v_exp_f32 v44, v44\n"
        "v_sub_f32 v45, v45, v25\n"
        "v_exp_f32 v45, v45\n"
        "v_sub_f32 v46, v46, v25\n"
        "v_exp_f32 v46, v46\n"
        "v_sub_f32 v47, v47, v25\n"
        "v_exp_f32 v47, v47\n"
        "v_cvt_pk_bf16_f32 v56, v40, v41\n"
        "v_cvt_pk_bf16_f32 v57, v42, v43\n"
        "v_cvt_pk_bf16_f32 v58, v44, v45\n"
        "v_cvt_pk_bf16_f32 v59, v46, v47\n"
        "v_mul_f32 v112, v112, v26\n"
        "v_mul_f32 v113, v113, v26\n"
        "v_mul_f32 v114, v114, v26\n"
        "v_mul_f32 v115, v115, v26\n"
        "v_mul_f32 v116, v116, v26\n"
        "v_mul_f32 v117, v117, v26\n"
        "v_mul_f32 v118, v118, v26\n"
        "v_mul_f32 v119, v119, v26\n"
        "v_mul_f32 v120, v120, v26\n"
        "v_mul_f32 v121, v121, v26\n"
        "v_mul_f32 v122, v122, v26\n"
        "v_mul_f32 v123, v123, v26\n"
        "v_mul_f32 v124, v124, v26\n"
        "v_mul_f32 v125, v125, v26\n"
        "v_mul_f32 v126, v126, v26\n"
        "v_mul_f32 v127, v127, v26\n"
        "v_mul_f32 v144, v144, v26\n"
        "v_mul_f32 v145, v145, v26\n"
        "v_mul_f32 v146, v146, v26\n"
        "v_mul_f32 v147, v147, v26\n"
        "v_mul_f32 v148, v148, v26\n"
        "v_mul_f32 v149, v149, v26\n"
        "v_mul_f32 v150, v150, v26\n"
        "v_mul_f32 v151, v151, v26\n"
        "v_mul_f32 v152, v152, v26\n"
        "v_mul_f32 v153, v153, v26\n"
        "v_mul_f32 v154, v154, v26\n"
        "v_mul_f32 v155, v155, v26\n"
        "v_mul_f32 v156, v156, v26\n"
        "v_mul_f32 v157, v157, v26\n"
        "v_mul_f32 v158, v158, v26\n"
        "v_mul_f32 v159, v159, v26\n"
        "v_mul_f32 v60, v60, v26\n"
        "v_mul_f32 v61, v61, v26\n"
        "v_mul_f32 v62, v62, v26\n"
        "v_mul_f32 v63, v63, v26\n"
        "s_nop 1\n"
        "v_mfma_f32_16x16x32_bf16 v[60:63], a[80:83], v[56:59], v[60:63]\n"
        "global_load_dwordx4 a[160:163], v254, s[86:87] offset:-4096 sc1\n"
        "global_load_dwordx4 a[164:167], v254, s[86:87] offset:-3072 sc1\n"
        "global_load_dwordx4 a[168:171], v254, s[86:87] offset:-2048 sc1\n"
        "global_load_dwordx4 a[172:175], v254, s[86:87] offset:-1024 sc1\n"
        "s_waitcnt vmcnt(0)\n"
        "v_mfma_f32_16x16x32_bf16 v[112:115], a[160:163], v[56:59], v[112:115]\n"
        "v_mfma_f32_16x16x32_bf16 v[116:119], a[164:167], v[56:59], v[116:119]\n"
        "v_mfma_f32_16x16x32_bf16 v[120:123], a[168:171], v[56:59], v[120:123]\n"
        "v_mfma_f32_16x16x32_bf16 v[124:127], a[172:175], v[56:59], v[124:127]\n"
        "s_nop 7\n"
        "global_load_dwordx4 a[160:163], v254, s[86:87] offset:0 sc1\n"
        "global_load_dwordx4 a[164:167], v254, s[86:87] offset:1024 sc1\n"
        "global_load_dwordx4 a[168:171], v254, s[86:87] offset:2048 sc1\n"
        "global_load_dwordx4 a[172:175], v254, s[86:87] offset:3072 sc1\n"
        "s_waitcnt vmcnt(0)\n"
        "v_mfma_f32_16x16x32_bf16 v[144:147], a[160:163], v[56:59], v[144:147]\n"
        "v_mfma_f32_16x16x32_bf16 v[148:151], a[164:167], v[56:59], v[148:151]\n"
        "v_mfma_f32_16x16x32_bf16 v[152:155], a[168:171], v[56:59], v[152:155]\n"
        "v_mfma_f32_16x16x32_bf16 v[156:159], a[172:175], v[56:59], v[156:159]\n"
        "s_nop 7\n"
        "s_add_u32 s84, s84, 0x2000\n"
        "s_addc_u32 s85, s85, 0\n"
        "s_add_u32 s86, s86, 0x2000\n"
        "s_addc_u32 s87, s87, 0\n"
        "v_subrev_u32 v27, 32, v27\n"
        "s_sub_u32 s97, s97, 1\n"
        "s_cmp_lg_u32 s97, 0\n"
        "s_cbranch_scc1 LRLOOP_%=\n"
        "s_mul_i32 s97, s53, 1052672\n"
        "s_lshl_b32 s86, s43, 15\n"
        "s_add_u32 s97, s97, s86\n"
        "s_lshl_b32 s86, s40, 10\n"
        "s_add_u32 s97, s97, s86\n"
        "s_lshl_b32 s86, s96, 6\n"
        "s_add_u32 s97, s97, s86\n"
        "s_add_u32 s97, s97, 0x2200000\n"
        "s_add_u32 s84, s38, s97\n"
        "s_addc_u32 s85, s39, 0\n"
        "v_rcp_f32 v26, v60\n"
        "s_nop 0\n"
        "v_mul_f32 v112, v112, v26\n"
        "v_mul_f32 v113, v113, v26\n"
        "v_mul_f32 v114, v114, v26\n"
        "v_mul_f32 v115, v115, v26\n"
        "v_mul_f32 v116, v116, v26\n"
        "v_mul_f32 v117, v117, v26\n"
        "v_mul_f32 v118, v118, v26\n"
        "v_mul_f32 v119, v119, v26\n"
        "v_cvt_pk_bf16_f32 v112, v112, v113\n"
        "v_cvt_pk_bf16_f32 v113, v114, v115\n"
        "v_cvt_pk_bf16_f32 v114, v116, v117\n"
        "v_cvt_pk_bf16_f32 v115, v118, v119\n"
        "global_store_dwordx4 v254, v[112:115], s[84:85] sc1\n"
        "s_add_u32 s84, s84, 0x2000\n"
        "s_addc_u32 s85, s85, 0\n"
        "v_mul_f32 v120, v120, v26\n"
        "v_mul_f32 v121, v121, v26\n"
        "v_mul_f32 v122, v122, v26\n"
        "v_mul_f32 v123, v123, v26\n"
        "v_mul_f32 v124, v124, v26\n"
        "v_mul_f32 v125, v125, v26\n"
        "v_mul_f32 v126, v126, v26\n"
        "v_mul_f32 v127, v127, v26\n"
        "v_cvt_pk_bf16_f32 v120, v120, v121\n"
        "v_cvt_pk_bf16_f32 v121, v122, v123\n"
        "v_cvt_pk_bf16_f32 v122, v124, v125\n"
        "v_cvt_pk_bf16_f32 v123, v126, v127\n"
        "global_store_dwordx4 v254, v[120:123], s[84:85] sc1\n"
        "s_add_u32 s84, s84, 0x2000\n"
        "s_addc_u32 s85, s85, 0\n"
        "v_mul_f32 v144, v144, v26\n"
        "v_mul_f32 v145, v145, v26\n"
        "v_mul_f32 v146, v146, v26\n"
        "v_mul_f32 v147, v147, v26\n"
        "v_mul_f32 v148, v148, v26\n"
        "v_mul_f32 v149, v149, v26\n"
        "v_mul_f32 v150, v150, v26\n"
        "v_mul_f32 v151, v151, v26\n"
        "v_cvt_pk_bf16_f32 v144, v144, v145\n"
        "v_cvt_pk_bf16_f32 v145, v146, v147\n"
        "v_cvt_pk_bf16_f32 v146, v148, v149\n"
        "v_cvt_pk_bf16_f32 v147, v150, v151\n"
        "global_store_dwordx4 v254, v[144:147], s[84:85] sc1\n"
        "s_add_u32 s84, s84, 0x2000\n"
        "s_addc_u32 s85, s85, 0\n"
        "v_mul_f32 v152, v152, v26\n"
        "v_mul_f32 v153, v153, v26\n"
        "v_mul_f32 v154, v154, v26\n"
        "v_mul_f32 v155, v155, v26\n"
        "v_mul_f32 v156, v156, v26\n"
        "v_mul_f32 v157, v157, v26\n"
        "v_mul_f32 v158, v158, v26\n"
        "v_mul_f32 v159, v159, v26\n"
        "v_cvt_pk_bf16_f32 v152, v152, v153\n"
        "v_cvt_pk_bf16_f32 v153, v154, v155\n"
        "v_cvt_pk_bf16_f32 v154, v156, v157\n"
        "v_cvt_pk_bf16_f32 v155, v158, v159\n"
        "global_store_dwordx4 v254, v[152:155], s[84:85] sc1\n"
        "s_add_u32 s84, s84, 0x2000\n"
        "s_addc_u32 s85, s85, 0\n"
        "s_waitcnt vmcnt(0)\n"
        "global_load_dwordx4 v[144:147], v254, s[88:89] offset:0 sc1\n"
        "global_load_dwordx4 v[148:151], v254, s[88:89] offset:1024 sc1\n"
        "global_load_dwordx4 v[152:155], v254, s[88:89] offset:2048 sc1\n"
        "global_load_dwordx4 v[156:159], v254, s[88:89] offset:3072 sc1\n"
        "s_waitcnt vmcnt(0)\n"
        "s_cmp_eq_u32 s52, 0\n"
        "s_cbranch_scc1 LRET0_%=\n"
        "s_cmp_eq_u32 s52, 1\n"
        "s_cbranch_scc1 LRET1_%=\n"
        "s_cmp_eq_u32 s52, 2\n"
        "s_cbranch_scc1 LRET2_%=\n"
        "s_branch LRET3_%=\n"
        "LEND_%=:\n"
        ".p2align 12\n"
        :
        : "s"(__builtin_amdgcn_kernarg_segment_ptr()), "s"(blockIdx.x), "s"(__builtin_amdgcn_readfirstlane(threadIdx.x >> 6))
        : "v0", "v1", "v2", "v3", "v4", "v5", "v6", "v7", "v8", "v9", "v10", "v11", "v12", "v13", "v14", "v15", "v16", "v17", "v18", "v19", "v20", "v21", "v22", "v23", "v24", "v25", "v26", "v27", "v28", "v29", "v30", "v31",
        "v32", "v33", "v34", "v35", "v36", "v37", "v38", "v39", "v40", "v41", "v42", "v43", "v44", "v45", "v46", "v47", "v48", "v49", "v50", "v51", "v52", "v53", "v54", "v55", "v56", "v57", "v58", "v59", "v60", "v61", "v62", "v63",
        "v64", "v65", "v66", "v67", "v68", "v69", "v70", "v71", "v72", "v73", "v74", "v75", "v76", "v77", "v78", "v79", "v80", "v81", "v82", "v83", "v84", "v85", "v86", "v87", "v88", "v89", "v90", "v91", "v92", "v93", "v94", "v95",
        "v96", "v97", "v98", "v99", "v100", "v101", "v102", "v103", "v104", "v105", "v106", "v107", "v108", "v109", "v110", "v111", "v112", "v113", "v114", "v115", "v116", "v117", "v118", "v119", "v120", "v121", "v122", "v123", "v124", "v125", "v126", "v127",
        "v128", "v129", "v130", "v131", "v132", "v133", "v134", "v135", "v136", "v137", "v138", "v139", "v140", "v141", "v142", "v143", "v144", "v145", "v146", "v147", "v148", "v149", "v150", "v151", "v152", "v153", "v154", "v155", "v156", "v157", "v158", "v159",
        "v160", "v161", "v162", "v163", "v164", "v165", "v166", "v167", "v168", "v169", "v170", "v171", "v172", "v173", "v174", "v175", "v176", "v177", "v178", "v179", "v180", "v181", "v182", "v183", "v184", "v185", "v186", "v187", "v188", "v189", "v190", "v191",
        "v192", "v193", "v194", "v195", "v196", "v197", "v198", "v199", "v200", "v201", "v202", "v203", "v204", "v205", "v206", "v207", "v208", "v209", "v210", "v211", "v212", "v213", "v214", "v215", "v216", "v217", "v218", "v219", "v220", "v221", "v222", "v223",
        "v224", "v225", "v226", "v227", "v228", "v229", "v230", "v231", "v232", "v233", "v234", "v235", "v236", "v237", "v238", "v239", "v240", "v241", "v242", "v243", "v244", "v245", "v246", "v247", "v248", "v249", "v250", "v251", "v252", "v253", "v254", "v255",
        "a0", "a1", "a2", "a3", "a4", "a5", "a6", "a7", "a8", "a9", "a10", "a11", "a12", "a13", "a14", "a15", "a16", "a17", "a18", "a19", "a20", "a21", "a22", "a23", "a24", "a25", "a26", "a27", "a28", "a29", "a30", "a31",
        "a32", "a33", "a34", "a35", "a36", "a37", "a38", "a39", "a40", "a41", "a42", "a43", "a44", "a45", "a46", "a47", "a48", "a49", "a50", "a51", "a52", "a53", "a54", "a55", "a56", "a57", "a58", "a59", "a60", "a61", "a62", "a63",
        "a64", "a65", "a66", "a67", "a68", "a69", "a70", "a71", "a72", "a73", "a74", "a75", "a76", "a77", "a78", "a79", "a80", "a81", "a82", "a83", "a84", "a85", "a86", "a87", "a88", "a89", "a90", "a91", "a92", "a93", "a94", "a95",
        "a96", "a97", "a98", "a99", "a100", "a101", "a102", "a103", "a104", "a105", "a106", "a107", "a108", "a109", "a110", "a111", "a112", "a113", "a114", "a115", "a116", "a117", "a118", "a119", "a120", "a121", "a122", "a123", "a124", "a125", "a126", "a127",
        "a128", "a129", "a130", "a131", "a132", "a133", "a134", "a135", "a136", "a137", "a138", "a139", "a140", "a141", "a142", "a143", "a144", "a145", "a146", "a147", "a148", "a149", "a150", "a151", "a152", "a153", "a154", "a155", "a156", "a157", "a158", "a159",
        "a160", "a161", "a162", "a163", "a164", "a165", "a166", "a167", "a168", "a169", "a170", "a171", "a172", "a173", "a174", "a175", "a176", "a177", "a178", "a179", "a180", "a181", "a182", "a183", "a184", "a185", "a186", "a187", "a188", "a189", "a190", "a191",
        "a192", "a193", "a194", "a195", "a196", "a197", "a198", "a199", "a200", "a201", "a202", "a203", "a204", "a205", "a206", "a207", "a208", "a209", "a210", "a211", "a212", "a213", "a214", "a215", "a216", "a217", "a218", "a219", "a220", "a221", "a222", "a223",
        "a224", "a225", "a226", "a227", "a228", "a229", "a230", "a231", "a232", "a233", "a234", "a235", "a236", "a237", "a238", "a239", "a240", "a241", "a242", "a243", "a244", "a245", "a246", "a247", "a248", "a249", "a250", "a251", "a252", "a253", "a254", "a255",
        "s8", "s9", "s10", "s11", "s12", "s13", "s14", "s15", "s16", "s17", "s18", "s19", "s20", "s21", "s22", "s23", "s24", "s25", "s26", "s27", "s28", "s29", "s30", "s31", "s34", "s35", "s36", "s37", "s38", "s39", "s40", "s41",
        "s42", "s43", "s44", "s45", "s46", "s47", "s48", "s49", "s50", "s51", "s52", "s53", "s54", "s55", "s56", "s57", "s58", "s59", "s60", "s61", "s62", "s63", "s64", "s65", "s66", "s67", "s68", "s69", "s70", "s71", "s72", "s73",
        "s74", "s75", "s76", "s77", "s78", "s79", "s80", "s81", "s82", "s83", "s84", "s85", "s86", "s87", "s88", "s89", "s90", "s91", "s92", "s93", "s94", "s95", "s96", "s97", "s98", "s99", "vcc", "scc", "memory");
}

extern "C" void kernel_launch(void* const* d_in, const int* in_sizes, int n_in, void* d_out, int out_size, void* d_ws, size_t ws_size, hipStream_t stream) {
    (void)in_sizes; (void)n_in; (void)out_size;
    constexpr size_t WS_USED = (size_t)85 << 20;
    Params P;
    P.x = d_in[0]; P.ckv = d_in[1]; P.wq = d_in[2]; P.wuk = d_in[3]; P.wuv = d_in[4]; P.wo = d_in[5];
    P.bk = d_in[7]; P.bv = d_in[8]; P.shk = d_in[9]; P.ssk = d_in[10]; P.ssd = d_in[11]; P.gw = d_in[12];
    P.out = d_out; P.ws = (char*)d_ws;
    if (ws_size > WS_USED) P.ws += (ws_size - WS_USED) & ~(size_t)0xfffff;
    constexpr int LDS_MAX = 163840;
    (void)hipFuncSetAttribute((const void*)k_main, hipFuncAttributeMaxDynamicSharedMemorySize, LDS_MAX);
    k_main<<<256, 256, LDS_MAX, stream>>>(P);
}
```

```cpp
#include <hip/hip_runtime.h>
#include <stdint.h>

struct Params {
    const void *x, *ckv, *wq, *wuk, *wuv, *wo, *bk, *bv, *shk, *ssk, *ssd, *gw;
    void* out; char* ws;
};

__global__ __launch_bounds__(256, 1) void k_main(Params P) {
    asm volatile(
        "; ---- PRO\n"
        "s_load_dwordx16 s[8:23], %0, 0\n"
        "s_load_dwordx8 s[24:31], %0, 64\n"
        "s_load_dwordx4 s[36:39], %0, 96\n"
        "s_mov_b32 s55, %1\n"
        "s_mov_b32 s40, %2\n"
        "v_mbcnt_lo_u32_b32 v0, -1, 0\n"
        "v_mbcnt_hi_u32_b32 v0, -1, v0\n"
        "s_getpc_b64 s[46:47]\n"
        "v_min_u32 v4, 12, v0\n"
        "v_lshlrev_b32 v4, 12, v4\n"
        "global_load_dword v253, v4, s[46:47] offset:0\n"
        "s_and_b32 s41, s55, 7\n"
        "s_lshr_b32 s42, s55, 3\n"
        "s_lshr_b32 s50, s42, 3\n"
        "s_lshl2_add_u32 s43, s41, s50\n"
        "s_and_b32 s44, s42, 7\n"
        "s_sub_u32 s45, 15, s44\n"
        "s_lshl_b32 s72, s40, 13\n"
        "s_lshl_b32 s73, s40, 12\n"
        "v_lshlrev_b32 v254, 4, v0\n"
        "v_lshrrev_b32 v2, 4, v0\n"
        "v_and_b32 v1, 15, v0\n"
        "v_lshrrev_b32 v4, 3, v0\n"
        "v_and_b32 v5, 7, v0\n"
        "v_and_b32 v8, 6, v4\n"
        "v_xor_b32 v8, v8, v5\n"
        "v_lshlrev_b32 v8, 4, v8\n"
        "v_lshl_add_u32 v9, v4, 13, v8\n"
        "v_xor_b32 v10, 4, v4\n"
        "v_lshl_add_u32 v10, v10, 13, v8\n"
        "v_add_u32 v238, 4096, v9\n"
        "v_add_u32 v239, 3200, v9\n"
        "v_add_u32 v240, 67584, v10\n"
        "v_add_u32 v241, 66688, v10\n"
        "s_cmp_ge_u32 s40, 2\n"
        "s_cselect_b32 s50, s45, s44\n"
        "s_and_b32 s51, s40, 1\n"
        "s_lshl1_add_u32 s50, s50, s51\n"
        "s_lshl_b32 s50, s50, 16\n"
        "s_lshl_b32 s51, s40, 17\n"
        "s_lshl_b32 s52, s43, 8\n"
        "s_add_u32 s51, s51, s52\n"
        "s_waitcnt lgkmcnt(0)\n"
        "s_add_u32 s60, s14, s51\n"
        "s_addc_u32 s61, s15, 0\n"
        "s_sub_u32 s60, s60, 4096\n"
        "s_subb_u32 s61, s61, 0\n"
        "s_add_u32 m0, s73, 0x10000\n"
        "s_nop 0\n"
        "global_load_lds_dwordx4 v238, s[60:61] offset:0\n"
        "global_load_lds_dwordx4 v239, s[60:61] offset:1024\n"
        "global_load_lds_dwordx4 v240, s[60:61] offset:2048\n"
        "global_load_lds_dwordx4 v241, s[60:61] offset:3072\n"
        "v_xor_b32 v6, v5, v2\n"
        "v_lshlrev_b32 v6, 4, v6\n"
        "v_xor_b32 v7, 64, v6\n"
        "v_lshl_add_u32 v234, v4, 10, v6\n"
        "v_lshl_add_u32 v8, v4, 10, v7\n"
        "v_add_u32 v235, 7168, v8\n"
        "v_add_u32 v236, 14336, v234\n"
        "v_add_u32 v237, 21504, v8\n"
        "s_add_u32 s56, s10, s50\n"
        "s_addc_u32 s57, s11, 0\n"
        "s_add_u32 m0, s72, 0x0\n"
        "s_nop 0\n"
        "global_load_lds_dwordx4 v234, s[56:57] offset:0\n"
        "global_load_lds_dwordx4 v235, s[56:57] offset:1024\n"
        "global_load_lds_dwordx4 v236, s[56:57] offset:2048\n"
        "global_load_lds_dwordx4 v237, s[56:57] offset:3072\n"
        "s_getpc_b64 s[46:47]\n"
        "LPC_%=:\n"
        "s_lshl2_add_u32 s53, s42, s40\n"
        "s_lshl_b32 s53, s53, 10\n"
        "v_lshlrev_b32 v12, 6, v1\n"
        "v_add_u32 v12, s53, v12\n"
        "v_min_u32 v252, LEND_%=-LPC_%=-4, v12\n"
        "global_load_dword v253, v252, s[46:47] offset:0\n"
        "s_add_u32 s62, s16, s51\n"
        "s_addc_u32 s63, s17, 0\n"
        "s_sub_u32 s62, s62, 4096\n"
        "s_subb_u32 s63, s63, 0\n"
        "s_add_u32 m0, s73, 0x14000\n"
        "s_nop 0\n"
        "global_load_lds_dwordx4 v238, s[62:63] offset:0\n"
        "global_load_lds_dwordx4 v239, s[62:63] offset:1024\n"
        "global_load_lds_dwordx4 v240, s[62:63] offset:2048\n"
        "global_load_lds_dwordx4 v241, s[62:63] offset:3072\n"
        "s_add_u32 s58, s56, 0x8000\n"
        "s_addc_u32 s59, s57, 0\n"
        "s_add_u32 s68, s12, s51\n"
        "s_addc_u32 s69, s13, 0\n"
        "s_add_u32 m0, s72, 0x1000\n"
        "s_nop 0\n"
        "global_load_lds_dwordx4 v234, s[58:59] offset:0\n"
        "global_load_lds_dwordx4 v235, s[58:59] offset:1024\n"
        "global_load_lds_dwordx4 v236, s[58:59] offset:2048\n"
        "global_load_lds_dwordx4 v237, s[58:59] offset:3072\n"
        "s_sub_u32 s68, s68, 4096\n"
        "s_subb_u32 s69, s69, 0\n"
        "v_lshrrev_b32 v8, 1, v1\n"
        "v_xor_b32 v8, v8, v2\n"
        "v_lshlrev_b32 v8, 4, v8\n"
        "v_lshl_add_u32 v8, v1, 7, v8\n"
        "v_xor_b32 v9, 64, v8\n"
        "s_lshr_b32 s50, s40, 1\n"
        "s_lshl_b32 s50, s50, 13\n"
        "v_add_u32 v224, s50, v8\n"
        "v_add_u32 v246, s72, v8\n"
        "v_add_u32 v248, 0x10000, v246\n"
        "v_add_u32 v225, s50, v9\n"
        "v_add_u32 v247, s72, v9\n"
        "v_add_u32 v249, 0x10000, v247\n"
        "s_and_b32 s50, s40, 1\n"
        "s_lshl_b32 s50, s50, 10\n"
        "s_add_u32 s50, s50, 0x10000\n"
        "v_lshlrev_b32 v8, 11, v2\n"
        "v_add_u32 v8, s50, v8\n"
        "v_lshrrev_b32 v9, 2, v1\n"
        "v_or_b32 v10, 4, v9\n"
        "v_and_b32 v11, 1, v2\n"
        "v_and_b32 v13, 3, v0\n"
        "v_lshlrev_b32 v14, 2, v11\n"
        "v_xor_b32 v14, v14, v9\n"
        "v_bfe_u32 v15, v14, 1, 2\n"
        "v_xor_b32 v15, 0, v15\n"
        "v_lshlrev_b32 v15, 5, v15\n"
        "v_lshl_add_u32 v14, v14, 7, v15\n"
        "v_lshl_add_u32 v14, v13, 3, v14\n"
        "v_add_u32 v226, v14, v8\n"
        "v_xor_b32 v228, 32, v226\n"
        "v_xor_b32 v230, 64, v226\n"
        "v_xor_b32 v232, 96, v226\n"
        "v_lshlrev_b32 v14, 2, v11\n"
        "v_xor_b32 v14, v14, v10\n"
        "v_bfe_u32 v15, v14, 1, 2\n"
        "v_xor_b32 v15, 0, v15\n"
        "v_lshlrev_b32 v15, 5, v15\n"
        "v_lshl_add_u32 v14, v14, 7, v15\n"
        "v_lshl_add_u32 v14, v13, 3, v14\n"
        "v_add_u32 v227, v14, v8\n"
        "v_xor_b32 v229, 32, v227\n"
        "v_xor_b32 v231, 64, v227\n"
        "v_xor_b32 v233, 96, v227\n"
        "v_add_u32 v216, 0x10000, v226\n"
        "v_add_u32 v217, 0x10000, v227\n"
        "v_add_u32 v218, 0x10000, v228\n"
        "v_add_u32 v219, 0x10000, v229\n"
        "v_add_u32 v220, 0x10000, v230\n"
        "v_add_u32 v221, 0x10000, v231\n"
        "v_add_u32 v222, 0x10000, v232\n"
        "v_add_u32 v223, 0x10000, v233\n"
        "v_lshl_add_u32 v242, v4, 11, v6\n"
        "v_lshl_add_u32 v8, v4, 11, v7\n"
        "v_add_u32 v243, 15360, v8\n"
        "v_add_u32 v244, 129024, v242\n"
        "v_add_u32 v245, 144384, v8\n"
        "s_lshl_b32 s50, s45, 18\n"
        "s_lshl_b32 s51, s40, 15\n"
        "s_add_u32 s50, s50, s51\n"
        "s_add_u32 s64, s8, s50\n"
        "s_addc_u32 s65, s9, 0\n"
        "s_lshl_b32 s50, s44, 18\n"
        "s_add_u32 s50, s50, s51\n"
        "s_add_u32 s66, s8, s50\n"
        "s_addc_u32 s67, s9, 0\n"
        "s_add_u32 s48, s38, 0x3b00000\n"
        "s_addc_u32 s49, s39, 0\n"
        "s_mov_b32 s35, 0xffff0000\n"
        "s_add_u32 s34, s43, s44\n"
        "v_mov_b32 v11, 0\n"
        "global_load_dwordx2 v[250:251], v11, s[48:49] sc1\n"
        "s_lshl2_add_u32 s82, s55, s40\n"
        "s_lshl_b32 s82, s82, 11\n"
        "v_and_b32 v20, 0xf0, v254\n"
        "v_lshl_add_u32 v20, v20, 3, s82\n"
        "global_load_dword v253, v20, s[10:11] offset:0\n"
        "s_lshl2_add_u32 s82, s42, s40\n"
        "s_lshl3_add_u32 s82, s82, s41\n"
        "s_lshl_b32 s82, s82, 10\n"
        "v_and_b32 v21, 0x70, v254\n"
        "v_lshl_add_u32 v173, v21, 3, s82\n"
        "s_mov_b64 exec, 0\n"
        "s_cmp_eq_u32 s40, 0\n"
        "s_cbranch_scc1 LHA0_%=\n"
        "s_cmp_eq_u32 s40, 1\n"
        "s_cbranch_scc1 LHA1_%=\n"
        "s_cmp_eq_u32 s40, 2\n"
        "s_cbranch_scc1 LHA2_%=\n"
        "s_branch LHA3_%=\n"
        "LHRA_%=:\n"
        "s_mov_b64 exec, -1\n"
        "s_add_u32 m0, s73, 0x18000\n"
        "s_add_u32 s86, s60, 0x80000\n"
        "s_addc_u32 s87, s61, 0\n"
        "global_load_lds_dwordx4 v238, s[86:87] offset:0\n"
        "global_load_lds_dwordx4 v239, s[86:87] offset:1024\n"
        "global_load_lds_dwordx4 v240, s[86:87] offset:2048\n"
        "global_load_lds_dwordx4 v241, s[86:87] offset:3072\n"
        "s_add_u32 m0, s73, 0x1c000\n"
        "s_add_u32 s86, s62, 0x80000\n"
        "s_addc_u32 s87, s63, 0\n"
        "global_load_lds_dwordx4 v238, s[86:87] offset:0\n"
        "global_load_lds_dwordx4 v239, s[86:87] offset:1024\n"
        "global_load_lds_dwordx4 v240, s[86:87] offset:2048\n"
        "global_load_lds_dwordx4 v241, s[86:87] offset:3072\n"
        "; ---- FRONT\n"
        "s_add_u32 m0, s72, 0x8000\n"
        "s_add_u32 s86, s56, 0x80\n"
        "s_addc_u32 s87, s57, 0\n"
        "global_load_lds_dwordx4 v234, s[86:87] offset:0\n"
        "s_waitcnt vmcnt(20)\n"
        "s_barrier\n"
        "ds_read_b64_tr_b16 v[32:33], v226 offset:0\n"
        "ds_read_b64_tr_b16 v[34:35], v227 offset:0\n"
        "ds_read_b128 v[0:3], v224 offset:16384\n"
        "global_load_lds_dwordx4 v235, s[86:87] offset:1024\n"
        "global_load_lds_dwordx4 v236, s[86:87] offset:2048\n"
        "ds_read_b128 v[4:7], v225 offset:16384\n"
        "ds_read_b64_tr_b16 v[64:65], v226 offset:8192\n"
        "ds_read_b64_tr_b16 v[66:67], v227 offset:8192\n"
        "ds_read_b128 v[12:15], v225 offset:18432\n"
        "s_lshl_b32 s94, s41, 7\n"
        "s_waitcnt lgkmcnt(4)\n"
        "v_mfma_f32_16x16x32_bf16 a[0:3], v[32:35], v[0:3], 0\n"
        "global_load_lds_dwordx4 v237, s[86:87] offset:3072\n"
        "ds_read_b128 v[8:11], v224 offset:18432\n"
        "s_add_u32 m0, s72, 0x9000\n"
        "s_add_u32 s86, s58, 0x80\n"
        "s_addc_u32 s87, s59, 0\n"
        "global_load_lds_dwordx4 v234, s[86:87] offset:0\n"
        "ds_read_b64_tr_b16 v[36:37], v228 offset:0\n"
        "s_lshl2_add_u32 s94, s42, s94\n"
        "s_add_u32 s94, s94, s40\n"
        "s_waitcnt lgkmcnt(3)\n"
        "v_mfma_f32_16x16x32_bf16 a[0:3], v[64:67], v[4:7], a[0:3]\n"
        "ds_read_b64_tr_b16 v[38:39], v229 offset:0\n"
        "s_lshl_b32 s94, s94, 1\n"
        "s_waitcnt lgkmcnt(3)\n"
        "v_mfma_f32_16x16x32_bf16 a[32:35], v[64:67], v[12:15], 0\n"
        "ds_read_b64_tr_b16 v[68:69], v228 offset:8192\n"
        "ds_read_b64_tr_b16 v[70:71], v229 offset:8192\n"
        "ds_read_b64_tr_b16 v[40:41], v230 offset:0\n"
        "s_lshl_b32 s50, s42, 6\n"
        "s_waitcnt lgkmcnt(5)\n"
        "v_mfma_f32_16x16x32_bf16 a[32:35], v[32:35], v[8:11], a[32:35]\n"
        "ds_read_b64_tr_b16 v[42:43], v231 offset:0\n"
        "ds_read_b64_tr_b16 v[72:73], v230 offset:8192\n"
        "ds_read_b64_tr_b16 v[74:75], v231 offset:8192\n"
        "ds_read_b64_tr_b16 v[44:45], v232 offset:0\n"
        "ds_read_b64_tr_b16 v[46:47], v233 offset:0\n"
        "ds_read_b64_tr_b16 v[76:77], v232 offset:8192\n"
        "ds_read_b64_tr_b16 v[78:79], v233 offset:8192\n"
        "ds_read_b128 v[16:19], v224 offset:0\n"
        "ds_read_b128 v[20:23], v225 offset:0\n"
        "global_load_lds_dwordx4 v235, s[86:87] offset:1024\n"
        "s_lshl_b32 s51, s41, 7\n"
        "s_waitcnt lgkmcnt(12)\n"
        "v_mfma_f32_16x16x32_bf16 a[36:39], v[36:39], v[8:11], 0\n"
        "ds_read_b128 v[24:27], v224 offset:2048\n"
        "ds_read_b128 v[28:31], v225 offset:2048\n"
        "s_lshl_b32 s52, s40, 5\n"
        "s_waitcnt lgkmcnt(12)\n"
        "v_mfma_f32_16x16x32_bf16 a[36:39], v[68:71], v[12:15], a[36:39]\n"
        "s_add_u32 s82, s12, 0x0\n"
        "s_addc_u32 s83, s13, 0\n"
        "v_mov_b32 v172, s94\n"
        "v_mfma_f32_16x16x32_bf16 a[4:7], v[68:71], v[4:7], 0\n"
        "s_waitcnt lgkmcnt(10)\n"
        "v_lshlrev_b32 v198, 11, v172\n"
        "v_lshrrev_b32 v166, 4, v254\n"
        "v_mfma_f32_16x16x32_bf16 a[4:7], v[36:39], v[0:3], a[4:7]\n"
        "global_load_lds_dwordx4 v236, s[86:87] offset:2048\n"
        "s_add_u32 s51, s51, s52\n"
        "v_lshrrev_b32 v167, 3, v166\n"
        "v_mfma_f32_16x16x32_bf16 a[8:11], v[40:43], v[0:3], 0\n"
        "s_waitcnt lgkmcnt(7)\n"
        "v_and_b32 v168, 7, v166\n"
        "v_lshl_add_u32 v167, v167, 3, s50\n"
        "v_mfma_f32_16x16x32_bf16 a[8:11], v[72:75], v[4:7], a[8:11]\n"
        "s_waitcnt lgkmcnt(5)\n"
        "v_lshl_add_u32 v168, v168, 2, s51\n"
        "v_lshlrev_b32 v169, 11, v167\n"
        "v_mfma_f32_16x16x32_bf16 a[40:43], v[72:75], v[12:15], 0\n"
        "s_waitcnt lgkmcnt(3)\n"
        "v_lshl_add_u32 v169, v168, 1, v169\n"
        "v_cmp_eq_u32 s[78:79], 0, v167\n"
        "v_mfma_f32_16x16x32_bf16 a[40:43], v[40:43], v[8:11], a[40:43]\n"
        "global_load_lds_dwordx4 v237, s[86:87] offset:3072\n"
        "s_add_u32 m0, s73, 0x20000\n"
        "s_waitcnt vmcnt(22)\n"
        "v_mfma_f32_16x16x32_bf16 a[44:47], v[44:47], v[8:11], 0\n"
        "s_add_u32 s86, s60, 0x100000\n"
        "s_addc_u32 s87, s61, 0\n"
        "v_mov_b32 v171, 6144\n"
        "v_mfma_f32_16x16x32_bf16 a[44:47], v[76:79], v[12:15], a[44:47]\n"
        "s_barrier\n"
        "v_mfma_f32_16x16x32_bf16 a[12:15], v[76:79], v[4:7], 0\n"
        "ds_read_b64_tr_b16 v[48:49], v226 offset:16384\n"
        "s_and_b32 s91, s43, 15\n"
        "v_cndmask_b32 v170, 0, v171, s[78:79]\n"
        "v_mfma_f32_16x16x32_bf16 a[12:15], v[44:47], v[0:3], a[12:15]\n"
        "global_load_lds_dwordx4 v238, s[86:87] offset:0\n"
        "ds_read_b64_tr_b16 v[50:51], v227 offset:16384\n"
        "s_mul_i32 s96, s40, 4224\n"
        "v_mfma_f32_16x16x32_bf16 a[128:131], v[32:35], v[16:19], 0\n"
        "ds_read_b64_tr_b16 v[80:81], v226 offset:24576\n"
        "s_add_u32 s96, s96, 0x20240\n"
        "s_waitcnt lgkmcnt(3)\n"
        "v_mfma_f32_16x16x32_bf16 a[128:131], v[64:67], v[20:23], a[128:131]\n"
        "ds_read_b64_tr_b16 v[82:83], v227 offset:24576\n"
        "v_add_u32 v170, v170, v169\n"
        "v_mfma_f32_16x16x32_bf16 a[160:163], v[64:67], v[28:31], 0\n"
        "ds_read_b64_tr_b16 v[52:53], v228 offset:16384\n"
        "v_sub_u32 v171, 2040, v167\n"
        "v_mfma_f32_16x16x32_bf16 a[160:163], v[32:35], v[24:27], a[160:163]\n"
        "ds_read_b64_tr_b16 v[54:55], v229 offset:16384\n"
        "global_load_lds_dwordx4 v239, s[86:87] offset:1024\n"
        "v_mfma_f32_16x16x32_bf16 a[164:167], v[36:39], v[24:27], 0\n"
        "ds_read_b64_tr_b16 v[84:85], v228 offset:24576\n"
        "v_lshlrev_b32 v171, 1, v171\n"
        "v_mfma_f32_16x16x32_bf16 a[164:167], v[68:71], v[28:31], a[164:167]\n"
        "ds_read_b64_tr_b16 v[86:87], v229 offset:24576\n"
        "v_lshl_add_u32 v171, v168, 12, v171\n"
        "v_mfma_f32_16x16x32_bf16 a[132:135], v[68:71], v[20:23], 0\n"
        "ds_read_b64_tr_b16 v[56:57], v230 offset:16384\n"
        "v_lshlrev_b32 v168, 3, v168\n"
        "v_mfma_f32_16x16x32_bf16 a[132:135], v[36:39], v[16:19], a[132:135]\n"
        "ds_read_b64_tr_b16 v[58:59], v231 offset:16384\n"
        "global_load_lds_dwordx4 v240, s[86:87] offset:2048\n"
        "v_mfma_f32_16x16x32_bf16 a[136:139], v[40:43], v[16:19], 0\n"
        "ds_read_b64_tr_b16 v[88:89], v230 offset:24576\n"
        "s_sub_u32 s74, s20, 2048\n"
        "s_subb_u32 s75, s21, 0\n"
        "v_mfma_f32_16x16x32_bf16 a[136:139], v[72:75], v[20:23], a[136:139]\n"
        "ds_read_b64_tr_b16 v[90:91], v231 offset:24576\n"
        "s_cmp_lt_u32 s43, 16\n"
        "s_cselect_b32 s90, s44, s45\n"
        "v_mfma_f32_16x16x32_bf16 a[168:171], v[72:75], v[28:31], 0\n"
        "ds_read_b64_tr_b16 v[60:61], v232 offset:16384\n"
        "s_lshl4_add_u32 s90, s90, s91\n"
        "s_lshl2_add_u32 s90, s90, s40\n"
        "v_mfma_f32_16x16x32_bf16 a[168:171], v[40:43], v[24:27], a[168:171]\n"
        "ds_read_b64_tr_b16 v[62:63], v233 offset:16384\n"
        "global_load_dwordx4 v[158:161], v254, s[30:31] offset:0\n"
        "s_lshl_b32 s90, s90, 12\n"
        "v_mfma_f32_16x16x32_bf16 a[172:175], v[44:47], v[24:27], 0\n"
        "ds_read_b64_tr_b16 v[92:93], v232 offset:24576\n"
        "s_add_u32 s92, s8, s90\n"
        "s_addc_u32 s93, s9, 0\n"
        "v_mfma_f32_16x16x32_bf16 a[172:175], v[76:79], v[28:31], a[172:175]\n"
        "ds_read_b64_tr_b16 v[94:95], v233 offset:24576\n"
        "s_lshl_b32 s91, s44, 5\n"
        "s_add_u32 s91, s91, s43\n"
        "v_mfma_f32_16x16x32_bf16 a[140:143], v[76:79], v[20:23], 0\n"
        "s_lshl2_add_u32 s91, s91, s40\n"
        "s_lshl_b32 s80, s91, 1\n"
        "s_add_u32 s81, s80, 1\n"
        "v_mfma_f32_16x16x32_bf16 a[140:143], v[44:47], v[16:19], a[140:143]\n"
        "s_waitcnt lgkmcnt(6)\n"
        "global_load_lds_dwordx4 v241, s[86:87] offset:3072\n"
        "s_add_u32 m0, s73, 0x24000\n"
        "v_mfma_f32_16x16x32_bf16 a[16:19], v[0:3], v[48:51], 0\n"
        "s_add_u32 s86, s62, 0x100000\n"
        "s_addc_u32 s87, s63, 0\n"
        "s_lshl_b32 s80, s80, 7\n"
        "v_mfma_f32_16x16x32_bf16 a[16:19], v[4:7], v[80:83], a[16:19]\n"
        "s_add_u32 s76, s38, 0x3301000\n"
        "s_addc_u32 s77, s39, 0\n"
        "s_lshl_b32 s81, s81, 7\n"
        "v_mfma_f32_16x16x32_bf16 a[48:51], v[12:15], v[80:83], 0\n"
        "v_mfma_f32_16x16x32_bf16 a[48:51], v[8:11], v[48:51], a[48:51]\n"
        "global_load_lds_dwordx4 v238, s[86:87] offset:0\n"
        "v_mfma_f32_16x16x32_bf16 a[52:55], v[8:11], v[52:55], 0\n"
        "v_mfma_f32_16x16x32_bf16 a[52:55], v[12:15], v[84:87], a[52:55]\n"
        "s_waitcnt lgkmcnt(0)\n"
        "v_mfma_f32_16x16x32_bf16 a[20:23], v[4:7], v[84:87], 0\n"
        "v_mfma_f32_16x16x32_bf16 a[20:23], v[0:3], v[52:55], a[20:23]\n"
        "global_load_dwordx4 v[162:165], v254, s[30:31] offset:1024\n"
        "v_mfma_f32_16x16x32_bf16 a[24:27], v[0:3], v[56:59], 0\n"
        "v_mfma_f32_16x16x32_bf16 a[24:27], v[4:7], v[88:91], a[24:27]\n"
        "v_mfma_f32_16x16x32_bf16 a[56:59], v[12:15], v[88:91], 0\n"
        "v_mfma_f32_16x16x32_bf16 a[56:59], v[8:11], v[56:59], a[56:59]\n"
        "global_load_lds_dwordx4 v239, s[86:87] offset:1024\n"
        "v_mfma_f32_16x16x32_bf16 a[60:63], v[8:11], v[60:63], 0\n"
        "v_mfma_f32_16x16x32_bf16 a[60:63], v[12:15], v[92:95], a[60:63]\n"
        "v_mfma_f32_16x16x32_bf16 a[28:31], v[4:7], v[92:95], 0\n"
        "v_mfma_f32_16x16x32_bf16 a[28:31], v[0:3], v[60:63], a[28:31]\n"
        "global_load_lds_dwordx4 v240, s[86:87] offset:2048\n"
        "v_mfma_f32_16x16x32_bf16 a[144:147], v[16:19], v[48:51], 0\n"
        "s_waitcnt vmcnt(27)\n"
        "v_mfma_f32_16x16x32_bf16 a[144:147], v[20:23], v[80:83], a[144:147]\n"
        "s_barrier\n"
        "v_mfma_f32_16x16x32_bf16 a[176:179], v[28:31], v[80:83], 0\n"
        "ds_read_b128 v[0:3], v224 offset:20480\n"
        "v_mfma_f32_16x16x32_bf16 a[176:179], v[24:27], v[48:51], a[176:179]\n"
        "global_load_ushort v172, v172, s[28:29]\n"
        "v_mfma_f32_16x16x32_bf16 a[180:183], v[24:27], v[52:55], 0\n"
        "global_load_lds_dwordx4 v241, s[86:87] offset:3072\n"
        "ds_read_b128 v[4:7], v225 offset:20480\n"
        "s_add_u32 m0, s72, 0x0\n"
        "v_mfma_f32_16x16x32_bf16 a[180:183], v[28:31], v[84:87], a[180:183]\n"
        "ds_read_b128 v[8:11], v224 offset:22528\n"
        "s_add_u32 s86, s56, 0x100\n"
        "s_addc_u32 s87, s57, 0\n"
        "v_mfma_f32_16x16x32_bf16 a[148:151], v[20:23], v[84:87], 0\n"
        "ds_read_b128 v[12:15], v225 offset:22528\n"
        "v_mfma_f32_16x16x32_bf16 a[148:151], v[16:19], v[52:55], a[148:151]\n"
        "v_mfma_f32_16x16x32_bf16 a[152:155], v[16:19], v[56:59], 0\n"
        "global_load_ushort v198, v198, s[26:27]\n"
        "v_mfma_f32_16x16x32_bf16 a[152:155], v[20:23], v[88:91], a[152:155]\n"
        "global_load_dword v253, v173, s[82:83] offset:0\n"
        "s_add_u32 s82, s12, 0x100000\n"
        "s_addc_u32 s83, s13, 0\n"
        "v_mfma_f32_16x16x32_bf16 a[184:187], v[28:31], v[88:91], 0\n"
        "global_load_dwordx4 v[134:137], v168, s[24:25] offset:0\n"
        "v_mfma_f32_16x16x32_bf16 a[184:187], v[24:27], v[56:59], a[184:187]\n"
        "v_mfma_f32_16x16x32_bf16 a[188:191], v[24:27], v[60:63], 0\n"
        "ds_read_b128 v[24:27], v224 offset:6144\n"
        "s_waitcnt lgkmcnt(3)\n"
        "v_mfma_f32_16x16x32_bf16 a[188:191], v[28:31], v[92:95], a[188:191]\n"
        "ds_read_b128 v[28:31], v225 offset:6144\n"
        "v_mfma_f32_16x16x32_bf16 a[156:159], v[20:23], v[92:95], 0\n"
        "ds_read_b128 v[20:23], v225 offset:4096\n"
        "s_waitcnt lgkmcnt(3)\n"
        "v_mfma_f32_16x16x32_bf16 a[156:159], v[16:19], v[60:63], a[156:159]\n"
        "ds_read_b128 v[16:19], v224 offset:4096\n"
        "v_mfma_f32_16x16x32_bf16 a[64:67], v[32:35], v[0:3], 0\n"
        "v_mfma_f32_16x16x32_bf16 a[64:67], v[64:67], v[4:7], a[64:67]\n"
        "v_mfma_f32_16x16x32_bf16 a[96:99], v[64:67], v[12:15], 0\n"
        "v_mfma_f32_16x16x32_bf16 a[96:99], v[32:35], v[8:11], a[96:99]\n"
        "v_mfma_f32_16x16x32_bf16 a[100:103], v[36:39], v[8:11], 0\n"
        "v_mfma_f32_16x16x32_bf16 a[100:103], v[68:71], v[12:15], a[100:103]\n"
        "v_mfma_f32_16x16x32_bf16 a[68:71], v[68:71], v[4:7], 0\n"
        "v_mfma_f32_16x16x32_bf16 a[68:71], v[36:39], v[0:3], a[68:71]\n"
        "global_load_dwordx4 v[138:141], v168, s[24:25] offset:16\n"
        "v_mfma_f32_16x16x32_bf16 a[72:75], v[40:43], v[0:3], 0\n"
        "v_mfma_f32_16x16x32_bf16 a[72:75], v[72:75], v[4:7], a[72:75]\n"
        "v_mfma_f32_16x16x32_bf16 a[104:107], v[72:75], v[12:15], 0\n"
        "v_mfma_f32_16x16x32_bf16 a[104:107], v[40:43], v[8:11], a[104:107]\n"
        "v_mfma_f32_16x16x32_bf16 a[108:111], v[44:47], v[8:11], 0\n"
        "v_mfma_f32_16x16x32_bf16 a[108:111], v[76:79], v[12:15], a[108:111]\n"
        "v_mfma_f32_16x16x32_bf16 a[76:79], v[76:79], v[4:7], 0\n"
        "v_mfma_f32_16x16x32_bf16 a[76:79], v[44:47], v[0:3], a[76:79]\n"
        "v_mfma_f32_16x16x32_bf16 a[80:83], v[0:3], v[48:51], 0\n"
        "v_mfma_f32_16x16x32_bf16 a[80:83], v[4:7], v[80:83], a[80:83]\n"
        "v_mfma_f32_16x16x32_bf16 a[112:115], v[12:15], v[80:83], 0\n"
        "v_mfma_f32_16x16x32_bf16 a[112:115], v[8:11], v[48:51], a[112:115]\n"
        "v_mfma_f32_16x16x32_bf16 a[116:119], v[8:11], v[52:55], 0\n"
        "v_mfma_f32_16x16x32_bf16 a[116:119], v[12:15], v[84:87], a[116:119]\n"
        "v_mfma_f32_16x16x32_bf16 a[84:87], v[4:7], v[84:87], 0\n"
        "v_mfma_f32_16x16x32_bf16 a[84:87], v[0:3], v[52:55], a[84:87]\n"
        "global_load_dwordx2 v[96:97], v170, s[74:75] offset:-4096\n"
        "v_mfma_f32_16x16x32_bf16 a[88:91], v[0:3], v[56:59], 0\n"
        "v_mfma_f32_16x16x32_bf16 a[88:91], v[4:7], v[88:91], a[88:91]\n"
        "v_mfma_f32_16x16x32_bf16 a[120:123], v[12:15], v[88:91], 0\n"
        "v_mfma_f32_16x16x32_bf16 a[120:123], v[8:11], v[56:59], a[120:123]\n"
        "s_waitcnt vmcnt(16) lgkmcnt(0)\n"
        "v_mfma_f32_16x16x32_bf16 a[124:127], v[8:11], v[60:63], 0\n"
        "s_barrier\n"
        "v_mfma_f32_16x16x32_bf16 a[124:127], v[12:15], v[92:95], a[124:127]\n"
        "global_load_lds_dwordx4 v234, s[86:87] offset:0\n"
        "ds_read_b128 v[8:11], v224 offset:51200\n"
        "v_mfma_f32_16x16x32_bf16 a[92:95], v[4:7], v[92:95], 0\n"
        "ds_read_b128 v[4:7], v225 offset:49152\n"
        "v_mfma_f32_16x16x32_bf16 a[92:95], v[0:3], v[60:63], a[92:95]\n"
        "ds_read_b128 v[0:3], v224 offset:49152\n"
        "v_mfma_f32_16x16x32_bf16 a[192:195], v[32:35], v[16:19], 0\n"
        "ds_read_b128 v[12:15], v225 offset:51200\n"
        "v_mfma_f32_16x16x32_bf16 a[192:195], v[64:67], v[20:23], a[192:195]\n"
        "global_load_lds_dwordx4 v235, s[86:87] offset:1024\n"
        "v_mfma_f32_16x16x32_bf16 a[224:227], v[64:67], v[28:31], 0\n"
        "ds_read_b64_tr_b16 v[64:65], v226 offset:40960\n"
        "v_mfma_f32_16x16x32_bf16 a[224:227], v[32:35], v[24:27], a[224:227]\n"
        "ds_read_b64_tr_b16 v[32:33], v226 offset:32768\n"
        "v_mfma_f32_16x16x32_bf16 a[228:231], v[36:39], v[24:27], 0\n"
        "ds_read_b64_tr_b16 v[34:35], v227 offset:32768\n"
        "v_mfma_f32_16x16x32_bf16 a[228:231], v[68:71], v[28:31], a[228:231]\n"
        "ds_read_b64_tr_b16 v[66:67], v227 offset:40960\n"
        "global_load_lds_dwordx4 v236, s[86:87] offset:2048\n"
        "v_mfma_f32_16x16x32_bf16 a[196:199], v[68:71], v[20:23], 0\n"
        "ds_read_b64_tr_b16 v[68:69], v228 offset:40960\n"
        "v_mfma_f32_16x16x32_bf16 a[196:199], v[36:39], v[16:19], a[196:199]\n"
        "ds_read_b64_tr_b16 v[36:37], v228 offset:32768\n"
        "v_mfma_f32_16x16x32_bf16 a[200:203], v[40:43], v[16:19], 0\n"
        "ds_read_b64_tr_b16 v[38:39], v229 offset:32768\n"
        "v_mfma_f32_16x16x32_bf16 a[200:203], v[72:75], v[20:23], a[200:203]\n"
        "ds_read_b64_tr_b16 v[70:71], v229 offset:40960\n"
        "global_load_lds_dwordx4 v237, s[86:87] offset:3072\n"
        "s_add_u32 m0, s72, 0x1000\n"
        "v_mfma_f32_16x16x32_bf16 a[232:235], v[72:75], v[28:31], 0\n"
        "ds_read_b64_tr_b16 v[72:73], v230 offset:40960\n"
        "s_add_u32 s86, s58, 0x100\n"
        "s_addc_u32 s87, s59, 0\n"
        "v_mfma_f32_16x16x32_bf16 a[232:235], v[40:43], v[24:27], a[232:235]\n"
        "ds_read_b64_tr_b16 v[40:41], v230 offset:32768\n"
        "v_mfma_f32_16x16x32_bf16 a[236:239], v[44:47], v[24:27], 0\n"
        "ds_read_b64_tr_b16 v[42:43], v231 offset:32768\n"
        "v_mfma_f32_16x16x32_bf16 a[236:239], v[76:79], v[28:31], a[236:239]\n"
        "ds_read_b64_tr_b16 v[74:75], v231 offset:40960\n"
        "global_load_dwordx2 v[98:99], v170, s[74:75] offset:-2048\n"
        "v_mfma_f32_16x16x32_bf16 a[204:207], v[76:79], v[20:23], 0\n"
        "ds_read_b64_tr_b16 v[76:77], v232 offset:40960\n"
        "global_load_lds_dwordx4 v234, s[86:87] offset:0\n"
        "v_mfma_f32_16x16x32_bf16 a[204:207], v[44:47], v[16:19], a[204:207]\n"
        "ds_read_b64_tr_b16 v[44:45], v232 offset:32768\n"
        "v_mfma_f32_16x16x32_bf16 a[208:211], v[16:19], v[48:51], 0\n"
        "ds_read_b64_tr_b16 v[46:47], v233 offset:32768\n"
        "v_mfma_f32_16x16x32_bf16 a[208:211], v[20:23], v[80:83], a[208:211]\n"
        "ds_read_b64_tr_b16 v[78:79], v233 offset:40960\n"
        "v_mfma_f32_16x16x32_bf16 a[240:243], v[28:31], v[80:83], 0\n"
        "ds_read_b64_tr_b16 v[80:81], v226 offset:57344\n"
        "global_load_lds_dwordx4 v235, s[86:87] offset:1024\n"
        "v_mfma_f32_16x16x32_bf16 a[240:243], v[24:27], v[48:51], a[240:243]\n"
        "ds_read_b64_tr_b16 v[48:49], v226 offset:49152\n"
        "v_mfma_f32_16x16x32_bf16 a[244:247], v[24:27], v[52:55], 0\n"
        "ds_read_b64_tr_b16 v[50:51], v227 offset:49152\n"
        "v_mfma_f32_16x16x32_bf16 a[244:247], v[28:31], v[84:87], a[244:247]\n"
        "ds_read_b64_tr_b16 v[82:83], v227 offset:57344\n"
        "v_mfma_f32_16x16x32_bf16 a[212:215], v[20:23], v[84:87], 0\n"
        "ds_read_b64_tr_b16 v[84:85], v228 offset:57344\n"
        "global_load_lds_dwordx4 v236, s[86:87] offset:2048\n"
        "v_mfma_f32_16x16x32_bf16 a[212:215], v[16:19], v[52:55], a[212:215]\n"
        "ds_read_b64_tr_b16 v[52:53], v228 offset:49152\n"
        "v_mfma_f32_16x16x32_bf16 a[216:219], v[16:19], v[56:59], 0\n"
        "ds_read_b64_tr_b16 v[54:55], v229 offset:49152\n"
        "v_mfma_f32_16x16x32_bf16 a[216:219], v[20:23], v[88:91], a[216:219]\n"
        "ds_read_b64_tr_b16 v[86:87], v229 offset:57344\n"
        "v_mfma_f32_16x16x32_bf16 a[248:251], v[28:31], v[88:91], 0\n"
        "ds_read_b64_tr_b16 v[88:89], v230 offset:57344\n"
        "global_load_lds_dwordx4 v237, s[86:87] offset:3072\n"
        "s_add_u32 m0, s73, 0x10000\n"
        "v_mfma_f32_16x16x32_bf16 a[248:251], v[24:27], v[56:59], a[248:251]\n"
        "ds_read_b64_tr_b16 v[56:57], v230 offset:49152\n"
        "s_add_u32 s86, s60, 0x180000\n"
        "s_addc_u32 s87, s61, 0\n"
        "v_mfma_f32_16x16x32_bf16 a[252:255], v[24:27], v[60:63], 0\n"
        "ds_read_b64_tr_b16 v[58:59], v231 offset:49152\n"
        "s_waitcnt lgkmcnt(8)\n"
        "v_mfma_f32_16x16x32_bf16 a[252:255], v[28:31], v[92:95], a[252:255]\n"
        "ds_read_b64_tr_b16 v[90:91], v231 offset:57344\n"
        "v_mfma_f32_16x16x32_bf16 a[220:223], v[20:23], v[92:95], 0\n"
        "ds_read_b64_tr_b16 v[92:93], v232 offset:57344\n"
        "global_load_dwordx2 v[100:101], v170, s[74:75] offset:0\n"
        "v_mfma_f32_16x16x32_bf16 a[220:223], v[16:19], v[60:63], a[220:223]\n"
        "ds_read_b64_tr_b16 v[60:61], v232 offset:49152\n"
        "global_load_lds_dwordx4 v238, s[86:87] offset:0\n"
        "v_mfma_f32_16x16x32_bf16 a[0:3], v[32:35], v[0:3], a[0:3]\n"
        "ds_read_b64_tr_b16 v[62:63], v233 offset:49152\n"
        "v_mfma_f32_16x16x32_bf16 a[0:3], v[64:67], v[4:7], a[0:3]\n"
        "ds_read_b64_tr_b16 v[94:95], v233 offset:57344\n"
        "v_mfma_f32_16x16x32_bf16 a[32:35], v[64:67], v[12:15], a[32:35]\n"
        "ds_read_b128 v[16:19], v224 offset:53248\n"
        "v_mfma_f32_16x16x32_bf16 a[32:35], v[32:35], v[8:11], a[32:35]\n"
        "ds_read_b128 v[20:23], v225 offset:53248\n"
        "global_load_lds_dwordx4 v239, s[86:87] offset:1024\n"
        "v_mfma_f32_16x16x32_bf16 a[36:39], v[36:39], v[8:11], a[36:39]\n"
        "ds_read_b128 v[24:27], v224 offset:55296\n"
        "v_mfma_f32_16x16x32_bf16 a[36:39], v[68:71], v[12:15], a[36:39]\n"
        "ds_read_b128 v[28:31], v225 offset:55296\n"
        "v_mfma_f32_16x16x32_bf16 a[4:7], v[68:71], v[4:7], a[4:7]\n"
        "v_mfma_f32_16x16x32_bf16 a[4:7], v[36:39], v[0:3], a[4:7]\n"
        "global_load_lds_dwordx4 v240, s[86:87] offset:2048\n"
        "v_mfma_f32_16x16x32_bf16 a[8:11], v[40:43], v[0:3], a[8:11]\n"
        "v_mfma_f32_16x16x32_bf16 a[8:11], v[72:75], v[4:7], a[8:11]\n"
        "v_mfma_f32_16x16x32_bf16 a[40:43], v[72:75], v[12:15], a[40:43]\n"
        "v_mfma_f32_16x16x32_bf16 a[40:43], v[40:43], v[8:11], a[40:43]\n"
        "global_load_lds_dwordx4 v241, s[86:87] offset:3072\n"
        "s_add_u32 m0, s73, 0x14000\n"
        "v_mfma_f32_16x16x32_bf16 a[44:47], v[44:47], v[8:11], a[44:47]\n"
        "s_add_u32 s86, s62, 0x180000\n"
        "s_addc_u32 s87, s63, 0\n"
        "v_mfma_f32_16x16x32_bf16 a[44:47], v[76:79], v[12:15], a[44:47]\n"
        "s_waitcnt lgkmcnt(0)\n"
        "v_mfma_f32_16x16x32_bf16 a[12:15], v[76:79], v[4:7], a[12:15]\n"
        "v_mfma_f32_16x16x32_bf16 a[12:15], v[44:47], v[0:3], a[12:15]\n"
        "global_load_dwordx2 v[102:103], v169, s[74:75] offset:2048\n"
        "v_mfma_f32_16x16x32_bf16 a[16:19], v[0:3], v[48:51], a[16:19]\n"
        "global_load_lds_dwordx4 v238, s[86:87] offset:0\n"
        "s_add_u32 s74, s20, 0x1800\n"
        "s_addc_u32 s75, s21, 0\n"
        "v_mfma_f32_16x16x32_bf16 a[16:19], v[4:7], v[80:83], a[16:19]\n"
        "v_mfma_f32_16x16x32_bf16 a[48:51], v[12:15], v[80:83], a[48:51]\n"
        "v_mfma_f32_16x16x32_bf16 a[48:51], v[8:11], v[48:51], a[48:51]\n"
        "v_mfma_f32_16x16x32_bf16 a[52:55], v[8:11], v[52:55], a[52:55]\n"
        "global_load_lds_dwordx4 v239, s[86:87] offset:1024\n"
        "v_mfma_f32_16x16x32_bf16 a[52:55], v[12:15], v[84:87], a[52:55]\n"
        "v_mfma_f32_16x16x32_bf16 a[20:23], v[4:7], v[84:87], a[20:23]\n"
        "v_mfma_f32_16x16x32_bf16 a[20:23], v[0:3], v[52:55], a[20:23]\n"
        "v_mfma_f32_16x16x32_bf16 a[24:27], v[0:3], v[56:59], a[24:27]\n"
        "global_load_lds_dwordx4 v240, s[86:87] offset:2048\n"
        "v_mfma_f32_16x16x32_bf16 a[24:27], v[4:7], v[88:91], a[24:27]\n"
        "v_mfma_f32_16x16x32_bf16 a[56:59], v[12:15], v[88:91], a[56:59]\n"
        "v_mfma_f32_16x16x32_bf16 a[56:59], v[8:11], v[56:59], a[56:59]\n"
        "v_mfma_f32_16x16x32_bf16 a[60:63], v[8:11], v[60:63], a[60:63]\n"
        "global_load_lds_dwordx4 v241, s[86:87] offset:3072\n"
        "ds_read_b128 v[8:11], v224 offset:34816\n"
        "s_add_u32 m0, s72, 0x8000\n"
        "v_mfma_f32_16x16x32_bf16 a[60:63], v[12:15], v[92:95], a[60:63]\n"
        "ds_read_b128 v[12:15], v225 offset:34816\n"
        "s_add_u32 s86, s56, 0x180\n"
        "s_addc_u32 s87, s57, 0\n"
        "v_mfma_f32_16x16x32_bf16 a[28:31], v[4:7], v[92:95], a[28:31]\n"
        "ds_read_b128 v[4:7], v225 offset:32768\n"
        "v_mfma_f32_16x16x32_bf16 a[28:31], v[0:3], v[60:63], a[28:31]\n"
        "ds_read_b128 v[0:3], v224 offset:32768\n"
        "v_mfma_f32_16x16x32_bf16 a[64:67], v[32:35], v[16:19], a[64:67]\n"
        "global_load_dwordx2 v[104:105], v169, s[74:75] offset:-4096\n"
        "v_mfma_f32_16x16x32_bf16 a[64:67], v[64:67], v[20:23], a[64:67]\n"
        "v_mfma_f32_16x16x32_bf16 a[96:99], v[64:67], v[28:31], a[96:99]\n"
        "LHA0_%=:\n"
        "s_cbranch_execz LHA4_%=\n"
        "v_mfma_f32_16x16x32_bf16 a[96:99], v[32:35], v[24:27], a[96:99]\n"
        "v_mfma_f32_16x16x32_bf16 a[100:103], v[36:39], v[24:27], a[100:103]\n"
        "v_mfma_f32_16x16x32_bf16 a[100:103], v[68:71], v[28:31], a[100:103]\n"
        "v_mfma_f32_16x16x32_bf16 a[68:71], v[68:71], v[20:23], a[68:71]\n"
        "v_mfma_f32_16x16x32_bf16 a[68:71], v[36:39], v[16:19], a[68:71]\n"
        "global_load_dwordx2 v[106:107], v169, s[74:75] offset:-2048\n"
        "v_mfma_f32_16x16x32_bf16 a[72:75], v[40:43], v[16:19], a[72:75]\n"
        "v_mfma_f32_16x16x32_bf16 a[72:75], v[72:75], v[20:23], a[72:75]\n"
        "v_mfma_f32_16x16x32_bf16 a[104:107], v[72:75], v[28:31], a[104:107]\n"
        "v_mfma_f32_16x16x32_bf16 a[104:107], v[40:43], v[24:27], a[104:107]\n"
        "v_mfma_f32_16x16x32_bf16 a[108:111], v[44:47], v[24:27], a[108:111]\n"
        "v_mfma_f32_16x16x32_bf16 a[108:111], v[76:79], v[28:31], a[108:111]\n"
        "v_mfma_f32_16x16x32_bf16 a[76:79], v[76:79], v[20:23], a[76:79]\n"
        "v_mfma_f32_16x16x32_bf16 a[76:79], v[44:47], v[16:19], a[76:79]\n"
        "global_load_dword v253, v173, s[82:83] offset:0\n"
        "s_add_u32 s82, s12, 0x200000\n"
        "s_addc_u32 s83, s13, 0\n"
        "v_mfma_f32_16x16x32_bf16 a[80:83], v[16:19], v[48:51], a[80:83]\n"
        "v_mfma_f32_16x16x32_bf16 a[80:83], v[20:23], v[80:83], a[80:83]\n"
        "v_mfma_f32_16x16x32_bf16 a[112:115], v[28:31], v[80:83], a[112:115]\n"
        "v_mfma_f32_16x16x32_bf16 a[112:115], v[24:27], v[48:51], a[112:115]\n"
        "v_mfma_f32_16x16x32_bf16 a[116:119], v[24:27], v[52:55], a[116:119]\n"
        "v_mfma_f32_16x16x32_bf16 a[116:119], v[28:31], v[84:87], a[116:119]\n"
        "s_waitcnt vmcnt(26) lgkmcnt(0)\n"
        "v_lshl_or_b32 v172, v172, 16, v198\n"
        "v_mfma_f32_16x16x32_bf16 a[84:87], v[20:23], v[84:87], a[84:87]\n"
        "v_readfirstlane_b32 s54, v172\n"
        "v_mfma_f32_16x16x32_bf16 a[84:87], v[16:19], v[52:55], a[84:87]\n"
        "global_load_dwordx2 v[108:109], v169, s[74:75] offset:0\n"
        "s_waitcnt vmcnt(25)\n"
        "v_mfma_f32_16x16x32_bf16 a[88:91], v[16:19], v[56:59], a[88:91]\n"
        "v_lshlrev_b32 v185, 16, v134\n"
        "v_and_b32 v186, s35, v134\n"
        "v_mfma_f32_16x16x32_bf16 a[88:91], v[20:23], v[88:91], a[88:91]\n"
        "v_lshlrev_b32 v187, 16, v135\n"
        "v_and_b32 v188, s35, v135\n"
        "v_mfma_f32_16x16x32_bf16 a[120:123], v[28:31], v[88:91], a[120:123]\n"
        "v_mfma_f32_16x16x32_bf16 a[120:123], v[24:27], v[56:59], a[120:123]\n"
        "v_mfma_f32_16x16x32_bf16 a[124:127], v[24:27], v[60:63], a[124:127]\n"
        "ds_read_b128 v[24:27], v224 offset:38912\n"
        "v_mfma_f32_16x16x32_bf16 a[124:127], v[28:31], v[92:95], a[124:127]\n"
        "ds_read_b128 v[28:31], v225 offset:38912\n"
        "v_mfma_f32_16x16x32_bf16 a[92:95], v[20:23], v[92:95], a[92:95]\n"
        "ds_read_b128 v[20:23], v225 offset:36864\n"
        "v_mfma_f32_16x16x32_bf16 a[92:95], v[16:19], v[60:63], a[92:95]\n"
        "ds_read_b128 v[16:19], v224 offset:36864\n"
        "v_mfma_f32_16x16x32_bf16 a[128:131], v[32:35], v[0:3], a[128:131]\n"
        "v_mfma_f32_16x16x32_bf16 a[128:131], v[64:67], v[4:7], a[128:131]\n"
        "v_mfma_f32_16x16x32_bf16 a[160:163], v[64:67], v[12:15], a[160:163]\n"
        "v_mfma_f32_16x16x32_bf16 a[160:163], v[32:35], v[8:11], a[160:163]\n"
        "v_mfma_f32_16x16x32_bf16 a[164:167], v[36:39], v[8:11], a[164:167]\n"
        "v_mfma_f32_16x16x32_bf16 a[164:167], v[68:71], v[12:15], a[164:167]\n"
        "v_mfma_f32_16x16x32_bf16 a[132:135], v[68:71], v[4:7], a[132:135]\n"
        "v_mfma_f32_16x16x32_bf16 a[132:135], v[36:39], v[0:3], a[132:135]\n"
        "global_load_dwordx2 v[110:111], v169, s[74:75] offset:2048\n"
        "s_add_u32 s74, s20, 0x3800\n"
        "s_addc_u32 s75, s21, 0\n"
        "v_mfma_f32_16x16x32_bf16 a[136:139], v[40:43], v[0:3], a[136:139]\n"
        "v_mfma_f32_16x16x32_bf16 a[136:139], v[72:75], v[4:7], a[136:139]\n"
        "v_mfma_f32_16x16x32_bf16 a[168:171], v[72:75], v[12:15], a[168:171]\n"
        "v_mfma_f32_16x16x32_bf16 a[168:171], v[40:43], v[8:11], a[168:171]\n"
        "v_mfma_f32_16x16x32_bf16 a[172:175], v[44:47], v[8:11], a[172:175]\n"
        "v_mfma_f32_16x16x32_bf16 a[172:175], v[76:79], v[12:15], a[172:175]\n"
        "v_mfma_f32_16x16x32_bf16 a[140:143], v[76:79], v[4:7], a[140:143]\n"
        "v_mfma_f32_16x16x32_bf16 a[140:143], v[44:47], v[0:3], a[140:143]\n"
        "v_mfma_f32_16x16x32_bf16 a[144:147], v[0:3], v[48:51], a[144:147]\n"
        "v_mfma_f32_16x16x32_bf16 a[144:147], v[4:7], v[80:83], a[144:147]\n"
        "v_mfma_f32_16x16x32_bf16 a[176:179], v[12:15], v[80:83], a[176:179]\n"
        "v_mfma_f32_16x16x32_bf16 a[176:179], v[8:11], v[48:51], a[176:179]\n"
        "v_mfma_f32_16x16x32_bf16 a[180:183], v[8:11], v[52:55], a[180:183]\n"
        "s_waitcnt vmcnt(24) lgkmcnt(0)\n"
        "v_cndmask_b32 v96, v96, 0, s[78:79]\n"
        "v_cndmask_b32 v97, v97, 0, s[78:79]\n"
        "v_mfma_f32_16x16x32_bf16 a[180:183], v[12:15], v[84:87], a[180:183]\n"
        "v_lshlrev_b32 v174, 16, v96\n"
        "v_mul_f32 v189, v174, v188\n"
        "v_mfma_f32_16x16x32_bf16 a[148:151], v[4:7], v[84:87], a[148:151]\n"
        "v_cvt_pk_bf16_f32 v189, 0, v189\n"
        "v_and_b32 v174, s35, v96\n"
        "v_mfma_f32_16x16x32_bf16 a[148:151], v[0:3], v[52:55], a[148:151]\n"
        "global_load_dwordx2 v[112:113], v169, s[74:75] offset:-4096\n"
        "v_mfma_f32_16x16x32_bf16 a[152:155], v[0:3], v[56:59], a[152:155]\n"
        "v_mfma_f32_16x16x32_bf16 a[152:155], v[4:7], v[88:91], a[152:155]\n"
        "v_mfma_f32_16x16x32_bf16 a[184:187], v[12:15], v[88:91], a[184:187]\n"
        "v_mfma_f32_16x16x32_bf16 a[184:187], v[8:11], v[56:59], a[184:187]\n"
        "s_waitcnt vmcnt(16)\n"
        "v_mfma_f32_16x16x32_bf16 a[188:191], v[8:11], v[60:63], a[188:191]\n"
        "s_barrier\n"
        "v_mfma_f32_16x16x32_bf16 a[188:191], v[12:15], v[92:95], a[188:191]\n"
        "global_load_lds_dwordx4 v234, s[86:87] offset:0\n"
        "ds_read_b128 v[8:11], v224 offset:18432\n"
        "v_mfma_f32_16x16x32_bf16 a[156:159], v[4:7], v[92:95], a[156:159]\n"
        "ds_read_b128 v[4:7], v225 offset:16384\n"
        "v_mfma_f32_16x16x32_bf16 a[156:159], v[0:3], v[60:63], a[156:159]\n"
        "ds_read_b128 v[0:3], v224 offset:16384\n"
        "v_mfma_f32_16x16x32_bf16 a[192:195], v[32:35], v[16:19], a[192:195]\n"
        "ds_read_b128 v[12:15], v225 offset:18432\n"
        "v_mfma_f32_16x16x32_bf16 a[192:195], v[64:67], v[20:23], a[192:195]\n"
        "global_load_lds_dwordx4 v235, s[86:87] offset:1024\n"
        "v_mfma_f32_16x16x32_bf16 a[224:227], v[64:67], v[28:31], a[224:227]\n"
        "ds_read_b64_tr_b16 v[64:65], v216 offset:8192\n"
        "v_mfma_f32_16x16x32_bf16 a[224:227], v[32:35], v[24:27], a[224:227]\n"
        "ds_read_b64_tr_b16 v[32:33], v216 offset:0\n"
        "v_mfma_f32_16x16x32_bf16 a[228:231], v[36:39], v[24:27], a[228:231]\n"
        "ds_read_b64_tr_b16 v[34:35], v217 offset:0\n"
        "v_mfma_f32_16x16x32_bf16 a[228:231], v[68:71], v[28:31], a[228:231]\n"
        "ds_read_b64_tr_b16 v[66:67], v217 offset:8192\n"
        "global_load_lds_dwordx4 v236, s[86:87] offset:2048\n"
        "v_mfma_f32_16x16x32_bf16 a[196:199], v[68:71], v[20:23], a[196:199]\n"
        "ds_read_b64_tr_b16 v[68:69], v218 offset:8192\n"
        "v_mfma_f32_16x16x32_bf16 a[196:199], v[36:39], v[16:19], a[196:199]\n"
        "ds_read_b64_tr_b16 v[36:37], v218 offset:0\n"
        "v_mfma_f32_16x16x32_bf16 a[200:203], v[40:43], v[16:19], a[200:203]\n"
        "ds_read_b64_tr_b16 v[38:39], v219 offset:0\n"
        "v_mfma_f32_16x16x32_bf16 a[200:203], v[72:75], v[20:23], a[200:203]\n"
        "ds_read_b64_tr_b16 v[70:71], v219 offset:8192\n"
        "global_load_lds_dwordx4 v237, s[86:87] offset:3072\n"
        "s_add_u32 m0, s72, 0x9000\n"
        "v_mfma_f32_16x16x32_bf16 a[232:235], v[72:75], v[28:31], a[232:235]\n"
        "ds_read_b64_tr_b16 v[72:73], v220 offset:8192\n"
        "s_add_u32 s86, s58, 0x180\n"
        "s_addc_u32 s87, s59, 0\n"
        "v_mfma_f32_16x16x32_bf16 a[232:235], v[40:43], v[24:27], a[232:235]\n"
        "ds_read_b64_tr_b16 v[40:41], v220 offset:0\n"
        "v_cndmask_b32 v98, v98, 0, s[78:79]\n"
        "v_mfma_f32_16x16x32_bf16 a[236:239], v[44:47], v[24:27], a[236:239]\n"
        "ds_read_b64_tr_b16 v[42:43], v221 offset:0\n"
        "v_cndmask_b32 v99, v99, 0, s[78:79]\n"
        "v_mfma_f32_16x16x32_bf16 a[236:239], v[76:79], v[28:31], a[236:239]\n"
        "ds_read_b64_tr_b16 v[74:75], v221 offset:8192\n"
        "global_load_dwordx2 v[114:115], v169, s[74:75] offset:-2048\n"
        "v_mfma_f32_16x16x32_bf16 a[204:207], v[76:79], v[20:23], a[204:207]\n"
        "ds_read_b64_tr_b16 v[76:77], v222 offset:8192\n"
        "global_load_lds_dwordx4 v234, s[86:87] offset:0\n"
        "v_mfma_f32_16x16x32_bf16 a[204:207], v[44:47], v[16:19], a[204:207]\n"
        "ds_read_b64_tr_b16 v[44:45], v222 offset:0\n"
        "v_lshlrev_b32 v175, 16, v98\n"
        "v_mfma_f32_16x16x32_bf16 a[208:211], v[16:19], v[48:51], a[208:211]\n"
        "ds_read_b64_tr_b16 v[46:47], v223 offset:0\n"
        "v_mul_f32 v191, v175, v188\n"
        "v_mfma_f32_16x16x32_bf16 a[208:211], v[20:23], v[80:83], a[208:211]\n"
        "ds_read_b64_tr_b16 v[78:79], v223 offset:8192\n"
        "v_cvt_pk_bf16_f32 v191, 0, v191\n"
        "v_mfma_f32_16x16x32_bf16 a[240:243], v[28:31], v[80:83], a[240:243]\n"
        "ds_read_b64_tr_b16 v[80:81], v216 offset:24576\n"
        "global_load_lds_dwordx4 v235, s[86:87] offset:1024\n"
        "v_mfma_f32_16x16x32_bf16 a[240:243], v[24:27], v[48:51], a[240:243]\n"
        "ds_read_b64_tr_b16 v[48:49], v216 offset:16384\n"
        "v_mul_f32 v190, v175, v187\n"
        "v_mfma_f32_16x16x32_bf16 a[244:247], v[24:27], v[52:55], a[244:247]\n"
        "ds_read_b64_tr_b16 v[50:51], v217 offset:16384\n"
        "v_cvt_pk_bf16_f32 v190, 0, v190\n"
        "v_mfma_f32_16x16x32_bf16 a[244:247], v[28:31], v[84:87], a[244:247]\n"
        "ds_read_b64_tr_b16 v[82:83], v217 offset:24576\n"
        "v_add_f32 v189, v189, v190\n"
        "v_mfma_f32_16x16x32_bf16 a[212:215], v[20:23], v[84:87], a[212:215]\n"
        "ds_read_b64_tr_b16 v[84:85], v218 offset:24576\n"
        "global_load_lds_dwordx4 v236, s[86:87] offset:2048\n"
        "v_mfma_f32_16x16x32_bf16 a[212:215], v[16:19], v[52:55], a[212:215]\n"
        "ds_read_b64_tr_b16 v[52:53], v218 offset:16384\n"
        "v_cvt_pk_bf16_f32 v189, 0, v189\n"
        "v_mfma_f32_16x16x32_bf16 a[216:219], v[16:19], v[56:59], a[216:219]\n"
        "ds_read_b64_tr_b16 v[54:55], v219 offset:16384\n"
        "v_and_b32 v175, s35, v98\n"
        "v_mfma_f32_16x16x32_bf16 a[216:219], v[20:23], v[88:91], a[216:219]\n"
        "ds_read_b64_tr_b16 v[86:87], v219 offset:24576\n"
        "v_mfma_f32_16x16x32_bf16 a[248:251], v[28:31], v[88:91], a[248:251]\n"
        "ds_read_b64_tr_b16 v[88:89], v220 offset:24576\n"
        "global_load_lds_dwordx4 v237, s[86:87] offset:3072\n"
        "s_add_u32 m0, s73, 0x18000\n"
        "v_mfma_f32_16x16x32_bf16 a[248:251], v[24:27], v[56:59], a[248:251]\n"
        "ds_read_b64_tr_b16 v[56:57], v220 offset:16384\n"
        "s_add_u32 s86, s60, 0x200000\n"
        "s_addc_u32 s87, s61, 0\n"
        "v_mfma_f32_16x16x32_bf16 a[252:255], v[24:27], v[60:63], a[252:255]\n"
        "ds_read_b64_tr_b16 v[58:59], v221 offset:16384\n"
        "s_waitcnt lgkmcnt(8)\n"
        "v_mfma_f32_16x16x32_bf16 a[252:255], v[28:31], v[92:95], a[252:255]\n"
        "ds_read_b64_tr_b16 v[90:91], v221 offset:24576\n"
        "s_waitcnt vmcnt(24) lgkmcnt(8)\n"
        "v_mfma_f32_16x16x32_bf16 a[220:223], v[20:23], v[92:95], a[220:223]\n"
        "ds_read_b64_tr_b16 v[92:93], v222 offset:24576\n"
        "global_load_dwordx2 v[116:117], v169, s[74:75] offset:0\n"
        "v_mfma_f32_16x16x32_bf16 a[220:223], v[16:19], v[60:63], a[220:223]\n"
        "ds_read_b64_tr_b16 v[60:61], v222 offset:16384\n"
        "global_load_lds_dwordx4 v238, s[86:87] offset:0\n"
        "v_mfma_f32_16x16x32_bf16 a[0:3], v[32:35], v[0:3], a[0:3]\n"
        "ds_read_b64_tr_b16 v[62:63], v223 offset:16384\n"
        "s_add_u32 s74, s22, 0x1000\n"
        "s_addc_u32 s75, s23, 0\n"
        "v_mfma_f32_16x16x32_bf16 a[0:3], v[64:67], v[4:7], a[0:3]\n"
        "ds_read_b64_tr_b16 v[94:95], v223 offset:24576\n"
        "v_cndmask_b32 v100, v100, 0, s[78:79]\n"
        "v_mfma_f32_16x16x32_bf16 a[32:35], v[64:67], v[12:15], a[32:35]\n"
        "ds_read_b128 v[16:19], v224 offset:20480\n"
        "v_cndmask_b32 v101, v101, 0, s[78:79]\n"
        "v_mfma_f32_16x16x32_bf16 a[32:35], v[32:35], v[8:11], a[32:35]\n"
        "ds_read_b128 v[20:23], v225 offset:20480\n"
        "global_load_lds_dwordx4 v239, s[86:87] offset:1024\n"
        "v_mfma_f32_16x16x32_bf16 a[36:39], v[36:39], v[8:11], a[36:39]\n"
        "ds_read_b128 v[24:27], v224 offset:22528\n"
        "v_lshlrev_b32 v176, 16, v100\n"
        "v_mfma_f32_16x16x32_bf16 a[36:39], v[68:71], v[12:15], a[36:39]\n"
        "ds_read_b128 v[28:31], v225 offset:22528\n"
        "v_mul_f32 v192, v176, v187\n"
        "v_mfma_f32_16x16x32_bf16 a[4:7], v[68:71], v[4:7], a[4:7]\n"
        "v_cvt_pk_bf16_f32 v192, 0, v192\n"
        "v_add_f32 v191, v191, v192\n"
        "v_mfma_f32_16x16x32_bf16 a[4:7], v[36:39], v[0:3], a[4:7]\n"
        "global_load_lds_dwordx4 v240, s[86:87] offset:2048\n"
        "v_mfma_f32_16x16x32_bf16 a[8:11], v[40:43], v[0:3], a[8:11]\n"
        "v_cvt_pk_bf16_f32 v191, 0, v191\n"
        "v_mul_f32 v190, v176, v186\n"
        "v_mfma_f32_16x16x32_bf16 a[8:11], v[72:75], v[4:7], a[8:11]\n"
        "v_cvt_pk_bf16_f32 v190, 0, v190\n"
        "v_add_f32 v189, v189, v190\n"
        "v_mfma_f32_16x16x32_bf16 a[40:43], v[72:75], v[12:15], a[40:43]\n"
        "v_cvt_pk_bf16_f32 v189, 0, v189\n"
        "v_mul_f32 v193, v176, v188\n"
        "v_mfma_f32_16x16x32_bf16 a[40:43], v[40:43], v[8:11], a[40:43]\n"
        "global_load_lds_dwordx4 v241, s[86:87] offset:3072\n"
        "s_add_u32 m0, s73, 0x1c000\n"
        "v_mfma_f32_16x16x32_bf16 a[44:47], v[44:47], v[8:11], a[44:47]\n"
        "s_add_u32 s86, s62, 0x200000\n"
        "s_addc_u32 s87, s63, 0\n"
        "v_cvt_pk_bf16_f32 v193, 0, v193\n"
        "v_mfma_f32_16x16x32_bf16 a[44:47], v[76:79], v[12:15], a[44:47]\n"
        "s_waitcnt vmcnt(24) lgkmcnt(0)\n"
        "v_lshlrev_b32 v177, 16, v102\n"
        "v_mul_f32 v192, v177, v186\n"
        "v_mfma_f32_16x16x32_bf16 a[12:15], v[76:79], v[4:7], a[12:15]\n"
        "v_cvt_pk_bf16_f32 v192, 0, v192\n"
        "v_add_f32 v191, v191, v192\n"
        "v_mfma_f32_16x16x32_bf16 a[12:15], v[44:47], v[0:3], a[12:15]\n"
        "global_load_dwordx2 v[118:119], v169, s[74:75] offset:-4096\n"
        "v_mfma_f32_16x16x32_bf16 a[16:19], v[0:3], v[48:51], a[16:19]\n"
        "global_load_lds_dwordx4 v238, s[86:87] offset:0\n"
        "v_cvt_pk_bf16_f32 v191, 0, v191\n"
        "v_mfma_f32_16x16x32_bf16 a[16:19], v[4:7], v[80:83], a[16:19]\n"
        "v_mul_f32 v190, v177, v185\n"
        "v_cvt_pk_bf16_f32 v190, 0, v190\n"
        "v_mfma_f32_16x16x32_bf16 a[48:51], v[12:15], v[80:83], a[48:51]\n"
        "v_add_f32 v189, v189, v190\n"
        "v_cvt_pk_bf16_f32 v189, 0, v189\n"
        "v_mfma_f32_16x16x32_bf16 a[48:51], v[8:11], v[48:51], a[48:51]\n"
        "v_mul_f32 v195, v177, v188\n"
        "v_cvt_pk_bf16_f32 v195, 0, v195\n"
        "v_mfma_f32_16x16x32_bf16 a[52:55], v[8:11], v[52:55], a[52:55]\n"
        "global_load_lds_dwordx4 v239, s[86:87] offset:1024\n"
        "v_mul_f32 v194, v177, v187\n"
        "v_mfma_f32_16x16x32_bf16 a[52:55], v[12:15], v[84:87], a[52:55]\n"
        "v_cvt_pk_bf16_f32 v194, 0, v194\n"
        "v_add_f32 v193, v193, v194\n"
        "v_mfma_f32_16x16x32_bf16 a[20:23], v[4:7], v[84:87], a[20:23]\n"
        "v_cvt_pk_bf16_f32 v193, 0, v193\n"
        "v_and_b32 v176, s35, v100\n"
        "v_mfma_f32_16x16x32_bf16 a[20:23], v[0:3], v[52:55], a[20:23]\n"
        "v_and_b32 v177, s35, v102\n"
        "v_mfma_f32_16x16x32_bf16 a[24:27], v[0:3], v[56:59], a[24:27]\n"
        "global_load_lds_dwordx4 v240, s[86:87] offset:2048\n"
        "v_mfma_f32_16x16x32_bf16 a[24:27], v[4:7], v[88:91], a[24:27]\n"
        "v_mfma_f32_16x16x32_bf16 a[56:59], v[12:15], v[88:91], a[56:59]\n"
        "v_mfma_f32_16x16x32_bf16 a[56:59], v[8:11], v[56:59], a[56:59]\n"
        "v_mfma_f32_16x16x32_bf16 a[60:63], v[8:11], v[60:63], a[60:63]\n"
        "global_load_lds_dwordx4 v241, s[86:87] offset:3072\n"
        "ds_read_b128 v[8:11], v224 offset:2048\n"
        "s_add_u32 m0, s72, 0x0\n"
        "v_mfma_f32_16x16x32_bf16 a[60:63], v[12:15], v[92:95], a[60:63]\n"
        "ds_read_b128 v[12:15], v225 offset:2048\n"
        "s_add_u32 s86, s56, 0x200\n"
        "s_addc_u32 s87, s57, 0\n"
        "v_mfma_f32_16x16x32_bf16 a[28:31], v[4:7], v[92:95], a[28:31]\n"
        "ds_read_b128 v[4:7], v225 offset:0\n"
        "s_waitcnt vmcnt(24)\n"
        "v_mfma_f32_16x16x32_bf16 a[28:31], v[0:3], v[60:63], a[28:31]\n"
        "ds_read_b128 v[0:3], v224 offset:0\n"
        "v_lshlrev_b32 v178, 16, v104\n"
        "v_mfma_f32_16x16x32_bf16 a[64:67], v[32:35], v[16:19], a[64:67]\n"
        "global_load_dwordx2 v[120:121], v169, s[74:75] offset:-2048\n"
        "v_mul_f32 v192, v178, v185\n"
        "v_mfma_f32_16x16x32_bf16 a[64:67], v[64:67], v[20:23], a[64:67]\n"
        "v_cvt_pk_bf16_f32 v192, 0, v192\n"
        "v_add_f32 v191, v191, v192\n"
        "v_mfma_f32_16x16x32_bf16 a[96:99], v[64:67], v[28:31], a[96:99]\n"
        "v_cvt_pk_bf16_f32 v191, 0, v191\n"
        "v_mul_f32 v196, v178, v187\n"
        "v_mfma_f32_16x16x32_bf16 a[96:99], v[32:35], v[24:27], a[96:99]\n"
        "v_cvt_pk_bf16_f32 v196, 0, v196\n"
        "v_add_f32 v195, v195, v196\n"
        "v_mfma_f32_16x16x32_bf16 a[100:103], v[36:39], v[24:27], a[100:103]\n"
        "s_waitcnt vmcnt(24)\n"
        "v_lshlrev_b32 v179, 16, v106\n"
        "v_cvt_pk_bf16_f32 v195, 0, v195\n"
        "v_mfma_f32_16x16x32_bf16 a[100:103], v[68:71], v[28:31], a[100:103]\n"
        "v_mul_f32 v194, v178, v186\n"
        "v_mul_f32 v196, v179, v186\n"
        "v_mfma_f32_16x16x32_bf16 a[68:71], v[68:71], v[20:23], a[68:71]\n"
        "v_cvt_pk_bf16_f32 v194, 0, v194\n"
        "v_cvt_pk_bf16_f32 v196, 0, v196\n"
        "v_mfma_f32_16x16x32_bf16 a[68:71], v[36:39], v[16:19], a[68:71]\n"
        "global_load_dwordx2 v[122:123], v169, s[74:75] offset:0\n"
        "v_add_f32 v193, v193, v194\n"
        "v_mfma_f32_16x16x32_bf16 a[72:75], v[40:43], v[16:19], a[72:75]\n"
        "v_add_f32 v195, v195, v196\n"
        "v_cvt_pk_bf16_f32 v193, 0, v193\n"
        "v_mfma_f32_16x16x32_bf16 a[72:75], v[72:75], v[20:23], a[72:75]\n"
        "v_cvt_pk_bf16_f32 v195, 0, v195\n"
        "v_mul_f32 v194, v179, v185\n"
        "v_mfma_f32_16x16x32_bf16 a[104:107], v[72:75], v[28:31], a[104:107]\n"
        "v_cvt_pk_bf16_f32 v194, 0, v194\n"
        "v_add_f32 v193, v193, v194\n"
        "v_mfma_f32_16x16x32_bf16 a[104:107], v[40:43], v[24:27], a[104:107]\n"
        "v_cvt_pk_bf16_f32 v193, 0, v193\n"
        "v_mfma_f32_16x16x32_bf16 a[108:111], v[44:47], v[24:27], a[108:111]\n"
        "v_mfma_f32_16x16x32_bf16 a[108:111], v[76:79], v[28:31], a[108:111]\n"
        "v_mfma_f32_16x16x32_bf16 a[76:79], v[76:79], v[20:23], a[76:79]\n"
        "v_mfma_f32_16x16x32_bf16 a[76:79], v[44:47], v[16:19], a[76:79]\n"
        "global_load_dword v253, v173, s[82:83] offset:0\n"
        "s_add_u32 s82, s12, 0x300000\n"
        "s_addc_u32 s83, s13, 0\n"
        "v_mfma_f32_16x16x32_bf16 a[80:83], v[16:19], v[48:51], a[80:83]\n"
        "v_mfma_f32_16x16x32_bf16 a[80:83], v[20:23], v[80:83], a[80:83]\n"
        "v_mfma_f32_16x16x32_bf16 a[112:115], v[28:31], v[80:83], a[112:115]\n"
        "v_mfma_f32_16x16x32_bf16 a[112:115], v[24:27], v[48:51], a[112:115]\n"
        "v_mfma_f32_16x16x32_bf16 a[116:119], v[24:27], v[52:55], a[116:119]\n"
        "s_waitcnt vmcnt(24) lgkmcnt(0)\n"
        "v_lshlrev_b32 v180, 16, v108\n"
        "v_mul_f32 v196, v180, v185\n"
        "v_mfma_f32_16x16x32_bf16 a[116:119], v[28:31], v[84:87], a[116:119]\n"
        "v_cvt_pk_bf16_f32 v196, 0, v196\n"
        "v_add_f32 v195, v195, v196\n"
        "v_mfma_f32_16x16x32_bf16 a[84:87], v[20:23], v[84:87], a[84:87]\n"
        "v_cvt_pk_bf16_f32 v195, 0, v195\n"
        "v_mfma_f32_16x16x32_bf16 a[84:87], v[16:19], v[52:55], a[84:87]\n"
        "global_load_dwordx2 v[124:125], v169, s[74:75] offset:2048\n"
        "s_add_u32 s74, s22, 0x3000\n"
        "s_addc_u32 s75, s23, 0\n"
        "v_mfma_f32_16x16x32_bf16 a[88:91], v[16:19], v[56:59], a[88:91]\n"
        "v_mfma_f32_16x16x32_bf16 a[88:91], v[20:23], v[88:91], a[88:91]\n"
        "v_mfma_f32_16x16x32_bf16 a[120:123], v[28:31], v[88:91], a[120:123]\n"
        "v_mfma_f32_16x16x32_bf16 a[120:123], v[24:27], v[56:59], a[120:123]\n"
        "v_mfma_f32_16x16x32_bf16 a[124:127], v[24:27], v[60:63], a[124:127]\n"
        "ds_read_b128 v[24:27], v224 offset:6144\n"
        "v_mfma_f32_16x16x32_bf16 a[124:127], v[28:31], v[92:95], a[124:127]\n"
        "ds_read_b128 v[28:31], v225 offset:6144\n"
        "v_mfma_f32_16x16x32_bf16 a[92:95], v[20:23], v[92:95], a[92:95]\n"
        "ds_read_b128 v[20:23], v225 offset:4096\n"
        "v_mfma_f32_16x16x32_bf16 a[92:95], v[16:19], v[60:63], a[92:95]\n"
        "ds_read_b128 v[16:19], v224 offset:4096\n"
        "v_mfma_f32_16x16x32_bf16 a[128:131], v[32:35], v[0:3], a[128:131]\n"
        "v_mfma_f32_16x16x32_bf16 a[128:131], v[64:67], v[4:7], a[128:131]\n"
        "v_mfma_f32_16x16x32_bf16 a[160:163], v[64:67], v[12:15], a[160:163]\n"
        "v_mfma_f32_16x16x32_bf16 a[160:163], v[32:35], v[8:11], a[160:163]\n"
        "v_mfma_f32_16x16x32_bf16 a[164:167], v[36:39], v[8:11], a[164:167]\n"
        "s_waitcnt vmcnt(24) lgkmcnt(3)\n"
        "v_lshlrev_b32 v181, 16, v110\n"
        "v_mfma_f32_16x16x32_bf16 a[164:167], v[68:71], v[12:15], a[164:167]\n"
        "v_mfma_f32_16x16x32_bf16 a[132:135], v[68:71], v[4:7], a[132:135]\n"
        "v_mfma_f32_16x16x32_bf16 a[132:135], v[36:39], v[0:3], a[132:135]\n"
        "global_load_dwordx2 v[126:127], v169, s[74:75] offset:-4096\n"
        "v_mfma_f32_16x16x32_bf16 a[136:139], v[40:43], v[0:3], a[136:139]\n"
        "v_mfma_f32_16x16x32_bf16 a[136:139], v[72:75], v[4:7], a[136:139]\n"
        "v_mfma_f32_16x16x32_bf16 a[168:171], v[72:75], v[12:15], a[168:171]\n"
        "v_mfma_f32_16x16x32_bf16 a[168:171], v[40:43], v[8:11], a[168:171]\n"
        "v_mfma_f32_16x16x32_bf16 a[172:175], v[44:47], v[8:11], a[172:175]\n"
        "v_mfma_f32_16x16x32_bf16 a[172:175], v[76:79], v[12:15], a[172:175]\n"
        "v_mfma_f32_16x16x32_bf16 a[140:143], v[76:79], v[4:7], a[140:143]\n"
        "v_mfma_f32_16x16x32_bf16 a[140:143], v[44:47], v[0:3], a[140:143]\n"
        "v_mfma_f32_16x16x32_bf16 a[144:147], v[0:3], v[48:51], a[144:147]\n"
        "v_mfma_f32_16x16x32_bf16 a[144:147], v[4:7], v[80:83], a[144:147]\n"
        "v_mfma_f32_16x16x32_bf16 a[176:179], v[12:15], v[80:83], a[176:179]\n"
        "v_mfma_f32_16x16x32_bf16 a[176:179], v[8:11], v[48:51], a[176:179]\n"
        "v_mfma_f32_16x16x32_bf16 a[180:183], v[8:11], v[52:55], a[180:183]\n"
        "s_waitcnt vmcnt(24) lgkmcnt(0)\n"
        "v_lshlrev_b32 v182, 16, v112\n"
        "v_mfma_f32_16x16x32_bf16 a[180:183], v[12:15], v[84:87], a[180:183]\n"
        "v_mfma_f32_16x16x32_bf16 a[148:151], v[4:7], v[84:87], a[148:151]\n"
        "v_mfma_f32_16x16x32_bf16 a[148:151], v[0:3], v[52:55], a[148:151]\n"
        "global_load_dwordx2 v[128:129], v169, s[74:75] offset:-2048\n"
        "v_mfma_f32_16x16x32_bf16 a[152:155], v[0:3], v[56:59], a[152:155]\n"
        "v_mfma_f32_16x16x32_bf16 a[152:155], v[4:7], v[88:91], a[152:155]\n"
        "v_mfma_f32_16x16x32_bf16 a[184:187], v[12:15], v[88:91], a[184:187]\n"
        "v_mfma_f32_16x16x32_bf16 a[184:187], v[8:11], v[56:59], a[184:187]\n"
        "s_waitcnt vmcnt(16)\n"
        "v_mfma_f32_16x16x32_bf16 a[188:191], v[8:11], v[60:63], a[188:191]\n"
        "s_barrier\n"
        "v_mfma_f32_16x16x32_bf16 a[188:191], v[12:15], v[92:95], a[188:191]\n"
        "global_load_lds_dwordx4 v234, s[86:87] offset:0\n"
        "ds_read_b128 v[8:11], v224 offset:51200\n"
        "v_mfma_f32_16x16x32_bf16 a[156:159], v[4:7], v[92:95], a[156:159]\n"
        "ds_read_b128 v[4:7], v225 offset:49152\n"
        "v_mfma_f32_16x16x32_bf16 a[156:159], v[0:3], v[60:63], a[156:159]\n"
        "ds_read_b128 v[0:3], v224 offset:49152\n"
        "v_mfma_f32_16x16x32_bf16 a[192:195], v[32:35], v[16:19], a[192:195]\n"
        "ds_read_b128 v[12:15], v225 offset:51200\n"
        "v_mfma_f32_16x16x32_bf16 a[192:195], v[64:67], v[20:23], a[192:195]\n"
        "global_load_lds_dwordx4 v235, s[86:87] offset:1024\n"
        "v_mfma_f32_16x16x32_bf16 a[224:227], v[64:67], v[28:31], a[224:227]\n"
        "ds_read_b64_tr_b16 v[64:65], v226 offset:8192\n"
        "v_mfma_f32_16x16x32_bf16 a[224:227], v[32:35], v[24:27], a[224:227]\n"
        "ds_read_b64_tr_b16 v[32:33], v226 offset:0\n"
        "v_mfma_f32_16x16x32_bf16 a[228:231], v[36:39], v[24:27], a[228:231]\n"
        "ds_read_b64_tr_b16 v[34:35], v227 offset:0\n"
        "v_mfma_f32_16x16x32_bf16 a[228:231], v[68:71], v[28:31], a[228:231]\n"
        "ds_read_b64_tr_b16 v[66:67], v227 offset:8192\n"
        "global_load_lds_dwordx4 v236, s[86:87] offset:2048\n"
        "v_mfma_f32_16x16x32_bf16 a[196:199], v[68:71], v[20:23], a[196:199]\n"
        "ds_read_b64_tr_b16 v[68:69], v228 offset:8192\n"
        "v_mfma_f32_16x16x32_bf16 a[196:199], v[36:39], v[16:19], a[196:199]\n"
        "ds_read_b64_tr_b16 v[36:37], v228 offset:0\n"
        "v_mfma_f32_16x16x32_bf16 a[200:203], v[40:43], v[16:19], a[200:203]\n"
        "ds_read_b64_tr_b16 v[38:39], v229 offset:0\n"
        "v_mfma_f32_16x16x32_bf16 a[200:203], v[72:75], v[20:23], a[200:203]\n"
        "ds_read_b64_tr_b16 v[70:71], v229 offset:8192\n"
        "global_load_lds_dwordx4 v237, s[86:87] offset:3072\n"
        "s_add_u32 m0, s72, 0x1000\n"
        "v_mfma_f32_16x16x32_bf16 a[232:235], v[72:75], v[28:31], a[232:235]\n"
        "ds_read_b64_tr_b16 v[72:73], v230 offset:8192\n"
        "s_add_u32 s86, s58, 0x200\n"
        "s_addc_u32 s87, s59, 0\n"
        "v_mfma_f32_16x16x32_bf16 a[232:235], v[40:43], v[24:27], a[232:235]\n"
        "ds_read_b64_tr_b16 v[40:41], v230 offset:0\n"
        "v_lshlrev_b32 v183, 16, v114\n"
        "v_mfma_f32_16x16x32_bf16 a[236:239], v[44:47], v[24:27], a[236:239]\n"
        "ds_read_b64_tr_b16 v[42:43], v231 offset:0\n"
        "v_mfma_f32_16x16x32_bf16 a[236:239], v[76:79], v[28:31], a[236:239]\n"
        "ds_read_b64_tr_b16 v[74:75], v231 offset:8192\n"
        "global_load_dwordx2 v[130:131], v169, s[74:75] offset:0\n"
        "v_mfma_f32_16x16x32_bf16 a[204:207], v[76:79], v[20:23], a[204:207]\n"
        "ds_read_b64_tr_b16 v[76:77], v232 offset:8192\n"
        "global_load_lds_dwordx4 v234, s[86:87] offset:0\n"
        "v_mfma_f32_16x16x32_bf16 a[204:207], v[44:47], v[16:19], a[204:207]\n"
        "ds_read_b64_tr_b16 v[44:45], v232 offset:0\n"
        "v_mfma_f32_16x16x32_bf16 a[208:211], v[16:19], v[48:51], a[208:211]\n"
        "ds_read_b64_tr_b16 v[46:47], v233 offset:0\n"
        "v_mfma_f32_16x16x32_bf16 a[208:211], v[20:23], v[80:83], a[208:211]\n"
        "ds_read_b64_tr_b16 v[78:79], v233 offset:8192\n"
        "v_mfma_f32_16x16x32_bf16 a[240:243], v[28:31], v[80:83], a[240:243]\n"
        "ds_read_b64_tr_b16 v[80:81], v226 offset:24576\n"
        "global_load_lds_dwordx4 v235, s[86:87] offset:1024\n"
        "v_mfma_f32_16x16x32_bf16 a[240:243], v[24:27], v[48:51], a[240:243]\n"
        "ds_read_b64_tr_b16 v[48:49], v226 offset:16384\n"
        "v_mfma_f32_16x16x32_bf16 a[244:247], v[24:27], v[52:55], a[244:247]\n"
        "ds_read_b64_tr_b16 v[50:51], v227 offset:16384\n"
        "v_mfma_f32_16x16x32_bf16 a[244:247], v[28:31], v[84:87], a[244:247]\n"
        "ds_read_b64_tr_b16 v[82:83], v227 offset:24576\n"
        "v_mfma_f32_16x16x32_bf16 a[212:215], v[20:23], v[84:87], a[212:215]\n"
        "ds_read_b64_tr_b16 v[84:85], v228 offset:24576\n"
        "global_load_lds_dwordx4 v236, s[86:87] offset:2048\n"
        "v_mfma_f32_16x16x32_bf16 a[212:215], v[16:19], v[52:55], a[212:215]\n"
        "ds_read_b64_tr_b16 v[52:53], v228 offset:16384\n"
        "v_mfma_f32_16x16x32_bf16 a[216:219], v[16:19], v[56:59], a[216:219]\n"
        "ds_read_b64_tr_b16 v[54:55], v229 offset:16384\n"
        "v_mfma_f32_16x16x32_bf16 a[216:219], v[20:23], v[88:91], a[216:219]\n"
        "ds_read_b64_tr_b16 v[86:87], v229 offset:24576\n"
        "v_mfma_f32_16x16x32_bf16 a[248:251], v[28:31], v[88:91], a[248:251]\n"
        "ds_read_b64_tr_b16 v[88:89], v230 offset:24576\n"
        "global_load_lds_dwordx4 v237, s[86:87] offset:3072\n"
        "s_add_u32 m0, s73, 0x20000\n"
        "v_mfma_f32_16x16x32_bf16 a[248:251], v[24:27], v[56:59], a[248:251]\n"
        "ds_read_b64_tr_b16 v[56:57], v230 offset:16384\n"
        "s_add_u32 s86, s60, 0x280000\n"
        "s_addc_u32 s87, s61, 0\n"
        "v_mfma_f32_16x16x32_bf16 a[252:255], v[24:27], v[60:63], a[252:255]\n"
        "ds_read_b64_tr_b16 v[58:59], v231 offset:16384\n"
        "s_waitcnt lgkmcnt(8)\n"
        "v_mfma_f32_16x16x32_bf16 a[252:255], v[28:31], v[92:95], a[252:255]\n"
        "ds_read_b64_tr_b16 v[90:91], v231 offset:24576\n"
        "s_waitcnt vmcnt(24) lgkmcnt(8)\n"
        "v_mfma_f32_16x16x32_bf16 a[220:223], v[20:23], v[92:95], a[220:223]\n"
        "ds_read_b64_tr_b16 v[92:93], v232 offset:24576\n"
        "global_load_dwordx2 v[132:133], v169, s[74:75] offset:2048\n"
        "v_mfma_f32_16x16x32_bf16 a[220:223], v[16:19], v[60:63], a[220:223]\n"
        "ds_read_b64_tr_b16 v[60:61], v232 offset:16384\n"
        "global_load_lds_dwordx4 v238, s[86:87] offset:0\n"
        "v_mfma_f32_16x16x32_bf16 a[0:3], v[32:35], v[0:3], a[0:3]\n"
        "ds_read_b64_tr_b16 v[62:63], v233 offset:16384\n"
        "v_lshlrev_b32 v184, 16, v116\n"
        "v_mfma_f32_16x16x32_bf16 a[0:3], v[64:67], v[4:7], a[0:3]\n"
        "ds_read_b64_tr_b16 v[94:95], v233 offset:24576\n"
        "v_mfma_f32_16x16x32_bf16 a[32:35], v[64:67], v[12:15], a[32:35]\n"
        "ds_read_b128 v[16:19], v224 offset:53248\n"
        "v_mfma_f32_16x16x32_bf16 a[32:35], v[32:35], v[8:11], a[32:35]\n"
        "ds_read_b128 v[20:23], v225 offset:53248\n"
        "global_load_lds_dwordx4 v239, s[86:87] offset:1024\n"
        "v_mfma_f32_16x16x32_bf16 a[36:39], v[36:39], v[8:11], a[36:39]\n"
        "ds_read_b128 v[24:27], v224 offset:55296\n"
        "v_mfma_f32_16x16x32_bf16 a[36:39], v[68:71], v[12:15], a[36:39]\n"
        "ds_read_b128 v[28:31], v225 offset:55296\n"
        "v_mfma_f32_16x16x32_bf16 a[4:7], v[68:71], v[4:7], a[4:7]\n"
        "v_mfma_f32_16x16x32_bf16 a[4:7], v[36:39], v[0:3], a[4:7]\n"
        "global_load_lds_dwordx4 v240, s[86:87] offset:2048\n"
        "v_mfma_f32_16x16x32_bf16 a[8:11], v[40:43], v[0:3], a[8:11]\n"
        "v_mfma_f32_16x16x32_bf16 a[8:11], v[72:75], v[4:7], a[8:11]\n"
        "v_mfma_f32_16x16x32_bf16 a[40:43], v[72:75], v[12:15], a[40:43]\n"
        "v_mfma_f32_16x16x32_bf16 a[40:43], v[40:43], v[8:11], a[40:43]\n"
        "global_load_lds_dwordx4 v241, s[86:87] offset:3072\n"
        "s_add_u32 m0, s73, 0x24000\n"
        "v_mfma_f32_16x16x32_bf16 a[44:47], v[44:47], v[8:11], a[44:47]\n"
        "s_add_u32 s86, s62, 0x280000\n"
        "s_addc_u32 s87, s63, 0\n"
        "s_waitcnt vmcnt(24) lgkmcnt(1)\n"
        "v_mfma_f32_16x16x32_bf16 a[44:47], v[76:79], v[12:15], a[44:47]\n"
        "v_lshlrev_b32 v190, 16, v118\n"
        "v_mul_f32 v189, v189, v190\n"
        "v_mfma_f32_16x16x32_bf16 a[12:15], v[76:79], v[4:7], a[12:15]\n"
        "v_mul_f32 v190, v179, v187\n"
        "v_cvt_pk_bf16_f32 v190, 0, v190\n"
        "v_mfma_f32_16x16x32_bf16 a[12:15], v[44:47], v[0:3], a[12:15]\n"
        "global_load_lds_dwordx4 v238, s[86:87] offset:0\n"
        "v_mfma_f32_16x16x32_bf16 a[16:19], v[0:3], v[48:51], a[16:19]\n"
        "v_mfma_f32_16x16x32_bf16 a[16:19], v[4:7], v[80:83], a[16:19]\n"
        "v_mfma_f32_16x16x32_bf16 a[48:51], v[12:15], v[80:83], a[48:51]\n"
        "v_mfma_f32_16x16x32_bf16 a[48:51], v[8:11], v[48:51], a[48:51]\n"
        "global_load_lds_dwordx4 v239, s[86:87] offset:1024\n"
        "v_mfma_f32_16x16x32_bf16 a[52:55], v[8:11], v[52:55], a[52:55]\n"
        "v_mfma_f32_16x16x32_bf16 a[52:55], v[12:15], v[84:87], a[52:55]\n"
        "v_mfma_f32_16x16x32_bf16 a[20:23], v[4:7], v[84:87], a[20:23]\n"
        "v_mfma_f32_16x16x32_bf16 a[20:23], v[0:3], v[52:55], a[20:23]\n"
        "global_load_lds_dwordx4 v240, s[86:87] offset:2048\n"
        "v_mfma_f32_16x16x32_bf16 a[24:27], v[0:3], v[56:59], a[24:27]\n"
        "v_mfma_f32_16x16x32_bf16 a[24:27], v[4:7], v[88:91], a[24:27]\n"
        "v_mfma_f32_16x16x32_bf16 a[56:59], v[12:15], v[88:91], a[56:59]\n"
        "v_mfma_f32_16x16x32_bf16 a[56:59], v[8:11], v[56:59], a[56:59]\n"
        "global_load_lds_dwordx4 v241, s[86:87] offset:3072\n"
        "s_add_u32 m0, s72, 0x8000\n"
        "v_mfma_f32_16x16x32_bf16 a[60:63], v[8:11], v[60:63], a[60:63]\n"
        "ds_read_b128 v[8:11], v224 offset:34816\n"
        "s_add_u32 s86, s56, 0x280\n"
        "s_addc_u32 s87, s57, 0\n"
        "v_mfma_f32_16x16x32_bf16 a[60:63], v[12:15], v[92:95], a[60:63]\n"
        "ds_read_b128 v[12:15], v225 offset:34816\n"
        "s_waitcnt vmcnt(23) lgkmcnt(2)\n"
        "v_mfma_f32_16x16x32_bf16 a[28:31], v[4:7], v[92:95], a[28:31]\n"
        "ds_read_b128 v[4:7], v225 offset:32768\n"
        "v_lshlrev_b32 v192, 16, v120\n"
        "v_mfma_f32_16x16x32_bf16 a[28:31], v[0:3], v[60:63], a[28:31]\n"
        "ds_read_b128 v[0:3], v224 offset:32768\n"
        "v_mul_f32 v191, v191, v192\n"
        "v_mfma_f32_16x16x32_bf16 a[64:67], v[32:35], v[16:19], a[64:67]\n"
        "v_cvt_pk_bf16_f32 v203, v191, v189\n"
        "v_mul_f32 v189, v178, v188\n"
        "v_mfma_f32_16x16x32_bf16 a[64:67], v[64:67], v[20:23], a[64:67]\n"
        "v_mul_f32 v191, v179, v188\n"
        "v_cvt_pk_bf16_f32 v189, 0, v189\n"
        "v_mfma_f32_16x16x32_bf16 a[96:99], v[64:67], v[28:31], a[96:99]\n"
        "v_cvt_pk_bf16_f32 v191, 0, v191\n"
        "v_mul_f32 v192, v180, v187\n"
        "v_mfma_f32_16x16x32_bf16 a[96:99], v[32:35], v[24:27], a[96:99]\n"
        "v_cvt_pk_bf16_f32 v192, 0, v192\n"
        "v_add_f32 v189, v189, v190\n"
        "v_mfma_f32_16x16x32_bf16 a[100:103], v[36:39], v[24:27], a[100:103]\n"
        "s_waitcnt vmcnt(22) lgkmcnt(3)\n"
        "v_lshlrev_b32 v194, 16, v122\n"
        "v_mul_f32 v193, v193, v194\n"
        "v_mfma_f32_16x16x32_bf16 a[100:103], v[68:71], v[28:31], a[100:103]\n"
        "v_add_f32 v191, v191, v192\n"
        "v_cvt_pk_bf16_f32 v189, 0, v189\n"
        "v_mfma_f32_16x16x32_bf16 a[68:71], v[68:71], v[20:23], a[68:71]\n"
        "LHA1_%=:\n"
        "s_cbranch_execz LHA5_%=\n"
        "v_cvt_pk_bf16_f32 v191, 0, v191\n"
        "v_mul_f32 v190, v180, v186\n"
        "v_mfma_f32_16x16x32_bf16 a[68:71], v[36:39], v[16:19], a[68:71]\n"
        "v_mul_f32 v192, v181, v186\n"
        "v_cvt_pk_bf16_f32 v190, 0, v190\n"
        "v_mfma_f32_16x16x32_bf16 a[72:75], v[40:43], v[16:19], a[72:75]\n"
        "v_cvt_pk_bf16_f32 v192, 0, v192\n"
        "v_add_f32 v189, v189, v190\n"
        "v_mfma_f32_16x16x32_bf16 a[72:75], v[72:75], v[20:23], a[72:75]\n"
        "v_add_f32 v191, v191, v192\n"
        "v_cvt_pk_bf16_f32 v189, 0, v189\n"
        "v_mfma_f32_16x16x32_bf16 a[104:107], v[72:75], v[28:31], a[104:107]\n"
        "v_cvt_pk_bf16_f32 v191, 0, v191\n"
        "v_mul_f32 v190, v181, v185\n"
        "v_mfma_f32_16x16x32_bf16 a[104:107], v[40:43], v[24:27], a[104:107]\n"
        "v_mul_f32 v192, v182, v185\n"
        "v_cvt_pk_bf16_f32 v190, 0, v190\n"
        "v_mfma_f32_16x16x32_bf16 a[108:111], v[44:47], v[24:27], a[108:111]\n"
        "v_cvt_pk_bf16_f32 v192, 0, v192\n"
        "v_add_f32 v189, v189, v190\n"
        "v_mfma_f32_16x16x32_bf16 a[108:111], v[76:79], v[28:31], a[108:111]\n"
        "v_add_f32 v191, v191, v192\n"
        "v_cvt_pk_bf16_f32 v189, 0, v189\n"
        "v_mfma_f32_16x16x32_bf16 a[76:79], v[76:79], v[20:23], a[76:79]\n"
        "v_cvt_pk_bf16_f32 v191, 0, v191\n"
        "v_mul_f32 v194, v181, v187\n"
        "v_mfma_f32_16x16x32_bf16 a[76:79], v[44:47], v[16:19], a[76:79]\n"
        "global_load_dword v253, v173, s[82:83] offset:0\n"
        "s_add_u32 s82, s12, 0x400000\n"
        "s_addc_u32 s83, s13, 0\n"
        "v_mfma_f32_16x16x32_bf16 a[80:83], v[16:19], v[48:51], a[80:83]\n"
        "v_cvt_pk_bf16_f32 v194, 0, v194\n"
        "v_and_b32 v178, s35, v104\n"
        "v_mfma_f32_16x16x32_bf16 a[80:83], v[20:23], v[80:83], a[80:83]\n"
        "v_and_b32 v179, s35, v106\n"
        "v_mfma_f32_16x16x32_bf16 a[112:115], v[28:31], v[80:83], a[112:115]\n"
        "v_mfma_f32_16x16x32_bf16 a[112:115], v[24:27], v[48:51], a[112:115]\n"
        "v_mfma_f32_16x16x32_bf16 a[116:119], v[24:27], v[52:55], a[116:119]\n"
        "s_waitcnt vmcnt(21) lgkmcnt(0)\n"
        "v_lshlrev_b32 v196, 16, v124\n"
        "v_mul_f32 v195, v195, v196\n"
        "v_mfma_f32_16x16x32_bf16 a[116:119], v[28:31], v[84:87], a[116:119]\n"
        "v_cvt_pk_bf16_f32 v202, v195, v193\n"
        "v_mul_f32 v193, v180, v188\n"
        "v_mfma_f32_16x16x32_bf16 a[84:87], v[20:23], v[84:87], a[84:87]\n"
        "v_mul_f32 v195, v181, v188\n"
        "v_cvt_pk_bf16_f32 v193, 0, v193\n"
        "v_mfma_f32_16x16x32_bf16 a[84:87], v[16:19], v[52:55], a[84:87]\n"
        "v_cvt_pk_bf16_f32 v195, 0, v195\n"
        "v_mul_f32 v196, v182, v187\n"
        "v_mfma_f32_16x16x32_bf16 a[88:91], v[16:19], v[56:59], a[88:91]\n"
        "v_cvt_pk_bf16_f32 v196, 0, v196\n"
        "v_add_f32 v193, v193, v194\n"
        "v_mfma_f32_16x16x32_bf16 a[88:91], v[20:23], v[88:91], a[88:91]\n"
        "v_add_f32 v195, v195, v196\n"
        "v_cvt_pk_bf16_f32 v193, 0, v193\n"
        "v_mfma_f32_16x16x32_bf16 a[120:123], v[28:31], v[88:91], a[120:123]\n"
        "v_cvt_pk_bf16_f32 v195, 0, v195\n"
        "v_mul_f32 v194, v182, v186\n"
        "v_mfma_f32_16x16x32_bf16 a[120:123], v[24:27], v[56:59], a[120:123]\n"
        "v_mul_f32 v196, v183, v186\n"
        "v_cvt_pk_bf16_f32 v194, 0, v194\n"
        "v_mfma_f32_16x16x32_bf16 a[124:127], v[24:27], v[60:63], a[124:127]\n"
        "ds_read_b128 v[24:27], v224 offset:38912\n"
        "v_cvt_pk_bf16_f32 v196, 0, v196\n"
        "v_mfma_f32_16x16x32_bf16 a[124:127], v[28:31], v[92:95], a[124:127]\n"
        "ds_read_b128 v[28:31], v225 offset:38912\n"
        "v_add_f32 v193, v193, v194\n"
        "v_mfma_f32_16x16x32_bf16 a[92:95], v[20:23], v[92:95], a[92:95]\n"
        "ds_read_b128 v[20:23], v225 offset:36864\n"
        "v_add_f32 v195, v195, v196\n"
        "v_mfma_f32_16x16x32_bf16 a[92:95], v[16:19], v[60:63], a[92:95]\n"
        "ds_read_b128 v[16:19], v224 offset:36864\n"
        "global_load_dwordx4 v[142:145], v254, s[92:93] offset:0\n"
        "v_mfma_f32_16x16x32_bf16 a[128:131], v[32:35], v[0:3], a[128:131]\n"
        "v_cvt_pk_bf16_f32 v193, 0, v193\n"
        "v_cvt_pk_bf16_f32 v195, 0, v195\n"
        "v_mfma_f32_16x16x32_bf16 a[128:131], v[64:67], v[4:7], a[128:131]\n"
        "v_mul_f32 v194, v183, v185\n"
        "v_mul_f32 v196, v184, v185\n"
        "v_mfma_f32_16x16x32_bf16 a[160:163], v[64:67], v[12:15], a[160:163]\n"
        "v_cvt_pk_bf16_f32 v194, 0, v194\n"
        "v_cvt_pk_bf16_f32 v196, 0, v196\n"
        "v_mfma_f32_16x16x32_bf16 a[160:163], v[32:35], v[8:11], a[160:163]\n"
        "v_add_f32 v193, v193, v194\n"
        "v_add_f32 v195, v195, v196\n"
        "v_mfma_f32_16x16x32_bf16 a[164:167], v[36:39], v[8:11], a[164:167]\n"
        "s_waitcnt vmcnt(21) lgkmcnt(3)\n"
        "v_lshlrev_b32 v190, 16, v126\n"
        "v_mul_f32 v189, v189, v190\n"
        "v_mfma_f32_16x16x32_bf16 a[164:167], v[68:71], v[12:15], a[164:167]\n"
        "v_cvt_pk_bf16_f32 v193, 0, v193\n"
        "v_cvt_pk_bf16_f32 v195, 0, v195\n"
        "v_mfma_f32_16x16x32_bf16 a[132:135], v[68:71], v[4:7], a[132:135]\n"
        "v_lshlrev_b32 v185, 16, v136\n"
        "v_and_b32 v186, s35, v136\n"
        "v_mfma_f32_16x16x32_bf16 a[132:135], v[36:39], v[0:3], a[132:135]\n"
        "v_lshlrev_b32 v187, 16, v137\n"
        "v_and_b32 v188, s35, v137\n"
        "v_mfma_f32_16x16x32_bf16 a[136:139], v[40:43], v[0:3], a[136:139]\n"
        "v_and_b32 v180, s35, v108\n"
        "v_and_b32 v181, s35, v110\n"
        "v_mfma_f32_16x16x32_bf16 a[136:139], v[72:75], v[4:7], a[136:139]\n"
        "v_and_b32 v182, s35, v112\n"
        "v_and_b32 v183, s35, v114\n"
        "v_mfma_f32_16x16x32_bf16 a[168:171], v[72:75], v[12:15], a[168:171]\n"
        "v_and_b32 v184, s35, v116\n"
        "v_mul_f32 v190, v175, v187\n"
        "v_mfma_f32_16x16x32_bf16 a[168:171], v[40:43], v[8:11], a[168:171]\n"
        "v_cvt_pk_bf16_f32 v190, 0, v190\n"
        "v_mfma_f32_16x16x32_bf16 a[172:175], v[44:47], v[8:11], a[172:175]\n"
        "v_mfma_f32_16x16x32_bf16 a[172:175], v[76:79], v[12:15], a[172:175]\n"
        "v_mfma_f32_16x16x32_bf16 a[140:143], v[76:79], v[4:7], a[140:143]\n"
        "v_mfma_f32_16x16x32_bf16 a[140:143], v[44:47], v[0:3], a[140:143]\n"
        "v_mfma_f32_16x16x32_bf16 a[144:147], v[0:3], v[48:51], a[144:147]\n"
        "v_mfma_f32_16x16x32_bf16 a[144:147], v[4:7], v[80:83], a[144:147]\n"
        "v_mfma_f32_16x16x32_bf16 a[176:179], v[12:15], v[80:83], a[176:179]\n"
        "v_mfma_f32_16x16x32_bf16 a[176:179], v[8:11], v[48:51], a[176:179]\n"
        "v_mfma_f32_16x16x32_bf16 a[180:183], v[8:11], v[52:55], a[180:183]\n"
        "s_waitcnt vmcnt(20) lgkmcnt(0)\n"
        "v_lshlrev_b32 v192, 16, v128\n"
        "v_mul_f32 v191, v191, v192\n"
        "v_mfma_f32_16x16x32_bf16 a[180:183], v[12:15], v[84:87], a[180:183]\n"
        "v_cvt_pk_bf16_f32 v201, v191, v189\n"
        "v_mul_f32 v189, v174, v188\n"
        "v_mfma_f32_16x16x32_bf16 a[148:151], v[4:7], v[84:87], a[148:151]\n"
        "v_mul_f32 v191, v175, v188\n"
        "v_cvt_pk_bf16_f32 v189, 0, v189\n"
        "v_mfma_f32_16x16x32_bf16 a[148:151], v[0:3], v[52:55], a[148:151]\n"
        "v_cvt_pk_bf16_f32 v191, 0, v191\n"
        "v_mul_f32 v192, v176, v187\n"
        "v_mfma_f32_16x16x32_bf16 a[152:155], v[0:3], v[56:59], a[152:155]\n"
        "v_cvt_pk_bf16_f32 v192, 0, v192\n"
        "v_add_f32 v189, v189, v190\n"
        "v_mfma_f32_16x16x32_bf16 a[152:155], v[4:7], v[88:91], a[152:155]\n"
        "v_add_f32 v191, v191, v192\n"
        "v_cvt_pk_bf16_f32 v189, 0, v189\n"
        "v_mfma_f32_16x16x32_bf16 a[184:187], v[12:15], v[88:91], a[184:187]\n"
        "v_cvt_pk_bf16_f32 v191, 0, v191\n"
        "v_mul_f32 v190, v176, v186\n"
        "v_mfma_f32_16x16x32_bf16 a[184:187], v[8:11], v[56:59], a[184:187]\n"
        "s_waitcnt vmcnt(11)\n"
        "v_mul_f32 v192, v177, v186\n"
        "v_cvt_pk_bf16_f32 v190, 0, v190\n"
        "v_mfma_f32_16x16x32_bf16 a[188:191], v[8:11], v[60:63], a[188:191]\n"
        "s_barrier\n"
        "v_mfma_f32_16x16x32_bf16 a[188:191], v[12:15], v[92:95], a[188:191]\n"
        "global_load_lds_dwordx4 v234, s[86:87] offset:0\n"
        "ds_read_b128 v[8:11], v224 offset:18432\n"
        "v_mfma_f32_16x16x32_bf16 a[156:159], v[4:7], v[92:95], a[156:159]\n"
        "ds_read_b128 v[4:7], v225 offset:16384\n"
        "v_cvt_pk_bf16_f32 v192, 0, v192\n"
        "v_mfma_f32_16x16x32_bf16 a[156:159], v[0:3], v[60:63], a[156:159]\n"
        "ds_read_b128 v[0:3], v224 offset:16384\n"
        "v_add_f32 v189, v189, v190\n"
        "v_mfma_f32_16x16x32_bf16 a[192:195], v[32:35], v[16:19], a[192:195]\n"
        "ds_read_b128 v[12:15], v225 offset:18432\n"
        "v_add_f32 v191, v191, v192\n"
        "v_mfma_f32_16x16x32_bf16 a[192:195], v[64:67], v[20:23], a[192:195]\n"
        "global_load_lds_dwordx4 v235, s[86:87] offset:1024\n"
        "v_cvt_pk_bf16_f32 v189, 0, v189\n"
        "v_mfma_f32_16x16x32_bf16 a[224:227], v[64:67], v[28:31], a[224:227]\n"
        "ds_read_b64_tr_b16 v[64:65], v226 offset:40960\n"
        "v_cvt_pk_bf16_f32 v191, 0, v191\n"
        "v_mfma_f32_16x16x32_bf16 a[224:227], v[32:35], v[24:27], a[224:227]\n"
        "ds_read_b64_tr_b16 v[32:33], v226 offset:32768\n"
        "v_mul_f32 v190, v177, v185\n"
        "v_mfma_f32_16x16x32_bf16 a[228:231], v[36:39], v[24:27], a[228:231]\n"
        "ds_read_b64_tr_b16 v[34:35], v227 offset:32768\n"
        "v_mul_f32 v192, v178, v185\n"
        "v_mfma_f32_16x16x32_bf16 a[228:231], v[68:71], v[28:31], a[228:231]\n"
        "ds_read_b64_tr_b16 v[66:67], v227 offset:40960\n"
        "global_load_lds_dwordx4 v236, s[86:87] offset:2048\n"
        "v_mfma_f32_16x16x32_bf16 a[196:199], v[68:71], v[20:23], a[196:199]\n"
        "ds_read_b64_tr_b16 v[68:69], v228 offset:40960\n"
        "v_cvt_pk_bf16_f32 v190, 0, v190\n"
        "v_mfma_f32_16x16x32_bf16 a[196:199], v[36:39], v[16:19], a[196:199]\n"
        "ds_read_b64_tr_b16 v[36:37], v228 offset:32768\n"
        "v_cvt_pk_bf16_f32 v192, 0, v192\n"
        "v_mfma_f32_16x16x32_bf16 a[200:203], v[40:43], v[16:19], a[200:203]\n"
        "ds_read_b64_tr_b16 v[38:39], v229 offset:32768\n"
        "v_add_f32 v189, v189, v190\n"
        "v_mfma_f32_16x16x32_bf16 a[200:203], v[72:75], v[20:23], a[200:203]\n"
        "ds_read_b64_tr_b16 v[70:71], v229 offset:40960\n"
        "global_load_lds_dwordx4 v237, s[86:87] offset:3072\n"
        "s_add_u32 m0, s72, 0x9000\n"
        "v_mfma_f32_16x16x32_bf16 a[232:235], v[72:75], v[28:31], a[232:235]\n"
        "ds_read_b64_tr_b16 v[72:73], v230 offset:40960\n"
        "s_add_u32 s86, s58, 0x280\n"
        "s_addc_u32 s87, s59, 0\n"
        "v_mfma_f32_16x16x32_bf16 a[232:235], v[40:43], v[24:27], a[232:235]\n"
        "ds_read_b64_tr_b16 v[40:41], v230 offset:32768\n"
        "v_lshlrev_b32 v194, 16, v130\n"
        "v_mfma_f32_16x16x32_bf16 a[236:239], v[44:47], v[24:27], a[236:239]\n"
        "ds_read_b64_tr_b16 v[42:43], v231 offset:32768\n"
        "v_mul_f32 v193, v193, v194\n"
        "v_mfma_f32_16x16x32_bf16 a[236:239], v[76:79], v[28:31], a[236:239]\n"
        "ds_read_b64_tr_b16 v[74:75], v231 offset:40960\n"
        "global_load_lds_dwordx4 v234, s[86:87] offset:0\n"
        "v_mfma_f32_16x16x32_bf16 a[204:207], v[76:79], v[20:23], a[204:207]\n"
        "ds_read_b64_tr_b16 v[76:77], v232 offset:40960\n"
        "v_add_f32 v191, v191, v192\n"
        "v_mfma_f32_16x16x32_bf16 a[204:207], v[44:47], v[16:19], a[204:207]\n"
        "ds_read_b64_tr_b16 v[44:45], v232 offset:32768\n"
        "v_cvt_pk_bf16_f32 v189, 0, v189\n"
        "v_mfma_f32_16x16x32_bf16 a[208:211], v[16:19], v[48:51], a[208:211]\n"
        "ds_read_b64_tr_b16 v[46:47], v233 offset:32768\n"
        "v_cvt_pk_bf16_f32 v191, 0, v191\n"
        "v_mfma_f32_16x16x32_bf16 a[208:211], v[20:23], v[80:83], a[208:211]\n"
        "ds_read_b64_tr_b16 v[78:79], v233 offset:40960\n"
        "global_load_lds_dwordx4 v235, s[86:87] offset:1024\n"
        "v_mfma_f32_16x16x32_bf16 a[240:243], v[28:31], v[80:83], a[240:243]\n"
        "ds_read_b64_tr_b16 v[80:81], v226 offset:57344\n"
        "v_and_b32 v190, s35, v118\n"
        "v_mfma_f32_16x16x32_bf16 a[240:243], v[24:27], v[48:51], a[240:243]\n"
        "ds_read_b64_tr_b16 v[48:49], v226 offset:49152\n"
        "v_and_b32 v192, s35, v120\n"
        "v_mfma_f32_16x16x32_bf16 a[244:247], v[24:27], v[52:55], a[244:247]\n"
        "ds_read_b64_tr_b16 v[50:51], v227 offset:49152\n"
        "v_mul_f32 v189, v189, v190\n"
        "v_mfma_f32_16x16x32_bf16 a[244:247], v[28:31], v[84:87], a[244:247]\n"
        "ds_read_b64_tr_b16 v[82:83], v227 offset:57344\n"
        "global_load_lds_dwordx4 v236, s[86:87] offset:2048\n"
        "v_mfma_f32_16x16x32_bf16 a[212:215], v[20:23], v[84:87], a[212:215]\n"
        "ds_read_b64_tr_b16 v[84:85], v228 offset:57344\n"
        "v_mul_f32 v191, v191, v192\n"
        "v_mfma_f32_16x16x32_bf16 a[212:215], v[16:19], v[52:55], a[212:215]\n"
        "ds_read_b64_tr_b16 v[52:53], v228 offset:49152\n"
        "v_cvt_pk_bf16_f32 v211, v191, v189\n"
        "v_mfma_f32_16x16x32_bf16 a[216:219], v[16:19], v[56:59], a[216:219]\n"
        "ds_read_b64_tr_b16 v[54:55], v229 offset:49152\n"
        "v_mul_f32 v194, v177, v187\n"
        "v_mfma_f32_16x16x32_bf16 a[216:219], v[20:23], v[88:91], a[216:219]\n"
        "ds_read_b64_tr_b16 v[86:87], v229 offset:57344\n"
        "global_load_lds_dwordx4 v237, s[86:87] offset:3072\n"
        "s_add_u32 m0, s73, 0x10000\n"
        "v_mfma_f32_16x16x32_bf16 a[248:251], v[28:31], v[88:91], a[248:251]\n"
        "ds_read_b64_tr_b16 v[88:89], v230 offset:57344\n"
        "s_add_u32 s86, s60, 0x300000\n"
        "s_addc_u32 s87, s61, 0\n"
        "v_mfma_f32_16x16x32_bf16 a[248:251], v[24:27], v[56:59], a[248:251]\n"
        "ds_read_b64_tr_b16 v[56:57], v230 offset:49152\n"
        "v_cvt_pk_bf16_f32 v194, 0, v194\n"
        "v_mfma_f32_16x16x32_bf16 a[252:255], v[24:27], v[60:63], a[252:255]\n"
        "ds_read_b64_tr_b16 v[58:59], v231 offset:49152\n"
        "v_mul_f32 v189, v178, v188\n"
        "v_mfma_f32_16x16x32_bf16 a[252:255], v[28:31], v[92:95], a[252:255]\n"
        "ds_read_b64_tr_b16 v[90:91], v231 offset:57344\n"
        "global_load_lds_dwordx4 v238, s[86:87] offset:0\n"
        "v_mfma_f32_16x16x32_bf16 a[220:223], v[20:23], v[92:95], a[220:223]\n"
        "ds_read_b64_tr_b16 v[92:93], v232 offset:57344\n"
        "v_mul_f32 v191, v179, v188\n"
        "v_mfma_f32_16x16x32_bf16 a[220:223], v[16:19], v[60:63], a[220:223]\n"
        "ds_read_b64_tr_b16 v[60:61], v232 offset:49152\n"
        "s_waitcnt lgkmcnt(8)\n"
        "v_mfma_f32_16x16x32_bf16 a[0:3], v[32:35], v[0:3], a[0:3]\n"
        "ds_read_b64_tr_b16 v[62:63], v233 offset:49152\n"
        "v_cvt_pk_bf16_f32 v189, 0, v189\n"
        "v_mfma_f32_16x16x32_bf16 a[0:3], v[64:67], v[4:7], a[0:3]\n"
        "ds_read_b64_tr_b16 v[94:95], v233 offset:57344\n"
        "global_load_lds_dwordx4 v239, s[86:87] offset:1024\n"
        "v_mfma_f32_16x16x32_bf16 a[32:35], v[64:67], v[12:15], a[32:35]\n"
        "ds_read_b128 v[16:19], v224 offset:20480\n"
        "v_cvt_pk_bf16_f32 v191, 0, v191\n"
        "v_mfma_f32_16x16x32_bf16 a[32:35], v[32:35], v[8:11], a[32:35]\n"
        "ds_read_b128 v[20:23], v225 offset:20480\n"
        "v_mul_f32 v190, v179, v187\n"
        "v_mfma_f32_16x16x32_bf16 a[36:39], v[36:39], v[8:11], a[36:39]\n"
        "ds_read_b128 v[24:27], v224 offset:22528\n"
        "v_mul_f32 v192, v180, v187\n"
        "v_mfma_f32_16x16x32_bf16 a[36:39], v[68:71], v[12:15], a[36:39]\n"
        "ds_read_b128 v[28:31], v225 offset:22528\n"
        "global_load_dwordx4 v[146:149], v254, s[92:93] offset:1024\n"
        "v_mfma_f32_16x16x32_bf16 a[4:7], v[68:71], v[4:7], a[4:7]\n"
        "s_waitcnt vmcnt(21) lgkmcnt(7)\n"
        "v_lshlrev_b32 v196, 16, v132\n"
        "v_mul_f32 v195, v195, v196\n"
        "v_mfma_f32_16x16x32_bf16 a[4:7], v[36:39], v[0:3], a[4:7]\n"
        "v_cvt_pk_bf16_f32 v200, v195, v193\n"
        "v_mul_f32 v193, v176, v188\n"
        "v_mfma_f32_16x16x32_bf16 a[8:11], v[40:43], v[0:3], a[8:11]\n"
        "v_mul_f32 v195, v177, v188\n"
        "v_cvt_pk_bf16_f32 v193, 0, v193\n"
        "v_mfma_f32_16x16x32_bf16 a[8:11], v[72:75], v[4:7], a[8:11]\n"
        "global_load_lds_dwordx4 v240, s[86:87] offset:2048\n"
        "v_mfma_f32_16x16x32_bf16 a[40:43], v[72:75], v[12:15], a[40:43]\n"
        "v_cvt_pk_bf16_f32 v195, 0, v195\n"
        "v_mul_f32 v196, v178, v187\n"
        "v_mfma_f32_16x16x32_bf16 a[40:43], v[40:43], v[8:11], a[40:43]\n"
        "v_cvt_pk_bf16_f32 v196, 0, v196\n"
        "v_add_f32 v193, v193, v194\n"
        "v_mfma_f32_16x16x32_bf16 a[44:47], v[44:47], v[8:11], a[44:47]\n"
        "v_add_f32 v195, v195, v196\n"
        "v_cvt_pk_bf16_f32 v193, 0, v193\n"
        "v_mfma_f32_16x16x32_bf16 a[44:47], v[76:79], v[12:15], a[44:47]\n"
        "global_load_lds_dwordx4 v241, s[86:87] offset:3072\n"
        "s_add_u32 m0, s73, 0x14000\n"
        "v_mfma_f32_16x16x32_bf16 a[12:15], v[76:79], v[4:7], a[12:15]\n"
        "s_add_u32 s86, s62, 0x300000\n"
        "s_addc_u32 s87, s63, 0\n"
        "v_cvt_pk_bf16_f32 v195, 0, v195\n"
        "v_mfma_f32_16x16x32_bf16 a[12:15], v[44:47], v[0:3], a[12:15]\n"
        "v_mul_f32 v194, v178, v186\n"
        "v_mul_f32 v196, v179, v186\n"
        "v_mfma_f32_16x16x32_bf16 a[16:19], v[0:3], v[48:51], a[16:19]\n"
        "v_cvt_pk_bf16_f32 v194, 0, v194\n"
        "v_cvt_pk_bf16_f32 v196, 0, v196\n"
        "v_mfma_f32_16x16x32_bf16 a[16:19], v[4:7], v[80:83], a[16:19]\n"
        "global_load_lds_dwordx4 v238, s[86:87] offset:0\n"
        "v_mfma_f32_16x16x32_bf16 a[48:51], v[12:15], v[80:83], a[48:51]\n"
        "v_add_f32 v193, v193, v194\n"
        "v_add_f32 v195, v195, v196\n"
        "v_mfma_f32_16x16x32_bf16 a[48:51], v[8:11], v[48:51], a[48:51]\n"
        "v_cvt_pk_bf16_f32 v193, 0, v193\n"
        "v_cvt_pk_bf16_f32 v195, 0, v195\n"
        "v_mfma_f32_16x16x32_bf16 a[52:55], v[8:11], v[52:55], a[52:55]\n"
        "v_mul_f32 v194, v179, v185\n"
        "v_mul_f32 v196, v180, v185\n"
        "v_mfma_f32_16x16x32_bf16 a[52:55], v[12:15], v[84:87], a[52:55]\n"
        "global_load_lds_dwordx4 v239, s[86:87] offset:1024\n"
        "v_mfma_f32_16x16x32_bf16 a[20:23], v[4:7], v[84:87], a[20:23]\n"
        "v_cvt_pk_bf16_f32 v194, 0, v194\n"
        "v_cvt_pk_bf16_f32 v196, 0, v196\n"
        "v_mfma_f32_16x16x32_bf16 a[20:23], v[0:3], v[52:55], a[20:23]\n"
        "v_add_f32 v193, v193, v194\n"
        "v_add_f32 v195, v195, v196\n"
        "v_mfma_f32_16x16x32_bf16 a[24:27], v[0:3], v[56:59], a[24:27]\n"
        "s_waitcnt lgkmcnt(0)\n"
        "v_cvt_pk_bf16_f32 v193, 0, v193\n"
        "v_cvt_pk_bf16_f32 v195, 0, v195\n"
        "v_mfma_f32_16x16x32_bf16 a[24:27], v[4:7], v[88:91], a[24:27]\n"
        "global_load_lds_dwordx4 v240, s[86:87] offset:2048\n"
        "v_mfma_f32_16x16x32_bf16 a[56:59], v[12:15], v[88:91], a[56:59]\n"
        "v_and_b32 v194, s35, v122\n"
        "v_and_b32 v196, s35, v124\n"
        "v_mfma_f32_16x16x32_bf16 a[56:59], v[8:11], v[56:59], a[56:59]\n"
        "v_mul_f32 v193, v193, v194\n"
        "v_mul_f32 v195, v195, v196\n"
        "v_mfma_f32_16x16x32_bf16 a[60:63], v[8:11], v[60:63], a[60:63]\n"
        "ds_read_b128 v[8:11], v224 offset:2048\n"
        "v_cvt_pk_bf16_f32 v210, v195, v193\n"
        "v_mfma_f32_16x16x32_bf16 a[60:63], v[12:15], v[92:95], a[60:63]\n"
        "global_load_lds_dwordx4 v241, s[86:87] offset:3072\n"
        "s_add_u32 m0, s72, 0x0\n"
        "v_mfma_f32_16x16x32_bf16 a[28:31], v[4:7], v[92:95], a[28:31]\n"
        "ds_read_b128 v[4:7], v225 offset:0\n"
        "s_add_u32 s86, s56, 0x300\n"
        "s_addc_u32 s87, s57, 0\n"
        "v_mfma_f32_16x16x32_bf16 a[28:31], v[0:3], v[60:63], a[28:31]\n"
        "ds_read_b128 v[0:3], v224 offset:0\n"
        "v_cvt_pk_bf16_f32 v190, 0, v190\n"
        "v_mfma_f32_16x16x32_bf16 a[64:67], v[32:35], v[16:19], a[64:67]\n"
        "ds_read_b128 v[12:15], v225 offset:2048\n"
        "v_cvt_pk_bf16_f32 v192, 0, v192\n"
        "v_mfma_f32_16x16x32_bf16 a[64:67], v[64:67], v[20:23], a[64:67]\n"
        "v_add_f32 v189, v189, v190\n"
        "v_add_f32 v191, v191, v192\n"
        "v_mfma_f32_16x16x32_bf16 a[96:99], v[64:67], v[28:31], a[96:99]\n"
        "global_store_dwordx4 v171, v[200:203], s[76:77] offset:-4096 sc1\n"
        "v_cvt_pk_bf16_f32 v189, 0, v189\n"
        "v_cvt_pk_bf16_f32 v191, 0, v191\n"
        "v_mul_f32 v190, v180, v186\n"
        "v_mul_f32 v192, v181, v186\n"
        "v_mfma_f32_16x16x32_bf16 a[96:99], v[32:35], v[24:27], a[96:99]\n"
        "v_cvt_pk_bf16_f32 v190, 0, v190\n"
        "v_cvt_pk_bf16_f32 v192, 0, v192\n"
        "v_mfma_f32_16x16x32_bf16 a[100:103], v[36:39], v[24:27], a[100:103]\n"
        "v_add_f32 v189, v189, v190\n"
        "v_add_f32 v191, v191, v192\n"
        "v_mfma_f32_16x16x32_bf16 a[100:103], v[68:71], v[28:31], a[100:103]\n"
        "v_cvt_pk_bf16_f32 v189, 0, v189\n"
        "v_cvt_pk_bf16_f32 v191, 0, v191\n"
        "v_mfma_f32_16x16x32_bf16 a[68:71], v[68:71], v[20:23], a[68:71]\n"
        "v_mul_f32 v190, v181, v185\n"
        "v_mul_f32 v192, v182, v185\n"
        "v_mfma_f32_16x16x32_bf16 a[68:71], v[36:39], v[16:19], a[68:71]\n"
        "v_cvt_pk_bf16_f32 v190, 0, v190\n"
        "v_cvt_pk_bf16_f32 v192, 0, v192\n"
        "v_mfma_f32_16x16x32_bf16 a[72:75], v[40:43], v[16:19], a[72:75]\n"
        "v_add_f32 v189, v189, v190\n"
        "v_add_f32 v191, v191, v192\n"
        "v_mfma_f32_16x16x32_bf16 a[72:75], v[72:75], v[20:23], a[72:75]\n"
        "v_cvt_pk_bf16_f32 v189, 0, v189\n"
        "v_cvt_pk_bf16_f32 v191, 0, v191\n"
        "v_mfma_f32_16x16x32_bf16 a[104:107], v[72:75], v[28:31], a[104:107]\n"
        "v_and_b32 v190, s35, v126\n"
        "v_and_b32 v192, s35, v128\n"
        "v_mfma_f32_16x16x32_bf16 a[104:107], v[40:43], v[24:27], a[104:107]\n"
        "v_mul_f32 v189, v189, v190\n"
        "v_mul_f32 v191, v191, v192\n"
        "v_mfma_f32_16x16x32_bf16 a[108:111], v[44:47], v[24:27], a[108:111]\n"
        "v_cvt_pk_bf16_f32 v209, v191, v189\n"
        "v_mul_f32 v193, v180, v188\n"
        "v_mfma_f32_16x16x32_bf16 a[108:111], v[76:79], v[28:31], a[108:111]\n"
        "v_mul_f32 v195, v181, v188\n"
        "v_cvt_pk_bf16_f32 v193, 0, v193\n"
        "v_mfma_f32_16x16x32_bf16 a[76:79], v[76:79], v[20:23], a[76:79]\n"
        "v_cvt_pk_bf16_f32 v195, 0, v195\n"
        "v_mul_f32 v194, v181, v187\n"
        "v_mfma_f32_16x16x32_bf16 a[76:79], v[44:47], v[16:19], a[76:79]\n"
        "global_load_dword v253, v173, s[82:83] offset:0\n"
        "s_add_u32 s82, s12, 0x500000\n"
        "s_addc_u32 s83, s13, 0\n"
        "v_mfma_f32_16x16x32_bf16 a[80:83], v[16:19], v[48:51], a[80:83]\n"
        "v_mul_f32 v196, v182, v187\n"
        "v_cvt_pk_bf16_f32 v194, 0, v194\n"
        "v_mfma_f32_16x16x32_bf16 a[80:83], v[20:23], v[80:83], a[80:83]\n"
        "v_cvt_pk_bf16_f32 v196, 0, v196\n"
        "v_add_f32 v193, v193, v194\n"
        "v_mfma_f32_16x16x32_bf16 a[112:115], v[28:31], v[80:83], a[112:115]\n"
        "v_add_f32 v195, v195, v196\n"
        "v_cvt_pk_bf16_f32 v193, 0, v193\n"
        "v_mfma_f32_16x16x32_bf16 a[112:115], v[24:27], v[48:51], a[112:115]\n"
        "v_cvt_pk_bf16_f32 v195, 0, v195\n"
        "v_mul_f32 v194, v182, v186\n"
        "v_mfma_f32_16x16x32_bf16 a[116:119], v[24:27], v[52:55], a[116:119]\n"
        "v_mul_f32 v196, v183, v186\n"
        "v_cvt_pk_bf16_f32 v194, 0, v194\n"
        "v_mfma_f32_16x16x32_bf16 a[116:119], v[28:31], v[84:87], a[116:119]\n"
        "v_cvt_pk_bf16_f32 v196, 0, v196\n"
        "v_add_f32 v193, v193, v194\n"
        "v_mfma_f32_16x16x32_bf16 a[84:87], v[20:23], v[84:87], a[84:87]\n"
        "v_add_f32 v195, v195, v196\n"
        "v_cvt_pk_bf16_f32 v193, 0, v193\n"
        "v_mfma_f32_16x16x32_bf16 a[84:87], v[16:19], v[52:55], a[84:87]\n"
        "v_cvt_pk_bf16_f32 v195, 0, v195\n"
        "v_mul_f32 v194, v183, v185\n"
        "v_mfma_f32_16x16x32_bf16 a[88:91], v[16:19], v[56:59], a[88:91]\n"
        "v_mul_f32 v196, v184, v185\n"
        "v_cvt_pk_bf16_f32 v194, 0, v194\n"
        "v_mfma_f32_16x16x32_bf16 a[88:91], v[20:23], v[88:91], a[88:91]\n"
        "v_cvt_pk_bf16_f32 v196, 0, v196\n"
        "v_add_f32 v193, v193, v194\n"
        "v_mfma_f32_16x16x32_bf16 a[120:123], v[28:31], v[88:91], a[120:123]\n"
        "v_add_f32 v195, v195, v196\n"
        "v_cvt_pk_bf16_f32 v193, 0, v193\n"
        "v_mfma_f32_16x16x32_bf16 a[120:123], v[24:27], v[56:59], a[120:123]\n"
        "v_cvt_pk_bf16_f32 v195, 0, v195\n"
        "v_and_b32 v194, s35, v130\n"
        "v_mfma_f32_16x16x32_bf16 a[124:127], v[24:27], v[60:63], a[124:127]\n"
        "ds_read_b128 v[24:27], v224 offset:6144\n"
        "v_and_b32 v196, s35, v132\n"
        "v_mfma_f32_16x16x32_bf16 a[124:127], v[28:31], v[92:95], a[124:127]\n"
        "ds_read_b128 v[28:31], v225 offset:6144\n"
        "v_mul_f32 v193, v193, v194\n"
        "v_mfma_f32_16x16x32_bf16 a[92:95], v[20:23], v[92:95], a[92:95]\n"
        "ds_read_b128 v[20:23], v225 offset:4096\n"
        "v_mul_f32 v195, v195, v196\n"
        "v_mfma_f32_16x16x32_bf16 a[92:95], v[16:19], v[60:63], a[92:95]\n"
        "ds_read_b128 v[16:19], v224 offset:4096\n"
        "s_waitcnt lgkmcnt(4)\n"
        "v_mfma_f32_16x16x32_bf16 a[128:131], v[32:35], v[0:3], a[128:131]\n"
        "global_load_dwordx4 v[150:153], v254, s[92:93] offset:2048\n"
        "v_cvt_pk_bf16_f32 v208, v195, v193\n"
        "v_mfma_f32_16x16x32_bf16 a[128:131], v[64:67], v[4:7], a[128:131]\n"
        "v_lshlrev_b32 v185, 16, v138\n"
        "v_and_b32 v186, s35, v138\n"
        "v_mfma_f32_16x16x32_bf16 a[160:163], v[64:67], v[12:15], a[160:163]\n"
        "v_lshlrev_b32 v187, 16, v139\n"
        "v_and_b32 v188, s35, v139\n"
        "v_mfma_f32_16x16x32_bf16 a[160:163], v[32:35], v[8:11], a[160:163]\n"
        "v_lshlrev_b32 v174, 16, v97\n"
        "v_lshlrev_b32 v175, 16, v99\n"
        "v_mfma_f32_16x16x32_bf16 a[164:167], v[36:39], v[8:11], a[164:167]\n"
        "global_store_dwordx4 v171, v[208:211], s[76:77] offset:0 sc1\n"
        "s_add_u32 s76, s76, 0x2000\n"
        "s_addc_u32 s77, s77, 0\n"
        "v_lshlrev_b32 v176, 16, v101\n"
        "v_lshlrev_b32 v177, 16, v103\n"
        "v_lshlrev_b32 v178, 16, v105\n"
        "v_mfma_f32_16x16x32_bf16 a[164:167], v[68:71], v[12:15], a[164:167]\n"
        "v_lshlrev_b32 v179, 16, v107\n"
        "v_lshlrev_b32 v180, 16, v109\n"
        "v_mfma_f32_16x16x32_bf16 a[132:135], v[68:71], v[4:7], a[132:135]\n"
        "v_lshlrev_b32 v181, 16, v111\n"
        "v_lshlrev_b32 v182, 16, v113\n"
        "v_mfma_f32_16x16x32_bf16 a[132:135], v[36:39], v[0:3], a[132:135]\n"
        "v_lshlrev_b32 v183, 16, v115\n"
        "v_lshlrev_b32 v184, 16, v117\n"
        "v_mfma_f32_16x16x32_bf16 a[136:139], v[40:43], v[0:3], a[136:139]\n"
        "v_mul_f32 v189, v174, v188\n"
        "v_mul_f32 v191, v175, v188\n"
        "v_mfma_f32_16x16x32_bf16 a[136:139], v[72:75], v[4:7], a[136:139]\n"
        "v_cvt_pk_bf16_f32 v189, 0, v189\n"
        "v_cvt_pk_bf16_f32 v191, 0, v191\n"
        "v_mfma_f32_16x16x32_bf16 a[168:171], v[72:75], v[12:15], a[168:171]\n"
        "v_mul_f32 v190, v175, v187\n"
        "v_mul_f32 v192, v176, v187\n"
        "v_mfma_f32_16x16x32_bf16 a[168:171], v[40:43], v[8:11], a[168:171]\n"
        "v_cvt_pk_bf16_f32 v190, 0, v190\n"
        "v_cvt_pk_bf16_f32 v192, 0, v192\n"
        "v_mfma_f32_16x16x32_bf16 a[172:175], v[44:47], v[8:11], a[172:175]\n"
        "v_add_f32 v189, v189, v190\n"
        "v_add_f32 v191, v191, v192\n"
        "v_mfma_f32_16x16x32_bf16 a[172:175], v[76:79], v[12:15], a[172:175]\n"
        "v_cvt_pk_bf16_f32 v189, 0, v189\n"
        "v_cvt_pk_bf16_f32 v191, 0, v191\n"
        "v_mfma_f32_16x16x32_bf16 a[140:143], v[76:79], v[4:7], a[140:143]\n"
        "v_mul_f32 v190, v176, v186\n"
        "v_mul_f32 v192, v177, v186\n"
        "v_mfma_f32_16x16x32_bf16 a[140:143], v[44:47], v[0:3], a[140:143]\n"
        "v_cvt_pk_bf16_f32 v190, 0, v190\n"
        "v_cvt_pk_bf16_f32 v192, 0, v192\n"
        "v_mfma_f32_16x16x32_bf16 a[144:147], v[0:3], v[48:51], a[144:147]\n"
        "v_add_f32 v189, v189, v190\n"
        "v_add_f32 v191, v191, v192\n"
        "v_mfma_f32_16x16x32_bf16 a[144:147], v[4:7], v[80:83], a[144:147]\n"
        "v_cvt_pk_bf16_f32 v189, 0, v189\n"
        "v_cvt_pk_bf16_f32 v191, 0, v191\n"
        "v_mfma_f32_16x16x32_bf16 a[176:179], v[12:15], v[80:83], a[176:179]\n"
        "v_mul_f32 v190, v177, v185\n"
        "v_mul_f32 v192, v178, v185\n"
        "v_mfma_f32_16x16x32_bf16 a[176:179], v[8:11], v[48:51], a[176:179]\n"
        "v_cvt_pk_bf16_f32 v190, 0, v190\n"
        "v_cvt_pk_bf16_f32 v192, 0, v192\n"
        "v_mfma_f32_16x16x32_bf16 a[180:183], v[8:11], v[52:55], a[180:183]\n"
        "v_add_f32 v189, v189, v190\n"
        "v_add_f32 v191, v191, v192\n"
        "v_mfma_f32_16x16x32_bf16 a[180:183], v[12:15], v[84:87], a[180:183]\n"
        "v_cvt_pk_bf16_f32 v189, 0, v189\n"
        "v_cvt_pk_bf16_f32 v191, 0, v191\n"
        "v_mfma_f32_16x16x32_bf16 a[148:151], v[4:7], v[84:87], a[148:151]\n"
        "v_lshlrev_b32 v190, 16, v119\n"
        "v_lshlrev_b32 v192, 16, v121\n"
        "v_mfma_f32_16x16x32_bf16 a[148:151], v[0:3], v[52:55], a[148:151]\n"
        "v_mul_f32 v189, v189, v190\n"
        "v_mul_f32 v191, v191, v192\n"
        "v_mfma_f32_16x16x32_bf16 a[152:155], v[0:3], v[56:59], a[152:155]\n"
        "v_cvt_pk_bf16_f32 v203, v191, v189\n"
        "v_mul_f32 v193, v176, v188\n"
        "v_mfma_f32_16x16x32_bf16 a[152:155], v[4:7], v[88:91], a[152:155]\n"
        "v_mul_f32 v195, v177, v188\n"
        "v_cvt_pk_bf16_f32 v193, 0, v193\n"
        "v_mfma_f32_16x16x32_bf16 a[184:187], v[12:15], v[88:91], a[184:187]\n"
        "v_cvt_pk_bf16_f32 v195, 0, v195\n"
        "v_mul_f32 v194, v177, v187\n"
        "v_mfma_f32_16x16x32_bf16 a[184:187], v[8:11], v[56:59], a[184:187]\n"
        "s_waitcnt vmcnt(13) lgkmcnt(0)\n"
        "v_mul_f32 v196, v178, v187\n"
        "v_cvt_pk_bf16_f32 v194, 0, v194\n"
        "v_mfma_f32_16x16x32_bf16 a[188:191], v[8:11], v[60:63], a[188:191]\n"
        "s_barrier\n"
        "v_mfma_f32_16x16x32_bf16 a[188:191], v[12:15], v[92:95], a[188:191]\n"
        "global_load_lds_dwordx4 v234, s[86:87] offset:0\n"
        "ds_read_b128 v[8:11], v224 offset:51200\n"
        "v_mfma_f32_16x16x32_bf16 a[156:159], v[4:7], v[92:95], a[156:159]\n"
        "ds_read_b128 v[4:7], v225 offset:49152\n"
        "v_cvt_pk_bf16_f32 v196, 0, v196\n"
        "v_mfma_f32_16x16x32_bf16 a[156:159], v[0:3], v[60:63], a[156:159]\n"
        "ds_read_b128 v[0:3], v224 offset:49152\n"
        "v_add_f32 v193, v193, v194\n"
        "v_mfma_f32_16x16x32_bf16 a[192:195], v[32:35], v[16:19], a[192:195]\n"
        "ds_read_b128 v[12:15], v225 offset:51200\n"
        "v_add_f32 v195, v195, v196\n"
        "v_mfma_f32_16x16x32_bf16 a[192:195], v[64:67], v[20:23], a[192:195]\n"
        "global_load_lds_dwordx4 v235, s[86:87] offset:1024\n"
        "v_cvt_pk_bf16_f32 v193, 0, v193\n"
        "v_mfma_f32_16x16x32_bf16 a[224:227], v[64:67], v[28:31], a[224:227]\n"
        "ds_read_b64_tr_b16 v[64:65], v216 offset:8192\n"
        "v_cvt_pk_bf16_f32 v195, 0, v195\n"
        "v_mfma_f32_16x16x32_bf16 a[224:227], v[32:35], v[24:27], a[224:227]\n"
        "ds_read_b64_tr_b16 v[32:33], v216 offset:0\n"
        "v_mul_f32 v194, v178, v186\n"
        "v_mfma_f32_16x16x32_bf16 a[228:231], v[36:39], v[24:27], a[228:231]\n"
        "ds_read_b64_tr_b16 v[34:35], v217 offset:0\n"
        "v_mul_f32 v196, v179, v186\n"
        "v_mfma_f32_16x16x32_bf16 a[228:231], v[68:71], v[28:31], a[228:231]\n"
        "ds_read_b64_tr_b16 v[66:67], v217 offset:8192\n"
        "global_load_lds_dwordx4 v236, s[86:87] offset:2048\n"
        "v_mfma_f32_16x16x32_bf16 a[196:199], v[68:71], v[20:23], a[196:199]\n"
        "ds_read_b64_tr_b16 v[68:69], v218 offset:8192\n"
        "v_cvt_pk_bf16_f32 v194, 0, v194\n"
        "v_mfma_f32_16x16x32_bf16 a[196:199], v[36:39], v[16:19], a[196:199]\n"
        "ds_read_b64_tr_b16 v[36:37], v218 offset:0\n"
        "v_cvt_pk_bf16_f32 v196, 0, v196\n"
        "v_mfma_f32_16x16x32_bf16 a[200:203], v[40:43], v[16:19], a[200:203]\n"
        "ds_read_b64_tr_b16 v[38:39], v219 offset:0\n"
        "v_add_f32 v193, v193, v194\n"
        "v_mfma_f32_16x16x32_bf16 a[200:203], v[72:75], v[20:23], a[200:203]\n"
        "ds_read_b64_tr_b16 v[70:71], v219 offset:8192\n"
        "global_load_lds_dwordx4 v237, s[86:87] offset:3072\n"
        "s_add_u32 m0, s72, 0x1000\n"
        "v_mfma_f32_16x16x32_bf16 a[232:235], v[72:75], v[28:31], a[232:235]\n"
        "ds_read_b64_tr_b16 v[72:73], v220 offset:8192\n"
        "s_add_u32 s86, s58, 0x300\n"
        "s_addc_u32 s87, s59, 0\n"
        "v_mfma_f32_16x16x32_bf16 a[232:235], v[40:43], v[24:27], a[232:235]\n"
        "ds_read_b64_tr_b16 v[40:41], v220 offset:0\n"
        "v_add_f32 v195, v195, v196\n"
        "v_mfma_f32_16x16x32_bf16 a[236:239], v[44:47], v[24:27], a[236:239]\n"
        "ds_read_b64_tr_b16 v[42:43], v221 offset:0\n"
        "v_cvt_pk_bf16_f32 v193, 0, v193\n"
        "v_mfma_f32_16x16x32_bf16 a[236:239], v[76:79], v[28:31], a[236:239]\n"
        "ds_read_b64_tr_b16 v[74:75], v221 offset:8192\n"
        "global_load_lds_dwordx4 v234, s[86:87] offset:0\n"
        "v_mfma_f32_16x16x32_bf16 a[204:207], v[76:79], v[20:23], a[204:207]\n"
        "ds_read_b64_tr_b16 v[76:77], v222 offset:8192\n"
        "v_cvt_pk_bf16_f32 v195, 0, v195\n"
        "v_mfma_f32_16x16x32_bf16 a[204:207], v[44:47], v[16:19], a[204:207]\n"
        "ds_read_b64_tr_b16 v[44:45], v222 offset:0\n"
        "LHA2_%=:\n"
        "s_cbranch_execz LHA6_%=\n"
        "v_mul_f32 v194, v179, v185\n"
        "v_mfma_f32_16x16x32_bf16 a[208:211], v[16:19], v[48:51], a[208:211]\n"
        "ds_read_b64_tr_b16 v[46:47], v223 offset:0\n"
        "v_mul_f32 v196, v180, v185\n"
        "v_mfma_f32_16x16x32_bf16 a[208:211], v[20:23], v[80:83], a[208:211]\n"
        "ds_read_b64_tr_b16 v[78:79], v223 offset:8192\n"
        "global_load_lds_dwordx4 v235, s[86:87] offset:1024\n"
        "v_mfma_f32_16x16x32_bf16 a[240:243], v[28:31], v[80:83], a[240:243]\n"
        "ds_read_b64_tr_b16 v[80:81], v216 offset:24576\n"
        "v_cvt_pk_bf16_f32 v194, 0, v194\n"
        "v_mfma_f32_16x16x32_bf16 a[240:243], v[24:27], v[48:51], a[240:243]\n"
        "ds_read_b64_tr_b16 v[48:49], v216 offset:16384\n"
        "v_cvt_pk_bf16_f32 v196, 0, v196\n"
        "v_mfma_f32_16x16x32_bf16 a[244:247], v[24:27], v[52:55], a[244:247]\n"
        "ds_read_b64_tr_b16 v[50:51], v217 offset:16384\n"
        "v_add_f32 v193, v193, v194\n"
        "v_mfma_f32_16x16x32_bf16 a[244:247], v[28:31], v[84:87], a[244:247]\n"
        "ds_read_b64_tr_b16 v[82:83], v217 offset:24576\n"
        "global_load_lds_dwordx4 v236, s[86:87] offset:2048\n"
        "v_mfma_f32_16x16x32_bf16 a[212:215], v[20:23], v[84:87], a[212:215]\n"
        "ds_read_b64_tr_b16 v[84:85], v218 offset:24576\n"
        "v_add_f32 v195, v195, v196\n"
        "v_mfma_f32_16x16x32_bf16 a[212:215], v[16:19], v[52:55], a[212:215]\n"
        "ds_read_b64_tr_b16 v[52:53], v218 offset:16384\n"
        "v_cvt_pk_bf16_f32 v193, 0, v193\n"
        "v_mfma_f32_16x16x32_bf16 a[216:219], v[16:19], v[56:59], a[216:219]\n"
        "ds_read_b64_tr_b16 v[54:55], v219 offset:16384\n"
        "v_cvt_pk_bf16_f32 v195, 0, v195\n"
        "v_mfma_f32_16x16x32_bf16 a[216:219], v[20:23], v[88:91], a[216:219]\n"
        "ds_read_b64_tr_b16 v[86:87], v219 offset:24576\n"
        "global_load_lds_dwordx4 v237, s[86:87] offset:3072\n"
        "s_add_u32 m0, s73, 0x18000\n"
        "v_mfma_f32_16x16x32_bf16 a[248:251], v[28:31], v[88:91], a[248:251]\n"
        "ds_read_b64_tr_b16 v[88:89], v220 offset:24576\n"
        "s_add_u32 s86, s60, 0x380000\n"
        "s_addc_u32 s87, s61, 0\n"
        "v_mfma_f32_16x16x32_bf16 a[248:251], v[24:27], v[56:59], a[248:251]\n"
        "ds_read_b64_tr_b16 v[56:57], v220 offset:16384\n"
        "v_lshlrev_b32 v194, 16, v123\n"
        "v_mfma_f32_16x16x32_bf16 a[252:255], v[24:27], v[60:63], a[252:255]\n"
        "ds_read_b64_tr_b16 v[58:59], v221 offset:16384\n"
        "v_lshlrev_b32 v196, 16, v125\n"
        "v_mfma_f32_16x16x32_bf16 a[252:255], v[28:31], v[92:95], a[252:255]\n"
        "ds_read_b64_tr_b16 v[90:91], v221 offset:24576\n"
        "global_load_lds_dwordx4 v238, s[86:87] offset:0\n"
        "v_mfma_f32_16x16x32_bf16 a[220:223], v[20:23], v[92:95], a[220:223]\n"
        "ds_read_b64_tr_b16 v[92:93], v222 offset:24576\n"
        "v_mul_f32 v193, v193, v194\n"
        "v_mfma_f32_16x16x32_bf16 a[220:223], v[16:19], v[60:63], a[220:223]\n"
        "ds_read_b64_tr_b16 v[60:61], v222 offset:16384\n"
        "s_waitcnt lgkmcnt(8)\n"
        "v_mfma_f32_16x16x32_bf16 a[0:3], v[32:35], v[0:3], a[0:3]\n"
        "ds_read_b64_tr_b16 v[62:63], v223 offset:16384\n"
        "v_mul_f32 v195, v195, v196\n"
        "v_mfma_f32_16x16x32_bf16 a[0:3], v[64:67], v[4:7], a[0:3]\n"
        "ds_read_b64_tr_b16 v[94:95], v223 offset:24576\n"
        "global_load_lds_dwordx4 v239, s[86:87] offset:1024\n"
        "v_mfma_f32_16x16x32_bf16 a[32:35], v[64:67], v[12:15], a[32:35]\n"
        "ds_read_b128 v[16:19], v224 offset:53248\n"
        "v_cvt_pk_bf16_f32 v202, v195, v193\n"
        "v_mfma_f32_16x16x32_bf16 a[32:35], v[32:35], v[8:11], a[32:35]\n"
        "ds_read_b128 v[20:23], v225 offset:53248\n"
        "v_mul_f32 v189, v178, v188\n"
        "v_mfma_f32_16x16x32_bf16 a[36:39], v[36:39], v[8:11], a[36:39]\n"
        "ds_read_b128 v[24:27], v224 offset:55296\n"
        "v_mul_f32 v191, v179, v188\n"
        "v_mfma_f32_16x16x32_bf16 a[36:39], v[68:71], v[12:15], a[36:39]\n"
        "ds_read_b128 v[28:31], v225 offset:55296\n"
        "global_load_dwordx4 v[154:157], v254, s[92:93] offset:3072\n"
        "v_mfma_f32_16x16x32_bf16 a[4:7], v[68:71], v[4:7], a[4:7]\n"
        "v_cvt_pk_bf16_f32 v189, 0, v189\n"
        "v_cvt_pk_bf16_f32 v191, 0, v191\n"
        "v_mfma_f32_16x16x32_bf16 a[4:7], v[36:39], v[0:3], a[4:7]\n"
        "v_mul_f32 v190, v179, v187\n"
        "v_mul_f32 v192, v180, v187\n"
        "v_mfma_f32_16x16x32_bf16 a[8:11], v[40:43], v[0:3], a[8:11]\n"
        "v_cvt_pk_bf16_f32 v190, 0, v190\n"
        "v_cvt_pk_bf16_f32 v192, 0, v192\n"
        "v_mfma_f32_16x16x32_bf16 a[8:11], v[72:75], v[4:7], a[8:11]\n"
        "global_load_lds_dwordx4 v240, s[86:87] offset:2048\n"
        "v_mfma_f32_16x16x32_bf16 a[40:43], v[72:75], v[12:15], a[40:43]\n"
        "v_add_f32 v189, v189, v190\n"
        "v_add_f32 v191, v191, v192\n"
        "v_mfma_f32_16x16x32_bf16 a[40:43], v[40:43], v[8:11], a[40:43]\n"
        "v_cvt_pk_bf16_f32 v189, 0, v189\n"
        "v_cvt_pk_bf16_f32 v191, 0, v191\n"
        "v_mfma_f32_16x16x32_bf16 a[44:47], v[44:47], v[8:11], a[44:47]\n"
        "v_mul_f32 v190, v180, v186\n"
        "v_mul_f32 v192, v181, v186\n"
        "v_mfma_f32_16x16x32_bf16 a[44:47], v[76:79], v[12:15], a[44:47]\n"
        "global_load_lds_dwordx4 v241, s[86:87] offset:3072\n"
        "s_add_u32 m0, s73, 0x1c000\n"
        "v_mfma_f32_16x16x32_bf16 a[12:15], v[76:79], v[4:7], a[12:15]\n"
        "s_add_u32 s86, s62, 0x380000\n"
        "s_addc_u32 s87, s63, 0\n"
        "v_cvt_pk_bf16_f32 v190, 0, v190\n"
        "v_mfma_f32_16x16x32_bf16 a[12:15], v[44:47], v[0:3], a[12:15]\n"
        "v_cvt_pk_bf16_f32 v192, 0, v192\n"
        "v_add_f32 v189, v189, v190\n"
        "v_mfma_f32_16x16x32_bf16 a[16:19], v[0:3], v[48:51], a[16:19]\n"
        "s_waitcnt lgkmcnt(0)\n"
        "v_add_f32 v191, v191, v192\n"
        "v_cvt_pk_bf16_f32 v189, 0, v189\n"
        "v_mfma_f32_16x16x32_bf16 a[16:19], v[4:7], v[80:83], a[16:19]\n"
        "global_load_lds_dwordx4 v238, s[86:87] offset:0\n"
        "v_mfma_f32_16x16x32_bf16 a[48:51], v[12:15], v[80:83], a[48:51]\n"
        "v_cvt_pk_bf16_f32 v191, 0, v191\n"
        "v_mul_f32 v190, v181, v185\n"
        "v_mfma_f32_16x16x32_bf16 a[48:51], v[8:11], v[48:51], a[48:51]\n"
        "v_mul_f32 v192, v182, v185\n"
        "v_cvt_pk_bf16_f32 v190, 0, v190\n"
        "v_mfma_f32_16x16x32_bf16 a[52:55], v[8:11], v[52:55], a[52:55]\n"
        "v_cvt_pk_bf16_f32 v192, 0, v192\n"
        "v_add_f32 v189, v189, v190\n"
        "v_mfma_f32_16x16x32_bf16 a[52:55], v[12:15], v[84:87], a[52:55]\n"
        "global_load_lds_dwordx4 v239, s[86:87] offset:1024\n"
        "v_mfma_f32_16x16x32_bf16 a[20:23], v[4:7], v[84:87], a[20:23]\n"
        "v_add_f32 v191, v191, v192\n"
        "v_cvt_pk_bf16_f32 v189, 0, v189\n"
        "v_mfma_f32_16x16x32_bf16 a[20:23], v[0:3], v[52:55], a[20:23]\n"
        "v_cvt_pk_bf16_f32 v191, 0, v191\n"
        "v_lshlrev_b32 v190, 16, v127\n"
        "v_mfma_f32_16x16x32_bf16 a[24:27], v[0:3], v[56:59], a[24:27]\n"
        "v_lshlrev_b32 v192, 16, v129\n"
        "v_mul_f32 v189, v189, v190\n"
        "v_mfma_f32_16x16x32_bf16 a[24:27], v[4:7], v[88:91], a[24:27]\n"
        "global_load_lds_dwordx4 v240, s[86:87] offset:2048\n"
        "v_mfma_f32_16x16x32_bf16 a[56:59], v[12:15], v[88:91], a[56:59]\n"
        "v_mul_f32 v191, v191, v192\n"
        "v_cvt_pk_bf16_f32 v201, v191, v189\n"
        "v_mfma_f32_16x16x32_bf16 a[56:59], v[8:11], v[56:59], a[56:59]\n"
        "v_mul_f32 v193, v180, v188\n"
        "v_mul_f32 v195, v181, v188\n"
        "v_mfma_f32_16x16x32_bf16 a[60:63], v[8:11], v[60:63], a[60:63]\n"
        "ds_read_b128 v[8:11], v224 offset:34816\n"
        "v_cvt_pk_bf16_f32 v193, 0, v193\n"
        "v_mfma_f32_16x16x32_bf16 a[60:63], v[12:15], v[92:95], a[60:63]\n"
        "global_load_lds_dwordx4 v241, s[86:87] offset:3072\n"
        "s_add_u32 m0, s72, 0x8000\n"
        "v_mfma_f32_16x16x32_bf16 a[28:31], v[4:7], v[92:95], a[28:31]\n"
        "ds_read_b128 v[4:7], v225 offset:32768\n"
        "s_add_u32 s86, s56, 0x380\n"
        "s_addc_u32 s87, s57, 0\n"
        "v_mfma_f32_16x16x32_bf16 a[28:31], v[0:3], v[60:63], a[28:31]\n"
        "ds_read_b128 v[0:3], v224 offset:32768\n"
        "v_cvt_pk_bf16_f32 v195, 0, v195\n"
        "v_mfma_f32_16x16x32_bf16 a[64:67], v[32:35], v[16:19], a[64:67]\n"
        "ds_read_b128 v[12:15], v225 offset:34816\n"
        "v_mul_f32 v194, v181, v187\n"
        "v_mfma_f32_16x16x32_bf16 a[64:67], v[64:67], v[20:23], a[64:67]\n"
        "v_mul_f32 v196, v182, v187\n"
        "v_cvt_pk_bf16_f32 v194, 0, v194\n"
        "v_mfma_f32_16x16x32_bf16 a[96:99], v[64:67], v[28:31], a[96:99]\n"
        "v_cvt_pk_bf16_f32 v196, 0, v196\n"
        "v_add_f32 v193, v193, v194\n"
        "v_mfma_f32_16x16x32_bf16 a[96:99], v[32:35], v[24:27], a[96:99]\n"
        "v_add_f32 v195, v195, v196\n"
        "v_cvt_pk_bf16_f32 v193, 0, v193\n"
        "v_mfma_f32_16x16x32_bf16 a[100:103], v[36:39], v[24:27], a[100:103]\n"
        "v_cvt_pk_bf16_f32 v195, 0, v195\n"
        "v_mul_f32 v194, v182, v186\n"
        "v_mfma_f32_16x16x32_bf16 a[100:103], v[68:71], v[28:31], a[100:103]\n"
        "v_mul_f32 v196, v183, v186\n"
        "v_cvt_pk_bf16_f32 v194, 0, v194\n"
        "v_mfma_f32_16x16x32_bf16 a[68:71], v[68:71], v[20:23], a[68:71]\n"
        "v_cvt_pk_bf16_f32 v196, 0, v196\n"
        "v_add_f32 v193, v193, v194\n"
        "v_mfma_f32_16x16x32_bf16 a[68:71], v[36:39], v[16:19], a[68:71]\n"
        "v_add_f32 v195, v195, v196\n"
        "v_cvt_pk_bf16_f32 v193, 0, v193\n"
        "v_mfma_f32_16x16x32_bf16 a[72:75], v[40:43], v[16:19], a[72:75]\n"
        "v_cvt_pk_bf16_f32 v195, 0, v195\n"
        "v_mul_f32 v194, v183, v185\n"
        "v_mfma_f32_16x16x32_bf16 a[72:75], v[72:75], v[20:23], a[72:75]\n"
        "v_mul_f32 v196, v184, v185\n"
        "v_cvt_pk_bf16_f32 v194, 0, v194\n"
        "v_mfma_f32_16x16x32_bf16 a[104:107], v[72:75], v[28:31], a[104:107]\n"
        "v_cvt_pk_bf16_f32 v196, 0, v196\n"
        "v_add_f32 v193, v193, v194\n"
        "v_mfma_f32_16x16x32_bf16 a[104:107], v[40:43], v[24:27], a[104:107]\n"
        "v_add_f32 v195, v195, v196\n"
        "v_cvt_pk_bf16_f32 v193, 0, v193\n"
        "v_mfma_f32_16x16x32_bf16 a[108:111], v[44:47], v[24:27], a[108:111]\n"
        "v_cvt_pk_bf16_f32 v195, 0, v195\n"
        "v_lshlrev_b32 v194, 16, v131\n"
        "v_mfma_f32_16x16x32_bf16 a[108:111], v[76:79], v[28:31], a[108:111]\n"
        "v_lshlrev_b32 v196, 16, v133\n"
        "v_mul_f32 v193, v193, v194\n"
        "v_mfma_f32_16x16x32_bf16 a[76:79], v[76:79], v[20:23], a[76:79]\n"
        "v_mul_f32 v195, v195, v196\n"
        "v_cvt_pk_bf16_f32 v200, v195, v193\n"
        "v_mfma_f32_16x16x32_bf16 a[76:79], v[44:47], v[16:19], a[76:79]\n"
        "global_load_dword v253, v173, s[82:83] offset:0\n"
        "s_add_u32 s82, s12, 0x600000\n"
        "s_addc_u32 s83, s13, 0\n"
        "v_mfma_f32_16x16x32_bf16 a[80:83], v[16:19], v[48:51], a[80:83]\n"
        "global_store_dwordx4 v171, v[200:203], s[76:77] offset:-4096 sc1\n"
        "v_lshlrev_b32 v185, 16, v140\n"
        "v_and_b32 v186, s35, v140\n"
        "v_lshlrev_b32 v187, 16, v141\n"
        "v_and_b32 v188, s35, v141\n"
        "v_mfma_f32_16x16x32_bf16 a[80:83], v[20:23], v[80:83], a[80:83]\n"
        "v_and_b32 v174, s35, v97\n"
        "v_and_b32 v175, s35, v99\n"
        "v_mfma_f32_16x16x32_bf16 a[112:115], v[28:31], v[80:83], a[112:115]\n"
        "v_and_b32 v176, s35, v101\n"
        "v_and_b32 v177, s35, v103\n"
        "v_mfma_f32_16x16x32_bf16 a[112:115], v[24:27], v[48:51], a[112:115]\n"
        "v_and_b32 v178, s35, v105\n"
        "v_and_b32 v179, s35, v107\n"
        "v_mfma_f32_16x16x32_bf16 a[116:119], v[24:27], v[52:55], a[116:119]\n"
        "v_and_b32 v180, s35, v109\n"
        "v_and_b32 v181, s35, v111\n"
        "v_mfma_f32_16x16x32_bf16 a[116:119], v[28:31], v[84:87], a[116:119]\n"
        "v_and_b32 v182, s35, v113\n"
        "v_and_b32 v183, s35, v115\n"
        "v_mfma_f32_16x16x32_bf16 a[84:87], v[20:23], v[84:87], a[84:87]\n"
        "v_and_b32 v184, s35, v117\n"
        "v_mul_f32 v189, v174, v188\n"
        "v_mfma_f32_16x16x32_bf16 a[84:87], v[16:19], v[52:55], a[84:87]\n"
        "v_mul_f32 v191, v175, v188\n"
        "v_cvt_pk_bf16_f32 v189, 0, v189\n"
        "v_mfma_f32_16x16x32_bf16 a[88:91], v[16:19], v[56:59], a[88:91]\n"
        "v_cvt_pk_bf16_f32 v191, 0, v191\n"
        "v_mul_f32 v190, v175, v187\n"
        "v_mfma_f32_16x16x32_bf16 a[88:91], v[20:23], v[88:91], a[88:91]\n"
        "v_mul_f32 v192, v176, v187\n"
        "v_cvt_pk_bf16_f32 v190, 0, v190\n"
        "v_mfma_f32_16x16x32_bf16 a[120:123], v[28:31], v[88:91], a[120:123]\n"
        "v_cvt_pk_bf16_f32 v192, 0, v192\n"
        "v_add_f32 v189, v189, v190\n"
        "v_mfma_f32_16x16x32_bf16 a[120:123], v[24:27], v[56:59], a[120:123]\n"
        "v_add_f32 v191, v191, v192\n"
        "v_cvt_pk_bf16_f32 v189, 0, v189\n"
        "v_mfma_f32_16x16x32_bf16 a[124:127], v[24:27], v[60:63], a[124:127]\n"
        "ds_read_b128 v[24:27], v224 offset:38912\n"
        "v_cvt_pk_bf16_f32 v191, 0, v191\n"
        "v_mfma_f32_16x16x32_bf16 a[124:127], v[28:31], v[92:95], a[124:127]\n"
        "ds_read_b128 v[28:31], v225 offset:38912\n"
        "v_mul_f32 v190, v176, v186\n"
        "v_mfma_f32_16x16x32_bf16 a[92:95], v[20:23], v[92:95], a[92:95]\n"
        "ds_read_b128 v[20:23], v225 offset:36864\n"
        "v_mul_f32 v192, v177, v186\n"
        "v_mfma_f32_16x16x32_bf16 a[92:95], v[16:19], v[60:63], a[92:95]\n"
        "ds_read_b128 v[16:19], v224 offset:36864\n"
        "s_waitcnt lgkmcnt(4)\n"
        "v_mfma_f32_16x16x32_bf16 a[128:131], v[32:35], v[0:3], a[128:131]\n"
        "v_cvt_pk_bf16_f32 v190, 0, v190\n"
        "v_cvt_pk_bf16_f32 v192, 0, v192\n"
        "v_mfma_f32_16x16x32_bf16 a[128:131], v[64:67], v[4:7], a[128:131]\n"
        "v_add_f32 v189, v189, v190\n"
        "v_add_f32 v191, v191, v192\n"
        "v_mfma_f32_16x16x32_bf16 a[160:163], v[64:67], v[12:15], a[160:163]\n"
        "v_cvt_pk_bf16_f32 v189, 0, v189\n"
        "v_cvt_pk_bf16_f32 v191, 0, v191\n"
        "v_mfma_f32_16x16x32_bf16 a[160:163], v[32:35], v[8:11], a[160:163]\n"
        "v_mul_f32 v190, v177, v185\n"
        "v_mul_f32 v192, v178, v185\n"
        "v_mfma_f32_16x16x32_bf16 a[164:167], v[36:39], v[8:11], a[164:167]\n"
        "v_cvt_pk_bf16_f32 v190, 0, v190\n"
        "v_cvt_pk_bf16_f32 v192, 0, v192\n"
        "v_mfma_f32_16x16x32_bf16 a[164:167], v[68:71], v[12:15], a[164:167]\n"
        "v_add_f32 v189, v189, v190\n"
        "v_add_f32 v191, v191, v192\n"
        "v_mfma_f32_16x16x32_bf16 a[132:135], v[68:71], v[4:7], a[132:135]\n"
        "v_cvt_pk_bf16_f32 v189, 0, v189\n"
        "v_cvt_pk_bf16_f32 v191, 0, v191\n"
        "v_mfma_f32_16x16x32_bf16 a[132:135], v[36:39], v[0:3], a[132:135]\n"
        "v_and_b32 v190, s35, v119\n"
        "v_and_b32 v192, s35, v121\n"
        "v_mfma_f32_16x16x32_bf16 a[136:139], v[40:43], v[0:3], a[136:139]\n"
        "v_mul_f32 v189, v189, v190\n"
        "v_mul_f32 v191, v191, v192\n"
        "v_mfma_f32_16x16x32_bf16 a[136:139], v[72:75], v[4:7], a[136:139]\n"
        "v_cvt_pk_bf16_f32 v211, v191, v189\n"
        "v_mul_f32 v193, v176, v188\n"
        "v_mfma_f32_16x16x32_bf16 a[168:171], v[72:75], v[12:15], a[168:171]\n"
        "v_mul_f32 v195, v177, v188\n"
        "v_cvt_pk_bf16_f32 v193, 0, v193\n"
        "v_mfma_f32_16x16x32_bf16 a[168:171], v[40:43], v[8:11], a[168:171]\n"
        "v_cvt_pk_bf16_f32 v195, 0, v195\n"
        "v_mul_f32 v194, v177, v187\n"
        "v_mfma_f32_16x16x32_bf16 a[172:175], v[44:47], v[8:11], a[172:175]\n"
        "v_mul_f32 v196, v178, v187\n"
        "v_cvt_pk_bf16_f32 v194, 0, v194\n"
        "v_mfma_f32_16x16x32_bf16 a[172:175], v[76:79], v[12:15], a[172:175]\n"
        "v_cvt_pk_bf16_f32 v196, 0, v196\n"
        "v_add_f32 v193, v193, v194\n"
        "v_mfma_f32_16x16x32_bf16 a[140:143], v[76:79], v[4:7], a[140:143]\n"
        "v_add_f32 v195, v195, v196\n"
        "v_cvt_pk_bf16_f32 v193, 0, v193\n"
        "v_mfma_f32_16x16x32_bf16 a[140:143], v[44:47], v[0:3], a[140:143]\n"
        "v_cvt_pk_bf16_f32 v195, 0, v195\n"
        "v_mul_f32 v194, v178, v186\n"
        "v_mfma_f32_16x16x32_bf16 a[144:147], v[0:3], v[48:51], a[144:147]\n"
        "v_mul_f32 v196, v179, v186\n"
        "v_cvt_pk_bf16_f32 v194, 0, v194\n"
        "v_mfma_f32_16x16x32_bf16 a[144:147], v[4:7], v[80:83], a[144:147]\n"
        "v_cvt_pk_bf16_f32 v196, 0, v196\n"
        "v_add_f32 v193, v193, v194\n"
        "v_mfma_f32_16x16x32_bf16 a[176:179], v[12:15], v[80:83], a[176:179]\n"
        "v_add_f32 v195, v195, v196\n"
        "v_cvt_pk_bf16_f32 v193, 0, v193\n"
        "v_mfma_f32_16x16x32_bf16 a[176:179], v[8:11], v[48:51], a[176:179]\n"
        "v_cvt_pk_bf16_f32 v195, 0, v195\n"
        "v_mul_f32 v194, v179, v185\n"
        "v_mfma_f32_16x16x32_bf16 a[180:183], v[8:11], v[52:55], a[180:183]\n"
        "v_mul_f32 v196, v180, v185\n"
        "v_cvt_pk_bf16_f32 v194, 0, v194\n"
        "v_mfma_f32_16x16x32_bf16 a[180:183], v[12:15], v[84:87], a[180:183]\n"
        "v_cvt_pk_bf16_f32 v196, 0, v196\n"
        "v_add_f32 v193, v193, v194\n"
        "v_mfma_f32_16x16x32_bf16 a[148:151], v[4:7], v[84:87], a[148:151]\n"
        "v_add_f32 v195, v195, v196\n"
        "v_cvt_pk_bf16_f32 v193, 0, v193\n"
        "v_mfma_f32_16x16x32_bf16 a[148:151], v[0:3], v[52:55], a[148:151]\n"
        "v_cvt_pk_bf16_f32 v195, 0, v195\n"
        "v_and_b32 v194, s35, v123\n"
        "v_mfma_f32_16x16x32_bf16 a[152:155], v[0:3], v[56:59], a[152:155]\n"
        "v_and_b32 v196, s35, v125\n"
        "v_mul_f32 v193, v193, v194\n"
        "v_mfma_f32_16x16x32_bf16 a[152:155], v[4:7], v[88:91], a[152:155]\n"
        "v_mul_f32 v195, v195, v196\n"
        "v_cvt_pk_bf16_f32 v210, v195, v193\n"
        "v_mfma_f32_16x16x32_bf16 a[184:187], v[12:15], v[88:91], a[184:187]\n"
        "v_mul_f32 v189, v178, v188\n"
        "v_mul_f32 v191, v179, v188\n"
        "v_mfma_f32_16x16x32_bf16 a[184:187], v[8:11], v[56:59], a[184:187]\n"
        "s_waitcnt vmcnt(11) lgkmcnt(0)\n"
        "v_cvt_pk_bf16_f32 v189, 0, v189\n"
        "v_cvt_pk_bf16_f32 v191, 0, v191\n"
        "v_mfma_f32_16x16x32_bf16 a[188:191], v[8:11], v[60:63], a[188:191]\n"
        "s_barrier\n"
        "v_mfma_f32_16x16x32_bf16 a[188:191], v[12:15], v[92:95], a[188:191]\n"
        "global_load_lds_dwordx4 v234, s[86:87] offset:0\n"
        "ds_read_b128 v[8:11], v224 offset:18432\n"
        "v_mfma_f32_16x16x32_bf16 a[156:159], v[4:7], v[92:95], a[156:159]\n"
        "ds_read_b128 v[4:7], v225 offset:16384\n"
        "v_mul_f32 v190, v179, v187\n"
        "v_mfma_f32_16x16x32_bf16 a[156:159], v[0:3], v[60:63], a[156:159]\n"
        "ds_read_b128 v[0:3], v224 offset:16384\n"
        "v_mul_f32 v192, v180, v187\n"
        "v_mfma_f32_16x16x32_bf16 a[192:195], v[32:35], v[16:19], a[192:195]\n"
        "ds_read_b128 v[12:15], v225 offset:18432\n"
        "v_cvt_pk_bf16_f32 v190, 0, v190\n"
        "v_mfma_f32_16x16x32_bf16 a[192:195], v[64:67], v[20:23], a[192:195]\n"
        "global_load_lds_dwordx4 v235, s[86:87] offset:1024\n"
        "v_cvt_pk_bf16_f32 v192, 0, v192\n"
        "v_mfma_f32_16x16x32_bf16 a[224:227], v[64:67], v[28:31], a[224:227]\n"
        "ds_read_b64_tr_b16 v[64:65], v226 offset:8192\n"
        "v_add_f32 v189, v189, v190\n"
        "v_mfma_f32_16x16x32_bf16 a[224:227], v[32:35], v[24:27], a[224:227]\n"
        "ds_read_b64_tr_b16 v[32:33], v226 offset:0\n"
        "v_add_f32 v191, v191, v192\n"
        "v_mfma_f32_16x16x32_bf16 a[228:231], v[36:39], v[24:27], a[228:231]\n"
        "ds_read_b64_tr_b16 v[34:35], v227 offset:0\n"
        "v_cvt_pk_bf16_f32 v189, 0, v189\n"
        "v_mfma_f32_16x16x32_bf16 a[228:231], v[68:71], v[28:31], a[228:231]\n"
        "ds_read_b64_tr_b16 v[66:67], v227 offset:8192\n"
        "global_load_lds_dwordx4 v236, s[86:87] offset:2048\n"
        "v_mfma_f32_16x16x32_bf16 a[196:199], v[68:71], v[20:23], a[196:199]\n"
        "ds_read_b64_tr_b16 v[68:69], v228 offset:8192\n"
        "v_cvt_pk_bf16_f32 v191, 0, v191\n"
        "v_mfma_f32_16x16x32_bf16 a[196:199], v[36:39], v[16:19], a[196:199]\n"
        "ds_read_b64_tr_b16 v[36:37], v228 offset:0\n"
        "v_mul_f32 v190, v180, v186\n"
        "v_mfma_f32_16x16x32_bf16 a[200:203], v[40:43], v[16:19], a[200:203]\n"
        "ds_read_b64_tr_b16 v[38:39], v229 offset:0\n"
        "v_mul_f32 v192, v181, v186\n"
        "v_mfma_f32_16x16x32_bf16 a[200:203], v[72:75], v[20:23], a[200:203]\n"
        "ds_read_b64_tr_b16 v[70:71], v229 offset:8192\n"
        "global_load_lds_dwordx4 v237, s[86:87] offset:3072\n"
        "s_add_u32 m0, s72, 0x9000\n"
        "v_mfma_f32_16x16x32_bf16 a[232:235], v[72:75], v[28:31], a[232:235]\n"
        "ds_read_b64_tr_b16 v[72:73], v230 offset:8192\n"
        "s_add_u32 s86, s58, 0x380\n"
        "s_addc_u32 s87, s59, 0\n"
        "v_mfma_f32_16x16x32_bf16 a[232:235], v[40:43], v[24:27], a[232:235]\n"
        "ds_read_b64_tr_b16 v[40:41], v230 offset:0\n"
        "v_cvt_pk_bf16_f32 v190, 0, v190\n"
        "v_mfma_f32_16x16x32_bf16 a[236:239], v[44:47], v[24:27], a[236:239]\n"
        "ds_read_b64_tr_b16 v[42:43], v231 offset:0\n"
        "v_cvt_pk_bf16_f32 v192, 0, v192\n"
        "v_mfma_f32_16x16x32_bf16 a[236:239], v[76:79], v[28:31], a[236:239]\n"
        "ds_read_b64_tr_b16 v[74:75], v231 offset:8192\n"
        "global_load_lds_dwordx4 v234, s[86:87] offset:0\n"
        "v_mfma_f32_16x16x32_bf16 a[204:207], v[76:79], v[20:23], a[204:207]\n"
        "ds_read_b64_tr_b16 v[76:77], v232 offset:8192\n"
        "v_add_f32 v189, v189, v190\n"
        "v_mfma_f32_16x16x32_bf16 a[204:207], v[44:47], v[16:19], a[204:207]\n"
        "ds_read_b64_tr_b16 v[44:45], v232 offset:0\n"
        "v_add_f32 v191, v191, v192\n"
        "v_mfma_f32_16x16x32_bf16 a[208:211], v[16:19], v[48:51], a[208:211]\n"
        "ds_read_b64_tr_b16 v[46:47], v233 offset:0\n"
        "v_cvt_pk_bf16_f32 v189, 0, v189\n"
        "v_mfma_f32_16x16x32_bf16 a[208:211], v[20:23], v[80:83], a[208:211]\n"
        "ds_read_b64_tr_b16 v[78:79], v233 offset:8192\n"
        "global_load_lds_dwordx4 v235, s[86:87] offset:1024\n"
        "v_mfma_f32_16x16x32_bf16 a[240:243], v[28:31], v[80:83], a[240:243]\n"
        "ds_read_b64_tr_b16 v[80:81], v226 offset:24576\n"
        "v_cvt_pk_bf16_f32 v191, 0, v191\n"
        "v_mfma_f32_16x16x32_bf16 a[240:243], v[24:27], v[48:51], a[240:243]\n"
        "ds_read_b64_tr_b16 v[48:49], v226 offset:16384\n"
        "v_mul_f32 v190, v181, v185\n"
        "v_mfma_f32_16x16x32_bf16 a[244:247], v[24:27], v[52:55], a[244:247]\n"
        "ds_read_b64_tr_b16 v[50:51], v227 offset:16384\n"
        "v_mul_f32 v192, v182, v185\n"
        "v_mfma_f32_16x16x32_bf16 a[244:247], v[28:31], v[84:87], a[244:247]\n"
        "ds_read_b64_tr_b16 v[82:83], v227 offset:24576\n"
        "global_load_lds_dwordx4 v236, s[86:87] offset:2048\n"
        "v_mfma_f32_16x16x32_bf16 a[212:215], v[20:23], v[84:87], a[212:215]\n"
        "ds_read_b64_tr_b16 v[84:85], v228 offset:24576\n"
        "v_cvt_pk_bf16_f32 v190, 0, v190\n"
        "v_mfma_f32_16x16x32_bf16 a[212:215], v[16:19], v[52:55], a[212:215]\n"
        "ds_read_b64_tr_b16 v[52:53], v228 offset:16384\n"
        "v_cvt_pk_bf16_f32 v192, 0, v192\n"
        "v_mfma_f32_16x16x32_bf16 a[216:219], v[16:19], v[56:59], a[216:219]\n"
        "ds_read_b64_tr_b16 v[54:55], v229 offset:16384\n"
        "v_add_f32 v189, v189, v190\n"
        "v_mfma_f32_16x16x32_bf16 a[216:219], v[20:23], v[88:91], a[216:219]\n"
        "ds_read_b64_tr_b16 v[86:87], v229 offset:24576\n"
        "global_load_lds_dwordx4 v237, s[86:87] offset:3072\n"
        "s_add_u32 m0, s73, 0x20000\n"
        "v_mfma_f32_16x16x32_bf16 a[248:251], v[28:31], v[88:91], a[248:251]\n"
        "ds_read_b64_tr_b16 v[88:89], v230 offset:24576\n"
        "s_add_u32 s86, s68, 0x80000\n"
        "s_addc_u32 s87, s69, 0\n"
        "v_mfma_f32_16x16x32_bf16 a[248:251], v[24:27], v[56:59], a[248:251]\n"
        "ds_read_b64_tr_b16 v[56:57], v230 offset:16384\n"
        "v_add_f32 v191, v191, v192\n"
        "v_mfma_f32_16x16x32_bf16 a[252:255], v[24:27], v[60:63], a[252:255]\n"
        "ds_read_b64_tr_b16 v[58:59], v231 offset:16384\n"
        "v_cvt_pk_bf16_f32 v189, 0, v189\n"
        "v_mfma_f32_16x16x32_bf16 a[252:255], v[28:31], v[92:95], a[252:255]\n"
        "ds_read_b64_tr_b16 v[90:91], v231 offset:24576\n"
        "global_load_lds_dwordx4 v238, s[68:69] offset:0\n"
        "v_mfma_f32_16x16x32_bf16 a[220:223], v[20:23], v[92:95], a[220:223]\n"
        "ds_read_b64_tr_b16 v[92:93], v232 offset:24576\n"
        "v_cvt_pk_bf16_f32 v191, 0, v191\n"
        "v_mfma_f32_16x16x32_bf16 a[220:223], v[16:19], v[60:63], a[220:223]\n"
        "ds_read_b64_tr_b16 v[60:61], v232 offset:16384\n"
        "s_waitcnt lgkmcnt(8)\n"
        "v_mfma_f32_16x16x32_bf16 a[0:3], v[32:35], v[0:3], a[0:3]\n"
        "ds_read_b64_tr_b16 v[62:63], v233 offset:16384\n"
        "v_and_b32 v190, s35, v127\n"
        "v_mfma_f32_16x16x32_bf16 a[0:3], v[64:67], v[4:7], a[0:3]\n"
        "ds_read_b64_tr_b16 v[94:95], v233 offset:24576\n"
        "global_load_lds_dwordx4 v239, s[68:69] offset:1024\n"
        "v_mfma_f32_16x16x32_bf16 a[32:35], v[64:67], v[12:15], a[32:35]\n"
        "ds_read_b128 v[16:19], v224 offset:20480\n"
        "v_and_b32 v192, s35, v129\n"
        "v_mfma_f32_16x16x32_bf16 a[32:35], v[32:35], v[8:11], a[32:35]\n"
        "ds_read_b128 v[20:23], v225 offset:20480\n"
        "v_mul_f32 v189, v189, v190\n"
        "v_mfma_f32_16x16x32_bf16 a[36:39], v[36:39], v[8:11], a[36:39]\n"
        "ds_read_b128 v[24:27], v224 offset:22528\n"
        "v_mul_f32 v191, v191, v192\n"
        "v_mfma_f32_16x16x32_bf16 a[36:39], v[68:71], v[12:15], a[36:39]\n"
        "ds_read_b128 v[28:31], v225 offset:22528\n"
        "global_load_lds_dwordx4 v240, s[68:69] offset:2048\n"
        "v_mfma_f32_16x16x32_bf16 a[4:7], v[68:71], v[4:7], a[4:7]\n"
        "v_cvt_pk_bf16_f32 v209, v191, v189\n"
        "v_mul_f32 v193, v180, v188\n"
        "v_mfma_f32_16x16x32_bf16 a[4:7], v[36:39], v[0:3], a[4:7]\n"
        "v_mul_f32 v195, v181, v188\n"
        "v_cvt_pk_bf16_f32 v193, 0, v193\n"
        "v_mfma_f32_16x16x32_bf16 a[8:11], v[40:43], v[0:3], a[8:11]\n"
        "v_cvt_pk_bf16_f32 v195, 0, v195\n"
        "v_mul_f32 v194, v181, v187\n"
        "v_mfma_f32_16x16x32_bf16 a[8:11], v[72:75], v[4:7], a[8:11]\n"
        "global_load_lds_dwordx4 v241, s[68:69] offset:3072\n"
        "s_add_u32 m0, s73, 0x24000\n"
        "v_mfma_f32_16x16x32_bf16 a[40:43], v[72:75], v[12:15], a[40:43]\n"
        "v_mul_f32 v196, v182, v187\n"
        "v_cvt_pk_bf16_f32 v194, 0, v194\n"
        "v_mfma_f32_16x16x32_bf16 a[40:43], v[40:43], v[8:11], a[40:43]\n"
        "v_cvt_pk_bf16_f32 v196, 0, v196\n"
        "v_add_f32 v193, v193, v194\n"
        "v_mfma_f32_16x16x32_bf16 a[44:47], v[44:47], v[8:11], a[44:47]\n"
        "v_add_f32 v195, v195, v196\n"
        "v_cvt_pk_bf16_f32 v193, 0, v193\n"
        "v_mfma_f32_16x16x32_bf16 a[44:47], v[76:79], v[12:15], a[44:47]\n"
        "global_load_lds_dwordx4 v238, s[86:87] offset:0\n"
        "v_mfma_f32_16x16x32_bf16 a[12:15], v[76:79], v[4:7], a[12:15]\n"
        "v_cvt_pk_bf16_f32 v195, 0, v195\n"
        "v_mul_f32 v194, v182, v186\n"
        "v_mfma_f32_16x16x32_bf16 a[12:15], v[44:47], v[0:3], a[12:15]\n"
        "v_mul_f32 v196, v183, v186\n"
        "v_cvt_pk_bf16_f32 v194, 0, v194\n"
        "v_mfma_f32_16x16x32_bf16 a[16:19], v[0:3], v[48:51], a[16:19]\n"
        "s_waitcnt lgkmcnt(0)\n"
        "v_cvt_pk_bf16_f32 v196, 0, v196\n"
        "v_add_f32 v193, v193, v194\n"
        "v_mfma_f32_16x16x32_bf16 a[16:19], v[4:7], v[80:83], a[16:19]\n"
        "global_load_lds_dwordx4 v239, s[86:87] offset:1024\n"
        "v_mfma_f32_16x16x32_bf16 a[48:51], v[12:15], v[80:83], a[48:51]\n"
        "v_add_f32 v195, v195, v196\n"
        "v_cvt_pk_bf16_f32 v193, 0, v193\n"
        "v_mfma_f32_16x16x32_bf16 a[48:51], v[8:11], v[48:51], a[48:51]\n"
        "v_cvt_pk_bf16_f32 v195, 0, v195\n"
        "v_mul_f32 v194, v183, v185\n"
        "v_mfma_f32_16x16x32_bf16 a[52:55], v[8:11], v[52:55], a[52:55]\n"
        "v_mul_f32 v196, v184, v185\n"
        "v_cvt_pk_bf16_f32 v194, 0, v194\n"
        "v_mfma_f32_16x16x32_bf16 a[52:55], v[12:15], v[84:87], a[52:55]\n"
        "global_load_lds_dwordx4 v240, s[86:87] offset:2048\n"
        "v_mfma_f32_16x16x32_bf16 a[20:23], v[4:7], v[84:87], a[20:23]\n"
        "v_cvt_pk_bf16_f32 v196, 0, v196\n"
        "v_add_f32 v193, v193, v194\n"
        "v_mfma_f32_16x16x32_bf16 a[20:23], v[0:3], v[52:55], a[20:23]\n"
        "s_and_b32 s53, s40, 1\n"
        "v_add_f32 v195, v195, v196\n"
        "v_cvt_pk_bf16_f32 v193, 0, v193\n"
        "v_mfma_f32_16x16x32_bf16 a[24:27], v[0:3], v[56:59], a[24:27]\n"
        "s_lshl_b32 s53, s53, 10\n"
        "v_cvt_pk_bf16_f32 v195, 0, v195\n"
        "v_and_b32 v194, s35, v131\n"
        "v_mfma_f32_16x16x32_bf16 a[24:27], v[4:7], v[88:91], a[24:27]\n"
        "global_load_lds_dwordx4 v241, s[86:87] offset:3072\n"
        "s_add_u32 m0, s72, 0x0\n"
        "v_mfma_f32_16x16x32_bf16 a[56:59], v[12:15], v[88:91], a[56:59]\n"
        "s_add_u32 s86, s64, 0x80\n"
        "s_addc_u32 s87, s65, 0\n"
        "v_and_b32 v196, s35, v133\n"
        "v_mfma_f32_16x16x32_bf16 a[56:59], v[8:11], v[56:59], a[56:59]\n"
        "s_sub_u32 s53, 0x8000, s53\n"
        "v_mul_f32 v193, v193, v194\n"
        "v_mul_f32 v195, v195, v196\n"
        "v_mfma_f32_16x16x32_bf16 a[60:63], v[8:11], v[60:63], a[60:63]\n"
        "ds_read_b128 v[8:11], v224 offset:2048\n"
        "v_cvt_pk_bf16_f32 v208, v195, v193\n"
        "v_mfma_f32_16x16x32_bf16 a[60:63], v[12:15], v[92:95], a[60:63]\n"
        "ds_read_b128 v[12:15], v225 offset:2048\n"
        "v_readfirstlane_b32 s70, v250\n"
        "v_mfma_f32_16x16x32_bf16 a[28:31], v[4:7], v[92:95], a[28:31]\n"
        "ds_read_b128 v[4:7], v225 offset:0\n"
        "v_readfirstlane_b32 s71, v251\n"
        "v_mfma_f32_16x16x32_bf16 a[28:31], v[0:3], v[60:63], a[28:31]\n"
        "ds_read_b128 v[0:3], v224 offset:0\n"
        "s_add_u32 s70, s70, 0x1\n"
        "s_addc_u32 s71, s71, 0\n"
        "v_mfma_f32_16x16x32_bf16 a[64:67], v[32:35], v[16:19], a[64:67]\n"
        "global_store_dwordx4 v171, v[208:211], s[76:77] offset:0 sc1\n"
        "s_add_u32 s76, s38, 0x3b20040\n"
        "s_addc_u32 s77, s39, 0\n"
        "v_mov_b32 v250, s70\n"
        "v_mov_b32 v251, s71\n"
        "v_lshlrev_b32 v174, 16, v158\n"
        "v_mfma_f32_16x16x32_bf16 a[64:67], v[64:67], v[20:23], a[64:67]\n"
        "v_and_b32 v175, s35, v158\n"
        "v_lshlrev_b32 v176, 16, v159\n"
        "v_mfma_f32_16x16x32_bf16 a[96:99], v[64:67], v[28:31], a[96:99]\n"
        "v_and_b32 v177, s35, v159\n"
        "v_lshlrev_b32 v178, 16, v160\n"
        "v_mfma_f32_16x16x32_bf16 a[96:99], v[32:35], v[24:27], a[96:99]\n"
        "v_and_b32 v179, s35, v160\n"
        "v_lshlrev_b32 v180, 16, v161\n"
        "v_mfma_f32_16x16x32_bf16 a[100:103], v[36:39], v[24:27], a[100:103]\n"
        "v_and_b32 v181, s35, v161\n"
        "v_lshlrev_b32 v182, 16, v162\n"
        "v_mfma_f32_16x16x32_bf16 a[100:103], v[68:71], v[28:31], a[100:103]\n"
        "v_and_b32 v183, s35, v162\n"
        "v_lshlrev_b32 v184, 16, v163\n"
        "v_mfma_f32_16x16x32_bf16 a[68:71], v[68:71], v[20:23], a[68:71]\n"
        "v_and_b32 v185, s35, v163\n"
        "v_lshlrev_b32 v186, 16, v164\n"
        "v_mfma_f32_16x16x32_bf16 a[68:71], v[36:39], v[16:19], a[68:71]\n"
        "v_and_b32 v187, s35, v164\n"
        "v_lshlrev_b32 v188, 16, v165\n"
        "v_mfma_f32_16x16x32_bf16 a[72:75], v[40:43], v[16:19], a[72:75]\n"
        "v_and_b32 v189, s35, v165\n"
        "v_lshlrev_b32 v192, 16, v142\n"
        "v_mfma_f32_16x16x32_bf16 a[72:75], v[72:75], v[20:23], a[72:75]\n"
        "v_and_b32 v193, s35, v142\n"
        "v_mul_f32 v192, v192, v174\n"
        "v_mfma_f32_16x16x32_bf16 a[104:107], v[72:75], v[28:31], a[104:107]\n"
        "v_mul_f32 v193, v193, v175\n"
        "v_cvt_pk_bf16_f32 v192, 0, v192\n"
        "v_mfma_f32_16x16x32_bf16 a[104:107], v[40:43], v[24:27], a[104:107]\n"
        "v_cvt_pk_bf16_f32 v193, 0, v193\n"
        "v_add_f32 v190, v192, v193\n"
        "v_mfma_f32_16x16x32_bf16 a[108:111], v[44:47], v[24:27], a[108:111]\n"
        "v_lshlrev_b32 v194, 16, v143\n"
        "v_and_b32 v195, s35, v143\n"
        "v_mfma_f32_16x16x32_bf16 a[108:111], v[76:79], v[28:31], a[108:111]\n"
        "v_mul_f32 v194, v194, v176\n"
        "v_mul_f32 v195, v195, v177\n"
        "v_mfma_f32_16x16x32_bf16 a[76:79], v[76:79], v[20:23], a[76:79]\n"
        "v_cvt_pk_bf16_f32 v194, 0, v194\n"
        "v_cvt_pk_bf16_f32 v195, 0, v195\n"
        "v_mfma_f32_16x16x32_bf16 a[76:79], v[44:47], v[16:19], a[76:79]\n"
        "global_load_dword v253, v173, s[82:83] offset:0\n"
        "s_add_u32 s82, s12, 0x700000\n"
        "s_addc_u32 s83, s13, 0\n"
        "v_mfma_f32_16x16x32_bf16 a[80:83], v[16:19], v[48:51], a[80:83]\n"
        "v_add_f32 v194, v194, v195\n"
        "v_add_f32 v190, v190, v194\n"
        "v_mfma_f32_16x16x32_bf16 a[80:83], v[20:23], v[80:83], a[80:83]\n"
        "v_lshlrev_b32 v192, 16, v144\n"
        "v_and_b32 v193, s35, v144\n"
        "v_mfma_f32_16x16x32_bf16 a[112:115], v[28:31], v[80:83], a[112:115]\n"
        "v_mul_f32 v192, v192, v178\n"
        "v_mul_f32 v193, v193, v179\n"
        "v_mfma_f32_16x16x32_bf16 a[112:115], v[24:27], v[48:51], a[112:115]\n"
        "v_cvt_pk_bf16_f32 v192, 0, v192\n"
        "v_cvt_pk_bf16_f32 v193, 0, v193\n"
        "v_mfma_f32_16x16x32_bf16 a[116:119], v[24:27], v[52:55], a[116:119]\n"
        "LHA3_%=:\n"
        "s_cbranch_execz LHA7_%=\n"
        "v_add_f32 v192, v192, v193\n"
        "v_add_f32 v190, v190, v192\n"
        "v_mfma_f32_16x16x32_bf16 a[116:119], v[28:31], v[84:87], a[116:119]\n"
        "v_lshlrev_b32 v194, 16, v145\n"
        "v_and_b32 v195, s35, v145\n"
        "v_mfma_f32_16x16x32_bf16 a[84:87], v[20:23], v[84:87], a[84:87]\n"
        "v_mul_f32 v194, v194, v180\n"
        "v_mul_f32 v195, v195, v181\n"
        "v_mfma_f32_16x16x32_bf16 a[84:87], v[16:19], v[52:55], a[84:87]\n"
        "s_mov_b32 s88, 0x3e0293ee\n"
        "v_cvt_pk_bf16_f32 v194, 0, v194\n"
        "v_cvt_pk_bf16_f32 v195, 0, v195\n"
        "v_mfma_f32_16x16x32_bf16 a[88:91], v[16:19], v[56:59], a[88:91]\n"
        "s_sub_u32 s75, s45, s44\n"
        "v_add_f32 v194, v194, v195\n"
        "v_add_f32 v190, v190, v194\n"
        "v_mfma_f32_16x16x32_bf16 a[88:91], v[20:23], v[88:91], a[88:91]\n"
        "s_lshl_b32 s75, s75, 15\n"
        "v_lshlrev_b32 v192, 16, v146\n"
        "v_and_b32 v193, s35, v146\n"
        "v_mfma_f32_16x16x32_bf16 a[120:123], v[28:31], v[88:91], a[120:123]\n"
        "s_lshr_b32 s50, s40, 1\n"
        "v_mul_f32 v192, v192, v182\n"
        "v_mul_f32 v193, v193, v183\n"
        "v_mfma_f32_16x16x32_bf16 a[120:123], v[24:27], v[56:59], a[120:123]\n"
        "s_lshl1_add_u32 s50, s44, s50\n"
        "v_cvt_pk_bf16_f32 v192, 0, v192\n"
        "v_cvt_pk_bf16_f32 v193, 0, v193\n"
        "v_mfma_f32_16x16x32_bf16 a[124:127], v[24:27], v[60:63], a[124:127]\n"
        "ds_read_b128 v[24:27], v224 offset:6144\n"
        "s_lshl_b32 s50, s50, 14\n"
        "v_add_f32 v192, v192, v193\n"
        "v_mfma_f32_16x16x32_bf16 a[124:127], v[28:31], v[92:95], a[124:127]\n"
        "ds_read_b128 v[28:31], v225 offset:6144\n"
        "s_mul_i32 s51, s43, 529408\n"
        "v_add_f32 v190, v190, v192\n"
        "v_mfma_f32_16x16x32_bf16 a[92:95], v[20:23], v[92:95], a[92:95]\n"
        "ds_read_b128 v[20:23], v225 offset:4096\n"
        "s_add_u32 s50, s50, s51\n"
        "v_lshlrev_b32 v194, 16, v147\n"
        "v_mfma_f32_16x16x32_bf16 a[92:95], v[16:19], v[60:63], a[92:95]\n"
        "ds_read_b128 v[16:19], v224 offset:4096\n"
        "s_and_b32 s51, s40, 1\n"
        "s_waitcnt lgkmcnt(4)\n"
        "v_mfma_f32_16x16x32_bf16 a[128:131], v[32:35], v[0:3], a[128:131]\n"
        "s_lshl_b32 s51, s51, 11\n"
        "v_and_b32 v195, s35, v147\n"
        "v_mul_f32 v194, v194, v184\n"
        "v_mfma_f32_16x16x32_bf16 a[128:131], v[64:67], v[4:7], a[128:131]\n"
        "s_add_u32 s52, s50, s51\n"
        "v_mul_f32 v195, v195, v185\n"
        "v_cvt_pk_bf16_f32 v194, 0, v194\n"
        "v_mfma_f32_16x16x32_bf16 a[160:163], v[64:67], v[12:15], a[160:163]\n"
        "v_cvt_pk_bf16_f32 v195, 0, v195\n"
        "v_add_f32 v194, v194, v195\n"
        "v_mfma_f32_16x16x32_bf16 a[160:163], v[32:35], v[8:11], a[160:163]\n"
        "v_add_f32 v190, v190, v194\n"
        "v_lshlrev_b32 v192, 16, v148\n"
        "v_mfma_f32_16x16x32_bf16 a[164:167], v[36:39], v[8:11], a[164:167]\n"
        "v_and_b32 v193, s35, v148\n"
        "v_mul_f32 v192, v192, v186\n"
        "v_mfma_f32_16x16x32_bf16 a[164:167], v[68:71], v[12:15], a[164:167]\n"
        "v_mul_f32 v193, v193, v187\n"
        "v_cvt_pk_bf16_f32 v192, 0, v192\n"
        "v_mfma_f32_16x16x32_bf16 a[132:135], v[68:71], v[4:7], a[132:135]\n"
        "v_cvt_pk_bf16_f32 v193, 0, v193\n"
        "v_add_f32 v192, v192, v193\n"
        "v_mfma_f32_16x16x32_bf16 a[132:135], v[36:39], v[0:3], a[132:135]\n"
        "v_add_f32 v190, v190, v192\n"
        "v_lshlrev_b32 v194, 16, v149\n"
        "v_mfma_f32_16x16x32_bf16 a[136:139], v[40:43], v[0:3], a[136:139]\n"
        "v_and_b32 v195, s35, v149\n"
        "v_mul_f32 v194, v194, v188\n"
        "v_mfma_f32_16x16x32_bf16 a[136:139], v[72:75], v[4:7], a[136:139]\n"
        "v_mul_f32 v195, v195, v189\n"
        "v_cvt_pk_bf16_f32 v194, 0, v194\n"
        "v_mfma_f32_16x16x32_bf16 a[168:171], v[72:75], v[12:15], a[168:171]\n"
        "v_cvt_pk_bf16_f32 v195, 0, v195\n"
        "v_add_f32 v194, v194, v195\n"
        "v_mfma_f32_16x16x32_bf16 a[168:171], v[40:43], v[8:11], a[168:171]\n"
        "v_add_f32 v190, v190, v194\n"
        "v_lshlrev_b32 v192, 16, v150\n"
        "v_mfma_f32_16x16x32_bf16 a[172:175], v[44:47], v[8:11], a[172:175]\n"
        "v_and_b32 v193, s35, v150\n"
        "v_mul_f32 v192, v192, v174\n"
        "v_mfma_f32_16x16x32_bf16 a[172:175], v[76:79], v[12:15], a[172:175]\n"
        "v_mul_f32 v193, v193, v175\n"
        "v_cvt_pk_bf16_f32 v192, 0, v192\n"
        "v_mfma_f32_16x16x32_bf16 a[140:143], v[76:79], v[4:7], a[140:143]\n"
        "v_cvt_pk_bf16_f32 v193, 0, v193\n"
        "v_add_f32 v191, v192, v193\n"
        "v_mfma_f32_16x16x32_bf16 a[140:143], v[44:47], v[0:3], a[140:143]\n"
        "v_lshlrev_b32 v194, 16, v151\n"
        "v_and_b32 v195, s35, v151\n"
        "v_mfma_f32_16x16x32_bf16 a[144:147], v[0:3], v[48:51], a[144:147]\n"
        "v_mul_f32 v194, v194, v176\n"
        "v_mul_f32 v195, v195, v177\n"
        "v_mfma_f32_16x16x32_bf16 a[144:147], v[4:7], v[80:83], a[144:147]\n"
        "v_cvt_pk_bf16_f32 v194, 0, v194\n"
        "v_cvt_pk_bf16_f32 v195, 0, v195\n"
        "v_mfma_f32_16x16x32_bf16 a[176:179], v[12:15], v[80:83], a[176:179]\n"
        "v_add_f32 v194, v194, v195\n"
        "v_add_f32 v191, v191, v194\n"
        "v_mfma_f32_16x16x32_bf16 a[176:179], v[8:11], v[48:51], a[176:179]\n"
        "v_lshlrev_b32 v192, 16, v152\n"
        "v_and_b32 v193, s35, v152\n"
        "v_mfma_f32_16x16x32_bf16 a[180:183], v[8:11], v[52:55], a[180:183]\n"
        "v_mul_f32 v192, v192, v178\n"
        "v_mul_f32 v193, v193, v179\n"
        "v_mfma_f32_16x16x32_bf16 a[180:183], v[12:15], v[84:87], a[180:183]\n"
        "v_cvt_pk_bf16_f32 v192, 0, v192\n"
        "v_cvt_pk_bf16_f32 v193, 0, v193\n"
        "v_mfma_f32_16x16x32_bf16 a[148:151], v[4:7], v[84:87], a[148:151]\n"
        "v_add_f32 v192, v192, v193\n"
        "v_add_f32 v191, v191, v192\n"
        "v_mfma_f32_16x16x32_bf16 a[148:151], v[0:3], v[52:55], a[148:151]\n"
        "s_waitcnt vmcnt(26) lgkmcnt(0)\n"
        "v_lshlrev_b32 v194, 16, v153\n"
        "v_and_b32 v195, s35, v153\n"
        "v_mfma_f32_16x16x32_bf16 a[152:155], v[0:3], v[56:59], a[152:155]\n"
        "v_mul_f32 v194, v194, v180\n"
        "v_mul_f32 v195, v195, v181\n"
        "v_mfma_f32_16x16x32_bf16 a[152:155], v[4:7], v[88:91], a[152:155]\n"
        "v_cvt_pk_bf16_f32 v194, 0, v194\n"
        "v_cvt_pk_bf16_f32 v195, 0, v195\n"
        "v_mfma_f32_16x16x32_bf16 a[184:187], v[12:15], v[88:91], a[184:187]\n"
        "v_add_f32 v194, v194, v195\n"
        "v_add_f32 v191, v191, v194\n"
        "v_mfma_f32_16x16x32_bf16 a[184:187], v[8:11], v[56:59], a[184:187]\n"
        "s_waitcnt vmcnt(10)\n"
        "v_lshlrev_b32 v192, 16, v154\n"
        "v_and_b32 v193, s35, v154\n"
        "v_mfma_f32_16x16x32_bf16 a[188:191], v[8:11], v[60:63], a[188:191]\n"
        "s_barrier\n"
        "v_mfma_f32_16x16x32_bf16 a[188:191], v[12:15], v[92:95], a[188:191]\n"
        "global_load_lds_dwordx4 v242, s[64:65] offset:0\n"
        "ds_read_b128 v[8:11], v224 offset:51200\n"
        "v_mfma_f32_16x16x32_bf16 a[156:159], v[4:7], v[92:95], a[156:159]\n"
        "ds_read_b128 v[4:7], v225 offset:49152\n"
        "v_mul_f32 v192, v192, v182\n"
        "v_mfma_f32_16x16x32_bf16 a[156:159], v[0:3], v[60:63], a[156:159]\n"
        "ds_read_b128 v[0:3], v224 offset:49152\n"
        "v_mul_f32 v193, v193, v183\n"
        "v_mfma_f32_16x16x32_bf16 a[192:195], v[32:35], v[16:19], a[192:195]\n"
        "ds_read_b128 v[12:15], v225 offset:51200\n"
        "v_cvt_pk_bf16_f32 v192, 0, v192\n"
        "v_mfma_f32_16x16x32_bf16 a[192:195], v[64:67], v[20:23], a[192:195]\n"
        "global_load_lds_dwordx4 v243, s[64:65] offset:1024\n"
        "v_cvt_pk_bf16_f32 v193, 0, v193\n"
        "v_mfma_f32_16x16x32_bf16 a[224:227], v[64:67], v[28:31], a[224:227]\n"
        "ds_read_b64_tr_b16 v[64:65], v226 offset:40960\n"
        "v_add_f32 v192, v192, v193\n"
        "v_mfma_f32_16x16x32_bf16 a[224:227], v[32:35], v[24:27], a[224:227]\n"
        "ds_read_b64_tr_b16 v[32:33], v226 offset:32768\n"
        "s_add_u32 s97, s52, 0x3000\n"
        "v_add_f32 v191, v191, v192\n"
        "v_mfma_f32_16x16x32_bf16 a[228:231], v[36:39], v[24:27], a[228:231]\n"
        "ds_read_b64_tr_b16 v[34:35], v227 offset:32768\n"
        "v_lshlrev_b32 v194, 16, v155\n"
        "v_mfma_f32_16x16x32_bf16 a[228:231], v[68:71], v[28:31], a[228:231]\n"
        "ds_read_b64_tr_b16 v[66:67], v227 offset:40960\n"
        "global_load_lds_dwordx4 v244, s[64:65] offset:2048\n"
        "v_mfma_f32_16x16x32_bf16 a[196:199], v[68:71], v[20:23], a[196:199]\n"
        "ds_read_b64_tr_b16 v[68:69], v228 offset:40960\n"
        "v_and_b32 v195, s35, v155\n"
        "v_mfma_f32_16x16x32_bf16 a[196:199], v[36:39], v[16:19], a[196:199]\n"
        "ds_read_b64_tr_b16 v[36:37], v228 offset:32768\n"
        "v_mul_f32 v194, v194, v184\n"
        "v_mfma_f32_16x16x32_bf16 a[200:203], v[40:43], v[16:19], a[200:203]\n"
        "ds_read_b64_tr_b16 v[38:39], v229 offset:32768\n"
        "v_mul_f32 v195, v195, v185\n"
        "v_mfma_f32_16x16x32_bf16 a[200:203], v[72:75], v[20:23], a[200:203]\n"
        "ds_read_b64_tr_b16 v[70:71], v229 offset:40960\n"
        "global_load_lds_dwordx4 v245, s[64:65] offset:3072\n"
        "s_add_u32 m0, s72, 0x1000\n"
        "v_mfma_f32_16x16x32_bf16 a[232:235], v[72:75], v[28:31], a[232:235]\n"
        "ds_read_b64_tr_b16 v[72:73], v230 offset:40960\n"
        "v_cvt_pk_bf16_f32 v194, 0, v194\n"
        "v_mfma_f32_16x16x32_bf16 a[232:235], v[40:43], v[24:27], a[232:235]\n"
        "ds_read_b64_tr_b16 v[40:41], v230 offset:32768\n"
        "v_cvt_pk_bf16_f32 v195, 0, v195\n"
        "v_mfma_f32_16x16x32_bf16 a[236:239], v[44:47], v[24:27], a[236:239]\n"
        "ds_read_b64_tr_b16 v[42:43], v231 offset:32768\n"
        "v_add_f32 v194, v194, v195\n"
        "v_mfma_f32_16x16x32_bf16 a[236:239], v[76:79], v[28:31], a[236:239]\n"
        "ds_read_b64_tr_b16 v[74:75], v231 offset:40960\n"
        "global_load_lds_dwordx4 v242, s[66:67] offset:0\n"
        "v_mfma_f32_16x16x32_bf16 a[204:207], v[76:79], v[20:23], a[204:207]\n"
        "ds_read_b64_tr_b16 v[76:77], v232 offset:40960\n"
        "v_add_f32 v191, v191, v194\n"
        "v_mfma_f32_16x16x32_bf16 a[204:207], v[44:47], v[16:19], a[204:207]\n"
        "ds_read_b64_tr_b16 v[44:45], v232 offset:32768\n"
        "v_lshlrev_b32 v192, 16, v156\n"
        "v_mfma_f32_16x16x32_bf16 a[208:211], v[16:19], v[48:51], a[208:211]\n"
        "ds_read_b64_tr_b16 v[46:47], v233 offset:32768\n"
        "v_and_b32 v193, s35, v156\n"
        "v_mfma_f32_16x16x32_bf16 a[208:211], v[20:23], v[80:83], a[208:211]\n"
        "ds_read_b64_tr_b16 v[78:79], v233 offset:40960\n"
        "global_load_lds_dwordx4 v243, s[66:67] offset:1024\n"
        "v_mfma_f32_16x16x32_bf16 a[240:243], v[28:31], v[80:83], a[240:243]\n"
        "ds_read_b64_tr_b16 v[80:81], v226 offset:57344\n"
        "v_mul_f32 v192, v192, v186\n"
        "v_mfma_f32_16x16x32_bf16 a[240:243], v[24:27], v[48:51], a[240:243]\n"
        "ds_read_b64_tr_b16 v[48:49], v226 offset:49152\n"
        "v_mul_f32 v193, v193, v187\n"
        "v_mfma_f32_16x16x32_bf16 a[244:247], v[24:27], v[52:55], a[244:247]\n"
        "ds_read_b64_tr_b16 v[50:51], v227 offset:49152\n"
        "v_cvt_pk_bf16_f32 v192, 0, v192\n"
        "v_mfma_f32_16x16x32_bf16 a[244:247], v[28:31], v[84:87], a[244:247]\n"
        "ds_read_b64_tr_b16 v[82:83], v227 offset:57344\n"
        "global_load_lds_dwordx4 v244, s[66:67] offset:2048\n"
        "v_mfma_f32_16x16x32_bf16 a[212:215], v[20:23], v[84:87], a[212:215]\n"
        "ds_read_b64_tr_b16 v[84:85], v228 offset:57344\n"
        "v_cvt_pk_bf16_f32 v193, 0, v193\n"
        "v_mfma_f32_16x16x32_bf16 a[212:215], v[16:19], v[52:55], a[212:215]\n"
        "ds_read_b64_tr_b16 v[52:53], v228 offset:49152\n"
        "v_add_f32 v192, v192, v193\n"
        "v_mfma_f32_16x16x32_bf16 a[216:219], v[16:19], v[56:59], a[216:219]\n"
        "ds_read_b64_tr_b16 v[54:55], v229 offset:49152\n"
        "v_add_f32 v191, v191, v192\n"
        "v_mfma_f32_16x16x32_bf16 a[216:219], v[20:23], v[88:91], a[216:219]\n"
        "ds_read_b64_tr_b16 v[86:87], v229 offset:57344\n"
        "global_load_lds_dwordx4 v245, s[66:67] offset:3072\n"
        "s_add_u32 m0, s72, 0x10000\n"
        "v_mfma_f32_16x16x32_bf16 a[248:251], v[28:31], v[88:91], a[248:251]\n"
        "ds_read_b64_tr_b16 v[88:89], v230 offset:57344\n"
        "v_lshlrev_b32 v194, 16, v157\n"
        "v_mfma_f32_16x16x32_bf16 a[248:251], v[24:27], v[56:59], a[248:251]\n"
        "ds_read_b64_tr_b16 v[56:57], v230 offset:49152\n"
        "v_and_b32 v195, s35, v157\n"
        "v_mfma_f32_16x16x32_bf16 a[252:255], v[24:27], v[60:63], a[252:255]\n"
        "ds_read_b64_tr_b16 v[58:59], v231 offset:49152\n"
        "v_mul_f32 v194, v194, v188\n"
        "v_mfma_f32_16x16x32_bf16 a[252:255], v[28:31], v[92:95], a[252:255]\n"
        "ds_read_b64_tr_b16 v[90:91], v231 offset:57344\n"
        "global_load_lds_dwordx4 v242, s[86:87] offset:0\n"
        "v_mfma_f32_16x16x32_bf16 a[220:223], v[20:23], v[92:95], a[220:223]\n"
        "ds_read_b64_tr_b16 v[92:93], v232 offset:57344\n"
        "v_mul_f32 v195, v195, v189\n"
        "v_mfma_f32_16x16x32_bf16 a[220:223], v[16:19], v[60:63], a[220:223]\n"
        "ds_read_b64_tr_b16 v[60:61], v232 offset:49152\n"
        "s_waitcnt lgkmcnt(8)\n"
        "v_mfma_f32_16x16x32_bf16 a[0:3], v[32:35], v[0:3], a[0:3]\n"
        "ds_read_b64_tr_b16 v[62:63], v233 offset:49152\n"
        "v_cvt_pk_bf16_f32 v194, 0, v194\n"
        "v_mfma_f32_16x16x32_bf16 a[0:3], v[64:67], v[4:7], a[0:3]\n"
        "ds_read_b64_tr_b16 v[94:95], v233 offset:57344\n"
        "global_load_lds_dwordx4 v243, s[86:87] offset:1024\n"
        "v_mfma_f32_16x16x32_bf16 a[32:35], v[64:67], v[12:15], a[32:35]\n"
        "ds_read_b128 v[16:19], v224 offset:53248\n"
        "v_cvt_pk_bf16_f32 v195, 0, v195\n"
        "v_mfma_f32_16x16x32_bf16 a[32:35], v[32:35], v[8:11], a[32:35]\n"
        "ds_read_b128 v[20:23], v225 offset:53248\n"
        "v_add_f32 v194, v194, v195\n"
        "v_mfma_f32_16x16x32_bf16 a[36:39], v[36:39], v[8:11], a[36:39]\n"
        "ds_read_b128 v[24:27], v224 offset:55296\n"
        "v_add_f32 v191, v191, v194\n"
        "v_mfma_f32_16x16x32_bf16 a[36:39], v[68:71], v[12:15], a[36:39]\n"
        "ds_read_b128 v[28:31], v225 offset:55296\n"
        "global_load_lds_dwordx4 v244, s[86:87] offset:2048\n"
        "v_mfma_f32_16x16x32_bf16 a[4:7], v[68:71], v[4:7], a[4:7]\n"
        "s_add_u32 s70, s38, s97\n"
        "s_addc_u32 s71, s39, 0\n"
        "v_add_f32_dpp v190, v190, v190 row_shr:1 row_mask:0xf bank_mask:0xf bound_ctrl:1\n"
        "v_mfma_f32_16x16x32_bf16 a[4:7], v[36:39], v[0:3], a[4:7]\n"
        "v_add_f32_dpp v191, v191, v191 row_shr:1 row_mask:0xf bank_mask:0xf bound_ctrl:1\n"
        "v_add_f32_dpp v190, v190, v190 row_shr:2 row_mask:0xf bank_mask:0xf bound_ctrl:1\n"
        "v_mfma_f32_16x16x32_bf16 a[8:11], v[40:43], v[0:3], a[8:11]\n"
        "v_add_f32_dpp v191, v191, v191 row_shr:2 row_mask:0xf bank_mask:0xf bound_ctrl:1\n"
        "v_add_f32_dpp v190, v190, v190 row_shr:4 row_mask:0xf bank_mask:0xf bound_ctrl:1\n"
        "v_mfma_f32_16x16x32_bf16 a[8:11], v[72:75], v[4:7], a[8:11]\n"
        "global_load_lds_dwordx4 v245, s[86:87] offset:3072\n"
        "s_add_u32 m0, s72, 0x11000\n"
        "v_mfma_f32_16x16x32_bf16 a[40:43], v[72:75], v[12:15], a[40:43]\n"
        "s_add_u32 s86, s66, 0x80\n"
        "s_addc_u32 s87, s67, 0\n"
        "v_add_f32_dpp v191, v191, v191 row_shr:4 row_mask:0xf bank_mask:0xf bound_ctrl:1\n"
        "v_mfma_f32_16x16x32_bf16 a[40:43], v[40:43], v[8:11], a[40:43]\n"
        "v_add_f32_dpp v190, v190, v190 row_shr:8 row_mask:0xf bank_mask:0xf bound_ctrl:1\n"
        "v_add_f32_dpp v191, v191, v191 row_shr:8 row_mask:0xf bank_mask:0xf bound_ctrl:1\n"
        "v_mfma_f32_16x16x32_bf16 a[44:47], v[44:47], v[8:11], a[44:47]\n"
        "v_add_f32_dpp v190, v190, v190 row_bcast:15 row_mask:0xa bank_mask:0xf bound_ctrl:1\n"
        "v_add_f32_dpp v191, v191, v191 row_bcast:15 row_mask:0xa bank_mask:0xf bound_ctrl:1\n"
        "v_mfma_f32_16x16x32_bf16 a[44:47], v[76:79], v[12:15], a[44:47]\n"
        "global_load_lds_dwordx4 v242, s[86:87] offset:0\n"
        "v_mfma_f32_16x16x32_bf16 a[12:15], v[76:79], v[4:7], a[12:15]\n"
        "v_add_f32_dpp v190, v190, v190 row_bcast:31 row_mask:0xc bank_mask:0xf bound_ctrl:1\n"
        "v_add_f32_dpp v191, v191, v191 row_bcast:31 row_mask:0xc bank_mask:0xf bound_ctrl:1\n"
        "v_mfma_f32_16x16x32_bf16 a[12:15], v[44:47], v[0:3], a[12:15]\n"
        "v_cvt_pk_bf16_f32 v190, 0, v190\n"
        "v_mul_f32 v190, 0xbfb8aa3b, v190\n"
        "v_mfma_f32_16x16x32_bf16 a[16:19], v[0:3], v[48:51], a[16:19]\n"
        "s_waitcnt lgkmcnt(0)\n"
        "v_exp_f32 v190, v190\n"
        "v_mfma_f32_16x16x32_bf16 a[16:19], v[4:7], v[80:83], a[16:19]\n"
        "global_load_lds_dwordx4 v243, s[86:87] offset:1024\n"
        "v_mfma_f32_16x16x32_bf16 a[48:51], v[12:15], v[80:83], a[48:51]\n"
        "v_add_f32 v190, 1.0, v190\n"
        "v_lshrrev_b32 v192, 2, v254\n"
        "v_mfma_f32_16x16x32_bf16 a[48:51], v[8:11], v[48:51], a[48:51]\n"
        "v_rcp_f32 v190, v190\n"
        "v_mfma_f32_16x16x32_bf16 a[52:55], v[8:11], v[52:55], a[52:55]\n"
        "v_cvt_pk_bf16_f32 v190, 0, v190\n"
        "v_add_u32 v192, s80, v192\n"
        "v_mfma_f32_16x16x32_bf16 a[52:55], v[12:15], v[84:87], a[52:55]\n"
        "global_load_lds_dwordx4 v244, s[86:87] offset:2048\n"
        "v_mfma_f32_16x16x32_bf16 a[20:23], v[4:7], v[84:87], a[20:23]\n"
        "v_readlane_b32 s84, v190, 63\n"
        "v_cvt_pk_bf16_f32 v191, 0, v191\n"
        "v_mfma_f32_16x16x32_bf16 a[20:23], v[0:3], v[52:55], a[20:23]\n"
        "v_mov_b32 v190, s84\n"
        "v_mul_f32 v191, 0xbfb8aa3b, v191\n"
        "v_mfma_f32_16x16x32_bf16 a[24:27], v[0:3], v[56:59], a[24:27]\n"
        "v_exp_f32 v191, v191\n"
        "v_mfma_f32_16x16x32_bf16 a[24:27], v[4:7], v[88:91], a[24:27]\n"
        "global_load_lds_dwordx4 v245, s[86:87] offset:3072\n"
        "s_add_u32 m0, s73, 0x1c000\n"
        "v_mfma_f32_16x16x32_bf16 a[56:59], v[12:15], v[88:91], a[56:59]\n"
        "s_add_u32 s86, s68, 0x100000\n"
        "s_addc_u32 s87, s69, 0\n"
        "v_add_f32 v191, 1.0, v191\n"
        "v_mfma_f32_16x16x32_bf16 a[56:59], v[8:11], v[56:59], a[56:59]\n"
        "v_rcp_f32 v191, v191\n"
        "v_mfma_f32_16x16x32_bf16 a[60:63], v[8:11], v[60:63], a[60:63]\n"
        "ds_read_b128 v[8:11], v224 offset:34816\n"
        "v_cvt_pk_bf16_f32 v191, 0, v191\n"
        "v_mfma_f32_16x16x32_bf16 a[60:63], v[12:15], v[92:95], a[60:63]\n"
        "ds_read_b128 v[12:15], v225 offset:34816\n"
        "v_readlane_b32 s85, v191, 63\n"
        "v_mfma_f32_16x16x32_bf16 a[28:31], v[4:7], v[92:95], a[28:31]\n"
        "ds_read_b128 v[4:7], v225 offset:32768\n"
        "v_mov_b32 v191, s85\n"
        "v_mfma_f32_16x16x32_bf16 a[28:31], v[0:3], v[60:63], a[28:31]\n"
        "ds_read_b128 v[0:3], v224 offset:32768\n"
        "v_lshrrev_b32 v193, 2, v254\n"
        "v_mfma_f32_16x16x32_bf16 a[64:67], v[32:35], v[16:19], a[64:67]\n"
        "s_mov_b64 exec, 0xffff\n"
        "global_store_dword v192, v190, s[76:77] sc1\n"
        "s_mov_b64 exec, -1\n"
        "v_mfma_f32_16x16x32_bf16 a[64:67], v[64:67], v[20:23], a[64:67]\n"
        "v_add_u32 v193, s81, v193\n"
        "v_add_u32 v226, s53, v226\n"
        "v_mfma_f32_16x16x32_bf16 a[96:99], v[64:67], v[28:31], a[96:99]\n"
        "s_mov_b64 exec, 0xffff\n"
        "global_store_dword v193, v191, s[76:77] sc1\n"
        "s_mov_b64 exec, -1\n"
        "v_mfma_f32_16x16x32_bf16 a[96:99], v[32:35], v[24:27], a[96:99]\n"
        "v_add_u32 v227, s53, v227\n"
        "v_add_u32 v228, s53, v228\n"
        "v_mfma_f32_16x16x32_bf16 a[100:103], v[36:39], v[24:27], a[100:103]\n"
        "v_add_u32 v229, s53, v229\n"
        "v_add_u32 v230, s53, v230\n"
        "v_mfma_f32_16x16x32_bf16 a[100:103], v[68:71], v[28:31], a[100:103]\n"
        "v_add_u32 v231, s53, v231\n"
        "v_add_u32 v232, s53, v232\n"
        "v_mfma_f32_16x16x32_bf16 a[68:71], v[68:71], v[20:23], a[68:71]\n"
        "v_add_u32 v233, s53, v233\n"
        "s_add_u32 s53, s52, s51\n"
        "v_mfma_f32_16x16x32_bf16 a[68:71], v[36:39], v[16:19], a[68:71]\n"
        "s_add_u32 s53, s53, 0x1100000\n"
        "v_mfma_f32_16x16x32_bf16 a[72:75], v[40:43], v[16:19], a[72:75]\n"
        "v_mfma_f32_16x16x32_bf16 a[72:75], v[72:75], v[20:23], a[72:75]\n"
        "v_mfma_f32_16x16x32_bf16 a[104:107], v[72:75], v[28:31], a[104:107]\n"
        "v_mfma_f32_16x16x32_bf16 a[104:107], v[40:43], v[24:27], a[104:107]\n"
        "v_mfma_f32_16x16x32_bf16 a[108:111], v[44:47], v[24:27], a[108:111]\n"
        "v_mfma_f32_16x16x32_bf16 a[108:111], v[76:79], v[28:31], a[108:111]\n"
        "v_mfma_f32_16x16x32_bf16 a[76:79], v[76:79], v[20:23], a[76:79]\n"
        "v_mfma_f32_16x16x32_bf16 a[76:79], v[44:47], v[16:19], a[76:79]\n"
        "global_load_dword v253, v173, s[82:83] offset:0\n"
        "v_mfma_f32_16x16x32_bf16 a[80:83], v[16:19], v[48:51], a[80:83]\n"
        "v_mfma_f32_16x16x32_bf16 a[80:83], v[20:23], v[80:83], a[80:83]\n"
        "v_mfma_f32_16x16x32_bf16 a[112:115], v[28:31], v[80:83], a[112:115]\n"
        "v_mfma_f32_16x16x32_bf16 a[112:115], v[24:27], v[48:51], a[112:115]\n"
        "v_mfma_f32_16x16x32_bf16 a[116:119], v[24:27], v[52:55], a[116:119]\n"
        "v_mfma_f32_16x16x32_bf16 a[116:119], v[28:31], v[84:87], a[116:119]\n"
        "v_mfma_f32_16x16x32_bf16 a[84:87], v[20:23], v[84:87], a[84:87]\n"
        "v_mfma_f32_16x16x32_bf16 a[84:87], v[16:19], v[52:55], a[84:87]\n"
        "v_mfma_f32_16x16x32_bf16 a[88:91], v[16:19], v[56:59], a[88:91]\n"
        "v_mfma_f32_16x16x32_bf16 a[88:91], v[20:23], v[88:91], a[88:91]\n"
        "v_mfma_f32_16x16x32_bf16 a[120:123], v[28:31], v[88:91], a[120:123]\n"
        "v_mfma_f32_16x16x32_bf16 a[120:123], v[24:27], v[56:59], a[120:123]\n"
        "v_mfma_f32_16x16x32_bf16 a[124:127], v[24:27], v[60:63], a[124:127]\n"
        "ds_read_b128 v[24:27], v224 offset:38912\n"
        "s_waitcnt lgkmcnt(1)\n"
        "v_mfma_f32_16x16x32_bf16 a[124:127], v[28:31], v[92:95], a[124:127]\n"
        "ds_read_b128 v[28:31], v225 offset:38912\n"
        "v_mfma_f32_16x16x32_bf16 a[92:95], v[20:23], v[92:95], a[92:95]\n"
        "ds_read_b128 v[20:23], v225 offset:36864\n"
        "v_mfma_f32_16x16x32_bf16 a[92:95], v[16:19], v[60:63], a[92:95]\n"
        "ds_read_b128 v[16:19], v224 offset:36864\n"
        "v_mfma_f32_16x16x32_bf16 a[128:131], v[32:35], v[0:3], a[128:131]\n"
        "v_mfma_f32_16x16x32_bf16 a[128:131], v[64:67], v[4:7], a[128:131]\n"
        "v_mfma_f32_16x16x32_bf16 a[160:163], v[64:67], v[12:15], a[160:163]\n"
        "v_mfma_f32_16x16x32_bf16 a[160:163], v[32:35], v[8:11], a[160:163]\n"
        "v_mfma_f32_16x16x32_bf16 a[164:167], v[36:39], v[8:11], a[164:167]\n"
        "v_mfma_f32_16x16x32_bf16 a[164:167], v[68:71], v[12:15], a[164:167]\n"
        "v_mfma_f32_16x16x32_bf16 a[132:135], v[68:71], v[4:7], a[132:135]\n"
        "v_mfma_f32_16x16x32_bf16 a[132:135], v[36:39], v[0:3], a[132:135]\n"
        "v_mfma_f32_16x16x32_bf16 a[136:139], v[40:43], v[0:3], a[136:139]\n"
        "v_mfma_f32_16x16x32_bf16 a[136:139], v[72:75], v[4:7], a[136:139]\n"
        "v_mfma_f32_16x16x32_bf16 a[168:171], v[72:75], v[12:15], a[168:171]\n"
        "v_mfma_f32_16x16x32_bf16 a[168:171], v[40:43], v[8:11], a[168:171]\n"
        "v_mfma_f32_16x16x32_bf16 a[172:175], v[44:47], v[8:11], a[172:175]\n"
        "v_mfma_f32_16x16x32_bf16 a[172:175], v[76:79], v[12:15], a[172:175]\n"
        "v_mfma_f32_16x16x32_bf16 a[140:143], v[76:79], v[4:7], a[140:143]\n"
        "v_mfma_f32_16x16x32_bf16 a[140:143], v[44:47], v[0:3], a[140:143]\n"
        "v_mfma_f32_16x16x32_bf16 a[144:147], v[0:3], v[48:51], a[144:147]\n"
        "v_mfma_f32_16x16x32_bf16 a[144:147], v[4:7], v[80:83], a[144:147]\n"
        "v_mfma_f32_16x16x32_bf16 a[176:179], v[12:15], v[80:83], a[176:179]\n"
        "v_mfma_f32_16x16x32_bf16 a[176:179], v[8:11], v[48:51], a[176:179]\n"
        "v_mfma_f32_16x16x32_bf16 a[180:183], v[8:11], v[52:55], a[180:183]\n"
        "v_mfma_f32_16x16x32_bf16 a[180:183], v[12:15], v[84:87], a[180:183]\n"
        "v_mfma_f32_16x16x32_bf16 a[148:151], v[4:7], v[84:87], a[148:151]\n"
        "v_mfma_f32_16x16x32_bf16 a[148:151], v[0:3], v[52:55], a[148:151]\n"
        "v_mfma_f32_16x16x32_bf16 a[152:155], v[0:3], v[56:59], a[152:155]\n"
        "v_mfma_f32_16x16x32_bf16 a[152:155], v[4:7], v[88:91], a[152:155]\n"
        "v_mfma_f32_16x16x32_bf16 a[184:187], v[12:15], v[88:91], a[184:187]\n"
        "v_mfma_f32_16x16x32_bf16 a[184:187], v[8:11], v[56:59], a[184:187]\n"
        "s_waitcnt vmcnt(11) lgkmcnt(0)\n"
        "v_mfma_f32_16x16x32_bf16 a[188:191], v[8:11], v[60:63], a[188:191]\n"
        "s_barrier\n"
        "v_mfma_f32_16x16x32_bf16 a[188:191], v[12:15], v[92:95], a[188:191]\n"
        "global_load_lds_dwordx4 v238, s[86:87] offset:0\n"
        "ds_read_b64_tr_b16 v[160:161], v226 offset:32768\n"
        "v_mfma_f32_16x16x32_bf16 a[156:159], v[4:7], v[92:95], a[156:159]\n"
        "ds_read_b64_tr_b16 v[162:163], v227 offset:32768\n"
        "v_mfma_f32_16x16x32_bf16 a[156:159], v[0:3], v[60:63], a[156:159]\n"
        "ds_read_b64_tr_b16 v[164:165], v226 offset:40960\n"
        "v_mfma_f32_16x16x32_bf16 a[192:195], v[32:35], v[16:19], a[192:195]\n"
        "ds_read_b64_tr_b16 v[166:167], v227 offset:40960\n"
        "v_mfma_f32_16x16x32_bf16 a[192:195], v[64:67], v[20:23], a[192:195]\n"
        "ds_read_b64_tr_b16 v[172:173], v228 offset:40960\n"
        "global_load_lds_dwordx4 v239, s[86:87] offset:1024\n"
        "v_mfma_f32_16x16x32_bf16 a[224:227], v[64:67], v[28:31], a[224:227]\n"
        "ds_read_b64_tr_b16 v[174:175], v229 offset:40960\n"
        "v_mfma_f32_16x16x32_bf16 a[224:227], v[32:35], v[24:27], a[224:227]\n"
        "ds_read_b64_tr_b16 v[168:169], v228 offset:32768\n"
        "v_mfma_f32_16x16x32_bf16 a[228:231], v[36:39], v[24:27], a[228:231]\n"
        "ds_read_b64_tr_b16 v[170:171], v229 offset:32768\n"
        "v_mfma_f32_16x16x32_bf16 a[228:231], v[68:71], v[28:31], a[228:231]\n"
        "ds_read_b128 v[192:195], v246 offset:2048\n"
        "global_load_lds_dwordx4 v240, s[86:87] offset:2048\n"
        "v_mfma_f32_16x16x32_bf16 a[196:199], v[68:71], v[20:23], a[196:199]\n"
        "ds_read_b128 v[208:211], v247 offset:2048\n"
        "v_accvgpr_read_b32 v177, a225\n"
        "v_mfma_f32_16x16x32_bf16 a[196:199], v[36:39], v[16:19], a[196:199]\n"
        "ds_read_b128 v[196:199], v246 offset:0\n"
        "v_accvgpr_read_b32 v178, a226\n"
        "v_mfma_f32_16x16x32_bf16 a[200:203], v[40:43], v[16:19], a[200:203]\n"
        "ds_read_b128 v[212:215], v247 offset:0\n"
        "v_accvgpr_read_b32 v179, a227\n"
        "v_mfma_f32_16x16x32_bf16 a[200:203], v[72:75], v[20:23], a[200:203]\n"
        "ds_read_b128 v[200:203], v246 offset:6144\n"
        "global_load_lds_dwordx4 v241, s[86:87] offset:3072\n"
        "s_add_u32 m0, s72, 0x8000\n"
        "v_mfma_f32_16x16x32_bf16 a[232:235], v[72:75], v[28:31], a[232:235]\n"
        "ds_read_b128 v[216:219], v247 offset:6144\n"
        "s_add_u32 s86, s64, 0x100\n"
        "s_addc_u32 s87, s65, 0\n"
        "v_mfma_f32_16x16x32_bf16 a[232:235], v[40:43], v[24:27], a[232:235]\n"
        "ds_read_b128 v[204:207], v246 offset:4096\n"
        "v_accvgpr_read_b32 v180, a228\n"
        "v_mfma_f32_16x16x32_bf16 a[236:239], v[44:47], v[24:27], a[236:239]\n"
        "ds_read_b128 v[220:223], v247 offset:4096\n"
        "v_accvgpr_read_b32 v181, a229\n"
        "v_mfma_f32_16x16x32_bf16 a[236:239], v[76:79], v[28:31], a[236:239]\n"
        "ds_read_b64_tr_b16 v[32:33], v230 offset:32768\n"
        "global_load_lds_dwordx4 v242, s[86:87] offset:0\n"
        "v_mfma_f32_16x16x32_bf16 a[204:207], v[76:79], v[20:23], a[204:207]\n"
        "ds_read_b64_tr_b16 v[34:35], v231 offset:32768\n"
        "v_accvgpr_read_b32 v182, a230\n"
        "v_mfma_f32_16x16x32_bf16 a[204:207], v[44:47], v[16:19], a[204:207]\n"
        "ds_read_b64_tr_b16 v[36:37], v230 offset:40960\n"
        "s_add_u32 s97, s52, 0x2000\n"
        "v_accvgpr_read_b32 v183, a231\n"
        "v_mfma_f32_16x16x32_bf16 a[208:211], v[16:19], v[48:51], a[208:211]\n"
        "ds_read_b64_tr_b16 v[38:39], v231 offset:40960\n"
        "v_mul_f32 v177, s88, v177\n"
        "v_mfma_f32_16x16x32_bf16 a[208:211], v[20:23], v[80:83], a[208:211]\n"
        "ds_read_b64_tr_b16 v[44:45], v232 offset:40960\n"
        "global_load_lds_dwordx4 v243, s[86:87] offset:1024\n"
        "v_mfma_f32_16x16x32_bf16 a[240:243], v[28:31], v[80:83], a[240:243]\n"
        "ds_read_b64_tr_b16 v[46:47], v233 offset:40960\n"
        "v_mul_f32 v178, s88, v178\n"
        "v_mfma_f32_16x16x32_bf16 a[240:243], v[24:27], v[48:51], a[240:243]\n"
        "ds_read_b64_tr_b16 v[40:41], v232 offset:32768\n"
        "v_mul_f32 v179, s88, v179\n"
        "v_mfma_f32_16x16x32_bf16 a[244:247], v[24:27], v[52:55], a[244:247]\n"
        "ds_read_b64_tr_b16 v[42:43], v233 offset:32768\n"
        "v_mul_f32 v180, s88, v180\n"
        "v_mfma_f32_16x16x32_bf16 a[244:247], v[28:31], v[84:87], a[244:247]\n"
        "global_load_lds_dwordx4 v244, s[86:87] offset:2048\n"
        "v_mfma_f32_16x16x32_bf16 a[212:215], v[20:23], v[84:87], a[212:215]\n"
        "ds_read_b64_tr_b16 v[48:49], v226 offset:33792\n"
        "v_mul_f32 v181, s88, v181\n"
        "v_mfma_f32_16x16x32_bf16 a[212:215], v[16:19], v[52:55], a[212:215]\n"
        "ds_read_b64_tr_b16 v[50:51], v227 offset:33792\n"
        "v_mul_f32 v182, s88, v182\n"
        "v_mfma_f32_16x16x32_bf16 a[216:219], v[16:19], v[56:59], a[216:219]\n"
        "ds_read_b64_tr_b16 v[52:53], v226 offset:41984\n"
        "v_mul_f32 v183, s88, v183\n"
        "v_mfma_f32_16x16x32_bf16 a[216:219], v[20:23], v[88:91], a[216:219]\n"
        "global_load_lds_dwordx4 v245, s[86:87] offset:3072\n"
        "s_add_u32 m0, s72, 0x9000\n"
        "v_mfma_f32_16x16x32_bf16 a[248:251], v[28:31], v[88:91], a[248:251]\n"
        "s_add_u32 s86, s66, 0x100\n"
        "s_addc_u32 s87, s67, 0\n"
        "v_accvgpr_read_b32 v185, a233\n"
        "v_mfma_f32_16x16x32_bf16 a[248:251], v[24:27], v[56:59], a[248:251]\n"
        "ds_read_b64_tr_b16 v[54:55], v227 offset:41984\n"
        "v_accvgpr_read_b32 v186, a234\n"
        "v_mfma_f32_16x16x32_bf16 a[252:255], v[24:27], v[60:63], a[252:255]\n"
        "ds_read_b64_tr_b16 v[56:57], v228 offset:33792\n"
        "v_accvgpr_read_b32 v187, a235\n"
        "v_mfma_f32_16x16x32_bf16 a[252:255], v[28:31], v[92:95], a[252:255]\n"
        "global_load_lds_dwordx4 v242, s[86:87] offset:0\n"
        "v_mfma_f32_16x16x32_bf16 a[220:223], v[20:23], v[92:95], a[220:223]\n"
        "ds_read_b64_tr_b16 v[58:59], v229 offset:33792\n"
        "v_accvgpr_read_b32 v188, a236\n"
        "v_mfma_f32_16x16x32_bf16 a[220:223], v[16:19], v[60:63], a[220:223]\n"
        "ds_read_b64_tr_b16 v[60:61], v228 offset:41984\n"
        "s_waitcnt lgkmcnt(5)\n"
        "v_mfma_f32_16x16x32_bf16 v[0:3], v[160:163], v[192:195], 0\n"
        "ds_read_b64_tr_b16 v[62:63], v229 offset:41984\n"
        "v_accvgpr_read_b32 v189, a237\n"
        "v_mfma_f32_16x16x32_bf16 v[0:3], v[164:167], v[208:211], v[0:3]\n"
        "global_load_lds_dwordx4 v243, s[86:87] offset:1024\n"
        "v_mfma_f32_16x16x32_bf16 v[4:7], v[172:175], v[208:211], 0\n"
        "v_accvgpr_read_b32 v190, a238\n"
        "v_accvgpr_read_b32 v191, a239\n"
        "v_mfma_f32_16x16x32_bf16 v[4:7], v[168:171], v[192:195], v[4:7]\n"
        "v_mul_f32 v185, s88, v185\n"
        "v_mul_f32 v186, s88, v186\n"
        "v_mfma_f32_16x16x32_bf16 v[68:71], v[168:171], v[196:199], 0\n"
        "v_mul_f32 v187, s88, v187\n"
        "v_mul_f32 v188, s88, v188\n"
        "v_mfma_f32_16x16x32_bf16 v[68:71], v[172:175], v[212:215], v[68:71]\n"
        "global_load_lds_dwordx4 v244, s[86:87] offset:2048\n"
        "v_mfma_f32_16x16x32_bf16 v[64:67], v[164:167], v[212:215], 0\n"
        "v_mul_f32 v189, s88, v189\n"
        "v_mul_f32 v190, s88, v190\n"
        "v_mfma_f32_16x16x32_bf16 v[64:67], v[160:163], v[196:199], v[64:67]\n"
        "v_accvgpr_read_b32 v176, a224\n"
        "v_mul_f32 v176, s88, v176\n"
        "v_mfma_f32_16x16x32_bf16 v[96:99], v[160:163], v[200:203], 0\n"
        "v_cvt_pk_bf16_f32 v176, v176, v177\n"
        "v_cvt_pk_bf16_f32 v177, v178, v179\n"
        "v_mfma_f32_16x16x32_bf16 v[96:99], v[164:167], v[216:219], v[96:99]\n"
        "v_cvt_pk_bf16_f32 v178, v180, v181\n"
        "v_cvt_pk_bf16_f32 v179, v182, v183\n"
        "v_mfma_f32_16x16x32_bf16 v[100:103], v[172:175], v[216:219], 0\n"
        "global_load_lds_dwordx4 v245, s[86:87] offset:3072\n"
        "s_add_u32 m0, s73, 0x20000\n"
        "v_accvgpr_write_b32 a224, v176\n"
        "v_mfma_f32_16x16x32_bf16 v[100:103], v[168:171], v[200:203], v[100:103]\n"
        "s_add_u32 s86, s68, 0x180000\n"
        "s_addc_u32 s87, s69, 0\n"
        "v_accvgpr_write_b32 a225, v177\n"
        "v_mfma_f32_16x16x32_bf16 v[132:135], v[168:171], v[204:207], 0\n"
        "ds_read_b64_tr_b16 v[168:169], v232 offset:33792\n"
        "v_accvgpr_write_b32 a226, v178\n"
        "v_mfma_f32_16x16x32_bf16 v[132:135], v[172:175], v[220:223], v[132:135]\n"
        "ds_read_b64_tr_b16 v[172:173], v232 offset:41984\n"
        "v_accvgpr_write_b32 a227, v179\n"
        "v_mfma_f32_16x16x32_bf16 v[128:131], v[164:167], v[220:223], 0\n"
        "ds_read_b64_tr_b16 v[164:165], v230 offset:41984\n"
        "v_mul_f32 v191, s88, v191\n"
        "v_mfma_f32_16x16x32_bf16 v[128:131], v[160:163], v[204:207], v[128:131]\n"
        "ds_read_b64_tr_b16 v[160:161], v230 offset:33792\n"
        "v_accvgpr_read_b32 v177, a193\n"
        "v_mfma_f32_16x16x32_bf16 v[8:11], v[32:35], v[192:195], 0\n"
        "ds_read_b64_tr_b16 v[162:163], v231 offset:33792\n"
        "v_accvgpr_read_b32 v178, a194\n"
        "v_mfma_f32_16x16x32_bf16 v[8:11], v[36:39], v[208:211], v[8:11]\n"
        "ds_read_b64_tr_b16 v[166:167], v231 offset:41984\n"
        "v_accvgpr_read_b32 v179, a195\n"
        "v_mfma_f32_16x16x32_bf16 v[12:15], v[44:47], v[208:211], 0\n"
        "ds_read_b64_tr_b16 v[174:175], v233 offset:41984\n"
        "v_accvgpr_read_b32 v180, a196\n"
        "v_mfma_f32_16x16x32_bf16 v[12:15], v[40:43], v[192:195], v[12:15]\n"
        "ds_read_b64_tr_b16 v[170:171], v233 offset:33792\n"
        "v_accvgpr_read_b32 v181, a197\n"
        "v_mfma_f32_16x16x32_bf16 v[76:79], v[40:43], v[196:199], 0\n"
        "LHA4_%=:\n"
        "s_cbranch_execz LHA8_%=\n"
        "v_accvgpr_read_b32 v182, a198\n"
        "v_accvgpr_read_b32 v183, a199\n"
        "v_mfma_f32_16x16x32_bf16 v[76:79], v[44:47], v[212:215], v[76:79]\n"
        "v_mul_f32 v177, s88, v177\n"
        "v_mul_f32 v178, s88, v178\n"
        "v_mfma_f32_16x16x32_bf16 v[72:75], v[36:39], v[212:215], 0\n"
        "v_mul_f32 v179, s88, v179\n"
        "v_mul_f32 v180, s88, v180\n"
        "v_mfma_f32_16x16x32_bf16 v[72:75], v[32:35], v[196:199], v[72:75]\n"
        "v_mul_f32 v181, s88, v181\n"
        "v_mul_f32 v182, s88, v182\n"
        "v_mfma_f32_16x16x32_bf16 v[104:107], v[32:35], v[200:203], 0\n"
        "v_mul_f32 v183, s88, v183\n"
        "v_mfma_f32_16x16x32_bf16 v[104:107], v[36:39], v[216:219], v[104:107]\n"
        "v_mfma_f32_16x16x32_bf16 v[108:111], v[44:47], v[216:219], 0\n"
        "v_mfma_f32_16x16x32_bf16 v[108:111], v[40:43], v[200:203], v[108:111]\n"
        "s_waitcnt vmcnt(15) lgkmcnt(0)\n"
        "v_mfma_f32_16x16x32_bf16 v[140:143], v[40:43], v[204:207], 0\n"
        "s_barrier\n"
        "v_mfma_f32_16x16x32_bf16 v[140:143], v[44:47], v[220:223], v[140:143]\n"
        "global_load_lds_dwordx4 v238, s[86:87] offset:0\n"
        "ds_read_b64_tr_b16 v[44:45], v228 offset:57344\n"
        "v_mfma_f32_16x16x32_bf16 v[136:139], v[36:39], v[220:223], 0\n"
        "ds_read_b64_tr_b16 v[36:37], v226 offset:57344\n"
        "v_mfma_f32_16x16x32_bf16 v[136:139], v[32:35], v[204:207], v[136:139]\n"
        "ds_read_b64_tr_b16 v[32:33], v226 offset:49152\n"
        "v_accvgpr_read_b32 v184, a232\n"
        "v_mfma_f32_16x16x32_bf16 v[16:19], v[48:51], v[192:195], 0\n"
        "ds_read_b64_tr_b16 v[34:35], v227 offset:49152\n"
        "v_mul_f32 v184, s88, v184\n"
        "v_mfma_f32_16x16x32_bf16 v[16:19], v[52:55], v[208:211], v[16:19]\n"
        "ds_read_b64_tr_b16 v[38:39], v227 offset:57344\n"
        "v_cvt_pk_bf16_f32 v184, v184, v185\n"
        "v_mfma_f32_16x16x32_bf16 v[20:23], v[60:63], v[208:211], 0\n"
        "ds_read_b64_tr_b16 v[46:47], v229 offset:57344\n"
        "v_cvt_pk_bf16_f32 v185, v186, v187\n"
        "v_mfma_f32_16x16x32_bf16 v[20:23], v[56:59], v[192:195], v[20:23]\n"
        "ds_read_b64_tr_b16 v[40:41], v228 offset:49152\n"
        "v_cvt_pk_bf16_f32 v186, v188, v189\n"
        "v_mfma_f32_16x16x32_bf16 v[84:87], v[56:59], v[196:199], 0\n"
        "ds_read_b64_tr_b16 v[42:43], v229 offset:49152\n"
        "v_cvt_pk_bf16_f32 v187, v190, v191\n"
        "v_mfma_f32_16x16x32_bf16 v[84:87], v[60:63], v[212:215], v[84:87]\n"
        "global_load_lds_dwordx4 v239, s[86:87] offset:1024\n"
        "v_accvgpr_write_b32 a228, v184\n"
        "v_mfma_f32_16x16x32_bf16 v[80:83], v[52:55], v[212:215], 0\n"
        "v_accvgpr_write_b32 a229, v185\n"
        "v_accvgpr_write_b32 a230, v186\n"
        "v_mfma_f32_16x16x32_bf16 v[80:83], v[48:51], v[196:199], v[80:83]\n"
        "v_accvgpr_write_b32 a231, v187\n"
        "v_accvgpr_read_b32 v185, a201\n"
        "v_mfma_f32_16x16x32_bf16 v[112:115], v[48:51], v[200:203], 0\n"
        "v_accvgpr_read_b32 v186, a202\n"
        "v_accvgpr_read_b32 v187, a203\n"
        "v_mfma_f32_16x16x32_bf16 v[112:115], v[52:55], v[216:219], v[112:115]\n"
        "global_load_lds_dwordx4 v240, s[86:87] offset:2048\n"
        "v_accvgpr_read_b32 v188, a204\n"
        "v_mfma_f32_16x16x32_bf16 v[116:119], v[60:63], v[216:219], 0\n"
        "v_accvgpr_read_b32 v189, a205\n"
        "v_accvgpr_read_b32 v190, a206\n"
        "v_mfma_f32_16x16x32_bf16 v[116:119], v[56:59], v[200:203], v[116:119]\n"
        "v_accvgpr_read_b32 v191, a207\n"
        "v_mul_f32 v185, s88, v185\n"
        "v_mfma_f32_16x16x32_bf16 v[148:151], v[56:59], v[204:207], 0\n"
        "ds_read_b64_tr_b16 v[56:57], v232 offset:49152\n"
        "v_mul_f32 v186, s88, v186\n"
        "v_mfma_f32_16x16x32_bf16 v[148:151], v[60:63], v[220:223], v[148:151]\n"
        "global_store_dwordx4 v254, a[224:227], s[70:71] offset:0 sc1\n"
        "ds_read_b64_tr_b16 v[60:61], v232 offset:57344\n"
        "ds_read_b64_tr_b16 v[62:63], v233 offset:57344\n"
        "v_mul_f32 v187, s88, v187\n"
        "v_mfma_f32_16x16x32_bf16 v[144:147], v[52:55], v[220:223], 0\n"
        "ds_read_b64_tr_b16 v[52:53], v230 offset:57344\n"
        "v_mul_f32 v188, s88, v188\n"
        "v_mfma_f32_16x16x32_bf16 v[144:147], v[48:51], v[204:207], v[144:147]\n"
        "ds_read_b64_tr_b16 v[48:49], v230 offset:49152\n"
        "v_mul_f32 v189, s88, v189\n"
        "v_mfma_f32_16x16x32_bf16 v[24:27], v[160:163], v[192:195], 0\n"
        "global_load_lds_dwordx4 v241, s[86:87] offset:3072\n"
        "s_add_u32 m0, s72, 0x0\n"
        "v_mfma_f32_16x16x32_bf16 v[24:27], v[164:167], v[208:211], v[24:27]\n"
        "ds_read_b64_tr_b16 v[50:51], v231 offset:49152\n"
        "s_add_u32 s86, s64, 0x180\n"
        "s_addc_u32 s87, s65, 0\n"
        "v_mfma_f32_16x16x32_bf16 v[28:31], v[172:175], v[208:211], 0\n"
        "ds_read_b64_tr_b16 v[54:55], v231 offset:57344\n"
        "v_mul_f32 v190, s88, v190\n"
        "v_mfma_f32_16x16x32_bf16 v[28:31], v[168:171], v[192:195], v[28:31]\n"
        "ds_read_b64_tr_b16 v[58:59], v233 offset:49152\n"
        "v_mul_f32 v191, s88, v191\n"
        "v_mfma_f32_16x16x32_bf16 v[92:95], v[168:171], v[196:199], 0\n"
        "global_load_lds_dwordx4 v242, s[86:87] offset:0\n"
        "v_mfma_f32_16x16x32_bf16 v[92:95], v[172:175], v[212:215], v[92:95]\n"
        "ds_read_b128 v[192:195], v248 offset:2048\n"
        "v_mfma_f32_16x16x32_bf16 v[88:91], v[164:167], v[212:215], 0\n"
        "ds_read_b128 v[208:211], v249 offset:2048\n"
        "v_mfma_f32_16x16x32_bf16 v[88:91], v[160:163], v[196:199], v[88:91]\n"
        "ds_read_b128 v[196:199], v248 offset:0\n"
        "v_accvgpr_read_b32 v176, a192\n"
        "v_mfma_f32_16x16x32_bf16 v[120:123], v[160:163], v[200:203], 0\n"
        "global_load_lds_dwordx4 v243, s[86:87] offset:1024\n"
        "v_mfma_f32_16x16x32_bf16 v[120:123], v[164:167], v[216:219], v[120:123]\n"
        "ds_read_b128 v[212:215], v249 offset:0\n"
        "v_mul_f32 v176, s88, v176\n"
        "v_mfma_f32_16x16x32_bf16 v[124:127], v[172:175], v[216:219], 0\n"
        "ds_read_b128 v[216:219], v249 offset:6144\n"
        "v_cvt_pk_bf16_f32 v176, v176, v177\n"
        "v_mfma_f32_16x16x32_bf16 v[124:127], v[168:171], v[200:203], v[124:127]\n"
        "ds_read_b128 v[200:203], v248 offset:6144\n"
        "v_cvt_pk_bf16_f32 v177, v178, v179\n"
        "v_mfma_f32_16x16x32_bf16 v[156:159], v[168:171], v[204:207], 0\n"
        "ds_read_b64_tr_b16 v[168:169], v228 offset:50176\n"
        "v_cvt_pk_bf16_f32 v178, v180, v181\n"
        "v_mfma_f32_16x16x32_bf16 v[156:159], v[172:175], v[220:223], v[156:159]\n"
        "s_waitcnt lgkmcnt(6)\n"
        "v_cvt_pk_bf16_f32 v179, v182, v183\n"
        "v_accvgpr_write_b32 a232, v176\n"
        "v_mfma_f32_16x16x32_bf16 v[152:155], v[164:167], v[220:223], 0\n"
        "s_waitcnt lgkmcnt(5)\n"
        "v_accvgpr_write_b32 a233, v177\n"
        "v_accvgpr_write_b32 a234, v178\n"
        "v_mfma_f32_16x16x32_bf16 v[152:155], v[160:163], v[204:207], v[152:155]\n"
        "global_load_lds_dwordx4 v244, s[86:87] offset:2048\n"
        "v_accvgpr_write_b32 a235, v179\n"
        "v_mfma_f32_16x16x32_bf16 v[0:3], v[32:35], v[192:195], v[0:3]\n"
        "ds_read_b128 v[204:207], v248 offset:4096\n"
        "v_accvgpr_read_b32 v177, a161\n"
        "v_mfma_f32_16x16x32_bf16 v[0:3], v[36:39], v[208:211], v[0:3]\n"
        "ds_read_b128 v[220:223], v249 offset:4096\n"
        "v_accvgpr_read_b32 v178, a162\n"
        "v_mfma_f32_16x16x32_bf16 v[4:7], v[44:47], v[208:211], v[4:7]\n"
        "ds_read_b64_tr_b16 v[160:161], v226 offset:50176\n"
        "v_accvgpr_read_b32 v179, a163\n"
        "v_mfma_f32_16x16x32_bf16 v[4:7], v[40:43], v[192:195], v[4:7]\n"
        "global_load_lds_dwordx4 v245, s[86:87] offset:3072\n"
        "s_add_u32 m0, s72, 0x1000\n"
        "s_waitcnt lgkmcnt(4)\n"
        "v_mfma_f32_16x16x32_bf16 v[68:71], v[40:43], v[196:199], v[68:71]\n"
        "ds_read_b64_tr_b16 v[162:163], v227 offset:50176\n"
        "s_add_u32 s86, s66, 0x180\n"
        "s_addc_u32 s87, s67, 0\n"
        "v_mfma_f32_16x16x32_bf16 v[68:71], v[44:47], v[212:215], v[68:71]\n"
        "ds_read_b64_tr_b16 v[164:165], v226 offset:58368\n"
        "v_accvgpr_read_b32 v180, a164\n"
        "v_mfma_f32_16x16x32_bf16 v[64:67], v[36:39], v[212:215], v[64:67]\n"
        "ds_read_b64_tr_b16 v[166:167], v227 offset:58368\n"
        "v_accvgpr_read_b32 v181, a165\n"
        "v_mfma_f32_16x16x32_bf16 v[64:67], v[32:35], v[196:199], v[64:67]\n"
        "ds_read_b64_tr_b16 v[172:173], v228 offset:58368\n"
        "global_load_lds_dwordx4 v242, s[86:87] offset:0\n"
        "v_mfma_f32_16x16x32_bf16 v[96:99], v[32:35], v[200:203], v[96:99]\n"
        "ds_read_b64_tr_b16 v[174:175], v229 offset:58368\n"
        "v_accvgpr_read_b32 v182, a166\n"
        "v_mfma_f32_16x16x32_bf16 v[96:99], v[36:39], v[216:219], v[96:99]\n"
        "ds_read_b64_tr_b16 v[170:171], v229 offset:50176\n"
        "v_accvgpr_read_b32 v183, a167\n"
        "v_mfma_f32_16x16x32_bf16 v[100:103], v[44:47], v[216:219], v[100:103]\n"
        "s_waitcnt lgkmcnt(6)\n"
        "v_mul_f32 v177, s88, v177\n"
        "v_mul_f32 v178, s88, v178\n"
        "v_mfma_f32_16x16x32_bf16 v[100:103], v[40:43], v[200:203], v[100:103]\n"
        "global_load_lds_dwordx4 v243, s[86:87] offset:1024\n"
        "v_mfma_f32_16x16x32_bf16 v[132:135], v[40:43], v[204:207], v[132:135]\n"
        "ds_read_b64_tr_b16 v[40:41], v232 offset:50176\n"
        "v_mul_f32 v179, s88, v179\n"
        "v_mfma_f32_16x16x32_bf16 v[132:135], v[44:47], v[220:223], v[132:135]\n"
        "ds_read_b64_tr_b16 v[44:45], v232 offset:58368\n"
        "v_mul_f32 v180, s88, v180\n"
        "v_mfma_f32_16x16x32_bf16 v[128:131], v[36:39], v[220:223], v[128:131]\n"
        "ds_read_b64_tr_b16 v[36:37], v230 offset:58368\n"
        "v_mul_f32 v181, s88, v181\n"
        "v_mfma_f32_16x16x32_bf16 v[128:131], v[32:35], v[204:207], v[128:131]\n"
        "global_load_lds_dwordx4 v244, s[86:87] offset:2048\n"
        "v_mfma_f32_16x16x32_bf16 v[8:11], v[48:51], v[192:195], v[8:11]\n"
        "ds_read_b64_tr_b16 v[32:33], v230 offset:50176\n"
        "v_accvgpr_read_b32 v184, a200\n"
        "v_mfma_f32_16x16x32_bf16 v[8:11], v[52:55], v[208:211], v[8:11]\n"
        "ds_read_b64_tr_b16 v[34:35], v231 offset:50176\n"
        "v_mul_f32 v184, s88, v184\n"
        "v_mfma_f32_16x16x32_bf16 v[12:15], v[60:63], v[208:211], v[12:15]\n"
        "ds_read_b64_tr_b16 v[38:39], v231 offset:58368\n"
        "v_cvt_pk_bf16_f32 v184, v184, v185\n"
        "v_mfma_f32_16x16x32_bf16 v[12:15], v[56:59], v[192:195], v[12:15]\n"
        "global_load_lds_dwordx4 v245, s[86:87] offset:3072\n"
        "s_add_u32 m0, s73, 0x24000\n"
        "v_mfma_f32_16x16x32_bf16 v[76:79], v[56:59], v[196:199], v[76:79]\n"
        "ds_read_b64_tr_b16 v[46:47], v233 offset:58368\n"
        "v_cvt_pk_bf16_f32 v185, v186, v187\n"
        "v_mfma_f32_16x16x32_bf16 v[76:79], v[60:63], v[212:215], v[76:79]\n"
        "ds_read_b64_tr_b16 v[42:43], v233 offset:50176\n"
        "v_cvt_pk_bf16_f32 v186, v188, v189\n"
        "v_mfma_f32_16x16x32_bf16 v[72:75], v[52:55], v[212:215], v[72:75]\n"
        "s_add_u32 s86, s68, 0x200000\n"
        "s_addc_u32 s87, s69, 0\n"
        "v_cvt_pk_bf16_f32 v187, v190, v191\n"
        "v_mfma_f32_16x16x32_bf16 v[72:75], v[48:51], v[196:199], v[72:75]\n"
        "v_accvgpr_write_b32 a236, v184\n"
        "v_accvgpr_write_b32 a237, v185\n"
        "v_mfma_f32_16x16x32_bf16 v[104:107], v[48:51], v[200:203], v[104:107]\n"
        "v_accvgpr_write_b32 a238, v186\n"
        "v_accvgpr_write_b32 a239, v187\n"
        "v_mfma_f32_16x16x32_bf16 v[104:107], v[52:55], v[216:219], v[104:107]\n"
        "v_mul_f32 v182, s88, v182\n"
        "v_mul_f32 v183, s88, v183\n"
        "v_mfma_f32_16x16x32_bf16 v[108:111], v[60:63], v[216:219], v[108:111]\n"
        "v_accvgpr_read_b32 v185, a169\n"
        "v_accvgpr_read_b32 v186, a170\n"
        "v_mfma_f32_16x16x32_bf16 v[108:111], v[56:59], v[200:203], v[108:111]\n"
        "v_accvgpr_read_b32 v187, a171\n"
        "v_accvgpr_read_b32 v188, a172\n"
        "v_mfma_f32_16x16x32_bf16 v[140:143], v[56:59], v[204:207], v[140:143]\n"
        "s_waitcnt lgkmcnt(1)\n"
        "v_accvgpr_read_b32 v189, a173\n"
        "v_accvgpr_read_b32 v190, a174\n"
        "v_mfma_f32_16x16x32_bf16 v[140:143], v[60:63], v[220:223], v[140:143]\n"
        "s_waitcnt vmcnt(13) lgkmcnt(0)\n"
        "v_accvgpr_read_b32 v191, a175\n"
        "v_mul_f32 v185, s88, v185\n"
        "v_mfma_f32_16x16x32_bf16 v[136:139], v[52:55], v[220:223], v[136:139]\n"
        "s_barrier\n"
        "v_mfma_f32_16x16x32_bf16 v[136:139], v[48:51], v[204:207], v[136:139]\n"
        "global_load_lds_dwordx4 v238, s[86:87] offset:0\n"
        "ds_read_b64_tr_b16 v[48:49], v226 offset:16384\n"
        "v_mfma_f32_16x16x32_bf16 v[16:19], v[160:163], v[192:195], v[16:19]\n"
        "ds_read_b64_tr_b16 v[50:51], v227 offset:16384\n"
        "v_mul_f32 v186, s88, v186\n"
        "v_mfma_f32_16x16x32_bf16 v[16:19], v[164:167], v[208:211], v[16:19]\n"
        "ds_read_b64_tr_b16 v[52:53], v226 offset:24576\n"
        "v_mul_f32 v187, s88, v187\n"
        "v_mfma_f32_16x16x32_bf16 v[20:23], v[172:175], v[208:211], v[20:23]\n"
        "ds_read_b64_tr_b16 v[54:55], v227 offset:24576\n"
        "v_mul_f32 v188, s88, v188\n"
        "v_mfma_f32_16x16x32_bf16 v[20:23], v[168:171], v[192:195], v[20:23]\n"
        "ds_read_b64_tr_b16 v[60:61], v228 offset:24576\n"
        "global_load_lds_dwordx4 v239, s[86:87] offset:1024\n"
        "v_mfma_f32_16x16x32_bf16 v[84:87], v[168:171], v[196:199], v[84:87]\n"
        "ds_read_b64_tr_b16 v[62:63], v229 offset:24576\n"
        "v_mul_f32 v189, s88, v189\n"
        "v_mfma_f32_16x16x32_bf16 v[84:87], v[172:175], v[212:215], v[84:87]\n"
        "ds_read_b64_tr_b16 v[56:57], v228 offset:16384\n"
        "v_mul_f32 v190, s88, v190\n"
        "v_mfma_f32_16x16x32_bf16 v[80:83], v[164:167], v[212:215], v[80:83]\n"
        "ds_read_b64_tr_b16 v[58:59], v229 offset:16384\n"
        "v_mul_f32 v191, s88, v191\n"
        "v_mfma_f32_16x16x32_bf16 v[80:83], v[160:163], v[196:199], v[80:83]\n"
        "v_accvgpr_read_b32 v176, a160\n"
        "v_mul_f32 v176, s88, v176\n"
        "v_mfma_f32_16x16x32_bf16 v[112:115], v[160:163], v[200:203], v[112:115]\n"
        "global_load_lds_dwordx4 v240, s[86:87] offset:2048\n"
        "v_cvt_pk_bf16_f32 v176, v176, v177\n"
        "v_mfma_f32_16x16x32_bf16 v[112:115], v[164:167], v[216:219], v[112:115]\n"
        "v_cvt_pk_bf16_f32 v177, v178, v179\n"
        "v_cvt_pk_bf16_f32 v178, v180, v181\n"
        "v_mfma_f32_16x16x32_bf16 v[116:119], v[172:175], v[216:219], v[116:119]\n"
        "v_cvt_pk_bf16_f32 v179, v182, v183\n"
        "v_accvgpr_write_b32 a192, v176\n"
        "v_mfma_f32_16x16x32_bf16 v[116:119], v[168:171], v[200:203], v[116:119]\n"
        "v_accvgpr_write_b32 a193, v177\n"
        "v_accvgpr_write_b32 a194, v178\n"
        "v_mfma_f32_16x16x32_bf16 v[148:151], v[168:171], v[204:207], v[148:151]\n"
        "global_load_lds_dwordx4 v241, s[86:87] offset:3072\n"
        "s_add_u32 m0, s72, 0x10000\n"
        "v_accvgpr_write_b32 a195, v179\n"
        "v_mfma_f32_16x16x32_bf16 v[148:151], v[172:175], v[220:223], v[148:151]\n"
        "ds_read_b64_tr_b16 v[172:173], v232 offset:24576\n"
        "s_add_u32 s86, s64, 0x200\n"
        "s_addc_u32 s87, s65, 0\n"
        "v_mfma_f32_16x16x32_bf16 v[144:147], v[164:167], v[220:223], v[144:147]\n"
        "ds_read_b64_tr_b16 v[164:165], v230 offset:24576\n"
        "v_accvgpr_read_b32 v177, a1\n"
        "v_mfma_f32_16x16x32_bf16 v[144:147], v[160:163], v[204:207], v[144:147]\n"
        "ds_read_b64_tr_b16 v[160:161], v230 offset:16384\n"
        "v_accvgpr_read_b32 v178, a2\n"
        "v_mfma_f32_16x16x32_bf16 v[24:27], v[32:35], v[192:195], v[24:27]\n"
        "global_store_dwordx4 v254, a[228:231], s[70:71] offset:1024 sc1\n"
        "ds_read_b64_tr_b16 v[162:163], v231 offset:16384\n"
        "ds_read_b64_tr_b16 v[166:167], v231 offset:24576\n"
        "v_accvgpr_read_b32 v179, a3\n"
        "v_mfma_f32_16x16x32_bf16 v[24:27], v[36:39], v[208:211], v[24:27]\n"
        "ds_read_b64_tr_b16 v[174:175], v233 offset:24576\n"
        "v_accvgpr_read_b32 v180, a4\n"
        "v_mfma_f32_16x16x32_bf16 v[28:31], v[44:47], v[208:211], v[28:31]\n"
        "ds_read_b64_tr_b16 v[168:169], v232 offset:16384\n"
        "v_accvgpr_read_b32 v181, a5\n"
        "v_mfma_f32_16x16x32_bf16 v[28:31], v[40:43], v[192:195], v[28:31]\n"
        "ds_read_b64_tr_b16 v[170:171], v233 offset:16384\n"
        "global_load_lds_dwordx4 v242, s[86:87] offset:0\n"
        "v_mfma_f32_16x16x32_bf16 v[92:95], v[40:43], v[196:199], v[92:95]\n"
        "ds_read_b128 v[192:195], v246 offset:34816\n"
        "v_accvgpr_read_b32 v182, a6\n"
        "v_mfma_f32_16x16x32_bf16 v[92:95], v[44:47], v[212:215], v[92:95]\n"
        "ds_read_b128 v[208:211], v247 offset:34816\n"
        "v_accvgpr_read_b32 v183, a7\n"
        "v_mfma_f32_16x16x32_bf16 v[88:91], v[36:39], v[212:215], v[88:91]\n"
        "ds_read_b128 v[212:215], v247 offset:32768\n"
        "v_mul_f32 v177, s88, v177\n"
        "v_mfma_f32_16x16x32_bf16 v[88:91], v[32:35], v[196:199], v[88:91]\n"
        "global_load_lds_dwordx4 v243, s[86:87] offset:1024\n"
        "v_mfma_f32_16x16x32_bf16 v[120:123], v[32:35], v[200:203], v[120:123]\n"
        "ds_read_b128 v[196:199], v246 offset:32768\n"
        "v_mul_f32 v178, s88, v178\n"
        "v_mfma_f32_16x16x32_bf16 v[120:123], v[36:39], v[216:219], v[120:123]\n"
        "s_add_u32 s70, s38, s97\n"
        "s_addc_u32 s71, s39, 0\n"
        "v_mul_f32 v179, s88, v179\n"
        "v_mfma_f32_16x16x32_bf16 v[124:127], v[44:47], v[216:219], v[124:127]\n"
        "ds_read_b128 v[216:219], v247 offset:38912\n"
        "v_mul_f32 v180, s88, v180\n"
        "v_mfma_f32_16x16x32_bf16 v[124:127], v[40:43], v[200:203], v[124:127]\n"
        "global_load_lds_dwordx4 v244, s[86:87] offset:2048\n"
        "s_add_u32 s97, s52, 0x1000\n"
        "v_mfma_f32_16x16x32_bf16 v[156:159], v[40:43], v[204:207], v[156:159]\n"
        "ds_read_b128 v[200:203], v246 offset:38912\n"
        "v_mul_f32 v181, s88, v181\n"
        "v_mfma_f32_16x16x32_bf16 v[156:159], v[44:47], v[220:223], v[156:159]\n"
        "ds_read_b64_tr_b16 v[44:45], v228 offset:25600\n"
        "v_mul_f32 v182, s88, v182\n"
        "v_mfma_f32_16x16x32_bf16 v[152:155], v[36:39], v[220:223], v[152:155]\n"
        "ds_read_b128 v[220:223], v247 offset:36864\n"
        "v_mul_f32 v183, s88, v183\n"
        "v_mfma_f32_16x16x32_bf16 v[152:155], v[32:35], v[204:207], v[152:155]\n"
        "s_waitcnt lgkmcnt(5)\n"
        "global_load_lds_dwordx4 v245, s[86:87] offset:3072\n"
        "s_add_u32 m0, s72, 0x11000\n"
        "v_mfma_f32_16x16x32_bf16 v[0:3], v[48:51], v[192:195], v[0:3]\n"
        "ds_read_b128 v[204:207], v246 offset:36864\n"
        "v_accvgpr_read_b32 v184, a168\n"
        "v_mfma_f32_16x16x32_bf16 v[0:3], v[52:55], v[208:211], v[0:3]\n"
        "s_waitcnt lgkmcnt(5)\n"
        "v_mul_f32 v184, s88, v184\n"
        "v_cvt_pk_bf16_f32 v184, v184, v185\n"
        "v_mfma_f32_16x16x32_bf16 v[4:7], v[60:63], v[208:211], v[4:7]\n"
        "s_add_u32 s86, s66, 0x200\n"
        "s_addc_u32 s87, s67, 0\n"
        "v_cvt_pk_bf16_f32 v185, v186, v187\n"
        "v_mfma_f32_16x16x32_bf16 v[4:7], v[56:59], v[192:195], v[4:7]\n"
        "global_load_lds_dwordx4 v242, s[86:87] offset:0\n"
        "v_mfma_f32_16x16x32_bf16 v[68:71], v[56:59], v[196:199], v[68:71]\n"
        "s_waitcnt lgkmcnt(3)\n"
        "v_cvt_pk_bf16_f32 v186, v188, v189\n"
        "v_cvt_pk_bf16_f32 v187, v190, v191\n"
        "v_mfma_f32_16x16x32_bf16 v[68:71], v[60:63], v[212:215], v[68:71]\n"
        "ds_read_b64_tr_b16 v[32:33], v226 offset:17408\n"
        "v_accvgpr_write_b32 a196, v184\n"
        "v_mfma_f32_16x16x32_bf16 v[64:67], v[52:55], v[212:215], v[64:67]\n"
        "ds_read_b64_tr_b16 v[34:35], v227 offset:17408\n"
        "v_accvgpr_write_b32 a197, v185\n"
        "v_mfma_f32_16x16x32_bf16 v[64:67], v[48:51], v[196:199], v[64:67]\n"
        "ds_read_b64_tr_b16 v[36:37], v226 offset:25600\n"
        "v_accvgpr_write_b32 a198, v186\n"
        "v_mfma_f32_16x16x32_bf16 v[96:99], v[48:51], v[200:203], v[96:99]\n"
        "s_waitcnt lgkmcnt(3)\n"
        "v_accvgpr_write_b32 a199, v187\n"
        "v_accvgpr_read_b32 v185, a9\n"
        "v_mfma_f32_16x16x32_bf16 v[96:99], v[52:55], v[216:219], v[96:99]\n"
        "ds_read_b64_tr_b16 v[38:39], v227 offset:25600\n"
        "global_load_lds_dwordx4 v243, s[86:87] offset:1024\n"
        "v_mfma_f32_16x16x32_bf16 v[100:103], v[60:63], v[216:219], v[100:103]\n"
        "ds_read_b64_tr_b16 v[46:47], v229 offset:25600\n"
        "v_accvgpr_read_b32 v186, a10\n"
        "v_mfma_f32_16x16x32_bf16 v[100:103], v[56:59], v[200:203], v[100:103]\n"
        "ds_read_b64_tr_b16 v[40:41], v228 offset:17408\n"
        "v_accvgpr_read_b32 v187, a11\n"
        "v_mfma_f32_16x16x32_bf16 v[132:135], v[56:59], v[204:207], v[132:135]\n"
        "ds_read_b64_tr_b16 v[42:43], v229 offset:17408\n"
        "v_accvgpr_read_b32 v188, a12\n"
        "v_mfma_f32_16x16x32_bf16 v[132:135], v[60:63], v[220:223], v[132:135]\n"
        "global_load_lds_dwordx4 v244, s[86:87] offset:2048\n"
        "v_mfma_f32_16x16x32_bf16 v[128:131], v[52:55], v[220:223], v[128:131]\n"
        "ds_read_b64_tr_b16 v[52:53], v230 offset:25600\n"
        "v_accvgpr_read_b32 v189, a13\n"
        "v_mfma_f32_16x16x32_bf16 v[128:131], v[48:51], v[204:207], v[128:131]\n"
        "ds_read_b64_tr_b16 v[48:49], v230 offset:17408\n"
        "v_accvgpr_read_b32 v190, a14\n"
        "v_mfma_f32_16x16x32_bf16 v[8:11], v[160:163], v[192:195], v[8:11]\n"
        "ds_read_b64_tr_b16 v[50:51], v231 offset:17408\n"
        "v_accvgpr_read_b32 v191, a15\n"
        "v_mfma_f32_16x16x32_bf16 v[8:11], v[164:167], v[208:211], v[8:11]\n"
        "global_load_lds_dwordx4 v245, s[86:87] offset:3072\n"
        "s_add_u32 m0, s73, 0x1c000\n"
        "v_mfma_f32_16x16x32_bf16 v[12:15], v[172:175], v[208:211], v[12:15]\n"
        "ds_read_b64_tr_b16 v[54:55], v231 offset:25600\n"
        "s_add_u32 s86, s68, 0x280000\n"
        "s_addc_u32 s87, s69, 0\n"
        "v_mfma_f32_16x16x32_bf16 v[12:15], v[168:171], v[192:195], v[12:15]\n"
        "ds_read_b64_tr_b16 v[60:61], v232 offset:25600\n"
        "v_mul_f32 v185, s88, v185\n"
        "v_mfma_f32_16x16x32_bf16 v[76:79], v[168:171], v[196:199], v[76:79]\n"
        "ds_read_b64_tr_b16 v[62:63], v233 offset:25600\n"
        "v_mul_f32 v186, s88, v186\n"
        "v_mfma_f32_16x16x32_bf16 v[76:79], v[172:175], v[212:215], v[76:79]\n"
        "ds_read_b64_tr_b16 v[56:57], v232 offset:17408\n"
        "v_mul_f32 v187, s88, v187\n"
        "v_mfma_f32_16x16x32_bf16 v[72:75], v[164:167], v[212:215], v[72:75]\n"
        "ds_read_b64_tr_b16 v[58:59], v233 offset:17408\n"
        "v_mul_f32 v188, s88, v188\n"
        "v_mfma_f32_16x16x32_bf16 v[72:75], v[160:163], v[196:199], v[72:75]\n"
        "v_accvgpr_read_b32 v176, a0\n"
        "v_mul_f32 v176, s88, v176\n"
        "v_mfma_f32_16x16x32_bf16 v[104:107], v[160:163], v[200:203], v[104:107]\n"
        "v_cvt_pk_bf16_f32 v176, v176, v177\n"
        "v_cvt_pk_bf16_f32 v177, v178, v179\n"
        "v_mfma_f32_16x16x32_bf16 v[104:107], v[164:167], v[216:219], v[104:107]\n"
        "v_cvt_pk_bf16_f32 v178, v180, v181\n"
        "v_cvt_pk_bf16_f32 v179, v182, v183\n"
        "v_mfma_f32_16x16x32_bf16 v[108:111], v[172:175], v[216:219], v[108:111]\n"
        "v_accvgpr_write_b32 a200, v176\n"
        "v_accvgpr_write_b32 a201, v177\n"
        "v_mfma_f32_16x16x32_bf16 v[108:111], v[168:171], v[200:203], v[108:111]\n"
        "v_accvgpr_write_b32 a202, v178\n"
        "v_accvgpr_write_b32 a203, v179\n"
        "v_mfma_f32_16x16x32_bf16 v[140:143], v[168:171], v[204:207], v[140:143]\n"
        "s_waitcnt lgkmcnt(2)\n"
        "v_mul_f32 v189, s88, v189\n"
        "v_mul_f32 v190, s88, v190\n"
        "v_mfma_f32_16x16x32_bf16 v[140:143], v[172:175], v[220:223], v[140:143]\n"
        "v_mul_f32 v191, s88, v191\n"
        "v_accvgpr_read_b32 v177, a33\n"
        "v_mfma_f32_16x16x32_bf16 v[136:139], v[164:167], v[220:223], v[136:139]\n"
        "s_waitcnt vmcnt(13) lgkmcnt(0)\n"
        "v_accvgpr_read_b32 v178, a34\n"
        "v_accvgpr_read_b32 v179, a35\n"
        "v_mfma_f32_16x16x32_bf16 v[136:139], v[160:163], v[204:207], v[136:139]\n"
        "s_barrier\n"
        "v_mfma_f32_16x16x32_bf16 v[16:19], v[32:35], v[192:195], v[16:19]\n"
        "global_load_lds_dwordx4 v238, s[86:87] offset:0\n"
        "ds_read_b64_tr_b16 v[160:161], v226 offset:32768\n"
        "v_mfma_f32_16x16x32_bf16 v[16:19], v[36:39], v[208:211], v[16:19]\n"
        "ds_read_b64_tr_b16 v[162:163], v227 offset:32768\n"
        "v_accvgpr_read_b32 v180, a36\n"
        "v_mfma_f32_16x16x32_bf16 v[20:23], v[44:47], v[208:211], v[20:23]\n"
        "ds_read_b64_tr_b16 v[164:165], v226 offset:40960\n"
        "v_accvgpr_read_b32 v181, a37\n"
        "v_mfma_f32_16x16x32_bf16 v[20:23], v[40:43], v[192:195], v[20:23]\n"
        "ds_read_b64_tr_b16 v[166:167], v227 offset:40960\n"
        "v_accvgpr_read_b32 v182, a38\n"
        "v_mfma_f32_16x16x32_bf16 v[84:87], v[40:43], v[196:199], v[84:87]\n"
        "ds_read_b64_tr_b16 v[172:173], v228 offset:40960\n"
        "global_load_lds_dwordx4 v239, s[86:87] offset:1024\n"
        "v_mfma_f32_16x16x32_bf16 v[84:87], v[44:47], v[212:215], v[84:87]\n"
        "ds_read_b64_tr_b16 v[174:175], v229 offset:40960\n"
        "v_accvgpr_read_b32 v183, a39\n"
        "v_mfma_f32_16x16x32_bf16 v[80:83], v[36:39], v[212:215], v[80:83]\n"
        "ds_read_b64_tr_b16 v[168:169], v228 offset:32768\n"
        "v_mul_f32 v177, s88, v177\n"
        "v_mfma_f32_16x16x32_bf16 v[80:83], v[32:35], v[196:199], v[80:83]\n"
        "ds_read_b64_tr_b16 v[170:171], v229 offset:32768\n"
        "v_mul_f32 v178, s88, v178\n"
        "v_mfma_f32_16x16x32_bf16 v[112:115], v[32:35], v[200:203], v[112:115]\n"
        "global_load_lds_dwordx4 v240, s[86:87] offset:2048\n"
        "v_mfma_f32_16x16x32_bf16 v[112:115], v[36:39], v[216:219], v[112:115]\n"
        "v_mul_f32 v179, s88, v179\n"
        "v_mul_f32 v180, s88, v180\n"
        "v_mfma_f32_16x16x32_bf16 v[116:119], v[44:47], v[216:219], v[116:119]\n"
        "v_mul_f32 v181, s88, v181\n"
        "v_mul_f32 v182, s88, v182\n"
        "v_mfma_f32_16x16x32_bf16 v[116:119], v[40:43], v[200:203], v[116:119]\n"
        "v_mul_f32 v183, s88, v183\n"
        "v_mfma_f32_16x16x32_bf16 v[148:151], v[40:43], v[204:207], v[148:151]\n"
        "global_load_lds_dwordx4 v241, s[86:87] offset:3072\n"
        "s_add_u32 m0, s72, 0x8000\n"
        "v_mfma_f32_16x16x32_bf16 v[148:151], v[44:47], v[220:223], v[148:151]\n"
        "s_add_u32 s86, s64, 0x280\n"
        "s_addc_u32 s87, s65, 0\n"
        "v_mfma_f32_16x16x32_bf16 v[144:147], v[36:39], v[220:223], v[144:147]\n"
        "ds_read_b64_tr_b16 v[36:37], v230 offset:40960\n"
        "v_mfma_f32_16x16x32_bf16 v[144:147], v[32:35], v[204:207], v[144:147]\n"
        "ds_read_b64_tr_b16 v[32:33], v230 offset:32768\n"
        "v_accvgpr_read_b32 v184, a8\n"
        "v_mfma_f32_16x16x32_bf16 v[24:27], v[48:51], v[192:195], v[24:27]\n"
        "ds_read_b64_tr_b16 v[34:35], v231 offset:32768\n"
        "v_mul_f32 v184, s88, v184\n"
        "v_mfma_f32_16x16x32_bf16 v[24:27], v[52:55], v[208:211], v[24:27]\n"
        "global_load_lds_dwordx4 v242, s[86:87] offset:0\n"
        "v_cvt_pk_bf16_f32 v184, v184, v185\n"
        "v_mfma_f32_16x16x32_bf16 v[28:31], v[60:63], v[208:211], v[28:31]\n"
        "ds_read_b64_tr_b16 v[38:39], v231 offset:40960\n"
        "v_cvt_pk_bf16_f32 v185, v186, v187\n"
        "v_mfma_f32_16x16x32_bf16 v[28:31], v[56:59], v[192:195], v[28:31]\n"
        "ds_read_b64_tr_b16 v[44:45], v232 offset:40960\n"
        "v_cvt_pk_bf16_f32 v186, v188, v189\n"
        "v_mfma_f32_16x16x32_bf16 v[92:95], v[56:59], v[196:199], v[92:95]\n"
        "ds_read_b64_tr_b16 v[46:47], v233 offset:40960\n"
        "v_cvt_pk_bf16_f32 v187, v190, v191\n"
        "v_mfma_f32_16x16x32_bf16 v[92:95], v[60:63], v[212:215], v[92:95]\n"
        "ds_read_b64_tr_b16 v[40:41], v232 offset:32768\n"
        "v_accvgpr_write_b32 a204, v184\n"
        "v_mfma_f32_16x16x32_bf16 v[88:91], v[52:55], v[212:215], v[88:91]\n"
        "ds_read_b64_tr_b16 v[42:43], v233 offset:32768\n"
        "v_accvgpr_write_b32 a205, v185\n"
        "v_mfma_f32_16x16x32_bf16 v[88:91], v[48:51], v[196:199], v[88:91]\n"
        "ds_read_b128 v[192:195], v246 offset:2048\n"
        "v_accvgpr_write_b32 a206, v186\n"
        "v_mfma_f32_16x16x32_bf16 v[120:123], v[48:51], v[200:203], v[120:123]\n"
        "ds_read_b128 v[208:211], v247 offset:2048\n"
        "v_accvgpr_write_b32 a207, v187\n"
        "v_mfma_f32_16x16x32_bf16 v[120:123], v[52:55], v[216:219], v[120:123]\n"
        "global_load_lds_dwordx4 v243, s[86:87] offset:1024\n"
        "ds_read_b128 v[196:199], v246 offset:0\n"
        "v_mfma_f32_16x16x32_bf16 v[124:127], v[60:63], v[216:219], v[124:127]\n"
        "ds_read_b128 v[212:215], v247 offset:0\n"
        "v_accvgpr_read_b32 v185, a41\n"
        "v_mfma_f32_16x16x32_bf16 v[124:127], v[56:59], v[200:203], v[124:127]\n"
        "ds_read_b128 v[200:203], v246 offset:6144\n"
        "v_accvgpr_read_b32 v186, a42\n"
        "v_mfma_f32_16x16x32_bf16 v[156:159], v[56:59], v[204:207], v[156:159]\n"
        "ds_read_b128 v[216:219], v247 offset:6144\n"
        "v_accvgpr_read_b32 v187, a43\n"
        "v_mfma_f32_16x16x32_bf16 v[156:159], v[60:63], v[220:223], v[156:159]\n"
        "global_store_dwordx4 v254, a[232:235], s[70:71] offset:0 sc1\n"
        "ds_read_b64_tr_b16 v[60:61], v228 offset:41984\n"
        "ds_read_b64_tr_b16 v[62:63], v229 offset:41984\n"
        "v_accvgpr_read_b32 v188, a44\n"
        "v_mfma_f32_16x16x32_bf16 v[152:155], v[52:55], v[220:223], v[152:155]\n"
        "ds_read_b128 v[220:223], v247 offset:4096\n"
        "v_accvgpr_read_b32 v189, a45\n"
        "v_mfma_f32_16x16x32_bf16 v[152:155], v[48:51], v[204:207], v[152:155]\n"
        "ds_read_b128 v[204:207], v246 offset:4096\n"
        "s_waitcnt lgkmcnt(8)\n"
        "v_mfma_f32_16x16x32_bf16 v[0:3], v[160:163], v[192:195], v[0:3]\n"
        "global_load_lds_dwordx4 v244, s[86:87] offset:2048\n"
        "v_mfma_f32_16x16x32_bf16 v[0:3], v[164:167], v[208:211], v[0:3]\n"
        "ds_read_b64_tr_b16 v[48:49], v226 offset:33792\n"
        "v_accvgpr_read_b32 v190, a46\n"
        "v_mfma_f32_16x16x32_bf16 v[4:7], v[172:175], v[208:211], v[4:7]\n"
        "ds_read_b64_tr_b16 v[50:51], v227 offset:33792\n"
        "v_accvgpr_read_b32 v191, a47\n"
        "v_mfma_f32_16x16x32_bf16 v[4:7], v[168:171], v[192:195], v[4:7]\n"
        "ds_read_b64_tr_b16 v[52:53], v226 offset:41984\n"
        "s_waitcnt lgkmcnt(7)\n"
        "v_mfma_f32_16x16x32_bf16 v[68:71], v[168:171], v[196:199], v[68:71]\n"
        "global_load_lds_dwordx4 v245, s[86:87] offset:3072\n"
        "s_add_u32 m0, s72, 0x9000\n"
        "v_mfma_f32_16x16x32_bf16 v[68:71], v[172:175], v[212:215], v[68:71]\n"
        "ds_read_b64_tr_b16 v[54:55], v227 offset:41984\n"
        "s_add_u32 s86, s66, 0x280\n"
        "s_addc_u32 s87, s67, 0\n"
        "v_mfma_f32_16x16x32_bf16 v[64:67], v[164:167], v[212:215], v[64:67]\n"
        "ds_read_b64_tr_b16 v[56:57], v228 offset:33792\n"
        "v_mul_f32 v185, s88, v185\n"
        "v_mfma_f32_16x16x32_bf16 v[64:67], v[160:163], v[196:199], v[64:67]\n"
        "ds_read_b64_tr_b16 v[58:59], v229 offset:33792\n"
        "v_accvgpr_read_b32 v176, a32\n"
        "v_mfma_f32_16x16x32_bf16 v[96:99], v[160:163], v[200:203], v[96:99]\n"
        "global_load_lds_dwordx4 v242, s[86:87] offset:0\n"
        "v_mfma_f32_16x16x32_bf16 v[96:99], v[164:167], v[216:219], v[96:99]\n"
        "s_waitcnt lgkmcnt(5)\n"
        "v_mul_f32 v176, s88, v176\n"
        "v_cvt_pk_bf16_f32 v176, v176, v177\n"
        "v_mfma_f32_16x16x32_bf16 v[100:103], v[172:175], v[216:219], v[100:103]\n"
        "v_cvt_pk_bf16_f32 v177, v178, v179\n"
        "v_cvt_pk_bf16_f32 v178, v180, v181\n"
        "v_mfma_f32_16x16x32_bf16 v[100:103], v[168:171], v[200:203], v[100:103]\n"
        "LHA5_%=:\n"
        "s_cbranch_execz LHA9_%=\n"
        "v_cvt_pk_bf16_f32 v179, v182, v183\n"
        "v_accvgpr_write_b32 a160, v176\n"
        "v_mfma_f32_16x16x32_bf16 v[132:135], v[168:171], v[204:207], v[132:135]\n"
        "global_load_lds_dwordx4 v243, s[86:87] offset:1024\n"
        "v_mfma_f32_16x16x32_bf16 v[132:135], v[172:175], v[220:223], v[132:135]\n"
        "ds_read_b64_tr_b16 v[172:173], v232 offset:41984\n"
        "v_accvgpr_write_b32 a161, v177\n"
        "v_mfma_f32_16x16x32_bf16 v[128:131], v[164:167], v[220:223], v[128:131]\n"
        "ds_read_b64_tr_b16 v[164:165], v230 offset:41984\n"
        "v_accvgpr_write_b32 a162, v178\n"
        "v_mfma_f32_16x16x32_bf16 v[128:131], v[160:163], v[204:207], v[128:131]\n"
        "ds_read_b64_tr_b16 v[160:161], v230 offset:33792\n"
        "v_accvgpr_write_b32 a163, v179\n"
        "v_mfma_f32_16x16x32_bf16 v[8:11], v[32:35], v[192:195], v[8:11]\n"
        "global_load_lds_dwordx4 v244, s[86:87] offset:2048\n"
        "v_mfma_f32_16x16x32_bf16 v[8:11], v[36:39], v[208:211], v[8:11]\n"
        "ds_read_b64_tr_b16 v[162:163], v231 offset:33792\n"
        "v_mul_f32 v186, s88, v186\n"
        "v_mfma_f32_16x16x32_bf16 v[12:15], v[44:47], v[208:211], v[12:15]\n"
        "ds_read_b64_tr_b16 v[166:167], v231 offset:41984\n"
        "v_mul_f32 v187, s88, v187\n"
        "v_mfma_f32_16x16x32_bf16 v[12:15], v[40:43], v[192:195], v[12:15]\n"
        "ds_read_b64_tr_b16 v[174:175], v233 offset:41984\n"
        "v_mul_f32 v188, s88, v188\n"
        "v_mfma_f32_16x16x32_bf16 v[76:79], v[40:43], v[196:199], v[76:79]\n"
        "global_load_lds_dwordx4 v245, s[86:87] offset:3072\n"
        "s_add_u32 m0, s73, 0x20000\n"
        "v_mfma_f32_16x16x32_bf16 v[76:79], v[44:47], v[212:215], v[76:79]\n"
        "ds_read_b64_tr_b16 v[168:169], v232 offset:33792\n"
        "v_mul_f32 v189, s88, v189\n"
        "v_mfma_f32_16x16x32_bf16 v[72:75], v[36:39], v[212:215], v[72:75]\n"
        "ds_read_b64_tr_b16 v[170:171], v233 offset:33792\n"
        "v_mul_f32 v190, s88, v190\n"
        "v_mfma_f32_16x16x32_bf16 v[72:75], v[32:35], v[196:199], v[72:75]\n"
        "s_add_u32 s86, s68, 0x300000\n"
        "s_addc_u32 s87, s69, 0\n"
        "v_mul_f32 v191, s88, v191\n"
        "v_mfma_f32_16x16x32_bf16 v[104:107], v[32:35], v[200:203], v[104:107]\n"
        "v_accvgpr_read_b32 v177, a65\n"
        "v_accvgpr_read_b32 v178, a66\n"
        "v_mfma_f32_16x16x32_bf16 v[104:107], v[36:39], v[216:219], v[104:107]\n"
        "v_accvgpr_read_b32 v179, a67\n"
        "v_accvgpr_read_b32 v180, a68\n"
        "v_mfma_f32_16x16x32_bf16 v[108:111], v[44:47], v[216:219], v[108:111]\n"
        "v_accvgpr_read_b32 v181, a69\n"
        "v_accvgpr_read_b32 v182, a70\n"
        "v_mfma_f32_16x16x32_bf16 v[108:111], v[40:43], v[200:203], v[108:111]\n"
        "v_accvgpr_read_b32 v183, a71\n"
        "v_mul_f32 v177, s88, v177\n"
        "v_mfma_f32_16x16x32_bf16 v[140:143], v[40:43], v[204:207], v[140:143]\n"
        "s_waitcnt lgkmcnt(2)\n"
        "v_mul_f32 v178, s88, v178\n"
        "v_mul_f32 v179, s88, v179\n"
        "v_mfma_f32_16x16x32_bf16 v[140:143], v[44:47], v[220:223], v[140:143]\n"
        "v_mul_f32 v180, s88, v180\n"
        "v_mul_f32 v181, s88, v181\n"
        "v_mfma_f32_16x16x32_bf16 v[136:139], v[36:39], v[220:223], v[136:139]\n"
        "s_waitcnt vmcnt(13) lgkmcnt(0)\n"
        "v_mul_f32 v182, s88, v182\n"
        "v_mul_f32 v183, s88, v183\n"
        "v_mfma_f32_16x16x32_bf16 v[136:139], v[32:35], v[204:207], v[136:139]\n"
        "v_accvgpr_read_b32 v184, a40\n"
        "v_mul_f32 v184, s88, v184\n"
        "v_mfma_f32_16x16x32_bf16 v[16:19], v[48:51], v[192:195], v[16:19]\n"
        "v_cvt_pk_bf16_f32 v184, v184, v185\n"
        "v_cvt_pk_bf16_f32 v185, v186, v187\n"
        "v_mfma_f32_16x16x32_bf16 v[16:19], v[52:55], v[208:211], v[16:19]\n"
        "v_cvt_pk_bf16_f32 v186, v188, v189\n"
        "v_cvt_pk_bf16_f32 v187, v190, v191\n"
        "v_mfma_f32_16x16x32_bf16 v[20:23], v[60:63], v[208:211], v[20:23]\n"
        "v_accvgpr_write_b32 a164, v184\n"
        "v_accvgpr_write_b32 a165, v185\n"
        "v_mfma_f32_16x16x32_bf16 v[20:23], v[56:59], v[192:195], v[20:23]\n"
        "v_accvgpr_write_b32 a166, v186\n"
        "v_accvgpr_write_b32 a167, v187\n"
        "v_mfma_f32_16x16x32_bf16 v[84:87], v[56:59], v[196:199], v[84:87]\n"
        "s_barrier\n"
        "v_mfma_f32_16x16x32_bf16 v[84:87], v[60:63], v[212:215], v[84:87]\n"
        "global_load_lds_dwordx4 v238, s[86:87] offset:0\n"
        "ds_read_b64_tr_b16 v[32:33], v226 offset:49152\n"
        "v_mfma_f32_16x16x32_bf16 v[80:83], v[52:55], v[212:215], v[80:83]\n"
        "ds_read_b64_tr_b16 v[34:35], v227 offset:49152\n"
        "v_accvgpr_read_b32 v185, a73\n"
        "v_mfma_f32_16x16x32_bf16 v[80:83], v[48:51], v[196:199], v[80:83]\n"
        "ds_read_b64_tr_b16 v[36:37], v226 offset:57344\n"
        "v_accvgpr_read_b32 v186, a74\n"
        "v_mfma_f32_16x16x32_bf16 v[112:115], v[48:51], v[200:203], v[112:115]\n"
        "ds_read_b64_tr_b16 v[38:39], v227 offset:57344\n"
        "v_accvgpr_read_b32 v187, a75\n"
        "v_mfma_f32_16x16x32_bf16 v[112:115], v[52:55], v[216:219], v[112:115]\n"
        "ds_read_b64_tr_b16 v[44:45], v228 offset:57344\n"
        "global_load_lds_dwordx4 v239, s[86:87] offset:1024\n"
        "v_mfma_f32_16x16x32_bf16 v[116:119], v[60:63], v[216:219], v[116:119]\n"
        "ds_read_b64_tr_b16 v[46:47], v229 offset:57344\n"
        "v_accvgpr_read_b32 v188, a76\n"
        "v_mfma_f32_16x16x32_bf16 v[116:119], v[56:59], v[200:203], v[116:119]\n"
        "ds_read_b64_tr_b16 v[40:41], v228 offset:49152\n"
        "v_accvgpr_read_b32 v189, a77\n"
        "v_mfma_f32_16x16x32_bf16 v[148:151], v[56:59], v[204:207], v[148:151]\n"
        "ds_read_b64_tr_b16 v[42:43], v229 offset:49152\n"
        "v_accvgpr_read_b32 v190, a78\n"
        "v_mfma_f32_16x16x32_bf16 v[148:151], v[60:63], v[220:223], v[148:151]\n"
        "global_load_lds_dwordx4 v240, s[86:87] offset:2048\n"
        "v_mfma_f32_16x16x32_bf16 v[144:147], v[52:55], v[220:223], v[144:147]\n"
        "ds_read_b64_tr_b16 v[52:53], v230 offset:57344\n"
        "v_accvgpr_read_b32 v191, a79\n"
        "v_mfma_f32_16x16x32_bf16 v[144:147], v[48:51], v[204:207], v[144:147]\n"
        "ds_read_b64_tr_b16 v[48:49], v230 offset:49152\n"
        "v_mul_f32 v185, s88, v185\n"
        "v_mfma_f32_16x16x32_bf16 v[24:27], v[160:163], v[192:195], v[24:27]\n"
        "ds_read_b64_tr_b16 v[50:51], v231 offset:49152\n"
        "v_mul_f32 v186, s88, v186\n"
        "v_mfma_f32_16x16x32_bf16 v[24:27], v[164:167], v[208:211], v[24:27]\n"
        "global_load_lds_dwordx4 v241, s[86:87] offset:3072\n"
        "s_add_u32 m0, s72, 0x0\n"
        "v_mfma_f32_16x16x32_bf16 v[28:31], v[172:175], v[208:211], v[28:31]\n"
        "ds_read_b64_tr_b16 v[54:55], v231 offset:57344\n"
        "s_add_u32 s86, s64, 0x300\n"
        "s_addc_u32 s87, s65, 0\n"
        "v_mfma_f32_16x16x32_bf16 v[28:31], v[168:171], v[192:195], v[28:31]\n"
        "ds_read_b64_tr_b16 v[60:61], v232 offset:57344\n"
        "v_mul_f32 v187, s88, v187\n"
        "v_mfma_f32_16x16x32_bf16 v[92:95], v[168:171], v[196:199], v[92:95]\n"
        "ds_read_b64_tr_b16 v[62:63], v233 offset:57344\n"
        "v_mul_f32 v188, s88, v188\n"
        "v_mfma_f32_16x16x32_bf16 v[92:95], v[172:175], v[212:215], v[92:95]\n"
        "ds_read_b64_tr_b16 v[56:57], v232 offset:49152\n"
        "global_load_lds_dwordx4 v242, s[86:87] offset:0\n"
        "v_mfma_f32_16x16x32_bf16 v[88:91], v[164:167], v[212:215], v[88:91]\n"
        "ds_read_b64_tr_b16 v[58:59], v233 offset:49152\n"
        "v_mul_f32 v189, s88, v189\n"
        "v_mfma_f32_16x16x32_bf16 v[88:91], v[160:163], v[196:199], v[88:91]\n"
        "ds_read_b128 v[192:195], v248 offset:2048\n"
        "v_accvgpr_read_b32 v176, a64\n"
        "v_mfma_f32_16x16x32_bf16 v[120:123], v[160:163], v[200:203], v[120:123]\n"
        "ds_read_b128 v[208:211], v249 offset:2048\n"
        "v_mul_f32 v176, s88, v176\n"
        "v_mfma_f32_16x16x32_bf16 v[120:123], v[164:167], v[216:219], v[120:123]\n"
        "global_load_lds_dwordx4 v243, s[86:87] offset:1024\n"
        "v_mfma_f32_16x16x32_bf16 v[124:127], v[172:175], v[216:219], v[124:127]\n"
        "ds_read_b128 v[196:199], v248 offset:0\n"
        "v_cvt_pk_bf16_f32 v176, v176, v177\n"
        "v_mfma_f32_16x16x32_bf16 v[124:127], v[168:171], v[200:203], v[124:127]\n"
        "ds_read_b128 v[212:215], v249 offset:0\n"
        "v_cvt_pk_bf16_f32 v177, v178, v179\n"
        "v_mfma_f32_16x16x32_bf16 v[156:159], v[168:171], v[204:207], v[156:159]\n"
        "ds_read_b128 v[200:203], v248 offset:6144\n"
        "v_cvt_pk_bf16_f32 v178, v180, v181\n"
        "v_mfma_f32_16x16x32_bf16 v[156:159], v[172:175], v[220:223], v[156:159]\n"
        "ds_read_b128 v[216:219], v249 offset:6144\n"
        "v_cvt_pk_bf16_f32 v179, v182, v183\n"
        "v_mfma_f32_16x16x32_bf16 v[152:155], v[164:167], v[220:223], v[152:155]\n"
        "global_load_lds_dwordx4 v244, s[86:87] offset:2048\n"
        "v_accvgpr_write_b32 a168, v176\n"
        "v_mfma_f32_16x16x32_bf16 v[152:155], v[160:163], v[204:207], v[152:155]\n"
        "s_waitcnt lgkmcnt(5)\n"
        "v_accvgpr_write_b32 a169, v177\n"
        "v_accvgpr_write_b32 a170, v178\n"
        "v_mfma_f32_16x16x32_bf16 v[0:3], v[32:35], v[192:195], v[0:3]\n"
        "s_waitcnt lgkmcnt(4)\n"
        "v_accvgpr_write_b32 a171, v179\n"
        "v_mul_f32 v190, s88, v190\n"
        "v_mfma_f32_16x16x32_bf16 v[0:3], v[36:39], v[208:211], v[0:3]\n"
        "ds_read_b128 v[204:207], v248 offset:4096\n"
        "v_mul_f32 v191, s88, v191\n"
        "v_mfma_f32_16x16x32_bf16 v[4:7], v[44:47], v[208:211], v[4:7]\n"
        "global_store_dwordx4 v254, a[236:239], s[70:71] offset:1024 sc1\n"
        "ds_read_b128 v[220:223], v249 offset:4096\n"
        "ds_read_b64_tr_b16 v[160:161], v226 offset:50176\n"
        "v_accvgpr_read_b32 v177, a97\n"
        "v_mfma_f32_16x16x32_bf16 v[4:7], v[40:43], v[192:195], v[4:7]\n"
        "ds_read_b64_tr_b16 v[162:163], v227 offset:50176\n"
        "s_waitcnt lgkmcnt(5)\n"
        "v_mfma_f32_16x16x32_bf16 v[68:71], v[40:43], v[196:199], v[68:71]\n"
        "ds_read_b64_tr_b16 v[164:165], v226 offset:58368\n"
        "v_accvgpr_read_b32 v178, a98\n"
        "v_mfma_f32_16x16x32_bf16 v[68:71], v[44:47], v[212:215], v[68:71]\n"
        "global_load_lds_dwordx4 v245, s[86:87] offset:3072\n"
        "s_add_u32 m0, s72, 0x1000\n"
        "v_mfma_f32_16x16x32_bf16 v[64:67], v[36:39], v[212:215], v[64:67]\n"
        "ds_read_b64_tr_b16 v[166:167], v227 offset:58368\n"
        "s_add_u32 s86, s66, 0x300\n"
        "s_addc_u32 s87, s67, 0\n"
        "v_mfma_f32_16x16x32_bf16 v[64:67], v[32:35], v[196:199], v[64:67]\n"
        "ds_read_b64_tr_b16 v[172:173], v228 offset:58368\n"
        "v_accvgpr_read_b32 v179, a99\n"
        "v_mfma_f32_16x16x32_bf16 v[96:99], v[32:35], v[200:203], v[96:99]\n"
        "ds_read_b64_tr_b16 v[174:175], v229 offset:58368\n"
        "s_waitcnt lgkmcnt(7)\n"
        "v_mfma_f32_16x16x32_bf16 v[96:99], v[36:39], v[216:219], v[96:99]\n"
        "ds_read_b64_tr_b16 v[168:169], v228 offset:50176\n"
        "global_load_lds_dwordx4 v242, s[86:87] offset:0\n"
        "v_mfma_f32_16x16x32_bf16 v[100:103], v[44:47], v[216:219], v[100:103]\n"
        "ds_read_b64_tr_b16 v[170:171], v229 offset:50176\n"
        "v_accvgpr_read_b32 v180, a100\n"
        "v_mfma_f32_16x16x32_bf16 v[100:103], v[40:43], v[200:203], v[100:103]\n"
        "s_waitcnt lgkmcnt(6)\n"
        "v_accvgpr_read_b32 v181, a101\n"
        "v_accvgpr_read_b32 v182, a102\n"
        "v_mfma_f32_16x16x32_bf16 v[132:135], v[40:43], v[204:207], v[132:135]\n"
        "ds_read_b64_tr_b16 v[40:41], v232 offset:50176\n"
        "v_accvgpr_read_b32 v183, a103\n"
        "v_mfma_f32_16x16x32_bf16 v[132:135], v[44:47], v[220:223], v[132:135]\n"
        "global_load_lds_dwordx4 v243, s[86:87] offset:1024\n"
        "v_mfma_f32_16x16x32_bf16 v[128:131], v[36:39], v[220:223], v[128:131]\n"
        "ds_read_b64_tr_b16 v[36:37], v230 offset:58368\n"
        "v_mul_f32 v177, s88, v177\n"
        "v_mfma_f32_16x16x32_bf16 v[128:131], v[32:35], v[204:207], v[128:131]\n"
        "ds_read_b64_tr_b16 v[32:33], v230 offset:50176\n"
        "v_accvgpr_read_b32 v184, a72\n"
        "v_mfma_f32_16x16x32_bf16 v[8:11], v[48:51], v[192:195], v[8:11]\n"
        "ds_read_b64_tr_b16 v[34:35], v231 offset:50176\n"
        "v_mul_f32 v184, s88, v184\n"
        "v_mfma_f32_16x16x32_bf16 v[8:11], v[52:55], v[208:211], v[8:11]\n"
        "global_load_lds_dwordx4 v244, s[86:87] offset:2048\n"
        "v_mfma_f32_16x16x32_bf16 v[12:15], v[60:63], v[208:211], v[12:15]\n"
        "ds_read_b64_tr_b16 v[38:39], v231 offset:58368\n"
        "v_cvt_pk_bf16_f32 v184, v184, v185\n"
        "v_mfma_f32_16x16x32_bf16 v[12:15], v[56:59], v[192:195], v[12:15]\n"
        "ds_read_b64_tr_b16 v[44:45], v232 offset:58368\n"
        "v_cvt_pk_bf16_f32 v185, v186, v187\n"
        "v_mfma_f32_16x16x32_bf16 v[76:79], v[56:59], v[196:199], v[76:79]\n"
        "ds_read_b64_tr_b16 v[46:47], v233 offset:58368\n"
        "v_cvt_pk_bf16_f32 v186, v188, v189\n"
        "v_mfma_f32_16x16x32_bf16 v[76:79], v[60:63], v[212:215], v[76:79]\n"
        "global_load_lds_dwordx4 v245, s[86:87] offset:3072\n"
        "s_add_u32 m0, s73, 0x24000\n"
        "v_mfma_f32_16x16x32_bf16 v[72:75], v[52:55], v[212:215], v[72:75]\n"
        "ds_read_b64_tr_b16 v[42:43], v233 offset:50176\n"
        "v_cvt_pk_bf16_f32 v187, v190, v191\n"
        "v_mfma_f32_16x16x32_bf16 v[72:75], v[48:51], v[196:199], v[72:75]\n"
        "s_add_u32 s86, s68, 0x380000\n"
        "s_addc_u32 s87, s69, 0\n"
        "v_accvgpr_write_b32 a172, v184\n"
        "v_mfma_f32_16x16x32_bf16 v[104:107], v[48:51], v[200:203], v[104:107]\n"
        "s_add_u32 s70, s38, s97\n"
        "s_addc_u32 s71, s39, 0\n"
        "v_accvgpr_write_b32 a173, v185\n"
        "v_mfma_f32_16x16x32_bf16 v[104:107], v[52:55], v[216:219], v[104:107]\n"
        "s_add_u32 s97, s52, 0x0\n"
        "v_accvgpr_write_b32 a174, v186\n"
        "v_accvgpr_write_b32 a175, v187\n"
        "v_mfma_f32_16x16x32_bf16 v[108:111], v[60:63], v[216:219], v[108:111]\n"
        "s_add_u32 s97, s97, s75\n"
        "v_mul_f32 v178, s88, v178\n"
        "v_mul_f32 v179, s88, v179\n"
        "v_mfma_f32_16x16x32_bf16 v[108:111], v[56:59], v[200:203], v[108:111]\n"
        "v_mul_f32 v180, s88, v180\n"
        "v_mul_f32 v181, s88, v181\n"
        "v_mfma_f32_16x16x32_bf16 v[140:143], v[56:59], v[204:207], v[140:143]\n"
        "v_mul_f32 v182, s88, v182\n"
        "v_mul_f32 v183, s88, v183\n"
        "v_mfma_f32_16x16x32_bf16 v[140:143], v[60:63], v[220:223], v[140:143]\n"
        "s_waitcnt lgkmcnt(1)\n"
        "v_accvgpr_read_b32 v185, a105\n"
        "v_accvgpr_read_b32 v186, a106\n"
        "v_mfma_f32_16x16x32_bf16 v[136:139], v[52:55], v[220:223], v[136:139]\n"
        "s_waitcnt vmcnt(13) lgkmcnt(0)\n"
        "v_accvgpr_read_b32 v187, a107\n"
        "v_accvgpr_read_b32 v188, a108\n"
        "v_mfma_f32_16x16x32_bf16 v[136:139], v[48:51], v[204:207], v[136:139]\n"
        "s_barrier\n"
        "v_mfma_f32_16x16x32_bf16 v[16:19], v[160:163], v[192:195], v[16:19]\n"
        "global_load_lds_dwordx4 v238, s[86:87] offset:0\n"
        "ds_read_b64_tr_b16 v[48:49], v226 offset:16384\n"
        "v_mfma_f32_16x16x32_bf16 v[16:19], v[164:167], v[208:211], v[16:19]\n"
        "ds_read_b64_tr_b16 v[50:51], v227 offset:16384\n"
        "v_accvgpr_read_b32 v189, a109\n"
        "v_mfma_f32_16x16x32_bf16 v[20:23], v[172:175], v[208:211], v[20:23]\n"
        "ds_read_b64_tr_b16 v[52:53], v226 offset:24576\n"
        "v_accvgpr_read_b32 v190, a110\n"
        "v_mfma_f32_16x16x32_bf16 v[20:23], v[168:171], v[192:195], v[20:23]\n"
        "ds_read_b64_tr_b16 v[54:55], v227 offset:24576\n"
        "v_accvgpr_read_b32 v191, a111\n"
        "v_mfma_f32_16x16x32_bf16 v[84:87], v[168:171], v[196:199], v[84:87]\n"
        "ds_read_b64_tr_b16 v[60:61], v228 offset:24576\n"
        "global_load_lds_dwordx4 v239, s[86:87] offset:1024\n"
        "v_mfma_f32_16x16x32_bf16 v[84:87], v[172:175], v[212:215], v[84:87]\n"
        "ds_read_b64_tr_b16 v[62:63], v229 offset:24576\n"
        "v_mul_f32 v185, s88, v185\n"
        "v_mfma_f32_16x16x32_bf16 v[80:83], v[164:167], v[212:215], v[80:83]\n"
        "ds_read_b64_tr_b16 v[56:57], v228 offset:16384\n"
        "v_mul_f32 v186, s88, v186\n"
        "v_mfma_f32_16x16x32_bf16 v[80:83], v[160:163], v[196:199], v[80:83]\n"
        "ds_read_b64_tr_b16 v[58:59], v229 offset:16384\n"
        "v_accvgpr_read_b32 v176, a96\n"
        "v_mfma_f32_16x16x32_bf16 v[112:115], v[160:163], v[200:203], v[112:115]\n"
        "global_load_lds_dwordx4 v240, s[86:87] offset:2048\n"
        "v_mfma_f32_16x16x32_bf16 v[112:115], v[164:167], v[216:219], v[112:115]\n"
        "v_mul_f32 v176, s88, v176\n"
        "v_cvt_pk_bf16_f32 v176, v176, v177\n"
        "v_mfma_f32_16x16x32_bf16 v[116:119], v[172:175], v[216:219], v[116:119]\n"
        "v_cvt_pk_bf16_f32 v177, v178, v179\n"
        "v_cvt_pk_bf16_f32 v178, v180, v181\n"
        "v_mfma_f32_16x16x32_bf16 v[116:119], v[168:171], v[200:203], v[116:119]\n"
        "v_cvt_pk_bf16_f32 v179, v182, v183\n"
        "v_accvgpr_write_b32 a0, v176\n"
        "v_mfma_f32_16x16x32_bf16 v[148:151], v[168:171], v[204:207], v[148:151]\n"
        "ds_read_b64_tr_b16 v[168:169], v232 offset:16384\n"
        "v_accvgpr_write_b32 a1, v177\n"
        "v_mfma_f32_16x16x32_bf16 v[148:151], v[172:175], v[220:223], v[148:151]\n"
        "global_load_lds_dwordx4 v241, s[86:87] offset:3072\n"
        "s_add_u32 m0, s72, 0x10000\n"
        "v_accvgpr_write_b32 a2, v178\n"
        "v_mfma_f32_16x16x32_bf16 v[144:147], v[164:167], v[220:223], v[144:147]\n"
        "ds_read_b64_tr_b16 v[164:165], v230 offset:24576\n"
        "v_accvgpr_write_b32 a3, v179\n"
        "v_mfma_f32_16x16x32_bf16 v[144:147], v[160:163], v[204:207], v[144:147]\n"
        "ds_read_b64_tr_b16 v[160:161], v230 offset:16384\n"
        "s_add_u32 s86, s64, 0x380\n"
        "s_addc_u32 s87, s65, 0\n"
        "v_mfma_f32_16x16x32_bf16 v[24:27], v[32:35], v[192:195], v[24:27]\n"
        "ds_read_b64_tr_b16 v[162:163], v231 offset:16384\n"
        "v_mul_f32 v187, s88, v187\n"
        "v_mfma_f32_16x16x32_bf16 v[24:27], v[36:39], v[208:211], v[24:27]\n"
        "ds_read_b64_tr_b16 v[166:167], v231 offset:24576\n"
        "global_load_lds_dwordx4 v242, s[86:87] offset:0\n"
        "v_mfma_f32_16x16x32_bf16 v[28:31], v[44:47], v[208:211], v[28:31]\n"
        "ds_read_b64_tr_b16 v[172:173], v232 offset:24576\n"
        "v_mul_f32 v188, s88, v188\n"
        "v_mfma_f32_16x16x32_bf16 v[28:31], v[40:43], v[192:195], v[28:31]\n"
        "ds_read_b64_tr_b16 v[174:175], v233 offset:24576\n"
        "v_mul_f32 v189, s88, v189\n"
        "v_mfma_f32_16x16x32_bf16 v[92:95], v[40:43], v[196:199], v[92:95]\n"
        "ds_read_b64_tr_b16 v[170:171], v233 offset:16384\n"
        "v_mul_f32 v190, s88, v190\n"
        "v_mfma_f32_16x16x32_bf16 v[92:95], v[44:47], v[212:215], v[92:95]\n"
        "ds_read_b128 v[192:195], v246 offset:34816\n"
        "global_load_lds_dwordx4 v243, s[86:87] offset:1024\n"
        "v_mfma_f32_16x16x32_bf16 v[88:91], v[36:39], v[212:215], v[88:91]\n"
        "ds_read_b128 v[208:211], v247 offset:34816\n"
        "v_mul_f32 v191, s88, v191\n"
        "v_mfma_f32_16x16x32_bf16 v[88:91], v[32:35], v[196:199], v[88:91]\n"
        "ds_read_b128 v[196:199], v246 offset:32768\n"
        "v_accvgpr_read_b32 v177, a129\n"
        "v_mfma_f32_16x16x32_bf16 v[120:123], v[32:35], v[200:203], v[120:123]\n"
        "ds_read_b128 v[212:215], v247 offset:32768\n"
        "v_accvgpr_read_b32 v178, a130\n"
        "v_mfma_f32_16x16x32_bf16 v[120:123], v[36:39], v[216:219], v[120:123]\n"
        "global_load_lds_dwordx4 v244, s[86:87] offset:2048\n"
        "v_mfma_f32_16x16x32_bf16 v[124:127], v[44:47], v[216:219], v[124:127]\n"
        "ds_read_b128 v[216:219], v247 offset:38912\n"
        "v_accvgpr_read_b32 v179, a131\n"
        "v_mfma_f32_16x16x32_bf16 v[124:127], v[40:43], v[200:203], v[124:127]\n"
        "ds_read_b128 v[200:203], v246 offset:38912\n"
        "v_accvgpr_read_b32 v180, a132\n"
        "v_mfma_f32_16x16x32_bf16 v[156:159], v[40:43], v[204:207], v[156:159]\n"
        "ds_read_b64_tr_b16 v[40:41], v228 offset:17408\n"
        "v_accvgpr_read_b32 v181, a133\n"
        "v_mfma_f32_16x16x32_bf16 v[156:159], v[44:47], v[220:223], v[156:159]\n"
        "global_load_lds_dwordx4 v245, s[86:87] offset:3072\n"
        "s_add_u32 m0, s72, 0x11000\n"
        "v_mfma_f32_16x16x32_bf16 v[152:155], v[36:39], v[220:223], v[152:155]\n"
        "ds_read_b128 v[220:223], v247 offset:36864\n"
        "s_add_u32 s86, s66, 0x380\n"
        "s_addc_u32 s87, s67, 0\n"
        "v_mfma_f32_16x16x32_bf16 v[152:155], v[32:35], v[204:207], v[152:155]\n"
        "ds_read_b128 v[204:207], v246 offset:36864\n"
        "s_waitcnt lgkmcnt(6)\n"
        "v_mfma_f32_16x16x32_bf16 v[0:3], v[48:51], v[192:195], v[0:3]\n"
        "ds_read_b64_tr_b16 v[32:33], v226 offset:17408\n"
        "v_accvgpr_read_b32 v184, a104\n"
        "v_mfma_f32_16x16x32_bf16 v[0:3], v[52:55], v[208:211], v[0:3]\n"
        "s_waitcnt lgkmcnt(6)\n"
        "v_mul_f32 v184, s88, v184\n"
        "v_cvt_pk_bf16_f32 v184, v184, v185\n"
        "v_mfma_f32_16x16x32_bf16 v[4:7], v[60:63], v[208:211], v[4:7]\n"
        "global_load_lds_dwordx4 v242, s[86:87] offset:0\n"
        "v_cvt_pk_bf16_f32 v185, v186, v187\n"
        "v_mfma_f32_16x16x32_bf16 v[4:7], v[56:59], v[192:195], v[4:7]\n"
        "ds_read_b64_tr_b16 v[34:35], v227 offset:17408\n"
        "v_cvt_pk_bf16_f32 v186, v188, v189\n"
        "v_mfma_f32_16x16x32_bf16 v[68:71], v[56:59], v[196:199], v[68:71]\n"
        "s_waitcnt lgkmcnt(4)\n"
        "v_cvt_pk_bf16_f32 v187, v190, v191\n"
        "v_accvgpr_write_b32 a4, v184\n"
        "v_mfma_f32_16x16x32_bf16 v[68:71], v[60:63], v[212:215], v[68:71]\n"
        "ds_read_b64_tr_b16 v[36:37], v226 offset:25600\n"
        "v_accvgpr_write_b32 a5, v185\n"
        "v_mfma_f32_16x16x32_bf16 v[64:67], v[52:55], v[212:215], v[64:67]\n"
        "ds_read_b64_tr_b16 v[38:39], v227 offset:25600\n"
        "v_accvgpr_write_b32 a6, v186\n"
        "v_mfma_f32_16x16x32_bf16 v[64:67], v[48:51], v[196:199], v[64:67]\n"
        "ds_read_b64_tr_b16 v[44:45], v228 offset:25600\n"
        "v_accvgpr_write_b32 a7, v187\n"
        "v_mfma_f32_16x16x32_bf16 v[96:99], v[48:51], v[200:203], v[96:99]\n"
        "ds_read_b64_tr_b16 v[46:47], v229 offset:25600\n"
        "global_load_lds_dwordx4 v243, s[86:87] offset:1024\n"
        "v_mfma_f32_16x16x32_bf16 v[96:99], v[52:55], v[216:219], v[96:99]\n"
        "ds_read_b64_tr_b16 v[42:43], v229 offset:17408\n"
        "v_accvgpr_read_b32 v182, a134\n"
        "v_mfma_f32_16x16x32_bf16 v[100:103], v[60:63], v[216:219], v[100:103]\n"
        "s_waitcnt lgkmcnt(6)\n"
        "v_accvgpr_read_b32 v183, a135\n"
        "v_mul_f32 v177, s88, v177\n"
        "v_mfma_f32_16x16x32_bf16 v[100:103], v[56:59], v[200:203], v[100:103]\n"
        "v_mul_f32 v178, s88, v178\n"
        "v_mul_f32 v179, s88, v179\n"
        "v_mfma_f32_16x16x32_bf16 v[132:135], v[56:59], v[204:207], v[132:135]\n"
        "global_load_lds_dwordx4 v244, s[86:87] offset:2048\n"
        "v_mfma_f32_16x16x32_bf16 v[132:135], v[60:63], v[220:223], v[132:135]\n"
        "ds_read_b64_tr_b16 v[60:61], v232 offset:25600\n"
        "v_mul_f32 v180, s88, v180\n"
        "v_mfma_f32_16x16x32_bf16 v[128:131], v[52:55], v[220:223], v[128:131]\n"
        "ds_read_b64_tr_b16 v[52:53], v230 offset:25600\n"
        "v_mul_f32 v181, s88, v181\n"
        "v_mfma_f32_16x16x32_bf16 v[128:131], v[48:51], v[204:207], v[128:131]\n"
        "ds_read_b64_tr_b16 v[48:49], v230 offset:17408\n"
        "v_mul_f32 v182, s88, v182\n"
        "v_mfma_f32_16x16x32_bf16 v[8:11], v[160:163], v[192:195], v[8:11]\n"
        "global_load_lds_dwordx4 v245, s[86:87] offset:3072\n"
        "s_add_u32 m0, s73, 0x1c000\n"
        "v_mfma_f32_16x16x32_bf16 v[8:11], v[164:167], v[208:211], v[8:11]\n"
        "ds_read_b64_tr_b16 v[50:51], v231 offset:17408\n"
        "v_mul_f32 v183, s88, v183\n"
        "v_mfma_f32_16x16x32_bf16 v[12:15], v[172:175], v[208:211], v[12:15]\n"
        "ds_read_b64_tr_b16 v[54:55], v231 offset:25600\n"
        "s_add_u32 s86, s68, 0x400000\n"
        "s_addc_u32 s87, s69, 0\n"
        "v_mfma_f32_16x16x32_bf16 v[12:15], v[168:171], v[192:195], v[12:15]\n"
        "ds_read_b64_tr_b16 v[62:63], v233 offset:25600\n"
        "v_accvgpr_read_b32 v185, a137\n"
        "v_mfma_f32_16x16x32_bf16 v[76:79], v[168:171], v[196:199], v[76:79]\n"
        "ds_read_b64_tr_b16 v[56:57], v232 offset:17408\n"
        "v_accvgpr_read_b32 v186, a138\n"
        "v_mfma_f32_16x16x32_bf16 v[76:79], v[172:175], v[212:215], v[76:79]\n"
        "global_store_dwordx4 v254, a[192:195], s[70:71] offset:0 sc1\n"
        "ds_read_b64_tr_b16 v[58:59], v233 offset:17408\n"
        "v_accvgpr_read_b32 v187, a139\n"
        "v_accvgpr_read_b32 v188, a140\n"
        "v_accvgpr_read_b32 v189, a141\n"
        "v_mfma_f32_16x16x32_bf16 v[72:75], v[164:167], v[212:215], v[72:75]\n"
        "v_accvgpr_read_b32 v190, a142\n"
        "v_accvgpr_read_b32 v191, a143\n"
        "v_mfma_f32_16x16x32_bf16 v[72:75], v[160:163], v[196:199], v[72:75]\n"
        "v_accvgpr_read_b32 v176, a128\n"
        "v_mul_f32 v176, s88, v176\n"
        "v_mfma_f32_16x16x32_bf16 v[104:107], v[160:163], v[200:203], v[104:107]\n"
        "v_cvt_pk_bf16_f32 v176, v176, v177\n"
        "v_cvt_pk_bf16_f32 v177, v178, v179\n"
        "v_mfma_f32_16x16x32_bf16 v[104:107], v[164:167], v[216:219], v[104:107]\n"
        "v_cvt_pk_bf16_f32 v178, v180, v181\n"
        "v_cvt_pk_bf16_f32 v179, v182, v183\n"
        "v_mfma_f32_16x16x32_bf16 v[108:111], v[172:175], v[216:219], v[108:111]\n"
        "v_accvgpr_write_b32 a8, v176\n"
        "v_accvgpr_write_b32 a9, v177\n"
        "v_mfma_f32_16x16x32_bf16 v[108:111], v[168:171], v[200:203], v[108:111]\n"
        "v_accvgpr_write_b32 a10, v178\n"
        "v_accvgpr_write_b32 a11, v179\n"
        "v_mfma_f32_16x16x32_bf16 v[140:143], v[168:171], v[204:207], v[140:143]\n"
        "s_waitcnt vmcnt(13) lgkmcnt(0)\n"
        "v_mfma_f32_16x16x32_bf16 v[140:143], v[172:175], v[220:223], v[140:143]\n"
        "s_barrier\n"
        "v_mfma_f32_16x16x32_bf16 v[136:139], v[164:167], v[220:223], v[136:139]\n"
        "global_load_lds_dwordx4 v238, s[86:87] offset:0\n"
        "ds_read_b64_tr_b16 v[164:165], v226 offset:40960\n"
        "v_mfma_f32_16x16x32_bf16 v[136:139], v[160:163], v[204:207], v[136:139]\n"
        "ds_read_b64_tr_b16 v[160:161], v226 offset:32768\n"
        "v_mul_f32 v185, s88, v185\n"
        "v_mfma_f32_16x16x32_bf16 v[16:19], v[32:35], v[192:195], v[16:19]\n"
        "ds_read_b64_tr_b16 v[162:163], v227 offset:32768\n"
        "v_mul_f32 v186, s88, v186\n"
        "v_mfma_f32_16x16x32_bf16 v[16:19], v[36:39], v[208:211], v[16:19]\n"
        "ds_read_b64_tr_b16 v[166:167], v227 offset:40960\n"
        "v_mul_f32 v187, s88, v187\n"
        "v_mfma_f32_16x16x32_bf16 v[20:23], v[44:47], v[208:211], v[20:23]\n"
        "ds_read_b64_tr_b16 v[172:173], v228 offset:40960\n"
        "global_load_lds_dwordx4 v239, s[86:87] offset:1024\n"
        "v_mfma_f32_16x16x32_bf16 v[20:23], v[40:43], v[192:195], v[20:23]\n"
        "ds_read_b64_tr_b16 v[174:175], v229 offset:40960\n"
        "v_mul_f32 v188, s88, v188\n"
        "v_mfma_f32_16x16x32_bf16 v[84:87], v[40:43], v[196:199], v[84:87]\n"
        "ds_read_b64_tr_b16 v[168:169], v228 offset:32768\n"
        "v_mul_f32 v189, s88, v189\n"
        "v_mfma_f32_16x16x32_bf16 v[84:87], v[44:47], v[212:215], v[84:87]\n"
        "ds_read_b64_tr_b16 v[170:171], v229 offset:32768\n"
        "v_mul_f32 v190, s88, v190\n"
        "v_mfma_f32_16x16x32_bf16 v[80:83], v[36:39], v[212:215], v[80:83]\n"
        "global_load_lds_dwordx4 v240, s[86:87] offset:2048\n"
        "v_mfma_f32_16x16x32_bf16 v[80:83], v[32:35], v[196:199], v[80:83]\n"
        "v_mul_f32 v191, s88, v191\n"
        "v_accvgpr_read_b32 v177, a209\n"
        "v_mfma_f32_16x16x32_bf16 v[112:115], v[32:35], v[200:203], v[112:115]\n"
        "v_accvgpr_read_b32 v178, a210\n"
        "v_accvgpr_read_b32 v179, a211\n"
        "v_mfma_f32_16x16x32_bf16 v[112:115], v[36:39], v[216:219], v[112:115]\n"
        "v_accvgpr_read_b32 v180, a240\n"
        "v_accvgpr_read_b32 v181, a241\n"
        "v_mfma_f32_16x16x32_bf16 v[116:119], v[44:47], v[216:219], v[116:119]\n"
        "global_load_lds_dwordx4 v241, s[86:87] offset:3072\n"
        "s_add_u32 m0, s72, 0x8000\n"
        "v_mfma_f32_16x16x32_bf16 v[116:119], v[40:43], v[200:203], v[116:119]\n"
        "s_add_u32 s86, s64, 0x400\n"
        "s_addc_u32 s87, s65, 0\n"
        "v_accvgpr_read_b32 v182, a242\n"
        "v_mfma_f32_16x16x32_bf16 v[148:151], v[40:43], v[204:207], v[148:151]\n"
        "ds_read_b64_tr_b16 v[40:41], v232 offset:32768\n"
        "v_accvgpr_read_b32 v183, a243\n"
        "v_mfma_f32_16x16x32_bf16 v[148:151], v[44:47], v[220:223], v[148:151]\n"
        "ds_read_b64_tr_b16 v[44:45], v232 offset:40960\n"
        "v_mfma_f32_16x16x32_bf16 v[144:147], v[36:39], v[220:223], v[144:147]\n"
        "global_load_lds_dwordx4 v242, s[86:87] offset:0\n"
        "v_mfma_f32_16x16x32_bf16 v[144:147], v[32:35], v[204:207], v[144:147]\n"
        "ds_read_b64_tr_b16 v[32:33], v230 offset:32768\n"
        "v_accvgpr_read_b32 v184, a136\n"
        "v_mfma_f32_16x16x32_bf16 v[24:27], v[48:51], v[192:195], v[24:27]\n"
        "ds_read_b64_tr_b16 v[34:35], v231 offset:32768\n"
        "v_mul_f32 v184, s88, v184\n"
        "v_mfma_f32_16x16x32_bf16 v[24:27], v[52:55], v[208:211], v[24:27]\n"
        "ds_read_b64_tr_b16 v[36:37], v230 offset:40960\n"
        "v_cvt_pk_bf16_f32 v184, v184, v185\n"
        "v_mfma_f32_16x16x32_bf16 v[28:31], v[60:63], v[208:211], v[28:31]\n"
        "ds_read_b64_tr_b16 v[38:39], v231 offset:40960\n"
        "v_cvt_pk_bf16_f32 v185, v186, v187\n"
        "v_mfma_f32_16x16x32_bf16 v[28:31], v[56:59], v[192:195], v[28:31]\n"
        "ds_read_b64_tr_b16 v[46:47], v233 offset:40960\n"
        "v_cvt_pk_bf16_f32 v186, v188, v189\n"
        "v_mfma_f32_16x16x32_bf16 v[92:95], v[56:59], v[196:199], v[92:95]\n"
        "ds_read_b64_tr_b16 v[42:43], v233 offset:32768\n"
        "v_cvt_pk_bf16_f32 v187, v190, v191\n"
        "v_mfma_f32_16x16x32_bf16 v[92:95], v[60:63], v[212:215], v[92:95]\n"
        "ds_read_b128 v[192:195], v246 offset:2048\n"
        "v_accvgpr_write_b32 a12, v184\n"
        "v_mfma_f32_16x16x32_bf16 v[88:91], v[52:55], v[212:215], v[88:91]\n"
        "ds_read_b128 v[208:211], v247 offset:2048\n"
        "v_accvgpr_write_b32 a13, v185\n"
        "v_mfma_f32_16x16x32_bf16 v[88:91], v[48:51], v[196:199], v[88:91]\n"
        "global_load_lds_dwordx4 v243, s[86:87] offset:1024\n"
        "v_accvgpr_write_b32 a14, v186\n"
        "v_mfma_f32_16x16x32_bf16 v[120:123], v[48:51], v[200:203], v[120:123]\n"
        "ds_read_b128 v[196:199], v246 offset:0\n"
        "v_accvgpr_write_b32 a15, v187\n"
        "v_mfma_f32_16x16x32_bf16 v[120:123], v[52:55], v[216:219], v[120:123]\n"
        "ds_read_b128 v[212:215], v247 offset:0\n"
        "v_accvgpr_read_b32 v185, a213\n"
        "v_mfma_f32_16x16x32_bf16 v[124:127], v[60:63], v[216:219], v[124:127]\n"
        "ds_read_b128 v[216:219], v247 offset:6144\n"
        "v_accvgpr_read_b32 v186, a214\n"
        "v_mfma_f32_16x16x32_bf16 v[124:127], v[56:59], v[200:203], v[124:127]\n"
        "global_load_lds_dwordx4 v244, s[86:87] offset:2048\n"
        "ds_read_b128 v[200:203], v246 offset:6144\n"
        "v_mfma_f32_16x16x32_bf16 v[156:159], v[56:59], v[204:207], v[156:159]\n"
        "ds_read_b64_tr_b16 v[56:57], v228 offset:33792\n"
        "v_accvgpr_read_b32 v187, a215\n"
        "v_mfma_f32_16x16x32_bf16 v[156:159], v[60:63], v[220:223], v[156:159]\n"
        "ds_read_b64_tr_b16 v[60:61], v228 offset:41984\n"
        "v_accvgpr_read_b32 v188, a244\n"
        "v_mfma_f32_16x16x32_bf16 v[152:155], v[52:55], v[220:223], v[152:155]\n"
        "ds_read_b128 v[220:223], v247 offset:4096\n"
        "v_accvgpr_read_b32 v189, a245\n"
        "v_mfma_f32_16x16x32_bf16 v[152:155], v[48:51], v[204:207], v[152:155]\n"
        "global_load_lds_dwordx4 v245, s[86:87] offset:3072\n"
        "s_add_u32 m0, s72, 0x9000\n"
        "s_waitcnt lgkmcnt(7)\n"
        "v_mfma_f32_16x16x32_bf16 v[0:3], v[160:163], v[192:195], v[0:3]\n"
        "LHA6_%=:\n"
        "s_cbranch_execz LHA10_%=\n"
        "ds_read_b128 v[204:207], v246 offset:4096\n"
        "s_add_u32 s86, s66, 0x400\n"
        "s_addc_u32 s87, s67, 0\n"
        "v_mfma_f32_16x16x32_bf16 v[0:3], v[164:167], v[208:211], v[0:3]\n"
        "ds_read_b64_tr_b16 v[48:49], v226 offset:33792\n"
        "v_accvgpr_read_b32 v190, a246\n"
        "v_mfma_f32_16x16x32_bf16 v[4:7], v[172:175], v[208:211], v[4:7]\n"
        "ds_read_b64_tr_b16 v[50:51], v227 offset:33792\n"
        "v_accvgpr_read_b32 v191, a247\n"
        "v_mfma_f32_16x16x32_bf16 v[4:7], v[168:171], v[192:195], v[4:7]\n"
        "ds_read_b64_tr_b16 v[52:53], v226 offset:41984\n"
        "s_waitcnt lgkmcnt(7)\n"
        "v_mfma_f32_16x16x32_bf16 v[68:71], v[168:171], v[196:199], v[68:71]\n"
        "ds_read_b64_tr_b16 v[54:55], v227 offset:41984\n"
        "global_load_lds_dwordx4 v242, s[86:87] offset:0\n"
        "v_mfma_f32_16x16x32_bf16 v[68:71], v[172:175], v[212:215], v[68:71]\n"
        "ds_read_b64_tr_b16 v[62:63], v229 offset:41984\n"
        "v_mfma_f32_16x16x32_bf16 v[64:67], v[164:167], v[212:215], v[64:67]\n"
        "ds_read_b64_tr_b16 v[58:59], v229 offset:33792\n"
        "v_mfma_f32_16x16x32_bf16 v[64:67], v[160:163], v[196:199], v[64:67]\n"
        "v_accvgpr_read_b32 v176, a208\n"
        "v_cvt_pk_bf16_f32 v176, v176, v177\n"
        "v_mfma_f32_16x16x32_bf16 v[96:99], v[160:163], v[200:203], v[96:99]\n"
        "global_load_lds_dwordx4 v243, s[86:87] offset:1024\n"
        "v_mfma_f32_16x16x32_bf16 v[96:99], v[164:167], v[216:219], v[96:99]\n"
        "s_waitcnt lgkmcnt(5)\n"
        "v_cvt_pk_bf16_f32 v177, v178, v179\n"
        "v_cvt_pk_bf16_f32 v178, v180, v181\n"
        "v_mfma_f32_16x16x32_bf16 v[100:103], v[172:175], v[216:219], v[100:103]\n"
        "v_cvt_pk_bf16_f32 v179, v182, v183\n"
        "v_accvgpr_write_b32 a208, v176\n"
        "v_mfma_f32_16x16x32_bf16 v[100:103], v[168:171], v[200:203], v[100:103]\n"
        "v_accvgpr_write_b32 a209, v177\n"
        "v_accvgpr_write_b32 a210, v178\n"
        "v_mfma_f32_16x16x32_bf16 v[132:135], v[168:171], v[204:207], v[132:135]\n"
        "ds_read_b64_tr_b16 v[168:169], v232 offset:33792\n"
        "v_accvgpr_write_b32 a211, v179\n"
        "v_mfma_f32_16x16x32_bf16 v[132:135], v[172:175], v[220:223], v[132:135]\n"
        "global_store_dwordx4 v254, a[196:199], s[70:71] offset:1024 sc1\n"
        "ds_read_b64_tr_b16 v[172:173], v232 offset:41984\n"
        "ds_read_b64_tr_b16 v[174:175], v233 offset:41984\n"
        "v_accvgpr_read_b32 v177, a217\n"
        "v_mfma_f32_16x16x32_bf16 v[128:131], v[164:167], v[220:223], v[128:131]\n"
        "ds_read_b64_tr_b16 v[164:165], v230 offset:41984\n"
        "v_accvgpr_read_b32 v178, a218\n"
        "v_mfma_f32_16x16x32_bf16 v[128:131], v[160:163], v[204:207], v[128:131]\n"
        "ds_read_b64_tr_b16 v[160:161], v230 offset:33792\n"
        "v_accvgpr_read_b32 v179, a219\n"
        "v_mfma_f32_16x16x32_bf16 v[8:11], v[32:35], v[192:195], v[8:11]\n"
        "global_load_lds_dwordx4 v244, s[86:87] offset:2048\n"
        "v_mfma_f32_16x16x32_bf16 v[8:11], v[36:39], v[208:211], v[8:11]\n"
        "ds_read_b64_tr_b16 v[162:163], v231 offset:33792\n"
        "v_accvgpr_read_b32 v180, a248\n"
        "v_mfma_f32_16x16x32_bf16 v[12:15], v[44:47], v[208:211], v[12:15]\n"
        "ds_read_b64_tr_b16 v[166:167], v231 offset:41984\n"
        "v_accvgpr_read_b32 v181, a249\n"
        "v_mfma_f32_16x16x32_bf16 v[12:15], v[40:43], v[192:195], v[12:15]\n"
        "ds_read_b64_tr_b16 v[170:171], v233 offset:33792\n"
        "v_accvgpr_read_b32 v182, a250\n"
        "v_mfma_f32_16x16x32_bf16 v[76:79], v[40:43], v[196:199], v[76:79]\n"
        "global_load_lds_dwordx4 v245, s[86:87] offset:3072\n"
        "s_add_u32 m0, s73, 0x20000\n"
        "v_mfma_f32_16x16x32_bf16 v[76:79], v[44:47], v[212:215], v[76:79]\n"
        "s_add_u32 s86, s68, 0x480000\n"
        "s_addc_u32 s87, s69, 0\n"
        "v_accvgpr_read_b32 v183, a251\n"
        "v_mfma_f32_16x16x32_bf16 v[72:75], v[36:39], v[212:215], v[72:75]\n"
        "s_add_u32 s70, s38, s97\n"
        "s_addc_u32 s71, s39, 0\n"
        "s_add_u32 s97, s52, 0x1000\n"
        "v_mfma_f32_16x16x32_bf16 v[72:75], v[32:35], v[196:199], v[72:75]\n"
        "s_add_u32 s97, s97, s75\n"
        "v_mfma_f32_16x16x32_bf16 v[104:107], v[32:35], v[200:203], v[104:107]\n"
        "v_mfma_f32_16x16x32_bf16 v[104:107], v[36:39], v[216:219], v[104:107]\n"
        "v_mfma_f32_16x16x32_bf16 v[108:111], v[44:47], v[216:219], v[108:111]\n"
        "v_mfma_f32_16x16x32_bf16 v[108:111], v[40:43], v[200:203], v[108:111]\n"
        "s_waitcnt vmcnt(14) lgkmcnt(0)\n"
        "v_mfma_f32_16x16x32_bf16 v[140:143], v[40:43], v[204:207], v[140:143]\n"
        "s_barrier\n"
        "v_mfma_f32_16x16x32_bf16 v[140:143], v[44:47], v[220:223], v[140:143]\n"
        "global_load_lds_dwordx4 v238, s[86:87] offset:0\n"
        "ds_read_b64_tr_b16 v[44:45], v228 offset:57344\n"
        "v_mfma_f32_16x16x32_bf16 v[136:139], v[36:39], v[220:223], v[136:139]\n"
        "ds_read_b64_tr_b16 v[36:37], v226 offset:57344\n"
        "v_mfma_f32_16x16x32_bf16 v[136:139], v[32:35], v[204:207], v[136:139]\n"
        "ds_read_b64_tr_b16 v[32:33], v226 offset:49152\n"
        "v_accvgpr_read_b32 v184, a212\n"
        "v_mfma_f32_16x16x32_bf16 v[16:19], v[48:51], v[192:195], v[16:19]\n"
        "ds_read_b64_tr_b16 v[34:35], v227 offset:49152\n"
        "v_cvt_pk_bf16_f32 v184, v184, v185\n"
        "v_mfma_f32_16x16x32_bf16 v[16:19], v[52:55], v[208:211], v[16:19]\n"
        "ds_read_b64_tr_b16 v[38:39], v227 offset:57344\n"
        "v_cvt_pk_bf16_f32 v185, v186, v187\n"
        "v_mfma_f32_16x16x32_bf16 v[20:23], v[60:63], v[208:211], v[20:23]\n"
        "ds_read_b64_tr_b16 v[46:47], v229 offset:57344\n"
        "v_cvt_pk_bf16_f32 v186, v188, v189\n"
        "v_mfma_f32_16x16x32_bf16 v[20:23], v[56:59], v[192:195], v[20:23]\n"
        "ds_read_b64_tr_b16 v[40:41], v228 offset:49152\n"
        "v_cvt_pk_bf16_f32 v187, v190, v191\n"
        "v_mfma_f32_16x16x32_bf16 v[84:87], v[56:59], v[196:199], v[84:87]\n"
        "ds_read_b64_tr_b16 v[42:43], v229 offset:49152\n"
        "v_accvgpr_write_b32 a240, v184\n"
        "v_mfma_f32_16x16x32_bf16 v[84:87], v[60:63], v[212:215], v[84:87]\n"
        "global_load_lds_dwordx4 v239, s[86:87] offset:1024\n"
        "v_accvgpr_write_b32 a241, v185\n"
        "v_mfma_f32_16x16x32_bf16 v[80:83], v[52:55], v[212:215], v[80:83]\n"
        "v_accvgpr_write_b32 a242, v186\n"
        "v_accvgpr_write_b32 a243, v187\n"
        "v_mfma_f32_16x16x32_bf16 v[80:83], v[48:51], v[196:199], v[80:83]\n"
        "v_accvgpr_read_b32 v185, a221\n"
        "v_accvgpr_read_b32 v186, a222\n"
        "v_mfma_f32_16x16x32_bf16 v[112:115], v[48:51], v[200:203], v[112:115]\n"
        "v_accvgpr_read_b32 v187, a223\n"
        "v_accvgpr_read_b32 v188, a252\n"
        "v_mfma_f32_16x16x32_bf16 v[112:115], v[52:55], v[216:219], v[112:115]\n"
        "global_load_lds_dwordx4 v240, s[86:87] offset:2048\n"
        "v_accvgpr_read_b32 v189, a253\n"
        "v_mfma_f32_16x16x32_bf16 v[116:119], v[60:63], v[216:219], v[116:119]\n"
        "v_accvgpr_read_b32 v190, a254\n"
        "v_accvgpr_read_b32 v191, a255\n"
        "v_mfma_f32_16x16x32_bf16 v[116:119], v[56:59], v[200:203], v[116:119]\n"
        "v_mfma_f32_16x16x32_bf16 v[148:151], v[56:59], v[204:207], v[148:151]\n"
        "ds_read_b64_tr_b16 v[56:57], v232 offset:49152\n"
        "v_mfma_f32_16x16x32_bf16 v[148:151], v[60:63], v[220:223], v[148:151]\n"
        "global_load_lds_dwordx4 v241, s[86:87] offset:3072\n"
        "ds_read_b64_tr_b16 v[60:61], v232 offset:57344\n"
        "s_add_u32 m0, s72, 0x0\n"
        "v_mfma_f32_16x16x32_bf16 v[144:147], v[52:55], v[220:223], v[144:147]\n"
        "ds_read_b64_tr_b16 v[52:53], v230 offset:57344\n"
        "s_add_u32 s86, s64, 0x480\n"
        "s_addc_u32 s87, s65, 0\n"
        "v_mfma_f32_16x16x32_bf16 v[144:147], v[48:51], v[204:207], v[144:147]\n"
        "ds_read_b64_tr_b16 v[48:49], v230 offset:49152\n"
        "v_mfma_f32_16x16x32_bf16 v[24:27], v[160:163], v[192:195], v[24:27]\n"
        "ds_read_b64_tr_b16 v[50:51], v231 offset:49152\n"
        "v_mfma_f32_16x16x32_bf16 v[24:27], v[164:167], v[208:211], v[24:27]\n"
        "ds_read_b64_tr_b16 v[54:55], v231 offset:57344\n"
        "global_load_lds_dwordx4 v242, s[86:87] offset:0\n"
        "v_mfma_f32_16x16x32_bf16 v[28:31], v[172:175], v[208:211], v[28:31]\n"
        "ds_read_b64_tr_b16 v[62:63], v233 offset:57344\n"
        "v_mfma_f32_16x16x32_bf16 v[28:31], v[168:171], v[192:195], v[28:31]\n"
        "ds_read_b64_tr_b16 v[58:59], v233 offset:49152\n"
        "v_mfma_f32_16x16x32_bf16 v[92:95], v[168:171], v[196:199], v[92:95]\n"
        "ds_read_b128 v[192:195], v248 offset:2048\n"
        "v_mfma_f32_16x16x32_bf16 v[92:95], v[172:175], v[212:215], v[92:95]\n"
        "ds_read_b128 v[208:211], v249 offset:2048\n"
        "global_load_lds_dwordx4 v243, s[86:87] offset:1024\n"
        "v_mfma_f32_16x16x32_bf16 v[88:91], v[164:167], v[212:215], v[88:91]\n"
        "ds_read_b128 v[212:215], v249 offset:0\n"
        "v_mfma_f32_16x16x32_bf16 v[88:91], v[160:163], v[196:199], v[88:91]\n"
        "ds_read_b128 v[196:199], v248 offset:0\n"
        "v_accvgpr_read_b32 v176, a216\n"
        "v_mfma_f32_16x16x32_bf16 v[120:123], v[160:163], v[200:203], v[120:123]\n"
        "v_cvt_pk_bf16_f32 v176, v176, v177\n"
        "v_cvt_pk_bf16_f32 v177, v178, v179\n"
        "v_mfma_f32_16x16x32_bf16 v[120:123], v[164:167], v[216:219], v[120:123]\n"
        "v_cvt_pk_bf16_f32 v178, v180, v181\n"
        "v_cvt_pk_bf16_f32 v179, v182, v183\n"
        "v_mfma_f32_16x16x32_bf16 v[124:127], v[172:175], v[216:219], v[124:127]\n"
        "global_load_lds_dwordx4 v244, s[86:87] offset:2048\n"
        "v_accvgpr_write_b32 a212, v176\n"
        "v_mfma_f32_16x16x32_bf16 v[124:127], v[168:171], v[200:203], v[124:127]\n"
        "ds_read_b128 v[200:203], v248 offset:6144\n"
        "v_accvgpr_write_b32 a213, v177\n"
        "v_mfma_f32_16x16x32_bf16 v[156:159], v[168:171], v[204:207], v[156:159]\n"
        "s_waitcnt lgkmcnt(4)\n"
        "v_accvgpr_write_b32 a214, v178\n"
        "v_accvgpr_write_b32 a215, v179\n"
        "v_mfma_f32_16x16x32_bf16 v[156:159], v[172:175], v[220:223], v[156:159]\n"
        "ds_read_b128 v[216:219], v249 offset:6144\n"
        "v_accvgpr_read_b32 v177, a145\n"
        "v_mfma_f32_16x16x32_bf16 v[152:155], v[164:167], v[220:223], v[152:155]\n"
        "global_load_lds_dwordx4 v245, s[86:87] offset:3072\n"
        "ds_read_b128 v[220:223], v249 offset:4096\n"
        "s_add_u32 m0, s72, 0x1000\n"
        "v_mfma_f32_16x16x32_bf16 v[152:155], v[160:163], v[204:207], v[152:155]\n"
        "ds_read_b128 v[204:207], v248 offset:4096\n"
        "s_add_u32 s86, s66, 0x480\n"
        "s_addc_u32 s87, s67, 0\n"
        "v_mfma_f32_16x16x32_bf16 v[0:3], v[32:35], v[192:195], v[0:3]\n"
        "ds_read_b64_tr_b16 v[160:161], v226 offset:50176\n"
        "s_waitcnt lgkmcnt(5)\n"
        "v_mfma_f32_16x16x32_bf16 v[0:3], v[36:39], v[208:211], v[0:3]\n"
        "ds_read_b64_tr_b16 v[162:163], v227 offset:50176\n"
        "v_accvgpr_read_b32 v178, a146\n"
        "v_mfma_f32_16x16x32_bf16 v[4:7], v[44:47], v[208:211], v[4:7]\n"
        "ds_read_b64_tr_b16 v[164:165], v226 offset:58368\n"
        "global_load_lds_dwordx4 v242, s[86:87] offset:0\n"
        "v_mfma_f32_16x16x32_bf16 v[4:7], v[40:43], v[192:195], v[4:7]\n"
        "ds_read_b64_tr_b16 v[166:167], v227 offset:58368\n"
        "v_accvgpr_read_b32 v179, a147\n"
        "v_mfma_f32_16x16x32_bf16 v[68:71], v[40:43], v[196:199], v[68:71]\n"
        "ds_read_b64_tr_b16 v[172:173], v228 offset:58368\n"
        "v_accvgpr_read_b32 v180, a176\n"
        "v_mfma_f32_16x16x32_bf16 v[68:71], v[44:47], v[212:215], v[68:71]\n"
        "ds_read_b64_tr_b16 v[174:175], v229 offset:58368\n"
        "v_accvgpr_read_b32 v181, a177\n"
        "v_mfma_f32_16x16x32_bf16 v[64:67], v[36:39], v[212:215], v[64:67]\n"
        "ds_read_b64_tr_b16 v[168:169], v228 offset:50176\n"
        "global_load_lds_dwordx4 v243, s[86:87] offset:1024\n"
        "v_mfma_f32_16x16x32_bf16 v[64:67], v[32:35], v[196:199], v[64:67]\n"
        "ds_read_b64_tr_b16 v[170:171], v229 offset:50176\n"
        "s_waitcnt lgkmcnt(8)\n"
        "v_mfma_f32_16x16x32_bf16 v[96:99], v[32:35], v[200:203], v[96:99]\n"
        "v_accvgpr_read_b32 v182, a178\n"
        "v_accvgpr_read_b32 v183, a179\n"
        "v_mfma_f32_16x16x32_bf16 v[96:99], v[36:39], v[216:219], v[96:99]\n"
        "v_mfma_f32_16x16x32_bf16 v[100:103], v[44:47], v[216:219], v[100:103]\n"
        "global_load_lds_dwordx4 v244, s[86:87] offset:2048\n"
        "v_mfma_f32_16x16x32_bf16 v[100:103], v[40:43], v[200:203], v[100:103]\n"
        "v_mfma_f32_16x16x32_bf16 v[132:135], v[40:43], v[204:207], v[132:135]\n"
        "ds_read_b64_tr_b16 v[40:41], v232 offset:50176\n"
        "v_mfma_f32_16x16x32_bf16 v[132:135], v[44:47], v[220:223], v[132:135]\n"
        "ds_read_b64_tr_b16 v[44:45], v232 offset:58368\n"
        "v_mfma_f32_16x16x32_bf16 v[128:131], v[36:39], v[220:223], v[128:131]\n"
        "global_load_lds_dwordx4 v245, s[86:87] offset:3072\n"
        "s_add_u32 m0, s73, 0x24000\n"
        "v_mfma_f32_16x16x32_bf16 v[128:131], v[32:35], v[204:207], v[128:131]\n"
        "ds_read_b64_tr_b16 v[32:33], v230 offset:50176\n"
        "v_accvgpr_read_b32 v184, a220\n"
        "v_mfma_f32_16x16x32_bf16 v[8:11], v[48:51], v[192:195], v[8:11]\n"
        "ds_read_b64_tr_b16 v[34:35], v231 offset:50176\n"
        "v_cvt_pk_bf16_f32 v184, v184, v185\n"
        "v_mfma_f32_16x16x32_bf16 v[8:11], v[52:55], v[208:211], v[8:11]\n"
        "ds_read_b64_tr_b16 v[36:37], v230 offset:58368\n"
        "v_cvt_pk_bf16_f32 v185, v186, v187\n"
        "v_mfma_f32_16x16x32_bf16 v[12:15], v[60:63], v[208:211], v[12:15]\n"
        "ds_read_b64_tr_b16 v[38:39], v231 offset:58368\n"
        "v_cvt_pk_bf16_f32 v186, v188, v189\n"
        "v_mfma_f32_16x16x32_bf16 v[12:15], v[56:59], v[192:195], v[12:15]\n"
        "ds_read_b64_tr_b16 v[46:47], v233 offset:58368\n"
        "v_cvt_pk_bf16_f32 v187, v190, v191\n"
        "v_mfma_f32_16x16x32_bf16 v[76:79], v[56:59], v[196:199], v[76:79]\n"
        "ds_read_b64_tr_b16 v[42:43], v233 offset:50176\n"
        "v_accvgpr_write_b32 a244, v184\n"
        "v_mfma_f32_16x16x32_bf16 v[76:79], v[60:63], v[212:215], v[76:79]\n"
        "s_add_u32 s86, s68, 0x500000\n"
        "s_addc_u32 s87, s69, 0\n"
        "v_accvgpr_write_b32 a245, v185\n"
        "v_mfma_f32_16x16x32_bf16 v[72:75], v[52:55], v[212:215], v[72:75]\n"
        "v_accvgpr_write_b32 a246, v186\n"
        "v_accvgpr_write_b32 a247, v187\n"
        "v_mfma_f32_16x16x32_bf16 v[72:75], v[48:51], v[196:199], v[72:75]\n"
        "global_store_dwordx4 v254, a[200:203], s[70:71] offset:0 sc1\n"
        "v_accvgpr_read_b32 v185, a149\n"
        "v_accvgpr_read_b32 v186, a150\n"
        "v_accvgpr_read_b32 v187, a151\n"
        "v_accvgpr_read_b32 v188, a180\n"
        "v_mfma_f32_16x16x32_bf16 v[104:107], v[48:51], v[200:203], v[104:107]\n"
        "v_accvgpr_read_b32 v189, a181\n"
        "v_accvgpr_read_b32 v190, a182\n"
        "v_mfma_f32_16x16x32_bf16 v[104:107], v[52:55], v[216:219], v[104:107]\n"
        "v_accvgpr_read_b32 v191, a183\n"
        "v_mfma_f32_16x16x32_bf16 v[108:111], v[60:63], v[216:219], v[108:111]\n"
        "v_mfma_f32_16x16x32_bf16 v[108:111], v[56:59], v[200:203], v[108:111]\n"
        "s_waitcnt vmcnt(13) lgkmcnt(0)\n"
        "v_mfma_f32_16x16x32_bf16 v[140:143], v[56:59], v[204:207], v[140:143]\n"
        "s_barrier\n"
        "v_mfma_f32_16x16x32_bf16 v[140:143], v[60:63], v[220:223], v[140:143]\n"
        "global_load_lds_dwordx4 v238, s[86:87] offset:0\n"
        "ds_read_b64_tr_b16 v[60:61], v228 offset:24576\n"
        "v_mfma_f32_16x16x32_bf16 v[136:139], v[52:55], v[220:223], v[136:139]\n"
        "ds_read_b64_tr_b16 v[52:53], v226 offset:24576\n"
        "v_mfma_f32_16x16x32_bf16 v[136:139], v[48:51], v[204:207], v[136:139]\n"
        "ds_read_b64_tr_b16 v[48:49], v226 offset:16384\n"
        "v_mfma_f32_16x16x32_bf16 v[16:19], v[160:163], v[192:195], v[16:19]\n"
        "ds_read_b64_tr_b16 v[50:51], v227 offset:16384\n"
        "v_mfma_f32_16x16x32_bf16 v[16:19], v[164:167], v[208:211], v[16:19]\n"
        "ds_read_b64_tr_b16 v[54:55], v227 offset:24576\n"
        "global_load_lds_dwordx4 v239, s[86:87] offset:1024\n"
        "v_mfma_f32_16x16x32_bf16 v[20:23], v[172:175], v[208:211], v[20:23]\n"
        "ds_read_b64_tr_b16 v[62:63], v229 offset:24576\n"
        "v_mfma_f32_16x16x32_bf16 v[20:23], v[168:171], v[192:195], v[20:23]\n"
        "ds_read_b64_tr_b16 v[56:57], v228 offset:16384\n"
        "v_mfma_f32_16x16x32_bf16 v[84:87], v[168:171], v[196:199], v[84:87]\n"
        "ds_read_b64_tr_b16 v[58:59], v229 offset:16384\n"
        "v_mfma_f32_16x16x32_bf16 v[84:87], v[172:175], v[212:215], v[84:87]\n"
        "global_load_lds_dwordx4 v240, s[86:87] offset:2048\n"
        "v_mfma_f32_16x16x32_bf16 v[80:83], v[164:167], v[212:215], v[80:83]\n"
        "v_mfma_f32_16x16x32_bf16 v[80:83], v[160:163], v[196:199], v[80:83]\n"
        "v_accvgpr_read_b32 v176, a144\n"
        "v_cvt_pk_bf16_f32 v176, v176, v177\n"
        "v_mfma_f32_16x16x32_bf16 v[112:115], v[160:163], v[200:203], v[112:115]\n"
        "v_cvt_pk_bf16_f32 v177, v178, v179\n"
        "v_cvt_pk_bf16_f32 v178, v180, v181\n"
        "v_mfma_f32_16x16x32_bf16 v[112:115], v[164:167], v[216:219], v[112:115]\n"
        "v_cvt_pk_bf16_f32 v179, v182, v183\n"
        "v_accvgpr_write_b32 a216, v176\n"
        "v_mfma_f32_16x16x32_bf16 v[116:119], v[172:175], v[216:219], v[116:119]\n"
        "global_load_lds_dwordx4 v241, s[86:87] offset:3072\n"
        "s_add_u32 m0, s72, 0x10000\n"
        "v_accvgpr_write_b32 a217, v177\n"
        "v_mfma_f32_16x16x32_bf16 v[116:119], v[168:171], v[200:203], v[116:119]\n"
        "s_add_u32 s86, s64, 0x500\n"
        "s_addc_u32 s87, s65, 0\n"
        "v_accvgpr_write_b32 a218, v178\n"
        "v_mfma_f32_16x16x32_bf16 v[148:151], v[168:171], v[204:207], v[148:151]\n"
        "ds_read_b64_tr_b16 v[168:169], v232 offset:16384\n"
        "v_accvgpr_write_b32 a219, v179\n"
        "v_mfma_f32_16x16x32_bf16 v[148:151], v[172:175], v[220:223], v[148:151]\n"
        "ds_read_b64_tr_b16 v[172:173], v232 offset:24576\n"
        "v_accvgpr_read_b32 v177, a153\n"
        "v_mfma_f32_16x16x32_bf16 v[144:147], v[164:167], v[220:223], v[144:147]\n"
        "ds_read_b64_tr_b16 v[164:165], v230 offset:24576\n"
        "global_load_lds_dwordx4 v242, s[86:87] offset:0\n"
        "v_mfma_f32_16x16x32_bf16 v[144:147], v[160:163], v[204:207], v[144:147]\n"
        "ds_read_b64_tr_b16 v[160:161], v230 offset:16384\n"
        "v_accvgpr_read_b32 v178, a154\n"
        "v_mfma_f32_16x16x32_bf16 v[24:27], v[32:35], v[192:195], v[24:27]\n"
        "ds_read_b64_tr_b16 v[162:163], v231 offset:16384\n"
        "v_accvgpr_read_b32 v179, a155\n"
        "v_mfma_f32_16x16x32_bf16 v[24:27], v[36:39], v[208:211], v[24:27]\n"
        "ds_read_b64_tr_b16 v[166:167], v231 offset:24576\n"
        "v_accvgpr_read_b32 v180, a184\n"
        "v_mfma_f32_16x16x32_bf16 v[28:31], v[44:47], v[208:211], v[28:31]\n"
        "ds_read_b64_tr_b16 v[174:175], v233 offset:24576\n"
        "global_load_lds_dwordx4 v243, s[86:87] offset:1024\n"
        "v_mfma_f32_16x16x32_bf16 v[28:31], v[40:43], v[192:195], v[28:31]\n"
        "ds_read_b64_tr_b16 v[170:171], v233 offset:16384\n"
        "v_accvgpr_read_b32 v181, a185\n"
        "v_mfma_f32_16x16x32_bf16 v[92:95], v[40:43], v[196:199], v[92:95]\n"
        "ds_read_b128 v[192:195], v246 offset:34816\n"
        "v_accvgpr_read_b32 v182, a186\n"
        "v_mfma_f32_16x16x32_bf16 v[92:95], v[44:47], v[212:215], v[92:95]\n"
        "ds_read_b128 v[208:211], v247 offset:34816\n"
        "v_accvgpr_read_b32 v183, a187\n"
        "v_mfma_f32_16x16x32_bf16 v[88:91], v[36:39], v[212:215], v[88:91]\n"
        "ds_read_b128 v[212:215], v247 offset:32768\n"
        "global_load_lds_dwordx4 v244, s[86:87] offset:2048\n"
        "v_mfma_f32_16x16x32_bf16 v[88:91], v[32:35], v[196:199], v[88:91]\n"
        "ds_read_b128 v[196:199], v246 offset:32768\n"
        "v_mfma_f32_16x16x32_bf16 v[120:123], v[32:35], v[200:203], v[120:123]\n"
        "v_mfma_f32_16x16x32_bf16 v[120:123], v[36:39], v[216:219], v[120:123]\n"
        "v_mfma_f32_16x16x32_bf16 v[124:127], v[44:47], v[216:219], v[124:127]\n"
        "ds_read_b128 v[216:219], v247 offset:38912\n"
        "global_load_lds_dwordx4 v245, s[86:87] offset:3072\n"
        "s_add_u32 m0, s72, 0x11000\n"
        "v_mfma_f32_16x16x32_bf16 v[124:127], v[40:43], v[200:203], v[124:127]\n"
        "ds_read_b128 v[200:203], v246 offset:38912\n"
        "s_add_u32 s86, s66, 0x500\n"
        "s_addc_u32 s87, s67, 0\n"
        "v_mfma_f32_16x16x32_bf16 v[156:159], v[40:43], v[204:207], v[156:159]\n"
        "ds_read_b64_tr_b16 v[40:41], v228 offset:17408\n"
        "s_waitcnt lgkmcnt(6)\n"
        "v_mfma_f32_16x16x32_bf16 v[156:159], v[44:47], v[220:223], v[156:159]\n"
        "ds_read_b64_tr_b16 v[44:45], v228 offset:25600\n"
        "s_waitcnt lgkmcnt(6)\n"
        "v_mfma_f32_16x16x32_bf16 v[152:155], v[36:39], v[220:223], v[152:155]\n"
        "ds_read_b128 v[220:223], v247 offset:36864\n"
        "global_load_lds_dwordx4 v242, s[86:87] offset:0\n"
        "v_mfma_f32_16x16x32_bf16 v[152:155], v[32:35], v[204:207], v[152:155]\n"
        "ds_read_b128 v[204:207], v246 offset:36864\n"
        "v_accvgpr_read_b32 v184, a148\n"
        "v_mfma_f32_16x16x32_bf16 v[0:3], v[48:51], v[192:195], v[0:3]\n"
        "s_waitcnt lgkmcnt(6)\n"
        "v_cvt_pk_bf16_f32 v184, v184, v185\n"
        "v_cvt_pk_bf16_f32 v185, v186, v187\n"
        "v_mfma_f32_16x16x32_bf16 v[0:3], v[52:55], v[208:211], v[0:3]\n"
        "ds_read_b64_tr_b16 v[32:33], v226 offset:17408\n"
        "v_cvt_pk_bf16_f32 v186, v188, v189\n"
        "v_mfma_f32_16x16x32_bf16 v[4:7], v[60:63], v[208:211], v[4:7]\n"
        "ds_read_b64_tr_b16 v[34:35], v227 offset:17408\n"
        "v_cvt_pk_bf16_f32 v187, v190, v191\n"
        "v_mfma_f32_16x16x32_bf16 v[4:7], v[56:59], v[192:195], v[4:7]\n"
        "ds_read_b64_tr_b16 v[36:37], v226 offset:25600\n"
        "v_accvgpr_write_b32 a248, v184\n"
        "v_mfma_f32_16x16x32_bf16 v[68:71], v[56:59], v[196:199], v[68:71]\n"
        "s_waitcnt lgkmcnt(6)\n"
        "v_accvgpr_write_b32 a249, v185\n"
        "v_accvgpr_write_b32 a250, v186\n"
        "v_mfma_f32_16x16x32_bf16 v[68:71], v[60:63], v[212:215], v[68:71]\n"
        "ds_read_b64_tr_b16 v[38:39], v227 offset:25600\n"
        "v_accvgpr_write_b32 a251, v187\n"
        "v_mfma_f32_16x16x32_bf16 v[64:67], v[52:55], v[212:215], v[64:67]\n"
        "ds_read_b64_tr_b16 v[46:47], v229 offset:25600\n"
        "global_load_lds_dwordx4 v243, s[86:87] offset:1024\n"
        "v_mfma_f32_16x16x32_bf16 v[64:67], v[48:51], v[196:199], v[64:67]\n"
        "ds_read_b64_tr_b16 v[42:43], v229 offset:17408\n"
        "v_accvgpr_read_b32 v185, a157\n"
        "v_mfma_f32_16x16x32_bf16 v[96:99], v[48:51], v[200:203], v[96:99]\n"
        "s_waitcnt lgkmcnt(6)\n"
        "v_accvgpr_read_b32 v186, a158\n"
        "v_accvgpr_read_b32 v187, a159\n"
        "v_mfma_f32_16x16x32_bf16 v[96:99], v[52:55], v[216:219], v[96:99]\n"
        "v_accvgpr_read_b32 v188, a188\n"
        "v_accvgpr_read_b32 v189, a189\n"
        "v_mfma_f32_16x16x32_bf16 v[100:103], v[60:63], v[216:219], v[100:103]\n"
        "global_load_lds_dwordx4 v244, s[86:87] offset:2048\n"
        "v_mfma_f32_16x16x32_bf16 v[100:103], v[56:59], v[200:203], v[100:103]\n"
        "v_accvgpr_read_b32 v190, a190\n"
        "v_accvgpr_read_b32 v191, a191\n"
        "v_mfma_f32_16x16x32_bf16 v[132:135], v[56:59], v[204:207], v[132:135]\n"
        "ds_read_b64_tr_b16 v[56:57], v232 offset:17408\n"
        "v_mfma_f32_16x16x32_bf16 v[132:135], v[60:63], v[220:223], v[132:135]\n"
        "ds_read_b64_tr_b16 v[60:61], v232 offset:25600\n"
        "v_mfma_f32_16x16x32_bf16 v[128:131], v[52:55], v[220:223], v[128:131]\n"
        "global_load_lds_dwordx4 v245, s[86:87] offset:3072\n"
        "s_add_u32 m0, s73, 0x1c000\n"
        "v_mfma_f32_16x16x32_bf16 v[128:131], v[48:51], v[204:207], v[128:131]\n"
        "ds_read_b64_tr_b16 v[48:49], v230 offset:17408\n"
        "s_add_u32 s86, s68, 0x580000\n"
        "s_addc_u32 s87, s69, 0\n"
        "v_mfma_f32_16x16x32_bf16 v[8:11], v[160:163], v[192:195], v[8:11]\n"
        "ds_read_b64_tr_b16 v[50:51], v231 offset:17408\n"
        "v_mfma_f32_16x16x32_bf16 v[8:11], v[164:167], v[208:211], v[8:11]\n"
        "ds_read_b64_tr_b16 v[52:53], v230 offset:25600\n"
        "v_mfma_f32_16x16x32_bf16 v[12:15], v[172:175], v[208:211], v[12:15]\n"
        "ds_read_b64_tr_b16 v[54:55], v231 offset:25600\n"
        "v_mfma_f32_16x16x32_bf16 v[12:15], v[168:171], v[192:195], v[12:15]\n"
        "ds_read_b64_tr_b16 v[62:63], v233 offset:25600\n"
        "v_mfma_f32_16x16x32_bf16 v[76:79], v[168:171], v[196:199], v[76:79]\n"
        "ds_read_b64_tr_b16 v[58:59], v233 offset:17408\n"
        "v_mfma_f32_16x16x32_bf16 v[76:79], v[172:175], v[212:215], v[76:79]\n"
        "global_store_dwordx4 v254, a[204:207], s[70:71] offset:1024 sc1\n"
        "s_add_u32 s70, s38, s97\n"
        "s_addc_u32 s71, s39, 0\n"
        "v_mfma_f32_16x16x32_bf16 v[72:75], v[164:167], v[212:215], v[72:75]\n"
        "v_mfma_f32_16x16x32_bf16 v[72:75], v[160:163], v[196:199], v[72:75]\n"
        "v_accvgpr_read_b32 v176, a152\n"
        "v_cvt_pk_bf16_f32 v176, v176, v177\n"
        "v_mfma_f32_16x16x32_bf16 v[104:107], v[160:163], v[200:203], v[104:107]\n"
        "v_cvt_pk_bf16_f32 v177, v178, v179\n"
        "v_cvt_pk_bf16_f32 v178, v180, v181\n"
        "v_mfma_f32_16x16x32_bf16 v[104:107], v[164:167], v[216:219], v[104:107]\n"
        "v_cvt_pk_bf16_f32 v179, v182, v183\n"
        "v_accvgpr_write_b32 a220, v176\n"
        "v_mfma_f32_16x16x32_bf16 v[108:111], v[172:175], v[216:219], v[108:111]\n"
        "v_accvgpr_write_b32 a221, v177\n"
        "v_accvgpr_write_b32 a222, v178\n"
        "v_mfma_f32_16x16x32_bf16 v[108:111], v[168:171], v[200:203], v[108:111]\n"
        "s_waitcnt vmcnt(14) lgkmcnt(0)\n"
        "v_accvgpr_write_b32 a223, v179\n"
        "v_mfma_f32_16x16x32_bf16 v[140:143], v[168:171], v[204:207], v[140:143]\n"
        "s_barrier\n"
        "v_mfma_f32_16x16x32_bf16 v[140:143], v[172:175], v[220:223], v[140:143]\n"
        "global_load_lds_dwordx4 v238, s[86:87] offset:0\n"
        "ds_read_b64_tr_b16 v[172:173], v228 offset:40960\n"
        "v_mfma_f32_16x16x32_bf16 v[136:139], v[164:167], v[220:223], v[136:139]\n"
        "ds_read_b64_tr_b16 v[164:165], v226 offset:40960\n"
        "v_accvgpr_read_b32 v177, a17\n"
        "v_mfma_f32_16x16x32_bf16 v[136:139], v[160:163], v[204:207], v[136:139]\n"
        "ds_read_b64_tr_b16 v[160:161], v226 offset:32768\n"
        "s_add_u32 s97, s52, 0x2000\n"
        "v_accvgpr_read_b32 v178, a18\n"
        "v_mfma_f32_16x16x32_bf16 v[16:19], v[32:35], v[192:195], v[16:19]\n"
        "ds_read_b64_tr_b16 v[162:163], v227 offset:32768\n"
        "s_add_u32 s97, s97, s75\n"
        "v_accvgpr_read_b32 v179, a19\n"
        "v_mfma_f32_16x16x32_bf16 v[16:19], v[36:39], v[208:211], v[16:19]\n"
        "ds_read_b64_tr_b16 v[166:167], v227 offset:40960\n"
        "global_load_lds_dwordx4 v239, s[86:87] offset:1024\n"
        "v_mfma_f32_16x16x32_bf16 v[20:23], v[44:47], v[208:211], v[20:23]\n"
        "ds_read_b64_tr_b16 v[174:175], v229 offset:40960\n"
        "v_accvgpr_read_b32 v180, a48\n"
        "v_mfma_f32_16x16x32_bf16 v[20:23], v[40:43], v[192:195], v[20:23]\n"
        "ds_read_b64_tr_b16 v[168:169], v228 offset:32768\n"
        "v_accvgpr_read_b32 v181, a49\n"
        "v_mfma_f32_16x16x32_bf16 v[84:87], v[40:43], v[196:199], v[84:87]\n"
        "ds_read_b64_tr_b16 v[170:171], v229 offset:32768\n"
        "v_accvgpr_read_b32 v182, a50\n"
        "v_mfma_f32_16x16x32_bf16 v[84:87], v[44:47], v[212:215], v[84:87]\n"
        "global_load_lds_dwordx4 v240, s[86:87] offset:2048\n"
        "v_mfma_f32_16x16x32_bf16 v[80:83], v[36:39], v[212:215], v[80:83]\n"
        "v_accvgpr_read_b32 v183, a51\n"
        "v_accvgpr_write_b32 a48, 0\n"
        "v_mfma_f32_16x16x32_bf16 v[80:83], v[32:35], v[196:199], v[80:83]\n"
        "v_accvgpr_write_b32 a49, 0\n"
        "v_accvgpr_write_b32 a50, 0\n"
        "v_mfma_f32_16x16x32_bf16 v[112:115], v[32:35], v[200:203], v[112:115]\n"
        "v_accvgpr_write_b32 a51, 0\n"
        "v_mfma_f32_16x16x32_bf16 v[112:115], v[36:39], v[216:219], v[112:115]\n"
        "global_load_lds_dwordx4 v241, s[86:87] offset:3072\n"
        "s_add_u32 m0, s72, 0x8000\n"
        "v_mfma_f32_16x16x32_bf16 v[116:119], v[44:47], v[216:219], v[116:119]\n"
        "s_add_u32 s86, s64, 0x580\n"
        "s_addc_u32 s87, s65, 0\n"
        "v_mfma_f32_16x16x32_bf16 v[116:119], v[40:43], v[200:203], v[116:119]\n"
        "v_mfma_f32_16x16x32_bf16 v[148:151], v[40:43], v[204:207], v[148:151]\n"
        "ds_read_b64_tr_b16 v[40:41], v232 offset:32768\n"
        "v_mfma_f32_16x16x32_bf16 v[148:151], v[44:47], v[220:223], v[148:151]\n"
        "global_load_lds_dwordx4 v242, s[86:87] offset:0\n"
        "v_mfma_f32_16x16x32_bf16 v[144:147], v[36:39], v[220:223], v[144:147]\n"
        "ds_read_b64_tr_b16 v[36:37], v230 offset:40960\n"
        "v_mfma_f32_16x16x32_bf16 v[144:147], v[32:35], v[204:207], v[144:147]\n"
        "ds_read_b64_tr_b16 v[32:33], v230 offset:32768\n"
        "v_accvgpr_read_b32 v184, a156\n"
        "v_mfma_f32_16x16x32_bf16 v[24:27], v[48:51], v[192:195], v[24:27]\n"
        "ds_read_b64_tr_b16 v[34:35], v231 offset:32768\n"
        "v_cvt_pk_bf16_f32 v184, v184, v185\n"
        "v_mfma_f32_16x16x32_bf16 v[24:27], v[52:55], v[208:211], v[24:27]\n"
        "ds_read_b64_tr_b16 v[38:39], v231 offset:40960\n"
        "v_cvt_pk_bf16_f32 v185, v186, v187\n"
        "v_mfma_f32_16x16x32_bf16 v[28:31], v[60:63], v[208:211], v[28:31]\n"
        "ds_read_b64_tr_b16 v[44:45], v232 offset:40960\n"
        "v_cvt_pk_bf16_f32 v186, v188, v189\n"
        "v_mfma_f32_16x16x32_bf16 v[28:31], v[56:59], v[192:195], v[28:31]\n"
        "ds_read_b64_tr_b16 v[46:47], v233 offset:40960\n"
        "v_cvt_pk_bf16_f32 v187, v190, v191\n"
        "v_mfma_f32_16x16x32_bf16 v[92:95], v[56:59], v[196:199], v[92:95]\n"
        "ds_read_b64_tr_b16 v[42:43], v233 offset:32768\n"
        "v_accvgpr_write_b32 a252, v184\n"
        "v_mfma_f32_16x16x32_bf16 v[92:95], v[60:63], v[212:215], v[92:95]\n"
        "ds_read_b128 v[192:195], v246 offset:2048\n"
        "v_accvgpr_write_b32 a253, v185\n"
        "v_mfma_f32_16x16x32_bf16 v[88:91], v[52:55], v[212:215], v[88:91]\n"
        "ds_read_b128 v[208:211], v247 offset:2048\n"
        "v_accvgpr_write_b32 a254, v186\n"
        "v_mfma_f32_16x16x32_bf16 v[88:91], v[48:51], v[196:199], v[88:91]\n"
        "global_load_lds_dwordx4 v243, s[86:87] offset:1024\n"
        "v_accvgpr_write_b32 a255, v187\n"
        "v_mfma_f32_16x16x32_bf16 v[120:123], v[48:51], v[200:203], v[120:123]\n"
        "ds_read_b128 v[196:199], v246 offset:0\n"
        "v_accvgpr_read_b32 v185, a21\n"
        "v_mfma_f32_16x16x32_bf16 v[120:123], v[52:55], v[216:219], v[120:123]\n"
        "ds_read_b128 v[212:215], v247 offset:0\n"
        "v_accvgpr_read_b32 v186, a22\n"
        "v_mfma_f32_16x16x32_bf16 v[124:127], v[60:63], v[216:219], v[124:127]\n"
        "ds_read_b128 v[216:219], v247 offset:6144\n"
        "v_accvgpr_read_b32 v187, a23\n"
        "v_mfma_f32_16x16x32_bf16 v[124:127], v[56:59], v[200:203], v[124:127]\n"
        "global_load_lds_dwordx4 v244, s[86:87] offset:2048\n"
        "ds_read_b128 v[200:203], v246 offset:6144\n"
        "v_mfma_f32_16x16x32_bf16 v[156:159], v[56:59], v[204:207], v[156:159]\n"
        "ds_read_b64_tr_b16 v[56:57], v228 offset:33792\n"
        "v_accvgpr_read_b32 v188, a52\n"
        "v_mfma_f32_16x16x32_bf16 v[156:159], v[60:63], v[220:223], v[156:159]\n"
        "ds_read_b64_tr_b16 v[60:61], v228 offset:41984\n"
        "LHA7_%=:\n"
        "s_cbranch_execz LHA11_%=\n"
        "v_accvgpr_read_b32 v189, a53\n"
        "v_mfma_f32_16x16x32_bf16 v[152:155], v[52:55], v[220:223], v[152:155]\n"
        "ds_read_b128 v[220:223], v247 offset:4096\n"
        "v_accvgpr_read_b32 v190, a54\n"
        "v_mfma_f32_16x16x32_bf16 v[152:155], v[48:51], v[204:207], v[152:155]\n"
        "global_load_lds_dwordx4 v245, s[86:87] offset:3072\n"
        "s_add_u32 m0, s72, 0x9000\n"
        "s_waitcnt lgkmcnt(7)\n"
        "v_mfma_f32_16x16x32_bf16 v[0:3], v[160:163], v[192:195], v[0:3]\n"
        "ds_read_b128 v[204:207], v246 offset:4096\n"
        "s_add_u32 s86, s66, 0x580\n"
        "s_addc_u32 s87, s67, 0\n"
        "v_mfma_f32_16x16x32_bf16 v[0:3], v[164:167], v[208:211], v[0:3]\n"
        "ds_read_b64_tr_b16 v[48:49], v226 offset:33792\n"
        "v_accvgpr_read_b32 v191, a55\n"
        "v_mfma_f32_16x16x32_bf16 v[4:7], v[172:175], v[208:211], v[4:7]\n"
        "ds_read_b64_tr_b16 v[50:51], v227 offset:33792\n"
        "v_accvgpr_write_b32 a52, 0\n"
        "v_mfma_f32_16x16x32_bf16 v[4:7], v[168:171], v[192:195], v[4:7]\n"
        "ds_read_b64_tr_b16 v[52:53], v226 offset:41984\n"
        "s_waitcnt lgkmcnt(7)\n"
        "v_mfma_f32_16x16x32_bf16 v[68:71], v[168:171], v[196:199], v[68:71]\n"
        "ds_read_b64_tr_b16 v[54:55], v227 offset:41984\n"
        "global_load_lds_dwordx4 v242, s[86:87] offset:0\n"
        "v_mfma_f32_16x16x32_bf16 v[68:71], v[172:175], v[212:215], v[68:71]\n"
        "ds_read_b64_tr_b16 v[62:63], v229 offset:41984\n"
        "v_accvgpr_write_b32 a53, 0\n"
        "v_mfma_f32_16x16x32_bf16 v[64:67], v[164:167], v[212:215], v[64:67]\n"
        "ds_read_b64_tr_b16 v[58:59], v229 offset:33792\n"
        "v_accvgpr_write_b32 a54, 0\n"
        "v_mfma_f32_16x16x32_bf16 v[64:67], v[160:163], v[196:199], v[64:67]\n"
        "v_accvgpr_read_b32 v176, a16\n"
        "v_cvt_pk_bf16_f32 v176, v176, v177\n"
        "v_mfma_f32_16x16x32_bf16 v[96:99], v[160:163], v[200:203], v[96:99]\n"
        "global_load_lds_dwordx4 v243, s[86:87] offset:1024\n"
        "v_mfma_f32_16x16x32_bf16 v[96:99], v[164:167], v[216:219], v[96:99]\n"
        "s_waitcnt lgkmcnt(5)\n"
        "v_cvt_pk_bf16_f32 v177, v178, v179\n"
        "v_cvt_pk_bf16_f32 v178, v180, v181\n"
        "v_mfma_f32_16x16x32_bf16 v[100:103], v[172:175], v[216:219], v[100:103]\n"
        "v_cvt_pk_bf16_f32 v179, v182, v183\n"
        "v_accvgpr_write_b32 a176, v176\n"
        "v_mfma_f32_16x16x32_bf16 v[100:103], v[168:171], v[200:203], v[100:103]\n"
        "v_accvgpr_write_b32 a177, v177\n"
        "v_accvgpr_write_b32 a178, v178\n"
        "v_mfma_f32_16x16x32_bf16 v[132:135], v[168:171], v[204:207], v[132:135]\n"
        "ds_read_b64_tr_b16 v[168:169], v232 offset:33792\n"
        "v_accvgpr_write_b32 a179, v179\n"
        "v_mfma_f32_16x16x32_bf16 v[132:135], v[172:175], v[220:223], v[132:135]\n"
        "global_load_lds_dwordx4 v244, s[86:87] offset:2048\n"
        "ds_read_b64_tr_b16 v[172:173], v232 offset:41984\n"
        "v_mfma_f32_16x16x32_bf16 v[128:131], v[164:167], v[220:223], v[128:131]\n"
        "ds_read_b64_tr_b16 v[164:165], v230 offset:41984\n"
        "v_accvgpr_read_b32 v177, a25\n"
        "v_mfma_f32_16x16x32_bf16 v[128:131], v[160:163], v[204:207], v[128:131]\n"
        "ds_read_b64_tr_b16 v[160:161], v230 offset:33792\n"
        "v_accvgpr_read_b32 v178, a26\n"
        "v_mfma_f32_16x16x32_bf16 v[8:11], v[32:35], v[192:195], v[8:11]\n"
        "ds_read_b64_tr_b16 v[162:163], v231 offset:33792\n"
        "v_accvgpr_read_b32 v179, a27\n"
        "v_mfma_f32_16x16x32_bf16 v[8:11], v[36:39], v[208:211], v[8:11]\n"
        "global_load_lds_dwordx4 v245, s[86:87] offset:3072\n"
        "ds_read_b64_tr_b16 v[166:167], v231 offset:41984\n"
        "s_add_u32 m0, s73, 0x20000\n"
        "v_mfma_f32_16x16x32_bf16 v[12:15], v[44:47], v[208:211], v[12:15]\n"
        "ds_read_b64_tr_b16 v[174:175], v233 offset:41984\n"
        "s_add_u32 s86, s68, 0x600000\n"
        "s_addc_u32 s87, s69, 0\n"
        "v_mfma_f32_16x16x32_bf16 v[12:15], v[40:43], v[192:195], v[12:15]\n"
        "ds_read_b64_tr_b16 v[170:171], v233 offset:33792\n"
        "v_accvgpr_read_b32 v180, a56\n"
        "v_mfma_f32_16x16x32_bf16 v[76:79], v[40:43], v[196:199], v[76:79]\n"
        "v_accvgpr_read_b32 v181, a57\n"
        "v_accvgpr_read_b32 v182, a58\n"
        "v_mfma_f32_16x16x32_bf16 v[76:79], v[44:47], v[212:215], v[76:79]\n"
        "v_accvgpr_read_b32 v183, a59\n"
        "v_accvgpr_write_b32 a55, 0\n"
        "v_mfma_f32_16x16x32_bf16 v[72:75], v[36:39], v[212:215], v[72:75]\n"
        "v_accvgpr_write_b32 a56, 0\n"
        "v_accvgpr_write_b32 a57, 0\n"
        "v_mfma_f32_16x16x32_bf16 v[72:75], v[32:35], v[196:199], v[72:75]\n"
        "v_accvgpr_write_b32 a58, 0\n"
        "v_accvgpr_write_b32 a59, 0\n"
        "v_mfma_f32_16x16x32_bf16 v[104:107], v[32:35], v[200:203], v[104:107]\n"
        "v_mfma_f32_16x16x32_bf16 v[104:107], v[36:39], v[216:219], v[104:107]\n"
        "v_mfma_f32_16x16x32_bf16 v[108:111], v[44:47], v[216:219], v[108:111]\n"
        "v_mfma_f32_16x16x32_bf16 v[108:111], v[40:43], v[200:203], v[108:111]\n"
        "global_store_dwordx4 v254, a[160:163], s[70:71] offset:0 sc1\n"
        "s_waitcnt vmcnt(14) lgkmcnt(0)\n"
        "s_barrier\n"
        "v_mfma_f32_16x16x32_bf16 v[140:143], v[40:43], v[204:207], v[140:143]\n"
        "ds_read_b64_tr_b16 v[40:41], v228 offset:49152\n"
        "v_mfma_f32_16x16x32_bf16 v[140:143], v[44:47], v[220:223], v[140:143]\n"
        "ds_read_b64_tr_b16 v[44:45], v228 offset:57344\n"
        "v_mfma_f32_16x16x32_bf16 v[136:139], v[36:39], v[220:223], v[136:139]\n"
        "global_load_lds_dwordx4 v238, s[86:87] offset:0\n"
        "v_mfma_f32_16x16x32_bf16 v[136:139], v[32:35], v[204:207], v[136:139]\n"
        "ds_read_b64_tr_b16 v[32:33], v226 offset:49152\n"
        "v_accvgpr_read_b32 v184, a20\n"
        "v_mfma_f32_16x16x32_bf16 v[16:19], v[48:51], v[192:195], v[16:19]\n"
        "ds_read_b64_tr_b16 v[34:35], v227 offset:49152\n"
        "v_cvt_pk_bf16_f32 v184, v184, v185\n"
        "v_mfma_f32_16x16x32_bf16 v[16:19], v[52:55], v[208:211], v[16:19]\n"
        "ds_read_b64_tr_b16 v[36:37], v226 offset:57344\n"
        "v_cvt_pk_bf16_f32 v185, v186, v187\n"
        "v_mfma_f32_16x16x32_bf16 v[20:23], v[60:63], v[208:211], v[20:23]\n"
        "ds_read_b64_tr_b16 v[38:39], v227 offset:57344\n"
        "v_cvt_pk_bf16_f32 v186, v188, v189\n"
        "v_mfma_f32_16x16x32_bf16 v[20:23], v[56:59], v[192:195], v[20:23]\n"
        "ds_read_b64_tr_b16 v[46:47], v229 offset:57344\n"
        "v_cvt_pk_bf16_f32 v187, v190, v191\n"
        "v_mfma_f32_16x16x32_bf16 v[84:87], v[56:59], v[196:199], v[84:87]\n"
        "ds_read_b64_tr_b16 v[42:43], v229 offset:49152\n"
        "v_accvgpr_write_b32 a180, v184\n"
        "v_mfma_f32_16x16x32_bf16 v[84:87], v[60:63], v[212:215], v[84:87]\n"
        "global_load_lds_dwordx4 v239, s[86:87] offset:1024\n"
        "v_accvgpr_write_b32 a181, v185\n"
        "v_mfma_f32_16x16x32_bf16 v[80:83], v[52:55], v[212:215], v[80:83]\n"
        "v_accvgpr_write_b32 a182, v186\n"
        "v_accvgpr_write_b32 a183, v187\n"
        "v_mfma_f32_16x16x32_bf16 v[80:83], v[48:51], v[196:199], v[80:83]\n"
        "v_accvgpr_read_b32 v185, a29\n"
        "v_accvgpr_read_b32 v186, a30\n"
        "v_mfma_f32_16x16x32_bf16 v[112:115], v[48:51], v[200:203], v[112:115]\n"
        "v_accvgpr_read_b32 v187, a31\n"
        "v_accvgpr_read_b32 v188, a60\n"
        "v_mfma_f32_16x16x32_bf16 v[112:115], v[52:55], v[216:219], v[112:115]\n"
        "global_load_lds_dwordx4 v240, s[86:87] offset:2048\n"
        "v_accvgpr_read_b32 v189, a61\n"
        "v_mfma_f32_16x16x32_bf16 v[116:119], v[60:63], v[216:219], v[116:119]\n"
        "v_accvgpr_read_b32 v190, a62\n"
        "v_accvgpr_read_b32 v191, a63\n"
        "v_mfma_f32_16x16x32_bf16 v[116:119], v[56:59], v[200:203], v[116:119]\n"
        "v_accvgpr_write_b32 a60, 0\n"
        "v_accvgpr_write_b32 a61, 0\n"
        "v_mfma_f32_16x16x32_bf16 v[148:151], v[56:59], v[204:207], v[148:151]\n"
        "ds_read_b64_tr_b16 v[56:57], v232 offset:49152\n"
        "v_accvgpr_write_b32 a62, 0\n"
        "v_mfma_f32_16x16x32_bf16 v[148:151], v[60:63], v[220:223], v[148:151]\n"
        "global_load_lds_dwordx4 v241, s[86:87] offset:3072\n"
        "ds_read_b64_tr_b16 v[60:61], v232 offset:57344\n"
        "s_add_u32 m0, s72, 0x0\n"
        "v_mfma_f32_16x16x32_bf16 v[144:147], v[52:55], v[220:223], v[144:147]\n"
        "ds_read_b64_tr_b16 v[52:53], v230 offset:57344\n"
        "s_add_u32 s86, s64, 0x600\n"
        "s_addc_u32 s87, s65, 0\n"
        "v_mfma_f32_16x16x32_bf16 v[144:147], v[48:51], v[204:207], v[144:147]\n"
        "ds_read_b64_tr_b16 v[48:49], v230 offset:49152\n"
        "v_accvgpr_write_b32 a63, 0\n"
        "v_mfma_f32_16x16x32_bf16 v[24:27], v[160:163], v[192:195], v[24:27]\n"
        "ds_read_b64_tr_b16 v[50:51], v231 offset:49152\n"
        "v_mfma_f32_16x16x32_bf16 v[24:27], v[164:167], v[208:211], v[24:27]\n"
        "ds_read_b64_tr_b16 v[54:55], v231 offset:57344\n"
        "global_load_lds_dwordx4 v242, s[86:87] offset:0\n"
        "v_mfma_f32_16x16x32_bf16 v[28:31], v[172:175], v[208:211], v[28:31]\n"
        "ds_read_b64_tr_b16 v[62:63], v233 offset:57344\n"
        "v_mfma_f32_16x16x32_bf16 v[28:31], v[168:171], v[192:195], v[28:31]\n"
        "ds_read_b64_tr_b16 v[58:59], v233 offset:49152\n"
        "v_mfma_f32_16x16x32_bf16 v[92:95], v[168:171], v[196:199], v[92:95]\n"
        "ds_read_b128 v[192:195], v248 offset:2048\n"
        "v_mfma_f32_16x16x32_bf16 v[92:95], v[172:175], v[212:215], v[92:95]\n"
        "ds_read_b128 v[208:211], v249 offset:2048\n"
        "global_load_lds_dwordx4 v243, s[86:87] offset:1024\n"
        "v_mfma_f32_16x16x32_bf16 v[88:91], v[164:167], v[212:215], v[88:91]\n"
        "ds_read_b128 v[212:215], v249 offset:0\n"
        "v_mfma_f32_16x16x32_bf16 v[88:91], v[160:163], v[196:199], v[88:91]\n"
        "ds_read_b128 v[196:199], v248 offset:0\n"
        "v_accvgpr_read_b32 v176, a24\n"
        "v_mfma_f32_16x16x32_bf16 v[120:123], v[160:163], v[200:203], v[120:123]\n"
        "v_cvt_pk_bf16_f32 v176, v176, v177\n"
        "v_cvt_pk_bf16_f32 v177, v178, v179\n"
        "v_mfma_f32_16x16x32_bf16 v[120:123], v[164:167], v[216:219], v[120:123]\n"
        "v_cvt_pk_bf16_f32 v178, v180, v181\n"
        "v_cvt_pk_bf16_f32 v179, v182, v183\n"
        "v_mfma_f32_16x16x32_bf16 v[124:127], v[172:175], v[216:219], v[124:127]\n"
        "global_load_lds_dwordx4 v244, s[86:87] offset:2048\n"
        "v_accvgpr_write_b32 a184, v176\n"
        "v_mfma_f32_16x16x32_bf16 v[124:127], v[168:171], v[200:203], v[124:127]\n"
        "ds_read_b128 v[200:203], v248 offset:6144\n"
        "v_accvgpr_write_b32 a185, v177\n"
        "v_mfma_f32_16x16x32_bf16 v[156:159], v[168:171], v[204:207], v[156:159]\n"
        "s_waitcnt lgkmcnt(4)\n"
        "v_accvgpr_write_b32 a186, v178\n"
        "v_accvgpr_write_b32 a187, v179\n"
        "v_mfma_f32_16x16x32_bf16 v[156:159], v[172:175], v[220:223], v[156:159]\n"
        "ds_read_b128 v[216:219], v249 offset:6144\n"
        "v_accvgpr_read_b32 v177, a81\n"
        "v_mfma_f32_16x16x32_bf16 v[152:155], v[164:167], v[220:223], v[152:155]\n"
        "global_load_lds_dwordx4 v245, s[86:87] offset:3072\n"
        "ds_read_b128 v[220:223], v249 offset:4096\n"
        "s_add_u32 m0, s72, 0x1000\n"
        "v_mfma_f32_16x16x32_bf16 v[152:155], v[160:163], v[204:207], v[152:155]\n"
        "ds_read_b128 v[204:207], v248 offset:4096\n"
        "s_add_u32 s86, s66, 0x600\n"
        "s_addc_u32 s87, s67, 0\n"
        "v_mfma_f32_16x16x32_bf16 v[0:3], v[32:35], v[192:195], v[0:3]\n"
        "ds_read_b64_tr_b16 v[160:161], v226 offset:50176\n"
        "s_waitcnt lgkmcnt(5)\n"
        "v_mfma_f32_16x16x32_bf16 v[0:3], v[36:39], v[208:211], v[0:3]\n"
        "ds_read_b64_tr_b16 v[162:163], v227 offset:50176\n"
        "v_accvgpr_read_b32 v178, a82\n"
        "v_mfma_f32_16x16x32_bf16 v[4:7], v[44:47], v[208:211], v[4:7]\n"
        "ds_read_b64_tr_b16 v[164:165], v226 offset:58368\n"
        "global_load_lds_dwordx4 v242, s[86:87] offset:0\n"
        "v_mfma_f32_16x16x32_bf16 v[4:7], v[40:43], v[192:195], v[4:7]\n"
        "ds_read_b64_tr_b16 v[166:167], v227 offset:58368\n"
        "v_accvgpr_read_b32 v179, a83\n"
        "v_mfma_f32_16x16x32_bf16 v[68:71], v[40:43], v[196:199], v[68:71]\n"
        "ds_read_b64_tr_b16 v[172:173], v228 offset:58368\n"
        "v_accvgpr_read_b32 v180, a112\n"
        "v_mfma_f32_16x16x32_bf16 v[68:71], v[44:47], v[212:215], v[68:71]\n"
        "ds_read_b64_tr_b16 v[174:175], v229 offset:58368\n"
        "v_accvgpr_read_b32 v181, a113\n"
        "v_mfma_f32_16x16x32_bf16 v[64:67], v[36:39], v[212:215], v[64:67]\n"
        "ds_read_b64_tr_b16 v[168:169], v228 offset:50176\n"
        "global_load_lds_dwordx4 v243, s[86:87] offset:1024\n"
        "v_mfma_f32_16x16x32_bf16 v[64:67], v[32:35], v[196:199], v[64:67]\n"
        "ds_read_b64_tr_b16 v[170:171], v229 offset:50176\n"
        "s_waitcnt lgkmcnt(8)\n"
        "v_mfma_f32_16x16x32_bf16 v[96:99], v[32:35], v[200:203], v[96:99]\n"
        "v_accvgpr_read_b32 v182, a114\n"
        "v_accvgpr_read_b32 v183, a115\n"
        "v_mfma_f32_16x16x32_bf16 v[96:99], v[36:39], v[216:219], v[96:99]\n"
        "v_mfma_f32_16x16x32_bf16 v[100:103], v[44:47], v[216:219], v[100:103]\n"
        "global_load_lds_dwordx4 v244, s[86:87] offset:2048\n"
        "v_mfma_f32_16x16x32_bf16 v[100:103], v[40:43], v[200:203], v[100:103]\n"
        "v_mfma_f32_16x16x32_bf16 v[132:135], v[40:43], v[204:207], v[132:135]\n"
        "ds_read_b64_tr_b16 v[40:41], v232 offset:50176\n"
        "v_mfma_f32_16x16x32_bf16 v[132:135], v[44:47], v[220:223], v[132:135]\n"
        "ds_read_b64_tr_b16 v[44:45], v232 offset:58368\n"
        "v_mfma_f32_16x16x32_bf16 v[128:131], v[36:39], v[220:223], v[128:131]\n"
        "global_load_lds_dwordx4 v245, s[86:87] offset:3072\n"
        "s_add_u32 m0, s73, 0x24000\n"
        "v_mfma_f32_16x16x32_bf16 v[128:131], v[32:35], v[204:207], v[128:131]\n"
        "ds_read_b64_tr_b16 v[32:33], v230 offset:50176\n"
        "v_accvgpr_read_b32 v184, a28\n"
        "v_mfma_f32_16x16x32_bf16 v[8:11], v[48:51], v[192:195], v[8:11]\n"
        "ds_read_b64_tr_b16 v[34:35], v231 offset:50176\n"
        "v_cvt_pk_bf16_f32 v184, v184, v185\n"
        "v_mfma_f32_16x16x32_bf16 v[8:11], v[52:55], v[208:211], v[8:11]\n"
        "ds_read_b64_tr_b16 v[36:37], v230 offset:58368\n"
        "v_cvt_pk_bf16_f32 v185, v186, v187\n"
        "v_mfma_f32_16x16x32_bf16 v[12:15], v[60:63], v[208:211], v[12:15]\n"
        "ds_read_b64_tr_b16 v[38:39], v231 offset:58368\n"
        "v_cvt_pk_bf16_f32 v186, v188, v189\n"
        "v_mfma_f32_16x16x32_bf16 v[12:15], v[56:59], v[192:195], v[12:15]\n"
        "ds_read_b64_tr_b16 v[46:47], v233 offset:58368\n"
        "v_cvt_pk_bf16_f32 v187, v190, v191\n"
        "v_mfma_f32_16x16x32_bf16 v[76:79], v[56:59], v[196:199], v[76:79]\n"
        "ds_read_b64_tr_b16 v[42:43], v233 offset:50176\n"
        "v_accvgpr_write_b32 a188, v184\n"
        "v_mfma_f32_16x16x32_bf16 v[76:79], v[60:63], v[212:215], v[76:79]\n"
        "s_add_u32 s86, s68, 0x680000\n"
        "s_addc_u32 s87, s69, 0\n"
        "v_accvgpr_write_b32 a189, v185\n"
        "v_mfma_f32_16x16x32_bf16 v[72:75], v[52:55], v[212:215], v[72:75]\n"
        "v_accvgpr_write_b32 a190, v186\n"
        "v_accvgpr_write_b32 a191, v187\n"
        "v_mfma_f32_16x16x32_bf16 v[72:75], v[48:51], v[196:199], v[72:75]\n"
        "v_accvgpr_read_b32 v185, a85\n"
        "v_accvgpr_read_b32 v186, a86\n"
        "v_mfma_f32_16x16x32_bf16 v[104:107], v[48:51], v[200:203], v[104:107]\n"
        "v_accvgpr_read_b32 v187, a87\n"
        "v_accvgpr_read_b32 v188, a116\n"
        "v_mfma_f32_16x16x32_bf16 v[104:107], v[52:55], v[216:219], v[104:107]\n"
        "v_accvgpr_read_b32 v189, a117\n"
        "v_accvgpr_read_b32 v190, a118\n"
        "v_mfma_f32_16x16x32_bf16 v[108:111], v[60:63], v[216:219], v[108:111]\n"
        "v_accvgpr_read_b32 v191, a119\n"
        "v_mfma_f32_16x16x32_bf16 v[108:111], v[56:59], v[200:203], v[108:111]\n"
        "v_mfma_f32_16x16x32_bf16 v[140:143], v[56:59], v[204:207], v[140:143]\n"
        "s_waitcnt vmcnt(13) lgkmcnt(0)\n"
        "v_mfma_f32_16x16x32_bf16 v[140:143], v[60:63], v[220:223], v[140:143]\n"
        "s_barrier\n"
        "v_mfma_f32_16x16x32_bf16 v[136:139], v[52:55], v[220:223], v[136:139]\n"
        "global_load_lds_dwordx4 v238, s[86:87] offset:0\n"
        "ds_read_b64_tr_b16 v[52:53], v226 offset:24576\n"
        "v_mfma_f32_16x16x32_bf16 v[136:139], v[48:51], v[204:207], v[136:139]\n"
        "ds_read_b64_tr_b16 v[48:49], v226 offset:16384\n"
        "v_mfma_f32_16x16x32_bf16 v[16:19], v[160:163], v[192:195], v[16:19]\n"
        "ds_read_b64_tr_b16 v[50:51], v227 offset:16384\n"
        "v_mfma_f32_16x16x32_bf16 v[16:19], v[164:167], v[208:211], v[16:19]\n"
        "ds_read_b64_tr_b16 v[54:55], v227 offset:24576\n"
        "v_mfma_f32_16x16x32_bf16 v[20:23], v[172:175], v[208:211], v[20:23]\n"
        "global_store_dwordx4 v254, a[164:167], s[70:71] offset:1024 sc1\n"
        "ds_read_b64_tr_b16 v[60:61], v228 offset:24576\n"
        "ds_read_b64_tr_b16 v[62:63], v229 offset:24576\n"
        "s_add_u32 s70, s38, s97\n"
        "s_addc_u32 s71, s39, 0\n"
        "v_mfma_f32_16x16x32_bf16 v[20:23], v[168:171], v[192:195], v[20:23]\n"
        "ds_read_b64_tr_b16 v[56:57], v228 offset:16384\n"
        "s_add_u32 s97, s52, 0x3000\n"
        "s_add_u32 s97, s97, s75\n"
        "v_mfma_f32_16x16x32_bf16 v[84:87], v[168:171], v[196:199], v[84:87]\n"
        "ds_read_b64_tr_b16 v[58:59], v229 offset:16384\n"
        "v_mfma_f32_16x16x32_bf16 v[84:87], v[172:175], v[212:215], v[84:87]\n"
        "global_load_lds_dwordx4 v239, s[86:87] offset:1024\n"
        "v_mfma_f32_16x16x32_bf16 v[80:83], v[164:167], v[212:215], v[80:83]\n"
        "v_mfma_f32_16x16x32_bf16 v[80:83], v[160:163], v[196:199], v[80:83]\n"
        "v_accvgpr_read_b32 v176, a80\n"
        "v_cvt_pk_bf16_f32 v176, v176, v177\n"
        "v_mfma_f32_16x16x32_bf16 v[112:115], v[160:163], v[200:203], v[112:115]\n"
        "v_cvt_pk_bf16_f32 v177, v178, v179\n"
        "v_cvt_pk_bf16_f32 v178, v180, v181\n"
        "v_mfma_f32_16x16x32_bf16 v[112:115], v[164:167], v[216:219], v[112:115]\n"
        "global_load_lds_dwordx4 v240, s[86:87] offset:2048\n"
        "v_mfma_f32_16x16x32_bf16 v[116:119], v[172:175], v[216:219], v[116:119]\n"
        "v_cvt_pk_bf16_f32 v179, v182, v183\n"
        "v_accvgpr_write_b32 a16, v176\n"
        "v_mfma_f32_16x16x32_bf16 v[116:119], v[168:171], v[200:203], v[116:119]\n"
        "v_accvgpr_write_b32 a17, v177\n"
        "v_accvgpr_write_b32 a18, v178\n"
        "v_mfma_f32_16x16x32_bf16 v[148:151], v[168:171], v[204:207], v[148:151]\n"
        "ds_read_b64_tr_b16 v[168:169], v232 offset:16384\n"
        "v_accvgpr_write_b32 a19, v179\n"
        "v_mfma_f32_16x16x32_bf16 v[148:151], v[172:175], v[220:223], v[148:151]\n"
        "global_load_lds_dwordx4 v241, s[86:87] offset:3072\n"
        "s_add_u32 m0, s72, 0x10000\n"
        "v_mfma_f32_16x16x32_bf16 v[144:147], v[164:167], v[220:223], v[144:147]\n"
        "ds_read_b64_tr_b16 v[164:165], v230 offset:24576\n"
        "s_add_u32 s86, s64, 0x680\n"
        "s_addc_u32 s87, s65, 0\n"
        "v_mfma_f32_16x16x32_bf16 v[144:147], v[160:163], v[204:207], v[144:147]\n"
        "ds_read_b64_tr_b16 v[160:161], v230 offset:16384\n"
        "v_accvgpr_read_b32 v177, a89\n"
        "v_mfma_f32_16x16x32_bf16 v[24:27], v[32:35], v[192:195], v[24:27]\n"
        "ds_read_b64_tr_b16 v[162:163], v231 offset:16384\n"
        "v_accvgpr_read_b32 v178, a90\n"
        "v_mfma_f32_16x16x32_bf16 v[24:27], v[36:39], v[208:211], v[24:27]\n"
        "ds_read_b64_tr_b16 v[166:167], v231 offset:24576\n"
        "global_load_lds_dwordx4 v242, s[86:87] offset:0\n"
        "v_mfma_f32_16x16x32_bf16 v[28:31], v[44:47], v[208:211], v[28:31]\n"
        "ds_read_b64_tr_b16 v[172:173], v232 offset:24576\n"
        "v_accvgpr_read_b32 v179, a91\n"
        "v_mfma_f32_16x16x32_bf16 v[28:31], v[40:43], v[192:195], v[28:31]\n"
        "ds_read_b64_tr_b16 v[174:175], v233 offset:24576\n"
        "v_accvgpr_read_b32 v180, a120\n"
        "v_mfma_f32_16x16x32_bf16 v[92:95], v[40:43], v[196:199], v[92:95]\n"
        "ds_read_b64_tr_b16 v[170:171], v233 offset:16384\n"
        "v_accvgpr_read_b32 v181, a121\n"
        "v_mfma_f32_16x16x32_bf16 v[92:95], v[44:47], v[212:215], v[92:95]\n"
        "ds_read_b128 v[192:195], v246 offset:34816\n"
        "global_load_lds_dwordx4 v243, s[86:87] offset:1024\n"
        "v_mfma_f32_16x16x32_bf16 v[88:91], v[36:39], v[212:215], v[88:91]\n"
        "ds_read_b128 v[208:211], v247 offset:34816\n"
        "v_accvgpr_read_b32 v182, a122\n"
        "v_mfma_f32_16x16x32_bf16 v[88:91], v[32:35], v[196:199], v[88:91]\n"
        "ds_read_b128 v[196:199], v246 offset:32768\n"
        "v_accvgpr_read_b32 v183, a123\n"
        "v_mfma_f32_16x16x32_bf16 v[120:123], v[32:35], v[200:203], v[120:123]\n"
        "ds_read_b128 v[212:215], v247 offset:32768\n"
        "v_mfma_f32_16x16x32_bf16 v[120:123], v[36:39], v[216:219], v[120:123]\n"
        "global_load_lds_dwordx4 v244, s[86:87] offset:2048\n"
        "v_mfma_f32_16x16x32_bf16 v[124:127], v[44:47], v[216:219], v[124:127]\n"
        "ds_read_b128 v[216:219], v247 offset:38912\n"
        "v_mfma_f32_16x16x32_bf16 v[124:127], v[40:43], v[200:203], v[124:127]\n"
        "ds_read_b128 v[200:203], v246 offset:38912\n"
        "v_mfma_f32_16x16x32_bf16 v[156:159], v[40:43], v[204:207], v[156:159]\n"
        "ds_read_b64_tr_b16 v[40:41], v228 offset:17408\n"
        "v_mfma_f32_16x16x32_bf16 v[156:159], v[44:47], v[220:223], v[156:159]\n"
        "global_load_lds_dwordx4 v245, s[86:87] offset:3072\n"
        "s_add_u32 m0, s72, 0x11000\n"
        "v_mfma_f32_16x16x32_bf16 v[152:155], v[36:39], v[220:223], v[152:155]\n"
        "ds_read_b128 v[220:223], v247 offset:36864\n"
        "s_add_u32 s86, s66, 0x680\n"
        "s_addc_u32 s87, s67, 0\n"
        "v_mfma_f32_16x16x32_bf16 v[152:155], v[32:35], v[204:207], v[152:155]\n"
        "ds_read_b128 v[204:207], v246 offset:36864\n"
        "s_waitcnt lgkmcnt(6)\n"
        "v_mfma_f32_16x16x32_bf16 v[0:3], v[48:51], v[192:195], v[0:3]\n"
        "ds_read_b64_tr_b16 v[32:33], v226 offset:17408\n"
        "v_accvgpr_read_b32 v184, a84\n"
        "v_mfma_f32_16x16x32_bf16 v[0:3], v[52:55], v[208:211], v[0:3]\n"
        "s_waitcnt lgkmcnt(6)\n"
        "v_cvt_pk_bf16_f32 v184, v184, v185\n"
        "v_cvt_pk_bf16_f32 v185, v186, v187\n"
        "v_mfma_f32_16x16x32_bf16 v[4:7], v[60:63], v[208:211], v[4:7]\n"
        "global_load_lds_dwordx4 v242, s[86:87] offset:0\n"
        "v_cvt_pk_bf16_f32 v186, v188, v189\n"
        "v_mfma_f32_16x16x32_bf16 v[4:7], v[56:59], v[192:195], v[4:7]\n"
        "ds_read_b64_tr_b16 v[34:35], v227 offset:17408\n"
        "v_cvt_pk_bf16_f32 v187, v190, v191\n"
        "v_mfma_f32_16x16x32_bf16 v[68:71], v[56:59], v[196:199], v[68:71]\n"
        "s_waitcnt lgkmcnt(4)\n"
        "v_accvgpr_write_b32 a20, v184\n"
        "v_accvgpr_write_b32 a21, v185\n"
        "v_mfma_f32_16x16x32_bf16 v[68:71], v[60:63], v[212:215], v[68:71]\n"
        "ds_read_b64_tr_b16 v[36:37], v226 offset:25600\n"
        "v_accvgpr_write_b32 a22, v186\n"
        "v_mfma_f32_16x16x32_bf16 v[64:67], v[52:55], v[212:215], v[64:67]\n"
        "ds_read_b64_tr_b16 v[38:39], v227 offset:25600\n"
        "v_accvgpr_write_b32 a23, v187\n"
        "v_mfma_f32_16x16x32_bf16 v[64:67], v[48:51], v[196:199], v[64:67]\n"
        "ds_read_b64_tr_b16 v[44:45], v228 offset:25600\n"
        "global_load_lds_dwordx4 v243, s[86:87] offset:1024\n"
        "v_mfma_f32_16x16x32_bf16 v[96:99], v[48:51], v[200:203], v[96:99]\n"
        "ds_read_b64_tr_b16 v[46:47], v229 offset:25600\n"
        "v_accvgpr_read_b32 v185, a93\n"
        "v_mfma_f32_16x16x32_bf16 v[96:99], v[52:55], v[216:219], v[96:99]\n"
        "ds_read_b64_tr_b16 v[42:43], v229 offset:17408\n"
        "v_accvgpr_read_b32 v186, a94\n"
        "v_mfma_f32_16x16x32_bf16 v[100:103], v[60:63], v[216:219], v[100:103]\n"
        "s_waitcnt lgkmcnt(6)\n"
        "v_accvgpr_read_b32 v187, a95\n"
        "v_accvgpr_read_b32 v188, a124\n"
        "v_mfma_f32_16x16x32_bf16 v[100:103], v[56:59], v[200:203], v[100:103]\n"
        "global_load_lds_dwordx4 v244, s[86:87] offset:2048\n"
        "v_mfma_f32_16x16x32_bf16 v[132:135], v[56:59], v[204:207], v[132:135]\n"
        "ds_read_b64_tr_b16 v[56:57], v232 offset:17408\n"
        "v_accvgpr_read_b32 v189, a125\n"
        "v_mfma_f32_16x16x32_bf16 v[132:135], v[60:63], v[220:223], v[132:135]\n"
        "ds_read_b64_tr_b16 v[60:61], v232 offset:25600\n"
        "v_accvgpr_read_b32 v190, a126\n"
        "v_mfma_f32_16x16x32_bf16 v[128:131], v[52:55], v[220:223], v[128:131]\n"
        "ds_read_b64_tr_b16 v[52:53], v230 offset:25600\n"
        "v_accvgpr_read_b32 v191, a127\n"
        "v_mfma_f32_16x16x32_bf16 v[128:131], v[48:51], v[204:207], v[128:131]\n"
        "global_load_lds_dwordx4 v245, s[86:87] offset:3072\n"
        "s_add_u32 m0, s73, 0x1c000\n"
        "v_mfma_f32_16x16x32_bf16 v[8:11], v[160:163], v[192:195], v[8:11]\n"
        "ds_read_b64_tr_b16 v[48:49], v230 offset:17408\n"
        "s_add_u32 s86, s68, 0x700000\n"
        "s_addc_u32 s87, s69, 0\n"
        "v_mfma_f32_16x16x32_bf16 v[8:11], v[164:167], v[208:211], v[8:11]\n"
        "ds_read_b64_tr_b16 v[50:51], v231 offset:17408\n"
        "v_accvgpr_write_b32 a93, v252\n"
        "v_mfma_f32_16x16x32_bf16 v[12:15], v[172:175], v[208:211], v[12:15]\n"
        "ds_read_b64_tr_b16 v[54:55], v231 offset:25600\n"
        "v_mfma_f32_16x16x32_bf16 v[12:15], v[168:171], v[192:195], v[12:15]\n"
        "ds_read_b64_tr_b16 v[62:63], v233 offset:25600\n"
        "v_mfma_f32_16x16x32_bf16 v[76:79], v[168:171], v[196:199], v[76:79]\n"
        "ds_read_b64_tr_b16 v[58:59], v233 offset:17408\n"
        "v_mfma_f32_16x16x32_bf16 v[76:79], v[172:175], v[212:215], v[76:79]\n"
        "v_mfma_f32_16x16x32_bf16 v[72:75], v[164:167], v[212:215], v[72:75]\n"
        "v_mfma_f32_16x16x32_bf16 v[72:75], v[160:163], v[196:199], v[72:75]\n"
        "v_accvgpr_read_b32 v176, a88\n"
        "v_cvt_pk_bf16_f32 v176, v176, v177\n"
        "v_mfma_f32_16x16x32_bf16 v[104:107], v[160:163], v[200:203], v[104:107]\n"
        "s_mul_i32 s90, s43, 529408\n"
        "v_cvt_pk_bf16_f32 v177, v178, v179\n"
        "v_cvt_pk_bf16_f32 v178, v180, v181\n"
        "v_mfma_f32_16x16x32_bf16 v[104:107], v[164:167], v[216:219], v[104:107]\n"
        "s_add_u32 s90, s90, s72\n"
        "v_cvt_pk_bf16_f32 v179, v182, v183\n"
        "v_accvgpr_write_b32 a24, v176\n"
        "v_mfma_f32_16x16x32_bf16 v[108:111], v[172:175], v[216:219], v[108:111]\n"
        "s_and_b32 s78, s45, 1\n"
        "v_accvgpr_write_b32 a25, v177\n"
        "v_accvgpr_write_b32 a26, v178\n"
        "v_mfma_f32_16x16x32_bf16 v[108:111], v[168:171], v[200:203], v[108:111]\n"
        "s_lshl_b32 s78, s78, 15\n"
        "v_accvgpr_write_b32 a27, v179\n"
        "s_add_u32 s78, s78, 0x10000\n"
        "v_mfma_f32_16x16x32_bf16 v[140:143], v[168:171], v[204:207], v[140:143]\n"
        "s_waitcnt vmcnt(13) lgkmcnt(0)\n"
        "s_and_b32 s80, s40, 1\n"
        "s_lshl_b32 s80, s80, 11\n"
        "v_mfma_f32_16x16x32_bf16 v[140:143], v[172:175], v[220:223], v[140:143]\n"
        "s_barrier\n"
        "v_mfma_f32_16x16x32_bf16 v[136:139], v[164:167], v[220:223], v[136:139]\n"
        "global_load_lds_dwordx4 v238, s[86:87] offset:0\n"
        "ds_read_b64_tr_b16 v[164:165], v226 offset:40960\n"
        "s_and_b32 s79, s45, 1\n"
        "v_mfma_f32_16x16x32_bf16 v[136:139], v[160:163], v[204:207], v[136:139]\n"
        "ds_read_b64_tr_b16 v[160:161], v226 offset:32768\n"
        "s_lshl_b32 s79, s79, 15\n"
        "s_lshl_b32 s82, s40, 4\n"
        "v_mfma_f32_16x16x32_bf16 v[16:19], v[32:35], v[192:195], v[16:19]\n"
        "ds_read_b64_tr_b16 v[162:163], v227 offset:32768\n"
        "s_sub_u32 s83, 14, s44\n"
        "v_accvgpr_write_b32 a64, 0\n"
        "v_mfma_f32_16x16x32_bf16 v[16:19], v[36:39], v[208:211], v[16:19]\n"
        "ds_read_b64_tr_b16 v[166:167], v227 offset:40960\n"
        "s_lshl_b32 s50, s41, 7\n"
        "v_accvgpr_write_b32 a65, 0\n"
        "v_mfma_f32_16x16x32_bf16 v[20:23], v[44:47], v[208:211], v[20:23]\n"
        "ds_read_b64_tr_b16 v[172:173], v228 offset:40960\n"
        "global_load_lds_dwordx4 v239, s[86:87] offset:1024\n"
        "s_lshl2_add_u32 s50, s42, s50\n"
        "v_mfma_f32_16x16x32_bf16 v[20:23], v[40:43], v[192:195], v[20:23]\n"
        "ds_read_b64_tr_b16 v[174:175], v229 offset:40960\n"
        "s_add_u32 s50, s50, s40\n"
        "v_accvgpr_write_b32 a66, 0\n"
        "v_mfma_f32_16x16x32_bf16 v[84:87], v[40:43], v[196:199], v[84:87]\n"
        "ds_read_b64_tr_b16 v[168:169], v228 offset:32768\n"
        "s_lshl_b32 s50, s50, 12\n"
        "v_accvgpr_write_b32 a67, 0\n"
        "v_mfma_f32_16x16x32_bf16 v[84:87], v[44:47], v[212:215], v[84:87]\n"
        "ds_read_b64_tr_b16 v[170:171], v229 offset:32768\n"
        "s_mul_i32 s51, s40, 8320\n"
        "v_accvgpr_write_b32 a68, 0\n"
        "v_mfma_f32_16x16x32_bf16 v[80:83], v[36:39], v[212:215], v[80:83]\n"
        "global_load_lds_dwordx4 v240, s[86:87] offset:2048\n"
        "s_add_u32 s51, s51, 0x18000\n"
        "v_mfma_f32_16x16x32_bf16 v[80:83], v[32:35], v[196:199], v[80:83]\n"
        "s_add_u32 s92, s26, s50\n"
        "s_addc_u32 s93, s27, 0\n"
        "v_accvgpr_write_b32 a69, 0\n"
        "v_mfma_f32_16x16x32_bf16 v[112:115], v[32:35], v[200:203], v[112:115]\n"
        "s_add_u32 s50, s50, 0x3300000\n"
        "v_accvgpr_write_b32 a70, 0\n"
        "v_accvgpr_write_b32 a71, 0\n"
        "v_mfma_f32_16x16x32_bf16 v[112:115], v[36:39], v[216:219], v[112:115]\n"
        "s_add_u32 s94, s38, s50\n"
        "s_addc_u32 s95, s39, 0\n"
        "s_add_u32 s50, s51, 4128\n"
        "v_mfma_f32_16x16x32_bf16 v[116:119], v[44:47], v[216:219], v[116:119]\n"
        "global_load_lds_dwordx4 v241, s[86:87] offset:3072\n"
        "s_add_u32 m0, s72, 0x8000\n"
        "v_mfma_f32_16x16x32_bf16 v[116:119], v[40:43], v[200:203], v[116:119]\n"
        "s_add_u32 s86, s64, 0x700\n"
        "s_addc_u32 s87, s65, 0\n"
        "v_bfe_u32 v176, v254, 4, 4\n"
        "v_mfma_f32_16x16x32_bf16 v[148:151], v[40:43], v[204:207], v[148:151]\n"
        "ds_read_b64_tr_b16 v[40:41], v232 offset:32768\n"
        "v_lshrrev_b32 v177, 8, v254\n"
        "v_mfma_f32_16x16x32_bf16 v[148:151], v[44:47], v[220:223], v[148:151]\n"
        "ds_read_b64_tr_b16 v[44:45], v232 offset:40960\n"
        "v_lshrrev_b32 v178, 7, v254\n"
        "v_mfma_f32_16x16x32_bf16 v[144:147], v[36:39], v[220:223], v[144:147]\n"
        "global_load_lds_dwordx4 v242, s[86:87] offset:0\n"
        "v_mfma_f32_16x16x32_bf16 v[144:147], v[32:35], v[204:207], v[144:147]\n"
        "ds_read_b64_tr_b16 v[32:33], v230 offset:32768\n"
        "v_accvgpr_read_b32 v184, a92\n"
        "v_mfma_f32_16x16x32_bf16 v[24:27], v[48:51], v[192:195], v[24:27]\n"
        "ds_read_b64_tr_b16 v[34:35], v231 offset:32768\n"
        "v_cvt_pk_bf16_f32 v184, v184, v185\n"
        "v_mfma_f32_16x16x32_bf16 v[24:27], v[52:55], v[208:211], v[24:27]\n"
        "ds_read_b64_tr_b16 v[36:37], v230 offset:40960\n"
        "v_cvt_pk_bf16_f32 v185, v186, v187\n"
        "v_mfma_f32_16x16x32_bf16 v[28:31], v[60:63], v[208:211], v[28:31]\n"
        "ds_read_b64_tr_b16 v[38:39], v231 offset:40960\n"
        "v_cvt_pk_bf16_f32 v186, v188, v189\n"
        "v_mfma_f32_16x16x32_bf16 v[28:31], v[56:59], v[192:195], v[28:31]\n"
        "global_store_dwordx4 v254, a[168:171], s[70:71] offset:0 sc1\n"
        "ds_read_b64_tr_b16 v[46:47], v233 offset:40960\n"
        "LHA8_%=:\n"
        "s_cbranch_execz LHRA_%=\n"
        "ds_read_b64_tr_b16 v[42:43], v233 offset:32768\n"
        "v_cvt_pk_bf16_f32 v187, v190, v191\n"
        "v_mfma_f32_16x16x32_bf16 v[92:95], v[56:59], v[196:199], v[92:95]\n"
        "ds_read_b128 v[192:195], v246 offset:2048\n"
        "v_accvgpr_write_b32 a28, v184\n"
        "v_mfma_f32_16x16x32_bf16 v[92:95], v[60:63], v[212:215], v[92:95]\n"
        "ds_read_b128 v[208:211], v247 offset:2048\n"
        "v_accvgpr_write_b32 a29, v185\n"
        "v_mfma_f32_16x16x32_bf16 v[88:91], v[52:55], v[212:215], v[88:91]\n"
        "ds_read_b128 v[212:215], v247 offset:0\n"
        "v_accvgpr_write_b32 a30, v186\n"
        "v_mfma_f32_16x16x32_bf16 v[88:91], v[48:51], v[196:199], v[88:91]\n"
        "global_load_lds_dwordx4 v243, s[86:87] offset:1024\n"
        "v_accvgpr_write_b32 a31, v187\n"
        "v_mfma_f32_16x16x32_bf16 v[120:123], v[48:51], v[200:203], v[120:123]\n"
        "ds_read_b128 v[196:199], v246 offset:0\n"
        "v_lshrrev_b32 v186, 5, v254\n"
        "v_mfma_f32_16x16x32_bf16 v[120:123], v[52:55], v[216:219], v[120:123]\n"
        "v_lshrrev_b32 v187, 9, v254\n"
        "v_xor_b32 v186, v186, v187\n"
        "v_mfma_f32_16x16x32_bf16 v[124:127], v[60:63], v[216:219], v[124:127]\n"
        "ds_read_b128 v[216:219], v247 offset:6144\n"
        "v_and_b32 v186, 1, v186\n"
        "v_mfma_f32_16x16x32_bf16 v[124:127], v[56:59], v[200:203], v[124:127]\n"
        "global_load_lds_dwordx4 v244, s[86:87] offset:2048\n"
        "ds_read_b128 v[200:203], v246 offset:6144\n"
        "v_mfma_f32_16x16x32_bf16 v[156:159], v[56:59], v[204:207], v[156:159]\n"
        "ds_read_b64_tr_b16 v[56:57], v228 offset:33792\n"
        "v_cmp_eq_u32 vcc, 0, v186\n"
        "v_mfma_f32_16x16x32_bf16 v[156:159], v[60:63], v[220:223], v[156:159]\n"
        "ds_read_b64_tr_b16 v[60:61], v228 offset:41984\n"
        "v_and_b32 v187, 0x30, v254\n"
        "v_mfma_f32_16x16x32_bf16 v[152:155], v[52:55], v[220:223], v[152:155]\n"
        "ds_read_b128 v[220:223], v247 offset:4096\n"
        "v_bfe_u32 v179, v254, 4, 3\n"
        "v_mfma_f32_16x16x32_bf16 v[152:155], v[48:51], v[204:207], v[152:155]\n"
        "global_load_lds_dwordx4 v245, s[86:87] offset:3072\n"
        "s_add_u32 m0, s72, 0x9000\n"
        "s_waitcnt lgkmcnt(6)\n"
        "v_mfma_f32_16x16x32_bf16 v[0:3], v[160:163], v[192:195], v[0:3]\n"
        "ds_read_b128 v[204:207], v246 offset:4096\n"
        "s_add_u32 s86, s66, 0x700\n"
        "s_addc_u32 s87, s67, 0\n"
        "v_mfma_f32_16x16x32_bf16 v[0:3], v[164:167], v[208:211], v[0:3]\n"
        "ds_read_b64_tr_b16 v[48:49], v226 offset:33792\n"
        "v_and_b32 v180, 6, v178\n"
        "v_mfma_f32_16x16x32_bf16 v[4:7], v[172:175], v[208:211], v[4:7]\n"
        "ds_read_b64_tr_b16 v[50:51], v227 offset:33792\n"
        "v_xor_b32 v180, v180, v179\n"
        "v_mfma_f32_16x16x32_bf16 v[4:7], v[168:171], v[192:195], v[4:7]\n"
        "ds_read_b64_tr_b16 v[52:53], v226 offset:41984\n"
        "s_waitcnt lgkmcnt(7)\n"
        "v_mfma_f32_16x16x32_bf16 v[68:71], v[168:171], v[196:199], v[68:71]\n"
        "ds_read_b64_tr_b16 v[54:55], v227 offset:41984\n"
        "global_load_lds_dwordx4 v242, s[86:87] offset:0\n"
        "v_mfma_f32_16x16x32_bf16 v[68:71], v[172:175], v[212:215], v[68:71]\n"
        "ds_read_b64_tr_b16 v[62:63], v229 offset:41984\n"
        "v_lshlrev_b32 v180, 4, v180\n"
        "v_mfma_f32_16x16x32_bf16 v[64:67], v[164:167], v[212:215], v[64:67]\n"
        "ds_read_b64_tr_b16 v[58:59], v229 offset:33792\n"
        "v_lshl_add_u32 v181, v178, 11, v180\n"
        "v_mfma_f32_16x16x32_bf16 v[64:67], v[160:163], v[196:199], v[64:67]\n"
        "v_xor_b32 v178, 4, v178\n"
        "v_lshl_add_u32 v178, v178, 11, v180\n"
        "v_mfma_f32_16x16x32_bf16 v[96:99], v[160:163], v[200:203], v[96:99]\n"
        "global_load_lds_dwordx4 v243, s[86:87] offset:1024\n"
        "v_mfma_f32_16x16x32_bf16 v[96:99], v[164:167], v[216:219], v[96:99]\n"
        "s_waitcnt lgkmcnt(5)\n"
        "v_add_u32 v182, 4096, v181\n"
        "v_accvgpr_write_b32 a84, v182\n"
        "v_mfma_f32_16x16x32_bf16 v[100:103], v[172:175], v[216:219], v[100:103]\n"
        "v_add_u32 v182, 3200, v181\n"
        "v_accvgpr_write_b32 a85, v182\n"
        "v_mfma_f32_16x16x32_bf16 v[100:103], v[168:171], v[200:203], v[100:103]\n"
        "v_add_u32 v182, 18432, v178\n"
        "v_accvgpr_write_b32 a86, v182\n"
        "v_mfma_f32_16x16x32_bf16 v[132:135], v[168:171], v[204:207], v[132:135]\n"
        "global_load_lds_dwordx4 v244, s[86:87] offset:2048\n"
        "v_mfma_f32_16x16x32_bf16 v[132:135], v[172:175], v[220:223], v[132:135]\n"
        "ds_read_b64_tr_b16 v[172:173], v232 offset:41984\n"
        "v_add_u32 v182, 17536, v178\n"
        "v_mfma_f32_16x16x32_bf16 v[128:131], v[164:167], v[220:223], v[128:131]\n"
        "ds_read_b64_tr_b16 v[164:165], v230 offset:41984\n"
        "v_accvgpr_write_b32 a87, v182\n"
        "v_mfma_f32_16x16x32_bf16 v[128:131], v[160:163], v[204:207], v[128:131]\n"
        "ds_read_b64_tr_b16 v[160:161], v230 offset:33792\n"
        "v_mov_b32 v178, s96\n"
        "v_mfma_f32_16x16x32_bf16 v[8:11], v[32:35], v[192:195], v[8:11]\n"
        "global_load_lds_dwordx4 v245, s[86:87] offset:3072\n"
        "s_add_u32 m0, s73, 0x20000\n"
        "v_mfma_f32_16x16x32_bf16 v[8:11], v[36:39], v[208:211], v[8:11]\n"
        "ds_read_b64_tr_b16 v[162:163], v231 offset:33792\n"
        "s_add_u32 s86, s68, 0x780000\n"
        "s_addc_u32 s87, s69, 0\n"
        "v_mfma_f32_16x16x32_bf16 v[12:15], v[44:47], v[208:211], v[12:15]\n"
        "ds_read_b64_tr_b16 v[166:167], v231 offset:41984\n"
        "v_add_u32 v180, s51, v254\n"
        "v_mfma_f32_16x16x32_bf16 v[12:15], v[40:43], v[192:195], v[12:15]\n"
        "ds_read_b64_tr_b16 v[174:175], v233 offset:41984\n"
        "v_add_u32 v180, 4096, v180\n"
        "v_mfma_f32_16x16x32_bf16 v[76:79], v[40:43], v[196:199], v[76:79]\n"
        "ds_read_b64_tr_b16 v[168:169], v232 offset:33792\n"
        "v_writelane_b32 v180, s96, 2\n"
        "v_mfma_f32_16x16x32_bf16 v[76:79], v[44:47], v[212:215], v[76:79]\n"
        "ds_read_b64_tr_b16 v[170:171], v233 offset:33792\n"
        "s_add_u32 s96, s96, 16\n"
        "v_writelane_b32 v180, s96, 3\n"
        "v_mfma_f32_16x16x32_bf16 v[72:75], v[36:39], v[212:215], v[72:75]\n"
        "s_add_u32 s96, s51, 4098\n"
        "v_accvgpr_write_b32 a90, v180\n"
        "v_sub_u32 v179, s96, v178\n"
        "v_mfma_f32_16x16x32_bf16 v[72:75], v[32:35], v[196:199], v[72:75]\n"
        "s_add_u32 s96, s51, 480\n"
        "v_and_b32 v181, 1, v176\n"
        "v_mul_lo_u32 v179, v179, v181\n"
        "v_mfma_f32_16x16x32_bf16 v[104:107], v[32:35], v[200:203], v[104:107]\n"
        "v_add_u32 v178, v178, v179\n"
        "v_lshl_add_u32 v178, v176, 1, v178\n"
        "v_mfma_f32_16x16x32_bf16 v[104:107], v[36:39], v[216:219], v[104:107]\n"
        "v_lshl_add_u32 v178, v177, 4, v178\n"
        "v_accvgpr_write_b32 a88, v178\n"
        "v_mfma_f32_16x16x32_bf16 v[108:111], v[44:47], v[216:219], v[108:111]\n"
        "v_lshlrev_b32 v179, 5, v176\n"
        "v_sub_u32 v179, s96, v179\n"
        "v_mfma_f32_16x16x32_bf16 v[108:111], v[40:43], v[200:203], v[108:111]\n"
        "s_lshl_b32 s96, s54, 16\n"
        "v_lshl_add_u32 v179, v177, 4, v179\n"
        "v_accvgpr_write_b32 a89, v179\n"
        "v_mfma_f32_16x16x32_bf16 v[140:143], v[40:43], v[204:207], v[140:143]\n"
        "s_waitcnt lgkmcnt(1)\n"
        "v_mov_b32 v181, 0\n"
        "v_writelane_b32 v181, s96, 3\n"
        "v_mfma_f32_16x16x32_bf16 v[140:143], v[44:47], v[220:223], v[140:143]\n"
        "s_waitcnt vmcnt(13) lgkmcnt(0)\n"
        "v_accvgpr_write_b32 a91, v181\n"
        "s_and_b32 s54, s54, 0xffff0000\n"
        "v_mfma_f32_16x16x32_bf16 v[136:139], v[36:39], v[220:223], v[136:139]\n"
        "s_barrier\n"
        "v_mfma_f32_16x16x32_bf16 v[136:139], v[32:35], v[204:207], v[136:139]\n"
        "global_load_lds_dwordx4 v238, s[86:87] offset:0\n"
        "ds_read_b64_tr_b16 v[32:33], v226 offset:49152\n"
        "s_lshr_b32 s96, s40, 1\n"
        "v_mfma_f32_16x16x32_bf16 v[16:19], v[48:51], v[192:195], v[16:19]\n"
        "ds_read_b64_tr_b16 v[34:35], v227 offset:49152\n"
        "s_lshl_b32 s96, s96, 10\n"
        "v_lshrrev_b32 v178, 1, v177\n"
        "v_mfma_f32_16x16x32_bf16 v[16:19], v[52:55], v[208:211], v[16:19]\n"
        "ds_read_b64_tr_b16 v[36:37], v226 offset:57344\n"
        "v_lshlrev_b32 v179, 11, v178\n"
        "v_mfma_f32_16x16x32_bf16 v[20:23], v[60:63], v[208:211], v[20:23]\n"
        "ds_read_b64_tr_b16 v[38:39], v227 offset:57344\n"
        "v_add_u32 v179, s96, v179\n"
        "v_mfma_f32_16x16x32_bf16 v[20:23], v[56:59], v[192:195], v[20:23]\n"
        "ds_read_b64_tr_b16 v[44:45], v228 offset:57344\n"
        "global_load_lds_dwordx4 v239, s[86:87] offset:1024\n"
        "s_and_b32 s96, s40, 1\n"
        "v_mfma_f32_16x16x32_bf16 v[84:87], v[56:59], v[196:199], v[84:87]\n"
        "ds_read_b64_tr_b16 v[46:47], v229 offset:57344\n"
        "s_lshl_b32 s96, s96, 6\n"
        "v_bfe_u32 v180, v254, 6, 3\n"
        "v_mfma_f32_16x16x32_bf16 v[84:87], v[60:63], v[212:215], v[84:87]\n"
        "ds_read_b64_tr_b16 v[40:41], v228 offset:49152\n"
        "v_bfe_u32 v181, v254, 4, 2\n"
        "v_mfma_f32_16x16x32_bf16 v[80:83], v[52:55], v[212:215], v[80:83]\n"
        "ds_read_b64_tr_b16 v[42:43], v229 offset:49152\n"
        "v_lshlrev_b32 v183, 2, v178\n"
        "v_mfma_f32_16x16x32_bf16 v[80:83], v[48:51], v[196:199], v[80:83]\n"
        "global_load_lds_dwordx4 v240, s[86:87] offset:2048\n"
        "v_mfma_f32_16x16x32_bf16 v[112:115], v[48:51], v[200:203], v[112:115]\n"
        "v_add_u32 v240, s78, v254\n"
        "v_xor_b32 v183, v183, v180\n"
        "v_mfma_f32_16x16x32_bf16 v[112:115], v[52:55], v[216:219], v[112:115]\n"
        "v_bfe_u32 v184, v183, 1, 2\n"
        "v_xor_b32 v184, 0, v184\n"
        "v_mfma_f32_16x16x32_bf16 v[116:119], v[60:63], v[216:219], v[116:119]\n"
        "v_lshlrev_b32 v184, 5, v184\n"
        "v_lshl_add_u32 v183, v183, 7, v184\n"
        "v_mfma_f32_16x16x32_bf16 v[116:119], v[56:59], v[200:203], v[116:119]\n"
        "global_load_lds_dwordx4 v241, s[86:87] offset:3072\n"
        "s_add_u32 m0, s72, 0x0\n"
        "v_mfma_f32_16x16x32_bf16 v[148:151], v[56:59], v[204:207], v[148:151]\n"
        "ds_read_b64_tr_b16 v[56:57], v232 offset:49152\n"
        "s_add_u32 s86, s64, 0x780\n"
        "s_addc_u32 s87, s65, 0\n"
        "v_mfma_f32_16x16x32_bf16 v[148:151], v[60:63], v[220:223], v[148:151]\n"
        "ds_read_b64_tr_b16 v[60:61], v232 offset:57344\n"
        "s_add_u32 s64, s38, s90\n"
        "s_addc_u32 s65, s39, 0\n"
        "v_mfma_f32_16x16x32_bf16 v[144:147], v[52:55], v[220:223], v[144:147]\n"
        "ds_read_b64_tr_b16 v[52:53], v230 offset:57344\n"
        "s_lshl_b32 s90, s40, 13\n"
        "s_and_b32 s90, s90, 0x4000\n"
        "v_mfma_f32_16x16x32_bf16 v[144:147], v[48:51], v[204:207], v[144:147]\n"
        "ds_read_b64_tr_b16 v[48:49], v230 offset:49152\n"
        "global_load_lds_dwordx4 v242, s[86:87] offset:0\n"
        "s_add_u32 s91, s78, s90\n"
        "v_mfma_f32_16x16x32_bf16 v[24:27], v[160:163], v[192:195], v[24:27]\n"
        "ds_read_b64_tr_b16 v[50:51], v231 offset:49152\n"
        "s_add_u32 s91, s91, s80\n"
        "s_xor_b32 s78, s78, 0x8000\n"
        "v_mfma_f32_16x16x32_bf16 v[24:27], v[164:167], v[208:211], v[24:27]\n"
        "ds_read_b64_tr_b16 v[54:55], v231 offset:57344\n"
        "s_add_u32 s78, s78, s72\n"
        "v_lshl_add_u32 v183, v181, 3, v183\n"
        "v_mfma_f32_16x16x32_bf16 v[28:31], v[172:175], v[208:211], v[28:31]\n"
        "ds_read_b64_tr_b16 v[62:63], v233 offset:57344\n"
        "v_add_u32 v182, v183, v179\n"
        "v_mfma_f32_16x16x32_bf16 v[28:31], v[168:171], v[192:195], v[28:31]\n"
        "ds_read_b64_tr_b16 v[58:59], v233 offset:49152\n"
        "global_load_lds_dwordx4 v243, s[86:87] offset:1024\n"
        "v_mfma_f32_16x16x32_bf16 v[92:95], v[168:171], v[196:199], v[92:95]\n"
        "ds_read_b128 v[192:195], v248 offset:2048\n"
        "v_xor_b32 v182, s96, v182\n"
        "v_mfma_f32_16x16x32_bf16 v[92:95], v[172:175], v[212:215], v[92:95]\n"
        "ds_read_b128 v[208:211], v249 offset:2048\n"
        "s_lshl_b32 s96, s41, 12\n"
        "v_accvgpr_write_b32 a92, v182\n"
        "v_mfma_f32_16x16x32_bf16 v[88:91], v[164:167], v[212:215], v[88:91]\n"
        "ds_read_b128 v[212:215], v249 offset:0\n"
        "s_add_u32 s96, s96, 32768\n"
        "v_bfe_u32 v188, v254, 4, 5\n"
        "v_mfma_f32_16x16x32_bf16 v[88:91], v[160:163], v[196:199], v[88:91]\n"
        "ds_read_b128 v[196:199], v248 offset:0\n"
        "global_load_lds_dwordx4 v244, s[86:87] offset:2048\n"
        "v_mfma_f32_16x16x32_bf16 v[120:123], v[160:163], v[200:203], v[120:123]\n"
        "v_lshl_add_u32 v188, v188, 7, s96\n"
        "v_accvgpr_write_b32 a94, v188\n"
        "v_mfma_f32_16x16x32_bf16 v[120:123], v[164:167], v[216:219], v[120:123]\n"
        "v_add_u32 v241, s79, v254\n"
        "v_mfma_f32_16x16x32_bf16 v[124:127], v[172:175], v[216:219], v[124:127]\n"
        "ds_read_b128 v[216:219], v249 offset:6144\n"
        "v_mfma_f32_16x16x32_bf16 v[124:127], v[168:171], v[200:203], v[124:127]\n"
        "ds_read_b128 v[200:203], v248 offset:6144\n"
        "global_load_lds_dwordx4 v245, s[86:87] offset:3072\n"
        "s_add_u32 m0, s72, 0x1000\n"
        "v_mfma_f32_16x16x32_bf16 v[156:159], v[168:171], v[204:207], v[156:159]\n"
        "ds_read_b64_tr_b16 v[168:169], v228 offset:50176\n"
        "s_add_u32 s86, s66, 0x780\n"
        "s_addc_u32 s87, s67, 0\n"
        "v_mfma_f32_16x16x32_bf16 v[156:159], v[172:175], v[220:223], v[156:159]\n"
        "ds_read_b64_tr_b16 v[172:173], v228 offset:58368\n"
        "s_add_u32 s66, s64, 0x1100000\n"
        "s_addc_u32 s67, s65, 0\n"
        "v_mfma_f32_16x16x32_bf16 v[152:155], v[164:167], v[220:223], v[152:155]\n"
        "ds_read_b128 v[220:223], v249 offset:4096\n"
        "s_waitcnt lgkmcnt(6)\n"
        "v_mfma_f32_16x16x32_bf16 v[152:155], v[160:163], v[204:207], v[152:155]\n"
        "ds_read_b128 v[204:207], v248 offset:4096\n"
        "global_load_lds_dwordx4 v242, s[86:87] offset:0\n"
        "v_mfma_f32_16x16x32_bf16 v[0:3], v[32:35], v[192:195], v[0:3]\n"
        "ds_read_b64_tr_b16 v[160:161], v226 offset:50176\n"
        "v_lshrrev_b32 v242, 6, v254\n"
        "v_mfma_f32_16x16x32_bf16 v[0:3], v[36:39], v[208:211], v[0:3]\n"
        "ds_read_b64_tr_b16 v[162:163], v227 offset:50176\n"
        "v_and_b32 v242, 12, v242\n"
        "v_mfma_f32_16x16x32_bf16 v[4:7], v[44:47], v[208:211], v[4:7]\n"
        "ds_read_b64_tr_b16 v[164:165], v226 offset:58368\n"
        "s_waitcnt lgkmcnt(8)\n"
        "v_mfma_f32_16x16x32_bf16 v[4:7], v[40:43], v[192:195], v[4:7]\n"
        "ds_read_b64_tr_b16 v[166:167], v227 offset:58368\n"
        "global_load_lds_dwordx4 v243, s[86:87] offset:1024\n"
        "v_mfma_f32_16x16x32_bf16 v[68:71], v[40:43], v[196:199], v[68:71]\n"
        "ds_read_b64_tr_b16 v[174:175], v229 offset:58368\n"
        "v_bfe_u32 v243, v254, 4, 4\n"
        "v_mfma_f32_16x16x32_bf16 v[68:71], v[44:47], v[212:215], v[68:71]\n"
        "ds_read_b64_tr_b16 v[170:171], v229 offset:50176\n"
        "v_sub_u32 v243, v243, v242\n"
        "v_mfma_f32_16x16x32_bf16 v[64:67], v[36:39], v[212:215], v[64:67]\n"
        "s_waitcnt lgkmcnt(7)\n"
        "v_add_u32 v243, s82, v243\n"
        "v_cmp_gt_i32 s[10:11], 0, v243\n"
        "v_mfma_f32_16x16x32_bf16 v[64:67], v[32:35], v[196:199], v[64:67]\n"
        "global_load_lds_dwordx4 v244, s[86:87] offset:2048\n"
        "s_mov_b32 s82, -1\n"
        "v_mfma_f32_16x16x32_bf16 v[96:99], v[32:35], v[200:203], v[96:99]\n"
        "s_waitcnt lgkmcnt(5)\n"
        "v_add_u32 v244, s91, v254\n"
        "s_xor_b32 s91, s79, 0x8000\n"
        "v_mfma_f32_16x16x32_bf16 v[96:99], v[36:39], v[216:219], v[96:99]\n"
        "s_add_u32 s91, s91, s90\n"
        "s_lshl1_add_u32 s91, s80, s91\n"
        "v_cmp_gt_i32 s[12:13], 1, v243\n"
        "v_mfma_f32_16x16x32_bf16 v[100:103], v[44:47], v[216:219], v[100:103]\n"
        "s_add_u32 s79, s79, s72\n"
        "v_cmp_gt_i32 s[14:15], 2, v243\n"
        "v_cmp_gt_i32 s[16:17], 3, v243\n"
        "v_mfma_f32_16x16x32_bf16 v[100:103], v[40:43], v[200:203], v[100:103]\n"
        "global_load_lds_dwordx4 v245, s[86:87] offset:3072\n"
        "s_lshl_b32 s80, s41, 5\n"
        "v_mfma_f32_16x16x32_bf16 v[132:135], v[40:43], v[204:207], v[132:135]\n"
        "ds_read_b64_tr_b16 v[40:41], v232 offset:50176\n"
        "s_add_u32 s80, s80, 256\n"
        "v_add_u32 v245, s91, v254\n"
        "v_mfma_f32_16x16x32_bf16 v[132:135], v[44:47], v[220:223], v[132:135]\n"
        "ds_read_b64_tr_b16 v[44:45], v232 offset:58368\n"
        "s_add_u32 s81, s80, s42\n"
        "v_cmp_gt_i32 s[20:21], 16, v243\n"
        "v_mfma_f32_16x16x32_bf16 v[128:131], v[36:39], v[220:223], v[128:131]\n"
        "ds_read_b64_tr_b16 v[36:37], v230 offset:58368\n"
        "s_lshl_b32 s81, s81, 7\n"
        "v_cmp_gt_i32 s[22:23], 17, v243\n"
        "v_mfma_f32_16x16x32_bf16 v[128:131], v[32:35], v[204:207], v[128:131]\n"
        "ds_read_b64_tr_b16 v[32:33], v230 offset:50176\n"
        "s_and_b32 s80, s81, 0xfffffc7f\n"
        "v_cmp_gt_i32 s[24:25], 18, v243\n"
        "v_mfma_f32_16x16x32_bf16 v[8:11], v[48:51], v[192:195], v[8:11]\n"
        "global_store_dwordx4 v254, a[172:175], s[70:71] offset:1024 sc1\n"
        "ds_read_b64_tr_b16 v[34:35], v231 offset:50176\n"
        "ds_read_b64_tr_b16 v[38:39], v231 offset:58368\n"
        "s_add_u32 s70, s38, s97\n"
        "s_addc_u32 s71, s39, 0\n"
        "v_mfma_f32_16x16x32_bf16 v[8:11], v[52:55], v[208:211], v[8:11]\n"
        "ds_read_b64_tr_b16 v[46:47], v233 offset:58368\n"
        "s_add_u32 s97, s52, 0x0\n"
        "v_cmp_gt_i32 s[28:29], 19, v243\n"
        "v_mfma_f32_16x16x32_bf16 v[12:15], v[60:63], v[208:211], v[12:15]\n"
        "ds_read_b64_tr_b16 v[42:43], v233 offset:50176\n"
        "s_xor_b32 s79, s79, 0x8000\n"
        "v_cmp_gt_i32 s[30:31], 32, v243\n"
        "v_mfma_f32_16x16x32_bf16 v[12:15], v[56:59], v[192:195], v[12:15]\n"
        "v_cmp_gt_i32 s[56:57], 33, v243\n"
        "v_cmp_gt_i32 s[58:59], 34, v243\n"
        "v_mfma_f32_16x16x32_bf16 v[76:79], v[56:59], v[196:199], v[76:79]\n"
        "global_store_dwordx4 v254, a[0:3], s[70:71] offset:0 sc1\n"
        "v_cmp_gt_i32 s[60:61], 35, v243\n"
        "v_cmp_gt_i32 s[62:63], 48, v243\n"
        "v_cmp_gt_i32 s[84:85], 49, v243\n"
        "v_cmp_gt_i32 s[86:87], 50, v243\n"
        "v_mfma_f32_16x16x32_bf16 v[76:79], v[60:63], v[212:215], v[76:79]\n"
        "v_cmp_gt_i32 s[88:89], 51, v243\n"
        "v_mov_b32 v242, 0x3f803f80\n"
        "v_mfma_f32_16x16x32_bf16 v[72:75], v[52:55], v[212:215], v[72:75]\n"
        "v_accvgpr_write_b32 a80, v242\n"
        "v_accvgpr_write_b32 a81, v242\n"
        "v_mfma_f32_16x16x32_bf16 v[72:75], v[48:51], v[196:199], v[72:75]\n"
        "v_accvgpr_write_b32 a82, v242\n"
        "v_accvgpr_write_b32 a83, v242\n"
        "v_mfma_f32_16x16x32_bf16 v[104:107], v[48:51], v[200:203], v[104:107]\n"
        "v_cndmask_b32 v186, 0, v242, vcc\n"
        "v_accvgpr_write_b32 a72, v186\n"
        "v_mfma_f32_16x16x32_bf16 v[104:107], v[52:55], v[216:219], v[104:107]\n"
        "s_waitcnt vmcnt(14) lgkmcnt(0)\n"
        "v_accvgpr_write_b32 a73, v186\n"
        "v_mfma_f32_16x16x32_bf16 v[108:111], v[60:63], v[216:219], v[108:111]\n"
        "s_barrier\n"
        "v_mfma_f32_16x16x32_bf16 v[108:111], v[56:59], v[200:203], v[108:111]\n"
        "v_accvgpr_write_b32 a74, v186\n"
        "v_accvgpr_write_b32 a75, v186\n"
        "v_mfma_f32_16x16x32_bf16 v[140:143], v[56:59], v[204:207], v[140:143]\n"
        "ds_read_b64_tr_b16 v[56:57], v228 offset:16384\n"
        "v_cmp_eq_u32 vcc, 0, v187\n"
        "v_mfma_f32_16x16x32_bf16 v[140:143], v[60:63], v[220:223], v[140:143]\n"
        "ds_read_b64_tr_b16 v[60:61], v228 offset:24576\n"
        "v_cndmask_b32 v187, 0, v242, vcc\n"
        "v_mfma_f32_16x16x32_bf16 v[136:139], v[52:55], v[220:223], v[136:139]\n"
        "ds_read_b64_tr_b16 v[52:53], v226 offset:24576\n"
        "v_accvgpr_write_b32 a76, v187\n"
        "v_mfma_f32_16x16x32_bf16 v[136:139], v[48:51], v[204:207], v[136:139]\n"
        "ds_read_b64_tr_b16 v[48:49], v226 offset:16384\n"
        "v_accvgpr_write_b32 a77, v187\n"
        "v_mfma_f32_16x16x32_bf16 v[16:19], v[160:163], v[192:195], v[16:19]\n"
        "ds_read_b64_tr_b16 v[50:51], v227 offset:16384\n"
        "v_accvgpr_write_b32 a78, v187\n"
        "v_mfma_f32_16x16x32_bf16 v[16:19], v[164:167], v[208:211], v[16:19]\n"
        "ds_read_b64_tr_b16 v[54:55], v227 offset:24576\n"
        "v_accvgpr_write_b32 a79, v187\n"
        "v_mfma_f32_16x16x32_bf16 v[20:23], v[172:175], v[208:211], v[20:23]\n"
        "ds_read_b64_tr_b16 v[62:63], v229 offset:24576\n"
        "v_mov_b32 v242, 0xff800000\n"
        "v_mfma_f32_16x16x32_bf16 v[20:23], v[168:171], v[192:195], v[20:23]\n"
        "ds_read_b64_tr_b16 v[58:59], v229 offset:16384\n"
        "v_xor_b32 v241, 0x8000, v241\n"
        "v_mfma_f32_16x16x32_bf16 v[84:87], v[168:171], v[196:199], v[84:87]\n"
        "global_store_dwordx4 v254, a[4:7], s[70:71] offset:1024 sc1\n"
        "s_add_u32 s70, s38, s97\n"
        "s_addc_u32 s71, s39, 0\n"
        "s_add_u32 s97, s53, 0x2000\n"
        "v_mfma_f32_16x16x32_bf16 v[84:87], v[172:175], v[212:215], v[84:87]\n"
        "v_mfma_f32_16x16x32_bf16 v[80:83], v[164:167], v[212:215], v[80:83]\n"
        "v_mfma_f32_16x16x32_bf16 v[80:83], v[160:163], v[196:199], v[80:83]\n"
        "v_mfma_f32_16x16x32_bf16 v[112:115], v[160:163], v[200:203], v[112:115]\n"
        "v_mfma_f32_16x16x32_bf16 v[112:115], v[164:167], v[216:219], v[112:115]\n"
        "v_mfma_f32_16x16x32_bf16 v[116:119], v[172:175], v[216:219], v[116:119]\n"
        "v_mfma_f32_16x16x32_bf16 v[116:119], v[168:171], v[200:203], v[116:119]\n"
        "v_mfma_f32_16x16x32_bf16 v[148:151], v[168:171], v[204:207], v[148:151]\n"
        "ds_read_b64_tr_b16 v[168:169], v232 offset:16384\n"
        "v_mfma_f32_16x16x32_bf16 v[148:151], v[172:175], v[220:223], v[148:151]\n"
        "ds_read_b64_tr_b16 v[172:173], v232 offset:24576\n"
        "v_mfma_f32_16x16x32_bf16 v[144:147], v[164:167], v[220:223], v[144:147]\n"
        "ds_read_b64_tr_b16 v[164:165], v230 offset:24576\n"
        "v_mfma_f32_16x16x32_bf16 v[144:147], v[160:163], v[204:207], v[144:147]\n"
        "global_store_dwordx4 v254, a[8:11], s[70:71] offset:0 sc1\n"
        "ds_read_b64_tr_b16 v[160:161], v230 offset:16384\n"
        "ds_read_b64_tr_b16 v[162:163], v231 offset:16384\n"
        "v_mfma_f32_16x16x32_bf16 v[24:27], v[32:35], v[192:195], v[24:27]\n"
        "ds_read_b64_tr_b16 v[166:167], v231 offset:24576\n"
        "v_mfma_f32_16x16x32_bf16 v[24:27], v[36:39], v[208:211], v[24:27]\n"
        "ds_read_b64_tr_b16 v[174:175], v233 offset:24576\n"
        "v_mfma_f32_16x16x32_bf16 v[28:31], v[44:47], v[208:211], v[28:31]\n"
        "ds_read_b64_tr_b16 v[170:171], v233 offset:16384\n"
        "v_mfma_f32_16x16x32_bf16 v[28:31], v[40:43], v[192:195], v[28:31]\n"
        "ds_read_b128 v[192:195], v246 offset:34816\n"
        "v_mfma_f32_16x16x32_bf16 v[92:95], v[40:43], v[196:199], v[92:95]\n"
        "ds_read_b128 v[208:211], v247 offset:34816\n"
        "v_mfma_f32_16x16x32_bf16 v[92:95], v[44:47], v[212:215], v[92:95]\n"
        "v_mfma_f32_16x16x32_bf16 v[88:91], v[36:39], v[212:215], v[88:91]\n"
        "ds_read_b128 v[212:215], v247 offset:32768\n"
        "v_mfma_f32_16x16x32_bf16 v[88:91], v[32:35], v[196:199], v[88:91]\n"
        "ds_read_b128 v[196:199], v246 offset:32768\n"
        "v_mfma_f32_16x16x32_bf16 v[120:123], v[32:35], v[200:203], v[120:123]\n"
        "v_mfma_f32_16x16x32_bf16 v[120:123], v[36:39], v[216:219], v[120:123]\n"
        "v_mfma_f32_16x16x32_bf16 v[124:127], v[44:47], v[216:219], v[124:127]\n"
        "ds_read_b128 v[216:219], v247 offset:38912\n"
        "v_mfma_f32_16x16x32_bf16 v[124:127], v[40:43], v[200:203], v[124:127]\n"
        "ds_read_b128 v[200:203], v246 offset:38912\n"
        "v_mfma_f32_16x16x32_bf16 v[156:159], v[40:43], v[204:207], v[156:159]\n"
        "ds_read_b64_tr_b16 v[40:41], v228 offset:17408\n"
        "s_waitcnt lgkmcnt(5)\n"
        "v_mfma_f32_16x16x32_bf16 v[156:159], v[44:47], v[220:223], v[156:159]\n"
        "ds_read_b64_tr_b16 v[44:45], v228 offset:25600\n"
        "v_mfma_f32_16x16x32_bf16 v[152:155], v[36:39], v[220:223], v[152:155]\n"
        "ds_read_b128 v[220:223], v247 offset:36864\n"
        "v_mfma_f32_16x16x32_bf16 v[152:155], v[32:35], v[204:207], v[152:155]\n"
        "ds_read_b128 v[204:207], v246 offset:36864\n"
        "v_mfma_f32_16x16x32_bf16 v[0:3], v[48:51], v[192:195], v[0:3]\n"
        "global_store_dwordx4 v254, a[12:15], s[70:71] offset:1024 sc1\n"
        "ds_read_b64_tr_b16 v[32:33], v226 offset:17408\n"
        "ds_read_b64_tr_b16 v[34:35], v227 offset:17408\n"
        "s_add_u32 s70, s38, s97\n"
        "s_addc_u32 s71, s39, 0\n"
        "v_mfma_f32_16x16x32_bf16 v[0:3], v[52:55], v[208:211], v[0:3]\n"
        "ds_read_b64_tr_b16 v[36:37], v226 offset:25600\n"
        "s_add_u32 s97, s53, 0x0\n"
        "s_waitcnt lgkmcnt(8)\n"
        "v_mfma_f32_16x16x32_bf16 v[4:7], v[60:63], v[208:211], v[4:7]\n"
        "ds_read_b64_tr_b16 v[38:39], v227 offset:25600\n"
        "v_mfma_f32_16x16x32_bf16 v[4:7], v[56:59], v[192:195], v[4:7]\n"
        "ds_read_b64_tr_b16 v[46:47], v229 offset:25600\n"
        "v_mfma_f32_16x16x32_bf16 v[68:71], v[56:59], v[196:199], v[68:71]\n"
        "ds_read_b64_tr_b16 v[42:43], v229 offset:17408\n"
        "s_waitcnt lgkmcnt(8)\n"
        "v_mfma_f32_16x16x32_bf16 v[68:71], v[60:63], v[212:215], v[68:71]\n"
        "v_mfma_f32_16x16x32_bf16 v[64:67], v[52:55], v[212:215], v[64:67]\n"
        "v_mfma_f32_16x16x32_bf16 v[64:67], v[48:51], v[196:199], v[64:67]\n"
        "v_mfma_f32_16x16x32_bf16 v[96:99], v[48:51], v[200:203], v[96:99]\n"
        "s_waitcnt lgkmcnt(4)\n"
        "v_mfma_f32_16x16x32_bf16 v[96:99], v[52:55], v[216:219], v[96:99]\n"
        "v_mfma_f32_16x16x32_bf16 v[100:103], v[60:63], v[216:219], v[100:103]\n"
        "v_mfma_f32_16x16x32_bf16 v[100:103], v[56:59], v[200:203], v[100:103]\n"
        "global_store_dwordx4 v254, a[208:211], s[70:71] offset:0 sc1\n"
        "v_mfma_f32_16x16x32_bf16 v[132:135], v[56:59], v[204:207], v[132:135]\n"
        "ds_read_b64_tr_b16 v[56:57], v232 offset:17408\n"
        "v_mfma_f32_16x16x32_bf16 v[132:135], v[60:63], v[220:223], v[132:135]\n"
        "ds_read_b64_tr_b16 v[60:61], v232 offset:25600\n"
        "v_mfma_f32_16x16x32_bf16 v[128:131], v[52:55], v[220:223], v[128:131]\n"
        "ds_read_b64_tr_b16 v[52:53], v230 offset:25600\n"
        "v_mfma_f32_16x16x32_bf16 v[128:131], v[48:51], v[204:207], v[128:131]\n"
        "ds_read_b64_tr_b16 v[48:49], v230 offset:17408\n"
        "v_mfma_f32_16x16x32_bf16 v[8:11], v[160:163], v[192:195], v[8:11]\n"
        "ds_read_b64_tr_b16 v[50:51], v231 offset:17408\n"
        "v_mfma_f32_16x16x32_bf16 v[8:11], v[164:167], v[208:211], v[8:11]\n"
        "ds_read_b64_tr_b16 v[54:55], v231 offset:25600\n"
        "v_mfma_f32_16x16x32_bf16 v[12:15], v[172:175], v[208:211], v[12:15]\n"
        "ds_read_b64_tr_b16 v[62:63], v233 offset:25600\n"
        "v_mfma_f32_16x16x32_bf16 v[12:15], v[168:171], v[192:195], v[12:15]\n"
        "ds_read_b64_tr_b16 v[58:59], v233 offset:17408\n"
        "v_mfma_f32_16x16x32_bf16 v[76:79], v[168:171], v[196:199], v[76:79]\n"
        "v_mfma_f32_16x16x32_bf16 v[76:79], v[172:175], v[212:215], v[76:79]\n"
        "v_mfma_f32_16x16x32_bf16 v[72:75], v[164:167], v[212:215], v[72:75]\n"
        "v_mfma_f32_16x16x32_bf16 v[72:75], v[160:163], v[196:199], v[72:75]\n"
        "v_mfma_f32_16x16x32_bf16 v[104:107], v[160:163], v[200:203], v[104:107]\n"
        "v_mfma_f32_16x16x32_bf16 v[104:107], v[164:167], v[216:219], v[104:107]\n"
        "v_mfma_f32_16x16x32_bf16 v[108:111], v[172:175], v[216:219], v[108:111]\n"
        "v_mfma_f32_16x16x32_bf16 v[108:111], v[168:171], v[200:203], v[108:111]\n"
        "global_store_dwordx4 v254, a[240:243], s[70:71] offset:1024 sc1\n"
        "s_waitcnt vmcnt(7) lgkmcnt(0)\n"
        "s_barrier\n"
        "v_mfma_f32_16x16x32_bf16 v[140:143], v[168:171], v[204:207], v[140:143]\n"
        "ds_write_b128 v244, a[200:203] offset:0\n"
        "v_mfma_f32_16x16x32_bf16 v[140:143], v[172:175], v[220:223], v[140:143]\n"
        "ds_read_b64_tr_b16 v[172:173], v228 offset:40960\n"
        "v_mfma_f32_16x16x32_bf16 v[136:139], v[164:167], v[220:223], v[136:139]\n"
        "ds_read_b64_tr_b16 v[164:165], v226 offset:40960\n"
        "v_mfma_f32_16x16x32_bf16 v[136:139], v[160:163], v[204:207], v[136:139]\n"
        "ds_write_b128 v244, a[204:207] offset:1024\n"
        "v_mfma_f32_16x16x32_bf16 v[16:19], v[32:35], v[192:195], v[16:19]\n"
        "ds_read_b64_tr_b16 v[160:161], v226 offset:32768\n"
        "global_load_dword v253, v252, s[46:47] offset:0\n"
        "v_mfma_f32_16x16x32_bf16 v[16:19], v[36:39], v[208:211], v[16:19]\n"
        "ds_read_b64_tr_b16 v[162:163], v227 offset:32768\n"
        "v_mfma_f32_16x16x32_bf16 v[20:23], v[44:47], v[208:211], v[20:23]\n"
        "ds_write_b128 v244, a[160:163] offset:4096\n"
        "v_mfma_f32_16x16x32_bf16 v[20:23], v[40:43], v[192:195], v[20:23]\n"
        "ds_read_b64_tr_b16 v[166:167], v227 offset:40960\n"
        "v_mfma_f32_16x16x32_bf16 v[84:87], v[40:43], v[196:199], v[84:87]\n"
        "ds_read_b64_tr_b16 v[174:175], v229 offset:40960\n"
        "v_mfma_f32_16x16x32_bf16 v[84:87], v[44:47], v[212:215], v[84:87]\n"
        "ds_write_b128 v244, a[164:167] offset:5120\n"
        "v_mfma_f32_16x16x32_bf16 v[80:83], v[36:39], v[212:215], v[80:83]\n"
        "ds_read_b64_tr_b16 v[168:169], v228 offset:32768\n"
        "v_mfma_f32_16x16x32_bf16 v[80:83], v[32:35], v[196:199], v[80:83]\n"
        "ds_read_b64_tr_b16 v[170:171], v229 offset:32768\n"
        "v_mfma_f32_16x16x32_bf16 v[112:115], v[32:35], v[200:203], v[112:115]\n"
        "ds_write_b128 v244, a[168:171] offset:8192\n"
        "v_mfma_f32_16x16x32_bf16 v[112:115], v[36:39], v[216:219], v[112:115]\n"
        "global_store_dwordx4 v254, a[212:215], s[70:71] offset:2048 sc1\n"
        "v_mfma_f32_16x16x32_bf16 v[116:119], v[44:47], v[216:219], v[116:119]\n"
        "v_mfma_f32_16x16x32_bf16 v[116:119], v[40:43], v[200:203], v[116:119]\n"
        "ds_write_b128 v244, a[172:175] offset:9216\n"
        "v_mfma_f32_16x16x32_bf16 v[148:151], v[40:43], v[204:207], v[148:151]\n"
        "ds_read_b64_tr_b16 v[40:41], v232 offset:32768\n"
        "v_mfma_f32_16x16x32_bf16 v[148:151], v[44:47], v[220:223], v[148:151]\n"
        "ds_read_b64_tr_b16 v[44:45], v232 offset:40960\n"
        "v_mfma_f32_16x16x32_bf16 v[144:147], v[36:39], v[220:223], v[144:147]\n"
        "ds_write_b128 v244, a[0:3] offset:12288\n"
        "v_mfma_f32_16x16x32_bf16 v[144:147], v[32:35], v[204:207], v[144:147]\n"
        "ds_read_b64_tr_b16 v[32:33], v230 offset:32768\n"
        "v_mfma_f32_16x16x32_bf16 v[24:27], v[48:51], v[192:195], v[24:27]\n"
        "ds_read_b64_tr_b16 v[34:35], v231 offset:32768\n"
        "v_mfma_f32_16x16x32_bf16 v[24:27], v[52:55], v[208:211], v[24:27]\n"
        "ds_write_b128 v244, a[4:7] offset:13312\n"
        "v_mfma_f32_16x16x32_bf16 v[28:31], v[60:63], v[208:211], v[28:31]\n"
        "ds_read_b64_tr_b16 v[36:37], v230 offset:40960\n"
        "v_xor_b32 v244, 0xc000, v244\n"
        "v_mfma_f32_16x16x32_bf16 v[28:31], v[56:59], v[192:195], v[28:31]\n"
        "ds_read_b64_tr_b16 v[38:39], v231 offset:40960\n"
        "v_mfma_f32_16x16x32_bf16 v[92:95], v[56:59], v[196:199], v[92:95]\n"
        "ds_read_b64_tr_b16 v[46:47], v233 offset:40960\n"
        "v_mfma_f32_16x16x32_bf16 v[92:95], v[60:63], v[212:215], v[92:95]\n"
        "ds_read_b64_tr_b16 v[42:43], v233 offset:32768\n"
        "v_mfma_f32_16x16x32_bf16 v[88:91], v[52:55], v[212:215], v[88:91]\n"
        "ds_read_b128 v[192:195], v246 offset:2048\n"
        "v_mfma_f32_16x16x32_bf16 v[88:91], v[48:51], v[196:199], v[88:91]\n"
        "ds_read_b128 v[208:211], v247 offset:2048\n"
        "v_mfma_f32_16x16x32_bf16 v[120:123], v[48:51], v[200:203], v[120:123]\n"
        "ds_read_b128 v[196:199], v246 offset:0\n"
        "v_mfma_f32_16x16x32_bf16 v[120:123], v[52:55], v[216:219], v[120:123]\n"
        "ds_read_b128 v[212:215], v247 offset:0\n"
        "v_mfma_f32_16x16x32_bf16 v[124:127], v[60:63], v[216:219], v[124:127]\n"
        "global_store_dwordx4 v254, a[244:247], s[70:71] offset:3072 sc1\n"
        "ds_read_b128 v[216:219], v247 offset:6144\n"
        "s_add_u32 s70, s38, s97\n"
        "s_addc_u32 s71, s39, 0\n"
        "s_add_u32 s97, s53, 0x0\n"
        "s_add_u32 s97, s97, s75\n"
        "v_mfma_f32_16x16x32_bf16 v[124:127], v[56:59], v[200:203], v[124:127]\n"
        "ds_read_b128 v[200:203], v246 offset:6144\n"
        "v_mfma_f32_16x16x32_bf16 v[156:159], v[56:59], v[204:207], v[156:159]\n"
        "ds_read_b64_tr_b16 v[56:57], v228 offset:33792\n"
        "v_mfma_f32_16x16x32_bf16 v[156:159], v[60:63], v[220:223], v[156:159]\n"
        "ds_read_b64_tr_b16 v[60:61], v228 offset:41984\n"
        "s_waitcnt lgkmcnt(7)\n"
        "v_mfma_f32_16x16x32_bf16 v[152:155], v[52:55], v[220:223], v[152:155]\n"
        "ds_read_b128 v[220:223], v247 offset:4096\n"
        "s_waitcnt lgkmcnt(7)\n"
        "v_mfma_f32_16x16x32_bf16 v[152:155], v[48:51], v[204:207], v[152:155]\n"
        "ds_read_b128 v[204:207], v246 offset:4096\n"
        "v_mfma_f32_16x16x32_bf16 v[0:3], v[160:163], v[192:195], v[0:3]\n"
        "ds_read_b64_tr_b16 v[48:49], v226 offset:33792\n"
        "s_waitcnt lgkmcnt(7)\n"
        "v_mfma_f32_16x16x32_bf16 v[0:3], v[164:167], v[208:211], v[0:3]\n"
        "LHA9_%=:\n"
        "s_cbranch_execz LHRA_%=\n"
        "ds_read_b64_tr_b16 v[50:51], v227 offset:33792\n"
        "v_mfma_f32_16x16x32_bf16 v[4:7], v[172:175], v[208:211], v[4:7]\n"
        "ds_read_b64_tr_b16 v[52:53], v226 offset:41984\n"
        "v_mfma_f32_16x16x32_bf16 v[4:7], v[168:171], v[192:195], v[4:7]\n"
        "ds_read_b64_tr_b16 v[54:55], v227 offset:41984\n"
        "v_mfma_f32_16x16x32_bf16 v[68:71], v[168:171], v[196:199], v[68:71]\n"
        "ds_read_b64_tr_b16 v[62:63], v229 offset:41984\n"
        "s_waitcnt lgkmcnt(8)\n"
        "v_mfma_f32_16x16x32_bf16 v[68:71], v[172:175], v[212:215], v[68:71]\n"
        "ds_read_b64_tr_b16 v[58:59], v229 offset:33792\n"
        "v_cvt_pk_bf16_f32 v0, v0, v1\n"
        "v_mfma_f32_16x16x32_bf16 v[64:67], v[164:167], v[212:215], v[64:67]\n"
        "ds_write_b128 v244, a[8:11] offset:0\n"
        "v_mfma_f32_16x16x32_bf16 v[64:67], v[160:163], v[196:199], v[64:67]\n"
        "global_store_dwordx4 v254, a[216:219], s[70:71] offset:0 sc1\n"
        "v_cvt_pk_bf16_f32 v1, v2, v3\n"
        "v_cvt_pk_bf16_f32 v2, v4, v5\n"
        "v_cvt_pk_bf16_f32 v3, v6, v7\n"
        "v_mfma_f32_16x16x32_bf16 v[96:99], v[160:163], v[200:203], v[96:99]\n"
        "s_waitcnt lgkmcnt(5)\n"
        "v_mfma_f32_16x16x32_bf16 v[96:99], v[164:167], v[216:219], v[96:99]\n"
        "ds_write_b128 v244, a[12:15] offset:1024\n"
        "v_mfma_f32_16x16x32_bf16 v[100:103], v[172:175], v[216:219], v[100:103]\n"
        "v_cvt_pk_bf16_f32 v64, v64, v65\n"
        "v_cvt_pk_bf16_f32 v65, v66, v67\n"
        "v_mfma_f32_16x16x32_bf16 v[100:103], v[168:171], v[200:203], v[100:103]\n"
        "v_cvt_pk_bf16_f32 v66, v68, v69\n"
        "v_cvt_pk_bf16_f32 v67, v70, v71\n"
        "v_mfma_f32_16x16x32_bf16 v[132:135], v[168:171], v[204:207], v[132:135]\n"
        "ds_write_b128 v244, a[192:195] offset:4096\n"
        "v_mfma_f32_16x16x32_bf16 v[132:135], v[172:175], v[220:223], v[132:135]\n"
        "ds_read_b64_tr_b16 v[172:173], v232 offset:41984\n"
        "v_cvt_pk_bf16_f32 v96, v96, v97\n"
        "v_mfma_f32_16x16x32_bf16 v[128:131], v[164:167], v[220:223], v[128:131]\n"
        "ds_read_b64_tr_b16 v[164:165], v230 offset:41984\n"
        "v_cvt_pk_bf16_f32 v97, v98, v99\n"
        "v_mfma_f32_16x16x32_bf16 v[128:131], v[160:163], v[204:207], v[128:131]\n"
        "ds_write_b128 v244, a[196:199] offset:5120\n"
        "v_mfma_f32_16x16x32_bf16 v[8:11], v[32:35], v[192:195], v[8:11]\n"
        "ds_read_b64_tr_b16 v[160:161], v230 offset:33792\n"
        "v_cvt_pk_bf16_f32 v98, v100, v101\n"
        "v_mfma_f32_16x16x32_bf16 v[8:11], v[36:39], v[208:211], v[8:11]\n"
        "ds_read_b64_tr_b16 v[162:163], v231 offset:33792\n"
        "v_cvt_pk_bf16_f32 v99, v102, v103\n"
        "v_mfma_f32_16x16x32_bf16 v[12:15], v[44:47], v[208:211], v[12:15]\n"
        "ds_read_b64_tr_b16 v[166:167], v231 offset:41984\n"
        "v_cvt_pk_bf16_f32 v128, v128, v129\n"
        "v_mfma_f32_16x16x32_bf16 v[12:15], v[40:43], v[192:195], v[12:15]\n"
        "ds_read_b64_tr_b16 v[174:175], v233 offset:41984\n"
        "v_cvt_pk_bf16_f32 v129, v130, v131\n"
        "v_mfma_f32_16x16x32_bf16 v[76:79], v[40:43], v[196:199], v[76:79]\n"
        "ds_read_b64_tr_b16 v[168:169], v232 offset:33792\n"
        "v_cvt_pk_bf16_f32 v4, v8, v9\n"
        "v_mfma_f32_16x16x32_bf16 v[76:79], v[44:47], v[212:215], v[76:79]\n"
        "ds_read_b64_tr_b16 v[170:171], v233 offset:33792\n"
        "v_cvt_pk_bf16_f32 v5, v10, v11\n"
        "v_mfma_f32_16x16x32_bf16 v[72:75], v[36:39], v[212:215], v[72:75]\n"
        "ds_write_b128 v244, a[232:235] offset:8192\n"
        "v_mfma_f32_16x16x32_bf16 v[72:75], v[32:35], v[196:199], v[72:75]\n"
        "global_store_dwordx4 v254, a[248:251], s[70:71] offset:1024 sc1\n"
        "s_add_u32 s82, s82, 1\n"
        "v_cvt_pk_bf16_f32 v6, v12, v13\n"
        "v_cvt_pk_bf16_f32 v7, v14, v15\n"
        "v_cvt_pk_bf16_f32 v70, v76, v77\n"
        "v_cvt_pk_bf16_f32 v71, v78, v79\n"
        "v_mfma_f32_16x16x32_bf16 v[104:107], v[32:35], v[200:203], v[104:107]\n"
        "s_cmp_ge_u32 s82, s44\n"
        "s_addc_u32 s90, s82, 0\n"
        "v_cvt_pk_bf16_f32 v68, v72, v73\n"
        "v_mfma_f32_16x16x32_bf16 v[104:107], v[36:39], v[216:219], v[104:107]\n"
        "ds_write_b128 v244, a[236:239] offset:9216\n"
        "v_mfma_f32_16x16x32_bf16 v[108:111], v[44:47], v[216:219], v[108:111]\n"
        "s_lshl_b32 s90, s90, 15\n"
        "v_cvt_pk_bf16_f32 v69, v74, v75\n"
        "v_cvt_pk_bf16_f32 v131, v134, v135\n"
        "v_mfma_f32_16x16x32_bf16 v[108:111], v[40:43], v[200:203], v[108:111]\n"
        "s_waitcnt lgkmcnt(2)\n"
        "v_cvt_pk_bf16_f32 v130, v132, v133\n"
        "v_cvt_pk_bf16_f32 v100, v104, v105\n"
        "v_mfma_f32_16x16x32_bf16 v[140:143], v[40:43], v[204:207], v[140:143]\n"
        "ds_write_b128 v244, a[224:227] offset:12288\n"
        "v_mfma_f32_16x16x32_bf16 v[140:143], v[44:47], v[220:223], v[140:143]\n"
        "s_barrier\n"
        "v_mfma_f32_16x16x32_bf16 v[136:139], v[36:39], v[220:223], v[136:139]\n"
        "s_add_u32 s68, s64, s90\n"
        "s_addc_u32 s69, s65, 0\n"
        "v_cndmask_b32 v36, 0, v242, s[10:11]\n"
        "v_mfma_f32_16x16x32_bf16 v[136:139], v[32:35], v[204:207], v[136:139]\n"
        "ds_write_b128 v244, a[228:231] offset:13312\n"
        "v_mfma_f32_16x16x32_bf16 v[16:19], v[48:51], v[192:195], v[16:19]\n"
        "global_store_dwordx4 v254, a[220:223], s[70:71] offset:2048 sc1\n"
        "s_xor_b32 s78, s78, 0x8000\n"
        "v_cndmask_b32 v37, 0, v242, s[12:13]\n"
        "v_cndmask_b32 v38, 0, v242, s[14:15]\n"
        "v_cndmask_b32 v39, 0, v242, s[16:17]\n"
        "v_cvt_pk_bf16_f32 v134, v140, v141\n"
        "v_mfma_f32_16x16x32_bf16 v[16:19], v[52:55], v[208:211], v[16:19]\n"
        "s_lshr_b32 s52, s41, 1\n"
        "v_cvt_pk_bf16_f32 v101, v106, v107\n"
        "v_cvt_pk_bf16_f32 v102, v108, v109\n"
        "v_mfma_f32_16x16x32_bf16 v[20:23], v[60:63], v[208:211], v[20:23]\n"
        "ds_write_b128 v245, a[176:179] offset:0\n"
        "v_mfma_f32_16x16x32_bf16 v[20:23], v[56:59], v[192:195], v[20:23]\n"
        "v_cvt_pk_bf16_f32 v103, v110, v111\n"
        "s_nop 0\n"
        "v_cvt_pk_bf16_f32 v8, v16, v17\n"
        "v_mfma_f32_16x16x32_bf16 v[84:87], v[56:59], v[196:199], v[84:87]\n"
        "s_barrier\n"
        "v_mfma_f32_16x16x32_bf16 v[84:87], v[60:63], v[212:215], v[84:87]\n"
        "ds_read_b128 a[96:99], v240 offset:0\n"
        "s_lshl_b32 s52, s52, 11\n"
        "v_cvt_pk_bf16_f32 v9, v18, v19\n"
        "v_mfma_f32_16x16x32_bf16 v[80:83], v[52:55], v[212:215], v[80:83]\n"
        "ds_read_b128 a[100:103], v240 offset:1024\n"
        "v_cvt_pk_bf16_f32 v10, v20, v21\n"
        "v_mfma_f32_16x16x32_bf16 v[80:83], v[48:51], v[196:199], v[80:83]\n"
        "ds_read_b128 a[104:107], v240 offset:2048\n"
        "v_cvt_pk_bf16_f32 v11, v22, v23\n"
        "v_mfma_f32_16x16x32_bf16 v[112:115], v[48:51], v[200:203], v[112:115]\n"
        "ds_read_b128 a[108:111], v240 offset:3072\n"
        "v_cvt_pk_bf16_f32 v74, v84, v85\n"
        "v_mfma_f32_16x16x32_bf16 v[112:115], v[52:55], v[216:219], v[112:115]\n"
        "ds_write_b128 v245, a[180:183] offset:1024\n"
        "v_mfma_f32_16x16x32_bf16 v[116:119], v[60:63], v[216:219], v[116:119]\n"
        "ds_read_b128 a[112:115], v240 offset:4096\n"
        "v_cvt_pk_bf16_f32 v72, v80, v81\n"
        "v_mfma_f32_16x16x32_bf16 v[116:119], v[56:59], v[200:203], v[116:119]\n"
        "ds_read_b128 a[116:119], v240 offset:5120\n"
        "v_cvt_pk_bf16_f32 v73, v82, v83\n"
        "v_mfma_f32_16x16x32_bf16 v[148:151], v[56:59], v[204:207], v[148:151]\n"
        "ds_read_b128 a[120:123], v240 offset:6144\n"
        "v_cvt_pk_bf16_f32 v75, v86, v87\n"
        "v_mfma_f32_16x16x32_bf16 v[148:151], v[60:63], v[220:223], v[148:151]\n"
        "ds_write_b128 v245, a[184:187] offset:2048\n"
        "v_mfma_f32_16x16x32_bf16 v[144:147], v[52:55], v[220:223], v[144:147]\n"
        "ds_read_b128 a[124:127], v240 offset:7168\n"
        "v_cndmask_b32 v52, 0, v242, s[20:21]\n"
        "v_mfma_f32_16x16x32_bf16 v[144:147], v[48:51], v[204:207], v[144:147]\n"
        "global_store_dwordx4 v254, a[252:255], s[70:71] offset:3072 sc1\n"
        "s_add_u32 s70, s38, s97\n"
        "s_addc_u32 s71, s39, 0\n"
        "s_add_u32 s97, s53, 0x2000\n"
        "v_cndmask_b32 v53, 0, v242, s[22:23]\n"
        "v_cndmask_b32 v54, 0, v242, s[24:25]\n"
        "v_mfma_f32_16x16x32_bf16 v[24:27], v[160:163], v[192:195], v[24:27]\n"
        "ds_write_b128 v245, a[188:191] offset:3072\n"
        "v_mfma_f32_16x16x32_bf16 v[24:27], v[164:167], v[208:211], v[24:27]\n"
        "s_add_u32 s97, s97, s75\n"
        "v_cndmask_b32 v55, 0, v242, s[28:29]\n"
        "v_cvt_pk_bf16_f32 v104, v112, v113\n"
        "v_mfma_f32_16x16x32_bf16 v[28:31], v[172:175], v[208:211], v[28:31]\n"
        "global_store_dwordx4 v254, a[180:183], s[70:71] offset:1024 sc1\n"
        "s_add_u32 s74, s68, 0x1000\n"
        "s_addc_u32 s75, s69, 0\n"
        "s_lshl2_add_u32 s53, s42, s40\n"
        "v_cvt_pk_bf16_f32 v105, v114, v115\n"
        "v_cvt_pk_bf16_f32 v12, v24, v25\n"
        "v_mfma_f32_16x16x32_bf16 v[28:31], v[168:171], v[192:195], v[28:31]\n"
        "ds_write_b128 v245, a[16:19] offset:8192\n"
        "v_mfma_f32_16x16x32_bf16 v[92:95], v[168:171], v[196:199], v[92:95]\n"
        "s_add_u32 s52, s52, s53\n"
        "v_cvt_pk_bf16_f32 v13, v26, v27\n"
        "v_cvt_pk_bf16_f32 v106, v116, v117\n"
        "v_mfma_f32_16x16x32_bf16 v[92:95], v[172:175], v[212:215], v[92:95]\n"
        "s_waitcnt lgkmcnt(2)\n"
        "v_cvt_pk_bf16_f32 v107, v118, v119\n"
        "v_cvt_pk_bf16_f32 v14, v28, v29\n"
        "v_mfma_f32_16x16x32_bf16 v[88:91], v[164:167], v[212:215], v[88:91]\n"
        "ds_write_b128 v245, a[20:23] offset:9216\n"
        "v_mfma_f32_16x16x32_bf16 v[88:91], v[160:163], v[196:199], v[88:91]\n"
        "s_add_u32 s52, s52, 0\n"
        "v_cvt_pk_bf16_f32 v15, v30, v31\n"
        "v_cvt_pk_bf16_f32 v78, v92, v93\n"
        "v_mfma_f32_16x16x32_bf16 v[120:123], v[160:163], v[200:203], v[120:123]\n"
        "s_lshl_b32 s52, s52, 10\n"
        "v_cvt_pk_bf16_f32 v79, v94, v95\n"
        "v_cvt_pk_bf16_f32 v132, v136, v137\n"
        "v_mfma_f32_16x16x32_bf16 v[32:35], a[96:99], v[0:3], 0\n"
        "ds_write_b128 v245, a[24:27] offset:10240\n"
        "v_mfma_f32_16x16x32_bf16 v[32:35], a[100:103], v[4:7], v[32:35]\n"
        "s_add_u32 s26, s18, s52\n"
        "s_addc_u32 s27, s19, 0\n"
        "v_cvt_pk_bf16_f32 v76, v88, v89\n"
        "v_mfma_f32_16x16x32_bf16 v[32:35], a[104:107], v[8:11], v[32:35]\n"
        "s_add_u32 s76, s66, s90\n"
        "s_addc_u32 s77, s67, 0\n"
        "v_cvt_pk_bf16_f32 v77, v90, v91\n"
        "v_mfma_f32_16x16x32_bf16 v[32:35], a[108:111], v[12:15], v[32:35]\n"
        "ds_write_b128 v245, a[28:31] offset:11264\n"
        "v_mfma_f32_16x16x32_bf16 v[120:123], v[164:167], v[216:219], v[120:123]\n"
        "v_xor_b32 v245, 0xc000, v245\n"
        "v_cvt_pk_bf16_f32 v133, v138, v139\n"
        "v_mfma_f32_16x16x32_bf16 v[124:127], v[172:175], v[216:219], v[124:127]\n"
        "v_cvt_pk_bf16_f32 v135, v142, v143\n"
        "v_cvt_pk_bf16_f32 v136, v144, v145\n"
        "v_mfma_f32_16x16x32_bf16 v[124:127], v[168:171], v[200:203], v[124:127]\n"
        "v_max_f32 v246, v32, v33\n"
        "v_max3_f32 v246, v34, v35, v246\n"
        "v_mfma_f32_16x16x32_bf16 v[156:159], v[168:171], v[204:207], v[156:159]\n"
        "v_mov_b32 v247, v246\n"
        "v_cvt_pk_bf16_f32 v108, v120, v121\n"
        "v_mfma_f32_16x16x32_bf16 v[36:39], a[108:111], v[76:79], v[36:39]\n"
        "v_permlane16_swap_b32 v246, v247\n"
        "v_mfma_f32_16x16x32_bf16 v[36:39], a[104:107], v[72:75], v[36:39]\n"
        "ds_read_b128 a[104:107], v240 offset:10240\n"
        "v_max_f32 v246, v246, v247\n"
        "v_mfma_f32_16x16x32_bf16 v[36:39], a[100:103], v[68:71], v[36:39]\n"
        "ds_read_b128 a[100:103], v240 offset:9216\n"
        "v_mov_b32 v247, v246\n"
        "v_mfma_f32_16x16x32_bf16 v[36:39], a[96:99], v[64:67], v[36:39]\n"
        "global_store_dwordx4 v254, a[176:179], s[70:71] offset:0 sc1\n"
        "ds_read_b128 a[96:99], v240 offset:8192\n"
        "s_waitcnt lgkmcnt(3)\n"
        "v_permlane32_swap_b32 v246, v247\n"
        "v_mfma_f32_16x16x32_bf16 v[156:159], v[172:175], v[220:223], v[156:159]\n"
        "ds_read_b128 a[108:111], v240 offset:11264\n"
        "v_max_f32 v246, v246, v247\n"
        "v_mfma_f32_16x16x32_bf16 v[152:155], v[164:167], v[220:223], v[152:155]\n"
        "v_sub_f32 v224, 0, v246\n"
        "v_sub_f32 v225, 0, v246\n"
        "v_mfma_f32_16x16x32_bf16 v[152:155], v[160:163], v[204:207], v[152:155]\n"
        "v_sub_f32 v226, 0, v246\n"
        "v_sub_f32 v227, 0, v246\n"
        "v_mfma_f32_16x16x32_bf16 v[48:51], a[112:115], v[0:3], 0\n"
        "v_max_f32 v248, v36, v37\n"
        "v_max3_f32 v248, v38, v39, v248\n"
        "v_mfma_f32_16x16x32_bf16 v[48:51], a[116:119], v[4:7], v[48:51]\n"
        "v_mov_b32 v249, v248\n"
        "v_add_f32 v32, v32, v224\n"
        "v_mfma_f32_16x16x32_bf16 v[48:51], a[120:123], v[8:11], v[48:51]\n"
        "v_permlane16_swap_b32 v248, v249\n"
        "v_mfma_f32_16x16x32_bf16 v[48:51], a[124:127], v[12:15], v[48:51]\n"
        "v_max_f32 v248, v248, v249\n"
        "v_mov_b32 v249, v248\n"
        "v_mfma_f32_16x16x32_bf16 v[52:55], a[124:127], v[76:79], v[52:55]\n"
        "global_store_dwordx4 v254, a[184:187], s[70:71] offset:2048 sc1\n"
        "s_waitcnt lgkmcnt(1)\n"
        "v_permlane32_swap_b32 v248, v249\n"
        "v_max_f32 v248, v248, v249\n"
        "v_mfma_f32_16x16x32_bf16 v[52:55], a[120:123], v[72:75], v[52:55]\n"
        "ds_read_b128 a[120:123], v240 offset:14336\n"
        "v_sub_f32 v228, 0, v248\n"
        "v_mfma_f32_16x16x32_bf16 v[52:55], a[116:119], v[68:71], v[52:55]\n"
        "ds_read_b128 a[116:119], v240 offset:13312\n"
        "v_sub_f32 v229, 0, v248\n"
        "v_mfma_f32_16x16x32_bf16 v[52:55], a[112:115], v[64:67], v[52:55]\n"
        "v_sub_f32 v230, 0, v248\n"
        "v_sub_f32 v231, 0, v248\n"
        "s_barrier\n"
        "ds_read_b128 a[144:147], v241 offset:0\n"
        "v_exp_f32 v32, v32\n"
        "v_add_f32 v33, v33, v224\n"
        "v_exp_f32 v33, v33\n"
        "ds_read_b128 a[148:151], v241 offset:1024\n"
        "v_cvt_pk_bf16_f32 v16, v32, v33\n"
        "v_add_f32 v34, v34, v224\n"
        "v_exp_f32 v34, v34\n"
        "v_add_f32 v35, v35, v224\n"
        "v_exp_f32 v35, v35\n"
        "ds_read_b128 a[152:155], v241 offset:2048\n"
        "v_cvt_pk_bf16_f32 v17, v34, v35\n"
        "v_mfma_f32_16x16x32_bf16 v[32:35], a[96:99], v[0:3], v[224:227]\n"
        "global_store_dwordx4 v254, a[188:191], s[70:71] offset:3072 sc1\n"
        "s_waitcnt lgkmcnt(4)\n"
        "v_add_f32 v36, v36, v228\n"
        "v_exp_f32 v36, v36\n"
        "v_mfma_f32_16x16x32_bf16 v[32:35], a[100:103], v[4:7], v[32:35]\n"
        "ds_read_b128 a[112:115], v240 offset:12288\n"
        "v_add_f32 v37, v37, v228\n"
        "v_mfma_f32_16x16x32_bf16 v[32:35], a[104:107], v[8:11], v[32:35]\n"
        "v_exp_f32 v37, v37\n"
        "v_mfma_f32_16x16x32_bf16 v[32:35], a[108:111], v[12:15], v[32:35]\n"
        "v_cvt_pk_bf16_f32 v20, v36, v37\n"
        "v_add_f32 v38, v38, v228\n"
        "v_exp_f32 v38, v38\n"
        "v_add_f32 v39, v39, v228\n"
        "v_exp_f32 v39, v39\n"
        "ds_read_b128 a[156:159], v241 offset:3072\n"
        "v_cvt_pk_bf16_f32 v21, v38, v39\n"
        "ds_read_b128 a[128:131], v241 offset:4096\n"
        "v_add_f32 v48, v48, v224\n"
        "v_exp_f32 v48, v48\n"
        "v_add_f32 v49, v49, v224\n"
        "v_exp_f32 v49, v49\n"
        "ds_read_b128 a[124:127], v240 offset:15360\n"
        "v_cvt_pk_bf16_f32 v18, v48, v49\n"
        "ds_read_b128 a[132:135], v241 offset:5120\n"
        "s_add_u32 s70, s38, s97\n"
        "s_addc_u32 s71, s39, 0\n"
        "global_store_dwordx4 v254, a[16:19], s[70:71] offset:0 sc1\n"
        "v_add_f32 v50, v50, v224\n"
        "v_exp_f32 v50, v50\n"
        "v_add_f32 v51, v51, v224\n"
        "v_exp_f32 v51, v51\n"
        "ds_read_b128 a[136:139], v241 offset:6144\n"
        "v_cvt_pk_bf16_f32 v19, v50, v51\n"
        "s_waitcnt lgkmcnt(5)\n"
        "v_mfma_f32_16x16x32_bf16 v[48:51], a[112:115], v[0:3], v[224:227]\n"
        "ds_read_b128 a[140:143], v241 offset:7168\n"
        "s_mul_i32 s96, s40, 4224\n"
        "v_cndmask_b32 v36, v228, v242, s[30:31]\n"
        "v_mfma_f32_16x16x32_bf16 v[48:51], a[116:119], v[4:7], v[48:51]\n"
        "v_cndmask_b32 v37, v228, v242, s[56:57]\n"
        "v_cndmask_b32 v38, v228, v242, s[58:59]\n"
        "v_mfma_f32_16x16x32_bf16 v[48:51], a[120:123], v[8:11], v[48:51]\n"
        "v_cvt_pk_bf16_f32 v141, v154, v155\n"
        "v_cndmask_b32 v39, v228, v242, s[60:61]\n"
        "s_nop 1\n"
        "v_mfma_f32_16x16x32_bf16 v[36:39], a[108:111], v[76:79], v[36:39]\n"
        "ds_read_b128 a[108:111], v240 offset:19456\n"
        "v_bfe_u32 v154, v254, 4, 1\n"
        "v_mfma_f32_16x16x32_bf16 v[36:39], a[104:107], v[72:75], v[36:39]\n"
        "ds_read_b128 a[104:107], v240 offset:18432\n"
        "v_mul_u32_u24 v154, 0xaaaa, v154\n"
        "v_mfma_f32_16x16x32_bf16 v[36:39], a[100:103], v[68:71], v[36:39]\n"
        "s_waitcnt lgkmcnt(4)\n"
        "v_add_u32 v154, 0x4444, v154\n"
        "v_add_f32 v52, v52, v228\n"
        "v_mfma_f32_16x16x32_bf16 v[36:39], a[96:99], v[64:67], v[36:39]\n"
        "v_exp_f32 v52, v52\n"
        "v_mfma_f32_16x16x32_bf16 v[48:51], a[124:127], v[12:15], v[48:51]\n"
        "ds_read_b128 a[96:99], v240 offset:16384\n"
        "v_add_f32 v53, v53, v228\n"
        "v_mfma_f32_16x16x32_bf16 v[160:163], a[144:147], v[16:19], 0\n"
        "v_exp_f32 v53, v53\n"
        "v_mfma_f32_16x16x32_bf16 v[164:167], a[148:151], v[16:19], 0\n"
        "ds_read_b128 a[100:103], v240 offset:17408\n"
        "v_cvt_pk_bf16_f32 v22, v52, v53\n"
        "v_mfma_f32_16x16x32_bf16 v[168:171], a[152:155], v[16:19], 0\n"
        "v_add_f32 v54, v54, v228\n"
        "v_add_f32 v55, v55, v228\n"
        "v_mfma_f32_16x16x32_bf16 v[172:175], a[156:159], v[16:19], 0\n"
        "v_exp_f32 v54, v54\n"
        "v_mfma_f32_16x16x32_bf16 v[176:179], a[128:131], v[16:19], 0\n"
        "v_exp_f32 v55, v55\n"
        "v_mfma_f32_16x16x32_bf16 v[180:183], a[132:135], v[16:19], 0\n"
        "s_waitcnt lgkmcnt(2)\n"
        "v_cvt_pk_bf16_f32 v23, v54, v55\n"
        "s_nop 1\n"
        "v_smfmac_f32_16x16x64_bf16 a[64:67], a[72:75], v[16:23], v154\n"
        "global_store_dwordx4 v254, a[20:23], s[70:71] offset:1024 sc1\n"
        "v_exp_f32 v32, v32\n"
        "v_exp_f32 v33, v33\n"
        "v_mfma_f32_16x16x32_bf16 v[192:195], a[144:147], v[20:23], 0\n"
        "ds_read_b128 a[144:147], v241 offset:8192\n"
        "v_cvt_pk_bf16_f32 v80, v32, v33\n"
        "v_mfma_f32_16x16x32_bf16 v[196:199], a[148:151], v[20:23], 0\n"
        "ds_read_b128 a[148:151], v241 offset:9216\n"
        "v_cndmask_b32 v52, v228, v242, s[62:63]\n"
        "v_mfma_f32_16x16x32_bf16 v[200:203], a[152:155], v[20:23], 0\n"
        "ds_read_b128 a[152:155], v241 offset:10240\n"
        "v_cndmask_b32 v53, v228, v242, s[84:85]\n"
        "v_mfma_f32_16x16x32_bf16 v[204:207], a[156:159], v[20:23], 0\n"
        "ds_read_b128 a[156:159], v241 offset:11264\n"
        "v_cndmask_b32 v54, v228, v242, s[86:87]\n"
        "v_mfma_f32_16x16x32_bf16 v[208:211], a[128:131], v[20:23], 0\n"
        "ds_read_b128 a[128:131], v241 offset:12288\n"
        "v_cndmask_b32 v55, v228, v242, s[88:89]\n"
        "s_nop 1\n"
        "v_mfma_f32_16x16x32_bf16 v[52:55], a[124:127], v[76:79], v[52:55]\n"
        "v_exp_f32 v34, v34\n"
        "v_mfma_f32_16x16x32_bf16 v[52:55], a[120:123], v[72:75], v[52:55]\n"
        "v_exp_f32 v35, v35\n"
        "v_mfma_f32_16x16x32_bf16 v[52:55], a[116:119], v[68:71], v[52:55]\n"
        "ds_read_b128 a[116:119], v240 offset:21504\n"
        "v_cvt_pk_bf16_f32 v81, v34, v35\n"
        "v_mfma_f32_16x16x32_bf16 v[52:55], a[112:115], v[64:67], v[52:55]\n"
        "ds_read_b128 a[112:115], v240 offset:20480\n"
        "v_cndmask_b32 v32, v224, v242, s[10:11]\n"
        "v_mfma_f32_16x16x32_bf16 v[212:215], a[132:135], v[20:23], 0\n"
        "s_waitcnt lgkmcnt(5)\n"
        "v_exp_f32 v36, v36\n"
        "v_mfma_f32_16x16x32_bf16 v[184:187], a[136:139], v[16:19], 0\n"
        "v_exp_f32 v37, v37\n"
        "v_mfma_f32_16x16x32_bf16 v[216:219], a[136:139], v[20:23], 0\n"
        "ds_read_b128 a[120:123], v240 offset:22528\n"
        "v_cvt_pk_bf16_f32 v84, v36, v37\n"
        "v_mfma_f32_16x16x32_bf16 v[220:223], a[140:143], v[20:23], 0\n"
        "global_store_dwordx4 v254, a[24:27], s[70:71] offset:2048 sc1\n"
        "ds_read_b128 a[124:127], v240 offset:23552\n"
        "s_add_u32 s82, s82, 1\n"
        "v_exp_f32 v38, v38\n"
        "v_cndmask_b32 v33, v224, v242, s[12:13]\n"
        "v_mfma_f32_16x16x32_bf16 v[188:191], a[140:143], v[16:19], 0\n"
        "v_exp_f32 v39, v39\n"
        "ds_write_b128 v245, a[216:219] offset:0\n"
        "v_cvt_pk_bf16_f32 v85, v38, v39\n"
        "v_cndmask_b32 v34, v224, v242, s[14:15]\n"
        "v_cndmask_b32 v35, v224, v242, s[16:17]\n"
        "s_nop 1\n"
        "v_mfma_f32_16x16x32_bf16 v[32:35], a[96:99], v[0:3], v[32:35]\n"
        "v_exp_f32 v48, v48\n"
        "v_mfma_f32_16x16x32_bf16 v[32:35], a[100:103], v[4:7], v[32:35]\n"
        "v_exp_f32 v49, v49\n"
        "v_mfma_f32_16x16x32_bf16 v[32:35], a[104:107], v[8:11], v[32:35]\n"
        "ds_read_b128 a[96:99], v240 offset:24576\n"
        "v_cvt_pk_bf16_f32 v82, v48, v49\n"
        "v_mfma_f32_16x16x32_bf16 v[32:35], a[108:111], v[12:15], v[32:35]\n"
        "v_exp_f32 v50, v50\n"
        "v_exp_f32 v51, v51\n"
        "global_store_dwordx4 v254, a[28:31], s[70:71] offset:3072 sc1\n"
        "v_cvt_pk_bf16_f32 v83, v50, v51\n"
        "s_nop 1\n"
        "v_mfma_f32_16x16x32_bf16 v[160:163], a[144:147], v[80:83], v[160:163]\n"
        "ds_write_b128 v245, a[248:251] offset:1024\n"
        "v_mfma_f32_16x16x32_bf16 v[164:167], a[148:151], v[80:83], v[164:167]\n"
        "s_waitcnt lgkmcnt(2)\n"
        "v_exp_f32 v52, v52\n"
        "v_mfma_f32_16x16x32_bf16 v[168:171], a[152:155], v[80:83], v[168:171]\n"
        "v_exp_f32 v53, v53\n"
        "v_mfma_f32_16x16x32_bf16 v[172:175], a[156:159], v[80:83], v[172:175]\n"
        "ds_read_b128 a[132:135], v241 offset:13312\n"
        "v_cvt_pk_bf16_f32 v86, v52, v53\n"
        "v_mfma_f32_16x16x32_bf16 v[176:179], a[128:131], v[80:83], v[176:179]\n"
        "ds_read_b128 a[100:103], v240 offset:25600\n"
        "ds_read_b128 a[136:139], v241 offset:14336\n"
        "ds_read_b128 a[104:107], v240 offset:26624\n"
        "ds_read_b128 a[140:143], v241 offset:15360\n"
        "ds_write_b128 v245, a[220:223] offset:2048\n"
        "ds_read_b128 a[108:111], v240 offset:27648\n"
        "s_add_u32 s70, s92, 0x2\n"
        "s_addc_u32 s71, s93, 0\n"
        "s_cmp_ge_u32 s82, s44\n"
        "s_addc_u32 s90, s82, 0\n"
        "s_lshl_b32 s90, s90, 15\n"
        "v_exp_f32 v54, v54\n"
        "v_exp_f32 v55, v55\n"
        "s_waitcnt lgkmcnt(6)\n"
        "v_cvt_pk_bf16_f32 v87, v54, v55\n"
        "s_nop 1\n"
        "v_smfmac_f32_16x16x64_bf16 a[64:67], a[72:75], v[80:87], v154\n"
        "v_cndmask_b32 v48, v224, v242, s[20:21]\n"
        "v_mfma_f32_16x16x32_bf16 v[192:195], a[144:147], v[84:87], v[192:195]\n"
        "ds_read_b128 a[144:147], v241 offset:16384\n"
        "v_cndmask_b32 v49, v224, v242, s[22:23]\n"
        "v_mfma_f32_16x16x32_bf16 v[196:199], a[148:151], v[84:87], v[196:199]\n"
        "ds_read_b128 a[148:151], v241 offset:17408\n"
        "v_cndmask_b32 v50, v224, v242, s[24:25]\n"
        "v_mfma_f32_16x16x32_bf16 v[200:203], a[152:155], v[84:87], v[200:203]\n"
        "ds_read_b128 a[152:155], v241 offset:18432\n"
        "v_cndmask_b32 v51, v224, v242, s[28:29]\n"
        "s_nop 1\n"
        "v_mfma_f32_16x16x32_bf16 v[48:51], a[112:115], v[0:3], v[48:51]\n"
        "v_exp_f32 v32, v32\n"
        "v_mfma_f32_16x16x32_bf16 v[48:51], a[116:119], v[4:7], v[48:51]\n"
        "v_exp_f32 v33, v33\n"
        "v_mfma_f32_16x16x32_bf16 v[48:51], a[120:123], v[8:11], v[48:51]\n"
        "ds_read_b128 a[112:115], v240 offset:28672\n"
        "v_cvt_pk_bf16_f32 v16, v32, v33\n"
        "v_mfma_f32_16x16x32_bf16 v[48:51], a[124:127], v[12:15], v[48:51]\n"
        "s_waitcnt lgkmcnt(4)\n"
        "v_cvt_pk_bf16_f32 v109, v122, v123\n"
        "v_cvt_pk_bf16_f32 v110, v124, v125\n"
        "v_mfma_f32_16x16x32_bf16 v[204:207], a[156:159], v[84:87], v[204:207]\n"
        "ds_read_b128 a[116:119], v240 offset:29696\n"
        "v_cvt_pk_bf16_f32 v111, v126, v127\n"
        "v_mfma_f32_16x16x32_bf16 v[208:211], a[128:131], v[84:87], v[208:211]\n"
        "v_exp_f32 v34, v34\n"
        "v_mfma_f32_16x16x32_bf16 v[212:215], a[132:135], v[84:87], v[212:215]\n"
        "v_exp_f32 v35, v35\n"
        "v_mfma_f32_16x16x32_bf16 v[180:183], a[132:135], v[80:83], v[180:183]\n"
        "ds_read_b128 a[120:123], v240 offset:30720\n"
        "v_cvt_pk_bf16_f32 v17, v34, v35\n"
        "v_mfma_f32_16x16x32_bf16 v[184:187], a[136:139], v[80:83], v[184:187]\n"
        "ds_write_b128 v245, a[252:255] offset:3072\n"
        "v_mfma_f32_16x16x32_bf16 v[216:219], a[136:139], v[84:87], v[216:219]\n"
        "ds_read_b128 a[124:127], v240 offset:31744\n"
        "v_cndmask_b32 v32, v224, v242, s[30:31]\n"
        "v_mfma_f32_16x16x32_bf16 v[220:223], a[140:143], v[84:87], v[220:223]\n"
        "ds_read_b128 a[156:159], v241 offset:19456\n"
        "v_cndmask_b32 v33, v224, v242, s[56:57]\n"
        "v_mfma_f32_16x16x32_bf16 v[188:191], a[140:143], v[80:83], v[188:191]\n"
        "v_cndmask_b32 v34, v224, v242, s[58:59]\n"
        "v_cndmask_b32 v35, v224, v242, s[60:61]\n"
        "s_nop 1\n"
        "v_mfma_f32_16x16x32_bf16 v[32:35], a[96:99], v[0:3], v[32:35]\n"
        "v_exp_f32 v48, v48\n"
        "v_mfma_f32_16x16x32_bf16 v[32:35], a[100:103], v[4:7], v[32:35]\n"
        "v_exp_f32 v49, v49\n"
        "v_mfma_f32_16x16x32_bf16 v[32:35], a[104:107], v[8:11], v[32:35]\n"
        "s_waitcnt lgkmcnt(3)\n"
        "v_cvt_pk_bf16_f32 v18, v48, v49\n"
        "v_xor_b32 v240, 0x8000, v240\n"
        "v_mfma_f32_16x16x32_bf16 v[32:35], a[108:111], v[12:15], v[32:35]\n"
        "ds_write_b128 v245, a[208:211] offset:8192\n"
        "s_waitcnt lgkmcnt(2)\n"
        "s_barrier\n"
        "ds_read_b128 a[96:99], v240 offset:0\n"
        "ds_read_b128 a[128:131], v241 offset:20480\n"
        "v_exp_f32 v50, v50\n"
        "v_exp_f32 v51, v51\n"
        "v_cndmask_b32 v48, v224, v242, s[62:63]\n"
        "v_cvt_pk_bf16_f32 v19, v50, v51\n"
        "s_nop 1\n"
        "v_mfma_f32_16x16x32_bf16 a[64:67], a[76:79], v[16:19], a[64:67]\n"
        "ds_read_b128 a[132:135], v241 offset:21504\n"
        "v_cndmask_b32 v49, v224, v242, s[84:85]\n"
        "v_mfma_f32_16x16x32_bf16 v[160:163], a[144:147], v[16:19], v[160:163]\n"
        "ds_read_b128 a[100:103], v240 offset:1024\n"
        "v_cndmask_b32 v50, v224, v242, s[86:87]\n"
        "v_mfma_f32_16x16x32_bf16 v[164:167], a[148:151], v[16:19], v[164:167]\n"
        "ds_read_b128 a[136:139], v241 offset:22528\n"
        "v_cndmask_b32 v51, v224, v242, s[88:89]\n"
        "s_nop 1\n"
        "v_mfma_f32_16x16x32_bf16 v[48:51], a[112:115], v[0:3], v[48:51]\n"
        "ds_read_b128 a[112:115], v240 offset:4096\n"
        "v_cvt_pk_bf16_f32 v137, v146, v147\n"
        "v_mfma_f32_16x16x32_bf16 v[48:51], a[116:119], v[4:7], v[48:51]\n"
        "ds_read_b128 a[116:119], v240 offset:5120\n"
        "v_cvt_pk_bf16_f32 v138, v148, v149\n"
        "v_mfma_f32_16x16x32_bf16 v[48:51], a[120:123], v[8:11], v[48:51]\n"
        "ds_read_b128 a[120:123], v240 offset:6144\n"
        "v_cvt_pk_bf16_f32 v139, v150, v151\n"
        "v_mfma_f32_16x16x32_bf16 v[48:51], a[124:127], v[12:15], v[48:51]\n"
        "ds_read_b128 a[124:127], v240 offset:7168\n"
        "v_lshrrev_b32 v244, 1, v254\n"
        "v_mfma_f32_16x16x32_bf16 v[168:171], a[152:155], v[16:19], v[168:171]\n"
        "ds_read_b128 a[140:143], v241 offset:23552\n"
        "s_waitcnt lgkmcnt(8)\n"
        "v_mfma_f32_16x16x32_bf16 v[172:175], a[156:159], v[16:19], v[172:175]\n"
        "ds_read_b128 a[104:107], v240 offset:2048\n"
        "v_add_u32 v244, s81, v244\n"
        "v_mfma_f32_16x16x32_bf16 v[176:179], a[128:131], v[16:19], v[176:179]\n"
        "s_waitcnt lgkmcnt(7)\n"
        "v_exp_f32 v32, v32\n"
        "v_mfma_f32_16x16x32_bf16 v[180:183], a[132:135], v[16:19], v[180:183]\n"
        "s_waitcnt lgkmcnt(6)\n"
        "v_exp_f32 v33, v33\n"
        "v_mfma_f32_16x16x32_bf16 v[184:187], a[136:139], v[16:19], v[184:187]\n"
        "ds_write_b128 v245, a[240:243] offset:9216\n"
        "ds_read_b128 a[108:111], v240 offset:3072\n"
        "ds_read_b128 a[144:147], v241 offset:24576\n"
        "s_add_u32 s81, s78, 4096\n"
        "v_cvt_pk_bf16_f32 v80, v32, v33\n"
        "s_waitcnt lgkmcnt(5)\n"
        "v_mfma_f32_16x16x32_bf16 v[52:55], a[124:127], v[76:79], v[228:231]\n"
        "v_exp_f32 v34, v34\n"
        "v_mfma_f32_16x16x32_bf16 v[52:55], a[120:123], v[72:75], v[52:55]\n"
        "ds_write_b128 v245, a[212:215] offset:10240\n"
        "v_mfma_f32_16x16x32_bf16 v[52:55], a[116:119], v[68:71], v[52:55]\n"
        "v_exp_f32 v35, v35\n"
        "v_mfma_f32_16x16x32_bf16 v[52:55], a[112:115], v[64:67], v[52:55]\n"
        "s_waitcnt vmcnt(0)\n"
        "v_cvt_pk_bf16_f32 v81, v34, v35\n"
        "s_waitcnt lgkmcnt(4)\n"
        "v_mfma_f32_16x16x32_bf16 v[188:191], a[140:143], v[16:19], v[188:191]\n"
        "s_barrier\n"
        "v_mfma_f32_16x16x32_bf16 v[32:35], a[96:99], v[0:3], v[224:227]\n"
        "s_cmp_lg_u32 s40, 0\n"
        "s_cbranch_scc1 LW1_%=\n"
        "s_mov_b64 exec, 0xffff\n"
        "global_store_dwordx2 v244, v[250:251], s[48:49] sc1\n"
        "s_mov_b64 exec, -1\n"
        "LW1_%=:\n"
        "v_mfma_f32_16x16x32_bf16 v[32:35], a[100:103], v[4:7], v[32:35]\n"
        "ds_write_b128 v245, a[244:247] offset:11264\n"
        "v_mfma_f32_16x16x32_bf16 v[32:35], a[104:107], v[8:11], v[32:35]\n"
        "s_waitcnt lgkmcnt(1)\n"
        "v_bfe_u32 v245, v254, 4, 3\n"
        "v_lshl_add_u32 v245, v245, 7, s80\n"
        "v_mfma_f32_16x16x32_bf16 v[32:35], a[108:111], v[12:15], v[32:35]\n"
        "ds_read_b128 a[148:151], v241 offset:25600\n"
        "s_mov_b32 s80, s78\n"
        "v_cvt_pk_bf16_f32 v140, v152, v153\n"
        "v_mfma_f32_16x16x32_bf16 v[36:39], a[108:111], v[76:79], v[228:231]\n"
        "ds_read_b128 a[152:155], v241 offset:26624\n"
        "s_add_u32 m0, s80, 0x0\n"
        "v_cvt_pk_bf16_f32 v142, v156, v157\n"
        "v_mfma_f32_16x16x32_bf16 v[36:39], a[104:107], v[72:75], v[36:39]\n"
        "ds_read_b128 a[156:159], v241 offset:27648\n"
        "s_xor_b32 s78, s78, 0x8000\n"
        "v_cvt_pk_bf16_f32 v143, v158, v159\n"
        "v_mfma_f32_16x16x32_bf16 v[36:39], a[100:103], v[68:71], v[36:39]\n"
        "ds_read_b128 a[128:131], v241 offset:28672\n"
        "s_mov_b32 s80, s78\n"
        "v_cndmask_b32 v40, 0, v242, s[10:11]\n"
        "v_mfma_f32_16x16x32_bf16 v[36:39], a[96:99], v[64:67], v[36:39]\n"
        "ds_read_b128 a[132:135], v241 offset:29696\n"
        "ds_read_b128 a[136:139], v241 offset:30720\n"
        "ds_read_b128 a[140:143], v241 offset:31744\n"
        "v_exp_f32 v48, v48\n"
        "v_exp_f32 v49, v49\n"
        "v_exp_f32 v50, v50\n"
        "v_cvt_pk_bf16_f32 v82, v48, v49\n"
        "v_exp_f32 v51, v51\n"
        "v_xor_b32 v241, 0x8000, v241\n"
        "v_cvt_pk_bf16_f32 v83, v50, v51\n"
        "s_waitcnt lgkmcnt(6)\n"
        "s_nop 0\n"
        "v_mfma_f32_16x16x32_bf16 v[164:167], a[148:151], v[80:83], v[164:167]\n"
        "s_add_u32 s91, s44, 1\n"
        "v_cndmask_b32 v41, 0, v242, s[12:13]\n"
        "v_cndmask_b32 v42, 0, v242, s[14:15]\n"
        "v_mfma_f32_16x16x32_bf16 v[48:51], a[112:115], v[0:3], v[224:227]\n"
        "LHA10_%=:\n"
        "s_cbranch_execz LHRA_%=\n"
        "v_cndmask_b32 v43, 0, v242, s[16:17]\n"
        "s_nop 1\n"
        "v_mfma_f32_16x16x32_bf16 v[40:43], a[96:99], v[96:99], v[40:43]\n"
        "ds_read_b128 a[96:99], v240 offset:8192\n"
        "v_cndmask_b32 v56, 0, v242, s[20:21]\n"
        "v_mfma_f32_16x16x32_bf16 v[40:43], a[100:103], v[100:103], v[40:43]\n"
        "ds_read_b128 a[100:103], v240 offset:9216\n"
        "v_cndmask_b32 v57, 0, v242, s[22:23]\n"
        "v_mfma_f32_16x16x32_bf16 v[40:43], a[104:107], v[104:107], v[40:43]\n"
        "s_waitcnt lgkmcnt(2)\n"
        "v_exp_f32 v32, v32\n"
        "v_mfma_f32_16x16x32_bf16 v[40:43], a[108:111], v[108:111], v[40:43]\n"
        "s_barrier\n"
        "v_mfma_f32_16x16x32_bf16 v[48:51], a[116:119], v[4:7], v[48:51]\n"
        "ds_read_b128 a[148:151], v241 offset:1024\n"
        "v_cndmask_b32 v58, 0, v242, s[24:25]\n"
        "v_mfma_f32_16x16x32_bf16 v[48:51], a[120:123], v[8:11], v[48:51]\n"
        "v_exp_f32 v33, v33\n"
        "v_mfma_f32_16x16x32_bf16 v[48:51], a[124:127], v[12:15], v[48:51]\n"
        "v_exp_f32 v34, v34\n"
        "v_mfma_f32_16x16x32_bf16 a[64:67], a[76:79], v[80:83], a[64:67]\n"
        "ds_read_b128 a[104:107], v240 offset:10240\n"
        "v_max_f32 v246, v40, v41\n"
        "v_mfma_f32_16x16x32_bf16 v[160:163], a[144:147], v[80:83], v[160:163]\n"
        "ds_read_b128 a[108:111], v240 offset:11264\n"
        "v_max3_f32 v246, v42, v43, v246\n"
        "v_mfma_f32_16x16x32_bf16 v[168:171], a[152:155], v[80:83], v[168:171]\n"
        "ds_read_b128 a[144:147], v241 offset:0\n"
        "v_mov_b32 v247, v246\n"
        "v_mfma_f32_16x16x32_bf16 v[172:175], a[156:159], v[80:83], v[172:175]\n"
        "v_exp_f32 v35, v35\n"
        "v_mfma_f32_16x16x32_bf16 v[176:179], a[128:131], v[80:83], v[176:179]\n"
        "v_permlane16_swap_b32 v246, v247\n"
        "v_mfma_f32_16x16x32_bf16 v[180:183], a[132:135], v[80:83], v[180:183]\n"
        "s_waitcnt lgkmcnt(3)\n"
        "v_max_f32 v246, v246, v247\n"
        "v_mov_b32 v247, v246\n"
        "v_mfma_f32_16x16x32_bf16 v[184:187], a[136:139], v[80:83], v[184:187]\n"
        "v_exp_f32 v48, v48\n"
        "v_mfma_f32_16x16x32_bf16 v[188:191], a[140:143], v[80:83], v[188:191]\n"
        "v_permlane32_swap_b32 v246, v247\n"
        "v_max_f32 v246, v246, v247\n"
        "v_sub_f32 v232, 0, v246\n"
        "v_sub_f32 v233, 0, v246\n"
        "v_sub_f32 v234, 0, v246\n"
        "v_sub_f32 v235, 0, v246\n"
        "v_exp_f32 v49, v49\n"
        "v_exp_f32 v50, v50\n"
        "v_exp_f32 v51, v51\n"
        "v_cvt_pk_bf16_f32 v16, v32, v33\n"
        "v_cvt_pk_bf16_f32 v17, v34, v35\n"
        "v_mfma_f32_16x16x32_bf16 v[32:35], a[96:99], v[0:3], v[224:227]\n"
        "ds_read_b128 a[152:155], v241 offset:2048\n"
        "v_cvt_pk_bf16_f32 v18, v48, v49\n"
        "v_mfma_f32_16x16x32_bf16 v[32:35], a[100:103], v[4:7], v[32:35]\n"
        "s_waitcnt lgkmcnt(1)\n"
        "v_cvt_pk_bf16_f32 v19, v50, v51\n"
        "s_nop 1\n"
        "v_mfma_f32_16x16x32_bf16 v[164:167], a[148:151], v[16:19], v[164:167]\n"
        "global_load_dwordx2 v[252:253], v245, s[48:49] sc1\n"
        "v_cndmask_b32 v59, 0, v242, s[28:29]\n"
        "s_nop 1\n"
        "v_mfma_f32_16x16x32_bf16 v[56:59], a[112:115], v[96:99], v[56:59]\n"
        "v_exp_f32 v36, v36\n"
        "v_mfma_f32_16x16x32_bf16 v[56:59], a[116:119], v[100:103], v[56:59]\n"
        "v_exp_f32 v37, v37\n"
        "v_mfma_f32_16x16x32_bf16 v[56:59], a[120:123], v[104:107], v[56:59]\n"
        "ds_read_b128 a[112:115], v240 offset:12288\n"
        "v_cvt_pk_bf16_f32 v20, v36, v37\n"
        "v_mfma_f32_16x16x32_bf16 v[56:59], a[124:127], v[108:111], v[56:59]\n"
        "v_exp_f32 v38, v38\n"
        "v_mfma_f32_16x16x32_bf16 v[32:35], a[104:107], v[8:11], v[32:35]\n"
        "v_exp_f32 v39, v39\n"
        "v_mfma_f32_16x16x32_bf16 v[32:35], a[108:111], v[12:15], v[32:35]\n"
        "ds_read_b128 a[116:119], v240 offset:13312\n"
        "v_cvt_pk_bf16_f32 v21, v38, v39\n"
        "v_mfma_f32_16x16x32_bf16 v[36:39], a[108:111], v[76:79], v[228:231]\n"
        "ds_read_b128 a[120:123], v240 offset:14336\n"
        "v_add_f32 v40, v40, v232\n"
        "v_mfma_f32_16x16x32_bf16 v[36:39], a[104:107], v[72:75], v[36:39]\n"
        "v_exp_f32 v40, v40\n"
        "v_mfma_f32_16x16x32_bf16 v[36:39], a[100:103], v[68:71], v[36:39]\n"
        "ds_read_b128 a[156:159], v241 offset:3072\n"
        "v_add_f32 v41, v41, v232\n"
        "v_mfma_f32_16x16x32_bf16 v[36:39], a[96:99], v[64:67], v[36:39]\n"
        "v_exp_f32 v41, v41\n"
        "v_mfma_f32_16x16x32_bf16 v[160:163], a[144:147], v[16:19], v[160:163]\n"
        "s_waitcnt lgkmcnt(3)\n"
        "v_cvt_pk_bf16_f32 v24, v40, v41\n"
        "v_add_f32 v42, v42, v232\n"
        "v_mfma_f32_16x16x32_bf16 v[48:51], a[112:115], v[0:3], v[224:227]\n"
        "v_exp_f32 v42, v42\n"
        "v_mfma_f32_16x16x32_bf16 v[168:171], a[152:155], v[16:19], v[168:171]\n"
        "ds_read_b128 a[124:127], v240 offset:15360\n"
        "v_add_f32 v43, v43, v232\n"
        "s_waitcnt lgkmcnt(3)\n"
        "v_mfma_f32_16x16x32_bf16 v[48:51], a[116:119], v[4:7], v[48:51]\n"
        "s_waitcnt lgkmcnt(2)\n"
        "v_exp_f32 v43, v43\n"
        "v_mfma_f32_16x16x32_bf16 v[48:51], a[120:123], v[8:11], v[48:51]\n"
        "ds_read_b128 a[128:131], v241 offset:4096\n"
        "ds_read_b128 a[132:135], v241 offset:5120\n"
        "v_cvt_pk_bf16_f32 v25, v42, v43\n"
        "v_cndmask_b32 v40, v232, v242, s[30:31]\n"
        "s_waitcnt lgkmcnt(3)\n"
        "v_mfma_f32_16x16x32_bf16 v[172:175], a[156:159], v[16:19], v[172:175]\n"
        "s_mov_b64 exec, 0\n"
        "s_cmp_eq_u32 s40, 0\n"
        "s_cbranch_scc1 LHC0_%=\n"
        "s_cmp_eq_u32 s40, 1\n"
        "s_cbranch_scc1 LHC1_%=\n"
        "s_cmp_eq_u32 s40, 2\n"
        "s_cbranch_scc1 LHC2_%=\n"
        "s_branch LHC3_%=\n"
        "LHRC_%=:\n"
        "s_mov_b64 exec, -1\n"
        "ds_read_b128 a[136:139], v241 offset:6144\n"
        "ds_read_b128 a[140:143], v241 offset:7168\n"
        "v_exp_f32 v52, v52\n"
        "v_exp_f32 v53, v53\n"
        "s_waitcnt lgkmcnt(4)\n"
        "v_cvt_pk_bf16_f32 v22, v52, v53\n"
        "v_mfma_f32_16x16x32_bf16 v[48:51], a[124:127], v[12:15], v[48:51]\n"
        "v_cndmask_b32 v41, v232, v242, s[56:57]\n"
        "v_cndmask_b32 v42, v232, v242, s[58:59]\n"
        "v_cndmask_b32 v43, v232, v242, s[60:61]\n"
        "s_nop 1\n"
        "v_mfma_f32_16x16x32_bf16 v[40:43], a[96:99], v[96:99], v[40:43]\n"
        "ds_read_b128 a[96:99], v240 offset:16384\n"
        "v_add_f32 v56, v56, v232\n"
        "v_mfma_f32_16x16x32_bf16 v[40:43], a[100:103], v[100:103], v[40:43]\n"
        "v_exp_f32 v54, v54\n"
        "v_mfma_f32_16x16x32_bf16 v[40:43], a[104:107], v[104:107], v[40:43]\n"
        "s_waitcnt vmcnt(0) lgkmcnt(3)\n"
        "v_cmp_ne_u64 vcc, v[252:253], v[250:251]\n"
        "v_add_f32 v57, v57, v232\n"
        "v_mfma_f32_16x16x32_bf16 v[40:43], a[108:111], v[108:111], v[40:43]\n"
        "s_cbranch_vccnz LPKV_%=\n"
        "LBKV_%=:\n"
        "v_exp_f32 v55, v55\n"
        "v_mfma_f32_16x16x32_bf16 v[176:179], a[128:131], v[16:19], v[176:179]\n"
        "ds_read_b128 a[100:103], v240 offset:17408\n"
        "v_cvt_pk_bf16_f32 v23, v54, v55\n"
        "v_mfma_f32_16x16x32_bf16 v[52:55], a[124:127], v[76:79], v[228:231]\n"
        "v_exp_f32 v56, v56\n"
        "v_mfma_f32_16x16x32_bf16 v[52:55], a[120:123], v[72:75], v[52:55]\n"
        "ds_read_b128 a[104:107], v240 offset:18432\n"
        "v_mov_b32 v144, v254\n"
        "v_mfma_f32_16x16x32_bf16 v[52:55], a[116:119], v[68:71], v[52:55]\n"
        "v_exp_f32 v57, v57\n"
        "v_mfma_f32_16x16x32_bf16 v[52:55], a[112:115], v[64:67], v[52:55]\n"
        "ds_read_b128 a[108:111], v240 offset:19456\n"
        "v_mov_b32 v145, v254\n"
        "v_smfmac_f32_16x16x64_bf16 a[64:67], a[72:75], v[16:23], v154\n"
        "global_load_lds_dwordx4 v254, s[68:69] offset:0 sc1\n"
        "v_cvt_pk_bf16_f32 v26, v56, v57\n"
        "v_mfma_f32_16x16x32_bf16 v[192:195], a[144:147], v[20:23], v[192:195]\n"
        "v_mov_b32 v146, v254\n"
        "v_mov_b32 v147, v254\n"
        "v_mfma_f32_16x16x32_bf16 v[196:199], a[148:151], v[20:23], v[196:199]\n"
        "v_accvgpr_read_b32 v148, a88\n"
        "v_accvgpr_read_b32 v152, a89\n"
        "v_mfma_f32_16x16x32_bf16 v[200:203], a[152:155], v[20:23], v[200:203]\n"
        "v_accvgpr_read_b32 v156, a90\n"
        "v_accvgpr_read_b32 v153, a92\n"
        "v_mfma_f32_16x16x32_bf16 v[204:207], a[156:159], v[20:23], v[204:207]\n"
        "global_load_lds_dwordx4 v254, s[68:69] offset:1024 sc1\n"
        "v_accvgpr_read_b32 v155, a94\n"
        "v_mfma_f32_16x16x32_bf16 v[208:211], a[128:131], v[20:23], v[208:211]\n"
        "v_add_u32 v149, 1024, v148\n"
        "v_accvgpr_write_b32 a88, 0\n"
        "v_mfma_f32_16x16x32_bf16 v[212:215], a[132:135], v[20:23], v[212:215]\n"
        "v_add_u32 v150, 2048, v148\n"
        "v_accvgpr_write_b32 a89, 0\n"
        "v_mfma_f32_16x16x32_bf16 v[180:183], a[132:135], v[16:19], v[180:183]\n"
        "s_waitcnt lgkmcnt(0)\n"
        "v_add_u32 v151, 3072, v148\n"
        "v_accvgpr_write_b32 a90, 0\n"
        "v_mfma_f32_16x16x32_bf16 v[184:187], a[136:139], v[16:19], v[184:187]\n"
        "global_load_lds_dwordx4 v254, s[68:69] offset:2048 sc1\n"
        "v_add_f32 v58, v58, v232\n"
        "v_mfma_f32_16x16x32_bf16 v[216:219], a[136:139], v[20:23], v[216:219]\n"
        "v_exp_f32 v58, v58\n"
        "v_mfma_f32_16x16x32_bf16 v[220:223], a[140:143], v[20:23], v[220:223]\n"
        "v_add_f32 v59, v59, v232\n"
        "v_cndmask_b32 v56, v232, v242, s[62:63]\n"
        "v_mfma_f32_16x16x32_bf16 v[188:191], a[140:143], v[16:19], v[188:191]\n"
        "v_exp_f32 v59, v59\n"
        "v_exp_f32 v32, v32\n"
        "v_cvt_pk_bf16_f32 v27, v58, v59\n"
        "s_nop 1\n"
        "v_mfma_f32_16x16x32_bf16 a[68:71], a[76:79], v[24:27], a[68:71]\n"
        "v_exp_f32 v33, v33\n"
        "v_mfma_f32_16x16x32_bf16 a[0:3], a[144:147], v[24:27], 0\n"
        "ds_read_b128 a[144:147], v241 offset:8192\n"
        "v_cvt_pk_bf16_f32 v80, v32, v33\n"
        "v_mfma_f32_16x16x32_bf16 a[4:7], a[148:151], v[24:27], 0\n"
        "v_exp_f32 v34, v34\n"
        "v_mfma_f32_16x16x32_bf16 a[8:11], a[152:155], v[24:27], 0\n"
        "v_exp_f32 v35, v35\n"
        "v_mfma_f32_16x16x32_bf16 a[12:15], a[156:159], v[24:27], 0\n"
        "ds_read_b128 a[148:151], v241 offset:9216\n"
        "v_cvt_pk_bf16_f32 v81, v34, v35\n"
        "v_mfma_f32_16x16x32_bf16 v[32:35], a[96:99], v[0:3], v[224:227]\n"
        "ds_read_b128 a[152:155], v241 offset:10240\n"
        "v_cndmask_b32 v57, v232, v242, s[84:85]\n"
        "v_mfma_f32_16x16x32_bf16 v[32:35], a[100:103], v[4:7], v[32:35]\n"
        "ds_read_b128 a[156:159], v241 offset:11264\n"
        "v_cndmask_b32 v58, v232, v242, s[86:87]\n"
        "v_mfma_f32_16x16x32_bf16 v[32:35], a[104:107], v[8:11], v[32:35]\n"
        "global_load_lds_dwordx4 v254, s[68:69] offset:3072 sc1\n"
        "s_add_u32 m0, s81, 0x0\n"
        "v_cndmask_b32 v59, v232, v242, s[88:89]\n"
        "s_nop 1\n"
        "v_mfma_f32_16x16x32_bf16 v[56:59], a[112:115], v[96:99], v[56:59]\n"
        "s_add_u32 s81, s78, 4096\n"
        "v_exp_f32 v36, v36\n"
        "v_mfma_f32_16x16x32_bf16 v[56:59], a[116:119], v[100:103], v[56:59]\n"
        "v_exp_f32 v37, v37\n"
        "v_mfma_f32_16x16x32_bf16 v[56:59], a[120:123], v[104:107], v[56:59]\n"
        "ds_read_b128 a[112:115], v240 offset:20480\n"
        "v_cvt_pk_bf16_f32 v84, v36, v37\n"
        "v_mfma_f32_16x16x32_bf16 v[56:59], a[124:127], v[108:111], v[56:59]\n"
        "v_exp_f32 v38, v38\n"
        "v_mfma_f32_16x16x32_bf16 v[32:35], a[108:111], v[12:15], v[32:35]\n"
        "v_exp_f32 v39, v39\n"
        "v_mfma_f32_16x16x32_bf16 a[16:19], a[128:131], v[24:27], 0\n"
        "ds_read_b128 a[116:119], v240 offset:21504\n"
        "v_cvt_pk_bf16_f32 v85, v38, v39\n"
        "v_mfma_f32_16x16x32_bf16 v[36:39], a[108:111], v[76:79], v[228:231]\n"
        "v_exp_f32 v40, v40\n"
        "v_mfma_f32_16x16x32_bf16 v[36:39], a[104:107], v[72:75], v[36:39]\n"
        "v_exp_f32 v41, v41\n"
        "v_mfma_f32_16x16x32_bf16 v[36:39], a[100:103], v[68:71], v[36:39]\n"
        "ds_read_b128 a[120:123], v240 offset:22528\n"
        "v_cvt_pk_bf16_f32 v88, v40, v41\n"
        "v_mfma_f32_16x16x32_bf16 v[36:39], a[96:99], v[64:67], v[36:39]\n"
        "ds_read_b128 a[124:127], v240 offset:23552\n"
        "v_and_b32 v245, 0x70, v254\n"
        "v_mfma_f32_16x16x32_bf16 a[20:23], a[132:135], v[24:27], 0\n"
        "global_load_lds_dwordx4 v254, s[74:75] offset:0 sc1\n"
        "v_lshlrev_b32 v245, 3, v245\n"
        "v_mfma_f32_16x16x32_bf16 a[24:27], a[136:139], v[24:27], 0\n"
        "v_exp_f32 v42, v42\n"
        "v_mfma_f32_16x16x32_bf16 a[28:31], a[140:143], v[24:27], 0\n"
        "v_exp_f32 v43, v43\n"
        "v_exp_f32 v48, v48\n"
        "v_cvt_pk_bf16_f32 v89, v42, v43\n"
        "v_mfma_f32_16x16x32_bf16 v[40:43], a[96:99], v[96:99], v[232:235]\n"
        "v_exp_f32 v49, v49\n"
        "v_mfma_f32_16x16x32_bf16 v[40:43], a[100:103], v[100:103], v[40:43]\n"
        "global_load_lds_dwordx4 v254, s[74:75] offset:1024 sc1\n"
        "v_cvt_pk_bf16_f32 v82, v48, v49\n"
        "v_mfma_f32_16x16x32_bf16 v[40:43], a[104:107], v[104:107], v[40:43]\n"
        "v_exp_f32 v50, v50\n"
        "v_mfma_f32_16x16x32_bf16 v[40:43], a[108:111], v[108:111], v[40:43]\n"
        "v_exp_f32 v51, v51\n"
        "v_exp_f32 v52, v52\n"
        "v_cvt_pk_bf16_f32 v83, v50, v51\n"
        "s_waitcnt lgkmcnt(0)\n"
        "v_mfma_f32_16x16x32_bf16 v[48:51], a[112:115], v[0:3], v[224:227]\n"
        "v_exp_f32 v53, v53\n"
        "v_mfma_f32_16x16x32_bf16 v[48:51], a[116:119], v[4:7], v[48:51]\n"
        "global_load_lds_dwordx4 v254, s[74:75] offset:2048 sc1\n"
        "v_cvt_pk_bf16_f32 v86, v52, v53\n"
        "v_mfma_f32_16x16x32_bf16 v[48:51], a[120:123], v[8:11], v[48:51]\n"
        "ds_read_b128 a[128:131], v241 offset:12288\n"
        "v_cndmask_b32 v44, 0, v242, s[10:11]\n"
        "v_mfma_f32_16x16x32_bf16 v[48:51], a[124:127], v[12:15], v[48:51]\n"
        "ds_read_b128 a[132:135], v241 offset:13312\n"
        "s_lshr_b32 s10, s41, 1\n"
        "v_cndmask_b32 v45, 0, v242, s[12:13]\n"
        "v_mfma_f32_16x16x32_bf16 v[160:163], a[144:147], v[80:83], v[160:163]\n"
        "ds_read_b128 a[136:139], v241 offset:14336\n"
        "s_and_b32 s11, s42, 7\n"
        "v_cndmask_b32 v46, 0, v242, s[14:15]\n"
        "v_mfma_f32_16x16x32_bf16 v[164:167], a[148:151], v[80:83], v[164:167]\n"
        "global_load_lds_dwordx4 v254, s[74:75] offset:3072 sc1\n"
        "s_add_u32 m0, s79, 0x0\n"
        "v_cndmask_b32 v47, 0, v242, s[16:17]\n"
        "s_nop 1\n"
        "v_mfma_f32_16x16x32_bf16 v[44:47], a[108:111], v[140:143], v[44:47]\n"
        "s_and_b32 s12, s41, 1\n"
        "v_exp_f32 v54, v54\n"
        "v_mfma_f32_16x16x32_bf16 v[44:47], a[104:107], v[136:139], v[44:47]\n"
        "s_lshr_b32 s14, s42, 3\n"
        "v_exp_f32 v55, v55\n"
        "v_mfma_f32_16x16x32_bf16 v[44:47], a[100:103], v[132:135], v[44:47]\n"
        "ds_read_b128 a[100:103], v240 offset:25600\n"
        "s_lshl2_add_u32 s12, s12, s14\n"
        "v_cvt_pk_bf16_f32 v87, v54, v55\n"
        "v_mfma_f32_16x16x32_bf16 v[44:47], a[96:99], v[128:131], v[44:47]\n"
        "ds_read_b128 a[96:99], v240 offset:24576\n"
        "global_load_lds_dwordx4 v254, s[76:77] offset:0 sc1\n"
        "s_lshl3_add_u32 s13, s12, s11\n"
        "v_mfma_f32_16x16x32_bf16 v[52:55], a[124:127], v[76:79], v[228:231]\n"
        "s_lshl_b32 s14, s10, 21\n"
        "v_exp_f32 v56, v56\n"
        "v_mfma_f32_16x16x32_bf16 v[52:55], a[120:123], v[72:75], v[52:55]\n"
        "s_lshl_b32 s15, s40, 15\n"
        "v_exp_f32 v57, v57\n"
        "v_mfma_f32_16x16x32_bf16 v[52:55], a[116:119], v[68:71], v[52:55]\n"
        "ds_read_b128 a[104:107], v240 offset:26624\n"
        "s_add_u32 s14, s14, s15\n"
        "v_cvt_pk_bf16_f32 v90, v56, v57\n"
        "v_mfma_f32_16x16x32_bf16 v[52:55], a[112:115], v[64:67], v[52:55]\n"
        "s_lshl_b32 s15, s11, 8\n"
        "v_exp_f32 v58, v58\n"
        "v_smfmac_f32_16x16x64_bf16 a[64:67], a[72:75], v[80:87], v154\n"
        "ds_read_b128 a[140:143], v241 offset:15360\n"
        "global_load_lds_dwordx4 v254, s[76:77] offset:1024 sc1\n"
        "s_add_u32 s14, s14, s15\n"
        "v_mfma_f32_16x16x32_bf16 v[192:195], a[144:147], v[84:87], v[192:195]\n"
        "s_add_u32 s17, s11, s12\n"
        "v_exp_f32 v59, v59\n"
        "v_mfma_f32_16x16x32_bf16 v[196:199], a[148:151], v[84:87], v[196:199]\n"
        "ds_read_b128 a[108:111], v240 offset:27648\n"
        "s_and_b32 s17, s17, 7\n"
        "v_cvt_pk_bf16_f32 v91, v58, v59\n"
        "v_mfma_f32_16x16x32_bf16 v[56:59], a[112:115], v[96:99], v[232:235]\n"
        "v_exp_f32 v32, v32\n"
        "v_mfma_f32_16x16x32_bf16 v[56:59], a[116:119], v[100:103], v[56:59]\n"
        "v_exp_f32 v33, v33\n"
        "v_mfma_f32_16x16x32_bf16 v[56:59], a[120:123], v[104:107], v[56:59]\n"
        "global_load_lds_dwordx4 v254, s[76:77] offset:2048 sc1\n"
        "v_cvt_pk_bf16_f32 v16, v32, v33\n"
        "v_mfma_f32_16x16x32_bf16 v[56:59], a[124:127], v[108:111], v[56:59]\n"
        "v_exp_f32 v34, v34\n"
        "v_mfma_f32_16x16x32_bf16 a[68:71], a[76:79], v[88:91], a[68:71]\n"
        "v_exp_f32 v35, v35\n"
        "v_mfma_f32_16x16x32_bf16 a[0:3], a[144:147], v[88:91], a[0:3]\n"
        "ds_read_b128 a[144:147], v241 offset:16384\n"
        "v_cvt_pk_bf16_f32 v17, v34, v35\n"
        "v_mfma_f32_16x16x32_bf16 a[4:7], a[148:151], v[88:91], a[4:7]\n"
        "v_exp_f32 v36, v36\n"
        "v_mfma_f32_16x16x32_bf16 v[168:171], a[152:155], v[80:83], v[168:171]\n"
        "v_exp_f32 v37, v37\n"
        "v_mfma_f32_16x16x32_bf16 v[200:203], a[152:155], v[84:87], v[200:203]\n"
        "global_load_lds_dwordx4 v254, s[76:77] offset:3072 sc1\n"
        "s_add_u32 m0, s79, 0x1000\n"
        "v_max_f32 v248, v44, v45\n"
        "v_mfma_f32_16x16x32_bf16 a[8:11], a[152:155], v[88:91], a[8:11]\n"
        "ds_read_b128 a[148:151], v241 offset:17408\n"
        "v_cvt_pk_bf16_f32 v20, v36, v37\n"
        "v_mfma_f32_16x16x32_bf16 a[12:15], a[156:159], v[88:91], a[12:15]\n"
        "ds_read_b128 a[152:155], v241 offset:18432\n"
        "v_max3_f32 v248, v46, v47, v248\n"
        "v_mfma_f32_16x16x32_bf16 v[204:207], a[156:159], v[84:87], v[204:207]\n"
        "s_add_u32 s76, s76, 0x1000\n"
        "s_addc_u32 s77, s77, 0\n"
        "v_mov_b32 v249, v248\n"
        "v_mfma_f32_16x16x32_bf16 v[172:175], a[156:159], v[80:83], v[172:175]\n"
        "s_waitcnt lgkmcnt(3)\n"
        "v_permlane16_swap_b32 v248, v249\n"
        "v_mfma_f32_16x16x32_bf16 v[32:35], a[96:99], v[0:3], v[224:227]\n"
        "global_load_lds_dwordx4 v254, s[76:77] offset:0 sc1\n"
        "v_max_f32 v248, v248, v249\n"
        "v_mfma_f32_16x16x32_bf16 v[32:35], a[100:103], v[4:7], v[32:35]\n"
        "ds_read_b128 a[156:159], v241 offset:19456\n"
        "v_mov_b32 v249, v248\n"
        "v_mfma_f32_16x16x32_bf16 v[32:35], a[104:107], v[8:11], v[32:35]\n"
        "v_exp_f32 v38, v38\n"
        "v_mfma_f32_16x16x32_bf16 v[32:35], a[108:111], v[12:15], v[32:35]\n"
        "v_permlane32_swap_b32 v248, v249\n"
        "v_mfma_f32_16x16x32_bf16 v[176:179], a[128:131], v[80:83], v[176:179]\n"
        "global_load_lds_dwordx4 v254, s[76:77] offset:1024 sc1\n"
        "v_max_f32 v248, v248, v249\n"
        "v_mfma_f32_16x16x32_bf16 v[208:211], a[128:131], v[84:87], v[208:211]\n"
        "s_add_u32 s68, s64, s90\n"
        "s_addc_u32 s69, s65, 0\n"
        "v_sub_f32 v236, 0, v248\n"
        "v_mfma_f32_16x16x32_bf16 a[16:19], a[128:131], v[88:91], a[16:19]\n"
        "ds_read_b128 a[128:131], v241 offset:20480\n"
        "v_sub_f32 v237, 0, v248\n"
        "v_mfma_f32_16x16x32_bf16 a[20:23], a[132:135], v[88:91], a[20:23]\n"
        "s_add_u32 s74, s68, 0x1000\n"
        "s_addc_u32 s75, s69, 0\n"
        "v_sub_f32 v238, 0, v248\n"
        "v_mfma_f32_16x16x32_bf16 v[212:215], a[132:135], v[84:87], v[212:215]\n"
        "global_load_lds_dwordx4 v254, s[76:77] offset:2048 sc1\n"
        "v_sub_f32 v239, 0, v248\n"
        "v_mfma_f32_16x16x32_bf16 v[180:183], a[132:135], v[80:83], v[180:183]\n"
        "v_exp_f32 v39, v39\n"
        "v_mfma_f32_16x16x32_bf16 v[184:187], a[136:139], v[80:83], v[184:187]\n"
        "ds_read_b128 a[132:135], v241 offset:21504\n"
        "v_cvt_pk_bf16_f32 v21, v38, v39\n"
        "v_mfma_f32_16x16x32_bf16 v[36:39], a[108:111], v[76:79], v[228:231]\n"
        "v_cndmask_b32 v60, 0, v242, s[20:21]\n"
        "v_cndmask_b32 v61, 0, v242, s[22:23]\n"
        "v_mfma_f32_16x16x32_bf16 v[36:39], a[104:107], v[72:75], v[36:39]\n"
        "global_load_lds_dwordx4 v254, s[76:77] offset:3072 sc1\n"
        "s_add_u32 m0, s96, 0x20260\n"
        "v_cndmask_b32 v62, 0, v242, s[24:25]\n"
        "v_mfma_f32_16x16x32_bf16 v[36:39], a[100:103], v[68:71], v[36:39]\n"
        "s_sub_u32 s24, s83, 1\n"
        "v_cndmask_b32 v63, 0, v242, s[28:29]\n"
        "s_nop 1\n"
        "v_mfma_f32_16x16x32_bf16 v[60:63], a[124:127], v[140:143], v[60:63]\n"
        "s_min_u32 s24, s24, s91\n"
        "v_exp_f32 v40, v40\n"
        "v_mfma_f32_16x16x32_bf16 v[60:63], a[120:123], v[136:139], v[60:63]\n"
        "s_mov_b32 s25, 4096\n"
        "v_exp_f32 v41, v41\n"
        "v_mfma_f32_16x16x32_bf16 v[60:63], a[116:119], v[132:135], v[60:63]\n"
        "ds_read_b128 a[116:119], v240 offset:29696\n"
        "s_mov_b32 s28, 4096\n"
        "v_cvt_pk_bf16_f32 v24, v40, v41\n"
        "v_mfma_f32_16x16x32_bf16 v[60:63], a[112:115], v[128:131], v[60:63]\n"
        "ds_read_b128 a[112:115], v240 offset:28672\n"
        "s_lshl_b32 s29, s17, 18\n"
        "v_add_f32 v44, v44, v236\n"
        "v_mfma_f32_16x16x32_bf16 v[36:39], a[96:99], v[64:67], v[36:39]\n"
        "v_exp_f32 v42, v42\n"
        "v_mfma_f32_16x16x32_bf16 v[216:219], a[136:139], v[84:87], v[216:219]\n"
        "v_exp_f32 v43, v43\n"
        "v_mfma_f32_16x16x32_bf16 a[24:27], a[136:139], v[88:91], a[24:27]\n"
        "ds_read_b128 a[120:123], v240 offset:30720\n"
        "v_cvt_pk_bf16_f32 v25, v42, v43\n"
        "v_mfma_f32_16x16x32_bf16 v[40:43], a[96:99], v[96:99], v[232:235]\n"
        "v_exp_f32 v44, v44\n"
        "v_mfma_f32_16x16x32_bf16 v[40:43], a[100:103], v[100:103], v[40:43]\n"
        "global_load_lds_dwordx4 v254, s[70:71] offset:0\n"
        "v_add_f32 v45, v45, v236\n"
        "v_mfma_f32_16x16x32_bf16 v[40:43], a[104:107], v[104:107], v[40:43]\n"
        "v_exp_f32 v45, v45\n"
        "v_mfma_f32_16x16x32_bf16 v[40:43], a[108:111], v[108:111], v[40:43]\n"
        "ds_read_b128 a[124:127], v240 offset:31744\n"
        "v_cvt_pk_bf16_f32 v28, v44, v45\n"
        "v_mfma_f32_16x16x32_bf16 a[28:31], a[140:143], v[88:91], a[28:31]\n"
        "ds_read_b128 a[136:139], v241 offset:22528\n"
        "v_add_f32 v46, v46, v236\n"
        "v_mfma_f32_16x16x32_bf16 v[220:223], a[140:143], v[84:87], v[220:223]\n"
        "v_exp_f32 v46, v46\n"
        "v_mfma_f32_16x16x32_bf16 v[188:191], a[140:143], v[80:83], v[188:191]\n"
        "v_add_f32 v47, v47, v236\n"
        "v_exp_f32 v47, v47\n"
        "v_exp_f32 v48, v48\n"
        "v_cvt_pk_bf16_f32 v29, v46, v47\n"
        "v_exp_f32 v49, v49\n"
        "global_load_lds_dwordx4 v254, s[70:71] offset:1024\n"
        "v_cvt_pk_bf16_f32 v18, v48, v49\n"
        "v_exp_f32 v50, v50\n"
        "v_exp_f32 v51, v51\n"
        "v_exp_f32 v52, v52\n"
        "v_cvt_pk_bf16_f32 v19, v50, v51\n"
        "s_waitcnt lgkmcnt(2)\n"
        "v_mfma_f32_16x16x32_bf16 v[48:51], a[112:115], v[0:3], v[224:227]\n"
        "s_waitcnt vmcnt(10) lgkmcnt(1)\n"
        "v_exp_f32 v53, v53\n"
        "v_mfma_f32_16x16x32_bf16 v[48:51], a[116:119], v[4:7], v[48:51]\n"
        "global_load_lds_dwordx4 v254, s[70:71] offset:2048\n"
        "v_cvt_pk_bf16_f32 v22, v52, v53\n"
        "v_mfma_f32_16x16x32_bf16 v[48:51], a[120:123], v[8:11], v[48:51]\n"
        "v_exp_f32 v54, v54\n"
        "v_mfma_f32_16x16x32_bf16 v[48:51], a[124:127], v[12:15], v[48:51]\n"
        "v_exp_f32 v55, v55\n"
        "v_mfma_f32_16x16x32_bf16 v[160:163], a[144:147], v[16:19], v[160:163]\n"
        "ds_read_b128 a[140:143], v241 offset:23552\n"
        "v_cvt_pk_bf16_f32 v23, v54, v55\n"
        "v_mfma_f32_16x16x32_bf16 v[52:55], a[124:127], v[76:79], v[228:231]\n"
        "global_load_dword v244, v245, s[26:27] offset:0\n"
        "v_cndmask_b32 v44, v236, v242, s[30:31]\n"
        "v_mfma_f32_16x16x32_bf16 v[52:55], a[120:123], v[72:75], v[52:55]\n"
        "s_add_u32 s26, s26, 0x20000\n"
        "s_addc_u32 s27, s27, 0\n"
        "v_cndmask_b32 v45, v236, v242, s[56:57]\n"
        "v_mfma_f32_16x16x32_bf16 v[52:55], a[116:119], v[68:71], v[52:55]\n"
        "global_load_dword v244, v245, s[26:27] offset:0\n"
        "s_mov_b32 s30, 131072\n"
        "v_cndmask_b32 v46, v236, v242, s[58:59]\n"
        "v_mfma_f32_16x16x32_bf16 v[52:55], a[112:115], v[64:67], v[52:55]\n"
        "s_add_u32 s26, s26, 0x20000\n"
        "s_addc_u32 s27, s27, 0\n"
        "v_cndmask_b32 v47, v236, v242, s[60:61]\n"
        "s_nop 1\n"
        "v_mfma_f32_16x16x32_bf16 v[44:47], a[108:111], v[140:143], v[44:47]\n"
        "v_exp_f32 v56, v56\n"
        "v_mfma_f32_16x16x32_bf16 v[44:47], a[104:107], v[136:139], v[44:47]\n"
        "v_exp_f32 v57, v57\n"
        "v_mfma_f32_16x16x32_bf16 v[44:47], a[100:103], v[132:135], v[44:47]\n"
        "s_add_u32 s22, s18, s14\n"
        "s_addc_u32 s23, s19, 0\n"
        "v_cvt_pk_bf16_f32 v26, v56, v57\n"
        "v_mfma_f32_16x16x32_bf16 v[44:47], a[96:99], v[128:131], v[44:47]\n"
        "s_barrier\n"
        "v_smfmac_f32_16x16x64_bf16 a[64:67], a[72:75], v[16:23], v154\n"
        "s_sub_u32 s22, s22, 4096\n"
        "s_subb_u32 s23, s23, 0\n"
        "v_xor_b32 v240, 0x8000, v240\n"
        "v_mfma_f32_16x16x32_bf16 v[192:195], a[144:147], v[20:23], v[192:195]\n"
        "ds_read_b128 a[96:99], v240 offset:0\n"
        "s_lshl_b32 s14, s10, 18\n"
        "v_lshrrev_b32 v252, 2, v254\n"
        "v_mfma_f32_16x16x32_bf16 v[196:199], a[148:151], v[20:23], v[196:199]\n"
        "s_add_u32 s14, s14, s72\n"
        "v_exp_f32 v58, v58\n"
        "v_mfma_f32_16x16x32_bf16 v[164:167], a[148:151], v[16:19], v[164:167]\n"
        "s_add_u32 s14, s14, 0x2200000\n"
        "v_exp_f32 v59, v59\n"
        "v_mfma_f32_16x16x32_bf16 v[168:171], a[152:155], v[16:19], v[168:171]\n"
        "ds_read_b128 a[100:103], v240 offset:1024\n"
        "v_cvt_pk_bf16_f32 v27, v58, v59\n"
        "v_mfma_f32_16x16x32_bf16 v[56:59], a[112:115], v[96:99], v[232:235]\n"
        "ds_read_b128 a[104:107], v240 offset:2048\n"
        "v_add_f32 v60, v60, v236\n"
        "v_mfma_f32_16x16x32_bf16 v[56:59], a[116:119], v[100:103], v[56:59]\n"
        "v_exp_f32 v60, v60\n"
        "v_mfma_f32_16x16x32_bf16 v[56:59], a[120:123], v[104:107], v[56:59]\n"
        "ds_read_b128 a[108:111], v240 offset:3072\n"
        "v_add_f32 v61, v61, v236\n"
        "v_mfma_f32_16x16x32_bf16 v[56:59], a[124:127], v[108:111], v[56:59]\n"
        "v_exp_f32 v61, v61\n"
        "v_mfma_f32_16x16x32_bf16 a[0:3], a[144:147], v[24:27], a[0:3]\n"
        "s_add_u32 s20, s38, s14\n"
        "s_addc_u32 s21, s39, 0\n"
        "v_cvt_pk_bf16_f32 v30, v60, v61\n"
        "v_mfma_f32_16x16x32_bf16 a[4:7], a[148:151], v[24:27], a[4:7]\n"
        "s_waitcnt lgkmcnt(3)\n"
        "v_add_f32 v62, v62, v236\n"
        "v_add_f32 v63, v63, v236\n"
        "v_mfma_f32_16x16x32_bf16 v[200:203], a[152:155], v[20:23], v[200:203]\n"
        "v_exp_f32 v62, v62\n"
        "v_mfma_f32_16x16x32_bf16 a[8:11], a[152:155], v[24:27], a[8:11]\n"
        "v_exp_f32 v63, v63\n"
        "v_mfma_f32_16x16x32_bf16 a[12:15], a[156:159], v[24:27], a[12:15]\n"
        "v_cvt_pk_bf16_f32 v31, v62, v63\n"
        "s_nop 1\n"
        "v_smfmac_f32_16x16x64_bf16 a[68:71], a[72:75], v[24:31], v154\n"
        "v_cndmask_b32 v60, v236, v242, s[62:63]\n"
        "v_mfma_f32_16x16x32_bf16 a[32:35], a[144:147], v[28:31], 0\n"
        "v_exp_f32 v32, v32\n"
        "v_mfma_f32_16x16x32_bf16 a[36:39], a[148:151], v[28:31], 0\n"
        "v_exp_f32 v33, v33\n"
        "v_mfma_f32_16x16x32_bf16 a[40:43], a[152:155], v[28:31], 0\n"
        "ds_read_b128 a[144:147], v241 offset:24576\n"
        "v_cvt_pk_bf16_f32 v80, v32, v33\n"
        "v_mfma_f32_16x16x32_bf16 a[44:47], a[156:159], v[28:31], 0\n"
        "v_exp_f32 v34, v34\n"
        "v_mfma_f32_16x16x32_bf16 v[204:207], a[156:159], v[20:23], v[204:207]\n"
        "v_exp_f32 v35, v35\n"
        "v_mfma_f32_16x16x32_bf16 v[172:175], a[156:159], v[16:19], v[172:175]\n"
        "s_mov_b32 exec_hi, 0x7fffffff\n"
        "global_load_lds_dwordx4 v254, s[70:71] offset:3072\n"
        "s_mov_b64 exec, -1\n"
        "v_cvt_pk_bf16_f32 v81, v34, v35\n"
        "v_exp_f32 v36, v36\n"
        "v_exp_f32 v37, v37\n"
        "v_cndmask_b32 v61, v236, v242, s[84:85]\n"
        "v_cvt_pk_bf16_f32 v84, v36, v37\n"
        "v_cndmask_b32 v62, v236, v242, s[86:87]\n"
        "v_cndmask_b32 v63, v236, v242, s[88:89]\n"
        "s_nop 1\n"
        "v_mfma_f32_16x16x32_bf16 v[60:63], a[124:127], v[140:143], v[60:63]\n"
        "v_exp_f32 v38, v38\n"
        "v_mfma_f32_16x16x32_bf16 v[60:63], a[120:123], v[136:139], v[60:63]\n"
        "v_exp_f32 v39, v39\n"
        "v_mfma_f32_16x16x32_bf16 v[60:63], a[116:119], v[132:135], v[60:63]\n"
        "ds_read_b128 a[116:119], v240 offset:5120\n"
        "v_cvt_pk_bf16_f32 v85, v38, v39\n"
        "v_mfma_f32_16x16x32_bf16 v[60:63], a[112:115], v[128:131], v[60:63]\n"
        "s_mov_b64 exec, 7\n"
        "global_load_lds_dword v252, s[70:71] offset:4080\n"
        "s_mov_b64 exec, -1\n"
        "ds_read_b128 a[112:115], v240 offset:4096\n"
        "v_exp_f32 v40, v40\n"
        "v_exp_f32 v41, v41\n"
        "v_exp_f32 v42, v42\n"
        "v_cvt_pk_bf16_f32 v88, v40, v41\n"
        "v_exp_f32 v43, v43\n"
        "ds_read_b128 a[148:151], v241 offset:25600\n"
        "v_cvt_pk_bf16_f32 v89, v42, v43\n"
        "v_exp_f32 v44, v44\n"
        "v_exp_f32 v45, v45\n"
        "ds_read_b128 a[152:155], v241 offset:26624\n"
        "v_cvt_pk_bf16_f32 v92, v44, v45\n"
        "v_exp_f32 v46, v46\n"
        "v_exp_f32 v47, v47\n"
        "ds_read_b128 a[120:123], v240 offset:6144\n"
        "v_cvt_pk_bf16_f32 v93, v46, v47\n"
        "s_cmp_eq_u32 s44, 0\n"
        "s_cbranch_scc1 LLD_%=\n"
        "; ---- DUAL\n"
        "s_branch LDU_%=\n"
        "s_nop 0\n"
        "s_nop 0\n"
        "s_nop 0\n"
        "s_nop 0\n"
        "s_nop 0\n"
        "s_nop 0\n"
        "s_nop 0\n"
        "s_nop 0\n"
        "s_nop 0\n"
        "s_nop 0\n"
        "s_nop 0\n"
        "s_nop 0\n"
        "s_nop 0\n"
        "s_nop 0\n"
        "s_nop 0\n"
        "s_nop 0\n"
        "s_nop 0\n"
        "s_nop 0\n"
        "s_nop 0\n"
        "s_nop 0\n"
        "s_nop 0\n"
        "s_nop 0\n"
        "s_nop 0\n"
        "s_nop 0\n"
        "s_nop 0\n"
        "s_nop 0\n"
        "s_nop 0\n"
        "s_nop 0\n"
        "s_nop 0\n"
        "s_nop 0\n"
        "s_nop 0\n"
        "s_nop 0\n"
        "s_nop 0\n"
        "s_nop 0\n"
        "s_nop 0\n"
        "s_nop 0\n"
        "s_nop 0\n"
        "s_nop 0\n"
        "s_nop 0\n"
        "s_nop 0\n"
        "s_nop 0\n"
        "s_nop 0\n"
        "s_nop 0\n"
        "s_nop 0\n"
        "s_nop 0\n"
        "s_nop 0\n"
        "s_nop 0\n"
        "s_nop 0\n"
        "s_nop 0\n"
        "s_nop 0\n"
        "s_nop 0\n"
        "s_nop 0\n"
        "s_nop 0\n"
        "s_nop 0\n"
        "s_nop 0\n"
        "s_nop 0\n"
        "s_nop 0\n"
        "s_nop 0\n"
        "s_nop 0\n"
        "s_nop 0\n"
        "s_nop 0\n"
        "s_nop 0\n"
        "s_nop 0\n"
        "s_nop 0\n"
        "s_nop 0\n"
        "s_nop 0\n"
        "s_nop 0\n"
        "s_nop 0\n"
        "s_nop 0\n"
        "s_nop 0\n"
        "s_nop 0\n"
        "s_nop 0\n"
        "s_nop 0\n"
        "s_nop 0\n"
        "s_nop 0\n"
        "s_nop 0\n"
        "s_nop 0\n"
        "LHA11_%=:\n"
        "s_cbranch_execz LHRA_%=\n"
        "s_nop 0\n"
        "s_nop 0\n"
        "s_nop 0\n"
        "s_nop 0\n"
        "s_nop 0\n"
        "s_nop 0\n"
        "s_nop 0\n"
        "s_nop 0\n"
        "s_nop 0\n"
        "s_nop 0\n"
        "s_nop 0\n"
        "s_nop 0\n"
        "s_nop 0\n"
        "s_nop 0\n"
        "s_nop 0\n"
        "LDU_%=:\n"
        "v_mfma_f32_16x16x32_bf16 v[32:35], a[96:99], v[0:3], v[224:227]\n"
        "s_waitcnt lgkmcnt(4)\n"
        "ds_read_b128 a[156:159], v241 offset:27648\n"
        "s_add_u32 m0, s80, 0x0\n"
        "v_mfma_f32_16x16x32_bf16 v[32:35], a[100:103], v[4:7], v[32:35]\n"
        "ds_read_b128 a[124:127], v240 offset:7168\n"
        "global_load_lds_dwordx4 v254, s[68:69] offset:0 sc1\n"
        "s_xor_b32 s79, s79, 0x8000\n"
        "v_mfma_f32_16x16x32_bf16 v[32:35], a[104:107], v[8:11], v[32:35]\n"
        "v_exp_f32 v48, v48\n"
        "v_mfma_f32_16x16x32_bf16 v[32:35], a[108:111], v[12:15], v[32:35]\n"
        "v_exp_f32 v49, v49\n"
        "v_mfma_f32_16x16x32_bf16 v[36:39], a[108:111], v[76:79], v[228:231]\n"
        "v_exp_f32 v50, v50\n"
        "v_mfma_f32_16x16x32_bf16 v[36:39], a[104:107], v[72:75], v[36:39]\n"
        "v_exp_f32 v51, v51\n"
        "v_mfma_f32_16x16x32_bf16 v[36:39], a[100:103], v[68:71], v[36:39]\n"
        "v_exp_f32 v52, v52\n"
        "v_mfma_f32_16x16x32_bf16 v[36:39], a[96:99], v[64:67], v[36:39]\n"
        "v_exp_f32 v53, v53\n"
        "v_mfma_f32_16x16x32_bf16 v[40:43], a[96:99], v[96:99], v[232:235]\n"
        "v_exp_f32 v56, v56\n"
        "v_mfma_f32_16x16x32_bf16 v[40:43], a[100:103], v[100:103], v[40:43]\n"
        "v_exp_f32 v57, v57\n"
        "v_mfma_f32_16x16x32_bf16 v[40:43], a[104:107], v[104:107], v[40:43]\n"
        "v_exp_f32 v54, v54\n"
        "v_mfma_f32_16x16x32_bf16 v[40:43], a[108:111], v[108:111], v[40:43]\n"
        "v_exp_f32 v55, v55\n"
        "v_mfma_f32_16x16x32_bf16 v[44:47], a[108:111], v[140:143], v[236:239]\n"
        "ds_read_b128 a[108:111], v240 offset:11264\n"
        "v_cvt_pk_bf16_f32 v82, v48, v49\n"
        "v_mfma_f32_16x16x32_bf16 v[44:47], a[104:107], v[136:139], v[44:47]\n"
        "ds_read_b128 a[104:107], v240 offset:10240\n"
        "global_load_lds_dwordx4 v254, s[68:69] offset:1024 sc1\n"
        "v_mfma_f32_16x16x32_bf16 v[44:47], a[100:103], v[132:135], v[44:47]\n"
        "ds_read_b128 a[100:103], v240 offset:9216\n"
        "v_cvt_pk_bf16_f32 v83, v50, v51\n"
        "v_mfma_f32_16x16x32_bf16 v[44:47], a[96:99], v[128:131], v[44:47]\n"
        "ds_read_b128 a[96:99], v240 offset:8192\n"
        "v_cvt_pk_bf16_f32 v86, v52, v53\n"
        "v_mfma_f32_16x16x32_bf16 v[176:179], a[128:131], v[16:19], v[176:179]\n"
        "v_exp_f32 v58, v58\n"
        "v_mfma_f32_16x16x32_bf16 v[208:211], a[128:131], v[20:23], v[208:211]\n"
        "v_exp_f32 v59, v59\n"
        "v_mfma_f32_16x16x32_bf16 a[16:19], a[128:131], v[24:27], a[16:19]\n"
        "v_exp_f32 v60, v60\n"
        "v_mfma_f32_16x16x32_bf16 a[48:51], a[128:131], v[28:31], a[48:51]\n"
        "ds_read_b128 a[128:131], v241 offset:28672\n"
        "global_load_lds_dwordx4 v254, s[68:69] offset:2048 sc1\n"
        "v_mfma_f32_16x16x32_bf16 a[52:55], a[132:135], v[28:31], a[52:55]\n"
        "v_exp_f32 v61, v61\n"
        "v_mfma_f32_16x16x32_bf16 a[20:23], a[132:135], v[24:27], a[20:23]\n"
        "v_exp_f32 v62, v62\n"
        "v_mfma_f32_16x16x32_bf16 v[212:215], a[132:135], v[20:23], v[212:215]\n"
        "v_exp_f32 v63, v63\n"
        "v_mfma_f32_16x16x32_bf16 v[180:183], a[132:135], v[16:19], v[180:183]\n"
        "ds_read_b128 a[132:135], v241 offset:29696\n"
        "global_load_lds_dwordx4 v254, s[68:69] offset:3072 sc1\n"
        "s_add_u32 m0, s81, 0x0\n"
        "v_mfma_f32_16x16x32_bf16 v[184:187], a[136:139], v[16:19], v[184:187]\n"
        "v_exp_f32 v32, v32\n"
        "v_mfma_f32_16x16x32_bf16 v[216:219], a[136:139], v[20:23], v[216:219]\n"
        "v_exp_f32 v33, v33\n"
        "v_mfma_f32_16x16x32_bf16 a[24:27], a[136:139], v[24:27], a[24:27]\n"
        "v_exp_f32 v34, v34\n"
        "v_mfma_f32_16x16x32_bf16 a[56:59], a[136:139], v[28:31], a[56:59]\n"
        "ds_read_b128 a[136:139], v241 offset:30720\n"
        "global_load_lds_dwordx4 v254, s[74:75] offset:0 sc1\n"
        "v_mfma_f32_16x16x32_bf16 a[60:63], a[140:143], v[28:31], a[60:63]\n"
        "v_exp_f32 v35, v35\n"
        "v_mfma_f32_16x16x32_bf16 a[28:31], a[140:143], v[24:27], a[28:31]\n"
        "v_exp_f32 v36, v36\n"
        "v_mfma_f32_16x16x32_bf16 v[220:223], a[140:143], v[20:23], v[220:223]\n"
        "s_waitcnt lgkmcnt(2)\n"
        "v_exp_f32 v37, v37\n"
        "v_mfma_f32_16x16x32_bf16 v[188:191], a[140:143], v[16:19], v[188:191]\n"
        "ds_read_b128 a[140:143], v241 offset:31744\n"
        "v_cvt_pk_bf16_f32 v90, v56, v57\n"
        "v_mfma_f32_16x16x32_bf16 v[48:51], a[112:115], v[0:3], v[224:227]\n"
        "v_exp_f32 v38, v38\n"
        "v_mfma_f32_16x16x32_bf16 v[48:51], a[116:119], v[4:7], v[48:51]\n"
        "v_exp_f32 v39, v39\n"
        "v_mfma_f32_16x16x32_bf16 v[48:51], a[120:123], v[8:11], v[48:51]\n"
        "v_exp_f32 v40, v40\n"
        "v_mfma_f32_16x16x32_bf16 v[48:51], a[124:127], v[12:15], v[48:51]\n"
        "v_exp_f32 v41, v41\n"
        "v_mfma_f32_16x16x32_bf16 v[160:163], a[144:147], v[80:83], v[160:163]\n"
        "v_exp_f32 v42, v42\n"
        "v_mfma_f32_16x16x32_bf16 v[164:167], a[148:151], v[80:83], v[164:167]\n"
        "v_exp_f32 v43, v43\n"
        "v_mfma_f32_16x16x32_bf16 v[168:171], a[152:155], v[80:83], v[168:171]\n"
        "v_exp_f32 v44, v44\n"
        "v_mfma_f32_16x16x32_bf16 v[172:175], a[156:159], v[80:83], v[172:175]\n"
        "s_waitcnt vmcnt(7) lgkmcnt(0)\n"
        "v_exp_f32 v45, v45\n"
        "v_cvt_pk_bf16_f32 v87, v54, v55\n"
        "v_mfma_f32_16x16x32_bf16 v[52:55], a[124:127], v[76:79], v[228:231]\n"
        "s_barrier\n"
        "v_mfma_f32_16x16x32_bf16 v[52:55], a[120:123], v[72:75], v[52:55]\n"
        "s_add_u32 s82, s82, 1\n"
        "v_exp_f32 v46, v46\n"
        "v_mfma_f32_16x16x32_bf16 v[52:55], a[116:119], v[68:71], v[52:55]\n"
        "s_xor_b32 s78, s78, 0x8000\n"
        "v_exp_f32 v47, v47\n"
        "v_mfma_f32_16x16x32_bf16 v[52:55], a[112:115], v[64:67], v[52:55]\n"
        "s_add_u32 s81, s78, 4096\n"
        "v_exp_f32 v48, v48\n"
        "v_smfmac_f32_16x16x64_bf16 a[64:67], a[72:75], v[80:87], v154\n"
        "global_load_lds_dwordx4 v254, s[74:75] offset:1024 sc1\n"
        "s_mov_b32 s80, s78\n"
        "v_cvt_pk_bf16_f32 v91, v58, v59\n"
        "v_mfma_f32_16x16x32_bf16 v[56:59], a[112:115], v[96:99], v[232:235]\n"
        "v_exp_f32 v49, v49\n"
        "v_mfma_f32_16x16x32_bf16 v[56:59], a[116:119], v[100:103], v[56:59]\n"
        "v_exp_f32 v50, v50\n"
        "v_mfma_f32_16x16x32_bf16 v[56:59], a[120:123], v[104:107], v[56:59]\n"
        "v_exp_f32 v51, v51\n"
        "v_mfma_f32_16x16x32_bf16 v[56:59], a[124:127], v[108:111], v[56:59]\n"
        "v_exp_f32 v52, v52\n"
        "v_mfma_f32_16x16x32_bf16 v[192:195], a[144:147], v[84:87], v[192:195]\n"
        "v_exp_f32 v53, v53\n"
        "v_mfma_f32_16x16x32_bf16 a[0:3], a[144:147], v[88:91], a[0:3]\n"
        "v_exp_f32 v54, v54\n"
        "v_mfma_f32_16x16x32_bf16 a[4:7], a[148:151], v[88:91], a[4:7]\n"
        "v_exp_f32 v55, v55\n"
        "v_mfma_f32_16x16x32_bf16 v[196:199], a[148:151], v[84:87], v[196:199]\n"
        "v_exp_f32 v56, v56\n"
        "v_mfma_f32_16x16x32_bf16 v[200:203], a[152:155], v[84:87], v[200:203]\n"
        "v_exp_f32 v57, v57\n"
        "v_mfma_f32_16x16x32_bf16 a[8:11], a[152:155], v[88:91], a[8:11]\n"
        "v_exp_f32 v58, v58\n"
        "v_mfma_f32_16x16x32_bf16 a[12:15], a[156:159], v[88:91], a[12:15]\n"
        "v_exp_f32 v59, v59\n"
        "v_mfma_f32_16x16x32_bf16 v[204:207], a[156:159], v[84:87], v[204:207]\n"
        "global_load_lds_dwordx4 v254, s[74:75] offset:2048 sc1\n"
        "v_cvt_pk_bf16_f32 v94, v60, v61\n"
        "v_mfma_f32_16x16x32_bf16 v[176:179], a[128:131], v[80:83], v[176:179]\n"
        "s_add_u32 s76, s66, s90\n"
        "s_addc_u32 s77, s67, 0\n"
        "v_cvt_pk_bf16_f32 v95, v62, v63\n"
        "v_mfma_f32_16x16x32_bf16 v[60:63], a[124:127], v[140:143], v[236:239]\n"
        "ds_read_b128 a[124:127], v240 offset:15360\n"
        "v_cvt_pk_bf16_f32 v16, v32, v33\n"
        "v_mfma_f32_16x16x32_bf16 v[60:63], a[120:123], v[136:139], v[60:63]\n"
        "ds_read_b128 a[120:123], v240 offset:14336\n"
        "v_cvt_pk_bf16_f32 v17, v34, v35\n"
        "v_mfma_f32_16x16x32_bf16 v[60:63], a[116:119], v[132:135], v[60:63]\n"
        "ds_read_b128 a[116:119], v240 offset:13312\n"
        "global_load_lds_dwordx4 v254, s[74:75] offset:3072 sc1\n"
        "s_add_u32 m0, s79, 0x0\n"
        "v_mfma_f32_16x16x32_bf16 v[60:63], a[112:115], v[128:131], v[60:63]\n"
        "ds_read_b128 a[112:115], v240 offset:12288\n"
        "v_cvt_pk_bf16_f32 v20, v36, v37\n"
        "v_smfmac_f32_16x16x64_bf16 a[68:71], a[72:75], v[88:95], v154\n"
        "s_cmp_ge_u32 s82, s44\n"
        "s_addc_u32 s90, s82, 0\n"
        "v_cvt_pk_bf16_f32 v21, v38, v39\n"
        "v_mfma_f32_16x16x32_bf16 a[32:35], a[144:147], v[92:95], a[32:35]\n"
        "s_lshl_b32 s90, s90, 15\n"
        "v_cvt_pk_bf16_f32 v24, v40, v41\n"
        "v_xor_b32 v241, 0x8000, v241\n"
        "v_mfma_f32_16x16x32_bf16 a[36:39], a[148:151], v[92:95], a[36:39]\n"
        "ds_read_b128 a[144:147], v241 offset:0\n"
        "global_load_lds_dwordx4 v144, s[76:77] offset:0 sc1\n"
        "v_mfma_f32_16x16x32_bf16 a[40:43], a[152:155], v[92:95], a[40:43]\n"
        "ds_read_b128 a[148:151], v241 offset:1024\n"
        "v_cvt_pk_bf16_f32 v25, v42, v43\n"
        "v_mfma_f32_16x16x32_bf16 a[44:47], a[156:159], v[92:95], a[44:47]\n"
        "ds_read_b128 a[152:155], v241 offset:2048\n"
        "v_cvt_pk_bf16_f32 v28, v44, v45\n"
        "v_mfma_f32_16x16x32_bf16 v[32:35], a[96:99], v[0:3], v[224:227]\n"
        "ds_read_b128 a[156:159], v241 offset:3072\n"
        "v_cvt_pk_bf16_f32 v29, v46, v47\n"
        "v_mfma_f32_16x16x32_bf16 v[32:35], a[100:103], v[4:7], v[32:35]\n"
        "v_exp_f32 v60, v60\n"
        "v_mfma_f32_16x16x32_bf16 v[32:35], a[104:107], v[8:11], v[32:35]\n"
        "v_exp_f32 v61, v61\n"
        "v_mfma_f32_16x16x32_bf16 v[32:35], a[108:111], v[12:15], v[32:35]\n"
        "v_exp_f32 v62, v62\n"
        "v_mfma_f32_16x16x32_bf16 v[36:39], a[108:111], v[76:79], v[228:231]\n"
        "v_exp_f32 v63, v63\n"
        "v_mfma_f32_16x16x32_bf16 v[36:39], a[104:107], v[72:75], v[36:39]\n"
        "global_load_lds_dwordx4 v145, s[76:77] offset:1024 sc1\n"
        "v_cvt_pk_bf16_f32 v18, v48, v49\n"
        "v_mfma_f32_16x16x32_bf16 v[36:39], a[100:103], v[68:71], v[36:39]\n"
        "s_add_u32 s68, s64, s90\n"
        "s_addc_u32 s69, s65, 0\n"
        "v_cvt_pk_bf16_f32 v19, v50, v51\n"
        "v_mfma_f32_16x16x32_bf16 v[36:39], a[96:99], v[64:67], v[36:39]\n"
        "v_exp_f32 v32, v32\n"
        "v_mfma_f32_16x16x32_bf16 v[40:43], a[96:99], v[96:99], v[232:235]\n"
        "v_exp_f32 v33, v33\n"
        "v_mfma_f32_16x16x32_bf16 v[40:43], a[100:103], v[100:103], v[40:43]\n"
        "v_exp_f32 v34, v34\n"
        "v_mfma_f32_16x16x32_bf16 v[40:43], a[104:107], v[104:107], v[40:43]\n"
        "v_exp_f32 v35, v35\n"
        "v_mfma_f32_16x16x32_bf16 v[40:43], a[108:111], v[108:111], v[40:43]\n"
        "v_exp_f32 v36, v36\n"
        "v_mfma_f32_16x16x32_bf16 v[44:47], a[108:111], v[140:143], v[236:239]\n"
        "ds_read_b128 a[108:111], v240 offset:19456\n"
        "global_load_lds_dwordx4 v146, s[76:77] offset:2048 sc1\n"
        "v_mfma_f32_16x16x32_bf16 v[44:47], a[104:107], v[136:139], v[44:47]\n"
        "ds_read_b128 a[104:107], v240 offset:18432\n"
        "v_cvt_pk_bf16_f32 v22, v52, v53\n"
        "v_mfma_f32_16x16x32_bf16 v[44:47], a[100:103], v[132:135], v[44:47]\n"
        "ds_read_b128 a[100:103], v240 offset:17408\n"
        "v_cvt_pk_bf16_f32 v23, v54, v55\n"
        "v_mfma_f32_16x16x32_bf16 v[44:47], a[96:99], v[128:131], v[44:47]\n"
        "ds_read_b128 a[96:99], v240 offset:16384\n"
        "v_cvt_pk_bf16_f32 v26, v56, v57\n"
        "v_mfma_f32_16x16x32_bf16 v[208:211], a[128:131], v[84:87], v[208:211]\n"
        "v_exp_f32 v37, v37\n"
        "v_mfma_f32_16x16x32_bf16 a[16:19], a[128:131], v[88:91], a[16:19]\n"
        "v_exp_f32 v38, v38\n"
        "v_mfma_f32_16x16x32_bf16 a[48:51], a[128:131], v[92:95], a[48:51]\n"
        "ds_read_b128 a[128:131], v241 offset:4096\n"
        "global_load_lds_dwordx4 v147, s[76:77] offset:3072 sc1\n"
        "s_add_u32 m0, s79, s28\n"
        "v_mfma_f32_16x16x32_bf16 a[52:55], a[132:135], v[92:95], a[52:55]\n"
        "v_exp_f32 v39, v39\n"
        "v_mfma_f32_16x16x32_bf16 a[20:23], a[132:135], v[88:91], a[20:23]\n"
        "v_exp_f32 v40, v40\n"
        "v_mfma_f32_16x16x32_bf16 v[212:215], a[132:135], v[84:87], v[212:215]\n"
        "v_exp_f32 v41, v41\n"
        "v_mfma_f32_16x16x32_bf16 v[180:183], a[132:135], v[80:83], v[180:183]\n"
        "ds_read_b128 a[132:135], v241 offset:5120\n"
        "s_add_u32 s76, s76, s25\n"
        "s_addc_u32 s77, s77, 0\n"
        "v_mfma_f32_16x16x32_bf16 v[184:187], a[136:139], v[80:83], v[184:187]\n"
        "v_exp_f32 v42, v42\n"
        "v_mfma_f32_16x16x32_bf16 v[216:219], a[136:139], v[84:87], v[216:219]\n"
        "v_exp_f32 v43, v43\n"
        "v_mfma_f32_16x16x32_bf16 a[24:27], a[136:139], v[88:91], a[24:27]\n"
        "v_exp_f32 v44, v44\n"
        "v_mfma_f32_16x16x32_bf16 a[56:59], a[136:139], v[92:95], a[56:59]\n"
        "ds_read_b128 a[136:139], v241 offset:6144\n"
        "global_load_lds_dwordx4 v144, s[76:77] offset:0 sc1\n"
        "v_mfma_f32_16x16x32_bf16 a[60:63], a[140:143], v[92:95], a[60:63]\n"
        "s_waitcnt lgkmcnt(2)\n"
        "v_exp_f32 v45, v45\n"
        "v_mfma_f32_16x16x32_bf16 a[28:31], a[140:143], v[88:91], a[28:31]\n"
        "v_exp_f32 v46, v46\n"
        "v_mfma_f32_16x16x32_bf16 v[220:223], a[140:143], v[84:87], v[220:223]\n"
        "v_exp_f32 v47, v47\n"
        "v_mfma_f32_16x16x32_bf16 v[188:191], a[140:143], v[80:83], v[188:191]\n"
        "ds_read_b128 a[140:143], v241 offset:7168\n"
        "global_load_lds_dwordx4 v145, s[76:77] offset:1024 sc1\n"
        "v_mfma_f32_16x16x32_bf16 v[48:51], a[112:115], v[0:3], v[224:227]\n"
        "s_add_u32 s74, s68, 0x1000\n"
        "s_addc_u32 s75, s69, 0\n"
        "v_cvt_pk_bf16_f32 v27, v58, v59\n"
        "v_mfma_f32_16x16x32_bf16 v[48:51], a[116:119], v[4:7], v[48:51]\n"
        "v_cvt_pk_bf16_f32 v30, v60, v61\n"
        "v_cvt_pk_bf16_f32 v31, v62, v63\n"
        "v_mfma_f32_16x16x32_bf16 v[48:51], a[120:123], v[8:11], v[48:51]\n"
        "v_cvt_pk_bf16_f32 v80, v32, v33\n"
        "v_cvt_pk_bf16_f32 v81, v34, v35\n"
        "v_mfma_f32_16x16x32_bf16 v[48:51], a[124:127], v[12:15], v[48:51]\n"
        "global_load_lds_dwordx4 v146, s[76:77] offset:2048 sc1\n"
        "v_mfma_f32_16x16x32_bf16 v[52:55], a[124:127], v[76:79], v[228:231]\n"
        "v_cvt_pk_bf16_f32 v84, v36, v37\n"
        "v_cvt_pk_bf16_f32 v85, v38, v39\n"
        "v_mfma_f32_16x16x32_bf16 v[52:55], a[120:123], v[72:75], v[52:55]\n"
        "v_cvt_pk_bf16_f32 v88, v40, v41\n"
        "v_cvt_pk_bf16_f32 v89, v42, v43\n"
        "v_mfma_f32_16x16x32_bf16 v[52:55], a[116:119], v[68:71], v[52:55]\n"
        "v_exp_f32 v48, v48\n"
        "v_mfma_f32_16x16x32_bf16 v[52:55], a[112:115], v[64:67], v[52:55]\n"
        "v_exp_f32 v49, v49\n"
        "v_mfma_f32_16x16x32_bf16 v[56:59], a[112:115], v[96:99], v[232:235]\n"
        "v_exp_f32 v50, v50\n"
        "v_mfma_f32_16x16x32_bf16 v[56:59], a[116:119], v[100:103], v[56:59]\n"
        "v_exp_f32 v51, v51\n"
        "v_mfma_f32_16x16x32_bf16 v[56:59], a[120:123], v[104:107], v[56:59]\n"
        "global_load_lds_dwordx4 v147, s[76:77] offset:3072 sc1\n"
        "v_cvt_pk_bf16_f32 v92, v44, v45\n"
        "v_mfma_f32_16x16x32_bf16 v[56:59], a[124:127], v[108:111], v[56:59]\n"
        "v_exp_f32 v52, v52\n"
        "v_mfma_f32_16x16x32_bf16 v[60:63], a[124:127], v[140:143], v[236:239]\n"
        "ds_read_b128 a[124:127], v240 offset:23552\n"
        "v_cvt_pk_bf16_f32 v93, v46, v47\n"
        "v_mfma_f32_16x16x32_bf16 v[60:63], a[120:123], v[136:139], v[60:63]\n"
        "ds_read_b128 a[120:123], v240 offset:22528\n"
        "v_cvt_pk_bf16_f32 v82, v48, v49\n"
        "v_mfma_f32_16x16x32_bf16 v[60:63], a[116:119], v[132:135], v[60:63]\n"
        "ds_read_b128 a[116:119], v240 offset:21504\n"
        "global_load_dword v244, v245, s[26:27] offset:0\n"
        "v_mfma_f32_16x16x32_bf16 v[60:63], a[112:115], v[128:131], v[60:63]\n"
        "ds_read_b128 a[112:115], v240 offset:20480\n"
        "s_add_u32 s26, s26, 0x20000\n"
        "s_addc_u32 s27, s27, 0\n"
        "v_smfmac_f32_16x16x64_bf16 a[64:67], a[72:75], v[16:23], v154\n"
        "v_cvt_pk_bf16_f32 v83, v50, v51\n"
        "v_smfmac_f32_16x16x64_bf16 a[68:71], a[72:75], v[24:31], v154\n"
        "v_mfma_f32_16x16x32_bf16 v[160:163], a[144:147], v[16:19], v[160:163]\n"
        "v_exp_f32 v53, v53\n"
        "v_mfma_f32_16x16x32_bf16 v[192:195], a[144:147], v[20:23], v[192:195]\n"
        "v_exp_f32 v54, v54\n"
        "v_mfma_f32_16x16x32_bf16 a[0:3], a[144:147], v[24:27], a[0:3]\n"
        "v_exp_f32 v55, v55\n"
        "v_mfma_f32_16x16x32_bf16 a[32:35], a[144:147], v[28:31], a[32:35]\n"
        "ds_read_b128 a[144:147], v241 offset:8192\n"
        "v_cvt_pk_bf16_f32 v86, v52, v53\n"
        "v_mfma_f32_16x16x32_bf16 a[36:39], a[148:151], v[28:31], a[36:39]\n"
        "v_exp_f32 v56, v56\n"
        "v_mfma_f32_16x16x32_bf16 a[4:7], a[148:151], v[24:27], a[4:7]\n"
        "v_exp_f32 v57, v57\n"
        "v_mfma_f32_16x16x32_bf16 v[196:199], a[148:151], v[20:23], v[196:199]\n"
        "v_exp_f32 v58, v58\n"
        "v_mfma_f32_16x16x32_bf16 v[164:167], a[148:151], v[16:19], v[164:167]\n"
        "ds_read_b128 a[148:151], v241 offset:9216\n"
        "v_cvt_pk_bf16_f32 v87, v54, v55\n"
        "v_mfma_f32_16x16x32_bf16 v[168:171], a[152:155], v[16:19], v[168:171]\n"
        "v_exp_f32 v59, v59\n"
        "v_mfma_f32_16x16x32_bf16 v[200:203], a[152:155], v[20:23], v[200:203]\n"
        "v_exp_f32 v60, v60\n"
        "v_mfma_f32_16x16x32_bf16 a[8:11], a[152:155], v[24:27], a[8:11]\n"
        "v_exp_f32 v61, v61\n"
        "v_mfma_f32_16x16x32_bf16 a[40:43], a[152:155], v[28:31], a[40:43]\n"
        "ds_read_b128 a[152:155], v241 offset:10240\n"
        "v_cvt_pk_bf16_f32 v90, v56, v57\n"
        "v_mfma_f32_16x16x32_bf16 a[44:47], a[156:159], v[28:31], a[44:47]\n"
        "v_exp_f32 v62, v62\n"
        "v_mfma_f32_16x16x32_bf16 a[12:15], a[156:159], v[24:27], a[12:15]\n"
        "v_exp_f32 v63, v63\n"
        "v_mfma_f32_16x16x32_bf16 v[204:207], a[156:159], v[20:23], v[204:207]\n"
        "v_cvt_pk_bf16_f32 v91, v58, v59\n"
        "v_cvt_pk_bf16_f32 v94, v60, v61\n"
        "v_mfma_f32_16x16x32_bf16 v[172:175], a[156:159], v[16:19], v[172:175]\n"
        "ds_read_b128 a[156:159], v241 offset:11264\n"
        "v_cvt_pk_bf16_f32 v95, v62, v63\n"
        "v_mfma_f32_16x16x32_bf16 v[32:35], a[96:99], v[0:3], v[224:227]\n"
        "v_mfma_f32_16x16x32_bf16 v[32:35], a[100:103], v[4:7], v[32:35]\n"
        "v_mfma_f32_16x16x32_bf16 v[32:35], a[104:107], v[8:11], v[32:35]\n"
        "v_mfma_f32_16x16x32_bf16 v[32:35], a[108:111], v[12:15], v[32:35]\n"
        "v_mfma_f32_16x16x32_bf16 v[36:39], a[108:111], v[76:79], v[228:231]\n"
        "v_mfma_f32_16x16x32_bf16 v[36:39], a[104:107], v[72:75], v[36:39]\n"
        "v_mfma_f32_16x16x32_bf16 v[36:39], a[100:103], v[68:71], v[36:39]\n"
        "v_mfma_f32_16x16x32_bf16 v[36:39], a[96:99], v[64:67], v[36:39]\n"
        "v_mfma_f32_16x16x32_bf16 v[40:43], a[96:99], v[96:99], v[232:235]\n"
        "v_mfma_f32_16x16x32_bf16 v[40:43], a[100:103], v[100:103], v[40:43]\n"
        "v_mfma_f32_16x16x32_bf16 v[40:43], a[104:107], v[104:107], v[40:43]\n"
        "v_mfma_f32_16x16x32_bf16 v[40:43], a[108:111], v[108:111], v[40:43]\n"
        "v_exp_f32 v32, v32\n"
        "v_mfma_f32_16x16x32_bf16 v[44:47], a[108:111], v[140:143], v[236:239]\n"
        "v_exp_f32 v33, v33\n"
        "v_mfma_f32_16x16x32_bf16 v[44:47], a[104:107], v[136:139], v[44:47]\n"
        "ds_read_b128 a[104:107], v240 offset:26624\n"
        "v_mfma_f32_16x16x32_bf16 v[44:47], a[100:103], v[132:135], v[44:47]\n"
        "ds_read_b128 a[100:103], v240 offset:25600\n"
        "v_mfma_f32_16x16x32_bf16 v[44:47], a[96:99], v[128:131], v[44:47]\n"
        "ds_read_b128 a[96:99], v240 offset:24576\n"
        "v_mfma_f32_16x16x32_bf16 v[176:179], a[128:131], v[16:19], v[176:179]\n"
        "s_waitcnt lgkmcnt(3)\n"
        "v_exp_f32 v34, v34\n"
        "v_mfma_f32_16x16x32_bf16 v[208:211], a[128:131], v[20:23], v[208:211]\n"
        "v_exp_f32 v35, v35\n"
        "v_mfma_f32_16x16x32_bf16 a[16:19], a[128:131], v[24:27], a[16:19]\n"
        "ds_read_b128 a[108:111], v240 offset:27648\n"
        "v_mfma_f32_16x16x32_bf16 a[48:51], a[128:131], v[28:31], a[48:51]\n"
        "ds_read_b128 a[128:131], v241 offset:12288\n"
        "v_mfma_f32_16x16x32_bf16 a[52:55], a[132:135], v[28:31], a[52:55]\n"
        "v_exp_f32 v36, v36\n"
        "v_mfma_f32_16x16x32_bf16 a[20:23], a[132:135], v[24:27], a[20:23]\n"
        "v_exp_f32 v37, v37\n"
        "v_mfma_f32_16x16x32_bf16 v[212:215], a[132:135], v[20:23], v[212:215]\n"
        "v_exp_f32 v38, v38\n"
        "v_mfma_f32_16x16x32_bf16 v[180:183], a[132:135], v[16:19], v[180:183]\n"
        "ds_read_b128 a[132:135], v241 offset:13312\n"
        "v_mfma_f32_16x16x32_bf16 v[184:187], a[136:139], v[16:19], v[184:187]\n"
        "v_exp_f32 v39, v39\n"
        "v_mfma_f32_16x16x32_bf16 v[216:219], a[136:139], v[20:23], v[216:219]\n"
        "v_exp_f32 v40, v40\n"
        "v_mfma_f32_16x16x32_bf16 a[24:27], a[136:139], v[24:27], a[24:27]\n"
        "v_exp_f32 v41, v41\n"
        "v_mfma_f32_16x16x32_bf16 a[56:59], a[136:139], v[28:31], a[56:59]\n"
        "ds_read_b128 a[136:139], v241 offset:14336\n"
        "v_mfma_f32_16x16x32_bf16 a[60:63], a[140:143], v[28:31], a[60:63]\n"
        "v_exp_f32 v42, v42\n"
        "v_mfma_f32_16x16x32_bf16 a[28:31], a[140:143], v[24:27], a[28:31]\n"
        "v_exp_f32 v43, v43\n"
        "v_mfma_f32_16x16x32_bf16 v[220:223], a[140:143], v[20:23], v[220:223]\n"
        "v_exp_f32 v44, v44\n"
        "v_mfma_f32_16x16x32_bf16 v[188:191], a[140:143], v[16:19], v[188:191]\n"
        "ds_read_b128 a[140:143], v241 offset:15360\n"
        "global_load_dword v244, v245, s[26:27] offset:0\n"
        "v_mfma_f32_16x16x32_bf16 v[48:51], a[112:115], v[0:3], v[224:227]\n"
        "v_exp_f32 v45, v45\n"
        "v_mfma_f32_16x16x32_bf16 v[48:51], a[116:119], v[4:7], v[48:51]\n"
        "v_exp_f32 v46, v46\n"
        "v_mfma_f32_16x16x32_bf16 v[48:51], a[120:123], v[8:11], v[48:51]\n"
        "v_exp_f32 v47, v47\n"
        "v_mfma_f32_16x16x32_bf16 v[48:51], a[124:127], v[12:15], v[48:51]\n"
        "s_add_u32 s26, s26, 0x20000\n"
        "s_addc_u32 s27, s27, 0\n"
        "v_cvt_pk_bf16_f32 v16, v32, v33\n"
        "v_mfma_f32_16x16x32_bf16 v[52:55], a[124:127], v[76:79], v[228:231]\n"
        "v_cvt_pk_bf16_f32 v17, v34, v35\n"
        "v_cvt_pk_bf16_f32 v20, v36, v37\n"
        "v_mfma_f32_16x16x32_bf16 v[52:55], a[120:123], v[72:75], v[52:55]\n"
        "v_cvt_pk_bf16_f32 v21, v38, v39\n"
        "v_cvt_pk_bf16_f32 v24, v40, v41\n"
        "v_mfma_f32_16x16x32_bf16 v[52:55], a[116:119], v[68:71], v[52:55]\n"
        "v_exp_f32 v48, v48\n"
        "v_mfma_f32_16x16x32_bf16 v[52:55], a[112:115], v[64:67], v[52:55]\n"
        "v_exp_f32 v49, v49\n"
        "v_mfma_f32_16x16x32_bf16 v[56:59], a[112:115], v[96:99], v[232:235]\n"
        "v_exp_f32 v50, v50\n"
        "v_mfma_f32_16x16x32_bf16 v[56:59], a[116:119], v[100:103], v[56:59]\n"
        "v_exp_f32 v51, v51\n"
        "v_mfma_f32_16x16x32_bf16 v[56:59], a[120:123], v[104:107], v[56:59]\n"
        "v_cvt_pk_bf16_f32 v25, v42, v43\n"
        "v_cvt_pk_bf16_f32 v28, v44, v45\n"
        "v_mfma_f32_16x16x32_bf16 v[56:59], a[124:127], v[108:111], v[56:59]\n"
        "v_exp_f32 v52, v52\n"
        "v_mfma_f32_16x16x32_bf16 v[60:63], a[124:127], v[140:143], v[236:239]\n"
        "ds_read_b128 a[124:127], v240 offset:31744\n"
        "v_cvt_pk_bf16_f32 v29, v46, v47\n"
        "v_mfma_f32_16x16x32_bf16 v[60:63], a[120:123], v[136:139], v[60:63]\n"
        "ds_read_b128 a[120:123], v240 offset:30720\n"
        "v_cvt_pk_bf16_f32 v18, v48, v49\n"
        "v_mfma_f32_16x16x32_bf16 v[60:63], a[116:119], v[132:135], v[60:63]\n"
        "ds_read_b128 a[116:119], v240 offset:29696\n"
        "v_cvt_pk_bf16_f32 v19, v50, v51\n"
        "v_mfma_f32_16x16x32_bf16 v[60:63], a[112:115], v[128:131], v[60:63]\n"
        "ds_read_b128 a[112:115], v240 offset:28672\n"
        "v_xor_b32 v240, 0x8000, v240\n"
        "v_smfmac_f32_16x16x64_bf16 a[64:67], a[72:75], v[80:87], v154\n"
        "v_smfmac_f32_16x16x64_bf16 a[68:71], a[72:75], v[88:95], v154\n"
        "v_mfma_f32_16x16x32_bf16 v[160:163], a[144:147], v[80:83], v[160:163]\n"
        "v_exp_f32 v53, v53\n"
        "v_mfma_f32_16x16x32_bf16 v[192:195], a[144:147], v[84:87], v[192:195]\n"
        "v_exp_f32 v54, v54\n"
        "v_mfma_f32_16x16x32_bf16 a[0:3], a[144:147], v[88:91], a[0:3]\n"
        "v_exp_f32 v55, v55\n"
        "v_mfma_f32_16x16x32_bf16 a[32:35], a[144:147], v[92:95], a[32:35]\n"
        "ds_read_b128 a[144:147], v241 offset:16384\n"
        "v_cvt_pk_bf16_f32 v22, v52, v53\n"
        "v_mfma_f32_16x16x32_bf16 a[36:39], a[148:151], v[92:95], a[36:39]\n"
        "v_exp_f32 v56, v56\n"
        "v_mfma_f32_16x16x32_bf16 a[4:7], a[148:151], v[88:91], a[4:7]\n"
        "s_waitcnt vmcnt(10) lgkmcnt(1)\n"
        "v_exp_f32 v57, v57\n"
        "v_mfma_f32_16x16x32_bf16 v[196:199], a[148:151], v[84:87], v[196:199]\n"
        "s_barrier\n"
        "v_mfma_f32_16x16x32_bf16 v[164:167], a[148:151], v[80:83], v[164:167]\n"
        "ds_read_b128 a[148:151], v241 offset:17408\n"
        "v_cvt_pk_bf16_f32 v23, v54, v55\n"
        "v_mfma_f32_16x16x32_bf16 v[168:171], a[152:155], v[80:83], v[168:171]\n"
        "v_exp_f32 v58, v58\n"
        "v_mfma_f32_16x16x32_bf16 v[200:203], a[152:155], v[84:87], v[200:203]\n"
        "v_exp_f32 v59, v59\n"
        "v_mfma_f32_16x16x32_bf16 a[8:11], a[152:155], v[88:91], a[8:11]\n"
        "v_exp_f32 v60, v60\n"
        "v_mfma_f32_16x16x32_bf16 a[40:43], a[152:155], v[92:95], a[40:43]\n"
        "ds_read_b128 a[152:155], v241 offset:18432\n"
        "v_cvt_pk_bf16_f32 v26, v56, v57\n"
        "v_mfma_f32_16x16x32_bf16 a[44:47], a[156:159], v[92:95], a[44:47]\n"
        "v_exp_f32 v61, v61\n"
        "v_mfma_f32_16x16x32_bf16 a[12:15], a[156:159], v[88:91], a[12:15]\n"
        "v_exp_f32 v62, v62\n"
        "v_mfma_f32_16x16x32_bf16 v[204:207], a[156:159], v[84:87], v[204:207]\n"
        "v_exp_f32 v63, v63\n"
        "v_mfma_f32_16x16x32_bf16 v[172:175], a[156:159], v[80:83], v[172:175]\n"
        "ds_read_b128 a[156:159], v241 offset:19456\n"
        "v_cvt_pk_bf16_f32 v27, v58, v59\n"
        "v_mfma_f32_16x16x32_bf16 v[32:35], a[96:99], v[0:3], v[224:227]\n"
        "v_cvt_pk_bf16_f32 v30, v60, v61\n"
        "v_cvt_pk_bf16_f32 v31, v62, v63\n"
        "v_mfma_f32_16x16x32_bf16 v[32:35], a[100:103], v[4:7], v[32:35]\n"
        "v_mfma_f32_16x16x32_bf16 v[32:35], a[104:107], v[8:11], v[32:35]\n"
        "v_mfma_f32_16x16x32_bf16 v[32:35], a[108:111], v[12:15], v[32:35]\n"
        "v_mfma_f32_16x16x32_bf16 v[36:39], a[108:111], v[76:79], v[228:231]\n"
        "v_mfma_f32_16x16x32_bf16 v[36:39], a[104:107], v[72:75], v[36:39]\n"
        "v_mfma_f32_16x16x32_bf16 v[36:39], a[100:103], v[68:71], v[36:39]\n"
        "v_mfma_f32_16x16x32_bf16 v[36:39], a[96:99], v[64:67], v[36:39]\n"
        "v_mfma_f32_16x16x32_bf16 v[40:43], a[96:99], v[96:99], v[232:235]\n"
        "v_mfma_f32_16x16x32_bf16 v[40:43], a[100:103], v[100:103], v[40:43]\n"
        "v_mfma_f32_16x16x32_bf16 v[40:43], a[104:107], v[104:107], v[40:43]\n"
        "v_mfma_f32_16x16x32_bf16 v[40:43], a[108:111], v[108:111], v[40:43]\n"
        "v_exp_f32 v32, v32\n"
        "v_mfma_f32_16x16x32_bf16 v[44:47], a[108:111], v[140:143], v[236:239]\n"
        "v_exp_f32 v33, v33\n"
        "v_mfma_f32_16x16x32_bf16 v[44:47], a[104:107], v[136:139], v[44:47]\n"
        "ds_read_b128 a[104:107], v240 offset:2048\n"
        "v_mfma_f32_16x16x32_bf16 v[44:47], a[100:103], v[132:135], v[44:47]\n"
        "ds_read_b128 a[100:103], v240 offset:1024\n"
        "v_mfma_f32_16x16x32_bf16 v[44:47], a[96:99], v[128:131], v[44:47]\n"
        "ds_read_b128 a[96:99], v240 offset:0\n"
        "v_mfma_f32_16x16x32_bf16 v[176:179], a[128:131], v[80:83], v[176:179]\n"
        "v_exp_f32 v34, v34\n"
        "v_mfma_f32_16x16x32_bf16 v[208:211], a[128:131], v[84:87], v[208:211]\n"
        "v_exp_f32 v35, v35\n"
        "v_mfma_f32_16x16x32_bf16 a[16:19], a[128:131], v[88:91], a[16:19]\n"
        "ds_read_b128 a[108:111], v240 offset:3072\n"
        "v_mfma_f32_16x16x32_bf16 a[48:51], a[128:131], v[92:95], a[48:51]\n"
        "ds_read_b128 a[128:131], v241 offset:20480\n"
        "v_mfma_f32_16x16x32_bf16 a[52:55], a[132:135], v[92:95], a[52:55]\n"
        "v_exp_f32 v36, v36\n"
        "v_mfma_f32_16x16x32_bf16 a[20:23], a[132:135], v[88:91], a[20:23]\n"
        "v_exp_f32 v37, v37\n"
        "v_mfma_f32_16x16x32_bf16 v[212:215], a[132:135], v[84:87], v[212:215]\n"
        "v_exp_f32 v38, v38\n"
        "v_mfma_f32_16x16x32_bf16 v[180:183], a[132:135], v[80:83], v[180:183]\n"
        "ds_read_b128 a[132:135], v241 offset:21504\n"
        "v_mfma_f32_16x16x32_bf16 v[184:187], a[136:139], v[80:83], v[184:187]\n"
        "v_exp_f32 v39, v39\n"
        "v_mfma_f32_16x16x32_bf16 v[216:219], a[136:139], v[84:87], v[216:219]\n"
        "v_exp_f32 v40, v40\n"
        "v_mfma_f32_16x16x32_bf16 a[24:27], a[136:139], v[88:91], a[24:27]\n"
        "v_exp_f32 v41, v41\n"
        "v_mfma_f32_16x16x32_bf16 a[56:59], a[136:139], v[92:95], a[56:59]\n"
        "ds_read_b128 a[136:139], v241 offset:22528\n"
        "s_waitcnt lgkmcnt(2)\n"
        "v_mfma_f32_16x16x32_bf16 a[60:63], a[140:143], v[92:95], a[60:63]\n"
        "v_exp_f32 v42, v42\n"
        "v_mfma_f32_16x16x32_bf16 a[28:31], a[140:143], v[88:91], a[28:31]\n"
        "v_exp_f32 v43, v43\n"
        "v_mfma_f32_16x16x32_bf16 v[220:223], a[140:143], v[84:87], v[220:223]\n"
        "v_exp_f32 v44, v44\n"
        "v_mfma_f32_16x16x32_bf16 v[188:191], a[140:143], v[80:83], v[188:191]\n"
        "ds_read_b128 a[140:143], v241 offset:23552\n"
        "v_cvt_pk_bf16_f32 v80, v32, v33\n"
        "v_mfma_f32_16x16x32_bf16 v[48:51], a[112:115], v[0:3], v[224:227]\n"
        "v_exp_f32 v45, v45\n"
        "v_mfma_f32_16x16x32_bf16 v[48:51], a[116:119], v[4:7], v[48:51]\n"
        "v_exp_f32 v46, v46\n"
        "v_mfma_f32_16x16x32_bf16 v[48:51], a[120:123], v[8:11], v[48:51]\n"
        "v_exp_f32 v47, v47\n"
        "v_mfma_f32_16x16x32_bf16 v[48:51], a[124:127], v[12:15], v[48:51]\n"
        "v_cvt_pk_bf16_f32 v81, v34, v35\n"
        "v_cvt_pk_bf16_f32 v84, v36, v37\n"
        "v_mfma_f32_16x16x32_bf16 v[52:55], a[124:127], v[76:79], v[228:231]\n"
        "v_cvt_pk_bf16_f32 v85, v38, v39\n"
        "v_cvt_pk_bf16_f32 v88, v40, v41\n"
        "v_mfma_f32_16x16x32_bf16 v[52:55], a[120:123], v[72:75], v[52:55]\n"
        "v_cvt_pk_bf16_f32 v89, v42, v43\n"
        "v_cvt_pk_bf16_f32 v92, v44, v45\n"
        "v_mfma_f32_16x16x32_bf16 v[52:55], a[116:119], v[68:71], v[52:55]\n"
        "v_cvt_pk_bf16_f32 v93, v46, v47\n"
        "v_mfma_f32_16x16x32_bf16 v[52:55], a[112:115], v[64:67], v[52:55]\n"
        "v_mfma_f32_16x16x32_bf16 v[56:59], a[112:115], v[96:99], v[232:235]\n"
        "v_mfma_f32_16x16x32_bf16 v[56:59], a[116:119], v[100:103], v[56:59]\n"
        "v_mfma_f32_16x16x32_bf16 v[56:59], a[120:123], v[104:107], v[56:59]\n"
        "v_mfma_f32_16x16x32_bf16 v[56:59], a[124:127], v[108:111], v[56:59]\n"
        "v_mfma_f32_16x16x32_bf16 v[60:63], a[124:127], v[140:143], v[236:239]\n"
        "v_mfma_f32_16x16x32_bf16 v[60:63], a[120:123], v[136:139], v[60:63]\n"
        "ds_read_b128 a[120:123], v240 offset:6144\n"
        "v_mfma_f32_16x16x32_bf16 v[60:63], a[116:119], v[132:135], v[60:63]\n"
        "ds_read_b128 a[116:119], v240 offset:5120\n"
        "v_mfma_f32_16x16x32_bf16 v[60:63], a[112:115], v[128:131], v[60:63]\n"
        "ds_read_b128 a[112:115], v240 offset:4096\n"
        "v_smfmac_f32_16x16x64_bf16 a[64:67], a[72:75], v[16:23], v154\n"
        "v_smfmac_f32_16x16x64_bf16 a[68:71], a[72:75], v[24:31], v154\n"
        "v_mfma_f32_16x16x32_bf16 v[160:163], a[144:147], v[16:19], v[160:163]\n"
        "v_mfma_f32_16x16x32_bf16 v[192:195], a[144:147], v[20:23], v[192:195]\n"
        "v_mfma_f32_16x16x32_bf16 a[0:3], a[144:147], v[24:27], a[0:3]\n"
        "v_mfma_f32_16x16x32_bf16 a[32:35], a[144:147], v[28:31], a[32:35]\n"
        "ds_read_b128 a[144:147], v241 offset:24576\n"
        "v_mfma_f32_16x16x32_bf16 a[36:39], a[148:151], v[28:31], a[36:39]\n"
        "v_mfma_f32_16x16x32_bf16 a[4:7], a[148:151], v[24:27], a[4:7]\n"
        "v_mfma_f32_16x16x32_bf16 v[196:199], a[148:151], v[20:23], v[196:199]\n"
        "v_mfma_f32_16x16x32_bf16 v[164:167], a[148:151], v[16:19], v[164:167]\n"
        "ds_read_b128 a[148:151], v241 offset:25600\n"
        "v_mfma_f32_16x16x32_bf16 v[168:171], a[152:155], v[16:19], v[168:171]\n"
        "v_mfma_f32_16x16x32_bf16 v[200:203], a[152:155], v[20:23], v[200:203]\n"
        "v_mfma_f32_16x16x32_bf16 a[8:11], a[152:155], v[24:27], a[8:11]\n"
        "v_mfma_f32_16x16x32_bf16 a[40:43], a[152:155], v[28:31], a[40:43]\n"
        "ds_read_b128 a[152:155], v241 offset:26624\n"
        "v_mfma_f32_16x16x32_bf16 a[44:47], a[156:159], v[28:31], a[44:47]\n"
        "v_mfma_f32_16x16x32_bf16 a[12:15], a[156:159], v[24:27], a[12:15]\n"
        "v_mfma_f32_16x16x32_bf16 v[204:207], a[156:159], v[20:23], v[204:207]\n"
        "s_cmp_lg_u32 s82, s24\n"
        "v_mfma_f32_16x16x32_bf16 v[172:175], a[156:159], v[16:19], v[172:175]\n"
        "s_cbranch_scc1 LDU_%=\n"
        "s_sub_u32 s97, s83, s82\n"
        "s_cmp_gt_u32 s97, 1\n"
        "s_cbranch_scc1 LLD_%=\n"
        "s_cmp_eq_u32 s97, 0\n"
        "s_cbranch_scc1 LE3D_%=\n"
        "global_load_dwordx2 v[252:253], v155, s[48:49] sc1\n"
        "v_accvgpr_read_b32 v246, a93\n"
        "global_load_dword v244, v246, s[46:47]\n"
        "s_branch LEWD_%=\n"
        "LE3D_%=:\n"
        "v_cmp_ne_u64 vcc, v[252:253], v[250:251]\n"
        "s_mov_b64 s[68:69], s[92:93]\n"
        "s_mov_b64 s[74:75], s[94:95]\n"
        "LHC0_%=:\n"
        "s_cbranch_execz LHRC_%=\n"
        "s_mov_b32 s80, s50\n"
        "s_mov_b32 s81, s51\n"
        "s_mov_b64 s[66:67], s[22:23]\n"
        "s_mov_b32 s90, s29\n"
        "s_mov_b32 s25, s30\n"
        "s_lshl_b32 s28, s28, 2\n"
        "s_xor_b32 s79, s73, 0x8000\n"
        "v_accvgpr_read_b32 v144, a84\n"
        "v_accvgpr_read_b32 v145, a85\n"
        "v_accvgpr_read_b32 v146, a86\n"
        "v_accvgpr_read_b32 v147, a87\n"
        "s_mov_b64 exec, 15\n"
        "ds_write_b128 v156, a[88:91]\n"
        "s_mov_b64 exec, -1\n"
        "s_cbranch_vccnz LE4D_%=\n"
        "LEWD_%=:\n"
        "s_add_u32 s24, s82, 1\n"
        "s_cmp_lg_u32 s82, s91\n"
        "s_cbranch_scc1 LDU_%=\n"
        "; ---- LDRAIN\n"
        "LLD_%=:\n"
        "v_mfma_f32_16x16x32_bf16 a[16:19], a[128:131], v[24:27], a[16:19]\n"
        "s_waitcnt lgkmcnt(4)\n"
        "v_exp_f32 v62, v62\n"
        "v_mfma_f32_16x16x32_bf16 a[48:51], a[128:131], v[28:31], a[48:51]\n"
        "s_mul_i32 s53, s44, 1052672\n"
        "v_exp_f32 v63, v63\n"
        "v_mfma_f32_16x16x32_bf16 a[52:55], a[132:135], v[28:31], a[52:55]\n"
        "ds_read_b128 a[172:175], v241 offset:27648\n"
        "s_lshl_b32 s96, s43, 15\n"
        "v_cvt_pk_bf16_f32 v95, v62, v63\n"
        "v_mfma_f32_16x16x32_bf16 a[20:23], a[132:135], v[24:27], a[20:23]\n"
        "s_add_u32 s53, s53, s96\n"
        "v_exp_f32 v59, v59\n"
        "v_mfma_f32_16x16x32_bf16 a[24:27], a[136:139], v[24:27], a[24:27]\n"
        "s_lshl_b32 s96, s40, 10\n"
        "v_exp_f32 v58, v58\n"
        "v_mfma_f32_16x16x32_bf16 a[56:59], a[136:139], v[28:31], a[56:59]\n"
        "ds_read_b128 a[176:179], v241 offset:28672\n"
        "s_add_u32 s53, s53, s96\n"
        "v_cvt_pk_bf16_f32 v91, v58, v59\n"
        "v_mfma_f32_16x16x32_bf16 a[60:63], a[140:143], v[28:31], a[60:63]\n"
        "s_add_u32 s53, s53, 0x2201000\n"
        "v_exp_f32 v57, v57\n"
        "v_mfma_f32_16x16x32_bf16 a[28:31], a[140:143], v[24:27], a[28:31]\n"
        "v_exp_f32 v56, v56\n"
        "v_exp_f32 v61, v61\n"
        "v_cvt_pk_bf16_f32 v90, v56, v57\n"
        "s_nop 1\n"
        "s_waitcnt lgkmcnt(4)\n"
        "v_mfma_f32_16x16x32_bf16 a[0:3], a[144:147], v[88:91], a[0:3]\n"
        "s_waitcnt lgkmcnt(2)\n"
        "v_exp_f32 v60, v60\n"
        "v_mfma_f32_16x16x32_bf16 a[4:7], a[148:151], v[88:91], a[4:7]\n"
        "ds_read_b128 a[180:183], v241 offset:29696\n"
        "s_sub_u32 s24, s83, 1\n"
        "v_cvt_pk_bf16_f32 v94, v60, v61\n"
        "s_nop 1\n"
        "v_smfmac_f32_16x16x64_bf16 a[68:71], a[72:75], v[88:95], v154\n"
        "ds_read_b128 a[184:187], v241 offset:30720\n"
        "s_add_u32 s70, s38, s53\n"
        "s_addc_u32 s71, s39, 0\n"
        "v_mfma_f32_16x16x32_bf16 a[32:35], a[144:147], v[92:95], a[32:35]\n"
        "ds_read_b128 a[188:191], v241 offset:31744\n"
        "s_add_u32 s91, s82, 1\n"
        "v_accvgpr_read_b32 v120, a0\n"
        "v_mfma_f32_16x16x32_bf16 a[36:39], a[148:151], v[92:95], a[36:39]\n"
        "s_waitcnt lgkmcnt(3)\n"
        "v_accvgpr_read_b32 v248, a68\n"
        "v_accvgpr_read_b32 v246, a69\n"
        "v_mfma_f32_16x16x32_bf16 a[8:11], a[152:155], v[88:91], a[8:11]\n"
        "s_max_u32 s24, s24, s91\n"
        "v_add_f32 v248, v248, v246\n"
        "v_accvgpr_read_b32 v249, a70\n"
        "v_mfma_f32_16x16x32_bf16 a[40:43], a[152:155], v[92:95], a[40:43]\n"
        "s_add_u32 s91, s83, 1\n"
        "v_rcp_f32 v246, v248\n"
        "v_mfma_f32_16x16x32_bf16 a[44:47], a[172:175], v[92:95], a[44:47]\n"
        "v_accvgpr_read_b32 v247, a71\n"
        "v_add_f32 v249, v249, v247\n"
        "v_mfma_f32_16x16x32_bf16 a[12:15], a[172:175], v[88:91], a[12:15]\n"
        "v_rcp_f32 v247, v249\n"
        "v_mfma_f32_16x16x32_bf16 a[16:19], a[176:179], v[88:91], a[16:19]\n"
        "s_waitcnt lgkmcnt(2)\n"
        "v_accvgpr_read_b32 v112, a32\n"
        "v_accvgpr_read_b32 v113, a33\n"
        "v_mfma_f32_16x16x32_bf16 a[48:51], a[176:179], v[92:95], a[48:51]\n"
        "s_waitcnt lgkmcnt(1)\n"
        "v_accvgpr_read_b32 v114, a34\n"
        "v_accvgpr_read_b32 v115, a35\n"
        "v_mfma_f32_16x16x32_bf16 a[52:55], a[180:183], v[92:95], a[52:55]\n"
        "s_waitcnt lgkmcnt(0)\n"
        "v_accvgpr_read_b32 v116, a36\n"
        "v_accvgpr_read_b32 v117, a37\n"
        "v_mfma_f32_16x16x32_bf16 a[20:23], a[180:183], v[88:91], a[20:23]\n"
        "v_accvgpr_read_b32 v118, a38\n"
        "v_accvgpr_read_b32 v119, a39\n"
        "v_mfma_f32_16x16x32_bf16 a[24:27], a[184:187], v[88:91], a[24:27]\n"
        "v_accvgpr_read_b32 v121, a1\n"
        "v_accvgpr_read_b32 v122, a2\n"
        "v_mfma_f32_16x16x32_bf16 a[56:59], a[184:187], v[92:95], a[56:59]\n"
        "v_accvgpr_read_b32 v123, a3\n"
        "v_accvgpr_read_b32 v124, a4\n"
        "v_mfma_f32_16x16x32_bf16 a[60:63], a[188:191], v[92:95], a[60:63]\n"
        "v_accvgpr_read_b32 v125, a5\n"
        "v_accvgpr_read_b32 v126, a6\n"
        "v_mfma_f32_16x16x32_bf16 a[28:31], a[188:191], v[88:91], a[28:31]\n"
        "v_pk_mul_f32 v[112:113], v[112:113], v[246:247] op_sel:[0,1] op_sel_hi:[1,1]\n"
        "v_pk_mul_f32 v[114:115], v[114:115], v[246:247] op_sel:[0,1] op_sel_hi:[1,1]\n"
        "v_pk_mul_f32 v[116:117], v[116:117], v[246:247] op_sel:[0,1] op_sel_hi:[1,1]\n"
        "v_pk_mul_f32 v[118:119], v[118:119], v[246:247] op_sel:[0,1] op_sel_hi:[1,1]\n"
        "v_cvt_pk_bf16_f32 v112, v112, v113\n"
        "v_cvt_pk_bf16_f32 v113, v114, v115\n"
        "v_cvt_pk_bf16_f32 v114, v116, v117\n"
        "v_cvt_pk_bf16_f32 v115, v118, v119\n"
        "global_store_dwordx4 v254, v[112:115], s[70:71] offset:-4096 sc1\n"
        "v_accvgpr_read_b32 v127, a7\n"
        "v_pk_mul_f32 v[120:121], v[120:121], v[246:247] op_sel:[0,0] op_sel_hi:[1,0]\n"
        "v_pk_mul_f32 v[122:123], v[122:123], v[246:247] op_sel:[0,0] op_sel_hi:[1,0]\n"
        "v_pk_mul_f32 v[124:125], v[124:125], v[246:247] op_sel:[0,0] op_sel_hi:[1,0]\n"
        "v_pk_mul_f32 v[126:127], v[126:127], v[246:247] op_sel:[0,0] op_sel_hi:[1,0]\n"
        "v_cvt_pk_bf16_f32 v120, v120, v121\n"
        "v_cvt_pk_bf16_f32 v121, v122, v123\n"
        "v_cvt_pk_bf16_f32 v122, v124, v125\n"
        "v_cvt_pk_bf16_f32 v123, v126, v127\n"
        "global_store_dwordx4 v254, v[120:123], s[70:71] offset:0 sc1\n"
        "s_add_u32 s70, s70, 0x2000\n"
        "s_addc_u32 s71, s71, 0\n"
        "v_accvgpr_read_b32 v40, a40\n"
        "v_accvgpr_read_b32 v41, a41\n"
        "v_accvgpr_read_b32 v42, a42\n"
        "v_accvgpr_read_b32 v43, a43\n"
        "v_accvgpr_read_b32 v44, a44\n"
        "v_accvgpr_read_b32 v45, a45\n"
        "v_accvgpr_read_b32 v46, a46\n"
        "v_accvgpr_read_b32 v47, a47\n"
        "v_pk_mul_f32 v[40:41], v[40:41], v[246:247] op_sel:[0,1] op_sel_hi:[1,1]\n"
        "v_pk_mul_f32 v[42:43], v[42:43], v[246:247] op_sel:[0,1] op_sel_hi:[1,1]\n"
        "v_pk_mul_f32 v[44:45], v[44:45], v[246:247] op_sel:[0,1] op_sel_hi:[1,1]\n"
        "v_pk_mul_f32 v[46:47], v[46:47], v[246:247] op_sel:[0,1] op_sel_hi:[1,1]\n"
        "v_cvt_pk_bf16_f32 v40, v40, v41\n"
        "v_cvt_pk_bf16_f32 v41, v42, v43\n"
        "v_cvt_pk_bf16_f32 v42, v44, v45\n"
        "v_cvt_pk_bf16_f32 v43, v46, v47\n"
        "global_store_dwordx4 v254, v[40:43], s[70:71] offset:-4096 sc1\n"
        "v_accvgpr_read_b32 v56, a8\n"
        "v_accvgpr_read_b32 v57, a9\n"
        "v_accvgpr_read_b32 v58, a10\n"
        "v_accvgpr_read_b32 v59, a11\n"
        "v_accvgpr_read_b32 v60, a12\n"
        "v_accvgpr_read_b32 v61, a13\n"
        "v_accvgpr_read_b32 v62, a14\n"
        "v_accvgpr_read_b32 v63, a15\n"
        "v_pk_mul_f32 v[56:57], v[56:57], v[246:247] op_sel:[0,0] op_sel_hi:[1,0]\n"
        "v_pk_mul_f32 v[58:59], v[58:59], v[246:247] op_sel:[0,0] op_sel_hi:[1,0]\n"
        "v_pk_mul_f32 v[60:61], v[60:61], v[246:247] op_sel:[0,0] op_sel_hi:[1,0]\n"
        "v_pk_mul_f32 v[62:63], v[62:63], v[246:247] op_sel:[0,0] op_sel_hi:[1,0]\n"
        "v_cvt_pk_bf16_f32 v56, v56, v57\n"
        "v_cvt_pk_bf16_f32 v57, v58, v59\n"
        "v_cvt_pk_bf16_f32 v58, v60, v61\n"
        "v_cvt_pk_bf16_f32 v59, v62, v63\n"
        "global_store_dwordx4 v254, v[56:59], s[70:71] offset:0 sc1\n"
        "s_add_u32 s70, s70, 0x2000\n"
        "s_addc_u32 s71, s71, 0\n"
        "v_accvgpr_read_b32 v112, a48\n"
        "v_accvgpr_read_b32 v113, a49\n"
        "v_accvgpr_read_b32 v114, a50\n"
        "v_accvgpr_read_b32 v115, a51\n"
        "v_accvgpr_read_b32 v116, a52\n"
        "v_accvgpr_read_b32 v117, a53\n"
        "v_accvgpr_read_b32 v118, a54\n"
        "v_accvgpr_read_b32 v119, a55\n"
        "v_pk_mul_f32 v[112:113], v[112:113], v[246:247] op_sel:[0,1] op_sel_hi:[1,1]\n"
        "v_pk_mul_f32 v[114:115], v[114:115], v[246:247] op_sel:[0,1] op_sel_hi:[1,1]\n"
        "v_pk_mul_f32 v[116:117], v[116:117], v[246:247] op_sel:[0,1] op_sel_hi:[1,1]\n"
        "v_pk_mul_f32 v[118:119], v[118:119], v[246:247] op_sel:[0,1] op_sel_hi:[1,1]\n"
        "v_cvt_pk_bf16_f32 v112, v112, v113\n"
        "v_cvt_pk_bf16_f32 v113, v114, v115\n"
        "v_cvt_pk_bf16_f32 v114, v116, v117\n"
        "v_cvt_pk_bf16_f32 v115, v118, v119\n"
        "global_store_dwordx4 v254, v[112:115], s[70:71] offset:-4096 sc1\n"
        "v_accvgpr_read_b32 v120, a16\n"
        "v_accvgpr_read_b32 v121, a17\n"
        "v_accvgpr_read_b32 v122, a18\n"
        "v_accvgpr_read_b32 v123, a19\n"
        "v_accvgpr_read_b32 v124, a20\n"
        "v_accvgpr_read_b32 v125, a21\n"
        "v_accvgpr_read_b32 v126, a22\n"
        "v_accvgpr_read_b32 v127, a23\n"
        "v_pk_mul_f32 v[120:121], v[120:121], v[246:247] op_sel:[0,0] op_sel_hi:[1,0]\n"
        "v_pk_mul_f32 v[122:123], v[122:123], v[246:247] op_sel:[0,0] op_sel_hi:[1,0]\n"
        "v_pk_mul_f32 v[124:125], v[124:125], v[246:247] op_sel:[0,0] op_sel_hi:[1,0]\n"
        "v_pk_mul_f32 v[126:127], v[126:127], v[246:247] op_sel:[0,0] op_sel_hi:[1,0]\n"
        "v_cvt_pk_bf16_f32 v120, v120, v121\n"
        "v_cvt_pk_bf16_f32 v121, v122, v123\n"
        "v_cvt_pk_bf16_f32 v122, v124, v125\n"
        "v_cvt_pk_bf16_f32 v123, v126, v127\n"
        "global_store_dwordx4 v254, v[120:123], s[70:71] offset:0 sc1\n"
        "s_add_u32 s70, s70, 0x2000\n"
        "s_addc_u32 s71, s71, 0\n"
        "v_accvgpr_read_b32 v40, a56\n"
        "v_accvgpr_read_b32 v41, a57\n"
        "v_accvgpr_read_b32 v42, a58\n"
        "v_accvgpr_read_b32 v43, a59\n"
        "v_accvgpr_read_b32 v44, a60\n"
        "v_accvgpr_read_b32 v45, a61\n"
        "v_accvgpr_read_b32 v46, a62\n"
        "v_accvgpr_read_b32 v47, a63\n"
        "v_pk_mul_f32 v[40:41], v[40:41], v[246:247] op_sel:[0,1] op_sel_hi:[1,1]\n"
        "v_pk_mul_f32 v[42:43], v[42:43], v[246:247] op_sel:[0,1] op_sel_hi:[1,1]\n"
        "v_pk_mul_f32 v[44:45], v[44:45], v[246:247] op_sel:[0,1] op_sel_hi:[1,1]\n"
        "v_pk_mul_f32 v[46:47], v[46:47], v[246:247] op_sel:[0,1] op_sel_hi:[1,1]\n"
        "v_cvt_pk_bf16_f32 v40, v40, v41\n"
        "v_cvt_pk_bf16_f32 v41, v42, v43\n"
        "v_cvt_pk_bf16_f32 v42, v44, v45\n"
        "v_cvt_pk_bf16_f32 v43, v46, v47\n"
        "global_store_dwordx4 v254, v[40:43], s[70:71] offset:-4096 sc1\n"
        "v_accvgpr_read_b32 v56, a24\n"
        "v_accvgpr_read_b32 v57, a25\n"
        "v_accvgpr_read_b32 v58, a26\n"
        "v_accvgpr_read_b32 v59, a27\n"
        "v_accvgpr_read_b32 v60, a28\n"
        "v_accvgpr_read_b32 v61, a29\n"
        "v_accvgpr_read_b32 v62, a30\n"
        "v_accvgpr_read_b32 v63, a31\n"
        "v_pk_mul_f32 v[56:57], v[56:57], v[246:247] op_sel:[0,0] op_sel_hi:[1,0]\n"
        "v_pk_mul_f32 v[58:59], v[58:59], v[246:247] op_sel:[0,0] op_sel_hi:[1,0]\n"
        "v_pk_mul_f32 v[60:61], v[60:61], v[246:247] op_sel:[0,0] op_sel_hi:[1,0]\n"
        "v_pk_mul_f32 v[62:63], v[62:63], v[246:247] op_sel:[0,0] op_sel_hi:[1,0]\n"
        "v_cvt_pk_bf16_f32 v56, v56, v57\n"
        "v_cvt_pk_bf16_f32 v57, v58, v59\n"
        "v_cvt_pk_bf16_f32 v58, v60, v61\n"
        "v_cvt_pk_bf16_f32 v59, v62, v63\n"
        "global_store_dwordx4 v254, v[56:59], s[70:71] offset:0 sc1\n"
        "v_add_f32 v247, v248, v249\n"
        "v_cmp_nge_f32 vcc, 0x4e6e6b28, v247\n"
        "s_nop 0\n"
        "s_cbranch_vccnz LRL_%=\n"
        "LSL_%=:\n"
        "s_cmp_eq_u32 s44, 7\n"
        "s_cbranch_scc1 LTL_%=\n"
        "; ---- SINGLE\n"
        "s_branch LSI_%=\n"
        "s_nop 0\n"
        "s_nop 0\n"
        "s_nop 0\n"
        "s_nop 0\n"
        "s_nop 0\n"
        "s_nop 0\n"
        "s_nop 0\n"
        "LSI_%=:\n"
        "v_mfma_f32_16x16x32_bf16 v[32:35], a[96:99], v[0:3], v[224:227]\n"
        "s_waitcnt lgkmcnt(4)\n"
        "ds_read_b128 a[156:159], v241 offset:27648\n"
        "s_add_u32 m0, s80, 0x0\n"
        "v_mfma_f32_16x16x32_bf16 v[32:35], a[100:103], v[4:7], v[32:35]\n"
        "ds_read_b128 a[124:127], v240 offset:7168\n"
        "global_load_lds_dwordx4 v254, s[68:69] offset:0 sc1\n"
        "s_xor_b32 s79, s79, 0x8000\n"
        "v_mfma_f32_16x16x32_bf16 v[32:35], a[104:107], v[8:11], v[32:35]\n"
        "s_add_u32 s82, s82, 1\n"
        "v_exp_f32 v48, v48\n"
        "v_mfma_f32_16x16x32_bf16 v[32:35], a[108:111], v[12:15], v[32:35]\n"
        "s_xor_b32 s78, s78, 0x8000\n"
        "v_exp_f32 v49, v49\n"
        "v_mfma_f32_16x16x32_bf16 v[36:39], a[108:111], v[76:79], v[228:231]\n"
        "ds_read_b128 a[108:111], v240 offset:11264\n"
        "s_mov_b32 s80, s78\n"
        "v_cvt_pk_bf16_f32 v82, v48, v49\n"
        "v_mfma_f32_16x16x32_bf16 v[36:39], a[104:107], v[72:75], v[36:39]\n"
        "ds_read_b128 a[104:107], v240 offset:10240\n"
        "global_load_lds_dwordx4 v254, s[68:69] offset:1024 sc1\n"
        "v_mfma_f32_16x16x32_bf16 v[36:39], a[100:103], v[68:71], v[36:39]\n"
        "v_exp_f32 v50, v50\n"
        "v_mfma_f32_16x16x32_bf16 v[36:39], a[96:99], v[64:67], v[36:39]\n"
        "v_exp_f32 v51, v51\n"
        "v_mfma_f32_16x16x32_bf16 v[176:179], a[128:131], v[16:19], v[176:179]\n"
        "ds_read_b128 a[96:99], v240 offset:8192\n"
        "v_cvt_pk_bf16_f32 v83, v50, v51\n"
        "v_mfma_f32_16x16x32_bf16 v[208:211], a[128:131], v[20:23], v[208:211]\n"
        "ds_read_b128 a[128:131], v241 offset:28672\n"
        "global_load_lds_dwordx4 v254, s[68:69] offset:2048 sc1\n"
        "v_mfma_f32_16x16x32_bf16 v[212:215], a[132:135], v[20:23], v[212:215]\n"
        "v_exp_f32 v52, v52\n"
        "v_mfma_f32_16x16x32_bf16 v[180:183], a[132:135], v[16:19], v[180:183]\n"
        "v_exp_f32 v53, v53\n"
        "v_mfma_f32_16x16x32_bf16 v[184:187], a[136:139], v[16:19], v[184:187]\n"
        "ds_read_b128 a[100:103], v240 offset:9216\n"
        "v_cvt_pk_bf16_f32 v86, v52, v53\n"
        "v_mfma_f32_16x16x32_bf16 v[216:219], a[136:139], v[20:23], v[216:219]\n"
        "ds_read_b128 a[132:135], v241 offset:29696\n"
        "global_load_lds_dwordx4 v254, s[68:69] offset:3072 sc1\n"
        "s_add_u32 m0, s81, 0x0\n"
        "v_mfma_f32_16x16x32_bf16 v[220:223], a[140:143], v[20:23], v[220:223]\n"
        "s_waitcnt lgkmcnt(4)\n"
        "v_exp_f32 v54, v54\n"
        "v_mfma_f32_16x16x32_bf16 v[188:191], a[140:143], v[16:19], v[188:191]\n"
        "s_add_u32 s81, s78, 4096\n"
        "v_exp_f32 v55, v55\n"
        "v_mfma_f32_16x16x32_bf16 v[48:51], a[112:115], v[0:3], v[224:227]\n"
        "ds_read_b128 a[136:139], v241 offset:30720\n"
        "v_cvt_pk_bf16_f32 v87, v54, v55\n"
        "v_mfma_f32_16x16x32_bf16 v[48:51], a[116:119], v[4:7], v[48:51]\n"
        "ds_read_b128 a[140:143], v241 offset:31744\n"
        "global_load_lds_dwordx4 v254, s[74:75] offset:0 sc1\n"
        "v_mfma_f32_16x16x32_bf16 v[48:51], a[120:123], v[8:11], v[48:51]\n"
        "v_exp_f32 v32, v32\n"
        "v_mfma_f32_16x16x32_bf16 v[48:51], a[124:127], v[12:15], v[48:51]\n"
        "v_exp_f32 v33, v33\n"
        "v_mfma_f32_16x16x32_bf16 v[52:55], a[124:127], v[76:79], v[228:231]\n"
        "ds_read_b128 a[124:127], v240 offset:15360\n"
        "v_cvt_pk_bf16_f32 v16, v32, v33\n"
        "v_mfma_f32_16x16x32_bf16 v[52:55], a[120:123], v[72:75], v[52:55]\n"
        "ds_read_b128 a[120:123], v240 offset:14336\n"
        "global_load_lds_dwordx4 v254, s[74:75] offset:1024 sc1\n"
        "v_mfma_f32_16x16x32_bf16 v[52:55], a[116:119], v[68:71], v[52:55]\n"
        "v_exp_f32 v34, v34\n"
        "v_mfma_f32_16x16x32_bf16 v[52:55], a[112:115], v[64:67], v[52:55]\n"
        "v_exp_f32 v35, v35\n"
        "v_smfmac_f32_16x16x64_bf16 a[64:67], a[72:75], v[80:87], v154\n"
        "ds_read_b128 a[112:115], v240 offset:12288\n"
        "v_cvt_pk_bf16_f32 v17, v34, v35\n"
        "v_mfma_f32_16x16x32_bf16 v[160:163], a[144:147], v[80:83], v[160:163]\n"
        "ds_read_b128 a[116:119], v240 offset:13312\n"
        "global_load_lds_dwordx4 v254, s[74:75] offset:2048 sc1\n"
        "v_mfma_f32_16x16x32_bf16 v[192:195], a[144:147], v[84:87], v[192:195]\n"
        "s_waitcnt vmcnt(8) lgkmcnt(4)\n"
        "v_exp_f32 v38, v38\n"
        "v_mfma_f32_16x16x32_bf16 v[196:199], a[148:151], v[84:87], v[196:199]\n"
        "s_barrier\n"
        "v_mfma_f32_16x16x32_bf16 v[164:167], a[148:151], v[80:83], v[164:167]\n"
        "v_exp_f32 v39, v39\n"
        "v_mfma_f32_16x16x32_bf16 v[168:171], a[152:155], v[80:83], v[168:171]\n"
        "v_exp_f32 v36, v36\n"
        "v_mfma_f32_16x16x32_bf16 v[200:203], a[152:155], v[84:87], v[200:203]\n"
        "v_exp_f32 v37, v37\n"
        "v_mfma_f32_16x16x32_bf16 v[204:207], a[156:159], v[84:87], v[204:207]\n"
        "v_exp_f32 v48, v48\n"
        "v_mfma_f32_16x16x32_bf16 v[172:175], a[156:159], v[80:83], v[172:175]\n"
        "v_exp_f32 v49, v49\n"
        "v_mfma_f32_16x16x32_bf16 v[32:35], a[96:99], v[0:3], v[224:227]\n"
        "v_exp_f32 v50, v50\n"
        "v_mfma_f32_16x16x32_bf16 v[32:35], a[100:103], v[4:7], v[32:35]\n"
        "v_exp_f32 v51, v51\n"
        "v_mfma_f32_16x16x32_bf16 v[32:35], a[104:107], v[8:11], v[32:35]\n"
        "v_exp_f32 v52, v52\n"
        "v_mfma_f32_16x16x32_bf16 v[32:35], a[108:111], v[12:15], v[32:35]\n"
        "v_exp_f32 v53, v53\n"
        "v_mfma_f32_16x16x32_bf16 v[176:179], a[128:131], v[80:83], v[176:179]\n"
        "v_exp_f32 v54, v54\n"
        "v_mfma_f32_16x16x32_bf16 v[208:211], a[128:131], v[84:87], v[208:211]\n"
        "v_exp_f32 v55, v55\n"
        "v_mfma_f32_16x16x32_bf16 v[212:215], a[132:135], v[84:87], v[212:215]\n"
        "global_load_lds_dwordx4 v254, s[74:75] offset:3072 sc1\n"
        "s_add_u32 m0, s79, 0x0\n"
        "v_cvt_pk_bf16_f32 v21, v38, v39\n"
        "v_mfma_f32_16x16x32_bf16 v[180:183], a[132:135], v[80:83], v[180:183]\n"
        "v_exp_f32 v32, v32\n"
        "v_mfma_f32_16x16x32_bf16 v[184:187], a[136:139], v[80:83], v[184:187]\n"
        "v_exp_f32 v33, v33\n"
        "v_mfma_f32_16x16x32_bf16 v[216:219], a[136:139], v[84:87], v[216:219]\n"
        "v_exp_f32 v34, v34\n"
        "v_mfma_f32_16x16x32_bf16 v[220:223], a[140:143], v[84:87], v[220:223]\n"
        "v_exp_f32 v35, v35\n"
        "v_mfma_f32_16x16x32_bf16 v[188:191], a[140:143], v[80:83], v[188:191]\n"
        "v_cvt_pk_bf16_f32 v20, v36, v37\n"
        "v_xor_b32 v241, 0x8000, v241\n"
        "v_mfma_f32_16x16x32_bf16 v[36:39], a[108:111], v[76:79], v[228:231]\n"
        "ds_read_b128 a[144:147], v241 offset:0\n"
        "s_add_u32 s76, s66, s90\n"
        "s_addc_u32 s77, s67, 0\n"
        "v_mfma_f32_16x16x32_bf16 v[36:39], a[104:107], v[72:75], v[36:39]\n"
        "ds_read_b128 a[148:151], v241 offset:1024\n"
        "global_load_lds_dwordx4 v144, s[76:77] offset:0 sc1\n"
        "v_mfma_f32_16x16x32_bf16 v[36:39], a[100:103], v[68:71], v[36:39]\n"
        "ds_read_b128 a[152:155], v241 offset:2048\n"
        "v_cvt_pk_bf16_f32 v18, v48, v49\n"
        "v_mfma_f32_16x16x32_bf16 v[36:39], a[96:99], v[64:67], v[36:39]\n"
        "ds_read_b128 a[156:159], v241 offset:3072\n"
        "v_cvt_pk_bf16_f32 v19, v50, v51\n"
        "s_waitcnt lgkmcnt(4)\n"
        "v_mfma_f32_16x16x32_bf16 v[48:51], a[112:115], v[0:3], v[224:227]\n"
        "ds_read_b128 a[96:99], v240 offset:16384\n"
        "v_cvt_pk_bf16_f32 v22, v52, v53\n"
        "v_mfma_f32_16x16x32_bf16 v[48:51], a[116:119], v[4:7], v[48:51]\n"
        "ds_read_b128 a[128:131], v241 offset:4096\n"
        "global_load_lds_dwordx4 v145, s[76:77] offset:1024 sc1\n"
        "v_mfma_f32_16x16x32_bf16 v[48:51], a[120:123], v[8:11], v[48:51]\n"
        "ds_read_b128 a[100:103], v240 offset:17408\n"
        "v_cvt_pk_bf16_f32 v23, v54, v55\n"
        "v_mfma_f32_16x16x32_bf16 v[48:51], a[124:127], v[12:15], v[48:51]\n"
        "ds_read_b128 a[132:135], v241 offset:5120\n"
        "v_cvt_pk_bf16_f32 v80, v32, v33\n"
        "v_mfma_f32_16x16x32_bf16 v[52:55], a[124:127], v[76:79], v[228:231]\n"
        "ds_read_b128 a[136:139], v241 offset:6144\n"
        "v_cvt_pk_bf16_f32 v81, v34, v35\n"
        "v_mfma_f32_16x16x32_bf16 v[52:55], a[120:123], v[72:75], v[52:55]\n"
        "s_waitcnt lgkmcnt(7)\n"
        "ds_read_b128 a[104:107], v240 offset:18432\n"
        "v_mfma_f32_16x16x32_bf16 v[52:55], a[116:119], v[68:71], v[52:55]\n"
        "ds_read_b128 a[140:143], v241 offset:7168\n"
        "global_load_lds_dwordx4 v146, s[76:77] offset:2048 sc1\n"
        "v_mfma_f32_16x16x32_bf16 v[52:55], a[112:115], v[64:67], v[52:55]\n"
        "ds_read_b128 a[108:111], v240 offset:19456\n"
        "s_cmp_ge_u32 s82, s44\n"
        "s_addc_u32 s90, s82, 0\n"
        "v_smfmac_f32_16x16x64_bf16 a[64:67], a[72:75], v[16:23], v154\n"
        "ds_read_b128 a[112:115], v240 offset:20480\n"
        "s_lshl_b32 s90, s90, 15\n"
        "v_mfma_f32_16x16x32_bf16 v[160:163], a[144:147], v[16:19], v[160:163]\n"
        "s_waitcnt lgkmcnt(7)\n"
        "v_exp_f32 v36, v36\n"
        "v_mfma_f32_16x16x32_bf16 v[192:195], a[144:147], v[20:23], v[192:195]\n"
        "ds_read_b128 a[144:147], v241 offset:8192\n"
        "global_load_lds_dwordx4 v147, s[76:77] offset:3072 sc1\n"
        "s_add_u32 m0, s79, s28\n"
        "v_mfma_f32_16x16x32_bf16 v[196:199], a[148:151], v[20:23], v[196:199]\n"
        "ds_read_b128 a[116:119], v240 offset:21504\n"
        "s_add_u32 s76, s76, s25\n"
        "s_addc_u32 s77, s77, 0\n"
        "v_mfma_f32_16x16x32_bf16 v[164:167], a[148:151], v[16:19], v[164:167]\n"
        "v_exp_f32 v37, v37\n"
        "v_mfma_f32_16x16x32_bf16 v[168:171], a[152:155], v[16:19], v[168:171]\n"
        "ds_read_b128 a[148:151], v241 offset:9216\n"
        "v_cvt_pk_bf16_f32 v84, v36, v37\n"
        "v_mfma_f32_16x16x32_bf16 v[200:203], a[152:155], v[20:23], v[200:203]\n"
        "s_waitcnt lgkmcnt(5)\n"
        "ds_read_b128 a[152:155], v241 offset:10240\n"
        "v_mfma_f32_16x16x32_bf16 v[204:207], a[156:159], v[20:23], v[204:207]\n"
        "ds_read_b128 a[120:123], v240 offset:22528\n"
        "global_load_lds_dwordx4 v144, s[76:77] offset:0 sc1\n"
        "v_mfma_f32_16x16x32_bf16 v[172:175], a[156:159], v[16:19], v[172:175]\n"
        "s_waitcnt lgkmcnt(5)\n"
        "v_exp_f32 v38, v38\n"
        "v_mfma_f32_16x16x32_bf16 v[32:35], a[96:99], v[0:3], v[224:227]\n"
        "v_exp_f32 v39, v39\n"
        "v_mfma_f32_16x16x32_bf16 v[32:35], a[100:103], v[4:7], v[32:35]\n"
        "ds_read_b128 a[156:159], v241 offset:11264\n"
        "v_cvt_pk_bf16_f32 v85, v38, v39\n"
        "v_mfma_f32_16x16x32_bf16 v[32:35], a[104:107], v[8:11], v[32:35]\n"
        "ds_read_b128 a[124:127], v240 offset:23552\n"
        "global_load_lds_dwordx4 v145, s[76:77] offset:1024 sc1\n"
        "v_mfma_f32_16x16x32_bf16 v[32:35], a[108:111], v[12:15], v[32:35]\n"
        "v_exp_f32 v48, v48\n"
        "v_mfma_f32_16x16x32_bf16 v[36:39], a[108:111], v[76:79], v[228:231]\n"
        "v_exp_f32 v49, v49\n"
        "v_mfma_f32_16x16x32_bf16 v[36:39], a[104:107], v[72:75], v[36:39]\n"
        "ds_read_b128 a[104:107], v240 offset:26624\n"
        "v_cvt_pk_bf16_f32 v82, v48, v49\n"
        "v_mfma_f32_16x16x32_bf16 v[36:39], a[100:103], v[68:71], v[36:39]\n"
        "ds_read_b128 a[100:103], v240 offset:25600\n"
        "global_load_lds_dwordx4 v146, s[76:77] offset:2048 sc1\n"
        "v_mfma_f32_16x16x32_bf16 v[36:39], a[96:99], v[64:67], v[36:39]\n"
        "v_exp_f32 v50, v50\n"
        "v_mfma_f32_16x16x32_bf16 v[176:179], a[128:131], v[16:19], v[176:179]\n"
        "v_exp_f32 v51, v51\n"
        "v_mfma_f32_16x16x32_bf16 v[208:211], a[128:131], v[20:23], v[208:211]\n"
        "ds_read_b128 a[96:99], v240 offset:24576\n"
        "v_cvt_pk_bf16_f32 v83, v50, v51\n"
        "v_mfma_f32_16x16x32_bf16 v[212:215], a[132:135], v[20:23], v[212:215]\n"
        "ds_read_b128 a[128:131], v241 offset:12288\n"
        "global_load_lds_dwordx4 v147, s[76:77] offset:3072 sc1\n"
        "v_mfma_f32_16x16x32_bf16 v[180:183], a[132:135], v[16:19], v[180:183]\n"
        "v_exp_f32 v52, v52\n"
        "v_mfma_f32_16x16x32_bf16 v[184:187], a[136:139], v[16:19], v[184:187]\n"
        "v_exp_f32 v53, v53\n"
        "v_mfma_f32_16x16x32_bf16 v[216:219], a[136:139], v[20:23], v[216:219]\n"
        "s_waitcnt lgkmcnt(3)\n"
        "ds_read_b128 a[132:135], v241 offset:13312\n"
        "v_mfma_f32_16x16x32_bf16 v[220:223], a[140:143], v[20:23], v[220:223]\n"
        "ds_read_b128 a[136:139], v241 offset:14336\n"
        "v_cvt_pk_bf16_f32 v86, v52, v53\n"
        "v_mfma_f32_16x16x32_bf16 v[188:191], a[140:143], v[16:19], v[188:191]\n"
        "v_exp_f32 v54, v54\n"
        "v_mfma_f32_16x16x32_bf16 v[48:51], a[112:115], v[0:3], v[224:227]\n"
        "v_exp_f32 v55, v55\n"
        "v_mfma_f32_16x16x32_bf16 v[48:51], a[116:119], v[4:7], v[48:51]\n"
        "ds_read_b128 a[140:143], v241 offset:15360\n"
        "v_cvt_pk_bf16_f32 v87, v54, v55\n"
        "v_mfma_f32_16x16x32_bf16 v[48:51], a[120:123], v[8:11], v[48:51]\n"
        "ds_read_b128 a[108:111], v240 offset:27648\n"
        "global_load_dword v244, v245, s[26:27] offset:0\n"
        "v_mfma_f32_16x16x32_bf16 v[48:51], a[124:127], v[12:15], v[48:51]\n"
        "v_exp_f32 v32, v32\n"
        "v_mfma_f32_16x16x32_bf16 v[52:55], a[124:127], v[76:79], v[228:231]\n"
        "ds_read_b128 a[124:127], v240 offset:31744\n"
        "s_add_u32 s26, s26, 0x20000\n"
        "s_addc_u32 s27, s27, 0\n"
        "v_mfma_f32_16x16x32_bf16 v[52:55], a[120:123], v[72:75], v[52:55]\n"
        "v_exp_f32 v33, v33\n"
        "v_mfma_f32_16x16x32_bf16 v[52:55], a[116:119], v[68:71], v[52:55]\n"
        "ds_read_b128 a[116:119], v240 offset:29696\n"
        "v_cvt_pk_bf16_f32 v16, v32, v33\n"
        "v_mfma_f32_16x16x32_bf16 v[52:55], a[112:115], v[64:67], v[52:55]\n"
        "v_exp_f32 v34, v34\n"
        "v_smfmac_f32_16x16x64_bf16 a[64:67], a[72:75], v[80:87], v154\n"
        "ds_read_b128 a[112:115], v240 offset:28672\n"
        "s_add_u32 s68, s64, s90\n"
        "s_addc_u32 s69, s65, 0\n"
        "v_mfma_f32_16x16x32_bf16 v[160:163], a[144:147], v[80:83], v[160:163]\n"
        "v_exp_f32 v35, v35\n"
        "v_mfma_f32_16x16x32_bf16 v[192:195], a[144:147], v[84:87], v[192:195]\n"
        "ds_read_b128 a[144:147], v241 offset:16384\n"
        "v_cvt_pk_bf16_f32 v17, v34, v35\n"
        "v_mfma_f32_16x16x32_bf16 v[196:199], a[148:151], v[84:87], v[196:199]\n"
        "v_exp_f32 v36, v36\n"
        "v_mfma_f32_16x16x32_bf16 v[164:167], a[148:151], v[80:83], v[164:167]\n"
        "v_exp_f32 v37, v37\n"
        "v_mfma_f32_16x16x32_bf16 v[168:171], a[152:155], v[80:83], v[168:171]\n"
        "s_waitcnt lgkmcnt(3)\n"
        "ds_read_b128 a[148:151], v241 offset:17408\n"
        "v_mfma_f32_16x16x32_bf16 v[200:203], a[152:155], v[84:87], v[200:203]\n"
        "ds_read_b128 a[152:155], v241 offset:18432\n"
        "v_cvt_pk_bf16_f32 v20, v36, v37\n"
        "v_mfma_f32_16x16x32_bf16 v[204:207], a[156:159], v[84:87], v[204:207]\n"
        "v_exp_f32 v38, v38\n"
        "v_mfma_f32_16x16x32_bf16 v[172:175], a[156:159], v[80:83], v[172:175]\n"
        "v_exp_f32 v39, v39\n"
        "v_mfma_f32_16x16x32_bf16 v[32:35], a[96:99], v[0:3], v[224:227]\n"
        "ds_read_b128 a[120:123], v240 offset:30720\n"
        "v_cvt_pk_bf16_f32 v21, v38, v39\n"
        "v_mfma_f32_16x16x32_bf16 v[32:35], a[100:103], v[4:7], v[32:35]\n"
        "ds_read_b128 a[156:159], v241 offset:19456\n"
        "v_xor_b32 v240, 0x8000, v240\n"
        "v_mfma_f32_16x16x32_bf16 v[32:35], a[104:107], v[8:11], v[32:35]\n"
        "v_exp_f32 v48, v48\n"
        "v_mfma_f32_16x16x32_bf16 v[32:35], a[108:111], v[12:15], v[32:35]\n"
        "v_exp_f32 v49, v49\n"
        "v_mfma_f32_16x16x32_bf16 v[36:39], a[108:111], v[76:79], v[228:231]\n"
        "v_exp_f32 v50, v50\n"
        "v_mfma_f32_16x16x32_bf16 v[36:39], a[104:107], v[72:75], v[36:39]\n"
        "v_exp_f32 v51, v51\n"
        "v_mfma_f32_16x16x32_bf16 v[36:39], a[100:103], v[68:71], v[36:39]\n"
        "v_exp_f32 v52, v52\n"
        "v_mfma_f32_16x16x32_bf16 v[36:39], a[96:99], v[64:67], v[36:39]\n"
        "v_exp_f32 v53, v53\n"
        "v_mfma_f32_16x16x32_bf16 v[176:179], a[128:131], v[80:83], v[176:179]\n"
        "s_waitcnt vmcnt(9) lgkmcnt(1)\n"
        "v_exp_f32 v54, v54\n"
        "v_mfma_f32_16x16x32_bf16 v[208:211], a[128:131], v[84:87], v[208:211]\n"
        "s_barrier\n"
        "v_mfma_f32_16x16x32_bf16 v[212:215], a[132:135], v[84:87], v[212:215]\n"
        "ds_read_b128 a[96:99], v240 offset:0\n"
        "v_cvt_pk_bf16_f32 v18, v48, v49\n"
        "v_mfma_f32_16x16x32_bf16 v[180:183], a[132:135], v[80:83], v[180:183]\n"
        "ds_read_b128 a[128:131], v241 offset:20480\n"
        "v_cvt_pk_bf16_f32 v19, v50, v51\n"
        "v_mfma_f32_16x16x32_bf16 v[184:187], a[136:139], v[80:83], v[184:187]\n"
        "ds_read_b128 a[100:103], v240 offset:1024\n"
        "s_add_u32 s74, s68, 0x1000\n"
        "s_addc_u32 s75, s69, 0\n"
        "v_mfma_f32_16x16x32_bf16 v[216:219], a[136:139], v[84:87], v[216:219]\n"
        "ds_read_b128 a[132:135], v241 offset:21504\n"
        "v_cvt_pk_bf16_f32 v22, v52, v53\n"
        "v_mfma_f32_16x16x32_bf16 v[220:223], a[140:143], v[84:87], v[220:223]\n"
        "v_exp_f32 v55, v55\n"
        "v_mfma_f32_16x16x32_bf16 v[188:191], a[140:143], v[80:83], v[188:191]\n"
        "ds_read_b128 a[136:139], v241 offset:22528\n"
        "v_cvt_pk_bf16_f32 v23, v54, v55\n"
        "v_mfma_f32_16x16x32_bf16 v[48:51], a[112:115], v[0:3], v[224:227]\n"
        "ds_read_b128 a[104:107], v240 offset:2048\n"
        "v_mfma_f32_16x16x32_bf16 v[48:51], a[116:119], v[4:7], v[48:51]\n"
        "ds_read_b128 a[140:143], v241 offset:23552\n"
        "v_mfma_f32_16x16x32_bf16 v[48:51], a[120:123], v[8:11], v[48:51]\n"
        "v_exp_f32 v32, v32\n"
        "v_mfma_f32_16x16x32_bf16 v[48:51], a[124:127], v[12:15], v[48:51]\n"
        "s_waitcnt lgkmcnt(5)\n"
        "v_exp_f32 v33, v33\n"
        "v_mfma_f32_16x16x32_bf16 v[52:55], a[124:127], v[76:79], v[228:231]\n"
        "ds_read_b128 a[108:111], v240 offset:3072\n"
        "v_cvt_pk_bf16_f32 v80, v32, v33\n"
        "v_mfma_f32_16x16x32_bf16 v[52:55], a[120:123], v[72:75], v[52:55]\n"
        "v_exp_f32 v34, v34\n"
        "v_mfma_f32_16x16x32_bf16 v[52:55], a[116:119], v[68:71], v[52:55]\n"
        "v_exp_f32 v35, v35\n"
        "v_mfma_f32_16x16x32_bf16 v[52:55], a[112:115], v[64:67], v[52:55]\n"
        "ds_read_b128 a[112:115], v240 offset:4096\n"
        "v_cvt_pk_bf16_f32 v81, v34, v35\n"
        "v_smfmac_f32_16x16x64_bf16 a[64:67], a[72:75], v[16:23], v154\n"
        "ds_read_b128 a[116:119], v240 offset:5120\n"
        "v_mfma_f32_16x16x32_bf16 v[160:163], a[144:147], v[16:19], v[160:163]\n"
        "v_exp_f32 v36, v36\n"
        "v_mfma_f32_16x16x32_bf16 v[192:195], a[144:147], v[20:23], v[192:195]\n"
        "v_exp_f32 v37, v37\n"
        "v_mfma_f32_16x16x32_bf16 v[196:199], a[148:151], v[20:23], v[196:199]\n"
        "ds_read_b128 a[144:147], v241 offset:24576\n"
        "v_cvt_pk_bf16_f32 v84, v36, v37\n"
        "v_mfma_f32_16x16x32_bf16 v[164:167], a[148:151], v[16:19], v[164:167]\n"
        "ds_read_b128 a[148:151], v241 offset:25600\n"
        "v_mfma_f32_16x16x32_bf16 v[168:171], a[152:155], v[16:19], v[168:171]\n"
        "v_exp_f32 v38, v38\n"
        "v_mfma_f32_16x16x32_bf16 v[200:203], a[152:155], v[20:23], v[200:203]\n"
        "s_cmp_lg_u32 s82, s24\n"
        "v_exp_f32 v39, v39\n"
        "v_mfma_f32_16x16x32_bf16 v[204:207], a[156:159], v[20:23], v[204:207]\n"
        "ds_read_b128 a[152:155], v241 offset:26624\n"
        "v_cvt_pk_bf16_f32 v85, v38, v39\n"
        "v_mfma_f32_16x16x32_bf16 v[172:175], a[156:159], v[16:19], v[172:175]\n"
        "ds_read_b128 a[120:123], v240 offset:6144\n"
        "s_cbranch_scc1 LSI_%=\n"
        "LHC1_%=:\n"
        "s_cbranch_execz LHRC_%=\n"
        "s_sub_u32 s97, s83, s82\n"
        "s_cmp_gt_u32 s97, 1\n"
        "s_cbranch_scc1 LTL_%=\n"
        "s_cmp_eq_u32 s97, 0\n"
        "s_cbranch_scc1 LE3S_%=\n"
        "global_load_dwordx2 v[252:253], v155, s[48:49] sc1\n"
        "v_accvgpr_read_b32 v246, a93\n"
        "global_load_dword v244, v246, s[46:47]\n"
        "s_branch LEWS_%=\n"
        "LE3S_%=:\n"
        "v_cmp_ne_u64 vcc, v[252:253], v[250:251]\n"
        "s_mov_b64 s[68:69], s[92:93]\n"
        "s_mov_b64 s[74:75], s[94:95]\n"
        "s_mov_b32 s80, s50\n"
        "s_mov_b32 s81, s51\n"
        "s_mov_b64 s[66:67], s[22:23]\n"
        "s_mov_b32 s90, s29\n"
        "s_mov_b32 s25, s30\n"
        "s_lshl_b32 s28, s28, 2\n"
        "s_xor_b32 s79, s73, 0x8000\n"
        "v_accvgpr_read_b32 v144, a84\n"
        "v_accvgpr_read_b32 v145, a85\n"
        "v_accvgpr_read_b32 v146, a86\n"
        "v_accvgpr_read_b32 v147, a87\n"
        "s_mov_b64 exec, 15\n"
        "ds_write_b128 v156, a[88:91]\n"
        "s_mov_b64 exec, -1\n"
        "s_cbranch_vccnz LE4S_%=\n"
        "LEWS_%=:\n"
        "s_add_u32 s24, s82, 1\n"
        "s_cmp_lg_u32 s82, s91\n"
        "s_cbranch_scc1 LSI_%=\n"
        "; ---- BACK\n"
        "LTL_%=:\n"
        "v_mfma_f32_16x16x32_bf16 v[176:179], a[128:131], v[16:19], v[176:179]\n"
        "s_waitcnt lgkmcnt(4)\n"
        "ds_read_b128 a[156:159], v241 offset:27648\n"
        "s_mul_i32 s53, s45, 1052672\n"
        "v_mfma_f32_16x16x32_bf16 v[208:211], a[128:131], v[20:23], v[208:211]\n"
        "s_lshl_b32 s96, s43, 15\n"
        "v_exp_f32 v48, v48\n"
        "v_mfma_f32_16x16x32_bf16 v[212:215], a[132:135], v[20:23], v[212:215]\n"
        "s_add_u32 s53, s53, s96\n"
        "v_exp_f32 v49, v49\n"
        "v_mfma_f32_16x16x32_bf16 v[180:183], a[132:135], v[16:19], v[180:183]\n"
        "ds_read_b128 a[128:131], v241 offset:28672\n"
        "s_lshl_b32 s96, s40, 10\n"
        "v_cvt_pk_bf16_f32 v82, v48, v49\n"
        "v_mfma_f32_16x16x32_bf16 v[184:187], a[136:139], v[16:19], v[184:187]\n"
        "s_add_u32 s53, s53, s96\n"
        "v_exp_f32 v50, v50\n"
        "v_mfma_f32_16x16x32_bf16 v[216:219], a[136:139], v[20:23], v[216:219]\n"
        "s_add_u32 s53, s53, 0x2201000\n"
        "v_exp_f32 v51, v51\n"
        "v_mfma_f32_16x16x32_bf16 v[220:223], a[140:143], v[20:23], v[220:223]\n"
        "s_waitcnt lgkmcnt(3)\n"
        "ds_read_b128 a[132:135], v241 offset:29696\n"
        "s_add_u32 s16, s51, 4096\n"
        "v_mfma_f32_16x16x32_bf16 v[188:191], a[140:143], v[16:19], v[188:191]\n"
        "v_cvt_pk_bf16_f32 v83, v50, v51\n"
        "s_nop 1\n"
        "v_mfma_f32_16x16x32_bf16 v[160:163], a[144:147], v[80:83], v[160:163]\n"
        "s_lshl_b32 s14, s44, 5\n"
        "v_exp_f32 v52, v52\n"
        "v_mfma_f32_16x16x32_bf16 v[164:167], a[148:151], v[80:83], v[164:167]\n"
        "s_add_u32 s14, s14, s43\n"
        "v_exp_f32 v53, v53\n"
        "v_mfma_f32_16x16x32_bf16 v[168:171], a[152:155], v[80:83], v[168:171]\n"
        "s_waitcnt lgkmcnt(2)\n"
        "ds_read_b128 a[136:139], v241 offset:30720\n"
        "s_lshl2_add_u32 s14, s14, s40\n"
        "v_mfma_f32_16x16x32_bf16 v[172:175], a[156:159], v[80:83], v[172:175]\n"
        "s_waitcnt lgkmcnt(2)\n"
        "ds_read_b128 a[140:143], v241 offset:31744\n"
        "s_lshl_b32 s14, s14, 8\n"
        "v_mfma_f32_16x16x32_bf16 v[176:179], a[128:131], v[80:83], v[176:179]\n"
        "s_add_u32 s70, s38, s53\n"
        "s_addc_u32 s71, s39, 0\n"
        "s_add_u32 s14, s14, 131072\n"
        "s_add_u32 m0, s73, 0x8000\n"
        "s_xor_b32 s66, s29, 0x40000\n"
        "v_cvt_pk_bf16_f32 v86, v52, v53\n"
        "v_add_u32 v230, 3584, v152\n"
        "s_waitcnt lgkmcnt(2)\n"
        "v_mfma_f32_16x16x32_bf16 v[180:183], a[132:135], v[80:83], v[180:183]\n"
        "s_add_u32 s60, s22, s66\n"
        "s_addc_u32 s61, s23, 0\n"
        "s_xor_b32 s66, s29, 0x60000\n"
        "s_cmp_le_u32 s10, 0\n"
        "s_addc_u32 s74, 0, 0\n"
        "s_cmp_le_u32 s10, 1\n"
        "s_addc_u32 s75, 1, 0\n"
        "s_cmp_le_u32 s10, 2\n"
        "s_addc_u32 s76, 2, 0\n"
        "s_sub_u32 s62, 15, s12\n"
        "s_cmp_lt_u32 s10, 2\n"
        "s_cselect_b32 s62, s12, s62\n"
        "s_lshl2_add_u32 s63, s11, s40\n"
        "v_exp_f32 v54, v54\n"
        "s_waitcnt lgkmcnt(1)\n"
        "v_exp_f32 v55, v55\n"
        "v_mfma_f32_16x16x32_bf16 v[184:187], a[136:139], v[80:83], v[184:187]\n"
        "s_waitcnt lgkmcnt(0)\n"
        "v_cvt_pk_bf16_f32 v87, v54, v55\n"
        "s_nop 1\n"
        "v_smfmac_f32_16x16x64_bf16 a[64:67], a[72:75], v[80:87], v154\n"
        "ds_read2_b32 a[0:1], v148 offset0:0 offset1:1\n"
        "s_lshl_b32 s63, s63, 5\n"
        "v_min_u32 v230, s16, v230\n"
        "v_mfma_f32_16x16x32_bf16 v[192:195], a[144:147], v[84:87], v[192:195]\n"
        "ds_read2_b32 a[2:3], v148 offset0:2 offset1:3\n"
        "v_add_u32 v231, 3648, v152\n"
        "v_mfma_f32_16x16x32_bf16 v[196:199], a[148:151], v[84:87], v[196:199]\n"
        "ds_read_b128 a[192:195], v230 offset:0\n"
        "v_accvgpr_read_b32 v248, a64\n"
        "v_mfma_f32_16x16x32_bf16 v[200:203], a[152:155], v[84:87], v[200:203]\n"
        "ds_read2_b32 a[4:5], v148 offset0:128 offset1:129\n"
        "v_accvgpr_read_b32 v246, a65\n"
        "v_mfma_f32_16x16x32_bf16 v[204:207], a[156:159], v[84:87], v[204:207]\n"
        "ds_read2_b32 a[6:7], v148 offset0:130 offset1:131\n"
        "v_add_f32 v248, v248, v246\n"
        "v_mfma_f32_16x16x32_bf16 v[208:211], a[128:131], v[84:87], v[208:211]\n"
        "v_rcp_f32 v246, v248\n"
        "v_mfma_f32_16x16x32_bf16 v[212:215], a[132:135], v[84:87], v[212:215]\n"
        "ds_read_b128 a[196:199], v152 offset:3072\n"
        "v_accvgpr_read_b32 v249, a66\n"
        "v_mfma_f32_16x16x32_bf16 v[216:219], a[136:139], v[84:87], v[216:219]\n"
        "ds_read2_b32 a[8:9], v149 offset0:0 offset1:1\n"
        "v_accvgpr_read_b32 v247, a67\n"
        "v_mfma_f32_16x16x32_bf16 v[220:223], a[140:143], v[84:87], v[220:223]\n"
        "ds_read2_b32 a[10:11], v149 offset0:2 offset1:3\n"
        "v_add_f32 v249, v249, v247\n"
        "v_mfma_f32_16x16x32_bf16 v[188:191], a[140:143], v[80:83], v[188:191]\n"
        "v_rcp_f32 v247, v249\n"
        "s_waitcnt lgkmcnt(5)\n"
        "v_mul_f32 v192, v192, v247\n"
        "v_mul_f32 v193, v193, v247\n"
        "v_mul_f32 v194, v194, v247\n"
        "v_mul_f32 v195, v195, v247\n"
        "v_mul_f32 v196, v196, v247\n"
        "v_mul_f32 v197, v197, v247\n"
        "v_mul_f32 v198, v198, v247\n"
        "v_mul_f32 v199, v199, v247\n"
        "v_cvt_pk_bf16_f32 v192, v192, v193\n"
        "v_cvt_pk_bf16_f32 v193, v194, v195\n"
        "v_cvt_pk_bf16_f32 v194, v196, v197\n"
        "v_cvt_pk_bf16_f32 v195, v198, v199\n"
        "global_store_dwordx4 v254, v[192:195], s[70:71] offset:-4096 sc1\n"
        "ds_read_b128 a[200:203], v152 offset:2560\n"
        "s_waitcnt lgkmcnt(3)\n"
        "v_mul_f32 v160, v160, v246\n"
        "v_mul_f32 v161, v161, v246\n"
        "v_mul_f32 v162, v162, v246\n"
        "v_mul_f32 v163, v163, v246\n"
        "v_mul_f32 v164, v164, v246\n"
        "v_mul_f32 v165, v165, v246\n"
        "v_mul_f32 v166, v166, v246\n"
        "v_mul_f32 v167, v167, v246\n"
        "v_cvt_pk_bf16_f32 v160, v160, v161\n"
        "v_cvt_pk_bf16_f32 v161, v162, v163\n"
        "v_cvt_pk_bf16_f32 v162, v164, v165\n"
        "v_cvt_pk_bf16_f32 v163, v166, v167\n"
        "global_store_dwordx4 v254, v[160:163], s[70:71] offset:0 sc1\n"
        "s_add_u32 s70, s70, 0x2000\n"
        "s_addc_u32 s71, s71, 0\n"
        "s_waitcnt lgkmcnt(1)\n"
        "v_mul_f32 v200, v200, v247\n"
        "v_mul_f32 v201, v201, v247\n"
        "v_mul_f32 v202, v202, v247\n"
        "v_mul_f32 v203, v203, v247\n"
        "v_mul_f32 v204, v204, v247\n"
        "v_mul_f32 v205, v205, v247\n"
        "v_mul_f32 v206, v206, v247\n"
        "v_mul_f32 v207, v207, v247\n"
        "v_cvt_pk_bf16_f32 v200, v200, v201\n"
        "v_cvt_pk_bf16_f32 v201, v202, v203\n"
        "v_cvt_pk_bf16_f32 v202, v204, v205\n"
        "v_cvt_pk_bf16_f32 v203, v206, v207\n"
        "global_store_dwordx4 v254, v[200:203], s[70:71] offset:-4096 sc1\n"
        "ds_read2_b32 a[12:13], v149 offset0:128 offset1:129\n"
        "ds_read2_b32 a[14:15], v149 offset0:130 offset1:131\n"
        "v_mul_f32 v168, v168, v246\n"
        "v_mul_f32 v169, v169, v246\n"
        "v_mul_f32 v170, v170, v246\n"
        "v_mul_f32 v171, v171, v246\n"
        "v_mul_f32 v172, v172, v246\n"
        "v_mul_f32 v173, v173, v246\n"
        "v_mul_f32 v174, v174, v246\n"
        "v_mul_f32 v175, v175, v246\n"
        "v_cvt_pk_bf16_f32 v168, v168, v169\n"
        "v_cvt_pk_bf16_f32 v169, v170, v171\n"
        "v_cvt_pk_bf16_f32 v170, v172, v173\n"
        "v_cvt_pk_bf16_f32 v171, v174, v175\n"
        "global_store_dwordx4 v254, v[168:171], s[70:71] offset:0 sc1\n"
        "s_add_u32 s70, s70, 0x2000\n"
        "s_addc_u32 s71, s71, 0\n"
        "ds_read_b128 a[204:207], v152 offset:2048\n"
        "v_mul_f32 v208, v208, v247\n"
        "v_mul_f32 v209, v209, v247\n"
        "v_mul_f32 v210, v210, v247\n"
        "v_mul_f32 v211, v211, v247\n"
        "v_mul_f32 v212, v212, v247\n"
        "v_mul_f32 v213, v213, v247\n"
        "v_mul_f32 v214, v214, v247\n"
        "v_mul_f32 v215, v215, v247\n"
        "v_cvt_pk_bf16_f32 v208, v208, v209\n"
        "v_cvt_pk_bf16_f32 v209, v210, v211\n"
        "v_cvt_pk_bf16_f32 v210, v212, v213\n"
        "v_cvt_pk_bf16_f32 v211, v214, v215\n"
        "global_store_dwordx4 v254, v[208:211], s[70:71] offset:-4096 sc1\n"
        "ds_read2_b32 a[16:17], v150 offset0:0 offset1:1\n"
        "ds_read2_b32 a[18:19], v150 offset0:2 offset1:3\n"
        "v_mul_f32 v176, v176, v246\n"
        "v_mul_f32 v177, v177, v246\n"
        "v_mul_f32 v178, v178, v246\n"
        "v_mul_f32 v179, v179, v246\n"
        "v_mul_f32 v180, v180, v246\n"
        "v_mul_f32 v181, v181, v246\n"
        "v_mul_f32 v182, v182, v246\n"
        "v_mul_f32 v183, v183, v246\n"
        "v_cvt_pk_bf16_f32 v176, v176, v177\n"
        "v_cvt_pk_bf16_f32 v177, v178, v179\n"
        "v_cvt_pk_bf16_f32 v178, v180, v181\n"
        "v_cvt_pk_bf16_f32 v179, v182, v183\n"
        "global_store_dwordx4 v254, v[176:179], s[70:71] offset:0 sc1\n"
        "s_add_u32 s70, s70, 0x2000\n"
        "s_addc_u32 s71, s71, 0\n"
        "ds_read_b128 a[208:211], v152 offset:1536\n"
        "v_mul_f32 v216, v216, v247\n"
        "v_mfma_f32_16x16x32_bf16 v[96:99], a[0:3], a[192:195], 0\n"
        "ds_read2_b32 a[20:21], v150 offset0:128 offset1:129\n"
        "v_mul_f32 v217, v217, v247\n"
        "v_mfma_f32_16x16x32_bf16 v[100:103], a[0:3], a[196:199], 0\n"
        "ds_read2_b32 a[22:23], v150 offset0:130 offset1:131\n"
        "v_mul_f32 v218, v218, v247\n"
        "v_mfma_f32_16x16x32_bf16 v[100:103], a[4:7], a[192:195], v[100:103]\n"
        "ds_read_b128 a[212:215], v152 offset:1024\n"
        "v_mul_f32 v219, v219, v247\n"
        "v_mul_f32 v220, v220, v247\n"
        "v_mul_f32 v221, v221, v247\n"
        "v_mul_f32 v222, v222, v247\n"
        "v_mul_f32 v223, v223, v247\n"
        "v_cvt_pk_bf16_f32 v216, v216, v217\n"
        "v_cvt_pk_bf16_f32 v217, v218, v219\n"
        "v_cvt_pk_bf16_f32 v218, v220, v221\n"
        "v_cvt_pk_bf16_f32 v219, v222, v223\n"
        "global_store_dwordx4 v254, v[216:219], s[70:71] offset:-4096 sc1\n"
        "s_waitcnt lgkmcnt(4)\n"
        "v_mul_f32 v184, v184, v246\n"
        "v_mul_f32 v185, v185, v246\n"
        "v_mul_f32 v186, v186, v246\n"
        "v_mfma_f32_16x16x32_bf16 v[104:107], a[8:11], a[192:195], 0\n"
        "ds_read2_b32 a[24:25], v151 offset0:0 offset1:1\n"
        "v_mul_f32 v187, v187, v246\n"
        "v_mul_f32 v188, v188, v246\n"
        "v_mul_f32 v189, v189, v246\n"
        "v_mul_f32 v190, v190, v246\n"
        "v_mul_f32 v191, v191, v246\n"
        "v_cvt_pk_bf16_f32 v184, v184, v185\n"
        "v_cvt_pk_bf16_f32 v185, v186, v187\n"
        "v_cvt_pk_bf16_f32 v186, v188, v189\n"
        "v_cvt_pk_bf16_f32 v187, v190, v191\n"
        "global_store_dwordx4 v254, v[184:187], s[70:71] offset:0 sc1\n"
        "ds_read2_b32 a[26:27], v151 offset0:2 offset1:3\n"
        "ds_read_b128 a[216:219], v152 offset:512\n"
        "v_add_f32 v247, v248, v249\n"
        "v_mfma_f32_16x16x32_bf16 v[104:107], a[4:7], a[196:199], v[104:107]\n"
        "ds_read2_b32 a[28:29], v151 offset0:128 offset1:129\n"
        "v_cmp_nge_f32 vcc, 0x4e6e6b28, v247\n"
        "v_mfma_f32_16x16x32_bf16 v[104:107], a[0:3], a[200:203], v[104:107]\n"
        "ds_read2_b32 a[30:31], v151 offset0:130 offset1:131\n"
        "s_cbranch_vccnz LRH_%=\n"
        "LSH_%=:\n"
        "v_min_u32 v231, s16, v231\n"
        "v_mfma_f32_16x16x32_bf16 v[108:111], a[0:3], a[204:207], 0\n"
        "ds_read_b128 a[220:223], v152 offset:0\n"
        "v_and_b32 v234, 0x70, v254\n"
        "v_mfma_f32_16x16x32_bf16 v[108:111], a[4:7], a[200:203], v[108:111]\n"
        "ds_read2_b32 a[32:33], v148 offset0:16 offset1:17\n"
        "v_lshrrev_b32 v234, 1, v234\n"
        "v_mfma_f32_16x16x32_bf16 v[108:111], a[8:11], a[196:199], v[108:111]\n"
        "ds_read2_b32 a[34:35], v148 offset0:18 offset1:19\n"
        "v_and_b32 v235, 0x80, v254\n"
        "v_mfma_f32_16x16x32_bf16 v[108:111], a[12:15], a[192:195], v[108:111]\n"
        "ds_read_b128 a[224:227], v231 offset:0\n"
        "v_add3_u32 v234, v234, v235, s14\n"
        "v_mfma_f32_16x16x32_bf16 v[128:131], a[16:19], a[192:195], 0\n"
        "ds_read2_b32 a[36:37], v148 offset0:144 offset1:145\n"
        "s_and_b32 s14, s10, 1\n"
        "v_xor_b32 v154, 32, v153\n"
        "v_mfma_f32_16x16x32_bf16 v[128:131], a[12:15], a[196:199], v[128:131]\n"
        "ds_read2_b32 a[38:39], v148 offset0:146 offset1:147\n"
        "s_lshl1_add_u32 s62, s62, s14\n"
        "v_add_u32 v155, 0x10000, v153\n"
        "v_mfma_f32_16x16x32_bf16 v[128:131], a[8:11], a[200:203], v[128:131]\n"
        "ds_read_b128 a[228:231], v152 offset:3136\n"
        "s_lshl_b32 s62, s62, 6\n"
        "v_add_u32 v156, 0x10000, v154\n"
        "v_mfma_f32_16x16x32_bf16 v[128:131], a[4:7], a[204:207], v[128:131]\n"
        "ds_read2_b32 a[40:41], v149 offset0:16 offset1:17\n"
        "s_lshl_b32 s14, s62, 11\n"
        "s_waitcnt lgkmcnt(8)\n"
        "v_mfma_f32_16x16x32_bf16 v[128:131], a[0:3], a[208:211], v[128:131]\n"
        "ds_read2_b32 a[42:43], v149 offset0:18 offset1:19\n"
        "s_lshl1_add_u32 s14, s63, s14\n"
        "v_add_u32 v157, 0x20000, v153\n"
        "v_mfma_f32_16x16x32_bf16 v[132:135], a[0:3], a[212:215], 0\n"
        "ds_read_b128 a[232:235], v152 offset:2624\n"
        "v_add_u32 v158, 0x20000, v154\n"
        "v_mfma_f32_16x16x32_bf16 v[132:135], a[4:7], a[208:211], v[132:135]\n"
        "ds_read2_b32 a[44:45], v149 offset0:144 offset1:145\n"
        "v_add_u32 v230, 3712, v152\n"
        "v_mfma_f32_16x16x32_bf16 v[132:135], a[8:11], a[204:207], v[132:135]\n"
        "ds_read2_b32 a[46:47], v149 offset0:146 offset1:147\n"
        "v_min_u32 v230, s16, v230\n"
        "v_mfma_f32_16x16x32_bf16 v[132:135], a[12:15], a[200:203], v[132:135]\n"
        "ds_read_b128 a[236:239], v152 offset:2112\n"
        "s_and_b32 s67, s74, 1\n"
        "v_bfe_u32 v232, v254, 4, 4\n"
        "v_mfma_f32_16x16x32_bf16 v[132:135], a[16:19], a[196:199], v[132:135]\n"
        "ds_read2_b32 a[48:49], v150 offset0:16 offset1:17\n"
        "s_waitcnt vmcnt(9)\n"
        "v_mfma_f32_16x16x32_bf16 v[132:135], a[20:23], a[192:195], v[132:135]\n"
        "s_barrier\n"
        "v_mfma_f32_16x16x32_bf16 v[136:139], a[24:27], a[192:195], 0\n"
        "ds_read2_b32 a[50:51], v150 offset0:18 offset1:19\n"
        "s_lshl_b32 s67, s67, 12\n"
        "v_lshrrev_b32 v233, 8, v254\n"
        "v_mfma_f32_16x16x32_bf16 v[136:139], a[20:23], a[196:199], v[136:139]\n"
        "ds_read_b128 a[240:243], v152 offset:1600\n"
        "v_add_u32 v231, 3776, v152\n"
        "v_mfma_f32_16x16x32_bf16 v[136:139], a[16:19], a[200:203], v[136:139]\n"
        "ds_read2_b32 a[52:53], v150 offset0:144 offset1:145\n"
        "v_min_u32 v231, s16, v231\n"
        "v_mfma_f32_16x16x32_bf16 v[136:139], a[12:15], a[204:207], v[136:139]\n"
        "ds_read2_b32 a[54:55], v150 offset0:146 offset1:147\n"
        "s_add_u32 s64, s8, s14\n"
        "s_addc_u32 s65, s9, 0\n"
        "v_mfma_f32_16x16x32_bf16 v[136:139], a[8:11], a[208:211], v[136:139]\n"
        "ds_read_b128 a[244:247], v152 offset:1088\n"
        "s_add_u32 s56, s36, s14\n"
        "s_addc_u32 s57, s37, 0\n"
        "v_mfma_f32_16x16x32_bf16 v[136:139], a[4:7], a[212:215], v[136:139]\n"
        "ds_read2_b32 a[56:57], v151 offset0:16 offset1:17\n"
        "s_lshl2_add_u32 s14, s12, s10\n"
        "s_lshl_b32 s14, s14, 6\n"
        "v_mfma_f32_16x16x32_bf16 v[136:139], a[0:3], a[216:219], v[136:139]\n"
        "ds_read2_b32 a[58:59], v151 offset0:18 offset1:19\n"
        "s_lshl4_add_u32 s14, s40, s14\n"
        "s_waitcnt lgkmcnt(7)\n"
        "v_mfma_f32_16x16x32_bf16 v[140:143], a[0:3], a[220:223], 0\n"
        "ds_read_b128 a[248:251], v152 offset:576\n"
        "s_add_u32 s14, s14, 1024\n"
        "s_lshl_b32 s14, s14, 7\n"
        "v_mfma_f32_16x16x32_bf16 v[140:143], a[4:7], a[216:219], v[140:143]\n"
        "ds_read2_b32 a[60:61], v151 offset0:144 offset1:145\n"
        "v_lshl_add_u32 v159, v232, 7, s14\n"
        "v_mfma_f32_16x16x32_bf16 v[140:143], a[8:11], a[212:215], v[140:143]\n"
        "ds_read2_b32 a[62:63], v151 offset0:146 offset1:147\n"
        "s_add_u32 s24, s94, 0x400000\n"
        "s_addc_u32 s25, s95, 0\n"
        "v_mfma_f32_16x16x32_bf16 v[140:143], a[12:15], a[208:211], v[140:143]\n"
        "ds_read_b128 a[252:255], v152 offset:64\n"
        "v_mfma_f32_16x16x32_bf16 v[140:143], a[16:19], a[204:207], v[140:143]\n"
        "ds_read_b64_tr_b16 a[96:97], v153 offset:0\n"
        "v_mfma_f32_16x16x32_bf16 v[140:143], a[20:23], a[200:203], v[140:143]\n"
        "ds_read_b64_tr_b16 a[98:99], v153 offset:4096\n"
        "v_mfma_f32_16x16x32_bf16 v[140:143], a[24:27], a[196:199], v[140:143]\n"
        "ds_read2_b32 a[0:1], v148 offset0:32 offset1:33\n"
        "v_mfma_f32_16x16x32_bf16 v[140:143], a[28:31], a[192:195], v[140:143]\n"
        "s_mov_b64 exec, 0\n"
        "s_cmp_eq_u32 s40, 0\n"
        "s_cbranch_scc1 LHB0_%=\n"
        "s_cmp_eq_u32 s40, 1\n"
        "s_cbranch_scc1 LHB1_%=\n"
        "s_cmp_eq_u32 s40, 2\n"
        "s_cbranch_scc1 LHB2_%=\n"
        "s_branch LHB3_%=\n"
        "LHRB_%=:\n"
        "s_mov_b64 exec, -1\n"
        "v_mfma_f32_16x16x32_bf16 v[96:99], a[32:35], a[224:227], v[96:99]\n"
        "ds_read2_b32 a[2:3], v148 offset0:34 offset1:35\n"
        "s_waitcnt vmcnt(0)\n"
        "v_mfma_f32_16x16x32_bf16 v[100:103], a[32:35], a[228:231], v[100:103]\n"
        "s_mov_b64 exec, 0xffff\n"
        "global_store_dwordx2 v234, v[250:251], s[48:49] sc1\n"
        "s_mov_b64 exec, -1\n"
        "v_mfma_f32_16x16x32_bf16 v[100:103], a[36:39], a[224:227], v[100:103]\n"
        "ds_read_b128 a[192:195], v230 offset:0\n"
        "global_load_lds_dwordx4 v144, s[60:61] offset:0\n"
        "v_mfma_f32_16x16x32_bf16 v[104:107], a[40:43], a[224:227], v[104:107]\n"
        "ds_read_b64_tr_b16 a[100:101], v154 offset:0\n"
        "v_add_u32 v244, 0x10000, v254\n"
        "v_mfma_f32_16x16x32_bf16 v[104:107], a[36:39], a[228:231], v[104:107]\n"
        "ds_read_b64_tr_b16 a[102:103], v154 offset:4096\n"
        "v_add_u32 v245, 0x20000, v254\n"
        "v_mfma_f32_16x16x32_bf16 v[104:107], a[32:35], a[232:235], v[104:107]\n"
        "ds_read2_b32 a[4:5], v148 offset0:160 offset1:161\n"
        "v_add_u32 v230, 3840, v152\n"
        "v_mfma_f32_16x16x32_bf16 v[108:111], a[32:35], a[236:239], v[108:111]\n"
        "ds_read2_b32 a[6:7], v148 offset0:162 offset1:163\n"
        "global_load_lds_dwordx4 v145, s[60:61] offset:1024\n"
        "v_mfma_f32_16x16x32_bf16 v[108:111], a[36:39], a[232:235], v[108:111]\n"
        "ds_read_b128 a[196:199], v152 offset:3200\n"
        "s_add_u32 s14, s51, 504\n"
        "v_min_u32 v230, s16, v230\n"
        "v_mfma_f32_16x16x32_bf16 v[108:111], a[40:43], a[228:231], v[108:111]\n"
        "ds_read_b64_tr_b16 a[104:105], v153 offset:8192\n"
        "v_lshlrev_b32 v229, 5, v232\n"
        "v_mfma_f32_16x16x32_bf16 v[108:111], a[44:47], a[224:227], v[108:111]\n"
        "ds_read_b64_tr_b16 a[106:107], v153 offset:12288\n"
        "s_waitcnt lgkmcnt(8)\n"
        "v_mfma_f32_16x16x32_bf16 v[128:131], a[48:51], a[224:227], v[128:131]\n"
        "ds_read2_b32 a[8:9], v149 offset0:32 offset1:33\n"
        "global_load_lds_dwordx4 v146, s[60:61] offset:2048\n"
        "v_mfma_f32_16x16x32_bf16 v[128:131], a[44:47], a[228:231], v[128:131]\n"
        "ds_read2_b32 a[10:11], v149 offset0:34 offset1:35\n"
        "v_lshl_add_u32 v229, v233, 3, v229\n"
        "v_mfma_f32_16x16x32_bf16 v[128:131], a[40:43], a[232:235], v[128:131]\n"
        "ds_read_b128 a[200:203], v152 offset:2688\n"
        "v_sub_u32 v229, s14, v229\n"
        "v_mfma_f32_16x16x32_bf16 v[128:131], a[36:39], a[236:239], v[128:131]\n"
        "ds_read_b64_tr_b16 a[108:109], v154 offset:8192\n"
        "s_lshl_b32 s14, s41, 5\n"
        "s_add_u32 s14, s14, s42\n"
        "v_mfma_f32_16x16x32_bf16 v[128:131], a[32:35], a[240:243], v[128:131]\n"
        "ds_read_b64_tr_b16 a[110:111], v154 offset:12288\n"
        "global_load_lds_dwordx4 v147, s[60:61] offset:3072\n"
        "s_add_u32 m0, s73, 0xc000\n"
        "v_mfma_f32_16x16x32_bf16 v[132:135], a[32:35], a[244:247], v[132:135]\n"
        "ds_read2_b32 a[12:13], v149 offset0:160 offset1:161\n"
        "s_add_u32 s60, s22, s66\n"
        "s_addc_u32 s61, s23, 0\n"
        "v_mfma_f32_16x16x32_bf16 v[132:135], a[36:39], a[240:243], v[132:135]\n"
        "ds_read2_b32 a[14:15], v149 offset0:162 offset1:163\n"
        "s_xor_b32 s66, s29, 0x80000\n"
        "s_add_u32 s14, s14, 512\n"
        "v_mfma_f32_16x16x32_bf16 v[132:135], a[40:43], a[236:239], v[132:135]\n"
        "ds_read_b128 a[204:207], v152 offset:2176\n"
        "s_lshl_b32 s14, s14, 7\n"
        "v_lshrrev_b32 v234, 1, v254\n"
        "v_mfma_f32_16x16x32_bf16 v[132:135], a[44:47], a[232:235], v[132:135]\n"
        "ds_read_b64_tr_b16 a[112:113], v153 offset:16384\n"
        "global_load_lds_dwordx4 v144, s[60:61] offset:0\n"
        "v_mfma_f32_16x16x32_bf16 v[132:135], a[48:51], a[228:231], v[132:135]\n"
        "ds_read_b64_tr_b16 a[114:115], v153 offset:20480\n"
        "v_add_u32 v234, s14, v234\n"
        "v_mfma_f32_16x16x32_bf16 v[132:135], a[52:55], a[224:227], v[132:135]\n"
        "ds_read2_b32 a[16:17], v150 offset0:32 offset1:33\n"
        "s_lshr_b32 s14, s63, 2\n"
        "s_add_u32 s14, s14, 512\n"
        "v_mfma_f32_16x16x32_bf16 v[136:139], a[56:59], a[224:227], v[136:139]\n"
        "ds_read2_b32 a[18:19], v150 offset0:34 offset1:35\n"
        "s_lshl_b32 s14, s14, 7\n"
        "v_mfma_f32_16x16x32_bf16 v[136:139], a[52:55], a[228:231], v[136:139]\n"
        "ds_read_b128 a[208:211], v152 offset:1664\n"
        "global_load_lds_dwordx4 v145, s[60:61] offset:1024\n"
        "v_mfma_f32_16x16x32_bf16 v[136:139], a[48:51], a[232:235], v[136:139]\n"
        "ds_read_b64_tr_b16 a[116:117], v154 offset:16384\n"
        "v_mfma_f32_16x16x32_bf16 v[136:139], a[44:47], a[236:239], v[136:139]\n"
        "ds_read_b64_tr_b16 a[118:119], v154 offset:20480\n"
        "v_mfma_f32_16x16x32_bf16 v[136:139], a[40:43], a[240:243], v[136:139]\n"
        "ds_read2_b32 a[20:21], v150 offset0:160 offset1:161\n"
        "v_mfma_f32_16x16x32_bf16 v[136:139], a[36:39], a[244:247], v[136:139]\n"
        "ds_read2_b32 a[22:23], v150 offset0:162 offset1:163\n"
        "global_load_lds_dwordx4 v146, s[60:61] offset:2048\n"
        "v_mfma_f32_16x16x32_bf16 v[136:139], a[32:35], a[248:251], v[136:139]\n"
        "ds_read_b128 a[212:215], v152 offset:1152\n"
        "v_mfma_f32_16x16x32_bf16 v[140:143], a[32:35], a[252:255], v[140:143]\n"
        "ds_read_b64_tr_b16 a[120:121], v153 offset:24576\n"
        "v_mfma_f32_16x16x32_bf16 v[140:143], a[36:39], a[248:251], v[140:143]\n"
        "ds_read_b64_tr_b16 a[122:123], v153 offset:28672\n"
        "v_mfma_f32_16x16x32_bf16 v[140:143], a[40:43], a[244:247], v[140:143]\n"
        "ds_read_b64_tr_b16 a[124:125], v154 offset:24576\n"
        "global_load_lds_dwordx4 v147, s[60:61] offset:3072\n"
        "s_add_u32 m0, s73, 0x10000\n"
        "v_mfma_f32_16x16x32_bf16 v[140:143], a[44:47], a[240:243], v[140:143]\n"
        "ds_read_b64_tr_b16 a[126:127], v154 offset:28672\n"
        "s_add_u32 s60, s22, s66\n"
        "s_addc_u32 s61, s23, 0\n"
        "v_mfma_f32_16x16x32_bf16 v[140:143], a[48:51], a[236:239], v[140:143]\n"
        "ds_read2_b32 a[24:25], v151 offset0:32 offset1:33\n"
        "s_xor_b32 s66, s29, 0xa0000\n"
        "s_waitcnt lgkmcnt(8)\n"
        "v_mfma_f32_16x16x32_bf16 v[140:143], a[52:55], a[232:235], v[140:143]\n"
        "ds_read2_b32 a[26:27], v151 offset0:34 offset1:35\n"
        "v_mfma_f32_16x16x32_bf16 v[140:143], a[56:59], a[228:231], v[140:143]\n"
        "ds_read_b128 a[216:219], v152 offset:640\n"
        "global_load_lds_dwordx4 v144, s[60:61] offset:0\n"
        "v_mfma_f32_16x16x32_bf16 v[140:143], a[60:63], a[224:227], v[140:143]\n"
        "ds_read2_b32 a[28:29], v151 offset0:160 offset1:161\n"
        "v_mfma_f32_16x16x32_bf16 v[96:99], a[0:3], a[192:195], v[96:99]\n"
        "ds_read2_b32 a[30:31], v151 offset0:162 offset1:163\n"
        "v_mfma_f32_16x16x32_bf16 v[100:103], a[0:3], a[196:199], v[100:103]\n"
        "ds_read_b128 a[220:223], v152 offset:128\n"
        "v_mfma_f32_16x16x32_bf16 v[100:103], a[4:7], a[192:195], v[100:103]\n"
        "ds_read2_b32 a[32:33], v148 offset0:48 offset1:49\n"
        "global_load_lds_dwordx4 v145, s[60:61] offset:1024\n"
        "v_mfma_f32_16x16x32_bf16 v[104:107], a[8:11], a[192:195], v[104:107]\n"
        "ds_read2_b32 a[34:35], v148 offset0:50 offset1:51\n"
        "s_waitcnt lgkmcnt(8)\n"
        "v_mfma_f32_16x16x32_bf16 v[104:107], a[4:7], a[196:199], v[104:107]\n"
        "s_barrier\n"
        "v_mfma_f32_16x16x32_bf16 v[104:107], a[0:3], a[200:203], v[104:107]\n"
        "ds_read_b128 a[224:227], v231 offset:0\n"
        "v_add_u32 v231, 3904, v152\n"
        "v_mfma_f32_16x16x32_bf16 v[108:111], a[0:3], a[204:207], v[108:111]\n"
        "ds_read2_b32 a[36:37], v148 offset0:176 offset1:177\n"
        "global_load_lds_dwordx4 v146, s[60:61] offset:2048\n"
        "v_mfma_f32_16x16x32_bf16 v[108:111], a[4:7], a[200:203], v[108:111]\n"
        "ds_read2_b32 a[38:39], v148 offset0:178 offset1:179\n"
        "v_min_u32 v231, s16, v231\n"
        "v_mfma_f32_16x16x32_bf16 v[108:111], a[8:11], a[196:199], v[108:111]\n"
        "ds_read_b128 a[228:231], v152 offset:3264\n"
        "v_mfma_f32_16x16x32_bf16 v[108:111], a[12:15], a[192:195], v[108:111]\n"
        "ds_read2_b32 a[40:41], v149 offset0:48 offset1:49\n"
        "v_mfma_f32_16x16x32_bf16 v[128:131], a[16:19], a[192:195], v[128:131]\n"
        "ds_read2_b32 a[42:43], v149 offset0:50 offset1:51\n"
        "global_load_lds_dwordx4 v147, s[60:61] offset:3072\n"
        "s_add_u32 m0, s73, 0x14000\n"
        "v_mfma_f32_16x16x32_bf16 v[128:131], a[12:15], a[196:199], v[128:131]\n"
        "ds_read_b128 a[232:235], v152 offset:2752\n"
        "s_add_u32 s60, s22, s66\n"
        "s_addc_u32 s61, s23, 0\n"
        "v_mfma_f32_16x16x32_bf16 v[128:131], a[8:11], a[200:203], v[128:131]\n"
        "ds_read2_b32 a[44:45], v149 offset0:176 offset1:177\n"
        "s_xor_b32 s66, s29, 0xc0000\n"
        "v_mfma_f32_16x16x32_bf16 v[128:131], a[4:7], a[204:207], v[128:131]\n"
        "ds_read2_b32 a[46:47], v149 offset0:178 offset1:179\n"
        "v_mfma_f32_16x16x32_bf16 v[128:131], a[0:3], a[208:211], v[128:131]\n"
        "ds_read_b128 a[236:239], v152 offset:2240\n"
        "global_load_lds_dwordx4 v144, s[60:61] offset:0\n"
        "v_mfma_f32_16x16x32_bf16 v[132:135], a[0:3], a[212:215], v[132:135]\n"
        "ds_read2_b32 a[48:49], v150 offset0:48 offset1:49\n"
        "v_mfma_f32_16x16x32_bf16 v[132:135], a[4:7], a[208:211], v[132:135]\n"
        "ds_read2_b32 a[50:51], v150 offset0:50 offset1:51\n"
        "v_mfma_f32_16x16x32_bf16 v[132:135], a[8:11], a[204:207], v[132:135]\n"
        "ds_read_b128 a[240:243], v152 offset:1728\n"
        "s_waitcnt lgkmcnt(8)\n"
        "v_mfma_f32_16x16x32_bf16 v[132:135], a[12:15], a[200:203], v[132:135]\n"
        "ds_read2_b32 a[52:53], v150 offset0:176 offset1:177\n"
        "global_load_lds_dwordx4 v145, s[60:61] offset:1024\n"
        "v_mfma_f32_16x16x32_bf16 v[132:135], a[16:19], a[196:199], v[132:135]\n"
        "ds_read2_b32 a[54:55], v150 offset0:178 offset1:179\n"
        "v_mfma_f32_16x16x32_bf16 v[132:135], a[20:23], a[192:195], v[132:135]\n"
        "ds_read_b128 a[244:247], v152 offset:1216\n"
        "v_mfma_f32_16x16x32_bf16 v[136:139], a[24:27], a[192:195], v[136:139]\n"
        "ds_read2_b32 a[56:57], v151 offset0:48 offset1:49\n"
        "v_mfma_f32_16x16x32_bf16 v[136:139], a[20:23], a[196:199], v[136:139]\n"
        "ds_read2_b32 a[58:59], v151 offset0:50 offset1:51\n"
        "global_load_lds_dwordx4 v146, s[60:61] offset:2048\n"
        "v_mfma_f32_16x16x32_bf16 v[136:139], a[16:19], a[200:203], v[136:139]\n"
        "ds_read_b128 a[248:251], v152 offset:704\n"
        "v_mfma_f32_16x16x32_bf16 v[136:139], a[12:15], a[204:207], v[136:139]\n"
        "ds_read2_b32 a[60:61], v151 offset0:176 offset1:177\n"
        "v_mfma_f32_16x16x32_bf16 v[136:139], a[8:11], a[208:211], v[136:139]\n"
        "ds_read2_b32 a[62:63], v151 offset0:178 offset1:179\n"
        "v_mfma_f32_16x16x32_bf16 v[136:139], a[4:7], a[212:215], v[136:139]\n"
        "ds_read_b128 a[252:255], v152 offset:192\n"
        "global_load_lds_dwordx4 v147, s[60:61] offset:3072\n"
        "s_add_u32 m0, s73, 0x0\n"
        "v_mfma_f32_16x16x32_bf16 v[136:139], a[0:3], a[216:219], v[136:139]\n"
        "ds_read_b64 v[64:65], v229 offset:3584\n"
        "s_add_u32 s60, s22, s66\n"
        "s_addc_u32 s61, s23, 0\n"
        "v_mfma_f32_16x16x32_bf16 v[140:143], a[0:3], a[220:223], v[140:143]\n"
        "ds_read2_b32 a[0:1], v148 offset0:64 offset1:65\n"
        "s_xor_b32 s66, s29, 0xe0000\n"
        "v_mfma_f32_16x16x32_bf16 v[140:143], a[4:7], a[216:219], v[140:143]\n"
        "ds_read2_b32 a[2:3], v148 offset0:66 offset1:67\n"
        "v_mfma_f32_16x16x32_bf16 v[140:143], a[8:11], a[212:215], v[140:143]\n"
        "ds_read2_b32 a[4:5], v148 offset0:192 offset1:193\n"
        "global_load_lds_dwordx4 v144, s[60:61] offset:0\n"
        "v_mfma_f32_16x16x32_bf16 v[140:143], a[12:15], a[208:211], v[140:143]\n"
        "ds_read2_b32 a[6:7], v148 offset0:194 offset1:195\n"
        "v_mfma_f32_16x16x32_bf16 v[140:143], a[16:19], a[204:207], v[140:143]\n"
        "ds_read2_b32 a[8:9], v149 offset0:64 offset1:65\n"
        "v_mfma_f32_16x16x32_bf16 v[140:143], a[20:23], a[200:203], v[140:143]\n"
        "ds_read2_b32 a[10:11], v149 offset0:66 offset1:67\n"
        "v_mfma_f32_16x16x32_bf16 v[140:143], a[24:27], a[196:199], v[140:143]\n"
        "ds_read_b128 a[196:199], v152 offset:3328\n"
        "global_load_lds_dwordx4 v145, s[60:61] offset:1024\n"
        "v_mfma_f32_16x16x32_bf16 v[140:143], a[28:31], a[192:195], v[140:143]\n"
        "ds_read_b128 a[192:195], v230 offset:0\n"
        "v_add_u32 v230, 3968, v152\n"
        "v_mfma_f32_16x16x32_bf16 v[96:99], a[32:35], a[224:227], v[96:99]\n"
        "ds_read_b128 a[200:203], v152 offset:2816\n"
        "v_min_u32 v230, s16, v230\n"
        "v_mfma_f32_16x16x32_bf16 v[100:103], a[32:35], a[228:231], v[100:103]\n"
        "ds_read2_b32 a[12:13], v149 offset0:192 offset1:193\n"
        "s_waitcnt lgkmcnt(8)\n"
        "v_mfma_f32_16x16x32_bf16 v[100:103], a[36:39], a[224:227], v[100:103]\n"
        "ds_read2_b32 a[14:15], v149 offset0:194 offset1:195\n"
        "global_load_lds_dwordx4 v146, s[60:61] offset:2048\n"
        "v_mfma_f32_16x16x32_bf16 v[104:107], a[40:43], a[224:227], v[104:107]\n"
        "ds_read_b128 a[204:207], v152 offset:2304\n"
        "v_and_b32 v80, s35, v65\n"
        "v_mfma_f32_16x16x32_bf16 v[104:107], a[36:39], a[228:231], v[104:107]\n"
        "ds_read2_b32 a[16:17], v150 offset0:64 offset1:65\n"
        "v_lshlrev_b32 v81, 16, v65\n"
        "v_mfma_f32_16x16x32_bf16 v[104:107], a[32:35], a[232:235], v[104:107]\n"
        "ds_read2_b32 a[18:19], v150 offset0:66 offset1:67\n"
        "v_and_b32 v82, s35, v64\n"
        "v_mfma_f32_16x16x32_bf16 v[108:111], a[32:35], a[236:239], v[108:111]\n"
        "ds_read_b128 a[208:211], v152 offset:1792\n"
        "global_load_lds_dwordx4 v147, s[60:61] offset:3072\n"
        "s_add_u32 m0, s73, 0x4000\n"
        "v_mfma_f32_16x16x32_bf16 v[108:111], a[36:39], a[232:235], v[108:111]\n"
        "ds_read2_b32 a[20:21], v150 offset0:192 offset1:193\n"
        "s_add_u32 s60, s22, s66\n"
        "s_addc_u32 s61, s23, 0\n"
        "v_mfma_f32_16x16x32_bf16 v[108:111], a[40:43], a[228:231], v[108:111]\n"
        "ds_read2_b32 a[22:23], v150 offset0:194 offset1:195\n"
        "s_waitcnt vmcnt(12)\n"
        "v_mfma_f32_16x16x32_bf16 v[108:111], a[44:47], a[224:227], v[108:111]\n"
        "s_barrier\n"
        "v_mfma_f32_16x16x32_bf16 v[128:131], a[48:51], a[224:227], v[128:131]\n"
        "ds_read_b64_tr_b16 a[128:129], v153 offset:32768\n"
        "LHC2_%=:\n"
        "s_cbranch_execz LHRC_%=\n"
        "global_load_lds_dwordx4 v144, s[60:61] offset:0\n"
        "s_xor_b32 s66, s29, 0x100000\n"
        "v_mfma_f32_16x16x32_bf16 v[128:131], a[44:47], a[228:231], v[128:131]\n"
        "ds_read_b64_tr_b16 a[130:131], v153 offset:36864\n"
        "v_lshlrev_b32 v83, 16, v64\n"
        "v_mfma_f32_16x16x32_bf16 v[128:131], a[40:43], a[232:235], v[128:131]\n"
        "ds_read_b64_tr_b16 a[132:133], v154 offset:32768\n"
        "v_mul_f32 v80, s54, v80\n"
        "v_mfma_f32_16x16x32_bf16 v[128:131], a[36:39], a[236:239], v[128:131]\n"
        "ds_read_b64_tr_b16 a[134:135], v154 offset:36864\n"
        "v_mul_f32 v81, s54, v81\n"
        "v_mfma_f32_16x16x32_bf16 v[128:131], a[32:35], a[240:243], v[128:131]\n"
        "ds_read_b64_tr_b16 a[136:137], v153 offset:40960\n"
        "global_load_lds_dwordx4 v145, s[60:61] offset:1024\n"
        "v_mfma_f32_16x16x32_bf16 v[132:135], a[32:35], a[244:247], v[132:135]\n"
        "ds_read_b64_tr_b16 a[138:139], v153 offset:45056\n"
        "v_mul_f32 v82, s54, v82\n"
        "v_mfma_f32_16x16x32_bf16 v[132:135], a[36:39], a[240:243], v[132:135]\n"
        "ds_read_b64_tr_b16 a[140:141], v154 offset:40960\n"
        "v_mul_f32 v83, s54, v83\n"
        "v_mfma_f32_16x16x32_bf16 v[132:135], a[40:43], a[236:239], v[132:135]\n"
        "ds_read_b64_tr_b16 a[142:143], v154 offset:45056\n"
        "v_cvt_pk_bf16_f32 v80, 0, v80\n"
        "v_mfma_f32_16x16x32_bf16 v[132:135], a[44:47], a[232:235], v[132:135]\n"
        "ds_read_b64_tr_b16 a[144:145], v153 offset:49152\n"
        "global_load_lds_dwordx4 v146, s[60:61] offset:2048\n"
        "v_mfma_f32_16x16x32_bf16 v[132:135], a[48:51], a[228:231], v[132:135]\n"
        "ds_read_b64_tr_b16 a[146:147], v153 offset:53248\n"
        "v_cvt_pk_bf16_f32 v81, 0, v81\n"
        "v_mfma_f32_16x16x32_bf16 v[132:135], a[52:55], a[224:227], v[132:135]\n"
        "ds_read_b64_tr_b16 a[148:149], v154 offset:49152\n"
        "v_cvt_pk_bf16_f32 v82, 0, v82\n"
        "v_mfma_f32_16x16x32_bf16 v[136:139], a[56:59], a[224:227], v[136:139]\n"
        "ds_read_b64_tr_b16 a[150:151], v154 offset:53248\n"
        "v_cvt_pk_bf16_f32 v83, 0, v83\n"
        "v_mfma_f32_16x16x32_bf16 v[136:139], a[52:55], a[228:231], v[136:139]\n"
        "ds_read_b128 a[212:215], v152 offset:1280\n"
        "global_load_lds_dwordx4 v147, s[60:61] offset:3072\n"
        "s_add_u32 m0, s73, 0x8000\n"
        "v_mfma_f32_16x16x32_bf16 v[136:139], a[48:51], a[232:235], v[136:139]\n"
        "ds_read_b64_tr_b16 a[152:153], v153 offset:57344\n"
        "s_add_u32 s60, s22, s66\n"
        "s_addc_u32 s61, s23, 0\n"
        "v_mfma_f32_16x16x32_bf16 v[136:139], a[44:47], a[236:239], v[136:139]\n"
        "ds_read_b64_tr_b16 a[154:155], v153 offset:61440\n"
        "s_xor_b32 s66, s29, 0x120000\n"
        "v_mfma_f32_16x16x32_bf16 v[136:139], a[40:43], a[240:243], v[136:139]\n"
        "ds_read_b64_tr_b16 a[156:157], v154 offset:57344\n"
        "v_mfma_f32_16x16x32_bf16 v[136:139], a[36:39], a[244:247], v[136:139]\n"
        "ds_read_b64_tr_b16 a[158:159], v154 offset:61440\n"
        "v_mfma_f32_16x16x32_bf16 v[136:139], a[32:35], a[248:251], v[136:139]\n"
        "ds_read2_b32 a[24:25], v151 offset0:64 offset1:65\n"
        "v_mfma_f32_16x16x32_bf16 v[140:143], a[32:35], a[252:255], v[140:143]\n"
        "ds_read2_b32 a[26:27], v151 offset0:66 offset1:67\n"
        "v_mfma_f32_16x16x32_bf16 v[140:143], a[36:39], a[248:251], v[140:143]\n"
        "ds_read_b128 a[216:219], v152 offset:768\n"
        "v_mfma_f32_16x16x32_bf16 v[140:143], a[40:43], a[244:247], v[140:143]\n"
        "ds_read2_b32 a[28:29], v151 offset0:192 offset1:193\n"
        "v_mfma_f32_16x16x32_bf16 v[140:143], a[44:47], a[240:243], v[140:143]\n"
        "ds_read2_b32 a[30:31], v151 offset0:194 offset1:195\n"
        "v_mfma_f32_16x16x32_bf16 v[140:143], a[48:51], a[236:239], v[140:143]\n"
        "ds_read_b128 a[220:223], v152 offset:256\n"
        "s_waitcnt lgkmcnt(8)\n"
        "v_mfma_f32_16x16x32_bf16 v[140:143], a[52:55], a[232:235], v[140:143]\n"
        "ds_read2_b32 a[32:33], v148 offset0:80 offset1:81\n"
        "v_mfma_f32_16x16x32_bf16 v[140:143], a[56:59], a[228:231], v[140:143]\n"
        "ds_read2_b32 a[34:35], v148 offset0:82 offset1:83\n"
        "v_mfma_f32_16x16x32_bf16 v[140:143], a[60:63], a[224:227], v[140:143]\n"
        "ds_read_b128 a[224:227], v231 offset:0\n"
        "v_add_u32 v231, 4032, v152\n"
        "v_mfma_f32_16x16x32_bf16 v[96:99], a[0:3], a[192:195], v[96:99]\n"
        "ds_read2_b32 a[36:37], v148 offset0:208 offset1:209\n"
        "v_min_u32 v231, s16, v231\n"
        "v_mfma_f32_16x16x32_bf16 v[100:103], a[0:3], a[196:199], v[100:103]\n"
        "ds_read2_b32 a[38:39], v148 offset0:210 offset1:211\n"
        "v_mfma_f32_16x16x32_bf16 v[100:103], a[4:7], a[192:195], v[100:103]\n"
        "ds_read_b128 a[228:231], v152 offset:3392\n"
        "v_mfma_f32_16x16x32_bf16 v[104:107], a[8:11], a[192:195], v[104:107]\n"
        "ds_read2_b32 a[40:41], v149 offset0:80 offset1:81\n"
        "v_mfma_f32_16x16x32_bf16 v[104:107], a[4:7], a[196:199], v[104:107]\n"
        "ds_read2_b32 a[42:43], v149 offset0:82 offset1:83\n"
        "s_waitcnt vmcnt(8) lgkmcnt(14)\n"
        "v_mfma_f32_16x16x32_bf16 v[104:107], a[0:3], a[200:203], v[104:107]\n"
        "s_barrier\n"
        "v_mfma_f32_16x16x32_bf16 v[108:111], a[0:3], a[204:207], v[108:111]\n"
        "ds_read_b64_tr_b16 v[160:161], v155 offset:0\n"
        "global_load_lds_dwordx4 v144, s[60:61] offset:0\n"
        "v_mfma_f32_16x16x32_bf16 v[108:111], a[4:7], a[200:203], v[108:111]\n"
        "ds_read_b64_tr_b16 v[162:163], v155 offset:4096\n"
        "v_mfma_f32_16x16x32_bf16 v[108:111], a[8:11], a[196:199], v[108:111]\n"
        "ds_read_b64_tr_b16 v[164:165], v156 offset:0\n"
        "v_mfma_f32_16x16x32_bf16 v[108:111], a[12:15], a[192:195], v[108:111]\n"
        "ds_read_b64_tr_b16 v[166:167], v156 offset:4096\n"
        "v_mfma_f32_16x16x32_bf16 v[128:131], a[16:19], a[192:195], v[128:131]\n"
        "ds_read_b64_tr_b16 v[168:169], v155 offset:8192\n"
        "global_load_lds_dwordx4 v145, s[60:61] offset:1024\n"
        "v_mfma_f32_16x16x32_bf16 v[128:131], a[12:15], a[196:199], v[128:131]\n"
        "ds_read_b64_tr_b16 v[170:171], v155 offset:12288\n"
        "v_mfma_f32_16x16x32_bf16 v[128:131], a[8:11], a[200:203], v[128:131]\n"
        "ds_read_b128 a[232:235], v152 offset:2880\n"
        "v_mfma_f32_16x16x32_bf16 v[128:131], a[4:7], a[204:207], v[128:131]\n"
        "ds_read_b64_tr_b16 v[172:173], v156 offset:8192\n"
        "v_mfma_f32_16x16x32_bf16 v[128:131], a[0:3], a[208:211], v[128:131]\n"
        "ds_read_b64_tr_b16 v[174:175], v156 offset:12288\n"
        "global_load_lds_dwordx4 v146, s[60:61] offset:2048\n"
        "v_mfma_f32_16x16x32_bf16 v[132:135], a[0:3], a[212:215], v[132:135]\n"
        "ds_read2_b32 a[44:45], v149 offset0:208 offset1:209\n"
        "v_mfma_f32_16x16x32_bf16 v[132:135], a[4:7], a[208:211], v[132:135]\n"
        "ds_read2_b32 a[46:47], v149 offset0:210 offset1:211\n"
        "v_mfma_f32_16x16x32_bf16 v[132:135], a[8:11], a[204:207], v[132:135]\n"
        "ds_read_b128 a[236:239], v152 offset:2368\n"
        "s_waitcnt lgkmcnt(8)\n"
        "v_mfma_f32_16x16x32_bf16 v[132:135], a[12:15], a[200:203], v[132:135]\n"
        "ds_read_b64_tr_b16 v[176:177], v155 offset:16384\n"
        "global_load_lds_dwordx4 v147, s[60:61] offset:3072\n"
        "s_add_u32 m0, s73, 0xc000\n"
        "v_mfma_f32_16x16x32_bf16 v[132:135], a[16:19], a[196:199], v[132:135]\n"
        "ds_read_b64_tr_b16 v[178:179], v155 offset:20480\n"
        "s_add_u32 s60, s22, s66\n"
        "s_addc_u32 s61, s23, 0\n"
        "v_mfma_f32_16x16x32_bf16 v[132:135], a[20:23], a[192:195], v[132:135]\n"
        "ds_read2_b32 a[48:49], v150 offset0:80 offset1:81\n"
        "s_xor_b32 s66, s29, 0x140000\n"
        "v_mfma_f32_16x16x32_bf16 v[136:139], a[24:27], a[192:195], v[136:139]\n"
        "ds_read2_b32 a[50:51], v150 offset0:82 offset1:83\n"
        "v_mfma_f32_16x16x32_bf16 v[136:139], a[20:23], a[196:199], v[136:139]\n"
        "ds_read_b128 a[240:243], v152 offset:1856\n"
        "global_load_lds_dwordx4 v144, s[60:61] offset:0\n"
        "v_mfma_f32_16x16x32_bf16 v[136:139], a[16:19], a[200:203], v[136:139]\n"
        "ds_read_b64_tr_b16 v[180:181], v156 offset:16384\n"
        "v_mfma_f32_16x16x32_bf16 v[136:139], a[12:15], a[204:207], v[136:139]\n"
        "ds_read_b64_tr_b16 v[182:183], v156 offset:20480\n"
        "v_mfma_f32_16x16x32_bf16 v[136:139], a[8:11], a[208:211], v[136:139]\n"
        "ds_read2_b32 a[52:53], v150 offset0:208 offset1:209\n"
        "v_mfma_f32_16x16x32_bf16 v[136:139], a[4:7], a[212:215], v[136:139]\n"
        "ds_read2_b32 a[54:55], v150 offset0:210 offset1:211\n"
        "global_load_lds_dwordx4 v145, s[60:61] offset:1024\n"
        "v_mfma_f32_16x16x32_bf16 v[136:139], a[0:3], a[216:219], v[136:139]\n"
        "ds_read_b128 a[244:247], v152 offset:1344\n"
        "v_mfma_f32_16x16x32_bf16 v[140:143], a[0:3], a[220:223], v[140:143]\n"
        "ds_read_b64_tr_b16 v[184:185], v155 offset:24576\n"
        "v_mfma_f32_16x16x32_bf16 v[140:143], a[4:7], a[216:219], v[140:143]\n"
        "ds_read_b64_tr_b16 v[186:187], v155 offset:28672\n"
        "v_mfma_f32_16x16x32_bf16 v[140:143], a[8:11], a[212:215], v[140:143]\n"
        "ds_read2_b32 a[56:57], v151 offset0:80 offset1:81\n"
        "global_load_lds_dwordx4 v146, s[60:61] offset:2048\n"
        "v_mfma_f32_16x16x32_bf16 v[140:143], a[12:15], a[208:211], v[140:143]\n"
        "ds_read2_b32 a[58:59], v151 offset0:82 offset1:83\n"
        "v_mfma_f32_16x16x32_bf16 v[140:143], a[16:19], a[204:207], v[140:143]\n"
        "ds_read_b128 a[248:251], v152 offset:832\n"
        "v_mfma_f32_16x16x32_bf16 v[140:143], a[20:23], a[200:203], v[140:143]\n"
        "ds_read_b64_tr_b16 v[188:189], v156 offset:24576\n"
        "v_mfma_f32_16x16x32_bf16 v[140:143], a[24:27], a[196:199], v[140:143]\n"
        "ds_read_b64_tr_b16 v[190:191], v156 offset:28672\n"
        "global_load_lds_dwordx4 v147, s[60:61] offset:3072\n"
        "s_add_u32 m0, s73, 0x10000\n"
        "v_mfma_f32_16x16x32_bf16 v[140:143], a[28:31], a[192:195], v[140:143]\n"
        "ds_read2_b32 a[60:61], v151 offset0:208 offset1:209\n"
        "s_add_u32 s60, s22, s66\n"
        "s_addc_u32 s61, s23, 0\n"
        "v_mfma_f32_16x16x32_bf16 v[96:99], a[32:35], a[224:227], v[96:99]\n"
        "ds_read2_b32 a[62:63], v151 offset0:210 offset1:211\n"
        "s_xor_b32 s66, s29, 0x160000\n"
        "v_mfma_f32_16x16x32_bf16 v[100:103], a[32:35], a[228:231], v[100:103]\n"
        "ds_read_b128 a[252:255], v152 offset:320\n"
        "s_waitcnt lgkmcnt(8)\n"
        "v_mfma_f32_16x16x32_bf16 v[100:103], a[36:39], a[224:227], v[100:103]\n"
        "ds_read2_b32 a[0:1], v148 offset0:96 offset1:97\n"
        "global_load_dwordx2 v[236:237], v159, s[48:49] sc1\n"
        "v_mfma_f32_16x16x32_bf16 v[104:107], a[40:43], a[224:227], v[104:107]\n"
        "ds_read2_b32 a[2:3], v148 offset0:98 offset1:99\n"
        "v_mfma_f32_16x16x32_bf16 v[104:107], a[36:39], a[228:231], v[104:107]\n"
        "ds_read_b128 a[192:195], v230 offset:0\n"
        "v_lshrrev_b32 v230, 1, v233\n"
        "v_mfma_f32_16x16x32_bf16 v[104:107], a[32:35], a[232:235], v[104:107]\n"
        "ds_read2_b32 a[4:5], v148 offset0:224 offset1:225\n"
        "v_mfma_f32_16x16x32_bf16 v[108:111], a[32:35], a[236:239], v[108:111]\n"
        "ds_read2_b32 a[6:7], v148 offset0:226 offset1:227\n"
        "s_waitcnt vmcnt(9) lgkmcnt(8)\n"
        "v_mfma_f32_16x16x32_bf16 v[108:111], a[36:39], a[232:235], v[108:111]\n"
        "s_barrier\n"
        "v_mfma_f32_16x16x32_bf16 v[108:111], a[40:43], a[228:231], v[108:111]\n"
        "ds_read_b64_tr_b16 v[192:193], v153 offset:0\n"
        "global_load_lds_dwordx4 v144, s[60:61] offset:0\n"
        "v_mfma_f32_16x16x32_bf16 v[108:111], a[44:47], a[224:227], v[108:111]\n"
        "ds_read_b64_tr_b16 v[194:195], v153 offset:4096\n"
        "v_mfma_f32_16x16x32_bf16 v[128:131], a[48:51], a[224:227], v[128:131]\n"
        "ds_read_b64_tr_b16 v[196:197], v154 offset:0\n"
        "v_mfma_f32_16x16x32_bf16 v[128:131], a[44:47], a[228:231], v[128:131]\n"
        "ds_read_b64_tr_b16 v[198:199], v154 offset:4096\n"
        "v_mfma_f32_16x16x32_bf16 v[128:131], a[40:43], a[232:235], v[128:131]\n"
        "ds_read_b128 a[196:199], v152 offset:3456\n"
        "global_load_lds_dwordx4 v145, s[60:61] offset:1024\n"
        "v_mfma_f32_16x16x32_bf16 v[128:131], a[36:39], a[236:239], v[128:131]\n"
        "ds_read_b64_tr_b16 v[200:201], v153 offset:8192\n"
        "v_mfma_f32_16x16x32_bf16 v[128:131], a[32:35], a[240:243], v[128:131]\n"
        "ds_read_b64_tr_b16 v[202:203], v153 offset:12288\n"
        "v_mfma_f32_16x16x32_bf16 v[132:135], a[32:35], a[244:247], v[132:135]\n"
        "ds_read2_b32 a[8:9], v149 offset0:96 offset1:97\n"
        "v_mfma_f32_16x16x32_bf16 v[132:135], a[36:39], a[240:243], v[132:135]\n"
        "ds_read2_b32 a[10:11], v149 offset0:98 offset1:99\n"
        "global_load_lds_dwordx4 v146, s[60:61] offset:2048\n"
        "v_mfma_f32_16x16x32_bf16 v[132:135], a[40:43], a[236:239], v[132:135]\n"
        "ds_read_b128 a[200:203], v152 offset:2944\n"
        "v_mfma_f32_16x16x32_bf16 v[132:135], a[44:47], a[232:235], v[132:135]\n"
        "ds_read_b64_tr_b16 v[204:205], v154 offset:8192\n"
        "v_mfma_f32_16x16x32_bf16 v[132:135], a[48:51], a[228:231], v[132:135]\n"
        "ds_read_b64_tr_b16 v[206:207], v154 offset:12288\n"
        "v_mfma_f32_16x16x32_bf16 v[132:135], a[52:55], a[224:227], v[132:135]\n"
        "ds_read2_b32 a[12:13], v149 offset0:224 offset1:225\n"
        "global_load_lds_dwordx4 v147, s[60:61] offset:3072\n"
        "s_add_u32 m0, s73, 0x14000\n"
        "v_mfma_f32_16x16x32_bf16 v[136:139], a[56:59], a[224:227], v[136:139]\n"
        "ds_read2_b32 a[14:15], v149 offset0:226 offset1:227\n"
        "s_add_u32 s60, s22, s66\n"
        "s_addc_u32 s61, s23, 0\n"
        "v_mfma_f32_16x16x32_bf16 v[136:139], a[52:55], a[228:231], v[136:139]\n"
        "ds_read_b128 a[204:207], v152 offset:2432\n"
        "s_sub_u32 s66, 15, s12\n"
        "v_mfma_f32_16x16x32_bf16 v[136:139], a[48:51], a[232:235], v[136:139]\n"
        "ds_read_b64_tr_b16 v[208:209], v153 offset:16384\n"
        "s_cmp_lt_u32 s74, 2\n"
        "s_cselect_b32 s66, s12, s66\n"
        "v_mfma_f32_16x16x32_bf16 v[136:139], a[44:47], a[236:239], v[136:139]\n"
        "ds_read_b64_tr_b16 v[210:211], v153 offset:20480\n"
        "global_load_lds_dwordx4 v144, s[60:61] offset:0\n"
        "s_mul_i32 s66, s66, 1052672\n"
        "v_mfma_f32_16x16x32_bf16 v[136:139], a[40:43], a[240:243], v[136:139]\n"
        "ds_read2_b32 a[16:17], v150 offset0:96 offset1:97\n"
        "s_add_u32 s66, s66, s67\n"
        "s_and_b32 s67, s75, 1\n"
        "v_mfma_f32_16x16x32_bf16 v[136:139], a[36:39], a[244:247], v[136:139]\n"
        "ds_read2_b32 a[18:19], v150 offset0:98 offset1:99\n"
        "s_add_u32 s68, s20, s66\n"
        "s_addc_u32 s69, s21, 0\n"
        "v_mfma_f32_16x16x32_bf16 v[136:139], a[32:35], a[248:251], v[136:139]\n"
        "ds_read_b128 a[208:211], v152 offset:1920\n"
        "s_xor_b32 s66, s17, 0\n"
        "s_waitcnt lgkmcnt(8)\n"
        "v_mfma_f32_16x16x32_bf16 v[140:143], a[32:35], a[252:255], v[140:143]\n"
        "ds_read_b64_tr_b16 v[212:213], v154 offset:16384\n"
        "global_load_lds_dwordx4 v145, s[60:61] offset:1024\n"
        "s_lshl_b32 s66, s66, 15\n"
        "v_mfma_f32_16x16x32_bf16 v[140:143], a[36:39], a[248:251], v[140:143]\n"
        "ds_read_b64_tr_b16 v[214:215], v154 offset:20480\n"
        "s_lshl_b32 s67, s67, 12\n"
        "v_mfma_f32_16x16x32_bf16 v[140:143], a[40:43], a[244:247], v[140:143]\n"
        "ds_read2_b32 a[20:21], v150 offset0:224 offset1:225\n"
        "v_mfma_f32_16x16x32_bf16 v[140:143], a[44:47], a[240:243], v[140:143]\n"
        "ds_read2_b32 a[22:23], v150 offset0:226 offset1:227\n"
        "v_mfma_f32_16x16x32_bf16 v[140:143], a[48:51], a[236:239], v[140:143]\n"
        "ds_read_b128 a[212:215], v152 offset:1408\n"
        "global_load_lds_dwordx4 v146, s[60:61] offset:2048\n"
        "v_mfma_f32_16x16x32_bf16 v[140:143], a[52:55], a[232:235], v[140:143]\n"
        "ds_read_b64_tr_b16 v[216:217], v153 offset:24576\n"
        "s_waitcnt vmcnt(7) lgkmcnt(8)\n"
        "v_mfma_f32_16x16x32_bf16 v[140:143], a[56:59], a[228:231], v[140:143]\n"
        "ds_read_b64_tr_b16 v[218:219], v153 offset:28672\n"
        "v_cmp_ne_u64 vcc, v[236:237], v[250:251]\n"
        "v_mfma_f32_16x16x32_bf16 v[140:143], a[60:63], a[224:227], v[140:143]\n"
        "ds_read2_b32 a[24:25], v151 offset0:96 offset1:97\n"
        "s_cbranch_vccnz LPCX_%=\n"
        "LBCX_%=:\n"
        "v_mfma_f32_16x16x32_bf16 v[96:99], a[0:3], a[192:195], v[96:99]\n"
        "ds_read2_b32 a[26:27], v151 offset0:98 offset1:99\n"
        "global_load_lds_dwordx4 v147, s[60:61] offset:3072\n"
        "s_add_u32 m0, s73, 0x0\n"
        "v_mfma_f32_16x16x32_bf16 v[100:103], a[0:3], a[196:199], v[100:103]\n"
        "ds_read_b128 a[216:219], v152 offset:896\n"
        "s_add_u32 s60, s68, s66\n"
        "s_addc_u32 s61, s69, 0\n"
        "v_mfma_f32_16x16x32_bf16 v[100:103], a[4:7], a[192:195], v[100:103]\n"
        "ds_read_b64_tr_b16 v[220:221], v154 offset:24576\n"
        "s_xor_b32 s66, s17, 1\n"
        "s_lshl_b32 s66, s66, 15\n"
        "v_mfma_f32_16x16x32_bf16 v[104:107], a[8:11], a[192:195], v[104:107]\n"
        "ds_read_b64_tr_b16 v[222:223], v154 offset:28672\n"
        "v_mfma_f32_16x16x32_bf16 v[104:107], a[4:7], a[196:199], v[104:107]\n"
        "ds_read2_b32 a[28:29], v151 offset0:224 offset1:225\n"
        "global_load_dword v224, v159, s[48:49] offset:64 sc1\n"
        "v_mfma_f32_16x16x32_bf16 v[104:107], a[0:3], a[200:203], v[104:107]\n"
        "ds_read2_b32 a[30:31], v151 offset0:226 offset1:227\n"
        "v_mfma_f32_16x16x32_bf16 v[108:111], a[0:3], a[204:207], v[108:111]\n"
        "ds_read_b128 a[220:223], v152 offset:384\n"
        "s_waitcnt lgkmcnt(8)\n"
        "v_mfma_f32_16x16x32_bf16 v[108:111], a[4:7], a[200:203], v[108:111]\n"
        "ds_read2_b32 a[32:33], v148 offset0:112 offset1:113\n"
        "v_mfma_f32_16x16x32_bf16 v[108:111], a[8:11], a[196:199], v[108:111]\n"
        "ds_read2_b32 a[34:35], v148 offset0:114 offset1:115\n"
        "v_mfma_f32_16x16x32_bf16 v[108:111], a[12:15], a[192:195], v[108:111]\n"
        "ds_read_b128 a[224:227], v231 offset:0\n"
        "v_mfma_f32_16x16x32_bf16 v[128:131], a[16:19], a[192:195], v[128:131]\n"
        "ds_read2_b32 a[36:37], v148 offset0:240 offset1:241\n"
        "v_mfma_f32_16x16x32_bf16 v[128:131], a[12:15], a[196:199], v[128:131]\n"
        "ds_read2_b32 a[38:39], v148 offset0:242 offset1:243\n"
        "v_mfma_f32_16x16x32_bf16 v[128:131], a[8:11], a[200:203], v[128:131]\n"
        "ds_read_b128 a[228:231], v152 offset:3520\n"
        "s_waitcnt lgkmcnt(9)\n"
        "v_mfma_f32_16x16x32_bf16 v[128:131], a[4:7], a[204:207], v[128:131]\n"
        "s_barrier\n"
        "v_mfma_f32_16x16x32_bf16 v[128:131], a[0:3], a[208:211], v[128:131]\n"
        "ds_read_b64_tr_b16 v[112:113], v153 offset:32768\n"
        "global_load_lds_dwordx4 v254, s[60:61] offset:0 sc1\n"
        "v_mfma_f32_16x16x32_bf16 v[132:135], a[0:3], a[212:215], v[132:135]\n"
        "ds_read_b64_tr_b16 v[114:115], v153 offset:36864\n"
        "v_mfma_f32_16x16x32_bf16 v[132:135], a[4:7], a[208:211], v[132:135]\n"
        "ds_read_b64_tr_b16 v[116:117], v154 offset:32768\n"
        "v_mfma_f32_16x16x32_bf16 v[132:135], a[8:11], a[204:207], v[132:135]\n"
        "ds_read_b64_tr_b16 v[118:119], v154 offset:36864\n"
        "v_mfma_f32_16x16x32_bf16 v[132:135], a[12:15], a[200:203], v[132:135]\n"
        "ds_read_b64_tr_b16 v[120:121], v153 offset:40960\n"
        "global_load_lds_dwordx4 v254, s[60:61] offset:1024 sc1\n"
        "v_mfma_f32_16x16x32_bf16 v[132:135], a[16:19], a[196:199], v[132:135]\n"
        "ds_read_b64_tr_b16 v[122:123], v153 offset:45056\n"
        "v_mfma_f32_16x16x32_bf16 v[132:135], a[20:23], a[192:195], v[132:135]\n"
        "ds_read2_b32 a[40:41], v149 offset0:112 offset1:113\n"
        "v_mfma_f32_16x16x32_bf16 v[136:139], a[24:27], a[192:195], v[136:139]\n"
        "ds_read2_b32 a[42:43], v149 offset0:114 offset1:115\n"
        "v_mfma_f32_16x16x32_bf16 v[136:139], a[20:23], a[196:199], v[136:139]\n"
        "ds_read_b128 a[232:235], v152 offset:3008\n"
        "global_load_lds_dwordx4 v254, s[60:61] offset:2048 sc1\n"
        "v_mfma_f32_16x16x32_bf16 v[136:139], a[16:19], a[200:203], v[136:139]\n"
        "ds_read_b64_tr_b16 v[124:125], v154 offset:40960\n"
        "v_mfma_f32_16x16x32_bf16 v[136:139], a[12:15], a[204:207], v[136:139]\n"
        "ds_read_b64_tr_b16 v[126:127], v154 offset:45056\n"
        "s_waitcnt lgkmcnt(8)\n"
        "v_mfma_f32_16x16x32_bf16 v[136:139], a[8:11], a[208:211], v[136:139]\n"
        "ds_read2_b32 a[44:45], v149 offset0:240 offset1:241\n"
        "v_mfma_f32_16x16x32_bf16 v[136:139], a[4:7], a[212:215], v[136:139]\n"
        "ds_read2_b32 a[46:47], v149 offset0:242 offset1:243\n"
        "global_load_lds_dwordx4 v254, s[60:61] offset:3072 sc1\n"
        "s_add_u32 m0, s73, 0x4000\n"
        "v_mfma_f32_16x16x32_bf16 v[136:139], a[0:3], a[216:219], v[136:139]\n"
        "ds_read_b128 a[236:239], v152 offset:2496\n"
        "s_add_u32 s60, s68, s66\n"
        "s_addc_u32 s61, s69, 0\n"
        "v_mfma_f32_16x16x32_bf16 v[140:143], a[0:3], a[220:223], v[140:143]\n"
        "ds_read_b64_tr_b16 a[64:65], v153 offset:49152\n"
        "s_xor_b32 s66, s29, 0x180000\n"
        "v_mfma_f32_16x16x32_bf16 v[140:143], a[4:7], a[216:219], v[140:143]\n"
        "ds_read_b64_tr_b16 a[66:67], v153 offset:53248\n"
        "v_mfma_f32_16x16x32_bf16 v[140:143], a[8:11], a[212:215], v[140:143]\n"
        "ds_read2_b32 a[48:49], v150 offset0:112 offset1:113\n"
        "global_load_lds_dwordx4 v254, s[60:61] offset:0 sc1\n"
        "v_mfma_f32_16x16x32_bf16 v[140:143], a[12:15], a[208:211], v[140:143]\n"
        "ds_read2_b32 a[50:51], v150 offset0:114 offset1:115\n"
        "v_mfma_f32_16x16x32_bf16 v[140:143], a[16:19], a[204:207], v[140:143]\n"
        "ds_read_b128 a[240:243], v152 offset:1984\n"
        "v_mfma_f32_16x16x32_bf16 v[140:143], a[20:23], a[200:203], v[140:143]\n"
        "ds_read_b64_tr_b16 a[68:69], v154 offset:49152\n"
        "v_mfma_f32_16x16x32_bf16 v[140:143], a[24:27], a[196:199], v[140:143]\n"
        "ds_read_b64_tr_b16 a[70:71], v154 offset:53248\n"
        "global_load_lds_dwordx4 v254, s[60:61] offset:1024 sc1\n"
        "v_mfma_f32_16x16x32_bf16 v[140:143], a[28:31], a[192:195], v[140:143]\n"
        "ds_read2_b32 a[52:53], v150 offset0:240 offset1:241\n"
        "s_waitcnt lgkmcnt(8)\n"
        "v_mfma_f32_16x16x32_bf16 v[96:99], a[32:35], a[224:227], v[96:99]\n"
        "ds_read2_b32 a[54:55], v150 offset0:242 offset1:243\n"
        "v_mfma_f32_16x16x32_bf16 v[100:103], a[32:35], a[228:231], v[100:103]\n"
        "ds_read_b128 a[244:247], v152 offset:1472\n"
        "v_mfma_f32_16x16x32_bf16 v[100:103], a[36:39], a[224:227], v[100:103]\n"
        "ds_read_b64_tr_b16 a[72:73], v153 offset:57344\n"
        "global_load_lds_dwordx4 v254, s[60:61] offset:2048 sc1\n"
        "v_mfma_f32_16x16x32_bf16 v[104:107], a[40:43], a[224:227], v[104:107]\n"
        "ds_read_b64_tr_b16 a[74:75], v153 offset:61440\n"
        "v_cvt_pk_bf16_f32 v96, 0, v96\n"
        "v_mfma_f32_16x16x32_bf16 v[104:107], a[36:39], a[228:231], v[104:107]\n"
        "ds_read_b64_tr_b16 a[76:77], v154 offset:57344\n"
        "v_cvt_pk_bf16_f32 v97, 0, v97\n"
        "v_mfma_f32_16x16x32_bf16 v[104:107], a[32:35], a[232:235], v[104:107]\n"
        "ds_read_b64_tr_b16 a[78:79], v154 offset:61440\n"
        "v_cvt_pk_bf16_f32 v98, 0, v98\n"
        "v_mfma_f32_16x16x32_bf16 v[108:111], a[32:35], a[236:239], v[108:111]\n"
        "ds_read2_b32 a[56:57], v151 offset0:112 offset1:113\n"
        "global_load_lds_dwordx4 v254, s[60:61] offset:3072 sc1\n"
        "s_add_u32 m0, s73, 0x8000\n"
        "v_mfma_f32_16x16x32_bf16 v[108:111], a[36:39], a[232:235], v[108:111]\n"
        "ds_read2_b32 a[58:59], v151 offset0:114 offset1:115\n"
        "s_add_u32 s60, s22, s66\n"
        "s_addc_u32 s61, s23, 0\n"
        "v_mfma_f32_16x16x32_bf16 v[108:111], a[40:43], a[228:231], v[108:111]\n"
        "ds_read_b128 a[248:251], v152 offset:960\n"
        "s_xor_b32 s66, s17, 2\n"
        "s_lshl_b32 s66, s66, 15\n"
        "v_mfma_f32_16x16x32_bf16 v[108:111], a[44:47], a[224:227], v[108:111]\n"
        "ds_read_b64 v[66:67], v229 offset:3072\n"
        "s_waitcnt lgkmcnt(8)\n"
        "v_mfma_f32_16x16x32_bf16 v[128:131], a[48:51], a[224:227], v[128:131]\n"
        "ds_read_b64 v[68:69], v229 offset:2560\n"
        "v_cvt_pk_bf16_f32 v99, 0, v99\n"
        "v_mfma_f32_16x16x32_bf16 v[128:131], a[44:47], a[228:231], v[128:131]\n"
        "ds_read_b64 v[70:71], v229 offset:2048\n"
        "v_cvt_pk_bf16_f32 v100, 0, v100\n"
        "v_mfma_f32_16x16x32_bf16 v[128:131], a[40:43], a[232:235], v[128:131]\n"
        "ds_read_b64 v[72:73], v229 offset:1536\n"
        "v_cvt_pk_bf16_f32 v101, 0, v101\n"
        "v_mfma_f32_16x16x32_bf16 v[128:131], a[36:39], a[236:239], v[128:131]\n"
        "ds_read_b64 v[74:75], v229 offset:1024\n"
        "v_cvt_pk_bf16_f32 v102, 0, v102\n"
        "v_mfma_f32_16x16x32_bf16 v[128:131], a[32:35], a[240:243], v[128:131]\n"
        "ds_read_b64 v[76:77], v229 offset:512\n"
        "s_waitcnt vmcnt(9) lgkmcnt(9)\n"
        "v_mfma_f32_16x16x32_bf16 v[132:135], a[32:35], a[244:247], v[132:135]\n"
        "s_barrier\n"
        "v_mfma_f32_16x16x32_bf16 v[132:135], a[36:39], a[240:243], v[132:135]\n"
        "ds_read_b64 v[78:79], v229 offset:0\n"
        "global_load_lds_dwordx4 v144, s[60:61] offset:0\n"
        "v_mfma_f32_16x16x32_bf16 v[132:135], a[40:43], a[236:239], v[132:135]\n"
        "ds_read_b128 a[252:255], v152 offset:448\n"
        "v_cvt_pk_bf16_f32 v103, 0, v103\n"
        "v_mfma_f32_16x16x32_bf16 v[132:135], a[44:47], a[232:235], v[132:135]\n"
        "ds_read2_b32 a[60:61], v151 offset0:240 offset1:241\n"
        "v_add_f32 v80, v80, v96\n"
        "v_mfma_f32_16x16x32_bf16 v[132:135], a[48:51], a[228:231], v[132:135]\n"
        "ds_read2_b32 a[62:63], v151 offset0:242 offset1:243\n"
        "v_add_f32 v81, v81, v97\n"
        "v_mfma_f32_16x16x32_bf16 v[132:135], a[52:55], a[224:227], v[132:135]\n"
        "ds_read_b64_tr_b16 a[80:81], v155 offset:0\n"
        "s_waitcnt lgkmcnt(7)\n"
        "v_mfma_f32_16x16x32_bf16 v[136:139], a[56:59], a[224:227], v[136:139]\n"
        "ds_read_b64_tr_b16 a[82:83], v155 offset:4096\n"
        "global_load_lds_dwordx4 v145, s[60:61] offset:1024\n"
        "v_mfma_f32_16x16x32_bf16 v[136:139], a[52:55], a[228:231], v[136:139]\n"
        "ds_read_b64_tr_b16 a[84:85], v156 offset:0\n"
        "v_and_b32 v84, s35, v67\n"
        "v_mfma_f32_16x16x32_bf16 v[136:139], a[48:51], a[232:235], v[136:139]\n"
        "ds_read_b64_tr_b16 a[86:87], v156 offset:4096\n"
        "v_lshlrev_b32 v85, 16, v67\n"
        "v_mfma_f32_16x16x32_bf16 v[136:139], a[44:47], a[236:239], v[136:139]\n"
        "ds_read_b64_tr_b16 a[88:89], v155 offset:8192\n"
        "v_and_b32 v86, s35, v66\n"
        "v_mfma_f32_16x16x32_bf16 v[136:139], a[40:43], a[240:243], v[136:139]\n"
        "ds_read_b64_tr_b16 a[90:91], v155 offset:12288\n"
        "global_load_lds_dwordx4 v146, s[60:61] offset:2048\n"
        "v_mfma_f32_16x16x32_bf16 v[136:139], a[36:39], a[244:247], v[136:139]\n"
        "ds_read_b64_tr_b16 a[92:93], v156 offset:8192\n"
        "v_lshlrev_b32 v87, 16, v66\n"
        "v_mfma_f32_16x16x32_bf16 v[136:139], a[32:35], a[248:251], v[136:139]\n"
        "ds_read_b64_tr_b16 a[94:95], v156 offset:12288\n"
        "s_waitcnt lgkmcnt(8)\n"
        "v_mfma_f32_16x16x32_bf16 v[140:143], a[32:35], a[252:255], v[140:143]\n"
        "ds_read_b64_tr_b16 a[160:161], v155 offset:16384\n"
        "v_mfma_f32_16x16x32_bf16 v[140:143], a[36:39], a[248:251], v[140:143]\n"
        "s_barrier\n"
        "v_mfma_f32_16x16x32_bf16 v[140:143], a[40:43], a[244:247], v[140:143]\n"
        "ds_read_b64_tr_b16 a[162:163], v155 offset:20480\n"
        "global_load_lds_dwordx4 v147, s[60:61] offset:3072\n"
        "s_add_u32 m0, s73, 0xc000\n"
        "v_mfma_f32_16x16x32_bf16 v[140:143], a[44:47], a[240:243], v[140:143]\n"
        "ds_read_b64_tr_b16 a[164:165], v156 offset:16384\n"
        "s_add_u32 s60, s68, s66\n"
        "s_addc_u32 s61, s69, 0\n"
        "v_mfma_f32_16x16x32_bf16 v[140:143], a[48:51], a[236:239], v[140:143]\n"
        "ds_read_b64_tr_b16 a[166:167], v156 offset:20480\n"
        "s_xor_b32 s66, s29, 0x1a0000\n"
        "v_mul_f32 v84, s54, v84\n"
        "v_mfma_f32_16x16x32_bf16 v[140:143], a[52:55], a[232:235], v[140:143]\n"
        "ds_read_b64_tr_b16 a[168:169], v155 offset:24576\n"
        "v_mul_f32 v85, s54, v85\n"
        "v_mfma_f32_16x16x32_bf16 v[140:143], a[56:59], a[228:231], v[140:143]\n"
        "ds_read_b64_tr_b16 a[170:171], v155 offset:28672\n"
        "global_load_lds_dwordx4 v254, s[60:61] offset:0 sc1\n"
        "v_mfma_f32_16x16x32_bf16 v[140:143], a[60:63], a[224:227], v[140:143]\n"
        "ds_read_b64_tr_b16 a[172:173], v156 offset:24576\n"
        "ds_read_b64_tr_b16 a[174:175], v156 offset:28672\n"
        "v_mul_f32 v86, s54, v86\n"
        "v_mul_f32 v87, s54, v87\n"
        "v_cvt_pk_bf16_f32 v84, 0, v84\n"
        "v_cvt_pk_bf16_f32 v85, 0, v85\n"
        "v_cvt_pk_bf16_f32 v86, 0, v86\n"
        "global_load_lds_dwordx4 v254, s[60:61] offset:1024 sc1\n"
        "v_cvt_pk_bf16_f32 v87, 0, v87\n"
        "v_add_f32 v82, v82, v98\n"
        "v_add_f32 v83, v83, v99\n"
        "v_add_f32 v84, v84, v100\n"
        "v_add_f32 v85, v85, v101\n"
        "v_add_f32 v86, v86, v102\n"
        "v_add_f32 v87, v87, v103\n"
        "v_cvt_pk_bf16_f32 v88, v80, v81\n"
        "v_cvt_pk_bf16_f32 v89, v82, v83\n"
        "v_cvt_pk_bf16_f32 v90, v84, v85\n"
        "global_load_lds_dwordx4 v254, s[60:61] offset:2048 sc1\n"
        "v_cvt_pk_bf16_f32 v91, v86, v87\n"
        "v_and_b32 v80, s35, v69\n"
        "v_lshlrev_b32 v81, 16, v69\n"
        "v_and_b32 v82, s35, v68\n"
        "v_lshlrev_b32 v83, 16, v68\n"
        "v_and_b32 v84, s35, v71\n"
        "v_lshlrev_b32 v85, 16, v71\n"
        "v_and_b32 v86, s35, v70\n"
        "v_lshlrev_b32 v87, 16, v70\n"
        "v_mul_f32 v80, s54, v80\n"
        "global_load_lds_dwordx4 v254, s[60:61] offset:3072 sc1\n"
        "s_add_u32 m0, s73, 0x18000\n"
        "s_add_u32 s60, s22, s66\n"
        "s_addc_u32 s61, s23, 0\n"
        "s_xor_b32 s66, s17, 3\n"
        "s_lshl_b32 s66, s66, 15\n"
        "v_mul_f32 v81, s54, v81\n"
        "v_mul_f32 v82, s54, v82\n"
        "v_mul_f32 v83, s54, v83\n"
        "v_mul_f32 v84, s54, v84\n"
        "v_mul_f32 v85, s54, v85\n"
        "v_mul_f32 v86, s54, v86\n"
        "global_load_lds_dwordx4 v144, s[60:61] offset:0\n"
        "v_mul_f32 v87, s54, v87\n"
        "v_cvt_pk_bf16_f32 v104, 0, v104\n"
        "v_cvt_pk_bf16_f32 v105, 0, v105\n"
        "v_cvt_pk_bf16_f32 v106, 0, v106\n"
        "v_cvt_pk_bf16_f32 v107, 0, v107\n"
        "v_cvt_pk_bf16_f32 v108, 0, v108\n"
        "v_cvt_pk_bf16_f32 v109, 0, v109\n"
        "v_cvt_pk_bf16_f32 v110, 0, v110\n"
        "v_cvt_pk_bf16_f32 v111, 0, v111\n"
        "v_cvt_pk_bf16_f32 v80, 0, v80\n"
        "global_load_lds_dwordx4 v145, s[60:61] offset:1024\n"
        "v_cvt_pk_bf16_f32 v81, 0, v81\n"
        "v_cvt_pk_bf16_f32 v82, 0, v82\n"
        "v_cvt_pk_bf16_f32 v83, 0, v83\n"
        "v_cvt_pk_bf16_f32 v84, 0, v84\n"
        "v_cvt_pk_bf16_f32 v85, 0, v85\n"
        "v_cvt_pk_bf16_f32 v86, 0, v86\n"
        "v_cvt_pk_bf16_f32 v87, 0, v87\n"
        "s_waitcnt vmcnt(14) lgkmcnt(0)\n"
        "s_barrier\n"
        "ds_read_b128 a[0:3], v254 offset:0\n"
        "ds_read_b128 a[4:7], v254 offset:4096\n"
        "ds_read_b128 a[8:11], v254 offset:8192\n"
        "ds_read_b128 a[12:15], v254 offset:12288\n"
        "global_load_lds_dwordx4 v146, s[60:61] offset:2048\n"
        "global_load_lds_dwordx4 v147, s[60:61] offset:3072\n"
        "s_add_u32 m0, s73, 0x1c000\n"
        "s_add_u32 s60, s68, s66\n"
        "s_addc_u32 s61, s69, 0\n"
        "s_xor_b32 s66, s29, 0x1c0000\n"
        "ds_read_b128 a[16:19], v254 offset:1024\n"
        "ds_read_b128 a[20:23], v254 offset:5120\n"
        "ds_read_b128 a[24:27], v254 offset:9216\n"
        "s_waitcnt lgkmcnt(6)\n"
        "v_mfma_f32_16x16x32_bf16 v[0:3], a[96:99], a[0:3], 0\n"
        "s_waitcnt lgkmcnt(5)\n"
        "ds_read_b128 a[28:31], v254 offset:13312\n"
        "v_mfma_f32_16x16x32_bf16 v[0:3], a[104:107], a[4:7], v[0:3]\n"
        "s_waitcnt lgkmcnt(5)\n"
        "v_permlane16_swap_b32 v88, v90\n"
        "v_mfma_f32_16x16x32_bf16 v[0:3], a[112:115], a[8:11], v[0:3]\n"
        "s_waitcnt lgkmcnt(4)\n"
        "ds_read_b128 a[192:195], v254 offset:2048\n"
        "v_mfma_f32_16x16x32_bf16 v[0:3], a[120:123], a[12:15], v[0:3]\n"
        "ds_read_b128 a[196:199], v254 offset:6144\n"
        "global_load_lds_dwordx4 v254, s[60:61] offset:0 sc1\n"
        "v_mfma_f32_16x16x32_bf16 v[4:7], a[124:127], a[12:15], 0\n"
        "ds_read_b128 a[200:203], v254 offset:10240\n"
        "v_add_f32 v80, v80, v104\n"
        "v_mfma_f32_16x16x32_bf16 v[4:7], a[116:119], a[8:11], v[4:7]\n"
        "ds_read_b128 a[204:207], v254 offset:14336\n"
        "v_add_f32 v81, v81, v105\n"
        "v_mfma_f32_16x16x32_bf16 v[4:7], a[108:111], a[4:7], v[4:7]\n"
        "s_waitcnt lgkmcnt(7)\n"
        "v_permlane16_swap_b32 v89, v91\n"
        "v_mfma_f32_16x16x32_bf16 v[4:7], a[100:103], a[0:3], v[4:7]\n"
        "ds_read_b128 a[208:211], v254 offset:3072\n"
        "global_load_lds_dwordx4 v254, s[60:61] offset:1024 sc1\n"
        "v_mfma_f32_16x16x32_bf16 v[12:15], a[100:103], a[16:19], 0\n"
        "ds_read_b128 a[212:215], v254 offset:7168\n"
        "s_waitcnt lgkmcnt(6)\n"
        "v_mfma_f32_16x16x32_bf16 v[12:15], a[108:111], a[20:23], v[12:15]\n"
        "ds_read_b128 a[216:219], v254 offset:11264\n"
        "LHC3_%=:\n"
        "s_cbranch_execz LHRC_%=\n"
        "v_add_f32 v82, v82, v106\n"
        "v_mfma_f32_16x16x32_bf16 v[12:15], a[116:119], a[24:27], v[12:15]\n"
        "ds_read_b128 a[220:223], v254 offset:15360\n"
        "v_add_f32 v83, v83, v107\n"
        "v_mfma_f32_16x16x32_bf16 v[12:15], a[124:127], a[28:31], v[12:15]\n"
        "global_load_lds_dwordx4 v254, s[60:61] offset:2048 sc1\n"
        "v_add_f32 v84, v84, v108\n"
        "v_mfma_f32_16x16x32_bf16 v[8:11], a[120:123], a[28:31], 0\n"
        "s_waitcnt lgkmcnt(5)\n"
        "v_add_f32 v85, v85, v109\n"
        "v_add_f32 v86, v86, v110\n"
        "v_mfma_f32_16x16x32_bf16 v[8:11], a[112:115], a[24:27], v[8:11]\n"
        "v_add_f32 v87, v87, v111\n"
        "v_and_b32 v229, 1, v233\n"
        "v_mfma_f32_16x16x32_bf16 v[8:11], a[104:107], a[20:23], v[8:11]\n"
        "global_load_lds_dwordx4 v254, s[60:61] offset:3072 sc1\n"
        "s_add_u32 m0, s73, 0x20000\n"
        "v_mfma_f32_16x16x32_bf16 v[8:11], a[96:99], a[16:19], v[8:11]\n"
        "s_add_u32 s60, s22, s66\n"
        "s_addc_u32 s61, s23, 0\n"
        "s_sub_u32 s66, 15, s12\n"
        "v_mfma_f32_16x16x32_bf16 v[16:19], a[96:99], a[192:195], 0\n"
        "s_cmp_lt_u32 s75, 2\n"
        "s_cselect_b32 s66, s12, s66\n"
        "s_mul_i32 s66, s66, 1052672\n"
        "v_mfma_f32_16x16x32_bf16 v[16:19], a[104:107], a[196:199], v[16:19]\n"
        "s_add_u32 s66, s66, s67\n"
        "s_add_u32 s68, s20, s66\n"
        "s_addc_u32 s69, s21, 0\n"
        "v_mfma_f32_16x16x32_bf16 v[16:19], a[112:115], a[200:203], v[16:19]\n"
        "s_xor_b32 s66, s17, 0\n"
        "s_lshl_b32 s66, s66, 15\n"
        "s_waitcnt lgkmcnt(0)\n"
        "v_mfma_f32_16x16x32_bf16 v[16:19], a[120:123], a[204:207], v[16:19]\n"
        "global_load_lds_dwordx4 v144, s[60:61] offset:0\n"
        "s_waitcnt vmcnt(17)\n"
        "v_mfma_f32_16x16x32_bf16 v[20:23], a[124:127], a[204:207], 0\n"
        "s_barrier\n"
        "v_mfma_f32_16x16x32_bf16 v[20:23], a[116:119], a[200:203], v[20:23]\n"
        "ds_read_b128 a[0:3], v254 offset:16384\n"
        "s_and_b32 s67, s74, 1\n"
        "s_lshl_b32 s67, s67, 12\n"
        "v_mfma_f32_16x16x32_bf16 v[20:23], a[108:111], a[196:199], v[20:23]\n"
        "ds_read_b128 a[4:7], v254 offset:20480\n"
        "global_load_lds_dwordx4 v145, s[60:61] offset:1024\n"
        "v_mfma_f32_16x16x32_bf16 v[20:23], a[100:103], a[192:195], v[20:23]\n"
        "ds_read_b128 a[8:11], v254 offset:24576\n"
        "v_cvt_pk_bf16_f32 v92, v80, v81\n"
        "v_mfma_f32_16x16x32_bf16 v[28:31], a[100:103], a[208:211], 0\n"
        "ds_read_b128 a[12:15], v254 offset:28672\n"
        "v_cvt_pk_bf16_f32 v93, v82, v83\n"
        "v_mfma_f32_16x16x32_bf16 v[28:31], a[108:111], a[212:215], v[28:31]\n"
        "ds_read_b128 a[16:19], v254 offset:17408\n"
        "v_cvt_pk_bf16_f32 v94, v84, v85\n"
        "v_mfma_f32_16x16x32_bf16 v[28:31], a[116:119], a[216:219], v[28:31]\n"
        "ds_read_b128 a[20:23], v254 offset:21504\n"
        "global_load_lds_dwordx4 v146, s[60:61] offset:2048\n"
        "v_mfma_f32_16x16x32_bf16 v[28:31], a[124:127], a[220:223], v[28:31]\n"
        "ds_read_b128 a[24:27], v254 offset:25600\n"
        "v_cvt_pk_bf16_f32 v95, v86, v87\n"
        "v_mfma_f32_16x16x32_bf16 v[24:27], a[120:123], a[220:223], 0\n"
        "ds_read_b128 a[28:31], v254 offset:29696\n"
        "v_lshlrev_b32 v229, 9, v229\n"
        "v_mfma_f32_16x16x32_bf16 v[24:27], a[112:115], a[216:219], v[24:27]\n"
        "s_waitcnt lgkmcnt(7)\n"
        "ds_read_b128 a[192:195], v254 offset:18432\n"
        "v_mfma_f32_16x16x32_bf16 v[24:27], a[104:107], a[212:215], v[24:27]\n"
        "s_waitcnt lgkmcnt(7)\n"
        "ds_read_b128 a[196:199], v254 offset:22528\n"
        "v_mfma_f32_16x16x32_bf16 v[24:27], a[96:99], a[208:211], v[24:27]\n"
        "s_waitcnt lgkmcnt(7)\n"
        "ds_read_b128 a[200:203], v254 offset:26624\n"
        "v_mfma_f32_16x16x32_bf16 v[0:3], a[128:131], a[0:3], v[0:3]\n"
        "s_waitcnt lgkmcnt(7)\n"
        "ds_read_b128 a[204:207], v254 offset:30720\n"
        "v_mfma_f32_16x16x32_bf16 v[0:3], a[136:139], a[4:7], v[0:3]\n"
        "ds_read_b128 a[208:211], v254 offset:19456\n"
        "global_load_lds_dwordx4 v147, s[60:61] offset:3072\n"
        "s_add_u32 m0, s73, 0x24000\n"
        "v_mfma_f32_16x16x32_bf16 v[0:3], a[144:147], a[8:11], v[0:3]\n"
        "ds_read_b128 a[212:215], v254 offset:23552\n"
        "s_add_u32 s60, s68, s66\n"
        "s_addc_u32 s61, s69, 0\n"
        "v_mfma_f32_16x16x32_bf16 v[0:3], a[152:155], a[12:15], v[0:3]\n"
        "ds_read_b128 a[216:219], v254 offset:27648\n"
        "s_xor_b32 s66, s29, 0x1e0000\n"
        "v_and_b32 v80, s35, v73\n"
        "v_mfma_f32_16x16x32_bf16 v[4:7], a[156:159], a[12:15], v[4:7]\n"
        "s_waitcnt lgkmcnt(7)\n"
        "ds_read_b128 a[220:223], v254 offset:31744\n"
        "v_mfma_f32_16x16x32_bf16 v[4:7], a[148:151], a[8:11], v[4:7]\n"
        "v_permlane16_swap_b32 v92, v94\n"
        "v_mfma_f32_16x16x32_bf16 v[4:7], a[140:143], a[4:7], v[4:7]\n"
        "v_permlane16_swap_b32 v93, v95\n"
        "v_mfma_f32_16x16x32_bf16 v[4:7], a[132:135], a[0:3], v[4:7]\n"
        "global_load_lds_dwordx4 v254, s[60:61] offset:0 sc1\n"
        "v_lshlrev_b32 v81, 16, v73\n"
        "v_mfma_f32_16x16x32_bf16 v[12:15], a[132:135], a[16:19], v[12:15]\n"
        "v_and_b32 v82, s35, v72\n"
        "v_lshlrev_b32 v83, 16, v72\n"
        "v_mfma_f32_16x16x32_bf16 v[12:15], a[140:143], a[20:23], v[12:15]\n"
        "v_and_b32 v84, s35, v75\n"
        "v_lshlrev_b32 v85, 16, v75\n"
        "v_mfma_f32_16x16x32_bf16 v[12:15], a[148:151], a[24:27], v[12:15]\n"
        "global_load_lds_dwordx4 v254, s[60:61] offset:1024 sc1\n"
        "v_mfma_f32_16x16x32_bf16 v[12:15], a[156:159], a[28:31], v[12:15]\n"
        "v_and_b32 v86, s35, v74\n"
        "v_lshlrev_b32 v87, 16, v74\n"
        "v_mfma_f32_16x16x32_bf16 v[8:11], a[152:155], a[28:31], v[8:11]\n"
        "s_waitcnt lgkmcnt(1)\n"
        "v_lshl_add_u32 v229, v232, 5, v229\n"
        "v_mul_f32 v80, s54, v80\n"
        "v_mfma_f32_16x16x32_bf16 v[8:11], a[144:147], a[24:27], v[8:11]\n"
        "v_mul_f32 v81, s54, v81\n"
        "v_mul_f32 v82, s54, v82\n"
        "v_mfma_f32_16x16x32_bf16 v[8:11], a[136:139], a[20:23], v[8:11]\n"
        "global_load_lds_dwordx4 v254, s[60:61] offset:2048 sc1\n"
        "v_mul_f32 v83, s54, v83\n"
        "v_mfma_f32_16x16x32_bf16 v[8:11], a[128:131], a[16:19], v[8:11]\n"
        "s_waitcnt vmcnt(15)\n"
        "v_mul_f32 v84, s54, v84\n"
        "v_mul_f32 v85, s54, v85\n"
        "v_mfma_f32_16x16x32_bf16 v[16:19], a[128:131], a[192:195], v[16:19]\n"
        "s_barrier\n"
        "v_mfma_f32_16x16x32_bf16 v[16:19], a[136:139], a[196:199], v[16:19]\n"
        "ds_read_b64_tr_b16 a[176:177], v153 offset:32768\n"
        "global_load_lds_dwordx4 v254, s[60:61] offset:3072 sc1\n"
        "s_add_u32 m0, s73, 0x10000\n"
        "v_mfma_f32_16x16x32_bf16 v[16:19], a[144:147], a[200:203], v[16:19]\n"
        "ds_read_b64_tr_b16 a[178:179], v153 offset:36864\n"
        "s_add_u32 s60, s22, s66\n"
        "s_addc_u32 s61, s23, 0\n"
        "v_mfma_f32_16x16x32_bf16 v[16:19], a[152:155], a[204:207], v[16:19]\n"
        "ds_read_b64_tr_b16 a[180:181], v154 offset:32768\n"
        "s_xor_b32 s66, s17, 1\n"
        "s_lshl_b32 s66, s66, 15\n"
        "v_mfma_f32_16x16x32_bf16 v[20:23], a[156:159], a[204:207], v[20:23]\n"
        "ds_read_b64_tr_b16 a[182:183], v154 offset:36864\n"
        "v_mul_f32 v86, s54, v86\n"
        "v_mfma_f32_16x16x32_bf16 v[20:23], a[148:151], a[200:203], v[20:23]\n"
        "ds_read_b64_tr_b16 a[184:185], v153 offset:40960\n"
        "global_load_lds_dwordx4 v144, s[60:61] offset:0\n"
        "v_mfma_f32_16x16x32_bf16 v[20:23], a[140:143], a[196:199], v[20:23]\n"
        "ds_read_b64_tr_b16 a[186:187], v153 offset:45056\n"
        "v_mul_f32 v87, s54, v87\n"
        "v_mfma_f32_16x16x32_bf16 v[20:23], a[132:135], a[192:195], v[20:23]\n"
        "ds_read_b64_tr_b16 a[188:189], v154 offset:40960\n"
        "v_cvt_pk_bf16_f32 v128, 0, v128\n"
        "v_mfma_f32_16x16x32_bf16 v[28:31], a[132:135], a[208:211], v[28:31]\n"
        "ds_read_b64_tr_b16 a[190:191], v154 offset:45056\n"
        "v_cvt_pk_bf16_f32 v129, 0, v129\n"
        "v_mfma_f32_16x16x32_bf16 v[28:31], a[140:143], a[212:215], v[28:31]\n"
        "ds_read_b128 a[0:3], v254 offset:49152\n"
        "global_load_lds_dwordx4 v145, s[60:61] offset:1024\n"
        "v_mfma_f32_16x16x32_bf16 v[28:31], a[148:151], a[216:219], v[28:31]\n"
        "ds_read_b128 a[4:7], v254 offset:53248\n"
        "s_waitcnt lgkmcnt(8)\n"
        "v_mfma_f32_16x16x32_bf16 v[28:31], a[156:159], a[220:223], v[28:31]\n"
        "ds_read_b128 a[8:11], v254 offset:57344\n"
        "v_cvt_pk_bf16_f32 v130, 0, v130\n"
        "v_mfma_f32_16x16x32_bf16 v[24:27], a[152:155], a[220:223], v[24:27]\n"
        "ds_read_b128 a[12:15], v254 offset:61440\n"
        "v_cvt_pk_bf16_f32 v131, 0, v131\n"
        "v_mfma_f32_16x16x32_bf16 v[24:27], a[144:147], a[216:219], v[24:27]\n"
        "ds_read_b128 a[16:19], v254 offset:50176\n"
        "global_load_lds_dwordx4 v146, s[60:61] offset:2048\n"
        "v_mfma_f32_16x16x32_bf16 v[24:27], a[136:139], a[212:215], v[24:27]\n"
        "ds_read_b128 a[20:23], v254 offset:54272\n"
        "v_cvt_pk_bf16_f32 v132, 0, v132\n"
        "v_mfma_f32_16x16x32_bf16 v[24:27], a[128:131], a[208:211], v[24:27]\n"
        "ds_read_b128 a[24:27], v254 offset:58368\n"
        "s_waitcnt lgkmcnt(6)\n"
        "global_load_lds_dwordx4 v147, s[60:61] offset:3072\n"
        "s_add_u32 m0, s73, 0x14000\n"
        "v_mfma_f32_16x16x32_bf16 v[0:3], v[160:163], a[0:3], v[0:3]\n"
        "ds_read_b128 a[28:31], v254 offset:62464\n"
        "s_waitcnt lgkmcnt(6)\n"
        "v_mfma_f32_16x16x32_bf16 v[0:3], v[168:171], a[4:7], v[0:3]\n"
        "ds_read_b128 a[192:195], v254 offset:51200\n"
        "s_waitcnt lgkmcnt(6)\n"
        "v_mfma_f32_16x16x32_bf16 v[0:3], v[176:179], a[8:11], v[0:3]\n"
        "ds_read_b128 a[196:199], v254 offset:55296\n"
        "s_waitcnt lgkmcnt(6)\n"
        "v_mfma_f32_16x16x32_bf16 v[0:3], v[184:187], a[12:15], v[0:3]\n"
        "ds_read_b128 a[200:203], v254 offset:59392\n"
        "s_add_u32 s60, s68, s66\n"
        "s_addc_u32 s61, s69, 0\n"
        "v_mfma_f32_16x16x32_bf16 v[4:7], v[188:191], a[12:15], v[4:7]\n"
        "ds_read_b128 a[204:207], v254 offset:63488\n"
        "global_load_lds_dwordx4 v254, s[60:61] offset:0 sc1\n"
        "s_xor_b32 s66, s17, 2\n"
        "v_mfma_f32_16x16x32_bf16 v[4:7], v[180:183], a[8:11], v[4:7]\n"
        "ds_read_b128 a[208:211], v254 offset:52224\n"
        "s_lshl_b32 s66, s66, 15\n"
        "v_cvt_pk_bf16_f32 v133, 0, v133\n"
        "v_mfma_f32_16x16x32_bf16 v[4:7], v[172:175], a[4:7], v[4:7]\n"
        "ds_read_b128 a[212:215], v254 offset:56320\n"
        "v_cvt_pk_bf16_f32 v134, 0, v134\n"
        "v_mfma_f32_16x16x32_bf16 v[4:7], v[164:167], a[0:3], v[4:7]\n"
        "ds_read_b128 a[216:219], v254 offset:60416\n"
        "s_waitcnt lgkmcnt(8)\n"
        "v_mfma_f32_16x16x32_bf16 v[12:15], v[164:167], a[16:19], v[12:15]\n"
        "ds_read_b128 a[220:223], v254 offset:64512\n"
        "global_load_lds_dwordx4 v254, s[60:61] offset:1024 sc1\n"
        "v_mfma_f32_16x16x32_bf16 v[12:15], v[172:175], a[20:23], v[12:15]\n"
        "s_waitcnt lgkmcnt(7)\n"
        "v_cvt_pk_bf16_f32 v135, 0, v135\n"
        "v_lshl_add_u32 v229, v230, 4, v229\n"
        "v_mfma_f32_16x16x32_bf16 v[12:15], v[180:183], a[24:27], v[12:15]\n"
        "v_cvt_pk_bf16_f32 v80, 0, v80\n"
        "v_cvt_pk_bf16_f32 v81, 0, v81\n"
        "v_mfma_f32_16x16x32_bf16 v[12:15], v[188:191], a[28:31], v[12:15]\n"
        "v_cvt_pk_bf16_f32 v82, 0, v82\n"
        "v_cvt_pk_bf16_f32 v83, 0, v83\n"
        "v_mfma_f32_16x16x32_bf16 v[8:11], v[184:187], a[28:31], v[8:11]\n"
        "global_load_lds_dwordx4 v254, s[60:61] offset:2048 sc1\n"
        "v_cvt_pk_bf16_f32 v84, 0, v84\n"
        "v_mfma_f32_16x16x32_bf16 v[8:11], v[176:179], a[24:27], v[8:11]\n"
        "s_waitcnt lgkmcnt(3)\n"
        "v_cvt_pk_bf16_f32 v85, 0, v85\n"
        "v_cvt_pk_bf16_f32 v86, 0, v86\n"
        "v_mfma_f32_16x16x32_bf16 v[8:11], v[168:171], a[20:23], v[8:11]\n"
        "v_cvt_pk_bf16_f32 v87, 0, v87\n"
        "v_add_f32 v80, v80, v128\n"
        "v_mfma_f32_16x16x32_bf16 v[8:11], v[160:163], a[16:19], v[8:11]\n"
        "global_load_lds_dwordx4 v254, s[60:61] offset:3072 sc1\n"
        "s_add_u32 m0, s73, 0x0\n"
        "v_mfma_f32_16x16x32_bf16 v[16:19], v[160:163], a[192:195], v[16:19]\n"
        "s_add_u32 s60, s68, s66\n"
        "s_addc_u32 s61, s69, 0\n"
        "s_xor_b32 s66, s17, 3\n"
        "v_mfma_f32_16x16x32_bf16 v[16:19], v[168:171], a[196:199], v[16:19]\n"
        "s_lshl_b32 s66, s66, 15\n"
        "v_add_f32 v81, v81, v129\n"
        "v_add_f32 v82, v82, v130\n"
        "v_mfma_f32_16x16x32_bf16 v[16:19], v[176:179], a[200:203], v[16:19]\n"
        "v_add_f32 v83, v83, v131\n"
        "v_add_f32 v84, v84, v132\n"
        "v_mfma_f32_16x16x32_bf16 v[16:19], v[184:187], a[204:207], v[16:19]\n"
        "global_load_lds_dwordx4 v254, s[60:61] offset:0 sc1\n"
        "v_add_f32 v85, v85, v133\n"
        "v_mfma_f32_16x16x32_bf16 v[20:23], v[188:191], a[204:207], v[20:23]\n"
        "v_add_f32 v86, v86, v134\n"
        "v_add_f32 v87, v87, v135\n"
        "v_mfma_f32_16x16x32_bf16 v[20:23], v[180:183], a[200:203], v[20:23]\n"
        "s_waitcnt lgkmcnt(0)\n"
        "v_cvt_pk_bf16_f32 v136, 0, v136\n"
        "v_cvt_pk_bf16_f32 v137, 0, v137\n"
        "v_mfma_f32_16x16x32_bf16 v[20:23], v[172:175], a[196:199], v[20:23]\n"
        "global_load_lds_dwordx4 v254, s[60:61] offset:1024 sc1\n"
        "v_mfma_f32_16x16x32_bf16 v[20:23], v[164:167], a[192:195], v[20:23]\n"
        "v_cvt_pk_bf16_f32 v138, 0, v138\n"
        "v_cvt_pk_bf16_f32 v139, 0, v139\n"
        "v_mfma_f32_16x16x32_bf16 v[28:31], v[164:167], a[208:211], v[28:31]\n"
        "v_cvt_pk_bf16_f32 v140, 0, v140\n"
        "v_cvt_pk_bf16_f32 v141, 0, v141\n"
        "v_mfma_f32_16x16x32_bf16 v[28:31], v[172:175], a[212:215], v[28:31]\n"
        "v_cvt_pk_bf16_f32 v142, 0, v142\n"
        "v_cvt_pk_bf16_f32 v143, 0, v143\n"
        "v_mfma_f32_16x16x32_bf16 v[28:31], v[180:183], a[216:219], v[28:31]\n"
        "global_load_lds_dwordx4 v254, s[60:61] offset:2048 sc1\n"
        "v_mfma_f32_16x16x32_bf16 v[28:31], v[188:191], a[220:223], v[28:31]\n"
        "v_mfma_f32_16x16x32_bf16 v[24:27], v[184:187], a[220:223], v[24:27]\n"
        "v_mfma_f32_16x16x32_bf16 v[24:27], v[176:179], a[216:219], v[24:27]\n"
        "global_load_lds_dwordx4 v254, s[60:61] offset:3072 sc1\n"
        "s_add_u32 m0, s73, 0x4000\n"
        "v_mfma_f32_16x16x32_bf16 v[24:27], v[168:171], a[212:215], v[24:27]\n"
        "s_add_u32 s60, s68, s66\n"
        "s_addc_u32 s61, s69, 0\n"
        "s_sub_u32 s66, 15, s12\n"
        "v_mfma_f32_16x16x32_bf16 v[24:27], v[160:163], a[208:211], v[24:27]\n"
        "s_cmp_lt_u32 s74, 2\n"
        "s_cselect_b32 s66, s12, s66\n"
        "s_mul_i32 s66, s66, 1052672\n"
        "s_add_u32 s66, s66, s67\n"
        "s_add_u32 s68, s20, s66\n"
        "s_addc_u32 s69, s21, 0\n"
        "s_xor_b32 s66, s17, 4\n"
        "s_lshl_b32 s66, s66, 15\n"
        "s_and_b32 s67, s75, 1\n"
        "s_lshl_b32 s67, s67, 12\n"
        "s_waitcnt vmcnt(8)\n"
        "s_barrier\n"
        "ds_read_b64_tr_b16 a[32:33], v155 offset:32768\n"
        "ds_read_b64_tr_b16 a[34:35], v155 offset:36864\n"
        "ds_read_b64_tr_b16 a[36:37], v156 offset:32768\n"
        "ds_read_b64_tr_b16 a[38:39], v156 offset:36864\n"
        "ds_read_b64_tr_b16 a[40:41], v155 offset:40960\n"
        "ds_read_b64_tr_b16 a[42:43], v155 offset:45056\n"
        "ds_read_b64_tr_b16 a[44:45], v156 offset:40960\n"
        "ds_read_b64_tr_b16 a[46:47], v156 offset:45056\n"
        "ds_read_b64_tr_b16 a[224:225], v155 offset:0\n"
        "ds_read_b64_tr_b16 a[226:227], v155 offset:4096\n"
        "ds_read_b64_tr_b16 a[228:229], v156 offset:0\n"
        "ds_read_b64_tr_b16 a[230:231], v156 offset:4096\n"
        "ds_read_b64_tr_b16 a[232:233], v155 offset:8192\n"
        "ds_read_b64_tr_b16 a[234:235], v155 offset:12288\n"
        "ds_read_b64_tr_b16 a[236:237], v156 offset:8192\n"
        "ds_read_b64_tr_b16 a[238:239], v156 offset:12288\n"
        "global_load_lds_dwordx4 v254, s[60:61] offset:0 sc1\n"
        "ds_read_b128 a[0:3], v244 offset:49152\n"
        "ds_read_b128 a[4:7], v244 offset:53248\n"
        "ds_read_b128 a[8:11], v244 offset:57344\n"
        "ds_read_b128 a[12:15], v244 offset:61440\n"
        "global_load_lds_dwordx4 v254, s[60:61] offset:1024 sc1\n"
        "global_load_lds_dwordx4 v254, s[60:61] offset:2048 sc1\n"
        "ds_read_b128 a[16:19], v244 offset:50176\n"
        "ds_read_b128 a[20:23], v244 offset:54272\n"
        "s_waitcnt lgkmcnt(5)\n"
        "v_mfma_f32_16x16x32_bf16 v[0:3], v[192:195], a[0:3], v[0:3]\n"
        "ds_read_b128 a[24:27], v244 offset:58368\n"
        "s_waitcnt lgkmcnt(5)\n"
        "v_mfma_f32_16x16x32_bf16 v[0:3], v[200:203], a[4:7], v[0:3]\n"
        "ds_read_b128 a[28:31], v244 offset:62464\n"
        "s_waitcnt lgkmcnt(4)\n"
        "v_mfma_f32_16x16x32_bf16 v[0:3], v[208:211], a[8:11], v[0:3]\n"
        "ds_read_b128 a[192:195], v244 offset:51200\n"
        "global_load_lds_dwordx4 v254, s[60:61] offset:3072 sc1\n"
        "s_add_u32 m0, s73, 0x8000\n"
        "v_mfma_f32_16x16x32_bf16 v[0:3], v[216:219], a[12:15], v[0:3]\n"
        "ds_read_b128 a[196:199], v244 offset:55296\n"
        "s_add_u32 s60, s68, s66\n"
        "s_addc_u32 s61, s69, 0\n"
        "v_mfma_f32_16x16x32_bf16 v[4:7], v[220:223], a[12:15], v[4:7]\n"
        "ds_read_b128 a[200:203], v244 offset:59392\n"
        "s_xor_b32 s66, s17, 5\n"
        "s_lshl_b32 s66, s66, 15\n"
        "v_mfma_f32_16x16x32_bf16 v[4:7], v[212:215], a[8:11], v[4:7]\n"
        "ds_read_b128 a[204:207], v244 offset:63488\n"
        "v_mfma_f32_16x16x32_bf16 v[4:7], v[204:207], a[4:7], v[4:7]\n"
        "ds_read_b128 a[208:211], v244 offset:52224\n"
        "global_load_lds_dwordx4 v254, s[60:61] offset:0 sc1\n"
        "v_mfma_f32_16x16x32_bf16 v[4:7], v[196:199], a[0:3], v[4:7]\n"
        "ds_read_b128 a[212:215], v244 offset:56320\n"
        "s_waitcnt lgkmcnt(8)\n"
        "v_mfma_f32_16x16x32_bf16 v[12:15], v[196:199], a[16:19], v[12:15]\n"
        "ds_read_b128 a[216:219], v244 offset:60416\n"
        "s_waitcnt lgkmcnt(8)\n"
        "v_mfma_f32_16x16x32_bf16 v[12:15], v[204:207], a[20:23], v[12:15]\n"
        "ds_read_b128 a[220:223], v244 offset:64512\n"
        "s_waitcnt lgkmcnt(8)\n"
        "v_mfma_f32_16x16x32_bf16 v[12:15], v[212:215], a[24:27], v[12:15]\n"
        "global_load_lds_dwordx4 v254, s[60:61] offset:1024 sc1\n"
        "v_mfma_f32_16x16x32_bf16 v[12:15], v[220:223], a[28:31], v[12:15]\n"
        "v_mfma_f32_16x16x32_bf16 v[8:11], v[216:219], a[28:31], v[8:11]\n"
        "s_waitcnt lgkmcnt(5)\n"
        "v_mfma_f32_16x16x32_bf16 v[8:11], v[208:211], a[24:27], v[8:11]\n"
        "global_load_lds_dwordx4 v254, s[60:61] offset:2048 sc1\n"
        "v_mfma_f32_16x16x32_bf16 v[8:11], v[200:203], a[20:23], v[8:11]\n"
        "v_mfma_f32_16x16x32_bf16 v[8:11], v[192:195], a[16:19], v[8:11]\n"
        "s_waitcnt lgkmcnt(2)\n"
        "v_mfma_f32_16x16x32_bf16 v[16:19], v[192:195], a[192:195], v[16:19]\n"
        "s_barrier\n"
        "v_mfma_f32_16x16x32_bf16 v[16:19], v[200:203], a[196:199], v[16:19]\n"
        "ds_read_b64_tr_b16 a[48:49], v157 offset:0\n"
        "global_load_lds_dwordx4 v254, s[60:61] offset:3072 sc1\n"
        "s_add_u32 m0, s73, 0xc000\n"
        "v_mfma_f32_16x16x32_bf16 v[16:19], v[208:211], a[200:203], v[16:19]\n"
        "ds_read_b64_tr_b16 a[50:51], v157 offset:4096\n"
        "s_add_u32 s60, s68, s66\n"
        "s_addc_u32 s61, s69, 0\n"
        "v_mfma_f32_16x16x32_bf16 v[16:19], v[216:219], a[204:207], v[16:19]\n"
        "ds_read_b64_tr_b16 a[52:53], v158 offset:0\n"
        "s_xor_b32 s66, s17, 6\n"
        "s_lshl_b32 s66, s66, 15\n"
        "v_mfma_f32_16x16x32_bf16 v[20:23], v[220:223], a[204:207], v[20:23]\n"
        "ds_read_b64_tr_b16 a[54:55], v158 offset:4096\n"
        "v_mfma_f32_16x16x32_bf16 v[20:23], v[212:215], a[200:203], v[20:23]\n"
        "ds_read_b64_tr_b16 a[56:57], v157 offset:8192\n"
        "global_load_lds_dwordx4 v254, s[60:61] offset:0 sc1\n"
        "v_mfma_f32_16x16x32_bf16 v[20:23], v[204:207], a[196:199], v[20:23]\n"
        "ds_read_b64_tr_b16 a[58:59], v157 offset:12288\n"
        "s_waitcnt lgkmcnt(6)\n"
        "v_mfma_f32_16x16x32_bf16 v[20:23], v[196:199], a[192:195], v[20:23]\n"
        "ds_read_b64_tr_b16 a[60:61], v158 offset:8192\n"
        "v_mfma_f32_16x16x32_bf16 v[28:31], v[196:199], a[208:211], v[28:31]\n"
        "ds_read_b64_tr_b16 a[62:63], v158 offset:12288\n"
        "v_mfma_f32_16x16x32_bf16 v[28:31], v[204:207], a[212:215], v[28:31]\n"
        "ds_read_b128 a[0:3], v245 offset:16384\n"
        "global_load_lds_dwordx4 v254, s[60:61] offset:1024 sc1\n"
        "v_mfma_f32_16x16x32_bf16 v[28:31], v[212:215], a[216:219], v[28:31]\n"
        "ds_read_b128 a[4:7], v245 offset:20480\n"
        "v_mfma_f32_16x16x32_bf16 v[28:31], v[220:223], a[220:223], v[28:31]\n"
        "ds_read_b128 a[8:11], v245 offset:24576\n"
        "v_mfma_f32_16x16x32_bf16 v[24:27], v[216:219], a[220:223], v[24:27]\n"
        "ds_read_b128 a[12:15], v245 offset:28672\n"
        "v_mfma_f32_16x16x32_bf16 v[24:27], v[208:211], a[216:219], v[24:27]\n"
        "ds_read_b128 a[16:19], v245 offset:17408\n"
        "global_load_lds_dwordx4 v254, s[60:61] offset:2048 sc1\n"
        "v_mfma_f32_16x16x32_bf16 v[24:27], v[200:203], a[212:215], v[24:27]\n"
        "ds_read_b128 a[20:23], v245 offset:21504\n"
        "v_mfma_f32_16x16x32_bf16 v[24:27], v[192:195], a[208:211], v[24:27]\n"
        "ds_read_b128 a[24:27], v245 offset:25600\n"
        "ds_read_b128 a[28:31], v245 offset:29696\n"
        "s_waitcnt lgkmcnt(7)\n"
        "global_load_lds_dwordx4 v254, s[60:61] offset:3072 sc1\n"
        "s_add_u32 m0, s73, 0x18000\n"
        "v_mfma_f32_16x16x32_bf16 v[32:35], a[96:99], a[0:3], 0\n"
        "ds_read_b128 a[192:195], v245 offset:18432\n"
        "s_waitcnt lgkmcnt(7)\n"
        "v_mfma_f32_16x16x32_bf16 v[32:35], a[104:107], a[4:7], v[32:35]\n"
        "ds_read_b128 a[196:199], v245 offset:22528\n"
        "s_waitcnt lgkmcnt(7)\n"
        "v_mfma_f32_16x16x32_bf16 v[32:35], a[112:115], a[8:11], v[32:35]\n"
        "ds_read_b128 a[200:203], v245 offset:26624\n"
        "s_waitcnt lgkmcnt(7)\n"
        "v_mfma_f32_16x16x32_bf16 v[32:35], a[120:123], a[12:15], v[32:35]\n"
        "ds_read_b128 a[204:207], v245 offset:30720\n"
        "s_add_u32 s60, s68, s66\n"
        "s_addc_u32 s61, s69, 0\n"
        "v_mfma_f32_16x16x32_bf16 v[36:39], a[124:127], a[12:15], 0\n"
        "ds_read_b128 a[208:211], v245 offset:19456\n"
        "global_load_lds_dwordx4 v254, s[60:61] offset:0 sc1\n"
        "s_xor_b32 s66, s17, 7\n"
        "v_mfma_f32_16x16x32_bf16 v[36:39], a[116:119], a[8:11], v[36:39]\n"
        "ds_read_b128 a[212:215], v245 offset:23552\n"
        "s_lshl_b32 s66, s66, 15\n"
        "s_waitcnt lgkmcnt(8)\n"
        "v_mfma_f32_16x16x32_bf16 v[36:39], a[108:111], a[4:7], v[36:39]\n"
        "ds_read_b128 a[216:219], v245 offset:27648\n"
        "s_waitcnt lgkmcnt(7)\n"
        "v_mfma_f32_16x16x32_bf16 v[36:39], a[100:103], a[0:3], v[36:39]\n"
        "ds_read_b128 a[220:223], v245 offset:31744\n"
        "v_mfma_f32_16x16x32_bf16 v[44:47], a[100:103], a[16:19], 0\n"
        "global_load_lds_dwordx4 v254, s[60:61] offset:1024 sc1\n"
        "v_mfma_f32_16x16x32_bf16 v[44:47], a[108:111], a[20:23], v[44:47]\n"
        "v_mfma_f32_16x16x32_bf16 v[44:47], a[116:119], a[24:27], v[44:47]\n"
        "v_mfma_f32_16x16x32_bf16 v[44:47], a[124:127], a[28:31], v[44:47]\n"
        "global_load_lds_dwordx4 v254, s[60:61] offset:2048 sc1\n"
        "v_mfma_f32_16x16x32_bf16 v[40:43], a[120:123], a[28:31], 0\n"
        "s_waitcnt lgkmcnt(3)\n"
        "v_mfma_f32_16x16x32_bf16 v[40:43], a[112:115], a[24:27], v[40:43]\n"
        "v_mfma_f32_16x16x32_bf16 v[40:43], a[104:107], a[20:23], v[40:43]\n"
        "v_mfma_f32_16x16x32_bf16 v[40:43], a[96:99], a[16:19], v[40:43]\n"
        "global_load_lds_dwordx4 v254, s[60:61] offset:3072 sc1\n"
        "s_add_u32 m0, s73, 0x10000\n"
        "s_waitcnt vmcnt(20)\n"
        "v_mfma_f32_16x16x32_bf16 v[48:51], a[96:99], a[192:195], 0\n"
        "s_barrier\n"
        "v_mfma_f32_16x16x32_bf16 v[48:51], a[104:107], a[196:199], v[48:51]\n"
        "ds_read_b128 a[0:3], v244 offset:16384\n"
        "s_add_u32 s60, s68, s66\n"
        "s_addc_u32 s61, s69, 0\n"
        "v_mfma_f32_16x16x32_bf16 v[48:51], a[112:115], a[200:203], v[48:51]\n"
        "ds_read_b128 a[4:7], v244 offset:20480\n"
        "global_load_lds_dwordx4 v254, s[60:61] offset:0 sc1\n"
        "s_sub_u32 s66, 15, s12\n"
        "v_mfma_f32_16x16x32_bf16 v[48:51], a[120:123], a[204:207], v[48:51]\n"
        "ds_read_b128 a[8:11], v244 offset:24576\n"
        "s_cmp_lt_u32 s75, 2\n"
        "s_cselect_b32 s66, s12, s66\n"
        "v_mfma_f32_16x16x32_bf16 v[52:55], a[124:127], a[204:207], 0\n"
        "ds_read_b128 a[12:15], v244 offset:28672\n"
        "s_mul_i32 s66, s66, 1052672\n"
        "s_add_u32 s66, s66, s67\n"
        "v_mfma_f32_16x16x32_bf16 v[52:55], a[116:119], a[200:203], v[52:55]\n"
        "ds_read_b128 a[16:19], v244 offset:17408\n"
        "s_add_u32 s68, s20, s66\n"
        "s_addc_u32 s69, s21, 0\n"
        "v_mfma_f32_16x16x32_bf16 v[52:55], a[108:111], a[196:199], v[52:55]\n"
        "ds_read_b128 a[20:23], v244 offset:21504\n"
        "global_load_lds_dwordx4 v254, s[60:61] offset:1024 sc1\n"
        "s_xor_b32 s66, s17, 4\n"
        "v_mfma_f32_16x16x32_bf16 v[52:55], a[100:103], a[192:195], v[52:55]\n"
        "ds_read_b128 a[24:27], v244 offset:25600\n"
        "s_lshl_b32 s66, s66, 15\n"
        "s_and_b32 s67, s76, 1\n"
        "v_mfma_f32_16x16x32_bf16 v[60:63], a[100:103], a[208:211], 0\n"
        "ds_read_b128 a[28:31], v244 offset:29696\n"
        "s_lshl_b32 s67, s67, 12\n"
        "s_waitcnt lgkmcnt(8)\n"
        "v_mfma_f32_16x16x32_bf16 v[60:63], a[108:111], a[212:215], v[60:63]\n"
        "ds_read_b128 a[192:195], v244 offset:18432\n"
        "s_lshl2_add_u32 s15, s74, s10\n"
        "s_mul_i32 s16, s13, 263168\n"
        "v_mfma_f32_16x16x32_bf16 v[60:63], a[116:119], a[216:219], v[60:63]\n"
        "ds_read_b128 a[196:199], v244 offset:22528\n"
        "global_load_lds_dwordx4 v254, s[60:61] offset:2048 sc1\n"
        "s_lshl_b32 s15, s15, 14\n"
        "v_mfma_f32_16x16x32_bf16 v[60:63], a[124:127], a[220:223], v[60:63]\n"
        "ds_read_b128 a[200:203], v244 offset:26624\n"
        "s_add_u32 s16, s16, s15\n"
        "s_add_u32 s16, s16, s73\n"
        "v_mfma_f32_16x16x32_bf16 v[56:59], a[120:123], a[220:223], 0\n"
        "ds_read_b128 a[204:207], v244 offset:30720\n"
        "s_add_u32 s16, s16, 0x4000000\n"
        "s_waitcnt lgkmcnt(8)\n"
        "v_mfma_f32_16x16x32_bf16 v[56:59], a[112:115], a[216:219], v[56:59]\n"
        "ds_read_b128 a[216:219], v244 offset:27648\n"
        "s_add_u32 s28, s38, s16\n"
        "s_addc_u32 s29, s39, 0\n"
        "v_mfma_f32_16x16x32_bf16 v[56:59], a[104:107], a[212:215], v[56:59]\n"
        "ds_read_b128 a[212:215], v244 offset:23552\n"
        "global_load_lds_dwordx4 v254, s[60:61] offset:3072 sc1\n"
        "s_add_u32 m0, s73, 0x1c000\n"
        "v_mfma_f32_16x16x32_bf16 v[56:59], a[96:99], a[208:211], v[56:59]\n"
        "ds_read_b128 a[208:211], v244 offset:19456\n"
        "s_add_u32 s60, s68, s66\n"
        "s_addc_u32 s61, s69, 0\n"
        "v_mfma_f32_16x16x32_bf16 v[32:35], a[128:131], a[0:3], v[32:35]\n"
        "ds_read_b128 a[220:223], v244 offset:31744\n"
        "s_xor_b32 s66, s17, 5\n"
        "s_lshl_b32 s66, s66, 15\n"
        "v_mfma_f32_16x16x32_bf16 v[32:35], a[136:139], a[4:7], v[32:35]\n"
        "v_mfma_f32_16x16x32_bf16 v[32:35], a[144:147], a[8:11], v[32:35]\n"
        "global_load_lds_dwordx4 v254, s[60:61] offset:0 sc1\n"
        "v_mfma_f32_16x16x32_bf16 v[32:35], a[152:155], a[12:15], v[32:35]\n"
        "v_mfma_f32_16x16x32_bf16 v[36:39], a[156:159], a[12:15], v[36:39]\n"
        "s_waitcnt lgkmcnt(4)\n"
        "v_mfma_f32_16x16x32_bf16 v[36:39], a[148:151], a[8:11], v[36:39]\n"
        "global_load_lds_dwordx4 v254, s[60:61] offset:1024 sc1\n"
        "v_mfma_f32_16x16x32_bf16 v[36:39], a[140:143], a[4:7], v[36:39]\n"
        "v_mfma_f32_16x16x32_bf16 v[36:39], a[132:135], a[0:3], v[36:39]\n"
        "v_mfma_f32_16x16x32_bf16 v[44:47], a[132:135], a[16:19], v[44:47]\n"
        "v_mfma_f32_16x16x32_bf16 v[44:47], a[140:143], a[20:23], v[44:47]\n"
        "global_load_lds_dwordx4 v254, s[60:61] offset:2048 sc1\n"
        "v_mfma_f32_16x16x32_bf16 v[44:47], a[148:151], a[24:27], v[44:47]\n"
        "v_mfma_f32_16x16x32_bf16 v[44:47], a[156:159], a[28:31], v[44:47]\n"
        "v_mfma_f32_16x16x32_bf16 v[40:43], a[152:155], a[28:31], v[40:43]\n"
        "global_load_lds_dwordx4 v254, s[60:61] offset:3072 sc1\n"
        "s_add_u32 m0, s73, 0x20000\n"
        "v_mfma_f32_16x16x32_bf16 v[40:43], a[144:147], a[24:27], v[40:43]\n"
        "s_add_u32 s60, s68, s66\n"
        "s_addc_u32 s61, s69, 0\n"
        "s_xor_b32 s66, s17, 6\n"
        "v_mfma_f32_16x16x32_bf16 v[40:43], a[136:139], a[20:23], v[40:43]\n"
        "s_lshl_b32 s66, s66, 15\n"
        "v_mfma_f32_16x16x32_bf16 v[40:43], a[128:131], a[16:19], v[40:43]\n"
        "s_waitcnt vmcnt(24)\n"
        "v_mfma_f32_16x16x32_bf16 v[48:51], a[128:131], a[192:195], v[48:51]\n"
        "s_barrier\n"
        "v_mfma_f32_16x16x32_bf16 v[48:51], a[136:139], a[196:199], v[48:51]\n"
        "ds_read_b128 a[0:3], v254 offset:0\n"
        "global_load_lds_dwordx4 v254, s[60:61] offset:0 sc1\n"
        "v_mfma_f32_16x16x32_bf16 v[48:51], a[144:147], a[200:203], v[48:51]\n"
        "ds_read_b128 a[4:7], v254 offset:4096\n"
        "v_mfma_f32_16x16x32_bf16 v[48:51], a[152:155], a[204:207], v[48:51]\n"
        "ds_read_b128 a[8:11], v254 offset:8192\n"
        "v_mfma_f32_16x16x32_bf16 v[52:55], a[156:159], a[204:207], v[52:55]\n"
        "ds_read_b128 a[12:15], v254 offset:12288\n"
        "s_waitcnt lgkmcnt(4)\n"
        "v_mfma_f32_16x16x32_bf16 v[52:55], a[148:151], a[200:203], v[52:55]\n"
        "ds_read_b128 a[16:19], v254 offset:1024\n"
        "global_load_lds_dwordx4 v254, s[60:61] offset:1024 sc1\n"
        "v_mfma_f32_16x16x32_bf16 v[52:55], a[140:143], a[196:199], v[52:55]\n"
        "ds_read_b128 a[20:23], v254 offset:5120\n"
        "v_mfma_f32_16x16x32_bf16 v[52:55], a[132:135], a[192:195], v[52:55]\n"
        "ds_read_b128 a[24:27], v254 offset:9216\n"
        "v_mfma_f32_16x16x32_bf16 v[60:63], a[132:135], a[208:211], v[60:63]\n"
        "ds_read_b128 a[28:31], v254 offset:13312\n"
        "v_mfma_f32_16x16x32_bf16 v[60:63], a[140:143], a[212:215], v[60:63]\n"
        "ds_read_b128 a[192:195], v254 offset:2048\n"
        "global_load_lds_dwordx4 v254, s[60:61] offset:2048 sc1\n"
        "v_mfma_f32_16x16x32_bf16 v[60:63], a[148:151], a[216:219], v[60:63]\n"
        "ds_read_b128 a[196:199], v254 offset:6144\n"
        "v_mfma_f32_16x16x32_bf16 v[60:63], a[156:159], a[220:223], v[60:63]\n"
        "ds_read_b128 a[200:203], v254 offset:10240\n"
        "v_mfma_f32_16x16x32_bf16 v[56:59], a[152:155], a[220:223], v[56:59]\n"
        "ds_read_b128 a[204:207], v254 offset:14336\n"
        "s_waitcnt lgkmcnt(8)\n"
        "v_mfma_f32_16x16x32_bf16 v[56:59], a[144:147], a[216:219], v[56:59]\n"
        "ds_read_b128 a[216:219], v254 offset:11264\n"
        "global_load_lds_dwordx4 v254, s[60:61] offset:3072 sc1\n"
        "s_add_u32 m0, s73, 0x24000\n"
        "v_mfma_f32_16x16x32_bf16 v[56:59], a[136:139], a[212:215], v[56:59]\n"
        "ds_read_b128 a[212:215], v254 offset:7168\n"
        "s_add_u32 s60, s68, s66\n"
        "s_addc_u32 s61, s69, 0\n"
        "v_mfma_f32_16x16x32_bf16 v[56:59], a[128:131], a[208:211], v[56:59]\n"
        "ds_read_b128 a[208:211], v254 offset:3072\n"
        "s_xor_b32 s66, s17, 7\n"
        "s_lshl_b32 s66, s66, 15\n"
        "v_mfma_f32_16x16x32_bf16 v[32:35], v[160:163], a[0:3], v[32:35]\n"
        "ds_read_b128 a[220:223], v254 offset:15360\n"
        "v_mfma_f32_16x16x32_bf16 v[32:35], v[168:171], a[4:7], v[32:35]\n"
        "global_load_lds_dwordx4 v254, s[60:61] offset:0 sc1\n"
        "v_mfma_f32_16x16x32_bf16 v[32:35], v[176:179], a[8:11], v[32:35]\n"
        "v_mfma_f32_16x16x32_bf16 v[32:35], v[184:187], a[12:15], v[32:35]\n"
        "v_mfma_f32_16x16x32_bf16 v[36:39], v[188:191], a[12:15], v[36:39]\n"
        "global_load_lds_dwordx4 v254, s[60:61] offset:1024 sc1\n"
        "v_mfma_f32_16x16x32_bf16 v[36:39], v[180:183], a[8:11], v[36:39]\n"
        "s_waitcnt lgkmcnt(3)\n"
        "v_mfma_f32_16x16x32_bf16 v[36:39], v[172:175], a[4:7], v[36:39]\n"
        "v_mfma_f32_16x16x32_bf16 v[36:39], v[164:167], a[0:3], v[36:39]\n"
        "v_mfma_f32_16x16x32_bf16 v[44:47], v[164:167], a[16:19], v[44:47]\n"
        "global_load_lds_dwordx4 v254, s[60:61] offset:2048 sc1\n"
        "v_mfma_f32_16x16x32_bf16 v[44:47], v[172:175], a[20:23], v[44:47]\n"
        "v_mfma_f32_16x16x32_bf16 v[44:47], v[180:183], a[24:27], v[44:47]\n"
        "v_mfma_f32_16x16x32_bf16 v[44:47], v[188:191], a[28:31], v[44:47]\n"
        "global_load_lds_dwordx4 v254, s[60:61] offset:3072 sc1\n"
        "s_add_u32 m0, s73, 0x14000\n"
        "v_mfma_f32_16x16x32_bf16 v[40:43], v[184:187], a[28:31], v[40:43]\n"
        "s_add_u32 s60, s68, s66\n"
        "s_addc_u32 s61, s69, 0\n"
        "s_sub_u32 s66, 15, s12\n"
        "LHB0_%=:\n"
        "s_cbranch_execz LHRB_%=\n"
        "v_mfma_f32_16x16x32_bf16 v[40:43], v[176:179], a[24:27], v[40:43]\n"
        "s_cmp_lt_u32 s76, 2\n"
        "s_cselect_b32 s66, s12, s66\n"
        "s_mul_i32 s66, s66, 1052672\n"
        "v_mfma_f32_16x16x32_bf16 v[40:43], v[168:171], a[20:23], v[40:43]\n"
        "s_add_u32 s66, s66, s67\n"
        "s_add_u32 s68, s20, s66\n"
        "s_addc_u32 s69, s21, 0\n"
        "v_mfma_f32_16x16x32_bf16 v[40:43], v[160:163], a[16:19], v[40:43]\n"
        "s_waitcnt vmcnt(28)\n"
        "s_xor_b32 s66, s17, 0\n"
        "s_lshl_b32 s66, s66, 15\n"
        "v_mfma_f32_16x16x32_bf16 v[48:51], v[160:163], a[192:195], v[48:51]\n"
        "s_barrier\n"
        "v_mfma_f32_16x16x32_bf16 v[48:51], v[168:171], a[196:199], v[48:51]\n"
        "ds_read_b128 a[0:3], v254 offset:16384\n"
        "global_load_lds_dwordx4 v254, s[60:61] offset:0 sc1\n"
        "v_mfma_f32_16x16x32_bf16 v[48:51], v[176:179], a[200:203], v[48:51]\n"
        "ds_read_b128 a[4:7], v254 offset:20480\n"
        "v_mfma_f32_16x16x32_bf16 v[48:51], v[184:187], a[204:207], v[48:51]\n"
        "ds_read_b128 a[8:11], v254 offset:24576\n"
        "v_mfma_f32_16x16x32_bf16 v[52:55], v[188:191], a[204:207], v[52:55]\n"
        "ds_read_b128 a[12:15], v254 offset:28672\n"
        "s_waitcnt lgkmcnt(4)\n"
        "v_mfma_f32_16x16x32_bf16 v[52:55], v[180:183], a[200:203], v[52:55]\n"
        "ds_read_b128 a[16:19], v254 offset:17408\n"
        "global_load_lds_dwordx4 v254, s[60:61] offset:1024 sc1\n"
        "v_mfma_f32_16x16x32_bf16 v[52:55], v[172:175], a[196:199], v[52:55]\n"
        "ds_read_b128 a[20:23], v254 offset:21504\n"
        "v_mfma_f32_16x16x32_bf16 v[52:55], v[164:167], a[192:195], v[52:55]\n"
        "ds_read_b128 a[24:27], v254 offset:25600\n"
        "v_mfma_f32_16x16x32_bf16 v[60:63], v[164:167], a[208:211], v[60:63]\n"
        "ds_read_b128 a[28:31], v254 offset:29696\n"
        "v_mfma_f32_16x16x32_bf16 v[60:63], v[172:175], a[212:215], v[60:63]\n"
        "ds_read_b128 a[192:195], v254 offset:18432\n"
        "global_load_lds_dwordx4 v254, s[60:61] offset:2048 sc1\n"
        "v_mfma_f32_16x16x32_bf16 v[60:63], v[180:183], a[216:219], v[60:63]\n"
        "ds_read_b128 a[196:199], v254 offset:22528\n"
        "v_mfma_f32_16x16x32_bf16 v[60:63], v[188:191], a[220:223], v[60:63]\n"
        "ds_read_b128 a[200:203], v254 offset:26624\n"
        "v_mfma_f32_16x16x32_bf16 v[56:59], v[184:187], a[220:223], v[56:59]\n"
        "ds_read_b128 a[204:207], v254 offset:30720\n"
        "s_waitcnt lgkmcnt(8)\n"
        "v_mfma_f32_16x16x32_bf16 v[56:59], v[176:179], a[216:219], v[56:59]\n"
        "ds_read_b128 a[216:219], v254 offset:27648\n"
        "global_load_lds_dwordx4 v254, s[60:61] offset:3072 sc1\n"
        "s_add_u32 m0, s73, 0x0\n"
        "v_mfma_f32_16x16x32_bf16 v[56:59], v[168:171], a[212:215], v[56:59]\n"
        "ds_read_b128 a[212:215], v254 offset:23552\n"
        "s_add_u32 s60, s68, s66\n"
        "s_addc_u32 s61, s69, 0\n"
        "v_mfma_f32_16x16x32_bf16 v[56:59], v[160:163], a[208:211], v[56:59]\n"
        "ds_read_b128 a[208:211], v254 offset:19456\n"
        "s_xor_b32 s66, s17, 1\n"
        "s_lshl_b32 s66, s66, 15\n"
        "v_mfma_f32_16x16x32_bf16 v[32:35], v[192:195], a[0:3], v[32:35]\n"
        "ds_read_b128 a[220:223], v254 offset:31744\n"
        "v_mfma_f32_16x16x32_bf16 v[32:35], v[200:203], a[4:7], v[32:35]\n"
        "global_store_dwordx4 v229, v[88:91], s[24:25] offset:0 sc1\n"
        "v_mfma_f32_16x16x32_bf16 v[32:35], v[208:211], a[8:11], v[32:35]\n"
        "v_mfma_f32_16x16x32_bf16 v[32:35], v[216:219], a[12:15], v[32:35]\n"
        "v_cvt_pk_bf16_f32 v88, v80, v81\n"
        "v_cvt_pk_bf16_f32 v89, v82, v83\n"
        "v_mfma_f32_16x16x32_bf16 v[36:39], v[220:223], a[12:15], v[36:39]\n"
        "s_waitcnt lgkmcnt(4)\n"
        "v_cvt_pk_bf16_f32 v90, v84, v85\n"
        "v_cvt_pk_bf16_f32 v91, v86, v87\n"
        "v_mfma_f32_16x16x32_bf16 v[36:39], v[212:215], a[8:11], v[36:39]\n"
        "v_permlane16_swap_b32 v88, v90\n"
        "v_mfma_f32_16x16x32_bf16 v[36:39], v[204:207], a[4:7], v[36:39]\n"
        "v_permlane16_swap_b32 v89, v91\n"
        "v_mfma_f32_16x16x32_bf16 v[36:39], v[196:199], a[0:3], v[36:39]\n"
        "v_and_b32 v80, s35, v77\n"
        "v_lshlrev_b32 v81, 16, v77\n"
        "v_mfma_f32_16x16x32_bf16 v[44:47], v[196:199], a[16:19], v[44:47]\n"
        "v_and_b32 v82, s35, v76\n"
        "v_lshlrev_b32 v83, 16, v76\n"
        "v_mfma_f32_16x16x32_bf16 v[44:47], v[204:207], a[20:23], v[44:47]\n"
        "v_and_b32 v84, s35, v79\n"
        "v_lshlrev_b32 v85, 16, v79\n"
        "v_mfma_f32_16x16x32_bf16 v[44:47], v[212:215], a[24:27], v[44:47]\n"
        "v_and_b32 v86, s35, v78\n"
        "v_lshlrev_b32 v87, 16, v78\n"
        "v_mfma_f32_16x16x32_bf16 v[44:47], v[220:223], a[28:31], v[44:47]\n"
        "v_mul_f32 v80, s54, v80\n"
        "v_mul_f32 v81, s54, v81\n"
        "v_mfma_f32_16x16x32_bf16 v[40:43], v[216:219], a[28:31], v[40:43]\n"
        "v_mul_f32 v82, s54, v82\n"
        "v_mul_f32 v83, s54, v83\n"
        "v_mfma_f32_16x16x32_bf16 v[40:43], v[208:211], a[24:27], v[40:43]\n"
        "v_mul_f32 v84, s54, v84\n"
        "v_mul_f32 v85, s54, v85\n"
        "v_mfma_f32_16x16x32_bf16 v[40:43], v[200:203], a[20:23], v[40:43]\n"
        "v_mul_f32 v86, s54, v86\n"
        "v_mul_f32 v87, s54, v87\n"
        "v_mfma_f32_16x16x32_bf16 v[40:43], v[192:195], a[16:19], v[40:43]\n"
        "s_waitcnt vmcnt(29)\n"
        "v_cvt_pk_bf16_f32 v80, 0, v80\n"
        "v_cvt_pk_bf16_f32 v81, 0, v81\n"
        "v_mfma_f32_16x16x32_bf16 v[48:51], v[192:195], a[192:195], v[48:51]\n"
        "s_barrier\n"
        "v_mfma_f32_16x16x32_bf16 v[48:51], v[200:203], a[196:199], v[48:51]\n"
        "ds_read_b128 a[0:3], v254 offset:32768\n"
        "global_load_lds_dwordx4 v254, s[60:61] offset:0 sc1\n"
        "v_mfma_f32_16x16x32_bf16 v[48:51], v[208:211], a[200:203], v[48:51]\n"
        "ds_read_b128 a[4:7], v254 offset:36864\n"
        "v_cvt_pk_bf16_f32 v82, 0, v82\n"
        "v_mfma_f32_16x16x32_bf16 v[48:51], v[216:219], a[204:207], v[48:51]\n"
        "ds_read_b128 a[8:11], v254 offset:40960\n"
        "v_cvt_pk_bf16_f32 v83, 0, v83\n"
        "v_mfma_f32_16x16x32_bf16 v[52:55], v[220:223], a[204:207], v[52:55]\n"
        "ds_read_b128 a[12:15], v254 offset:45056\n"
        "v_cvt_pk_bf16_f32 v84, 0, v84\n"
        "v_mfma_f32_16x16x32_bf16 v[52:55], v[212:215], a[200:203], v[52:55]\n"
        "ds_read_b128 a[16:19], v254 offset:33792\n"
        "global_load_lds_dwordx4 v254, s[60:61] offset:1024 sc1\n"
        "v_mfma_f32_16x16x32_bf16 v[52:55], v[204:207], a[196:199], v[52:55]\n"
        "ds_read_b128 a[20:23], v254 offset:37888\n"
        "v_cvt_pk_bf16_f32 v85, 0, v85\n"
        "v_mfma_f32_16x16x32_bf16 v[52:55], v[196:199], a[192:195], v[52:55]\n"
        "ds_read_b128 a[24:27], v254 offset:41984\n"
        "s_waitcnt lgkmcnt(7)\n"
        "v_mfma_f32_16x16x32_bf16 v[60:63], v[196:199], a[208:211], v[60:63]\n"
        "ds_read_b128 a[28:31], v254 offset:46080\n"
        "v_cvt_pk_bf16_f32 v86, 0, v86\n"
        "v_mfma_f32_16x16x32_bf16 v[60:63], v[204:207], a[212:215], v[60:63]\n"
        "ds_read_b128 a[192:195], v254 offset:34816\n"
        "global_load_lds_dwordx4 v254, s[60:61] offset:2048 sc1\n"
        "v_mfma_f32_16x16x32_bf16 v[60:63], v[212:215], a[216:219], v[60:63]\n"
        "ds_read_b128 a[196:199], v254 offset:38912\n"
        "v_cvt_pk_bf16_f32 v87, 0, v87\n"
        "v_mfma_f32_16x16x32_bf16 v[60:63], v[220:223], a[220:223], v[60:63]\n"
        "ds_read_b128 a[200:203], v254 offset:43008\n"
        "v_add_f32 v80, v80, v136\n"
        "v_mfma_f32_16x16x32_bf16 v[56:59], v[216:219], a[220:223], v[56:59]\n"
        "ds_read_b128 a[204:207], v254 offset:47104\n"
        "v_add_f32 v81, v81, v137\n"
        "v_mfma_f32_16x16x32_bf16 v[56:59], v[208:211], a[216:219], v[56:59]\n"
        "ds_read_b128 a[216:219], v254 offset:44032\n"
        "global_load_lds_dwordx4 v254, s[60:61] offset:3072 sc1\n"
        "s_add_u32 m0, s73, 0x4000\n"
        "v_mfma_f32_16x16x32_bf16 v[56:59], v[200:203], a[212:215], v[56:59]\n"
        "ds_read_b128 a[212:215], v254 offset:39936\n"
        "s_add_u32 s60, s68, s66\n"
        "s_addc_u32 s61, s69, 0\n"
        "v_mfma_f32_16x16x32_bf16 v[56:59], v[192:195], a[208:211], v[56:59]\n"
        "ds_read_b128 a[208:211], v254 offset:35840\n"
        "s_xor_b32 s66, s17, 2\n"
        "s_waitcnt lgkmcnt(8)\n"
        "v_mfma_f32_16x16x32_bf16 v[0:3], v[112:115], a[0:3], v[0:3]\n"
        "ds_read_b128 a[220:223], v254 offset:48128\n"
        "s_lshl_b32 s66, s66, 15\n"
        "v_add_f32 v82, v82, v138\n"
        "v_mfma_f32_16x16x32_bf16 v[0:3], v[120:123], a[4:7], v[0:3]\n"
        "global_store_dwordx4 v229, v[92:95], s[24:25] offset:1024 sc1\n"
        "v_add_f32 v83, v83, v139\n"
        "v_add_f32 v84, v84, v140\n"
        "v_add_f32 v85, v85, v141\n"
        "v_add_f32 v86, v86, v142\n"
        "v_mfma_f32_16x16x32_bf16 v[0:3], a[64:67], a[8:11], v[0:3]\n"
        "v_add_f32 v87, v87, v143\n"
        "v_cvt_pk_bf16_f32 v92, v80, v81\n"
        "v_mfma_f32_16x16x32_bf16 v[0:3], a[72:75], a[12:15], v[0:3]\n"
        "v_cvt_pk_bf16_f32 v93, v82, v83\n"
        "v_cvt_pk_bf16_f32 v94, v84, v85\n"
        "v_mfma_f32_16x16x32_bf16 v[4:7], a[76:79], a[12:15], v[4:7]\n"
        "v_cvt_pk_bf16_f32 v95, v86, v87\n"
        "v_mfma_f32_16x16x32_bf16 v[4:7], a[68:71], a[8:11], v[4:7]\n"
        "v_permlane16_swap_b32 v92, v94\n"
        "v_mfma_f32_16x16x32_bf16 v[4:7], v[124:127], a[4:7], v[4:7]\n"
        "v_permlane16_swap_b32 v93, v95\n"
        "v_mfma_f32_16x16x32_bf16 v[4:7], v[116:119], a[0:3], v[4:7]\n"
        "global_store_dwordx4 v229, v[88:91], s[24:25] offset:2048 sc1\n"
        "s_waitcnt lgkmcnt(0)\n"
        "v_mfma_f32_16x16x32_bf16 v[12:15], v[116:119], a[16:19], v[12:15]\n"
        "v_mfma_f32_16x16x32_bf16 v[12:15], v[124:127], a[20:23], v[12:15]\n"
        "v_mfma_f32_16x16x32_bf16 v[12:15], a[68:71], a[24:27], v[12:15]\n"
        "global_store_dwordx4 v229, v[92:95], s[24:25] offset:3072 sc1\n"
        "v_mfma_f32_16x16x32_bf16 v[12:15], a[76:79], a[28:31], v[12:15]\n"
        "s_lshl2_add_u32 s15, s75, s10\n"
        "s_mul_i32 s16, s13, 263168\n"
        "s_lshl_b32 s15, s15, 14\n"
        "v_mfma_f32_16x16x32_bf16 v[8:11], a[72:75], a[28:31], v[8:11]\n"
        "s_add_u32 s16, s16, s15\n"
        "s_add_u32 s16, s16, s73\n"
        "s_add_u32 s16, s16, 0x4000000\n"
        "v_mfma_f32_16x16x32_bf16 v[8:11], a[64:67], a[24:27], v[8:11]\n"
        "v_mfma_f32_16x16x32_bf16 v[8:11], v[120:123], a[20:23], v[8:11]\n"
        "v_mfma_f32_16x16x32_bf16 v[8:11], v[112:115], a[16:19], v[8:11]\n"
        "s_waitcnt vmcnt(32)\n"
        "v_mfma_f32_16x16x32_bf16 v[16:19], v[112:115], a[192:195], v[16:19]\n"
        "s_barrier\n"
        "v_mfma_f32_16x16x32_bf16 v[16:19], v[120:123], a[196:199], v[16:19]\n"
        "ds_read_b128 a[0:3], v254 offset:49152\n"
        "global_load_lds_dwordx4 v254, s[60:61] offset:0 sc1\n"
        "v_mfma_f32_16x16x32_bf16 v[16:19], a[64:67], a[200:203], v[16:19]\n"
        "ds_read_b128 a[4:7], v254 offset:53248\n"
        "v_mfma_f32_16x16x32_bf16 v[16:19], a[72:75], a[204:207], v[16:19]\n"
        "ds_read_b128 a[8:11], v254 offset:57344\n"
        "v_mfma_f32_16x16x32_bf16 v[20:23], a[76:79], a[204:207], v[20:23]\n"
        "ds_read_b128 a[12:15], v254 offset:61440\n"
        "v_mfma_f32_16x16x32_bf16 v[20:23], a[68:71], a[200:203], v[20:23]\n"
        "ds_read_b128 a[16:19], v254 offset:50176\n"
        "global_load_lds_dwordx4 v254, s[60:61] offset:1024 sc1\n"
        "v_mfma_f32_16x16x32_bf16 v[20:23], v[124:127], a[196:199], v[20:23]\n"
        "ds_read_b128 a[20:23], v254 offset:54272\n"
        "v_mfma_f32_16x16x32_bf16 v[20:23], v[116:119], a[192:195], v[20:23]\n"
        "ds_read_b128 a[24:27], v254 offset:58368\n"
        "v_mfma_f32_16x16x32_bf16 v[28:31], v[116:119], a[208:211], v[28:31]\n"
        "ds_read_b128 a[28:31], v254 offset:62464\n"
        "v_mfma_f32_16x16x32_bf16 v[28:31], v[124:127], a[212:215], v[28:31]\n"
        "ds_read_b128 a[192:195], v254 offset:51200\n"
        "global_load_lds_dwordx4 v254, s[60:61] offset:2048 sc1\n"
        "v_mfma_f32_16x16x32_bf16 v[28:31], a[68:71], a[216:219], v[28:31]\n"
        "ds_read_b128 a[196:199], v254 offset:55296\n"
        "v_mfma_f32_16x16x32_bf16 v[28:31], a[76:79], a[220:223], v[28:31]\n"
        "ds_read_b128 a[200:203], v254 offset:59392\n"
        "v_mfma_f32_16x16x32_bf16 v[24:27], a[72:75], a[220:223], v[24:27]\n"
        "ds_read_b128 a[204:207], v254 offset:63488\n"
        "s_waitcnt lgkmcnt(8)\n"
        "v_mfma_f32_16x16x32_bf16 v[24:27], a[64:67], a[216:219], v[24:27]\n"
        "ds_read_b128 a[216:219], v254 offset:60416\n"
        "global_load_lds_dwordx4 v254, s[60:61] offset:3072 sc1\n"
        "s_add_u32 m0, s73, 0x8000\n"
        "v_mfma_f32_16x16x32_bf16 v[24:27], v[120:123], a[212:215], v[24:27]\n"
        "ds_read_b128 a[212:215], v254 offset:56320\n"
        "s_add_u32 s60, s68, s66\n"
        "s_addc_u32 s61, s69, 0\n"
        "v_mfma_f32_16x16x32_bf16 v[24:27], v[112:115], a[208:211], v[24:27]\n"
        "ds_read_b128 a[208:211], v254 offset:52224\n"
        "s_xor_b32 s66, s17, 3\n"
        "s_lshl_b32 s66, s66, 15\n"
        "v_mfma_f32_16x16x32_bf16 v[0:3], a[80:83], a[0:3], v[0:3]\n"
        "ds_read_b128 a[220:223], v254 offset:64512\n"
        "v_mfma_f32_16x16x32_bf16 v[0:3], a[88:91], a[4:7], v[0:3]\n"
        "v_mfma_f32_16x16x32_bf16 v[0:3], a[160:163], a[8:11], v[0:3]\n"
        "v_mfma_f32_16x16x32_bf16 v[0:3], a[168:171], a[12:15], v[0:3]\n"
        "v_mfma_f32_16x16x32_bf16 v[4:7], a[172:175], a[12:15], v[4:7]\n"
        "s_waitcnt lgkmcnt(4)\n"
        "v_mfma_f32_16x16x32_bf16 v[4:7], a[164:167], a[8:11], v[4:7]\n"
        "v_mfma_f32_16x16x32_bf16 v[4:7], a[92:95], a[4:7], v[4:7]\n"
        "v_mfma_f32_16x16x32_bf16 v[4:7], a[84:87], a[0:3], v[4:7]\n"
        "s_and_b32 s67, s10, 1\n"
        "s_lshl_b32 s67, s67, 12\n"
        "v_mfma_f32_16x16x32_bf16 v[12:15], a[84:87], a[16:19], v[12:15]\n"
        "v_mfma_f32_16x16x32_bf16 v[12:15], a[92:95], a[20:23], v[12:15]\n"
        "v_mfma_f32_16x16x32_bf16 v[12:15], a[164:167], a[24:27], v[12:15]\n"
        "v_mfma_f32_16x16x32_bf16 v[12:15], a[172:175], a[28:31], v[12:15]\n"
        "v_mfma_f32_16x16x32_bf16 v[8:11], a[168:171], a[28:31], v[8:11]\n"
        "v_mfma_f32_16x16x32_bf16 v[8:11], a[160:163], a[24:27], v[8:11]\n"
        "v_mfma_f32_16x16x32_bf16 v[8:11], a[88:91], a[20:23], v[8:11]\n"
        "v_mfma_f32_16x16x32_bf16 v[8:11], a[80:83], a[16:19], v[8:11]\n"
        "s_waitcnt vmcnt(32)\n"
        "v_mfma_f32_16x16x32_bf16 v[16:19], a[80:83], a[192:195], v[16:19]\n"
        "s_barrier\n"
        "v_mfma_f32_16x16x32_bf16 v[16:19], a[88:91], a[196:199], v[16:19]\n"
        "ds_read_b128 a[0:3], v244 offset:32768\n"
        "global_load_lds_dwordx4 v254, s[60:61] offset:0 sc1\n"
        "v_mfma_f32_16x16x32_bf16 v[16:19], a[160:163], a[200:203], v[16:19]\n"
        "ds_read_b128 a[4:7], v244 offset:36864\n"
        "v_mfma_f32_16x16x32_bf16 v[16:19], a[168:171], a[204:207], v[16:19]\n"
        "ds_read_b128 a[8:11], v244 offset:40960\n"
        "v_mfma_f32_16x16x32_bf16 v[20:23], a[172:175], a[204:207], v[20:23]\n"
        "ds_read_b128 a[12:15], v244 offset:45056\n"
        "s_waitcnt lgkmcnt(4)\n"
        "v_mfma_f32_16x16x32_bf16 v[20:23], a[164:167], a[200:203], v[20:23]\n"
        "ds_read_b128 a[16:19], v244 offset:33792\n"
        "global_load_lds_dwordx4 v254, s[60:61] offset:1024 sc1\n"
        "v_mfma_f32_16x16x32_bf16 v[20:23], a[92:95], a[196:199], v[20:23]\n"
        "ds_read_b128 a[20:23], v244 offset:37888\n"
        "v_mfma_f32_16x16x32_bf16 v[20:23], a[84:87], a[192:195], v[20:23]\n"
        "ds_read_b128 a[24:27], v244 offset:41984\n"
        "v_mfma_f32_16x16x32_bf16 v[28:31], a[84:87], a[208:211], v[28:31]\n"
        "ds_read_b128 a[28:31], v244 offset:46080\n"
        "v_mfma_f32_16x16x32_bf16 v[28:31], a[92:95], a[212:215], v[28:31]\n"
        "ds_read_b128 a[192:195], v244 offset:34816\n"
        "global_load_lds_dwordx4 v254, s[60:61] offset:2048 sc1\n"
        "v_mfma_f32_16x16x32_bf16 v[28:31], a[164:167], a[216:219], v[28:31]\n"
        "ds_read_b128 a[196:199], v244 offset:38912\n"
        "v_mfma_f32_16x16x32_bf16 v[28:31], a[172:175], a[220:223], v[28:31]\n"
        "ds_read_b128 a[200:203], v244 offset:43008\n"
        "v_mfma_f32_16x16x32_bf16 v[24:27], a[168:171], a[220:223], v[24:27]\n"
        "ds_read_b128 a[204:207], v244 offset:47104\n"
        "s_waitcnt lgkmcnt(8)\n"
        "v_mfma_f32_16x16x32_bf16 v[24:27], a[160:163], a[216:219], v[24:27]\n"
        "ds_read_b128 a[216:219], v244 offset:44032\n"
        "global_load_lds_dwordx4 v254, s[60:61] offset:3072 sc1\n"
        "s_add_u32 m0, s73, 0xc000\n"
        "v_mfma_f32_16x16x32_bf16 v[24:27], a[88:91], a[212:215], v[24:27]\n"
        "ds_read_b128 a[212:215], v244 offset:39936\n"
        "s_add_u32 s60, s68, s66\n"
        "s_addc_u32 s61, s69, 0\n"
        "v_mfma_f32_16x16x32_bf16 v[24:27], a[80:83], a[208:211], v[24:27]\n"
        "ds_read_b128 a[208:211], v244 offset:35840\n"
        "s_xor_b32 s66, s17, 4\n"
        "s_lshl_b32 s66, s66, 15\n"
        "v_mfma_f32_16x16x32_bf16 v[0:3], a[176:179], a[0:3], v[0:3]\n"
        "ds_read_b128 a[220:223], v244 offset:48128\n"
        "v_mfma_f32_16x16x32_bf16 v[0:3], a[184:187], a[4:7], v[0:3]\n"
        "v_mfma_f32_16x16x32_bf16 v[0:3], a[32:35], a[8:11], v[0:3]\n"
        "v_mfma_f32_16x16x32_bf16 v[0:3], a[40:43], a[12:15], v[0:3]\n"
        "v_mfma_f32_16x16x32_bf16 v[4:7], a[44:47], a[12:15], v[4:7]\n"
        "s_waitcnt lgkmcnt(4)\n"
        "v_mfma_f32_16x16x32_bf16 v[4:7], a[36:39], a[8:11], v[4:7]\n"
        "v_mfma_f32_16x16x32_bf16 v[4:7], a[188:191], a[4:7], v[4:7]\n"
        "v_mfma_f32_16x16x32_bf16 v[4:7], a[180:183], a[0:3], v[4:7]\n"
        "v_mfma_f32_16x16x32_bf16 v[12:15], a[180:183], a[16:19], v[12:15]\n"
        "v_mfma_f32_16x16x32_bf16 v[12:15], a[188:191], a[20:23], v[12:15]\n"
        "v_mfma_f32_16x16x32_bf16 v[12:15], a[36:39], a[24:27], v[12:15]\n"
        "v_mfma_f32_16x16x32_bf16 v[12:15], a[44:47], a[28:31], v[12:15]\n"
        "v_mfma_f32_16x16x32_bf16 v[8:11], a[40:43], a[28:31], v[8:11]\n"
        "v_mfma_f32_16x16x32_bf16 v[8:11], a[32:35], a[24:27], v[8:11]\n"
        "v_mfma_f32_16x16x32_bf16 v[8:11], a[184:187], a[20:23], v[8:11]\n"
        "v_mfma_f32_16x16x32_bf16 v[8:11], a[176:179], a[16:19], v[8:11]\n"
        "s_waitcnt vmcnt(32)\n"
        "v_mfma_f32_16x16x32_bf16 v[16:19], a[176:179], a[192:195], v[16:19]\n"
        "s_barrier\n"
        "v_mfma_f32_16x16x32_bf16 v[16:19], a[184:187], a[196:199], v[16:19]\n"
        "ds_read_b128 a[0:3], v244 offset:0\n"
        "global_load_lds_dwordx4 v254, s[60:61] offset:0 sc1\n"
        "v_mfma_f32_16x16x32_bf16 v[16:19], a[32:35], a[200:203], v[16:19]\n"
        "ds_read_b128 a[4:7], v244 offset:4096\n"
        "v_mfma_f32_16x16x32_bf16 v[16:19], a[40:43], a[204:207], v[16:19]\n"
        "ds_read_b128 a[8:11], v244 offset:8192\n"
        "v_mfma_f32_16x16x32_bf16 v[20:23], a[44:47], a[204:207], v[20:23]\n"
        "ds_read_b128 a[12:15], v244 offset:12288\n"
        "v_mfma_f32_16x16x32_bf16 v[20:23], a[36:39], a[200:203], v[20:23]\n"
        "ds_read_b128 a[28:31], v244 offset:13312\n"
        "global_load_lds_dwordx4 v254, s[60:61] offset:1024 sc1\n"
        "v_mfma_f32_16x16x32_bf16 v[20:23], a[188:191], a[196:199], v[20:23]\n"
        "ds_read_b128 a[24:27], v244 offset:9216\n"
        "v_mfma_f32_16x16x32_bf16 v[20:23], a[180:183], a[192:195], v[20:23]\n"
        "ds_read_b128 a[20:23], v244 offset:5120\n"
        "s_waitcnt lgkmcnt(7)\n"
        "v_mfma_f32_16x16x32_bf16 v[28:31], a[180:183], a[208:211], v[28:31]\n"
        "ds_read_b128 a[16:19], v244 offset:1024\n"
        "v_mfma_f32_16x16x32_bf16 v[28:31], a[188:191], a[212:215], v[28:31]\n"
        "ds_read_b128 a[192:195], v244 offset:2048\n"
        "s_waitcnt lgkmcnt(8)\n"
        "v_mfma_f32_16x16x32_bf16 v[0:3], a[48:51], a[0:3], v[0:3]\n"
        "ds_read_b128 a[196:199], v244 offset:6144\n"
        "s_waitcnt lgkmcnt(8)\n"
        "v_mfma_f32_16x16x32_bf16 v[0:3], a[56:59], a[4:7], v[0:3]\n"
        "ds_read_b128 a[200:203], v244 offset:10240\n"
        "s_waitcnt lgkmcnt(8)\n"
        "v_mfma_f32_16x16x32_bf16 v[0:3], a[224:227], a[8:11], v[0:3]\n"
        "ds_read_b128 a[204:207], v244 offset:14336\n"
        "s_waitcnt lgkmcnt(8)\n"
        "v_mfma_f32_16x16x32_bf16 v[0:3], a[232:235], a[12:15], v[0:3]\n"
        "global_load_lds_dwordx4 v254, s[60:61] offset:2048 sc1\n"
        "v_mfma_f32_16x16x32_bf16 v[4:7], a[236:239], a[12:15], v[4:7]\n"
        "v_mfma_f32_16x16x32_bf16 v[4:7], a[228:231], a[8:11], v[4:7]\n"
        "v_mfma_f32_16x16x32_bf16 v[4:7], a[60:63], a[4:7], v[4:7]\n"
        "global_load_lds_dwordx4 v254, s[60:61] offset:3072 sc1\n"
        "s_add_u32 m0, s73, 0x18000\n"
        "v_mfma_f32_16x16x32_bf16 v[4:7], a[52:55], a[0:3], v[4:7]\n"
        "s_add_u32 s60, s68, s66\n"
        "s_addc_u32 s61, s69, 0\n"
        "v_cvt_pk_bf16_f32 v0, v0, v1\n"
        "v_mfma_f32_16x16x32_bf16 v[28:31], a[36:39], a[216:219], v[28:31]\n"
        "s_waitcnt lgkmcnt(2)\n"
        "v_cvt_pk_bf16_f32 v1, v2, v3\n"
        "s_xor_b32 s66, s17, 5\n"
        "v_mfma_f32_16x16x32_bf16 v[28:31], a[44:47], a[220:223], v[28:31]\n"
        "s_lshl_b32 s66, s66, 15\n"
        "v_cvt_pk_bf16_f32 v2, v4, v5\n"
        "v_cvt_pk_bf16_f32 v3, v6, v7\n"
        "v_mfma_f32_16x16x32_bf16 v[8:11], a[232:235], a[28:31], v[8:11]\n"
        "global_store_dwordx4 v254, v[0:3], s[28:29] offset:0 sc1\n"
        "v_mfma_f32_16x16x32_bf16 v[8:11], a[224:227], a[24:27], v[8:11]\n"
        "v_mfma_f32_16x16x32_bf16 v[8:11], a[56:59], a[20:23], v[8:11]\n"
        "v_mfma_f32_16x16x32_bf16 v[8:11], a[48:51], a[16:19], v[8:11]\n"
        "s_waitcnt vmcnt(13)\n"
        "v_mfma_f32_16x16x32_bf16 v[12:15], a[52:55], a[16:19], v[12:15]\n"
        "s_barrier\n"
        "v_mfma_f32_16x16x32_bf16 v[12:15], a[60:63], a[20:23], v[12:15]\n"
        "ds_read_b128 a[0:3], v244 offset:49152\n"
        "global_load_lds_dwordx4 v254, s[60:61] offset:0 sc1\n"
        "v_mfma_f32_16x16x32_bf16 v[12:15], a[228:231], a[24:27], v[12:15]\n"
        "ds_read_b128 a[4:7], v244 offset:53248\n"
        "v_cvt_pk_bf16_f32 v8, v8, v9\n"
        "v_mfma_f32_16x16x32_bf16 v[12:15], a[236:239], a[28:31], v[12:15]\n"
        "ds_read_b128 a[8:11], v244 offset:57344\n"
        "v_cvt_pk_bf16_f32 v9, v10, v11\n"
        "v_mfma_f32_16x16x32_bf16 v[24:27], a[40:43], a[220:223], v[24:27]\n"
        "ds_read_b128 a[220:223], v244 offset:15360\n"
        "v_mfma_f32_16x16x32_bf16 v[24:27], a[32:35], a[216:219], v[24:27]\n"
        "ds_read_b128 a[216:219], v244 offset:11264\n"
        "global_load_lds_dwordx4 v254, s[60:61] offset:1024 sc1\n"
        "v_mfma_f32_16x16x32_bf16 v[24:27], a[184:187], a[212:215], v[24:27]\n"
        "ds_read_b128 a[212:215], v244 offset:7168\n"
        "v_cvt_pk_bf16_f32 v10, v12, v13\n"
        "v_mfma_f32_16x16x32_bf16 v[24:27], a[176:179], a[208:211], v[24:27]\n"
        "ds_read_b128 a[208:211], v244 offset:3072\n"
        "v_cvt_pk_bf16_f32 v11, v14, v15\n"
        "v_mfma_f32_16x16x32_bf16 v[16:19], a[48:51], a[192:195], v[16:19]\n"
        "ds_read_b128 a[12:15], v244 offset:61440\n"
        "s_waitcnt lgkmcnt(8)\n"
        "v_mfma_f32_16x16x32_bf16 v[16:19], a[56:59], a[196:199], v[16:19]\n"
        "global_store_dwordx4 v254, v[8:11], s[28:29] offset:1024 sc1\n"
        "ds_read_b128 a[16:19], v244 offset:50176\n"
        "ds_read_b128 a[20:23], v244 offset:54272\n"
        "v_mfma_f32_16x16x32_bf16 v[16:19], a[224:227], a[200:203], v[16:19]\n"
        "ds_read_b128 a[24:27], v244 offset:58368\n"
        "v_mfma_f32_16x16x32_bf16 v[16:19], a[232:235], a[204:207], v[16:19]\n"
        "ds_read_b128 a[28:31], v244 offset:62464\n"
        "v_mfma_f32_16x16x32_bf16 v[20:23], a[236:239], a[204:207], v[20:23]\n"
        "ds_read_b128 a[204:207], v244 offset:63488\n"
        "global_load_lds_dwordx4 v254, s[60:61] offset:2048 sc1\n"
        "v_mfma_f32_16x16x32_bf16 v[20:23], a[228:231], a[200:203], v[20:23]\n"
        "ds_read_b128 a[200:203], v244 offset:59392\n"
        "s_waitcnt lgkmcnt(8)\n"
        "v_mfma_f32_16x16x32_bf16 v[20:23], a[60:63], a[196:199], v[20:23]\n"
        "ds_read_b128 a[196:199], v244 offset:55296\n"
        "v_cvt_pk_bf16_f32 v16, v16, v17\n"
        "v_mfma_f32_16x16x32_bf16 v[20:23], a[52:55], a[192:195], v[20:23]\n"
        "ds_read_b128 a[192:195], v244 offset:51200\n"
        "v_cvt_pk_bf16_f32 v17, v18, v19\n"
        "v_mfma_f32_16x16x32_bf16 v[24:27], a[232:235], a[220:223], v[24:27]\n"
        "global_load_lds_dwordx4 v254, s[60:61] offset:3072 sc1\n"
        "s_add_u32 m0, s73, 0x10000\n"
        "s_waitcnt lgkmcnt(7)\n"
        "v_mfma_f32_16x16x32_bf16 v[24:27], a[224:227], a[216:219], v[24:27]\n"
        "s_add_u32 s60, s68, s66\n"
        "s_addc_u32 s61, s69, 0\n"
        "v_cvt_pk_bf16_f32 v18, v20, v21\n"
        "v_mfma_f32_16x16x32_bf16 v[24:27], a[56:59], a[212:215], v[24:27]\n"
        "s_xor_b32 s66, s17, 6\n"
        "v_cvt_pk_bf16_f32 v19, v22, v23\n"
        "s_lshl_b32 s66, s66, 15\n"
        "v_mfma_f32_16x16x32_bf16 v[24:27], a[48:51], a[208:211], v[24:27]\n"
        "v_mfma_f32_16x16x32_bf16 v[28:31], a[52:55], a[208:211], v[28:31]\n"
        "global_store_dwordx4 v254, v[16:19], s[28:29] offset:2048 sc1\n"
        "ds_read_b128 a[208:211], v244 offset:52224\n"
        "v_lshlrev_b32 v159, 2, v232\n"
        "v_add_u32 v159, 0x0, v159\n"
        "v_mfma_f32_16x16x32_bf16 v[28:31], a[60:63], a[212:215], v[28:31]\n"
        "ds_read_b128 a[212:215], v244 offset:56320\n"
        "v_mfma_f32_16x16x32_bf16 v[28:31], a[228:231], a[216:219], v[28:31]\n"
        "ds_read_b128 a[216:219], v244 offset:60416\n"
        "v_cvt_pk_bf16_f32 v24, v24, v25\n"
        "v_mfma_f32_16x16x32_bf16 v[28:31], a[236:239], a[220:223], v[28:31]\n"
        "ds_read_b128 a[220:223], v244 offset:64512\n"
        "v_cvt_pk_bf16_f32 v25, v26, v27\n"
        "v_mfma_f32_16x16x32_bf16 v[32:35], v[112:115], a[0:3], v[32:35]\n"
        "s_cmp_lg_u32 s40, 0\n"
        "s_cbranch_scc1 LW2_%=\n"
        "s_mov_b64 exec, 0xffff\n"
        "global_store_dwordx2 v234, v[250:251], s[48:49] sc1\n"
        "s_mov_b64 exec, -1\n"
        "LW2_%=:\n"
        "v_mfma_f32_16x16x32_bf16 v[32:35], v[120:123], a[4:7], v[32:35]\n"
        "v_bfe_u32 v234, v254, 4, 3\n"
        "v_lshl_add_u32 v234, v234, 7, s14\n"
        "v_mfma_f32_16x16x32_bf16 v[32:35], a[64:67], a[8:11], v[32:35]\n"
        "s_lshl_b32 s14, s40, 6\n"
        "v_cvt_pk_bf16_f32 v26, v28, v29\n"
        "v_cvt_pk_bf16_f32 v27, v30, v31\n"
        "v_mfma_f32_16x16x32_bf16 v[32:35], a[72:75], a[12:15], v[32:35]\n"
        "global_store_dwordx4 v254, v[24:27], s[28:29] offset:3072 sc1\n"
        "s_add_u32 s28, s38, s16\n"
        "s_addc_u32 s29, s39, 0\n"
        "v_add_u32 v225, s14, v159\n"
        "v_mfma_f32_16x16x32_bf16 v[36:39], a[76:79], a[12:15], v[36:39]\n"
        "v_mfma_f32_16x16x32_bf16 v[36:39], a[68:71], a[8:11], v[36:39]\n"
        "s_waitcnt lgkmcnt(1)\n"
        "v_mfma_f32_16x16x32_bf16 v[36:39], v[124:127], a[4:7], v[36:39]\n"
        "v_mfma_f32_16x16x32_bf16 v[36:39], v[116:119], a[0:3], v[36:39]\n"
        "s_lshl_b32 s16, s63, 12\n"
        "v_and_b32 v150, 1, v233\n"
        "v_lshlrev_b32 v151, 11, v232\n"
        "v_mfma_f32_16x16x32_bf16 v[44:47], v[116:119], a[16:19], v[44:47]\n"
        "s_lshl1_add_u32 s16, s62, s16\n"
        "v_lshl_add_u32 v151, v150, 5, v151\n"
        "v_lshrrev_b32 v150, 1, v233\n"
        "v_mfma_f32_16x16x32_bf16 v[44:47], v[124:127], a[20:23], v[44:47]\n"
        "s_add_u32 s16, s16, 0x3700000\n"
        "v_lshl_add_u32 v151, v150, 4, v151\n"
        "v_lshrrev_b32 v148, 7, v254\n"
        "v_mfma_f32_16x16x32_bf16 v[44:47], a[68:71], a[24:27], v[44:47]\n"
        "s_add_u32 s58, s38, s16\n"
        "s_addc_u32 s59, s39, 0\n"
        "v_lshlrev_b32 v148, 12, v148\n"
        "v_mfma_f32_16x16x32_bf16 v[44:47], a[76:79], a[28:31], v[44:47]\n"
        "s_add_u32 s16, s73, 0x4000\n"
        "v_and_b32 v150, 0x70, v254\n"
        "v_add_u32 v148, v148, v150\n"
        "v_mfma_f32_16x16x32_bf16 v[40:43], a[72:75], a[28:31], v[40:43]\n"
        "v_lshrrev_b32 v149, 6, v254\n"
        "v_lshlrev_b32 v149, 7, v149\n"
        "v_mfma_f32_16x16x32_bf16 v[40:43], a[64:67], a[24:27], v[40:43]\n"
        "v_bfe_u32 v150, v254, 4, 2\n"
        "v_lshl_add_u32 v149, v150, 3, v149\n"
        "v_mfma_f32_16x16x32_bf16 v[40:43], v[120:123], a[20:23], v[40:43]\n"
        "v_mov_b32 v238, 0\n"
        "v_mov_b32 v239, 0\n"
        "v_mfma_f32_16x16x32_bf16 v[40:43], v[112:115], a[16:19], v[40:43]\n"
        "v_mov_b32 v242, 0\n"
        "v_mov_b32 v243, 0\n"
        "v_mfma_f32_16x16x32_bf16 v[48:51], v[112:115], a[192:195], v[48:51]\n"
        "s_barrier\n"
        "v_mfma_f32_16x16x32_bf16 v[48:51], v[120:123], a[196:199], v[48:51]\n"
        "ds_read_b128 a[0:3], v245 offset:0\n"
        "global_load_lds_dwordx4 v254, s[60:61] offset:0 sc1\n"
        "v_mfma_f32_16x16x32_bf16 v[48:51], a[64:67], a[200:203], v[48:51]\n"
        "ds_read_b128 a[4:7], v245 offset:4096\n"
        "v_and_b32 v246, 1, v232\n"
        "v_mfma_f32_16x16x32_bf16 v[48:51], a[72:75], a[204:207], v[48:51]\n"
        "ds_read_b128 a[8:11], v245 offset:8192\n"
        "v_lshlrev_b32 v246, 4, v246\n"
        "v_mfma_f32_16x16x32_bf16 v[52:55], a[76:79], a[204:207], v[52:55]\n"
        "ds_read_b128 a[12:15], v245 offset:12288\n"
        "v_mov_b32 v247, 0x3f80\n"
        "v_mfma_f32_16x16x32_bf16 v[52:55], a[68:71], a[200:203], v[52:55]\n"
        "ds_read_b128 a[16:19], v245 offset:1024\n"
        "global_load_lds_dwordx4 v254, s[60:61] offset:1024 sc1\n"
        "v_mfma_f32_16x16x32_bf16 v[52:55], v[124:127], a[196:199], v[52:55]\n"
        "ds_read_b128 a[20:23], v245 offset:5120\n"
        "v_lshlrev_b32 v246, v246, v247\n"
        "v_mfma_f32_16x16x32_bf16 v[52:55], v[116:119], a[192:195], v[52:55]\n"
        "ds_read_b128 a[24:27], v245 offset:9216\n"
        "v_lshrrev_b32 v247, 1, v232\n"
        "v_mfma_f32_16x16x32_bf16 v[60:63], v[116:119], a[208:211], v[60:63]\n"
        "ds_read_b128 a[28:31], v245 offset:13312\n"
        "v_lshlrev_b32 v248, 1, v233\n"
        "v_mfma_f32_16x16x32_bf16 v[60:63], v[124:127], a[212:215], v[60:63]\n"
        "ds_read_b128 a[192:195], v245 offset:2048\n"
        "global_load_lds_dwordx4 v254, s[60:61] offset:2048 sc1\n"
        "v_mfma_f32_16x16x32_bf16 v[60:63], a[68:71], a[216:219], v[60:63]\n"
        "ds_read_b128 a[196:199], v245 offset:6144\n"
        "s_waitcnt lgkmcnt(8)\n"
        "v_mfma_f32_16x16x32_bf16 v[60:63], a[76:79], a[220:223], v[60:63]\n"
        "ds_read_b128 a[200:203], v245 offset:10240\n"
        "v_or_b32 v249, 1, v248\n"
        "v_mfma_f32_16x16x32_bf16 v[56:59], a[72:75], a[220:223], v[56:59]\n"
        "ds_read_b128 a[204:207], v245 offset:14336\n"
        "v_cmp_eq_u32 s[14:15], v247, v248\n"
        "v_mfma_f32_16x16x32_bf16 v[56:59], a[64:67], a[216:219], v[56:59]\n"
        "ds_read_b128 a[216:219], v245 offset:11264\n"
        "global_load_lds_dwordx4 v254, s[60:61] offset:3072 sc1\n"
        "s_add_u32 m0, s73, 0x1c000\n"
        "v_mfma_f32_16x16x32_bf16 v[56:59], v[120:123], a[212:215], v[56:59]\n"
        "ds_read_b128 a[212:215], v245 offset:7168\n"
        "s_add_u32 s60, s68, s66\n"
        "s_addc_u32 s61, s69, 0\n"
        "v_mfma_f32_16x16x32_bf16 v[56:59], v[112:115], a[208:211], v[56:59]\n"
        "ds_read_b128 a[208:211], v245 offset:3072\n"
        "s_xor_b32 s66, s17, 7\n"
        "s_lshl_b32 s66, s66, 15\n"
        "v_mfma_f32_16x16x32_bf16 v[32:35], a[80:83], a[0:3], v[32:35]\n"
        "ds_read_b128 a[220:223], v245 offset:15360\n"
        "v_cndmask_b32 v240, 0, v246, s[14:15]\n"
        "v_mfma_f32_16x16x32_bf16 v[32:35], a[88:91], a[4:7], v[32:35]\n"
        "s_waitcnt lgkmcnt(7)\n"
        "v_cmp_eq_u32 s[14:15], v247, v249\n"
        "v_add_u32 v149, s16, v149\n"
        "v_mfma_f32_16x16x32_bf16 v[32:35], a[160:163], a[8:11], v[32:35]\n"
        "v_cndmask_b32 v241, 0, v246, s[14:15]\n"
        "v_mfma_f32_16x16x32_bf16 v[32:35], a[168:171], a[12:15], v[32:35]\n"
        "v_mfma_f32_16x16x32_bf16 v[36:39], a[172:175], a[12:15], v[36:39]\n"
        "v_mfma_f32_16x16x32_bf16 v[36:39], a[164:167], a[8:11], v[36:39]\n"
        "v_mfma_f32_16x16x32_bf16 v[36:39], a[92:95], a[4:7], v[36:39]\n"
        "v_mfma_f32_16x16x32_bf16 v[36:39], a[84:87], a[0:3], v[36:39]\n"
        "v_mfma_f32_16x16x32_bf16 v[44:47], a[84:87], a[16:19], v[44:47]\n"
        "v_mfma_f32_16x16x32_bf16 v[44:47], a[92:95], a[20:23], v[44:47]\n"
        "v_mfma_f32_16x16x32_bf16 v[44:47], a[164:167], a[24:27], v[44:47]\n"
        "v_mfma_f32_16x16x32_bf16 v[44:47], a[172:175], a[28:31], v[44:47]\n"
        "v_mfma_f32_16x16x32_bf16 v[40:43], a[168:171], a[28:31], v[40:43]\n"
        "v_mfma_f32_16x16x32_bf16 v[40:43], a[160:163], a[24:27], v[40:43]\n"
        "s_waitcnt lgkmcnt(0)\n"
        "v_mfma_f32_16x16x32_bf16 v[40:43], a[88:91], a[20:23], v[40:43]\n"
        "v_mfma_f32_16x16x32_bf16 v[40:43], a[80:83], a[16:19], v[40:43]\n"
        "v_mfma_f32_16x16x32_bf16 v[48:51], a[80:83], a[192:195], v[48:51]\n"
        "s_barrier\n"
        "v_mfma_f32_16x16x32_bf16 v[48:51], a[88:91], a[196:199], v[48:51]\n"
        "ds_read_b128 a[0:3], v245 offset:16384\n"
        "global_load_lds_dwordx4 v254, s[60:61] offset:0 sc1\n"
        "v_mfma_f32_16x16x32_bf16 v[48:51], a[160:163], a[200:203], v[48:51]\n"
        "ds_read_b128 a[4:7], v245 offset:20480\n"
        "v_mfma_f32_16x16x32_bf16 v[48:51], a[168:171], a[204:207], v[48:51]\n"
        "ds_read_b128 a[8:11], v245 offset:24576\n"
        "v_mfma_f32_16x16x32_bf16 v[52:55], a[172:175], a[204:207], v[52:55]\n"
        "ds_read_b128 a[12:15], v245 offset:28672\n"
        "v_mfma_f32_16x16x32_bf16 v[52:55], a[164:167], a[200:203], v[52:55]\n"
        "ds_read_b128 a[16:19], v245 offset:17408\n"
        "global_load_lds_dwordx4 v254, s[60:61] offset:1024 sc1\n"
        "v_mfma_f32_16x16x32_bf16 v[52:55], a[92:95], a[196:199], v[52:55]\n"
        "LHB1_%=:\n"
        "s_cbranch_execz LHRB_%=\n"
        "ds_read_b128 a[20:23], v245 offset:21504\n"
        "v_mfma_f32_16x16x32_bf16 v[52:55], a[84:87], a[192:195], v[52:55]\n"
        "ds_read_b128 a[24:27], v245 offset:25600\n"
        "v_mfma_f32_16x16x32_bf16 v[60:63], a[84:87], a[208:211], v[60:63]\n"
        "ds_read_b128 a[28:31], v245 offset:29696\n"
        "v_mfma_f32_16x16x32_bf16 v[60:63], a[92:95], a[212:215], v[60:63]\n"
        "ds_read_b128 a[192:195], v245 offset:18432\n"
        "global_load_lds_dwordx4 v254, s[60:61] offset:2048 sc1\n"
        "v_mfma_f32_16x16x32_bf16 v[60:63], a[164:167], a[216:219], v[60:63]\n"
        "ds_read_b128 a[196:199], v245 offset:22528\n"
        "v_mfma_f32_16x16x32_bf16 v[60:63], a[172:175], a[220:223], v[60:63]\n"
        "ds_read_b128 a[200:203], v245 offset:26624\n"
        "v_mfma_f32_16x16x32_bf16 v[56:59], a[168:171], a[220:223], v[56:59]\n"
        "ds_read_b128 a[204:207], v245 offset:30720\n"
        "s_waitcnt lgkmcnt(8)\n"
        "v_mfma_f32_16x16x32_bf16 v[56:59], a[160:163], a[216:219], v[56:59]\n"
        "ds_read_b128 a[216:219], v245 offset:27648\n"
        "global_load_lds_dwordx4 v254, s[60:61] offset:3072 sc1\n"
        "s_add_u32 m0, s73, 0x20000\n"
        "v_mfma_f32_16x16x32_bf16 v[56:59], a[88:91], a[212:215], v[56:59]\n"
        "ds_read_b128 a[212:215], v245 offset:23552\n"
        "s_add_u32 s60, s68, s66\n"
        "s_addc_u32 s61, s69, 0\n"
        "v_mfma_f32_16x16x32_bf16 v[56:59], a[80:83], a[208:211], v[56:59]\n"
        "ds_read_b128 a[208:211], v245 offset:19456\n"
        "s_sub_u32 s66, 15, s12\n"
        "v_mfma_f32_16x16x32_bf16 v[32:35], a[176:179], a[0:3], v[32:35]\n"
        "ds_read_b128 a[220:223], v245 offset:31744\n"
        "s_cmp_lt_u32 s10, 2\n"
        "s_cselect_b32 s66, s12, s66\n"
        "v_mfma_f32_16x16x32_bf16 v[32:35], a[184:187], a[4:7], v[32:35]\n"
        "s_mul_i32 s66, s66, 1052672\n"
        "s_add_u32 s66, s66, s67\n"
        "v_mfma_f32_16x16x32_bf16 v[32:35], a[32:35], a[8:11], v[32:35]\n"
        "s_add_u32 s68, s20, s66\n"
        "s_addc_u32 s69, s21, 0\n"
        "s_xor_b32 s66, s17, 0\n"
        "v_mfma_f32_16x16x32_bf16 v[32:35], a[40:43], a[12:15], v[32:35]\n"
        "s_lshl_b32 s66, s66, 15\n"
        "v_mfma_f32_16x16x32_bf16 v[36:39], a[44:47], a[12:15], v[36:39]\n"
        "s_waitcnt lgkmcnt(4)\n"
        "v_mfma_f32_16x16x32_bf16 v[36:39], a[36:39], a[8:11], v[36:39]\n"
        "v_mfma_f32_16x16x32_bf16 v[36:39], a[188:191], a[4:7], v[36:39]\n"
        "v_mfma_f32_16x16x32_bf16 v[36:39], a[180:183], a[0:3], v[36:39]\n"
        "v_mfma_f32_16x16x32_bf16 v[44:47], a[180:183], a[16:19], v[44:47]\n"
        "v_mfma_f32_16x16x32_bf16 v[44:47], a[188:191], a[20:23], v[44:47]\n"
        "v_mfma_f32_16x16x32_bf16 v[44:47], a[36:39], a[24:27], v[44:47]\n"
        "v_mfma_f32_16x16x32_bf16 v[44:47], a[44:47], a[28:31], v[44:47]\n"
        "v_mfma_f32_16x16x32_bf16 v[40:43], a[40:43], a[28:31], v[40:43]\n"
        "v_mfma_f32_16x16x32_bf16 v[40:43], a[32:35], a[24:27], v[40:43]\n"
        "v_mfma_f32_16x16x32_bf16 v[40:43], a[184:187], a[20:23], v[40:43]\n"
        "v_mfma_f32_16x16x32_bf16 v[40:43], a[176:179], a[16:19], v[40:43]\n"
        "v_mfma_f32_16x16x32_bf16 v[48:51], a[176:179], a[192:195], v[48:51]\n"
        "s_barrier\n"
        "v_mfma_f32_16x16x32_bf16 v[48:51], a[184:187], a[196:199], v[48:51]\n"
        "ds_read_b128 a[0:3], v244 offset:16384\n"
        "global_load_lds_dwordx4 v254, s[60:61] offset:0 sc1\n"
        "v_mfma_f32_16x16x32_bf16 v[48:51], a[32:35], a[200:203], v[48:51]\n"
        "ds_read_b128 a[4:7], v244 offset:20480\n"
        "v_mfma_f32_16x16x32_bf16 v[48:51], a[40:43], a[204:207], v[48:51]\n"
        "ds_read_b128 a[8:11], v244 offset:24576\n"
        "v_mfma_f32_16x16x32_bf16 v[52:55], a[44:47], a[204:207], v[52:55]\n"
        "ds_read_b128 a[12:15], v244 offset:28672\n"
        "v_mfma_f32_16x16x32_bf16 v[52:55], a[36:39], a[200:203], v[52:55]\n"
        "ds_read_b128 a[28:31], v244 offset:29696\n"
        "global_load_lds_dwordx4 v254, s[60:61] offset:1024 sc1\n"
        "v_mfma_f32_16x16x32_bf16 v[52:55], a[188:191], a[196:199], v[52:55]\n"
        "ds_read_b128 a[24:27], v244 offset:25600\n"
        "v_mfma_f32_16x16x32_bf16 v[52:55], a[180:183], a[192:195], v[52:55]\n"
        "ds_read_b128 a[20:23], v244 offset:21504\n"
        "s_waitcnt lgkmcnt(7)\n"
        "v_mfma_f32_16x16x32_bf16 v[60:63], a[180:183], a[208:211], v[60:63]\n"
        "ds_read_b128 a[16:19], v244 offset:17408\n"
        "v_mfma_f32_16x16x32_bf16 v[60:63], a[188:191], a[212:215], v[60:63]\n"
        "ds_read_b128 a[192:195], v244 offset:18432\n"
        "s_waitcnt lgkmcnt(8)\n"
        "v_mfma_f32_16x16x32_bf16 v[32:35], a[48:51], a[0:3], v[32:35]\n"
        "ds_read_b128 a[196:199], v244 offset:22528\n"
        "s_waitcnt lgkmcnt(8)\n"
        "v_mfma_f32_16x16x32_bf16 v[32:35], a[56:59], a[4:7], v[32:35]\n"
        "ds_read_b128 a[200:203], v244 offset:26624\n"
        "s_waitcnt lgkmcnt(8)\n"
        "v_mfma_f32_16x16x32_bf16 v[32:35], a[224:227], a[8:11], v[32:35]\n"
        "ds_read_b128 a[204:207], v244 offset:30720\n"
        "s_waitcnt lgkmcnt(8)\n"
        "v_mfma_f32_16x16x32_bf16 v[32:35], a[232:235], a[12:15], v[32:35]\n"
        "global_load_lds_dwordx4 v254, s[60:61] offset:2048 sc1\n"
        "v_mfma_f32_16x16x32_bf16 v[36:39], a[236:239], a[12:15], v[36:39]\n"
        "v_mfma_f32_16x16x32_bf16 v[36:39], a[228:231], a[8:11], v[36:39]\n"
        "v_mfma_f32_16x16x32_bf16 v[36:39], a[60:63], a[4:7], v[36:39]\n"
        "global_load_lds_dwordx4 v254, s[60:61] offset:3072 sc1\n"
        "s_add_u32 m0, s73, 0x24000\n"
        "v_mfma_f32_16x16x32_bf16 v[36:39], a[52:55], a[0:3], v[36:39]\n"
        "s_add_u32 s60, s68, s66\n"
        "s_addc_u32 s61, s69, 0\n"
        "v_cvt_pk_bf16_f32 v32, v32, v33\n"
        "v_mfma_f32_16x16x32_bf16 v[60:63], a[36:39], a[216:219], v[60:63]\n"
        "s_waitcnt lgkmcnt(2)\n"
        "v_cvt_pk_bf16_f32 v33, v34, v35\n"
        "s_xor_b32 s66, s17, 1\n"
        "v_mfma_f32_16x16x32_bf16 v[60:63], a[44:47], a[220:223], v[60:63]\n"
        "s_lshl_b32 s66, s66, 15\n"
        "v_cvt_pk_bf16_f32 v34, v36, v37\n"
        "v_cvt_pk_bf16_f32 v35, v38, v39\n"
        "v_mfma_f32_16x16x32_bf16 v[40:43], a[232:235], a[28:31], v[40:43]\n"
        "global_store_dwordx4 v254, v[32:35], s[28:29] offset:0 sc1\n"
        "v_mfma_f32_16x16x32_bf16 v[40:43], a[224:227], a[24:27], v[40:43]\n"
        "v_mfma_f32_16x16x32_bf16 v[40:43], a[56:59], a[20:23], v[40:43]\n"
        "v_mfma_f32_16x16x32_bf16 v[40:43], a[48:51], a[16:19], v[40:43]\n"
        "global_load_dwordx2 v[236:237], v234, s[48:49] sc1\n"
        "v_mfma_f32_16x16x32_bf16 v[44:47], a[52:55], a[16:19], v[44:47]\n"
        "s_barrier\n"
        "v_mfma_f32_16x16x32_bf16 v[44:47], a[60:63], a[20:23], v[44:47]\n"
        "ds_read_b128 a[0:3], v254 offset:0\n"
        "global_load_lds_dwordx4 v254, s[60:61] offset:0 sc1\n"
        "v_mfma_f32_16x16x32_bf16 v[44:47], a[228:231], a[24:27], v[44:47]\n"
        "ds_read_b128 a[4:7], v254 offset:4096\n"
        "v_cvt_pk_bf16_f32 v40, v40, v41\n"
        "v_mfma_f32_16x16x32_bf16 v[44:47], a[236:239], a[28:31], v[44:47]\n"
        "ds_read_b128 a[8:11], v254 offset:8192\n"
        "v_cvt_pk_bf16_f32 v41, v42, v43\n"
        "v_mfma_f32_16x16x32_bf16 v[56:59], a[40:43], a[220:223], v[56:59]\n"
        "ds_read_b128 a[220:223], v244 offset:31744\n"
        "v_mfma_f32_16x16x32_bf16 v[56:59], a[32:35], a[216:219], v[56:59]\n"
        "ds_read_b128 a[216:219], v244 offset:27648\n"
        "global_load_lds_dwordx4 v254, s[60:61] offset:1024 sc1\n"
        "v_mfma_f32_16x16x32_bf16 v[56:59], a[184:187], a[212:215], v[56:59]\n"
        "ds_read_b128 a[212:215], v244 offset:23552\n"
        "v_cvt_pk_bf16_f32 v42, v44, v45\n"
        "v_mfma_f32_16x16x32_bf16 v[56:59], a[176:179], a[208:211], v[56:59]\n"
        "ds_read_b128 a[208:211], v244 offset:19456\n"
        "v_cvt_pk_bf16_f32 v43, v46, v47\n"
        "v_mfma_f32_16x16x32_bf16 v[48:51], a[48:51], a[192:195], v[48:51]\n"
        "ds_read_b128 a[12:15], v254 offset:12288\n"
        "s_waitcnt lgkmcnt(8)\n"
        "v_mfma_f32_16x16x32_bf16 v[48:51], a[56:59], a[196:199], v[48:51]\n"
        "global_store_dwordx4 v254, v[40:43], s[28:29] offset:1024 sc1\n"
        "ds_read_b128 a[16:19], v254 offset:1024\n"
        "ds_read_b128 a[20:23], v254 offset:5120\n"
        "v_mfma_f32_16x16x32_bf16 v[48:51], a[224:227], a[200:203], v[48:51]\n"
        "ds_read_b128 a[24:27], v254 offset:9216\n"
        "v_mfma_f32_16x16x32_bf16 v[48:51], a[232:235], a[204:207], v[48:51]\n"
        "ds_read_b128 a[28:31], v254 offset:13312\n"
        "v_mfma_f32_16x16x32_bf16 v[52:55], a[236:239], a[204:207], v[52:55]\n"
        "ds_read_b128 a[204:207], v254 offset:14336\n"
        "global_load_lds_dwordx4 v254, s[60:61] offset:2048 sc1\n"
        "v_mfma_f32_16x16x32_bf16 v[52:55], a[228:231], a[200:203], v[52:55]\n"
        "ds_read_b128 a[200:203], v254 offset:10240\n"
        "s_waitcnt lgkmcnt(8)\n"
        "v_mfma_f32_16x16x32_bf16 v[52:55], a[60:63], a[196:199], v[52:55]\n"
        "ds_read_b128 a[196:199], v254 offset:6144\n"
        "v_cvt_pk_bf16_f32 v48, v48, v49\n"
        "v_mfma_f32_16x16x32_bf16 v[52:55], a[52:55], a[192:195], v[52:55]\n"
        "ds_read_b128 a[192:195], v254 offset:2048\n"
        "v_cvt_pk_bf16_f32 v49, v50, v51\n"
        "v_mfma_f32_16x16x32_bf16 v[56:59], a[232:235], a[220:223], v[56:59]\n"
        "global_load_lds_dwordx4 v254, s[60:61] offset:3072 sc1\n"
        "s_add_u32 m0, s73, 0x14000\n"
        "s_waitcnt lgkmcnt(7)\n"
        "v_mfma_f32_16x16x32_bf16 v[56:59], a[224:227], a[216:219], v[56:59]\n"
        "s_add_u32 s60, s68, s66\n"
        "s_addc_u32 s61, s69, 0\n"
        "v_cvt_pk_bf16_f32 v50, v52, v53\n"
        "v_mfma_f32_16x16x32_bf16 v[56:59], a[56:59], a[212:215], v[56:59]\n"
        "s_xor_b32 s66, s17, 2\n"
        "v_cvt_pk_bf16_f32 v51, v54, v55\n"
        "s_lshl_b32 s66, s66, 15\n"
        "v_mfma_f32_16x16x32_bf16 v[56:59], a[48:51], a[208:211], v[56:59]\n"
        "v_mfma_f32_16x16x32_bf16 v[60:63], a[52:55], a[208:211], v[60:63]\n"
        "global_store_dwordx4 v254, v[48:51], s[28:29] offset:2048 sc1\n"
        "ds_read_b128 a[208:211], v254 offset:3072\n"
        "v_mfma_f32_16x16x32_bf16 v[60:63], a[60:63], a[212:215], v[60:63]\n"
        "ds_read_b128 a[212:215], v254 offset:7168\n"
        "v_mfma_f32_16x16x32_bf16 v[60:63], a[228:231], a[216:219], v[60:63]\n"
        "ds_read_b128 a[216:219], v254 offset:11264\n"
        "v_mfma_f32_16x16x32_bf16 v[60:63], a[236:239], a[220:223], v[60:63]\n"
        "ds_read_b128 a[220:223], v254 offset:15360\n"
        "v_cvt_pk_bf16_f32 v56, v56, v57\n"
        "v_mfma_f32_16x16x32_bf16 v[0:3], a[96:99], a[0:3], 0\n"
        "v_cvt_pk_bf16_f32 v57, v58, v59\n"
        "v_mfma_f32_16x16x32_bf16 v[0:3], a[104:107], a[4:7], v[0:3]\n"
        "v_mfma_f32_16x16x32_bf16 v[0:3], a[112:115], a[8:11], v[0:3]\n"
        "v_mfma_f32_16x16x32_bf16 v[0:3], a[120:123], a[12:15], v[0:3]\n"
        "v_mfma_f32_16x16x32_bf16 v[4:7], a[124:127], a[12:15], 0\n"
        "v_cvt_pk_bf16_f32 v58, v60, v61\n"
        "v_cvt_pk_bf16_f32 v59, v62, v63\n"
        "v_mfma_f32_16x16x32_bf16 v[4:7], a[116:119], a[8:11], v[4:7]\n"
        "global_store_dwordx4 v254, v[56:59], s[28:29] offset:3072 sc1\n"
        "s_waitcnt lgkmcnt(2)\n"
        "v_mfma_f32_16x16x32_bf16 v[4:7], a[108:111], a[4:7], v[4:7]\n"
        "v_mfma_f32_16x16x32_bf16 v[4:7], a[100:103], a[0:3], v[4:7]\n"
        "v_mfma_f32_16x16x32_bf16 v[12:15], a[100:103], a[16:19], 0\n"
        "v_mfma_f32_16x16x32_bf16 v[12:15], a[108:111], a[20:23], v[12:15]\n"
        "v_mfma_f32_16x16x32_bf16 v[12:15], a[116:119], a[24:27], v[12:15]\n"
        "v_mfma_f32_16x16x32_bf16 v[12:15], a[124:127], a[28:31], v[12:15]\n"
        "s_lshl2_add_u32 s15, s76, s10\n"
        "s_mul_i32 s16, s13, 263168\n"
        "s_lshl_b32 s15, s15, 14\n"
        "v_mfma_f32_16x16x32_bf16 v[8:11], a[120:123], a[28:31], 0\n"
        "s_add_u32 s16, s16, s15\n"
        "s_add_u32 s16, s16, s73\n"
        "s_add_u32 s16, s16, 0x4000000\n"
        "v_mfma_f32_16x16x32_bf16 v[8:11], a[112:115], a[24:27], v[8:11]\n"
        "s_add_u32 s28, s38, s16\n"
        "s_addc_u32 s29, s39, 0\n"
        "v_mfma_f32_16x16x32_bf16 v[8:11], a[104:107], a[20:23], v[8:11]\n"
        "v_mfma_f32_16x16x32_bf16 v[8:11], a[96:99], a[16:19], v[8:11]\n"
        "s_waitcnt vmcnt(37)\n"
        "v_mfma_f32_16x16x32_bf16 v[16:19], a[96:99], a[192:195], 0\n"
        "s_barrier\n"
        "v_mfma_f32_16x16x32_bf16 v[16:19], a[104:107], a[196:199], v[16:19]\n"
        "ds_read_b128 a[0:3], v254 offset:16384\n"
        "global_load_lds_dwordx4 v254, s[60:61] offset:0 sc1\n"
        "v_mfma_f32_16x16x32_bf16 v[16:19], a[112:115], a[200:203], v[16:19]\n"
        "ds_read_b128 a[4:7], v254 offset:20480\n"
        "v_mfma_f32_16x16x32_bf16 v[16:19], a[120:123], a[204:207], v[16:19]\n"
        "ds_read_b128 a[8:11], v254 offset:24576\n"
        "v_mfma_f32_16x16x32_bf16 v[20:23], a[124:127], a[204:207], 0\n"
        "ds_read_b128 a[12:15], v254 offset:28672\n"
        "v_mfma_f32_16x16x32_bf16 v[20:23], a[116:119], a[200:203], v[20:23]\n"
        "ds_read_b128 a[16:19], v254 offset:17408\n"
        "global_load_lds_dwordx4 v254, s[60:61] offset:1024 sc1\n"
        "v_mfma_f32_16x16x32_bf16 v[20:23], a[108:111], a[196:199], v[20:23]\n"
        "ds_read_b128 a[20:23], v254 offset:21504\n"
        "s_waitcnt lgkmcnt(6)\n"
        "v_mfma_f32_16x16x32_bf16 v[20:23], a[100:103], a[192:195], v[20:23]\n"
        "ds_read_b128 a[24:27], v254 offset:25600\n"
        "v_mfma_f32_16x16x32_bf16 v[28:31], a[100:103], a[208:211], 0\n"
        "ds_read_b128 a[28:31], v254 offset:29696\n"
        "v_mfma_f32_16x16x32_bf16 v[28:31], a[108:111], a[212:215], v[28:31]\n"
        "ds_read_b128 a[192:195], v254 offset:18432\n"
        "global_load_lds_dwordx4 v254, s[60:61] offset:2048 sc1\n"
        "v_mfma_f32_16x16x32_bf16 v[28:31], a[116:119], a[216:219], v[28:31]\n"
        "ds_read_b128 a[196:199], v254 offset:22528\n"
        "v_mfma_f32_16x16x32_bf16 v[28:31], a[124:127], a[220:223], v[28:31]\n"
        "ds_read_b128 a[200:203], v254 offset:26624\n"
        "v_mfma_f32_16x16x32_bf16 v[24:27], a[120:123], a[220:223], 0\n"
        "ds_read_b128 a[204:207], v254 offset:30720\n"
        "s_waitcnt lgkmcnt(8)\n"
        "v_mfma_f32_16x16x32_bf16 v[24:27], a[112:115], a[216:219], v[24:27]\n"
        "ds_read_b128 a[216:219], v254 offset:27648\n"
        "global_load_lds_dwordx4 v254, s[60:61] offset:3072 sc1\n"
        "s_add_u32 m0, s73, 0x4000\n"
        "v_mfma_f32_16x16x32_bf16 v[24:27], a[104:107], a[212:215], v[24:27]\n"
        "ds_read_b128 a[212:215], v254 offset:23552\n"
        "s_add_u32 s60, s68, s66\n"
        "s_addc_u32 s61, s69, 0\n"
        "v_mfma_f32_16x16x32_bf16 v[24:27], a[96:99], a[208:211], v[24:27]\n"
        "ds_read_b128 a[208:211], v254 offset:19456\n"
        "s_xor_b32 s66, s17, 3\n"
        "s_lshl_b32 s66, s66, 15\n"
        "v_mfma_f32_16x16x32_bf16 v[0:3], a[128:131], a[0:3], v[0:3]\n"
        "ds_read_b128 a[220:223], v254 offset:31744\n"
        "v_mfma_f32_16x16x32_bf16 v[0:3], a[136:139], a[4:7], v[0:3]\n"
        "v_mfma_f32_16x16x32_bf16 v[0:3], a[144:147], a[8:11], v[0:3]\n"
        "v_mfma_f32_16x16x32_bf16 v[0:3], a[152:155], a[12:15], v[0:3]\n"
        "v_mfma_f32_16x16x32_bf16 v[4:7], a[156:159], a[12:15], v[4:7]\n"
        "s_waitcnt lgkmcnt(4)\n"
        "v_mfma_f32_16x16x32_bf16 v[4:7], a[148:151], a[8:11], v[4:7]\n"
        "v_mfma_f32_16x16x32_bf16 v[4:7], a[140:143], a[4:7], v[4:7]\n"
        "v_mfma_f32_16x16x32_bf16 v[4:7], a[132:135], a[0:3], v[4:7]\n"
        "v_mfma_f32_16x16x32_bf16 v[12:15], a[132:135], a[16:19], v[12:15]\n"
        "v_mfma_f32_16x16x32_bf16 v[12:15], a[140:143], a[20:23], v[12:15]\n"
        "v_mfma_f32_16x16x32_bf16 v[12:15], a[148:151], a[24:27], v[12:15]\n"
        "v_mfma_f32_16x16x32_bf16 v[12:15], a[156:159], a[28:31], v[12:15]\n"
        "v_mfma_f32_16x16x32_bf16 v[8:11], a[152:155], a[28:31], v[8:11]\n"
        "v_mfma_f32_16x16x32_bf16 v[8:11], a[144:147], a[24:27], v[8:11]\n"
        "v_mfma_f32_16x16x32_bf16 v[8:11], a[136:139], a[20:23], v[8:11]\n"
        "v_mfma_f32_16x16x32_bf16 v[8:11], a[128:131], a[16:19], v[8:11]\n"
        "s_waitcnt vmcnt(37)\n"
        "v_mfma_f32_16x16x32_bf16 v[16:19], a[128:131], a[192:195], v[16:19]\n"
        "s_barrier\n"
        "v_mfma_f32_16x16x32_bf16 v[16:19], a[136:139], a[196:199], v[16:19]\n"
        "ds_write_b32 v225, v224\n"
        "v_mfma_f32_16x16x32_bf16 v[16:19], a[144:147], a[200:203], v[16:19]\n"
        "ds_read_b128 a[0:3], v254 offset:32768\n"
        "v_mfma_f32_16x16x32_bf16 v[16:19], a[152:155], a[204:207], v[16:19]\n"
        "ds_read_b128 a[4:7], v254 offset:36864\n"
        "v_mfma_f32_16x16x32_bf16 v[20:23], a[156:159], a[204:207], v[20:23]\n"
        "ds_read_b128 a[8:11], v254 offset:40960\n"
        "s_waitcnt lgkmcnt(4)\n"
        "v_mfma_f32_16x16x32_bf16 v[20:23], a[148:151], a[200:203], v[20:23]\n"
        "ds_read_b128 a[12:15], v254 offset:45056\n"
        "v_mfma_f32_16x16x32_bf16 v[20:23], a[140:143], a[196:199], v[20:23]\n"
        "ds_read_b128 a[16:19], v254 offset:33792\n"
        "v_mfma_f32_16x16x32_bf16 v[20:23], a[132:135], a[192:195], v[20:23]\n"
        "ds_read_b128 a[20:23], v254 offset:37888\n"
        "v_mfma_f32_16x16x32_bf16 v[28:31], a[132:135], a[208:211], v[28:31]\n"
        "ds_read_b128 a[24:27], v254 offset:41984\n"
        "v_mfma_f32_16x16x32_bf16 v[28:31], a[140:143], a[212:215], v[28:31]\n"
        "ds_read_b128 a[28:31], v254 offset:46080\n"
        "v_mfma_f32_16x16x32_bf16 v[28:31], a[148:151], a[216:219], v[28:31]\n"
        "ds_read_b128 a[192:195], v254 offset:34816\n"
        "v_mfma_f32_16x16x32_bf16 v[28:31], a[156:159], a[220:223], v[28:31]\n"
        "ds_read_b128 a[196:199], v254 offset:38912\n"
        "v_mfma_f32_16x16x32_bf16 v[24:27], a[152:155], a[220:223], v[24:27]\n"
        "ds_read_b128 a[200:203], v254 offset:43008\n"
        "s_waitcnt lgkmcnt(8)\n"
        "v_mfma_f32_16x16x32_bf16 v[24:27], a[144:147], a[216:219], v[24:27]\n"
        "ds_read_b128 a[204:207], v254 offset:47104\n"
        "v_mfma_f32_16x16x32_bf16 v[24:27], a[136:139], a[212:215], v[24:27]\n"
        "ds_read_b128 a[212:215], v254 offset:39936\n"
        "v_mfma_f32_16x16x32_bf16 v[24:27], a[128:131], a[208:211], v[24:27]\n"
        "ds_read_b128 a[208:211], v254 offset:35840\n"
        "s_waitcnt lgkmcnt(8)\n"
        "v_mfma_f32_16x16x32_bf16 v[0:3], v[160:163], a[0:3], v[0:3]\n"
        "ds_read_b128 a[216:219], v254 offset:44032\n"
        "v_mfma_f32_16x16x32_bf16 v[0:3], v[168:171], a[4:7], v[0:3]\n"
        "ds_read_b128 a[220:223], v254 offset:48128\n"
        "v_mfma_f32_16x16x32_bf16 v[0:3], v[176:179], a[8:11], v[0:3]\n"
        "v_mfma_f32_16x16x32_bf16 v[0:3], v[184:187], a[12:15], v[0:3]\n"
        "s_waitcnt vmcnt(11) lgkmcnt(6)\n"
        "v_cmp_ne_u64 vcc, v[236:237], v[250:251]\n"
        "v_mfma_f32_16x16x32_bf16 v[4:7], v[188:191], a[12:15], v[4:7]\n"
        "s_cbranch_vccnz LPZZ_%=\n"
        "LBZZ_%=:\n"
        "v_mfma_f32_16x16x32_bf16 v[4:7], v[180:183], a[8:11], v[4:7]\n"
        "v_mfma_f32_16x16x32_bf16 v[4:7], v[172:175], a[4:7], v[4:7]\n"
        "v_mfma_f32_16x16x32_bf16 v[4:7], v[164:167], a[0:3], v[4:7]\n"
        "v_mfma_f32_16x16x32_bf16 v[12:15], v[164:167], a[16:19], v[12:15]\n"
        "v_mfma_f32_16x16x32_bf16 v[12:15], v[172:175], a[20:23], v[12:15]\n"
        "v_mfma_f32_16x16x32_bf16 v[12:15], v[180:183], a[24:27], v[12:15]\n"
        "v_mfma_f32_16x16x32_bf16 v[12:15], v[188:191], a[28:31], v[12:15]\n"
        "v_mfma_f32_16x16x32_bf16 v[8:11], v[184:187], a[28:31], v[8:11]\n"
        "v_mfma_f32_16x16x32_bf16 v[8:11], v[176:179], a[24:27], v[8:11]\n"
        "v_mfma_f32_16x16x32_bf16 v[8:11], v[168:171], a[20:23], v[8:11]\n"
        "s_waitcnt lgkmcnt(0)\n"
        "v_mfma_f32_16x16x32_bf16 v[8:11], v[160:163], a[16:19], v[8:11]\n"
        "v_mfma_f32_16x16x32_bf16 v[16:19], v[160:163], a[192:195], v[16:19]\n"
        "s_barrier\n"
        "v_mfma_f32_16x16x32_bf16 v[16:19], v[168:171], a[196:199], v[16:19]\n"
        "ds_read_b128 a[0:3], v254 offset:49152\n"
        "global_load_lds_dwordx4 v148, s[58:59] offset:0 sc1\n"
        "s_add_u32 m0, s73, 0x8000\n"
        "v_mfma_f32_16x16x32_bf16 v[16:19], v[176:179], a[200:203], v[16:19]\n"
        "ds_read_b128 a[4:7], v254 offset:53248\n"
        "s_add_u32 s58, s58, 0x8000\n"
        "s_addc_u32 s59, s59, 0\n"
        "v_mfma_f32_16x16x32_bf16 v[16:19], v[184:187], a[204:207], v[16:19]\n"
        "ds_read_b128 a[8:11], v254 offset:57344\n"
        "v_mfma_f32_16x16x32_bf16 v[20:23], v[188:191], a[204:207], v[20:23]\n"
        "ds_read_b128 a[12:15], v254 offset:61440\n"
        "v_mfma_f32_16x16x32_bf16 v[20:23], v[180:183], a[200:203], v[20:23]\n"
        "ds_read_b128 a[16:19], v254 offset:50176\n"
        "global_load_dwordx4 v[96:99], v151, s[64:65]\n"
        "v_mfma_f32_16x16x32_bf16 v[20:23], v[172:175], a[196:199], v[20:23]\n"
        "ds_read_b128 a[20:23], v254 offset:54272\n"
        "s_add_u32 s64, s64, 0x8000\n"
        "s_addc_u32 s65, s65, 0\n"
        "v_mfma_f32_16x16x32_bf16 v[20:23], v[164:167], a[192:195], v[20:23]\n"
        "ds_read_b128 a[24:27], v254 offset:58368\n"
        "v_mfma_f32_16x16x32_bf16 v[28:31], v[164:167], a[208:211], v[28:31]\n"
        "ds_read_b128 a[28:31], v254 offset:62464\n"
        "s_lshl2_add_u32 s14, s13, s10\n"
        "s_lshl2_add_u32 s14, s14, s40\n"
        "v_mfma_f32_16x16x32_bf16 v[28:31], v[172:175], a[212:215], v[28:31]\n"
        "ds_read_b128 a[192:195], v254 offset:51200\n"
        "s_add_u32 s14, s14, 3072\n"
        "v_lshrrev_b32 v154, 1, v254\n"
        "v_mfma_f32_16x16x32_bf16 v[28:31], v[180:183], a[216:219], v[28:31]\n"
        "ds_read_b128 a[196:199], v254 offset:55296\n"
        "s_lshl_b32 s14, s14, 7\n"
        "v_add_u32 v154, s14, v154\n"
        "v_mfma_f32_16x16x32_bf16 v[28:31], v[188:191], a[220:223], v[28:31]\n"
        "ds_read_b128 a[200:203], v254 offset:59392\n"
        "v_mfma_f32_16x16x32_bf16 v[24:27], v[184:187], a[220:223], v[24:27]\n"
        "ds_read_b128 a[204:207], v254 offset:63488\n"
        "s_waitcnt lgkmcnt(8)\n"
        "v_mfma_f32_16x16x32_bf16 v[24:27], v[176:179], a[216:219], v[24:27]\n"
        "ds_read_b128 a[216:219], v254 offset:60416\n"
        "v_mfma_f32_16x16x32_bf16 v[24:27], v[168:171], a[212:215], v[24:27]\n"
        "ds_read_b128 a[212:215], v254 offset:56320\n"
        "v_mfma_f32_16x16x32_bf16 v[24:27], v[160:163], a[208:211], v[24:27]\n"
        "ds_read_b128 a[208:211], v254 offset:52224\n"
        "v_mfma_f32_16x16x32_bf16 v[0:3], v[192:195], a[0:3], v[0:3]\n"
        "ds_read_b128 a[220:223], v254 offset:64512\n"
        "v_mfma_f32_16x16x32_bf16 v[0:3], v[200:203], a[4:7], v[0:3]\n"
        "v_mfma_f32_16x16x32_bf16 v[0:3], v[208:211], a[8:11], v[0:3]\n"
        "v_mfma_f32_16x16x32_bf16 v[0:3], v[216:219], a[12:15], v[0:3]\n"
        "v_mfma_f32_16x16x32_bf16 v[4:7], v[220:223], a[12:15], v[4:7]\n"
        "s_waitcnt lgkmcnt(4)\n"
        "v_mfma_f32_16x16x32_bf16 v[4:7], v[212:215], a[8:11], v[4:7]\n"
        "v_mfma_f32_16x16x32_bf16 v[4:7], v[204:207], a[4:7], v[4:7]\n"
        "v_mfma_f32_16x16x32_bf16 v[4:7], v[196:199], a[0:3], v[4:7]\n"
        "v_mfma_f32_16x16x32_bf16 v[12:15], v[196:199], a[16:19], v[12:15]\n"
        "v_mfma_f32_16x16x32_bf16 v[12:15], v[204:207], a[20:23], v[12:15]\n"
        "v_mfma_f32_16x16x32_bf16 v[12:15], v[212:215], a[24:27], v[12:15]\n"
        "v_mfma_f32_16x16x32_bf16 v[12:15], v[220:223], a[28:31], v[12:15]\n"
        "v_mfma_f32_16x16x32_bf16 v[8:11], v[216:219], a[28:31], v[8:11]\n"
        "v_mfma_f32_16x16x32_bf16 v[8:11], v[208:211], a[24:27], v[8:11]\n"
        "v_mfma_f32_16x16x32_bf16 v[8:11], v[200:203], a[20:23], v[8:11]\n"
        "v_mfma_f32_16x16x32_bf16 v[8:11], v[192:195], a[16:19], v[8:11]\n"
        "v_mfma_f32_16x16x32_bf16 v[16:19], v[192:195], a[192:195], v[16:19]\n"
        "s_barrier\n"
        "v_mfma_f32_16x16x32_bf16 v[16:19], v[200:203], a[196:199], v[16:19]\n"
        "ds_read_b128 a[0:3], v244 offset:32768\n"
        "global_load_lds_dwordx4 v254, s[60:61] offset:0 sc1\n"
        "v_mfma_f32_16x16x32_bf16 v[16:19], v[208:211], a[200:203], v[16:19]\n"
        "ds_read_b128 a[4:7], v244 offset:36864\n"
        "v_mfma_f32_16x16x32_bf16 v[16:19], v[216:219], a[204:207], v[16:19]\n"
        "ds_read_b128 a[8:11], v244 offset:40960\n"
        "v_mfma_f32_16x16x32_bf16 v[20:23], v[220:223], a[204:207], v[20:23]\n"
        "ds_read_b128 a[12:15], v244 offset:45056\n"
        "s_waitcnt lgkmcnt(4)\n"
        "v_mfma_f32_16x16x32_bf16 v[20:23], v[212:215], a[200:203], v[20:23]\n"
        "ds_read_b128 a[16:19], v244 offset:33792\n"
        "global_load_lds_dwordx4 v254, s[60:61] offset:1024 sc1\n"
        "v_mfma_f32_16x16x32_bf16 v[20:23], v[204:207], a[196:199], v[20:23]\n"
        "ds_read_b128 a[20:23], v244 offset:37888\n"
        "v_mfma_f32_16x16x32_bf16 v[20:23], v[196:199], a[192:195], v[20:23]\n"
        "ds_read_b128 a[24:27], v244 offset:41984\n"
        "v_mfma_f32_16x16x32_bf16 v[28:31], v[196:199], a[208:211], v[28:31]\n"
        "ds_read_b128 a[28:31], v244 offset:46080\n"
        "v_mfma_f32_16x16x32_bf16 v[28:31], v[204:207], a[212:215], v[28:31]\n"
        "ds_read_b128 a[192:195], v244 offset:34816\n"
        "global_load_lds_dwordx4 v254, s[60:61] offset:2048 sc1\n"
        "v_mfma_f32_16x16x32_bf16 v[28:31], v[212:215], a[216:219], v[28:31]\n"
        "ds_read_b128 a[196:199], v244 offset:38912\n"
        "v_mfma_f32_16x16x32_bf16 v[28:31], v[220:223], a[220:223], v[28:31]\n"
        "ds_read_b128 a[200:203], v244 offset:43008\n"
        "v_mfma_f32_16x16x32_bf16 v[24:27], v[216:219], a[220:223], v[24:27]\n"
        "ds_read_b128 a[204:207], v244 offset:47104\n"
        "s_waitcnt lgkmcnt(8)\n"
        "v_mfma_f32_16x16x32_bf16 v[24:27], v[208:211], a[216:219], v[24:27]\n"
        "ds_read_b128 a[216:219], v244 offset:44032\n"
        "global_load_lds_dwordx4 v254, s[60:61] offset:3072 sc1\n"
        "s_add_u32 m0, s73, 0x4400\n"
        "v_mfma_f32_16x16x32_bf16 v[24:27], v[200:203], a[212:215], v[24:27]\n"
        "ds_read_b128 a[212:215], v244 offset:39936\n"
        "s_add_u32 s60, s68, s66\n"
        "s_addc_u32 s61, s69, 0\n"
        "v_mfma_f32_16x16x32_bf16 v[24:27], v[192:195], a[208:211], v[24:27]\n"
        "ds_read_b128 a[208:211], v244 offset:35840\n"
        "s_xor_b32 s66, s17, 4\n"
        "s_lshl_b32 s66, s66, 15\n"
        "v_mfma_f32_16x16x32_bf16 v[0:3], v[112:115], a[0:3], v[0:3]\n"
        "ds_read_b128 a[220:223], v244 offset:48128\n"
        "v_mfma_f32_16x16x32_bf16 v[0:3], v[120:123], a[4:7], v[0:3]\n"
        "ds_read_b32 v224, v159 offset:0\n"
        "global_load_lds_dwordx4 v148, s[58:59] offset:0 sc1\n"
        "s_add_u32 m0, s73, 0xc000\n"
        "v_mfma_f32_16x16x32_bf16 v[0:3], a[64:67], a[8:11], v[0:3]\n"
        "ds_read_b32 v225, v159 offset:64\n"
        "s_add_u32 s58, s58, 0x8000\n"
        "s_addc_u32 s59, s59, 0\n"
        "v_mfma_f32_16x16x32_bf16 v[0:3], a[72:75], a[12:15], v[0:3]\n"
        "ds_read_b32 v226, v159 offset:128\n"
        "s_and_b32 s14, s14, 0xfffff9ff\n"
        "v_bfe_u32 v155, v254, 4, 2\n"
        "v_mfma_f32_16x16x32_bf16 v[4:7], a[76:79], a[12:15], v[4:7]\n"
        "ds_read_b32 v227, v159 offset:192\n"
        "v_lshl_add_u32 v155, v155, 9, s14\n"
        "v_mfma_f32_16x16x32_bf16 v[4:7], a[68:71], a[8:11], v[4:7]\n"
        "global_load_dwordx4 v[100:103], v151, s[64:65]\n"
        "s_add_u32 s64, s64, 0x8000\n"
        "s_addc_u32 s65, s65, 0\n"
        "v_mfma_f32_16x16x32_bf16 v[4:7], v[124:127], a[4:7], v[4:7]\n"
        "s_waitcnt lgkmcnt(6)\n"
        "v_mfma_f32_16x16x32_bf16 v[4:7], v[116:119], a[0:3], v[4:7]\n"
        "v_mfma_f32_16x16x32_bf16 v[12:15], v[116:119], a[16:19], v[12:15]\n"
        "v_mfma_f32_16x16x32_bf16 v[12:15], v[124:127], a[20:23], v[12:15]\n"
        "s_waitcnt lgkmcnt(3)\n"
        "v_sub_f32 v228, 1.0, v224\n"
        "v_cvt_pk_bf16_f32 v228, 0, v228\n"
        "v_mfma_f32_16x16x32_bf16 v[12:15], a[68:71], a[24:27], v[12:15]\n"
        "s_waitcnt lgkmcnt(2)\n"
        "v_sub_f32 v229, 1.0, v225\n"
        "v_cvt_pk_bf16_f32 v229, 0, v229\n"
        "v_mfma_f32_16x16x32_bf16 v[12:15], a[76:79], a[28:31], v[12:15]\n"
        "s_waitcnt lgkmcnt(1)\n"
        "v_sub_f32 v230, 1.0, v226\n"
        "v_cvt_pk_bf16_f32 v230, 0, v230\n"
        "v_mfma_f32_16x16x32_bf16 v[8:11], a[72:75], a[28:31], v[8:11]\n"
        "s_waitcnt lgkmcnt(0)\n"
        "v_sub_f32 v231, 1.0, v227\n"
        "v_cvt_pk_bf16_f32 v231, 0, v231\n"
        "v_mfma_f32_16x16x32_bf16 v[8:11], a[64:67], a[24:27], v[8:11]\n"
        "v_mfma_f32_16x16x32_bf16 v[8:11], v[120:123], a[20:23], v[8:11]\n"
        "v_mfma_f32_16x16x32_bf16 v[8:11], v[112:115], a[16:19], v[8:11]\n"
        "v_mfma_f32_16x16x32_bf16 v[16:19], v[112:115], a[192:195], v[16:19]\n"
        "s_barrier\n"
        "v_mfma_f32_16x16x32_bf16 v[16:19], v[120:123], a[196:199], v[16:19]\n"
        "ds_read_b128 a[0:3], v244 offset:0\n"
        "global_load_lds_dwordx4 v254, s[60:61] offset:0 sc1\n"
        "v_mfma_f32_16x16x32_bf16 v[16:19], a[64:67], a[200:203], v[16:19]\n"
        "ds_read_b128 a[4:7], v244 offset:4096\n"
        "v_mfma_f32_16x16x32_bf16 v[16:19], a[72:75], a[204:207], v[16:19]\n"
        "ds_read_b128 a[8:11], v244 offset:8192\n"
        "v_mfma_f32_16x16x32_bf16 v[20:23], a[76:79], a[204:207], v[20:23]\n"
        "ds_read_b128 a[12:15], v244 offset:12288\n"
        "v_mfma_f32_16x16x32_bf16 v[20:23], a[68:71], a[200:203], v[20:23]\n"
        "ds_read_b128 a[16:19], v244 offset:1024\n"
        "global_load_lds_dwordx4 v254, s[60:61] offset:1024 sc1\n"
        "v_mfma_f32_16x16x32_bf16 v[20:23], v[124:127], a[196:199], v[20:23]\n"
        "ds_read_b128 a[20:23], v244 offset:5120\n"
        "v_mfma_f32_16x16x32_bf16 v[20:23], v[116:119], a[192:195], v[20:23]\n"
        "ds_read_b128 a[24:27], v244 offset:9216\n"
        "v_mfma_f32_16x16x32_bf16 v[28:31], v[116:119], a[208:211], v[28:31]\n"
        "ds_read_b128 a[28:31], v244 offset:13312\n"
        "v_mfma_f32_16x16x32_bf16 v[28:31], v[124:127], a[212:215], v[28:31]\n"
        "ds_read_b128 a[192:195], v244 offset:2048\n"
        "global_load_lds_dwordx4 v254, s[60:61] offset:2048 sc1\n"
        "v_mfma_f32_16x16x32_bf16 v[28:31], a[68:71], a[216:219], v[28:31]\n"
        "ds_read_b128 a[196:199], v244 offset:6144\n"
        "v_mfma_f32_16x16x32_bf16 v[28:31], a[76:79], a[220:223], v[28:31]\n"
        "ds_read_b128 a[200:203], v244 offset:10240\n"
        "v_mfma_f32_16x16x32_bf16 v[24:27], a[72:75], a[220:223], v[24:27]\n"
        "ds_read_b128 a[204:207], v244 offset:14336\n"
        "s_waitcnt lgkmcnt(8)\n"
        "v_mfma_f32_16x16x32_bf16 v[24:27], a[64:67], a[216:219], v[24:27]\n"
        "ds_read_b128 a[216:219], v244 offset:11264\n"
        "global_load_lds_dwordx4 v254, s[60:61] offset:3072 sc1\n"
        "s_add_u32 m0, s73, 0x4800\n"
        "v_mfma_f32_16x16x32_bf16 v[24:27], v[120:123], a[212:215], v[24:27]\n"
        "ds_read_b128 a[212:215], v244 offset:7168\n"
        "s_add_u32 s60, s68, s66\n"
        "s_addc_u32 s61, s69, 0\n"
        "v_mfma_f32_16x16x32_bf16 v[24:27], v[112:115], a[208:211], v[24:27]\n"
        "ds_read_b128 a[208:211], v244 offset:3072\n"
        "s_xor_b32 s66, s17, 5\n"
        "s_lshl_b32 s66, s66, 15\n"
        "v_mfma_f32_16x16x32_bf16 v[0:3], a[80:83], a[0:3], v[0:3]\n"
        "ds_read_b128 a[220:223], v244 offset:15360\n"
        "v_mfma_f32_16x16x32_bf16 v[0:3], a[88:91], a[4:7], v[0:3]\n"
        "global_load_lds_dwordx4 v148, s[58:59] offset:0 sc1\n"
        "s_add_u32 s58, s58, 0x8000\n"
        "s_addc_u32 s59, s59, 0\n"
        "v_mfma_f32_16x16x32_bf16 v[0:3], a[160:163], a[8:11], v[0:3]\n"
        "s_add_u32 m0, s73, 0x18000\n"
        "v_mfma_f32_16x16x32_bf16 v[0:3], a[168:171], a[12:15], v[0:3]\n"
        "v_mfma_f32_16x16x32_bf16 v[4:7], a[172:175], a[12:15], v[4:7]\n"
        "global_load_dwordx4 v[104:107], v151, s[64:65]\n"
        "v_mfma_f32_16x16x32_bf16 v[4:7], a[164:167], a[8:11], v[4:7]\n"
        "s_add_u32 s64, s64, 0x8000\n"
        "s_addc_u32 s65, s65, 0\n"
        "s_waitcnt lgkmcnt(3)\n"
        "v_mfma_f32_16x16x32_bf16 v[4:7], a[92:95], a[4:7], v[4:7]\n"
        "v_mfma_f32_16x16x32_bf16 v[4:7], a[84:87], a[0:3], v[4:7]\n"
        "v_mfma_f32_16x16x32_bf16 v[12:15], a[84:87], a[16:19], v[12:15]\n"
        "v_mfma_f32_16x16x32_bf16 v[12:15], a[92:95], a[20:23], v[12:15]\n"
        "v_mfma_f32_16x16x32_bf16 v[12:15], a[164:167], a[24:27], v[12:15]\n"
        "v_mfma_f32_16x16x32_bf16 v[12:15], a[172:175], a[28:31], v[12:15]\n"
        "v_mfma_f32_16x16x32_bf16 v[8:11], a[168:171], a[28:31], v[8:11]\n"
        "v_mfma_f32_16x16x32_bf16 v[8:11], a[160:163], a[24:27], v[8:11]\n"
        "v_mfma_f32_16x16x32_bf16 v[8:11], a[88:91], a[20:23], v[8:11]\n"
        "v_mfma_f32_16x16x32_bf16 v[8:11], a[80:83], a[16:19], v[8:11]\n"
        "v_mfma_f32_16x16x32_bf16 v[16:19], a[80:83], a[192:195], v[16:19]\n"
        "s_barrier\n"
        "v_mfma_f32_16x16x32_bf16 v[16:19], a[88:91], a[196:199], v[16:19]\n"
        "ds_read_b128 a[0:3], v244 offset:49152\n"
        "global_load_lds_dwordx4 v254, s[60:61] offset:0 sc1\n"
        "s_mul_i32 s16, s13, 263168\n"
        "v_mfma_f32_16x16x32_bf16 v[16:19], a[160:163], a[200:203], v[16:19]\n"
        "ds_read_b128 a[4:7], v244 offset:53248\n"
        "s_lshl2_add_u32 s15, s10, s74\n"
        "s_lshl_b32 s15, s15, 14\n"
        "LHB2_%=:\n"
        "s_cbranch_execz LHRB_%=\n"
        "v_mfma_f32_16x16x32_bf16 v[16:19], a[168:171], a[204:207], v[16:19]\n"
        "ds_read_b128 a[8:11], v244 offset:57344\n"
        "s_add_u32 s16, s16, s15\n"
        "s_add_u32 s16, s16, s73\n"
        "v_mfma_f32_16x16x32_bf16 v[20:23], a[172:175], a[204:207], v[20:23]\n"
        "ds_read_b128 a[12:15], v244 offset:61440\n"
        "s_add_u32 s16, s16, 0x4000000\n"
        "s_lshl2_add_u32 s15, s10, s75\n"
        "v_mfma_f32_16x16x32_bf16 v[20:23], a[164:167], a[200:203], v[20:23]\n"
        "ds_read_b128 a[16:19], v244 offset:50176\n"
        "global_load_lds_dwordx4 v254, s[60:61] offset:1024 sc1\n"
        "s_lshl_b32 s15, s15, 14\n"
        "v_mfma_f32_16x16x32_bf16 v[20:23], a[92:95], a[196:199], v[20:23]\n"
        "ds_read_b128 a[20:23], v244 offset:54272\n"
        "s_add_u32 s30, s38, s16\n"
        "s_addc_u32 s31, s39, 0\n"
        "v_mfma_f32_16x16x32_bf16 v[20:23], a[84:87], a[192:195], v[20:23]\n"
        "ds_read_b128 a[24:27], v244 offset:58368\n"
        "s_mul_i32 s16, s13, 263168\n"
        "s_waitcnt lgkmcnt(7)\n"
        "v_mfma_f32_16x16x32_bf16 v[28:31], a[84:87], a[208:211], v[28:31]\n"
        "ds_read_b128 a[28:31], v244 offset:62464\n"
        "s_add_u32 s16, s16, s15\n"
        "s_add_u32 s16, s16, s73\n"
        "v_mfma_f32_16x16x32_bf16 v[28:31], a[92:95], a[212:215], v[28:31]\n"
        "ds_read_b128 a[192:195], v244 offset:51200\n"
        "global_load_lds_dwordx4 v254, s[60:61] offset:2048 sc1\n"
        "s_add_u32 s16, s16, 0x4000000\n"
        "v_mfma_f32_16x16x32_bf16 v[28:31], a[164:167], a[216:219], v[28:31]\n"
        "ds_read_b128 a[196:199], v244 offset:55296\n"
        "s_lshl2_add_u32 s15, s10, s76\n"
        "s_lshl_b32 s15, s15, 14\n"
        "v_mfma_f32_16x16x32_bf16 v[28:31], a[172:175], a[220:223], v[28:31]\n"
        "ds_read_b128 a[200:203], v244 offset:59392\n"
        "s_waitcnt vmcnt(15) lgkmcnt(8)\n"
        "v_mfma_f32_16x16x32_bf16 v[24:27], a[168:171], a[220:223], v[24:27]\n"
        "ds_read_b128 a[204:207], v244 offset:63488\n"
        "v_mfma_f32_16x16x32_bf16 v[24:27], a[160:163], a[216:219], v[24:27]\n"
        "ds_read_b128 a[216:219], v244 offset:60416\n"
        "global_load_lds_dwordx4 v254, s[60:61] offset:3072 sc1\n"
        "s_add_u32 m0, s73, 0x4c00\n"
        "v_mfma_f32_16x16x32_bf16 v[24:27], a[88:91], a[212:215], v[24:27]\n"
        "ds_read_b128 a[212:215], v244 offset:56320\n"
        "s_add_u32 s60, s68, s66\n"
        "s_addc_u32 s61, s69, 0\n"
        "v_mfma_f32_16x16x32_bf16 v[24:27], a[80:83], a[208:211], v[24:27]\n"
        "s_waitcnt lgkmcnt(7)\n"
        "ds_read_b128 a[208:211], v244 offset:52224\n"
        "s_xor_b32 s66, s17, 6\n"
        "v_mfma_f32_16x16x32_bf16 v[0:3], a[176:179], a[0:3], v[0:3]\n"
        "ds_read_b128 a[220:223], v244 offset:64512\n"
        "s_lshl_b32 s66, s66, 15\n"
        "v_mfma_f32_16x16x32_bf16 v[0:3], a[184:187], a[4:7], v[0:3]\n"
        "v_permlane16_swap_b32 v96, v98\n"
        "v_mfma_f32_16x16x32_bf16 v[0:3], a[32:35], a[8:11], v[0:3]\n"
        "v_permlane16_swap_b32 v97, v99\n"
        "v_mfma_f32_16x16x32_bf16 v[0:3], a[40:43], a[12:15], v[0:3]\n"
        "global_load_lds_dwordx4 v148, s[58:59] offset:0 sc1\n"
        "s_add_u32 m0, s73, 0x10000\n"
        "v_lshlrev_b32 v64, 16, v96\n"
        "v_mfma_f32_16x16x32_bf16 v[4:7], a[44:47], a[12:15], v[4:7]\n"
        "v_and_b32 v65, s35, v96\n"
        "v_lshlrev_b32 v66, 16, v97\n"
        "v_mfma_f32_16x16x32_bf16 v[4:7], a[36:39], a[8:11], v[4:7]\n"
        "v_and_b32 v67, s35, v97\n"
        "v_lshlrev_b32 v68, 16, v98\n"
        "v_mfma_f32_16x16x32_bf16 v[4:7], a[188:191], a[4:7], v[4:7]\n"
        "global_load_dwordx4 v[108:111], v151, s[64:65]\n"
        "v_mfma_f32_16x16x32_bf16 v[4:7], a[180:183], a[0:3], v[4:7]\n"
        "s_waitcnt lgkmcnt(2)\n"
        "v_and_b32 v69, s35, v98\n"
        "v_lshlrev_b32 v70, 16, v99\n"
        "v_mfma_f32_16x16x32_bf16 v[12:15], a[180:183], a[16:19], v[12:15]\n"
        "v_and_b32 v71, s35, v99\n"
        "v_mfma_f32_16x16x32_bf16 v[12:15], a[188:191], a[20:23], v[12:15]\n"
        "v_mfma_f32_16x16x32_bf16 v[12:15], a[36:39], a[24:27], v[12:15]\n"
        "v_mfma_f32_16x16x32_bf16 v[12:15], a[44:47], a[28:31], v[12:15]\n"
        "v_mfma_f32_16x16x32_bf16 v[8:11], a[40:43], a[28:31], v[8:11]\n"
        "v_mfma_f32_16x16x32_bf16 v[8:11], a[32:35], a[24:27], v[8:11]\n"
        "v_mfma_f32_16x16x32_bf16 v[8:11], a[184:187], a[20:23], v[8:11]\n"
        "v_mfma_f32_16x16x32_bf16 v[8:11], a[176:179], a[16:19], v[8:11]\n"
        "v_mfma_f32_16x16x32_bf16 v[16:19], a[176:179], a[192:195], v[16:19]\n"
        "s_barrier\n"
        "v_mfma_f32_16x16x32_bf16 v[16:19], a[184:187], a[196:199], v[16:19]\n"
        "ds_read_b128 a[0:3], v245 offset:0\n"
        "global_load_lds_dwordx4 v254, s[60:61] offset:0 sc1\n"
        "v_mfma_f32_16x16x32_bf16 v[16:19], a[32:35], a[200:203], v[16:19]\n"
        "ds_read_b128 a[4:7], v245 offset:4096\n"
        "v_mfma_f32_16x16x32_bf16 v[16:19], a[40:43], a[204:207], v[16:19]\n"
        "ds_read_b128 a[8:11], v245 offset:8192\n"
        "v_mfma_f32_16x16x32_bf16 v[20:23], a[44:47], a[204:207], v[20:23]\n"
        "ds_read_b128 a[12:15], v245 offset:12288\n"
        "v_mfma_f32_16x16x32_bf16 v[20:23], a[36:39], a[200:203], v[20:23]\n"
        "ds_read_b128 a[28:31], v245 offset:13312\n"
        "global_load_lds_dwordx4 v254, s[60:61] offset:1024 sc1\n"
        "v_mfma_f32_16x16x32_bf16 v[20:23], a[188:191], a[196:199], v[20:23]\n"
        "ds_read_b128 a[24:27], v245 offset:9216\n"
        "v_mfma_f32_16x16x32_bf16 v[20:23], a[180:183], a[192:195], v[20:23]\n"
        "ds_read_b128 a[20:23], v245 offset:5120\n"
        "s_waitcnt lgkmcnt(7)\n"
        "v_mfma_f32_16x16x32_bf16 v[28:31], a[180:183], a[208:211], v[28:31]\n"
        "ds_read_b128 a[16:19], v245 offset:1024\n"
        "v_mfma_f32_16x16x32_bf16 v[28:31], a[188:191], a[212:215], v[28:31]\n"
        "ds_read_b128 a[192:195], v245 offset:2048\n"
        "s_waitcnt lgkmcnt(8)\n"
        "v_mfma_f32_16x16x32_bf16 v[0:3], a[48:51], a[0:3], v[0:3]\n"
        "ds_read_b128 a[196:199], v245 offset:6144\n"
        "s_waitcnt lgkmcnt(8)\n"
        "v_mfma_f32_16x16x32_bf16 v[0:3], a[56:59], a[4:7], v[0:3]\n"
        "ds_read_b128 a[200:203], v245 offset:10240\n"
        "s_waitcnt lgkmcnt(8)\n"
        "v_mfma_f32_16x16x32_bf16 v[0:3], a[224:227], a[8:11], v[0:3]\n"
        "ds_read_b128 a[204:207], v245 offset:14336\n"
        "s_waitcnt lgkmcnt(8)\n"
        "v_mfma_f32_16x16x32_bf16 v[0:3], a[232:235], a[12:15], v[0:3]\n"
        "global_load_lds_dwordx4 v254, s[60:61] offset:2048 sc1\n"
        "v_mfma_f32_16x16x32_bf16 v[4:7], a[236:239], a[12:15], v[4:7]\n"
        "v_mfma_f32_16x16x32_bf16 v[4:7], a[228:231], a[8:11], v[4:7]\n"
        "v_mfma_f32_16x16x32_bf16 v[4:7], a[60:63], a[4:7], v[4:7]\n"
        "global_load_lds_dwordx4 v254, s[60:61] offset:3072 sc1\n"
        "s_add_u32 m0, s73, 0x1c000\n"
        "v_mfma_f32_16x16x32_bf16 v[4:7], a[52:55], a[0:3], v[4:7]\n"
        "s_add_u32 s60, s68, s66\n"
        "s_addc_u32 s61, s69, 0\n"
        "v_cvt_pk_bf16_f32 v0, v0, v1\n"
        "v_mfma_f32_16x16x32_bf16 v[28:31], a[36:39], a[216:219], v[28:31]\n"
        "s_waitcnt lgkmcnt(2)\n"
        "v_cvt_pk_bf16_f32 v1, v2, v3\n"
        "s_xor_b32 s66, s17, 7\n"
        "v_mfma_f32_16x16x32_bf16 v[28:31], a[44:47], a[220:223], v[28:31]\n"
        "s_lshl_b32 s66, s66, 15\n"
        "v_cvt_pk_bf16_f32 v2, v4, v5\n"
        "v_cvt_pk_bf16_f32 v3, v6, v7\n"
        "v_mfma_f32_16x16x32_bf16 v[8:11], a[232:235], a[28:31], v[8:11]\n"
        "global_store_dwordx4 v254, v[0:3], s[28:29] offset:0 sc1\n"
        "s_waitcnt vmcnt(17) lgkmcnt(0)\n"
        "v_permlane16_swap_b32 v100, v102\n"
        "v_lshlrev_b32 v72, 16, v100\n"
        "v_mfma_f32_16x16x32_bf16 v[8:11], a[224:227], a[24:27], v[8:11]\n"
        "v_permlane16_swap_b32 v101, v103\n"
        "v_mfma_f32_16x16x32_bf16 v[8:11], a[56:59], a[20:23], v[8:11]\n"
        "v_and_b32 v73, s35, v100\n"
        "v_lshlrev_b32 v74, 16, v101\n"
        "v_mfma_f32_16x16x32_bf16 v[8:11], a[48:51], a[16:19], v[8:11]\n"
        "v_and_b32 v75, s35, v101\n"
        "v_lshlrev_b32 v76, 16, v102\n"
        "v_mfma_f32_16x16x32_bf16 v[12:15], a[52:55], a[16:19], v[12:15]\n"
        "s_barrier\n"
        "v_mfma_f32_16x16x32_bf16 v[12:15], a[60:63], a[20:23], v[12:15]\n"
        "ds_read_b128 a[0:3], v245 offset:16384\n"
        "global_load_lds_dwordx4 v254, s[60:61] offset:0 sc1\n"
        "v_mfma_f32_16x16x32_bf16 v[12:15], a[228:231], a[24:27], v[12:15]\n"
        "ds_read_b128 a[4:7], v245 offset:20480\n"
        "v_cvt_pk_bf16_f32 v8, v8, v9\n"
        "v_mfma_f32_16x16x32_bf16 v[12:15], a[236:239], a[28:31], v[12:15]\n"
        "ds_read_b128 a[8:11], v245 offset:24576\n"
        "v_cvt_pk_bf16_f32 v9, v10, v11\n"
        "v_mfma_f32_16x16x32_bf16 v[24:27], a[40:43], a[220:223], v[24:27]\n"
        "ds_read_b128 a[220:223], v245 offset:15360\n"
        "v_and_b32 v77, s35, v102\n"
        "v_mfma_f32_16x16x32_bf16 v[24:27], a[32:35], a[216:219], v[24:27]\n"
        "ds_read_b128 a[216:219], v245 offset:11264\n"
        "global_load_lds_dwordx4 v254, s[60:61] offset:1024 sc1\n"
        "v_mfma_f32_16x16x32_bf16 v[24:27], a[184:187], a[212:215], v[24:27]\n"
        "ds_read_b128 a[212:215], v245 offset:7168\n"
        "v_cvt_pk_bf16_f32 v10, v12, v13\n"
        "v_mfma_f32_16x16x32_bf16 v[24:27], a[176:179], a[208:211], v[24:27]\n"
        "ds_read_b128 a[208:211], v245 offset:3072\n"
        "v_cvt_pk_bf16_f32 v11, v14, v15\n"
        "v_mfma_f32_16x16x32_bf16 v[16:19], a[48:51], a[192:195], v[16:19]\n"
        "ds_read_b128 a[12:15], v245 offset:28672\n"
        "v_lshlrev_b32 v78, 16, v103\n"
        "v_mfma_f32_16x16x32_bf16 v[16:19], a[56:59], a[196:199], v[16:19]\n"
        "global_store_dwordx4 v254, v[8:11], s[28:29] offset:1024 sc1\n"
        "ds_read_b128 a[16:19], v245 offset:17408\n"
        "ds_read_b128 a[20:23], v245 offset:21504\n"
        "v_and_b32 v79, s35, v103\n"
        "v_mfma_f32_16x16x32_bf16 v[16:19], a[224:227], a[200:203], v[16:19]\n"
        "ds_read_b128 a[24:27], v245 offset:25600\n"
        "v_mfma_f32_16x16x32_bf16 v[16:19], a[232:235], a[204:207], v[16:19]\n"
        "ds_read_b128 a[28:31], v245 offset:29696\n"
        "v_mfma_f32_16x16x32_bf16 v[20:23], a[236:239], a[204:207], v[20:23]\n"
        "ds_read_b128 a[204:207], v245 offset:30720\n"
        "global_load_lds_dwordx4 v254, s[60:61] offset:2048 sc1\n"
        "v_mfma_f32_16x16x32_bf16 v[20:23], a[228:231], a[200:203], v[20:23]\n"
        "ds_read_b128 a[200:203], v245 offset:26624\n"
        "s_waitcnt lgkmcnt(8)\n"
        "v_mfma_f32_16x16x32_bf16 v[20:23], a[60:63], a[196:199], v[20:23]\n"
        "ds_read_b128 a[196:199], v245 offset:22528\n"
        "v_cvt_pk_bf16_f32 v16, v16, v17\n"
        "v_mfma_f32_16x16x32_bf16 v[20:23], a[52:55], a[192:195], v[20:23]\n"
        "ds_read_b128 a[192:195], v245 offset:18432\n"
        "v_cvt_pk_bf16_f32 v17, v18, v19\n"
        "v_mfma_f32_16x16x32_bf16 v[24:27], a[232:235], a[220:223], v[24:27]\n"
        "global_load_lds_dwordx4 v254, s[60:61] offset:3072 sc1\n"
        "s_add_u32 m0, s73, 0x20000\n"
        "s_waitcnt lgkmcnt(7)\n"
        "v_mfma_f32_16x16x32_bf16 v[24:27], a[224:227], a[216:219], v[24:27]\n"
        "s_add_u32 s60, s68, s66\n"
        "s_addc_u32 s61, s69, 0\n"
        "v_cvt_pk_bf16_f32 v18, v20, v21\n"
        "v_mfma_f32_16x16x32_bf16 v[28:31], a[52:55], a[208:211], v[28:31]\n"
        "v_cvt_pk_bf16_f32 v19, v22, v23\n"
        "v_mfma_f32_16x16x32_bf16 v[24:27], a[56:59], a[212:215], v[24:27]\n"
        "v_mfma_f32_16x16x32_bf16 v[28:31], a[60:63], a[212:215], v[28:31]\n"
        "global_store_dwordx4 v254, v[16:19], s[28:29] offset:2048 sc1\n"
        "ds_read_b128 a[212:215], v245 offset:23552\n"
        "v_mfma_f32_16x16x32_bf16 v[24:27], a[48:51], a[208:211], v[24:27]\n"
        "ds_read_b128 a[208:211], v245 offset:19456\n"
        "v_mfma_f32_16x16x32_bf16 v[28:31], a[228:231], a[216:219], v[28:31]\n"
        "ds_read_b128 a[216:219], v245 offset:27648\n"
        "v_mfma_f32_16x16x32_bf16 v[28:31], a[236:239], a[220:223], v[28:31]\n"
        "ds_read_b128 a[220:223], v245 offset:31744\n"
        "s_waitcnt vmcnt(17) lgkmcnt(4)\n"
        "v_mfma_f32_16x16x32_bf16 v[32:35], a[96:99], a[0:3], 0\n"
        "v_permlane16_swap_b32 v104, v106\n"
        "v_mfma_f32_16x16x32_bf16 v[32:35], a[104:107], a[4:7], v[32:35]\n"
        "v_cvt_pk_bf16_f32 v24, v24, v25\n"
        "v_cvt_pk_bf16_f32 v25, v26, v27\n"
        "v_mfma_f32_16x16x32_bf16 v[32:35], a[112:115], a[8:11], v[32:35]\n"
        "v_cvt_pk_bf16_f32 v26, v28, v29\n"
        "v_cvt_pk_bf16_f32 v27, v30, v31\n"
        "v_mfma_f32_16x16x32_bf16 v[32:35], a[120:123], a[12:15], v[32:35]\n"
        "global_store_dwordx4 v254, v[24:27], s[28:29] offset:3072 sc1\n"
        "v_permlane16_swap_b32 v105, v107\n"
        "v_lshlrev_b32 v80, 16, v104\n"
        "v_and_b32 v81, s35, v104\n"
        "v_mfma_f32_16x16x32_bf16 v[36:39], a[124:127], a[12:15], 0\n"
        "v_lshlrev_b32 v82, 16, v105\n"
        "v_and_b32 v83, s35, v105\n"
        "v_mfma_f32_16x16x32_bf16 v[36:39], a[116:119], a[8:11], v[36:39]\n"
        "v_lshlrev_b32 v84, 16, v106\n"
        "v_and_b32 v85, s35, v106\n"
        "v_mfma_f32_16x16x32_bf16 v[36:39], a[108:111], a[4:7], v[36:39]\n"
        "v_lshlrev_b32 v86, 16, v107\n"
        "v_and_b32 v87, s35, v107\n"
        "v_mfma_f32_16x16x32_bf16 v[36:39], a[100:103], a[0:3], v[36:39]\n"
        "v_mfma_f32_16x16x32_bf16 v[44:47], a[100:103], a[16:19], 0\n"
        "v_mfma_f32_16x16x32_bf16 v[44:47], a[108:111], a[20:23], v[44:47]\n"
        "v_mfma_f32_16x16x32_bf16 v[44:47], a[116:119], a[24:27], v[44:47]\n"
        "v_mfma_f32_16x16x32_bf16 v[44:47], a[124:127], a[28:31], v[44:47]\n"
        "v_mfma_f32_16x16x32_bf16 v[40:43], a[120:123], a[28:31], 0\n"
        "v_mfma_f32_16x16x32_bf16 v[40:43], a[112:115], a[24:27], v[40:43]\n"
        "v_mfma_f32_16x16x32_bf16 v[40:43], a[104:107], a[20:23], v[40:43]\n"
        "v_mfma_f32_16x16x32_bf16 v[40:43], a[96:99], a[16:19], v[40:43]\n"
        "v_mfma_f32_16x16x32_bf16 v[48:51], a[96:99], a[192:195], 0\n"
        "s_barrier\n"
        "v_mfma_f32_16x16x32_bf16 v[48:51], a[104:107], a[196:199], v[48:51]\n"
        "ds_read_b128 a[0:3], v244 offset:16384\n"
        "global_load_lds_dwordx4 v254, s[60:61] offset:0 sc1\n"
        "v_mfma_f32_16x16x32_bf16 v[48:51], a[112:115], a[200:203], v[48:51]\n"
        "ds_read_b128 a[4:7], v244 offset:20480\n"
        "v_mfma_f32_16x16x32_bf16 v[48:51], a[120:123], a[204:207], v[48:51]\n"
        "ds_read_b128 a[8:11], v244 offset:24576\n"
        "v_mfma_f32_16x16x32_bf16 v[52:55], a[124:127], a[204:207], 0\n"
        "ds_read_b128 a[12:15], v244 offset:28672\n"
        "s_waitcnt lgkmcnt(4)\n"
        "v_mfma_f32_16x16x32_bf16 v[52:55], a[116:119], a[200:203], v[52:55]\n"
        "ds_read_b128 a[16:19], v244 offset:17408\n"
        "global_load_lds_dwordx4 v254, s[60:61] offset:1024 sc1\n"
        "v_mfma_f32_16x16x32_bf16 v[52:55], a[108:111], a[196:199], v[52:55]\n"
        "ds_read_b128 a[20:23], v244 offset:21504\n"
        "v_mfma_f32_16x16x32_bf16 v[52:55], a[100:103], a[192:195], v[52:55]\n"
        "ds_read_b128 a[24:27], v244 offset:25600\n"
        "v_mfma_f32_16x16x32_bf16 v[60:63], a[100:103], a[208:211], 0\n"
        "ds_read_b128 a[28:31], v244 offset:29696\n"
        "v_mfma_f32_16x16x32_bf16 v[60:63], a[108:111], a[212:215], v[60:63]\n"
        "ds_read_b128 a[192:195], v244 offset:18432\n"
        "global_load_lds_dwordx4 v254, s[60:61] offset:2048 sc1\n"
        "v_mfma_f32_16x16x32_bf16 v[60:63], a[116:119], a[216:219], v[60:63]\n"
        "ds_read_b128 a[196:199], v244 offset:22528\n"
        "v_mfma_f32_16x16x32_bf16 v[60:63], a[124:127], a[220:223], v[60:63]\n"
        "ds_read_b128 a[200:203], v244 offset:26624\n"
        "v_mfma_f32_16x16x32_bf16 v[56:59], a[120:123], a[220:223], 0\n"
        "ds_read_b128 a[204:207], v244 offset:30720\n"
        "s_waitcnt lgkmcnt(8)\n"
        "v_mfma_f32_16x16x32_bf16 v[56:59], a[112:115], a[216:219], v[56:59]\n"
        "ds_read_b128 a[216:219], v244 offset:27648\n"
        "global_load_lds_dwordx4 v254, s[60:61] offset:3072 sc1\n"
        "v_mfma_f32_16x16x32_bf16 v[56:59], a[104:107], a[212:215], v[56:59]\n"
        "ds_read_b128 a[212:215], v244 offset:23552\n"
        "s_waitcnt vmcnt(17)\n"
        "v_mfma_f32_16x16x32_bf16 v[56:59], a[96:99], a[208:211], v[56:59]\n"
        "ds_read_b128 a[208:211], v244 offset:19456\n"
        "v_mfma_f32_16x16x32_bf16 v[32:35], a[128:131], a[0:3], v[32:35]\n"
        "ds_read_b128 a[220:223], v244 offset:31744\n"
        "s_waitcnt vmcnt(16) lgkmcnt(8)\n"
        "v_mfma_f32_16x16x32_bf16 v[32:35], a[136:139], a[4:7], v[32:35]\n"
        "ds_read_b64_tr_b16 v[128:129], v149 offset:0\n"
        "v_mfma_f32_16x16x32_bf16 v[32:35], a[144:147], a[8:11], v[32:35]\n"
        "ds_read_b64_tr_b16 v[130:131], v149 offset:2048\n"
        "v_mfma_f32_16x16x32_bf16 v[32:35], a[152:155], a[12:15], v[32:35]\n"
        "ds_read_b64_tr_b16 v[132:133], v149 offset:32\n"
        "v_mfma_f32_16x16x32_bf16 v[36:39], a[156:159], a[12:15], v[36:39]\n"
        "ds_read_b64_tr_b16 v[134:135], v149 offset:2080\n"
        "v_mfma_f32_16x16x32_bf16 v[36:39], a[148:151], a[8:11], v[36:39]\n"
        "ds_read_b64_tr_b16 v[136:137], v149 offset:64\n"
        "v_mfma_f32_16x16x32_bf16 v[36:39], a[140:143], a[4:7], v[36:39]\n"
        "ds_read_b64_tr_b16 v[138:139], v149 offset:2112\n"
        "v_mfma_f32_16x16x32_bf16 v[36:39], a[132:135], a[0:3], v[36:39]\n"
        "ds_read_b64_tr_b16 v[140:141], v149 offset:96\n"
        "v_mfma_f32_16x16x32_bf16 v[44:47], a[132:135], a[16:19], v[44:47]\n"
        "ds_read_b64_tr_b16 v[142:143], v149 offset:2144\n"
        "v_mfma_f32_16x16x32_bf16 v[44:47], a[140:143], a[20:23], v[44:47]\n"
        "s_waitcnt lgkmcnt(7)\n"
        "v_permlane16_swap_b32 v108, v110\n"
        "v_mfma_f32_16x16x32_bf16 v[44:47], a[148:151], a[24:27], v[44:47]\n"
        "s_waitcnt lgkmcnt(6)\n"
        "v_permlane16_swap_b32 v109, v111\n"
        "v_mfma_f32_16x16x32_bf16 v[44:47], a[156:159], a[28:31], v[44:47]\n"
        "v_lshlrev_b32 v246, 16, v128\n"
        "v_mul_f32 v64, v64, v246\n"
        "v_mfma_f32_16x16x32_bf16 v[40:43], a[152:155], a[28:31], v[40:43]\n"
        "s_waitcnt lgkmcnt(4)\n"
        "v_cvt_pk_bf16_f32 v64, 0, v64\n"
        "v_mul_f32 v64, v64, v228\n"
        "v_mfma_f32_16x16x32_bf16 v[40:43], a[144:147], a[24:27], v[40:43]\n"
        "v_cvt_pk_bf16_f32 v64, 0, v64\n"
        "v_and_b32 v247, s35, v128\n"
        "v_mfma_f32_16x16x32_bf16 v[40:43], a[136:139], a[20:23], v[40:43]\n"
        "v_mul_f32 v65, v65, v247\n"
        "v_cvt_pk_bf16_f32 v65, 0, v65\n"
        "v_mfma_f32_16x16x32_bf16 v[40:43], a[128:131], a[16:19], v[40:43]\n"
        "v_mul_f32 v65, v65, v228\n"
        "v_cvt_pk_bf16_f32 v65, 0, v65\n"
        "v_mfma_f32_16x16x32_bf16 v[48:51], a[128:131], a[192:195], v[48:51]\n"
        "s_barrier\n"
        "v_mfma_f32_16x16x32_bf16 v[48:51], a[136:139], a[196:199], v[48:51]\n"
        "ds_read_b128 a[0:3], v254 offset:32768\n"
        "v_lshlrev_b32 v248, 16, v129\n"
        "v_mfma_f32_16x16x32_bf16 v[48:51], a[144:147], a[200:203], v[48:51]\n"
        "ds_read_b128 a[4:7], v254 offset:36864\n"
        "v_mul_f32 v66, v66, v248\n"
        "v_mfma_f32_16x16x32_bf16 v[48:51], a[152:155], a[204:207], v[48:51]\n"
        "ds_read_b128 a[8:11], v254 offset:40960\n"
        "v_cvt_pk_bf16_f32 v66, 0, v66\n"
        "v_mfma_f32_16x16x32_bf16 v[52:55], a[156:159], a[204:207], v[52:55]\n"
        "ds_read_b128 a[12:15], v254 offset:45056\n"
        "v_mul_f32 v66, v66, v228\n"
        "v_mfma_f32_16x16x32_bf16 v[52:55], a[148:151], a[200:203], v[52:55]\n"
        "ds_read_b128 a[16:19], v254 offset:33792\n"
        "v_cvt_pk_bf16_f32 v66, 0, v66\n"
        "v_mfma_f32_16x16x32_bf16 v[52:55], a[140:143], a[196:199], v[52:55]\n"
        "ds_read_b128 a[20:23], v254 offset:37888\n"
        "v_and_b32 v249, s35, v129\n"
        "v_mfma_f32_16x16x32_bf16 v[52:55], a[132:135], a[192:195], v[52:55]\n"
        "ds_read_b128 a[24:27], v254 offset:41984\n"
        "v_mul_f32 v67, v67, v249\n"
        "v_mfma_f32_16x16x32_bf16 v[60:63], a[132:135], a[208:211], v[60:63]\n"
        "ds_read_b128 a[28:31], v254 offset:46080\n"
        "v_cvt_pk_bf16_f32 v67, 0, v67\n"
        "v_mfma_f32_16x16x32_bf16 v[60:63], a[140:143], a[212:215], v[60:63]\n"
        "ds_read_b128 a[192:195], v254 offset:34816\n"
        "v_mul_f32 v67, v67, v228\n"
        "v_mfma_f32_16x16x32_bf16 v[60:63], a[148:151], a[216:219], v[60:63]\n"
        "ds_read_b128 a[196:199], v254 offset:38912\n"
        "s_waitcnt vmcnt(4)\n"
        "v_mfma_f32_16x16x32_bf16 v[60:63], a[156:159], a[220:223], v[60:63]\n"
        "s_mov_b64 exec, 0xffff\n"
        "global_store_dwordx2 v154, v[250:251], s[48:49] sc1\n"
        "s_mov_b64 exec, -1\n"
        "v_mfma_f32_16x16x32_bf16 v[56:59], a[152:155], a[220:223], v[56:59]\n"
        "ds_read_b128 a[200:203], v254 offset:43008\n"
        "v_cvt_pk_bf16_f32 v67, 0, v67\n"
        "v_mfma_f32_16x16x32_bf16 v[56:59], a[144:147], a[216:219], v[56:59]\n"
        "ds_read_b128 a[204:207], v254 offset:47104\n"
        "v_lshlrev_b32 v144, 16, v130\n"
        "v_mfma_f32_16x16x32_bf16 v[56:59], a[136:139], a[212:215], v[56:59]\n"
        "ds_read_b128 a[212:215], v254 offset:39936\n"
        "v_mul_f32 v68, v68, v144\n"
        "v_mfma_f32_16x16x32_bf16 v[56:59], a[128:131], a[208:211], v[56:59]\n"
        "ds_read_b128 a[208:211], v254 offset:35840\n"
        "s_waitcnt lgkmcnt(7)\n"
        "v_mfma_f32_16x16x32_bf16 v[32:35], v[160:163], a[0:3], v[32:35]\n"
        "ds_read_b128 a[216:219], v254 offset:44032\n"
        "v_cvt_pk_bf16_f32 v68, 0, v68\n"
        "v_mfma_f32_16x16x32_bf16 v[32:35], v[168:171], a[4:7], v[32:35]\n"
        "ds_read_b128 a[220:223], v254 offset:48128\n"
        "v_mul_f32 v68, v68, v228\n"
        "v_mfma_f32_16x16x32_bf16 v[32:35], v[176:179], a[8:11], v[32:35]\n"
        "v_cvt_pk_bf16_f32 v68, 0, v68\n"
        "v_and_b32 v145, s35, v130\n"
        "v_mfma_f32_16x16x32_bf16 v[32:35], v[184:187], a[12:15], v[32:35]\n"
        "v_mul_f32 v69, v69, v145\n"
        "v_cvt_pk_bf16_f32 v69, 0, v69\n"
        "v_mfma_f32_16x16x32_bf16 v[36:39], v[188:191], a[12:15], v[36:39]\n"
        "v_mul_f32 v69, v69, v228\n"
        "v_cvt_pk_bf16_f32 v69, 0, v69\n"
        "v_mfma_f32_16x16x32_bf16 v[36:39], v[180:183], a[8:11], v[36:39]\n"
        "v_lshlrev_b32 v146, 16, v131\n"
        "v_mul_f32 v70, v70, v146\n"
        "v_mfma_f32_16x16x32_bf16 v[36:39], v[172:175], a[4:7], v[36:39]\n"
        "s_waitcnt lgkmcnt(3)\n"
        "v_cvt_pk_bf16_f32 v70, 0, v70\n"
        "v_mul_f32 v70, v70, v228\n"
        "v_mfma_f32_16x16x32_bf16 v[36:39], v[164:167], a[0:3], v[36:39]\n"
        "v_cvt_pk_bf16_f32 v70, 0, v70\n"
        "v_and_b32 v147, s35, v131\n"
        "v_mfma_f32_16x16x32_bf16 v[44:47], v[164:167], a[16:19], v[44:47]\n"
        "v_mul_f32 v71, v71, v147\n"
        "v_cvt_pk_bf16_f32 v71, 0, v71\n"
        "v_mfma_f32_16x16x32_bf16 v[44:47], v[172:175], a[20:23], v[44:47]\n"
        "v_mul_f32 v71, v71, v228\n"
        "v_cvt_pk_bf16_f32 v71, 0, v71\n"
        "v_mfma_f32_16x16x32_bf16 v[40:43], v[184:187], a[28:31], v[40:43]\n"
        "v_lshlrev_b32 v246, 16, v132\n"
        "v_and_b32 v247, s35, v132\n"
        "v_mfma_f32_16x16x32_bf16 v[40:43], v[176:179], a[24:27], v[40:43]\n"
        "v_lshlrev_b32 v248, 16, v133\n"
        "v_and_b32 v249, s35, v133\n"
        "v_mfma_f32_16x16x32_bf16 v[48:51], v[160:163], a[192:195], v[48:51]\n"
        "v_mul_f32 v72, v72, v246\n"
        "v_mul_f32 v73, v73, v247\n"
        "v_mfma_f32_16x16x32_bf16 v[40:43], v[168:171], a[20:23], v[40:43]\n"
        "v_mul_f32 v74, v74, v248\n"
        "v_mul_f32 v75, v75, v249\n"
        "v_mfma_f32_16x16x32_bf16 v[48:51], v[168:171], a[196:199], v[48:51]\n"
        "v_cvt_pk_bf16_f32 v72, 0, v72\n"
        "v_cvt_pk_bf16_f32 v73, 0, v73\n"
        "v_mfma_f32_16x16x32_bf16 v[40:43], v[160:163], a[16:19], v[40:43]\n"
        "v_cvt_pk_bf16_f32 v74, 0, v74\n"
        "v_cvt_pk_bf16_f32 v75, 0, v75\n"
        "v_mfma_f32_16x16x32_bf16 v[48:51], v[176:179], a[200:203], v[48:51]\n"
        "s_barrier\n"
        "v_mfma_f32_16x16x32_bf16 v[52:55], v[188:191], a[204:207], v[52:55]\n"
        "ds_read_b128 a[0:3], v254 offset:49152\n"
        "v_mul_f32 v72, v72, v229\n"
        "v_mfma_f32_16x16x32_bf16 v[48:51], v[184:187], a[204:207], v[48:51]\n"
        "ds_read_b128 a[204:207], v254 offset:63488\n"
        "v_mul_f32 v73, v73, v229\n"
        "v_mfma_f32_16x16x32_bf16 v[52:55], v[180:183], a[200:203], v[52:55]\n"
        "ds_read_b128 a[200:203], v254 offset:59392\n"
        "v_mul_f32 v74, v74, v229\n"
        "v_mfma_f32_16x16x32_bf16 v[52:55], v[172:175], a[196:199], v[52:55]\n"
        "ds_read_b128 a[196:199], v254 offset:55296\n"
        "v_mul_f32 v75, v75, v229\n"
        "v_mfma_f32_16x16x32_bf16 v[52:55], v[164:167], a[192:195], v[52:55]\n"
        "ds_read_b128 a[192:195], v254 offset:51200\n"
        "v_cvt_pk_bf16_f32 v72, 0, v72\n"
        "v_mfma_f32_16x16x32_bf16 v[44:47], v[180:183], a[24:27], v[44:47]\n"
        "ds_read_b128 a[4:7], v254 offset:53248\n"
        "v_cvt_pk_bf16_f32 v73, 0, v73\n"
        "v_mfma_f32_16x16x32_bf16 v[44:47], v[188:191], a[28:31], v[44:47]\n"
        "ds_read_b128 a[8:11], v254 offset:57344\n"
        "s_waitcnt lgkmcnt(7)\n"
        "v_mfma_f32_16x16x32_bf16 v[60:63], v[164:167], a[208:211], v[60:63]\n"
        "ds_read_b128 a[12:15], v254 offset:61440\n"
        "v_cvt_pk_bf16_f32 v74, 0, v74\n"
        "v_mfma_f32_16x16x32_bf16 v[60:63], v[172:175], a[212:215], v[60:63]\n"
        "ds_read_b128 a[16:19], v254 offset:50176\n"
        "v_cvt_pk_bf16_f32 v75, 0, v75\n"
        "v_mfma_f32_16x16x32_bf16 v[60:63], v[180:183], a[216:219], v[60:63]\n"
        "ds_read_b128 a[20:23], v254 offset:54272\n"
        "s_waitcnt lgkmcnt(8)\n"
        "v_mfma_f32_16x16x32_bf16 v[52:55], v[220:223], a[204:207], v[52:55]\n"
        "ds_read_b128 a[24:27], v254 offset:58368\n"
        "s_waitcnt lgkmcnt(8)\n"
        "v_mfma_f32_16x16x32_bf16 v[52:55], v[212:215], a[200:203], v[52:55]\n"
        "ds_read_b128 a[28:31], v254 offset:62464\n"
        "s_waitcnt lgkmcnt(8)\n"
        "v_mfma_f32_16x16x32_bf16 v[52:55], v[204:207], a[196:199], v[52:55]\n"
        "s_waitcnt lgkmcnt(7)\n"
        "v_lshlrev_b32 v144, 16, v134\n"
        "v_and_b32 v145, s35, v134\n"
        "v_mfma_f32_16x16x32_bf16 v[52:55], v[196:199], a[192:195], v[52:55]\n"
        "global_load_dwordx2 v[156:157], v155, s[48:49] sc1\n"
        "v_lshlrev_b32 v146, 16, v135\n"
        "v_mfma_f32_16x16x32_bf16 v[60:63], v[188:191], a[220:223], v[60:63]\n"
        "v_and_b32 v147, s35, v135\n"
        "v_mul_f32 v76, v76, v144\n"
        "v_mfma_f32_16x16x32_bf16 v[56:59], v[184:187], a[220:223], v[56:59]\n"
        "ds_read_b128 a[220:223], v254 offset:64512\n"
        "v_mul_f32 v77, v77, v145\n"
        "v_mfma_f32_16x16x32_bf16 v[56:59], v[176:179], a[216:219], v[56:59]\n"
        "ds_read_b128 a[216:219], v254 offset:60416\n"
        "v_mul_f32 v78, v78, v146\n"
        "v_mfma_f32_16x16x32_bf16 v[56:59], v[168:171], a[212:215], v[56:59]\n"
        "ds_read_b128 a[212:215], v254 offset:56320\n"
        "v_mul_f32 v79, v79, v147\n"
        "v_mfma_f32_16x16x32_bf16 v[56:59], v[160:163], a[208:211], v[56:59]\n"
        "ds_read_b128 a[208:211], v254 offset:52224\n"
        "v_cvt_pk_bf16_f32 v76, 0, v76\n"
        "v_mfma_f32_16x16x32_bf16 v[32:35], v[192:195], a[0:3], v[32:35]\n"
        "s_waitcnt lgkmcnt(4)\n"
        "v_cvt_pk_bf16_f32 v77, 0, v77\n"
        "v_cvt_pk_bf16_f32 v78, 0, v78\n"
        "v_mfma_f32_16x16x32_bf16 v[32:35], v[200:203], a[4:7], v[32:35]\n"
        "v_cvt_pk_bf16_f32 v79, 0, v79\n"
        "v_mul_f32 v76, v76, v229\n"
        "v_mfma_f32_16x16x32_bf16 v[32:35], v[208:211], a[8:11], v[32:35]\n"
        "v_mul_f32 v77, v77, v229\n"
        "v_mul_f32 v78, v78, v229\n"
        "v_mfma_f32_16x16x32_bf16 v[32:35], v[216:219], a[12:15], v[32:35]\n"
        "v_mul_f32 v79, v79, v229\n"
        "v_cvt_pk_bf16_f32 v76, 0, v76\n"
        "v_mfma_f32_16x16x32_bf16 v[36:39], v[220:223], a[12:15], v[36:39]\n"
        "v_cvt_pk_bf16_f32 v77, 0, v77\n"
        "v_cvt_pk_bf16_f32 v78, 0, v78\n"
        "v_mfma_f32_16x16x32_bf16 v[36:39], v[212:215], a[8:11], v[36:39]\n"
        "v_cvt_pk_bf16_f32 v79, 0, v79\n"
        "v_lshlrev_b32 v246, 16, v136\n"
        "v_mfma_f32_16x16x32_bf16 v[36:39], v[204:207], a[4:7], v[36:39]\n"
        "v_and_b32 v247, s35, v136\n"
        "v_lshlrev_b32 v248, 16, v137\n"
        "v_mfma_f32_16x16x32_bf16 v[36:39], v[196:199], a[0:3], v[36:39]\n"
        "v_and_b32 v249, s35, v137\n"
        "v_mul_f32 v80, v80, v246\n"
        "v_mfma_f32_16x16x32_bf16 v[44:47], v[196:199], a[16:19], v[44:47]\n"
        "v_mul_f32 v81, v81, v247\n"
        "v_mul_f32 v82, v82, v248\n"
        "v_mfma_f32_16x16x32_bf16 v[44:47], v[204:207], a[20:23], v[44:47]\n"
        "v_mul_f32 v83, v83, v249\n"
        "v_cvt_pk_bf16_f32 v80, 0, v80\n"
        "v_mfma_f32_16x16x32_bf16 v[44:47], v[212:215], a[24:27], v[44:47]\n"
        "v_cvt_pk_bf16_f32 v81, 0, v81\n"
        "v_cvt_pk_bf16_f32 v82, 0, v82\n"
        "v_mfma_f32_16x16x32_bf16 v[44:47], v[220:223], a[28:31], v[44:47]\n"
        "v_cvt_pk_bf16_f32 v83, 0, v83\n"
        "v_mul_f32 v80, v80, v230\n"
        "v_mfma_f32_16x16x32_bf16 v[40:43], v[216:219], a[28:31], v[40:43]\n"
        "v_mul_f32 v81, v81, v230\n"
        "v_mul_f32 v82, v82, v230\n"
        "v_mfma_f32_16x16x32_bf16 v[40:43], v[208:211], a[24:27], v[40:43]\n"
        "v_mul_f32 v83, v83, v230\n"
        "v_cvt_pk_bf16_f32 v80, 0, v80\n"
        "v_mfma_f32_16x16x32_bf16 v[40:43], v[200:203], a[20:23], v[40:43]\n"
        "v_cvt_pk_bf16_f32 v81, 0, v81\n"
        "v_cvt_pk_bf16_f32 v82, 0, v82\n"
        "v_mfma_f32_16x16x32_bf16 v[40:43], v[192:195], a[16:19], v[40:43]\n"
        "v_cvt_pk_bf16_f32 v83, 0, v83\n"
        "v_lshlrev_b32 v144, 16, v138\n"
        "v_mfma_f32_16x16x32_bf16 v[48:51], v[192:195], a[192:195], v[48:51]\n"
        "s_barrier\n"
        "v_mfma_f32_16x16x32_bf16 v[48:51], v[200:203], a[196:199], v[48:51]\n"
        "ds_read_b128 a[0:3], v244 offset:32768\n"
        "v_and_b32 v145, s35, v138\n"
        "v_mfma_f32_16x16x32_bf16 v[48:51], v[208:211], a[200:203], v[48:51]\n"
        "ds_read_b128 a[4:7], v244 offset:36864\n"
        "v_lshlrev_b32 v146, 16, v139\n"
        "v_mfma_f32_16x16x32_bf16 v[48:51], v[216:219], a[204:207], v[48:51]\n"
        "ds_read_b128 a[8:11], v244 offset:40960\n"
        "s_waitcnt lgkmcnt(3)\n"
        "v_mfma_f32_16x16x32_bf16 v[60:63], v[196:199], a[208:211], v[60:63]\n"
        "ds_read_b128 a[12:15], v244 offset:45056\n"
        "v_and_b32 v147, s35, v139\n"
        "v_mfma_f32_16x16x32_bf16 v[60:63], v[204:207], a[212:215], v[60:63]\n"
        "ds_read_b128 a[16:19], v244 offset:33792\n"
        "v_mul_f32 v84, v84, v144\n"
        "v_mfma_f32_16x16x32_bf16 v[60:63], v[212:215], a[216:219], v[60:63]\n"
        "ds_read_b128 a[20:23], v244 offset:37888\n"
        "v_mul_f32 v85, v85, v145\n"
        "v_mfma_f32_16x16x32_bf16 v[60:63], v[220:223], a[220:223], v[60:63]\n"
        "ds_read_b128 a[24:27], v244 offset:41984\n"
        "v_mul_f32 v86, v86, v146\n"
        "v_mfma_f32_16x16x32_bf16 v[56:59], v[216:219], a[220:223], v[56:59]\n"
        "ds_read_b128 a[28:31], v244 offset:46080\n"
        "v_mul_f32 v87, v87, v147\n"
        "v_mfma_f32_16x16x32_bf16 v[56:59], v[208:211], a[216:219], v[56:59]\n"
        "ds_read_b128 a[192:195], v244 offset:34816\n"
        "v_cvt_pk_bf16_f32 v84, 0, v84\n"
        "v_mfma_f32_16x16x32_bf16 v[56:59], v[200:203], a[212:215], v[56:59]\n"
        "ds_read_b128 a[196:199], v244 offset:38912\n"
        "v_cvt_pk_bf16_f32 v85, 0, v85\n"
        "v_mfma_f32_16x16x32_bf16 v[56:59], v[192:195], a[208:211], v[56:59]\n"
        "ds_read_b128 a[200:203], v244 offset:43008\n"
        "s_waitcnt lgkmcnt(8)\n"
        "v_mfma_f32_16x16x32_bf16 v[32:35], v[112:115], a[0:3], v[32:35]\n"
        "ds_read_b128 a[204:207], v244 offset:47104\n"
        "v_cvt_pk_bf16_f32 v86, 0, v86\n"
        "v_mfma_f32_16x16x32_bf16 v[32:35], v[120:123], a[4:7], v[32:35]\n"
        "ds_read_b128 a[208:211], v244 offset:35840\n"
        "v_cvt_pk_bf16_f32 v87, 0, v87\n"
        "v_mfma_f32_16x16x32_bf16 v[32:35], a[64:67], a[8:11], v[32:35]\n"
        "ds_read_b128 a[212:215], v244 offset:39936\n"
        "s_waitcnt lgkmcnt(8)\n"
        "v_mfma_f32_16x16x32_bf16 v[32:35], a[72:75], a[12:15], v[32:35]\n"
        "ds_read_b128 a[216:219], v244 offset:44032\n"
        "v_mul_f32 v84, v84, v230\n"
        "v_mfma_f32_16x16x32_bf16 v[36:39], a[76:79], a[12:15], v[36:39]\n"
        "ds_read_b128 a[220:223], v244 offset:48128\n"
        "v_mul_f32 v85, v85, v230\n"
        "v_mfma_f32_16x16x32_bf16 v[36:39], a[68:71], a[8:11], v[36:39]\n"
        "s_waitcnt vmcnt(0) lgkmcnt(7)\n"
        "v_cmp_ne_u64 vcc, v[156:157], v[250:251]\n"
        "v_mul_f32 v86, v86, v230\n"
        "v_mfma_f32_16x16x32_bf16 v[36:39], v[124:127], a[4:7], v[36:39]\n"
        "s_cbranch_vccnz LPPR_%=\n"
        "LBPR_%=:\n"
        "global_load_dwordx4 a[240:243], v254, s[30:31] offset:0 sc1\n"
        "v_mfma_f32_16x16x32_bf16 v[36:39], v[116:119], a[0:3], v[36:39]\n"
        "v_mul_f32 v87, v87, v230\n"
        "v_cvt_pk_bf16_f32 v84, 0, v84\n"
        "v_mfma_f32_16x16x32_bf16 v[44:47], v[116:119], a[16:19], v[44:47]\n"
        "global_load_dwordx4 a[244:247], v254, s[30:31] offset:1024 sc1\n"
        "v_mfma_f32_16x16x32_bf16 v[44:47], v[124:127], a[20:23], v[44:47]\n"
        "v_cvt_pk_bf16_f32 v85, 0, v85\n"
        "v_cvt_pk_bf16_f32 v86, 0, v86\n"
        "v_mfma_f32_16x16x32_bf16 v[44:47], a[68:71], a[24:27], v[44:47]\n"
        "global_load_dwordx4 a[248:251], v254, s[30:31] offset:2048 sc1\n"
        "v_mfma_f32_16x16x32_bf16 v[44:47], a[76:79], a[28:31], v[44:47]\n"
        "LHB3_%=:\n"
        "s_cbranch_execz LHRB_%=\n"
        "v_cvt_pk_bf16_f32 v87, 0, v87\n"
        "v_lshlrev_b32 v88, 16, v108\n"
        "v_mfma_f32_16x16x32_bf16 v[40:43], a[72:75], a[28:31], v[40:43]\n"
        "global_load_dwordx4 a[252:255], v254, s[30:31] offset:3072 sc1\n"
        "v_mfma_f32_16x16x32_bf16 v[40:43], a[64:67], a[24:27], v[40:43]\n"
        "s_add_u32 s30, s38, s16\n"
        "s_addc_u32 s31, s39, 0\n"
        "s_mul_i32 s16, s13, 263168\n"
        "v_mfma_f32_16x16x32_bf16 v[40:43], v[120:123], a[20:23], v[40:43]\n"
        "global_load_dwordx4 v[0:3], v254, s[30:31] offset:0 sc1\n"
        "s_add_u32 s16, s16, s15\n"
        "v_mfma_f32_16x16x32_bf16 v[40:43], v[112:115], a[16:19], v[40:43]\n"
        "s_add_u32 s16, s16, s73\n"
        "s_add_u32 s16, s16, 0x4000000\n"
        "v_mfma_f32_16x16x32_bf16 v[48:51], v[112:115], a[192:195], v[48:51]\n"
        "s_waitcnt lgkmcnt(0)\n"
        "global_load_dwordx4 v[4:7], v254, s[30:31] offset:1024 sc1\n"
        "v_mfma_f32_16x16x32_bf16 v[48:51], v[120:123], a[196:199], v[48:51]\n"
        "s_barrier\n"
        "v_mfma_f32_16x16x32_bf16 v[48:51], a[64:67], a[200:203], v[48:51]\n"
        "ds_read_b128 a[0:3], v244 offset:0\n"
        "global_load_dwordx4 v[8:11], v254, s[30:31] offset:2048 sc1\n"
        "v_mfma_f32_16x16x32_bf16 v[48:51], a[72:75], a[204:207], v[48:51]\n"
        "ds_read_b128 a[4:7], v244 offset:4096\n"
        "v_and_b32 v89, s35, v108\n"
        "v_mfma_f32_16x16x32_bf16 v[52:55], a[76:79], a[204:207], v[52:55]\n"
        "ds_read_b128 a[8:11], v244 offset:8192\n"
        "global_load_dwordx4 v[12:15], v254, s[30:31] offset:3072 sc1\n"
        "v_mfma_f32_16x16x32_bf16 v[52:55], a[68:71], a[200:203], v[52:55]\n"
        "ds_read_b128 a[12:15], v244 offset:12288\n"
        "s_add_u32 s30, s38, s16\n"
        "s_addc_u32 s31, s39, 0\n"
        "v_mfma_f32_16x16x32_bf16 v[52:55], v[124:127], a[196:199], v[52:55]\n"
        "ds_read_b128 a[16:19], v244 offset:1024\n"
        "global_load_dwordx4 v[16:19], v254, s[30:31] offset:0 sc1\n"
        "v_mfma_f32_16x16x32_bf16 v[52:55], v[116:119], a[192:195], v[52:55]\n"
        "ds_read_b128 a[20:23], v244 offset:5120\n"
        "v_lshlrev_b32 v90, 16, v109\n"
        "v_mfma_f32_16x16x32_bf16 v[60:63], v[116:119], a[208:211], v[60:63]\n"
        "ds_read_b128 a[24:27], v244 offset:9216\n"
        "global_load_dwordx4 v[20:23], v254, s[30:31] offset:1024 sc1\n"
        "v_mfma_f32_16x16x32_bf16 v[60:63], v[124:127], a[212:215], v[60:63]\n"
        "ds_read_b128 a[28:31], v244 offset:13312\n"
        "v_and_b32 v91, s35, v109\n"
        "v_mfma_f32_16x16x32_bf16 v[60:63], a[68:71], a[216:219], v[60:63]\n"
        "ds_read_b128 a[192:195], v244 offset:2048\n"
        "global_load_dwordx4 v[24:27], v254, s[30:31] offset:2048 sc1\n"
        "v_mfma_f32_16x16x32_bf16 v[60:63], a[76:79], a[220:223], v[60:63]\n"
        "ds_read_b128 a[196:199], v244 offset:6144\n"
        "v_lshlrev_b32 v246, 16, v140\n"
        "v_mfma_f32_16x16x32_bf16 v[56:59], a[72:75], a[220:223], v[56:59]\n"
        "ds_read_b128 a[200:203], v244 offset:10240\n"
        "global_load_dwordx4 v[28:31], v254, s[30:31] offset:3072 sc1\n"
        "v_mfma_f32_16x16x32_bf16 v[56:59], a[64:67], a[216:219], v[56:59]\n"
        "ds_read_b128 a[204:207], v244 offset:14336\n"
        "v_and_b32 v247, s35, v140\n"
        "v_mfma_f32_16x16x32_bf16 v[56:59], v[120:123], a[212:215], v[56:59]\n"
        "ds_read_b128 a[212:215], v244 offset:7168\n"
        "v_lshlrev_b32 v248, 16, v141\n"
        "v_mfma_f32_16x16x32_bf16 v[56:59], v[112:115], a[208:211], v[56:59]\n"
        "ds_read_b128 a[208:211], v244 offset:3072\n"
        "s_waitcnt lgkmcnt(8)\n"
        "v_mfma_f32_16x16x32_bf16 v[32:35], a[80:83], a[0:3], v[32:35]\n"
        "ds_read_b128 a[216:219], v244 offset:11264\n"
        "v_and_b32 v249, s35, v141\n"
        "v_mfma_f32_16x16x32_bf16 v[32:35], a[88:91], a[4:7], v[32:35]\n"
        "ds_read_b128 a[220:223], v244 offset:15360\n"
        "v_mul_f32 v88, v88, v246\n"
        "v_mfma_f32_16x16x32_bf16 v[32:35], a[160:163], a[8:11], v[32:35]\n"
        "v_mul_f32 v89, v89, v247\n"
        "v_mul_f32 v90, v90, v248\n"
        "v_mfma_f32_16x16x32_bf16 v[32:35], a[168:171], a[12:15], v[32:35]\n"
        "v_mul_f32 v91, v91, v249\n"
        "v_cvt_pk_bf16_f32 v88, 0, v88\n"
        "v_mfma_f32_16x16x32_bf16 v[36:39], a[172:175], a[12:15], v[36:39]\n"
        "v_cvt_pk_bf16_f32 v89, 0, v89\n"
        "v_cvt_pk_bf16_f32 v90, 0, v90\n"
        "v_mfma_f32_16x16x32_bf16 v[36:39], a[164:167], a[8:11], v[36:39]\n"
        "v_cvt_pk_bf16_f32 v91, 0, v91\n"
        "v_mul_f32 v88, v88, v231\n"
        "v_mfma_f32_16x16x32_bf16 v[36:39], a[92:95], a[4:7], v[36:39]\n"
        "s_waitcnt lgkmcnt(3)\n"
        "v_mul_f32 v89, v89, v231\n"
        "v_mul_f32 v90, v90, v231\n"
        "v_mfma_f32_16x16x32_bf16 v[36:39], a[84:87], a[0:3], v[36:39]\n"
        "v_mul_f32 v91, v91, v231\n"
        "v_cvt_pk_bf16_f32 v88, 0, v88\n"
        "v_mfma_f32_16x16x32_bf16 v[44:47], a[84:87], a[16:19], v[44:47]\n"
        "v_cvt_pk_bf16_f32 v89, 0, v89\n"
        "v_cvt_pk_bf16_f32 v90, 0, v90\n"
        "v_mfma_f32_16x16x32_bf16 v[44:47], a[92:95], a[20:23], v[44:47]\n"
        "v_cvt_pk_bf16_f32 v91, 0, v91\n"
        "v_lshlrev_b32 v92, 16, v110\n"
        "v_mfma_f32_16x16x32_bf16 v[44:47], a[164:167], a[24:27], v[44:47]\n"
        "v_and_b32 v93, s35, v110\n"
        "v_lshlrev_b32 v94, 16, v111\n"
        "v_mfma_f32_16x16x32_bf16 v[44:47], a[172:175], a[28:31], v[44:47]\n"
        "v_and_b32 v95, s35, v111\n"
        "v_lshlrev_b32 v144, 16, v142\n"
        "v_mfma_f32_16x16x32_bf16 v[40:43], a[168:171], a[28:31], v[40:43]\n"
        "v_and_b32 v145, s35, v142\n"
        "v_lshlrev_b32 v146, 16, v143\n"
        "v_mfma_f32_16x16x32_bf16 v[40:43], a[160:163], a[24:27], v[40:43]\n"
        "v_and_b32 v147, s35, v143\n"
        "v_mul_f32 v92, v92, v144\n"
        "v_mfma_f32_16x16x32_bf16 v[40:43], a[88:91], a[20:23], v[40:43]\n"
        "v_mul_f32 v93, v93, v145\n"
        "v_mul_f32 v94, v94, v146\n"
        "v_mfma_f32_16x16x32_bf16 v[40:43], a[80:83], a[16:19], v[40:43]\n"
        "v_mul_f32 v95, v95, v147\n"
        "v_cvt_pk_bf16_f32 v92, 0, v92\n"
        "v_mfma_f32_16x16x32_bf16 v[48:51], a[80:83], a[192:195], v[48:51]\n"
        "s_barrier\n"
        "v_mfma_f32_16x16x32_bf16 v[48:51], a[88:91], a[196:199], v[48:51]\n"
        "ds_read_b128 a[0:3], v244 offset:49152\n"
        "v_cvt_pk_bf16_f32 v93, 0, v93\n"
        "v_mfma_f32_16x16x32_bf16 v[48:51], a[160:163], a[200:203], v[48:51]\n"
        "ds_read_b128 a[4:7], v244 offset:53248\n"
        "v_cvt_pk_bf16_f32 v94, 0, v94\n"
        "v_mfma_f32_16x16x32_bf16 v[48:51], a[168:171], a[204:207], v[48:51]\n"
        "ds_read_b128 a[8:11], v244 offset:57344\n"
        "v_cvt_pk_bf16_f32 v95, 0, v95\n"
        "v_mfma_f32_16x16x32_bf16 v[52:55], a[172:175], a[204:207], v[52:55]\n"
        "ds_read_b128 a[12:15], v244 offset:61440\n"
        "v_mul_f32 v92, v92, v231\n"
        "v_mfma_f32_16x16x32_bf16 v[52:55], a[164:167], a[200:203], v[52:55]\n"
        "ds_read_b128 a[200:203], v244 offset:58368\n"
        "v_mul_f32 v93, v93, v231\n"
        "v_mfma_f32_16x16x32_bf16 v[52:55], a[92:95], a[196:199], v[52:55]\n"
        "ds_read_b128 a[196:199], v244 offset:54272\n"
        "v_mul_f32 v94, v94, v231\n"
        "v_mfma_f32_16x16x32_bf16 v[52:55], a[84:87], a[192:195], v[52:55]\n"
        "ds_read_b128 a[192:195], v244 offset:50176\n"
        "s_waitcnt lgkmcnt(7)\n"
        "v_mfma_f32_16x16x32_bf16 v[60:63], a[84:87], a[208:211], v[60:63]\n"
        "ds_read_b128 a[204:207], v244 offset:62464\n"
        "v_mfma_f32_16x16x32_bf16 v[60:63], a[92:95], a[212:215], v[60:63]\n"
        "s_barrier\n"
        "v_mfma_f32_16x16x32_bf16 v[60:63], a[164:167], a[216:219], v[60:63]\n"
        "ds_read_b128 a[16:19], v245 offset:0\n"
        "v_mul_f32 v95, v95, v231\n"
        "v_mfma_f32_16x16x32_bf16 v[60:63], a[172:175], a[220:223], v[60:63]\n"
        "ds_read_b128 a[20:23], v245 offset:4096\n"
        "v_cvt_pk_bf16_f32 v92, 0, v92\n"
        "v_mfma_f32_16x16x32_bf16 v[56:59], a[168:171], a[220:223], v[56:59]\n"
        "ds_read_b128 a[24:27], v245 offset:8192\n"
        "v_cvt_pk_bf16_f32 v93, 0, v93\n"
        "v_mfma_f32_16x16x32_bf16 v[56:59], a[160:163], a[216:219], v[56:59]\n"
        "ds_read_b128 a[28:31], v245 offset:12288\n"
        "v_cvt_pk_bf16_f32 v94, 0, v94\n"
        "v_mfma_f32_16x16x32_bf16 v[56:59], a[88:91], a[212:215], v[56:59]\n"
        "ds_read_b128 a[212:215], v245 offset:5120\n"
        "v_cvt_pk_bf16_f32 v95, 0, v95\n"
        "v_mfma_f32_16x16x32_bf16 v[56:59], a[80:83], a[208:211], v[56:59]\n"
        "ds_read_b128 a[208:211], v245 offset:1024\n"
        "s_waitcnt lgkmcnt(7)\n"
        "v_mfma_f32_16x16x32_bf16 v[32:35], a[176:179], a[0:3], v[32:35]\n"
        "ds_read_b128 a[216:219], v245 offset:9216\n"
        "v_mov_b32 v144, 0\n"
        "v_mfma_f32_16x16x32_bf16 v[32:35], a[184:187], a[4:7], v[32:35]\n"
        "ds_read_b128 a[220:223], v245 offset:13312\n"
        "v_mfma_f32_16x16x32_bf16 v[32:35], a[32:35], a[8:11], v[32:35]\n"
        "v_mfma_f32_16x16x32_bf16 v[32:35], a[40:43], a[12:15], v[32:35]\n"
        "v_mfma_f32_16x16x32_bf16 v[36:39], a[44:47], a[12:15], v[36:39]\n"
        "ds_read_b128 a[12:15], v244 offset:63488\n"
        "s_waitcnt lgkmcnt(6)\n"
        "v_mfma_f32_16x16x32_bf16 v[36:39], a[36:39], a[8:11], v[36:39]\n"
        "ds_read_b128 a[8:11], v244 offset:59392\n"
        "v_mfma_f32_16x16x32_bf16 v[36:39], a[188:191], a[4:7], v[36:39]\n"
        "ds_read_b128 a[4:7], v244 offset:55296\n"
        "v_mfma_f32_16x16x32_bf16 v[36:39], a[180:183], a[0:3], v[36:39]\n"
        "ds_read_b128 a[0:3], v244 offset:51200\n"
        "s_waitcnt lgkmcnt(6)\n"
        "v_mfma_f32_16x16x32_bf16 v[32:35], a[48:51], a[16:19], v[32:35]\n"
        "v_mfma_f32_16x16x32_bf16 v[32:35], a[56:59], a[20:23], v[32:35]\n"
        "v_mfma_f32_16x16x32_bf16 v[32:35], a[224:227], a[24:27], v[32:35]\n"
        "v_mfma_f32_16x16x32_bf16 v[32:35], a[232:235], a[28:31], v[32:35]\n"
        "v_mfma_f32_16x16x32_bf16 v[36:39], a[236:239], a[28:31], v[36:39]\n"
        "ds_read_b128 a[28:31], v245 offset:14336\n"
        "v_mfma_f32_16x16x32_bf16 v[36:39], a[228:231], a[24:27], v[36:39]\n"
        "ds_read_b128 a[24:27], v245 offset:10240\n"
        "v_mfma_f32_16x16x32_bf16 v[36:39], a[60:63], a[20:23], v[36:39]\n"
        "ds_read_b128 a[20:23], v245 offset:6144\n"
        "v_mfma_f32_16x16x32_bf16 v[36:39], a[52:55], a[16:19], v[36:39]\n"
        "ds_read_b128 a[16:19], v245 offset:2048\n"
        "s_waitcnt vmcnt(11) lgkmcnt(4)\n"
        "v_mfma_f32_16x16x32_bf16 v[32:35], v[240:243], a[240:243], v[32:35]\n"
        "v_mfma_f32_16x16x32_bf16 v[36:39], v[238:241], a[240:243], v[36:39]\n"
        "s_waitcnt vmcnt(7)\n"
        "v_mfma_f32_16x16x32_bf16 v[32:35], v[240:243], v[0:3], v[32:35]\n"
        "v_mfma_f32_16x16x32_bf16 v[36:39], v[238:241], v[0:3], v[36:39]\n"
        "v_mfma_f32_16x16x32_bf16 v[40:43], a[176:179], a[192:195], v[40:43]\n"
        "s_waitcnt vmcnt(3) lgkmcnt(3)\n"
        "v_mfma_f32_16x16x32_bf16 v[32:35], v[240:243], v[16:19], v[32:35]\n"
        "v_mfma_f32_16x16x32_bf16 v[36:39], v[238:241], v[16:19], v[36:39]\n"
        "v_mfma_f32_16x16x32_bf16 v[40:43], a[184:187], a[196:199], v[40:43]\n"
        "v_mfma_f32_16x16x32_bf16 v[40:43], a[32:35], a[200:203], v[40:43]\n"
        "v_mfma_f32_16x16x32_bf16 v[40:43], a[40:43], a[204:207], v[40:43]\n"
        "v_mfma_f32_16x16x32_bf16 v[44:47], a[44:47], a[204:207], v[44:47]\n"
        "ds_read_b128 a[204:207], v244 offset:64512\n"
        "v_mfma_f32_16x16x32_bf16 v[44:47], a[36:39], a[200:203], v[44:47]\n"
        "ds_read_b128 a[200:203], v244 offset:60416\n"
        "v_fmac_f32 v64, v32, v224\n"
        "v_mfma_f32_16x16x32_bf16 v[44:47], a[188:191], a[196:199], v[44:47]\n"
        "ds_read_b128 a[196:199], v244 offset:56320\n"
        "v_fmac_f32 v65, v33, v224\n"
        "v_mfma_f32_16x16x32_bf16 v[44:47], a[180:183], a[192:195], v[44:47]\n"
        "ds_read_b128 a[192:195], v244 offset:52224\n"
        "v_fmac_f32 v66, v34, v224\n"
        "v_mfma_f32_16x16x32_bf16 v[40:43], a[48:51], a[208:211], v[40:43]\n"
        "v_fmac_f32 v67, v35, v224\n"
        "v_fmac_f32 v68, v36, v224\n"
        "v_mfma_f32_16x16x32_bf16 v[40:43], a[56:59], a[212:215], v[40:43]\n"
        "v_fmac_f32 v69, v37, v224\n"
        "v_fmac_f32 v70, v38, v224\n"
        "v_mfma_f32_16x16x32_bf16 v[40:43], a[224:227], a[216:219], v[40:43]\n"
        "v_fmac_f32 v71, v39, v224\n"
        "v_cvt_pk_bf16_f32 v64, v64, v65\n"
        "v_mfma_f32_16x16x32_bf16 v[40:43], a[232:235], a[220:223], v[40:43]\n"
        "v_cvt_pk_bf16_f32 v65, v66, v67\n"
        "v_cvt_pk_bf16_f32 v66, v68, v69\n"
        "v_mfma_f32_16x16x32_bf16 v[44:47], a[236:239], a[220:223], v[44:47]\n"
        "ds_read_b128 a[220:223], v245 offset:15360\n"
        "v_cvt_pk_bf16_f32 v67, v70, v71\n"
        "v_mfma_f32_16x16x32_bf16 v[44:47], a[228:231], a[216:219], v[44:47]\n"
        "v_permlane16_swap_b32 v64, v66\n"
        "v_mfma_f32_16x16x32_bf16 v[44:47], a[60:63], a[212:215], v[44:47]\n"
        "v_permlane16_swap_b32 v65, v67\n"
        "v_mfma_f32_16x16x32_bf16 v[44:47], a[52:55], a[208:211], v[44:47]\n"
        "global_store_dwordx4 v151, v[64:67], s[56:57] sc0 sc1\n"
        "ds_read_b128 a[208:211], v245 offset:3072\n"
        "s_add_u32 s56, s56, 0x8000\n"
        "s_addc_u32 s57, s57, 0\n"
        "ds_read_b128 a[212:215], v245 offset:7168\n"
        "v_mfma_f32_16x16x32_bf16 v[40:43], v[240:243], a[244:247], v[40:43]\n"
        "ds_read_b128 a[216:219], v245 offset:11264\n"
        "v_mfma_f32_16x16x32_bf16 v[44:47], v[238:241], a[244:247], v[44:47]\n"
        "v_mfma_f32_16x16x32_bf16 v[40:43], v[240:243], v[4:7], v[40:43]\n"
        "v_mfma_f32_16x16x32_bf16 v[44:47], v[238:241], v[4:7], v[44:47]\n"
        "s_waitcnt vmcnt(3) lgkmcnt(4)\n"
        "v_mfma_f32_16x16x32_bf16 v[40:43], v[240:243], v[20:23], v[40:43]\n"
        "v_mfma_f32_16x16x32_bf16 v[44:47], v[238:241], v[20:23], v[44:47]\n"
        "v_mfma_f32_16x16x32_bf16 v[48:51], a[176:179], a[0:3], v[48:51]\n"
        "v_mfma_f32_16x16x32_bf16 v[48:51], a[184:187], a[4:7], v[48:51]\n"
        "v_mfma_f32_16x16x32_bf16 v[48:51], a[32:35], a[8:11], v[48:51]\n"
        "v_mfma_f32_16x16x32_bf16 v[48:51], a[40:43], a[12:15], v[48:51]\n"
        "v_mfma_f32_16x16x32_bf16 v[52:55], a[44:47], a[12:15], v[52:55]\n"
        "v_mfma_f32_16x16x32_bf16 v[52:55], a[36:39], a[8:11], v[52:55]\n"
        "v_mfma_f32_16x16x32_bf16 v[52:55], a[188:191], a[4:7], v[52:55]\n"
        "v_fmac_f32 v72, v40, v225\n"
        "v_fmac_f32 v73, v41, v225\n"
        "v_mfma_f32_16x16x32_bf16 v[52:55], a[180:183], a[0:3], v[52:55]\n"
        "v_fmac_f32 v74, v42, v225\n"
        "v_fmac_f32 v75, v43, v225\n"
        "v_mfma_f32_16x16x32_bf16 v[48:51], a[48:51], a[16:19], v[48:51]\n"
        "v_fmac_f32 v76, v44, v225\n"
        "v_fmac_f32 v77, v45, v225\n"
        "v_mfma_f32_16x16x32_bf16 v[48:51], a[56:59], a[20:23], v[48:51]\n"
        "v_fmac_f32 v78, v46, v225\n"
        "v_fmac_f32 v79, v47, v225\n"
        "v_mfma_f32_16x16x32_bf16 v[48:51], a[224:227], a[24:27], v[48:51]\n"
        "v_cvt_pk_bf16_f32 v72, v72, v73\n"
        "v_cvt_pk_bf16_f32 v73, v74, v75\n"
        "v_mfma_f32_16x16x32_bf16 v[48:51], a[232:235], a[28:31], v[48:51]\n"
        "v_cvt_pk_bf16_f32 v74, v76, v77\n"
        "v_cvt_pk_bf16_f32 v75, v78, v79\n"
        "v_mfma_f32_16x16x32_bf16 v[52:55], a[236:239], a[28:31], v[52:55]\n"
        "v_permlane16_swap_b32 v72, v74\n"
        "v_mfma_f32_16x16x32_bf16 v[52:55], a[228:231], a[24:27], v[52:55]\n"
        "v_permlane16_swap_b32 v73, v75\n"
        "v_mfma_f32_16x16x32_bf16 v[52:55], a[60:63], a[20:23], v[52:55]\n"
        "global_store_dwordx4 v151, v[72:75], s[56:57] sc0 sc1\n"
        "s_add_u32 s56, s56, 0x8000\n"
        "s_addc_u32 s57, s57, 0\n"
        "v_mfma_f32_16x16x32_bf16 v[52:55], a[52:55], a[16:19], v[52:55]\n"
        "v_mfma_f32_16x16x32_bf16 v[48:51], v[240:243], a[248:251], v[48:51]\n"
        "v_mfma_f32_16x16x32_bf16 v[52:55], v[238:241], a[248:251], v[52:55]\n"
        "v_mfma_f32_16x16x32_bf16 v[48:51], v[240:243], v[8:11], v[48:51]\n"
        "v_mfma_f32_16x16x32_bf16 v[52:55], v[238:241], v[8:11], v[52:55]\n"
        "s_waitcnt vmcnt(3) lgkmcnt(0)\n"
        "v_mfma_f32_16x16x32_bf16 v[48:51], v[240:243], v[24:27], v[48:51]\n"
        "v_mfma_f32_16x16x32_bf16 v[52:55], v[238:241], v[24:27], v[52:55]\n"
        "v_mfma_f32_16x16x32_bf16 v[56:59], a[176:179], a[192:195], v[56:59]\n"
        "v_mfma_f32_16x16x32_bf16 v[56:59], a[184:187], a[196:199], v[56:59]\n"
        "v_mfma_f32_16x16x32_bf16 v[56:59], a[32:35], a[200:203], v[56:59]\n"
        "v_mfma_f32_16x16x32_bf16 v[56:59], a[40:43], a[204:207], v[56:59]\n"
        "v_mfma_f32_16x16x32_bf16 v[60:63], a[44:47], a[204:207], v[60:63]\n"
        "v_mfma_f32_16x16x32_bf16 v[60:63], a[36:39], a[200:203], v[60:63]\n"
        "v_mfma_f32_16x16x32_bf16 v[60:63], a[188:191], a[196:199], v[60:63]\n"
        "v_fmac_f32 v80, v48, v226\n"
        "v_fmac_f32 v81, v49, v226\n"
        "v_mfma_f32_16x16x32_bf16 v[60:63], a[180:183], a[192:195], v[60:63]\n"
        "v_fmac_f32 v82, v50, v226\n"
        "v_fmac_f32 v83, v51, v226\n"
        "v_mfma_f32_16x16x32_bf16 v[56:59], a[48:51], a[208:211], v[56:59]\n"
        "v_fmac_f32 v84, v52, v226\n"
        "v_fmac_f32 v85, v53, v226\n"
        "v_mfma_f32_16x16x32_bf16 v[56:59], a[56:59], a[212:215], v[56:59]\n"
        "v_fmac_f32 v86, v54, v226\n"
        "v_fmac_f32 v87, v55, v226\n"
        "v_mfma_f32_16x16x32_bf16 v[56:59], a[224:227], a[216:219], v[56:59]\n"
        "v_cvt_pk_bf16_f32 v80, v80, v81\n"
        "v_cvt_pk_bf16_f32 v81, v82, v83\n"
        "v_mfma_f32_16x16x32_bf16 v[56:59], a[232:235], a[220:223], v[56:59]\n"
        "v_cvt_pk_bf16_f32 v82, v84, v85\n"
        "v_cvt_pk_bf16_f32 v83, v86, v87\n"
        "v_mfma_f32_16x16x32_bf16 v[60:63], a[236:239], a[220:223], v[60:63]\n"
        "v_permlane16_swap_b32 v80, v82\n"
        "v_mfma_f32_16x16x32_bf16 v[60:63], a[228:231], a[216:219], v[60:63]\n"
        "v_permlane16_swap_b32 v81, v83\n"
        "v_mfma_f32_16x16x32_bf16 v[60:63], a[60:63], a[212:215], v[60:63]\n"
        "global_store_dwordx4 v151, v[80:83], s[56:57] sc0 sc1\n"
        "s_add_u32 s56, s56, 0x8000\n"
        "s_addc_u32 s57, s57, 0\n"
        "v_mfma_f32_16x16x32_bf16 v[60:63], a[52:55], a[208:211], v[60:63]\n"
        "v_mfma_f32_16x16x32_bf16 v[56:59], v[240:243], a[252:255], v[56:59]\n"
        "v_mfma_f32_16x16x32_bf16 v[60:63], v[238:241], a[252:255], v[60:63]\n"
        "v_mfma_f32_16x16x32_bf16 v[56:59], v[240:243], v[12:15], v[56:59]\n"
        "v_mfma_f32_16x16x32_bf16 v[60:63], v[238:241], v[12:15], v[60:63]\n"
        "s_waitcnt vmcnt(3)\n"
        "v_mfma_f32_16x16x32_bf16 v[56:59], v[240:243], v[28:31], v[56:59]\n"
        "v_mfma_f32_16x16x32_bf16 v[60:63], v[238:241], v[28:31], v[60:63]\n"
        "s_nop 6\n"
        "v_fmac_f32 v88, v56, v227\n"
        "v_fmac_f32 v89, v57, v227\n"
        "v_fmac_f32 v90, v58, v227\n"
        "v_fmac_f32 v91, v59, v227\n"
        "v_fmac_f32 v92, v60, v227\n"
        "v_fmac_f32 v93, v61, v227\n"
        "v_fmac_f32 v94, v62, v227\n"
        "v_fmac_f32 v95, v63, v227\n"
        "v_cvt_pk_bf16_f32 v88, v88, v89\n"
        "v_cvt_pk_bf16_f32 v89, v90, v91\n"
        "v_cvt_pk_bf16_f32 v90, v92, v93\n"
        "v_cvt_pk_bf16_f32 v91, v94, v95\n"
        "s_nop 0\n"
        "v_permlane16_swap_b32 v88, v90\n"
        "v_permlane16_swap_b32 v89, v91\n"
        "global_store_dwordx4 v151, v[88:91], s[56:57] sc0 sc1\n"
        "s_or_b32 s97, s55, s40\n"
        "s_cbranch_scc1 LEB_%=\n"
        "s_mov_b64 exec, 1\n"
        "global_store_dwordx2 v144, v[250:251], s[48:49] sc1\n"
        "s_mov_b64 exec, -1\n"
        "LEB_%=:\n"
        "; ---- END\n"
        "s_endpgm\n"
        "LPKV_%=:\n"
        "s_mov_b32 s97, 0x200000\n"
        "LQKV_%=:\n"
        "global_load_dwordx2 v[252:253], v245, s[48:49] sc1\n"
        "s_waitcnt vmcnt(0)\n"
        "v_cmp_ne_u64 vcc, v[252:253], v[250:251]\n"
        "s_nop 1\n"
        "s_cbranch_vccz LBKV_%=\n"
        "s_sleep 2\n"
        "s_sub_u32 s97, s97, 1\n"
        "s_cmp_lg_u32 s97, 0\n"
        "s_cbranch_scc1 LQKV_%=\n"
        "s_branch LBKV_%=\n"
        "LE4D_%=:\n"
        "s_mov_b32 s97, 0x200000\n"
        "LE5D_%=:\n"
        "s_sleep 2\n"
        "global_load_dwordx2 v[252:253], v155, s[48:49] sc1\n"
        "s_waitcnt vmcnt(0)\n"
        "v_cmp_ne_u64 vcc, v[252:253], v[250:251]\n"
        "s_nop 1\n"
        "s_cbranch_vccz LEWD_%=\n"
        "s_sub_u32 s97, s97, 1\n"
        "s_cmp_lg_u32 s97, 0\n"
        "s_cbranch_scc1 LE5D_%=\n"
        "s_branch LEWD_%=\n"
        "LRL_%=:\n"
        "s_waitcnt vmcnt(0) lgkmcnt(0)\n"
        "s_nop 7\n"
        "v_accvgpr_write_b32 a176, v96\n"
        "v_accvgpr_write_b32 a177, v97\n"
        "v_accvgpr_write_b32 a178, v98\n"
        "v_accvgpr_write_b32 a179, v99\n"
        "v_accvgpr_write_b32 a180, v100\n"
        "v_accvgpr_write_b32 a181, v101\n"
        "v_accvgpr_write_b32 a182, v102\n"
        "v_accvgpr_write_b32 a183, v103\n"
        "v_accvgpr_write_b32 a184, v104\n"
        "v_accvgpr_write_b32 a185, v105\n"
        "v_accvgpr_write_b32 a186, v106\n"
        "v_accvgpr_write_b32 a187, v107\n"
        "v_accvgpr_write_b32 a188, v108\n"
        "v_accvgpr_write_b32 a189, v109\n"
        "v_accvgpr_write_b32 a190, v110\n"
        "v_accvgpr_write_b32 a191, v111\n"
        "s_mov_b32 s53, s44\n"
        "s_mov_b32 s96, 64\n"
        "s_mov_b32 s52, 0\n"
        "s_branch LREDO_%=\n"
        "LRET0_%=:\n"
        "v_accvgpr_write_b32 a176, v128\n"
        "v_accvgpr_write_b32 a177, v129\n"
        "v_accvgpr_write_b32 a178, v130\n"
        "v_accvgpr_write_b32 a179, v131\n"
        "v_accvgpr_write_b32 a180, v132\n"
        "v_accvgpr_write_b32 a181, v133\n"
        "v_accvgpr_write_b32 a182, v134\n"
        "v_accvgpr_write_b32 a183, v135\n"
        "v_accvgpr_write_b32 a184, v136\n"
        "v_accvgpr_write_b32 a185, v137\n"
        "v_accvgpr_write_b32 a186, v138\n"
        "v_accvgpr_write_b32 a187, v139\n"
        "v_accvgpr_write_b32 a188, v140\n"
        "v_accvgpr_write_b32 a189, v141\n"
        "v_accvgpr_write_b32 a190, v142\n"
        "v_accvgpr_write_b32 a191, v143\n"
        "s_mov_b32 s53, s44\n"
        "s_mov_b32 s96, 0\n"
        "s_mov_b32 s52, 1\n"
        "s_branch LREDO_%=\n"
        "LRET1_%=:\n"
        "s_branch LSL_%=\n"
        "LE4S_%=:\n"
        "s_mov_b32 s97, 0x200000\n"
        "LE5S_%=:\n"
        "s_sleep 2\n"
        "global_load_dwordx2 v[252:253], v155, s[48:49] sc1\n"
        "s_waitcnt vmcnt(0)\n"
        "v_cmp_ne_u64 vcc, v[252:253], v[250:251]\n"
        "s_nop 1\n"
        "s_cbranch_vccz LEWS_%=\n"
        "s_sub_u32 s97, s97, 1\n"
        "s_cmp_lg_u32 s97, 0\n"
        "s_cbranch_scc1 LE5S_%=\n"
        "s_branch LEWS_%=\n"
        "LRH_%=:\n"
        "s_waitcnt vmcnt(0) lgkmcnt(0)\n"
        "s_nop 7\n"
        "v_accvgpr_write_b32 a176, v0\n"
        "v_accvgpr_write_b32 a177, v1\n"
        "v_accvgpr_write_b32 a178, v2\n"
        "v_accvgpr_write_b32 a179, v3\n"
        "v_accvgpr_write_b32 a180, v4\n"
        "v_accvgpr_write_b32 a181, v5\n"
        "v_accvgpr_write_b32 a182, v6\n"
        "v_accvgpr_write_b32 a183, v7\n"
        "v_accvgpr_write_b32 a184, v8\n"
        "v_accvgpr_write_b32 a185, v9\n"
        "v_accvgpr_write_b32 a186, v10\n"
        "v_accvgpr_write_b32 a187, v11\n"
        "v_accvgpr_write_b32 a188, v12\n"
        "v_accvgpr_write_b32 a189, v13\n"
        "v_accvgpr_write_b32 a190, v14\n"
        "v_accvgpr_write_b32 a191, v15\n"
        "s_mov_b32 s53, s45\n"
        "s_mov_b32 s96, 64\n"
        "s_mov_b32 s52, 2\n"
        "s_branch LREDO_%=\n"
        "LRET2_%=:\n"
        "v_accvgpr_write_b32 a176, v64\n"
        "v_accvgpr_write_b32 a177, v65\n"
        "v_accvgpr_write_b32 a178, v66\n"
        "v_accvgpr_write_b32 a179, v67\n"
        "v_accvgpr_write_b32 a180, v68\n"
        "v_accvgpr_write_b32 a181, v69\n"
        "v_accvgpr_write_b32 a182, v70\n"
        "v_accvgpr_write_b32 a183, v71\n"
        "v_accvgpr_write_b32 a184, v72\n"
        "v_accvgpr_write_b32 a185, v73\n"
        "v_accvgpr_write_b32 a186, v74\n"
        "v_accvgpr_write_b32 a187, v75\n"
        "v_accvgpr_write_b32 a188, v76\n"
        "v_accvgpr_write_b32 a189, v77\n"
        "v_accvgpr_write_b32 a190, v78\n"
        "v_accvgpr_write_b32 a191, v79\n"
        "s_mov_b32 s53, s45\n"
        "s_mov_b32 s96, 0\n"
        "s_mov_b32 s52, 3\n"
        "s_branch LREDO_%=\n"
        "LRET3_%=:\n"
        "s_branch LSH_%=\n"
        "LPCX_%=:\n"
        "s_mov_b32 s97, 0x200000\n"
        "LQCX_%=:\n"
        "global_load_dwordx2 v[236:237], v159, s[48:49] sc1\n"
        "s_waitcnt vmcnt(0)\n"
        "v_cmp_ne_u64 vcc, v[236:237], v[250:251]\n"
        "s_nop 1\n"
        "s_cbranch_vccz LBCX_%=\n"
        "s_sleep 2\n"
        "s_sub_u32 s97, s97, 1\n"
        "s_cmp_lg_u32 s97, 0\n"
        "s_cbranch_scc1 LQCX_%=\n"
        "s_branch LBCX_%=\n"
        "LPZZ_%=:\n"
        "s_mov_b32 s97, 0x200000\n"
        "LQZZ_%=:\n"
        "global_load_dwordx2 v[236:237], v234, s[48:49] sc1\n"
        "s_waitcnt vmcnt(0)\n"
        "v_cmp_ne_u64 vcc, v[236:237], v[250:251]\n"
        "s_nop 1\n"
        "s_cbranch_vccz LBZZ_%=\n"
        "s_sleep 2\n"
        "s_sub_u32 s97, s97, 1\n"
        "s_cmp_lg_u32 s97, 0\n"
        "s_cbranch_scc1 LQZZ_%=\n"
        "s_branch LBZZ_%=\n"
        "LPPR_%=:\n"
        "s_mov_b32 s97, 0x200000\n"
        "LQPR_%=:\n"
        "global_load_dwordx2 v[156:157], v155, s[48:49] sc1\n"
        "s_waitcnt vmcnt(0)\n"
        "v_cmp_ne_u64 vcc, v[156:157], v[250:251]\n"
        "s_nop 1\n"
        "s_cbranch_vccz LBPR_%=\n"
        "s_sleep 2\n"
        "s_sub_u32 s97, s97, 1\n"
        "s_cmp_lg_u32 s97, 0\n"
        "s_cbranch_scc1 LQPR_%=\n"
        "s_branch LBPR_%=\n"
        "LREDO_%=:\n"
        "s_lshl2_add_u32 s97, s55, s40\n"
        "s_lshl_b32 s97, s97, 12\n"
        "s_add_u32 s97, s97, 0x5100000\n"
        "s_add_u32 s88, s38, s97\n"
        "s_addc_u32 s89, s39, 0\n"
        "global_store_dwordx4 v254, v[144:147], s[88:89] offset:0\n"
        "global_store_dwordx4 v254, v[148:151], s[88:89] offset:1024\n"
        "global_store_dwordx4 v254, v[152:155], s[88:89] offset:2048\n"
        "global_store_dwordx4 v254, v[156:159], s[88:89] offset:3072\n"
        "s_waitcnt vmcnt(0)\n"
        "v_mov_b32 v112, 0\n"
        "v_mov_b32 v113, 0\n"
        "v_mov_b32 v114, 0\n"
        "v_mov_b32 v115, 0\n"
        "v_mov_b32 v116, 0\n"
        "v_mov_b32 v117, 0\n"
        "v_mov_b32 v118, 0\n"
        "v_mov_b32 v119, 0\n"
        "v_mov_b32 v120, 0\n"
        "v_mov_b32 v121, 0\n"
        "v_mov_b32 v122, 0\n"
        "v_mov_b32 v123, 0\n"
        "v_mov_b32 v124, 0\n"
        "v_mov_b32 v125, 0\n"
        "v_mov_b32 v126, 0\n"
        "v_mov_b32 v127, 0\n"
        "v_mov_b32 v144, 0\n"
        "v_mov_b32 v145, 0\n"
        "v_mov_b32 v146, 0\n"
        "v_mov_b32 v147, 0\n"
        "v_mov_b32 v148, 0\n"
        "v_mov_b32 v149, 0\n"
        "v_mov_b32 v150, 0\n"
        "v_mov_b32 v151, 0\n"
        "v_mov_b32 v152, 0\n"
        "v_mov_b32 v153, 0\n"
        "v_mov_b32 v154, 0\n"
        "v_mov_b32 v155, 0\n"
        "v_mov_b32 v156, 0\n"
        "v_mov_b32 v157, 0\n"
        "v_mov_b32 v158, 0\n"
        "v_mov_b32 v159, 0\n"
        "v_mov_b32 v60, 0\n"
        "v_mov_b32 v61, 0\n"
        "v_mov_b32 v62, 0\n"
        "v_mov_b32 v63, 0\n"
        "v_mov_b32 v24, 0xf149f2ca\n"
        "s_lshl_b32 s97, s53, 7\n"
        "s_add_u32 s97, s97, s96\n"
        "v_add_u32 v27, s97, v243\n"
        "s_mul_i32 s97, s43, 529408\n"
        "s_add_u32 s84, s38, s97\n"
        "s_addc_u32 s85, s39, 0\n"
        "s_add_u32 s84, s84, 0x1000\n"
        "s_addc_u32 s85, s85, 0\n"
        "s_add_u32 s86, s84, 0x1100000\n"
        "s_addc_u32 s87, s85, 0\n"
        "s_lshl_b32 s97, s53, 2\n"
        "s_add_u32 s97, s97, 4\n"
        "LRLOOP_%=:\n"
        "global_load_dwordx4 a[160:163], v254, s[84:85] offset:-4096 sc1\n"
        "global_load_dwordx4 a[164:167], v254, s[84:85] offset:-3072 sc1\n"
        "global_load_dwordx4 a[168:171], v254, s[84:85] offset:-2048 sc1\n"
        "global_load_dwordx4 a[172:175], v254, s[84:85] offset:-1024 sc1\n"
        "s_waitcnt vmcnt(0)\n"
        "v_mfma_f32_16x16x32_bf16 v[40:43], a[160:163], a[176:179], 0\n"
        "v_mfma_f32_16x16x32_bf16 v[40:43], a[164:167], a[180:183], v[40:43]\n"
        "v_mfma_f32_16x16x32_bf16 v[40:43], a[168:171], a[184:187], v[40:43]\n"
        "v_mfma_f32_16x16x32_bf16 v[40:43], a[172:175], a[188:191], v[40:43]\n"
        "s_nop 7\n"
        "global_load_dwordx4 a[160:163], v254, s[84:85] offset:0 sc1\n"
        "global_load_dwordx4 a[164:167], v254, s[84:85] offset:1024 sc1\n"
        "global_load_dwordx4 a[168:171], v254, s[84:85] offset:2048 sc1\n"
        "global_load_dwordx4 a[172:175], v254, s[84:85] offset:3072 sc1\n"
        "s_waitcnt vmcnt(0)\n"
        "v_mfma_f32_16x16x32_bf16 v[44:47], a[160:163], a[176:179], 0\n"
        "v_mfma_f32_16x16x32_bf16 v[44:47], a[164:167], a[180:183], v[44:47]\n"
        "v_mfma_f32_16x16x32_bf16 v[44:47], a[168:171], a[184:187], v[44:47]\n"
        "v_mfma_f32_16x16x32_bf16 v[44:47], a[172:175], a[188:191], v[44:47]\n"
        "s_nop 7\n"
        "v_cmp_gt_i32 vcc, 0, v27\n"
        "s_nop 1\n"
        "v_cndmask_b32 v40, v40, v242, vcc\n"
        "v_cmp_gt_i32 vcc, 1, v27\n"
        "s_nop 1\n"
        "v_cndmask_b32 v41, v41, v242, vcc\n"
        "v_cmp_gt_i32 vcc, 2, v27\n"
        "s_nop 1\n"
        "v_cndmask_b32 v42, v42, v242, vcc\n"
        "v_cmp_gt_i32 vcc, 3, v27\n"
        "s_nop 1\n"
        "v_cndmask_b32 v43, v43, v242, vcc\n"
        "v_cmp_gt_i32 vcc, 16, v27\n"
        "s_nop 1\n"
        "v_cndmask_b32 v44, v44, v242, vcc\n"
        "v_cmp_gt_i32 vcc, 17, v27\n"
        "s_nop 1\n"
        "v_cndmask_b32 v45, v45, v242, vcc\n"
        "v_cmp_gt_i32 vcc, 18, v27\n"
        "s_nop 1\n"
        "v_cndmask_b32 v46, v46, v242, vcc\n"
        "v_cmp_gt_i32 vcc, 19, v27\n"
        "s_nop 1\n"
        "v_cndmask_b32 v47, v47, v242, vcc\n"
        "v_max3_f32 v25, v40, v41, v24\n"
        "v_max3_f32 v25, v42, v43, v25\n"
        "v_max3_f32 v25, v44, v45, v25\n"
        "v_max3_f32 v25, v46, v47, v25\n"
        "v_mov_b32 v28, v25\n"
        "s_nop 1\n"
        "v_permlane16_swap_b32 v25, v28\n"
        "v_max_f32 v25, v25, v28\n"
        "v_mov_b32 v28, v25\n"
        "s_nop 1\n"
        "v_permlane32_swap_b32 v25, v28\n"
        "v_max_f32 v25, v25, v28\n"
        "v_sub_f32 v26, v24, v25\n"
        "v_exp_f32 v26, v26\n"
        "v_mov_b32 v24, v25\n"
        "v_sub_f32 v40, v40, v25\n"
        "v_exp_f32 v40, v40\n"
        "v_sub_f32 v41, v41, v25\n"
        "v_exp_f32 v41, v41\n"
        "v_sub_f32 v42, v42, v25\n"
        "v_exp_f32 v42, v42\n"
        "v_sub_f32 v43, v43, v25\n"
        "v_exp_f32 v43, v43\n"
        "v_sub_f32 v44, v44, v25\n"
        "v_exp_f32 v44, v44\n"
        "v_sub_f32 v45, v45, v25\n"
        "v_exp_f32 v45, v45\n"
        "v_sub_f32 v46, v46, v25\n"
        "v_exp_f32 v46, v46\n"
        "v_sub_f32 v47, v47, v25\n"
        "v_exp_f32 v47, v47\n"
        "v_cvt_pk_bf16_f32 v56, v40, v41\n"
        "v_cvt_pk_bf16_f32 v57, v42, v43\n"
        "v_cvt_pk_bf16_f32 v58, v44, v45\n"
        "v_cvt_pk_bf16_f32 v59, v46, v47\n"
        "v_mul_f32 v112, v112, v26\n"
        "v_mul_f32 v113, v113, v26\n"
        "v_mul_f32 v114, v114, v26\n"
        "v_mul_f32 v115, v115, v26\n"
        "v_mul_f32 v116, v116, v26\n"
        "v_mul_f32 v117, v117, v26\n"
        "v_mul_f32 v118, v118, v26\n"
        "v_mul_f32 v119, v119, v26\n"
        "v_mul_f32 v120, v120, v26\n"
        "v_mul_f32 v121, v121, v26\n"
        "v_mul_f32 v122, v122, v26\n"
        "v_mul_f32 v123, v123, v26\n"
        "v_mul_f32 v124, v124, v26\n"
        "v_mul_f32 v125, v125, v26\n"
        "v_mul_f32 v126, v126, v26\n"
        "v_mul_f32 v127, v127, v26\n"
        "v_mul_f32 v144, v144, v26\n"
        "v_mul_f32 v145, v145, v26\n"
        "v_mul_f32 v146, v146, v26\n"
        "v_mul_f32 v147, v147, v26\n"
        "v_mul_f32 v148, v148, v26\n"
        "v_mul_f32 v149, v149, v26\n"
        "v_mul_f32 v150, v150, v26\n"
        "v_mul_f32 v151, v151, v26\n"
        "v_mul_f32 v152, v152, v26\n"
        "v_mul_f32 v153, v153, v26\n"
        "v_mul_f32 v154, v154, v26\n"
        "v_mul_f32 v155, v155, v26\n"
        "v_mul_f32 v156, v156, v26\n"
        "v_mul_f32 v157, v157, v26\n"
        "v_mul_f32 v158, v158, v26\n"
        "v_mul_f32 v159, v159, v26\n"
        "v_mul_f32 v60, v60, v26\n"
        "v_mul_f32 v61, v61, v26\n"
        "v_mul_f32 v62, v62, v26\n"
        "v_mul_f32 v63, v63, v26\n"
        "s_nop 1\n"
        "v_mfma_f32_16x16x32_bf16 v[60:63], a[80:83], v[56:59], v[60:63]\n"
        "global_load_dwordx4 a[160:163], v254, s[86:87] offset:-4096 sc1\n"
        "global_load_dwordx4 a[164:167], v254, s[86:87] offset:-3072 sc1\n"
        "global_load_dwordx4 a[168:171], v254, s[86:87] offset:-2048 sc1\n"
        "global_load_dwordx4 a[172:175], v254, s[86:87] offset:-1024 sc1\n"
        "s_waitcnt vmcnt(0)\n"
        "v_mfma_f32_16x16x32_bf16 v[112:115], a[160:163], v[56:59], v[112:115]\n"
        "v_mfma_f32_16x16x32_bf16 v[116:119], a[164:167], v[56:59], v[116:119]\n"
        "v_mfma_f32_16x16x32_bf16 v[120:123], a[168:171], v[56:59], v[120:123]\n"
        "v_mfma_f32_16x16x32_bf16 v[124:127], a[172:175], v[56:59], v[124:127]\n"
        "s_nop 7\n"
        "global_load_dwordx4 a[160:163], v254, s[86:87] offset:0 sc1\n"
        "global_load_dwordx4 a[164:167], v254, s[86:87] offset:1024 sc1\n"
        "global_load_dwordx4 a[168:171], v254, s[86:87] offset:2048 sc1\n"
        "global_load_dwordx4 a[172:175], v254, s[86:87] offset:3072 sc1\n"
        "s_waitcnt vmcnt(0)\n"
        "v_mfma_f32_16x16x32_bf16 v[144:147], a[160:163], v[56:59], v[144:147]\n"
        "v_mfma_f32_16x16x32_bf16 v[148:151], a[164:167], v[56:59], v[148:151]\n"
        "v_mfma_f32_16x16x32_bf16 v[152:155], a[168:171], v[56:59], v[152:155]\n"
        "v_mfma_f32_16x16x32_bf16 v[156:159], a[172:175], v[56:59], v[156:159]\n"
        "s_nop 7\n"
        "s_add_u32 s84, s84, 0x2000\n"
        "s_addc_u32 s85, s85, 0\n"
        "s_add_u32 s86, s86, 0x2000\n"
        "s_addc_u32 s87, s87, 0\n"
        "v_subrev_u32 v27, 32, v27\n"
        "s_sub_u32 s97, s97, 1\n"
        "s_cmp_lg_u32 s97, 0\n"
        "s_cbranch_scc1 LRLOOP_%=\n"
        "s_mul_i32 s97, s53, 1052672\n"
        "s_lshl_b32 s86, s43, 15\n"
        "s_add_u32 s97, s97, s86\n"
        "s_lshl_b32 s86, s40, 10\n"
        "s_add_u32 s97, s97, s86\n"
        "s_lshl_b32 s86, s96, 6\n"
        "s_add_u32 s97, s97, s86\n"
        "s_add_u32 s97, s97, 0x2200000\n"
        "s_add_u32 s84, s38, s97\n"
        "s_addc_u32 s85, s39, 0\n"
        "v_rcp_f32 v26, v60\n"
        "s_nop 0\n"
        "v_mul_f32 v112, v112, v26\n"
        "v_mul_f32 v113, v113, v26\n"
        "v_mul_f32 v114, v114, v26\n"
        "v_mul_f32 v115, v115, v26\n"
        "v_mul_f32 v116, v116, v26\n"
        "v_mul_f32 v117, v117, v26\n"
        "v_mul_f32 v118, v118, v26\n"
        "v_mul_f32 v119, v119, v26\n"
        "v_cvt_pk_bf16_f32 v112, v112, v113\n"
        "v_cvt_pk_bf16_f32 v113, v114, v115\n"
        "v_cvt_pk_bf16_f32 v114, v116, v117\n"
        "v_cvt_pk_bf16_f32 v115, v118, v119\n"
        "global_store_dwordx4 v254, v[112:115], s[84:85] sc1\n"
        "s_add_u32 s84, s84, 0x2000\n"
        "s_addc_u32 s85, s85, 0\n"
        "v_mul_f32 v120, v120, v26\n"
        "v_mul_f32 v121, v121, v26\n"
        "v_mul_f32 v122, v122, v26\n"
        "v_mul_f32 v123, v123, v26\n"
        "v_mul_f32 v124, v124, v26\n"
        "v_mul_f32 v125, v125, v26\n"
        "v_mul_f32 v126, v126, v26\n"
        "v_mul_f32 v127, v127, v26\n"
        "v_cvt_pk_bf16_f32 v120, v120, v121\n"
        "v_cvt_pk_bf16_f32 v121, v122, v123\n"
        "v_cvt_pk_bf16_f32 v122, v124, v125\n"
        "v_cvt_pk_bf16_f32 v123, v126, v127\n"
        "global_store_dwordx4 v254, v[120:123], s[84:85] sc1\n"
        "s_add_u32 s84, s84, 0x2000\n"
        "s_addc_u32 s85, s85, 0\n"
        "v_mul_f32 v144, v144, v26\n"
        "v_mul_f32 v145, v145, v26\n"
        "v_mul_f32 v146, v146, v26\n"
        "v_mul_f32 v147, v147, v26\n"
        "v_mul_f32 v148, v148, v26\n"
        "v_mul_f32 v149, v149, v26\n"
        "v_mul_f32 v150, v150, v26\n"
        "v_mul_f32 v151, v151, v26\n"
        "v_cvt_pk_bf16_f32 v144, v144, v145\n"
        "v_cvt_pk_bf16_f32 v145, v146, v147\n"
        "v_cvt_pk_bf16_f32 v146, v148, v149\n"
        "v_cvt_pk_bf16_f32 v147, v150, v151\n"
        "global_store_dwordx4 v254, v[144:147], s[84:85] sc1\n"
        "s_add_u32 s84, s84, 0x2000\n"
        "s_addc_u32 s85, s85, 0\n"
        "v_mul_f32 v152, v152, v26\n"
        "v_mul_f32 v153, v153, v26\n"
        "v_mul_f32 v154, v154, v26\n"
        "v_mul_f32 v155, v155, v26\n"
        "v_mul_f32 v156, v156, v26\n"
        "v_mul_f32 v157, v157, v26\n"
        "v_mul_f32 v158, v158, v26\n"
        "v_mul_f32 v159, v159, v26\n"
        "v_cvt_pk_bf16_f32 v152, v152, v153\n"
        "v_cvt_pk_bf16_f32 v153, v154, v155\n"
        "v_cvt_pk_bf16_f32 v154, v156, v157\n"
        "v_cvt_pk_bf16_f32 v155, v158, v159\n"
        "global_store_dwordx4 v254, v[152:155], s[84:85] sc1\n"
        "s_add_u32 s84, s84, 0x2000\n"
        "s_addc_u32 s85, s85, 0\n"
        "s_waitcnt vmcnt(0)\n"
        "global_load_dwordx4 v[144:147], v254, s[88:89] offset:0 sc1\n"
        "global_load_dwordx4 v[148:151], v254, s[88:89] offset:1024 sc1\n"
        "global_load_dwordx4 v[152:155], v254, s[88:89] offset:2048 sc1\n"
        "global_load_dwordx4 v[156:159], v254, s[88:89] offset:3072 sc1\n"
        "s_waitcnt vmcnt(0)\n"
        "s_cmp_eq_u32 s52, 0\n"
        "s_cbranch_scc1 LRET0_%=\n"
        "s_cmp_eq_u32 s52, 1\n"
        "s_cbranch_scc1 LRET1_%=\n"
        "s_cmp_eq_u32 s52, 2\n"
        "s_cbranch_scc1 LRET2_%=\n"
        "s_branch LRET3_%=\n"
        "LEND_%=:\n"
        ".p2align 12\n"
        :
        : "s"(__builtin_amdgcn_kernarg_segment_ptr()), "s"(blockIdx.x), "s"(__builtin_amdgcn_readfirstlane(threadIdx.x >> 6))
        : "v0", "v1", "v2", "v3", "v4", "v5", "v6", "v7", "v8", "v9", "v10", "v11", "v12", "v13", "v14", "v15", "v16", "v17", "v18", "v19", "v20", "v21", "v22", "v23", "v24", "v25", "v26", "v27", "v28", "v29", "v30", "v31",
        "v32", "v33", "v34", "v35", "v36", "v37", "v38", "v39", "v40", "v41", "v42", "v43", "v44", "v45", "v46", "v47", "v48", "v49", "v50", "v51", "v52", "v53", "v54", "v55", "v56", "v57", "v58", "v59", "v60", "v61", "v62", "v63",
        "v64", "v65", "v66", "v67", "v68", "v69", "v70", "v71", "v72", "v73", "v74", "v75", "v76", "v77", "v78", "v79", "v80", "v81", "v82", "v83", "v84", "v85", "v86", "v87", "v88", "v89", "v90", "v91", "v92", "v93", "v94", "v95",
        "v96", "v97", "v98", "v99", "v100", "v101", "v102", "v103", "v104", "v105", "v106", "v107", "v108", "v109", "v110", "v111", "v112", "v113", "v114", "v115", "v116", "v117", "v118", "v119", "v120", "v121", "v122", "v123", "v124", "v125", "v126", "v127",
        "v128", "v129", "v130", "v131", "v132", "v133", "v134", "v135", "v136", "v137", "v138", "v139", "v140", "v141", "v142", "v143", "v144", "v145", "v146", "v147", "v148", "v149", "v150", "v151", "v152", "v153", "v154", "v155", "v156", "v157", "v158", "v159",
        "v160", "v161", "v162", "v163", "v164", "v165", "v166", "v167", "v168", "v169", "v170", "v171", "v172", "v173", "v174", "v175", "v176", "v177", "v178", "v179", "v180", "v181", "v182", "v183", "v184", "v185", "v186", "v187", "v188", "v189", "v190", "v191",
        "v192", "v193", "v194", "v195", "v196", "v197", "v198", "v199", "v200", "v201", "v202", "v203", "v204", "v205", "v206", "v207", "v208", "v209", "v210", "v211", "v212", "v213", "v214", "v215", "v216", "v217", "v218", "v219", "v220", "v221", "v222", "v223",
        "v224", "v225", "v226", "v227", "v228", "v229", "v230", "v231", "v232", "v233", "v234", "v235", "v236", "v237", "v238", "v239", "v240", "v241", "v242", "v243", "v244", "v245", "v246", "v247", "v248", "v249", "v250", "v251", "v252", "v253", "v254", "v255",
        "a0", "a1", "a2", "a3", "a4", "a5", "a6", "a7", "a8", "a9", "a10", "a11", "a12", "a13", "a14", "a15", "a16", "a17", "a18", "a19", "a20", "a21", "a22", "a23", "a24", "a25", "a26", "a27", "a28", "a29", "a30", "a31",
        "a32", "a33", "a34", "a35", "a36", "a37", "a38", "a39", "a40", "a41", "a42", "a43", "a44", "a45", "a46", "a47", "a48", "a49", "a50", "a51", "a52", "a53", "a54", "a55", "a56", "a57", "a58", "a59", "a60", "a61", "a62", "a63",
        "a64", "a65", "a66", "a67", "a68", "a69", "a70", "a71", "a72", "a73", "a74", "a75", "a76", "a77", "a78", "a79", "a80", "a81", "a82", "a83", "a84", "a85", "a86", "a87", "a88", "a89", "a90", "a91", "a92", "a93", "a94", "a95",
        "a96", "a97", "a98", "a99", "a100", "a101", "a102", "a103", "a104", "a105", "a106", "a107", "a108", "a109", "a110", "a111", "a112", "a113", "a114", "a115", "a116", "a117", "a118", "a119", "a120", "a121", "a122", "a123", "a124", "a125", "a126", "a127",
        "a128", "a129", "a130", "a131", "a132", "a133", "a134", "a135", "a136", "a137", "a138", "a139", "a140", "a141", "a142", "a143", "a144", "a145", "a146", "a147", "a148", "a149", "a150", "a151", "a152", "a153", "a154", "a155", "a156", "a157", "a158", "a159",
        "a160", "a161", "a162", "a163", "a164", "a165", "a166", "a167", "a168", "a169", "a170", "a171", "a172", "a173", "a174", "a175", "a176", "a177", "a178", "a179", "a180", "a181", "a182", "a183", "a184", "a185", "a186", "a187", "a188", "a189", "a190", "a191",
        "a192", "a193", "a194", "a195", "a196", "a197", "a198", "a199", "a200", "a201", "a202", "a203", "a204", "a205", "a206", "a207", "a208", "a209", "a210", "a211", "a212", "a213", "a214", "a215", "a216", "a217", "a218", "a219", "a220", "a221", "a222", "a223",
        "a224", "a225", "a226", "a227", "a228", "a229", "a230", "a231", "a232", "a233", "a234", "a235", "a236", "a237", "a238", "a239", "a240", "a241", "a242", "a243", "a244", "a245", "a246", "a247", "a248", "a249", "a250", "a251", "a252", "a253", "a254", "a255",
        "s8", "s9", "s10", "s11", "s12", "s13", "s14", "s15", "s16", "s17", "s18", "s19", "s20", "s21", "s22", "s23", "s24", "s25", "s26", "s27", "s28", "s29", "s30", "s31", "s34", "s35", "s36", "s37", "s38", "s39", "s40", "s41",
        "s42", "s43", "s44", "s45", "s46", "s47", "s48", "s49", "s50", "s51", "s52", "s53", "s54", "s55", "s56", "s57", "s58", "s59", "s60", "s61", "s62", "s63", "s64", "s65", "s66", "s67", "s68", "s69", "s70", "s71", "s72", "s73",
        "s74", "s75", "s76", "s77", "s78", "s79", "s80", "s81", "s82", "s83", "s84", "s85", "s86", "s87", "s88", "s89", "s90", "s91", "s92", "s93", "s94", "s95", "s96", "s97", "s98", "s99", "vcc", "scc", "memory");
}

extern "C" void kernel_launch(void* const* d_in, const int* in_sizes, int n_in, void* d_out, int out_size, void* d_ws, size_t ws_size, hipStream_t stream) {
    (void)in_sizes; (void)n_in; (void)out_size;
    constexpr size_t WS_USED = (size_t)85 << 20;
    Params P;
    P.x = d_in[0]; P.ckv = d_in[1]; P.wq = d_in[2]; P.wuk = d_in[3]; P.wuv = d_in[4]; P.wo = d_in[5];
    P.bk = d_in[7]; P.bv = d_in[8]; P.shk = d_in[9]; P.ssk = d_in[10]; P.ssd = d_in[11]; P.gw = d_in[12];
    P.out = d_out; P.ws = (char*)d_ws;
    if (ws_size > WS_USED) P.ws += (ws_size - WS_USED) & ~(size_t)0xfffff;
    constexpr int LDS_MAX = 163840;
    (void)hipFuncSetAttribute((const void*)k_main, hipFuncAttributeMaxDynamicSharedMemorySize, LDS_MAX);
    k_main<<<256, 256, LDS_MAX, stream>>>(P);
}
```

```cpp
#include <hip/hip_runtime.h>
#include <stdint.h>

struct Params {
    const void *x, *ckv, *wq, *wuk, *wuv, *wo, *bk, *bv, *shk, *ssk, *ssd, *gw;
    void* out; char* ws;
};

__global__ __launch_bounds__(256, 1) void k_main(Params P) {
    asm volatile(
        "; ---- PRO\n"
        "s_load_dwordx16 s[8:23], %0, 0\n"
        "s_load_dwordx8 s[24:31], %0, 64\n"
        "s_load_dwordx4 s[36:39], %0, 96\n"
        "s_mov_b32 s55, %1\n"
        "s_mov_b32 s40, %2\n"
        "v_mbcnt_lo_u32_b32 v0, -1, 0\n"
        "v_mbcnt_hi_u32_b32 v0, -1, v0\n"
        "s_getpc_b64 s[46:47]\n"
        "v_min_u32 v4, 12, v0\n"
        "v_lshlrev_b32 v4, 12, v4\n"
        "global_load_dword v253, v4, s[46:47] offset:0\n"
        "s_and_b32 s41, s55, 7\n"
        "s_lshr_b32 s42, s55, 3\n"
        "s_lshr_b32 s50, s42, 3\n"
        "s_lshl2_add_u32 s43, s41, s50\n"
        "s_and_b32 s44, s42, 7\n"
        "s_sub_u32 s45, 15, s44\n"
        "s_lshl_b32 s72, s40, 13\n"
        "s_lshl_b32 s73, s40, 12\n"
        "v_lshlrev_b32 v254, 4, v0\n"
        "v_lshrrev_b32 v2, 4, v0\n"
        "v_and_b32 v1, 15, v0\n"
        "v_lshrrev_b32 v4, 3, v0\n"
        "v_and_b32 v5, 7, v0\n"
        "v_and_b32 v8, 6, v4\n"
        "v_xor_b32 v8, v8, v5\n"
        "v_lshlrev_b32 v8, 4, v8\n"
        "v_lshl_add_u32 v9, v4, 13, v8\n"
        "v_xor_b32 v10, 4, v4\n"
        "v_lshl_add_u32 v10, v10, 13, v8\n"
        "v_add_u32 v238, 4096, v9\n"
        "v_add_u32 v239, 3200, v9\n"
        "v_add_u32 v240, 67584, v10\n"
        "v_add_u32 v241, 66688, v10\n"
        "s_cmp_ge_u32 s40, 2\n"
        "s_cselect_b32 s50, s45, s44\n"
        "s_and_b32 s51, s40, 1\n"
        "s_lshl1_add_u32 s50, s50, s51\n"
        "s_lshl_b32 s50, s50, 16\n"
        "s_lshl_b32 s51, s40, 17\n"
        "s_lshl_b32 s52, s43, 8\n"
        "s_add_u32 s51, s51, s52\n"
        "s_waitcnt lgkmcnt(0)\n"
        "s_add_u32 s60, s14, s51\n"
        "s_addc_u32 s61, s15, 0\n"
        "s_sub_u32 s60, s60, 4096\n"
        "s_subb_u32 s61, s61, 0\n"
        "s_add_u32 m0, s73, 0x10000\n"
        "s_add_u32 s86, s60, 0x200000\n"
        "s_addc_u32 s87, s61, 0\n"
        "global_load_lds_dwordx4 v238, s[86:87] offset:0\n"
        "global_load_lds_dwordx4 v239, s[86:87] offset:1024\n"
        "global_load_lds_dwordx4 v240, s[86:87] offset:2048\n"
        "global_load_lds_dwordx4 v241, s[86:87] offset:3072\n"
        "v_xor_b32 v6, v5, v2\n"
        "v_lshlrev_b32 v6, 4, v6\n"
        "v_xor_b32 v7, 64, v6\n"
        "v_lshl_add_u32 v234, v4, 10, v6\n"
        "v_lshl_add_u32 v8, v4, 10, v7\n"
        "v_add_u32 v235, 7168, v8\n"
        "v_add_u32 v236, 14336, v234\n"
        "v_add_u32 v237, 21504, v8\n"
        "s_add_u32 s56, s10, s50\n"
        "s_addc_u32 s57, s11, 0\n"
        "s_add_u32 m0, s72, 0x0\n"
        "s_add_u32 s86, s56, 0x200\n"
        "s_addc_u32 s87, s57, 0\n"
        "global_load_lds_dwordx4 v234, s[86:87] offset:0\n"
        "global_load_lds_dwordx4 v235, s[86:87] offset:1024\n"
        "global_load_lds_dwordx4 v236, s[86:87] offset:2048\n"
        "global_load_lds_dwordx4 v237, s[86:87] offset:3072\n"
        "s_getpc_b64 s[46:47]\n"
        "LPC_%=:\n"
        "s_lshl2_add_u32 s53, s42, s40\n"
        "s_lshl_b32 s53, s53, 10\n"
        "v_lshlrev_b32 v12, 6, v1\n"
        "v_add_u32 v12, s53, v12\n"
        "v_min_u32 v252, LEND_%=-LPC_%=-4, v12\n"
        "global_load_dword v253, v252, s[46:47] offset:0\n"
        "s_add_u32 s62, s16, s51\n"
        "s_addc_u32 s63, s17, 0\n"
        "s_sub_u32 s62, s62, 4096\n"
        "s_subb_u32 s63, s63, 0\n"
        "s_add_u32 m0, s73, 0x14000\n"
        "s_add_u32 s86, s62, 0x200000\n"
        "s_addc_u32 s87, s63, 0\n"
        "global_load_lds_dwordx4 v238, s[86:87] offset:0\n"
        "global_load_lds_dwordx4 v239, s[86:87] offset:1024\n"
        "global_load_lds_dwordx4 v240, s[86:87] offset:2048\n"
        "global_load_lds_dwordx4 v241, s[86:87] offset:3072\n"
        "s_add_u32 s58, s56, 0x8000\n"
        "s_addc_u32 s59, s57, 0\n"
        "s_add_u32 s68, s12, s51\n"
        "s_addc_u32 s69, s13, 0\n"
        "s_add_u32 m0, s72, 0x1000\n"
        "s_add_u32 s86, s58, 0x200\n"
        "s_addc_u32 s87, s59, 0\n"
        "global_load_lds_dwordx4 v234, s[86:87] offset:0\n"
        "global_load_lds_dwordx4 v235, s[86:87] offset:1024\n"
        "global_load_lds_dwordx4 v236, s[86:87] offset:2048\n"
        "global_load_lds_dwordx4 v237, s[86:87] offset:3072\n"
        "s_sub_u32 s68, s68, 4096\n"
        "s_subb_u32 s69, s69, 0\n"
        "v_lshrrev_b32 v8, 1, v1\n"
        "v_xor_b32 v8, v8, v2\n"
        "v_lshlrev_b32 v8, 4, v8\n"
        "v_lshl_add_u32 v8, v1, 7, v8\n"
        "v_xor_b32 v9, 64, v8\n"
        "s_lshr_b32 s50, s40, 1\n"
        "s_lshl_b32 s50, s50, 13\n"
        "v_add_u32 v224, s50, v8\n"
        "v_add_u32 v246, s72, v8\n"
        "v_add_u32 v248, 0x10000, v246\n"
        "v_add_u32 v225, s50, v9\n"
        "v_add_u32 v247, s72, v9\n"
        "v_add_u32 v249, 0x10000, v247\n"
        "s_and_b32 s50, s40, 1\n"
        "s_lshl_b32 s50, s50, 10\n"
        "s_add_u32 s50, s50, 0x10000\n"
        "v_lshlrev_b32 v8, 11, v2\n"
        "v_add_u32 v8, s50, v8\n"
        "v_lshrrev_b32 v9, 2, v1\n"
        "v_or_b32 v10, 4, v9\n"
        "v_and_b32 v11, 1, v2\n"
        "v_and_b32 v13, 3, v0\n"
        "v_lshlrev_b32 v14, 2, v11\n"
        "v_xor_b32 v14, v14, v9\n"
        "v_bfe_u32 v15, v14, 1, 2\n"
        "v_xor_b32 v15, 0, v15\n"
        "v_lshlrev_b32 v15, 5, v15\n"
        "v_lshl_add_u32 v14, v14, 7, v15\n"
        "v_lshl_add_u32 v14, v13, 3, v14\n"
        "v_add_u32 v226, v14, v8\n"
        "v_xor_b32 v228, 32, v226\n"
        "v_xor_b32 v230, 64, v226\n"
        "v_xor_b32 v232, 96, v226\n"
        "v_lshlrev_b32 v14, 2, v11\n"
        "v_xor_b32 v14, v14, v10\n"
        "v_bfe_u32 v15, v14, 1, 2\n"
        "v_xor_b32 v15, 0, v15\n"
        "v_lshlrev_b32 v15, 5, v15\n"
        "v_lshl_add_u32 v14, v14, 7, v15\n"
        "v_lshl_add_u32 v14, v13, 3, v14\n"
        "v_add_u32 v227, v14, v8\n"
        "v_xor_b32 v229, 32, v227\n"
        "v_xor_b32 v231, 64, v227\n"
        "v_xor_b32 v233, 96, v227\n"
        "v_add_u32 v216, 0x10000, v226\n"
        "v_add_u32 v217, 0x10000, v227\n"
        "v_add_u32 v218, 0x10000, v228\n"
        "v_add_u32 v219, 0x10000, v229\n"
        "v_add_u32 v220, 0x10000, v230\n"
        "v_add_u32 v221, 0x10000, v231\n"
        "v_add_u32 v222, 0x10000, v232\n"
        "v_add_u32 v223, 0x10000, v233\n"
        "v_lshl_add_u32 v242, v4, 11, v6\n"
        "v_lshl_add_u32 v8, v4, 11, v7\n"
        "v_add_u32 v243, 15360, v8\n"
        "v_add_u32 v244, 129024, v242\n"
        "v_add_u32 v245, 144384, v8\n"
        "s_lshl_b32 s50, s45, 18\n"
        "s_lshl_b32 s51, s40, 15\n"
        "s_add_u32 s50, s50, s51\n"
        "s_add_u32 s64, s8, s50\n"
        "s_addc_u32 s65, s9, 0\n"
        "s_lshl_b32 s50, s44, 18\n"
        "s_add_u32 s50, s50, s51\n"
        "s_add_u32 s66, s8, s50\n"
        "s_addc_u32 s67, s9, 0\n"
        "s_add_u32 s48, s38, 0x3b00000\n"
        "s_addc_u32 s49, s39, 0\n"
        "s_mov_b32 s35, 0xffff0000\n"
        "v_mov_b32 v11, 0\n"
        "global_load_dwordx2 v[250:251], v11, s[48:49] sc1\n"
        "s_lshl2_add_u32 s82, s55, s40\n"
        "s_lshl_b32 s82, s82, 11\n"
        "v_and_b32 v20, 0xf0, v254\n"
        "v_lshl_add_u32 v20, v20, 3, s82\n"
        "global_load_dword v253, v20, s[10:11] offset:0\n"
        "s_lshl2_add_u32 s82, s42, s40\n"
        "s_lshl3_add_u32 s82, s82, s41\n"
        "s_lshl_b32 s82, s82, 10\n"
        "v_and_b32 v21, 0x70, v254\n"
        "v_lshl_add_u32 v173, v21, 3, s82\n"
        "s_mov_b64 exec, 0\n"
        "s_cmp_eq_u32 s40, 0\n"
        "s_cbranch_scc1 LHA0_%=\n"
        "s_cmp_eq_u32 s40, 1\n"
        "s_cbranch_scc1 LHA1_%=\n"
        "s_cmp_eq_u32 s40, 2\n"
        "s_cbranch_scc1 LHA2_%=\n"
        "s_branch LHA3_%=\n"
        "LHRA_%=:\n"
        "s_mov_b64 exec, -1\n"
        "s_add_u32 m0, s73, 0x18000\n"
        "s_add_u32 s86, s60, 0x300000\n"
        "s_addc_u32 s87, s61, 0\n"
        "global_load_lds_dwordx4 v238, s[86:87] offset:0\n"
        "global_load_lds_dwordx4 v239, s[86:87] offset:1024\n"
        "global_load_lds_dwordx4 v240, s[86:87] offset:2048\n"
        "global_load_lds_dwordx4 v241, s[86:87] offset:3072\n"
        "s_add_u32 m0, s73, 0x1c000\n"
        "s_add_u32 s86, s62, 0x300000\n"
        "s_addc_u32 s87, s63, 0\n"
        "global_load_lds_dwordx4 v238, s[86:87] offset:0\n"
        "global_load_lds_dwordx4 v239, s[86:87] offset:1024\n"
        "global_load_lds_dwordx4 v240, s[86:87] offset:2048\n"
        "global_load_lds_dwordx4 v241, s[86:87] offset:3072\n"
        "; ---- FRONT\n"
        "s_add_u32 m0, s72, 0x8000\n"
        "s_add_u32 s86, s56, 0x300\n"
        "s_addc_u32 s87, s57, 0\n"
        "global_load_lds_dwordx4 v234, s[86:87] offset:0\n"
        "s_waitcnt vmcnt(20)\n"
        "s_barrier\n"
        "ds_read_b64_tr_b16 v[32:33], v226 offset:0\n"
        "ds_read_b64_tr_b16 v[34:35], v227 offset:0\n"
        "ds_read_b128 v[0:3], v224 offset:16384\n"
        "global_load_lds_dwordx4 v235, s[86:87] offset:1024\n"
        "global_load_lds_dwordx4 v236, s[86:87] offset:2048\n"
        "ds_read_b128 v[4:7], v225 offset:16384\n"
        "ds_read_b64_tr_b16 v[64:65], v226 offset:8192\n"
        "ds_read_b64_tr_b16 v[66:67], v227 offset:8192\n"
        "ds_read_b128 v[12:15], v225 offset:18432\n"
        "s_lshl_b32 s94, s41, 7\n"
        "s_waitcnt lgkmcnt(4)\n"
        "v_mfma_f32_16x16x32_bf16 a[0:3], v[32:35], v[0:3], 0\n"
        "global_load_lds_dwordx4 v237, s[86:87] offset:3072\n"
        "ds_read_b128 v[8:11], v224 offset:18432\n"
        "s_add_u32 m0, s72, 0x9000\n"
        "s_add_u32 s86, s58, 0x300\n"
        "s_addc_u32 s87, s59, 0\n"
        "global_load_lds_dwordx4 v234, s[86:87] offset:0\n"
        "ds_read_b64_tr_b16 v[36:37], v228 offset:0\n"
        "s_add_u32 s82, s12, 0x0\n"
        "s_addc_u32 s83, s13, 0\n"
        "s_waitcnt lgkmcnt(3)\n"
        "v_mfma_f32_16x16x32_bf16 a[0:3], v[64:67], v[4:7], a[0:3]\n"
        "ds_read_b64_tr_b16 v[38:39], v229 offset:0\n"
        "s_lshl2_add_u32 s94, s42, s94\n"
        "s_waitcnt lgkmcnt(3)\n"
        "v_mfma_f32_16x16x32_bf16 a[32:35], v[64:67], v[12:15], 0\n"
        "ds_read_b64_tr_b16 v[68:69], v228 offset:8192\n"
        "ds_read_b64_tr_b16 v[70:71], v229 offset:8192\n"
        "ds_read_b64_tr_b16 v[40:41], v230 offset:0\n"
        "s_add_u32 s94, s94, s40\n"
        "s_waitcnt lgkmcnt(5)\n"
        "v_mfma_f32_16x16x32_bf16 a[32:35], v[32:35], v[8:11], a[32:35]\n"
        "ds_read_b64_tr_b16 v[42:43], v231 offset:0\n"
        "ds_read_b64_tr_b16 v[72:73], v230 offset:8192\n"
        "ds_read_b64_tr_b16 v[74:75], v231 offset:8192\n"
        "ds_read_b64_tr_b16 v[44:45], v232 offset:0\n"
        "ds_read_b64_tr_b16 v[46:47], v233 offset:0\n"
        "ds_read_b64_tr_b16 v[76:77], v232 offset:8192\n"
        "ds_read_b64_tr_b16 v[78:79], v233 offset:8192\n"
        "ds_read_b128 v[16:19], v224 offset:0\n"
        "ds_read_b128 v[20:23], v225 offset:0\n"
        "global_load_lds_dwordx4 v235, s[86:87] offset:1024\n"
        "s_lshl_b32 s94, s94, 1\n"
        "s_waitcnt lgkmcnt(12)\n"
        "v_mfma_f32_16x16x32_bf16 a[36:39], v[36:39], v[8:11], 0\n"
        "ds_read_b128 v[24:27], v224 offset:2048\n"
        "ds_read_b128 v[28:31], v225 offset:2048\n"
        "s_lshl_b32 s50, s42, 6\n"
        "s_waitcnt lgkmcnt(12)\n"
        "v_mfma_f32_16x16x32_bf16 a[36:39], v[68:71], v[12:15], a[36:39]\n"
        "s_lshl_b32 s51, s41, 7\n"
        "v_mov_b32 v172, s94\n"
        "v_lshlrev_b32 v198, 11, v172\n"
        "v_mfma_f32_16x16x32_bf16 a[4:7], v[68:71], v[4:7], 0\n"
        "s_waitcnt lgkmcnt(10)\n"
        "v_lshrrev_b32 v166, 4, v254\n"
        "v_lshrrev_b32 v167, 3, v166\n"
        "v_mfma_f32_16x16x32_bf16 a[4:7], v[36:39], v[0:3], a[4:7]\n"
        "global_load_lds_dwordx4 v236, s[86:87] offset:2048\n"
        "s_lshl_b32 s52, s40, 5\n"
        "v_and_b32 v168, 7, v166\n"
        "v_mfma_f32_16x16x32_bf16 a[8:11], v[40:43], v[0:3], 0\n"
        "s_add_u32 s51, s51, s52\n"
        "v_lshl_add_u32 v167, v167, 3, s50\n"
        "s_waitcnt lgkmcnt(6)\n"
        "v_mfma_f32_16x16x32_bf16 a[8:11], v[72:75], v[4:7], a[8:11]\n"
        "s_waitcnt lgkmcnt(4)\n"
        "v_lshl_add_u32 v168, v168, 2, s51\n"
        "v_lshlrev_b32 v169, 11, v167\n"
        "v_mfma_f32_16x16x32_bf16 a[40:43], v[72:75], v[12:15], 0\n"
        "s_sub_u32 s74, s20, 2048\n"
        "s_subb_u32 s75, s21, 0\n"
        "v_lshl_add_u32 v169, v168, 1, v169\n"
        "v_mfma_f32_16x16x32_bf16 a[40:43], v[40:43], v[8:11], a[40:43]\n"
        "global_load_lds_dwordx4 v237, s[86:87] offset:3072\n"
        "s_add_u32 m0, s73, 0x20000\n"
        "s_waitcnt vmcnt(22)\n"
        "v_mfma_f32_16x16x32_bf16 a[44:47], v[44:47], v[8:11], 0\n"
        "s_barrier\n"
        "v_mfma_f32_16x16x32_bf16 a[44:47], v[76:79], v[12:15], a[44:47]\n"
        "ds_read_b64_tr_b16 v[48:49], v226 offset:16384\n"
        "s_and_b32 s91, s43, 15\n"
        "v_cmp_eq_u32 s[78:79], 0, v167\n"
        "v_mfma_f32_16x16x32_bf16 a[12:15], v[76:79], v[4:7], 0\n"
        "ds_read_b64_tr_b16 v[50:51], v227 offset:16384\n"
        "v_mov_b32 v171, 6144\n"
        "v_mfma_f32_16x16x32_bf16 a[12:15], v[44:47], v[0:3], a[12:15]\n"
        "global_load_lds_dwordx4 v238, s[60:61] offset:0\n"
        "s_waitcnt lgkmcnt(2)\n"
        "v_mfma_f32_16x16x32_bf16 a[128:131], v[32:35], v[16:19], 0\n"
        "ds_read_b64_tr_b16 v[80:81], v226 offset:24576\n"
        "v_cndmask_b32 v170, 0, v171, s[78:79]\n"
        "v_mfma_f32_16x16x32_bf16 a[128:131], v[64:67], v[20:23], a[128:131]\n"
        "ds_read_b64_tr_b16 v[82:83], v227 offset:24576\n"
        "v_add_u32 v170, v170, v169\n"
        "v_mfma_f32_16x16x32_bf16 a[160:163], v[64:67], v[28:31], 0\n"
        "ds_read_b64_tr_b16 v[52:53], v228 offset:16384\n"
        "v_sub_u32 v171, 2040, v167\n"
        "v_mfma_f32_16x16x32_bf16 a[160:163], v[32:35], v[24:27], a[160:163]\n"
        "global_load_dword v253, v173, s[82:83] offset:0\n"
        "ds_read_b64_tr_b16 v[54:55], v229 offset:16384\n"
        "v_mfma_f32_16x16x32_bf16 a[164:167], v[36:39], v[24:27], 0\n"
        "ds_read_b64_tr_b16 v[84:85], v228 offset:24576\n"
        "global_load_lds_dwordx4 v239, s[60:61] offset:1024\n"
        "v_mfma_f32_16x16x32_bf16 a[164:167], v[68:71], v[28:31], a[164:167]\n"
        "ds_read_b64_tr_b16 v[86:87], v229 offset:24576\n"
        "v_lshlrev_b32 v171, 1, v171\n"
        "v_mfma_f32_16x16x32_bf16 a[132:135], v[68:71], v[20:23], 0\n"
        "ds_read_b64_tr_b16 v[56:57], v230 offset:16384\n"
        "v_lshl_add_u32 v171, v168, 12, v171\n"
        "v_mfma_f32_16x16x32_bf16 a[132:135], v[36:39], v[16:19], a[132:135]\n"
        "ds_read_b64_tr_b16 v[58:59], v231 offset:16384\n"
        "v_lshlrev_b32 v168, 3, v168\n"
        "v_mfma_f32_16x16x32_bf16 a[136:139], v[40:43], v[16:19], 0\n"
        "ds_read_b64_tr_b16 v[88:89], v230 offset:24576\n"
        "global_load_lds_dwordx4 v240, s[60:61] offset:2048\n"
        "v_mfma_f32_16x16x32_bf16 a[136:139], v[72:75], v[20:23], a[136:139]\n"
        "ds_read_b64_tr_b16 v[90:91], v231 offset:24576\n"
        "s_add_u32 s82, s12, 0x100000\n"
        "s_addc_u32 s83, s13, 0\n"
        "v_mfma_f32_16x16x32_bf16 a[168:171], v[72:75], v[28:31], 0\n"
        "ds_read_b64_tr_b16 v[60:61], v232 offset:16384\n"
        "s_add_u32 s86, s60, 0x80000\n"
        "s_addc_u32 s87, s61, 0\n"
        "v_mfma_f32_16x16x32_bf16 a[168:171], v[40:43], v[24:27], a[168:171]\n"
        "ds_read_b64_tr_b16 v[62:63], v233 offset:16384\n"
        "s_cmp_lt_u32 s43, 16\n"
        "s_cselect_b32 s90, s44, s45\n"
        "v_mfma_f32_16x16x32_bf16 a[172:175], v[44:47], v[24:27], 0\n"
        "ds_read_b64_tr_b16 v[92:93], v232 offset:24576\n"
        "global_load_dwordx4 v[158:161], v254, s[30:31] offset:0\n"
        "s_lshl4_add_u32 s90, s90, s91\n"
        "v_mfma_f32_16x16x32_bf16 a[172:175], v[76:79], v[28:31], a[172:175]\n"
        "ds_read_b64_tr_b16 v[94:95], v233 offset:24576\n"
        "s_lshl2_add_u32 s90, s90, s40\n"
        "s_lshl_b32 s90, s90, 12\n"
        "v_mfma_f32_16x16x32_bf16 a[140:143], v[76:79], v[20:23], 0\n"
        "s_add_u32 s92, s8, s90\n"
        "s_addc_u32 s93, s9, 0\n"
        "s_lshl_b32 s91, s44, 5\n"
        "v_mfma_f32_16x16x32_bf16 a[140:143], v[44:47], v[16:19], a[140:143]\n"
        "s_add_u32 s91, s91, s43\n"
        "s_lshl2_add_u32 s91, s91, s40\n"
        "s_waitcnt lgkmcnt(6)\n"
        "v_mfma_f32_16x16x32_bf16 a[16:19], v[0:3], v[48:51], 0\n"
        "global_load_lds_dwordx4 v241, s[60:61] offset:3072\n"
        "s_add_u32 m0, s73, 0x24000\n"
        "v_mfma_f32_16x16x32_bf16 a[16:19], v[4:7], v[80:83], a[16:19]\n"
        "s_lshl_b32 s80, s91, 1\n"
        "s_add_u32 s81, s80, 1\n"
        "s_lshl_b32 s80, s80, 7\n"
        "v_mfma_f32_16x16x32_bf16 a[48:51], v[12:15], v[80:83], 0\n"
        "s_add_u32 s76, s38, 0x3301000\n"
        "s_addc_u32 s77, s39, 0\n"
        "s_lshl_b32 s81, s81, 7\n"
        "v_mfma_f32_16x16x32_bf16 a[48:51], v[8:11], v[48:51], a[48:51]\n"
        "v_mfma_f32_16x16x32_bf16 a[52:55], v[8:11], v[52:55], 0\n"
        "global_load_lds_dwordx4 v238, s[62:63] offset:0\n"
        "v_mfma_f32_16x16x32_bf16 a[52:55], v[12:15], v[84:87], a[52:55]\n"
        "s_waitcnt lgkmcnt(0)\n"
        "v_mfma_f32_16x16x32_bf16 a[20:23], v[4:7], v[84:87], 0\n"
        "v_mfma_f32_16x16x32_bf16 a[20:23], v[0:3], v[52:55], a[20:23]\n"
        "v_mfma_f32_16x16x32_bf16 a[24:27], v[0:3], v[56:59], 0\n"
        "global_load_dwordx4 v[162:165], v254, s[30:31] offset:1024\n"
        "v_mfma_f32_16x16x32_bf16 a[24:27], v[4:7], v[88:91], a[24:27]\n"
        "v_mfma_f32_16x16x32_bf16 a[56:59], v[12:15], v[88:91], 0\n"
        "v_mfma_f32_16x16x32_bf16 a[56:59], v[8:11], v[56:59], a[56:59]\n"
        "v_mfma_f32_16x16x32_bf16 a[60:63], v[8:11], v[60:63], 0\n"
        "global_load_lds_dwordx4 v239, s[62:63] offset:1024\n"
        "v_mfma_f32_16x16x32_bf16 a[60:63], v[12:15], v[92:95], a[60:63]\n"
        "v_mfma_f32_16x16x32_bf16 a[28:31], v[4:7], v[92:95], 0\n"
        "v_mfma_f32_16x16x32_bf16 a[28:31], v[0:3], v[60:63], a[28:31]\n"
        "s_waitcnt vmcnt(27)\n"
        "v_mfma_f32_16x16x32_bf16 a[144:147], v[16:19], v[48:51], 0\n"
        "global_load_lds_dwordx4 v240, s[62:63] offset:2048\n"
        "v_mfma_f32_16x16x32_bf16 a[144:147], v[20:23], v[80:83], a[144:147]\n"
        "s_barrier\n"
        "v_mfma_f32_16x16x32_bf16 a[176:179], v[28:31], v[80:83], 0\n"
        "ds_read_b128 v[0:3], v224 offset:20480\n"
        "v_mfma_f32_16x16x32_bf16 a[176:179], v[24:27], v[48:51], a[176:179]\n"
        "ds_read_b128 v[4:7], v225 offset:20480\n"
        "v_mfma_f32_16x16x32_bf16 a[180:183], v[24:27], v[52:55], 0\n"
        "global_load_ushort v172, v172, s[28:29]\n"
        "v_mfma_f32_16x16x32_bf16 a[180:183], v[28:31], v[84:87], a[180:183]\n"
        "global_load_lds_dwordx4 v241, s[62:63] offset:3072\n"
        "ds_read_b128 v[8:11], v224 offset:22528\n"
        "s_add_u32 m0, s72, 0x0\n"
        "v_mfma_f32_16x16x32_bf16 a[148:151], v[20:23], v[84:87], 0\n"
        "ds_read_b128 v[12:15], v225 offset:22528\n"
        "v_mfma_f32_16x16x32_bf16 a[148:151], v[16:19], v[52:55], a[148:151]\n"
        "v_mfma_f32_16x16x32_bf16 a[152:155], v[16:19], v[56:59], 0\n"
        "v_mfma_f32_16x16x32_bf16 a[152:155], v[20:23], v[88:91], a[152:155]\n"
        "global_load_ushort v198, v198, s[26:27]\n"
        "v_mfma_f32_16x16x32_bf16 a[184:187], v[28:31], v[88:91], 0\n"
        "global_load_dwordx4 v[134:137], v168, s[24:25] offset:0\n"
        "v_mfma_f32_16x16x32_bf16 a[184:187], v[24:27], v[56:59], a[184:187]\n"
        "v_mfma_f32_16x16x32_bf16 a[188:191], v[24:27], v[60:63], 0\n"
        "ds_read_b128 v[24:27], v224 offset:6144\n"
        "s_waitcnt lgkmcnt(3)\n"
        "v_mfma_f32_16x16x32_bf16 a[188:191], v[28:31], v[92:95], a[188:191]\n"
        "ds_read_b128 v[28:31], v225 offset:6144\n"
        "v_mfma_f32_16x16x32_bf16 a[156:159], v[20:23], v[92:95], 0\n"
        "ds_read_b128 v[20:23], v225 offset:4096\n"
        "s_waitcnt lgkmcnt(3)\n"
        "v_mfma_f32_16x16x32_bf16 a[156:159], v[16:19], v[60:63], a[156:159]\n"
        "ds_read_b128 v[16:19], v224 offset:4096\n"
        "v_mfma_f32_16x16x32_bf16 a[64:67], v[32:35], v[0:3], 0\n"
        "v_mfma_f32_16x16x32_bf16 a[64:67], v[64:67], v[4:7], a[64:67]\n"
        "v_mfma_f32_16x16x32_bf16 a[96:99], v[64:67], v[12:15], 0\n"
        "v_mfma_f32_16x16x32_bf16 a[96:99], v[32:35], v[8:11], a[96:99]\n"
        "v_mfma_f32_16x16x32_bf16 a[100:103], v[36:39], v[8:11], 0\n"
        "v_mfma_f32_16x16x32_bf16 a[100:103], v[68:71], v[12:15], a[100:103]\n"
        "v_mfma_f32_16x16x32_bf16 a[68:71], v[68:71], v[4:7], 0\n"
        "v_mfma_f32_16x16x32_bf16 a[68:71], v[36:39], v[0:3], a[68:71]\n"
        "global_load_dwordx4 v[138:141], v168, s[24:25] offset:16\n"
        "v_mfma_f32_16x16x32_bf16 a[72:75], v[40:43], v[0:3], 0\n"
        "v_mfma_f32_16x16x32_bf16 a[72:75], v[72:75], v[4:7], a[72:75]\n"
        "v_mfma_f32_16x16x32_bf16 a[104:107], v[72:75], v[12:15], 0\n"
        "v_mfma_f32_16x16x32_bf16 a[104:107], v[40:43], v[8:11], a[104:107]\n"
        "v_mfma_f32_16x16x32_bf16 a[108:111], v[44:47], v[8:11], 0\n"
        "v_mfma_f32_16x16x32_bf16 a[108:111], v[76:79], v[12:15], a[108:111]\n"
        "v_mfma_f32_16x16x32_bf16 a[76:79], v[76:79], v[4:7], 0\n"
        "v_mfma_f32_16x16x32_bf16 a[76:79], v[44:47], v[0:3], a[76:79]\n"
        "v_mfma_f32_16x16x32_bf16 a[80:83], v[0:3], v[48:51], 0\n"
        "v_mfma_f32_16x16x32_bf16 a[80:83], v[4:7], v[80:83], a[80:83]\n"
        "v_mfma_f32_16x16x32_bf16 a[112:115], v[12:15], v[80:83], 0\n"
        "v_mfma_f32_16x16x32_bf16 a[112:115], v[8:11], v[48:51], a[112:115]\n"
        "v_mfma_f32_16x16x32_bf16 a[116:119], v[8:11], v[52:55], 0\n"
        "v_mfma_f32_16x16x32_bf16 a[116:119], v[12:15], v[84:87], a[116:119]\n"
        "v_mfma_f32_16x16x32_bf16 a[84:87], v[4:7], v[84:87], 0\n"
        "v_mfma_f32_16x16x32_bf16 a[84:87], v[0:3], v[52:55], a[84:87]\n"
        "global_load_dwordx2 v[96:97], v170, s[74:75] offset:-4096\n"
        "v_mfma_f32_16x16x32_bf16 a[88:91], v[0:3], v[56:59], 0\n"
        "v_mfma_f32_16x16x32_bf16 a[88:91], v[4:7], v[88:91], a[88:91]\n"
        "v_mfma_f32_16x16x32_bf16 a[120:123], v[12:15], v[88:91], 0\n"
        "v_mfma_f32_16x16x32_bf16 a[120:123], v[8:11], v[56:59], a[120:123]\n"
        "s_waitcnt vmcnt(16) lgkmcnt(0)\n"
        "v_mfma_f32_16x16x32_bf16 a[124:127], v[8:11], v[60:63], 0\n"
        "s_barrier\n"
        "v_mfma_f32_16x16x32_bf16 a[124:127], v[12:15], v[92:95], a[124:127]\n"
        "global_load_lds_dwordx4 v234, s[56:57] offset:0\n"
        "ds_read_b128 v[8:11], v224 offset:51200\n"
        "v_mfma_f32_16x16x32_bf16 a[92:95], v[4:7], v[92:95], 0\n"
        "ds_read_b128 v[4:7], v225 offset:49152\n"
        "v_mfma_f32_16x16x32_bf16 a[92:95], v[0:3], v[60:63], a[92:95]\n"
        "ds_read_b128 v[0:3], v224 offset:49152\n"
        "v_mfma_f32_16x16x32_bf16 a[192:195], v[32:35], v[16:19], 0\n"
        "ds_read_b128 v[12:15], v225 offset:51200\n"
        "v_mfma_f32_16x16x32_bf16 a[192:195], v[64:67], v[20:23], a[192:195]\n"
        "global_load_lds_dwordx4 v235, s[56:57] offset:1024\n"
        "v_mfma_f32_16x16x32_bf16 a[224:227], v[64:67], v[28:31], 0\n"
        "ds_read_b64_tr_b16 v[64:65], v226 offset:40960\n"
        "v_mfma_f32_16x16x32_bf16 a[224:227], v[32:35], v[24:27], a[224:227]\n"
        "ds_read_b64_tr_b16 v[32:33], v226 offset:32768\n"
        "v_mfma_f32_16x16x32_bf16 a[228:231], v[36:39], v[24:27], 0\n"
        "ds_read_b64_tr_b16 v[34:35], v227 offset:32768\n"
        "v_mfma_f32_16x16x32_bf16 a[228:231], v[68:71], v[28:31], a[228:231]\n"
        "ds_read_b64_tr_b16 v[66:67], v227 offset:40960\n"
        "global_load_lds_dwordx4 v236, s[56:57] offset:2048\n"
        "v_mfma_f32_16x16x32_bf16 a[196:199], v[68:71], v[20:23], 0\n"
        "ds_read_b64_tr_b16 v[68:69], v228 offset:40960\n"
        "v_mfma_f32_16x16x32_bf16 a[196:199], v[36:39], v[16:19], a[196:199]\n"
        "ds_read_b64_tr_b16 v[36:37], v228 offset:32768\n"
        "v_mfma_f32_16x16x32_bf16 a[200:203], v[40:43], v[16:19], 0\n"
        "ds_read_b64_tr_b16 v[38:39], v229 offset:32768\n"
        "v_mfma_f32_16x16x32_bf16 a[200:203], v[72:75], v[20:23], a[200:203]\n"
        "ds_read_b64_tr_b16 v[70:71], v229 offset:40960\n"
        "global_load_lds_dwordx4 v237, s[56:57] offset:3072\n"
        "s_add_u32 m0, s72, 0x1000\n"
        "v_mfma_f32_16x16x32_bf16 a[232:235], v[72:75], v[28:31], 0\n"
        "ds_read_b64_tr_b16 v[72:73], v230 offset:40960\n"
        "v_mfma_f32_16x16x32_bf16 a[232:235], v[40:43], v[24:27], a[232:235]\n"
        "ds_read_b64_tr_b16 v[40:41], v230 offset:32768\n"
        "v_mfma_f32_16x16x32_bf16 a[236:239], v[44:47], v[24:27], 0\n"
        "ds_read_b64_tr_b16 v[42:43], v231 offset:32768\n"
        "v_mfma_f32_16x16x32_bf16 a[236:239], v[76:79], v[28:31], a[236:239]\n"
        "ds_read_b64_tr_b16 v[74:75], v231 offset:40960\n"
        "global_load_dwordx2 v[98:99], v170, s[74:75] offset:-2048\n"
        "v_mfma_f32_16x16x32_bf16 a[204:207], v[76:79], v[20:23], 0\n"
        "ds_read_b64_tr_b16 v[76:77], v232 offset:40960\n"
        "global_load_lds_dwordx4 v234, s[58:59] offset:0\n"
        "v_mfma_f32_16x16x32_bf16 a[204:207], v[44:47], v[16:19], a[204:207]\n"
        "ds_read_b64_tr_b16 v[44:45], v232 offset:32768\n"
        "v_mfma_f32_16x16x32_bf16 a[208:211], v[16:19], v[48:51], 0\n"
        "ds_read_b64_tr_b16 v[46:47], v233 offset:32768\n"
        "v_mfma_f32_16x16x32_bf16 a[208:211], v[20:23], v[80:83], a[208:211]\n"
        "ds_read_b64_tr_b16 v[78:79], v233 offset:40960\n"
        "v_mfma_f32_16x16x32_bf16 a[240:243], v[28:31], v[80:83], 0\n"
        "ds_read_b64_tr_b16 v[80:81], v226 offset:57344\n"
        "global_load_lds_dwordx4 v235, s[58:59] offset:1024\n"
        "v_mfma_f32_16x16x32_bf16 a[240:243], v[24:27], v[48:51], a[240:243]\n"
        "ds_read_b64_tr_b16 v[48:49], v226 offset:49152\n"
        "v_mfma_f32_16x16x32_bf16 a[244:247], v[24:27], v[52:55], 0\n"
        "ds_read_b64_tr_b16 v[50:51], v227 offset:49152\n"
        "v_mfma_f32_16x16x32_bf16 a[244:247], v[28:31], v[84:87], a[244:247]\n"
        "ds_read_b64_tr_b16 v[82:83], v227 offset:57344\n"
        "v_mfma_f32_16x16x32_bf16 a[212:215], v[20:23], v[84:87], 0\n"
        "ds_read_b64_tr_b16 v[84:85], v228 offset:57344\n"
        "global_load_lds_dwordx4 v236, s[58:59] offset:2048\n"
        "v_mfma_f32_16x16x32_bf16 a[212:215], v[16:19], v[52:55], a[212:215]\n"
        "ds_read_b64_tr_b16 v[52:53], v228 offset:49152\n"
        "v_mfma_f32_16x16x32_bf16 a[216:219], v[16:19], v[56:59], 0\n"
        "ds_read_b64_tr_b16 v[54:55], v229 offset:49152\n"
        "v_mfma_f32_16x16x32_bf16 a[216:219], v[20:23], v[88:91], a[216:219]\n"
        "ds_read_b64_tr_b16 v[86:87], v229 offset:57344\n"
        "v_mfma_f32_16x16x32_bf16 a[248:251], v[28:31], v[88:91], 0\n"
        "ds_read_b64_tr_b16 v[88:89], v230 offset:57344\n"
        "global_load_lds_dwordx4 v237, s[58:59] offset:3072\n"
        "s_add_u32 m0, s73, 0x10000\n"
        "v_mfma_f32_16x16x32_bf16 a[248:251], v[24:27], v[56:59], a[248:251]\n"
        "ds_read_b64_tr_b16 v[56:57], v230 offset:49152\n"
        "v_mfma_f32_16x16x32_bf16 a[252:255], v[24:27], v[60:63], 0\n"
        "ds_read_b64_tr_b16 v[58:59], v231 offset:49152\n"
        "s_waitcnt lgkmcnt(8)\n"
        "v_mfma_f32_16x16x32_bf16 a[252:255], v[28:31], v[92:95], a[252:255]\n"
        "ds_read_b64_tr_b16 v[90:91], v231 offset:57344\n"
        "v_mfma_f32_16x16x32_bf16 a[220:223], v[20:23], v[92:95], 0\n"
        "ds_read_b64_tr_b16 v[92:93], v232 offset:57344\n"
        "global_load_dwordx2 v[100:101], v170, s[74:75] offset:0\n"
        "v_mfma_f32_16x16x32_bf16 a[220:223], v[16:19], v[60:63], a[220:223]\n"
        "ds_read_b64_tr_b16 v[60:61], v232 offset:49152\n"
        "v_mfma_f32_16x16x32_bf16 a[0:3], v[32:35], v[0:3], a[0:3]\n"
        "ds_read_b64_tr_b16 v[62:63], v233 offset:49152\n"
        "v_mfma_f32_16x16x32_bf16 a[0:3], v[64:67], v[4:7], a[0:3]\n"
        "ds_read_b64_tr_b16 v[94:95], v233 offset:57344\n"
        "v_mfma_f32_16x16x32_bf16 a[32:35], v[64:67], v[12:15], a[32:35]\n"
        "ds_read_b128 v[16:19], v224 offset:53248\n"
        "v_mfma_f32_16x16x32_bf16 a[32:35], v[32:35], v[8:11], a[32:35]\n"
        "ds_read_b128 v[20:23], v225 offset:53248\n"
        "v_mfma_f32_16x16x32_bf16 a[36:39], v[36:39], v[8:11], a[36:39]\n"
        "ds_read_b128 v[24:27], v224 offset:55296\n"
        "v_mfma_f32_16x16x32_bf16 a[36:39], v[68:71], v[12:15], a[36:39]\n"
        "ds_read_b128 v[28:31], v225 offset:55296\n"
        "v_mfma_f32_16x16x32_bf16 a[4:7], v[68:71], v[4:7], a[4:7]\n"
        "v_mfma_f32_16x16x32_bf16 a[4:7], v[36:39], v[0:3], a[4:7]\n"
        "global_load_dwordx2 v[102:103], v169, s[74:75] offset:2048\n"
        "s_add_u32 s74, s20, 0x1800\n"
        "s_addc_u32 s75, s21, 0\n"
        "v_mfma_f32_16x16x32_bf16 a[8:11], v[40:43], v[0:3], a[8:11]\n"
        "v_mfma_f32_16x16x32_bf16 a[8:11], v[72:75], v[4:7], a[8:11]\n"
        "v_mfma_f32_16x16x32_bf16 a[40:43], v[72:75], v[12:15], a[40:43]\n"
        "v_mfma_f32_16x16x32_bf16 a[40:43], v[40:43], v[8:11], a[40:43]\n"
        "v_mfma_f32_16x16x32_bf16 a[44:47], v[44:47], v[8:11], a[44:47]\n"
        "v_mfma_f32_16x16x32_bf16 a[44:47], v[76:79], v[12:15], a[44:47]\n"
        "s_waitcnt lgkmcnt(0)\n"
        "v_mfma_f32_16x16x32_bf16 a[12:15], v[76:79], v[4:7], a[12:15]\n"
        "v_mfma_f32_16x16x32_bf16 a[12:15], v[44:47], v[0:3], a[12:15]\n"
        "global_load_dword v253, v173, s[82:83] offset:0\n"
        "s_add_u32 s82, s12, 0x200000\n"
        "s_addc_u32 s83, s13, 0\n"
        "v_mfma_f32_16x16x32_bf16 a[16:19], v[0:3], v[48:51], a[16:19]\n"
        "v_mfma_f32_16x16x32_bf16 a[16:19], v[4:7], v[80:83], a[16:19]\n"
        "v_mfma_f32_16x16x32_bf16 a[48:51], v[12:15], v[80:83], a[48:51]\n"
        "v_mfma_f32_16x16x32_bf16 a[48:51], v[8:11], v[48:51], a[48:51]\n"
        "v_mfma_f32_16x16x32_bf16 a[52:55], v[8:11], v[52:55], a[52:55]\n"
        "v_mfma_f32_16x16x32_bf16 a[52:55], v[12:15], v[84:87], a[52:55]\n"
        "v_mfma_f32_16x16x32_bf16 a[20:23], v[4:7], v[84:87], a[20:23]\n"
        "v_mfma_f32_16x16x32_bf16 a[20:23], v[0:3], v[52:55], a[20:23]\n"
        "global_load_dwordx2 v[104:105], v169, s[74:75] offset:-4096\n"
        "v_mfma_f32_16x16x32_bf16 a[24:27], v[0:3], v[56:59], a[24:27]\n"
        "v_mfma_f32_16x16x32_bf16 a[24:27], v[4:7], v[88:91], a[24:27]\n"
        "v_mfma_f32_16x16x32_bf16 a[56:59], v[12:15], v[88:91], a[56:59]\n"
        "v_mfma_f32_16x16x32_bf16 a[56:59], v[8:11], v[56:59], a[56:59]\n"
        "v_mfma_f32_16x16x32_bf16 a[60:63], v[8:11], v[60:63], a[60:63]\n"
        "ds_read_b128 v[8:11], v224 offset:34816\n"
        "v_mfma_f32_16x16x32_bf16 a[60:63], v[12:15], v[92:95], a[60:63]\n"
        "ds_read_b128 v[12:15], v225 offset:34816\n"
        "v_mfma_f32_16x16x32_bf16 a[28:31], v[4:7], v[92:95], a[28:31]\n"
        "ds_read_b128 v[4:7], v225 offset:32768\n"
        "v_mfma_f32_16x16x32_bf16 a[28:31], v[0:3], v[60:63], a[28:31]\n"
        "global_load_lds_dwordx4 v238, s[86:87] offset:0\n"
        "ds_read_b128 v[0:3], v224 offset:32768\n"
        "v_mfma_f32_16x16x32_bf16 a[64:67], v[32:35], v[16:19], a[64:67]\n"
        "v_mfma_f32_16x16x32_bf16 a[64:67], v[64:67], v[20:23], a[64:67]\n"
        "v_mfma_f32_16x16x32_bf16 a[96:99], v[64:67], v[28:31], a[96:99]\n"
        "v_mfma_f32_16x16x32_bf16 a[96:99], v[32:35], v[24:27], a[96:99]\n"
        "v_mfma_f32_16x16x32_bf16 a[100:103], v[36:39], v[24:27], a[100:103]\n"
        "v_mfma_f32_16x16x32_bf16 a[100:103], v[68:71], v[28:31], a[100:103]\n"
        "global_load_lds_dwordx4 v239, s[86:87] offset:1024\n"
        "v_mfma_f32_16x16x32_bf16 a[68:71], v[68:71], v[20:23], a[68:71]\n"
        "v_mfma_f32_16x16x32_bf16 a[68:71], v[36:39], v[16:19], a[68:71]\n"
        "v_mfma_f32_16x16x32_bf16 a[72:75], v[40:43], v[16:19], a[72:75]\n"
        "v_mfma_f32_16x16x32_bf16 a[72:75], v[72:75], v[20:23], a[72:75]\n"
        "global_load_dwordx2 v[106:107], v169, s[74:75] offset:-2048\n"
        "v_mfma_f32_16x16x32_bf16 a[104:107], v[72:75], v[28:31], a[104:107]\n"
        "v_mfma_f32_16x16x32_bf16 a[104:107], v[40:43], v[24:27], a[104:107]\n"
        "global_load_lds_dwordx4 v240, s[86:87] offset:2048\n"
        "v_mfma_f32_16x16x32_bf16 a[108:111], v[44:47], v[24:27], a[108:111]\n"
        "v_mfma_f32_16x16x32_bf16 a[108:111], v[76:79], v[28:31], a[108:111]\n"
        "LHA0_%=:\n"
        "s_cbranch_execz LHA4_%=\n"
        "v_mfma_f32_16x16x32_bf16 a[76:79], v[76:79], v[20:23], a[76:79]\n"
        "v_mfma_f32_16x16x32_bf16 a[76:79], v[44:47], v[16:19], a[76:79]\n"
        "v_mfma_f32_16x16x32_bf16 a[80:83], v[16:19], v[48:51], a[80:83]\n"
        "v_mfma_f32_16x16x32_bf16 a[80:83], v[20:23], v[80:83], a[80:83]\n"
        "global_load_lds_dwordx4 v241, s[86:87] offset:3072\n"
        "s_add_u32 m0, s73, 0x14000\n"
        "v_mfma_f32_16x16x32_bf16 a[112:115], v[28:31], v[80:83], a[112:115]\n"
        "s_add_u32 s86, s62, 0x80000\n"
        "s_addc_u32 s87, s63, 0\n"
        "v_mfma_f32_16x16x32_bf16 a[112:115], v[24:27], v[48:51], a[112:115]\n"
        "v_mfma_f32_16x16x32_bf16 a[116:119], v[24:27], v[52:55], a[116:119]\n"
        "v_mfma_f32_16x16x32_bf16 a[116:119], v[28:31], v[84:87], a[116:119]\n"
        "v_mfma_f32_16x16x32_bf16 a[84:87], v[20:23], v[84:87], a[84:87]\n"
        "s_waitcnt vmcnt(21) lgkmcnt(0)\n"
        "v_lshl_or_b32 v172, v172, 16, v198\n"
        "v_mfma_f32_16x16x32_bf16 a[84:87], v[16:19], v[52:55], a[84:87]\n"
        "global_load_lds_dwordx4 v238, s[86:87] offset:0\n"
        "v_readfirstlane_b32 s54, v172\n"
        "v_mfma_f32_16x16x32_bf16 a[88:91], v[16:19], v[56:59], a[88:91]\n"
        "s_waitcnt vmcnt(21)\n"
        "v_lshlrev_b32 v185, 16, v134\n"
        "v_and_b32 v186, s35, v134\n"
        "v_mfma_f32_16x16x32_bf16 a[88:91], v[20:23], v[88:91], a[88:91]\n"
        "v_lshlrev_b32 v187, 16, v135\n"
        "v_and_b32 v188, s35, v135\n"
        "v_mfma_f32_16x16x32_bf16 a[120:123], v[28:31], v[88:91], a[120:123]\n"
        "v_mfma_f32_16x16x32_bf16 a[120:123], v[24:27], v[56:59], a[120:123]\n"
        "global_load_dwordx2 v[108:109], v169, s[74:75] offset:0\n"
        "v_mfma_f32_16x16x32_bf16 a[124:127], v[24:27], v[60:63], a[124:127]\n"
        "ds_read_b128 v[24:27], v224 offset:38912\n"
        "v_mfma_f32_16x16x32_bf16 a[124:127], v[28:31], v[92:95], a[124:127]\n"
        "global_load_lds_dwordx4 v239, s[86:87] offset:1024\n"
        "ds_read_b128 v[28:31], v225 offset:38912\n"
        "v_mfma_f32_16x16x32_bf16 a[92:95], v[20:23], v[92:95], a[92:95]\n"
        "ds_read_b128 v[20:23], v225 offset:36864\n"
        "v_mfma_f32_16x16x32_bf16 a[92:95], v[16:19], v[60:63], a[92:95]\n"
        "ds_read_b128 v[16:19], v224 offset:36864\n"
        "v_mfma_f32_16x16x32_bf16 a[128:131], v[32:35], v[0:3], a[128:131]\n"
        "v_mfma_f32_16x16x32_bf16 a[128:131], v[64:67], v[4:7], a[128:131]\n"
        "v_mfma_f32_16x16x32_bf16 a[160:163], v[64:67], v[12:15], a[160:163]\n"
        "v_mfma_f32_16x16x32_bf16 a[160:163], v[32:35], v[8:11], a[160:163]\n"
        "global_load_lds_dwordx4 v240, s[86:87] offset:2048\n"
        "v_mfma_f32_16x16x32_bf16 a[164:167], v[36:39], v[8:11], a[164:167]\n"
        "v_mfma_f32_16x16x32_bf16 a[164:167], v[68:71], v[12:15], a[164:167]\n"
        "v_mfma_f32_16x16x32_bf16 a[132:135], v[68:71], v[4:7], a[132:135]\n"
        "v_mfma_f32_16x16x32_bf16 a[132:135], v[36:39], v[0:3], a[132:135]\n"
        "global_load_dwordx2 v[110:111], v169, s[74:75] offset:2048\n"
        "s_add_u32 s74, s20, 0x3800\n"
        "s_addc_u32 s75, s21, 0\n"
        "v_mfma_f32_16x16x32_bf16 a[136:139], v[40:43], v[0:3], a[136:139]\n"
        "v_mfma_f32_16x16x32_bf16 a[136:139], v[72:75], v[4:7], a[136:139]\n"
        "global_load_lds_dwordx4 v241, s[86:87] offset:3072\n"
        "s_add_u32 m0, s72, 0x8000\n"
        "v_mfma_f32_16x16x32_bf16 a[168:171], v[72:75], v[12:15], a[168:171]\n"
        "s_add_u32 s86, s56, 0x80\n"
        "s_addc_u32 s87, s57, 0\n"
        "v_mfma_f32_16x16x32_bf16 a[168:171], v[40:43], v[8:11], a[168:171]\n"
        "v_mfma_f32_16x16x32_bf16 a[172:175], v[44:47], v[8:11], a[172:175]\n"
        "v_mfma_f32_16x16x32_bf16 a[172:175], v[76:79], v[12:15], a[172:175]\n"
        "v_mfma_f32_16x16x32_bf16 a[140:143], v[76:79], v[4:7], a[140:143]\n"
        "v_mfma_f32_16x16x32_bf16 a[140:143], v[44:47], v[0:3], a[140:143]\n"
        "v_mfma_f32_16x16x32_bf16 a[144:147], v[0:3], v[48:51], a[144:147]\n"
        "v_mfma_f32_16x16x32_bf16 a[144:147], v[4:7], v[80:83], a[144:147]\n"
        "v_mfma_f32_16x16x32_bf16 a[176:179], v[12:15], v[80:83], a[176:179]\n"
        "v_mfma_f32_16x16x32_bf16 a[176:179], v[8:11], v[48:51], a[176:179]\n"
        "v_mfma_f32_16x16x32_bf16 a[180:183], v[8:11], v[52:55], a[180:183]\n"
        "s_waitcnt vmcnt(24) lgkmcnt(0)\n"
        "v_cndmask_b32 v96, v96, 0, s[78:79]\n"
        "v_cndmask_b32 v97, v97, 0, s[78:79]\n"
        "v_mfma_f32_16x16x32_bf16 a[180:183], v[12:15], v[84:87], a[180:183]\n"
        "v_lshlrev_b32 v174, 16, v96\n"
        "v_mul_f32 v189, v174, v188\n"
        "v_mfma_f32_16x16x32_bf16 a[148:151], v[4:7], v[84:87], a[148:151]\n"
        "v_cvt_pk_bf16_f32 v189, 0, v189\n"
        "v_and_b32 v174, s35, v96\n"
        "v_mfma_f32_16x16x32_bf16 a[148:151], v[0:3], v[52:55], a[148:151]\n"
        "global_load_dwordx2 v[112:113], v169, s[74:75] offset:-4096\n"
        "v_mfma_f32_16x16x32_bf16 a[152:155], v[0:3], v[56:59], a[152:155]\n"
        "v_mfma_f32_16x16x32_bf16 a[152:155], v[4:7], v[88:91], a[152:155]\n"
        "v_mfma_f32_16x16x32_bf16 a[184:187], v[12:15], v[88:91], a[184:187]\n"
        "v_mfma_f32_16x16x32_bf16 a[184:187], v[8:11], v[56:59], a[184:187]\n"
        "s_waitcnt vmcnt(16)\n"
        "v_mfma_f32_16x16x32_bf16 a[188:191], v[8:11], v[60:63], a[188:191]\n"
        "s_barrier\n"
        "v_mfma_f32_16x16x32_bf16 a[188:191], v[12:15], v[92:95], a[188:191]\n"
        "global_load_lds_dwordx4 v234, s[86:87] offset:0\n"
        "ds_read_b128 v[8:11], v224 offset:18432\n"
        "v_mfma_f32_16x16x32_bf16 a[156:159], v[4:7], v[92:95], a[156:159]\n"
        "ds_read_b128 v[4:7], v225 offset:16384\n"
        "v_mfma_f32_16x16x32_bf16 a[156:159], v[0:3], v[60:63], a[156:159]\n"
        "ds_read_b128 v[0:3], v224 offset:16384\n"
        "v_mfma_f32_16x16x32_bf16 a[192:195], v[32:35], v[16:19], a[192:195]\n"
        "ds_read_b128 v[12:15], v225 offset:18432\n"
        "v_mfma_f32_16x16x32_bf16 a[192:195], v[64:67], v[20:23], a[192:195]\n"
        "global_load_lds_dwordx4 v235, s[86:87] offset:1024\n"
        "v_mfma_f32_16x16x32_bf16 a[224:227], v[64:67], v[28:31], a[224:227]\n"
        "ds_read_b64_tr_b16 v[64:65], v216 offset:8192\n"
        "v_mfma_f32_16x16x32_bf16 a[224:227], v[32:35], v[24:27], a[224:227]\n"
        "ds_read_b64_tr_b16 v[32:33], v216 offset:0\n"
        "v_mfma_f32_16x16x32_bf16 a[228:231], v[36:39], v[24:27], a[228:231]\n"
        "ds_read_b64_tr_b16 v[34:35], v217 offset:0\n"
        "v_mfma_f32_16x16x32_bf16 a[228:231], v[68:71], v[28:31], a[228:231]\n"
        "ds_read_b64_tr_b16 v[66:67], v217 offset:8192\n"
        "global_load_lds_dwordx4 v236, s[86:87] offset:2048\n"
        "v_mfma_f32_16x16x32_bf16 a[196:199], v[68:71], v[20:23], a[196:199]\n"
        "ds_read_b64_tr_b16 v[68:69], v218 offset:8192\n"
        "v_mfma_f32_16x16x32_bf16 a[196:199], v[36:39], v[16:19], a[196:199]\n"
        "ds_read_b64_tr_b16 v[36:37], v218 offset:0\n"
        "v_mfma_f32_16x16x32_bf16 a[200:203], v[40:43], v[16:19], a[200:203]\n"
        "ds_read_b64_tr_b16 v[38:39], v219 offset:0\n"
        "v_mfma_f32_16x16x32_bf16 a[200:203], v[72:75], v[20:23], a[200:203]\n"
        "ds_read_b64_tr_b16 v[70:71], v219 offset:8192\n"
        "global_load_lds_dwordx4 v237, s[86:87] offset:3072\n"
        "s_add_u32 m0, s72, 0x9000\n"
        "v_mfma_f32_16x16x32_bf16 a[232:235], v[72:75], v[28:31], a[232:235]\n"
        "ds_read_b64_tr_b16 v[72:73], v220 offset:8192\n"
        "s_add_u32 s86, s58, 0x80\n"
        "s_addc_u32 s87, s59, 0\n"
        "v_mfma_f32_16x16x32_bf16 a[232:235], v[40:43], v[24:27], a[232:235]\n"
        "ds_read_b64_tr_b16 v[40:41], v220 offset:0\n"
        "v_cndmask_b32 v98, v98, 0, s[78:79]\n"
        "v_mfma_f32_16x16x32_bf16 a[236:239], v[44:47], v[24:27], a[236:239]\n"
        "ds_read_b64_tr_b16 v[42:43], v221 offset:0\n"
        "v_cndmask_b32 v99, v99, 0, s[78:79]\n"
        "v_mfma_f32_16x16x32_bf16 a[236:239], v[76:79], v[28:31], a[236:239]\n"
        "ds_read_b64_tr_b16 v[74:75], v221 offset:8192\n"
        "global_load_dwordx2 v[114:115], v169, s[74:75] offset:-2048\n"
        "v_mfma_f32_16x16x32_bf16 a[204:207], v[76:79], v[20:23], a[204:207]\n"
        "ds_read_b64_tr_b16 v[76:77], v222 offset:8192\n"
        "global_load_lds_dwordx4 v234, s[86:87] offset:0\n"
        "v_mfma_f32_16x16x32_bf16 a[204:207], v[44:47], v[16:19], a[204:207]\n"
        "ds_read_b64_tr_b16 v[44:45], v222 offset:0\n"
        "v_lshlrev_b32 v175, 16, v98\n"
        "v_mfma_f32_16x16x32_bf16 a[208:211], v[16:19], v[48:51], a[208:211]\n"
        "ds_read_b64_tr_b16 v[46:47], v223 offset:0\n"
        "v_mul_f32 v191, v175, v188\n"
        "v_mfma_f32_16x16x32_bf16 a[208:211], v[20:23], v[80:83], a[208:211]\n"
        "ds_read_b64_tr_b16 v[78:79], v223 offset:8192\n"
        "v_cvt_pk_bf16_f32 v191, 0, v191\n"
        "v_mfma_f32_16x16x32_bf16 a[240:243], v[28:31], v[80:83], a[240:243]\n"
        "ds_read_b64_tr_b16 v[80:81], v216 offset:24576\n"
        "global_load_lds_dwordx4 v235, s[86:87] offset:1024\n"
        "v_mfma_f32_16x16x32_bf16 a[240:243], v[24:27], v[48:51], a[240:243]\n"
        "ds_read_b64_tr_b16 v[48:49], v216 offset:16384\n"
        "v_mul_f32 v190, v175, v187\n"
        "v_mfma_f32_16x16x32_bf16 a[244:247], v[24:27], v[52:55], a[244:247]\n"
        "ds_read_b64_tr_b16 v[50:51], v217 offset:16384\n"
        "v_cvt_pk_bf16_f32 v190, 0, v190\n"
        "v_mfma_f32_16x16x32_bf16 a[244:247], v[28:31], v[84:87], a[244:247]\n"
        "ds_read_b64_tr_b16 v[82:83], v217 offset:24576\n"
        "v_add_f32 v189, v189, v190\n"
        "v_mfma_f32_16x16x32_bf16 a[212:215], v[20:23], v[84:87], a[212:215]\n"
        "ds_read_b64_tr_b16 v[84:85], v218 offset:24576\n"
        "global_load_lds_dwordx4 v236, s[86:87] offset:2048\n"
        "v_mfma_f32_16x16x32_bf16 a[212:215], v[16:19], v[52:55], a[212:215]\n"
        "ds_read_b64_tr_b16 v[52:53], v218 offset:16384\n"
        "v_cvt_pk_bf16_f32 v189, 0, v189\n"
        "v_mfma_f32_16x16x32_bf16 a[216:219], v[16:19], v[56:59], a[216:219]\n"
        "ds_read_b64_tr_b16 v[54:55], v219 offset:16384\n"
        "v_and_b32 v175, s35, v98\n"
        "v_mfma_f32_16x16x32_bf16 a[216:219], v[20:23], v[88:91], a[216:219]\n"
        "ds_read_b64_tr_b16 v[86:87], v219 offset:24576\n"
        "v_mfma_f32_16x16x32_bf16 a[248:251], v[28:31], v[88:91], a[248:251]\n"
        "ds_read_b64_tr_b16 v[88:89], v220 offset:24576\n"
        "global_load_lds_dwordx4 v237, s[86:87] offset:3072\n"
        "s_add_u32 m0, s73, 0x18000\n"
        "v_mfma_f32_16x16x32_bf16 a[248:251], v[24:27], v[56:59], a[248:251]\n"
        "ds_read_b64_tr_b16 v[56:57], v220 offset:16384\n"
        "s_add_u32 s86, s60, 0x100000\n"
        "s_addc_u32 s87, s61, 0\n"
        "v_mfma_f32_16x16x32_bf16 a[252:255], v[24:27], v[60:63], a[252:255]\n"
        "ds_read_b64_tr_b16 v[58:59], v221 offset:16384\n"
        "s_waitcnt lgkmcnt(8)\n"
        "v_mfma_f32_16x16x32_bf16 a[252:255], v[28:31], v[92:95], a[252:255]\n"
        "ds_read_b64_tr_b16 v[90:91], v221 offset:24576\n"
        "s_waitcnt vmcnt(24) lgkmcnt(8)\n"
        "v_mfma_f32_16x16x32_bf16 a[220:223], v[20:23], v[92:95], a[220:223]\n"
        "ds_read_b64_tr_b16 v[92:93], v222 offset:24576\n"
        "global_load_dwordx2 v[116:117], v169, s[74:75] offset:0\n"
        "v_mfma_f32_16x16x32_bf16 a[220:223], v[16:19], v[60:63], a[220:223]\n"
        "ds_read_b64_tr_b16 v[60:61], v222 offset:16384\n"
        "s_add_u32 s74, s22, 0x1000\n"
        "s_addc_u32 s75, s23, 0\n"
        "v_mfma_f32_16x16x32_bf16 a[0:3], v[32:35], v[0:3], a[0:3]\n"
        "ds_read_b64_tr_b16 v[62:63], v223 offset:16384\n"
        "v_cndmask_b32 v100, v100, 0, s[78:79]\n"
        "v_mfma_f32_16x16x32_bf16 a[0:3], v[64:67], v[4:7], a[0:3]\n"
        "ds_read_b64_tr_b16 v[94:95], v223 offset:24576\n"
        "v_cndmask_b32 v101, v101, 0, s[78:79]\n"
        "v_mfma_f32_16x16x32_bf16 a[32:35], v[64:67], v[12:15], a[32:35]\n"
        "ds_read_b128 v[16:19], v224 offset:20480\n"
        "v_lshlrev_b32 v176, 16, v100\n"
        "v_mfma_f32_16x16x32_bf16 a[32:35], v[32:35], v[8:11], a[32:35]\n"
        "ds_read_b128 v[20:23], v225 offset:20480\n"
        "v_mul_f32 v192, v176, v187\n"
        "v_mfma_f32_16x16x32_bf16 a[36:39], v[36:39], v[8:11], a[36:39]\n"
        "ds_read_b128 v[24:27], v224 offset:22528\n"
        "v_cvt_pk_bf16_f32 v192, 0, v192\n"
        "v_mfma_f32_16x16x32_bf16 a[36:39], v[68:71], v[12:15], a[36:39]\n"
        "ds_read_b128 v[28:31], v225 offset:22528\n"
        "v_add_f32 v191, v191, v192\n"
        "v_mfma_f32_16x16x32_bf16 a[4:7], v[68:71], v[4:7], a[4:7]\n"
        "s_waitcnt vmcnt(24) lgkmcnt(7)\n"
        "v_lshlrev_b32 v177, 16, v102\n"
        "v_cvt_pk_bf16_f32 v191, 0, v191\n"
        "v_mfma_f32_16x16x32_bf16 a[4:7], v[36:39], v[0:3], a[4:7]\n"
        "global_load_dwordx2 v[118:119], v169, s[74:75] offset:-4096\n"
        "v_mul_f32 v190, v176, v186\n"
        "v_mfma_f32_16x16x32_bf16 a[8:11], v[40:43], v[0:3], a[8:11]\n"
        "v_mul_f32 v192, v177, v186\n"
        "v_cvt_pk_bf16_f32 v190, 0, v190\n"
        "v_mfma_f32_16x16x32_bf16 a[8:11], v[72:75], v[4:7], a[8:11]\n"
        "v_cvt_pk_bf16_f32 v192, 0, v192\n"
        "v_add_f32 v189, v189, v190\n"
        "v_mfma_f32_16x16x32_bf16 a[40:43], v[72:75], v[12:15], a[40:43]\n"
        "v_add_f32 v191, v191, v192\n"
        "v_cvt_pk_bf16_f32 v189, 0, v189\n"
        "v_mfma_f32_16x16x32_bf16 a[40:43], v[40:43], v[8:11], a[40:43]\n"
        "v_cvt_pk_bf16_f32 v191, 0, v191\n"
        "v_mul_f32 v190, v177, v185\n"
        "v_mfma_f32_16x16x32_bf16 a[44:47], v[44:47], v[8:11], a[44:47]\n"
        "v_cvt_pk_bf16_f32 v190, 0, v190\n"
        "v_add_f32 v189, v189, v190\n"
        "v_mfma_f32_16x16x32_bf16 a[44:47], v[76:79], v[12:15], a[44:47]\n"
        "v_cvt_pk_bf16_f32 v189, 0, v189\n"
        "v_mul_f32 v193, v176, v188\n"
        "v_mfma_f32_16x16x32_bf16 a[12:15], v[76:79], v[4:7], a[12:15]\n"
        "v_mul_f32 v195, v177, v188\n"
        "v_cvt_pk_bf16_f32 v193, 0, v193\n"
        "v_mfma_f32_16x16x32_bf16 a[12:15], v[44:47], v[0:3], a[12:15]\n"
        "global_load_dword v253, v173, s[82:83] offset:0\n"
        "s_add_u32 s82, s12, 0x300000\n"
        "s_addc_u32 s83, s13, 0\n"
        "v_mfma_f32_16x16x32_bf16 a[16:19], v[0:3], v[48:51], a[16:19]\n"
        "v_cvt_pk_bf16_f32 v195, 0, v195\n"
        "v_mul_f32 v194, v177, v187\n"
        "v_mfma_f32_16x16x32_bf16 a[16:19], v[4:7], v[80:83], a[16:19]\n"
        "v_cvt_pk_bf16_f32 v194, 0, v194\n"
        "v_add_f32 v193, v193, v194\n"
        "v_mfma_f32_16x16x32_bf16 a[48:51], v[12:15], v[80:83], a[48:51]\n"
        "v_cvt_pk_bf16_f32 v193, 0, v193\n"
        "v_and_b32 v176, s35, v100\n"
        "v_mfma_f32_16x16x32_bf16 a[48:51], v[8:11], v[48:51], a[48:51]\n"
        "v_and_b32 v177, s35, v102\n"
        "v_mfma_f32_16x16x32_bf16 a[52:55], v[8:11], v[52:55], a[52:55]\n"
        "s_waitcnt vmcnt(24) lgkmcnt(0)\n"
        "v_lshlrev_b32 v178, 16, v104\n"
        "v_mul_f32 v192, v178, v185\n"
        "v_mfma_f32_16x16x32_bf16 a[52:55], v[12:15], v[84:87], a[52:55]\n"
        "v_cvt_pk_bf16_f32 v192, 0, v192\n"
        "v_add_f32 v191, v191, v192\n"
        "v_mfma_f32_16x16x32_bf16 a[20:23], v[4:7], v[84:87], a[20:23]\n"
        "v_cvt_pk_bf16_f32 v191, 0, v191\n"
        "v_mul_f32 v196, v178, v187\n"
        "v_mfma_f32_16x16x32_bf16 a[20:23], v[0:3], v[52:55], a[20:23]\n"
        "global_load_dwordx2 v[120:121], v169, s[74:75] offset:-2048\n"
        "v_cvt_pk_bf16_f32 v196, 0, v196\n"
        "v_mfma_f32_16x16x32_bf16 a[24:27], v[0:3], v[56:59], a[24:27]\n"
        "v_add_f32 v195, v195, v196\n"
        "v_cvt_pk_bf16_f32 v195, 0, v195\n"
        "v_mfma_f32_16x16x32_bf16 a[24:27], v[4:7], v[88:91], a[24:27]\n"
        "v_mul_f32 v194, v178, v186\n"
        "v_cvt_pk_bf16_f32 v194, 0, v194\n"
        "v_mfma_f32_16x16x32_bf16 a[56:59], v[12:15], v[88:91], a[56:59]\n"
        "v_add_f32 v193, v193, v194\n"
        "v_cvt_pk_bf16_f32 v193, 0, v193\n"
        "v_mfma_f32_16x16x32_bf16 a[56:59], v[8:11], v[56:59], a[56:59]\n"
        "v_mfma_f32_16x16x32_bf16 a[60:63], v[8:11], v[60:63], a[60:63]\n"
        "ds_read_b128 v[8:11], v224 offset:2048\n"
        "v_mfma_f32_16x16x32_bf16 a[60:63], v[12:15], v[92:95], a[60:63]\n"
        "ds_read_b128 v[12:15], v225 offset:2048\n"
        "v_mfma_f32_16x16x32_bf16 a[28:31], v[4:7], v[92:95], a[28:31]\n"
        "ds_read_b128 v[4:7], v225 offset:0\n"
        "v_mfma_f32_16x16x32_bf16 a[28:31], v[0:3], v[60:63], a[28:31]\n"
        "global_load_lds_dwordx4 v238, s[86:87] offset:0\n"
        "ds_read_b128 v[0:3], v224 offset:0\n"
        "v_mfma_f32_16x16x32_bf16 a[64:67], v[32:35], v[16:19], a[64:67]\n"
        "v_mfma_f32_16x16x32_bf16 a[64:67], v[64:67], v[20:23], a[64:67]\n"
        "v_mfma_f32_16x16x32_bf16 a[96:99], v[64:67], v[28:31], a[96:99]\n"
        "v_mfma_f32_16x16x32_bf16 a[96:99], v[32:35], v[24:27], a[96:99]\n"
        "v_mfma_f32_16x16x32_bf16 a[100:103], v[36:39], v[24:27], a[100:103]\n"
        "v_mfma_f32_16x16x32_bf16 a[100:103], v[68:71], v[28:31], a[100:103]\n"
        "global_load_lds_dwordx4 v239, s[86:87] offset:1024\n"
        "v_mfma_f32_16x16x32_bf16 a[68:71], v[68:71], v[20:23], a[68:71]\n"
        "s_waitcnt vmcnt(24) lgkmcnt(1)\n"
        "v_lshlrev_b32 v179, 16, v106\n"
        "v_mul_f32 v196, v179, v186\n"
        "v_mfma_f32_16x16x32_bf16 a[68:71], v[36:39], v[16:19], a[68:71]\n"
        "v_cvt_pk_bf16_f32 v196, 0, v196\n"
        "v_add_f32 v195, v195, v196\n"
        "v_mfma_f32_16x16x32_bf16 a[72:75], v[40:43], v[16:19], a[72:75]\n"
        "v_cvt_pk_bf16_f32 v195, 0, v195\n"
        "v_mul_f32 v194, v179, v185\n"
        "v_mfma_f32_16x16x32_bf16 a[72:75], v[72:75], v[20:23], a[72:75]\n"
        "global_load_dwordx2 v[122:123], v169, s[74:75] offset:0\n"
        "v_cvt_pk_bf16_f32 v194, 0, v194\n"
        "v_mfma_f32_16x16x32_bf16 a[104:107], v[72:75], v[28:31], a[104:107]\n"
        "v_add_f32 v193, v193, v194\n"
        "v_cvt_pk_bf16_f32 v193, 0, v193\n"
        "v_mfma_f32_16x16x32_bf16 a[104:107], v[40:43], v[24:27], a[104:107]\n"
        "global_load_lds_dwordx4 v240, s[86:87] offset:2048\n"
        "v_mfma_f32_16x16x32_bf16 a[108:111], v[44:47], v[24:27], a[108:111]\n"
        "v_mfma_f32_16x16x32_bf16 a[108:111], v[76:79], v[28:31], a[108:111]\n"
        "v_mfma_f32_16x16x32_bf16 a[76:79], v[76:79], v[20:23], a[76:79]\n"
        "v_mfma_f32_16x16x32_bf16 a[76:79], v[44:47], v[16:19], a[76:79]\n"
        "v_mfma_f32_16x16x32_bf16 a[80:83], v[16:19], v[48:51], a[80:83]\n"
        "v_mfma_f32_16x16x32_bf16 a[80:83], v[20:23], v[80:83], a[80:83]\n"
        "global_load_lds_dwordx4 v241, s[86:87] offset:3072\n"
        "s_add_u32 m0, s73, 0x1c000\n"
        "v_mfma_f32_16x16x32_bf16 a[112:115], v[28:31], v[80:83], a[112:115]\n"
        "s_add_u32 s86, s62, 0x100000\n"
        "s_addc_u32 s87, s63, 0\n"
        "v_mfma_f32_16x16x32_bf16 a[112:115], v[24:27], v[48:51], a[112:115]\n"
        "v_mfma_f32_16x16x32_bf16 a[116:119], v[24:27], v[52:55], a[116:119]\n"
        "v_mfma_f32_16x16x32_bf16 a[116:119], v[28:31], v[84:87], a[116:119]\n"
        "v_mfma_f32_16x16x32_bf16 a[84:87], v[20:23], v[84:87], a[84:87]\n"
        "v_mfma_f32_16x16x32_bf16 a[84:87], v[16:19], v[52:55], a[84:87]\n"
        "global_load_lds_dwordx4 v238, s[86:87] offset:0\n"
        "v_mfma_f32_16x16x32_bf16 a[88:91], v[16:19], v[56:59], a[88:91]\n"
        "s_waitcnt vmcnt(24) lgkmcnt(0)\n"
        "v_lshlrev_b32 v180, 16, v108\n"
        "v_mul_f32 v196, v180, v185\n"
        "v_mfma_f32_16x16x32_bf16 a[88:91], v[20:23], v[88:91], a[88:91]\n"
        "v_cvt_pk_bf16_f32 v196, 0, v196\n"
        "v_add_f32 v195, v195, v196\n"
        "v_mfma_f32_16x16x32_bf16 a[120:123], v[28:31], v[88:91], a[120:123]\n"
        "v_cvt_pk_bf16_f32 v195, 0, v195\n"
        "v_mfma_f32_16x16x32_bf16 a[120:123], v[24:27], v[56:59], a[120:123]\n"
        "global_load_dwordx2 v[124:125], v169, s[74:75] offset:2048\n"
        "s_add_u32 s74, s22, 0x3000\n"
        "s_addc_u32 s75, s23, 0\n"
        "v_mfma_f32_16x16x32_bf16 a[124:127], v[24:27], v[60:63], a[124:127]\n"
        "ds_read_b128 v[24:27], v224 offset:6144\n"
        "v_mfma_f32_16x16x32_bf16 a[124:127], v[28:31], v[92:95], a[124:127]\n"
        "global_load_lds_dwordx4 v239, s[86:87] offset:1024\n"
        "ds_read_b128 v[28:31], v225 offset:6144\n"
        "v_mfma_f32_16x16x32_bf16 a[92:95], v[20:23], v[92:95], a[92:95]\n"
        "ds_read_b128 v[20:23], v225 offset:4096\n"
        "v_mfma_f32_16x16x32_bf16 a[92:95], v[16:19], v[60:63], a[92:95]\n"
        "ds_read_b128 v[16:19], v224 offset:4096\n"
        "v_mfma_f32_16x16x32_bf16 a[128:131], v[32:35], v[0:3], a[128:131]\n"
        "v_mfma_f32_16x16x32_bf16 a[128:131], v[64:67], v[4:7], a[128:131]\n"
        "v_mfma_f32_16x16x32_bf16 a[160:163], v[64:67], v[12:15], a[160:163]\n"
        "v_mfma_f32_16x16x32_bf16 a[160:163], v[32:35], v[8:11], a[160:163]\n"
        "global_load_lds_dwordx4 v240, s[86:87] offset:2048\n"
        "v_mfma_f32_16x16x32_bf16 a[164:167], v[36:39], v[8:11], a[164:167]\n"
        "s_waitcnt vmcnt(24) lgkmcnt(3)\n"
        "v_lshlrev_b32 v181, 16, v110\n"
        "v_mfma_f32_16x16x32_bf16 a[164:167], v[68:71], v[12:15], a[164:167]\n"
        "v_mfma_f32_16x16x32_bf16 a[132:135], v[68:71], v[4:7], a[132:135]\n"
        "v_mfma_f32_16x16x32_bf16 a[132:135], v[36:39], v[0:3], a[132:135]\n"
        "global_load_dwordx2 v[126:127], v169, s[74:75] offset:-4096\n"
        "v_mfma_f32_16x16x32_bf16 a[136:139], v[40:43], v[0:3], a[136:139]\n"
        "v_mfma_f32_16x16x32_bf16 a[136:139], v[72:75], v[4:7], a[136:139]\n"
        "global_load_lds_dwordx4 v241, s[86:87] offset:3072\n"
        "s_add_u32 m0, s72, 0x0\n"
        "v_mfma_f32_16x16x32_bf16 a[168:171], v[72:75], v[12:15], a[168:171]\n"
        "s_add_u32 s86, s56, 0x100\n"
        "s_addc_u32 s87, s57, 0\n"
        "v_mfma_f32_16x16x32_bf16 a[168:171], v[40:43], v[8:11], a[168:171]\n"
        "v_mfma_f32_16x16x32_bf16 a[172:175], v[44:47], v[8:11], a[172:175]\n"
        "v_mfma_f32_16x16x32_bf16 a[172:175], v[76:79], v[12:15], a[172:175]\n"
        "v_mfma_f32_16x16x32_bf16 a[140:143], v[76:79], v[4:7], a[140:143]\n"
        "v_mfma_f32_16x16x32_bf16 a[140:143], v[44:47], v[0:3], a[140:143]\n"
        "v_mfma_f32_16x16x32_bf16 a[144:147], v[0:3], v[48:51], a[144:147]\n"
        "v_mfma_f32_16x16x32_bf16 a[144:147], v[4:7], v[80:83], a[144:147]\n"
        "v_mfma_f32_16x16x32_bf16 a[176:179], v[12:15], v[80:83], a[176:179]\n"
        "v_mfma_f32_16x16x32_bf16 a[176:179], v[8:11], v[48:51], a[176:179]\n"
        "v_mfma_f32_16x16x32_bf16 a[180:183], v[8:11], v[52:55], a[180:183]\n"
        "s_waitcnt vmcnt(24) lgkmcnt(0)\n"
        "v_lshlrev_b32 v182, 16, v112\n"
        "v_mfma_f32_16x16x32_bf16 a[180:183], v[12:15], v[84:87], a[180:183]\n"
        "v_mfma_f32_16x16x32_bf16 a[148:151], v[4:7], v[84:87], a[148:151]\n"
        "v_mfma_f32_16x16x32_bf16 a[148:151], v[0:3], v[52:55], a[148:151]\n"
        "global_load_dwordx2 v[128:129], v169, s[74:75] offset:-2048\n"
        "v_mfma_f32_16x16x32_bf16 a[152:155], v[0:3], v[56:59], a[152:155]\n"
        "v_mfma_f32_16x16x32_bf16 a[152:155], v[4:7], v[88:91], a[152:155]\n"
        "v_mfma_f32_16x16x32_bf16 a[184:187], v[12:15], v[88:91], a[184:187]\n"
        "v_mfma_f32_16x16x32_bf16 a[184:187], v[8:11], v[56:59], a[184:187]\n"
        "s_waitcnt vmcnt(16)\n"
        "v_mfma_f32_16x16x32_bf16 a[188:191], v[8:11], v[60:63], a[188:191]\n"
        "s_barrier\n"
        "v_mfma_f32_16x16x32_bf16 a[188:191], v[12:15], v[92:95], a[188:191]\n"
        "global_load_lds_dwordx4 v234, s[86:87] offset:0\n"
        "ds_read_b128 v[8:11], v224 offset:51200\n"
        "v_mfma_f32_16x16x32_bf16 a[156:159], v[4:7], v[92:95], a[156:159]\n"
        "ds_read_b128 v[4:7], v225 offset:49152\n"
        "v_mfma_f32_16x16x32_bf16 a[156:159], v[0:3], v[60:63], a[156:159]\n"
        "ds_read_b128 v[0:3], v224 offset:49152\n"
        "v_mfma_f32_16x16x32_bf16 a[192:195], v[32:35], v[16:19], a[192:195]\n"
        "ds_read_b128 v[12:15], v225 offset:51200\n"
        "v_mfma_f32_16x16x32_bf16 a[192:195], v[64:67], v[20:23], a[192:195]\n"
        "global_load_lds_dwordx4 v235, s[86:87] offset:1024\n"
        "v_mfma_f32_16x16x32_bf16 a[224:227], v[64:67], v[28:31], a[224:227]\n"
        "ds_read_b64_tr_b16 v[64:65], v226 offset:8192\n"
        "v_mfma_f32_16x16x32_bf16 a[224:227], v[32:35], v[24:27], a[224:227]\n"
        "ds_read_b64_tr_b16 v[32:33], v226 offset:0\n"
        "v_mfma_f32_16x16x32_bf16 a[228:231], v[36:39], v[24:27], a[228:231]\n"
        "ds_read_b64_tr_b16 v[34:35], v227 offset:0\n"
        "v_mfma_f32_16x16x32_bf16 a[228:231], v[68:71], v[28:31], a[228:231]\n"
        "ds_read_b64_tr_b16 v[66:67], v227 offset:8192\n"
        "global_load_lds_dwordx4 v236, s[86:87] offset:2048\n"
        "v_mfma_f32_16x16x32_bf16 a[196:199], v[68:71], v[20:23], a[196:199]\n"
        "ds_read_b64_tr_b16 v[68:69], v228 offset:8192\n"
        "v_mfma_f32_16x16x32_bf16 a[196:199], v[36:39], v[16:19], a[196:199]\n"
        "ds_read_b64_tr_b16 v[36:37], v228 offset:0\n"
        "v_mfma_f32_16x16x32_bf16 a[200:203], v[40:43], v[16:19], a[200:203]\n"
        "ds_read_b64_tr_b16 v[38:39], v229 offset:0\n"
        "v_mfma_f32_16x16x32_bf16 a[200:203], v[72:75], v[20:23], a[200:203]\n"
        "ds_read_b64_tr_b16 v[70:71], v229 offset:8192\n"
        "global_load_lds_dwordx4 v237, s[86:87] offset:3072\n"
        "s_add_u32 m0, s72, 0x1000\n"
        "v_mfma_f32_16x16x32_bf16 a[232:235], v[72:75], v[28:31], a[232:235]\n"
        "ds_read_b64_tr_b16 v[72:73], v230 offset:8192\n"
        "s_add_u32 s86, s58, 0x100\n"
        "s_addc_u32 s87, s59, 0\n"
        "v_mfma_f32_16x16x32_bf16 a[232:235], v[40:43], v[24:27], a[232:235]\n"
        "ds_read_b64_tr_b16 v[40:41], v230 offset:0\n"
        "v_lshlrev_b32 v183, 16, v114\n"
        "v_mfma_f32_16x16x32_bf16 a[236:239], v[44:47], v[24:27], a[236:239]\n"
        "ds_read_b64_tr_b16 v[42:43], v231 offset:0\n"
        "v_mfma_f32_16x16x32_bf16 a[236:239], v[76:79], v[28:31], a[236:239]\n"
        "ds_read_b64_tr_b16 v[74:75], v231 offset:8192\n"
        "global_load_dwordx2 v[130:131], v169, s[74:75] offset:0\n"
        "v_mfma_f32_16x16x32_bf16 a[204:207], v[76:79], v[20:23], a[204:207]\n"
        "ds_read_b64_tr_b16 v[76:77], v232 offset:8192\n"
        "global_load_lds_dwordx4 v234, s[86:87] offset:0\n"
        "v_mfma_f32_16x16x32_bf16 a[204:207], v[44:47], v[16:19], a[204:207]\n"
        "ds_read_b64_tr_b16 v[44:45], v232 offset:0\n"
        "v_mfma_f32_16x16x32_bf16 a[208:211], v[16:19], v[48:51], a[208:211]\n"
        "ds_read_b64_tr_b16 v[46:47], v233 offset:0\n"
        "v_mfma_f32_16x16x32_bf16 a[208:211], v[20:23], v[80:83], a[208:211]\n"
        "ds_read_b64_tr_b16 v[78:79], v233 offset:8192\n"
        "v_mfma_f32_16x16x32_bf16 a[240:243], v[28:31], v[80:83], a[240:243]\n"
        "ds_read_b64_tr_b16 v[80:81], v226 offset:24576\n"
        "global_load_lds_dwordx4 v235, s[86:87] offset:1024\n"
        "v_mfma_f32_16x16x32_bf16 a[240:243], v[24:27], v[48:51], a[240:243]\n"
        "ds_read_b64_tr_b16 v[48:49], v226 offset:16384\n"
        "v_mfma_f32_16x16x32_bf16 a[244:247], v[24:27], v[52:55], a[244:247]\n"
        "ds_read_b64_tr_b16 v[50:51], v227 offset:16384\n"
        "v_mfma_f32_16x16x32_bf16 a[244:247], v[28:31], v[84:87], a[244:247]\n"
        "ds_read_b64_tr_b16 v[82:83], v227 offset:24576\n"
        "v_mfma_f32_16x16x32_bf16 a[212:215], v[20:23], v[84:87], a[212:215]\n"
        "ds_read_b64_tr_b16 v[84:85], v228 offset:24576\n"
        "global_load_lds_dwordx4 v236, s[86:87] offset:2048\n"
        "v_mfma_f32_16x16x32_bf16 a[212:215], v[16:19], v[52:55], a[212:215]\n"
        "ds_read_b64_tr_b16 v[52:53], v228 offset:16384\n"
        "v_mfma_f32_16x16x32_bf16 a[216:219], v[16:19], v[56:59], a[216:219]\n"
        "ds_read_b64_tr_b16 v[54:55], v229 offset:16384\n"
        "v_mfma_f32_16x16x32_bf16 a[216:219], v[20:23], v[88:91], a[216:219]\n"
        "ds_read_b64_tr_b16 v[86:87], v229 offset:24576\n"
        "v_mfma_f32_16x16x32_bf16 a[248:251], v[28:31], v[88:91], a[248:251]\n"
        "ds_read_b64_tr_b16 v[88:89], v230 offset:24576\n"
        "global_load_lds_dwordx4 v237, s[86:87] offset:3072\n"
        "s_add_u32 m0, s73, 0x20000\n"
        "v_mfma_f32_16x16x32_bf16 a[248:251], v[24:27], v[56:59], a[248:251]\n"
        "ds_read_b64_tr_b16 v[56:57], v230 offset:16384\n"
        "s_add_u32 s86, s60, 0x280000\n"
        "s_addc_u32 s87, s61, 0\n"
        "v_mfma_f32_16x16x32_bf16 a[252:255], v[24:27], v[60:63], a[252:255]\n"
        "ds_read_b64_tr_b16 v[58:59], v231 offset:16384\n"
        "s_waitcnt lgkmcnt(8)\n"
        "v_mfma_f32_16x16x32_bf16 a[252:255], v[28:31], v[92:95], a[252:255]\n"
        "ds_read_b64_tr_b16 v[90:91], v231 offset:24576\n"
        "s_waitcnt vmcnt(24) lgkmcnt(8)\n"
        "v_mfma_f32_16x16x32_bf16 a[220:223], v[20:23], v[92:95], a[220:223]\n"
        "ds_read_b64_tr_b16 v[92:93], v232 offset:24576\n"
        "global_load_dwordx2 v[132:133], v169, s[74:75] offset:2048\n"
        "v_mfma_f32_16x16x32_bf16 a[220:223], v[16:19], v[60:63], a[220:223]\n"
        "ds_read_b64_tr_b16 v[60:61], v232 offset:16384\n"
        "v_lshlrev_b32 v184, 16, v116\n"
        "v_mfma_f32_16x16x32_bf16 a[0:3], v[32:35], v[0:3], a[0:3]\n"
        "ds_read_b64_tr_b16 v[62:63], v233 offset:16384\n"
        "v_mfma_f32_16x16x32_bf16 a[0:3], v[64:67], v[4:7], a[0:3]\n"
        "ds_read_b64_tr_b16 v[94:95], v233 offset:24576\n"
        "v_mfma_f32_16x16x32_bf16 a[32:35], v[64:67], v[12:15], a[32:35]\n"
        "ds_read_b128 v[16:19], v224 offset:53248\n"
        "v_mfma_f32_16x16x32_bf16 a[32:35], v[32:35], v[8:11], a[32:35]\n"
        "ds_read_b128 v[20:23], v225 offset:53248\n"
        "v_mfma_f32_16x16x32_bf16 a[36:39], v[36:39], v[8:11], a[36:39]\n"
        "ds_read_b128 v[24:27], v224 offset:55296\n"
        "s_waitcnt vmcnt(24) lgkmcnt(8)\n"
        "v_mfma_f32_16x16x32_bf16 a[36:39], v[68:71], v[12:15], a[36:39]\n"
        "ds_read_b128 v[28:31], v225 offset:55296\n"
        "v_lshlrev_b32 v190, 16, v118\n"
        "v_mfma_f32_16x16x32_bf16 a[4:7], v[68:71], v[4:7], a[4:7]\n"
        "v_mul_f32 v189, v189, v190\n"
        "v_mul_f32 v190, v179, v187\n"
        "v_mfma_f32_16x16x32_bf16 a[4:7], v[36:39], v[0:3], a[4:7]\n"
        "v_cvt_pk_bf16_f32 v190, 0, v190\n"
        "v_mfma_f32_16x16x32_bf16 a[8:11], v[40:43], v[0:3], a[8:11]\n"
        "v_mfma_f32_16x16x32_bf16 a[8:11], v[72:75], v[4:7], a[8:11]\n"
        "v_mfma_f32_16x16x32_bf16 a[40:43], v[72:75], v[12:15], a[40:43]\n"
        "v_mfma_f32_16x16x32_bf16 a[40:43], v[40:43], v[8:11], a[40:43]\n"
        "v_mfma_f32_16x16x32_bf16 a[44:47], v[44:47], v[8:11], a[44:47]\n"
        "v_mfma_f32_16x16x32_bf16 a[44:47], v[76:79], v[12:15], a[44:47]\n"
        "v_mfma_f32_16x16x32_bf16 a[12:15], v[76:79], v[4:7], a[12:15]\n"
        "v_mfma_f32_16x16x32_bf16 a[12:15], v[44:47], v[0:3], a[12:15]\n"
        "global_load_dword v253, v173, s[82:83] offset:0\n"
        "s_add_u32 s82, s12, 0x400000\n"
        "s_addc_u32 s83, s13, 0\n"
        "v_mfma_f32_16x16x32_bf16 a[16:19], v[0:3], v[48:51], a[16:19]\n"
        "v_mfma_f32_16x16x32_bf16 a[16:19], v[4:7], v[80:83], a[16:19]\n"
        "v_mfma_f32_16x16x32_bf16 a[48:51], v[12:15], v[80:83], a[48:51]\n"
        "v_mfma_f32_16x16x32_bf16 a[48:51], v[8:11], v[48:51], a[48:51]\n"
        "v_mfma_f32_16x16x32_bf16 a[52:55], v[8:11], v[52:55], a[52:55]\n"
        "s_waitcnt vmcnt(23) lgkmcnt(0)\n"
        "v_lshlrev_b32 v192, 16, v120\n"
        "v_mul_f32 v191, v191, v192\n"
        "v_mfma_f32_16x16x32_bf16 a[52:55], v[12:15], v[84:87], a[52:55]\n"
        "v_cvt_pk_bf16_f32 v203, v191, v189\n"
        "v_mul_f32 v189, v178, v188\n"
        "v_mfma_f32_16x16x32_bf16 a[20:23], v[4:7], v[84:87], a[20:23]\n"
        "v_mul_f32 v191, v179, v188\n"
        "v_cvt_pk_bf16_f32 v189, 0, v189\n"
        "v_mfma_f32_16x16x32_bf16 a[20:23], v[0:3], v[52:55], a[20:23]\n"
        "v_cvt_pk_bf16_f32 v191, 0, v191\n"
        "v_mul_f32 v192, v180, v187\n"
        "v_mfma_f32_16x16x32_bf16 a[24:27], v[0:3], v[56:59], a[24:27]\n"
        "v_cvt_pk_bf16_f32 v192, 0, v192\n"
        "v_add_f32 v189, v189, v190\n"
        "v_mfma_f32_16x16x32_bf16 a[24:27], v[4:7], v[88:91], a[24:27]\n"
        "v_add_f32 v191, v191, v192\n"
        "v_cvt_pk_bf16_f32 v189, 0, v189\n"
        "v_mfma_f32_16x16x32_bf16 a[56:59], v[12:15], v[88:91], a[56:59]\n"
        "v_cvt_pk_bf16_f32 v191, 0, v191\n"
        "v_mul_f32 v190, v180, v186\n"
        "v_mfma_f32_16x16x32_bf16 a[56:59], v[8:11], v[56:59], a[56:59]\n"
        "v_mul_f32 v192, v181, v186\n"
        "v_cvt_pk_bf16_f32 v190, 0, v190\n"
        "v_mfma_f32_16x16x32_bf16 a[60:63], v[8:11], v[60:63], a[60:63]\n"
        "ds_read_b128 v[8:11], v224 offset:34816\n"
        "v_cvt_pk_bf16_f32 v192, 0, v192\n"
        "v_mfma_f32_16x16x32_bf16 a[60:63], v[12:15], v[92:95], a[60:63]\n"
        "ds_read_b128 v[12:15], v225 offset:34816\n"
        "v_add_f32 v189, v189, v190\n"
        "v_mfma_f32_16x16x32_bf16 a[28:31], v[4:7], v[92:95], a[28:31]\n"
        "ds_read_b128 v[4:7], v225 offset:32768\n"
        "v_add_f32 v191, v191, v192\n"
        "v_mfma_f32_16x16x32_bf16 a[28:31], v[0:3], v[60:63], a[28:31]\n"
        "global_load_lds_dwordx4 v238, s[86:87] offset:0\n"
        "ds_read_b128 v[0:3], v224 offset:32768\n"
        "v_mfma_f32_16x16x32_bf16 a[64:67], v[32:35], v[16:19], a[64:67]\n"
        "v_cvt_pk_bf16_f32 v189, 0, v189\n"
        "v_cvt_pk_bf16_f32 v191, 0, v191\n"
        "v_mfma_f32_16x16x32_bf16 a[64:67], v[64:67], v[20:23], a[64:67]\n"
        "v_mul_f32 v190, v181, v185\n"
        "v_mul_f32 v192, v182, v185\n"
        "v_mfma_f32_16x16x32_bf16 a[96:99], v[64:67], v[28:31], a[96:99]\n"
        "v_cvt_pk_bf16_f32 v190, 0, v190\n"
        "v_cvt_pk_bf16_f32 v192, 0, v192\n"
        "v_mfma_f32_16x16x32_bf16 a[96:99], v[32:35], v[24:27], a[96:99]\n"
        "v_add_f32 v189, v189, v190\n"
        "v_add_f32 v191, v191, v192\n"
        "v_mfma_f32_16x16x32_bf16 a[100:103], v[36:39], v[24:27], a[100:103]\n"
        "v_cvt_pk_bf16_f32 v189, 0, v189\n"
        "v_cvt_pk_bf16_f32 v191, 0, v191\n"
        "v_mfma_f32_16x16x32_bf16 a[100:103], v[68:71], v[28:31], a[100:103]\n"
        "global_load_lds_dwordx4 v239, s[86:87] offset:1024\n"
        "v_and_b32 v178, s35, v104\n"
        "v_mfma_f32_16x16x32_bf16 a[68:71], v[68:71], v[20:23], a[68:71]\n"
        "s_waitcnt vmcnt(22) lgkmcnt(1)\n"
        "v_lshlrev_b32 v194, 16, v122\n"
        "v_mul_f32 v193, v193, v194\n"
        "v_mfma_f32_16x16x32_bf16 a[68:71], v[36:39], v[16:19], a[68:71]\n"
        "LHA1_%=:\n"
        "s_cbranch_execz LHA5_%=\n"
        "v_mul_f32 v194, v181, v187\n"
        "v_cvt_pk_bf16_f32 v194, 0, v194\n"
        "v_mfma_f32_16x16x32_bf16 a[72:75], v[40:43], v[16:19], a[72:75]\n"
        "v_and_b32 v179, s35, v106\n"
        "v_mfma_f32_16x16x32_bf16 a[72:75], v[72:75], v[20:23], a[72:75]\n"
        "v_mfma_f32_16x16x32_bf16 a[104:107], v[72:75], v[28:31], a[104:107]\n"
        "v_mfma_f32_16x16x32_bf16 a[104:107], v[40:43], v[24:27], a[104:107]\n"
        "global_load_lds_dwordx4 v240, s[86:87] offset:2048\n"
        "v_mfma_f32_16x16x32_bf16 a[108:111], v[44:47], v[24:27], a[108:111]\n"
        "v_mfma_f32_16x16x32_bf16 a[108:111], v[76:79], v[28:31], a[108:111]\n"
        "v_mfma_f32_16x16x32_bf16 a[76:79], v[76:79], v[20:23], a[76:79]\n"
        "v_mfma_f32_16x16x32_bf16 a[76:79], v[44:47], v[16:19], a[76:79]\n"
        "v_mfma_f32_16x16x32_bf16 a[80:83], v[16:19], v[48:51], a[80:83]\n"
        "v_mfma_f32_16x16x32_bf16 a[80:83], v[20:23], v[80:83], a[80:83]\n"
        "global_load_lds_dwordx4 v241, s[86:87] offset:3072\n"
        "s_add_u32 m0, s73, 0x24000\n"
        "v_mfma_f32_16x16x32_bf16 a[112:115], v[28:31], v[80:83], a[112:115]\n"
        "s_add_u32 s86, s62, 0x280000\n"
        "s_addc_u32 s87, s63, 0\n"
        "v_mfma_f32_16x16x32_bf16 a[112:115], v[24:27], v[48:51], a[112:115]\n"
        "v_mfma_f32_16x16x32_bf16 a[116:119], v[24:27], v[52:55], a[116:119]\n"
        "v_mfma_f32_16x16x32_bf16 a[116:119], v[28:31], v[84:87], a[116:119]\n"
        "v_mfma_f32_16x16x32_bf16 a[84:87], v[20:23], v[84:87], a[84:87]\n"
        "v_mfma_f32_16x16x32_bf16 a[84:87], v[16:19], v[52:55], a[84:87]\n"
        "global_load_lds_dwordx4 v238, s[86:87] offset:0\n"
        "v_mfma_f32_16x16x32_bf16 a[88:91], v[16:19], v[56:59], a[88:91]\n"
        "s_waitcnt vmcnt(21) lgkmcnt(0)\n"
        "v_lshlrev_b32 v196, 16, v124\n"
        "v_mul_f32 v195, v195, v196\n"
        "v_mfma_f32_16x16x32_bf16 a[88:91], v[20:23], v[88:91], a[88:91]\n"
        "v_cvt_pk_bf16_f32 v202, v195, v193\n"
        "v_mul_f32 v193, v180, v188\n"
        "v_mfma_f32_16x16x32_bf16 a[120:123], v[28:31], v[88:91], a[120:123]\n"
        "v_mul_f32 v195, v181, v188\n"
        "v_cvt_pk_bf16_f32 v193, 0, v193\n"
        "v_mfma_f32_16x16x32_bf16 a[120:123], v[24:27], v[56:59], a[120:123]\n"
        "v_cvt_pk_bf16_f32 v195, 0, v195\n"
        "v_mul_f32 v196, v182, v187\n"
        "v_mfma_f32_16x16x32_bf16 a[124:127], v[24:27], v[60:63], a[124:127]\n"
        "ds_read_b128 v[24:27], v224 offset:38912\n"
        "v_cvt_pk_bf16_f32 v196, 0, v196\n"
        "v_mfma_f32_16x16x32_bf16 a[124:127], v[28:31], v[92:95], a[124:127]\n"
        "global_load_lds_dwordx4 v239, s[86:87] offset:1024\n"
        "ds_read_b128 v[28:31], v225 offset:38912\n"
        "v_mfma_f32_16x16x32_bf16 a[92:95], v[20:23], v[92:95], a[92:95]\n"
        "ds_read_b128 v[20:23], v225 offset:36864\n"
        "v_add_f32 v193, v193, v194\n"
        "v_mfma_f32_16x16x32_bf16 a[92:95], v[16:19], v[60:63], a[92:95]\n"
        "ds_read_b128 v[16:19], v224 offset:36864\n"
        "v_add_f32 v195, v195, v196\n"
        "v_mfma_f32_16x16x32_bf16 a[128:131], v[32:35], v[0:3], a[128:131]\n"
        "v_cvt_pk_bf16_f32 v193, 0, v193\n"
        "v_cvt_pk_bf16_f32 v195, 0, v195\n"
        "v_mfma_f32_16x16x32_bf16 a[128:131], v[64:67], v[4:7], a[128:131]\n"
        "global_load_dwordx4 v[142:145], v254, s[92:93] offset:0\n"
        "v_mul_f32 v194, v182, v186\n"
        "v_mfma_f32_16x16x32_bf16 a[160:163], v[64:67], v[12:15], a[160:163]\n"
        "v_mul_f32 v196, v183, v186\n"
        "v_cvt_pk_bf16_f32 v194, 0, v194\n"
        "v_mfma_f32_16x16x32_bf16 a[160:163], v[32:35], v[8:11], a[160:163]\n"
        "v_cvt_pk_bf16_f32 v196, 0, v196\n"
        "v_add_f32 v193, v193, v194\n"
        "v_mfma_f32_16x16x32_bf16 a[164:167], v[36:39], v[8:11], a[164:167]\n"
        "s_waitcnt vmcnt(20) lgkmcnt(3)\n"
        "v_lshlrev_b32 v190, 16, v126\n"
        "v_mul_f32 v189, v189, v190\n"
        "v_mfma_f32_16x16x32_bf16 a[164:167], v[68:71], v[12:15], a[164:167]\n"
        "v_add_f32 v195, v195, v196\n"
        "v_cvt_pk_bf16_f32 v193, 0, v193\n"
        "v_mfma_f32_16x16x32_bf16 a[132:135], v[68:71], v[4:7], a[132:135]\n"
        "v_cvt_pk_bf16_f32 v195, 0, v195\n"
        "v_mul_f32 v194, v183, v185\n"
        "v_mfma_f32_16x16x32_bf16 a[132:135], v[36:39], v[0:3], a[132:135]\n"
        "global_load_lds_dwordx4 v240, s[86:87] offset:2048\n"
        "v_mul_f32 v196, v184, v185\n"
        "v_mfma_f32_16x16x32_bf16 a[136:139], v[40:43], v[0:3], a[136:139]\n"
        "v_cvt_pk_bf16_f32 v194, 0, v194\n"
        "v_cvt_pk_bf16_f32 v196, 0, v196\n"
        "v_mfma_f32_16x16x32_bf16 a[136:139], v[72:75], v[4:7], a[136:139]\n"
        "v_add_f32 v193, v193, v194\n"
        "v_add_f32 v195, v195, v196\n"
        "v_mfma_f32_16x16x32_bf16 a[168:171], v[72:75], v[12:15], a[168:171]\n"
        "v_cvt_pk_bf16_f32 v193, 0, v193\n"
        "v_cvt_pk_bf16_f32 v195, 0, v195\n"
        "v_mfma_f32_16x16x32_bf16 a[168:171], v[40:43], v[8:11], a[168:171]\n"
        "v_lshlrev_b32 v185, 16, v136\n"
        "v_and_b32 v186, s35, v136\n"
        "v_mfma_f32_16x16x32_bf16 a[172:175], v[44:47], v[8:11], a[172:175]\n"
        "v_lshlrev_b32 v187, 16, v137\n"
        "v_and_b32 v188, s35, v137\n"
        "v_mfma_f32_16x16x32_bf16 a[172:175], v[76:79], v[12:15], a[172:175]\n"
        "global_load_lds_dwordx4 v241, s[86:87] offset:3072\n"
        "s_add_u32 m0, s72, 0x8000\n"
        "v_and_b32 v180, s35, v108\n"
        "v_mfma_f32_16x16x32_bf16 a[140:143], v[76:79], v[4:7], a[140:143]\n"
        "s_add_u32 s86, s56, 0x280\n"
        "s_addc_u32 s87, s57, 0\n"
        "v_and_b32 v181, s35, v110\n"
        "v_mfma_f32_16x16x32_bf16 a[140:143], v[44:47], v[0:3], a[140:143]\n"
        "v_and_b32 v182, s35, v112\n"
        "v_and_b32 v183, s35, v114\n"
        "v_mfma_f32_16x16x32_bf16 a[144:147], v[0:3], v[48:51], a[144:147]\n"
        "v_and_b32 v184, s35, v116\n"
        "v_mul_f32 v190, v175, v187\n"
        "v_mfma_f32_16x16x32_bf16 a[144:147], v[4:7], v[80:83], a[144:147]\n"
        "v_cvt_pk_bf16_f32 v190, 0, v190\n"
        "v_mfma_f32_16x16x32_bf16 a[176:179], v[12:15], v[80:83], a[176:179]\n"
        "v_mfma_f32_16x16x32_bf16 a[176:179], v[8:11], v[48:51], a[176:179]\n"
        "v_mfma_f32_16x16x32_bf16 a[180:183], v[8:11], v[52:55], a[180:183]\n"
        "s_waitcnt vmcnt(20) lgkmcnt(0)\n"
        "v_lshlrev_b32 v192, 16, v128\n"
        "v_mul_f32 v191, v191, v192\n"
        "v_mfma_f32_16x16x32_bf16 a[180:183], v[12:15], v[84:87], a[180:183]\n"
        "v_cvt_pk_bf16_f32 v201, v191, v189\n"
        "v_mul_f32 v189, v174, v188\n"
        "v_mfma_f32_16x16x32_bf16 a[148:151], v[4:7], v[84:87], a[148:151]\n"
        "v_mul_f32 v191, v175, v188\n"
        "v_cvt_pk_bf16_f32 v189, 0, v189\n"
        "v_mfma_f32_16x16x32_bf16 a[148:151], v[0:3], v[52:55], a[148:151]\n"
        "v_cvt_pk_bf16_f32 v191, 0, v191\n"
        "v_mul_f32 v192, v176, v187\n"
        "v_mfma_f32_16x16x32_bf16 a[152:155], v[0:3], v[56:59], a[152:155]\n"
        "v_cvt_pk_bf16_f32 v192, 0, v192\n"
        "v_add_f32 v189, v189, v190\n"
        "v_mfma_f32_16x16x32_bf16 a[152:155], v[4:7], v[88:91], a[152:155]\n"
        "v_add_f32 v191, v191, v192\n"
        "v_cvt_pk_bf16_f32 v189, 0, v189\n"
        "v_mfma_f32_16x16x32_bf16 a[184:187], v[12:15], v[88:91], a[184:187]\n"
        "v_cvt_pk_bf16_f32 v191, 0, v191\n"
        "v_mul_f32 v190, v176, v186\n"
        "v_mfma_f32_16x16x32_bf16 a[184:187], v[8:11], v[56:59], a[184:187]\n"
        "s_waitcnt vmcnt(11)\n"
        "v_mul_f32 v192, v177, v186\n"
        "v_cvt_pk_bf16_f32 v190, 0, v190\n"
        "v_mfma_f32_16x16x32_bf16 a[188:191], v[8:11], v[60:63], a[188:191]\n"
        "s_barrier\n"
        "v_mfma_f32_16x16x32_bf16 a[188:191], v[12:15], v[92:95], a[188:191]\n"
        "global_load_lds_dwordx4 v234, s[86:87] offset:0\n"
        "ds_read_b128 v[8:11], v224 offset:18432\n"
        "v_mfma_f32_16x16x32_bf16 a[156:159], v[4:7], v[92:95], a[156:159]\n"
        "ds_read_b128 v[4:7], v225 offset:16384\n"
        "v_cvt_pk_bf16_f32 v192, 0, v192\n"
        "v_mfma_f32_16x16x32_bf16 a[156:159], v[0:3], v[60:63], a[156:159]\n"
        "ds_read_b128 v[0:3], v224 offset:16384\n"
        "v_add_f32 v189, v189, v190\n"
        "v_mfma_f32_16x16x32_bf16 a[192:195], v[32:35], v[16:19], a[192:195]\n"
        "ds_read_b128 v[12:15], v225 offset:18432\n"
        "v_add_f32 v191, v191, v192\n"
        "v_mfma_f32_16x16x32_bf16 a[192:195], v[64:67], v[20:23], a[192:195]\n"
        "global_load_lds_dwordx4 v235, s[86:87] offset:1024\n"
        "v_cvt_pk_bf16_f32 v189, 0, v189\n"
        "v_mfma_f32_16x16x32_bf16 a[224:227], v[64:67], v[28:31], a[224:227]\n"
        "ds_read_b64_tr_b16 v[64:65], v226 offset:40960\n"
        "v_cvt_pk_bf16_f32 v191, 0, v191\n"
        "v_mfma_f32_16x16x32_bf16 a[224:227], v[32:35], v[24:27], a[224:227]\n"
        "ds_read_b64_tr_b16 v[32:33], v226 offset:32768\n"
        "v_mul_f32 v190, v177, v185\n"
        "v_mfma_f32_16x16x32_bf16 a[228:231], v[36:39], v[24:27], a[228:231]\n"
        "ds_read_b64_tr_b16 v[34:35], v227 offset:32768\n"
        "v_mul_f32 v192, v178, v185\n"
        "v_mfma_f32_16x16x32_bf16 a[228:231], v[68:71], v[28:31], a[228:231]\n"
        "ds_read_b64_tr_b16 v[66:67], v227 offset:40960\n"
        "global_load_lds_dwordx4 v236, s[86:87] offset:2048\n"
        "v_mfma_f32_16x16x32_bf16 a[196:199], v[68:71], v[20:23], a[196:199]\n"
        "ds_read_b64_tr_b16 v[68:69], v228 offset:40960\n"
        "v_cvt_pk_bf16_f32 v190, 0, v190\n"
        "v_mfma_f32_16x16x32_bf16 a[196:199], v[36:39], v[16:19], a[196:199]\n"
        "ds_read_b64_tr_b16 v[36:37], v228 offset:32768\n"
        "v_cvt_pk_bf16_f32 v192, 0, v192\n"
        "v_mfma_f32_16x16x32_bf16 a[200:203], v[40:43], v[16:19], a[200:203]\n"
        "ds_read_b64_tr_b16 v[38:39], v229 offset:32768\n"
        "v_add_f32 v189, v189, v190\n"
        "v_mfma_f32_16x16x32_bf16 a[200:203], v[72:75], v[20:23], a[200:203]\n"
        "ds_read_b64_tr_b16 v[70:71], v229 offset:40960\n"
        "global_load_lds_dwordx4 v237, s[86:87] offset:3072\n"
        "s_add_u32 m0, s72, 0x9000\n"
        "v_mfma_f32_16x16x32_bf16 a[232:235], v[72:75], v[28:31], a[232:235]\n"
        "ds_read_b64_tr_b16 v[72:73], v230 offset:40960\n"
        "s_add_u32 s86, s58, 0x280\n"
        "s_addc_u32 s87, s59, 0\n"
        "v_mfma_f32_16x16x32_bf16 a[232:235], v[40:43], v[24:27], a[232:235]\n"
        "ds_read_b64_tr_b16 v[40:41], v230 offset:32768\n"
        "v_lshlrev_b32 v194, 16, v130\n"
        "v_mfma_f32_16x16x32_bf16 a[236:239], v[44:47], v[24:27], a[236:239]\n"
        "ds_read_b64_tr_b16 v[42:43], v231 offset:32768\n"
        "v_mul_f32 v193, v193, v194\n"
        "v_mfma_f32_16x16x32_bf16 a[236:239], v[76:79], v[28:31], a[236:239]\n"
        "ds_read_b64_tr_b16 v[74:75], v231 offset:40960\n"
        "global_load_lds_dwordx4 v234, s[86:87] offset:0\n"
        "v_mfma_f32_16x16x32_bf16 a[204:207], v[76:79], v[20:23], a[204:207]\n"
        "ds_read_b64_tr_b16 v[76:77], v232 offset:40960\n"
        "v_add_f32 v191, v191, v192\n"
        "v_mfma_f32_16x16x32_bf16 a[204:207], v[44:47], v[16:19], a[204:207]\n"
        "ds_read_b64_tr_b16 v[44:45], v232 offset:32768\n"
        "v_cvt_pk_bf16_f32 v189, 0, v189\n"
        "v_mfma_f32_16x16x32_bf16 a[208:211], v[16:19], v[48:51], a[208:211]\n"
        "ds_read_b64_tr_b16 v[46:47], v233 offset:32768\n"
        "v_cvt_pk_bf16_f32 v191, 0, v191\n"
        "v_mfma_f32_16x16x32_bf16 a[208:211], v[20:23], v[80:83], a[208:211]\n"
        "ds_read_b64_tr_b16 v[78:79], v233 offset:40960\n"
        "global_load_lds_dwordx4 v235, s[86:87] offset:1024\n"
        "v_mfma_f32_16x16x32_bf16 a[240:243], v[28:31], v[80:83], a[240:243]\n"
        "ds_read_b64_tr_b16 v[80:81], v226 offset:57344\n"
        "v_and_b32 v190, s35, v118\n"
        "v_mfma_f32_16x16x32_bf16 a[240:243], v[24:27], v[48:51], a[240:243]\n"
        "ds_read_b64_tr_b16 v[48:49], v226 offset:49152\n"
        "v_and_b32 v192, s35, v120\n"
        "v_mfma_f32_16x16x32_bf16 a[244:247], v[24:27], v[52:55], a[244:247]\n"
        "ds_read_b64_tr_b16 v[50:51], v227 offset:49152\n"
        "v_mul_f32 v189, v189, v190\n"
        "v_mfma_f32_16x16x32_bf16 a[244:247], v[28:31], v[84:87], a[244:247]\n"
        "ds_read_b64_tr_b16 v[82:83], v227 offset:57344\n"
        "global_load_lds_dwordx4 v236, s[86:87] offset:2048\n"
        "v_mfma_f32_16x16x32_bf16 a[212:215], v[20:23], v[84:87], a[212:215]\n"
        "ds_read_b64_tr_b16 v[84:85], v228 offset:57344\n"
        "v_mul_f32 v191, v191, v192\n"
        "v_mfma_f32_16x16x32_bf16 a[212:215], v[16:19], v[52:55], a[212:215]\n"
        "ds_read_b64_tr_b16 v[52:53], v228 offset:49152\n"
        "v_cvt_pk_bf16_f32 v211, v191, v189\n"
        "v_mfma_f32_16x16x32_bf16 a[216:219], v[16:19], v[56:59], a[216:219]\n"
        "ds_read_b64_tr_b16 v[54:55], v229 offset:49152\n"
        "v_mul_f32 v194, v177, v187\n"
        "v_mfma_f32_16x16x32_bf16 a[216:219], v[20:23], v[88:91], a[216:219]\n"
        "ds_read_b64_tr_b16 v[86:87], v229 offset:57344\n"
        "global_load_lds_dwordx4 v237, s[86:87] offset:3072\n"
        "s_add_u32 m0, s73, 0x10000\n"
        "v_mfma_f32_16x16x32_bf16 a[248:251], v[28:31], v[88:91], a[248:251]\n"
        "ds_read_b64_tr_b16 v[88:89], v230 offset:57344\n"
        "s_add_u32 s86, s60, 0x380000\n"
        "s_addc_u32 s87, s61, 0\n"
        "v_mfma_f32_16x16x32_bf16 a[248:251], v[24:27], v[56:59], a[248:251]\n"
        "ds_read_b64_tr_b16 v[56:57], v230 offset:49152\n"
        "v_cvt_pk_bf16_f32 v194, 0, v194\n"
        "v_mfma_f32_16x16x32_bf16 a[252:255], v[24:27], v[60:63], a[252:255]\n"
        "ds_read_b64_tr_b16 v[58:59], v231 offset:49152\n"
        "v_mul_f32 v189, v178, v188\n"
        "v_mfma_f32_16x16x32_bf16 a[252:255], v[28:31], v[92:95], a[252:255]\n"
        "ds_read_b64_tr_b16 v[90:91], v231 offset:57344\n"
        "v_mul_f32 v191, v179, v188\n"
        "v_mfma_f32_16x16x32_bf16 a[220:223], v[20:23], v[92:95], a[220:223]\n"
        "ds_read_b64_tr_b16 v[92:93], v232 offset:57344\n"
        "v_cvt_pk_bf16_f32 v189, 0, v189\n"
        "v_mfma_f32_16x16x32_bf16 a[220:223], v[16:19], v[60:63], a[220:223]\n"
        "ds_read_b64_tr_b16 v[60:61], v232 offset:49152\n"
        "s_waitcnt lgkmcnt(8)\n"
        "v_mfma_f32_16x16x32_bf16 a[0:3], v[32:35], v[0:3], a[0:3]\n"
        "ds_read_b64_tr_b16 v[62:63], v233 offset:49152\n"
        "global_load_dwordx4 v[146:149], v254, s[92:93] offset:1024\n"
        "v_mfma_f32_16x16x32_bf16 a[0:3], v[64:67], v[4:7], a[0:3]\n"
        "ds_read_b64_tr_b16 v[94:95], v233 offset:57344\n"
        "v_cvt_pk_bf16_f32 v191, 0, v191\n"
        "v_mfma_f32_16x16x32_bf16 a[32:35], v[64:67], v[12:15], a[32:35]\n"
        "ds_read_b128 v[16:19], v224 offset:20480\n"
        "v_mul_f32 v190, v179, v187\n"
        "v_mfma_f32_16x16x32_bf16 a[32:35], v[32:35], v[8:11], a[32:35]\n"
        "ds_read_b128 v[20:23], v225 offset:20480\n"
        "v_mul_f32 v192, v180, v187\n"
        "v_mfma_f32_16x16x32_bf16 a[36:39], v[36:39], v[8:11], a[36:39]\n"
        "ds_read_b128 v[24:27], v224 offset:22528\n"
        "v_cvt_pk_bf16_f32 v190, 0, v190\n"
        "v_mfma_f32_16x16x32_bf16 a[36:39], v[68:71], v[12:15], a[36:39]\n"
        "ds_read_b128 v[28:31], v225 offset:22528\n"
        "v_cvt_pk_bf16_f32 v192, 0, v192\n"
        "v_mfma_f32_16x16x32_bf16 a[4:7], v[68:71], v[4:7], a[4:7]\n"
        "s_waitcnt vmcnt(19) lgkmcnt(7)\n"
        "v_lshlrev_b32 v196, 16, v132\n"
        "v_mul_f32 v195, v195, v196\n"
        "v_mfma_f32_16x16x32_bf16 a[4:7], v[36:39], v[0:3], a[4:7]\n"
        "v_cvt_pk_bf16_f32 v200, v195, v193\n"
        "v_mul_f32 v193, v176, v188\n"
        "v_mfma_f32_16x16x32_bf16 a[8:11], v[40:43], v[0:3], a[8:11]\n"
        "global_store_dwordx4 v171, v[200:203], s[76:77] offset:-4096 sc1\n"
        "v_mul_f32 v195, v177, v188\n"
        "v_cvt_pk_bf16_f32 v193, 0, v193\n"
        "v_cvt_pk_bf16_f32 v195, 0, v195\n"
        "v_mul_f32 v196, v178, v187\n"
        "v_mfma_f32_16x16x32_bf16 a[8:11], v[72:75], v[4:7], a[8:11]\n"
        "v_cvt_pk_bf16_f32 v196, 0, v196\n"
        "v_add_f32 v193, v193, v194\n"
        "v_mfma_f32_16x16x32_bf16 a[40:43], v[72:75], v[12:15], a[40:43]\n"
        "v_add_f32 v195, v195, v196\n"
        "v_cvt_pk_bf16_f32 v193, 0, v193\n"
        "v_mfma_f32_16x16x32_bf16 a[40:43], v[40:43], v[8:11], a[40:43]\n"
        "v_cvt_pk_bf16_f32 v195, 0, v195\n"
        "v_mul_f32 v194, v178, v186\n"
        "v_mfma_f32_16x16x32_bf16 a[44:47], v[44:47], v[8:11], a[44:47]\n"
        "v_mul_f32 v196, v179, v186\n"
        "v_cvt_pk_bf16_f32 v194, 0, v194\n"
        "v_mfma_f32_16x16x32_bf16 a[44:47], v[76:79], v[12:15], a[44:47]\n"
        "v_cvt_pk_bf16_f32 v196, 0, v196\n"
        "v_add_f32 v193, v193, v194\n"
        "v_mfma_f32_16x16x32_bf16 a[12:15], v[76:79], v[4:7], a[12:15]\n"
        "v_add_f32 v195, v195, v196\n"
        "v_cvt_pk_bf16_f32 v193, 0, v193\n"
        "v_mfma_f32_16x16x32_bf16 a[12:15], v[44:47], v[0:3], a[12:15]\n"
        "global_load_dword v253, v173, s[82:83] offset:0\n"
        "s_add_u32 s82, s12, 0x500000\n"
        "s_addc_u32 s83, s13, 0\n"
        "v_mfma_f32_16x16x32_bf16 a[16:19], v[0:3], v[48:51], a[16:19]\n"
        "v_cvt_pk_bf16_f32 v195, 0, v195\n"
        "v_mul_f32 v194, v179, v185\n"
        "v_mfma_f32_16x16x32_bf16 a[16:19], v[4:7], v[80:83], a[16:19]\n"
        "v_mul_f32 v196, v180, v185\n"
        "v_cvt_pk_bf16_f32 v194, 0, v194\n"
        "v_mfma_f32_16x16x32_bf16 a[48:51], v[12:15], v[80:83], a[48:51]\n"
        "v_cvt_pk_bf16_f32 v196, 0, v196\n"
        "v_add_f32 v193, v193, v194\n"
        "v_mfma_f32_16x16x32_bf16 a[48:51], v[8:11], v[48:51], a[48:51]\n"
        "v_add_f32 v195, v195, v196\n"
        "v_cvt_pk_bf16_f32 v193, 0, v193\n"
        "v_mfma_f32_16x16x32_bf16 a[52:55], v[8:11], v[52:55], a[52:55]\n"
        "v_cvt_pk_bf16_f32 v195, 0, v195\n"
        "v_and_b32 v194, s35, v122\n"
        "v_mfma_f32_16x16x32_bf16 a[52:55], v[12:15], v[84:87], a[52:55]\n"
        "v_and_b32 v196, s35, v124\n"
        "v_mul_f32 v193, v193, v194\n"
        "v_mfma_f32_16x16x32_bf16 a[20:23], v[4:7], v[84:87], a[20:23]\n"
        "v_mul_f32 v195, v195, v196\n"
        "v_cvt_pk_bf16_f32 v210, v195, v193\n"
        "v_mfma_f32_16x16x32_bf16 a[20:23], v[0:3], v[52:55], a[20:23]\n"
        "v_add_f32 v189, v189, v190\n"
        "v_add_f32 v191, v191, v192\n"
        "v_mfma_f32_16x16x32_bf16 a[24:27], v[0:3], v[56:59], a[24:27]\n"
        "s_waitcnt lgkmcnt(0)\n"
        "v_cvt_pk_bf16_f32 v189, 0, v189\n"
        "v_cvt_pk_bf16_f32 v191, 0, v191\n"
        "v_mfma_f32_16x16x32_bf16 a[24:27], v[4:7], v[88:91], a[24:27]\n"
        "v_mul_f32 v190, v180, v186\n"
        "v_mul_f32 v192, v181, v186\n"
        "v_mfma_f32_16x16x32_bf16 a[56:59], v[12:15], v[88:91], a[56:59]\n"
        "v_cvt_pk_bf16_f32 v190, 0, v190\n"
        "v_cvt_pk_bf16_f32 v192, 0, v192\n"
        "v_mfma_f32_16x16x32_bf16 a[56:59], v[8:11], v[56:59], a[56:59]\n"
        "v_add_f32 v189, v189, v190\n"
        "v_add_f32 v191, v191, v192\n"
        "v_mfma_f32_16x16x32_bf16 a[60:63], v[8:11], v[60:63], a[60:63]\n"
        "ds_read_b128 v[8:11], v224 offset:2048\n"
        "v_cvt_pk_bf16_f32 v189, 0, v189\n"
        "v_mfma_f32_16x16x32_bf16 a[60:63], v[12:15], v[92:95], a[60:63]\n"
        "ds_read_b128 v[12:15], v225 offset:2048\n"
        "v_cvt_pk_bf16_f32 v191, 0, v191\n"
        "v_mfma_f32_16x16x32_bf16 a[28:31], v[4:7], v[92:95], a[28:31]\n"
        "ds_read_b128 v[4:7], v225 offset:0\n"
        "v_mul_f32 v190, v181, v185\n"
        "v_mfma_f32_16x16x32_bf16 a[28:31], v[0:3], v[60:63], a[28:31]\n"
        "global_load_lds_dwordx4 v238, s[86:87] offset:0\n"
        "ds_read_b128 v[0:3], v224 offset:0\n"
        "v_mfma_f32_16x16x32_bf16 a[64:67], v[32:35], v[16:19], a[64:67]\n"
        "v_mul_f32 v192, v182, v185\n"
        "v_cvt_pk_bf16_f32 v190, 0, v190\n"
        "v_mfma_f32_16x16x32_bf16 a[64:67], v[64:67], v[20:23], a[64:67]\n"
        "v_cvt_pk_bf16_f32 v192, 0, v192\n"
        "v_add_f32 v189, v189, v190\n"
        "v_mfma_f32_16x16x32_bf16 a[96:99], v[64:67], v[28:31], a[96:99]\n"
        "v_add_f32 v191, v191, v192\n"
        "v_cvt_pk_bf16_f32 v189, 0, v189\n"
        "v_mfma_f32_16x16x32_bf16 a[96:99], v[32:35], v[24:27], a[96:99]\n"
        "v_cvt_pk_bf16_f32 v191, 0, v191\n"
        "v_and_b32 v190, s35, v126\n"
        "v_mfma_f32_16x16x32_bf16 a[100:103], v[36:39], v[24:27], a[100:103]\n"
        "v_and_b32 v192, s35, v128\n"
        "v_mul_f32 v189, v189, v190\n"
        "v_mfma_f32_16x16x32_bf16 a[100:103], v[68:71], v[28:31], a[100:103]\n"
        "global_load_lds_dwordx4 v239, s[86:87] offset:1024\n"
        "v_mul_f32 v191, v191, v192\n"
        "v_mfma_f32_16x16x32_bf16 a[68:71], v[68:71], v[20:23], a[68:71]\n"
        "v_cvt_pk_bf16_f32 v209, v191, v189\n"
        "v_mul_f32 v193, v180, v188\n"
        "v_mfma_f32_16x16x32_bf16 a[68:71], v[36:39], v[16:19], a[68:71]\n"
        "v_mul_f32 v195, v181, v188\n"
        "v_cvt_pk_bf16_f32 v193, 0, v193\n"
        "v_mfma_f32_16x16x32_bf16 a[72:75], v[40:43], v[16:19], a[72:75]\n"
        "v_cvt_pk_bf16_f32 v195, 0, v195\n"
        "v_mul_f32 v194, v181, v187\n"
        "v_mfma_f32_16x16x32_bf16 a[72:75], v[72:75], v[20:23], a[72:75]\n"
        "v_mul_f32 v196, v182, v187\n"
        "v_cvt_pk_bf16_f32 v194, 0, v194\n"
        "v_mfma_f32_16x16x32_bf16 a[104:107], v[72:75], v[28:31], a[104:107]\n"
        "v_cvt_pk_bf16_f32 v196, 0, v196\n"
        "v_add_f32 v193, v193, v194\n"
        "v_mfma_f32_16x16x32_bf16 a[104:107], v[40:43], v[24:27], a[104:107]\n"
        "global_load_lds_dwordx4 v240, s[86:87] offset:2048\n"
        "v_add_f32 v195, v195, v196\n"
        "v_mfma_f32_16x16x32_bf16 a[108:111], v[44:47], v[24:27], a[108:111]\n"
        "v_cvt_pk_bf16_f32 v193, 0, v193\n"
        "v_cvt_pk_bf16_f32 v195, 0, v195\n"
        "v_mfma_f32_16x16x32_bf16 a[108:111], v[76:79], v[28:31], a[108:111]\n"
        "v_mul_f32 v194, v182, v186\n"
        "v_mul_f32 v196, v183, v186\n"
        "v_mfma_f32_16x16x32_bf16 a[76:79], v[76:79], v[20:23], a[76:79]\n"
        "v_cvt_pk_bf16_f32 v194, 0, v194\n"
        "v_cvt_pk_bf16_f32 v196, 0, v196\n"
        "v_mfma_f32_16x16x32_bf16 a[76:79], v[44:47], v[16:19], a[76:79]\n"
        "v_add_f32 v193, v193, v194\n"
        "v_add_f32 v195, v195, v196\n"
        "v_mfma_f32_16x16x32_bf16 a[80:83], v[16:19], v[48:51], a[80:83]\n"
        "v_cvt_pk_bf16_f32 v193, 0, v193\n"
        "v_cvt_pk_bf16_f32 v195, 0, v195\n"
        "v_mfma_f32_16x16x32_bf16 a[80:83], v[20:23], v[80:83], a[80:83]\n"
        "global_load_lds_dwordx4 v241, s[86:87] offset:3072\n"
        "s_add_u32 m0, s73, 0x14000\n"
        "v_mul_f32 v194, v183, v185\n"
        "v_mfma_f32_16x16x32_bf16 a[112:115], v[28:31], v[80:83], a[112:115]\n"
        "s_add_u32 s86, s62, 0x380000\n"
        "s_addc_u32 s87, s63, 0\n"
        "v_mul_f32 v196, v184, v185\n"
        "v_mfma_f32_16x16x32_bf16 a[112:115], v[24:27], v[48:51], a[112:115]\n"
        "v_cvt_pk_bf16_f32 v194, 0, v194\n"
        "v_cvt_pk_bf16_f32 v196, 0, v196\n"
        "v_mfma_f32_16x16x32_bf16 a[116:119], v[24:27], v[52:55], a[116:119]\n"
        "v_add_f32 v193, v193, v194\n"
        "v_add_f32 v195, v195, v196\n"
        "v_mfma_f32_16x16x32_bf16 a[116:119], v[28:31], v[84:87], a[116:119]\n"
        "v_cvt_pk_bf16_f32 v193, 0, v193\n"
        "v_cvt_pk_bf16_f32 v195, 0, v195\n"
        "v_mfma_f32_16x16x32_bf16 a[84:87], v[20:23], v[84:87], a[84:87]\n"
        "v_and_b32 v194, s35, v130\n"
        "v_and_b32 v196, s35, v132\n"
        "v_mfma_f32_16x16x32_bf16 a[84:87], v[16:19], v[52:55], a[84:87]\n"
        "global_load_lds_dwordx4 v238, s[86:87] offset:0\n"
        "v_mul_f32 v193, v193, v194\n"
        "v_mfma_f32_16x16x32_bf16 a[88:91], v[16:19], v[56:59], a[88:91]\n"
        "v_mul_f32 v195, v195, v196\n"
        "v_cvt_pk_bf16_f32 v208, v195, v193\n"
        "v_mfma_f32_16x16x32_bf16 a[88:91], v[20:23], v[88:91], a[88:91]\n"
        "v_lshlrev_b32 v185, 16, v138\n"
        "v_and_b32 v186, s35, v138\n"
        "v_mfma_f32_16x16x32_bf16 a[120:123], v[28:31], v[88:91], a[120:123]\n"
        "v_lshlrev_b32 v187, 16, v139\n"
        "v_and_b32 v188, s35, v139\n"
        "v_mfma_f32_16x16x32_bf16 a[120:123], v[24:27], v[56:59], a[120:123]\n"
        "global_store_dwordx4 v171, v[208:211], s[76:77] offset:0 sc1\n"
        "s_add_u32 s76, s76, 0x2000\n"
        "s_addc_u32 s77, s77, 0\n"
        "v_lshlrev_b32 v174, 16, v97\n"
        "v_lshlrev_b32 v175, 16, v99\n"
        "v_lshlrev_b32 v176, 16, v101\n"
        "v_mfma_f32_16x16x32_bf16 a[124:127], v[24:27], v[60:63], a[124:127]\n"
        "ds_read_b128 v[24:27], v224 offset:6144\n"
        "v_lshlrev_b32 v177, 16, v103\n"
        "v_mfma_f32_16x16x32_bf16 a[124:127], v[28:31], v[92:95], a[124:127]\n"
        "ds_read_b128 v[28:31], v225 offset:6144\n"
        "v_lshlrev_b32 v178, 16, v105\n"
        "v_mfma_f32_16x16x32_bf16 a[92:95], v[20:23], v[92:95], a[92:95]\n"
        "ds_read_b128 v[20:23], v225 offset:4096\n"
        "v_lshlrev_b32 v179, 16, v107\n"
        "v_mfma_f32_16x16x32_bf16 a[92:95], v[16:19], v[60:63], a[92:95]\n"
        "ds_read_b128 v[16:19], v224 offset:4096\n"
        "s_waitcnt lgkmcnt(4)\n"
        "v_mfma_f32_16x16x32_bf16 a[128:131], v[32:35], v[0:3], a[128:131]\n"
        "global_load_lds_dwordx4 v239, s[86:87] offset:1024\n"
        "v_mfma_f32_16x16x32_bf16 a[128:131], v[64:67], v[4:7], a[128:131]\n"
        "v_lshlrev_b32 v180, 16, v109\n"
        "v_lshlrev_b32 v181, 16, v111\n"
        "v_mfma_f32_16x16x32_bf16 a[160:163], v[64:67], v[12:15], a[160:163]\n"
        "v_lshlrev_b32 v182, 16, v113\n"
        "v_lshlrev_b32 v183, 16, v115\n"
        "v_mfma_f32_16x16x32_bf16 a[160:163], v[32:35], v[8:11], a[160:163]\n"
        "v_lshlrev_b32 v184, 16, v117\n"
        "v_mul_f32 v189, v174, v188\n"
        "v_mfma_f32_16x16x32_bf16 a[164:167], v[36:39], v[8:11], a[164:167]\n"
        "global_load_dwordx4 v[150:153], v254, s[92:93] offset:2048\n"
        "v_mfma_f32_16x16x32_bf16 a[164:167], v[68:71], v[12:15], a[164:167]\n"
        "v_mul_f32 v191, v175, v188\n"
        "v_cvt_pk_bf16_f32 v189, 0, v189\n"
        "v_mfma_f32_16x16x32_bf16 a[132:135], v[68:71], v[4:7], a[132:135]\n"
        "v_cvt_pk_bf16_f32 v191, 0, v191\n"
        "v_mul_f32 v190, v175, v187\n"
        "v_mfma_f32_16x16x32_bf16 a[132:135], v[36:39], v[0:3], a[132:135]\n"
        "v_mul_f32 v192, v176, v187\n"
        "v_cvt_pk_bf16_f32 v190, 0, v190\n"
        "v_mfma_f32_16x16x32_bf16 a[136:139], v[40:43], v[0:3], a[136:139]\n"
        "v_cvt_pk_bf16_f32 v192, 0, v192\n"
        "v_add_f32 v189, v189, v190\n"
        "v_mfma_f32_16x16x32_bf16 a[136:139], v[72:75], v[4:7], a[136:139]\n"
        "v_add_f32 v191, v191, v192\n"
        "v_cvt_pk_bf16_f32 v189, 0, v189\n"
        "v_mfma_f32_16x16x32_bf16 a[168:171], v[72:75], v[12:15], a[168:171]\n"
        "global_load_lds_dwordx4 v240, s[86:87] offset:2048\n"
        "v_mfma_f32_16x16x32_bf16 a[168:171], v[40:43], v[8:11], a[168:171]\n"
        "v_cvt_pk_bf16_f32 v191, 0, v191\n"
        "v_mul_f32 v190, v176, v186\n"
        "v_mfma_f32_16x16x32_bf16 a[172:175], v[44:47], v[8:11], a[172:175]\n"
        "v_mul_f32 v192, v177, v186\n"
        "v_cvt_pk_bf16_f32 v190, 0, v190\n"
        "v_mfma_f32_16x16x32_bf16 a[172:175], v[76:79], v[12:15], a[172:175]\n"
        "v_cvt_pk_bf16_f32 v192, 0, v192\n"
        "v_add_f32 v189, v189, v190\n"
        "v_mfma_f32_16x16x32_bf16 a[140:143], v[76:79], v[4:7], a[140:143]\n"
        "v_add_f32 v191, v191, v192\n"
        "v_cvt_pk_bf16_f32 v189, 0, v189\n"
        "v_mfma_f32_16x16x32_bf16 a[140:143], v[44:47], v[0:3], a[140:143]\n"
        "v_cvt_pk_bf16_f32 v191, 0, v191\n"
        "v_mul_f32 v190, v177, v185\n"
        "v_mfma_f32_16x16x32_bf16 a[144:147], v[0:3], v[48:51], a[144:147]\n"
        "global_load_lds_dwordx4 v241, s[86:87] offset:3072\n"
        "s_add_u32 m0, s72, 0x0\n"
        "v_mfma_f32_16x16x32_bf16 a[144:147], v[4:7], v[80:83], a[144:147]\n"
        "s_add_u32 s86, s56, 0x380\n"
        "s_addc_u32 s87, s57, 0\n"
        "v_mul_f32 v192, v178, v185\n"
        "v_mfma_f32_16x16x32_bf16 a[176:179], v[12:15], v[80:83], a[176:179]\n"
        "v_cvt_pk_bf16_f32 v190, 0, v190\n"
        "v_cvt_pk_bf16_f32 v192, 0, v192\n"
        "v_mfma_f32_16x16x32_bf16 a[176:179], v[8:11], v[48:51], a[176:179]\n"
        "v_add_f32 v189, v189, v190\n"
        "v_add_f32 v191, v191, v192\n"
        "v_mfma_f32_16x16x32_bf16 a[180:183], v[8:11], v[52:55], a[180:183]\n"
        "v_cvt_pk_bf16_f32 v189, 0, v189\n"
        "v_cvt_pk_bf16_f32 v191, 0, v191\n"
        "v_mfma_f32_16x16x32_bf16 a[180:183], v[12:15], v[84:87], a[180:183]\n"
        "v_lshlrev_b32 v190, 16, v119\n"
        "v_lshlrev_b32 v192, 16, v121\n"
        "v_mfma_f32_16x16x32_bf16 a[148:151], v[4:7], v[84:87], a[148:151]\n"
        "v_mul_f32 v189, v189, v190\n"
        "v_mul_f32 v191, v191, v192\n"
        "v_mfma_f32_16x16x32_bf16 a[148:151], v[0:3], v[52:55], a[148:151]\n"
        "v_cvt_pk_bf16_f32 v203, v191, v189\n"
        "v_mul_f32 v193, v176, v188\n"
        "v_mfma_f32_16x16x32_bf16 a[152:155], v[0:3], v[56:59], a[152:155]\n"
        "v_mul_f32 v195, v177, v188\n"
        "v_cvt_pk_bf16_f32 v193, 0, v193\n"
        "v_mfma_f32_16x16x32_bf16 a[152:155], v[4:7], v[88:91], a[152:155]\n"
        "v_cvt_pk_bf16_f32 v195, 0, v195\n"
        "v_mul_f32 v194, v177, v187\n"
        "v_mfma_f32_16x16x32_bf16 a[184:187], v[12:15], v[88:91], a[184:187]\n"
        "v_mul_f32 v196, v178, v187\n"
        "v_cvt_pk_bf16_f32 v194, 0, v194\n"
        "v_mfma_f32_16x16x32_bf16 a[184:187], v[8:11], v[56:59], a[184:187]\n"
        "s_waitcnt vmcnt(13) lgkmcnt(0)\n"
        "v_cvt_pk_bf16_f32 v196, 0, v196\n"
        "v_add_f32 v193, v193, v194\n"
        "v_mfma_f32_16x16x32_bf16 a[188:191], v[8:11], v[60:63], a[188:191]\n"
        "s_barrier\n"
        "v_mfma_f32_16x16x32_bf16 a[188:191], v[12:15], v[92:95], a[188:191]\n"
        "global_load_lds_dwordx4 v234, s[86:87] offset:0\n"
        "ds_read_b128 v[8:11], v224 offset:51200\n"
        "v_mfma_f32_16x16x32_bf16 a[156:159], v[4:7], v[92:95], a[156:159]\n"
        "ds_read_b128 v[4:7], v225 offset:49152\n"
        "v_add_f32 v195, v195, v196\n"
        "v_mfma_f32_16x16x32_bf16 a[156:159], v[0:3], v[60:63], a[156:159]\n"
        "ds_read_b128 v[0:3], v224 offset:49152\n"
        "v_cvt_pk_bf16_f32 v193, 0, v193\n"
        "v_mfma_f32_16x16x32_bf16 a[192:195], v[32:35], v[16:19], a[192:195]\n"
        "ds_read_b128 v[12:15], v225 offset:51200\n"
        "v_cvt_pk_bf16_f32 v195, 0, v195\n"
        "v_mfma_f32_16x16x32_bf16 a[192:195], v[64:67], v[20:23], a[192:195]\n"
        "global_load_lds_dwordx4 v235, s[86:87] offset:1024\n"
        "v_mul_f32 v194, v178, v186\n"
        "v_mfma_f32_16x16x32_bf16 a[224:227], v[64:67], v[28:31], a[224:227]\n"
        "ds_read_b64_tr_b16 v[64:65], v216 offset:8192\n"
        "v_mul_f32 v196, v179, v186\n"
        "v_mfma_f32_16x16x32_bf16 a[224:227], v[32:35], v[24:27], a[224:227]\n"
        "ds_read_b64_tr_b16 v[32:33], v216 offset:0\n"
        "v_cvt_pk_bf16_f32 v194, 0, v194\n"
        "v_mfma_f32_16x16x32_bf16 a[228:231], v[36:39], v[24:27], a[228:231]\n"
        "ds_read_b64_tr_b16 v[34:35], v217 offset:0\n"
        "v_cvt_pk_bf16_f32 v196, 0, v196\n"
        "v_mfma_f32_16x16x32_bf16 a[228:231], v[68:71], v[28:31], a[228:231]\n"
        "ds_read_b64_tr_b16 v[66:67], v217 offset:8192\n"
        "global_load_lds_dwordx4 v236, s[86:87] offset:2048\n"
        "v_mfma_f32_16x16x32_bf16 a[196:199], v[68:71], v[20:23], a[196:199]\n"
        "ds_read_b64_tr_b16 v[68:69], v218 offset:8192\n"
        "v_add_f32 v193, v193, v194\n"
        "v_mfma_f32_16x16x32_bf16 a[196:199], v[36:39], v[16:19], a[196:199]\n"
        "ds_read_b64_tr_b16 v[36:37], v218 offset:0\n"
        "v_add_f32 v195, v195, v196\n"
        "v_mfma_f32_16x16x32_bf16 a[200:203], v[40:43], v[16:19], a[200:203]\n"
        "ds_read_b64_tr_b16 v[38:39], v219 offset:0\n"
        "v_cvt_pk_bf16_f32 v193, 0, v193\n"
        "v_mfma_f32_16x16x32_bf16 a[200:203], v[72:75], v[20:23], a[200:203]\n"
        "ds_read_b64_tr_b16 v[70:71], v219 offset:8192\n"
        "global_load_lds_dwordx4 v237, s[86:87] offset:3072\n"
        "s_add_u32 m0, s72, 0x1000\n"
        "v_mfma_f32_16x16x32_bf16 a[232:235], v[72:75], v[28:31], a[232:235]\n"
        "ds_read_b64_tr_b16 v[72:73], v220 offset:8192\n"
        "s_add_u32 s86, s58, 0x380\n"
        "s_addc_u32 s87, s59, 0\n"
        "v_mfma_f32_16x16x32_bf16 a[232:235], v[40:43], v[24:27], a[232:235]\n"
        "ds_read_b64_tr_b16 v[40:41], v220 offset:0\n"
        "v_cvt_pk_bf16_f32 v195, 0, v195\n"
        "v_mfma_f32_16x16x32_bf16 a[236:239], v[44:47], v[24:27], a[236:239]\n"
        "ds_read_b64_tr_b16 v[42:43], v221 offset:0\n"
        "v_mul_f32 v194, v179, v185\n"
        "v_mfma_f32_16x16x32_bf16 a[236:239], v[76:79], v[28:31], a[236:239]\n"
        "ds_read_b64_tr_b16 v[74:75], v221 offset:8192\n"
        "global_load_lds_dwordx4 v234, s[86:87] offset:0\n"
        "v_mfma_f32_16x16x32_bf16 a[204:207], v[76:79], v[20:23], a[204:207]\n"
        "ds_read_b64_tr_b16 v[76:77], v222 offset:8192\n"
        "v_mul_f32 v196, v180, v185\n"
        "v_mfma_f32_16x16x32_bf16 a[204:207], v[44:47], v[16:19], a[204:207]\n"
        "ds_read_b64_tr_b16 v[44:45], v222 offset:0\n"
        "v_cvt_pk_bf16_f32 v194, 0, v194\n"
        "v_mfma_f32_16x16x32_bf16 a[208:211], v[16:19], v[48:51], a[208:211]\n"
        "ds_read_b64_tr_b16 v[46:47], v223 offset:0\n"
        "v_cvt_pk_bf16_f32 v196, 0, v196\n"
        "LHA2_%=:\n"
        "s_cbranch_execz LHA6_%=\n"
        "v_mfma_f32_16x16x32_bf16 a[208:211], v[20:23], v[80:83], a[208:211]\n"
        "ds_read_b64_tr_b16 v[78:79], v223 offset:8192\n"
        "global_load_lds_dwordx4 v235, s[86:87] offset:1024\n"
        "v_mfma_f32_16x16x32_bf16 a[240:243], v[28:31], v[80:83], a[240:243]\n"
        "ds_read_b64_tr_b16 v[80:81], v216 offset:24576\n"
        "v_add_f32 v193, v193, v194\n"
        "v_mfma_f32_16x16x32_bf16 a[240:243], v[24:27], v[48:51], a[240:243]\n"
        "ds_read_b64_tr_b16 v[48:49], v216 offset:16384\n"
        "v_add_f32 v195, v195, v196\n"
        "v_mfma_f32_16x16x32_bf16 a[244:247], v[24:27], v[52:55], a[244:247]\n"
        "ds_read_b64_tr_b16 v[50:51], v217 offset:16384\n"
        "v_cvt_pk_bf16_f32 v193, 0, v193\n"
        "v_mfma_f32_16x16x32_bf16 a[244:247], v[28:31], v[84:87], a[244:247]\n"
        "ds_read_b64_tr_b16 v[82:83], v217 offset:24576\n"
        "global_load_lds_dwordx4 v236, s[86:87] offset:2048\n"
        "v_mfma_f32_16x16x32_bf16 a[212:215], v[20:23], v[84:87], a[212:215]\n"
        "ds_read_b64_tr_b16 v[84:85], v218 offset:24576\n"
        "v_cvt_pk_bf16_f32 v195, 0, v195\n"
        "v_mfma_f32_16x16x32_bf16 a[212:215], v[16:19], v[52:55], a[212:215]\n"
        "ds_read_b64_tr_b16 v[52:53], v218 offset:16384\n"
        "v_lshlrev_b32 v194, 16, v123\n"
        "v_mfma_f32_16x16x32_bf16 a[216:219], v[16:19], v[56:59], a[216:219]\n"
        "ds_read_b64_tr_b16 v[54:55], v219 offset:16384\n"
        "v_lshlrev_b32 v196, 16, v125\n"
        "v_mfma_f32_16x16x32_bf16 a[216:219], v[20:23], v[88:91], a[216:219]\n"
        "ds_read_b64_tr_b16 v[86:87], v219 offset:24576\n"
        "global_load_lds_dwordx4 v237, s[86:87] offset:3072\n"
        "s_add_u32 m0, s73, 0x18000\n"
        "v_mfma_f32_16x16x32_bf16 a[248:251], v[28:31], v[88:91], a[248:251]\n"
        "ds_read_b64_tr_b16 v[88:89], v220 offset:24576\n"
        "s_add_u32 s86, s60, 0x180000\n"
        "s_addc_u32 s87, s61, 0\n"
        "v_mfma_f32_16x16x32_bf16 a[248:251], v[24:27], v[56:59], a[248:251]\n"
        "ds_read_b64_tr_b16 v[56:57], v220 offset:16384\n"
        "v_mul_f32 v193, v193, v194\n"
        "v_mfma_f32_16x16x32_bf16 a[252:255], v[24:27], v[60:63], a[252:255]\n"
        "ds_read_b64_tr_b16 v[58:59], v221 offset:16384\n"
        "v_mul_f32 v195, v195, v196\n"
        "v_mfma_f32_16x16x32_bf16 a[252:255], v[28:31], v[92:95], a[252:255]\n"
        "ds_read_b64_tr_b16 v[90:91], v221 offset:24576\n"
        "v_cvt_pk_bf16_f32 v202, v195, v193\n"
        "v_mfma_f32_16x16x32_bf16 a[220:223], v[20:23], v[92:95], a[220:223]\n"
        "ds_read_b64_tr_b16 v[92:93], v222 offset:24576\n"
        "v_mul_f32 v189, v178, v188\n"
        "v_mfma_f32_16x16x32_bf16 a[220:223], v[16:19], v[60:63], a[220:223]\n"
        "ds_read_b64_tr_b16 v[60:61], v222 offset:16384\n"
        "s_waitcnt lgkmcnt(8)\n"
        "v_mfma_f32_16x16x32_bf16 a[0:3], v[32:35], v[0:3], a[0:3]\n"
        "ds_read_b64_tr_b16 v[62:63], v223 offset:16384\n"
        "global_load_dwordx4 v[154:157], v254, s[92:93] offset:3072\n"
        "v_mfma_f32_16x16x32_bf16 a[0:3], v[64:67], v[4:7], a[0:3]\n"
        "ds_read_b64_tr_b16 v[94:95], v223 offset:24576\n"
        "v_mul_f32 v191, v179, v188\n"
        "v_mfma_f32_16x16x32_bf16 a[32:35], v[64:67], v[12:15], a[32:35]\n"
        "ds_read_b128 v[16:19], v224 offset:53248\n"
        "v_cvt_pk_bf16_f32 v189, 0, v189\n"
        "v_mfma_f32_16x16x32_bf16 a[32:35], v[32:35], v[8:11], a[32:35]\n"
        "ds_read_b128 v[20:23], v225 offset:53248\n"
        "v_cvt_pk_bf16_f32 v191, 0, v191\n"
        "v_mfma_f32_16x16x32_bf16 a[36:39], v[36:39], v[8:11], a[36:39]\n"
        "ds_read_b128 v[24:27], v224 offset:55296\n"
        "v_mul_f32 v190, v179, v187\n"
        "v_mfma_f32_16x16x32_bf16 a[36:39], v[68:71], v[12:15], a[36:39]\n"
        "ds_read_b128 v[28:31], v225 offset:55296\n"
        "v_mul_f32 v192, v180, v187\n"
        "v_mfma_f32_16x16x32_bf16 a[4:7], v[68:71], v[4:7], a[4:7]\n"
        "v_cvt_pk_bf16_f32 v190, 0, v190\n"
        "v_cvt_pk_bf16_f32 v192, 0, v192\n"
        "v_mfma_f32_16x16x32_bf16 a[4:7], v[36:39], v[0:3], a[4:7]\n"
        "v_add_f32 v189, v189, v190\n"
        "v_add_f32 v191, v191, v192\n"
        "v_mfma_f32_16x16x32_bf16 a[8:11], v[40:43], v[0:3], a[8:11]\n"
        "v_cvt_pk_bf16_f32 v189, 0, v189\n"
        "v_cvt_pk_bf16_f32 v191, 0, v191\n"
        "v_mfma_f32_16x16x32_bf16 a[8:11], v[72:75], v[4:7], a[8:11]\n"
        "v_mul_f32 v190, v180, v186\n"
        "v_mul_f32 v192, v181, v186\n"
        "v_mfma_f32_16x16x32_bf16 a[40:43], v[72:75], v[12:15], a[40:43]\n"
        "v_cvt_pk_bf16_f32 v190, 0, v190\n"
        "v_cvt_pk_bf16_f32 v192, 0, v192\n"
        "v_mfma_f32_16x16x32_bf16 a[40:43], v[40:43], v[8:11], a[40:43]\n"
        "v_add_f32 v189, v189, v190\n"
        "v_add_f32 v191, v191, v192\n"
        "v_mfma_f32_16x16x32_bf16 a[44:47], v[44:47], v[8:11], a[44:47]\n"
        "v_cvt_pk_bf16_f32 v189, 0, v189\n"
        "v_cvt_pk_bf16_f32 v191, 0, v191\n"
        "v_mfma_f32_16x16x32_bf16 a[44:47], v[76:79], v[12:15], a[44:47]\n"
        "v_mul_f32 v190, v181, v185\n"
        "v_mul_f32 v192, v182, v185\n"
        "v_mfma_f32_16x16x32_bf16 a[12:15], v[76:79], v[4:7], a[12:15]\n"
        "v_cvt_pk_bf16_f32 v190, 0, v190\n"
        "v_cvt_pk_bf16_f32 v192, 0, v192\n"
        "v_mfma_f32_16x16x32_bf16 a[12:15], v[44:47], v[0:3], a[12:15]\n"
        "global_load_dword v253, v173, s[82:83] offset:0\n"
        "s_add_u32 s82, s12, 0x600000\n"
        "s_addc_u32 s83, s13, 0\n"
        "v_mfma_f32_16x16x32_bf16 a[16:19], v[0:3], v[48:51], a[16:19]\n"
        "s_waitcnt lgkmcnt(0)\n"
        "v_add_f32 v189, v189, v190\n"
        "v_add_f32 v191, v191, v192\n"
        "v_mfma_f32_16x16x32_bf16 a[16:19], v[4:7], v[80:83], a[16:19]\n"
        "v_cvt_pk_bf16_f32 v189, 0, v189\n"
        "v_cvt_pk_bf16_f32 v191, 0, v191\n"
        "v_mfma_f32_16x16x32_bf16 a[48:51], v[12:15], v[80:83], a[48:51]\n"
        "v_lshlrev_b32 v190, 16, v127\n"
        "v_lshlrev_b32 v192, 16, v129\n"
        "v_mfma_f32_16x16x32_bf16 a[48:51], v[8:11], v[48:51], a[48:51]\n"
        "v_mul_f32 v189, v189, v190\n"
        "v_mul_f32 v191, v191, v192\n"
        "v_mfma_f32_16x16x32_bf16 a[52:55], v[8:11], v[52:55], a[52:55]\n"
        "v_cvt_pk_bf16_f32 v201, v191, v189\n"
        "v_mul_f32 v193, v180, v188\n"
        "v_mfma_f32_16x16x32_bf16 a[52:55], v[12:15], v[84:87], a[52:55]\n"
        "v_mul_f32 v195, v181, v188\n"
        "v_cvt_pk_bf16_f32 v193, 0, v193\n"
        "v_mfma_f32_16x16x32_bf16 a[20:23], v[4:7], v[84:87], a[20:23]\n"
        "v_cvt_pk_bf16_f32 v195, 0, v195\n"
        "v_mul_f32 v194, v181, v187\n"
        "v_mfma_f32_16x16x32_bf16 a[20:23], v[0:3], v[52:55], a[20:23]\n"
        "v_mul_f32 v196, v182, v187\n"
        "v_cvt_pk_bf16_f32 v194, 0, v194\n"
        "v_mfma_f32_16x16x32_bf16 a[24:27], v[0:3], v[56:59], a[24:27]\n"
        "v_cvt_pk_bf16_f32 v196, 0, v196\n"
        "v_add_f32 v193, v193, v194\n"
        "v_mfma_f32_16x16x32_bf16 a[24:27], v[4:7], v[88:91], a[24:27]\n"
        "v_add_f32 v195, v195, v196\n"
        "v_cvt_pk_bf16_f32 v193, 0, v193\n"
        "v_mfma_f32_16x16x32_bf16 a[56:59], v[12:15], v[88:91], a[56:59]\n"
        "v_cvt_pk_bf16_f32 v195, 0, v195\n"
        "v_mul_f32 v194, v182, v186\n"
        "v_mfma_f32_16x16x32_bf16 a[56:59], v[8:11], v[56:59], a[56:59]\n"
        "v_mul_f32 v196, v183, v186\n"
        "v_cvt_pk_bf16_f32 v194, 0, v194\n"
        "v_mfma_f32_16x16x32_bf16 a[60:63], v[8:11], v[60:63], a[60:63]\n"
        "ds_read_b128 v[8:11], v224 offset:34816\n"
        "v_cvt_pk_bf16_f32 v196, 0, v196\n"
        "v_mfma_f32_16x16x32_bf16 a[60:63], v[12:15], v[92:95], a[60:63]\n"
        "ds_read_b128 v[12:15], v225 offset:34816\n"
        "v_add_f32 v193, v193, v194\n"
        "v_mfma_f32_16x16x32_bf16 a[28:31], v[4:7], v[92:95], a[28:31]\n"
        "ds_read_b128 v[4:7], v225 offset:32768\n"
        "v_add_f32 v195, v195, v196\n"
        "v_mfma_f32_16x16x32_bf16 a[28:31], v[0:3], v[60:63], a[28:31]\n"
        "global_load_lds_dwordx4 v238, s[86:87] offset:0\n"
        "ds_read_b128 v[0:3], v224 offset:32768\n"
        "v_mfma_f32_16x16x32_bf16 a[64:67], v[32:35], v[16:19], a[64:67]\n"
        "v_cvt_pk_bf16_f32 v193, 0, v193\n"
        "v_cvt_pk_bf16_f32 v195, 0, v195\n"
        "v_mfma_f32_16x16x32_bf16 a[64:67], v[64:67], v[20:23], a[64:67]\n"
        "v_mul_f32 v194, v183, v185\n"
        "v_mul_f32 v196, v184, v185\n"
        "v_mfma_f32_16x16x32_bf16 a[96:99], v[64:67], v[28:31], a[96:99]\n"
        "v_cvt_pk_bf16_f32 v194, 0, v194\n"
        "v_cvt_pk_bf16_f32 v196, 0, v196\n"
        "v_mfma_f32_16x16x32_bf16 a[96:99], v[32:35], v[24:27], a[96:99]\n"
        "v_add_f32 v193, v193, v194\n"
        "v_add_f32 v195, v195, v196\n"
        "v_mfma_f32_16x16x32_bf16 a[100:103], v[36:39], v[24:27], a[100:103]\n"
        "v_cvt_pk_bf16_f32 v193, 0, v193\n"
        "v_cvt_pk_bf16_f32 v195, 0, v195\n"
        "v_mfma_f32_16x16x32_bf16 a[100:103], v[68:71], v[28:31], a[100:103]\n"
        "global_load_lds_dwordx4 v239, s[86:87] offset:1024\n"
        "v_lshlrev_b32 v194, 16, v131\n"
        "v_mfma_f32_16x16x32_bf16 a[68:71], v[68:71], v[20:23], a[68:71]\n"
        "v_lshlrev_b32 v196, 16, v133\n"
        "v_mul_f32 v193, v193, v194\n"
        "v_mfma_f32_16x16x32_bf16 a[68:71], v[36:39], v[16:19], a[68:71]\n"
        "v_mul_f32 v195, v195, v196\n"
        "v_cvt_pk_bf16_f32 v200, v195, v193\n"
        "v_mfma_f32_16x16x32_bf16 a[72:75], v[40:43], v[16:19], a[72:75]\n"
        "v_lshlrev_b32 v185, 16, v140\n"
        "v_and_b32 v186, s35, v140\n"
        "v_mfma_f32_16x16x32_bf16 a[72:75], v[72:75], v[20:23], a[72:75]\n"
        "global_store_dwordx4 v171, v[200:203], s[76:77] offset:-4096 sc1\n"
        "v_lshlrev_b32 v187, 16, v141\n"
        "v_and_b32 v188, s35, v141\n"
        "v_and_b32 v174, s35, v97\n"
        "v_and_b32 v175, s35, v99\n"
        "v_mfma_f32_16x16x32_bf16 a[104:107], v[72:75], v[28:31], a[104:107]\n"
        "v_and_b32 v176, s35, v101\n"
        "v_and_b32 v177, s35, v103\n"
        "v_mfma_f32_16x16x32_bf16 a[104:107], v[40:43], v[24:27], a[104:107]\n"
        "v_and_b32 v178, s35, v105\n"
        "v_and_b32 v179, s35, v107\n"
        "v_mfma_f32_16x16x32_bf16 a[108:111], v[44:47], v[24:27], a[108:111]\n"
        "v_and_b32 v180, s35, v109\n"
        "v_and_b32 v181, s35, v111\n"
        "v_mfma_f32_16x16x32_bf16 a[108:111], v[76:79], v[28:31], a[108:111]\n"
        "v_and_b32 v182, s35, v113\n"
        "v_and_b32 v183, s35, v115\n"
        "v_mfma_f32_16x16x32_bf16 a[76:79], v[76:79], v[20:23], a[76:79]\n"
        "global_load_lds_dwordx4 v240, s[86:87] offset:2048\n"
        "v_mfma_f32_16x16x32_bf16 a[76:79], v[44:47], v[16:19], a[76:79]\n"
        "v_and_b32 v184, s35, v117\n"
        "v_mul_f32 v189, v174, v188\n"
        "v_mfma_f32_16x16x32_bf16 a[80:83], v[16:19], v[48:51], a[80:83]\n"
        "v_mul_f32 v191, v175, v188\n"
        "v_cvt_pk_bf16_f32 v189, 0, v189\n"
        "v_mfma_f32_16x16x32_bf16 a[80:83], v[20:23], v[80:83], a[80:83]\n"
        "v_cvt_pk_bf16_f32 v191, 0, v191\n"
        "v_mul_f32 v190, v175, v187\n"
        "v_mfma_f32_16x16x32_bf16 a[112:115], v[28:31], v[80:83], a[112:115]\n"
        "v_mul_f32 v192, v176, v187\n"
        "v_cvt_pk_bf16_f32 v190, 0, v190\n"
        "v_mfma_f32_16x16x32_bf16 a[112:115], v[24:27], v[48:51], a[112:115]\n"
        "v_cvt_pk_bf16_f32 v192, 0, v192\n"
        "v_add_f32 v189, v189, v190\n"
        "v_mfma_f32_16x16x32_bf16 a[116:119], v[24:27], v[52:55], a[116:119]\n"
        "global_load_lds_dwordx4 v241, s[86:87] offset:3072\n"
        "s_add_u32 m0, s73, 0x1c000\n"
        "v_mfma_f32_16x16x32_bf16 a[116:119], v[28:31], v[84:87], a[116:119]\n"
        "s_add_u32 s86, s62, 0x180000\n"
        "s_addc_u32 s87, s63, 0\n"
        "v_add_f32 v191, v191, v192\n"
        "v_mfma_f32_16x16x32_bf16 a[84:87], v[20:23], v[84:87], a[84:87]\n"
        "v_cvt_pk_bf16_f32 v189, 0, v189\n"
        "v_cvt_pk_bf16_f32 v191, 0, v191\n"
        "v_mfma_f32_16x16x32_bf16 a[84:87], v[16:19], v[52:55], a[84:87]\n"
        "v_mul_f32 v190, v176, v186\n"
        "v_mul_f32 v192, v177, v186\n"
        "v_mfma_f32_16x16x32_bf16 a[88:91], v[16:19], v[56:59], a[88:91]\n"
        "v_cvt_pk_bf16_f32 v190, 0, v190\n"
        "v_cvt_pk_bf16_f32 v192, 0, v192\n"
        "v_mfma_f32_16x16x32_bf16 a[88:91], v[20:23], v[88:91], a[88:91]\n"
        "v_add_f32 v189, v189, v190\n"
        "v_add_f32 v191, v191, v192\n"
        "v_mfma_f32_16x16x32_bf16 a[120:123], v[28:31], v[88:91], a[120:123]\n"
        "global_load_lds_dwordx4 v238, s[86:87] offset:0\n"
        "v_mfma_f32_16x16x32_bf16 a[120:123], v[24:27], v[56:59], a[120:123]\n"
        "v_cvt_pk_bf16_f32 v189, 0, v189\n"
        "v_cvt_pk_bf16_f32 v191, 0, v191\n"
        "v_mfma_f32_16x16x32_bf16 a[124:127], v[24:27], v[60:63], a[124:127]\n"
        "ds_read_b128 v[24:27], v224 offset:38912\n"
        "v_mul_f32 v190, v177, v185\n"
        "v_mfma_f32_16x16x32_bf16 a[124:127], v[28:31], v[92:95], a[124:127]\n"
        "ds_read_b128 v[28:31], v225 offset:38912\n"
        "v_mul_f32 v192, v178, v185\n"
        "v_mfma_f32_16x16x32_bf16 a[92:95], v[20:23], v[92:95], a[92:95]\n"
        "ds_read_b128 v[20:23], v225 offset:36864\n"
        "v_cvt_pk_bf16_f32 v190, 0, v190\n"
        "v_mfma_f32_16x16x32_bf16 a[92:95], v[16:19], v[60:63], a[92:95]\n"
        "ds_read_b128 v[16:19], v224 offset:36864\n"
        "s_waitcnt lgkmcnt(4)\n"
        "v_mfma_f32_16x16x32_bf16 a[128:131], v[32:35], v[0:3], a[128:131]\n"
        "global_load_lds_dwordx4 v239, s[86:87] offset:1024\n"
        "v_mfma_f32_16x16x32_bf16 a[128:131], v[64:67], v[4:7], a[128:131]\n"
        "v_cvt_pk_bf16_f32 v192, 0, v192\n"
        "v_add_f32 v189, v189, v190\n"
        "v_mfma_f32_16x16x32_bf16 a[160:163], v[64:67], v[12:15], a[160:163]\n"
        "v_add_f32 v191, v191, v192\n"
        "v_cvt_pk_bf16_f32 v189, 0, v189\n"
        "v_mfma_f32_16x16x32_bf16 a[160:163], v[32:35], v[8:11], a[160:163]\n"
        "v_cvt_pk_bf16_f32 v191, 0, v191\n"
        "v_and_b32 v190, s35, v119\n"
        "v_mfma_f32_16x16x32_bf16 a[164:167], v[36:39], v[8:11], a[164:167]\n"
        "v_and_b32 v192, s35, v121\n"
        "v_mul_f32 v189, v189, v190\n"
        "v_mfma_f32_16x16x32_bf16 a[164:167], v[68:71], v[12:15], a[164:167]\n"
        "v_mul_f32 v191, v191, v192\n"
        "v_cvt_pk_bf16_f32 v211, v191, v189\n"
        "v_mfma_f32_16x16x32_bf16 a[132:135], v[68:71], v[4:7], a[132:135]\n"
        "global_load_lds_dwordx4 v240, s[86:87] offset:2048\n"
        "v_mfma_f32_16x16x32_bf16 a[132:135], v[36:39], v[0:3], a[132:135]\n"
        "v_mul_f32 v193, v176, v188\n"
        "v_mul_f32 v195, v177, v188\n"
        "v_mfma_f32_16x16x32_bf16 a[136:139], v[40:43], v[0:3], a[136:139]\n"
        "v_cvt_pk_bf16_f32 v193, 0, v193\n"
        "v_cvt_pk_bf16_f32 v195, 0, v195\n"
        "v_mfma_f32_16x16x32_bf16 a[136:139], v[72:75], v[4:7], a[136:139]\n"
        "v_mul_f32 v194, v177, v187\n"
        "v_mul_f32 v196, v178, v187\n"
        "v_mfma_f32_16x16x32_bf16 a[168:171], v[72:75], v[12:15], a[168:171]\n"
        "v_cvt_pk_bf16_f32 v194, 0, v194\n"
        "v_cvt_pk_bf16_f32 v196, 0, v196\n"
        "v_mfma_f32_16x16x32_bf16 a[168:171], v[40:43], v[8:11], a[168:171]\n"
        "v_add_f32 v193, v193, v194\n"
        "v_add_f32 v195, v195, v196\n"
        "v_mfma_f32_16x16x32_bf16 a[172:175], v[44:47], v[8:11], a[172:175]\n"
        "global_load_lds_dwordx4 v241, s[86:87] offset:3072\n"
        "s_add_u32 m0, s72, 0x8000\n"
        "v_mfma_f32_16x16x32_bf16 a[172:175], v[76:79], v[12:15], a[172:175]\n"
        "s_add_u32 s86, s56, 0x180\n"
        "s_addc_u32 s87, s57, 0\n"
        "v_cvt_pk_bf16_f32 v193, 0, v193\n"
        "v_mfma_f32_16x16x32_bf16 a[140:143], v[76:79], v[4:7], a[140:143]\n"
        "v_cvt_pk_bf16_f32 v195, 0, v195\n"
        "v_mul_f32 v194, v178, v186\n"
        "v_mfma_f32_16x16x32_bf16 a[140:143], v[44:47], v[0:3], a[140:143]\n"
        "v_mul_f32 v196, v179, v186\n"
        "v_cvt_pk_bf16_f32 v194, 0, v194\n"
        "v_mfma_f32_16x16x32_bf16 a[144:147], v[0:3], v[48:51], a[144:147]\n"
        "v_cvt_pk_bf16_f32 v196, 0, v196\n"
        "v_add_f32 v193, v193, v194\n"
        "v_mfma_f32_16x16x32_bf16 a[144:147], v[4:7], v[80:83], a[144:147]\n"
        "v_add_f32 v195, v195, v196\n"
        "v_cvt_pk_bf16_f32 v193, 0, v193\n"
        "v_mfma_f32_16x16x32_bf16 a[176:179], v[12:15], v[80:83], a[176:179]\n"
        "v_cvt_pk_bf16_f32 v195, 0, v195\n"
        "v_mul_f32 v194, v179, v185\n"
        "v_mfma_f32_16x16x32_bf16 a[176:179], v[8:11], v[48:51], a[176:179]\n"
        "v_mul_f32 v196, v180, v185\n"
        "v_cvt_pk_bf16_f32 v194, 0, v194\n"
        "v_mfma_f32_16x16x32_bf16 a[180:183], v[8:11], v[52:55], a[180:183]\n"
        "v_cvt_pk_bf16_f32 v196, 0, v196\n"
        "v_add_f32 v193, v193, v194\n"
        "v_mfma_f32_16x16x32_bf16 a[180:183], v[12:15], v[84:87], a[180:183]\n"
        "v_add_f32 v195, v195, v196\n"
        "v_cvt_pk_bf16_f32 v193, 0, v193\n"
        "v_mfma_f32_16x16x32_bf16 a[148:151], v[4:7], v[84:87], a[148:151]\n"
        "v_cvt_pk_bf16_f32 v195, 0, v195\n"
        "v_and_b32 v194, s35, v123\n"
        "v_mfma_f32_16x16x32_bf16 a[148:151], v[0:3], v[52:55], a[148:151]\n"
        "v_and_b32 v196, s35, v125\n"
        "v_mul_f32 v193, v193, v194\n"
        "v_mfma_f32_16x16x32_bf16 a[152:155], v[0:3], v[56:59], a[152:155]\n"
        "v_mul_f32 v195, v195, v196\n"
        "v_cvt_pk_bf16_f32 v210, v195, v193\n"
        "v_mfma_f32_16x16x32_bf16 a[152:155], v[4:7], v[88:91], a[152:155]\n"
        "v_mul_f32 v189, v178, v188\n"
        "v_mul_f32 v191, v179, v188\n"
        "v_mfma_f32_16x16x32_bf16 a[184:187], v[12:15], v[88:91], a[184:187]\n"
        "v_cvt_pk_bf16_f32 v189, 0, v189\n"
        "v_cvt_pk_bf16_f32 v191, 0, v191\n"
        "v_mfma_f32_16x16x32_bf16 a[184:187], v[8:11], v[56:59], a[184:187]\n"
        "s_waitcnt vmcnt(11) lgkmcnt(0)\n"
        "v_mul_f32 v190, v179, v187\n"
        "v_mul_f32 v192, v180, v187\n"
        "v_mfma_f32_16x16x32_bf16 a[188:191], v[8:11], v[60:63], a[188:191]\n"
        "s_barrier\n"
        "v_mfma_f32_16x16x32_bf16 a[188:191], v[12:15], v[92:95], a[188:191]\n"
        "global_load_lds_dwordx4 v234, s[86:87] offset:0\n"
        "ds_read_b128 v[8:11], v224 offset:18432\n"
        "v_mfma_f32_16x16x32_bf16 a[156:159], v[4:7], v[92:95], a[156:159]\n"
        "ds_read_b128 v[4:7], v225 offset:16384\n"
        "v_cvt_pk_bf16_f32 v190, 0, v190\n"
        "v_mfma_f32_16x16x32_bf16 a[156:159], v[0:3], v[60:63], a[156:159]\n"
        "ds_read_b128 v[0:3], v224 offset:16384\n"
        "v_cvt_pk_bf16_f32 v192, 0, v192\n"
        "v_mfma_f32_16x16x32_bf16 a[192:195], v[32:35], v[16:19], a[192:195]\n"
        "ds_read_b128 v[12:15], v225 offset:18432\n"
        "v_add_f32 v189, v189, v190\n"
        "v_mfma_f32_16x16x32_bf16 a[192:195], v[64:67], v[20:23], a[192:195]\n"
        "global_load_lds_dwordx4 v235, s[86:87] offset:1024\n"
        "v_add_f32 v191, v191, v192\n"
        "v_mfma_f32_16x16x32_bf16 a[224:227], v[64:67], v[28:31], a[224:227]\n"
        "ds_read_b64_tr_b16 v[64:65], v226 offset:8192\n"
        "v_cvt_pk_bf16_f32 v189, 0, v189\n"
        "v_mfma_f32_16x16x32_bf16 a[224:227], v[32:35], v[24:27], a[224:227]\n"
        "ds_read_b64_tr_b16 v[32:33], v226 offset:0\n"
        "v_cvt_pk_bf16_f32 v191, 0, v191\n"
        "v_mfma_f32_16x16x32_bf16 a[228:231], v[36:39], v[24:27], a[228:231]\n"
        "ds_read_b64_tr_b16 v[34:35], v227 offset:0\n"
        "v_mul_f32 v190, v180, v186\n"
        "v_mfma_f32_16x16x32_bf16 a[228:231], v[68:71], v[28:31], a[228:231]\n"
        "ds_read_b64_tr_b16 v[66:67], v227 offset:8192\n"
        "global_load_lds_dwordx4 v236, s[86:87] offset:2048\n"
        "v_mfma_f32_16x16x32_bf16 a[196:199], v[68:71], v[20:23], a[196:199]\n"
        "ds_read_b64_tr_b16 v[68:69], v228 offset:8192\n"
        "v_mul_f32 v192, v181, v186\n"
        "v_mfma_f32_16x16x32_bf16 a[196:199], v[36:39], v[16:19], a[196:199]\n"
        "ds_read_b64_tr_b16 v[36:37], v228 offset:0\n"
        "v_cvt_pk_bf16_f32 v190, 0, v190\n"
        "v_mfma_f32_16x16x32_bf16 a[200:203], v[40:43], v[16:19], a[200:203]\n"
        "ds_read_b64_tr_b16 v[38:39], v229 offset:0\n"
        "v_cvt_pk_bf16_f32 v192, 0, v192\n"
        "v_mfma_f32_16x16x32_bf16 a[200:203], v[72:75], v[20:23], a[200:203]\n"
        "ds_read_b64_tr_b16 v[70:71], v229 offset:8192\n"
        "global_load_lds_dwordx4 v237, s[86:87] offset:3072\n"
        "s_add_u32 m0, s72, 0x9000\n"
        "v_mfma_f32_16x16x32_bf16 a[232:235], v[72:75], v[28:31], a[232:235]\n"
        "ds_read_b64_tr_b16 v[72:73], v230 offset:8192\n"
        "s_add_u32 s86, s58, 0x180\n"
        "s_addc_u32 s87, s59, 0\n"
        "v_mfma_f32_16x16x32_bf16 a[232:235], v[40:43], v[24:27], a[232:235]\n"
        "ds_read_b64_tr_b16 v[40:41], v230 offset:0\n"
        "v_add_f32 v189, v189, v190\n"
        "v_mfma_f32_16x16x32_bf16 a[236:239], v[44:47], v[24:27], a[236:239]\n"
        "ds_read_b64_tr_b16 v[42:43], v231 offset:0\n"
        "v_add_f32 v191, v191, v192\n"
        "v_mfma_f32_16x16x32_bf16 a[236:239], v[76:79], v[28:31], a[236:239]\n"
        "ds_read_b64_tr_b16 v[74:75], v231 offset:8192\n"
        "global_load_lds_dwordx4 v234, s[86:87] offset:0\n"
        "v_mfma_f32_16x16x32_bf16 a[204:207], v[76:79], v[20:23], a[204:207]\n"
        "ds_read_b64_tr_b16 v[76:77], v232 offset:8192\n"
        "v_cvt_pk_bf16_f32 v189, 0, v189\n"
        "v_mfma_f32_16x16x32_bf16 a[204:207], v[44:47], v[16:19], a[204:207]\n"
        "ds_read_b64_tr_b16 v[44:45], v232 offset:0\n"
        "v_cvt_pk_bf16_f32 v191, 0, v191\n"
        "v_mfma_f32_16x16x32_bf16 a[208:211], v[16:19], v[48:51], a[208:211]\n"
        "ds_read_b64_tr_b16 v[46:47], v233 offset:0\n"
        "v_mul_f32 v190, v181, v185\n"
        "v_mfma_f32_16x16x32_bf16 a[208:211], v[20:23], v[80:83], a[208:211]\n"
        "ds_read_b64_tr_b16 v[78:79], v233 offset:8192\n"
        "global_load_lds_dwordx4 v235, s[86:87] offset:1024\n"
        "v_mfma_f32_16x16x32_bf16 a[240:243], v[28:31], v[80:83], a[240:243]\n"
        "ds_read_b64_tr_b16 v[80:81], v226 offset:24576\n"
        "v_mul_f32 v192, v182, v185\n"
        "v_mfma_f32_16x16x32_bf16 a[240:243], v[24:27], v[48:51], a[240:243]\n"
        "ds_read_b64_tr_b16 v[48:49], v226 offset:16384\n"
        "v_cvt_pk_bf16_f32 v190, 0, v190\n"
        "v_mfma_f32_16x16x32_bf16 a[244:247], v[24:27], v[52:55], a[244:247]\n"
        "ds_read_b64_tr_b16 v[50:51], v227 offset:16384\n"
        "v_cvt_pk_bf16_f32 v192, 0, v192\n"
        "v_mfma_f32_16x16x32_bf16 a[244:247], v[28:31], v[84:87], a[244:247]\n"
        "ds_read_b64_tr_b16 v[82:83], v227 offset:24576\n"
        "global_load_lds_dwordx4 v236, s[86:87] offset:2048\n"
        "v_mfma_f32_16x16x32_bf16 a[212:215], v[20:23], v[84:87], a[212:215]\n"
        "ds_read_b64_tr_b16 v[84:85], v228 offset:24576\n"
        "v_add_f32 v189, v189, v190\n"
        "v_mfma_f32_16x16x32_bf16 a[212:215], v[16:19], v[52:55], a[212:215]\n"
        "ds_read_b64_tr_b16 v[52:53], v228 offset:16384\n"
        "v_add_f32 v191, v191, v192\n"
        "v_mfma_f32_16x16x32_bf16 a[216:219], v[16:19], v[56:59], a[216:219]\n"
        "ds_read_b64_tr_b16 v[54:55], v229 offset:16384\n"
        "v_cvt_pk_bf16_f32 v189, 0, v189\n"
        "v_mfma_f32_16x16x32_bf16 a[216:219], v[20:23], v[88:91], a[216:219]\n"
        "ds_read_b64_tr_b16 v[86:87], v229 offset:24576\n"
        "global_load_lds_dwordx4 v237, s[86:87] offset:3072\n"
        "s_add_u32 m0, s73, 0x20000\n"
        "v_mfma_f32_16x16x32_bf16 a[248:251], v[28:31], v[88:91], a[248:251]\n"
        "ds_read_b64_tr_b16 v[88:89], v230 offset:24576\n"
        "s_add_u32 s86, s68, 0x80000\n"
        "s_addc_u32 s87, s69, 0\n"
        "v_mfma_f32_16x16x32_bf16 a[248:251], v[24:27], v[56:59], a[248:251]\n"
        "ds_read_b64_tr_b16 v[56:57], v230 offset:16384\n"
        "v_cvt_pk_bf16_f32 v191, 0, v191\n"
        "v_mfma_f32_16x16x32_bf16 a[252:255], v[24:27], v[60:63], a[252:255]\n"
        "ds_read_b64_tr_b16 v[58:59], v231 offset:16384\n"
        "v_and_b32 v190, s35, v127\n"
        "v_mfma_f32_16x16x32_bf16 a[252:255], v[28:31], v[92:95], a[252:255]\n"
        "ds_read_b64_tr_b16 v[90:91], v231 offset:24576\n"
        "v_and_b32 v192, s35, v129\n"
        "v_mfma_f32_16x16x32_bf16 a[220:223], v[20:23], v[92:95], a[220:223]\n"
        "ds_read_b64_tr_b16 v[92:93], v232 offset:24576\n"
        "v_mul_f32 v189, v189, v190\n"
        "v_mfma_f32_16x16x32_bf16 a[220:223], v[16:19], v[60:63], a[220:223]\n"
        "ds_read_b64_tr_b16 v[60:61], v232 offset:16384\n"
        "s_waitcnt lgkmcnt(8)\n"
        "v_mfma_f32_16x16x32_bf16 a[0:3], v[32:35], v[0:3], a[0:3]\n"
        "ds_read_b64_tr_b16 v[62:63], v233 offset:16384\n"
        "v_mul_f32 v191, v191, v192\n"
        "v_mfma_f32_16x16x32_bf16 a[0:3], v[64:67], v[4:7], a[0:3]\n"
        "ds_read_b64_tr_b16 v[94:95], v233 offset:24576\n"
        "v_cvt_pk_bf16_f32 v209, v191, v189\n"
        "v_mfma_f32_16x16x32_bf16 a[32:35], v[64:67], v[12:15], a[32:35]\n"
        "ds_read_b128 v[16:19], v224 offset:20480\n"
        "v_mul_f32 v193, v180, v188\n"
        "v_mfma_f32_16x16x32_bf16 a[32:35], v[32:35], v[8:11], a[32:35]\n"
        "ds_read_b128 v[20:23], v225 offset:20480\n"
        "v_mul_f32 v195, v181, v188\n"
        "v_mfma_f32_16x16x32_bf16 a[36:39], v[36:39], v[8:11], a[36:39]\n"
        "ds_read_b128 v[24:27], v224 offset:22528\n"
        "v_cvt_pk_bf16_f32 v193, 0, v193\n"
        "v_mfma_f32_16x16x32_bf16 a[36:39], v[68:71], v[12:15], a[36:39]\n"
        "ds_read_b128 v[28:31], v225 offset:22528\n"
        "v_cvt_pk_bf16_f32 v195, 0, v195\n"
        "v_mfma_f32_16x16x32_bf16 a[4:7], v[68:71], v[4:7], a[4:7]\n"
        "v_mul_f32 v194, v181, v187\n"
        "v_mul_f32 v196, v182, v187\n"
        "v_mfma_f32_16x16x32_bf16 a[4:7], v[36:39], v[0:3], a[4:7]\n"
        "v_cvt_pk_bf16_f32 v194, 0, v194\n"
        "v_cvt_pk_bf16_f32 v196, 0, v196\n"
        "v_mfma_f32_16x16x32_bf16 a[8:11], v[40:43], v[0:3], a[8:11]\n"
        "v_add_f32 v193, v193, v194\n"
        "v_add_f32 v195, v195, v196\n"
        "v_mfma_f32_16x16x32_bf16 a[8:11], v[72:75], v[4:7], a[8:11]\n"
        "v_cvt_pk_bf16_f32 v193, 0, v193\n"
        "v_cvt_pk_bf16_f32 v195, 0, v195\n"
        "v_mfma_f32_16x16x32_bf16 a[40:43], v[72:75], v[12:15], a[40:43]\n"
        "v_mul_f32 v194, v182, v186\n"
        "v_mul_f32 v196, v183, v186\n"
        "v_mfma_f32_16x16x32_bf16 a[40:43], v[40:43], v[8:11], a[40:43]\n"
        "v_cvt_pk_bf16_f32 v194, 0, v194\n"
        "v_cvt_pk_bf16_f32 v196, 0, v196\n"
        "v_mfma_f32_16x16x32_bf16 a[44:47], v[44:47], v[8:11], a[44:47]\n"
        "v_add_f32 v193, v193, v194\n"
        "v_add_f32 v195, v195, v196\n"
        "v_mfma_f32_16x16x32_bf16 a[44:47], v[76:79], v[12:15], a[44:47]\n"
        "v_cvt_pk_bf16_f32 v193, 0, v193\n"
        "v_cvt_pk_bf16_f32 v195, 0, v195\n"
        "v_mfma_f32_16x16x32_bf16 a[12:15], v[76:79], v[4:7], a[12:15]\n"
        "v_mul_f32 v194, v183, v185\n"
        "v_mul_f32 v196, v184, v185\n"
        "v_mfma_f32_16x16x32_bf16 a[12:15], v[44:47], v[0:3], a[12:15]\n"
        "global_load_dword v253, v173, s[82:83] offset:0\n"
        "s_add_u32 s82, s12, 0x700000\n"
        "s_addc_u32 s83, s13, 0\n"
        "v_mfma_f32_16x16x32_bf16 a[16:19], v[0:3], v[48:51], a[16:19]\n"
        "s_waitcnt lgkmcnt(0)\n"
        "v_cvt_pk_bf16_f32 v194, 0, v194\n"
        "v_cvt_pk_bf16_f32 v196, 0, v196\n"
        "v_mfma_f32_16x16x32_bf16 a[16:19], v[4:7], v[80:83], a[16:19]\n"
        "v_add_f32 v193, v193, v194\n"
        "v_add_f32 v195, v195, v196\n"
        "v_mfma_f32_16x16x32_bf16 a[48:51], v[12:15], v[80:83], a[48:51]\n"
        "v_cvt_pk_bf16_f32 v193, 0, v193\n"
        "v_cvt_pk_bf16_f32 v195, 0, v195\n"
        "v_mfma_f32_16x16x32_bf16 a[48:51], v[8:11], v[48:51], a[48:51]\n"
        "v_and_b32 v194, s35, v131\n"
        "v_and_b32 v196, s35, v133\n"
        "v_mfma_f32_16x16x32_bf16 a[52:55], v[8:11], v[52:55], a[52:55]\n"
        "v_mul_f32 v193, v193, v194\n"
        "v_mul_f32 v195, v195, v196\n"
        "v_mfma_f32_16x16x32_bf16 a[52:55], v[12:15], v[84:87], a[52:55]\n"
        "v_cvt_pk_bf16_f32 v208, v195, v193\n"
        "v_readfirstlane_b32 s70, v250\n"
        "v_mfma_f32_16x16x32_bf16 a[20:23], v[4:7], v[84:87], a[20:23]\n"
        "global_store_dwordx4 v171, v[208:211], s[76:77] offset:0 sc1\n"
        "s_add_u32 s76, s38, 0x3b20040\n"
        "s_addc_u32 s77, s39, 0\n"
        "v_readfirstlane_b32 s71, v251\n"
        "v_lshlrev_b32 v174, 16, v158\n"
        "v_and_b32 v175, s35, v158\n"
        "v_mfma_f32_16x16x32_bf16 a[20:23], v[0:3], v[52:55], a[20:23]\n"
        "s_add_u32 s70, s70, 0x1\n"
        "s_addc_u32 s71, s71, 0\n"
        "v_mov_b32 v250, s70\n"
        "v_mfma_f32_16x16x32_bf16 a[24:27], v[0:3], v[56:59], a[24:27]\n"
        "s_and_b32 s53, s40, 1\n"
        "v_mov_b32 v251, s71\n"
        "v_lshlrev_b32 v176, 16, v159\n"
        "v_mfma_f32_16x16x32_bf16 a[24:27], v[4:7], v[88:91], a[24:27]\n"
        "s_lshl_b32 s53, s53, 10\n"
        "v_and_b32 v177, s35, v159\n"
        "v_lshlrev_b32 v178, 16, v160\n"
        "v_mfma_f32_16x16x32_bf16 a[56:59], v[12:15], v[88:91], a[56:59]\n"
        "s_sub_u32 s53, 0x8000, s53\n"
        "v_and_b32 v179, s35, v160\n"
        "v_lshlrev_b32 v180, 16, v161\n"
        "v_mfma_f32_16x16x32_bf16 a[56:59], v[8:11], v[56:59], a[56:59]\n"
        "v_and_b32 v181, s35, v161\n"
        "v_lshlrev_b32 v182, 16, v162\n"
        "v_mfma_f32_16x16x32_bf16 a[60:63], v[8:11], v[60:63], a[60:63]\n"
        "ds_read_b128 v[8:11], v224 offset:2048\n"
        "v_and_b32 v183, s35, v162\n"
        "v_mfma_f32_16x16x32_bf16 a[60:63], v[12:15], v[92:95], a[60:63]\n"
        "ds_read_b128 v[12:15], v225 offset:2048\n"
        "v_lshlrev_b32 v184, 16, v163\n"
        "v_mfma_f32_16x16x32_bf16 a[28:31], v[4:7], v[92:95], a[28:31]\n"
        "ds_read_b128 v[4:7], v225 offset:0\n"
        "v_and_b32 v185, s35, v163\n"
        "v_mfma_f32_16x16x32_bf16 a[28:31], v[0:3], v[60:63], a[28:31]\n"
        "global_load_lds_dwordx4 v238, s[68:69] offset:0\n"
        "ds_read_b128 v[0:3], v224 offset:0\n"
        "v_mfma_f32_16x16x32_bf16 a[64:67], v[32:35], v[16:19], a[64:67]\n"
        "v_lshlrev_b32 v186, 16, v164\n"
        "v_and_b32 v187, s35, v164\n"
        "v_mfma_f32_16x16x32_bf16 a[64:67], v[64:67], v[20:23], a[64:67]\n"
        "v_lshlrev_b32 v188, 16, v165\n"
        "v_and_b32 v189, s35, v165\n"
        "v_mfma_f32_16x16x32_bf16 a[96:99], v[64:67], v[28:31], a[96:99]\n"
        "v_lshlrev_b32 v192, 16, v142\n"
        "v_and_b32 v193, s35, v142\n"
        "v_mfma_f32_16x16x32_bf16 a[96:99], v[32:35], v[24:27], a[96:99]\n"
        "v_mul_f32 v192, v192, v174\n"
        "v_mul_f32 v193, v193, v175\n"
        "v_mfma_f32_16x16x32_bf16 a[100:103], v[36:39], v[24:27], a[100:103]\n"
        "v_cvt_pk_bf16_f32 v192, 0, v192\n"
        "v_cvt_pk_bf16_f32 v193, 0, v193\n"
        "v_mfma_f32_16x16x32_bf16 a[100:103], v[68:71], v[28:31], a[100:103]\n"
        "global_load_lds_dwordx4 v239, s[68:69] offset:1024\n"
        "v_add_f32 v190, v192, v193\n"
        "v_mfma_f32_16x16x32_bf16 a[68:71], v[68:71], v[20:23], a[68:71]\n"
        "v_lshlrev_b32 v194, 16, v143\n"
        "v_and_b32 v195, s35, v143\n"
        "v_mfma_f32_16x16x32_bf16 a[68:71], v[36:39], v[16:19], a[68:71]\n"
        "v_mul_f32 v194, v194, v176\n"
        "v_mul_f32 v195, v195, v177\n"
        "v_mfma_f32_16x16x32_bf16 a[72:75], v[40:43], v[16:19], a[72:75]\n"
        "v_cvt_pk_bf16_f32 v194, 0, v194\n"
        "v_cvt_pk_bf16_f32 v195, 0, v195\n"
        "v_mfma_f32_16x16x32_bf16 a[72:75], v[72:75], v[20:23], a[72:75]\n"
        "v_add_f32 v194, v194, v195\n"
        "v_add_f32 v190, v190, v194\n"
        "v_mfma_f32_16x16x32_bf16 a[104:107], v[72:75], v[28:31], a[104:107]\n"
        "v_lshlrev_b32 v192, 16, v144\n"
        "v_and_b32 v193, s35, v144\n"
        "v_mfma_f32_16x16x32_bf16 a[104:107], v[40:43], v[24:27], a[104:107]\n"
        "global_load_lds_dwordx4 v240, s[68:69] offset:2048\n"
        "v_mul_f32 v192, v192, v178\n"
        "v_mfma_f32_16x16x32_bf16 a[108:111], v[44:47], v[24:27], a[108:111]\n"
        "v_mul_f32 v193, v193, v179\n"
        "v_cvt_pk_bf16_f32 v192, 0, v192\n"
        "v_mfma_f32_16x16x32_bf16 a[108:111], v[76:79], v[28:31], a[108:111]\n"
        "v_cvt_pk_bf16_f32 v193, 0, v193\n"
        "v_add_f32 v192, v192, v193\n"
        "v_mfma_f32_16x16x32_bf16 a[76:79], v[76:79], v[20:23], a[76:79]\n"
        "v_add_f32 v190, v190, v192\n"
        "v_lshlrev_b32 v194, 16, v145\n"
        "v_mfma_f32_16x16x32_bf16 a[76:79], v[44:47], v[16:19], a[76:79]\n"
        "v_and_b32 v195, s35, v145\n"
        "v_mul_f32 v194, v194, v180\n"
        "v_mfma_f32_16x16x32_bf16 a[80:83], v[16:19], v[48:51], a[80:83]\n"
        "v_mul_f32 v195, v195, v181\n"
        "v_cvt_pk_bf16_f32 v194, 0, v194\n"
        "v_mfma_f32_16x16x32_bf16 a[80:83], v[20:23], v[80:83], a[80:83]\n"
        "global_load_lds_dwordx4 v241, s[68:69] offset:3072\n"
        "s_add_u32 m0, s73, 0x24000\n"
        "v_cvt_pk_bf16_f32 v195, 0, v195\n"
        "v_mfma_f32_16x16x32_bf16 a[112:115], v[28:31], v[80:83], a[112:115]\n"
        "v_add_f32 v194, v194, v195\n"
        "v_add_f32 v190, v190, v194\n"
        "v_mfma_f32_16x16x32_bf16 a[112:115], v[24:27], v[48:51], a[112:115]\n"
        "v_lshlrev_b32 v192, 16, v146\n"
        "v_and_b32 v193, s35, v146\n"
        "v_mfma_f32_16x16x32_bf16 a[116:119], v[24:27], v[52:55], a[116:119]\n"
        "v_mul_f32 v192, v192, v182\n"
        "v_mul_f32 v193, v193, v183\n"
        "v_mfma_f32_16x16x32_bf16 a[116:119], v[28:31], v[84:87], a[116:119]\n"
        "LHA3_%=:\n"
        "s_cbranch_execz LHA7_%=\n"
        "v_cvt_pk_bf16_f32 v192, 0, v192\n"
        "v_cvt_pk_bf16_f32 v193, 0, v193\n"
        "v_mfma_f32_16x16x32_bf16 a[84:87], v[20:23], v[84:87], a[84:87]\n"
        "v_add_f32 v192, v192, v193\n"
        "v_add_f32 v190, v190, v192\n"
        "v_mfma_f32_16x16x32_bf16 a[84:87], v[16:19], v[52:55], a[84:87]\n"
        "global_load_lds_dwordx4 v238, s[86:87] offset:0\n"
        "s_mov_b32 s88, 0x3e0293ee\n"
        "v_lshlrev_b32 v194, 16, v147\n"
        "v_mfma_f32_16x16x32_bf16 a[88:91], v[16:19], v[56:59], a[88:91]\n"
        "s_sub_u32 s75, s45, s44\n"
        "v_and_b32 v195, s35, v147\n"
        "v_mul_f32 v194, v194, v184\n"
        "v_mfma_f32_16x16x32_bf16 a[88:91], v[20:23], v[88:91], a[88:91]\n"
        "s_lshl_b32 s75, s75, 15\n"
        "v_mul_f32 v195, v195, v185\n"
        "v_cvt_pk_bf16_f32 v194, 0, v194\n"
        "v_mfma_f32_16x16x32_bf16 a[120:123], v[28:31], v[88:91], a[120:123]\n"
        "s_lshr_b32 s50, s40, 1\n"
        "v_cvt_pk_bf16_f32 v195, 0, v195\n"
        "v_add_f32 v194, v194, v195\n"
        "v_mfma_f32_16x16x32_bf16 a[120:123], v[24:27], v[56:59], a[120:123]\n"
        "s_lshl1_add_u32 s50, s44, s50\n"
        "v_add_f32 v190, v190, v194\n"
        "v_lshlrev_b32 v192, 16, v148\n"
        "v_mfma_f32_16x16x32_bf16 a[124:127], v[24:27], v[60:63], a[124:127]\n"
        "ds_read_b128 v[24:27], v224 offset:6144\n"
        "s_lshl_b32 s50, s50, 14\n"
        "v_and_b32 v193, s35, v148\n"
        "v_mfma_f32_16x16x32_bf16 a[124:127], v[28:31], v[92:95], a[124:127]\n"
        "global_load_lds_dwordx4 v239, s[86:87] offset:1024\n"
        "ds_read_b128 v[28:31], v225 offset:6144\n"
        "s_mul_i32 s51, s43, 529408\n"
        "v_mfma_f32_16x16x32_bf16 a[92:95], v[20:23], v[92:95], a[92:95]\n"
        "ds_read_b128 v[20:23], v225 offset:4096\n"
        "s_add_u32 s50, s50, s51\n"
        "v_mul_f32 v192, v192, v186\n"
        "v_mfma_f32_16x16x32_bf16 a[92:95], v[16:19], v[60:63], a[92:95]\n"
        "ds_read_b128 v[16:19], v224 offset:4096\n"
        "s_and_b32 s51, s40, 1\n"
        "s_waitcnt lgkmcnt(4)\n"
        "v_mfma_f32_16x16x32_bf16 a[128:131], v[32:35], v[0:3], a[128:131]\n"
        "s_lshl_b32 s51, s51, 11\n"
        "v_mul_f32 v193, v193, v187\n"
        "v_cvt_pk_bf16_f32 v192, 0, v192\n"
        "v_mfma_f32_16x16x32_bf16 a[128:131], v[64:67], v[4:7], a[128:131]\n"
        "s_add_u32 s52, s50, s51\n"
        "v_cvt_pk_bf16_f32 v193, 0, v193\n"
        "v_add_f32 v192, v192, v193\n"
        "v_mfma_f32_16x16x32_bf16 a[160:163], v[64:67], v[12:15], a[160:163]\n"
        "v_add_f32 v190, v190, v192\n"
        "v_lshlrev_b32 v194, 16, v149\n"
        "v_mfma_f32_16x16x32_bf16 a[160:163], v[32:35], v[8:11], a[160:163]\n"
        "global_load_lds_dwordx4 v240, s[86:87] offset:2048\n"
        "v_and_b32 v195, s35, v149\n"
        "v_mfma_f32_16x16x32_bf16 a[164:167], v[36:39], v[8:11], a[164:167]\n"
        "v_mul_f32 v194, v194, v188\n"
        "v_mul_f32 v195, v195, v189\n"
        "v_mfma_f32_16x16x32_bf16 a[164:167], v[68:71], v[12:15], a[164:167]\n"
        "v_cvt_pk_bf16_f32 v194, 0, v194\n"
        "v_cvt_pk_bf16_f32 v195, 0, v195\n"
        "v_mfma_f32_16x16x32_bf16 a[132:135], v[68:71], v[4:7], a[132:135]\n"
        "v_add_f32 v194, v194, v195\n"
        "v_add_f32 v190, v190, v194\n"
        "v_mfma_f32_16x16x32_bf16 a[132:135], v[36:39], v[0:3], a[132:135]\n"
        "v_lshlrev_b32 v192, 16, v150\n"
        "v_and_b32 v193, s35, v150\n"
        "v_mfma_f32_16x16x32_bf16 a[136:139], v[40:43], v[0:3], a[136:139]\n"
        "v_mul_f32 v192, v192, v174\n"
        "v_mul_f32 v193, v193, v175\n"
        "v_mfma_f32_16x16x32_bf16 a[136:139], v[72:75], v[4:7], a[136:139]\n"
        "global_load_lds_dwordx4 v241, s[86:87] offset:3072\n"
        "s_add_u32 m0, s72, 0x0\n"
        "v_cvt_pk_bf16_f32 v192, 0, v192\n"
        "v_mfma_f32_16x16x32_bf16 a[168:171], v[72:75], v[12:15], a[168:171]\n"
        "s_add_u32 s86, s64, 0x80\n"
        "s_addc_u32 s87, s65, 0\n"
        "v_cvt_pk_bf16_f32 v193, 0, v193\n"
        "v_mfma_f32_16x16x32_bf16 a[168:171], v[40:43], v[8:11], a[168:171]\n"
        "v_add_f32 v191, v192, v193\n"
        "v_lshlrev_b32 v194, 16, v151\n"
        "v_mfma_f32_16x16x32_bf16 a[172:175], v[44:47], v[8:11], a[172:175]\n"
        "v_and_b32 v195, s35, v151\n"
        "v_mul_f32 v194, v194, v176\n"
        "v_mfma_f32_16x16x32_bf16 a[172:175], v[76:79], v[12:15], a[172:175]\n"
        "v_mul_f32 v195, v195, v177\n"
        "v_cvt_pk_bf16_f32 v194, 0, v194\n"
        "v_mfma_f32_16x16x32_bf16 a[140:143], v[76:79], v[4:7], a[140:143]\n"
        "v_cvt_pk_bf16_f32 v195, 0, v195\n"
        "v_add_f32 v194, v194, v195\n"
        "v_mfma_f32_16x16x32_bf16 a[140:143], v[44:47], v[0:3], a[140:143]\n"
        "v_add_f32 v191, v191, v194\n"
        "v_lshlrev_b32 v192, 16, v152\n"
        "v_mfma_f32_16x16x32_bf16 a[144:147], v[0:3], v[48:51], a[144:147]\n"
        "v_and_b32 v193, s35, v152\n"
        "v_mul_f32 v192, v192, v178\n"
        "v_mfma_f32_16x16x32_bf16 a[144:147], v[4:7], v[80:83], a[144:147]\n"
        "v_mul_f32 v193, v193, v179\n"
        "v_cvt_pk_bf16_f32 v192, 0, v192\n"
        "v_mfma_f32_16x16x32_bf16 a[176:179], v[12:15], v[80:83], a[176:179]\n"
        "v_cvt_pk_bf16_f32 v193, 0, v193\n"
        "v_add_f32 v192, v192, v193\n"
        "v_mfma_f32_16x16x32_bf16 a[176:179], v[8:11], v[48:51], a[176:179]\n"
        "s_waitcnt vmcnt(28) lgkmcnt(0)\n"
        "v_add_f32 v191, v191, v192\n"
        "v_lshlrev_b32 v194, 16, v153\n"
        "v_mfma_f32_16x16x32_bf16 a[180:183], v[8:11], v[52:55], a[180:183]\n"
        "v_and_b32 v195, s35, v153\n"
        "v_mul_f32 v194, v194, v180\n"
        "v_mfma_f32_16x16x32_bf16 a[180:183], v[12:15], v[84:87], a[180:183]\n"
        "v_mul_f32 v195, v195, v181\n"
        "v_cvt_pk_bf16_f32 v194, 0, v194\n"
        "v_mfma_f32_16x16x32_bf16 a[148:151], v[4:7], v[84:87], a[148:151]\n"
        "v_cvt_pk_bf16_f32 v195, 0, v195\n"
        "v_add_f32 v194, v194, v195\n"
        "v_mfma_f32_16x16x32_bf16 a[148:151], v[0:3], v[52:55], a[148:151]\n"
        "v_add_f32 v191, v191, v194\n"
        "v_lshlrev_b32 v192, 16, v154\n"
        "v_mfma_f32_16x16x32_bf16 a[152:155], v[0:3], v[56:59], a[152:155]\n"
        "v_and_b32 v193, s35, v154\n"
        "v_mul_f32 v192, v192, v182\n"
        "v_mfma_f32_16x16x32_bf16 a[152:155], v[4:7], v[88:91], a[152:155]\n"
        "v_mul_f32 v193, v193, v183\n"
        "v_cvt_pk_bf16_f32 v192, 0, v192\n"
        "v_mfma_f32_16x16x32_bf16 a[184:187], v[12:15], v[88:91], a[184:187]\n"
        "v_cvt_pk_bf16_f32 v193, 0, v193\n"
        "v_add_f32 v192, v192, v193\n"
        "v_mfma_f32_16x16x32_bf16 a[184:187], v[8:11], v[56:59], a[184:187]\n"
        "s_waitcnt vmcnt(10)\n"
        "v_add_f32 v191, v191, v192\n"
        "v_lshlrev_b32 v194, 16, v155\n"
        "v_mfma_f32_16x16x32_bf16 a[188:191], v[8:11], v[60:63], a[188:191]\n"
        "s_barrier\n"
        "v_mfma_f32_16x16x32_bf16 a[188:191], v[12:15], v[92:95], a[188:191]\n"
        "global_load_lds_dwordx4 v242, s[64:65] offset:0\n"
        "ds_read_b128 v[8:11], v224 offset:51200\n"
        "v_mfma_f32_16x16x32_bf16 a[156:159], v[4:7], v[92:95], a[156:159]\n"
        "ds_read_b128 v[4:7], v225 offset:49152\n"
        "v_and_b32 v195, s35, v155\n"
        "v_mfma_f32_16x16x32_bf16 a[156:159], v[0:3], v[60:63], a[156:159]\n"
        "ds_read_b128 v[0:3], v224 offset:49152\n"
        "v_mul_f32 v194, v194, v184\n"
        "v_mfma_f32_16x16x32_bf16 a[192:195], v[32:35], v[16:19], a[192:195]\n"
        "ds_read_b128 v[12:15], v225 offset:51200\n"
        "v_mul_f32 v195, v195, v185\n"
        "v_mfma_f32_16x16x32_bf16 a[192:195], v[64:67], v[20:23], a[192:195]\n"
        "global_load_lds_dwordx4 v243, s[64:65] offset:1024\n"
        "v_cvt_pk_bf16_f32 v194, 0, v194\n"
        "v_mfma_f32_16x16x32_bf16 a[224:227], v[64:67], v[28:31], a[224:227]\n"
        "ds_read_b64_tr_b16 v[64:65], v226 offset:40960\n"
        "v_cvt_pk_bf16_f32 v195, 0, v195\n"
        "v_mfma_f32_16x16x32_bf16 a[224:227], v[32:35], v[24:27], a[224:227]\n"
        "ds_read_b64_tr_b16 v[32:33], v226 offset:32768\n"
        "s_add_u32 s96, s52, 0x3000\n"
        "v_add_f32 v194, v194, v195\n"
        "v_mfma_f32_16x16x32_bf16 a[228:231], v[36:39], v[24:27], a[228:231]\n"
        "ds_read_b64_tr_b16 v[34:35], v227 offset:32768\n"
        "v_add_f32 v191, v191, v194\n"
        "v_mfma_f32_16x16x32_bf16 a[228:231], v[68:71], v[28:31], a[228:231]\n"
        "ds_read_b64_tr_b16 v[66:67], v227 offset:40960\n"
        "global_load_lds_dwordx4 v244, s[64:65] offset:2048\n"
        "v_mfma_f32_16x16x32_bf16 a[196:199], v[68:71], v[20:23], a[196:199]\n"
        "ds_read_b64_tr_b16 v[68:69], v228 offset:40960\n"
        "v_lshlrev_b32 v192, 16, v156\n"
        "v_mfma_f32_16x16x32_bf16 a[196:199], v[36:39], v[16:19], a[196:199]\n"
        "ds_read_b64_tr_b16 v[36:37], v228 offset:32768\n"
        "v_and_b32 v193, s35, v156\n"
        "v_mfma_f32_16x16x32_bf16 a[200:203], v[40:43], v[16:19], a[200:203]\n"
        "ds_read_b64_tr_b16 v[38:39], v229 offset:32768\n"
        "v_mul_f32 v192, v192, v186\n"
        "v_mfma_f32_16x16x32_bf16 a[200:203], v[72:75], v[20:23], a[200:203]\n"
        "ds_read_b64_tr_b16 v[70:71], v229 offset:40960\n"
        "global_load_lds_dwordx4 v245, s[64:65] offset:3072\n"
        "s_add_u32 m0, s72, 0x1000\n"
        "v_mfma_f32_16x16x32_bf16 a[232:235], v[72:75], v[28:31], a[232:235]\n"
        "ds_read_b64_tr_b16 v[72:73], v230 offset:40960\n"
        "v_mul_f32 v193, v193, v187\n"
        "v_mfma_f32_16x16x32_bf16 a[232:235], v[40:43], v[24:27], a[232:235]\n"
        "ds_read_b64_tr_b16 v[40:41], v230 offset:32768\n"
        "v_cvt_pk_bf16_f32 v192, 0, v192\n"
        "v_mfma_f32_16x16x32_bf16 a[236:239], v[44:47], v[24:27], a[236:239]\n"
        "ds_read_b64_tr_b16 v[42:43], v231 offset:32768\n"
        "v_cvt_pk_bf16_f32 v193, 0, v193\n"
        "v_mfma_f32_16x16x32_bf16 a[236:239], v[76:79], v[28:31], a[236:239]\n"
        "ds_read_b64_tr_b16 v[74:75], v231 offset:40960\n"
        "global_load_lds_dwordx4 v242, s[66:67] offset:0\n"
        "v_mfma_f32_16x16x32_bf16 a[204:207], v[76:79], v[20:23], a[204:207]\n"
        "ds_read_b64_tr_b16 v[76:77], v232 offset:40960\n"
        "v_add_f32 v192, v192, v193\n"
        "v_mfma_f32_16x16x32_bf16 a[204:207], v[44:47], v[16:19], a[204:207]\n"
        "ds_read_b64_tr_b16 v[44:45], v232 offset:32768\n"
        "v_add_f32 v191, v191, v192\n"
        "v_mfma_f32_16x16x32_bf16 a[208:211], v[16:19], v[48:51], a[208:211]\n"
        "ds_read_b64_tr_b16 v[46:47], v233 offset:32768\n"
        "v_lshlrev_b32 v194, 16, v157\n"
        "v_mfma_f32_16x16x32_bf16 a[208:211], v[20:23], v[80:83], a[208:211]\n"
        "ds_read_b64_tr_b16 v[78:79], v233 offset:40960\n"
        "global_load_lds_dwordx4 v243, s[66:67] offset:1024\n"
        "v_mfma_f32_16x16x32_bf16 a[240:243], v[28:31], v[80:83], a[240:243]\n"
        "ds_read_b64_tr_b16 v[80:81], v226 offset:57344\n"
        "v_and_b32 v195, s35, v157\n"
        "v_mfma_f32_16x16x32_bf16 a[240:243], v[24:27], v[48:51], a[240:243]\n"
        "ds_read_b64_tr_b16 v[48:49], v226 offset:49152\n"
        "v_mul_f32 v194, v194, v188\n"
        "v_mfma_f32_16x16x32_bf16 a[244:247], v[24:27], v[52:55], a[244:247]\n"
        "ds_read_b64_tr_b16 v[50:51], v227 offset:49152\n"
        "v_mul_f32 v195, v195, v189\n"
        "v_mfma_f32_16x16x32_bf16 a[244:247], v[28:31], v[84:87], a[244:247]\n"
        "ds_read_b64_tr_b16 v[82:83], v227 offset:57344\n"
        "global_load_lds_dwordx4 v244, s[66:67] offset:2048\n"
        "v_mfma_f32_16x16x32_bf16 a[212:215], v[20:23], v[84:87], a[212:215]\n"
        "ds_read_b64_tr_b16 v[84:85], v228 offset:57344\n"
        "v_cvt_pk_bf16_f32 v194, 0, v194\n"
        "v_mfma_f32_16x16x32_bf16 a[212:215], v[16:19], v[52:55], a[212:215]\n"
        "ds_read_b64_tr_b16 v[52:53], v228 offset:49152\n"
        "v_cvt_pk_bf16_f32 v195, 0, v195\n"
        "v_mfma_f32_16x16x32_bf16 a[216:219], v[16:19], v[56:59], a[216:219]\n"
        "ds_read_b64_tr_b16 v[54:55], v229 offset:49152\n"
        "v_add_f32 v194, v194, v195\n"
        "v_mfma_f32_16x16x32_bf16 a[216:219], v[20:23], v[88:91], a[216:219]\n"
        "ds_read_b64_tr_b16 v[86:87], v229 offset:57344\n"
        "global_load_lds_dwordx4 v245, s[66:67] offset:3072\n"
        "s_add_u32 m0, s72, 0x10000\n"
        "v_mfma_f32_16x16x32_bf16 a[248:251], v[28:31], v[88:91], a[248:251]\n"
        "ds_read_b64_tr_b16 v[88:89], v230 offset:57344\n"
        "v_add_f32 v191, v191, v194\n"
        "v_mfma_f32_16x16x32_bf16 a[248:251], v[24:27], v[56:59], a[248:251]\n"
        "ds_read_b64_tr_b16 v[56:57], v230 offset:49152\n"
        "v_add_f32_dpp v190, v190, v190 row_shr:1 row_mask:0xf bank_mask:0xf bound_ctrl:1\n"
        "v_mfma_f32_16x16x32_bf16 a[252:255], v[24:27], v[60:63], a[252:255]\n"
        "ds_read_b64_tr_b16 v[58:59], v231 offset:49152\n"
        "v_add_f32_dpp v191, v191, v191 row_shr:1 row_mask:0xf bank_mask:0xf bound_ctrl:1\n"
        "v_mfma_f32_16x16x32_bf16 a[252:255], v[28:31], v[92:95], a[252:255]\n"
        "ds_read_b64_tr_b16 v[90:91], v231 offset:57344\n"
        "global_load_lds_dwordx4 v242, s[86:87] offset:0\n"
        "v_mfma_f32_16x16x32_bf16 a[220:223], v[20:23], v[92:95], a[220:223]\n"
        "ds_read_b64_tr_b16 v[92:93], v232 offset:57344\n"
        "v_add_f32_dpp v190, v190, v190 row_shr:2 row_mask:0xf bank_mask:0xf bound_ctrl:1\n"
        "v_mfma_f32_16x16x32_bf16 a[220:223], v[16:19], v[60:63], a[220:223]\n"
        "ds_read_b64_tr_b16 v[60:61], v232 offset:49152\n"
        "s_waitcnt lgkmcnt(8)\n"
        "v_mfma_f32_16x16x32_bf16 a[0:3], v[32:35], v[0:3], a[0:3]\n"
        "ds_read_b64_tr_b16 v[62:63], v233 offset:49152\n"
        "v_add_f32_dpp v191, v191, v191 row_shr:2 row_mask:0xf bank_mask:0xf bound_ctrl:1\n"
        "v_mfma_f32_16x16x32_bf16 a[0:3], v[64:67], v[4:7], a[0:3]\n"
        "ds_read_b64_tr_b16 v[94:95], v233 offset:57344\n"
        "global_load_lds_dwordx4 v243, s[86:87] offset:1024\n"
        "v_mfma_f32_16x16x32_bf16 a[32:35], v[64:67], v[12:15], a[32:35]\n"
        "ds_read_b128 v[16:19], v224 offset:53248\n"
        "v_add_f32_dpp v190, v190, v190 row_shr:4 row_mask:0xf bank_mask:0xf bound_ctrl:1\n"
        "v_mfma_f32_16x16x32_bf16 a[32:35], v[32:35], v[8:11], a[32:35]\n"
        "ds_read_b128 v[20:23], v225 offset:53248\n"
        "v_add_f32_dpp v191, v191, v191 row_shr:4 row_mask:0xf bank_mask:0xf bound_ctrl:1\n"
        "v_mfma_f32_16x16x32_bf16 a[36:39], v[36:39], v[8:11], a[36:39]\n"
        "ds_read_b128 v[24:27], v224 offset:55296\n"
        "v_add_f32_dpp v190, v190, v190 row_shr:8 row_mask:0xf bank_mask:0xf bound_ctrl:1\n"
        "v_mfma_f32_16x16x32_bf16 a[36:39], v[68:71], v[12:15], a[36:39]\n"
        "ds_read_b128 v[28:31], v225 offset:55296\n"
        "global_load_lds_dwordx4 v244, s[86:87] offset:2048\n"
        "v_mfma_f32_16x16x32_bf16 a[4:7], v[68:71], v[4:7], a[4:7]\n"
        "s_add_u32 s70, s38, s96\n"
        "s_addc_u32 s71, s39, 0\n"
        "v_add_f32_dpp v191, v191, v191 row_shr:8 row_mask:0xf bank_mask:0xf bound_ctrl:1\n"
        "v_mfma_f32_16x16x32_bf16 a[4:7], v[36:39], v[0:3], a[4:7]\n"
        "v_add_f32_dpp v190, v190, v190 row_bcast:15 row_mask:0xa bank_mask:0xf bound_ctrl:1\n"
        "v_add_f32_dpp v191, v191, v191 row_bcast:15 row_mask:0xa bank_mask:0xf bound_ctrl:1\n"
        "v_mfma_f32_16x16x32_bf16 a[8:11], v[40:43], v[0:3], a[8:11]\n"
        "v_add_f32_dpp v190, v190, v190 row_bcast:31 row_mask:0xc bank_mask:0xf bound_ctrl:1\n"
        "v_add_f32_dpp v191, v191, v191 row_bcast:31 row_mask:0xc bank_mask:0xf bound_ctrl:1\n"
        "v_mfma_f32_16x16x32_bf16 a[8:11], v[72:75], v[4:7], a[8:11]\n"
        "global_load_lds_dwordx4 v245, s[86:87] offset:3072\n"
        "s_add_u32 m0, s72, 0x11000\n"
        "v_mfma_f32_16x16x32_bf16 a[40:43], v[72:75], v[12:15], a[40:43]\n"
        "s_add_u32 s86, s66, 0x80\n"
        "s_addc_u32 s87, s67, 0\n"
        "v_cvt_pk_bf16_f32 v190, 0, v190\n"
        "v_mfma_f32_16x16x32_bf16 a[40:43], v[40:43], v[8:11], a[40:43]\n"
        "v_mul_f32 v190, 0xbfb8aa3b, v190\n"
        "v_lshrrev_b32 v192, 2, v254\n"
        "v_mfma_f32_16x16x32_bf16 a[44:47], v[44:47], v[8:11], a[44:47]\n"
        "v_exp_f32 v190, v190\n"
        "v_mfma_f32_16x16x32_bf16 a[44:47], v[76:79], v[12:15], a[44:47]\n"
        "global_load_lds_dwordx4 v242, s[86:87] offset:0\n"
        "v_mfma_f32_16x16x32_bf16 a[12:15], v[76:79], v[4:7], a[12:15]\n"
        "v_add_f32 v190, 1.0, v190\n"
        "v_add_u32 v192, s80, v192\n"
        "v_mfma_f32_16x16x32_bf16 a[12:15], v[44:47], v[0:3], a[12:15]\n"
        "v_rcp_f32 v190, v190\n"
        "v_mfma_f32_16x16x32_bf16 a[16:19], v[0:3], v[48:51], a[16:19]\n"
        "s_waitcnt lgkmcnt(0)\n"
        "v_cvt_pk_bf16_f32 v190, 0, v190\n"
        "v_cvt_pk_bf16_f32 v191, 0, v191\n"
        "v_mfma_f32_16x16x32_bf16 a[16:19], v[4:7], v[80:83], a[16:19]\n"
        "global_load_dword v253, v173, s[82:83] offset:0\n"
        "v_mfma_f32_16x16x32_bf16 a[48:51], v[12:15], v[80:83], a[48:51]\n"
        "global_load_lds_dwordx4 v243, s[86:87] offset:1024\n"
        "v_readlane_b32 s84, v190, 63\n"
        "v_mfma_f32_16x16x32_bf16 a[48:51], v[8:11], v[48:51], a[48:51]\n"
        "v_mul_f32 v191, 0xbfb8aa3b, v191\n"
        "v_mov_b32 v190, s84\n"
        "v_mfma_f32_16x16x32_bf16 a[52:55], v[8:11], v[52:55], a[52:55]\n"
        "v_exp_f32 v191, v191\n"
        "v_mfma_f32_16x16x32_bf16 a[52:55], v[12:15], v[84:87], a[52:55]\n"
        "v_add_f32 v191, 1.0, v191\n"
        "v_lshrrev_b32 v193, 2, v254\n"
        "v_mfma_f32_16x16x32_bf16 a[20:23], v[4:7], v[84:87], a[20:23]\n"
        "global_load_lds_dwordx4 v244, s[86:87] offset:2048\n"
        "v_add_u32 v193, s81, v193\n"
        "v_mfma_f32_16x16x32_bf16 a[20:23], v[0:3], v[52:55], a[20:23]\n"
        "v_rcp_f32 v191, v191\n"
        "v_mfma_f32_16x16x32_bf16 a[24:27], v[0:3], v[56:59], a[24:27]\n"
        "v_cvt_pk_bf16_f32 v191, 0, v191\n"
        "v_add_u32 v226, s53, v226\n"
        "v_mfma_f32_16x16x32_bf16 a[24:27], v[4:7], v[88:91], a[24:27]\n"
        "v_readlane_b32 s85, v191, 63\n"
        "v_add_u32 v227, s53, v227\n"
        "v_mfma_f32_16x16x32_bf16 a[56:59], v[12:15], v[88:91], a[56:59]\n"
        "global_load_lds_dwordx4 v245, s[86:87] offset:3072\n"
        "s_add_u32 m0, s73, 0x1c000\n"
        "v_mov_b32 v191, s85\n"
        "v_mfma_f32_16x16x32_bf16 a[56:59], v[8:11], v[56:59], a[56:59]\n"
        "s_add_u32 s86, s68, 0x100000\n"
        "s_addc_u32 s87, s69, 0\n"
        "v_add_u32 v228, s53, v228\n"
        "v_mfma_f32_16x16x32_bf16 a[60:63], v[8:11], v[60:63], a[60:63]\n"
        "ds_read_b128 v[8:11], v224 offset:34816\n"
        "v_add_u32 v229, s53, v229\n"
        "v_mfma_f32_16x16x32_bf16 a[60:63], v[12:15], v[92:95], a[60:63]\n"
        "ds_read_b128 v[12:15], v225 offset:34816\n"
        "v_add_u32 v230, s53, v230\n"
        "v_mfma_f32_16x16x32_bf16 a[28:31], v[4:7], v[92:95], a[28:31]\n"
        "ds_read_b128 v[4:7], v225 offset:32768\n"
        "v_add_u32 v231, s53, v231\n"
        "v_mfma_f32_16x16x32_bf16 a[28:31], v[0:3], v[60:63], a[28:31]\n"
        "ds_read_b128 v[0:3], v224 offset:32768\n"
        "v_add_u32 v232, s53, v232\n"
        "v_mfma_f32_16x16x32_bf16 a[64:67], v[32:35], v[16:19], a[64:67]\n"
        "s_mov_b64 exec, 0xffff\n"
        "global_store_dword v192, v190, s[76:77] sc1\n"
        "s_mov_b64 exec, -1\n"
        "v_mfma_f32_16x16x32_bf16 a[64:67], v[64:67], v[20:23], a[64:67]\n"
        "s_mov_b64 exec, 0xffff\n"
        "global_store_dword v193, v191, s[76:77] sc1\n"
        "s_mov_b64 exec, -1\n"
        "v_mfma_f32_16x16x32_bf16 a[96:99], v[64:67], v[28:31], a[96:99]\n"
        "v_add_u32 v233, s53, v233\n"
        "s_add_u32 s53, s52, s51\n"
        "v_mfma_f32_16x16x32_bf16 a[96:99], v[32:35], v[24:27], a[96:99]\n"
        "s_add_u32 s53, s53, 0x1100000\n"
        "v_mfma_f32_16x16x32_bf16 a[100:103], v[36:39], v[24:27], a[100:103]\n"
        "v_mfma_f32_16x16x32_bf16 a[100:103], v[68:71], v[28:31], a[100:103]\n"
        "v_mfma_f32_16x16x32_bf16 a[68:71], v[68:71], v[20:23], a[68:71]\n"
        "v_mfma_f32_16x16x32_bf16 a[68:71], v[36:39], v[16:19], a[68:71]\n"
        "v_mfma_f32_16x16x32_bf16 a[72:75], v[40:43], v[16:19], a[72:75]\n"
        "v_mfma_f32_16x16x32_bf16 a[72:75], v[72:75], v[20:23], a[72:75]\n"
        "v_mfma_f32_16x16x32_bf16 a[104:107], v[72:75], v[28:31], a[104:107]\n"
        "v_mfma_f32_16x16x32_bf16 a[104:107], v[40:43], v[24:27], a[104:107]\n"
        "v_mfma_f32_16x16x32_bf16 a[108:111], v[44:47], v[24:27], a[108:111]\n"
        "v_mfma_f32_16x16x32_bf16 a[108:111], v[76:79], v[28:31], a[108:111]\n"
        "v_mfma_f32_16x16x32_bf16 a[76:79], v[76:79], v[20:23], a[76:79]\n"
        "v_mfma_f32_16x16x32_bf16 a[76:79], v[44:47], v[16:19], a[76:79]\n"
        "v_mfma_f32_16x16x32_bf16 a[80:83], v[16:19], v[48:51], a[80:83]\n"
        "v_mfma_f32_16x16x32_bf16 a[80:83], v[20:23], v[80:83], a[80:83]\n"
        "v_mfma_f32_16x16x32_bf16 a[112:115], v[28:31], v[80:83], a[112:115]\n"
        "v_mfma_f32_16x16x32_bf16 a[112:115], v[24:27], v[48:51], a[112:115]\n"
        "v_mfma_f32_16x16x32_bf16 a[116:119], v[24:27], v[52:55], a[116:119]\n"
        "v_mfma_f32_16x16x32_bf16 a[116:119], v[28:31], v[84:87], a[116:119]\n"
        "v_mfma_f32_16x16x32_bf16 a[84:87], v[20:23], v[84:87], a[84:87]\n"
        "v_mfma_f32_16x16x32_bf16 a[84:87], v[16:19], v[52:55], a[84:87]\n"
        "v_mfma_f32_16x16x32_bf16 a[88:91], v[16:19], v[56:59], a[88:91]\n"
        "v_mfma_f32_16x16x32_bf16 a[88:91], v[20:23], v[88:91], a[88:91]\n"
        "v_mfma_f32_16x16x32_bf16 a[120:123], v[28:31], v[88:91], a[120:123]\n"
        "v_mfma_f32_16x16x32_bf16 a[120:123], v[24:27], v[56:59], a[120:123]\n"
        "v_mfma_f32_16x16x32_bf16 a[124:127], v[24:27], v[60:63], a[124:127]\n"
        "ds_read_b128 v[24:27], v224 offset:38912\n"
        "s_waitcnt lgkmcnt(1)\n"
        "v_mfma_f32_16x16x32_bf16 a[124:127], v[28:31], v[92:95], a[124:127]\n"
        "ds_read_b128 v[28:31], v225 offset:38912\n"
        "v_mfma_f32_16x16x32_bf16 a[92:95], v[20:23], v[92:95], a[92:95]\n"
        "ds_read_b128 v[20:23], v225 offset:36864\n"
        "v_mfma_f32_16x16x32_bf16 a[92:95], v[16:19], v[60:63], a[92:95]\n"
        "ds_read_b128 v[16:19], v224 offset:36864\n"
        "v_mfma_f32_16x16x32_bf16 a[128:131], v[32:35], v[0:3], a[128:131]\n"
        "v_mfma_f32_16x16x32_bf16 a[128:131], v[64:67], v[4:7], a[128:131]\n"
        "v_mfma_f32_16x16x32_bf16 a[160:163], v[64:67], v[12:15], a[160:163]\n"
        "v_mfma_f32_16x16x32_bf16 a[160:163], v[32:35], v[8:11], a[160:163]\n"
        "v_mfma_f32_16x16x32_bf16 a[164:167], v[36:39], v[8:11], a[164:167]\n"
        "v_mfma_f32_16x16x32_bf16 a[164:167], v[68:71], v[12:15], a[164:167]\n"
        "v_mfma_f32_16x16x32_bf16 a[132:135], v[68:71], v[4:7], a[132:135]\n"
        "v_mfma_f32_16x16x32_bf16 a[132:135], v[36:39], v[0:3], a[132:135]\n"
        "v_mfma_f32_16x16x32_bf16 a[136:139], v[40:43], v[0:3], a[136:139]\n"
        "v_mfma_f32_16x16x32_bf16 a[136:139], v[72:75], v[4:7], a[136:139]\n"
        "v_mfma_f32_16x16x32_bf16 a[168:171], v[72:75], v[12:15], a[168:171]\n"
        "v_mfma_f32_16x16x32_bf16 a[168:171], v[40:43], v[8:11], a[168:171]\n"
        "v_mfma_f32_16x16x32_bf16 a[172:175], v[44:47], v[8:11], a[172:175]\n"
        "v_mfma_f32_16x16x32_bf16 a[172:175], v[76:79], v[12:15], a[172:175]\n"
        "v_mfma_f32_16x16x32_bf16 a[140:143], v[76:79], v[4:7], a[140:143]\n"
        "v_mfma_f32_16x16x32_bf16 a[140:143], v[44:47], v[0:3], a[140:143]\n"
        "v_mfma_f32_16x16x32_bf16 a[144:147], v[0:3], v[48:51], a[144:147]\n"
        "v_mfma_f32_16x16x32_bf16 a[144:147], v[4:7], v[80:83], a[144:147]\n"
        "v_mfma_f32_16x16x32_bf16 a[176:179], v[12:15], v[80:83], a[176:179]\n"
        "v_mfma_f32_16x16x32_bf16 a[176:179], v[8:11], v[48:51], a[176:179]\n"
        "v_mfma_f32_16x16x32_bf16 a[180:183], v[8:11], v[52:55], a[180:183]\n"
        "v_mfma_f32_16x16x32_bf16 a[180:183], v[12:15], v[84:87], a[180:183]\n"
        "v_mfma_f32_16x16x32_bf16 a[148:151], v[4:7], v[84:87], a[148:151]\n"
        "v_mfma_f32_16x16x32_bf16 a[148:151], v[0:3], v[52:55], a[148:151]\n"
        "v_mfma_f32_16x16x32_bf16 a[152:155], v[0:3], v[56:59], a[152:155]\n"
        "v_mfma_f32_16x16x32_bf16 a[152:155], v[4:7], v[88:91], a[152:155]\n"
        "v_mfma_f32_16x16x32_bf16 a[184:187], v[12:15], v[88:91], a[184:187]\n"
        "v_mfma_f32_16x16x32_bf16 a[184:187], v[8:11], v[56:59], a[184:187]\n"
        "s_waitcnt vmcnt(11) lgkmcnt(0)\n"
        "v_mfma_f32_16x16x32_bf16 a[188:191], v[8:11], v[60:63], a[188:191]\n"
        "s_barrier\n"
        "v_mfma_f32_16x16x32_bf16 a[188:191], v[12:15], v[92:95], a[188:191]\n"
        "global_load_lds_dwordx4 v238, s[86:87] offset:0\n"
        "ds_read_b64_tr_b16 v[160:161], v226 offset:32768\n"
        "v_mfma_f32_16x16x32_bf16 a[156:159], v[4:7], v[92:95], a[156:159]\n"
        "ds_read_b64_tr_b16 v[162:163], v227 offset:32768\n"
        "v_mfma_f32_16x16x32_bf16 a[156:159], v[0:3], v[60:63], a[156:159]\n"
        "ds_read_b64_tr_b16 v[164:165], v226 offset:40960\n"
        "v_mfma_f32_16x16x32_bf16 a[192:195], v[32:35], v[16:19], a[192:195]\n"
        "ds_read_b64_tr_b16 v[166:167], v227 offset:40960\n"
        "v_mfma_f32_16x16x32_bf16 a[192:195], v[64:67], v[20:23], a[192:195]\n"
        "ds_read_b64_tr_b16 v[172:173], v228 offset:40960\n"
        "global_load_lds_dwordx4 v239, s[86:87] offset:1024\n"
        "v_mfma_f32_16x16x32_bf16 a[224:227], v[64:67], v[28:31], a[224:227]\n"
        "ds_read_b64_tr_b16 v[174:175], v229 offset:40960\n"
        "v_mfma_f32_16x16x32_bf16 a[224:227], v[32:35], v[24:27], a[224:227]\n"
        "ds_read_b64_tr_b16 v[168:169], v228 offset:32768\n"
        "v_mfma_f32_16x16x32_bf16 a[228:231], v[36:39], v[24:27], a[228:231]\n"
        "ds_read_b64_tr_b16 v[170:171], v229 offset:32768\n"
        "v_mfma_f32_16x16x32_bf16 a[228:231], v[68:71], v[28:31], a[228:231]\n"
        "ds_read_b128 v[192:195], v246 offset:2048\n"
        "global_load_lds_dwordx4 v240, s[86:87] offset:2048\n"
        "v_mfma_f32_16x16x32_bf16 a[196:199], v[68:71], v[20:23], a[196:199]\n"
        "ds_read_b128 v[208:211], v247 offset:2048\n"
        "v_accvgpr_read_b32 v177, a225\n"
        "v_mfma_f32_16x16x32_bf16 a[196:199], v[36:39], v[16:19], a[196:199]\n"
        "ds_read_b128 v[196:199], v246 offset:0\n"
        "v_accvgpr_read_b32 v178, a226\n"
        "v_mfma_f32_16x16x32_bf16 a[200:203], v[40:43], v[16:19], a[200:203]\n"
        "ds_read_b128 v[212:215], v247 offset:0\n"
        "v_accvgpr_read_b32 v179, a227\n"
        "v_mfma_f32_16x16x32_bf16 a[200:203], v[72:75], v[20:23], a[200:203]\n"
        "ds_read_b128 v[200:203], v246 offset:6144\n"
        "global_load_lds_dwordx4 v241, s[86:87] offset:3072\n"
        "s_add_u32 m0, s72, 0x8000\n"
        "v_mfma_f32_16x16x32_bf16 a[232:235], v[72:75], v[28:31], a[232:235]\n"
        "ds_read_b128 v[216:219], v247 offset:6144\n"
        "s_add_u32 s86, s64, 0x100\n"
        "s_addc_u32 s87, s65, 0\n"
        "v_mfma_f32_16x16x32_bf16 a[232:235], v[40:43], v[24:27], a[232:235]\n"
        "ds_read_b128 v[204:207], v246 offset:4096\n"
        "v_accvgpr_read_b32 v180, a228\n"
        "v_mfma_f32_16x16x32_bf16 a[236:239], v[44:47], v[24:27], a[236:239]\n"
        "ds_read_b128 v[220:223], v247 offset:4096\n"
        "v_accvgpr_read_b32 v181, a229\n"
        "v_mfma_f32_16x16x32_bf16 a[236:239], v[76:79], v[28:31], a[236:239]\n"
        "ds_read_b64_tr_b16 v[32:33], v230 offset:32768\n"
        "global_load_lds_dwordx4 v242, s[86:87] offset:0\n"
        "v_mfma_f32_16x16x32_bf16 a[204:207], v[76:79], v[20:23], a[204:207]\n"
        "ds_read_b64_tr_b16 v[34:35], v231 offset:32768\n"
        "v_accvgpr_read_b32 v182, a230\n"
        "v_mfma_f32_16x16x32_bf16 a[204:207], v[44:47], v[16:19], a[204:207]\n"
        "ds_read_b64_tr_b16 v[36:37], v230 offset:40960\n"
        "s_add_u32 s96, s52, 0x2000\n"
        "v_accvgpr_read_b32 v183, a231\n"
        "v_mfma_f32_16x16x32_bf16 a[208:211], v[16:19], v[48:51], a[208:211]\n"
        "ds_read_b64_tr_b16 v[38:39], v231 offset:40960\n"
        "v_mul_f32 v177, s88, v177\n"
        "v_mfma_f32_16x16x32_bf16 a[208:211], v[20:23], v[80:83], a[208:211]\n"
        "ds_read_b64_tr_b16 v[44:45], v232 offset:40960\n"
        "global_load_lds_dwordx4 v243, s[86:87] offset:1024\n"
        "v_mfma_f32_16x16x32_bf16 a[240:243], v[28:31], v[80:83], a[240:243]\n"
        "ds_read_b64_tr_b16 v[46:47], v233 offset:40960\n"
        "v_mul_f32 v178, s88, v178\n"
        "v_mfma_f32_16x16x32_bf16 a[240:243], v[24:27], v[48:51], a[240:243]\n"
        "ds_read_b64_tr_b16 v[40:41], v232 offset:32768\n"
        "v_mul_f32 v179, s88, v179\n"
        "v_mfma_f32_16x16x32_bf16 a[244:247], v[24:27], v[52:55], a[244:247]\n"
        "ds_read_b64_tr_b16 v[42:43], v233 offset:32768\n"
        "v_mul_f32 v180, s88, v180\n"
        "v_mfma_f32_16x16x32_bf16 a[244:247], v[28:31], v[84:87], a[244:247]\n"
        "global_load_lds_dwordx4 v244, s[86:87] offset:2048\n"
        "v_mfma_f32_16x16x32_bf16 a[212:215], v[20:23], v[84:87], a[212:215]\n"
        "ds_read_b64_tr_b16 v[48:49], v226 offset:33792\n"
        "v_mul_f32 v181, s88, v181\n"
        "v_mfma_f32_16x16x32_bf16 a[212:215], v[16:19], v[52:55], a[212:215]\n"
        "ds_read_b64_tr_b16 v[50:51], v227 offset:33792\n"
        "v_mul_f32 v182, s88, v182\n"
        "v_mfma_f32_16x16x32_bf16 a[216:219], v[16:19], v[56:59], a[216:219]\n"
        "ds_read_b64_tr_b16 v[52:53], v226 offset:41984\n"
        "v_mul_f32 v183, s88, v183\n"
        "v_mfma_f32_16x16x32_bf16 a[216:219], v[20:23], v[88:91], a[216:219]\n"
        "global_load_lds_dwordx4 v245, s[86:87] offset:3072\n"
        "s_add_u32 m0, s72, 0x9000\n"
        "v_mfma_f32_16x16x32_bf16 a[248:251], v[28:31], v[88:91], a[248:251]\n"
        "s_add_u32 s86, s66, 0x100\n"
        "s_addc_u32 s87, s67, 0\n"
        "v_accvgpr_read_b32 v185, a233\n"
        "v_mfma_f32_16x16x32_bf16 a[248:251], v[24:27], v[56:59], a[248:251]\n"
        "ds_read_b64_tr_b16 v[54:55], v227 offset:41984\n"
        "v_accvgpr_read_b32 v186, a234\n"
        "v_mfma_f32_16x16x32_bf16 a[252:255], v[24:27], v[60:63], a[252:255]\n"
        "ds_read_b64_tr_b16 v[56:57], v228 offset:33792\n"
        "v_accvgpr_read_b32 v187, a235\n"
        "v_mfma_f32_16x16x32_bf16 a[252:255], v[28:31], v[92:95], a[252:255]\n"
        "global_load_lds_dwordx4 v242, s[86:87] offset:0\n"
        "v_mfma_f32_16x16x32_bf16 a[220:223], v[20:23], v[92:95], a[220:223]\n"
        "ds_read_b64_tr_b16 v[58:59], v229 offset:33792\n"
        "v_accvgpr_read_b32 v188, a236\n"
        "v_mfma_f32_16x16x32_bf16 a[220:223], v[16:19], v[60:63], a[220:223]\n"
        "ds_read_b64_tr_b16 v[60:61], v228 offset:41984\n"
        "s_waitcnt lgkmcnt(5)\n"
        "v_mfma_f32_16x16x32_bf16 v[0:3], v[160:163], v[192:195], 0\n"
        "ds_read_b64_tr_b16 v[62:63], v229 offset:41984\n"
        "v_accvgpr_read_b32 v189, a237\n"
        "v_mfma_f32_16x16x32_bf16 v[0:3], v[164:167], v[208:211], v[0:3]\n"
        "global_load_lds_dwordx4 v243, s[86:87] offset:1024\n"
        "v_mfma_f32_16x16x32_bf16 v[4:7], v[172:175], v[208:211], 0\n"
        "v_accvgpr_read_b32 v190, a238\n"
        "v_accvgpr_read_b32 v191, a239\n"
        "v_mfma_f32_16x16x32_bf16 v[4:7], v[168:171], v[192:195], v[4:7]\n"
        "v_mul_f32 v185, s88, v185\n"
        "v_mul_f32 v186, s88, v186\n"
        "v_mfma_f32_16x16x32_bf16 v[68:71], v[168:171], v[196:199], 0\n"
        "v_mul_f32 v187, s88, v187\n"
        "v_mul_f32 v188, s88, v188\n"
        "v_mfma_f32_16x16x32_bf16 v[68:71], v[172:175], v[212:215], v[68:71]\n"
        "global_load_lds_dwordx4 v244, s[86:87] offset:2048\n"
        "v_mfma_f32_16x16x32_bf16 v[64:67], v[164:167], v[212:215], 0\n"
        "v_mul_f32 v189, s88, v189\n"
        "v_mul_f32 v190, s88, v190\n"
        "v_mfma_f32_16x16x32_bf16 v[64:67], v[160:163], v[196:199], v[64:67]\n"
        "v_accvgpr_read_b32 v176, a224\n"
        "v_mul_f32 v176, s88, v176\n"
        "v_mfma_f32_16x16x32_bf16 v[96:99], v[160:163], v[200:203], 0\n"
        "v_cvt_pk_bf16_f32 v176, v176, v177\n"
        "v_cvt_pk_bf16_f32 v177, v178, v179\n"
        "v_mfma_f32_16x16x32_bf16 v[96:99], v[164:167], v[216:219], v[96:99]\n"
        "v_cvt_pk_bf16_f32 v178, v180, v181\n"
        "v_cvt_pk_bf16_f32 v179, v182, v183\n"
        "v_mfma_f32_16x16x32_bf16 v[100:103], v[172:175], v[216:219], 0\n"
        "global_load_lds_dwordx4 v245, s[86:87] offset:3072\n"
        "s_add_u32 m0, s73, 0x20000\n"
        "v_accvgpr_write_b32 a224, v176\n"
        "v_mfma_f32_16x16x32_bf16 v[100:103], v[168:171], v[200:203], v[100:103]\n"
        "s_add_u32 s86, s68, 0x180000\n"
        "s_addc_u32 s87, s69, 0\n"
        "v_accvgpr_write_b32 a225, v177\n"
        "v_mfma_f32_16x16x32_bf16 v[132:135], v[168:171], v[204:207], 0\n"
        "ds_read_b64_tr_b16 v[168:169], v232 offset:33792\n"
        "v_accvgpr_write_b32 a226, v178\n"
        "v_mfma_f32_16x16x32_bf16 v[132:135], v[172:175], v[220:223], v[132:135]\n"
        "ds_read_b64_tr_b16 v[172:173], v232 offset:41984\n"
        "v_accvgpr_write_b32 a227, v179\n"
        "v_mfma_f32_16x16x32_bf16 v[128:131], v[164:167], v[220:223], 0\n"
        "ds_read_b64_tr_b16 v[164:165], v230 offset:41984\n"
        "v_mul_f32 v191, s88, v191\n"
        "v_mfma_f32_16x16x32_bf16 v[128:131], v[160:163], v[204:207], v[128:131]\n"
        "ds_read_b64_tr_b16 v[160:161], v230 offset:33792\n"
        "v_accvgpr_read_b32 v177, a193\n"
        "v_mfma_f32_16x16x32_bf16 v[8:11], v[32:35], v[192:195], 0\n"
        "ds_read_b64_tr_b16 v[162:163], v231 offset:33792\n"
        "v_accvgpr_read_b32 v178, a194\n"
        "v_mfma_f32_16x16x32_bf16 v[8:11], v[36:39], v[208:211], v[8:11]\n"
        "ds_read_b64_tr_b16 v[166:167], v231 offset:41984\n"
        "v_accvgpr_read_b32 v179, a195\n"
        "v_mfma_f32_16x16x32_bf16 v[12:15], v[44:47], v[208:211], 0\n"
        "ds_read_b64_tr_b16 v[174:175], v233 offset:41984\n"
        "v_accvgpr_read_b32 v180, a196\n"
        "v_mfma_f32_16x16x32_bf16 v[12:15], v[40:43], v[192:195], v[12:15]\n"
        "ds_read_b64_tr_b16 v[170:171], v233 offset:33792\n"
        "v_accvgpr_read_b32 v181, a197\n"
        "v_mfma_f32_16x16x32_bf16 v[76:79], v[40:43], v[196:199], 0\n"
        "v_accvgpr_read_b32 v182, a198\n"
        "v_accvgpr_read_b32 v183, a199\n"
        "v_mfma_f32_16x16x32_bf16 v[76:79], v[44:47], v[212:215], v[76:79]\n"
        "LHA4_%=:\n"
        "s_cbranch_execz LHA8_%=\n"
        "v_mul_f32 v177, s88, v177\n"
        "v_mul_f32 v178, s88, v178\n"
        "v_mfma_f32_16x16x32_bf16 v[72:75], v[36:39], v[212:215], 0\n"
        "v_mul_f32 v179, s88, v179\n"
        "v_mul_f32 v180, s88, v180\n"
        "v_mfma_f32_16x16x32_bf16 v[72:75], v[32:35], v[196:199], v[72:75]\n"
        "v_mul_f32 v181, s88, v181\n"
        "v_mul_f32 v182, s88, v182\n"
        "v_mfma_f32_16x16x32_bf16 v[104:107], v[32:35], v[200:203], 0\n"
        "v_mul_f32 v183, s88, v183\n"
        "v_mfma_f32_16x16x32_bf16 v[104:107], v[36:39], v[216:219], v[104:107]\n"
        "v_mfma_f32_16x16x32_bf16 v[108:111], v[44:47], v[216:219], 0\n"
        "v_mfma_f32_16x16x32_bf16 v[108:111], v[40:43], v[200:203], v[108:111]\n"
        "s_waitcnt vmcnt(14) lgkmcnt(0)\n"
        "v_mfma_f32_16x16x32_bf16 v[140:143], v[40:43], v[204:207], 0\n"
        "s_barrier\n"
        "v_mfma_f32_16x16x32_bf16 v[140:143], v[44:47], v[220:223], v[140:143]\n"
        "global_load_lds_dwordx4 v238, s[86:87] offset:0\n"
        "ds_read_b64_tr_b16 v[44:45], v228 offset:57344\n"
        "v_mfma_f32_16x16x32_bf16 v[136:139], v[36:39], v[220:223], 0\n"
        "ds_read_b64_tr_b16 v[36:37], v226 offset:57344\n"
        "v_mfma_f32_16x16x32_bf16 v[136:139], v[32:35], v[204:207], v[136:139]\n"
        "ds_read_b64_tr_b16 v[32:33], v226 offset:49152\n"
        "v_accvgpr_read_b32 v184, a232\n"
        "v_mfma_f32_16x16x32_bf16 v[16:19], v[48:51], v[192:195], 0\n"
        "ds_read_b64_tr_b16 v[34:35], v227 offset:49152\n"
        "v_mul_f32 v184, s88, v184\n"
        "v_mfma_f32_16x16x32_bf16 v[16:19], v[52:55], v[208:211], v[16:19]\n"
        "ds_read_b64_tr_b16 v[38:39], v227 offset:57344\n"
        "v_cvt_pk_bf16_f32 v184, v184, v185\n"
        "v_mfma_f32_16x16x32_bf16 v[20:23], v[60:63], v[208:211], 0\n"
        "ds_read_b64_tr_b16 v[46:47], v229 offset:57344\n"
        "v_cvt_pk_bf16_f32 v185, v186, v187\n"
        "v_mfma_f32_16x16x32_bf16 v[20:23], v[56:59], v[192:195], v[20:23]\n"
        "ds_read_b64_tr_b16 v[40:41], v228 offset:49152\n"
        "v_cvt_pk_bf16_f32 v186, v188, v189\n"
        "v_mfma_f32_16x16x32_bf16 v[84:87], v[56:59], v[196:199], 0\n"
        "ds_read_b64_tr_b16 v[42:43], v229 offset:49152\n"
        "v_cvt_pk_bf16_f32 v187, v190, v191\n"
        "v_mfma_f32_16x16x32_bf16 v[84:87], v[60:63], v[212:215], v[84:87]\n"
        "global_load_lds_dwordx4 v239, s[86:87] offset:1024\n"
        "v_accvgpr_write_b32 a228, v184\n"
        "v_mfma_f32_16x16x32_bf16 v[80:83], v[52:55], v[212:215], 0\n"
        "v_accvgpr_write_b32 a229, v185\n"
        "v_accvgpr_write_b32 a230, v186\n"
        "v_mfma_f32_16x16x32_bf16 v[80:83], v[48:51], v[196:199], v[80:83]\n"
        "v_accvgpr_write_b32 a231, v187\n"
        "v_accvgpr_read_b32 v185, a201\n"
        "v_mfma_f32_16x16x32_bf16 v[112:115], v[48:51], v[200:203], 0\n"
        "v_accvgpr_read_b32 v186, a202\n"
        "v_accvgpr_read_b32 v187, a203\n"
        "v_mfma_f32_16x16x32_bf16 v[112:115], v[52:55], v[216:219], v[112:115]\n"
        "v_accvgpr_read_b32 v188, a204\n"
        "v_accvgpr_read_b32 v189, a205\n"
        "v_mfma_f32_16x16x32_bf16 v[116:119], v[60:63], v[216:219], 0\n"
        "global_load_lds_dwordx4 v240, s[86:87] offset:2048\n"
        "v_accvgpr_read_b32 v190, a206\n"
        "v_mfma_f32_16x16x32_bf16 v[116:119], v[56:59], v[200:203], v[116:119]\n"
        "v_accvgpr_read_b32 v191, a207\n"
        "v_mul_f32 v185, s88, v185\n"
        "v_mfma_f32_16x16x32_bf16 v[148:151], v[56:59], v[204:207], 0\n"
        "ds_read_b64_tr_b16 v[56:57], v232 offset:49152\n"
        "v_mul_f32 v186, s88, v186\n"
        "v_mfma_f32_16x16x32_bf16 v[148:151], v[60:63], v[220:223], v[148:151]\n"
        "ds_read_b64_tr_b16 v[60:61], v232 offset:57344\n"
        "v_mul_f32 v187, s88, v187\n"
        "v_mfma_f32_16x16x32_bf16 v[144:147], v[52:55], v[220:223], 0\n"
        "global_store_dwordx4 v254, a[224:227], s[70:71] offset:0 sc1\n"
        "ds_read_b64_tr_b16 v[52:53], v230 offset:57344\n"
        "ds_read_b64_tr_b16 v[54:55], v231 offset:57344\n"
        "v_mul_f32 v188, s88, v188\n"
        "v_mfma_f32_16x16x32_bf16 v[144:147], v[48:51], v[204:207], v[144:147]\n"
        "ds_read_b64_tr_b16 v[48:49], v230 offset:49152\n"
        "v_mul_f32 v189, s88, v189\n"
        "v_mfma_f32_16x16x32_bf16 v[24:27], v[160:163], v[192:195], 0\n"
        "ds_read_b64_tr_b16 v[50:51], v231 offset:49152\n"
        "v_mul_f32 v190, s88, v190\n"
        "v_mfma_f32_16x16x32_bf16 v[24:27], v[164:167], v[208:211], v[24:27]\n"
        "ds_read_b64_tr_b16 v[62:63], v233 offset:57344\n"
        "v_mul_f32 v191, s88, v191\n"
        "v_mfma_f32_16x16x32_bf16 v[28:31], v[172:175], v[208:211], 0\n"
        "global_load_lds_dwordx4 v241, s[86:87] offset:3072\n"
        "s_add_u32 m0, s72, 0x0\n"
        "v_mfma_f32_16x16x32_bf16 v[28:31], v[168:171], v[192:195], v[28:31]\n"
        "ds_read_b64_tr_b16 v[58:59], v233 offset:49152\n"
        "s_add_u32 s86, s64, 0x180\n"
        "s_addc_u32 s87, s65, 0\n"
        "v_mfma_f32_16x16x32_bf16 v[92:95], v[168:171], v[196:199], 0\n"
        "ds_read_b128 v[192:195], v248 offset:2048\n"
        "v_mfma_f32_16x16x32_bf16 v[92:95], v[172:175], v[212:215], v[92:95]\n"
        "ds_read_b128 v[208:211], v249 offset:2048\n"
        "v_mfma_f32_16x16x32_bf16 v[88:91], v[164:167], v[212:215], 0\n"
        "ds_read_b128 v[212:215], v249 offset:0\n"
        "v_mfma_f32_16x16x32_bf16 v[88:91], v[160:163], v[196:199], v[88:91]\n"
        "global_load_lds_dwordx4 v242, s[86:87] offset:0\n"
        "v_mfma_f32_16x16x32_bf16 v[120:123], v[160:163], v[200:203], 0\n"
        "ds_read_b128 v[196:199], v248 offset:0\n"
        "v_accvgpr_read_b32 v176, a192\n"
        "v_mfma_f32_16x16x32_bf16 v[120:123], v[164:167], v[216:219], v[120:123]\n"
        "v_mul_f32 v176, s88, v176\n"
        "v_cvt_pk_bf16_f32 v176, v176, v177\n"
        "v_mfma_f32_16x16x32_bf16 v[124:127], v[172:175], v[216:219], 0\n"
        "ds_read_b128 v[216:219], v249 offset:6144\n"
        "v_cvt_pk_bf16_f32 v177, v178, v179\n"
        "v_mfma_f32_16x16x32_bf16 v[124:127], v[168:171], v[200:203], v[124:127]\n"
        "ds_read_b128 v[200:203], v248 offset:6144\n"
        "v_cvt_pk_bf16_f32 v178, v180, v181\n"
        "v_mfma_f32_16x16x32_bf16 v[156:159], v[168:171], v[204:207], 0\n"
        "s_waitcnt lgkmcnt(5)\n"
        "v_cvt_pk_bf16_f32 v179, v182, v183\n"
        "v_accvgpr_write_b32 a232, v176\n"
        "v_mfma_f32_16x16x32_bf16 v[156:159], v[172:175], v[220:223], v[156:159]\n"
        "s_waitcnt lgkmcnt(4)\n"
        "v_accvgpr_write_b32 a233, v177\n"
        "v_accvgpr_write_b32 a234, v178\n"
        "v_mfma_f32_16x16x32_bf16 v[152:155], v[164:167], v[220:223], 0\n"
        "global_load_lds_dwordx4 v243, s[86:87] offset:1024\n"
        "v_accvgpr_write_b32 a235, v179\n"
        "v_mfma_f32_16x16x32_bf16 v[152:155], v[160:163], v[204:207], v[152:155]\n"
        "ds_read_b128 v[204:207], v248 offset:4096\n"
        "v_accvgpr_read_b32 v177, a161\n"
        "v_mfma_f32_16x16x32_bf16 v[0:3], v[32:35], v[192:195], v[0:3]\n"
        "ds_read_b128 v[220:223], v249 offset:4096\n"
        "v_accvgpr_read_b32 v178, a162\n"
        "v_mfma_f32_16x16x32_bf16 v[0:3], v[36:39], v[208:211], v[0:3]\n"
        "ds_read_b64_tr_b16 v[160:161], v226 offset:50176\n"
        "v_accvgpr_read_b32 v179, a163\n"
        "v_mfma_f32_16x16x32_bf16 v[4:7], v[44:47], v[208:211], v[4:7]\n"
        "ds_read_b64_tr_b16 v[162:163], v227 offset:50176\n"
        "v_accvgpr_read_b32 v180, a164\n"
        "v_mfma_f32_16x16x32_bf16 v[4:7], v[40:43], v[192:195], v[4:7]\n"
        "global_load_lds_dwordx4 v244, s[86:87] offset:2048\n"
        "s_waitcnt lgkmcnt(4)\n"
        "v_mfma_f32_16x16x32_bf16 v[68:71], v[40:43], v[196:199], v[68:71]\n"
        "ds_read_b64_tr_b16 v[164:165], v226 offset:58368\n"
        "v_accvgpr_read_b32 v181, a165\n"
        "v_mfma_f32_16x16x32_bf16 v[68:71], v[44:47], v[212:215], v[68:71]\n"
        "ds_read_b64_tr_b16 v[166:167], v227 offset:58368\n"
        "v_accvgpr_read_b32 v182, a166\n"
        "v_mfma_f32_16x16x32_bf16 v[64:67], v[36:39], v[212:215], v[64:67]\n"
        "ds_read_b64_tr_b16 v[172:173], v228 offset:58368\n"
        "v_accvgpr_read_b32 v183, a167\n"
        "v_mfma_f32_16x16x32_bf16 v[64:67], v[32:35], v[196:199], v[64:67]\n"
        "ds_read_b64_tr_b16 v[174:175], v229 offset:58368\n"
        "v_mul_f32 v177, s88, v177\n"
        "v_mfma_f32_16x16x32_bf16 v[96:99], v[32:35], v[200:203], v[96:99]\n"
        "ds_read_b64_tr_b16 v[168:169], v228 offset:50176\n"
        "global_load_lds_dwordx4 v245, s[86:87] offset:3072\n"
        "s_add_u32 m0, s72, 0x1000\n"
        "v_mfma_f32_16x16x32_bf16 v[96:99], v[36:39], v[216:219], v[96:99]\n"
        "ds_read_b64_tr_b16 v[170:171], v229 offset:50176\n"
        "s_add_u32 s86, s66, 0x180\n"
        "s_addc_u32 s87, s67, 0\n"
        "v_mfma_f32_16x16x32_bf16 v[100:103], v[44:47], v[216:219], v[100:103]\n"
        "s_waitcnt lgkmcnt(6)\n"
        "v_mul_f32 v178, s88, v178\n"
        "v_mul_f32 v179, s88, v179\n"
        "v_mfma_f32_16x16x32_bf16 v[100:103], v[40:43], v[200:203], v[100:103]\n"
        "v_mul_f32 v180, s88, v180\n"
        "v_mul_f32 v181, s88, v181\n"
        "v_mfma_f32_16x16x32_bf16 v[132:135], v[40:43], v[204:207], v[132:135]\n"
        "ds_read_b64_tr_b16 v[40:41], v232 offset:50176\n"
        "v_mul_f32 v182, s88, v182\n"
        "v_mfma_f32_16x16x32_bf16 v[132:135], v[44:47], v[220:223], v[132:135]\n"
        "global_load_lds_dwordx4 v242, s[86:87] offset:0\n"
        "v_mfma_f32_16x16x32_bf16 v[128:131], v[36:39], v[220:223], v[128:131]\n"
        "ds_read_b64_tr_b16 v[36:37], v230 offset:58368\n"
        "v_mul_f32 v183, s88, v183\n"
        "v_mfma_f32_16x16x32_bf16 v[128:131], v[32:35], v[204:207], v[128:131]\n"
        "ds_read_b64_tr_b16 v[32:33], v230 offset:50176\n"
        "v_accvgpr_read_b32 v184, a200\n"
        "v_mfma_f32_16x16x32_bf16 v[8:11], v[48:51], v[192:195], v[8:11]\n"
        "ds_read_b64_tr_b16 v[34:35], v231 offset:50176\n"
        "v_mul_f32 v184, s88, v184\n"
        "v_mfma_f32_16x16x32_bf16 v[8:11], v[52:55], v[208:211], v[8:11]\n"
        "ds_read_b64_tr_b16 v[38:39], v231 offset:58368\n"
        "v_cvt_pk_bf16_f32 v184, v184, v185\n"
        "v_mfma_f32_16x16x32_bf16 v[12:15], v[60:63], v[208:211], v[12:15]\n"
        "global_load_lds_dwordx4 v243, s[86:87] offset:1024\n"
        "v_mfma_f32_16x16x32_bf16 v[12:15], v[56:59], v[192:195], v[12:15]\n"
        "ds_read_b64_tr_b16 v[44:45], v232 offset:58368\n"
        "v_cvt_pk_bf16_f32 v185, v186, v187\n"
        "v_mfma_f32_16x16x32_bf16 v[76:79], v[56:59], v[196:199], v[76:79]\n"
        "ds_read_b64_tr_b16 v[46:47], v233 offset:58368\n"
        "v_cvt_pk_bf16_f32 v186, v188, v189\n"
        "v_mfma_f32_16x16x32_bf16 v[76:79], v[60:63], v[212:215], v[76:79]\n"
        "ds_read_b64_tr_b16 v[42:43], v233 offset:50176\n"
        "v_cvt_pk_bf16_f32 v187, v190, v191\n"
        "v_mfma_f32_16x16x32_bf16 v[72:75], v[52:55], v[212:215], v[72:75]\n"
        "v_accvgpr_write_b32 a236, v184\n"
        "v_accvgpr_write_b32 a237, v185\n"
        "v_mfma_f32_16x16x32_bf16 v[72:75], v[48:51], v[196:199], v[72:75]\n"
        "v_accvgpr_write_b32 a238, v186\n"
        "v_accvgpr_write_b32 a239, v187\n"
        "v_mfma_f32_16x16x32_bf16 v[104:107], v[48:51], v[200:203], v[104:107]\n"
        "global_load_lds_dwordx4 v244, s[86:87] offset:2048\n"
        "v_accvgpr_read_b32 v185, a169\n"
        "v_mfma_f32_16x16x32_bf16 v[104:107], v[52:55], v[216:219], v[104:107]\n"
        "v_accvgpr_read_b32 v186, a170\n"
        "v_accvgpr_read_b32 v187, a171\n"
        "v_mfma_f32_16x16x32_bf16 v[108:111], v[60:63], v[216:219], v[108:111]\n"
        "v_accvgpr_read_b32 v188, a172\n"
        "v_accvgpr_read_b32 v189, a173\n"
        "v_mfma_f32_16x16x32_bf16 v[108:111], v[56:59], v[200:203], v[108:111]\n"
        "v_accvgpr_read_b32 v190, a174\n"
        "v_accvgpr_read_b32 v191, a175\n"
        "v_mfma_f32_16x16x32_bf16 v[140:143], v[56:59], v[204:207], v[140:143]\n"
        "v_mul_f32 v185, s88, v185\n"
        "v_mul_f32 v186, s88, v186\n"
        "v_mfma_f32_16x16x32_bf16 v[140:143], v[60:63], v[220:223], v[140:143]\n"
        "global_load_lds_dwordx4 v245, s[86:87] offset:3072\n"
        "s_waitcnt vmcnt(13) lgkmcnt(0)\n"
        "v_mfma_f32_16x16x32_bf16 v[136:139], v[52:55], v[220:223], v[136:139]\n"
        "s_barrier\n"
        "v_mfma_f32_16x16x32_bf16 v[136:139], v[48:51], v[204:207], v[136:139]\n"
        "s_add_u32 m0, s73, 0x24000\n"
        "s_add_u32 s86, s68, 0x200000\n"
        "s_addc_u32 s87, s69, 0\n"
        "v_mfma_f32_16x16x32_bf16 v[16:19], v[160:163], v[192:195], v[16:19]\n"
        "ds_read_b64_tr_b16 v[48:49], v226 offset:16384\n"
        "v_mul_f32 v187, s88, v187\n"
        "v_mfma_f32_16x16x32_bf16 v[16:19], v[164:167], v[208:211], v[16:19]\n"
        "ds_read_b64_tr_b16 v[50:51], v227 offset:16384\n"
        "v_mul_f32 v188, s88, v188\n"
        "v_mfma_f32_16x16x32_bf16 v[20:23], v[172:175], v[208:211], v[20:23]\n"
        "global_load_lds_dwordx4 v238, s[86:87] offset:0\n"
        "ds_read_b64_tr_b16 v[52:53], v226 offset:24576\n"
        "v_mfma_f32_16x16x32_bf16 v[20:23], v[168:171], v[192:195], v[20:23]\n"
        "ds_read_b64_tr_b16 v[54:55], v227 offset:24576\n"
        "v_mul_f32 v189, s88, v189\n"
        "v_mfma_f32_16x16x32_bf16 v[84:87], v[168:171], v[196:199], v[84:87]\n"
        "ds_read_b64_tr_b16 v[60:61], v228 offset:24576\n"
        "v_mul_f32 v190, s88, v190\n"
        "v_mfma_f32_16x16x32_bf16 v[84:87], v[172:175], v[212:215], v[84:87]\n"
        "ds_read_b64_tr_b16 v[62:63], v229 offset:24576\n"
        "v_mul_f32 v191, s88, v191\n"
        "v_mfma_f32_16x16x32_bf16 v[80:83], v[164:167], v[212:215], v[80:83]\n"
        "ds_read_b64_tr_b16 v[56:57], v228 offset:16384\n"
        "v_mfma_f32_16x16x32_bf16 v[80:83], v[160:163], v[196:199], v[80:83]\n"
        "ds_read_b64_tr_b16 v[58:59], v229 offset:16384\n"
        "global_load_lds_dwordx4 v239, s[86:87] offset:1024\n"
        "v_mfma_f32_16x16x32_bf16 v[112:115], v[160:163], v[200:203], v[112:115]\n"
        "v_accvgpr_read_b32 v176, a160\n"
        "v_mul_f32 v176, s88, v176\n"
        "v_mfma_f32_16x16x32_bf16 v[112:115], v[164:167], v[216:219], v[112:115]\n"
        "v_cvt_pk_bf16_f32 v176, v176, v177\n"
        "v_cvt_pk_bf16_f32 v177, v178, v179\n"
        "v_mfma_f32_16x16x32_bf16 v[116:119], v[172:175], v[216:219], v[116:119]\n"
        "v_cvt_pk_bf16_f32 v178, v180, v181\n"
        "v_cvt_pk_bf16_f32 v179, v182, v183\n"
        "v_mfma_f32_16x16x32_bf16 v[116:119], v[168:171], v[200:203], v[116:119]\n"
        "v_accvgpr_write_b32 a192, v176\n"
        "v_accvgpr_write_b32 a193, v177\n"
        "v_mfma_f32_16x16x32_bf16 v[148:151], v[168:171], v[204:207], v[148:151]\n"
        "ds_read_b64_tr_b16 v[168:169], v232 offset:16384\n"
        "v_accvgpr_write_b32 a194, v178\n"
        "v_mfma_f32_16x16x32_bf16 v[148:151], v[172:175], v[220:223], v[148:151]\n"
        "global_load_lds_dwordx4 v240, s[86:87] offset:2048\n"
        "v_accvgpr_write_b32 a195, v179\n"
        "v_mfma_f32_16x16x32_bf16 v[144:147], v[164:167], v[220:223], v[144:147]\n"
        "ds_read_b64_tr_b16 v[164:165], v230 offset:24576\n"
        "v_accvgpr_read_b32 v177, a1\n"
        "v_mfma_f32_16x16x32_bf16 v[144:147], v[160:163], v[204:207], v[144:147]\n"
        "ds_read_b64_tr_b16 v[160:161], v230 offset:16384\n"
        "v_accvgpr_read_b32 v178, a2\n"
        "v_mfma_f32_16x16x32_bf16 v[24:27], v[32:35], v[192:195], v[24:27]\n"
        "ds_read_b64_tr_b16 v[162:163], v231 offset:16384\n"
        "v_accvgpr_read_b32 v179, a3\n"
        "v_mfma_f32_16x16x32_bf16 v[24:27], v[36:39], v[208:211], v[24:27]\n"
        "global_store_dwordx4 v254, a[228:231], s[70:71] offset:1024 sc1\n"
        "ds_read_b64_tr_b16 v[166:167], v231 offset:24576\n"
        "ds_read_b64_tr_b16 v[172:173], v232 offset:24576\n"
        "v_accvgpr_read_b32 v180, a4\n"
        "v_mfma_f32_16x16x32_bf16 v[28:31], v[44:47], v[208:211], v[28:31]\n"
        "ds_read_b64_tr_b16 v[174:175], v233 offset:24576\n"
        "v_accvgpr_read_b32 v181, a5\n"
        "v_mfma_f32_16x16x32_bf16 v[28:31], v[40:43], v[192:195], v[28:31]\n"
        "ds_read_b64_tr_b16 v[170:171], v233 offset:16384\n"
        "v_accvgpr_read_b32 v182, a6\n"
        "v_mfma_f32_16x16x32_bf16 v[92:95], v[40:43], v[196:199], v[92:95]\n"
        "ds_read_b128 v[192:195], v246 offset:34816\n"
        "v_accvgpr_read_b32 v183, a7\n"
        "v_mfma_f32_16x16x32_bf16 v[92:95], v[44:47], v[212:215], v[92:95]\n"
        "global_load_lds_dwordx4 v241, s[86:87] offset:3072\n"
        "s_add_u32 m0, s72, 0x10000\n"
        "v_mfma_f32_16x16x32_bf16 v[88:91], v[36:39], v[212:215], v[88:91]\n"
        "ds_read_b128 v[208:211], v247 offset:34816\n"
        "s_add_u32 s86, s64, 0x200\n"
        "s_addc_u32 s87, s65, 0\n"
        "v_mfma_f32_16x16x32_bf16 v[88:91], v[32:35], v[196:199], v[88:91]\n"
        "ds_read_b128 v[196:199], v246 offset:32768\n"
        "v_mul_f32 v177, s88, v177\n"
        "v_mfma_f32_16x16x32_bf16 v[120:123], v[32:35], v[200:203], v[120:123]\n"
        "ds_read_b128 v[212:215], v247 offset:32768\n"
        "v_mul_f32 v178, s88, v178\n"
        "v_mfma_f32_16x16x32_bf16 v[120:123], v[36:39], v[216:219], v[120:123]\n"
        "s_add_u32 s70, s38, s96\n"
        "s_addc_u32 s71, s39, 0\n"
        "v_mul_f32 v179, s88, v179\n"
        "v_mfma_f32_16x16x32_bf16 v[124:127], v[44:47], v[216:219], v[124:127]\n"
        "global_load_lds_dwordx4 v242, s[86:87] offset:0\n"
        "v_mfma_f32_16x16x32_bf16 v[124:127], v[40:43], v[200:203], v[124:127]\n"
        "ds_read_b128 v[200:203], v246 offset:38912\n"
        "v_mul_f32 v180, s88, v180\n"
        "v_mfma_f32_16x16x32_bf16 v[156:159], v[40:43], v[204:207], v[156:159]\n"
        "ds_read_b128 v[216:219], v247 offset:38912\n"
        "v_mul_f32 v181, s88, v181\n"
        "v_mfma_f32_16x16x32_bf16 v[156:159], v[44:47], v[220:223], v[156:159]\n"
        "ds_read_b64_tr_b16 v[44:45], v228 offset:25600\n"
        "v_mul_f32 v182, s88, v182\n"
        "v_mfma_f32_16x16x32_bf16 v[152:155], v[36:39], v[220:223], v[152:155]\n"
        "ds_read_b128 v[220:223], v247 offset:36864\n"
        "v_mul_f32 v183, s88, v183\n"
        "v_mfma_f32_16x16x32_bf16 v[152:155], v[32:35], v[204:207], v[152:155]\n"
        "s_waitcnt lgkmcnt(5)\n"
        "global_load_lds_dwordx4 v243, s[86:87] offset:1024\n"
        "s_add_u32 s96, s52, 0x1000\n"
        "v_mfma_f32_16x16x32_bf16 v[0:3], v[48:51], v[192:195], v[0:3]\n"
        "ds_read_b128 v[204:207], v246 offset:36864\n"
        "v_accvgpr_read_b32 v184, a168\n"
        "v_mfma_f32_16x16x32_bf16 v[0:3], v[52:55], v[208:211], v[0:3]\n"
        "s_waitcnt lgkmcnt(5)\n"
        "v_mul_f32 v184, s88, v184\n"
        "v_cvt_pk_bf16_f32 v184, v184, v185\n"
        "v_mfma_f32_16x16x32_bf16 v[4:7], v[60:63], v[208:211], v[4:7]\n"
        "ds_read_b64_tr_b16 v[32:33], v226 offset:17408\n"
        "v_cvt_pk_bf16_f32 v185, v186, v187\n"
        "v_mfma_f32_16x16x32_bf16 v[4:7], v[56:59], v[192:195], v[4:7]\n"
        "ds_read_b64_tr_b16 v[34:35], v227 offset:17408\n"
        "v_cvt_pk_bf16_f32 v186, v188, v189\n"
        "v_mfma_f32_16x16x32_bf16 v[68:71], v[56:59], v[196:199], v[68:71]\n"
        "global_load_lds_dwordx4 v244, s[86:87] offset:2048\n"
        "v_mfma_f32_16x16x32_bf16 v[68:71], v[60:63], v[212:215], v[68:71]\n"
        "s_waitcnt lgkmcnt(4)\n"
        "v_cvt_pk_bf16_f32 v187, v190, v191\n"
        "v_accvgpr_write_b32 a196, v184\n"
        "v_mfma_f32_16x16x32_bf16 v[64:67], v[52:55], v[212:215], v[64:67]\n"
        "ds_read_b64_tr_b16 v[36:37], v226 offset:25600\n"
        "v_accvgpr_write_b32 a197, v185\n"
        "v_mfma_f32_16x16x32_bf16 v[64:67], v[48:51], v[196:199], v[64:67]\n"
        "ds_read_b64_tr_b16 v[38:39], v227 offset:25600\n"
        "v_accvgpr_write_b32 a198, v186\n"
        "v_mfma_f32_16x16x32_bf16 v[96:99], v[48:51], v[200:203], v[96:99]\n"
        "s_waitcnt lgkmcnt(4)\n"
        "v_accvgpr_write_b32 a199, v187\n"
        "v_accvgpr_read_b32 v185, a9\n"
        "v_mfma_f32_16x16x32_bf16 v[96:99], v[52:55], v[216:219], v[96:99]\n"
        "ds_read_b64_tr_b16 v[46:47], v229 offset:25600\n"
        "global_load_lds_dwordx4 v245, s[86:87] offset:3072\n"
        "s_add_u32 m0, s72, 0x11000\n"
        "v_mfma_f32_16x16x32_bf16 v[100:103], v[60:63], v[216:219], v[100:103]\n"
        "ds_read_b64_tr_b16 v[40:41], v228 offset:17408\n"
        "s_add_u32 s86, s66, 0x200\n"
        "s_addc_u32 s87, s67, 0\n"
        "v_mfma_f32_16x16x32_bf16 v[100:103], v[56:59], v[200:203], v[100:103]\n"
        "ds_read_b64_tr_b16 v[42:43], v229 offset:17408\n"
        "v_accvgpr_read_b32 v186, a10\n"
        "v_mfma_f32_16x16x32_bf16 v[132:135], v[56:59], v[204:207], v[132:135]\n"
        "ds_read_b64_tr_b16 v[56:57], v232 offset:17408\n"
        "v_accvgpr_read_b32 v187, a11\n"
        "v_mfma_f32_16x16x32_bf16 v[132:135], v[60:63], v[220:223], v[132:135]\n"
        "ds_read_b64_tr_b16 v[60:61], v232 offset:25600\n"
        "v_accvgpr_read_b32 v188, a12\n"
        "v_mfma_f32_16x16x32_bf16 v[128:131], v[52:55], v[220:223], v[128:131]\n"
        "global_load_lds_dwordx4 v242, s[86:87] offset:0\n"
        "v_mfma_f32_16x16x32_bf16 v[128:131], v[48:51], v[204:207], v[128:131]\n"
        "ds_read_b64_tr_b16 v[48:49], v230 offset:17408\n"
        "v_accvgpr_read_b32 v189, a13\n"
        "v_mfma_f32_16x16x32_bf16 v[8:11], v[160:163], v[192:195], v[8:11]\n"
        "ds_read_b64_tr_b16 v[50:51], v231 offset:17408\n"
        "v_accvgpr_read_b32 v190, a14\n"
        "v_mfma_f32_16x16x32_bf16 v[8:11], v[164:167], v[208:211], v[8:11]\n"
        "ds_read_b64_tr_b16 v[52:53], v230 offset:25600\n"
        "v_accvgpr_read_b32 v191, a15\n"
        "v_mfma_f32_16x16x32_bf16 v[12:15], v[172:175], v[208:211], v[12:15]\n"
        "ds_read_b64_tr_b16 v[54:55], v231 offset:25600\n"
        "v_mul_f32 v185, s88, v185\n"
        "v_mfma_f32_16x16x32_bf16 v[12:15], v[168:171], v[192:195], v[12:15]\n"
        "global_load_lds_dwordx4 v243, s[86:87] offset:1024\n"
        "v_mfma_f32_16x16x32_bf16 v[76:79], v[168:171], v[196:199], v[76:79]\n"
        "ds_read_b64_tr_b16 v[62:63], v233 offset:25600\n"
        "v_mul_f32 v186, s88, v186\n"
        "v_mfma_f32_16x16x32_bf16 v[76:79], v[172:175], v[212:215], v[76:79]\n"
        "ds_read_b64_tr_b16 v[58:59], v233 offset:17408\n"
        "v_mul_f32 v187, s88, v187\n"
        "v_mfma_f32_16x16x32_bf16 v[72:75], v[164:167], v[212:215], v[72:75]\n"
        "v_mul_f32 v188, s88, v188\n"
        "v_mul_f32 v189, s88, v189\n"
        "v_mfma_f32_16x16x32_bf16 v[72:75], v[160:163], v[196:199], v[72:75]\n"
        "v_accvgpr_read_b32 v176, a0\n"
        "v_mul_f32 v176, s88, v176\n"
        "v_mfma_f32_16x16x32_bf16 v[104:107], v[160:163], v[200:203], v[104:107]\n"
        "global_load_lds_dwordx4 v244, s[86:87] offset:2048\n"
        "v_mfma_f32_16x16x32_bf16 v[104:107], v[164:167], v[216:219], v[104:107]\n"
        "v_cvt_pk_bf16_f32 v176, v176, v177\n"
        "v_cvt_pk_bf16_f32 v177, v178, v179\n"
        "v_mfma_f32_16x16x32_bf16 v[108:111], v[172:175], v[216:219], v[108:111]\n"
        "v_cvt_pk_bf16_f32 v178, v180, v181\n"
        "v_cvt_pk_bf16_f32 v179, v182, v183\n"
        "v_mfma_f32_16x16x32_bf16 v[108:111], v[168:171], v[200:203], v[108:111]\n"
        "v_accvgpr_write_b32 a200, v176\n"
        "v_accvgpr_write_b32 a201, v177\n"
        "v_mfma_f32_16x16x32_bf16 v[140:143], v[168:171], v[204:207], v[140:143]\n"
        "s_waitcnt lgkmcnt(1)\n"
        "v_accvgpr_write_b32 a202, v178\n"
        "v_accvgpr_write_b32 a203, v179\n"
        "v_mfma_f32_16x16x32_bf16 v[140:143], v[172:175], v[220:223], v[140:143]\n"
        "global_load_lds_dwordx4 v245, s[86:87] offset:3072\n"
        "s_add_u32 m0, s73, 0x1c000\n"
        "v_mfma_f32_16x16x32_bf16 v[136:139], v[164:167], v[220:223], v[136:139]\n"
        "s_waitcnt vmcnt(13) lgkmcnt(0)\n"
        "s_add_u32 s86, s68, 0x280000\n"
        "s_addc_u32 s87, s69, 0\n"
        "v_mfma_f32_16x16x32_bf16 v[136:139], v[160:163], v[204:207], v[136:139]\n"
        "s_barrier\n"
        "v_mfma_f32_16x16x32_bf16 v[16:19], v[32:35], v[192:195], v[16:19]\n"
        "ds_read_b64_tr_b16 v[160:161], v226 offset:32768\n"
        "v_mul_f32 v190, s88, v190\n"
        "v_mfma_f32_16x16x32_bf16 v[16:19], v[36:39], v[208:211], v[16:19]\n"
        "ds_read_b64_tr_b16 v[162:163], v227 offset:32768\n"
        "v_mul_f32 v191, s88, v191\n"
        "v_mfma_f32_16x16x32_bf16 v[20:23], v[44:47], v[208:211], v[20:23]\n"
        "global_load_lds_dwordx4 v238, s[86:87] offset:0\n"
        "v_mfma_f32_16x16x32_bf16 v[20:23], v[40:43], v[192:195], v[20:23]\n"
        "ds_read_b64_tr_b16 v[164:165], v226 offset:40960\n"
        "v_accvgpr_read_b32 v177, a33\n"
        "v_mfma_f32_16x16x32_bf16 v[84:87], v[40:43], v[196:199], v[84:87]\n"
        "ds_read_b64_tr_b16 v[166:167], v227 offset:40960\n"
        "v_accvgpr_read_b32 v178, a34\n"
        "v_mfma_f32_16x16x32_bf16 v[84:87], v[44:47], v[212:215], v[84:87]\n"
        "ds_read_b64_tr_b16 v[172:173], v228 offset:40960\n"
        "v_accvgpr_read_b32 v179, a35\n"
        "v_mfma_f32_16x16x32_bf16 v[80:83], v[36:39], v[212:215], v[80:83]\n"
        "ds_read_b64_tr_b16 v[174:175], v229 offset:40960\n"
        "v_accvgpr_read_b32 v180, a36\n"
        "v_mfma_f32_16x16x32_bf16 v[80:83], v[32:35], v[196:199], v[80:83]\n"
        "ds_read_b64_tr_b16 v[168:169], v228 offset:32768\n"
        "global_load_lds_dwordx4 v239, s[86:87] offset:1024\n"
        "v_mfma_f32_16x16x32_bf16 v[112:115], v[32:35], v[200:203], v[112:115]\n"
        "ds_read_b64_tr_b16 v[170:171], v229 offset:32768\n"
        "v_accvgpr_read_b32 v181, a37\n"
        "v_mfma_f32_16x16x32_bf16 v[112:115], v[36:39], v[216:219], v[112:115]\n"
        "v_accvgpr_read_b32 v182, a38\n"
        "v_accvgpr_read_b32 v183, a39\n"
        "v_mfma_f32_16x16x32_bf16 v[116:119], v[44:47], v[216:219], v[116:119]\n"
        "v_mul_f32 v177, s88, v177\n"
        "v_mul_f32 v178, s88, v178\n"
        "v_mfma_f32_16x16x32_bf16 v[116:119], v[40:43], v[200:203], v[116:119]\n"
        "v_mul_f32 v179, s88, v179\n"
        "v_mul_f32 v180, s88, v180\n"
        "v_mfma_f32_16x16x32_bf16 v[148:151], v[40:43], v[204:207], v[148:151]\n"
        "global_load_lds_dwordx4 v240, s[86:87] offset:2048\n"
        "v_mfma_f32_16x16x32_bf16 v[148:151], v[44:47], v[220:223], v[148:151]\n"
        "ds_read_b64_tr_b16 v[44:45], v232 offset:40960\n"
        "v_mul_f32 v181, s88, v181\n"
        "v_mfma_f32_16x16x32_bf16 v[144:147], v[36:39], v[220:223], v[144:147]\n"
        "ds_read_b64_tr_b16 v[36:37], v230 offset:40960\n"
        "v_mul_f32 v182, s88, v182\n"
        "v_mfma_f32_16x16x32_bf16 v[144:147], v[32:35], v[204:207], v[144:147]\n"
        "ds_read_b64_tr_b16 v[32:33], v230 offset:32768\n"
        "v_accvgpr_read_b32 v184, a8\n"
        "v_mfma_f32_16x16x32_bf16 v[24:27], v[48:51], v[192:195], v[24:27]\n"
        "ds_read_b64_tr_b16 v[34:35], v231 offset:32768\n"
        "v_mul_f32 v184, s88, v184\n"
        "v_mfma_f32_16x16x32_bf16 v[24:27], v[52:55], v[208:211], v[24:27]\n"
        "global_load_lds_dwordx4 v241, s[86:87] offset:3072\n"
        "s_add_u32 m0, s72, 0x8000\n"
        "v_mfma_f32_16x16x32_bf16 v[28:31], v[60:63], v[208:211], v[28:31]\n"
        "ds_read_b64_tr_b16 v[38:39], v231 offset:40960\n"
        "v_cvt_pk_bf16_f32 v184, v184, v185\n"
        "v_mfma_f32_16x16x32_bf16 v[28:31], v[56:59], v[192:195], v[28:31]\n"
        "ds_read_b64_tr_b16 v[46:47], v233 offset:40960\n"
        "v_cvt_pk_bf16_f32 v185, v186, v187\n"
        "v_mfma_f32_16x16x32_bf16 v[92:95], v[56:59], v[196:199], v[92:95]\n"
        "ds_read_b64_tr_b16 v[40:41], v232 offset:32768\n"
        "v_cvt_pk_bf16_f32 v186, v188, v189\n"
        "v_mfma_f32_16x16x32_bf16 v[92:95], v[60:63], v[212:215], v[92:95]\n"
        "ds_read_b64_tr_b16 v[42:43], v233 offset:32768\n"
        "v_cvt_pk_bf16_f32 v187, v190, v191\n"
        "v_mfma_f32_16x16x32_bf16 v[88:91], v[52:55], v[212:215], v[88:91]\n"
        "ds_read_b128 v[192:195], v246 offset:2048\n"
        "v_accvgpr_write_b32 a204, v184\n"
        "v_mfma_f32_16x16x32_bf16 v[88:91], v[48:51], v[196:199], v[88:91]\n"
        "ds_read_b128 v[208:211], v247 offset:2048\n"
        "v_accvgpr_write_b32 a205, v185\n"
        "v_mfma_f32_16x16x32_bf16 v[120:123], v[48:51], v[200:203], v[120:123]\n"
        "ds_read_b128 v[196:199], v246 offset:0\n"
        "v_accvgpr_write_b32 a206, v186\n"
        "v_mfma_f32_16x16x32_bf16 v[120:123], v[52:55], v[216:219], v[120:123]\n"
        "ds_read_b128 v[212:215], v247 offset:0\n"
        "v_accvgpr_write_b32 a207, v187\n"
        "v_mfma_f32_16x16x32_bf16 v[124:127], v[60:63], v[216:219], v[124:127]\n"
        "s_add_u32 s86, s64, 0x280\n"
        "s_addc_u32 s87, s65, 0\n"
        "v_mul_f32 v183, s88, v183\n"
        "v_mfma_f32_16x16x32_bf16 v[124:127], v[56:59], v[200:203], v[124:127]\n"
        "global_load_lds_dwordx4 v242, s[86:87] offset:0\n"
        "ds_read_b128 v[200:203], v246 offset:6144\n"
        "v_mfma_f32_16x16x32_bf16 v[156:159], v[56:59], v[204:207], v[156:159]\n"
        "ds_read_b128 v[216:219], v247 offset:6144\n"
        "v_accvgpr_read_b32 v185, a41\n"
        "v_mfma_f32_16x16x32_bf16 v[156:159], v[60:63], v[220:223], v[156:159]\n"
        "ds_read_b64_tr_b16 v[60:61], v228 offset:41984\n"
        "v_accvgpr_read_b32 v186, a42\n"
        "v_mfma_f32_16x16x32_bf16 v[152:155], v[52:55], v[220:223], v[152:155]\n"
        "ds_read_b128 v[220:223], v247 offset:4096\n"
        "v_accvgpr_read_b32 v187, a43\n"
        "v_mfma_f32_16x16x32_bf16 v[152:155], v[48:51], v[204:207], v[152:155]\n"
        "ds_read_b128 v[204:207], v246 offset:4096\n"
        "s_waitcnt lgkmcnt(7)\n"
        "v_mfma_f32_16x16x32_bf16 v[0:3], v[160:163], v[192:195], v[0:3]\n"
        "global_store_dwordx4 v254, a[232:235], s[70:71] offset:0 sc1\n"
        "ds_read_b64_tr_b16 v[48:49], v226 offset:33792\n"
        "ds_read_b64_tr_b16 v[50:51], v227 offset:33792\n"
        "v_accvgpr_read_b32 v188, a44\n"
        "v_mfma_f32_16x16x32_bf16 v[0:3], v[164:167], v[208:211], v[0:3]\n"
        "ds_read_b64_tr_b16 v[52:53], v226 offset:41984\n"
        "v_accvgpr_read_b32 v189, a45\n"
        "v_mfma_f32_16x16x32_bf16 v[4:7], v[172:175], v[208:211], v[4:7]\n"
        "ds_read_b64_tr_b16 v[54:55], v227 offset:41984\n"
        "v_accvgpr_read_b32 v190, a46\n"
        "v_mfma_f32_16x16x32_bf16 v[4:7], v[168:171], v[192:195], v[4:7]\n"
        "ds_read_b64_tr_b16 v[62:63], v229 offset:41984\n"
        "s_waitcnt lgkmcnt(8)\n"
        "v_mfma_f32_16x16x32_bf16 v[68:71], v[168:171], v[196:199], v[68:71]\n"
        "global_load_lds_dwordx4 v243, s[86:87] offset:1024\n"
        "v_mfma_f32_16x16x32_bf16 v[68:71], v[172:175], v[212:215], v[68:71]\n"
        "ds_read_b64_tr_b16 v[56:57], v228 offset:33792\n"
        "v_accvgpr_read_b32 v191, a47\n"
        "v_mfma_f32_16x16x32_bf16 v[64:67], v[164:167], v[212:215], v[64:67]\n"
        "ds_read_b64_tr_b16 v[58:59], v229 offset:33792\n"
        "v_mul_f32 v185, s88, v185\n"
        "v_mfma_f32_16x16x32_bf16 v[64:67], v[160:163], v[196:199], v[64:67]\n"
        "v_accvgpr_read_b32 v176, a32\n"
        "v_mul_f32 v176, s88, v176\n"
        "v_mfma_f32_16x16x32_bf16 v[96:99], v[160:163], v[200:203], v[96:99]\n"
        "s_waitcnt lgkmcnt(5)\n"
        "v_cvt_pk_bf16_f32 v176, v176, v177\n"
        "v_cvt_pk_bf16_f32 v177, v178, v179\n"
        "v_mfma_f32_16x16x32_bf16 v[96:99], v[164:167], v[216:219], v[96:99]\n"
        "global_load_lds_dwordx4 v244, s[86:87] offset:2048\n"
        "v_mfma_f32_16x16x32_bf16 v[100:103], v[172:175], v[216:219], v[100:103]\n"
        "v_cvt_pk_bf16_f32 v178, v180, v181\n"
        "v_cvt_pk_bf16_f32 v179, v182, v183\n"
        "v_mfma_f32_16x16x32_bf16 v[100:103], v[168:171], v[200:203], v[100:103]\n"
        "v_accvgpr_write_b32 a160, v176\n"
        "v_accvgpr_write_b32 a161, v177\n"
        "v_mfma_f32_16x16x32_bf16 v[132:135], v[168:171], v[204:207], v[132:135]\n"
        "ds_read_b64_tr_b16 v[168:169], v232 offset:33792\n"
        "v_accvgpr_write_b32 a162, v178\n"
        "v_mfma_f32_16x16x32_bf16 v[132:135], v[172:175], v[220:223], v[132:135]\n"
        "ds_read_b64_tr_b16 v[172:173], v232 offset:41984\n"
        "v_accvgpr_write_b32 a163, v179\n"
        "v_mfma_f32_16x16x32_bf16 v[128:131], v[164:167], v[220:223], v[128:131]\n"
        "LHA5_%=:\n"
        "s_cbranch_execz LHA9_%=\n"
        "global_load_lds_dwordx4 v245, s[86:87] offset:3072\n"
        "s_add_u32 m0, s72, 0x9000\n"
        "v_mfma_f32_16x16x32_bf16 v[128:131], v[160:163], v[204:207], v[128:131]\n"
        "s_add_u32 s86, s66, 0x280\n"
        "s_addc_u32 s87, s67, 0\n"
        "v_mul_f32 v186, s88, v186\n"
        "v_mfma_f32_16x16x32_bf16 v[8:11], v[32:35], v[192:195], v[8:11]\n"
        "ds_read_b64_tr_b16 v[160:161], v230 offset:33792\n"
        "v_mul_f32 v187, s88, v187\n"
        "v_mfma_f32_16x16x32_bf16 v[8:11], v[36:39], v[208:211], v[8:11]\n"
        "ds_read_b64_tr_b16 v[162:163], v231 offset:33792\n"
        "v_mul_f32 v188, s88, v188\n"
        "v_mfma_f32_16x16x32_bf16 v[12:15], v[44:47], v[208:211], v[12:15]\n"
        "ds_read_b64_tr_b16 v[164:165], v230 offset:41984\n"
        "v_mul_f32 v189, s88, v189\n"
        "v_mfma_f32_16x16x32_bf16 v[12:15], v[40:43], v[192:195], v[12:15]\n"
        "global_load_lds_dwordx4 v242, s[86:87] offset:0\n"
        "v_mfma_f32_16x16x32_bf16 v[76:79], v[40:43], v[196:199], v[76:79]\n"
        "ds_read_b64_tr_b16 v[166:167], v231 offset:41984\n"
        "v_mul_f32 v190, s88, v190\n"
        "v_mfma_f32_16x16x32_bf16 v[76:79], v[44:47], v[212:215], v[76:79]\n"
        "ds_read_b64_tr_b16 v[174:175], v233 offset:41984\n"
        "v_mul_f32 v191, s88, v191\n"
        "v_mfma_f32_16x16x32_bf16 v[72:75], v[36:39], v[212:215], v[72:75]\n"
        "ds_read_b64_tr_b16 v[170:171], v233 offset:33792\n"
        "v_accvgpr_read_b32 v177, a65\n"
        "v_mfma_f32_16x16x32_bf16 v[72:75], v[32:35], v[196:199], v[72:75]\n"
        "v_accvgpr_read_b32 v178, a66\n"
        "v_accvgpr_read_b32 v179, a67\n"
        "v_mfma_f32_16x16x32_bf16 v[104:107], v[32:35], v[200:203], v[104:107]\n"
        "global_load_lds_dwordx4 v243, s[86:87] offset:1024\n"
        "v_mfma_f32_16x16x32_bf16 v[104:107], v[36:39], v[216:219], v[104:107]\n"
        "v_accvgpr_read_b32 v180, a68\n"
        "v_accvgpr_read_b32 v181, a69\n"
        "v_mfma_f32_16x16x32_bf16 v[108:111], v[44:47], v[216:219], v[108:111]\n"
        "v_accvgpr_read_b32 v182, a70\n"
        "v_accvgpr_read_b32 v183, a71\n"
        "v_mfma_f32_16x16x32_bf16 v[108:111], v[40:43], v[200:203], v[108:111]\n"
        "v_mul_f32 v177, s88, v177\n"
        "v_mul_f32 v178, s88, v178\n"
        "v_mfma_f32_16x16x32_bf16 v[140:143], v[40:43], v[204:207], v[140:143]\n"
        "v_mul_f32 v179, s88, v179\n"
        "v_mul_f32 v180, s88, v180\n"
        "v_mfma_f32_16x16x32_bf16 v[140:143], v[44:47], v[220:223], v[140:143]\n"
        "global_load_lds_dwordx4 v244, s[86:87] offset:2048\n"
        "v_mfma_f32_16x16x32_bf16 v[136:139], v[36:39], v[220:223], v[136:139]\n"
        "s_waitcnt vmcnt(12) lgkmcnt(0)\n"
        "v_mul_f32 v181, s88, v181\n"
        "v_mul_f32 v182, s88, v182\n"
        "v_mfma_f32_16x16x32_bf16 v[136:139], v[32:35], v[204:207], v[136:139]\n"
        "v_accvgpr_read_b32 v184, a40\n"
        "v_mul_f32 v184, s88, v184\n"
        "v_mfma_f32_16x16x32_bf16 v[16:19], v[48:51], v[192:195], v[16:19]\n"
        "v_cvt_pk_bf16_f32 v184, v184, v185\n"
        "v_cvt_pk_bf16_f32 v185, v186, v187\n"
        "v_mfma_f32_16x16x32_bf16 v[16:19], v[52:55], v[208:211], v[16:19]\n"
        "v_cvt_pk_bf16_f32 v186, v188, v189\n"
        "v_cvt_pk_bf16_f32 v187, v190, v191\n"
        "v_mfma_f32_16x16x32_bf16 v[20:23], v[60:63], v[208:211], v[20:23]\n"
        "global_load_lds_dwordx4 v245, s[86:87] offset:3072\n"
        "s_add_u32 m0, s73, 0x20000\n"
        "v_mfma_f32_16x16x32_bf16 v[20:23], v[56:59], v[192:195], v[20:23]\n"
        "s_add_u32 s86, s68, 0x300000\n"
        "s_addc_u32 s87, s69, 0\n"
        "v_accvgpr_write_b32 a164, v184\n"
        "v_mfma_f32_16x16x32_bf16 v[84:87], v[56:59], v[196:199], v[84:87]\n"
        "v_accvgpr_write_b32 a165, v185\n"
        "v_accvgpr_write_b32 a166, v186\n"
        "v_mfma_f32_16x16x32_bf16 v[84:87], v[60:63], v[212:215], v[84:87]\n"
        "v_accvgpr_write_b32 a167, v187\n"
        "v_mul_f32 v183, s88, v183\n"
        "v_mfma_f32_16x16x32_bf16 v[80:83], v[52:55], v[212:215], v[80:83]\n"
        "s_barrier\n"
        "v_mfma_f32_16x16x32_bf16 v[80:83], v[48:51], v[196:199], v[80:83]\n"
        "global_load_lds_dwordx4 v238, s[86:87] offset:0\n"
        "v_mfma_f32_16x16x32_bf16 v[112:115], v[48:51], v[200:203], v[112:115]\n"
        "ds_read_b64_tr_b16 v[32:33], v226 offset:49152\n"
        "v_accvgpr_read_b32 v185, a73\n"
        "v_mfma_f32_16x16x32_bf16 v[112:115], v[52:55], v[216:219], v[112:115]\n"
        "ds_read_b64_tr_b16 v[34:35], v227 offset:49152\n"
        "v_accvgpr_read_b32 v186, a74\n"
        "v_mfma_f32_16x16x32_bf16 v[116:119], v[60:63], v[216:219], v[116:119]\n"
        "ds_read_b64_tr_b16 v[36:37], v226 offset:57344\n"
        "v_accvgpr_read_b32 v187, a75\n"
        "v_mfma_f32_16x16x32_bf16 v[116:119], v[56:59], v[200:203], v[116:119]\n"
        "ds_read_b64_tr_b16 v[38:39], v227 offset:57344\n"
        "v_accvgpr_read_b32 v188, a76\n"
        "v_mfma_f32_16x16x32_bf16 v[148:151], v[56:59], v[204:207], v[148:151]\n"
        "ds_read_b64_tr_b16 v[44:45], v228 offset:57344\n"
        "global_load_lds_dwordx4 v239, s[86:87] offset:1024\n"
        "v_mfma_f32_16x16x32_bf16 v[148:151], v[60:63], v[220:223], v[148:151]\n"
        "ds_read_b64_tr_b16 v[46:47], v229 offset:57344\n"
        "v_accvgpr_read_b32 v189, a77\n"
        "v_mfma_f32_16x16x32_bf16 v[144:147], v[52:55], v[220:223], v[144:147]\n"
        "ds_read_b64_tr_b16 v[40:41], v228 offset:49152\n"
        "v_accvgpr_read_b32 v190, a78\n"
        "v_mfma_f32_16x16x32_bf16 v[144:147], v[48:51], v[204:207], v[144:147]\n"
        "ds_read_b64_tr_b16 v[42:43], v229 offset:49152\n"
        "v_accvgpr_read_b32 v191, a79\n"
        "v_mfma_f32_16x16x32_bf16 v[24:27], v[160:163], v[192:195], v[24:27]\n"
        "ds_read_b64_tr_b16 v[48:49], v230 offset:49152\n"
        "v_mul_f32 v185, s88, v185\n"
        "v_mfma_f32_16x16x32_bf16 v[24:27], v[164:167], v[208:211], v[24:27]\n"
        "global_load_lds_dwordx4 v240, s[86:87] offset:2048\n"
        "v_mfma_f32_16x16x32_bf16 v[28:31], v[172:175], v[208:211], v[28:31]\n"
        "ds_read_b64_tr_b16 v[50:51], v231 offset:49152\n"
        "v_mul_f32 v186, s88, v186\n"
        "v_mfma_f32_16x16x32_bf16 v[28:31], v[168:171], v[192:195], v[28:31]\n"
        "ds_read_b64_tr_b16 v[52:53], v230 offset:57344\n"
        "v_mul_f32 v187, s88, v187\n"
        "v_mfma_f32_16x16x32_bf16 v[92:95], v[168:171], v[196:199], v[92:95]\n"
        "ds_read_b64_tr_b16 v[54:55], v231 offset:57344\n"
        "v_mul_f32 v188, s88, v188\n"
        "v_mfma_f32_16x16x32_bf16 v[92:95], v[172:175], v[212:215], v[92:95]\n"
        "ds_read_b64_tr_b16 v[60:61], v232 offset:57344\n"
        "v_mul_f32 v189, s88, v189\n"
        "v_mfma_f32_16x16x32_bf16 v[88:91], v[164:167], v[212:215], v[88:91]\n"
        "global_load_lds_dwordx4 v241, s[86:87] offset:3072\n"
        "s_add_u32 m0, s72, 0x0\n"
        "v_mfma_f32_16x16x32_bf16 v[88:91], v[160:163], v[196:199], v[88:91]\n"
        "ds_read_b64_tr_b16 v[62:63], v233 offset:57344\n"
        "s_add_u32 s86, s64, 0x300\n"
        "s_addc_u32 s87, s65, 0\n"
        "v_mfma_f32_16x16x32_bf16 v[120:123], v[160:163], v[200:203], v[120:123]\n"
        "ds_read_b64_tr_b16 v[56:57], v232 offset:49152\n"
        "v_accvgpr_read_b32 v176, a64\n"
        "v_mfma_f32_16x16x32_bf16 v[120:123], v[164:167], v[216:219], v[120:123]\n"
        "ds_read_b64_tr_b16 v[58:59], v233 offset:49152\n"
        "v_mul_f32 v176, s88, v176\n"
        "v_mfma_f32_16x16x32_bf16 v[124:127], v[172:175], v[216:219], v[124:127]\n"
        "ds_read_b128 v[192:195], v248 offset:2048\n"
        "v_cvt_pk_bf16_f32 v176, v176, v177\n"
        "v_mfma_f32_16x16x32_bf16 v[124:127], v[168:171], v[200:203], v[124:127]\n"
        "ds_read_b128 v[208:211], v249 offset:2048\n"
        "global_load_lds_dwordx4 v242, s[86:87] offset:0\n"
        "v_mfma_f32_16x16x32_bf16 v[156:159], v[168:171], v[204:207], v[156:159]\n"
        "ds_read_b128 v[196:199], v248 offset:0\n"
        "v_cvt_pk_bf16_f32 v177, v178, v179\n"
        "v_mfma_f32_16x16x32_bf16 v[156:159], v[172:175], v[220:223], v[156:159]\n"
        "ds_read_b128 v[212:215], v249 offset:0\n"
        "v_cvt_pk_bf16_f32 v178, v180, v181\n"
        "v_mfma_f32_16x16x32_bf16 v[152:155], v[164:167], v[220:223], v[152:155]\n"
        "ds_read_b128 v[200:203], v248 offset:6144\n"
        "v_cvt_pk_bf16_f32 v179, v182, v183\n"
        "v_mfma_f32_16x16x32_bf16 v[152:155], v[160:163], v[204:207], v[152:155]\n"
        "global_load_lds_dwordx4 v243, s[86:87] offset:1024\n"
        "ds_read_b128 v[216:219], v249 offset:6144\n"
        "ds_read_b128 v[204:207], v248 offset:4096\n"
        "v_accvgpr_write_b32 a168, v176\n"
        "s_waitcnt lgkmcnt(6)\n"
        "v_mfma_f32_16x16x32_bf16 v[0:3], v[32:35], v[192:195], v[0:3]\n"
        "v_accvgpr_write_b32 a169, v177\n"
        "s_waitcnt lgkmcnt(5)\n"
        "v_mfma_f32_16x16x32_bf16 v[0:3], v[36:39], v[208:211], v[0:3]\n"
        "ds_read_b128 v[220:223], v249 offset:4096\n"
        "v_accvgpr_write_b32 a170, v178\n"
        "v_mfma_f32_16x16x32_bf16 v[4:7], v[44:47], v[208:211], v[4:7]\n"
        "s_waitcnt lgkmcnt(5)\n"
        "v_accvgpr_write_b32 a171, v179\n"
        "v_mul_f32 v190, s88, v190\n"
        "v_mfma_f32_16x16x32_bf16 v[4:7], v[40:43], v[192:195], v[4:7]\n"
        "global_store_dwordx4 v254, a[236:239], s[70:71] offset:1024 sc1\n"
        "ds_read_b64_tr_b16 v[160:161], v226 offset:50176\n"
        "ds_read_b64_tr_b16 v[162:163], v227 offset:50176\n"
        "v_mul_f32 v191, s88, v191\n"
        "v_mfma_f32_16x16x32_bf16 v[68:71], v[40:43], v[196:199], v[68:71]\n"
        "ds_read_b64_tr_b16 v[164:165], v226 offset:58368\n"
        "s_waitcnt lgkmcnt(6)\n"
        "v_mfma_f32_16x16x32_bf16 v[68:71], v[44:47], v[212:215], v[68:71]\n"
        "ds_read_b64_tr_b16 v[166:167], v227 offset:58368\n"
        "v_accvgpr_read_b32 v177, a97\n"
        "v_mfma_f32_16x16x32_bf16 v[64:67], v[36:39], v[212:215], v[64:67]\n"
        "ds_read_b64_tr_b16 v[172:173], v228 offset:58368\n"
        "v_accvgpr_read_b32 v178, a98\n"
        "v_mfma_f32_16x16x32_bf16 v[64:67], v[32:35], v[196:199], v[64:67]\n"
        "global_load_lds_dwordx4 v244, s[86:87] offset:2048\n"
        "v_mfma_f32_16x16x32_bf16 v[96:99], v[32:35], v[200:203], v[96:99]\n"
        "ds_read_b64_tr_b16 v[174:175], v229 offset:58368\n"
        "s_waitcnt lgkmcnt(6)\n"
        "v_mfma_f32_16x16x32_bf16 v[96:99], v[36:39], v[216:219], v[96:99]\n"
        "ds_read_b64_tr_b16 v[168:169], v228 offset:50176\n"
        "v_accvgpr_read_b32 v179, a99\n"
        "v_mfma_f32_16x16x32_bf16 v[100:103], v[44:47], v[216:219], v[100:103]\n"
        "ds_read_b64_tr_b16 v[170:171], v229 offset:50176\n"
        "v_accvgpr_read_b32 v180, a100\n"
        "v_mfma_f32_16x16x32_bf16 v[100:103], v[40:43], v[200:203], v[100:103]\n"
        "s_add_u32 s70, s38, s96\n"
        "s_addc_u32 s71, s39, 0\n"
        "v_accvgpr_read_b32 v181, a101\n"
        "v_mfma_f32_16x16x32_bf16 v[132:135], v[40:43], v[204:207], v[132:135]\n"
        "global_load_lds_dwordx4 v245, s[86:87] offset:3072\n"
        "s_add_u32 m0, s72, 0x1000\n"
        "v_mfma_f32_16x16x32_bf16 v[132:135], v[44:47], v[220:223], v[132:135]\n"
        "s_add_u32 s86, s66, 0x300\n"
        "s_addc_u32 s87, s67, 0\n"
        "v_accvgpr_read_b32 v182, a102\n"
        "v_mfma_f32_16x16x32_bf16 v[128:131], v[36:39], v[220:223], v[128:131]\n"
        "ds_read_b64_tr_b16 v[36:37], v230 offset:58368\n"
        "s_add_u32 s96, s52, 0x0\n"
        "v_accvgpr_read_b32 v183, a103\n"
        "v_mfma_f32_16x16x32_bf16 v[128:131], v[32:35], v[204:207], v[128:131]\n"
        "ds_read_b64_tr_b16 v[32:33], v230 offset:50176\n"
        "s_add_u32 s96, s96, s75\n"
        "v_accvgpr_read_b32 v184, a72\n"
        "v_mfma_f32_16x16x32_bf16 v[8:11], v[48:51], v[192:195], v[8:11]\n"
        "ds_read_b64_tr_b16 v[34:35], v231 offset:50176\n"
        "v_mul_f32 v184, s88, v184\n"
        "v_mfma_f32_16x16x32_bf16 v[8:11], v[52:55], v[208:211], v[8:11]\n"
        "global_load_lds_dwordx4 v242, s[86:87] offset:0\n"
        "v_mfma_f32_16x16x32_bf16 v[12:15], v[60:63], v[208:211], v[12:15]\n"
        "ds_read_b64_tr_b16 v[38:39], v231 offset:58368\n"
        "v_cvt_pk_bf16_f32 v184, v184, v185\n"
        "v_mfma_f32_16x16x32_bf16 v[12:15], v[56:59], v[192:195], v[12:15]\n"
        "ds_read_b64_tr_b16 v[44:45], v232 offset:58368\n"
        "v_cvt_pk_bf16_f32 v185, v186, v187\n"
        "v_mfma_f32_16x16x32_bf16 v[76:79], v[56:59], v[196:199], v[76:79]\n"
        "ds_read_b64_tr_b16 v[46:47], v233 offset:58368\n"
        "v_cvt_pk_bf16_f32 v186, v188, v189\n"
        "v_mfma_f32_16x16x32_bf16 v[76:79], v[60:63], v[212:215], v[76:79]\n"
        "ds_read_b64_tr_b16 v[40:41], v232 offset:50176\n"
        "v_cvt_pk_bf16_f32 v187, v190, v191\n"
        "v_mfma_f32_16x16x32_bf16 v[72:75], v[52:55], v[212:215], v[72:75]\n"
        "global_load_lds_dwordx4 v243, s[86:87] offset:1024\n"
        "v_mfma_f32_16x16x32_bf16 v[72:75], v[48:51], v[196:199], v[72:75]\n"
        "ds_read_b64_tr_b16 v[42:43], v233 offset:50176\n"
        "v_accvgpr_write_b32 a172, v184\n"
        "v_mfma_f32_16x16x32_bf16 v[104:107], v[48:51], v[200:203], v[104:107]\n"
        "v_accvgpr_write_b32 a173, v185\n"
        "v_accvgpr_write_b32 a174, v186\n"
        "v_mfma_f32_16x16x32_bf16 v[104:107], v[52:55], v[216:219], v[104:107]\n"
        "v_accvgpr_write_b32 a175, v187\n"
        "v_mul_f32 v177, s88, v177\n"
        "v_mfma_f32_16x16x32_bf16 v[108:111], v[60:63], v[216:219], v[108:111]\n"
        "v_mul_f32 v178, s88, v178\n"
        "v_mul_f32 v179, s88, v179\n"
        "v_mfma_f32_16x16x32_bf16 v[108:111], v[56:59], v[200:203], v[108:111]\n"
        "global_load_lds_dwordx4 v244, s[86:87] offset:2048\n"
        "v_mfma_f32_16x16x32_bf16 v[140:143], v[56:59], v[204:207], v[140:143]\n"
        "s_waitcnt lgkmcnt(2)\n"
        "v_mul_f32 v180, s88, v180\n"
        "v_mul_f32 v181, s88, v181\n"
        "v_mfma_f32_16x16x32_bf16 v[140:143], v[60:63], v[220:223], v[140:143]\n"
        "v_mul_f32 v182, s88, v182\n"
        "v_mul_f32 v183, s88, v183\n"
        "v_mfma_f32_16x16x32_bf16 v[136:139], v[52:55], v[220:223], v[136:139]\n"
        "v_accvgpr_read_b32 v185, a105\n"
        "v_accvgpr_read_b32 v186, a106\n"
        "v_mfma_f32_16x16x32_bf16 v[136:139], v[48:51], v[204:207], v[136:139]\n"
        "s_waitcnt vmcnt(12) lgkmcnt(0)\n"
        "v_accvgpr_read_b32 v187, a107\n"
        "v_accvgpr_read_b32 v188, a108\n"
        "v_mfma_f32_16x16x32_bf16 v[16:19], v[160:163], v[192:195], v[16:19]\n"
        "global_load_lds_dwordx4 v245, s[86:87] offset:3072\n"
        "s_add_u32 m0, s73, 0x24000\n"
        "v_mfma_f32_16x16x32_bf16 v[16:19], v[164:167], v[208:211], v[16:19]\n"
        "s_barrier\n"
        "v_mfma_f32_16x16x32_bf16 v[20:23], v[172:175], v[208:211], v[20:23]\n"
        "s_add_u32 s86, s68, 0x380000\n"
        "s_addc_u32 s87, s69, 0\n"
        "v_accvgpr_read_b32 v189, a109\n"
        "v_mfma_f32_16x16x32_bf16 v[20:23], v[168:171], v[192:195], v[20:23]\n"
        "ds_read_b64_tr_b16 v[48:49], v226 offset:16384\n"
        "v_accvgpr_read_b32 v190, a110\n"
        "v_mfma_f32_16x16x32_bf16 v[84:87], v[168:171], v[196:199], v[84:87]\n"
        "ds_read_b64_tr_b16 v[50:51], v227 offset:16384\n"
        "v_accvgpr_read_b32 v191, a111\n"
        "v_mfma_f32_16x16x32_bf16 v[84:87], v[172:175], v[212:215], v[84:87]\n"
        "global_load_lds_dwordx4 v238, s[86:87] offset:0\n"
        "v_mfma_f32_16x16x32_bf16 v[80:83], v[164:167], v[212:215], v[80:83]\n"
        "ds_read_b64_tr_b16 v[52:53], v226 offset:24576\n"
        "v_mul_f32 v185, s88, v185\n"
        "v_mfma_f32_16x16x32_bf16 v[80:83], v[160:163], v[196:199], v[80:83]\n"
        "ds_read_b64_tr_b16 v[54:55], v227 offset:24576\n"
        "v_accvgpr_read_b32 v176, a96\n"
        "v_mfma_f32_16x16x32_bf16 v[112:115], v[160:163], v[200:203], v[112:115]\n"
        "ds_read_b64_tr_b16 v[60:61], v228 offset:24576\n"
        "v_mul_f32 v176, s88, v176\n"
        "v_mfma_f32_16x16x32_bf16 v[112:115], v[164:167], v[216:219], v[112:115]\n"
        "ds_read_b64_tr_b16 v[62:63], v229 offset:24576\n"
        "v_cvt_pk_bf16_f32 v176, v176, v177\n"
        "v_mfma_f32_16x16x32_bf16 v[116:119], v[172:175], v[216:219], v[116:119]\n"
        "ds_read_b64_tr_b16 v[56:57], v228 offset:16384\n"
        "global_load_lds_dwordx4 v239, s[86:87] offset:1024\n"
        "v_mfma_f32_16x16x32_bf16 v[116:119], v[168:171], v[200:203], v[116:119]\n"
        "ds_read_b64_tr_b16 v[58:59], v229 offset:16384\n"
        "v_cvt_pk_bf16_f32 v177, v178, v179\n"
        "v_mfma_f32_16x16x32_bf16 v[148:151], v[168:171], v[204:207], v[148:151]\n"
        "ds_read_b64_tr_b16 v[168:169], v232 offset:16384\n"
        "v_cvt_pk_bf16_f32 v178, v180, v181\n"
        "v_mfma_f32_16x16x32_bf16 v[148:151], v[172:175], v[220:223], v[148:151]\n"
        "ds_read_b64_tr_b16 v[172:173], v232 offset:24576\n"
        "v_cvt_pk_bf16_f32 v179, v182, v183\n"
        "v_mfma_f32_16x16x32_bf16 v[144:147], v[164:167], v[220:223], v[144:147]\n"
        "ds_read_b64_tr_b16 v[164:165], v230 offset:24576\n"
        "v_accvgpr_write_b32 a0, v176\n"
        "v_mfma_f32_16x16x32_bf16 v[144:147], v[160:163], v[204:207], v[144:147]\n"
        "ds_read_b64_tr_b16 v[160:161], v230 offset:16384\n"
        "v_accvgpr_write_b32 a1, v177\n"
        "v_mfma_f32_16x16x32_bf16 v[24:27], v[32:35], v[192:195], v[24:27]\n"
        "global_load_lds_dwordx4 v240, s[86:87] offset:2048\n"
        "v_accvgpr_write_b32 a2, v178\n"
        "v_mfma_f32_16x16x32_bf16 v[24:27], v[36:39], v[208:211], v[24:27]\n"
        "ds_read_b64_tr_b16 v[162:163], v231 offset:16384\n"
        "v_accvgpr_write_b32 a3, v179\n"
        "v_mfma_f32_16x16x32_bf16 v[28:31], v[44:47], v[208:211], v[28:31]\n"
        "ds_read_b64_tr_b16 v[166:167], v231 offset:24576\n"
        "v_mul_f32 v186, s88, v186\n"
        "v_mfma_f32_16x16x32_bf16 v[28:31], v[40:43], v[192:195], v[28:31]\n"
        "ds_read_b64_tr_b16 v[174:175], v233 offset:24576\n"
        "v_mul_f32 v187, s88, v187\n"
        "v_mfma_f32_16x16x32_bf16 v[92:95], v[40:43], v[196:199], v[92:95]\n"
        "ds_read_b64_tr_b16 v[170:171], v233 offset:16384\n"
        "v_mul_f32 v188, s88, v188\n"
        "v_mfma_f32_16x16x32_bf16 v[92:95], v[44:47], v[212:215], v[92:95]\n"
        "global_load_lds_dwordx4 v241, s[86:87] offset:3072\n"
        "ds_read_b128 v[192:195], v246 offset:34816\n"
        "s_add_u32 m0, s72, 0x10000\n"
        "v_mfma_f32_16x16x32_bf16 v[88:91], v[36:39], v[212:215], v[88:91]\n"
        "ds_read_b128 v[208:211], v247 offset:34816\n"
        "s_add_u32 s86, s64, 0x380\n"
        "s_addc_u32 s87, s65, 0\n"
        "v_mfma_f32_16x16x32_bf16 v[88:91], v[32:35], v[196:199], v[88:91]\n"
        "ds_read_b128 v[196:199], v246 offset:32768\n"
        "v_mul_f32 v189, s88, v189\n"
        "v_mfma_f32_16x16x32_bf16 v[120:123], v[32:35], v[200:203], v[120:123]\n"
        "ds_read_b128 v[212:215], v247 offset:32768\n"
        "v_mul_f32 v190, s88, v190\n"
        "v_mfma_f32_16x16x32_bf16 v[120:123], v[36:39], v[216:219], v[120:123]\n"
        "v_mul_f32 v191, s88, v191\n"
        "v_accvgpr_read_b32 v177, a129\n"
        "v_mfma_f32_16x16x32_bf16 v[124:127], v[44:47], v[216:219], v[124:127]\n"
        "global_load_lds_dwordx4 v242, s[86:87] offset:0\n"
        "ds_read_b128 v[216:219], v247 offset:38912\n"
        "v_mfma_f32_16x16x32_bf16 v[124:127], v[40:43], v[200:203], v[124:127]\n"
        "ds_read_b128 v[200:203], v246 offset:38912\n"
        "v_accvgpr_read_b32 v178, a130\n"
        "v_mfma_f32_16x16x32_bf16 v[156:159], v[40:43], v[204:207], v[156:159]\n"
        "ds_read_b64_tr_b16 v[40:41], v228 offset:17408\n"
        "v_accvgpr_read_b32 v179, a131\n"
        "v_mfma_f32_16x16x32_bf16 v[156:159], v[44:47], v[220:223], v[156:159]\n"
        "ds_read_b64_tr_b16 v[44:45], v228 offset:25600\n"
        "v_accvgpr_read_b32 v180, a132\n"
        "v_mfma_f32_16x16x32_bf16 v[152:155], v[36:39], v[220:223], v[152:155]\n"
        "ds_read_b128 v[220:223], v247 offset:36864\n"
        "v_accvgpr_read_b32 v181, a133\n"
        "v_mfma_f32_16x16x32_bf16 v[152:155], v[32:35], v[204:207], v[152:155]\n"
        "global_load_lds_dwordx4 v243, s[86:87] offset:1024\n"
        "s_waitcnt lgkmcnt(6)\n"
        "v_mfma_f32_16x16x32_bf16 v[0:3], v[48:51], v[192:195], v[0:3]\n"
        "ds_read_b128 v[204:207], v246 offset:36864\n"
        "v_accvgpr_read_b32 v184, a104\n"
        "v_mfma_f32_16x16x32_bf16 v[0:3], v[52:55], v[208:211], v[0:3]\n"
        "s_waitcnt lgkmcnt(6)\n"
        "v_mul_f32 v184, s88, v184\n"
        "v_cvt_pk_bf16_f32 v184, v184, v185\n"
        "v_mfma_f32_16x16x32_bf16 v[4:7], v[60:63], v[208:211], v[4:7]\n"
        "ds_read_b64_tr_b16 v[32:33], v226 offset:17408\n"
        "v_cvt_pk_bf16_f32 v185, v186, v187\n"
        "v_mfma_f32_16x16x32_bf16 v[4:7], v[56:59], v[192:195], v[4:7]\n"
        "ds_read_b64_tr_b16 v[34:35], v227 offset:17408\n"
        "v_cvt_pk_bf16_f32 v186, v188, v189\n"
        "v_mfma_f32_16x16x32_bf16 v[68:71], v[56:59], v[196:199], v[68:71]\n"
        "global_load_lds_dwordx4 v244, s[86:87] offset:2048\n"
        "v_cvt_pk_bf16_f32 v187, v190, v191\n"
        "v_mfma_f32_16x16x32_bf16 v[68:71], v[60:63], v[212:215], v[68:71]\n"
        "s_waitcnt lgkmcnt(4)\n"
        "v_accvgpr_write_b32 a4, v184\n"
        "v_accvgpr_write_b32 a5, v185\n"
        "v_mfma_f32_16x16x32_bf16 v[64:67], v[52:55], v[212:215], v[64:67]\n"
        "ds_read_b64_tr_b16 v[36:37], v226 offset:25600\n"
        "v_accvgpr_write_b32 a6, v186\n"
        "v_mfma_f32_16x16x32_bf16 v[64:67], v[48:51], v[196:199], v[64:67]\n"
        "ds_read_b64_tr_b16 v[38:39], v227 offset:25600\n"
        "v_accvgpr_write_b32 a7, v187\n"
        "v_mfma_f32_16x16x32_bf16 v[96:99], v[48:51], v[200:203], v[96:99]\n"
        "ds_read_b64_tr_b16 v[46:47], v229 offset:25600\n"
        "v_accvgpr_read_b32 v182, a134\n"
        "v_mfma_f32_16x16x32_bf16 v[96:99], v[52:55], v[216:219], v[96:99]\n"
        "ds_read_b64_tr_b16 v[42:43], v229 offset:17408\n"
        "global_load_lds_dwordx4 v245, s[86:87] offset:3072\n"
        "s_add_u32 m0, s72, 0x11000\n"
        "v_mfma_f32_16x16x32_bf16 v[100:103], v[60:63], v[216:219], v[100:103]\n"
        "s_add_u32 s86, s66, 0x380\n"
        "s_addc_u32 s87, s67, 0\n"
        "v_accvgpr_read_b32 v183, a135\n"
        "v_mfma_f32_16x16x32_bf16 v[100:103], v[56:59], v[200:203], v[100:103]\n"
        "s_waitcnt lgkmcnt(4)\n"
        "v_mul_f32 v177, s88, v177\n"
        "v_mul_f32 v178, s88, v178\n"
        "v_mfma_f32_16x16x32_bf16 v[132:135], v[56:59], v[204:207], v[132:135]\n"
        "ds_read_b64_tr_b16 v[56:57], v232 offset:17408\n"
        "v_mul_f32 v179, s88, v179\n"
        "v_mfma_f32_16x16x32_bf16 v[132:135], v[60:63], v[220:223], v[132:135]\n"
        "ds_read_b64_tr_b16 v[60:61], v232 offset:25600\n"
        "v_mul_f32 v180, s88, v180\n"
        "v_mfma_f32_16x16x32_bf16 v[128:131], v[52:55], v[220:223], v[128:131]\n"
        "global_store_dwordx4 v254, a[192:195], s[70:71] offset:0 sc1\n"
        "ds_read_b64_tr_b16 v[52:53], v230 offset:25600\n"
        "ds_read_b64_tr_b16 v[54:55], v231 offset:25600\n"
        "v_mul_f32 v181, s88, v181\n"
        "v_mfma_f32_16x16x32_bf16 v[128:131], v[48:51], v[204:207], v[128:131]\n"
        "ds_read_b64_tr_b16 v[48:49], v230 offset:17408\n"
        "v_mul_f32 v182, s88, v182\n"
        "v_mfma_f32_16x16x32_bf16 v[8:11], v[160:163], v[192:195], v[8:11]\n"
        "ds_read_b64_tr_b16 v[50:51], v231 offset:17408\n"
        "v_mul_f32 v183, s88, v183\n"
        "v_mfma_f32_16x16x32_bf16 v[8:11], v[164:167], v[208:211], v[8:11]\n"
        "ds_read_b64_tr_b16 v[62:63], v233 offset:25600\n"
        "v_accvgpr_read_b32 v185, a137\n"
        "v_mfma_f32_16x16x32_bf16 v[12:15], v[172:175], v[208:211], v[12:15]\n"
        "global_load_lds_dwordx4 v242, s[86:87] offset:0\n"
        "v_mfma_f32_16x16x32_bf16 v[12:15], v[168:171], v[192:195], v[12:15]\n"
        "ds_read_b64_tr_b16 v[58:59], v233 offset:17408\n"
        "v_accvgpr_read_b32 v186, a138\n"
        "v_mfma_f32_16x16x32_bf16 v[76:79], v[168:171], v[196:199], v[76:79]\n"
        "v_accvgpr_read_b32 v187, a139\n"
        "v_accvgpr_read_b32 v188, a140\n"
        "v_mfma_f32_16x16x32_bf16 v[76:79], v[172:175], v[212:215], v[76:79]\n"
        "v_accvgpr_read_b32 v189, a141\n"
        "v_accvgpr_read_b32 v190, a142\n"
        "v_mfma_f32_16x16x32_bf16 v[72:75], v[164:167], v[212:215], v[72:75]\n"
        "v_accvgpr_read_b32 v191, a143\n"
        "v_mul_f32 v185, s88, v185\n"
        "v_mfma_f32_16x16x32_bf16 v[72:75], v[160:163], v[196:199], v[72:75]\n"
        "global_load_lds_dwordx4 v243, s[86:87] offset:1024\n"
        "v_mfma_f32_16x16x32_bf16 v[104:107], v[160:163], v[200:203], v[104:107]\n"
        "v_accvgpr_read_b32 v176, a128\n"
        "v_mul_f32 v176, s88, v176\n"
        "v_mfma_f32_16x16x32_bf16 v[104:107], v[164:167], v[216:219], v[104:107]\n"
        "v_cvt_pk_bf16_f32 v176, v176, v177\n"
        "v_cvt_pk_bf16_f32 v177, v178, v179\n"
        "v_mfma_f32_16x16x32_bf16 v[108:111], v[172:175], v[216:219], v[108:111]\n"
        "v_cvt_pk_bf16_f32 v178, v180, v181\n"
        "v_cvt_pk_bf16_f32 v179, v182, v183\n"
        "v_mfma_f32_16x16x32_bf16 v[108:111], v[168:171], v[200:203], v[108:111]\n"
        "s_waitcnt vmcnt(11) lgkmcnt(0)\n"
        "v_accvgpr_write_b32 a8, v176\n"
        "v_accvgpr_write_b32 a9, v177\n"
        "v_mfma_f32_16x16x32_bf16 v[140:143], v[168:171], v[204:207], v[140:143]\n"
        "global_load_lds_dwordx4 v244, s[86:87] offset:2048\n"
        "v_mfma_f32_16x16x32_bf16 v[140:143], v[172:175], v[220:223], v[140:143]\n"
        "v_accvgpr_write_b32 a10, v178\n"
        "v_accvgpr_write_b32 a11, v179\n"
        "v_mfma_f32_16x16x32_bf16 v[136:139], v[164:167], v[220:223], v[136:139]\n"
        "s_barrier\n"
        "v_mfma_f32_16x16x32_bf16 v[136:139], v[160:163], v[204:207], v[136:139]\n"
        "ds_read_b64_tr_b16 v[160:161], v226 offset:32768\n"
        "v_mul_f32 v186, s88, v186\n"
        "v_mfma_f32_16x16x32_bf16 v[16:19], v[32:35], v[192:195], v[16:19]\n"
        "ds_read_b64_tr_b16 v[162:163], v227 offset:32768\n"
        "v_mul_f32 v187, s88, v187\n"
        "v_mfma_f32_16x16x32_bf16 v[16:19], v[36:39], v[208:211], v[16:19]\n"
        "global_load_lds_dwordx4 v245, s[86:87] offset:3072\n"
        "s_add_u32 m0, s73, 0x1c000\n"
        "v_mfma_f32_16x16x32_bf16 v[20:23], v[44:47], v[208:211], v[20:23]\n"
        "s_add_u32 s86, s68, 0x400000\n"
        "s_addc_u32 s87, s69, 0\n"
        "v_mul_f32 v188, s88, v188\n"
        "v_mfma_f32_16x16x32_bf16 v[20:23], v[40:43], v[192:195], v[20:23]\n"
        "ds_read_b64_tr_b16 v[164:165], v226 offset:40960\n"
        "v_mul_f32 v189, s88, v189\n"
        "v_mfma_f32_16x16x32_bf16 v[84:87], v[40:43], v[196:199], v[84:87]\n"
        "ds_read_b64_tr_b16 v[166:167], v227 offset:40960\n"
        "v_mul_f32 v190, s88, v190\n"
        "v_mfma_f32_16x16x32_bf16 v[84:87], v[44:47], v[212:215], v[84:87]\n"
        "ds_read_b64_tr_b16 v[172:173], v228 offset:40960\n"
        "v_mul_f32 v191, s88, v191\n"
        "v_mfma_f32_16x16x32_bf16 v[80:83], v[36:39], v[212:215], v[80:83]\n"
        "global_load_lds_dwordx4 v238, s[86:87] offset:0\n"
        "v_mfma_f32_16x16x32_bf16 v[80:83], v[32:35], v[196:199], v[80:83]\n"
        "ds_read_b64_tr_b16 v[174:175], v229 offset:40960\n"
        "v_accvgpr_read_b32 v177, a209\n"
        "v_mfma_f32_16x16x32_bf16 v[112:115], v[32:35], v[200:203], v[112:115]\n"
        "ds_read_b64_tr_b16 v[168:169], v228 offset:32768\n"
        "v_accvgpr_read_b32 v178, a210\n"
        "v_mfma_f32_16x16x32_bf16 v[112:115], v[36:39], v[216:219], v[112:115]\n"
        "ds_read_b64_tr_b16 v[170:171], v229 offset:32768\n"
        "v_accvgpr_read_b32 v179, a211\n"
        "v_mfma_f32_16x16x32_bf16 v[116:119], v[44:47], v[216:219], v[116:119]\n"
        "v_accvgpr_read_b32 v180, a240\n"
        "v_accvgpr_read_b32 v181, a241\n"
        "v_mfma_f32_16x16x32_bf16 v[116:119], v[40:43], v[200:203], v[116:119]\n"
        "global_load_lds_dwordx4 v239, s[86:87] offset:1024\n"
        "v_mfma_f32_16x16x32_bf16 v[148:151], v[40:43], v[204:207], v[148:151]\n"
        "ds_read_b64_tr_b16 v[40:41], v232 offset:32768\n"
        "v_accvgpr_read_b32 v182, a242\n"
        "v_mfma_f32_16x16x32_bf16 v[148:151], v[44:47], v[220:223], v[148:151]\n"
        "ds_read_b64_tr_b16 v[44:45], v232 offset:40960\n"
        "v_accvgpr_read_b32 v183, a243\n"
        "v_mfma_f32_16x16x32_bf16 v[144:147], v[36:39], v[220:223], v[144:147]\n"
        "ds_read_b64_tr_b16 v[36:37], v230 offset:40960\n"
        "v_mfma_f32_16x16x32_bf16 v[144:147], v[32:35], v[204:207], v[144:147]\n"
        "ds_read_b64_tr_b16 v[32:33], v230 offset:32768\n"
        "v_accvgpr_read_b32 v184, a136\n"
        "v_mfma_f32_16x16x32_bf16 v[24:27], v[48:51], v[192:195], v[24:27]\n"
        "global_load_lds_dwordx4 v240, s[86:87] offset:2048\n"
        "v_mfma_f32_16x16x32_bf16 v[24:27], v[52:55], v[208:211], v[24:27]\n"
        "ds_read_b64_tr_b16 v[34:35], v231 offset:32768\n"
        "v_mul_f32 v184, s88, v184\n"
        "v_mfma_f32_16x16x32_bf16 v[28:31], v[60:63], v[208:211], v[28:31]\n"
        "ds_read_b64_tr_b16 v[38:39], v231 offset:40960\n"
        "v_cvt_pk_bf16_f32 v184, v184, v185\n"
        "v_mfma_f32_16x16x32_bf16 v[28:31], v[56:59], v[192:195], v[28:31]\n"
        "ds_read_b64_tr_b16 v[46:47], v233 offset:40960\n"
        "v_cvt_pk_bf16_f32 v185, v186, v187\n"
        "v_mfma_f32_16x16x32_bf16 v[92:95], v[56:59], v[196:199], v[92:95]\n"
        "ds_read_b64_tr_b16 v[42:43], v233 offset:32768\n"
        "v_cvt_pk_bf16_f32 v186, v188, v189\n"
        "v_mfma_f32_16x16x32_bf16 v[92:95], v[60:63], v[212:215], v[92:95]\n"
        "global_load_lds_dwordx4 v241, s[86:87] offset:3072\n"
        "s_add_u32 m0, s72, 0x8000\n"
        "v_mfma_f32_16x16x32_bf16 v[88:91], v[52:55], v[212:215], v[88:91]\n"
        "ds_read_b128 v[192:195], v246 offset:2048\n"
        "v_cvt_pk_bf16_f32 v187, v190, v191\n"
        "v_mfma_f32_16x16x32_bf16 v[88:91], v[48:51], v[196:199], v[88:91]\n"
        "ds_read_b128 v[208:211], v247 offset:2048\n"
        "v_accvgpr_write_b32 a12, v184\n"
        "v_mfma_f32_16x16x32_bf16 v[120:123], v[48:51], v[200:203], v[120:123]\n"
        "ds_read_b128 v[196:199], v246 offset:0\n"
        "v_accvgpr_write_b32 a13, v185\n"
        "v_mfma_f32_16x16x32_bf16 v[120:123], v[52:55], v[216:219], v[120:123]\n"
        "ds_read_b128 v[212:215], v247 offset:0\n"
        "v_accvgpr_write_b32 a14, v186\n"
        "v_mfma_f32_16x16x32_bf16 v[124:127], v[60:63], v[216:219], v[124:127]\n"
        "ds_read_b128 v[216:219], v247 offset:6144\n"
        "v_accvgpr_write_b32 a15, v187\n"
        "v_mfma_f32_16x16x32_bf16 v[124:127], v[56:59], v[200:203], v[124:127]\n"
        "s_add_u32 s86, s64, 0x400\n"
        "s_addc_u32 s87, s65, 0\n"
        "v_accvgpr_read_b32 v185, a213\n"
        "v_mfma_f32_16x16x32_bf16 v[156:159], v[56:59], v[204:207], v[156:159]\n"
        "global_load_lds_dwordx4 v242, s[86:87] offset:0\n"
        "ds_read_b128 v[200:203], v246 offset:6144\n"
        "v_mfma_f32_16x16x32_bf16 v[156:159], v[60:63], v[220:223], v[156:159]\n"
        "ds_read_b64_tr_b16 v[60:61], v228 offset:41984\n"
        "v_accvgpr_read_b32 v186, a214\n"
        "v_mfma_f32_16x16x32_bf16 v[152:155], v[52:55], v[220:223], v[152:155]\n"
        "ds_read_b128 v[220:223], v247 offset:4096\n"
        "v_accvgpr_read_b32 v187, a215\n"
        "v_mfma_f32_16x16x32_bf16 v[152:155], v[48:51], v[204:207], v[152:155]\n"
        "ds_read_b128 v[204:207], v246 offset:4096\n"
        "s_waitcnt lgkmcnt(7)\n"
        "v_mfma_f32_16x16x32_bf16 v[0:3], v[160:163], v[192:195], v[0:3]\n"
        "ds_read_b64_tr_b16 v[48:49], v226 offset:33792\n"
        "v_accvgpr_read_b32 v188, a244\n"
        "v_mfma_f32_16x16x32_bf16 v[0:3], v[164:167], v[208:211], v[0:3]\n"
        "global_load_lds_dwordx4 v243, s[86:87] offset:1024\n"
        "ds_read_b64_tr_b16 v[50:51], v227 offset:33792\n"
        "v_mfma_f32_16x16x32_bf16 v[4:7], v[172:175], v[208:211], v[4:7]\n"
        "ds_read_b64_tr_b16 v[52:53], v226 offset:41984\n"
        "v_accvgpr_read_b32 v189, a245\n"
        "v_mfma_f32_16x16x32_bf16 v[4:7], v[168:171], v[192:195], v[4:7]\n"
        "ds_read_b64_tr_b16 v[54:55], v227 offset:41984\n"
        "s_waitcnt lgkmcnt(8)\n"
        "v_mfma_f32_16x16x32_bf16 v[68:71], v[168:171], v[196:199], v[68:71]\n"
        "LHA6_%=:\n"
        "s_cbranch_execz LHA10_%=\n"
        "ds_read_b64_tr_b16 v[62:63], v229 offset:41984\n"
        "v_accvgpr_read_b32 v190, a246\n"
        "v_mfma_f32_16x16x32_bf16 v[68:71], v[172:175], v[212:215], v[68:71]\n"
        "ds_read_b64_tr_b16 v[56:57], v228 offset:33792\n"
        "v_accvgpr_read_b32 v191, a247\n"
        "v_mfma_f32_16x16x32_bf16 v[64:67], v[164:167], v[212:215], v[64:67]\n"
        "global_load_lds_dwordx4 v244, s[86:87] offset:2048\n"
        "ds_read_b64_tr_b16 v[58:59], v229 offset:33792\n"
        "v_mfma_f32_16x16x32_bf16 v[64:67], v[160:163], v[196:199], v[64:67]\n"
        "s_waitcnt lgkmcnt(7)\n"
        "v_accvgpr_read_b32 v176, a208\n"
        "v_cvt_pk_bf16_f32 v176, v176, v177\n"
        "v_mfma_f32_16x16x32_bf16 v[96:99], v[160:163], v[200:203], v[96:99]\n"
        "v_cvt_pk_bf16_f32 v177, v178, v179\n"
        "v_cvt_pk_bf16_f32 v178, v180, v181\n"
        "v_mfma_f32_16x16x32_bf16 v[96:99], v[164:167], v[216:219], v[96:99]\n"
        "v_cvt_pk_bf16_f32 v179, v182, v183\n"
        "v_accvgpr_write_b32 a208, v176\n"
        "v_mfma_f32_16x16x32_bf16 v[100:103], v[172:175], v[216:219], v[100:103]\n"
        "v_accvgpr_write_b32 a209, v177\n"
        "v_accvgpr_write_b32 a210, v178\n"
        "v_mfma_f32_16x16x32_bf16 v[100:103], v[168:171], v[200:203], v[100:103]\n"
        "global_load_lds_dwordx4 v245, s[86:87] offset:3072\n"
        "s_add_u32 m0, s72, 0x9000\n"
        "v_accvgpr_write_b32 a211, v179\n"
        "v_mfma_f32_16x16x32_bf16 v[132:135], v[168:171], v[204:207], v[132:135]\n"
        "s_add_u32 s86, s66, 0x400\n"
        "s_addc_u32 s87, s67, 0\n"
        "v_accvgpr_read_b32 v177, a217\n"
        "v_mfma_f32_16x16x32_bf16 v[132:135], v[172:175], v[220:223], v[132:135]\n"
        "ds_read_b64_tr_b16 v[172:173], v232 offset:41984\n"
        "v_accvgpr_read_b32 v178, a218\n"
        "v_mfma_f32_16x16x32_bf16 v[128:131], v[164:167], v[220:223], v[128:131]\n"
        "ds_read_b64_tr_b16 v[164:165], v230 offset:41984\n"
        "v_accvgpr_read_b32 v179, a219\n"
        "v_mfma_f32_16x16x32_bf16 v[128:131], v[160:163], v[204:207], v[128:131]\n"
        "global_store_dwordx4 v254, a[196:199], s[70:71] offset:1024 sc1\n"
        "ds_read_b64_tr_b16 v[160:161], v230 offset:33792\n"
        "ds_read_b64_tr_b16 v[162:163], v231 offset:33792\n"
        "v_accvgpr_read_b32 v180, a248\n"
        "v_mfma_f32_16x16x32_bf16 v[8:11], v[32:35], v[192:195], v[8:11]\n"
        "ds_read_b64_tr_b16 v[166:167], v231 offset:41984\n"
        "v_accvgpr_read_b32 v181, a249\n"
        "v_mfma_f32_16x16x32_bf16 v[8:11], v[36:39], v[208:211], v[8:11]\n"
        "ds_read_b64_tr_b16 v[174:175], v233 offset:41984\n"
        "v_accvgpr_read_b32 v182, a250\n"
        "v_mfma_f32_16x16x32_bf16 v[12:15], v[44:47], v[208:211], v[12:15]\n"
        "ds_read_b64_tr_b16 v[168:169], v232 offset:33792\n"
        "v_accvgpr_read_b32 v183, a251\n"
        "v_mfma_f32_16x16x32_bf16 v[12:15], v[40:43], v[192:195], v[12:15]\n"
        "global_load_lds_dwordx4 v242, s[86:87] offset:0\n"
        "v_mfma_f32_16x16x32_bf16 v[76:79], v[40:43], v[196:199], v[76:79]\n"
        "ds_read_b64_tr_b16 v[170:171], v233 offset:33792\n"
        "s_add_u32 s70, s38, s96\n"
        "s_addc_u32 s71, s39, 0\n"
        "v_mfma_f32_16x16x32_bf16 v[76:79], v[44:47], v[212:215], v[76:79]\n"
        "v_mfma_f32_16x16x32_bf16 v[72:75], v[36:39], v[212:215], v[72:75]\n"
        "v_mfma_f32_16x16x32_bf16 v[72:75], v[32:35], v[196:199], v[72:75]\n"
        "v_mfma_f32_16x16x32_bf16 v[104:107], v[32:35], v[200:203], v[104:107]\n"
        "global_load_lds_dwordx4 v243, s[86:87] offset:1024\n"
        "v_mfma_f32_16x16x32_bf16 v[104:107], v[36:39], v[216:219], v[104:107]\n"
        "v_mfma_f32_16x16x32_bf16 v[108:111], v[44:47], v[216:219], v[108:111]\n"
        "v_mfma_f32_16x16x32_bf16 v[108:111], v[40:43], v[200:203], v[108:111]\n"
        "v_mfma_f32_16x16x32_bf16 v[140:143], v[40:43], v[204:207], v[140:143]\n"
        "s_waitcnt vmcnt(11) lgkmcnt(0)\n"
        "v_mfma_f32_16x16x32_bf16 v[140:143], v[44:47], v[220:223], v[140:143]\n"
        "global_load_lds_dwordx4 v244, s[86:87] offset:2048\n"
        "s_add_u32 s96, s52, 0x1000\n"
        "v_mfma_f32_16x16x32_bf16 v[136:139], v[36:39], v[220:223], v[136:139]\n"
        "s_barrier\n"
        "v_mfma_f32_16x16x32_bf16 v[136:139], v[32:35], v[204:207], v[136:139]\n"
        "ds_read_b64_tr_b16 v[32:33], v226 offset:49152\n"
        "s_add_u32 s96, s96, s75\n"
        "v_accvgpr_read_b32 v184, a212\n"
        "v_mfma_f32_16x16x32_bf16 v[16:19], v[48:51], v[192:195], v[16:19]\n"
        "ds_read_b64_tr_b16 v[34:35], v227 offset:49152\n"
        "v_cvt_pk_bf16_f32 v184, v184, v185\n"
        "v_mfma_f32_16x16x32_bf16 v[16:19], v[52:55], v[208:211], v[16:19]\n"
        "ds_read_b64_tr_b16 v[36:37], v226 offset:57344\n"
        "v_cvt_pk_bf16_f32 v185, v186, v187\n"
        "v_mfma_f32_16x16x32_bf16 v[20:23], v[60:63], v[208:211], v[20:23]\n"
        "global_load_lds_dwordx4 v245, s[86:87] offset:3072\n"
        "s_add_u32 m0, s73, 0x20000\n"
        "v_mfma_f32_16x16x32_bf16 v[20:23], v[56:59], v[192:195], v[20:23]\n"
        "ds_read_b64_tr_b16 v[38:39], v227 offset:57344\n"
        "v_cvt_pk_bf16_f32 v186, v188, v189\n"
        "v_mfma_f32_16x16x32_bf16 v[84:87], v[56:59], v[196:199], v[84:87]\n"
        "ds_read_b64_tr_b16 v[44:45], v228 offset:57344\n"
        "v_cvt_pk_bf16_f32 v187, v190, v191\n"
        "v_mfma_f32_16x16x32_bf16 v[84:87], v[60:63], v[212:215], v[84:87]\n"
        "ds_read_b64_tr_b16 v[46:47], v229 offset:57344\n"
        "v_accvgpr_write_b32 a240, v184\n"
        "v_mfma_f32_16x16x32_bf16 v[80:83], v[52:55], v[212:215], v[80:83]\n"
        "ds_read_b64_tr_b16 v[40:41], v228 offset:49152\n"
        "v_accvgpr_write_b32 a241, v185\n"
        "v_mfma_f32_16x16x32_bf16 v[80:83], v[48:51], v[196:199], v[80:83]\n"
        "ds_read_b64_tr_b16 v[42:43], v229 offset:49152\n"
        "v_accvgpr_write_b32 a242, v186\n"
        "v_mfma_f32_16x16x32_bf16 v[112:115], v[48:51], v[200:203], v[112:115]\n"
        "s_add_u32 s86, s68, 0x480000\n"
        "s_addc_u32 s87, s69, 0\n"
        "v_accvgpr_write_b32 a243, v187\n"
        "v_mfma_f32_16x16x32_bf16 v[112:115], v[52:55], v[216:219], v[112:115]\n"
        "global_load_lds_dwordx4 v238, s[86:87] offset:0\n"
        "v_accvgpr_read_b32 v185, a221\n"
        "v_mfma_f32_16x16x32_bf16 v[116:119], v[60:63], v[216:219], v[116:119]\n"
        "v_accvgpr_read_b32 v186, a222\n"
        "v_accvgpr_read_b32 v187, a223\n"
        "v_mfma_f32_16x16x32_bf16 v[116:119], v[56:59], v[200:203], v[116:119]\n"
        "v_accvgpr_read_b32 v188, a252\n"
        "v_accvgpr_read_b32 v189, a253\n"
        "v_mfma_f32_16x16x32_bf16 v[148:151], v[56:59], v[204:207], v[148:151]\n"
        "ds_read_b64_tr_b16 v[56:57], v232 offset:49152\n"
        "v_accvgpr_read_b32 v190, a254\n"
        "v_mfma_f32_16x16x32_bf16 v[148:151], v[60:63], v[220:223], v[148:151]\n"
        "ds_read_b64_tr_b16 v[60:61], v232 offset:57344\n"
        "v_accvgpr_read_b32 v191, a255\n"
        "v_mfma_f32_16x16x32_bf16 v[144:147], v[52:55], v[220:223], v[144:147]\n"
        "global_load_lds_dwordx4 v239, s[86:87] offset:1024\n"
        "ds_read_b64_tr_b16 v[52:53], v230 offset:57344\n"
        "v_mfma_f32_16x16x32_bf16 v[144:147], v[48:51], v[204:207], v[144:147]\n"
        "ds_read_b64_tr_b16 v[48:49], v230 offset:49152\n"
        "v_mfma_f32_16x16x32_bf16 v[24:27], v[160:163], v[192:195], v[24:27]\n"
        "ds_read_b64_tr_b16 v[50:51], v231 offset:49152\n"
        "v_mfma_f32_16x16x32_bf16 v[24:27], v[164:167], v[208:211], v[24:27]\n"
        "ds_read_b64_tr_b16 v[54:55], v231 offset:57344\n"
        "v_mfma_f32_16x16x32_bf16 v[28:31], v[172:175], v[208:211], v[28:31]\n"
        "ds_read_b64_tr_b16 v[62:63], v233 offset:57344\n"
        "v_mfma_f32_16x16x32_bf16 v[28:31], v[168:171], v[192:195], v[28:31]\n"
        "global_load_lds_dwordx4 v240, s[86:87] offset:2048\n"
        "ds_read_b64_tr_b16 v[58:59], v233 offset:49152\n"
        "v_mfma_f32_16x16x32_bf16 v[92:95], v[168:171], v[196:199], v[92:95]\n"
        "ds_read_b128 v[192:195], v248 offset:2048\n"
        "v_mfma_f32_16x16x32_bf16 v[92:95], v[172:175], v[212:215], v[92:95]\n"
        "ds_read_b128 v[208:211], v249 offset:2048\n"
        "v_mfma_f32_16x16x32_bf16 v[88:91], v[164:167], v[212:215], v[88:91]\n"
        "ds_read_b128 v[212:215], v249 offset:0\n"
        "v_mfma_f32_16x16x32_bf16 v[88:91], v[160:163], v[196:199], v[88:91]\n"
        "ds_read_b128 v[196:199], v248 offset:0\n"
        "v_accvgpr_read_b32 v176, a216\n"
        "v_mfma_f32_16x16x32_bf16 v[120:123], v[160:163], v[200:203], v[120:123]\n"
        "global_load_lds_dwordx4 v241, s[86:87] offset:3072\n"
        "s_add_u32 m0, s72, 0x0\n"
        "v_cvt_pk_bf16_f32 v176, v176, v177\n"
        "v_mfma_f32_16x16x32_bf16 v[120:123], v[164:167], v[216:219], v[120:123]\n"
        "s_add_u32 s86, s64, 0x480\n"
        "s_addc_u32 s87, s65, 0\n"
        "v_cvt_pk_bf16_f32 v177, v178, v179\n"
        "v_mfma_f32_16x16x32_bf16 v[124:127], v[172:175], v[216:219], v[124:127]\n"
        "ds_read_b128 v[216:219], v249 offset:6144\n"
        "v_cvt_pk_bf16_f32 v178, v180, v181\n"
        "v_mfma_f32_16x16x32_bf16 v[124:127], v[168:171], v[200:203], v[124:127]\n"
        "ds_read_b128 v[200:203], v248 offset:6144\n"
        "v_cvt_pk_bf16_f32 v179, v182, v183\n"
        "v_mfma_f32_16x16x32_bf16 v[156:159], v[168:171], v[204:207], v[156:159]\n"
        "s_waitcnt lgkmcnt(5)\n"
        "v_accvgpr_write_b32 a212, v176\n"
        "v_accvgpr_write_b32 a213, v177\n"
        "v_mfma_f32_16x16x32_bf16 v[156:159], v[172:175], v[220:223], v[156:159]\n"
        "global_load_lds_dwordx4 v242, s[86:87] offset:0\n"
        "v_accvgpr_write_b32 a214, v178\n"
        "v_mfma_f32_16x16x32_bf16 v[152:155], v[164:167], v[220:223], v[152:155]\n"
        "s_waitcnt lgkmcnt(3)\n"
        "v_accvgpr_write_b32 a215, v179\n"
        "v_accvgpr_read_b32 v177, a145\n"
        "v_mfma_f32_16x16x32_bf16 v[152:155], v[160:163], v[204:207], v[152:155]\n"
        "ds_read_b128 v[204:207], v248 offset:4096\n"
        "v_accvgpr_read_b32 v178, a146\n"
        "v_mfma_f32_16x16x32_bf16 v[0:3], v[32:35], v[192:195], v[0:3]\n"
        "ds_read_b128 v[220:223], v249 offset:4096\n"
        "v_accvgpr_read_b32 v179, a147\n"
        "v_mfma_f32_16x16x32_bf16 v[0:3], v[36:39], v[208:211], v[0:3]\n"
        "ds_read_b64_tr_b16 v[160:161], v226 offset:50176\n"
        "v_accvgpr_read_b32 v180, a176\n"
        "v_mfma_f32_16x16x32_bf16 v[4:7], v[44:47], v[208:211], v[4:7]\n"
        "global_load_lds_dwordx4 v243, s[86:87] offset:1024\n"
        "ds_read_b64_tr_b16 v[162:163], v227 offset:50176\n"
        "v_mfma_f32_16x16x32_bf16 v[4:7], v[40:43], v[192:195], v[4:7]\n"
        "ds_read_b64_tr_b16 v[164:165], v226 offset:58368\n"
        "s_waitcnt lgkmcnt(5)\n"
        "v_mfma_f32_16x16x32_bf16 v[68:71], v[40:43], v[196:199], v[68:71]\n"
        "ds_read_b64_tr_b16 v[166:167], v227 offset:58368\n"
        "v_accvgpr_read_b32 v181, a177\n"
        "v_mfma_f32_16x16x32_bf16 v[68:71], v[44:47], v[212:215], v[68:71]\n"
        "ds_read_b64_tr_b16 v[172:173], v228 offset:58368\n"
        "v_accvgpr_read_b32 v182, a178\n"
        "v_mfma_f32_16x16x32_bf16 v[64:67], v[36:39], v[212:215], v[64:67]\n"
        "ds_read_b64_tr_b16 v[174:175], v229 offset:58368\n"
        "v_accvgpr_read_b32 v183, a179\n"
        "v_mfma_f32_16x16x32_bf16 v[64:67], v[32:35], v[196:199], v[64:67]\n"
        "global_load_lds_dwordx4 v244, s[86:87] offset:2048\n"
        "ds_read_b64_tr_b16 v[168:169], v228 offset:50176\n"
        "v_mfma_f32_16x16x32_bf16 v[96:99], v[32:35], v[200:203], v[96:99]\n"
        "ds_read_b64_tr_b16 v[170:171], v229 offset:50176\n"
        "s_waitcnt lgkmcnt(8)\n"
        "v_mfma_f32_16x16x32_bf16 v[96:99], v[36:39], v[216:219], v[96:99]\n"
        "v_mfma_f32_16x16x32_bf16 v[100:103], v[44:47], v[216:219], v[100:103]\n"
        "v_mfma_f32_16x16x32_bf16 v[100:103], v[40:43], v[200:203], v[100:103]\n"
        "v_mfma_f32_16x16x32_bf16 v[132:135], v[40:43], v[204:207], v[132:135]\n"
        "global_load_lds_dwordx4 v245, s[86:87] offset:3072\n"
        "s_add_u32 m0, s72, 0x1000\n"
        "v_mfma_f32_16x16x32_bf16 v[132:135], v[44:47], v[220:223], v[132:135]\n"
        "s_add_u32 s86, s66, 0x480\n"
        "s_addc_u32 s87, s67, 0\n"
        "v_mfma_f32_16x16x32_bf16 v[128:131], v[36:39], v[220:223], v[128:131]\n"
        "ds_read_b64_tr_b16 v[36:37], v230 offset:58368\n"
        "v_mfma_f32_16x16x32_bf16 v[128:131], v[32:35], v[204:207], v[128:131]\n"
        "ds_read_b64_tr_b16 v[32:33], v230 offset:50176\n"
        "v_accvgpr_read_b32 v184, a220\n"
        "v_mfma_f32_16x16x32_bf16 v[8:11], v[48:51], v[192:195], v[8:11]\n"
        "ds_read_b64_tr_b16 v[34:35], v231 offset:50176\n"
        "v_cvt_pk_bf16_f32 v184, v184, v185\n"
        "v_mfma_f32_16x16x32_bf16 v[8:11], v[52:55], v[208:211], v[8:11]\n"
        "global_load_lds_dwordx4 v242, s[86:87] offset:0\n"
        "v_cvt_pk_bf16_f32 v185, v186, v187\n"
        "v_mfma_f32_16x16x32_bf16 v[12:15], v[60:63], v[208:211], v[12:15]\n"
        "ds_read_b64_tr_b16 v[38:39], v231 offset:58368\n"
        "v_cvt_pk_bf16_f32 v186, v188, v189\n"
        "v_mfma_f32_16x16x32_bf16 v[12:15], v[56:59], v[192:195], v[12:15]\n"
        "ds_read_b64_tr_b16 v[44:45], v232 offset:58368\n"
        "v_cvt_pk_bf16_f32 v187, v190, v191\n"
        "v_mfma_f32_16x16x32_bf16 v[76:79], v[56:59], v[196:199], v[76:79]\n"
        "ds_read_b64_tr_b16 v[46:47], v233 offset:58368\n"
        "v_accvgpr_write_b32 a244, v184\n"
        "v_mfma_f32_16x16x32_bf16 v[76:79], v[60:63], v[212:215], v[76:79]\n"
        "ds_read_b64_tr_b16 v[40:41], v232 offset:50176\n"
        "v_accvgpr_write_b32 a245, v185\n"
        "v_mfma_f32_16x16x32_bf16 v[72:75], v[52:55], v[212:215], v[72:75]\n"
        "global_load_lds_dwordx4 v243, s[86:87] offset:1024\n"
        "v_accvgpr_write_b32 a246, v186\n"
        "v_mfma_f32_16x16x32_bf16 v[72:75], v[48:51], v[196:199], v[72:75]\n"
        "ds_read_b64_tr_b16 v[42:43], v233 offset:50176\n"
        "v_accvgpr_write_b32 a247, v187\n"
        "v_mfma_f32_16x16x32_bf16 v[104:107], v[48:51], v[200:203], v[104:107]\n"
        "v_accvgpr_read_b32 v185, a149\n"
        "v_accvgpr_read_b32 v186, a150\n"
        "v_mfma_f32_16x16x32_bf16 v[104:107], v[52:55], v[216:219], v[104:107]\n"
        "v_accvgpr_read_b32 v187, a151\n"
        "v_accvgpr_read_b32 v188, a180\n"
        "v_mfma_f32_16x16x32_bf16 v[108:111], v[60:63], v[216:219], v[108:111]\n"
        "global_store_dwordx4 v254, a[200:203], s[70:71] offset:0 sc1\n"
        "v_accvgpr_read_b32 v189, a181\n"
        "v_accvgpr_read_b32 v190, a182\n"
        "v_accvgpr_read_b32 v191, a183\n"
        "v_mfma_f32_16x16x32_bf16 v[108:111], v[56:59], v[200:203], v[108:111]\n"
        "v_mfma_f32_16x16x32_bf16 v[140:143], v[56:59], v[204:207], v[140:143]\n"
        "s_waitcnt lgkmcnt(2)\n"
        "v_mfma_f32_16x16x32_bf16 v[140:143], v[60:63], v[220:223], v[140:143]\n"
        "v_mfma_f32_16x16x32_bf16 v[136:139], v[52:55], v[220:223], v[136:139]\n"
        "global_load_lds_dwordx4 v244, s[86:87] offset:2048\n"
        "v_mfma_f32_16x16x32_bf16 v[136:139], v[48:51], v[204:207], v[136:139]\n"
        "s_waitcnt vmcnt(12) lgkmcnt(0)\n"
        "v_mfma_f32_16x16x32_bf16 v[16:19], v[160:163], v[192:195], v[16:19]\n"
        "s_barrier\n"
        "v_mfma_f32_16x16x32_bf16 v[16:19], v[164:167], v[208:211], v[16:19]\n"
        "ds_read_b64_tr_b16 v[48:49], v226 offset:16384\n"
        "v_mfma_f32_16x16x32_bf16 v[20:23], v[172:175], v[208:211], v[20:23]\n"
        "ds_read_b64_tr_b16 v[50:51], v227 offset:16384\n"
        "v_mfma_f32_16x16x32_bf16 v[20:23], v[168:171], v[192:195], v[20:23]\n"
        "global_load_lds_dwordx4 v245, s[86:87] offset:3072\n"
        "s_add_u32 m0, s73, 0x24000\n"
        "v_mfma_f32_16x16x32_bf16 v[84:87], v[168:171], v[196:199], v[84:87]\n"
        "s_add_u32 s86, s68, 0x500000\n"
        "s_addc_u32 s87, s69, 0\n"
        "v_mfma_f32_16x16x32_bf16 v[84:87], v[172:175], v[212:215], v[84:87]\n"
        "ds_read_b64_tr_b16 v[52:53], v226 offset:24576\n"
        "v_mfma_f32_16x16x32_bf16 v[80:83], v[164:167], v[212:215], v[80:83]\n"
        "ds_read_b64_tr_b16 v[54:55], v227 offset:24576\n"
        "v_mfma_f32_16x16x32_bf16 v[80:83], v[160:163], v[196:199], v[80:83]\n"
        "ds_read_b64_tr_b16 v[60:61], v228 offset:24576\n"
        "v_accvgpr_read_b32 v176, a144\n"
        "v_mfma_f32_16x16x32_bf16 v[112:115], v[160:163], v[200:203], v[112:115]\n"
        "global_load_lds_dwordx4 v238, s[86:87] offset:0\n"
        "v_mfma_f32_16x16x32_bf16 v[112:115], v[164:167], v[216:219], v[112:115]\n"
        "ds_read_b64_tr_b16 v[62:63], v229 offset:24576\n"
        "v_cvt_pk_bf16_f32 v176, v176, v177\n"
        "v_mfma_f32_16x16x32_bf16 v[116:119], v[172:175], v[216:219], v[116:119]\n"
        "ds_read_b64_tr_b16 v[56:57], v228 offset:16384\n"
        "v_cvt_pk_bf16_f32 v177, v178, v179\n"
        "v_mfma_f32_16x16x32_bf16 v[116:119], v[168:171], v[200:203], v[116:119]\n"
        "ds_read_b64_tr_b16 v[58:59], v229 offset:16384\n"
        "v_cvt_pk_bf16_f32 v178, v180, v181\n"
        "v_mfma_f32_16x16x32_bf16 v[148:151], v[168:171], v[204:207], v[148:151]\n"
        "ds_read_b64_tr_b16 v[168:169], v232 offset:16384\n"
        "v_cvt_pk_bf16_f32 v179, v182, v183\n"
        "v_mfma_f32_16x16x32_bf16 v[148:151], v[172:175], v[220:223], v[148:151]\n"
        "global_load_lds_dwordx4 v239, s[86:87] offset:1024\n"
        "v_mfma_f32_16x16x32_bf16 v[144:147], v[164:167], v[220:223], v[144:147]\n"
        "ds_read_b64_tr_b16 v[164:165], v230 offset:24576\n"
        "v_accvgpr_write_b32 a216, v176\n"
        "v_mfma_f32_16x16x32_bf16 v[144:147], v[160:163], v[204:207], v[144:147]\n"
        "ds_read_b64_tr_b16 v[160:161], v230 offset:16384\n"
        "v_accvgpr_write_b32 a217, v177\n"
        "v_mfma_f32_16x16x32_bf16 v[24:27], v[32:35], v[192:195], v[24:27]\n"
        "ds_read_b64_tr_b16 v[162:163], v231 offset:16384\n"
        "v_accvgpr_write_b32 a218, v178\n"
        "v_mfma_f32_16x16x32_bf16 v[24:27], v[36:39], v[208:211], v[24:27]\n"
        "ds_read_b64_tr_b16 v[166:167], v231 offset:24576\n"
        "v_accvgpr_write_b32 a219, v179\n"
        "v_mfma_f32_16x16x32_bf16 v[28:31], v[44:47], v[208:211], v[28:31]\n"
        "global_load_lds_dwordx4 v240, s[86:87] offset:2048\n"
        "v_mfma_f32_16x16x32_bf16 v[28:31], v[40:43], v[192:195], v[28:31]\n"
        "ds_read_b64_tr_b16 v[172:173], v232 offset:24576\n"
        "v_accvgpr_read_b32 v177, a153\n"
        "v_mfma_f32_16x16x32_bf16 v[92:95], v[40:43], v[196:199], v[92:95]\n"
        "ds_read_b64_tr_b16 v[174:175], v233 offset:24576\n"
        "v_accvgpr_read_b32 v178, a154\n"
        "v_mfma_f32_16x16x32_bf16 v[92:95], v[44:47], v[212:215], v[92:95]\n"
        "ds_read_b64_tr_b16 v[170:171], v233 offset:16384\n"
        "v_accvgpr_read_b32 v179, a155\n"
        "v_mfma_f32_16x16x32_bf16 v[88:91], v[36:39], v[212:215], v[88:91]\n"
        "ds_read_b128 v[192:195], v246 offset:34816\n"
        "v_accvgpr_read_b32 v180, a184\n"
        "v_mfma_f32_16x16x32_bf16 v[88:91], v[32:35], v[196:199], v[88:91]\n"
        "global_load_lds_dwordx4 v241, s[86:87] offset:3072\n"
        "s_add_u32 m0, s72, 0x10000\n"
        "v_mfma_f32_16x16x32_bf16 v[120:123], v[32:35], v[200:203], v[120:123]\n"
        "ds_read_b128 v[208:211], v247 offset:34816\n"
        "s_add_u32 s86, s64, 0x500\n"
        "s_addc_u32 s87, s65, 0\n"
        "v_mfma_f32_16x16x32_bf16 v[120:123], v[36:39], v[216:219], v[120:123]\n"
        "ds_read_b128 v[196:199], v246 offset:32768\n"
        "v_accvgpr_read_b32 v181, a185\n"
        "v_mfma_f32_16x16x32_bf16 v[124:127], v[44:47], v[216:219], v[124:127]\n"
        "ds_read_b128 v[212:215], v247 offset:32768\n"
        "v_accvgpr_read_b32 v182, a186\n"
        "v_mfma_f32_16x16x32_bf16 v[124:127], v[40:43], v[200:203], v[124:127]\n"
        "ds_read_b128 v[200:203], v246 offset:38912\n"
        "v_accvgpr_read_b32 v183, a187\n"
        "v_mfma_f32_16x16x32_bf16 v[156:159], v[40:43], v[204:207], v[156:159]\n"
        "global_load_lds_dwordx4 v242, s[86:87] offset:0\n"
        "v_mfma_f32_16x16x32_bf16 v[156:159], v[44:47], v[220:223], v[156:159]\n"
        "ds_read_b128 v[216:219], v247 offset:38912\n"
        "v_mfma_f32_16x16x32_bf16 v[152:155], v[36:39], v[220:223], v[152:155]\n"
        "ds_read_b128 v[220:223], v247 offset:36864\n"
        "s_waitcnt lgkmcnt(6)\n"
        "v_mfma_f32_16x16x32_bf16 v[152:155], v[32:35], v[204:207], v[152:155]\n"
        "ds_read_b128 v[204:207], v246 offset:36864\n"
        "v_accvgpr_read_b32 v184, a148\n"
        "v_mfma_f32_16x16x32_bf16 v[0:3], v[48:51], v[192:195], v[0:3]\n"
        "s_waitcnt lgkmcnt(6)\n"
        "v_cvt_pk_bf16_f32 v184, v184, v185\n"
        "v_cvt_pk_bf16_f32 v185, v186, v187\n"
        "v_mfma_f32_16x16x32_bf16 v[0:3], v[52:55], v[208:211], v[0:3]\n"
        "global_load_lds_dwordx4 v243, s[86:87] offset:1024\n"
        "v_mfma_f32_16x16x32_bf16 v[4:7], v[60:63], v[208:211], v[4:7]\n"
        "s_waitcnt lgkmcnt(4)\n"
        "v_cvt_pk_bf16_f32 v186, v188, v189\n"
        "v_cvt_pk_bf16_f32 v187, v190, v191\n"
        "v_mfma_f32_16x16x32_bf16 v[4:7], v[56:59], v[192:195], v[4:7]\n"
        "ds_read_b64_tr_b16 v[32:33], v226 offset:17408\n"
        "v_accvgpr_write_b32 a248, v184\n"
        "v_mfma_f32_16x16x32_bf16 v[68:71], v[56:59], v[196:199], v[68:71]\n"
        "s_waitcnt lgkmcnt(4)\n"
        "v_accvgpr_write_b32 a249, v185\n"
        "v_accvgpr_write_b32 a250, v186\n"
        "v_mfma_f32_16x16x32_bf16 v[68:71], v[60:63], v[212:215], v[68:71]\n"
        "s_waitcnt lgkmcnt(3)\n"
        "v_accvgpr_write_b32 a251, v187\n"
        "v_accvgpr_read_b32 v185, a157\n"
        "v_mfma_f32_16x16x32_bf16 v[64:67], v[52:55], v[212:215], v[64:67]\n"
        "global_load_lds_dwordx4 v244, s[86:87] offset:2048\n"
        "v_mfma_f32_16x16x32_bf16 v[64:67], v[48:51], v[196:199], v[64:67]\n"
        "ds_read_b64_tr_b16 v[34:35], v227 offset:17408\n"
        "v_accvgpr_read_b32 v186, a158\n"
        "v_mfma_f32_16x16x32_bf16 v[96:99], v[48:51], v[200:203], v[96:99]\n"
        "ds_read_b64_tr_b16 v[36:37], v226 offset:25600\n"
        "v_accvgpr_read_b32 v187, a159\n"
        "v_mfma_f32_16x16x32_bf16 v[96:99], v[52:55], v[216:219], v[96:99]\n"
        "ds_read_b64_tr_b16 v[38:39], v227 offset:25600\n"
        "v_accvgpr_read_b32 v188, a188\n"
        "v_mfma_f32_16x16x32_bf16 v[100:103], v[60:63], v[216:219], v[100:103]\n"
        "ds_read_b64_tr_b16 v[44:45], v228 offset:25600\n"
        "v_accvgpr_read_b32 v189, a189\n"
        "v_mfma_f32_16x16x32_bf16 v[100:103], v[56:59], v[200:203], v[100:103]\n"
        "ds_read_b64_tr_b16 v[46:47], v229 offset:25600\n"
        "s_waitcnt lgkmcnt(5)\n"
        "v_mfma_f32_16x16x32_bf16 v[132:135], v[56:59], v[204:207], v[132:135]\n"
        "ds_read_b64_tr_b16 v[40:41], v228 offset:17408\n"
        "global_load_lds_dwordx4 v245, s[86:87] offset:3072\n"
        "s_add_u32 m0, s72, 0x11000\n"
        "v_mfma_f32_16x16x32_bf16 v[132:135], v[60:63], v[220:223], v[132:135]\n"
        "ds_read_b64_tr_b16 v[42:43], v229 offset:17408\n"
        "s_add_u32 s86, s66, 0x500\n"
        "s_addc_u32 s87, s67, 0\n"
        "v_mfma_f32_16x16x32_bf16 v[128:131], v[52:55], v[220:223], v[128:131]\n"
        "ds_read_b64_tr_b16 v[52:53], v230 offset:25600\n"
        "v_accvgpr_read_b32 v190, a190\n"
        "v_mfma_f32_16x16x32_bf16 v[128:131], v[48:51], v[204:207], v[128:131]\n"
        "ds_read_b64_tr_b16 v[48:49], v230 offset:17408\n"
        "v_accvgpr_read_b32 v191, a191\n"
        "v_mfma_f32_16x16x32_bf16 v[8:11], v[160:163], v[192:195], v[8:11]\n"
        "ds_read_b64_tr_b16 v[50:51], v231 offset:17408\n"
        "v_mfma_f32_16x16x32_bf16 v[8:11], v[164:167], v[208:211], v[8:11]\n"
        "global_load_lds_dwordx4 v242, s[86:87] offset:0\n"
        "v_mfma_f32_16x16x32_bf16 v[12:15], v[172:175], v[208:211], v[12:15]\n"
        "ds_read_b64_tr_b16 v[54:55], v231 offset:25600\n"
        "v_mfma_f32_16x16x32_bf16 v[12:15], v[168:171], v[192:195], v[12:15]\n"
        "ds_read_b64_tr_b16 v[60:61], v232 offset:25600\n"
        "v_mfma_f32_16x16x32_bf16 v[76:79], v[168:171], v[196:199], v[76:79]\n"
        "ds_read_b64_tr_b16 v[62:63], v233 offset:25600\n"
        "v_mfma_f32_16x16x32_bf16 v[76:79], v[172:175], v[212:215], v[76:79]\n"
        "ds_read_b64_tr_b16 v[56:57], v232 offset:17408\n"
        "v_mfma_f32_16x16x32_bf16 v[72:75], v[164:167], v[212:215], v[72:75]\n"
        "global_load_lds_dwordx4 v243, s[86:87] offset:1024\n"
        "v_mfma_f32_16x16x32_bf16 v[72:75], v[160:163], v[196:199], v[72:75]\n"
        "ds_read_b64_tr_b16 v[58:59], v233 offset:17408\n"
        "v_accvgpr_read_b32 v176, a152\n"
        "v_mfma_f32_16x16x32_bf16 v[104:107], v[160:163], v[200:203], v[104:107]\n"
        "v_cvt_pk_bf16_f32 v176, v176, v177\n"
        "v_cvt_pk_bf16_f32 v177, v178, v179\n"
        "v_mfma_f32_16x16x32_bf16 v[104:107], v[164:167], v[216:219], v[104:107]\n"
        "v_cvt_pk_bf16_f32 v178, v180, v181\n"
        "v_cvt_pk_bf16_f32 v179, v182, v183\n"
        "v_mfma_f32_16x16x32_bf16 v[108:111], v[172:175], v[216:219], v[108:111]\n"
        "v_accvgpr_write_b32 a220, v176\n"
        "v_accvgpr_write_b32 a221, v177\n"
        "v_mfma_f32_16x16x32_bf16 v[108:111], v[168:171], v[200:203], v[108:111]\n"
        "global_store_dwordx4 v254, a[204:207], s[70:71] offset:1024 sc1\n"
        "s_add_u32 s70, s38, s96\n"
        "s_addc_u32 s71, s39, 0\n"
        "v_accvgpr_write_b32 a222, v178\n"
        "v_accvgpr_write_b32 a223, v179\n"
        "v_accvgpr_read_b32 v177, a17\n"
        "v_mfma_f32_16x16x32_bf16 v[140:143], v[168:171], v[204:207], v[140:143]\n"
        "s_waitcnt lgkmcnt(2)\n"
        "v_accvgpr_read_b32 v178, a18\n"
        "v_accvgpr_read_b32 v179, a19\n"
        "v_mfma_f32_16x16x32_bf16 v[140:143], v[172:175], v[220:223], v[140:143]\n"
        "v_accvgpr_read_b32 v180, a48\n"
        "v_accvgpr_read_b32 v181, a49\n"
        "v_mfma_f32_16x16x32_bf16 v[136:139], v[164:167], v[220:223], v[136:139]\n"
        "s_waitcnt vmcnt(11) lgkmcnt(0)\n"
        "v_accvgpr_read_b32 v182, a50\n"
        "v_mfma_f32_16x16x32_bf16 v[136:139], v[160:163], v[204:207], v[136:139]\n"
        "global_load_lds_dwordx4 v244, s[86:87] offset:2048\n"
        "v_mfma_f32_16x16x32_bf16 v[16:19], v[32:35], v[192:195], v[16:19]\n"
        "s_barrier\n"
        "v_mfma_f32_16x16x32_bf16 v[16:19], v[36:39], v[208:211], v[16:19]\n"
        "ds_read_b64_tr_b16 v[160:161], v226 offset:32768\n"
        "v_accvgpr_read_b32 v183, a51\n"
        "v_mfma_f32_16x16x32_bf16 v[20:23], v[44:47], v[208:211], v[20:23]\n"
        "ds_read_b64_tr_b16 v[162:163], v227 offset:32768\n"
        "v_accvgpr_write_b32 a48, 0\n"
        "v_mfma_f32_16x16x32_bf16 v[20:23], v[40:43], v[192:195], v[20:23]\n"
        "ds_read_b64_tr_b16 v[164:165], v226 offset:40960\n"
        "v_accvgpr_write_b32 a49, 0\n"
        "v_mfma_f32_16x16x32_bf16 v[84:87], v[40:43], v[196:199], v[84:87]\n"
        "global_load_lds_dwordx4 v245, s[86:87] offset:3072\n"
        "s_add_u32 m0, s73, 0x1c000\n"
        "v_mfma_f32_16x16x32_bf16 v[84:87], v[44:47], v[212:215], v[84:87]\n"
        "s_add_u32 s86, s68, 0x580000\n"
        "s_addc_u32 s87, s69, 0\n"
        "s_add_u32 s96, s52, 0x2000\n"
        "v_mfma_f32_16x16x32_bf16 v[80:83], v[36:39], v[212:215], v[80:83]\n"
        "ds_read_b64_tr_b16 v[166:167], v227 offset:40960\n"
        "s_add_u32 s96, s96, s75\n"
        "v_accvgpr_write_b32 a50, 0\n"
        "v_mfma_f32_16x16x32_bf16 v[80:83], v[32:35], v[196:199], v[80:83]\n"
        "ds_read_b64_tr_b16 v[172:173], v228 offset:40960\n"
        "v_accvgpr_write_b32 a51, 0\n"
        "v_mfma_f32_16x16x32_bf16 v[112:115], v[32:35], v[200:203], v[112:115]\n"
        "ds_read_b64_tr_b16 v[174:175], v229 offset:40960\n"
        "v_mfma_f32_16x16x32_bf16 v[112:115], v[36:39], v[216:219], v[112:115]\n"
        "global_load_lds_dwordx4 v238, s[86:87] offset:0\n"
        "v_mfma_f32_16x16x32_bf16 v[116:119], v[44:47], v[216:219], v[116:119]\n"
        "ds_read_b64_tr_b16 v[168:169], v228 offset:32768\n"
        "v_mfma_f32_16x16x32_bf16 v[116:119], v[40:43], v[200:203], v[116:119]\n"
        "ds_read_b64_tr_b16 v[170:171], v229 offset:32768\n"
        "v_mfma_f32_16x16x32_bf16 v[148:151], v[40:43], v[204:207], v[148:151]\n"
        "ds_read_b64_tr_b16 v[40:41], v232 offset:32768\n"
        "v_mfma_f32_16x16x32_bf16 v[148:151], v[44:47], v[220:223], v[148:151]\n"
        "ds_read_b64_tr_b16 v[44:45], v232 offset:40960\n"
        "v_mfma_f32_16x16x32_bf16 v[144:147], v[36:39], v[220:223], v[144:147]\n"
        "global_load_lds_dwordx4 v239, s[86:87] offset:1024\n"
        "v_mfma_f32_16x16x32_bf16 v[144:147], v[32:35], v[204:207], v[144:147]\n"
        "ds_read_b64_tr_b16 v[32:33], v230 offset:32768\n"
        "v_accvgpr_read_b32 v184, a156\n"
        "v_mfma_f32_16x16x32_bf16 v[24:27], v[48:51], v[192:195], v[24:27]\n"
        "ds_read_b64_tr_b16 v[34:35], v231 offset:32768\n"
        "v_cvt_pk_bf16_f32 v184, v184, v185\n"
        "v_mfma_f32_16x16x32_bf16 v[24:27], v[52:55], v[208:211], v[24:27]\n"
        "ds_read_b64_tr_b16 v[36:37], v230 offset:40960\n"
        "v_cvt_pk_bf16_f32 v185, v186, v187\n"
        "v_mfma_f32_16x16x32_bf16 v[28:31], v[60:63], v[208:211], v[28:31]\n"
        "ds_read_b64_tr_b16 v[38:39], v231 offset:40960\n"
        "v_cvt_pk_bf16_f32 v186, v188, v189\n"
        "v_mfma_f32_16x16x32_bf16 v[28:31], v[56:59], v[192:195], v[28:31]\n"
        "global_load_lds_dwordx4 v240, s[86:87] offset:2048\n"
        "v_mfma_f32_16x16x32_bf16 v[92:95], v[56:59], v[196:199], v[92:95]\n"
        "ds_read_b64_tr_b16 v[46:47], v233 offset:40960\n"
        "v_cvt_pk_bf16_f32 v187, v190, v191\n"
        "v_mfma_f32_16x16x32_bf16 v[92:95], v[60:63], v[212:215], v[92:95]\n"
        "ds_read_b64_tr_b16 v[42:43], v233 offset:32768\n"
        "v_accvgpr_write_b32 a252, v184\n"
        "v_mfma_f32_16x16x32_bf16 v[88:91], v[52:55], v[212:215], v[88:91]\n"
        "ds_read_b128 v[192:195], v246 offset:2048\n"
        "v_accvgpr_write_b32 a253, v185\n"
        "v_mfma_f32_16x16x32_bf16 v[88:91], v[48:51], v[196:199], v[88:91]\n"
        "ds_read_b128 v[208:211], v247 offset:2048\n"
        "v_accvgpr_write_b32 a254, v186\n"
        "v_mfma_f32_16x16x32_bf16 v[120:123], v[48:51], v[200:203], v[120:123]\n"
        "global_load_lds_dwordx4 v241, s[86:87] offset:3072\n"
        "s_add_u32 m0, s72, 0x8000\n"
        "v_mfma_f32_16x16x32_bf16 v[120:123], v[52:55], v[216:219], v[120:123]\n"
        "ds_read_b128 v[196:199], v246 offset:0\n"
        "v_accvgpr_write_b32 a255, v187\n"
        "v_mfma_f32_16x16x32_bf16 v[124:127], v[60:63], v[216:219], v[124:127]\n"
        "s_add_u32 s86, s64, 0x580\n"
        "s_addc_u32 s87, s65, 0\n"
        "v_accvgpr_read_b32 v185, a21\n"
        "v_mfma_f32_16x16x32_bf16 v[124:127], v[56:59], v[200:203], v[124:127]\n"
        "ds_read_b128 v[212:215], v247 offset:0\n"
        "v_accvgpr_read_b32 v186, a22\n"
        "v_mfma_f32_16x16x32_bf16 v[156:159], v[56:59], v[204:207], v[156:159]\n"
        "ds_read_b128 v[200:203], v246 offset:6144\n"
        "v_accvgpr_read_b32 v187, a23\n"
        "v_mfma_f32_16x16x32_bf16 v[156:159], v[60:63], v[220:223], v[156:159]\n"
        "global_load_lds_dwordx4 v242, s[86:87] offset:0\n"
        "v_mfma_f32_16x16x32_bf16 v[152:155], v[52:55], v[220:223], v[152:155]\n"
        "ds_read_b128 v[216:219], v247 offset:6144\n"
        "v_accvgpr_read_b32 v188, a52\n"
        "v_mfma_f32_16x16x32_bf16 v[152:155], v[48:51], v[204:207], v[152:155]\n"
        "ds_read_b128 v[204:207], v246 offset:4096\n"
        "s_waitcnt lgkmcnt(5)\n"
        "v_mfma_f32_16x16x32_bf16 v[0:3], v[160:163], v[192:195], v[0:3]\n"
        "ds_read_b128 v[220:223], v247 offset:4096\n"
        "v_accvgpr_read_b32 v189, a53\n"
        "v_mfma_f32_16x16x32_bf16 v[0:3], v[164:167], v[208:211], v[0:3]\n"
        "ds_read_b64_tr_b16 v[48:49], v226 offset:33792\n"
        "LHA7_%=:\n"
        "s_cbranch_execz LHA11_%=\n"
        "v_accvgpr_read_b32 v190, a54\n"
        "v_mfma_f32_16x16x32_bf16 v[4:7], v[172:175], v[208:211], v[4:7]\n"
        "global_load_lds_dwordx4 v243, s[86:87] offset:1024\n"
        "v_mfma_f32_16x16x32_bf16 v[4:7], v[168:171], v[192:195], v[4:7]\n"
        "ds_read_b64_tr_b16 v[50:51], v227 offset:33792\n"
        "s_waitcnt lgkmcnt(6)\n"
        "v_mfma_f32_16x16x32_bf16 v[68:71], v[168:171], v[196:199], v[68:71]\n"
        "ds_read_b64_tr_b16 v[52:53], v226 offset:41984\n"
        "v_accvgpr_read_b32 v191, a55\n"
        "v_mfma_f32_16x16x32_bf16 v[68:71], v[172:175], v[212:215], v[68:71]\n"
        "ds_read_b64_tr_b16 v[54:55], v227 offset:41984\n"
        "v_accvgpr_write_b32 a52, 0\n"
        "v_mfma_f32_16x16x32_bf16 v[64:67], v[164:167], v[212:215], v[64:67]\n"
        "ds_read_b64_tr_b16 v[60:61], v228 offset:41984\n"
        "v_accvgpr_write_b32 a53, 0\n"
        "v_mfma_f32_16x16x32_bf16 v[64:67], v[160:163], v[196:199], v[64:67]\n"
        "s_waitcnt lgkmcnt(6)\n"
        "global_load_lds_dwordx4 v244, s[86:87] offset:2048\n"
        "v_mfma_f32_16x16x32_bf16 v[96:99], v[160:163], v[200:203], v[96:99]\n"
        "ds_read_b64_tr_b16 v[62:63], v229 offset:41984\n"
        "v_accvgpr_read_b32 v176, a16\n"
        "v_mfma_f32_16x16x32_bf16 v[96:99], v[164:167], v[216:219], v[96:99]\n"
        "ds_read_b64_tr_b16 v[56:57], v228 offset:33792\n"
        "v_cvt_pk_bf16_f32 v176, v176, v177\n"
        "v_mfma_f32_16x16x32_bf16 v[100:103], v[172:175], v[216:219], v[100:103]\n"
        "ds_read_b64_tr_b16 v[58:59], v229 offset:33792\n"
        "v_cvt_pk_bf16_f32 v177, v178, v179\n"
        "v_mfma_f32_16x16x32_bf16 v[100:103], v[168:171], v[200:203], v[100:103]\n"
        "s_waitcnt lgkmcnt(6)\n"
        "v_cvt_pk_bf16_f32 v178, v180, v181\n"
        "v_cvt_pk_bf16_f32 v179, v182, v183\n"
        "v_mfma_f32_16x16x32_bf16 v[132:135], v[168:171], v[204:207], v[132:135]\n"
        "global_load_lds_dwordx4 v245, s[86:87] offset:3072\n"
        "s_add_u32 m0, s72, 0x9000\n"
        "v_mfma_f32_16x16x32_bf16 v[132:135], v[172:175], v[220:223], v[132:135]\n"
        "ds_read_b64_tr_b16 v[172:173], v232 offset:41984\n"
        "v_accvgpr_write_b32 a176, v176\n"
        "v_mfma_f32_16x16x32_bf16 v[128:131], v[164:167], v[220:223], v[128:131]\n"
        "ds_read_b64_tr_b16 v[164:165], v230 offset:41984\n"
        "v_accvgpr_write_b32 a177, v177\n"
        "v_mfma_f32_16x16x32_bf16 v[128:131], v[160:163], v[204:207], v[128:131]\n"
        "ds_read_b64_tr_b16 v[160:161], v230 offset:33792\n"
        "v_accvgpr_write_b32 a178, v178\n"
        "v_mfma_f32_16x16x32_bf16 v[8:11], v[32:35], v[192:195], v[8:11]\n"
        "ds_read_b64_tr_b16 v[162:163], v231 offset:33792\n"
        "v_accvgpr_write_b32 a179, v179\n"
        "v_mfma_f32_16x16x32_bf16 v[8:11], v[36:39], v[208:211], v[8:11]\n"
        "s_add_u32 s86, s66, 0x580\n"
        "s_addc_u32 s87, s67, 0\n"
        "v_accvgpr_read_b32 v177, a25\n"
        "v_mfma_f32_16x16x32_bf16 v[12:15], v[44:47], v[208:211], v[12:15]\n"
        "global_load_lds_dwordx4 v242, s[86:87] offset:0\n"
        "ds_read_b64_tr_b16 v[166:167], v231 offset:41984\n"
        "v_mfma_f32_16x16x32_bf16 v[12:15], v[40:43], v[192:195], v[12:15]\n"
        "ds_read_b64_tr_b16 v[174:175], v233 offset:41984\n"
        "v_accvgpr_read_b32 v178, a26\n"
        "v_mfma_f32_16x16x32_bf16 v[76:79], v[40:43], v[196:199], v[76:79]\n"
        "ds_read_b64_tr_b16 v[168:169], v232 offset:33792\n"
        "v_accvgpr_read_b32 v179, a27\n"
        "v_mfma_f32_16x16x32_bf16 v[76:79], v[44:47], v[212:215], v[76:79]\n"
        "ds_read_b64_tr_b16 v[170:171], v233 offset:33792\n"
        "v_accvgpr_read_b32 v180, a56\n"
        "v_mfma_f32_16x16x32_bf16 v[72:75], v[36:39], v[212:215], v[72:75]\n"
        "v_accvgpr_read_b32 v181, a57\n"
        "v_accvgpr_read_b32 v182, a58\n"
        "v_mfma_f32_16x16x32_bf16 v[72:75], v[32:35], v[196:199], v[72:75]\n"
        "global_load_lds_dwordx4 v243, s[86:87] offset:1024\n"
        "v_accvgpr_read_b32 v183, a59\n"
        "v_mfma_f32_16x16x32_bf16 v[104:107], v[32:35], v[200:203], v[104:107]\n"
        "v_accvgpr_write_b32 a54, 0\n"
        "v_accvgpr_write_b32 a55, 0\n"
        "v_mfma_f32_16x16x32_bf16 v[104:107], v[36:39], v[216:219], v[104:107]\n"
        "v_accvgpr_write_b32 a56, 0\n"
        "v_accvgpr_write_b32 a57, 0\n"
        "v_mfma_f32_16x16x32_bf16 v[108:111], v[44:47], v[216:219], v[108:111]\n"
        "v_accvgpr_write_b32 a58, 0\n"
        "v_accvgpr_write_b32 a59, 0\n"
        "v_mfma_f32_16x16x32_bf16 v[108:111], v[40:43], v[200:203], v[108:111]\n"
        "global_store_dwordx4 v254, a[160:163], s[70:71] offset:0 sc1\n"
        "v_mfma_f32_16x16x32_bf16 v[140:143], v[40:43], v[204:207], v[140:143]\n"
        "s_waitcnt vmcnt(11) lgkmcnt(0)\n"
        "v_mfma_f32_16x16x32_bf16 v[140:143], v[44:47], v[220:223], v[140:143]\n"
        "s_barrier\n"
        "v_mfma_f32_16x16x32_bf16 v[136:139], v[36:39], v[220:223], v[136:139]\n"
        "ds_read_b64_tr_b16 v[36:37], v226 offset:57344\n"
        "v_mfma_f32_16x16x32_bf16 v[136:139], v[32:35], v[204:207], v[136:139]\n"
        "global_load_lds_dwordx4 v244, s[86:87] offset:2048\n"
        "v_mfma_f32_16x16x32_bf16 v[16:19], v[48:51], v[192:195], v[16:19]\n"
        "ds_read_b64_tr_b16 v[32:33], v226 offset:49152\n"
        "v_accvgpr_read_b32 v184, a20\n"
        "v_mfma_f32_16x16x32_bf16 v[16:19], v[52:55], v[208:211], v[16:19]\n"
        "ds_read_b64_tr_b16 v[34:35], v227 offset:49152\n"
        "v_cvt_pk_bf16_f32 v184, v184, v185\n"
        "v_mfma_f32_16x16x32_bf16 v[20:23], v[60:63], v[208:211], v[20:23]\n"
        "ds_read_b64_tr_b16 v[38:39], v227 offset:57344\n"
        "v_cvt_pk_bf16_f32 v185, v186, v187\n"
        "v_mfma_f32_16x16x32_bf16 v[20:23], v[56:59], v[192:195], v[20:23]\n"
        "ds_read_b64_tr_b16 v[44:45], v228 offset:57344\n"
        "v_cvt_pk_bf16_f32 v186, v188, v189\n"
        "v_mfma_f32_16x16x32_bf16 v[84:87], v[56:59], v[196:199], v[84:87]\n"
        "global_load_lds_dwordx4 v245, s[86:87] offset:3072\n"
        "s_add_u32 m0, s73, 0x20000\n"
        "v_mfma_f32_16x16x32_bf16 v[84:87], v[60:63], v[212:215], v[84:87]\n"
        "ds_read_b64_tr_b16 v[46:47], v229 offset:57344\n"
        "v_cvt_pk_bf16_f32 v187, v190, v191\n"
        "v_mfma_f32_16x16x32_bf16 v[80:83], v[52:55], v[212:215], v[80:83]\n"
        "ds_read_b64_tr_b16 v[40:41], v228 offset:49152\n"
        "v_accvgpr_write_b32 a180, v184\n"
        "v_mfma_f32_16x16x32_bf16 v[80:83], v[48:51], v[196:199], v[80:83]\n"
        "ds_read_b64_tr_b16 v[42:43], v229 offset:49152\n"
        "v_accvgpr_write_b32 a181, v185\n"
        "v_mfma_f32_16x16x32_bf16 v[112:115], v[48:51], v[200:203], v[112:115]\n"
        "s_add_u32 s86, s68, 0x600000\n"
        "s_addc_u32 s87, s69, 0\n"
        "v_accvgpr_write_b32 a182, v186\n"
        "v_mfma_f32_16x16x32_bf16 v[112:115], v[52:55], v[216:219], v[112:115]\n"
        "v_accvgpr_write_b32 a183, v187\n"
        "v_accvgpr_read_b32 v185, a29\n"
        "v_mfma_f32_16x16x32_bf16 v[116:119], v[60:63], v[216:219], v[116:119]\n"
        "global_load_lds_dwordx4 v238, s[86:87] offset:0\n"
        "v_accvgpr_read_b32 v186, a30\n"
        "v_mfma_f32_16x16x32_bf16 v[116:119], v[56:59], v[200:203], v[116:119]\n"
        "v_accvgpr_read_b32 v187, a31\n"
        "v_accvgpr_read_b32 v188, a60\n"
        "v_mfma_f32_16x16x32_bf16 v[148:151], v[56:59], v[204:207], v[148:151]\n"
        "ds_read_b64_tr_b16 v[56:57], v232 offset:49152\n"
        "v_accvgpr_read_b32 v189, a61\n"
        "v_mfma_f32_16x16x32_bf16 v[148:151], v[60:63], v[220:223], v[148:151]\n"
        "ds_read_b64_tr_b16 v[60:61], v232 offset:57344\n"
        "v_accvgpr_read_b32 v190, a62\n"
        "v_mfma_f32_16x16x32_bf16 v[144:147], v[52:55], v[220:223], v[144:147]\n"
        "ds_read_b64_tr_b16 v[52:53], v230 offset:57344\n"
        "v_accvgpr_read_b32 v191, a63\n"
        "v_mfma_f32_16x16x32_bf16 v[144:147], v[48:51], v[204:207], v[144:147]\n"
        "global_load_lds_dwordx4 v239, s[86:87] offset:1024\n"
        "ds_read_b64_tr_b16 v[48:49], v230 offset:49152\n"
        "v_mfma_f32_16x16x32_bf16 v[24:27], v[160:163], v[192:195], v[24:27]\n"
        "ds_read_b64_tr_b16 v[50:51], v231 offset:49152\n"
        "v_accvgpr_write_b32 a60, 0\n"
        "v_mfma_f32_16x16x32_bf16 v[24:27], v[164:167], v[208:211], v[24:27]\n"
        "ds_read_b64_tr_b16 v[54:55], v231 offset:57344\n"
        "v_accvgpr_write_b32 a61, 0\n"
        "v_mfma_f32_16x16x32_bf16 v[28:31], v[172:175], v[208:211], v[28:31]\n"
        "ds_read_b64_tr_b16 v[62:63], v233 offset:57344\n"
        "v_accvgpr_write_b32 a62, 0\n"
        "v_mfma_f32_16x16x32_bf16 v[28:31], v[168:171], v[192:195], v[28:31]\n"
        "ds_read_b64_tr_b16 v[58:59], v233 offset:49152\n"
        "v_accvgpr_write_b32 a63, 0\n"
        "v_mfma_f32_16x16x32_bf16 v[92:95], v[168:171], v[196:199], v[92:95]\n"
        "global_load_lds_dwordx4 v240, s[86:87] offset:2048\n"
        "ds_read_b128 v[192:195], v248 offset:2048\n"
        "v_mfma_f32_16x16x32_bf16 v[92:95], v[172:175], v[212:215], v[92:95]\n"
        "ds_read_b128 v[208:211], v249 offset:2048\n"
        "v_mfma_f32_16x16x32_bf16 v[88:91], v[164:167], v[212:215], v[88:91]\n"
        "ds_read_b128 v[212:215], v249 offset:0\n"
        "v_mfma_f32_16x16x32_bf16 v[88:91], v[160:163], v[196:199], v[88:91]\n"
        "ds_read_b128 v[196:199], v248 offset:0\n"
        "v_accvgpr_read_b32 v176, a24\n"
        "v_mfma_f32_16x16x32_bf16 v[120:123], v[160:163], v[200:203], v[120:123]\n"
        "v_cvt_pk_bf16_f32 v176, v176, v177\n"
        "v_cvt_pk_bf16_f32 v177, v178, v179\n"
        "v_mfma_f32_16x16x32_bf16 v[120:123], v[164:167], v[216:219], v[120:123]\n"
        "global_load_lds_dwordx4 v241, s[86:87] offset:3072\n"
        "s_add_u32 m0, s72, 0x0\n"
        "v_cvt_pk_bf16_f32 v178, v180, v181\n"
        "v_mfma_f32_16x16x32_bf16 v[124:127], v[172:175], v[216:219], v[124:127]\n"
        "s_add_u32 s86, s64, 0x600\n"
        "s_addc_u32 s87, s65, 0\n"
        "v_cvt_pk_bf16_f32 v179, v182, v183\n"
        "v_mfma_f32_16x16x32_bf16 v[124:127], v[168:171], v[200:203], v[124:127]\n"
        "ds_read_b128 v[200:203], v248 offset:6144\n"
        "v_accvgpr_write_b32 a184, v176\n"
        "v_mfma_f32_16x16x32_bf16 v[156:159], v[168:171], v[204:207], v[156:159]\n"
        "s_waitcnt lgkmcnt(4)\n"
        "v_accvgpr_write_b32 a185, v177\n"
        "v_accvgpr_write_b32 a186, v178\n"
        "v_mfma_f32_16x16x32_bf16 v[156:159], v[172:175], v[220:223], v[156:159]\n"
        "s_waitcnt lgkmcnt(3)\n"
        "v_accvgpr_write_b32 a187, v179\n"
        "v_accvgpr_read_b32 v177, a81\n"
        "v_mfma_f32_16x16x32_bf16 v[152:155], v[164:167], v[220:223], v[152:155]\n"
        "global_load_lds_dwordx4 v242, s[86:87] offset:0\n"
        "ds_read_b128 v[216:219], v249 offset:6144\n"
        "v_mfma_f32_16x16x32_bf16 v[152:155], v[160:163], v[204:207], v[152:155]\n"
        "ds_read_b128 v[204:207], v248 offset:4096\n"
        "v_accvgpr_read_b32 v178, a82\n"
        "v_mfma_f32_16x16x32_bf16 v[0:3], v[32:35], v[192:195], v[0:3]\n"
        "ds_read_b128 v[220:223], v249 offset:4096\n"
        "v_accvgpr_read_b32 v179, a83\n"
        "v_mfma_f32_16x16x32_bf16 v[0:3], v[36:39], v[208:211], v[0:3]\n"
        "ds_read_b64_tr_b16 v[160:161], v226 offset:50176\n"
        "v_accvgpr_read_b32 v180, a112\n"
        "v_mfma_f32_16x16x32_bf16 v[4:7], v[44:47], v[208:211], v[4:7]\n"
        "ds_read_b64_tr_b16 v[162:163], v227 offset:50176\n"
        "v_accvgpr_read_b32 v181, a113\n"
        "v_mfma_f32_16x16x32_bf16 v[4:7], v[40:43], v[192:195], v[4:7]\n"
        "global_load_lds_dwordx4 v243, s[86:87] offset:1024\n"
        "s_waitcnt lgkmcnt(5)\n"
        "v_mfma_f32_16x16x32_bf16 v[68:71], v[40:43], v[196:199], v[68:71]\n"
        "ds_read_b64_tr_b16 v[164:165], v226 offset:58368\n"
        "v_accvgpr_read_b32 v182, a114\n"
        "v_mfma_f32_16x16x32_bf16 v[68:71], v[44:47], v[212:215], v[68:71]\n"
        "ds_read_b64_tr_b16 v[166:167], v227 offset:58368\n"
        "v_accvgpr_read_b32 v183, a115\n"
        "v_mfma_f32_16x16x32_bf16 v[64:67], v[36:39], v[212:215], v[64:67]\n"
        "ds_read_b64_tr_b16 v[172:173], v228 offset:58368\n"
        "s_waitcnt lgkmcnt(7)\n"
        "v_mfma_f32_16x16x32_bf16 v[64:67], v[32:35], v[196:199], v[64:67]\n"
        "ds_read_b64_tr_b16 v[174:175], v229 offset:58368\n"
        "v_mfma_f32_16x16x32_bf16 v[96:99], v[32:35], v[200:203], v[96:99]\n"
        "global_load_lds_dwordx4 v244, s[86:87] offset:2048\n"
        "ds_read_b64_tr_b16 v[168:169], v228 offset:50176\n"
        "v_mfma_f32_16x16x32_bf16 v[96:99], v[36:39], v[216:219], v[96:99]\n"
        "ds_read_b64_tr_b16 v[170:171], v229 offset:50176\n"
        "s_waitcnt lgkmcnt(7)\n"
        "v_mfma_f32_16x16x32_bf16 v[100:103], v[44:47], v[216:219], v[100:103]\n"
        "v_mfma_f32_16x16x32_bf16 v[100:103], v[40:43], v[200:203], v[100:103]\n"
        "v_mfma_f32_16x16x32_bf16 v[132:135], v[40:43], v[204:207], v[132:135]\n"
        "ds_read_b64_tr_b16 v[40:41], v232 offset:50176\n"
        "v_mfma_f32_16x16x32_bf16 v[132:135], v[44:47], v[220:223], v[132:135]\n"
        "global_load_lds_dwordx4 v245, s[86:87] offset:3072\n"
        "s_add_u32 m0, s72, 0x1000\n"
        "v_mfma_f32_16x16x32_bf16 v[128:131], v[36:39], v[220:223], v[128:131]\n"
        "s_add_u32 s86, s66, 0x600\n"
        "s_addc_u32 s87, s67, 0\n"
        "v_mfma_f32_16x16x32_bf16 v[128:131], v[32:35], v[204:207], v[128:131]\n"
        "ds_read_b64_tr_b16 v[32:33], v230 offset:50176\n"
        "v_accvgpr_read_b32 v184, a28\n"
        "v_mfma_f32_16x16x32_bf16 v[8:11], v[48:51], v[192:195], v[8:11]\n"
        "ds_read_b64_tr_b16 v[34:35], v231 offset:50176\n"
        "v_cvt_pk_bf16_f32 v184, v184, v185\n"
        "v_mfma_f32_16x16x32_bf16 v[8:11], v[52:55], v[208:211], v[8:11]\n"
        "ds_read_b64_tr_b16 v[36:37], v230 offset:58368\n"
        "v_cvt_pk_bf16_f32 v185, v186, v187\n"
        "v_mfma_f32_16x16x32_bf16 v[12:15], v[60:63], v[208:211], v[12:15]\n"
        "global_load_lds_dwordx4 v242, s[86:87] offset:0\n"
        "v_cvt_pk_bf16_f32 v186, v188, v189\n"
        "v_mfma_f32_16x16x32_bf16 v[12:15], v[56:59], v[192:195], v[12:15]\n"
        "ds_read_b64_tr_b16 v[38:39], v231 offset:58368\n"
        "v_cvt_pk_bf16_f32 v187, v190, v191\n"
        "v_mfma_f32_16x16x32_bf16 v[76:79], v[56:59], v[196:199], v[76:79]\n"
        "ds_read_b64_tr_b16 v[44:45], v232 offset:58368\n"
        "v_accvgpr_write_b32 a188, v184\n"
        "v_mfma_f32_16x16x32_bf16 v[76:79], v[60:63], v[212:215], v[76:79]\n"
        "ds_read_b64_tr_b16 v[46:47], v233 offset:58368\n"
        "v_accvgpr_write_b32 a189, v185\n"
        "v_mfma_f32_16x16x32_bf16 v[72:75], v[52:55], v[212:215], v[72:75]\n"
        "ds_read_b64_tr_b16 v[42:43], v233 offset:50176\n"
        "v_accvgpr_write_b32 a190, v186\n"
        "v_mfma_f32_16x16x32_bf16 v[72:75], v[48:51], v[196:199], v[72:75]\n"
        "global_load_lds_dwordx4 v243, s[86:87] offset:1024\n"
        "v_accvgpr_write_b32 a191, v187\n"
        "v_mfma_f32_16x16x32_bf16 v[104:107], v[48:51], v[200:203], v[104:107]\n"
        "v_accvgpr_read_b32 v185, a85\n"
        "v_accvgpr_read_b32 v186, a86\n"
        "v_mfma_f32_16x16x32_bf16 v[104:107], v[52:55], v[216:219], v[104:107]\n"
        "v_accvgpr_read_b32 v187, a87\n"
        "v_accvgpr_read_b32 v188, a116\n"
        "v_mfma_f32_16x16x32_bf16 v[108:111], v[60:63], v[216:219], v[108:111]\n"
        "v_accvgpr_read_b32 v189, a117\n"
        "v_accvgpr_read_b32 v190, a118\n"
        "v_mfma_f32_16x16x32_bf16 v[108:111], v[56:59], v[200:203], v[108:111]\n"
        "v_accvgpr_read_b32 v191, a119\n"
        "v_mfma_f32_16x16x32_bf16 v[140:143], v[56:59], v[204:207], v[140:143]\n"
        "global_load_lds_dwordx4 v244, s[86:87] offset:2048\n"
        "s_waitcnt lgkmcnt(2)\n"
        "v_mfma_f32_16x16x32_bf16 v[140:143], v[60:63], v[220:223], v[140:143]\n"
        "v_mfma_f32_16x16x32_bf16 v[136:139], v[52:55], v[220:223], v[136:139]\n"
        "s_waitcnt vmcnt(11) lgkmcnt(0)\n"
        "v_mfma_f32_16x16x32_bf16 v[136:139], v[48:51], v[204:207], v[136:139]\n"
        "s_barrier\n"
        "v_mfma_f32_16x16x32_bf16 v[16:19], v[160:163], v[192:195], v[16:19]\n"
        "global_store_dwordx4 v254, a[164:167], s[70:71] offset:1024 sc1\n"
        "ds_read_b64_tr_b16 v[48:49], v226 offset:16384\n"
        "ds_read_b64_tr_b16 v[50:51], v227 offset:16384\n"
        "s_add_u32 s70, s38, s96\n"
        "s_addc_u32 s71, s39, 0\n"
        "v_mfma_f32_16x16x32_bf16 v[16:19], v[164:167], v[208:211], v[16:19]\n"
        "ds_read_b64_tr_b16 v[52:53], v226 offset:24576\n"
        "s_add_u32 s96, s52, 0x3000\n"
        "s_add_u32 s96, s96, s75\n"
        "v_mfma_f32_16x16x32_bf16 v[20:23], v[172:175], v[208:211], v[20:23]\n"
        "ds_read_b64_tr_b16 v[54:55], v227 offset:24576\n"
        "v_mfma_f32_16x16x32_bf16 v[20:23], v[168:171], v[192:195], v[20:23]\n"
        "ds_read_b64_tr_b16 v[60:61], v228 offset:24576\n"
        "v_mfma_f32_16x16x32_bf16 v[84:87], v[168:171], v[196:199], v[84:87]\n"
        "global_load_lds_dwordx4 v245, s[86:87] offset:3072\n"
        "s_add_u32 m0, s73, 0x24000\n"
        "v_mfma_f32_16x16x32_bf16 v[84:87], v[172:175], v[212:215], v[84:87]\n"
        "s_add_u32 s86, s68, 0x680000\n"
        "s_addc_u32 s87, s69, 0\n"
        "v_mfma_f32_16x16x32_bf16 v[80:83], v[164:167], v[212:215], v[80:83]\n"
        "ds_read_b64_tr_b16 v[62:63], v229 offset:24576\n"
        "v_mfma_f32_16x16x32_bf16 v[80:83], v[160:163], v[196:199], v[80:83]\n"
        "ds_read_b64_tr_b16 v[56:57], v228 offset:16384\n"
        "v_accvgpr_read_b32 v176, a80\n"
        "v_mfma_f32_16x16x32_bf16 v[112:115], v[160:163], v[200:203], v[112:115]\n"
        "ds_read_b64_tr_b16 v[58:59], v229 offset:16384\n"
        "v_cvt_pk_bf16_f32 v176, v176, v177\n"
        "v_mfma_f32_16x16x32_bf16 v[112:115], v[164:167], v[216:219], v[112:115]\n"
        "global_load_lds_dwordx4 v238, s[86:87] offset:0\n"
        "v_mfma_f32_16x16x32_bf16 v[116:119], v[172:175], v[216:219], v[116:119]\n"
        "v_cvt_pk_bf16_f32 v177, v178, v179\n"
        "v_cvt_pk_bf16_f32 v178, v180, v181\n"
        "v_mfma_f32_16x16x32_bf16 v[116:119], v[168:171], v[200:203], v[116:119]\n"
        "v_cvt_pk_bf16_f32 v179, v182, v183\n"
        "v_accvgpr_write_b32 a16, v176\n"
        "v_mfma_f32_16x16x32_bf16 v[148:151], v[168:171], v[204:207], v[148:151]\n"
        "ds_read_b64_tr_b16 v[168:169], v232 offset:16384\n"
        "v_accvgpr_write_b32 a17, v177\n"
        "v_mfma_f32_16x16x32_bf16 v[148:151], v[172:175], v[220:223], v[148:151]\n"
        "ds_read_b64_tr_b16 v[172:173], v232 offset:24576\n"
        "v_accvgpr_write_b32 a18, v178\n"
        "v_mfma_f32_16x16x32_bf16 v[144:147], v[164:167], v[220:223], v[144:147]\n"
        "global_load_lds_dwordx4 v239, s[86:87] offset:1024\n"
        "v_mfma_f32_16x16x32_bf16 v[144:147], v[160:163], v[204:207], v[144:147]\n"
        "ds_read_b64_tr_b16 v[160:161], v230 offset:16384\n"
        "v_accvgpr_write_b32 a19, v179\n"
        "v_mfma_f32_16x16x32_bf16 v[24:27], v[32:35], v[192:195], v[24:27]\n"
        "ds_read_b64_tr_b16 v[162:163], v231 offset:16384\n"
        "v_accvgpr_read_b32 v177, a89\n"
        "v_mfma_f32_16x16x32_bf16 v[24:27], v[36:39], v[208:211], v[24:27]\n"
        "ds_read_b64_tr_b16 v[164:165], v230 offset:24576\n"
        "v_accvgpr_read_b32 v178, a90\n"
        "v_mfma_f32_16x16x32_bf16 v[28:31], v[44:47], v[208:211], v[28:31]\n"
        "ds_read_b64_tr_b16 v[166:167], v231 offset:24576\n"
        "v_accvgpr_read_b32 v179, a91\n"
        "v_mfma_f32_16x16x32_bf16 v[28:31], v[40:43], v[192:195], v[28:31]\n"
        "global_load_lds_dwordx4 v240, s[86:87] offset:2048\n"
        "v_mfma_f32_16x16x32_bf16 v[92:95], v[40:43], v[196:199], v[92:95]\n"
        "ds_read_b64_tr_b16 v[174:175], v233 offset:24576\n"
        "v_accvgpr_read_b32 v180, a120\n"
        "v_mfma_f32_16x16x32_bf16 v[92:95], v[44:47], v[212:215], v[92:95]\n"
        "ds_read_b64_tr_b16 v[170:171], v233 offset:16384\n"
        "v_accvgpr_read_b32 v181, a121\n"
        "v_mfma_f32_16x16x32_bf16 v[88:91], v[36:39], v[212:215], v[88:91]\n"
        "ds_read_b128 v[192:195], v246 offset:34816\n"
        "v_accvgpr_read_b32 v182, a122\n"
        "v_mfma_f32_16x16x32_bf16 v[88:91], v[32:35], v[196:199], v[88:91]\n"
        "ds_read_b128 v[208:211], v247 offset:34816\n"
        "v_accvgpr_read_b32 v183, a123\n"
        "v_mfma_f32_16x16x32_bf16 v[120:123], v[32:35], v[200:203], v[120:123]\n"
        "global_load_lds_dwordx4 v241, s[86:87] offset:3072\n"
        "s_add_u32 m0, s72, 0x10000\n"
        "v_mfma_f32_16x16x32_bf16 v[120:123], v[36:39], v[216:219], v[120:123]\n"
        "s_add_u32 s86, s64, 0x680\n"
        "s_addc_u32 s87, s65, 0\n"
        "v_mfma_f32_16x16x32_bf16 v[124:127], v[44:47], v[216:219], v[124:127]\n"
        "ds_read_b128 v[196:199], v246 offset:32768\n"
        "v_mfma_f32_16x16x32_bf16 v[124:127], v[40:43], v[200:203], v[124:127]\n"
        "ds_read_b128 v[212:215], v247 offset:32768\n"
        "v_mfma_f32_16x16x32_bf16 v[156:159], v[40:43], v[204:207], v[156:159]\n"
        "ds_read_b128 v[200:203], v246 offset:38912\n"
        "v_mfma_f32_16x16x32_bf16 v[156:159], v[44:47], v[220:223], v[156:159]\n"
        "global_load_lds_dwordx4 v242, s[86:87] offset:0\n"
        "v_mfma_f32_16x16x32_bf16 v[152:155], v[36:39], v[220:223], v[152:155]\n"
        "ds_read_b128 v[216:219], v247 offset:38912\n"
        "s_waitcnt lgkmcnt(5)\n"
        "v_mfma_f32_16x16x32_bf16 v[152:155], v[32:35], v[204:207], v[152:155]\n"
        "ds_read_b128 v[204:207], v246 offset:36864\n"
        "v_accvgpr_read_b32 v184, a84\n"
        "v_mfma_f32_16x16x32_bf16 v[0:3], v[48:51], v[192:195], v[0:3]\n"
        "ds_read_b128 v[220:223], v247 offset:36864\n"
        "s_waitcnt lgkmcnt(6)\n"
        "v_mfma_f32_16x16x32_bf16 v[0:3], v[52:55], v[208:211], v[0:3]\n"
        "ds_read_b64_tr_b16 v[32:33], v226 offset:17408\n"
        "v_cvt_pk_bf16_f32 v184, v184, v185\n"
        "v_mfma_f32_16x16x32_bf16 v[4:7], v[60:63], v[208:211], v[4:7]\n"
        "global_load_lds_dwordx4 v243, s[86:87] offset:1024\n"
        "v_mfma_f32_16x16x32_bf16 v[4:7], v[56:59], v[192:195], v[4:7]\n"
        "s_waitcnt lgkmcnt(5)\n"
        "v_cvt_pk_bf16_f32 v185, v186, v187\n"
        "v_cvt_pk_bf16_f32 v186, v188, v189\n"
        "v_mfma_f32_16x16x32_bf16 v[68:71], v[56:59], v[196:199], v[68:71]\n"
        "s_waitcnt lgkmcnt(4)\n"
        "v_cvt_pk_bf16_f32 v187, v190, v191\n"
        "v_accvgpr_write_b32 a20, v184\n"
        "v_mfma_f32_16x16x32_bf16 v[68:71], v[60:63], v[212:215], v[68:71]\n"
        "ds_read_b64_tr_b16 v[34:35], v227 offset:17408\n"
        "v_accvgpr_write_b32 a21, v185\n"
        "v_mfma_f32_16x16x32_bf16 v[64:67], v[52:55], v[212:215], v[64:67]\n"
        "s_waitcnt lgkmcnt(4)\n"
        "v_accvgpr_write_b32 a22, v186\n"
        "v_accvgpr_write_b32 a23, v187\n"
        "v_mfma_f32_16x16x32_bf16 v[64:67], v[48:51], v[196:199], v[64:67]\n"
        "global_load_lds_dwordx4 v244, s[86:87] offset:2048\n"
        "v_mfma_f32_16x16x32_bf16 v[96:99], v[48:51], v[200:203], v[96:99]\n"
        "ds_read_b64_tr_b16 v[36:37], v226 offset:25600\n"
        "v_accvgpr_read_b32 v185, a93\n"
        "v_mfma_f32_16x16x32_bf16 v[96:99], v[52:55], v[216:219], v[96:99]\n"
        "ds_read_b64_tr_b16 v[38:39], v227 offset:25600\n"
        "v_accvgpr_read_b32 v186, a94\n"
        "v_mfma_f32_16x16x32_bf16 v[100:103], v[60:63], v[216:219], v[100:103]\n"
        "ds_read_b64_tr_b16 v[44:45], v228 offset:25600\n"
        "v_accvgpr_read_b32 v187, a95\n"
        "v_mfma_f32_16x16x32_bf16 v[100:103], v[56:59], v[200:203], v[100:103]\n"
        "ds_read_b64_tr_b16 v[46:47], v229 offset:25600\n"
        "s_waitcnt lgkmcnt(5)\n"
        "v_mfma_f32_16x16x32_bf16 v[132:135], v[56:59], v[204:207], v[132:135]\n"
        "ds_read_b64_tr_b16 v[40:41], v228 offset:17408\n"
        "global_load_lds_dwordx4 v245, s[86:87] offset:3072\n"
        "s_add_u32 m0, s72, 0x11000\n"
        "v_mfma_f32_16x16x32_bf16 v[132:135], v[60:63], v[220:223], v[132:135]\n"
        "ds_read_b64_tr_b16 v[42:43], v229 offset:17408\n"
        "s_add_u32 s86, s66, 0x680\n"
        "s_addc_u32 s87, s67, 0\n"
        "v_mfma_f32_16x16x32_bf16 v[128:131], v[52:55], v[220:223], v[128:131]\n"
        "ds_read_b64_tr_b16 v[52:53], v230 offset:25600\n"
        "v_accvgpr_read_b32 v188, a124\n"
        "v_mfma_f32_16x16x32_bf16 v[128:131], v[48:51], v[204:207], v[128:131]\n"
        "ds_read_b64_tr_b16 v[48:49], v230 offset:17408\n"
        "v_accvgpr_read_b32 v189, a125\n"
        "v_mfma_f32_16x16x32_bf16 v[8:11], v[160:163], v[192:195], v[8:11]\n"
        "ds_read_b64_tr_b16 v[50:51], v231 offset:17408\n"
        "v_accvgpr_read_b32 v190, a126\n"
        "v_mfma_f32_16x16x32_bf16 v[8:11], v[164:167], v[208:211], v[8:11]\n"
        "global_load_lds_dwordx4 v242, s[86:87] offset:0\n"
        "v_mfma_f32_16x16x32_bf16 v[12:15], v[172:175], v[208:211], v[12:15]\n"
        "ds_read_b64_tr_b16 v[54:55], v231 offset:25600\n"
        "v_accvgpr_read_b32 v191, a127\n"
        "v_mfma_f32_16x16x32_bf16 v[12:15], v[168:171], v[192:195], v[12:15]\n"
        "ds_read_b64_tr_b16 v[60:61], v232 offset:25600\n"
        "v_accvgpr_write_b32 a93, v252\n"
        "v_mfma_f32_16x16x32_bf16 v[76:79], v[168:171], v[196:199], v[76:79]\n"
        "ds_read_b64_tr_b16 v[62:63], v233 offset:25600\n"
        "v_mfma_f32_16x16x32_bf16 v[76:79], v[172:175], v[212:215], v[76:79]\n"
        "ds_read_b64_tr_b16 v[56:57], v232 offset:17408\n"
        "v_mfma_f32_16x16x32_bf16 v[72:75], v[164:167], v[212:215], v[72:75]\n"
        "global_load_lds_dwordx4 v243, s[86:87] offset:1024\n"
        "v_mfma_f32_16x16x32_bf16 v[72:75], v[160:163], v[196:199], v[72:75]\n"
        "ds_read_b64_tr_b16 v[58:59], v233 offset:17408\n"
        "v_accvgpr_read_b32 v176, a88\n"
        "v_mfma_f32_16x16x32_bf16 v[104:107], v[160:163], v[200:203], v[104:107]\n"
        "v_cvt_pk_bf16_f32 v176, v176, v177\n"
        "v_cvt_pk_bf16_f32 v177, v178, v179\n"
        "v_mfma_f32_16x16x32_bf16 v[104:107], v[164:167], v[216:219], v[104:107]\n"
        "v_cvt_pk_bf16_f32 v178, v180, v181\n"
        "v_cvt_pk_bf16_f32 v179, v182, v183\n"
        "v_mfma_f32_16x16x32_bf16 v[108:111], v[172:175], v[216:219], v[108:111]\n"
        "v_accvgpr_write_b32 a24, v176\n"
        "v_accvgpr_write_b32 a25, v177\n"
        "v_mfma_f32_16x16x32_bf16 v[108:111], v[168:171], v[200:203], v[108:111]\n"
        "global_load_lds_dwordx4 v244, s[86:87] offset:2048\n"
        "v_mfma_f32_16x16x32_bf16 v[140:143], v[168:171], v[204:207], v[140:143]\n"
        "s_waitcnt lgkmcnt(2)\n"
        "v_accvgpr_write_b32 a26, v178\n"
        "v_accvgpr_write_b32 a27, v179\n"
        "v_mfma_f32_16x16x32_bf16 v[140:143], v[172:175], v[220:223], v[140:143]\n"
        "v_lshrrev_b32 v178, 7, v254\n"
        "v_bfe_u32 v179, v254, 4, 3\n"
        "v_mfma_f32_16x16x32_bf16 v[136:139], v[164:167], v[220:223], v[136:139]\n"
        "v_and_b32 v180, 6, v178\n"
        "v_xor_b32 v180, v180, v179\n"
        "v_mfma_f32_16x16x32_bf16 v[136:139], v[160:163], v[204:207], v[136:139]\n"
        "s_waitcnt vmcnt(11) lgkmcnt(0)\n"
        "v_lshlrev_b32 v180, 4, v180\n"
        "v_lshl_add_u32 v181, v178, 11, v180\n"
        "v_mfma_f32_16x16x32_bf16 v[16:19], v[32:35], v[192:195], v[16:19]\n"
        "global_load_lds_dwordx4 v245, s[86:87] offset:3072\n"
        "s_add_u32 m0, s73, 0x1c000\n"
        "v_mfma_f32_16x16x32_bf16 v[16:19], v[36:39], v[208:211], v[16:19]\n"
        "s_barrier\n"
        "v_mfma_f32_16x16x32_bf16 v[20:23], v[44:47], v[208:211], v[20:23]\n"
        "s_add_u32 s86, s68, 0x700000\n"
        "s_addc_u32 s87, s69, 0\n"
        "s_mul_i32 s90, s43, 529408\n"
        "v_mfma_f32_16x16x32_bf16 v[20:23], v[40:43], v[192:195], v[20:23]\n"
        "ds_read_b64_tr_b16 v[160:161], v226 offset:32768\n"
        "s_add_u32 s90, s90, s72\n"
        "s_and_b32 s78, s45, 1\n"
        "v_mfma_f32_16x16x32_bf16 v[84:87], v[40:43], v[196:199], v[84:87]\n"
        "ds_read_b64_tr_b16 v[162:163], v227 offset:32768\n"
        "s_lshl_b32 s78, s78, 15\n"
        "s_add_u32 s78, s78, 0x10000\n"
        "v_mfma_f32_16x16x32_bf16 v[84:87], v[44:47], v[212:215], v[84:87]\n"
        "global_load_lds_dwordx4 v238, s[86:87] offset:0\n"
        "s_and_b32 s80, s40, 1\n"
        "v_mfma_f32_16x16x32_bf16 v[80:83], v[36:39], v[212:215], v[80:83]\n"
        "ds_read_b64_tr_b16 v[164:165], v226 offset:40960\n"
        "s_lshl_b32 s80, s80, 11\n"
        "s_and_b32 s79, s45, 1\n"
        "v_mfma_f32_16x16x32_bf16 v[80:83], v[32:35], v[196:199], v[80:83]\n"
        "ds_read_b64_tr_b16 v[166:167], v227 offset:40960\n"
        "s_lshl_b32 s79, s79, 15\n"
        "s_lshl_b32 s82, s40, 4\n"
        "v_mfma_f32_16x16x32_bf16 v[112:115], v[32:35], v[200:203], v[112:115]\n"
        "ds_read_b64_tr_b16 v[172:173], v228 offset:40960\n"
        "s_sub_u32 s83, 14, s44\n"
        "v_accvgpr_write_b32 a64, 0\n"
        "v_mfma_f32_16x16x32_bf16 v[112:115], v[36:39], v[216:219], v[112:115]\n"
        "ds_read_b64_tr_b16 v[174:175], v229 offset:40960\n"
        "s_lshl_b32 s50, s41, 7\n"
        "v_accvgpr_write_b32 a65, 0\n"
        "v_mfma_f32_16x16x32_bf16 v[116:119], v[44:47], v[216:219], v[116:119]\n"
        "ds_read_b64_tr_b16 v[168:169], v228 offset:32768\n"
        "global_load_lds_dwordx4 v239, s[86:87] offset:1024\n"
        "s_lshl2_add_u32 s50, s42, s50\n"
        "v_mfma_f32_16x16x32_bf16 v[116:119], v[40:43], v[200:203], v[116:119]\n"
        "ds_read_b64_tr_b16 v[170:171], v229 offset:32768\n"
        "s_add_u32 s50, s50, s40\n"
        "v_accvgpr_write_b32 a66, 0\n"
        "v_mfma_f32_16x16x32_bf16 v[148:151], v[40:43], v[204:207], v[148:151]\n"
        "ds_read_b64_tr_b16 v[40:41], v232 offset:32768\n"
        "s_lshl_b32 s50, s50, 12\n"
        "v_accvgpr_write_b32 a67, 0\n"
        "v_mfma_f32_16x16x32_bf16 v[148:151], v[44:47], v[220:223], v[148:151]\n"
        "ds_read_b64_tr_b16 v[44:45], v232 offset:40960\n"
        "s_mul_i32 s51, s40, 8320\n"
        "v_accvgpr_write_b32 a68, 0\n"
        "v_mfma_f32_16x16x32_bf16 v[144:147], v[36:39], v[220:223], v[144:147]\n"
        "ds_read_b64_tr_b16 v[36:37], v230 offset:40960\n"
        "s_add_u32 s51, s51, 0x18000\n"
        "v_accvgpr_write_b32 a69, 0\n"
        "v_mfma_f32_16x16x32_bf16 v[144:147], v[32:35], v[204:207], v[144:147]\n"
        "global_store_dwordx4 v254, a[168:171], s[70:71] offset:0 sc1\n"
        "ds_read_b64_tr_b16 v[32:33], v230 offset:32768\n"
        "ds_read_b64_tr_b16 v[34:35], v231 offset:32768\n"
        "v_accvgpr_read_b32 v184, a92\n"
        "v_mfma_f32_16x16x32_bf16 v[24:27], v[48:51], v[192:195], v[24:27]\n"
        "ds_read_b64_tr_b16 v[38:39], v231 offset:40960\n"
        "v_cvt_pk_bf16_f32 v184, v184, v185\n"
        "v_mfma_f32_16x16x32_bf16 v[24:27], v[52:55], v[208:211], v[24:27]\n"
        "ds_read_b64_tr_b16 v[46:47], v233 offset:40960\n"
        "v_cvt_pk_bf16_f32 v185, v186, v187\n"
        "v_mfma_f32_16x16x32_bf16 v[28:31], v[60:63], v[208:211], v[28:31]\n"
        "ds_read_b64_tr_b16 v[42:43], v233 offset:32768\n"
        "v_cvt_pk_bf16_f32 v186, v188, v189\n"
        "v_mfma_f32_16x16x32_bf16 v[28:31], v[56:59], v[192:195], v[28:31]\n"
        "global_load_lds_dwordx4 v240, s[86:87] offset:2048\n"
        "v_mfma_f32_16x16x32_bf16 v[92:95], v[56:59], v[196:199], v[92:95]\n"
        "ds_read_b128 v[192:195], v246 offset:2048\n"
        "v_cvt_pk_bf16_f32 v187, v190, v191\n"
        "v_mfma_f32_16x16x32_bf16 v[92:95], v[60:63], v[212:215], v[92:95]\n"
        "ds_read_b128 v[208:211], v247 offset:2048\n"
        "v_accvgpr_write_b32 a28, v184\n"
        "v_mfma_f32_16x16x32_bf16 v[88:91], v[52:55], v[212:215], v[88:91]\n"
        "ds_read_b128 v[212:215], v247 offset:0\n"
        "LHA8_%=:\n"
        "s_cbranch_execz LHRA_%=\n"
        "v_accvgpr_write_b32 a29, v185\n"
        "v_mfma_f32_16x16x32_bf16 v[88:91], v[48:51], v[196:199], v[88:91]\n"
        "ds_read_b128 v[196:199], v246 offset:0\n"
        "v_accvgpr_write_b32 a30, v186\n"
        "v_mfma_f32_16x16x32_bf16 v[120:123], v[48:51], v[200:203], v[120:123]\n"
        "global_load_lds_dwordx4 v241, s[86:87] offset:3072\n"
        "s_add_u32 m0, s72, 0x8000\n"
        "v_mfma_f32_16x16x32_bf16 v[120:123], v[52:55], v[216:219], v[120:123]\n"
        "s_add_u32 s86, s64, 0x700\n"
        "s_addc_u32 s87, s65, 0\n"
        "v_accvgpr_write_b32 a31, v187\n"
        "v_mfma_f32_16x16x32_bf16 v[124:127], v[60:63], v[216:219], v[124:127]\n"
        "ds_read_b128 v[216:219], v247 offset:6144\n"
        "v_accvgpr_write_b32 a70, 0\n"
        "v_mfma_f32_16x16x32_bf16 v[124:127], v[56:59], v[200:203], v[124:127]\n"
        "ds_read_b128 v[200:203], v246 offset:6144\n"
        "v_accvgpr_write_b32 a71, 0\n"
        "v_mfma_f32_16x16x32_bf16 v[156:159], v[56:59], v[204:207], v[156:159]\n"
        "ds_read_b64_tr_b16 v[56:57], v228 offset:33792\n"
        "v_lshrrev_b32 v186, 5, v254\n"
        "v_mfma_f32_16x16x32_bf16 v[156:159], v[60:63], v[220:223], v[156:159]\n"
        "global_load_lds_dwordx4 v242, s[86:87] offset:0\n"
        "v_mfma_f32_16x16x32_bf16 v[152:155], v[52:55], v[220:223], v[152:155]\n"
        "ds_read_b128 v[220:223], v247 offset:4096\n"
        "v_lshrrev_b32 v187, 9, v254\n"
        "v_mfma_f32_16x16x32_bf16 v[152:155], v[48:51], v[204:207], v[152:155]\n"
        "ds_read_b128 v[204:207], v246 offset:4096\n"
        "s_waitcnt lgkmcnt(5)\n"
        "v_mfma_f32_16x16x32_bf16 v[0:3], v[160:163], v[192:195], v[0:3]\n"
        "ds_read_b64_tr_b16 v[48:49], v226 offset:33792\n"
        "v_xor_b32 v186, v186, v187\n"
        "v_mfma_f32_16x16x32_bf16 v[0:3], v[164:167], v[208:211], v[0:3]\n"
        "ds_read_b64_tr_b16 v[50:51], v227 offset:33792\n"
        "v_and_b32 v186, 1, v186\n"
        "v_mfma_f32_16x16x32_bf16 v[4:7], v[172:175], v[208:211], v[4:7]\n"
        "global_load_lds_dwordx4 v243, s[86:87] offset:1024\n"
        "v_mfma_f32_16x16x32_bf16 v[4:7], v[168:171], v[192:195], v[4:7]\n"
        "ds_read_b64_tr_b16 v[52:53], v226 offset:41984\n"
        "v_cmp_eq_u32 vcc, 0, v186\n"
        "v_mfma_f32_16x16x32_bf16 v[68:71], v[168:171], v[196:199], v[68:71]\n"
        "ds_read_b64_tr_b16 v[54:55], v227 offset:41984\n"
        "v_and_b32 v187, 0x30, v254\n"
        "v_mfma_f32_16x16x32_bf16 v[68:71], v[172:175], v[212:215], v[68:71]\n"
        "ds_read_b64_tr_b16 v[60:61], v228 offset:41984\n"
        "s_add_u32 s92, s26, s50\n"
        "s_addc_u32 s93, s27, 0\n"
        "v_mfma_f32_16x16x32_bf16 v[64:67], v[164:167], v[212:215], v[64:67]\n"
        "ds_read_b64_tr_b16 v[62:63], v229 offset:41984\n"
        "s_add_u32 s50, s50, 0x3300000\n"
        "v_add_u32 v182, 4096, v181\n"
        "v_mfma_f32_16x16x32_bf16 v[64:67], v[160:163], v[196:199], v[64:67]\n"
        "s_waitcnt lgkmcnt(6)\n"
        "global_load_lds_dwordx4 v244, s[86:87] offset:2048\n"
        "v_mfma_f32_16x16x32_bf16 v[96:99], v[160:163], v[200:203], v[96:99]\n"
        "ds_read_b64_tr_b16 v[58:59], v229 offset:33792\n"
        "s_add_u32 s94, s38, s50\n"
        "s_addc_u32 s95, s39, 0\n"
        "v_mfma_f32_16x16x32_bf16 v[96:99], v[164:167], v[216:219], v[96:99]\n"
        "s_add_u32 s50, s51, 4128\n"
        "v_accvgpr_write_b32 a84, v182\n"
        "v_xor_b32 v178, 4, v178\n"
        "v_mfma_f32_16x16x32_bf16 v[100:103], v[172:175], v[216:219], v[100:103]\n"
        "v_add_u32 v182, 3200, v181\n"
        "v_lshl_add_u32 v178, v178, 11, v180\n"
        "v_mfma_f32_16x16x32_bf16 v[100:103], v[168:171], v[200:203], v[100:103]\n"
        "v_accvgpr_write_b32 a85, v182\n"
        "v_add_u32 v182, 18432, v178\n"
        "v_mfma_f32_16x16x32_bf16 v[132:135], v[168:171], v[204:207], v[132:135]\n"
        "global_load_lds_dwordx4 v245, s[86:87] offset:3072\n"
        "s_add_u32 m0, s72, 0x9000\n"
        "v_mfma_f32_16x16x32_bf16 v[132:135], v[172:175], v[220:223], v[132:135]\n"
        "s_add_u32 s86, s66, 0x700\n"
        "s_addc_u32 s87, s67, 0\n"
        "v_add_u32 v180, s51, v254\n"
        "v_mfma_f32_16x16x32_bf16 v[128:131], v[164:167], v[220:223], v[128:131]\n"
        "ds_read_b64_tr_b16 v[164:165], v230 offset:41984\n"
        "v_accvgpr_write_b32 a86, v182\n"
        "v_mfma_f32_16x16x32_bf16 v[128:131], v[160:163], v[204:207], v[128:131]\n"
        "ds_read_b64_tr_b16 v[160:161], v230 offset:33792\n"
        "v_add_u32 v180, 4096, v180\n"
        "v_mfma_f32_16x16x32_bf16 v[8:11], v[32:35], v[192:195], v[8:11]\n"
        "ds_read_b64_tr_b16 v[162:163], v231 offset:33792\n"
        "v_add_u32 v182, 17536, v178\n"
        "v_mfma_f32_16x16x32_bf16 v[8:11], v[36:39], v[208:211], v[8:11]\n"
        "global_load_lds_dwordx4 v242, s[86:87] offset:0\n"
        "v_mfma_f32_16x16x32_bf16 v[12:15], v[44:47], v[208:211], v[12:15]\n"
        "ds_read_b64_tr_b16 v[166:167], v231 offset:41984\n"
        "v_bfe_u32 v176, v254, 4, 4\n"
        "v_mfma_f32_16x16x32_bf16 v[12:15], v[40:43], v[192:195], v[12:15]\n"
        "ds_read_b64_tr_b16 v[172:173], v232 offset:41984\n"
        "v_and_b32 v181, 1, v176\n"
        "v_mfma_f32_16x16x32_bf16 v[76:79], v[40:43], v[196:199], v[76:79]\n"
        "ds_read_b64_tr_b16 v[174:175], v233 offset:41984\n"
        "v_lshrrev_b32 v177, 8, v254\n"
        "v_mfma_f32_16x16x32_bf16 v[76:79], v[44:47], v[212:215], v[76:79]\n"
        "ds_read_b64_tr_b16 v[168:169], v232 offset:33792\n"
        "v_accvgpr_write_b32 a87, v182\n"
        "v_mfma_f32_16x16x32_bf16 v[72:75], v[36:39], v[212:215], v[72:75]\n"
        "global_load_lds_dwordx4 v243, s[86:87] offset:1024\n"
        "v_mfma_f32_16x16x32_bf16 v[72:75], v[32:35], v[196:199], v[72:75]\n"
        "ds_read_b64_tr_b16 v[170:171], v233 offset:33792\n"
        "v_bfe_u32 v188, v254, 4, 5\n"
        "v_mfma_f32_16x16x32_bf16 v[104:107], v[32:35], v[200:203], v[104:107]\n"
        "v_mfma_f32_16x16x32_bf16 v[104:107], v[36:39], v[216:219], v[104:107]\n"
        "v_mfma_f32_16x16x32_bf16 v[108:111], v[44:47], v[216:219], v[108:111]\n"
        "v_mfma_f32_16x16x32_bf16 v[108:111], v[40:43], v[200:203], v[108:111]\n"
        "global_load_lds_dwordx4 v244, s[86:87] offset:2048\n"
        "v_mfma_f32_16x16x32_bf16 v[140:143], v[40:43], v[204:207], v[140:143]\n"
        "s_waitcnt lgkmcnt(2)\n"
        "v_mfma_f32_16x16x32_bf16 v[140:143], v[44:47], v[220:223], v[140:143]\n"
        "v_mfma_f32_16x16x32_bf16 v[136:139], v[36:39], v[220:223], v[136:139]\n"
        "v_mfma_f32_16x16x32_bf16 v[136:139], v[32:35], v[204:207], v[136:139]\n"
        "s_waitcnt vmcnt(12) lgkmcnt(0)\n"
        "v_mfma_f32_16x16x32_bf16 v[16:19], v[48:51], v[192:195], v[16:19]\n"
        "global_load_lds_dwordx4 v245, s[86:87] offset:3072\n"
        "s_add_u32 m0, s73, 0x20000\n"
        "v_mfma_f32_16x16x32_bf16 v[16:19], v[52:55], v[208:211], v[16:19]\n"
        "s_barrier\n"
        "v_mfma_f32_16x16x32_bf16 v[20:23], v[60:63], v[208:211], v[20:23]\n"
        "s_add_u32 s86, s68, 0x780000\n"
        "s_addc_u32 s87, s69, 0\n"
        "v_mfma_f32_16x16x32_bf16 v[20:23], v[56:59], v[192:195], v[20:23]\n"
        "ds_read_b64_tr_b16 v[32:33], v226 offset:49152\n"
        "v_mfma_f32_16x16x32_bf16 v[84:87], v[56:59], v[196:199], v[84:87]\n"
        "ds_read_b64_tr_b16 v[34:35], v227 offset:49152\n"
        "v_mfma_f32_16x16x32_bf16 v[84:87], v[60:63], v[212:215], v[84:87]\n"
        "global_load_lds_dwordx4 v238, s[86:87] offset:0\n"
        "v_mfma_f32_16x16x32_bf16 v[80:83], v[52:55], v[212:215], v[80:83]\n"
        "ds_read_b64_tr_b16 v[36:37], v226 offset:57344\n"
        "v_mfma_f32_16x16x32_bf16 v[80:83], v[48:51], v[196:199], v[80:83]\n"
        "ds_read_b64_tr_b16 v[38:39], v227 offset:57344\n"
        "v_mfma_f32_16x16x32_bf16 v[112:115], v[48:51], v[200:203], v[112:115]\n"
        "ds_read_b64_tr_b16 v[44:45], v228 offset:57344\n"
        "v_mfma_f32_16x16x32_bf16 v[112:115], v[52:55], v[216:219], v[112:115]\n"
        "ds_read_b64_tr_b16 v[46:47], v229 offset:57344\n"
        "v_mfma_f32_16x16x32_bf16 v[116:119], v[60:63], v[216:219], v[116:119]\n"
        "ds_read_b64_tr_b16 v[40:41], v228 offset:49152\n"
        "global_load_lds_dwordx4 v239, s[86:87] offset:1024\n"
        "v_mfma_f32_16x16x32_bf16 v[116:119], v[56:59], v[200:203], v[116:119]\n"
        "ds_read_b64_tr_b16 v[42:43], v229 offset:49152\n"
        "v_mfma_f32_16x16x32_bf16 v[148:151], v[56:59], v[204:207], v[148:151]\n"
        "ds_read_b64_tr_b16 v[56:57], v232 offset:49152\n"
        "v_mfma_f32_16x16x32_bf16 v[148:151], v[60:63], v[220:223], v[148:151]\n"
        "ds_read_b64_tr_b16 v[60:61], v232 offset:57344\n"
        "v_mfma_f32_16x16x32_bf16 v[144:147], v[52:55], v[220:223], v[144:147]\n"
        "ds_read_b64_tr_b16 v[52:53], v230 offset:57344\n"
        "v_mfma_f32_16x16x32_bf16 v[144:147], v[48:51], v[204:207], v[144:147]\n"
        "global_store_dwordx4 v254, a[172:175], s[70:71] offset:1024 sc1\n"
        "ds_read_b64_tr_b16 v[48:49], v230 offset:49152\n"
        "ds_read_b64_tr_b16 v[50:51], v231 offset:49152\n"
        "s_add_u32 s70, s38, s96\n"
        "s_addc_u32 s71, s39, 0\n"
        "v_mfma_f32_16x16x32_bf16 v[24:27], v[160:163], v[192:195], v[24:27]\n"
        "ds_read_b64_tr_b16 v[54:55], v231 offset:57344\n"
        "s_add_u32 s96, s52, 0x0\n"
        "v_mfma_f32_16x16x32_bf16 v[24:27], v[164:167], v[208:211], v[24:27]\n"
        "ds_read_b64_tr_b16 v[62:63], v233 offset:57344\n"
        "v_mfma_f32_16x16x32_bf16 v[28:31], v[172:175], v[208:211], v[28:31]\n"
        "ds_read_b64_tr_b16 v[58:59], v233 offset:49152\n"
        "v_mfma_f32_16x16x32_bf16 v[28:31], v[168:171], v[192:195], v[28:31]\n"
        "global_load_lds_dwordx4 v240, s[86:87] offset:2048\n"
        "v_mfma_f32_16x16x32_bf16 v[92:95], v[168:171], v[196:199], v[92:95]\n"
        "ds_read_b128 v[192:195], v248 offset:2048\n"
        "v_add_u32 v240, s78, v254\n"
        "v_mfma_f32_16x16x32_bf16 v[92:95], v[172:175], v[212:215], v[92:95]\n"
        "ds_read_b128 v[208:211], v249 offset:2048\n"
        "v_mfma_f32_16x16x32_bf16 v[88:91], v[164:167], v[212:215], v[88:91]\n"
        "ds_read_b128 v[212:215], v249 offset:0\n"
        "v_mfma_f32_16x16x32_bf16 v[88:91], v[160:163], v[196:199], v[88:91]\n"
        "ds_read_b128 v[196:199], v248 offset:0\n"
        "v_mfma_f32_16x16x32_bf16 v[120:123], v[160:163], v[200:203], v[120:123]\n"
        "global_load_lds_dwordx4 v241, s[86:87] offset:3072\n"
        "s_add_u32 m0, s72, 0x0\n"
        "v_mfma_f32_16x16x32_bf16 v[120:123], v[164:167], v[216:219], v[120:123]\n"
        "s_add_u32 s86, s64, 0x780\n"
        "s_addc_u32 s87, s65, 0\n"
        "v_add_u32 v241, s79, v254\n"
        "v_mfma_f32_16x16x32_bf16 v[124:127], v[172:175], v[216:219], v[124:127]\n"
        "ds_read_b128 v[216:219], v249 offset:6144\n"
        "s_add_u32 s64, s38, s90\n"
        "s_addc_u32 s65, s39, 0\n"
        "v_mfma_f32_16x16x32_bf16 v[124:127], v[168:171], v[200:203], v[124:127]\n"
        "ds_read_b128 v[200:203], v248 offset:6144\n"
        "s_lshl_b32 s90, s40, 13\n"
        "s_and_b32 s90, s90, 0x4000\n"
        "v_mfma_f32_16x16x32_bf16 v[156:159], v[168:171], v[204:207], v[156:159]\n"
        "ds_read_b64_tr_b16 v[168:169], v228 offset:50176\n"
        "s_add_u32 s91, s78, s90\n"
        "s_add_u32 s91, s91, s80\n"
        "v_mfma_f32_16x16x32_bf16 v[156:159], v[172:175], v[220:223], v[156:159]\n"
        "global_load_lds_dwordx4 v242, s[86:87] offset:0\n"
        "s_xor_b32 s78, s78, 0x8000\n"
        "v_mfma_f32_16x16x32_bf16 v[152:155], v[164:167], v[220:223], v[152:155]\n"
        "ds_read_b128 v[220:223], v249 offset:4096\n"
        "s_add_u32 s78, s78, s72\n"
        "s_waitcnt lgkmcnt(5)\n"
        "v_mfma_f32_16x16x32_bf16 v[152:155], v[160:163], v[204:207], v[152:155]\n"
        "ds_read_b128 v[204:207], v248 offset:4096\n"
        "v_mfma_f32_16x16x32_bf16 v[0:3], v[32:35], v[192:195], v[0:3]\n"
        "ds_read_b64_tr_b16 v[160:161], v226 offset:50176\n"
        "s_waitcnt lgkmcnt(6)\n"
        "v_mfma_f32_16x16x32_bf16 v[0:3], v[36:39], v[208:211], v[0:3]\n"
        "ds_read_b64_tr_b16 v[162:163], v227 offset:50176\n"
        "v_mfma_f32_16x16x32_bf16 v[4:7], v[44:47], v[208:211], v[4:7]\n"
        "global_load_lds_dwordx4 v243, s[86:87] offset:1024\n"
        "v_mfma_f32_16x16x32_bf16 v[4:7], v[40:43], v[192:195], v[4:7]\n"
        "ds_read_b64_tr_b16 v[164:165], v226 offset:58368\n"
        "v_mfma_f32_16x16x32_bf16 v[68:71], v[40:43], v[196:199], v[68:71]\n"
        "ds_read_b64_tr_b16 v[166:167], v227 offset:58368\n"
        "s_waitcnt lgkmcnt(6)\n"
        "v_mfma_f32_16x16x32_bf16 v[68:71], v[44:47], v[212:215], v[68:71]\n"
        "ds_read_b64_tr_b16 v[172:173], v228 offset:58368\n"
        "v_mfma_f32_16x16x32_bf16 v[64:67], v[36:39], v[212:215], v[64:67]\n"
        "ds_read_b64_tr_b16 v[174:175], v229 offset:58368\n"
        "v_mfma_f32_16x16x32_bf16 v[64:67], v[32:35], v[196:199], v[64:67]\n"
        "global_load_lds_dwordx4 v244, s[86:87] offset:2048\n"
        "v_mfma_f32_16x16x32_bf16 v[96:99], v[32:35], v[200:203], v[96:99]\n"
        "ds_read_b64_tr_b16 v[170:171], v229 offset:50176\n"
        "v_xor_b32 v241, 0x8000, v241\n"
        "v_mfma_f32_16x16x32_bf16 v[96:99], v[36:39], v[216:219], v[96:99]\n"
        "s_waitcnt lgkmcnt(5)\n"
        "v_mfma_f32_16x16x32_bf16 v[100:103], v[44:47], v[216:219], v[100:103]\n"
        "v_mfma_f32_16x16x32_bf16 v[100:103], v[40:43], v[200:203], v[100:103]\n"
        "v_mfma_f32_16x16x32_bf16 v[132:135], v[40:43], v[204:207], v[132:135]\n"
        "global_load_lds_dwordx4 v245, s[86:87] offset:3072\n"
        "s_add_u32 m0, s72, 0x1000\n"
        "v_mfma_f32_16x16x32_bf16 v[132:135], v[44:47], v[220:223], v[132:135]\n"
        "s_add_u32 s86, s66, 0x780\n"
        "s_addc_u32 s87, s67, 0\n"
        "v_mfma_f32_16x16x32_bf16 v[128:131], v[36:39], v[220:223], v[128:131]\n"
        "ds_read_b64_tr_b16 v[36:37], v230 offset:58368\n"
        "s_add_u32 s66, s64, 0x1100000\n"
        "s_addc_u32 s67, s65, 0\n"
        "v_mfma_f32_16x16x32_bf16 v[128:131], v[32:35], v[204:207], v[128:131]\n"
        "ds_read_b64_tr_b16 v[32:33], v230 offset:50176\n"
        "v_mfma_f32_16x16x32_bf16 v[8:11], v[48:51], v[192:195], v[8:11]\n"
        "ds_read_b64_tr_b16 v[34:35], v231 offset:50176\n"
        "v_mfma_f32_16x16x32_bf16 v[8:11], v[52:55], v[208:211], v[8:11]\n"
        "global_load_lds_dwordx4 v242, s[86:87] offset:0\n"
        "v_mfma_f32_16x16x32_bf16 v[12:15], v[60:63], v[208:211], v[12:15]\n"
        "ds_read_b64_tr_b16 v[38:39], v231 offset:58368\n"
        "v_lshrrev_b32 v242, 6, v254\n"
        "v_mfma_f32_16x16x32_bf16 v[12:15], v[56:59], v[192:195], v[12:15]\n"
        "ds_read_b64_tr_b16 v[44:45], v232 offset:58368\n"
        "v_and_b32 v242, 12, v242\n"
        "v_mfma_f32_16x16x32_bf16 v[76:79], v[56:59], v[196:199], v[76:79]\n"
        "ds_read_b64_tr_b16 v[46:47], v233 offset:58368\n"
        "v_mfma_f32_16x16x32_bf16 v[76:79], v[60:63], v[212:215], v[76:79]\n"
        "ds_read_b64_tr_b16 v[40:41], v232 offset:50176\n"
        "v_mfma_f32_16x16x32_bf16 v[72:75], v[52:55], v[212:215], v[72:75]\n"
        "global_load_lds_dwordx4 v243, s[86:87] offset:1024\n"
        "v_mfma_f32_16x16x32_bf16 v[72:75], v[48:51], v[196:199], v[72:75]\n"
        "ds_read_b64_tr_b16 v[42:43], v233 offset:50176\n"
        "v_bfe_u32 v243, v254, 4, 4\n"
        "v_mfma_f32_16x16x32_bf16 v[104:107], v[48:51], v[200:203], v[104:107]\n"
        "v_sub_u32 v243, v243, v242\n"
        "v_add_u32 v243, s82, v243\n"
        "v_mfma_f32_16x16x32_bf16 v[104:107], v[52:55], v[216:219], v[104:107]\n"
        "s_mov_b32 s82, -1\n"
        "v_cmp_gt_i32 s[10:11], 0, v243\n"
        "v_cmp_gt_i32 s[12:13], 1, v243\n"
        "v_mfma_f32_16x16x32_bf16 v[108:111], v[60:63], v[216:219], v[108:111]\n"
        "v_cmp_gt_i32 s[14:15], 2, v243\n"
        "v_cmp_gt_i32 s[16:17], 3, v243\n"
        "v_mfma_f32_16x16x32_bf16 v[108:111], v[56:59], v[200:203], v[108:111]\n"
        "global_store_dwordx4 v254, a[0:3], s[70:71] offset:0 sc1\n"
        "v_cmp_gt_i32 s[20:21], 16, v243\n"
        "v_cmp_gt_i32 s[22:23], 17, v243\n"
        "v_cmp_gt_i32 s[24:25], 18, v243\n"
        "v_cmp_gt_i32 s[28:29], 19, v243\n"
        "v_mfma_f32_16x16x32_bf16 v[140:143], v[56:59], v[204:207], v[140:143]\n"
        "v_cmp_gt_i32 s[30:31], 32, v243\n"
        "v_cmp_gt_i32 s[56:57], 33, v243\n"
        "v_mfma_f32_16x16x32_bf16 v[140:143], v[60:63], v[220:223], v[140:143]\n"
        "s_waitcnt lgkmcnt(1)\n"
        "v_cmp_gt_i32 s[58:59], 34, v243\n"
        "v_cmp_gt_i32 s[60:61], 35, v243\n"
        "v_mfma_f32_16x16x32_bf16 v[136:139], v[52:55], v[220:223], v[136:139]\n"
        "s_waitcnt vmcnt(12) lgkmcnt(0)\n"
        "v_cmp_gt_i32 s[62:63], 48, v243\n"
        "v_mfma_f32_16x16x32_bf16 v[136:139], v[48:51], v[204:207], v[136:139]\n"
        "global_load_lds_dwordx4 v244, s[86:87] offset:2048\n"
        "v_mfma_f32_16x16x32_bf16 v[16:19], v[160:163], v[192:195], v[16:19]\n"
        "s_barrier\n"
        "v_mfma_f32_16x16x32_bf16 v[16:19], v[164:167], v[208:211], v[16:19]\n"
        "ds_read_b64_tr_b16 v[48:49], v226 offset:16384\n"
        "v_add_u32 v244, s91, v254\n"
        "v_mfma_f32_16x16x32_bf16 v[20:23], v[172:175], v[208:211], v[20:23]\n"
        "ds_read_b64_tr_b16 v[50:51], v227 offset:16384\n"
        "s_xor_b32 s91, s79, 0x8000\n"
        "s_add_u32 s91, s91, s90\n"
        "v_mfma_f32_16x16x32_bf16 v[20:23], v[168:171], v[192:195], v[20:23]\n"
        "ds_read_b64_tr_b16 v[52:53], v226 offset:24576\n"
        "s_lshl1_add_u32 s91, s80, s91\n"
        "v_cmp_gt_i32 s[84:85], 49, v243\n"
        "v_mfma_f32_16x16x32_bf16 v[84:87], v[168:171], v[196:199], v[84:87]\n"
        "global_load_lds_dwordx4 v245, s[86:87] offset:3072\n"
        "s_add_u32 s79, s79, s72\n"
        "v_mfma_f32_16x16x32_bf16 v[84:87], v[172:175], v[212:215], v[84:87]\n"
        "ds_read_b64_tr_b16 v[54:55], v227 offset:24576\n"
        "s_lshl_b32 s80, s41, 5\n"
        "v_add_u32 v245, s91, v254\n"
        "v_mfma_f32_16x16x32_bf16 v[80:83], v[164:167], v[212:215], v[80:83]\n"
        "ds_read_b64_tr_b16 v[60:61], v228 offset:24576\n"
        "s_add_u32 s80, s80, 256\n"
        "v_cmp_gt_i32 s[86:87], 50, v243\n"
        "v_mfma_f32_16x16x32_bf16 v[80:83], v[160:163], v[196:199], v[80:83]\n"
        "ds_read_b64_tr_b16 v[62:63], v229 offset:24576\n"
        "s_add_u32 s81, s80, s42\n"
        "v_cmp_gt_i32 s[88:89], 51, v243\n"
        "v_mfma_f32_16x16x32_bf16 v[112:115], v[160:163], v[200:203], v[112:115]\n"
        "ds_read_b64_tr_b16 v[56:57], v228 offset:16384\n"
        "s_lshl_b32 s81, s81, 7\n"
        "v_mov_b32 v242, 0x3f803f80\n"
        "v_mfma_f32_16x16x32_bf16 v[112:115], v[164:167], v[216:219], v[112:115]\n"
        "ds_read_b64_tr_b16 v[58:59], v229 offset:16384\n"
        "s_and_b32 s80, s81, 0xfffffc7f\n"
        "v_accvgpr_write_b32 a80, v242\n"
        "v_mfma_f32_16x16x32_bf16 v[116:119], v[172:175], v[216:219], v[116:119]\n"
        "global_store_dwordx4 v254, a[4:7], s[70:71] offset:1024 sc1\n"
        "s_add_u32 s70, s38, s96\n"
        "s_addc_u32 s71, s39, 0\n"
        "s_add_u32 s96, s53, 0x2000\n"
        "v_accvgpr_write_b32 a81, v242\n"
        "v_accvgpr_write_b32 a82, v242\n"
        "v_mfma_f32_16x16x32_bf16 v[116:119], v[168:171], v[200:203], v[116:119]\n"
        "s_xor_b32 s79, s79, 0x8000\n"
        "v_accvgpr_write_b32 a83, v242\n"
        "v_cndmask_b32 v186, 0, v242, vcc\n"
        "v_mfma_f32_16x16x32_bf16 v[148:151], v[168:171], v[204:207], v[148:151]\n"
        "ds_read_b64_tr_b16 v[168:169], v232 offset:16384\n"
        "v_accvgpr_write_b32 a72, v186\n"
        "v_mfma_f32_16x16x32_bf16 v[148:151], v[172:175], v[220:223], v[148:151]\n"
        "ds_read_b64_tr_b16 v[172:173], v232 offset:24576\n"
        "v_accvgpr_write_b32 a73, v186\n"
        "v_mfma_f32_16x16x32_bf16 v[144:147], v[164:167], v[220:223], v[144:147]\n"
        "ds_read_b64_tr_b16 v[164:165], v230 offset:24576\n"
        "v_accvgpr_write_b32 a74, v186\n"
        "v_mfma_f32_16x16x32_bf16 v[144:147], v[160:163], v[204:207], v[144:147]\n"
        "global_store_dwordx4 v254, a[8:11], s[70:71] offset:0 sc1\n"
        "ds_read_b64_tr_b16 v[160:161], v230 offset:16384\n"
        "ds_read_b64_tr_b16 v[162:163], v231 offset:16384\n"
        "v_accvgpr_write_b32 a75, v186\n"
        "v_mfma_f32_16x16x32_bf16 v[24:27], v[32:35], v[192:195], v[24:27]\n"
        "ds_read_b64_tr_b16 v[166:167], v231 offset:24576\n"
        "v_cmp_eq_u32 vcc, 0, v187\n"
        "v_mfma_f32_16x16x32_bf16 v[24:27], v[36:39], v[208:211], v[24:27]\n"
        "ds_read_b64_tr_b16 v[174:175], v233 offset:24576\n"
        "v_cndmask_b32 v187, 0, v242, vcc\n"
        "v_mfma_f32_16x16x32_bf16 v[28:31], v[44:47], v[208:211], v[28:31]\n"
        "ds_read_b64_tr_b16 v[170:171], v233 offset:16384\n"
        "v_accvgpr_write_b32 a76, v187\n"
        "v_mfma_f32_16x16x32_bf16 v[28:31], v[40:43], v[192:195], v[28:31]\n"
        "ds_read_b128 v[192:195], v246 offset:34816\n"
        "v_accvgpr_write_b32 a77, v187\n"
        "v_mfma_f32_16x16x32_bf16 v[92:95], v[40:43], v[196:199], v[92:95]\n"
        "ds_read_b128 v[208:211], v247 offset:34816\n"
        "v_accvgpr_write_b32 a78, v187\n"
        "v_mfma_f32_16x16x32_bf16 v[92:95], v[44:47], v[212:215], v[92:95]\n"
        "v_accvgpr_write_b32 a79, v187\n"
        "v_mov_b32 v242, 0xff800000\n"
        "v_mfma_f32_16x16x32_bf16 v[88:91], v[36:39], v[212:215], v[88:91]\n"
        "ds_read_b128 v[212:215], v247 offset:32768\n"
        "v_mfma_f32_16x16x32_bf16 v[88:91], v[32:35], v[196:199], v[88:91]\n"
        "ds_read_b128 v[196:199], v246 offset:32768\n"
        "v_mfma_f32_16x16x32_bf16 v[120:123], v[32:35], v[200:203], v[120:123]\n"
        "v_mfma_f32_16x16x32_bf16 v[120:123], v[36:39], v[216:219], v[120:123]\n"
        "v_mfma_f32_16x16x32_bf16 v[124:127], v[44:47], v[216:219], v[124:127]\n"
        "ds_read_b128 v[216:219], v247 offset:38912\n"
        "v_mfma_f32_16x16x32_bf16 v[124:127], v[40:43], v[200:203], v[124:127]\n"
        "ds_read_b128 v[200:203], v246 offset:38912\n"
        "v_mfma_f32_16x16x32_bf16 v[156:159], v[40:43], v[204:207], v[156:159]\n"
        "ds_read_b64_tr_b16 v[40:41], v228 offset:17408\n"
        "s_waitcnt lgkmcnt(5)\n"
        "v_mfma_f32_16x16x32_bf16 v[156:159], v[44:47], v[220:223], v[156:159]\n"
        "ds_read_b64_tr_b16 v[44:45], v228 offset:25600\n"
        "v_mfma_f32_16x16x32_bf16 v[152:155], v[36:39], v[220:223], v[152:155]\n"
        "ds_read_b128 v[220:223], v247 offset:36864\n"
        "v_mfma_f32_16x16x32_bf16 v[152:155], v[32:35], v[204:207], v[152:155]\n"
        "ds_read_b128 v[204:207], v246 offset:36864\n"
        "v_mfma_f32_16x16x32_bf16 v[0:3], v[48:51], v[192:195], v[0:3]\n"
        "global_store_dwordx4 v254, a[12:15], s[70:71] offset:1024 sc1\n"
        "ds_read_b64_tr_b16 v[32:33], v226 offset:17408\n"
        "ds_read_b64_tr_b16 v[34:35], v227 offset:17408\n"
        "s_add_u32 s70, s38, s96\n"
        "s_addc_u32 s71, s39, 0\n"
        "v_mfma_f32_16x16x32_bf16 v[0:3], v[52:55], v[208:211], v[0:3]\n"
        "ds_read_b64_tr_b16 v[36:37], v226 offset:25600\n"
        "s_mul_i32 s96, s40, 4224\n"
        "s_add_u32 s96, s96, 0x20240\n"
        "v_mfma_f32_16x16x32_bf16 v[4:7], v[60:63], v[208:211], v[4:7]\n"
        "ds_read_b64_tr_b16 v[38:39], v227 offset:25600\n"
        "v_mov_b32 v178, s96\n"
        "v_mfma_f32_16x16x32_bf16 v[4:7], v[56:59], v[192:195], v[4:7]\n"
        "ds_read_b64_tr_b16 v[46:47], v229 offset:25600\n"
        "s_waitcnt lgkmcnt(8)\n"
        "v_mfma_f32_16x16x32_bf16 v[68:71], v[56:59], v[196:199], v[68:71]\n"
        "ds_read_b64_tr_b16 v[42:43], v229 offset:17408\n"
        "v_writelane_b32 v180, s96, 2\n"
        "v_mfma_f32_16x16x32_bf16 v[68:71], v[60:63], v[212:215], v[68:71]\n"
        "s_add_u32 s96, s96, 16\n"
        "v_writelane_b32 v180, s96, 3\n"
        "s_add_u32 s96, s51, 4098\n"
        "v_mfma_f32_16x16x32_bf16 v[64:67], v[52:55], v[212:215], v[64:67]\n"
        "v_sub_u32 v179, s96, v178\n"
        "v_mul_lo_u32 v179, v179, v181\n"
        "v_mfma_f32_16x16x32_bf16 v[64:67], v[48:51], v[196:199], v[64:67]\n"
        "s_add_u32 s96, s51, 480\n"
        "v_add_u32 v178, v178, v179\n"
        "v_lshl_add_u32 v178, v176, 1, v178\n"
        "v_mfma_f32_16x16x32_bf16 v[96:99], v[48:51], v[200:203], v[96:99]\n"
        "s_waitcnt lgkmcnt(4)\n"
        "v_lshl_add_u32 v178, v177, 4, v178\n"
        "v_accvgpr_write_b32 a88, v178\n"
        "v_mfma_f32_16x16x32_bf16 v[96:99], v[52:55], v[216:219], v[96:99]\n"
        "v_accvgpr_write_b32 a90, v180\n"
        "v_lshlrev_b32 v179, 5, v176\n"
        "v_mfma_f32_16x16x32_bf16 v[100:103], v[60:63], v[216:219], v[100:103]\n"
        "v_lshrrev_b32 v178, 1, v177\n"
        "v_bfe_u32 v180, v254, 6, 3\n"
        "v_mfma_f32_16x16x32_bf16 v[100:103], v[56:59], v[200:203], v[100:103]\n"
        "global_store_dwordx4 v254, a[208:211], s[70:71] offset:0 sc1\n"
        "v_sub_u32 v179, s96, v179\n"
        "v_lshlrev_b32 v183, 2, v178\n"
        "v_mov_b32 v181, 0\n"
        "s_lshl_b32 s96, s54, 16\n"
        "v_mfma_f32_16x16x32_bf16 v[132:135], v[56:59], v[204:207], v[132:135]\n"
        "ds_read_b64_tr_b16 v[56:57], v232 offset:17408\n"
        "s_and_b32 s54, s54, 0xffff0000\n"
        "v_xor_b32 v183, v183, v180\n"
        "v_mfma_f32_16x16x32_bf16 v[132:135], v[60:63], v[220:223], v[132:135]\n"
        "ds_read_b64_tr_b16 v[60:61], v232 offset:25600\n"
        "v_lshl_add_u32 v179, v177, 4, v179\n"
        "v_mfma_f32_16x16x32_bf16 v[128:131], v[52:55], v[220:223], v[128:131]\n"
        "ds_read_b64_tr_b16 v[52:53], v230 offset:25600\n"
        "v_writelane_b32 v181, s96, 3\n"
        "v_mfma_f32_16x16x32_bf16 v[128:131], v[48:51], v[204:207], v[128:131]\n"
        "ds_read_b64_tr_b16 v[48:49], v230 offset:17408\n"
        "s_lshr_b32 s96, s40, 1\n"
        "v_bfe_u32 v184, v183, 1, 2\n"
        "v_mfma_f32_16x16x32_bf16 v[8:11], v[160:163], v[192:195], v[8:11]\n"
        "ds_read_b64_tr_b16 v[50:51], v231 offset:17408\n"
        "s_lshl_b32 s96, s96, 10\n"
        "v_accvgpr_write_b32 a89, v179\n"
        "v_mfma_f32_16x16x32_bf16 v[8:11], v[164:167], v[208:211], v[8:11]\n"
        "ds_read_b64_tr_b16 v[54:55], v231 offset:25600\n"
        "v_xor_b32 v184, 0, v184\n"
        "v_mfma_f32_16x16x32_bf16 v[12:15], v[172:175], v[208:211], v[12:15]\n"
        "ds_read_b64_tr_b16 v[62:63], v233 offset:25600\n"
        "v_accvgpr_write_b32 a91, v181\n"
        "v_mfma_f32_16x16x32_bf16 v[12:15], v[168:171], v[192:195], v[12:15]\n"
        "ds_read_b64_tr_b16 v[58:59], v233 offset:17408\n"
        "v_lshlrev_b32 v179, 11, v178\n"
        "v_mfma_f32_16x16x32_bf16 v[76:79], v[168:171], v[196:199], v[76:79]\n"
        "v_lshlrev_b32 v184, 5, v184\n"
        "v_bfe_u32 v181, v254, 4, 2\n"
        "v_mfma_f32_16x16x32_bf16 v[76:79], v[172:175], v[212:215], v[76:79]\n"
        "v_add_u32 v179, s96, v179\n"
        "v_lshl_add_u32 v183, v183, 7, v184\n"
        "v_mfma_f32_16x16x32_bf16 v[72:75], v[164:167], v[212:215], v[72:75]\n"
        "s_and_b32 s96, s40, 1\n"
        "v_lshl_add_u32 v183, v181, 3, v183\n"
        "s_lshl_b32 s96, s96, 6\n"
        "v_mfma_f32_16x16x32_bf16 v[72:75], v[160:163], v[196:199], v[72:75]\n"
        "v_add_u32 v182, v183, v179\n"
        "v_xor_b32 v182, s96, v182\n"
        "v_mfma_f32_16x16x32_bf16 v[104:107], v[160:163], v[200:203], v[104:107]\n"
        "s_lshl_b32 s96, s41, 12\n"
        "s_add_u32 s96, s96, 32768\n"
        "v_lshl_add_u32 v188, v188, 7, s96\n"
        "v_mfma_f32_16x16x32_bf16 v[104:107], v[164:167], v[216:219], v[104:107]\n"
        "s_add_u32 s96, s53, 0x0\n"
        "v_accvgpr_write_b32 a92, v182\n"
        "v_accvgpr_write_b32 a94, v188\n"
        "v_mfma_f32_16x16x32_bf16 v[108:111], v[172:175], v[216:219], v[108:111]\n"
        "v_mfma_f32_16x16x32_bf16 v[108:111], v[168:171], v[200:203], v[108:111]\n"
        "global_store_dwordx4 v254, a[240:243], s[70:71] offset:1024 sc1\n"
        "s_waitcnt vmcnt(5) lgkmcnt(0)\n"
        "s_barrier\n"
        "v_mfma_f32_16x16x32_bf16 v[140:143], v[168:171], v[204:207], v[140:143]\n"
        "ds_write_b128 v244, a[200:203] offset:0\n"
        "v_mfma_f32_16x16x32_bf16 v[140:143], v[172:175], v[220:223], v[140:143]\n"
        "ds_read_b64_tr_b16 v[172:173], v228 offset:40960\n"
        "v_mfma_f32_16x16x32_bf16 v[136:139], v[164:167], v[220:223], v[136:139]\n"
        "ds_read_b64_tr_b16 v[164:165], v226 offset:40960\n"
        "v_mfma_f32_16x16x32_bf16 v[136:139], v[160:163], v[204:207], v[136:139]\n"
        "ds_write_b128 v244, a[204:207] offset:1024\n"
        "v_mfma_f32_16x16x32_bf16 v[16:19], v[32:35], v[192:195], v[16:19]\n"
        "ds_read_b64_tr_b16 v[160:161], v226 offset:32768\n"
        "global_load_dword v253, v252, s[46:47] offset:0\n"
        "v_mfma_f32_16x16x32_bf16 v[16:19], v[36:39], v[208:211], v[16:19]\n"
        "ds_read_b64_tr_b16 v[162:163], v227 offset:32768\n"
        "v_mfma_f32_16x16x32_bf16 v[20:23], v[44:47], v[208:211], v[20:23]\n"
        "ds_write_b128 v244, a[160:163] offset:4096\n"
        "v_mfma_f32_16x16x32_bf16 v[20:23], v[40:43], v[192:195], v[20:23]\n"
        "ds_read_b64_tr_b16 v[166:167], v227 offset:40960\n"
        "v_mfma_f32_16x16x32_bf16 v[84:87], v[40:43], v[196:199], v[84:87]\n"
        "ds_read_b64_tr_b16 v[174:175], v229 offset:40960\n"
        "v_mfma_f32_16x16x32_bf16 v[84:87], v[44:47], v[212:215], v[84:87]\n"
        "ds_write_b128 v244, a[164:167] offset:5120\n"
        "v_mfma_f32_16x16x32_bf16 v[80:83], v[36:39], v[212:215], v[80:83]\n"
        "ds_read_b64_tr_b16 v[168:169], v228 offset:32768\n"
        "v_mfma_f32_16x16x32_bf16 v[80:83], v[32:35], v[196:199], v[80:83]\n"
        "ds_read_b64_tr_b16 v[170:171], v229 offset:32768\n"
        "v_mfma_f32_16x16x32_bf16 v[112:115], v[32:35], v[200:203], v[112:115]\n"
        "ds_write_b128 v244, a[168:171] offset:8192\n"
        "v_mfma_f32_16x16x32_bf16 v[112:115], v[36:39], v[216:219], v[112:115]\n"
        "global_store_dwordx4 v254, a[212:215], s[70:71] offset:2048 sc1\n"
        "v_mfma_f32_16x16x32_bf16 v[116:119], v[44:47], v[216:219], v[116:119]\n"
        "v_mfma_f32_16x16x32_bf16 v[116:119], v[40:43], v[200:203], v[116:119]\n"
        "ds_write_b128 v244, a[172:175] offset:9216\n"
        "v_mfma_f32_16x16x32_bf16 v[148:151], v[40:43], v[204:207], v[148:151]\n"
        "ds_read_b64_tr_b16 v[40:41], v232 offset:32768\n"
        "v_mfma_f32_16x16x32_bf16 v[148:151], v[44:47], v[220:223], v[148:151]\n"
        "ds_read_b64_tr_b16 v[44:45], v232 offset:40960\n"
        "v_mfma_f32_16x16x32_bf16 v[144:147], v[36:39], v[220:223], v[144:147]\n"
        "ds_write_b128 v244, a[0:3] offset:12288\n"
        "v_mfma_f32_16x16x32_bf16 v[144:147], v[32:35], v[204:207], v[144:147]\n"
        "ds_read_b64_tr_b16 v[32:33], v230 offset:32768\n"
        "v_mfma_f32_16x16x32_bf16 v[24:27], v[48:51], v[192:195], v[24:27]\n"
        "ds_read_b64_tr_b16 v[34:35], v231 offset:32768\n"
        "v_mfma_f32_16x16x32_bf16 v[24:27], v[52:55], v[208:211], v[24:27]\n"
        "ds_write_b128 v244, a[4:7] offset:13312\n"
        "v_mfma_f32_16x16x32_bf16 v[28:31], v[60:63], v[208:211], v[28:31]\n"
        "ds_read_b64_tr_b16 v[36:37], v230 offset:40960\n"
        "v_xor_b32 v244, 0xc000, v244\n"
        "v_mfma_f32_16x16x32_bf16 v[28:31], v[56:59], v[192:195], v[28:31]\n"
        "ds_read_b64_tr_b16 v[38:39], v231 offset:40960\n"
        "v_mfma_f32_16x16x32_bf16 v[92:95], v[56:59], v[196:199], v[92:95]\n"
        "ds_read_b64_tr_b16 v[46:47], v233 offset:40960\n"
        "v_mfma_f32_16x16x32_bf16 v[92:95], v[60:63], v[212:215], v[92:95]\n"
        "ds_read_b64_tr_b16 v[42:43], v233 offset:32768\n"
        "v_mfma_f32_16x16x32_bf16 v[88:91], v[52:55], v[212:215], v[88:91]\n"
        "ds_read_b128 v[192:195], v246 offset:2048\n"
        "v_mfma_f32_16x16x32_bf16 v[88:91], v[48:51], v[196:199], v[88:91]\n"
        "ds_read_b128 v[208:211], v247 offset:2048\n"
        "v_mfma_f32_16x16x32_bf16 v[120:123], v[48:51], v[200:203], v[120:123]\n"
        "ds_read_b128 v[196:199], v246 offset:0\n"
        "v_mfma_f32_16x16x32_bf16 v[120:123], v[52:55], v[216:219], v[120:123]\n"
        "ds_read_b128 v[212:215], v247 offset:0\n"
        "v_mfma_f32_16x16x32_bf16 v[124:127], v[60:63], v[216:219], v[124:127]\n"
        "global_store_dwordx4 v254, a[244:247], s[70:71] offset:3072 sc1\n"
        "ds_read_b128 v[216:219], v247 offset:6144\n"
        "s_add_u32 s70, s38, s96\n"
        "s_addc_u32 s71, s39, 0\n"
        "s_add_u32 s96, s53, 0x0\n"
        "s_add_u32 s96, s96, s75\n"
        "v_mfma_f32_16x16x32_bf16 v[124:127], v[56:59], v[200:203], v[124:127]\n"
        "ds_read_b128 v[200:203], v246 offset:6144\n"
        "v_mfma_f32_16x16x32_bf16 v[156:159], v[56:59], v[204:207], v[156:159]\n"
        "ds_read_b64_tr_b16 v[56:57], v228 offset:33792\n"
        "v_mfma_f32_16x16x32_bf16 v[156:159], v[60:63], v[220:223], v[156:159]\n"
        "ds_read_b64_tr_b16 v[60:61], v228 offset:41984\n"
        "s_waitcnt lgkmcnt(7)\n"
        "v_mfma_f32_16x16x32_bf16 v[152:155], v[52:55], v[220:223], v[152:155]\n"
        "ds_read_b128 v[220:223], v247 offset:4096\n"
        "s_waitcnt lgkmcnt(7)\n"
        "v_mfma_f32_16x16x32_bf16 v[152:155], v[48:51], v[204:207], v[152:155]\n"
        "ds_read_b128 v[204:207], v246 offset:4096\n"
        "v_mfma_f32_16x16x32_bf16 v[0:3], v[160:163], v[192:195], v[0:3]\n"
        "ds_read_b64_tr_b16 v[48:49], v226 offset:33792\n"
        "s_waitcnt lgkmcnt(7)\n"
        "v_mfma_f32_16x16x32_bf16 v[0:3], v[164:167], v[208:211], v[0:3]\n"
        "LHA9_%=:\n"
        "s_cbranch_execz LHRA_%=\n"
        "ds_read_b64_tr_b16 v[50:51], v227 offset:33792\n"
        "v_mfma_f32_16x16x32_bf16 v[4:7], v[172:175], v[208:211], v[4:7]\n"
        "ds_read_b64_tr_b16 v[52:53], v226 offset:41984\n"
        "v_mfma_f32_16x16x32_bf16 v[4:7], v[168:171], v[192:195], v[4:7]\n"
        "ds_read_b64_tr_b16 v[54:55], v227 offset:41984\n"
        "v_mfma_f32_16x16x32_bf16 v[68:71], v[168:171], v[196:199], v[68:71]\n"
        "ds_read_b64_tr_b16 v[62:63], v229 offset:41984\n"
        "s_waitcnt lgkmcnt(8)\n"
        "v_mfma_f32_16x16x32_bf16 v[68:71], v[172:175], v[212:215], v[68:71]\n"
        "ds_read_b64_tr_b16 v[58:59], v229 offset:33792\n"
        "v_cvt_pk_bf16_f32 v0, v0, v1\n"
        "v_mfma_f32_16x16x32_bf16 v[64:67], v[164:167], v[212:215], v[64:67]\n"
        "ds_write_b128 v244, a[8:11] offset:0\n"
        "v_mfma_f32_16x16x32_bf16 v[64:67], v[160:163], v[196:199], v[64:67]\n"
        "global_store_dwordx4 v254, a[216:219], s[70:71] offset:0 sc1\n"
        "v_cvt_pk_bf16_f32 v1, v2, v3\n"
        "v_cvt_pk_bf16_f32 v2, v4, v5\n"
        "v_cvt_pk_bf16_f32 v3, v6, v7\n"
        "v_mfma_f32_16x16x32_bf16 v[96:99], v[160:163], v[200:203], v[96:99]\n"
        "s_waitcnt lgkmcnt(5)\n"
        "v_mfma_f32_16x16x32_bf16 v[96:99], v[164:167], v[216:219], v[96:99]\n"
        "ds_write_b128 v244, a[12:15] offset:1024\n"
        "v_mfma_f32_16x16x32_bf16 v[100:103], v[172:175], v[216:219], v[100:103]\n"
        "v_cvt_pk_bf16_f32 v64, v64, v65\n"
        "v_cvt_pk_bf16_f32 v65, v66, v67\n"
        "v_mfma_f32_16x16x32_bf16 v[100:103], v[168:171], v[200:203], v[100:103]\n"
        "v_cvt_pk_bf16_f32 v66, v68, v69\n"
        "v_cvt_pk_bf16_f32 v67, v70, v71\n"
        "v_mfma_f32_16x16x32_bf16 v[132:135], v[168:171], v[204:207], v[132:135]\n"
        "ds_write_b128 v244, a[192:195] offset:4096\n"
        "v_mfma_f32_16x16x32_bf16 v[132:135], v[172:175], v[220:223], v[132:135]\n"
        "ds_read_b64_tr_b16 v[172:173], v232 offset:41984\n"
        "v_cvt_pk_bf16_f32 v96, v96, v97\n"
        "v_mfma_f32_16x16x32_bf16 v[128:131], v[164:167], v[220:223], v[128:131]\n"
        "ds_read_b64_tr_b16 v[164:165], v230 offset:41984\n"
        "v_cvt_pk_bf16_f32 v97, v98, v99\n"
        "v_mfma_f32_16x16x32_bf16 v[128:131], v[160:163], v[204:207], v[128:131]\n"
        "ds_write_b128 v244, a[196:199] offset:5120\n"
        "v_mfma_f32_16x16x32_bf16 v[8:11], v[32:35], v[192:195], v[8:11]\n"
        "ds_read_b64_tr_b16 v[160:161], v230 offset:33792\n"
        "v_cvt_pk_bf16_f32 v98, v100, v101\n"
        "v_mfma_f32_16x16x32_bf16 v[8:11], v[36:39], v[208:211], v[8:11]\n"
        "ds_read_b64_tr_b16 v[162:163], v231 offset:33792\n"
        "v_cvt_pk_bf16_f32 v99, v102, v103\n"
        "v_mfma_f32_16x16x32_bf16 v[12:15], v[44:47], v[208:211], v[12:15]\n"
        "ds_read_b64_tr_b16 v[166:167], v231 offset:41984\n"
        "v_cvt_pk_bf16_f32 v128, v128, v129\n"
        "v_mfma_f32_16x16x32_bf16 v[12:15], v[40:43], v[192:195], v[12:15]\n"
        "ds_read_b64_tr_b16 v[174:175], v233 offset:41984\n"
        "v_cvt_pk_bf16_f32 v129, v130, v131\n"
        "v_mfma_f32_16x16x32_bf16 v[76:79], v[40:43], v[196:199], v[76:79]\n"
        "ds_read_b64_tr_b16 v[168:169], v232 offset:33792\n"
        "v_cvt_pk_bf16_f32 v4, v8, v9\n"
        "v_mfma_f32_16x16x32_bf16 v[76:79], v[44:47], v[212:215], v[76:79]\n"
        "ds_read_b64_tr_b16 v[170:171], v233 offset:33792\n"
        "v_cvt_pk_bf16_f32 v5, v10, v11\n"
        "v_mfma_f32_16x16x32_bf16 v[72:75], v[36:39], v[212:215], v[72:75]\n"
        "ds_write_b128 v244, a[232:235] offset:8192\n"
        "v_mfma_f32_16x16x32_bf16 v[72:75], v[32:35], v[196:199], v[72:75]\n"
        "global_store_dwordx4 v254, a[248:251], s[70:71] offset:1024 sc1\n"
        "s_add_u32 s82, s82, 1\n"
        "v_cvt_pk_bf16_f32 v6, v12, v13\n"
        "v_cvt_pk_bf16_f32 v7, v14, v15\n"
        "v_cvt_pk_bf16_f32 v70, v76, v77\n"
        "v_cvt_pk_bf16_f32 v71, v78, v79\n"
        "v_mfma_f32_16x16x32_bf16 v[104:107], v[32:35], v[200:203], v[104:107]\n"
        "s_cmp_ge_u32 s82, s44\n"
        "s_addc_u32 s90, s82, 0\n"
        "v_cvt_pk_bf16_f32 v68, v72, v73\n"
        "v_mfma_f32_16x16x32_bf16 v[104:107], v[36:39], v[216:219], v[104:107]\n"
        "ds_write_b128 v244, a[236:239] offset:9216\n"
        "v_mfma_f32_16x16x32_bf16 v[108:111], v[44:47], v[216:219], v[108:111]\n"
        "s_lshl_b32 s90, s90, 15\n"
        "v_cvt_pk_bf16_f32 v69, v74, v75\n"
        "v_cvt_pk_bf16_f32 v131, v134, v135\n"
        "v_mfma_f32_16x16x32_bf16 v[108:111], v[40:43], v[200:203], v[108:111]\n"
        "s_waitcnt lgkmcnt(2)\n"
        "v_cvt_pk_bf16_f32 v130, v132, v133\n"
        "v_cvt_pk_bf16_f32 v100, v104, v105\n"
        "v_mfma_f32_16x16x32_bf16 v[140:143], v[40:43], v[204:207], v[140:143]\n"
        "ds_write_b128 v244, a[224:227] offset:12288\n"
        "v_mfma_f32_16x16x32_bf16 v[140:143], v[44:47], v[220:223], v[140:143]\n"
        "s_barrier\n"
        "v_mfma_f32_16x16x32_bf16 v[136:139], v[36:39], v[220:223], v[136:139]\n"
        "s_add_u32 s68, s64, s90\n"
        "s_addc_u32 s69, s65, 0\n"
        "v_cndmask_b32 v36, 0, v242, s[10:11]\n"
        "v_mfma_f32_16x16x32_bf16 v[136:139], v[32:35], v[204:207], v[136:139]\n"
        "ds_write_b128 v244, a[228:231] offset:13312\n"
        "v_mfma_f32_16x16x32_bf16 v[16:19], v[48:51], v[192:195], v[16:19]\n"
        "global_store_dwordx4 v254, a[220:223], s[70:71] offset:2048 sc1\n"
        "s_xor_b32 s78, s78, 0x8000\n"
        "v_cndmask_b32 v37, 0, v242, s[12:13]\n"
        "v_cndmask_b32 v38, 0, v242, s[14:15]\n"
        "v_cndmask_b32 v39, 0, v242, s[16:17]\n"
        "v_cvt_pk_bf16_f32 v134, v140, v141\n"
        "v_mfma_f32_16x16x32_bf16 v[16:19], v[52:55], v[208:211], v[16:19]\n"
        "s_lshr_b32 s52, s41, 1\n"
        "v_cvt_pk_bf16_f32 v101, v106, v107\n"
        "v_cvt_pk_bf16_f32 v102, v108, v109\n"
        "v_mfma_f32_16x16x32_bf16 v[20:23], v[60:63], v[208:211], v[20:23]\n"
        "ds_write_b128 v245, a[176:179] offset:0\n"
        "v_mfma_f32_16x16x32_bf16 v[20:23], v[56:59], v[192:195], v[20:23]\n"
        "v_cvt_pk_bf16_f32 v103, v110, v111\n"
        "s_nop 0\n"
        "v_cvt_pk_bf16_f32 v8, v16, v17\n"
        "v_mfma_f32_16x16x32_bf16 v[84:87], v[56:59], v[196:199], v[84:87]\n"
        "s_barrier\n"
        "v_mfma_f32_16x16x32_bf16 v[84:87], v[60:63], v[212:215], v[84:87]\n"
        "ds_read_b128 a[96:99], v240 offset:0\n"
        "s_lshl_b32 s52, s52, 11\n"
        "v_cvt_pk_bf16_f32 v9, v18, v19\n"
        "v_mfma_f32_16x16x32_bf16 v[80:83], v[52:55], v[212:215], v[80:83]\n"
        "ds_read_b128 a[100:103], v240 offset:1024\n"
        "v_cvt_pk_bf16_f32 v10, v20, v21\n"
        "v_mfma_f32_16x16x32_bf16 v[80:83], v[48:51], v[196:199], v[80:83]\n"
        "ds_read_b128 a[104:107], v240 offset:2048\n"
        "v_cvt_pk_bf16_f32 v11, v22, v23\n"
        "v_mfma_f32_16x16x32_bf16 v[112:115], v[48:51], v[200:203], v[112:115]\n"
        "ds_read_b128 a[108:111], v240 offset:3072\n"
        "v_cvt_pk_bf16_f32 v74, v84, v85\n"
        "v_mfma_f32_16x16x32_bf16 v[112:115], v[52:55], v[216:219], v[112:115]\n"
        "ds_write_b128 v245, a[180:183] offset:1024\n"
        "v_mfma_f32_16x16x32_bf16 v[116:119], v[60:63], v[216:219], v[116:119]\n"
        "ds_read_b128 a[112:115], v240 offset:4096\n"
        "v_cvt_pk_bf16_f32 v72, v80, v81\n"
        "v_mfma_f32_16x16x32_bf16 v[116:119], v[56:59], v[200:203], v[116:119]\n"
        "ds_read_b128 a[116:119], v240 offset:5120\n"
        "v_cvt_pk_bf16_f32 v73, v82, v83\n"
        "v_mfma_f32_16x16x32_bf16 v[148:151], v[56:59], v[204:207], v[148:151]\n"
        "ds_read_b128 a[120:123], v240 offset:6144\n"
        "v_cvt_pk_bf16_f32 v75, v86, v87\n"
        "v_mfma_f32_16x16x32_bf16 v[148:151], v[60:63], v[220:223], v[148:151]\n"
        "ds_write_b128 v245, a[184:187] offset:2048\n"
        "v_mfma_f32_16x16x32_bf16 v[144:147], v[52:55], v[220:223], v[144:147]\n"
        "ds_read_b128 a[124:127], v240 offset:7168\n"
        "v_cndmask_b32 v52, 0, v242, s[20:21]\n"
        "v_mfma_f32_16x16x32_bf16 v[144:147], v[48:51], v[204:207], v[144:147]\n"
        "global_store_dwordx4 v254, a[252:255], s[70:71] offset:3072 sc1\n"
        "s_add_u32 s70, s38, s96\n"
        "s_addc_u32 s71, s39, 0\n"
        "s_add_u32 s96, s53, 0x2000\n"
        "v_cndmask_b32 v53, 0, v242, s[22:23]\n"
        "v_cndmask_b32 v54, 0, v242, s[24:25]\n"
        "v_mfma_f32_16x16x32_bf16 v[24:27], v[160:163], v[192:195], v[24:27]\n"
        "ds_write_b128 v245, a[188:191] offset:3072\n"
        "v_mfma_f32_16x16x32_bf16 v[24:27], v[164:167], v[208:211], v[24:27]\n"
        "s_add_u32 s96, s96, s75\n"
        "v_cndmask_b32 v55, 0, v242, s[28:29]\n"
        "v_cvt_pk_bf16_f32 v104, v112, v113\n"
        "v_mfma_f32_16x16x32_bf16 v[28:31], v[172:175], v[208:211], v[28:31]\n"
        "global_store_dwordx4 v254, a[180:183], s[70:71] offset:1024 sc1\n"
        "s_add_u32 s74, s68, 0x1000\n"
        "s_addc_u32 s75, s69, 0\n"
        "s_lshl2_add_u32 s53, s42, s40\n"
        "v_cvt_pk_bf16_f32 v105, v114, v115\n"
        "v_cvt_pk_bf16_f32 v12, v24, v25\n"
        "v_mfma_f32_16x16x32_bf16 v[28:31], v[168:171], v[192:195], v[28:31]\n"
        "ds_write_b128 v245, a[16:19] offset:8192\n"
        "v_mfma_f32_16x16x32_bf16 v[92:95], v[168:171], v[196:199], v[92:95]\n"
        "s_add_u32 s52, s52, s53\n"
        "v_cvt_pk_bf16_f32 v13, v26, v27\n"
        "v_cvt_pk_bf16_f32 v106, v116, v117\n"
        "v_mfma_f32_16x16x32_bf16 v[92:95], v[172:175], v[212:215], v[92:95]\n"
        "s_waitcnt lgkmcnt(2)\n"
        "v_cvt_pk_bf16_f32 v107, v118, v119\n"
        "v_cvt_pk_bf16_f32 v14, v28, v29\n"
        "v_mfma_f32_16x16x32_bf16 v[88:91], v[164:167], v[212:215], v[88:91]\n"
        "ds_write_b128 v245, a[20:23] offset:9216\n"
        "v_mfma_f32_16x16x32_bf16 v[88:91], v[160:163], v[196:199], v[88:91]\n"
        "s_add_u32 s52, s52, 0\n"
        "v_cvt_pk_bf16_f32 v15, v30, v31\n"
        "v_cvt_pk_bf16_f32 v78, v92, v93\n"
        "v_mfma_f32_16x16x32_bf16 v[120:123], v[160:163], v[200:203], v[120:123]\n"
        "s_lshl_b32 s52, s52, 10\n"
        "v_cvt_pk_bf16_f32 v79, v94, v95\n"
        "v_cvt_pk_bf16_f32 v132, v136, v137\n"
        "v_mfma_f32_16x16x32_bf16 v[32:35], a[96:99], v[0:3], 0\n"
        "ds_write_b128 v245, a[24:27] offset:10240\n"
        "v_mfma_f32_16x16x32_bf16 v[32:35], a[100:103], v[4:7], v[32:35]\n"
        "s_add_u32 s26, s18, s52\n"
        "s_addc_u32 s27, s19, 0\n"
        "v_cvt_pk_bf16_f32 v76, v88, v89\n"
        "v_mfma_f32_16x16x32_bf16 v[32:35], a[104:107], v[8:11], v[32:35]\n"
        "s_add_u32 s76, s66, s90\n"
        "s_addc_u32 s77, s67, 0\n"
        "v_cvt_pk_bf16_f32 v77, v90, v91\n"
        "v_mfma_f32_16x16x32_bf16 v[32:35], a[108:111], v[12:15], v[32:35]\n"
        "ds_write_b128 v245, a[28:31] offset:11264\n"
        "v_mfma_f32_16x16x32_bf16 v[120:123], v[164:167], v[216:219], v[120:123]\n"
        "v_xor_b32 v245, 0xc000, v245\n"
        "v_cvt_pk_bf16_f32 v133, v138, v139\n"
        "v_mfma_f32_16x16x32_bf16 v[124:127], v[172:175], v[216:219], v[124:127]\n"
        "v_cvt_pk_bf16_f32 v135, v142, v143\n"
        "v_cvt_pk_bf16_f32 v136, v144, v145\n"
        "v_mfma_f32_16x16x32_bf16 v[124:127], v[168:171], v[200:203], v[124:127]\n"
        "v_max_f32 v246, v32, v33\n"
        "v_max3_f32 v246, v34, v35, v246\n"
        "v_mfma_f32_16x16x32_bf16 v[156:159], v[168:171], v[204:207], v[156:159]\n"
        "v_mov_b32 v247, v246\n"
        "v_cvt_pk_bf16_f32 v108, v120, v121\n"
        "v_mfma_f32_16x16x32_bf16 v[36:39], a[108:111], v[76:79], v[36:39]\n"
        "v_permlane16_swap_b32 v246, v247\n"
        "v_mfma_f32_16x16x32_bf16 v[36:39], a[104:107], v[72:75], v[36:39]\n"
        "ds_read_b128 a[104:107], v240 offset:10240\n"
        "v_max_f32 v246, v246, v247\n"
        "v_mfma_f32_16x16x32_bf16 v[36:39], a[100:103], v[68:71], v[36:39]\n"
        "ds_read_b128 a[100:103], v240 offset:9216\n"
        "v_mov_b32 v247, v246\n"
        "v_mfma_f32_16x16x32_bf16 v[36:39], a[96:99], v[64:67], v[36:39]\n"
        "global_store_dwordx4 v254, a[176:179], s[70:71] offset:0 sc1\n"
        "ds_read_b128 a[96:99], v240 offset:8192\n"
        "s_waitcnt lgkmcnt(3)\n"
        "v_permlane32_swap_b32 v246, v247\n"
        "v_mfma_f32_16x16x32_bf16 v[156:159], v[172:175], v[220:223], v[156:159]\n"
        "ds_read_b128 a[108:111], v240 offset:11264\n"
        "v_max_f32 v246, v246, v247\n"
        "v_mfma_f32_16x16x32_bf16 v[152:155], v[164:167], v[220:223], v[152:155]\n"
        "v_sub_f32 v224, 0, v246\n"
        "v_sub_f32 v225, 0, v246\n"
        "v_mfma_f32_16x16x32_bf16 v[152:155], v[160:163], v[204:207], v[152:155]\n"
        "v_sub_f32 v226, 0, v246\n"
        "v_sub_f32 v227, 0, v246\n"
        "v_mfma_f32_16x16x32_bf16 v[48:51], a[112:115], v[0:3], 0\n"
        "v_max_f32 v248, v36, v37\n"
        "v_max3_f32 v248, v38, v39, v248\n"
        "v_mfma_f32_16x16x32_bf16 v[48:51], a[116:119], v[4:7], v[48:51]\n"
        "v_mov_b32 v249, v248\n"
        "v_add_f32 v32, v32, v224\n"
        "v_mfma_f32_16x16x32_bf16 v[48:51], a[120:123], v[8:11], v[48:51]\n"
        "v_permlane16_swap_b32 v248, v249\n"
        "v_mfma_f32_16x16x32_bf16 v[48:51], a[124:127], v[12:15], v[48:51]\n"
        "v_max_f32 v248, v248, v249\n"
        "v_mov_b32 v249, v248\n"
        "v_mfma_f32_16x16x32_bf16 v[52:55], a[124:127], v[76:79], v[52:55]\n"
        "global_store_dwordx4 v254, a[184:187], s[70:71] offset:2048 sc1\n"
        "s_waitcnt lgkmcnt(1)\n"
        "v_permlane32_swap_b32 v248, v249\n"
        "v_max_f32 v248, v248, v249\n"
        "v_mfma_f32_16x16x32_bf16 v[52:55], a[120:123], v[72:75], v[52:55]\n"
        "ds_read_b128 a[120:123], v240 offset:14336\n"
        "v_sub_f32 v228, 0, v248\n"
        "v_mfma_f32_16x16x32_bf16 v[52:55], a[116:119], v[68:71], v[52:55]\n"
        "ds_read_b128 a[116:119], v240 offset:13312\n"
        "v_sub_f32 v229, 0, v248\n"
        "v_mfma_f32_16x16x32_bf16 v[52:55], a[112:115], v[64:67], v[52:55]\n"
        "v_sub_f32 v230, 0, v248\n"
        "v_sub_f32 v231, 0, v248\n"
        "s_barrier\n"
        "ds_read_b128 a[144:147], v241 offset:0\n"
        "v_exp_f32 v32, v32\n"
        "v_add_f32 v33, v33, v224\n"
        "v_exp_f32 v33, v33\n"
        "ds_read_b128 a[148:151], v241 offset:1024\n"
        "v_cvt_pk_bf16_f32 v16, v32, v33\n"
        "v_add_f32 v34, v34, v224\n"
        "v_exp_f32 v34, v34\n"
        "v_add_f32 v35, v35, v224\n"
        "v_exp_f32 v35, v35\n"
        "ds_read_b128 a[152:155], v241 offset:2048\n"
        "v_cvt_pk_bf16_f32 v17, v34, v35\n"
        "v_mfma_f32_16x16x32_bf16 v[32:35], a[96:99], v[0:3], v[224:227]\n"
        "global_store_dwordx4 v254, a[188:191], s[70:71] offset:3072 sc1\n"
        "s_waitcnt lgkmcnt(4)\n"
        "v_add_f32 v36, v36, v228\n"
        "v_exp_f32 v36, v36\n"
        "v_mfma_f32_16x16x32_bf16 v[32:35], a[100:103], v[4:7], v[32:35]\n"
        "ds_read_b128 a[112:115], v240 offset:12288\n"
        "v_add_f32 v37, v37, v228\n"
        "v_mfma_f32_16x16x32_bf16 v[32:35], a[104:107], v[8:11], v[32:35]\n"
        "v_exp_f32 v37, v37\n"
        "v_mfma_f32_16x16x32_bf16 v[32:35], a[108:111], v[12:15], v[32:35]\n"
        "v_cvt_pk_bf16_f32 v20, v36, v37\n"
        "v_add_f32 v38, v38, v228\n"
        "v_exp_f32 v38, v38\n"
        "v_add_f32 v39, v39, v228\n"
        "v_exp_f32 v39, v39\n"
        "ds_read_b128 a[156:159], v241 offset:3072\n"
        "v_cvt_pk_bf16_f32 v21, v38, v39\n"
        "ds_read_b128 a[128:131], v241 offset:4096\n"
        "v_add_f32 v48, v48, v224\n"
        "v_exp_f32 v48, v48\n"
        "v_add_f32 v49, v49, v224\n"
        "v_exp_f32 v49, v49\n"
        "ds_read_b128 a[124:127], v240 offset:15360\n"
        "v_cvt_pk_bf16_f32 v18, v48, v49\n"
        "ds_read_b128 a[132:135], v241 offset:5120\n"
        "s_add_u32 s70, s38, s96\n"
        "s_addc_u32 s71, s39, 0\n"
        "global_store_dwordx4 v254, a[16:19], s[70:71] offset:0 sc1\n"
        "v_add_f32 v50, v50, v224\n"
        "v_exp_f32 v50, v50\n"
        "v_add_f32 v51, v51, v224\n"
        "v_exp_f32 v51, v51\n"
        "ds_read_b128 a[136:139], v241 offset:6144\n"
        "v_cvt_pk_bf16_f32 v19, v50, v51\n"
        "s_waitcnt lgkmcnt(5)\n"
        "v_mfma_f32_16x16x32_bf16 v[48:51], a[112:115], v[0:3], v[224:227]\n"
        "ds_read_b128 a[140:143], v241 offset:7168\n"
        "s_mul_i32 s96, s40, 4224\n"
        "v_cndmask_b32 v36, v228, v242, s[30:31]\n"
        "v_mfma_f32_16x16x32_bf16 v[48:51], a[116:119], v[4:7], v[48:51]\n"
        "v_cndmask_b32 v37, v228, v242, s[56:57]\n"
        "v_cndmask_b32 v38, v228, v242, s[58:59]\n"
        "v_mfma_f32_16x16x32_bf16 v[48:51], a[120:123], v[8:11], v[48:51]\n"
        "v_cvt_pk_bf16_f32 v141, v154, v155\n"
        "v_cndmask_b32 v39, v228, v242, s[60:61]\n"
        "s_nop 1\n"
        "v_mfma_f32_16x16x32_bf16 v[36:39], a[108:111], v[76:79], v[36:39]\n"
        "ds_read_b128 a[108:111], v240 offset:19456\n"
        "v_bfe_u32 v154, v254, 4, 1\n"
        "v_mfma_f32_16x16x32_bf16 v[36:39], a[104:107], v[72:75], v[36:39]\n"
        "ds_read_b128 a[104:107], v240 offset:18432\n"
        "v_mul_u32_u24 v154, 0xaaaa, v154\n"
        "v_mfma_f32_16x16x32_bf16 v[36:39], a[100:103], v[68:71], v[36:39]\n"
        "s_waitcnt lgkmcnt(4)\n"
        "v_add_u32 v154, 0x4444, v154\n"
        "v_add_f32 v52, v52, v228\n"
        "v_mfma_f32_16x16x32_bf16 v[36:39], a[96:99], v[64:67], v[36:39]\n"
        "v_exp_f32 v52, v52\n"
        "v_mfma_f32_16x16x32_bf16 v[48:51], a[124:127], v[12:15], v[48:51]\n"
        "ds_read_b128 a[96:99], v240 offset:16384\n"
        "v_add_f32 v53, v53, v228\n"
        "v_mfma_f32_16x16x32_bf16 v[160:163], a[144:147], v[16:19], 0\n"
        "v_exp_f32 v53, v53\n"
        "v_mfma_f32_16x16x32_bf16 v[164:167], a[148:151], v[16:19], 0\n"
        "ds_read_b128 a[100:103], v240 offset:17408\n"
        "v_cvt_pk_bf16_f32 v22, v52, v53\n"
        "v_mfma_f32_16x16x32_bf16 v[168:171], a[152:155], v[16:19], 0\n"
        "v_add_f32 v54, v54, v228\n"
        "v_add_f32 v55, v55, v228\n"
        "v_mfma_f32_16x16x32_bf16 v[172:175], a[156:159], v[16:19], 0\n"
        "v_exp_f32 v54, v54\n"
        "v_mfma_f32_16x16x32_bf16 v[176:179], a[128:131], v[16:19], 0\n"
        "v_exp_f32 v55, v55\n"
        "v_mfma_f32_16x16x32_bf16 v[180:183], a[132:135], v[16:19], 0\n"
        "s_waitcnt lgkmcnt(2)\n"
        "v_cvt_pk_bf16_f32 v23, v54, v55\n"
        "s_nop 1\n"
        "v_smfmac_f32_16x16x64_bf16 a[64:67], a[72:75], v[16:23], v154\n"
        "global_store_dwordx4 v254, a[20:23], s[70:71] offset:1024 sc1\n"
        "v_exp_f32 v32, v32\n"
        "v_exp_f32 v33, v33\n"
        "v_mfma_f32_16x16x32_bf16 v[192:195], a[144:147], v[20:23], 0\n"
        "ds_read_b128 a[144:147], v241 offset:8192\n"
        "v_cvt_pk_bf16_f32 v80, v32, v33\n"
        "v_mfma_f32_16x16x32_bf16 v[196:199], a[148:151], v[20:23], 0\n"
        "ds_read_b128 a[148:151], v241 offset:9216\n"
        "v_cndmask_b32 v52, v228, v242, s[62:63]\n"
        "v_mfma_f32_16x16x32_bf16 v[200:203], a[152:155], v[20:23], 0\n"
        "ds_read_b128 a[152:155], v241 offset:10240\n"
        "v_cndmask_b32 v53, v228, v242, s[84:85]\n"
        "v_mfma_f32_16x16x32_bf16 v[204:207], a[156:159], v[20:23], 0\n"
        "ds_read_b128 a[156:159], v241 offset:11264\n"
        "v_cndmask_b32 v54, v228, v242, s[86:87]\n"
        "v_mfma_f32_16x16x32_bf16 v[208:211], a[128:131], v[20:23], 0\n"
        "ds_read_b128 a[128:131], v241 offset:12288\n"
        "v_cndmask_b32 v55, v228, v242, s[88:89]\n"
        "s_nop 1\n"
        "v_mfma_f32_16x16x32_bf16 v[52:55], a[124:127], v[76:79], v[52:55]\n"
        "v_exp_f32 v34, v34\n"
        "v_mfma_f32_16x16x32_bf16 v[52:55], a[120:123], v[72:75], v[52:55]\n"
        "v_exp_f32 v35, v35\n"
        "v_mfma_f32_16x16x32_bf16 v[52:55], a[116:119], v[68:71], v[52:55]\n"
        "ds_read_b128 a[116:119], v240 offset:21504\n"
        "v_cvt_pk_bf16_f32 v81, v34, v35\n"
        "v_mfma_f32_16x16x32_bf16 v[52:55], a[112:115], v[64:67], v[52:55]\n"
        "ds_read_b128 a[112:115], v240 offset:20480\n"
        "v_cndmask_b32 v32, v224, v242, s[10:11]\n"
        "v_mfma_f32_16x16x32_bf16 v[212:215], a[132:135], v[20:23], 0\n"
        "s_waitcnt lgkmcnt(5)\n"
        "v_exp_f32 v36, v36\n"
        "v_mfma_f32_16x16x32_bf16 v[184:187], a[136:139], v[16:19], 0\n"
        "v_exp_f32 v37, v37\n"
        "v_mfma_f32_16x16x32_bf16 v[216:219], a[136:139], v[20:23], 0\n"
        "ds_read_b128 a[120:123], v240 offset:22528\n"
        "v_cvt_pk_bf16_f32 v84, v36, v37\n"
        "v_mfma_f32_16x16x32_bf16 v[220:223], a[140:143], v[20:23], 0\n"
        "global_store_dwordx4 v254, a[24:27], s[70:71] offset:2048 sc1\n"
        "ds_read_b128 a[124:127], v240 offset:23552\n"
        "s_add_u32 s82, s82, 1\n"
        "v_exp_f32 v38, v38\n"
        "v_cndmask_b32 v33, v224, v242, s[12:13]\n"
        "v_mfma_f32_16x16x32_bf16 v[188:191], a[140:143], v[16:19], 0\n"
        "v_exp_f32 v39, v39\n"
        "ds_write_b128 v245, a[216:219] offset:0\n"
        "v_cvt_pk_bf16_f32 v85, v38, v39\n"
        "v_cndmask_b32 v34, v224, v242, s[14:15]\n"
        "v_cndmask_b32 v35, v224, v242, s[16:17]\n"
        "s_nop 1\n"
        "v_mfma_f32_16x16x32_bf16 v[32:35], a[96:99], v[0:3], v[32:35]\n"
        "v_exp_f32 v48, v48\n"
        "v_mfma_f32_16x16x32_bf16 v[32:35], a[100:103], v[4:7], v[32:35]\n"
        "v_exp_f32 v49, v49\n"
        "v_mfma_f32_16x16x32_bf16 v[32:35], a[104:107], v[8:11], v[32:35]\n"
        "ds_read_b128 a[96:99], v240 offset:24576\n"
        "v_cvt_pk_bf16_f32 v82, v48, v49\n"
        "v_mfma_f32_16x16x32_bf16 v[32:35], a[108:111], v[12:15], v[32:35]\n"
        "v_exp_f32 v50, v50\n"
        "v_exp_f32 v51, v51\n"
        "global_store_dwordx4 v254, a[28:31], s[70:71] offset:3072 sc1\n"
        "v_cvt_pk_bf16_f32 v83, v50, v51\n"
        "s_nop 1\n"
        "v_mfma_f32_16x16x32_bf16 v[160:163], a[144:147], v[80:83], v[160:163]\n"
        "ds_write_b128 v245, a[248:251] offset:1024\n"
        "v_mfma_f32_16x16x32_bf16 v[164:167], a[148:151], v[80:83], v[164:167]\n"
        "s_waitcnt lgkmcnt(2)\n"
        "v_exp_f32 v52, v52\n"
        "v_mfma_f32_16x16x32_bf16 v[168:171], a[152:155], v[80:83], v[168:171]\n"
        "v_exp_f32 v53, v53\n"
        "v_mfma_f32_16x16x32_bf16 v[172:175], a[156:159], v[80:83], v[172:175]\n"
        "ds_read_b128 a[132:135], v241 offset:13312\n"
        "v_cvt_pk_bf16_f32 v86, v52, v53\n"
        "v_mfma_f32_16x16x32_bf16 v[176:179], a[128:131], v[80:83], v[176:179]\n"
        "ds_read_b128 a[100:103], v240 offset:25600\n"
        "ds_read_b128 a[136:139], v241 offset:14336\n"
        "ds_read_b128 a[104:107], v240 offset:26624\n"
        "ds_read_b128 a[140:143], v241 offset:15360\n"
        "ds_write_b128 v245, a[220:223] offset:2048\n"
        "ds_read_b128 a[108:111], v240 offset:27648\n"
        "s_add_u32 s70, s92, 0x2\n"
        "s_addc_u32 s71, s93, 0\n"
        "s_cmp_ge_u32 s82, s44\n"
        "s_addc_u32 s90, s82, 0\n"
        "s_lshl_b32 s90, s90, 15\n"
        "v_exp_f32 v54, v54\n"
        "v_exp_f32 v55, v55\n"
        "s_waitcnt lgkmcnt(6)\n"
        "v_cvt_pk_bf16_f32 v87, v54, v55\n"
        "s_nop 1\n"
        "v_smfmac_f32_16x16x64_bf16 a[64:67], a[72:75], v[80:87], v154\n"
        "v_cndmask_b32 v48, v224, v242, s[20:21]\n"
        "v_mfma_f32_16x16x32_bf16 v[192:195], a[144:147], v[84:87], v[192:195]\n"
        "ds_read_b128 a[144:147], v241 offset:16384\n"
        "v_cndmask_b32 v49, v224, v242, s[22:23]\n"
        "v_mfma_f32_16x16x32_bf16 v[196:199], a[148:151], v[84:87], v[196:199]\n"
        "ds_read_b128 a[148:151], v241 offset:17408\n"
        "v_cndmask_b32 v50, v224, v242, s[24:25]\n"
        "v_mfma_f32_16x16x32_bf16 v[200:203], a[152:155], v[84:87], v[200:203]\n"
        "ds_read_b128 a[152:155], v241 offset:18432\n"
        "v_cndmask_b32 v51, v224, v242, s[28:29]\n"
        "s_nop 1\n"
        "v_mfma_f32_16x16x32_bf16 v[48:51], a[112:115], v[0:3], v[48:51]\n"
        "v_exp_f32 v32, v32\n"
        "v_mfma_f32_16x16x32_bf16 v[48:51], a[116:119], v[4:7], v[48:51]\n"
        "v_exp_f32 v33, v33\n"
        "v_mfma_f32_16x16x32_bf16 v[48:51], a[120:123], v[8:11], v[48:51]\n"
        "ds_read_b128 a[112:115], v240 offset:28672\n"
        "v_cvt_pk_bf16_f32 v16, v32, v33\n"
        "v_mfma_f32_16x16x32_bf16 v[48:51], a[124:127], v[12:15], v[48:51]\n"
        "s_waitcnt lgkmcnt(4)\n"
        "v_cvt_pk_bf16_f32 v109, v122, v123\n"
        "v_cvt_pk_bf16_f32 v110, v124, v125\n"
        "v_mfma_f32_16x16x32_bf16 v[204:207], a[156:159], v[84:87], v[204:207]\n"
        "ds_read_b128 a[116:119], v240 offset:29696\n"
        "v_cvt_pk_bf16_f32 v111, v126, v127\n"
        "v_mfma_f32_16x16x32_bf16 v[208:211], a[128:131], v[84:87], v[208:211]\n"
        "v_exp_f32 v34, v34\n"
        "v_mfma_f32_16x16x32_bf16 v[212:215], a[132:135], v[84:87], v[212:215]\n"
        "v_exp_f32 v35, v35\n"
        "v_mfma_f32_16x16x32_bf16 v[180:183], a[132:135], v[80:83], v[180:183]\n"
        "ds_read_b128 a[120:123], v240 offset:30720\n"
        "v_cvt_pk_bf16_f32 v17, v34, v35\n"
        "v_mfma_f32_16x16x32_bf16 v[184:187], a[136:139], v[80:83], v[184:187]\n"
        "ds_write_b128 v245, a[252:255] offset:3072\n"
        "v_mfma_f32_16x16x32_bf16 v[216:219], a[136:139], v[84:87], v[216:219]\n"
        "ds_read_b128 a[124:127], v240 offset:31744\n"
        "v_cndmask_b32 v32, v224, v242, s[30:31]\n"
        "v_mfma_f32_16x16x32_bf16 v[220:223], a[140:143], v[84:87], v[220:223]\n"
        "ds_read_b128 a[156:159], v241 offset:19456\n"
        "v_cndmask_b32 v33, v224, v242, s[56:57]\n"
        "v_mfma_f32_16x16x32_bf16 v[188:191], a[140:143], v[80:83], v[188:191]\n"
        "v_cndmask_b32 v34, v224, v242, s[58:59]\n"
        "v_cndmask_b32 v35, v224, v242, s[60:61]\n"
        "s_nop 1\n"
        "v_mfma_f32_16x16x32_bf16 v[32:35], a[96:99], v[0:3], v[32:35]\n"
        "v_exp_f32 v48, v48\n"
        "v_mfma_f32_16x16x32_bf16 v[32:35], a[100:103], v[4:7], v[32:35]\n"
        "v_exp_f32 v49, v49\n"
        "v_mfma_f32_16x16x32_bf16 v[32:35], a[104:107], v[8:11], v[32:35]\n"
        "s_waitcnt lgkmcnt(3)\n"
        "v_cvt_pk_bf16_f32 v18, v48, v49\n"
        "v_xor_b32 v240, 0x8000, v240\n"
        "v_mfma_f32_16x16x32_bf16 v[32:35], a[108:111], v[12:15], v[32:35]\n"
        "ds_write_b128 v245, a[208:211] offset:8192\n"
        "s_waitcnt lgkmcnt(2)\n"
        "s_barrier\n"
        "ds_read_b128 a[96:99], v240 offset:0\n"
        "ds_read_b128 a[128:131], v241 offset:20480\n"
        "v_exp_f32 v50, v50\n"
        "v_exp_f32 v51, v51\n"
        "v_cndmask_b32 v48, v224, v242, s[62:63]\n"
        "v_cvt_pk_bf16_f32 v19, v50, v51\n"
        "s_nop 1\n"
        "v_mfma_f32_16x16x32_bf16 a[64:67], a[76:79], v[16:19], a[64:67]\n"
        "ds_read_b128 a[132:135], v241 offset:21504\n"
        "v_cndmask_b32 v49, v224, v242, s[84:85]\n"
        "v_mfma_f32_16x16x32_bf16 v[160:163], a[144:147], v[16:19], v[160:163]\n"
        "ds_read_b128 a[100:103], v240 offset:1024\n"
        "v_cndmask_b32 v50, v224, v242, s[86:87]\n"
        "v_mfma_f32_16x16x32_bf16 v[164:167], a[148:151], v[16:19], v[164:167]\n"
        "ds_read_b128 a[136:139], v241 offset:22528\n"
        "v_cndmask_b32 v51, v224, v242, s[88:89]\n"
        "s_nop 1\n"
        "v_mfma_f32_16x16x32_bf16 v[48:51], a[112:115], v[0:3], v[48:51]\n"
        "ds_read_b128 a[112:115], v240 offset:4096\n"
        "v_cvt_pk_bf16_f32 v137, v146, v147\n"
        "v_mfma_f32_16x16x32_bf16 v[48:51], a[116:119], v[4:7], v[48:51]\n"
        "ds_read_b128 a[116:119], v240 offset:5120\n"
        "v_cvt_pk_bf16_f32 v138, v148, v149\n"
        "v_mfma_f32_16x16x32_bf16 v[48:51], a[120:123], v[8:11], v[48:51]\n"
        "ds_read_b128 a[120:123], v240 offset:6144\n"
        "v_cvt_pk_bf16_f32 v139, v150, v151\n"
        "v_mfma_f32_16x16x32_bf16 v[48:51], a[124:127], v[12:15], v[48:51]\n"
        "ds_read_b128 a[124:127], v240 offset:7168\n"
        "v_lshrrev_b32 v244, 1, v254\n"
        "v_mfma_f32_16x16x32_bf16 v[168:171], a[152:155], v[16:19], v[168:171]\n"
        "ds_read_b128 a[140:143], v241 offset:23552\n"
        "s_waitcnt lgkmcnt(8)\n"
        "v_mfma_f32_16x16x32_bf16 v[172:175], a[156:159], v[16:19], v[172:175]\n"
        "ds_read_b128 a[104:107], v240 offset:2048\n"
        "v_add_u32 v244, s81, v244\n"
        "v_mfma_f32_16x16x32_bf16 v[176:179], a[128:131], v[16:19], v[176:179]\n"
        "s_waitcnt lgkmcnt(7)\n"
        "v_exp_f32 v32, v32\n"
        "v_mfma_f32_16x16x32_bf16 v[180:183], a[132:135], v[16:19], v[180:183]\n"
        "s_waitcnt lgkmcnt(6)\n"
        "v_exp_f32 v33, v33\n"
        "v_mfma_f32_16x16x32_bf16 v[184:187], a[136:139], v[16:19], v[184:187]\n"
        "ds_write_b128 v245, a[240:243] offset:9216\n"
        "ds_read_b128 a[108:111], v240 offset:3072\n"
        "ds_read_b128 a[144:147], v241 offset:24576\n"
        "s_add_u32 s81, s78, 4096\n"
        "v_cvt_pk_bf16_f32 v80, v32, v33\n"
        "s_waitcnt lgkmcnt(5)\n"
        "v_mfma_f32_16x16x32_bf16 v[52:55], a[124:127], v[76:79], v[228:231]\n"
        "v_exp_f32 v34, v34\n"
        "v_mfma_f32_16x16x32_bf16 v[52:55], a[120:123], v[72:75], v[52:55]\n"
        "ds_write_b128 v245, a[212:215] offset:10240\n"
        "v_mfma_f32_16x16x32_bf16 v[52:55], a[116:119], v[68:71], v[52:55]\n"
        "v_exp_f32 v35, v35\n"
        "v_mfma_f32_16x16x32_bf16 v[52:55], a[112:115], v[64:67], v[52:55]\n"
        "s_waitcnt vmcnt(0)\n"
        "v_cvt_pk_bf16_f32 v81, v34, v35\n"
        "s_waitcnt lgkmcnt(4)\n"
        "v_mfma_f32_16x16x32_bf16 v[188:191], a[140:143], v[16:19], v[188:191]\n"
        "s_barrier\n"
        "v_mfma_f32_16x16x32_bf16 v[32:35], a[96:99], v[0:3], v[224:227]\n"
        "s_cmp_lg_u32 s40, 0\n"
        "s_cbranch_scc1 LW1_%=\n"
        "s_mov_b64 exec, 0xffff\n"
        "global_store_dwordx2 v244, v[250:251], s[48:49] sc1\n"
        "s_mov_b64 exec, -1\n"
        "LW1_%=:\n"
        "v_mfma_f32_16x16x32_bf16 v[32:35], a[100:103], v[4:7], v[32:35]\n"
        "ds_write_b128 v245, a[244:247] offset:11264\n"
        "v_mfma_f32_16x16x32_bf16 v[32:35], a[104:107], v[8:11], v[32:35]\n"
        "s_waitcnt lgkmcnt(1)\n"
        "v_bfe_u32 v245, v254, 4, 3\n"
        "v_lshl_add_u32 v245, v245, 7, s80\n"
        "v_mfma_f32_16x16x32_bf16 v[32:35], a[108:111], v[12:15], v[32:35]\n"
        "ds_read_b128 a[148:151], v241 offset:25600\n"
        "s_mov_b32 s80, s78\n"
        "v_cvt_pk_bf16_f32 v140, v152, v153\n"
        "v_mfma_f32_16x16x32_bf16 v[36:39], a[108:111], v[76:79], v[228:231]\n"
        "ds_read_b128 a[152:155], v241 offset:26624\n"
        "s_add_u32 m0, s80, 0x0\n"
        "v_cvt_pk_bf16_f32 v142, v156, v157\n"
        "v_mfma_f32_16x16x32_bf16 v[36:39], a[104:107], v[72:75], v[36:39]\n"
        "ds_read_b128 a[156:159], v241 offset:27648\n"
        "s_xor_b32 s78, s78, 0x8000\n"
        "v_cvt_pk_bf16_f32 v143, v158, v159\n"
        "v_mfma_f32_16x16x32_bf16 v[36:39], a[100:103], v[68:71], v[36:39]\n"
        "ds_read_b128 a[128:131], v241 offset:28672\n"
        "s_mov_b32 s80, s78\n"
        "v_cndmask_b32 v40, 0, v242, s[10:11]\n"
        "v_mfma_f32_16x16x32_bf16 v[36:39], a[96:99], v[64:67], v[36:39]\n"
        "ds_read_b128 a[132:135], v241 offset:29696\n"
        "ds_read_b128 a[136:139], v241 offset:30720\n"
        "ds_read_b128 a[140:143], v241 offset:31744\n"
        "v_exp_f32 v48, v48\n"
        "v_exp_f32 v49, v49\n"
        "v_exp_f32 v50, v50\n"
        "v_cvt_pk_bf16_f32 v82, v48, v49\n"
        "v_exp_f32 v51, v51\n"
        "v_xor_b32 v241, 0x8000, v241\n"
        "v_cvt_pk_bf16_f32 v83, v50, v51\n"
        "s_waitcnt lgkmcnt(6)\n"
        "s_nop 0\n"
        "v_mfma_f32_16x16x32_bf16 v[164:167], a[148:151], v[80:83], v[164:167]\n"
        "s_add_u32 s91, s44, 1\n"
        "v_cndmask_b32 v41, 0, v242, s[12:13]\n"
        "v_cndmask_b32 v42, 0, v242, s[14:15]\n"
        "v_mfma_f32_16x16x32_bf16 v[48:51], a[112:115], v[0:3], v[224:227]\n"
        "LHA10_%=:\n"
        "s_cbranch_execz LHRA_%=\n"
        "v_cndmask_b32 v43, 0, v242, s[16:17]\n"
        "s_nop 1\n"
        "v_mfma_f32_16x16x32_bf16 v[40:43], a[96:99], v[96:99], v[40:43]\n"
        "ds_read_b128 a[96:99], v240 offset:8192\n"
        "v_cndmask_b32 v56, 0, v242, s[20:21]\n"
        "v_mfma_f32_16x16x32_bf16 v[40:43], a[100:103], v[100:103], v[40:43]\n"
        "ds_read_b128 a[100:103], v240 offset:9216\n"
        "v_cndmask_b32 v57, 0, v242, s[22:23]\n"
        "v_mfma_f32_16x16x32_bf16 v[40:43], a[104:107], v[104:107], v[40:43]\n"
        "s_waitcnt lgkmcnt(2)\n"
        "v_exp_f32 v32, v32\n"
        "v_mfma_f32_16x16x32_bf16 v[40:43], a[108:111], v[108:111], v[40:43]\n"
        "s_barrier\n"
        "v_mfma_f32_16x16x32_bf16 v[48:51], a[116:119], v[4:7], v[48:51]\n"
        "ds_read_b128 a[148:151], v241 offset:1024\n"
        "v_cndmask_b32 v58, 0, v242, s[24:25]\n"
        "v_mfma_f32_16x16x32_bf16 v[48:51], a[120:123], v[8:11], v[48:51]\n"
        "v_exp_f32 v33, v33\n"
        "v_mfma_f32_16x16x32_bf16 v[48:51], a[124:127], v[12:15], v[48:51]\n"
        "v_exp_f32 v34, v34\n"
        "v_mfma_f32_16x16x32_bf16 a[64:67], a[76:79], v[80:83], a[64:67]\n"
        "ds_read_b128 a[104:107], v240 offset:10240\n"
        "v_max_f32 v246, v40, v41\n"
        "v_mfma_f32_16x16x32_bf16 v[160:163], a[144:147], v[80:83], v[160:163]\n"
        "ds_read_b128 a[108:111], v240 offset:11264\n"
        "v_max3_f32 v246, v42, v43, v246\n"
        "v_mfma_f32_16x16x32_bf16 v[168:171], a[152:155], v[80:83], v[168:171]\n"
        "ds_read_b128 a[144:147], v241 offset:0\n"
        "v_mov_b32 v247, v246\n"
        "v_mfma_f32_16x16x32_bf16 v[172:175], a[156:159], v[80:83], v[172:175]\n"
        "v_exp_f32 v35, v35\n"
        "v_mfma_f32_16x16x32_bf16 v[176:179], a[128:131], v[80:83], v[176:179]\n"
        "v_permlane16_swap_b32 v246, v247\n"
        "v_mfma_f32_16x16x32_bf16 v[180:183], a[132:135], v[80:83], v[180:183]\n"
        "s_waitcnt lgkmcnt(3)\n"
        "v_max_f32 v246, v246, v247\n"
        "v_mov_b32 v247, v246\n"
        "v_mfma_f32_16x16x32_bf16 v[184:187], a[136:139], v[80:83], v[184:187]\n"
        "v_exp_f32 v48, v48\n"
        "v_mfma_f32_16x16x32_bf16 v[188:191], a[140:143], v[80:83], v[188:191]\n"
        "v_permlane32_swap_b32 v246, v247\n"
        "v_max_f32 v246, v246, v247\n"
        "v_sub_f32 v232, 0, v246\n"
        "v_sub_f32 v233, 0, v246\n"
        "v_sub_f32 v234, 0, v246\n"
        "v_sub_f32 v235, 0, v246\n"
        "v_exp_f32 v49, v49\n"
        "v_exp_f32 v50, v50\n"
        "v_exp_f32 v51, v51\n"
        "v_cvt_pk_bf16_f32 v16, v32, v33\n"
        "v_cvt_pk_bf16_f32 v17, v34, v35\n"
        "v_mfma_f32_16x16x32_bf16 v[32:35], a[96:99], v[0:3], v[224:227]\n"
        "ds_read_b128 a[152:155], v241 offset:2048\n"
        "v_cvt_pk_bf16_f32 v18, v48, v49\n"
        "v_mfma_f32_16x16x32_bf16 v[32:35], a[100:103], v[4:7], v[32:35]\n"
        "s_waitcnt lgkmcnt(1)\n"
        "v_cvt_pk_bf16_f32 v19, v50, v51\n"
        "s_nop 1\n"
        "v_mfma_f32_16x16x32_bf16 v[164:167], a[148:151], v[16:19], v[164:167]\n"
        "global_load_dwordx2 v[252:253], v245, s[48:49] sc1\n"
        "v_cndmask_b32 v59, 0, v242, s[28:29]\n"
        "s_nop 1\n"
        "v_mfma_f32_16x16x32_bf16 v[56:59], a[112:115], v[96:99], v[56:59]\n"
        "v_exp_f32 v36, v36\n"
        "v_mfma_f32_16x16x32_bf16 v[56:59], a[116:119], v[100:103], v[56:59]\n"
        "v_exp_f32 v37, v37\n"
        "v_mfma_f32_16x16x32_bf16 v[56:59], a[120:123], v[104:107], v[56:59]\n"
        "ds_read_b128 a[112:115], v240 offset:12288\n"
        "v_cvt_pk_bf16_f32 v20, v36, v37\n"
        "v_mfma_f32_16x16x32_bf16 v[56:59], a[124:127], v[108:111], v[56:59]\n"
        "v_exp_f32 v38, v38\n"
        "v_mfma_f32_16x16x32_bf16 v[32:35], a[104:107], v[8:11], v[32:35]\n"
        "v_exp_f32 v39, v39\n"
        "v_mfma_f32_16x16x32_bf16 v[32:35], a[108:111], v[12:15], v[32:35]\n"
        "ds_read_b128 a[116:119], v240 offset:13312\n"
        "v_cvt_pk_bf16_f32 v21, v38, v39\n"
        "v_mfma_f32_16x16x32_bf16 v[36:39], a[108:111], v[76:79], v[228:231]\n"
        "ds_read_b128 a[120:123], v240 offset:14336\n"
        "v_add_f32 v40, v40, v232\n"
        "v_mfma_f32_16x16x32_bf16 v[36:39], a[104:107], v[72:75], v[36:39]\n"
        "v_exp_f32 v40, v40\n"
        "v_mfma_f32_16x16x32_bf16 v[36:39], a[100:103], v[68:71], v[36:39]\n"
        "ds_read_b128 a[156:159], v241 offset:3072\n"
        "v_add_f32 v41, v41, v232\n"
        "v_mfma_f32_16x16x32_bf16 v[36:39], a[96:99], v[64:67], v[36:39]\n"
        "v_exp_f32 v41, v41\n"
        "v_mfma_f32_16x16x32_bf16 v[160:163], a[144:147], v[16:19], v[160:163]\n"
        "s_waitcnt lgkmcnt(3)\n"
        "v_cvt_pk_bf16_f32 v24, v40, v41\n"
        "v_add_f32 v42, v42, v232\n"
        "v_mfma_f32_16x16x32_bf16 v[48:51], a[112:115], v[0:3], v[224:227]\n"
        "v_exp_f32 v42, v42\n"
        "v_mfma_f32_16x16x32_bf16 v[168:171], a[152:155], v[16:19], v[168:171]\n"
        "ds_read_b128 a[124:127], v240 offset:15360\n"
        "v_add_f32 v43, v43, v232\n"
        "s_waitcnt lgkmcnt(3)\n"
        "v_mfma_f32_16x16x32_bf16 v[48:51], a[116:119], v[4:7], v[48:51]\n"
        "s_waitcnt lgkmcnt(2)\n"
        "v_exp_f32 v43, v43\n"
        "v_mfma_f32_16x16x32_bf16 v[48:51], a[120:123], v[8:11], v[48:51]\n"
        "ds_read_b128 a[128:131], v241 offset:4096\n"
        "ds_read_b128 a[132:135], v241 offset:5120\n"
        "v_cvt_pk_bf16_f32 v25, v42, v43\n"
        "v_cndmask_b32 v40, v232, v242, s[30:31]\n"
        "s_waitcnt lgkmcnt(3)\n"
        "v_mfma_f32_16x16x32_bf16 v[172:175], a[156:159], v[16:19], v[172:175]\n"
        "s_mov_b64 exec, 0\n"
        "s_cmp_eq_u32 s40, 0\n"
        "s_cbranch_scc1 LHC0_%=\n"
        "s_cmp_eq_u32 s40, 1\n"
        "s_cbranch_scc1 LHC1_%=\n"
        "s_cmp_eq_u32 s40, 2\n"
        "s_cbranch_scc1 LHC2_%=\n"
        "s_branch LHC3_%=\n"
        "LHRC_%=:\n"
        "s_mov_b64 exec, -1\n"
        "ds_read_b128 a[136:139], v241 offset:6144\n"
        "ds_read_b128 a[140:143], v241 offset:7168\n"
        "v_exp_f32 v52, v52\n"
        "v_exp_f32 v53, v53\n"
        "s_waitcnt lgkmcnt(4)\n"
        "v_cvt_pk_bf16_f32 v22, v52, v53\n"
        "v_mfma_f32_16x16x32_bf16 v[48:51], a[124:127], v[12:15], v[48:51]\n"
        "v_cndmask_b32 v41, v232, v242, s[56:57]\n"
        "v_cndmask_b32 v42, v232, v242, s[58:59]\n"
        "v_cndmask_b32 v43, v232, v242, s[60:61]\n"
        "s_nop 1\n"
        "v_mfma_f32_16x16x32_bf16 v[40:43], a[96:99], v[96:99], v[40:43]\n"
        "ds_read_b128 a[96:99], v240 offset:16384\n"
        "v_add_f32 v56, v56, v232\n"
        "v_mfma_f32_16x16x32_bf16 v[40:43], a[100:103], v[100:103], v[40:43]\n"
        "v_exp_f32 v54, v54\n"
        "v_mfma_f32_16x16x32_bf16 v[40:43], a[104:107], v[104:107], v[40:43]\n"
        "s_waitcnt vmcnt(0) lgkmcnt(3)\n"
        "v_cmp_ne_u64 vcc, v[252:253], v[250:251]\n"
        "v_add_f32 v57, v57, v232\n"
        "v_mfma_f32_16x16x32_bf16 v[40:43], a[108:111], v[108:111], v[40:43]\n"
        "s_cbranch_vccnz LPKV_%=\n"
        "LBKV_%=:\n"
        "v_exp_f32 v55, v55\n"
        "v_mfma_f32_16x16x32_bf16 v[176:179], a[128:131], v[16:19], v[176:179]\n"
        "ds_read_b128 a[100:103], v240 offset:17408\n"
        "v_cvt_pk_bf16_f32 v23, v54, v55\n"
        "v_mfma_f32_16x16x32_bf16 v[52:55], a[124:127], v[76:79], v[228:231]\n"
        "v_exp_f32 v56, v56\n"
        "v_mfma_f32_16x16x32_bf16 v[52:55], a[120:123], v[72:75], v[52:55]\n"
        "ds_read_b128 a[104:107], v240 offset:18432\n"
        "v_mov_b32 v144, v254\n"
        "v_mfma_f32_16x16x32_bf16 v[52:55], a[116:119], v[68:71], v[52:55]\n"
        "v_exp_f32 v57, v57\n"
        "v_mfma_f32_16x16x32_bf16 v[52:55], a[112:115], v[64:67], v[52:55]\n"
        "ds_read_b128 a[108:111], v240 offset:19456\n"
        "v_mov_b32 v145, v254\n"
        "v_smfmac_f32_16x16x64_bf16 a[64:67], a[72:75], v[16:23], v154\n"
        "global_load_lds_dwordx4 v254, s[68:69] offset:0 sc1\n"
        "v_cvt_pk_bf16_f32 v26, v56, v57\n"
        "v_mfma_f32_16x16x32_bf16 v[192:195], a[144:147], v[20:23], v[192:195]\n"
        "v_mov_b32 v146, v254\n"
        "v_mov_b32 v147, v254\n"
        "v_mfma_f32_16x16x32_bf16 v[196:199], a[148:151], v[20:23], v[196:199]\n"
        "v_accvgpr_read_b32 v148, a88\n"
        "v_accvgpr_read_b32 v152, a89\n"
        "v_mfma_f32_16x16x32_bf16 v[200:203], a[152:155], v[20:23], v[200:203]\n"
        "v_accvgpr_read_b32 v156, a90\n"
        "v_accvgpr_read_b32 v153, a92\n"
        "v_mfma_f32_16x16x32_bf16 v[204:207], a[156:159], v[20:23], v[204:207]\n"
        "global_load_lds_dwordx4 v254, s[68:69] offset:1024 sc1\n"
        "v_accvgpr_read_b32 v155, a94\n"
        "v_mfma_f32_16x16x32_bf16 v[208:211], a[128:131], v[20:23], v[208:211]\n"
        "v_add_u32 v149, 1024, v148\n"
        "v_accvgpr_write_b32 a88, 0\n"
        "v_mfma_f32_16x16x32_bf16 v[212:215], a[132:135], v[20:23], v[212:215]\n"
        "v_add_u32 v150, 2048, v148\n"
        "v_accvgpr_write_b32 a89, 0\n"
        "v_mfma_f32_16x16x32_bf16 v[180:183], a[132:135], v[16:19], v[180:183]\n"
        "s_waitcnt lgkmcnt(0)\n"
        "v_add_u32 v151, 3072, v148\n"
        "v_accvgpr_write_b32 a90, 0\n"
        "v_mfma_f32_16x16x32_bf16 v[184:187], a[136:139], v[16:19], v[184:187]\n"
        "global_load_lds_dwordx4 v254, s[68:69] offset:2048 sc1\n"
        "v_add_f32 v58, v58, v232\n"
        "v_mfma_f32_16x16x32_bf16 v[216:219], a[136:139], v[20:23], v[216:219]\n"
        "v_exp_f32 v58, v58\n"
        "v_mfma_f32_16x16x32_bf16 v[220:223], a[140:143], v[20:23], v[220:223]\n"
        "v_add_f32 v59, v59, v232\n"
        "v_cndmask_b32 v56, v232, v242, s[62:63]\n"
        "v_mfma_f32_16x16x32_bf16 v[188:191], a[140:143], v[16:19], v[188:191]\n"
        "v_exp_f32 v59, v59\n"
        "v_exp_f32 v32, v32\n"
        "v_cvt_pk_bf16_f32 v27, v58, v59\n"
        "s_nop 1\n"
        "v_mfma_f32_16x16x32_bf16 a[68:71], a[76:79], v[24:27], a[68:71]\n"
        "v_exp_f32 v33, v33\n"
        "v_mfma_f32_16x16x32_bf16 a[0:3], a[144:147], v[24:27], 0\n"
        "ds_read_b128 a[144:147], v241 offset:8192\n"
        "v_cvt_pk_bf16_f32 v80, v32, v33\n"
        "v_mfma_f32_16x16x32_bf16 a[4:7], a[148:151], v[24:27], 0\n"
        "v_exp_f32 v34, v34\n"
        "v_mfma_f32_16x16x32_bf16 a[8:11], a[152:155], v[24:27], 0\n"
        "v_exp_f32 v35, v35\n"
        "v_mfma_f32_16x16x32_bf16 a[12:15], a[156:159], v[24:27], 0\n"
        "ds_read_b128 a[148:151], v241 offset:9216\n"
        "v_cvt_pk_bf16_f32 v81, v34, v35\n"
        "v_mfma_f32_16x16x32_bf16 v[32:35], a[96:99], v[0:3], v[224:227]\n"
        "ds_read_b128 a[152:155], v241 offset:10240\n"
        "v_cndmask_b32 v57, v232, v242, s[84:85]\n"
        "v_mfma_f32_16x16x32_bf16 v[32:35], a[100:103], v[4:7], v[32:35]\n"
        "ds_read_b128 a[156:159], v241 offset:11264\n"
        "v_cndmask_b32 v58, v232, v242, s[86:87]\n"
        "v_mfma_f32_16x16x32_bf16 v[32:35], a[104:107], v[8:11], v[32:35]\n"
        "global_load_lds_dwordx4 v254, s[68:69] offset:3072 sc1\n"
        "s_add_u32 m0, s81, 0x0\n"
        "v_cndmask_b32 v59, v232, v242, s[88:89]\n"
        "s_nop 1\n"
        "v_mfma_f32_16x16x32_bf16 v[56:59], a[112:115], v[96:99], v[56:59]\n"
        "s_add_u32 s81, s78, 4096\n"
        "v_exp_f32 v36, v36\n"
        "v_mfma_f32_16x16x32_bf16 v[56:59], a[116:119], v[100:103], v[56:59]\n"
        "v_exp_f32 v37, v37\n"
        "v_mfma_f32_16x16x32_bf16 v[56:59], a[120:123], v[104:107], v[56:59]\n"
        "ds_read_b128 a[112:115], v240 offset:20480\n"
        "v_cvt_pk_bf16_f32 v84, v36, v37\n"
        "v_mfma_f32_16x16x32_bf16 v[56:59], a[124:127], v[108:111], v[56:59]\n"
        "v_exp_f32 v38, v38\n"
        "v_mfma_f32_16x16x32_bf16 v[32:35], a[108:111], v[12:15], v[32:35]\n"
        "v_exp_f32 v39, v39\n"
        "v_mfma_f32_16x16x32_bf16 a[16:19], a[128:131], v[24:27], 0\n"
        "ds_read_b128 a[116:119], v240 offset:21504\n"
        "v_cvt_pk_bf16_f32 v85, v38, v39\n"
        "v_mfma_f32_16x16x32_bf16 v[36:39], a[108:111], v[76:79], v[228:231]\n"
        "v_exp_f32 v40, v40\n"
        "v_mfma_f32_16x16x32_bf16 v[36:39], a[104:107], v[72:75], v[36:39]\n"
        "v_exp_f32 v41, v41\n"
        "v_mfma_f32_16x16x32_bf16 v[36:39], a[100:103], v[68:71], v[36:39]\n"
        "ds_read_b128 a[120:123], v240 offset:22528\n"
        "v_cvt_pk_bf16_f32 v88, v40, v41\n"
        "v_mfma_f32_16x16x32_bf16 v[36:39], a[96:99], v[64:67], v[36:39]\n"
        "ds_read_b128 a[124:127], v240 offset:23552\n"
        "v_and_b32 v245, 0x70, v254\n"
        "v_mfma_f32_16x16x32_bf16 a[20:23], a[132:135], v[24:27], 0\n"
        "global_load_lds_dwordx4 v254, s[74:75] offset:0 sc1\n"
        "v_lshlrev_b32 v245, 3, v245\n"
        "v_mfma_f32_16x16x32_bf16 a[24:27], a[136:139], v[24:27], 0\n"
        "v_exp_f32 v42, v42\n"
        "v_mfma_f32_16x16x32_bf16 a[28:31], a[140:143], v[24:27], 0\n"
        "v_exp_f32 v43, v43\n"
        "v_exp_f32 v48, v48\n"
        "v_cvt_pk_bf16_f32 v89, v42, v43\n"
        "v_mfma_f32_16x16x32_bf16 v[40:43], a[96:99], v[96:99], v[232:235]\n"
        "v_exp_f32 v49, v49\n"
        "v_mfma_f32_16x16x32_bf16 v[40:43], a[100:103], v[100:103], v[40:43]\n"
        "global_load_lds_dwordx4 v254, s[74:75] offset:1024 sc1\n"
        "v_cvt_pk_bf16_f32 v82, v48, v49\n"
        "v_mfma_f32_16x16x32_bf16 v[40:43], a[104:107], v[104:107], v[40:43]\n"
        "v_exp_f32 v50, v50\n"
        "v_mfma_f32_16x16x32_bf16 v[40:43], a[108:111], v[108:111], v[40:43]\n"
        "v_exp_f32 v51, v51\n"
        "v_exp_f32 v52, v52\n"
        "v_cvt_pk_bf16_f32 v83, v50, v51\n"
        "s_waitcnt lgkmcnt(0)\n"
        "v_mfma_f32_16x16x32_bf16 v[48:51], a[112:115], v[0:3], v[224:227]\n"
        "v_exp_f32 v53, v53\n"
        "v_mfma_f32_16x16x32_bf16 v[48:51], a[116:119], v[4:7], v[48:51]\n"
        "global_load_lds_dwordx4 v254, s[74:75] offset:2048 sc1\n"
        "v_cvt_pk_bf16_f32 v86, v52, v53\n"
        "v_mfma_f32_16x16x32_bf16 v[48:51], a[120:123], v[8:11], v[48:51]\n"
        "ds_read_b128 a[128:131], v241 offset:12288\n"
        "v_cndmask_b32 v44, 0, v242, s[10:11]\n"
        "v_mfma_f32_16x16x32_bf16 v[48:51], a[124:127], v[12:15], v[48:51]\n"
        "ds_read_b128 a[132:135], v241 offset:13312\n"
        "s_lshr_b32 s10, s41, 1\n"
        "v_cndmask_b32 v45, 0, v242, s[12:13]\n"
        "v_mfma_f32_16x16x32_bf16 v[160:163], a[144:147], v[80:83], v[160:163]\n"
        "ds_read_b128 a[136:139], v241 offset:14336\n"
        "s_and_b32 s11, s42, 7\n"
        "v_cndmask_b32 v46, 0, v242, s[14:15]\n"
        "v_mfma_f32_16x16x32_bf16 v[164:167], a[148:151], v[80:83], v[164:167]\n"
        "global_load_lds_dwordx4 v254, s[74:75] offset:3072 sc1\n"
        "s_add_u32 m0, s79, 0x0\n"
        "v_cndmask_b32 v47, 0, v242, s[16:17]\n"
        "s_nop 1\n"
        "v_mfma_f32_16x16x32_bf16 v[44:47], a[108:111], v[140:143], v[44:47]\n"
        "s_and_b32 s12, s41, 1\n"
        "v_exp_f32 v54, v54\n"
        "v_mfma_f32_16x16x32_bf16 v[44:47], a[104:107], v[136:139], v[44:47]\n"
        "s_lshr_b32 s14, s42, 3\n"
        "v_exp_f32 v55, v55\n"
        "v_mfma_f32_16x16x32_bf16 v[44:47], a[100:103], v[132:135], v[44:47]\n"
        "ds_read_b128 a[100:103], v240 offset:25600\n"
        "s_lshl2_add_u32 s12, s12, s14\n"
        "v_cvt_pk_bf16_f32 v87, v54, v55\n"
        "v_mfma_f32_16x16x32_bf16 v[44:47], a[96:99], v[128:131], v[44:47]\n"
        "ds_read_b128 a[96:99], v240 offset:24576\n"
        "global_load_lds_dwordx4 v254, s[76:77] offset:0 sc1\n"
        "s_lshl3_add_u32 s13, s12, s11\n"
        "v_mfma_f32_16x16x32_bf16 v[52:55], a[124:127], v[76:79], v[228:231]\n"
        "s_lshl_b32 s14, s10, 21\n"
        "v_exp_f32 v56, v56\n"
        "v_mfma_f32_16x16x32_bf16 v[52:55], a[120:123], v[72:75], v[52:55]\n"
        "s_lshl_b32 s15, s40, 15\n"
        "v_exp_f32 v57, v57\n"
        "v_mfma_f32_16x16x32_bf16 v[52:55], a[116:119], v[68:71], v[52:55]\n"
        "ds_read_b128 a[104:107], v240 offset:26624\n"
        "s_add_u32 s14, s14, s15\n"
        "v_cvt_pk_bf16_f32 v90, v56, v57\n"
        "v_mfma_f32_16x16x32_bf16 v[52:55], a[112:115], v[64:67], v[52:55]\n"
        "s_lshl_b32 s15, s11, 8\n"
        "v_exp_f32 v58, v58\n"
        "v_smfmac_f32_16x16x64_bf16 a[64:67], a[72:75], v[80:87], v154\n"
        "ds_read_b128 a[140:143], v241 offset:15360\n"
        "global_load_lds_dwordx4 v254, s[76:77] offset:1024 sc1\n"
        "s_add_u32 s14, s14, s15\n"
        "v_mfma_f32_16x16x32_bf16 v[192:195], a[144:147], v[84:87], v[192:195]\n"
        "s_add_u32 s17, s11, s12\n"
        "v_exp_f32 v59, v59\n"
        "v_mfma_f32_16x16x32_bf16 v[196:199], a[148:151], v[84:87], v[196:199]\n"
        "ds_read_b128 a[108:111], v240 offset:27648\n"
        "s_and_b32 s17, s17, 7\n"
        "v_cvt_pk_bf16_f32 v91, v58, v59\n"
        "v_mfma_f32_16x16x32_bf16 v[56:59], a[112:115], v[96:99], v[232:235]\n"
        "v_exp_f32 v32, v32\n"
        "v_mfma_f32_16x16x32_bf16 v[56:59], a[116:119], v[100:103], v[56:59]\n"
        "v_exp_f32 v33, v33\n"
        "v_mfma_f32_16x16x32_bf16 v[56:59], a[120:123], v[104:107], v[56:59]\n"
        "global_load_lds_dwordx4 v254, s[76:77] offset:2048 sc1\n"
        "v_cvt_pk_bf16_f32 v16, v32, v33\n"
        "v_mfma_f32_16x16x32_bf16 v[56:59], a[124:127], v[108:111], v[56:59]\n"
        "v_exp_f32 v34, v34\n"
        "v_mfma_f32_16x16x32_bf16 a[68:71], a[76:79], v[88:91], a[68:71]\n"
        "v_exp_f32 v35, v35\n"
        "v_mfma_f32_16x16x32_bf16 a[0:3], a[144:147], v[88:91], a[0:3]\n"
        "ds_read_b128 a[144:147], v241 offset:16384\n"
        "v_cvt_pk_bf16_f32 v17, v34, v35\n"
        "v_mfma_f32_16x16x32_bf16 a[4:7], a[148:151], v[88:91], a[4:7]\n"
        "v_exp_f32 v36, v36\n"
        "v_mfma_f32_16x16x32_bf16 v[168:171], a[152:155], v[80:83], v[168:171]\n"
        "v_exp_f32 v37, v37\n"
        "v_mfma_f32_16x16x32_bf16 v[200:203], a[152:155], v[84:87], v[200:203]\n"
        "global_load_lds_dwordx4 v254, s[76:77] offset:3072 sc1\n"
        "s_add_u32 m0, s79, 0x1000\n"
        "v_max_f32 v248, v44, v45\n"
        "v_mfma_f32_16x16x32_bf16 a[8:11], a[152:155], v[88:91], a[8:11]\n"
        "ds_read_b128 a[148:151], v241 offset:17408\n"
        "v_cvt_pk_bf16_f32 v20, v36, v37\n"
        "v_mfma_f32_16x16x32_bf16 a[12:15], a[156:159], v[88:91], a[12:15]\n"
        "ds_read_b128 a[152:155], v241 offset:18432\n"
        "v_max3_f32 v248, v46, v47, v248\n"
        "v_mfma_f32_16x16x32_bf16 v[204:207], a[156:159], v[84:87], v[204:207]\n"
        "s_add_u32 s76, s76, 0x1000\n"
        "s_addc_u32 s77, s77, 0\n"
        "v_mov_b32 v249, v248\n"
        "v_mfma_f32_16x16x32_bf16 v[172:175], a[156:159], v[80:83], v[172:175]\n"
        "s_waitcnt lgkmcnt(3)\n"
        "v_permlane16_swap_b32 v248, v249\n"
        "v_mfma_f32_16x16x32_bf16 v[32:35], a[96:99], v[0:3], v[224:227]\n"
        "global_load_lds_dwordx4 v254, s[76:77] offset:0 sc1\n"
        "v_max_f32 v248, v248, v249\n"
        "v_mfma_f32_16x16x32_bf16 v[32:35], a[100:103], v[4:7], v[32:35]\n"
        "ds_read_b128 a[156:159], v241 offset:19456\n"
        "v_mov_b32 v249, v248\n"
        "v_mfma_f32_16x16x32_bf16 v[32:35], a[104:107], v[8:11], v[32:35]\n"
        "v_exp_f32 v38, v38\n"
        "v_mfma_f32_16x16x32_bf16 v[32:35], a[108:111], v[12:15], v[32:35]\n"
        "v_permlane32_swap_b32 v248, v249\n"
        "v_mfma_f32_16x16x32_bf16 v[176:179], a[128:131], v[80:83], v[176:179]\n"
        "global_load_lds_dwordx4 v254, s[76:77] offset:1024 sc1\n"
        "v_max_f32 v248, v248, v249\n"
        "v_mfma_f32_16x16x32_bf16 v[208:211], a[128:131], v[84:87], v[208:211]\n"
        "s_add_u32 s68, s64, s90\n"
        "s_addc_u32 s69, s65, 0\n"
        "v_sub_f32 v236, 0, v248\n"
        "v_mfma_f32_16x16x32_bf16 a[16:19], a[128:131], v[88:91], a[16:19]\n"
        "ds_read_b128 a[128:131], v241 offset:20480\n"
        "v_sub_f32 v237, 0, v248\n"
        "v_mfma_f32_16x16x32_bf16 a[20:23], a[132:135], v[88:91], a[20:23]\n"
        "s_add_u32 s74, s68, 0x1000\n"
        "s_addc_u32 s75, s69, 0\n"
        "v_sub_f32 v238, 0, v248\n"
        "v_mfma_f32_16x16x32_bf16 v[212:215], a[132:135], v[84:87], v[212:215]\n"
        "global_load_lds_dwordx4 v254, s[76:77] offset:2048 sc1\n"
        "v_sub_f32 v239, 0, v248\n"
        "v_mfma_f32_16x16x32_bf16 v[180:183], a[132:135], v[80:83], v[180:183]\n"
        "v_exp_f32 v39, v39\n"
        "v_mfma_f32_16x16x32_bf16 v[184:187], a[136:139], v[80:83], v[184:187]\n"
        "ds_read_b128 a[132:135], v241 offset:21504\n"
        "v_cvt_pk_bf16_f32 v21, v38, v39\n"
        "v_mfma_f32_16x16x32_bf16 v[36:39], a[108:111], v[76:79], v[228:231]\n"
        "v_cndmask_b32 v60, 0, v242, s[20:21]\n"
        "v_cndmask_b32 v61, 0, v242, s[22:23]\n"
        "v_mfma_f32_16x16x32_bf16 v[36:39], a[104:107], v[72:75], v[36:39]\n"
        "global_load_lds_dwordx4 v254, s[76:77] offset:3072 sc1\n"
        "s_add_u32 m0, s96, 0x20260\n"
        "v_cndmask_b32 v62, 0, v242, s[24:25]\n"
        "v_mfma_f32_16x16x32_bf16 v[36:39], a[100:103], v[68:71], v[36:39]\n"
        "s_sub_u32 s24, s83, 1\n"
        "v_cndmask_b32 v63, 0, v242, s[28:29]\n"
        "s_nop 1\n"
        "v_mfma_f32_16x16x32_bf16 v[60:63], a[124:127], v[140:143], v[60:63]\n"
        "s_min_u32 s24, s24, s91\n"
        "v_exp_f32 v40, v40\n"
        "v_mfma_f32_16x16x32_bf16 v[60:63], a[120:123], v[136:139], v[60:63]\n"
        "s_mov_b32 s25, 4096\n"
        "v_exp_f32 v41, v41\n"
        "v_mfma_f32_16x16x32_bf16 v[60:63], a[116:119], v[132:135], v[60:63]\n"
        "ds_read_b128 a[116:119], v240 offset:29696\n"
        "s_mov_b32 s28, 4096\n"
        "v_cvt_pk_bf16_f32 v24, v40, v41\n"
        "v_mfma_f32_16x16x32_bf16 v[60:63], a[112:115], v[128:131], v[60:63]\n"
        "ds_read_b128 a[112:115], v240 offset:28672\n"
        "s_lshl_b32 s29, s17, 18\n"
        "v_add_f32 v44, v44, v236\n"
        "v_mfma_f32_16x16x32_bf16 v[36:39], a[96:99], v[64:67], v[36:39]\n"
        "v_exp_f32 v42, v42\n"
        "v_mfma_f32_16x16x32_bf16 v[216:219], a[136:139], v[84:87], v[216:219]\n"
        "v_exp_f32 v43, v43\n"
        "v_mfma_f32_16x16x32_bf16 a[24:27], a[136:139], v[88:91], a[24:27]\n"
        "ds_read_b128 a[120:123], v240 offset:30720\n"
        "v_cvt_pk_bf16_f32 v25, v42, v43\n"
        "v_mfma_f32_16x16x32_bf16 v[40:43], a[96:99], v[96:99], v[232:235]\n"
        "v_exp_f32 v44, v44\n"
        "v_mfma_f32_16x16x32_bf16 v[40:43], a[100:103], v[100:103], v[40:43]\n"
        "global_load_lds_dwordx4 v254, s[70:71] offset:0\n"
        "v_add_f32 v45, v45, v236\n"
        "v_mfma_f32_16x16x32_bf16 v[40:43], a[104:107], v[104:107], v[40:43]\n"
        "v_exp_f32 v45, v45\n"
        "v_mfma_f32_16x16x32_bf16 v[40:43], a[108:111], v[108:111], v[40:43]\n"
        "ds_read_b128 a[124:127], v240 offset:31744\n"
        "v_cvt_pk_bf16_f32 v28, v44, v45\n"
        "v_mfma_f32_16x16x32_bf16 a[28:31], a[140:143], v[88:91], a[28:31]\n"
        "ds_read_b128 a[136:139], v241 offset:22528\n"
        "v_add_f32 v46, v46, v236\n"
        "v_mfma_f32_16x16x32_bf16 v[220:223], a[140:143], v[84:87], v[220:223]\n"
        "v_exp_f32 v46, v46\n"
        "v_mfma_f32_16x16x32_bf16 v[188:191], a[140:143], v[80:83], v[188:191]\n"
        "v_add_f32 v47, v47, v236\n"
        "v_exp_f32 v47, v47\n"
        "v_exp_f32 v48, v48\n"
        "v_cvt_pk_bf16_f32 v29, v46, v47\n"
        "v_exp_f32 v49, v49\n"
        "global_load_lds_dwordx4 v254, s[70:71] offset:1024\n"
        "v_cvt_pk_bf16_f32 v18, v48, v49\n"
        "v_exp_f32 v50, v50\n"
        "v_exp_f32 v51, v51\n"
        "v_exp_f32 v52, v52\n"
        "v_cvt_pk_bf16_f32 v19, v50, v51\n"
        "s_waitcnt lgkmcnt(2)\n"
        "v_mfma_f32_16x16x32_bf16 v[48:51], a[112:115], v[0:3], v[224:227]\n"
        "s_waitcnt vmcnt(10) lgkmcnt(1)\n"
        "v_exp_f32 v53, v53\n"
        "v_mfma_f32_16x16x32_bf16 v[48:51], a[116:119], v[4:7], v[48:51]\n"
        "global_load_lds_dwordx4 v254, s[70:71] offset:2048\n"
        "v_cvt_pk_bf16_f32 v22, v52, v53\n"
        "v_mfma_f32_16x16x32_bf16 v[48:51], a[120:123], v[8:11], v[48:51]\n"
        "v_exp_f32 v54, v54\n"
        "v_mfma_f32_16x16x32_bf16 v[48:51], a[124:127], v[12:15], v[48:51]\n"
        "v_exp_f32 v55, v55\n"
        "v_mfma_f32_16x16x32_bf16 v[160:163], a[144:147], v[16:19], v[160:163]\n"
        "ds_read_b128 a[140:143], v241 offset:23552\n"
        "v_cvt_pk_bf16_f32 v23, v54, v55\n"
        "v_mfma_f32_16x16x32_bf16 v[52:55], a[124:127], v[76:79], v[228:231]\n"
        "global_load_dword v244, v245, s[26:27] offset:0\n"
        "v_cndmask_b32 v44, v236, v242, s[30:31]\n"
        "v_mfma_f32_16x16x32_bf16 v[52:55], a[120:123], v[72:75], v[52:55]\n"
        "s_add_u32 s26, s26, 0x20000\n"
        "s_addc_u32 s27, s27, 0\n"
        "v_cndmask_b32 v45, v236, v242, s[56:57]\n"
        "v_mfma_f32_16x16x32_bf16 v[52:55], a[116:119], v[68:71], v[52:55]\n"
        "global_load_dword v244, v245, s[26:27] offset:0\n"
        "s_mov_b32 s30, 131072\n"
        "v_cndmask_b32 v46, v236, v242, s[58:59]\n"
        "v_mfma_f32_16x16x32_bf16 v[52:55], a[112:115], v[64:67], v[52:55]\n"
        "s_add_u32 s26, s26, 0x20000\n"
        "s_addc_u32 s27, s27, 0\n"
        "v_cndmask_b32 v47, v236, v242, s[60:61]\n"
        "s_nop 1\n"
        "v_mfma_f32_16x16x32_bf16 v[44:47], a[108:111], v[140:143], v[44:47]\n"
        "v_exp_f32 v56, v56\n"
        "v_mfma_f32_16x16x32_bf16 v[44:47], a[104:107], v[136:139], v[44:47]\n"
        "v_exp_f32 v57, v57\n"
        "v_mfma_f32_16x16x32_bf16 v[44:47], a[100:103], v[132:135], v[44:47]\n"
        "s_add_u32 s22, s18, s14\n"
        "s_addc_u32 s23, s19, 0\n"
        "v_cvt_pk_bf16_f32 v26, v56, v57\n"
        "v_mfma_f32_16x16x32_bf16 v[44:47], a[96:99], v[128:131], v[44:47]\n"
        "s_barrier\n"
        "v_smfmac_f32_16x16x64_bf16 a[64:67], a[72:75], v[16:23], v154\n"
        "s_sub_u32 s22, s22, 4096\n"
        "s_subb_u32 s23, s23, 0\n"
        "v_xor_b32 v240, 0x8000, v240\n"
        "v_mfma_f32_16x16x32_bf16 v[192:195], a[144:147], v[20:23], v[192:195]\n"
        "ds_read_b128 a[96:99], v240 offset:0\n"
        "s_lshl_b32 s14, s10, 18\n"
        "v_lshrrev_b32 v252, 2, v254\n"
        "v_mfma_f32_16x16x32_bf16 v[196:199], a[148:151], v[20:23], v[196:199]\n"
        "s_add_u32 s14, s14, s72\n"
        "v_exp_f32 v58, v58\n"
        "v_mfma_f32_16x16x32_bf16 v[164:167], a[148:151], v[16:19], v[164:167]\n"
        "s_add_u32 s14, s14, 0x2200000\n"
        "v_exp_f32 v59, v59\n"
        "v_mfma_f32_16x16x32_bf16 v[168:171], a[152:155], v[16:19], v[168:171]\n"
        "ds_read_b128 a[100:103], v240 offset:1024\n"
        "v_cvt_pk_bf16_f32 v27, v58, v59\n"
        "v_mfma_f32_16x16x32_bf16 v[56:59], a[112:115], v[96:99], v[232:235]\n"
        "ds_read_b128 a[104:107], v240 offset:2048\n"
        "v_add_f32 v60, v60, v236\n"
        "v_mfma_f32_16x16x32_bf16 v[56:59], a[116:119], v[100:103], v[56:59]\n"
        "v_exp_f32 v60, v60\n"
        "v_mfma_f32_16x16x32_bf16 v[56:59], a[120:123], v[104:107], v[56:59]\n"
        "ds_read_b128 a[108:111], v240 offset:3072\n"
        "v_add_f32 v61, v61, v236\n"
        "v_mfma_f32_16x16x32_bf16 v[56:59], a[124:127], v[108:111], v[56:59]\n"
        "v_exp_f32 v61, v61\n"
        "v_mfma_f32_16x16x32_bf16 a[0:3], a[144:147], v[24:27], a[0:3]\n"
        "s_add_u32 s20, s38, s14\n"
        "s_addc_u32 s21, s39, 0\n"
        "v_cvt_pk_bf16_f32 v30, v60, v61\n"
        "v_mfma_f32_16x16x32_bf16 a[4:7], a[148:151], v[24:27], a[4:7]\n"
        "s_waitcnt lgkmcnt(3)\n"
        "v_add_f32 v62, v62, v236\n"
        "v_add_f32 v63, v63, v236\n"
        "v_mfma_f32_16x16x32_bf16 v[200:203], a[152:155], v[20:23], v[200:203]\n"
        "v_exp_f32 v62, v62\n"
        "v_mfma_f32_16x16x32_bf16 a[8:11], a[152:155], v[24:27], a[8:11]\n"
        "v_exp_f32 v63, v63\n"
        "v_mfma_f32_16x16x32_bf16 a[12:15], a[156:159], v[24:27], a[12:15]\n"
        "v_cvt_pk_bf16_f32 v31, v62, v63\n"
        "s_nop 1\n"
        "v_smfmac_f32_16x16x64_bf16 a[68:71], a[72:75], v[24:31], v154\n"
        "v_cndmask_b32 v60, v236, v242, s[62:63]\n"
        "v_mfma_f32_16x16x32_bf16 a[32:35], a[144:147], v[28:31], 0\n"
        "v_exp_f32 v32, v32\n"
        "v_mfma_f32_16x16x32_bf16 a[36:39], a[148:151], v[28:31], 0\n"
        "v_exp_f32 v33, v33\n"
        "v_mfma_f32_16x16x32_bf16 a[40:43], a[152:155], v[28:31], 0\n"
        "ds_read_b128 a[144:147], v241 offset:24576\n"
        "v_cvt_pk_bf16_f32 v80, v32, v33\n"
        "v_mfma_f32_16x16x32_bf16 a[44:47], a[156:159], v[28:31], 0\n"
        "v_exp_f32 v34, v34\n"
        "v_mfma_f32_16x16x32_bf16 v[204:207], a[156:159], v[20:23], v[204:207]\n"
        "v_exp_f32 v35, v35\n"
        "v_mfma_f32_16x16x32_bf16 v[172:175], a[156:159], v[16:19], v[172:175]\n"
        "s_mov_b32 exec_hi, 0x7fffffff\n"
        "global_load_lds_dwordx4 v254, s[70:71] offset:3072\n"
        "s_mov_b64 exec, -1\n"
        "v_cvt_pk_bf16_f32 v81, v34, v35\n"
        "v_exp_f32 v36, v36\n"
        "v_exp_f32 v37, v37\n"
        "v_cndmask_b32 v61, v236, v242, s[84:85]\n"
        "v_cvt_pk_bf16_f32 v84, v36, v37\n"
        "v_cndmask_b32 v62, v236, v242, s[86:87]\n"
        "v_cndmask_b32 v63, v236, v242, s[88:89]\n"
        "s_nop 1\n"
        "v_mfma_f32_16x16x32_bf16 v[60:63], a[124:127], v[140:143], v[60:63]\n"
        "v_exp_f32 v38, v38\n"
        "v_mfma_f32_16x16x32_bf16 v[60:63], a[120:123], v[136:139], v[60:63]\n"
        "v_exp_f32 v39, v39\n"
        "v_mfma_f32_16x16x32_bf16 v[60:63], a[116:119], v[132:135], v[60:63]\n"
        "ds_read_b128 a[116:119], v240 offset:5120\n"
        "v_cvt_pk_bf16_f32 v85, v38, v39\n"
        "v_mfma_f32_16x16x32_bf16 v[60:63], a[112:115], v[128:131], v[60:63]\n"
        "s_mov_b64 exec, 7\n"
        "global_load_lds_dword v252, s[70:71] offset:4080\n"
        "s_mov_b64 exec, -1\n"
        "ds_read_b128 a[112:115], v240 offset:4096\n"
        "v_exp_f32 v40, v40\n"
        "v_exp_f32 v41, v41\n"
        "v_exp_f32 v42, v42\n"
        "v_cvt_pk_bf16_f32 v88, v40, v41\n"
        "v_exp_f32 v43, v43\n"
        "ds_read_b128 a[148:151], v241 offset:25600\n"
        "v_cvt_pk_bf16_f32 v89, v42, v43\n"
        "v_exp_f32 v44, v44\n"
        "v_exp_f32 v45, v45\n"
        "ds_read_b128 a[152:155], v241 offset:26624\n"
        "v_cvt_pk_bf16_f32 v92, v44, v45\n"
        "v_exp_f32 v46, v46\n"
        "v_exp_f32 v47, v47\n"
        "ds_read_b128 a[120:123], v240 offset:6144\n"
        "v_cvt_pk_bf16_f32 v93, v46, v47\n"
        "s_cmp_eq_u32 s44, 0\n"
        "s_cbranch_scc1 LLD_%=\n"
        "; ---- DUAL\n"
        "s_branch LDU_%=\n"
        "s_nop 0\n"
        "s_nop 0\n"
        "s_nop 0\n"
        "s_nop 0\n"
        "s_nop 0\n"
        "s_nop 0\n"
        "s_nop 0\n"
        "s_nop 0\n"
        "s_nop 0\n"
        "s_nop 0\n"
        "s_nop 0\n"
        "s_nop 0\n"
        "s_nop 0\n"
        "s_nop 0\n"
        "s_nop 0\n"
        "s_nop 0\n"
        "s_nop 0\n"
        "s_nop 0\n"
        "s_nop 0\n"
        "s_nop 0\n"
        "s_nop 0\n"
        "s_nop 0\n"
        "s_nop 0\n"
        "s_nop 0\n"
        "s_nop 0\n"
        "s_nop 0\n"
        "s_nop 0\n"
        "s_nop 0\n"
        "s_nop 0\n"
        "s_nop 0\n"
        "s_nop 0\n"
        "s_nop 0\n"
        "s_nop 0\n"
        "s_nop 0\n"
        "s_nop 0\n"
        "s_nop 0\n"
        "s_nop 0\n"
        "s_nop 0\n"
        "s_nop 0\n"
        "s_nop 0\n"
        "s_nop 0\n"
        "s_nop 0\n"
        "s_nop 0\n"
        "s_nop 0\n"
        "s_nop 0\n"
        "s_nop 0\n"
        "s_nop 0\n"
        "s_nop 0\n"
        "s_nop 0\n"
        "s_nop 0\n"
        "s_nop 0\n"
        "s_nop 0\n"
        "s_nop 0\n"
        "s_nop 0\n"
        "s_nop 0\n"
        "s_nop 0\n"
        "s_nop 0\n"
        "s_nop 0\n"
        "s_nop 0\n"
        "s_nop 0\n"
        "s_nop 0\n"
        "s_nop 0\n"
        "s_nop 0\n"
        "s_nop 0\n"
        "s_nop 0\n"
        "s_nop 0\n"
        "s_nop 0\n"
        "s_nop 0\n"
        "s_nop 0\n"
        "s_nop 0\n"
        "s_nop 0\n"
        "s_nop 0\n"
        "s_nop 0\n"
        "s_nop 0\n"
        "s_nop 0\n"
        "LHA11_%=:\n"
        "s_cbranch_execz LHRA_%=\n"
        "s_nop 0\n"
        "s_nop 0\n"
        "s_nop 0\n"
        "s_nop 0\n"
        "s_nop 0\n"
        "s_nop 0\n"
        "s_nop 0\n"
        "s_nop 0\n"
        "s_nop 0\n"
        "s_nop 0\n"
        "s_nop 0\n"
        "s_nop 0\n"
        "s_nop 0\n"
        "s_nop 0\n"
        "s_nop 0\n"
        "LDU_%=:\n"
        "v_mfma_f32_16x16x32_bf16 v[32:35], a[96:99], v[0:3], v[224:227]\n"
        "s_waitcnt lgkmcnt(4)\n"
        "ds_read_b128 a[156:159], v241 offset:27648\n"
        "s_add_u32 m0, s80, 0x0\n"
        "v_mfma_f32_16x16x32_bf16 v[32:35], a[100:103], v[4:7], v[32:35]\n"
        "ds_read_b128 a[124:127], v240 offset:7168\n"
        "global_load_lds_dwordx4 v254, s[68:69] offset:0 sc1\n"
        "s_xor_b32 s79, s79, 0x8000\n"
        "v_mfma_f32_16x16x32_bf16 v[32:35], a[104:107], v[8:11], v[32:35]\n"
        "v_exp_f32 v48, v48\n"
        "v_mfma_f32_16x16x32_bf16 v[32:35], a[108:111], v[12:15], v[32:35]\n"
        "v_exp_f32 v49, v49\n"
        "v_mfma_f32_16x16x32_bf16 v[36:39], a[108:111], v[76:79], v[228:231]\n"
        "v_exp_f32 v50, v50\n"
        "v_mfma_f32_16x16x32_bf16 v[36:39], a[104:107], v[72:75], v[36:39]\n"
        "v_exp_f32 v51, v51\n"
        "v_mfma_f32_16x16x32_bf16 v[36:39], a[100:103], v[68:71], v[36:39]\n"
        "v_exp_f32 v52, v52\n"
        "v_mfma_f32_16x16x32_bf16 v[36:39], a[96:99], v[64:67], v[36:39]\n"
        "v_exp_f32 v53, v53\n"
        "v_mfma_f32_16x16x32_bf16 v[40:43], a[96:99], v[96:99], v[232:235]\n"
        "v_exp_f32 v55, v55\n"
        "v_mfma_f32_16x16x32_bf16 v[40:43], a[100:103], v[100:103], v[40:43]\n"
        "v_exp_f32 v54, v54\n"
        "v_mfma_f32_16x16x32_bf16 v[40:43], a[104:107], v[104:107], v[40:43]\n"
        "v_exp_f32 v56, v56\n"
        "v_mfma_f32_16x16x32_bf16 v[40:43], a[108:111], v[108:111], v[40:43]\n"
        "v_exp_f32 v57, v57\n"
        "v_mfma_f32_16x16x32_bf16 v[44:47], a[108:111], v[140:143], v[236:239]\n"
        "ds_read_b128 a[108:111], v240 offset:11264\n"
        "v_cvt_pk_bf16_f32 v82, v48, v49\n"
        "v_mfma_f32_16x16x32_bf16 v[44:47], a[104:107], v[136:139], v[44:47]\n"
        "ds_read_b128 a[104:107], v240 offset:10240\n"
        "global_load_lds_dwordx4 v254, s[68:69] offset:1024 sc1\n"
        "v_mfma_f32_16x16x32_bf16 v[44:47], a[100:103], v[132:135], v[44:47]\n"
        "ds_read_b128 a[100:103], v240 offset:9216\n"
        "v_cvt_pk_bf16_f32 v83, v50, v51\n"
        "v_mfma_f32_16x16x32_bf16 v[44:47], a[96:99], v[128:131], v[44:47]\n"
        "ds_read_b128 a[96:99], v240 offset:8192\n"
        "v_cvt_pk_bf16_f32 v86, v52, v53\n"
        "v_mfma_f32_16x16x32_bf16 v[176:179], a[128:131], v[16:19], v[176:179]\n"
        "v_exp_f32 v58, v58\n"
        "v_mfma_f32_16x16x32_bf16 v[208:211], a[128:131], v[20:23], v[208:211]\n"
        "v_exp_f32 v59, v59\n"
        "v_mfma_f32_16x16x32_bf16 a[16:19], a[128:131], v[24:27], a[16:19]\n"
        "v_exp_f32 v60, v60\n"
        "v_mfma_f32_16x16x32_bf16 a[48:51], a[128:131], v[28:31], a[48:51]\n"
        "ds_read_b128 a[128:131], v241 offset:28672\n"
        "global_load_lds_dwordx4 v254, s[68:69] offset:2048 sc1\n"
        "v_mfma_f32_16x16x32_bf16 a[52:55], a[132:135], v[28:31], a[52:55]\n"
        "v_exp_f32 v61, v61\n"
        "v_mfma_f32_16x16x32_bf16 a[20:23], a[132:135], v[24:27], a[20:23]\n"
        "v_exp_f32 v62, v62\n"
        "v_mfma_f32_16x16x32_bf16 v[212:215], a[132:135], v[20:23], v[212:215]\n"
        "v_exp_f32 v63, v63\n"
        "v_mfma_f32_16x16x32_bf16 v[180:183], a[132:135], v[16:19], v[180:183]\n"
        "ds_read_b128 a[132:135], v241 offset:29696\n"
        "v_cvt_pk_bf16_f32 v87, v54, v55\n"
        "v_mfma_f32_16x16x32_bf16 v[184:187], a[136:139], v[16:19], v[184:187]\n"
        "v_exp_f32 v32, v32\n"
        "v_mfma_f32_16x16x32_bf16 v[216:219], a[136:139], v[20:23], v[216:219]\n"
        "v_exp_f32 v33, v33\n"
        "v_mfma_f32_16x16x32_bf16 a[24:27], a[136:139], v[24:27], a[24:27]\n"
        "v_exp_f32 v34, v34\n"
        "v_mfma_f32_16x16x32_bf16 a[56:59], a[136:139], v[28:31], a[56:59]\n"
        "ds_read_b128 a[136:139], v241 offset:30720\n"
        "v_cvt_pk_bf16_f32 v90, v56, v57\n"
        "v_mfma_f32_16x16x32_bf16 a[60:63], a[140:143], v[28:31], a[60:63]\n"
        "v_exp_f32 v35, v35\n"
        "v_mfma_f32_16x16x32_bf16 a[28:31], a[140:143], v[24:27], a[28:31]\n"
        "v_exp_f32 v36, v36\n"
        "v_mfma_f32_16x16x32_bf16 v[220:223], a[140:143], v[20:23], v[220:223]\n"
        "s_waitcnt lgkmcnt(2)\n"
        "v_exp_f32 v37, v37\n"
        "v_mfma_f32_16x16x32_bf16 v[188:191], a[140:143], v[16:19], v[188:191]\n"
        "ds_read_b128 a[140:143], v241 offset:31744\n"
        "v_cvt_pk_bf16_f32 v91, v58, v59\n"
        "v_mfma_f32_16x16x32_bf16 v[48:51], a[112:115], v[0:3], v[224:227]\n"
        "v_exp_f32 v38, v38\n"
        "v_mfma_f32_16x16x32_bf16 v[48:51], a[116:119], v[4:7], v[48:51]\n"
        "v_exp_f32 v39, v39\n"
        "v_mfma_f32_16x16x32_bf16 v[48:51], a[120:123], v[8:11], v[48:51]\n"
        "v_exp_f32 v40, v40\n"
        "v_mfma_f32_16x16x32_bf16 v[48:51], a[124:127], v[12:15], v[48:51]\n"
        "v_exp_f32 v41, v41\n"
        "v_mfma_f32_16x16x32_bf16 v[52:55], a[124:127], v[76:79], v[228:231]\n"
        "v_exp_f32 v42, v42\n"
        "v_mfma_f32_16x16x32_bf16 v[52:55], a[120:123], v[72:75], v[52:55]\n"
        "v_exp_f32 v43, v43\n"
        "v_mfma_f32_16x16x32_bf16 v[52:55], a[116:119], v[68:71], v[52:55]\n"
        "v_exp_f32 v44, v44\n"
        "v_mfma_f32_16x16x32_bf16 v[52:55], a[112:115], v[64:67], v[52:55]\n"
        "s_waitcnt vmcnt(5) lgkmcnt(0)\n"
        "v_exp_f32 v45, v45\n"
        "v_mfma_f32_16x16x32_bf16 v[56:59], a[112:115], v[96:99], v[232:235]\n"
        "s_barrier\n"
        "v_mfma_f32_16x16x32_bf16 v[56:59], a[116:119], v[100:103], v[56:59]\n"
        "v_exp_f32 v46, v46\n"
        "v_mfma_f32_16x16x32_bf16 v[56:59], a[120:123], v[104:107], v[56:59]\n"
        "v_exp_f32 v47, v47\n"
        "v_mfma_f32_16x16x32_bf16 v[56:59], a[124:127], v[108:111], v[56:59]\n"
        "v_exp_f32 v48, v48\n"
        "v_smfmac_f32_16x16x64_bf16 a[64:67], a[72:75], v[80:87], v154\n"
        "global_load_lds_dwordx4 v254, s[68:69] offset:3072 sc1\n"
        "s_add_u32 m0, s81, 0x0\n"
        "v_cvt_pk_bf16_f32 v94, v60, v61\n"
        "v_mfma_f32_16x16x32_bf16 v[160:163], a[144:147], v[80:83], v[160:163]\n"
        "s_add_u32 s82, s82, 1\n"
        "v_exp_f32 v49, v49\n"
        "v_mfma_f32_16x16x32_bf16 v[192:195], a[144:147], v[84:87], v[192:195]\n"
        "s_xor_b32 s78, s78, 0x8000\n"
        "v_exp_f32 v50, v50\n"
        "v_mfma_f32_16x16x32_bf16 a[0:3], a[144:147], v[88:91], a[0:3]\n"
        "s_add_u32 s81, s78, 4096\n"
        "v_exp_f32 v51, v51\n"
        "v_mfma_f32_16x16x32_bf16 a[4:7], a[148:151], v[88:91], a[4:7]\n"
        "s_mov_b32 s80, s78\n"
        "v_exp_f32 v52, v52\n"
        "v_mfma_f32_16x16x32_bf16 v[196:199], a[148:151], v[84:87], v[196:199]\n"
        "v_exp_f32 v53, v53\n"
        "v_mfma_f32_16x16x32_bf16 v[164:167], a[148:151], v[80:83], v[164:167]\n"
        "v_exp_f32 v54, v54\n"
        "v_mfma_f32_16x16x32_bf16 v[168:171], a[152:155], v[80:83], v[168:171]\n"
        "v_exp_f32 v55, v55\n"
        "v_mfma_f32_16x16x32_bf16 v[200:203], a[152:155], v[84:87], v[200:203]\n"
        "v_exp_f32 v56, v56\n"
        "v_mfma_f32_16x16x32_bf16 a[8:11], a[152:155], v[88:91], a[8:11]\n"
        "v_exp_f32 v57, v57\n"
        "v_mfma_f32_16x16x32_bf16 a[12:15], a[156:159], v[88:91], a[12:15]\n"
        "v_exp_f32 v58, v58\n"
        "v_mfma_f32_16x16x32_bf16 v[204:207], a[156:159], v[84:87], v[204:207]\n"
        "v_exp_f32 v59, v59\n"
        "v_mfma_f32_16x16x32_bf16 v[172:175], a[156:159], v[80:83], v[172:175]\n"
        "global_load_lds_dwordx4 v254, s[74:75] offset:0 sc1\n"
        "v_cvt_pk_bf16_f32 v95, v62, v63\n"
        "v_mfma_f32_16x16x32_bf16 v[60:63], a[124:127], v[140:143], v[236:239]\n"
        "ds_read_b128 a[124:127], v240 offset:15360\n"
        "v_cvt_pk_bf16_f32 v16, v32, v33\n"
        "v_mfma_f32_16x16x32_bf16 v[60:63], a[120:123], v[136:139], v[60:63]\n"
        "ds_read_b128 a[120:123], v240 offset:14336\n"
        "v_cvt_pk_bf16_f32 v17, v34, v35\n"
        "v_mfma_f32_16x16x32_bf16 v[60:63], a[116:119], v[132:135], v[60:63]\n"
        "ds_read_b128 a[116:119], v240 offset:13312\n"
        "v_cvt_pk_bf16_f32 v20, v36, v37\n"
        "v_mfma_f32_16x16x32_bf16 v[60:63], a[112:115], v[128:131], v[60:63]\n"
        "ds_read_b128 a[112:115], v240 offset:12288\n"
        "global_load_lds_dwordx4 v254, s[74:75] offset:1024 sc1\n"
        "v_smfmac_f32_16x16x64_bf16 a[68:71], a[72:75], v[88:95], v154\n"
        "s_add_u32 s76, s66, s90\n"
        "s_addc_u32 s77, s67, 0\n"
        "v_cvt_pk_bf16_f32 v21, v38, v39\n"
        "v_mfma_f32_16x16x32_bf16 a[32:35], a[144:147], v[92:95], a[32:35]\n"
        "s_cmp_ge_u32 s82, s44\n"
        "s_addc_u32 s90, s82, 0\n"
        "v_cvt_pk_bf16_f32 v24, v40, v41\n"
        "v_mfma_f32_16x16x32_bf16 a[36:39], a[148:151], v[92:95], a[36:39]\n"
        "s_lshl_b32 s90, s90, 15\n"
        "v_exp_f32 v60, v60\n"
        "v_mfma_f32_16x16x32_bf16 a[40:43], a[152:155], v[92:95], a[40:43]\n"
        "v_exp_f32 v61, v61\n"
        "v_mfma_f32_16x16x32_bf16 a[44:47], a[156:159], v[92:95], a[44:47]\n"
        "v_exp_f32 v62, v62\n"
        "v_mfma_f32_16x16x32_bf16 v[32:35], a[96:99], v[0:3], v[224:227]\n"
        "v_exp_f32 v63, v63\n"
        "v_mfma_f32_16x16x32_bf16 v[32:35], a[100:103], v[4:7], v[32:35]\n"
        "global_load_lds_dwordx4 v254, s[74:75] offset:2048 sc1\n"
        "v_xor_b32 v241, 0x8000, v241\n"
        "v_mfma_f32_16x16x32_bf16 v[32:35], a[104:107], v[8:11], v[32:35]\n"
        "ds_read_b128 a[144:147], v241 offset:0\n"
        "v_cvt_pk_bf16_f32 v25, v42, v43\n"
        "v_mfma_f32_16x16x32_bf16 v[32:35], a[108:111], v[12:15], v[32:35]\n"
        "ds_read_b128 a[148:151], v241 offset:1024\n"
        "v_cvt_pk_bf16_f32 v28, v44, v45\n"
        "v_mfma_f32_16x16x32_bf16 v[36:39], a[108:111], v[76:79], v[228:231]\n"
        "ds_read_b128 a[152:155], v241 offset:2048\n"
        "v_cvt_pk_bf16_f32 v29, v46, v47\n"
        "v_mfma_f32_16x16x32_bf16 v[36:39], a[104:107], v[72:75], v[36:39]\n"
        "ds_read_b128 a[156:159], v241 offset:3072\n"
        "global_load_lds_dwordx4 v254, s[74:75] offset:3072 sc1\n"
        "s_add_u32 m0, s79, 0x0\n"
        "v_mfma_f32_16x16x32_bf16 v[36:39], a[100:103], v[68:71], v[36:39]\n"
        "v_exp_f32 v32, v32\n"
        "v_mfma_f32_16x16x32_bf16 v[36:39], a[96:99], v[64:67], v[36:39]\n"
        "v_exp_f32 v33, v33\n"
        "v_mfma_f32_16x16x32_bf16 v[40:43], a[96:99], v[96:99], v[232:235]\n"
        "v_exp_f32 v34, v34\n"
        "v_mfma_f32_16x16x32_bf16 v[40:43], a[100:103], v[100:103], v[40:43]\n"
        "v_exp_f32 v35, v35\n"
        "v_mfma_f32_16x16x32_bf16 v[40:43], a[104:107], v[104:107], v[40:43]\n"
        "global_load_lds_dwordx4 v144, s[76:77] offset:0 sc1\n"
        "v_cvt_pk_bf16_f32 v18, v48, v49\n"
        "v_mfma_f32_16x16x32_bf16 v[40:43], a[108:111], v[108:111], v[40:43]\n"
        "v_exp_f32 v36, v36\n"
        "v_mfma_f32_16x16x32_bf16 v[44:47], a[108:111], v[140:143], v[236:239]\n"
        "ds_read_b128 a[108:111], v240 offset:19456\n"
        "v_cvt_pk_bf16_f32 v19, v50, v51\n"
        "v_mfma_f32_16x16x32_bf16 v[44:47], a[104:107], v[136:139], v[44:47]\n"
        "ds_read_b128 a[104:107], v240 offset:18432\n"
        "v_cvt_pk_bf16_f32 v22, v52, v53\n"
        "v_mfma_f32_16x16x32_bf16 v[44:47], a[100:103], v[132:135], v[44:47]\n"
        "ds_read_b128 a[100:103], v240 offset:17408\n"
        "global_load_lds_dwordx4 v145, s[76:77] offset:1024 sc1\n"
        "v_mfma_f32_16x16x32_bf16 v[44:47], a[96:99], v[128:131], v[44:47]\n"
        "ds_read_b128 a[96:99], v240 offset:16384\n"
        "v_cvt_pk_bf16_f32 v23, v54, v55\n"
        "v_mfma_f32_16x16x32_bf16 v[176:179], a[128:131], v[80:83], v[176:179]\n"
        "v_exp_f32 v37, v37\n"
        "v_mfma_f32_16x16x32_bf16 v[208:211], a[128:131], v[84:87], v[208:211]\n"
        "v_exp_f32 v38, v38\n"
        "v_mfma_f32_16x16x32_bf16 a[16:19], a[128:131], v[88:91], a[16:19]\n"
        "v_exp_f32 v39, v39\n"
        "v_mfma_f32_16x16x32_bf16 a[48:51], a[128:131], v[92:95], a[48:51]\n"
        "ds_read_b128 a[128:131], v241 offset:4096\n"
        "global_load_lds_dwordx4 v146, s[76:77] offset:2048 sc1\n"
        "v_mfma_f32_16x16x32_bf16 a[52:55], a[132:135], v[92:95], a[52:55]\n"
        "v_exp_f32 v40, v40\n"
        "v_mfma_f32_16x16x32_bf16 a[20:23], a[132:135], v[88:91], a[20:23]\n"
        "v_exp_f32 v41, v41\n"
        "v_mfma_f32_16x16x32_bf16 v[212:215], a[132:135], v[84:87], v[212:215]\n"
        "v_exp_f32 v42, v42\n"
        "v_mfma_f32_16x16x32_bf16 v[180:183], a[132:135], v[80:83], v[180:183]\n"
        "ds_read_b128 a[132:135], v241 offset:5120\n"
        "global_load_lds_dwordx4 v147, s[76:77] offset:3072 sc1\n"
        "s_add_u32 m0, s79, s28\n"
        "v_mfma_f32_16x16x32_bf16 v[184:187], a[136:139], v[80:83], v[184:187]\n"
        "v_exp_f32 v43, v43\n"
        "v_mfma_f32_16x16x32_bf16 v[216:219], a[136:139], v[84:87], v[216:219]\n"
        "v_exp_f32 v44, v44\n"
        "v_mfma_f32_16x16x32_bf16 a[24:27], a[136:139], v[88:91], a[24:27]\n"
        "v_exp_f32 v45, v45\n"
        "v_mfma_f32_16x16x32_bf16 a[56:59], a[136:139], v[92:95], a[56:59]\n"
        "ds_read_b128 a[136:139], v241 offset:6144\n"
        "s_add_u32 s76, s76, s25\n"
        "s_addc_u32 s77, s77, 0\n"
        "v_mfma_f32_16x16x32_bf16 a[60:63], a[140:143], v[92:95], a[60:63]\n"
        "s_waitcnt lgkmcnt(2)\n"
        "v_exp_f32 v46, v46\n"
        "v_mfma_f32_16x16x32_bf16 a[28:31], a[140:143], v[88:91], a[28:31]\n"
        "v_exp_f32 v47, v47\n"
        "v_mfma_f32_16x16x32_bf16 v[220:223], a[140:143], v[84:87], v[220:223]\n"
        "global_load_lds_dwordx4 v144, s[76:77] offset:0 sc1\n"
        "v_cvt_pk_bf16_f32 v26, v56, v57\n"
        "v_mfma_f32_16x16x32_bf16 v[188:191], a[140:143], v[80:83], v[188:191]\n"
        "ds_read_b128 a[140:143], v241 offset:7168\n"
        "v_cvt_pk_bf16_f32 v27, v58, v59\n"
        "v_mfma_f32_16x16x32_bf16 v[48:51], a[112:115], v[0:3], v[224:227]\n"
        "s_add_u32 s68, s64, s90\n"
        "s_addc_u32 s69, s65, 0\n"
        "v_cvt_pk_bf16_f32 v30, v60, v61\n"
        "v_mfma_f32_16x16x32_bf16 v[48:51], a[116:119], v[4:7], v[48:51]\n"
        "s_add_u32 s74, s68, 0x1000\n"
        "s_addc_u32 s75, s69, 0\n"
        "v_cvt_pk_bf16_f32 v31, v62, v63\n"
        "v_mfma_f32_16x16x32_bf16 v[48:51], a[120:123], v[8:11], v[48:51]\n"
        "global_load_lds_dwordx4 v145, s[76:77] offset:1024 sc1\n"
        "v_cvt_pk_bf16_f32 v80, v32, v33\n"
        "v_mfma_f32_16x16x32_bf16 v[48:51], a[124:127], v[12:15], v[48:51]\n"
        "v_cvt_pk_bf16_f32 v81, v34, v35\n"
        "v_cvt_pk_bf16_f32 v84, v36, v37\n"
        "v_mfma_f32_16x16x32_bf16 v[52:55], a[124:127], v[76:79], v[228:231]\n"
        "v_cvt_pk_bf16_f32 v85, v38, v39\n"
        "v_cvt_pk_bf16_f32 v88, v40, v41\n"
        "v_mfma_f32_16x16x32_bf16 v[52:55], a[120:123], v[72:75], v[52:55]\n"
        "v_cvt_pk_bf16_f32 v89, v42, v43\n"
        "v_cvt_pk_bf16_f32 v92, v44, v45\n"
        "v_mfma_f32_16x16x32_bf16 v[52:55], a[116:119], v[68:71], v[52:55]\n"
        "v_exp_f32 v48, v48\n"
        "v_mfma_f32_16x16x32_bf16 v[52:55], a[112:115], v[64:67], v[52:55]\n"
        "v_exp_f32 v49, v49\n"
        "v_mfma_f32_16x16x32_bf16 v[56:59], a[112:115], v[96:99], v[232:235]\n"
        "v_exp_f32 v50, v50\n"
        "v_mfma_f32_16x16x32_bf16 v[56:59], a[116:119], v[100:103], v[56:59]\n"
        "v_exp_f32 v51, v51\n"
        "v_mfma_f32_16x16x32_bf16 v[56:59], a[120:123], v[104:107], v[56:59]\n"
        "global_load_lds_dwordx4 v146, s[76:77] offset:2048 sc1\n"
        "v_cvt_pk_bf16_f32 v93, v46, v47\n"
        "v_mfma_f32_16x16x32_bf16 v[56:59], a[124:127], v[108:111], v[56:59]\n"
        "v_exp_f32 v52, v52\n"
        "v_mfma_f32_16x16x32_bf16 v[60:63], a[124:127], v[140:143], v[236:239]\n"
        "ds_read_b128 a[124:127], v240 offset:23552\n"
        "v_cvt_pk_bf16_f32 v82, v48, v49\n"
        "v_mfma_f32_16x16x32_bf16 v[60:63], a[120:123], v[136:139], v[60:63]\n"
        "ds_read_b128 a[120:123], v240 offset:22528\n"
        "v_cvt_pk_bf16_f32 v83, v50, v51\n"
        "v_mfma_f32_16x16x32_bf16 v[60:63], a[116:119], v[132:135], v[60:63]\n"
        "ds_read_b128 a[116:119], v240 offset:21504\n"
        "global_load_lds_dwordx4 v147, s[76:77] offset:3072 sc1\n"
        "v_mfma_f32_16x16x32_bf16 v[60:63], a[112:115], v[128:131], v[60:63]\n"
        "ds_read_b128 a[112:115], v240 offset:20480\n"
        "v_smfmac_f32_16x16x64_bf16 a[64:67], a[72:75], v[16:23], v154\n"
        "v_smfmac_f32_16x16x64_bf16 a[68:71], a[72:75], v[24:31], v154\n"
        "v_mfma_f32_16x16x32_bf16 v[160:163], a[144:147], v[16:19], v[160:163]\n"
        "v_exp_f32 v53, v53\n"
        "v_mfma_f32_16x16x32_bf16 v[192:195], a[144:147], v[20:23], v[192:195]\n"
        "v_exp_f32 v54, v54\n"
        "v_mfma_f32_16x16x32_bf16 a[0:3], a[144:147], v[24:27], a[0:3]\n"
        "v_exp_f32 v55, v55\n"
        "v_mfma_f32_16x16x32_bf16 a[32:35], a[144:147], v[28:31], a[32:35]\n"
        "ds_read_b128 a[144:147], v241 offset:8192\n"
        "global_load_dword v244, v245, s[26:27] offset:0\n"
        "v_mfma_f32_16x16x32_bf16 a[36:39], a[148:151], v[28:31], a[36:39]\n"
        "v_exp_f32 v56, v56\n"
        "v_mfma_f32_16x16x32_bf16 a[4:7], a[148:151], v[24:27], a[4:7]\n"
        "v_exp_f32 v57, v57\n"
        "v_mfma_f32_16x16x32_bf16 v[196:199], a[148:151], v[20:23], v[196:199]\n"
        "v_exp_f32 v58, v58\n"
        "v_mfma_f32_16x16x32_bf16 v[164:167], a[148:151], v[16:19], v[164:167]\n"
        "ds_read_b128 a[148:151], v241 offset:9216\n"
        "s_add_u32 s26, s26, 0x20000\n"
        "s_addc_u32 s27, s27, 0\n"
        "v_mfma_f32_16x16x32_bf16 v[168:171], a[152:155], v[16:19], v[168:171]\n"
        "v_exp_f32 v59, v59\n"
        "v_mfma_f32_16x16x32_bf16 v[200:203], a[152:155], v[20:23], v[200:203]\n"
        "v_exp_f32 v60, v60\n"
        "v_mfma_f32_16x16x32_bf16 a[8:11], a[152:155], v[24:27], a[8:11]\n"
        "v_exp_f32 v61, v61\n"
        "v_mfma_f32_16x16x32_bf16 a[40:43], a[152:155], v[28:31], a[40:43]\n"
        "ds_read_b128 a[152:155], v241 offset:10240\n"
        "v_cvt_pk_bf16_f32 v86, v52, v53\n"
        "v_mfma_f32_16x16x32_bf16 a[44:47], a[156:159], v[28:31], a[44:47]\n"
        "v_exp_f32 v62, v62\n"
        "v_mfma_f32_16x16x32_bf16 a[12:15], a[156:159], v[24:27], a[12:15]\n"
        "v_exp_f32 v63, v63\n"
        "v_mfma_f32_16x16x32_bf16 v[204:207], a[156:159], v[20:23], v[204:207]\n"
        "v_cvt_pk_bf16_f32 v87, v54, v55\n"
        "v_cvt_pk_bf16_f32 v90, v56, v57\n"
        "v_mfma_f32_16x16x32_bf16 v[172:175], a[156:159], v[16:19], v[172:175]\n"
        "ds_read_b128 a[156:159], v241 offset:11264\n"
        "v_cvt_pk_bf16_f32 v91, v58, v59\n"
        "v_mfma_f32_16x16x32_bf16 v[32:35], a[96:99], v[0:3], v[224:227]\n"
        "v_cvt_pk_bf16_f32 v94, v60, v61\n"
        "v_cvt_pk_bf16_f32 v95, v62, v63\n"
        "v_mfma_f32_16x16x32_bf16 v[32:35], a[100:103], v[4:7], v[32:35]\n"
        "v_mfma_f32_16x16x32_bf16 v[32:35], a[104:107], v[8:11], v[32:35]\n"
        "v_mfma_f32_16x16x32_bf16 v[32:35], a[108:111], v[12:15], v[32:35]\n"
        "v_mfma_f32_16x16x32_bf16 v[36:39], a[108:111], v[76:79], v[228:231]\n"
        "v_mfma_f32_16x16x32_bf16 v[36:39], a[104:107], v[72:75], v[36:39]\n"
        "v_mfma_f32_16x16x32_bf16 v[36:39], a[100:103], v[68:71], v[36:39]\n"
        "v_mfma_f32_16x16x32_bf16 v[36:39], a[96:99], v[64:67], v[36:39]\n"
        "v_mfma_f32_16x16x32_bf16 v[40:43], a[96:99], v[96:99], v[232:235]\n"
        "v_mfma_f32_16x16x32_bf16 v[40:43], a[100:103], v[100:103], v[40:43]\n"
        "v_mfma_f32_16x16x32_bf16 v[40:43], a[104:107], v[104:107], v[40:43]\n"
        "v_mfma_f32_16x16x32_bf16 v[40:43], a[108:111], v[108:111], v[40:43]\n"
        "v_exp_f32 v32, v32\n"
        "v_mfma_f32_16x16x32_bf16 v[44:47], a[108:111], v[140:143], v[236:239]\n"
        "v_exp_f32 v33, v33\n"
        "v_mfma_f32_16x16x32_bf16 v[44:47], a[104:107], v[136:139], v[44:47]\n"
        "ds_read_b128 a[104:107], v240 offset:26624\n"
        "v_mfma_f32_16x16x32_bf16 v[44:47], a[100:103], v[132:135], v[44:47]\n"
        "ds_read_b128 a[100:103], v240 offset:25600\n"
        "v_mfma_f32_16x16x32_bf16 v[44:47], a[96:99], v[128:131], v[44:47]\n"
        "ds_read_b128 a[96:99], v240 offset:24576\n"
        "v_mfma_f32_16x16x32_bf16 v[176:179], a[128:131], v[16:19], v[176:179]\n"
        "s_waitcnt lgkmcnt(3)\n"
        "v_exp_f32 v34, v34\n"
        "v_mfma_f32_16x16x32_bf16 v[208:211], a[128:131], v[20:23], v[208:211]\n"
        "v_exp_f32 v35, v35\n"
        "v_mfma_f32_16x16x32_bf16 a[16:19], a[128:131], v[24:27], a[16:19]\n"
        "ds_read_b128 a[108:111], v240 offset:27648\n"
        "v_mfma_f32_16x16x32_bf16 a[48:51], a[128:131], v[28:31], a[48:51]\n"
        "ds_read_b128 a[128:131], v241 offset:12288\n"
        "v_mfma_f32_16x16x32_bf16 a[52:55], a[132:135], v[28:31], a[52:55]\n"
        "v_exp_f32 v36, v36\n"
        "v_mfma_f32_16x16x32_bf16 a[20:23], a[132:135], v[24:27], a[20:23]\n"
        "v_exp_f32 v37, v37\n"
        "v_mfma_f32_16x16x32_bf16 v[212:215], a[132:135], v[20:23], v[212:215]\n"
        "v_exp_f32 v38, v38\n"
        "v_mfma_f32_16x16x32_bf16 v[180:183], a[132:135], v[16:19], v[180:183]\n"
        "ds_read_b128 a[132:135], v241 offset:13312\n"
        "v_mfma_f32_16x16x32_bf16 v[184:187], a[136:139], v[16:19], v[184:187]\n"
        "v_exp_f32 v39, v39\n"
        "v_mfma_f32_16x16x32_bf16 v[216:219], a[136:139], v[20:23], v[216:219]\n"
        "v_exp_f32 v40, v40\n"
        "v_mfma_f32_16x16x32_bf16 a[24:27], a[136:139], v[24:27], a[24:27]\n"
        "v_exp_f32 v41, v41\n"
        "v_mfma_f32_16x16x32_bf16 a[56:59], a[136:139], v[28:31], a[56:59]\n"
        "ds_read_b128 a[136:139], v241 offset:14336\n"
        "v_mfma_f32_16x16x32_bf16 a[60:63], a[140:143], v[28:31], a[60:63]\n"
        "v_exp_f32 v42, v42\n"
        "v_mfma_f32_16x16x32_bf16 a[28:31], a[140:143], v[24:27], a[28:31]\n"
        "v_exp_f32 v43, v43\n"
        "v_mfma_f32_16x16x32_bf16 v[220:223], a[140:143], v[20:23], v[220:223]\n"
        "v_exp_f32 v44, v44\n"
        "v_mfma_f32_16x16x32_bf16 v[188:191], a[140:143], v[16:19], v[188:191]\n"
        "ds_read_b128 a[140:143], v241 offset:15360\n"
        "global_load_dword v244, v245, s[26:27] offset:0\n"
        "v_mfma_f32_16x16x32_bf16 v[48:51], a[112:115], v[0:3], v[224:227]\n"
        "v_exp_f32 v45, v45\n"
        "v_mfma_f32_16x16x32_bf16 v[48:51], a[116:119], v[4:7], v[48:51]\n"
        "v_exp_f32 v46, v46\n"
        "v_mfma_f32_16x16x32_bf16 v[48:51], a[120:123], v[8:11], v[48:51]\n"
        "v_exp_f32 v47, v47\n"
        "v_mfma_f32_16x16x32_bf16 v[48:51], a[124:127], v[12:15], v[48:51]\n"
        "s_add_u32 s26, s26, 0x20000\n"
        "s_addc_u32 s27, s27, 0\n"
        "v_cvt_pk_bf16_f32 v16, v32, v33\n"
        "v_mfma_f32_16x16x32_bf16 v[52:55], a[124:127], v[76:79], v[228:231]\n"
        "v_cvt_pk_bf16_f32 v17, v34, v35\n"
        "v_cvt_pk_bf16_f32 v20, v36, v37\n"
        "v_mfma_f32_16x16x32_bf16 v[52:55], a[120:123], v[72:75], v[52:55]\n"
        "v_cvt_pk_bf16_f32 v21, v38, v39\n"
        "v_cvt_pk_bf16_f32 v24, v40, v41\n"
        "v_mfma_f32_16x16x32_bf16 v[52:55], a[116:119], v[68:71], v[52:55]\n"
        "v_exp_f32 v48, v48\n"
        "v_mfma_f32_16x16x32_bf16 v[52:55], a[112:115], v[64:67], v[52:55]\n"
        "v_exp_f32 v49, v49\n"
        "v_mfma_f32_16x16x32_bf16 v[56:59], a[112:115], v[96:99], v[232:235]\n"
        "v_exp_f32 v50, v50\n"
        "v_mfma_f32_16x16x32_bf16 v[56:59], a[116:119], v[100:103], v[56:59]\n"
        "v_exp_f32 v51, v51\n"
        "v_mfma_f32_16x16x32_bf16 v[56:59], a[120:123], v[104:107], v[56:59]\n"
        "v_cvt_pk_bf16_f32 v25, v42, v43\n"
        "v_cvt_pk_bf16_f32 v28, v44, v45\n"
        "v_mfma_f32_16x16x32_bf16 v[56:59], a[124:127], v[108:111], v[56:59]\n"
        "v_exp_f32 v52, v52\n"
        "v_mfma_f32_16x16x32_bf16 v[60:63], a[124:127], v[140:143], v[236:239]\n"
        "ds_read_b128 a[124:127], v240 offset:31744\n"
        "v_cvt_pk_bf16_f32 v29, v46, v47\n"
        "v_mfma_f32_16x16x32_bf16 v[60:63], a[120:123], v[136:139], v[60:63]\n"
        "ds_read_b128 a[120:123], v240 offset:30720\n"
        "v_cvt_pk_bf16_f32 v18, v48, v49\n"
        "v_mfma_f32_16x16x32_bf16 v[60:63], a[116:119], v[132:135], v[60:63]\n"
        "ds_read_b128 a[116:119], v240 offset:29696\n"
        "v_cvt_pk_bf16_f32 v19, v50, v51\n"
        "v_mfma_f32_16x16x32_bf16 v[60:63], a[112:115], v[128:131], v[60:63]\n"
        "ds_read_b128 a[112:115], v240 offset:28672\n"
        "v_xor_b32 v240, 0x8000, v240\n"
        "v_smfmac_f32_16x16x64_bf16 a[64:67], a[72:75], v[80:87], v154\n"
        "v_smfmac_f32_16x16x64_bf16 a[68:71], a[72:75], v[88:95], v154\n"
        "v_mfma_f32_16x16x32_bf16 v[160:163], a[144:147], v[80:83], v[160:163]\n"
        "v_exp_f32 v53, v53\n"
        "v_mfma_f32_16x16x32_bf16 v[192:195], a[144:147], v[84:87], v[192:195]\n"
        "v_exp_f32 v54, v54\n"
        "v_mfma_f32_16x16x32_bf16 a[0:3], a[144:147], v[88:91], a[0:3]\n"
        "v_exp_f32 v55, v55\n"
        "v_mfma_f32_16x16x32_bf16 a[32:35], a[144:147], v[92:95], a[32:35]\n"
        "ds_read_b128 a[144:147], v241 offset:16384\n"
        "v_cvt_pk_bf16_f32 v22, v52, v53\n"
        "v_mfma_f32_16x16x32_bf16 a[36:39], a[148:151], v[92:95], a[36:39]\n"
        "v_exp_f32 v56, v56\n"
        "v_mfma_f32_16x16x32_bf16 a[4:7], a[148:151], v[88:91], a[4:7]\n"
        "s_waitcnt vmcnt(10) lgkmcnt(1)\n"
        "v_exp_f32 v57, v57\n"
        "v_mfma_f32_16x16x32_bf16 v[196:199], a[148:151], v[84:87], v[196:199]\n"
        "s_barrier\n"
        "v_mfma_f32_16x16x32_bf16 v[164:167], a[148:151], v[80:83], v[164:167]\n"
        "ds_read_b128 a[148:151], v241 offset:17408\n"
        "v_cvt_pk_bf16_f32 v23, v54, v55\n"
        "v_mfma_f32_16x16x32_bf16 v[168:171], a[152:155], v[80:83], v[168:171]\n"
        "v_exp_f32 v58, v58\n"
        "v_mfma_f32_16x16x32_bf16 v[200:203], a[152:155], v[84:87], v[200:203]\n"
        "v_exp_f32 v59, v59\n"
        "v_mfma_f32_16x16x32_bf16 a[8:11], a[152:155], v[88:91], a[8:11]\n"
        "v_exp_f32 v60, v60\n"
        "v_mfma_f32_16x16x32_bf16 a[40:43], a[152:155], v[92:95], a[40:43]\n"
        "ds_read_b128 a[152:155], v241 offset:18432\n"
        "v_cvt_pk_bf16_f32 v26, v56, v57\n"
        "v_mfma_f32_16x16x32_bf16 a[44:47], a[156:159], v[92:95], a[44:47]\n"
        "v_exp_f32 v61, v61\n"
        "v_mfma_f32_16x16x32_bf16 a[12:15], a[156:159], v[88:91], a[12:15]\n"
        "v_exp_f32 v62, v62\n"
        "v_mfma_f32_16x16x32_bf16 v[204:207], a[156:159], v[84:87], v[204:207]\n"
        "v_exp_f32 v63, v63\n"
        "v_mfma_f32_16x16x32_bf16 v[172:175], a[156:159], v[80:83], v[172:175]\n"
        "ds_read_b128 a[156:159], v241 offset:19456\n"
        "v_cvt_pk_bf16_f32 v27, v58, v59\n"
        "v_mfma_f32_16x16x32_bf16 v[32:35], a[96:99], v[0:3], v[224:227]\n"
        "v_cvt_pk_bf16_f32 v30, v60, v61\n"
        "v_cvt_pk_bf16_f32 v31, v62, v63\n"
        "v_mfma_f32_16x16x32_bf16 v[32:35], a[100:103], v[4:7], v[32:35]\n"
        "v_mfma_f32_16x16x32_bf16 v[32:35], a[104:107], v[8:11], v[32:35]\n"
        "v_mfma_f32_16x16x32_bf16 v[32:35], a[108:111], v[12:15], v[32:35]\n"
        "v_mfma_f32_16x16x32_bf16 v[36:39], a[108:111], v[76:79], v[228:231]\n"
        "v_mfma_f32_16x16x32_bf16 v[36:39], a[104:107], v[72:75], v[36:39]\n"
        "v_mfma_f32_16x16x32_bf16 v[36:39], a[100:103], v[68:71], v[36:39]\n"
        "v_mfma_f32_16x16x32_bf16 v[36:39], a[96:99], v[64:67], v[36:39]\n"
        "v_mfma_f32_16x16x32_bf16 v[40:43], a[96:99], v[96:99], v[232:235]\n"
        "v_mfma_f32_16x16x32_bf16 v[40:43], a[100:103], v[100:103], v[40:43]\n"
        "v_mfma_f32_16x16x32_bf16 v[40:43], a[104:107], v[104:107], v[40:43]\n"
        "v_mfma_f32_16x16x32_bf16 v[40:43], a[108:111], v[108:111], v[40:43]\n"
        "v_exp_f32 v32, v32\n"
        "v_mfma_f32_16x16x32_bf16 v[44:47], a[108:111], v[140:143], v[236:239]\n"
        "v_exp_f32 v33, v33\n"
        "v_mfma_f32_16x16x32_bf16 v[44:47], a[104:107], v[136:139], v[44:47]\n"
        "ds_read_b128 a[104:107], v240 offset:2048\n"
        "v_mfma_f32_16x16x32_bf16 v[44:47], a[100:103], v[132:135], v[44:47]\n"
        "ds_read_b128 a[100:103], v240 offset:1024\n"
        "v_mfma_f32_16x16x32_bf16 v[44:47], a[96:99], v[128:131], v[44:47]\n"
        "ds_read_b128 a[96:99], v240 offset:0\n"
        "v_mfma_f32_16x16x32_bf16 v[176:179], a[128:131], v[80:83], v[176:179]\n"
        "v_exp_f32 v34, v34\n"
        "v_mfma_f32_16x16x32_bf16 v[208:211], a[128:131], v[84:87], v[208:211]\n"
        "v_exp_f32 v35, v35\n"
        "v_mfma_f32_16x16x32_bf16 a[16:19], a[128:131], v[88:91], a[16:19]\n"
        "ds_read_b128 a[108:111], v240 offset:3072\n"
        "v_mfma_f32_16x16x32_bf16 a[48:51], a[128:131], v[92:95], a[48:51]\n"
        "ds_read_b128 a[128:131], v241 offset:20480\n"
        "v_mfma_f32_16x16x32_bf16 a[52:55], a[132:135], v[92:95], a[52:55]\n"
        "v_exp_f32 v36, v36\n"
        "v_mfma_f32_16x16x32_bf16 a[20:23], a[132:135], v[88:91], a[20:23]\n"
        "v_exp_f32 v37, v37\n"
        "v_mfma_f32_16x16x32_bf16 v[212:215], a[132:135], v[84:87], v[212:215]\n"
        "v_exp_f32 v38, v38\n"
        "v_mfma_f32_16x16x32_bf16 v[180:183], a[132:135], v[80:83], v[180:183]\n"
        "ds_read_b128 a[132:135], v241 offset:21504\n"
        "v_mfma_f32_16x16x32_bf16 v[184:187], a[136:139], v[80:83], v[184:187]\n"
        "v_exp_f32 v39, v39\n"
        "v_mfma_f32_16x16x32_bf16 v[216:219], a[136:139], v[84:87], v[216:219]\n"
        "v_exp_f32 v40, v40\n"
        "v_mfma_f32_16x16x32_bf16 a[24:27], a[136:139], v[88:91], a[24:27]\n"
        "v_exp_f32 v41, v41\n"
        "v_mfma_f32_16x16x32_bf16 a[56:59], a[136:139], v[92:95], a[56:59]\n"
        "ds_read_b128 a[136:139], v241 offset:22528\n"
        "s_waitcnt lgkmcnt(2)\n"
        "v_mfma_f32_16x16x32_bf16 a[60:63], a[140:143], v[92:95], a[60:63]\n"
        "v_exp_f32 v42, v42\n"
        "v_mfma_f32_16x16x32_bf16 a[28:31], a[140:143], v[88:91], a[28:31]\n"
        "v_exp_f32 v43, v43\n"
        "v_mfma_f32_16x16x32_bf16 v[220:223], a[140:143], v[84:87], v[220:223]\n"
        "v_exp_f32 v44, v44\n"
        "v_mfma_f32_16x16x32_bf16 v[188:191], a[140:143], v[80:83], v[188:191]\n"
        "ds_read_b128 a[140:143], v241 offset:23552\n"
        "v_cvt_pk_bf16_f32 v80, v32, v33\n"
        "v_mfma_f32_16x16x32_bf16 v[48:51], a[112:115], v[0:3], v[224:227]\n"
        "v_exp_f32 v45, v45\n"
        "v_mfma_f32_16x16x32_bf16 v[48:51], a[116:119], v[4:7], v[48:51]\n"
        "v_exp_f32 v46, v46\n"
        "v_mfma_f32_16x16x32_bf16 v[48:51], a[120:123], v[8:11], v[48:51]\n"
        "v_exp_f32 v47, v47\n"
        "v_mfma_f32_16x16x32_bf16 v[48:51], a[124:127], v[12:15], v[48:51]\n"
        "v_cvt_pk_bf16_f32 v81, v34, v35\n"
        "v_cvt_pk_bf16_f32 v84, v36, v37\n"
        "v_mfma_f32_16x16x32_bf16 v[52:55], a[124:127], v[76:79], v[228:231]\n"
        "v_cvt_pk_bf16_f32 v85, v38, v39\n"
        "v_cvt_pk_bf16_f32 v88, v40, v41\n"
        "v_mfma_f32_16x16x32_bf16 v[52:55], a[120:123], v[72:75], v[52:55]\n"
        "v_cvt_pk_bf16_f32 v89, v42, v43\n"
        "v_cvt_pk_bf16_f32 v92, v44, v45\n"
        "v_mfma_f32_16x16x32_bf16 v[52:55], a[116:119], v[68:71], v[52:55]\n"
        "v_cvt_pk_bf16_f32 v93, v46, v47\n"
        "v_mfma_f32_16x16x32_bf16 v[52:55], a[112:115], v[64:67], v[52:55]\n"
        "v_mfma_f32_16x16x32_bf16 v[56:59], a[112:115], v[96:99], v[232:235]\n"
        "v_mfma_f32_16x16x32_bf16 v[56:59], a[116:119], v[100:103], v[56:59]\n"
        "v_mfma_f32_16x16x32_bf16 v[56:59], a[120:123], v[104:107], v[56:59]\n"
        "v_mfma_f32_16x16x32_bf16 v[56:59], a[124:127], v[108:111], v[56:59]\n"
        "v_mfma_f32_16x16x32_bf16 v[60:63], a[124:127], v[140:143], v[236:239]\n"
        "v_mfma_f32_16x16x32_bf16 v[60:63], a[120:123], v[136:139], v[60:63]\n"
        "ds_read_b128 a[120:123], v240 offset:6144\n"
        "v_mfma_f32_16x16x32_bf16 v[60:63], a[116:119], v[132:135], v[60:63]\n"
        "ds_read_b128 a[116:119], v240 offset:5120\n"
        "v_mfma_f32_16x16x32_bf16 v[60:63], a[112:115], v[128:131], v[60:63]\n"
        "ds_read_b128 a[112:115], v240 offset:4096\n"
        "v_smfmac_f32_16x16x64_bf16 a[64:67], a[72:75], v[16:23], v154\n"
        "v_smfmac_f32_16x16x64_bf16 a[68:71], a[72:75], v[24:31], v154\n"
        "v_mfma_f32_16x16x32_bf16 v[160:163], a[144:147], v[16:19], v[160:163]\n"
        "v_mfma_f32_16x16x32_bf16 v[192:195], a[144:147], v[20:23], v[192:195]\n"
        "v_mfma_f32_16x16x32_bf16 a[0:3], a[144:147], v[24:27], a[0:3]\n"
        "v_mfma_f32_16x16x32_bf16 a[32:35], a[144:147], v[28:31], a[32:35]\n"
        "ds_read_b128 a[144:147], v241 offset:24576\n"
        "v_mfma_f32_16x16x32_bf16 a[36:39], a[148:151], v[28:31], a[36:39]\n"
        "v_mfma_f32_16x16x32_bf16 a[4:7], a[148:151], v[24:27], a[4:7]\n"
        "v_mfma_f32_16x16x32_bf16 v[196:199], a[148:151], v[20:23], v[196:199]\n"
        "v_mfma_f32_16x16x32_bf16 v[164:167], a[148:151], v[16:19], v[164:167]\n"
        "ds_read_b128 a[148:151], v241 offset:25600\n"
        "v_mfma_f32_16x16x32_bf16 v[168:171], a[152:155], v[16:19], v[168:171]\n"
        "v_mfma_f32_16x16x32_bf16 v[200:203], a[152:155], v[20:23], v[200:203]\n"
        "v_mfma_f32_16x16x32_bf16 a[8:11], a[152:155], v[24:27], a[8:11]\n"
        "v_mfma_f32_16x16x32_bf16 a[40:43], a[152:155], v[28:31], a[40:43]\n"
        "ds_read_b128 a[152:155], v241 offset:26624\n"
        "v_mfma_f32_16x16x32_bf16 a[44:47], a[156:159], v[28:31], a[44:47]\n"
        "v_mfma_f32_16x16x32_bf16 a[12:15], a[156:159], v[24:27], a[12:15]\n"
        "v_mfma_f32_16x16x32_bf16 v[204:207], a[156:159], v[20:23], v[204:207]\n"
        "s_cmp_lg_u32 s82, s24\n"
        "v_mfma_f32_16x16x32_bf16 v[172:175], a[156:159], v[16:19], v[172:175]\n"
        "s_cbranch_scc1 LDU_%=\n"
        "s_sub_u32 s97, s83, s82\n"
        "s_cmp_gt_u32 s97, 1\n"
        "s_cbranch_scc1 LLD_%=\n"
        "s_cmp_eq_u32 s97, 0\n"
        "s_cbranch_scc1 LE3D_%=\n"
        "global_load_dwordx2 v[252:253], v155, s[48:49] sc1\n"
        "v_accvgpr_read_b32 v246, a93\n"
        "global_load_dword v244, v246, s[46:47]\n"
        "s_branch LEWD_%=\n"
        "LE3D_%=:\n"
        "v_cmp_ne_u64 vcc, v[252:253], v[250:251]\n"
        "s_mov_b64 s[68:69], s[92:93]\n"
        "s_mov_b64 s[74:75], s[94:95]\n"
        "LHC0_%=:\n"
        "s_cbranch_execz LHRC_%=\n"
        "s_mov_b32 s80, s50\n"
        "s_mov_b32 s81, s51\n"
        "s_mov_b64 s[66:67], s[22:23]\n"
        "s_mov_b32 s90, s29\n"
        "s_mov_b32 s25, s30\n"
        "s_lshl_b32 s28, s28, 2\n"
        "s_xor_b32 s79, s73, 0x8000\n"
        "v_accvgpr_read_b32 v144, a84\n"
        "v_accvgpr_read_b32 v145, a85\n"
        "v_accvgpr_read_b32 v146, a86\n"
        "v_accvgpr_read_b32 v147, a87\n"
        "s_mov_b64 exec, 15\n"
        "ds_write_b128 v156, a[88:91]\n"
        "s_mov_b64 exec, -1\n"
        "s_cbranch_vccnz LE4D_%=\n"
        "LEWD_%=:\n"
        "s_add_u32 s24, s82, 1\n"
        "s_cmp_lg_u32 s82, s91\n"
        "s_cbranch_scc1 LDU_%=\n"
        "; ---- LDRAIN\n"
        "LLD_%=:\n"
        "v_mfma_f32_16x16x32_bf16 a[16:19], a[128:131], v[24:27], a[16:19]\n"
        "s_waitcnt lgkmcnt(4)\n"
        "v_exp_f32 v62, v62\n"
        "v_mfma_f32_16x16x32_bf16 a[48:51], a[128:131], v[28:31], a[48:51]\n"
        "s_mul_i32 s53, s44, 1052672\n"
        "v_exp_f32 v63, v63\n"
        "v_mfma_f32_16x16x32_bf16 a[52:55], a[132:135], v[28:31], a[52:55]\n"
        "ds_read_b128 a[172:175], v241 offset:27648\n"
        "s_lshl_b32 s96, s43, 15\n"
        "v_cvt_pk_bf16_f32 v95, v62, v63\n"
        "v_mfma_f32_16x16x32_bf16 a[20:23], a[132:135], v[24:27], a[20:23]\n"
        "s_add_u32 s53, s53, s96\n"
        "v_exp_f32 v59, v59\n"
        "v_mfma_f32_16x16x32_bf16 a[24:27], a[136:139], v[24:27], a[24:27]\n"
        "s_lshl_b32 s96, s40, 10\n"
        "v_exp_f32 v58, v58\n"
        "v_mfma_f32_16x16x32_bf16 a[56:59], a[136:139], v[28:31], a[56:59]\n"
        "ds_read_b128 a[176:179], v241 offset:28672\n"
        "s_add_u32 s53, s53, s96\n"
        "v_cvt_pk_bf16_f32 v91, v58, v59\n"
        "v_mfma_f32_16x16x32_bf16 a[60:63], a[140:143], v[28:31], a[60:63]\n"
        "s_add_u32 s53, s53, 0x2201000\n"
        "v_exp_f32 v57, v57\n"
        "v_mfma_f32_16x16x32_bf16 a[28:31], a[140:143], v[24:27], a[28:31]\n"
        "v_exp_f32 v56, v56\n"
        "v_exp_f32 v61, v61\n"
        "v_cvt_pk_bf16_f32 v90, v56, v57\n"
        "s_nop 1\n"
        "s_waitcnt lgkmcnt(4)\n"
        "v_mfma_f32_16x16x32_bf16 a[0:3], a[144:147], v[88:91], a[0:3]\n"
        "s_waitcnt lgkmcnt(2)\n"
        "v_exp_f32 v60, v60\n"
        "v_mfma_f32_16x16x32_bf16 a[4:7], a[148:151], v[88:91], a[4:7]\n"
        "ds_read_b128 a[180:183], v241 offset:29696\n"
        "s_sub_u32 s24, s83, 1\n"
        "v_cvt_pk_bf16_f32 v94, v60, v61\n"
        "s_nop 1\n"
        "v_smfmac_f32_16x16x64_bf16 a[68:71], a[72:75], v[88:95], v154\n"
        "ds_read_b128 a[184:187], v241 offset:30720\n"
        "s_add_u32 s70, s38, s53\n"
        "s_addc_u32 s71, s39, 0\n"
        "v_mfma_f32_16x16x32_bf16 a[32:35], a[144:147], v[92:95], a[32:35]\n"
        "ds_read_b128 a[188:191], v241 offset:31744\n"
        "s_add_u32 s91, s82, 1\n"
        "v_accvgpr_read_b32 v120, a0\n"
        "v_mfma_f32_16x16x32_bf16 a[36:39], a[148:151], v[92:95], a[36:39]\n"
        "s_waitcnt lgkmcnt(3)\n"
        "v_accvgpr_read_b32 v248, a68\n"
        "v_accvgpr_read_b32 v246, a69\n"
        "v_mfma_f32_16x16x32_bf16 a[8:11], a[152:155], v[88:91], a[8:11]\n"
        "s_max_u32 s24, s24, s91\n"
        "v_add_f32 v248, v248, v246\n"
        "v_accvgpr_read_b32 v249, a70\n"
        "v_mfma_f32_16x16x32_bf16 a[40:43], a[152:155], v[92:95], a[40:43]\n"
        "s_add_u32 s91, s83, 1\n"
        "v_rcp_f32 v246, v248\n"
        "v_mfma_f32_16x16x32_bf16 a[44:47], a[172:175], v[92:95], a[44:47]\n"
        "v_accvgpr_read_b32 v247, a71\n"
        "v_add_f32 v249, v249, v247\n"
        "v_mfma_f32_16x16x32_bf16 a[12:15], a[172:175], v[88:91], a[12:15]\n"
        "v_rcp_f32 v247, v249\n"
        "v_mfma_f32_16x16x32_bf16 a[16:19], a[176:179], v[88:91], a[16:19]\n"
        "s_waitcnt lgkmcnt(2)\n"
        "v_accvgpr_read_b32 v112, a32\n"
        "v_accvgpr_read_b32 v113, a33\n"
        "v_mfma_f32_16x16x32_bf16 a[48:51], a[176:179], v[92:95], a[48:51]\n"
        "s_waitcnt lgkmcnt(1)\n"
        "v_accvgpr_read_b32 v114, a34\n"
        "v_accvgpr_read_b32 v115, a35\n"
        "v_mfma_f32_16x16x32_bf16 a[52:55], a[180:183], v[92:95], a[52:55]\n"
        "s_waitcnt lgkmcnt(0)\n"
        "v_accvgpr_read_b32 v116, a36\n"
        "v_accvgpr_read_b32 v117, a37\n"
        "v_mfma_f32_16x16x32_bf16 a[20:23], a[180:183], v[88:91], a[20:23]\n"
        "v_accvgpr_read_b32 v118, a38\n"
        "v_accvgpr_read_b32 v119, a39\n"
        "v_mfma_f32_16x16x32_bf16 a[24:27], a[184:187], v[88:91], a[24:27]\n"
        "v_accvgpr_read_b32 v121, a1\n"
        "v_accvgpr_read_b32 v122, a2\n"
        "v_mfma_f32_16x16x32_bf16 a[56:59], a[184:187], v[92:95], a[56:59]\n"
        "v_accvgpr_read_b32 v123, a3\n"
        "v_accvgpr_read_b32 v124, a4\n"
        "v_mfma_f32_16x16x32_bf16 a[60:63], a[188:191], v[92:95], a[60:63]\n"
        "v_accvgpr_read_b32 v125, a5\n"
        "v_accvgpr_read_b32 v126, a6\n"
        "v_mfma_f32_16x16x32_bf16 a[28:31], a[188:191], v[88:91], a[28:31]\n"
        "v_pk_mul_f32 v[112:113], v[112:113], v[246:247] op_sel:[0,1] op_sel_hi:[1,1]\n"
        "v_pk_mul_f32 v[114:115], v[114:115], v[246:247] op_sel:[0,1] op_sel_hi:[1,1]\n"
        "v_pk_mul_f32 v[116:117], v[116:117], v[246:247] op_sel:[0,1] op_sel_hi:[1,1]\n"
        "v_pk_mul_f32 v[118:119], v[118:119], v[246:247] op_sel:[0,1] op_sel_hi:[1,1]\n"
        "v_cvt_pk_bf16_f32 v112, v112, v113\n"
        "v_cvt_pk_bf16_f32 v113, v114, v115\n"
        "v_cvt_pk_bf16_f32 v114, v116, v117\n"
        "v_cvt_pk_bf16_f32 v115, v118, v119\n"
        "global_store_dwordx4 v254, v[112:115], s[70:71] offset:-4096 sc1\n"
        "v_accvgpr_read_b32 v127, a7\n"
        "v_pk_mul_f32 v[120:121], v[120:121], v[246:247] op_sel:[0,0] op_sel_hi:[1,0]\n"
        "v_pk_mul_f32 v[122:123], v[122:123], v[246:247] op_sel:[0,0] op_sel_hi:[1,0]\n"
        "v_pk_mul_f32 v[124:125], v[124:125], v[246:247] op_sel:[0,0] op_sel_hi:[1,0]\n"
        "v_pk_mul_f32 v[126:127], v[126:127], v[246:247] op_sel:[0,0] op_sel_hi:[1,0]\n"
        "v_cvt_pk_bf16_f32 v120, v120, v121\n"
        "v_cvt_pk_bf16_f32 v121, v122, v123\n"
        "v_cvt_pk_bf16_f32 v122, v124, v125\n"
        "v_cvt_pk_bf16_f32 v123, v126, v127\n"
        "global_store_dwordx4 v254, v[120:123], s[70:71] offset:0 sc1\n"
        "s_add_u32 s70, s70, 0x2000\n"
        "s_addc_u32 s71, s71, 0\n"
        "v_accvgpr_read_b32 v40, a40\n"
        "v_accvgpr_read_b32 v41, a41\n"
        "v_accvgpr_read_b32 v42, a42\n"
        "v_accvgpr_read_b32 v43, a43\n"
        "v_accvgpr_read_b32 v44, a44\n"
        "v_accvgpr_read_b32 v45, a45\n"
        "v_accvgpr_read_b32 v46, a46\n"
        "v_accvgpr_read_b32 v47, a47\n"
        "v_pk_mul_f32 v[40:41], v[40:41], v[246:247] op_sel:[0,1] op_sel_hi:[1,1]\n"
        "v_pk_mul_f32 v[42:43], v[42:43], v[246:247] op_sel:[0,1] op_sel_hi:[1,1]\n"
        "v_pk_mul_f32 v[44:45], v[44:45], v[246:247] op_sel:[0,1] op_sel_hi:[1,1]\n"
        "v_pk_mul_f32 v[46:47], v[46:47], v[246:247] op_sel:[0,1] op_sel_hi:[1,1]\n"
        "v_cvt_pk_bf16_f32 v40, v40, v41\n"
        "v_cvt_pk_bf16_f32 v41, v42, v43\n"
        "v_cvt_pk_bf16_f32 v42, v44, v45\n"
        "v_cvt_pk_bf16_f32 v43, v46, v47\n"
        "global_store_dwordx4 v254, v[40:43], s[70:71] offset:-4096 sc1\n"
        "v_accvgpr_read_b32 v56, a8\n"
        "v_accvgpr_read_b32 v57, a9\n"
        "v_accvgpr_read_b32 v58, a10\n"
        "v_accvgpr_read_b32 v59, a11\n"
        "v_accvgpr_read_b32 v60, a12\n"
        "v_accvgpr_read_b32 v61, a13\n"
        "v_accvgpr_read_b32 v62, a14\n"
        "v_accvgpr_read_b32 v63, a15\n"
        "v_pk_mul_f32 v[56:57], v[56:57], v[246:247] op_sel:[0,0] op_sel_hi:[1,0]\n"
        "v_pk_mul_f32 v[58:59], v[58:59], v[246:247] op_sel:[0,0] op_sel_hi:[1,0]\n"
        "v_pk_mul_f32 v[60:61], v[60:61], v[246:247] op_sel:[0,0] op_sel_hi:[1,0]\n"
        "v_pk_mul_f32 v[62:63], v[62:63], v[246:247] op_sel:[0,0] op_sel_hi:[1,0]\n"
        "v_cvt_pk_bf16_f32 v56, v56, v57\n"
        "v_cvt_pk_bf16_f32 v57, v58, v59\n"
        "v_cvt_pk_bf16_f32 v58, v60, v61\n"
        "v_cvt_pk_bf16_f32 v59, v62, v63\n"
        "global_store_dwordx4 v254, v[56:59], s[70:71] offset:0 sc1\n"
        "s_add_u32 s70, s70, 0x2000\n"
        "s_addc_u32 s71, s71, 0\n"
        "v_accvgpr_read_b32 v112, a48\n"
        "v_accvgpr_read_b32 v113, a49\n"
        "v_accvgpr_read_b32 v114, a50\n"
        "v_accvgpr_read_b32 v115, a51\n"
        "v_accvgpr_read_b32 v116, a52\n"
        "v_accvgpr_read_b32 v117, a53\n"
        "v_accvgpr_read_b32 v118, a54\n"
        "v_accvgpr_read_b32 v119, a55\n"
        "v_pk_mul_f32 v[112:113], v[112:113], v[246:247] op_sel:[0,1] op_sel_hi:[1,1]\n"
        "v_pk_mul_f32 v[114:115], v[114:115], v[246:247] op_sel:[0,1] op_sel_hi:[1,1]\n"
        "v_pk_mul_f32 v[116:117], v[116:117], v[246:247] op_sel:[0,1] op_sel_hi:[1,1]\n"
        "v_pk_mul_f32 v[118:119], v[118:119], v[246:247] op_sel:[0,1] op_sel_hi:[1,1]\n"
        "v_cvt_pk_bf16_f32 v112, v112, v113\n"
        "v_cvt_pk_bf16_f32 v113, v114, v115\n"
        "v_cvt_pk_bf16_f32 v114, v116, v117\n"
        "v_cvt_pk_bf16_f32 v115, v118, v119\n"
        "global_store_dwordx4 v254, v[112:115], s[70:71] offset:-4096 sc1\n"
        "v_accvgpr_read_b32 v120, a16\n"
        "v_accvgpr_read_b32 v121, a17\n"
        "v_accvgpr_read_b32 v122, a18\n"
        "v_accvgpr_read_b32 v123, a19\n"
        "v_accvgpr_read_b32 v124, a20\n"
        "v_accvgpr_read_b32 v125, a21\n"
        "v_accvgpr_read_b32 v126, a22\n"
        "v_accvgpr_read_b32 v127, a23\n"
        "v_pk_mul_f32 v[120:121], v[120:121], v[246:247] op_sel:[0,0] op_sel_hi:[1,0]\n"
        "v_pk_mul_f32 v[122:123], v[122:123], v[246:247] op_sel:[0,0] op_sel_hi:[1,0]\n"
        "v_pk_mul_f32 v[124:125], v[124:125], v[246:247] op_sel:[0,0] op_sel_hi:[1,0]\n"
        "v_pk_mul_f32 v[126:127], v[126:127], v[246:247] op_sel:[0,0] op_sel_hi:[1,0]\n"
        "v_cvt_pk_bf16_f32 v120, v120, v121\n"
        "v_cvt_pk_bf16_f32 v121, v122, v123\n"
        "v_cvt_pk_bf16_f32 v122, v124, v125\n"
        "v_cvt_pk_bf16_f32 v123, v126, v127\n"
        "global_store_dwordx4 v254, v[120:123], s[70:71] offset:0 sc1\n"
        "s_add_u32 s70, s70, 0x2000\n"
        "s_addc_u32 s71, s71, 0\n"
        "v_accvgpr_read_b32 v40, a56\n"
        "v_accvgpr_read_b32 v41, a57\n"
        "v_accvgpr_read_b32 v42, a58\n"
        "v_accvgpr_read_b32 v43, a59\n"
        "v_accvgpr_read_b32 v44, a60\n"
        "v_accvgpr_read_b32 v45, a61\n"
        "v_accvgpr_read_b32 v46, a62\n"
        "v_accvgpr_read_b32 v47, a63\n"
        "v_pk_mul_f32 v[40:41], v[40:41], v[246:247] op_sel:[0,1] op_sel_hi:[1,1]\n"
        "v_pk_mul_f32 v[42:43], v[42:43], v[246:247] op_sel:[0,1] op_sel_hi:[1,1]\n"
        "v_pk_mul_f32 v[44:45], v[44:45], v[246:247] op_sel:[0,1] op_sel_hi:[1,1]\n"
        "v_pk_mul_f32 v[46:47], v[46:47], v[246:247] op_sel:[0,1] op_sel_hi:[1,1]\n"
        "v_cvt_pk_bf16_f32 v40, v40, v41\n"
        "v_cvt_pk_bf16_f32 v41, v42, v43\n"
        "v_cvt_pk_bf16_f32 v42, v44, v45\n"
        "v_cvt_pk_bf16_f32 v43, v46, v47\n"
        "global_store_dwordx4 v254, v[40:43], s[70:71] offset:-4096 sc1\n"
        "v_accvgpr_read_b32 v56, a24\n"
        "v_accvgpr_read_b32 v57, a25\n"
        "v_accvgpr_read_b32 v58, a26\n"
        "v_accvgpr_read_b32 v59, a27\n"
        "v_accvgpr_read_b32 v60, a28\n"
        "v_accvgpr_read_b32 v61, a29\n"
        "v_accvgpr_read_b32 v62, a30\n"
        "v_accvgpr_read_b32 v63, a31\n"
        "v_pk_mul_f32 v[56:57], v[56:57], v[246:247] op_sel:[0,0] op_sel_hi:[1,0]\n"
        "v_pk_mul_f32 v[58:59], v[58:59], v[246:247] op_sel:[0,0] op_sel_hi:[1,0]\n"
        "v_pk_mul_f32 v[60:61], v[60:61], v[246:247] op_sel:[0,0] op_sel_hi:[1,0]\n"
        "v_pk_mul_f32 v[62:63], v[62:63], v[246:247] op_sel:[0,0] op_sel_hi:[1,0]\n"
        "v_cvt_pk_bf16_f32 v56, v56, v57\n"
        "v_cvt_pk_bf16_f32 v57, v58, v59\n"
        "v_cvt_pk_bf16_f32 v58, v60, v61\n"
        "v_cvt_pk_bf16_f32 v59, v62, v63\n"
        "global_store_dwordx4 v254, v[56:59], s[70:71] offset:0 sc1\n"
        "v_add_f32 v247, v248, v249\n"
        "v_cmp_nge_f32 vcc, 0x4e6e6b28, v247\n"
        "s_nop 0\n"
        "s_cbranch_vccnz LRL_%=\n"
        "LSL_%=:\n"
        "s_cmp_eq_u32 s44, 7\n"
        "s_cbranch_scc1 LTL_%=\n"
        "; ---- SINGLE\n"
        "s_branch LSI_%=\n"
        "s_nop 0\n"
        "s_nop 0\n"
        "s_nop 0\n"
        "s_nop 0\n"
        "s_nop 0\n"
        "s_nop 0\n"
        "s_nop 0\n"
        "LSI_%=:\n"
        "v_mfma_f32_16x16x32_bf16 v[32:35], a[96:99], v[0:3], v[224:227]\n"
        "s_waitcnt lgkmcnt(4)\n"
        "ds_read_b128 a[156:159], v241 offset:27648\n"
        "s_add_u32 m0, s80, 0x0\n"
        "v_mfma_f32_16x16x32_bf16 v[32:35], a[100:103], v[4:7], v[32:35]\n"
        "ds_read_b128 a[124:127], v240 offset:7168\n"
        "global_load_lds_dwordx4 v254, s[68:69] offset:0 sc1\n"
        "s_xor_b32 s79, s79, 0x8000\n"
        "v_mfma_f32_16x16x32_bf16 v[32:35], a[104:107], v[8:11], v[32:35]\n"
        "s_add_u32 s82, s82, 1\n"
        "v_exp_f32 v48, v48\n"
        "v_mfma_f32_16x16x32_bf16 v[32:35], a[108:111], v[12:15], v[32:35]\n"
        "s_xor_b32 s78, s78, 0x8000\n"
        "v_exp_f32 v49, v49\n"
        "v_mfma_f32_16x16x32_bf16 v[36:39], a[108:111], v[76:79], v[228:231]\n"
        "ds_read_b128 a[108:111], v240 offset:11264\n"
        "s_mov_b32 s80, s78\n"
        "v_cvt_pk_bf16_f32 v82, v48, v49\n"
        "v_mfma_f32_16x16x32_bf16 v[36:39], a[104:107], v[72:75], v[36:39]\n"
        "ds_read_b128 a[104:107], v240 offset:10240\n"
        "global_load_lds_dwordx4 v254, s[68:69] offset:1024 sc1\n"
        "v_mfma_f32_16x16x32_bf16 v[36:39], a[100:103], v[68:71], v[36:39]\n"
        "v_exp_f32 v50, v50\n"
        "v_mfma_f32_16x16x32_bf16 v[36:39], a[96:99], v[64:67], v[36:39]\n"
        "v_exp_f32 v51, v51\n"
        "v_mfma_f32_16x16x32_bf16 v[176:179], a[128:131], v[16:19], v[176:179]\n"
        "ds_read_b128 a[96:99], v240 offset:8192\n"
        "v_cvt_pk_bf16_f32 v83, v50, v51\n"
        "v_mfma_f32_16x16x32_bf16 v[208:211], a[128:131], v[20:23], v[208:211]\n"
        "ds_read_b128 a[128:131], v241 offset:28672\n"
        "global_load_lds_dwordx4 v254, s[68:69] offset:2048 sc1\n"
        "v_mfma_f32_16x16x32_bf16 v[212:215], a[132:135], v[20:23], v[212:215]\n"
        "v_exp_f32 v52, v52\n"
        "v_mfma_f32_16x16x32_bf16 v[180:183], a[132:135], v[16:19], v[180:183]\n"
        "v_exp_f32 v53, v53\n"
        "v_mfma_f32_16x16x32_bf16 v[184:187], a[136:139], v[16:19], v[184:187]\n"
        "ds_read_b128 a[100:103], v240 offset:9216\n"
        "v_cvt_pk_bf16_f32 v86, v52, v53\n"
        "v_mfma_f32_16x16x32_bf16 v[216:219], a[136:139], v[20:23], v[216:219]\n"
        "ds_read_b128 a[132:135], v241 offset:29696\n"
        "global_load_lds_dwordx4 v254, s[68:69] offset:3072 sc1\n"
        "s_add_u32 m0, s81, 0x0\n"
        "v_mfma_f32_16x16x32_bf16 v[220:223], a[140:143], v[20:23], v[220:223]\n"
        "s_waitcnt lgkmcnt(4)\n"
        "v_exp_f32 v54, v54\n"
        "v_mfma_f32_16x16x32_bf16 v[188:191], a[140:143], v[16:19], v[188:191]\n"
        "s_add_u32 s81, s78, 4096\n"
        "v_exp_f32 v55, v55\n"
        "v_mfma_f32_16x16x32_bf16 v[48:51], a[112:115], v[0:3], v[224:227]\n"
        "ds_read_b128 a[136:139], v241 offset:30720\n"
        "v_cvt_pk_bf16_f32 v87, v54, v55\n"
        "v_mfma_f32_16x16x32_bf16 v[48:51], a[116:119], v[4:7], v[48:51]\n"
        "ds_read_b128 a[140:143], v241 offset:31744\n"
        "global_load_lds_dwordx4 v254, s[74:75] offset:0 sc1\n"
        "v_mfma_f32_16x16x32_bf16 v[48:51], a[120:123], v[8:11], v[48:51]\n"
        "v_exp_f32 v32, v32\n"
        "v_mfma_f32_16x16x32_bf16 v[48:51], a[124:127], v[12:15], v[48:51]\n"
        "v_exp_f32 v33, v33\n"
        "v_mfma_f32_16x16x32_bf16 v[52:55], a[124:127], v[76:79], v[228:231]\n"
        "ds_read_b128 a[124:127], v240 offset:15360\n"
        "v_cvt_pk_bf16_f32 v16, v32, v33\n"
        "v_mfma_f32_16x16x32_bf16 v[52:55], a[120:123], v[72:75], v[52:55]\n"
        "ds_read_b128 a[120:123], v240 offset:14336\n"
        "global_load_lds_dwordx4 v254, s[74:75] offset:1024 sc1\n"
        "v_mfma_f32_16x16x32_bf16 v[52:55], a[116:119], v[68:71], v[52:55]\n"
        "v_exp_f32 v34, v34\n"
        "v_mfma_f32_16x16x32_bf16 v[52:55], a[112:115], v[64:67], v[52:55]\n"
        "v_exp_f32 v35, v35\n"
        "v_smfmac_f32_16x16x64_bf16 a[64:67], a[72:75], v[80:87], v154\n"
        "ds_read_b128 a[112:115], v240 offset:12288\n"
        "v_cvt_pk_bf16_f32 v17, v34, v35\n"
        "v_mfma_f32_16x16x32_bf16 v[160:163], a[144:147], v[80:83], v[160:163]\n"
        "ds_read_b128 a[116:119], v240 offset:13312\n"
        "global_load_lds_dwordx4 v254, s[74:75] offset:2048 sc1\n"
        "v_mfma_f32_16x16x32_bf16 v[192:195], a[144:147], v[84:87], v[192:195]\n"
        "s_waitcnt vmcnt(8) lgkmcnt(4)\n"
        "v_exp_f32 v38, v38\n"
        "v_mfma_f32_16x16x32_bf16 v[196:199], a[148:151], v[84:87], v[196:199]\n"
        "s_barrier\n"
        "v_mfma_f32_16x16x32_bf16 v[164:167], a[148:151], v[80:83], v[164:167]\n"
        "v_exp_f32 v39, v39\n"
        "v_mfma_f32_16x16x32_bf16 v[168:171], a[152:155], v[80:83], v[168:171]\n"
        "v_exp_f32 v36, v36\n"
        "v_mfma_f32_16x16x32_bf16 v[200:203], a[152:155], v[84:87], v[200:203]\n"
        "v_exp_f32 v37, v37\n"
        "v_mfma_f32_16x16x32_bf16 v[204:207], a[156:159], v[84:87], v[204:207]\n"
        "v_exp_f32 v48, v48\n"
        "v_mfma_f32_16x16x32_bf16 v[172:175], a[156:159], v[80:83], v[172:175]\n"
        "v_exp_f32 v49, v49\n"
        "v_mfma_f32_16x16x32_bf16 v[32:35], a[96:99], v[0:3], v[224:227]\n"
        "v_exp_f32 v50, v50\n"
        "v_mfma_f32_16x16x32_bf16 v[32:35], a[100:103], v[4:7], v[32:35]\n"
        "v_exp_f32 v51, v51\n"
        "v_mfma_f32_16x16x32_bf16 v[32:35], a[104:107], v[8:11], v[32:35]\n"
        "v_exp_f32 v52, v52\n"
        "v_mfma_f32_16x16x32_bf16 v[32:35], a[108:111], v[12:15], v[32:35]\n"
        "v_exp_f32 v53, v53\n"
        "v_mfma_f32_16x16x32_bf16 v[176:179], a[128:131], v[80:83], v[176:179]\n"
        "v_exp_f32 v54, v54\n"
        "v_mfma_f32_16x16x32_bf16 v[208:211], a[128:131], v[84:87], v[208:211]\n"
        "v_exp_f32 v55, v55\n"
        "v_mfma_f32_16x16x32_bf16 v[212:215], a[132:135], v[84:87], v[212:215]\n"
        "global_load_lds_dwordx4 v254, s[74:75] offset:3072 sc1\n"
        "s_add_u32 m0, s79, 0x0\n"
        "v_cvt_pk_bf16_f32 v21, v38, v39\n"
        "v_mfma_f32_16x16x32_bf16 v[180:183], a[132:135], v[80:83], v[180:183]\n"
        "v_exp_f32 v32, v32\n"
        "v_mfma_f32_16x16x32_bf16 v[184:187], a[136:139], v[80:83], v[184:187]\n"
        "v_exp_f32 v33, v33\n"
        "v_mfma_f32_16x16x32_bf16 v[216:219], a[136:139], v[84:87], v[216:219]\n"
        "v_exp_f32 v34, v34\n"
        "v_mfma_f32_16x16x32_bf16 v[220:223], a[140:143], v[84:87], v[220:223]\n"
        "v_exp_f32 v35, v35\n"
        "v_mfma_f32_16x16x32_bf16 v[188:191], a[140:143], v[80:83], v[188:191]\n"
        "v_cvt_pk_bf16_f32 v20, v36, v37\n"
        "v_xor_b32 v241, 0x8000, v241\n"
        "v_mfma_f32_16x16x32_bf16 v[36:39], a[108:111], v[76:79], v[228:231]\n"
        "ds_read_b128 a[144:147], v241 offset:0\n"
        "s_add_u32 s76, s66, s90\n"
        "s_addc_u32 s77, s67, 0\n"
        "v_mfma_f32_16x16x32_bf16 v[36:39], a[104:107], v[72:75], v[36:39]\n"
        "ds_read_b128 a[148:151], v241 offset:1024\n"
        "global_load_lds_dwordx4 v144, s[76:77] offset:0 sc1\n"
        "v_mfma_f32_16x16x32_bf16 v[36:39], a[100:103], v[68:71], v[36:39]\n"
        "ds_read_b128 a[152:155], v241 offset:2048\n"
        "v_cvt_pk_bf16_f32 v18, v48, v49\n"
        "v_mfma_f32_16x16x32_bf16 v[36:39], a[96:99], v[64:67], v[36:39]\n"
        "ds_read_b128 a[156:159], v241 offset:3072\n"
        "v_cvt_pk_bf16_f32 v19, v50, v51\n"
        "s_waitcnt lgkmcnt(4)\n"
        "v_mfma_f32_16x16x32_bf16 v[48:51], a[112:115], v[0:3], v[224:227]\n"
        "ds_read_b128 a[96:99], v240 offset:16384\n"
        "v_cvt_pk_bf16_f32 v22, v52, v53\n"
        "v_mfma_f32_16x16x32_bf16 v[48:51], a[116:119], v[4:7], v[48:51]\n"
        "ds_read_b128 a[128:131], v241 offset:4096\n"
        "global_load_lds_dwordx4 v145, s[76:77] offset:1024 sc1\n"
        "v_mfma_f32_16x16x32_bf16 v[48:51], a[120:123], v[8:11], v[48:51]\n"
        "ds_read_b128 a[100:103], v240 offset:17408\n"
        "v_cvt_pk_bf16_f32 v23, v54, v55\n"
        "v_mfma_f32_16x16x32_bf16 v[48:51], a[124:127], v[12:15], v[48:51]\n"
        "ds_read_b128 a[132:135], v241 offset:5120\n"
        "v_cvt_pk_bf16_f32 v80, v32, v33\n"
        "v_mfma_f32_16x16x32_bf16 v[52:55], a[124:127], v[76:79], v[228:231]\n"
        "ds_read_b128 a[136:139], v241 offset:6144\n"
        "v_cvt_pk_bf16_f32 v81, v34, v35\n"
        "v_mfma_f32_16x16x32_bf16 v[52:55], a[120:123], v[72:75], v[52:55]\n"
        "s_waitcnt lgkmcnt(7)\n"
        "ds_read_b128 a[104:107], v240 offset:18432\n"
        "v_mfma_f32_16x16x32_bf16 v[52:55], a[116:119], v[68:71], v[52:55]\n"
        "ds_read_b128 a[140:143], v241 offset:7168\n"
        "global_load_lds_dwordx4 v146, s[76:77] offset:2048 sc1\n"
        "v_mfma_f32_16x16x32_bf16 v[52:55], a[112:115], v[64:67], v[52:55]\n"
        "ds_read_b128 a[108:111], v240 offset:19456\n"
        "s_cmp_ge_u32 s82, s44\n"
        "s_addc_u32 s90, s82, 0\n"
        "v_smfmac_f32_16x16x64_bf16 a[64:67], a[72:75], v[16:23], v154\n"
        "ds_read_b128 a[112:115], v240 offset:20480\n"
        "s_lshl_b32 s90, s90, 15\n"
        "v_mfma_f32_16x16x32_bf16 v[160:163], a[144:147], v[16:19], v[160:163]\n"
        "s_waitcnt lgkmcnt(7)\n"
        "v_exp_f32 v36, v36\n"
        "v_mfma_f32_16x16x32_bf16 v[192:195], a[144:147], v[20:23], v[192:195]\n"
        "ds_read_b128 a[144:147], v241 offset:8192\n"
        "global_load_lds_dwordx4 v147, s[76:77] offset:3072 sc1\n"
        "s_add_u32 m0, s79, s28\n"
        "v_mfma_f32_16x16x32_bf16 v[196:199], a[148:151], v[20:23], v[196:199]\n"
        "ds_read_b128 a[116:119], v240 offset:21504\n"
        "s_add_u32 s76, s76, s25\n"
        "s_addc_u32 s77, s77, 0\n"
        "v_mfma_f32_16x16x32_bf16 v[164:167], a[148:151], v[16:19], v[164:167]\n"
        "v_exp_f32 v37, v37\n"
        "v_mfma_f32_16x16x32_bf16 v[168:171], a[152:155], v[16:19], v[168:171]\n"
        "ds_read_b128 a[148:151], v241 offset:9216\n"
        "v_cvt_pk_bf16_f32 v84, v36, v37\n"
        "v_mfma_f32_16x16x32_bf16 v[200:203], a[152:155], v[20:23], v[200:203]\n"
        "s_waitcnt lgkmcnt(5)\n"
        "ds_read_b128 a[152:155], v241 offset:10240\n"
        "v_mfma_f32_16x16x32_bf16 v[204:207], a[156:159], v[20:23], v[204:207]\n"
        "ds_read_b128 a[120:123], v240 offset:22528\n"
        "global_load_lds_dwordx4 v144, s[76:77] offset:0 sc1\n"
        "v_mfma_f32_16x16x32_bf16 v[172:175], a[156:159], v[16:19], v[172:175]\n"
        "s_waitcnt lgkmcnt(5)\n"
        "v_exp_f32 v38, v38\n"
        "v_mfma_f32_16x16x32_bf16 v[32:35], a[96:99], v[0:3], v[224:227]\n"
        "v_exp_f32 v39, v39\n"
        "v_mfma_f32_16x16x32_bf16 v[32:35], a[100:103], v[4:7], v[32:35]\n"
        "ds_read_b128 a[156:159], v241 offset:11264\n"
        "v_cvt_pk_bf16_f32 v85, v38, v39\n"
        "v_mfma_f32_16x16x32_bf16 v[32:35], a[104:107], v[8:11], v[32:35]\n"
        "ds_read_b128 a[124:127], v240 offset:23552\n"
        "global_load_lds_dwordx4 v145, s[76:77] offset:1024 sc1\n"
        "v_mfma_f32_16x16x32_bf16 v[32:35], a[108:111], v[12:15], v[32:35]\n"
        "v_exp_f32 v48, v48\n"
        "v_mfma_f32_16x16x32_bf16 v[36:39], a[108:111], v[76:79], v[228:231]\n"
        "v_exp_f32 v49, v49\n"
        "v_mfma_f32_16x16x32_bf16 v[36:39], a[104:107], v[72:75], v[36:39]\n"
        "ds_read_b128 a[104:107], v240 offset:26624\n"
        "v_cvt_pk_bf16_f32 v82, v48, v49\n"
        "v_mfma_f32_16x16x32_bf16 v[36:39], a[100:103], v[68:71], v[36:39]\n"
        "ds_read_b128 a[100:103], v240 offset:25600\n"
        "global_load_lds_dwordx4 v146, s[76:77] offset:2048 sc1\n"
        "v_mfma_f32_16x16x32_bf16 v[36:39], a[96:99], v[64:67], v[36:39]\n"
        "v_exp_f32 v50, v50\n"
        "v_mfma_f32_16x16x32_bf16 v[176:179], a[128:131], v[16:19], v[176:179]\n"
        "v_exp_f32 v51, v51\n"
        "v_mfma_f32_16x16x32_bf16 v[208:211], a[128:131], v[20:23], v[208:211]\n"
        "ds_read_b128 a[96:99], v240 offset:24576\n"
        "v_cvt_pk_bf16_f32 v83, v50, v51\n"
        "v_mfma_f32_16x16x32_bf16 v[212:215], a[132:135], v[20:23], v[212:215]\n"
        "ds_read_b128 a[128:131], v241 offset:12288\n"
        "global_load_lds_dwordx4 v147, s[76:77] offset:3072 sc1\n"
        "v_mfma_f32_16x16x32_bf16 v[180:183], a[132:135], v[16:19], v[180:183]\n"
        "v_exp_f32 v52, v52\n"
        "v_mfma_f32_16x16x32_bf16 v[184:187], a[136:139], v[16:19], v[184:187]\n"
        "v_exp_f32 v53, v53\n"
        "v_mfma_f32_16x16x32_bf16 v[216:219], a[136:139], v[20:23], v[216:219]\n"
        "s_waitcnt lgkmcnt(3)\n"
        "ds_read_b128 a[132:135], v241 offset:13312\n"
        "v_mfma_f32_16x16x32_bf16 v[220:223], a[140:143], v[20:23], v[220:223]\n"
        "ds_read_b128 a[136:139], v241 offset:14336\n"
        "v_cvt_pk_bf16_f32 v86, v52, v53\n"
        "v_mfma_f32_16x16x32_bf16 v[188:191], a[140:143], v[16:19], v[188:191]\n"
        "v_exp_f32 v54, v54\n"
        "v_mfma_f32_16x16x32_bf16 v[48:51], a[112:115], v[0:3], v[224:227]\n"
        "v_exp_f32 v55, v55\n"
        "v_mfma_f32_16x16x32_bf16 v[48:51], a[116:119], v[4:7], v[48:51]\n"
        "ds_read_b128 a[140:143], v241 offset:15360\n"
        "v_cvt_pk_bf16_f32 v87, v54, v55\n"
        "v_mfma_f32_16x16x32_bf16 v[48:51], a[120:123], v[8:11], v[48:51]\n"
        "ds_read_b128 a[108:111], v240 offset:27648\n"
        "global_load_dword v244, v245, s[26:27] offset:0\n"
        "v_mfma_f32_16x16x32_bf16 v[48:51], a[124:127], v[12:15], v[48:51]\n"
        "v_exp_f32 v32, v32\n"
        "v_mfma_f32_16x16x32_bf16 v[52:55], a[124:127], v[76:79], v[228:231]\n"
        "ds_read_b128 a[124:127], v240 offset:31744\n"
        "s_add_u32 s26, s26, 0x20000\n"
        "s_addc_u32 s27, s27, 0\n"
        "v_mfma_f32_16x16x32_bf16 v[52:55], a[120:123], v[72:75], v[52:55]\n"
        "v_exp_f32 v33, v33\n"
        "v_mfma_f32_16x16x32_bf16 v[52:55], a[116:119], v[68:71], v[52:55]\n"
        "ds_read_b128 a[116:119], v240 offset:29696\n"
        "v_cvt_pk_bf16_f32 v16, v32, v33\n"
        "v_mfma_f32_16x16x32_bf16 v[52:55], a[112:115], v[64:67], v[52:55]\n"
        "v_exp_f32 v34, v34\n"
        "v_smfmac_f32_16x16x64_bf16 a[64:67], a[72:75], v[80:87], v154\n"
        "ds_read_b128 a[112:115], v240 offset:28672\n"
        "s_add_u32 s68, s64, s90\n"
        "s_addc_u32 s69, s65, 0\n"
        "v_mfma_f32_16x16x32_bf16 v[160:163], a[144:147], v[80:83], v[160:163]\n"
        "v_exp_f32 v35, v35\n"
        "v_mfma_f32_16x16x32_bf16 v[192:195], a[144:147], v[84:87], v[192:195]\n"
        "ds_read_b128 a[144:147], v241 offset:16384\n"
        "v_cvt_pk_bf16_f32 v17, v34, v35\n"
        "v_mfma_f32_16x16x32_bf16 v[196:199], a[148:151], v[84:87], v[196:199]\n"
        "v_exp_f32 v36, v36\n"
        "v_mfma_f32_16x16x32_bf16 v[164:167], a[148:151], v[80:83], v[164:167]\n"
        "v_exp_f32 v37, v37\n"
        "v_mfma_f32_16x16x32_bf16 v[168:171], a[152:155], v[80:83], v[168:171]\n"
        "s_waitcnt lgkmcnt(3)\n"
        "ds_read_b128 a[148:151], v241 offset:17408\n"
        "v_mfma_f32_16x16x32_bf16 v[200:203], a[152:155], v[84:87], v[200:203]\n"
        "ds_read_b128 a[152:155], v241 offset:18432\n"
        "v_cvt_pk_bf16_f32 v20, v36, v37\n"
        "v_mfma_f32_16x16x32_bf16 v[204:207], a[156:159], v[84:87], v[204:207]\n"
        "v_exp_f32 v38, v38\n"
        "v_mfma_f32_16x16x32_bf16 v[172:175], a[156:159], v[80:83], v[172:175]\n"
        "v_exp_f32 v39, v39\n"
        "v_mfma_f32_16x16x32_bf16 v[32:35], a[96:99], v[0:3], v[224:227]\n"
        "ds_read_b128 a[120:123], v240 offset:30720\n"
        "v_cvt_pk_bf16_f32 v21, v38, v39\n"
        "v_mfma_f32_16x16x32_bf16 v[32:35], a[100:103], v[4:7], v[32:35]\n"
        "ds_read_b128 a[156:159], v241 offset:19456\n"
        "v_xor_b32 v240, 0x8000, v240\n"
        "v_mfma_f32_16x16x32_bf16 v[32:35], a[104:107], v[8:11], v[32:35]\n"
        "v_exp_f32 v48, v48\n"
        "v_mfma_f32_16x16x32_bf16 v[32:35], a[108:111], v[12:15], v[32:35]\n"
        "v_exp_f32 v49, v49\n"
        "v_mfma_f32_16x16x32_bf16 v[36:39], a[108:111], v[76:79], v[228:231]\n"
        "v_exp_f32 v50, v50\n"
        "v_mfma_f32_16x16x32_bf16 v[36:39], a[104:107], v[72:75], v[36:39]\n"
        "v_exp_f32 v51, v51\n"
        "v_mfma_f32_16x16x32_bf16 v[36:39], a[100:103], v[68:71], v[36:39]\n"
        "v_exp_f32 v52, v52\n"
        "v_mfma_f32_16x16x32_bf16 v[36:39], a[96:99], v[64:67], v[36:39]\n"
        "v_exp_f32 v53, v53\n"
        "v_mfma_f32_16x16x32_bf16 v[176:179], a[128:131], v[80:83], v[176:179]\n"
        "s_waitcnt vmcnt(9) lgkmcnt(1)\n"
        "v_exp_f32 v54, v54\n"
        "v_mfma_f32_16x16x32_bf16 v[208:211], a[128:131], v[84:87], v[208:211]\n"
        "s_barrier\n"
        "v_mfma_f32_16x16x32_bf16 v[212:215], a[132:135], v[84:87], v[212:215]\n"
        "ds_read_b128 a[96:99], v240 offset:0\n"
        "v_cvt_pk_bf16_f32 v18, v48, v49\n"
        "v_mfma_f32_16x16x32_bf16 v[180:183], a[132:135], v[80:83], v[180:183]\n"
        "ds_read_b128 a[128:131], v241 offset:20480\n"
        "v_cvt_pk_bf16_f32 v19, v50, v51\n"
        "v_mfma_f32_16x16x32_bf16 v[184:187], a[136:139], v[80:83], v[184:187]\n"
        "ds_read_b128 a[100:103], v240 offset:1024\n"
        "s_add_u32 s74, s68, 0x1000\n"
        "s_addc_u32 s75, s69, 0\n"
        "v_mfma_f32_16x16x32_bf16 v[216:219], a[136:139], v[84:87], v[216:219]\n"
        "ds_read_b128 a[132:135], v241 offset:21504\n"
        "v_cvt_pk_bf16_f32 v22, v52, v53\n"
        "v_mfma_f32_16x16x32_bf16 v[220:223], a[140:143], v[84:87], v[220:223]\n"
        "v_exp_f32 v55, v55\n"
        "v_mfma_f32_16x16x32_bf16 v[188:191], a[140:143], v[80:83], v[188:191]\n"
        "ds_read_b128 a[136:139], v241 offset:22528\n"
        "v_cvt_pk_bf16_f32 v23, v54, v55\n"
        "v_mfma_f32_16x16x32_bf16 v[48:51], a[112:115], v[0:3], v[224:227]\n"
        "ds_read_b128 a[104:107], v240 offset:2048\n"
        "v_mfma_f32_16x16x32_bf16 v[48:51], a[116:119], v[4:7], v[48:51]\n"
        "ds_read_b128 a[140:143], v241 offset:23552\n"
        "v_mfma_f32_16x16x32_bf16 v[48:51], a[120:123], v[8:11], v[48:51]\n"
        "v_exp_f32 v32, v32\n"
        "v_mfma_f32_16x16x32_bf16 v[48:51], a[124:127], v[12:15], v[48:51]\n"
        "s_waitcnt lgkmcnt(5)\n"
        "v_exp_f32 v33, v33\n"
        "v_mfma_f32_16x16x32_bf16 v[52:55], a[124:127], v[76:79], v[228:231]\n"
        "ds_read_b128 a[108:111], v240 offset:3072\n"
        "v_cvt_pk_bf16_f32 v80, v32, v33\n"
        "v_mfma_f32_16x16x32_bf16 v[52:55], a[120:123], v[72:75], v[52:55]\n"
        "v_exp_f32 v34, v34\n"
        "v_mfma_f32_16x16x32_bf16 v[52:55], a[116:119], v[68:71], v[52:55]\n"
        "v_exp_f32 v35, v35\n"
        "v_mfma_f32_16x16x32_bf16 v[52:55], a[112:115], v[64:67], v[52:55]\n"
        "ds_read_b128 a[112:115], v240 offset:4096\n"
        "v_cvt_pk_bf16_f32 v81, v34, v35\n"
        "v_smfmac_f32_16x16x64_bf16 a[64:67], a[72:75], v[16:23], v154\n"
        "ds_read_b128 a[116:119], v240 offset:5120\n"
        "v_mfma_f32_16x16x32_bf16 v[160:163], a[144:147], v[16:19], v[160:163]\n"
        "v_exp_f32 v36, v36\n"
        "v_mfma_f32_16x16x32_bf16 v[192:195], a[144:147], v[20:23], v[192:195]\n"
        "v_exp_f32 v37, v37\n"
        "v_mfma_f32_16x16x32_bf16 v[196:199], a[148:151], v[20:23], v[196:199]\n"
        "ds_read_b128 a[144:147], v241 offset:24576\n"
        "v_cvt_pk_bf16_f32 v84, v36, v37\n"
        "v_mfma_f32_16x16x32_bf16 v[164:167], a[148:151], v[16:19], v[164:167]\n"
        "ds_read_b128 a[148:151], v241 offset:25600\n"
        "v_mfma_f32_16x16x32_bf16 v[168:171], a[152:155], v[16:19], v[168:171]\n"
        "v_exp_f32 v38, v38\n"
        "v_mfma_f32_16x16x32_bf16 v[200:203], a[152:155], v[20:23], v[200:203]\n"
        "s_cmp_lg_u32 s82, s24\n"
        "v_exp_f32 v39, v39\n"
        "v_mfma_f32_16x16x32_bf16 v[204:207], a[156:159], v[20:23], v[204:207]\n"
        "ds_read_b128 a[152:155], v241 offset:26624\n"
        "v_cvt_pk_bf16_f32 v85, v38, v39\n"
        "v_mfma_f32_16x16x32_bf16 v[172:175], a[156:159], v[16:19], v[172:175]\n"
        "ds_read_b128 a[120:123], v240 offset:6144\n"
        "s_cbranch_scc1 LSI_%=\n"
        "LHC1_%=:\n"
        "s_cbranch_execz LHRC_%=\n"
        "s_sub_u32 s97, s83, s82\n"
        "s_cmp_gt_u32 s97, 1\n"
        "s_cbranch_scc1 LTL_%=\n"
        "s_cmp_eq_u32 s97, 0\n"
        "s_cbranch_scc1 LE3S_%=\n"
        "global_load_dwordx2 v[252:253], v155, s[48:49] sc1\n"
        "v_accvgpr_read_b32 v246, a93\n"
        "global_load_dword v244, v246, s[46:47]\n"
        "s_branch LEWS_%=\n"
        "LE3S_%=:\n"
        "v_cmp_ne_u64 vcc, v[252:253], v[250:251]\n"
        "s_mov_b64 s[68:69], s[92:93]\n"
        "s_mov_b64 s[74:75], s[94:95]\n"
        "s_mov_b32 s80, s50\n"
        "s_mov_b32 s81, s51\n"
        "s_mov_b64 s[66:67], s[22:23]\n"
        "s_mov_b32 s90, s29\n"
        "s_mov_b32 s25, s30\n"
        "s_lshl_b32 s28, s28, 2\n"
        "s_xor_b32 s79, s73, 0x8000\n"
        "v_accvgpr_read_b32 v144, a84\n"
        "v_accvgpr_read_b32 v145, a85\n"
        "v_accvgpr_read_b32 v146, a86\n"
        "v_accvgpr_read_b32 v147, a87\n"
        "s_mov_b64 exec, 15\n"
        "ds_write_b128 v156, a[88:91]\n"
        "s_mov_b64 exec, -1\n"
        "s_cbranch_vccnz LE4S_%=\n"
        "LEWS_%=:\n"
        "s_add_u32 s24, s82, 1\n"
        "s_cmp_lg_u32 s82, s91\n"
        "s_cbranch_scc1 LSI_%=\n"
        "; ---- BACK\n"
        "LTL_%=:\n"
        "v_mfma_f32_16x16x32_bf16 v[176:179], a[128:131], v[16:19], v[176:179]\n"
        "s_waitcnt lgkmcnt(4)\n"
        "ds_read_b128 a[156:159], v241 offset:27648\n"
        "s_mul_i32 s53, s45, 1052672\n"
        "v_mfma_f32_16x16x32_bf16 v[208:211], a[128:131], v[20:23], v[208:211]\n"
        "s_lshl_b32 s96, s43, 15\n"
        "v_exp_f32 v48, v48\n"
        "v_mfma_f32_16x16x32_bf16 v[212:215], a[132:135], v[20:23], v[212:215]\n"
        "s_add_u32 s53, s53, s96\n"
        "v_exp_f32 v49, v49\n"
        "v_mfma_f32_16x16x32_bf16 v[180:183], a[132:135], v[16:19], v[180:183]\n"
        "ds_read_b128 a[128:131], v241 offset:28672\n"
        "s_lshl_b32 s96, s40, 10\n"
        "v_cvt_pk_bf16_f32 v82, v48, v49\n"
        "v_mfma_f32_16x16x32_bf16 v[184:187], a[136:139], v[16:19], v[184:187]\n"
        "s_add_u32 s53, s53, s96\n"
        "v_exp_f32 v50, v50\n"
        "v_mfma_f32_16x16x32_bf16 v[216:219], a[136:139], v[20:23], v[216:219]\n"
        "s_add_u32 s53, s53, 0x2201000\n"
        "v_exp_f32 v51, v51\n"
        "v_mfma_f32_16x16x32_bf16 v[220:223], a[140:143], v[20:23], v[220:223]\n"
        "s_waitcnt lgkmcnt(3)\n"
        "ds_read_b128 a[132:135], v241 offset:29696\n"
        "s_add_u32 s16, s51, 4096\n"
        "v_mfma_f32_16x16x32_bf16 v[188:191], a[140:143], v[16:19], v[188:191]\n"
        "v_cvt_pk_bf16_f32 v83, v50, v51\n"
        "s_nop 1\n"
        "v_mfma_f32_16x16x32_bf16 v[160:163], a[144:147], v[80:83], v[160:163]\n"
        "s_lshl_b32 s14, s44, 5\n"
        "v_exp_f32 v52, v52\n"
        "v_mfma_f32_16x16x32_bf16 v[164:167], a[148:151], v[80:83], v[164:167]\n"
        "s_add_u32 s14, s14, s43\n"
        "v_exp_f32 v53, v53\n"
        "v_mfma_f32_16x16x32_bf16 v[168:171], a[152:155], v[80:83], v[168:171]\n"
        "s_waitcnt lgkmcnt(2)\n"
        "ds_read_b128 a[136:139], v241 offset:30720\n"
        "s_lshl2_add_u32 s14, s14, s40\n"
        "v_mfma_f32_16x16x32_bf16 v[172:175], a[156:159], v[80:83], v[172:175]\n"
        "s_waitcnt lgkmcnt(2)\n"
        "ds_read_b128 a[140:143], v241 offset:31744\n"
        "s_lshl_b32 s14, s14, 8\n"
        "v_mfma_f32_16x16x32_bf16 v[176:179], a[128:131], v[80:83], v[176:179]\n"
        "s_add_u32 s70, s38, s53\n"
        "s_addc_u32 s71, s39, 0\n"
        "s_add_u32 s14, s14, 131072\n"
        "s_add_u32 m0, s73, 0x8000\n"
        "s_xor_b32 s66, s29, 0x40000\n"
        "v_cvt_pk_bf16_f32 v86, v52, v53\n"
        "v_add_u32 v230, 3584, v152\n"
        "s_waitcnt lgkmcnt(2)\n"
        "v_mfma_f32_16x16x32_bf16 v[180:183], a[132:135], v[80:83], v[180:183]\n"
        "s_add_u32 s60, s22, s66\n"
        "s_addc_u32 s61, s23, 0\n"
        "s_xor_b32 s66, s29, 0x60000\n"
        "s_cmp_le_u32 s10, 0\n"
        "s_addc_u32 s74, 0, 0\n"
        "s_cmp_le_u32 s10, 1\n"
        "s_addc_u32 s75, 1, 0\n"
        "s_cmp_le_u32 s10, 2\n"
        "s_addc_u32 s76, 2, 0\n"
        "s_sub_u32 s62, 15, s12\n"
        "s_cmp_lt_u32 s10, 2\n"
        "s_cselect_b32 s62, s12, s62\n"
        "s_lshl2_add_u32 s63, s11, s40\n"
        "v_exp_f32 v54, v54\n"
        "s_waitcnt lgkmcnt(1)\n"
        "v_exp_f32 v55, v55\n"
        "v_mfma_f32_16x16x32_bf16 v[184:187], a[136:139], v[80:83], v[184:187]\n"
        "s_waitcnt lgkmcnt(0)\n"
        "v_cvt_pk_bf16_f32 v87, v54, v55\n"
        "s_nop 1\n"
        "v_smfmac_f32_16x16x64_bf16 a[64:67], a[72:75], v[80:87], v154\n"
        "ds_read2_b32 a[0:1], v148 offset0:0 offset1:1\n"
        "s_lshl_b32 s63, s63, 5\n"
        "v_min_u32 v230, s16, v230\n"
        "v_mfma_f32_16x16x32_bf16 v[192:195], a[144:147], v[84:87], v[192:195]\n"
        "ds_read2_b32 a[2:3], v148 offset0:2 offset1:3\n"
        "v_add_u32 v231, 3648, v152\n"
        "v_mfma_f32_16x16x32_bf16 v[196:199], a[148:151], v[84:87], v[196:199]\n"
        "ds_read_b128 a[192:195], v230 offset:0\n"
        "v_accvgpr_read_b32 v248, a64\n"
        "v_mfma_f32_16x16x32_bf16 v[200:203], a[152:155], v[84:87], v[200:203]\n"
        "ds_read2_b32 a[4:5], v148 offset0:128 offset1:129\n"
        "v_accvgpr_read_b32 v246, a65\n"
        "v_mfma_f32_16x16x32_bf16 v[204:207], a[156:159], v[84:87], v[204:207]\n"
        "ds_read2_b32 a[6:7], v148 offset0:130 offset1:131\n"
        "v_add_f32 v248, v248, v246\n"
        "v_mfma_f32_16x16x32_bf16 v[208:211], a[128:131], v[84:87], v[208:211]\n"
        "v_rcp_f32 v246, v248\n"
        "v_mfma_f32_16x16x32_bf16 v[212:215], a[132:135], v[84:87], v[212:215]\n"
        "ds_read_b128 a[196:199], v152 offset:3072\n"
        "v_accvgpr_read_b32 v249, a66\n"
        "v_mfma_f32_16x16x32_bf16 v[216:219], a[136:139], v[84:87], v[216:219]\n"
        "ds_read2_b32 a[8:9], v149 offset0:0 offset1:1\n"
        "v_accvgpr_read_b32 v247, a67\n"
        "v_mfma_f32_16x16x32_bf16 v[220:223], a[140:143], v[84:87], v[220:223]\n"
        "ds_read2_b32 a[10:11], v149 offset0:2 offset1:3\n"
        "v_add_f32 v249, v249, v247\n"
        "v_mfma_f32_16x16x32_bf16 v[188:191], a[140:143], v[80:83], v[188:191]\n"
        "v_rcp_f32 v247, v249\n"
        "s_waitcnt lgkmcnt(5)\n"
        "v_mul_f32 v192, v192, v247\n"
        "v_mul_f32 v193, v193, v247\n"
        "v_mul_f32 v194, v194, v247\n"
        "v_mul_f32 v195, v195, v247\n"
        "v_mul_f32 v196, v196, v247\n"
        "v_mul_f32 v197, v197, v247\n"
        "v_mul_f32 v198, v198, v247\n"
        "v_mul_f32 v199, v199, v247\n"
        "v_cvt_pk_bf16_f32 v192, v192, v193\n"
        "v_cvt_pk_bf16_f32 v193, v194, v195\n"
        "v_cvt_pk_bf16_f32 v194, v196, v197\n"
        "v_cvt_pk_bf16_f32 v195, v198, v199\n"
        "global_store_dwordx4 v254, v[192:195], s[70:71] offset:-4096 sc1\n"
        "ds_read_b128 a[200:203], v152 offset:2560\n"
        "s_waitcnt lgkmcnt(3)\n"
        "v_mul_f32 v160, v160, v246\n"
        "v_mul_f32 v161, v161, v246\n"
        "v_mul_f32 v162, v162, v246\n"
        "v_mul_f32 v163, v163, v246\n"
        "v_mul_f32 v164, v164, v246\n"
        "v_mul_f32 v165, v165, v246\n"
        "v_mul_f32 v166, v166, v246\n"
        "v_mul_f32 v167, v167, v246\n"
        "v_cvt_pk_bf16_f32 v160, v160, v161\n"
        "v_cvt_pk_bf16_f32 v161, v162, v163\n"
        "v_cvt_pk_bf16_f32 v162, v164, v165\n"
        "v_cvt_pk_bf16_f32 v163, v166, v167\n"
        "global_store_dwordx4 v254, v[160:163], s[70:71] offset:0 sc1\n"
        "s_add_u32 s70, s70, 0x2000\n"
        "s_addc_u32 s71, s71, 0\n"
        "s_waitcnt lgkmcnt(1)\n"
        "v_mul_f32 v200, v200, v247\n"
        "v_mul_f32 v201, v201, v247\n"
        "v_mul_f32 v202, v202, v247\n"
        "v_mul_f32 v203, v203, v247\n"
        "v_mul_f32 v204, v204, v247\n"
        "v_mul_f32 v205, v205, v247\n"
        "v_mul_f32 v206, v206, v247\n"
        "v_mul_f32 v207, v207, v247\n"
        "v_cvt_pk_bf16_f32 v200, v200, v201\n"
        "v_cvt_pk_bf16_f32 v201, v202, v203\n"
        "v_cvt_pk_bf16_f32 v202, v204, v205\n"
        "v_cvt_pk_bf16_f32 v203, v206, v207\n"
        "global_store_dwordx4 v254, v[200:203], s[70:71] offset:-4096 sc1\n"
        "ds_read2_b32 a[12:13], v149 offset0:128 offset1:129\n"
        "ds_read2_b32 a[14:15], v149 offset0:130 offset1:131\n"
        "v_mul_f32 v168, v168, v246\n"
        "v_mul_f32 v169, v169, v246\n"
        "v_mul_f32 v170, v170, v246\n"
        "v_mul_f32 v171, v171, v246\n"
        "v_mul_f32 v172, v172, v246\n"
        "v_mul_f32 v173, v173, v246\n"
        "v_mul_f32 v174, v174, v246\n"
        "v_mul_f32 v175, v175, v246\n"
        "v_cvt_pk_bf16_f32 v168, v168, v169\n"
        "v_cvt_pk_bf16_f32 v169, v170, v171\n"
        "v_cvt_pk_bf16_f32 v170, v172, v173\n"
        "v_cvt_pk_bf16_f32 v171, v174, v175\n"
        "global_store_dwordx4 v254, v[168:171], s[70:71] offset:0 sc1\n"
        "s_add_u32 s70, s70, 0x2000\n"
        "s_addc_u32 s71, s71, 0\n"
        "ds_read_b128 a[204:207], v152 offset:2048\n"
        "v_mul_f32 v208, v208, v247\n"
        "v_mul_f32 v209, v209, v247\n"
        "v_mul_f32 v210, v210, v247\n"
        "v_mul_f32 v211, v211, v247\n"
        "v_mul_f32 v212, v212, v247\n"
        "v_mul_f32 v213, v213, v247\n"
        "v_mul_f32 v214, v214, v247\n"
        "v_mul_f32 v215, v215, v247\n"
        "v_cvt_pk_bf16_f32 v208, v208, v209\n"
        "v_cvt_pk_bf16_f32 v209, v210, v211\n"
        "v_cvt_pk_bf16_f32 v210, v212, v213\n"
        "v_cvt_pk_bf16_f32 v211, v214, v215\n"
        "global_store_dwordx4 v254, v[208:211], s[70:71] offset:-4096 sc1\n"
        "ds_read2_b32 a[16:17], v150 offset0:0 offset1:1\n"
        "ds_read2_b32 a[18:19], v150 offset0:2 offset1:3\n"
        "v_mul_f32 v176, v176, v246\n"
        "v_mul_f32 v177, v177, v246\n"
        "v_mul_f32 v178, v178, v246\n"
        "v_mul_f32 v179, v179, v246\n"
        "v_mul_f32 v180, v180, v246\n"
        "v_mul_f32 v181, v181, v246\n"
        "v_mul_f32 v182, v182, v246\n"
        "v_mul_f32 v183, v183, v246\n"
        "v_cvt_pk_bf16_f32 v176, v176, v177\n"
        "v_cvt_pk_bf16_f32 v177, v178, v179\n"
        "v_cvt_pk_bf16_f32 v178, v180, v181\n"
        "v_cvt_pk_bf16_f32 v179, v182, v183\n"
        "global_store_dwordx4 v254, v[176:179], s[70:71] offset:0 sc1\n"
        "s_add_u32 s70, s70, 0x2000\n"
        "s_addc_u32 s71, s71, 0\n"
        "ds_read_b128 a[208:211], v152 offset:1536\n"
        "v_mul_f32 v216, v216, v247\n"
        "v_mfma_f32_16x16x32_bf16 v[96:99], a[0:3], a[192:195], 0\n"
        "ds_read2_b32 a[20:21], v150 offset0:128 offset1:129\n"
        "v_mul_f32 v217, v217, v247\n"
        "v_mfma_f32_16x16x32_bf16 v[100:103], a[0:3], a[196:199], 0\n"
        "ds_read2_b32 a[22:23], v150 offset0:130 offset1:131\n"
        "v_mul_f32 v218, v218, v247\n"
        "v_mfma_f32_16x16x32_bf16 v[100:103], a[4:7], a[192:195], v[100:103]\n"
        "ds_read_b128 a[212:215], v152 offset:1024\n"
        "v_mul_f32 v219, v219, v247\n"
        "v_mul_f32 v220, v220, v247\n"
        "v_mul_f32 v221, v221, v247\n"
        "v_mul_f32 v222, v222, v247\n"
        "v_mul_f32 v223, v223, v247\n"
        "v_cvt_pk_bf16_f32 v216, v216, v217\n"
        "v_cvt_pk_bf16_f32 v217, v218, v219\n"
        "v_cvt_pk_bf16_f32 v218, v220, v221\n"
        "v_cvt_pk_bf16_f32 v219, v222, v223\n"
        "global_store_dwordx4 v254, v[216:219], s[70:71] offset:-4096 sc1\n"
        "s_waitcnt lgkmcnt(4)\n"
        "v_mul_f32 v184, v184, v246\n"
        "v_mul_f32 v185, v185, v246\n"
        "v_mul_f32 v186, v186, v246\n"
        "v_mfma_f32_16x16x32_bf16 v[104:107], a[8:11], a[192:195], 0\n"
        "ds_read2_b32 a[24:25], v151 offset0:0 offset1:1\n"
        "v_mul_f32 v187, v187, v246\n"
        "v_mul_f32 v188, v188, v246\n"
        "v_mul_f32 v189, v189, v246\n"
        "v_mul_f32 v190, v190, v246\n"
        "v_mul_f32 v191, v191, v246\n"
        "v_cvt_pk_bf16_f32 v184, v184, v185\n"
        "v_cvt_pk_bf16_f32 v185, v186, v187\n"
        "v_cvt_pk_bf16_f32 v186, v188, v189\n"
        "v_cvt_pk_bf16_f32 v187, v190, v191\n"
        "global_store_dwordx4 v254, v[184:187], s[70:71] offset:0 sc1\n"
        "ds_read2_b32 a[26:27], v151 offset0:2 offset1:3\n"
        "ds_read_b128 a[216:219], v152 offset:512\n"
        "v_add_f32 v247, v248, v249\n"
        "v_mfma_f32_16x16x32_bf16 v[104:107], a[4:7], a[196:199], v[104:107]\n"
        "ds_read2_b32 a[28:29], v151 offset0:128 offset1:129\n"
        "v_cmp_nge_f32 vcc, 0x4e6e6b28, v247\n"
        "v_mfma_f32_16x16x32_bf16 v[104:107], a[0:3], a[200:203], v[104:107]\n"
        "ds_read2_b32 a[30:31], v151 offset0:130 offset1:131\n"
        "s_cbranch_vccnz LRH_%=\n"
        "LSH_%=:\n"
        "v_min_u32 v231, s16, v231\n"
        "v_mfma_f32_16x16x32_bf16 v[108:111], a[0:3], a[204:207], 0\n"
        "ds_read_b128 a[220:223], v152 offset:0\n"
        "v_and_b32 v234, 0x70, v254\n"
        "v_mfma_f32_16x16x32_bf16 v[108:111], a[4:7], a[200:203], v[108:111]\n"
        "ds_read2_b32 a[32:33], v148 offset0:16 offset1:17\n"
        "v_lshrrev_b32 v234, 1, v234\n"
        "v_mfma_f32_16x16x32_bf16 v[108:111], a[8:11], a[196:199], v[108:111]\n"
        "ds_read2_b32 a[34:35], v148 offset0:18 offset1:19\n"
        "v_and_b32 v235, 0x80, v254\n"
        "v_mfma_f32_16x16x32_bf16 v[108:111], a[12:15], a[192:195], v[108:111]\n"
        "ds_read_b128 a[224:227], v231 offset:0\n"
        "v_add3_u32 v234, v234, v235, s14\n"
        "v_mfma_f32_16x16x32_bf16 v[128:131], a[16:19], a[192:195], 0\n"
        "ds_read2_b32 a[36:37], v148 offset0:144 offset1:145\n"
        "s_and_b32 s14, s10, 1\n"
        "v_xor_b32 v154, 32, v153\n"
        "v_mfma_f32_16x16x32_bf16 v[128:131], a[12:15], a[196:199], v[128:131]\n"
        "ds_read2_b32 a[38:39], v148 offset0:146 offset1:147\n"
        "s_lshl1_add_u32 s62, s62, s14\n"
        "v_add_u32 v155, 0x10000, v153\n"
        "v_mfma_f32_16x16x32_bf16 v[128:131], a[8:11], a[200:203], v[128:131]\n"
        "ds_read_b128 a[228:231], v152 offset:3136\n"
        "s_lshl_b32 s62, s62, 6\n"
        "v_add_u32 v156, 0x10000, v154\n"
        "v_mfma_f32_16x16x32_bf16 v[128:131], a[4:7], a[204:207], v[128:131]\n"
        "ds_read2_b32 a[40:41], v149 offset0:16 offset1:17\n"
        "s_lshl_b32 s14, s62, 11\n"
        "s_waitcnt lgkmcnt(8)\n"
        "v_mfma_f32_16x16x32_bf16 v[128:131], a[0:3], a[208:211], v[128:131]\n"
        "ds_read2_b32 a[42:43], v149 offset0:18 offset1:19\n"
        "s_lshl1_add_u32 s14, s63, s14\n"
        "v_add_u32 v157, 0x20000, v153\n"
        "v_mfma_f32_16x16x32_bf16 v[132:135], a[0:3], a[212:215], 0\n"
        "ds_read_b128 a[232:235], v152 offset:2624\n"
        "v_add_u32 v158, 0x20000, v154\n"
        "v_mfma_f32_16x16x32_bf16 v[132:135], a[4:7], a[208:211], v[132:135]\n"
        "ds_read2_b32 a[44:45], v149 offset0:144 offset1:145\n"
        "v_add_u32 v230, 3712, v152\n"
        "v_mfma_f32_16x16x32_bf16 v[132:135], a[8:11], a[204:207], v[132:135]\n"
        "ds_read2_b32 a[46:47], v149 offset0:146 offset1:147\n"
        "v_min_u32 v230, s16, v230\n"
        "v_mfma_f32_16x16x32_bf16 v[132:135], a[12:15], a[200:203], v[132:135]\n"
        "ds_read_b128 a[236:239], v152 offset:2112\n"
        "s_and_b32 s67, s74, 1\n"
        "v_bfe_u32 v232, v254, 4, 4\n"
        "v_mfma_f32_16x16x32_bf16 v[132:135], a[16:19], a[196:199], v[132:135]\n"
        "ds_read2_b32 a[48:49], v150 offset0:16 offset1:17\n"
        "s_waitcnt vmcnt(9)\n"
        "v_mfma_f32_16x16x32_bf16 v[132:135], a[20:23], a[192:195], v[132:135]\n"
        "s_barrier\n"
        "v_mfma_f32_16x16x32_bf16 v[136:139], a[24:27], a[192:195], 0\n"
        "ds_read2_b32 a[50:51], v150 offset0:18 offset1:19\n"
        "s_lshl_b32 s67, s67, 12\n"
        "v_lshrrev_b32 v233, 8, v254\n"
        "v_mfma_f32_16x16x32_bf16 v[136:139], a[20:23], a[196:199], v[136:139]\n"
        "ds_read_b128 a[240:243], v152 offset:1600\n"
        "v_add_u32 v231, 3776, v152\n"
        "v_mfma_f32_16x16x32_bf16 v[136:139], a[16:19], a[200:203], v[136:139]\n"
        "ds_read2_b32 a[52:53], v150 offset0:144 offset1:145\n"
        "v_min_u32 v231, s16, v231\n"
        "v_mfma_f32_16x16x32_bf16 v[136:139], a[12:15], a[204:207], v[136:139]\n"
        "ds_read2_b32 a[54:55], v150 offset0:146 offset1:147\n"
        "s_add_u32 s64, s8, s14\n"
        "s_addc_u32 s65, s9, 0\n"
        "v_mfma_f32_16x16x32_bf16 v[136:139], a[8:11], a[208:211], v[136:139]\n"
        "ds_read_b128 a[244:247], v152 offset:1088\n"
        "s_add_u32 s56, s36, s14\n"
        "s_addc_u32 s57, s37, 0\n"
        "v_mfma_f32_16x16x32_bf16 v[136:139], a[4:7], a[212:215], v[136:139]\n"
        "ds_read2_b32 a[56:57], v151 offset0:16 offset1:17\n"
        "s_lshl2_add_u32 s14, s12, s10\n"
        "s_lshl_b32 s14, s14, 6\n"
        "v_mfma_f32_16x16x32_bf16 v[136:139], a[0:3], a[216:219], v[136:139]\n"
        "ds_read2_b32 a[58:59], v151 offset0:18 offset1:19\n"
        "s_lshl4_add_u32 s14, s40, s14\n"
        "s_waitcnt lgkmcnt(7)\n"
        "v_mfma_f32_16x16x32_bf16 v[140:143], a[0:3], a[220:223], 0\n"
        "ds_read_b128 a[248:251], v152 offset:576\n"
        "s_add_u32 s14, s14, 1024\n"
        "s_lshl_b32 s14, s14, 7\n"
        "v_mfma_f32_16x16x32_bf16 v[140:143], a[4:7], a[216:219], v[140:143]\n"
        "ds_read2_b32 a[60:61], v151 offset0:144 offset1:145\n"
        "v_lshl_add_u32 v159, v232, 7, s14\n"
        "v_mfma_f32_16x16x32_bf16 v[140:143], a[8:11], a[212:215], v[140:143]\n"
        "ds_read2_b32 a[62:63], v151 offset0:146 offset1:147\n"
        "s_add_u32 s24, s94, 0x400000\n"
        "s_addc_u32 s25, s95, 0\n"
        "v_mfma_f32_16x16x32_bf16 v[140:143], a[12:15], a[208:211], v[140:143]\n"
        "ds_read_b128 a[252:255], v152 offset:64\n"
        "v_mfma_f32_16x16x32_bf16 v[140:143], a[16:19], a[204:207], v[140:143]\n"
        "ds_read_b64_tr_b16 a[96:97], v153 offset:0\n"
        "v_mfma_f32_16x16x32_bf16 v[140:143], a[20:23], a[200:203], v[140:143]\n"
        "ds_read_b64_tr_b16 a[98:99], v153 offset:4096\n"
        "v_mfma_f32_16x16x32_bf16 v[140:143], a[24:27], a[196:199], v[140:143]\n"
        "ds_read2_b32 a[0:1], v148 offset0:32 offset1:33\n"
        "v_mfma_f32_16x16x32_bf16 v[140:143], a[28:31], a[192:195], v[140:143]\n"
        "s_mov_b64 exec, 0\n"
        "s_cmp_eq_u32 s40, 0\n"
        "s_cbranch_scc1 LHB0_%=\n"
        "s_cmp_eq_u32 s40, 1\n"
        "s_cbranch_scc1 LHB1_%=\n"
        "s_cmp_eq_u32 s40, 2\n"
        "s_cbranch_scc1 LHB2_%=\n"
        "s_branch LHB3_%=\n"
        "LHRB_%=:\n"
        "s_mov_b64 exec, -1\n"
        "v_mfma_f32_16x16x32_bf16 v[96:99], a[32:35], a[224:227], v[96:99]\n"
        "ds_read2_b32 a[2:3], v148 offset0:34 offset1:35\n"
        "s_waitcnt vmcnt(0)\n"
        "v_mfma_f32_16x16x32_bf16 v[100:103], a[32:35], a[228:231], v[100:103]\n"
        "s_mov_b64 exec, 0xffff\n"
        "global_store_dwordx2 v234, v[250:251], s[48:49] sc1\n"
        "s_mov_b64 exec, -1\n"
        "v_mfma_f32_16x16x32_bf16 v[100:103], a[36:39], a[224:227], v[100:103]\n"
        "ds_read_b128 a[192:195], v230 offset:0\n"
        "global_load_lds_dwordx4 v144, s[60:61] offset:0\n"
        "v_mfma_f32_16x16x32_bf16 v[104:107], a[40:43], a[224:227], v[104:107]\n"
        "ds_read_b64_tr_b16 a[100:101], v154 offset:0\n"
        "v_add_u32 v244, 0x10000, v254\n"
        "v_mfma_f32_16x16x32_bf16 v[104:107], a[36:39], a[228:231], v[104:107]\n"
        "ds_read_b64_tr_b16 a[102:103], v154 offset:4096\n"
        "v_add_u32 v245, 0x20000, v254\n"
        "v_mfma_f32_16x16x32_bf16 v[104:107], a[32:35], a[232:235], v[104:107]\n"
        "ds_read2_b32 a[4:5], v148 offset0:160 offset1:161\n"
        "v_add_u32 v230, 3840, v152\n"
        "v_mfma_f32_16x16x32_bf16 v[108:111], a[32:35], a[236:239], v[108:111]\n"
        "ds_read2_b32 a[6:7], v148 offset0:162 offset1:163\n"
        "global_load_lds_dwordx4 v145, s[60:61] offset:1024\n"
        "v_mfma_f32_16x16x32_bf16 v[108:111], a[36:39], a[232:235], v[108:111]\n"
        "ds_read_b128 a[196:199], v152 offset:3200\n"
        "s_add_u32 s14, s51, 504\n"
        "v_min_u32 v230, s16, v230\n"
        "v_mfma_f32_16x16x32_bf16 v[108:111], a[40:43], a[228:231], v[108:111]\n"
        "ds_read_b64_tr_b16 a[104:105], v153 offset:8192\n"
        "v_lshlrev_b32 v229, 5, v232\n"
        "v_mfma_f32_16x16x32_bf16 v[108:111], a[44:47], a[224:227], v[108:111]\n"
        "ds_read_b64_tr_b16 a[106:107], v153 offset:12288\n"
        "s_waitcnt lgkmcnt(8)\n"
        "v_mfma_f32_16x16x32_bf16 v[128:131], a[48:51], a[224:227], v[128:131]\n"
        "ds_read2_b32 a[8:9], v149 offset0:32 offset1:33\n"
        "global_load_lds_dwordx4 v146, s[60:61] offset:2048\n"
        "v_mfma_f32_16x16x32_bf16 v[128:131], a[44:47], a[228:231], v[128:131]\n"
        "ds_read2_b32 a[10:11], v149 offset0:34 offset1:35\n"
        "v_lshl_add_u32 v229, v233, 3, v229\n"
        "v_mfma_f32_16x16x32_bf16 v[128:131], a[40:43], a[232:235], v[128:131]\n"
        "ds_read_b128 a[200:203], v152 offset:2688\n"
        "v_sub_u32 v229, s14, v229\n"
        "v_mfma_f32_16x16x32_bf16 v[128:131], a[36:39], a[236:239], v[128:131]\n"
        "ds_read_b64_tr_b16 a[108:109], v154 offset:8192\n"
        "s_lshl_b32 s14, s41, 5\n"
        "s_add_u32 s14, s14, s42\n"
        "v_mfma_f32_16x16x32_bf16 v[128:131], a[32:35], a[240:243], v[128:131]\n"
        "ds_read_b64_tr_b16 a[110:111], v154 offset:12288\n"
        "global_load_lds_dwordx4 v147, s[60:61] offset:3072\n"
        "s_add_u32 m0, s73, 0xc000\n"
        "v_mfma_f32_16x16x32_bf16 v[132:135], a[32:35], a[244:247], v[132:135]\n"
        "ds_read2_b32 a[12:13], v149 offset0:160 offset1:161\n"
        "s_add_u32 s60, s22, s66\n"
        "s_addc_u32 s61, s23, 0\n"
        "v_mfma_f32_16x16x32_bf16 v[132:135], a[36:39], a[240:243], v[132:135]\n"
        "ds_read2_b32 a[14:15], v149 offset0:162 offset1:163\n"
        "s_xor_b32 s66, s29, 0x80000\n"
        "s_add_u32 s14, s14, 512\n"
        "v_mfma_f32_16x16x32_bf16 v[132:135], a[40:43], a[236:239], v[132:135]\n"
        "ds_read_b128 a[204:207], v152 offset:2176\n"
        "s_lshl_b32 s14, s14, 7\n"
        "v_lshrrev_b32 v234, 1, v254\n"
        "v_mfma_f32_16x16x32_bf16 v[132:135], a[44:47], a[232:235], v[132:135]\n"
        "ds_read_b64_tr_b16 a[112:113], v153 offset:16384\n"
        "global_load_lds_dwordx4 v144, s[60:61] offset:0\n"
        "v_mfma_f32_16x16x32_bf16 v[132:135], a[48:51], a[228:231], v[132:135]\n"
        "ds_read_b64_tr_b16 a[114:115], v153 offset:20480\n"
        "v_add_u32 v234, s14, v234\n"
        "v_mfma_f32_16x16x32_bf16 v[132:135], a[52:55], a[224:227], v[132:135]\n"
        "ds_read2_b32 a[16:17], v150 offset0:32 offset1:33\n"
        "s_lshr_b32 s14, s63, 2\n"
        "s_add_u32 s14, s14, 512\n"
        "v_mfma_f32_16x16x32_bf16 v[136:139], a[56:59], a[224:227], v[136:139]\n"
        "ds_read2_b32 a[18:19], v150 offset0:34 offset1:35\n"
        "s_lshl_b32 s14, s14, 7\n"
        "v_mfma_f32_16x16x32_bf16 v[136:139], a[52:55], a[228:231], v[136:139]\n"
        "ds_read_b128 a[208:211], v152 offset:1664\n"
        "global_load_lds_dwordx4 v145, s[60:61] offset:1024\n"
        "v_mfma_f32_16x16x32_bf16 v[136:139], a[48:51], a[232:235], v[136:139]\n"
        "ds_read_b64_tr_b16 a[116:117], v154 offset:16384\n"
        "v_mfma_f32_16x16x32_bf16 v[136:139], a[44:47], a[236:239], v[136:139]\n"
        "ds_read_b64_tr_b16 a[118:119], v154 offset:20480\n"
        "v_mfma_f32_16x16x32_bf16 v[136:139], a[40:43], a[240:243], v[136:139]\n"
        "ds_read2_b32 a[20:21], v150 offset0:160 offset1:161\n"
        "v_mfma_f32_16x16x32_bf16 v[136:139], a[36:39], a[244:247], v[136:139]\n"
        "ds_read2_b32 a[22:23], v150 offset0:162 offset1:163\n"
        "global_load_lds_dwordx4 v146, s[60:61] offset:2048\n"
        "v_mfma_f32_16x16x32_bf16 v[136:139], a[32:35], a[248:251], v[136:139]\n"
        "ds_read_b128 a[212:215], v152 offset:1152\n"
        "v_mfma_f32_16x16x32_bf16 v[140:143], a[32:35], a[252:255], v[140:143]\n"
        "ds_read_b64_tr_b16 a[120:121], v153 offset:24576\n"
        "v_mfma_f32_16x16x32_bf16 v[140:143], a[36:39], a[248:251], v[140:143]\n"
        "ds_read_b64_tr_b16 a[122:123], v153 offset:28672\n"
        "v_mfma_f32_16x16x32_bf16 v[140:143], a[40:43], a[244:247], v[140:143]\n"
        "ds_read_b64_tr_b16 a[124:125], v154 offset:24576\n"
        "global_load_lds_dwordx4 v147, s[60:61] offset:3072\n"
        "s_add_u32 m0, s73, 0x10000\n"
        "v_mfma_f32_16x16x32_bf16 v[140:143], a[44:47], a[240:243], v[140:143]\n"
        "ds_read_b64_tr_b16 a[126:127], v154 offset:28672\n"
        "s_add_u32 s60, s22, s66\n"
        "s_addc_u32 s61, s23, 0\n"
        "v_mfma_f32_16x16x32_bf16 v[140:143], a[48:51], a[236:239], v[140:143]\n"
        "ds_read2_b32 a[24:25], v151 offset0:32 offset1:33\n"
        "s_xor_b32 s66, s29, 0xa0000\n"
        "s_waitcnt lgkmcnt(8)\n"
        "v_mfma_f32_16x16x32_bf16 v[140:143], a[52:55], a[232:235], v[140:143]\n"
        "ds_read2_b32 a[26:27], v151 offset0:34 offset1:35\n"
        "v_mfma_f32_16x16x32_bf16 v[140:143], a[56:59], a[228:231], v[140:143]\n"
        "ds_read_b128 a[216:219], v152 offset:640\n"
        "global_load_lds_dwordx4 v144, s[60:61] offset:0\n"
        "v_mfma_f32_16x16x32_bf16 v[140:143], a[60:63], a[224:227], v[140:143]\n"
        "ds_read2_b32 a[28:29], v151 offset0:160 offset1:161\n"
        "v_mfma_f32_16x16x32_bf16 v[96:99], a[0:3], a[192:195], v[96:99]\n"
        "ds_read2_b32 a[30:31], v151 offset0:162 offset1:163\n"
        "v_mfma_f32_16x16x32_bf16 v[100:103], a[0:3], a[196:199], v[100:103]\n"
        "ds_read_b128 a[220:223], v152 offset:128\n"
        "v_mfma_f32_16x16x32_bf16 v[100:103], a[4:7], a[192:195], v[100:103]\n"
        "ds_read2_b32 a[32:33], v148 offset0:48 offset1:49\n"
        "global_load_lds_dwordx4 v145, s[60:61] offset:1024\n"
        "v_mfma_f32_16x16x32_bf16 v[104:107], a[8:11], a[192:195], v[104:107]\n"
        "ds_read2_b32 a[34:35], v148 offset0:50 offset1:51\n"
        "s_waitcnt lgkmcnt(8)\n"
        "v_mfma_f32_16x16x32_bf16 v[104:107], a[4:7], a[196:199], v[104:107]\n"
        "s_barrier\n"
        "v_mfma_f32_16x16x32_bf16 v[104:107], a[0:3], a[200:203], v[104:107]\n"
        "ds_read_b128 a[224:227], v231 offset:0\n"
        "v_add_u32 v231, 3904, v152\n"
        "v_mfma_f32_16x16x32_bf16 v[108:111], a[0:3], a[204:207], v[108:111]\n"
        "ds_read2_b32 a[36:37], v148 offset0:176 offset1:177\n"
        "global_load_lds_dwordx4 v146, s[60:61] offset:2048\n"
        "v_mfma_f32_16x16x32_bf16 v[108:111], a[4:7], a[200:203], v[108:111]\n"
        "ds_read2_b32 a[38:39], v148 offset0:178 offset1:179\n"
        "v_min_u32 v231, s16, v231\n"
        "v_mfma_f32_16x16x32_bf16 v[108:111], a[8:11], a[196:199], v[108:111]\n"
        "ds_read_b128 a[228:231], v152 offset:3264\n"
        "v_mfma_f32_16x16x32_bf16 v[108:111], a[12:15], a[192:195], v[108:111]\n"
        "ds_read2_b32 a[40:41], v149 offset0:48 offset1:49\n"
        "v_mfma_f32_16x16x32_bf16 v[128:131], a[16:19], a[192:195], v[128:131]\n"
        "ds_read2_b32 a[42:43], v149 offset0:50 offset1:51\n"
        "global_load_lds_dwordx4 v147, s[60:61] offset:3072\n"
        "s_add_u32 m0, s73, 0x14000\n"
        "v_mfma_f32_16x16x32_bf16 v[128:131], a[12:15], a[196:199], v[128:131]\n"
        "ds_read_b128 a[232:235], v152 offset:2752\n"
        "s_add_u32 s60, s22, s66\n"
        "s_addc_u32 s61, s23, 0\n"
        "v_mfma_f32_16x16x32_bf16 v[128:131], a[8:11], a[200:203], v[128:131]\n"
        "ds_read2_b32 a[44:45], v149 offset0:176 offset1:177\n"
        "s_xor_b32 s66, s29, 0xc0000\n"
        "v_mfma_f32_16x16x32_bf16 v[128:131], a[4:7], a[204:207], v[128:131]\n"
        "ds_read2_b32 a[46:47], v149 offset0:178 offset1:179\n"
        "v_mfma_f32_16x16x32_bf16 v[128:131], a[0:3], a[208:211], v[128:131]\n"
        "ds_read_b128 a[236:239], v152 offset:2240\n"
        "global_load_lds_dwordx4 v144, s[60:61] offset:0\n"
        "v_mfma_f32_16x16x32_bf16 v[132:135], a[0:3], a[212:215], v[132:135]\n"
        "ds_read2_b32 a[48:49], v150 offset0:48 offset1:49\n"
        "v_mfma_f32_16x16x32_bf16 v[132:135], a[4:7], a[208:211], v[132:135]\n"
        "ds_read2_b32 a[50:51], v150 offset0:50 offset1:51\n"
        "v_mfma_f32_16x16x32_bf16 v[132:135], a[8:11], a[204:207], v[132:135]\n"
        "ds_read_b128 a[240:243], v152 offset:1728\n"
        "s_waitcnt lgkmcnt(8)\n"
        "v_mfma_f32_16x16x32_bf16 v[132:135], a[12:15], a[200:203], v[132:135]\n"
        "ds_read2_b32 a[52:53], v150 offset0:176 offset1:177\n"
        "global_load_lds_dwordx4 v145, s[60:61] offset:1024\n"
        "v_mfma_f32_16x16x32_bf16 v[132:135], a[16:19], a[196:199], v[132:135]\n"
        "ds_read2_b32 a[54:55], v150 offset0:178 offset1:179\n"
        "v_mfma_f32_16x16x32_bf16 v[132:135], a[20:23], a[192:195], v[132:135]\n"
        "ds_read_b128 a[244:247], v152 offset:1216\n"
        "v_mfma_f32_16x16x32_bf16 v[136:139], a[24:27], a[192:195], v[136:139]\n"
        "ds_read2_b32 a[56:57], v151 offset0:48 offset1:49\n"
        "v_mfma_f32_16x16x32_bf16 v[136:139], a[20:23], a[196:199], v[136:139]\n"
        "ds_read2_b32 a[58:59], v151 offset0:50 offset1:51\n"
        "global_load_lds_dwordx4 v146, s[60:61] offset:2048\n"
        "v_mfma_f32_16x16x32_bf16 v[136:139], a[16:19], a[200:203], v[136:139]\n"
        "ds_read_b128 a[248:251], v152 offset:704\n"
        "v_mfma_f32_16x16x32_bf16 v[136:139], a[12:15], a[204:207], v[136:139]\n"
        "ds_read2_b32 a[60:61], v151 offset0:176 offset1:177\n"
        "v_mfma_f32_16x16x32_bf16 v[136:139], a[8:11], a[208:211], v[136:139]\n"
        "ds_read2_b32 a[62:63], v151 offset0:178 offset1:179\n"
        "v_mfma_f32_16x16x32_bf16 v[136:139], a[4:7], a[212:215], v[136:139]\n"
        "ds_read_b128 a[252:255], v152 offset:192\n"
        "global_load_lds_dwordx4 v147, s[60:61] offset:3072\n"
        "s_add_u32 m0, s73, 0x0\n"
        "v_mfma_f32_16x16x32_bf16 v[136:139], a[0:3], a[216:219], v[136:139]\n"
        "ds_read_b64 v[64:65], v229 offset:3584\n"
        "s_add_u32 s60, s22, s66\n"
        "s_addc_u32 s61, s23, 0\n"
        "v_mfma_f32_16x16x32_bf16 v[140:143], a[0:3], a[220:223], v[140:143]\n"
        "ds_read2_b32 a[0:1], v148 offset0:64 offset1:65\n"
        "s_xor_b32 s66, s29, 0xe0000\n"
        "v_mfma_f32_16x16x32_bf16 v[140:143], a[4:7], a[216:219], v[140:143]\n"
        "ds_read2_b32 a[2:3], v148 offset0:66 offset1:67\n"
        "v_mfma_f32_16x16x32_bf16 v[140:143], a[8:11], a[212:215], v[140:143]\n"
        "ds_read2_b32 a[4:5], v148 offset0:192 offset1:193\n"
        "global_load_lds_dwordx4 v144, s[60:61] offset:0\n"
        "v_mfma_f32_16x16x32_bf16 v[140:143], a[12:15], a[208:211], v[140:143]\n"
        "ds_read2_b32 a[6:7], v148 offset0:194 offset1:195\n"
        "v_mfma_f32_16x16x32_bf16 v[140:143], a[16:19], a[204:207], v[140:143]\n"
        "ds_read2_b32 a[8:9], v149 offset0:64 offset1:65\n"
        "v_mfma_f32_16x16x32_bf16 v[140:143], a[20:23], a[200:203], v[140:143]\n"
        "ds_read2_b32 a[10:11], v149 offset0:66 offset1:67\n"
        "v_mfma_f32_16x16x32_bf16 v[140:143], a[24:27], a[196:199], v[140:143]\n"
        "ds_read_b128 a[196:199], v152 offset:3328\n"
        "global_load_lds_dwordx4 v145, s[60:61] offset:1024\n"
        "v_mfma_f32_16x16x32_bf16 v[140:143], a[28:31], a[192:195], v[140:143]\n"
        "ds_read_b128 a[192:195], v230 offset:0\n"
        "v_add_u32 v230, 3968, v152\n"
        "v_mfma_f32_16x16x32_bf16 v[96:99], a[32:35], a[224:227], v[96:99]\n"
        "ds_read_b128 a[200:203], v152 offset:2816\n"
        "v_min_u32 v230, s16, v230\n"
        "v_mfma_f32_16x16x32_bf16 v[100:103], a[32:35], a[228:231], v[100:103]\n"
        "ds_read2_b32 a[12:13], v149 offset0:192 offset1:193\n"
        "s_waitcnt lgkmcnt(8)\n"
        "v_mfma_f32_16x16x32_bf16 v[100:103], a[36:39], a[224:227], v[100:103]\n"
        "ds_read2_b32 a[14:15], v149 offset0:194 offset1:195\n"
        "global_load_lds_dwordx4 v146, s[60:61] offset:2048\n"
        "v_mfma_f32_16x16x32_bf16 v[104:107], a[40:43], a[224:227], v[104:107]\n"
        "ds_read_b128 a[204:207], v152 offset:2304\n"
        "v_and_b32 v80, s35, v65\n"
        "v_mfma_f32_16x16x32_bf16 v[104:107], a[36:39], a[228:231], v[104:107]\n"
        "ds_read2_b32 a[16:17], v150 offset0:64 offset1:65\n"
        "v_lshlrev_b32 v81, 16, v65\n"
        "v_mfma_f32_16x16x32_bf16 v[104:107], a[32:35], a[232:235], v[104:107]\n"
        "ds_read2_b32 a[18:19], v150 offset0:66 offset1:67\n"
        "v_and_b32 v82, s35, v64\n"
        "v_mfma_f32_16x16x32_bf16 v[108:111], a[32:35], a[236:239], v[108:111]\n"
        "ds_read_b128 a[208:211], v152 offset:1792\n"
        "global_load_lds_dwordx4 v147, s[60:61] offset:3072\n"
        "s_add_u32 m0, s73, 0x4000\n"
        "v_mfma_f32_16x16x32_bf16 v[108:111], a[36:39], a[232:235], v[108:111]\n"
        "ds_read2_b32 a[20:21], v150 offset0:192 offset1:193\n"
        "s_add_u32 s60, s22, s66\n"
        "s_addc_u32 s61, s23, 0\n"
        "v_mfma_f32_16x16x32_bf16 v[108:111], a[40:43], a[228:231], v[108:111]\n"
        "ds_read2_b32 a[22:23], v150 offset0:194 offset1:195\n"
        "s_waitcnt vmcnt(12)\n"
        "v_mfma_f32_16x16x32_bf16 v[108:111], a[44:47], a[224:227], v[108:111]\n"
        "s_barrier\n"
        "v_mfma_f32_16x16x32_bf16 v[128:131], a[48:51], a[224:227], v[128:131]\n"
        "ds_read_b64_tr_b16 a[128:129], v153 offset:32768\n"
        "LHC2_%=:\n"
        "s_cbranch_execz LHRC_%=\n"
        "global_load_lds_dwordx4 v144, s[60:61] offset:0\n"
        "s_xor_b32 s66, s29, 0x100000\n"
        "v_mfma_f32_16x16x32_bf16 v[128:131], a[44:47], a[228:231], v[128:131]\n"
        "ds_read_b64_tr_b16 a[130:131], v153 offset:36864\n"
        "v_lshlrev_b32 v83, 16, v64\n"
        "v_mfma_f32_16x16x32_bf16 v[128:131], a[40:43], a[232:235], v[128:131]\n"
        "ds_read_b64_tr_b16 a[132:133], v154 offset:32768\n"
        "v_mul_f32 v80, s54, v80\n"
        "v_mfma_f32_16x16x32_bf16 v[128:131], a[36:39], a[236:239], v[128:131]\n"
        "ds_read_b64_tr_b16 a[134:135], v154 offset:36864\n"
        "v_mul_f32 v81, s54, v81\n"
        "v_mfma_f32_16x16x32_bf16 v[128:131], a[32:35], a[240:243], v[128:131]\n"
        "ds_read_b64_tr_b16 a[136:137], v153 offset:40960\n"
        "global_load_lds_dwordx4 v145, s[60:61] offset:1024\n"
        "v_mfma_f32_16x16x32_bf16 v[132:135], a[32:35], a[244:247], v[132:135]\n"
        "ds_read_b64_tr_b16 a[138:139], v153 offset:45056\n"
        "v_mul_f32 v82, s54, v82\n"
        "v_mfma_f32_16x16x32_bf16 v[132:135], a[36:39], a[240:243], v[132:135]\n"
        "ds_read_b64_tr_b16 a[140:141], v154 offset:40960\n"
        "v_mul_f32 v83, s54, v83\n"
        "v_mfma_f32_16x16x32_bf16 v[132:135], a[40:43], a[236:239], v[132:135]\n"
        "ds_read_b64_tr_b16 a[142:143], v154 offset:45056\n"
        "v_cvt_pk_bf16_f32 v80, 0, v80\n"
        "v_mfma_f32_16x16x32_bf16 v[132:135], a[44:47], a[232:235], v[132:135]\n"
        "ds_read_b64_tr_b16 a[144:145], v153 offset:49152\n"
        "global_load_lds_dwordx4 v146, s[60:61] offset:2048\n"
        "v_mfma_f32_16x16x32_bf16 v[132:135], a[48:51], a[228:231], v[132:135]\n"
        "ds_read_b64_tr_b16 a[146:147], v153 offset:53248\n"
        "v_cvt_pk_bf16_f32 v81, 0, v81\n"
        "v_mfma_f32_16x16x32_bf16 v[132:135], a[52:55], a[224:227], v[132:135]\n"
        "ds_read_b64_tr_b16 a[148:149], v154 offset:49152\n"
        "v_cvt_pk_bf16_f32 v82, 0, v82\n"
        "v_mfma_f32_16x16x32_bf16 v[136:139], a[56:59], a[224:227], v[136:139]\n"
        "ds_read_b64_tr_b16 a[150:151], v154 offset:53248\n"
        "v_cvt_pk_bf16_f32 v83, 0, v83\n"
        "v_mfma_f32_16x16x32_bf16 v[136:139], a[52:55], a[228:231], v[136:139]\n"
        "ds_read_b128 a[212:215], v152 offset:1280\n"
        "global_load_lds_dwordx4 v147, s[60:61] offset:3072\n"
        "s_add_u32 m0, s73, 0x8000\n"
        "v_mfma_f32_16x16x32_bf16 v[136:139], a[48:51], a[232:235], v[136:139]\n"
        "ds_read_b64_tr_b16 a[152:153], v153 offset:57344\n"
        "s_add_u32 s60, s22, s66\n"
        "s_addc_u32 s61, s23, 0\n"
        "v_mfma_f32_16x16x32_bf16 v[136:139], a[44:47], a[236:239], v[136:139]\n"
        "ds_read_b64_tr_b16 a[154:155], v153 offset:61440\n"
        "s_xor_b32 s66, s29, 0x120000\n"
        "v_mfma_f32_16x16x32_bf16 v[136:139], a[40:43], a[240:243], v[136:139]\n"
        "ds_read_b64_tr_b16 a[156:157], v154 offset:57344\n"
        "v_mfma_f32_16x16x32_bf16 v[136:139], a[36:39], a[244:247], v[136:139]\n"
        "ds_read_b64_tr_b16 a[158:159], v154 offset:61440\n"
        "v_mfma_f32_16x16x32_bf16 v[136:139], a[32:35], a[248:251], v[136:139]\n"
        "ds_read2_b32 a[24:25], v151 offset0:64 offset1:65\n"
        "v_mfma_f32_16x16x32_bf16 v[140:143], a[32:35], a[252:255], v[140:143]\n"
        "ds_read2_b32 a[26:27], v151 offset0:66 offset1:67\n"
        "v_mfma_f32_16x16x32_bf16 v[140:143], a[36:39], a[248:251], v[140:143]\n"
        "ds_read_b128 a[216:219], v152 offset:768\n"
        "v_mfma_f32_16x16x32_bf16 v[140:143], a[40:43], a[244:247], v[140:143]\n"
        "ds_read2_b32 a[28:29], v151 offset0:192 offset1:193\n"
        "v_mfma_f32_16x16x32_bf16 v[140:143], a[44:47], a[240:243], v[140:143]\n"
        "ds_read2_b32 a[30:31], v151 offset0:194 offset1:195\n"
        "v_mfma_f32_16x16x32_bf16 v[140:143], a[48:51], a[236:239], v[140:143]\n"
        "ds_read_b128 a[220:223], v152 offset:256\n"
        "s_waitcnt lgkmcnt(8)\n"
        "v_mfma_f32_16x16x32_bf16 v[140:143], a[52:55], a[232:235], v[140:143]\n"
        "ds_read2_b32 a[32:33], v148 offset0:80 offset1:81\n"
        "v_mfma_f32_16x16x32_bf16 v[140:143], a[56:59], a[228:231], v[140:143]\n"
        "ds_read2_b32 a[34:35], v148 offset0:82 offset1:83\n"
        "v_mfma_f32_16x16x32_bf16 v[140:143], a[60:63], a[224:227], v[140:143]\n"
        "ds_read_b128 a[224:227], v231 offset:0\n"
        "v_add_u32 v231, 4032, v152\n"
        "v_mfma_f32_16x16x32_bf16 v[96:99], a[0:3], a[192:195], v[96:99]\n"
        "ds_read2_b32 a[36:37], v148 offset0:208 offset1:209\n"
        "v_min_u32 v231, s16, v231\n"
        "v_mfma_f32_16x16x32_bf16 v[100:103], a[0:3], a[196:199], v[100:103]\n"
        "ds_read2_b32 a[38:39], v148 offset0:210 offset1:211\n"
        "v_mfma_f32_16x16x32_bf16 v[100:103], a[4:7], a[192:195], v[100:103]\n"
        "ds_read_b128 a[228:231], v152 offset:3392\n"
        "v_mfma_f32_16x16x32_bf16 v[104:107], a[8:11], a[192:195], v[104:107]\n"
        "ds_read2_b32 a[40:41], v149 offset0:80 offset1:81\n"
        "v_mfma_f32_16x16x32_bf16 v[104:107], a[4:7], a[196:199], v[104:107]\n"
        "ds_read2_b32 a[42:43], v149 offset0:82 offset1:83\n"
        "s_waitcnt vmcnt(8) lgkmcnt(14)\n"
        "v_mfma_f32_16x16x32_bf16 v[104:107], a[0:3], a[200:203], v[104:107]\n"
        "s_barrier\n"
        "v_mfma_f32_16x16x32_bf16 v[108:111], a[0:3], a[204:207], v[108:111]\n"
        "ds_read_b64_tr_b16 v[160:161], v155 offset:0\n"
        "global_load_lds_dwordx4 v144, s[60:61] offset:0\n"
        "v_mfma_f32_16x16x32_bf16 v[108:111], a[4:7], a[200:203], v[108:111]\n"
        "ds_read_b64_tr_b16 v[162:163], v155 offset:4096\n"
        "v_mfma_f32_16x16x32_bf16 v[108:111], a[8:11], a[196:199], v[108:111]\n"
        "ds_read_b64_tr_b16 v[164:165], v156 offset:0\n"
        "v_mfma_f32_16x16x32_bf16 v[108:111], a[12:15], a[192:195], v[108:111]\n"
        "ds_read_b64_tr_b16 v[166:167], v156 offset:4096\n"
        "v_mfma_f32_16x16x32_bf16 v[128:131], a[16:19], a[192:195], v[128:131]\n"
        "ds_read_b64_tr_b16 v[168:169], v155 offset:8192\n"
        "global_load_lds_dwordx4 v145, s[60:61] offset:1024\n"
        "v_mfma_f32_16x16x32_bf16 v[128:131], a[12:15], a[196:199], v[128:131]\n"
        "ds_read_b64_tr_b16 v[170:171], v155 offset:12288\n"
        "v_mfma_f32_16x16x32_bf16 v[128:131], a[8:11], a[200:203], v[128:131]\n"
        "ds_read_b128 a[232:235], v152 offset:2880\n"
        "v_mfma_f32_16x16x32_bf16 v[128:131], a[4:7], a[204:207], v[128:131]\n"
        "ds_read_b64_tr_b16 v[172:173], v156 offset:8192\n"
        "v_mfma_f32_16x16x32_bf16 v[128:131], a[0:3], a[208:211], v[128:131]\n"
        "ds_read_b64_tr_b16 v[174:175], v156 offset:12288\n"
        "global_load_lds_dwordx4 v146, s[60:61] offset:2048\n"
        "v_mfma_f32_16x16x32_bf16 v[132:135], a[0:3], a[212:215], v[132:135]\n"
        "ds_read2_b32 a[44:45], v149 offset0:208 offset1:209\n"
        "v_mfma_f32_16x16x32_bf16 v[132:135], a[4:7], a[208:211], v[132:135]\n"
        "ds_read2_b32 a[46:47], v149 offset0:210 offset1:211\n"
        "v_mfma_f32_16x16x32_bf16 v[132:135], a[8:11], a[204:207], v[132:135]\n"
        "ds_read_b128 a[236:239], v152 offset:2368\n"
        "s_waitcnt lgkmcnt(8)\n"
        "v_mfma_f32_16x16x32_bf16 v[132:135], a[12:15], a[200:203], v[132:135]\n"
        "ds_read_b64_tr_b16 v[176:177], v155 offset:16384\n"
        "global_load_lds_dwordx4 v147, s[60:61] offset:3072\n"
        "s_add_u32 m0, s73, 0xc000\n"
        "v_mfma_f32_16x16x32_bf16 v[132:135], a[16:19], a[196:199], v[132:135]\n"
        "ds_read_b64_tr_b16 v[178:179], v155 offset:20480\n"
        "s_add_u32 s60, s22, s66\n"
        "s_addc_u32 s61, s23, 0\n"
        "v_mfma_f32_16x16x32_bf16 v[132:135], a[20:23], a[192:195], v[132:135]\n"
        "ds_read2_b32 a[48:49], v150 offset0:80 offset1:81\n"
        "s_xor_b32 s66, s29, 0x140000\n"
        "v_mfma_f32_16x16x32_bf16 v[136:139], a[24:27], a[192:195], v[136:139]\n"
        "ds_read2_b32 a[50:51], v150 offset0:82 offset1:83\n"
        "v_mfma_f32_16x16x32_bf16 v[136:139], a[20:23], a[196:199], v[136:139]\n"
        "ds_read_b128 a[240:243], v152 offset:1856\n"
        "global_load_lds_dwordx4 v144, s[60:61] offset:0\n"
        "v_mfma_f32_16x16x32_bf16 v[136:139], a[16:19], a[200:203], v[136:139]\n"
        "ds_read_b64_tr_b16 v[180:181], v156 offset:16384\n"
        "v_mfma_f32_16x16x32_bf16 v[136:139], a[12:15], a[204:207], v[136:139]\n"
        "ds_read_b64_tr_b16 v[182:183], v156 offset:20480\n"
        "v_mfma_f32_16x16x32_bf16 v[136:139], a[8:11], a[208:211], v[136:139]\n"
        "ds_read2_b32 a[52:53], v150 offset0:208 offset1:209\n"
        "v_mfma_f32_16x16x32_bf16 v[136:139], a[4:7], a[212:215], v[136:139]\n"
        "ds_read2_b32 a[54:55], v150 offset0:210 offset1:211\n"
        "global_load_lds_dwordx4 v145, s[60:61] offset:1024\n"
        "v_mfma_f32_16x16x32_bf16 v[136:139], a[0:3], a[216:219], v[136:139]\n"
        "ds_read_b128 a[244:247], v152 offset:1344\n"
        "v_mfma_f32_16x16x32_bf16 v[140:143], a[0:3], a[220:223], v[140:143]\n"
        "ds_read_b64_tr_b16 v[184:185], v155 offset:24576\n"
        "v_mfma_f32_16x16x32_bf16 v[140:143], a[4:7], a[216:219], v[140:143]\n"
        "ds_read_b64_tr_b16 v[186:187], v155 offset:28672\n"
        "v_mfma_f32_16x16x32_bf16 v[140:143], a[8:11], a[212:215], v[140:143]\n"
        "ds_read2_b32 a[56:57], v151 offset0:80 offset1:81\n"
        "global_load_lds_dwordx4 v146, s[60:61] offset:2048\n"
        "v_mfma_f32_16x16x32_bf16 v[140:143], a[12:15], a[208:211], v[140:143]\n"
        "ds_read2_b32 a[58:59], v151 offset0:82 offset1:83\n"
        "v_mfma_f32_16x16x32_bf16 v[140:143], a[16:19], a[204:207], v[140:143]\n"
        "ds_read_b128 a[248:251], v152 offset:832\n"
        "v_mfma_f32_16x16x32_bf16 v[140:143], a[20:23], a[200:203], v[140:143]\n"
        "ds_read_b64_tr_b16 v[188:189], v156 offset:24576\n"
        "v_mfma_f32_16x16x32_bf16 v[140:143], a[24:27], a[196:199], v[140:143]\n"
        "ds_read_b64_tr_b16 v[190:191], v156 offset:28672\n"
        "global_load_lds_dwordx4 v147, s[60:61] offset:3072\n"
        "s_add_u32 m0, s73, 0x10000\n"
        "v_mfma_f32_16x16x32_bf16 v[140:143], a[28:31], a[192:195], v[140:143]\n"
        "ds_read2_b32 a[60:61], v151 offset0:208 offset1:209\n"
        "s_add_u32 s60, s22, s66\n"
        "s_addc_u32 s61, s23, 0\n"
        "v_mfma_f32_16x16x32_bf16 v[96:99], a[32:35], a[224:227], v[96:99]\n"
        "ds_read2_b32 a[62:63], v151 offset0:210 offset1:211\n"
        "s_xor_b32 s66, s29, 0x160000\n"
        "v_mfma_f32_16x16x32_bf16 v[100:103], a[32:35], a[228:231], v[100:103]\n"
        "ds_read_b128 a[252:255], v152 offset:320\n"
        "s_waitcnt lgkmcnt(8)\n"
        "v_mfma_f32_16x16x32_bf16 v[100:103], a[36:39], a[224:227], v[100:103]\n"
        "ds_read2_b32 a[0:1], v148 offset0:96 offset1:97\n"
        "global_load_dwordx2 v[236:237], v159, s[48:49] sc1\n"
        "v_mfma_f32_16x16x32_bf16 v[104:107], a[40:43], a[224:227], v[104:107]\n"
        "ds_read2_b32 a[2:3], v148 offset0:98 offset1:99\n"
        "v_mfma_f32_16x16x32_bf16 v[104:107], a[36:39], a[228:231], v[104:107]\n"
        "ds_read_b128 a[192:195], v230 offset:0\n"
        "v_lshrrev_b32 v230, 1, v233\n"
        "v_mfma_f32_16x16x32_bf16 v[104:107], a[32:35], a[232:235], v[104:107]\n"
        "ds_read2_b32 a[4:5], v148 offset0:224 offset1:225\n"
        "v_mfma_f32_16x16x32_bf16 v[108:111], a[32:35], a[236:239], v[108:111]\n"
        "ds_read2_b32 a[6:7], v148 offset0:226 offset1:227\n"
        "s_waitcnt vmcnt(9) lgkmcnt(8)\n"
        "v_mfma_f32_16x16x32_bf16 v[108:111], a[36:39], a[232:235], v[108:111]\n"
        "s_barrier\n"
        "v_mfma_f32_16x16x32_bf16 v[108:111], a[40:43], a[228:231], v[108:111]\n"
        "ds_read_b64_tr_b16 v[192:193], v153 offset:0\n"
        "global_load_lds_dwordx4 v144, s[60:61] offset:0\n"
        "v_mfma_f32_16x16x32_bf16 v[108:111], a[44:47], a[224:227], v[108:111]\n"
        "ds_read_b64_tr_b16 v[194:195], v153 offset:4096\n"
        "v_mfma_f32_16x16x32_bf16 v[128:131], a[48:51], a[224:227], v[128:131]\n"
        "ds_read_b64_tr_b16 v[196:197], v154 offset:0\n"
        "v_mfma_f32_16x16x32_bf16 v[128:131], a[44:47], a[228:231], v[128:131]\n"
        "ds_read_b64_tr_b16 v[198:199], v154 offset:4096\n"
        "v_mfma_f32_16x16x32_bf16 v[128:131], a[40:43], a[232:235], v[128:131]\n"
        "ds_read_b128 a[196:199], v152 offset:3456\n"
        "global_load_lds_dwordx4 v145, s[60:61] offset:1024\n"
        "v_mfma_f32_16x16x32_bf16 v[128:131], a[36:39], a[236:239], v[128:131]\n"
        "ds_read_b64_tr_b16 v[200:201], v153 offset:8192\n"
        "v_mfma_f32_16x16x32_bf16 v[128:131], a[32:35], a[240:243], v[128:131]\n"
        "ds_read_b64_tr_b16 v[202:203], v153 offset:12288\n"
        "v_mfma_f32_16x16x32_bf16 v[132:135], a[32:35], a[244:247], v[132:135]\n"
        "ds_read2_b32 a[8:9], v149 offset0:96 offset1:97\n"
        "v_mfma_f32_16x16x32_bf16 v[132:135], a[36:39], a[240:243], v[132:135]\n"
        "ds_read2_b32 a[10:11], v149 offset0:98 offset1:99\n"
        "global_load_lds_dwordx4 v146, s[60:61] offset:2048\n"
        "v_mfma_f32_16x16x32_bf16 v[132:135], a[40:43], a[236:239], v[132:135]\n"
        "ds_read_b128 a[200:203], v152 offset:2944\n"
        "v_mfma_f32_16x16x32_bf16 v[132:135], a[44:47], a[232:235], v[132:135]\n"
        "ds_read_b64_tr_b16 v[204:205], v154 offset:8192\n"
        "v_mfma_f32_16x16x32_bf16 v[132:135], a[48:51], a[228:231], v[132:135]\n"
        "ds_read_b64_tr_b16 v[206:207], v154 offset:12288\n"
        "v_mfma_f32_16x16x32_bf16 v[132:135], a[52:55], a[224:227], v[132:135]\n"
        "ds_read2_b32 a[12:13], v149 offset0:224 offset1:225\n"
        "global_load_lds_dwordx4 v147, s[60:61] offset:3072\n"
        "s_add_u32 m0, s73, 0x14000\n"
        "v_mfma_f32_16x16x32_bf16 v[136:139], a[56:59], a[224:227], v[136:139]\n"
        "ds_read2_b32 a[14:15], v149 offset0:226 offset1:227\n"
        "s_add_u32 s60, s22, s66\n"
        "s_addc_u32 s61, s23, 0\n"
        "v_mfma_f32_16x16x32_bf16 v[136:139], a[52:55], a[228:231], v[136:139]\n"
        "ds_read_b128 a[204:207], v152 offset:2432\n"
        "s_sub_u32 s66, 15, s12\n"
        "v_mfma_f32_16x16x32_bf16 v[136:139], a[48:51], a[232:235], v[136:139]\n"
        "ds_read_b64_tr_b16 v[208:209], v153 offset:16384\n"
        "s_cmp_lt_u32 s74, 2\n"
        "s_cselect_b32 s66, s12, s66\n"
        "v_mfma_f32_16x16x32_bf16 v[136:139], a[44:47], a[236:239], v[136:139]\n"
        "ds_read_b64_tr_b16 v[210:211], v153 offset:20480\n"
        "global_load_lds_dwordx4 v144, s[60:61] offset:0\n"
        "s_mul_i32 s66, s66, 1052672\n"
        "v_mfma_f32_16x16x32_bf16 v[136:139], a[40:43], a[240:243], v[136:139]\n"
        "ds_read2_b32 a[16:17], v150 offset0:96 offset1:97\n"
        "s_add_u32 s66, s66, s67\n"
        "s_and_b32 s67, s75, 1\n"
        "v_mfma_f32_16x16x32_bf16 v[136:139], a[36:39], a[244:247], v[136:139]\n"
        "ds_read2_b32 a[18:19], v150 offset0:98 offset1:99\n"
        "s_add_u32 s68, s20, s66\n"
        "s_addc_u32 s69, s21, 0\n"
        "v_mfma_f32_16x16x32_bf16 v[136:139], a[32:35], a[248:251], v[136:139]\n"
        "ds_read_b128 a[208:211], v152 offset:1920\n"
        "s_xor_b32 s66, s17, 0\n"
        "s_waitcnt lgkmcnt(8)\n"
        "v_mfma_f32_16x16x32_bf16 v[140:143], a[32:35], a[252:255], v[140:143]\n"
        "ds_read_b64_tr_b16 v[212:213], v154 offset:16384\n"
        "global_load_lds_dwordx4 v145, s[60:61] offset:1024\n"
        "s_lshl_b32 s66, s66, 15\n"
        "v_mfma_f32_16x16x32_bf16 v[140:143], a[36:39], a[248:251], v[140:143]\n"
        "ds_read_b64_tr_b16 v[214:215], v154 offset:20480\n"
        "s_lshl_b32 s67, s67, 12\n"
        "v_mfma_f32_16x16x32_bf16 v[140:143], a[40:43], a[244:247], v[140:143]\n"
        "ds_read2_b32 a[20:21], v150 offset0:224 offset1:225\n"
        "v_mfma_f32_16x16x32_bf16 v[140:143], a[44:47], a[240:243], v[140:143]\n"
        "ds_read2_b32 a[22:23], v150 offset0:226 offset1:227\n"
        "v_mfma_f32_16x16x32_bf16 v[140:143], a[48:51], a[236:239], v[140:143]\n"
        "ds_read_b128 a[212:215], v152 offset:1408\n"
        "global_load_lds_dwordx4 v146, s[60:61] offset:2048\n"
        "v_mfma_f32_16x16x32_bf16 v[140:143], a[52:55], a[232:235], v[140:143]\n"
        "ds_read_b64_tr_b16 v[216:217], v153 offset:24576\n"
        "s_waitcnt vmcnt(7) lgkmcnt(8)\n"
        "v_mfma_f32_16x16x32_bf16 v[140:143], a[56:59], a[228:231], v[140:143]\n"
        "ds_read_b64_tr_b16 v[218:219], v153 offset:28672\n"
        "v_cmp_ne_u64 vcc, v[236:237], v[250:251]\n"
        "v_mfma_f32_16x16x32_bf16 v[140:143], a[60:63], a[224:227], v[140:143]\n"
        "ds_read2_b32 a[24:25], v151 offset0:96 offset1:97\n"
        "s_cbranch_vccnz LPCX_%=\n"
        "LBCX_%=:\n"
        "v_mfma_f32_16x16x32_bf16 v[96:99], a[0:3], a[192:195], v[96:99]\n"
        "ds_read2_b32 a[26:27], v151 offset0:98 offset1:99\n"
        "global_load_lds_dwordx4 v147, s[60:61] offset:3072\n"
        "s_add_u32 m0, s73, 0x0\n"
        "v_mfma_f32_16x16x32_bf16 v[100:103], a[0:3], a[196:199], v[100:103]\n"
        "ds_read_b128 a[216:219], v152 offset:896\n"
        "s_add_u32 s60, s68, s66\n"
        "s_addc_u32 s61, s69, 0\n"
        "v_mfma_f32_16x16x32_bf16 v[100:103], a[4:7], a[192:195], v[100:103]\n"
        "ds_read_b64_tr_b16 v[220:221], v154 offset:24576\n"
        "s_xor_b32 s66, s17, 1\n"
        "s_lshl_b32 s66, s66, 15\n"
        "v_mfma_f32_16x16x32_bf16 v[104:107], a[8:11], a[192:195], v[104:107]\n"
        "ds_read_b64_tr_b16 v[222:223], v154 offset:28672\n"
        "v_mfma_f32_16x16x32_bf16 v[104:107], a[4:7], a[196:199], v[104:107]\n"
        "ds_read2_b32 a[28:29], v151 offset0:224 offset1:225\n"
        "global_load_dword v224, v159, s[48:49] offset:64 sc1\n"
        "v_mfma_f32_16x16x32_bf16 v[104:107], a[0:3], a[200:203], v[104:107]\n"
        "ds_read2_b32 a[30:31], v151 offset0:226 offset1:227\n"
        "v_mfma_f32_16x16x32_bf16 v[108:111], a[0:3], a[204:207], v[108:111]\n"
        "ds_read_b128 a[220:223], v152 offset:384\n"
        "s_waitcnt lgkmcnt(8)\n"
        "v_mfma_f32_16x16x32_bf16 v[108:111], a[4:7], a[200:203], v[108:111]\n"
        "ds_read2_b32 a[32:33], v148 offset0:112 offset1:113\n"
        "v_mfma_f32_16x16x32_bf16 v[108:111], a[8:11], a[196:199], v[108:111]\n"
        "ds_read2_b32 a[34:35], v148 offset0:114 offset1:115\n"
        "v_mfma_f32_16x16x32_bf16 v[108:111], a[12:15], a[192:195], v[108:111]\n"
        "ds_read_b128 a[224:227], v231 offset:0\n"
        "v_mfma_f32_16x16x32_bf16 v[128:131], a[16:19], a[192:195], v[128:131]\n"
        "ds_read2_b32 a[36:37], v148 offset0:240 offset1:241\n"
        "v_mfma_f32_16x16x32_bf16 v[128:131], a[12:15], a[196:199], v[128:131]\n"
        "ds_read2_b32 a[38:39], v148 offset0:242 offset1:243\n"
        "v_mfma_f32_16x16x32_bf16 v[128:131], a[8:11], a[200:203], v[128:131]\n"
        "ds_read_b128 a[228:231], v152 offset:3520\n"
        "s_waitcnt lgkmcnt(9)\n"
        "v_mfma_f32_16x16x32_bf16 v[128:131], a[4:7], a[204:207], v[128:131]\n"
        "s_barrier\n"
        "v_mfma_f32_16x16x32_bf16 v[128:131], a[0:3], a[208:211], v[128:131]\n"
        "ds_read_b64_tr_b16 v[112:113], v153 offset:32768\n"
        "global_load_lds_dwordx4 v254, s[60:61] offset:0 sc1\n"
        "v_mfma_f32_16x16x32_bf16 v[132:135], a[0:3], a[212:215], v[132:135]\n"
        "ds_read_b64_tr_b16 v[114:115], v153 offset:36864\n"
        "v_mfma_f32_16x16x32_bf16 v[132:135], a[4:7], a[208:211], v[132:135]\n"
        "ds_read_b64_tr_b16 v[116:117], v154 offset:32768\n"
        "v_mfma_f32_16x16x32_bf16 v[132:135], a[8:11], a[204:207], v[132:135]\n"
        "ds_read_b64_tr_b16 v[118:119], v154 offset:36864\n"
        "v_mfma_f32_16x16x32_bf16 v[132:135], a[12:15], a[200:203], v[132:135]\n"
        "ds_read_b64_tr_b16 v[120:121], v153 offset:40960\n"
        "global_load_lds_dwordx4 v254, s[60:61] offset:1024 sc1\n"
        "v_mfma_f32_16x16x32_bf16 v[132:135], a[16:19], a[196:199], v[132:135]\n"
        "ds_read_b64_tr_b16 v[122:123], v153 offset:45056\n"
        "v_mfma_f32_16x16x32_bf16 v[132:135], a[20:23], a[192:195], v[132:135]\n"
        "ds_read2_b32 a[40:41], v149 offset0:112 offset1:113\n"
        "v_mfma_f32_16x16x32_bf16 v[136:139], a[24:27], a[192:195], v[136:139]\n"
        "ds_read2_b32 a[42:43], v149 offset0:114 offset1:115\n"
        "v_mfma_f32_16x16x32_bf16 v[136:139], a[20:23], a[196:199], v[136:139]\n"
        "ds_read_b128 a[232:235], v152 offset:3008\n"
        "global_load_lds_dwordx4 v254, s[60:61] offset:2048 sc1\n"
        "v_mfma_f32_16x16x32_bf16 v[136:139], a[16:19], a[200:203], v[136:139]\n"
        "ds_read_b64_tr_b16 v[124:125], v154 offset:40960\n"
        "v_mfma_f32_16x16x32_bf16 v[136:139], a[12:15], a[204:207], v[136:139]\n"
        "ds_read_b64_tr_b16 v[126:127], v154 offset:45056\n"
        "s_waitcnt lgkmcnt(8)\n"
        "v_mfma_f32_16x16x32_bf16 v[136:139], a[8:11], a[208:211], v[136:139]\n"
        "ds_read2_b32 a[44:45], v149 offset0:240 offset1:241\n"
        "v_mfma_f32_16x16x32_bf16 v[136:139], a[4:7], a[212:215], v[136:139]\n"
        "ds_read2_b32 a[46:47], v149 offset0:242 offset1:243\n"
        "global_load_lds_dwordx4 v254, s[60:61] offset:3072 sc1\n"
        "s_add_u32 m0, s73, 0x4000\n"
        "v_mfma_f32_16x16x32_bf16 v[136:139], a[0:3], a[216:219], v[136:139]\n"
        "ds_read_b128 a[236:239], v152 offset:2496\n"
        "s_add_u32 s60, s68, s66\n"
        "s_addc_u32 s61, s69, 0\n"
        "v_mfma_f32_16x16x32_bf16 v[140:143], a[0:3], a[220:223], v[140:143]\n"
        "ds_read_b64_tr_b16 a[64:65], v153 offset:49152\n"
        "s_xor_b32 s66, s17, 2\n"
        "s_lshl_b32 s66, s66, 15\n"
        "v_mfma_f32_16x16x32_bf16 v[140:143], a[4:7], a[216:219], v[140:143]\n"
        "ds_read_b64_tr_b16 a[66:67], v153 offset:53248\n"
        "v_mfma_f32_16x16x32_bf16 v[140:143], a[8:11], a[212:215], v[140:143]\n"
        "ds_read2_b32 a[48:49], v150 offset0:112 offset1:113\n"
        "global_load_lds_dwordx4 v254, s[60:61] offset:0 sc1\n"
        "v_mfma_f32_16x16x32_bf16 v[140:143], a[12:15], a[208:211], v[140:143]\n"
        "ds_read2_b32 a[50:51], v150 offset0:114 offset1:115\n"
        "v_mfma_f32_16x16x32_bf16 v[140:143], a[16:19], a[204:207], v[140:143]\n"
        "ds_read_b128 a[240:243], v152 offset:1984\n"
        "v_mfma_f32_16x16x32_bf16 v[140:143], a[20:23], a[200:203], v[140:143]\n"
        "ds_read_b64_tr_b16 a[68:69], v154 offset:49152\n"
        "v_mfma_f32_16x16x32_bf16 v[140:143], a[24:27], a[196:199], v[140:143]\n"
        "ds_read_b64_tr_b16 a[70:71], v154 offset:53248\n"
        "global_load_lds_dwordx4 v254, s[60:61] offset:1024 sc1\n"
        "v_mfma_f32_16x16x32_bf16 v[140:143], a[28:31], a[192:195], v[140:143]\n"
        "ds_read2_b32 a[52:53], v150 offset0:240 offset1:241\n"
        "s_waitcnt lgkmcnt(8)\n"
        "v_mfma_f32_16x16x32_bf16 v[96:99], a[32:35], a[224:227], v[96:99]\n"
        "ds_read2_b32 a[54:55], v150 offset0:242 offset1:243\n"
        "v_mfma_f32_16x16x32_bf16 v[100:103], a[32:35], a[228:231], v[100:103]\n"
        "ds_read_b128 a[244:247], v152 offset:1472\n"
        "v_mfma_f32_16x16x32_bf16 v[100:103], a[36:39], a[224:227], v[100:103]\n"
        "ds_read_b64_tr_b16 a[72:73], v153 offset:57344\n"
        "global_load_lds_dwordx4 v254, s[60:61] offset:2048 sc1\n"
        "v_mfma_f32_16x16x32_bf16 v[104:107], a[40:43], a[224:227], v[104:107]\n"
        "ds_read_b64_tr_b16 a[74:75], v153 offset:61440\n"
        "v_cvt_pk_bf16_f32 v96, 0, v96\n"
        "v_mfma_f32_16x16x32_bf16 v[104:107], a[36:39], a[228:231], v[104:107]\n"
        "ds_read_b64_tr_b16 a[76:77], v154 offset:57344\n"
        "v_cvt_pk_bf16_f32 v97, 0, v97\n"
        "v_mfma_f32_16x16x32_bf16 v[104:107], a[32:35], a[232:235], v[104:107]\n"
        "ds_read_b64_tr_b16 a[78:79], v154 offset:61440\n"
        "v_cvt_pk_bf16_f32 v98, 0, v98\n"
        "v_mfma_f32_16x16x32_bf16 v[108:111], a[32:35], a[236:239], v[108:111]\n"
        "ds_read2_b32 a[56:57], v151 offset0:112 offset1:113\n"
        "global_load_lds_dwordx4 v254, s[60:61] offset:3072 sc1\n"
        "s_add_u32 m0, s73, 0x8000\n"
        "v_mfma_f32_16x16x32_bf16 v[108:111], a[36:39], a[232:235], v[108:111]\n"
        "ds_read2_b32 a[58:59], v151 offset0:114 offset1:115\n"
        "s_add_u32 s60, s68, s66\n"
        "s_addc_u32 s61, s69, 0\n"
        "v_mfma_f32_16x16x32_bf16 v[108:111], a[40:43], a[228:231], v[108:111]\n"
        "ds_read_b128 a[248:251], v152 offset:960\n"
        "s_xor_b32 s66, s29, 0x180000\n"
        "v_cvt_pk_bf16_f32 v99, 0, v99\n"
        "v_mfma_f32_16x16x32_bf16 v[108:111], a[44:47], a[224:227], v[108:111]\n"
        "ds_read_b64 v[66:67], v229 offset:3072\n"
        "s_waitcnt lgkmcnt(8)\n"
        "v_mfma_f32_16x16x32_bf16 v[128:131], a[48:51], a[224:227], v[128:131]\n"
        "ds_read_b64 v[68:69], v229 offset:2560\n"
        "v_cvt_pk_bf16_f32 v100, 0, v100\n"
        "v_mfma_f32_16x16x32_bf16 v[128:131], a[44:47], a[228:231], v[128:131]\n"
        "ds_read_b64 v[70:71], v229 offset:2048\n"
        "v_cvt_pk_bf16_f32 v101, 0, v101\n"
        "v_mfma_f32_16x16x32_bf16 v[128:131], a[40:43], a[232:235], v[128:131]\n"
        "ds_read_b64 v[72:73], v229 offset:1536\n"
        "v_cvt_pk_bf16_f32 v102, 0, v102\n"
        "v_mfma_f32_16x16x32_bf16 v[128:131], a[36:39], a[236:239], v[128:131]\n"
        "ds_read_b64 v[74:75], v229 offset:1024\n"
        "v_cvt_pk_bf16_f32 v103, 0, v103\n"
        "v_mfma_f32_16x16x32_bf16 v[128:131], a[32:35], a[240:243], v[128:131]\n"
        "ds_read_b64 v[76:77], v229 offset:512\n"
        "s_waitcnt vmcnt(9) lgkmcnt(9)\n"
        "v_mfma_f32_16x16x32_bf16 v[132:135], a[32:35], a[244:247], v[132:135]\n"
        "s_barrier\n"
        "v_mfma_f32_16x16x32_bf16 v[132:135], a[36:39], a[240:243], v[132:135]\n"
        "ds_read_b64 v[78:79], v229 offset:0\n"
        "global_load_lds_dwordx4 v254, s[60:61] offset:0 sc1\n"
        "v_mfma_f32_16x16x32_bf16 v[132:135], a[40:43], a[236:239], v[132:135]\n"
        "ds_read_b128 a[252:255], v152 offset:448\n"
        "v_add_f32 v80, v80, v96\n"
        "v_mfma_f32_16x16x32_bf16 v[132:135], a[44:47], a[232:235], v[132:135]\n"
        "ds_read2_b32 a[60:61], v151 offset0:240 offset1:241\n"
        "v_add_f32 v81, v81, v97\n"
        "v_mfma_f32_16x16x32_bf16 v[132:135], a[48:51], a[228:231], v[132:135]\n"
        "ds_read2_b32 a[62:63], v151 offset0:242 offset1:243\n"
        "v_add_f32 v82, v82, v98\n"
        "v_mfma_f32_16x16x32_bf16 v[132:135], a[52:55], a[224:227], v[132:135]\n"
        "ds_read_b64_tr_b16 a[80:81], v155 offset:0\n"
        "s_waitcnt lgkmcnt(7)\n"
        "v_mfma_f32_16x16x32_bf16 v[136:139], a[56:59], a[224:227], v[136:139]\n"
        "ds_read_b64_tr_b16 a[82:83], v155 offset:4096\n"
        "global_load_lds_dwordx4 v254, s[60:61] offset:1024 sc1\n"
        "v_mfma_f32_16x16x32_bf16 v[136:139], a[52:55], a[228:231], v[136:139]\n"
        "ds_read_b64_tr_b16 a[84:85], v156 offset:0\n"
        "v_and_b32 v84, s35, v67\n"
        "v_mfma_f32_16x16x32_bf16 v[136:139], a[48:51], a[232:235], v[136:139]\n"
        "ds_read_b64_tr_b16 a[86:87], v156 offset:4096\n"
        "v_lshlrev_b32 v85, 16, v67\n"
        "v_mfma_f32_16x16x32_bf16 v[136:139], a[44:47], a[236:239], v[136:139]\n"
        "ds_read_b64_tr_b16 a[88:89], v155 offset:8192\n"
        "v_and_b32 v86, s35, v66\n"
        "v_mfma_f32_16x16x32_bf16 v[136:139], a[40:43], a[240:243], v[136:139]\n"
        "ds_read_b64_tr_b16 a[90:91], v155 offset:12288\n"
        "global_load_lds_dwordx4 v254, s[60:61] offset:2048 sc1\n"
        "v_mfma_f32_16x16x32_bf16 v[136:139], a[36:39], a[244:247], v[136:139]\n"
        "ds_read_b64_tr_b16 a[92:93], v156 offset:8192\n"
        "v_lshlrev_b32 v87, 16, v66\n"
        "v_mfma_f32_16x16x32_bf16 v[136:139], a[32:35], a[248:251], v[136:139]\n"
        "ds_read_b64_tr_b16 a[94:95], v156 offset:12288\n"
        "s_waitcnt lgkmcnt(8)\n"
        "v_mfma_f32_16x16x32_bf16 v[140:143], a[32:35], a[252:255], v[140:143]\n"
        "ds_read_b64_tr_b16 a[160:161], v155 offset:16384\n"
        "v_mfma_f32_16x16x32_bf16 v[140:143], a[36:39], a[248:251], v[140:143]\n"
        "s_barrier\n"
        "v_mfma_f32_16x16x32_bf16 v[140:143], a[40:43], a[244:247], v[140:143]\n"
        "ds_read_b64_tr_b16 a[162:163], v155 offset:20480\n"
        "global_load_lds_dwordx4 v254, s[60:61] offset:3072 sc1\n"
        "s_add_u32 m0, s73, 0xc000\n"
        "v_mfma_f32_16x16x32_bf16 v[140:143], a[44:47], a[240:243], v[140:143]\n"
        "ds_read_b64_tr_b16 a[164:165], v156 offset:16384\n"
        "s_add_u32 s60, s22, s66\n"
        "s_addc_u32 s61, s23, 0\n"
        "v_mfma_f32_16x16x32_bf16 v[140:143], a[48:51], a[236:239], v[140:143]\n"
        "ds_read_b64_tr_b16 a[166:167], v156 offset:20480\n"
        "s_xor_b32 s66, s17, 3\n"
        "s_lshl_b32 s66, s66, 15\n"
        "v_mfma_f32_16x16x32_bf16 v[140:143], a[52:55], a[232:235], v[140:143]\n"
        "ds_read_b64_tr_b16 a[168:169], v155 offset:24576\n"
        "v_mul_f32 v84, s54, v84\n"
        "v_mfma_f32_16x16x32_bf16 v[140:143], a[56:59], a[228:231], v[140:143]\n"
        "ds_read_b64_tr_b16 a[170:171], v155 offset:28672\n"
        "global_load_lds_dwordx4 v144, s[60:61] offset:0\n"
        "v_mfma_f32_16x16x32_bf16 v[140:143], a[60:63], a[224:227], v[140:143]\n"
        "ds_read_b64_tr_b16 a[172:173], v156 offset:24576\n"
        "ds_read_b64_tr_b16 a[174:175], v156 offset:28672\n"
        "v_mul_f32 v85, s54, v85\n"
        "v_mul_f32 v86, s54, v86\n"
        "v_mul_f32 v87, s54, v87\n"
        "v_cvt_pk_bf16_f32 v84, 0, v84\n"
        "v_cvt_pk_bf16_f32 v85, 0, v85\n"
        "global_load_lds_dwordx4 v145, s[60:61] offset:1024\n"
        "v_cvt_pk_bf16_f32 v86, 0, v86\n"
        "v_cvt_pk_bf16_f32 v87, 0, v87\n"
        "v_add_f32 v83, v83, v99\n"
        "v_add_f32 v84, v84, v100\n"
        "v_add_f32 v85, v85, v101\n"
        "v_add_f32 v86, v86, v102\n"
        "v_add_f32 v87, v87, v103\n"
        "v_cvt_pk_bf16_f32 v88, v80, v81\n"
        "v_cvt_pk_bf16_f32 v89, v82, v83\n"
        "v_cvt_pk_bf16_f32 v90, v84, v85\n"
        "global_load_lds_dwordx4 v146, s[60:61] offset:2048\n"
        "v_cvt_pk_bf16_f32 v91, v86, v87\n"
        "v_and_b32 v80, s35, v69\n"
        "v_lshlrev_b32 v81, 16, v69\n"
        "v_and_b32 v82, s35, v68\n"
        "v_lshlrev_b32 v83, 16, v68\n"
        "v_and_b32 v84, s35, v71\n"
        "v_lshlrev_b32 v85, 16, v71\n"
        "v_and_b32 v86, s35, v70\n"
        "v_lshlrev_b32 v87, 16, v70\n"
        "v_mul_f32 v80, s54, v80\n"
        "global_load_lds_dwordx4 v147, s[60:61] offset:3072\n"
        "s_add_u32 m0, s73, 0x18000\n"
        "s_add_u32 s60, s68, s66\n"
        "s_addc_u32 s61, s69, 0\n"
        "s_sub_u32 s66, 15, s12\n"
        "s_cmp_lt_u32 s75, 2\n"
        "s_cselect_b32 s66, s12, s66\n"
        "s_mul_i32 s66, s66, 1052672\n"
        "s_add_u32 s66, s66, s67\n"
        "s_add_u32 s68, s20, s66\n"
        "s_addc_u32 s69, s21, 0\n"
        "s_xor_b32 s66, s17, 0\n"
        "s_lshl_b32 s66, s66, 15\n"
        "global_load_lds_dwordx4 v254, s[60:61] offset:0 sc1\n"
        "s_and_b32 s67, s74, 1\n"
        "s_lshl_b32 s67, s67, 12\n"
        "v_mul_f32 v81, s54, v81\n"
        "v_mul_f32 v82, s54, v82\n"
        "v_mul_f32 v83, s54, v83\n"
        "v_mul_f32 v84, s54, v84\n"
        "v_mul_f32 v85, s54, v85\n"
        "v_mul_f32 v86, s54, v86\n"
        "v_mul_f32 v87, s54, v87\n"
        "v_cvt_pk_bf16_f32 v104, 0, v104\n"
        "v_cvt_pk_bf16_f32 v105, 0, v105\n"
        "global_load_lds_dwordx4 v254, s[60:61] offset:1024 sc1\n"
        "v_cvt_pk_bf16_f32 v106, 0, v106\n"
        "v_cvt_pk_bf16_f32 v107, 0, v107\n"
        "v_cvt_pk_bf16_f32 v108, 0, v108\n"
        "v_cvt_pk_bf16_f32 v109, 0, v109\n"
        "v_cvt_pk_bf16_f32 v110, 0, v110\n"
        "v_cvt_pk_bf16_f32 v111, 0, v111\n"
        "v_cvt_pk_bf16_f32 v80, 0, v80\n"
        "s_waitcnt vmcnt(14) lgkmcnt(0)\n"
        "s_barrier\n"
        "ds_read_b128 a[0:3], v254 offset:0\n"
        "ds_read_b128 a[4:7], v254 offset:4096\n"
        "ds_read_b128 a[8:11], v254 offset:8192\n"
        "ds_read_b128 a[12:15], v254 offset:12288\n"
        "global_load_lds_dwordx4 v254, s[60:61] offset:2048 sc1\n"
        "global_load_lds_dwordx4 v254, s[60:61] offset:3072 sc1\n"
        "s_add_u32 m0, s73, 0x1c000\n"
        "s_add_u32 s60, s68, s66\n"
        "s_addc_u32 s61, s69, 0\n"
        "s_xor_b32 s66, s29, 0x1a0000\n"
        "ds_read_b128 a[16:19], v254 offset:1024\n"
        "ds_read_b128 a[20:23], v254 offset:5120\n"
        "ds_read_b128 a[24:27], v254 offset:9216\n"
        "s_waitcnt lgkmcnt(6)\n"
        "v_mfma_f32_16x16x32_bf16 v[0:3], a[96:99], a[0:3], 0\n"
        "s_waitcnt lgkmcnt(5)\n"
        "ds_read_b128 a[28:31], v254 offset:13312\n"
        "v_mfma_f32_16x16x32_bf16 v[0:3], a[104:107], a[4:7], v[0:3]\n"
        "s_waitcnt lgkmcnt(5)\n"
        "v_permlane16_swap_b32 v88, v90\n"
        "v_mfma_f32_16x16x32_bf16 v[0:3], a[112:115], a[8:11], v[0:3]\n"
        "s_waitcnt lgkmcnt(4)\n"
        "ds_read_b128 a[192:195], v254 offset:2048\n"
        "v_mfma_f32_16x16x32_bf16 v[0:3], a[120:123], a[12:15], v[0:3]\n"
        "ds_read_b128 a[196:199], v254 offset:6144\n"
        "global_load_lds_dwordx4 v254, s[60:61] offset:0 sc1\n"
        "v_mfma_f32_16x16x32_bf16 v[4:7], a[124:127], a[12:15], 0\n"
        "ds_read_b128 a[200:203], v254 offset:10240\n"
        "v_cvt_pk_bf16_f32 v81, 0, v81\n"
        "v_mfma_f32_16x16x32_bf16 v[4:7], a[116:119], a[8:11], v[4:7]\n"
        "ds_read_b128 a[204:207], v254 offset:14336\n"
        "v_cvt_pk_bf16_f32 v82, 0, v82\n"
        "v_mfma_f32_16x16x32_bf16 v[4:7], a[108:111], a[4:7], v[4:7]\n"
        "s_waitcnt lgkmcnt(7)\n"
        "v_permlane16_swap_b32 v89, v91\n"
        "v_mfma_f32_16x16x32_bf16 v[4:7], a[100:103], a[0:3], v[4:7]\n"
        "ds_read_b128 a[208:211], v254 offset:3072\n"
        "global_load_lds_dwordx4 v254, s[60:61] offset:1024 sc1\n"
        "v_mfma_f32_16x16x32_bf16 v[12:15], a[100:103], a[16:19], 0\n"
        "ds_read_b128 a[212:215], v254 offset:7168\n"
        "s_waitcnt lgkmcnt(6)\n"
        "v_mfma_f32_16x16x32_bf16 v[12:15], a[108:111], a[20:23], v[12:15]\n"
        "ds_read_b128 a[216:219], v254 offset:11264\n"
        "v_cvt_pk_bf16_f32 v83, 0, v83\n"
        "LHC3_%=:\n"
        "s_cbranch_execz LHRC_%=\n"
        "v_mfma_f32_16x16x32_bf16 v[12:15], a[116:119], a[24:27], v[12:15]\n"
        "ds_read_b128 a[220:223], v254 offset:15360\n"
        "v_cvt_pk_bf16_f32 v84, 0, v84\n"
        "v_mfma_f32_16x16x32_bf16 v[12:15], a[124:127], a[28:31], v[12:15]\n"
        "global_load_lds_dwordx4 v254, s[60:61] offset:2048 sc1\n"
        "v_cvt_pk_bf16_f32 v85, 0, v85\n"
        "v_mfma_f32_16x16x32_bf16 v[8:11], a[120:123], a[28:31], 0\n"
        "s_waitcnt lgkmcnt(5)\n"
        "v_cvt_pk_bf16_f32 v86, 0, v86\n"
        "v_cvt_pk_bf16_f32 v87, 0, v87\n"
        "v_mfma_f32_16x16x32_bf16 v[8:11], a[112:115], a[24:27], v[8:11]\n"
        "v_add_f32 v80, v80, v104\n"
        "v_add_f32 v81, v81, v105\n"
        "v_mfma_f32_16x16x32_bf16 v[8:11], a[104:107], a[20:23], v[8:11]\n"
        "global_load_lds_dwordx4 v254, s[60:61] offset:3072 sc1\n"
        "s_add_u32 m0, s73, 0x20000\n"
        "v_mfma_f32_16x16x32_bf16 v[8:11], a[96:99], a[16:19], v[8:11]\n"
        "s_add_u32 s60, s22, s66\n"
        "s_addc_u32 s61, s23, 0\n"
        "s_xor_b32 s66, s17, 1\n"
        "v_mfma_f32_16x16x32_bf16 v[16:19], a[96:99], a[192:195], 0\n"
        "s_lshl_b32 s66, s66, 15\n"
        "s_waitcnt vmcnt(16)\n"
        "v_add_f32 v82, v82, v106\n"
        "v_mfma_f32_16x16x32_bf16 v[16:19], a[104:107], a[196:199], v[16:19]\n"
        "s_barrier\n"
        "v_mfma_f32_16x16x32_bf16 v[16:19], a[112:115], a[200:203], v[16:19]\n"
        "ds_read_b128 a[0:3], v254 offset:16384\n"
        "s_waitcnt lgkmcnt(1)\n"
        "v_mfma_f32_16x16x32_bf16 v[16:19], a[120:123], a[204:207], v[16:19]\n"
        "ds_read_b128 a[4:7], v254 offset:20480\n"
        "global_load_lds_dwordx4 v144, s[60:61] offset:0\n"
        "v_mfma_f32_16x16x32_bf16 v[20:23], a[124:127], a[204:207], 0\n"
        "ds_read_b128 a[8:11], v254 offset:24576\n"
        "v_add_f32 v83, v83, v107\n"
        "v_mfma_f32_16x16x32_bf16 v[20:23], a[116:119], a[200:203], v[20:23]\n"
        "ds_read_b128 a[12:15], v254 offset:28672\n"
        "v_add_f32 v84, v84, v108\n"
        "v_mfma_f32_16x16x32_bf16 v[20:23], a[108:111], a[196:199], v[20:23]\n"
        "ds_read_b128 a[16:19], v254 offset:17408\n"
        "v_add_f32 v85, v85, v109\n"
        "v_mfma_f32_16x16x32_bf16 v[20:23], a[100:103], a[192:195], v[20:23]\n"
        "ds_read_b128 a[20:23], v254 offset:21504\n"
        "global_load_lds_dwordx4 v145, s[60:61] offset:1024\n"
        "v_mfma_f32_16x16x32_bf16 v[28:31], a[100:103], a[208:211], 0\n"
        "ds_read_b128 a[24:27], v254 offset:25600\n"
        "v_add_f32 v86, v86, v110\n"
        "v_mfma_f32_16x16x32_bf16 v[28:31], a[108:111], a[212:215], v[28:31]\n"
        "ds_read_b128 a[28:31], v254 offset:29696\n"
        "v_add_f32 v87, v87, v111\n"
        "v_mfma_f32_16x16x32_bf16 v[28:31], a[116:119], a[216:219], v[28:31]\n"
        "ds_read_b128 a[192:195], v254 offset:18432\n"
        "v_and_b32 v229, 1, v233\n"
        "v_mfma_f32_16x16x32_bf16 v[28:31], a[124:127], a[220:223], v[28:31]\n"
        "ds_read_b128 a[196:199], v254 offset:22528\n"
        "global_load_lds_dwordx4 v146, s[60:61] offset:2048\n"
        "v_mfma_f32_16x16x32_bf16 v[24:27], a[120:123], a[220:223], 0\n"
        "ds_read_b128 a[200:203], v254 offset:26624\n"
        "v_cvt_pk_bf16_f32 v92, v80, v81\n"
        "v_mfma_f32_16x16x32_bf16 v[24:27], a[112:115], a[216:219], v[24:27]\n"
        "ds_read_b128 a[204:207], v254 offset:30720\n"
        "v_cvt_pk_bf16_f32 v93, v82, v83\n"
        "v_mfma_f32_16x16x32_bf16 v[24:27], a[104:107], a[212:215], v[24:27]\n"
        "ds_read_b128 a[212:215], v254 offset:23552\n"
        "v_cvt_pk_bf16_f32 v94, v84, v85\n"
        "v_mfma_f32_16x16x32_bf16 v[24:27], a[96:99], a[208:211], v[24:27]\n"
        "s_waitcnt lgkmcnt(7)\n"
        "ds_read_b128 a[208:211], v254 offset:19456\n"
        "v_mfma_f32_16x16x32_bf16 v[0:3], a[128:131], a[0:3], v[0:3]\n"
        "ds_read_b128 a[216:219], v254 offset:27648\n"
        "global_load_lds_dwordx4 v147, s[60:61] offset:3072\n"
        "s_add_u32 m0, s73, 0x24000\n"
        "v_mfma_f32_16x16x32_bf16 v[0:3], a[136:139], a[4:7], v[0:3]\n"
        "ds_read_b128 a[220:223], v254 offset:31744\n"
        "s_add_u32 s60, s68, s66\n"
        "s_addc_u32 s61, s69, 0\n"
        "v_mfma_f32_16x16x32_bf16 v[0:3], a[144:147], a[8:11], v[0:3]\n"
        "s_xor_b32 s66, s17, 2\n"
        "v_permlane16_swap_b32 v92, v94\n"
        "v_mfma_f32_16x16x32_bf16 v[0:3], a[152:155], a[12:15], v[0:3]\n"
        "s_lshl_b32 s66, s66, 15\n"
        "v_cvt_pk_bf16_f32 v95, v86, v87\n"
        "v_lshlrev_b32 v229, 9, v229\n"
        "v_mfma_f32_16x16x32_bf16 v[4:7], a[156:159], a[12:15], v[4:7]\n"
        "v_permlane16_swap_b32 v93, v95\n"
        "v_mfma_f32_16x16x32_bf16 v[4:7], a[148:151], a[8:11], v[4:7]\n"
        "global_load_lds_dwordx4 v254, s[60:61] offset:0 sc1\n"
        "v_and_b32 v80, s35, v73\n"
        "v_mfma_f32_16x16x32_bf16 v[4:7], a[140:143], a[4:7], v[4:7]\n"
        "s_waitcnt lgkmcnt(3)\n"
        "v_lshlrev_b32 v81, 16, v73\n"
        "v_and_b32 v82, s35, v72\n"
        "v_mfma_f32_16x16x32_bf16 v[4:7], a[132:135], a[0:3], v[4:7]\n"
        "v_lshlrev_b32 v83, 16, v72\n"
        "v_and_b32 v84, s35, v75\n"
        "v_mfma_f32_16x16x32_bf16 v[12:15], a[132:135], a[16:19], v[12:15]\n"
        "global_load_lds_dwordx4 v254, s[60:61] offset:1024 sc1\n"
        "v_mfma_f32_16x16x32_bf16 v[12:15], a[140:143], a[20:23], v[12:15]\n"
        "v_lshlrev_b32 v85, 16, v75\n"
        "v_and_b32 v86, s35, v74\n"
        "v_mfma_f32_16x16x32_bf16 v[12:15], a[148:151], a[24:27], v[12:15]\n"
        "v_lshlrev_b32 v87, 16, v74\n"
        "v_lshl_add_u32 v229, v232, 5, v229\n"
        "v_mfma_f32_16x16x32_bf16 v[12:15], a[156:159], a[28:31], v[12:15]\n"
        "v_mul_f32 v80, s54, v80\n"
        "v_mul_f32 v81, s54, v81\n"
        "v_mfma_f32_16x16x32_bf16 v[8:11], a[152:155], a[28:31], v[8:11]\n"
        "global_load_lds_dwordx4 v254, s[60:61] offset:2048 sc1\n"
        "v_mul_f32 v82, s54, v82\n"
        "v_mfma_f32_16x16x32_bf16 v[8:11], a[144:147], a[24:27], v[8:11]\n"
        "v_mul_f32 v83, s54, v83\n"
        "v_mul_f32 v84, s54, v84\n"
        "v_mfma_f32_16x16x32_bf16 v[8:11], a[136:139], a[20:23], v[8:11]\n"
        "v_mul_f32 v85, s54, v85\n"
        "v_mul_f32 v86, s54, v86\n"
        "v_mfma_f32_16x16x32_bf16 v[8:11], a[128:131], a[16:19], v[8:11]\n"
        "global_load_lds_dwordx4 v254, s[60:61] offset:3072 sc1\n"
        "s_add_u32 m0, s73, 0x10000\n"
        "v_mfma_f32_16x16x32_bf16 v[16:19], a[128:131], a[192:195], v[16:19]\n"
        "s_add_u32 s60, s68, s66\n"
        "s_addc_u32 s61, s69, 0\n"
        "s_xor_b32 s66, s29, 0x1c0000\n"
        "v_mfma_f32_16x16x32_bf16 v[16:19], a[136:139], a[196:199], v[16:19]\n"
        "s_waitcnt vmcnt(20)\n"
        "v_mul_f32 v87, s54, v87\n"
        "v_cvt_pk_bf16_f32 v128, 0, v128\n"
        "v_mfma_f32_16x16x32_bf16 v[16:19], a[144:147], a[200:203], v[16:19]\n"
        "s_barrier\n"
        "v_mfma_f32_16x16x32_bf16 v[16:19], a[152:155], a[204:207], v[16:19]\n"
        "ds_read_b128 a[0:3], v254 offset:32768\n"
        "global_load_lds_dwordx4 v254, s[60:61] offset:0 sc1\n"
        "v_mfma_f32_16x16x32_bf16 v[20:23], a[156:159], a[204:207], v[20:23]\n"
        "ds_read_b128 a[4:7], v254 offset:36864\n"
        "v_cvt_pk_bf16_f32 v129, 0, v129\n"
        "v_mfma_f32_16x16x32_bf16 v[20:23], a[148:151], a[200:203], v[20:23]\n"
        "ds_read_b128 a[8:11], v254 offset:40960\n"
        "v_cvt_pk_bf16_f32 v130, 0, v130\n"
        "v_mfma_f32_16x16x32_bf16 v[20:23], a[140:143], a[196:199], v[20:23]\n"
        "ds_read_b128 a[12:15], v254 offset:45056\n"
        "v_cvt_pk_bf16_f32 v131, 0, v131\n"
        "v_mfma_f32_16x16x32_bf16 v[20:23], a[132:135], a[192:195], v[20:23]\n"
        "ds_read_b128 a[16:19], v254 offset:33792\n"
        "s_waitcnt lgkmcnt(5)\n"
        "v_mfma_f32_16x16x32_bf16 v[28:31], a[132:135], a[208:211], v[28:31]\n"
        "ds_read_b128 a[20:23], v254 offset:37888\n"
        "global_load_lds_dwordx4 v254, s[60:61] offset:1024 sc1\n"
        "v_mfma_f32_16x16x32_bf16 v[28:31], a[140:143], a[212:215], v[28:31]\n"
        "ds_read_b128 a[24:27], v254 offset:41984\n"
        "v_cvt_pk_bf16_f32 v132, 0, v132\n"
        "v_mfma_f32_16x16x32_bf16 v[28:31], a[148:151], a[216:219], v[28:31]\n"
        "ds_read_b128 a[28:31], v254 offset:46080\n"
        "v_cvt_pk_bf16_f32 v133, 0, v133\n"
        "v_mfma_f32_16x16x32_bf16 v[28:31], a[156:159], a[220:223], v[28:31]\n"
        "ds_read_b128 a[192:195], v254 offset:34816\n"
        "v_cvt_pk_bf16_f32 v134, 0, v134\n"
        "v_mfma_f32_16x16x32_bf16 v[24:27], a[152:155], a[220:223], v[24:27]\n"
        "ds_read_b128 a[196:199], v254 offset:38912\n"
        "global_load_lds_dwordx4 v254, s[60:61] offset:2048 sc1\n"
        "v_mfma_f32_16x16x32_bf16 v[24:27], a[144:147], a[216:219], v[24:27]\n"
        "ds_read_b128 a[200:203], v254 offset:43008\n"
        "v_cvt_pk_bf16_f32 v135, 0, v135\n"
        "v_mfma_f32_16x16x32_bf16 v[24:27], a[136:139], a[212:215], v[24:27]\n"
        "ds_read_b128 a[204:207], v254 offset:47104\n"
        "v_lshl_add_u32 v229, v230, 4, v229\n"
        "v_mfma_f32_16x16x32_bf16 v[24:27], a[128:131], a[208:211], v[24:27]\n"
        "ds_read_b128 a[208:211], v254 offset:35840\n"
        "s_waitcnt lgkmcnt(8)\n"
        "v_mfma_f32_16x16x32_bf16 v[0:3], v[160:163], a[0:3], v[0:3]\n"
        "ds_read_b128 a[212:215], v254 offset:39936\n"
        "global_load_lds_dwordx4 v254, s[60:61] offset:3072 sc1\n"
        "s_add_u32 m0, s73, 0x14000\n"
        "v_mfma_f32_16x16x32_bf16 v[0:3], v[168:171], a[4:7], v[0:3]\n"
        "ds_read_b128 a[216:219], v254 offset:44032\n"
        "s_add_u32 s60, s22, s66\n"
        "s_addc_u32 s61, s23, 0\n"
        "v_mfma_f32_16x16x32_bf16 v[0:3], v[176:179], a[8:11], v[0:3]\n"
        "ds_read_b128 a[220:223], v254 offset:48128\n"
        "s_xor_b32 s66, s17, 3\n"
        "s_lshl_b32 s66, s66, 15\n"
        "v_mfma_f32_16x16x32_bf16 v[0:3], v[184:187], a[12:15], v[0:3]\n"
        "v_cvt_pk_bf16_f32 v80, 0, v80\n"
        "v_cvt_pk_bf16_f32 v81, 0, v81\n"
        "v_mfma_f32_16x16x32_bf16 v[4:7], v[188:191], a[12:15], v[4:7]\n"
        "global_load_lds_dwordx4 v144, s[60:61] offset:0\n"
        "v_cvt_pk_bf16_f32 v82, 0, v82\n"
        "v_mfma_f32_16x16x32_bf16 v[4:7], v[180:183], a[8:11], v[4:7]\n"
        "s_waitcnt lgkmcnt(5)\n"
        "v_cvt_pk_bf16_f32 v83, 0, v83\n"
        "v_cvt_pk_bf16_f32 v84, 0, v84\n"
        "v_mfma_f32_16x16x32_bf16 v[4:7], v[172:175], a[4:7], v[4:7]\n"
        "v_cvt_pk_bf16_f32 v85, 0, v85\n"
        "v_cvt_pk_bf16_f32 v86, 0, v86\n"
        "v_mfma_f32_16x16x32_bf16 v[4:7], v[164:167], a[0:3], v[4:7]\n"
        "global_load_lds_dwordx4 v145, s[60:61] offset:1024\n"
        "v_mfma_f32_16x16x32_bf16 v[12:15], v[164:167], a[16:19], v[12:15]\n"
        "v_cvt_pk_bf16_f32 v87, 0, v87\n"
        "v_add_f32 v80, v80, v128\n"
        "v_mfma_f32_16x16x32_bf16 v[12:15], v[172:175], a[20:23], v[12:15]\n"
        "v_add_f32 v81, v81, v129\n"
        "v_add_f32 v82, v82, v130\n"
        "v_mfma_f32_16x16x32_bf16 v[12:15], v[180:183], a[24:27], v[12:15]\n"
        "v_add_f32 v83, v83, v131\n"
        "v_add_f32 v84, v84, v132\n"
        "v_mfma_f32_16x16x32_bf16 v[12:15], v[188:191], a[28:31], v[12:15]\n"
        "global_load_lds_dwordx4 v146, s[60:61] offset:2048\n"
        "v_add_f32 v85, v85, v133\n"
        "v_mfma_f32_16x16x32_bf16 v[8:11], v[184:187], a[28:31], v[8:11]\n"
        "v_add_f32 v86, v86, v134\n"
        "v_add_f32 v87, v87, v135\n"
        "v_mfma_f32_16x16x32_bf16 v[8:11], v[176:179], a[24:27], v[8:11]\n"
        "v_cvt_pk_bf16_f32 v136, 0, v136\n"
        "v_cvt_pk_bf16_f32 v137, 0, v137\n"
        "v_mfma_f32_16x16x32_bf16 v[8:11], v[168:171], a[20:23], v[8:11]\n"
        "global_load_lds_dwordx4 v147, s[60:61] offset:3072\n"
        "s_add_u32 m0, s73, 0x0\n"
        "v_mfma_f32_16x16x32_bf16 v[8:11], v[160:163], a[16:19], v[8:11]\n"
        "s_add_u32 s60, s68, s66\n"
        "s_addc_u32 s61, s69, 0\n"
        "s_waitcnt vmcnt(20)\n"
        "v_mfma_f32_16x16x32_bf16 v[16:19], v[160:163], a[192:195], v[16:19]\n"
        "s_barrier\n"
        "v_mfma_f32_16x16x32_bf16 v[16:19], v[168:171], a[196:199], v[16:19]\n"
        "ds_read_b64_tr_b16 a[176:177], v153 offset:49152\n"
        "s_sub_u32 s66, 15, s12\n"
        "v_cvt_pk_bf16_f32 v138, 0, v138\n"
        "v_mfma_f32_16x16x32_bf16 v[16:19], v[176:179], a[200:203], v[16:19]\n"
        "ds_read_b64_tr_b16 a[178:179], v153 offset:53248\n"
        "s_waitcnt lgkmcnt(2)\n"
        "v_mfma_f32_16x16x32_bf16 v[16:19], v[184:187], a[204:207], v[16:19]\n"
        "ds_read_b64_tr_b16 a[180:181], v154 offset:49152\n"
        "global_load_lds_dwordx4 v254, s[60:61] offset:0 sc1\n"
        "v_mfma_f32_16x16x32_bf16 v[20:23], v[188:191], a[204:207], v[20:23]\n"
        "ds_read_b64_tr_b16 a[182:183], v154 offset:53248\n"
        "s_cmp_lt_u32 s74, 2\n"
        "s_cselect_b32 s66, s12, s66\n"
        "v_mfma_f32_16x16x32_bf16 v[20:23], v[180:183], a[200:203], v[20:23]\n"
        "ds_read_b64_tr_b16 a[184:185], v153 offset:57344\n"
        "s_mul_i32 s66, s66, 1052672\n"
        "s_add_u32 s66, s66, s67\n"
        "v_mfma_f32_16x16x32_bf16 v[20:23], v[172:175], a[196:199], v[20:23]\n"
        "ds_read_b64_tr_b16 a[186:187], v153 offset:61440\n"
        "s_add_u32 s68, s20, s66\n"
        "s_addc_u32 s69, s21, 0\n"
        "v_mfma_f32_16x16x32_bf16 v[20:23], v[164:167], a[192:195], v[20:23]\n"
        "ds_read_b64_tr_b16 a[188:189], v154 offset:57344\n"
        "global_load_lds_dwordx4 v254, s[60:61] offset:1024 sc1\n"
        "s_xor_b32 s66, s17, 4\n"
        "v_mfma_f32_16x16x32_bf16 v[28:31], v[164:167], a[208:211], v[28:31]\n"
        "ds_read_b64_tr_b16 a[190:191], v154 offset:61440\n"
        "s_lshl_b32 s66, s66, 15\n"
        "s_and_b32 s67, s75, 1\n"
        "v_mfma_f32_16x16x32_bf16 v[28:31], v[172:175], a[212:215], v[28:31]\n"
        "ds_read_b128 a[0:3], v244 offset:32768\n"
        "s_lshl_b32 s67, s67, 12\n"
        "v_cvt_pk_bf16_f32 v139, 0, v139\n"
        "v_mfma_f32_16x16x32_bf16 v[28:31], v[180:183], a[216:219], v[28:31]\n"
        "ds_read_b128 a[4:7], v244 offset:36864\n"
        "v_cvt_pk_bf16_f32 v140, 0, v140\n"
        "v_mfma_f32_16x16x32_bf16 v[28:31], v[188:191], a[220:223], v[28:31]\n"
        "ds_read_b128 a[8:11], v244 offset:40960\n"
        "global_load_lds_dwordx4 v254, s[60:61] offset:2048 sc1\n"
        "v_mfma_f32_16x16x32_bf16 v[24:27], v[184:187], a[220:223], v[24:27]\n"
        "ds_read_b128 a[12:15], v244 offset:45056\n"
        "v_cvt_pk_bf16_f32 v141, 0, v141\n"
        "v_mfma_f32_16x16x32_bf16 v[24:27], v[176:179], a[216:219], v[24:27]\n"
        "ds_read_b128 a[16:19], v244 offset:33792\n"
        "v_cvt_pk_bf16_f32 v142, 0, v142\n"
        "v_mfma_f32_16x16x32_bf16 v[24:27], v[168:171], a[212:215], v[24:27]\n"
        "ds_read_b128 a[20:23], v244 offset:37888\n"
        "v_cvt_pk_bf16_f32 v143, 0, v143\n"
        "v_mfma_f32_16x16x32_bf16 v[24:27], v[160:163], a[208:211], v[24:27]\n"
        "ds_read_b128 a[24:27], v244 offset:41984\n"
        "global_load_lds_dwordx4 v254, s[60:61] offset:3072 sc1\n"
        "s_add_u32 m0, s73, 0x4000\n"
        "s_add_u32 s60, s68, s66\n"
        "s_addc_u32 s61, s69, 0\n"
        "ds_read_b128 a[28:31], v244 offset:46080\n"
        "s_xor_b32 s66, s29, 0x1e0000\n"
        "s_waitcnt lgkmcnt(7)\n"
        "v_mfma_f32_16x16x32_bf16 v[0:3], v[192:195], a[0:3], v[0:3]\n"
        "ds_read_b128 a[192:195], v244 offset:34816\n"
        "s_waitcnt lgkmcnt(7)\n"
        "v_mfma_f32_16x16x32_bf16 v[0:3], v[200:203], a[4:7], v[0:3]\n"
        "ds_read_b128 a[196:199], v244 offset:38912\n"
        "s_waitcnt lgkmcnt(7)\n"
        "v_mfma_f32_16x16x32_bf16 v[0:3], v[208:211], a[8:11], v[0:3]\n"
        "ds_read_b128 a[200:203], v244 offset:43008\n"
        "s_waitcnt lgkmcnt(7)\n"
        "v_mfma_f32_16x16x32_bf16 v[0:3], v[216:219], a[12:15], v[0:3]\n"
        "ds_read_b128 a[204:207], v244 offset:47104\n"
        "global_load_lds_dwordx4 v254, s[60:61] offset:0 sc1\n"
        "v_mfma_f32_16x16x32_bf16 v[4:7], v[220:223], a[12:15], v[4:7]\n"
        "ds_read_b128 a[208:211], v244 offset:35840\n"
        "s_waitcnt lgkmcnt(8)\n"
        "v_mfma_f32_16x16x32_bf16 v[4:7], v[212:215], a[8:11], v[4:7]\n"
        "ds_read_b128 a[212:215], v244 offset:39936\n"
        "s_waitcnt lgkmcnt(8)\n"
        "v_mfma_f32_16x16x32_bf16 v[4:7], v[204:207], a[4:7], v[4:7]\n"
        "ds_read_b128 a[216:219], v244 offset:44032\n"
        "s_waitcnt lgkmcnt(8)\n"
        "v_mfma_f32_16x16x32_bf16 v[4:7], v[196:199], a[0:3], v[4:7]\n"
        "ds_read_b128 a[220:223], v244 offset:48128\n"
        "global_load_lds_dwordx4 v254, s[60:61] offset:1024 sc1\n"
        "v_mfma_f32_16x16x32_bf16 v[12:15], v[196:199], a[16:19], v[12:15]\n"
        "s_waitcnt lgkmcnt(7)\n"
        "v_mfma_f32_16x16x32_bf16 v[12:15], v[204:207], a[20:23], v[12:15]\n"
        "v_mfma_f32_16x16x32_bf16 v[12:15], v[212:215], a[24:27], v[12:15]\n"
        "v_mfma_f32_16x16x32_bf16 v[12:15], v[220:223], a[28:31], v[12:15]\n"
        "global_load_lds_dwordx4 v254, s[60:61] offset:2048 sc1\n"
        "v_mfma_f32_16x16x32_bf16 v[8:11], v[216:219], a[28:31], v[8:11]\n"
        "v_mfma_f32_16x16x32_bf16 v[8:11], v[208:211], a[24:27], v[8:11]\n"
        "s_waitcnt lgkmcnt(2)\n"
        "v_mfma_f32_16x16x32_bf16 v[8:11], v[200:203], a[20:23], v[8:11]\n"
        "global_load_lds_dwordx4 v254, s[60:61] offset:3072 sc1\n"
        "s_add_u32 m0, s73, 0x8000\n"
        "v_mfma_f32_16x16x32_bf16 v[8:11], v[192:195], a[16:19], v[8:11]\n"
        "s_waitcnt vmcnt(24)\n"
        "s_add_u32 s60, s22, s66\n"
        "s_addc_u32 s61, s23, 0\n"
        "v_mfma_f32_16x16x32_bf16 v[16:19], v[192:195], a[192:195], v[16:19]\n"
        "s_barrier\n"
        "v_mfma_f32_16x16x32_bf16 v[16:19], v[200:203], a[196:199], v[16:19]\n"
        "ds_read_b128 a[0:3], v244 offset:49152\n"
        "s_xor_b32 s66, s17, 5\n"
        "s_lshl_b32 s66, s66, 15\n"
        "v_mfma_f32_16x16x32_bf16 v[16:19], v[208:211], a[200:203], v[16:19]\n"
        "ds_read_b128 a[4:7], v244 offset:53248\n"
        "global_load_lds_dwordx4 v144, s[60:61] offset:0\n"
        "v_mfma_f32_16x16x32_bf16 v[16:19], v[216:219], a[204:207], v[16:19]\n"
        "ds_read_b128 a[8:11], v244 offset:57344\n"
        "v_mfma_f32_16x16x32_bf16 v[20:23], v[220:223], a[204:207], v[20:23]\n"
        "ds_read_b128 a[12:15], v244 offset:61440\n"
        "v_mfma_f32_16x16x32_bf16 v[20:23], v[212:215], a[200:203], v[20:23]\n"
        "ds_read_b128 a[16:19], v244 offset:50176\n"
        "v_mfma_f32_16x16x32_bf16 v[20:23], v[204:207], a[196:199], v[20:23]\n"
        "ds_read_b128 a[20:23], v244 offset:54272\n"
        "global_load_lds_dwordx4 v145, s[60:61] offset:1024\n"
        "v_mfma_f32_16x16x32_bf16 v[20:23], v[196:199], a[192:195], v[20:23]\n"
        "ds_read_b128 a[24:27], v244 offset:58368\n"
        "s_waitcnt lgkmcnt(7)\n"
        "v_mfma_f32_16x16x32_bf16 v[28:31], v[196:199], a[208:211], v[28:31]\n"
        "ds_read_b128 a[28:31], v244 offset:62464\n"
        "v_mfma_f32_16x16x32_bf16 v[28:31], v[204:207], a[212:215], v[28:31]\n"
        "ds_read_b128 a[192:195], v244 offset:51200\n"
        "v_mfma_f32_16x16x32_bf16 v[28:31], v[212:215], a[216:219], v[28:31]\n"
        "ds_read_b128 a[196:199], v244 offset:55296\n"
        "global_load_lds_dwordx4 v146, s[60:61] offset:2048\n"
        "v_mfma_f32_16x16x32_bf16 v[28:31], v[220:223], a[220:223], v[28:31]\n"
        "ds_read_b128 a[200:203], v244 offset:59392\n"
        "v_mfma_f32_16x16x32_bf16 v[24:27], v[216:219], a[220:223], v[24:27]\n"
        "ds_read_b128 a[204:207], v244 offset:63488\n"
        "s_waitcnt lgkmcnt(8)\n"
        "v_mfma_f32_16x16x32_bf16 v[24:27], v[208:211], a[216:219], v[24:27]\n"
        "ds_read_b128 a[216:219], v244 offset:60416\n"
        "v_mfma_f32_16x16x32_bf16 v[24:27], v[200:203], a[212:215], v[24:27]\n"
        "ds_read_b128 a[212:215], v244 offset:56320\n"
        "global_load_lds_dwordx4 v147, s[60:61] offset:3072\n"
        "s_add_u32 m0, s73, 0xc000\n"
        "v_mfma_f32_16x16x32_bf16 v[24:27], v[192:195], a[208:211], v[24:27]\n"
        "ds_read_b128 a[208:211], v244 offset:52224\n"
        "s_add_u32 s60, s68, s66\n"
        "s_addc_u32 s61, s69, 0\n"
        "v_mfma_f32_16x16x32_bf16 v[32:35], a[96:99], a[0:3], 0\n"
        "ds_read_b128 a[220:223], v244 offset:64512\n"
        "s_xor_b32 s66, s17, 6\n"
        "s_lshl_b32 s66, s66, 15\n"
        "v_mfma_f32_16x16x32_bf16 v[32:35], a[104:107], a[4:7], v[32:35]\n"
        "v_mfma_f32_16x16x32_bf16 v[32:35], a[112:115], a[8:11], v[32:35]\n"
        "global_load_lds_dwordx4 v254, s[60:61] offset:0 sc1\n"
        "v_mfma_f32_16x16x32_bf16 v[32:35], a[120:123], a[12:15], v[32:35]\n"
        "v_mfma_f32_16x16x32_bf16 v[36:39], a[124:127], a[12:15], 0\n"
        "s_waitcnt lgkmcnt(4)\n"
        "v_mfma_f32_16x16x32_bf16 v[36:39], a[116:119], a[8:11], v[36:39]\n"
        "global_load_lds_dwordx4 v254, s[60:61] offset:1024 sc1\n"
        "v_mfma_f32_16x16x32_bf16 v[36:39], a[108:111], a[4:7], v[36:39]\n"
        "v_mfma_f32_16x16x32_bf16 v[36:39], a[100:103], a[0:3], v[36:39]\n"
        "v_mfma_f32_16x16x32_bf16 v[44:47], a[100:103], a[16:19], 0\n"
        "v_mfma_f32_16x16x32_bf16 v[44:47], a[108:111], a[20:23], v[44:47]\n"
        "global_load_lds_dwordx4 v254, s[60:61] offset:2048 sc1\n"
        "v_mfma_f32_16x16x32_bf16 v[44:47], a[116:119], a[24:27], v[44:47]\n"
        "v_mfma_f32_16x16x32_bf16 v[44:47], a[124:127], a[28:31], v[44:47]\n"
        "v_mfma_f32_16x16x32_bf16 v[40:43], a[120:123], a[28:31], 0\n"
        "global_load_lds_dwordx4 v254, s[60:61] offset:3072 sc1\n"
        "s_add_u32 m0, s73, 0x18000\n"
        "v_mfma_f32_16x16x32_bf16 v[40:43], a[112:115], a[24:27], v[40:43]\n"
        "s_add_u32 s60, s68, s66\n"
        "s_addc_u32 s61, s69, 0\n"
        "s_xor_b32 s66, s17, 7\n"
        "v_mfma_f32_16x16x32_bf16 v[40:43], a[104:107], a[20:23], v[40:43]\n"
        "s_lshl_b32 s66, s66, 15\n"
        "s_lshl2_add_u32 s15, s74, s10\n"
        "s_mul_i32 s16, s13, 263168\n"
        "v_mfma_f32_16x16x32_bf16 v[40:43], a[96:99], a[16:19], v[40:43]\n"
        "s_waitcnt vmcnt(24)\n"
        "s_lshl_b32 s15, s15, 14\n"
        "s_add_u32 s16, s16, s15\n"
        "v_mfma_f32_16x16x32_bf16 v[48:51], a[96:99], a[192:195], 0\n"
        "s_barrier\n"
        "v_mfma_f32_16x16x32_bf16 v[48:51], a[104:107], a[196:199], v[48:51]\n"
        "ds_read_b64_tr_b16 a[32:33], v157 offset:0\n"
        "global_load_lds_dwordx4 v254, s[60:61] offset:0 sc1\n"
        "s_add_u32 s16, s16, s73\n"
        "v_mfma_f32_16x16x32_bf16 v[48:51], a[112:115], a[200:203], v[48:51]\n"
        "ds_read_b64_tr_b16 a[34:35], v157 offset:4096\n"
        "s_add_u32 s16, s16, 0x4000000\n"
        "v_mfma_f32_16x16x32_bf16 v[48:51], a[120:123], a[204:207], v[48:51]\n"
        "ds_read_b64_tr_b16 a[36:37], v158 offset:0\n"
        "s_add_u32 s28, s38, s16\n"
        "s_addc_u32 s29, s39, 0\n"
        "v_mfma_f32_16x16x32_bf16 v[52:55], a[124:127], a[204:207], 0\n"
        "ds_read_b64_tr_b16 a[38:39], v158 offset:4096\n"
        "s_waitcnt lgkmcnt(4)\n"
        "v_mfma_f32_16x16x32_bf16 v[52:55], a[116:119], a[200:203], v[52:55]\n"
        "ds_read_b64_tr_b16 a[40:41], v157 offset:8192\n"
        "global_load_lds_dwordx4 v254, s[60:61] offset:1024 sc1\n"
        "v_mfma_f32_16x16x32_bf16 v[52:55], a[108:111], a[196:199], v[52:55]\n"
        "ds_read_b64_tr_b16 a[42:43], v157 offset:12288\n"
        "v_mfma_f32_16x16x32_bf16 v[52:55], a[100:103], a[192:195], v[52:55]\n"
        "ds_read_b64_tr_b16 a[44:45], v158 offset:8192\n"
        "v_mfma_f32_16x16x32_bf16 v[60:63], a[100:103], a[208:211], 0\n"
        "ds_read_b64_tr_b16 a[46:47], v158 offset:12288\n"
        "v_mfma_f32_16x16x32_bf16 v[60:63], a[108:111], a[212:215], v[60:63]\n"
        "ds_read_b128 a[0:3], v245 offset:16384\n"
        "global_load_lds_dwordx4 v254, s[60:61] offset:2048 sc1\n"
        "v_mfma_f32_16x16x32_bf16 v[60:63], a[116:119], a[216:219], v[60:63]\n"
        "ds_read_b128 a[4:7], v245 offset:20480\n"
        "v_mfma_f32_16x16x32_bf16 v[60:63], a[124:127], a[220:223], v[60:63]\n"
        "ds_read_b128 a[8:11], v245 offset:24576\n"
        "v_mfma_f32_16x16x32_bf16 v[56:59], a[120:123], a[220:223], 0\n"
        "ds_read_b128 a[12:15], v245 offset:28672\n"
        "v_mfma_f32_16x16x32_bf16 v[56:59], a[112:115], a[216:219], v[56:59]\n"
        "ds_read_b128 a[16:19], v245 offset:17408\n"
        "global_load_lds_dwordx4 v254, s[60:61] offset:3072 sc1\n"
        "s_add_u32 m0, s73, 0x1c000\n"
        "v_mfma_f32_16x16x32_bf16 v[56:59], a[104:107], a[212:215], v[56:59]\n"
        "ds_read_b128 a[20:23], v245 offset:21504\n"
        "s_add_u32 s60, s68, s66\n"
        "s_addc_u32 s61, s69, 0\n"
        "v_mfma_f32_16x16x32_bf16 v[56:59], a[96:99], a[208:211], v[56:59]\n"
        "ds_read_b128 a[24:27], v245 offset:25600\n"
        "ds_read_b128 a[28:31], v245 offset:29696\n"
        "ds_read_b128 a[192:195], v245 offset:18432\n"
        "s_sub_u32 s66, 15, s12\n"
        "s_waitcnt lgkmcnt(8)\n"
        "v_mfma_f32_16x16x32_bf16 v[32:35], a[128:131], a[0:3], v[32:35]\n"
        "ds_read_b128 a[196:199], v245 offset:22528\n"
        "s_waitcnt lgkmcnt(8)\n"
        "v_mfma_f32_16x16x32_bf16 v[32:35], a[136:139], a[4:7], v[32:35]\n"
        "ds_read_b128 a[200:203], v245 offset:26624\n"
        "s_waitcnt lgkmcnt(8)\n"
        "v_mfma_f32_16x16x32_bf16 v[32:35], a[144:147], a[8:11], v[32:35]\n"
        "ds_read_b128 a[204:207], v245 offset:30720\n"
        "s_waitcnt lgkmcnt(8)\n"
        "v_mfma_f32_16x16x32_bf16 v[32:35], a[152:155], a[12:15], v[32:35]\n"
        "ds_read_b128 a[208:211], v245 offset:19456\n"
        "s_cmp_lt_u32 s75, 2\n"
        "s_cselect_b32 s66, s12, s66\n"
        "v_mfma_f32_16x16x32_bf16 v[36:39], a[156:159], a[12:15], v[36:39]\n"
        "ds_read_b128 a[212:215], v245 offset:23552\n"
        "s_mul_i32 s66, s66, 1052672\n"
        "s_add_u32 s66, s66, s67\n"
        "v_mfma_f32_16x16x32_bf16 v[36:39], a[148:151], a[8:11], v[36:39]\n"
        "ds_read_b128 a[216:219], v245 offset:27648\n"
        "s_add_u32 s68, s20, s66\n"
        "s_addc_u32 s69, s21, 0\n"
        "v_mfma_f32_16x16x32_bf16 v[36:39], a[140:143], a[4:7], v[36:39]\n"
        "ds_read_b128 a[220:223], v245 offset:31744\n"
        "s_xor_b32 s66, s17, 4\n"
        "s_lshl_b32 s66, s66, 15\n"
        "v_mfma_f32_16x16x32_bf16 v[36:39], a[132:135], a[0:3], v[36:39]\n"
        "s_and_b32 s67, s76, 1\n"
        "s_lshl_b32 s67, s67, 12\n"
        "s_waitcnt lgkmcnt(7)\n"
        "v_mfma_f32_16x16x32_bf16 v[44:47], a[132:135], a[16:19], v[44:47]\n"
        "global_store_dwordx4 v229, v[88:91], s[24:25] offset:0 sc1\n"
        "v_mfma_f32_16x16x32_bf16 v[44:47], a[140:143], a[20:23], v[44:47]\n"
        "v_mfma_f32_16x16x32_bf16 v[44:47], a[148:151], a[24:27], v[44:47]\n"
        "v_cvt_pk_bf16_f32 v88, v80, v81\n"
        "v_cvt_pk_bf16_f32 v89, v82, v83\n"
        "v_mfma_f32_16x16x32_bf16 v[44:47], a[156:159], a[28:31], v[44:47]\n"
        "v_cvt_pk_bf16_f32 v90, v84, v85\n"
        "v_cvt_pk_bf16_f32 v91, v86, v87\n"
        "v_mfma_f32_16x16x32_bf16 v[40:43], a[152:155], a[28:31], v[40:43]\n"
        "v_permlane16_swap_b32 v88, v90\n"
        "v_mfma_f32_16x16x32_bf16 v[40:43], a[144:147], a[24:27], v[40:43]\n"
        "s_waitcnt lgkmcnt(1)\n"
        "v_permlane16_swap_b32 v89, v91\n"
        "v_mfma_f32_16x16x32_bf16 v[40:43], a[136:139], a[20:23], v[40:43]\n"
        "v_and_b32 v80, s35, v77\n"
        "v_lshlrev_b32 v81, 16, v77\n"
        "v_mfma_f32_16x16x32_bf16 v[40:43], a[128:131], a[16:19], v[40:43]\n"
        "s_waitcnt vmcnt(25)\n"
        "v_and_b32 v82, s35, v76\n"
        "v_lshlrev_b32 v83, 16, v76\n"
        "v_mfma_f32_16x16x32_bf16 v[48:51], a[128:131], a[192:195], v[48:51]\n"
        "s_barrier\n"
        "v_mfma_f32_16x16x32_bf16 v[48:51], a[136:139], a[196:199], v[48:51]\n"
        "ds_read_b128 a[0:3], v244 offset:0\n"
        "global_load_lds_dwordx4 v254, s[60:61] offset:0 sc1\n"
        "v_mfma_f32_16x16x32_bf16 v[48:51], a[144:147], a[200:203], v[48:51]\n"
        "ds_read_b128 a[4:7], v244 offset:4096\n"
        "v_and_b32 v84, s35, v79\n"
        "v_mfma_f32_16x16x32_bf16 v[48:51], a[152:155], a[204:207], v[48:51]\n"
        "ds_read_b128 a[8:11], v244 offset:8192\n"
        "v_lshlrev_b32 v85, 16, v79\n"
        "v_mfma_f32_16x16x32_bf16 v[52:55], a[156:159], a[204:207], v[52:55]\n"
        "ds_read_b128 a[12:15], v244 offset:12288\n"
        "v_and_b32 v86, s35, v78\n"
        "v_mfma_f32_16x16x32_bf16 v[52:55], a[148:151], a[200:203], v[52:55]\n"
        "ds_read_b128 a[16:19], v244 offset:1024\n"
        "global_load_lds_dwordx4 v254, s[60:61] offset:1024 sc1\n"
        "v_mfma_f32_16x16x32_bf16 v[52:55], a[140:143], a[196:199], v[52:55]\n"
        "ds_read_b128 a[20:23], v244 offset:5120\n"
        "v_lshlrev_b32 v87, 16, v78\n"
        "v_mfma_f32_16x16x32_bf16 v[52:55], a[132:135], a[192:195], v[52:55]\n"
        "ds_read_b128 a[24:27], v244 offset:9216\n"
        "v_mul_f32 v80, s54, v80\n"
        "v_mfma_f32_16x16x32_bf16 v[60:63], a[132:135], a[208:211], v[60:63]\n"
        "ds_read_b128 a[28:31], v244 offset:13312\n"
        "v_mul_f32 v81, s54, v81\n"
        "v_mfma_f32_16x16x32_bf16 v[60:63], a[140:143], a[212:215], v[60:63]\n"
        "ds_read_b128 a[192:195], v244 offset:2048\n"
        "global_load_lds_dwordx4 v254, s[60:61] offset:2048 sc1\n"
        "v_mfma_f32_16x16x32_bf16 v[60:63], a[148:151], a[216:219], v[60:63]\n"
        "ds_read_b128 a[196:199], v244 offset:6144\n"
        "s_waitcnt lgkmcnt(8)\n"
        "v_mfma_f32_16x16x32_bf16 v[60:63], a[156:159], a[220:223], v[60:63]\n"
        "ds_read_b128 a[200:203], v244 offset:10240\n"
        "v_mul_f32 v82, s54, v82\n"
        "v_mfma_f32_16x16x32_bf16 v[56:59], a[152:155], a[220:223], v[56:59]\n"
        "ds_read_b128 a[204:207], v244 offset:14336\n"
        "v_mul_f32 v83, s54, v83\n"
        "v_mfma_f32_16x16x32_bf16 v[56:59], a[144:147], a[216:219], v[56:59]\n"
        "ds_read_b128 a[216:219], v244 offset:11264\n"
        "global_load_lds_dwordx4 v254, s[60:61] offset:3072 sc1\n"
        "s_add_u32 m0, s73, 0x20000\n"
        "v_mfma_f32_16x16x32_bf16 v[56:59], a[136:139], a[212:215], v[56:59]\n"
        "ds_read_b128 a[212:215], v244 offset:7168\n"
        "s_add_u32 s60, s68, s66\n"
        "s_addc_u32 s61, s69, 0\n"
        "v_mfma_f32_16x16x32_bf16 v[56:59], a[128:131], a[208:211], v[56:59]\n"
        "ds_read_b128 a[208:211], v244 offset:3072\n"
        "s_xor_b32 s66, s17, 5\n"
        "s_lshl_b32 s66, s66, 15\n"
        "v_mfma_f32_16x16x32_bf16 v[32:35], v[160:163], a[0:3], v[32:35]\n"
        "ds_read_b128 a[220:223], v244 offset:15360\n"
        "v_mul_f32 v84, s54, v84\n"
        "v_mfma_f32_16x16x32_bf16 v[32:35], v[168:171], a[4:7], v[32:35]\n"
        "global_load_lds_dwordx4 v254, s[60:61] offset:0 sc1\n"
        "s_waitcnt lgkmcnt(7)\n"
        "v_mfma_f32_16x16x32_bf16 v[32:35], v[176:179], a[8:11], v[32:35]\n"
        "v_mul_f32 v85, s54, v85\n"
        "v_mul_f32 v86, s54, v86\n"
        "v_mfma_f32_16x16x32_bf16 v[32:35], v[184:187], a[12:15], v[32:35]\n"
        "v_mul_f32 v87, s54, v87\n"
        "v_cvt_pk_bf16_f32 v80, 0, v80\n"
        "v_mfma_f32_16x16x32_bf16 v[36:39], v[188:191], a[12:15], v[36:39]\n"
        "global_load_lds_dwordx4 v254, s[60:61] offset:1024 sc1\n"
        "v_mfma_f32_16x16x32_bf16 v[36:39], v[180:183], a[8:11], v[36:39]\n"
        "v_cvt_pk_bf16_f32 v81, 0, v81\n"
        "v_cvt_pk_bf16_f32 v82, 0, v82\n"
        "v_mfma_f32_16x16x32_bf16 v[36:39], v[172:175], a[4:7], v[36:39]\n"
        "v_cvt_pk_bf16_f32 v83, 0, v83\n"
        "v_cvt_pk_bf16_f32 v84, 0, v84\n"
        "v_mfma_f32_16x16x32_bf16 v[36:39], v[164:167], a[0:3], v[36:39]\n"
        "v_cvt_pk_bf16_f32 v85, 0, v85\n"
        "v_cvt_pk_bf16_f32 v86, 0, v86\n"
        "v_mfma_f32_16x16x32_bf16 v[44:47], v[164:167], a[16:19], v[44:47]\n"
        "global_load_lds_dwordx4 v254, s[60:61] offset:2048 sc1\n"
        "v_cvt_pk_bf16_f32 v87, 0, v87\n"
        "v_mfma_f32_16x16x32_bf16 v[44:47], v[172:175], a[20:23], v[44:47]\n"
        "v_add_f32 v80, v80, v136\n"
        "v_add_f32 v81, v81, v137\n"
        "v_mfma_f32_16x16x32_bf16 v[44:47], v[180:183], a[24:27], v[44:47]\n"
        "v_add_f32 v82, v82, v138\n"
        "v_add_f32 v83, v83, v139\n"
        "v_mfma_f32_16x16x32_bf16 v[44:47], v[188:191], a[28:31], v[44:47]\n"
        "global_load_lds_dwordx4 v254, s[60:61] offset:3072 sc1\n"
        "s_add_u32 m0, s73, 0x24000\n"
        "v_mfma_f32_16x16x32_bf16 v[40:43], v[184:187], a[28:31], v[40:43]\n"
        "s_add_u32 s60, s68, s66\n"
        "s_addc_u32 s61, s69, 0\n"
        "s_xor_b32 s66, s17, 6\n"
        "v_mfma_f32_16x16x32_bf16 v[40:43], v[176:179], a[24:27], v[40:43]\n"
        "s_lshl_b32 s66, s66, 15\n"
        "v_add_f32 v84, v84, v140\n"
        "v_add_f32 v85, v85, v141\n"
        "v_mfma_f32_16x16x32_bf16 v[40:43], v[168:171], a[20:23], v[40:43]\n"
        "s_waitcnt lgkmcnt(0)\n"
        "v_add_f32 v86, v86, v142\n"
        "v_add_f32 v87, v87, v143\n"
        "v_mfma_f32_16x16x32_bf16 v[40:43], v[160:163], a[16:19], v[40:43]\n"
        "s_waitcnt vmcnt(25)\n"
        "v_mfma_f32_16x16x32_bf16 v[48:51], v[160:163], a[192:195], v[48:51]\n"
        "s_barrier\n"
        "v_mfma_f32_16x16x32_bf16 v[48:51], v[168:171], a[196:199], v[48:51]\n"
        "ds_read_b64_tr_b16 a[48:49], v155 offset:16384\n"
        "global_load_lds_dwordx4 v254, s[60:61] offset:0 sc1\n"
        "v_mfma_f32_16x16x32_bf16 v[48:51], v[176:179], a[200:203], v[48:51]\n"
        "LHB0_%=:\n"
        "s_cbranch_execz LHRB_%=\n"
        "ds_read_b64_tr_b16 a[50:51], v155 offset:20480\n"
        "v_mfma_f32_16x16x32_bf16 v[48:51], v[184:187], a[204:207], v[48:51]\n"
        "ds_read_b64_tr_b16 a[52:53], v156 offset:16384\n"
        "v_mfma_f32_16x16x32_bf16 v[52:55], v[188:191], a[204:207], v[52:55]\n"
        "ds_read_b64_tr_b16 a[54:55], v156 offset:20480\n"
        "v_mfma_f32_16x16x32_bf16 v[52:55], v[180:183], a[200:203], v[52:55]\n"
        "ds_read_b64_tr_b16 a[56:57], v155 offset:24576\n"
        "global_load_lds_dwordx4 v254, s[60:61] offset:1024 sc1\n"
        "v_mfma_f32_16x16x32_bf16 v[52:55], v[172:175], a[196:199], v[52:55]\n"
        "ds_read_b64_tr_b16 a[58:59], v155 offset:28672\n"
        "v_mfma_f32_16x16x32_bf16 v[52:55], v[164:167], a[192:195], v[52:55]\n"
        "ds_read_b64_tr_b16 a[60:61], v156 offset:24576\n"
        "v_mfma_f32_16x16x32_bf16 v[60:63], v[164:167], a[208:211], v[60:63]\n"
        "ds_read_b64_tr_b16 a[62:63], v156 offset:28672\n"
        "v_mfma_f32_16x16x32_bf16 v[60:63], v[172:175], a[212:215], v[60:63]\n"
        "ds_read_b128 a[0:3], v254 offset:0\n"
        "global_load_lds_dwordx4 v254, s[60:61] offset:2048 sc1\n"
        "v_mfma_f32_16x16x32_bf16 v[60:63], v[180:183], a[216:219], v[60:63]\n"
        "ds_read_b128 a[4:7], v254 offset:4096\n"
        "v_mfma_f32_16x16x32_bf16 v[60:63], v[188:191], a[220:223], v[60:63]\n"
        "ds_read_b128 a[8:11], v254 offset:8192\n"
        "v_mfma_f32_16x16x32_bf16 v[56:59], v[184:187], a[220:223], v[56:59]\n"
        "ds_read_b128 a[12:15], v254 offset:12288\n"
        "v_mfma_f32_16x16x32_bf16 v[56:59], v[176:179], a[216:219], v[56:59]\n"
        "ds_read_b128 a[16:19], v254 offset:1024\n"
        "global_load_lds_dwordx4 v254, s[60:61] offset:3072 sc1\n"
        "s_add_u32 m0, s73, 0x10000\n"
        "v_mfma_f32_16x16x32_bf16 v[56:59], v[168:171], a[212:215], v[56:59]\n"
        "ds_read_b128 a[20:23], v254 offset:5120\n"
        "s_add_u32 s60, s68, s66\n"
        "s_addc_u32 s61, s69, 0\n"
        "v_mfma_f32_16x16x32_bf16 v[56:59], v[160:163], a[208:211], v[56:59]\n"
        "ds_read_b128 a[24:27], v254 offset:9216\n"
        "ds_read_b128 a[28:31], v254 offset:13312\n"
        "ds_read_b128 a[192:195], v254 offset:2048\n"
        "s_xor_b32 s66, s17, 7\n"
        "s_lshl_b32 s66, s66, 15\n"
        "s_waitcnt lgkmcnt(8)\n"
        "v_mfma_f32_16x16x32_bf16 v[32:35], v[192:195], a[0:3], v[32:35]\n"
        "ds_read_b128 a[196:199], v254 offset:6144\n"
        "s_waitcnt lgkmcnt(8)\n"
        "v_mfma_f32_16x16x32_bf16 v[32:35], v[200:203], a[4:7], v[32:35]\n"
        "ds_read_b128 a[200:203], v254 offset:10240\n"
        "s_waitcnt lgkmcnt(8)\n"
        "v_mfma_f32_16x16x32_bf16 v[32:35], v[208:211], a[8:11], v[32:35]\n"
        "ds_read_b128 a[204:207], v254 offset:14336\n"
        "s_waitcnt lgkmcnt(8)\n"
        "v_mfma_f32_16x16x32_bf16 v[32:35], v[216:219], a[12:15], v[32:35]\n"
        "ds_read_b128 a[208:211], v254 offset:3072\n"
        "v_mfma_f32_16x16x32_bf16 v[36:39], v[220:223], a[12:15], v[36:39]\n"
        "ds_read_b128 a[212:215], v254 offset:7168\n"
        "s_waitcnt lgkmcnt(9)\n"
        "v_mfma_f32_16x16x32_bf16 v[36:39], v[212:215], a[8:11], v[36:39]\n"
        "ds_read_b128 a[216:219], v254 offset:11264\n"
        "s_waitcnt lgkmcnt(9)\n"
        "v_mfma_f32_16x16x32_bf16 v[36:39], v[204:207], a[4:7], v[36:39]\n"
        "ds_read_b128 a[220:223], v254 offset:15360\n"
        "s_waitcnt lgkmcnt(8)\n"
        "v_mfma_f32_16x16x32_bf16 v[36:39], v[196:199], a[0:3], v[36:39]\n"
        "global_store_dwordx4 v229, v[92:95], s[24:25] offset:1024 sc1\n"
        "v_mfma_f32_16x16x32_bf16 v[44:47], v[196:199], a[16:19], v[44:47]\n"
        "v_mfma_f32_16x16x32_bf16 v[44:47], v[204:207], a[20:23], v[44:47]\n"
        "v_cvt_pk_bf16_f32 v92, v80, v81\n"
        "v_cvt_pk_bf16_f32 v93, v82, v83\n"
        "v_mfma_f32_16x16x32_bf16 v[44:47], v[212:215], a[24:27], v[44:47]\n"
        "v_cvt_pk_bf16_f32 v94, v84, v85\n"
        "v_cvt_pk_bf16_f32 v95, v86, v87\n"
        "v_mfma_f32_16x16x32_bf16 v[44:47], v[220:223], a[28:31], v[44:47]\n"
        "v_permlane16_swap_b32 v92, v94\n"
        "v_mfma_f32_16x16x32_bf16 v[40:43], v[216:219], a[28:31], v[40:43]\n"
        "s_waitcnt lgkmcnt(2)\n"
        "v_permlane16_swap_b32 v93, v95\n"
        "v_mfma_f32_16x16x32_bf16 v[40:43], v[208:211], a[24:27], v[40:43]\n"
        "global_store_dwordx4 v229, v[88:91], s[24:25] offset:2048 sc1\n"
        "v_mfma_f32_16x16x32_bf16 v[40:43], v[200:203], a[20:23], v[40:43]\n"
        "v_mfma_f32_16x16x32_bf16 v[40:43], v[192:195], a[16:19], v[40:43]\n"
        "s_waitcnt vmcnt(27)\n"
        "v_mfma_f32_16x16x32_bf16 v[48:51], v[192:195], a[192:195], v[48:51]\n"
        "s_barrier\n"
        "v_mfma_f32_16x16x32_bf16 v[48:51], v[200:203], a[196:199], v[48:51]\n"
        "ds_read_b128 a[0:3], v254 offset:16384\n"
        "global_load_lds_dwordx4 v254, s[60:61] offset:0 sc1\n"
        "v_mfma_f32_16x16x32_bf16 v[48:51], v[208:211], a[200:203], v[48:51]\n"
        "ds_read_b128 a[4:7], v254 offset:20480\n"
        "v_mfma_f32_16x16x32_bf16 v[48:51], v[216:219], a[204:207], v[48:51]\n"
        "ds_read_b128 a[8:11], v254 offset:24576\n"
        "v_mfma_f32_16x16x32_bf16 v[52:55], v[220:223], a[204:207], v[52:55]\n"
        "ds_read_b128 a[12:15], v254 offset:28672\n"
        "v_mfma_f32_16x16x32_bf16 v[52:55], v[212:215], a[200:203], v[52:55]\n"
        "ds_read_b128 a[16:19], v254 offset:17408\n"
        "global_load_lds_dwordx4 v254, s[60:61] offset:1024 sc1\n"
        "v_mfma_f32_16x16x32_bf16 v[52:55], v[204:207], a[196:199], v[52:55]\n"
        "ds_read_b128 a[20:23], v254 offset:21504\n"
        "s_waitcnt lgkmcnt(6)\n"
        "v_mfma_f32_16x16x32_bf16 v[52:55], v[196:199], a[192:195], v[52:55]\n"
        "ds_read_b128 a[24:27], v254 offset:25600\n"
        "v_mfma_f32_16x16x32_bf16 v[60:63], v[196:199], a[208:211], v[60:63]\n"
        "ds_read_b128 a[28:31], v254 offset:29696\n"
        "v_mfma_f32_16x16x32_bf16 v[60:63], v[204:207], a[212:215], v[60:63]\n"
        "ds_read_b128 a[192:195], v254 offset:18432\n"
        "global_load_lds_dwordx4 v254, s[60:61] offset:2048 sc1\n"
        "v_mfma_f32_16x16x32_bf16 v[60:63], v[212:215], a[216:219], v[60:63]\n"
        "ds_read_b128 a[196:199], v254 offset:22528\n"
        "v_mfma_f32_16x16x32_bf16 v[60:63], v[220:223], a[220:223], v[60:63]\n"
        "ds_read_b128 a[200:203], v254 offset:26624\n"
        "v_mfma_f32_16x16x32_bf16 v[56:59], v[216:219], a[220:223], v[56:59]\n"
        "ds_read_b128 a[204:207], v254 offset:30720\n"
        "s_waitcnt lgkmcnt(8)\n"
        "v_mfma_f32_16x16x32_bf16 v[56:59], v[208:211], a[216:219], v[56:59]\n"
        "ds_read_b128 a[216:219], v254 offset:27648\n"
        "global_load_lds_dwordx4 v254, s[60:61] offset:3072 sc1\n"
        "s_add_u32 m0, s73, 0x14000\n"
        "v_mfma_f32_16x16x32_bf16 v[56:59], v[200:203], a[212:215], v[56:59]\n"
        "ds_read_b128 a[212:215], v254 offset:23552\n"
        "s_add_u32 s60, s68, s66\n"
        "s_addc_u32 s61, s69, 0\n"
        "v_mfma_f32_16x16x32_bf16 v[56:59], v[192:195], a[208:211], v[56:59]\n"
        "ds_read_b128 a[208:211], v254 offset:19456\n"
        "s_sub_u32 s66, 15, s12\n"
        "v_mfma_f32_16x16x32_bf16 v[0:3], v[112:115], a[0:3], v[0:3]\n"
        "ds_read_b128 a[220:223], v254 offset:31744\n"
        "s_cmp_lt_u32 s76, 2\n"
        "s_cselect_b32 s66, s12, s66\n"
        "v_mfma_f32_16x16x32_bf16 v[0:3], v[120:123], a[4:7], v[0:3]\n"
        "global_load_lds_dwordx4 v254, s[60:61] offset:0 sc1\n"
        "s_mul_i32 s66, s66, 1052672\n"
        "s_add_u32 s66, s66, s67\n"
        "v_mfma_f32_16x16x32_bf16 v[0:3], a[64:67], a[8:11], v[0:3]\n"
        "s_add_u32 s68, s20, s66\n"
        "s_addc_u32 s69, s21, 0\n"
        "s_xor_b32 s66, s17, 0\n"
        "v_mfma_f32_16x16x32_bf16 v[0:3], a[72:75], a[12:15], v[0:3]\n"
        "s_lshl_b32 s66, s66, 15\n"
        "v_mfma_f32_16x16x32_bf16 v[4:7], a[76:79], a[12:15], v[4:7]\n"
        "global_load_lds_dwordx4 v254, s[60:61] offset:1024 sc1\n"
        "v_mfma_f32_16x16x32_bf16 v[4:7], a[68:71], a[8:11], v[4:7]\n"
        "s_waitcnt lgkmcnt(3)\n"
        "v_mfma_f32_16x16x32_bf16 v[4:7], v[124:127], a[4:7], v[4:7]\n"
        "v_mfma_f32_16x16x32_bf16 v[4:7], v[116:119], a[0:3], v[4:7]\n"
        "v_mfma_f32_16x16x32_bf16 v[12:15], v[116:119], a[16:19], v[12:15]\n"
        "global_load_lds_dwordx4 v254, s[60:61] offset:2048 sc1\n"
        "v_mfma_f32_16x16x32_bf16 v[12:15], v[124:127], a[20:23], v[12:15]\n"
        "v_mfma_f32_16x16x32_bf16 v[12:15], a[68:71], a[24:27], v[12:15]\n"
        "v_mfma_f32_16x16x32_bf16 v[12:15], a[76:79], a[28:31], v[12:15]\n"
        "global_load_lds_dwordx4 v254, s[60:61] offset:3072 sc1\n"
        "s_add_u32 m0, s73, 0x0\n"
        "v_mfma_f32_16x16x32_bf16 v[8:11], a[72:75], a[28:31], v[8:11]\n"
        "s_add_u32 s60, s68, s66\n"
        "s_addc_u32 s61, s69, 0\n"
        "s_xor_b32 s66, s17, 1\n"
        "v_mfma_f32_16x16x32_bf16 v[8:11], a[64:67], a[24:27], v[8:11]\n"
        "s_lshl_b32 s66, s66, 15\n"
        "s_lshl2_add_u32 s15, s75, s10\n"
        "s_mul_i32 s16, s13, 263168\n"
        "v_mfma_f32_16x16x32_bf16 v[8:11], v[120:123], a[20:23], v[8:11]\n"
        "s_lshl_b32 s15, s15, 14\n"
        "s_add_u32 s16, s16, s15\n"
        "s_add_u32 s16, s16, s73\n"
        "v_mfma_f32_16x16x32_bf16 v[8:11], v[112:115], a[16:19], v[8:11]\n"
        "s_waitcnt vmcnt(27)\n"
        "s_add_u32 s16, s16, 0x4000000\n"
        "v_mfma_f32_16x16x32_bf16 v[16:19], v[112:115], a[192:195], v[16:19]\n"
        "s_barrier\n"
        "v_mfma_f32_16x16x32_bf16 v[16:19], v[120:123], a[196:199], v[16:19]\n"
        "ds_read_b64_tr_b16 a[224:225], v153 offset:32768\n"
        "global_load_lds_dwordx4 v254, s[60:61] offset:0 sc1\n"
        "v_mfma_f32_16x16x32_bf16 v[16:19], a[64:67], a[200:203], v[16:19]\n"
        "ds_read_b64_tr_b16 a[226:227], v153 offset:36864\n"
        "v_mfma_f32_16x16x32_bf16 v[16:19], a[72:75], a[204:207], v[16:19]\n"
        "ds_read_b64_tr_b16 a[228:229], v154 offset:32768\n"
        "v_mfma_f32_16x16x32_bf16 v[20:23], a[76:79], a[204:207], v[20:23]\n"
        "ds_read_b64_tr_b16 a[230:231], v154 offset:36864\n"
        "s_waitcnt lgkmcnt(4)\n"
        "v_mfma_f32_16x16x32_bf16 v[20:23], a[68:71], a[200:203], v[20:23]\n"
        "ds_read_b64_tr_b16 a[232:233], v153 offset:40960\n"
        "global_load_lds_dwordx4 v254, s[60:61] offset:1024 sc1\n"
        "v_mfma_f32_16x16x32_bf16 v[20:23], v[124:127], a[196:199], v[20:23]\n"
        "ds_read_b64_tr_b16 a[234:235], v153 offset:45056\n"
        "v_mfma_f32_16x16x32_bf16 v[20:23], v[116:119], a[192:195], v[20:23]\n"
        "ds_read_b64_tr_b16 a[236:237], v154 offset:40960\n"
        "v_mfma_f32_16x16x32_bf16 v[28:31], v[116:119], a[208:211], v[28:31]\n"
        "ds_read_b64_tr_b16 a[238:239], v154 offset:45056\n"
        "v_mfma_f32_16x16x32_bf16 v[28:31], v[124:127], a[212:215], v[28:31]\n"
        "ds_read_b128 a[0:3], v254 offset:49152\n"
        "global_load_lds_dwordx4 v254, s[60:61] offset:2048 sc1\n"
        "v_mfma_f32_16x16x32_bf16 v[28:31], a[68:71], a[216:219], v[28:31]\n"
        "ds_read_b128 a[4:7], v254 offset:53248\n"
        "v_mfma_f32_16x16x32_bf16 v[28:31], a[76:79], a[220:223], v[28:31]\n"
        "ds_read_b128 a[8:11], v254 offset:57344\n"
        "v_mfma_f32_16x16x32_bf16 v[24:27], a[72:75], a[220:223], v[24:27]\n"
        "ds_read_b128 a[12:15], v254 offset:61440\n"
        "v_mfma_f32_16x16x32_bf16 v[24:27], a[64:67], a[216:219], v[24:27]\n"
        "ds_read_b128 a[16:19], v254 offset:50176\n"
        "global_load_lds_dwordx4 v254, s[60:61] offset:3072 sc1\n"
        "s_add_u32 m0, s73, 0x4000\n"
        "v_mfma_f32_16x16x32_bf16 v[24:27], v[120:123], a[212:215], v[24:27]\n"
        "ds_read_b128 a[20:23], v254 offset:54272\n"
        "s_add_u32 s60, s68, s66\n"
        "s_addc_u32 s61, s69, 0\n"
        "v_mfma_f32_16x16x32_bf16 v[24:27], v[112:115], a[208:211], v[24:27]\n"
        "ds_read_b128 a[24:27], v254 offset:58368\n"
        "ds_read_b128 a[28:31], v254 offset:62464\n"
        "ds_read_b128 a[192:195], v254 offset:51200\n"
        "s_xor_b32 s66, s17, 2\n"
        "s_lshl_b32 s66, s66, 15\n"
        "s_waitcnt lgkmcnt(8)\n"
        "v_mfma_f32_16x16x32_bf16 v[0:3], a[80:83], a[0:3], v[0:3]\n"
        "ds_read_b128 a[196:199], v254 offset:55296\n"
        "s_waitcnt lgkmcnt(8)\n"
        "v_mfma_f32_16x16x32_bf16 v[0:3], a[88:91], a[4:7], v[0:3]\n"
        "ds_read_b128 a[200:203], v254 offset:59392\n"
        "s_waitcnt lgkmcnt(8)\n"
        "v_mfma_f32_16x16x32_bf16 v[0:3], a[160:163], a[8:11], v[0:3]\n"
        "ds_read_b128 a[204:207], v254 offset:63488\n"
        "s_waitcnt lgkmcnt(8)\n"
        "v_mfma_f32_16x16x32_bf16 v[0:3], a[168:171], a[12:15], v[0:3]\n"
        "ds_read_b128 a[208:211], v254 offset:52224\n"
        "v_mfma_f32_16x16x32_bf16 v[4:7], a[172:175], a[12:15], v[4:7]\n"
        "ds_read_b128 a[212:215], v254 offset:56320\n"
        "s_waitcnt lgkmcnt(9)\n"
        "v_mfma_f32_16x16x32_bf16 v[4:7], a[164:167], a[8:11], v[4:7]\n"
        "ds_read_b128 a[216:219], v254 offset:60416\n"
        "s_waitcnt lgkmcnt(9)\n"
        "v_mfma_f32_16x16x32_bf16 v[4:7], a[92:95], a[4:7], v[4:7]\n"
        "ds_read_b128 a[220:223], v254 offset:64512\n"
        "s_waitcnt lgkmcnt(8)\n"
        "v_mfma_f32_16x16x32_bf16 v[4:7], a[84:87], a[0:3], v[4:7]\n"
        "global_store_dwordx4 v229, v[92:95], s[24:25] offset:3072 sc1\n"
        "s_and_b32 s67, s10, 1\n"
        "s_lshl_b32 s67, s67, 12\n"
        "v_mfma_f32_16x16x32_bf16 v[12:15], a[84:87], a[16:19], v[12:15]\n"
        "v_mfma_f32_16x16x32_bf16 v[12:15], a[92:95], a[20:23], v[12:15]\n"
        "v_mfma_f32_16x16x32_bf16 v[12:15], a[164:167], a[24:27], v[12:15]\n"
        "v_mfma_f32_16x16x32_bf16 v[12:15], a[172:175], a[28:31], v[12:15]\n"
        "v_mfma_f32_16x16x32_bf16 v[8:11], a[168:171], a[28:31], v[8:11]\n"
        "s_waitcnt lgkmcnt(2)\n"
        "v_mfma_f32_16x16x32_bf16 v[8:11], a[160:163], a[24:27], v[8:11]\n"
        "v_mfma_f32_16x16x32_bf16 v[8:11], a[88:91], a[20:23], v[8:11]\n"
        "v_mfma_f32_16x16x32_bf16 v[8:11], a[80:83], a[16:19], v[8:11]\n"
        "s_waitcnt vmcnt(28)\n"
        "v_mfma_f32_16x16x32_bf16 v[16:19], a[80:83], a[192:195], v[16:19]\n"
        "s_barrier\n"
        "v_mfma_f32_16x16x32_bf16 v[16:19], a[88:91], a[196:199], v[16:19]\n"
        "ds_read_b128 a[0:3], v244 offset:32768\n"
        "global_load_lds_dwordx4 v254, s[60:61] offset:0 sc1\n"
        "v_mfma_f32_16x16x32_bf16 v[16:19], a[160:163], a[200:203], v[16:19]\n"
        "ds_read_b128 a[4:7], v244 offset:36864\n"
        "v_mfma_f32_16x16x32_bf16 v[16:19], a[168:171], a[204:207], v[16:19]\n"
        "ds_read_b128 a[8:11], v244 offset:40960\n"
        "v_mfma_f32_16x16x32_bf16 v[20:23], a[172:175], a[204:207], v[20:23]\n"
        "ds_read_b128 a[12:15], v244 offset:45056\n"
        "v_mfma_f32_16x16x32_bf16 v[20:23], a[164:167], a[200:203], v[20:23]\n"
        "ds_read_b128 a[16:19], v244 offset:33792\n"
        "global_load_lds_dwordx4 v254, s[60:61] offset:1024 sc1\n"
        "v_mfma_f32_16x16x32_bf16 v[20:23], a[92:95], a[196:199], v[20:23]\n"
        "ds_read_b128 a[20:23], v244 offset:37888\n"
        "s_waitcnt lgkmcnt(6)\n"
        "v_mfma_f32_16x16x32_bf16 v[20:23], a[84:87], a[192:195], v[20:23]\n"
        "ds_read_b128 a[24:27], v244 offset:41984\n"
        "v_mfma_f32_16x16x32_bf16 v[28:31], a[84:87], a[208:211], v[28:31]\n"
        "ds_read_b128 a[28:31], v244 offset:46080\n"
        "v_mfma_f32_16x16x32_bf16 v[28:31], a[92:95], a[212:215], v[28:31]\n"
        "ds_read_b128 a[192:195], v244 offset:34816\n"
        "global_load_lds_dwordx4 v254, s[60:61] offset:2048 sc1\n"
        "v_mfma_f32_16x16x32_bf16 v[28:31], a[164:167], a[216:219], v[28:31]\n"
        "ds_read_b128 a[196:199], v244 offset:38912\n"
        "v_mfma_f32_16x16x32_bf16 v[28:31], a[172:175], a[220:223], v[28:31]\n"
        "ds_read_b128 a[200:203], v244 offset:43008\n"
        "v_mfma_f32_16x16x32_bf16 v[24:27], a[168:171], a[220:223], v[24:27]\n"
        "ds_read_b128 a[204:207], v244 offset:47104\n"
        "s_waitcnt lgkmcnt(8)\n"
        "v_mfma_f32_16x16x32_bf16 v[24:27], a[160:163], a[216:219], v[24:27]\n"
        "ds_read_b128 a[216:219], v244 offset:44032\n"
        "global_load_lds_dwordx4 v254, s[60:61] offset:3072 sc1\n"
        "s_add_u32 m0, s73, 0x8000\n"
        "v_mfma_f32_16x16x32_bf16 v[24:27], a[88:91], a[212:215], v[24:27]\n"
        "ds_read_b128 a[212:215], v244 offset:39936\n"
        "s_add_u32 s60, s68, s66\n"
        "s_addc_u32 s61, s69, 0\n"
        "v_mfma_f32_16x16x32_bf16 v[24:27], a[80:83], a[208:211], v[24:27]\n"
        "ds_read_b128 a[208:211], v244 offset:35840\n"
        "s_xor_b32 s66, s17, 3\n"
        "s_lshl_b32 s66, s66, 15\n"
        "v_mfma_f32_16x16x32_bf16 v[0:3], a[176:179], a[0:3], v[0:3]\n"
        "ds_read_b128 a[220:223], v244 offset:48128\n"
        "v_mfma_f32_16x16x32_bf16 v[0:3], a[184:187], a[4:7], v[0:3]\n"
        "global_load_lds_dwordx4 v254, s[60:61] offset:0 sc1\n"
        "v_mfma_f32_16x16x32_bf16 v[0:3], a[32:35], a[8:11], v[0:3]\n"
        "v_mfma_f32_16x16x32_bf16 v[0:3], a[40:43], a[12:15], v[0:3]\n"
        "v_mfma_f32_16x16x32_bf16 v[4:7], a[44:47], a[12:15], v[4:7]\n"
        "global_load_lds_dwordx4 v254, s[60:61] offset:1024 sc1\n"
        "v_mfma_f32_16x16x32_bf16 v[4:7], a[36:39], a[8:11], v[4:7]\n"
        "s_waitcnt lgkmcnt(3)\n"
        "v_mfma_f32_16x16x32_bf16 v[4:7], a[188:191], a[4:7], v[4:7]\n"
        "v_mfma_f32_16x16x32_bf16 v[4:7], a[180:183], a[0:3], v[4:7]\n"
        "v_mfma_f32_16x16x32_bf16 v[12:15], a[180:183], a[16:19], v[12:15]\n"
        "global_load_lds_dwordx4 v254, s[60:61] offset:2048 sc1\n"
        "v_mfma_f32_16x16x32_bf16 v[12:15], a[188:191], a[20:23], v[12:15]\n"
        "v_mfma_f32_16x16x32_bf16 v[12:15], a[36:39], a[24:27], v[12:15]\n"
        "v_mfma_f32_16x16x32_bf16 v[12:15], a[44:47], a[28:31], v[12:15]\n"
        "global_load_lds_dwordx4 v254, s[60:61] offset:3072 sc1\n"
        "s_add_u32 m0, s73, 0xc000\n"
        "v_mfma_f32_16x16x32_bf16 v[8:11], a[40:43], a[28:31], v[8:11]\n"
        "s_add_u32 s60, s68, s66\n"
        "s_addc_u32 s61, s69, 0\n"
        "s_xor_b32 s66, s17, 4\n"
        "v_mfma_f32_16x16x32_bf16 v[8:11], a[32:35], a[24:27], v[8:11]\n"
        "s_lshl_b32 s66, s66, 15\n"
        "v_mfma_f32_16x16x32_bf16 v[8:11], a[184:187], a[20:23], v[8:11]\n"
        "v_mfma_f32_16x16x32_bf16 v[8:11], a[176:179], a[16:19], v[8:11]\n"
        "s_waitcnt vmcnt(31)\n"
        "v_mfma_f32_16x16x32_bf16 v[16:19], a[176:179], a[192:195], v[16:19]\n"
        "s_barrier\n"
        "v_mfma_f32_16x16x32_bf16 v[16:19], a[184:187], a[196:199], v[16:19]\n"
        "ds_read_b128 a[0:3], v244 offset:49152\n"
        "global_load_lds_dwordx4 v254, s[60:61] offset:0 sc1\n"
        "v_mfma_f32_16x16x32_bf16 v[16:19], a[32:35], a[200:203], v[16:19]\n"
        "ds_read_b128 a[4:7], v244 offset:53248\n"
        "v_mfma_f32_16x16x32_bf16 v[16:19], a[40:43], a[204:207], v[16:19]\n"
        "ds_read_b128 a[8:11], v244 offset:57344\n"
        "v_mfma_f32_16x16x32_bf16 v[20:23], a[44:47], a[204:207], v[20:23]\n"
        "ds_read_b128 a[12:15], v244 offset:61440\n"
        "v_mfma_f32_16x16x32_bf16 v[20:23], a[36:39], a[200:203], v[20:23]\n"
        "ds_read_b128 a[28:31], v244 offset:62464\n"
        "global_load_lds_dwordx4 v254, s[60:61] offset:1024 sc1\n"
        "v_mfma_f32_16x16x32_bf16 v[20:23], a[188:191], a[196:199], v[20:23]\n"
        "ds_read_b128 a[24:27], v244 offset:58368\n"
        "v_mfma_f32_16x16x32_bf16 v[20:23], a[180:183], a[192:195], v[20:23]\n"
        "ds_read_b128 a[20:23], v244 offset:54272\n"
        "s_waitcnt lgkmcnt(7)\n"
        "v_mfma_f32_16x16x32_bf16 v[28:31], a[180:183], a[208:211], v[28:31]\n"
        "ds_read_b128 a[16:19], v244 offset:50176\n"
        "v_mfma_f32_16x16x32_bf16 v[28:31], a[188:191], a[212:215], v[28:31]\n"
        "ds_read_b128 a[192:195], v244 offset:51200\n"
        "s_waitcnt lgkmcnt(8)\n"
        "v_mfma_f32_16x16x32_bf16 v[0:3], a[48:51], a[0:3], v[0:3]\n"
        "ds_read_b128 a[196:199], v244 offset:55296\n"
        "s_waitcnt lgkmcnt(8)\n"
        "v_mfma_f32_16x16x32_bf16 v[0:3], a[56:59], a[4:7], v[0:3]\n"
        "ds_read_b128 a[200:203], v244 offset:59392\n"
        "s_waitcnt lgkmcnt(8)\n"
        "v_mfma_f32_16x16x32_bf16 v[0:3], a[224:227], a[8:11], v[0:3]\n"
        "ds_read_b128 a[204:207], v244 offset:63488\n"
        "s_waitcnt lgkmcnt(8)\n"
        "v_mfma_f32_16x16x32_bf16 v[0:3], a[232:235], a[12:15], v[0:3]\n"
        "global_load_lds_dwordx4 v254, s[60:61] offset:2048 sc1\n"
        "v_mfma_f32_16x16x32_bf16 v[4:7], a[236:239], a[12:15], v[4:7]\n"
        "v_mfma_f32_16x16x32_bf16 v[4:7], a[228:231], a[8:11], v[4:7]\n"
        "v_mfma_f32_16x16x32_bf16 v[4:7], a[60:63], a[4:7], v[4:7]\n"
        "global_load_lds_dwordx4 v254, s[60:61] offset:3072 sc1\n"
        "s_add_u32 m0, s73, 0x18000\n"
        "v_mfma_f32_16x16x32_bf16 v[4:7], a[52:55], a[0:3], v[4:7]\n"
        "s_add_u32 s60, s68, s66\n"
        "s_addc_u32 s61, s69, 0\n"
        "v_cvt_pk_bf16_f32 v0, v0, v1\n"
        "v_mfma_f32_16x16x32_bf16 v[28:31], a[36:39], a[216:219], v[28:31]\n"
        "s_waitcnt lgkmcnt(2)\n"
        "v_cvt_pk_bf16_f32 v1, v2, v3\n"
        "s_xor_b32 s66, s17, 5\n"
        "v_mfma_f32_16x16x32_bf16 v[28:31], a[44:47], a[220:223], v[28:31]\n"
        "s_lshl_b32 s66, s66, 15\n"
        "v_cvt_pk_bf16_f32 v2, v4, v5\n"
        "v_cvt_pk_bf16_f32 v3, v6, v7\n"
        "v_mfma_f32_16x16x32_bf16 v[8:11], a[232:235], a[28:31], v[8:11]\n"
        "global_store_dwordx4 v254, v[0:3], s[28:29] offset:0 sc1\n"
        "v_mfma_f32_16x16x32_bf16 v[8:11], a[224:227], a[24:27], v[8:11]\n"
        "v_mfma_f32_16x16x32_bf16 v[8:11], a[56:59], a[20:23], v[8:11]\n"
        "v_mfma_f32_16x16x32_bf16 v[8:11], a[48:51], a[16:19], v[8:11]\n"
        "s_waitcnt vmcnt(13)\n"
        "v_mfma_f32_16x16x32_bf16 v[12:15], a[52:55], a[16:19], v[12:15]\n"
        "s_barrier\n"
        "v_mfma_f32_16x16x32_bf16 v[12:15], a[60:63], a[20:23], v[12:15]\n"
        "ds_read_b128 a[0:3], v245 offset:0\n"
        "v_mfma_f32_16x16x32_bf16 v[12:15], a[228:231], a[24:27], v[12:15]\n"
        "ds_read_b128 a[4:7], v245 offset:4096\n"
        "global_load_lds_dwordx4 v254, s[60:61] offset:0 sc1\n"
        "v_mfma_f32_16x16x32_bf16 v[12:15], a[236:239], a[28:31], v[12:15]\n"
        "ds_read_b128 a[8:11], v245 offset:8192\n"
        "v_cvt_pk_bf16_f32 v8, v8, v9\n"
        "v_mfma_f32_16x16x32_bf16 v[24:27], a[40:43], a[220:223], v[24:27]\n"
        "ds_read_b128 a[220:223], v244 offset:64512\n"
        "v_cvt_pk_bf16_f32 v9, v10, v11\n"
        "v_mfma_f32_16x16x32_bf16 v[24:27], a[32:35], a[216:219], v[24:27]\n"
        "ds_read_b128 a[216:219], v244 offset:60416\n"
        "v_mfma_f32_16x16x32_bf16 v[24:27], a[184:187], a[212:215], v[24:27]\n"
        "ds_read_b128 a[212:215], v244 offset:56320\n"
        "v_cvt_pk_bf16_f32 v10, v12, v13\n"
        "v_mfma_f32_16x16x32_bf16 v[24:27], a[176:179], a[208:211], v[24:27]\n"
        "ds_read_b128 a[208:211], v244 offset:52224\n"
        "v_cvt_pk_bf16_f32 v11, v14, v15\n"
        "v_mfma_f32_16x16x32_bf16 v[16:19], a[48:51], a[192:195], v[16:19]\n"
        "global_store_dwordx4 v254, v[8:11], s[28:29] offset:1024 sc1\n"
        "ds_read_b128 a[12:15], v245 offset:12288\n"
        "ds_read_b128 a[16:19], v245 offset:1024\n"
        "s_waitcnt lgkmcnt(8)\n"
        "v_mfma_f32_16x16x32_bf16 v[16:19], a[56:59], a[196:199], v[16:19]\n"
        "ds_read_b128 a[20:23], v245 offset:5120\n"
        "v_mfma_f32_16x16x32_bf16 v[16:19], a[224:227], a[200:203], v[16:19]\n"
        "ds_read_b128 a[24:27], v245 offset:9216\n"
        "v_mfma_f32_16x16x32_bf16 v[16:19], a[232:235], a[204:207], v[16:19]\n"
        "ds_read_b128 a[28:31], v245 offset:13312\n"
        "v_mfma_f32_16x16x32_bf16 v[20:23], a[236:239], a[204:207], v[20:23]\n"
        "ds_read_b128 a[204:207], v245 offset:14336\n"
        "s_waitcnt lgkmcnt(8)\n"
        "v_mfma_f32_16x16x32_bf16 v[20:23], a[228:231], a[200:203], v[20:23]\n"
        "ds_read_b128 a[200:203], v245 offset:10240\n"
        "v_mfma_f32_16x16x32_bf16 v[20:23], a[60:63], a[196:199], v[20:23]\n"
        "ds_read_b128 a[196:199], v245 offset:6144\n"
        "v_cvt_pk_bf16_f32 v16, v16, v17\n"
        "v_mfma_f32_16x16x32_bf16 v[20:23], a[52:55], a[192:195], v[20:23]\n"
        "ds_read_b128 a[192:195], v245 offset:2048\n"
        "v_cvt_pk_bf16_f32 v17, v18, v19\n"
        "v_mfma_f32_16x16x32_bf16 v[24:27], a[232:235], a[220:223], v[24:27]\n"
        "global_load_lds_dwordx4 v254, s[60:61] offset:1024 sc1\n"
        "s_waitcnt lgkmcnt(7)\n"
        "v_mfma_f32_16x16x32_bf16 v[24:27], a[224:227], a[216:219], v[24:27]\n"
        "v_mfma_f32_16x16x32_bf16 v[24:27], a[56:59], a[212:215], v[24:27]\n"
        "v_mfma_f32_16x16x32_bf16 v[24:27], a[48:51], a[208:211], v[24:27]\n"
        "v_cvt_pk_bf16_f32 v18, v20, v21\n"
        "v_cvt_pk_bf16_f32 v19, v22, v23\n"
        "v_mfma_f32_16x16x32_bf16 v[28:31], a[52:55], a[208:211], v[28:31]\n"
        "global_store_dwordx4 v254, v[16:19], s[28:29] offset:2048 sc1\n"
        "ds_read_b128 a[208:211], v245 offset:3072\n"
        "v_lshlrev_b32 v159, 2, v232\n"
        "v_add_u32 v159, 0x0, v159\n"
        "v_mfma_f32_16x16x32_bf16 v[28:31], a[60:63], a[212:215], v[28:31]\n"
        "ds_read_b128 a[212:215], v245 offset:7168\n"
        "v_cvt_pk_bf16_f32 v24, v24, v25\n"
        "v_mfma_f32_16x16x32_bf16 v[28:31], a[228:231], a[216:219], v[28:31]\n"
        "ds_read_b128 a[216:219], v245 offset:11264\n"
        "v_cvt_pk_bf16_f32 v25, v26, v27\n"
        "v_mfma_f32_16x16x32_bf16 v[28:31], a[236:239], a[220:223], v[28:31]\n"
        "ds_read_b128 a[220:223], v245 offset:15360\n"
        "v_mfma_f32_16x16x32_bf16 v[32:35], v[112:115], a[0:3], v[32:35]\n"
        "v_mfma_f32_16x16x32_bf16 v[32:35], v[120:123], a[4:7], v[32:35]\n"
        "v_mfma_f32_16x16x32_bf16 v[32:35], a[64:67], a[8:11], v[32:35]\n"
        "global_load_lds_dwordx4 v254, s[60:61] offset:2048 sc1\n"
        "v_mfma_f32_16x16x32_bf16 v[32:35], a[72:75], a[12:15], v[32:35]\n"
        "v_mfma_f32_16x16x32_bf16 v[36:39], a[76:79], a[12:15], v[36:39]\n"
        "v_mfma_f32_16x16x32_bf16 v[36:39], a[68:71], a[8:11], v[36:39]\n"
        "s_waitcnt lgkmcnt(2)\n"
        "v_cvt_pk_bf16_f32 v26, v28, v29\n"
        "v_cvt_pk_bf16_f32 v27, v30, v31\n"
        "v_mfma_f32_16x16x32_bf16 v[36:39], v[124:127], a[4:7], v[36:39]\n"
        "global_store_dwordx4 v254, v[24:27], s[28:29] offset:3072 sc1\n"
        "s_add_u32 s28, s38, s16\n"
        "s_addc_u32 s29, s39, 0\n"
        "v_mfma_f32_16x16x32_bf16 v[36:39], v[116:119], a[0:3], v[36:39]\n"
        "v_mfma_f32_16x16x32_bf16 v[44:47], v[116:119], a[16:19], v[44:47]\n"
        "v_mfma_f32_16x16x32_bf16 v[44:47], v[124:127], a[20:23], v[44:47]\n"
        "v_mfma_f32_16x16x32_bf16 v[44:47], a[68:71], a[24:27], v[44:47]\n"
        "v_mfma_f32_16x16x32_bf16 v[44:47], a[76:79], a[28:31], v[44:47]\n"
        "v_mfma_f32_16x16x32_bf16 v[40:43], a[72:75], a[28:31], v[40:43]\n"
        "global_load_lds_dwordx4 v254, s[60:61] offset:3072 sc1\n"
        "s_add_u32 m0, s73, 0x1c000\n"
        "v_mfma_f32_16x16x32_bf16 v[40:43], a[64:67], a[24:27], v[40:43]\n"
        "s_add_u32 s60, s68, s66\n"
        "s_addc_u32 s61, s69, 0\n"
        "s_xor_b32 s66, s17, 6\n"
        "v_mfma_f32_16x16x32_bf16 v[40:43], v[120:123], a[20:23], v[40:43]\n"
        "s_lshl_b32 s66, s66, 15\n"
        "v_and_b32 v150, 1, v233\n"
        "v_lshlrev_b32 v151, 11, v232\n"
        "v_mfma_f32_16x16x32_bf16 v[40:43], v[112:115], a[16:19], v[40:43]\n"
        "v_lshl_add_u32 v151, v150, 5, v151\n"
        "v_lshrrev_b32 v150, 1, v233\n"
        "v_mfma_f32_16x16x32_bf16 v[48:51], v[112:115], a[192:195], v[48:51]\n"
        "s_barrier\n"
        "v_mfma_f32_16x16x32_bf16 v[48:51], v[120:123], a[196:199], v[48:51]\n"
        "ds_read_b128 a[0:3], v245 offset:16384\n"
        "s_lshl_b32 s16, s63, 12\n"
        "v_lshl_add_u32 v151, v150, 4, v151\n"
        "v_mfma_f32_16x16x32_bf16 v[48:51], a[64:67], a[200:203], v[48:51]\n"
        "ds_read_b128 a[4:7], v245 offset:20480\n"
        "s_lshl1_add_u32 s16, s62, s16\n"
        "s_add_u32 s16, s16, 0x3700000\n"
        "v_mfma_f32_16x16x32_bf16 v[48:51], a[72:75], a[204:207], v[48:51]\n"
        "ds_read_b128 a[8:11], v245 offset:24576\n"
        "global_load_lds_dwordx4 v254, s[60:61] offset:0 sc1\n"
        "v_mfma_f32_16x16x32_bf16 v[52:55], a[76:79], a[204:207], v[52:55]\n"
        "ds_read_b128 a[12:15], v245 offset:28672\n"
        "s_add_u32 s58, s38, s16\n"
        "s_addc_u32 s59, s39, 0\n"
        "v_mfma_f32_16x16x32_bf16 v[52:55], a[68:71], a[200:203], v[52:55]\n"
        "ds_read_b128 a[16:19], v245 offset:17408\n"
        "s_add_u32 s16, s73, 0x4000\n"
        "v_lshrrev_b32 v148, 7, v254\n"
        "v_mfma_f32_16x16x32_bf16 v[52:55], v[124:127], a[196:199], v[52:55]\n"
        "ds_read_b128 a[20:23], v245 offset:21504\n"
        "v_lshlrev_b32 v148, 12, v148\n"
        "v_mfma_f32_16x16x32_bf16 v[52:55], v[116:119], a[192:195], v[52:55]\n"
        "ds_read_b128 a[24:27], v245 offset:25600\n"
        "v_and_b32 v150, 0x70, v254\n"
        "v_mfma_f32_16x16x32_bf16 v[60:63], v[116:119], a[208:211], v[60:63]\n"
        "ds_read_b128 a[28:31], v245 offset:29696\n"
        "v_add_u32 v148, v148, v150\n"
        "v_mfma_f32_16x16x32_bf16 v[60:63], v[124:127], a[212:215], v[60:63]\n"
        "ds_read_b128 a[192:195], v245 offset:18432\n"
        "s_waitcnt lgkmcnt(8)\n"
        "v_mfma_f32_16x16x32_bf16 v[60:63], a[68:71], a[216:219], v[60:63]\n"
        "ds_read_b128 a[196:199], v245 offset:22528\n"
        "global_load_lds_dwordx4 v254, s[60:61] offset:1024 sc1\n"
        "v_mfma_f32_16x16x32_bf16 v[60:63], a[76:79], a[220:223], v[60:63]\n"
        "ds_read_b128 a[200:203], v245 offset:26624\n"
        "v_lshrrev_b32 v149, 6, v254\n"
        "v_mfma_f32_16x16x32_bf16 v[56:59], a[72:75], a[220:223], v[56:59]\n"
        "ds_read_b128 a[204:207], v245 offset:30720\n"
        "v_lshlrev_b32 v149, 7, v149\n"
        "v_mfma_f32_16x16x32_bf16 v[56:59], a[64:67], a[216:219], v[56:59]\n"
        "ds_read_b128 a[216:219], v245 offset:27648\n"
        "v_bfe_u32 v150, v254, 4, 2\n"
        "v_mfma_f32_16x16x32_bf16 v[56:59], v[120:123], a[212:215], v[56:59]\n"
        "ds_read_b128 a[212:215], v245 offset:23552\n"
        "v_lshl_add_u32 v149, v150, 3, v149\n"
        "v_mfma_f32_16x16x32_bf16 v[56:59], v[112:115], a[208:211], v[56:59]\n"
        "ds_read_b128 a[208:211], v245 offset:19456\n"
        "v_mov_b32 v238, 0\n"
        "v_mfma_f32_16x16x32_bf16 v[32:35], a[80:83], a[0:3], v[32:35]\n"
        "ds_read_b128 a[220:223], v245 offset:31744\n"
        "s_waitcnt lgkmcnt(8)\n"
        "v_mfma_f32_16x16x32_bf16 v[32:35], a[88:91], a[4:7], v[32:35]\n"
        "global_load_lds_dwordx4 v254, s[60:61] offset:2048 sc1\n"
        "v_mfma_f32_16x16x32_bf16 v[32:35], a[160:163], a[8:11], v[32:35]\n"
        "v_mov_b32 v239, 0\n"
        "v_mov_b32 v242, 0\n"
        "v_mfma_f32_16x16x32_bf16 v[32:35], a[168:171], a[12:15], v[32:35]\n"
        "v_mov_b32 v243, 0\n"
        "v_and_b32 v246, 1, v232\n"
        "v_mfma_f32_16x16x32_bf16 v[36:39], a[172:175], a[12:15], v[36:39]\n"
        "v_lshlrev_b32 v246, 4, v246\n"
        "v_mov_b32 v247, 0x3f80\n"
        "v_mfma_f32_16x16x32_bf16 v[36:39], a[164:167], a[8:11], v[36:39]\n"
        "v_lshlrev_b32 v246, v246, v247\n"
        "v_lshrrev_b32 v247, 1, v232\n"
        "v_mfma_f32_16x16x32_bf16 v[36:39], a[92:95], a[4:7], v[36:39]\n"
        "v_lshlrev_b32 v248, 1, v233\n"
        "v_or_b32 v249, 1, v248\n"
        "v_mfma_f32_16x16x32_bf16 v[36:39], a[84:87], a[0:3], v[36:39]\n"
        "v_add_u32 v149, s16, v149\n"
        "v_mfma_f32_16x16x32_bf16 v[44:47], a[84:87], a[16:19], v[44:47]\n"
        "global_load_lds_dwordx4 v254, s[60:61] offset:3072 sc1\n"
        "s_add_u32 m0, s73, 0x20000\n"
        "v_mfma_f32_16x16x32_bf16 v[44:47], a[92:95], a[20:23], v[44:47]\n"
        "s_add_u32 s60, s68, s66\n"
        "s_addc_u32 s61, s69, 0\n"
        "s_xor_b32 s66, s17, 7\n"
        "v_mfma_f32_16x16x32_bf16 v[44:47], a[164:167], a[24:27], v[44:47]\n"
        "s_lshl_b32 s66, s66, 15\n"
        "v_mfma_f32_16x16x32_bf16 v[44:47], a[172:175], a[28:31], v[44:47]\n"
        "v_mfma_f32_16x16x32_bf16 v[40:43], a[168:171], a[28:31], v[40:43]\n"
        "s_waitcnt lgkmcnt(0)\n"
        "v_mfma_f32_16x16x32_bf16 v[40:43], a[160:163], a[24:27], v[40:43]\n"
        "v_mfma_f32_16x16x32_bf16 v[40:43], a[88:91], a[20:23], v[40:43]\n"
        "v_mfma_f32_16x16x32_bf16 v[40:43], a[80:83], a[16:19], v[40:43]\n"
        "v_mfma_f32_16x16x32_bf16 v[48:51], a[80:83], a[192:195], v[48:51]\n"
        "s_cmp_lg_u32 s40, 0\n"
        "s_cbranch_scc1 LW2_%=\n"
        "s_mov_b64 exec, 0xffff\n"
        "global_store_dwordx2 v234, v[250:251], s[48:49] sc1\n"
        "s_mov_b64 exec, -1\n"
        "LW2_%=:\n"
        "v_mfma_f32_16x16x32_bf16 v[48:51], a[88:91], a[196:199], v[48:51]\n"
        "s_barrier\n"
        "v_mfma_f32_16x16x32_bf16 v[48:51], a[160:163], a[200:203], v[48:51]\n"
        "ds_read_b128 a[0:3], v244 offset:0\n"
        "global_load_lds_dwordx4 v254, s[60:61] offset:0 sc1\n"
        "v_mfma_f32_16x16x32_bf16 v[48:51], a[168:171], a[204:207], v[48:51]\n"
        "ds_read_b128 a[4:7], v244 offset:4096\n"
        "v_bfe_u32 v234, v254, 4, 3\n"
        "v_mfma_f32_16x16x32_bf16 v[52:55], a[172:175], a[204:207], v[52:55]\n"
        "ds_read_b128 a[8:11], v244 offset:8192\n"
        "v_lshl_add_u32 v234, v234, 7, s14\n"
        "v_mfma_f32_16x16x32_bf16 v[52:55], a[164:167], a[200:203], v[52:55]\n"
        "ds_read_b128 a[12:15], v244 offset:12288\n"
        "s_lshl_b32 s14, s40, 6\n"
        "v_add_u32 v225, s14, v159\n"
        "v_mfma_f32_16x16x32_bf16 v[52:55], a[92:95], a[196:199], v[52:55]\n"
        "LHB1_%=:\n"
        "s_cbranch_execz LHRB_%=\n"
        "ds_read_b128 a[16:19], v244 offset:1024\n"
        "v_cmp_eq_u32 s[14:15], v247, v248\n"
        "v_mfma_f32_16x16x32_bf16 v[52:55], a[84:87], a[192:195], v[52:55]\n"
        "ds_read_b128 a[20:23], v244 offset:5120\n"
        "v_cndmask_b32 v240, 0, v246, s[14:15]\n"
        "v_mfma_f32_16x16x32_bf16 v[60:63], a[84:87], a[208:211], v[60:63]\n"
        "ds_read_b128 a[24:27], v244 offset:9216\n"
        "v_cmp_eq_u32 s[14:15], v247, v249\n"
        "v_mfma_f32_16x16x32_bf16 v[60:63], a[92:95], a[212:215], v[60:63]\n"
        "ds_read_b128 a[28:31], v244 offset:13312\n"
        "global_load_lds_dwordx4 v254, s[60:61] offset:1024 sc1\n"
        "v_mfma_f32_16x16x32_bf16 v[60:63], a[164:167], a[216:219], v[60:63]\n"
        "ds_read_b128 a[192:195], v244 offset:2048\n"
        "v_cndmask_b32 v241, 0, v246, s[14:15]\n"
        "v_mfma_f32_16x16x32_bf16 v[60:63], a[172:175], a[220:223], v[60:63]\n"
        "ds_read_b128 a[196:199], v244 offset:6144\n"
        "v_mfma_f32_16x16x32_bf16 v[56:59], a[168:171], a[220:223], v[56:59]\n"
        "ds_read_b128 a[200:203], v244 offset:10240\n"
        "s_waitcnt lgkmcnt(8)\n"
        "v_mfma_f32_16x16x32_bf16 v[56:59], a[160:163], a[216:219], v[56:59]\n"
        "ds_read_b128 a[204:207], v244 offset:14336\n"
        "v_mfma_f32_16x16x32_bf16 v[56:59], a[88:91], a[212:215], v[56:59]\n"
        "ds_read_b128 a[212:215], v244 offset:7168\n"
        "v_mfma_f32_16x16x32_bf16 v[56:59], a[80:83], a[208:211], v[56:59]\n"
        "ds_read_b128 a[208:211], v244 offset:3072\n"
        "s_waitcnt lgkmcnt(8)\n"
        "v_mfma_f32_16x16x32_bf16 v[32:35], a[176:179], a[0:3], v[32:35]\n"
        "ds_read_b128 a[216:219], v244 offset:11264\n"
        "global_load_lds_dwordx4 v254, s[60:61] offset:2048 sc1\n"
        "v_mfma_f32_16x16x32_bf16 v[32:35], a[184:187], a[4:7], v[32:35]\n"
        "ds_read_b128 a[220:223], v244 offset:15360\n"
        "v_mfma_f32_16x16x32_bf16 v[32:35], a[32:35], a[8:11], v[32:35]\n"
        "v_mfma_f32_16x16x32_bf16 v[32:35], a[40:43], a[12:15], v[32:35]\n"
        "v_mfma_f32_16x16x32_bf16 v[36:39], a[44:47], a[12:15], v[36:39]\n"
        "v_mfma_f32_16x16x32_bf16 v[36:39], a[36:39], a[8:11], v[36:39]\n"
        "v_mfma_f32_16x16x32_bf16 v[36:39], a[188:191], a[4:7], v[36:39]\n"
        "s_waitcnt lgkmcnt(3)\n"
        "v_mfma_f32_16x16x32_bf16 v[36:39], a[180:183], a[0:3], v[36:39]\n"
        "global_load_lds_dwordx4 v254, s[60:61] offset:3072 sc1\n"
        "s_add_u32 m0, s73, 0x24000\n"
        "v_mfma_f32_16x16x32_bf16 v[44:47], a[180:183], a[16:19], v[44:47]\n"
        "s_add_u32 s60, s68, s66\n"
        "s_addc_u32 s61, s69, 0\n"
        "s_sub_u32 s66, 15, s12\n"
        "v_mfma_f32_16x16x32_bf16 v[44:47], a[188:191], a[20:23], v[44:47]\n"
        "s_cmp_lt_u32 s10, 2\n"
        "s_cselect_b32 s66, s12, s66\n"
        "s_mul_i32 s66, s66, 1052672\n"
        "v_mfma_f32_16x16x32_bf16 v[44:47], a[36:39], a[24:27], v[44:47]\n"
        "s_add_u32 s66, s66, s67\n"
        "s_add_u32 s68, s20, s66\n"
        "s_addc_u32 s69, s21, 0\n"
        "v_mfma_f32_16x16x32_bf16 v[44:47], a[44:47], a[28:31], v[44:47]\n"
        "s_xor_b32 s66, s17, 0\n"
        "s_lshl_b32 s66, s66, 15\n"
        "v_mfma_f32_16x16x32_bf16 v[40:43], a[40:43], a[28:31], v[40:43]\n"
        "v_mfma_f32_16x16x32_bf16 v[40:43], a[32:35], a[24:27], v[40:43]\n"
        "v_mfma_f32_16x16x32_bf16 v[40:43], a[184:187], a[20:23], v[40:43]\n"
        "v_mfma_f32_16x16x32_bf16 v[40:43], a[176:179], a[16:19], v[40:43]\n"
        "v_mfma_f32_16x16x32_bf16 v[48:51], a[176:179], a[192:195], v[48:51]\n"
        "s_barrier\n"
        "v_mfma_f32_16x16x32_bf16 v[48:51], a[184:187], a[196:199], v[48:51]\n"
        "ds_read_b128 a[0:3], v244 offset:16384\n"
        "global_load_lds_dwordx4 v254, s[60:61] offset:0 sc1\n"
        "v_mfma_f32_16x16x32_bf16 v[48:51], a[32:35], a[200:203], v[48:51]\n"
        "ds_read_b128 a[4:7], v244 offset:20480\n"
        "v_mfma_f32_16x16x32_bf16 v[48:51], a[40:43], a[204:207], v[48:51]\n"
        "ds_read_b128 a[8:11], v244 offset:24576\n"
        "v_mfma_f32_16x16x32_bf16 v[52:55], a[44:47], a[204:207], v[52:55]\n"
        "ds_read_b128 a[12:15], v244 offset:28672\n"
        "v_mfma_f32_16x16x32_bf16 v[52:55], a[36:39], a[200:203], v[52:55]\n"
        "ds_read_b128 a[28:31], v244 offset:29696\n"
        "v_mfma_f32_16x16x32_bf16 v[52:55], a[188:191], a[196:199], v[52:55]\n"
        "ds_read_b128 a[24:27], v244 offset:25600\n"
        "v_mfma_f32_16x16x32_bf16 v[52:55], a[180:183], a[192:195], v[52:55]\n"
        "ds_read_b128 a[20:23], v244 offset:21504\n"
        "s_waitcnt lgkmcnt(7)\n"
        "v_mfma_f32_16x16x32_bf16 v[60:63], a[180:183], a[208:211], v[60:63]\n"
        "ds_read_b128 a[16:19], v244 offset:17408\n"
        "global_load_lds_dwordx4 v254, s[60:61] offset:1024 sc1\n"
        "v_mfma_f32_16x16x32_bf16 v[60:63], a[188:191], a[212:215], v[60:63]\n"
        "ds_read_b128 a[192:195], v244 offset:18432\n"
        "s_waitcnt lgkmcnt(8)\n"
        "v_mfma_f32_16x16x32_bf16 v[32:35], a[48:51], a[0:3], v[32:35]\n"
        "ds_read_b128 a[196:199], v244 offset:22528\n"
        "s_waitcnt lgkmcnt(8)\n"
        "v_mfma_f32_16x16x32_bf16 v[32:35], a[56:59], a[4:7], v[32:35]\n"
        "ds_read_b128 a[200:203], v244 offset:26624\n"
        "s_waitcnt lgkmcnt(8)\n"
        "v_mfma_f32_16x16x32_bf16 v[32:35], a[224:227], a[8:11], v[32:35]\n"
        "ds_read_b128 a[204:207], v244 offset:30720\n"
        "s_waitcnt lgkmcnt(8)\n"
        "v_mfma_f32_16x16x32_bf16 v[32:35], a[232:235], a[12:15], v[32:35]\n"
        "v_mfma_f32_16x16x32_bf16 v[36:39], a[236:239], a[12:15], v[36:39]\n"
        "v_mfma_f32_16x16x32_bf16 v[36:39], a[228:231], a[8:11], v[36:39]\n"
        "global_load_lds_dwordx4 v254, s[60:61] offset:2048 sc1\n"
        "v_mfma_f32_16x16x32_bf16 v[36:39], a[60:63], a[4:7], v[36:39]\n"
        "v_mfma_f32_16x16x32_bf16 v[36:39], a[52:55], a[0:3], v[36:39]\n"
        "s_waitcnt lgkmcnt(3)\n"
        "v_mfma_f32_16x16x32_bf16 v[60:63], a[36:39], a[216:219], v[60:63]\n"
        "v_mfma_f32_16x16x32_bf16 v[60:63], a[44:47], a[220:223], v[60:63]\n"
        "v_cvt_pk_bf16_f32 v32, v32, v33\n"
        "v_cvt_pk_bf16_f32 v33, v34, v35\n"
        "v_mfma_f32_16x16x32_bf16 v[40:43], a[232:235], a[28:31], v[40:43]\n"
        "v_mfma_f32_16x16x32_bf16 v[40:43], a[224:227], a[24:27], v[40:43]\n"
        "v_mfma_f32_16x16x32_bf16 v[40:43], a[56:59], a[20:23], v[40:43]\n"
        "v_cvt_pk_bf16_f32 v34, v36, v37\n"
        "v_cvt_pk_bf16_f32 v35, v38, v39\n"
        "v_mfma_f32_16x16x32_bf16 v[40:43], a[48:51], a[16:19], v[40:43]\n"
        "global_store_dwordx4 v254, v[32:35], s[28:29] offset:0 sc1\n"
        "s_barrier\n"
        "v_mfma_f32_16x16x32_bf16 v[44:47], a[52:55], a[16:19], v[44:47]\n"
        "ds_read_b128 a[0:3], v254 offset:0\n"
        "v_mfma_f32_16x16x32_bf16 v[44:47], a[60:63], a[20:23], v[44:47]\n"
        "ds_read_b128 a[4:7], v254 offset:4096\n"
        "v_mfma_f32_16x16x32_bf16 v[44:47], a[228:231], a[24:27], v[44:47]\n"
        "ds_read_b128 a[8:11], v254 offset:8192\n"
        "v_cvt_pk_bf16_f32 v40, v40, v41\n"
        "v_mfma_f32_16x16x32_bf16 v[44:47], a[236:239], a[28:31], v[44:47]\n"
        "ds_read_b128 a[12:15], v254 offset:12288\n"
        "v_cvt_pk_bf16_f32 v41, v42, v43\n"
        "v_mfma_f32_16x16x32_bf16 v[56:59], a[40:43], a[220:223], v[56:59]\n"
        "ds_read_b128 a[220:223], v244 offset:31744\n"
        "v_mfma_f32_16x16x32_bf16 v[56:59], a[32:35], a[216:219], v[56:59]\n"
        "ds_read_b128 a[216:219], v244 offset:27648\n"
        "global_load_lds_dwordx4 v254, s[60:61] offset:3072 sc1\n"
        "s_add_u32 m0, s73, 0x10000\n"
        "v_mfma_f32_16x16x32_bf16 v[56:59], a[184:187], a[212:215], v[56:59]\n"
        "ds_read_b128 a[212:215], v244 offset:23552\n"
        "v_cvt_pk_bf16_f32 v42, v44, v45\n"
        "v_mfma_f32_16x16x32_bf16 v[56:59], a[176:179], a[208:211], v[56:59]\n"
        "ds_read_b128 a[208:211], v244 offset:19456\n"
        "v_cvt_pk_bf16_f32 v43, v46, v47\n"
        "v_mfma_f32_16x16x32_bf16 v[48:51], a[48:51], a[192:195], v[48:51]\n"
        "ds_read_b128 a[16:19], v254 offset:1024\n"
        "s_waitcnt lgkmcnt(8)\n"
        "v_mfma_f32_16x16x32_bf16 v[48:51], a[56:59], a[196:199], v[48:51]\n"
        "global_store_dwordx4 v254, v[40:43], s[28:29] offset:1024 sc1\n"
        "ds_read_b128 a[20:23], v254 offset:5120\n"
        "s_add_u32 s60, s68, s66\n"
        "s_addc_u32 s61, s69, 0\n"
        "ds_read_b128 a[24:27], v254 offset:9216\n"
        "s_xor_b32 s66, s17, 1\n"
        "v_mfma_f32_16x16x32_bf16 v[48:51], a[224:227], a[200:203], v[48:51]\n"
        "ds_read_b128 a[28:31], v254 offset:13312\n"
        "s_lshl_b32 s66, s66, 15\n"
        "v_mfma_f32_16x16x32_bf16 v[48:51], a[232:235], a[204:207], v[48:51]\n"
        "v_mfma_f32_16x16x32_bf16 v[52:55], a[236:239], a[204:207], v[52:55]\n"
        "ds_read_b128 a[204:207], v254 offset:14336\n"
        "s_waitcnt lgkmcnt(7)\n"
        "v_mfma_f32_16x16x32_bf16 v[52:55], a[228:231], a[200:203], v[52:55]\n"
        "ds_read_b128 a[200:203], v254 offset:10240\n"
        "v_mfma_f32_16x16x32_bf16 v[52:55], a[60:63], a[196:199], v[52:55]\n"
        "ds_read_b128 a[196:199], v254 offset:6144\n"
        "s_waitcnt lgkmcnt(7)\n"
        "v_mfma_f32_16x16x32_bf16 v[52:55], a[52:55], a[192:195], v[52:55]\n"
        "ds_read_b128 a[192:195], v254 offset:2048\n"
        "v_cvt_pk_bf16_f32 v48, v48, v49\n"
        "v_mfma_f32_16x16x32_bf16 v[56:59], a[232:235], a[220:223], v[56:59]\n"
        "global_load_lds_dwordx4 v254, s[60:61] offset:0 sc1\n"
        "v_cvt_pk_bf16_f32 v49, v50, v51\n"
        "v_mfma_f32_16x16x32_bf16 v[56:59], a[224:227], a[216:219], v[56:59]\n"
        "v_mfma_f32_16x16x32_bf16 v[56:59], a[56:59], a[212:215], v[56:59]\n"
        "v_mfma_f32_16x16x32_bf16 v[56:59], a[48:51], a[208:211], v[56:59]\n"
        "v_cvt_pk_bf16_f32 v50, v52, v53\n"
        "v_cvt_pk_bf16_f32 v51, v54, v55\n"
        "v_mfma_f32_16x16x32_bf16 v[60:63], a[52:55], a[208:211], v[60:63]\n"
        "global_store_dwordx4 v254, v[48:51], s[28:29] offset:2048 sc1\n"
        "ds_read_b128 a[208:211], v254 offset:3072\n"
        "v_mfma_f32_16x16x32_bf16 v[60:63], a[60:63], a[212:215], v[60:63]\n"
        "ds_read_b128 a[212:215], v254 offset:7168\n"
        "v_mfma_f32_16x16x32_bf16 v[60:63], a[228:231], a[216:219], v[60:63]\n"
        "ds_read_b128 a[216:219], v254 offset:11264\n"
        "v_cvt_pk_bf16_f32 v56, v56, v57\n"
        "v_mfma_f32_16x16x32_bf16 v[60:63], a[236:239], a[220:223], v[60:63]\n"
        "ds_read_b128 a[220:223], v254 offset:15360\n"
        "v_cvt_pk_bf16_f32 v57, v58, v59\n"
        "v_mfma_f32_16x16x32_bf16 v[0:3], a[96:99], a[0:3], 0\n"
        "v_mfma_f32_16x16x32_bf16 v[0:3], a[104:107], a[4:7], v[0:3]\n"
        "v_mfma_f32_16x16x32_bf16 v[0:3], a[112:115], a[8:11], v[0:3]\n"
        "global_load_dwordx2 v[236:237], v234, s[48:49] sc1\n"
        "v_mfma_f32_16x16x32_bf16 v[0:3], a[120:123], a[12:15], v[0:3]\n"
        "global_load_lds_dwordx4 v254, s[60:61] offset:1024 sc1\n"
        "v_cvt_pk_bf16_f32 v58, v60, v61\n"
        "v_mfma_f32_16x16x32_bf16 v[4:7], a[124:127], a[12:15], 0\n"
        "s_waitcnt lgkmcnt(3)\n"
        "v_cvt_pk_bf16_f32 v59, v62, v63\n"
        "v_mfma_f32_16x16x32_bf16 v[4:7], a[116:119], a[8:11], v[4:7]\n"
        "v_mfma_f32_16x16x32_bf16 v[4:7], a[108:111], a[4:7], v[4:7]\n"
        "v_mfma_f32_16x16x32_bf16 v[4:7], a[100:103], a[0:3], v[4:7]\n"
        "global_store_dwordx4 v254, v[56:59], s[28:29] offset:3072 sc1\n"
        "v_mfma_f32_16x16x32_bf16 v[12:15], a[100:103], a[16:19], 0\n"
        "v_mfma_f32_16x16x32_bf16 v[12:15], a[108:111], a[20:23], v[12:15]\n"
        "v_mfma_f32_16x16x32_bf16 v[12:15], a[116:119], a[24:27], v[12:15]\n"
        "v_mfma_f32_16x16x32_bf16 v[12:15], a[124:127], a[28:31], v[12:15]\n"
        "v_mfma_f32_16x16x32_bf16 v[8:11], a[120:123], a[28:31], 0\n"
        "v_mfma_f32_16x16x32_bf16 v[8:11], a[112:115], a[24:27], v[8:11]\n"
        "global_load_lds_dwordx4 v254, s[60:61] offset:2048 sc1\n"
        "v_mfma_f32_16x16x32_bf16 v[8:11], a[104:107], a[20:23], v[8:11]\n"
        "v_mfma_f32_16x16x32_bf16 v[8:11], a[96:99], a[16:19], v[8:11]\n"
        "s_waitcnt vmcnt(36)\n"
        "v_mfma_f32_16x16x32_bf16 v[16:19], a[96:99], a[192:195], 0\n"
        "s_barrier\n"
        "v_mfma_f32_16x16x32_bf16 v[16:19], a[104:107], a[196:199], v[16:19]\n"
        "ds_read_b128 a[0:3], v254 offset:16384\n"
        "v_mfma_f32_16x16x32_bf16 v[16:19], a[112:115], a[200:203], v[16:19]\n"
        "ds_read_b128 a[4:7], v254 offset:20480\n"
        "v_mfma_f32_16x16x32_bf16 v[16:19], a[120:123], a[204:207], v[16:19]\n"
        "ds_read_b128 a[8:11], v254 offset:24576\n"
        "v_mfma_f32_16x16x32_bf16 v[20:23], a[124:127], a[204:207], 0\n"
        "ds_read_b128 a[12:15], v254 offset:28672\n"
        "global_load_lds_dwordx4 v254, s[60:61] offset:3072 sc1\n"
        "s_add_u32 m0, s73, 0x14000\n"
        "v_mfma_f32_16x16x32_bf16 v[20:23], a[116:119], a[200:203], v[20:23]\n"
        "ds_read_b128 a[16:19], v254 offset:17408\n"
        "s_add_u32 s60, s68, s66\n"
        "s_addc_u32 s61, s69, 0\n"
        "v_mfma_f32_16x16x32_bf16 v[20:23], a[108:111], a[196:199], v[20:23]\n"
        "ds_read_b128 a[20:23], v254 offset:21504\n"
        "s_xor_b32 s66, s17, 2\n"
        "s_lshl_b32 s66, s66, 15\n"
        "v_mfma_f32_16x16x32_bf16 v[20:23], a[100:103], a[192:195], v[20:23]\n"
        "ds_read_b128 a[24:27], v254 offset:25600\n"
        "s_lshl2_add_u32 s15, s76, s10\n"
        "s_mul_i32 s16, s13, 263168\n"
        "v_mfma_f32_16x16x32_bf16 v[28:31], a[100:103], a[208:211], 0\n"
        "ds_read_b128 a[28:31], v254 offset:29696\n"
        "s_lshl_b32 s15, s15, 14\n"
        "s_waitcnt lgkmcnt(8)\n"
        "v_mfma_f32_16x16x32_bf16 v[28:31], a[108:111], a[212:215], v[28:31]\n"
        "ds_read_b128 a[192:195], v254 offset:18432\n"
        "s_add_u32 s16, s16, s15\n"
        "s_add_u32 s16, s16, s73\n"
        "v_mfma_f32_16x16x32_bf16 v[28:31], a[116:119], a[216:219], v[28:31]\n"
        "ds_read_b128 a[196:199], v254 offset:22528\n"
        "s_add_u32 s16, s16, 0x4000000\n"
        "v_mfma_f32_16x16x32_bf16 v[28:31], a[124:127], a[220:223], v[28:31]\n"
        "ds_read_b128 a[200:203], v254 offset:26624\n"
        "global_load_lds_dwordx4 v254, s[60:61] offset:0 sc1\n"
        "v_mfma_f32_16x16x32_bf16 v[24:27], a[120:123], a[220:223], 0\n"
        "ds_read_b128 a[204:207], v254 offset:30720\n"
        "s_add_u32 s28, s38, s16\n"
        "s_addc_u32 s29, s39, 0\n"
        "v_mfma_f32_16x16x32_bf16 v[24:27], a[112:115], a[216:219], v[24:27]\n"
        "ds_read_b128 a[216:219], v254 offset:27648\n"
        "s_waitcnt lgkmcnt(8)\n"
        "v_mfma_f32_16x16x32_bf16 v[24:27], a[104:107], a[212:215], v[24:27]\n"
        "ds_read_b128 a[212:215], v254 offset:23552\n"
        "v_mfma_f32_16x16x32_bf16 v[24:27], a[96:99], a[208:211], v[24:27]\n"
        "ds_read_b128 a[208:211], v254 offset:19456\n"
        "v_mfma_f32_16x16x32_bf16 v[0:3], a[128:131], a[0:3], v[0:3]\n"
        "ds_read_b128 a[220:223], v254 offset:31744\n"
        "v_mfma_f32_16x16x32_bf16 v[0:3], a[136:139], a[4:7], v[0:3]\n"
        "v_mfma_f32_16x16x32_bf16 v[0:3], a[144:147], a[8:11], v[0:3]\n"
        "global_load_lds_dwordx4 v254, s[60:61] offset:1024 sc1\n"
        "v_mfma_f32_16x16x32_bf16 v[0:3], a[152:155], a[12:15], v[0:3]\n"
        "v_mfma_f32_16x16x32_bf16 v[4:7], a[156:159], a[12:15], v[4:7]\n"
        "v_mfma_f32_16x16x32_bf16 v[4:7], a[148:151], a[8:11], v[4:7]\n"
        "s_waitcnt lgkmcnt(3)\n"
        "v_mfma_f32_16x16x32_bf16 v[4:7], a[140:143], a[4:7], v[4:7]\n"
        "v_mfma_f32_16x16x32_bf16 v[4:7], a[132:135], a[0:3], v[4:7]\n"
        "v_mfma_f32_16x16x32_bf16 v[12:15], a[132:135], a[16:19], v[12:15]\n"
        "v_mfma_f32_16x16x32_bf16 v[12:15], a[140:143], a[20:23], v[12:15]\n"
        "global_load_lds_dwordx4 v254, s[60:61] offset:2048 sc1\n"
        "v_mfma_f32_16x16x32_bf16 v[12:15], a[148:151], a[24:27], v[12:15]\n"
        "v_mfma_f32_16x16x32_bf16 v[12:15], a[156:159], a[28:31], v[12:15]\n"
        "v_mfma_f32_16x16x32_bf16 v[8:11], a[152:155], a[28:31], v[8:11]\n"
        "v_mfma_f32_16x16x32_bf16 v[8:11], a[144:147], a[24:27], v[8:11]\n"
        "v_mfma_f32_16x16x32_bf16 v[8:11], a[136:139], a[20:23], v[8:11]\n"
        "v_mfma_f32_16x16x32_bf16 v[8:11], a[128:131], a[16:19], v[8:11]\n"
        "s_waitcnt vmcnt(36)\n"
        "v_mfma_f32_16x16x32_bf16 v[16:19], a[128:131], a[192:195], v[16:19]\n"
        "s_barrier\n"
        "v_mfma_f32_16x16x32_bf16 v[16:19], a[136:139], a[196:199], v[16:19]\n"
        "ds_write_b32 v225, v224\n"
        "v_mfma_f32_16x16x32_bf16 v[16:19], a[144:147], a[200:203], v[16:19]\n"
        "ds_read_b128 a[0:3], v254 offset:32768\n"
        "global_load_lds_dwordx4 v254, s[60:61] offset:3072 sc1\n"
        "s_add_u32 m0, s73, 0x4000\n"
        "v_mfma_f32_16x16x32_bf16 v[16:19], a[152:155], a[204:207], v[16:19]\n"
        "ds_read_b128 a[4:7], v254 offset:36864\n"
        "s_add_u32 s60, s68, s66\n"
        "s_addc_u32 s61, s69, 0\n"
        "v_mfma_f32_16x16x32_bf16 v[20:23], a[156:159], a[204:207], v[20:23]\n"
        "ds_read_b128 a[8:11], v254 offset:40960\n"
        "s_xor_b32 s66, s17, 3\n"
        "s_lshl_b32 s66, s66, 15\n"
        "v_mfma_f32_16x16x32_bf16 v[20:23], a[148:151], a[200:203], v[20:23]\n"
        "ds_read_b128 a[12:15], v254 offset:45056\n"
        "s_waitcnt lgkmcnt(5)\n"
        "v_mfma_f32_16x16x32_bf16 v[20:23], a[140:143], a[196:199], v[20:23]\n"
        "ds_read_b128 a[16:19], v254 offset:33792\n"
        "v_mfma_f32_16x16x32_bf16 v[20:23], a[132:135], a[192:195], v[20:23]\n"
        "ds_read_b128 a[20:23], v254 offset:37888\n"
        "v_mfma_f32_16x16x32_bf16 v[28:31], a[132:135], a[208:211], v[28:31]\n"
        "ds_read_b128 a[24:27], v254 offset:41984\n"
        "v_mfma_f32_16x16x32_bf16 v[28:31], a[140:143], a[212:215], v[28:31]\n"
        "ds_read_b128 a[28:31], v254 offset:46080\n"
        "v_mfma_f32_16x16x32_bf16 v[28:31], a[148:151], a[216:219], v[28:31]\n"
        "ds_read_b128 a[192:195], v254 offset:34816\n"
        "v_mfma_f32_16x16x32_bf16 v[28:31], a[156:159], a[220:223], v[28:31]\n"
        "ds_read_b128 a[196:199], v254 offset:38912\n"
        "v_mfma_f32_16x16x32_bf16 v[24:27], a[152:155], a[220:223], v[24:27]\n"
        "ds_read_b128 a[200:203], v254 offset:43008\n"
        "s_waitcnt lgkmcnt(8)\n"
        "v_mfma_f32_16x16x32_bf16 v[24:27], a[144:147], a[216:219], v[24:27]\n"
        "ds_read_b128 a[204:207], v254 offset:47104\n"
        "v_mfma_f32_16x16x32_bf16 v[24:27], a[136:139], a[212:215], v[24:27]\n"
        "ds_read_b128 a[212:215], v254 offset:39936\n"
        "v_mfma_f32_16x16x32_bf16 v[24:27], a[128:131], a[208:211], v[24:27]\n"
        "ds_read_b128 a[208:211], v254 offset:35840\n"
        "s_waitcnt lgkmcnt(8)\n"
        "v_mfma_f32_16x16x32_bf16 v[0:3], v[160:163], a[0:3], v[0:3]\n"
        "ds_read_b128 a[216:219], v254 offset:44032\n"
        "v_mfma_f32_16x16x32_bf16 v[0:3], v[168:171], a[4:7], v[0:3]\n"
        "ds_read_b128 a[220:223], v254 offset:48128\n"
        "v_mfma_f32_16x16x32_bf16 v[0:3], v[176:179], a[8:11], v[0:3]\n"
        "v_mfma_f32_16x16x32_bf16 v[0:3], v[184:187], a[12:15], v[0:3]\n"
        "v_mfma_f32_16x16x32_bf16 v[4:7], v[188:191], a[12:15], v[4:7]\n"
        "v_mfma_f32_16x16x32_bf16 v[4:7], v[180:183], a[8:11], v[4:7]\n"
        "v_mfma_f32_16x16x32_bf16 v[4:7], v[172:175], a[4:7], v[4:7]\n"
        "s_waitcnt lgkmcnt(3)\n"
        "v_mfma_f32_16x16x32_bf16 v[4:7], v[164:167], a[0:3], v[4:7]\n"
        "v_mfma_f32_16x16x32_bf16 v[12:15], v[164:167], a[16:19], v[12:15]\n"
        "v_mfma_f32_16x16x32_bf16 v[12:15], v[172:175], a[20:23], v[12:15]\n"
        "v_mfma_f32_16x16x32_bf16 v[12:15], v[180:183], a[24:27], v[12:15]\n"
        "v_mfma_f32_16x16x32_bf16 v[12:15], v[188:191], a[28:31], v[12:15]\n"
        "v_mfma_f32_16x16x32_bf16 v[8:11], v[184:187], a[28:31], v[8:11]\n"
        "v_mfma_f32_16x16x32_bf16 v[8:11], v[176:179], a[24:27], v[8:11]\n"
        "v_mfma_f32_16x16x32_bf16 v[8:11], v[168:171], a[20:23], v[8:11]\n"
        "v_mfma_f32_16x16x32_bf16 v[8:11], v[160:163], a[16:19], v[8:11]\n"
        "s_waitcnt vmcnt(33)\n"
        "v_mfma_f32_16x16x32_bf16 v[16:19], v[160:163], a[192:195], v[16:19]\n"
        "s_barrier\n"
        "v_mfma_f32_16x16x32_bf16 v[16:19], v[168:171], a[196:199], v[16:19]\n"
        "ds_read_b128 a[0:3], v254 offset:49152\n"
        "v_mfma_f32_16x16x32_bf16 v[16:19], v[176:179], a[200:203], v[16:19]\n"
        "ds_read_b128 a[4:7], v254 offset:53248\n"
        "v_mfma_f32_16x16x32_bf16 v[16:19], v[184:187], a[204:207], v[16:19]\n"
        "ds_read_b128 a[8:11], v254 offset:57344\n"
        "v_mfma_f32_16x16x32_bf16 v[20:23], v[188:191], a[204:207], v[20:23]\n"
        "ds_read_b128 a[12:15], v254 offset:61440\n"
        "s_waitcnt lgkmcnt(4)\n"
        "v_mfma_f32_16x16x32_bf16 v[20:23], v[180:183], a[200:203], v[20:23]\n"
        "ds_read_b128 a[16:19], v254 offset:50176\n"
        "v_mfma_f32_16x16x32_bf16 v[20:23], v[172:175], a[196:199], v[20:23]\n"
        "ds_read_b128 a[20:23], v254 offset:54272\n"
        "v_mfma_f32_16x16x32_bf16 v[20:23], v[164:167], a[192:195], v[20:23]\n"
        "ds_read_b128 a[24:27], v254 offset:58368\n"
        "v_mfma_f32_16x16x32_bf16 v[28:31], v[164:167], a[208:211], v[28:31]\n"
        "ds_read_b128 a[28:31], v254 offset:62464\n"
        "v_mfma_f32_16x16x32_bf16 v[28:31], v[172:175], a[212:215], v[28:31]\n"
        "ds_read_b128 a[192:195], v254 offset:51200\n"
        "v_mfma_f32_16x16x32_bf16 v[28:31], v[180:183], a[216:219], v[28:31]\n"
        "ds_read_b128 a[196:199], v254 offset:55296\n"
        "v_mfma_f32_16x16x32_bf16 v[28:31], v[188:191], a[220:223], v[28:31]\n"
        "ds_read_b128 a[200:203], v254 offset:59392\n"
        "v_mfma_f32_16x16x32_bf16 v[24:27], v[184:187], a[220:223], v[24:27]\n"
        "ds_read_b128 a[204:207], v254 offset:63488\n"
        "s_waitcnt lgkmcnt(8)\n"
        "v_mfma_f32_16x16x32_bf16 v[24:27], v[176:179], a[216:219], v[24:27]\n"
        "ds_read_b128 a[216:219], v254 offset:60416\n"
        "v_mfma_f32_16x16x32_bf16 v[24:27], v[168:171], a[212:215], v[24:27]\n"
        "ds_read_b128 a[212:215], v254 offset:56320\n"
        "v_mfma_f32_16x16x32_bf16 v[24:27], v[160:163], a[208:211], v[24:27]\n"
        "ds_read_b128 a[208:211], v254 offset:52224\n"
        "s_waitcnt vmcnt(8) lgkmcnt(8)\n"
        "v_mfma_f32_16x16x32_bf16 v[0:3], v[192:195], a[0:3], v[0:3]\n"
        "ds_read_b128 a[220:223], v254 offset:64512\n"
        "v_cmp_ne_u64 vcc, v[236:237], v[250:251]\n"
        "v_mfma_f32_16x16x32_bf16 v[0:3], v[200:203], a[4:7], v[0:3]\n"
        "s_cbranch_vccnz LPZZ_%=\n"
        "LBZZ_%=:\n"
        "global_load_lds_dwordx4 v148, s[58:59] offset:0 sc1\n"
        "s_add_u32 m0, s73, 0x8000\n"
        "v_mfma_f32_16x16x32_bf16 v[0:3], v[208:211], a[8:11], v[0:3]\n"
        "s_add_u32 s58, s58, 0x8000\n"
        "s_addc_u32 s59, s59, 0\n"
        "v_mfma_f32_16x16x32_bf16 v[0:3], v[216:219], a[12:15], v[0:3]\n"
        "v_mfma_f32_16x16x32_bf16 v[4:7], v[220:223], a[12:15], v[4:7]\n"
        "v_mfma_f32_16x16x32_bf16 v[4:7], v[212:215], a[8:11], v[4:7]\n"
        "v_mfma_f32_16x16x32_bf16 v[4:7], v[204:207], a[4:7], v[4:7]\n"
        "v_mfma_f32_16x16x32_bf16 v[4:7], v[196:199], a[0:3], v[4:7]\n"
        "s_waitcnt lgkmcnt(1)\n"
        "v_mfma_f32_16x16x32_bf16 v[12:15], v[196:199], a[16:19], v[12:15]\n"
        "global_load_dwordx4 v[96:99], v151, s[64:65]\n"
        "s_lshl2_add_u32 s14, s13, s10\n"
        "v_mfma_f32_16x16x32_bf16 v[12:15], v[204:207], a[20:23], v[12:15]\n"
        "s_add_u32 s64, s64, 0x8000\n"
        "s_addc_u32 s65, s65, 0\n"
        "s_lshl2_add_u32 s14, s14, s40\n"
        "v_mfma_f32_16x16x32_bf16 v[12:15], v[212:215], a[24:27], v[12:15]\n"
        "s_add_u32 s14, s14, 3072\n"
        "v_lshrrev_b32 v154, 1, v254\n"
        "s_lshl_b32 s14, s14, 7\n"
        "v_mfma_f32_16x16x32_bf16 v[12:15], v[220:223], a[28:31], v[12:15]\n"
        "v_add_u32 v154, s14, v154\n"
        "v_mfma_f32_16x16x32_bf16 v[8:11], v[216:219], a[28:31], v[8:11]\n"
        "v_mfma_f32_16x16x32_bf16 v[8:11], v[208:211], a[24:27], v[8:11]\n"
        "v_mfma_f32_16x16x32_bf16 v[8:11], v[200:203], a[20:23], v[8:11]\n"
        "v_mfma_f32_16x16x32_bf16 v[8:11], v[192:195], a[16:19], v[8:11]\n"
        "v_mfma_f32_16x16x32_bf16 v[16:19], v[192:195], a[192:195], v[16:19]\n"
        "s_barrier\n"
        "v_mfma_f32_16x16x32_bf16 v[16:19], v[200:203], a[196:199], v[16:19]\n"
        "ds_read_b128 a[0:3], v244 offset:32768\n"
        "global_load_lds_dwordx4 v254, s[60:61] offset:0 sc1\n"
        "v_mfma_f32_16x16x32_bf16 v[16:19], v[208:211], a[200:203], v[16:19]\n"
        "ds_read_b128 a[4:7], v244 offset:36864\n"
        "v_mfma_f32_16x16x32_bf16 v[16:19], v[216:219], a[204:207], v[16:19]\n"
        "ds_read_b128 a[8:11], v244 offset:40960\n"
        "v_mfma_f32_16x16x32_bf16 v[20:23], v[220:223], a[204:207], v[20:23]\n"
        "ds_read_b128 a[12:15], v244 offset:45056\n"
        "v_mfma_f32_16x16x32_bf16 v[20:23], v[212:215], a[200:203], v[20:23]\n"
        "ds_read_b128 a[16:19], v244 offset:33792\n"
        "v_mfma_f32_16x16x32_bf16 v[20:23], v[204:207], a[196:199], v[20:23]\n"
        "ds_read_b128 a[20:23], v244 offset:37888\n"
        "v_mfma_f32_16x16x32_bf16 v[20:23], v[196:199], a[192:195], v[20:23]\n"
        "ds_read_b128 a[24:27], v244 offset:41984\n"
        "s_waitcnt lgkmcnt(7)\n"
        "v_mfma_f32_16x16x32_bf16 v[28:31], v[196:199], a[208:211], v[28:31]\n"
        "ds_read_b128 a[28:31], v244 offset:46080\n"
        "global_load_lds_dwordx4 v254, s[60:61] offset:1024 sc1\n"
        "v_mfma_f32_16x16x32_bf16 v[28:31], v[204:207], a[212:215], v[28:31]\n"
        "ds_read_b128 a[192:195], v244 offset:34816\n"
        "v_mfma_f32_16x16x32_bf16 v[28:31], v[212:215], a[216:219], v[28:31]\n"
        "ds_read_b128 a[196:199], v244 offset:38912\n"
        "v_mfma_f32_16x16x32_bf16 v[28:31], v[220:223], a[220:223], v[28:31]\n"
        "ds_read_b128 a[200:203], v244 offset:43008\n"
        "v_mfma_f32_16x16x32_bf16 v[24:27], v[216:219], a[220:223], v[24:27]\n"
        "ds_read_b128 a[204:207], v244 offset:47104\n"
        "s_waitcnt lgkmcnt(8)\n"
        "v_mfma_f32_16x16x32_bf16 v[24:27], v[208:211], a[216:219], v[24:27]\n"
        "ds_read_b128 a[216:219], v244 offset:44032\n"
        "v_mfma_f32_16x16x32_bf16 v[24:27], v[200:203], a[212:215], v[24:27]\n"
        "ds_read_b128 a[212:215], v244 offset:39936\n"
        "v_mfma_f32_16x16x32_bf16 v[24:27], v[192:195], a[208:211], v[24:27]\n"
        "ds_read_b128 a[208:211], v244 offset:35840\n"
        "global_load_lds_dwordx4 v254, s[60:61] offset:2048 sc1\n"
        "s_and_b32 s14, s14, 0xfffff9ff\n"
        "v_mfma_f32_16x16x32_bf16 v[0:3], v[112:115], a[0:3], v[0:3]\n"
        "ds_read_b128 a[220:223], v244 offset:48128\n"
        "v_bfe_u32 v155, v254, 4, 2\n"
        "v_mfma_f32_16x16x32_bf16 v[0:3], v[120:123], a[4:7], v[0:3]\n"
        "ds_read_b32 v224, v159 offset:0\n"
        "v_lshl_add_u32 v155, v155, 9, s14\n"
        "v_mfma_f32_16x16x32_bf16 v[0:3], a[64:67], a[8:11], v[0:3]\n"
        "ds_read_b32 v225, v159 offset:64\n"
        "v_mfma_f32_16x16x32_bf16 v[0:3], a[72:75], a[12:15], v[0:3]\n"
        "ds_read_b32 v226, v159 offset:128\n"
        "v_mfma_f32_16x16x32_bf16 v[4:7], a[76:79], a[12:15], v[4:7]\n"
        "ds_read_b32 v227, v159 offset:192\n"
        "s_waitcnt lgkmcnt(8)\n"
        "v_mfma_f32_16x16x32_bf16 v[4:7], a[68:71], a[8:11], v[4:7]\n"
        "v_mfma_f32_16x16x32_bf16 v[4:7], v[124:127], a[4:7], v[4:7]\n"
        "global_load_lds_dwordx4 v254, s[60:61] offset:3072 sc1\n"
        "s_add_u32 m0, s73, 0x4400\n"
        "v_mfma_f32_16x16x32_bf16 v[4:7], v[116:119], a[0:3], v[4:7]\n"
        "s_add_u32 s60, s68, s66\n"
        "s_addc_u32 s61, s69, 0\n"
        "s_xor_b32 s66, s17, 4\n"
        "v_mfma_f32_16x16x32_bf16 v[12:15], v[116:119], a[16:19], v[12:15]\n"
        "s_lshl_b32 s66, s66, 15\n"
        "v_mfma_f32_16x16x32_bf16 v[12:15], v[124:127], a[20:23], v[12:15]\n"
        "s_waitcnt lgkmcnt(3)\n"
        "v_sub_f32 v228, 1.0, v224\n"
        "v_cvt_pk_bf16_f32 v228, 0, v228\n"
        "v_mfma_f32_16x16x32_bf16 v[12:15], a[68:71], a[24:27], v[12:15]\n"
        "s_waitcnt lgkmcnt(2)\n"
        "v_sub_f32 v229, 1.0, v225\n"
        "v_cvt_pk_bf16_f32 v229, 0, v229\n"
        "v_mfma_f32_16x16x32_bf16 v[12:15], a[76:79], a[28:31], v[12:15]\n"
        "s_waitcnt lgkmcnt(1)\n"
        "v_sub_f32 v230, 1.0, v226\n"
        "v_cvt_pk_bf16_f32 v230, 0, v230\n"
        "v_mfma_f32_16x16x32_bf16 v[8:11], a[72:75], a[28:31], v[8:11]\n"
        "s_waitcnt lgkmcnt(0)\n"
        "v_sub_f32 v231, 1.0, v227\n"
        "v_cvt_pk_bf16_f32 v231, 0, v231\n"
        "v_mfma_f32_16x16x32_bf16 v[8:11], a[64:67], a[24:27], v[8:11]\n"
        "global_load_lds_dwordx4 v148, s[58:59] offset:0 sc1\n"
        "s_add_u32 m0, s73, 0xc000\n"
        "v_mfma_f32_16x16x32_bf16 v[8:11], v[120:123], a[20:23], v[8:11]\n"
        "s_add_u32 s58, s58, 0x8000\n"
        "s_addc_u32 s59, s59, 0\n"
        "v_mfma_f32_16x16x32_bf16 v[8:11], v[112:115], a[16:19], v[8:11]\n"
        "v_mfma_f32_16x16x32_bf16 v[16:19], v[112:115], a[192:195], v[16:19]\n"
        "s_barrier\n"
        "v_mfma_f32_16x16x32_bf16 v[16:19], v[120:123], a[196:199], v[16:19]\n"
        "ds_read_b128 a[0:3], v244 offset:49152\n"
        "v_mfma_f32_16x16x32_bf16 v[16:19], a[64:67], a[200:203], v[16:19]\n"
        "ds_read_b128 a[4:7], v244 offset:53248\n"
        "v_mfma_f32_16x16x32_bf16 v[16:19], a[72:75], a[204:207], v[16:19]\n"
        "ds_read_b128 a[8:11], v244 offset:57344\n"
        "v_mfma_f32_16x16x32_bf16 v[20:23], a[76:79], a[204:207], v[20:23]\n"
        "ds_read_b128 a[12:15], v244 offset:61440\n"
        "global_load_lds_dwordx4 v254, s[60:61] offset:0 sc1\n"
        "v_mfma_f32_16x16x32_bf16 v[20:23], a[68:71], a[200:203], v[20:23]\n"
        "ds_read_b128 a[16:19], v244 offset:50176\n"
        "v_mfma_f32_16x16x32_bf16 v[20:23], v[124:127], a[196:199], v[20:23]\n"
        "ds_read_b128 a[20:23], v244 offset:54272\n"
        "v_mfma_f32_16x16x32_bf16 v[20:23], v[116:119], a[192:195], v[20:23]\n"
        "ds_read_b128 a[24:27], v244 offset:58368\n"
        "v_mfma_f32_16x16x32_bf16 v[28:31], v[116:119], a[208:211], v[28:31]\n"
        "ds_read_b128 a[28:31], v244 offset:62464\n"
        "v_mfma_f32_16x16x32_bf16 v[28:31], v[124:127], a[212:215], v[28:31]\n"
        "ds_read_b128 a[192:195], v244 offset:51200\n"
        "v_mfma_f32_16x16x32_bf16 v[28:31], a[68:71], a[216:219], v[28:31]\n"
        "ds_read_b128 a[196:199], v244 offset:55296\n"
        "v_mfma_f32_16x16x32_bf16 v[28:31], a[76:79], a[220:223], v[28:31]\n"
        "ds_read_b128 a[200:203], v244 offset:59392\n"
        "global_load_dwordx4 v[100:103], v151, s[64:65]\n"
        "v_mfma_f32_16x16x32_bf16 v[24:27], a[72:75], a[220:223], v[24:27]\n"
        "ds_read_b128 a[204:207], v244 offset:63488\n"
        "s_add_u32 s64, s64, 0x8000\n"
        "s_addc_u32 s65, s65, 0\n"
        "v_mfma_f32_16x16x32_bf16 v[24:27], a[64:67], a[216:219], v[24:27]\n"
        "ds_read_b128 a[216:219], v244 offset:60416\n"
        "s_waitcnt lgkmcnt(8)\n"
        "v_mfma_f32_16x16x32_bf16 v[24:27], v[120:123], a[212:215], v[24:27]\n"
        "ds_read_b128 a[212:215], v244 offset:56320\n"
        "v_mfma_f32_16x16x32_bf16 v[24:27], v[112:115], a[208:211], v[24:27]\n"
        "ds_read_b128 a[208:211], v244 offset:52224\n"
        "v_mfma_f32_16x16x32_bf16 v[0:3], a[80:83], a[0:3], v[0:3]\n"
        "ds_read_b128 a[220:223], v244 offset:64512\n"
        "v_mfma_f32_16x16x32_bf16 v[0:3], a[88:91], a[4:7], v[0:3]\n"
        "v_mfma_f32_16x16x32_bf16 v[0:3], a[160:163], a[8:11], v[0:3]\n"
        "global_load_lds_dwordx4 v254, s[60:61] offset:1024 sc1\n"
        "v_mfma_f32_16x16x32_bf16 v[0:3], a[168:171], a[12:15], v[0:3]\n"
        "v_mfma_f32_16x16x32_bf16 v[4:7], a[172:175], a[12:15], v[4:7]\n"
        "v_mfma_f32_16x16x32_bf16 v[4:7], a[164:167], a[8:11], v[4:7]\n"
        "s_waitcnt lgkmcnt(3)\n"
        "v_mfma_f32_16x16x32_bf16 v[4:7], a[92:95], a[4:7], v[4:7]\n"
        "v_mfma_f32_16x16x32_bf16 v[4:7], a[84:87], a[0:3], v[4:7]\n"
        "v_mfma_f32_16x16x32_bf16 v[12:15], a[84:87], a[16:19], v[12:15]\n"
        "v_mfma_f32_16x16x32_bf16 v[12:15], a[92:95], a[20:23], v[12:15]\n"
        "global_load_lds_dwordx4 v254, s[60:61] offset:2048 sc1\n"
        "v_mfma_f32_16x16x32_bf16 v[12:15], a[164:167], a[24:27], v[12:15]\n"
        "v_mfma_f32_16x16x32_bf16 v[12:15], a[172:175], a[28:31], v[12:15]\n"
        "v_mfma_f32_16x16x32_bf16 v[8:11], a[168:171], a[28:31], v[8:11]\n"
        "v_mfma_f32_16x16x32_bf16 v[8:11], a[160:163], a[24:27], v[8:11]\n"
        "v_mfma_f32_16x16x32_bf16 v[8:11], a[88:91], a[20:23], v[8:11]\n"
        "v_mfma_f32_16x16x32_bf16 v[8:11], a[80:83], a[16:19], v[8:11]\n"
        "v_mfma_f32_16x16x32_bf16 v[16:19], a[80:83], a[192:195], v[16:19]\n"
        "s_barrier\n"
        "v_mfma_f32_16x16x32_bf16 v[16:19], a[88:91], a[196:199], v[16:19]\n"
        "ds_read_b128 a[0:3], v245 offset:0\n"
        "global_load_lds_dwordx4 v254, s[60:61] offset:3072 sc1\n"
        "s_add_u32 m0, s73, 0x4800\n"
        "v_mfma_f32_16x16x32_bf16 v[16:19], a[160:163], a[200:203], v[16:19]\n"
        "ds_read_b128 a[4:7], v245 offset:4096\n"
        "s_add_u32 s60, s68, s66\n"
        "s_addc_u32 s61, s69, 0\n"
        "v_mfma_f32_16x16x32_bf16 v[16:19], a[168:171], a[204:207], v[16:19]\n"
        "ds_read_b128 a[8:11], v245 offset:8192\n"
        "s_xor_b32 s66, s17, 5\n"
        "s_lshl_b32 s66, s66, 15\n"
        "v_mfma_f32_16x16x32_bf16 v[20:23], a[172:175], a[204:207], v[20:23]\n"
        "ds_read_b128 a[12:15], v245 offset:12288\n"
        "s_waitcnt lgkmcnt(4)\n"
        "v_mfma_f32_16x16x32_bf16 v[20:23], a[164:167], a[200:203], v[20:23]\n"
        "LHB2_%=:\n"
        "s_cbranch_execz LHRB_%=\n"
        "ds_read_b128 a[16:19], v245 offset:1024\n"
        "v_mfma_f32_16x16x32_bf16 v[20:23], a[92:95], a[196:199], v[20:23]\n"
        "ds_read_b128 a[20:23], v245 offset:5120\n"
        "v_mfma_f32_16x16x32_bf16 v[20:23], a[84:87], a[192:195], v[20:23]\n"
        "ds_read_b128 a[24:27], v245 offset:9216\n"
        "v_mfma_f32_16x16x32_bf16 v[28:31], a[84:87], a[208:211], v[28:31]\n"
        "ds_read_b128 a[28:31], v245 offset:13312\n"
        "global_load_lds_dwordx4 v148, s[58:59] offset:0 sc1\n"
        "s_add_u32 m0, s73, 0x18000\n"
        "v_mfma_f32_16x16x32_bf16 v[28:31], a[92:95], a[212:215], v[28:31]\n"
        "ds_read_b128 a[192:195], v245 offset:2048\n"
        "s_add_u32 s58, s58, 0x8000\n"
        "s_addc_u32 s59, s59, 0\n"
        "v_mfma_f32_16x16x32_bf16 v[28:31], a[164:167], a[216:219], v[28:31]\n"
        "ds_read_b128 a[196:199], v245 offset:6144\n"
        "v_mfma_f32_16x16x32_bf16 v[28:31], a[172:175], a[220:223], v[28:31]\n"
        "ds_read_b128 a[200:203], v245 offset:10240\n"
        "v_mfma_f32_16x16x32_bf16 v[24:27], a[168:171], a[220:223], v[24:27]\n"
        "ds_read_b128 a[204:207], v245 offset:14336\n"
        "s_waitcnt lgkmcnt(8)\n"
        "v_mfma_f32_16x16x32_bf16 v[24:27], a[160:163], a[216:219], v[24:27]\n"
        "ds_read_b128 a[216:219], v245 offset:11264\n"
        "v_mfma_f32_16x16x32_bf16 v[24:27], a[88:91], a[212:215], v[24:27]\n"
        "ds_read_b128 a[212:215], v245 offset:7168\n"
        "v_mfma_f32_16x16x32_bf16 v[24:27], a[80:83], a[208:211], v[24:27]\n"
        "ds_read_b128 a[208:211], v245 offset:3072\n"
        "global_load_lds_dwordx4 v254, s[60:61] offset:0 sc1\n"
        "s_mul_i32 s16, s13, 263168\n"
        "v_mfma_f32_16x16x32_bf16 v[0:3], a[176:179], a[0:3], v[0:3]\n"
        "ds_read_b128 a[220:223], v245 offset:15360\n"
        "s_lshl2_add_u32 s15, s10, s74\n"
        "s_lshl_b32 s15, s15, 14\n"
        "v_mfma_f32_16x16x32_bf16 v[0:3], a[184:187], a[4:7], v[0:3]\n"
        "s_add_u32 s16, s16, s15\n"
        "s_add_u32 s16, s16, s73\n"
        "s_add_u32 s16, s16, 0x4000000\n"
        "v_mfma_f32_16x16x32_bf16 v[0:3], a[32:35], a[8:11], v[0:3]\n"
        "s_add_u32 s30, s38, s16\n"
        "s_addc_u32 s31, s39, 0\n"
        "s_mul_i32 s16, s13, 263168\n"
        "v_mfma_f32_16x16x32_bf16 v[0:3], a[40:43], a[12:15], v[0:3]\n"
        "s_lshl2_add_u32 s15, s10, s75\n"
        "s_lshl_b32 s15, s15, 14\n"
        "s_add_u32 s16, s16, s15\n"
        "v_mfma_f32_16x16x32_bf16 v[4:7], a[44:47], a[12:15], v[4:7]\n"
        "s_add_u32 s16, s16, s73\n"
        "s_add_u32 s16, s16, 0x4000000\n"
        "s_lshl2_add_u32 s15, s10, s76\n"
        "v_mfma_f32_16x16x32_bf16 v[4:7], a[36:39], a[8:11], v[4:7]\n"
        "s_lshl_b32 s15, s15, 14\n"
        "s_waitcnt lgkmcnt(3)\n"
        "v_mfma_f32_16x16x32_bf16 v[4:7], a[188:191], a[4:7], v[4:7]\n"
        "global_load_dwordx4 v[104:107], v151, s[64:65]\n"
        "v_mfma_f32_16x16x32_bf16 v[4:7], a[180:183], a[0:3], v[4:7]\n"
        "s_add_u32 s64, s64, 0x8000\n"
        "s_addc_u32 s65, s65, 0\n"
        "s_waitcnt vmcnt(13) lgkmcnt(1)\n"
        "v_mfma_f32_16x16x32_bf16 v[12:15], a[180:183], a[16:19], v[12:15]\n"
        "v_permlane16_swap_b32 v96, v98\n"
        "v_mfma_f32_16x16x32_bf16 v[12:15], a[188:191], a[20:23], v[12:15]\n"
        "v_permlane16_swap_b32 v97, v99\n"
        "v_mfma_f32_16x16x32_bf16 v[12:15], a[36:39], a[24:27], v[12:15]\n"
        "v_lshlrev_b32 v64, 16, v96\n"
        "v_and_b32 v65, s35, v96\n"
        "v_mfma_f32_16x16x32_bf16 v[12:15], a[44:47], a[28:31], v[12:15]\n"
        "v_lshlrev_b32 v66, 16, v97\n"
        "v_and_b32 v67, s35, v97\n"
        "v_mfma_f32_16x16x32_bf16 v[8:11], a[40:43], a[28:31], v[8:11]\n"
        "v_lshlrev_b32 v68, 16, v98\n"
        "v_and_b32 v69, s35, v98\n"
        "v_mfma_f32_16x16x32_bf16 v[8:11], a[32:35], a[24:27], v[8:11]\n"
        "global_load_lds_dwordx4 v254, s[60:61] offset:1024 sc1\n"
        "v_mfma_f32_16x16x32_bf16 v[8:11], a[184:187], a[20:23], v[8:11]\n"
        "v_lshlrev_b32 v70, 16, v99\n"
        "v_and_b32 v71, s35, v99\n"
        "v_mfma_f32_16x16x32_bf16 v[8:11], a[176:179], a[16:19], v[8:11]\n"
        "v_mfma_f32_16x16x32_bf16 v[16:19], a[176:179], a[192:195], v[16:19]\n"
        "s_barrier\n"
        "v_mfma_f32_16x16x32_bf16 v[16:19], a[184:187], a[196:199], v[16:19]\n"
        "ds_read_b128 a[0:3], v245 offset:16384\n"
        "v_mfma_f32_16x16x32_bf16 v[16:19], a[32:35], a[200:203], v[16:19]\n"
        "ds_read_b128 a[4:7], v245 offset:20480\n"
        "v_mfma_f32_16x16x32_bf16 v[16:19], a[40:43], a[204:207], v[16:19]\n"
        "ds_read_b128 a[8:11], v245 offset:24576\n"
        "v_mfma_f32_16x16x32_bf16 v[20:23], a[44:47], a[204:207], v[20:23]\n"
        "ds_read_b128 a[12:15], v245 offset:28672\n"
        "global_load_lds_dwordx4 v254, s[60:61] offset:2048 sc1\n"
        "v_mfma_f32_16x16x32_bf16 v[20:23], a[36:39], a[200:203], v[20:23]\n"
        "ds_read_b128 a[28:31], v245 offset:29696\n"
        "v_mfma_f32_16x16x32_bf16 v[20:23], a[188:191], a[196:199], v[20:23]\n"
        "ds_read_b128 a[24:27], v245 offset:25600\n"
        "v_mfma_f32_16x16x32_bf16 v[20:23], a[180:183], a[192:195], v[20:23]\n"
        "ds_read_b128 a[20:23], v245 offset:21504\n"
        "v_mfma_f32_16x16x32_bf16 v[28:31], a[180:183], a[208:211], v[28:31]\n"
        "ds_read_b128 a[16:19], v245 offset:17408\n"
        "v_mfma_f32_16x16x32_bf16 v[28:31], a[188:191], a[212:215], v[28:31]\n"
        "ds_read_b128 a[192:195], v245 offset:18432\n"
        "s_waitcnt lgkmcnt(8)\n"
        "v_mfma_f32_16x16x32_bf16 v[0:3], a[48:51], a[0:3], v[0:3]\n"
        "ds_read_b128 a[196:199], v245 offset:22528\n"
        "s_waitcnt lgkmcnt(8)\n"
        "v_mfma_f32_16x16x32_bf16 v[0:3], a[56:59], a[4:7], v[0:3]\n"
        "ds_read_b128 a[200:203], v245 offset:26624\n"
        "s_waitcnt lgkmcnt(8)\n"
        "v_mfma_f32_16x16x32_bf16 v[0:3], a[224:227], a[8:11], v[0:3]\n"
        "ds_read_b128 a[204:207], v245 offset:30720\n"
        "s_waitcnt lgkmcnt(8)\n"
        "v_mfma_f32_16x16x32_bf16 v[0:3], a[232:235], a[12:15], v[0:3]\n"
        "global_load_lds_dwordx4 v254, s[60:61] offset:3072 sc1\n"
        "s_add_u32 m0, s73, 0x4c00\n"
        "v_mfma_f32_16x16x32_bf16 v[4:7], a[236:239], a[12:15], v[4:7]\n"
        "s_add_u32 s60, s68, s66\n"
        "s_addc_u32 s61, s69, 0\n"
        "s_xor_b32 s66, s17, 6\n"
        "v_mfma_f32_16x16x32_bf16 v[4:7], a[228:231], a[8:11], v[4:7]\n"
        "s_lshl_b32 s66, s66, 15\n"
        "v_mfma_f32_16x16x32_bf16 v[4:7], a[60:63], a[4:7], v[4:7]\n"
        "v_cvt_pk_bf16_f32 v0, v0, v1\n"
        "v_cvt_pk_bf16_f32 v1, v2, v3\n"
        "v_mfma_f32_16x16x32_bf16 v[4:7], a[52:55], a[0:3], v[4:7]\n"
        "s_waitcnt lgkmcnt(3)\n"
        "v_mfma_f32_16x16x32_bf16 v[28:31], a[36:39], a[216:219], v[28:31]\n"
        "v_mfma_f32_16x16x32_bf16 v[28:31], a[44:47], a[220:223], v[28:31]\n"
        "v_mfma_f32_16x16x32_bf16 v[8:11], a[232:235], a[28:31], v[8:11]\n"
        "global_load_lds_dwordx4 v148, s[58:59] offset:0 sc1\n"
        "s_add_u32 m0, s73, 0x1c000\n"
        "v_mfma_f32_16x16x32_bf16 v[8:11], a[224:227], a[24:27], v[8:11]\n"
        "v_mfma_f32_16x16x32_bf16 v[8:11], a[56:59], a[20:23], v[8:11]\n"
        "v_cvt_pk_bf16_f32 v2, v4, v5\n"
        "v_cvt_pk_bf16_f32 v3, v6, v7\n"
        "v_mfma_f32_16x16x32_bf16 v[8:11], a[48:51], a[16:19], v[8:11]\n"
        "v_mfma_f32_16x16x32_bf16 v[12:15], a[52:55], a[16:19], v[12:15]\n"
        "global_store_dwordx4 v254, v[0:3], s[28:29] offset:0 sc1\n"
        "s_barrier\n"
        "ds_read_b128 a[0:3], v244 offset:0\n"
        "v_mfma_f32_16x16x32_bf16 v[12:15], a[60:63], a[20:23], v[12:15]\n"
        "ds_read_b128 a[4:7], v244 offset:4096\n"
        "v_mfma_f32_16x16x32_bf16 v[12:15], a[228:231], a[24:27], v[12:15]\n"
        "ds_read_b128 a[8:11], v244 offset:8192\n"
        "v_cvt_pk_bf16_f32 v8, v8, v9\n"
        "v_mfma_f32_16x16x32_bf16 v[12:15], a[236:239], a[28:31], v[12:15]\n"
        "ds_read_b128 a[12:15], v244 offset:12288\n"
        "v_cvt_pk_bf16_f32 v9, v10, v11\n"
        "v_mfma_f32_16x16x32_bf16 v[24:27], a[40:43], a[220:223], v[24:27]\n"
        "ds_read_b128 a[220:223], v245 offset:31744\n"
        "v_mfma_f32_16x16x32_bf16 v[24:27], a[32:35], a[216:219], v[24:27]\n"
        "ds_read_b128 a[216:219], v245 offset:27648\n"
        "s_waitcnt lgkmcnt(6)\n"
        "v_mfma_f32_16x16x32_bf16 v[24:27], a[184:187], a[212:215], v[24:27]\n"
        "ds_read_b128 a[212:215], v245 offset:23552\n"
        "global_load_lds_dwordx4 v254, s[60:61] offset:0 sc1\n"
        "v_mfma_f32_16x16x32_bf16 v[24:27], a[176:179], a[208:211], v[24:27]\n"
        "ds_read_b128 a[208:211], v245 offset:19456\n"
        "v_cvt_pk_bf16_f32 v10, v12, v13\n"
        "v_mfma_f32_16x16x32_bf16 v[16:19], a[48:51], a[192:195], v[16:19]\n"
        "ds_read_b128 a[16:19], v244 offset:1024\n"
        "v_cvt_pk_bf16_f32 v11, v14, v15\n"
        "v_mfma_f32_16x16x32_bf16 v[16:19], a[56:59], a[196:199], v[16:19]\n"
        "ds_read_b128 a[20:23], v244 offset:5120\n"
        "v_mfma_f32_16x16x32_bf16 v[16:19], a[224:227], a[200:203], v[16:19]\n"
        "global_store_dwordx4 v254, v[8:11], s[28:29] offset:1024 sc1\n"
        "ds_read_b128 a[24:27], v244 offset:9216\n"
        "ds_read_b128 a[28:31], v244 offset:13312\n"
        "v_mfma_f32_16x16x32_bf16 v[16:19], a[232:235], a[204:207], v[16:19]\n"
        "v_mfma_f32_16x16x32_bf16 v[20:23], a[236:239], a[204:207], v[20:23]\n"
        "ds_read_b128 a[204:207], v244 offset:14336\n"
        "s_waitcnt lgkmcnt(7)\n"
        "v_mfma_f32_16x16x32_bf16 v[20:23], a[228:231], a[200:203], v[20:23]\n"
        "ds_read_b128 a[200:203], v244 offset:10240\n"
        "v_mfma_f32_16x16x32_bf16 v[20:23], a[60:63], a[196:199], v[20:23]\n"
        "ds_read_b128 a[196:199], v244 offset:6144\n"
        "s_waitcnt lgkmcnt(7)\n"
        "v_mfma_f32_16x16x32_bf16 v[20:23], a[52:55], a[192:195], v[20:23]\n"
        "ds_read_b128 a[192:195], v244 offset:2048\n"
        "v_cvt_pk_bf16_f32 v16, v16, v17\n"
        "v_mfma_f32_16x16x32_bf16 v[24:27], a[232:235], a[220:223], v[24:27]\n"
        "global_load_dwordx4 v[108:111], v151, s[64:65]\n"
        "v_mfma_f32_16x16x32_bf16 v[24:27], a[224:227], a[216:219], v[24:27]\n"
        "s_waitcnt vmcnt(14) lgkmcnt(6)\n"
        "v_permlane16_swap_b32 v100, v102\n"
        "v_mfma_f32_16x16x32_bf16 v[28:31], a[52:55], a[208:211], v[28:31]\n"
        "v_permlane16_swap_b32 v101, v103\n"
        "v_mfma_f32_16x16x32_bf16 v[24:27], a[56:59], a[212:215], v[24:27]\n"
        "v_cvt_pk_bf16_f32 v17, v18, v19\n"
        "v_cvt_pk_bf16_f32 v18, v20, v21\n"
        "v_mfma_f32_16x16x32_bf16 v[28:31], a[60:63], a[212:215], v[28:31]\n"
        "ds_read_b128 a[212:215], v244 offset:7168\n"
        "v_cvt_pk_bf16_f32 v19, v22, v23\n"
        "v_mfma_f32_16x16x32_bf16 v[24:27], a[48:51], a[208:211], v[24:27]\n"
        "global_store_dwordx4 v254, v[16:19], s[28:29] offset:2048 sc1\n"
        "ds_read_b128 a[208:211], v244 offset:3072\n"
        "v_lshlrev_b32 v72, 16, v100\n"
        "v_and_b32 v73, s35, v100\n"
        "v_lshlrev_b32 v74, 16, v101\n"
        "v_mfma_f32_16x16x32_bf16 v[28:31], a[228:231], a[216:219], v[28:31]\n"
        "ds_read_b128 a[216:219], v244 offset:11264\n"
        "v_and_b32 v75, s35, v101\n"
        "v_mfma_f32_16x16x32_bf16 v[28:31], a[236:239], a[220:223], v[28:31]\n"
        "ds_read_b128 a[220:223], v244 offset:15360\n"
        "v_cvt_pk_bf16_f32 v24, v24, v25\n"
        "v_mfma_f32_16x16x32_bf16 v[32:35], a[96:99], a[0:3], 0\n"
        "v_cvt_pk_bf16_f32 v25, v26, v27\n"
        "v_lshlrev_b32 v76, 16, v102\n"
        "v_mfma_f32_16x16x32_bf16 v[32:35], a[104:107], a[4:7], v[32:35]\n"
        "v_and_b32 v77, s35, v102\n"
        "v_lshlrev_b32 v78, 16, v103\n"
        "v_mfma_f32_16x16x32_bf16 v[32:35], a[112:115], a[8:11], v[32:35]\n"
        "v_cvt_pk_bf16_f32 v26, v28, v29\n"
        "v_cvt_pk_bf16_f32 v27, v30, v31\n"
        "v_mfma_f32_16x16x32_bf16 v[32:35], a[120:123], a[12:15], v[32:35]\n"
        "global_store_dwordx4 v254, v[24:27], s[28:29] offset:3072 sc1\n"
        "v_and_b32 v79, s35, v103\n"
        "v_mfma_f32_16x16x32_bf16 v[36:39], a[124:127], a[12:15], 0\n"
        "v_mfma_f32_16x16x32_bf16 v[36:39], a[116:119], a[8:11], v[36:39]\n"
        "v_mfma_f32_16x16x32_bf16 v[36:39], a[108:111], a[4:7], v[36:39]\n"
        "s_waitcnt lgkmcnt(1)\n"
        "v_mfma_f32_16x16x32_bf16 v[36:39], a[100:103], a[0:3], v[36:39]\n"
        "v_mfma_f32_16x16x32_bf16 v[44:47], a[100:103], a[16:19], 0\n"
        "v_mfma_f32_16x16x32_bf16 v[44:47], a[108:111], a[20:23], v[44:47]\n"
        "global_load_lds_dwordx4 v254, s[60:61] offset:1024 sc1\n"
        "v_mfma_f32_16x16x32_bf16 v[44:47], a[116:119], a[24:27], v[44:47]\n"
        "v_mfma_f32_16x16x32_bf16 v[44:47], a[124:127], a[28:31], v[44:47]\n"
        "v_mfma_f32_16x16x32_bf16 v[40:43], a[120:123], a[28:31], 0\n"
        "v_mfma_f32_16x16x32_bf16 v[40:43], a[112:115], a[24:27], v[40:43]\n"
        "v_mfma_f32_16x16x32_bf16 v[40:43], a[104:107], a[20:23], v[40:43]\n"
        "v_mfma_f32_16x16x32_bf16 v[40:43], a[96:99], a[16:19], v[40:43]\n"
        "v_mfma_f32_16x16x32_bf16 v[48:51], a[96:99], a[192:195], 0\n"
        "s_barrier\n"
        "v_mfma_f32_16x16x32_bf16 v[48:51], a[104:107], a[196:199], v[48:51]\n"
        "ds_read_b128 a[0:3], v244 offset:16384\n"
        "global_load_lds_dwordx4 v254, s[60:61] offset:2048 sc1\n"
        "v_mfma_f32_16x16x32_bf16 v[48:51], a[112:115], a[200:203], v[48:51]\n"
        "ds_read_b128 a[4:7], v244 offset:20480\n"
        "v_mfma_f32_16x16x32_bf16 v[48:51], a[120:123], a[204:207], v[48:51]\n"
        "ds_read_b128 a[8:11], v244 offset:24576\n"
        "v_mfma_f32_16x16x32_bf16 v[52:55], a[124:127], a[204:207], 0\n"
        "ds_read_b128 a[12:15], v244 offset:28672\n"
        "v_mfma_f32_16x16x32_bf16 v[52:55], a[116:119], a[200:203], v[52:55]\n"
        "ds_read_b128 a[16:19], v244 offset:17408\n"
        "v_mfma_f32_16x16x32_bf16 v[52:55], a[108:111], a[196:199], v[52:55]\n"
        "ds_read_b128 a[20:23], v244 offset:21504\n"
        "v_mfma_f32_16x16x32_bf16 v[52:55], a[100:103], a[192:195], v[52:55]\n"
        "ds_read_b128 a[24:27], v244 offset:25600\n"
        "s_waitcnt lgkmcnt(7)\n"
        "v_mfma_f32_16x16x32_bf16 v[60:63], a[100:103], a[208:211], 0\n"
        "ds_read_b128 a[28:31], v244 offset:29696\n"
        "global_load_lds_dwordx4 v254, s[60:61] offset:3072 sc1\n"
        "s_add_u32 m0, s73, 0x20000\n"
        "v_mfma_f32_16x16x32_bf16 v[60:63], a[108:111], a[212:215], v[60:63]\n"
        "ds_read_b128 a[192:195], v244 offset:18432\n"
        "s_add_u32 s60, s68, s66\n"
        "s_addc_u32 s61, s69, 0\n"
        "v_mfma_f32_16x16x32_bf16 v[60:63], a[116:119], a[216:219], v[60:63]\n"
        "ds_read_b128 a[196:199], v244 offset:22528\n"
        "s_xor_b32 s66, s17, 7\n"
        "s_lshl_b32 s66, s66, 15\n"
        "v_mfma_f32_16x16x32_bf16 v[60:63], a[124:127], a[220:223], v[60:63]\n"
        "ds_read_b128 a[200:203], v244 offset:26624\n"
        "v_mfma_f32_16x16x32_bf16 v[56:59], a[120:123], a[220:223], 0\n"
        "ds_read_b128 a[204:207], v244 offset:30720\n"
        "s_waitcnt lgkmcnt(8)\n"
        "v_mfma_f32_16x16x32_bf16 v[56:59], a[112:115], a[216:219], v[56:59]\n"
        "ds_read_b128 a[216:219], v244 offset:27648\n"
        "v_mfma_f32_16x16x32_bf16 v[56:59], a[104:107], a[212:215], v[56:59]\n"
        "ds_read_b128 a[212:215], v244 offset:23552\n"
        "v_mfma_f32_16x16x32_bf16 v[56:59], a[96:99], a[208:211], v[56:59]\n"
        "ds_read_b128 a[208:211], v244 offset:19456\n"
        "global_load_lds_dwordx4 v254, s[60:61] offset:0 sc1\n"
        "v_mfma_f32_16x16x32_bf16 v[32:35], a[128:131], a[0:3], v[32:35]\n"
        "ds_read_b128 a[220:223], v244 offset:31744\n"
        "s_waitcnt vmcnt(14) lgkmcnt(8)\n"
        "v_mfma_f32_16x16x32_bf16 v[32:35], a[136:139], a[4:7], v[32:35]\n"
        "v_permlane16_swap_b32 v104, v106\n"
        "v_mfma_f32_16x16x32_bf16 v[32:35], a[144:147], a[8:11], v[32:35]\n"
        "v_permlane16_swap_b32 v105, v107\n"
        "v_mfma_f32_16x16x32_bf16 v[32:35], a[152:155], a[12:15], v[32:35]\n"
        "v_lshlrev_b32 v80, 16, v104\n"
        "v_and_b32 v81, s35, v104\n"
        "v_mfma_f32_16x16x32_bf16 v[36:39], a[156:159], a[12:15], v[36:39]\n"
        "v_lshlrev_b32 v82, 16, v105\n"
        "v_and_b32 v83, s35, v105\n"
        "v_mfma_f32_16x16x32_bf16 v[36:39], a[148:151], a[8:11], v[36:39]\n"
        "v_lshlrev_b32 v84, 16, v106\n"
        "v_and_b32 v85, s35, v106\n"
        "v_mfma_f32_16x16x32_bf16 v[36:39], a[140:143], a[4:7], v[36:39]\n"
        "global_load_lds_dwordx4 v254, s[60:61] offset:1024 sc1\n"
        "v_mfma_f32_16x16x32_bf16 v[36:39], a[132:135], a[0:3], v[36:39]\n"
        "v_lshlrev_b32 v86, 16, v107\n"
        "v_and_b32 v87, s35, v107\n"
        "v_mfma_f32_16x16x32_bf16 v[44:47], a[132:135], a[16:19], v[44:47]\n"
        "v_mfma_f32_16x16x32_bf16 v[44:47], a[140:143], a[20:23], v[44:47]\n"
        "v_mfma_f32_16x16x32_bf16 v[44:47], a[148:151], a[24:27], v[44:47]\n"
        "v_mfma_f32_16x16x32_bf16 v[44:47], a[156:159], a[28:31], v[44:47]\n"
        "v_mfma_f32_16x16x32_bf16 v[40:43], a[152:155], a[28:31], v[40:43]\n"
        "s_waitcnt lgkmcnt(0)\n"
        "v_mfma_f32_16x16x32_bf16 v[40:43], a[144:147], a[24:27], v[40:43]\n"
        "global_load_lds_dwordx4 v254, s[60:61] offset:2048 sc1\n"
        "v_mfma_f32_16x16x32_bf16 v[40:43], a[136:139], a[20:23], v[40:43]\n"
        "v_mfma_f32_16x16x32_bf16 v[40:43], a[128:131], a[16:19], v[40:43]\n"
        "v_mfma_f32_16x16x32_bf16 v[48:51], a[128:131], a[192:195], v[48:51]\n"
        "s_barrier\n"
        "v_mfma_f32_16x16x32_bf16 v[48:51], a[136:139], a[196:199], v[48:51]\n"
        "ds_read_b128 a[0:3], v254 offset:32768\n"
        "v_mfma_f32_16x16x32_bf16 v[48:51], a[144:147], a[200:203], v[48:51]\n"
        "ds_read_b128 a[4:7], v254 offset:36864\n"
        "v_mfma_f32_16x16x32_bf16 v[48:51], a[152:155], a[204:207], v[48:51]\n"
        "ds_read_b128 a[8:11], v254 offset:40960\n"
        "v_mfma_f32_16x16x32_bf16 v[52:55], a[156:159], a[204:207], v[52:55]\n"
        "ds_read_b128 a[12:15], v254 offset:45056\n"
        "global_load_lds_dwordx4 v254, s[60:61] offset:3072 sc1\n"
        "s_add_u32 m0, s73, 0x24000\n"
        "v_mfma_f32_16x16x32_bf16 v[52:55], a[148:151], a[200:203], v[52:55]\n"
        "ds_read_b128 a[16:19], v254 offset:33792\n"
        "s_add_u32 s60, s68, s66\n"
        "s_addc_u32 s61, s69, 0\n"
        "v_mfma_f32_16x16x32_bf16 v[52:55], a[140:143], a[196:199], v[52:55]\n"
        "ds_read_b128 a[20:23], v254 offset:37888\n"
        "v_mfma_f32_16x16x32_bf16 v[52:55], a[132:135], a[192:195], v[52:55]\n"
        "ds_read_b128 a[24:27], v254 offset:41984\n"
        "v_mfma_f32_16x16x32_bf16 v[60:63], a[132:135], a[208:211], v[60:63]\n"
        "ds_read_b128 a[28:31], v254 offset:46080\n"
        "v_mfma_f32_16x16x32_bf16 v[60:63], a[140:143], a[212:215], v[60:63]\n"
        "ds_read_b128 a[192:195], v254 offset:34816\n"
        "v_mfma_f32_16x16x32_bf16 v[60:63], a[148:151], a[216:219], v[60:63]\n"
        "ds_read_b128 a[196:199], v254 offset:38912\n"
        "s_waitcnt vmcnt(7)\n"
        "v_mfma_f32_16x16x32_bf16 v[60:63], a[156:159], a[220:223], v[60:63]\n"
        "s_mov_b64 exec, 0xffff\n"
        "global_store_dwordx2 v154, v[250:251], s[48:49] sc1\n"
        "s_mov_b64 exec, -1\n"
        "v_mfma_f32_16x16x32_bf16 v[56:59], a[152:155], a[220:223], v[56:59]\n"
        "ds_read_b128 a[200:203], v254 offset:43008\n"
        "global_load_lds_dwordx4 v254, s[60:61] offset:0 sc1\n"
        "v_mfma_f32_16x16x32_bf16 v[56:59], a[144:147], a[216:219], v[56:59]\n"
        "ds_read_b128 a[204:207], v254 offset:47104\n"
        "s_waitcnt lgkmcnt(7)\n"
        "v_mfma_f32_16x16x32_bf16 v[56:59], a[136:139], a[212:215], v[56:59]\n"
        "ds_read_b128 a[212:215], v254 offset:39936\n"
        "v_mfma_f32_16x16x32_bf16 v[56:59], a[128:131], a[208:211], v[56:59]\n"
        "ds_read_b128 a[208:211], v254 offset:35840\n"
        "v_mfma_f32_16x16x32_bf16 v[32:35], v[160:163], a[0:3], v[32:35]\n"
        "ds_read_b128 a[216:219], v254 offset:44032\n"
        "v_mfma_f32_16x16x32_bf16 v[32:35], v[168:171], a[4:7], v[32:35]\n"
        "ds_read_b128 a[220:223], v254 offset:48128\n"
        "v_mfma_f32_16x16x32_bf16 v[32:35], v[176:179], a[8:11], v[32:35]\n"
        "ds_read_b64_tr_b16 v[128:129], v149 offset:0\n"
        "v_mfma_f32_16x16x32_bf16 v[32:35], v[184:187], a[12:15], v[32:35]\n"
        "ds_read_b64_tr_b16 v[130:131], v149 offset:2048\n"
        "global_load_lds_dwordx4 v254, s[60:61] offset:1024 sc1\n"
        "v_mfma_f32_16x16x32_bf16 v[36:39], v[188:191], a[12:15], v[36:39]\n"
        "ds_read_b64_tr_b16 v[132:133], v149 offset:32\n"
        "v_mfma_f32_16x16x32_bf16 v[36:39], v[180:183], a[8:11], v[36:39]\n"
        "ds_read_b64_tr_b16 v[134:135], v149 offset:2080\n"
        "s_waitcnt lgkmcnt(8)\n"
        "v_mfma_f32_16x16x32_bf16 v[36:39], v[172:175], a[4:7], v[36:39]\n"
        "ds_read_b64_tr_b16 v[136:137], v149 offset:64\n"
        "v_mfma_f32_16x16x32_bf16 v[36:39], v[164:167], a[0:3], v[36:39]\n"
        "ds_read_b64_tr_b16 v[138:139], v149 offset:2112\n"
        "v_mfma_f32_16x16x32_bf16 v[44:47], v[164:167], a[16:19], v[44:47]\n"
        "ds_read_b64_tr_b16 v[140:141], v149 offset:96\n"
        "v_mfma_f32_16x16x32_bf16 v[44:47], v[172:175], a[20:23], v[44:47]\n"
        "ds_read_b64_tr_b16 v[142:143], v149 offset:2144\n"
        "v_mfma_f32_16x16x32_bf16 v[40:43], v[184:187], a[28:31], v[40:43]\n"
        "global_load_lds_dwordx4 v254, s[60:61] offset:2048 sc1\n"
        "v_mfma_f32_16x16x32_bf16 v[40:43], v[176:179], a[24:27], v[40:43]\n"
        "s_waitcnt lgkmcnt(6)\n"
        "v_lshlrev_b32 v246, 16, v128\n"
        "v_mul_f32 v64, v64, v246\n"
        "v_mfma_f32_16x16x32_bf16 v[48:51], v[160:163], a[192:195], v[48:51]\n"
        "s_waitcnt lgkmcnt(5)\n"
        "v_cvt_pk_bf16_f32 v64, 0, v64\n"
        "v_mul_f32 v64, v64, v228\n"
        "v_mfma_f32_16x16x32_bf16 v[40:43], v[168:171], a[20:23], v[40:43]\n"
        "v_cvt_pk_bf16_f32 v64, 0, v64\n"
        "v_and_b32 v247, s35, v128\n"
        "v_mfma_f32_16x16x32_bf16 v[48:51], v[168:171], a[196:199], v[48:51]\n"
        "v_mul_f32 v65, v65, v247\n"
        "v_cvt_pk_bf16_f32 v65, 0, v65\n"
        "v_mfma_f32_16x16x32_bf16 v[40:43], v[160:163], a[16:19], v[40:43]\n"
        "v_mul_f32 v65, v65, v228\n"
        "v_cvt_pk_bf16_f32 v65, 0, v65\n"
        "v_mfma_f32_16x16x32_bf16 v[48:51], v[176:179], a[200:203], v[48:51]\n"
        "s_barrier\n"
        "v_mfma_f32_16x16x32_bf16 v[52:55], v[188:191], a[204:207], v[52:55]\n"
        "ds_read_b128 a[0:3], v254 offset:49152\n"
        "global_load_lds_dwordx4 v254, s[60:61] offset:3072 sc1\n"
        "v_mfma_f32_16x16x32_bf16 v[48:51], v[184:187], a[204:207], v[48:51]\n"
        "ds_read_b128 a[204:207], v254 offset:63488\n"
        "v_lshlrev_b32 v248, 16, v129\n"
        "v_mfma_f32_16x16x32_bf16 v[52:55], v[180:183], a[200:203], v[52:55]\n"
        "ds_read_b128 a[200:203], v254 offset:59392\n"
        "v_mul_f32 v66, v66, v248\n"
        "v_mfma_f32_16x16x32_bf16 v[52:55], v[172:175], a[196:199], v[52:55]\n"
        "ds_read_b128 a[196:199], v254 offset:55296\n"
        "v_cvt_pk_bf16_f32 v66, 0, v66\n"
        "v_mfma_f32_16x16x32_bf16 v[52:55], v[164:167], a[192:195], v[52:55]\n"
        "ds_read_b128 a[192:195], v254 offset:51200\n"
        "v_mul_f32 v66, v66, v228\n"
        "v_mfma_f32_16x16x32_bf16 v[44:47], v[180:183], a[24:27], v[44:47]\n"
        "ds_read_b128 a[4:7], v254 offset:53248\n"
        "v_cvt_pk_bf16_f32 v66, 0, v66\n"
        "v_mfma_f32_16x16x32_bf16 v[44:47], v[188:191], a[28:31], v[44:47]\n"
        "ds_read_b128 a[8:11], v254 offset:57344\n"
        "v_and_b32 v249, s35, v129\n"
        "v_mfma_f32_16x16x32_bf16 v[60:63], v[164:167], a[208:211], v[60:63]\n"
        "ds_read_b128 a[12:15], v254 offset:61440\n"
        "v_mul_f32 v67, v67, v249\n"
        "v_mfma_f32_16x16x32_bf16 v[60:63], v[172:175], a[212:215], v[60:63]\n"
        "ds_read_b128 a[16:19], v254 offset:50176\n"
        "v_cvt_pk_bf16_f32 v67, 0, v67\n"
        "v_mfma_f32_16x16x32_bf16 v[60:63], v[180:183], a[216:219], v[60:63]\n"
        "s_waitcnt lgkmcnt(7)\n"
        "ds_read_b128 a[20:23], v254 offset:54272\n"
        "v_mfma_f32_16x16x32_bf16 v[52:55], v[220:223], a[204:207], v[52:55]\n"
        "s_waitcnt lgkmcnt(7)\n"
        "ds_read_b128 a[24:27], v254 offset:58368\n"
        "v_mfma_f32_16x16x32_bf16 v[52:55], v[212:215], a[200:203], v[52:55]\n"
        "s_waitcnt lgkmcnt(7)\n"
        "ds_read_b128 a[28:31], v254 offset:62464\n"
        "v_mfma_f32_16x16x32_bf16 v[52:55], v[204:207], a[196:199], v[52:55]\n"
        "s_waitcnt lgkmcnt(7)\n"
        "v_permlane16_swap_b32 v108, v110\n"
        "v_mfma_f32_16x16x32_bf16 v[52:55], v[196:199], a[192:195], v[52:55]\n"
        "v_permlane16_swap_b32 v109, v111\n"
        "v_mfma_f32_16x16x32_bf16 v[60:63], v[188:191], a[220:223], v[60:63]\n"
        "global_load_dwordx2 v[156:157], v155, s[48:49] sc1\n"
        "v_mul_f32 v67, v67, v228\n"
        "v_mfma_f32_16x16x32_bf16 v[56:59], v[184:187], a[220:223], v[56:59]\n"
        "ds_read_b128 a[220:223], v254 offset:64512\n"
        "v_cvt_pk_bf16_f32 v67, 0, v67\n"
        "v_mfma_f32_16x16x32_bf16 v[56:59], v[176:179], a[216:219], v[56:59]\n"
        "ds_read_b128 a[216:219], v254 offset:60416\n"
        "v_lshlrev_b32 v144, 16, v130\n"
        "v_mfma_f32_16x16x32_bf16 v[56:59], v[168:171], a[212:215], v[56:59]\n"
        "ds_read_b128 a[212:215], v254 offset:56320\n"
        "v_mul_f32 v68, v68, v144\n"
        "v_mfma_f32_16x16x32_bf16 v[56:59], v[160:163], a[208:211], v[56:59]\n"
        "ds_read_b128 a[208:211], v254 offset:52224\n"
        "v_cvt_pk_bf16_f32 v68, 0, v68\n"
        "v_mfma_f32_16x16x32_bf16 v[32:35], v[192:195], a[0:3], v[32:35]\n"
        "s_waitcnt lgkmcnt(5)\n"
        "v_mul_f32 v68, v68, v228\n"
        "v_cvt_pk_bf16_f32 v68, 0, v68\n"
        "v_mfma_f32_16x16x32_bf16 v[32:35], v[200:203], a[4:7], v[32:35]\n"
        "v_and_b32 v145, s35, v130\n"
        "v_mul_f32 v69, v69, v145\n"
        "v_mfma_f32_16x16x32_bf16 v[32:35], v[208:211], a[8:11], v[32:35]\n"
        "v_cvt_pk_bf16_f32 v69, 0, v69\n"
        "v_mul_f32 v69, v69, v228\n"
        "v_mfma_f32_16x16x32_bf16 v[32:35], v[216:219], a[12:15], v[32:35]\n"
        "v_cvt_pk_bf16_f32 v69, 0, v69\n"
        "v_lshlrev_b32 v146, 16, v131\n"
        "v_mfma_f32_16x16x32_bf16 v[36:39], v[220:223], a[12:15], v[36:39]\n"
        "v_mul_f32 v70, v70, v146\n"
        "v_cvt_pk_bf16_f32 v70, 0, v70\n"
        "v_mfma_f32_16x16x32_bf16 v[36:39], v[212:215], a[8:11], v[36:39]\n"
        "v_mul_f32 v70, v70, v228\n"
        "v_cvt_pk_bf16_f32 v70, 0, v70\n"
        "v_mfma_f32_16x16x32_bf16 v[36:39], v[204:207], a[4:7], v[36:39]\n"
        "v_and_b32 v147, s35, v131\n"
        "v_mul_f32 v71, v71, v147\n"
        "v_mfma_f32_16x16x32_bf16 v[36:39], v[196:199], a[0:3], v[36:39]\n"
        "s_waitcnt lgkmcnt(0)\n"
        "v_cvt_pk_bf16_f32 v71, 0, v71\n"
        "v_mul_f32 v71, v71, v228\n"
        "v_mfma_f32_16x16x32_bf16 v[44:47], v[196:199], a[16:19], v[44:47]\n"
        "v_cvt_pk_bf16_f32 v71, 0, v71\n"
        "v_lshlrev_b32 v246, 16, v132\n"
        "v_mfma_f32_16x16x32_bf16 v[44:47], v[204:207], a[20:23], v[44:47]\n"
        "v_and_b32 v247, s35, v132\n"
        "v_lshlrev_b32 v248, 16, v133\n"
        "v_mfma_f32_16x16x32_bf16 v[44:47], v[212:215], a[24:27], v[44:47]\n"
        "v_and_b32 v249, s35, v133\n"
        "v_mul_f32 v72, v72, v246\n"
        "v_mfma_f32_16x16x32_bf16 v[44:47], v[220:223], a[28:31], v[44:47]\n"
        "v_mul_f32 v73, v73, v247\n"
        "v_mul_f32 v74, v74, v248\n"
        "v_mfma_f32_16x16x32_bf16 v[40:43], v[216:219], a[28:31], v[40:43]\n"
        "v_mul_f32 v75, v75, v249\n"
        "v_cvt_pk_bf16_f32 v72, 0, v72\n"
        "v_mfma_f32_16x16x32_bf16 v[40:43], v[208:211], a[24:27], v[40:43]\n"
        "v_cvt_pk_bf16_f32 v73, 0, v73\n"
        "v_cvt_pk_bf16_f32 v74, 0, v74\n"
        "v_mfma_f32_16x16x32_bf16 v[40:43], v[200:203], a[20:23], v[40:43]\n"
        "v_cvt_pk_bf16_f32 v75, 0, v75\n"
        "v_mul_f32 v72, v72, v229\n"
        "v_mfma_f32_16x16x32_bf16 v[40:43], v[192:195], a[16:19], v[40:43]\n"
        "v_mul_f32 v73, v73, v229\n"
        "v_mul_f32 v74, v74, v229\n"
        "v_mfma_f32_16x16x32_bf16 v[48:51], v[192:195], a[192:195], v[48:51]\n"
        "s_barrier\n"
        "v_mfma_f32_16x16x32_bf16 v[48:51], v[200:203], a[196:199], v[48:51]\n"
        "ds_read_b128 a[0:3], v244 offset:32768\n"
        "v_mul_f32 v75, v75, v229\n"
        "v_mfma_f32_16x16x32_bf16 v[48:51], v[208:211], a[200:203], v[48:51]\n"
        "ds_read_b128 a[4:7], v244 offset:36864\n"
        "v_cvt_pk_bf16_f32 v72, 0, v72\n"
        "v_mfma_f32_16x16x32_bf16 v[48:51], v[216:219], a[204:207], v[48:51]\n"
        "ds_read_b128 a[8:11], v244 offset:40960\n"
        "v_cvt_pk_bf16_f32 v73, 0, v73\n"
        "v_mfma_f32_16x16x32_bf16 v[60:63], v[196:199], a[208:211], v[60:63]\n"
        "ds_read_b128 a[12:15], v244 offset:45056\n"
        "v_cvt_pk_bf16_f32 v74, 0, v74\n"
        "v_mfma_f32_16x16x32_bf16 v[60:63], v[204:207], a[212:215], v[60:63]\n"
        "ds_read_b128 a[16:19], v244 offset:33792\n"
        "v_cvt_pk_bf16_f32 v75, 0, v75\n"
        "v_mfma_f32_16x16x32_bf16 v[60:63], v[212:215], a[216:219], v[60:63]\n"
        "ds_read_b128 a[20:23], v244 offset:37888\n"
        "v_lshlrev_b32 v144, 16, v134\n"
        "v_mfma_f32_16x16x32_bf16 v[60:63], v[220:223], a[220:223], v[60:63]\n"
        "ds_read_b128 a[24:27], v244 offset:41984\n"
        "v_and_b32 v145, s35, v134\n"
        "v_mfma_f32_16x16x32_bf16 v[56:59], v[216:219], a[220:223], v[56:59]\n"
        "ds_read_b128 a[28:31], v244 offset:46080\n"
        "v_lshlrev_b32 v146, 16, v135\n"
        "v_mfma_f32_16x16x32_bf16 v[56:59], v[208:211], a[216:219], v[56:59]\n"
        "ds_read_b128 a[192:195], v244 offset:34816\n"
        "v_and_b32 v147, s35, v135\n"
        "v_mfma_f32_16x16x32_bf16 v[56:59], v[200:203], a[212:215], v[56:59]\n"
        "ds_read_b128 a[196:199], v244 offset:38912\n"
        "v_mul_f32 v76, v76, v144\n"
        "v_mfma_f32_16x16x32_bf16 v[56:59], v[192:195], a[208:211], v[56:59]\n"
        "ds_read_b128 a[200:203], v244 offset:43008\n"
        "s_waitcnt lgkmcnt(8)\n"
        "v_mfma_f32_16x16x32_bf16 v[32:35], v[112:115], a[0:3], v[32:35]\n"
        "ds_read_b128 a[204:207], v244 offset:47104\n"
        "v_mul_f32 v77, v77, v145\n"
        "v_mfma_f32_16x16x32_bf16 v[32:35], v[120:123], a[4:7], v[32:35]\n"
        "ds_read_b128 a[208:211], v244 offset:35840\n"
        "v_mul_f32 v78, v78, v146\n"
        "v_mfma_f32_16x16x32_bf16 v[32:35], a[64:67], a[8:11], v[32:35]\n"
        "ds_read_b128 a[212:215], v244 offset:39936\n"
        "s_waitcnt lgkmcnt(8)\n"
        "v_mfma_f32_16x16x32_bf16 v[32:35], a[72:75], a[12:15], v[32:35]\n"
        "ds_read_b128 a[216:219], v244 offset:44032\n"
        "v_mul_f32 v79, v79, v147\n"
        "v_mfma_f32_16x16x32_bf16 v[36:39], a[76:79], a[12:15], v[36:39]\n"
        "ds_read_b128 a[220:223], v244 offset:48128\n"
        "v_cvt_pk_bf16_f32 v76, 0, v76\n"
        "v_mfma_f32_16x16x32_bf16 v[36:39], a[68:71], a[8:11], v[36:39]\n"
        "s_waitcnt vmcnt(0) lgkmcnt(7)\n"
        "v_cmp_ne_u64 vcc, v[156:157], v[250:251]\n"
        "v_cvt_pk_bf16_f32 v77, 0, v77\n"
        "v_mfma_f32_16x16x32_bf16 v[36:39], v[124:127], a[4:7], v[36:39]\n"
        "s_cbranch_vccnz LPPR_%=\n"
        "LBPR_%=:\n"
        "global_load_dwordx4 a[240:243], v254, s[30:31] offset:0 sc1\n"
        "v_mfma_f32_16x16x32_bf16 v[36:39], v[116:119], a[0:3], v[36:39]\n"
        "v_cvt_pk_bf16_f32 v78, 0, v78\n"
        "v_cvt_pk_bf16_f32 v79, 0, v79\n"
        "v_mfma_f32_16x16x32_bf16 v[44:47], v[116:119], a[16:19], v[44:47]\n"
        "global_load_dwordx4 a[244:247], v254, s[30:31] offset:1024 sc1\n"
        "v_mfma_f32_16x16x32_bf16 v[44:47], v[124:127], a[20:23], v[44:47]\n"
        "v_mul_f32 v76, v76, v229\n"
        "v_mul_f32 v77, v77, v229\n"
        "v_mfma_f32_16x16x32_bf16 v[44:47], a[68:71], a[24:27], v[44:47]\n"
        "global_load_dwordx4 a[248:251], v254, s[30:31] offset:2048 sc1\n"
        "v_mfma_f32_16x16x32_bf16 v[44:47], a[76:79], a[28:31], v[44:47]\n"
        "v_mul_f32 v78, v78, v229\n"
        "v_mul_f32 v79, v79, v229\n"
        "v_mfma_f32_16x16x32_bf16 v[40:43], a[72:75], a[28:31], v[40:43]\n"
        "global_load_dwordx4 a[252:255], v254, s[30:31] offset:3072 sc1\n"
        "v_mfma_f32_16x16x32_bf16 v[40:43], a[64:67], a[24:27], v[40:43]\n"
        "s_add_u32 s30, s38, s16\n"
        "s_addc_u32 s31, s39, 0\n"
        "s_mul_i32 s16, s13, 263168\n"
        "v_mfma_f32_16x16x32_bf16 v[40:43], v[120:123], a[20:23], v[40:43]\n"
        "global_load_dwordx4 v[0:3], v254, s[30:31] offset:0 sc1\n"
        "s_add_u32 s16, s16, s15\n"
        "v_mfma_f32_16x16x32_bf16 v[40:43], v[112:115], a[16:19], v[40:43]\n"
        "s_add_u32 s16, s16, s73\n"
        "s_add_u32 s16, s16, 0x4000000\n"
        "v_mfma_f32_16x16x32_bf16 v[48:51], v[112:115], a[192:195], v[48:51]\n"
        "s_waitcnt lgkmcnt(0)\n"
        "global_load_dwordx4 v[4:7], v254, s[30:31] offset:1024 sc1\n"
        "v_mfma_f32_16x16x32_bf16 v[48:51], v[120:123], a[196:199], v[48:51]\n"
        "s_barrier\n"
        "v_mfma_f32_16x16x32_bf16 v[48:51], a[64:67], a[200:203], v[48:51]\n"
        "ds_read_b128 a[0:3], v244 offset:49152\n"
        "global_load_dwordx4 v[8:11], v254, s[30:31] offset:2048 sc1\n"
        "v_mfma_f32_16x16x32_bf16 v[48:51], a[72:75], a[204:207], v[48:51]\n"
        "ds_read_b128 a[4:7], v244 offset:53248\n"
        "v_cvt_pk_bf16_f32 v76, 0, v76\n"
        "v_mfma_f32_16x16x32_bf16 v[52:55], a[76:79], a[204:207], v[52:55]\n"
        "ds_read_b128 a[8:11], v244 offset:57344\n"
        "global_load_dwordx4 v[12:15], v254, s[30:31] offset:3072 sc1\n"
        "v_mfma_f32_16x16x32_bf16 v[52:55], a[68:71], a[200:203], v[52:55]\n"
        "ds_read_b128 a[12:15], v244 offset:61440\n"
        "LHB3_%=:\n"
        "s_cbranch_execz LHRB_%=\n"
        "s_add_u32 s30, s38, s16\n"
        "s_addc_u32 s31, s39, 0\n"
        "v_mfma_f32_16x16x32_bf16 v[52:55], v[124:127], a[196:199], v[52:55]\n"
        "ds_read_b128 a[16:19], v244 offset:50176\n"
        "global_load_dwordx4 v[16:19], v254, s[30:31] offset:0 sc1\n"
        "v_mfma_f32_16x16x32_bf16 v[52:55], v[116:119], a[192:195], v[52:55]\n"
        "ds_read_b128 a[20:23], v244 offset:54272\n"
        "v_cvt_pk_bf16_f32 v77, 0, v77\n"
        "v_mfma_f32_16x16x32_bf16 v[60:63], v[116:119], a[208:211], v[60:63]\n"
        "ds_read_b128 a[24:27], v244 offset:58368\n"
        "global_load_dwordx4 v[20:23], v254, s[30:31] offset:1024 sc1\n"
        "v_mfma_f32_16x16x32_bf16 v[60:63], v[124:127], a[212:215], v[60:63]\n"
        "ds_read_b128 a[28:31], v244 offset:62464\n"
        "v_cvt_pk_bf16_f32 v78, 0, v78\n"
        "v_mfma_f32_16x16x32_bf16 v[60:63], a[68:71], a[216:219], v[60:63]\n"
        "ds_read_b128 a[192:195], v244 offset:51200\n"
        "global_load_dwordx4 v[24:27], v254, s[30:31] offset:2048 sc1\n"
        "v_mfma_f32_16x16x32_bf16 v[60:63], a[76:79], a[220:223], v[60:63]\n"
        "ds_read_b128 a[196:199], v244 offset:55296\n"
        "v_cvt_pk_bf16_f32 v79, 0, v79\n"
        "v_mfma_f32_16x16x32_bf16 v[56:59], a[72:75], a[220:223], v[56:59]\n"
        "ds_read_b128 a[200:203], v244 offset:59392\n"
        "global_load_dwordx4 v[28:31], v254, s[30:31] offset:3072 sc1\n"
        "v_mfma_f32_16x16x32_bf16 v[56:59], a[64:67], a[216:219], v[56:59]\n"
        "ds_read_b128 a[204:207], v244 offset:63488\n"
        "v_lshlrev_b32 v246, 16, v136\n"
        "v_mfma_f32_16x16x32_bf16 v[56:59], v[120:123], a[212:215], v[56:59]\n"
        "ds_read_b128 a[212:215], v244 offset:56320\n"
        "v_and_b32 v247, s35, v136\n"
        "v_mfma_f32_16x16x32_bf16 v[56:59], v[112:115], a[208:211], v[56:59]\n"
        "ds_read_b128 a[208:211], v244 offset:52224\n"
        "s_waitcnt lgkmcnt(8)\n"
        "v_mfma_f32_16x16x32_bf16 v[32:35], a[80:83], a[0:3], v[32:35]\n"
        "ds_read_b128 a[216:219], v244 offset:60416\n"
        "v_lshlrev_b32 v248, 16, v137\n"
        "v_mfma_f32_16x16x32_bf16 v[32:35], a[88:91], a[4:7], v[32:35]\n"
        "ds_read_b128 a[220:223], v244 offset:64512\n"
        "v_and_b32 v249, s35, v137\n"
        "v_mfma_f32_16x16x32_bf16 v[32:35], a[160:163], a[8:11], v[32:35]\n"
        "v_mul_f32 v80, v80, v246\n"
        "v_mul_f32 v81, v81, v247\n"
        "v_mfma_f32_16x16x32_bf16 v[32:35], a[168:171], a[12:15], v[32:35]\n"
        "v_mul_f32 v82, v82, v248\n"
        "v_mul_f32 v83, v83, v249\n"
        "v_mfma_f32_16x16x32_bf16 v[36:39], a[172:175], a[12:15], v[36:39]\n"
        "v_cvt_pk_bf16_f32 v80, 0, v80\n"
        "v_cvt_pk_bf16_f32 v81, 0, v81\n"
        "v_mfma_f32_16x16x32_bf16 v[36:39], a[164:167], a[8:11], v[36:39]\n"
        "v_cvt_pk_bf16_f32 v82, 0, v82\n"
        "v_cvt_pk_bf16_f32 v83, 0, v83\n"
        "v_mfma_f32_16x16x32_bf16 v[36:39], a[92:95], a[4:7], v[36:39]\n"
        "s_waitcnt lgkmcnt(3)\n"
        "v_mul_f32 v80, v80, v230\n"
        "v_mul_f32 v81, v81, v230\n"
        "v_mfma_f32_16x16x32_bf16 v[36:39], a[84:87], a[0:3], v[36:39]\n"
        "v_mul_f32 v82, v82, v230\n"
        "v_mul_f32 v83, v83, v230\n"
        "v_mfma_f32_16x16x32_bf16 v[44:47], a[84:87], a[16:19], v[44:47]\n"
        "v_cvt_pk_bf16_f32 v80, 0, v80\n"
        "v_cvt_pk_bf16_f32 v81, 0, v81\n"
        "v_mfma_f32_16x16x32_bf16 v[44:47], a[92:95], a[20:23], v[44:47]\n"
        "v_cvt_pk_bf16_f32 v82, 0, v82\n"
        "v_cvt_pk_bf16_f32 v83, 0, v83\n"
        "v_mfma_f32_16x16x32_bf16 v[44:47], a[164:167], a[24:27], v[44:47]\n"
        "v_lshlrev_b32 v144, 16, v138\n"
        "v_and_b32 v145, s35, v138\n"
        "v_mfma_f32_16x16x32_bf16 v[44:47], a[172:175], a[28:31], v[44:47]\n"
        "v_lshlrev_b32 v146, 16, v139\n"
        "v_and_b32 v147, s35, v139\n"
        "v_mfma_f32_16x16x32_bf16 v[40:43], a[168:171], a[28:31], v[40:43]\n"
        "v_mul_f32 v84, v84, v144\n"
        "v_mul_f32 v85, v85, v145\n"
        "v_mfma_f32_16x16x32_bf16 v[40:43], a[160:163], a[24:27], v[40:43]\n"
        "v_mul_f32 v86, v86, v146\n"
        "v_mul_f32 v87, v87, v147\n"
        "v_mfma_f32_16x16x32_bf16 v[40:43], a[88:91], a[20:23], v[40:43]\n"
        "v_cvt_pk_bf16_f32 v84, 0, v84\n"
        "v_cvt_pk_bf16_f32 v85, 0, v85\n"
        "v_mfma_f32_16x16x32_bf16 v[40:43], a[80:83], a[16:19], v[40:43]\n"
        "v_cvt_pk_bf16_f32 v86, 0, v86\n"
        "v_cvt_pk_bf16_f32 v87, 0, v87\n"
        "v_mfma_f32_16x16x32_bf16 v[48:51], a[80:83], a[192:195], v[48:51]\n"
        "s_barrier\n"
        "v_mfma_f32_16x16x32_bf16 v[48:51], a[88:91], a[196:199], v[48:51]\n"
        "ds_read_b128 a[0:3], v245 offset:0\n"
        "v_mul_f32 v84, v84, v230\n"
        "v_mfma_f32_16x16x32_bf16 v[48:51], a[160:163], a[200:203], v[48:51]\n"
        "ds_read_b128 a[4:7], v245 offset:4096\n"
        "v_mul_f32 v85, v85, v230\n"
        "v_mfma_f32_16x16x32_bf16 v[48:51], a[168:171], a[204:207], v[48:51]\n"
        "ds_read_b128 a[8:11], v245 offset:8192\n"
        "v_mul_f32 v86, v86, v230\n"
        "v_mfma_f32_16x16x32_bf16 v[52:55], a[172:175], a[204:207], v[52:55]\n"
        "ds_read_b128 a[12:15], v245 offset:12288\n"
        "v_mul_f32 v87, v87, v230\n"
        "v_mfma_f32_16x16x32_bf16 v[52:55], a[164:167], a[200:203], v[52:55]\n"
        "ds_read_b128 a[200:203], v245 offset:9216\n"
        "v_cvt_pk_bf16_f32 v84, 0, v84\n"
        "v_mfma_f32_16x16x32_bf16 v[52:55], a[92:95], a[196:199], v[52:55]\n"
        "ds_read_b128 a[196:199], v245 offset:5120\n"
        "v_cvt_pk_bf16_f32 v85, 0, v85\n"
        "v_mfma_f32_16x16x32_bf16 v[52:55], a[84:87], a[192:195], v[52:55]\n"
        "ds_read_b128 a[192:195], v245 offset:1024\n"
        "s_waitcnt lgkmcnt(7)\n"
        "v_mfma_f32_16x16x32_bf16 v[60:63], a[84:87], a[208:211], v[60:63]\n"
        "ds_read_b128 a[204:207], v245 offset:13312\n"
        "v_mfma_f32_16x16x32_bf16 v[60:63], a[92:95], a[212:215], v[60:63]\n"
        "s_barrier\n"
        "v_mfma_f32_16x16x32_bf16 v[60:63], a[164:167], a[216:219], v[60:63]\n"
        "ds_read_b128 a[16:19], v245 offset:16384\n"
        "v_cvt_pk_bf16_f32 v86, 0, v86\n"
        "v_mfma_f32_16x16x32_bf16 v[60:63], a[172:175], a[220:223], v[60:63]\n"
        "ds_read_b128 a[20:23], v245 offset:20480\n"
        "v_cvt_pk_bf16_f32 v87, 0, v87\n"
        "v_mfma_f32_16x16x32_bf16 v[56:59], a[168:171], a[220:223], v[56:59]\n"
        "ds_read_b128 a[24:27], v245 offset:24576\n"
        "v_lshlrev_b32 v88, 16, v108\n"
        "v_mfma_f32_16x16x32_bf16 v[56:59], a[160:163], a[216:219], v[56:59]\n"
        "ds_read_b128 a[28:31], v245 offset:28672\n"
        "v_and_b32 v89, s35, v108\n"
        "v_mfma_f32_16x16x32_bf16 v[56:59], a[88:91], a[212:215], v[56:59]\n"
        "ds_read_b128 a[212:215], v245 offset:21504\n"
        "v_lshlrev_b32 v90, 16, v109\n"
        "v_mfma_f32_16x16x32_bf16 v[56:59], a[80:83], a[208:211], v[56:59]\n"
        "ds_read_b128 a[208:211], v245 offset:17408\n"
        "s_waitcnt lgkmcnt(7)\n"
        "v_mfma_f32_16x16x32_bf16 v[32:35], a[176:179], a[0:3], v[32:35]\n"
        "ds_read_b128 a[216:219], v245 offset:25600\n"
        "v_and_b32 v91, s35, v109\n"
        "v_mfma_f32_16x16x32_bf16 v[32:35], a[184:187], a[4:7], v[32:35]\n"
        "ds_read_b128 a[220:223], v245 offset:29696\n"
        "v_lshlrev_b32 v246, 16, v140\n"
        "v_mfma_f32_16x16x32_bf16 v[32:35], a[32:35], a[8:11], v[32:35]\n"
        "v_and_b32 v247, s35, v140\n"
        "v_lshlrev_b32 v248, 16, v141\n"
        "v_mfma_f32_16x16x32_bf16 v[32:35], a[40:43], a[12:15], v[32:35]\n"
        "v_and_b32 v249, s35, v141\n"
        "v_mul_f32 v88, v88, v246\n"
        "v_mfma_f32_16x16x32_bf16 v[36:39], a[44:47], a[12:15], v[36:39]\n"
        "ds_read_b128 a[12:15], v245 offset:14336\n"
        "v_mul_f32 v89, v89, v247\n"
        "v_mfma_f32_16x16x32_bf16 v[36:39], a[36:39], a[8:11], v[36:39]\n"
        "ds_read_b128 a[8:11], v245 offset:10240\n"
        "v_mul_f32 v90, v90, v248\n"
        "v_mfma_f32_16x16x32_bf16 v[36:39], a[188:191], a[4:7], v[36:39]\n"
        "ds_read_b128 a[4:7], v245 offset:6144\n"
        "v_mul_f32 v91, v91, v249\n"
        "v_mfma_f32_16x16x32_bf16 v[36:39], a[180:183], a[0:3], v[36:39]\n"
        "ds_read_b128 a[0:3], v245 offset:2048\n"
        "s_waitcnt lgkmcnt(6)\n"
        "v_mfma_f32_16x16x32_bf16 v[32:35], a[48:51], a[16:19], v[32:35]\n"
        "v_cvt_pk_bf16_f32 v88, 0, v88\n"
        "v_cvt_pk_bf16_f32 v89, 0, v89\n"
        "v_mfma_f32_16x16x32_bf16 v[32:35], a[56:59], a[20:23], v[32:35]\n"
        "v_cvt_pk_bf16_f32 v90, 0, v90\n"
        "v_cvt_pk_bf16_f32 v91, 0, v91\n"
        "v_mfma_f32_16x16x32_bf16 v[32:35], a[224:227], a[24:27], v[32:35]\n"
        "v_mul_f32 v88, v88, v231\n"
        "v_mul_f32 v89, v89, v231\n"
        "v_mfma_f32_16x16x32_bf16 v[32:35], a[232:235], a[28:31], v[32:35]\n"
        "v_mul_f32 v90, v90, v231\n"
        "v_mul_f32 v91, v91, v231\n"
        "v_mfma_f32_16x16x32_bf16 v[36:39], a[236:239], a[28:31], v[36:39]\n"
        "ds_read_b128 a[28:31], v245 offset:30720\n"
        "v_cvt_pk_bf16_f32 v88, 0, v88\n"
        "v_mfma_f32_16x16x32_bf16 v[36:39], a[228:231], a[24:27], v[36:39]\n"
        "ds_read_b128 a[24:27], v245 offset:26624\n"
        "v_cvt_pk_bf16_f32 v89, 0, v89\n"
        "v_mfma_f32_16x16x32_bf16 v[36:39], a[60:63], a[20:23], v[36:39]\n"
        "ds_read_b128 a[20:23], v245 offset:22528\n"
        "v_cvt_pk_bf16_f32 v90, 0, v90\n"
        "v_mfma_f32_16x16x32_bf16 v[36:39], a[52:55], a[16:19], v[36:39]\n"
        "ds_read_b128 a[16:19], v245 offset:18432\n"
        "s_waitcnt vmcnt(11) lgkmcnt(4)\n"
        "v_mfma_f32_16x16x32_bf16 v[32:35], v[240:243], a[240:243], v[32:35]\n"
        "v_cvt_pk_bf16_f32 v91, 0, v91\n"
        "v_lshlrev_b32 v92, 16, v110\n"
        "v_mfma_f32_16x16x32_bf16 v[36:39], v[238:241], a[240:243], v[36:39]\n"
        "s_waitcnt vmcnt(7)\n"
        "v_and_b32 v93, s35, v110\n"
        "v_lshlrev_b32 v94, 16, v111\n"
        "v_mfma_f32_16x16x32_bf16 v[32:35], v[240:243], v[0:3], v[32:35]\n"
        "v_and_b32 v95, s35, v111\n"
        "v_lshlrev_b32 v144, 16, v142\n"
        "v_mfma_f32_16x16x32_bf16 v[36:39], v[238:241], v[0:3], v[36:39]\n"
        "v_and_b32 v145, s35, v142\n"
        "v_lshlrev_b32 v146, 16, v143\n"
        "v_mfma_f32_16x16x32_bf16 v[40:43], a[176:179], a[192:195], v[40:43]\n"
        "v_and_b32 v147, s35, v143\n"
        "s_waitcnt vmcnt(3) lgkmcnt(3)\n"
        "v_mfma_f32_16x16x32_bf16 v[32:35], v[240:243], v[16:19], v[32:35]\n"
        "v_mul_f32 v92, v92, v144\n"
        "v_mul_f32 v93, v93, v145\n"
        "v_mfma_f32_16x16x32_bf16 v[36:39], v[238:241], v[16:19], v[36:39]\n"
        "v_mul_f32 v94, v94, v146\n"
        "v_mul_f32 v95, v95, v147\n"
        "v_mfma_f32_16x16x32_bf16 v[40:43], a[184:187], a[196:199], v[40:43]\n"
        "v_cvt_pk_bf16_f32 v92, 0, v92\n"
        "v_cvt_pk_bf16_f32 v93, 0, v93\n"
        "v_mfma_f32_16x16x32_bf16 v[40:43], a[32:35], a[200:203], v[40:43]\n"
        "v_fmac_f32 v64, v32, v224\n"
        "v_fmac_f32 v65, v33, v224\n"
        "v_mfma_f32_16x16x32_bf16 v[40:43], a[40:43], a[204:207], v[40:43]\n"
        "v_fmac_f32 v66, v34, v224\n"
        "v_fmac_f32 v67, v35, v224\n"
        "v_mfma_f32_16x16x32_bf16 v[44:47], a[44:47], a[204:207], v[44:47]\n"
        "ds_read_b128 a[204:207], v245 offset:15360\n"
        "v_fmac_f32 v68, v36, v224\n"
        "v_mfma_f32_16x16x32_bf16 v[44:47], a[36:39], a[200:203], v[44:47]\n"
        "ds_read_b128 a[200:203], v245 offset:11264\n"
        "v_fmac_f32 v69, v37, v224\n"
        "v_mfma_f32_16x16x32_bf16 v[44:47], a[188:191], a[196:199], v[44:47]\n"
        "ds_read_b128 a[196:199], v245 offset:7168\n"
        "v_fmac_f32 v70, v38, v224\n"
        "v_mfma_f32_16x16x32_bf16 v[44:47], a[180:183], a[192:195], v[44:47]\n"
        "ds_read_b128 a[192:195], v245 offset:3072\n"
        "v_fmac_f32 v71, v39, v224\n"
        "v_mfma_f32_16x16x32_bf16 v[40:43], a[48:51], a[208:211], v[40:43]\n"
        "v_cvt_pk_bf16_f32 v64, v64, v65\n"
        "v_cvt_pk_bf16_f32 v65, v66, v67\n"
        "v_mfma_f32_16x16x32_bf16 v[40:43], a[56:59], a[212:215], v[40:43]\n"
        "v_cvt_pk_bf16_f32 v66, v68, v69\n"
        "v_cvt_pk_bf16_f32 v67, v70, v71\n"
        "v_mfma_f32_16x16x32_bf16 v[40:43], a[224:227], a[216:219], v[40:43]\n"
        "v_permlane16_swap_b32 v64, v66\n"
        "v_mfma_f32_16x16x32_bf16 v[40:43], a[232:235], a[220:223], v[40:43]\n"
        "v_permlane16_swap_b32 v65, v67\n"
        "v_mfma_f32_16x16x32_bf16 v[44:47], a[236:239], a[220:223], v[44:47]\n"
        "global_store_dwordx4 v151, v[64:67], s[56:57] sc0 sc1\n"
        "ds_read_b128 a[220:223], v245 offset:31744\n"
        "s_add_u32 s56, s56, 0x8000\n"
        "s_addc_u32 s57, s57, 0\n"
        "v_cvt_pk_bf16_f32 v94, 0, v94\n"
        "v_cvt_pk_bf16_f32 v95, 0, v95\n"
        "v_mfma_f32_16x16x32_bf16 v[44:47], a[228:231], a[216:219], v[44:47]\n"
        "ds_read_b128 a[216:219], v245 offset:27648\n"
        "v_mul_f32 v92, v92, v231\n"
        "v_mfma_f32_16x16x32_bf16 v[44:47], a[60:63], a[212:215], v[44:47]\n"
        "ds_read_b128 a[212:215], v245 offset:23552\n"
        "v_mul_f32 v93, v93, v231\n"
        "v_mfma_f32_16x16x32_bf16 v[44:47], a[52:55], a[208:211], v[44:47]\n"
        "ds_read_b128 a[208:211], v245 offset:19456\n"
        "v_mul_f32 v94, v94, v231\n"
        "v_mfma_f32_16x16x32_bf16 v[40:43], v[240:243], a[244:247], v[40:43]\n"
        "v_mul_f32 v95, v95, v231\n"
        "v_cvt_pk_bf16_f32 v92, 0, v92\n"
        "v_mfma_f32_16x16x32_bf16 v[44:47], v[238:241], a[244:247], v[44:47]\n"
        "v_cvt_pk_bf16_f32 v93, 0, v93\n"
        "v_cvt_pk_bf16_f32 v94, 0, v94\n"
        "v_mfma_f32_16x16x32_bf16 v[40:43], v[240:243], v[4:7], v[40:43]\n"
        "v_cvt_pk_bf16_f32 v95, 0, v95\n"
        "v_mov_b32 v144, 0\n"
        "v_mfma_f32_16x16x32_bf16 v[44:47], v[238:241], v[4:7], v[44:47]\n"
        "s_waitcnt vmcnt(3) lgkmcnt(4)\n"
        "v_mfma_f32_16x16x32_bf16 v[40:43], v[240:243], v[20:23], v[40:43]\n"
        "v_mfma_f32_16x16x32_bf16 v[44:47], v[238:241], v[20:23], v[44:47]\n"
        "v_mfma_f32_16x16x32_bf16 v[48:51], a[176:179], a[0:3], v[48:51]\n"
        "v_mfma_f32_16x16x32_bf16 v[48:51], a[184:187], a[4:7], v[48:51]\n"
        "v_mfma_f32_16x16x32_bf16 v[48:51], a[32:35], a[8:11], v[48:51]\n"
        "v_mfma_f32_16x16x32_bf16 v[48:51], a[40:43], a[12:15], v[48:51]\n"
        "v_mfma_f32_16x16x32_bf16 v[52:55], a[44:47], a[12:15], v[52:55]\n"
        "v_mfma_f32_16x16x32_bf16 v[52:55], a[36:39], a[8:11], v[52:55]\n"
        "v_mfma_f32_16x16x32_bf16 v[52:55], a[188:191], a[4:7], v[52:55]\n"
        "v_fmac_f32 v72, v40, v225\n"
        "v_fmac_f32 v73, v41, v225\n"
        "v_mfma_f32_16x16x32_bf16 v[52:55], a[180:183], a[0:3], v[52:55]\n"
        "v_fmac_f32 v74, v42, v225\n"
        "v_fmac_f32 v75, v43, v225\n"
        "v_mfma_f32_16x16x32_bf16 v[48:51], a[48:51], a[16:19], v[48:51]\n"
        "v_fmac_f32 v76, v44, v225\n"
        "v_fmac_f32 v77, v45, v225\n"
        "v_mfma_f32_16x16x32_bf16 v[48:51], a[56:59], a[20:23], v[48:51]\n"
        "v_fmac_f32 v78, v46, v225\n"
        "v_fmac_f32 v79, v47, v225\n"
        "v_mfma_f32_16x16x32_bf16 v[48:51], a[224:227], a[24:27], v[48:51]\n"
        "v_cvt_pk_bf16_f32 v72, v72, v73\n"
        "v_cvt_pk_bf16_f32 v73, v74, v75\n"
        "v_mfma_f32_16x16x32_bf16 v[48:51], a[232:235], a[28:31], v[48:51]\n"
        "v_cvt_pk_bf16_f32 v74, v76, v77\n"
        "v_cvt_pk_bf16_f32 v75, v78, v79\n"
        "v_mfma_f32_16x16x32_bf16 v[52:55], a[236:239], a[28:31], v[52:55]\n"
        "v_permlane16_swap_b32 v72, v74\n"
        "v_mfma_f32_16x16x32_bf16 v[52:55], a[228:231], a[24:27], v[52:55]\n"
        "v_permlane16_swap_b32 v73, v75\n"
        "v_mfma_f32_16x16x32_bf16 v[52:55], a[60:63], a[20:23], v[52:55]\n"
        "global_store_dwordx4 v151, v[72:75], s[56:57] sc0 sc1\n"
        "s_add_u32 s56, s56, 0x8000\n"
        "s_addc_u32 s57, s57, 0\n"
        "v_mfma_f32_16x16x32_bf16 v[52:55], a[52:55], a[16:19], v[52:55]\n"
        "v_mfma_f32_16x16x32_bf16 v[48:51], v[240:243], a[248:251], v[48:51]\n"
        "v_mfma_f32_16x16x32_bf16 v[52:55], v[238:241], a[248:251], v[52:55]\n"
        "v_mfma_f32_16x16x32_bf16 v[48:51], v[240:243], v[8:11], v[48:51]\n"
        "v_mfma_f32_16x16x32_bf16 v[52:55], v[238:241], v[8:11], v[52:55]\n"
        "s_waitcnt vmcnt(3) lgkmcnt(0)\n"
        "v_mfma_f32_16x16x32_bf16 v[48:51], v[240:243], v[24:27], v[48:51]\n"
        "v_mfma_f32_16x16x32_bf16 v[52:55], v[238:241], v[24:27], v[52:55]\n"
        "v_mfma_f32_16x16x32_bf16 v[56:59], a[176:179], a[192:195], v[56:59]\n"
        "v_mfma_f32_16x16x32_bf16 v[56:59], a[184:187], a[196:199], v[56:59]\n"
        "v_mfma_f32_16x16x32_bf16 v[56:59], a[32:35], a[200:203], v[56:59]\n"
        "v_mfma_f32_16x16x32_bf16 v[56:59], a[40:43], a[204:207], v[56:59]\n"
        "v_mfma_f32_16x16x32_bf16 v[60:63], a[44:47], a[204:207], v[60:63]\n"
        "v_mfma_f32_16x16x32_bf16 v[60:63], a[36:39], a[200:203], v[60:63]\n"
        "v_mfma_f32_16x16x32_bf16 v[60:63], a[188:191], a[196:199], v[60:63]\n"
        "v_fmac_f32 v80, v48, v226\n"
        "v_fmac_f32 v81, v49, v226\n"
        "v_mfma_f32_16x16x32_bf16 v[60:63], a[180:183], a[192:195], v[60:63]\n"
        "v_fmac_f32 v82, v50, v226\n"
        "v_fmac_f32 v83, v51, v226\n"
        "v_mfma_f32_16x16x32_bf16 v[56:59], a[48:51], a[208:211], v[56:59]\n"
        "v_fmac_f32 v84, v52, v226\n"
        "v_fmac_f32 v85, v53, v226\n"
        "v_mfma_f32_16x16x32_bf16 v[56:59], a[56:59], a[212:215], v[56:59]\n"
        "v_fmac_f32 v86, v54, v226\n"
        "v_fmac_f32 v87, v55, v226\n"
        "v_mfma_f32_16x16x32_bf16 v[56:59], a[224:227], a[216:219], v[56:59]\n"
        "v_cvt_pk_bf16_f32 v80, v80, v81\n"
        "v_cvt_pk_bf16_f32 v81, v82, v83\n"
        "v_mfma_f32_16x16x32_bf16 v[56:59], a[232:235], a[220:223], v[56:59]\n"
        "v_cvt_pk_bf16_f32 v82, v84, v85\n"
        "v_cvt_pk_bf16_f32 v83, v86, v87\n"
        "v_mfma_f32_16x16x32_bf16 v[60:63], a[236:239], a[220:223], v[60:63]\n"
        "v_permlane16_swap_b32 v80, v82\n"
        "v_mfma_f32_16x16x32_bf16 v[60:63], a[228:231], a[216:219], v[60:63]\n"
        "v_permlane16_swap_b32 v81, v83\n"
        "v_mfma_f32_16x16x32_bf16 v[60:63], a[60:63], a[212:215], v[60:63]\n"
        "global_store_dwordx4 v151, v[80:83], s[56:57] sc0 sc1\n"
        "s_add_u32 s56, s56, 0x8000\n"
        "s_addc_u32 s57, s57, 0\n"
        "v_mfma_f32_16x16x32_bf16 v[60:63], a[52:55], a[208:211], v[60:63]\n"
        "v_mfma_f32_16x16x32_bf16 v[56:59], v[240:243], a[252:255], v[56:59]\n"
        "v_mfma_f32_16x16x32_bf16 v[60:63], v[238:241], a[252:255], v[60:63]\n"
        "v_mfma_f32_16x16x32_bf16 v[56:59], v[240:243], v[12:15], v[56:59]\n"
        "v_mfma_f32_16x16x32_bf16 v[60:63], v[238:241], v[12:15], v[60:63]\n"
        "s_waitcnt vmcnt(3)\n"
        "v_mfma_f32_16x16x32_bf16 v[56:59], v[240:243], v[28:31], v[56:59]\n"
        "v_mfma_f32_16x16x32_bf16 v[60:63], v[238:241], v[28:31], v[60:63]\n"
        "s_nop 6\n"
        "v_fmac_f32 v88, v56, v227\n"
        "v_fmac_f32 v89, v57, v227\n"
        "v_fmac_f32 v90, v58, v227\n"
        "v_fmac_f32 v91, v59, v227\n"
        "v_fmac_f32 v92, v60, v227\n"
        "v_fmac_f32 v93, v61, v227\n"
        "v_fmac_f32 v94, v62, v227\n"
        "v_fmac_f32 v95, v63, v227\n"
        "v_cvt_pk_bf16_f32 v88, v88, v89\n"
        "v_cvt_pk_bf16_f32 v89, v90, v91\n"
        "v_cvt_pk_bf16_f32 v90, v92, v93\n"
        "v_cvt_pk_bf16_f32 v91, v94, v95\n"
        "s_nop 0\n"
        "v_permlane16_swap_b32 v88, v90\n"
        "v_permlane16_swap_b32 v89, v91\n"
        "global_store_dwordx4 v151, v[88:91], s[56:57] sc0 sc1\n"
        "s_or_b32 s97, s55, s40\n"
        "s_cbranch_scc1 LEB_%=\n"
        "s_mov_b64 exec, 1\n"
        "global_store_dwordx2 v144, v[250:251], s[48:49] sc1\n"
        "s_mov_b64 exec, -1\n"
        "LEB_%=:\n"
        "; ---- END\n"
        "s_endpgm\n"
        "LPKV_%=:\n"
        "s_mov_b32 s97, 0x200000\n"
        "LQKV_%=:\n"
        "global_load_dwordx2 v[252:253], v245, s[48:49] sc1\n"
        "s_waitcnt vmcnt(0)\n"
        "v_cmp_ne_u64 vcc, v[252:253], v[250:251]\n"
        "s_nop 1\n"
        "s_cbranch_vccz LBKV_%=\n"
        "s_sleep 2\n"
        "s_sub_u32 s97, s97, 1\n"
        "s_cmp_lg_u32 s97, 0\n"
        "s_cbranch_scc1 LQKV_%=\n"
        "s_branch LBKV_%=\n"
        "LE4D_%=:\n"
        "s_mov_b32 s97, 0x200000\n"
        "LE5D_%=:\n"
        "s_sleep 2\n"
        "global_load_dwordx2 v[252:253], v155, s[48:49] sc1\n"
        "s_waitcnt vmcnt(0)\n"
        "v_cmp_ne_u64 vcc, v[252:253], v[250:251]\n"
        "s_nop 1\n"
        "s_cbranch_vccz LEWD_%=\n"
        "s_sub_u32 s97, s97, 1\n"
        "s_cmp_lg_u32 s97, 0\n"
        "s_cbranch_scc1 LE5D_%=\n"
        "s_branch LEWD_%=\n"
        "LRL_%=:\n"
        "s_waitcnt vmcnt(0) lgkmcnt(0)\n"
        "s_nop 7\n"
        "v_accvgpr_write_b32 a176, v96\n"
        "v_accvgpr_write_b32 a177, v97\n"
        "v_accvgpr_write_b32 a178, v98\n"
        "v_accvgpr_write_b32 a179, v99\n"
        "v_accvgpr_write_b32 a180, v100\n"
        "v_accvgpr_write_b32 a181, v101\n"
        "v_accvgpr_write_b32 a182, v102\n"
        "v_accvgpr_write_b32 a183, v103\n"
        "v_accvgpr_write_b32 a184, v104\n"
        "v_accvgpr_write_b32 a185, v105\n"
        "v_accvgpr_write_b32 a186, v106\n"
        "v_accvgpr_write_b32 a187, v107\n"
        "v_accvgpr_write_b32 a188, v108\n"
        "v_accvgpr_write_b32 a189, v109\n"
        "v_accvgpr_write_b32 a190, v110\n"
        "v_accvgpr_write_b32 a191, v111\n"
        "s_mov_b32 s53, s44\n"
        "s_mov_b32 s96, 64\n"
        "s_mov_b32 s52, 0\n"
        "s_branch LREDO_%=\n"
        "LRET0_%=:\n"
        "v_accvgpr_write_b32 a176, v128\n"
        "v_accvgpr_write_b32 a177, v129\n"
        "v_accvgpr_write_b32 a178, v130\n"
        "v_accvgpr_write_b32 a179, v131\n"
        "v_accvgpr_write_b32 a180, v132\n"
        "v_accvgpr_write_b32 a181, v133\n"
        "v_accvgpr_write_b32 a182, v134\n"
        "v_accvgpr_write_b32 a183, v135\n"
        "v_accvgpr_write_b32 a184, v136\n"
        "v_accvgpr_write_b32 a185, v137\n"
        "v_accvgpr_write_b32 a186, v138\n"
        "v_accvgpr_write_b32 a187, v139\n"
        "v_accvgpr_write_b32 a188, v140\n"
        "v_accvgpr_write_b32 a189, v141\n"
        "v_accvgpr_write_b32 a190, v142\n"
        "v_accvgpr_write_b32 a191, v143\n"
        "s_mov_b32 s53, s44\n"
        "s_mov_b32 s96, 0\n"
        "s_mov_b32 s52, 1\n"
        "s_branch LREDO_%=\n"
        "LRET1_%=:\n"
        "s_branch LSL_%=\n"
        "LE4S_%=:\n"
        "s_mov_b32 s97, 0x200000\n"
        "LE5S_%=:\n"
        "s_sleep 2\n"
        "global_load_dwordx2 v[252:253], v155, s[48:49] sc1\n"
        "s_waitcnt vmcnt(0)\n"
        "v_cmp_ne_u64 vcc, v[252:253], v[250:251]\n"
        "s_nop 1\n"
        "s_cbranch_vccz LEWS_%=\n"
        "s_sub_u32 s97, s97, 1\n"
        "s_cmp_lg_u32 s97, 0\n"
        "s_cbranch_scc1 LE5S_%=\n"
        "s_branch LEWS_%=\n"
        "LRH_%=:\n"
        "s_waitcnt vmcnt(0) lgkmcnt(0)\n"
        "s_nop 7\n"
        "v_accvgpr_write_b32 a176, v0\n"
        "v_accvgpr_write_b32 a177, v1\n"
        "v_accvgpr_write_b32 a178, v2\n"
        "v_accvgpr_write_b32 a179, v3\n"
        "v_accvgpr_write_b32 a180, v4\n"
        "v_accvgpr_write_b32 a181, v5\n"
        "v_accvgpr_write_b32 a182, v6\n"
        "v_accvgpr_write_b32 a183, v7\n"
        "v_accvgpr_write_b32 a184, v8\n"
        "v_accvgpr_write_b32 a185, v9\n"
        "v_accvgpr_write_b32 a186, v10\n"
        "v_accvgpr_write_b32 a187, v11\n"
        "v_accvgpr_write_b32 a188, v12\n"
        "v_accvgpr_write_b32 a189, v13\n"
        "v_accvgpr_write_b32 a190, v14\n"
        "v_accvgpr_write_b32 a191, v15\n"
        "s_mov_b32 s53, s45\n"
        "s_mov_b32 s96, 64\n"
        "s_mov_b32 s52, 2\n"
        "s_branch LREDO_%=\n"
        "LRET2_%=:\n"
        "v_accvgpr_write_b32 a176, v64\n"
        "v_accvgpr_write_b32 a177, v65\n"
        "v_accvgpr_write_b32 a178, v66\n"
        "v_accvgpr_write_b32 a179, v67\n"
        "v_accvgpr_write_b32 a180, v68\n"
        "v_accvgpr_write_b32 a181, v69\n"
        "v_accvgpr_write_b32 a182, v70\n"
        "v_accvgpr_write_b32 a183, v71\n"
        "v_accvgpr_write_b32 a184, v72\n"
        "v_accvgpr_write_b32 a185, v73\n"
        "v_accvgpr_write_b32 a186, v74\n"
        "v_accvgpr_write_b32 a187, v75\n"
        "v_accvgpr_write_b32 a188, v76\n"
        "v_accvgpr_write_b32 a189, v77\n"
        "v_accvgpr_write_b32 a190, v78\n"
        "v_accvgpr_write_b32 a191, v79\n"
        "s_mov_b32 s53, s45\n"
        "s_mov_b32 s96, 0\n"
        "s_mov_b32 s52, 3\n"
        "s_branch LREDO_%=\n"
        "LRET3_%=:\n"
        "s_branch LSH_%=\n"
        "LPCX_%=:\n"
        "s_mov_b32 s97, 0x200000\n"
        "LQCX_%=:\n"
        "global_load_dwordx2 v[236:237], v159, s[48:49] sc1\n"
        "s_waitcnt vmcnt(0)\n"
        "v_cmp_ne_u64 vcc, v[236:237], v[250:251]\n"
        "s_nop 1\n"
        "s_cbranch_vccz LBCX_%=\n"
        "s_sleep 2\n"
        "s_sub_u32 s97, s97, 1\n"
        "s_cmp_lg_u32 s97, 0\n"
        "s_cbranch_scc1 LQCX_%=\n"
        "s_branch LBCX_%=\n"
        "LPZZ_%=:\n"
        "s_mov_b32 s97, 0x200000\n"
        "LQZZ_%=:\n"
        "global_load_dwordx2 v[236:237], v234, s[48:49] sc1\n"
        "s_waitcnt vmcnt(0)\n"
        "v_cmp_ne_u64 vcc, v[236:237], v[250:251]\n"
        "s_nop 1\n"
        "s_cbranch_vccz LBZZ_%=\n"
        "s_sleep 2\n"
        "s_sub_u32 s97, s97, 1\n"
        "s_cmp_lg_u32 s97, 0\n"
        "s_cbranch_scc1 LQZZ_%=\n"
        "s_branch LBZZ_%=\n"
        "LPPR_%=:\n"
        "s_mov_b32 s97, 0x200000\n"
        "LQPR_%=:\n"
        "global_load_dwordx2 v[156:157], v155, s[48:49] sc1\n"
        "s_waitcnt vmcnt(0)\n"
        "v_cmp_ne_u64 vcc, v[156:157], v[250:251]\n"
        "s_nop 1\n"
        "s_cbranch_vccz LBPR_%=\n"
        "s_sleep 2\n"
        "s_sub_u32 s97, s97, 1\n"
        "s_cmp_lg_u32 s97, 0\n"
        "s_cbranch_scc1 LQPR_%=\n"
        "s_branch LBPR_%=\n"
        "LREDO_%=:\n"
        "s_lshl2_add_u32 s97, s55, s40\n"
        "s_lshl_b32 s97, s97, 12\n"
        "s_add_u32 s97, s97, 0x5100000\n"
        "s_add_u32 s88, s38, s97\n"
        "s_addc_u32 s89, s39, 0\n"
        "global_store_dwordx4 v254, v[144:147], s[88:89] offset:0\n"
        "global_store_dwordx4 v254, v[148:151], s[88:89] offset:1024\n"
        "global_store_dwordx4 v254, v[152:155], s[88:89] offset:2048\n"
        "global_store_dwordx4 v254, v[156:159], s[88:89] offset:3072\n"
        "s_waitcnt vmcnt(0)\n"
        "v_mov_b32 v112, 0\n"
        "v_mov_b32 v113, 0\n"
        "v_mov_b32 v114, 0\n"
        "v_mov_b32 v115, 0\n"
        "v_mov_b32 v116, 0\n"
        "v_mov_b32 v117, 0\n"
        "v_mov_b32 v118, 0\n"
        "v_mov_b32 v119, 0\n"
        "v_mov_b32 v120, 0\n"
        "v_mov_b32 v121, 0\n"
        "v_mov_b32 v122, 0\n"
        "v_mov_b32 v123, 0\n"
        "v_mov_b32 v124, 0\n"
        "v_mov_b32 v125, 0\n"
        "v_mov_b32 v126, 0\n"
        "v_mov_b32 v127, 0\n"
        "v_mov_b32 v144, 0\n"
        "v_mov_b32 v145, 0\n"
        "v_mov_b32 v146, 0\n"
        "v_mov_b32 v147, 0\n"
        "v_mov_b32 v148, 0\n"
        "v_mov_b32 v149, 0\n"
        "v_mov_b32 v150, 0\n"
        "v_mov_b32 v151, 0\n"
        "v_mov_b32 v152, 0\n"
        "v_mov_b32 v153, 0\n"
        "v_mov_b32 v154, 0\n"
        "v_mov_b32 v155, 0\n"
        "v_mov_b32 v156, 0\n"
        "v_mov_b32 v157, 0\n"
        "v_mov_b32 v158, 0\n"
        "v_mov_b32 v159, 0\n"
        "v_mov_b32 v60, 0\n"
        "v_mov_b32 v61, 0\n"
        "v_mov_b32 v62, 0\n"
        "v_mov_b32 v63, 0\n"
        "v_mov_b32 v24, 0xf149f2ca\n"
        "s_lshl_b32 s97, s53, 7\n"
        "s_add_u32 s97, s97, s96\n"
        "v_add_u32 v27, s97, v243\n"
        "s_mul_i32 s97, s43, 529408\n"
        "s_add_u32 s84, s38, s97\n"
        "s_addc_u32 s85, s39, 0\n"
        "s_add_u32 s84, s84, 0x1000\n"
        "s_addc_u32 s85, s85, 0\n"
        "s_add_u32 s86, s84, 0x1100000\n"
        "s_addc_u32 s87, s85, 0\n"
        "s_lshl_b32 s97, s53, 2\n"
        "s_add_u32 s97, s97, 4\n"
        "LRLOOP_%=:\n"
        "global_load_dwordx4 a[160:163], v254, s[84:85] offset:-4096 sc1\n"
        "global_load_dwordx4 a[164:167], v254, s[84:85] offset:-3072 sc1\n"
        "global_load_dwordx4 a[168:171], v254, s[84:85] offset:-2048 sc1\n"
        "global_load_dwordx4 a[172:175], v254, s[84:85] offset:-1024 sc1\n"
        "s_waitcnt vmcnt(0)\n"
        "v_mfma_f32_16x16x32_bf16 v[40:43], a[160:163], a[176:179], 0\n"
        "v_mfma_f32_16x16x32_bf16 v[40:43], a[164:167], a[180:183], v[40:43]\n"
        "v_mfma_f32_16x16x32_bf16 v[40:43], a[168:171], a[184:187], v[40:43]\n"
        "v_mfma_f32_16x16x32_bf16 v[40:43], a[172:175], a[188:191], v[40:43]\n"
        "s_nop 7\n"
        "global_load_dwordx4 a[160:163], v254, s[84:85] offset:0 sc1\n"
        "global_load_dwordx4 a[164:167], v254, s[84:85] offset:1024 sc1\n"
        "global_load_dwordx4 a[168:171], v254, s[84:85] offset:2048 sc1\n"
        "global_load_dwordx4 a[172:175], v254, s[84:85] offset:3072 sc1\n"
        "s_waitcnt vmcnt(0)\n"
        "v_mfma_f32_16x16x32_bf16 v[44:47], a[160:163], a[176:179], 0\n"
        "v_mfma_f32_16x16x32_bf16 v[44:47], a[164:167], a[180:183], v[44:47]\n"
        "v_mfma_f32_16x16x32_bf16 v[44:47], a[168:171], a[184:187], v[44:47]\n"
        "v_mfma_f32_16x16x32_bf16 v[44:47], a[172:175], a[188:191], v[44:47]\n"
        "s_nop 7\n"
        "v_cmp_gt_i32 vcc, 0, v27\n"
        "s_nop 1\n"
        "v_cndmask_b32 v40, v40, v242, vcc\n"
        "v_cmp_gt_i32 vcc, 1, v27\n"
        "s_nop 1\n"
        "v_cndmask_b32 v41, v41, v242, vcc\n"
        "v_cmp_gt_i32 vcc, 2, v27\n"
        "s_nop 1\n"
        "v_cndmask_b32 v42, v42, v242, vcc\n"
        "v_cmp_gt_i32 vcc, 3, v27\n"
        "s_nop 1\n"
        "v_cndmask_b32 v43, v43, v242, vcc\n"
        "v_cmp_gt_i32 vcc, 16, v27\n"
        "s_nop 1\n"
        "v_cndmask_b32 v44, v44, v242, vcc\n"
        "v_cmp_gt_i32 vcc, 17, v27\n"
        "s_nop 1\n"
        "v_cndmask_b32 v45, v45, v242, vcc\n"
        "v_cmp_gt_i32 vcc, 18, v27\n"
        "s_nop 1\n"
        "v_cndmask_b32 v46, v46, v242, vcc\n"
        "v_cmp_gt_i32 vcc, 19, v27\n"
        "s_nop 1\n"
        "v_cndmask_b32 v47, v47, v242, vcc\n"
        "v_max3_f32 v25, v40, v41, v24\n"
        "v_max3_f32 v25, v42, v43, v25\n"
        "v_max3_f32 v25, v44, v45, v25\n"
        "v_max3_f32 v25, v46, v47, v25\n"
        "v_mov_b32 v28, v25\n"
        "s_nop 1\n"
        "v_permlane16_swap_b32 v25, v28\n"
        "v_max_f32 v25, v25, v28\n"
        "v_mov_b32 v28, v25\n"
        "s_nop 1\n"
        "v_permlane32_swap_b32 v25, v28\n"
        "v_max_f32 v25, v25, v28\n"
        "v_sub_f32 v26, v24, v25\n"
        "v_exp_f32 v26, v26\n"
        "v_mov_b32 v24, v25\n"
        "v_sub_f32 v40, v40, v25\n"
        "v_exp_f32 v40, v40\n"
        "v_sub_f32 v41, v41, v25\n"
        "v_exp_f32 v41, v41\n"
        "v_sub_f32 v42, v42, v25\n"
        "v_exp_f32 v42, v42\n"
        "v_sub_f32 v43, v43, v25\n"
        "v_exp_f32 v43, v43\n"
        "v_sub_f32 v44, v44, v25\n"
        "v_exp_f32 v44, v44\n"
        "v_sub_f32 v45, v45, v25\n"
        "v_exp_f32 v45, v45\n"
        "v_sub_f32 v46, v46, v25\n"
        "v_exp_f32 v46, v46\n"
        "v_sub_f32 v47, v47, v25\n"
        "v_exp_f32 v47, v47\n"
        "v_cvt_pk_bf16_f32 v56, v40, v41\n"
        "v_cvt_pk_bf16_f32 v57, v42, v43\n"
        "v_cvt_pk_bf16_f32 v58, v44, v45\n"
        "v_cvt_pk_bf16_f32 v59, v46, v47\n"
        "v_mul_f32 v112, v112, v26\n"
        "v_mul_f32 v113, v113, v26\n"
        "v_mul_f32 v114, v114, v26\n"
        "v_mul_f32 v115, v115, v26\n"
        "v_mul_f32 v116, v116, v26\n"
        "v_mul_f32 v117, v117, v26\n"
        "v_mul_f32 v118, v118, v26\n"
        "v_mul_f32 v119, v119, v26\n"
        "v_mul_f32 v120, v120, v26\n"
        "v_mul_f32 v121, v121, v26\n"
        "v_mul_f32 v122, v122, v26\n"
        "v_mul_f32 v123, v123, v26\n"
        "v_mul_f32 v124, v124, v26\n"
        "v_mul_f32 v125, v125, v26\n"
        "v_mul_f32 v126, v126, v26\n"
        "v_mul_f32 v127, v127, v26\n"
        "v_mul_f32 v144, v144, v26\n"
        "v_mul_f32 v145, v145, v26\n"
        "v_mul_f32 v146, v146, v26\n"
        "v_mul_f32 v147, v147, v26\n"
        "v_mul_f32 v148, v148, v26\n"
        "v_mul_f32 v149, v149, v26\n"
        "v_mul_f32 v150, v150, v26\n"
        "v_mul_f32 v151, v151, v26\n"
        "v_mul_f32 v152, v152, v26\n"
        "v_mul_f32 v153, v153, v26\n"
        "v_mul_f32 v154, v154, v26\n"
        "v_mul_f32 v155, v155, v26\n"
        "v_mul_f32 v156, v156, v26\n"
        "v_mul_f32 v157, v157, v26\n"
        "v_mul_f32 v158, v158, v26\n"
        "v_mul_f32 v159, v159, v26\n"
        "v_mul_f32 v60, v60, v26\n"
        "v_mul_f32 v61, v61, v26\n"
        "v_mul_f32 v62, v62, v26\n"
        "v_mul_f32 v63, v63, v26\n"
        "s_nop 1\n"
        "v_mfma_f32_16x16x32_bf16 v[60:63], a[80:83], v[56:59], v[60:63]\n"
        "global_load_dwordx4 a[160:163], v254, s[86:87] offset:-4096 sc1\n"
        "global_load_dwordx4 a[164:167], v254, s[86:87] offset:-3072 sc1\n"
        "global_load_dwordx4 a[168:171], v254, s[86:87] offset:-2048 sc1\n"
        "global_load_dwordx4 a[172:175], v254, s[86:87] offset:-1024 sc1\n"
        "s_waitcnt vmcnt(0)\n"
        "v_mfma_f32_16x16x32_bf16 v[112:115], a[160:163], v[56:59], v[112:115]\n"
        "v_mfma_f32_16x16x32_bf16 v[116:119], a[164:167], v[56:59], v[116:119]\n"
        "v_mfma_f32_16x16x32_bf16 v[120:123], a[168:171], v[56:59], v[120:123]\n"
        "v_mfma_f32_16x16x32_bf16 v[124:127], a[172:175], v[56:59], v[124:127]\n"
        "s_nop 7\n"
        "global_load_dwordx4 a[160:163], v254, s[86:87] offset:0 sc1\n"
        "global_load_dwordx4 a[164:167], v254, s[86:87] offset:1024 sc1\n"
        "global_load_dwordx4 a[168:171], v254, s[86:87] offset:2048 sc1\n"
        "global_load_dwordx4 a[172:175], v254, s[86:87] offset:3072 sc1\n"
        "s_waitcnt vmcnt(0)\n"
        "v_mfma_f32_16x16x32_bf16 v[144:147], a[160:163], v[56:59], v[144:147]\n"
        "v_mfma_f32_16x16x32_bf16 v[148:151], a[164:167], v[56:59], v[148:151]\n"
        "v_mfma_f32_16x16x32_bf16 v[152:155], a[168:171], v[56:59], v[152:155]\n"
        "v_mfma_f32_16x16x32_bf16 v[156:159], a[172:175], v[56:59], v[156:159]\n"
        "s_nop 7\n"
        "s_add_u32 s84, s84, 0x2000\n"
        "s_addc_u32 s85, s85, 0\n"
        "s_add_u32 s86, s86, 0x2000\n"
        "s_addc_u32 s87, s87, 0\n"
        "v_subrev_u32 v27, 32, v27\n"
        "s_sub_u32 s97, s97, 1\n"
        "s_cmp_lg_u32 s97, 0\n"
        "s_cbranch_scc1 LRLOOP_%=\n"
        "s_mul_i32 s97, s53, 1052672\n"
        "s_lshl_b32 s86, s43, 15\n"
        "s_add_u32 s97, s97, s86\n"
        "s_lshl_b32 s86, s40, 10\n"
        "s_add_u32 s97, s97, s86\n"
        "s_lshl_b32 s86, s96, 6\n"
        "s_add_u32 s97, s97, s86\n"
        "s_add_u32 s97, s97, 0x2200000\n"
        "s_add_u32 s84, s38, s97\n"
        "s_addc_u32 s85, s39, 0\n"
        "v_rcp_f32 v26, v60\n"
        "s_nop 0\n"
        "v_mul_f32 v112, v112, v26\n"
        "v_mul_f32 v113, v113, v26\n"
        "v_mul_f32 v114, v114, v26\n"
        "v_mul_f32 v115, v115, v26\n"
        "v_mul_f32 v116, v116, v26\n"
        "v_mul_f32 v117, v117, v26\n"
        "v_mul_f32 v118, v118, v26\n"
        "v_mul_f32 v119, v119, v26\n"
        "v_cvt_pk_bf16_f32 v112, v112, v113\n"
        "v_cvt_pk_bf16_f32 v113, v114, v115\n"
        "v_cvt_pk_bf16_f32 v114, v116, v117\n"
        "v_cvt_pk_bf16_f32 v115, v118, v119\n"
        "global_store_dwordx4 v254, v[112:115], s[84:85] sc1\n"
        "s_add_u32 s84, s84, 0x2000\n"
        "s_addc_u32 s85, s85, 0\n"
        "v_mul_f32 v120, v120, v26\n"
        "v_mul_f32 v121, v121, v26\n"
        "v_mul_f32 v122, v122, v26\n"
        "v_mul_f32 v123, v123, v26\n"
        "v_mul_f32 v124, v124, v26\n"
        "v_mul_f32 v125, v125, v26\n"
        "v_mul_f32 v126, v126, v26\n"
        "v_mul_f32 v127, v127, v26\n"
        "v_cvt_pk_bf16_f32 v120, v120, v121\n"
        "v_cvt_pk_bf16_f32 v121, v122, v123\n"
        "v_cvt_pk_bf16_f32 v122, v124, v125\n"
        "v_cvt_pk_bf16_f32 v123, v126, v127\n"
        "global_store_dwordx4 v254, v[120:123], s[84:85] sc1\n"
        "s_add_u32 s84, s84, 0x2000\n"
        "s_addc_u32 s85, s85, 0\n"
        "v_mul_f32 v144, v144, v26\n"
        "v_mul_f32 v145, v145, v26\n"
        "v_mul_f32 v146, v146, v26\n"
        "v_mul_f32 v147, v147, v26\n"
        "v_mul_f32 v148, v148, v26\n"
        "v_mul_f32 v149, v149, v26\n"
        "v_mul_f32 v150, v150, v26\n"
        "v_mul_f32 v151, v151, v26\n"
        "v_cvt_pk_bf16_f32 v144, v144, v145\n"
        "v_cvt_pk_bf16_f32 v145, v146, v147\n"
        "v_cvt_pk_bf16_f32 v146, v148, v149\n"
        "v_cvt_pk_bf16_f32 v147, v150, v151\n"
        "global_store_dwordx4 v254, v[144:147], s[84:85] sc1\n"
        "s_add_u32 s84, s84, 0x2000\n"
        "s_addc_u32 s85, s85, 0\n"
        "v_mul_f32 v152, v152, v26\n"
        "v_mul_f32 v153, v153, v26\n"
        "v_mul_f32 v154, v154, v26\n"
        "v_mul_f32 v155, v155, v26\n"
        "v_mul_f32 v156, v156, v26\n"
        "v_mul_f32 v157, v157, v26\n"
        "v_mul_f32 v158, v158, v26\n"
        "v_mul_f32 v159, v159, v26\n"
        "v_cvt_pk_bf16_f32 v152, v152, v153\n"
        "v_cvt_pk_bf16_f32 v153, v154, v155\n"
        "v_cvt_pk_bf16_f32 v154, v156, v157\n"
        "v_cvt_pk_bf16_f32 v155, v158, v159\n"
        "global_store_dwordx4 v254, v[152:155], s[84:85] sc1\n"
        "s_add_u32 s84, s84, 0x2000\n"
        "s_addc_u32 s85, s85, 0\n"
        "s_waitcnt vmcnt(0)\n"
        "global_load_dwordx4 v[144:147], v254, s[88:89] offset:0 sc1\n"
        "global_load_dwordx4 v[148:151], v254, s[88:89] offset:1024 sc1\n"
        "global_load_dwordx4 v[152:155], v254, s[88:89] offset:2048 sc1\n"
        "global_load_dwordx4 v[156:159], v254, s[88:89] offset:3072 sc1\n"
        "s_waitcnt vmcnt(0)\n"
        "s_cmp_eq_u32 s52, 0\n"
        "s_cbranch_scc1 LRET0_%=\n"
        "s_cmp_eq_u32 s52, 1\n"
        "s_cbranch_scc1 LRET1_%=\n"
        "s_cmp_eq_u32 s52, 2\n"
        "s_cbranch_scc1 LRET2_%=\n"
        "s_branch LRET3_%=\n"
        "LEND_%=:\n"
        ".p2align 12\n"
        :
        : "s"(__builtin_amdgcn_kernarg_segment_ptr()), "s"(blockIdx.x), "s"(__builtin_amdgcn_readfirstlane(threadIdx.x >> 6))
        : "v0", "v1", "v2", "v3", "v4", "v5", "v6", "v7", "v8", "v9", "v10", "v11", "v12", "v13", "v14", "v15", "v16", "v17", "v18", "v19", "v20", "v21", "v22", "v23", "v24", "v25", "v26", "v27", "v28", "v29", "v30", "v31",
        "v32", "v33", "v34", "v35", "v36", "v37", "v38", "v39", "v40", "v41", "v42", "v43", "v44", "v45", "v46", "v47", "v48", "v49", "v50", "v51", "v52", "v53", "v54", "v55", "v56", "v57", "v58", "v59", "v60", "v61", "v62", "v63",
        "v64", "v65", "v66", "v67", "v68", "v69", "v70", "v71", "v72", "v73", "v74", "v75", "v76", "v77", "v78", "v79", "v80", "v81", "v82", "v83", "v84", "v85", "v86", "v87", "v88", "v89", "v90", "v91", "v92", "v93", "v94", "v95",
        "v96", "v97", "v98", "v99", "v100", "v101", "v102", "v103", "v104", "v105", "v106", "v107", "v108", "v109", "v110", "v111", "v112", "v113", "v114", "v115", "v116", "v117", "v118", "v119", "v120", "v121", "v122", "v123", "v124", "v125", "v126", "v127",
        "v128", "v129", "v130", "v131", "v132", "v133", "v134", "v135", "v136", "v137", "v138", "v139", "v140", "v141", "v142", "v143", "v144", "v145", "v146", "v147", "v148", "v149", "v150", "v151", "v152", "v153", "v154", "v155", "v156", "v157", "v158", "v159",
        "v160", "v161", "v162", "v163", "v164", "v165", "v166", "v167", "v168", "v169", "v170", "v171", "v172", "v173", "v174", "v175", "v176", "v177", "v178", "v179", "v180", "v181", "v182", "v183", "v184", "v185", "v186", "v187", "v188", "v189", "v190", "v191",
        "v192", "v193", "v194", "v195", "v196", "v197", "v198", "v199", "v200", "v201", "v202", "v203", "v204", "v205", "v206", "v207", "v208", "v209", "v210", "v211", "v212", "v213", "v214", "v215", "v216", "v217", "v218", "v219", "v220", "v221", "v222", "v223",
        "v224", "v225", "v226", "v227", "v228", "v229", "v230", "v231", "v232", "v233", "v234", "v235", "v236", "v237", "v238", "v239", "v240", "v241", "v242", "v243", "v244", "v245", "v246", "v247", "v248", "v249", "v250", "v251", "v252", "v253", "v254", "v255",
        "a0", "a1", "a2", "a3", "a4", "a5", "a6", "a7", "a8", "a9", "a10", "a11", "a12", "a13", "a14", "a15", "a16", "a17", "a18", "a19", "a20", "a21", "a22", "a23", "a24", "a25", "a26", "a27", "a28", "a29", "a30", "a31",
        "a32", "a33", "a34", "a35", "a36", "a37", "a38", "a39", "a40", "a41", "a42", "a43", "a44", "a45", "a46", "a47", "a48", "a49", "a50", "a51", "a52", "a53", "a54", "a55", "a56", "a57", "a58", "a59", "a60", "a61", "a62", "a63",
        "a64", "a65", "a66", "a67", "a68", "a69", "a70", "a71", "a72", "a73", "a74", "a75", "a76", "a77", "a78", "a79", "a80", "a81", "a82", "a83", "a84", "a85", "a86", "a87", "a88", "a89", "a90", "a91", "a92", "a93", "a94", "a95",
        "a96", "a97", "a98", "a99", "a100", "a101", "a102", "a103", "a104", "a105", "a106", "a107", "a108", "a109", "a110", "a111", "a112", "a113", "a114", "a115", "a116", "a117", "a118", "a119", "a120", "a121", "a122", "a123", "a124", "a125", "a126", "a127",
        "a128", "a129", "a130", "a131", "a132", "a133", "a134", "a135", "a136", "a137", "a138", "a139", "a140", "a141", "a142", "a143", "a144", "a145", "a146", "a147", "a148", "a149", "a150", "a151", "a152", "a153", "a154", "a155", "a156", "a157", "a158", "a159",
        "a160", "a161", "a162", "a163", "a164", "a165", "a166", "a167", "a168", "a169", "a170", "a171", "a172", "a173", "a174", "a175", "a176", "a177", "a178", "a179", "a180", "a181", "a182", "a183", "a184", "a185", "a186", "a187", "a188", "a189", "a190", "a191",
        "a192", "a193", "a194", "a195", "a196", "a197", "a198", "a199", "a200", "a201", "a202", "a203", "a204", "a205", "a206", "a207", "a208", "a209", "a210", "a211", "a212", "a213", "a214", "a215", "a216", "a217", "a218", "a219", "a220", "a221", "a222", "a223",
        "a224", "a225", "a226", "a227", "a228", "a229", "a230", "a231", "a232", "a233", "a234", "a235", "a236", "a237", "a238", "a239", "a240", "a241", "a242", "a243", "a244", "a245", "a246", "a247", "a248", "a249", "a250", "a251", "a252", "a253", "a254", "a255",
        "s8", "s9", "s10", "s11", "s12", "s13", "s14", "s15", "s16", "s17", "s18", "s19", "s20", "s21", "s22", "s23", "s24", "s25", "s26", "s27", "s28", "s29", "s30", "s31", "s34", "s35", "s36", "s37", "s38", "s39", "s40", "s41",
        "s42", "s43", "s44", "s45", "s46", "s47", "s48", "s49", "s50", "s51", "s52", "s53", "s54", "s55", "s56", "s57", "s58", "s59", "s60", "s61", "s62", "s63", "s64", "s65", "s66", "s67", "s68", "s69", "s70", "s71", "s72", "s73",
        "s74", "s75", "s76", "s77", "s78", "s79", "s80", "s81", "s82", "s83", "s84", "s85", "s86", "s87", "s88", "s89", "s90", "s91", "s92", "s93", "s94", "s95", "s96", "s97", "s98", "s99", "vcc", "scc", "memory");
}

extern "C" void kernel_launch(void* const* d_in, const int* in_sizes, int n_in, void* d_out, int out_size, void* d_ws, size_t ws_size, hipStream_t stream) {
    (void)in_sizes; (void)n_in; (void)out_size;
    constexpr size_t WS_USED = (size_t)85 << 20;
    Params P;
    P.x = d_in[0]; P.ckv = d_in[1]; P.wq = d_in[2]; P.wuk = d_in[3]; P.wuv = d_in[4]; P.wo = d_in[5];
    P.bk = d_in[7]; P.bv = d_in[8]; P.shk = d_in[9]; P.ssk = d_in[10]; P.ssd = d_in[11]; P.gw = d_in[12];
    P.out = d_out; P.ws = (char*)d_ws;
    if (ws_size > WS_USED) P.ws += (ws_size - WS_USED) & ~(size_t)0xfffff;
    constexpr int LDS_MAX = 163840;
    (void)hipFuncSetAttribute((const void*)k_main, hipFuncAttributeMaxDynamicSharedMemorySize, LDS_MAX);
    k_main<<<256, 256, LDS_MAX, stream>>>(P);
}
```

```cpp
#include <hip/hip_runtime.h>
#include <stdint.h>

struct Params {
    const void *x, *ckv, *wq, *wuk, *wuv, *wo, *bk, *bv, *shk, *ssk, *ssd, *gw;
    void* out; char* ws;
};

__global__ __launch_bounds__(256, 1) void k_main(Params P) {
    asm volatile(
        "; ---- PRO\n"
        "s_load_dwordx16 s[8:23], %0, 0\n"
        "s_load_dwordx8 s[24:31], %0, 64\n"
        "s_load_dwordx4 s[36:39], %0, 96\n"
        "s_mov_b32 s55, %1\n"
        "s_mov_b32 s40, %2\n"
        "v_mbcnt_lo_u32_b32 v0, -1, 0\n"
        "v_mbcnt_hi_u32_b32 v0, -1, v0\n"
        "s_getpc_b64 s[46:47]\n"
        "v_min_u32 v5, 12, v0\n"
        "v_lshlrev_b32 v5, 7, v5\n"
        "global_load_dword v253, v5, s[46:47] offset:0\n"
        "s_getpc_b64 s[46:47]\n"
        "v_min_u32 v4, 12, v0\n"
        "v_lshlrev_b32 v4, 12, v4\n"
        "global_load_dword v253, v4, s[46:47] offset:0\n"
        "s_and_b32 s41, s55, 7\n"
        "s_lshr_b32 s42, s55, 3\n"
        "s_lshr_b32 s50, s42, 3\n"
        "s_lshl2_add_u32 s43, s41, s50\n"
        "s_and_b32 s44, s42, 7\n"
        "s_sub_u32 s45, 15, s44\n"
        "s_lshl_b32 s72, s40, 13\n"
        "s_lshl_b32 s73, s40, 12\n"
        "v_lshlrev_b32 v254, 4, v0\n"
        "v_lshrrev_b32 v2, 4, v0\n"
        "v_and_b32 v1, 15, v0\n"
        "v_lshrrev_b32 v4, 3, v0\n"
        "v_and_b32 v5, 7, v0\n"
        "v_and_b32 v8, 6, v4\n"
        "v_xor_b32 v8, v8, v5\n"
        "v_lshlrev_b32 v8, 4, v8\n"
        "v_lshl_add_u32 v9, v4, 13, v8\n"
        "v_xor_b32 v10, 4, v4\n"
        "v_lshl_add_u32 v10, v10, 13, v8\n"
        "v_add_u32 v238, 4096, v9\n"
        "v_add_u32 v239, 3200, v9\n"
        "v_add_u32 v240, 67584, v10\n"
        "v_add_u32 v241, 66688, v10\n"
        "s_cmp_ge_u32 s40, 2\n"
        "s_cselect_b32 s50, s45, s44\n"
        "s_and_b32 s51, s40, 1\n"
        "s_lshl1_add_u32 s50, s50, s51\n"
        "s_lshl_b32 s50, s50, 16\n"
        "s_lshl_b32 s51, s40, 17\n"
        "s_lshl_b32 s52, s43, 8\n"
        "s_add_u32 s51, s51, s52\n"
        "s_waitcnt lgkmcnt(0)\n"
        "s_add_u32 s60, s14, s51\n"
        "s_addc_u32 s61, s15, 0\n"
        "s_sub_u32 s60, s60, 4096\n"
        "s_subb_u32 s61, s61, 0\n"
        "s_add_u32 m0, s73, 0x10000\n"
        "s_add_u32 s86, s60, 0x200000\n"
        "s_addc_u32 s87, s61, 0\n"
        "global_load_lds_dwordx4 v238, s[86:87] offset:0\n"
        "global_load_lds_dwordx4 v239, s[86:87] offset:1024\n"
        "global_load_lds_dwordx4 v240, s[86:87] offset:2048\n"
        "global_load_lds_dwordx4 v241, s[86:87] offset:3072\n"
        "v_xor_b32 v6, v5, v2\n"
        "v_lshlrev_b32 v6, 4, v6\n"
        "v_xor_b32 v7, 64, v6\n"
        "v_lshl_add_u32 v234, v4, 10, v6\n"
        "v_lshl_add_u32 v8, v4, 10, v7\n"
        "v_add_u32 v235, 7168, v8\n"
        "v_add_u32 v236, 14336, v234\n"
        "v_add_u32 v237, 21504, v8\n"
        "s_add_u32 s56, s10, s50\n"
        "s_addc_u32 s57, s11, 0\n"
        "s_add_u32 m0, s72, 0x0\n"
        "s_add_u32 s86, s56, 0x200\n"
        "s_addc_u32 s87, s57, 0\n"
        "global_load_lds_dwordx4 v234, s[86:87] offset:0\n"
        "global_load_lds_dwordx4 v235, s[86:87] offset:1024\n"
        "global_load_lds_dwordx4 v236, s[86:87] offset:2048\n"
        "global_load_lds_dwordx4 v237, s[86:87] offset:3072\n"
        "s_getpc_b64 s[46:47]\n"
        "LPC_%=:\n"
        "s_lshl2_add_u32 s53, s42, s40\n"
        "s_lshl_b32 s53, s53, 10\n"
        "v_lshlrev_b32 v12, 6, v1\n"
        "v_add_u32 v12, s53, v12\n"
        "v_min_u32 v252, LEND_%=-LPC_%=-4, v12\n"
        "global_load_dword v253, v252, s[46:47] offset:0\n"
        "s_add_u32 s62, s16, s51\n"
        "s_addc_u32 s63, s17, 0\n"
        "s_sub_u32 s62, s62, 4096\n"
        "s_subb_u32 s63, s63, 0\n"
        "s_add_u32 m0, s73, 0x14000\n"
        "s_add_u32 s86, s62, 0x200000\n"
        "s_addc_u32 s87, s63, 0\n"
        "global_load_lds_dwordx4 v238, s[86:87] offset:0\n"
        "global_load_lds_dwordx4 v239, s[86:87] offset:1024\n"
        "global_load_lds_dwordx4 v240, s[86:87] offset:2048\n"
        "global_load_lds_dwordx4 v241, s[86:87] offset:3072\n"
        "s_add_u32 s58, s56, 0x8000\n"
        "s_addc_u32 s59, s57, 0\n"
        "s_add_u32 s68, s12, s51\n"
        "s_addc_u32 s69, s13, 0\n"
        "s_add_u32 m0, s72, 0x1000\n"
        "s_add_u32 s86, s58, 0x200\n"
        "s_addc_u32 s87, s59, 0\n"
        "global_load_lds_dwordx4 v234, s[86:87] offset:0\n"
        "global_load_lds_dwordx4 v235, s[86:87] offset:1024\n"
        "global_load_lds_dwordx4 v236, s[86:87] offset:2048\n"
        "global_load_lds_dwordx4 v237, s[86:87] offset:3072\n"
        "s_sub_u32 s68, s68, 4096\n"
        "s_subb_u32 s69, s69, 0\n"
        "v_lshrrev_b32 v8, 1, v1\n"
        "v_xor_b32 v8, v8, v2\n"
        "v_lshlrev_b32 v8, 4, v8\n"
        "v_lshl_add_u32 v8, v1, 7, v8\n"
        "v_xor_b32 v9, 64, v8\n"
        "s_lshr_b32 s50, s40, 1\n"
        "s_lshl_b32 s50, s50, 13\n"
        "v_add_u32 v224, s50, v8\n"
        "v_add_u32 v246, s72, v8\n"
        "v_add_u32 v248, 0x10000, v246\n"
        "v_add_u32 v225, s50, v9\n"
        "v_add_u32 v247, s72, v9\n"
        "v_add_u32 v249, 0x10000, v247\n"
        "s_and_b32 s50, s40, 1\n"
        "s_lshl_b32 s50, s50, 10\n"
        "s_add_u32 s50, s50, 0x10000\n"
        "v_lshlrev_b32 v8, 11, v2\n"
        "v_add_u32 v8, s50, v8\n"
        "v_lshrrev_b32 v9, 2, v1\n"
        "v_or_b32 v10, 4, v9\n"
        "v_and_b32 v11, 1, v2\n"
        "v_and_b32 v13, 3, v0\n"
        "v_lshlrev_b32 v14, 2, v11\n"
        "v_xor_b32 v14, v14, v9\n"
        "v_bfe_u32 v15, v14, 1, 2\n"
        "v_xor_b32 v15, 0, v15\n"
        "v_lshlrev_b32 v15, 5, v15\n"
        "v_lshl_add_u32 v14, v14, 7, v15\n"
        "v_lshl_add_u32 v14, v13, 3, v14\n"
        "v_add_u32 v226, v14, v8\n"
        "v_xor_b32 v228, 32, v226\n"
        "v_xor_b32 v230, 64, v226\n"
        "v_xor_b32 v232, 96, v226\n"
        "v_lshlrev_b32 v14, 2, v11\n"
        "v_xor_b32 v14, v14, v10\n"
        "v_bfe_u32 v15, v14, 1, 2\n"
        "v_xor_b32 v15, 0, v15\n"
        "v_lshlrev_b32 v15, 5, v15\n"
        "v_lshl_add_u32 v14, v14, 7, v15\n"
        "v_lshl_add_u32 v14, v13, 3, v14\n"
        "v_add_u32 v227, v14, v8\n"
        "v_xor_b32 v229, 32, v227\n"
        "v_xor_b32 v231, 64, v227\n"
        "v_xor_b32 v233, 96, v227\n"
        "v_add_u32 v216, 0x10000, v226\n"
        "v_add_u32 v217, 0x10000, v227\n"
        "v_add_u32 v218, 0x10000, v228\n"
        "v_add_u32 v219, 0x10000, v229\n"
        "v_add_u32 v220, 0x10000, v230\n"
        "v_add_u32 v221, 0x10000, v231\n"
        "v_add_u32 v222, 0x10000, v232\n"
        "v_add_u32 v223, 0x10000, v233\n"
        "v_lshl_add_u32 v242, v4, 11, v6\n"
        "v_lshl_add_u32 v8, v4, 11, v7\n"
        "v_add_u32 v243, 15360, v8\n"
        "v_add_u32 v244, 129024, v242\n"
        "v_add_u32 v245, 144384, v8\n"
        "s_lshl_b32 s50, s45, 18\n"
        "s_lshl_b32 s51, s40, 15\n"
        "s_add_u32 s50, s50, s51\n"
        "s_add_u32 s64, s8, s50\n"
        "s_addc_u32 s65, s9, 0\n"
        "s_lshl_b32 s50, s44, 18\n"
        "s_add_u32 s50, s50, s51\n"
        "s_add_u32 s66, s8, s50\n"
        "s_addc_u32 s67, s9, 0\n"
        "s_add_u32 s48, s38, 0x3b00000\n"
        "s_addc_u32 s49, s39, 0\n"
        "s_mov_b32 s35, 0xffff0000\n"
        "v_mov_b32 v11, 0\n"
        "global_load_dwordx2 v[250:251], v11, s[48:49] sc1\n"
        "s_lshl2_add_u32 s82, s55, s40\n"
        "s_lshl_b32 s82, s82, 11\n"
        "v_and_b32 v20, 0xf0, v254\n"
        "v_lshl_add_u32 v20, v20, 3, s82\n"
        "global_load_dword v253, v20, s[10:11] offset:0\n"
        "s_lshl2_add_u32 s82, s42, s40\n"
        "s_lshl3_add_u32 s82, s82, s41\n"
        "s_lshl_b32 s82, s82, 10\n"
        "v_and_b32 v21, 0x70, v254\n"
        "v_lshl_add_u32 v173, v21, 3, s82\n"
        "s_mov_b64 exec, 0\n"
        "s_cmp_eq_u32 s40, 0\n"
        "s_cbranch_scc1 LHA0_%=\n"
        "s_cmp_eq_u32 s40, 1\n"
        "s_cbranch_scc1 LHA1_%=\n"
        "s_cmp_eq_u32 s40, 2\n"
        "s_cbranch_scc1 LHA2_%=\n"
        "s_branch LHA3_%=\n"
        "LHRA_%=:\n"
        "s_mov_b64 exec, -1\n"
        "s_add_u32 m0, s73, 0x18000\n"
        "s_add_u32 s86, s60, 0x300000\n"
        "s_addc_u32 s87, s61, 0\n"
        "global_load_lds_dwordx4 v238, s[86:87] offset:0\n"
        "global_load_lds_dwordx4 v239, s[86:87] offset:1024\n"
        "global_load_lds_dwordx4 v240, s[86:87] offset:2048\n"
        "global_load_lds_dwordx4 v241, s[86:87] offset:3072\n"
        "s_add_u32 m0, s73, 0x1c000\n"
        "s_add_u32 s86, s62, 0x300000\n"
        "s_addc_u32 s87, s63, 0\n"
        "global_load_lds_dwordx4 v238, s[86:87] offset:0\n"
        "global_load_lds_dwordx4 v239, s[86:87] offset:1024\n"
        "global_load_lds_dwordx4 v240, s[86:87] offset:2048\n"
        "global_load_lds_dwordx4 v241, s[86:87] offset:3072\n"
        "; ---- FRONT\n"
        "s_add_u32 m0, s72, 0x8000\n"
        "s_add_u32 s86, s56, 0x300\n"
        "s_addc_u32 s87, s57, 0\n"
        "global_load_lds_dwordx4 v234, s[86:87] offset:0\n"
        "s_waitcnt vmcnt(20)\n"
        "s_barrier\n"
        "ds_read_b64_tr_b16 v[32:33], v226 offset:0\n"
        "ds_read_b64_tr_b16 v[34:35], v227 offset:0\n"
        "ds_read_b128 v[0:3], v224 offset:16384\n"
        "global_load_lds_dwordx4 v235, s[86:87] offset:1024\n"
        "global_load_lds_dwordx4 v236, s[86:87] offset:2048\n"
        "ds_read_b128 v[4:7], v225 offset:16384\n"
        "ds_read_b64_tr_b16 v[64:65], v226 offset:8192\n"
        "ds_read_b64_tr_b16 v[66:67], v227 offset:8192\n"
        "ds_read_b128 v[12:15], v225 offset:18432\n"
        "s_lshl_b32 s94, s41, 7\n"
        "s_waitcnt lgkmcnt(4)\n"
        "v_mfma_f32_16x16x32_bf16 a[0:3], v[32:35], v[0:3], 0\n"
        "global_load_lds_dwordx4 v237, s[86:87] offset:3072\n"
        "ds_read_b128 v[8:11], v224 offset:18432\n"
        "s_add_u32 m0, s72, 0x9000\n"
        "s_add_u32 s86, s58, 0x300\n"
        "s_addc_u32 s87, s59, 0\n"
        "global_load_lds_dwordx4 v234, s[86:87] offset:0\n"
        "ds_read_b64_tr_b16 v[36:37], v228 offset:0\n"
        "s_add_u32 s82, s12, 0x0\n"
        "s_addc_u32 s83, s13, 0\n"
        "s_waitcnt lgkmcnt(3)\n"
        "v_mfma_f32_16x16x32_bf16 a[0:3], v[64:67], v[4:7], a[0:3]\n"
        "ds_read_b64_tr_b16 v[38:39], v229 offset:0\n"
        "s_lshl2_add_u32 s94, s42, s94\n"
        "s_waitcnt lgkmcnt(3)\n"
        "v_mfma_f32_16x16x32_bf16 a[32:35], v[64:67], v[12:15], 0\n"
        "ds_read_b64_tr_b16 v[68:69], v228 offset:8192\n"
        "ds_read_b64_tr_b16 v[70:71], v229 offset:8192\n"
        "ds_read_b64_tr_b16 v[40:41], v230 offset:0\n"
        "s_add_u32 s94, s94, s40\n"
        "s_waitcnt lgkmcnt(5)\n"
        "v_mfma_f32_16x16x32_bf16 a[32:35], v[32:35], v[8:11], a[32:35]\n"
        "ds_read_b64_tr_b16 v[42:43], v231 offset:0\n"
        "ds_read_b64_tr_b16 v[72:73], v230 offset:8192\n"
        "ds_read_b64_tr_b16 v[74:75], v231 offset:8192\n"
        "ds_read_b64_tr_b16 v[44:45], v232 offset:0\n"
        "ds_read_b64_tr_b16 v[46:47], v233 offset:0\n"
        "ds_read_b64_tr_b16 v[76:77], v232 offset:8192\n"
        "ds_read_b64_tr_b16 v[78:79], v233 offset:8192\n"
        "ds_read_b128 v[16:19], v224 offset:0\n"
        "ds_read_b128 v[20:23], v225 offset:0\n"
        "global_load_lds_dwordx4 v235, s[86:87] offset:1024\n"
        "s_lshl_b32 s94, s94, 1\n"
        "s_waitcnt lgkmcnt(12)\n"
        "v_mfma_f32_16x16x32_bf16 a[36:39], v[36:39], v[8:11], 0\n"
        "ds_read_b128 v[24:27], v224 offset:2048\n"
        "ds_read_b128 v[28:31], v225 offset:2048\n"
        "s_lshl_b32 s50, s42, 6\n"
        "s_waitcnt lgkmcnt(12)\n"
        "v_mfma_f32_16x16x32_bf16 a[36:39], v[68:71], v[12:15], a[36:39]\n"
        "s_lshl_b32 s51, s41, 7\n"
        "v_mov_b32 v172, s94\n"
        "v_lshlrev_b32 v198, 11, v172\n"
        "v_mfma_f32_16x16x32_bf16 a[4:7], v[68:71], v[4:7], 0\n"
        "s_waitcnt lgkmcnt(10)\n"
        "v_lshrrev_b32 v166, 4, v254\n"
        "v_lshrrev_b32 v167, 3, v166\n"
        "v_mfma_f32_16x16x32_bf16 a[4:7], v[36:39], v[0:3], a[4:7]\n"
        "global_load_lds_dwordx4 v236, s[86:87] offset:2048\n"
        "s_lshl_b32 s52, s40, 5\n"
        "v_and_b32 v168, 7, v166\n"
        "v_mfma_f32_16x16x32_bf16 a[8:11], v[40:43], v[0:3], 0\n"
        "s_add_u32 s51, s51, s52\n"
        "v_lshl_add_u32 v167, v167, 3, s50\n"
        "s_waitcnt lgkmcnt(6)\n"
        "v_mfma_f32_16x16x32_bf16 a[8:11], v[72:75], v[4:7], a[8:11]\n"
        "s_waitcnt lgkmcnt(4)\n"
        "v_lshl_add_u32 v168, v168, 2, s51\n"
        "v_lshlrev_b32 v169, 11, v167\n"
        "v_mfma_f32_16x16x32_bf16 a[40:43], v[72:75], v[12:15], 0\n"
        "s_sub_u32 s74, s20, 2048\n"
        "s_subb_u32 s75, s21, 0\n"
        "v_lshl_add_u32 v169, v168, 1, v169\n"
        "v_mfma_f32_16x16x32_bf16 a[40:43], v[40:43], v[8:11], a[40:43]\n"
        "global_load_lds_dwordx4 v237, s[86:87] offset:3072\n"
        "s_add_u32 m0, s73, 0x20000\n"
        "s_waitcnt vmcnt(22)\n"
        "v_mfma_f32_16x16x32_bf16 a[44:47], v[44:47], v[8:11], 0\n"
        "s_barrier\n"
        "v_mfma_f32_16x16x32_bf16 a[44:47], v[76:79], v[12:15], a[44:47]\n"
        "ds_read_b64_tr_b16 v[48:49], v226 offset:16384\n"
        "s_and_b32 s91, s43, 15\n"
        "v_cmp_eq_u32 s[78:79], 0, v167\n"
        "v_mfma_f32_16x16x32_bf16 a[12:15], v[76:79], v[4:7], 0\n"
        "ds_read_b64_tr_b16 v[50:51], v227 offset:16384\n"
        "v_mov_b32 v171, 6144\n"
        "v_mfma_f32_16x16x32_bf16 a[12:15], v[44:47], v[0:3], a[12:15]\n"
        "global_load_lds_dwordx4 v238, s[60:61] offset:0\n"
        "s_waitcnt lgkmcnt(2)\n"
        "v_mfma_f32_16x16x32_bf16 a[128:131], v[32:35], v[16:19], 0\n"
        "ds_read_b64_tr_b16 v[80:81], v226 offset:24576\n"
        "v_cndmask_b32 v170, 0, v171, s[78:79]\n"
        "v_mfma_f32_16x16x32_bf16 a[128:131], v[64:67], v[20:23], a[128:131]\n"
        "ds_read_b64_tr_b16 v[82:83], v227 offset:24576\n"
        "v_add_u32 v170, v170, v169\n"
        "v_mfma_f32_16x16x32_bf16 a[160:163], v[64:67], v[28:31], 0\n"
        "ds_read_b64_tr_b16 v[52:53], v228 offset:16384\n"
        "v_sub_u32 v171, 2040, v167\n"
        "v_mfma_f32_16x16x32_bf16 a[160:163], v[32:35], v[24:27], a[160:163]\n"
        "global_load_dword v253, v173, s[82:83] offset:0\n"
        "ds_read_b64_tr_b16 v[54:55], v229 offset:16384\n"
        "v_mfma_f32_16x16x32_bf16 a[164:167], v[36:39], v[24:27], 0\n"
        "ds_read_b64_tr_b16 v[84:85], v228 offset:24576\n"
        "global_load_lds_dwordx4 v239, s[60:61] offset:1024\n"
        "v_mfma_f32_16x16x32_bf16 a[164:167], v[68:71], v[28:31], a[164:167]\n"
        "ds_read_b64_tr_b16 v[86:87], v229 offset:24576\n"
        "v_lshlrev_b32 v171, 1, v171\n"
        "v_mfma_f32_16x16x32_bf16 a[132:135], v[68:71], v[20:23], 0\n"
        "ds_read_b64_tr_b16 v[56:57], v230 offset:16384\n"
        "v_lshl_add_u32 v171, v168, 12, v171\n"
        "v_mfma_f32_16x16x32_bf16 a[132:135], v[36:39], v[16:19], a[132:135]\n"
        "ds_read_b64_tr_b16 v[58:59], v231 offset:16384\n"
        "v_lshlrev_b32 v168, 3, v168\n"
        "v_mfma_f32_16x16x32_bf16 a[136:139], v[40:43], v[16:19], 0\n"
        "ds_read_b64_tr_b16 v[88:89], v230 offset:24576\n"
        "global_load_lds_dwordx4 v240, s[60:61] offset:2048\n"
        "v_mfma_f32_16x16x32_bf16 a[136:139], v[72:75], v[20:23], a[136:139]\n"
        "ds_read_b64_tr_b16 v[90:91], v231 offset:24576\n"
        "s_add_u32 s82, s12, 0x100000\n"
        "s_addc_u32 s83, s13, 0\n"
        "v_mfma_f32_16x16x32_bf16 a[168:171], v[72:75], v[28:31], 0\n"
        "ds_read_b64_tr_b16 v[60:61], v232 offset:16384\n"
        "s_add_u32 s86, s60, 0x80000\n"
        "s_addc_u32 s87, s61, 0\n"
        "v_mfma_f32_16x16x32_bf16 a[168:171], v[40:43], v[24:27], a[168:171]\n"
        "ds_read_b64_tr_b16 v[62:63], v233 offset:16384\n"
        "s_cmp_lt_u32 s43, 16\n"
        "s_cselect_b32 s90, s44, s45\n"
        "v_mfma_f32_16x16x32_bf16 a[172:175], v[44:47], v[24:27], 0\n"
        "ds_read_b64_tr_b16 v[92:93], v232 offset:24576\n"
        "global_load_dwordx4 v[158:161], v254, s[30:31] offset:0\n"
        "s_lshl4_add_u32 s90, s90, s91\n"
        "v_mfma_f32_16x16x32_bf16 a[172:175], v[76:79], v[28:31], a[172:175]\n"
        "ds_read_b64_tr_b16 v[94:95], v233 offset:24576\n"
        "s_lshl2_add_u32 s90, s90, s40\n"
        "s_lshl_b32 s90, s90, 12\n"
        "v_mfma_f32_16x16x32_bf16 a[140:143], v[76:79], v[20:23], 0\n"
        "s_add_u32 s92, s8, s90\n"
        "s_addc_u32 s93, s9, 0\n"
        "s_lshl_b32 s91, s44, 5\n"
        "v_mfma_f32_16x16x32_bf16 a[140:143], v[44:47], v[16:19], a[140:143]\n"
        "s_add_u32 s91, s91, s43\n"
        "s_lshl2_add_u32 s91, s91, s40\n"
        "s_waitcnt lgkmcnt(6)\n"
        "v_mfma_f32_16x16x32_bf16 a[16:19], v[0:3], v[48:51], 0\n"
        "global_load_lds_dwordx4 v241, s[60:61] offset:3072\n"
        "s_add_u32 m0, s73, 0x24000\n"
        "v_mfma_f32_16x16x32_bf16 a[16:19], v[4:7], v[80:83], a[16:19]\n"
        "s_lshl_b32 s80, s91, 1\n"
        "s_add_u32 s81, s80, 1\n"
        "s_lshl_b32 s80, s80, 7\n"
        "v_mfma_f32_16x16x32_bf16 a[48:51], v[12:15], v[80:83], 0\n"
        "s_add_u32 s76, s38, 0x3301000\n"
        "s_addc_u32 s77, s39, 0\n"
        "s_lshl_b32 s81, s81, 7\n"
        "v_mfma_f32_16x16x32_bf16 a[48:51], v[8:11], v[48:51], a[48:51]\n"
        "v_mfma_f32_16x16x32_bf16 a[52:55], v[8:11], v[52:55], 0\n"
        "global_load_lds_dwordx4 v238, s[62:63] offset:0\n"
        "v_mfma_f32_16x16x32_bf16 a[52:55], v[12:15], v[84:87], a[52:55]\n"
        "s_waitcnt lgkmcnt(0)\n"
        "v_mfma_f32_16x16x32_bf16 a[20:23], v[4:7], v[84:87], 0\n"
        "v_mfma_f32_16x16x32_bf16 a[20:23], v[0:3], v[52:55], a[20:23]\n"
        "v_mfma_f32_16x16x32_bf16 a[24:27], v[0:3], v[56:59], 0\n"
        "global_load_dwordx4 v[162:165], v254, s[30:31] offset:1024\n"
        "v_mfma_f32_16x16x32_bf16 a[24:27], v[4:7], v[88:91], a[24:27]\n"
        "v_mfma_f32_16x16x32_bf16 a[56:59], v[12:15], v[88:91], 0\n"
        "v_mfma_f32_16x16x32_bf16 a[56:59], v[8:11], v[56:59], a[56:59]\n"
        "v_mfma_f32_16x16x32_bf16 a[60:63], v[8:11], v[60:63], 0\n"
        "global_load_lds_dwordx4 v239, s[62:63] offset:1024\n"
        "v_mfma_f32_16x16x32_bf16 a[60:63], v[12:15], v[92:95], a[60:63]\n"
        "v_mfma_f32_16x16x32_bf16 a[28:31], v[4:7], v[92:95], 0\n"
        "v_mfma_f32_16x16x32_bf16 a[28:31], v[0:3], v[60:63], a[28:31]\n"
        "s_waitcnt vmcnt(27)\n"
        "v_mfma_f32_16x16x32_bf16 a[144:147], v[16:19], v[48:51], 0\n"
        "global_load_lds_dwordx4 v240, s[62:63] offset:2048\n"
        "v_mfma_f32_16x16x32_bf16 a[144:147], v[20:23], v[80:83], a[144:147]\n"
        "s_barrier\n"
        "v_mfma_f32_16x16x32_bf16 a[176:179], v[28:31], v[80:83], 0\n"
        "ds_read_b128 v[0:3], v224 offset:20480\n"
        "v_mfma_f32_16x16x32_bf16 a[176:179], v[24:27], v[48:51], a[176:179]\n"
        "ds_read_b128 v[4:7], v225 offset:20480\n"
        "v_mfma_f32_16x16x32_bf16 a[180:183], v[24:27], v[52:55], 0\n"
        "global_load_ushort v172, v172, s[28:29]\n"
        "v_mfma_f32_16x16x32_bf16 a[180:183], v[28:31], v[84:87], a[180:183]\n"
        "global_load_lds_dwordx4 v241, s[62:63] offset:3072\n"
        "ds_read_b128 v[8:11], v224 offset:22528\n"
        "s_add_u32 m0, s72, 0x0\n"
        "v_mfma_f32_16x16x32_bf16 a[148:151], v[20:23], v[84:87], 0\n"
        "ds_read_b128 v[12:15], v225 offset:22528\n"
        "v_mfma_f32_16x16x32_bf16 a[148:151], v[16:19], v[52:55], a[148:151]\n"
        "v_mfma_f32_16x16x32_bf16 a[152:155], v[16:19], v[56:59], 0\n"
        "v_mfma_f32_16x16x32_bf16 a[152:155], v[20:23], v[88:91], a[152:155]\n"
        "global_load_ushort v198, v198, s[26:27]\n"
        "v_mfma_f32_16x16x32_bf16 a[184:187], v[28:31], v[88:91], 0\n"
        "global_load_dwordx4 v[134:137], v168, s[24:25] offset:0\n"
        "v_mfma_f32_16x16x32_bf16 a[184:187], v[24:27], v[56:59], a[184:187]\n"
        "v_mfma_f32_16x16x32_bf16 a[188:191], v[24:27], v[60:63], 0\n"
        "ds_read_b128 v[24:27], v224 offset:6144\n"
        "s_waitcnt lgkmcnt(3)\n"
        "v_mfma_f32_16x16x32_bf16 a[188:191], v[28:31], v[92:95], a[188:191]\n"
        "ds_read_b128 v[28:31], v225 offset:6144\n"
        "v_mfma_f32_16x16x32_bf16 a[156:159], v[20:23], v[92:95], 0\n"
        "ds_read_b128 v[20:23], v225 offset:4096\n"
        "s_waitcnt lgkmcnt(3)\n"
        "v_mfma_f32_16x16x32_bf16 a[156:159], v[16:19], v[60:63], a[156:159]\n"
        "ds_read_b128 v[16:19], v224 offset:4096\n"
        "v_mfma_f32_16x16x32_bf16 a[64:67], v[32:35], v[0:3], 0\n"
        "v_mfma_f32_16x16x32_bf16 a[64:67], v[64:67], v[4:7], a[64:67]\n"
        "v_mfma_f32_16x16x32_bf16 a[96:99], v[64:67], v[12:15], 0\n"
        "v_mfma_f32_16x16x32_bf16 a[96:99], v[32:35], v[8:11], a[96:99]\n"
        "v_mfma_f32_16x16x32_bf16 a[100:103], v[36:39], v[8:11], 0\n"
        "v_mfma_f32_16x16x32_bf16 a[100:103], v[68:71], v[12:15], a[100:103]\n"
        "v_mfma_f32_16x16x32_bf16 a[68:71], v[68:71], v[4:7], 0\n"
        "v_mfma_f32_16x16x32_bf16 a[68:71], v[36:39], v[0:3], a[68:71]\n"
        "global_load_dwordx4 v[138:141], v168, s[24:25] offset:16\n"
        "v_mfma_f32_16x16x32_bf16 a[72:75], v[40:43], v[0:3], 0\n"
        "v_mfma_f32_16x16x32_bf16 a[72:75], v[72:75], v[4:7], a[72:75]\n"
        "v_mfma_f32_16x16x32_bf16 a[104:107], v[72:75], v[12:15], 0\n"
        "v_mfma_f32_16x16x32_bf16 a[104:107], v[40:43], v[8:11], a[104:107]\n"
        "v_mfma_f32_16x16x32_bf16 a[108:111], v[44:47], v[8:11], 0\n"
        "v_mfma_f32_16x16x32_bf16 a[108:111], v[76:79], v[12:15], a[108:111]\n"
        "v_mfma_f32_16x16x32_bf16 a[76:79], v[76:79], v[4:7], 0\n"
        "v_mfma_f32_16x16x32_bf16 a[76:79], v[44:47], v[0:3], a[76:79]\n"
        "v_mfma_f32_16x16x32_bf16 a[80:83], v[0:3], v[48:51], 0\n"
        "v_mfma_f32_16x16x32_bf16 a[80:83], v[4:7], v[80:83], a[80:83]\n"
        "v_mfma_f32_16x16x32_bf16 a[112:115], v[12:15], v[80:83], 0\n"
        "v_mfma_f32_16x16x32_bf16 a[112:115], v[8:11], v[48:51], a[112:115]\n"
        "v_mfma_f32_16x16x32_bf16 a[116:119], v[8:11], v[52:55], 0\n"
        "v_mfma_f32_16x16x32_bf16 a[116:119], v[12:15], v[84:87], a[116:119]\n"
        "v_mfma_f32_16x16x32_bf16 a[84:87], v[4:7], v[84:87], 0\n"
        "v_mfma_f32_16x16x32_bf16 a[84:87], v[0:3], v[52:55], a[84:87]\n"
        "global_load_dwordx2 v[96:97], v170, s[74:75] offset:-4096\n"
        "v_mfma_f32_16x16x32_bf16 a[88:91], v[0:3], v[56:59], 0\n"
        "v_mfma_f32_16x16x32_bf16 a[88:91], v[4:7], v[88:91], a[88:91]\n"
        "v_mfma_f32_16x16x32_bf16 a[120:123], v[12:15], v[88:91], 0\n"
        "v_mfma_f32_16x16x32_bf16 a[120:123], v[8:11], v[56:59], a[120:123]\n"
        "s_waitcnt vmcnt(16) lgkmcnt(0)\n"
        "v_mfma_f32_16x16x32_bf16 a[124:127], v[8:11], v[60:63], 0\n"
        "s_barrier\n"
        "v_mfma_f32_16x16x32_bf16 a[124:127], v[12:15], v[92:95], a[124:127]\n"
        "global_load_lds_dwordx4 v234, s[56:57] offset:0\n"
        "ds_read_b128 v[8:11], v224 offset:51200\n"
        "v_mfma_f32_16x16x32_bf16 a[92:95], v[4:7], v[92:95], 0\n"
        "ds_read_b128 v[4:7], v225 offset:49152\n"
        "v_mfma_f32_16x16x32_bf16 a[92:95], v[0:3], v[60:63], a[92:95]\n"
        "ds_read_b128 v[0:3], v224 offset:49152\n"
        "v_mfma_f32_16x16x32_bf16 a[192:195], v[32:35], v[16:19], 0\n"
        "ds_read_b128 v[12:15], v225 offset:51200\n"
        "v_mfma_f32_16x16x32_bf16 a[192:195], v[64:67], v[20:23], a[192:195]\n"
        "global_load_lds_dwordx4 v235, s[56:57] offset:1024\n"
        "v_mfma_f32_16x16x32_bf16 a[224:227], v[64:67], v[28:31], 0\n"
        "ds_read_b64_tr_b16 v[64:65], v226 offset:40960\n"
        "v_mfma_f32_16x16x32_bf16 a[224:227], v[32:35], v[24:27], a[224:227]\n"
        "ds_read_b64_tr_b16 v[32:33], v226 offset:32768\n"
        "v_mfma_f32_16x16x32_bf16 a[228:231], v[36:39], v[24:27], 0\n"
        "ds_read_b64_tr_b16 v[34:35], v227 offset:32768\n"
        "v_mfma_f32_16x16x32_bf16 a[228:231], v[68:71], v[28:31], a[228:231]\n"
        "ds_read_b64_tr_b16 v[66:67], v227 offset:40960\n"
        "global_load_lds_dwordx4 v236, s[56:57] offset:2048\n"
        "v_mfma_f32_16x16x32_bf16 a[196:199], v[68:71], v[20:23], 0\n"
        "ds_read_b64_tr_b16 v[68:69], v228 offset:40960\n"
        "v_mfma_f32_16x16x32_bf16 a[196:199], v[36:39], v[16:19], a[196:199]\n"
        "ds_read_b64_tr_b16 v[36:37], v228 offset:32768\n"
        "v_mfma_f32_16x16x32_bf16 a[200:203], v[40:43], v[16:19], 0\n"
        "ds_read_b64_tr_b16 v[38:39], v229 offset:32768\n"
        "v_mfma_f32_16x16x32_bf16 a[200:203], v[72:75], v[20:23], a[200:203]\n"
        "ds_read_b64_tr_b16 v[70:71], v229 offset:40960\n"
        "global_load_lds_dwordx4 v237, s[56:57] offset:3072\n"
        "s_add_u32 m0, s72, 0x1000\n"
        "v_mfma_f32_16x16x32_bf16 a[232:235], v[72:75], v[28:31], 0\n"
        "ds_read_b64_tr_b16 v[72:73], v230 offset:40960\n"
        "v_mfma_f32_16x16x32_bf16 a[232:235], v[40:43], v[24:27], a[232:235]\n"
        "ds_read_b64_tr_b16 v[40:41], v230 offset:32768\n"
        "v_mfma_f32_16x16x32_bf16 a[236:239], v[44:47], v[24:27], 0\n"
        "ds_read_b64_tr_b16 v[42:43], v231 offset:32768\n"
        "v_mfma_f32_16x16x32_bf16 a[236:239], v[76:79], v[28:31], a[236:239]\n"
        "ds_read_b64_tr_b16 v[74:75], v231 offset:40960\n"
        "global_load_dwordx2 v[98:99], v170, s[74:75] offset:-2048\n"
        "v_mfma_f32_16x16x32_bf16 a[204:207], v[76:79], v[20:23], 0\n"
        "ds_read_b64_tr_b16 v[76:77], v232 offset:40960\n"
        "global_load_lds_dwordx4 v234, s[58:59] offset:0\n"
        "v_mfma_f32_16x16x32_bf16 a[204:207], v[44:47], v[16:19], a[204:207]\n"
        "ds_read_b64_tr_b16 v[44:45], v232 offset:32768\n"
        "v_mfma_f32_16x16x32_bf16 a[208:211], v[16:19], v[48:51], 0\n"
        "ds_read_b64_tr_b16 v[46:47], v233 offset:32768\n"
        "v_mfma_f32_16x16x32_bf16 a[208:211], v[20:23], v[80:83], a[208:211]\n"
        "ds_read_b64_tr_b16 v[78:79], v233 offset:40960\n"
        "v_mfma_f32_16x16x32_bf16 a[240:243], v[28:31], v[80:83], 0\n"
        "ds_read_b64_tr_b16 v[80:81], v226 offset:57344\n"
        "global_load_lds_dwordx4 v235, s[58:59] offset:1024\n"
        "v_mfma_f32_16x16x32_bf16 a[240:243], v[24:27], v[48:51], a[240:243]\n"
        "ds_read_b64_tr_b16 v[48:49], v226 offset:49152\n"
        "v_mfma_f32_16x16x32_bf16 a[244:247], v[24:27], v[52:55], 0\n"
        "ds_read_b64_tr_b16 v[50:51], v227 offset:49152\n"
        "v_mfma_f32_16x16x32_bf16 a[244:247], v[28:31], v[84:87], a[244:247]\n"
        "ds_read_b64_tr_b16 v[82:83], v227 offset:57344\n"
        "v_mfma_f32_16x16x32_bf16 a[212:215], v[20:23], v[84:87], 0\n"
        "ds_read_b64_tr_b16 v[84:85], v228 offset:57344\n"
        "global_load_lds_dwordx4 v236, s[58:59] offset:2048\n"
        "v_mfma_f32_16x16x32_bf16 a[212:215], v[16:19], v[52:55], a[212:215]\n"
        "ds_read_b64_tr_b16 v[52:53], v228 offset:49152\n"
        "v_mfma_f32_16x16x32_bf16 a[216:219], v[16:19], v[56:59], 0\n"
        "ds_read_b64_tr_b16 v[54:55], v229 offset:49152\n"
        "v_mfma_f32_16x16x32_bf16 a[216:219], v[20:23], v[88:91], a[216:219]\n"
        "ds_read_b64_tr_b16 v[86:87], v229 offset:57344\n"
        "v_mfma_f32_16x16x32_bf16 a[248:251], v[28:31], v[88:91], 0\n"
        "ds_read_b64_tr_b16 v[88:89], v230 offset:57344\n"
        "global_load_lds_dwordx4 v237, s[58:59] offset:3072\n"
        "s_add_u32 m0, s73, 0x10000\n"
        "v_mfma_f32_16x16x32_bf16 a[248:251], v[24:27], v[56:59], a[248:251]\n"
        "ds_read_b64_tr_b16 v[56:57], v230 offset:49152\n"
        "v_mfma_f32_16x16x32_bf16 a[252:255], v[24:27], v[60:63], 0\n"
        "ds_read_b64_tr_b16 v[58:59], v231 offset:49152\n"
        "s_waitcnt lgkmcnt(8)\n"
        "v_mfma_f32_16x16x32_bf16 a[252:255], v[28:31], v[92:95], a[252:255]\n"
        "ds_read_b64_tr_b16 v[90:91], v231 offset:57344\n"
        "v_mfma_f32_16x16x32_bf16 a[220:223], v[20:23], v[92:95], 0\n"
        "ds_read_b64_tr_b16 v[92:93], v232 offset:57344\n"
        "global_load_dwordx2 v[100:101], v170, s[74:75] offset:0\n"
        "v_mfma_f32_16x16x32_bf16 a[220:223], v[16:19], v[60:63], a[220:223]\n"
        "ds_read_b64_tr_b16 v[60:61], v232 offset:49152\n"
        "v_mfma_f32_16x16x32_bf16 a[0:3], v[32:35], v[0:3], a[0:3]\n"
        "ds_read_b64_tr_b16 v[62:63], v233 offset:49152\n"
        "v_mfma_f32_16x16x32_bf16 a[0:3], v[64:67], v[4:7], a[0:3]\n"
        "ds_read_b64_tr_b16 v[94:95], v233 offset:57344\n"
        "v_mfma_f32_16x16x32_bf16 a[32:35], v[64:67], v[12:15], a[32:35]\n"
        "ds_read_b128 v[16:19], v224 offset:53248\n"
        "v_mfma_f32_16x16x32_bf16 a[32:35], v[32:35], v[8:11], a[32:35]\n"
        "ds_read_b128 v[20:23], v225 offset:53248\n"
        "v_mfma_f32_16x16x32_bf16 a[36:39], v[36:39], v[8:11], a[36:39]\n"
        "ds_read_b128 v[24:27], v224 offset:55296\n"
        "v_mfma_f32_16x16x32_bf16 a[36:39], v[68:71], v[12:15], a[36:39]\n"
        "ds_read_b128 v[28:31], v225 offset:55296\n"
        "v_mfma_f32_16x16x32_bf16 a[4:7], v[68:71], v[4:7], a[4:7]\n"
        "v_mfma_f32_16x16x32_bf16 a[4:7], v[36:39], v[0:3], a[4:7]\n"
        "global_load_dwordx2 v[102:103], v169, s[74:75] offset:2048\n"
        "s_add_u32 s74, s20, 0x1800\n"
        "s_addc_u32 s75, s21, 0\n"
        "v_mfma_f32_16x16x32_bf16 a[8:11], v[40:43], v[0:3], a[8:11]\n"
        "v_mfma_f32_16x16x32_bf16 a[8:11], v[72:75], v[4:7], a[8:11]\n"
        "v_mfma_f32_16x16x32_bf16 a[40:43], v[72:75], v[12:15], a[40:43]\n"
        "v_mfma_f32_16x16x32_bf16 a[40:43], v[40:43], v[8:11], a[40:43]\n"
        "v_mfma_f32_16x16x32_bf16 a[44:47], v[44:47], v[8:11], a[44:47]\n"
        "v_mfma_f32_16x16x32_bf16 a[44:47], v[76:79], v[12:15], a[44:47]\n"
        "s_waitcnt lgkmcnt(0)\n"
        "v_mfma_f32_16x16x32_bf16 a[12:15], v[76:79], v[4:7], a[12:15]\n"
        "v_mfma_f32_16x16x32_bf16 a[12:15], v[44:47], v[0:3], a[12:15]\n"
        "global_load_dword v253, v173, s[82:83] offset:0\n"
        "s_add_u32 s82, s12, 0x200000\n"
        "s_addc_u32 s83, s13, 0\n"
        "v_mfma_f32_16x16x32_bf16 a[16:19], v[0:3], v[48:51], a[16:19]\n"
        "v_mfma_f32_16x16x32_bf16 a[16:19], v[4:7], v[80:83], a[16:19]\n"
        "v_mfma_f32_16x16x32_bf16 a[48:51], v[12:15], v[80:83], a[48:51]\n"
        "v_mfma_f32_16x16x32_bf16 a[48:51], v[8:11], v[48:51], a[48:51]\n"
        "v_mfma_f32_16x16x32_bf16 a[52:55], v[8:11], v[52:55], a[52:55]\n"
        "v_mfma_f32_16x16x32_bf16 a[52:55], v[12:15], v[84:87], a[52:55]\n"
        "v_mfma_f32_16x16x32_bf16 a[20:23], v[4:7], v[84:87], a[20:23]\n"
        "v_mfma_f32_16x16x32_bf16 a[20:23], v[0:3], v[52:55], a[20:23]\n"
        "global_load_dwordx2 v[104:105], v169, s[74:75] offset:-4096\n"
        "v_mfma_f32_16x16x32_bf16 a[24:27], v[0:3], v[56:59], a[24:27]\n"
        "v_mfma_f32_16x16x32_bf16 a[24:27], v[4:7], v[88:91], a[24:27]\n"
        "v_mfma_f32_16x16x32_bf16 a[56:59], v[12:15], v[88:91], a[56:59]\n"
        "v_mfma_f32_16x16x32_bf16 a[56:59], v[8:11], v[56:59], a[56:59]\n"
        "v_mfma_f32_16x16x32_bf16 a[60:63], v[8:11], v[60:63], a[60:63]\n"
        "ds_read_b128 v[8:11], v224 offset:34816\n"
        "v_mfma_f32_16x16x32_bf16 a[60:63], v[12:15], v[92:95], a[60:63]\n"
        "ds_read_b128 v[12:15], v225 offset:34816\n"
        "v_mfma_f32_16x16x32_bf16 a[28:31], v[4:7], v[92:95], a[28:31]\n"
        "ds_read_b128 v[4:7], v225 offset:32768\n"
        "v_mfma_f32_16x16x32_bf16 a[28:31], v[0:3], v[60:63], a[28:31]\n"
        "global_load_lds_dwordx4 v238, s[86:87] offset:0\n"
        "ds_read_b128 v[0:3], v224 offset:32768\n"
        "v_mfma_f32_16x16x32_bf16 a[64:67], v[32:35], v[16:19], a[64:67]\n"
        "v_mfma_f32_16x16x32_bf16 a[64:67], v[64:67], v[20:23], a[64:67]\n"
        "v_mfma_f32_16x16x32_bf16 a[96:99], v[64:67], v[28:31], a[96:99]\n"
        "v_mfma_f32_16x16x32_bf16 a[96:99], v[32:35], v[24:27], a[96:99]\n"
        "v_mfma_f32_16x16x32_bf16 a[100:103], v[36:39], v[24:27], a[100:103]\n"
        "v_mfma_f32_16x16x32_bf16 a[100:103], v[68:71], v[28:31], a[100:103]\n"
        "global_load_lds_dwordx4 v239, s[86:87] offset:1024\n"
        "v_mfma_f32_16x16x32_bf16 a[68:71], v[68:71], v[20:23], a[68:71]\n"
        "v_mfma_f32_16x16x32_bf16 a[68:71], v[36:39], v[16:19], a[68:71]\n"
        "v_mfma_f32_16x16x32_bf16 a[72:75], v[40:43], v[16:19], a[72:75]\n"
        "v_mfma_f32_16x16x32_bf16 a[72:75], v[72:75], v[20:23], a[72:75]\n"
        "global_load_dwordx2 v[106:107], v169, s[74:75] offset:-2048\n"
        "v_mfma_f32_16x16x32_bf16 a[104:107], v[72:75], v[28:31], a[104:107]\n"
        "v_mfma_f32_16x16x32_bf16 a[104:107], v[40:43], v[24:27], a[104:107]\n"
        "global_load_lds_dwordx4 v240, s[86:87] offset:2048\n"
        "v_mfma_f32_16x16x32_bf16 a[108:111], v[44:47], v[24:27], a[108:111]\n"
        "LHA0_%=:\n"
        "s_cbranch_execz LHA4_%=\n"
        "v_mfma_f32_16x16x32_bf16 a[108:111], v[76:79], v[28:31], a[108:111]\n"
        "v_mfma_f32_16x16x32_bf16 a[76:79], v[76:79], v[20:23], a[76:79]\n"
        "v_mfma_f32_16x16x32_bf16 a[76:79], v[44:47], v[16:19], a[76:79]\n"
        "v_mfma_f32_16x16x32_bf16 a[80:83], v[16:19], v[48:51], a[80:83]\n"
        "v_mfma_f32_16x16x32_bf16 a[80:83], v[20:23], v[80:83], a[80:83]\n"
        "global_load_lds_dwordx4 v241, s[86:87] offset:3072\n"
        "s_add_u32 m0, s73, 0x14000\n"
        "v_mfma_f32_16x16x32_bf16 a[112:115], v[28:31], v[80:83], a[112:115]\n"
        "s_add_u32 s86, s62, 0x80000\n"
        "s_addc_u32 s87, s63, 0\n"
        "v_mfma_f32_16x16x32_bf16 a[112:115], v[24:27], v[48:51], a[112:115]\n"
        "v_mfma_f32_16x16x32_bf16 a[116:119], v[24:27], v[52:55], a[116:119]\n"
        "v_mfma_f32_16x16x32_bf16 a[116:119], v[28:31], v[84:87], a[116:119]\n"
        "v_mfma_f32_16x16x32_bf16 a[84:87], v[20:23], v[84:87], a[84:87]\n"
        "s_waitcnt vmcnt(21) lgkmcnt(0)\n"
        "v_lshl_or_b32 v172, v172, 16, v198\n"
        "v_mfma_f32_16x16x32_bf16 a[84:87], v[16:19], v[52:55], a[84:87]\n"
        "global_load_lds_dwordx4 v238, s[86:87] offset:0\n"
        "v_readfirstlane_b32 s54, v172\n"
        "v_mfma_f32_16x16x32_bf16 a[88:91], v[16:19], v[56:59], a[88:91]\n"
        "s_waitcnt vmcnt(21)\n"
        "v_lshlrev_b32 v185, 16, v134\n"
        "v_and_b32 v186, s35, v134\n"
        "v_mfma_f32_16x16x32_bf16 a[88:91], v[20:23], v[88:91], a[88:91]\n"
        "v_lshlrev_b32 v187, 16, v135\n"
        "v_and_b32 v188, s35, v135\n"
        "v_mfma_f32_16x16x32_bf16 a[120:123], v[28:31], v[88:91], a[120:123]\n"
        "v_mfma_f32_16x16x32_bf16 a[120:123], v[24:27], v[56:59], a[120:123]\n"
        "global_load_dwordx2 v[108:109], v169, s[74:75] offset:0\n"
        "v_mfma_f32_16x16x32_bf16 a[124:127], v[24:27], v[60:63], a[124:127]\n"
        "ds_read_b128 v[24:27], v224 offset:38912\n"
        "v_mfma_f32_16x16x32_bf16 a[124:127], v[28:31], v[92:95], a[124:127]\n"
        "global_load_lds_dwordx4 v239, s[86:87] offset:1024\n"
        "ds_read_b128 v[28:31], v225 offset:38912\n"
        "v_mfma_f32_16x16x32_bf16 a[92:95], v[20:23], v[92:95], a[92:95]\n"
        "ds_read_b128 v[20:23], v225 offset:36864\n"
        "v_mfma_f32_16x16x32_bf16 a[92:95], v[16:19], v[60:63], a[92:95]\n"
        "ds_read_b128 v[16:19], v224 offset:36864\n"
        "v_mfma_f32_16x16x32_bf16 a[128:131], v[32:35], v[0:3], a[128:131]\n"
        "v_mfma_f32_16x16x32_bf16 a[128:131], v[64:67], v[4:7], a[128:131]\n"
        "v_mfma_f32_16x16x32_bf16 a[160:163], v[64:67], v[12:15], a[160:163]\n"
        "v_mfma_f32_16x16x32_bf16 a[160:163], v[32:35], v[8:11], a[160:163]\n"
        "global_load_lds_dwordx4 v240, s[86:87] offset:2048\n"
        "v_mfma_f32_16x16x32_bf16 a[164:167], v[36:39], v[8:11], a[164:167]\n"
        "v_mfma_f32_16x16x32_bf16 a[164:167], v[68:71], v[12:15], a[164:167]\n"
        "v_mfma_f32_16x16x32_bf16 a[132:135], v[68:71], v[4:7], a[132:135]\n"
        "v_mfma_f32_16x16x32_bf16 a[132:135], v[36:39], v[0:3], a[132:135]\n"
        "global_load_dwordx2 v[110:111], v169, s[74:75] offset:2048\n"
        "s_add_u32 s74, s20, 0x3800\n"
        "s_addc_u32 s75, s21, 0\n"
        "v_mfma_f32_16x16x32_bf16 a[136:139], v[40:43], v[0:3], a[136:139]\n"
        "v_mfma_f32_16x16x32_bf16 a[136:139], v[72:75], v[4:7], a[136:139]\n"
        "global_load_lds_dwordx4 v241, s[86:87] offset:3072\n"
        "s_add_u32 m0, s72, 0x8000\n"
        "v_mfma_f32_16x16x32_bf16 a[168:171], v[72:75], v[12:15], a[168:171]\n"
        "s_add_u32 s86, s56, 0x80\n"
        "s_addc_u32 s87, s57, 0\n"
        "v_mfma_f32_16x16x32_bf16 a[168:171], v[40:43], v[8:11], a[168:171]\n"
        "v_mfma_f32_16x16x32_bf16 a[172:175], v[44:47], v[8:11], a[172:175]\n"
        "v_mfma_f32_16x16x32_bf16 a[172:175], v[76:79], v[12:15], a[172:175]\n"
        "v_mfma_f32_16x16x32_bf16 a[140:143], v[76:79], v[4:7], a[140:143]\n"
        "v_mfma_f32_16x16x32_bf16 a[140:143], v[44:47], v[0:3], a[140:143]\n"
        "v_mfma_f32_16x16x32_bf16 a[144:147], v[0:3], v[48:51], a[144:147]\n"
        "v_mfma_f32_16x16x32_bf16 a[144:147], v[4:7], v[80:83], a[144:147]\n"
        "v_mfma_f32_16x16x32_bf16 a[176:179], v[12:15], v[80:83], a[176:179]\n"
        "v_mfma_f32_16x16x32_bf16 a[176:179], v[8:11], v[48:51], a[176:179]\n"
        "v_mfma_f32_16x16x32_bf16 a[180:183], v[8:11], v[52:55], a[180:183]\n"
        "s_waitcnt vmcnt(24) lgkmcnt(0)\n"
        "v_cndmask_b32 v96, v96, 0, s[78:79]\n"
        "v_cndmask_b32 v97, v97, 0, s[78:79]\n"
        "v_mfma_f32_16x16x32_bf16 a[180:183], v[12:15], v[84:87], a[180:183]\n"
        "v_lshlrev_b32 v174, 16, v96\n"
        "v_mul_f32 v189, v174, v188\n"
        "v_mfma_f32_16x16x32_bf16 a[148:151], v[4:7], v[84:87], a[148:151]\n"
        "v_cvt_pk_bf16_f32 v189, 0, v189\n"
        "v_and_b32 v174, s35, v96\n"
        "v_mfma_f32_16x16x32_bf16 a[148:151], v[0:3], v[52:55], a[148:151]\n"
        "global_load_dwordx2 v[112:113], v169, s[74:75] offset:-4096\n"
        "v_mfma_f32_16x16x32_bf16 a[152:155], v[0:3], v[56:59], a[152:155]\n"
        "v_mfma_f32_16x16x32_bf16 a[152:155], v[4:7], v[88:91], a[152:155]\n"
        "v_mfma_f32_16x16x32_bf16 a[184:187], v[12:15], v[88:91], a[184:187]\n"
        "v_mfma_f32_16x16x32_bf16 a[184:187], v[8:11], v[56:59], a[184:187]\n"
        "s_waitcnt vmcnt(16)\n"
        "v_mfma_f32_16x16x32_bf16 a[188:191], v[8:11], v[60:63], a[188:191]\n"
        "s_barrier\n"
        "v_mfma_f32_16x16x32_bf16 a[188:191], v[12:15], v[92:95], a[188:191]\n"
        "global_load_lds_dwordx4 v234, s[86:87] offset:0\n"
        "ds_read_b128 v[8:11], v224 offset:18432\n"
        "v_mfma_f32_16x16x32_bf16 a[156:159], v[4:7], v[92:95], a[156:159]\n"
        "ds_read_b128 v[4:7], v225 offset:16384\n"
        "v_mfma_f32_16x16x32_bf16 a[156:159], v[0:3], v[60:63], a[156:159]\n"
        "ds_read_b128 v[0:3], v224 offset:16384\n"
        "v_mfma_f32_16x16x32_bf16 a[192:195], v[32:35], v[16:19], a[192:195]\n"
        "ds_read_b128 v[12:15], v225 offset:18432\n"
        "v_mfma_f32_16x16x32_bf16 a[192:195], v[64:67], v[20:23], a[192:195]\n"
        "global_load_lds_dwordx4 v235, s[86:87] offset:1024\n"
        "v_mfma_f32_16x16x32_bf16 a[224:227], v[64:67], v[28:31], a[224:227]\n"
        "ds_read_b64_tr_b16 v[64:65], v216 offset:8192\n"
        "v_mfma_f32_16x16x32_bf16 a[224:227], v[32:35], v[24:27], a[224:227]\n"
        "ds_read_b64_tr_b16 v[32:33], v216 offset:0\n"
        "v_mfma_f32_16x16x32_bf16 a[228:231], v[36:39], v[24:27], a[228:231]\n"
        "ds_read_b64_tr_b16 v[34:35], v217 offset:0\n"
        "v_mfma_f32_16x16x32_bf16 a[228:231], v[68:71], v[28:31], a[228:231]\n"
        "ds_read_b64_tr_b16 v[66:67], v217 offset:8192\n"
        "global_load_lds_dwordx4 v236, s[86:87] offset:2048\n"
        "v_mfma_f32_16x16x32_bf16 a[196:199], v[68:71], v[20:23], a[196:199]\n"
        "ds_read_b64_tr_b16 v[68:69], v218 offset:8192\n"
        "v_mfma_f32_16x16x32_bf16 a[196:199], v[36:39], v[16:19], a[196:199]\n"
        "ds_read_b64_tr_b16 v[36:37], v218 offset:0\n"
        "v_mfma_f32_16x16x32_bf16 a[200:203], v[40:43], v[16:19], a[200:203]\n"
        "ds_read_b64_tr_b16 v[38:39], v219 offset:0\n"
        "v_mfma_f32_16x16x32_bf16 a[200:203], v[72:75], v[20:23], a[200:203]\n"
        "ds_read_b64_tr_b16 v[70:71], v219 offset:8192\n"
        "global_load_lds_dwordx4 v237, s[86:87] offset:3072\n"
        "s_add_u32 m0, s72, 0x9000\n"
        "v_mfma_f32_16x16x32_bf16 a[232:235], v[72:75], v[28:31], a[232:235]\n"
        "ds_read_b64_tr_b16 v[72:73], v220 offset:8192\n"
        "s_add_u32 s86, s58, 0x80\n"
        "s_addc_u32 s87, s59, 0\n"
        "v_mfma_f32_16x16x32_bf16 a[232:235], v[40:43], v[24:27], a[232:235]\n"
        "ds_read_b64_tr_b16 v[40:41], v220 offset:0\n"
        "v_cndmask_b32 v98, v98, 0, s[78:79]\n"
        "v_mfma_f32_16x16x32_bf16 a[236:239], v[44:47], v[24:27], a[236:239]\n"
        "ds_read_b64_tr_b16 v[42:43], v221 offset:0\n"
        "v_cndmask_b32 v99, v99, 0, s[78:79]\n"
        "v_mfma_f32_16x16x32_bf16 a[236:239], v[76:79], v[28:31], a[236:239]\n"
        "ds_read_b64_tr_b16 v[74:75], v221 offset:8192\n"
        "global_load_dwordx2 v[114:115], v169, s[74:75] offset:-2048\n"
        "v_mfma_f32_16x16x32_bf16 a[204:207], v[76:79], v[20:23], a[204:207]\n"
        "ds_read_b64_tr_b16 v[76:77], v222 offset:8192\n"
        "global_load_lds_dwordx4 v234, s[86:87] offset:0\n"
        "v_mfma_f32_16x16x32_bf16 a[204:207], v[44:47], v[16:19], a[204:207]\n"
        "ds_read_b64_tr_b16 v[44:45], v222 offset:0\n"
        "v_lshlrev_b32 v175, 16, v98\n"
        "v_mfma_f32_16x16x32_bf16 a[208:211], v[16:19], v[48:51], a[208:211]\n"
        "ds_read_b64_tr_b16 v[46:47], v223 offset:0\n"
        "v_mul_f32 v191, v175, v188\n"
        "v_mfma_f32_16x16x32_bf16 a[208:211], v[20:23], v[80:83], a[208:211]\n"
        "ds_read_b64_tr_b16 v[78:79], v223 offset:8192\n"
        "v_cvt_pk_bf16_f32 v191, 0, v191\n"
        "v_mfma_f32_16x16x32_bf16 a[240:243], v[28:31], v[80:83], a[240:243]\n"
        "ds_read_b64_tr_b16 v[80:81], v216 offset:24576\n"
        "global_load_lds_dwordx4 v235, s[86:87] offset:1024\n"
        "v_mfma_f32_16x16x32_bf16 a[240:243], v[24:27], v[48:51], a[240:243]\n"
        "ds_read_b64_tr_b16 v[48:49], v216 offset:16384\n"
        "v_mul_f32 v190, v175, v187\n"
        "v_mfma_f32_16x16x32_bf16 a[244:247], v[24:27], v[52:55], a[244:247]\n"
        "ds_read_b64_tr_b16 v[50:51], v217 offset:16384\n"
        "v_cvt_pk_bf16_f32 v190, 0, v190\n"
        "v_mfma_f32_16x16x32_bf16 a[244:247], v[28:31], v[84:87], a[244:247]\n"
        "ds_read_b64_tr_b16 v[82:83], v217 offset:24576\n"
        "v_add_f32 v189, v189, v190\n"
        "v_mfma_f32_16x16x32_bf16 a[212:215], v[20:23], v[84:87], a[212:215]\n"
        "ds_read_b64_tr_b16 v[84:85], v218 offset:24576\n"
        "global_load_lds_dwordx4 v236, s[86:87] offset:2048\n"
        "v_mfma_f32_16x16x32_bf16 a[212:215], v[16:19], v[52:55], a[212:215]\n"
        "ds_read_b64_tr_b16 v[52:53], v218 offset:16384\n"
        "v_cvt_pk_bf16_f32 v189, 0, v189\n"
        "v_mfma_f32_16x16x32_bf16 a[216:219], v[16:19], v[56:59], a[216:219]\n"
        "ds_read_b64_tr_b16 v[54:55], v219 offset:16384\n"
        "v_and_b32 v175, s35, v98\n"
        "v_mfma_f32_16x16x32_bf16 a[216:219], v[20:23], v[88:91], a[216:219]\n"
        "ds_read_b64_tr_b16 v[86:87], v219 offset:24576\n"
        "v_mfma_f32_16x16x32_bf16 a[248:251], v[28:31], v[88:91], a[248:251]\n"
        "ds_read_b64_tr_b16 v[88:89], v220 offset:24576\n"
        "global_load_lds_dwordx4 v237, s[86:87] offset:3072\n"
        "s_add_u32 m0, s73, 0x18000\n"
        "v_mfma_f32_16x16x32_bf16 a[248:251], v[24:27], v[56:59], a[248:251]\n"
        "ds_read_b64_tr_b16 v[56:57], v220 offset:16384\n"
        "s_add_u32 s86, s60, 0x100000\n"
        "s_addc_u32 s87, s61, 0\n"
        "v_mfma_f32_16x16x32_bf16 a[252:255], v[24:27], v[60:63], a[252:255]\n"
        "ds_read_b64_tr_b16 v[58:59], v221 offset:16384\n"
        "s_waitcnt lgkmcnt(8)\n"
        "v_mfma_f32_16x16x32_bf16 a[252:255], v[28:31], v[92:95], a[252:255]\n"
        "ds_read_b64_tr_b16 v[90:91], v221 offset:24576\n"
        "s_waitcnt vmcnt(24) lgkmcnt(8)\n"
        "v_mfma_f32_16x16x32_bf16 a[220:223], v[20:23], v[92:95], a[220:223]\n"
        "ds_read_b64_tr_b16 v[92:93], v222 offset:24576\n"
        "global_load_dwordx2 v[116:117], v169, s[74:75] offset:0\n"
        "v_mfma_f32_16x16x32_bf16 a[220:223], v[16:19], v[60:63], a[220:223]\n"
        "ds_read_b64_tr_b16 v[60:61], v222 offset:16384\n"
        "s_add_u32 s74, s22, 0x1000\n"
        "s_addc_u32 s75, s23, 0\n"
        "v_mfma_f32_16x16x32_bf16 a[0:3], v[32:35], v[0:3], a[0:3]\n"
        "ds_read_b64_tr_b16 v[62:63], v223 offset:16384\n"
        "v_cndmask_b32 v100, v100, 0, s[78:79]\n"
        "v_mfma_f32_16x16x32_bf16 a[0:3], v[64:67], v[4:7], a[0:3]\n"
        "ds_read_b64_tr_b16 v[94:95], v223 offset:24576\n"
        "v_cndmask_b32 v101, v101, 0, s[78:79]\n"
        "v_mfma_f32_16x16x32_bf16 a[32:35], v[64:67], v[12:15], a[32:35]\n"
        "ds_read_b128 v[16:19], v224 offset:20480\n"
        "v_lshlrev_b32 v176, 16, v100\n"
        "v_mfma_f32_16x16x32_bf16 a[32:35], v[32:35], v[8:11], a[32:35]\n"
        "ds_read_b128 v[20:23], v225 offset:20480\n"
        "v_mul_f32 v192, v176, v187\n"
        "v_mfma_f32_16x16x32_bf16 a[36:39], v[36:39], v[8:11], a[36:39]\n"
        "ds_read_b128 v[24:27], v224 offset:22528\n"
        "v_cvt_pk_bf16_f32 v192, 0, v192\n"
        "v_mfma_f32_16x16x32_bf16 a[36:39], v[68:71], v[12:15], a[36:39]\n"
        "ds_read_b128 v[28:31], v225 offset:22528\n"
        "v_add_f32 v191, v191, v192\n"
        "v_mfma_f32_16x16x32_bf16 a[4:7], v[68:71], v[4:7], a[4:7]\n"
        "s_waitcnt vmcnt(24) lgkmcnt(7)\n"
        "v_lshlrev_b32 v177, 16, v102\n"
        "v_cvt_pk_bf16_f32 v191, 0, v191\n"
        "v_mfma_f32_16x16x32_bf16 a[4:7], v[36:39], v[0:3], a[4:7]\n"
        "global_load_dwordx2 v[118:119], v169, s[74:75] offset:-4096\n"
        "v_mul_f32 v190, v176, v186\n"
        "v_mfma_f32_16x16x32_bf16 a[8:11], v[40:43], v[0:3], a[8:11]\n"
        "v_mul_f32 v192, v177, v186\n"
        "v_cvt_pk_bf16_f32 v190, 0, v190\n"
        "v_mfma_f32_16x16x32_bf16 a[8:11], v[72:75], v[4:7], a[8:11]\n"
        "v_cvt_pk_bf16_f32 v192, 0, v192\n"
        "v_add_f32 v189, v189, v190\n"
        "v_mfma_f32_16x16x32_bf16 a[40:43], v[72:75], v[12:15], a[40:43]\n"
        "v_add_f32 v191, v191, v192\n"
        "v_cvt_pk_bf16_f32 v189, 0, v189\n"
        "v_mfma_f32_16x16x32_bf16 a[40:43], v[40:43], v[8:11], a[40:43]\n"
        "v_cvt_pk_bf16_f32 v191, 0, v191\n"
        "v_mul_f32 v190, v177, v185\n"
        "v_mfma_f32_16x16x32_bf16 a[44:47], v[44:47], v[8:11], a[44:47]\n"
        "v_cvt_pk_bf16_f32 v190, 0, v190\n"
        "v_add_f32 v189, v189, v190\n"
        "v_mfma_f32_16x16x32_bf16 a[44:47], v[76:79], v[12:15], a[44:47]\n"
        "v_cvt_pk_bf16_f32 v189, 0, v189\n"
        "v_mul_f32 v193, v176, v188\n"
        "v_mfma_f32_16x16x32_bf16 a[12:15], v[76:79], v[4:7], a[12:15]\n"
        "v_mul_f32 v195, v177, v188\n"
        "v_cvt_pk_bf16_f32 v193, 0, v193\n"
        "v_mfma_f32_16x16x32_bf16 a[12:15], v[44:47], v[0:3], a[12:15]\n"
        "global_load_dword v253, v173, s[82:83] offset:0\n"
        "s_add_u32 s82, s12, 0x300000\n"
        "s_addc_u32 s83, s13, 0\n"
        "v_mfma_f32_16x16x32_bf16 a[16:19], v[0:3], v[48:51], a[16:19]\n"
        "v_cvt_pk_bf16_f32 v195, 0, v195\n"
        "v_mul_f32 v194, v177, v187\n"
        "v_mfma_f32_16x16x32_bf16 a[16:19], v[4:7], v[80:83], a[16:19]\n"
        "v_cvt_pk_bf16_f32 v194, 0, v194\n"
        "v_add_f32 v193, v193, v194\n"
        "v_mfma_f32_16x16x32_bf16 a[48:51], v[12:15], v[80:83], a[48:51]\n"
        "v_cvt_pk_bf16_f32 v193, 0, v193\n"
        "v_and_b32 v176, s35, v100\n"
        "v_mfma_f32_16x16x32_bf16 a[48:51], v[8:11], v[48:51], a[48:51]\n"
        "v_and_b32 v177, s35, v102\n"
        "v_mfma_f32_16x16x32_bf16 a[52:55], v[8:11], v[52:55], a[52:55]\n"
        "s_waitcnt vmcnt(24) lgkmcnt(0)\n"
        "v_lshlrev_b32 v178, 16, v104\n"
        "v_mul_f32 v192, v178, v185\n"
        "v_mfma_f32_16x16x32_bf16 a[52:55], v[12:15], v[84:87], a[52:55]\n"
        "v_cvt_pk_bf16_f32 v192, 0, v192\n"
        "v_add_f32 v191, v191, v192\n"
        "v_mfma_f32_16x16x32_bf16 a[20:23], v[4:7], v[84:87], a[20:23]\n"
        "v_cvt_pk_bf16_f32 v191, 0, v191\n"
        "v_mul_f32 v196, v178, v187\n"
        "v_mfma_f32_16x16x32_bf16 a[20:23], v[0:3], v[52:55], a[20:23]\n"
        "global_load_dwordx2 v[120:121], v169, s[74:75] offset:-2048\n"
        "v_cvt_pk_bf16_f32 v196, 0, v196\n"
        "v_mfma_f32_16x16x32_bf16 a[24:27], v[0:3], v[56:59], a[24:27]\n"
        "v_add_f32 v195, v195, v196\n"
        "v_cvt_pk_bf16_f32 v195, 0, v195\n"
        "v_mfma_f32_16x16x32_bf16 a[24:27], v[4:7], v[88:91], a[24:27]\n"
        "v_mul_f32 v194, v178, v186\n"
        "v_cvt_pk_bf16_f32 v194, 0, v194\n"
        "v_mfma_f32_16x16x32_bf16 a[56:59], v[12:15], v[88:91], a[56:59]\n"
        "v_add_f32 v193, v193, v194\n"
        "v_cvt_pk_bf16_f32 v193, 0, v193\n"
        "v_mfma_f32_16x16x32_bf16 a[56:59], v[8:11], v[56:59], a[56:59]\n"
        "v_mfma_f32_16x16x32_bf16 a[60:63], v[8:11], v[60:63], a[60:63]\n"
        "ds_read_b128 v[8:11], v224 offset:2048\n"
        "v_mfma_f32_16x16x32_bf16 a[60:63], v[12:15], v[92:95], a[60:63]\n"
        "ds_read_b128 v[12:15], v225 offset:2048\n"
        "v_mfma_f32_16x16x32_bf16 a[28:31], v[4:7], v[92:95], a[28:31]\n"
        "ds_read_b128 v[4:7], v225 offset:0\n"
        "v_mfma_f32_16x16x32_bf16 a[28:31], v[0:3], v[60:63], a[28:31]\n"
        "global_load_lds_dwordx4 v238, s[86:87] offset:0\n"
        "ds_read_b128 v[0:3], v224 offset:0\n"
        "v_mfma_f32_16x16x32_bf16 a[64:67], v[32:35], v[16:19], a[64:67]\n"
        "v_mfma_f32_16x16x32_bf16 a[64:67], v[64:67], v[20:23], a[64:67]\n"
        "v_mfma_f32_16x16x32_bf16 a[96:99], v[64:67], v[28:31], a[96:99]\n"
        "v_mfma_f32_16x16x32_bf16 a[96:99], v[32:35], v[24:27], a[96:99]\n"
        "v_mfma_f32_16x16x32_bf16 a[100:103], v[36:39], v[24:27], a[100:103]\n"
        "v_mfma_f32_16x16x32_bf16 a[100:103], v[68:71], v[28:31], a[100:103]\n"
        "global_load_lds_dwordx4 v239, s[86:87] offset:1024\n"
        "v_mfma_f32_16x16x32_bf16 a[68:71], v[68:71], v[20:23], a[68:71]\n"
        "s_waitcnt vmcnt(24) lgkmcnt(1)\n"
        "v_lshlrev_b32 v179, 16, v106\n"
        "v_mul_f32 v196, v179, v186\n"
        "v_mfma_f32_16x16x32_bf16 a[68:71], v[36:39], v[16:19], a[68:71]\n"
        "v_cvt_pk_bf16_f32 v196, 0, v196\n"
        "v_add_f32 v195, v195, v196\n"
        "v_mfma_f32_16x16x32_bf16 a[72:75], v[40:43], v[16:19], a[72:75]\n"
        "v_cvt_pk_bf16_f32 v195, 0, v195\n"
        "v_mul_f32 v194, v179, v185\n"
        "v_mfma_f32_16x16x32_bf16 a[72:75], v[72:75], v[20:23], a[72:75]\n"
        "global_load_dwordx2 v[122:123], v169, s[74:75] offset:0\n"
        "v_cvt_pk_bf16_f32 v194, 0, v194\n"
        "v_mfma_f32_16x16x32_bf16 a[104:107], v[72:75], v[28:31], a[104:107]\n"
        "v_add_f32 v193, v193, v194\n"
        "v_cvt_pk_bf16_f32 v193, 0, v193\n"
        "v_mfma_f32_16x16x32_bf16 a[104:107], v[40:43], v[24:27], a[104:107]\n"
        "global_load_lds_dwordx4 v240, s[86:87] offset:2048\n"
        "v_mfma_f32_16x16x32_bf16 a[108:111], v[44:47], v[24:27], a[108:111]\n"
        "v_mfma_f32_16x16x32_bf16 a[108:111], v[76:79], v[28:31], a[108:111]\n"
        "v_mfma_f32_16x16x32_bf16 a[76:79], v[76:79], v[20:23], a[76:79]\n"
        "v_mfma_f32_16x16x32_bf16 a[76:79], v[44:47], v[16:19], a[76:79]\n"
        "v_mfma_f32_16x16x32_bf16 a[80:83], v[16:19], v[48:51], a[80:83]\n"
        "v_mfma_f32_16x16x32_bf16 a[80:83], v[20:23], v[80:83], a[80:83]\n"
        "global_load_lds_dwordx4 v241, s[86:87] offset:3072\n"
        "s_add_u32 m0, s73, 0x1c000\n"
        "v_mfma_f32_16x16x32_bf16 a[112:115], v[28:31], v[80:83], a[112:115]\n"
        "s_add_u32 s86, s62, 0x100000\n"
        "s_addc_u32 s87, s63, 0\n"
        "v_mfma_f32_16x16x32_bf16 a[112:115], v[24:27], v[48:51], a[112:115]\n"
        "v_mfma_f32_16x16x32_bf16 a[116:119], v[24:27], v[52:55], a[116:119]\n"
        "v_mfma_f32_16x16x32_bf16 a[116:119], v[28:31], v[84:87], a[116:119]\n"
        "v_mfma_f32_16x16x32_bf16 a[84:87], v[20:23], v[84:87], a[84:87]\n"
        "v_mfma_f32_16x16x32_bf16 a[84:87], v[16:19], v[52:55], a[84:87]\n"
        "global_load_lds_dwordx4 v238, s[86:87] offset:0\n"
        "v_mfma_f32_16x16x32_bf16 a[88:91], v[16:19], v[56:59], a[88:91]\n"
        "s_waitcnt vmcnt(24) lgkmcnt(0)\n"
        "v_lshlrev_b32 v180, 16, v108\n"
        "v_mul_f32 v196, v180, v185\n"
        "v_mfma_f32_16x16x32_bf16 a[88:91], v[20:23], v[88:91], a[88:91]\n"
        "v_cvt_pk_bf16_f32 v196, 0, v196\n"
        "v_add_f32 v195, v195, v196\n"
        "v_mfma_f32_16x16x32_bf16 a[120:123], v[28:31], v[88:91], a[120:123]\n"
        "v_cvt_pk_bf16_f32 v195, 0, v195\n"
        "v_mfma_f32_16x16x32_bf16 a[120:123], v[24:27], v[56:59], a[120:123]\n"
        "global_load_dwordx2 v[124:125], v169, s[74:75] offset:2048\n"
        "s_add_u32 s74, s22, 0x3000\n"
        "s_addc_u32 s75, s23, 0\n"
        "v_mfma_f32_16x16x32_bf16 a[124:127], v[24:27], v[60:63], a[124:127]\n"
        "ds_read_b128 v[24:27], v224 offset:6144\n"
        "v_mfma_f32_16x16x32_bf16 a[124:127], v[28:31], v[92:95], a[124:127]\n"
        "global_load_lds_dwordx4 v239, s[86:87] offset:1024\n"
        "ds_read_b128 v[28:31], v225 offset:6144\n"
        "v_mfma_f32_16x16x32_bf16 a[92:95], v[20:23], v[92:95], a[92:95]\n"
        "ds_read_b128 v[20:23], v225 offset:4096\n"
        "v_mfma_f32_16x16x32_bf16 a[92:95], v[16:19], v[60:63], a[92:95]\n"
        "ds_read_b128 v[16:19], v224 offset:4096\n"
        "v_mfma_f32_16x16x32_bf16 a[128:131], v[32:35], v[0:3], a[128:131]\n"
        "v_mfma_f32_16x16x32_bf16 a[128:131], v[64:67], v[4:7], a[128:131]\n"
        "v_mfma_f32_16x16x32_bf16 a[160:163], v[64:67], v[12:15], a[160:163]\n"
        "v_mfma_f32_16x16x32_bf16 a[160:163], v[32:35], v[8:11], a[160:163]\n"
        "global_load_lds_dwordx4 v240, s[86:87] offset:2048\n"
        "v_mfma_f32_16x16x32_bf16 a[164:167], v[36:39], v[8:11], a[164:167]\n"
        "s_waitcnt vmcnt(24) lgkmcnt(3)\n"
        "v_lshlrev_b32 v181, 16, v110\n"
        "v_mfma_f32_16x16x32_bf16 a[164:167], v[68:71], v[12:15], a[164:167]\n"
        "v_mfma_f32_16x16x32_bf16 a[132:135], v[68:71], v[4:7], a[132:135]\n"
        "v_mfma_f32_16x16x32_bf16 a[132:135], v[36:39], v[0:3], a[132:135]\n"
        "global_load_dwordx2 v[126:127], v169, s[74:75] offset:-4096\n"
        "v_mfma_f32_16x16x32_bf16 a[136:139], v[40:43], v[0:3], a[136:139]\n"
        "v_mfma_f32_16x16x32_bf16 a[136:139], v[72:75], v[4:7], a[136:139]\n"
        "global_load_lds_dwordx4 v241, s[86:87] offset:3072\n"
        "s_add_u32 m0, s72, 0x0\n"
        "v_mfma_f32_16x16x32_bf16 a[168:171], v[72:75], v[12:15], a[168:171]\n"
        "s_add_u32 s86, s56, 0x100\n"
        "s_addc_u32 s87, s57, 0\n"
        "v_mfma_f32_16x16x32_bf16 a[168:171], v[40:43], v[8:11], a[168:171]\n"
        "v_mfma_f32_16x16x32_bf16 a[172:175], v[44:47], v[8:11], a[172:175]\n"
        "v_mfma_f32_16x16x32_bf16 a[172:175], v[76:79], v[12:15], a[172:175]\n"
        "v_mfma_f32_16x16x32_bf16 a[140:143], v[76:79], v[4:7], a[140:143]\n"
        "v_mfma_f32_16x16x32_bf16 a[140:143], v[44:47], v[0:3], a[140:143]\n"
        "v_mfma_f32_16x16x32_bf16 a[144:147], v[0:3], v[48:51], a[144:147]\n"
        "v_mfma_f32_16x16x32_bf16 a[144:147], v[4:7], v[80:83], a[144:147]\n"
        "v_mfma_f32_16x16x32_bf16 a[176:179], v[12:15], v[80:83], a[176:179]\n"
        "v_mfma_f32_16x16x32_bf16 a[176:179], v[8:11], v[48:51], a[176:179]\n"
        "v_mfma_f32_16x16x32_bf16 a[180:183], v[8:11], v[52:55], a[180:183]\n"
        "s_waitcnt vmcnt(24) lgkmcnt(0)\n"
        "v_lshlrev_b32 v182, 16, v112\n"
        "v_mfma_f32_16x16x32_bf16 a[180:183], v[12:15], v[84:87], a[180:183]\n"
        "v_mfma_f32_16x16x32_bf16 a[148:151], v[4:7], v[84:87], a[148:151]\n"
        "v_mfma_f32_16x16x32_bf16 a[148:151], v[0:3], v[52:55], a[148:151]\n"
        "global_load_dwordx2 v[128:129], v169, s[74:75] offset:-2048\n"
        "v_mfma_f32_16x16x32_bf16 a[152:155], v[0:3], v[56:59], a[152:155]\n"
        "v_mfma_f32_16x16x32_bf16 a[152:155], v[4:7], v[88:91], a[152:155]\n"
        "v_mfma_f32_16x16x32_bf16 a[184:187], v[12:15], v[88:91], a[184:187]\n"
        "v_mfma_f32_16x16x32_bf16 a[184:187], v[8:11], v[56:59], a[184:187]\n"
        "s_waitcnt vmcnt(16)\n"
        "v_mfma_f32_16x16x32_bf16 a[188:191], v[8:11], v[60:63], a[188:191]\n"
        "s_barrier\n"
        "v_mfma_f32_16x16x32_bf16 a[188:191], v[12:15], v[92:95], a[188:191]\n"
        "global_load_lds_dwordx4 v234, s[86:87] offset:0\n"
        "ds_read_b128 v[8:11], v224 offset:51200\n"
        "v_mfma_f32_16x16x32_bf16 a[156:159], v[4:7], v[92:95], a[156:159]\n"
        "ds_read_b128 v[4:7], v225 offset:49152\n"
        "v_mfma_f32_16x16x32_bf16 a[156:159], v[0:3], v[60:63], a[156:159]\n"
        "ds_read_b128 v[0:3], v224 offset:49152\n"
        "v_mfma_f32_16x16x32_bf16 a[192:195], v[32:35], v[16:19], a[192:195]\n"
        "ds_read_b128 v[12:15], v225 offset:51200\n"
        "v_mfma_f32_16x16x32_bf16 a[192:195], v[64:67], v[20:23], a[192:195]\n"
        "global_load_lds_dwordx4 v235, s[86:87] offset:1024\n"
        "v_mfma_f32_16x16x32_bf16 a[224:227], v[64:67], v[28:31], a[224:227]\n"
        "ds_read_b64_tr_b16 v[64:65], v226 offset:8192\n"
        "v_mfma_f32_16x16x32_bf16 a[224:227], v[32:35], v[24:27], a[224:227]\n"
        "ds_read_b64_tr_b16 v[32:33], v226 offset:0\n"
        "v_mfma_f32_16x16x32_bf16 a[228:231], v[36:39], v[24:27], a[228:231]\n"
        "ds_read_b64_tr_b16 v[34:35], v227 offset:0\n"
        "v_mfma_f32_16x16x32_bf16 a[228:231], v[68:71], v[28:31], a[228:231]\n"
        "ds_read_b64_tr_b16 v[66:67], v227 offset:8192\n"
        "global_load_lds_dwordx4 v236, s[86:87] offset:2048\n"
        "v_mfma_f32_16x16x32_bf16 a[196:199], v[68:71], v[20:23], a[196:199]\n"
        "ds_read_b64_tr_b16 v[68:69], v228 offset:8192\n"
        "v_mfma_f32_16x16x32_bf16 a[196:199], v[36:39], v[16:19], a[196:199]\n"
        "ds_read_b64_tr_b16 v[36:37], v228 offset:0\n"
        "v_mfma_f32_16x16x32_bf16 a[200:203], v[40:43], v[16:19], a[200:203]\n"
        "ds_read_b64_tr_b16 v[38:39], v229 offset:0\n"
        "v_mfma_f32_16x16x32_bf16 a[200:203], v[72:75], v[20:23], a[200:203]\n"
        "ds_read_b64_tr_b16 v[70:71], v229 offset:8192\n"
        "global_load_lds_dwordx4 v237, s[86:87] offset:3072\n"
        "s_add_u32 m0, s72, 0x1000\n"
        "v_mfma_f32_16x16x32_bf16 a[232:235], v[72:75], v[28:31], a[232:235]\n"
        "ds_read_b64_tr_b16 v[72:73], v230 offset:8192\n"
        "s_add_u32 s86, s58, 0x100\n"
        "s_addc_u32 s87, s59, 0\n"
        "v_mfma_f32_16x16x32_bf16 a[232:235], v[40:43], v[24:27], a[232:235]\n"
        "ds_read_b64_tr_b16 v[40:41], v230 offset:0\n"
        "v_lshlrev_b32 v183, 16, v114\n"
        "v_mfma_f32_16x16x32_bf16 a[236:239], v[44:47], v[24:27], a[236:239]\n"
        "ds_read_b64_tr_b16 v[42:43], v231 offset:0\n"
        "v_mfma_f32_16x16x32_bf16 a[236:239], v[76:79], v[28:31], a[236:239]\n"
        "ds_read_b64_tr_b16 v[74:75], v231 offset:8192\n"
        "global_load_dwordx2 v[130:131], v169, s[74:75] offset:0\n"
        "v_mfma_f32_16x16x32_bf16 a[204:207], v[76:79], v[20:23], a[204:207]\n"
        "ds_read_b64_tr_b16 v[76:77], v232 offset:8192\n"
        "global_load_lds_dwordx4 v234, s[86:87] offset:0\n"
        "v_mfma_f32_16x16x32_bf16 a[204:207], v[44:47], v[16:19], a[204:207]\n"
        "ds_read_b64_tr_b16 v[44:45], v232 offset:0\n"
        "v_mfma_f32_16x16x32_bf16 a[208:211], v[16:19], v[48:51], a[208:211]\n"
        "ds_read_b64_tr_b16 v[46:47], v233 offset:0\n"
        "v_mfma_f32_16x16x32_bf16 a[208:211], v[20:23], v[80:83], a[208:211]\n"
        "ds_read_b64_tr_b16 v[78:79], v233 offset:8192\n"
        "v_mfma_f32_16x16x32_bf16 a[240:243], v[28:31], v[80:83], a[240:243]\n"
        "ds_read_b64_tr_b16 v[80:81], v226 offset:24576\n"
        "global_load_lds_dwordx4 v235, s[86:87] offset:1024\n"
        "v_mfma_f32_16x16x32_bf16 a[240:243], v[24:27], v[48:51], a[240:243]\n"
        "ds_read_b64_tr_b16 v[48:49], v226 offset:16384\n"
        "v_mfma_f32_16x16x32_bf16 a[244:247], v[24:27], v[52:55], a[244:247]\n"
        "ds_read_b64_tr_b16 v[50:51], v227 offset:16384\n"
        "v_mfma_f32_16x16x32_bf16 a[244:247], v[28:31], v[84:87], a[244:247]\n"
        "ds_read_b64_tr_b16 v[82:83], v227 offset:24576\n"
        "v_mfma_f32_16x16x32_bf16 a[212:215], v[20:23], v[84:87], a[212:215]\n"
        "ds_read_b64_tr_b16 v[84:85], v228 offset:24576\n"
        "global_load_lds_dwordx4 v236, s[86:87] offset:2048\n"
        "v_mfma_f32_16x16x32_bf16 a[212:215], v[16:19], v[52:55], a[212:215]\n"
        "ds_read_b64_tr_b16 v[52:53], v228 offset:16384\n"
        "v_mfma_f32_16x16x32_bf16 a[216:219], v[16:19], v[56:59], a[216:219]\n"
        "ds_read_b64_tr_b16 v[54:55], v229 offset:16384\n"
        "v_mfma_f32_16x16x32_bf16 a[216:219], v[20:23], v[88:91], a[216:219]\n"
        "ds_read_b64_tr_b16 v[86:87], v229 offset:24576\n"
        "v_mfma_f32_16x16x32_bf16 a[248:251], v[28:31], v[88:91], a[248:251]\n"
        "ds_read_b64_tr_b16 v[88:89], v230 offset:24576\n"
        "global_load_lds_dwordx4 v237, s[86:87] offset:3072\n"
        "s_add_u32 m0, s73, 0x20000\n"
        "v_mfma_f32_16x16x32_bf16 a[248:251], v[24:27], v[56:59], a[248:251]\n"
        "ds_read_b64_tr_b16 v[56:57], v230 offset:16384\n"
        "s_add_u32 s86, s60, 0x280000\n"
        "s_addc_u32 s87, s61, 0\n"
        "v_mfma_f32_16x16x32_bf16 a[252:255], v[24:27], v[60:63], a[252:255]\n"
        "ds_read_b64_tr_b16 v[58:59], v231 offset:16384\n"
        "s_waitcnt lgkmcnt(8)\n"
        "v_mfma_f32_16x16x32_bf16 a[252:255], v[28:31], v[92:95], a[252:255]\n"
        "ds_read_b64_tr_b16 v[90:91], v231 offset:24576\n"
        "s_waitcnt vmcnt(24) lgkmcnt(8)\n"
        "v_mfma_f32_16x16x32_bf16 a[220:223], v[20:23], v[92:95], a[220:223]\n"
        "ds_read_b64_tr_b16 v[92:93], v232 offset:24576\n"
        "global_load_dwordx2 v[132:133], v169, s[74:75] offset:2048\n"
        "v_mfma_f32_16x16x32_bf16 a[220:223], v[16:19], v[60:63], a[220:223]\n"
        "ds_read_b64_tr_b16 v[60:61], v232 offset:16384\n"
        "v_lshlrev_b32 v184, 16, v116\n"
        "v_mfma_f32_16x16x32_bf16 a[0:3], v[32:35], v[0:3], a[0:3]\n"
        "ds_read_b64_tr_b16 v[62:63], v233 offset:16384\n"
        "v_mfma_f32_16x16x32_bf16 a[0:3], v[64:67], v[4:7], a[0:3]\n"
        "ds_read_b64_tr_b16 v[94:95], v233 offset:24576\n"
        "v_mfma_f32_16x16x32_bf16 a[32:35], v[64:67], v[12:15], a[32:35]\n"
        "ds_read_b128 v[16:19], v224 offset:53248\n"
        "v_mfma_f32_16x16x32_bf16 a[32:35], v[32:35], v[8:11], a[32:35]\n"
        "ds_read_b128 v[20:23], v225 offset:53248\n"
        "v_mfma_f32_16x16x32_bf16 a[36:39], v[36:39], v[8:11], a[36:39]\n"
        "ds_read_b128 v[24:27], v224 offset:55296\n"
        "s_waitcnt vmcnt(24) lgkmcnt(8)\n"
        "v_mfma_f32_16x16x32_bf16 a[36:39], v[68:71], v[12:15], a[36:39]\n"
        "ds_read_b128 v[28:31], v225 offset:55296\n"
        "v_lshlrev_b32 v190, 16, v118\n"
        "v_mfma_f32_16x16x32_bf16 a[4:7], v[68:71], v[4:7], a[4:7]\n"
        "v_mul_f32 v189, v189, v190\n"
        "v_mul_f32 v190, v179, v187\n"
        "v_mfma_f32_16x16x32_bf16 a[4:7], v[36:39], v[0:3], a[4:7]\n"
        "v_cvt_pk_bf16_f32 v190, 0, v190\n"
        "v_mfma_f32_16x16x32_bf16 a[8:11], v[40:43], v[0:3], a[8:11]\n"
        "v_mfma_f32_16x16x32_bf16 a[8:11], v[72:75], v[4:7], a[8:11]\n"
        "v_mfma_f32_16x16x32_bf16 a[40:43], v[72:75], v[12:15], a[40:43]\n"
        "v_mfma_f32_16x16x32_bf16 a[40:43], v[40:43], v[8:11], a[40:43]\n"
        "v_mfma_f32_16x16x32_bf16 a[44:47], v[44:47], v[8:11], a[44:47]\n"
        "v_mfma_f32_16x16x32_bf16 a[44:47], v[76:79], v[12:15], a[44:47]\n"
        "v_mfma_f32_16x16x32_bf16 a[12:15], v[76:79], v[4:7], a[12:15]\n"
        "v_mfma_f32_16x16x32_bf16 a[12:15], v[44:47], v[0:3], a[12:15]\n"
        "global_load_dword v253, v173, s[82:83] offset:0\n"
        "s_add_u32 s82, s12, 0x400000\n"
        "s_addc_u32 s83, s13, 0\n"
        "v_mfma_f32_16x16x32_bf16 a[16:19], v[0:3], v[48:51], a[16:19]\n"
        "v_mfma_f32_16x16x32_bf16 a[16:19], v[4:7], v[80:83], a[16:19]\n"
        "v_mfma_f32_16x16x32_bf16 a[48:51], v[12:15], v[80:83], a[48:51]\n"
        "v_mfma_f32_16x16x32_bf16 a[48:51], v[8:11], v[48:51], a[48:51]\n"
        "v_mfma_f32_16x16x32_bf16 a[52:55], v[8:11], v[52:55], a[52:55]\n"
        "s_waitcnt vmcnt(23) lgkmcnt(0)\n"
        "v_lshlrev_b32 v192, 16, v120\n"
        "v_mul_f32 v191, v191, v192\n"
        "v_mfma_f32_16x16x32_bf16 a[52:55], v[12:15], v[84:87], a[52:55]\n"
        "v_cvt_pk_bf16_f32 v203, v191, v189\n"
        "v_mul_f32 v189, v178, v188\n"
        "v_mfma_f32_16x16x32_bf16 a[20:23], v[4:7], v[84:87], a[20:23]\n"
        "v_mul_f32 v191, v179, v188\n"
        "v_cvt_pk_bf16_f32 v189, 0, v189\n"
        "v_mfma_f32_16x16x32_bf16 a[20:23], v[0:3], v[52:55], a[20:23]\n"
        "v_cvt_pk_bf16_f32 v191, 0, v191\n"
        "v_mul_f32 v192, v180, v187\n"
        "v_mfma_f32_16x16x32_bf16 a[24:27], v[0:3], v[56:59], a[24:27]\n"
        "v_cvt_pk_bf16_f32 v192, 0, v192\n"
        "v_add_f32 v189, v189, v190\n"
        "v_mfma_f32_16x16x32_bf16 a[24:27], v[4:7], v[88:91], a[24:27]\n"
        "v_add_f32 v191, v191, v192\n"
        "v_cvt_pk_bf16_f32 v189, 0, v189\n"
        "v_mfma_f32_16x16x32_bf16 a[56:59], v[12:15], v[88:91], a[56:59]\n"
        "v_cvt_pk_bf16_f32 v191, 0, v191\n"
        "v_mul_f32 v190, v180, v186\n"
        "v_mfma_f32_16x16x32_bf16 a[56:59], v[8:11], v[56:59], a[56:59]\n"
        "v_mul_f32 v192, v181, v186\n"
        "v_cvt_pk_bf16_f32 v190, 0, v190\n"
        "v_mfma_f32_16x16x32_bf16 a[60:63], v[8:11], v[60:63], a[60:63]\n"
        "ds_read_b128 v[8:11], v224 offset:34816\n"
        "v_cvt_pk_bf16_f32 v192, 0, v192\n"
        "v_mfma_f32_16x16x32_bf16 a[60:63], v[12:15], v[92:95], a[60:63]\n"
        "ds_read_b128 v[12:15], v225 offset:34816\n"
        "v_add_f32 v189, v189, v190\n"
        "v_mfma_f32_16x16x32_bf16 a[28:31], v[4:7], v[92:95], a[28:31]\n"
        "ds_read_b128 v[4:7], v225 offset:32768\n"
        "v_add_f32 v191, v191, v192\n"
        "v_mfma_f32_16x16x32_bf16 a[28:31], v[0:3], v[60:63], a[28:31]\n"
        "global_load_lds_dwordx4 v238, s[86:87] offset:0\n"
        "ds_read_b128 v[0:3], v224 offset:32768\n"
        "v_mfma_f32_16x16x32_bf16 a[64:67], v[32:35], v[16:19], a[64:67]\n"
        "v_cvt_pk_bf16_f32 v189, 0, v189\n"
        "v_cvt_pk_bf16_f32 v191, 0, v191\n"
        "v_mfma_f32_16x16x32_bf16 a[64:67], v[64:67], v[20:23], a[64:67]\n"
        "v_mul_f32 v190, v181, v185\n"
        "v_mul_f32 v192, v182, v185\n"
        "v_mfma_f32_16x16x32_bf16 a[96:99], v[64:67], v[28:31], a[96:99]\n"
        "v_cvt_pk_bf16_f32 v190, 0, v190\n"
        "v_cvt_pk_bf16_f32 v192, 0, v192\n"
        "v_mfma_f32_16x16x32_bf16 a[96:99], v[32:35], v[24:27], a[96:99]\n"
        "v_add_f32 v189, v189, v190\n"
        "v_add_f32 v191, v191, v192\n"
        "v_mfma_f32_16x16x32_bf16 a[100:103], v[36:39], v[24:27], a[100:103]\n"
        "v_cvt_pk_bf16_f32 v189, 0, v189\n"
        "v_cvt_pk_bf16_f32 v191, 0, v191\n"
        "v_mfma_f32_16x16x32_bf16 a[100:103], v[68:71], v[28:31], a[100:103]\n"
        "global_load_lds_dwordx4 v239, s[86:87] offset:1024\n"
        "v_and_b32 v178, s35, v104\n"
        "v_mfma_f32_16x16x32_bf16 a[68:71], v[68:71], v[20:23], a[68:71]\n"
        "s_waitcnt vmcnt(22) lgkmcnt(1)\n"
        "v_lshlrev_b32 v194, 16, v122\n"
        "v_mul_f32 v193, v193, v194\n"
        "v_mfma_f32_16x16x32_bf16 a[68:71], v[36:39], v[16:19], a[68:71]\n"
        "LHA1_%=:\n"
        "s_cbranch_execz LHA5_%=\n"
        "v_mul_f32 v194, v181, v187\n"
        "v_cvt_pk_bf16_f32 v194, 0, v194\n"
        "v_mfma_f32_16x16x32_bf16 a[72:75], v[40:43], v[16:19], a[72:75]\n"
        "v_and_b32 v179, s35, v106\n"
        "v_mfma_f32_16x16x32_bf16 a[72:75], v[72:75], v[20:23], a[72:75]\n"
        "v_mfma_f32_16x16x32_bf16 a[104:107], v[72:75], v[28:31], a[104:107]\n"
        "v_mfma_f32_16x16x32_bf16 a[104:107], v[40:43], v[24:27], a[104:107]\n"
        "global_load_lds_dwordx4 v240, s[86:87] offset:2048\n"
        "v_mfma_f32_16x16x32_bf16 a[108:111], v[44:47], v[24:27], a[108:111]\n"
        "v_mfma_f32_16x16x32_bf16 a[108:111], v[76:79], v[28:31], a[108:111]\n"
        "v_mfma_f32_16x16x32_bf16 a[76:79], v[76:79], v[20:23], a[76:79]\n"
        "v_mfma_f32_16x16x32_bf16 a[76:79], v[44:47], v[16:19], a[76:79]\n"
        "v_mfma_f32_16x16x32_bf16 a[80:83], v[16:19], v[48:51], a[80:83]\n"
        "v_mfma_f32_16x16x32_bf16 a[80:83], v[20:23], v[80:83], a[80:83]\n"
        "global_load_lds_dwordx4 v241, s[86:87] offset:3072\n"
        "s_add_u32 m0, s73, 0x24000\n"
        "v_mfma_f32_16x16x32_bf16 a[112:115], v[28:31], v[80:83], a[112:115]\n"
        "s_add_u32 s86, s62, 0x280000\n"
        "s_addc_u32 s87, s63, 0\n"
        "v_mfma_f32_16x16x32_bf16 a[112:115], v[24:27], v[48:51], a[112:115]\n"
        "v_mfma_f32_16x16x32_bf16 a[116:119], v[24:27], v[52:55], a[116:119]\n"
        "v_mfma_f32_16x16x32_bf16 a[116:119], v[28:31], v[84:87], a[116:119]\n"
        "v_mfma_f32_16x16x32_bf16 a[84:87], v[20:23], v[84:87], a[84:87]\n"
        "v_mfma_f32_16x16x32_bf16 a[84:87], v[16:19], v[52:55], a[84:87]\n"
        "global_load_lds_dwordx4 v238, s[86:87] offset:0\n"
        "v_mfma_f32_16x16x32_bf16 a[88:91], v[16:19], v[56:59], a[88:91]\n"
        "s_waitcnt vmcnt(21) lgkmcnt(0)\n"
        "v_lshlrev_b32 v196, 16, v124\n"
        "v_mul_f32 v195, v195, v196\n"
        "v_mfma_f32_16x16x32_bf16 a[88:91], v[20:23], v[88:91], a[88:91]\n"
        "v_cvt_pk_bf16_f32 v202, v195, v193\n"
        "v_mul_f32 v193, v180, v188\n"
        "v_mfma_f32_16x16x32_bf16 a[120:123], v[28:31], v[88:91], a[120:123]\n"
        "v_mul_f32 v195, v181, v188\n"
        "v_cvt_pk_bf16_f32 v193, 0, v193\n"
        "v_mfma_f32_16x16x32_bf16 a[120:123], v[24:27], v[56:59], a[120:123]\n"
        "v_cvt_pk_bf16_f32 v195, 0, v195\n"
        "v_mul_f32 v196, v182, v187\n"
        "v_mfma_f32_16x16x32_bf16 a[124:127], v[24:27], v[60:63], a[124:127]\n"
        "ds_read_b128 v[24:27], v224 offset:38912\n"
        "v_cvt_pk_bf16_f32 v196, 0, v196\n"
        "v_mfma_f32_16x16x32_bf16 a[124:127], v[28:31], v[92:95], a[124:127]\n"
        "global_load_lds_dwordx4 v239, s[86:87] offset:1024\n"
        "ds_read_b128 v[28:31], v225 offset:38912\n"
        "v_mfma_f32_16x16x32_bf16 a[92:95], v[20:23], v[92:95], a[92:95]\n"
        "ds_read_b128 v[20:23], v225 offset:36864\n"
        "v_add_f32 v193, v193, v194\n"
        "v_mfma_f32_16x16x32_bf16 a[92:95], v[16:19], v[60:63], a[92:95]\n"
        "ds_read_b128 v[16:19], v224 offset:36864\n"
        "v_add_f32 v195, v195, v196\n"
        "v_mfma_f32_16x16x32_bf16 a[128:131], v[32:35], v[0:3], a[128:131]\n"
        "v_cvt_pk_bf16_f32 v193, 0, v193\n"
        "v_cvt_pk_bf16_f32 v195, 0, v195\n"
        "v_mfma_f32_16x16x32_bf16 a[128:131], v[64:67], v[4:7], a[128:131]\n"
        "global_load_dwordx4 v[142:145], v254, s[92:93] offset:0\n"
        "v_mul_f32 v194, v182, v186\n"
        "v_mfma_f32_16x16x32_bf16 a[160:163], v[64:67], v[12:15], a[160:163]\n"
        "v_mul_f32 v196, v183, v186\n"
        "v_cvt_pk_bf16_f32 v194, 0, v194\n"
        "v_mfma_f32_16x16x32_bf16 a[160:163], v[32:35], v[8:11], a[160:163]\n"
        "v_cvt_pk_bf16_f32 v196, 0, v196\n"
        "v_add_f32 v193, v193, v194\n"
        "v_mfma_f32_16x16x32_bf16 a[164:167], v[36:39], v[8:11], a[164:167]\n"
        "s_waitcnt vmcnt(20) lgkmcnt(3)\n"
        "v_lshlrev_b32 v190, 16, v126\n"
        "v_mul_f32 v189, v189, v190\n"
        "v_mfma_f32_16x16x32_bf16 a[164:167], v[68:71], v[12:15], a[164:167]\n"
        "v_add_f32 v195, v195, v196\n"
        "v_cvt_pk_bf16_f32 v193, 0, v193\n"
        "v_mfma_f32_16x16x32_bf16 a[132:135], v[68:71], v[4:7], a[132:135]\n"
        "v_cvt_pk_bf16_f32 v195, 0, v195\n"
        "v_mul_f32 v194, v183, v185\n"
        "v_mfma_f32_16x16x32_bf16 a[132:135], v[36:39], v[0:3], a[132:135]\n"
        "global_load_lds_dwordx4 v240, s[86:87] offset:2048\n"
        "v_mul_f32 v196, v184, v185\n"
        "v_mfma_f32_16x16x32_bf16 a[136:139], v[40:43], v[0:3], a[136:139]\n"
        "v_cvt_pk_bf16_f32 v194, 0, v194\n"
        "v_cvt_pk_bf16_f32 v196, 0, v196\n"
        "v_mfma_f32_16x16x32_bf16 a[136:139], v[72:75], v[4:7], a[136:139]\n"
        "v_add_f32 v193, v193, v194\n"
        "v_add_f32 v195, v195, v196\n"
        "v_mfma_f32_16x16x32_bf16 a[168:171], v[72:75], v[12:15], a[168:171]\n"
        "v_cvt_pk_bf16_f32 v193, 0, v193\n"
        "v_cvt_pk_bf16_f32 v195, 0, v195\n"
        "v_mfma_f32_16x16x32_bf16 a[168:171], v[40:43], v[8:11], a[168:171]\n"
        "v_lshlrev_b32 v185, 16, v136\n"
        "v_and_b32 v186, s35, v136\n"
        "v_mfma_f32_16x16x32_bf16 a[172:175], v[44:47], v[8:11], a[172:175]\n"
        "v_lshlrev_b32 v187, 16, v137\n"
        "v_and_b32 v188, s35, v137\n"
        "v_mfma_f32_16x16x32_bf16 a[172:175], v[76:79], v[12:15], a[172:175]\n"
        "global_load_lds_dwordx4 v241, s[86:87] offset:3072\n"
        "s_add_u32 m0, s72, 0x8000\n"
        "v_and_b32 v180, s35, v108\n"
        "v_mfma_f32_16x16x32_bf16 a[140:143], v[76:79], v[4:7], a[140:143]\n"
        "s_add_u32 s86, s56, 0x280\n"
        "s_addc_u32 s87, s57, 0\n"
        "v_and_b32 v181, s35, v110\n"
        "v_mfma_f32_16x16x32_bf16 a[140:143], v[44:47], v[0:3], a[140:143]\n"
        "v_and_b32 v182, s35, v112\n"
        "v_and_b32 v183, s35, v114\n"
        "v_mfma_f32_16x16x32_bf16 a[144:147], v[0:3], v[48:51], a[144:147]\n"
        "v_and_b32 v184, s35, v116\n"
        "v_mul_f32 v190, v175, v187\n"
        "v_mfma_f32_16x16x32_bf16 a[144:147], v[4:7], v[80:83], a[144:147]\n"
        "v_cvt_pk_bf16_f32 v190, 0, v190\n"
        "v_mfma_f32_16x16x32_bf16 a[176:179], v[12:15], v[80:83], a[176:179]\n"
        "v_mfma_f32_16x16x32_bf16 a[176:179], v[8:11], v[48:51], a[176:179]\n"
        "v_mfma_f32_16x16x32_bf16 a[180:183], v[8:11], v[52:55], a[180:183]\n"
        "s_waitcnt vmcnt(20) lgkmcnt(0)\n"
        "v_lshlrev_b32 v192, 16, v128\n"
        "v_mul_f32 v191, v191, v192\n"
        "v_mfma_f32_16x16x32_bf16 a[180:183], v[12:15], v[84:87], a[180:183]\n"
        "v_cvt_pk_bf16_f32 v201, v191, v189\n"
        "v_mul_f32 v189, v174, v188\n"
        "v_mfma_f32_16x16x32_bf16 a[148:151], v[4:7], v[84:87], a[148:151]\n"
        "v_mul_f32 v191, v175, v188\n"
        "v_cvt_pk_bf16_f32 v189, 0, v189\n"
        "v_mfma_f32_16x16x32_bf16 a[148:151], v[0:3], v[52:55], a[148:151]\n"
        "v_cvt_pk_bf16_f32 v191, 0, v191\n"
        "v_mul_f32 v192, v176, v187\n"
        "v_mfma_f32_16x16x32_bf16 a[152:155], v[0:3], v[56:59], a[152:155]\n"
        "v_cvt_pk_bf16_f32 v192, 0, v192\n"
        "v_add_f32 v189, v189, v190\n"
        "v_mfma_f32_16x16x32_bf16 a[152:155], v[4:7], v[88:91], a[152:155]\n"
        "v_add_f32 v191, v191, v192\n"
        "v_cvt_pk_bf16_f32 v189, 0, v189\n"
        "v_mfma_f32_16x16x32_bf16 a[184:187], v[12:15], v[88:91], a[184:187]\n"
        "v_cvt_pk_bf16_f32 v191, 0, v191\n"
        "v_mul_f32 v190, v176, v186\n"
        "v_mfma_f32_16x16x32_bf16 a[184:187], v[8:11], v[56:59], a[184:187]\n"
        "s_waitcnt vmcnt(11)\n"
        "v_mul_f32 v192, v177, v186\n"
        "v_cvt_pk_bf16_f32 v190, 0, v190\n"
        "v_mfma_f32_16x16x32_bf16 a[188:191], v[8:11], v[60:63], a[188:191]\n"
        "s_barrier\n"
        "v_mfma_f32_16x16x32_bf16 a[188:191], v[12:15], v[92:95], a[188:191]\n"
        "global_load_lds_dwordx4 v234, s[86:87] offset:0\n"
        "ds_read_b128 v[8:11], v224 offset:18432\n"
        "v_mfma_f32_16x16x32_bf16 a[156:159], v[4:7], v[92:95], a[156:159]\n"
        "ds_read_b128 v[4:7], v225 offset:16384\n"
        "v_cvt_pk_bf16_f32 v192, 0, v192\n"
        "v_mfma_f32_16x16x32_bf16 a[156:159], v[0:3], v[60:63], a[156:159]\n"
        "ds_read_b128 v[0:3], v224 offset:16384\n"
        "v_add_f32 v189, v189, v190\n"
        "v_mfma_f32_16x16x32_bf16 a[192:195], v[32:35], v[16:19], a[192:195]\n"
        "ds_read_b128 v[12:15], v225 offset:18432\n"
        "v_add_f32 v191, v191, v192\n"
        "v_mfma_f32_16x16x32_bf16 a[192:195], v[64:67], v[20:23], a[192:195]\n"
        "global_load_lds_dwordx4 v235, s[86:87] offset:1024\n"
        "v_cvt_pk_bf16_f32 v189, 0, v189\n"
        "v_mfma_f32_16x16x32_bf16 a[224:227], v[64:67], v[28:31], a[224:227]\n"
        "ds_read_b64_tr_b16 v[64:65], v226 offset:40960\n"
        "v_cvt_pk_bf16_f32 v191, 0, v191\n"
        "v_mfma_f32_16x16x32_bf16 a[224:227], v[32:35], v[24:27], a[224:227]\n"
        "ds_read_b64_tr_b16 v[32:33], v226 offset:32768\n"
        "v_mul_f32 v190, v177, v185\n"
        "v_mfma_f32_16x16x32_bf16 a[228:231], v[36:39], v[24:27], a[228:231]\n"
        "ds_read_b64_tr_b16 v[34:35], v227 offset:32768\n"
        "v_mul_f32 v192, v178, v185\n"
        "v_mfma_f32_16x16x32_bf16 a[228:231], v[68:71], v[28:31], a[228:231]\n"
        "ds_read_b64_tr_b16 v[66:67], v227 offset:40960\n"
        "global_load_lds_dwordx4 v236, s[86:87] offset:2048\n"
        "v_mfma_f32_16x16x32_bf16 a[196:199], v[68:71], v[20:23], a[196:199]\n"
        "ds_read_b64_tr_b16 v[68:69], v228 offset:40960\n"
        "v_cvt_pk_bf16_f32 v190, 0, v190\n"
        "v_mfma_f32_16x16x32_bf16 a[196:199], v[36:39], v[16:19], a[196:199]\n"
        "ds_read_b64_tr_b16 v[36:37], v228 offset:32768\n"
        "v_cvt_pk_bf16_f32 v192, 0, v192\n"
        "v_mfma_f32_16x16x32_bf16 a[200:203], v[40:43], v[16:19], a[200:203]\n"
        "ds_read_b64_tr_b16 v[38:39], v229 offset:32768\n"
        "v_add_f32 v189, v189, v190\n"
        "v_mfma_f32_16x16x32_bf16 a[200:203], v[72:75], v[20:23], a[200:203]\n"
        "ds_read_b64_tr_b16 v[70:71], v229 offset:40960\n"
        "global_load_lds_dwordx4 v237, s[86:87] offset:3072\n"
        "s_add_u32 m0, s72, 0x9000\n"
        "v_mfma_f32_16x16x32_bf16 a[232:235], v[72:75], v[28:31], a[232:235]\n"
        "ds_read_b64_tr_b16 v[72:73], v230 offset:40960\n"
        "s_add_u32 s86, s58, 0x280\n"
        "s_addc_u32 s87, s59, 0\n"
        "v_mfma_f32_16x16x32_bf16 a[232:235], v[40:43], v[24:27], a[232:235]\n"
        "ds_read_b64_tr_b16 v[40:41], v230 offset:32768\n"
        "v_lshlrev_b32 v194, 16, v130\n"
        "v_mfma_f32_16x16x32_bf16 a[236:239], v[44:47], v[24:27], a[236:239]\n"
        "ds_read_b64_tr_b16 v[42:43], v231 offset:32768\n"
        "v_mul_f32 v193, v193, v194\n"
        "v_mfma_f32_16x16x32_bf16 a[236:239], v[76:79], v[28:31], a[236:239]\n"
        "ds_read_b64_tr_b16 v[74:75], v231 offset:40960\n"
        "global_load_lds_dwordx4 v234, s[86:87] offset:0\n"
        "v_mfma_f32_16x16x32_bf16 a[204:207], v[76:79], v[20:23], a[204:207]\n"
        "ds_read_b64_tr_b16 v[76:77], v232 offset:40960\n"
        "v_add_f32 v191, v191, v192\n"
        "v_mfma_f32_16x16x32_bf16 a[204:207], v[44:47], v[16:19], a[204:207]\n"
        "ds_read_b64_tr_b16 v[44:45], v232 offset:32768\n"
        "v_cvt_pk_bf16_f32 v189, 0, v189\n"
        "v_mfma_f32_16x16x32_bf16 a[208:211], v[16:19], v[48:51], a[208:211]\n"
        "ds_read_b64_tr_b16 v[46:47], v233 offset:32768\n"
        "v_cvt_pk_bf16_f32 v191, 0, v191\n"
        "v_mfma_f32_16x16x32_bf16 a[208:211], v[20:23], v[80:83], a[208:211]\n"
        "ds_read_b64_tr_b16 v[78:79], v233 offset:40960\n"
        "global_load_lds_dwordx4 v235, s[86:87] offset:1024\n"
        "v_mfma_f32_16x16x32_bf16 a[240:243], v[28:31], v[80:83], a[240:243]\n"
        "ds_read_b64_tr_b16 v[80:81], v226 offset:57344\n"
        "v_and_b32 v190, s35, v118\n"
        "v_mfma_f32_16x16x32_bf16 a[240:243], v[24:27], v[48:51], a[240:243]\n"
        "ds_read_b64_tr_b16 v[48:49], v226 offset:49152\n"
        "v_and_b32 v192, s35, v120\n"
        "v_mfma_f32_16x16x32_bf16 a[244:247], v[24:27], v[52:55], a[244:247]\n"
        "ds_read_b64_tr_b16 v[50:51], v227 offset:49152\n"
        "v_mul_f32 v189, v189, v190\n"
        "v_mfma_f32_16x16x32_bf16 a[244:247], v[28:31], v[84:87], a[244:247]\n"
        "ds_read_b64_tr_b16 v[82:83], v227 offset:57344\n"
        "global_load_lds_dwordx4 v236, s[86:87] offset:2048\n"
        "v_mfma_f32_16x16x32_bf16 a[212:215], v[20:23], v[84:87], a[212:215]\n"
        "ds_read_b64_tr_b16 v[84:85], v228 offset:57344\n"
        "v_mul_f32 v191, v191, v192\n"
        "v_mfma_f32_16x16x32_bf16 a[212:215], v[16:19], v[52:55], a[212:215]\n"
        "ds_read_b64_tr_b16 v[52:53], v228 offset:49152\n"
        "v_cvt_pk_bf16_f32 v211, v191, v189\n"
        "v_mfma_f32_16x16x32_bf16 a[216:219], v[16:19], v[56:59], a[216:219]\n"
        "ds_read_b64_tr_b16 v[54:55], v229 offset:49152\n"
        "v_mul_f32 v194, v177, v187\n"
        "v_mfma_f32_16x16x32_bf16 a[216:219], v[20:23], v[88:91], a[216:219]\n"
        "ds_read_b64_tr_b16 v[86:87], v229 offset:57344\n"
        "global_load_lds_dwordx4 v237, s[86:87] offset:3072\n"
        "s_add_u32 m0, s73, 0x10000\n"
        "v_mfma_f32_16x16x32_bf16 a[248:251], v[28:31], v[88:91], a[248:251]\n"
        "ds_read_b64_tr_b16 v[88:89], v230 offset:57344\n"
        "s_add_u32 s86, s60, 0x380000\n"
        "s_addc_u32 s87, s61, 0\n"
        "v_mfma_f32_16x16x32_bf16 a[248:251], v[24:27], v[56:59], a[248:251]\n"
        "ds_read_b64_tr_b16 v[56:57], v230 offset:49152\n"
        "v_cvt_pk_bf16_f32 v194, 0, v194\n"
        "v_mfma_f32_16x16x32_bf16 a[252:255], v[24:27], v[60:63], a[252:255]\n"
        "ds_read_b64_tr_b16 v[58:59], v231 offset:49152\n"
        "v_mul_f32 v189, v178, v188\n"
        "v_mfma_f32_16x16x32_bf16 a[252:255], v[28:31], v[92:95], a[252:255]\n"
        "ds_read_b64_tr_b16 v[90:91], v231 offset:57344\n"
        "v_mul_f32 v191, v179, v188\n"
        "v_mfma_f32_16x16x32_bf16 a[220:223], v[20:23], v[92:95], a[220:223]\n"
        "ds_read_b64_tr_b16 v[92:93], v232 offset:57344\n"
        "v_cvt_pk_bf16_f32 v189, 0, v189\n"
        "v_mfma_f32_16x16x32_bf16 a[220:223], v[16:19], v[60:63], a[220:223]\n"
        "ds_read_b64_tr_b16 v[60:61], v232 offset:49152\n"
        "s_waitcnt lgkmcnt(8)\n"
        "v_mfma_f32_16x16x32_bf16 a[0:3], v[32:35], v[0:3], a[0:3]\n"
        "ds_read_b64_tr_b16 v[62:63], v233 offset:49152\n"
        "global_load_dwordx4 v[146:149], v254, s[92:93] offset:1024\n"
        "v_mfma_f32_16x16x32_bf16 a[0:3], v[64:67], v[4:7], a[0:3]\n"
        "ds_read_b64_tr_b16 v[94:95], v233 offset:57344\n"
        "v_cvt_pk_bf16_f32 v191, 0, v191\n"
        "v_mfma_f32_16x16x32_bf16 a[32:35], v[64:67], v[12:15], a[32:35]\n"
        "ds_read_b128 v[16:19], v224 offset:20480\n"
        "v_mul_f32 v190, v179, v187\n"
        "v_mfma_f32_16x16x32_bf16 a[32:35], v[32:35], v[8:11], a[32:35]\n"
        "ds_read_b128 v[20:23], v225 offset:20480\n"
        "v_mul_f32 v192, v180, v187\n"
        "v_mfma_f32_16x16x32_bf16 a[36:39], v[36:39], v[8:11], a[36:39]\n"
        "ds_read_b128 v[24:27], v224 offset:22528\n"
        "v_cvt_pk_bf16_f32 v190, 0, v190\n"
        "v_mfma_f32_16x16x32_bf16 a[36:39], v[68:71], v[12:15], a[36:39]\n"
        "ds_read_b128 v[28:31], v225 offset:22528\n"
        "v_cvt_pk_bf16_f32 v192, 0, v192\n"
        "v_mfma_f32_16x16x32_bf16 a[4:7], v[68:71], v[4:7], a[4:7]\n"
        "s_waitcnt vmcnt(19) lgkmcnt(7)\n"
        "v_lshlrev_b32 v196, 16, v132\n"
        "v_mul_f32 v195, v195, v196\n"
        "v_mfma_f32_16x16x32_bf16 a[4:7], v[36:39], v[0:3], a[4:7]\n"
        "v_cvt_pk_bf16_f32 v200, v195, v193\n"
        "v_mul_f32 v193, v176, v188\n"
        "v_mfma_f32_16x16x32_bf16 a[8:11], v[40:43], v[0:3], a[8:11]\n"
        "global_store_dwordx4 v171, v[200:203], s[76:77] offset:-4096 sc1\n"
        "v_mul_f32 v195, v177, v188\n"
        "v_cvt_pk_bf16_f32 v193, 0, v193\n"
        "v_cvt_pk_bf16_f32 v195, 0, v195\n"
        "v_mul_f32 v196, v178, v187\n"
        "v_mfma_f32_16x16x32_bf16 a[8:11], v[72:75], v[4:7], a[8:11]\n"
        "v_cvt_pk_bf16_f32 v196, 0, v196\n"
        "v_add_f32 v193, v193, v194\n"
        "v_mfma_f32_16x16x32_bf16 a[40:43], v[72:75], v[12:15], a[40:43]\n"
        "v_add_f32 v195, v195, v196\n"
        "v_cvt_pk_bf16_f32 v193, 0, v193\n"
        "v_mfma_f32_16x16x32_bf16 a[40:43], v[40:43], v[8:11], a[40:43]\n"
        "v_cvt_pk_bf16_f32 v195, 0, v195\n"
        "v_mul_f32 v194, v178, v186\n"
        "v_mfma_f32_16x16x32_bf16 a[44:47], v[44:47], v[8:11], a[44:47]\n"
        "v_mul_f32 v196, v179, v186\n"
        "v_cvt_pk_bf16_f32 v194, 0, v194\n"
        "v_mfma_f32_16x16x32_bf16 a[44:47], v[76:79], v[12:15], a[44:47]\n"
        "v_cvt_pk_bf16_f32 v196, 0, v196\n"
        "v_add_f32 v193, v193, v194\n"
        "v_mfma_f32_16x16x32_bf16 a[12:15], v[76:79], v[4:7], a[12:15]\n"
        "v_add_f32 v195, v195, v196\n"
        "v_cvt_pk_bf16_f32 v193, 0, v193\n"
        "v_mfma_f32_16x16x32_bf16 a[12:15], v[44:47], v[0:3], a[12:15]\n"
        "global_load_dword v253, v173, s[82:83] offset:0\n"
        "s_add_u32 s82, s12, 0x500000\n"
        "s_addc_u32 s83, s13, 0\n"
        "v_mfma_f32_16x16x32_bf16 a[16:19], v[0:3], v[48:51], a[16:19]\n"
        "v_cvt_pk_bf16_f32 v195, 0, v195\n"
        "v_mul_f32 v194, v179, v185\n"
        "v_mfma_f32_16x16x32_bf16 a[16:19], v[4:7], v[80:83], a[16:19]\n"
        "v_mul_f32 v196, v180, v185\n"
        "v_cvt_pk_bf16_f32 v194, 0, v194\n"
        "v_mfma_f32_16x16x32_bf16 a[48:51], v[12:15], v[80:83], a[48:51]\n"
        "v_cvt_pk_bf16_f32 v196, 0, v196\n"
        "v_add_f32 v193, v193, v194\n"
        "v_mfma_f32_16x16x32_bf16 a[48:51], v[8:11], v[48:51], a[48:51]\n"
        "v_add_f32 v195, v195, v196\n"
        "v_cvt_pk_bf16_f32 v193, 0, v193\n"
        "v_mfma_f32_16x16x32_bf16 a[52:55], v[8:11], v[52:55], a[52:55]\n"
        "v_cvt_pk_bf16_f32 v195, 0, v195\n"
        "v_and_b32 v194, s35, v122\n"
        "v_mfma_f32_16x16x32_bf16 a[52:55], v[12:15], v[84:87], a[52:55]\n"
        "v_and_b32 v196, s35, v124\n"
        "v_mul_f32 v193, v193, v194\n"
        "v_mfma_f32_16x16x32_bf16 a[20:23], v[4:7], v[84:87], a[20:23]\n"
        "v_mul_f32 v195, v195, v196\n"
        "v_cvt_pk_bf16_f32 v210, v195, v193\n"
        "v_mfma_f32_16x16x32_bf16 a[20:23], v[0:3], v[52:55], a[20:23]\n"
        "v_add_f32 v189, v189, v190\n"
        "v_add_f32 v191, v191, v192\n"
        "v_mfma_f32_16x16x32_bf16 a[24:27], v[0:3], v[56:59], a[24:27]\n"
        "s_waitcnt lgkmcnt(0)\n"
        "v_cvt_pk_bf16_f32 v189, 0, v189\n"
        "v_cvt_pk_bf16_f32 v191, 0, v191\n"
        "v_mfma_f32_16x16x32_bf16 a[24:27], v[4:7], v[88:91], a[24:27]\n"
        "v_mul_f32 v190, v180, v186\n"
        "v_mul_f32 v192, v181, v186\n"
        "v_mfma_f32_16x16x32_bf16 a[56:59], v[12:15], v[88:91], a[56:59]\n"
        "v_cvt_pk_bf16_f32 v190, 0, v190\n"
        "v_cvt_pk_bf16_f32 v192, 0, v192\n"
        "v_mfma_f32_16x16x32_bf16 a[56:59], v[8:11], v[56:59], a[56:59]\n"
        "v_add_f32 v189, v189, v190\n"
        "v_add_f32 v191, v191, v192\n"
        "v_mfma_f32_16x16x32_bf16 a[60:63], v[8:11], v[60:63], a[60:63]\n"
        "ds_read_b128 v[8:11], v224 offset:2048\n"
        "v_cvt_pk_bf16_f32 v189, 0, v189\n"
        "v_mfma_f32_16x16x32_bf16 a[60:63], v[12:15], v[92:95], a[60:63]\n"
        "ds_read_b128 v[12:15], v225 offset:2048\n"
        "v_cvt_pk_bf16_f32 v191, 0, v191\n"
        "v_mfma_f32_16x16x32_bf16 a[28:31], v[4:7], v[92:95], a[28:31]\n"
        "ds_read_b128 v[4:7], v225 offset:0\n"
        "v_mul_f32 v190, v181, v185\n"
        "v_mfma_f32_16x16x32_bf16 a[28:31], v[0:3], v[60:63], a[28:31]\n"
        "global_load_lds_dwordx4 v238, s[86:87] offset:0\n"
        "ds_read_b128 v[0:3], v224 offset:0\n"
        "v_mfma_f32_16x16x32_bf16 a[64:67], v[32:35], v[16:19], a[64:67]\n"
        "v_mul_f32 v192, v182, v185\n"
        "v_cvt_pk_bf16_f32 v190, 0, v190\n"
        "v_mfma_f32_16x16x32_bf16 a[64:67], v[64:67], v[20:23], a[64:67]\n"
        "v_cvt_pk_bf16_f32 v192, 0, v192\n"
        "v_add_f32 v189, v189, v190\n"
        "v_mfma_f32_16x16x32_bf16 a[96:99], v[64:67], v[28:31], a[96:99]\n"
        "v_add_f32 v191, v191, v192\n"
        "v_cvt_pk_bf16_f32 v189, 0, v189\n"
        "v_mfma_f32_16x16x32_bf16 a[96:99], v[32:35], v[24:27], a[96:99]\n"
        "v_cvt_pk_bf16_f32 v191, 0, v191\n"
        "v_and_b32 v190, s35, v126\n"
        "v_mfma_f32_16x16x32_bf16 a[100:103], v[36:39], v[24:27], a[100:103]\n"
        "v_and_b32 v192, s35, v128\n"
        "v_mul_f32 v189, v189, v190\n"
        "v_mfma_f32_16x16x32_bf16 a[100:103], v[68:71], v[28:31], a[100:103]\n"
        "global_load_lds_dwordx4 v239, s[86:87] offset:1024\n"
        "v_mul_f32 v191, v191, v192\n"
        "v_mfma_f32_16x16x32_bf16 a[68:71], v[68:71], v[20:23], a[68:71]\n"
        "v_cvt_pk_bf16_f32 v209, v191, v189\n"
        "v_mul_f32 v193, v180, v188\n"
        "v_mfma_f32_16x16x32_bf16 a[68:71], v[36:39], v[16:19], a[68:71]\n"
        "v_mul_f32 v195, v181, v188\n"
        "v_cvt_pk_bf16_f32 v193, 0, v193\n"
        "v_mfma_f32_16x16x32_bf16 a[72:75], v[40:43], v[16:19], a[72:75]\n"
        "v_cvt_pk_bf16_f32 v195, 0, v195\n"
        "v_mul_f32 v194, v181, v187\n"
        "v_mfma_f32_16x16x32_bf16 a[72:75], v[72:75], v[20:23], a[72:75]\n"
        "v_mul_f32 v196, v182, v187\n"
        "v_cvt_pk_bf16_f32 v194, 0, v194\n"
        "v_mfma_f32_16x16x32_bf16 a[104:107], v[72:75], v[28:31], a[104:107]\n"
        "v_cvt_pk_bf16_f32 v196, 0, v196\n"
        "v_add_f32 v193, v193, v194\n"
        "v_mfma_f32_16x16x32_bf16 a[104:107], v[40:43], v[24:27], a[104:107]\n"
        "global_load_lds_dwordx4 v240, s[86:87] offset:2048\n"
        "v_add_f32 v195, v195, v196\n"
        "v_mfma_f32_16x16x32_bf16 a[108:111], v[44:47], v[24:27], a[108:111]\n"
        "v_cvt_pk_bf16_f32 v193, 0, v193\n"
        "v_cvt_pk_bf16_f32 v195, 0, v195\n"
        "v_mfma_f32_16x16x32_bf16 a[108:111], v[76:79], v[28:31], a[108:111]\n"
        "v_mul_f32 v194, v182, v186\n"
        "v_mul_f32 v196, v183, v186\n"
        "v_mfma_f32_16x16x32_bf16 a[76:79], v[76:79], v[20:23], a[76:79]\n"
        "v_cvt_pk_bf16_f32 v194, 0, v194\n"
        "v_cvt_pk_bf16_f32 v196, 0, v196\n"
        "v_mfma_f32_16x16x32_bf16 a[76:79], v[44:47], v[16:19], a[76:79]\n"
        "v_add_f32 v193, v193, v194\n"
        "v_add_f32 v195, v195, v196\n"
        "v_mfma_f32_16x16x32_bf16 a[80:83], v[16:19], v[48:51], a[80:83]\n"
        "v_cvt_pk_bf16_f32 v193, 0, v193\n"
        "v_cvt_pk_bf16_f32 v195, 0, v195\n"
        "v_mfma_f32_16x16x32_bf16 a[80:83], v[20:23], v[80:83], a[80:83]\n"
        "global_load_lds_dwordx4 v241, s[86:87] offset:3072\n"
        "s_add_u32 m0, s73, 0x14000\n"
        "v_mul_f32 v194, v183, v185\n"
        "v_mfma_f32_16x16x32_bf16 a[112:115], v[28:31], v[80:83], a[112:115]\n"
        "s_add_u32 s86, s62, 0x380000\n"
        "s_addc_u32 s87, s63, 0\n"
        "v_mul_f32 v196, v184, v185\n"
        "v_mfma_f32_16x16x32_bf16 a[112:115], v[24:27], v[48:51], a[112:115]\n"
        "v_cvt_pk_bf16_f32 v194, 0, v194\n"
        "v_cvt_pk_bf16_f32 v196, 0, v196\n"
        "v_mfma_f32_16x16x32_bf16 a[116:119], v[24:27], v[52:55], a[116:119]\n"
        "v_add_f32 v193, v193, v194\n"
        "v_add_f32 v195, v195, v196\n"
        "v_mfma_f32_16x16x32_bf16 a[116:119], v[28:31], v[84:87], a[116:119]\n"
        "v_cvt_pk_bf16_f32 v193, 0, v193\n"
        "v_cvt_pk_bf16_f32 v195, 0, v195\n"
        "v_mfma_f32_16x16x32_bf16 a[84:87], v[20:23], v[84:87], a[84:87]\n"
        "v_and_b32 v194, s35, v130\n"
        "v_and_b32 v196, s35, v132\n"
        "v_mfma_f32_16x16x32_bf16 a[84:87], v[16:19], v[52:55], a[84:87]\n"
        "global_load_lds_dwordx4 v238, s[86:87] offset:0\n"
        "v_mul_f32 v193, v193, v194\n"
        "v_mfma_f32_16x16x32_bf16 a[88:91], v[16:19], v[56:59], a[88:91]\n"
        "v_mul_f32 v195, v195, v196\n"
        "v_cvt_pk_bf16_f32 v208, v195, v193\n"
        "v_mfma_f32_16x16x32_bf16 a[88:91], v[20:23], v[88:91], a[88:91]\n"
        "v_lshlrev_b32 v185, 16, v138\n"
        "v_and_b32 v186, s35, v138\n"
        "v_mfma_f32_16x16x32_bf16 a[120:123], v[28:31], v[88:91], a[120:123]\n"
        "v_lshlrev_b32 v187, 16, v139\n"
        "v_and_b32 v188, s35, v139\n"
        "v_mfma_f32_16x16x32_bf16 a[120:123], v[24:27], v[56:59], a[120:123]\n"
        "global_store_dwordx4 v171, v[208:211], s[76:77] offset:0 sc1\n"
        "s_add_u32 s76, s76, 0x2000\n"
        "s_addc_u32 s77, s77, 0\n"
        "v_lshlrev_b32 v174, 16, v97\n"
        "v_lshlrev_b32 v175, 16, v99\n"
        "v_lshlrev_b32 v176, 16, v101\n"
        "v_mfma_f32_16x16x32_bf16 a[124:127], v[24:27], v[60:63], a[124:127]\n"
        "ds_read_b128 v[24:27], v224 offset:6144\n"
        "v_lshlrev_b32 v177, 16, v103\n"
        "v_mfma_f32_16x16x32_bf16 a[124:127], v[28:31], v[92:95], a[124:127]\n"
        "ds_read_b128 v[28:31], v225 offset:6144\n"
        "v_lshlrev_b32 v178, 16, v105\n"
        "v_mfma_f32_16x16x32_bf16 a[92:95], v[20:23], v[92:95], a[92:95]\n"
        "ds_read_b128 v[20:23], v225 offset:4096\n"
        "v_lshlrev_b32 v179, 16, v107\n"
        "v_mfma_f32_16x16x32_bf16 a[92:95], v[16:19], v[60:63], a[92:95]\n"
        "ds_read_b128 v[16:19], v224 offset:4096\n"
        "s_waitcnt lgkmcnt(4)\n"
        "v_mfma_f32_16x16x32_bf16 a[128:131], v[32:35], v[0:3], a[128:131]\n"
        "global_load_lds_dwordx4 v239, s[86:87] offset:1024\n"
        "v_mfma_f32_16x16x32_bf16 a[128:131], v[64:67], v[4:7], a[128:131]\n"
        "v_lshlrev_b32 v180, 16, v109\n"
        "v_lshlrev_b32 v181, 16, v111\n"
        "v_mfma_f32_16x16x32_bf16 a[160:163], v[64:67], v[12:15], a[160:163]\n"
        "v_lshlrev_b32 v182, 16, v113\n"
        "v_lshlrev_b32 v183, 16, v115\n"
        "v_mfma_f32_16x16x32_bf16 a[160:163], v[32:35], v[8:11], a[160:163]\n"
        "v_lshlrev_b32 v184, 16, v117\n"
        "v_mul_f32 v189, v174, v188\n"
        "v_mfma_f32_16x16x32_bf16 a[164:167], v[36:39], v[8:11], a[164:167]\n"
        "global_load_dwordx4 v[150:153], v254, s[92:93] offset:2048\n"
        "v_mfma_f32_16x16x32_bf16 a[164:167], v[68:71], v[12:15], a[164:167]\n"
        "v_mul_f32 v191, v175, v188\n"
        "v_cvt_pk_bf16_f32 v189, 0, v189\n"
        "v_mfma_f32_16x16x32_bf16 a[132:135], v[68:71], v[4:7], a[132:135]\n"
        "v_cvt_pk_bf16_f32 v191, 0, v191\n"
        "v_mul_f32 v190, v175, v187\n"
        "v_mfma_f32_16x16x32_bf16 a[132:135], v[36:39], v[0:3], a[132:135]\n"
        "v_mul_f32 v192, v176, v187\n"
        "v_cvt_pk_bf16_f32 v190, 0, v190\n"
        "v_mfma_f32_16x16x32_bf16 a[136:139], v[40:43], v[0:3], a[136:139]\n"
        "v_cvt_pk_bf16_f32 v192, 0, v192\n"
        "v_add_f32 v189, v189, v190\n"
        "v_mfma_f32_16x16x32_bf16 a[136:139], v[72:75], v[4:7], a[136:139]\n"
        "v_add_f32 v191, v191, v192\n"
        "v_cvt_pk_bf16_f32 v189, 0, v189\n"
        "v_mfma_f32_16x16x32_bf16 a[168:171], v[72:75], v[12:15], a[168:171]\n"
        "global_load_lds_dwordx4 v240, s[86:87] offset:2048\n"
        "v_mfma_f32_16x16x32_bf16 a[168:171], v[40:43], v[8:11], a[168:171]\n"
        "v_cvt_pk_bf16_f32 v191, 0, v191\n"
        "v_mul_f32 v190, v176, v186\n"
        "v_mfma_f32_16x16x32_bf16 a[172:175], v[44:47], v[8:11], a[172:175]\n"
        "v_mul_f32 v192, v177, v186\n"
        "v_cvt_pk_bf16_f32 v190, 0, v190\n"
        "v_mfma_f32_16x16x32_bf16 a[172:175], v[76:79], v[12:15], a[172:175]\n"
        "v_cvt_pk_bf16_f32 v192, 0, v192\n"
        "v_add_f32 v189, v189, v190\n"
        "v_mfma_f32_16x16x32_bf16 a[140:143], v[76:79], v[4:7], a[140:143]\n"
        "v_add_f32 v191, v191, v192\n"
        "v_cvt_pk_bf16_f32 v189, 0, v189\n"
        "v_mfma_f32_16x16x32_bf16 a[140:143], v[44:47], v[0:3], a[140:143]\n"
        "v_cvt_pk_bf16_f32 v191, 0, v191\n"
        "v_mul_f32 v190, v177, v185\n"
        "v_mfma_f32_16x16x32_bf16 a[144:147], v[0:3], v[48:51], a[144:147]\n"
        "global_load_lds_dwordx4 v241, s[86:87] offset:3072\n"
        "s_add_u32 m0, s72, 0x0\n"
        "v_mfma_f32_16x16x32_bf16 a[144:147], v[4:7], v[80:83], a[144:147]\n"
        "s_add_u32 s86, s56, 0x380\n"
        "s_addc_u32 s87, s57, 0\n"
        "v_mul_f32 v192, v178, v185\n"
        "v_mfma_f32_16x16x32_bf16 a[176:179], v[12:15], v[80:83], a[176:179]\n"
        "v_cvt_pk_bf16_f32 v190, 0, v190\n"
        "v_cvt_pk_bf16_f32 v192, 0, v192\n"
        "v_mfma_f32_16x16x32_bf16 a[176:179], v[8:11], v[48:51], a[176:179]\n"
        "v_add_f32 v189, v189, v190\n"
        "v_add_f32 v191, v191, v192\n"
        "v_mfma_f32_16x16x32_bf16 a[180:183], v[8:11], v[52:55], a[180:183]\n"
        "v_cvt_pk_bf16_f32 v189, 0, v189\n"
        "v_cvt_pk_bf16_f32 v191, 0, v191\n"
        "v_mfma_f32_16x16x32_bf16 a[180:183], v[12:15], v[84:87], a[180:183]\n"
        "v_lshlrev_b32 v190, 16, v119\n"
        "v_lshlrev_b32 v192, 16, v121\n"
        "v_mfma_f32_16x16x32_bf16 a[148:151], v[4:7], v[84:87], a[148:151]\n"
        "v_mul_f32 v189, v189, v190\n"
        "v_mul_f32 v191, v191, v192\n"
        "v_mfma_f32_16x16x32_bf16 a[148:151], v[0:3], v[52:55], a[148:151]\n"
        "v_cvt_pk_bf16_f32 v203, v191, v189\n"
        "v_mul_f32 v193, v176, v188\n"
        "v_mfma_f32_16x16x32_bf16 a[152:155], v[0:3], v[56:59], a[152:155]\n"
        "v_mul_f32 v195, v177, v188\n"
        "v_cvt_pk_bf16_f32 v193, 0, v193\n"
        "v_mfma_f32_16x16x32_bf16 a[152:155], v[4:7], v[88:91], a[152:155]\n"
        "v_cvt_pk_bf16_f32 v195, 0, v195\n"
        "v_mul_f32 v194, v177, v187\n"
        "v_mfma_f32_16x16x32_bf16 a[184:187], v[12:15], v[88:91], a[184:187]\n"
        "v_mul_f32 v196, v178, v187\n"
        "v_cvt_pk_bf16_f32 v194, 0, v194\n"
        "v_mfma_f32_16x16x32_bf16 a[184:187], v[8:11], v[56:59], a[184:187]\n"
        "s_waitcnt vmcnt(13) lgkmcnt(0)\n"
        "v_cvt_pk_bf16_f32 v196, 0, v196\n"
        "v_add_f32 v193, v193, v194\n"
        "v_mfma_f32_16x16x32_bf16 a[188:191], v[8:11], v[60:63], a[188:191]\n"
        "s_barrier\n"
        "v_mfma_f32_16x16x32_bf16 a[188:191], v[12:15], v[92:95], a[188:191]\n"
        "global_load_lds_dwordx4 v234, s[86:87] offset:0\n"
        "ds_read_b128 v[8:11], v224 offset:51200\n"
        "v_mfma_f32_16x16x32_bf16 a[156:159], v[4:7], v[92:95], a[156:159]\n"
        "ds_read_b128 v[4:7], v225 offset:49152\n"
        "v_add_f32 v195, v195, v196\n"
        "v_mfma_f32_16x16x32_bf16 a[156:159], v[0:3], v[60:63], a[156:159]\n"
        "ds_read_b128 v[0:3], v224 offset:49152\n"
        "v_cvt_pk_bf16_f32 v193, 0, v193\n"
        "v_mfma_f32_16x16x32_bf16 a[192:195], v[32:35], v[16:19], a[192:195]\n"
        "ds_read_b128 v[12:15], v225 offset:51200\n"
        "v_cvt_pk_bf16_f32 v195, 0, v195\n"
        "v_mfma_f32_16x16x32_bf16 a[192:195], v[64:67], v[20:23], a[192:195]\n"
        "global_load_lds_dwordx4 v235, s[86:87] offset:1024\n"
        "v_mul_f32 v194, v178, v186\n"
        "v_mfma_f32_16x16x32_bf16 a[224:227], v[64:67], v[28:31], a[224:227]\n"
        "ds_read_b64_tr_b16 v[64:65], v216 offset:8192\n"
        "v_mul_f32 v196, v179, v186\n"
        "v_mfma_f32_16x16x32_bf16 a[224:227], v[32:35], v[24:27], a[224:227]\n"
        "ds_read_b64_tr_b16 v[32:33], v216 offset:0\n"
        "v_cvt_pk_bf16_f32 v194, 0, v194\n"
        "v_mfma_f32_16x16x32_bf16 a[228:231], v[36:39], v[24:27], a[228:231]\n"
        "ds_read_b64_tr_b16 v[34:35], v217 offset:0\n"
        "v_cvt_pk_bf16_f32 v196, 0, v196\n"
        "v_mfma_f32_16x16x32_bf16 a[228:231], v[68:71], v[28:31], a[228:231]\n"
        "ds_read_b64_tr_b16 v[66:67], v217 offset:8192\n"
        "global_load_lds_dwordx4 v236, s[86:87] offset:2048\n"
        "v_mfma_f32_16x16x32_bf16 a[196:199], v[68:71], v[20:23], a[196:199]\n"
        "ds_read_b64_tr_b16 v[68:69], v218 offset:8192\n"
        "v_add_f32 v193, v193, v194\n"
        "v_mfma_f32_16x16x32_bf16 a[196:199], v[36:39], v[16:19], a[196:199]\n"
        "ds_read_b64_tr_b16 v[36:37], v218 offset:0\n"
        "v_add_f32 v195, v195, v196\n"
        "v_mfma_f32_16x16x32_bf16 a[200:203], v[40:43], v[16:19], a[200:203]\n"
        "ds_read_b64_tr_b16 v[38:39], v219 offset:0\n"
        "v_cvt_pk_bf16_f32 v193, 0, v193\n"
        "v_mfma_f32_16x16x32_bf16 a[200:203], v[72:75], v[20:23], a[200:203]\n"
        "ds_read_b64_tr_b16 v[70:71], v219 offset:8192\n"
        "global_load_lds_dwordx4 v237, s[86:87] offset:3072\n"
        "s_add_u32 m0, s72, 0x1000\n"
        "v_mfma_f32_16x16x32_bf16 a[232:235], v[72:75], v[28:31], a[232:235]\n"
        "ds_read_b64_tr_b16 v[72:73], v220 offset:8192\n"
        "s_add_u32 s86, s58, 0x380\n"
        "s_addc_u32 s87, s59, 0\n"
        "v_mfma_f32_16x16x32_bf16 a[232:235], v[40:43], v[24:27], a[232:235]\n"
        "ds_read_b64_tr_b16 v[40:41], v220 offset:0\n"
        "v_cvt_pk_bf16_f32 v195, 0, v195\n"
        "v_mfma_f32_16x16x32_bf16 a[236:239], v[44:47], v[24:27], a[236:239]\n"
        "ds_read_b64_tr_b16 v[42:43], v221 offset:0\n"
        "v_mul_f32 v194, v179, v185\n"
        "v_mfma_f32_16x16x32_bf16 a[236:239], v[76:79], v[28:31], a[236:239]\n"
        "ds_read_b64_tr_b16 v[74:75], v221 offset:8192\n"
        "global_load_lds_dwordx4 v234, s[86:87] offset:0\n"
        "v_mfma_f32_16x16x32_bf16 a[204:207], v[76:79], v[20:23], a[204:207]\n"
        "ds_read_b64_tr_b16 v[76:77], v222 offset:8192\n"
        "v_mul_f32 v196, v180, v185\n"
        "v_mfma_f32_16x16x32_bf16 a[204:207], v[44:47], v[16:19], a[204:207]\n"
        "ds_read_b64_tr_b16 v[44:45], v222 offset:0\n"
        "v_cvt_pk_bf16_f32 v194, 0, v194\n"
        "LHA2_%=:\n"
        "s_cbranch_execz LHA6_%=\n"
        "v_mfma_f32_16x16x32_bf16 a[208:211], v[16:19], v[48:51], a[208:211]\n"
        "ds_read_b64_tr_b16 v[46:47], v223 offset:0\n"
        "v_cvt_pk_bf16_f32 v196, 0, v196\n"
        "v_mfma_f32_16x16x32_bf16 a[208:211], v[20:23], v[80:83], a[208:211]\n"
        "ds_read_b64_tr_b16 v[78:79], v223 offset:8192\n"
        "global_load_lds_dwordx4 v235, s[86:87] offset:1024\n"
        "v_mfma_f32_16x16x32_bf16 a[240:243], v[28:31], v[80:83], a[240:243]\n"
        "ds_read_b64_tr_b16 v[80:81], v216 offset:24576\n"
        "v_add_f32 v193, v193, v194\n"
        "v_mfma_f32_16x16x32_bf16 a[240:243], v[24:27], v[48:51], a[240:243]\n"
        "ds_read_b64_tr_b16 v[48:49], v216 offset:16384\n"
        "v_add_f32 v195, v195, v196\n"
        "v_mfma_f32_16x16x32_bf16 a[244:247], v[24:27], v[52:55], a[244:247]\n"
        "ds_read_b64_tr_b16 v[50:51], v217 offset:16384\n"
        "v_cvt_pk_bf16_f32 v193, 0, v193\n"
        "v_mfma_f32_16x16x32_bf16 a[244:247], v[28:31], v[84:87], a[244:247]\n"
        "ds_read_b64_tr_b16 v[82:83], v217 offset:24576\n"
        "global_load_lds_dwordx4 v236, s[86:87] offset:2048\n"
        "v_mfma_f32_16x16x32_bf16 a[212:215], v[20:23], v[84:87], a[212:215]\n"
        "ds_read_b64_tr_b16 v[84:85], v218 offset:24576\n"
        "v_cvt_pk_bf16_f32 v195, 0, v195\n"
        "v_mfma_f32_16x16x32_bf16 a[212:215], v[16:19], v[52:55], a[212:215]\n"
        "ds_read_b64_tr_b16 v[52:53], v218 offset:16384\n"
        "v_lshlrev_b32 v194, 16, v123\n"
        "v_mfma_f32_16x16x32_bf16 a[216:219], v[16:19], v[56:59], a[216:219]\n"
        "ds_read_b64_tr_b16 v[54:55], v219 offset:16384\n"
        "v_lshlrev_b32 v196, 16, v125\n"
        "v_mfma_f32_16x16x32_bf16 a[216:219], v[20:23], v[88:91], a[216:219]\n"
        "ds_read_b64_tr_b16 v[86:87], v219 offset:24576\n"
        "global_load_lds_dwordx4 v237, s[86:87] offset:3072\n"
        "s_add_u32 m0, s73, 0x18000\n"
        "v_mfma_f32_16x16x32_bf16 a[248:251], v[28:31], v[88:91], a[248:251]\n"
        "ds_read_b64_tr_b16 v[88:89], v220 offset:24576\n"
        "s_add_u32 s86, s60, 0x180000\n"
        "s_addc_u32 s87, s61, 0\n"
        "v_mfma_f32_16x16x32_bf16 a[248:251], v[24:27], v[56:59], a[248:251]\n"
        "ds_read_b64_tr_b16 v[56:57], v220 offset:16384\n"
        "v_mul_f32 v193, v193, v194\n"
        "v_mfma_f32_16x16x32_bf16 a[252:255], v[24:27], v[60:63], a[252:255]\n"
        "ds_read_b64_tr_b16 v[58:59], v221 offset:16384\n"
        "v_mul_f32 v195, v195, v196\n"
        "v_mfma_f32_16x16x32_bf16 a[252:255], v[28:31], v[92:95], a[252:255]\n"
        "ds_read_b64_tr_b16 v[90:91], v221 offset:24576\n"
        "v_cvt_pk_bf16_f32 v202, v195, v193\n"
        "v_mfma_f32_16x16x32_bf16 a[220:223], v[20:23], v[92:95], a[220:223]\n"
        "ds_read_b64_tr_b16 v[92:93], v222 offset:24576\n"
        "v_mul_f32 v189, v178, v188\n"
        "v_mfma_f32_16x16x32_bf16 a[220:223], v[16:19], v[60:63], a[220:223]\n"
        "ds_read_b64_tr_b16 v[60:61], v222 offset:16384\n"
        "s_waitcnt lgkmcnt(8)\n"
        "v_mfma_f32_16x16x32_bf16 a[0:3], v[32:35], v[0:3], a[0:3]\n"
        "ds_read_b64_tr_b16 v[62:63], v223 offset:16384\n"
        "global_load_dwordx4 v[154:157], v254, s[92:93] offset:3072\n"
        "v_mfma_f32_16x16x32_bf16 a[0:3], v[64:67], v[4:7], a[0:3]\n"
        "ds_read_b64_tr_b16 v[94:95], v223 offset:24576\n"
        "v_mul_f32 v191, v179, v188\n"
        "v_mfma_f32_16x16x32_bf16 a[32:35], v[64:67], v[12:15], a[32:35]\n"
        "ds_read_b128 v[16:19], v224 offset:53248\n"
        "v_cvt_pk_bf16_f32 v189, 0, v189\n"
        "v_mfma_f32_16x16x32_bf16 a[32:35], v[32:35], v[8:11], a[32:35]\n"
        "ds_read_b128 v[20:23], v225 offset:53248\n"
        "v_cvt_pk_bf16_f32 v191, 0, v191\n"
        "v_mfma_f32_16x16x32_bf16 a[36:39], v[36:39], v[8:11], a[36:39]\n"
        "ds_read_b128 v[24:27], v224 offset:55296\n"
        "v_mul_f32 v190, v179, v187\n"
        "v_mfma_f32_16x16x32_bf16 a[36:39], v[68:71], v[12:15], a[36:39]\n"
        "ds_read_b128 v[28:31], v225 offset:55296\n"
        "v_mul_f32 v192, v180, v187\n"
        "v_mfma_f32_16x16x32_bf16 a[4:7], v[68:71], v[4:7], a[4:7]\n"
        "v_cvt_pk_bf16_f32 v190, 0, v190\n"
        "v_cvt_pk_bf16_f32 v192, 0, v192\n"
        "v_mfma_f32_16x16x32_bf16 a[4:7], v[36:39], v[0:3], a[4:7]\n"
        "v_add_f32 v189, v189, v190\n"
        "v_add_f32 v191, v191, v192\n"
        "v_mfma_f32_16x16x32_bf16 a[8:11], v[40:43], v[0:3], a[8:11]\n"
        "v_cvt_pk_bf16_f32 v189, 0, v189\n"
        "v_cvt_pk_bf16_f32 v191, 0, v191\n"
        "v_mfma_f32_16x16x32_bf16 a[8:11], v[72:75], v[4:7], a[8:11]\n"
        "v_mul_f32 v190, v180, v186\n"
        "v_mul_f32 v192, v181, v186\n"
        "v_mfma_f32_16x16x32_bf16 a[40:43], v[72:75], v[12:15], a[40:43]\n"
        "v_cvt_pk_bf16_f32 v190, 0, v190\n"
        "v_cvt_pk_bf16_f32 v192, 0, v192\n"
        "v_mfma_f32_16x16x32_bf16 a[40:43], v[40:43], v[8:11], a[40:43]\n"
        "v_add_f32 v189, v189, v190\n"
        "v_add_f32 v191, v191, v192\n"
        "v_mfma_f32_16x16x32_bf16 a[44:47], v[44:47], v[8:11], a[44:47]\n"
        "v_cvt_pk_bf16_f32 v189, 0, v189\n"
        "v_cvt_pk_bf16_f32 v191, 0, v191\n"
        "v_mfma_f32_16x16x32_bf16 a[44:47], v[76:79], v[12:15], a[44:47]\n"
        "v_mul_f32 v190, v181, v185\n"
        "v_mul_f32 v192, v182, v185\n"
        "v_mfma_f32_16x16x32_bf16 a[12:15], v[76:79], v[4:7], a[12:15]\n"
        "v_cvt_pk_bf16_f32 v190, 0, v190\n"
        "v_cvt_pk_bf16_f32 v192, 0, v192\n"
        "v_mfma_f32_16x16x32_bf16 a[12:15], v[44:47], v[0:3], a[12:15]\n"
        "global_load_dword v253, v173, s[82:83] offset:0\n"
        "s_add_u32 s82, s12, 0x600000\n"
        "s_addc_u32 s83, s13, 0\n"
        "v_mfma_f32_16x16x32_bf16 a[16:19], v[0:3], v[48:51], a[16:19]\n"
        "s_waitcnt lgkmcnt(0)\n"
        "v_add_f32 v189, v189, v190\n"
        "v_add_f32 v191, v191, v192\n"
        "v_mfma_f32_16x16x32_bf16 a[16:19], v[4:7], v[80:83], a[16:19]\n"
        "v_cvt_pk_bf16_f32 v189, 0, v189\n"
        "v_cvt_pk_bf16_f32 v191, 0, v191\n"
        "v_mfma_f32_16x16x32_bf16 a[48:51], v[12:15], v[80:83], a[48:51]\n"
        "v_lshlrev_b32 v190, 16, v127\n"
        "v_lshlrev_b32 v192, 16, v129\n"
        "v_mfma_f32_16x16x32_bf16 a[48:51], v[8:11], v[48:51], a[48:51]\n"
        "v_mul_f32 v189, v189, v190\n"
        "v_mul_f32 v191, v191, v192\n"
        "v_mfma_f32_16x16x32_bf16 a[52:55], v[8:11], v[52:55], a[52:55]\n"
        "v_cvt_pk_bf16_f32 v201, v191, v189\n"
        "v_mul_f32 v193, v180, v188\n"
        "v_mfma_f32_16x16x32_bf16 a[52:55], v[12:15], v[84:87], a[52:55]\n"
        "v_mul_f32 v195, v181, v188\n"
        "v_cvt_pk_bf16_f32 v193, 0, v193\n"
        "v_mfma_f32_16x16x32_bf16 a[20:23], v[4:7], v[84:87], a[20:23]\n"
        "v_cvt_pk_bf16_f32 v195, 0, v195\n"
        "v_mul_f32 v194, v181, v187\n"
        "v_mfma_f32_16x16x32_bf16 a[20:23], v[0:3], v[52:55], a[20:23]\n"
        "v_mul_f32 v196, v182, v187\n"
        "v_cvt_pk_bf16_f32 v194, 0, v194\n"
        "v_mfma_f32_16x16x32_bf16 a[24:27], v[0:3], v[56:59], a[24:27]\n"
        "v_cvt_pk_bf16_f32 v196, 0, v196\n"
        "v_add_f32 v193, v193, v194\n"
        "v_mfma_f32_16x16x32_bf16 a[24:27], v[4:7], v[88:91], a[24:27]\n"
        "v_add_f32 v195, v195, v196\n"
        "v_cvt_pk_bf16_f32 v193, 0, v193\n"
        "v_mfma_f32_16x16x32_bf16 a[56:59], v[12:15], v[88:91], a[56:59]\n"
        "v_cvt_pk_bf16_f32 v195, 0, v195\n"
        "v_mul_f32 v194, v182, v186\n"
        "v_mfma_f32_16x16x32_bf16 a[56:59], v[8:11], v[56:59], a[56:59]\n"
        "v_mul_f32 v196, v183, v186\n"
        "v_cvt_pk_bf16_f32 v194, 0, v194\n"
        "v_mfma_f32_16x16x32_bf16 a[60:63], v[8:11], v[60:63], a[60:63]\n"
        "ds_read_b128 v[8:11], v224 offset:34816\n"
        "v_cvt_pk_bf16_f32 v196, 0, v196\n"
        "v_mfma_f32_16x16x32_bf16 a[60:63], v[12:15], v[92:95], a[60:63]\n"
        "ds_read_b128 v[12:15], v225 offset:34816\n"
        "v_add_f32 v193, v193, v194\n"
        "v_mfma_f32_16x16x32_bf16 a[28:31], v[4:7], v[92:95], a[28:31]\n"
        "ds_read_b128 v[4:7], v225 offset:32768\n"
        "v_add_f32 v195, v195, v196\n"
        "v_mfma_f32_16x16x32_bf16 a[28:31], v[0:3], v[60:63], a[28:31]\n"
        "global_load_lds_dwordx4 v238, s[86:87] offset:0\n"
        "ds_read_b128 v[0:3], v224 offset:32768\n"
        "v_mfma_f32_16x16x32_bf16 a[64:67], v[32:35], v[16:19], a[64:67]\n"
        "v_cvt_pk_bf16_f32 v193, 0, v193\n"
        "v_cvt_pk_bf16_f32 v195, 0, v195\n"
        "v_mfma_f32_16x16x32_bf16 a[64:67], v[64:67], v[20:23], a[64:67]\n"
        "v_mul_f32 v194, v183, v185\n"
        "v_mul_f32 v196, v184, v185\n"
        "v_mfma_f32_16x16x32_bf16 a[96:99], v[64:67], v[28:31], a[96:99]\n"
        "v_cvt_pk_bf16_f32 v194, 0, v194\n"
        "v_cvt_pk_bf16_f32 v196, 0, v196\n"
        "v_mfma_f32_16x16x32_bf16 a[96:99], v[32:35], v[24:27], a[96:99]\n"
        "v_add_f32 v193, v193, v194\n"
        "v_add_f32 v195, v195, v196\n"
        "v_mfma_f32_16x16x32_bf16 a[100:103], v[36:39], v[24:27], a[100:103]\n"
        "v_cvt_pk_bf16_f32 v193, 0, v193\n"
        "v_cvt_pk_bf16_f32 v195, 0, v195\n"
        "v_mfma_f32_16x16x32_bf16 a[100:103], v[68:71], v[28:31], a[100:103]\n"
        "global_load_lds_dwordx4 v239, s[86:87] offset:1024\n"
        "v_lshlrev_b32 v194, 16, v131\n"
        "v_mfma_f32_16x16x32_bf16 a[68:71], v[68:71], v[20:23], a[68:71]\n"
        "v_lshlrev_b32 v196, 16, v133\n"
        "v_mul_f32 v193, v193, v194\n"
        "v_mfma_f32_16x16x32_bf16 a[68:71], v[36:39], v[16:19], a[68:71]\n"
        "v_mul_f32 v195, v195, v196\n"
        "v_cvt_pk_bf16_f32 v200, v195, v193\n"
        "v_mfma_f32_16x16x32_bf16 a[72:75], v[40:43], v[16:19], a[72:75]\n"
        "v_lshlrev_b32 v185, 16, v140\n"
        "v_and_b32 v186, s35, v140\n"
        "v_mfma_f32_16x16x32_bf16 a[72:75], v[72:75], v[20:23], a[72:75]\n"
        "global_store_dwordx4 v171, v[200:203], s[76:77] offset:-4096 sc1\n"
        "v_lshlrev_b32 v187, 16, v141\n"
        "v_and_b32 v188, s35, v141\n"
        "v_and_b32 v174, s35, v97\n"
        "v_and_b32 v175, s35, v99\n"
        "v_mfma_f32_16x16x32_bf16 a[104:107], v[72:75], v[28:31], a[104:107]\n"
        "v_and_b32 v176, s35, v101\n"
        "v_and_b32 v177, s35, v103\n"
        "v_mfma_f32_16x16x32_bf16 a[104:107], v[40:43], v[24:27], a[104:107]\n"
        "v_and_b32 v178, s35, v105\n"
        "v_and_b32 v179, s35, v107\n"
        "v_mfma_f32_16x16x32_bf16 a[108:111], v[44:47], v[24:27], a[108:111]\n"
        "v_and_b32 v180, s35, v109\n"
        "v_and_b32 v181, s35, v111\n"
        "v_mfma_f32_16x16x32_bf16 a[108:111], v[76:79], v[28:31], a[108:111]\n"
        "v_and_b32 v182, s35, v113\n"
        "v_and_b32 v183, s35, v115\n"
        "v_mfma_f32_16x16x32_bf16 a[76:79], v[76:79], v[20:23], a[76:79]\n"
        "global_load_lds_dwordx4 v240, s[86:87] offset:2048\n"
        "v_mfma_f32_16x16x32_bf16 a[76:79], v[44:47], v[16:19], a[76:79]\n"
        "v_and_b32 v184, s35, v117\n"
        "v_mul_f32 v189, v174, v188\n"
        "v_mfma_f32_16x16x32_bf16 a[80:83], v[16:19], v[48:51], a[80:83]\n"
        "v_mul_f32 v191, v175, v188\n"
        "v_cvt_pk_bf16_f32 v189, 0, v189\n"
        "v_mfma_f32_16x16x32_bf16 a[80:83], v[20:23], v[80:83], a[80:83]\n"
        "v_cvt_pk_bf16_f32 v191, 0, v191\n"
        "v_mul_f32 v190, v175, v187\n"
        "v_mfma_f32_16x16x32_bf16 a[112:115], v[28:31], v[80:83], a[112:115]\n"
        "v_mul_f32 v192, v176, v187\n"
        "v_cvt_pk_bf16_f32 v190, 0, v190\n"
        "v_mfma_f32_16x16x32_bf16 a[112:115], v[24:27], v[48:51], a[112:115]\n"
        "v_cvt_pk_bf16_f32 v192, 0, v192\n"
        "v_add_f32 v189, v189, v190\n"
        "v_mfma_f32_16x16x32_bf16 a[116:119], v[24:27], v[52:55], a[116:119]\n"
        "global_load_lds_dwordx4 v241, s[86:87] offset:3072\n"
        "s_add_u32 m0, s73, 0x1c000\n"
        "v_mfma_f32_16x16x32_bf16 a[116:119], v[28:31], v[84:87], a[116:119]\n"
        "s_add_u32 s86, s62, 0x180000\n"
        "s_addc_u32 s87, s63, 0\n"
        "v_add_f32 v191, v191, v192\n"
        "v_mfma_f32_16x16x32_bf16 a[84:87], v[20:23], v[84:87], a[84:87]\n"
        "v_cvt_pk_bf16_f32 v189, 0, v189\n"
        "v_cvt_pk_bf16_f32 v191, 0, v191\n"
        "v_mfma_f32_16x16x32_bf16 a[84:87], v[16:19], v[52:55], a[84:87]\n"
        "v_mul_f32 v190, v176, v186\n"
        "v_mul_f32 v192, v177, v186\n"
        "v_mfma_f32_16x16x32_bf16 a[88:91], v[16:19], v[56:59], a[88:91]\n"
        "v_cvt_pk_bf16_f32 v190, 0, v190\n"
        "v_cvt_pk_bf16_f32 v192, 0, v192\n"
        "v_mfma_f32_16x16x32_bf16 a[88:91], v[20:23], v[88:91], a[88:91]\n"
        "v_add_f32 v189, v189, v190\n"
        "v_add_f32 v191, v191, v192\n"
        "v_mfma_f32_16x16x32_bf16 a[120:123], v[28:31], v[88:91], a[120:123]\n"
        "global_load_lds_dwordx4 v238, s[86:87] offset:0\n"
        "v_mfma_f32_16x16x32_bf16 a[120:123], v[24:27], v[56:59], a[120:123]\n"
        "v_cvt_pk_bf16_f32 v189, 0, v189\n"
        "v_cvt_pk_bf16_f32 v191, 0, v191\n"
        "v_mfma_f32_16x16x32_bf16 a[124:127], v[24:27], v[60:63], a[124:127]\n"
        "ds_read_b128 v[24:27], v224 offset:38912\n"
        "v_mul_f32 v190, v177, v185\n"
        "v_mfma_f32_16x16x32_bf16 a[124:127], v[28:31], v[92:95], a[124:127]\n"
        "ds_read_b128 v[28:31], v225 offset:38912\n"
        "v_mul_f32 v192, v178, v185\n"
        "v_mfma_f32_16x16x32_bf16 a[92:95], v[20:23], v[92:95], a[92:95]\n"
        "ds_read_b128 v[20:23], v225 offset:36864\n"
        "v_cvt_pk_bf16_f32 v190, 0, v190\n"
        "v_mfma_f32_16x16x32_bf16 a[92:95], v[16:19], v[60:63], a[92:95]\n"
        "ds_read_b128 v[16:19], v224 offset:36864\n"
        "s_waitcnt lgkmcnt(4)\n"
        "v_mfma_f32_16x16x32_bf16 a[128:131], v[32:35], v[0:3], a[128:131]\n"
        "global_load_lds_dwordx4 v239, s[86:87] offset:1024\n"
        "v_mfma_f32_16x16x32_bf16 a[128:131], v[64:67], v[4:7], a[128:131]\n"
        "v_cvt_pk_bf16_f32 v192, 0, v192\n"
        "v_add_f32 v189, v189, v190\n"
        "v_mfma_f32_16x16x32_bf16 a[160:163], v[64:67], v[12:15], a[160:163]\n"
        "v_add_f32 v191, v191, v192\n"
        "v_cvt_pk_bf16_f32 v189, 0, v189\n"
        "v_mfma_f32_16x16x32_bf16 a[160:163], v[32:35], v[8:11], a[160:163]\n"
        "v_cvt_pk_bf16_f32 v191, 0, v191\n"
        "v_and_b32 v190, s35, v119\n"
        "v_mfma_f32_16x16x32_bf16 a[164:167], v[36:39], v[8:11], a[164:167]\n"
        "v_and_b32 v192, s35, v121\n"
        "v_mul_f32 v189, v189, v190\n"
        "v_mfma_f32_16x16x32_bf16 a[164:167], v[68:71], v[12:15], a[164:167]\n"
        "v_mul_f32 v191, v191, v192\n"
        "v_cvt_pk_bf16_f32 v211, v191, v189\n"
        "v_mfma_f32_16x16x32_bf16 a[132:135], v[68:71], v[4:7], a[132:135]\n"
        "global_load_lds_dwordx4 v240, s[86:87] offset:2048\n"
        "v_mfma_f32_16x16x32_bf16 a[132:135], v[36:39], v[0:3], a[132:135]\n"
        "v_mul_f32 v193, v176, v188\n"
        "v_mul_f32 v195, v177, v188\n"
        "v_mfma_f32_16x16x32_bf16 a[136:139], v[40:43], v[0:3], a[136:139]\n"
        "v_cvt_pk_bf16_f32 v193, 0, v193\n"
        "v_cvt_pk_bf16_f32 v195, 0, v195\n"
        "v_mfma_f32_16x16x32_bf16 a[136:139], v[72:75], v[4:7], a[136:139]\n"
        "v_mul_f32 v194, v177, v187\n"
        "v_mul_f32 v196, v178, v187\n"
        "v_mfma_f32_16x16x32_bf16 a[168:171], v[72:75], v[12:15], a[168:171]\n"
        "v_cvt_pk_bf16_f32 v194, 0, v194\n"
        "v_cvt_pk_bf16_f32 v196, 0, v196\n"
        "v_mfma_f32_16x16x32_bf16 a[168:171], v[40:43], v[8:11], a[168:171]\n"
        "v_add_f32 v193, v193, v194\n"
        "v_add_f32 v195, v195, v196\n"
        "v_mfma_f32_16x16x32_bf16 a[172:175], v[44:47], v[8:11], a[172:175]\n"
        "global_load_lds_dwordx4 v241, s[86:87] offset:3072\n"
        "s_add_u32 m0, s72, 0x8000\n"
        "v_mfma_f32_16x16x32_bf16 a[172:175], v[76:79], v[12:15], a[172:175]\n"
        "s_add_u32 s86, s56, 0x180\n"
        "s_addc_u32 s87, s57, 0\n"
        "v_cvt_pk_bf16_f32 v193, 0, v193\n"
        "v_mfma_f32_16x16x32_bf16 a[140:143], v[76:79], v[4:7], a[140:143]\n"
        "v_cvt_pk_bf16_f32 v195, 0, v195\n"
        "v_mul_f32 v194, v178, v186\n"
        "v_mfma_f32_16x16x32_bf16 a[140:143], v[44:47], v[0:3], a[140:143]\n"
        "v_mul_f32 v196, v179, v186\n"
        "v_cvt_pk_bf16_f32 v194, 0, v194\n"
        "v_mfma_f32_16x16x32_bf16 a[144:147], v[0:3], v[48:51], a[144:147]\n"
        "v_cvt_pk_bf16_f32 v196, 0, v196\n"
        "v_add_f32 v193, v193, v194\n"
        "v_mfma_f32_16x16x32_bf16 a[144:147], v[4:7], v[80:83], a[144:147]\n"
        "v_add_f32 v195, v195, v196\n"
        "v_cvt_pk_bf16_f32 v193, 0, v193\n"
        "v_mfma_f32_16x16x32_bf16 a[176:179], v[12:15], v[80:83], a[176:179]\n"
        "v_cvt_pk_bf16_f32 v195, 0, v195\n"
        "v_mul_f32 v194, v179, v185\n"
        "v_mfma_f32_16x16x32_bf16 a[176:179], v[8:11], v[48:51], a[176:179]\n"
        "v_mul_f32 v196, v180, v185\n"
        "v_cvt_pk_bf16_f32 v194, 0, v194\n"
        "v_mfma_f32_16x16x32_bf16 a[180:183], v[8:11], v[52:55], a[180:183]\n"
        "v_cvt_pk_bf16_f32 v196, 0, v196\n"
        "v_add_f32 v193, v193, v194\n"
        "v_mfma_f32_16x16x32_bf16 a[180:183], v[12:15], v[84:87], a[180:183]\n"
        "v_add_f32 v195, v195, v196\n"
        "v_cvt_pk_bf16_f32 v193, 0, v193\n"
        "v_mfma_f32_16x16x32_bf16 a[148:151], v[4:7], v[84:87], a[148:151]\n"
        "v_cvt_pk_bf16_f32 v195, 0, v195\n"
        "v_and_b32 v194, s35, v123\n"
        "v_mfma_f32_16x16x32_bf16 a[148:151], v[0:3], v[52:55], a[148:151]\n"
        "v_and_b32 v196, s35, v125\n"
        "v_mul_f32 v193, v193, v194\n"
        "v_mfma_f32_16x16x32_bf16 a[152:155], v[0:3], v[56:59], a[152:155]\n"
        "v_mul_f32 v195, v195, v196\n"
        "v_cvt_pk_bf16_f32 v210, v195, v193\n"
        "v_mfma_f32_16x16x32_bf16 a[152:155], v[4:7], v[88:91], a[152:155]\n"
        "v_mul_f32 v189, v178, v188\n"
        "v_mul_f32 v191, v179, v188\n"
        "v_mfma_f32_16x16x32_bf16 a[184:187], v[12:15], v[88:91], a[184:187]\n"
        "v_cvt_pk_bf16_f32 v189, 0, v189\n"
        "v_cvt_pk_bf16_f32 v191, 0, v191\n"
        "v_mfma_f32_16x16x32_bf16 a[184:187], v[8:11], v[56:59], a[184:187]\n"
        "s_waitcnt vmcnt(11) lgkmcnt(0)\n"
        "v_mul_f32 v190, v179, v187\n"
        "v_mul_f32 v192, v180, v187\n"
        "v_mfma_f32_16x16x32_bf16 a[188:191], v[8:11], v[60:63], a[188:191]\n"
        "s_barrier\n"
        "v_mfma_f32_16x16x32_bf16 a[188:191], v[12:15], v[92:95], a[188:191]\n"
        "global_load_lds_dwordx4 v234, s[86:87] offset:0\n"
        "ds_read_b128 v[8:11], v224 offset:18432\n"
        "v_mfma_f32_16x16x32_bf16 a[156:159], v[4:7], v[92:95], a[156:159]\n"
        "ds_read_b128 v[4:7], v225 offset:16384\n"
        "v_cvt_pk_bf16_f32 v190, 0, v190\n"
        "v_mfma_f32_16x16x32_bf16 a[156:159], v[0:3], v[60:63], a[156:159]\n"
        "ds_read_b128 v[0:3], v224 offset:16384\n"
        "v_cvt_pk_bf16_f32 v192, 0, v192\n"
        "v_mfma_f32_16x16x32_bf16 a[192:195], v[32:35], v[16:19], a[192:195]\n"
        "ds_read_b128 v[12:15], v225 offset:18432\n"
        "v_add_f32 v189, v189, v190\n"
        "v_mfma_f32_16x16x32_bf16 a[192:195], v[64:67], v[20:23], a[192:195]\n"
        "global_load_lds_dwordx4 v235, s[86:87] offset:1024\n"
        "v_add_f32 v191, v191, v192\n"
        "v_mfma_f32_16x16x32_bf16 a[224:227], v[64:67], v[28:31], a[224:227]\n"
        "ds_read_b64_tr_b16 v[64:65], v226 offset:8192\n"
        "v_cvt_pk_bf16_f32 v189, 0, v189\n"
        "v_mfma_f32_16x16x32_bf16 a[224:227], v[32:35], v[24:27], a[224:227]\n"
        "ds_read_b64_tr_b16 v[32:33], v226 offset:0\n"
        "v_cvt_pk_bf16_f32 v191, 0, v191\n"
        "v_mfma_f32_16x16x32_bf16 a[228:231], v[36:39], v[24:27], a[228:231]\n"
        "ds_read_b64_tr_b16 v[34:35], v227 offset:0\n"
        "v_mul_f32 v190, v180, v186\n"
        "v_mfma_f32_16x16x32_bf16 a[228:231], v[68:71], v[28:31], a[228:231]\n"
        "ds_read_b64_tr_b16 v[66:67], v227 offset:8192\n"
        "global_load_lds_dwordx4 v236, s[86:87] offset:2048\n"
        "v_mfma_f32_16x16x32_bf16 a[196:199], v[68:71], v[20:23], a[196:199]\n"
        "ds_read_b64_tr_b16 v[68:69], v228 offset:8192\n"
        "v_mul_f32 v192, v181, v186\n"
        "v_mfma_f32_16x16x32_bf16 a[196:199], v[36:39], v[16:19], a[196:199]\n"
        "ds_read_b64_tr_b16 v[36:37], v228 offset:0\n"
        "v_cvt_pk_bf16_f32 v190, 0, v190\n"
        "v_mfma_f32_16x16x32_bf16 a[200:203], v[40:43], v[16:19], a[200:203]\n"
        "ds_read_b64_tr_b16 v[38:39], v229 offset:0\n"
        "v_cvt_pk_bf16_f32 v192, 0, v192\n"
        "v_mfma_f32_16x16x32_bf16 a[200:203], v[72:75], v[20:23], a[200:203]\n"
        "ds_read_b64_tr_b16 v[70:71], v229 offset:8192\n"
        "global_load_lds_dwordx4 v237, s[86:87] offset:3072\n"
        "s_add_u32 m0, s72, 0x9000\n"
        "v_mfma_f32_16x16x32_bf16 a[232:235], v[72:75], v[28:31], a[232:235]\n"
        "ds_read_b64_tr_b16 v[72:73], v230 offset:8192\n"
        "s_add_u32 s86, s58, 0x180\n"
        "s_addc_u32 s87, s59, 0\n"
        "v_mfma_f32_16x16x32_bf16 a[232:235], v[40:43], v[24:27], a[232:235]\n"
        "ds_read_b64_tr_b16 v[40:41], v230 offset:0\n"
        "v_add_f32 v189, v189, v190\n"
        "v_mfma_f32_16x16x32_bf16 a[236:239], v[44:47], v[24:27], a[236:239]\n"
        "ds_read_b64_tr_b16 v[42:43], v231 offset:0\n"
        "v_add_f32 v191, v191, v192\n"
        "v_mfma_f32_16x16x32_bf16 a[236:239], v[76:79], v[28:31], a[236:239]\n"
        "ds_read_b64_tr_b16 v[74:75], v231 offset:8192\n"
        "global_load_lds_dwordx4 v234, s[86:87] offset:0\n"
        "v_mfma_f32_16x16x32_bf16 a[204:207], v[76:79], v[20:23], a[204:207]\n"
        "ds_read_b64_tr_b16 v[76:77], v232 offset:8192\n"
        "v_cvt_pk_bf16_f32 v189, 0, v189\n"
        "v_mfma_f32_16x16x32_bf16 a[204:207], v[44:47], v[16:19], a[204:207]\n"
        "ds_read_b64_tr_b16 v[44:45], v232 offset:0\n"
        "v_cvt_pk_bf16_f32 v191, 0, v191\n"
        "v_mfma_f32_16x16x32_bf16 a[208:211], v[16:19], v[48:51], a[208:211]\n"
        "ds_read_b64_tr_b16 v[46:47], v233 offset:0\n"
        "v_mul_f32 v190, v181, v185\n"
        "v_mfma_f32_16x16x32_bf16 a[208:211], v[20:23], v[80:83], a[208:211]\n"
        "ds_read_b64_tr_b16 v[78:79], v233 offset:8192\n"
        "global_load_lds_dwordx4 v235, s[86:87] offset:1024\n"
        "v_mfma_f32_16x16x32_bf16 a[240:243], v[28:31], v[80:83], a[240:243]\n"
        "ds_read_b64_tr_b16 v[80:81], v226 offset:24576\n"
        "v_mul_f32 v192, v182, v185\n"
        "v_mfma_f32_16x16x32_bf16 a[240:243], v[24:27], v[48:51], a[240:243]\n"
        "ds_read_b64_tr_b16 v[48:49], v226 offset:16384\n"
        "v_cvt_pk_bf16_f32 v190, 0, v190\n"
        "v_mfma_f32_16x16x32_bf16 a[244:247], v[24:27], v[52:55], a[244:247]\n"
        "ds_read_b64_tr_b16 v[50:51], v227 offset:16384\n"
        "v_cvt_pk_bf16_f32 v192, 0, v192\n"
        "v_mfma_f32_16x16x32_bf16 a[244:247], v[28:31], v[84:87], a[244:247]\n"
        "ds_read_b64_tr_b16 v[82:83], v227 offset:24576\n"
        "global_load_lds_dwordx4 v236, s[86:87] offset:2048\n"
        "v_mfma_f32_16x16x32_bf16 a[212:215], v[20:23], v[84:87], a[212:215]\n"
        "ds_read_b64_tr_b16 v[84:85], v228 offset:24576\n"
        "v_add_f32 v189, v189, v190\n"
        "v_mfma_f32_16x16x32_bf16 a[212:215], v[16:19], v[52:55], a[212:215]\n"
        "ds_read_b64_tr_b16 v[52:53], v228 offset:16384\n"
        "v_add_f32 v191, v191, v192\n"
        "v_mfma_f32_16x16x32_bf16 a[216:219], v[16:19], v[56:59], a[216:219]\n"
        "ds_read_b64_tr_b16 v[54:55], v229 offset:16384\n"
        "v_cvt_pk_bf16_f32 v189, 0, v189\n"
        "v_mfma_f32_16x16x32_bf16 a[216:219], v[20:23], v[88:91], a[216:219]\n"
        "ds_read_b64_tr_b16 v[86:87], v229 offset:24576\n"
        "global_load_lds_dwordx4 v237, s[86:87] offset:3072\n"
        "s_add_u32 m0, s73, 0x20000\n"
        "v_mfma_f32_16x16x32_bf16 a[248:251], v[28:31], v[88:91], a[248:251]\n"
        "ds_read_b64_tr_b16 v[88:89], v230 offset:24576\n"
        "s_add_u32 s86, s68, 0x80000\n"
        "s_addc_u32 s87, s69, 0\n"
        "v_mfma_f32_16x16x32_bf16 a[248:251], v[24:27], v[56:59], a[248:251]\n"
        "ds_read_b64_tr_b16 v[56:57], v230 offset:16384\n"
        "v_cvt_pk_bf16_f32 v191, 0, v191\n"
        "v_mfma_f32_16x16x32_bf16 a[252:255], v[24:27], v[60:63], a[252:255]\n"
        "ds_read_b64_tr_b16 v[58:59], v231 offset:16384\n"
        "v_and_b32 v190, s35, v127\n"
        "v_mfma_f32_16x16x32_bf16 a[252:255], v[28:31], v[92:95], a[252:255]\n"
        "ds_read_b64_tr_b16 v[90:91], v231 offset:24576\n"
        "v_and_b32 v192, s35, v129\n"
        "v_mfma_f32_16x16x32_bf16 a[220:223], v[20:23], v[92:95], a[220:223]\n"
        "ds_read_b64_tr_b16 v[92:93], v232 offset:24576\n"
        "v_mul_f32 v189, v189, v190\n"
        "v_mfma_f32_16x16x32_bf16 a[220:223], v[16:19], v[60:63], a[220:223]\n"
        "ds_read_b64_tr_b16 v[60:61], v232 offset:16384\n"
        "s_waitcnt lgkmcnt(8)\n"
        "v_mfma_f32_16x16x32_bf16 a[0:3], v[32:35], v[0:3], a[0:3]\n"
        "ds_read_b64_tr_b16 v[62:63], v233 offset:16384\n"
        "v_mul_f32 v191, v191, v192\n"
        "v_mfma_f32_16x16x32_bf16 a[0:3], v[64:67], v[4:7], a[0:3]\n"
        "ds_read_b64_tr_b16 v[94:95], v233 offset:24576\n"
        "v_cvt_pk_bf16_f32 v209, v191, v189\n"
        "v_mfma_f32_16x16x32_bf16 a[32:35], v[64:67], v[12:15], a[32:35]\n"
        "ds_read_b128 v[16:19], v224 offset:20480\n"
        "v_mul_f32 v193, v180, v188\n"
        "v_mfma_f32_16x16x32_bf16 a[32:35], v[32:35], v[8:11], a[32:35]\n"
        "ds_read_b128 v[20:23], v225 offset:20480\n"
        "v_mul_f32 v195, v181, v188\n"
        "v_mfma_f32_16x16x32_bf16 a[36:39], v[36:39], v[8:11], a[36:39]\n"
        "ds_read_b128 v[24:27], v224 offset:22528\n"
        "v_cvt_pk_bf16_f32 v193, 0, v193\n"
        "v_mfma_f32_16x16x32_bf16 a[36:39], v[68:71], v[12:15], a[36:39]\n"
        "ds_read_b128 v[28:31], v225 offset:22528\n"
        "v_cvt_pk_bf16_f32 v195, 0, v195\n"
        "v_mfma_f32_16x16x32_bf16 a[4:7], v[68:71], v[4:7], a[4:7]\n"
        "v_mul_f32 v194, v181, v187\n"
        "v_mul_f32 v196, v182, v187\n"
        "v_mfma_f32_16x16x32_bf16 a[4:7], v[36:39], v[0:3], a[4:7]\n"
        "v_cvt_pk_bf16_f32 v194, 0, v194\n"
        "v_cvt_pk_bf16_f32 v196, 0, v196\n"
        "v_mfma_f32_16x16x32_bf16 a[8:11], v[40:43], v[0:3], a[8:11]\n"
        "v_add_f32 v193, v193, v194\n"
        "v_add_f32 v195, v195, v196\n"
        "v_mfma_f32_16x16x32_bf16 a[8:11], v[72:75], v[4:7], a[8:11]\n"
        "v_cvt_pk_bf16_f32 v193, 0, v193\n"
        "v_cvt_pk_bf16_f32 v195, 0, v195\n"
        "v_mfma_f32_16x16x32_bf16 a[40:43], v[72:75], v[12:15], a[40:43]\n"
        "v_mul_f32 v194, v182, v186\n"
        "v_mul_f32 v196, v183, v186\n"
        "v_mfma_f32_16x16x32_bf16 a[40:43], v[40:43], v[8:11], a[40:43]\n"
        "v_cvt_pk_bf16_f32 v194, 0, v194\n"
        "v_cvt_pk_bf16_f32 v196, 0, v196\n"
        "v_mfma_f32_16x16x32_bf16 a[44:47], v[44:47], v[8:11], a[44:47]\n"
        "v_add_f32 v193, v193, v194\n"
        "v_add_f32 v195, v195, v196\n"
        "v_mfma_f32_16x16x32_bf16 a[44:47], v[76:79], v[12:15], a[44:47]\n"
        "v_cvt_pk_bf16_f32 v193, 0, v193\n"
        "v_cvt_pk_bf16_f32 v195, 0, v195\n"
        "v_mfma_f32_16x16x32_bf16 a[12:15], v[76:79], v[4:7], a[12:15]\n"
        "v_mul_f32 v194, v183, v185\n"
        "v_mul_f32 v196, v184, v185\n"
        "v_mfma_f32_16x16x32_bf16 a[12:15], v[44:47], v[0:3], a[12:15]\n"
        "global_load_dword v253, v173, s[82:83] offset:0\n"
        "s_add_u32 s82, s12, 0x700000\n"
        "s_addc_u32 s83, s13, 0\n"
        "v_mfma_f32_16x16x32_bf16 a[16:19], v[0:3], v[48:51], a[16:19]\n"
        "s_waitcnt lgkmcnt(0)\n"
        "v_cvt_pk_bf16_f32 v194, 0, v194\n"
        "v_cvt_pk_bf16_f32 v196, 0, v196\n"
        "v_mfma_f32_16x16x32_bf16 a[16:19], v[4:7], v[80:83], a[16:19]\n"
        "v_add_f32 v193, v193, v194\n"
        "v_add_f32 v195, v195, v196\n"
        "v_mfma_f32_16x16x32_bf16 a[48:51], v[12:15], v[80:83], a[48:51]\n"
        "v_cvt_pk_bf16_f32 v193, 0, v193\n"
        "v_cvt_pk_bf16_f32 v195, 0, v195\n"
        "v_mfma_f32_16x16x32_bf16 a[48:51], v[8:11], v[48:51], a[48:51]\n"
        "v_and_b32 v194, s35, v131\n"
        "v_and_b32 v196, s35, v133\n"
        "v_mfma_f32_16x16x32_bf16 a[52:55], v[8:11], v[52:55], a[52:55]\n"
        "v_mul_f32 v193, v193, v194\n"
        "v_mul_f32 v195, v195, v196\n"
        "v_mfma_f32_16x16x32_bf16 a[52:55], v[12:15], v[84:87], a[52:55]\n"
        "v_cvt_pk_bf16_f32 v208, v195, v193\n"
        "v_readfirstlane_b32 s70, v250\n"
        "v_mfma_f32_16x16x32_bf16 a[20:23], v[4:7], v[84:87], a[20:23]\n"
        "global_store_dwordx4 v171, v[208:211], s[76:77] offset:0 sc1\n"
        "s_add_u32 s76, s38, 0x3b20040\n"
        "s_addc_u32 s77, s39, 0\n"
        "v_readfirstlane_b32 s71, v251\n"
        "v_lshlrev_b32 v174, 16, v158\n"
        "v_and_b32 v175, s35, v158\n"
        "v_mfma_f32_16x16x32_bf16 a[20:23], v[0:3], v[52:55], a[20:23]\n"
        "s_add_u32 s70, s70, 0x1\n"
        "s_addc_u32 s71, s71, 0\n"
        "v_mov_b32 v250, s70\n"
        "v_mfma_f32_16x16x32_bf16 a[24:27], v[0:3], v[56:59], a[24:27]\n"
        "s_and_b32 s53, s40, 1\n"
        "v_mov_b32 v251, s71\n"
        "v_lshlrev_b32 v176, 16, v159\n"
        "v_mfma_f32_16x16x32_bf16 a[24:27], v[4:7], v[88:91], a[24:27]\n"
        "s_lshl_b32 s53, s53, 10\n"
        "v_and_b32 v177, s35, v159\n"
        "v_lshlrev_b32 v178, 16, v160\n"
        "v_mfma_f32_16x16x32_bf16 a[56:59], v[12:15], v[88:91], a[56:59]\n"
        "s_sub_u32 s53, 0x8000, s53\n"
        "v_and_b32 v179, s35, v160\n"
        "v_lshlrev_b32 v180, 16, v161\n"
        "v_mfma_f32_16x16x32_bf16 a[56:59], v[8:11], v[56:59], a[56:59]\n"
        "v_and_b32 v181, s35, v161\n"
        "v_lshlrev_b32 v182, 16, v162\n"
        "v_mfma_f32_16x16x32_bf16 a[60:63], v[8:11], v[60:63], a[60:63]\n"
        "ds_read_b128 v[8:11], v224 offset:2048\n"
        "v_and_b32 v183, s35, v162\n"
        "v_mfma_f32_16x16x32_bf16 a[60:63], v[12:15], v[92:95], a[60:63]\n"
        "ds_read_b128 v[12:15], v225 offset:2048\n"
        "v_lshlrev_b32 v184, 16, v163\n"
        "v_mfma_f32_16x16x32_bf16 a[28:31], v[4:7], v[92:95], a[28:31]\n"
        "ds_read_b128 v[4:7], v225 offset:0\n"
        "v_and_b32 v185, s35, v163\n"
        "v_mfma_f32_16x16x32_bf16 a[28:31], v[0:3], v[60:63], a[28:31]\n"
        "global_load_lds_dwordx4 v238, s[68:69] offset:0\n"
        "ds_read_b128 v[0:3], v224 offset:0\n"
        "v_mfma_f32_16x16x32_bf16 a[64:67], v[32:35], v[16:19], a[64:67]\n"
        "v_lshlrev_b32 v186, 16, v164\n"
        "v_and_b32 v187, s35, v164\n"
        "v_mfma_f32_16x16x32_bf16 a[64:67], v[64:67], v[20:23], a[64:67]\n"
        "v_lshlrev_b32 v188, 16, v165\n"
        "v_and_b32 v189, s35, v165\n"
        "v_mfma_f32_16x16x32_bf16 a[96:99], v[64:67], v[28:31], a[96:99]\n"
        "v_lshlrev_b32 v192, 16, v142\n"
        "v_and_b32 v193, s35, v142\n"
        "v_mfma_f32_16x16x32_bf16 a[96:99], v[32:35], v[24:27], a[96:99]\n"
        "v_mul_f32 v192, v192, v174\n"
        "v_mul_f32 v193, v193, v175\n"
        "v_mfma_f32_16x16x32_bf16 a[100:103], v[36:39], v[24:27], a[100:103]\n"
        "v_cvt_pk_bf16_f32 v192, 0, v192\n"
        "v_cvt_pk_bf16_f32 v193, 0, v193\n"
        "v_mfma_f32_16x16x32_bf16 a[100:103], v[68:71], v[28:31], a[100:103]\n"
        "global_load_lds_dwordx4 v239, s[68:69] offset:1024\n"
        "v_add_f32 v190, v192, v193\n"
        "v_mfma_f32_16x16x32_bf16 a[68:71], v[68:71], v[20:23], a[68:71]\n"
        "v_lshlrev_b32 v194, 16, v143\n"
        "v_and_b32 v195, s35, v143\n"
        "v_mfma_f32_16x16x32_bf16 a[68:71], v[36:39], v[16:19], a[68:71]\n"
        "v_mul_f32 v194, v194, v176\n"
        "v_mul_f32 v195, v195, v177\n"
        "v_mfma_f32_16x16x32_bf16 a[72:75], v[40:43], v[16:19], a[72:75]\n"
        "v_cvt_pk_bf16_f32 v194, 0, v194\n"
        "v_cvt_pk_bf16_f32 v195, 0, v195\n"
        "v_mfma_f32_16x16x32_bf16 a[72:75], v[72:75], v[20:23], a[72:75]\n"
        "v_add_f32 v194, v194, v195\n"
        "v_add_f32 v190, v190, v194\n"
        "v_mfma_f32_16x16x32_bf16 a[104:107], v[72:75], v[28:31], a[104:107]\n"
        "v_lshlrev_b32 v192, 16, v144\n"
        "v_and_b32 v193, s35, v144\n"
        "v_mfma_f32_16x16x32_bf16 a[104:107], v[40:43], v[24:27], a[104:107]\n"
        "global_load_lds_dwordx4 v240, s[68:69] offset:2048\n"
        "v_mul_f32 v192, v192, v178\n"
        "v_mfma_f32_16x16x32_bf16 a[108:111], v[44:47], v[24:27], a[108:111]\n"
        "v_mul_f32 v193, v193, v179\n"
        "v_cvt_pk_bf16_f32 v192, 0, v192\n"
        "v_mfma_f32_16x16x32_bf16 a[108:111], v[76:79], v[28:31], a[108:111]\n"
        "v_cvt_pk_bf16_f32 v193, 0, v193\n"
        "v_add_f32 v192, v192, v193\n"
        "v_mfma_f32_16x16x32_bf16 a[76:79], v[76:79], v[20:23], a[76:79]\n"
        "v_add_f32 v190, v190, v192\n"
        "v_lshlrev_b32 v194, 16, v145\n"
        "v_mfma_f32_16x16x32_bf16 a[76:79], v[44:47], v[16:19], a[76:79]\n"
        "v_and_b32 v195, s35, v145\n"
        "v_mul_f32 v194, v194, v180\n"
        "v_mfma_f32_16x16x32_bf16 a[80:83], v[16:19], v[48:51], a[80:83]\n"
        "v_mul_f32 v195, v195, v181\n"
        "v_cvt_pk_bf16_f32 v194, 0, v194\n"
        "v_mfma_f32_16x16x32_bf16 a[80:83], v[20:23], v[80:83], a[80:83]\n"
        "global_load_lds_dwordx4 v241, s[68:69] offset:3072\n"
        "s_add_u32 m0, s73, 0x24000\n"
        "v_cvt_pk_bf16_f32 v195, 0, v195\n"
        "v_mfma_f32_16x16x32_bf16 a[112:115], v[28:31], v[80:83], a[112:115]\n"
        "v_add_f32 v194, v194, v195\n"
        "v_add_f32 v190, v190, v194\n"
        "v_mfma_f32_16x16x32_bf16 a[112:115], v[24:27], v[48:51], a[112:115]\n"
        "v_lshlrev_b32 v192, 16, v146\n"
        "v_and_b32 v193, s35, v146\n"
        "v_mfma_f32_16x16x32_bf16 a[116:119], v[24:27], v[52:55], a[116:119]\n"
        "LHA3_%=:\n"
        "s_cbranch_execz LHA7_%=\n"
        "v_mul_f32 v192, v192, v182\n"
        "v_mul_f32 v193, v193, v183\n"
        "v_mfma_f32_16x16x32_bf16 a[116:119], v[28:31], v[84:87], a[116:119]\n"
        "v_cvt_pk_bf16_f32 v192, 0, v192\n"
        "v_cvt_pk_bf16_f32 v193, 0, v193\n"
        "v_mfma_f32_16x16x32_bf16 a[84:87], v[20:23], v[84:87], a[84:87]\n"
        "v_add_f32 v192, v192, v193\n"
        "v_add_f32 v190, v190, v192\n"
        "v_mfma_f32_16x16x32_bf16 a[84:87], v[16:19], v[52:55], a[84:87]\n"
        "global_load_lds_dwordx4 v238, s[86:87] offset:0\n"
        "s_mov_b32 s88, 0x3e0293ee\n"
        "v_lshlrev_b32 v194, 16, v147\n"
        "v_mfma_f32_16x16x32_bf16 a[88:91], v[16:19], v[56:59], a[88:91]\n"
        "s_sub_u32 s75, s45, s44\n"
        "v_and_b32 v195, s35, v147\n"
        "v_mul_f32 v194, v194, v184\n"
        "v_mfma_f32_16x16x32_bf16 a[88:91], v[20:23], v[88:91], a[88:91]\n"
        "s_lshl_b32 s75, s75, 15\n"
        "v_mul_f32 v195, v195, v185\n"
        "v_cvt_pk_bf16_f32 v194, 0, v194\n"
        "v_mfma_f32_16x16x32_bf16 a[120:123], v[28:31], v[88:91], a[120:123]\n"
        "s_lshr_b32 s50, s40, 1\n"
        "v_cvt_pk_bf16_f32 v195, 0, v195\n"
        "v_add_f32 v194, v194, v195\n"
        "v_mfma_f32_16x16x32_bf16 a[120:123], v[24:27], v[56:59], a[120:123]\n"
        "s_lshl1_add_u32 s50, s44, s50\n"
        "v_add_f32 v190, v190, v194\n"
        "v_lshlrev_b32 v192, 16, v148\n"
        "v_mfma_f32_16x16x32_bf16 a[124:127], v[24:27], v[60:63], a[124:127]\n"
        "ds_read_b128 v[24:27], v224 offset:6144\n"
        "s_lshl_b32 s50, s50, 14\n"
        "v_and_b32 v193, s35, v148\n"
        "v_mfma_f32_16x16x32_bf16 a[124:127], v[28:31], v[92:95], a[124:127]\n"
        "global_load_lds_dwordx4 v239, s[86:87] offset:1024\n"
        "ds_read_b128 v[28:31], v225 offset:6144\n"
        "s_mul_i32 s51, s43, 529408\n"
        "v_mfma_f32_16x16x32_bf16 a[92:95], v[20:23], v[92:95], a[92:95]\n"
        "ds_read_b128 v[20:23], v225 offset:4096\n"
        "s_add_u32 s50, s50, s51\n"
        "v_mul_f32 v192, v192, v186\n"
        "v_mfma_f32_16x16x32_bf16 a[92:95], v[16:19], v[60:63], a[92:95]\n"
        "ds_read_b128 v[16:19], v224 offset:4096\n"
        "s_and_b32 s51, s40, 1\n"
        "s_waitcnt lgkmcnt(4)\n"
        "v_mfma_f32_16x16x32_bf16 a[128:131], v[32:35], v[0:3], a[128:131]\n"
        "s_lshl_b32 s51, s51, 11\n"
        "v_mul_f32 v193, v193, v187\n"
        "v_cvt_pk_bf16_f32 v192, 0, v192\n"
        "v_mfma_f32_16x16x32_bf16 a[128:131], v[64:67], v[4:7], a[128:131]\n"
        "s_add_u32 s52, s50, s51\n"
        "v_cvt_pk_bf16_f32 v193, 0, v193\n"
        "v_add_f32 v192, v192, v193\n"
        "v_mfma_f32_16x16x32_bf16 a[160:163], v[64:67], v[12:15], a[160:163]\n"
        "v_add_f32 v190, v190, v192\n"
        "v_lshlrev_b32 v194, 16, v149\n"
        "v_mfma_f32_16x16x32_bf16 a[160:163], v[32:35], v[8:11], a[160:163]\n"
        "global_load_lds_dwordx4 v240, s[86:87] offset:2048\n"
        "v_and_b32 v195, s35, v149\n"
        "v_mfma_f32_16x16x32_bf16 a[164:167], v[36:39], v[8:11], a[164:167]\n"
        "v_mul_f32 v194, v194, v188\n"
        "v_mul_f32 v195, v195, v189\n"
        "v_mfma_f32_16x16x32_bf16 a[164:167], v[68:71], v[12:15], a[164:167]\n"
        "v_cvt_pk_bf16_f32 v194, 0, v194\n"
        "v_cvt_pk_bf16_f32 v195, 0, v195\n"
        "v_mfma_f32_16x16x32_bf16 a[132:135], v[68:71], v[4:7], a[132:135]\n"
        "v_add_f32 v194, v194, v195\n"
        "v_add_f32 v190, v190, v194\n"
        "v_mfma_f32_16x16x32_bf16 a[132:135], v[36:39], v[0:3], a[132:135]\n"
        "v_lshlrev_b32 v192, 16, v150\n"
        "v_and_b32 v193, s35, v150\n"
        "v_mfma_f32_16x16x32_bf16 a[136:139], v[40:43], v[0:3], a[136:139]\n"
        "v_mul_f32 v192, v192, v174\n"
        "v_mul_f32 v193, v193, v175\n"
        "v_mfma_f32_16x16x32_bf16 a[136:139], v[72:75], v[4:7], a[136:139]\n"
        "global_load_lds_dwordx4 v241, s[86:87] offset:3072\n"
        "s_add_u32 m0, s72, 0x0\n"
        "v_cvt_pk_bf16_f32 v192, 0, v192\n"
        "v_mfma_f32_16x16x32_bf16 a[168:171], v[72:75], v[12:15], a[168:171]\n"
        "s_add_u32 s86, s64, 0x80\n"
        "s_addc_u32 s87, s65, 0\n"
        "v_cvt_pk_bf16_f32 v193, 0, v193\n"
        "v_mfma_f32_16x16x32_bf16 a[168:171], v[40:43], v[8:11], a[168:171]\n"
        "v_add_f32 v191, v192, v193\n"
        "v_lshlrev_b32 v194, 16, v151\n"
        "v_mfma_f32_16x16x32_bf16 a[172:175], v[44:47], v[8:11], a[172:175]\n"
        "v_and_b32 v195, s35, v151\n"
        "v_mul_f32 v194, v194, v176\n"
        "v_mfma_f32_16x16x32_bf16 a[172:175], v[76:79], v[12:15], a[172:175]\n"
        "v_mul_f32 v195, v195, v177\n"
        "v_cvt_pk_bf16_f32 v194, 0, v194\n"
        "v_mfma_f32_16x16x32_bf16 a[140:143], v[76:79], v[4:7], a[140:143]\n"
        "v_cvt_pk_bf16_f32 v195, 0, v195\n"
        "v_add_f32 v194, v194, v195\n"
        "v_mfma_f32_16x16x32_bf16 a[140:143], v[44:47], v[0:3], a[140:143]\n"
        "v_add_f32 v191, v191, v194\n"
        "v_lshlrev_b32 v192, 16, v152\n"
        "v_mfma_f32_16x16x32_bf16 a[144:147], v[0:3], v[48:51], a[144:147]\n"
        "v_and_b32 v193, s35, v152\n"
        "v_mul_f32 v192, v192, v178\n"
        "v_mfma_f32_16x16x32_bf16 a[144:147], v[4:7], v[80:83], a[144:147]\n"
        "v_mul_f32 v193, v193, v179\n"
        "v_cvt_pk_bf16_f32 v192, 0, v192\n"
        "v_mfma_f32_16x16x32_bf16 a[176:179], v[12:15], v[80:83], a[176:179]\n"
        "v_cvt_pk_bf16_f32 v193, 0, v193\n"
        "v_add_f32 v192, v192, v193\n"
        "v_mfma_f32_16x16x32_bf16 a[176:179], v[8:11], v[48:51], a[176:179]\n"
        "s_waitcnt vmcnt(28) lgkmcnt(0)\n"
        "v_add_f32 v191, v191, v192\n"
        "v_lshlrev_b32 v194, 16, v153\n"
        "v_mfma_f32_16x16x32_bf16 a[180:183], v[8:11], v[52:55], a[180:183]\n"
        "v_and_b32 v195, s35, v153\n"
        "v_mul_f32 v194, v194, v180\n"
        "v_mfma_f32_16x16x32_bf16 a[180:183], v[12:15], v[84:87], a[180:183]\n"
        "v_mul_f32 v195, v195, v181\n"
        "v_cvt_pk_bf16_f32 v194, 0, v194\n"
        "v_mfma_f32_16x16x32_bf16 a[148:151], v[4:7], v[84:87], a[148:151]\n"
        "v_cvt_pk_bf16_f32 v195, 0, v195\n"
        "v_add_f32 v194, v194, v195\n"
        "v_mfma_f32_16x16x32_bf16 a[148:151], v[0:3], v[52:55], a[148:151]\n"
        "v_add_f32 v191, v191, v194\n"
        "v_lshlrev_b32 v192, 16, v154\n"
        "v_mfma_f32_16x16x32_bf16 a[152:155], v[0:3], v[56:59], a[152:155]\n"
        "v_and_b32 v193, s35, v154\n"
        "v_mul_f32 v192, v192, v182\n"
        "v_mfma_f32_16x16x32_bf16 a[152:155], v[4:7], v[88:91], a[152:155]\n"
        "v_mul_f32 v193, v193, v183\n"
        "v_cvt_pk_bf16_f32 v192, 0, v192\n"
        "v_mfma_f32_16x16x32_bf16 a[184:187], v[12:15], v[88:91], a[184:187]\n"
        "v_cvt_pk_bf16_f32 v193, 0, v193\n"
        "v_add_f32 v192, v192, v193\n"
        "v_mfma_f32_16x16x32_bf16 a[184:187], v[8:11], v[56:59], a[184:187]\n"
        "s_waitcnt vmcnt(10)\n"
        "v_add_f32 v191, v191, v192\n"
        "v_lshlrev_b32 v194, 16, v155\n"
        "v_mfma_f32_16x16x32_bf16 a[188:191], v[8:11], v[60:63], a[188:191]\n"
        "s_barrier\n"
        "v_mfma_f32_16x16x32_bf16 a[188:191], v[12:15], v[92:95], a[188:191]\n"
        "global_load_lds_dwordx4 v242, s[64:65] offset:0\n"
        "ds_read_b128 v[8:11], v224 offset:51200\n"
        "v_mfma_f32_16x16x32_bf16 a[156:159], v[4:7], v[92:95], a[156:159]\n"
        "ds_read_b128 v[4:7], v225 offset:49152\n"
        "v_and_b32 v195, s35, v155\n"
        "v_mfma_f32_16x16x32_bf16 a[156:159], v[0:3], v[60:63], a[156:159]\n"
        "ds_read_b128 v[0:3], v224 offset:49152\n"
        "v_mul_f32 v194, v194, v184\n"
        "v_mfma_f32_16x16x32_bf16 a[192:195], v[32:35], v[16:19], a[192:195]\n"
        "ds_read_b128 v[12:15], v225 offset:51200\n"
        "v_mul_f32 v195, v195, v185\n"
        "v_mfma_f32_16x16x32_bf16 a[192:195], v[64:67], v[20:23], a[192:195]\n"
        "global_load_lds_dwordx4 v243, s[64:65] offset:1024\n"
        "v_cvt_pk_bf16_f32 v194, 0, v194\n"
        "v_mfma_f32_16x16x32_bf16 a[224:227], v[64:67], v[28:31], a[224:227]\n"
        "ds_read_b64_tr_b16 v[64:65], v226 offset:40960\n"
        "v_cvt_pk_bf16_f32 v195, 0, v195\n"
        "v_mfma_f32_16x16x32_bf16 a[224:227], v[32:35], v[24:27], a[224:227]\n"
        "ds_read_b64_tr_b16 v[32:33], v226 offset:32768\n"
        "s_add_u32 s96, s52, 0x3000\n"
        "v_add_f32 v194, v194, v195\n"
        "v_mfma_f32_16x16x32_bf16 a[228:231], v[36:39], v[24:27], a[228:231]\n"
        "ds_read_b64_tr_b16 v[34:35], v227 offset:32768\n"
        "v_add_f32 v191, v191, v194\n"
        "v_mfma_f32_16x16x32_bf16 a[228:231], v[68:71], v[28:31], a[228:231]\n"
        "ds_read_b64_tr_b16 v[66:67], v227 offset:40960\n"
        "global_load_lds_dwordx4 v244, s[64:65] offset:2048\n"
        "v_mfma_f32_16x16x32_bf16 a[196:199], v[68:71], v[20:23], a[196:199]\n"
        "ds_read_b64_tr_b16 v[68:69], v228 offset:40960\n"
        "v_lshlrev_b32 v192, 16, v156\n"
        "v_mfma_f32_16x16x32_bf16 a[196:199], v[36:39], v[16:19], a[196:199]\n"
        "ds_read_b64_tr_b16 v[36:37], v228 offset:32768\n"
        "v_and_b32 v193, s35, v156\n"
        "v_mfma_f32_16x16x32_bf16 a[200:203], v[40:43], v[16:19], a[200:203]\n"
        "ds_read_b64_tr_b16 v[38:39], v229 offset:32768\n"
        "v_mul_f32 v192, v192, v186\n"
        "v_mfma_f32_16x16x32_bf16 a[200:203], v[72:75], v[20:23], a[200:203]\n"
        "ds_read_b64_tr_b16 v[70:71], v229 offset:40960\n"
        "global_load_lds_dwordx4 v245, s[64:65] offset:3072\n"
        "s_add_u32 m0, s72, 0x1000\n"
        "v_mfma_f32_16x16x32_bf16 a[232:235], v[72:75], v[28:31], a[232:235]\n"
        "ds_read_b64_tr_b16 v[72:73], v230 offset:40960\n"
        "v_mul_f32 v193, v193, v187\n"
        "v_mfma_f32_16x16x32_bf16 a[232:235], v[40:43], v[24:27], a[232:235]\n"
        "ds_read_b64_tr_b16 v[40:41], v230 offset:32768\n"
        "v_cvt_pk_bf16_f32 v192, 0, v192\n"
        "v_mfma_f32_16x16x32_bf16 a[236:239], v[44:47], v[24:27], a[236:239]\n"
        "ds_read_b64_tr_b16 v[42:43], v231 offset:32768\n"
        "v_cvt_pk_bf16_f32 v193, 0, v193\n"
        "v_mfma_f32_16x16x32_bf16 a[236:239], v[76:79], v[28:31], a[236:239]\n"
        "ds_read_b64_tr_b16 v[74:75], v231 offset:40960\n"
        "global_load_lds_dwordx4 v242, s[66:67] offset:0\n"
        "v_mfma_f32_16x16x32_bf16 a[204:207], v[76:79], v[20:23], a[204:207]\n"
        "ds_read_b64_tr_b16 v[76:77], v232 offset:40960\n"
        "v_add_f32 v192, v192, v193\n"
        "v_mfma_f32_16x16x32_bf16 a[204:207], v[44:47], v[16:19], a[204:207]\n"
        "ds_read_b64_tr_b16 v[44:45], v232 offset:32768\n"
        "v_add_f32 v191, v191, v192\n"
        "v_mfma_f32_16x16x32_bf16 a[208:211], v[16:19], v[48:51], a[208:211]\n"
        "ds_read_b64_tr_b16 v[46:47], v233 offset:32768\n"
        "v_lshlrev_b32 v194, 16, v157\n"
        "v_mfma_f32_16x16x32_bf16 a[208:211], v[20:23], v[80:83], a[208:211]\n"
        "ds_read_b64_tr_b16 v[78:79], v233 offset:40960\n"
        "global_load_lds_dwordx4 v243, s[66:67] offset:1024\n"
        "v_mfma_f32_16x16x32_bf16 a[240:243], v[28:31], v[80:83], a[240:243]\n"
        "ds_read_b64_tr_b16 v[80:81], v226 offset:57344\n"
        "v_and_b32 v195, s35, v157\n"
        "v_mfma_f32_16x16x32_bf16 a[240:243], v[24:27], v[48:51], a[240:243]\n"
        "ds_read_b64_tr_b16 v[48:49], v226 offset:49152\n"
        "v_mul_f32 v194, v194, v188\n"
        "v_mfma_f32_16x16x32_bf16 a[244:247], v[24:27], v[52:55], a[244:247]\n"
        "ds_read_b64_tr_b16 v[50:51], v227 offset:49152\n"
        "v_mul_f32 v195, v195, v189\n"
        "v_mfma_f32_16x16x32_bf16 a[244:247], v[28:31], v[84:87], a[244:247]\n"
        "ds_read_b64_tr_b16 v[82:83], v227 offset:57344\n"
        "global_load_lds_dwordx4 v244, s[66:67] offset:2048\n"
        "v_mfma_f32_16x16x32_bf16 a[212:215], v[20:23], v[84:87], a[212:215]\n"
        "ds_read_b64_tr_b16 v[84:85], v228 offset:57344\n"
        "v_cvt_pk_bf16_f32 v194, 0, v194\n"
        "v_mfma_f32_16x16x32_bf16 a[212:215], v[16:19], v[52:55], a[212:215]\n"
        "ds_read_b64_tr_b16 v[52:53], v228 offset:49152\n"
        "v_cvt_pk_bf16_f32 v195, 0, v195\n"
        "v_mfma_f32_16x16x32_bf16 a[216:219], v[16:19], v[56:59], a[216:219]\n"
        "ds_read_b64_tr_b16 v[54:55], v229 offset:49152\n"
        "v_add_f32 v194, v194, v195\n"
        "v_mfma_f32_16x16x32_bf16 a[216:219], v[20:23], v[88:91], a[216:219]\n"
        "ds_read_b64_tr_b16 v[86:87], v229 offset:57344\n"
        "global_load_lds_dwordx4 v245, s[66:67] offset:3072\n"
        "s_add_u32 m0, s72, 0x10000\n"
        "v_mfma_f32_16x16x32_bf16 a[248:251], v[28:31], v[88:91], a[248:251]\n"
        "ds_read_b64_tr_b16 v[88:89], v230 offset:57344\n"
        "v_add_f32 v191, v191, v194\n"
        "v_mfma_f32_16x16x32_bf16 a[248:251], v[24:27], v[56:59], a[248:251]\n"
        "ds_read_b64_tr_b16 v[56:57], v230 offset:49152\n"
        "v_add_f32_dpp v190, v190, v190 row_shr:1 row_mask:0xf bank_mask:0xf bound_ctrl:1\n"
        "v_mfma_f32_16x16x32_bf16 a[252:255], v[24:27], v[60:63], a[252:255]\n"
        "ds_read_b64_tr_b16 v[58:59], v231 offset:49152\n"
        "v_add_f32_dpp v191, v191, v191 row_shr:1 row_mask:0xf bank_mask:0xf bound_ctrl:1\n"
        "v_mfma_f32_16x16x32_bf16 a[252:255], v[28:31], v[92:95], a[252:255]\n"
        "ds_read_b64_tr_b16 v[90:91], v231 offset:57344\n"
        "global_load_lds_dwordx4 v242, s[86:87] offset:0\n"
        "v_mfma_f32_16x16x32_bf16 a[220:223], v[20:23], v[92:95], a[220:223]\n"
        "ds_read_b64_tr_b16 v[92:93], v232 offset:57344\n"
        "v_add_f32_dpp v190, v190, v190 row_shr:2 row_mask:0xf bank_mask:0xf bound_ctrl:1\n"
        "v_mfma_f32_16x16x32_bf16 a[220:223], v[16:19], v[60:63], a[220:223]\n"
        "ds_read_b64_tr_b16 v[60:61], v232 offset:49152\n"
        "s_waitcnt lgkmcnt(8)\n"
        "v_mfma_f32_16x16x32_bf16 a[0:3], v[32:35], v[0:3], a[0:3]\n"
        "ds_read_b64_tr_b16 v[62:63], v233 offset:49152\n"
        "v_add_f32_dpp v191, v191, v191 row_shr:2 row_mask:0xf bank_mask:0xf bound_ctrl:1\n"
        "v_mfma_f32_16x16x32_bf16 a[0:3], v[64:67], v[4:7], a[0:3]\n"
        "ds_read_b64_tr_b16 v[94:95], v233 offset:57344\n"
        "global_load_lds_dwordx4 v243, s[86:87] offset:1024\n"
        "v_mfma_f32_16x16x32_bf16 a[32:35], v[64:67], v[12:15], a[32:35]\n"
        "ds_read_b128 v[16:19], v224 offset:53248\n"
        "v_add_f32_dpp v190, v190, v190 row_shr:4 row_mask:0xf bank_mask:0xf bound_ctrl:1\n"
        "v_mfma_f32_16x16x32_bf16 a[32:35], v[32:35], v[8:11], a[32:35]\n"
        "ds_read_b128 v[20:23], v225 offset:53248\n"
        "v_add_f32_dpp v191, v191, v191 row_shr:4 row_mask:0xf bank_mask:0xf bound_ctrl:1\n"
        "v_mfma_f32_16x16x32_bf16 a[36:39], v[36:39], v[8:11], a[36:39]\n"
        "ds_read_b128 v[24:27], v224 offset:55296\n"
        "v_add_f32_dpp v190, v190, v190 row_shr:8 row_mask:0xf bank_mask:0xf bound_ctrl:1\n"
        "v_mfma_f32_16x16x32_bf16 a[36:39], v[68:71], v[12:15], a[36:39]\n"
        "ds_read_b128 v[28:31], v225 offset:55296\n"
        "global_load_lds_dwordx4 v244, s[86:87] offset:2048\n"
        "v_mfma_f32_16x16x32_bf16 a[4:7], v[68:71], v[4:7], a[4:7]\n"
        "s_add_u32 s70, s38, s96\n"
        "s_addc_u32 s71, s39, 0\n"
        "v_add_f32_dpp v191, v191, v191 row_shr:8 row_mask:0xf bank_mask:0xf bound_ctrl:1\n"
        "v_mfma_f32_16x16x32_bf16 a[4:7], v[36:39], v[0:3], a[4:7]\n"
        "v_add_f32_dpp v190, v190, v190 row_bcast:15 row_mask:0xa bank_mask:0xf bound_ctrl:1\n"
        "v_add_f32_dpp v191, v191, v191 row_bcast:15 row_mask:0xa bank_mask:0xf bound_ctrl:1\n"
        "v_mfma_f32_16x16x32_bf16 a[8:11], v[40:43], v[0:3], a[8:11]\n"
        "v_add_f32_dpp v190, v190, v190 row_bcast:31 row_mask:0xc bank_mask:0xf bound_ctrl:1\n"
        "v_add_f32_dpp v191, v191, v191 row_bcast:31 row_mask:0xc bank_mask:0xf bound_ctrl:1\n"
        "v_mfma_f32_16x16x32_bf16 a[8:11], v[72:75], v[4:7], a[8:11]\n"
        "global_load_lds_dwordx4 v245, s[86:87] offset:3072\n"
        "s_add_u32 m0, s72, 0x11000\n"
        "v_mfma_f32_16x16x32_bf16 a[40:43], v[72:75], v[12:15], a[40:43]\n"
        "s_add_u32 s86, s66, 0x80\n"
        "s_addc_u32 s87, s67, 0\n"
        "v_cvt_pk_bf16_f32 v190, 0, v190\n"
        "v_mfma_f32_16x16x32_bf16 a[40:43], v[40:43], v[8:11], a[40:43]\n"
        "v_mul_f32 v190, 0xbfb8aa3b, v190\n"
        "v_lshrrev_b32 v192, 2, v254\n"
        "v_mfma_f32_16x16x32_bf16 a[44:47], v[44:47], v[8:11], a[44:47]\n"
        "v_exp_f32 v190, v190\n"
        "v_mfma_f32_16x16x32_bf16 a[44:47], v[76:79], v[12:15], a[44:47]\n"
        "global_load_lds_dwordx4 v242, s[86:87] offset:0\n"
        "v_mfma_f32_16x16x32_bf16 a[12:15], v[76:79], v[4:7], a[12:15]\n"
        "v_add_f32 v190, 1.0, v190\n"
        "v_add_u32 v192, s80, v192\n"
        "v_mfma_f32_16x16x32_bf16 a[12:15], v[44:47], v[0:3], a[12:15]\n"
        "v_rcp_f32 v190, v190\n"
        "v_mfma_f32_16x16x32_bf16 a[16:19], v[0:3], v[48:51], a[16:19]\n"
        "s_waitcnt lgkmcnt(0)\n"
        "v_cvt_pk_bf16_f32 v190, 0, v190\n"
        "v_cvt_pk_bf16_f32 v191, 0, v191\n"
        "v_mfma_f32_16x16x32_bf16 a[16:19], v[4:7], v[80:83], a[16:19]\n"
        "global_load_dword v253, v173, s[82:83] offset:0\n"
        "v_mfma_f32_16x16x32_bf16 a[48:51], v[12:15], v[80:83], a[48:51]\n"
        "global_load_lds_dwordx4 v243, s[86:87] offset:1024\n"
        "v_readlane_b32 s84, v190, 63\n"
        "v_mfma_f32_16x16x32_bf16 a[48:51], v[8:11], v[48:51], a[48:51]\n"
        "v_mul_f32 v191, 0xbfb8aa3b, v191\n"
        "v_mov_b32 v190, s84\n"
        "v_mfma_f32_16x16x32_bf16 a[52:55], v[8:11], v[52:55], a[52:55]\n"
        "v_exp_f32 v191, v191\n"
        "v_mfma_f32_16x16x32_bf16 a[52:55], v[12:15], v[84:87], a[52:55]\n"
        "v_add_f32 v191, 1.0, v191\n"
        "v_lshrrev_b32 v193, 2, v254\n"
        "v_mfma_f32_16x16x32_bf16 a[20:23], v[4:7], v[84:87], a[20:23]\n"
        "global_load_lds_dwordx4 v244, s[86:87] offset:2048\n"
        "v_add_u32 v193, s81, v193\n"
        "v_mfma_f32_16x16x32_bf16 a[20:23], v[0:3], v[52:55], a[20:23]\n"
        "v_rcp_f32 v191, v191\n"
        "v_mfma_f32_16x16x32_bf16 a[24:27], v[0:3], v[56:59], a[24:27]\n"
        "v_cvt_pk_bf16_f32 v191, 0, v191\n"
        "v_add_u32 v226, s53, v226\n"
        "v_mfma_f32_16x16x32_bf16 a[24:27], v[4:7], v[88:91], a[24:27]\n"
        "v_readlane_b32 s85, v191, 63\n"
        "v_add_u32 v227, s53, v227\n"
        "v_mfma_f32_16x16x32_bf16 a[56:59], v[12:15], v[88:91], a[56:59]\n"
        "global_load_lds_dwordx4 v245, s[86:87] offset:3072\n"
        "s_add_u32 m0, s73, 0x1c000\n"
        "v_mov_b32 v191, s85\n"
        "v_mfma_f32_16x16x32_bf16 a[56:59], v[8:11], v[56:59], a[56:59]\n"
        "s_add_u32 s86, s68, 0x100000\n"
        "s_addc_u32 s87, s69, 0\n"
        "v_add_u32 v228, s53, v228\n"
        "v_mfma_f32_16x16x32_bf16 a[60:63], v[8:11], v[60:63], a[60:63]\n"
        "ds_read_b128 v[8:11], v224 offset:34816\n"
        "v_add_u32 v229, s53, v229\n"
        "v_mfma_f32_16x16x32_bf16 a[60:63], v[12:15], v[92:95], a[60:63]\n"
        "ds_read_b128 v[12:15], v225 offset:34816\n"
        "v_add_u32 v230, s53, v230\n"
        "v_mfma_f32_16x16x32_bf16 a[28:31], v[4:7], v[92:95], a[28:31]\n"
        "ds_read_b128 v[4:7], v225 offset:32768\n"
        "v_add_u32 v231, s53, v231\n"
        "v_mfma_f32_16x16x32_bf16 a[28:31], v[0:3], v[60:63], a[28:31]\n"
        "ds_read_b128 v[0:3], v224 offset:32768\n"
        "v_add_u32 v232, s53, v232\n"
        "v_mfma_f32_16x16x32_bf16 a[64:67], v[32:35], v[16:19], a[64:67]\n"
        "s_mov_b64 exec, 0xffff\n"
        "global_store_dword v192, v190, s[76:77] sc1\n"
        "s_mov_b64 exec, -1\n"
        "v_mfma_f32_16x16x32_bf16 a[64:67], v[64:67], v[20:23], a[64:67]\n"
        "s_mov_b64 exec, 0xffff\n"
        "global_store_dword v193, v191, s[76:77] sc1\n"
        "s_mov_b64 exec, -1\n"
        "v_mfma_f32_16x16x32_bf16 a[96:99], v[64:67], v[28:31], a[96:99]\n"
        "v_add_u32 v233, s53, v233\n"
        "s_add_u32 s53, s52, s51\n"
        "v_mfma_f32_16x16x32_bf16 a[96:99], v[32:35], v[24:27], a[96:99]\n"
        "s_add_u32 s53, s53, 0x1100000\n"
        "v_mfma_f32_16x16x32_bf16 a[100:103], v[36:39], v[24:27], a[100:103]\n"
        "v_mfma_f32_16x16x32_bf16 a[100:103], v[68:71], v[28:31], a[100:103]\n"
        "v_mfma_f32_16x16x32_bf16 a[68:71], v[68:71], v[20:23], a[68:71]\n"
        "v_mfma_f32_16x16x32_bf16 a[68:71], v[36:39], v[16:19], a[68:71]\n"
        "v_mfma_f32_16x16x32_bf16 a[72:75], v[40:43], v[16:19], a[72:75]\n"
        "v_mfma_f32_16x16x32_bf16 a[72:75], v[72:75], v[20:23], a[72:75]\n"
        "v_mfma_f32_16x16x32_bf16 a[104:107], v[72:75], v[28:31], a[104:107]\n"
        "v_mfma_f32_16x16x32_bf16 a[104:107], v[40:43], v[24:27], a[104:107]\n"
        "v_mfma_f32_16x16x32_bf16 a[108:111], v[44:47], v[24:27], a[108:111]\n"
        "v_mfma_f32_16x16x32_bf16 a[108:111], v[76:79], v[28:31], a[108:111]\n"
        "v_mfma_f32_16x16x32_bf16 a[76:79], v[76:79], v[20:23], a[76:79]\n"
        "v_mfma_f32_16x16x32_bf16 a[76:79], v[44:47], v[16:19], a[76:79]\n"
        "v_mfma_f32_16x16x32_bf16 a[80:83], v[16:19], v[48:51], a[80:83]\n"
        "v_mfma_f32_16x16x32_bf16 a[80:83], v[20:23], v[80:83], a[80:83]\n"
        "v_mfma_f32_16x16x32_bf16 a[112:115], v[28:31], v[80:83], a[112:115]\n"
        "v_mfma_f32_16x16x32_bf16 a[112:115], v[24:27], v[48:51], a[112:115]\n"
        "v_mfma_f32_16x16x32_bf16 a[116:119], v[24:27], v[52:55], a[116:119]\n"
        "v_mfma_f32_16x16x32_bf16 a[116:119], v[28:31], v[84:87], a[116:119]\n"
        "v_mfma_f32_16x16x32_bf16 a[84:87], v[20:23], v[84:87], a[84:87]\n"
        "v_mfma_f32_16x16x32_bf16 a[84:87], v[16:19], v[52:55], a[84:87]\n"
        "v_mfma_f32_16x16x32_bf16 a[88:91], v[16:19], v[56:59], a[88:91]\n"
        "v_mfma_f32_16x16x32_bf16 a[88:91], v[20:23], v[88:91], a[88:91]\n"
        "v_mfma_f32_16x16x32_bf16 a[120:123], v[28:31], v[88:91], a[120:123]\n"
        "v_mfma_f32_16x16x32_bf16 a[120:123], v[24:27], v[56:59], a[120:123]\n"
        "v_mfma_f32_16x16x32_bf16 a[124:127], v[24:27], v[60:63], a[124:127]\n"
        "ds_read_b128 v[24:27], v224 offset:38912\n"
        "s_waitcnt lgkmcnt(1)\n"
        "v_mfma_f32_16x16x32_bf16 a[124:127], v[28:31], v[92:95], a[124:127]\n"
        "ds_read_b128 v[28:31], v225 offset:38912\n"
        "v_mfma_f32_16x16x32_bf16 a[92:95], v[20:23], v[92:95], a[92:95]\n"
        "ds_read_b128 v[20:23], v225 offset:36864\n"
        "v_mfma_f32_16x16x32_bf16 a[92:95], v[16:19], v[60:63], a[92:95]\n"
        "ds_read_b128 v[16:19], v224 offset:36864\n"
        "v_mfma_f32_16x16x32_bf16 a[128:131], v[32:35], v[0:3], a[128:131]\n"
        "v_mfma_f32_16x16x32_bf16 a[128:131], v[64:67], v[4:7], a[128:131]\n"
        "v_mfma_f32_16x16x32_bf16 a[160:163], v[64:67], v[12:15], a[160:163]\n"
        "v_mfma_f32_16x16x32_bf16 a[160:163], v[32:35], v[8:11], a[160:163]\n"
        "v_mfma_f32_16x16x32_bf16 a[164:167], v[36:39], v[8:11], a[164:167]\n"
        "v_mfma_f32_16x16x32_bf16 a[164:167], v[68:71], v[12:15], a[164:167]\n"
        "v_mfma_f32_16x16x32_bf16 a[132:135], v[68:71], v[4:7], a[132:135]\n"
        "v_mfma_f32_16x16x32_bf16 a[132:135], v[36:39], v[0:3], a[132:135]\n"
        "v_mfma_f32_16x16x32_bf16 a[136:139], v[40:43], v[0:3], a[136:139]\n"
        "v_mfma_f32_16x16x32_bf16 a[136:139], v[72:75], v[4:7], a[136:139]\n"
        "v_mfma_f32_16x16x32_bf16 a[168:171], v[72:75], v[12:15], a[168:171]\n"
        "v_mfma_f32_16x16x32_bf16 a[168:171], v[40:43], v[8:11], a[168:171]\n"
        "v_mfma_f32_16x16x32_bf16 a[172:175], v[44:47], v[8:11], a[172:175]\n"
        "v_mfma_f32_16x16x32_bf16 a[172:175], v[76:79], v[12:15], a[172:175]\n"
        "v_mfma_f32_16x16x32_bf16 a[140:143], v[76:79], v[4:7], a[140:143]\n"
        "v_mfma_f32_16x16x32_bf16 a[140:143], v[44:47], v[0:3], a[140:143]\n"
        "v_mfma_f32_16x16x32_bf16 a[144:147], v[0:3], v[48:51], a[144:147]\n"
        "v_mfma_f32_16x16x32_bf16 a[144:147], v[4:7], v[80:83], a[144:147]\n"
        "v_mfma_f32_16x16x32_bf16 a[176:179], v[12:15], v[80:83], a[176:179]\n"
        "v_mfma_f32_16x16x32_bf16 a[176:179], v[8:11], v[48:51], a[176:179]\n"
        "v_mfma_f32_16x16x32_bf16 a[180:183], v[8:11], v[52:55], a[180:183]\n"
        "v_mfma_f32_16x16x32_bf16 a[180:183], v[12:15], v[84:87], a[180:183]\n"
        "v_mfma_f32_16x16x32_bf16 a[148:151], v[4:7], v[84:87], a[148:151]\n"
        "v_mfma_f32_16x16x32_bf16 a[148:151], v[0:3], v[52:55], a[148:151]\n"
        "v_mfma_f32_16x16x32_bf16 a[152:155], v[0:3], v[56:59], a[152:155]\n"
        "v_mfma_f32_16x16x32_bf16 a[152:155], v[4:7], v[88:91], a[152:155]\n"
        "v_mfma_f32_16x16x32_bf16 a[184:187], v[12:15], v[88:91], a[184:187]\n"
        "v_mfma_f32_16x16x32_bf16 a[184:187], v[8:11], v[56:59], a[184:187]\n"
        "s_waitcnt vmcnt(11) lgkmcnt(0)\n"
        "v_mfma_f32_16x16x32_bf16 a[188:191], v[8:11], v[60:63], a[188:191]\n"
        "s_barrier\n"
        "v_mfma_f32_16x16x32_bf16 a[188:191], v[12:15], v[92:95], a[188:191]\n"
        "global_load_lds_dwordx4 v238, s[86:87] offset:0\n"
        "ds_read_b64_tr_b16 v[160:161], v226 offset:32768\n"
        "v_mfma_f32_16x16x32_bf16 a[156:159], v[4:7], v[92:95], a[156:159]\n"
        "ds_read_b64_tr_b16 v[162:163], v227 offset:32768\n"
        "v_mfma_f32_16x16x32_bf16 a[156:159], v[0:3], v[60:63], a[156:159]\n"
        "ds_read_b64_tr_b16 v[164:165], v226 offset:40960\n"
        "v_mfma_f32_16x16x32_bf16 a[192:195], v[32:35], v[16:19], a[192:195]\n"
        "ds_read_b64_tr_b16 v[166:167], v227 offset:40960\n"
        "v_mfma_f32_16x16x32_bf16 a[192:195], v[64:67], v[20:23], a[192:195]\n"
        "ds_read_b64_tr_b16 v[172:173], v228 offset:40960\n"
        "global_load_lds_dwordx4 v239, s[86:87] offset:1024\n"
        "v_mfma_f32_16x16x32_bf16 a[224:227], v[64:67], v[28:31], a[224:227]\n"
        "ds_read_b64_tr_b16 v[174:175], v229 offset:40960\n"
        "v_mfma_f32_16x16x32_bf16 a[224:227], v[32:35], v[24:27], a[224:227]\n"
        "ds_read_b64_tr_b16 v[168:169], v228 offset:32768\n"
        "v_mfma_f32_16x16x32_bf16 a[228:231], v[36:39], v[24:27], a[228:231]\n"
        "ds_read_b64_tr_b16 v[170:171], v229 offset:32768\n"
        "v_mfma_f32_16x16x32_bf16 a[228:231], v[68:71], v[28:31], a[228:231]\n"
        "ds_read_b128 v[192:195], v246 offset:2048\n"
        "global_load_lds_dwordx4 v240, s[86:87] offset:2048\n"
        "v_mfma_f32_16x16x32_bf16 a[196:199], v[68:71], v[20:23], a[196:199]\n"
        "ds_read_b128 v[208:211], v247 offset:2048\n"
        "v_accvgpr_read_b32 v177, a225\n"
        "v_mfma_f32_16x16x32_bf16 a[196:199], v[36:39], v[16:19], a[196:199]\n"
        "ds_read_b128 v[196:199], v246 offset:0\n"
        "v_accvgpr_read_b32 v178, a226\n"
        "v_mfma_f32_16x16x32_bf16 a[200:203], v[40:43], v[16:19], a[200:203]\n"
        "ds_read_b128 v[212:215], v247 offset:0\n"
        "v_accvgpr_read_b32 v179, a227\n"
        "v_mfma_f32_16x16x32_bf16 a[200:203], v[72:75], v[20:23], a[200:203]\n"
        "ds_read_b128 v[200:203], v246 offset:6144\n"
        "global_load_lds_dwordx4 v241, s[86:87] offset:3072\n"
        "s_add_u32 m0, s72, 0x8000\n"
        "v_mfma_f32_16x16x32_bf16 a[232:235], v[72:75], v[28:31], a[232:235]\n"
        "ds_read_b128 v[216:219], v247 offset:6144\n"
        "s_add_u32 s86, s64, 0x100\n"
        "s_addc_u32 s87, s65, 0\n"
        "v_mfma_f32_16x16x32_bf16 a[232:235], v[40:43], v[24:27], a[232:235]\n"
        "ds_read_b128 v[204:207], v246 offset:4096\n"
        "v_accvgpr_read_b32 v180, a228\n"
        "v_mfma_f32_16x16x32_bf16 a[236:239], v[44:47], v[24:27], a[236:239]\n"
        "ds_read_b128 v[220:223], v247 offset:4096\n"
        "v_accvgpr_read_b32 v181, a229\n"
        "v_mfma_f32_16x16x32_bf16 a[236:239], v[76:79], v[28:31], a[236:239]\n"
        "ds_read_b64_tr_b16 v[32:33], v230 offset:32768\n"
        "global_load_lds_dwordx4 v242, s[86:87] offset:0\n"
        "v_mfma_f32_16x16x32_bf16 a[204:207], v[76:79], v[20:23], a[204:207]\n"
        "ds_read_b64_tr_b16 v[34:35], v231 offset:32768\n"
        "v_accvgpr_read_b32 v182, a230\n"
        "v_mfma_f32_16x16x32_bf16 a[204:207], v[44:47], v[16:19], a[204:207]\n"
        "ds_read_b64_tr_b16 v[36:37], v230 offset:40960\n"
        "s_add_u32 s96, s52, 0x2000\n"
        "v_accvgpr_read_b32 v183, a231\n"
        "v_mfma_f32_16x16x32_bf16 a[208:211], v[16:19], v[48:51], a[208:211]\n"
        "ds_read_b64_tr_b16 v[38:39], v231 offset:40960\n"
        "v_mul_f32 v177, s88, v177\n"
        "v_mfma_f32_16x16x32_bf16 a[208:211], v[20:23], v[80:83], a[208:211]\n"
        "ds_read_b64_tr_b16 v[44:45], v232 offset:40960\n"
        "global_load_lds_dwordx4 v243, s[86:87] offset:1024\n"
        "v_mfma_f32_16x16x32_bf16 a[240:243], v[28:31], v[80:83], a[240:243]\n"
        "ds_read_b64_tr_b16 v[46:47], v233 offset:40960\n"
        "v_mul_f32 v178, s88, v178\n"
        "v_mfma_f32_16x16x32_bf16 a[240:243], v[24:27], v[48:51], a[240:243]\n"
        "ds_read_b64_tr_b16 v[40:41], v232 offset:32768\n"
        "v_mul_f32 v179, s88, v179\n"
        "v_mfma_f32_16x16x32_bf16 a[244:247], v[24:27], v[52:55], a[244:247]\n"
        "ds_read_b64_tr_b16 v[42:43], v233 offset:32768\n"
        "v_mul_f32 v180, s88, v180\n"
        "v_mfma_f32_16x16x32_bf16 a[244:247], v[28:31], v[84:87], a[244:247]\n"
        "global_load_lds_dwordx4 v244, s[86:87] offset:2048\n"
        "v_mfma_f32_16x16x32_bf16 a[212:215], v[20:23], v[84:87], a[212:215]\n"
        "ds_read_b64_tr_b16 v[48:49], v226 offset:33792\n"
        "v_mul_f32 v181, s88, v181\n"
        "v_mfma_f32_16x16x32_bf16 a[212:215], v[16:19], v[52:55], a[212:215]\n"
        "ds_read_b64_tr_b16 v[50:51], v227 offset:33792\n"
        "v_mul_f32 v182, s88, v182\n"
        "v_mfma_f32_16x16x32_bf16 a[216:219], v[16:19], v[56:59], a[216:219]\n"
        "ds_read_b64_tr_b16 v[52:53], v226 offset:41984\n"
        "v_mul_f32 v183, s88, v183\n"
        "v_mfma_f32_16x16x32_bf16 a[216:219], v[20:23], v[88:91], a[216:219]\n"
        "global_load_lds_dwordx4 v245, s[86:87] offset:3072\n"
        "s_add_u32 m0, s72, 0x9000\n"
        "v_mfma_f32_16x16x32_bf16 a[248:251], v[28:31], v[88:91], a[248:251]\n"
        "s_add_u32 s86, s66, 0x100\n"
        "s_addc_u32 s87, s67, 0\n"
        "v_accvgpr_read_b32 v185, a233\n"
        "v_mfma_f32_16x16x32_bf16 a[248:251], v[24:27], v[56:59], a[248:251]\n"
        "ds_read_b64_tr_b16 v[54:55], v227 offset:41984\n"
        "v_accvgpr_read_b32 v186, a234\n"
        "v_mfma_f32_16x16x32_bf16 a[252:255], v[24:27], v[60:63], a[252:255]\n"
        "ds_read_b64_tr_b16 v[56:57], v228 offset:33792\n"
        "v_accvgpr_read_b32 v187, a235\n"
        "v_mfma_f32_16x16x32_bf16 a[252:255], v[28:31], v[92:95], a[252:255]\n"
        "global_load_lds_dwordx4 v242, s[86:87] offset:0\n"
        "v_mfma_f32_16x16x32_bf16 a[220:223], v[20:23], v[92:95], a[220:223]\n"
        "ds_read_b64_tr_b16 v[58:59], v229 offset:33792\n"
        "v_accvgpr_read_b32 v188, a236\n"
        "v_mfma_f32_16x16x32_bf16 a[220:223], v[16:19], v[60:63], a[220:223]\n"
        "ds_read_b64_tr_b16 v[60:61], v228 offset:41984\n"
        "s_waitcnt lgkmcnt(5)\n"
        "v_mfma_f32_16x16x32_bf16 v[0:3], v[160:163], v[192:195], 0\n"
        "ds_read_b64_tr_b16 v[62:63], v229 offset:41984\n"
        "v_accvgpr_read_b32 v189, a237\n"
        "v_mfma_f32_16x16x32_bf16 v[0:3], v[164:167], v[208:211], v[0:3]\n"
        "global_load_lds_dwordx4 v243, s[86:87] offset:1024\n"
        "v_mfma_f32_16x16x32_bf16 v[4:7], v[172:175], v[208:211], 0\n"
        "v_accvgpr_read_b32 v190, a238\n"
        "v_accvgpr_read_b32 v191, a239\n"
        "v_mfma_f32_16x16x32_bf16 v[4:7], v[168:171], v[192:195], v[4:7]\n"
        "v_mul_f32 v185, s88, v185\n"
        "v_mul_f32 v186, s88, v186\n"
        "v_mfma_f32_16x16x32_bf16 v[68:71], v[168:171], v[196:199], 0\n"
        "v_mul_f32 v187, s88, v187\n"
        "v_mul_f32 v188, s88, v188\n"
        "v_mfma_f32_16x16x32_bf16 v[68:71], v[172:175], v[212:215], v[68:71]\n"
        "global_load_lds_dwordx4 v244, s[86:87] offset:2048\n"
        "v_mfma_f32_16x16x32_bf16 v[64:67], v[164:167], v[212:215], 0\n"
        "v_mul_f32 v189, s88, v189\n"
        "v_mul_f32 v190, s88, v190\n"
        "v_mfma_f32_16x16x32_bf16 v[64:67], v[160:163], v[196:199], v[64:67]\n"
        "v_accvgpr_read_b32 v176, a224\n"
        "v_mul_f32 v176, s88, v176\n"
        "v_mfma_f32_16x16x32_bf16 v[96:99], v[160:163], v[200:203], 0\n"
        "v_cvt_pk_bf16_f32 v176, v176, v177\n"
        "v_cvt_pk_bf16_f32 v177, v178, v179\n"
        "v_mfma_f32_16x16x32_bf16 v[96:99], v[164:167], v[216:219], v[96:99]\n"
        "v_cvt_pk_bf16_f32 v178, v180, v181\n"
        "v_cvt_pk_bf16_f32 v179, v182, v183\n"
        "v_mfma_f32_16x16x32_bf16 v[100:103], v[172:175], v[216:219], 0\n"
        "global_load_lds_dwordx4 v245, s[86:87] offset:3072\n"
        "s_add_u32 m0, s73, 0x20000\n"
        "v_accvgpr_write_b32 a224, v176\n"
        "v_mfma_f32_16x16x32_bf16 v[100:103], v[168:171], v[200:203], v[100:103]\n"
        "s_add_u32 s86, s68, 0x180000\n"
        "s_addc_u32 s87, s69, 0\n"
        "v_accvgpr_write_b32 a225, v177\n"
        "v_mfma_f32_16x16x32_bf16 v[132:135], v[168:171], v[204:207], 0\n"
        "ds_read_b64_tr_b16 v[168:169], v232 offset:33792\n"
        "v_accvgpr_write_b32 a226, v178\n"
        "v_mfma_f32_16x16x32_bf16 v[132:135], v[172:175], v[220:223], v[132:135]\n"
        "ds_read_b64_tr_b16 v[172:173], v232 offset:41984\n"
        "v_accvgpr_write_b32 a227, v179\n"
        "v_mfma_f32_16x16x32_bf16 v[128:131], v[164:167], v[220:223], 0\n"
        "ds_read_b64_tr_b16 v[164:165], v230 offset:41984\n"
        "v_mul_f32 v191, s88, v191\n"
        "v_mfma_f32_16x16x32_bf16 v[128:131], v[160:163], v[204:207], v[128:131]\n"
        "ds_read_b64_tr_b16 v[160:161], v230 offset:33792\n"
        "v_accvgpr_read_b32 v177, a193\n"
        "v_mfma_f32_16x16x32_bf16 v[8:11], v[32:35], v[192:195], 0\n"
        "ds_read_b64_tr_b16 v[162:163], v231 offset:33792\n"
        "v_accvgpr_read_b32 v178, a194\n"
        "v_mfma_f32_16x16x32_bf16 v[8:11], v[36:39], v[208:211], v[8:11]\n"
        "ds_read_b64_tr_b16 v[166:167], v231 offset:41984\n"
        "v_accvgpr_read_b32 v179, a195\n"
        "v_mfma_f32_16x16x32_bf16 v[12:15], v[44:47], v[208:211], 0\n"
        "ds_read_b64_tr_b16 v[174:175], v233 offset:41984\n"
        "v_accvgpr_read_b32 v180, a196\n"
        "v_mfma_f32_16x16x32_bf16 v[12:15], v[40:43], v[192:195], v[12:15]\n"
        "ds_read_b64_tr_b16 v[170:171], v233 offset:33792\n"
        "v_accvgpr_read_b32 v181, a197\n"
        "v_mfma_f32_16x16x32_bf16 v[76:79], v[40:43], v[196:199], 0\n"
        "LHA4_%=:\n"
        "s_cbranch_execz LHA8_%=\n"
        "v_accvgpr_read_b32 v182, a198\n"
        "v_accvgpr_read_b32 v183, a199\n"
        "v_mfma_f32_16x16x32_bf16 v[76:79], v[44:47], v[212:215], v[76:79]\n"
        "v_mul_f32 v177, s88, v177\n"
        "v_mul_f32 v178, s88, v178\n"
        "v_mfma_f32_16x16x32_bf16 v[72:75], v[36:39], v[212:215], 0\n"
        "v_mul_f32 v179, s88, v179\n"
        "v_mul_f32 v180, s88, v180\n"
        "v_mfma_f32_16x16x32_bf16 v[72:75], v[32:35], v[196:199], v[72:75]\n"
        "v_mul_f32 v181, s88, v181\n"
        "v_mul_f32 v182, s88, v182\n"
        "v_mfma_f32_16x16x32_bf16 v[104:107], v[32:35], v[200:203], 0\n"
        "v_mul_f32 v183, s88, v183\n"
        "v_mfma_f32_16x16x32_bf16 v[104:107], v[36:39], v[216:219], v[104:107]\n"
        "v_mfma_f32_16x16x32_bf16 v[108:111], v[44:47], v[216:219], 0\n"
        "v_mfma_f32_16x16x32_bf16 v[108:111], v[40:43], v[200:203], v[108:111]\n"
        "s_waitcnt vmcnt(14) lgkmcnt(0)\n"
        "v_mfma_f32_16x16x32_bf16 v[140:143], v[40:43], v[204:207], 0\n"
        "s_barrier\n"
        "v_mfma_f32_16x16x32_bf16 v[140:143], v[44:47], v[220:223], v[140:143]\n"
        "global_load_lds_dwordx4 v238, s[86:87] offset:0\n"
        "ds_read_b64_tr_b16 v[44:45], v228 offset:57344\n"
        "v_mfma_f32_16x16x32_bf16 v[136:139], v[36:39], v[220:223], 0\n"
        "ds_read_b64_tr_b16 v[36:37], v226 offset:57344\n"
        "v_mfma_f32_16x16x32_bf16 v[136:139], v[32:35], v[204:207], v[136:139]\n"
        "ds_read_b64_tr_b16 v[32:33], v226 offset:49152\n"
        "v_accvgpr_read_b32 v184, a232\n"
        "v_mfma_f32_16x16x32_bf16 v[16:19], v[48:51], v[192:195], 0\n"
        "ds_read_b64_tr_b16 v[34:35], v227 offset:49152\n"
        "v_mul_f32 v184, s88, v184\n"
        "v_mfma_f32_16x16x32_bf16 v[16:19], v[52:55], v[208:211], v[16:19]\n"
        "ds_read_b64_tr_b16 v[38:39], v227 offset:57344\n"
        "v_cvt_pk_bf16_f32 v184, v184, v185\n"
        "v_mfma_f32_16x16x32_bf16 v[20:23], v[60:63], v[208:211], 0\n"
        "ds_read_b64_tr_b16 v[46:47], v229 offset:57344\n"
        "v_cvt_pk_bf16_f32 v185, v186, v187\n"
        "v_mfma_f32_16x16x32_bf16 v[20:23], v[56:59], v[192:195], v[20:23]\n"
        "ds_read_b64_tr_b16 v[40:41], v228 offset:49152\n"
        "v_cvt_pk_bf16_f32 v186, v188, v189\n"
        "v_mfma_f32_16x16x32_bf16 v[84:87], v[56:59], v[196:199], 0\n"
        "ds_read_b64_tr_b16 v[42:43], v229 offset:49152\n"
        "v_cvt_pk_bf16_f32 v187, v190, v191\n"
        "v_mfma_f32_16x16x32_bf16 v[84:87], v[60:63], v[212:215], v[84:87]\n"
        "global_load_lds_dwordx4 v239, s[86:87] offset:1024\n"
        "v_accvgpr_write_b32 a228, v184\n"
        "v_mfma_f32_16x16x32_bf16 v[80:83], v[52:55], v[212:215], 0\n"
        "v_accvgpr_write_b32 a229, v185\n"
        "v_accvgpr_write_b32 a230, v186\n"
        "v_mfma_f32_16x16x32_bf16 v[80:83], v[48:51], v[196:199], v[80:83]\n"
        "v_accvgpr_write_b32 a231, v187\n"
        "v_accvgpr_read_b32 v185, a201\n"
        "v_mfma_f32_16x16x32_bf16 v[112:115], v[48:51], v[200:203], 0\n"
        "v_accvgpr_read_b32 v186, a202\n"
        "v_accvgpr_read_b32 v187, a203\n"
        "v_mfma_f32_16x16x32_bf16 v[112:115], v[52:55], v[216:219], v[112:115]\n"
        "v_accvgpr_read_b32 v188, a204\n"
        "v_accvgpr_read_b32 v189, a205\n"
        "v_mfma_f32_16x16x32_bf16 v[116:119], v[60:63], v[216:219], 0\n"
        "global_load_lds_dwordx4 v240, s[86:87] offset:2048\n"
        "v_accvgpr_read_b32 v190, a206\n"
        "v_mfma_f32_16x16x32_bf16 v[116:119], v[56:59], v[200:203], v[116:119]\n"
        "v_accvgpr_read_b32 v191, a207\n"
        "v_mul_f32 v185, s88, v185\n"
        "v_mfma_f32_16x16x32_bf16 v[148:151], v[56:59], v[204:207], 0\n"
        "ds_read_b64_tr_b16 v[56:57], v232 offset:49152\n"
        "v_mul_f32 v186, s88, v186\n"
        "v_mfma_f32_16x16x32_bf16 v[148:151], v[60:63], v[220:223], v[148:151]\n"
        "ds_read_b64_tr_b16 v[60:61], v232 offset:57344\n"
        "v_mul_f32 v187, s88, v187\n"
        "v_mfma_f32_16x16x32_bf16 v[144:147], v[52:55], v[220:223], 0\n"
        "global_store_dwordx4 v254, a[224:227], s[70:71] offset:0 sc1\n"
        "ds_read_b64_tr_b16 v[52:53], v230 offset:57344\n"
        "ds_read_b64_tr_b16 v[54:55], v231 offset:57344\n"
        "v_mul_f32 v188, s88, v188\n"
        "v_mfma_f32_16x16x32_bf16 v[144:147], v[48:51], v[204:207], v[144:147]\n"
        "ds_read_b64_tr_b16 v[48:49], v230 offset:49152\n"
        "v_mul_f32 v189, s88, v189\n"
        "v_mfma_f32_16x16x32_bf16 v[24:27], v[160:163], v[192:195], 0\n"
        "ds_read_b64_tr_b16 v[50:51], v231 offset:49152\n"
        "v_mul_f32 v190, s88, v190\n"
        "v_mfma_f32_16x16x32_bf16 v[24:27], v[164:167], v[208:211], v[24:27]\n"
        "ds_read_b64_tr_b16 v[62:63], v233 offset:57344\n"
        "v_mul_f32 v191, s88, v191\n"
        "v_mfma_f32_16x16x32_bf16 v[28:31], v[172:175], v[208:211], 0\n"
        "global_load_lds_dwordx4 v241, s[86:87] offset:3072\n"
        "s_add_u32 m0, s72, 0x0\n"
        "v_mfma_f32_16x16x32_bf16 v[28:31], v[168:171], v[192:195], v[28:31]\n"
        "ds_read_b64_tr_b16 v[58:59], v233 offset:49152\n"
        "s_add_u32 s86, s64, 0x180\n"
        "s_addc_u32 s87, s65, 0\n"
        "v_mfma_f32_16x16x32_bf16 v[92:95], v[168:171], v[196:199], 0\n"
        "ds_read_b128 v[192:195], v248 offset:2048\n"
        "v_mfma_f32_16x16x32_bf16 v[92:95], v[172:175], v[212:215], v[92:95]\n"
        "ds_read_b128 v[208:211], v249 offset:2048\n"
        "v_mfma_f32_16x16x32_bf16 v[88:91], v[164:167], v[212:215], 0\n"
        "ds_read_b128 v[212:215], v249 offset:0\n"
        "v_mfma_f32_16x16x32_bf16 v[88:91], v[160:163], v[196:199], v[88:91]\n"
        "global_load_lds_dwordx4 v242, s[86:87] offset:0\n"
        "v_mfma_f32_16x16x32_bf16 v[120:123], v[160:163], v[200:203], 0\n"
        "ds_read_b128 v[196:199], v248 offset:0\n"
        "v_accvgpr_read_b32 v176, a192\n"
        "v_mfma_f32_16x16x32_bf16 v[120:123], v[164:167], v[216:219], v[120:123]\n"
        "v_mul_f32 v176, s88, v176\n"
        "v_cvt_pk_bf16_f32 v176, v176, v177\n"
        "v_mfma_f32_16x16x32_bf16 v[124:127], v[172:175], v[216:219], 0\n"
        "ds_read_b128 v[216:219], v249 offset:6144\n"
        "v_cvt_pk_bf16_f32 v177, v178, v179\n"
        "v_mfma_f32_16x16x32_bf16 v[124:127], v[168:171], v[200:203], v[124:127]\n"
        "ds_read_b128 v[200:203], v248 offset:6144\n"
        "v_cvt_pk_bf16_f32 v178, v180, v181\n"
        "v_mfma_f32_16x16x32_bf16 v[156:159], v[168:171], v[204:207], 0\n"
        "s_waitcnt lgkmcnt(5)\n"
        "v_cvt_pk_bf16_f32 v179, v182, v183\n"
        "v_accvgpr_write_b32 a232, v176\n"
        "v_mfma_f32_16x16x32_bf16 v[156:159], v[172:175], v[220:223], v[156:159]\n"
        "s_waitcnt lgkmcnt(4)\n"
        "v_accvgpr_write_b32 a233, v177\n"
        "v_accvgpr_write_b32 a234, v178\n"
        "v_mfma_f32_16x16x32_bf16 v[152:155], v[164:167], v[220:223], 0\n"
        "global_load_lds_dwordx4 v243, s[86:87] offset:1024\n"
        "v_accvgpr_write_b32 a235, v179\n"
        "v_mfma_f32_16x16x32_bf16 v[152:155], v[160:163], v[204:207], v[152:155]\n"
        "ds_read_b128 v[204:207], v248 offset:4096\n"
        "v_accvgpr_read_b32 v177, a161\n"
        "v_mfma_f32_16x16x32_bf16 v[0:3], v[32:35], v[192:195], v[0:3]\n"
        "ds_read_b128 v[220:223], v249 offset:4096\n"
        "v_accvgpr_read_b32 v178, a162\n"
        "v_mfma_f32_16x16x32_bf16 v[0:3], v[36:39], v[208:211], v[0:3]\n"
        "ds_read_b64_tr_b16 v[160:161], v226 offset:50176\n"
        "v_accvgpr_read_b32 v179, a163\n"
        "v_mfma_f32_16x16x32_bf16 v[4:7], v[44:47], v[208:211], v[4:7]\n"
        "ds_read_b64_tr_b16 v[162:163], v227 offset:50176\n"
        "v_accvgpr_read_b32 v180, a164\n"
        "v_mfma_f32_16x16x32_bf16 v[4:7], v[40:43], v[192:195], v[4:7]\n"
        "global_load_lds_dwordx4 v244, s[86:87] offset:2048\n"
        "s_waitcnt lgkmcnt(4)\n"
        "v_mfma_f32_16x16x32_bf16 v[68:71], v[40:43], v[196:199], v[68:71]\n"
        "ds_read_b64_tr_b16 v[164:165], v226 offset:58368\n"
        "v_accvgpr_read_b32 v181, a165\n"
        "v_mfma_f32_16x16x32_bf16 v[68:71], v[44:47], v[212:215], v[68:71]\n"
        "ds_read_b64_tr_b16 v[166:167], v227 offset:58368\n"
        "v_accvgpr_read_b32 v182, a166\n"
        "v_mfma_f32_16x16x32_bf16 v[64:67], v[36:39], v[212:215], v[64:67]\n"
        "ds_read_b64_tr_b16 v[172:173], v228 offset:58368\n"
        "v_accvgpr_read_b32 v183, a167\n"
        "v_mfma_f32_16x16x32_bf16 v[64:67], v[32:35], v[196:199], v[64:67]\n"
        "ds_read_b64_tr_b16 v[174:175], v229 offset:58368\n"
        "v_mul_f32 v177, s88, v177\n"
        "v_mfma_f32_16x16x32_bf16 v[96:99], v[32:35], v[200:203], v[96:99]\n"
        "ds_read_b64_tr_b16 v[168:169], v228 offset:50176\n"
        "global_load_lds_dwordx4 v245, s[86:87] offset:3072\n"
        "s_add_u32 m0, s72, 0x1000\n"
        "v_mfma_f32_16x16x32_bf16 v[96:99], v[36:39], v[216:219], v[96:99]\n"
        "ds_read_b64_tr_b16 v[170:171], v229 offset:50176\n"
        "s_add_u32 s86, s66, 0x180\n"
        "s_addc_u32 s87, s67, 0\n"
        "v_mfma_f32_16x16x32_bf16 v[100:103], v[44:47], v[216:219], v[100:103]\n"
        "s_waitcnt lgkmcnt(6)\n"
        "v_mul_f32 v178, s88, v178\n"
        "v_mul_f32 v179, s88, v179\n"
        "v_mfma_f32_16x16x32_bf16 v[100:103], v[40:43], v[200:203], v[100:103]\n"
        "v_mul_f32 v180, s88, v180\n"
        "v_mul_f32 v181, s88, v181\n"
        "v_mfma_f32_16x16x32_bf16 v[132:135], v[40:43], v[204:207], v[132:135]\n"
        "ds_read_b64_tr_b16 v[40:41], v232 offset:50176\n"
        "v_mul_f32 v182, s88, v182\n"
        "v_mfma_f32_16x16x32_bf16 v[132:135], v[44:47], v[220:223], v[132:135]\n"
        "global_load_lds_dwordx4 v242, s[86:87] offset:0\n"
        "v_mfma_f32_16x16x32_bf16 v[128:131], v[36:39], v[220:223], v[128:131]\n"
        "ds_read_b64_tr_b16 v[36:37], v230 offset:58368\n"
        "v_mul_f32 v183, s88, v183\n"
        "v_mfma_f32_16x16x32_bf16 v[128:131], v[32:35], v[204:207], v[128:131]\n"
        "ds_read_b64_tr_b16 v[32:33], v230 offset:50176\n"
        "v_accvgpr_read_b32 v184, a200\n"
        "v_mfma_f32_16x16x32_bf16 v[8:11], v[48:51], v[192:195], v[8:11]\n"
        "ds_read_b64_tr_b16 v[34:35], v231 offset:50176\n"
        "v_mul_f32 v184, s88, v184\n"
        "v_mfma_f32_16x16x32_bf16 v[8:11], v[52:55], v[208:211], v[8:11]\n"
        "ds_read_b64_tr_b16 v[38:39], v231 offset:58368\n"
        "v_cvt_pk_bf16_f32 v184, v184, v185\n"
        "v_mfma_f32_16x16x32_bf16 v[12:15], v[60:63], v[208:211], v[12:15]\n"
        "global_load_lds_dwordx4 v243, s[86:87] offset:1024\n"
        "v_mfma_f32_16x16x32_bf16 v[12:15], v[56:59], v[192:195], v[12:15]\n"
        "ds_read_b64_tr_b16 v[44:45], v232 offset:58368\n"
        "v_cvt_pk_bf16_f32 v185, v186, v187\n"
        "v_mfma_f32_16x16x32_bf16 v[76:79], v[56:59], v[196:199], v[76:79]\n"
        "ds_read_b64_tr_b16 v[46:47], v233 offset:58368\n"
        "v_cvt_pk_bf16_f32 v186, v188, v189\n"
        "v_mfma_f32_16x16x32_bf16 v[76:79], v[60:63], v[212:215], v[76:79]\n"
        "ds_read_b64_tr_b16 v[42:43], v233 offset:50176\n"
        "v_cvt_pk_bf16_f32 v187, v190, v191\n"
        "v_mfma_f32_16x16x32_bf16 v[72:75], v[52:55], v[212:215], v[72:75]\n"
        "v_accvgpr_write_b32 a236, v184\n"
        "v_accvgpr_write_b32 a237, v185\n"
        "v_mfma_f32_16x16x32_bf16 v[72:75], v[48:51], v[196:199], v[72:75]\n"
        "v_accvgpr_write_b32 a238, v186\n"
        "v_accvgpr_write_b32 a239, v187\n"
        "v_mfma_f32_16x16x32_bf16 v[104:107], v[48:51], v[200:203], v[104:107]\n"
        "global_load_lds_dwordx4 v244, s[86:87] offset:2048\n"
        "v_accvgpr_read_b32 v185, a169\n"
        "v_mfma_f32_16x16x32_bf16 v[104:107], v[52:55], v[216:219], v[104:107]\n"
        "v_accvgpr_read_b32 v186, a170\n"
        "v_accvgpr_read_b32 v187, a171\n"
        "v_mfma_f32_16x16x32_bf16 v[108:111], v[60:63], v[216:219], v[108:111]\n"
        "v_accvgpr_read_b32 v188, a172\n"
        "v_accvgpr_read_b32 v189, a173\n"
        "v_mfma_f32_16x16x32_bf16 v[108:111], v[56:59], v[200:203], v[108:111]\n"
        "v_accvgpr_read_b32 v190, a174\n"
        "v_accvgpr_read_b32 v191, a175\n"
        "v_mfma_f32_16x16x32_bf16 v[140:143], v[56:59], v[204:207], v[140:143]\n"
        "v_mul_f32 v185, s88, v185\n"
        "v_mul_f32 v186, s88, v186\n"
        "v_mfma_f32_16x16x32_bf16 v[140:143], v[60:63], v[220:223], v[140:143]\n"
        "global_load_lds_dwordx4 v245, s[86:87] offset:3072\n"
        "s_waitcnt vmcnt(13) lgkmcnt(0)\n"
        "v_mfma_f32_16x16x32_bf16 v[136:139], v[52:55], v[220:223], v[136:139]\n"
        "s_barrier\n"
        "v_mfma_f32_16x16x32_bf16 v[136:139], v[48:51], v[204:207], v[136:139]\n"
        "s_add_u32 m0, s73, 0x24000\n"
        "s_add_u32 s86, s68, 0x200000\n"
        "s_addc_u32 s87, s69, 0\n"
        "v_mfma_f32_16x16x32_bf16 v[16:19], v[160:163], v[192:195], v[16:19]\n"
        "ds_read_b64_tr_b16 v[48:49], v226 offset:16384\n"
        "v_mul_f32 v187, s88, v187\n"
        "v_mfma_f32_16x16x32_bf16 v[16:19], v[164:167], v[208:211], v[16:19]\n"
        "ds_read_b64_tr_b16 v[50:51], v227 offset:16384\n"
        "v_mul_f32 v188, s88, v188\n"
        "v_mfma_f32_16x16x32_bf16 v[20:23], v[172:175], v[208:211], v[20:23]\n"
        "global_load_lds_dwordx4 v238, s[86:87] offset:0\n"
        "ds_read_b64_tr_b16 v[52:53], v226 offset:24576\n"
        "v_mfma_f32_16x16x32_bf16 v[20:23], v[168:171], v[192:195], v[20:23]\n"
        "ds_read_b64_tr_b16 v[54:55], v227 offset:24576\n"
        "v_mul_f32 v189, s88, v189\n"
        "v_mfma_f32_16x16x32_bf16 v[84:87], v[168:171], v[196:199], v[84:87]\n"
        "ds_read_b64_tr_b16 v[60:61], v228 offset:24576\n"
        "v_mul_f32 v190, s88, v190\n"
        "v_mfma_f32_16x16x32_bf16 v[84:87], v[172:175], v[212:215], v[84:87]\n"
        "ds_read_b64_tr_b16 v[62:63], v229 offset:24576\n"
        "v_mul_f32 v191, s88, v191\n"
        "v_mfma_f32_16x16x32_bf16 v[80:83], v[164:167], v[212:215], v[80:83]\n"
        "ds_read_b64_tr_b16 v[56:57], v228 offset:16384\n"
        "v_mfma_f32_16x16x32_bf16 v[80:83], v[160:163], v[196:199], v[80:83]\n"
        "ds_read_b64_tr_b16 v[58:59], v229 offset:16384\n"
        "global_load_lds_dwordx4 v239, s[86:87] offset:1024\n"
        "v_mfma_f32_16x16x32_bf16 v[112:115], v[160:163], v[200:203], v[112:115]\n"
        "v_accvgpr_read_b32 v176, a160\n"
        "v_mul_f32 v176, s88, v176\n"
        "v_mfma_f32_16x16x32_bf16 v[112:115], v[164:167], v[216:219], v[112:115]\n"
        "v_cvt_pk_bf16_f32 v176, v176, v177\n"
        "v_cvt_pk_bf16_f32 v177, v178, v179\n"
        "v_mfma_f32_16x16x32_bf16 v[116:119], v[172:175], v[216:219], v[116:119]\n"
        "v_cvt_pk_bf16_f32 v178, v180, v181\n"
        "v_cvt_pk_bf16_f32 v179, v182, v183\n"
        "v_mfma_f32_16x16x32_bf16 v[116:119], v[168:171], v[200:203], v[116:119]\n"
        "v_accvgpr_write_b32 a192, v176\n"
        "v_accvgpr_write_b32 a193, v177\n"
        "v_mfma_f32_16x16x32_bf16 v[148:151], v[168:171], v[204:207], v[148:151]\n"
        "ds_read_b64_tr_b16 v[168:169], v232 offset:16384\n"
        "v_accvgpr_write_b32 a194, v178\n"
        "v_mfma_f32_16x16x32_bf16 v[148:151], v[172:175], v[220:223], v[148:151]\n"
        "global_load_lds_dwordx4 v240, s[86:87] offset:2048\n"
        "v_accvgpr_write_b32 a195, v179\n"
        "v_mfma_f32_16x16x32_bf16 v[144:147], v[164:167], v[220:223], v[144:147]\n"
        "ds_read_b64_tr_b16 v[164:165], v230 offset:24576\n"
        "v_accvgpr_read_b32 v177, a1\n"
        "v_mfma_f32_16x16x32_bf16 v[144:147], v[160:163], v[204:207], v[144:147]\n"
        "ds_read_b64_tr_b16 v[160:161], v230 offset:16384\n"
        "v_accvgpr_read_b32 v178, a2\n"
        "v_mfma_f32_16x16x32_bf16 v[24:27], v[32:35], v[192:195], v[24:27]\n"
        "ds_read_b64_tr_b16 v[162:163], v231 offset:16384\n"
        "v_accvgpr_read_b32 v179, a3\n"
        "v_mfma_f32_16x16x32_bf16 v[24:27], v[36:39], v[208:211], v[24:27]\n"
        "global_store_dwordx4 v254, a[228:231], s[70:71] offset:1024 sc1\n"
        "ds_read_b64_tr_b16 v[166:167], v231 offset:24576\n"
        "ds_read_b64_tr_b16 v[172:173], v232 offset:24576\n"
        "v_accvgpr_read_b32 v180, a4\n"
        "v_mfma_f32_16x16x32_bf16 v[28:31], v[44:47], v[208:211], v[28:31]\n"
        "ds_read_b64_tr_b16 v[174:175], v233 offset:24576\n"
        "v_accvgpr_read_b32 v181, a5\n"
        "v_mfma_f32_16x16x32_bf16 v[28:31], v[40:43], v[192:195], v[28:31]\n"
        "ds_read_b64_tr_b16 v[170:171], v233 offset:16384\n"
        "v_accvgpr_read_b32 v182, a6\n"
        "v_mfma_f32_16x16x32_bf16 v[92:95], v[40:43], v[196:199], v[92:95]\n"
        "ds_read_b128 v[192:195], v246 offset:34816\n"
        "v_accvgpr_read_b32 v183, a7\n"
        "v_mfma_f32_16x16x32_bf16 v[92:95], v[44:47], v[212:215], v[92:95]\n"
        "global_load_lds_dwordx4 v241, s[86:87] offset:3072\n"
        "s_add_u32 m0, s72, 0x10000\n"
        "v_mfma_f32_16x16x32_bf16 v[88:91], v[36:39], v[212:215], v[88:91]\n"
        "ds_read_b128 v[208:211], v247 offset:34816\n"
        "s_add_u32 s86, s64, 0x200\n"
        "s_addc_u32 s87, s65, 0\n"
        "v_mfma_f32_16x16x32_bf16 v[88:91], v[32:35], v[196:199], v[88:91]\n"
        "ds_read_b128 v[196:199], v246 offset:32768\n"
        "v_mul_f32 v177, s88, v177\n"
        "v_mfma_f32_16x16x32_bf16 v[120:123], v[32:35], v[200:203], v[120:123]\n"
        "ds_read_b128 v[212:215], v247 offset:32768\n"
        "v_mul_f32 v178, s88, v178\n"
        "v_mfma_f32_16x16x32_bf16 v[120:123], v[36:39], v[216:219], v[120:123]\n"
        "s_add_u32 s70, s38, s96\n"
        "s_addc_u32 s71, s39, 0\n"
        "v_mul_f32 v179, s88, v179\n"
        "v_mfma_f32_16x16x32_bf16 v[124:127], v[44:47], v[216:219], v[124:127]\n"
        "global_load_lds_dwordx4 v242, s[86:87] offset:0\n"
        "v_mfma_f32_16x16x32_bf16 v[124:127], v[40:43], v[200:203], v[124:127]\n"
        "ds_read_b128 v[200:203], v246 offset:38912\n"
        "v_mul_f32 v180, s88, v180\n"
        "v_mfma_f32_16x16x32_bf16 v[156:159], v[40:43], v[204:207], v[156:159]\n"
        "ds_read_b128 v[216:219], v247 offset:38912\n"
        "v_mul_f32 v181, s88, v181\n"
        "v_mfma_f32_16x16x32_bf16 v[156:159], v[44:47], v[220:223], v[156:159]\n"
        "ds_read_b64_tr_b16 v[44:45], v228 offset:25600\n"
        "v_mul_f32 v182, s88, v182\n"
        "v_mfma_f32_16x16x32_bf16 v[152:155], v[36:39], v[220:223], v[152:155]\n"
        "ds_read_b128 v[220:223], v247 offset:36864\n"
        "v_mul_f32 v183, s88, v183\n"
        "v_mfma_f32_16x16x32_bf16 v[152:155], v[32:35], v[204:207], v[152:155]\n"
        "s_waitcnt lgkmcnt(5)\n"
        "global_load_lds_dwordx4 v243, s[86:87] offset:1024\n"
        "s_add_u32 s96, s52, 0x1000\n"
        "v_mfma_f32_16x16x32_bf16 v[0:3], v[48:51], v[192:195], v[0:3]\n"
        "ds_read_b128 v[204:207], v246 offset:36864\n"
        "v_accvgpr_read_b32 v184, a168\n"
        "v_mfma_f32_16x16x32_bf16 v[0:3], v[52:55], v[208:211], v[0:3]\n"
        "s_waitcnt lgkmcnt(5)\n"
        "v_mul_f32 v184, s88, v184\n"
        "v_cvt_pk_bf16_f32 v184, v184, v185\n"
        "v_mfma_f32_16x16x32_bf16 v[4:7], v[60:63], v[208:211], v[4:7]\n"
        "ds_read_b64_tr_b16 v[32:33], v226 offset:17408\n"
        "v_cvt_pk_bf16_f32 v185, v186, v187\n"
        "v_mfma_f32_16x16x32_bf16 v[4:7], v[56:59], v[192:195], v[4:7]\n"
        "ds_read_b64_tr_b16 v[34:35], v227 offset:17408\n"
        "v_cvt_pk_bf16_f32 v186, v188, v189\n"
        "v_mfma_f32_16x16x32_bf16 v[68:71], v[56:59], v[196:199], v[68:71]\n"
        "global_load_lds_dwordx4 v244, s[86:87] offset:2048\n"
        "v_mfma_f32_16x16x32_bf16 v[68:71], v[60:63], v[212:215], v[68:71]\n"
        "s_waitcnt lgkmcnt(4)\n"
        "v_cvt_pk_bf16_f32 v187, v190, v191\n"
        "v_accvgpr_write_b32 a196, v184\n"
        "v_mfma_f32_16x16x32_bf16 v[64:67], v[52:55], v[212:215], v[64:67]\n"
        "ds_read_b64_tr_b16 v[36:37], v226 offset:25600\n"
        "v_accvgpr_write_b32 a197, v185\n"
        "v_mfma_f32_16x16x32_bf16 v[64:67], v[48:51], v[196:199], v[64:67]\n"
        "ds_read_b64_tr_b16 v[38:39], v227 offset:25600\n"
        "v_accvgpr_write_b32 a198, v186\n"
        "v_mfma_f32_16x16x32_bf16 v[96:99], v[48:51], v[200:203], v[96:99]\n"
        "s_waitcnt lgkmcnt(4)\n"
        "v_accvgpr_write_b32 a199, v187\n"
        "v_accvgpr_read_b32 v185, a9\n"
        "v_mfma_f32_16x16x32_bf16 v[96:99], v[52:55], v[216:219], v[96:99]\n"
        "ds_read_b64_tr_b16 v[46:47], v229 offset:25600\n"
        "global_load_lds_dwordx4 v245, s[86:87] offset:3072\n"
        "s_add_u32 m0, s72, 0x11000\n"
        "v_mfma_f32_16x16x32_bf16 v[100:103], v[60:63], v[216:219], v[100:103]\n"
        "ds_read_b64_tr_b16 v[40:41], v228 offset:17408\n"
        "s_add_u32 s86, s66, 0x200\n"
        "s_addc_u32 s87, s67, 0\n"
        "v_mfma_f32_16x16x32_bf16 v[100:103], v[56:59], v[200:203], v[100:103]\n"
        "ds_read_b64_tr_b16 v[42:43], v229 offset:17408\n"
        "v_accvgpr_read_b32 v186, a10\n"
        "v_mfma_f32_16x16x32_bf16 v[132:135], v[56:59], v[204:207], v[132:135]\n"
        "ds_read_b64_tr_b16 v[56:57], v232 offset:17408\n"
        "v_accvgpr_read_b32 v187, a11\n"
        "v_mfma_f32_16x16x32_bf16 v[132:135], v[60:63], v[220:223], v[132:135]\n"
        "ds_read_b64_tr_b16 v[60:61], v232 offset:25600\n"
        "v_accvgpr_read_b32 v188, a12\n"
        "v_mfma_f32_16x16x32_bf16 v[128:131], v[52:55], v[220:223], v[128:131]\n"
        "global_load_lds_dwordx4 v242, s[86:87] offset:0\n"
        "v_mfma_f32_16x16x32_bf16 v[128:131], v[48:51], v[204:207], v[128:131]\n"
        "ds_read_b64_tr_b16 v[48:49], v230 offset:17408\n"
        "v_accvgpr_read_b32 v189, a13\n"
        "v_mfma_f32_16x16x32_bf16 v[8:11], v[160:163], v[192:195], v[8:11]\n"
        "ds_read_b64_tr_b16 v[50:51], v231 offset:17408\n"
        "v_accvgpr_read_b32 v190, a14\n"
        "v_mfma_f32_16x16x32_bf16 v[8:11], v[164:167], v[208:211], v[8:11]\n"
        "ds_read_b64_tr_b16 v[52:53], v230 offset:25600\n"
        "v_accvgpr_read_b32 v191, a15\n"
        "v_mfma_f32_16x16x32_bf16 v[12:15], v[172:175], v[208:211], v[12:15]\n"
        "ds_read_b64_tr_b16 v[54:55], v231 offset:25600\n"
        "v_mul_f32 v185, s88, v185\n"
        "v_mfma_f32_16x16x32_bf16 v[12:15], v[168:171], v[192:195], v[12:15]\n"
        "global_load_lds_dwordx4 v243, s[86:87] offset:1024\n"
        "v_mfma_f32_16x16x32_bf16 v[76:79], v[168:171], v[196:199], v[76:79]\n"
        "ds_read_b64_tr_b16 v[62:63], v233 offset:25600\n"
        "v_mul_f32 v186, s88, v186\n"
        "v_mfma_f32_16x16x32_bf16 v[76:79], v[172:175], v[212:215], v[76:79]\n"
        "ds_read_b64_tr_b16 v[58:59], v233 offset:17408\n"
        "v_mul_f32 v187, s88, v187\n"
        "v_mfma_f32_16x16x32_bf16 v[72:75], v[164:167], v[212:215], v[72:75]\n"
        "v_mul_f32 v188, s88, v188\n"
        "v_mul_f32 v189, s88, v189\n"
        "v_mfma_f32_16x16x32_bf16 v[72:75], v[160:163], v[196:199], v[72:75]\n"
        "v_accvgpr_read_b32 v176, a0\n"
        "v_mul_f32 v176, s88, v176\n"
        "v_mfma_f32_16x16x32_bf16 v[104:107], v[160:163], v[200:203], v[104:107]\n"
        "global_load_lds_dwordx4 v244, s[86:87] offset:2048\n"
        "v_mfma_f32_16x16x32_bf16 v[104:107], v[164:167], v[216:219], v[104:107]\n"
        "v_cvt_pk_bf16_f32 v176, v176, v177\n"
        "v_cvt_pk_bf16_f32 v177, v178, v179\n"
        "v_mfma_f32_16x16x32_bf16 v[108:111], v[172:175], v[216:219], v[108:111]\n"
        "v_cvt_pk_bf16_f32 v178, v180, v181\n"
        "v_cvt_pk_bf16_f32 v179, v182, v183\n"
        "v_mfma_f32_16x16x32_bf16 v[108:111], v[168:171], v[200:203], v[108:111]\n"
        "v_accvgpr_write_b32 a200, v176\n"
        "v_accvgpr_write_b32 a201, v177\n"
        "v_mfma_f32_16x16x32_bf16 v[140:143], v[168:171], v[204:207], v[140:143]\n"
        "s_waitcnt lgkmcnt(1)\n"
        "v_accvgpr_write_b32 a202, v178\n"
        "v_accvgpr_write_b32 a203, v179\n"
        "v_mfma_f32_16x16x32_bf16 v[140:143], v[172:175], v[220:223], v[140:143]\n"
        "global_load_lds_dwordx4 v245, s[86:87] offset:3072\n"
        "s_add_u32 m0, s73, 0x1c000\n"
        "v_mfma_f32_16x16x32_bf16 v[136:139], v[164:167], v[220:223], v[136:139]\n"
        "s_waitcnt vmcnt(13) lgkmcnt(0)\n"
        "s_add_u32 s86, s68, 0x280000\n"
        "s_addc_u32 s87, s69, 0\n"
        "v_mfma_f32_16x16x32_bf16 v[136:139], v[160:163], v[204:207], v[136:139]\n"
        "s_barrier\n"
        "v_mfma_f32_16x16x32_bf16 v[16:19], v[32:35], v[192:195], v[16:19]\n"
        "ds_read_b64_tr_b16 v[160:161], v226 offset:32768\n"
        "v_mul_f32 v190, s88, v190\n"
        "v_mfma_f32_16x16x32_bf16 v[16:19], v[36:39], v[208:211], v[16:19]\n"
        "ds_read_b64_tr_b16 v[162:163], v227 offset:32768\n"
        "v_mul_f32 v191, s88, v191\n"
        "v_mfma_f32_16x16x32_bf16 v[20:23], v[44:47], v[208:211], v[20:23]\n"
        "global_load_lds_dwordx4 v238, s[86:87] offset:0\n"
        "v_mfma_f32_16x16x32_bf16 v[20:23], v[40:43], v[192:195], v[20:23]\n"
        "ds_read_b64_tr_b16 v[164:165], v226 offset:40960\n"
        "v_accvgpr_read_b32 v177, a33\n"
        "v_mfma_f32_16x16x32_bf16 v[84:87], v[40:43], v[196:199], v[84:87]\n"
        "ds_read_b64_tr_b16 v[166:167], v227 offset:40960\n"
        "v_accvgpr_read_b32 v178, a34\n"
        "v_mfma_f32_16x16x32_bf16 v[84:87], v[44:47], v[212:215], v[84:87]\n"
        "ds_read_b64_tr_b16 v[172:173], v228 offset:40960\n"
        "v_accvgpr_read_b32 v179, a35\n"
        "v_mfma_f32_16x16x32_bf16 v[80:83], v[36:39], v[212:215], v[80:83]\n"
        "ds_read_b64_tr_b16 v[174:175], v229 offset:40960\n"
        "v_accvgpr_read_b32 v180, a36\n"
        "v_mfma_f32_16x16x32_bf16 v[80:83], v[32:35], v[196:199], v[80:83]\n"
        "ds_read_b64_tr_b16 v[168:169], v228 offset:32768\n"
        "global_load_lds_dwordx4 v239, s[86:87] offset:1024\n"
        "v_mfma_f32_16x16x32_bf16 v[112:115], v[32:35], v[200:203], v[112:115]\n"
        "ds_read_b64_tr_b16 v[170:171], v229 offset:32768\n"
        "v_accvgpr_read_b32 v181, a37\n"
        "v_mfma_f32_16x16x32_bf16 v[112:115], v[36:39], v[216:219], v[112:115]\n"
        "v_accvgpr_read_b32 v182, a38\n"
        "v_accvgpr_read_b32 v183, a39\n"
        "v_mfma_f32_16x16x32_bf16 v[116:119], v[44:47], v[216:219], v[116:119]\n"
        "v_mul_f32 v177, s88, v177\n"
        "v_mul_f32 v178, s88, v178\n"
        "v_mfma_f32_16x16x32_bf16 v[116:119], v[40:43], v[200:203], v[116:119]\n"
        "v_mul_f32 v179, s88, v179\n"
        "v_mul_f32 v180, s88, v180\n"
        "v_mfma_f32_16x16x32_bf16 v[148:151], v[40:43], v[204:207], v[148:151]\n"
        "global_load_lds_dwordx4 v240, s[86:87] offset:2048\n"
        "v_mfma_f32_16x16x32_bf16 v[148:151], v[44:47], v[220:223], v[148:151]\n"
        "ds_read_b64_tr_b16 v[44:45], v232 offset:40960\n"
        "v_mul_f32 v181, s88, v181\n"
        "v_mfma_f32_16x16x32_bf16 v[144:147], v[36:39], v[220:223], v[144:147]\n"
        "ds_read_b64_tr_b16 v[36:37], v230 offset:40960\n"
        "v_mul_f32 v182, s88, v182\n"
        "v_mfma_f32_16x16x32_bf16 v[144:147], v[32:35], v[204:207], v[144:147]\n"
        "ds_read_b64_tr_b16 v[32:33], v230 offset:32768\n"
        "v_accvgpr_read_b32 v184, a8\n"
        "v_mfma_f32_16x16x32_bf16 v[24:27], v[48:51], v[192:195], v[24:27]\n"
        "ds_read_b64_tr_b16 v[34:35], v231 offset:32768\n"
        "v_mul_f32 v184, s88, v184\n"
        "v_mfma_f32_16x16x32_bf16 v[24:27], v[52:55], v[208:211], v[24:27]\n"
        "global_load_lds_dwordx4 v241, s[86:87] offset:3072\n"
        "s_add_u32 m0, s72, 0x8000\n"
        "v_mfma_f32_16x16x32_bf16 v[28:31], v[60:63], v[208:211], v[28:31]\n"
        "ds_read_b64_tr_b16 v[38:39], v231 offset:40960\n"
        "v_cvt_pk_bf16_f32 v184, v184, v185\n"
        "v_mfma_f32_16x16x32_bf16 v[28:31], v[56:59], v[192:195], v[28:31]\n"
        "ds_read_b64_tr_b16 v[46:47], v233 offset:40960\n"
        "v_cvt_pk_bf16_f32 v185, v186, v187\n"
        "v_mfma_f32_16x16x32_bf16 v[92:95], v[56:59], v[196:199], v[92:95]\n"
        "ds_read_b64_tr_b16 v[40:41], v232 offset:32768\n"
        "v_cvt_pk_bf16_f32 v186, v188, v189\n"
        "v_mfma_f32_16x16x32_bf16 v[92:95], v[60:63], v[212:215], v[92:95]\n"
        "ds_read_b64_tr_b16 v[42:43], v233 offset:32768\n"
        "v_cvt_pk_bf16_f32 v187, v190, v191\n"
        "v_mfma_f32_16x16x32_bf16 v[88:91], v[52:55], v[212:215], v[88:91]\n"
        "ds_read_b128 v[192:195], v246 offset:2048\n"
        "v_accvgpr_write_b32 a204, v184\n"
        "v_mfma_f32_16x16x32_bf16 v[88:91], v[48:51], v[196:199], v[88:91]\n"
        "ds_read_b128 v[208:211], v247 offset:2048\n"
        "v_accvgpr_write_b32 a205, v185\n"
        "v_mfma_f32_16x16x32_bf16 v[120:123], v[48:51], v[200:203], v[120:123]\n"
        "ds_read_b128 v[196:199], v246 offset:0\n"
        "v_accvgpr_write_b32 a206, v186\n"
        "v_mfma_f32_16x16x32_bf16 v[120:123], v[52:55], v[216:219], v[120:123]\n"
        "ds_read_b128 v[212:215], v247 offset:0\n"
        "v_accvgpr_write_b32 a207, v187\n"
        "v_mfma_f32_16x16x32_bf16 v[124:127], v[60:63], v[216:219], v[124:127]\n"
        "s_add_u32 s86, s64, 0x280\n"
        "s_addc_u32 s87, s65, 0\n"
        "v_mul_f32 v183, s88, v183\n"
        "v_mfma_f32_16x16x32_bf16 v[124:127], v[56:59], v[200:203], v[124:127]\n"
        "global_load_lds_dwordx4 v242, s[86:87] offset:0\n"
        "ds_read_b128 v[200:203], v246 offset:6144\n"
        "v_mfma_f32_16x16x32_bf16 v[156:159], v[56:59], v[204:207], v[156:159]\n"
        "ds_read_b128 v[216:219], v247 offset:6144\n"
        "v_accvgpr_read_b32 v185, a41\n"
        "v_mfma_f32_16x16x32_bf16 v[156:159], v[60:63], v[220:223], v[156:159]\n"
        "ds_read_b64_tr_b16 v[60:61], v228 offset:41984\n"
        "v_accvgpr_read_b32 v186, a42\n"
        "v_mfma_f32_16x16x32_bf16 v[152:155], v[52:55], v[220:223], v[152:155]\n"
        "ds_read_b128 v[220:223], v247 offset:4096\n"
        "v_accvgpr_read_b32 v187, a43\n"
        "v_mfma_f32_16x16x32_bf16 v[152:155], v[48:51], v[204:207], v[152:155]\n"
        "ds_read_b128 v[204:207], v246 offset:4096\n"
        "s_waitcnt lgkmcnt(7)\n"
        "v_mfma_f32_16x16x32_bf16 v[0:3], v[160:163], v[192:195], v[0:3]\n"
        "global_store_dwordx4 v254, a[232:235], s[70:71] offset:0 sc1\n"
        "ds_read_b64_tr_b16 v[48:49], v226 offset:33792\n"
        "ds_read_b64_tr_b16 v[50:51], v227 offset:33792\n"
        "v_accvgpr_read_b32 v188, a44\n"
        "v_mfma_f32_16x16x32_bf16 v[0:3], v[164:167], v[208:211], v[0:3]\n"
        "ds_read_b64_tr_b16 v[52:53], v226 offset:41984\n"
        "v_accvgpr_read_b32 v189, a45\n"
        "v_mfma_f32_16x16x32_bf16 v[4:7], v[172:175], v[208:211], v[4:7]\n"
        "ds_read_b64_tr_b16 v[54:55], v227 offset:41984\n"
        "v_accvgpr_read_b32 v190, a46\n"
        "v_mfma_f32_16x16x32_bf16 v[4:7], v[168:171], v[192:195], v[4:7]\n"
        "ds_read_b64_tr_b16 v[62:63], v229 offset:41984\n"
        "s_waitcnt lgkmcnt(8)\n"
        "v_mfma_f32_16x16x32_bf16 v[68:71], v[168:171], v[196:199], v[68:71]\n"
        "global_load_lds_dwordx4 v243, s[86:87] offset:1024\n"
        "v_mfma_f32_16x16x32_bf16 v[68:71], v[172:175], v[212:215], v[68:71]\n"
        "ds_read_b64_tr_b16 v[56:57], v228 offset:33792\n"
        "v_accvgpr_read_b32 v191, a47\n"
        "v_mfma_f32_16x16x32_bf16 v[64:67], v[164:167], v[212:215], v[64:67]\n"
        "ds_read_b64_tr_b16 v[58:59], v229 offset:33792\n"
        "v_mul_f32 v185, s88, v185\n"
        "v_mfma_f32_16x16x32_bf16 v[64:67], v[160:163], v[196:199], v[64:67]\n"
        "v_accvgpr_read_b32 v176, a32\n"
        "v_mul_f32 v176, s88, v176\n"
        "v_mfma_f32_16x16x32_bf16 v[96:99], v[160:163], v[200:203], v[96:99]\n"
        "s_waitcnt lgkmcnt(5)\n"
        "v_cvt_pk_bf16_f32 v176, v176, v177\n"
        "v_cvt_pk_bf16_f32 v177, v178, v179\n"
        "v_mfma_f32_16x16x32_bf16 v[96:99], v[164:167], v[216:219], v[96:99]\n"
        "global_load_lds_dwordx4 v244, s[86:87] offset:2048\n"
        "v_mfma_f32_16x16x32_bf16 v[100:103], v[172:175], v[216:219], v[100:103]\n"
        "v_cvt_pk_bf16_f32 v178, v180, v181\n"
        "v_cvt_pk_bf16_f32 v179, v182, v183\n"
        "v_mfma_f32_16x16x32_bf16 v[100:103], v[168:171], v[200:203], v[100:103]\n"
        "v_accvgpr_write_b32 a160, v176\n"
        "v_accvgpr_write_b32 a161, v177\n"
        "v_mfma_f32_16x16x32_bf16 v[132:135], v[168:171], v[204:207], v[132:135]\n"
        "ds_read_b64_tr_b16 v[168:169], v232 offset:33792\n"
        "v_accvgpr_write_b32 a162, v178\n"
        "v_mfma_f32_16x16x32_bf16 v[132:135], v[172:175], v[220:223], v[132:135]\n"
        "ds_read_b64_tr_b16 v[172:173], v232 offset:41984\n"
        "LHA5_%=:\n"
        "s_cbranch_execz LHA9_%=\n"
        "v_accvgpr_write_b32 a163, v179\n"
        "v_mfma_f32_16x16x32_bf16 v[128:131], v[164:167], v[220:223], v[128:131]\n"
        "global_load_lds_dwordx4 v245, s[86:87] offset:3072\n"
        "s_add_u32 m0, s72, 0x9000\n"
        "v_mfma_f32_16x16x32_bf16 v[128:131], v[160:163], v[204:207], v[128:131]\n"
        "s_add_u32 s86, s66, 0x280\n"
        "s_addc_u32 s87, s67, 0\n"
        "v_mul_f32 v186, s88, v186\n"
        "v_mfma_f32_16x16x32_bf16 v[8:11], v[32:35], v[192:195], v[8:11]\n"
        "ds_read_b64_tr_b16 v[160:161], v230 offset:33792\n"
        "v_mul_f32 v187, s88, v187\n"
        "v_mfma_f32_16x16x32_bf16 v[8:11], v[36:39], v[208:211], v[8:11]\n"
        "ds_read_b64_tr_b16 v[162:163], v231 offset:33792\n"
        "v_mul_f32 v188, s88, v188\n"
        "v_mfma_f32_16x16x32_bf16 v[12:15], v[44:47], v[208:211], v[12:15]\n"
        "ds_read_b64_tr_b16 v[164:165], v230 offset:41984\n"
        "v_mul_f32 v189, s88, v189\n"
        "v_mfma_f32_16x16x32_bf16 v[12:15], v[40:43], v[192:195], v[12:15]\n"
        "global_load_lds_dwordx4 v242, s[86:87] offset:0\n"
        "v_mfma_f32_16x16x32_bf16 v[76:79], v[40:43], v[196:199], v[76:79]\n"
        "ds_read_b64_tr_b16 v[166:167], v231 offset:41984\n"
        "v_mul_f32 v190, s88, v190\n"
        "v_mfma_f32_16x16x32_bf16 v[76:79], v[44:47], v[212:215], v[76:79]\n"
        "ds_read_b64_tr_b16 v[174:175], v233 offset:41984\n"
        "v_mul_f32 v191, s88, v191\n"
        "v_mfma_f32_16x16x32_bf16 v[72:75], v[36:39], v[212:215], v[72:75]\n"
        "ds_read_b64_tr_b16 v[170:171], v233 offset:33792\n"
        "v_accvgpr_read_b32 v177, a65\n"
        "v_mfma_f32_16x16x32_bf16 v[72:75], v[32:35], v[196:199], v[72:75]\n"
        "v_accvgpr_read_b32 v178, a66\n"
        "v_accvgpr_read_b32 v179, a67\n"
        "v_mfma_f32_16x16x32_bf16 v[104:107], v[32:35], v[200:203], v[104:107]\n"
        "global_load_lds_dwordx4 v243, s[86:87] offset:1024\n"
        "v_mfma_f32_16x16x32_bf16 v[104:107], v[36:39], v[216:219], v[104:107]\n"
        "v_accvgpr_read_b32 v180, a68\n"
        "v_accvgpr_read_b32 v181, a69\n"
        "v_mfma_f32_16x16x32_bf16 v[108:111], v[44:47], v[216:219], v[108:111]\n"
        "v_accvgpr_read_b32 v182, a70\n"
        "v_accvgpr_read_b32 v183, a71\n"
        "v_mfma_f32_16x16x32_bf16 v[108:111], v[40:43], v[200:203], v[108:111]\n"
        "v_mul_f32 v177, s88, v177\n"
        "v_mul_f32 v178, s88, v178\n"
        "v_mfma_f32_16x16x32_bf16 v[140:143], v[40:43], v[204:207], v[140:143]\n"
        "v_mul_f32 v179, s88, v179\n"
        "v_mul_f32 v180, s88, v180\n"
        "v_mfma_f32_16x16x32_bf16 v[140:143], v[44:47], v[220:223], v[140:143]\n"
        "global_load_lds_dwordx4 v244, s[86:87] offset:2048\n"
        "v_mfma_f32_16x16x32_bf16 v[136:139], v[36:39], v[220:223], v[136:139]\n"
        "s_waitcnt vmcnt(12) lgkmcnt(0)\n"
        "v_mul_f32 v181, s88, v181\n"
        "v_mul_f32 v182, s88, v182\n"
        "v_mfma_f32_16x16x32_bf16 v[136:139], v[32:35], v[204:207], v[136:139]\n"
        "v_accvgpr_read_b32 v184, a40\n"
        "v_mul_f32 v184, s88, v184\n"
        "v_mfma_f32_16x16x32_bf16 v[16:19], v[48:51], v[192:195], v[16:19]\n"
        "v_cvt_pk_bf16_f32 v184, v184, v185\n"
        "v_cvt_pk_bf16_f32 v185, v186, v187\n"
        "v_mfma_f32_16x16x32_bf16 v[16:19], v[52:55], v[208:211], v[16:19]\n"
        "v_cvt_pk_bf16_f32 v186, v188, v189\n"
        "v_cvt_pk_bf16_f32 v187, v190, v191\n"
        "v_mfma_f32_16x16x32_bf16 v[20:23], v[60:63], v[208:211], v[20:23]\n"
        "global_load_lds_dwordx4 v245, s[86:87] offset:3072\n"
        "s_add_u32 m0, s73, 0x20000\n"
        "v_mfma_f32_16x16x32_bf16 v[20:23], v[56:59], v[192:195], v[20:23]\n"
        "s_add_u32 s86, s68, 0x300000\n"
        "s_addc_u32 s87, s69, 0\n"
        "v_accvgpr_write_b32 a164, v184\n"
        "v_mfma_f32_16x16x32_bf16 v[84:87], v[56:59], v[196:199], v[84:87]\n"
        "v_accvgpr_write_b32 a165, v185\n"
        "v_accvgpr_write_b32 a166, v186\n"
        "v_mfma_f32_16x16x32_bf16 v[84:87], v[60:63], v[212:215], v[84:87]\n"
        "v_accvgpr_write_b32 a167, v187\n"
        "v_mul_f32 v183, s88, v183\n"
        "v_mfma_f32_16x16x32_bf16 v[80:83], v[52:55], v[212:215], v[80:83]\n"
        "s_barrier\n"
        "v_mfma_f32_16x16x32_bf16 v[80:83], v[48:51], v[196:199], v[80:83]\n"
        "global_load_lds_dwordx4 v238, s[86:87] offset:0\n"
        "v_mfma_f32_16x16x32_bf16 v[112:115], v[48:51], v[200:203], v[112:115]\n"
        "ds_read_b64_tr_b16 v[32:33], v226 offset:49152\n"
        "v_accvgpr_read_b32 v185, a73\n"
        "v_mfma_f32_16x16x32_bf16 v[112:115], v[52:55], v[216:219], v[112:115]\n"
        "ds_read_b64_tr_b16 v[34:35], v227 offset:49152\n"
        "v_accvgpr_read_b32 v186, a74\n"
        "v_mfma_f32_16x16x32_bf16 v[116:119], v[60:63], v[216:219], v[116:119]\n"
        "ds_read_b64_tr_b16 v[36:37], v226 offset:57344\n"
        "v_accvgpr_read_b32 v187, a75\n"
        "v_mfma_f32_16x16x32_bf16 v[116:119], v[56:59], v[200:203], v[116:119]\n"
        "ds_read_b64_tr_b16 v[38:39], v227 offset:57344\n"
        "v_accvgpr_read_b32 v188, a76\n"
        "v_mfma_f32_16x16x32_bf16 v[148:151], v[56:59], v[204:207], v[148:151]\n"
        "ds_read_b64_tr_b16 v[44:45], v228 offset:57344\n"
        "global_load_lds_dwordx4 v239, s[86:87] offset:1024\n"
        "v_mfma_f32_16x16x32_bf16 v[148:151], v[60:63], v[220:223], v[148:151]\n"
        "ds_read_b64_tr_b16 v[46:47], v229 offset:57344\n"
        "v_accvgpr_read_b32 v189, a77\n"
        "v_mfma_f32_16x16x32_bf16 v[144:147], v[52:55], v[220:223], v[144:147]\n"
        "ds_read_b64_tr_b16 v[40:41], v228 offset:49152\n"
        "v_accvgpr_read_b32 v190, a78\n"
        "v_mfma_f32_16x16x32_bf16 v[144:147], v[48:51], v[204:207], v[144:147]\n"
        "ds_read_b64_tr_b16 v[42:43], v229 offset:49152\n"
        "v_accvgpr_read_b32 v191, a79\n"
        "v_mfma_f32_16x16x32_bf16 v[24:27], v[160:163], v[192:195], v[24:27]\n"
        "ds_read_b64_tr_b16 v[48:49], v230 offset:49152\n"
        "v_mul_f32 v185, s88, v185\n"
        "v_mfma_f32_16x16x32_bf16 v[24:27], v[164:167], v[208:211], v[24:27]\n"
        "global_load_lds_dwordx4 v240, s[86:87] offset:2048\n"
        "v_mfma_f32_16x16x32_bf16 v[28:31], v[172:175], v[208:211], v[28:31]\n"
        "ds_read_b64_tr_b16 v[50:51], v231 offset:49152\n"
        "v_mul_f32 v186, s88, v186\n"
        "v_mfma_f32_16x16x32_bf16 v[28:31], v[168:171], v[192:195], v[28:31]\n"
        "ds_read_b64_tr_b16 v[52:53], v230 offset:57344\n"
        "v_mul_f32 v187, s88, v187\n"
        "v_mfma_f32_16x16x32_bf16 v[92:95], v[168:171], v[196:199], v[92:95]\n"
        "ds_read_b64_tr_b16 v[54:55], v231 offset:57344\n"
        "v_mul_f32 v188, s88, v188\n"
        "v_mfma_f32_16x16x32_bf16 v[92:95], v[172:175], v[212:215], v[92:95]\n"
        "ds_read_b64_tr_b16 v[60:61], v232 offset:57344\n"
        "v_mul_f32 v189, s88, v189\n"
        "v_mfma_f32_16x16x32_bf16 v[88:91], v[164:167], v[212:215], v[88:91]\n"
        "global_load_lds_dwordx4 v241, s[86:87] offset:3072\n"
        "s_add_u32 m0, s72, 0x0\n"
        "v_mfma_f32_16x16x32_bf16 v[88:91], v[160:163], v[196:199], v[88:91]\n"
        "ds_read_b64_tr_b16 v[62:63], v233 offset:57344\n"
        "s_add_u32 s86, s64, 0x300\n"
        "s_addc_u32 s87, s65, 0\n"
        "v_mfma_f32_16x16x32_bf16 v[120:123], v[160:163], v[200:203], v[120:123]\n"
        "ds_read_b64_tr_b16 v[56:57], v232 offset:49152\n"
        "v_accvgpr_read_b32 v176, a64\n"
        "v_mfma_f32_16x16x32_bf16 v[120:123], v[164:167], v[216:219], v[120:123]\n"
        "ds_read_b64_tr_b16 v[58:59], v233 offset:49152\n"
        "v_mul_f32 v176, s88, v176\n"
        "v_mfma_f32_16x16x32_bf16 v[124:127], v[172:175], v[216:219], v[124:127]\n"
        "ds_read_b128 v[192:195], v248 offset:2048\n"
        "v_cvt_pk_bf16_f32 v176, v176, v177\n"
        "v_mfma_f32_16x16x32_bf16 v[124:127], v[168:171], v[200:203], v[124:127]\n"
        "ds_read_b128 v[208:211], v249 offset:2048\n"
        "global_load_lds_dwordx4 v242, s[86:87] offset:0\n"
        "v_mfma_f32_16x16x32_bf16 v[156:159], v[168:171], v[204:207], v[156:159]\n"
        "ds_read_b128 v[196:199], v248 offset:0\n"
        "v_cvt_pk_bf16_f32 v177, v178, v179\n"
        "v_mfma_f32_16x16x32_bf16 v[156:159], v[172:175], v[220:223], v[156:159]\n"
        "ds_read_b128 v[212:215], v249 offset:0\n"
        "v_cvt_pk_bf16_f32 v178, v180, v181\n"
        "v_mfma_f32_16x16x32_bf16 v[152:155], v[164:167], v[220:223], v[152:155]\n"
        "ds_read_b128 v[200:203], v248 offset:6144\n"
        "v_cvt_pk_bf16_f32 v179, v182, v183\n"
        "v_mfma_f32_16x16x32_bf16 v[152:155], v[160:163], v[204:207], v[152:155]\n"
        "global_load_lds_dwordx4 v243, s[86:87] offset:1024\n"
        "ds_read_b128 v[216:219], v249 offset:6144\n"
        "ds_read_b128 v[204:207], v248 offset:4096\n"
        "v_accvgpr_write_b32 a168, v176\n"
        "s_waitcnt lgkmcnt(6)\n"
        "v_mfma_f32_16x16x32_bf16 v[0:3], v[32:35], v[192:195], v[0:3]\n"
        "v_accvgpr_write_b32 a169, v177\n"
        "s_waitcnt lgkmcnt(5)\n"
        "v_mfma_f32_16x16x32_bf16 v[0:3], v[36:39], v[208:211], v[0:3]\n"
        "ds_read_b128 v[220:223], v249 offset:4096\n"
        "v_accvgpr_write_b32 a170, v178\n"
        "v_mfma_f32_16x16x32_bf16 v[4:7], v[44:47], v[208:211], v[4:7]\n"
        "s_waitcnt lgkmcnt(5)\n"
        "v_accvgpr_write_b32 a171, v179\n"
        "v_mul_f32 v190, s88, v190\n"
        "v_mfma_f32_16x16x32_bf16 v[4:7], v[40:43], v[192:195], v[4:7]\n"
        "global_store_dwordx4 v254, a[236:239], s[70:71] offset:1024 sc1\n"
        "ds_read_b64_tr_b16 v[160:161], v226 offset:50176\n"
        "ds_read_b64_tr_b16 v[162:163], v227 offset:50176\n"
        "v_mul_f32 v191, s88, v191\n"
        "v_mfma_f32_16x16x32_bf16 v[68:71], v[40:43], v[196:199], v[68:71]\n"
        "ds_read_b64_tr_b16 v[164:165], v226 offset:58368\n"
        "s_waitcnt lgkmcnt(6)\n"
        "v_mfma_f32_16x16x32_bf16 v[68:71], v[44:47], v[212:215], v[68:71]\n"
        "ds_read_b64_tr_b16 v[166:167], v227 offset:58368\n"
        "v_accvgpr_read_b32 v177, a97\n"
        "v_mfma_f32_16x16x32_bf16 v[64:67], v[36:39], v[212:215], v[64:67]\n"
        "ds_read_b64_tr_b16 v[172:173], v228 offset:58368\n"
        "v_accvgpr_read_b32 v178, a98\n"
        "v_mfma_f32_16x16x32_bf16 v[64:67], v[32:35], v[196:199], v[64:67]\n"
        "global_load_lds_dwordx4 v244, s[86:87] offset:2048\n"
        "v_mfma_f32_16x16x32_bf16 v[96:99], v[32:35], v[200:203], v[96:99]\n"
        "ds_read_b64_tr_b16 v[174:175], v229 offset:58368\n"
        "s_waitcnt lgkmcnt(6)\n"
        "v_mfma_f32_16x16x32_bf16 v[96:99], v[36:39], v[216:219], v[96:99]\n"
        "ds_read_b64_tr_b16 v[168:169], v228 offset:50176\n"
        "v_accvgpr_read_b32 v179, a99\n"
        "v_mfma_f32_16x16x32_bf16 v[100:103], v[44:47], v[216:219], v[100:103]\n"
        "ds_read_b64_tr_b16 v[170:171], v229 offset:50176\n"
        "v_accvgpr_read_b32 v180, a100\n"
        "v_mfma_f32_16x16x32_bf16 v[100:103], v[40:43], v[200:203], v[100:103]\n"
        "s_add_u32 s70, s38, s96\n"
        "s_addc_u32 s71, s39, 0\n"
        "v_accvgpr_read_b32 v181, a101\n"
        "v_mfma_f32_16x16x32_bf16 v[132:135], v[40:43], v[204:207], v[132:135]\n"
        "global_load_lds_dwordx4 v245, s[86:87] offset:3072\n"
        "s_add_u32 m0, s72, 0x1000\n"
        "v_mfma_f32_16x16x32_bf16 v[132:135], v[44:47], v[220:223], v[132:135]\n"
        "s_add_u32 s86, s66, 0x300\n"
        "s_addc_u32 s87, s67, 0\n"
        "v_accvgpr_read_b32 v182, a102\n"
        "v_mfma_f32_16x16x32_bf16 v[128:131], v[36:39], v[220:223], v[128:131]\n"
        "ds_read_b64_tr_b16 v[36:37], v230 offset:58368\n"
        "s_add_u32 s96, s52, 0x0\n"
        "v_accvgpr_read_b32 v183, a103\n"
        "v_mfma_f32_16x16x32_bf16 v[128:131], v[32:35], v[204:207], v[128:131]\n"
        "ds_read_b64_tr_b16 v[32:33], v230 offset:50176\n"
        "s_add_u32 s96, s96, s75\n"
        "v_accvgpr_read_b32 v184, a72\n"
        "v_mfma_f32_16x16x32_bf16 v[8:11], v[48:51], v[192:195], v[8:11]\n"
        "ds_read_b64_tr_b16 v[34:35], v231 offset:50176\n"
        "v_mul_f32 v184, s88, v184\n"
        "v_mfma_f32_16x16x32_bf16 v[8:11], v[52:55], v[208:211], v[8:11]\n"
        "global_load_lds_dwordx4 v242, s[86:87] offset:0\n"
        "v_mfma_f32_16x16x32_bf16 v[12:15], v[60:63], v[208:211], v[12:15]\n"
        "ds_read_b64_tr_b16 v[38:39], v231 offset:58368\n"
        "v_cvt_pk_bf16_f32 v184, v184, v185\n"
        "v_mfma_f32_16x16x32_bf16 v[12:15], v[56:59], v[192:195], v[12:15]\n"
        "ds_read_b64_tr_b16 v[44:45], v232 offset:58368\n"
        "v_cvt_pk_bf16_f32 v185, v186, v187\n"
        "v_mfma_f32_16x16x32_bf16 v[76:79], v[56:59], v[196:199], v[76:79]\n"
        "ds_read_b64_tr_b16 v[46:47], v233 offset:58368\n"
        "v_cvt_pk_bf16_f32 v186, v188, v189\n"
        "v_mfma_f32_16x16x32_bf16 v[76:79], v[60:63], v[212:215], v[76:79]\n"
        "ds_read_b64_tr_b16 v[40:41], v232 offset:50176\n"
        "v_cvt_pk_bf16_f32 v187, v190, v191\n"
        "v_mfma_f32_16x16x32_bf16 v[72:75], v[52:55], v[212:215], v[72:75]\n"
        "global_load_lds_dwordx4 v243, s[86:87] offset:1024\n"
        "v_mfma_f32_16x16x32_bf16 v[72:75], v[48:51], v[196:199], v[72:75]\n"
        "ds_read_b64_tr_b16 v[42:43], v233 offset:50176\n"
        "v_accvgpr_write_b32 a172, v184\n"
        "v_mfma_f32_16x16x32_bf16 v[104:107], v[48:51], v[200:203], v[104:107]\n"
        "v_accvgpr_write_b32 a173, v185\n"
        "v_accvgpr_write_b32 a174, v186\n"
        "v_mfma_f32_16x16x32_bf16 v[104:107], v[52:55], v[216:219], v[104:107]\n"
        "v_accvgpr_write_b32 a175, v187\n"
        "v_mul_f32 v177, s88, v177\n"
        "v_mfma_f32_16x16x32_bf16 v[108:111], v[60:63], v[216:219], v[108:111]\n"
        "v_mul_f32 v178, s88, v178\n"
        "v_mul_f32 v179, s88, v179\n"
        "v_mfma_f32_16x16x32_bf16 v[108:111], v[56:59], v[200:203], v[108:111]\n"
        "global_load_lds_dwordx4 v244, s[86:87] offset:2048\n"
        "v_mfma_f32_16x16x32_bf16 v[140:143], v[56:59], v[204:207], v[140:143]\n"
        "s_waitcnt lgkmcnt(2)\n"
        "v_mul_f32 v180, s88, v180\n"
        "v_mul_f32 v181, s88, v181\n"
        "v_mfma_f32_16x16x32_bf16 v[140:143], v[60:63], v[220:223], v[140:143]\n"
        "v_mul_f32 v182, s88, v182\n"
        "v_mul_f32 v183, s88, v183\n"
        "v_mfma_f32_16x16x32_bf16 v[136:139], v[52:55], v[220:223], v[136:139]\n"
        "v_accvgpr_read_b32 v185, a105\n"
        "v_accvgpr_read_b32 v186, a106\n"
        "v_mfma_f32_16x16x32_bf16 v[136:139], v[48:51], v[204:207], v[136:139]\n"
        "s_waitcnt vmcnt(12) lgkmcnt(0)\n"
        "v_accvgpr_read_b32 v187, a107\n"
        "v_accvgpr_read_b32 v188, a108\n"
        "v_mfma_f32_16x16x32_bf16 v[16:19], v[160:163], v[192:195], v[16:19]\n"
        "global_load_lds_dwordx4 v245, s[86:87] offset:3072\n"
        "s_add_u32 m0, s73, 0x24000\n"
        "v_mfma_f32_16x16x32_bf16 v[16:19], v[164:167], v[208:211], v[16:19]\n"
        "s_barrier\n"
        "v_mfma_f32_16x16x32_bf16 v[20:23], v[172:175], v[208:211], v[20:23]\n"
        "s_add_u32 s86, s68, 0x380000\n"
        "s_addc_u32 s87, s69, 0\n"
        "v_accvgpr_read_b32 v189, a109\n"
        "v_mfma_f32_16x16x32_bf16 v[20:23], v[168:171], v[192:195], v[20:23]\n"
        "ds_read_b64_tr_b16 v[48:49], v226 offset:16384\n"
        "v_accvgpr_read_b32 v190, a110\n"
        "v_mfma_f32_16x16x32_bf16 v[84:87], v[168:171], v[196:199], v[84:87]\n"
        "ds_read_b64_tr_b16 v[50:51], v227 offset:16384\n"
        "v_accvgpr_read_b32 v191, a111\n"
        "v_mfma_f32_16x16x32_bf16 v[84:87], v[172:175], v[212:215], v[84:87]\n"
        "global_load_lds_dwordx4 v238, s[86:87] offset:0\n"
        "v_mfma_f32_16x16x32_bf16 v[80:83], v[164:167], v[212:215], v[80:83]\n"
        "ds_read_b64_tr_b16 v[52:53], v226 offset:24576\n"
        "v_mul_f32 v185, s88, v185\n"
        "v_mfma_f32_16x16x32_bf16 v[80:83], v[160:163], v[196:199], v[80:83]\n"
        "ds_read_b64_tr_b16 v[54:55], v227 offset:24576\n"
        "v_accvgpr_read_b32 v176, a96\n"
        "v_mfma_f32_16x16x32_bf16 v[112:115], v[160:163], v[200:203], v[112:115]\n"
        "ds_read_b64_tr_b16 v[60:61], v228 offset:24576\n"
        "v_mul_f32 v176, s88, v176\n"
        "v_mfma_f32_16x16x32_bf16 v[112:115], v[164:167], v[216:219], v[112:115]\n"
        "ds_read_b64_tr_b16 v[62:63], v229 offset:24576\n"
        "v_cvt_pk_bf16_f32 v176, v176, v177\n"
        "v_mfma_f32_16x16x32_bf16 v[116:119], v[172:175], v[216:219], v[116:119]\n"
        "ds_read_b64_tr_b16 v[56:57], v228 offset:16384\n"
        "global_load_lds_dwordx4 v239, s[86:87] offset:1024\n"
        "v_mfma_f32_16x16x32_bf16 v[116:119], v[168:171], v[200:203], v[116:119]\n"
        "ds_read_b64_tr_b16 v[58:59], v229 offset:16384\n"
        "v_cvt_pk_bf16_f32 v177, v178, v179\n"
        "v_mfma_f32_16x16x32_bf16 v[148:151], v[168:171], v[204:207], v[148:151]\n"
        "ds_read_b64_tr_b16 v[168:169], v232 offset:16384\n"
        "v_cvt_pk_bf16_f32 v178, v180, v181\n"
        "v_mfma_f32_16x16x32_bf16 v[148:151], v[172:175], v[220:223], v[148:151]\n"
        "ds_read_b64_tr_b16 v[172:173], v232 offset:24576\n"
        "v_cvt_pk_bf16_f32 v179, v182, v183\n"
        "v_mfma_f32_16x16x32_bf16 v[144:147], v[164:167], v[220:223], v[144:147]\n"
        "ds_read_b64_tr_b16 v[164:165], v230 offset:24576\n"
        "v_accvgpr_write_b32 a0, v176\n"
        "v_mfma_f32_16x16x32_bf16 v[144:147], v[160:163], v[204:207], v[144:147]\n"
        "ds_read_b64_tr_b16 v[160:161], v230 offset:16384\n"
        "v_accvgpr_write_b32 a1, v177\n"
        "v_mfma_f32_16x16x32_bf16 v[24:27], v[32:35], v[192:195], v[24:27]\n"
        "global_load_lds_dwordx4 v240, s[86:87] offset:2048\n"
        "v_accvgpr_write_b32 a2, v178\n"
        "v_mfma_f32_16x16x32_bf16 v[24:27], v[36:39], v[208:211], v[24:27]\n"
        "ds_read_b64_tr_b16 v[162:163], v231 offset:16384\n"
        "v_accvgpr_write_b32 a3, v179\n"
        "v_mfma_f32_16x16x32_bf16 v[28:31], v[44:47], v[208:211], v[28:31]\n"
        "ds_read_b64_tr_b16 v[166:167], v231 offset:24576\n"
        "v_mul_f32 v186, s88, v186\n"
        "v_mfma_f32_16x16x32_bf16 v[28:31], v[40:43], v[192:195], v[28:31]\n"
        "ds_read_b64_tr_b16 v[174:175], v233 offset:24576\n"
        "v_mul_f32 v187, s88, v187\n"
        "v_mfma_f32_16x16x32_bf16 v[92:95], v[40:43], v[196:199], v[92:95]\n"
        "ds_read_b64_tr_b16 v[170:171], v233 offset:16384\n"
        "v_mul_f32 v188, s88, v188\n"
        "v_mfma_f32_16x16x32_bf16 v[92:95], v[44:47], v[212:215], v[92:95]\n"
        "global_load_lds_dwordx4 v241, s[86:87] offset:3072\n"
        "ds_read_b128 v[192:195], v246 offset:34816\n"
        "s_add_u32 m0, s72, 0x10000\n"
        "v_mfma_f32_16x16x32_bf16 v[88:91], v[36:39], v[212:215], v[88:91]\n"
        "ds_read_b128 v[208:211], v247 offset:34816\n"
        "s_add_u32 s86, s64, 0x380\n"
        "s_addc_u32 s87, s65, 0\n"
        "v_mfma_f32_16x16x32_bf16 v[88:91], v[32:35], v[196:199], v[88:91]\n"
        "ds_read_b128 v[196:199], v246 offset:32768\n"
        "v_mul_f32 v189, s88, v189\n"
        "v_mfma_f32_16x16x32_bf16 v[120:123], v[32:35], v[200:203], v[120:123]\n"
        "ds_read_b128 v[212:215], v247 offset:32768\n"
        "v_mul_f32 v190, s88, v190\n"
        "v_mfma_f32_16x16x32_bf16 v[120:123], v[36:39], v[216:219], v[120:123]\n"
        "v_mul_f32 v191, s88, v191\n"
        "v_accvgpr_read_b32 v177, a129\n"
        "v_mfma_f32_16x16x32_bf16 v[124:127], v[44:47], v[216:219], v[124:127]\n"
        "global_load_lds_dwordx4 v242, s[86:87] offset:0\n"
        "ds_read_b128 v[216:219], v247 offset:38912\n"
        "v_mfma_f32_16x16x32_bf16 v[124:127], v[40:43], v[200:203], v[124:127]\n"
        "ds_read_b128 v[200:203], v246 offset:38912\n"
        "v_accvgpr_read_b32 v178, a130\n"
        "v_mfma_f32_16x16x32_bf16 v[156:159], v[40:43], v[204:207], v[156:159]\n"
        "ds_read_b64_tr_b16 v[40:41], v228 offset:17408\n"
        "v_accvgpr_read_b32 v179, a131\n"
        "v_mfma_f32_16x16x32_bf16 v[156:159], v[44:47], v[220:223], v[156:159]\n"
        "ds_read_b64_tr_b16 v[44:45], v228 offset:25600\n"
        "v_accvgpr_read_b32 v180, a132\n"
        "v_mfma_f32_16x16x32_bf16 v[152:155], v[36:39], v[220:223], v[152:155]\n"
        "ds_read_b128 v[220:223], v247 offset:36864\n"
        "v_accvgpr_read_b32 v181, a133\n"
        "v_mfma_f32_16x16x32_bf16 v[152:155], v[32:35], v[204:207], v[152:155]\n"
        "global_load_lds_dwordx4 v243, s[86:87] offset:1024\n"
        "s_waitcnt lgkmcnt(6)\n"
        "v_mfma_f32_16x16x32_bf16 v[0:3], v[48:51], v[192:195], v[0:3]\n"
        "ds_read_b128 v[204:207], v246 offset:36864\n"
        "v_accvgpr_read_b32 v184, a104\n"
        "v_mfma_f32_16x16x32_bf16 v[0:3], v[52:55], v[208:211], v[0:3]\n"
        "s_waitcnt lgkmcnt(6)\n"
        "v_mul_f32 v184, s88, v184\n"
        "v_cvt_pk_bf16_f32 v184, v184, v185\n"
        "v_mfma_f32_16x16x32_bf16 v[4:7], v[60:63], v[208:211], v[4:7]\n"
        "ds_read_b64_tr_b16 v[32:33], v226 offset:17408\n"
        "v_cvt_pk_bf16_f32 v185, v186, v187\n"
        "v_mfma_f32_16x16x32_bf16 v[4:7], v[56:59], v[192:195], v[4:7]\n"
        "ds_read_b64_tr_b16 v[34:35], v227 offset:17408\n"
        "v_cvt_pk_bf16_f32 v186, v188, v189\n"
        "v_mfma_f32_16x16x32_bf16 v[68:71], v[56:59], v[196:199], v[68:71]\n"
        "global_load_lds_dwordx4 v244, s[86:87] offset:2048\n"
        "v_cvt_pk_bf16_f32 v187, v190, v191\n"
        "v_mfma_f32_16x16x32_bf16 v[68:71], v[60:63], v[212:215], v[68:71]\n"
        "s_waitcnt lgkmcnt(4)\n"
        "v_accvgpr_write_b32 a4, v184\n"
        "v_accvgpr_write_b32 a5, v185\n"
        "v_mfma_f32_16x16x32_bf16 v[64:67], v[52:55], v[212:215], v[64:67]\n"
        "ds_read_b64_tr_b16 v[36:37], v226 offset:25600\n"
        "v_accvgpr_write_b32 a6, v186\n"
        "v_mfma_f32_16x16x32_bf16 v[64:67], v[48:51], v[196:199], v[64:67]\n"
        "ds_read_b64_tr_b16 v[38:39], v227 offset:25600\n"
        "v_accvgpr_write_b32 a7, v187\n"
        "v_mfma_f32_16x16x32_bf16 v[96:99], v[48:51], v[200:203], v[96:99]\n"
        "ds_read_b64_tr_b16 v[46:47], v229 offset:25600\n"
        "v_accvgpr_read_b32 v182, a134\n"
        "v_mfma_f32_16x16x32_bf16 v[96:99], v[52:55], v[216:219], v[96:99]\n"
        "ds_read_b64_tr_b16 v[42:43], v229 offset:17408\n"
        "global_load_lds_dwordx4 v245, s[86:87] offset:3072\n"
        "s_add_u32 m0, s72, 0x11000\n"
        "v_mfma_f32_16x16x32_bf16 v[100:103], v[60:63], v[216:219], v[100:103]\n"
        "s_add_u32 s86, s66, 0x380\n"
        "s_addc_u32 s87, s67, 0\n"
        "v_accvgpr_read_b32 v183, a135\n"
        "v_mfma_f32_16x16x32_bf16 v[100:103], v[56:59], v[200:203], v[100:103]\n"
        "s_waitcnt lgkmcnt(4)\n"
        "v_mul_f32 v177, s88, v177\n"
        "v_mul_f32 v178, s88, v178\n"
        "v_mfma_f32_16x16x32_bf16 v[132:135], v[56:59], v[204:207], v[132:135]\n"
        "ds_read_b64_tr_b16 v[56:57], v232 offset:17408\n"
        "v_mul_f32 v179, s88, v179\n"
        "v_mfma_f32_16x16x32_bf16 v[132:135], v[60:63], v[220:223], v[132:135]\n"
        "ds_read_b64_tr_b16 v[60:61], v232 offset:25600\n"
        "v_mul_f32 v180, s88, v180\n"
        "v_mfma_f32_16x16x32_bf16 v[128:131], v[52:55], v[220:223], v[128:131]\n"
        "global_store_dwordx4 v254, a[192:195], s[70:71] offset:0 sc1\n"
        "ds_read_b64_tr_b16 v[52:53], v230 offset:25600\n"
        "ds_read_b64_tr_b16 v[54:55], v231 offset:25600\n"
        "v_mul_f32 v181, s88, v181\n"
        "v_mfma_f32_16x16x32_bf16 v[128:131], v[48:51], v[204:207], v[128:131]\n"
        "ds_read_b64_tr_b16 v[48:49], v230 offset:17408\n"
        "v_mul_f32 v182, s88, v182\n"
        "v_mfma_f32_16x16x32_bf16 v[8:11], v[160:163], v[192:195], v[8:11]\n"
        "ds_read_b64_tr_b16 v[50:51], v231 offset:17408\n"
        "v_mul_f32 v183, s88, v183\n"
        "v_mfma_f32_16x16x32_bf16 v[8:11], v[164:167], v[208:211], v[8:11]\n"
        "ds_read_b64_tr_b16 v[62:63], v233 offset:25600\n"
        "v_accvgpr_read_b32 v185, a137\n"
        "v_mfma_f32_16x16x32_bf16 v[12:15], v[172:175], v[208:211], v[12:15]\n"
        "global_load_lds_dwordx4 v242, s[86:87] offset:0\n"
        "v_mfma_f32_16x16x32_bf16 v[12:15], v[168:171], v[192:195], v[12:15]\n"
        "ds_read_b64_tr_b16 v[58:59], v233 offset:17408\n"
        "v_accvgpr_read_b32 v186, a138\n"
        "v_mfma_f32_16x16x32_bf16 v[76:79], v[168:171], v[196:199], v[76:79]\n"
        "v_accvgpr_read_b32 v187, a139\n"
        "v_accvgpr_read_b32 v188, a140\n"
        "v_mfma_f32_16x16x32_bf16 v[76:79], v[172:175], v[212:215], v[76:79]\n"
        "v_accvgpr_read_b32 v189, a141\n"
        "v_accvgpr_read_b32 v190, a142\n"
        "v_mfma_f32_16x16x32_bf16 v[72:75], v[164:167], v[212:215], v[72:75]\n"
        "v_accvgpr_read_b32 v191, a143\n"
        "v_mul_f32 v185, s88, v185\n"
        "v_mfma_f32_16x16x32_bf16 v[72:75], v[160:163], v[196:199], v[72:75]\n"
        "global_load_lds_dwordx4 v243, s[86:87] offset:1024\n"
        "v_mfma_f32_16x16x32_bf16 v[104:107], v[160:163], v[200:203], v[104:107]\n"
        "v_accvgpr_read_b32 v176, a128\n"
        "v_mul_f32 v176, s88, v176\n"
        "v_mfma_f32_16x16x32_bf16 v[104:107], v[164:167], v[216:219], v[104:107]\n"
        "v_cvt_pk_bf16_f32 v176, v176, v177\n"
        "v_cvt_pk_bf16_f32 v177, v178, v179\n"
        "v_mfma_f32_16x16x32_bf16 v[108:111], v[172:175], v[216:219], v[108:111]\n"
        "v_cvt_pk_bf16_f32 v178, v180, v181\n"
        "v_cvt_pk_bf16_f32 v179, v182, v183\n"
        "v_mfma_f32_16x16x32_bf16 v[108:111], v[168:171], v[200:203], v[108:111]\n"
        "s_waitcnt vmcnt(11) lgkmcnt(0)\n"
        "v_accvgpr_write_b32 a8, v176\n"
        "v_accvgpr_write_b32 a9, v177\n"
        "v_mfma_f32_16x16x32_bf16 v[140:143], v[168:171], v[204:207], v[140:143]\n"
        "global_load_lds_dwordx4 v244, s[86:87] offset:2048\n"
        "v_mfma_f32_16x16x32_bf16 v[140:143], v[172:175], v[220:223], v[140:143]\n"
        "v_accvgpr_write_b32 a10, v178\n"
        "v_accvgpr_write_b32 a11, v179\n"
        "v_mfma_f32_16x16x32_bf16 v[136:139], v[164:167], v[220:223], v[136:139]\n"
        "s_barrier\n"
        "v_mfma_f32_16x16x32_bf16 v[136:139], v[160:163], v[204:207], v[136:139]\n"
        "ds_read_b64_tr_b16 v[160:161], v226 offset:32768\n"
        "v_mul_f32 v186, s88, v186\n"
        "v_mfma_f32_16x16x32_bf16 v[16:19], v[32:35], v[192:195], v[16:19]\n"
        "ds_read_b64_tr_b16 v[162:163], v227 offset:32768\n"
        "v_mul_f32 v187, s88, v187\n"
        "v_mfma_f32_16x16x32_bf16 v[16:19], v[36:39], v[208:211], v[16:19]\n"
        "global_load_lds_dwordx4 v245, s[86:87] offset:3072\n"
        "s_add_u32 m0, s73, 0x1c000\n"
        "v_mfma_f32_16x16x32_bf16 v[20:23], v[44:47], v[208:211], v[20:23]\n"
        "s_add_u32 s86, s68, 0x400000\n"
        "s_addc_u32 s87, s69, 0\n"
        "v_mul_f32 v188, s88, v188\n"
        "v_mfma_f32_16x16x32_bf16 v[20:23], v[40:43], v[192:195], v[20:23]\n"
        "ds_read_b64_tr_b16 v[164:165], v226 offset:40960\n"
        "v_mul_f32 v189, s88, v189\n"
        "v_mfma_f32_16x16x32_bf16 v[84:87], v[40:43], v[196:199], v[84:87]\n"
        "ds_read_b64_tr_b16 v[166:167], v227 offset:40960\n"
        "v_mul_f32 v190, s88, v190\n"
        "v_mfma_f32_16x16x32_bf16 v[84:87], v[44:47], v[212:215], v[84:87]\n"
        "ds_read_b64_tr_b16 v[172:173], v228 offset:40960\n"
        "v_mul_f32 v191, s88, v191\n"
        "v_mfma_f32_16x16x32_bf16 v[80:83], v[36:39], v[212:215], v[80:83]\n"
        "global_load_lds_dwordx4 v238, s[86:87] offset:0\n"
        "v_mfma_f32_16x16x32_bf16 v[80:83], v[32:35], v[196:199], v[80:83]\n"
        "ds_read_b64_tr_b16 v[174:175], v229 offset:40960\n"
        "v_accvgpr_read_b32 v177, a209\n"
        "v_mfma_f32_16x16x32_bf16 v[112:115], v[32:35], v[200:203], v[112:115]\n"
        "ds_read_b64_tr_b16 v[168:169], v228 offset:32768\n"
        "v_accvgpr_read_b32 v178, a210\n"
        "v_mfma_f32_16x16x32_bf16 v[112:115], v[36:39], v[216:219], v[112:115]\n"
        "ds_read_b64_tr_b16 v[170:171], v229 offset:32768\n"
        "v_accvgpr_read_b32 v179, a211\n"
        "v_mfma_f32_16x16x32_bf16 v[116:119], v[44:47], v[216:219], v[116:119]\n"
        "v_accvgpr_read_b32 v180, a240\n"
        "v_accvgpr_read_b32 v181, a241\n"
        "v_mfma_f32_16x16x32_bf16 v[116:119], v[40:43], v[200:203], v[116:119]\n"
        "global_load_lds_dwordx4 v239, s[86:87] offset:1024\n"
        "v_mfma_f32_16x16x32_bf16 v[148:151], v[40:43], v[204:207], v[148:151]\n"
        "ds_read_b64_tr_b16 v[40:41], v232 offset:32768\n"
        "v_accvgpr_read_b32 v182, a242\n"
        "v_mfma_f32_16x16x32_bf16 v[148:151], v[44:47], v[220:223], v[148:151]\n"
        "ds_read_b64_tr_b16 v[44:45], v232 offset:40960\n"
        "v_accvgpr_read_b32 v183, a243\n"
        "v_mfma_f32_16x16x32_bf16 v[144:147], v[36:39], v[220:223], v[144:147]\n"
        "ds_read_b64_tr_b16 v[36:37], v230 offset:40960\n"
        "v_mfma_f32_16x16x32_bf16 v[144:147], v[32:35], v[204:207], v[144:147]\n"
        "ds_read_b64_tr_b16 v[32:33], v230 offset:32768\n"
        "v_accvgpr_read_b32 v184, a136\n"
        "v_mfma_f32_16x16x32_bf16 v[24:27], v[48:51], v[192:195], v[24:27]\n"
        "global_load_lds_dwordx4 v240, s[86:87] offset:2048\n"
        "v_mfma_f32_16x16x32_bf16 v[24:27], v[52:55], v[208:211], v[24:27]\n"
        "ds_read_b64_tr_b16 v[34:35], v231 offset:32768\n"
        "v_mul_f32 v184, s88, v184\n"
        "v_mfma_f32_16x16x32_bf16 v[28:31], v[60:63], v[208:211], v[28:31]\n"
        "ds_read_b64_tr_b16 v[38:39], v231 offset:40960\n"
        "v_cvt_pk_bf16_f32 v184, v184, v185\n"
        "v_mfma_f32_16x16x32_bf16 v[28:31], v[56:59], v[192:195], v[28:31]\n"
        "ds_read_b64_tr_b16 v[46:47], v233 offset:40960\n"
        "v_cvt_pk_bf16_f32 v185, v186, v187\n"
        "v_mfma_f32_16x16x32_bf16 v[92:95], v[56:59], v[196:199], v[92:95]\n"
        "ds_read_b64_tr_b16 v[42:43], v233 offset:32768\n"
        "v_cvt_pk_bf16_f32 v186, v188, v189\n"
        "v_mfma_f32_16x16x32_bf16 v[92:95], v[60:63], v[212:215], v[92:95]\n"
        "global_load_lds_dwordx4 v241, s[86:87] offset:3072\n"
        "s_add_u32 m0, s72, 0x8000\n"
        "v_mfma_f32_16x16x32_bf16 v[88:91], v[52:55], v[212:215], v[88:91]\n"
        "ds_read_b128 v[192:195], v246 offset:2048\n"
        "v_cvt_pk_bf16_f32 v187, v190, v191\n"
        "v_mfma_f32_16x16x32_bf16 v[88:91], v[48:51], v[196:199], v[88:91]\n"
        "ds_read_b128 v[208:211], v247 offset:2048\n"
        "v_accvgpr_write_b32 a12, v184\n"
        "v_mfma_f32_16x16x32_bf16 v[120:123], v[48:51], v[200:203], v[120:123]\n"
        "ds_read_b128 v[196:199], v246 offset:0\n"
        "v_accvgpr_write_b32 a13, v185\n"
        "v_mfma_f32_16x16x32_bf16 v[120:123], v[52:55], v[216:219], v[120:123]\n"
        "ds_read_b128 v[212:215], v247 offset:0\n"
        "v_accvgpr_write_b32 a14, v186\n"
        "v_mfma_f32_16x16x32_bf16 v[124:127], v[60:63], v[216:219], v[124:127]\n"
        "ds_read_b128 v[216:219], v247 offset:6144\n"
        "v_accvgpr_write_b32 a15, v187\n"
        "v_mfma_f32_16x16x32_bf16 v[124:127], v[56:59], v[200:203], v[124:127]\n"
        "s_add_u32 s86, s64, 0x400\n"
        "s_addc_u32 s87, s65, 0\n"
        "v_accvgpr_read_b32 v185, a213\n"
        "v_mfma_f32_16x16x32_bf16 v[156:159], v[56:59], v[204:207], v[156:159]\n"
        "global_load_lds_dwordx4 v242, s[86:87] offset:0\n"
        "ds_read_b128 v[200:203], v246 offset:6144\n"
        "v_mfma_f32_16x16x32_bf16 v[156:159], v[60:63], v[220:223], v[156:159]\n"
        "ds_read_b64_tr_b16 v[60:61], v228 offset:41984\n"
        "v_accvgpr_read_b32 v186, a214\n"
        "v_mfma_f32_16x16x32_bf16 v[152:155], v[52:55], v[220:223], v[152:155]\n"
        "ds_read_b128 v[220:223], v247 offset:4096\n"
        "v_accvgpr_read_b32 v187, a215\n"
        "v_mfma_f32_16x16x32_bf16 v[152:155], v[48:51], v[204:207], v[152:155]\n"
        "ds_read_b128 v[204:207], v246 offset:4096\n"
        "s_waitcnt lgkmcnt(7)\n"
        "v_mfma_f32_16x16x32_bf16 v[0:3], v[160:163], v[192:195], v[0:3]\n"
        "ds_read_b64_tr_b16 v[48:49], v226 offset:33792\n"
        "v_accvgpr_read_b32 v188, a244\n"
        "v_mfma_f32_16x16x32_bf16 v[0:3], v[164:167], v[208:211], v[0:3]\n"
        "global_load_lds_dwordx4 v243, s[86:87] offset:1024\n"
        "ds_read_b64_tr_b16 v[50:51], v227 offset:33792\n"
        "v_mfma_f32_16x16x32_bf16 v[4:7], v[172:175], v[208:211], v[4:7]\n"
        "ds_read_b64_tr_b16 v[52:53], v226 offset:41984\n"
        "v_accvgpr_read_b32 v189, a245\n"
        "v_mfma_f32_16x16x32_bf16 v[4:7], v[168:171], v[192:195], v[4:7]\n"
        "LHA6_%=:\n"
        "s_cbranch_execz LHA10_%=\n"
        "ds_read_b64_tr_b16 v[54:55], v227 offset:41984\n"
        "s_waitcnt lgkmcnt(8)\n"
        "v_mfma_f32_16x16x32_bf16 v[68:71], v[168:171], v[196:199], v[68:71]\n"
        "ds_read_b64_tr_b16 v[62:63], v229 offset:41984\n"
        "v_accvgpr_read_b32 v190, a246\n"
        "v_mfma_f32_16x16x32_bf16 v[68:71], v[172:175], v[212:215], v[68:71]\n"
        "ds_read_b64_tr_b16 v[56:57], v228 offset:33792\n"
        "v_accvgpr_read_b32 v191, a247\n"
        "v_mfma_f32_16x16x32_bf16 v[64:67], v[164:167], v[212:215], v[64:67]\n"
        "global_load_lds_dwordx4 v244, s[86:87] offset:2048\n"
        "ds_read_b64_tr_b16 v[58:59], v229 offset:33792\n"
        "v_mfma_f32_16x16x32_bf16 v[64:67], v[160:163], v[196:199], v[64:67]\n"
        "s_waitcnt lgkmcnt(7)\n"
        "v_accvgpr_read_b32 v176, a208\n"
        "v_cvt_pk_bf16_f32 v176, v176, v177\n"
        "v_mfma_f32_16x16x32_bf16 v[96:99], v[160:163], v[200:203], v[96:99]\n"
        "v_cvt_pk_bf16_f32 v177, v178, v179\n"
        "v_cvt_pk_bf16_f32 v178, v180, v181\n"
        "v_mfma_f32_16x16x32_bf16 v[96:99], v[164:167], v[216:219], v[96:99]\n"
        "v_cvt_pk_bf16_f32 v179, v182, v183\n"
        "v_accvgpr_write_b32 a208, v176\n"
        "v_mfma_f32_16x16x32_bf16 v[100:103], v[172:175], v[216:219], v[100:103]\n"
        "v_accvgpr_write_b32 a209, v177\n"
        "v_accvgpr_write_b32 a210, v178\n"
        "v_mfma_f32_16x16x32_bf16 v[100:103], v[168:171], v[200:203], v[100:103]\n"
        "global_load_lds_dwordx4 v245, s[86:87] offset:3072\n"
        "s_add_u32 m0, s72, 0x9000\n"
        "v_accvgpr_write_b32 a211, v179\n"
        "v_mfma_f32_16x16x32_bf16 v[132:135], v[168:171], v[204:207], v[132:135]\n"
        "s_add_u32 s86, s66, 0x400\n"
        "s_addc_u32 s87, s67, 0\n"
        "v_accvgpr_read_b32 v177, a217\n"
        "v_mfma_f32_16x16x32_bf16 v[132:135], v[172:175], v[220:223], v[132:135]\n"
        "ds_read_b64_tr_b16 v[172:173], v232 offset:41984\n"
        "v_accvgpr_read_b32 v178, a218\n"
        "v_mfma_f32_16x16x32_bf16 v[128:131], v[164:167], v[220:223], v[128:131]\n"
        "ds_read_b64_tr_b16 v[164:165], v230 offset:41984\n"
        "v_accvgpr_read_b32 v179, a219\n"
        "v_mfma_f32_16x16x32_bf16 v[128:131], v[160:163], v[204:207], v[128:131]\n"
        "global_store_dwordx4 v254, a[196:199], s[70:71] offset:1024 sc1\n"
        "ds_read_b64_tr_b16 v[160:161], v230 offset:33792\n"
        "ds_read_b64_tr_b16 v[162:163], v231 offset:33792\n"
        "v_accvgpr_read_b32 v180, a248\n"
        "v_mfma_f32_16x16x32_bf16 v[8:11], v[32:35], v[192:195], v[8:11]\n"
        "ds_read_b64_tr_b16 v[166:167], v231 offset:41984\n"
        "v_accvgpr_read_b32 v181, a249\n"
        "v_mfma_f32_16x16x32_bf16 v[8:11], v[36:39], v[208:211], v[8:11]\n"
        "ds_read_b64_tr_b16 v[174:175], v233 offset:41984\n"
        "v_accvgpr_read_b32 v182, a250\n"
        "v_mfma_f32_16x16x32_bf16 v[12:15], v[44:47], v[208:211], v[12:15]\n"
        "ds_read_b64_tr_b16 v[168:169], v232 offset:33792\n"
        "v_accvgpr_read_b32 v183, a251\n"
        "v_mfma_f32_16x16x32_bf16 v[12:15], v[40:43], v[192:195], v[12:15]\n"
        "global_load_lds_dwordx4 v242, s[86:87] offset:0\n"
        "v_mfma_f32_16x16x32_bf16 v[76:79], v[40:43], v[196:199], v[76:79]\n"
        "ds_read_b64_tr_b16 v[170:171], v233 offset:33792\n"
        "s_add_u32 s70, s38, s96\n"
        "s_addc_u32 s71, s39, 0\n"
        "v_mfma_f32_16x16x32_bf16 v[76:79], v[44:47], v[212:215], v[76:79]\n"
        "v_mfma_f32_16x16x32_bf16 v[72:75], v[36:39], v[212:215], v[72:75]\n"
        "v_mfma_f32_16x16x32_bf16 v[72:75], v[32:35], v[196:199], v[72:75]\n"
        "v_mfma_f32_16x16x32_bf16 v[104:107], v[32:35], v[200:203], v[104:107]\n"
        "global_load_lds_dwordx4 v243, s[86:87] offset:1024\n"
        "v_mfma_f32_16x16x32_bf16 v[104:107], v[36:39], v[216:219], v[104:107]\n"
        "v_mfma_f32_16x16x32_bf16 v[108:111], v[44:47], v[216:219], v[108:111]\n"
        "v_mfma_f32_16x16x32_bf16 v[108:111], v[40:43], v[200:203], v[108:111]\n"
        "v_mfma_f32_16x16x32_bf16 v[140:143], v[40:43], v[204:207], v[140:143]\n"
        "s_waitcnt vmcnt(11) lgkmcnt(0)\n"
        "v_mfma_f32_16x16x32_bf16 v[140:143], v[44:47], v[220:223], v[140:143]\n"
        "global_load_lds_dwordx4 v244, s[86:87] offset:2048\n"
        "s_add_u32 s96, s52, 0x1000\n"
        "v_mfma_f32_16x16x32_bf16 v[136:139], v[36:39], v[220:223], v[136:139]\n"
        "s_barrier\n"
        "v_mfma_f32_16x16x32_bf16 v[136:139], v[32:35], v[204:207], v[136:139]\n"
        "ds_read_b64_tr_b16 v[32:33], v226 offset:49152\n"
        "s_add_u32 s96, s96, s75\n"
        "v_accvgpr_read_b32 v184, a212\n"
        "v_mfma_f32_16x16x32_bf16 v[16:19], v[48:51], v[192:195], v[16:19]\n"
        "ds_read_b64_tr_b16 v[34:35], v227 offset:49152\n"
        "v_cvt_pk_bf16_f32 v184, v184, v185\n"
        "v_mfma_f32_16x16x32_bf16 v[16:19], v[52:55], v[208:211], v[16:19]\n"
        "ds_read_b64_tr_b16 v[36:37], v226 offset:57344\n"
        "v_cvt_pk_bf16_f32 v185, v186, v187\n"
        "v_mfma_f32_16x16x32_bf16 v[20:23], v[60:63], v[208:211], v[20:23]\n"
        "global_load_lds_dwordx4 v245, s[86:87] offset:3072\n"
        "s_add_u32 m0, s73, 0x20000\n"
        "v_mfma_f32_16x16x32_bf16 v[20:23], v[56:59], v[192:195], v[20:23]\n"
        "ds_read_b64_tr_b16 v[38:39], v227 offset:57344\n"
        "v_cvt_pk_bf16_f32 v186, v188, v189\n"
        "v_mfma_f32_16x16x32_bf16 v[84:87], v[56:59], v[196:199], v[84:87]\n"
        "ds_read_b64_tr_b16 v[44:45], v228 offset:57344\n"
        "v_cvt_pk_bf16_f32 v187, v190, v191\n"
        "v_mfma_f32_16x16x32_bf16 v[84:87], v[60:63], v[212:215], v[84:87]\n"
        "ds_read_b64_tr_b16 v[46:47], v229 offset:57344\n"
        "v_accvgpr_write_b32 a240, v184\n"
        "v_mfma_f32_16x16x32_bf16 v[80:83], v[52:55], v[212:215], v[80:83]\n"
        "ds_read_b64_tr_b16 v[40:41], v228 offset:49152\n"
        "v_accvgpr_write_b32 a241, v185\n"
        "v_mfma_f32_16x16x32_bf16 v[80:83], v[48:51], v[196:199], v[80:83]\n"
        "ds_read_b64_tr_b16 v[42:43], v229 offset:49152\n"
        "v_accvgpr_write_b32 a242, v186\n"
        "v_mfma_f32_16x16x32_bf16 v[112:115], v[48:51], v[200:203], v[112:115]\n"
        "s_add_u32 s86, s68, 0x480000\n"
        "s_addc_u32 s87, s69, 0\n"
        "v_accvgpr_write_b32 a243, v187\n"
        "v_mfma_f32_16x16x32_bf16 v[112:115], v[52:55], v[216:219], v[112:115]\n"
        "global_load_lds_dwordx4 v238, s[86:87] offset:0\n"
        "v_accvgpr_read_b32 v185, a221\n"
        "v_mfma_f32_16x16x32_bf16 v[116:119], v[60:63], v[216:219], v[116:119]\n"
        "v_accvgpr_read_b32 v186, a222\n"
        "v_accvgpr_read_b32 v187, a223\n"
        "v_mfma_f32_16x16x32_bf16 v[116:119], v[56:59], v[200:203], v[116:119]\n"
        "v_accvgpr_read_b32 v188, a252\n"
        "v_accvgpr_read_b32 v189, a253\n"
        "v_mfma_f32_16x16x32_bf16 v[148:151], v[56:59], v[204:207], v[148:151]\n"
        "ds_read_b64_tr_b16 v[56:57], v232 offset:49152\n"
        "v_accvgpr_read_b32 v190, a254\n"
        "v_mfma_f32_16x16x32_bf16 v[148:151], v[60:63], v[220:223], v[148:151]\n"
        "ds_read_b64_tr_b16 v[60:61], v232 offset:57344\n"
        "v_accvgpr_read_b32 v191, a255\n"
        "v_mfma_f32_16x16x32_bf16 v[144:147], v[52:55], v[220:223], v[144:147]\n"
        "global_load_lds_dwordx4 v239, s[86:87] offset:1024\n"
        "ds_read_b64_tr_b16 v[52:53], v230 offset:57344\n"
        "v_mfma_f32_16x16x32_bf16 v[144:147], v[48:51], v[204:207], v[144:147]\n"
        "ds_read_b64_tr_b16 v[48:49], v230 offset:49152\n"
        "v_mfma_f32_16x16x32_bf16 v[24:27], v[160:163], v[192:195], v[24:27]\n"
        "ds_read_b64_tr_b16 v[50:51], v231 offset:49152\n"
        "v_mfma_f32_16x16x32_bf16 v[24:27], v[164:167], v[208:211], v[24:27]\n"
        "ds_read_b64_tr_b16 v[54:55], v231 offset:57344\n"
        "v_mfma_f32_16x16x32_bf16 v[28:31], v[172:175], v[208:211], v[28:31]\n"
        "ds_read_b64_tr_b16 v[62:63], v233 offset:57344\n"
        "v_mfma_f32_16x16x32_bf16 v[28:31], v[168:171], v[192:195], v[28:31]\n"
        "global_load_lds_dwordx4 v240, s[86:87] offset:2048\n"
        "ds_read_b64_tr_b16 v[58:59], v233 offset:49152\n"
        "v_mfma_f32_16x16x32_bf16 v[92:95], v[168:171], v[196:199], v[92:95]\n"
        "ds_read_b128 v[192:195], v248 offset:2048\n"
        "v_mfma_f32_16x16x32_bf16 v[92:95], v[172:175], v[212:215], v[92:95]\n"
        "ds_read_b128 v[208:211], v249 offset:2048\n"
        "v_mfma_f32_16x16x32_bf16 v[88:91], v[164:167], v[212:215], v[88:91]\n"
        "ds_read_b128 v[212:215], v249 offset:0\n"
        "v_mfma_f32_16x16x32_bf16 v[88:91], v[160:163], v[196:199], v[88:91]\n"
        "ds_read_b128 v[196:199], v248 offset:0\n"
        "v_accvgpr_read_b32 v176, a216\n"
        "v_mfma_f32_16x16x32_bf16 v[120:123], v[160:163], v[200:203], v[120:123]\n"
        "global_load_lds_dwordx4 v241, s[86:87] offset:3072\n"
        "s_add_u32 m0, s72, 0x0\n"
        "v_cvt_pk_bf16_f32 v176, v176, v177\n"
        "v_mfma_f32_16x16x32_bf16 v[120:123], v[164:167], v[216:219], v[120:123]\n"
        "s_add_u32 s86, s64, 0x480\n"
        "s_addc_u32 s87, s65, 0\n"
        "v_cvt_pk_bf16_f32 v177, v178, v179\n"
        "v_mfma_f32_16x16x32_bf16 v[124:127], v[172:175], v[216:219], v[124:127]\n"
        "ds_read_b128 v[216:219], v249 offset:6144\n"
        "v_cvt_pk_bf16_f32 v178, v180, v181\n"
        "v_mfma_f32_16x16x32_bf16 v[124:127], v[168:171], v[200:203], v[124:127]\n"
        "ds_read_b128 v[200:203], v248 offset:6144\n"
        "v_cvt_pk_bf16_f32 v179, v182, v183\n"
        "v_mfma_f32_16x16x32_bf16 v[156:159], v[168:171], v[204:207], v[156:159]\n"
        "s_waitcnt lgkmcnt(5)\n"
        "v_accvgpr_write_b32 a212, v176\n"
        "v_accvgpr_write_b32 a213, v177\n"
        "v_mfma_f32_16x16x32_bf16 v[156:159], v[172:175], v[220:223], v[156:159]\n"
        "global_load_lds_dwordx4 v242, s[86:87] offset:0\n"
        "v_accvgpr_write_b32 a214, v178\n"
        "v_mfma_f32_16x16x32_bf16 v[152:155], v[164:167], v[220:223], v[152:155]\n"
        "s_waitcnt lgkmcnt(3)\n"
        "v_accvgpr_write_b32 a215, v179\n"
        "v_accvgpr_read_b32 v177, a145\n"
        "v_mfma_f32_16x16x32_bf16 v[152:155], v[160:163], v[204:207], v[152:155]\n"
        "ds_read_b128 v[204:207], v248 offset:4096\n"
        "v_accvgpr_read_b32 v178, a146\n"
        "v_mfma_f32_16x16x32_bf16 v[0:3], v[32:35], v[192:195], v[0:3]\n"
        "ds_read_b128 v[220:223], v249 offset:4096\n"
        "v_accvgpr_read_b32 v179, a147\n"
        "v_mfma_f32_16x16x32_bf16 v[0:3], v[36:39], v[208:211], v[0:3]\n"
        "ds_read_b64_tr_b16 v[160:161], v226 offset:50176\n"
        "v_accvgpr_read_b32 v180, a176\n"
        "v_mfma_f32_16x16x32_bf16 v[4:7], v[44:47], v[208:211], v[4:7]\n"
        "global_load_lds_dwordx4 v243, s[86:87] offset:1024\n"
        "ds_read_b64_tr_b16 v[162:163], v227 offset:50176\n"
        "v_mfma_f32_16x16x32_bf16 v[4:7], v[40:43], v[192:195], v[4:7]\n"
        "ds_read_b64_tr_b16 v[164:165], v226 offset:58368\n"
        "s_waitcnt lgkmcnt(5)\n"
        "v_mfma_f32_16x16x32_bf16 v[68:71], v[40:43], v[196:199], v[68:71]\n"
        "ds_read_b64_tr_b16 v[166:167], v227 offset:58368\n"
        "v_accvgpr_read_b32 v181, a177\n"
        "v_mfma_f32_16x16x32_bf16 v[68:71], v[44:47], v[212:215], v[68:71]\n"
        "ds_read_b64_tr_b16 v[172:173], v228 offset:58368\n"
        "v_accvgpr_read_b32 v182, a178\n"
        "v_mfma_f32_16x16x32_bf16 v[64:67], v[36:39], v[212:215], v[64:67]\n"
        "ds_read_b64_tr_b16 v[174:175], v229 offset:58368\n"
        "v_accvgpr_read_b32 v183, a179\n"
        "v_mfma_f32_16x16x32_bf16 v[64:67], v[32:35], v[196:199], v[64:67]\n"
        "global_load_lds_dwordx4 v244, s[86:87] offset:2048\n"
        "ds_read_b64_tr_b16 v[168:169], v228 offset:50176\n"
        "v_mfma_f32_16x16x32_bf16 v[96:99], v[32:35], v[200:203], v[96:99]\n"
        "ds_read_b64_tr_b16 v[170:171], v229 offset:50176\n"
        "s_waitcnt lgkmcnt(8)\n"
        "v_mfma_f32_16x16x32_bf16 v[96:99], v[36:39], v[216:219], v[96:99]\n"
        "v_mfma_f32_16x16x32_bf16 v[100:103], v[44:47], v[216:219], v[100:103]\n"
        "v_mfma_f32_16x16x32_bf16 v[100:103], v[40:43], v[200:203], v[100:103]\n"
        "v_mfma_f32_16x16x32_bf16 v[132:135], v[40:43], v[204:207], v[132:135]\n"
        "global_load_lds_dwordx4 v245, s[86:87] offset:3072\n"
        "s_add_u32 m0, s72, 0x1000\n"
        "v_mfma_f32_16x16x32_bf16 v[132:135], v[44:47], v[220:223], v[132:135]\n"
        "s_add_u32 s86, s66, 0x480\n"
        "s_addc_u32 s87, s67, 0\n"
        "v_mfma_f32_16x16x32_bf16 v[128:131], v[36:39], v[220:223], v[128:131]\n"
        "ds_read_b64_tr_b16 v[36:37], v230 offset:58368\n"
        "v_mfma_f32_16x16x32_bf16 v[128:131], v[32:35], v[204:207], v[128:131]\n"
        "ds_read_b64_tr_b16 v[32:33], v230 offset:50176\n"
        "v_accvgpr_read_b32 v184, a220\n"
        "v_mfma_f32_16x16x32_bf16 v[8:11], v[48:51], v[192:195], v[8:11]\n"
        "ds_read_b64_tr_b16 v[34:35], v231 offset:50176\n"
        "v_cvt_pk_bf16_f32 v184, v184, v185\n"
        "v_mfma_f32_16x16x32_bf16 v[8:11], v[52:55], v[208:211], v[8:11]\n"
        "global_load_lds_dwordx4 v242, s[86:87] offset:0\n"
        "v_cvt_pk_bf16_f32 v185, v186, v187\n"
        "v_mfma_f32_16x16x32_bf16 v[12:15], v[60:63], v[208:211], v[12:15]\n"
        "ds_read_b64_tr_b16 v[38:39], v231 offset:58368\n"
        "v_cvt_pk_bf16_f32 v186, v188, v189\n"
        "v_mfma_f32_16x16x32_bf16 v[12:15], v[56:59], v[192:195], v[12:15]\n"
        "ds_read_b64_tr_b16 v[44:45], v232 offset:58368\n"
        "v_cvt_pk_bf16_f32 v187, v190, v191\n"
        "v_mfma_f32_16x16x32_bf16 v[76:79], v[56:59], v[196:199], v[76:79]\n"
        "ds_read_b64_tr_b16 v[46:47], v233 offset:58368\n"
        "v_accvgpr_write_b32 a244, v184\n"
        "v_mfma_f32_16x16x32_bf16 v[76:79], v[60:63], v[212:215], v[76:79]\n"
        "ds_read_b64_tr_b16 v[40:41], v232 offset:50176\n"
        "v_accvgpr_write_b32 a245, v185\n"
        "v_mfma_f32_16x16x32_bf16 v[72:75], v[52:55], v[212:215], v[72:75]\n"
        "global_load_lds_dwordx4 v243, s[86:87] offset:1024\n"
        "v_accvgpr_write_b32 a246, v186\n"
        "v_mfma_f32_16x16x32_bf16 v[72:75], v[48:51], v[196:199], v[72:75]\n"
        "ds_read_b64_tr_b16 v[42:43], v233 offset:50176\n"
        "v_accvgpr_write_b32 a247, v187\n"
        "v_mfma_f32_16x16x32_bf16 v[104:107], v[48:51], v[200:203], v[104:107]\n"
        "v_accvgpr_read_b32 v185, a149\n"
        "v_accvgpr_read_b32 v186, a150\n"
        "v_mfma_f32_16x16x32_bf16 v[104:107], v[52:55], v[216:219], v[104:107]\n"
        "v_accvgpr_read_b32 v187, a151\n"
        "v_accvgpr_read_b32 v188, a180\n"
        "v_mfma_f32_16x16x32_bf16 v[108:111], v[60:63], v[216:219], v[108:111]\n"
        "global_store_dwordx4 v254, a[200:203], s[70:71] offset:0 sc1\n"
        "v_accvgpr_read_b32 v189, a181\n"
        "v_accvgpr_read_b32 v190, a182\n"
        "v_accvgpr_read_b32 v191, a183\n"
        "v_mfma_f32_16x16x32_bf16 v[108:111], v[56:59], v[200:203], v[108:111]\n"
        "v_mfma_f32_16x16x32_bf16 v[140:143], v[56:59], v[204:207], v[140:143]\n"
        "s_waitcnt lgkmcnt(2)\n"
        "v_mfma_f32_16x16x32_bf16 v[140:143], v[60:63], v[220:223], v[140:143]\n"
        "v_mfma_f32_16x16x32_bf16 v[136:139], v[52:55], v[220:223], v[136:139]\n"
        "global_load_lds_dwordx4 v244, s[86:87] offset:2048\n"
        "v_mfma_f32_16x16x32_bf16 v[136:139], v[48:51], v[204:207], v[136:139]\n"
        "s_waitcnt vmcnt(12) lgkmcnt(0)\n"
        "v_mfma_f32_16x16x32_bf16 v[16:19], v[160:163], v[192:195], v[16:19]\n"
        "s_barrier\n"
        "v_mfma_f32_16x16x32_bf16 v[16:19], v[164:167], v[208:211], v[16:19]\n"
        "ds_read_b64_tr_b16 v[48:49], v226 offset:16384\n"
        "v_mfma_f32_16x16x32_bf16 v[20:23], v[172:175], v[208:211], v[20:23]\n"
        "ds_read_b64_tr_b16 v[50:51], v227 offset:16384\n"
        "v_mfma_f32_16x16x32_bf16 v[20:23], v[168:171], v[192:195], v[20:23]\n"
        "global_load_lds_dwordx4 v245, s[86:87] offset:3072\n"
        "s_add_u32 m0, s73, 0x24000\n"
        "v_mfma_f32_16x16x32_bf16 v[84:87], v[168:171], v[196:199], v[84:87]\n"
        "s_add_u32 s86, s68, 0x500000\n"
        "s_addc_u32 s87, s69, 0\n"
        "v_mfma_f32_16x16x32_bf16 v[84:87], v[172:175], v[212:215], v[84:87]\n"
        "ds_read_b64_tr_b16 v[52:53], v226 offset:24576\n"
        "v_mfma_f32_16x16x32_bf16 v[80:83], v[164:167], v[212:215], v[80:83]\n"
        "ds_read_b64_tr_b16 v[54:55], v227 offset:24576\n"
        "v_mfma_f32_16x16x32_bf16 v[80:83], v[160:163], v[196:199], v[80:83]\n"
        "ds_read_b64_tr_b16 v[60:61], v228 offset:24576\n"
        "v_accvgpr_read_b32 v176, a144\n"
        "v_mfma_f32_16x16x32_bf16 v[112:115], v[160:163], v[200:203], v[112:115]\n"
        "global_load_lds_dwordx4 v238, s[86:87] offset:0\n"
        "v_mfma_f32_16x16x32_bf16 v[112:115], v[164:167], v[216:219], v[112:115]\n"
        "ds_read_b64_tr_b16 v[62:63], v229 offset:24576\n"
        "v_cvt_pk_bf16_f32 v176, v176, v177\n"
        "v_mfma_f32_16x16x32_bf16 v[116:119], v[172:175], v[216:219], v[116:119]\n"
        "ds_read_b64_tr_b16 v[56:57], v228 offset:16384\n"
        "v_cvt_pk_bf16_f32 v177, v178, v179\n"
        "v_mfma_f32_16x16x32_bf16 v[116:119], v[168:171], v[200:203], v[116:119]\n"
        "ds_read_b64_tr_b16 v[58:59], v229 offset:16384\n"
        "v_cvt_pk_bf16_f32 v178, v180, v181\n"
        "v_mfma_f32_16x16x32_bf16 v[148:151], v[168:171], v[204:207], v[148:151]\n"
        "ds_read_b64_tr_b16 v[168:169], v232 offset:16384\n"
        "v_cvt_pk_bf16_f32 v179, v182, v183\n"
        "v_mfma_f32_16x16x32_bf16 v[148:151], v[172:175], v[220:223], v[148:151]\n"
        "global_load_lds_dwordx4 v239, s[86:87] offset:1024\n"
        "v_mfma_f32_16x16x32_bf16 v[144:147], v[164:167], v[220:223], v[144:147]\n"
        "ds_read_b64_tr_b16 v[164:165], v230 offset:24576\n"
        "v_accvgpr_write_b32 a216, v176\n"
        "v_mfma_f32_16x16x32_bf16 v[144:147], v[160:163], v[204:207], v[144:147]\n"
        "ds_read_b64_tr_b16 v[160:161], v230 offset:16384\n"
        "v_accvgpr_write_b32 a217, v177\n"
        "v_mfma_f32_16x16x32_bf16 v[24:27], v[32:35], v[192:195], v[24:27]\n"
        "ds_read_b64_tr_b16 v[162:163], v231 offset:16384\n"
        "v_accvgpr_write_b32 a218, v178\n"
        "v_mfma_f32_16x16x32_bf16 v[24:27], v[36:39], v[208:211], v[24:27]\n"
        "ds_read_b64_tr_b16 v[166:167], v231 offset:24576\n"
        "v_accvgpr_write_b32 a219, v179\n"
        "v_mfma_f32_16x16x32_bf16 v[28:31], v[44:47], v[208:211], v[28:31]\n"
        "global_load_lds_dwordx4 v240, s[86:87] offset:2048\n"
        "v_mfma_f32_16x16x32_bf16 v[28:31], v[40:43], v[192:195], v[28:31]\n"
        "ds_read_b64_tr_b16 v[172:173], v232 offset:24576\n"
        "v_accvgpr_read_b32 v177, a153\n"
        "v_mfma_f32_16x16x32_bf16 v[92:95], v[40:43], v[196:199], v[92:95]\n"
        "ds_read_b64_tr_b16 v[174:175], v233 offset:24576\n"
        "v_accvgpr_read_b32 v178, a154\n"
        "v_mfma_f32_16x16x32_bf16 v[92:95], v[44:47], v[212:215], v[92:95]\n"
        "ds_read_b64_tr_b16 v[170:171], v233 offset:16384\n"
        "v_accvgpr_read_b32 v179, a155\n"
        "v_mfma_f32_16x16x32_bf16 v[88:91], v[36:39], v[212:215], v[88:91]\n"
        "ds_read_b128 v[192:195], v246 offset:34816\n"
        "v_accvgpr_read_b32 v180, a184\n"
        "v_mfma_f32_16x16x32_bf16 v[88:91], v[32:35], v[196:199], v[88:91]\n"
        "global_load_lds_dwordx4 v241, s[86:87] offset:3072\n"
        "s_add_u32 m0, s72, 0x10000\n"
        "v_mfma_f32_16x16x32_bf16 v[120:123], v[32:35], v[200:203], v[120:123]\n"
        "ds_read_b128 v[208:211], v247 offset:34816\n"
        "s_add_u32 s86, s64, 0x500\n"
        "s_addc_u32 s87, s65, 0\n"
        "v_mfma_f32_16x16x32_bf16 v[120:123], v[36:39], v[216:219], v[120:123]\n"
        "ds_read_b128 v[196:199], v246 offset:32768\n"
        "v_accvgpr_read_b32 v181, a185\n"
        "v_mfma_f32_16x16x32_bf16 v[124:127], v[44:47], v[216:219], v[124:127]\n"
        "ds_read_b128 v[212:215], v247 offset:32768\n"
        "v_accvgpr_read_b32 v182, a186\n"
        "v_mfma_f32_16x16x32_bf16 v[124:127], v[40:43], v[200:203], v[124:127]\n"
        "ds_read_b128 v[200:203], v246 offset:38912\n"
        "v_accvgpr_read_b32 v183, a187\n"
        "v_mfma_f32_16x16x32_bf16 v[156:159], v[40:43], v[204:207], v[156:159]\n"
        "global_load_lds_dwordx4 v242, s[86:87] offset:0\n"
        "v_mfma_f32_16x16x32_bf16 v[156:159], v[44:47], v[220:223], v[156:159]\n"
        "ds_read_b128 v[216:219], v247 offset:38912\n"
        "v_mfma_f32_16x16x32_bf16 v[152:155], v[36:39], v[220:223], v[152:155]\n"
        "ds_read_b128 v[220:223], v247 offset:36864\n"
        "s_waitcnt lgkmcnt(6)\n"
        "v_mfma_f32_16x16x32_bf16 v[152:155], v[32:35], v[204:207], v[152:155]\n"
        "ds_read_b128 v[204:207], v246 offset:36864\n"
        "v_accvgpr_read_b32 v184, a148\n"
        "v_mfma_f32_16x16x32_bf16 v[0:3], v[48:51], v[192:195], v[0:3]\n"
        "s_waitcnt lgkmcnt(6)\n"
        "v_cvt_pk_bf16_f32 v184, v184, v185\n"
        "v_cvt_pk_bf16_f32 v185, v186, v187\n"
        "v_mfma_f32_16x16x32_bf16 v[0:3], v[52:55], v[208:211], v[0:3]\n"
        "global_load_lds_dwordx4 v243, s[86:87] offset:1024\n"
        "v_mfma_f32_16x16x32_bf16 v[4:7], v[60:63], v[208:211], v[4:7]\n"
        "s_waitcnt lgkmcnt(4)\n"
        "v_cvt_pk_bf16_f32 v186, v188, v189\n"
        "v_cvt_pk_bf16_f32 v187, v190, v191\n"
        "v_mfma_f32_16x16x32_bf16 v[4:7], v[56:59], v[192:195], v[4:7]\n"
        "ds_read_b64_tr_b16 v[32:33], v226 offset:17408\n"
        "v_accvgpr_write_b32 a248, v184\n"
        "v_mfma_f32_16x16x32_bf16 v[68:71], v[56:59], v[196:199], v[68:71]\n"
        "s_waitcnt lgkmcnt(4)\n"
        "v_accvgpr_write_b32 a249, v185\n"
        "v_accvgpr_write_b32 a250, v186\n"
        "v_mfma_f32_16x16x32_bf16 v[68:71], v[60:63], v[212:215], v[68:71]\n"
        "s_waitcnt lgkmcnt(3)\n"
        "v_accvgpr_write_b32 a251, v187\n"
        "v_accvgpr_read_b32 v185, a157\n"
        "v_mfma_f32_16x16x32_bf16 v[64:67], v[52:55], v[212:215], v[64:67]\n"
        "global_load_lds_dwordx4 v244, s[86:87] offset:2048\n"
        "v_mfma_f32_16x16x32_bf16 v[64:67], v[48:51], v[196:199], v[64:67]\n"
        "ds_read_b64_tr_b16 v[34:35], v227 offset:17408\n"
        "v_accvgpr_read_b32 v186, a158\n"
        "v_mfma_f32_16x16x32_bf16 v[96:99], v[48:51], v[200:203], v[96:99]\n"
        "ds_read_b64_tr_b16 v[36:37], v226 offset:25600\n"
        "v_accvgpr_read_b32 v187, a159\n"
        "v_mfma_f32_16x16x32_bf16 v[96:99], v[52:55], v[216:219], v[96:99]\n"
        "ds_read_b64_tr_b16 v[38:39], v227 offset:25600\n"
        "v_accvgpr_read_b32 v188, a188\n"
        "v_mfma_f32_16x16x32_bf16 v[100:103], v[60:63], v[216:219], v[100:103]\n"
        "ds_read_b64_tr_b16 v[44:45], v228 offset:25600\n"
        "v_accvgpr_read_b32 v189, a189\n"
        "v_mfma_f32_16x16x32_bf16 v[100:103], v[56:59], v[200:203], v[100:103]\n"
        "ds_read_b64_tr_b16 v[46:47], v229 offset:25600\n"
        "s_waitcnt lgkmcnt(5)\n"
        "v_mfma_f32_16x16x32_bf16 v[132:135], v[56:59], v[204:207], v[132:135]\n"
        "ds_read_b64_tr_b16 v[40:41], v228 offset:17408\n"
        "global_load_lds_dwordx4 v245, s[86:87] offset:3072\n"
        "s_add_u32 m0, s72, 0x11000\n"
        "v_mfma_f32_16x16x32_bf16 v[132:135], v[60:63], v[220:223], v[132:135]\n"
        "ds_read_b64_tr_b16 v[42:43], v229 offset:17408\n"
        "s_add_u32 s86, s66, 0x500\n"
        "s_addc_u32 s87, s67, 0\n"
        "v_mfma_f32_16x16x32_bf16 v[128:131], v[52:55], v[220:223], v[128:131]\n"
        "ds_read_b64_tr_b16 v[52:53], v230 offset:25600\n"
        "v_accvgpr_read_b32 v190, a190\n"
        "v_mfma_f32_16x16x32_bf16 v[128:131], v[48:51], v[204:207], v[128:131]\n"
        "ds_read_b64_tr_b16 v[48:49], v230 offset:17408\n"
        "v_accvgpr_read_b32 v191, a191\n"
        "v_mfma_f32_16x16x32_bf16 v[8:11], v[160:163], v[192:195], v[8:11]\n"
        "ds_read_b64_tr_b16 v[50:51], v231 offset:17408\n"
        "v_mfma_f32_16x16x32_bf16 v[8:11], v[164:167], v[208:211], v[8:11]\n"
        "global_load_lds_dwordx4 v242, s[86:87] offset:0\n"
        "v_mfma_f32_16x16x32_bf16 v[12:15], v[172:175], v[208:211], v[12:15]\n"
        "ds_read_b64_tr_b16 v[54:55], v231 offset:25600\n"
        "v_mfma_f32_16x16x32_bf16 v[12:15], v[168:171], v[192:195], v[12:15]\n"
        "ds_read_b64_tr_b16 v[60:61], v232 offset:25600\n"
        "v_mfma_f32_16x16x32_bf16 v[76:79], v[168:171], v[196:199], v[76:79]\n"
        "ds_read_b64_tr_b16 v[62:63], v233 offset:25600\n"
        "v_mfma_f32_16x16x32_bf16 v[76:79], v[172:175], v[212:215], v[76:79]\n"
        "ds_read_b64_tr_b16 v[56:57], v232 offset:17408\n"
        "v_mfma_f32_16x16x32_bf16 v[72:75], v[164:167], v[212:215], v[72:75]\n"
        "global_load_lds_dwordx4 v243, s[86:87] offset:1024\n"
        "v_mfma_f32_16x16x32_bf16 v[72:75], v[160:163], v[196:199], v[72:75]\n"
        "ds_read_b64_tr_b16 v[58:59], v233 offset:17408\n"
        "v_accvgpr_read_b32 v176, a152\n"
        "v_mfma_f32_16x16x32_bf16 v[104:107], v[160:163], v[200:203], v[104:107]\n"
        "v_cvt_pk_bf16_f32 v176, v176, v177\n"
        "v_cvt_pk_bf16_f32 v177, v178, v179\n"
        "v_mfma_f32_16x16x32_bf16 v[104:107], v[164:167], v[216:219], v[104:107]\n"
        "v_cvt_pk_bf16_f32 v178, v180, v181\n"
        "v_cvt_pk_bf16_f32 v179, v182, v183\n"
        "v_mfma_f32_16x16x32_bf16 v[108:111], v[172:175], v[216:219], v[108:111]\n"
        "v_accvgpr_write_b32 a220, v176\n"
        "v_accvgpr_write_b32 a221, v177\n"
        "v_mfma_f32_16x16x32_bf16 v[108:111], v[168:171], v[200:203], v[108:111]\n"
        "global_store_dwordx4 v254, a[204:207], s[70:71] offset:1024 sc1\n"
        "s_add_u32 s70, s38, s96\n"
        "s_addc_u32 s71, s39, 0\n"
        "v_accvgpr_write_b32 a222, v178\n"
        "v_accvgpr_write_b32 a223, v179\n"
        "v_accvgpr_read_b32 v177, a17\n"
        "v_mfma_f32_16x16x32_bf16 v[140:143], v[168:171], v[204:207], v[140:143]\n"
        "s_waitcnt lgkmcnt(2)\n"
        "v_accvgpr_read_b32 v178, a18\n"
        "v_accvgpr_read_b32 v179, a19\n"
        "v_mfma_f32_16x16x32_bf16 v[140:143], v[172:175], v[220:223], v[140:143]\n"
        "v_accvgpr_read_b32 v180, a48\n"
        "v_accvgpr_read_b32 v181, a49\n"
        "v_mfma_f32_16x16x32_bf16 v[136:139], v[164:167], v[220:223], v[136:139]\n"
        "s_waitcnt vmcnt(11) lgkmcnt(0)\n"
        "v_accvgpr_read_b32 v182, a50\n"
        "v_mfma_f32_16x16x32_bf16 v[136:139], v[160:163], v[204:207], v[136:139]\n"
        "global_load_lds_dwordx4 v244, s[86:87] offset:2048\n"
        "v_mfma_f32_16x16x32_bf16 v[16:19], v[32:35], v[192:195], v[16:19]\n"
        "s_barrier\n"
        "v_mfma_f32_16x16x32_bf16 v[16:19], v[36:39], v[208:211], v[16:19]\n"
        "ds_read_b64_tr_b16 v[160:161], v226 offset:32768\n"
        "v_accvgpr_read_b32 v183, a51\n"
        "v_mfma_f32_16x16x32_bf16 v[20:23], v[44:47], v[208:211], v[20:23]\n"
        "ds_read_b64_tr_b16 v[162:163], v227 offset:32768\n"
        "v_accvgpr_write_b32 a48, 0\n"
        "v_mfma_f32_16x16x32_bf16 v[20:23], v[40:43], v[192:195], v[20:23]\n"
        "ds_read_b64_tr_b16 v[164:165], v226 offset:40960\n"
        "v_accvgpr_write_b32 a49, 0\n"
        "v_mfma_f32_16x16x32_bf16 v[84:87], v[40:43], v[196:199], v[84:87]\n"
        "global_load_lds_dwordx4 v245, s[86:87] offset:3072\n"
        "s_add_u32 m0, s73, 0x1c000\n"
        "v_mfma_f32_16x16x32_bf16 v[84:87], v[44:47], v[212:215], v[84:87]\n"
        "s_add_u32 s86, s68, 0x580000\n"
        "s_addc_u32 s87, s69, 0\n"
        "s_add_u32 s96, s52, 0x2000\n"
        "v_mfma_f32_16x16x32_bf16 v[80:83], v[36:39], v[212:215], v[80:83]\n"
        "ds_read_b64_tr_b16 v[166:167], v227 offset:40960\n"
        "s_add_u32 s96, s96, s75\n"
        "v_accvgpr_write_b32 a50, 0\n"
        "v_mfma_f32_16x16x32_bf16 v[80:83], v[32:35], v[196:199], v[80:83]\n"
        "ds_read_b64_tr_b16 v[172:173], v228 offset:40960\n"
        "v_accvgpr_write_b32 a51, 0\n"
        "v_mfma_f32_16x16x32_bf16 v[112:115], v[32:35], v[200:203], v[112:115]\n"
        "ds_read_b64_tr_b16 v[174:175], v229 offset:40960\n"
        "v_mfma_f32_16x16x32_bf16 v[112:115], v[36:39], v[216:219], v[112:115]\n"
        "global_load_lds_dwordx4 v238, s[86:87] offset:0\n"
        "v_mfma_f32_16x16x32_bf16 v[116:119], v[44:47], v[216:219], v[116:119]\n"
        "ds_read_b64_tr_b16 v[168:169], v228 offset:32768\n"
        "v_mfma_f32_16x16x32_bf16 v[116:119], v[40:43], v[200:203], v[116:119]\n"
        "ds_read_b64_tr_b16 v[170:171], v229 offset:32768\n"
        "v_mfma_f32_16x16x32_bf16 v[148:151], v[40:43], v[204:207], v[148:151]\n"
        "ds_read_b64_tr_b16 v[40:41], v232 offset:32768\n"
        "v_mfma_f32_16x16x32_bf16 v[148:151], v[44:47], v[220:223], v[148:151]\n"
        "ds_read_b64_tr_b16 v[44:45], v232 offset:40960\n"
        "v_mfma_f32_16x16x32_bf16 v[144:147], v[36:39], v[220:223], v[144:147]\n"
        "global_load_lds_dwordx4 v239, s[86:87] offset:1024\n"
        "v_mfma_f32_16x16x32_bf16 v[144:147], v[32:35], v[204:207], v[144:147]\n"
        "ds_read_b64_tr_b16 v[32:33], v230 offset:32768\n"
        "v_accvgpr_read_b32 v184, a156\n"
        "v_mfma_f32_16x16x32_bf16 v[24:27], v[48:51], v[192:195], v[24:27]\n"
        "ds_read_b64_tr_b16 v[34:35], v231 offset:32768\n"
        "v_cvt_pk_bf16_f32 v184, v184, v185\n"
        "v_mfma_f32_16x16x32_bf16 v[24:27], v[52:55], v[208:211], v[24:27]\n"
        "ds_read_b64_tr_b16 v[36:37], v230 offset:40960\n"
        "v_cvt_pk_bf16_f32 v185, v186, v187\n"
        "v_mfma_f32_16x16x32_bf16 v[28:31], v[60:63], v[208:211], v[28:31]\n"
        "ds_read_b64_tr_b16 v[38:39], v231 offset:40960\n"
        "v_cvt_pk_bf16_f32 v186, v188, v189\n"
        "v_mfma_f32_16x16x32_bf16 v[28:31], v[56:59], v[192:195], v[28:31]\n"
        "global_load_lds_dwordx4 v240, s[86:87] offset:2048\n"
        "v_mfma_f32_16x16x32_bf16 v[92:95], v[56:59], v[196:199], v[92:95]\n"
        "ds_read_b64_tr_b16 v[46:47], v233 offset:40960\n"
        "v_cvt_pk_bf16_f32 v187, v190, v191\n"
        "v_mfma_f32_16x16x32_bf16 v[92:95], v[60:63], v[212:215], v[92:95]\n"
        "ds_read_b64_tr_b16 v[42:43], v233 offset:32768\n"
        "v_accvgpr_write_b32 a252, v184\n"
        "v_mfma_f32_16x16x32_bf16 v[88:91], v[52:55], v[212:215], v[88:91]\n"
        "ds_read_b128 v[192:195], v246 offset:2048\n"
        "v_accvgpr_write_b32 a253, v185\n"
        "v_mfma_f32_16x16x32_bf16 v[88:91], v[48:51], v[196:199], v[88:91]\n"
        "ds_read_b128 v[208:211], v247 offset:2048\n"
        "v_accvgpr_write_b32 a254, v186\n"
        "v_mfma_f32_16x16x32_bf16 v[120:123], v[48:51], v[200:203], v[120:123]\n"
        "global_load_lds_dwordx4 v241, s[86:87] offset:3072\n"
        "s_add_u32 m0, s72, 0x8000\n"
        "v_mfma_f32_16x16x32_bf16 v[120:123], v[52:55], v[216:219], v[120:123]\n"
        "ds_read_b128 v[196:199], v246 offset:0\n"
        "v_accvgpr_write_b32 a255, v187\n"
        "v_mfma_f32_16x16x32_bf16 v[124:127], v[60:63], v[216:219], v[124:127]\n"
        "s_add_u32 s86, s64, 0x580\n"
        "s_addc_u32 s87, s65, 0\n"
        "v_accvgpr_read_b32 v185, a21\n"
        "v_mfma_f32_16x16x32_bf16 v[124:127], v[56:59], v[200:203], v[124:127]\n"
        "ds_read_b128 v[212:215], v247 offset:0\n"
        "v_accvgpr_read_b32 v186, a22\n"
        "v_mfma_f32_16x16x32_bf16 v[156:159], v[56:59], v[204:207], v[156:159]\n"
        "ds_read_b128 v[200:203], v246 offset:6144\n"
        "v_accvgpr_read_b32 v187, a23\n"
        "v_mfma_f32_16x16x32_bf16 v[156:159], v[60:63], v[220:223], v[156:159]\n"
        "global_load_lds_dwordx4 v242, s[86:87] offset:0\n"
        "v_mfma_f32_16x16x32_bf16 v[152:155], v[52:55], v[220:223], v[152:155]\n"
        "ds_read_b128 v[216:219], v247 offset:6144\n"
        "v_accvgpr_read_b32 v188, a52\n"
        "v_mfma_f32_16x16x32_bf16 v[152:155], v[48:51], v[204:207], v[152:155]\n"
        "ds_read_b128 v[204:207], v246 offset:4096\n"
        "s_waitcnt lgkmcnt(5)\n"
        "v_mfma_f32_16x16x32_bf16 v[0:3], v[160:163], v[192:195], v[0:3]\n"
        "ds_read_b128 v[220:223], v247 offset:4096\n"
        "LHA7_%=:\n"
        "s_cbranch_execz LHA11_%=\n"
        "v_accvgpr_read_b32 v189, a53\n"
        "v_mfma_f32_16x16x32_bf16 v[0:3], v[164:167], v[208:211], v[0:3]\n"
        "ds_read_b64_tr_b16 v[48:49], v226 offset:33792\n"
        "v_accvgpr_read_b32 v190, a54\n"
        "v_mfma_f32_16x16x32_bf16 v[4:7], v[172:175], v[208:211], v[4:7]\n"
        "global_load_lds_dwordx4 v243, s[86:87] offset:1024\n"
        "v_mfma_f32_16x16x32_bf16 v[4:7], v[168:171], v[192:195], v[4:7]\n"
        "ds_read_b64_tr_b16 v[50:51], v227 offset:33792\n"
        "s_waitcnt lgkmcnt(6)\n"
        "v_mfma_f32_16x16x32_bf16 v[68:71], v[168:171], v[196:199], v[68:71]\n"
        "ds_read_b64_tr_b16 v[52:53], v226 offset:41984\n"
        "v_accvgpr_read_b32 v191, a55\n"
        "v_mfma_f32_16x16x32_bf16 v[68:71], v[172:175], v[212:215], v[68:71]\n"
        "ds_read_b64_tr_b16 v[54:55], v227 offset:41984\n"
        "v_accvgpr_write_b32 a52, 0\n"
        "v_mfma_f32_16x16x32_bf16 v[64:67], v[164:167], v[212:215], v[64:67]\n"
        "ds_read_b64_tr_b16 v[60:61], v228 offset:41984\n"
        "v_accvgpr_write_b32 a53, 0\n"
        "v_mfma_f32_16x16x32_bf16 v[64:67], v[160:163], v[196:199], v[64:67]\n"
        "s_waitcnt lgkmcnt(6)\n"
        "global_load_lds_dwordx4 v244, s[86:87] offset:2048\n"
        "v_mfma_f32_16x16x32_bf16 v[96:99], v[160:163], v[200:203], v[96:99]\n"
        "ds_read_b64_tr_b16 v[62:63], v229 offset:41984\n"
        "v_accvgpr_read_b32 v176, a16\n"
        "v_mfma_f32_16x16x32_bf16 v[96:99], v[164:167], v[216:219], v[96:99]\n"
        "ds_read_b64_tr_b16 v[56:57], v228 offset:33792\n"
        "v_cvt_pk_bf16_f32 v176, v176, v177\n"
        "v_mfma_f32_16x16x32_bf16 v[100:103], v[172:175], v[216:219], v[100:103]\n"
        "ds_read_b64_tr_b16 v[58:59], v229 offset:33792\n"
        "v_cvt_pk_bf16_f32 v177, v178, v179\n"
        "v_mfma_f32_16x16x32_bf16 v[100:103], v[168:171], v[200:203], v[100:103]\n"
        "s_waitcnt lgkmcnt(6)\n"
        "v_cvt_pk_bf16_f32 v178, v180, v181\n"
        "v_cvt_pk_bf16_f32 v179, v182, v183\n"
        "v_mfma_f32_16x16x32_bf16 v[132:135], v[168:171], v[204:207], v[132:135]\n"
        "global_load_lds_dwordx4 v245, s[86:87] offset:3072\n"
        "s_add_u32 m0, s72, 0x9000\n"
        "v_mfma_f32_16x16x32_bf16 v[132:135], v[172:175], v[220:223], v[132:135]\n"
        "ds_read_b64_tr_b16 v[172:173], v232 offset:41984\n"
        "v_accvgpr_write_b32 a176, v176\n"
        "v_mfma_f32_16x16x32_bf16 v[128:131], v[164:167], v[220:223], v[128:131]\n"
        "ds_read_b64_tr_b16 v[164:165], v230 offset:41984\n"
        "v_accvgpr_write_b32 a177, v177\n"
        "v_mfma_f32_16x16x32_bf16 v[128:131], v[160:163], v[204:207], v[128:131]\n"
        "ds_read_b64_tr_b16 v[160:161], v230 offset:33792\n"
        "v_accvgpr_write_b32 a178, v178\n"
        "v_mfma_f32_16x16x32_bf16 v[8:11], v[32:35], v[192:195], v[8:11]\n"
        "ds_read_b64_tr_b16 v[162:163], v231 offset:33792\n"
        "v_accvgpr_write_b32 a179, v179\n"
        "v_mfma_f32_16x16x32_bf16 v[8:11], v[36:39], v[208:211], v[8:11]\n"
        "s_add_u32 s86, s66, 0x580\n"
        "s_addc_u32 s87, s67, 0\n"
        "v_accvgpr_read_b32 v177, a25\n"
        "v_mfma_f32_16x16x32_bf16 v[12:15], v[44:47], v[208:211], v[12:15]\n"
        "global_load_lds_dwordx4 v242, s[86:87] offset:0\n"
        "ds_read_b64_tr_b16 v[166:167], v231 offset:41984\n"
        "v_mfma_f32_16x16x32_bf16 v[12:15], v[40:43], v[192:195], v[12:15]\n"
        "ds_read_b64_tr_b16 v[174:175], v233 offset:41984\n"
        "v_accvgpr_read_b32 v178, a26\n"
        "v_mfma_f32_16x16x32_bf16 v[76:79], v[40:43], v[196:199], v[76:79]\n"
        "ds_read_b64_tr_b16 v[168:169], v232 offset:33792\n"
        "v_accvgpr_read_b32 v179, a27\n"
        "v_mfma_f32_16x16x32_bf16 v[76:79], v[44:47], v[212:215], v[76:79]\n"
        "ds_read_b64_tr_b16 v[170:171], v233 offset:33792\n"
        "v_accvgpr_read_b32 v180, a56\n"
        "v_mfma_f32_16x16x32_bf16 v[72:75], v[36:39], v[212:215], v[72:75]\n"
        "v_accvgpr_read_b32 v181, a57\n"
        "v_accvgpr_read_b32 v182, a58\n"
        "v_mfma_f32_16x16x32_bf16 v[72:75], v[32:35], v[196:199], v[72:75]\n"
        "global_load_lds_dwordx4 v243, s[86:87] offset:1024\n"
        "v_accvgpr_read_b32 v183, a59\n"
        "v_mfma_f32_16x16x32_bf16 v[104:107], v[32:35], v[200:203], v[104:107]\n"
        "v_accvgpr_write_b32 a54, 0\n"
        "v_accvgpr_write_b32 a55, 0\n"
        "v_mfma_f32_16x16x32_bf16 v[104:107], v[36:39], v[216:219], v[104:107]\n"
        "v_accvgpr_write_b32 a56, 0\n"
        "v_accvgpr_write_b32 a57, 0\n"
        "v_mfma_f32_16x16x32_bf16 v[108:111], v[44:47], v[216:219], v[108:111]\n"
        "v_accvgpr_write_b32 a58, 0\n"
        "v_accvgpr_write_b32 a59, 0\n"
        "v_mfma_f32_16x16x32_bf16 v[108:111], v[40:43], v[200:203], v[108:111]\n"
        "global_store_dwordx4 v254, a[160:163], s[70:71] offset:0 sc1\n"
        "v_mfma_f32_16x16x32_bf16 v[140:143], v[40:43], v[204:207], v[140:143]\n"
        "s_waitcnt vmcnt(11) lgkmcnt(0)\n"
        "v_mfma_f32_16x16x32_bf16 v[140:143], v[44:47], v[220:223], v[140:143]\n"
        "s_barrier\n"
        "v_mfma_f32_16x16x32_bf16 v[136:139], v[36:39], v[220:223], v[136:139]\n"
        "ds_read_b64_tr_b16 v[36:37], v226 offset:57344\n"
        "v_mfma_f32_16x16x32_bf16 v[136:139], v[32:35], v[204:207], v[136:139]\n"
        "global_load_lds_dwordx4 v244, s[86:87] offset:2048\n"
        "v_mfma_f32_16x16x32_bf16 v[16:19], v[48:51], v[192:195], v[16:19]\n"
        "ds_read_b64_tr_b16 v[32:33], v226 offset:49152\n"
        "v_accvgpr_read_b32 v184, a20\n"
        "v_mfma_f32_16x16x32_bf16 v[16:19], v[52:55], v[208:211], v[16:19]\n"
        "ds_read_b64_tr_b16 v[34:35], v227 offset:49152\n"
        "v_cvt_pk_bf16_f32 v184, v184, v185\n"
        "v_mfma_f32_16x16x32_bf16 v[20:23], v[60:63], v[208:211], v[20:23]\n"
        "ds_read_b64_tr_b16 v[38:39], v227 offset:57344\n"
        "v_cvt_pk_bf16_f32 v185, v186, v187\n"
        "v_mfma_f32_16x16x32_bf16 v[20:23], v[56:59], v[192:195], v[20:23]\n"
        "ds_read_b64_tr_b16 v[44:45], v228 offset:57344\n"
        "v_cvt_pk_bf16_f32 v186, v188, v189\n"
        "v_mfma_f32_16x16x32_bf16 v[84:87], v[56:59], v[196:199], v[84:87]\n"
        "global_load_lds_dwordx4 v245, s[86:87] offset:3072\n"
        "s_add_u32 m0, s73, 0x20000\n"
        "v_mfma_f32_16x16x32_bf16 v[84:87], v[60:63], v[212:215], v[84:87]\n"
        "ds_read_b64_tr_b16 v[46:47], v229 offset:57344\n"
        "v_cvt_pk_bf16_f32 v187, v190, v191\n"
        "v_mfma_f32_16x16x32_bf16 v[80:83], v[52:55], v[212:215], v[80:83]\n"
        "ds_read_b64_tr_b16 v[40:41], v228 offset:49152\n"
        "v_accvgpr_write_b32 a180, v184\n"
        "v_mfma_f32_16x16x32_bf16 v[80:83], v[48:51], v[196:199], v[80:83]\n"
        "ds_read_b64_tr_b16 v[42:43], v229 offset:49152\n"
        "v_accvgpr_write_b32 a181, v185\n"
        "v_mfma_f32_16x16x32_bf16 v[112:115], v[48:51], v[200:203], v[112:115]\n"
        "s_add_u32 s86, s68, 0x600000\n"
        "s_addc_u32 s87, s69, 0\n"
        "v_accvgpr_write_b32 a182, v186\n"
        "v_mfma_f32_16x16x32_bf16 v[112:115], v[52:55], v[216:219], v[112:115]\n"
        "v_accvgpr_write_b32 a183, v187\n"
        "v_accvgpr_read_b32 v185, a29\n"
        "v_mfma_f32_16x16x32_bf16 v[116:119], v[60:63], v[216:219], v[116:119]\n"
        "global_load_lds_dwordx4 v238, s[86:87] offset:0\n"
        "v_accvgpr_read_b32 v186, a30\n"
        "v_mfma_f32_16x16x32_bf16 v[116:119], v[56:59], v[200:203], v[116:119]\n"
        "v_accvgpr_read_b32 v187, a31\n"
        "v_accvgpr_read_b32 v188, a60\n"
        "v_mfma_f32_16x16x32_bf16 v[148:151], v[56:59], v[204:207], v[148:151]\n"
        "ds_read_b64_tr_b16 v[56:57], v232 offset:49152\n"
        "v_accvgpr_read_b32 v189, a61\n"
        "v_mfma_f32_16x16x32_bf16 v[148:151], v[60:63], v[220:223], v[148:151]\n"
        "ds_read_b64_tr_b16 v[60:61], v232 offset:57344\n"
        "v_accvgpr_read_b32 v190, a62\n"
        "v_mfma_f32_16x16x32_bf16 v[144:147], v[52:55], v[220:223], v[144:147]\n"
        "ds_read_b64_tr_b16 v[52:53], v230 offset:57344\n"
        "v_accvgpr_read_b32 v191, a63\n"
        "v_mfma_f32_16x16x32_bf16 v[144:147], v[48:51], v[204:207], v[144:147]\n"
        "global_load_lds_dwordx4 v239, s[86:87] offset:1024\n"
        "ds_read_b64_tr_b16 v[48:49], v230 offset:49152\n"
        "v_mfma_f32_16x16x32_bf16 v[24:27], v[160:163], v[192:195], v[24:27]\n"
        "ds_read_b64_tr_b16 v[50:51], v231 offset:49152\n"
        "v_accvgpr_write_b32 a60, 0\n"
        "v_mfma_f32_16x16x32_bf16 v[24:27], v[164:167], v[208:211], v[24:27]\n"
        "ds_read_b64_tr_b16 v[54:55], v231 offset:57344\n"
        "v_accvgpr_write_b32 a61, 0\n"
        "v_mfma_f32_16x16x32_bf16 v[28:31], v[172:175], v[208:211], v[28:31]\n"
        "ds_read_b64_tr_b16 v[62:63], v233 offset:57344\n"
        "v_accvgpr_write_b32 a62, 0\n"
        "v_mfma_f32_16x16x32_bf16 v[28:31], v[168:171], v[192:195], v[28:31]\n"
        "ds_read_b64_tr_b16 v[58:59], v233 offset:49152\n"
        "v_accvgpr_write_b32 a63, 0\n"
        "v_mfma_f32_16x16x32_bf16 v[92:95], v[168:171], v[196:199], v[92:95]\n"
        "global_load_lds_dwordx4 v240, s[86:87] offset:2048\n"
        "ds_read_b128 v[192:195], v248 offset:2048\n"
        "v_mfma_f32_16x16x32_bf16 v[92:95], v[172:175], v[212:215], v[92:95]\n"
        "ds_read_b128 v[208:211], v249 offset:2048\n"
        "v_mfma_f32_16x16x32_bf16 v[88:91], v[164:167], v[212:215], v[88:91]\n"
        "ds_read_b128 v[212:215], v249 offset:0\n"
        "v_mfma_f32_16x16x32_bf16 v[88:91], v[160:163], v[196:199], v[88:91]\n"
        "ds_read_b128 v[196:199], v248 offset:0\n"
        "v_accvgpr_read_b32 v176, a24\n"
        "v_mfma_f32_16x16x32_bf16 v[120:123], v[160:163], v[200:203], v[120:123]\n"
        "v_cvt_pk_bf16_f32 v176, v176, v177\n"
        "v_cvt_pk_bf16_f32 v177, v178, v179\n"
        "v_mfma_f32_16x16x32_bf16 v[120:123], v[164:167], v[216:219], v[120:123]\n"
        "global_load_lds_dwordx4 v241, s[86:87] offset:3072\n"
        "s_add_u32 m0, s72, 0x0\n"
        "v_cvt_pk_bf16_f32 v178, v180, v181\n"
        "v_mfma_f32_16x16x32_bf16 v[124:127], v[172:175], v[216:219], v[124:127]\n"
        "s_add_u32 s86, s64, 0x600\n"
        "s_addc_u32 s87, s65, 0\n"
        "v_cvt_pk_bf16_f32 v179, v182, v183\n"
        "v_mfma_f32_16x16x32_bf16 v[124:127], v[168:171], v[200:203], v[124:127]\n"
        "ds_read_b128 v[200:203], v248 offset:6144\n"
        "v_accvgpr_write_b32 a184, v176\n"
        "v_mfma_f32_16x16x32_bf16 v[156:159], v[168:171], v[204:207], v[156:159]\n"
        "s_waitcnt lgkmcnt(4)\n"
        "v_accvgpr_write_b32 a185, v177\n"
        "v_accvgpr_write_b32 a186, v178\n"
        "v_mfma_f32_16x16x32_bf16 v[156:159], v[172:175], v[220:223], v[156:159]\n"
        "s_waitcnt lgkmcnt(3)\n"
        "v_accvgpr_write_b32 a187, v179\n"
        "v_accvgpr_read_b32 v177, a81\n"
        "v_mfma_f32_16x16x32_bf16 v[152:155], v[164:167], v[220:223], v[152:155]\n"
        "global_load_lds_dwordx4 v242, s[86:87] offset:0\n"
        "ds_read_b128 v[216:219], v249 offset:6144\n"
        "v_mfma_f32_16x16x32_bf16 v[152:155], v[160:163], v[204:207], v[152:155]\n"
        "ds_read_b128 v[204:207], v248 offset:4096\n"
        "v_accvgpr_read_b32 v178, a82\n"
        "v_mfma_f32_16x16x32_bf16 v[0:3], v[32:35], v[192:195], v[0:3]\n"
        "ds_read_b128 v[220:223], v249 offset:4096\n"
        "v_accvgpr_read_b32 v179, a83\n"
        "v_mfma_f32_16x16x32_bf16 v[0:3], v[36:39], v[208:211], v[0:3]\n"
        "ds_read_b64_tr_b16 v[160:161], v226 offset:50176\n"
        "v_accvgpr_read_b32 v180, a112\n"
        "v_mfma_f32_16x16x32_bf16 v[4:7], v[44:47], v[208:211], v[4:7]\n"
        "ds_read_b64_tr_b16 v[162:163], v227 offset:50176\n"
        "v_accvgpr_read_b32 v181, a113\n"
        "v_mfma_f32_16x16x32_bf16 v[4:7], v[40:43], v[192:195], v[4:7]\n"
        "global_load_lds_dwordx4 v243, s[86:87] offset:1024\n"
        "s_waitcnt lgkmcnt(5)\n"
        "v_mfma_f32_16x16x32_bf16 v[68:71], v[40:43], v[196:199], v[68:71]\n"
        "ds_read_b64_tr_b16 v[164:165], v226 offset:58368\n"
        "v_accvgpr_read_b32 v182, a114\n"
        "v_mfma_f32_16x16x32_bf16 v[68:71], v[44:47], v[212:215], v[68:71]\n"
        "ds_read_b64_tr_b16 v[166:167], v227 offset:58368\n"
        "v_accvgpr_read_b32 v183, a115\n"
        "v_mfma_f32_16x16x32_bf16 v[64:67], v[36:39], v[212:215], v[64:67]\n"
        "ds_read_b64_tr_b16 v[172:173], v228 offset:58368\n"
        "s_waitcnt lgkmcnt(7)\n"
        "v_mfma_f32_16x16x32_bf16 v[64:67], v[32:35], v[196:199], v[64:67]\n"
        "ds_read_b64_tr_b16 v[174:175], v229 offset:58368\n"
        "v_mfma_f32_16x16x32_bf16 v[96:99], v[32:35], v[200:203], v[96:99]\n"
        "global_load_lds_dwordx4 v244, s[86:87] offset:2048\n"
        "ds_read_b64_tr_b16 v[168:169], v228 offset:50176\n"
        "v_mfma_f32_16x16x32_bf16 v[96:99], v[36:39], v[216:219], v[96:99]\n"
        "ds_read_b64_tr_b16 v[170:171], v229 offset:50176\n"
        "s_waitcnt lgkmcnt(7)\n"
        "v_mfma_f32_16x16x32_bf16 v[100:103], v[44:47], v[216:219], v[100:103]\n"
        "v_mfma_f32_16x16x32_bf16 v[100:103], v[40:43], v[200:203], v[100:103]\n"
        "v_mfma_f32_16x16x32_bf16 v[132:135], v[40:43], v[204:207], v[132:135]\n"
        "ds_read_b64_tr_b16 v[40:41], v232 offset:50176\n"
        "v_mfma_f32_16x16x32_bf16 v[132:135], v[44:47], v[220:223], v[132:135]\n"
        "global_load_lds_dwordx4 v245, s[86:87] offset:3072\n"
        "s_add_u32 m0, s72, 0x1000\n"
        "v_mfma_f32_16x16x32_bf16 v[128:131], v[36:39], v[220:223], v[128:131]\n"
        "s_add_u32 s86, s66, 0x600\n"
        "s_addc_u32 s87, s67, 0\n"
        "v_mfma_f32_16x16x32_bf16 v[128:131], v[32:35], v[204:207], v[128:131]\n"
        "ds_read_b64_tr_b16 v[32:33], v230 offset:50176\n"
        "v_accvgpr_read_b32 v184, a28\n"
        "v_mfma_f32_16x16x32_bf16 v[8:11], v[48:51], v[192:195], v[8:11]\n"
        "ds_read_b64_tr_b16 v[34:35], v231 offset:50176\n"
        "v_cvt_pk_bf16_f32 v184, v184, v185\n"
        "v_mfma_f32_16x16x32_bf16 v[8:11], v[52:55], v[208:211], v[8:11]\n"
        "ds_read_b64_tr_b16 v[36:37], v230 offset:58368\n"
        "v_cvt_pk_bf16_f32 v185, v186, v187\n"
        "v_mfma_f32_16x16x32_bf16 v[12:15], v[60:63], v[208:211], v[12:15]\n"
        "global_load_lds_dwordx4 v242, s[86:87] offset:0\n"
        "v_cvt_pk_bf16_f32 v186, v188, v189\n"
        "v_mfma_f32_16x16x32_bf16 v[12:15], v[56:59], v[192:195], v[12:15]\n"
        "ds_read_b64_tr_b16 v[38:39], v231 offset:58368\n"
        "v_cvt_pk_bf16_f32 v187, v190, v191\n"
        "v_mfma_f32_16x16x32_bf16 v[76:79], v[56:59], v[196:199], v[76:79]\n"
        "ds_read_b64_tr_b16 v[44:45], v232 offset:58368\n"
        "v_accvgpr_write_b32 a188, v184\n"
        "v_mfma_f32_16x16x32_bf16 v[76:79], v[60:63], v[212:215], v[76:79]\n"
        "ds_read_b64_tr_b16 v[46:47], v233 offset:58368\n"
        "v_accvgpr_write_b32 a189, v185\n"
        "v_mfma_f32_16x16x32_bf16 v[72:75], v[52:55], v[212:215], v[72:75]\n"
        "ds_read_b64_tr_b16 v[42:43], v233 offset:50176\n"
        "v_accvgpr_write_b32 a190, v186\n"
        "v_mfma_f32_16x16x32_bf16 v[72:75], v[48:51], v[196:199], v[72:75]\n"
        "global_load_lds_dwordx4 v243, s[86:87] offset:1024\n"
        "v_accvgpr_write_b32 a191, v187\n"
        "v_mfma_f32_16x16x32_bf16 v[104:107], v[48:51], v[200:203], v[104:107]\n"
        "v_accvgpr_read_b32 v185, a85\n"
        "v_accvgpr_read_b32 v186, a86\n"
        "v_mfma_f32_16x16x32_bf16 v[104:107], v[52:55], v[216:219], v[104:107]\n"
        "v_accvgpr_read_b32 v187, a87\n"
        "v_accvgpr_read_b32 v188, a116\n"
        "v_mfma_f32_16x16x32_bf16 v[108:111], v[60:63], v[216:219], v[108:111]\n"
        "v_accvgpr_read_b32 v189, a117\n"
        "v_accvgpr_read_b32 v190, a118\n"
        "v_mfma_f32_16x16x32_bf16 v[108:111], v[56:59], v[200:203], v[108:111]\n"
        "v_accvgpr_read_b32 v191, a119\n"
        "v_mfma_f32_16x16x32_bf16 v[140:143], v[56:59], v[204:207], v[140:143]\n"
        "global_load_lds_dwordx4 v244, s[86:87] offset:2048\n"
        "s_waitcnt lgkmcnt(2)\n"
        "v_mfma_f32_16x16x32_bf16 v[140:143], v[60:63], v[220:223], v[140:143]\n"
        "v_mfma_f32_16x16x32_bf16 v[136:139], v[52:55], v[220:223], v[136:139]\n"
        "s_waitcnt vmcnt(11) lgkmcnt(0)\n"
        "v_mfma_f32_16x16x32_bf16 v[136:139], v[48:51], v[204:207], v[136:139]\n"
        "s_barrier\n"
        "v_mfma_f32_16x16x32_bf16 v[16:19], v[160:163], v[192:195], v[16:19]\n"
        "global_store_dwordx4 v254, a[164:167], s[70:71] offset:1024 sc1\n"
        "ds_read_b64_tr_b16 v[48:49], v226 offset:16384\n"
        "ds_read_b64_tr_b16 v[50:51], v227 offset:16384\n"
        "s_add_u32 s70, s38, s96\n"
        "s_addc_u32 s71, s39, 0\n"
        "v_mfma_f32_16x16x32_bf16 v[16:19], v[164:167], v[208:211], v[16:19]\n"
        "ds_read_b64_tr_b16 v[52:53], v226 offset:24576\n"
        "s_add_u32 s96, s52, 0x3000\n"
        "s_add_u32 s96, s96, s75\n"
        "v_mfma_f32_16x16x32_bf16 v[20:23], v[172:175], v[208:211], v[20:23]\n"
        "ds_read_b64_tr_b16 v[54:55], v227 offset:24576\n"
        "v_mfma_f32_16x16x32_bf16 v[20:23], v[168:171], v[192:195], v[20:23]\n"
        "ds_read_b64_tr_b16 v[60:61], v228 offset:24576\n"
        "v_mfma_f32_16x16x32_bf16 v[84:87], v[168:171], v[196:199], v[84:87]\n"
        "global_load_lds_dwordx4 v245, s[86:87] offset:3072\n"
        "s_add_u32 m0, s73, 0x24000\n"
        "v_mfma_f32_16x16x32_bf16 v[84:87], v[172:175], v[212:215], v[84:87]\n"
        "s_add_u32 s86, s68, 0x680000\n"
        "s_addc_u32 s87, s69, 0\n"
        "v_mfma_f32_16x16x32_bf16 v[80:83], v[164:167], v[212:215], v[80:83]\n"
        "ds_read_b64_tr_b16 v[62:63], v229 offset:24576\n"
        "v_mfma_f32_16x16x32_bf16 v[80:83], v[160:163], v[196:199], v[80:83]\n"
        "ds_read_b64_tr_b16 v[56:57], v228 offset:16384\n"
        "v_accvgpr_read_b32 v176, a80\n"
        "v_mfma_f32_16x16x32_bf16 v[112:115], v[160:163], v[200:203], v[112:115]\n"
        "ds_read_b64_tr_b16 v[58:59], v229 offset:16384\n"
        "v_cvt_pk_bf16_f32 v176, v176, v177\n"
        "v_mfma_f32_16x16x32_bf16 v[112:115], v[164:167], v[216:219], v[112:115]\n"
        "global_load_lds_dwordx4 v238, s[86:87] offset:0\n"
        "v_mfma_f32_16x16x32_bf16 v[116:119], v[172:175], v[216:219], v[116:119]\n"
        "v_cvt_pk_bf16_f32 v177, v178, v179\n"
        "v_cvt_pk_bf16_f32 v178, v180, v181\n"
        "v_mfma_f32_16x16x32_bf16 v[116:119], v[168:171], v[200:203], v[116:119]\n"
        "v_cvt_pk_bf16_f32 v179, v182, v183\n"
        "v_accvgpr_write_b32 a16, v176\n"
        "v_mfma_f32_16x16x32_bf16 v[148:151], v[168:171], v[204:207], v[148:151]\n"
        "ds_read_b64_tr_b16 v[168:169], v232 offset:16384\n"
        "v_accvgpr_write_b32 a17, v177\n"
        "v_mfma_f32_16x16x32_bf16 v[148:151], v[172:175], v[220:223], v[148:151]\n"
        "ds_read_b64_tr_b16 v[172:173], v232 offset:24576\n"
        "v_accvgpr_write_b32 a18, v178\n"
        "v_mfma_f32_16x16x32_bf16 v[144:147], v[164:167], v[220:223], v[144:147]\n"
        "global_load_lds_dwordx4 v239, s[86:87] offset:1024\n"
        "v_mfma_f32_16x16x32_bf16 v[144:147], v[160:163], v[204:207], v[144:147]\n"
        "ds_read_b64_tr_b16 v[160:161], v230 offset:16384\n"
        "v_accvgpr_write_b32 a19, v179\n"
        "v_mfma_f32_16x16x32_bf16 v[24:27], v[32:35], v[192:195], v[24:27]\n"
        "ds_read_b64_tr_b16 v[162:163], v231 offset:16384\n"
        "v_accvgpr_read_b32 v177, a89\n"
        "v_mfma_f32_16x16x32_bf16 v[24:27], v[36:39], v[208:211], v[24:27]\n"
        "ds_read_b64_tr_b16 v[164:165], v230 offset:24576\n"
        "v_accvgpr_read_b32 v178, a90\n"
        "v_mfma_f32_16x16x32_bf16 v[28:31], v[44:47], v[208:211], v[28:31]\n"
        "ds_read_b64_tr_b16 v[166:167], v231 offset:24576\n"
        "v_accvgpr_read_b32 v179, a91\n"
        "v_mfma_f32_16x16x32_bf16 v[28:31], v[40:43], v[192:195], v[28:31]\n"
        "global_load_lds_dwordx4 v240, s[86:87] offset:2048\n"
        "v_mfma_f32_16x16x32_bf16 v[92:95], v[40:43], v[196:199], v[92:95]\n"
        "ds_read_b64_tr_b16 v[174:175], v233 offset:24576\n"
        "v_accvgpr_read_b32 v180, a120\n"
        "v_mfma_f32_16x16x32_bf16 v[92:95], v[44:47], v[212:215], v[92:95]\n"
        "ds_read_b64_tr_b16 v[170:171], v233 offset:16384\n"
        "v_accvgpr_read_b32 v181, a121\n"
        "v_mfma_f32_16x16x32_bf16 v[88:91], v[36:39], v[212:215], v[88:91]\n"
        "ds_read_b128 v[192:195], v246 offset:34816\n"
        "v_accvgpr_read_b32 v182, a122\n"
        "v_mfma_f32_16x16x32_bf16 v[88:91], v[32:35], v[196:199], v[88:91]\n"
        "ds_read_b128 v[208:211], v247 offset:34816\n"
        "v_accvgpr_read_b32 v183, a123\n"
        "v_mfma_f32_16x16x32_bf16 v[120:123], v[32:35], v[200:203], v[120:123]\n"
        "global_load_lds_dwordx4 v241, s[86:87] offset:3072\n"
        "s_add_u32 m0, s72, 0x10000\n"
        "v_mfma_f32_16x16x32_bf16 v[120:123], v[36:39], v[216:219], v[120:123]\n"
        "s_add_u32 s86, s64, 0x680\n"
        "s_addc_u32 s87, s65, 0\n"
        "v_mfma_f32_16x16x32_bf16 v[124:127], v[44:47], v[216:219], v[124:127]\n"
        "ds_read_b128 v[196:199], v246 offset:32768\n"
        "v_mfma_f32_16x16x32_bf16 v[124:127], v[40:43], v[200:203], v[124:127]\n"
        "ds_read_b128 v[212:215], v247 offset:32768\n"
        "v_mfma_f32_16x16x32_bf16 v[156:159], v[40:43], v[204:207], v[156:159]\n"
        "ds_read_b128 v[200:203], v246 offset:38912\n"
        "v_mfma_f32_16x16x32_bf16 v[156:159], v[44:47], v[220:223], v[156:159]\n"
        "global_load_lds_dwordx4 v242, s[86:87] offset:0\n"
        "v_mfma_f32_16x16x32_bf16 v[152:155], v[36:39], v[220:223], v[152:155]\n"
        "ds_read_b128 v[216:219], v247 offset:38912\n"
        "s_waitcnt lgkmcnt(5)\n"
        "v_mfma_f32_16x16x32_bf16 v[152:155], v[32:35], v[204:207], v[152:155]\n"
        "ds_read_b128 v[204:207], v246 offset:36864\n"
        "v_accvgpr_read_b32 v184, a84\n"
        "v_mfma_f32_16x16x32_bf16 v[0:3], v[48:51], v[192:195], v[0:3]\n"
        "ds_read_b128 v[220:223], v247 offset:36864\n"
        "s_waitcnt lgkmcnt(6)\n"
        "v_mfma_f32_16x16x32_bf16 v[0:3], v[52:55], v[208:211], v[0:3]\n"
        "ds_read_b64_tr_b16 v[32:33], v226 offset:17408\n"
        "v_cvt_pk_bf16_f32 v184, v184, v185\n"
        "v_mfma_f32_16x16x32_bf16 v[4:7], v[60:63], v[208:211], v[4:7]\n"
        "global_load_lds_dwordx4 v243, s[86:87] offset:1024\n"
        "v_mfma_f32_16x16x32_bf16 v[4:7], v[56:59], v[192:195], v[4:7]\n"
        "s_waitcnt lgkmcnt(5)\n"
        "v_cvt_pk_bf16_f32 v185, v186, v187\n"
        "v_cvt_pk_bf16_f32 v186, v188, v189\n"
        "v_mfma_f32_16x16x32_bf16 v[68:71], v[56:59], v[196:199], v[68:71]\n"
        "s_waitcnt lgkmcnt(4)\n"
        "v_cvt_pk_bf16_f32 v187, v190, v191\n"
        "v_accvgpr_write_b32 a20, v184\n"
        "v_mfma_f32_16x16x32_bf16 v[68:71], v[60:63], v[212:215], v[68:71]\n"
        "ds_read_b64_tr_b16 v[34:35], v227 offset:17408\n"
        "v_accvgpr_write_b32 a21, v185\n"
        "v_mfma_f32_16x16x32_bf16 v[64:67], v[52:55], v[212:215], v[64:67]\n"
        "s_waitcnt lgkmcnt(4)\n"
        "v_accvgpr_write_b32 a22, v186\n"
        "v_accvgpr_write_b32 a23, v187\n"
        "v_mfma_f32_16x16x32_bf16 v[64:67], v[48:51], v[196:199], v[64:67]\n"
        "global_load_lds_dwordx4 v244, s[86:87] offset:2048\n"
        "v_mfma_f32_16x16x32_bf16 v[96:99], v[48:51], v[200:203], v[96:99]\n"
        "ds_read_b64_tr_b16 v[36:37], v226 offset:25600\n"
        "v_accvgpr_read_b32 v185, a93\n"
        "v_mfma_f32_16x16x32_bf16 v[96:99], v[52:55], v[216:219], v[96:99]\n"
        "ds_read_b64_tr_b16 v[38:39], v227 offset:25600\n"
        "v_accvgpr_read_b32 v186, a94\n"
        "v_mfma_f32_16x16x32_bf16 v[100:103], v[60:63], v[216:219], v[100:103]\n"
        "ds_read_b64_tr_b16 v[44:45], v228 offset:25600\n"
        "v_accvgpr_read_b32 v187, a95\n"
        "v_mfma_f32_16x16x32_bf16 v[100:103], v[56:59], v[200:203], v[100:103]\n"
        "ds_read_b64_tr_b16 v[46:47], v229 offset:25600\n"
        "s_waitcnt lgkmcnt(5)\n"
        "v_mfma_f32_16x16x32_bf16 v[132:135], v[56:59], v[204:207], v[132:135]\n"
        "ds_read_b64_tr_b16 v[40:41], v228 offset:17408\n"
        "global_load_lds_dwordx4 v245, s[86:87] offset:3072\n"
        "s_add_u32 m0, s72, 0x11000\n"
        "v_mfma_f32_16x16x32_bf16 v[132:135], v[60:63], v[220:223], v[132:135]\n"
        "ds_read_b64_tr_b16 v[42:43], v229 offset:17408\n"
        "s_add_u32 s86, s66, 0x680\n"
        "s_addc_u32 s87, s67, 0\n"
        "v_mfma_f32_16x16x32_bf16 v[128:131], v[52:55], v[220:223], v[128:131]\n"
        "ds_read_b64_tr_b16 v[52:53], v230 offset:25600\n"
        "v_accvgpr_read_b32 v188, a124\n"
        "v_mfma_f32_16x16x32_bf16 v[128:131], v[48:51], v[204:207], v[128:131]\n"
        "ds_read_b64_tr_b16 v[48:49], v230 offset:17408\n"
        "v_accvgpr_read_b32 v189, a125\n"
        "v_mfma_f32_16x16x32_bf16 v[8:11], v[160:163], v[192:195], v[8:11]\n"
        "ds_read_b64_tr_b16 v[50:51], v231 offset:17408\n"
        "v_accvgpr_read_b32 v190, a126\n"
        "v_mfma_f32_16x16x32_bf16 v[8:11], v[164:167], v[208:211], v[8:11]\n"
        "global_load_lds_dwordx4 v242, s[86:87] offset:0\n"
        "v_mfma_f32_16x16x32_bf16 v[12:15], v[172:175], v[208:211], v[12:15]\n"
        "ds_read_b64_tr_b16 v[54:55], v231 offset:25600\n"
        "v_accvgpr_read_b32 v191, a127\n"
        "v_mfma_f32_16x16x32_bf16 v[12:15], v[168:171], v[192:195], v[12:15]\n"
        "ds_read_b64_tr_b16 v[60:61], v232 offset:25600\n"
        "v_accvgpr_write_b32 a93, v252\n"
        "v_mfma_f32_16x16x32_bf16 v[76:79], v[168:171], v[196:199], v[76:79]\n"
        "ds_read_b64_tr_b16 v[62:63], v233 offset:25600\n"
        "v_mfma_f32_16x16x32_bf16 v[76:79], v[172:175], v[212:215], v[76:79]\n"
        "ds_read_b64_tr_b16 v[56:57], v232 offset:17408\n"
        "v_mfma_f32_16x16x32_bf16 v[72:75], v[164:167], v[212:215], v[72:75]\n"
        "global_load_lds_dwordx4 v243, s[86:87] offset:1024\n"
        "v_mfma_f32_16x16x32_bf16 v[72:75], v[160:163], v[196:199], v[72:75]\n"
        "ds_read_b64_tr_b16 v[58:59], v233 offset:17408\n"
        "v_accvgpr_read_b32 v176, a88\n"
        "v_mfma_f32_16x16x32_bf16 v[104:107], v[160:163], v[200:203], v[104:107]\n"
        "v_cvt_pk_bf16_f32 v176, v176, v177\n"
        "v_cvt_pk_bf16_f32 v177, v178, v179\n"
        "v_mfma_f32_16x16x32_bf16 v[104:107], v[164:167], v[216:219], v[104:107]\n"
        "v_cvt_pk_bf16_f32 v178, v180, v181\n"
        "v_cvt_pk_bf16_f32 v179, v182, v183\n"
        "v_mfma_f32_16x16x32_bf16 v[108:111], v[172:175], v[216:219], v[108:111]\n"
        "v_accvgpr_write_b32 a24, v176\n"
        "v_accvgpr_write_b32 a25, v177\n"
        "v_mfma_f32_16x16x32_bf16 v[108:111], v[168:171], v[200:203], v[108:111]\n"
        "global_load_lds_dwordx4 v244, s[86:87] offset:2048\n"
        "v_mfma_f32_16x16x32_bf16 v[140:143], v[168:171], v[204:207], v[140:143]\n"
        "s_waitcnt lgkmcnt(2)\n"
        "v_accvgpr_write_b32 a26, v178\n"
        "v_accvgpr_write_b32 a27, v179\n"
        "v_mfma_f32_16x16x32_bf16 v[140:143], v[172:175], v[220:223], v[140:143]\n"
        "v_lshrrev_b32 v178, 7, v254\n"
        "v_bfe_u32 v179, v254, 4, 3\n"
        "v_mfma_f32_16x16x32_bf16 v[136:139], v[164:167], v[220:223], v[136:139]\n"
        "v_and_b32 v180, 6, v178\n"
        "v_xor_b32 v180, v180, v179\n"
        "v_mfma_f32_16x16x32_bf16 v[136:139], v[160:163], v[204:207], v[136:139]\n"
        "s_waitcnt vmcnt(11) lgkmcnt(0)\n"
        "v_lshlrev_b32 v180, 4, v180\n"
        "v_lshl_add_u32 v181, v178, 11, v180\n"
        "v_mfma_f32_16x16x32_bf16 v[16:19], v[32:35], v[192:195], v[16:19]\n"
        "global_load_lds_dwordx4 v245, s[86:87] offset:3072\n"
        "s_add_u32 m0, s73, 0x1c000\n"
        "v_mfma_f32_16x16x32_bf16 v[16:19], v[36:39], v[208:211], v[16:19]\n"
        "s_barrier\n"
        "v_mfma_f32_16x16x32_bf16 v[20:23], v[44:47], v[208:211], v[20:23]\n"
        "s_add_u32 s86, s68, 0x700000\n"
        "s_addc_u32 s87, s69, 0\n"
        "s_mul_i32 s90, s43, 529408\n"
        "v_mfma_f32_16x16x32_bf16 v[20:23], v[40:43], v[192:195], v[20:23]\n"
        "ds_read_b64_tr_b16 v[160:161], v226 offset:32768\n"
        "s_add_u32 s90, s90, s72\n"
        "s_and_b32 s78, s45, 1\n"
        "v_mfma_f32_16x16x32_bf16 v[84:87], v[40:43], v[196:199], v[84:87]\n"
        "ds_read_b64_tr_b16 v[162:163], v227 offset:32768\n"
        "s_lshl_b32 s78, s78, 15\n"
        "s_add_u32 s78, s78, 0x10000\n"
        "v_mfma_f32_16x16x32_bf16 v[84:87], v[44:47], v[212:215], v[84:87]\n"
        "global_load_lds_dwordx4 v238, s[86:87] offset:0\n"
        "s_and_b32 s80, s40, 1\n"
        "v_mfma_f32_16x16x32_bf16 v[80:83], v[36:39], v[212:215], v[80:83]\n"
        "ds_read_b64_tr_b16 v[164:165], v226 offset:40960\n"
        "s_lshl_b32 s80, s80, 11\n"
        "s_and_b32 s79, s45, 1\n"
        "v_mfma_f32_16x16x32_bf16 v[80:83], v[32:35], v[196:199], v[80:83]\n"
        "ds_read_b64_tr_b16 v[166:167], v227 offset:40960\n"
        "s_lshl_b32 s79, s79, 15\n"
        "s_lshl_b32 s82, s40, 4\n"
        "v_mfma_f32_16x16x32_bf16 v[112:115], v[32:35], v[200:203], v[112:115]\n"
        "ds_read_b64_tr_b16 v[172:173], v228 offset:40960\n"
        "s_sub_u32 s83, 14, s44\n"
        "v_accvgpr_write_b32 a64, 0\n"
        "v_mfma_f32_16x16x32_bf16 v[112:115], v[36:39], v[216:219], v[112:115]\n"
        "ds_read_b64_tr_b16 v[174:175], v229 offset:40960\n"
        "s_lshl_b32 s50, s41, 7\n"
        "v_accvgpr_write_b32 a65, 0\n"
        "v_mfma_f32_16x16x32_bf16 v[116:119], v[44:47], v[216:219], v[116:119]\n"
        "ds_read_b64_tr_b16 v[168:169], v228 offset:32768\n"
        "global_load_lds_dwordx4 v239, s[86:87] offset:1024\n"
        "s_lshl2_add_u32 s50, s42, s50\n"
        "v_mfma_f32_16x16x32_bf16 v[116:119], v[40:43], v[200:203], v[116:119]\n"
        "ds_read_b64_tr_b16 v[170:171], v229 offset:32768\n"
        "s_add_u32 s50, s50, s40\n"
        "v_accvgpr_write_b32 a66, 0\n"
        "v_mfma_f32_16x16x32_bf16 v[148:151], v[40:43], v[204:207], v[148:151]\n"
        "ds_read_b64_tr_b16 v[40:41], v232 offset:32768\n"
        "s_lshl_b32 s50, s50, 12\n"
        "v_accvgpr_write_b32 a67, 0\n"
        "v_mfma_f32_16x16x32_bf16 v[148:151], v[44:47], v[220:223], v[148:151]\n"
        "ds_read_b64_tr_b16 v[44:45], v232 offset:40960\n"
        "s_mul_i32 s51, s40, 8320\n"
        "v_accvgpr_write_b32 a68, 0\n"
        "v_mfma_f32_16x16x32_bf16 v[144:147], v[36:39], v[220:223], v[144:147]\n"
        "ds_read_b64_tr_b16 v[36:37], v230 offset:40960\n"
        "s_add_u32 s51, s51, 0x18000\n"
        "v_accvgpr_write_b32 a69, 0\n"
        "v_mfma_f32_16x16x32_bf16 v[144:147], v[32:35], v[204:207], v[144:147]\n"
        "global_store_dwordx4 v254, a[168:171], s[70:71] offset:0 sc1\n"
        "ds_read_b64_tr_b16 v[32:33], v230 offset:32768\n"
        "ds_read_b64_tr_b16 v[34:35], v231 offset:32768\n"
        "v_accvgpr_read_b32 v184, a92\n"
        "v_mfma_f32_16x16x32_bf16 v[24:27], v[48:51], v[192:195], v[24:27]\n"
        "ds_read_b64_tr_b16 v[38:39], v231 offset:40960\n"
        "v_cvt_pk_bf16_f32 v184, v184, v185\n"
        "v_mfma_f32_16x16x32_bf16 v[24:27], v[52:55], v[208:211], v[24:27]\n"
        "ds_read_b64_tr_b16 v[46:47], v233 offset:40960\n"
        "v_cvt_pk_bf16_f32 v185, v186, v187\n"
        "v_mfma_f32_16x16x32_bf16 v[28:31], v[60:63], v[208:211], v[28:31]\n"
        "ds_read_b64_tr_b16 v[42:43], v233 offset:32768\n"
        "v_cvt_pk_bf16_f32 v186, v188, v189\n"
        "v_mfma_f32_16x16x32_bf16 v[28:31], v[56:59], v[192:195], v[28:31]\n"
        "global_load_lds_dwordx4 v240, s[86:87] offset:2048\n"
        "v_mfma_f32_16x16x32_bf16 v[92:95], v[56:59], v[196:199], v[92:95]\n"
        "ds_read_b128 v[192:195], v246 offset:2048\n"
        "v_cvt_pk_bf16_f32 v187, v190, v191\n"
        "v_mfma_f32_16x16x32_bf16 v[92:95], v[60:63], v[212:215], v[92:95]\n"
        "ds_read_b128 v[208:211], v247 offset:2048\n"
        "LHA8_%=:\n"
        "s_cbranch_execz LHRA_%=\n"
        "v_accvgpr_write_b32 a28, v184\n"
        "v_mfma_f32_16x16x32_bf16 v[88:91], v[52:55], v[212:215], v[88:91]\n"
        "ds_read_b128 v[212:215], v247 offset:0\n"
        "v_accvgpr_write_b32 a29, v185\n"
        "v_mfma_f32_16x16x32_bf16 v[88:91], v[48:51], v[196:199], v[88:91]\n"
        "ds_read_b128 v[196:199], v246 offset:0\n"
        "v_accvgpr_write_b32 a30, v186\n"
        "v_mfma_f32_16x16x32_bf16 v[120:123], v[48:51], v[200:203], v[120:123]\n"
        "global_load_lds_dwordx4 v241, s[86:87] offset:3072\n"
        "s_add_u32 m0, s72, 0x8000\n"
        "v_mfma_f32_16x16x32_bf16 v[120:123], v[52:55], v[216:219], v[120:123]\n"
        "s_add_u32 s86, s64, 0x700\n"
        "s_addc_u32 s87, s65, 0\n"
        "v_accvgpr_write_b32 a31, v187\n"
        "v_mfma_f32_16x16x32_bf16 v[124:127], v[60:63], v[216:219], v[124:127]\n"
        "ds_read_b128 v[216:219], v247 offset:6144\n"
        "v_accvgpr_write_b32 a70, 0\n"
        "v_mfma_f32_16x16x32_bf16 v[124:127], v[56:59], v[200:203], v[124:127]\n"
        "ds_read_b128 v[200:203], v246 offset:6144\n"
        "v_accvgpr_write_b32 a71, 0\n"
        "v_mfma_f32_16x16x32_bf16 v[156:159], v[56:59], v[204:207], v[156:159]\n"
        "ds_read_b64_tr_b16 v[56:57], v228 offset:33792\n"
        "v_lshrrev_b32 v186, 5, v254\n"
        "v_mfma_f32_16x16x32_bf16 v[156:159], v[60:63], v[220:223], v[156:159]\n"
        "global_load_lds_dwordx4 v242, s[86:87] offset:0\n"
        "v_mfma_f32_16x16x32_bf16 v[152:155], v[52:55], v[220:223], v[152:155]\n"
        "ds_read_b128 v[220:223], v247 offset:4096\n"
        "v_lshrrev_b32 v187, 9, v254\n"
        "v_mfma_f32_16x16x32_bf16 v[152:155], v[48:51], v[204:207], v[152:155]\n"
        "ds_read_b128 v[204:207], v246 offset:4096\n"
        "s_waitcnt lgkmcnt(5)\n"
        "v_mfma_f32_16x16x32_bf16 v[0:3], v[160:163], v[192:195], v[0:3]\n"
        "ds_read_b64_tr_b16 v[48:49], v226 offset:33792\n"
        "v_xor_b32 v186, v186, v187\n"
        "v_mfma_f32_16x16x32_bf16 v[0:3], v[164:167], v[208:211], v[0:3]\n"
        "ds_read_b64_tr_b16 v[50:51], v227 offset:33792\n"
        "v_and_b32 v186, 1, v186\n"
        "v_mfma_f32_16x16x32_bf16 v[4:7], v[172:175], v[208:211], v[4:7]\n"
        "global_load_lds_dwordx4 v243, s[86:87] offset:1024\n"
        "v_mfma_f32_16x16x32_bf16 v[4:7], v[168:171], v[192:195], v[4:7]\n"
        "ds_read_b64_tr_b16 v[52:53], v226 offset:41984\n"
        "v_cmp_eq_u32 vcc, 0, v186\n"
        "v_mfma_f32_16x16x32_bf16 v[68:71], v[168:171], v[196:199], v[68:71]\n"
        "ds_read_b64_tr_b16 v[54:55], v227 offset:41984\n"
        "v_and_b32 v187, 0x30, v254\n"
        "v_mfma_f32_16x16x32_bf16 v[68:71], v[172:175], v[212:215], v[68:71]\n"
        "ds_read_b64_tr_b16 v[60:61], v228 offset:41984\n"
        "s_add_u32 s92, s26, s50\n"
        "s_addc_u32 s93, s27, 0\n"
        "v_mfma_f32_16x16x32_bf16 v[64:67], v[164:167], v[212:215], v[64:67]\n"
        "ds_read_b64_tr_b16 v[62:63], v229 offset:41984\n"
        "s_add_u32 s50, s50, 0x3300000\n"
        "v_add_u32 v182, 4096, v181\n"
        "v_mfma_f32_16x16x32_bf16 v[64:67], v[160:163], v[196:199], v[64:67]\n"
        "s_waitcnt lgkmcnt(6)\n"
        "global_load_lds_dwordx4 v244, s[86:87] offset:2048\n"
        "v_mfma_f32_16x16x32_bf16 v[96:99], v[160:163], v[200:203], v[96:99]\n"
        "ds_read_b64_tr_b16 v[58:59], v229 offset:33792\n"
        "s_add_u32 s94, s38, s50\n"
        "s_addc_u32 s95, s39, 0\n"
        "v_mfma_f32_16x16x32_bf16 v[96:99], v[164:167], v[216:219], v[96:99]\n"
        "s_add_u32 s50, s51, 4128\n"
        "v_accvgpr_write_b32 a84, v182\n"
        "v_xor_b32 v178, 4, v178\n"
        "v_mfma_f32_16x16x32_bf16 v[100:103], v[172:175], v[216:219], v[100:103]\n"
        "v_add_u32 v182, 3200, v181\n"
        "v_lshl_add_u32 v178, v178, 11, v180\n"
        "v_mfma_f32_16x16x32_bf16 v[100:103], v[168:171], v[200:203], v[100:103]\n"
        "v_accvgpr_write_b32 a85, v182\n"
        "v_add_u32 v182, 18432, v178\n"
        "v_mfma_f32_16x16x32_bf16 v[132:135], v[168:171], v[204:207], v[132:135]\n"
        "global_load_lds_dwordx4 v245, s[86:87] offset:3072\n"
        "s_add_u32 m0, s72, 0x9000\n"
        "v_mfma_f32_16x16x32_bf16 v[132:135], v[172:175], v[220:223], v[132:135]\n"
        "s_add_u32 s86, s66, 0x700\n"
        "s_addc_u32 s87, s67, 0\n"
        "v_add_u32 v180, s51, v254\n"
        "v_mfma_f32_16x16x32_bf16 v[128:131], v[164:167], v[220:223], v[128:131]\n"
        "ds_read_b64_tr_b16 v[164:165], v230 offset:41984\n"
        "v_accvgpr_write_b32 a86, v182\n"
        "v_mfma_f32_16x16x32_bf16 v[128:131], v[160:163], v[204:207], v[128:131]\n"
        "ds_read_b64_tr_b16 v[160:161], v230 offset:33792\n"
        "v_add_u32 v180, 4096, v180\n"
        "v_mfma_f32_16x16x32_bf16 v[8:11], v[32:35], v[192:195], v[8:11]\n"
        "ds_read_b64_tr_b16 v[162:163], v231 offset:33792\n"
        "v_add_u32 v182, 17536, v178\n"
        "v_mfma_f32_16x16x32_bf16 v[8:11], v[36:39], v[208:211], v[8:11]\n"
        "global_load_lds_dwordx4 v242, s[86:87] offset:0\n"
        "v_mfma_f32_16x16x32_bf16 v[12:15], v[44:47], v[208:211], v[12:15]\n"
        "ds_read_b64_tr_b16 v[166:167], v231 offset:41984\n"
        "v_bfe_u32 v176, v254, 4, 4\n"
        "v_mfma_f32_16x16x32_bf16 v[12:15], v[40:43], v[192:195], v[12:15]\n"
        "ds_read_b64_tr_b16 v[172:173], v232 offset:41984\n"
        "v_and_b32 v181, 1, v176\n"
        "v_mfma_f32_16x16x32_bf16 v[76:79], v[40:43], v[196:199], v[76:79]\n"
        "ds_read_b64_tr_b16 v[174:175], v233 offset:41984\n"
        "v_lshrrev_b32 v177, 8, v254\n"
        "v_mfma_f32_16x16x32_bf16 v[76:79], v[44:47], v[212:215], v[76:79]\n"
        "ds_read_b64_tr_b16 v[168:169], v232 offset:33792\n"
        "v_accvgpr_write_b32 a87, v182\n"
        "v_mfma_f32_16x16x32_bf16 v[72:75], v[36:39], v[212:215], v[72:75]\n"
        "global_load_lds_dwordx4 v243, s[86:87] offset:1024\n"
        "v_mfma_f32_16x16x32_bf16 v[72:75], v[32:35], v[196:199], v[72:75]\n"
        "ds_read_b64_tr_b16 v[170:171], v233 offset:33792\n"
        "v_bfe_u32 v188, v254, 4, 5\n"
        "v_mfma_f32_16x16x32_bf16 v[104:107], v[32:35], v[200:203], v[104:107]\n"
        "v_mfma_f32_16x16x32_bf16 v[104:107], v[36:39], v[216:219], v[104:107]\n"
        "v_mfma_f32_16x16x32_bf16 v[108:111], v[44:47], v[216:219], v[108:111]\n"
        "v_mfma_f32_16x16x32_bf16 v[108:111], v[40:43], v[200:203], v[108:111]\n"
        "global_load_lds_dwordx4 v244, s[86:87] offset:2048\n"
        "v_mfma_f32_16x16x32_bf16 v[140:143], v[40:43], v[204:207], v[140:143]\n"
        "s_waitcnt lgkmcnt(2)\n"
        "v_mfma_f32_16x16x32_bf16 v[140:143], v[44:47], v[220:223], v[140:143]\n"
        "v_mfma_f32_16x16x32_bf16 v[136:139], v[36:39], v[220:223], v[136:139]\n"
        "v_mfma_f32_16x16x32_bf16 v[136:139], v[32:35], v[204:207], v[136:139]\n"
        "s_waitcnt vmcnt(12) lgkmcnt(0)\n"
        "v_mfma_f32_16x16x32_bf16 v[16:19], v[48:51], v[192:195], v[16:19]\n"
        "global_load_lds_dwordx4 v245, s[86:87] offset:3072\n"
        "s_add_u32 m0, s73, 0x20000\n"
        "v_mfma_f32_16x16x32_bf16 v[16:19], v[52:55], v[208:211], v[16:19]\n"
        "s_barrier\n"
        "v_mfma_f32_16x16x32_bf16 v[20:23], v[60:63], v[208:211], v[20:23]\n"
        "s_add_u32 s86, s68, 0x780000\n"
        "s_addc_u32 s87, s69, 0\n"
        "v_mfma_f32_16x16x32_bf16 v[20:23], v[56:59], v[192:195], v[20:23]\n"
        "ds_read_b64_tr_b16 v[32:33], v226 offset:49152\n"
        "v_mfma_f32_16x16x32_bf16 v[84:87], v[56:59], v[196:199], v[84:87]\n"
        "ds_read_b64_tr_b16 v[34:35], v227 offset:49152\n"
        "v_mfma_f32_16x16x32_bf16 v[84:87], v[60:63], v[212:215], v[84:87]\n"
        "global_load_lds_dwordx4 v238, s[86:87] offset:0\n"
        "v_mfma_f32_16x16x32_bf16 v[80:83], v[52:55], v[212:215], v[80:83]\n"
        "ds_read_b64_tr_b16 v[36:37], v226 offset:57344\n"
        "v_mfma_f32_16x16x32_bf16 v[80:83], v[48:51], v[196:199], v[80:83]\n"
        "ds_read_b64_tr_b16 v[38:39], v227 offset:57344\n"
        "v_mfma_f32_16x16x32_bf16 v[112:115], v[48:51], v[200:203], v[112:115]\n"
        "ds_read_b64_tr_b16 v[44:45], v228 offset:57344\n"
        "v_mfma_f32_16x16x32_bf16 v[112:115], v[52:55], v[216:219], v[112:115]\n"
        "ds_read_b64_tr_b16 v[46:47], v229 offset:57344\n"
        "v_mfma_f32_16x16x32_bf16 v[116:119], v[60:63], v[216:219], v[116:119]\n"
        "ds_read_b64_tr_b16 v[40:41], v228 offset:49152\n"
        "global_load_lds_dwordx4 v239, s[86:87] offset:1024\n"
        "v_mfma_f32_16x16x32_bf16 v[116:119], v[56:59], v[200:203], v[116:119]\n"
        "ds_read_b64_tr_b16 v[42:43], v229 offset:49152\n"
        "v_mfma_f32_16x16x32_bf16 v[148:151], v[56:59], v[204:207], v[148:151]\n"
        "ds_read_b64_tr_b16 v[56:57], v232 offset:49152\n"
        "v_mfma_f32_16x16x32_bf16 v[148:151], v[60:63], v[220:223], v[148:151]\n"
        "ds_read_b64_tr_b16 v[60:61], v232 offset:57344\n"
        "v_mfma_f32_16x16x32_bf16 v[144:147], v[52:55], v[220:223], v[144:147]\n"
        "ds_read_b64_tr_b16 v[52:53], v230 offset:57344\n"
        "v_mfma_f32_16x16x32_bf16 v[144:147], v[48:51], v[204:207], v[144:147]\n"
        "global_store_dwordx4 v254, a[172:175], s[70:71] offset:1024 sc1\n"
        "ds_read_b64_tr_b16 v[48:49], v230 offset:49152\n"
        "ds_read_b64_tr_b16 v[50:51], v231 offset:49152\n"
        "s_add_u32 s70, s38, s96\n"
        "s_addc_u32 s71, s39, 0\n"
        "v_mfma_f32_16x16x32_bf16 v[24:27], v[160:163], v[192:195], v[24:27]\n"
        "ds_read_b64_tr_b16 v[54:55], v231 offset:57344\n"
        "s_add_u32 s96, s52, 0x0\n"
        "v_mfma_f32_16x16x32_bf16 v[24:27], v[164:167], v[208:211], v[24:27]\n"
        "ds_read_b64_tr_b16 v[62:63], v233 offset:57344\n"
        "v_mfma_f32_16x16x32_bf16 v[28:31], v[172:175], v[208:211], v[28:31]\n"
        "ds_read_b64_tr_b16 v[58:59], v233 offset:49152\n"
        "v_mfma_f32_16x16x32_bf16 v[28:31], v[168:171], v[192:195], v[28:31]\n"
        "global_load_lds_dwordx4 v240, s[86:87] offset:2048\n"
        "v_mfma_f32_16x16x32_bf16 v[92:95], v[168:171], v[196:199], v[92:95]\n"
        "ds_read_b128 v[192:195], v248 offset:2048\n"
        "v_add_u32 v240, s78, v254\n"
        "v_mfma_f32_16x16x32_bf16 v[92:95], v[172:175], v[212:215], v[92:95]\n"
        "ds_read_b128 v[208:211], v249 offset:2048\n"
        "v_mfma_f32_16x16x32_bf16 v[88:91], v[164:167], v[212:215], v[88:91]\n"
        "ds_read_b128 v[212:215], v249 offset:0\n"
        "v_mfma_f32_16x16x32_bf16 v[88:91], v[160:163], v[196:199], v[88:91]\n"
        "ds_read_b128 v[196:199], v248 offset:0\n"
        "v_mfma_f32_16x16x32_bf16 v[120:123], v[160:163], v[200:203], v[120:123]\n"
        "global_load_lds_dwordx4 v241, s[86:87] offset:3072\n"
        "s_add_u32 m0, s72, 0x0\n"
        "v_mfma_f32_16x16x32_bf16 v[120:123], v[164:167], v[216:219], v[120:123]\n"
        "s_add_u32 s86, s64, 0x780\n"
        "s_addc_u32 s87, s65, 0\n"
        "v_add_u32 v241, s79, v254\n"
        "v_mfma_f32_16x16x32_bf16 v[124:127], v[172:175], v[216:219], v[124:127]\n"
        "ds_read_b128 v[216:219], v249 offset:6144\n"
        "s_add_u32 s64, s38, s90\n"
        "s_addc_u32 s65, s39, 0\n"
        "v_mfma_f32_16x16x32_bf16 v[124:127], v[168:171], v[200:203], v[124:127]\n"
        "ds_read_b128 v[200:203], v248 offset:6144\n"
        "s_lshl_b32 s90, s40, 13\n"
        "s_and_b32 s90, s90, 0x4000\n"
        "v_mfma_f32_16x16x32_bf16 v[156:159], v[168:171], v[204:207], v[156:159]\n"
        "ds_read_b64_tr_b16 v[168:169], v228 offset:50176\n"
        "s_add_u32 s91, s78, s90\n"
        "s_add_u32 s91, s91, s80\n"
        "v_mfma_f32_16x16x32_bf16 v[156:159], v[172:175], v[220:223], v[156:159]\n"
        "global_load_lds_dwordx4 v242, s[86:87] offset:0\n"
        "s_xor_b32 s78, s78, 0x8000\n"
        "v_mfma_f32_16x16x32_bf16 v[152:155], v[164:167], v[220:223], v[152:155]\n"
        "ds_read_b128 v[220:223], v249 offset:4096\n"
        "s_add_u32 s78, s78, s72\n"
        "s_waitcnt lgkmcnt(5)\n"
        "v_mfma_f32_16x16x32_bf16 v[152:155], v[160:163], v[204:207], v[152:155]\n"
        "ds_read_b128 v[204:207], v248 offset:4096\n"
        "v_mfma_f32_16x16x32_bf16 v[0:3], v[32:35], v[192:195], v[0:3]\n"
        "ds_read_b64_tr_b16 v[160:161], v226 offset:50176\n"
        "s_waitcnt lgkmcnt(6)\n"
        "v_mfma_f32_16x16x32_bf16 v[0:3], v[36:39], v[208:211], v[0:3]\n"
        "ds_read_b64_tr_b16 v[162:163], v227 offset:50176\n"
        "v_mfma_f32_16x16x32_bf16 v[4:7], v[44:47], v[208:211], v[4:7]\n"
        "global_load_lds_dwordx4 v243, s[86:87] offset:1024\n"
        "v_mfma_f32_16x16x32_bf16 v[4:7], v[40:43], v[192:195], v[4:7]\n"
        "ds_read_b64_tr_b16 v[164:165], v226 offset:58368\n"
        "v_mfma_f32_16x16x32_bf16 v[68:71], v[40:43], v[196:199], v[68:71]\n"
        "ds_read_b64_tr_b16 v[166:167], v227 offset:58368\n"
        "s_waitcnt lgkmcnt(6)\n"
        "v_mfma_f32_16x16x32_bf16 v[68:71], v[44:47], v[212:215], v[68:71]\n"
        "ds_read_b64_tr_b16 v[172:173], v228 offset:58368\n"
        "v_mfma_f32_16x16x32_bf16 v[64:67], v[36:39], v[212:215], v[64:67]\n"
        "ds_read_b64_tr_b16 v[174:175], v229 offset:58368\n"
        "v_mfma_f32_16x16x32_bf16 v[64:67], v[32:35], v[196:199], v[64:67]\n"
        "global_load_lds_dwordx4 v244, s[86:87] offset:2048\n"
        "v_mfma_f32_16x16x32_bf16 v[96:99], v[32:35], v[200:203], v[96:99]\n"
        "ds_read_b64_tr_b16 v[170:171], v229 offset:50176\n"
        "v_xor_b32 v241, 0x8000, v241\n"
        "v_mfma_f32_16x16x32_bf16 v[96:99], v[36:39], v[216:219], v[96:99]\n"
        "s_waitcnt lgkmcnt(5)\n"
        "v_mfma_f32_16x16x32_bf16 v[100:103], v[44:47], v[216:219], v[100:103]\n"
        "v_mfma_f32_16x16x32_bf16 v[100:103], v[40:43], v[200:203], v[100:103]\n"
        "v_mfma_f32_16x16x32_bf16 v[132:135], v[40:43], v[204:207], v[132:135]\n"
        "global_load_lds_dwordx4 v245, s[86:87] offset:3072\n"
        "s_add_u32 m0, s72, 0x1000\n"
        "v_mfma_f32_16x16x32_bf16 v[132:135], v[44:47], v[220:223], v[132:135]\n"
        "s_add_u32 s86, s66, 0x780\n"
        "s_addc_u32 s87, s67, 0\n"
        "v_mfma_f32_16x16x32_bf16 v[128:131], v[36:39], v[220:223], v[128:131]\n"
        "ds_read_b64_tr_b16 v[36:37], v230 offset:58368\n"
        "s_add_u32 s66, s64, 0x1100000\n"
        "s_addc_u32 s67, s65, 0\n"
        "v_mfma_f32_16x16x32_bf16 v[128:131], v[32:35], v[204:207], v[128:131]\n"
        "ds_read_b64_tr_b16 v[32:33], v230 offset:50176\n"
        "v_mfma_f32_16x16x32_bf16 v[8:11], v[48:51], v[192:195], v[8:11]\n"
        "ds_read_b64_tr_b16 v[34:35], v231 offset:50176\n"
        "v_mfma_f32_16x16x32_bf16 v[8:11], v[52:55], v[208:211], v[8:11]\n"
        "global_load_lds_dwordx4 v242, s[86:87] offset:0\n"
        "v_mfma_f32_16x16x32_bf16 v[12:15], v[60:63], v[208:211], v[12:15]\n"
        "ds_read_b64_tr_b16 v[38:39], v231 offset:58368\n"
        "v_lshrrev_b32 v242, 6, v254\n"
        "v_mfma_f32_16x16x32_bf16 v[12:15], v[56:59], v[192:195], v[12:15]\n"
        "ds_read_b64_tr_b16 v[44:45], v232 offset:58368\n"
        "v_and_b32 v242, 12, v242\n"
        "v_mfma_f32_16x16x32_bf16 v[76:79], v[56:59], v[196:199], v[76:79]\n"
        "ds_read_b64_tr_b16 v[46:47], v233 offset:58368\n"
        "v_mfma_f32_16x16x32_bf16 v[76:79], v[60:63], v[212:215], v[76:79]\n"
        "ds_read_b64_tr_b16 v[40:41], v232 offset:50176\n"
        "v_mfma_f32_16x16x32_bf16 v[72:75], v[52:55], v[212:215], v[72:75]\n"
        "global_load_lds_dwordx4 v243, s[86:87] offset:1024\n"
        "v_mfma_f32_16x16x32_bf16 v[72:75], v[48:51], v[196:199], v[72:75]\n"
        "ds_read_b64_tr_b16 v[42:43], v233 offset:50176\n"
        "v_bfe_u32 v243, v254, 4, 4\n"
        "v_mfma_f32_16x16x32_bf16 v[104:107], v[48:51], v[200:203], v[104:107]\n"
        "v_sub_u32 v243, v243, v242\n"
        "v_add_u32 v243, s82, v243\n"
        "v_mfma_f32_16x16x32_bf16 v[104:107], v[52:55], v[216:219], v[104:107]\n"
        "s_mov_b32 s82, -1\n"
        "v_cmp_gt_i32 s[10:11], 0, v243\n"
        "v_cmp_gt_i32 s[12:13], 1, v243\n"
        "v_mfma_f32_16x16x32_bf16 v[108:111], v[60:63], v[216:219], v[108:111]\n"
        "v_cmp_gt_i32 s[14:15], 2, v243\n"
        "v_cmp_gt_i32 s[16:17], 3, v243\n"
        "v_mfma_f32_16x16x32_bf16 v[108:111], v[56:59], v[200:203], v[108:111]\n"
        "global_store_dwordx4 v254, a[0:3], s[70:71] offset:0 sc1\n"
        "v_cmp_gt_i32 s[20:21], 16, v243\n"
        "v_cmp_gt_i32 s[22:23], 17, v243\n"
        "v_cmp_gt_i32 s[24:25], 18, v243\n"
        "v_cmp_gt_i32 s[28:29], 19, v243\n"
        "v_mfma_f32_16x16x32_bf16 v[140:143], v[56:59], v[204:207], v[140:143]\n"
        "v_cmp_gt_i32 s[30:31], 32, v243\n"
        "v_cmp_gt_i32 s[56:57], 33, v243\n"
        "v_mfma_f32_16x16x32_bf16 v[140:143], v[60:63], v[220:223], v[140:143]\n"
        "s_waitcnt lgkmcnt(1)\n"
        "v_cmp_gt_i32 s[58:59], 34, v243\n"
        "v_cmp_gt_i32 s[60:61], 35, v243\n"
        "v_mfma_f32_16x16x32_bf16 v[136:139], v[52:55], v[220:223], v[136:139]\n"
        "s_waitcnt vmcnt(12) lgkmcnt(0)\n"
        "v_cmp_gt_i32 s[62:63], 48, v243\n"
        "v_mfma_f32_16x16x32_bf16 v[136:139], v[48:51], v[204:207], v[136:139]\n"
        "global_load_lds_dwordx4 v244, s[86:87] offset:2048\n"
        "v_mfma_f32_16x16x32_bf16 v[16:19], v[160:163], v[192:195], v[16:19]\n"
        "s_barrier\n"
        "v_mfma_f32_16x16x32_bf16 v[16:19], v[164:167], v[208:211], v[16:19]\n"
        "ds_read_b64_tr_b16 v[48:49], v226 offset:16384\n"
        "v_add_u32 v244, s91, v254\n"
        "v_mfma_f32_16x16x32_bf16 v[20:23], v[172:175], v[208:211], v[20:23]\n"
        "ds_read_b64_tr_b16 v[50:51], v227 offset:16384\n"
        "s_xor_b32 s91, s79, 0x8000\n"
        "s_add_u32 s91, s91, s90\n"
        "v_mfma_f32_16x16x32_bf16 v[20:23], v[168:171], v[192:195], v[20:23]\n"
        "ds_read_b64_tr_b16 v[52:53], v226 offset:24576\n"
        "s_lshl1_add_u32 s91, s80, s91\n"
        "v_cmp_gt_i32 s[84:85], 49, v243\n"
        "v_mfma_f32_16x16x32_bf16 v[84:87], v[168:171], v[196:199], v[84:87]\n"
        "global_load_lds_dwordx4 v245, s[86:87] offset:3072\n"
        "s_add_u32 s79, s79, s72\n"
        "v_mfma_f32_16x16x32_bf16 v[84:87], v[172:175], v[212:215], v[84:87]\n"
        "ds_read_b64_tr_b16 v[54:55], v227 offset:24576\n"
        "s_lshl_b32 s80, s41, 5\n"
        "v_add_u32 v245, s91, v254\n"
        "v_mfma_f32_16x16x32_bf16 v[80:83], v[164:167], v[212:215], v[80:83]\n"
        "ds_read_b64_tr_b16 v[60:61], v228 offset:24576\n"
        "s_add_u32 s80, s80, 256\n"
        "v_cmp_gt_i32 s[86:87], 50, v243\n"
        "v_mfma_f32_16x16x32_bf16 v[80:83], v[160:163], v[196:199], v[80:83]\n"
        "ds_read_b64_tr_b16 v[62:63], v229 offset:24576\n"
        "s_add_u32 s81, s80, s42\n"
        "v_cmp_gt_i32 s[88:89], 51, v243\n"
        "v_mfma_f32_16x16x32_bf16 v[112:115], v[160:163], v[200:203], v[112:115]\n"
        "ds_read_b64_tr_b16 v[56:57], v228 offset:16384\n"
        "s_lshl_b32 s81, s81, 7\n"
        "v_mov_b32 v242, 0x3f803f80\n"
        "v_mfma_f32_16x16x32_bf16 v[112:115], v[164:167], v[216:219], v[112:115]\n"
        "ds_read_b64_tr_b16 v[58:59], v229 offset:16384\n"
        "s_and_b32 s80, s81, 0xfffffc7f\n"
        "v_accvgpr_write_b32 a80, v242\n"
        "v_mfma_f32_16x16x32_bf16 v[116:119], v[172:175], v[216:219], v[116:119]\n"
        "global_store_dwordx4 v254, a[4:7], s[70:71] offset:1024 sc1\n"
        "s_add_u32 s70, s38, s96\n"
        "s_addc_u32 s71, s39, 0\n"
        "s_add_u32 s96, s53, 0x2000\n"
        "v_accvgpr_write_b32 a81, v242\n"
        "v_accvgpr_write_b32 a82, v242\n"
        "v_mfma_f32_16x16x32_bf16 v[116:119], v[168:171], v[200:203], v[116:119]\n"
        "s_xor_b32 s79, s79, 0x8000\n"
        "v_accvgpr_write_b32 a83, v242\n"
        "v_cndmask_b32 v186, 0, v242, vcc\n"
        "v_mfma_f32_16x16x32_bf16 v[148:151], v[168:171], v[204:207], v[148:151]\n"
        "ds_read_b64_tr_b16 v[168:169], v232 offset:16384\n"
        "v_accvgpr_write_b32 a72, v186\n"
        "v_mfma_f32_16x16x32_bf16 v[148:151], v[172:175], v[220:223], v[148:151]\n"
        "ds_read_b64_tr_b16 v[172:173], v232 offset:24576\n"
        "v_accvgpr_write_b32 a73, v186\n"
        "v_mfma_f32_16x16x32_bf16 v[144:147], v[164:167], v[220:223], v[144:147]\n"
        "ds_read_b64_tr_b16 v[164:165], v230 offset:24576\n"
        "v_accvgpr_write_b32 a74, v186\n"
        "v_mfma_f32_16x16x32_bf16 v[144:147], v[160:163], v[204:207], v[144:147]\n"
        "global_store_dwordx4 v254, a[8:11], s[70:71] offset:0 sc1\n"
        "ds_read_b64_tr_b16 v[160:161], v230 offset:16384\n"
        "ds_read_b64_tr_b16 v[162:163], v231 offset:16384\n"
        "v_accvgpr_write_b32 a75, v186\n"
        "v_mfma_f32_16x16x32_bf16 v[24:27], v[32:35], v[192:195], v[24:27]\n"
        "ds_read_b64_tr_b16 v[166:167], v231 offset:24576\n"
        "v_cmp_eq_u32 vcc, 0, v187\n"
        "v_mfma_f32_16x16x32_bf16 v[24:27], v[36:39], v[208:211], v[24:27]\n"
        "ds_read_b64_tr_b16 v[174:175], v233 offset:24576\n"
        "v_cndmask_b32 v187, 0, v242, vcc\n"
        "v_mfma_f32_16x16x32_bf16 v[28:31], v[44:47], v[208:211], v[28:31]\n"
        "ds_read_b64_tr_b16 v[170:171], v233 offset:16384\n"
        "v_accvgpr_write_b32 a76, v187\n"
        "v_mfma_f32_16x16x32_bf16 v[28:31], v[40:43], v[192:195], v[28:31]\n"
        "ds_read_b128 v[192:195], v246 offset:34816\n"
        "v_accvgpr_write_b32 a77, v187\n"
        "v_mfma_f32_16x16x32_bf16 v[92:95], v[40:43], v[196:199], v[92:95]\n"
        "ds_read_b128 v[208:211], v247 offset:34816\n"
        "v_accvgpr_write_b32 a78, v187\n"
        "v_mfma_f32_16x16x32_bf16 v[92:95], v[44:47], v[212:215], v[92:95]\n"
        "v_accvgpr_write_b32 a79, v187\n"
        "v_mov_b32 v242, 0xff800000\n"
        "v_mfma_f32_16x16x32_bf16 v[88:91], v[36:39], v[212:215], v[88:91]\n"
        "ds_read_b128 v[212:215], v247 offset:32768\n"
        "v_mfma_f32_16x16x32_bf16 v[88:91], v[32:35], v[196:199], v[88:91]\n"
        "ds_read_b128 v[196:199], v246 offset:32768\n"
        "v_mfma_f32_16x16x32_bf16 v[120:123], v[32:35], v[200:203], v[120:123]\n"
        "v_mfma_f32_16x16x32_bf16 v[120:123], v[36:39], v[216:219], v[120:123]\n"
        "v_mfma_f32_16x16x32_bf16 v[124:127], v[44:47], v[216:219], v[124:127]\n"
        "ds_read_b128 v[216:219], v247 offset:38912\n"
        "v_mfma_f32_16x16x32_bf16 v[124:127], v[40:43], v[200:203], v[124:127]\n"
        "ds_read_b128 v[200:203], v246 offset:38912\n"
        "v_mfma_f32_16x16x32_bf16 v[156:159], v[40:43], v[204:207], v[156:159]\n"
        "ds_read_b64_tr_b16 v[40:41], v228 offset:17408\n"
        "s_waitcnt lgkmcnt(5)\n"
        "v_mfma_f32_16x16x32_bf16 v[156:159], v[44:47], v[220:223], v[156:159]\n"
        "ds_read_b64_tr_b16 v[44:45], v228 offset:25600\n"
        "v_mfma_f32_16x16x32_bf16 v[152:155], v[36:39], v[220:223], v[152:155]\n"
        "ds_read_b128 v[220:223], v247 offset:36864\n"
        "v_mfma_f32_16x16x32_bf16 v[152:155], v[32:35], v[204:207], v[152:155]\n"
        "ds_read_b128 v[204:207], v246 offset:36864\n"
        "v_mfma_f32_16x16x32_bf16 v[0:3], v[48:51], v[192:195], v[0:3]\n"
        "global_store_dwordx4 v254, a[12:15], s[70:71] offset:1024 sc1\n"
        "ds_read_b64_tr_b16 v[32:33], v226 offset:17408\n"
        "ds_read_b64_tr_b16 v[34:35], v227 offset:17408\n"
        "s_add_u32 s70, s38, s96\n"
        "s_addc_u32 s71, s39, 0\n"
        "v_mfma_f32_16x16x32_bf16 v[0:3], v[52:55], v[208:211], v[0:3]\n"
        "ds_read_b64_tr_b16 v[36:37], v226 offset:25600\n"
        "s_mul_i32 s96, s40, 4224\n"
        "s_add_u32 s96, s96, 0x20240\n"
        "v_mfma_f32_16x16x32_bf16 v[4:7], v[60:63], v[208:211], v[4:7]\n"
        "ds_read_b64_tr_b16 v[38:39], v227 offset:25600\n"
        "v_mov_b32 v178, s96\n"
        "v_mfma_f32_16x16x32_bf16 v[4:7], v[56:59], v[192:195], v[4:7]\n"
        "ds_read_b64_tr_b16 v[46:47], v229 offset:25600\n"
        "s_waitcnt lgkmcnt(8)\n"
        "v_mfma_f32_16x16x32_bf16 v[68:71], v[56:59], v[196:199], v[68:71]\n"
        "ds_read_b64_tr_b16 v[42:43], v229 offset:17408\n"
        "v_writelane_b32 v180, s96, 2\n"
        "v_mfma_f32_16x16x32_bf16 v[68:71], v[60:63], v[212:215], v[68:71]\n"
        "s_add_u32 s96, s96, 16\n"
        "v_writelane_b32 v180, s96, 3\n"
        "s_add_u32 s96, s51, 4098\n"
        "v_mfma_f32_16x16x32_bf16 v[64:67], v[52:55], v[212:215], v[64:67]\n"
        "v_sub_u32 v179, s96, v178\n"
        "v_mul_lo_u32 v179, v179, v181\n"
        "v_mfma_f32_16x16x32_bf16 v[64:67], v[48:51], v[196:199], v[64:67]\n"
        "s_add_u32 s96, s51, 480\n"
        "v_add_u32 v178, v178, v179\n"
        "v_lshl_add_u32 v178, v176, 1, v178\n"
        "v_mfma_f32_16x16x32_bf16 v[96:99], v[48:51], v[200:203], v[96:99]\n"
        "s_waitcnt lgkmcnt(4)\n"
        "v_lshl_add_u32 v178, v177, 4, v178\n"
        "v_accvgpr_write_b32 a88, v178\n"
        "v_mfma_f32_16x16x32_bf16 v[96:99], v[52:55], v[216:219], v[96:99]\n"
        "v_accvgpr_write_b32 a90, v180\n"
        "v_lshlrev_b32 v179, 5, v176\n"
        "v_mfma_f32_16x16x32_bf16 v[100:103], v[60:63], v[216:219], v[100:103]\n"
        "v_lshrrev_b32 v178, 1, v177\n"
        "v_bfe_u32 v180, v254, 6, 3\n"
        "v_mfma_f32_16x16x32_bf16 v[100:103], v[56:59], v[200:203], v[100:103]\n"
        "global_store_dwordx4 v254, a[208:211], s[70:71] offset:0 sc1\n"
        "v_sub_u32 v179, s96, v179\n"
        "v_lshlrev_b32 v183, 2, v178\n"
        "v_mov_b32 v181, 0\n"
        "s_lshl_b32 s96, s54, 16\n"
        "v_mfma_f32_16x16x32_bf16 v[132:135], v[56:59], v[204:207], v[132:135]\n"
        "ds_read_b64_tr_b16 v[56:57], v232 offset:17408\n"
        "s_and_b32 s54, s54, 0xffff0000\n"
        "v_xor_b32 v183, v183, v180\n"
        "v_mfma_f32_16x16x32_bf16 v[132:135], v[60:63], v[220:223], v[132:135]\n"
        "ds_read_b64_tr_b16 v[60:61], v232 offset:25600\n"
        "v_lshl_add_u32 v179, v177, 4, v179\n"
        "v_mfma_f32_16x16x32_bf16 v[128:131], v[52:55], v[220:223], v[128:131]\n"
        "ds_read_b64_tr_b16 v[52:53], v230 offset:25600\n"
        "v_writelane_b32 v181, s96, 3\n"
        "v_mfma_f32_16x16x32_bf16 v[128:131], v[48:51], v[204:207], v[128:131]\n"
        "ds_read_b64_tr_b16 v[48:49], v230 offset:17408\n"
        "s_lshr_b32 s96, s40, 1\n"
        "v_bfe_u32 v184, v183, 1, 2\n"
        "v_mfma_f32_16x16x32_bf16 v[8:11], v[160:163], v[192:195], v[8:11]\n"
        "ds_read_b64_tr_b16 v[50:51], v231 offset:17408\n"
        "s_lshl_b32 s96, s96, 10\n"
        "v_accvgpr_write_b32 a89, v179\n"
        "v_mfma_f32_16x16x32_bf16 v[8:11], v[164:167], v[208:211], v[8:11]\n"
        "ds_read_b64_tr_b16 v[54:55], v231 offset:25600\n"
        "v_xor_b32 v184, 0, v184\n"
        "v_mfma_f32_16x16x32_bf16 v[12:15], v[172:175], v[208:211], v[12:15]\n"
        "ds_read_b64_tr_b16 v[62:63], v233 offset:25600\n"
        "v_accvgpr_write_b32 a91, v181\n"
        "v_mfma_f32_16x16x32_bf16 v[12:15], v[168:171], v[192:195], v[12:15]\n"
        "ds_read_b64_tr_b16 v[58:59], v233 offset:17408\n"
        "v_lshlrev_b32 v179, 11, v178\n"
        "v_mfma_f32_16x16x32_bf16 v[76:79], v[168:171], v[196:199], v[76:79]\n"
        "v_lshlrev_b32 v184, 5, v184\n"
        "v_bfe_u32 v181, v254, 4, 2\n"
        "v_mfma_f32_16x16x32_bf16 v[76:79], v[172:175], v[212:215], v[76:79]\n"
        "v_add_u32 v179, s96, v179\n"
        "v_lshl_add_u32 v183, v183, 7, v184\n"
        "v_mfma_f32_16x16x32_bf16 v[72:75], v[164:167], v[212:215], v[72:75]\n"
        "s_and_b32 s96, s40, 1\n"
        "v_lshl_add_u32 v183, v181, 3, v183\n"
        "s_lshl_b32 s96, s96, 6\n"
        "v_mfma_f32_16x16x32_bf16 v[72:75], v[160:163], v[196:199], v[72:75]\n"
        "v_add_u32 v182, v183, v179\n"
        "v_xor_b32 v182, s96, v182\n"
        "v_mfma_f32_16x16x32_bf16 v[104:107], v[160:163], v[200:203], v[104:107]\n"
        "s_lshl_b32 s96, s41, 12\n"
        "s_add_u32 s96, s96, 32768\n"
        "v_lshl_add_u32 v188, v188, 7, s96\n"
        "v_mfma_f32_16x16x32_bf16 v[104:107], v[164:167], v[216:219], v[104:107]\n"
        "s_add_u32 s96, s53, 0x0\n"
        "v_accvgpr_write_b32 a92, v182\n"
        "v_accvgpr_write_b32 a94, v188\n"
        "v_mfma_f32_16x16x32_bf16 v[108:111], v[172:175], v[216:219], v[108:111]\n"
        "v_mfma_f32_16x16x32_bf16 v[108:111], v[168:171], v[200:203], v[108:111]\n"
        "global_store_dwordx4 v254, a[240:243], s[70:71] offset:1024 sc1\n"
        "s_waitcnt vmcnt(5) lgkmcnt(0)\n"
        "s_barrier\n"
        "v_mfma_f32_16x16x32_bf16 v[140:143], v[168:171], v[204:207], v[140:143]\n"
        "ds_write_b128 v244, a[200:203] offset:0\n"
        "v_mfma_f32_16x16x32_bf16 v[140:143], v[172:175], v[220:223], v[140:143]\n"
        "ds_read_b64_tr_b16 v[172:173], v228 offset:40960\n"
        "v_mfma_f32_16x16x32_bf16 v[136:139], v[164:167], v[220:223], v[136:139]\n"
        "ds_read_b64_tr_b16 v[164:165], v226 offset:40960\n"
        "v_mfma_f32_16x16x32_bf16 v[136:139], v[160:163], v[204:207], v[136:139]\n"
        "ds_write_b128 v244, a[204:207] offset:1024\n"
        "v_mfma_f32_16x16x32_bf16 v[16:19], v[32:35], v[192:195], v[16:19]\n"
        "ds_read_b64_tr_b16 v[160:161], v226 offset:32768\n"
        "global_load_dword v253, v252, s[46:47] offset:0\n"
        "v_mfma_f32_16x16x32_bf16 v[16:19], v[36:39], v[208:211], v[16:19]\n"
        "ds_read_b64_tr_b16 v[162:163], v227 offset:32768\n"
        "v_mfma_f32_16x16x32_bf16 v[20:23], v[44:47], v[208:211], v[20:23]\n"
        "ds_write_b128 v244, a[160:163] offset:4096\n"
        "v_mfma_f32_16x16x32_bf16 v[20:23], v[40:43], v[192:195], v[20:23]\n"
        "ds_read_b64_tr_b16 v[166:167], v227 offset:40960\n"
        "v_mfma_f32_16x16x32_bf16 v[84:87], v[40:43], v[196:199], v[84:87]\n"
        "ds_read_b64_tr_b16 v[174:175], v229 offset:40960\n"
        "v_mfma_f32_16x16x32_bf16 v[84:87], v[44:47], v[212:215], v[84:87]\n"
        "ds_write_b128 v244, a[164:167] offset:5120\n"
        "v_mfma_f32_16x16x32_bf16 v[80:83], v[36:39], v[212:215], v[80:83]\n"
        "ds_read_b64_tr_b16 v[168:169], v228 offset:32768\n"
        "v_mfma_f32_16x16x32_bf16 v[80:83], v[32:35], v[196:199], v[80:83]\n"
        "ds_read_b64_tr_b16 v[170:171], v229 offset:32768\n"
        "v_mfma_f32_16x16x32_bf16 v[112:115], v[32:35], v[200:203], v[112:115]\n"
        "ds_write_b128 v244, a[168:171] offset:8192\n"
        "v_mfma_f32_16x16x32_bf16 v[112:115], v[36:39], v[216:219], v[112:115]\n"
        "global_store_dwordx4 v254, a[212:215], s[70:71] offset:2048 sc1\n"
        "v_mfma_f32_16x16x32_bf16 v[116:119], v[44:47], v[216:219], v[116:119]\n"
        "v_mfma_f32_16x16x32_bf16 v[116:119], v[40:43], v[200:203], v[116:119]\n"
        "ds_write_b128 v244, a[172:175] offset:9216\n"
        "v_mfma_f32_16x16x32_bf16 v[148:151], v[40:43], v[204:207], v[148:151]\n"
        "ds_read_b64_tr_b16 v[40:41], v232 offset:32768\n"
        "v_mfma_f32_16x16x32_bf16 v[148:151], v[44:47], v[220:223], v[148:151]\n"
        "ds_read_b64_tr_b16 v[44:45], v232 offset:40960\n"
        "v_mfma_f32_16x16x32_bf16 v[144:147], v[36:39], v[220:223], v[144:147]\n"
        "ds_write_b128 v244, a[0:3] offset:12288\n"
        "v_mfma_f32_16x16x32_bf16 v[144:147], v[32:35], v[204:207], v[144:147]\n"
        "ds_read_b64_tr_b16 v[32:33], v230 offset:32768\n"
        "v_mfma_f32_16x16x32_bf16 v[24:27], v[48:51], v[192:195], v[24:27]\n"
        "ds_read_b64_tr_b16 v[34:35], v231 offset:32768\n"
        "v_mfma_f32_16x16x32_bf16 v[24:27], v[52:55], v[208:211], v[24:27]\n"
        "ds_write_b128 v244, a[4:7] offset:13312\n"
        "v_mfma_f32_16x16x32_bf16 v[28:31], v[60:63], v[208:211], v[28:31]\n"
        "ds_read_b64_tr_b16 v[36:37], v230 offset:40960\n"
        "v_xor_b32 v244, 0xc000, v244\n"
        "v_mfma_f32_16x16x32_bf16 v[28:31], v[56:59], v[192:195], v[28:31]\n"
        "ds_read_b64_tr_b16 v[38:39], v231 offset:40960\n"
        "v_mfma_f32_16x16x32_bf16 v[92:95], v[56:59], v[196:199], v[92:95]\n"
        "ds_read_b64_tr_b16 v[46:47], v233 offset:40960\n"
        "v_mfma_f32_16x16x32_bf16 v[92:95], v[60:63], v[212:215], v[92:95]\n"
        "ds_read_b64_tr_b16 v[42:43], v233 offset:32768\n"
        "v_mfma_f32_16x16x32_bf16 v[88:91], v[52:55], v[212:215], v[88:91]\n"
        "ds_read_b128 v[192:195], v246 offset:2048\n"
        "v_mfma_f32_16x16x32_bf16 v[88:91], v[48:51], v[196:199], v[88:91]\n"
        "ds_read_b128 v[208:211], v247 offset:2048\n"
        "v_mfma_f32_16x16x32_bf16 v[120:123], v[48:51], v[200:203], v[120:123]\n"
        "ds_read_b128 v[196:199], v246 offset:0\n"
        "v_mfma_f32_16x16x32_bf16 v[120:123], v[52:55], v[216:219], v[120:123]\n"
        "ds_read_b128 v[212:215], v247 offset:0\n"
        "v_mfma_f32_16x16x32_bf16 v[124:127], v[60:63], v[216:219], v[124:127]\n"
        "global_store_dwordx4 v254, a[244:247], s[70:71] offset:3072 sc1\n"
        "ds_read_b128 v[216:219], v247 offset:6144\n"
        "s_add_u32 s70, s38, s96\n"
        "s_addc_u32 s71, s39, 0\n"
        "s_add_u32 s96, s53, 0x0\n"
        "s_add_u32 s96, s96, s75\n"
        "v_mfma_f32_16x16x32_bf16 v[124:127], v[56:59], v[200:203], v[124:127]\n"
        "ds_read_b128 v[200:203], v246 offset:6144\n"
        "v_mfma_f32_16x16x32_bf16 v[156:159], v[56:59], v[204:207], v[156:159]\n"
        "ds_read_b64_tr_b16 v[56:57], v228 offset:33792\n"
        "v_mfma_f32_16x16x32_bf16 v[156:159], v[60:63], v[220:223], v[156:159]\n"
        "ds_read_b64_tr_b16 v[60:61], v228 offset:41984\n"
        "s_waitcnt lgkmcnt(7)\n"
        "v_mfma_f32_16x16x32_bf16 v[152:155], v[52:55], v[220:223], v[152:155]\n"
        "ds_read_b128 v[220:223], v247 offset:4096\n"
        "s_waitcnt lgkmcnt(7)\n"
        "v_mfma_f32_16x16x32_bf16 v[152:155], v[48:51], v[204:207], v[152:155]\n"
        "LHA9_%=:\n"
        "s_cbranch_execz LHRA_%=\n"
        "ds_read_b128 v[204:207], v246 offset:4096\n"
        "v_mfma_f32_16x16x32_bf16 v[0:3], v[160:163], v[192:195], v[0:3]\n"
        "ds_read_b64_tr_b16 v[48:49], v226 offset:33792\n"
        "s_waitcnt lgkmcnt(7)\n"
        "v_mfma_f32_16x16x32_bf16 v[0:3], v[164:167], v[208:211], v[0:3]\n"
        "ds_read_b64_tr_b16 v[50:51], v227 offset:33792\n"
        "v_mfma_f32_16x16x32_bf16 v[4:7], v[172:175], v[208:211], v[4:7]\n"
        "ds_read_b64_tr_b16 v[52:53], v226 offset:41984\n"
        "v_mfma_f32_16x16x32_bf16 v[4:7], v[168:171], v[192:195], v[4:7]\n"
        "ds_read_b64_tr_b16 v[54:55], v227 offset:41984\n"
        "v_mfma_f32_16x16x32_bf16 v[68:71], v[168:171], v[196:199], v[68:71]\n"
        "ds_read_b64_tr_b16 v[62:63], v229 offset:41984\n"
        "s_waitcnt lgkmcnt(8)\n"
        "v_mfma_f32_16x16x32_bf16 v[68:71], v[172:175], v[212:215], v[68:71]\n"
        "ds_read_b64_tr_b16 v[58:59], v229 offset:33792\n"
        "v_cvt_pk_bf16_f32 v0, v0, v1\n"
        "v_mfma_f32_16x16x32_bf16 v[64:67], v[164:167], v[212:215], v[64:67]\n"
        "ds_write_b128 v244, a[8:11] offset:0\n"
        "v_mfma_f32_16x16x32_bf16 v[64:67], v[160:163], v[196:199], v[64:67]\n"
        "global_store_dwordx4 v254, a[216:219], s[70:71] offset:0 sc1\n"
        "v_cvt_pk_bf16_f32 v1, v2, v3\n"
        "v_cvt_pk_bf16_f32 v2, v4, v5\n"
        "v_cvt_pk_bf16_f32 v3, v6, v7\n"
        "v_mfma_f32_16x16x32_bf16 v[96:99], v[160:163], v[200:203], v[96:99]\n"
        "s_waitcnt lgkmcnt(5)\n"
        "v_mfma_f32_16x16x32_bf16 v[96:99], v[164:167], v[216:219], v[96:99]\n"
        "ds_write_b128 v244, a[12:15] offset:1024\n"
        "v_mfma_f32_16x16x32_bf16 v[100:103], v[172:175], v[216:219], v[100:103]\n"
        "v_cvt_pk_bf16_f32 v64, v64, v65\n"
        "v_cvt_pk_bf16_f32 v65, v66, v67\n"
        "v_mfma_f32_16x16x32_bf16 v[100:103], v[168:171], v[200:203], v[100:103]\n"
        "v_cvt_pk_bf16_f32 v66, v68, v69\n"
        "v_cvt_pk_bf16_f32 v67, v70, v71\n"
        "v_mfma_f32_16x16x32_bf16 v[132:135], v[168:171], v[204:207], v[132:135]\n"
        "ds_write_b128 v244, a[192:195] offset:4096\n"
        "v_mfma_f32_16x16x32_bf16 v[132:135], v[172:175], v[220:223], v[132:135]\n"
        "ds_read_b64_tr_b16 v[172:173], v232 offset:41984\n"
        "v_cvt_pk_bf16_f32 v96, v96, v97\n"
        "v_mfma_f32_16x16x32_bf16 v[128:131], v[164:167], v[220:223], v[128:131]\n"
        "ds_read_b64_tr_b16 v[164:165], v230 offset:41984\n"
        "v_cvt_pk_bf16_f32 v97, v98, v99\n"
        "v_mfma_f32_16x16x32_bf16 v[128:131], v[160:163], v[204:207], v[128:131]\n"
        "ds_write_b128 v244, a[196:199] offset:5120\n"
        "v_mfma_f32_16x16x32_bf16 v[8:11], v[32:35], v[192:195], v[8:11]\n"
        "ds_read_b64_tr_b16 v[160:161], v230 offset:33792\n"
        "v_cvt_pk_bf16_f32 v98, v100, v101\n"
        "v_mfma_f32_16x16x32_bf16 v[8:11], v[36:39], v[208:211], v[8:11]\n"
        "ds_read_b64_tr_b16 v[162:163], v231 offset:33792\n"
        "v_cvt_pk_bf16_f32 v99, v102, v103\n"
        "v_mfma_f32_16x16x32_bf16 v[12:15], v[44:47], v[208:211], v[12:15]\n"
        "ds_read_b64_tr_b16 v[166:167], v231 offset:41984\n"
        "v_cvt_pk_bf16_f32 v128, v128, v129\n"
        "v_mfma_f32_16x16x32_bf16 v[12:15], v[40:43], v[192:195], v[12:15]\n"
        "ds_read_b64_tr_b16 v[174:175], v233 offset:41984\n"
        "v_cvt_pk_bf16_f32 v129, v130, v131\n"
        "v_mfma_f32_16x16x32_bf16 v[76:79], v[40:43], v[196:199], v[76:79]\n"
        "ds_read_b64_tr_b16 v[168:169], v232 offset:33792\n"
        "v_cvt_pk_bf16_f32 v4, v8, v9\n"
        "v_mfma_f32_16x16x32_bf16 v[76:79], v[44:47], v[212:215], v[76:79]\n"
        "ds_read_b64_tr_b16 v[170:171], v233 offset:33792\n"
        "v_cvt_pk_bf16_f32 v5, v10, v11\n"
        "v_mfma_f32_16x16x32_bf16 v[72:75], v[36:39], v[212:215], v[72:75]\n"
        "ds_write_b128 v244, a[232:235] offset:8192\n"
        "v_mfma_f32_16x16x32_bf16 v[72:75], v[32:35], v[196:199], v[72:75]\n"
        "global_store_dwordx4 v254, a[248:251], s[70:71] offset:1024 sc1\n"
        "s_add_u32 s82, s82, 1\n"
        "v_cvt_pk_bf16_f32 v6, v12, v13\n"
        "v_cvt_pk_bf16_f32 v7, v14, v15\n"
        "v_cvt_pk_bf16_f32 v70, v76, v77\n"
        "v_cvt_pk_bf16_f32 v71, v78, v79\n"
        "v_mfma_f32_16x16x32_bf16 v[104:107], v[32:35], v[200:203], v[104:107]\n"
        "s_cmp_ge_u32 s82, s44\n"
        "s_addc_u32 s90, s82, 0\n"
        "v_cvt_pk_bf16_f32 v68, v72, v73\n"
        "v_mfma_f32_16x16x32_bf16 v[104:107], v[36:39], v[216:219], v[104:107]\n"
        "ds_write_b128 v244, a[236:239] offset:9216\n"
        "v_mfma_f32_16x16x32_bf16 v[108:111], v[44:47], v[216:219], v[108:111]\n"
        "s_lshl_b32 s90, s90, 15\n"
        "v_cvt_pk_bf16_f32 v69, v74, v75\n"
        "v_cvt_pk_bf16_f32 v131, v134, v135\n"
        "v_mfma_f32_16x16x32_bf16 v[108:111], v[40:43], v[200:203], v[108:111]\n"
        "s_waitcnt lgkmcnt(2)\n"
        "v_cvt_pk_bf16_f32 v130, v132, v133\n"
        "v_cvt_pk_bf16_f32 v100, v104, v105\n"
        "v_mfma_f32_16x16x32_bf16 v[140:143], v[40:43], v[204:207], v[140:143]\n"
        "ds_write_b128 v244, a[224:227] offset:12288\n"
        "v_mfma_f32_16x16x32_bf16 v[140:143], v[44:47], v[220:223], v[140:143]\n"
        "s_barrier\n"
        "v_mfma_f32_16x16x32_bf16 v[136:139], v[36:39], v[220:223], v[136:139]\n"
        "s_add_u32 s68, s64, s90\n"
        "s_addc_u32 s69, s65, 0\n"
        "v_cndmask_b32 v36, 0, v242, s[10:11]\n"
        "v_mfma_f32_16x16x32_bf16 v[136:139], v[32:35], v[204:207], v[136:139]\n"
        "ds_write_b128 v244, a[228:231] offset:13312\n"
        "v_mfma_f32_16x16x32_bf16 v[16:19], v[48:51], v[192:195], v[16:19]\n"
        "global_store_dwordx4 v254, a[220:223], s[70:71] offset:2048 sc1\n"
        "s_xor_b32 s78, s78, 0x8000\n"
        "v_cndmask_b32 v37, 0, v242, s[12:13]\n"
        "v_cndmask_b32 v38, 0, v242, s[14:15]\n"
        "v_cndmask_b32 v39, 0, v242, s[16:17]\n"
        "v_cvt_pk_bf16_f32 v134, v140, v141\n"
        "v_mfma_f32_16x16x32_bf16 v[16:19], v[52:55], v[208:211], v[16:19]\n"
        "s_lshr_b32 s52, s41, 1\n"
        "v_cvt_pk_bf16_f32 v101, v106, v107\n"
        "v_cvt_pk_bf16_f32 v102, v108, v109\n"
        "v_mfma_f32_16x16x32_bf16 v[20:23], v[60:63], v[208:211], v[20:23]\n"
        "ds_write_b128 v245, a[176:179] offset:0\n"
        "v_mfma_f32_16x16x32_bf16 v[20:23], v[56:59], v[192:195], v[20:23]\n"
        "v_cvt_pk_bf16_f32 v103, v110, v111\n"
        "s_nop 0\n"
        "v_cvt_pk_bf16_f32 v8, v16, v17\n"
        "v_mfma_f32_16x16x32_bf16 v[84:87], v[56:59], v[196:199], v[84:87]\n"
        "s_barrier\n"
        "v_mfma_f32_16x16x32_bf16 v[84:87], v[60:63], v[212:215], v[84:87]\n"
        "ds_read_b128 a[96:99], v240 offset:0\n"
        "s_lshl_b32 s52, s52, 11\n"
        "v_cvt_pk_bf16_f32 v9, v18, v19\n"
        "v_mfma_f32_16x16x32_bf16 v[80:83], v[52:55], v[212:215], v[80:83]\n"
        "ds_read_b128 a[100:103], v240 offset:1024\n"
        "v_cvt_pk_bf16_f32 v10, v20, v21\n"
        "v_mfma_f32_16x16x32_bf16 v[80:83], v[48:51], v[196:199], v[80:83]\n"
        "ds_read_b128 a[104:107], v240 offset:2048\n"
        "v_cvt_pk_bf16_f32 v11, v22, v23\n"
        "v_mfma_f32_16x16x32_bf16 v[112:115], v[48:51], v[200:203], v[112:115]\n"
        "ds_read_b128 a[108:111], v240 offset:3072\n"
        "v_cvt_pk_bf16_f32 v74, v84, v85\n"
        "v_mfma_f32_16x16x32_bf16 v[112:115], v[52:55], v[216:219], v[112:115]\n"
        "ds_write_b128 v245, a[180:183] offset:1024\n"
        "v_mfma_f32_16x16x32_bf16 v[116:119], v[60:63], v[216:219], v[116:119]\n"
        "ds_read_b128 a[112:115], v240 offset:4096\n"
        "v_cvt_pk_bf16_f32 v72, v80, v81\n"
        "v_mfma_f32_16x16x32_bf16 v[116:119], v[56:59], v[200:203], v[116:119]\n"
        "ds_read_b128 a[116:119], v240 offset:5120\n"
        "v_cvt_pk_bf16_f32 v73, v82, v83\n"
        "v_mfma_f32_16x16x32_bf16 v[148:151], v[56:59], v[204:207], v[148:151]\n"
        "ds_read_b128 a[120:123], v240 offset:6144\n"
        "v_cvt_pk_bf16_f32 v75, v86, v87\n"
        "v_mfma_f32_16x16x32_bf16 v[148:151], v[60:63], v[220:223], v[148:151]\n"
        "ds_write_b128 v245, a[184:187] offset:2048\n"
        "v_mfma_f32_16x16x32_bf16 v[144:147], v[52:55], v[220:223], v[144:147]\n"
        "ds_read_b128 a[124:127], v240 offset:7168\n"
        "v_cndmask_b32 v52, 0, v242, s[20:21]\n"
        "v_mfma_f32_16x16x32_bf16 v[144:147], v[48:51], v[204:207], v[144:147]\n"
        "global_store_dwordx4 v254, a[252:255], s[70:71] offset:3072 sc1\n"
        "s_add_u32 s70, s38, s96\n"
        "s_addc_u32 s71, s39, 0\n"
        "s_add_u32 s96, s53, 0x2000\n"
        "v_cndmask_b32 v53, 0, v242, s[22:23]\n"
        "v_cndmask_b32 v54, 0, v242, s[24:25]\n"
        "v_mfma_f32_16x16x32_bf16 v[24:27], v[160:163], v[192:195], v[24:27]\n"
        "ds_write_b128 v245, a[188:191] offset:3072\n"
        "v_mfma_f32_16x16x32_bf16 v[24:27], v[164:167], v[208:211], v[24:27]\n"
        "s_add_u32 s96, s96, s75\n"
        "v_cndmask_b32 v55, 0, v242, s[28:29]\n"
        "v_cvt_pk_bf16_f32 v104, v112, v113\n"
        "v_mfma_f32_16x16x32_bf16 v[28:31], v[172:175], v[208:211], v[28:31]\n"
        "global_store_dwordx4 v254, a[180:183], s[70:71] offset:1024 sc1\n"
        "s_add_u32 s74, s68, 0x1000\n"
        "s_addc_u32 s75, s69, 0\n"
        "s_lshl2_add_u32 s53, s42, s40\n"
        "v_cvt_pk_bf16_f32 v105, v114, v115\n"
        "v_cvt_pk_bf16_f32 v12, v24, v25\n"
        "v_mfma_f32_16x16x32_bf16 v[28:31], v[168:171], v[192:195], v[28:31]\n"
        "ds_write_b128 v245, a[16:19] offset:8192\n"
        "v_mfma_f32_16x16x32_bf16 v[92:95], v[168:171], v[196:199], v[92:95]\n"
        "s_add_u32 s52, s52, s53\n"
        "v_cvt_pk_bf16_f32 v13, v26, v27\n"
        "v_cvt_pk_bf16_f32 v106, v116, v117\n"
        "v_mfma_f32_16x16x32_bf16 v[92:95], v[172:175], v[212:215], v[92:95]\n"
        "s_waitcnt lgkmcnt(2)\n"
        "v_cvt_pk_bf16_f32 v107, v118, v119\n"
        "v_cvt_pk_bf16_f32 v14, v28, v29\n"
        "v_mfma_f32_16x16x32_bf16 v[88:91], v[164:167], v[212:215], v[88:91]\n"
        "ds_write_b128 v245, a[20:23] offset:9216\n"
        "v_mfma_f32_16x16x32_bf16 v[88:91], v[160:163], v[196:199], v[88:91]\n"
        "s_add_u32 s52, s52, 0\n"
        "v_cvt_pk_bf16_f32 v15, v30, v31\n"
        "v_cvt_pk_bf16_f32 v78, v92, v93\n"
        "v_mfma_f32_16x16x32_bf16 v[120:123], v[160:163], v[200:203], v[120:123]\n"
        "s_lshl_b32 s52, s52, 10\n"
        "v_cvt_pk_bf16_f32 v79, v94, v95\n"
        "v_cvt_pk_bf16_f32 v132, v136, v137\n"
        "v_mfma_f32_16x16x32_bf16 v[32:35], a[96:99], v[0:3], 0\n"
        "ds_write_b128 v245, a[24:27] offset:10240\n"
        "v_mfma_f32_16x16x32_bf16 v[32:35], a[100:103], v[4:7], v[32:35]\n"
        "s_add_u32 s26, s18, s52\n"
        "s_addc_u32 s27, s19, 0\n"
        "v_cvt_pk_bf16_f32 v76, v88, v89\n"
        "v_mfma_f32_16x16x32_bf16 v[32:35], a[104:107], v[8:11], v[32:35]\n"
        "s_add_u32 s76, s66, s90\n"
        "s_addc_u32 s77, s67, 0\n"
        "v_cvt_pk_bf16_f32 v77, v90, v91\n"
        "v_mfma_f32_16x16x32_bf16 v[32:35], a[108:111], v[12:15], v[32:35]\n"
        "ds_write_b128 v245, a[28:31] offset:11264\n"
        "v_mfma_f32_16x16x32_bf16 v[120:123], v[164:167], v[216:219], v[120:123]\n"
        "v_xor_b32 v245, 0xc000, v245\n"
        "v_cvt_pk_bf16_f32 v133, v138, v139\n"
        "v_mfma_f32_16x16x32_bf16 v[124:127], v[172:175], v[216:219], v[124:127]\n"
        "v_cvt_pk_bf16_f32 v135, v142, v143\n"
        "v_cvt_pk_bf16_f32 v136, v144, v145\n"
        "v_mfma_f32_16x16x32_bf16 v[124:127], v[168:171], v[200:203], v[124:127]\n"
        "v_max_f32 v246, v32, v33\n"
        "v_max3_f32 v246, v34, v35, v246\n"
        "v_mfma_f32_16x16x32_bf16 v[156:159], v[168:171], v[204:207], v[156:159]\n"
        "v_mov_b32 v247, v246\n"
        "v_cvt_pk_bf16_f32 v108, v120, v121\n"
        "v_mfma_f32_16x16x32_bf16 v[36:39], a[108:111], v[76:79], v[36:39]\n"
        "v_permlane16_swap_b32 v246, v247\n"
        "v_mfma_f32_16x16x32_bf16 v[36:39], a[104:107], v[72:75], v[36:39]\n"
        "ds_read_b128 a[104:107], v240 offset:10240\n"
        "v_max_f32 v246, v246, v247\n"
        "v_mfma_f32_16x16x32_bf16 v[36:39], a[100:103], v[68:71], v[36:39]\n"
        "ds_read_b128 a[100:103], v240 offset:9216\n"
        "v_mov_b32 v247, v246\n"
        "v_mfma_f32_16x16x32_bf16 v[36:39], a[96:99], v[64:67], v[36:39]\n"
        "global_store_dwordx4 v254, a[176:179], s[70:71] offset:0 sc1\n"
        "ds_read_b128 a[96:99], v240 offset:8192\n"
        "s_waitcnt lgkmcnt(3)\n"
        "v_permlane32_swap_b32 v246, v247\n"
        "v_mfma_f32_16x16x32_bf16 v[156:159], v[172:175], v[220:223], v[156:159]\n"
        "ds_read_b128 a[108:111], v240 offset:11264\n"
        "v_max_f32 v246, v246, v247\n"
        "v_mfma_f32_16x16x32_bf16 v[152:155], v[164:167], v[220:223], v[152:155]\n"
        "v_sub_f32 v224, 0, v246\n"
        "v_sub_f32 v225, 0, v246\n"
        "v_mfma_f32_16x16x32_bf16 v[152:155], v[160:163], v[204:207], v[152:155]\n"
        "v_sub_f32 v226, 0, v246\n"
        "v_sub_f32 v227, 0, v246\n"
        "v_mfma_f32_16x16x32_bf16 v[48:51], a[112:115], v[0:3], 0\n"
        "v_max_f32 v248, v36, v37\n"
        "v_max3_f32 v248, v38, v39, v248\n"
        "v_mfma_f32_16x16x32_bf16 v[48:51], a[116:119], v[4:7], v[48:51]\n"
        "v_mov_b32 v249, v248\n"
        "v_add_f32 v32, v32, v224\n"
        "v_mfma_f32_16x16x32_bf16 v[48:51], a[120:123], v[8:11], v[48:51]\n"
        "v_permlane16_swap_b32 v248, v249\n"
        "v_mfma_f32_16x16x32_bf16 v[48:51], a[124:127], v[12:15], v[48:51]\n"
        "v_max_f32 v248, v248, v249\n"
        "v_mov_b32 v249, v248\n"
        "v_mfma_f32_16x16x32_bf16 v[52:55], a[124:127], v[76:79], v[52:55]\n"
        "global_store_dwordx4 v254, a[184:187], s[70:71] offset:2048 sc1\n"
        "s_waitcnt lgkmcnt(1)\n"
        "v_permlane32_swap_b32 v248, v249\n"
        "v_max_f32 v248, v248, v249\n"
        "v_mfma_f32_16x16x32_bf16 v[52:55], a[120:123], v[72:75], v[52:55]\n"
        "ds_read_b128 a[120:123], v240 offset:14336\n"
        "v_sub_f32 v228, 0, v248\n"
        "v_mfma_f32_16x16x32_bf16 v[52:55], a[116:119], v[68:71], v[52:55]\n"
        "ds_read_b128 a[116:119], v240 offset:13312\n"
        "v_sub_f32 v229, 0, v248\n"
        "v_mfma_f32_16x16x32_bf16 v[52:55], a[112:115], v[64:67], v[52:55]\n"
        "v_sub_f32 v230, 0, v248\n"
        "v_sub_f32 v231, 0, v248\n"
        "s_barrier\n"
        "ds_read_b128 a[144:147], v241 offset:0\n"
        "v_exp_f32 v32, v32\n"
        "v_add_f32 v33, v33, v224\n"
        "v_exp_f32 v33, v33\n"
        "ds_read_b128 a[148:151], v241 offset:1024\n"
        "v_cvt_pk_bf16_f32 v16, v32, v33\n"
        "v_add_f32 v34, v34, v224\n"
        "v_exp_f32 v34, v34\n"
        "v_add_f32 v35, v35, v224\n"
        "v_exp_f32 v35, v35\n"
        "ds_read_b128 a[152:155], v241 offset:2048\n"
        "v_cvt_pk_bf16_f32 v17, v34, v35\n"
        "v_mfma_f32_16x16x32_bf16 v[32:35], a[96:99], v[0:3], v[224:227]\n"
        "global_store_dwordx4 v254, a[188:191], s[70:71] offset:3072 sc1\n"
        "s_waitcnt lgkmcnt(4)\n"
        "v_add_f32 v36, v36, v228\n"
        "v_exp_f32 v36, v36\n"
        "v_mfma_f32_16x16x32_bf16 v[32:35], a[100:103], v[4:7], v[32:35]\n"
        "ds_read_b128 a[112:115], v240 offset:12288\n"
        "v_add_f32 v37, v37, v228\n"
        "v_mfma_f32_16x16x32_bf16 v[32:35], a[104:107], v[8:11], v[32:35]\n"
        "v_exp_f32 v37, v37\n"
        "v_mfma_f32_16x16x32_bf16 v[32:35], a[108:111], v[12:15], v[32:35]\n"
        "v_cvt_pk_bf16_f32 v20, v36, v37\n"
        "v_add_f32 v38, v38, v228\n"
        "v_exp_f32 v38, v38\n"
        "v_add_f32 v39, v39, v228\n"
        "v_exp_f32 v39, v39\n"
        "ds_read_b128 a[156:159], v241 offset:3072\n"
        "v_cvt_pk_bf16_f32 v21, v38, v39\n"
        "ds_read_b128 a[128:131], v241 offset:4096\n"
        "v_add_f32 v48, v48, v224\n"
        "v_exp_f32 v48, v48\n"
        "v_add_f32 v49, v49, v224\n"
        "v_exp_f32 v49, v49\n"
        "ds_read_b128 a[124:127], v240 offset:15360\n"
        "v_cvt_pk_bf16_f32 v18, v48, v49\n"
        "ds_read_b128 a[132:135], v241 offset:5120\n"
        "s_add_u32 s70, s38, s96\n"
        "s_addc_u32 s71, s39, 0\n"
        "global_store_dwordx4 v254, a[16:19], s[70:71] offset:0 sc1\n"
        "v_add_f32 v50, v50, v224\n"
        "v_exp_f32 v50, v50\n"
        "v_add_f32 v51, v51, v224\n"
        "v_exp_f32 v51, v51\n"
        "ds_read_b128 a[136:139], v241 offset:6144\n"
        "v_cvt_pk_bf16_f32 v19, v50, v51\n"
        "s_waitcnt lgkmcnt(5)\n"
        "v_mfma_f32_16x16x32_bf16 v[48:51], a[112:115], v[0:3], v[224:227]\n"
        "ds_read_b128 a[140:143], v241 offset:7168\n"
        "s_mul_i32 s96, s40, 4224\n"
        "v_cndmask_b32 v36, v228, v242, s[30:31]\n"
        "v_mfma_f32_16x16x32_bf16 v[48:51], a[116:119], v[4:7], v[48:51]\n"
        "v_cndmask_b32 v37, v228, v242, s[56:57]\n"
        "v_cndmask_b32 v38, v228, v242, s[58:59]\n"
        "v_mfma_f32_16x16x32_bf16 v[48:51], a[120:123], v[8:11], v[48:51]\n"
        "v_cvt_pk_bf16_f32 v141, v154, v155\n"
        "v_cndmask_b32 v39, v228, v242, s[60:61]\n"
        "s_nop 1\n"
        "v_mfma_f32_16x16x32_bf16 v[36:39], a[108:111], v[76:79], v[36:39]\n"
        "ds_read_b128 a[108:111], v240 offset:19456\n"
        "v_bfe_u32 v154, v254, 4, 1\n"
        "v_mfma_f32_16x16x32_bf16 v[36:39], a[104:107], v[72:75], v[36:39]\n"
        "ds_read_b128 a[104:107], v240 offset:18432\n"
        "v_mul_u32_u24 v154, 0xaaaa, v154\n"
        "v_mfma_f32_16x16x32_bf16 v[36:39], a[100:103], v[68:71], v[36:39]\n"
        "s_waitcnt lgkmcnt(4)\n"
        "v_add_u32 v154, 0x4444, v154\n"
        "v_add_f32 v52, v52, v228\n"
        "v_mfma_f32_16x16x32_bf16 v[36:39], a[96:99], v[64:67], v[36:39]\n"
        "v_exp_f32 v52, v52\n"
        "v_mfma_f32_16x16x32_bf16 v[48:51], a[124:127], v[12:15], v[48:51]\n"
        "ds_read_b128 a[96:99], v240 offset:16384\n"
        "v_add_f32 v53, v53, v228\n"
        "v_mfma_f32_16x16x32_bf16 v[160:163], a[144:147], v[16:19], 0\n"
        "v_exp_f32 v53, v53\n"
        "v_mfma_f32_16x16x32_bf16 v[164:167], a[148:151], v[16:19], 0\n"
        "ds_read_b128 a[100:103], v240 offset:17408\n"
        "v_cvt_pk_bf16_f32 v22, v52, v53\n"
        "v_mfma_f32_16x16x32_bf16 v[168:171], a[152:155], v[16:19], 0\n"
        "v_add_f32 v54, v54, v228\n"
        "v_add_f32 v55, v55, v228\n"
        "v_mfma_f32_16x16x32_bf16 v[172:175], a[156:159], v[16:19], 0\n"
        "v_exp_f32 v54, v54\n"
        "v_mfma_f32_16x16x32_bf16 v[176:179], a[128:131], v[16:19], 0\n"
        "v_exp_f32 v55, v55\n"
        "v_mfma_f32_16x16x32_bf16 v[180:183], a[132:135], v[16:19], 0\n"
        "s_waitcnt lgkmcnt(2)\n"
        "v_cvt_pk_bf16_f32 v23, v54, v55\n"
        "s_nop 1\n"
        "v_smfmac_f32_16x16x64_bf16 a[64:67], a[72:75], v[16:23], v154\n"
        "global_store_dwordx4 v254, a[20:23], s[70:71] offset:1024 sc1\n"
        "v_exp_f32 v32, v32\n"
        "v_exp_f32 v33, v33\n"
        "v_mfma_f32_16x16x32_bf16 v[192:195], a[144:147], v[20:23], 0\n"
        "ds_read_b128 a[144:147], v241 offset:8192\n"
        "v_cvt_pk_bf16_f32 v80, v32, v33\n"
        "v_mfma_f32_16x16x32_bf16 v[196:199], a[148:151], v[20:23], 0\n"
        "ds_read_b128 a[148:151], v241 offset:9216\n"
        "v_cndmask_b32 v52, v228, v242, s[62:63]\n"
        "v_mfma_f32_16x16x32_bf16 v[200:203], a[152:155], v[20:23], 0\n"
        "ds_read_b128 a[152:155], v241 offset:10240\n"
        "v_cndmask_b32 v53, v228, v242, s[84:85]\n"
        "v_mfma_f32_16x16x32_bf16 v[204:207], a[156:159], v[20:23], 0\n"
        "ds_read_b128 a[156:159], v241 offset:11264\n"
        "v_cndmask_b32 v54, v228, v242, s[86:87]\n"
        "v_mfma_f32_16x16x32_bf16 v[208:211], a[128:131], v[20:23], 0\n"
        "ds_read_b128 a[128:131], v241 offset:12288\n"
        "v_cndmask_b32 v55, v228, v242, s[88:89]\n"
        "s_nop 1\n"
        "v_mfma_f32_16x16x32_bf16 v[52:55], a[124:127], v[76:79], v[52:55]\n"
        "v_exp_f32 v34, v34\n"
        "v_mfma_f32_16x16x32_bf16 v[52:55], a[120:123], v[72:75], v[52:55]\n"
        "v_exp_f32 v35, v35\n"
        "v_mfma_f32_16x16x32_bf16 v[52:55], a[116:119], v[68:71], v[52:55]\n"
        "ds_read_b128 a[116:119], v240 offset:21504\n"
        "v_cvt_pk_bf16_f32 v81, v34, v35\n"
        "v_mfma_f32_16x16x32_bf16 v[52:55], a[112:115], v[64:67], v[52:55]\n"
        "ds_read_b128 a[112:115], v240 offset:20480\n"
        "v_cndmask_b32 v32, v224, v242, s[10:11]\n"
        "v_mfma_f32_16x16x32_bf16 v[212:215], a[132:135], v[20:23], 0\n"
        "s_waitcnt lgkmcnt(5)\n"
        "v_exp_f32 v36, v36\n"
        "v_mfma_f32_16x16x32_bf16 v[184:187], a[136:139], v[16:19], 0\n"
        "v_exp_f32 v37, v37\n"
        "v_mfma_f32_16x16x32_bf16 v[216:219], a[136:139], v[20:23], 0\n"
        "ds_read_b128 a[120:123], v240 offset:22528\n"
        "v_cvt_pk_bf16_f32 v84, v36, v37\n"
        "v_mfma_f32_16x16x32_bf16 v[220:223], a[140:143], v[20:23], 0\n"
        "global_store_dwordx4 v254, a[24:27], s[70:71] offset:2048 sc1\n"
        "ds_read_b128 a[124:127], v240 offset:23552\n"
        "s_add_u32 s82, s82, 1\n"
        "v_exp_f32 v38, v38\n"
        "v_cndmask_b32 v33, v224, v242, s[12:13]\n"
        "v_mfma_f32_16x16x32_bf16 v[188:191], a[140:143], v[16:19], 0\n"
        "v_exp_f32 v39, v39\n"
        "ds_write_b128 v245, a[216:219] offset:0\n"
        "v_cvt_pk_bf16_f32 v85, v38, v39\n"
        "v_cndmask_b32 v34, v224, v242, s[14:15]\n"
        "v_cndmask_b32 v35, v224, v242, s[16:17]\n"
        "s_nop 1\n"
        "v_mfma_f32_16x16x32_bf16 v[32:35], a[96:99], v[0:3], v[32:35]\n"
        "v_exp_f32 v48, v48\n"
        "v_mfma_f32_16x16x32_bf16 v[32:35], a[100:103], v[4:7], v[32:35]\n"
        "v_exp_f32 v49, v49\n"
        "v_mfma_f32_16x16x32_bf16 v[32:35], a[104:107], v[8:11], v[32:35]\n"
        "ds_read_b128 a[96:99], v240 offset:24576\n"
        "v_cvt_pk_bf16_f32 v82, v48, v49\n"
        "v_mfma_f32_16x16x32_bf16 v[32:35], a[108:111], v[12:15], v[32:35]\n"
        "v_exp_f32 v50, v50\n"
        "v_exp_f32 v51, v51\n"
        "global_store_dwordx4 v254, a[28:31], s[70:71] offset:3072 sc1\n"
        "v_cvt_pk_bf16_f32 v83, v50, v51\n"
        "s_nop 1\n"
        "v_mfma_f32_16x16x32_bf16 v[160:163], a[144:147], v[80:83], v[160:163]\n"
        "ds_write_b128 v245, a[248:251] offset:1024\n"
        "v_mfma_f32_16x16x32_bf16 v[164:167], a[148:151], v[80:83], v[164:167]\n"
        "s_waitcnt lgkmcnt(2)\n"
        "v_exp_f32 v52, v52\n"
        "v_mfma_f32_16x16x32_bf16 v[168:171], a[152:155], v[80:83], v[168:171]\n"
        "v_exp_f32 v53, v53\n"
        "v_mfma_f32_16x16x32_bf16 v[172:175], a[156:159], v[80:83], v[172:175]\n"
        "ds_read_b128 a[132:135], v241 offset:13312\n"
        "v_cvt_pk_bf16_f32 v86, v52, v53\n"
        "v_mfma_f32_16x16x32_bf16 v[176:179], a[128:131], v[80:83], v[176:179]\n"
        "ds_read_b128 a[100:103], v240 offset:25600\n"
        "ds_read_b128 a[136:139], v241 offset:14336\n"
        "ds_read_b128 a[104:107], v240 offset:26624\n"
        "ds_read_b128 a[140:143], v241 offset:15360\n"
        "ds_write_b128 v245, a[220:223] offset:2048\n"
        "ds_read_b128 a[108:111], v240 offset:27648\n"
        "s_add_u32 s70, s92, 0x2\n"
        "s_addc_u32 s71, s93, 0\n"
        "s_cmp_ge_u32 s82, s44\n"
        "s_addc_u32 s90, s82, 0\n"
        "s_lshl_b32 s90, s90, 15\n"
        "v_exp_f32 v54, v54\n"
        "v_exp_f32 v55, v55\n"
        "s_waitcnt lgkmcnt(6)\n"
        "v_cvt_pk_bf16_f32 v87, v54, v55\n"
        "s_nop 1\n"
        "v_smfmac_f32_16x16x64_bf16 a[64:67], a[72:75], v[80:87], v154\n"
        "v_cndmask_b32 v48, v224, v242, s[20:21]\n"
        "v_mfma_f32_16x16x32_bf16 v[192:195], a[144:147], v[84:87], v[192:195]\n"
        "ds_read_b128 a[144:147], v241 offset:16384\n"
        "v_cndmask_b32 v49, v224, v242, s[22:23]\n"
        "v_mfma_f32_16x16x32_bf16 v[196:199], a[148:151], v[84:87], v[196:199]\n"
        "ds_read_b128 a[148:151], v241 offset:17408\n"
        "v_cndmask_b32 v50, v224, v242, s[24:25]\n"
        "v_mfma_f32_16x16x32_bf16 v[200:203], a[152:155], v[84:87], v[200:203]\n"
        "ds_read_b128 a[152:155], v241 offset:18432\n"
        "v_cndmask_b32 v51, v224, v242, s[28:29]\n"
        "s_nop 1\n"
        "v_mfma_f32_16x16x32_bf16 v[48:51], a[112:115], v[0:3], v[48:51]\n"
        "v_exp_f32 v32, v32\n"
        "v_mfma_f32_16x16x32_bf16 v[48:51], a[116:119], v[4:7], v[48:51]\n"
        "v_exp_f32 v33, v33\n"
        "v_mfma_f32_16x16x32_bf16 v[48:51], a[120:123], v[8:11], v[48:51]\n"
        "ds_read_b128 a[112:115], v240 offset:28672\n"
        "v_cvt_pk_bf16_f32 v16, v32, v33\n"
        "v_mfma_f32_16x16x32_bf16 v[48:51], a[124:127], v[12:15], v[48:51]\n"
        "s_waitcnt lgkmcnt(4)\n"
        "v_cvt_pk_bf16_f32 v109, v122, v123\n"
        "v_cvt_pk_bf16_f32 v110, v124, v125\n"
        "v_mfma_f32_16x16x32_bf16 v[204:207], a[156:159], v[84:87], v[204:207]\n"
        "ds_read_b128 a[116:119], v240 offset:29696\n"
        "v_cvt_pk_bf16_f32 v111, v126, v127\n"
        "v_mfma_f32_16x16x32_bf16 v[208:211], a[128:131], v[84:87], v[208:211]\n"
        "v_exp_f32 v34, v34\n"
        "v_mfma_f32_16x16x32_bf16 v[212:215], a[132:135], v[84:87], v[212:215]\n"
        "v_exp_f32 v35, v35\n"
        "v_mfma_f32_16x16x32_bf16 v[180:183], a[132:135], v[80:83], v[180:183]\n"
        "ds_read_b128 a[120:123], v240 offset:30720\n"
        "v_cvt_pk_bf16_f32 v17, v34, v35\n"
        "v_mfma_f32_16x16x32_bf16 v[184:187], a[136:139], v[80:83], v[184:187]\n"
        "ds_write_b128 v245, a[252:255] offset:3072\n"
        "v_mfma_f32_16x16x32_bf16 v[216:219], a[136:139], v[84:87], v[216:219]\n"
        "ds_read_b128 a[124:127], v240 offset:31744\n"
        "v_cndmask_b32 v32, v224, v242, s[30:31]\n"
        "v_mfma_f32_16x16x32_bf16 v[220:223], a[140:143], v[84:87], v[220:223]\n"
        "ds_read_b128 a[156:159], v241 offset:19456\n"
        "v_cndmask_b32 v33, v224, v242, s[56:57]\n"
        "v_mfma_f32_16x16x32_bf16 v[188:191], a[140:143], v[80:83], v[188:191]\n"
        "v_cndmask_b32 v34, v224, v242, s[58:59]\n"
        "v_cndmask_b32 v35, v224, v242, s[60:61]\n"
        "s_nop 1\n"
        "v_mfma_f32_16x16x32_bf16 v[32:35], a[96:99], v[0:3], v[32:35]\n"
        "v_exp_f32 v48, v48\n"
        "v_mfma_f32_16x16x32_bf16 v[32:35], a[100:103], v[4:7], v[32:35]\n"
        "v_exp_f32 v49, v49\n"
        "v_mfma_f32_16x16x32_bf16 v[32:35], a[104:107], v[8:11], v[32:35]\n"
        "s_waitcnt lgkmcnt(3)\n"
        "v_cvt_pk_bf16_f32 v18, v48, v49\n"
        "v_xor_b32 v240, 0x8000, v240\n"
        "v_mfma_f32_16x16x32_bf16 v[32:35], a[108:111], v[12:15], v[32:35]\n"
        "ds_write_b128 v245, a[208:211] offset:8192\n"
        "s_waitcnt lgkmcnt(2)\n"
        "s_barrier\n"
        "ds_read_b128 a[96:99], v240 offset:0\n"
        "ds_read_b128 a[128:131], v241 offset:20480\n"
        "v_exp_f32 v50, v50\n"
        "v_exp_f32 v51, v51\n"
        "v_cndmask_b32 v48, v224, v242, s[62:63]\n"
        "v_cvt_pk_bf16_f32 v19, v50, v51\n"
        "s_nop 1\n"
        "v_mfma_f32_16x16x32_bf16 a[64:67], a[76:79], v[16:19], a[64:67]\n"
        "ds_read_b128 a[132:135], v241 offset:21504\n"
        "v_cndmask_b32 v49, v224, v242, s[84:85]\n"
        "v_mfma_f32_16x16x32_bf16 v[160:163], a[144:147], v[16:19], v[160:163]\n"
        "ds_read_b128 a[100:103], v240 offset:1024\n"
        "v_cndmask_b32 v50, v224, v242, s[86:87]\n"
        "v_mfma_f32_16x16x32_bf16 v[164:167], a[148:151], v[16:19], v[164:167]\n"
        "ds_read_b128 a[136:139], v241 offset:22528\n"
        "v_cndmask_b32 v51, v224, v242, s[88:89]\n"
        "s_nop 1\n"
        "v_mfma_f32_16x16x32_bf16 v[48:51], a[112:115], v[0:3], v[48:51]\n"
        "ds_read_b128 a[112:115], v240 offset:4096\n"
        "v_cvt_pk_bf16_f32 v137, v146, v147\n"
        "v_mfma_f32_16x16x32_bf16 v[48:51], a[116:119], v[4:7], v[48:51]\n"
        "ds_read_b128 a[116:119], v240 offset:5120\n"
        "v_cvt_pk_bf16_f32 v138, v148, v149\n"
        "v_mfma_f32_16x16x32_bf16 v[48:51], a[120:123], v[8:11], v[48:51]\n"
        "ds_read_b128 a[120:123], v240 offset:6144\n"
        "v_cvt_pk_bf16_f32 v139, v150, v151\n"
        "v_mfma_f32_16x16x32_bf16 v[48:51], a[124:127], v[12:15], v[48:51]\n"
        "ds_read_b128 a[124:127], v240 offset:7168\n"
        "v_lshrrev_b32 v244, 1, v254\n"
        "v_mfma_f32_16x16x32_bf16 v[168:171], a[152:155], v[16:19], v[168:171]\n"
        "ds_read_b128 a[140:143], v241 offset:23552\n"
        "s_waitcnt lgkmcnt(8)\n"
        "v_mfma_f32_16x16x32_bf16 v[172:175], a[156:159], v[16:19], v[172:175]\n"
        "ds_read_b128 a[104:107], v240 offset:2048\n"
        "v_add_u32 v244, s81, v244\n"
        "v_mfma_f32_16x16x32_bf16 v[176:179], a[128:131], v[16:19], v[176:179]\n"
        "s_waitcnt lgkmcnt(7)\n"
        "v_exp_f32 v32, v32\n"
        "v_mfma_f32_16x16x32_bf16 v[180:183], a[132:135], v[16:19], v[180:183]\n"
        "s_waitcnt lgkmcnt(6)\n"
        "v_exp_f32 v33, v33\n"
        "v_mfma_f32_16x16x32_bf16 v[184:187], a[136:139], v[16:19], v[184:187]\n"
        "ds_write_b128 v245, a[240:243] offset:9216\n"
        "ds_read_b128 a[108:111], v240 offset:3072\n"
        "ds_read_b128 a[144:147], v241 offset:24576\n"
        "s_add_u32 s81, s78, 4096\n"
        "v_cvt_pk_bf16_f32 v80, v32, v33\n"
        "s_waitcnt lgkmcnt(5)\n"
        "v_mfma_f32_16x16x32_bf16 v[52:55], a[124:127], v[76:79], v[228:231]\n"
        "v_exp_f32 v34, v34\n"
        "v_mfma_f32_16x16x32_bf16 v[52:55], a[120:123], v[72:75], v[52:55]\n"
        "ds_write_b128 v245, a[212:215] offset:10240\n"
        "v_mfma_f32_16x16x32_bf16 v[52:55], a[116:119], v[68:71], v[52:55]\n"
        "v_exp_f32 v35, v35\n"
        "v_mfma_f32_16x16x32_bf16 v[52:55], a[112:115], v[64:67], v[52:55]\n"
        "s_waitcnt vmcnt(0)\n"
        "v_cvt_pk_bf16_f32 v81, v34, v35\n"
        "s_waitcnt lgkmcnt(4)\n"
        "v_mfma_f32_16x16x32_bf16 v[188:191], a[140:143], v[16:19], v[188:191]\n"
        "s_barrier\n"
        "v_mfma_f32_16x16x32_bf16 v[32:35], a[96:99], v[0:3], v[224:227]\n"
        "s_cmp_lg_u32 s40, 0\n"
        "s_cbranch_scc1 LW1_%=\n"
        "s_mov_b64 exec, 0xffff\n"
        "global_store_dwordx2 v244, v[250:251], s[48:49] sc1\n"
        "s_mov_b64 exec, -1\n"
        "LW1_%=:\n"
        "v_mfma_f32_16x16x32_bf16 v[32:35], a[100:103], v[4:7], v[32:35]\n"
        "ds_write_b128 v245, a[244:247] offset:11264\n"
        "v_mfma_f32_16x16x32_bf16 v[32:35], a[104:107], v[8:11], v[32:35]\n"
        "s_waitcnt lgkmcnt(1)\n"
        "v_bfe_u32 v245, v254, 4, 3\n"
        "v_lshl_add_u32 v245, v245, 7, s80\n"
        "v_mfma_f32_16x16x32_bf16 v[32:35], a[108:111], v[12:15], v[32:35]\n"
        "ds_read_b128 a[148:151], v241 offset:25600\n"
        "s_mov_b32 s80, s78\n"
        "v_cvt_pk_bf16_f32 v140, v152, v153\n"
        "v_mfma_f32_16x16x32_bf16 v[36:39], a[108:111], v[76:79], v[228:231]\n"
        "ds_read_b128 a[152:155], v241 offset:26624\n"
        "s_add_u32 m0, s80, 0x0\n"
        "v_cvt_pk_bf16_f32 v142, v156, v157\n"
        "v_mfma_f32_16x16x32_bf16 v[36:39], a[104:107], v[72:75], v[36:39]\n"
        "ds_read_b128 a[156:159], v241 offset:27648\n"
        "s_xor_b32 s78, s78, 0x8000\n"
        "v_cvt_pk_bf16_f32 v143, v158, v159\n"
        "v_mfma_f32_16x16x32_bf16 v[36:39], a[100:103], v[68:71], v[36:39]\n"
        "ds_read_b128 a[128:131], v241 offset:28672\n"
        "s_mov_b32 s80, s78\n"
        "v_cndmask_b32 v40, 0, v242, s[10:11]\n"
        "v_mfma_f32_16x16x32_bf16 v[36:39], a[96:99], v[64:67], v[36:39]\n"
        "ds_read_b128 a[132:135], v241 offset:29696\n"
        "ds_read_b128 a[136:139], v241 offset:30720\n"
        "ds_read_b128 a[140:143], v241 offset:31744\n"
        "v_exp_f32 v48, v48\n"
        "v_exp_f32 v49, v49\n"
        "v_exp_f32 v50, v50\n"
        "v_cvt_pk_bf16_f32 v82, v48, v49\n"
        "v_exp_f32 v51, v51\n"
        "v_xor_b32 v241, 0x8000, v241\n"
        "v_cvt_pk_bf16_f32 v83, v50, v51\n"
        "s_waitcnt lgkmcnt(6)\n"
        "s_nop 0\n"
        "v_mfma_f32_16x16x32_bf16 v[164:167], a[148:151], v[80:83], v[164:167]\n"
        "s_add_u32 s91, s44, 1\n"
        "v_cndmask_b32 v41, 0, v242, s[12:13]\n"
        "v_cndmask_b32 v42, 0, v242, s[14:15]\n"
        "v_mfma_f32_16x16x32_bf16 v[48:51], a[112:115], v[0:3], v[224:227]\n"
        "LHA10_%=:\n"
        "s_cbranch_execz LHRA_%=\n"
        "v_cndmask_b32 v43, 0, v242, s[16:17]\n"
        "s_nop 1\n"
        "v_mfma_f32_16x16x32_bf16 v[40:43], a[96:99], v[96:99], v[40:43]\n"
        "ds_read_b128 a[96:99], v240 offset:8192\n"
        "v_cndmask_b32 v56, 0, v242, s[20:21]\n"
        "v_mfma_f32_16x16x32_bf16 v[40:43], a[100:103], v[100:103], v[40:43]\n"
        "ds_read_b128 a[100:103], v240 offset:9216\n"
        "v_cndmask_b32 v57, 0, v242, s[22:23]\n"
        "v_mfma_f32_16x16x32_bf16 v[40:43], a[104:107], v[104:107], v[40:43]\n"
        "s_waitcnt lgkmcnt(2)\n"
        "v_exp_f32 v32, v32\n"
        "v_mfma_f32_16x16x32_bf16 v[40:43], a[108:111], v[108:111], v[40:43]\n"
        "s_barrier\n"
        "v_mfma_f32_16x16x32_bf16 v[48:51], a[116:119], v[4:7], v[48:51]\n"
        "ds_read_b128 a[148:151], v241 offset:1024\n"
        "v_cndmask_b32 v58, 0, v242, s[24:25]\n"
        "v_mfma_f32_16x16x32_bf16 v[48:51], a[120:123], v[8:11], v[48:51]\n"
        "v_exp_f32 v33, v33\n"
        "v_mfma_f32_16x16x32_bf16 v[48:51], a[124:127], v[12:15], v[48:51]\n"
        "v_exp_f32 v34, v34\n"
        "v_mfma_f32_16x16x32_bf16 a[64:67], a[76:79], v[80:83], a[64:67]\n"
        "ds_read_b128 a[104:107], v240 offset:10240\n"
        "v_max_f32 v246, v40, v41\n"
        "v_mfma_f32_16x16x32_bf16 v[160:163], a[144:147], v[80:83], v[160:163]\n"
        "ds_read_b128 a[108:111], v240 offset:11264\n"
        "v_max3_f32 v246, v42, v43, v246\n"
        "v_mfma_f32_16x16x32_bf16 v[168:171], a[152:155], v[80:83], v[168:171]\n"
        "ds_read_b128 a[144:147], v241 offset:0\n"
        "v_mov_b32 v247, v246\n"
        "v_mfma_f32_16x16x32_bf16 v[172:175], a[156:159], v[80:83], v[172:175]\n"
        "v_exp_f32 v35, v35\n"
        "v_mfma_f32_16x16x32_bf16 v[176:179], a[128:131], v[80:83], v[176:179]\n"
        "v_permlane16_swap_b32 v246, v247\n"
        "v_mfma_f32_16x16x32_bf16 v[180:183], a[132:135], v[80:83], v[180:183]\n"
        "s_waitcnt lgkmcnt(3)\n"
        "v_max_f32 v246, v246, v247\n"
        "v_mov_b32 v247, v246\n"
        "v_mfma_f32_16x16x32_bf16 v[184:187], a[136:139], v[80:83], v[184:187]\n"
        "v_exp_f32 v48, v48\n"
        "v_mfma_f32_16x16x32_bf16 v[188:191], a[140:143], v[80:83], v[188:191]\n"
        "v_permlane32_swap_b32 v246, v247\n"
        "v_max_f32 v246, v246, v247\n"
        "v_sub_f32 v232, 0, v246\n"
        "v_sub_f32 v233, 0, v246\n"
        "v_sub_f32 v234, 0, v246\n"
        "v_sub_f32 v235, 0, v246\n"
        "v_exp_f32 v49, v49\n"
        "v_exp_f32 v50, v50\n"
        "v_exp_f32 v51, v51\n"
        "v_cvt_pk_bf16_f32 v16, v32, v33\n"
        "v_cvt_pk_bf16_f32 v17, v34, v35\n"
        "v_mfma_f32_16x16x32_bf16 v[32:35], a[96:99], v[0:3], v[224:227]\n"
        "ds_read_b128 a[152:155], v241 offset:2048\n"
        "v_cvt_pk_bf16_f32 v18, v48, v49\n"
        "v_mfma_f32_16x16x32_bf16 v[32:35], a[100:103], v[4:7], v[32:35]\n"
        "s_waitcnt lgkmcnt(1)\n"
        "v_cvt_pk_bf16_f32 v19, v50, v51\n"
        "s_nop 1\n"
        "v_mfma_f32_16x16x32_bf16 v[164:167], a[148:151], v[16:19], v[164:167]\n"
        "global_load_dwordx2 v[252:253], v245, s[48:49] sc1\n"
        "v_cndmask_b32 v59, 0, v242, s[28:29]\n"
        "s_nop 1\n"
        "v_mfma_f32_16x16x32_bf16 v[56:59], a[112:115], v[96:99], v[56:59]\n"
        "v_exp_f32 v36, v36\n"
        "v_mfma_f32_16x16x32_bf16 v[56:59], a[116:119], v[100:103], v[56:59]\n"
        "v_exp_f32 v37, v37\n"
        "v_mfma_f32_16x16x32_bf16 v[56:59], a[120:123], v[104:107], v[56:59]\n"
        "ds_read_b128 a[112:115], v240 offset:12288\n"
        "v_cvt_pk_bf16_f32 v20, v36, v37\n"
        "v_mfma_f32_16x16x32_bf16 v[56:59], a[124:127], v[108:111], v[56:59]\n"
        "v_exp_f32 v38, v38\n"
        "v_mfma_f32_16x16x32_bf16 v[32:35], a[104:107], v[8:11], v[32:35]\n"
        "v_exp_f32 v39, v39\n"
        "v_mfma_f32_16x16x32_bf16 v[32:35], a[108:111], v[12:15], v[32:35]\n"
        "ds_read_b128 a[116:119], v240 offset:13312\n"
        "v_cvt_pk_bf16_f32 v21, v38, v39\n"
        "v_mfma_f32_16x16x32_bf16 v[36:39], a[108:111], v[76:79], v[228:231]\n"
        "ds_read_b128 a[120:123], v240 offset:14336\n"
        "v_add_f32 v40, v40, v232\n"
        "v_mfma_f32_16x16x32_bf16 v[36:39], a[104:107], v[72:75], v[36:39]\n"
        "v_exp_f32 v40, v40\n"
        "v_mfma_f32_16x16x32_bf16 v[36:39], a[100:103], v[68:71], v[36:39]\n"
        "ds_read_b128 a[156:159], v241 offset:3072\n"
        "v_add_f32 v41, v41, v232\n"
        "v_mfma_f32_16x16x32_bf16 v[36:39], a[96:99], v[64:67], v[36:39]\n"
        "v_exp_f32 v41, v41\n"
        "v_mfma_f32_16x16x32_bf16 v[160:163], a[144:147], v[16:19], v[160:163]\n"
        "s_waitcnt lgkmcnt(3)\n"
        "v_cvt_pk_bf16_f32 v24, v40, v41\n"
        "v_add_f32 v42, v42, v232\n"
        "v_mfma_f32_16x16x32_bf16 v[48:51], a[112:115], v[0:3], v[224:227]\n"
        "v_exp_f32 v42, v42\n"
        "v_mfma_f32_16x16x32_bf16 v[168:171], a[152:155], v[16:19], v[168:171]\n"
        "ds_read_b128 a[124:127], v240 offset:15360\n"
        "v_add_f32 v43, v43, v232\n"
        "s_waitcnt lgkmcnt(3)\n"
        "v_mfma_f32_16x16x32_bf16 v[48:51], a[116:119], v[4:7], v[48:51]\n"
        "s_waitcnt lgkmcnt(2)\n"
        "v_exp_f32 v43, v43\n"
        "v_mfma_f32_16x16x32_bf16 v[48:51], a[120:123], v[8:11], v[48:51]\n"
        "ds_read_b128 a[128:131], v241 offset:4096\n"
        "ds_read_b128 a[132:135], v241 offset:5120\n"
        "v_cvt_pk_bf16_f32 v25, v42, v43\n"
        "v_cndmask_b32 v40, v232, v242, s[30:31]\n"
        "s_waitcnt lgkmcnt(3)\n"
        "v_mfma_f32_16x16x32_bf16 v[172:175], a[156:159], v[16:19], v[172:175]\n"
        "s_mov_b64 exec, 0\n"
        "s_cmp_eq_u32 s40, 0\n"
        "s_cbranch_scc1 LHC0_%=\n"
        "s_cmp_eq_u32 s40, 1\n"
        "s_cbranch_scc1 LHC1_%=\n"
        "s_cmp_eq_u32 s40, 2\n"
        "s_cbranch_scc1 LHC2_%=\n"
        "s_branch LHC3_%=\n"
        "LHRC_%=:\n"
        "s_mov_b64 exec, -1\n"
        "ds_read_b128 a[136:139], v241 offset:6144\n"
        "ds_read_b128 a[140:143], v241 offset:7168\n"
        "v_exp_f32 v52, v52\n"
        "v_exp_f32 v53, v53\n"
        "s_waitcnt lgkmcnt(4)\n"
        "v_cvt_pk_bf16_f32 v22, v52, v53\n"
        "v_mfma_f32_16x16x32_bf16 v[48:51], a[124:127], v[12:15], v[48:51]\n"
        "v_cndmask_b32 v41, v232, v242, s[56:57]\n"
        "v_cndmask_b32 v42, v232, v242, s[58:59]\n"
        "v_cndmask_b32 v43, v232, v242, s[60:61]\n"
        "s_nop 1\n"
        "v_mfma_f32_16x16x32_bf16 v[40:43], a[96:99], v[96:99], v[40:43]\n"
        "ds_read_b128 a[96:99], v240 offset:16384\n"
        "v_add_f32 v56, v56, v232\n"
        "v_mfma_f32_16x16x32_bf16 v[40:43], a[100:103], v[100:103], v[40:43]\n"
        "v_exp_f32 v54, v54\n"
        "v_mfma_f32_16x16x32_bf16 v[40:43], a[104:107], v[104:107], v[40:43]\n"
        "s_waitcnt vmcnt(0) lgkmcnt(3)\n"
        "v_cmp_ne_u64 vcc, v[252:253], v[250:251]\n"
        "v_add_f32 v57, v57, v232\n"
        "v_mfma_f32_16x16x32_bf16 v[40:43], a[108:111], v[108:111], v[40:43]\n"
        "s_cbranch_vccnz LPKV_%=\n"
        "LBKV_%=:\n"
        "v_exp_f32 v55, v55\n"
        "v_mfma_f32_16x16x32_bf16 v[176:179], a[128:131], v[16:19], v[176:179]\n"
        "ds_read_b128 a[100:103], v240 offset:17408\n"
        "v_cvt_pk_bf16_f32 v23, v54, v55\n"
        "v_mfma_f32_16x16x32_bf16 v[52:55], a[124:127], v[76:79], v[228:231]\n"
        "v_exp_f32 v56, v56\n"
        "v_mfma_f32_16x16x32_bf16 v[52:55], a[120:123], v[72:75], v[52:55]\n"
        "ds_read_b128 a[104:107], v240 offset:18432\n"
        "v_mov_b32 v144, v254\n"
        "v_mfma_f32_16x16x32_bf16 v[52:55], a[116:119], v[68:71], v[52:55]\n"
        "v_exp_f32 v57, v57\n"
        "v_mfma_f32_16x16x32_bf16 v[52:55], a[112:115], v[64:67], v[52:55]\n"
        "ds_read_b128 a[108:111], v240 offset:19456\n"
        "v_mov_b32 v145, v254\n"
        "v_smfmac_f32_16x16x64_bf16 a[64:67], a[72:75], v[16:23], v154\n"
        "global_load_lds_dwordx4 v254, s[68:69] offset:0 sc1\n"
        "v_cvt_pk_bf16_f32 v26, v56, v57\n"
        "v_mfma_f32_16x16x32_bf16 v[192:195], a[144:147], v[20:23], v[192:195]\n"
        "v_mov_b32 v146, v254\n"
        "v_mov_b32 v147, v254\n"
        "v_mfma_f32_16x16x32_bf16 v[196:199], a[148:151], v[20:23], v[196:199]\n"
        "v_accvgpr_read_b32 v148, a88\n"
        "v_accvgpr_read_b32 v152, a89\n"
        "v_mfma_f32_16x16x32_bf16 v[200:203], a[152:155], v[20:23], v[200:203]\n"
        "v_accvgpr_read_b32 v156, a90\n"
        "v_accvgpr_read_b32 v153, a92\n"
        "v_mfma_f32_16x16x32_bf16 v[204:207], a[156:159], v[20:23], v[204:207]\n"
        "global_load_lds_dwordx4 v254, s[68:69] offset:1024 sc1\n"
        "v_accvgpr_read_b32 v155, a94\n"
        "v_mfma_f32_16x16x32_bf16 v[208:211], a[128:131], v[20:23], v[208:211]\n"
        "v_add_u32 v149, 1024, v148\n"
        "v_accvgpr_write_b32 a88, 0\n"
        "v_mfma_f32_16x16x32_bf16 v[212:215], a[132:135], v[20:23], v[212:215]\n"
        "v_add_u32 v150, 2048, v148\n"
        "v_accvgpr_write_b32 a89, 0\n"
        "v_mfma_f32_16x16x32_bf16 v[180:183], a[132:135], v[16:19], v[180:183]\n"
        "s_waitcnt lgkmcnt(0)\n"
        "v_add_u32 v151, 3072, v148\n"
        "v_accvgpr_write_b32 a90, 0\n"
        "v_mfma_f32_16x16x32_bf16 v[184:187], a[136:139], v[16:19], v[184:187]\n"
        "global_load_lds_dwordx4 v254, s[68:69] offset:2048 sc1\n"
        "v_add_f32 v58, v58, v232\n"
        "v_mfma_f32_16x16x32_bf16 v[216:219], a[136:139], v[20:23], v[216:219]\n"
        "v_exp_f32 v58, v58\n"
        "v_mfma_f32_16x16x32_bf16 v[220:223], a[140:143], v[20:23], v[220:223]\n"
        "v_add_f32 v59, v59, v232\n"
        "v_cndmask_b32 v56, v232, v242, s[62:63]\n"
        "v_mfma_f32_16x16x32_bf16 v[188:191], a[140:143], v[16:19], v[188:191]\n"
        "v_exp_f32 v59, v59\n"
        "v_exp_f32 v32, v32\n"
        "v_cvt_pk_bf16_f32 v27, v58, v59\n"
        "s_nop 1\n"
        "v_mfma_f32_16x16x32_bf16 a[68:71], a[76:79], v[24:27], a[68:71]\n"
        "v_exp_f32 v33, v33\n"
        "v_mfma_f32_16x16x32_bf16 a[0:3], a[144:147], v[24:27], 0\n"
        "ds_read_b128 a[144:147], v241 offset:8192\n"
        "v_cvt_pk_bf16_f32 v80, v32, v33\n"
        "v_mfma_f32_16x16x32_bf16 a[4:7], a[148:151], v[24:27], 0\n"
        "v_exp_f32 v34, v34\n"
        "v_mfma_f32_16x16x32_bf16 a[8:11], a[152:155], v[24:27], 0\n"
        "v_exp_f32 v35, v35\n"
        "v_mfma_f32_16x16x32_bf16 a[12:15], a[156:159], v[24:27], 0\n"
        "ds_read_b128 a[148:151], v241 offset:9216\n"
        "v_cvt_pk_bf16_f32 v81, v34, v35\n"
        "v_mfma_f32_16x16x32_bf16 v[32:35], a[96:99], v[0:3], v[224:227]\n"
        "ds_read_b128 a[152:155], v241 offset:10240\n"
        "v_cndmask_b32 v57, v232, v242, s[84:85]\n"
        "v_mfma_f32_16x16x32_bf16 v[32:35], a[100:103], v[4:7], v[32:35]\n"
        "ds_read_b128 a[156:159], v241 offset:11264\n"
        "v_cndmask_b32 v58, v232, v242, s[86:87]\n"
        "v_mfma_f32_16x16x32_bf16 v[32:35], a[104:107], v[8:11], v[32:35]\n"
        "global_load_lds_dwordx4 v254, s[68:69] offset:3072 sc1\n"
        "s_add_u32 m0, s81, 0x0\n"
        "v_cndmask_b32 v59, v232, v242, s[88:89]\n"
        "s_nop 1\n"
        "v_mfma_f32_16x16x32_bf16 v[56:59], a[112:115], v[96:99], v[56:59]\n"
        "s_add_u32 s81, s78, 4096\n"
        "v_exp_f32 v36, v36\n"
        "v_mfma_f32_16x16x32_bf16 v[56:59], a[116:119], v[100:103], v[56:59]\n"
        "v_exp_f32 v37, v37\n"
        "v_mfma_f32_16x16x32_bf16 v[56:59], a[120:123], v[104:107], v[56:59]\n"
        "ds_read_b128 a[112:115], v240 offset:20480\n"
        "v_cvt_pk_bf16_f32 v84, v36, v37\n"
        "v_mfma_f32_16x16x32_bf16 v[56:59], a[124:127], v[108:111], v[56:59]\n"
        "v_exp_f32 v38, v38\n"
        "v_mfma_f32_16x16x32_bf16 v[32:35], a[108:111], v[12:15], v[32:35]\n"
        "v_exp_f32 v39, v39\n"
        "v_mfma_f32_16x16x32_bf16 a[16:19], a[128:131], v[24:27], 0\n"
        "ds_read_b128 a[116:119], v240 offset:21504\n"
        "v_cvt_pk_bf16_f32 v85, v38, v39\n"
        "v_mfma_f32_16x16x32_bf16 v[36:39], a[108:111], v[76:79], v[228:231]\n"
        "v_exp_f32 v40, v40\n"
        "v_mfma_f32_16x16x32_bf16 v[36:39], a[104:107], v[72:75], v[36:39]\n"
        "v_exp_f32 v41, v41\n"
        "v_mfma_f32_16x16x32_bf16 v[36:39], a[100:103], v[68:71], v[36:39]\n"
        "ds_read_b128 a[120:123], v240 offset:22528\n"
        "v_cvt_pk_bf16_f32 v88, v40, v41\n"
        "v_mfma_f32_16x16x32_bf16 v[36:39], a[96:99], v[64:67], v[36:39]\n"
        "ds_read_b128 a[124:127], v240 offset:23552\n"
        "v_and_b32 v245, 0x70, v254\n"
        "v_mfma_f32_16x16x32_bf16 a[20:23], a[132:135], v[24:27], 0\n"
        "global_load_lds_dwordx4 v254, s[74:75] offset:0 sc1\n"
        "v_lshlrev_b32 v245, 3, v245\n"
        "v_mfma_f32_16x16x32_bf16 a[24:27], a[136:139], v[24:27], 0\n"
        "v_exp_f32 v42, v42\n"
        "v_mfma_f32_16x16x32_bf16 a[28:31], a[140:143], v[24:27], 0\n"
        "v_exp_f32 v43, v43\n"
        "v_exp_f32 v48, v48\n"
        "v_cvt_pk_bf16_f32 v89, v42, v43\n"
        "v_mfma_f32_16x16x32_bf16 v[40:43], a[96:99], v[96:99], v[232:235]\n"
        "v_exp_f32 v49, v49\n"
        "v_mfma_f32_16x16x32_bf16 v[40:43], a[100:103], v[100:103], v[40:43]\n"
        "global_load_lds_dwordx4 v254, s[74:75] offset:1024 sc1\n"
        "v_cvt_pk_bf16_f32 v82, v48, v49\n"
        "v_mfma_f32_16x16x32_bf16 v[40:43], a[104:107], v[104:107], v[40:43]\n"
        "v_exp_f32 v50, v50\n"
        "v_mfma_f32_16x16x32_bf16 v[40:43], a[108:111], v[108:111], v[40:43]\n"
        "v_exp_f32 v51, v51\n"
        "v_exp_f32 v52, v52\n"
        "v_cvt_pk_bf16_f32 v83, v50, v51\n"
        "s_waitcnt lgkmcnt(0)\n"
        "v_mfma_f32_16x16x32_bf16 v[48:51], a[112:115], v[0:3], v[224:227]\n"
        "v_exp_f32 v53, v53\n"
        "v_mfma_f32_16x16x32_bf16 v[48:51], a[116:119], v[4:7], v[48:51]\n"
        "global_load_lds_dwordx4 v254, s[74:75] offset:2048 sc1\n"
        "v_cvt_pk_bf16_f32 v86, v52, v53\n"
        "v_mfma_f32_16x16x32_bf16 v[48:51], a[120:123], v[8:11], v[48:51]\n"
        "ds_read_b128 a[128:131], v241 offset:12288\n"
        "v_cndmask_b32 v44, 0, v242, s[10:11]\n"
        "v_mfma_f32_16x16x32_bf16 v[48:51], a[124:127], v[12:15], v[48:51]\n"
        "ds_read_b128 a[132:135], v241 offset:13312\n"
        "s_lshr_b32 s10, s41, 1\n"
        "v_cndmask_b32 v45, 0, v242, s[12:13]\n"
        "v_mfma_f32_16x16x32_bf16 v[160:163], a[144:147], v[80:83], v[160:163]\n"
        "ds_read_b128 a[136:139], v241 offset:14336\n"
        "s_and_b32 s11, s42, 7\n"
        "v_cndmask_b32 v46, 0, v242, s[14:15]\n"
        "v_mfma_f32_16x16x32_bf16 v[164:167], a[148:151], v[80:83], v[164:167]\n"
        "global_load_lds_dwordx4 v254, s[74:75] offset:3072 sc1\n"
        "s_add_u32 m0, s79, 0x0\n"
        "v_cndmask_b32 v47, 0, v242, s[16:17]\n"
        "s_nop 1\n"
        "v_mfma_f32_16x16x32_bf16 v[44:47], a[108:111], v[140:143], v[44:47]\n"
        "s_and_b32 s12, s41, 1\n"
        "v_exp_f32 v54, v54\n"
        "v_mfma_f32_16x16x32_bf16 v[44:47], a[104:107], v[136:139], v[44:47]\n"
        "s_lshr_b32 s14, s42, 3\n"
        "v_exp_f32 v55, v55\n"
        "v_mfma_f32_16x16x32_bf16 v[44:47], a[100:103], v[132:135], v[44:47]\n"
        "ds_read_b128 a[100:103], v240 offset:25600\n"
        "s_lshl2_add_u32 s12, s12, s14\n"
        "v_cvt_pk_bf16_f32 v87, v54, v55\n"
        "v_mfma_f32_16x16x32_bf16 v[44:47], a[96:99], v[128:131], v[44:47]\n"
        "ds_read_b128 a[96:99], v240 offset:24576\n"
        "global_load_lds_dwordx4 v254, s[76:77] offset:0 sc1\n"
        "s_lshl3_add_u32 s13, s12, s11\n"
        "v_mfma_f32_16x16x32_bf16 v[52:55], a[124:127], v[76:79], v[228:231]\n"
        "s_lshl_b32 s14, s10, 21\n"
        "v_exp_f32 v56, v56\n"
        "v_mfma_f32_16x16x32_bf16 v[52:55], a[120:123], v[72:75], v[52:55]\n"
        "s_lshl_b32 s15, s40, 15\n"
        "v_exp_f32 v57, v57\n"
        "v_mfma_f32_16x16x32_bf16 v[52:55], a[116:119], v[68:71], v[52:55]\n"
        "ds_read_b128 a[104:107], v240 offset:26624\n"
        "s_add_u32 s14, s14, s15\n"
        "v_cvt_pk_bf16_f32 v90, v56, v57\n"
        "v_mfma_f32_16x16x32_bf16 v[52:55], a[112:115], v[64:67], v[52:55]\n"
        "s_lshl_b32 s15, s11, 8\n"
        "v_exp_f32 v58, v58\n"
        "v_smfmac_f32_16x16x64_bf16 a[64:67], a[72:75], v[80:87], v154\n"
        "ds_read_b128 a[140:143], v241 offset:15360\n"
        "global_load_lds_dwordx4 v254, s[76:77] offset:1024 sc1\n"
        "s_add_u32 s14, s14, s15\n"
        "v_mfma_f32_16x16x32_bf16 v[192:195], a[144:147], v[84:87], v[192:195]\n"
        "s_add_u32 s17, s11, s12\n"
        "v_exp_f32 v59, v59\n"
        "v_mfma_f32_16x16x32_bf16 v[196:199], a[148:151], v[84:87], v[196:199]\n"
        "ds_read_b128 a[108:111], v240 offset:27648\n"
        "s_and_b32 s17, s17, 7\n"
        "v_cvt_pk_bf16_f32 v91, v58, v59\n"
        "v_mfma_f32_16x16x32_bf16 v[56:59], a[112:115], v[96:99], v[232:235]\n"
        "v_exp_f32 v32, v32\n"
        "v_mfma_f32_16x16x32_bf16 v[56:59], a[116:119], v[100:103], v[56:59]\n"
        "v_exp_f32 v33, v33\n"
        "v_mfma_f32_16x16x32_bf16 v[56:59], a[120:123], v[104:107], v[56:59]\n"
        "global_load_lds_dwordx4 v254, s[76:77] offset:2048 sc1\n"
        "v_cvt_pk_bf16_f32 v16, v32, v33\n"
        "v_mfma_f32_16x16x32_bf16 v[56:59], a[124:127], v[108:111], v[56:59]\n"
        "v_exp_f32 v34, v34\n"
        "v_mfma_f32_16x16x32_bf16 a[68:71], a[76:79], v[88:91], a[68:71]\n"
        "v_exp_f32 v35, v35\n"
        "v_mfma_f32_16x16x32_bf16 a[0:3], a[144:147], v[88:91], a[0:3]\n"
        "ds_read_b128 a[144:147], v241 offset:16384\n"
        "v_cvt_pk_bf16_f32 v17, v34, v35\n"
        "v_mfma_f32_16x16x32_bf16 a[4:7], a[148:151], v[88:91], a[4:7]\n"
        "v_exp_f32 v36, v36\n"
        "v_mfma_f32_16x16x32_bf16 v[168:171], a[152:155], v[80:83], v[168:171]\n"
        "v_exp_f32 v37, v37\n"
        "v_mfma_f32_16x16x32_bf16 v[200:203], a[152:155], v[84:87], v[200:203]\n"
        "global_load_lds_dwordx4 v254, s[76:77] offset:3072 sc1\n"
        "s_add_u32 m0, s79, 0x1000\n"
        "v_max_f32 v248, v44, v45\n"
        "v_mfma_f32_16x16x32_bf16 a[8:11], a[152:155], v[88:91], a[8:11]\n"
        "ds_read_b128 a[148:151], v241 offset:17408\n"
        "v_cvt_pk_bf16_f32 v20, v36, v37\n"
        "v_mfma_f32_16x16x32_bf16 a[12:15], a[156:159], v[88:91], a[12:15]\n"
        "ds_read_b128 a[152:155], v241 offset:18432\n"
        "v_max3_f32 v248, v46, v47, v248\n"
        "v_mfma_f32_16x16x32_bf16 v[204:207], a[156:159], v[84:87], v[204:207]\n"
        "s_add_u32 s76, s76, 0x1000\n"
        "s_addc_u32 s77, s77, 0\n"
        "v_mov_b32 v249, v248\n"
        "v_mfma_f32_16x16x32_bf16 v[172:175], a[156:159], v[80:83], v[172:175]\n"
        "s_waitcnt lgkmcnt(3)\n"
        "v_permlane16_swap_b32 v248, v249\n"
        "v_mfma_f32_16x16x32_bf16 v[32:35], a[96:99], v[0:3], v[224:227]\n"
        "global_load_lds_dwordx4 v254, s[76:77] offset:0 sc1\n"
        "v_max_f32 v248, v248, v249\n"
        "v_mfma_f32_16x16x32_bf16 v[32:35], a[100:103], v[4:7], v[32:35]\n"
        "ds_read_b128 a[156:159], v241 offset:19456\n"
        "v_mov_b32 v249, v248\n"
        "v_mfma_f32_16x16x32_bf16 v[32:35], a[104:107], v[8:11], v[32:35]\n"
        "v_exp_f32 v38, v38\n"
        "v_mfma_f32_16x16x32_bf16 v[32:35], a[108:111], v[12:15], v[32:35]\n"
        "v_permlane32_swap_b32 v248, v249\n"
        "v_mfma_f32_16x16x32_bf16 v[176:179], a[128:131], v[80:83], v[176:179]\n"
        "global_load_lds_dwordx4 v254, s[76:77] offset:1024 sc1\n"
        "v_max_f32 v248, v248, v249\n"
        "v_mfma_f32_16x16x32_bf16 v[208:211], a[128:131], v[84:87], v[208:211]\n"
        "s_add_u32 s68, s64, s90\n"
        "s_addc_u32 s69, s65, 0\n"
        "v_sub_f32 v236, 0, v248\n"
        "v_mfma_f32_16x16x32_bf16 a[16:19], a[128:131], v[88:91], a[16:19]\n"
        "ds_read_b128 a[128:131], v241 offset:20480\n"
        "v_sub_f32 v237, 0, v248\n"
        "v_mfma_f32_16x16x32_bf16 a[20:23], a[132:135], v[88:91], a[20:23]\n"
        "s_add_u32 s74, s68, 0x1000\n"
        "s_addc_u32 s75, s69, 0\n"
        "v_sub_f32 v238, 0, v248\n"
        "v_mfma_f32_16x16x32_bf16 v[212:215], a[132:135], v[84:87], v[212:215]\n"
        "global_load_lds_dwordx4 v254, s[76:77] offset:2048 sc1\n"
        "v_sub_f32 v239, 0, v248\n"
        "v_mfma_f32_16x16x32_bf16 v[180:183], a[132:135], v[80:83], v[180:183]\n"
        "v_exp_f32 v39, v39\n"
        "v_mfma_f32_16x16x32_bf16 v[184:187], a[136:139], v[80:83], v[184:187]\n"
        "ds_read_b128 a[132:135], v241 offset:21504\n"
        "v_cvt_pk_bf16_f32 v21, v38, v39\n"
        "v_mfma_f32_16x16x32_bf16 v[36:39], a[108:111], v[76:79], v[228:231]\n"
        "v_cndmask_b32 v60, 0, v242, s[20:21]\n"
        "v_cndmask_b32 v61, 0, v242, s[22:23]\n"
        "v_mfma_f32_16x16x32_bf16 v[36:39], a[104:107], v[72:75], v[36:39]\n"
        "global_load_lds_dwordx4 v254, s[76:77] offset:3072 sc1\n"
        "s_add_u32 m0, s96, 0x20260\n"
        "v_cndmask_b32 v62, 0, v242, s[24:25]\n"
        "v_mfma_f32_16x16x32_bf16 v[36:39], a[100:103], v[68:71], v[36:39]\n"
        "s_sub_u32 s24, s83, 1\n"
        "v_cndmask_b32 v63, 0, v242, s[28:29]\n"
        "s_nop 1\n"
        "v_mfma_f32_16x16x32_bf16 v[60:63], a[124:127], v[140:143], v[60:63]\n"
        "s_min_u32 s24, s24, s91\n"
        "v_exp_f32 v40, v40\n"
        "v_mfma_f32_16x16x32_bf16 v[60:63], a[120:123], v[136:139], v[60:63]\n"
        "s_mov_b32 s25, 4096\n"
        "v_exp_f32 v41, v41\n"
        "v_mfma_f32_16x16x32_bf16 v[60:63], a[116:119], v[132:135], v[60:63]\n"
        "ds_read_b128 a[116:119], v240 offset:29696\n"
        "s_mov_b32 s28, 4096\n"
        "v_cvt_pk_bf16_f32 v24, v40, v41\n"
        "v_mfma_f32_16x16x32_bf16 v[60:63], a[112:115], v[128:131], v[60:63]\n"
        "ds_read_b128 a[112:115], v240 offset:28672\n"
        "s_lshl_b32 s29, s17, 18\n"
        "v_add_f32 v44, v44, v236\n"
        "v_mfma_f32_16x16x32_bf16 v[36:39], a[96:99], v[64:67], v[36:39]\n"
        "v_exp_f32 v42, v42\n"
        "v_mfma_f32_16x16x32_bf16 v[216:219], a[136:139], v[84:87], v[216:219]\n"
        "v_exp_f32 v43, v43\n"
        "v_mfma_f32_16x16x32_bf16 a[24:27], a[136:139], v[88:91], a[24:27]\n"
        "ds_read_b128 a[120:123], v240 offset:30720\n"
        "v_cvt_pk_bf16_f32 v25, v42, v43\n"
        "v_mfma_f32_16x16x32_bf16 v[40:43], a[96:99], v[96:99], v[232:235]\n"
        "v_exp_f32 v44, v44\n"
        "v_mfma_f32_16x16x32_bf16 v[40:43], a[100:103], v[100:103], v[40:43]\n"
        "global_load_lds_dwordx4 v254, s[70:71] offset:0\n"
        "v_add_f32 v45, v45, v236\n"
        "v_mfma_f32_16x16x32_bf16 v[40:43], a[104:107], v[104:107], v[40:43]\n"
        "v_exp_f32 v45, v45\n"
        "v_mfma_f32_16x16x32_bf16 v[40:43], a[108:111], v[108:111], v[40:43]\n"
        "ds_read_b128 a[124:127], v240 offset:31744\n"
        "v_cvt_pk_bf16_f32 v28, v44, v45\n"
        "v_mfma_f32_16x16x32_bf16 a[28:31], a[140:143], v[88:91], a[28:31]\n"
        "ds_read_b128 a[136:139], v241 offset:22528\n"
        "v_add_f32 v46, v46, v236\n"
        "v_mfma_f32_16x16x32_bf16 v[220:223], a[140:143], v[84:87], v[220:223]\n"
        "v_exp_f32 v46, v46\n"
        "v_mfma_f32_16x16x32_bf16 v[188:191], a[140:143], v[80:83], v[188:191]\n"
        "v_add_f32 v47, v47, v236\n"
        "v_exp_f32 v47, v47\n"
        "v_exp_f32 v48, v48\n"
        "v_cvt_pk_bf16_f32 v29, v46, v47\n"
        "v_exp_f32 v49, v49\n"
        "global_load_lds_dwordx4 v254, s[70:71] offset:1024\n"
        "v_cvt_pk_bf16_f32 v18, v48, v49\n"
        "v_exp_f32 v50, v50\n"
        "v_exp_f32 v51, v51\n"
        "v_exp_f32 v52, v52\n"
        "v_cvt_pk_bf16_f32 v19, v50, v51\n"
        "s_waitcnt lgkmcnt(2)\n"
        "v_mfma_f32_16x16x32_bf16 v[48:51], a[112:115], v[0:3], v[224:227]\n"
        "s_waitcnt vmcnt(10) lgkmcnt(1)\n"
        "v_exp_f32 v53, v53\n"
        "v_mfma_f32_16x16x32_bf16 v[48:51], a[116:119], v[4:7], v[48:51]\n"
        "global_load_lds_dwordx4 v254, s[70:71] offset:2048\n"
        "v_cvt_pk_bf16_f32 v22, v52, v53\n"
        "v_mfma_f32_16x16x32_bf16 v[48:51], a[120:123], v[8:11], v[48:51]\n"
        "v_exp_f32 v54, v54\n"
        "v_mfma_f32_16x16x32_bf16 v[48:51], a[124:127], v[12:15], v[48:51]\n"
        "v_exp_f32 v55, v55\n"
        "v_mfma_f32_16x16x32_bf16 v[160:163], a[144:147], v[16:19], v[160:163]\n"
        "ds_read_b128 a[140:143], v241 offset:23552\n"
        "v_cvt_pk_bf16_f32 v23, v54, v55\n"
        "v_mfma_f32_16x16x32_bf16 v[52:55], a[124:127], v[76:79], v[228:231]\n"
        "global_load_dword v244, v245, s[26:27] offset:0\n"
        "v_cndmask_b32 v44, v236, v242, s[30:31]\n"
        "v_mfma_f32_16x16x32_bf16 v[52:55], a[120:123], v[72:75], v[52:55]\n"
        "s_add_u32 s26, s26, 0x20000\n"
        "s_addc_u32 s27, s27, 0\n"
        "v_cndmask_b32 v45, v236, v242, s[56:57]\n"
        "v_mfma_f32_16x16x32_bf16 v[52:55], a[116:119], v[68:71], v[52:55]\n"
        "global_load_dword v244, v245, s[26:27] offset:0\n"
        "s_mov_b32 s30, 131072\n"
        "v_cndmask_b32 v46, v236, v242, s[58:59]\n"
        "v_mfma_f32_16x16x32_bf16 v[52:55], a[112:115], v[64:67], v[52:55]\n"
        "s_add_u32 s26, s26, 0x20000\n"
        "s_addc_u32 s27, s27, 0\n"
        "v_cndmask_b32 v47, v236, v242, s[60:61]\n"
        "s_nop 1\n"
        "v_mfma_f32_16x16x32_bf16 v[44:47], a[108:111], v[140:143], v[44:47]\n"
        "v_exp_f32 v56, v56\n"
        "v_mfma_f32_16x16x32_bf16 v[44:47], a[104:107], v[136:139], v[44:47]\n"
        "v_exp_f32 v57, v57\n"
        "v_mfma_f32_16x16x32_bf16 v[44:47], a[100:103], v[132:135], v[44:47]\n"
        "s_add_u32 s22, s18, s14\n"
        "s_addc_u32 s23, s19, 0\n"
        "v_cvt_pk_bf16_f32 v26, v56, v57\n"
        "v_mfma_f32_16x16x32_bf16 v[44:47], a[96:99], v[128:131], v[44:47]\n"
        "s_barrier\n"
        "v_smfmac_f32_16x16x64_bf16 a[64:67], a[72:75], v[16:23], v154\n"
        "s_sub_u32 s22, s22, 4096\n"
        "s_subb_u32 s23, s23, 0\n"
        "v_xor_b32 v240, 0x8000, v240\n"
        "v_mfma_f32_16x16x32_bf16 v[192:195], a[144:147], v[20:23], v[192:195]\n"
        "ds_read_b128 a[96:99], v240 offset:0\n"
        "s_lshl_b32 s14, s10, 18\n"
        "v_lshrrev_b32 v252, 2, v254\n"
        "v_mfma_f32_16x16x32_bf16 v[196:199], a[148:151], v[20:23], v[196:199]\n"
        "s_add_u32 s14, s14, s72\n"
        "v_exp_f32 v58, v58\n"
        "v_mfma_f32_16x16x32_bf16 v[164:167], a[148:151], v[16:19], v[164:167]\n"
        "s_add_u32 s14, s14, 0x2200000\n"
        "v_exp_f32 v59, v59\n"
        "v_mfma_f32_16x16x32_bf16 v[168:171], a[152:155], v[16:19], v[168:171]\n"
        "ds_read_b128 a[100:103], v240 offset:1024\n"
        "v_cvt_pk_bf16_f32 v27, v58, v59\n"
        "v_mfma_f32_16x16x32_bf16 v[56:59], a[112:115], v[96:99], v[232:235]\n"
        "ds_read_b128 a[104:107], v240 offset:2048\n"
        "v_add_f32 v60, v60, v236\n"
        "v_mfma_f32_16x16x32_bf16 v[56:59], a[116:119], v[100:103], v[56:59]\n"
        "v_exp_f32 v60, v60\n"
        "v_mfma_f32_16x16x32_bf16 v[56:59], a[120:123], v[104:107], v[56:59]\n"
        "ds_read_b128 a[108:111], v240 offset:3072\n"
        "v_add_f32 v61, v61, v236\n"
        "v_mfma_f32_16x16x32_bf16 v[56:59], a[124:127], v[108:111], v[56:59]\n"
        "v_exp_f32 v61, v61\n"
        "v_mfma_f32_16x16x32_bf16 a[0:3], a[144:147], v[24:27], a[0:3]\n"
        "s_add_u32 s20, s38, s14\n"
        "s_addc_u32 s21, s39, 0\n"
        "v_cvt_pk_bf16_f32 v30, v60, v61\n"
        "v_mfma_f32_16x16x32_bf16 a[4:7], a[148:151], v[24:27], a[4:7]\n"
        "s_waitcnt lgkmcnt(3)\n"
        "v_add_f32 v62, v62, v236\n"
        "v_add_f32 v63, v63, v236\n"
        "v_mfma_f32_16x16x32_bf16 v[200:203], a[152:155], v[20:23], v[200:203]\n"
        "v_exp_f32 v62, v62\n"
        "v_mfma_f32_16x16x32_bf16 a[8:11], a[152:155], v[24:27], a[8:11]\n"
        "v_exp_f32 v63, v63\n"
        "v_mfma_f32_16x16x32_bf16 a[12:15], a[156:159], v[24:27], a[12:15]\n"
        "v_cvt_pk_bf16_f32 v31, v62, v63\n"
        "s_nop 1\n"
        "v_smfmac_f32_16x16x64_bf16 a[68:71], a[72:75], v[24:31], v154\n"
        "v_cndmask_b32 v60, v236, v242, s[62:63]\n"
        "v_mfma_f32_16x16x32_bf16 a[32:35], a[144:147], v[28:31], 0\n"
        "v_exp_f32 v32, v32\n"
        "v_mfma_f32_16x16x32_bf16 a[36:39], a[148:151], v[28:31], 0\n"
        "v_exp_f32 v33, v33\n"
        "v_mfma_f32_16x16x32_bf16 a[40:43], a[152:155], v[28:31], 0\n"
        "ds_read_b128 a[144:147], v241 offset:24576\n"
        "v_cvt_pk_bf16_f32 v80, v32, v33\n"
        "v_mfma_f32_16x16x32_bf16 a[44:47], a[156:159], v[28:31], 0\n"
        "v_exp_f32 v34, v34\n"
        "v_mfma_f32_16x16x32_bf16 v[204:207], a[156:159], v[20:23], v[204:207]\n"
        "v_exp_f32 v35, v35\n"
        "v_mfma_f32_16x16x32_bf16 v[172:175], a[156:159], v[16:19], v[172:175]\n"
        "s_mov_b32 exec_hi, 0x7fffffff\n"
        "global_load_lds_dwordx4 v254, s[70:71] offset:3072\n"
        "s_mov_b64 exec, -1\n"
        "v_cvt_pk_bf16_f32 v81, v34, v35\n"
        "v_exp_f32 v36, v36\n"
        "v_exp_f32 v37, v37\n"
        "v_cndmask_b32 v61, v236, v242, s[84:85]\n"
        "v_cvt_pk_bf16_f32 v84, v36, v37\n"
        "v_cndmask_b32 v62, v236, v242, s[86:87]\n"
        "v_cndmask_b32 v63, v236, v242, s[88:89]\n"
        "s_nop 1\n"
        "v_mfma_f32_16x16x32_bf16 v[60:63], a[124:127], v[140:143], v[60:63]\n"
        "v_exp_f32 v38, v38\n"
        "v_mfma_f32_16x16x32_bf16 v[60:63], a[120:123], v[136:139], v[60:63]\n"
        "v_exp_f32 v39, v39\n"
        "v_mfma_f32_16x16x32_bf16 v[60:63], a[116:119], v[132:135], v[60:63]\n"
        "ds_read_b128 a[116:119], v240 offset:5120\n"
        "v_cvt_pk_bf16_f32 v85, v38, v39\n"
        "v_mfma_f32_16x16x32_bf16 v[60:63], a[112:115], v[128:131], v[60:63]\n"
        "s_mov_b64 exec, 7\n"
        "global_load_lds_dword v252, s[70:71] offset:4080\n"
        "s_mov_b64 exec, -1\n"
        "ds_read_b128 a[112:115], v240 offset:4096\n"
        "v_exp_f32 v40, v40\n"
        "v_exp_f32 v41, v41\n"
        "v_exp_f32 v42, v42\n"
        "v_cvt_pk_bf16_f32 v88, v40, v41\n"
        "v_exp_f32 v43, v43\n"
        "ds_read_b128 a[148:151], v241 offset:25600\n"
        "v_cvt_pk_bf16_f32 v89, v42, v43\n"
        "v_exp_f32 v44, v44\n"
        "v_exp_f32 v45, v45\n"
        "ds_read_b128 a[152:155], v241 offset:26624\n"
        "v_cvt_pk_bf16_f32 v92, v44, v45\n"
        "v_exp_f32 v46, v46\n"
        "v_exp_f32 v47, v47\n"
        "ds_read_b128 a[120:123], v240 offset:6144\n"
        "v_cvt_pk_bf16_f32 v93, v46, v47\n"
        "s_cmp_eq_u32 s44, 0\n"
        "s_cbranch_scc1 LLD_%=\n"
        "; ---- DUAL\n"
        "s_branch LDU_%=\n"
        "s_nop 0\n"
        "s_nop 0\n"
        "s_nop 0\n"
        "s_nop 0\n"
        "s_nop 0\n"
        "s_nop 0\n"
        "s_nop 0\n"
        "s_nop 0\n"
        "s_nop 0\n"
        "s_nop 0\n"
        "s_nop 0\n"
        "s_nop 0\n"
        "s_nop 0\n"
        "s_nop 0\n"
        "s_nop 0\n"
        "s_nop 0\n"
        "s_nop 0\n"
        "s_nop 0\n"
        "s_nop 0\n"
        "s_nop 0\n"
        "s_nop 0\n"
        "s_nop 0\n"
        "s_nop 0\n"
        "s_nop 0\n"
        "s_nop 0\n"
        "s_nop 0\n"
        "s_nop 0\n"
        "s_nop 0\n"
        "s_nop 0\n"
        "s_nop 0\n"
        "s_nop 0\n"
        "s_nop 0\n"
        "s_nop 0\n"
        "s_nop 0\n"
        "s_nop 0\n"
        "s_nop 0\n"
        "s_nop 0\n"
        "s_nop 0\n"
        "s_nop 0\n"
        "s_nop 0\n"
        "s_nop 0\n"
        "s_nop 0\n"
        "s_nop 0\n"
        "s_nop 0\n"
        "s_nop 0\n"
        "s_nop 0\n"
        "s_nop 0\n"
        "s_nop 0\n"
        "s_nop 0\n"
        "s_nop 0\n"
        "s_nop 0\n"
        "s_nop 0\n"
        "s_nop 0\n"
        "s_nop 0\n"
        "s_nop 0\n"
        "s_nop 0\n"
        "s_nop 0\n"
        "s_nop 0\n"
        "s_nop 0\n"
        "s_nop 0\n"
        "s_nop 0\n"
        "s_nop 0\n"
        "s_nop 0\n"
        "s_nop 0\n"
        "s_nop 0\n"
        "s_nop 0\n"
        "s_nop 0\n"
        "s_nop 0\n"
        "s_nop 0\n"
        "s_nop 0\n"
        "LHA11_%=:\n"
        "s_cbranch_execz LHRA_%=\n"
        "s_nop 0\n"
        "s_nop 0\n"
        "s_nop 0\n"
        "s_nop 0\n"
        "s_nop 0\n"
        "s_nop 0\n"
        "s_nop 0\n"
        "s_nop 0\n"
        "s_nop 0\n"
        "s_nop 0\n"
        "s_nop 0\n"
        "s_nop 0\n"
        "s_nop 0\n"
        "s_nop 0\n"
        "s_nop 0\n"
        "LDU_%=:\n"
        "v_mfma_f32_16x16x32_bf16 v[32:35], a[96:99], v[0:3], v[224:227]\n"
        "s_waitcnt lgkmcnt(4)\n"
        "ds_read_b128 a[156:159], v241 offset:27648\n"
        "s_add_u32 m0, s80, 0x0\n"
        "v_mfma_f32_16x16x32_bf16 v[32:35], a[100:103], v[4:7], v[32:35]\n"
        "ds_read_b128 a[124:127], v240 offset:7168\n"
        "global_load_lds_dwordx4 v254, s[68:69] offset:0 sc1\n"
        "s_xor_b32 s79, s79, 0x8000\n"
        "v_mfma_f32_16x16x32_bf16 v[32:35], a[104:107], v[8:11], v[32:35]\n"
        "v_exp_f32 v48, v48\n"
        "v_mfma_f32_16x16x32_bf16 v[32:35], a[108:111], v[12:15], v[32:35]\n"
        "v_exp_f32 v49, v49\n"
        "v_mfma_f32_16x16x32_bf16 v[36:39], a[108:111], v[76:79], v[228:231]\n"
        "v_exp_f32 v50, v50\n"
        "v_mfma_f32_16x16x32_bf16 v[36:39], a[104:107], v[72:75], v[36:39]\n"
        "v_exp_f32 v51, v51\n"
        "v_mfma_f32_16x16x32_bf16 v[36:39], a[100:103], v[68:71], v[36:39]\n"
        "v_exp_f32 v52, v52\n"
        "v_mfma_f32_16x16x32_bf16 v[36:39], a[96:99], v[64:67], v[36:39]\n"
        "v_exp_f32 v53, v53\n"
        "v_mfma_f32_16x16x32_bf16 v[40:43], a[96:99], v[96:99], v[232:235]\n"
        "v_exp_f32 v55, v55\n"
        "v_mfma_f32_16x16x32_bf16 v[40:43], a[100:103], v[100:103], v[40:43]\n"
        "v_exp_f32 v54, v54\n"
        "v_mfma_f32_16x16x32_bf16 v[40:43], a[104:107], v[104:107], v[40:43]\n"
        "v_exp_f32 v56, v56\n"
        "v_mfma_f32_16x16x32_bf16 v[40:43], a[108:111], v[108:111], v[40:43]\n"
        "v_exp_f32 v57, v57\n"
        "v_mfma_f32_16x16x32_bf16 v[44:47], a[108:111], v[140:143], v[236:239]\n"
        "ds_read_b128 a[108:111], v240 offset:11264\n"
        "v_cvt_pk_bf16_f32 v82, v48, v49\n"
        "v_mfma_f32_16x16x32_bf16 v[44:47], a[104:107], v[136:139], v[44:47]\n"
        "ds_read_b128 a[104:107], v240 offset:10240\n"
        "global_load_lds_dwordx4 v254, s[68:69] offset:1024 sc1\n"
        "v_mfma_f32_16x16x32_bf16 v[44:47], a[100:103], v[132:135], v[44:47]\n"
        "ds_read_b128 a[100:103], v240 offset:9216\n"
        "v_cvt_pk_bf16_f32 v83, v50, v51\n"
        "v_mfma_f32_16x16x32_bf16 v[44:47], a[96:99], v[128:131], v[44:47]\n"
        "ds_read_b128 a[96:99], v240 offset:8192\n"
        "v_cvt_pk_bf16_f32 v86, v52, v53\n"
        "v_mfma_f32_16x16x32_bf16 v[176:179], a[128:131], v[16:19], v[176:179]\n"
        "v_exp_f32 v58, v58\n"
        "v_mfma_f32_16x16x32_bf16 v[208:211], a[128:131], v[20:23], v[208:211]\n"
        "v_exp_f32 v59, v59\n"
        "v_mfma_f32_16x16x32_bf16 a[16:19], a[128:131], v[24:27], a[16:19]\n"
        "v_exp_f32 v60, v60\n"
        "v_mfma_f32_16x16x32_bf16 a[48:51], a[128:131], v[28:31], a[48:51]\n"
        "ds_read_b128 a[128:131], v241 offset:28672\n"
        "global_load_lds_dwordx4 v254, s[68:69] offset:2048 sc1\n"
        "v_mfma_f32_16x16x32_bf16 a[52:55], a[132:135], v[28:31], a[52:55]\n"
        "v_exp_f32 v61, v61\n"
        "v_mfma_f32_16x16x32_bf16 a[20:23], a[132:135], v[24:27], a[20:23]\n"
        "v_exp_f32 v62, v62\n"
        "v_mfma_f32_16x16x32_bf16 v[212:215], a[132:135], v[20:23], v[212:215]\n"
        "v_exp_f32 v63, v63\n"
        "v_mfma_f32_16x16x32_bf16 v[180:183], a[132:135], v[16:19], v[180:183]\n"
        "ds_read_b128 a[132:135], v241 offset:29696\n"
        "v_cvt_pk_bf16_f32 v87, v54, v55\n"
        "v_mfma_f32_16x16x32_bf16 v[184:187], a[136:139], v[16:19], v[184:187]\n"
        "v_exp_f32 v32, v32\n"
        "v_mfma_f32_16x16x32_bf16 v[216:219], a[136:139], v[20:23], v[216:219]\n"
        "v_exp_f32 v33, v33\n"
        "v_mfma_f32_16x16x32_bf16 a[24:27], a[136:139], v[24:27], a[24:27]\n"
        "v_exp_f32 v34, v34\n"
        "v_mfma_f32_16x16x32_bf16 a[56:59], a[136:139], v[28:31], a[56:59]\n"
        "ds_read_b128 a[136:139], v241 offset:30720\n"
        "v_cvt_pk_bf16_f32 v90, v56, v57\n"
        "v_mfma_f32_16x16x32_bf16 a[60:63], a[140:143], v[28:31], a[60:63]\n"
        "v_exp_f32 v35, v35\n"
        "v_mfma_f32_16x16x32_bf16 a[28:31], a[140:143], v[24:27], a[28:31]\n"
        "v_exp_f32 v36, v36\n"
        "v_mfma_f32_16x16x32_bf16 v[220:223], a[140:143], v[20:23], v[220:223]\n"
        "s_waitcnt lgkmcnt(2)\n"
        "v_exp_f32 v37, v37\n"
        "v_mfma_f32_16x16x32_bf16 v[188:191], a[140:143], v[16:19], v[188:191]\n"
        "ds_read_b128 a[140:143], v241 offset:31744\n"
        "v_cvt_pk_bf16_f32 v91, v58, v59\n"
        "v_mfma_f32_16x16x32_bf16 v[48:51], a[112:115], v[0:3], v[224:227]\n"
        "v_exp_f32 v38, v38\n"
        "v_mfma_f32_16x16x32_bf16 v[48:51], a[116:119], v[4:7], v[48:51]\n"
        "v_exp_f32 v39, v39\n"
        "v_mfma_f32_16x16x32_bf16 v[48:51], a[120:123], v[8:11], v[48:51]\n"
        "v_exp_f32 v40, v40\n"
        "v_mfma_f32_16x16x32_bf16 v[48:51], a[124:127], v[12:15], v[48:51]\n"
        "v_exp_f32 v41, v41\n"
        "v_mfma_f32_16x16x32_bf16 v[52:55], a[124:127], v[76:79], v[228:231]\n"
        "v_exp_f32 v42, v42\n"
        "v_mfma_f32_16x16x32_bf16 v[52:55], a[120:123], v[72:75], v[52:55]\n"
        "v_exp_f32 v43, v43\n"
        "v_mfma_f32_16x16x32_bf16 v[52:55], a[116:119], v[68:71], v[52:55]\n"
        "v_exp_f32 v44, v44\n"
        "v_mfma_f32_16x16x32_bf16 v[52:55], a[112:115], v[64:67], v[52:55]\n"
        "s_waitcnt vmcnt(5) lgkmcnt(0)\n"
        "v_exp_f32 v45, v45\n"
        "v_mfma_f32_16x16x32_bf16 v[56:59], a[112:115], v[96:99], v[232:235]\n"
        "s_barrier\n"
        "v_mfma_f32_16x16x32_bf16 v[56:59], a[116:119], v[100:103], v[56:59]\n"
        "v_exp_f32 v46, v46\n"
        "v_mfma_f32_16x16x32_bf16 v[56:59], a[120:123], v[104:107], v[56:59]\n"
        "v_exp_f32 v47, v47\n"
        "v_mfma_f32_16x16x32_bf16 v[56:59], a[124:127], v[108:111], v[56:59]\n"
        "v_exp_f32 v48, v48\n"
        "v_smfmac_f32_16x16x64_bf16 a[64:67], a[72:75], v[80:87], v154\n"
        "global_load_lds_dwordx4 v254, s[68:69] offset:3072 sc1\n"
        "s_add_u32 m0, s81, 0x0\n"
        "v_cvt_pk_bf16_f32 v94, v60, v61\n"
        "v_mfma_f32_16x16x32_bf16 v[160:163], a[144:147], v[80:83], v[160:163]\n"
        "s_add_u32 s82, s82, 1\n"
        "v_exp_f32 v49, v49\n"
        "v_mfma_f32_16x16x32_bf16 v[192:195], a[144:147], v[84:87], v[192:195]\n"
        "s_xor_b32 s78, s78, 0x8000\n"
        "v_exp_f32 v50, v50\n"
        "v_mfma_f32_16x16x32_bf16 a[0:3], a[144:147], v[88:91], a[0:3]\n"
        "s_add_u32 s81, s78, 4096\n"
        "v_exp_f32 v51, v51\n"
        "v_mfma_f32_16x16x32_bf16 a[4:7], a[148:151], v[88:91], a[4:7]\n"
        "s_mov_b32 s80, s78\n"
        "v_exp_f32 v52, v52\n"
        "v_mfma_f32_16x16x32_bf16 v[196:199], a[148:151], v[84:87], v[196:199]\n"
        "v_exp_f32 v53, v53\n"
        "v_mfma_f32_16x16x32_bf16 v[164:167], a[148:151], v[80:83], v[164:167]\n"
        "v_exp_f32 v54, v54\n"
        "v_mfma_f32_16x16x32_bf16 v[168:171], a[152:155], v[80:83], v[168:171]\n"
        "v_exp_f32 v55, v55\n"
        "v_mfma_f32_16x16x32_bf16 v[200:203], a[152:155], v[84:87], v[200:203]\n"
        "v_exp_f32 v56, v56\n"
        "v_mfma_f32_16x16x32_bf16 a[8:11], a[152:155], v[88:91], a[8:11]\n"
        "v_exp_f32 v57, v57\n"
        "v_mfma_f32_16x16x32_bf16 a[12:15], a[156:159], v[88:91], a[12:15]\n"
        "v_exp_f32 v58, v58\n"
        "v_mfma_f32_16x16x32_bf16 v[204:207], a[156:159], v[84:87], v[204:207]\n"
        "v_exp_f32 v59, v59\n"
        "v_mfma_f32_16x16x32_bf16 v[172:175], a[156:159], v[80:83], v[172:175]\n"
        "global_load_lds_dwordx4 v254, s[74:75] offset:0 sc1\n"
        "v_cvt_pk_bf16_f32 v95, v62, v63\n"
        "v_mfma_f32_16x16x32_bf16 v[60:63], a[124:127], v[140:143], v[236:239]\n"
        "ds_read_b128 a[124:127], v240 offset:15360\n"
        "v_cvt_pk_bf16_f32 v16, v32, v33\n"
        "v_mfma_f32_16x16x32_bf16 v[60:63], a[120:123], v[136:139], v[60:63]\n"
        "ds_read_b128 a[120:123], v240 offset:14336\n"
        "v_cvt_pk_bf16_f32 v17, v34, v35\n"
        "v_mfma_f32_16x16x32_bf16 v[60:63], a[116:119], v[132:135], v[60:63]\n"
        "ds_read_b128 a[116:119], v240 offset:13312\n"
        "v_cvt_pk_bf16_f32 v20, v36, v37\n"
        "v_mfma_f32_16x16x32_bf16 v[60:63], a[112:115], v[128:131], v[60:63]\n"
        "ds_read_b128 a[112:115], v240 offset:12288\n"
        "global_load_lds_dwordx4 v254, s[74:75] offset:1024 sc1\n"
        "v_smfmac_f32_16x16x64_bf16 a[68:71], a[72:75], v[88:95], v154\n"
        "s_add_u32 s76, s66, s90\n"
        "s_addc_u32 s77, s67, 0\n"
        "v_cvt_pk_bf16_f32 v21, v38, v39\n"
        "v_mfma_f32_16x16x32_bf16 a[32:35], a[144:147], v[92:95], a[32:35]\n"
        "s_cmp_ge_u32 s82, s44\n"
        "s_addc_u32 s90, s82, 0\n"
        "v_cvt_pk_bf16_f32 v24, v40, v41\n"
        "v_mfma_f32_16x16x32_bf16 a[36:39], a[148:151], v[92:95], a[36:39]\n"
        "s_lshl_b32 s90, s90, 15\n"
        "v_exp_f32 v60, v60\n"
        "v_mfma_f32_16x16x32_bf16 a[40:43], a[152:155], v[92:95], a[40:43]\n"
        "v_exp_f32 v61, v61\n"
        "v_mfma_f32_16x16x32_bf16 a[44:47], a[156:159], v[92:95], a[44:47]\n"
        "v_exp_f32 v62, v62\n"
        "v_mfma_f32_16x16x32_bf16 v[32:35], a[96:99], v[0:3], v[224:227]\n"
        "v_exp_f32 v63, v63\n"
        "v_mfma_f32_16x16x32_bf16 v[32:35], a[100:103], v[4:7], v[32:35]\n"
        "global_load_lds_dwordx4 v254, s[74:75] offset:2048 sc1\n"
        "v_xor_b32 v241, 0x8000, v241\n"
        "v_mfma_f32_16x16x32_bf16 v[32:35], a[104:107], v[8:11], v[32:35]\n"
        "ds_read_b128 a[144:147], v241 offset:0\n"
        "v_cvt_pk_bf16_f32 v25, v42, v43\n"
        "v_mfma_f32_16x16x32_bf16 v[32:35], a[108:111], v[12:15], v[32:35]\n"
        "ds_read_b128 a[148:151], v241 offset:1024\n"
        "v_cvt_pk_bf16_f32 v28, v44, v45\n"
        "v_mfma_f32_16x16x32_bf16 v[36:39], a[108:111], v[76:79], v[228:231]\n"
        "ds_read_b128 a[152:155], v241 offset:2048\n"
        "v_cvt_pk_bf16_f32 v29, v46, v47\n"
        "v_mfma_f32_16x16x32_bf16 v[36:39], a[104:107], v[72:75], v[36:39]\n"
        "ds_read_b128 a[156:159], v241 offset:3072\n"
        "global_load_lds_dwordx4 v254, s[74:75] offset:3072 sc1\n"
        "s_add_u32 m0, s79, 0x0\n"
        "v_mfma_f32_16x16x32_bf16 v[36:39], a[100:103], v[68:71], v[36:39]\n"
        "v_exp_f32 v32, v32\n"
        "v_mfma_f32_16x16x32_bf16 v[36:39], a[96:99], v[64:67], v[36:39]\n"
        "v_exp_f32 v33, v33\n"
        "v_mfma_f32_16x16x32_bf16 v[40:43], a[96:99], v[96:99], v[232:235]\n"
        "v_exp_f32 v34, v34\n"
        "v_mfma_f32_16x16x32_bf16 v[40:43], a[100:103], v[100:103], v[40:43]\n"
        "v_exp_f32 v35, v35\n"
        "v_mfma_f32_16x16x32_bf16 v[40:43], a[104:107], v[104:107], v[40:43]\n"
        "global_load_lds_dwordx4 v144, s[76:77] offset:0 sc1\n"
        "v_cvt_pk_bf16_f32 v18, v48, v49\n"
        "v_mfma_f32_16x16x32_bf16 v[40:43], a[108:111], v[108:111], v[40:43]\n"
        "v_exp_f32 v36, v36\n"
        "v_mfma_f32_16x16x32_bf16 v[44:47], a[108:111], v[140:143], v[236:239]\n"
        "ds_read_b128 a[108:111], v240 offset:19456\n"
        "v_cvt_pk_bf16_f32 v19, v50, v51\n"
        "v_mfma_f32_16x16x32_bf16 v[44:47], a[104:107], v[136:139], v[44:47]\n"
        "ds_read_b128 a[104:107], v240 offset:18432\n"
        "v_cvt_pk_bf16_f32 v22, v52, v53\n"
        "v_mfma_f32_16x16x32_bf16 v[44:47], a[100:103], v[132:135], v[44:47]\n"
        "ds_read_b128 a[100:103], v240 offset:17408\n"
        "global_load_lds_dwordx4 v145, s[76:77] offset:1024 sc1\n"
        "v_mfma_f32_16x16x32_bf16 v[44:47], a[96:99], v[128:131], v[44:47]\n"
        "ds_read_b128 a[96:99], v240 offset:16384\n"
        "v_cvt_pk_bf16_f32 v23, v54, v55\n"
        "v_mfma_f32_16x16x32_bf16 v[176:179], a[128:131], v[80:83], v[176:179]\n"
        "v_exp_f32 v37, v37\n"
        "v_mfma_f32_16x16x32_bf16 v[208:211], a[128:131], v[84:87], v[208:211]\n"
        "v_exp_f32 v38, v38\n"
        "v_mfma_f32_16x16x32_bf16 a[16:19], a[128:131], v[88:91], a[16:19]\n"
        "v_exp_f32 v39, v39\n"
        "v_mfma_f32_16x16x32_bf16 a[48:51], a[128:131], v[92:95], a[48:51]\n"
        "ds_read_b128 a[128:131], v241 offset:4096\n"
        "global_load_lds_dwordx4 v146, s[76:77] offset:2048 sc1\n"
        "v_mfma_f32_16x16x32_bf16 a[52:55], a[132:135], v[92:95], a[52:55]\n"
        "v_exp_f32 v40, v40\n"
        "v_mfma_f32_16x16x32_bf16 a[20:23], a[132:135], v[88:91], a[20:23]\n"
        "v_exp_f32 v41, v41\n"
        "v_mfma_f32_16x16x32_bf16 v[212:215], a[132:135], v[84:87], v[212:215]\n"
        "v_exp_f32 v42, v42\n"
        "v_mfma_f32_16x16x32_bf16 v[180:183], a[132:135], v[80:83], v[180:183]\n"
        "ds_read_b128 a[132:135], v241 offset:5120\n"
        "global_load_lds_dwordx4 v147, s[76:77] offset:3072 sc1\n"
        "s_add_u32 m0, s79, s28\n"
        "v_mfma_f32_16x16x32_bf16 v[184:187], a[136:139], v[80:83], v[184:187]\n"
        "v_exp_f32 v43, v43\n"
        "v_mfma_f32_16x16x32_bf16 v[216:219], a[136:139], v[84:87], v[216:219]\n"
        "v_exp_f32 v44, v44\n"
        "v_mfma_f32_16x16x32_bf16 a[24:27], a[136:139], v[88:91], a[24:27]\n"
        "v_exp_f32 v45, v45\n"
        "v_mfma_f32_16x16x32_bf16 a[56:59], a[136:139], v[92:95], a[56:59]\n"
        "ds_read_b128 a[136:139], v241 offset:6144\n"
        "s_add_u32 s76, s76, s25\n"
        "s_addc_u32 s77, s77, 0\n"
        "v_mfma_f32_16x16x32_bf16 a[60:63], a[140:143], v[92:95], a[60:63]\n"
        "s_waitcnt lgkmcnt(2)\n"
        "v_exp_f32 v46, v46\n"
        "v_mfma_f32_16x16x32_bf16 a[28:31], a[140:143], v[88:91], a[28:31]\n"
        "v_exp_f32 v47, v47\n"
        "v_mfma_f32_16x16x32_bf16 v[220:223], a[140:143], v[84:87], v[220:223]\n"
        "global_load_lds_dwordx4 v144, s[76:77] offset:0 sc1\n"
        "v_cvt_pk_bf16_f32 v26, v56, v57\n"
        "v_mfma_f32_16x16x32_bf16 v[188:191], a[140:143], v[80:83], v[188:191]\n"
        "ds_read_b128 a[140:143], v241 offset:7168\n"
        "v_cvt_pk_bf16_f32 v27, v58, v59\n"
        "v_mfma_f32_16x16x32_bf16 v[48:51], a[112:115], v[0:3], v[224:227]\n"
        "s_add_u32 s68, s64, s90\n"
        "s_addc_u32 s69, s65, 0\n"
        "v_cvt_pk_bf16_f32 v30, v60, v61\n"
        "v_mfma_f32_16x16x32_bf16 v[48:51], a[116:119], v[4:7], v[48:51]\n"
        "s_add_u32 s74, s68, 0x1000\n"
        "s_addc_u32 s75, s69, 0\n"
        "v_cvt_pk_bf16_f32 v31, v62, v63\n"
        "v_mfma_f32_16x16x32_bf16 v[48:51], a[120:123], v[8:11], v[48:51]\n"
        "global_load_lds_dwordx4 v145, s[76:77] offset:1024 sc1\n"
        "v_cvt_pk_bf16_f32 v80, v32, v33\n"
        "v_mfma_f32_16x16x32_bf16 v[48:51], a[124:127], v[12:15], v[48:51]\n"
        "v_cvt_pk_bf16_f32 v81, v34, v35\n"
        "v_cvt_pk_bf16_f32 v84, v36, v37\n"
        "v_mfma_f32_16x16x32_bf16 v[52:55], a[124:127], v[76:79], v[228:231]\n"
        "v_cvt_pk_bf16_f32 v85, v38, v39\n"
        "v_cvt_pk_bf16_f32 v88, v40, v41\n"
        "v_mfma_f32_16x16x32_bf16 v[52:55], a[120:123], v[72:75], v[52:55]\n"
        "v_cvt_pk_bf16_f32 v89, v42, v43\n"
        "v_cvt_pk_bf16_f32 v92, v44, v45\n"
        "v_mfma_f32_16x16x32_bf16 v[52:55], a[116:119], v[68:71], v[52:55]\n"
        "v_exp_f32 v48, v48\n"
        "v_mfma_f32_16x16x32_bf16 v[52:55], a[112:115], v[64:67], v[52:55]\n"
        "v_exp_f32 v49, v49\n"
        "v_mfma_f32_16x16x32_bf16 v[56:59], a[112:115], v[96:99], v[232:235]\n"
        "v_exp_f32 v50, v50\n"
        "v_mfma_f32_16x16x32_bf16 v[56:59], a[116:119], v[100:103], v[56:59]\n"
        "v_exp_f32 v51, v51\n"
        "v_mfma_f32_16x16x32_bf16 v[56:59], a[120:123], v[104:107], v[56:59]\n"
        "global_load_lds_dwordx4 v146, s[76:77] offset:2048 sc1\n"
        "v_cvt_pk_bf16_f32 v93, v46, v47\n"
        "v_mfma_f32_16x16x32_bf16 v[56:59], a[124:127], v[108:111], v[56:59]\n"
        "v_exp_f32 v52, v52\n"
        "v_mfma_f32_16x16x32_bf16 v[60:63], a[124:127], v[140:143], v[236:239]\n"
        "ds_read_b128 a[124:127], v240 offset:23552\n"
        "v_cvt_pk_bf16_f32 v82, v48, v49\n"
        "v_mfma_f32_16x16x32_bf16 v[60:63], a[120:123], v[136:139], v[60:63]\n"
        "ds_read_b128 a[120:123], v240 offset:22528\n"
        "v_cvt_pk_bf16_f32 v83, v50, v51\n"
        "v_mfma_f32_16x16x32_bf16 v[60:63], a[116:119], v[132:135], v[60:63]\n"
        "ds_read_b128 a[116:119], v240 offset:21504\n"
        "global_load_lds_dwordx4 v147, s[76:77] offset:3072 sc1\n"
        "v_mfma_f32_16x16x32_bf16 v[60:63], a[112:115], v[128:131], v[60:63]\n"
        "ds_read_b128 a[112:115], v240 offset:20480\n"
        "v_smfmac_f32_16x16x64_bf16 a[64:67], a[72:75], v[16:23], v154\n"
        "v_smfmac_f32_16x16x64_bf16 a[68:71], a[72:75], v[24:31], v154\n"
        "v_mfma_f32_16x16x32_bf16 v[160:163], a[144:147], v[16:19], v[160:163]\n"
        "v_exp_f32 v53, v53\n"
        "v_mfma_f32_16x16x32_bf16 v[192:195], a[144:147], v[20:23], v[192:195]\n"
        "v_exp_f32 v54, v54\n"
        "v_mfma_f32_16x16x32_bf16 a[0:3], a[144:147], v[24:27], a[0:3]\n"
        "v_exp_f32 v55, v55\n"
        "v_mfma_f32_16x16x32_bf16 a[32:35], a[144:147], v[28:31], a[32:35]\n"
        "ds_read_b128 a[144:147], v241 offset:8192\n"
        "global_load_dword v244, v245, s[26:27] offset:0\n"
        "v_mfma_f32_16x16x32_bf16 a[36:39], a[148:151], v[28:31], a[36:39]\n"
        "v_exp_f32 v56, v56\n"
        "v_mfma_f32_16x16x32_bf16 a[4:7], a[148:151], v[24:27], a[4:7]\n"
        "v_exp_f32 v57, v57\n"
        "v_mfma_f32_16x16x32_bf16 v[196:199], a[148:151], v[20:23], v[196:199]\n"
        "v_exp_f32 v58, v58\n"
        "v_mfma_f32_16x16x32_bf16 v[164:167], a[148:151], v[16:19], v[164:167]\n"
        "ds_read_b128 a[148:151], v241 offset:9216\n"
        "s_add_u32 s26, s26, 0x20000\n"
        "s_addc_u32 s27, s27, 0\n"
        "v_mfma_f32_16x16x32_bf16 v[168:171], a[152:155], v[16:19], v[168:171]\n"
        "v_exp_f32 v59, v59\n"
        "v_mfma_f32_16x16x32_bf16 v[200:203], a[152:155], v[20:23], v[200:203]\n"
        "v_exp_f32 v60, v60\n"
        "v_mfma_f32_16x16x32_bf16 a[8:11], a[152:155], v[24:27], a[8:11]\n"
        "v_exp_f32 v61, v61\n"
        "v_mfma_f32_16x16x32_bf16 a[40:43], a[152:155], v[28:31], a[40:43]\n"
        "ds_read_b128 a[152:155], v241 offset:10240\n"
        "v_cvt_pk_bf16_f32 v86, v52, v53\n"
        "v_mfma_f32_16x16x32_bf16 a[44:47], a[156:159], v[28:31], a[44:47]\n"
        "v_exp_f32 v62, v62\n"
        "v_mfma_f32_16x16x32_bf16 a[12:15], a[156:159], v[24:27], a[12:15]\n"
        "v_exp_f32 v63, v63\n"
        "v_mfma_f32_16x16x32_bf16 v[204:207], a[156:159], v[20:23], v[204:207]\n"
        "v_cvt_pk_bf16_f32 v87, v54, v55\n"
        "v_cvt_pk_bf16_f32 v90, v56, v57\n"
        "v_mfma_f32_16x16x32_bf16 v[172:175], a[156:159], v[16:19], v[172:175]\n"
        "ds_read_b128 a[156:159], v241 offset:11264\n"
        "v_cvt_pk_bf16_f32 v91, v58, v59\n"
        "v_mfma_f32_16x16x32_bf16 v[32:35], a[96:99], v[0:3], v[224:227]\n"
        "v_cvt_pk_bf16_f32 v94, v60, v61\n"
        "v_cvt_pk_bf16_f32 v95, v62, v63\n"
        "v_mfma_f32_16x16x32_bf16 v[32:35], a[100:103], v[4:7], v[32:35]\n"
        "v_mfma_f32_16x16x32_bf16 v[32:35], a[104:107], v[8:11], v[32:35]\n"
        "v_mfma_f32_16x16x32_bf16 v[32:35], a[108:111], v[12:15], v[32:35]\n"
        "v_mfma_f32_16x16x32_bf16 v[36:39], a[108:111], v[76:79], v[228:231]\n"
        "v_mfma_f32_16x16x32_bf16 v[36:39], a[104:107], v[72:75], v[36:39]\n"
        "v_mfma_f32_16x16x32_bf16 v[36:39], a[100:103], v[68:71], v[36:39]\n"
        "v_mfma_f32_16x16x32_bf16 v[36:39], a[96:99], v[64:67], v[36:39]\n"
        "v_mfma_f32_16x16x32_bf16 v[40:43], a[96:99], v[96:99], v[232:235]\n"
        "v_mfma_f32_16x16x32_bf16 v[40:43], a[100:103], v[100:103], v[40:43]\n"
        "v_mfma_f32_16x16x32_bf16 v[40:43], a[104:107], v[104:107], v[40:43]\n"
        "v_mfma_f32_16x16x32_bf16 v[40:43], a[108:111], v[108:111], v[40:43]\n"
        "v_exp_f32 v32, v32\n"
        "v_mfma_f32_16x16x32_bf16 v[44:47], a[108:111], v[140:143], v[236:239]\n"
        "v_exp_f32 v33, v33\n"
        "v_mfma_f32_16x16x32_bf16 v[44:47], a[104:107], v[136:139], v[44:47]\n"
        "ds_read_b128 a[104:107], v240 offset:26624\n"
        "v_mfma_f32_16x16x32_bf16 v[44:47], a[100:103], v[132:135], v[44:47]\n"
        "ds_read_b128 a[100:103], v240 offset:25600\n"
        "v_mfma_f32_16x16x32_bf16 v[44:47], a[96:99], v[128:131], v[44:47]\n"
        "ds_read_b128 a[96:99], v240 offset:24576\n"
        "v_mfma_f32_16x16x32_bf16 v[176:179], a[128:131], v[16:19], v[176:179]\n"
        "s_waitcnt lgkmcnt(3)\n"
        "v_exp_f32 v34, v34\n"
        "v_mfma_f32_16x16x32_bf16 v[208:211], a[128:131], v[20:23], v[208:211]\n"
        "v_exp_f32 v35, v35\n"
        "v_mfma_f32_16x16x32_bf16 a[16:19], a[128:131], v[24:27], a[16:19]\n"
        "ds_read_b128 a[108:111], v240 offset:27648\n"
        "v_mfma_f32_16x16x32_bf16 a[48:51], a[128:131], v[28:31], a[48:51]\n"
        "ds_read_b128 a[128:131], v241 offset:12288\n"
        "v_mfma_f32_16x16x32_bf16 a[52:55], a[132:135], v[28:31], a[52:55]\n"
        "v_exp_f32 v36, v36\n"
        "v_mfma_f32_16x16x32_bf16 a[20:23], a[132:135], v[24:27], a[20:23]\n"
        "v_exp_f32 v37, v37\n"
        "v_mfma_f32_16x16x32_bf16 v[212:215], a[132:135], v[20:23], v[212:215]\n"
        "v_exp_f32 v38, v38\n"
        "v_mfma_f32_16x16x32_bf16 v[180:183], a[132:135], v[16:19], v[180:183]\n"
        "ds_read_b128 a[132:135], v241 offset:13312\n"
        "v_mfma_f32_16x16x32_bf16 v[184:187], a[136:139], v[16:19], v[184:187]\n"
        "v_exp_f32 v39, v39\n"
        "v_mfma_f32_16x16x32_bf16 v[216:219], a[136:139], v[20:23], v[216:219]\n"
        "v_exp_f32 v40, v40\n"
        "v_mfma_f32_16x16x32_bf16 a[24:27], a[136:139], v[24:27], a[24:27]\n"
        "v_exp_f32 v41, v41\n"
        "v_mfma_f32_16x16x32_bf16 a[56:59], a[136:139], v[28:31], a[56:59]\n"
        "ds_read_b128 a[136:139], v241 offset:14336\n"
        "v_mfma_f32_16x16x32_bf16 a[60:63], a[140:143], v[28:31], a[60:63]\n"
        "v_exp_f32 v42, v42\n"
        "v_mfma_f32_16x16x32_bf16 a[28:31], a[140:143], v[24:27], a[28:31]\n"
        "v_exp_f32 v43, v43\n"
        "v_mfma_f32_16x16x32_bf16 v[220:223], a[140:143], v[20:23], v[220:223]\n"
        "v_exp_f32 v44, v44\n"
        "v_mfma_f32_16x16x32_bf16 v[188:191], a[140:143], v[16:19], v[188:191]\n"
        "ds_read_b128 a[140:143], v241 offset:15360\n"
        "global_load_dword v244, v245, s[26:27] offset:0\n"
        "v_mfma_f32_16x16x32_bf16 v[48:51], a[112:115], v[0:3], v[224:227]\n"
        "v_exp_f32 v45, v45\n"
        "v_mfma_f32_16x16x32_bf16 v[48:51], a[116:119], v[4:7], v[48:51]\n"
        "v_exp_f32 v46, v46\n"
        "v_mfma_f32_16x16x32_bf16 v[48:51], a[120:123], v[8:11], v[48:51]\n"
        "v_exp_f32 v47, v47\n"
        "v_mfma_f32_16x16x32_bf16 v[48:51], a[124:127], v[12:15], v[48:51]\n"
        "s_add_u32 s26, s26, 0x20000\n"
        "s_addc_u32 s27, s27, 0\n"
        "v_cvt_pk_bf16_f32 v16, v32, v33\n"
        "v_mfma_f32_16x16x32_bf16 v[52:55], a[124:127], v[76:79], v[228:231]\n"
        "v_cvt_pk_bf16_f32 v17, v34, v35\n"
        "v_cvt_pk_bf16_f32 v20, v36, v37\n"
        "v_mfma_f32_16x16x32_bf16 v[52:55], a[120:123], v[72:75], v[52:55]\n"
        "v_cvt_pk_bf16_f32 v21, v38, v39\n"
        "v_cvt_pk_bf16_f32 v24, v40, v41\n"
        "v_mfma_f32_16x16x32_bf16 v[52:55], a[116:119], v[68:71], v[52:55]\n"
        "v_exp_f32 v48, v48\n"
        "v_mfma_f32_16x16x32_bf16 v[52:55], a[112:115], v[64:67], v[52:55]\n"
        "v_exp_f32 v49, v49\n"
        "v_mfma_f32_16x16x32_bf16 v[56:59], a[112:115], v[96:99], v[232:235]\n"
        "v_exp_f32 v50, v50\n"
        "v_mfma_f32_16x16x32_bf16 v[56:59], a[116:119], v[100:103], v[56:59]\n"
        "v_exp_f32 v51, v51\n"
        "v_mfma_f32_16x16x32_bf16 v[56:59], a[120:123], v[104:107], v[56:59]\n"
        "v_cvt_pk_bf16_f32 v25, v42, v43\n"
        "v_cvt_pk_bf16_f32 v28, v44, v45\n"
        "v_mfma_f32_16x16x32_bf16 v[56:59], a[124:127], v[108:111], v[56:59]\n"
        "v_exp_f32 v52, v52\n"
        "v_mfma_f32_16x16x32_bf16 v[60:63], a[124:127], v[140:143], v[236:239]\n"
        "ds_read_b128 a[124:127], v240 offset:31744\n"
        "v_cvt_pk_bf16_f32 v29, v46, v47\n"
        "v_mfma_f32_16x16x32_bf16 v[60:63], a[120:123], v[136:139], v[60:63]\n"
        "ds_read_b128 a[120:123], v240 offset:30720\n"
        "v_cvt_pk_bf16_f32 v18, v48, v49\n"
        "v_mfma_f32_16x16x32_bf16 v[60:63], a[116:119], v[132:135], v[60:63]\n"
        "ds_read_b128 a[116:119], v240 offset:29696\n"
        "v_cvt_pk_bf16_f32 v19, v50, v51\n"
        "v_mfma_f32_16x16x32_bf16 v[60:63], a[112:115], v[128:131], v[60:63]\n"
        "ds_read_b128 a[112:115], v240 offset:28672\n"
        "v_xor_b32 v240, 0x8000, v240\n"
        "v_smfmac_f32_16x16x64_bf16 a[64:67], a[72:75], v[80:87], v154\n"
        "v_smfmac_f32_16x16x64_bf16 a[68:71], a[72:75], v[88:95], v154\n"
        "v_mfma_f32_16x16x32_bf16 v[160:163], a[144:147], v[80:83], v[160:163]\n"
        "v_exp_f32 v53, v53\n"
        "v_mfma_f32_16x16x32_bf16 v[192:195], a[144:147], v[84:87], v[192:195]\n"
        "v_exp_f32 v54, v54\n"
        "v_mfma_f32_16x16x32_bf16 a[0:3], a[144:147], v[88:91], a[0:3]\n"
        "v_exp_f32 v55, v55\n"
        "v_mfma_f32_16x16x32_bf16 a[32:35], a[144:147], v[92:95], a[32:35]\n"
        "ds_read_b128 a[144:147], v241 offset:16384\n"
        "v_cvt_pk_bf16_f32 v22, v52, v53\n"
        "v_mfma_f32_16x16x32_bf16 a[36:39], a[148:151], v[92:95], a[36:39]\n"
        "v_exp_f32 v56, v56\n"
        "v_mfma_f32_16x16x32_bf16 a[4:7], a[148:151], v[88:91], a[4:7]\n"
        "s_waitcnt vmcnt(10) lgkmcnt(1)\n"
        "v_exp_f32 v57, v57\n"
        "v_mfma_f32_16x16x32_bf16 v[196:199], a[148:151], v[84:87], v[196:199]\n"
        "s_barrier\n"
        "v_mfma_f32_16x16x32_bf16 v[164:167], a[148:151], v[80:83], v[164:167]\n"
        "ds_read_b128 a[148:151], v241 offset:17408\n"
        "v_cvt_pk_bf16_f32 v23, v54, v55\n"
        "v_mfma_f32_16x16x32_bf16 v[168:171], a[152:155], v[80:83], v[168:171]\n"
        "v_exp_f32 v58, v58\n"
        "v_mfma_f32_16x16x32_bf16 v[200:203], a[152:155], v[84:87], v[200:203]\n"
        "v_exp_f32 v59, v59\n"
        "v_mfma_f32_16x16x32_bf16 a[8:11], a[152:155], v[88:91], a[8:11]\n"
        "v_exp_f32 v60, v60\n"
        "v_mfma_f32_16x16x32_bf16 a[40:43], a[152:155], v[92:95], a[40:43]\n"
        "ds_read_b128 a[152:155], v241 offset:18432\n"
        "v_cvt_pk_bf16_f32 v26, v56, v57\n"
        "v_mfma_f32_16x16x32_bf16 a[44:47], a[156:159], v[92:95], a[44:47]\n"
        "v_exp_f32 v61, v61\n"
        "v_mfma_f32_16x16x32_bf16 a[12:15], a[156:159], v[88:91], a[12:15]\n"
        "v_exp_f32 v62, v62\n"
        "v_mfma_f32_16x16x32_bf16 v[204:207], a[156:159], v[84:87], v[204:207]\n"
        "v_exp_f32 v63, v63\n"
        "v_mfma_f32_16x16x32_bf16 v[172:175], a[156:159], v[80:83], v[172:175]\n"
        "ds_read_b128 a[156:159], v241 offset:19456\n"
        "v_cvt_pk_bf16_f32 v27, v58, v59\n"
        "v_mfma_f32_16x16x32_bf16 v[32:35], a[96:99], v[0:3], v[224:227]\n"
        "v_cvt_pk_bf16_f32 v30, v60, v61\n"
        "v_cvt_pk_bf16_f32 v31, v62, v63\n"
        "v_mfma_f32_16x16x32_bf16 v[32:35], a[100:103], v[4:7], v[32:35]\n"
        "v_mfma_f32_16x16x32_bf16 v[32:35], a[104:107], v[8:11], v[32:35]\n"
        "v_mfma_f32_16x16x32_bf16 v[32:35], a[108:111], v[12:15], v[32:35]\n"
        "v_mfma_f32_16x16x32_bf16 v[36:39], a[108:111], v[76:79], v[228:231]\n"
        "v_mfma_f32_16x16x32_bf16 v[36:39], a[104:107], v[72:75], v[36:39]\n"
        "v_mfma_f32_16x16x32_bf16 v[36:39], a[100:103], v[68:71], v[36:39]\n"
        "v_mfma_f32_16x16x32_bf16 v[36:39], a[96:99], v[64:67], v[36:39]\n"
        "v_mfma_f32_16x16x32_bf16 v[40:43], a[96:99], v[96:99], v[232:235]\n"
        "v_mfma_f32_16x16x32_bf16 v[40:43], a[100:103], v[100:103], v[40:43]\n"
        "v_mfma_f32_16x16x32_bf16 v[40:43], a[104:107], v[104:107], v[40:43]\n"
        "v_mfma_f32_16x16x32_bf16 v[40:43], a[108:111], v[108:111], v[40:43]\n"
        "v_exp_f32 v32, v32\n"
        "v_mfma_f32_16x16x32_bf16 v[44:47], a[108:111], v[140:143], v[236:239]\n"
        "v_exp_f32 v33, v33\n"
        "v_mfma_f32_16x16x32_bf16 v[44:47], a[104:107], v[136:139], v[44:47]\n"
        "ds_read_b128 a[104:107], v240 offset:2048\n"
        "v_mfma_f32_16x16x32_bf16 v[44:47], a[100:103], v[132:135], v[44:47]\n"
        "ds_read_b128 a[100:103], v240 offset:1024\n"
        "v_mfma_f32_16x16x32_bf16 v[44:47], a[96:99], v[128:131], v[44:47]\n"
        "ds_read_b128 a[96:99], v240 offset:0\n"
        "v_mfma_f32_16x16x32_bf16 v[176:179], a[128:131], v[80:83], v[176:179]\n"
        "v_exp_f32 v34, v34\n"
        "v_mfma_f32_16x16x32_bf16 v[208:211], a[128:131], v[84:87], v[208:211]\n"
        "v_exp_f32 v35, v35\n"
        "v_mfma_f32_16x16x32_bf16 a[16:19], a[128:131], v[88:91], a[16:19]\n"
        "ds_read_b128 a[108:111], v240 offset:3072\n"
        "v_mfma_f32_16x16x32_bf16 a[48:51], a[128:131], v[92:95], a[48:51]\n"
        "ds_read_b128 a[128:131], v241 offset:20480\n"
        "v_mfma_f32_16x16x32_bf16 a[52:55], a[132:135], v[92:95], a[52:55]\n"
        "v_exp_f32 v36, v36\n"
        "v_mfma_f32_16x16x32_bf16 a[20:23], a[132:135], v[88:91], a[20:23]\n"
        "v_exp_f32 v37, v37\n"
        "v_mfma_f32_16x16x32_bf16 v[212:215], a[132:135], v[84:87], v[212:215]\n"
        "v_exp_f32 v38, v38\n"
        "v_mfma_f32_16x16x32_bf16 v[180:183], a[132:135], v[80:83], v[180:183]\n"
        "ds_read_b128 a[132:135], v241 offset:21504\n"
        "v_mfma_f32_16x16x32_bf16 v[184:187], a[136:139], v[80:83], v[184:187]\n"
        "v_exp_f32 v39, v39\n"
        "v_mfma_f32_16x16x32_bf16 v[216:219], a[136:139], v[84:87], v[216:219]\n"
        "v_exp_f32 v40, v40\n"
        "v_mfma_f32_16x16x32_bf16 a[24:27], a[136:139], v[88:91], a[24:27]\n"
        "v_exp_f32 v41, v41\n"
        "v_mfma_f32_16x16x32_bf16 a[56:59], a[136:139], v[92:95], a[56:59]\n"
        "ds_read_b128 a[136:139], v241 offset:22528\n"
        "s_waitcnt lgkmcnt(2)\n"
        "v_mfma_f32_16x16x32_bf16 a[60:63], a[140:143], v[92:95], a[60:63]\n"
        "v_exp_f32 v42, v42\n"
        "v_mfma_f32_16x16x32_bf16 a[28:31], a[140:143], v[88:91], a[28:31]\n"
        "v_exp_f32 v43, v43\n"
        "v_mfma_f32_16x16x32_bf16 v[220:223], a[140:143], v[84:87], v[220:223]\n"
        "v_exp_f32 v44, v44\n"
        "v_mfma_f32_16x16x32_bf16 v[188:191], a[140:143], v[80:83], v[188:191]\n"
        "ds_read_b128 a[140:143], v241 offset:23552\n"
        "v_cvt_pk_bf16_f32 v80, v32, v33\n"
        "v_mfma_f32_16x16x32_bf16 v[48:51], a[112:115], v[0:3], v[224:227]\n"
        "v_exp_f32 v45, v45\n"
        "v_mfma_f32_16x16x32_bf16 v[48:51], a[116:119], v[4:7], v[48:51]\n"
        "v_exp_f32 v46, v46\n"
        "v_mfma_f32_16x16x32_bf16 v[48:51], a[120:123], v[8:11], v[48:51]\n"
        "v_exp_f32 v47, v47\n"
        "v_mfma_f32_16x16x32_bf16 v[48:51], a[124:127], v[12:15], v[48:51]\n"
        "v_cvt_pk_bf16_f32 v81, v34, v35\n"
        "v_cvt_pk_bf16_f32 v84, v36, v37\n"
        "v_mfma_f32_16x16x32_bf16 v[52:55], a[124:127], v[76:79], v[228:231]\n"
        "v_cvt_pk_bf16_f32 v85, v38, v39\n"
        "v_cvt_pk_bf16_f32 v88, v40, v41\n"
        "v_mfma_f32_16x16x32_bf16 v[52:55], a[120:123], v[72:75], v[52:55]\n"
        "v_cvt_pk_bf16_f32 v89, v42, v43\n"
        "v_cvt_pk_bf16_f32 v92, v44, v45\n"
        "v_mfma_f32_16x16x32_bf16 v[52:55], a[116:119], v[68:71], v[52:55]\n"
        "v_cvt_pk_bf16_f32 v93, v46, v47\n"
        "v_mfma_f32_16x16x32_bf16 v[52:55], a[112:115], v[64:67], v[52:55]\n"
        "v_mfma_f32_16x16x32_bf16 v[56:59], a[112:115], v[96:99], v[232:235]\n"
        "v_mfma_f32_16x16x32_bf16 v[56:59], a[116:119], v[100:103], v[56:59]\n"
        "v_mfma_f32_16x16x32_bf16 v[56:59], a[120:123], v[104:107], v[56:59]\n"
        "v_mfma_f32_16x16x32_bf16 v[56:59], a[124:127], v[108:111], v[56:59]\n"
        "v_mfma_f32_16x16x32_bf16 v[60:63], a[124:127], v[140:143], v[236:239]\n"
        "v_mfma_f32_16x16x32_bf16 v[60:63], a[120:123], v[136:139], v[60:63]\n"
        "ds_read_b128 a[120:123], v240 offset:6144\n"
        "v_mfma_f32_16x16x32_bf16 v[60:63], a[116:119], v[132:135], v[60:63]\n"
        "ds_read_b128 a[116:119], v240 offset:5120\n"
        "v_mfma_f32_16x16x32_bf16 v[60:63], a[112:115], v[128:131], v[60:63]\n"
        "ds_read_b128 a[112:115], v240 offset:4096\n"
        "v_smfmac_f32_16x16x64_bf16 a[64:67], a[72:75], v[16:23], v154\n"
        "v_smfmac_f32_16x16x64_bf16 a[68:71], a[72:75], v[24:31], v154\n"
        "v_mfma_f32_16x16x32_bf16 v[160:163], a[144:147], v[16:19], v[160:163]\n"
        "v_mfma_f32_16x16x32_bf16 v[192:195], a[144:147], v[20:23], v[192:195]\n"
        "v_mfma_f32_16x16x32_bf16 a[0:3], a[144:147], v[24:27], a[0:3]\n"
        "v_mfma_f32_16x16x32_bf16 a[32:35], a[144:147], v[28:31], a[32:35]\n"
        "ds_read_b128 a[144:147], v241 offset:24576\n"
        "v_mfma_f32_16x16x32_bf16 a[36:39], a[148:151], v[28:31], a[36:39]\n"
        "v_mfma_f32_16x16x32_bf16 a[4:7], a[148:151], v[24:27], a[4:7]\n"
        "v_mfma_f32_16x16x32_bf16 v[196:199], a[148:151], v[20:23], v[196:199]\n"
        "v_mfma_f32_16x16x32_bf16 v[164:167], a[148:151], v[16:19], v[164:167]\n"
        "ds_read_b128 a[148:151], v241 offset:25600\n"
        "v_mfma_f32_16x16x32_bf16 v[168:171], a[152:155], v[16:19], v[168:171]\n"
        "v_mfma_f32_16x16x32_bf16 v[200:203], a[152:155], v[20:23], v[200:203]\n"
        "v_mfma_f32_16x16x32_bf16 a[8:11], a[152:155], v[24:27], a[8:11]\n"
        "v_mfma_f32_16x16x32_bf16 a[40:43], a[152:155], v[28:31], a[40:43]\n"
        "ds_read_b128 a[152:155], v241 offset:26624\n"
        "v_mfma_f32_16x16x32_bf16 a[44:47], a[156:159], v[28:31], a[44:47]\n"
        "v_mfma_f32_16x16x32_bf16 a[12:15], a[156:159], v[24:27], a[12:15]\n"
        "v_mfma_f32_16x16x32_bf16 v[204:207], a[156:159], v[20:23], v[204:207]\n"
        "s_cmp_lg_u32 s82, s24\n"
        "v_mfma_f32_16x16x32_bf16 v[172:175], a[156:159], v[16:19], v[172:175]\n"
        "s_cbranch_scc1 LDU_%=\n"
        "s_sub_u32 s97, s83, s82\n"
        "s_cmp_gt_u32 s97, 1\n"
        "s_cbranch_scc1 LLD_%=\n"
        "s_cmp_eq_u32 s97, 0\n"
        "s_cbranch_scc1 LE3D_%=\n"
        "global_load_dwordx2 v[252:253], v155, s[48:49] sc1\n"
        "v_accvgpr_read_b32 v246, a93\n"
        "global_load_dword v244, v246, s[46:47]\n"
        "s_branch LEWD_%=\n"
        "LE3D_%=:\n"
        "v_cmp_ne_u64 vcc, v[252:253], v[250:251]\n"
        "s_mov_b64 s[68:69], s[92:93]\n"
        "s_mov_b64 s[74:75], s[94:95]\n"
        "LHC0_%=:\n"
        "s_cbranch_execz LHRC_%=\n"
        "s_mov_b32 s80, s50\n"
        "s_mov_b32 s81, s51\n"
        "s_mov_b64 s[66:67], s[22:23]\n"
        "s_mov_b32 s90, s29\n"
        "s_mov_b32 s25, s30\n"
        "s_lshl_b32 s28, s28, 2\n"
        "s_xor_b32 s79, s73, 0x8000\n"
        "v_accvgpr_read_b32 v144, a84\n"
        "v_accvgpr_read_b32 v145, a85\n"
        "v_accvgpr_read_b32 v146, a86\n"
        "v_accvgpr_read_b32 v147, a87\n"
        "s_mov_b64 exec, 15\n"
        "ds_write_b128 v156, a[88:91]\n"
        "s_mov_b64 exec, -1\n"
        "s_cbranch_vccnz LE4D_%=\n"
        "LEWD_%=:\n"
        "s_add_u32 s24, s82, 1\n"
        "s_cmp_lg_u32 s82, s91\n"
        "s_cbranch_scc1 LDU_%=\n"
        "; ---- LDRAIN\n"
        "LLD_%=:\n"
        "v_mfma_f32_16x16x32_bf16 a[16:19], a[128:131], v[24:27], a[16:19]\n"
        "s_waitcnt lgkmcnt(4)\n"
        "v_exp_f32 v62, v62\n"
        "v_mfma_f32_16x16x32_bf16 a[48:51], a[128:131], v[28:31], a[48:51]\n"
        "s_mul_i32 s53, s44, 1052672\n"
        "v_exp_f32 v63, v63\n"
        "v_mfma_f32_16x16x32_bf16 a[52:55], a[132:135], v[28:31], a[52:55]\n"
        "ds_read_b128 a[172:175], v241 offset:27648\n"
        "s_lshl_b32 s96, s43, 15\n"
        "v_cvt_pk_bf16_f32 v95, v62, v63\n"
        "v_mfma_f32_16x16x32_bf16 a[20:23], a[132:135], v[24:27], a[20:23]\n"
        "s_add_u32 s53, s53, s96\n"
        "v_exp_f32 v59, v59\n"
        "v_mfma_f32_16x16x32_bf16 a[24:27], a[136:139], v[24:27], a[24:27]\n"
        "s_lshl_b32 s96, s40, 10\n"
        "v_exp_f32 v58, v58\n"
        "v_mfma_f32_16x16x32_bf16 a[56:59], a[136:139], v[28:31], a[56:59]\n"
        "ds_read_b128 a[176:179], v241 offset:28672\n"
        "s_add_u32 s53, s53, s96\n"
        "v_cvt_pk_bf16_f32 v91, v58, v59\n"
        "v_mfma_f32_16x16x32_bf16 a[60:63], a[140:143], v[28:31], a[60:63]\n"
        "s_add_u32 s53, s53, 0x2201000\n"
        "v_exp_f32 v57, v57\n"
        "v_mfma_f32_16x16x32_bf16 a[28:31], a[140:143], v[24:27], a[28:31]\n"
        "v_exp_f32 v56, v56\n"
        "v_exp_f32 v61, v61\n"
        "v_cvt_pk_bf16_f32 v90, v56, v57\n"
        "s_nop 1\n"
        "s_waitcnt lgkmcnt(4)\n"
        "v_mfma_f32_16x16x32_bf16 a[0:3], a[144:147], v[88:91], a[0:3]\n"
        "s_waitcnt lgkmcnt(2)\n"
        "v_exp_f32 v60, v60\n"
        "v_mfma_f32_16x16x32_bf16 a[4:7], a[148:151], v[88:91], a[4:7]\n"
        "ds_read_b128 a[180:183], v241 offset:29696\n"
        "s_sub_u32 s24, s83, 1\n"
        "v_cvt_pk_bf16_f32 v94, v60, v61\n"
        "s_nop 1\n"
        "v_smfmac_f32_16x16x64_bf16 a[68:71], a[72:75], v[88:95], v154\n"
        "ds_read_b128 a[184:187], v241 offset:30720\n"
        "s_add_u32 s70, s38, s53\n"
        "s_addc_u32 s71, s39, 0\n"
        "v_mfma_f32_16x16x32_bf16 a[32:35], a[144:147], v[92:95], a[32:35]\n"
        "ds_read_b128 a[188:191], v241 offset:31744\n"
        "s_add_u32 s91, s82, 1\n"
        "v_accvgpr_read_b32 v120, a0\n"
        "v_mfma_f32_16x16x32_bf16 a[36:39], a[148:151], v[92:95], a[36:39]\n"
        "s_waitcnt lgkmcnt(3)\n"
        "v_accvgpr_read_b32 v248, a68\n"
        "v_accvgpr_read_b32 v246, a69\n"
        "v_mfma_f32_16x16x32_bf16 a[8:11], a[152:155], v[88:91], a[8:11]\n"
        "s_max_u32 s24, s24, s91\n"
        "v_add_f32 v248, v248, v246\n"
        "v_accvgpr_read_b32 v249, a70\n"
        "v_mfma_f32_16x16x32_bf16 a[40:43], a[152:155], v[92:95], a[40:43]\n"
        "s_add_u32 s91, s83, 1\n"
        "v_rcp_f32 v246, v248\n"
        "v_mfma_f32_16x16x32_bf16 a[44:47], a[172:175], v[92:95], a[44:47]\n"
        "v_accvgpr_read_b32 v247, a71\n"
        "v_add_f32 v249, v249, v247\n"
        "v_mfma_f32_16x16x32_bf16 a[12:15], a[172:175], v[88:91], a[12:15]\n"
        "v_rcp_f32 v247, v249\n"
        "v_mfma_f32_16x16x32_bf16 a[16:19], a[176:179], v[88:91], a[16:19]\n"
        "s_waitcnt lgkmcnt(2)\n"
        "v_accvgpr_read_b32 v112, a32\n"
        "v_accvgpr_read_b32 v113, a33\n"
        "v_mfma_f32_16x16x32_bf16 a[48:51], a[176:179], v[92:95], a[48:51]\n"
        "s_waitcnt lgkmcnt(1)\n"
        "v_accvgpr_read_b32 v114, a34\n"
        "v_accvgpr_read_b32 v115, a35\n"
        "v_mfma_f32_16x16x32_bf16 a[52:55], a[180:183], v[92:95], a[52:55]\n"
        "s_waitcnt lgkmcnt(0)\n"
        "v_accvgpr_read_b32 v116, a36\n"
        "v_accvgpr_read_b32 v117, a37\n"
        "v_mfma_f32_16x16x32_bf16 a[20:23], a[180:183], v[88:91], a[20:23]\n"
        "v_accvgpr_read_b32 v118, a38\n"
        "v_accvgpr_read_b32 v119, a39\n"
        "v_mfma_f32_16x16x32_bf16 a[24:27], a[184:187], v[88:91], a[24:27]\n"
        "v_accvgpr_read_b32 v121, a1\n"
        "v_accvgpr_read_b32 v122, a2\n"
        "v_mfma_f32_16x16x32_bf16 a[56:59], a[184:187], v[92:95], a[56:59]\n"
        "v_accvgpr_read_b32 v123, a3\n"
        "v_accvgpr_read_b32 v124, a4\n"
        "v_mfma_f32_16x16x32_bf16 a[60:63], a[188:191], v[92:95], a[60:63]\n"
        "v_accvgpr_read_b32 v125, a5\n"
        "v_accvgpr_read_b32 v126, a6\n"
        "v_mfma_f32_16x16x32_bf16 a[28:31], a[188:191], v[88:91], a[28:31]\n"
        "v_pk_mul_f32 v[112:113], v[112:113], v[246:247] op_sel:[0,1] op_sel_hi:[1,1]\n"
        "v_pk_mul_f32 v[114:115], v[114:115], v[246:247] op_sel:[0,1] op_sel_hi:[1,1]\n"
        "v_pk_mul_f32 v[116:117], v[116:117], v[246:247] op_sel:[0,1] op_sel_hi:[1,1]\n"
        "v_pk_mul_f32 v[118:119], v[118:119], v[246:247] op_sel:[0,1] op_sel_hi:[1,1]\n"
        "v_cvt_pk_bf16_f32 v112, v112, v113\n"
        "v_cvt_pk_bf16_f32 v113, v114, v115\n"
        "v_cvt_pk_bf16_f32 v114, v116, v117\n"
        "v_cvt_pk_bf16_f32 v115, v118, v119\n"
        "global_store_dwordx4 v254, v[112:115], s[70:71] offset:-4096 sc1\n"
        "v_accvgpr_read_b32 v127, a7\n"
        "v_pk_mul_f32 v[120:121], v[120:121], v[246:247] op_sel:[0,0] op_sel_hi:[1,0]\n"
        "v_pk_mul_f32 v[122:123], v[122:123], v[246:247] op_sel:[0,0] op_sel_hi:[1,0]\n"
        "v_pk_mul_f32 v[124:125], v[124:125], v[246:247] op_sel:[0,0] op_sel_hi:[1,0]\n"
        "v_pk_mul_f32 v[126:127], v[126:127], v[246:247] op_sel:[0,0] op_sel_hi:[1,0]\n"
        "v_cvt_pk_bf16_f32 v120, v120, v121\n"
        "v_cvt_pk_bf16_f32 v121, v122, v123\n"
        "v_cvt_pk_bf16_f32 v122, v124, v125\n"
        "v_cvt_pk_bf16_f32 v123, v126, v127\n"
        "global_store_dwordx4 v254, v[120:123], s[70:71] offset:0 sc1\n"
        "s_add_u32 s70, s70, 0x2000\n"
        "s_addc_u32 s71, s71, 0\n"
        "v_accvgpr_read_b32 v40, a40\n"
        "v_accvgpr_read_b32 v41, a41\n"
        "v_accvgpr_read_b32 v42, a42\n"
        "v_accvgpr_read_b32 v43, a43\n"
        "v_accvgpr_read_b32 v44, a44\n"
        "v_accvgpr_read_b32 v45, a45\n"
        "v_accvgpr_read_b32 v46, a46\n"
        "v_accvgpr_read_b32 v47, a47\n"
        "v_pk_mul_f32 v[40:41], v[40:41], v[246:247] op_sel:[0,1] op_sel_hi:[1,1]\n"
        "v_pk_mul_f32 v[42:43], v[42:43], v[246:247] op_sel:[0,1] op_sel_hi:[1,1]\n"
        "v_pk_mul_f32 v[44:45], v[44:45], v[246:247] op_sel:[0,1] op_sel_hi:[1,1]\n"
        "v_pk_mul_f32 v[46:47], v[46:47], v[246:247] op_sel:[0,1] op_sel_hi:[1,1]\n"
        "v_cvt_pk_bf16_f32 v40, v40, v41\n"
        "v_cvt_pk_bf16_f32 v41, v42, v43\n"
        "v_cvt_pk_bf16_f32 v42, v44, v45\n"
        "v_cvt_pk_bf16_f32 v43, v46, v47\n"
        "global_store_dwordx4 v254, v[40:43], s[70:71] offset:-4096 sc1\n"
        "v_accvgpr_read_b32 v56, a8\n"
        "v_accvgpr_read_b32 v57, a9\n"
        "v_accvgpr_read_b32 v58, a10\n"
        "v_accvgpr_read_b32 v59, a11\n"
        "v_accvgpr_read_b32 v60, a12\n"
        "v_accvgpr_read_b32 v61, a13\n"
        "v_accvgpr_read_b32 v62, a14\n"
        "v_accvgpr_read_b32 v63, a15\n"
        "v_pk_mul_f32 v[56:57], v[56:57], v[246:247] op_sel:[0,0] op_sel_hi:[1,0]\n"
        "v_pk_mul_f32 v[58:59], v[58:59], v[246:247] op_sel:[0,0] op_sel_hi:[1,0]\n"
        "v_pk_mul_f32 v[60:61], v[60:61], v[246:247] op_sel:[0,0] op_sel_hi:[1,0]\n"
        "v_pk_mul_f32 v[62:63], v[62:63], v[246:247] op_sel:[0,0] op_sel_hi:[1,0]\n"
        "v_cvt_pk_bf16_f32 v56, v56, v57\n"
        "v_cvt_pk_bf16_f32 v57, v58, v59\n"
        "v_cvt_pk_bf16_f32 v58, v60, v61\n"
        "v_cvt_pk_bf16_f32 v59, v62, v63\n"
        "global_store_dwordx4 v254, v[56:59], s[70:71] offset:0 sc1\n"
        "s_add_u32 s70, s70, 0x2000\n"
        "s_addc_u32 s71, s71, 0\n"
        "v_accvgpr_read_b32 v112, a48\n"
        "v_accvgpr_read_b32 v113, a49\n"
        "v_accvgpr_read_b32 v114, a50\n"
        "v_accvgpr_read_b32 v115, a51\n"
        "v_accvgpr_read_b32 v116, a52\n"
        "v_accvgpr_read_b32 v117, a53\n"
        "v_accvgpr_read_b32 v118, a54\n"
        "v_accvgpr_read_b32 v119, a55\n"
        "v_pk_mul_f32 v[112:113], v[112:113], v[246:247] op_sel:[0,1] op_sel_hi:[1,1]\n"
        "v_pk_mul_f32 v[114:115], v[114:115], v[246:247] op_sel:[0,1] op_sel_hi:[1,1]\n"
        "v_pk_mul_f32 v[116:117], v[116:117], v[246:247] op_sel:[0,1] op_sel_hi:[1,1]\n"
        "v_pk_mul_f32 v[118:119], v[118:119], v[246:247] op_sel:[0,1] op_sel_hi:[1,1]\n"
        "v_cvt_pk_bf16_f32 v112, v112, v113\n"
        "v_cvt_pk_bf16_f32 v113, v114, v115\n"
        "v_cvt_pk_bf16_f32 v114, v116, v117\n"
        "v_cvt_pk_bf16_f32 v115, v118, v119\n"
        "global_store_dwordx4 v254, v[112:115], s[70:71] offset:-4096 sc1\n"
        "v_accvgpr_read_b32 v120, a16\n"
        "v_accvgpr_read_b32 v121, a17\n"
        "v_accvgpr_read_b32 v122, a18\n"
        "v_accvgpr_read_b32 v123, a19\n"
        "v_accvgpr_read_b32 v124, a20\n"
        "v_accvgpr_read_b32 v125, a21\n"
        "v_accvgpr_read_b32 v126, a22\n"
        "v_accvgpr_read_b32 v127, a23\n"
        "v_pk_mul_f32 v[120:121], v[120:121], v[246:247] op_sel:[0,0] op_sel_hi:[1,0]\n"
        "v_pk_mul_f32 v[122:123], v[122:123], v[246:247] op_sel:[0,0] op_sel_hi:[1,0]\n"
        "v_pk_mul_f32 v[124:125], v[124:125], v[246:247] op_sel:[0,0] op_sel_hi:[1,0]\n"
        "v_pk_mul_f32 v[126:127], v[126:127], v[246:247] op_sel:[0,0] op_sel_hi:[1,0]\n"
        "v_cvt_pk_bf16_f32 v120, v120, v121\n"
        "v_cvt_pk_bf16_f32 v121, v122, v123\n"
        "v_cvt_pk_bf16_f32 v122, v124, v125\n"
        "v_cvt_pk_bf16_f32 v123, v126, v127\n"
        "global_store_dwordx4 v254, v[120:123], s[70:71] offset:0 sc1\n"
        "s_add_u32 s70, s70, 0x2000\n"
        "s_addc_u32 s71, s71, 0\n"
        "v_accvgpr_read_b32 v40, a56\n"
        "v_accvgpr_read_b32 v41, a57\n"
        "v_accvgpr_read_b32 v42, a58\n"
        "v_accvgpr_read_b32 v43, a59\n"
        "v_accvgpr_read_b32 v44, a60\n"
        "v_accvgpr_read_b32 v45, a61\n"
        "v_accvgpr_read_b32 v46, a62\n"
        "v_accvgpr_read_b32 v47, a63\n"
        "v_pk_mul_f32 v[40:41], v[40:41], v[246:247] op_sel:[0,1] op_sel_hi:[1,1]\n"
        "v_pk_mul_f32 v[42:43], v[42:43], v[246:247] op_sel:[0,1] op_sel_hi:[1,1]\n"
        "v_pk_mul_f32 v[44:45], v[44:45], v[246:247] op_sel:[0,1] op_sel_hi:[1,1]\n"
        "v_pk_mul_f32 v[46:47], v[46:47], v[246:247] op_sel:[0,1] op_sel_hi:[1,1]\n"
        "v_cvt_pk_bf16_f32 v40, v40, v41\n"
        "v_cvt_pk_bf16_f32 v41, v42, v43\n"
        "v_cvt_pk_bf16_f32 v42, v44, v45\n"
        "v_cvt_pk_bf16_f32 v43, v46, v47\n"
        "global_store_dwordx4 v254, v[40:43], s[70:71] offset:-4096 sc1\n"
        "v_accvgpr_read_b32 v56, a24\n"
        "v_accvgpr_read_b32 v57, a25\n"
        "v_accvgpr_read_b32 v58, a26\n"
        "v_accvgpr_read_b32 v59, a27\n"
        "v_accvgpr_read_b32 v60, a28\n"
        "v_accvgpr_read_b32 v61, a29\n"
        "v_accvgpr_read_b32 v62, a30\n"
        "v_accvgpr_read_b32 v63, a31\n"
        "v_pk_mul_f32 v[56:57], v[56:57], v[246:247] op_sel:[0,0] op_sel_hi:[1,0]\n"
        "v_pk_mul_f32 v[58:59], v[58:59], v[246:247] op_sel:[0,0] op_sel_hi:[1,0]\n"
        "v_pk_mul_f32 v[60:61], v[60:61], v[246:247] op_sel:[0,0] op_sel_hi:[1,0]\n"
        "v_pk_mul_f32 v[62:63], v[62:63], v[246:247] op_sel:[0,0] op_sel_hi:[1,0]\n"
        "v_cvt_pk_bf16_f32 v56, v56, v57\n"
        "v_cvt_pk_bf16_f32 v57, v58, v59\n"
        "v_cvt_pk_bf16_f32 v58, v60, v61\n"
        "v_cvt_pk_bf16_f32 v59, v62, v63\n"
        "global_store_dwordx4 v254, v[56:59], s[70:71] offset:0 sc1\n"
        "v_add_f32 v247, v248, v249\n"
        "v_cmp_nge_f32 vcc, 0x4e6e6b28, v247\n"
        "s_nop 0\n"
        "s_cbranch_vccnz LRL_%=\n"
        "LSL_%=:\n"
        "s_cmp_eq_u32 s44, 7\n"
        "s_cbranch_scc1 LTL_%=\n"
        "; ---- SINGLE\n"
        "s_branch LSI_%=\n"
        "s_nop 0\n"
        "s_nop 0\n"
        "s_nop 0\n"
        "s_nop 0\n"
        "s_nop 0\n"
        "s_nop 0\n"
        "s_nop 0\n"
        "LSI_%=:\n"
        "v_mfma_f32_16x16x32_bf16 v[32:35], a[96:99], v[0:3], v[224:227]\n"
        "s_waitcnt lgkmcnt(4)\n"
        "ds_read_b128 a[156:159], v241 offset:27648\n"
        "s_add_u32 m0, s80, 0x0\n"
        "v_mfma_f32_16x16x32_bf16 v[32:35], a[100:103], v[4:7], v[32:35]\n"
        "ds_read_b128 a[124:127], v240 offset:7168\n"
        "global_load_lds_dwordx4 v254, s[68:69] offset:0 sc1\n"
        "s_xor_b32 s79, s79, 0x8000\n"
        "v_mfma_f32_16x16x32_bf16 v[32:35], a[104:107], v[8:11], v[32:35]\n"
        "s_add_u32 s82, s82, 1\n"
        "v_exp_f32 v48, v48\n"
        "v_mfma_f32_16x16x32_bf16 v[32:35], a[108:111], v[12:15], v[32:35]\n"
        "s_xor_b32 s78, s78, 0x8000\n"
        "v_exp_f32 v49, v49\n"
        "v_mfma_f32_16x16x32_bf16 v[36:39], a[108:111], v[76:79], v[228:231]\n"
        "ds_read_b128 a[108:111], v240 offset:11264\n"
        "s_mov_b32 s80, s78\n"
        "v_cvt_pk_bf16_f32 v82, v48, v49\n"
        "v_mfma_f32_16x16x32_bf16 v[36:39], a[104:107], v[72:75], v[36:39]\n"
        "ds_read_b128 a[104:107], v240 offset:10240\n"
        "global_load_lds_dwordx4 v254, s[68:69] offset:1024 sc1\n"
        "v_mfma_f32_16x16x32_bf16 v[36:39], a[100:103], v[68:71], v[36:39]\n"
        "v_exp_f32 v50, v50\n"
        "v_mfma_f32_16x16x32_bf16 v[36:39], a[96:99], v[64:67], v[36:39]\n"
        "v_exp_f32 v51, v51\n"
        "v_mfma_f32_16x16x32_bf16 v[176:179], a[128:131], v[16:19], v[176:179]\n"
        "ds_read_b128 a[96:99], v240 offset:8192\n"
        "v_cvt_pk_bf16_f32 v83, v50, v51\n"
        "v_mfma_f32_16x16x32_bf16 v[208:211], a[128:131], v[20:23], v[208:211]\n"
        "ds_read_b128 a[128:131], v241 offset:28672\n"
        "global_load_lds_dwordx4 v254, s[68:69] offset:2048 sc1\n"
        "v_mfma_f32_16x16x32_bf16 v[212:215], a[132:135], v[20:23], v[212:215]\n"
        "v_exp_f32 v52, v52\n"
        "v_mfma_f32_16x16x32_bf16 v[180:183], a[132:135], v[16:19], v[180:183]\n"
        "v_exp_f32 v53, v53\n"
        "v_mfma_f32_16x16x32_bf16 v[184:187], a[136:139], v[16:19], v[184:187]\n"
        "ds_read_b128 a[100:103], v240 offset:9216\n"
        "v_cvt_pk_bf16_f32 v86, v52, v53\n"
        "v_mfma_f32_16x16x32_bf16 v[216:219], a[136:139], v[20:23], v[216:219]\n"
        "ds_read_b128 a[132:135], v241 offset:29696\n"
        "global_load_lds_dwordx4 v254, s[68:69] offset:3072 sc1\n"
        "s_add_u32 m0, s81, 0x0\n"
        "v_mfma_f32_16x16x32_bf16 v[220:223], a[140:143], v[20:23], v[220:223]\n"
        "s_waitcnt lgkmcnt(4)\n"
        "v_exp_f32 v54, v54\n"
        "v_mfma_f32_16x16x32_bf16 v[188:191], a[140:143], v[16:19], v[188:191]\n"
        "s_add_u32 s81, s78, 4096\n"
        "v_exp_f32 v55, v55\n"
        "v_mfma_f32_16x16x32_bf16 v[48:51], a[112:115], v[0:3], v[224:227]\n"
        "ds_read_b128 a[136:139], v241 offset:30720\n"
        "v_cvt_pk_bf16_f32 v87, v54, v55\n"
        "v_mfma_f32_16x16x32_bf16 v[48:51], a[116:119], v[4:7], v[48:51]\n"
        "ds_read_b128 a[140:143], v241 offset:31744\n"
        "global_load_lds_dwordx4 v254, s[74:75] offset:0 sc1\n"
        "v_mfma_f32_16x16x32_bf16 v[48:51], a[120:123], v[8:11], v[48:51]\n"
        "v_exp_f32 v32, v32\n"
        "v_mfma_f32_16x16x32_bf16 v[48:51], a[124:127], v[12:15], v[48:51]\n"
        "v_exp_f32 v33, v33\n"
        "v_mfma_f32_16x16x32_bf16 v[52:55], a[124:127], v[76:79], v[228:231]\n"
        "ds_read_b128 a[124:127], v240 offset:15360\n"
        "v_cvt_pk_bf16_f32 v16, v32, v33\n"
        "v_mfma_f32_16x16x32_bf16 v[52:55], a[120:123], v[72:75], v[52:55]\n"
        "ds_read_b128 a[120:123], v240 offset:14336\n"
        "global_load_lds_dwordx4 v254, s[74:75] offset:1024 sc1\n"
        "v_mfma_f32_16x16x32_bf16 v[52:55], a[116:119], v[68:71], v[52:55]\n"
        "v_exp_f32 v34, v34\n"
        "v_mfma_f32_16x16x32_bf16 v[52:55], a[112:115], v[64:67], v[52:55]\n"
        "v_exp_f32 v35, v35\n"
        "v_smfmac_f32_16x16x64_bf16 a[64:67], a[72:75], v[80:87], v154\n"
        "ds_read_b128 a[112:115], v240 offset:12288\n"
        "v_cvt_pk_bf16_f32 v17, v34, v35\n"
        "v_mfma_f32_16x16x32_bf16 v[160:163], a[144:147], v[80:83], v[160:163]\n"
        "ds_read_b128 a[116:119], v240 offset:13312\n"
        "global_load_lds_dwordx4 v254, s[74:75] offset:2048 sc1\n"
        "v_mfma_f32_16x16x32_bf16 v[192:195], a[144:147], v[84:87], v[192:195]\n"
        "s_waitcnt vmcnt(8) lgkmcnt(4)\n"
        "v_exp_f32 v38, v38\n"
        "v_mfma_f32_16x16x32_bf16 v[196:199], a[148:151], v[84:87], v[196:199]\n"
        "s_barrier\n"
        "v_mfma_f32_16x16x32_bf16 v[164:167], a[148:151], v[80:83], v[164:167]\n"
        "v_exp_f32 v39, v39\n"
        "v_mfma_f32_16x16x32_bf16 v[168:171], a[152:155], v[80:83], v[168:171]\n"
        "v_exp_f32 v36, v36\n"
        "v_mfma_f32_16x16x32_bf16 v[200:203], a[152:155], v[84:87], v[200:203]\n"
        "v_exp_f32 v37, v37\n"
        "v_mfma_f32_16x16x32_bf16 v[204:207], a[156:159], v[84:87], v[204:207]\n"
        "v_exp_f32 v48, v48\n"
        "v_mfma_f32_16x16x32_bf16 v[172:175], a[156:159], v[80:83], v[172:175]\n"
        "v_exp_f32 v49, v49\n"
        "v_mfma_f32_16x16x32_bf16 v[32:35], a[96:99], v[0:3], v[224:227]\n"
        "v_exp_f32 v50, v50\n"
        "v_mfma_f32_16x16x32_bf16 v[32:35], a[100:103], v[4:7], v[32:35]\n"
        "v_exp_f32 v51, v51\n"
        "v_mfma_f32_16x16x32_bf16 v[32:35], a[104:107], v[8:11], v[32:35]\n"
        "v_exp_f32 v52, v52\n"
        "v_mfma_f32_16x16x32_bf16 v[32:35], a[108:111], v[12:15], v[32:35]\n"
        "v_exp_f32 v53, v53\n"
        "v_mfma_f32_16x16x32_bf16 v[176:179], a[128:131], v[80:83], v[176:179]\n"
        "v_exp_f32 v54, v54\n"
        "v_mfma_f32_16x16x32_bf16 v[208:211], a[128:131], v[84:87], v[208:211]\n"
        "v_exp_f32 v55, v55\n"
        "v_mfma_f32_16x16x32_bf16 v[212:215], a[132:135], v[84:87], v[212:215]\n"
        "global_load_lds_dwordx4 v254, s[74:75] offset:3072 sc1\n"
        "s_add_u32 m0, s79, 0x0\n"
        "v_cvt_pk_bf16_f32 v21, v38, v39\n"
        "v_mfma_f32_16x16x32_bf16 v[180:183], a[132:135], v[80:83], v[180:183]\n"
        "v_exp_f32 v32, v32\n"
        "v_mfma_f32_16x16x32_bf16 v[184:187], a[136:139], v[80:83], v[184:187]\n"
        "v_exp_f32 v33, v33\n"
        "v_mfma_f32_16x16x32_bf16 v[216:219], a[136:139], v[84:87], v[216:219]\n"
        "v_exp_f32 v34, v34\n"
        "v_mfma_f32_16x16x32_bf16 v[220:223], a[140:143], v[84:87], v[220:223]\n"
        "v_exp_f32 v35, v35\n"
        "v_mfma_f32_16x16x32_bf16 v[188:191], a[140:143], v[80:83], v[188:191]\n"
        "v_cvt_pk_bf16_f32 v20, v36, v37\n"
        "v_xor_b32 v241, 0x8000, v241\n"
        "v_mfma_f32_16x16x32_bf16 v[36:39], a[108:111], v[76:79], v[228:231]\n"
        "ds_read_b128 a[144:147], v241 offset:0\n"
        "s_add_u32 s76, s66, s90\n"
        "s_addc_u32 s77, s67, 0\n"
        "v_mfma_f32_16x16x32_bf16 v[36:39], a[104:107], v[72:75], v[36:39]\n"
        "ds_read_b128 a[148:151], v241 offset:1024\n"
        "global_load_lds_dwordx4 v144, s[76:77] offset:0 sc1\n"
        "v_mfma_f32_16x16x32_bf16 v[36:39], a[100:103], v[68:71], v[36:39]\n"
        "ds_read_b128 a[152:155], v241 offset:2048\n"
        "v_cvt_pk_bf16_f32 v18, v48, v49\n"
        "v_mfma_f32_16x16x32_bf16 v[36:39], a[96:99], v[64:67], v[36:39]\n"
        "ds_read_b128 a[156:159], v241 offset:3072\n"
        "v_cvt_pk_bf16_f32 v19, v50, v51\n"
        "s_waitcnt lgkmcnt(4)\n"
        "v_mfma_f32_16x16x32_bf16 v[48:51], a[112:115], v[0:3], v[224:227]\n"
        "ds_read_b128 a[96:99], v240 offset:16384\n"
        "v_cvt_pk_bf16_f32 v22, v52, v53\n"
        "v_mfma_f32_16x16x32_bf16 v[48:51], a[116:119], v[4:7], v[48:51]\n"
        "ds_read_b128 a[128:131], v241 offset:4096\n"
        "global_load_lds_dwordx4 v145, s[76:77] offset:1024 sc1\n"
        "v_mfma_f32_16x16x32_bf16 v[48:51], a[120:123], v[8:11], v[48:51]\n"
        "ds_read_b128 a[100:103], v240 offset:17408\n"
        "v_cvt_pk_bf16_f32 v23, v54, v55\n"
        "v_mfma_f32_16x16x32_bf16 v[48:51], a[124:127], v[12:15], v[48:51]\n"
        "ds_read_b128 a[132:135], v241 offset:5120\n"
        "v_cvt_pk_bf16_f32 v80, v32, v33\n"
        "v_mfma_f32_16x16x32_bf16 v[52:55], a[124:127], v[76:79], v[228:231]\n"
        "ds_read_b128 a[136:139], v241 offset:6144\n"
        "v_cvt_pk_bf16_f32 v81, v34, v35\n"
        "v_mfma_f32_16x16x32_bf16 v[52:55], a[120:123], v[72:75], v[52:55]\n"
        "s_waitcnt lgkmcnt(7)\n"
        "ds_read_b128 a[104:107], v240 offset:18432\n"
        "v_mfma_f32_16x16x32_bf16 v[52:55], a[116:119], v[68:71], v[52:55]\n"
        "ds_read_b128 a[140:143], v241 offset:7168\n"
        "global_load_lds_dwordx4 v146, s[76:77] offset:2048 sc1\n"
        "v_mfma_f32_16x16x32_bf16 v[52:55], a[112:115], v[64:67], v[52:55]\n"
        "ds_read_b128 a[108:111], v240 offset:19456\n"
        "s_cmp_ge_u32 s82, s44\n"
        "s_addc_u32 s90, s82, 0\n"
        "v_smfmac_f32_16x16x64_bf16 a[64:67], a[72:75], v[16:23], v154\n"
        "ds_read_b128 a[112:115], v240 offset:20480\n"
        "s_lshl_b32 s90, s90, 15\n"
        "v_mfma_f32_16x16x32_bf16 v[160:163], a[144:147], v[16:19], v[160:163]\n"
        "s_waitcnt lgkmcnt(7)\n"
        "v_exp_f32 v36, v36\n"
        "v_mfma_f32_16x16x32_bf16 v[192:195], a[144:147], v[20:23], v[192:195]\n"
        "ds_read_b128 a[144:147], v241 offset:8192\n"
        "global_load_lds_dwordx4 v147, s[76:77] offset:3072 sc1\n"
        "s_add_u32 m0, s79, s28\n"
        "v_mfma_f32_16x16x32_bf16 v[196:199], a[148:151], v[20:23], v[196:199]\n"
        "ds_read_b128 a[116:119], v240 offset:21504\n"
        "s_add_u32 s76, s76, s25\n"
        "s_addc_u32 s77, s77, 0\n"
        "v_mfma_f32_16x16x32_bf16 v[164:167], a[148:151], v[16:19], v[164:167]\n"
        "v_exp_f32 v37, v37\n"
        "v_mfma_f32_16x16x32_bf16 v[168:171], a[152:155], v[16:19], v[168:171]\n"
        "ds_read_b128 a[148:151], v241 offset:9216\n"
        "v_cvt_pk_bf16_f32 v84, v36, v37\n"
        "v_mfma_f32_16x16x32_bf16 v[200:203], a[152:155], v[20:23], v[200:203]\n"
        "s_waitcnt lgkmcnt(5)\n"
        "ds_read_b128 a[152:155], v241 offset:10240\n"
        "v_mfma_f32_16x16x32_bf16 v[204:207], a[156:159], v[20:23], v[204:207]\n"
        "ds_read_b128 a[120:123], v240 offset:22528\n"
        "global_load_lds_dwordx4 v144, s[76:77] offset:0 sc1\n"
        "v_mfma_f32_16x16x32_bf16 v[172:175], a[156:159], v[16:19], v[172:175]\n"
        "s_waitcnt lgkmcnt(5)\n"
        "v_exp_f32 v38, v38\n"
        "v_mfma_f32_16x16x32_bf16 v[32:35], a[96:99], v[0:3], v[224:227]\n"
        "v_exp_f32 v39, v39\n"
        "v_mfma_f32_16x16x32_bf16 v[32:35], a[100:103], v[4:7], v[32:35]\n"
        "ds_read_b128 a[156:159], v241 offset:11264\n"
        "v_cvt_pk_bf16_f32 v85, v38, v39\n"
        "v_mfma_f32_16x16x32_bf16 v[32:35], a[104:107], v[8:11], v[32:35]\n"
        "ds_read_b128 a[124:127], v240 offset:23552\n"
        "global_load_lds_dwordx4 v145, s[76:77] offset:1024 sc1\n"
        "v_mfma_f32_16x16x32_bf16 v[32:35], a[108:111], v[12:15], v[32:35]\n"
        "v_exp_f32 v48, v48\n"
        "v_mfma_f32_16x16x32_bf16 v[36:39], a[108:111], v[76:79], v[228:231]\n"
        "v_exp_f32 v49, v49\n"
        "v_mfma_f32_16x16x32_bf16 v[36:39], a[104:107], v[72:75], v[36:39]\n"
        "ds_read_b128 a[104:107], v240 offset:26624\n"
        "v_cvt_pk_bf16_f32 v82, v48, v49\n"
        "v_mfma_f32_16x16x32_bf16 v[36:39], a[100:103], v[68:71], v[36:39]\n"
        "ds_read_b128 a[100:103], v240 offset:25600\n"
        "global_load_lds_dwordx4 v146, s[76:77] offset:2048 sc1\n"
        "v_mfma_f32_16x16x32_bf16 v[36:39], a[96:99], v[64:67], v[36:39]\n"
        "v_exp_f32 v50, v50\n"
        "v_mfma_f32_16x16x32_bf16 v[176:179], a[128:131], v[16:19], v[176:179]\n"
        "v_exp_f32 v51, v51\n"
        "v_mfma_f32_16x16x32_bf16 v[208:211], a[128:131], v[20:23], v[208:211]\n"
        "ds_read_b128 a[96:99], v240 offset:24576\n"
        "v_cvt_pk_bf16_f32 v83, v50, v51\n"
        "v_mfma_f32_16x16x32_bf16 v[212:215], a[132:135], v[20:23], v[212:215]\n"
        "ds_read_b128 a[128:131], v241 offset:12288\n"
        "global_load_lds_dwordx4 v147, s[76:77] offset:3072 sc1\n"
        "v_mfma_f32_16x16x32_bf16 v[180:183], a[132:135], v[16:19], v[180:183]\n"
        "v_exp_f32 v52, v52\n"
        "v_mfma_f32_16x16x32_bf16 v[184:187], a[136:139], v[16:19], v[184:187]\n"
        "v_exp_f32 v53, v53\n"
        "v_mfma_f32_16x16x32_bf16 v[216:219], a[136:139], v[20:23], v[216:219]\n"
        "s_waitcnt lgkmcnt(3)\n"
        "ds_read_b128 a[132:135], v241 offset:13312\n"
        "v_mfma_f32_16x16x32_bf16 v[220:223], a[140:143], v[20:23], v[220:223]\n"
        "ds_read_b128 a[136:139], v241 offset:14336\n"
        "v_cvt_pk_bf16_f32 v86, v52, v53\n"
        "v_mfma_f32_16x16x32_bf16 v[188:191], a[140:143], v[16:19], v[188:191]\n"
        "v_exp_f32 v54, v54\n"
        "v_mfma_f32_16x16x32_bf16 v[48:51], a[112:115], v[0:3], v[224:227]\n"
        "v_exp_f32 v55, v55\n"
        "v_mfma_f32_16x16x32_bf16 v[48:51], a[116:119], v[4:7], v[48:51]\n"
        "ds_read_b128 a[140:143], v241 offset:15360\n"
        "v_cvt_pk_bf16_f32 v87, v54, v55\n"
        "v_mfma_f32_16x16x32_bf16 v[48:51], a[120:123], v[8:11], v[48:51]\n"
        "ds_read_b128 a[108:111], v240 offset:27648\n"
        "global_load_dword v244, v245, s[26:27] offset:0\n"
        "v_mfma_f32_16x16x32_bf16 v[48:51], a[124:127], v[12:15], v[48:51]\n"
        "v_exp_f32 v32, v32\n"
        "v_mfma_f32_16x16x32_bf16 v[52:55], a[124:127], v[76:79], v[228:231]\n"
        "ds_read_b128 a[124:127], v240 offset:31744\n"
        "s_add_u32 s26, s26, 0x20000\n"
        "s_addc_u32 s27, s27, 0\n"
        "v_mfma_f32_16x16x32_bf16 v[52:55], a[120:123], v[72:75], v[52:55]\n"
        "v_exp_f32 v33, v33\n"
        "v_mfma_f32_16x16x32_bf16 v[52:55], a[116:119], v[68:71], v[52:55]\n"
        "ds_read_b128 a[116:119], v240 offset:29696\n"
        "v_cvt_pk_bf16_f32 v16, v32, v33\n"
        "v_mfma_f32_16x16x32_bf16 v[52:55], a[112:115], v[64:67], v[52:55]\n"
        "v_exp_f32 v34, v34\n"
        "v_smfmac_f32_16x16x64_bf16 a[64:67], a[72:75], v[80:87], v154\n"
        "ds_read_b128 a[112:115], v240 offset:28672\n"
        "s_add_u32 s68, s64, s90\n"
        "s_addc_u32 s69, s65, 0\n"
        "v_mfma_f32_16x16x32_bf16 v[160:163], a[144:147], v[80:83], v[160:163]\n"
        "v_exp_f32 v35, v35\n"
        "v_mfma_f32_16x16x32_bf16 v[192:195], a[144:147], v[84:87], v[192:195]\n"
        "ds_read_b128 a[144:147], v241 offset:16384\n"
        "v_cvt_pk_bf16_f32 v17, v34, v35\n"
        "v_mfma_f32_16x16x32_bf16 v[196:199], a[148:151], v[84:87], v[196:199]\n"
        "v_exp_f32 v36, v36\n"
        "v_mfma_f32_16x16x32_bf16 v[164:167], a[148:151], v[80:83], v[164:167]\n"
        "v_exp_f32 v37, v37\n"
        "v_mfma_f32_16x16x32_bf16 v[168:171], a[152:155], v[80:83], v[168:171]\n"
        "s_waitcnt lgkmcnt(3)\n"
        "ds_read_b128 a[148:151], v241 offset:17408\n"
        "v_mfma_f32_16x16x32_bf16 v[200:203], a[152:155], v[84:87], v[200:203]\n"
        "ds_read_b128 a[152:155], v241 offset:18432\n"
        "v_cvt_pk_bf16_f32 v20, v36, v37\n"
        "v_mfma_f32_16x16x32_bf16 v[204:207], a[156:159], v[84:87], v[204:207]\n"
        "v_exp_f32 v38, v38\n"
        "v_mfma_f32_16x16x32_bf16 v[172:175], a[156:159], v[80:83], v[172:175]\n"
        "v_exp_f32 v39, v39\n"
        "v_mfma_f32_16x16x32_bf16 v[32:35], a[96:99], v[0:3], v[224:227]\n"
        "ds_read_b128 a[120:123], v240 offset:30720\n"
        "v_cvt_pk_bf16_f32 v21, v38, v39\n"
        "v_mfma_f32_16x16x32_bf16 v[32:35], a[100:103], v[4:7], v[32:35]\n"
        "ds_read_b128 a[156:159], v241 offset:19456\n"
        "v_xor_b32 v240, 0x8000, v240\n"
        "v_mfma_f32_16x16x32_bf16 v[32:35], a[104:107], v[8:11], v[32:35]\n"
        "v_exp_f32 v48, v48\n"
        "v_mfma_f32_16x16x32_bf16 v[32:35], a[108:111], v[12:15], v[32:35]\n"
        "v_exp_f32 v49, v49\n"
        "v_mfma_f32_16x16x32_bf16 v[36:39], a[108:111], v[76:79], v[228:231]\n"
        "v_exp_f32 v50, v50\n"
        "v_mfma_f32_16x16x32_bf16 v[36:39], a[104:107], v[72:75], v[36:39]\n"
        "v_exp_f32 v51, v51\n"
        "v_mfma_f32_16x16x32_bf16 v[36:39], a[100:103], v[68:71], v[36:39]\n"
        "v_exp_f32 v52, v52\n"
        "v_mfma_f32_16x16x32_bf16 v[36:39], a[96:99], v[64:67], v[36:39]\n"
        "v_exp_f32 v53, v53\n"
        "v_mfma_f32_16x16x32_bf16 v[176:179], a[128:131], v[80:83], v[176:179]\n"
        "s_waitcnt vmcnt(9) lgkmcnt(1)\n"
        "v_exp_f32 v54, v54\n"
        "v_mfma_f32_16x16x32_bf16 v[208:211], a[128:131], v[84:87], v[208:211]\n"
        "s_barrier\n"
        "v_mfma_f32_16x16x32_bf16 v[212:215], a[132:135], v[84:87], v[212:215]\n"
        "ds_read_b128 a[96:99], v240 offset:0\n"
        "v_cvt_pk_bf16_f32 v18, v48, v49\n"
        "v_mfma_f32_16x16x32_bf16 v[180:183], a[132:135], v[80:83], v[180:183]\n"
        "ds_read_b128 a[128:131], v241 offset:20480\n"
        "v_cvt_pk_bf16_f32 v19, v50, v51\n"
        "v_mfma_f32_16x16x32_bf16 v[184:187], a[136:139], v[80:83], v[184:187]\n"
        "ds_read_b128 a[100:103], v240 offset:1024\n"
        "s_add_u32 s74, s68, 0x1000\n"
        "s_addc_u32 s75, s69, 0\n"
        "v_mfma_f32_16x16x32_bf16 v[216:219], a[136:139], v[84:87], v[216:219]\n"
        "ds_read_b128 a[132:135], v241 offset:21504\n"
        "v_cvt_pk_bf16_f32 v22, v52, v53\n"
        "v_mfma_f32_16x16x32_bf16 v[220:223], a[140:143], v[84:87], v[220:223]\n"
        "v_exp_f32 v55, v55\n"
        "v_mfma_f32_16x16x32_bf16 v[188:191], a[140:143], v[80:83], v[188:191]\n"
        "ds_read_b128 a[136:139], v241 offset:22528\n"
        "v_cvt_pk_bf16_f32 v23, v54, v55\n"
        "v_mfma_f32_16x16x32_bf16 v[48:51], a[112:115], v[0:3], v[224:227]\n"
        "ds_read_b128 a[104:107], v240 offset:2048\n"
        "v_mfma_f32_16x16x32_bf16 v[48:51], a[116:119], v[4:7], v[48:51]\n"
        "ds_read_b128 a[140:143], v241 offset:23552\n"
        "v_mfma_f32_16x16x32_bf16 v[48:51], a[120:123], v[8:11], v[48:51]\n"
        "v_exp_f32 v32, v32\n"
        "v_mfma_f32_16x16x32_bf16 v[48:51], a[124:127], v[12:15], v[48:51]\n"
        "s_waitcnt lgkmcnt(5)\n"
        "v_exp_f32 v33, v33\n"
        "v_mfma_f32_16x16x32_bf16 v[52:55], a[124:127], v[76:79], v[228:231]\n"
        "ds_read_b128 a[108:111], v240 offset:3072\n"
        "v_cvt_pk_bf16_f32 v80, v32, v33\n"
        "v_mfma_f32_16x16x32_bf16 v[52:55], a[120:123], v[72:75], v[52:55]\n"
        "v_exp_f32 v34, v34\n"
        "v_mfma_f32_16x16x32_bf16 v[52:55], a[116:119], v[68:71], v[52:55]\n"
        "v_exp_f32 v35, v35\n"
        "v_mfma_f32_16x16x32_bf16 v[52:55], a[112:115], v[64:67], v[52:55]\n"
        "ds_read_b128 a[112:115], v240 offset:4096\n"
        "v_cvt_pk_bf16_f32 v81, v34, v35\n"
        "v_smfmac_f32_16x16x64_bf16 a[64:67], a[72:75], v[16:23], v154\n"
        "ds_read_b128 a[116:119], v240 offset:5120\n"
        "v_mfma_f32_16x16x32_bf16 v[160:163], a[144:147], v[16:19], v[160:163]\n"
        "v_exp_f32 v36, v36\n"
        "v_mfma_f32_16x16x32_bf16 v[192:195], a[144:147], v[20:23], v[192:195]\n"
        "v_exp_f32 v37, v37\n"
        "v_mfma_f32_16x16x32_bf16 v[196:199], a[148:151], v[20:23], v[196:199]\n"
        "ds_read_b128 a[144:147], v241 offset:24576\n"
        "v_cvt_pk_bf16_f32 v84, v36, v37\n"
        "v_mfma_f32_16x16x32_bf16 v[164:167], a[148:151], v[16:19], v[164:167]\n"
        "ds_read_b128 a[148:151], v241 offset:25600\n"
        "v_mfma_f32_16x16x32_bf16 v[168:171], a[152:155], v[16:19], v[168:171]\n"
        "v_exp_f32 v38, v38\n"
        "v_mfma_f32_16x16x32_bf16 v[200:203], a[152:155], v[20:23], v[200:203]\n"
        "s_cmp_lg_u32 s82, s24\n"
        "v_exp_f32 v39, v39\n"
        "v_mfma_f32_16x16x32_bf16 v[204:207], a[156:159], v[20:23], v[204:207]\n"
        "ds_read_b128 a[152:155], v241 offset:26624\n"
        "v_cvt_pk_bf16_f32 v85, v38, v39\n"
        "v_mfma_f32_16x16x32_bf16 v[172:175], a[156:159], v[16:19], v[172:175]\n"
        "ds_read_b128 a[120:123], v240 offset:6144\n"
        "s_cbranch_scc1 LSI_%=\n"
        "LHC1_%=:\n"
        "s_cbranch_execz LHRC_%=\n"
        "s_sub_u32 s97, s83, s82\n"
        "s_cmp_gt_u32 s97, 1\n"
        "s_cbranch_scc1 LTL_%=\n"
        "s_cmp_eq_u32 s97, 0\n"
        "s_cbranch_scc1 LE3S_%=\n"
        "global_load_dwordx2 v[252:253], v155, s[48:49] sc1\n"
        "v_accvgpr_read_b32 v246, a93\n"
        "global_load_dword v244, v246, s[46:47]\n"
        "s_branch LEWS_%=\n"
        "LE3S_%=:\n"
        "v_cmp_ne_u64 vcc, v[252:253], v[250:251]\n"
        "s_mov_b64 s[68:69], s[92:93]\n"
        "s_mov_b64 s[74:75], s[94:95]\n"
        "s_mov_b32 s80, s50\n"
        "s_mov_b32 s81, s51\n"
        "s_mov_b64 s[66:67], s[22:23]\n"
        "s_mov_b32 s90, s29\n"
        "s_mov_b32 s25, s30\n"
        "s_lshl_b32 s28, s28, 2\n"
        "s_xor_b32 s79, s73, 0x8000\n"
        "v_accvgpr_read_b32 v144, a84\n"
        "v_accvgpr_read_b32 v145, a85\n"
        "v_accvgpr_read_b32 v146, a86\n"
        "v_accvgpr_read_b32 v147, a87\n"
        "s_mov_b64 exec, 15\n"
        "ds_write_b128 v156, a[88:91]\n"
        "s_mov_b64 exec, -1\n"
        "s_cbranch_vccnz LE4S_%=\n"
        "LEWS_%=:\n"
        "s_add_u32 s24, s82, 1\n"
        "s_cmp_lg_u32 s82, s91\n"
        "s_cbranch_scc1 LSI_%=\n"
        "; ---- BACK\n"
        "LTL_%=:\n"
        "v_mfma_f32_16x16x32_bf16 v[176:179], a[128:131], v[16:19], v[176:179]\n"
        "s_add_u32 m0, s73, 0x10000\n"
        "s_xor_b32 s66, s29, 0x40000\n"
        "s_add_u32 s16, s51, 4096\n"
        "v_mfma_f32_16x16x32_bf16 v[208:211], a[128:131], v[20:23], v[208:211]\n"
        "s_add_u32 s60, s22, s66\n"
        "s_addc_u32 s61, s23, 0\n"
        "s_waitcnt lgkmcnt(4)\n"
        "v_mfma_f32_16x16x32_bf16 v[212:215], a[132:135], v[20:23], v[212:215]\n"
        "global_load_lds_dwordx4 v144, s[60:61] offset:0\n"
        "s_xor_b32 s66, s29, 0x60000\n"
        "v_add_u32 v230, 3584, v152\n"
        "v_mfma_f32_16x16x32_bf16 v[180:183], a[132:135], v[16:19], v[180:183]\n"
        "ds_read_b128 a[156:159], v241 offset:27648\n"
        "s_mul_i32 s53, s45, 1052672\n"
        "v_min_u32 v230, s16, v230\n"
        "v_mfma_f32_16x16x32_bf16 v[184:187], a[136:139], v[16:19], v[184:187]\n"
        "s_lshl_b32 s96, s43, 15\n"
        "v_exp_f32 v48, v48\n"
        "v_mfma_f32_16x16x32_bf16 v[216:219], a[136:139], v[20:23], v[216:219]\n"
        "s_add_u32 s53, s53, s96\n"
        "v_exp_f32 v49, v49\n"
        "v_mfma_f32_16x16x32_bf16 v[220:223], a[140:143], v[20:23], v[220:223]\n"
        "s_waitcnt lgkmcnt(2)\n"
        "ds_read_b128 a[128:131], v241 offset:28672\n"
        "s_lshl_b32 s96, s40, 10\n"
        "v_mfma_f32_16x16x32_bf16 v[188:191], a[140:143], v[16:19], v[188:191]\n"
        "global_load_lds_dwordx4 v145, s[60:61] offset:1024\n"
        "v_cvt_pk_bf16_f32 v82, v48, v49\n"
        "v_exp_f32 v50, v50\n"
        "v_exp_f32 v51, v51\n"
        "ds_read_b128 a[132:135], v241 offset:29696\n"
        "v_cvt_pk_bf16_f32 v83, v50, v51\n"
        "s_nop 1\n"
        "v_mfma_f32_16x16x32_bf16 v[160:163], a[144:147], v[80:83], v[160:163]\n"
        "ds_read_b128 a[136:139], v241 offset:30720\n"
        "s_add_u32 s53, s53, s96\n"
        "s_add_u32 s53, s53, 0x2201000\n"
        "v_mfma_f32_16x16x32_bf16 v[164:167], a[148:151], v[80:83], v[164:167]\n"
        "ds_read_b128 a[140:143], v241 offset:31744\n"
        "global_load_lds_dwordx4 v146, s[60:61] offset:2048\n"
        "s_lshl_b32 s14, s44, 5\n"
        "v_mfma_f32_16x16x32_bf16 v[168:171], a[152:155], v[80:83], v[168:171]\n"
        "s_waitcnt lgkmcnt(4)\n"
        "v_exp_f32 v52, v52\n"
        "v_mfma_f32_16x16x32_bf16 v[172:175], a[156:159], v[80:83], v[172:175]\n"
        "s_add_u32 s14, s14, s43\n"
        "v_exp_f32 v53, v53\n"
        "s_waitcnt lgkmcnt(3)\n"
        "v_mfma_f32_16x16x32_bf16 v[176:179], a[128:131], v[80:83], v[176:179]\n"
        "s_add_u32 s70, s38, s53\n"
        "s_addc_u32 s71, s39, 0\n"
        "s_lshl2_add_u32 s14, s14, s40\n"
        "s_lshl_b32 s14, s14, 8\n"
        "s_add_u32 s14, s14, 131072\n"
        "s_sub_u32 s62, 15, s12\n"
        "global_load_lds_dwordx4 v147, s[60:61] offset:3072\n"
        "s_add_u32 m0, s73, 0x14000\n"
        "s_waitcnt lgkmcnt(2)\n"
        "v_mfma_f32_16x16x32_bf16 v[180:183], a[132:135], v[80:83], v[180:183]\n"
        "s_add_u32 s60, s22, s66\n"
        "s_addc_u32 s61, s23, 0\n"
        "s_xor_b32 s66, s29, 0x80000\n"
        "v_cvt_pk_bf16_f32 v86, v52, v53\n"
        "s_waitcnt lgkmcnt(1)\n"
        "v_mfma_f32_16x16x32_bf16 v[184:187], a[136:139], v[80:83], v[184:187]\n"
        "s_waitcnt lgkmcnt(0)\n"
        "v_exp_f32 v54, v54\n"
        "v_mfma_f32_16x16x32_bf16 v[188:191], a[140:143], v[80:83], v[188:191]\n"
        "v_exp_f32 v55, v55\n"
        "global_load_lds_dwordx4 v144, s[60:61] offset:0\n"
        "v_cvt_pk_bf16_f32 v87, v54, v55\n"
        "s_nop 1\n"
        "v_smfmac_f32_16x16x64_bf16 a[64:67], a[72:75], v[80:87], v154\n"
        "ds_read2_b32 a[0:1], v148 offset0:0 offset1:1\n"
        "s_lshl2_add_u32 s63, s11, s40\n"
        "v_add_u32 v231, 3648, v152\n"
        "v_mfma_f32_16x16x32_bf16 v[192:195], a[144:147], v[84:87], v[192:195]\n"
        "ds_read2_b32 a[2:3], v148 offset0:2 offset1:3\n"
        "s_lshl_b32 s63, s63, 5\n"
        "v_min_u32 v231, s16, v231\n"
        "v_mfma_f32_16x16x32_bf16 v[196:199], a[148:151], v[84:87], v[196:199]\n"
        "ds_read_b128 a[192:195], v230 offset:0\n"
        "v_accvgpr_read_b32 v248, a64\n"
        "v_mfma_f32_16x16x32_bf16 v[200:203], a[152:155], v[84:87], v[200:203]\n"
        "ds_read2_b32 a[4:5], v148 offset0:128 offset1:129\n"
        "global_load_lds_dwordx4 v145, s[60:61] offset:1024\n"
        "v_mfma_f32_16x16x32_bf16 v[204:207], a[156:159], v[84:87], v[204:207]\n"
        "ds_read2_b32 a[6:7], v148 offset0:130 offset1:131\n"
        "v_accvgpr_read_b32 v246, a65\n"
        "v_mfma_f32_16x16x32_bf16 v[208:211], a[128:131], v[84:87], v[208:211]\n"
        "ds_read_b128 a[196:199], v152 offset:3072\n"
        "v_add_f32 v248, v248, v246\n"
        "v_mfma_f32_16x16x32_bf16 v[212:215], a[132:135], v[84:87], v[212:215]\n"
        "v_rcp_f32 v246, v248\n"
        "v_mfma_f32_16x16x32_bf16 v[216:219], a[136:139], v[84:87], v[216:219]\n"
        "ds_read2_b32 a[8:9], v149 offset0:0 offset1:1\n"
        "global_load_lds_dwordx4 v146, s[60:61] offset:2048\n"
        "v_mfma_f32_16x16x32_bf16 v[220:223], a[140:143], v[84:87], v[220:223]\n"
        "ds_read2_b32 a[10:11], v149 offset0:2 offset1:3\n"
        "ds_read_b128 a[200:203], v152 offset:2560\n"
        "ds_read2_b32 a[12:13], v149 offset0:128 offset1:129\n"
        "v_accvgpr_read_b32 v249, a66\n"
        "s_waitcnt lgkmcnt(7)\n"
        "v_accvgpr_read_b32 v247, a67\n"
        "v_add_f32 v249, v249, v247\n"
        "v_rcp_f32 v247, v249\n"
        "v_mul_f32 v160, v160, v246\n"
        "global_load_lds_dwordx4 v147, s[60:61] offset:3072\n"
        "s_add_u32 m0, s73, 0x8000\n"
        "v_mul_f32 v192, v192, v247\n"
        "v_mul_f32 v193, v193, v247\n"
        "v_mul_f32 v194, v194, v247\n"
        "v_mul_f32 v195, v195, v247\n"
        "v_mul_f32 v196, v196, v247\n"
        "v_mul_f32 v197, v197, v247\n"
        "v_mul_f32 v198, v198, v247\n"
        "v_mul_f32 v199, v199, v247\n"
        "v_cvt_pk_bf16_f32 v192, v192, v193\n"
        "v_cvt_pk_bf16_f32 v193, v194, v195\n"
        "v_cvt_pk_bf16_f32 v194, v196, v197\n"
        "v_cvt_pk_bf16_f32 v195, v198, v199\n"
        "global_store_dwordx4 v254, v[192:195], s[70:71] offset:-4096 sc1\n"
        "s_waitcnt lgkmcnt(3)\n"
        "v_mul_f32 v161, v161, v246\n"
        "v_mul_f32 v162, v162, v246\n"
        "v_mul_f32 v163, v163, v246\n"
        "v_mul_f32 v164, v164, v246\n"
        "v_mul_f32 v165, v165, v246\n"
        "v_mul_f32 v166, v166, v246\n"
        "v_mul_f32 v167, v167, v246\n"
        "v_cvt_pk_bf16_f32 v160, v160, v161\n"
        "v_cvt_pk_bf16_f32 v161, v162, v163\n"
        "v_cvt_pk_bf16_f32 v162, v164, v165\n"
        "v_cvt_pk_bf16_f32 v163, v166, v167\n"
        "global_store_dwordx4 v254, v[160:163], s[70:71] offset:0 sc1\n"
        "s_add_u32 s70, s70, 0x2000\n"
        "s_addc_u32 s71, s71, 0\n"
        "s_waitcnt lgkmcnt(0)\n"
        "v_mul_f32 v200, v200, v247\n"
        "v_mul_f32 v201, v201, v247\n"
        "v_mul_f32 v202, v202, v247\n"
        "v_mul_f32 v203, v203, v247\n"
        "v_mul_f32 v204, v204, v247\n"
        "v_mul_f32 v205, v205, v247\n"
        "v_mul_f32 v206, v206, v247\n"
        "v_mul_f32 v207, v207, v247\n"
        "v_cvt_pk_bf16_f32 v200, v200, v201\n"
        "v_cvt_pk_bf16_f32 v201, v202, v203\n"
        "v_cvt_pk_bf16_f32 v202, v204, v205\n"
        "v_cvt_pk_bf16_f32 v203, v206, v207\n"
        "global_store_dwordx4 v254, v[200:203], s[70:71] offset:-4096 sc1\n"
        "ds_read2_b32 a[14:15], v149 offset0:130 offset1:131\n"
        "ds_read_b128 a[204:207], v152 offset:2048\n"
        "v_mul_f32 v168, v168, v246\n"
        "v_mul_f32 v169, v169, v246\n"
        "v_mul_f32 v170, v170, v246\n"
        "v_mul_f32 v171, v171, v246\n"
        "v_cvt_pk_bf16_f32 v168, v168, v169\n"
        "v_mul_f32 v172, v172, v246\n"
        "v_mul_f32 v173, v173, v246\n"
        "v_mul_f32 v174, v174, v246\n"
        "v_mul_f32 v175, v175, v246\n"
        "v_mul_f32 v176, v176, v246\n"
        "v_mul_f32 v177, v177, v246\n"
        "v_mul_f32 v208, v208, v247\n"
        "v_mul_f32 v209, v209, v247\n"
        "v_cvt_pk_bf16_f32 v169, v170, v171\n"
        "v_mul_f32 v178, v178, v246\n"
        "v_mul_f32 v179, v179, v246\n"
        "v_mul_f32 v210, v210, v247\n"
        "v_mul_f32 v211, v211, v247\n"
        "v_cvt_pk_bf16_f32 v176, v176, v177\n"
        "v_cvt_pk_bf16_f32 v208, v208, v209\n"
        "v_cvt_pk_bf16_f32 v170, v172, v173\n"
        "v_cvt_pk_bf16_f32 v171, v174, v175\n"
        "v_mul_f32 v180, v180, v246\n"
        "v_mul_f32 v181, v181, v246\n"
        "v_mul_f32 v182, v182, v246\n"
        "v_mul_f32 v183, v183, v246\n"
        "v_mul_f32 v184, v184, v246\n"
        "v_mul_f32 v185, v185, v246\n"
        "v_mul_f32 v212, v212, v247\n"
        "v_mul_f32 v213, v213, v247\n"
        "v_mul_f32 v214, v214, v247\n"
        "v_mul_f32 v215, v215, v247\n"
        "v_mul_f32 v216, v216, v247\n"
        "v_mul_f32 v217, v217, v247\n"
        "v_cvt_pk_bf16_f32 v177, v178, v179\n"
        "v_cvt_pk_bf16_f32 v209, v210, v211\n"
        "global_store_dwordx4 v254, v[168:171], s[70:71] offset:0 sc1\n"
        "ds_read2_b32 a[16:17], v150 offset0:0 offset1:1\n"
        "ds_read2_b32 a[18:19], v150 offset0:2 offset1:3\n"
        "v_mul_f32 v186, v186, v246\n"
        "v_mul_f32 v187, v187, v246\n"
        "v_mul_f32 v218, v218, v247\n"
        "v_mul_f32 v219, v219, v247\n"
        "v_cvt_pk_bf16_f32 v184, v184, v185\n"
        "v_cvt_pk_bf16_f32 v216, v216, v217\n"
        "v_cvt_pk_bf16_f32 v178, v180, v181\n"
        "v_cvt_pk_bf16_f32 v179, v182, v183\n"
        "v_cvt_pk_bf16_f32 v210, v212, v213\n"
        "v_cvt_pk_bf16_f32 v211, v214, v215\n"
        "s_add_u32 s70, s70, 0x2000\n"
        "s_addc_u32 s71, s71, 0\n"
        "v_mul_f32 v188, v188, v246\n"
        "v_mul_f32 v189, v189, v246\n"
        "v_mul_f32 v190, v190, v246\n"
        "v_mul_f32 v191, v191, v246\n"
        "v_mul_f32 v220, v220, v247\n"
        "v_mul_f32 v221, v221, v247\n"
        "v_mul_f32 v222, v222, v247\n"
        "v_mul_f32 v223, v223, v247\n"
        "v_cvt_pk_bf16_f32 v185, v186, v187\n"
        "v_cvt_pk_bf16_f32 v217, v218, v219\n"
        "global_store_dwordx4 v254, v[208:211], s[70:71] offset:-4096 sc1\n"
        "ds_read_b128 a[208:211], v152 offset:1536\n"
        "ds_read2_b32 a[20:21], v150 offset0:128 offset1:129\n"
        "v_cvt_pk_bf16_f32 v186, v188, v189\n"
        "global_store_dwordx4 v254, v[176:179], s[70:71] offset:0 sc1\n"
        "ds_read2_b32 a[22:23], v150 offset0:130 offset1:131\n"
        "ds_read_b128 a[212:215], v152 offset:1024\n"
        "v_cvt_pk_bf16_f32 v187, v190, v191\n"
        "v_cvt_pk_bf16_f32 v218, v220, v221\n"
        "v_cvt_pk_bf16_f32 v219, v222, v223\n"
        "s_add_u32 s70, s70, 0x2000\n"
        "s_addc_u32 s71, s71, 0\n"
        "global_store_dwordx4 v254, v[216:219], s[70:71] offset:-4096 sc1\n"
        "ds_read2_b32 a[24:25], v151 offset0:0 offset1:1\n"
        "ds_read2_b32 a[26:27], v151 offset0:2 offset1:3\n"
        "v_add_f32 v247, v248, v249\n"
        "global_store_dwordx4 v254, v[184:187], s[70:71] offset:0 sc1\n"
        "ds_read_b128 a[216:219], v152 offset:512\n"
        "ds_read2_b32 a[28:29], v151 offset0:128 offset1:129\n"
        "v_cmp_nge_f32 vcc, 0x4e6e6b28, v247\n"
        "v_mfma_f32_16x16x32_bf16 v[96:99], a[0:3], a[192:195], 0\n"
        "ds_read2_b32 a[30:31], v151 offset0:130 offset1:131\n"
        "s_cbranch_vccnz LRH_%=\n"
        "LSH_%=:\n"
        "v_and_b32 v234, 0x70, v254\n"
        "v_mfma_f32_16x16x32_bf16 v[100:103], a[0:3], a[196:199], 0\n"
        "ds_read_b128 a[220:223], v152 offset:0\n"
        "v_lshrrev_b32 v234, 1, v234\n"
        "v_mfma_f32_16x16x32_bf16 v[100:103], a[4:7], a[192:195], v[100:103]\n"
        "ds_read2_b32 a[32:33], v148 offset0:16 offset1:17\n"
        "v_and_b32 v235, 0x80, v254\n"
        "v_mfma_f32_16x16x32_bf16 v[104:107], a[8:11], a[192:195], 0\n"
        "ds_read2_b32 a[34:35], v148 offset0:18 offset1:19\n"
        "v_add3_u32 v234, v234, v235, s14\n"
        "v_mfma_f32_16x16x32_bf16 v[104:107], a[4:7], a[196:199], v[104:107]\n"
        "ds_read_b128 a[224:227], v231 offset:0\n"
        "s_and_b32 s14, s10, 1\n"
        "v_xor_b32 v154, 32, v153\n"
        "v_mfma_f32_16x16x32_bf16 v[104:107], a[0:3], a[200:203], v[104:107]\n"
        "ds_read2_b32 a[36:37], v148 offset0:144 offset1:145\n"
        "s_waitcnt lgkmcnt(8)\n"
        "v_mfma_f32_16x16x32_bf16 v[108:111], a[0:3], a[204:207], 0\n"
        "ds_read2_b32 a[38:39], v148 offset0:146 offset1:147\n"
        "v_add_u32 v155, 0x10000, v153\n"
        "v_mfma_f32_16x16x32_bf16 v[108:111], a[4:7], a[200:203], v[108:111]\n"
        "ds_read_b128 a[228:231], v152 offset:3136\n"
        "v_add_u32 v156, 0x10000, v154\n"
        "v_mfma_f32_16x16x32_bf16 v[108:111], a[8:11], a[196:199], v[108:111]\n"
        "ds_read2_b32 a[40:41], v149 offset0:16 offset1:17\n"
        "v_add_u32 v157, 0x20000, v153\n"
        "v_mfma_f32_16x16x32_bf16 v[108:111], a[12:15], a[192:195], v[108:111]\n"
        "ds_read2_b32 a[42:43], v149 offset0:18 offset1:19\n"
        "v_add_u32 v158, 0x20000, v154\n"
        "v_mfma_f32_16x16x32_bf16 v[128:131], a[16:19], a[192:195], 0\n"
        "ds_read_b128 a[232:235], v152 offset:2624\n"
        "s_add_u32 s60, s22, s66\n"
        "s_addc_u32 s61, s23, 0\n"
        "v_mfma_f32_16x16x32_bf16 v[128:131], a[12:15], a[196:199], v[128:131]\n"
        "ds_read2_b32 a[44:45], v149 offset0:144 offset1:145\n"
        "s_xor_b32 s66, s29, 0xa0000\n"
        "v_add_u32 v230, 3712, v152\n"
        "v_mfma_f32_16x16x32_bf16 v[128:131], a[8:11], a[200:203], v[128:131]\n"
        "ds_read2_b32 a[46:47], v149 offset0:146 offset1:147\n"
        "v_min_u32 v230, s16, v230\n"
        "v_mfma_f32_16x16x32_bf16 v[128:131], a[4:7], a[204:207], v[128:131]\n"
        "ds_read_b128 a[236:239], v152 offset:2112\n"
        "v_bfe_u32 v232, v254, 4, 4\n"
        "v_mfma_f32_16x16x32_bf16 v[128:131], a[0:3], a[208:211], v[128:131]\n"
        "ds_read2_b32 a[48:49], v150 offset0:16 offset1:17\n"
        "v_lshrrev_b32 v233, 8, v254\n"
        "v_mfma_f32_16x16x32_bf16 v[132:135], a[0:3], a[212:215], 0\n"
        "ds_read2_b32 a[50:51], v150 offset0:18 offset1:19\n"
        "v_add_u32 v231, 3776, v152\n"
        "v_mfma_f32_16x16x32_bf16 v[132:135], a[4:7], a[208:211], v[132:135]\n"
        "ds_read_b128 a[240:243], v152 offset:1600\n"
        "v_min_u32 v231, s16, v231\n"
        "v_mfma_f32_16x16x32_bf16 v[132:135], a[8:11], a[204:207], v[132:135]\n"
        "ds_read2_b32 a[52:53], v150 offset0:144 offset1:145\n"
        "s_cmp_le_u32 s10, 0\n"
        "s_addc_u32 s74, 0, 0\n"
        "v_mfma_f32_16x16x32_bf16 v[132:135], a[12:15], a[200:203], v[132:135]\n"
        "ds_read2_b32 a[54:55], v150 offset0:146 offset1:147\n"
        "s_cmp_le_u32 s10, 1\n"
        "s_addc_u32 s75, 1, 0\n"
        "v_mfma_f32_16x16x32_bf16 v[132:135], a[16:19], a[196:199], v[132:135]\n"
        "ds_read_b128 a[244:247], v152 offset:1088\n"
        "s_waitcnt vmcnt(17)\n"
        "v_mfma_f32_16x16x32_bf16 v[132:135], a[20:23], a[192:195], v[132:135]\n"
        "s_barrier\n"
        "v_mfma_f32_16x16x32_bf16 v[136:139], a[24:27], a[192:195], 0\n"
        "ds_read2_b32 a[56:57], v151 offset0:16 offset1:17\n"
        "s_cmp_le_u32 s10, 2\n"
        "s_addc_u32 s76, 2, 0\n"
        "v_mfma_f32_16x16x32_bf16 v[136:139], a[20:23], a[196:199], v[136:139]\n"
        "ds_read2_b32 a[58:59], v151 offset0:18 offset1:19\n"
        "s_cmp_lt_u32 s10, 2\n"
        "s_cselect_b32 s62, s12, s62\n"
        "v_mfma_f32_16x16x32_bf16 v[136:139], a[16:19], a[200:203], v[136:139]\n"
        "ds_read_b128 a[248:251], v152 offset:576\n"
        "s_lshl1_add_u32 s62, s62, s14\n"
        "s_lshl_b32 s62, s62, 6\n"
        "v_mfma_f32_16x16x32_bf16 v[136:139], a[12:15], a[204:207], v[136:139]\n"
        "ds_read2_b32 a[60:61], v151 offset0:144 offset1:145\n"
        "s_lshl_b32 s14, s62, 11\n"
        "s_lshl1_add_u32 s14, s63, s14\n"
        "v_mfma_f32_16x16x32_bf16 v[136:139], a[8:11], a[208:211], v[136:139]\n"
        "ds_read2_b32 a[62:63], v151 offset0:146 offset1:147\n"
        "s_add_u32 s64, s8, s14\n"
        "s_addc_u32 s65, s9, 0\n"
        "v_mfma_f32_16x16x32_bf16 v[136:139], a[4:7], a[212:215], v[136:139]\n"
        "ds_read_b128 a[252:255], v152 offset:64\n"
        "s_and_b32 s67, s74, 1\n"
        "s_waitcnt lgkmcnt(7)\n"
        "v_mfma_f32_16x16x32_bf16 v[136:139], a[0:3], a[216:219], v[136:139]\n"
        "ds_read_b64_tr_b16 a[96:97], v153 offset:0\n"
        "s_add_u32 s56, s36, s14\n"
        "s_addc_u32 s57, s37, 0\n"
        "v_mfma_f32_16x16x32_bf16 v[140:143], a[0:3], a[220:223], 0\n"
        "ds_read_b64_tr_b16 a[98:99], v153 offset:4096\n"
        "s_lshl2_add_u32 s14, s12, s10\n"
        "s_lshl_b32 s14, s14, 6\n"
        "v_mfma_f32_16x16x32_bf16 v[140:143], a[4:7], a[216:219], v[140:143]\n"
        "ds_read2_b32 a[0:1], v148 offset0:32 offset1:33\n"
        "s_lshl4_add_u32 s14, s40, s14\n"
        "s_add_u32 s14, s14, 1024\n"
        "v_mfma_f32_16x16x32_bf16 v[140:143], a[8:11], a[212:215], v[140:143]\n"
        "ds_read2_b32 a[2:3], v148 offset0:34 offset1:35\n"
        "s_lshl_b32 s14, s14, 7\n"
        "v_lshl_add_u32 v159, v232, 7, s14\n"
        "v_mfma_f32_16x16x32_bf16 v[140:143], a[12:15], a[208:211], v[140:143]\n"
        "ds_read_b64_tr_b16 a[100:101], v154 offset:0\n"
        "s_lshl_b32 s67, s67, 12\n"
        "v_mfma_f32_16x16x32_bf16 v[140:143], a[16:19], a[204:207], v[140:143]\n"
        "ds_read_b64_tr_b16 a[102:103], v154 offset:4096\n"
        "s_add_u32 s24, s94, 0x400000\n"
        "s_addc_u32 s25, s95, 0\n"
        "v_mfma_f32_16x16x32_bf16 v[140:143], a[20:23], a[200:203], v[140:143]\n"
        "ds_read2_b32 a[4:5], v148 offset0:160 offset1:161\n"
        "v_mfma_f32_16x16x32_bf16 v[140:143], a[24:27], a[196:199], v[140:143]\n"
        "ds_read2_b32 a[6:7], v148 offset0:162 offset1:163\n"
        "v_mfma_f32_16x16x32_bf16 v[140:143], a[28:31], a[192:195], v[140:143]\n"
        "ds_read_b128 a[192:195], v230 offset:0\n"
        "v_add_u32 v230, 3840, v152\n"
        "v_mfma_f32_16x16x32_bf16 v[96:99], a[32:35], a[224:227], v[96:99]\n"
        "ds_read_b128 a[196:199], v152 offset:3200\n"
        "v_min_u32 v230, s16, v230\n"
        "v_mfma_f32_16x16x32_bf16 v[100:103], a[32:35], a[228:231], v[100:103]\n"
        "ds_read_b64_tr_b16 a[104:105], v153 offset:8192\n"
        "v_mfma_f32_16x16x32_bf16 v[100:103], a[36:39], a[224:227], v[100:103]\n"
        "ds_read_b64_tr_b16 a[106:107], v153 offset:12288\n"
        "v_mfma_f32_16x16x32_bf16 v[104:107], a[40:43], a[224:227], v[104:107]\n"
        "ds_read2_b32 a[8:9], v149 offset0:32 offset1:33\n"
        "v_mfma_f32_16x16x32_bf16 v[104:107], a[36:39], a[228:231], v[104:107]\n"
        "ds_read2_b32 a[10:11], v149 offset0:34 offset1:35\n"
        "v_mfma_f32_16x16x32_bf16 v[104:107], a[32:35], a[232:235], v[104:107]\n"
        "ds_read_b128 a[200:203], v152 offset:2688\n"
        "v_mfma_f32_16x16x32_bf16 v[108:111], a[32:35], a[236:239], v[108:111]\n"
        "ds_read_b64_tr_b16 a[108:109], v154 offset:8192\n"
        "v_mfma_f32_16x16x32_bf16 v[108:111], a[36:39], a[232:235], v[108:111]\n"
        "ds_read_b64_tr_b16 a[110:111], v154 offset:12288\n"
        "v_mfma_f32_16x16x32_bf16 v[108:111], a[40:43], a[228:231], v[108:111]\n"
        "ds_read2_b32 a[12:13], v149 offset0:160 offset1:161\n"
        "v_mfma_f32_16x16x32_bf16 v[108:111], a[44:47], a[224:227], v[108:111]\n"
        "ds_read2_b32 a[14:15], v149 offset0:162 offset1:163\n"
        "v_mfma_f32_16x16x32_bf16 v[128:131], a[48:51], a[224:227], v[128:131]\n"
        "ds_read_b128 a[204:207], v152 offset:2176\n"
        "v_mfma_f32_16x16x32_bf16 v[128:131], a[44:47], a[228:231], v[128:131]\n"
        "ds_read_b64_tr_b16 a[112:113], v153 offset:16384\n"
        "s_waitcnt lgkmcnt(8)\n"
        "v_mfma_f32_16x16x32_bf16 v[128:131], a[40:43], a[232:235], v[128:131]\n"
        "ds_read_b64_tr_b16 a[114:115], v153 offset:20480\n"
        "v_mfma_f32_16x16x32_bf16 v[128:131], a[36:39], a[236:239], v[128:131]\n"
        "ds_read2_b32 a[16:17], v150 offset0:32 offset1:33\n"
        "v_mfma_f32_16x16x32_bf16 v[128:131], a[32:35], a[240:243], v[128:131]\n"
        "ds_read2_b32 a[18:19], v150 offset0:34 offset1:35\n"
        "v_mfma_f32_16x16x32_bf16 v[132:135], a[32:35], a[244:247], v[132:135]\n"
        "ds_read_b128 a[208:211], v152 offset:1664\n"
        "v_mfma_f32_16x16x32_bf16 v[132:135], a[36:39], a[240:243], v[132:135]\n"
        "ds_read_b64_tr_b16 a[116:117], v154 offset:16384\n"
        "v_mfma_f32_16x16x32_bf16 v[132:135], a[40:43], a[236:239], v[132:135]\n"
        "ds_read_b64_tr_b16 a[118:119], v154 offset:20480\n"
        "v_mfma_f32_16x16x32_bf16 v[132:135], a[44:47], a[232:235], v[132:135]\n"
        "ds_read2_b32 a[20:21], v150 offset0:160 offset1:161\n"
        "s_waitcnt vmcnt(0)\n"
        "v_mfma_f32_16x16x32_bf16 v[132:135], a[48:51], a[228:231], v[132:135]\n"
        "s_mov_b64 exec, 0xffff\n"
        "global_store_dwordx2 v234, v[250:251], s[48:49] sc1\n"
        "s_mov_b64 exec, -1\n"
        "v_mfma_f32_16x16x32_bf16 v[132:135], a[52:55], a[224:227], v[132:135]\n"
        "ds_read2_b32 a[22:23], v150 offset0:162 offset1:163\n"
        "global_load_lds_dwordx4 v144, s[60:61] offset:0\n"
        "v_mfma_f32_16x16x32_bf16 v[136:139], a[56:59], a[224:227], v[136:139]\n"
        "ds_read_b128 a[212:215], v152 offset:1152\n"
        "v_add_u32 v244, 0x10000, v254\n"
        "v_mfma_f32_16x16x32_bf16 v[136:139], a[52:55], a[228:231], v[136:139]\n"
        "ds_read_b64_tr_b16 a[120:121], v153 offset:24576\n"
        "v_add_u32 v245, 0x20000, v254\n"
        "v_mfma_f32_16x16x32_bf16 v[136:139], a[48:51], a[232:235], v[136:139]\n"
        "ds_read_b64_tr_b16 a[122:123], v153 offset:28672\n"
        "v_lshrrev_b32 v234, 1, v254\n"
        "v_mfma_f32_16x16x32_bf16 v[136:139], a[44:47], a[236:239], v[136:139]\n"
        "ds_read_b64_tr_b16 a[124:125], v154 offset:24576\n"
        "global_load_lds_dwordx4 v145, s[60:61] offset:1024\n"
        "s_add_u32 s14, s51, 504\n"
        "v_mfma_f32_16x16x32_bf16 v[136:139], a[40:43], a[240:243], v[136:139]\n"
        "ds_read_b64_tr_b16 a[126:127], v154 offset:28672\n"
        "v_lshlrev_b32 v229, 5, v232\n"
        "v_mfma_f32_16x16x32_bf16 v[136:139], a[36:39], a[244:247], v[136:139]\n"
        "ds_read2_b32 a[24:25], v151 offset0:32 offset1:33\n"
        "v_lshl_add_u32 v229, v233, 3, v229\n"
        "v_mfma_f32_16x16x32_bf16 v[136:139], a[32:35], a[248:251], v[136:139]\n"
        "ds_read2_b32 a[26:27], v151 offset0:34 offset1:35\n"
        "v_sub_u32 v229, s14, v229\n"
        "v_mfma_f32_16x16x32_bf16 v[140:143], a[32:35], a[252:255], v[140:143]\n"
        "ds_read_b128 a[216:219], v152 offset:640\n"
        "global_load_lds_dwordx4 v146, s[60:61] offset:2048\n"
        "s_lshl_b32 s14, s41, 5\n"
        "v_mfma_f32_16x16x32_bf16 v[140:143], a[36:39], a[248:251], v[140:143]\n"
        "ds_read2_b32 a[28:29], v151 offset0:160 offset1:161\n"
        "s_add_u32 s14, s14, s42\n"
        "s_add_u32 s14, s14, 512\n"
        "v_mfma_f32_16x16x32_bf16 v[140:143], a[40:43], a[244:247], v[140:143]\n"
        "ds_read2_b32 a[30:31], v151 offset0:162 offset1:163\n"
        "s_lshl_b32 s14, s14, 7\n"
        "v_add_u32 v234, s14, v234\n"
        "v_mfma_f32_16x16x32_bf16 v[140:143], a[44:47], a[240:243], v[140:143]\n"
        "ds_read_b128 a[220:223], v152 offset:128\n"
        "s_lshr_b32 s14, s63, 2\n"
        "s_add_u32 s14, s14, 512\n"
        "v_mfma_f32_16x16x32_bf16 v[140:143], a[48:51], a[236:239], v[140:143]\n"
        "ds_read2_b32 a[32:33], v148 offset0:48 offset1:49\n"
        "global_load_lds_dwordx4 v147, s[60:61] offset:3072\n"
        "s_add_u32 m0, s73, 0xc000\n"
        "v_mfma_f32_16x16x32_bf16 v[140:143], a[52:55], a[232:235], v[140:143]\n"
        "ds_read2_b32 a[34:35], v148 offset0:50 offset1:51\n"
        "s_add_u32 s60, s22, s66\n"
        "s_addc_u32 s61, s23, 0\n"
        "v_mfma_f32_16x16x32_bf16 v[140:143], a[56:59], a[228:231], v[140:143]\n"
        "ds_read2_b32 a[36:37], v148 offset0:176 offset1:177\n"
        "s_xor_b32 s66, s29, 0xc0000\n"
        "s_lshl_b32 s14, s14, 7\n"
        "v_mfma_f32_16x16x32_bf16 v[140:143], a[60:63], a[224:227], v[140:143]\n"
        "ds_read_b128 a[224:227], v231 offset:0\n"
        "s_waitcnt lgkmcnt(10)\n"
        "v_mfma_f32_16x16x32_bf16 v[96:99], a[0:3], a[192:195], v[96:99]\n"
        "s_barrier\n"
        "v_mfma_f32_16x16x32_bf16 v[100:103], a[0:3], a[196:199], v[100:103]\n"
        "ds_read_b64_tr_b16 a[128:129], v155 offset:0\n"
        "global_load_lds_dwordx4 v144, s[60:61] offset:0\n"
        "v_mfma_f32_16x16x32_bf16 v[100:103], a[4:7], a[192:195], v[100:103]\n"
        "ds_read_b64_tr_b16 a[130:131], v155 offset:4096\n"
        "v_add_u32 v231, 3904, v152\n"
        "v_mfma_f32_16x16x32_bf16 v[104:107], a[8:11], a[192:195], v[104:107]\n"
        "ds_read_b64_tr_b16 a[132:133], v156 offset:0\n"
        "v_min_u32 v231, s16, v231\n"
        "v_mfma_f32_16x16x32_bf16 v[104:107], a[4:7], a[196:199], v[104:107]\n"
        "ds_read_b64_tr_b16 a[134:135], v156 offset:4096\n"
        "v_mfma_f32_16x16x32_bf16 v[104:107], a[0:3], a[200:203], v[104:107]\n"
        "ds_read2_b32 a[38:39], v148 offset0:178 offset1:179\n"
        "global_load_lds_dwordx4 v145, s[60:61] offset:1024\n"
        "v_mfma_f32_16x16x32_bf16 v[108:111], a[0:3], a[204:207], v[108:111]\n"
        "ds_read_b128 a[228:231], v152 offset:3264\n"
        "v_mfma_f32_16x16x32_bf16 v[108:111], a[4:7], a[200:203], v[108:111]\n"
        "ds_read_b64_tr_b16 a[136:137], v155 offset:8192\n"
        "v_mfma_f32_16x16x32_bf16 v[108:111], a[8:11], a[196:199], v[108:111]\n"
        "ds_read_b64_tr_b16 a[138:139], v155 offset:12288\n"
        "v_mfma_f32_16x16x32_bf16 v[108:111], a[12:15], a[192:195], v[108:111]\n"
        "ds_read2_b32 a[40:41], v149 offset0:48 offset1:49\n"
        "global_load_lds_dwordx4 v146, s[60:61] offset:2048\n"
        "v_mfma_f32_16x16x32_bf16 v[128:131], a[16:19], a[192:195], v[128:131]\n"
        "ds_read2_b32 a[42:43], v149 offset0:50 offset1:51\n"
        "v_mfma_f32_16x16x32_bf16 v[128:131], a[12:15], a[196:199], v[128:131]\n"
        "ds_read_b128 a[232:235], v152 offset:2752\n"
        "v_mfma_f32_16x16x32_bf16 v[128:131], a[8:11], a[200:203], v[128:131]\n"
        "ds_read_b64_tr_b16 a[140:141], v156 offset:8192\n"
        "v_mfma_f32_16x16x32_bf16 v[128:131], a[4:7], a[204:207], v[128:131]\n"
        "ds_read_b64_tr_b16 a[142:143], v156 offset:12288\n"
        "global_load_lds_dwordx4 v147, s[60:61] offset:3072\n"
        "s_add_u32 m0, s73, 0x0\n"
        "v_mfma_f32_16x16x32_bf16 v[128:131], a[0:3], a[208:211], v[128:131]\n"
        "ds_read2_b32 a[44:45], v149 offset0:176 offset1:177\n"
        "s_add_u32 s60, s22, s66\n"
        "s_addc_u32 s61, s23, 0\n"
        "v_mfma_f32_16x16x32_bf16 v[132:135], a[0:3], a[212:215], v[132:135]\n"
        "ds_read2_b32 a[46:47], v149 offset0:178 offset1:179\n"
        "s_xor_b32 s66, s29, 0xe0000\n"
        "v_mfma_f32_16x16x32_bf16 v[132:135], a[4:7], a[208:211], v[132:135]\n"
        "ds_read_b128 a[236:239], v152 offset:2240\n"
        "v_mfma_f32_16x16x32_bf16 v[132:135], a[8:11], a[204:207], v[132:135]\n"
        "ds_read_b64_tr_b16 a[144:145], v155 offset:16384\n"
        "global_load_lds_dwordx4 v144, s[60:61] offset:0\n"
        "v_mfma_f32_16x16x32_bf16 v[132:135], a[12:15], a[200:203], v[132:135]\n"
        "ds_read_b64_tr_b16 a[146:147], v155 offset:20480\n"
        "s_waitcnt lgkmcnt(8)\n"
        "v_mfma_f32_16x16x32_bf16 v[132:135], a[16:19], a[196:199], v[132:135]\n"
        "ds_read2_b32 a[48:49], v150 offset0:48 offset1:49\n"
        "v_mfma_f32_16x16x32_bf16 v[132:135], a[20:23], a[192:195], v[132:135]\n"
        "ds_read2_b32 a[50:51], v150 offset0:50 offset1:51\n"
        "v_mfma_f32_16x16x32_bf16 v[136:139], a[24:27], a[192:195], v[136:139]\n"
        "ds_read_b128 a[240:243], v152 offset:1728\n"
        "global_load_lds_dwordx4 v145, s[60:61] offset:1024\n"
        "v_mfma_f32_16x16x32_bf16 v[136:139], a[20:23], a[196:199], v[136:139]\n"
        "ds_read_b64_tr_b16 a[148:149], v156 offset:16384\n"
        "v_mfma_f32_16x16x32_bf16 v[136:139], a[16:19], a[200:203], v[136:139]\n"
        "ds_read_b64_tr_b16 a[150:151], v156 offset:20480\n"
        "v_mfma_f32_16x16x32_bf16 v[136:139], a[12:15], a[204:207], v[136:139]\n"
        "ds_read2_b32 a[52:53], v150 offset0:176 offset1:177\n"
        "v_mfma_f32_16x16x32_bf16 v[136:139], a[8:11], a[208:211], v[136:139]\n"
        "ds_read2_b32 a[54:55], v150 offset0:178 offset1:179\n"
        "global_load_lds_dwordx4 v146, s[60:61] offset:2048\n"
        "v_mfma_f32_16x16x32_bf16 v[136:139], a[4:7], a[212:215], v[136:139]\n"
        "ds_read_b128 a[244:247], v152 offset:1216\n"
        "v_mfma_f32_16x16x32_bf16 v[136:139], a[0:3], a[216:219], v[136:139]\n"
        "ds_read_b64_tr_b16 a[152:153], v155 offset:24576\n"
        "v_mfma_f32_16x16x32_bf16 v[140:143], a[0:3], a[220:223], v[140:143]\n"
        "ds_read_b64_tr_b16 a[154:155], v155 offset:28672\n"
        "v_mfma_f32_16x16x32_bf16 v[140:143], a[4:7], a[216:219], v[140:143]\n"
        "ds_read_b64_tr_b16 a[156:157], v156 offset:24576\n"
        "global_load_lds_dwordx4 v147, s[60:61] offset:3072\n"
        "s_add_u32 m0, s73, 0x4000\n"
        "v_mfma_f32_16x16x32_bf16 v[140:143], a[8:11], a[212:215], v[140:143]\n"
        "ds_read_b64_tr_b16 a[158:159], v156 offset:28672\n"
        "s_add_u32 s60, s22, s66\n"
        "s_addc_u32 s61, s23, 0\n"
        "v_mfma_f32_16x16x32_bf16 v[140:143], a[12:15], a[208:211], v[140:143]\n"
        "ds_read2_b32 a[56:57], v151 offset0:48 offset1:49\n"
        "s_xor_b32 s66, s29, 0x100000\n"
        "v_mfma_f32_16x16x32_bf16 v[140:143], a[16:19], a[204:207], v[140:143]\n"
        "ds_read2_b32 a[58:59], v151 offset0:50 offset1:51\n"
        "v_mfma_f32_16x16x32_bf16 v[140:143], a[20:23], a[200:203], v[140:143]\n"
        "ds_read_b128 a[248:251], v152 offset:704\n"
        "global_load_lds_dwordx4 v144, s[60:61] offset:0\n"
        "v_mfma_f32_16x16x32_bf16 v[140:143], a[24:27], a[196:199], v[140:143]\n"
        "ds_read2_b32 a[60:61], v151 offset0:176 offset1:177\n"
        "v_mfma_f32_16x16x32_bf16 v[140:143], a[28:31], a[192:195], v[140:143]\n"
        "ds_read2_b32 a[62:63], v151 offset0:178 offset1:179\n"
        "v_mfma_f32_16x16x32_bf16 v[96:99], a[32:35], a[224:227], v[96:99]\n"
        "ds_read_b128 a[252:255], v152 offset:192\n"
        "v_mfma_f32_16x16x32_bf16 v[100:103], a[32:35], a[228:231], v[100:103]\n"
        "ds_read2_b32 a[0:1], v148 offset0:64 offset1:65\n"
        "global_load_lds_dwordx4 v145, s[60:61] offset:1024\n"
        "v_mfma_f32_16x16x32_bf16 v[100:103], a[36:39], a[224:227], v[100:103]\n"
        "ds_read2_b32 a[2:3], v148 offset0:66 offset1:67\n"
        "s_waitcnt lgkmcnt(8)\n"
        "v_mfma_f32_16x16x32_bf16 v[104:107], a[40:43], a[224:227], v[104:107]\n"
        "ds_read_b128 a[192:195], v230 offset:0\n"
        "v_add_u32 v230, 3968, v152\n"
        "v_mfma_f32_16x16x32_bf16 v[104:107], a[36:39], a[228:231], v[104:107]\n"
        "ds_read2_b32 a[4:5], v148 offset0:192 offset1:193\n"
        "v_min_u32 v230, s16, v230\n"
        "v_mfma_f32_16x16x32_bf16 v[104:107], a[32:35], a[232:235], v[104:107]\n"
        "ds_read2_b32 a[6:7], v148 offset0:194 offset1:195\n"
        "global_load_lds_dwordx4 v146, s[60:61] offset:2048\n"
        "v_mfma_f32_16x16x32_bf16 v[108:111], a[32:35], a[236:239], v[108:111]\n"
        "ds_read_b128 a[196:199], v152 offset:3328\n"
        "v_mfma_f32_16x16x32_bf16 v[108:111], a[36:39], a[232:235], v[108:111]\n"
        "ds_read2_b32 a[8:9], v149 offset0:64 offset1:65\n"
        "v_mfma_f32_16x16x32_bf16 v[108:111], a[40:43], a[228:231], v[108:111]\n"
        "ds_read2_b32 a[10:11], v149 offset0:66 offset1:67\n"
        "v_mfma_f32_16x16x32_bf16 v[108:111], a[44:47], a[224:227], v[108:111]\n"
        "LHC2_%=:\n"
        "s_cbranch_execz LHRC_%=\n"
        "ds_read_b128 a[200:203], v152 offset:2816\n"
        "global_load_lds_dwordx4 v147, s[60:61] offset:3072\n"
        "s_add_u32 m0, s73, 0x10000\n"
        "v_mfma_f32_16x16x32_bf16 v[128:131], a[48:51], a[224:227], v[128:131]\n"
        "ds_read2_b32 a[12:13], v149 offset0:192 offset1:193\n"
        "s_add_u32 s60, s22, s66\n"
        "s_addc_u32 s61, s23, 0\n"
        "v_mfma_f32_16x16x32_bf16 v[128:131], a[44:47], a[228:231], v[128:131]\n"
        "ds_read2_b32 a[14:15], v149 offset0:194 offset1:195\n"
        "s_xor_b32 s66, s29, 0x120000\n"
        "v_mfma_f32_16x16x32_bf16 v[128:131], a[40:43], a[232:235], v[128:131]\n"
        "ds_read_b128 a[204:207], v152 offset:2304\n"
        "v_mfma_f32_16x16x32_bf16 v[128:131], a[36:39], a[236:239], v[128:131]\n"
        "ds_read2_b32 a[16:17], v150 offset0:64 offset1:65\n"
        "v_mfma_f32_16x16x32_bf16 v[128:131], a[32:35], a[240:243], v[128:131]\n"
        "ds_read2_b32 a[18:19], v150 offset0:66 offset1:67\n"
        "v_mfma_f32_16x16x32_bf16 v[132:135], a[32:35], a[244:247], v[132:135]\n"
        "ds_read_b128 a[208:211], v152 offset:1792\n"
        "s_waitcnt vmcnt(8)\n"
        "v_mfma_f32_16x16x32_bf16 v[132:135], a[36:39], a[240:243], v[132:135]\n"
        "s_barrier\n"
        "v_mfma_f32_16x16x32_bf16 v[132:135], a[40:43], a[236:239], v[132:135]\n"
        "ds_read_b64_tr_b16 v[160:161], v153 offset:32768\n"
        "global_load_lds_dwordx4 v144, s[60:61] offset:0\n"
        "v_mfma_f32_16x16x32_bf16 v[132:135], a[44:47], a[232:235], v[132:135]\n"
        "ds_read_b64_tr_b16 v[162:163], v153 offset:36864\n"
        "s_waitcnt lgkmcnt(7)\n"
        "v_mfma_f32_16x16x32_bf16 v[132:135], a[48:51], a[228:231], v[132:135]\n"
        "ds_read_b64_tr_b16 v[164:165], v154 offset:32768\n"
        "v_mfma_f32_16x16x32_bf16 v[132:135], a[52:55], a[224:227], v[132:135]\n"
        "ds_read_b64_tr_b16 v[166:167], v154 offset:36864\n"
        "v_mfma_f32_16x16x32_bf16 v[136:139], a[56:59], a[224:227], v[136:139]\n"
        "ds_read_b64_tr_b16 v[168:169], v153 offset:40960\n"
        "global_load_lds_dwordx4 v145, s[60:61] offset:1024\n"
        "v_mfma_f32_16x16x32_bf16 v[136:139], a[52:55], a[228:231], v[136:139]\n"
        "ds_read_b64_tr_b16 v[170:171], v153 offset:45056\n"
        "v_mfma_f32_16x16x32_bf16 v[136:139], a[48:51], a[232:235], v[136:139]\n"
        "ds_read_b64_tr_b16 v[172:173], v154 offset:40960\n"
        "v_mfma_f32_16x16x32_bf16 v[136:139], a[44:47], a[236:239], v[136:139]\n"
        "ds_read_b64_tr_b16 v[174:175], v154 offset:45056\n"
        "v_mfma_f32_16x16x32_bf16 v[136:139], a[40:43], a[240:243], v[136:139]\n"
        "ds_read_b64_tr_b16 v[176:177], v153 offset:49152\n"
        "global_load_lds_dwordx4 v146, s[60:61] offset:2048\n"
        "v_mfma_f32_16x16x32_bf16 v[136:139], a[36:39], a[244:247], v[136:139]\n"
        "ds_read_b64_tr_b16 v[178:179], v153 offset:53248\n"
        "v_mfma_f32_16x16x32_bf16 v[136:139], a[32:35], a[248:251], v[136:139]\n"
        "ds_read_b64_tr_b16 v[180:181], v154 offset:49152\n"
        "v_mfma_f32_16x16x32_bf16 v[140:143], a[32:35], a[252:255], v[140:143]\n"
        "ds_read_b64_tr_b16 v[182:183], v154 offset:53248\n"
        "v_mfma_f32_16x16x32_bf16 v[140:143], a[36:39], a[248:251], v[140:143]\n"
        "ds_read2_b32 a[20:21], v150 offset0:192 offset1:193\n"
        "global_load_lds_dwordx4 v147, s[60:61] offset:3072\n"
        "s_add_u32 m0, s73, 0x14000\n"
        "v_mfma_f32_16x16x32_bf16 v[140:143], a[40:43], a[244:247], v[140:143]\n"
        "ds_read2_b32 a[22:23], v150 offset0:194 offset1:195\n"
        "s_add_u32 s60, s22, s66\n"
        "s_addc_u32 s61, s23, 0\n"
        "v_mfma_f32_16x16x32_bf16 v[140:143], a[44:47], a[240:243], v[140:143]\n"
        "ds_read_b128 a[212:215], v152 offset:1280\n"
        "s_xor_b32 s66, s29, 0x140000\n"
        "v_mfma_f32_16x16x32_bf16 v[140:143], a[48:51], a[236:239], v[140:143]\n"
        "ds_read_b64_tr_b16 v[184:185], v153 offset:57344\n"
        "v_mfma_f32_16x16x32_bf16 v[140:143], a[52:55], a[232:235], v[140:143]\n"
        "ds_read_b64_tr_b16 v[186:187], v153 offset:61440\n"
        "global_load_lds_dwordx4 v144, s[60:61] offset:0\n"
        "v_mfma_f32_16x16x32_bf16 v[140:143], a[56:59], a[228:231], v[140:143]\n"
        "ds_read_b64_tr_b16 v[188:189], v154 offset:57344\n"
        "v_mfma_f32_16x16x32_bf16 v[140:143], a[60:63], a[224:227], v[140:143]\n"
        "ds_read_b64_tr_b16 v[190:191], v154 offset:61440\n"
        "v_mfma_f32_16x16x32_bf16 v[96:99], a[0:3], a[192:195], v[96:99]\n"
        "ds_read2_b32 a[24:25], v151 offset0:64 offset1:65\n"
        "v_mfma_f32_16x16x32_bf16 v[100:103], a[0:3], a[196:199], v[100:103]\n"
        "ds_read2_b32 a[26:27], v151 offset0:66 offset1:67\n"
        "global_load_lds_dwordx4 v145, s[60:61] offset:1024\n"
        "v_mfma_f32_16x16x32_bf16 v[100:103], a[4:7], a[192:195], v[100:103]\n"
        "ds_read_b128 a[216:219], v152 offset:768\n"
        "s_waitcnt lgkmcnt(8)\n"
        "v_mfma_f32_16x16x32_bf16 v[104:107], a[8:11], a[192:195], v[104:107]\n"
        "ds_read2_b32 a[28:29], v151 offset0:192 offset1:193\n"
        "v_mfma_f32_16x16x32_bf16 v[104:107], a[4:7], a[196:199], v[104:107]\n"
        "ds_read2_b32 a[30:31], v151 offset0:194 offset1:195\n"
        "v_mfma_f32_16x16x32_bf16 v[104:107], a[0:3], a[200:203], v[104:107]\n"
        "ds_read_b128 a[220:223], v152 offset:256\n"
        "global_load_lds_dwordx4 v146, s[60:61] offset:2048\n"
        "v_mfma_f32_16x16x32_bf16 v[108:111], a[0:3], a[204:207], v[108:111]\n"
        "ds_read2_b32 a[32:33], v148 offset0:80 offset1:81\n"
        "v_mfma_f32_16x16x32_bf16 v[108:111], a[4:7], a[200:203], v[108:111]\n"
        "ds_read2_b32 a[34:35], v148 offset0:82 offset1:83\n"
        "v_mfma_f32_16x16x32_bf16 v[108:111], a[8:11], a[196:199], v[108:111]\n"
        "ds_read_b128 a[224:227], v231 offset:0\n"
        "v_add_u32 v231, 4032, v152\n"
        "v_mfma_f32_16x16x32_bf16 v[108:111], a[12:15], a[192:195], v[108:111]\n"
        "ds_read2_b32 a[36:37], v148 offset0:208 offset1:209\n"
        "global_load_lds_dwordx4 v147, s[60:61] offset:3072\n"
        "s_add_u32 m0, s73, 0x8000\n"
        "v_mfma_f32_16x16x32_bf16 v[128:131], a[16:19], a[192:195], v[128:131]\n"
        "ds_read2_b32 a[38:39], v148 offset0:210 offset1:211\n"
        "s_add_u32 s60, s22, s66\n"
        "s_addc_u32 s61, s23, 0\n"
        "v_mfma_f32_16x16x32_bf16 v[128:131], a[12:15], a[196:199], v[128:131]\n"
        "ds_read_b128 a[228:231], v152 offset:3392\n"
        "s_waitcnt vmcnt(8) lgkmcnt(12)\n"
        "v_mfma_f32_16x16x32_bf16 v[128:131], a[8:11], a[200:203], v[128:131]\n"
        "s_barrier\n"
        "v_mfma_f32_16x16x32_bf16 v[128:131], a[4:7], a[204:207], v[128:131]\n"
        "ds_read_b64_tr_b16 v[192:193], v153 offset:0\n"
        "global_load_lds_dwordx4 v144, s[60:61] offset:0\n"
        "s_xor_b32 s66, s29, 0x160000\n"
        "v_mfma_f32_16x16x32_bf16 v[128:131], a[0:3], a[208:211], v[128:131]\n"
        "ds_read_b64_tr_b16 v[194:195], v153 offset:4096\n"
        "v_min_u32 v231, s16, v231\n"
        "v_mfma_f32_16x16x32_bf16 v[132:135], a[0:3], a[212:215], v[132:135]\n"
        "ds_read_b64_tr_b16 v[196:197], v154 offset:0\n"
        "v_mfma_f32_16x16x32_bf16 v[132:135], a[4:7], a[208:211], v[132:135]\n"
        "ds_read_b64_tr_b16 v[198:199], v154 offset:4096\n"
        "v_mfma_f32_16x16x32_bf16 v[132:135], a[8:11], a[204:207], v[132:135]\n"
        "ds_read_b64_tr_b16 v[200:201], v153 offset:8192\n"
        "global_load_lds_dwordx4 v145, s[60:61] offset:1024\n"
        "v_mfma_f32_16x16x32_bf16 v[132:135], a[12:15], a[200:203], v[132:135]\n"
        "ds_read_b64_tr_b16 v[202:203], v153 offset:12288\n"
        "s_waitcnt lgkmcnt(7)\n"
        "v_mfma_f32_16x16x32_bf16 v[132:135], a[16:19], a[196:199], v[132:135]\n"
        "ds_read2_b32 a[40:41], v149 offset0:80 offset1:81\n"
        "v_mfma_f32_16x16x32_bf16 v[132:135], a[20:23], a[192:195], v[132:135]\n"
        "ds_read2_b32 a[42:43], v149 offset0:82 offset1:83\n"
        "v_mfma_f32_16x16x32_bf16 v[136:139], a[24:27], a[192:195], v[136:139]\n"
        "ds_read_b128 a[232:235], v152 offset:2880\n"
        "global_load_lds_dwordx4 v146, s[60:61] offset:2048\n"
        "v_mfma_f32_16x16x32_bf16 v[136:139], a[20:23], a[196:199], v[136:139]\n"
        "ds_read_b64_tr_b16 v[204:205], v154 offset:8192\n"
        "v_mfma_f32_16x16x32_bf16 v[136:139], a[16:19], a[200:203], v[136:139]\n"
        "ds_read_b64_tr_b16 v[206:207], v154 offset:12288\n"
        "v_mfma_f32_16x16x32_bf16 v[136:139], a[12:15], a[204:207], v[136:139]\n"
        "ds_read2_b32 a[44:45], v149 offset0:208 offset1:209\n"
        "v_mfma_f32_16x16x32_bf16 v[136:139], a[8:11], a[208:211], v[136:139]\n"
        "ds_read2_b32 a[46:47], v149 offset0:210 offset1:211\n"
        "global_load_lds_dwordx4 v147, s[60:61] offset:3072\n"
        "s_add_u32 m0, s73, 0xc000\n"
        "v_mfma_f32_16x16x32_bf16 v[136:139], a[4:7], a[212:215], v[136:139]\n"
        "ds_read_b128 a[236:239], v152 offset:2368\n"
        "s_add_u32 s60, s22, s66\n"
        "s_addc_u32 s61, s23, 0\n"
        "v_mfma_f32_16x16x32_bf16 v[136:139], a[0:3], a[216:219], v[136:139]\n"
        "ds_read_b64_tr_b16 v[208:209], v153 offset:16384\n"
        "s_xor_b32 s66, s29, 0x180000\n"
        "v_mfma_f32_16x16x32_bf16 v[140:143], a[0:3], a[220:223], v[140:143]\n"
        "ds_read_b64_tr_b16 v[210:211], v153 offset:20480\n"
        "v_mfma_f32_16x16x32_bf16 v[140:143], a[4:7], a[216:219], v[140:143]\n"
        "ds_read2_b32 a[48:49], v150 offset0:80 offset1:81\n"
        "global_load_lds_dwordx4 v144, s[60:61] offset:0\n"
        "v_mfma_f32_16x16x32_bf16 v[140:143], a[8:11], a[212:215], v[140:143]\n"
        "ds_read2_b32 a[50:51], v150 offset0:82 offset1:83\n"
        "v_mfma_f32_16x16x32_bf16 v[140:143], a[12:15], a[208:211], v[140:143]\n"
        "ds_read_b128 a[240:243], v152 offset:1856\n"
        "v_mfma_f32_16x16x32_bf16 v[140:143], a[16:19], a[204:207], v[140:143]\n"
        "ds_read_b64_tr_b16 v[212:213], v154 offset:16384\n"
        "v_mfma_f32_16x16x32_bf16 v[140:143], a[20:23], a[200:203], v[140:143]\n"
        "ds_read_b64_tr_b16 v[214:215], v154 offset:20480\n"
        "global_load_lds_dwordx4 v145, s[60:61] offset:1024\n"
        "v_mfma_f32_16x16x32_bf16 v[140:143], a[24:27], a[196:199], v[140:143]\n"
        "ds_read2_b32 a[52:53], v150 offset0:208 offset1:209\n"
        "s_waitcnt lgkmcnt(8)\n"
        "v_mfma_f32_16x16x32_bf16 v[140:143], a[28:31], a[192:195], v[140:143]\n"
        "ds_read2_b32 a[54:55], v150 offset0:210 offset1:211\n"
        "v_mfma_f32_16x16x32_bf16 v[96:99], a[32:35], a[224:227], v[96:99]\n"
        "ds_read_b128 a[244:247], v152 offset:1344\n"
        "v_mfma_f32_16x16x32_bf16 v[100:103], a[32:35], a[228:231], v[100:103]\n"
        "ds_read_b64_tr_b16 v[216:217], v153 offset:24576\n"
        "global_load_lds_dwordx4 v146, s[60:61] offset:2048\n"
        "v_mfma_f32_16x16x32_bf16 v[100:103], a[36:39], a[224:227], v[100:103]\n"
        "ds_read_b64_tr_b16 v[218:219], v153 offset:28672\n"
        "v_mfma_f32_16x16x32_bf16 v[104:107], a[40:43], a[224:227], v[104:107]\n"
        "ds_read2_b32 a[56:57], v151 offset0:80 offset1:81\n"
        "v_mfma_f32_16x16x32_bf16 v[104:107], a[36:39], a[228:231], v[104:107]\n"
        "ds_read2_b32 a[58:59], v151 offset0:82 offset1:83\n"
        "v_mfma_f32_16x16x32_bf16 v[104:107], a[32:35], a[232:235], v[104:107]\n"
        "ds_read_b128 a[248:251], v152 offset:832\n"
        "global_load_lds_dwordx4 v147, s[60:61] offset:3072\n"
        "s_add_u32 m0, s73, 0x0\n"
        "v_mfma_f32_16x16x32_bf16 v[108:111], a[32:35], a[236:239], v[108:111]\n"
        "ds_read_b64_tr_b16 v[220:221], v154 offset:24576\n"
        "s_add_u32 s60, s22, s66\n"
        "s_addc_u32 s61, s23, 0\n"
        "v_mfma_f32_16x16x32_bf16 v[108:111], a[36:39], a[232:235], v[108:111]\n"
        "ds_read_b64_tr_b16 v[222:223], v154 offset:28672\n"
        "s_xor_b32 s66, s29, 0x1a0000\n"
        "s_waitcnt lgkmcnt(8)\n"
        "v_mfma_f32_16x16x32_bf16 v[108:111], a[40:43], a[228:231], v[108:111]\n"
        "ds_read2_b32 a[60:61], v151 offset0:208 offset1:209\n"
        "v_mfma_f32_16x16x32_bf16 v[108:111], a[44:47], a[224:227], v[108:111]\n"
        "ds_read2_b32 a[62:63], v151 offset0:210 offset1:211\n"
        "global_load_dwordx2 v[236:237], v159, s[48:49] sc1\n"
        "v_mfma_f32_16x16x32_bf16 v[128:131], a[48:51], a[224:227], v[128:131]\n"
        "ds_read_b128 a[252:255], v152 offset:320\n"
        "v_mfma_f32_16x16x32_bf16 v[128:131], a[44:47], a[228:231], v[128:131]\n"
        "s_mov_b64 exec, 0\n"
        "s_cmp_eq_u32 s40, 0\n"
        "s_cbranch_scc1 LHB0_%=\n"
        "s_cmp_eq_u32 s40, 1\n"
        "s_cbranch_scc1 LHB1_%=\n"
        "s_cmp_eq_u32 s40, 2\n"
        "s_cbranch_scc1 LHB2_%=\n"
        "s_branch LHB3_%=\n"
        "LHRB_%=:\n"
        "s_mov_b64 exec, -1\n"
        "v_mfma_f32_16x16x32_bf16 v[128:131], a[40:43], a[232:235], v[128:131]\n"
        "ds_read2_b32 a[0:1], v148 offset0:96 offset1:97\n"
        "s_waitcnt lgkmcnt(7)\n"
        "v_mfma_f32_16x16x32_bf16 v[128:131], a[36:39], a[236:239], v[128:131]\n"
        "ds_read2_b32 a[2:3], v148 offset0:98 offset1:99\n"
        "v_mfma_f32_16x16x32_bf16 v[128:131], a[32:35], a[240:243], v[128:131]\n"
        "ds_read_b128 a[192:195], v230 offset:0\n"
        "s_waitcnt vmcnt(9) lgkmcnt(6)\n"
        "v_mfma_f32_16x16x32_bf16 v[132:135], a[32:35], a[244:247], v[132:135]\n"
        "s_barrier\n"
        "v_mfma_f32_16x16x32_bf16 v[132:135], a[36:39], a[240:243], v[132:135]\n"
        "ds_read_b64_tr_b16 v[112:113], v155 offset:0\n"
        "global_load_lds_dwordx4 v144, s[60:61] offset:0\n"
        "v_mfma_f32_16x16x32_bf16 v[132:135], a[40:43], a[236:239], v[132:135]\n"
        "ds_read_b64_tr_b16 v[114:115], v155 offset:4096\n"
        "v_lshrrev_b32 v230, 1, v233\n"
        "v_mfma_f32_16x16x32_bf16 v[132:135], a[44:47], a[232:235], v[132:135]\n"
        "ds_read_b64_tr_b16 v[116:117], v156 offset:0\n"
        "v_mfma_f32_16x16x32_bf16 v[132:135], a[48:51], a[228:231], v[132:135]\n"
        "ds_read_b64_tr_b16 v[118:119], v156 offset:4096\n"
        "v_mfma_f32_16x16x32_bf16 v[132:135], a[52:55], a[224:227], v[132:135]\n"
        "ds_read2_b32 a[4:5], v148 offset0:224 offset1:225\n"
        "global_load_lds_dwordx4 v145, s[60:61] offset:1024\n"
        "v_mfma_f32_16x16x32_bf16 v[136:139], a[56:59], a[224:227], v[136:139]\n"
        "ds_read2_b32 a[6:7], v148 offset0:226 offset1:227\n"
        "v_mfma_f32_16x16x32_bf16 v[136:139], a[52:55], a[228:231], v[136:139]\n"
        "ds_read_b128 a[196:199], v152 offset:3456\n"
        "v_mfma_f32_16x16x32_bf16 v[136:139], a[48:51], a[232:235], v[136:139]\n"
        "ds_read_b64_tr_b16 v[120:121], v155 offset:8192\n"
        "v_mfma_f32_16x16x32_bf16 v[136:139], a[44:47], a[236:239], v[136:139]\n"
        "ds_read_b64_tr_b16 v[122:123], v155 offset:12288\n"
        "global_load_lds_dwordx4 v146, s[60:61] offset:2048\n"
        "v_mfma_f32_16x16x32_bf16 v[136:139], a[40:43], a[240:243], v[136:139]\n"
        "ds_read2_b32 a[8:9], v149 offset0:96 offset1:97\n"
        "s_waitcnt lgkmcnt(8)\n"
        "v_mfma_f32_16x16x32_bf16 v[136:139], a[36:39], a[244:247], v[136:139]\n"
        "ds_read2_b32 a[10:11], v149 offset0:98 offset1:99\n"
        "v_mfma_f32_16x16x32_bf16 v[136:139], a[32:35], a[248:251], v[136:139]\n"
        "ds_read_b128 a[200:203], v152 offset:2944\n"
        "v_mfma_f32_16x16x32_bf16 v[140:143], a[32:35], a[252:255], v[140:143]\n"
        "ds_read_b64_tr_b16 v[124:125], v156 offset:8192\n"
        "global_load_lds_dwordx4 v147, s[60:61] offset:3072\n"
        "s_add_u32 m0, s73, 0x4000\n"
        "v_mfma_f32_16x16x32_bf16 v[140:143], a[36:39], a[248:251], v[140:143]\n"
        "ds_read_b64_tr_b16 v[126:127], v156 offset:12288\n"
        "s_add_u32 s60, s22, s66\n"
        "s_addc_u32 s61, s23, 0\n"
        "v_mfma_f32_16x16x32_bf16 v[140:143], a[40:43], a[244:247], v[140:143]\n"
        "ds_read2_b32 a[12:13], v149 offset0:224 offset1:225\n"
        "s_sub_u32 s66, 15, s12\n"
        "v_mfma_f32_16x16x32_bf16 v[140:143], a[44:47], a[240:243], v[140:143]\n"
        "ds_read2_b32 a[14:15], v149 offset0:226 offset1:227\n"
        "s_cmp_lt_u32 s74, 2\n"
        "s_cselect_b32 s66, s12, s66\n"
        "v_mfma_f32_16x16x32_bf16 v[140:143], a[48:51], a[236:239], v[140:143]\n"
        "ds_read_b128 a[204:207], v152 offset:2432\n"
        "global_load_lds_dwordx4 v144, s[60:61] offset:0\n"
        "s_mul_i32 s66, s66, 1052672\n"
        "v_mfma_f32_16x16x32_bf16 v[140:143], a[52:55], a[232:235], v[140:143]\n"
        "ds_read_b64_tr_b16 a[64:65], v155 offset:16384\n"
        "s_add_u32 s66, s66, s67\n"
        "s_and_b32 s67, s75, 1\n"
        "v_mfma_f32_16x16x32_bf16 v[140:143], a[56:59], a[228:231], v[140:143]\n"
        "ds_read_b64_tr_b16 a[66:67], v155 offset:20480\n"
        "s_add_u32 s68, s20, s66\n"
        "s_addc_u32 s69, s21, 0\n"
        "v_mfma_f32_16x16x32_bf16 v[140:143], a[60:63], a[224:227], v[140:143]\n"
        "ds_read2_b32 a[16:17], v150 offset0:96 offset1:97\n"
        "s_xor_b32 s66, s17, 0\n"
        "s_lshl_b32 s66, s66, 15\n"
        "v_mfma_f32_16x16x32_bf16 v[96:99], a[0:3], a[192:195], v[96:99]\n"
        "ds_read2_b32 a[18:19], v150 offset0:98 offset1:99\n"
        "s_lshl_b32 s67, s67, 12\n"
        "s_waitcnt lgkmcnt(8)\n"
        "v_mfma_f32_16x16x32_bf16 v[100:103], a[0:3], a[196:199], v[100:103]\n"
        "ds_read_b128 a[208:211], v152 offset:1920\n"
        "global_load_lds_dwordx4 v145, s[60:61] offset:1024\n"
        "v_mfma_f32_16x16x32_bf16 v[100:103], a[4:7], a[192:195], v[100:103]\n"
        "ds_read_b64_tr_b16 a[68:69], v156 offset:16384\n"
        "v_mfma_f32_16x16x32_bf16 v[104:107], a[8:11], a[192:195], v[104:107]\n"
        "ds_read_b64_tr_b16 a[70:71], v156 offset:20480\n"
        "v_mfma_f32_16x16x32_bf16 v[104:107], a[4:7], a[196:199], v[104:107]\n"
        "ds_read2_b32 a[20:21], v150 offset0:224 offset1:225\n"
        "s_waitcnt lgkmcnt(8)\n"
        "v_mfma_f32_16x16x32_bf16 v[104:107], a[0:3], a[200:203], v[104:107]\n"
        "ds_read2_b32 a[22:23], v150 offset0:226 offset1:227\n"
        "global_load_lds_dwordx4 v146, s[60:61] offset:2048\n"
        "v_mfma_f32_16x16x32_bf16 v[108:111], a[0:3], a[204:207], v[108:111]\n"
        "ds_read_b128 a[212:215], v152 offset:1408\n"
        "s_waitcnt vmcnt(7) lgkmcnt(8)\n"
        "v_mfma_f32_16x16x32_bf16 v[108:111], a[4:7], a[200:203], v[108:111]\n"
        "ds_read_b64_tr_b16 a[72:73], v155 offset:24576\n"
        "v_cmp_ne_u64 vcc, v[236:237], v[250:251]\n"
        "v_mfma_f32_16x16x32_bf16 v[108:111], a[8:11], a[196:199], v[108:111]\n"
        "ds_read_b64_tr_b16 a[74:75], v155 offset:28672\n"
        "s_cbranch_vccnz LPCX_%=\n"
        "LBCX_%=:\n"
        "s_waitcnt lgkmcnt(8)\n"
        "v_mfma_f32_16x16x32_bf16 v[108:111], a[12:15], a[192:195], v[108:111]\n"
        "ds_read2_b32 a[24:25], v151 offset0:96 offset1:97\n"
        "global_load_lds_dwordx4 v147, s[60:61] offset:3072\n"
        "s_add_u32 m0, s73, 0x10000\n"
        "v_mfma_f32_16x16x32_bf16 v[128:131], a[16:19], a[192:195], v[128:131]\n"
        "ds_read2_b32 a[26:27], v151 offset0:98 offset1:99\n"
        "s_add_u32 s60, s68, s66\n"
        "s_addc_u32 s61, s69, 0\n"
        "v_mfma_f32_16x16x32_bf16 v[128:131], a[12:15], a[196:199], v[128:131]\n"
        "ds_read_b128 a[216:219], v152 offset:896\n"
        "s_xor_b32 s66, s17, 1\n"
        "s_lshl_b32 s66, s66, 15\n"
        "v_mfma_f32_16x16x32_bf16 v[128:131], a[8:11], a[200:203], v[128:131]\n"
        "ds_read_b64_tr_b16 a[76:77], v156 offset:24576\n"
        "s_waitcnt lgkmcnt(8)\n"
        "v_mfma_f32_16x16x32_bf16 v[128:131], a[4:7], a[204:207], v[128:131]\n"
        "ds_read_b64_tr_b16 a[78:79], v156 offset:28672\n"
        "global_load_dword v224, v159, s[48:49] offset:64 sc1\n"
        "v_mfma_f32_16x16x32_bf16 v[128:131], a[0:3], a[208:211], v[128:131]\n"
        "ds_read2_b32 a[28:29], v151 offset0:224 offset1:225\n"
        "s_waitcnt lgkmcnt(8)\n"
        "v_mfma_f32_16x16x32_bf16 v[132:135], a[0:3], a[212:215], v[132:135]\n"
        "ds_read2_b32 a[30:31], v151 offset0:226 offset1:227\n"
        "v_mfma_f32_16x16x32_bf16 v[132:135], a[4:7], a[208:211], v[132:135]\n"
        "ds_read_b128 a[220:223], v152 offset:384\n"
        "v_mfma_f32_16x16x32_bf16 v[132:135], a[8:11], a[204:207], v[132:135]\n"
        "ds_read2_b32 a[32:33], v148 offset0:112 offset1:113\n"
        "v_mfma_f32_16x16x32_bf16 v[132:135], a[12:15], a[200:203], v[132:135]\n"
        "ds_read2_b32 a[34:35], v148 offset0:114 offset1:115\n"
        "s_waitcnt lgkmcnt(8)\n"
        "v_mfma_f32_16x16x32_bf16 v[132:135], a[16:19], a[196:199], v[132:135]\n"
        "ds_read_b128 a[224:227], v231 offset:0\n"
        "v_mfma_f32_16x16x32_bf16 v[132:135], a[20:23], a[192:195], v[132:135]\n"
        "ds_read2_b32 a[36:37], v148 offset0:240 offset1:241\n"
        "v_mfma_f32_16x16x32_bf16 v[136:139], a[24:27], a[192:195], v[136:139]\n"
        "ds_read2_b32 a[38:39], v148 offset0:242 offset1:243\n"
        "s_waitcnt lgkmcnt(8)\n"
        "v_mfma_f32_16x16x32_bf16 v[136:139], a[20:23], a[196:199], v[136:139]\n"
        "s_barrier\n"
        "v_mfma_f32_16x16x32_bf16 v[136:139], a[16:19], a[200:203], v[136:139]\n"
        "ds_read_b64_tr_b16 a[80:81], v153 offset:32768\n"
        "global_load_lds_dwordx4 v254, s[60:61] offset:0 sc1\n"
        "v_mfma_f32_16x16x32_bf16 v[136:139], a[12:15], a[204:207], v[136:139]\n"
        "ds_read_b64_tr_b16 a[82:83], v153 offset:36864\n"
        "s_waitcnt lgkmcnt(7)\n"
        "v_mfma_f32_16x16x32_bf16 v[136:139], a[8:11], a[208:211], v[136:139]\n"
        "ds_read_b64_tr_b16 a[84:85], v154 offset:32768\n"
        "v_mfma_f32_16x16x32_bf16 v[136:139], a[4:7], a[212:215], v[136:139]\n"
        "ds_read_b64_tr_b16 a[86:87], v154 offset:36864\n"
        "v_mfma_f32_16x16x32_bf16 v[136:139], a[0:3], a[216:219], v[136:139]\n"
        "ds_read_b128 a[228:231], v152 offset:3520\n"
        "global_load_lds_dwordx4 v254, s[60:61] offset:1024 sc1\n"
        "v_mfma_f32_16x16x32_bf16 v[140:143], a[0:3], a[220:223], v[140:143]\n"
        "ds_read_b64_tr_b16 a[88:89], v153 offset:40960\n"
        "v_mfma_f32_16x16x32_bf16 v[140:143], a[4:7], a[216:219], v[140:143]\n"
        "ds_read_b64_tr_b16 a[90:91], v153 offset:45056\n"
        "v_mfma_f32_16x16x32_bf16 v[140:143], a[8:11], a[212:215], v[140:143]\n"
        "ds_read2_b32 a[40:41], v149 offset0:112 offset1:113\n"
        "v_mfma_f32_16x16x32_bf16 v[140:143], a[12:15], a[208:211], v[140:143]\n"
        "ds_read2_b32 a[42:43], v149 offset0:114 offset1:115\n"
        "global_load_lds_dwordx4 v254, s[60:61] offset:2048 sc1\n"
        "v_mfma_f32_16x16x32_bf16 v[140:143], a[16:19], a[204:207], v[140:143]\n"
        "ds_read_b128 a[232:235], v152 offset:3008\n"
        "s_waitcnt lgkmcnt(8)\n"
        "v_mfma_f32_16x16x32_bf16 v[140:143], a[20:23], a[200:203], v[140:143]\n"
        "ds_read_b64_tr_b16 a[92:93], v154 offset:40960\n"
        "v_mfma_f32_16x16x32_bf16 v[140:143], a[24:27], a[196:199], v[140:143]\n"
        "ds_read_b64_tr_b16 a[94:95], v154 offset:45056\n"
        "v_mfma_f32_16x16x32_bf16 v[140:143], a[28:31], a[192:195], v[140:143]\n"
        "ds_read2_b32 a[44:45], v149 offset0:240 offset1:241\n"
        "global_load_lds_dwordx4 v254, s[60:61] offset:3072 sc1\n"
        "s_add_u32 m0, s73, 0x14000\n"
        "v_mfma_f32_16x16x32_bf16 v[96:99], a[32:35], a[224:227], v[96:99]\n"
        "ds_read2_b32 a[46:47], v149 offset0:242 offset1:243\n"
        "s_waitcnt lgkmcnt(8)\n"
        "v_mfma_f32_16x16x32_bf16 v[100:103], a[32:35], a[228:231], v[100:103]\n"
        "ds_read_b128 a[236:239], v152 offset:2496\n"
        "s_add_u32 s60, s68, s66\n"
        "s_addc_u32 s61, s69, 0\n"
        "v_mfma_f32_16x16x32_bf16 v[100:103], a[36:39], a[224:227], v[100:103]\n"
        "ds_read_b64_tr_b16 a[160:161], v153 offset:49152\n"
        "s_xor_b32 s66, s17, 2\n"
        "s_lshl_b32 s66, s66, 15\n"
        "s_waitcnt lgkmcnt(7)\n"
        "v_mfma_f32_16x16x32_bf16 v[104:107], a[40:43], a[224:227], v[104:107]\n"
        "ds_read_b64_tr_b16 a[162:163], v153 offset:53248\n"
        "global_load_lds_dwordx4 v254, s[60:61] offset:0 sc1\n"
        "v_mfma_f32_16x16x32_bf16 v[104:107], a[36:39], a[228:231], v[104:107]\n"
        "ds_read2_b32 a[48:49], v150 offset0:112 offset1:113\n"
        "s_waitcnt lgkmcnt(8)\n"
        "v_mfma_f32_16x16x32_bf16 v[104:107], a[32:35], a[232:235], v[104:107]\n"
        "ds_read2_b32 a[50:51], v150 offset0:114 offset1:115\n"
        "ds_read_b128 a[240:243], v152 offset:1984\n"
        "global_load_lds_dwordx4 v254, s[60:61] offset:1024 sc1\n"
        "ds_read_b64_tr_b16 a[164:165], v154 offset:49152\n"
        "ds_read_b64_tr_b16 a[166:167], v154 offset:53248\n"
        "s_waitcnt lgkmcnt(7)\n"
        "v_mfma_f32_16x16x32_bf16 v[108:111], a[32:35], a[236:239], v[108:111]\n"
        "ds_read2_b32 a[52:53], v150 offset0:240 offset1:241\n"
        "v_cvt_pk_bf16_f32 v96, 0, v96\n"
        "v_mfma_f32_16x16x32_bf16 v[108:111], a[36:39], a[232:235], v[108:111]\n"
        "ds_read2_b32 a[54:55], v150 offset0:242 offset1:243\n"
        "v_cvt_pk_bf16_f32 v97, 0, v97\n"
        "v_mfma_f32_16x16x32_bf16 v[108:111], a[40:43], a[228:231], v[108:111]\n"
        "ds_read_b128 a[244:247], v152 offset:1472\n"
        "global_load_lds_dwordx4 v254, s[60:61] offset:2048 sc1\n"
        "v_mfma_f32_16x16x32_bf16 v[108:111], a[44:47], a[224:227], v[108:111]\n"
        "ds_read_b64_tr_b16 a[168:169], v153 offset:57344\n"
        "v_cvt_pk_bf16_f32 v98, 0, v98\n"
        "s_waitcnt lgkmcnt(7)\n"
        "v_mfma_f32_16x16x32_bf16 v[128:131], a[48:51], a[224:227], v[128:131]\n"
        "ds_read_b64_tr_b16 a[170:171], v153 offset:61440\n"
        "v_cvt_pk_bf16_f32 v99, 0, v99\n"
        "v_mfma_f32_16x16x32_bf16 v[128:131], a[44:47], a[228:231], v[128:131]\n"
        "ds_read_b64_tr_b16 a[172:173], v154 offset:57344\n"
        "v_cvt_pk_bf16_f32 v100, 0, v100\n"
        "v_mfma_f32_16x16x32_bf16 v[128:131], a[40:43], a[232:235], v[128:131]\n"
        "ds_read_b64_tr_b16 a[174:175], v154 offset:61440\n"
        "global_load_lds_dwordx4 v254, s[60:61] offset:3072 sc1\n"
        "s_add_u32 m0, s73, 0x8000\n"
        "v_mfma_f32_16x16x32_bf16 v[128:131], a[36:39], a[236:239], v[128:131]\n"
        "ds_read2_b32 a[56:57], v151 offset0:112 offset1:113\n"
        "s_waitcnt lgkmcnt(8)\n"
        "v_mfma_f32_16x16x32_bf16 v[128:131], a[32:35], a[240:243], v[128:131]\n"
        "ds_read2_b32 a[58:59], v151 offset0:114 offset1:115\n"
        "ds_read_b128 a[248:251], v152 offset:960\n"
        "ds_read_b64 v[64:65], v229 offset:3584\n"
        "s_waitcnt lgkmcnt(8)\n"
        "v_mfma_f32_16x16x32_bf16 v[132:135], a[32:35], a[244:247], v[132:135]\n"
        "ds_read_b64 v[66:67], v229 offset:3072\n"
        "s_add_u32 s60, s68, s66\n"
        "s_addc_u32 s61, s69, 0\n"
        "v_mfma_f32_16x16x32_bf16 v[132:135], a[36:39], a[240:243], v[132:135]\n"
        "ds_read_b64 v[68:69], v229 offset:2560\n"
        "s_xor_b32 s66, s17, 3\n"
        "s_lshl_b32 s66, s66, 15\n"
        "v_mfma_f32_16x16x32_bf16 v[132:135], a[40:43], a[236:239], v[132:135]\n"
        "ds_read_b64 v[70:71], v229 offset:2048\n"
        "v_cvt_pk_bf16_f32 v101, 0, v101\n"
        "v_mfma_f32_16x16x32_bf16 v[132:135], a[44:47], a[232:235], v[132:135]\n"
        "ds_read_b64 v[72:73], v229 offset:1536\n"
        "v_cvt_pk_bf16_f32 v102, 0, v102\n"
        "v_mfma_f32_16x16x32_bf16 v[132:135], a[48:51], a[228:231], v[132:135]\n"
        "ds_read_b64 v[74:75], v229 offset:1024\n"
        "s_waitcnt vmcnt(13) lgkmcnt(9)\n"
        "v_mfma_f32_16x16x32_bf16 v[132:135], a[52:55], a[224:227], v[132:135]\n"
        "ds_read_b64 v[76:77], v229 offset:512\n"
        "v_cvt_pk_bf16_f32 v103, 0, v103\n"
        "s_waitcnt lgkmcnt(8)\n"
        "v_mfma_f32_16x16x32_bf16 v[136:139], a[56:59], a[224:227], v[136:139]\n"
        "s_barrier\n"
        "v_mfma_f32_16x16x32_bf16 v[136:139], a[52:55], a[228:231], v[136:139]\n"
        "ds_read_b64 v[78:79], v229 offset:0\n"
        "global_load_lds_dwordx4 v254, s[60:61] offset:0 sc1\n"
        "v_mfma_f32_16x16x32_bf16 v[136:139], a[48:51], a[232:235], v[136:139]\n"
        "ds_read_b128 a[252:255], v152 offset:448\n"
        "v_cvt_pk_bf16_f32 v104, 0, v104\n"
        "v_mfma_f32_16x16x32_bf16 v[136:139], a[44:47], a[236:239], v[136:139]\n"
        "ds_read2_b32 a[60:61], v151 offset0:240 offset1:241\n"
        "v_cvt_pk_bf16_f32 v105, 0, v105\n"
        "v_mfma_f32_16x16x32_bf16 v[136:139], a[40:43], a[240:243], v[136:139]\n"
        "ds_read2_b32 a[62:63], v151 offset0:242 offset1:243\n"
        "v_cvt_pk_bf16_f32 v106, 0, v106\n"
        "v_mfma_f32_16x16x32_bf16 v[136:139], a[36:39], a[244:247], v[136:139]\n"
        "ds_read_b64_tr_b16 a[176:177], v153 offset:0\n"
        "s_waitcnt lgkmcnt(7)\n"
        "v_mfma_f32_16x16x32_bf16 v[136:139], a[32:35], a[248:251], v[136:139]\n"
        "ds_read_b64_tr_b16 a[178:179], v153 offset:4096\n"
        "global_load_lds_dwordx4 v254, s[60:61] offset:1024 sc1\n"
        "ds_read_b64_tr_b16 a[180:181], v154 offset:0\n"
        "ds_read_b64_tr_b16 a[182:183], v154 offset:4096\n"
        "s_waitcnt lgkmcnt(6)\n"
        "global_load_lds_dwordx4 v254, s[60:61] offset:2048 sc1\n"
        "v_mfma_f32_16x16x32_bf16 v[140:143], a[32:35], a[252:255], v[140:143]\n"
        "ds_read_b64_tr_b16 a[184:185], v153 offset:8192\n"
        "v_and_b32 v80, s35, v65\n"
        "v_mfma_f32_16x16x32_bf16 v[140:143], a[36:39], a[248:251], v[140:143]\n"
        "ds_read_b64_tr_b16 a[186:187], v153 offset:12288\n"
        "v_lshlrev_b32 v81, 16, v65\n"
        "v_mfma_f32_16x16x32_bf16 v[140:143], a[40:43], a[244:247], v[140:143]\n"
        "ds_read_b64_tr_b16 a[188:189], v154 offset:8192\n"
        "s_waitcnt lgkmcnt(7)\n"
        "v_mfma_f32_16x16x32_bf16 v[140:143], a[44:47], a[240:243], v[140:143]\n"
        "s_barrier\n"
        "v_mfma_f32_16x16x32_bf16 v[140:143], a[48:51], a[236:239], v[140:143]\n"
        "ds_read_b64_tr_b16 a[190:191], v154 offset:12288\n"
        "global_load_lds_dwordx4 v254, s[60:61] offset:3072 sc1\n"
        "s_add_u32 m0, s73, 0xc000\n"
        "v_mfma_f32_16x16x32_bf16 v[140:143], a[52:55], a[232:235], v[140:143]\n"
        "s_add_u32 s60, s68, s66\n"
        "s_addc_u32 s61, s69, 0\n"
        "s_sub_u32 s66, 15, s12\n"
        "v_mfma_f32_16x16x32_bf16 v[140:143], a[56:59], a[228:231], v[140:143]\n"
        "s_cmp_lt_u32 s75, 2\n"
        "s_cselect_b32 s66, s12, s66\n"
        "s_mul_i32 s66, s66, 1052672\n"
        "v_mfma_f32_16x16x32_bf16 v[140:143], a[60:63], a[224:227], v[140:143]\n"
        "s_add_u32 s66, s66, s67\n"
        "s_add_u32 s68, s20, s66\n"
        "s_addc_u32 s69, s21, 0\n"
        "global_load_lds_dwordx4 v254, s[60:61] offset:0 sc1\n"
        "s_xor_b32 s66, s17, 0\n"
        "s_lshl_b32 s66, s66, 15\n"
        "s_and_b32 s67, s74, 1\n"
        "s_lshl_b32 s67, s67, 12\n"
        "v_and_b32 v82, s35, v64\n"
        "v_lshlrev_b32 v83, 16, v64\n"
        "v_and_b32 v84, s35, v67\n"
        "v_lshlrev_b32 v85, 16, v67\n"
        "v_and_b32 v86, s35, v66\n"
        "v_lshlrev_b32 v87, 16, v66\n"
        "v_mul_f32 v80, s54, v80\n"
        "global_load_lds_dwordx4 v254, s[60:61] offset:1024 sc1\n"
        "v_mul_f32 v81, s54, v81\n"
        "v_mul_f32 v82, s54, v82\n"
        "v_mul_f32 v83, s54, v83\n"
        "v_mul_f32 v84, s54, v84\n"
        "v_mul_f32 v85, s54, v85\n"
        "v_mul_f32 v86, s54, v86\n"
        "v_mul_f32 v87, s54, v87\n"
        "v_cvt_pk_bf16_f32 v80, 0, v80\n"
        "v_cvt_pk_bf16_f32 v81, 0, v81\n"
        "v_cvt_pk_bf16_f32 v82, 0, v82\n"
        "global_load_lds_dwordx4 v254, s[60:61] offset:2048 sc1\n"
        "v_cvt_pk_bf16_f32 v83, 0, v83\n"
        "v_cvt_pk_bf16_f32 v84, 0, v84\n"
        "v_cvt_pk_bf16_f32 v85, 0, v85\n"
        "v_cvt_pk_bf16_f32 v86, 0, v86\n"
        "v_cvt_pk_bf16_f32 v87, 0, v87\n"
        "v_add_f32 v80, v80, v96\n"
        "v_add_f32 v81, v81, v97\n"
        "v_add_f32 v82, v82, v98\n"
        "v_add_f32 v83, v83, v99\n"
        "v_add_f32 v84, v84, v100\n"
        "global_load_lds_dwordx4 v254, s[60:61] offset:3072 sc1\n"
        "s_add_u32 m0, s73, 0x18000\n"
        "s_add_u32 s60, s68, s66\n"
        "s_addc_u32 s61, s69, 0\n"
        "s_xor_b32 s66, s29, 0x1c0000\n"
        "v_add_f32 v85, v85, v101\n"
        "v_add_f32 v86, v86, v102\n"
        "v_add_f32 v87, v87, v103\n"
        "v_cvt_pk_bf16_f32 v88, v80, v81\n"
        "v_cvt_pk_bf16_f32 v89, v82, v83\n"
        "v_cvt_pk_bf16_f32 v90, v84, v85\n"
        "v_cvt_pk_bf16_f32 v91, v86, v87\n"
        "global_load_lds_dwordx4 v254, s[60:61] offset:0 sc1\n"
        "v_and_b32 v80, s35, v69\n"
        "v_lshlrev_b32 v81, 16, v69\n"
        "v_and_b32 v82, s35, v68\n"
        "v_lshlrev_b32 v83, 16, v68\n"
        "v_and_b32 v84, s35, v71\n"
        "v_lshlrev_b32 v85, 16, v71\n"
        "v_and_b32 v86, s35, v70\n"
        "v_lshlrev_b32 v87, 16, v70\n"
        "v_mul_f32 v80, s54, v80\n"
        "v_mul_f32 v81, s54, v81\n"
        "global_load_lds_dwordx4 v254, s[60:61] offset:1024 sc1\n"
        "v_mul_f32 v82, s54, v82\n"
        "v_mul_f32 v83, s54, v83\n"
        "v_mul_f32 v84, s54, v84\n"
        "v_mul_f32 v85, s54, v85\n"
        "v_mul_f32 v86, s54, v86\n"
        "s_waitcnt vmcnt(14) lgkmcnt(0)\n"
        "s_barrier\n"
        "ds_read_b128 a[0:3], v244 offset:0\n"
        "ds_read_b128 a[4:7], v244 offset:4096\n"
        "ds_read_b128 a[8:11], v244 offset:8192\n"
        "ds_read_b128 a[12:15], v244 offset:12288\n"
        "ds_read_b64_tr_b16 a[32:33], v153 offset:16384\n"
        "ds_read_b64_tr_b16 a[34:35], v153 offset:20480\n"
        "ds_read_b64_tr_b16 a[36:37], v154 offset:16384\n"
        "ds_read_b64_tr_b16 a[38:39], v154 offset:20480\n"
        "ds_read_b64_tr_b16 a[40:41], v153 offset:24576\n"
        "ds_read_b64_tr_b16 a[42:43], v153 offset:28672\n"
        "ds_read_b64_tr_b16 a[44:45], v154 offset:24576\n"
        "ds_read_b64_tr_b16 a[46:47], v154 offset:28672\n"
        "global_load_lds_dwordx4 v254, s[60:61] offset:2048 sc1\n"
        "ds_read_b128 a[16:19], v244 offset:1024\n"
        "ds_read_b128 a[20:23], v244 offset:5120\n"
        "ds_read_b128 a[24:27], v244 offset:9216\n"
        "LHC3_%=:\n"
        "s_cbranch_execz LHRC_%=\n"
        "v_mul_f32 v87, s54, v87\n"
        "s_waitcnt lgkmcnt(14)\n"
        "v_mfma_f32_16x16x32_bf16 v[0:3], a[96:99], a[0:3], 0\n"
        "s_waitcnt lgkmcnt(13)\n"
        "ds_read_b128 a[28:31], v244 offset:13312\n"
        "v_mfma_f32_16x16x32_bf16 v[0:3], a[104:107], a[4:7], v[0:3]\n"
        "s_waitcnt lgkmcnt(13)\n"
        "v_permlane16_swap_b32 v88, v90\n"
        "v_mfma_f32_16x16x32_bf16 v[0:3], a[112:115], a[8:11], v[0:3]\n"
        "s_waitcnt lgkmcnt(11)\n"
        "ds_read_b128 a[192:195], v244 offset:2048\n"
        "v_mfma_f32_16x16x32_bf16 v[0:3], a[120:123], a[12:15], v[0:3]\n"
        "ds_read_b128 a[196:199], v244 offset:6144\n"
        "global_load_lds_dwordx4 v254, s[60:61] offset:3072 sc1\n"
        "s_add_u32 m0, s73, 0x1c000\n"
        "v_mfma_f32_16x16x32_bf16 v[4:7], a[124:127], a[12:15], 0\n"
        "ds_read_b128 a[200:203], v244 offset:10240\n"
        "s_add_u32 s60, s22, s66\n"
        "s_addc_u32 s61, s23, 0\n"
        "v_mfma_f32_16x16x32_bf16 v[4:7], a[116:119], a[8:11], v[4:7]\n"
        "ds_read_b128 a[204:207], v244 offset:14336\n"
        "s_xor_b32 s66, s17, 1\n"
        "s_lshl_b32 s66, s66, 15\n"
        "v_mfma_f32_16x16x32_bf16 v[4:7], a[108:111], a[4:7], v[4:7]\n"
        "s_waitcnt lgkmcnt(7)\n"
        "v_permlane16_swap_b32 v89, v91\n"
        "v_mfma_f32_16x16x32_bf16 v[4:7], a[100:103], a[0:3], v[4:7]\n"
        "ds_read_b128 a[208:211], v244 offset:3072\n"
        "global_load_lds_dwordx4 v144, s[60:61] offset:0\n"
        "v_mfma_f32_16x16x32_bf16 v[12:15], a[100:103], a[16:19], 0\n"
        "ds_read_b128 a[212:215], v244 offset:7168\n"
        "s_waitcnt lgkmcnt(6)\n"
        "v_mfma_f32_16x16x32_bf16 v[12:15], a[108:111], a[20:23], v[12:15]\n"
        "ds_read_b128 a[216:219], v244 offset:11264\n"
        "v_cvt_pk_bf16_f32 v107, 0, v107\n"
        "v_mfma_f32_16x16x32_bf16 v[12:15], a[116:119], a[24:27], v[12:15]\n"
        "ds_read_b128 a[220:223], v244 offset:15360\n"
        "v_cvt_pk_bf16_f32 v108, 0, v108\n"
        "v_mfma_f32_16x16x32_bf16 v[12:15], a[124:127], a[28:31], v[12:15]\n"
        "global_load_lds_dwordx4 v145, s[60:61] offset:1024\n"
        "v_cvt_pk_bf16_f32 v109, 0, v109\n"
        "v_mfma_f32_16x16x32_bf16 v[8:11], a[120:123], a[28:31], 0\n"
        "s_waitcnt lgkmcnt(5)\n"
        "v_cvt_pk_bf16_f32 v110, 0, v110\n"
        "v_cvt_pk_bf16_f32 v111, 0, v111\n"
        "v_mfma_f32_16x16x32_bf16 v[8:11], a[112:115], a[24:27], v[8:11]\n"
        "v_cvt_pk_bf16_f32 v80, 0, v80\n"
        "v_cvt_pk_bf16_f32 v81, 0, v81\n"
        "v_mfma_f32_16x16x32_bf16 v[8:11], a[104:107], a[20:23], v[8:11]\n"
        "global_load_lds_dwordx4 v146, s[60:61] offset:2048\n"
        "v_mfma_f32_16x16x32_bf16 v[8:11], a[96:99], a[16:19], v[8:11]\n"
        "s_waitcnt vmcnt(15)\n"
        "v_cvt_pk_bf16_f32 v82, 0, v82\n"
        "v_cvt_pk_bf16_f32 v83, 0, v83\n"
        "v_mfma_f32_16x16x32_bf16 v[16:19], a[96:99], a[192:195], 0\n"
        "s_barrier\n"
        "v_mfma_f32_16x16x32_bf16 v[16:19], a[104:107], a[196:199], v[16:19]\n"
        "ds_read_b128 a[0:3], v244 offset:16384\n"
        "v_cvt_pk_bf16_f32 v84, 0, v84\n"
        "v_mfma_f32_16x16x32_bf16 v[16:19], a[112:115], a[200:203], v[16:19]\n"
        "ds_read_b128 a[4:7], v244 offset:20480\n"
        "s_waitcnt lgkmcnt(2)\n"
        "v_mfma_f32_16x16x32_bf16 v[16:19], a[120:123], a[204:207], v[16:19]\n"
        "ds_read_b128 a[8:11], v244 offset:24576\n"
        "global_load_lds_dwordx4 v147, s[60:61] offset:3072\n"
        "s_add_u32 m0, s73, 0x20000\n"
        "v_mfma_f32_16x16x32_bf16 v[20:23], a[124:127], a[204:207], 0\n"
        "ds_read_b128 a[12:15], v244 offset:28672\n"
        "s_add_u32 s60, s68, s66\n"
        "s_addc_u32 s61, s69, 0\n"
        "v_mfma_f32_16x16x32_bf16 v[20:23], a[116:119], a[200:203], v[20:23]\n"
        "ds_read_b128 a[16:19], v244 offset:17408\n"
        "s_xor_b32 s66, s17, 2\n"
        "s_lshl_b32 s66, s66, 15\n"
        "v_mfma_f32_16x16x32_bf16 v[20:23], a[108:111], a[196:199], v[20:23]\n"
        "ds_read_b128 a[20:23], v244 offset:21504\n"
        "v_cvt_pk_bf16_f32 v85, 0, v85\n"
        "v_mfma_f32_16x16x32_bf16 v[20:23], a[100:103], a[192:195], v[20:23]\n"
        "ds_read_b128 a[24:27], v244 offset:25600\n"
        "global_load_lds_dwordx4 v254, s[60:61] offset:0 sc1\n"
        "v_mfma_f32_16x16x32_bf16 v[28:31], a[100:103], a[208:211], 0\n"
        "ds_read_b128 a[28:31], v244 offset:29696\n"
        "v_cvt_pk_bf16_f32 v86, 0, v86\n"
        "v_mfma_f32_16x16x32_bf16 v[28:31], a[108:111], a[212:215], v[28:31]\n"
        "ds_read_b128 a[192:195], v244 offset:18432\n"
        "v_cvt_pk_bf16_f32 v87, 0, v87\n"
        "v_mfma_f32_16x16x32_bf16 v[28:31], a[116:119], a[216:219], v[28:31]\n"
        "ds_read_b128 a[196:199], v244 offset:22528\n"
        "v_add_f32 v80, v80, v104\n"
        "v_mfma_f32_16x16x32_bf16 v[28:31], a[124:127], a[220:223], v[28:31]\n"
        "ds_read_b128 a[200:203], v244 offset:26624\n"
        "global_load_lds_dwordx4 v254, s[60:61] offset:1024 sc1\n"
        "v_mfma_f32_16x16x32_bf16 v[24:27], a[120:123], a[220:223], 0\n"
        "ds_read_b128 a[204:207], v244 offset:30720\n"
        "v_add_f32 v81, v81, v105\n"
        "v_mfma_f32_16x16x32_bf16 v[24:27], a[112:115], a[216:219], v[24:27]\n"
        "ds_read_b128 a[216:219], v244 offset:27648\n"
        "v_add_f32 v82, v82, v106\n"
        "v_mfma_f32_16x16x32_bf16 v[24:27], a[104:107], a[212:215], v[24:27]\n"
        "ds_read_b128 a[212:215], v244 offset:23552\n"
        "v_add_f32 v83, v83, v107\n"
        "v_mfma_f32_16x16x32_bf16 v[24:27], a[96:99], a[208:211], v[24:27]\n"
        "ds_read_b128 a[208:211], v244 offset:19456\n"
        "s_waitcnt lgkmcnt(8)\n"
        "v_mfma_f32_16x16x32_bf16 v[0:3], a[128:131], a[0:3], v[0:3]\n"
        "ds_read_b128 a[220:223], v244 offset:31744\n"
        "global_load_lds_dwordx4 v254, s[60:61] offset:2048 sc1\n"
        "v_mfma_f32_16x16x32_bf16 v[0:3], a[136:139], a[4:7], v[0:3]\n"
        "v_add_f32 v84, v84, v108\n"
        "v_add_f32 v85, v85, v109\n"
        "v_mfma_f32_16x16x32_bf16 v[0:3], a[144:147], a[8:11], v[0:3]\n"
        "v_add_f32 v86, v86, v110\n"
        "v_add_f32 v87, v87, v111\n"
        "v_mfma_f32_16x16x32_bf16 v[0:3], a[152:155], a[12:15], v[0:3]\n"
        "v_and_b32 v229, 1, v233\n"
        "v_cvt_pk_bf16_f32 v92, v80, v81\n"
        "v_mfma_f32_16x16x32_bf16 v[4:7], a[156:159], a[12:15], v[4:7]\n"
        "global_load_lds_dwordx4 v254, s[60:61] offset:3072 sc1\n"
        "s_add_u32 m0, s73, 0x24000\n"
        "v_cvt_pk_bf16_f32 v93, v82, v83\n"
        "v_mfma_f32_16x16x32_bf16 v[4:7], a[148:151], a[8:11], v[4:7]\n"
        "s_add_u32 s60, s68, s66\n"
        "s_addc_u32 s61, s69, 0\n"
        "s_xor_b32 s66, s29, 0x1e0000\n"
        "v_mfma_f32_16x16x32_bf16 v[4:7], a[140:143], a[4:7], v[4:7]\n"
        "v_cvt_pk_bf16_f32 v94, v84, v85\n"
        "v_cvt_pk_bf16_f32 v95, v86, v87\n"
        "v_mfma_f32_16x16x32_bf16 v[4:7], a[132:135], a[0:3], v[4:7]\n"
        "s_waitcnt lgkmcnt(1)\n"
        "v_permlane16_swap_b32 v92, v94\n"
        "v_mfma_f32_16x16x32_bf16 v[12:15], a[132:135], a[16:19], v[12:15]\n"
        "v_permlane16_swap_b32 v93, v95\n"
        "v_mfma_f32_16x16x32_bf16 v[12:15], a[140:143], a[20:23], v[12:15]\n"
        "global_load_lds_dwordx4 v254, s[60:61] offset:0 sc1\n"
        "v_lshlrev_b32 v229, 9, v229\n"
        "v_mfma_f32_16x16x32_bf16 v[12:15], a[148:151], a[24:27], v[12:15]\n"
        "v_and_b32 v80, s35, v73\n"
        "v_lshlrev_b32 v81, 16, v73\n"
        "v_mfma_f32_16x16x32_bf16 v[12:15], a[156:159], a[28:31], v[12:15]\n"
        "v_and_b32 v82, s35, v72\n"
        "v_lshlrev_b32 v83, 16, v72\n"
        "v_mfma_f32_16x16x32_bf16 v[8:11], a[152:155], a[28:31], v[8:11]\n"
        "global_load_lds_dwordx4 v254, s[60:61] offset:1024 sc1\n"
        "v_mfma_f32_16x16x32_bf16 v[8:11], a[144:147], a[24:27], v[8:11]\n"
        "v_and_b32 v84, s35, v75\n"
        "v_lshlrev_b32 v85, 16, v75\n"
        "v_mfma_f32_16x16x32_bf16 v[8:11], a[136:139], a[20:23], v[8:11]\n"
        "v_and_b32 v86, s35, v74\n"
        "v_lshlrev_b32 v87, 16, v74\n"
        "v_mfma_f32_16x16x32_bf16 v[8:11], a[128:131], a[16:19], v[8:11]\n"
        "s_waitcnt vmcnt(18)\n"
        "v_lshl_add_u32 v229, v232, 5, v229\n"
        "v_mul_f32 v80, s54, v80\n"
        "v_mfma_f32_16x16x32_bf16 v[16:19], a[128:131], a[192:195], v[16:19]\n"
        "s_barrier\n"
        "v_mfma_f32_16x16x32_bf16 v[16:19], a[136:139], a[196:199], v[16:19]\n"
        "ds_read_b128 a[0:3], v254 offset:32768\n"
        "global_load_lds_dwordx4 v254, s[60:61] offset:2048 sc1\n"
        "v_mfma_f32_16x16x32_bf16 v[16:19], a[144:147], a[200:203], v[16:19]\n"
        "ds_read_b128 a[4:7], v254 offset:36864\n"
        "v_mul_f32 v81, s54, v81\n"
        "v_mfma_f32_16x16x32_bf16 v[16:19], a[152:155], a[204:207], v[16:19]\n"
        "ds_read_b128 a[8:11], v254 offset:40960\n"
        "v_mul_f32 v82, s54, v82\n"
        "v_mfma_f32_16x16x32_bf16 v[20:23], a[156:159], a[204:207], v[20:23]\n"
        "ds_read_b128 a[12:15], v254 offset:45056\n"
        "v_mul_f32 v83, s54, v83\n"
        "v_mfma_f32_16x16x32_bf16 v[20:23], a[148:151], a[200:203], v[20:23]\n"
        "ds_read_b128 a[16:19], v254 offset:33792\n"
        "global_load_lds_dwordx4 v254, s[60:61] offset:3072 sc1\n"
        "s_add_u32 m0, s73, 0x0\n"
        "v_mfma_f32_16x16x32_bf16 v[20:23], a[140:143], a[196:199], v[20:23]\n"
        "ds_read_b128 a[20:23], v254 offset:37888\n"
        "s_add_u32 s60, s22, s66\n"
        "s_addc_u32 s61, s23, 0\n"
        "v_mfma_f32_16x16x32_bf16 v[20:23], a[132:135], a[192:195], v[20:23]\n"
        "ds_read_b128 a[24:27], v254 offset:41984\n"
        "s_xor_b32 s66, s17, 3\n"
        "s_lshl_b32 s66, s66, 15\n"
        "v_mfma_f32_16x16x32_bf16 v[28:31], a[132:135], a[208:211], v[28:31]\n"
        "ds_read_b128 a[28:31], v254 offset:46080\n"
        "v_mul_f32 v84, s54, v84\n"
        "v_mfma_f32_16x16x32_bf16 v[28:31], a[140:143], a[212:215], v[28:31]\n"
        "ds_read_b128 a[192:195], v254 offset:34816\n"
        "global_load_lds_dwordx4 v144, s[60:61] offset:0\n"
        "v_mfma_f32_16x16x32_bf16 v[28:31], a[148:151], a[216:219], v[28:31]\n"
        "ds_read_b128 a[196:199], v254 offset:38912\n"
        "s_waitcnt lgkmcnt(8)\n"
        "v_mfma_f32_16x16x32_bf16 v[28:31], a[156:159], a[220:223], v[28:31]\n"
        "ds_read_b128 a[200:203], v254 offset:43008\n"
        "v_mul_f32 v85, s54, v85\n"
        "v_mfma_f32_16x16x32_bf16 v[24:27], a[152:155], a[220:223], v[24:27]\n"
        "ds_read_b128 a[204:207], v254 offset:47104\n"
        "v_mul_f32 v86, s54, v86\n"
        "v_mfma_f32_16x16x32_bf16 v[24:27], a[144:147], a[216:219], v[24:27]\n"
        "ds_read_b128 a[216:219], v254 offset:44032\n"
        "global_load_lds_dwordx4 v145, s[60:61] offset:1024\n"
        "v_mfma_f32_16x16x32_bf16 v[24:27], a[136:139], a[212:215], v[24:27]\n"
        "ds_read_b128 a[212:215], v254 offset:39936\n"
        "v_mul_f32 v87, s54, v87\n"
        "v_mfma_f32_16x16x32_bf16 v[24:27], a[128:131], a[208:211], v[24:27]\n"
        "ds_read_b128 a[208:211], v254 offset:35840\n"
        "v_cvt_pk_bf16_f32 v128, 0, v128\n"
        "v_mfma_f32_16x16x32_bf16 v[0:3], v[160:163], a[0:3], v[0:3]\n"
        "ds_read_b128 a[220:223], v254 offset:48128\n"
        "v_cvt_pk_bf16_f32 v129, 0, v129\n"
        "v_mfma_f32_16x16x32_bf16 v[0:3], v[168:171], a[4:7], v[0:3]\n"
        "global_load_lds_dwordx4 v146, s[60:61] offset:2048\n"
        "s_waitcnt lgkmcnt(7)\n"
        "v_mfma_f32_16x16x32_bf16 v[0:3], v[176:179], a[8:11], v[0:3]\n"
        "v_cvt_pk_bf16_f32 v130, 0, v130\n"
        "v_cvt_pk_bf16_f32 v131, 0, v131\n"
        "v_mfma_f32_16x16x32_bf16 v[0:3], v[184:187], a[12:15], v[0:3]\n"
        "v_cvt_pk_bf16_f32 v132, 0, v132\n"
        "v_cvt_pk_bf16_f32 v133, 0, v133\n"
        "v_mfma_f32_16x16x32_bf16 v[4:7], v[188:191], a[12:15], v[4:7]\n"
        "global_load_lds_dwordx4 v147, s[60:61] offset:3072\n"
        "s_add_u32 m0, s73, 0x4000\n"
        "v_mfma_f32_16x16x32_bf16 v[4:7], v[180:183], a[8:11], v[4:7]\n"
        "s_add_u32 s60, s68, s66\n"
        "s_addc_u32 s61, s69, 0\n"
        "s_sub_u32 s66, 15, s12\n"
        "v_mfma_f32_16x16x32_bf16 v[4:7], v[172:175], a[4:7], v[4:7]\n"
        "s_cmp_lt_u32 s74, 2\n"
        "s_cselect_b32 s66, s12, s66\n"
        "s_mul_i32 s66, s66, 1052672\n"
        "v_mfma_f32_16x16x32_bf16 v[4:7], v[164:167], a[0:3], v[4:7]\n"
        "s_add_u32 s66, s66, s67\n"
        "s_add_u32 s68, s20, s66\n"
        "s_addc_u32 s69, s21, 0\n"
        "v_mfma_f32_16x16x32_bf16 v[12:15], v[164:167], a[16:19], v[12:15]\n"
        "global_load_lds_dwordx4 v254, s[60:61] offset:0 sc1\n"
        "s_xor_b32 s66, s17, 4\n"
        "s_lshl_b32 s66, s66, 15\n"
        "v_mfma_f32_16x16x32_bf16 v[12:15], v[172:175], a[20:23], v[12:15]\n"
        "s_and_b32 s67, s75, 1\n"
        "s_lshl_b32 s67, s67, 12\n"
        "v_cvt_pk_bf16_f32 v134, 0, v134\n"
        "v_mfma_f32_16x16x32_bf16 v[12:15], v[180:183], a[24:27], v[12:15]\n"
        "v_cvt_pk_bf16_f32 v135, 0, v135\n"
        "v_lshl_add_u32 v229, v230, 4, v229\n"
        "v_mfma_f32_16x16x32_bf16 v[12:15], v[188:191], a[28:31], v[12:15]\n"
        "global_load_lds_dwordx4 v254, s[60:61] offset:1024 sc1\n"
        "v_mfma_f32_16x16x32_bf16 v[8:11], v[184:187], a[28:31], v[8:11]\n"
        "v_cvt_pk_bf16_f32 v80, 0, v80\n"
        "v_cvt_pk_bf16_f32 v81, 0, v81\n"
        "v_mfma_f32_16x16x32_bf16 v[8:11], v[176:179], a[24:27], v[8:11]\n"
        "s_waitcnt lgkmcnt(0)\n"
        "v_cvt_pk_bf16_f32 v82, 0, v82\n"
        "v_cvt_pk_bf16_f32 v83, 0, v83\n"
        "v_mfma_f32_16x16x32_bf16 v[8:11], v[168:171], a[20:23], v[8:11]\n"
        "v_cvt_pk_bf16_f32 v84, 0, v84\n"
        "v_cvt_pk_bf16_f32 v85, 0, v85\n"
        "v_mfma_f32_16x16x32_bf16 v[8:11], v[160:163], a[16:19], v[8:11]\n"
        "global_load_lds_dwordx4 v254, s[60:61] offset:2048 sc1\n"
        "s_waitcnt vmcnt(23)\n"
        "v_mfma_f32_16x16x32_bf16 v[16:19], v[160:163], a[192:195], v[16:19]\n"
        "s_barrier\n"
        "v_mfma_f32_16x16x32_bf16 v[16:19], v[168:171], a[196:199], v[16:19]\n"
        "ds_read_b128 a[0:3], v254 offset:49152\n"
        "v_cvt_pk_bf16_f32 v86, 0, v86\n"
        "v_mfma_f32_16x16x32_bf16 v[16:19], v[176:179], a[200:203], v[16:19]\n"
        "ds_read_b128 a[4:7], v254 offset:53248\n"
        "global_load_lds_dwordx4 v254, s[60:61] offset:3072 sc1\n"
        "s_add_u32 m0, s73, 0x10000\n"
        "v_mfma_f32_16x16x32_bf16 v[16:19], v[184:187], a[204:207], v[16:19]\n"
        "ds_read_b128 a[8:11], v254 offset:57344\n"
        "s_add_u32 s60, s68, s66\n"
        "s_addc_u32 s61, s69, 0\n"
        "v_mfma_f32_16x16x32_bf16 v[20:23], v[188:191], a[204:207], v[20:23]\n"
        "ds_read_b128 a[12:15], v254 offset:61440\n"
        "s_xor_b32 s66, s17, 5\n"
        "s_lshl_b32 s66, s66, 15\n"
        "v_mfma_f32_16x16x32_bf16 v[20:23], v[180:183], a[200:203], v[20:23]\n"
        "ds_read_b128 a[16:19], v254 offset:50176\n"
        "v_cvt_pk_bf16_f32 v87, 0, v87\n"
        "v_mfma_f32_16x16x32_bf16 v[20:23], v[172:175], a[196:199], v[20:23]\n"
        "ds_read_b128 a[20:23], v254 offset:54272\n"
        "global_load_lds_dwordx4 v254, s[60:61] offset:0 sc1\n"
        "v_mfma_f32_16x16x32_bf16 v[20:23], v[164:167], a[192:195], v[20:23]\n"
        "ds_read_b128 a[24:27], v254 offset:58368\n"
        "v_add_f32 v80, v80, v128\n"
        "v_mfma_f32_16x16x32_bf16 v[28:31], v[164:167], a[208:211], v[28:31]\n"
        "ds_read_b128 a[28:31], v254 offset:62464\n"
        "v_add_f32 v81, v81, v129\n"
        "v_mfma_f32_16x16x32_bf16 v[28:31], v[172:175], a[212:215], v[28:31]\n"
        "ds_read_b128 a[192:195], v254 offset:51200\n"
        "v_add_f32 v82, v82, v130\n"
        "v_mfma_f32_16x16x32_bf16 v[28:31], v[180:183], a[216:219], v[28:31]\n"
        "ds_read_b128 a[196:199], v254 offset:55296\n"
        "global_load_lds_dwordx4 v254, s[60:61] offset:1024 sc1\n"
        "v_mfma_f32_16x16x32_bf16 v[28:31], v[188:191], a[220:223], v[28:31]\n"
        "ds_read_b128 a[200:203], v254 offset:59392\n"
        "v_add_f32 v83, v83, v131\n"
        "v_mfma_f32_16x16x32_bf16 v[24:27], v[184:187], a[220:223], v[24:27]\n"
        "ds_read_b128 a[204:207], v254 offset:63488\n"
        "v_add_f32 v84, v84, v132\n"
        "v_mfma_f32_16x16x32_bf16 v[24:27], v[176:179], a[216:219], v[24:27]\n"
        "ds_read_b128 a[216:219], v254 offset:60416\n"
        "v_add_f32 v85, v85, v133\n"
        "v_mfma_f32_16x16x32_bf16 v[24:27], v[168:171], a[212:215], v[24:27]\n"
        "ds_read_b128 a[212:215], v254 offset:56320\n"
        "global_load_lds_dwordx4 v254, s[60:61] offset:2048 sc1\n"
        "v_mfma_f32_16x16x32_bf16 v[24:27], v[160:163], a[208:211], v[24:27]\n"
        "ds_read_b128 a[208:211], v254 offset:52224\n"
        "s_waitcnt lgkmcnt(8)\n"
        "v_mfma_f32_16x16x32_bf16 v[0:3], v[192:195], a[0:3], v[0:3]\n"
        "ds_read_b128 a[220:223], v254 offset:64512\n"
        "v_add_f32 v86, v86, v134\n"
        "v_mfma_f32_16x16x32_bf16 v[0:3], v[200:203], a[4:7], v[0:3]\n"
        "v_add_f32 v87, v87, v135\n"
        "v_cvt_pk_bf16_f32 v136, 0, v136\n"
        "v_mfma_f32_16x16x32_bf16 v[0:3], v[208:211], a[8:11], v[0:3]\n"
        "global_load_lds_dwordx4 v254, s[60:61] offset:3072 sc1\n"
        "s_add_u32 m0, s73, 0x14000\n"
        "v_cvt_pk_bf16_f32 v137, 0, v137\n"
        "v_mfma_f32_16x16x32_bf16 v[0:3], v[216:219], a[12:15], v[0:3]\n"
        "s_add_u32 s60, s68, s66\n"
        "s_addc_u32 s61, s69, 0\n"
        "s_xor_b32 s66, s17, 6\n"
        "v_mfma_f32_16x16x32_bf16 v[4:7], v[220:223], a[12:15], v[4:7]\n"
        "s_lshl_b32 s66, s66, 15\n"
        "v_cvt_pk_bf16_f32 v138, 0, v138\n"
        "v_cvt_pk_bf16_f32 v139, 0, v139\n"
        "v_mfma_f32_16x16x32_bf16 v[4:7], v[212:215], a[8:11], v[4:7]\n"
        "global_load_lds_dwordx4 v254, s[60:61] offset:0 sc1\n"
        "v_mfma_f32_16x16x32_bf16 v[4:7], v[204:207], a[4:7], v[4:7]\n"
        "v_cvt_pk_bf16_f32 v140, 0, v140\n"
        "v_cvt_pk_bf16_f32 v141, 0, v141\n"
        "v_mfma_f32_16x16x32_bf16 v[4:7], v[196:199], a[0:3], v[4:7]\n"
        "s_waitcnt lgkmcnt(1)\n"
        "v_cvt_pk_bf16_f32 v142, 0, v142\n"
        "v_cvt_pk_bf16_f32 v143, 0, v143\n"
        "v_mfma_f32_16x16x32_bf16 v[12:15], v[196:199], a[16:19], v[12:15]\n"
        "v_mfma_f32_16x16x32_bf16 v[12:15], v[204:207], a[20:23], v[12:15]\n"
        "global_load_lds_dwordx4 v254, s[60:61] offset:1024 sc1\n"
        "v_mfma_f32_16x16x32_bf16 v[12:15], v[212:215], a[24:27], v[12:15]\n"
        "v_mfma_f32_16x16x32_bf16 v[12:15], v[220:223], a[28:31], v[12:15]\n"
        "v_mfma_f32_16x16x32_bf16 v[8:11], v[216:219], a[28:31], v[8:11]\n"
        "global_load_lds_dwordx4 v254, s[60:61] offset:2048 sc1\n"
        "v_mfma_f32_16x16x32_bf16 v[8:11], v[208:211], a[24:27], v[8:11]\n"
        "v_mfma_f32_16x16x32_bf16 v[8:11], v[200:203], a[20:23], v[8:11]\n"
        "v_mfma_f32_16x16x32_bf16 v[8:11], v[192:195], a[16:19], v[8:11]\n"
        "s_waitcnt vmcnt(27)\n"
        "v_mfma_f32_16x16x32_bf16 v[16:19], v[192:195], a[192:195], v[16:19]\n"
        "s_barrier\n"
        "v_mfma_f32_16x16x32_bf16 v[16:19], v[200:203], a[196:199], v[16:19]\n"
        "ds_read_b128 a[0:3], v244 offset:32768\n"
        "global_load_lds_dwordx4 v254, s[60:61] offset:3072 sc1\n"
        "s_add_u32 m0, s73, 0x8000\n"
        "v_mfma_f32_16x16x32_bf16 v[16:19], v[208:211], a[200:203], v[16:19]\n"
        "ds_read_b128 a[4:7], v244 offset:36864\n"
        "s_add_u32 s60, s68, s66\n"
        "s_addc_u32 s61, s69, 0\n"
        "v_mfma_f32_16x16x32_bf16 v[16:19], v[216:219], a[204:207], v[16:19]\n"
        "ds_read_b128 a[8:11], v244 offset:40960\n"
        "s_xor_b32 s66, s17, 7\n"
        "s_lshl_b32 s66, s66, 15\n"
        "v_mfma_f32_16x16x32_bf16 v[20:23], v[220:223], a[204:207], v[20:23]\n"
        "ds_read_b128 a[12:15], v244 offset:45056\n"
        "v_mfma_f32_16x16x32_bf16 v[20:23], v[212:215], a[200:203], v[20:23]\n"
        "ds_read_b128 a[16:19], v244 offset:33792\n"
        "global_load_lds_dwordx4 v254, s[60:61] offset:0 sc1\n"
        "v_mfma_f32_16x16x32_bf16 v[20:23], v[204:207], a[196:199], v[20:23]\n"
        "ds_read_b128 a[20:23], v244 offset:37888\n"
        "v_mfma_f32_16x16x32_bf16 v[20:23], v[196:199], a[192:195], v[20:23]\n"
        "ds_read_b128 a[24:27], v244 offset:41984\n"
        "s_waitcnt lgkmcnt(7)\n"
        "v_mfma_f32_16x16x32_bf16 v[28:31], v[196:199], a[208:211], v[28:31]\n"
        "ds_read_b128 a[28:31], v244 offset:46080\n"
        "v_mfma_f32_16x16x32_bf16 v[28:31], v[204:207], a[212:215], v[28:31]\n"
        "ds_read_b128 a[192:195], v244 offset:34816\n"
        "global_load_lds_dwordx4 v254, s[60:61] offset:1024 sc1\n"
        "v_mfma_f32_16x16x32_bf16 v[28:31], v[212:215], a[216:219], v[28:31]\n"
        "ds_read_b128 a[196:199], v244 offset:38912\n"
        "v_mfma_f32_16x16x32_bf16 v[28:31], v[220:223], a[220:223], v[28:31]\n"
        "ds_read_b128 a[200:203], v244 offset:43008\n"
        "v_mfma_f32_16x16x32_bf16 v[24:27], v[216:219], a[220:223], v[24:27]\n"
        "ds_read_b128 a[204:207], v244 offset:47104\n"
        "s_waitcnt lgkmcnt(8)\n"
        "v_mfma_f32_16x16x32_bf16 v[24:27], v[208:211], a[216:219], v[24:27]\n"
        "ds_read_b128 a[216:219], v244 offset:44032\n"
        "global_load_lds_dwordx4 v254, s[60:61] offset:2048 sc1\n"
        "v_mfma_f32_16x16x32_bf16 v[24:27], v[200:203], a[212:215], v[24:27]\n"
        "ds_read_b128 a[212:215], v244 offset:39936\n"
        "v_mfma_f32_16x16x32_bf16 v[24:27], v[192:195], a[208:211], v[24:27]\n"
        "ds_read_b128 a[208:211], v244 offset:35840\n"
        "v_mfma_f32_16x16x32_bf16 v[32:35], a[96:99], a[0:3], 0\n"
        "ds_read_b128 a[220:223], v244 offset:48128\n"
        "v_mfma_f32_16x16x32_bf16 v[32:35], a[104:107], a[4:7], v[32:35]\n"
        "global_load_lds_dwordx4 v254, s[60:61] offset:3072 sc1\n"
        "s_add_u32 m0, s73, 0xc000\n"
        "v_mfma_f32_16x16x32_bf16 v[32:35], a[112:115], a[8:11], v[32:35]\n"
        "s_add_u32 s60, s68, s66\n"
        "s_addc_u32 s61, s69, 0\n"
        "s_sub_u32 s66, 15, s12\n"
        "v_mfma_f32_16x16x32_bf16 v[32:35], a[120:123], a[12:15], v[32:35]\n"
        "s_cmp_lt_u32 s75, 2\n"
        "s_cselect_b32 s66, s12, s66\n"
        "s_mul_i32 s66, s66, 1052672\n"
        "v_mfma_f32_16x16x32_bf16 v[36:39], a[124:127], a[12:15], 0\n"
        "s_add_u32 s66, s66, s67\n"
        "s_add_u32 s68, s20, s66\n"
        "s_addc_u32 s69, s21, 0\n"
        "v_mfma_f32_16x16x32_bf16 v[36:39], a[116:119], a[8:11], v[36:39]\n"
        "s_xor_b32 s66, s17, 4\n"
        "s_lshl_b32 s66, s66, 15\n"
        "s_and_b32 s67, s76, 1\n"
        "v_mfma_f32_16x16x32_bf16 v[36:39], a[108:111], a[4:7], v[36:39]\n"
        "s_lshl_b32 s67, s67, 12\n"
        "s_waitcnt lgkmcnt(2)\n"
        "v_mfma_f32_16x16x32_bf16 v[36:39], a[100:103], a[0:3], v[36:39]\n"
        "v_mfma_f32_16x16x32_bf16 v[44:47], a[100:103], a[16:19], 0\n"
        "v_mfma_f32_16x16x32_bf16 v[44:47], a[108:111], a[20:23], v[44:47]\n"
        "v_mfma_f32_16x16x32_bf16 v[44:47], a[116:119], a[24:27], v[44:47]\n"
        "v_mfma_f32_16x16x32_bf16 v[44:47], a[124:127], a[28:31], v[44:47]\n"
        "s_lshl2_add_u32 s15, s74, s10\n"
        "s_mul_i32 s16, s13, 263168\n"
        "s_lshl_b32 s15, s15, 14\n"
        "v_mfma_f32_16x16x32_bf16 v[40:43], a[120:123], a[28:31], 0\n"
        "s_add_u32 s16, s16, s15\n"
        "s_add_u32 s16, s16, s73\n"
        "s_add_u32 s16, s16, 0x4000000\n"
        "v_mfma_f32_16x16x32_bf16 v[40:43], a[112:115], a[24:27], v[40:43]\n"
        "s_add_u32 s28, s38, s16\n"
        "s_addc_u32 s29, s39, 0\n"
        "v_mfma_f32_16x16x32_bf16 v[40:43], a[104:107], a[20:23], v[40:43]\n"
        "v_mfma_f32_16x16x32_bf16 v[40:43], a[96:99], a[16:19], v[40:43]\n"
        "s_waitcnt vmcnt(24)\n"
        "v_mfma_f32_16x16x32_bf16 v[48:51], a[96:99], a[192:195], 0\n"
        "s_barrier\n"
        "v_mfma_f32_16x16x32_bf16 v[48:51], a[104:107], a[196:199], v[48:51]\n"
        "ds_read_b64_tr_b16 a[48:49], v155 offset:49152\n"
        "global_load_lds_dwordx4 v254, s[60:61] offset:0 sc1\n"
        "v_mfma_f32_16x16x32_bf16 v[48:51], a[112:115], a[200:203], v[48:51]\n"
        "ds_read_b64_tr_b16 a[50:51], v155 offset:53248\n"
        "v_mfma_f32_16x16x32_bf16 v[48:51], a[120:123], a[204:207], v[48:51]\n"
        "ds_read_b64_tr_b16 a[52:53], v156 offset:49152\n"
        "v_mfma_f32_16x16x32_bf16 v[52:55], a[124:127], a[204:207], 0\n"
        "ds_read_b64_tr_b16 a[54:55], v156 offset:53248\n"
        "s_waitcnt lgkmcnt(4)\n"
        "v_mfma_f32_16x16x32_bf16 v[52:55], a[116:119], a[200:203], v[52:55]\n"
        "ds_read_b64_tr_b16 a[56:57], v155 offset:57344\n"
        "global_load_lds_dwordx4 v254, s[60:61] offset:1024 sc1\n"
        "v_mfma_f32_16x16x32_bf16 v[52:55], a[108:111], a[196:199], v[52:55]\n"
        "ds_read_b64_tr_b16 a[58:59], v155 offset:61440\n"
        "v_mfma_f32_16x16x32_bf16 v[52:55], a[100:103], a[192:195], v[52:55]\n"
        "ds_read_b64_tr_b16 a[60:61], v156 offset:57344\n"
        "v_mfma_f32_16x16x32_bf16 v[60:63], a[100:103], a[208:211], 0\n"
        "ds_read_b64_tr_b16 a[62:63], v156 offset:61440\n"
        "v_mfma_f32_16x16x32_bf16 v[60:63], a[108:111], a[212:215], v[60:63]\n"
        "ds_read_b128 a[0:3], v245 offset:0\n"
        "global_load_lds_dwordx4 v254, s[60:61] offset:2048 sc1\n"
        "v_mfma_f32_16x16x32_bf16 v[60:63], a[116:119], a[216:219], v[60:63]\n"
        "ds_read_b128 a[4:7], v245 offset:4096\n"
        "v_mfma_f32_16x16x32_bf16 v[60:63], a[124:127], a[220:223], v[60:63]\n"
        "ds_read_b128 a[8:11], v245 offset:8192\n"
        "v_mfma_f32_16x16x32_bf16 v[56:59], a[120:123], a[220:223], 0\n"
        "ds_read_b128 a[12:15], v245 offset:12288\n"
        "v_mfma_f32_16x16x32_bf16 v[56:59], a[112:115], a[216:219], v[56:59]\n"
        "ds_read_b128 a[16:19], v245 offset:1024\n"
        "global_load_lds_dwordx4 v254, s[60:61] offset:3072 sc1\n"
        "s_add_u32 m0, s73, 0x18000\n"
        "v_mfma_f32_16x16x32_bf16 v[56:59], a[104:107], a[212:215], v[56:59]\n"
        "ds_read_b128 a[20:23], v245 offset:5120\n"
        "s_add_u32 s60, s68, s66\n"
        "s_addc_u32 s61, s69, 0\n"
        "v_mfma_f32_16x16x32_bf16 v[56:59], a[96:99], a[208:211], v[56:59]\n"
        "ds_read_b128 a[24:27], v245 offset:9216\n"
        "ds_read_b128 a[28:31], v245 offset:13312\n"
        "ds_read_b128 a[192:195], v245 offset:2048\n"
        "s_xor_b32 s66, s17, 5\n"
        "s_lshl_b32 s66, s66, 15\n"
        "s_waitcnt lgkmcnt(8)\n"
        "v_mfma_f32_16x16x32_bf16 v[32:35], a[128:131], a[0:3], v[32:35]\n"
        "ds_read_b128 a[196:199], v245 offset:6144\n"
        "s_waitcnt lgkmcnt(8)\n"
        "v_mfma_f32_16x16x32_bf16 v[32:35], a[136:139], a[4:7], v[32:35]\n"
        "ds_read_b128 a[200:203], v245 offset:10240\n"
        "s_waitcnt lgkmcnt(8)\n"
        "v_mfma_f32_16x16x32_bf16 v[32:35], a[144:147], a[8:11], v[32:35]\n"
        "ds_read_b128 a[204:207], v245 offset:14336\n"
        "s_waitcnt lgkmcnt(8)\n"
        "v_mfma_f32_16x16x32_bf16 v[32:35], a[152:155], a[12:15], v[32:35]\n"
        "ds_read_b128 a[208:211], v245 offset:3072\n"
        "v_mfma_f32_16x16x32_bf16 v[36:39], a[156:159], a[12:15], v[36:39]\n"
        "ds_read_b128 a[212:215], v245 offset:7168\n"
        "s_waitcnt lgkmcnt(9)\n"
        "v_mfma_f32_16x16x32_bf16 v[36:39], a[148:151], a[8:11], v[36:39]\n"
        "ds_read_b128 a[216:219], v245 offset:11264\n"
        "s_waitcnt lgkmcnt(9)\n"
        "v_mfma_f32_16x16x32_bf16 v[36:39], a[140:143], a[4:7], v[36:39]\n"
        "ds_read_b128 a[220:223], v245 offset:15360\n"
        "s_waitcnt lgkmcnt(8)\n"
        "v_mfma_f32_16x16x32_bf16 v[36:39], a[132:135], a[0:3], v[36:39]\n"
        "global_store_dwordx4 v229, v[88:91], s[24:25] offset:0 sc1\n"
        "v_mfma_f32_16x16x32_bf16 v[44:47], a[132:135], a[16:19], v[44:47]\n"
        "v_mfma_f32_16x16x32_bf16 v[44:47], a[140:143], a[20:23], v[44:47]\n"
        "v_cvt_pk_bf16_f32 v88, v80, v81\n"
        "v_cvt_pk_bf16_f32 v89, v82, v83\n"
        "v_mfma_f32_16x16x32_bf16 v[44:47], a[148:151], a[24:27], v[44:47]\n"
        "v_cvt_pk_bf16_f32 v90, v84, v85\n"
        "v_cvt_pk_bf16_f32 v91, v86, v87\n"
        "v_mfma_f32_16x16x32_bf16 v[44:47], a[156:159], a[28:31], v[44:47]\n"
        "v_permlane16_swap_b32 v88, v90\n"
        "v_mfma_f32_16x16x32_bf16 v[40:43], a[152:155], a[28:31], v[40:43]\n"
        "s_waitcnt lgkmcnt(2)\n"
        "v_permlane16_swap_b32 v89, v91\n"
        "v_mfma_f32_16x16x32_bf16 v[40:43], a[144:147], a[24:27], v[40:43]\n"
        "v_and_b32 v80, s35, v77\n"
        "v_lshlrev_b32 v81, 16, v77\n"
        "v_mfma_f32_16x16x32_bf16 v[40:43], a[136:139], a[20:23], v[40:43]\n"
        "v_and_b32 v82, s35, v76\n"
        "v_lshlrev_b32 v83, 16, v76\n"
        "v_mfma_f32_16x16x32_bf16 v[40:43], a[128:131], a[16:19], v[40:43]\n"
        "s_waitcnt vmcnt(25)\n"
        "v_and_b32 v84, s35, v79\n"
        "v_lshlrev_b32 v85, 16, v79\n"
        "v_mfma_f32_16x16x32_bf16 v[48:51], a[128:131], a[192:195], v[48:51]\n"
        "s_barrier\n"
        "v_mfma_f32_16x16x32_bf16 v[48:51], a[136:139], a[196:199], v[48:51]\n"
        "ds_read_b128 a[0:3], v245 offset:16384\n"
        "global_load_lds_dwordx4 v254, s[60:61] offset:0 sc1\n"
        "v_mfma_f32_16x16x32_bf16 v[48:51], a[144:147], a[200:203], v[48:51]\n"
        "ds_read_b128 a[4:7], v245 offset:20480\n"
        "v_and_b32 v86, s35, v78\n"
        "v_mfma_f32_16x16x32_bf16 v[48:51], a[152:155], a[204:207], v[48:51]\n"
        "ds_read_b128 a[8:11], v245 offset:24576\n"
        "v_lshlrev_b32 v87, 16, v78\n"
        "v_mfma_f32_16x16x32_bf16 v[52:55], a[156:159], a[204:207], v[52:55]\n"
        "ds_read_b128 a[12:15], v245 offset:28672\n"
        "v_mul_f32 v80, s54, v80\n"
        "v_mfma_f32_16x16x32_bf16 v[52:55], a[148:151], a[200:203], v[52:55]\n"
        "ds_read_b128 a[16:19], v245 offset:17408\n"
        "global_load_lds_dwordx4 v254, s[60:61] offset:1024 sc1\n"
        "v_mfma_f32_16x16x32_bf16 v[52:55], a[140:143], a[196:199], v[52:55]\n"
        "ds_read_b128 a[20:23], v245 offset:21504\n"
        "v_mul_f32 v81, s54, v81\n"
        "v_mfma_f32_16x16x32_bf16 v[52:55], a[132:135], a[192:195], v[52:55]\n"
        "ds_read_b128 a[24:27], v245 offset:25600\n"
        "v_mul_f32 v82, s54, v82\n"
        "v_mfma_f32_16x16x32_bf16 v[60:63], a[132:135], a[208:211], v[60:63]\n"
        "ds_read_b128 a[28:31], v245 offset:29696\n"
        "v_mul_f32 v83, s54, v83\n"
        "v_mfma_f32_16x16x32_bf16 v[60:63], a[140:143], a[212:215], v[60:63]\n"
        "ds_read_b128 a[192:195], v245 offset:18432\n"
        "s_waitcnt lgkmcnt(8)\n"
        "v_mfma_f32_16x16x32_bf16 v[60:63], a[148:151], a[216:219], v[60:63]\n"
        "ds_read_b128 a[196:199], v245 offset:22528\n"
        "global_load_lds_dwordx4 v254, s[60:61] offset:2048 sc1\n"
        "v_mfma_f32_16x16x32_bf16 v[60:63], a[156:159], a[220:223], v[60:63]\n"
        "ds_read_b128 a[200:203], v245 offset:26624\n"
        "v_mul_f32 v84, s54, v84\n"
        "v_mfma_f32_16x16x32_bf16 v[56:59], a[152:155], a[220:223], v[56:59]\n"
        "ds_read_b128 a[204:207], v245 offset:30720\n"
        "v_mul_f32 v85, s54, v85\n"
        "v_mfma_f32_16x16x32_bf16 v[56:59], a[144:147], a[216:219], v[56:59]\n"
        "ds_read_b128 a[216:219], v245 offset:27648\n"
        "v_mul_f32 v86, s54, v86\n"
        "v_mfma_f32_16x16x32_bf16 v[56:59], a[136:139], a[212:215], v[56:59]\n"
        "ds_read_b128 a[212:215], v245 offset:23552\n"
        "global_load_lds_dwordx4 v254, s[60:61] offset:3072 sc1\n"
        "s_add_u32 m0, s73, 0x1c000\n"
        "v_mfma_f32_16x16x32_bf16 v[56:59], a[128:131], a[208:211], v[56:59]\n"
        "ds_read_b128 a[208:211], v245 offset:19456\n"
        "s_add_u32 s60, s68, s66\n"
        "s_addc_u32 s61, s69, 0\n"
        "v_mfma_f32_16x16x32_bf16 v[32:35], v[160:163], a[0:3], v[32:35]\n"
        "ds_read_b128 a[220:223], v245 offset:31744\n"
        "s_xor_b32 s66, s17, 6\n"
        "s_waitcnt lgkmcnt(8)\n"
        "v_mfma_f32_16x16x32_bf16 v[32:35], v[168:171], a[4:7], v[32:35]\n"
        "s_lshl_b32 s66, s66, 15\n"
        "v_mul_f32 v87, s54, v87\n"
        "v_cvt_pk_bf16_f32 v80, 0, v80\n"
        "v_mfma_f32_16x16x32_bf16 v[32:35], v[176:179], a[8:11], v[32:35]\n"
        "global_load_lds_dwordx4 v254, s[60:61] offset:0 sc1\n"
        "v_cvt_pk_bf16_f32 v81, 0, v81\n"
        "v_mfma_f32_16x16x32_bf16 v[32:35], v[184:187], a[12:15], v[32:35]\n"
        "v_cvt_pk_bf16_f32 v82, 0, v82\n"
        "v_cvt_pk_bf16_f32 v83, 0, v83\n"
        "v_mfma_f32_16x16x32_bf16 v[36:39], v[188:191], a[12:15], v[36:39]\n"
        "v_cvt_pk_bf16_f32 v84, 0, v84\n"
        "v_cvt_pk_bf16_f32 v85, 0, v85\n"
        "v_mfma_f32_16x16x32_bf16 v[36:39], v[180:183], a[8:11], v[36:39]\n"
        "global_load_lds_dwordx4 v254, s[60:61] offset:1024 sc1\n"
        "v_mfma_f32_16x16x32_bf16 v[36:39], v[172:175], a[4:7], v[36:39]\n"
        "v_cvt_pk_bf16_f32 v86, 0, v86\n"
        "v_cvt_pk_bf16_f32 v87, 0, v87\n"
        "v_mfma_f32_16x16x32_bf16 v[36:39], v[164:167], a[0:3], v[36:39]\n"
        "v_add_f32 v80, v80, v136\n"
        "v_add_f32 v81, v81, v137\n"
        "v_mfma_f32_16x16x32_bf16 v[44:47], v[164:167], a[16:19], v[44:47]\n"
        "v_add_f32 v82, v82, v138\n"
        "v_add_f32 v83, v83, v139\n"
        "v_mfma_f32_16x16x32_bf16 v[44:47], v[172:175], a[20:23], v[44:47]\n"
        "global_load_lds_dwordx4 v254, s[60:61] offset:2048 sc1\n"
        "v_add_f32 v84, v84, v140\n"
        "v_mfma_f32_16x16x32_bf16 v[44:47], v[180:183], a[24:27], v[44:47]\n"
        "LHB0_%=:\n"
        "s_cbranch_execz LHRB_%=\n"
        "v_add_f32 v85, v85, v141\n"
        "v_add_f32 v86, v86, v142\n"
        "v_mfma_f32_16x16x32_bf16 v[44:47], v[188:191], a[28:31], v[44:47]\n"
        "v_add_f32 v87, v87, v143\n"
        "v_mfma_f32_16x16x32_bf16 v[40:43], v[184:187], a[28:31], v[40:43]\n"
        "global_load_lds_dwordx4 v254, s[60:61] offset:3072 sc1\n"
        "s_add_u32 m0, s73, 0x20000\n"
        "v_mfma_f32_16x16x32_bf16 v[40:43], v[176:179], a[24:27], v[40:43]\n"
        "s_add_u32 s60, s68, s66\n"
        "s_addc_u32 s61, s69, 0\n"
        "s_xor_b32 s66, s17, 7\n"
        "v_mfma_f32_16x16x32_bf16 v[40:43], v[168:171], a[20:23], v[40:43]\n"
        "s_lshl_b32 s66, s66, 15\n"
        "s_waitcnt lgkmcnt(0)\n"
        "v_mfma_f32_16x16x32_bf16 v[40:43], v[160:163], a[16:19], v[40:43]\n"
        "s_waitcnt vmcnt(25)\n"
        "v_mfma_f32_16x16x32_bf16 v[48:51], v[160:163], a[192:195], v[48:51]\n"
        "s_barrier\n"
        "v_mfma_f32_16x16x32_bf16 v[48:51], v[168:171], a[196:199], v[48:51]\n"
        "ds_read_b64_tr_b16 a[224:225], v153 offset:0\n"
        "global_load_lds_dwordx4 v254, s[60:61] offset:0 sc1\n"
        "v_mfma_f32_16x16x32_bf16 v[48:51], v[176:179], a[200:203], v[48:51]\n"
        "ds_read_b64_tr_b16 a[226:227], v153 offset:4096\n"
        "v_mfma_f32_16x16x32_bf16 v[48:51], v[184:187], a[204:207], v[48:51]\n"
        "ds_read_b64_tr_b16 a[228:229], v154 offset:0\n"
        "v_mfma_f32_16x16x32_bf16 v[52:55], v[188:191], a[204:207], v[52:55]\n"
        "ds_read_b64_tr_b16 a[230:231], v154 offset:4096\n"
        "v_mfma_f32_16x16x32_bf16 v[52:55], v[180:183], a[200:203], v[52:55]\n"
        "ds_read_b64_tr_b16 a[232:233], v153 offset:8192\n"
        "global_load_lds_dwordx4 v254, s[60:61] offset:1024 sc1\n"
        "v_mfma_f32_16x16x32_bf16 v[52:55], v[172:175], a[196:199], v[52:55]\n"
        "ds_read_b64_tr_b16 a[234:235], v153 offset:12288\n"
        "v_mfma_f32_16x16x32_bf16 v[52:55], v[164:167], a[192:195], v[52:55]\n"
        "ds_read_b64_tr_b16 a[236:237], v154 offset:8192\n"
        "v_mfma_f32_16x16x32_bf16 v[60:63], v[164:167], a[208:211], v[60:63]\n"
        "ds_read_b64_tr_b16 a[238:239], v154 offset:12288\n"
        "v_mfma_f32_16x16x32_bf16 v[60:63], v[172:175], a[212:215], v[60:63]\n"
        "ds_read_b128 a[0:3], v254 offset:16384\n"
        "global_load_lds_dwordx4 v254, s[60:61] offset:2048 sc1\n"
        "v_mfma_f32_16x16x32_bf16 v[60:63], v[180:183], a[216:219], v[60:63]\n"
        "ds_read_b128 a[4:7], v254 offset:20480\n"
        "v_mfma_f32_16x16x32_bf16 v[60:63], v[188:191], a[220:223], v[60:63]\n"
        "ds_read_b128 a[8:11], v254 offset:24576\n"
        "v_mfma_f32_16x16x32_bf16 v[56:59], v[184:187], a[220:223], v[56:59]\n"
        "ds_read_b128 a[12:15], v254 offset:28672\n"
        "v_mfma_f32_16x16x32_bf16 v[56:59], v[176:179], a[216:219], v[56:59]\n"
        "ds_read_b128 a[16:19], v254 offset:17408\n"
        "global_load_lds_dwordx4 v254, s[60:61] offset:3072 sc1\n"
        "s_add_u32 m0, s73, 0x24000\n"
        "v_mfma_f32_16x16x32_bf16 v[56:59], v[168:171], a[212:215], v[56:59]\n"
        "ds_read_b128 a[20:23], v254 offset:21504\n"
        "s_add_u32 s60, s68, s66\n"
        "s_addc_u32 s61, s69, 0\n"
        "v_mfma_f32_16x16x32_bf16 v[56:59], v[160:163], a[208:211], v[56:59]\n"
        "ds_read_b128 a[24:27], v254 offset:25600\n"
        "ds_read_b128 a[28:31], v254 offset:29696\n"
        "ds_read_b128 a[192:195], v254 offset:18432\n"
        "s_sub_u32 s66, 15, s12\n"
        "s_waitcnt lgkmcnt(8)\n"
        "v_mfma_f32_16x16x32_bf16 v[32:35], v[192:195], a[0:3], v[32:35]\n"
        "ds_read_b128 a[196:199], v254 offset:22528\n"
        "s_waitcnt lgkmcnt(8)\n"
        "v_mfma_f32_16x16x32_bf16 v[32:35], v[200:203], a[4:7], v[32:35]\n"
        "ds_read_b128 a[200:203], v254 offset:26624\n"
        "s_waitcnt lgkmcnt(8)\n"
        "v_mfma_f32_16x16x32_bf16 v[32:35], v[208:211], a[8:11], v[32:35]\n"
        "ds_read_b128 a[204:207], v254 offset:30720\n"
        "s_waitcnt lgkmcnt(8)\n"
        "v_mfma_f32_16x16x32_bf16 v[32:35], v[216:219], a[12:15], v[32:35]\n"
        "ds_read_b128 a[208:211], v254 offset:19456\n"
        "s_cmp_lt_u32 s76, 2\n"
        "s_cselect_b32 s66, s12, s66\n"
        "v_mfma_f32_16x16x32_bf16 v[36:39], v[220:223], a[12:15], v[36:39]\n"
        "ds_read_b128 a[212:215], v254 offset:23552\n"
        "s_mul_i32 s66, s66, 1052672\n"
        "s_add_u32 s66, s66, s67\n"
        "v_mfma_f32_16x16x32_bf16 v[36:39], v[212:215], a[8:11], v[36:39]\n"
        "ds_read_b128 a[216:219], v254 offset:27648\n"
        "s_add_u32 s68, s20, s66\n"
        "s_addc_u32 s69, s21, 0\n"
        "v_mfma_f32_16x16x32_bf16 v[36:39], v[204:207], a[4:7], v[36:39]\n"
        "ds_read_b128 a[220:223], v254 offset:31744\n"
        "s_xor_b32 s66, s17, 0\n"
        "s_lshl_b32 s66, s66, 15\n"
        "v_mfma_f32_16x16x32_bf16 v[36:39], v[196:199], a[0:3], v[36:39]\n"
        "s_waitcnt lgkmcnt(7)\n"
        "v_mfma_f32_16x16x32_bf16 v[44:47], v[196:199], a[16:19], v[44:47]\n"
        "global_store_dwordx4 v229, v[92:95], s[24:25] offset:1024 sc1\n"
        "v_mfma_f32_16x16x32_bf16 v[44:47], v[204:207], a[20:23], v[44:47]\n"
        "v_mfma_f32_16x16x32_bf16 v[44:47], v[212:215], a[24:27], v[44:47]\n"
        "v_cvt_pk_bf16_f32 v92, v80, v81\n"
        "v_cvt_pk_bf16_f32 v93, v82, v83\n"
        "v_mfma_f32_16x16x32_bf16 v[44:47], v[220:223], a[28:31], v[44:47]\n"
        "v_cvt_pk_bf16_f32 v94, v84, v85\n"
        "v_cvt_pk_bf16_f32 v95, v86, v87\n"
        "v_mfma_f32_16x16x32_bf16 v[40:43], v[216:219], a[28:31], v[40:43]\n"
        "v_permlane16_swap_b32 v92, v94\n"
        "v_mfma_f32_16x16x32_bf16 v[40:43], v[208:211], a[24:27], v[40:43]\n"
        "s_waitcnt lgkmcnt(1)\n"
        "v_permlane16_swap_b32 v93, v95\n"
        "v_mfma_f32_16x16x32_bf16 v[40:43], v[200:203], a[20:23], v[40:43]\n"
        "global_store_dwordx4 v229, v[88:91], s[24:25] offset:2048 sc1\n"
        "v_mfma_f32_16x16x32_bf16 v[40:43], v[192:195], a[16:19], v[40:43]\n"
        "s_waitcnt vmcnt(27)\n"
        "v_mfma_f32_16x16x32_bf16 v[48:51], v[192:195], a[192:195], v[48:51]\n"
        "s_barrier\n"
        "v_mfma_f32_16x16x32_bf16 v[48:51], v[200:203], a[196:199], v[48:51]\n"
        "ds_read_b128 a[0:3], v244 offset:0\n"
        "global_load_lds_dwordx4 v254, s[60:61] offset:0 sc1\n"
        "v_mfma_f32_16x16x32_bf16 v[48:51], v[208:211], a[200:203], v[48:51]\n"
        "ds_read_b128 a[4:7], v244 offset:4096\n"
        "v_mfma_f32_16x16x32_bf16 v[48:51], v[216:219], a[204:207], v[48:51]\n"
        "ds_read_b128 a[8:11], v244 offset:8192\n"
        "v_mfma_f32_16x16x32_bf16 v[52:55], v[220:223], a[204:207], v[52:55]\n"
        "ds_read_b128 a[12:15], v244 offset:12288\n"
        "v_mfma_f32_16x16x32_bf16 v[52:55], v[212:215], a[200:203], v[52:55]\n"
        "ds_read_b128 a[16:19], v244 offset:1024\n"
        "global_load_lds_dwordx4 v254, s[60:61] offset:1024 sc1\n"
        "v_mfma_f32_16x16x32_bf16 v[52:55], v[204:207], a[196:199], v[52:55]\n"
        "ds_read_b128 a[20:23], v244 offset:5120\n"
        "v_mfma_f32_16x16x32_bf16 v[52:55], v[196:199], a[192:195], v[52:55]\n"
        "ds_read_b128 a[24:27], v244 offset:9216\n"
        "s_waitcnt lgkmcnt(7)\n"
        "v_mfma_f32_16x16x32_bf16 v[60:63], v[196:199], a[208:211], v[60:63]\n"
        "ds_read_b128 a[28:31], v244 offset:13312\n"
        "v_mfma_f32_16x16x32_bf16 v[60:63], v[204:207], a[212:215], v[60:63]\n"
        "ds_read_b128 a[192:195], v244 offset:2048\n"
        "global_load_lds_dwordx4 v254, s[60:61] offset:2048 sc1\n"
        "v_mfma_f32_16x16x32_bf16 v[60:63], v[212:215], a[216:219], v[60:63]\n"
        "ds_read_b128 a[196:199], v244 offset:6144\n"
        "v_mfma_f32_16x16x32_bf16 v[60:63], v[220:223], a[220:223], v[60:63]\n"
        "ds_read_b128 a[200:203], v244 offset:10240\n"
        "v_mfma_f32_16x16x32_bf16 v[56:59], v[216:219], a[220:223], v[56:59]\n"
        "ds_read_b128 a[204:207], v244 offset:14336\n"
        "s_waitcnt lgkmcnt(8)\n"
        "v_mfma_f32_16x16x32_bf16 v[56:59], v[208:211], a[216:219], v[56:59]\n"
        "ds_read_b128 a[216:219], v244 offset:11264\n"
        "global_load_lds_dwordx4 v254, s[60:61] offset:3072 sc1\n"
        "s_add_u32 m0, s73, 0x0\n"
        "v_mfma_f32_16x16x32_bf16 v[56:59], v[200:203], a[212:215], v[56:59]\n"
        "ds_read_b128 a[212:215], v244 offset:7168\n"
        "s_add_u32 s60, s68, s66\n"
        "s_addc_u32 s61, s69, 0\n"
        "v_mfma_f32_16x16x32_bf16 v[56:59], v[192:195], a[208:211], v[56:59]\n"
        "ds_read_b128 a[208:211], v244 offset:3072\n"
        "s_xor_b32 s66, s17, 1\n"
        "s_lshl_b32 s66, s66, 15\n"
        "v_mfma_f32_16x16x32_bf16 v[0:3], v[112:115], a[0:3], v[0:3]\n"
        "ds_read_b128 a[220:223], v244 offset:15360\n"
        "v_mfma_f32_16x16x32_bf16 v[0:3], v[120:123], a[4:7], v[0:3]\n"
        "global_load_lds_dwordx4 v254, s[60:61] offset:0 sc1\n"
        "v_mfma_f32_16x16x32_bf16 v[0:3], a[64:67], a[8:11], v[0:3]\n"
        "v_mfma_f32_16x16x32_bf16 v[0:3], a[72:75], a[12:15], v[0:3]\n"
        "v_mfma_f32_16x16x32_bf16 v[4:7], a[76:79], a[12:15], v[4:7]\n"
        "global_load_lds_dwordx4 v254, s[60:61] offset:1024 sc1\n"
        "v_mfma_f32_16x16x32_bf16 v[4:7], a[68:71], a[8:11], v[4:7]\n"
        "s_waitcnt lgkmcnt(3)\n"
        "v_mfma_f32_16x16x32_bf16 v[4:7], v[124:127], a[4:7], v[4:7]\n"
        "v_mfma_f32_16x16x32_bf16 v[4:7], v[116:119], a[0:3], v[4:7]\n"
        "v_mfma_f32_16x16x32_bf16 v[12:15], v[116:119], a[16:19], v[12:15]\n"
        "global_load_lds_dwordx4 v254, s[60:61] offset:2048 sc1\n"
        "v_mfma_f32_16x16x32_bf16 v[12:15], v[124:127], a[20:23], v[12:15]\n"
        "v_mfma_f32_16x16x32_bf16 v[12:15], a[68:71], a[24:27], v[12:15]\n"
        "v_mfma_f32_16x16x32_bf16 v[12:15], a[76:79], a[28:31], v[12:15]\n"
        "global_load_lds_dwordx4 v254, s[60:61] offset:3072 sc1\n"
        "s_add_u32 m0, s73, 0x4000\n"
        "v_mfma_f32_16x16x32_bf16 v[8:11], a[72:75], a[28:31], v[8:11]\n"
        "s_add_u32 s60, s68, s66\n"
        "s_addc_u32 s61, s69, 0\n"
        "s_xor_b32 s66, s17, 2\n"
        "v_mfma_f32_16x16x32_bf16 v[8:11], a[64:67], a[24:27], v[8:11]\n"
        "s_lshl_b32 s66, s66, 15\n"
        "s_lshl2_add_u32 s15, s75, s10\n"
        "s_mul_i32 s16, s13, 263168\n"
        "v_mfma_f32_16x16x32_bf16 v[8:11], v[120:123], a[20:23], v[8:11]\n"
        "s_lshl_b32 s15, s15, 14\n"
        "s_add_u32 s16, s16, s15\n"
        "s_add_u32 s16, s16, s73\n"
        "v_mfma_f32_16x16x32_bf16 v[8:11], v[112:115], a[16:19], v[8:11]\n"
        "s_waitcnt vmcnt(31)\n"
        "s_add_u32 s16, s16, 0x4000000\n"
        "v_mfma_f32_16x16x32_bf16 v[16:19], v[112:115], a[192:195], v[16:19]\n"
        "s_barrier\n"
        "v_mfma_f32_16x16x32_bf16 v[16:19], v[120:123], a[196:199], v[16:19]\n"
        "ds_read_b128 a[0:3], v244 offset:16384\n"
        "global_load_lds_dwordx4 v254, s[60:61] offset:0 sc1\n"
        "v_mfma_f32_16x16x32_bf16 v[16:19], a[64:67], a[200:203], v[16:19]\n"
        "ds_read_b128 a[4:7], v244 offset:20480\n"
        "v_mfma_f32_16x16x32_bf16 v[16:19], a[72:75], a[204:207], v[16:19]\n"
        "ds_read_b128 a[8:11], v244 offset:24576\n"
        "v_mfma_f32_16x16x32_bf16 v[20:23], a[76:79], a[204:207], v[20:23]\n"
        "ds_read_b128 a[12:15], v244 offset:28672\n"
        "s_waitcnt lgkmcnt(4)\n"
        "v_mfma_f32_16x16x32_bf16 v[20:23], a[68:71], a[200:203], v[20:23]\n"
        "ds_read_b128 a[16:19], v244 offset:17408\n"
        "global_load_lds_dwordx4 v254, s[60:61] offset:1024 sc1\n"
        "v_mfma_f32_16x16x32_bf16 v[20:23], v[124:127], a[196:199], v[20:23]\n"
        "ds_read_b128 a[20:23], v244 offset:21504\n"
        "v_mfma_f32_16x16x32_bf16 v[20:23], v[116:119], a[192:195], v[20:23]\n"
        "ds_read_b128 a[24:27], v244 offset:25600\n"
        "v_mfma_f32_16x16x32_bf16 v[28:31], v[116:119], a[208:211], v[28:31]\n"
        "ds_read_b128 a[28:31], v244 offset:29696\n"
        "v_mfma_f32_16x16x32_bf16 v[28:31], v[124:127], a[212:215], v[28:31]\n"
        "ds_read_b128 a[192:195], v244 offset:18432\n"
        "global_load_lds_dwordx4 v254, s[60:61] offset:2048 sc1\n"
        "v_mfma_f32_16x16x32_bf16 v[28:31], a[68:71], a[216:219], v[28:31]\n"
        "ds_read_b128 a[196:199], v244 offset:22528\n"
        "v_mfma_f32_16x16x32_bf16 v[28:31], a[76:79], a[220:223], v[28:31]\n"
        "ds_read_b128 a[200:203], v244 offset:26624\n"
        "v_mfma_f32_16x16x32_bf16 v[24:27], a[72:75], a[220:223], v[24:27]\n"
        "ds_read_b128 a[204:207], v244 offset:30720\n"
        "s_waitcnt lgkmcnt(8)\n"
        "v_mfma_f32_16x16x32_bf16 v[24:27], a[64:67], a[216:219], v[24:27]\n"
        "ds_read_b128 a[216:219], v244 offset:27648\n"
        "global_load_lds_dwordx4 v254, s[60:61] offset:3072 sc1\n"
        "s_add_u32 m0, s73, 0x10000\n"
        "v_mfma_f32_16x16x32_bf16 v[24:27], v[120:123], a[212:215], v[24:27]\n"
        "ds_read_b128 a[212:215], v244 offset:23552\n"
        "s_add_u32 s60, s68, s66\n"
        "s_addc_u32 s61, s69, 0\n"
        "v_mfma_f32_16x16x32_bf16 v[24:27], v[112:115], a[208:211], v[24:27]\n"
        "ds_read_b128 a[208:211], v244 offset:19456\n"
        "s_xor_b32 s66, s17, 3\n"
        "s_lshl_b32 s66, s66, 15\n"
        "v_mfma_f32_16x16x32_bf16 v[0:3], a[80:83], a[0:3], v[0:3]\n"
        "ds_read_b128 a[220:223], v244 offset:31744\n"
        "v_mfma_f32_16x16x32_bf16 v[0:3], a[88:91], a[4:7], v[0:3]\n"
        "global_store_dwordx4 v229, v[92:95], s[24:25] offset:3072 sc1\n"
        "v_mfma_f32_16x16x32_bf16 v[0:3], a[160:163], a[8:11], v[0:3]\n"
        "v_mfma_f32_16x16x32_bf16 v[0:3], a[168:171], a[12:15], v[0:3]\n"
        "v_mfma_f32_16x16x32_bf16 v[4:7], a[172:175], a[12:15], v[4:7]\n"
        "s_waitcnt lgkmcnt(4)\n"
        "v_mfma_f32_16x16x32_bf16 v[4:7], a[164:167], a[8:11], v[4:7]\n"
        "v_mfma_f32_16x16x32_bf16 v[4:7], a[92:95], a[4:7], v[4:7]\n"
        "v_mfma_f32_16x16x32_bf16 v[4:7], a[84:87], a[0:3], v[4:7]\n"
        "s_and_b32 s67, s10, 1\n"
        "s_lshl_b32 s67, s67, 12\n"
        "v_mfma_f32_16x16x32_bf16 v[12:15], a[84:87], a[16:19], v[12:15]\n"
        "v_mfma_f32_16x16x32_bf16 v[12:15], a[92:95], a[20:23], v[12:15]\n"
        "v_mfma_f32_16x16x32_bf16 v[12:15], a[164:167], a[24:27], v[12:15]\n"
        "v_mfma_f32_16x16x32_bf16 v[12:15], a[172:175], a[28:31], v[12:15]\n"
        "v_mfma_f32_16x16x32_bf16 v[8:11], a[168:171], a[28:31], v[8:11]\n"
        "v_mfma_f32_16x16x32_bf16 v[8:11], a[160:163], a[24:27], v[8:11]\n"
        "v_mfma_f32_16x16x32_bf16 v[8:11], a[88:91], a[20:23], v[8:11]\n"
        "v_mfma_f32_16x16x32_bf16 v[8:11], a[80:83], a[16:19], v[8:11]\n"
        "s_waitcnt vmcnt(32)\n"
        "v_mfma_f32_16x16x32_bf16 v[16:19], a[80:83], a[192:195], v[16:19]\n"
        "s_barrier\n"
        "v_mfma_f32_16x16x32_bf16 v[16:19], a[88:91], a[196:199], v[16:19]\n"
        "ds_read_b128 a[0:3], v254 offset:32768\n"
        "global_load_lds_dwordx4 v254, s[60:61] offset:0 sc1\n"
        "v_mfma_f32_16x16x32_bf16 v[16:19], a[160:163], a[200:203], v[16:19]\n"
        "ds_read_b128 a[4:7], v254 offset:36864\n"
        "v_mfma_f32_16x16x32_bf16 v[16:19], a[168:171], a[204:207], v[16:19]\n"
        "ds_read_b128 a[8:11], v254 offset:40960\n"
        "v_mfma_f32_16x16x32_bf16 v[20:23], a[172:175], a[204:207], v[20:23]\n"
        "ds_read_b128 a[12:15], v254 offset:45056\n"
        "s_waitcnt lgkmcnt(4)\n"
        "v_mfma_f32_16x16x32_bf16 v[20:23], a[164:167], a[200:203], v[20:23]\n"
        "ds_read_b128 a[16:19], v254 offset:33792\n"
        "global_load_lds_dwordx4 v254, s[60:61] offset:1024 sc1\n"
        "v_mfma_f32_16x16x32_bf16 v[20:23], a[92:95], a[196:199], v[20:23]\n"
        "ds_read_b128 a[20:23], v254 offset:37888\n"
        "v_mfma_f32_16x16x32_bf16 v[20:23], a[84:87], a[192:195], v[20:23]\n"
        "ds_read_b128 a[24:27], v254 offset:41984\n"
        "v_mfma_f32_16x16x32_bf16 v[28:31], a[84:87], a[208:211], v[28:31]\n"
        "ds_read_b128 a[28:31], v254 offset:46080\n"
        "v_mfma_f32_16x16x32_bf16 v[28:31], a[92:95], a[212:215], v[28:31]\n"
        "ds_read_b128 a[192:195], v254 offset:34816\n"
        "global_load_lds_dwordx4 v254, s[60:61] offset:2048 sc1\n"
        "v_mfma_f32_16x16x32_bf16 v[28:31], a[164:167], a[216:219], v[28:31]\n"
        "ds_read_b128 a[196:199], v254 offset:38912\n"
        "v_mfma_f32_16x16x32_bf16 v[28:31], a[172:175], a[220:223], v[28:31]\n"
        "ds_read_b128 a[200:203], v254 offset:43008\n"
        "v_mfma_f32_16x16x32_bf16 v[24:27], a[168:171], a[220:223], v[24:27]\n"
        "ds_read_b128 a[204:207], v254 offset:47104\n"
        "s_waitcnt lgkmcnt(8)\n"
        "v_mfma_f32_16x16x32_bf16 v[24:27], a[160:163], a[216:219], v[24:27]\n"
        "ds_read_b128 a[216:219], v254 offset:44032\n"
        "global_load_lds_dwordx4 v254, s[60:61] offset:3072 sc1\n"
        "s_add_u32 m0, s73, 0x14000\n"
        "v_mfma_f32_16x16x32_bf16 v[24:27], a[88:91], a[212:215], v[24:27]\n"
        "ds_read_b128 a[212:215], v254 offset:39936\n"
        "s_add_u32 s60, s68, s66\n"
        "s_addc_u32 s61, s69, 0\n"
        "v_mfma_f32_16x16x32_bf16 v[24:27], a[80:83], a[208:211], v[24:27]\n"
        "ds_read_b128 a[208:211], v254 offset:35840\n"
        "s_xor_b32 s66, s17, 4\n"
        "s_lshl_b32 s66, s66, 15\n"
        "v_mfma_f32_16x16x32_bf16 v[0:3], a[176:179], a[0:3], v[0:3]\n"
        "ds_read_b128 a[220:223], v254 offset:48128\n"
        "v_mfma_f32_16x16x32_bf16 v[0:3], a[184:187], a[4:7], v[0:3]\n"
        "v_mfma_f32_16x16x32_bf16 v[0:3], a[32:35], a[8:11], v[0:3]\n"
        "v_mfma_f32_16x16x32_bf16 v[0:3], a[40:43], a[12:15], v[0:3]\n"
        "v_mfma_f32_16x16x32_bf16 v[4:7], a[44:47], a[12:15], v[4:7]\n"
        "s_waitcnt lgkmcnt(4)\n"
        "v_mfma_f32_16x16x32_bf16 v[4:7], a[36:39], a[8:11], v[4:7]\n"
        "v_mfma_f32_16x16x32_bf16 v[4:7], a[188:191], a[4:7], v[4:7]\n"
        "v_mfma_f32_16x16x32_bf16 v[4:7], a[180:183], a[0:3], v[4:7]\n"
        "v_mfma_f32_16x16x32_bf16 v[12:15], a[180:183], a[16:19], v[12:15]\n"
        "v_mfma_f32_16x16x32_bf16 v[12:15], a[188:191], a[20:23], v[12:15]\n"
        "v_mfma_f32_16x16x32_bf16 v[12:15], a[36:39], a[24:27], v[12:15]\n"
        "v_mfma_f32_16x16x32_bf16 v[12:15], a[44:47], a[28:31], v[12:15]\n"
        "v_mfma_f32_16x16x32_bf16 v[8:11], a[40:43], a[28:31], v[8:11]\n"
        "v_mfma_f32_16x16x32_bf16 v[8:11], a[32:35], a[24:27], v[8:11]\n"
        "v_mfma_f32_16x16x32_bf16 v[8:11], a[184:187], a[20:23], v[8:11]\n"
        "v_mfma_f32_16x16x32_bf16 v[8:11], a[176:179], a[16:19], v[8:11]\n"
        "s_waitcnt vmcnt(32)\n"
        "v_mfma_f32_16x16x32_bf16 v[16:19], a[176:179], a[192:195], v[16:19]\n"
        "s_barrier\n"
        "v_mfma_f32_16x16x32_bf16 v[16:19], a[184:187], a[196:199], v[16:19]\n"
        "ds_read_b128 a[0:3], v254 offset:49152\n"
        "global_load_lds_dwordx4 v254, s[60:61] offset:0 sc1\n"
        "v_mfma_f32_16x16x32_bf16 v[16:19], a[32:35], a[200:203], v[16:19]\n"
        "ds_read_b128 a[4:7], v254 offset:53248\n"
        "v_mfma_f32_16x16x32_bf16 v[16:19], a[40:43], a[204:207], v[16:19]\n"
        "ds_read_b128 a[8:11], v254 offset:57344\n"
        "v_mfma_f32_16x16x32_bf16 v[20:23], a[44:47], a[204:207], v[20:23]\n"
        "ds_read_b128 a[12:15], v254 offset:61440\n"
        "v_mfma_f32_16x16x32_bf16 v[20:23], a[36:39], a[200:203], v[20:23]\n"
        "ds_read_b128 a[28:31], v254 offset:62464\n"
        "global_load_lds_dwordx4 v254, s[60:61] offset:1024 sc1\n"
        "v_mfma_f32_16x16x32_bf16 v[20:23], a[188:191], a[196:199], v[20:23]\n"
        "ds_read_b128 a[24:27], v254 offset:58368\n"
        "v_mfma_f32_16x16x32_bf16 v[20:23], a[180:183], a[192:195], v[20:23]\n"
        "ds_read_b128 a[20:23], v254 offset:54272\n"
        "s_waitcnt lgkmcnt(7)\n"
        "v_mfma_f32_16x16x32_bf16 v[28:31], a[180:183], a[208:211], v[28:31]\n"
        "ds_read_b128 a[16:19], v254 offset:50176\n"
        "v_mfma_f32_16x16x32_bf16 v[28:31], a[188:191], a[212:215], v[28:31]\n"
        "ds_read_b128 a[192:195], v254 offset:51200\n"
        "s_waitcnt lgkmcnt(8)\n"
        "v_mfma_f32_16x16x32_bf16 v[0:3], a[48:51], a[0:3], v[0:3]\n"
        "ds_read_b128 a[196:199], v254 offset:55296\n"
        "s_waitcnt lgkmcnt(8)\n"
        "v_mfma_f32_16x16x32_bf16 v[0:3], a[56:59], a[4:7], v[0:3]\n"
        "ds_read_b128 a[200:203], v254 offset:59392\n"
        "s_waitcnt lgkmcnt(8)\n"
        "v_mfma_f32_16x16x32_bf16 v[0:3], a[224:227], a[8:11], v[0:3]\n"
        "ds_read_b128 a[204:207], v254 offset:63488\n"
        "s_waitcnt lgkmcnt(8)\n"
        "v_mfma_f32_16x16x32_bf16 v[0:3], a[232:235], a[12:15], v[0:3]\n"
        "global_load_lds_dwordx4 v254, s[60:61] offset:2048 sc1\n"
        "v_mfma_f32_16x16x32_bf16 v[4:7], a[236:239], a[12:15], v[4:7]\n"
        "v_mfma_f32_16x16x32_bf16 v[4:7], a[228:231], a[8:11], v[4:7]\n"
        "v_mfma_f32_16x16x32_bf16 v[4:7], a[60:63], a[4:7], v[4:7]\n"
        "global_load_lds_dwordx4 v254, s[60:61] offset:3072 sc1\n"
        "s_add_u32 m0, s73, 0x8000\n"
        "v_mfma_f32_16x16x32_bf16 v[4:7], a[52:55], a[0:3], v[4:7]\n"
        "s_add_u32 s60, s68, s66\n"
        "s_addc_u32 s61, s69, 0\n"
        "v_cvt_pk_bf16_f32 v0, v0, v1\n"
        "v_mfma_f32_16x16x32_bf16 v[28:31], a[36:39], a[216:219], v[28:31]\n"
        "s_waitcnt lgkmcnt(2)\n"
        "v_cvt_pk_bf16_f32 v1, v2, v3\n"
        "s_xor_b32 s66, s17, 5\n"
        "v_mfma_f32_16x16x32_bf16 v[28:31], a[44:47], a[220:223], v[28:31]\n"
        "s_lshl_b32 s66, s66, 15\n"
        "v_cvt_pk_bf16_f32 v2, v4, v5\n"
        "v_cvt_pk_bf16_f32 v3, v6, v7\n"
        "v_mfma_f32_16x16x32_bf16 v[8:11], a[232:235], a[28:31], v[8:11]\n"
        "global_store_dwordx4 v254, v[0:3], s[28:29] offset:0 sc1\n"
        "v_mfma_f32_16x16x32_bf16 v[8:11], a[224:227], a[24:27], v[8:11]\n"
        "v_mfma_f32_16x16x32_bf16 v[8:11], a[56:59], a[20:23], v[8:11]\n"
        "v_mfma_f32_16x16x32_bf16 v[8:11], a[48:51], a[16:19], v[8:11]\n"
        "s_waitcnt vmcnt(9)\n"
        "v_mfma_f32_16x16x32_bf16 v[12:15], a[52:55], a[16:19], v[12:15]\n"
        "s_barrier\n"
        "v_mfma_f32_16x16x32_bf16 v[12:15], a[60:63], a[20:23], v[12:15]\n"
        "ds_read_b128 a[0:3], v244 offset:32768\n"
        "v_mfma_f32_16x16x32_bf16 v[12:15], a[228:231], a[24:27], v[12:15]\n"
        "ds_read_b128 a[4:7], v244 offset:36864\n"
        "global_load_lds_dwordx4 v254, s[60:61] offset:0 sc1\n"
        "v_mfma_f32_16x16x32_bf16 v[12:15], a[236:239], a[28:31], v[12:15]\n"
        "ds_read_b128 a[8:11], v244 offset:40960\n"
        "v_cvt_pk_bf16_f32 v8, v8, v9\n"
        "v_mfma_f32_16x16x32_bf16 v[24:27], a[40:43], a[220:223], v[24:27]\n"
        "ds_read_b128 a[220:223], v254 offset:64512\n"
        "v_cvt_pk_bf16_f32 v9, v10, v11\n"
        "v_mfma_f32_16x16x32_bf16 v[24:27], a[32:35], a[216:219], v[24:27]\n"
        "ds_read_b128 a[216:219], v254 offset:60416\n"
        "v_mfma_f32_16x16x32_bf16 v[24:27], a[184:187], a[212:215], v[24:27]\n"
        "ds_read_b128 a[212:215], v254 offset:56320\n"
        "v_cvt_pk_bf16_f32 v10, v12, v13\n"
        "v_mfma_f32_16x16x32_bf16 v[24:27], a[176:179], a[208:211], v[24:27]\n"
        "ds_read_b128 a[208:211], v254 offset:52224\n"
        "v_cvt_pk_bf16_f32 v11, v14, v15\n"
        "v_mfma_f32_16x16x32_bf16 v[16:19], a[48:51], a[192:195], v[16:19]\n"
        "global_store_dwordx4 v254, v[8:11], s[28:29] offset:1024 sc1\n"
        "ds_read_b128 a[12:15], v244 offset:45056\n"
        "ds_read_b128 a[16:19], v244 offset:33792\n"
        "s_waitcnt lgkmcnt(8)\n"
        "v_mfma_f32_16x16x32_bf16 v[16:19], a[56:59], a[196:199], v[16:19]\n"
        "ds_read_b128 a[20:23], v244 offset:37888\n"
        "v_mfma_f32_16x16x32_bf16 v[16:19], a[224:227], a[200:203], v[16:19]\n"
        "ds_read_b128 a[24:27], v244 offset:41984\n"
        "v_mfma_f32_16x16x32_bf16 v[16:19], a[232:235], a[204:207], v[16:19]\n"
        "ds_read_b128 a[28:31], v244 offset:46080\n"
        "v_mfma_f32_16x16x32_bf16 v[20:23], a[236:239], a[204:207], v[20:23]\n"
        "ds_read_b128 a[204:207], v244 offset:47104\n"
        "s_waitcnt lgkmcnt(8)\n"
        "v_mfma_f32_16x16x32_bf16 v[20:23], a[228:231], a[200:203], v[20:23]\n"
        "ds_read_b128 a[200:203], v244 offset:43008\n"
        "v_mfma_f32_16x16x32_bf16 v[20:23], a[60:63], a[196:199], v[20:23]\n"
        "ds_read_b128 a[196:199], v244 offset:38912\n"
        "v_cvt_pk_bf16_f32 v16, v16, v17\n"
        "v_mfma_f32_16x16x32_bf16 v[20:23], a[52:55], a[192:195], v[20:23]\n"
        "ds_read_b128 a[192:195], v244 offset:34816\n"
        "v_cvt_pk_bf16_f32 v17, v18, v19\n"
        "v_mfma_f32_16x16x32_bf16 v[24:27], a[232:235], a[220:223], v[24:27]\n"
        "global_load_lds_dwordx4 v254, s[60:61] offset:1024 sc1\n"
        "s_waitcnt lgkmcnt(7)\n"
        "v_mfma_f32_16x16x32_bf16 v[24:27], a[224:227], a[216:219], v[24:27]\n"
        "v_mfma_f32_16x16x32_bf16 v[24:27], a[56:59], a[212:215], v[24:27]\n"
        "v_mfma_f32_16x16x32_bf16 v[24:27], a[48:51], a[208:211], v[24:27]\n"
        "v_cvt_pk_bf16_f32 v18, v20, v21\n"
        "v_cvt_pk_bf16_f32 v19, v22, v23\n"
        "v_mfma_f32_16x16x32_bf16 v[28:31], a[52:55], a[208:211], v[28:31]\n"
        "global_store_dwordx4 v254, v[16:19], s[28:29] offset:2048 sc1\n"
        "ds_read_b128 a[208:211], v244 offset:35840\n"
        "v_lshlrev_b32 v159, 2, v232\n"
        "v_add_u32 v159, 0x0, v159\n"
        "v_mfma_f32_16x16x32_bf16 v[28:31], a[60:63], a[212:215], v[28:31]\n"
        "ds_read_b128 a[212:215], v244 offset:39936\n"
        "v_cvt_pk_bf16_f32 v24, v24, v25\n"
        "v_mfma_f32_16x16x32_bf16 v[28:31], a[228:231], a[216:219], v[28:31]\n"
        "ds_read_b128 a[216:219], v244 offset:44032\n"
        "v_cvt_pk_bf16_f32 v25, v26, v27\n"
        "v_mfma_f32_16x16x32_bf16 v[28:31], a[236:239], a[220:223], v[28:31]\n"
        "ds_read_b128 a[220:223], v244 offset:48128\n"
        "v_mfma_f32_16x16x32_bf16 v[32:35], v[112:115], a[0:3], v[32:35]\n"
        "v_mfma_f32_16x16x32_bf16 v[32:35], v[120:123], a[4:7], v[32:35]\n"
        "v_mfma_f32_16x16x32_bf16 v[32:35], a[64:67], a[8:11], v[32:35]\n"
        "global_load_lds_dwordx4 v254, s[60:61] offset:2048 sc1\n"
        "v_mfma_f32_16x16x32_bf16 v[32:35], a[72:75], a[12:15], v[32:35]\n"
        "v_mfma_f32_16x16x32_bf16 v[36:39], a[76:79], a[12:15], v[36:39]\n"
        "v_mfma_f32_16x16x32_bf16 v[36:39], a[68:71], a[8:11], v[36:39]\n"
        "s_waitcnt lgkmcnt(2)\n"
        "v_cvt_pk_bf16_f32 v26, v28, v29\n"
        "v_cvt_pk_bf16_f32 v27, v30, v31\n"
        "v_mfma_f32_16x16x32_bf16 v[36:39], v[124:127], a[4:7], v[36:39]\n"
        "global_store_dwordx4 v254, v[24:27], s[28:29] offset:3072 sc1\n"
        "s_add_u32 s28, s38, s16\n"
        "s_addc_u32 s29, s39, 0\n"
        "v_mfma_f32_16x16x32_bf16 v[36:39], v[116:119], a[0:3], v[36:39]\n"
        "v_mfma_f32_16x16x32_bf16 v[44:47], v[116:119], a[16:19], v[44:47]\n"
        "v_mfma_f32_16x16x32_bf16 v[44:47], v[124:127], a[20:23], v[44:47]\n"
        "v_mfma_f32_16x16x32_bf16 v[44:47], a[68:71], a[24:27], v[44:47]\n"
        "v_mfma_f32_16x16x32_bf16 v[44:47], a[76:79], a[28:31], v[44:47]\n"
        "v_mfma_f32_16x16x32_bf16 v[40:43], a[72:75], a[28:31], v[40:43]\n"
        "global_load_lds_dwordx4 v254, s[60:61] offset:3072 sc1\n"
        "s_add_u32 m0, s73, 0xc000\n"
        "v_mfma_f32_16x16x32_bf16 v[40:43], a[64:67], a[24:27], v[40:43]\n"
        "s_add_u32 s60, s68, s66\n"
        "s_addc_u32 s61, s69, 0\n"
        "s_xor_b32 s66, s17, 6\n"
        "v_mfma_f32_16x16x32_bf16 v[40:43], v[120:123], a[20:23], v[40:43]\n"
        "s_lshl_b32 s66, s66, 15\n"
        "v_and_b32 v150, 1, v233\n"
        "v_lshlrev_b32 v151, 11, v232\n"
        "v_mfma_f32_16x16x32_bf16 v[40:43], v[112:115], a[16:19], v[40:43]\n"
        "v_lshl_add_u32 v151, v150, 5, v151\n"
        "v_lshrrev_b32 v150, 1, v233\n"
        "v_mfma_f32_16x16x32_bf16 v[48:51], v[112:115], a[192:195], v[48:51]\n"
        "s_barrier\n"
        "v_mfma_f32_16x16x32_bf16 v[48:51], v[120:123], a[196:199], v[48:51]\n"
        "ds_read_b128 a[0:3], v244 offset:49152\n"
        "s_lshl_b32 s16, s63, 12\n"
        "v_lshl_add_u32 v151, v150, 4, v151\n"
        "v_mfma_f32_16x16x32_bf16 v[48:51], a[64:67], a[200:203], v[48:51]\n"
        "ds_read_b128 a[4:7], v244 offset:53248\n"
        "s_lshl1_add_u32 s16, s62, s16\n"
        "s_add_u32 s16, s16, 0x3700000\n"
        "v_mfma_f32_16x16x32_bf16 v[48:51], a[72:75], a[204:207], v[48:51]\n"
        "ds_read_b128 a[8:11], v244 offset:57344\n"
        "global_load_lds_dwordx4 v254, s[60:61] offset:0 sc1\n"
        "v_mfma_f32_16x16x32_bf16 v[52:55], a[76:79], a[204:207], v[52:55]\n"
        "ds_read_b128 a[12:15], v244 offset:61440\n"
        "s_add_u32 s58, s38, s16\n"
        "s_addc_u32 s59, s39, 0\n"
        "v_mfma_f32_16x16x32_bf16 v[52:55], a[68:71], a[200:203], v[52:55]\n"
        "ds_read_b128 a[16:19], v244 offset:50176\n"
        "s_add_u32 s16, s73, 0x4000\n"
        "v_lshrrev_b32 v148, 7, v254\n"
        "v_mfma_f32_16x16x32_bf16 v[52:55], v[124:127], a[196:199], v[52:55]\n"
        "ds_read_b128 a[20:23], v244 offset:54272\n"
        "v_lshlrev_b32 v148, 12, v148\n"
        "v_mfma_f32_16x16x32_bf16 v[52:55], v[116:119], a[192:195], v[52:55]\n"
        "ds_read_b128 a[24:27], v244 offset:58368\n"
        "v_and_b32 v150, 0x70, v254\n"
        "v_mfma_f32_16x16x32_bf16 v[60:63], v[116:119], a[208:211], v[60:63]\n"
        "ds_read_b128 a[28:31], v244 offset:62464\n"
        "v_add_u32 v148, v148, v150\n"
        "v_mfma_f32_16x16x32_bf16 v[60:63], v[124:127], a[212:215], v[60:63]\n"
        "ds_read_b128 a[192:195], v244 offset:51200\n"
        "s_waitcnt lgkmcnt(8)\n"
        "v_mfma_f32_16x16x32_bf16 v[60:63], a[68:71], a[216:219], v[60:63]\n"
        "ds_read_b128 a[196:199], v244 offset:55296\n"
        "global_load_lds_dwordx4 v254, s[60:61] offset:1024 sc1\n"
        "v_mfma_f32_16x16x32_bf16 v[60:63], a[76:79], a[220:223], v[60:63]\n"
        "ds_read_b128 a[200:203], v244 offset:59392\n"
        "v_lshrrev_b32 v149, 6, v254\n"
        "v_mfma_f32_16x16x32_bf16 v[56:59], a[72:75], a[220:223], v[56:59]\n"
        "ds_read_b128 a[204:207], v244 offset:63488\n"
        "v_lshlrev_b32 v149, 7, v149\n"
        "v_mfma_f32_16x16x32_bf16 v[56:59], a[64:67], a[216:219], v[56:59]\n"
        "ds_read_b128 a[216:219], v244 offset:60416\n"
        "v_bfe_u32 v150, v254, 4, 2\n"
        "v_mfma_f32_16x16x32_bf16 v[56:59], v[120:123], a[212:215], v[56:59]\n"
        "ds_read_b128 a[212:215], v244 offset:56320\n"
        "v_lshl_add_u32 v149, v150, 3, v149\n"
        "v_mfma_f32_16x16x32_bf16 v[56:59], v[112:115], a[208:211], v[56:59]\n"
        "ds_read_b128 a[208:211], v244 offset:52224\n"
        "v_mov_b32 v238, 0\n"
        "v_mfma_f32_16x16x32_bf16 v[32:35], a[80:83], a[0:3], v[32:35]\n"
        "ds_read_b128 a[220:223], v244 offset:64512\n"
        "s_waitcnt lgkmcnt(8)\n"
        "v_mfma_f32_16x16x32_bf16 v[32:35], a[88:91], a[4:7], v[32:35]\n"
        "global_load_lds_dwordx4 v254, s[60:61] offset:2048 sc1\n"
        "v_mfma_f32_16x16x32_bf16 v[32:35], a[160:163], a[8:11], v[32:35]\n"
        "v_mov_b32 v239, 0\n"
        "v_mov_b32 v242, 0\n"
        "v_mfma_f32_16x16x32_bf16 v[32:35], a[168:171], a[12:15], v[32:35]\n"
        "v_mov_b32 v243, 0\n"
        "v_and_b32 v246, 1, v232\n"
        "v_mfma_f32_16x16x32_bf16 v[36:39], a[172:175], a[12:15], v[36:39]\n"
        "v_lshlrev_b32 v246, 4, v246\n"
        "v_mov_b32 v247, 0x3f80\n"
        "v_mfma_f32_16x16x32_bf16 v[36:39], a[164:167], a[8:11], v[36:39]\n"
        "v_lshlrev_b32 v246, v246, v247\n"
        "v_lshrrev_b32 v247, 1, v232\n"
        "v_mfma_f32_16x16x32_bf16 v[36:39], a[92:95], a[4:7], v[36:39]\n"
        "v_lshlrev_b32 v248, 1, v233\n"
        "v_or_b32 v249, 1, v248\n"
        "v_mfma_f32_16x16x32_bf16 v[36:39], a[84:87], a[0:3], v[36:39]\n"
        "v_add_u32 v149, s16, v149\n"
        "v_mfma_f32_16x16x32_bf16 v[44:47], a[84:87], a[16:19], v[44:47]\n"
        "global_load_lds_dwordx4 v254, s[60:61] offset:3072 sc1\n"
        "s_add_u32 m0, s73, 0x18000\n"
        "v_mfma_f32_16x16x32_bf16 v[44:47], a[92:95], a[20:23], v[44:47]\n"
        "s_add_u32 s60, s68, s66\n"
        "s_addc_u32 s61, s69, 0\n"
        "s_xor_b32 s66, s17, 7\n"
        "v_mfma_f32_16x16x32_bf16 v[44:47], a[164:167], a[24:27], v[44:47]\n"
        "s_lshl_b32 s66, s66, 15\n"
        "v_mfma_f32_16x16x32_bf16 v[44:47], a[172:175], a[28:31], v[44:47]\n"
        "v_mfma_f32_16x16x32_bf16 v[40:43], a[168:171], a[28:31], v[40:43]\n"
        "s_waitcnt lgkmcnt(0)\n"
        "v_mfma_f32_16x16x32_bf16 v[40:43], a[160:163], a[24:27], v[40:43]\n"
        "v_mfma_f32_16x16x32_bf16 v[40:43], a[88:91], a[20:23], v[40:43]\n"
        "v_mfma_f32_16x16x32_bf16 v[40:43], a[80:83], a[16:19], v[40:43]\n"
        "v_mfma_f32_16x16x32_bf16 v[48:51], a[80:83], a[192:195], v[48:51]\n"
        "s_cmp_lg_u32 s40, 0\n"
        "s_cbranch_scc1 LW2_%=\n"
        "s_mov_b64 exec, 0xffff\n"
        "global_store_dwordx2 v234, v[250:251], s[48:49] sc1\n"
        "s_mov_b64 exec, -1\n"
        "LW2_%=:\n"
        "v_mfma_f32_16x16x32_bf16 v[48:51], a[88:91], a[196:199], v[48:51]\n"
        "s_barrier\n"
        "v_mfma_f32_16x16x32_bf16 v[48:51], a[160:163], a[200:203], v[48:51]\n"
        "ds_read_b128 a[0:3], v245 offset:0\n"
        "global_load_lds_dwordx4 v254, s[60:61] offset:0 sc1\n"
        "v_mfma_f32_16x16x32_bf16 v[48:51], a[168:171], a[204:207], v[48:51]\n"
        "ds_read_b128 a[4:7], v245 offset:4096\n"
        "v_bfe_u32 v234, v254, 4, 3\n"
        "v_mfma_f32_16x16x32_bf16 v[52:55], a[172:175], a[204:207], v[52:55]\n"
        "ds_read_b128 a[8:11], v245 offset:8192\n"
        "v_lshl_add_u32 v234, v234, 7, s14\n"
        "v_mfma_f32_16x16x32_bf16 v[52:55], a[164:167], a[200:203], v[52:55]\n"
        "ds_read_b128 a[12:15], v245 offset:12288\n"
        "s_lshl_b32 s14, s40, 6\n"
        "v_add_u32 v225, s14, v159\n"
        "v_mfma_f32_16x16x32_bf16 v[52:55], a[92:95], a[196:199], v[52:55]\n"
        "LHB1_%=:\n"
        "s_cbranch_execz LHRB_%=\n"
        "ds_read_b128 a[16:19], v245 offset:1024\n"
        "v_cmp_eq_u32 s[14:15], v247, v248\n"
        "v_mfma_f32_16x16x32_bf16 v[52:55], a[84:87], a[192:195], v[52:55]\n"
        "ds_read_b128 a[20:23], v245 offset:5120\n"
        "v_cndmask_b32 v240, 0, v246, s[14:15]\n"
        "v_mfma_f32_16x16x32_bf16 v[60:63], a[84:87], a[208:211], v[60:63]\n"
        "ds_read_b128 a[24:27], v245 offset:9216\n"
        "v_cmp_eq_u32 s[14:15], v247, v249\n"
        "v_mfma_f32_16x16x32_bf16 v[60:63], a[92:95], a[212:215], v[60:63]\n"
        "ds_read_b128 a[28:31], v245 offset:13312\n"
        "global_load_lds_dwordx4 v254, s[60:61] offset:1024 sc1\n"
        "v_mfma_f32_16x16x32_bf16 v[60:63], a[164:167], a[216:219], v[60:63]\n"
        "ds_read_b128 a[192:195], v245 offset:2048\n"
        "v_cndmask_b32 v241, 0, v246, s[14:15]\n"
        "v_mfma_f32_16x16x32_bf16 v[60:63], a[172:175], a[220:223], v[60:63]\n"
        "ds_read_b128 a[196:199], v245 offset:6144\n"
        "v_mfma_f32_16x16x32_bf16 v[56:59], a[168:171], a[220:223], v[56:59]\n"
        "ds_read_b128 a[200:203], v245 offset:10240\n"
        "s_waitcnt lgkmcnt(8)\n"
        "v_mfma_f32_16x16x32_bf16 v[56:59], a[160:163], a[216:219], v[56:59]\n"
        "ds_read_b128 a[204:207], v245 offset:14336\n"
        "v_mfma_f32_16x16x32_bf16 v[56:59], a[88:91], a[212:215], v[56:59]\n"
        "ds_read_b128 a[212:215], v245 offset:7168\n"
        "v_mfma_f32_16x16x32_bf16 v[56:59], a[80:83], a[208:211], v[56:59]\n"
        "ds_read_b128 a[208:211], v245 offset:3072\n"
        "s_waitcnt lgkmcnt(8)\n"
        "v_mfma_f32_16x16x32_bf16 v[32:35], a[176:179], a[0:3], v[32:35]\n"
        "ds_read_b128 a[216:219], v245 offset:11264\n"
        "global_load_lds_dwordx4 v254, s[60:61] offset:2048 sc1\n"
        "v_mfma_f32_16x16x32_bf16 v[32:35], a[184:187], a[4:7], v[32:35]\n"
        "ds_read_b128 a[220:223], v245 offset:15360\n"
        "v_mfma_f32_16x16x32_bf16 v[32:35], a[32:35], a[8:11], v[32:35]\n"
        "v_mfma_f32_16x16x32_bf16 v[32:35], a[40:43], a[12:15], v[32:35]\n"
        "v_mfma_f32_16x16x32_bf16 v[36:39], a[44:47], a[12:15], v[36:39]\n"
        "v_mfma_f32_16x16x32_bf16 v[36:39], a[36:39], a[8:11], v[36:39]\n"
        "v_mfma_f32_16x16x32_bf16 v[36:39], a[188:191], a[4:7], v[36:39]\n"
        "s_waitcnt lgkmcnt(3)\n"
        "v_mfma_f32_16x16x32_bf16 v[36:39], a[180:183], a[0:3], v[36:39]\n"
        "global_load_lds_dwordx4 v254, s[60:61] offset:3072 sc1\n"
        "s_add_u32 m0, s73, 0x1c000\n"
        "v_mfma_f32_16x16x32_bf16 v[44:47], a[180:183], a[16:19], v[44:47]\n"
        "s_add_u32 s60, s68, s66\n"
        "s_addc_u32 s61, s69, 0\n"
        "s_sub_u32 s66, 15, s12\n"
        "v_mfma_f32_16x16x32_bf16 v[44:47], a[188:191], a[20:23], v[44:47]\n"
        "s_cmp_lt_u32 s10, 2\n"
        "s_cselect_b32 s66, s12, s66\n"
        "s_mul_i32 s66, s66, 1052672\n"
        "v_mfma_f32_16x16x32_bf16 v[44:47], a[36:39], a[24:27], v[44:47]\n"
        "s_add_u32 s66, s66, s67\n"
        "s_add_u32 s68, s20, s66\n"
        "s_addc_u32 s69, s21, 0\n"
        "v_mfma_f32_16x16x32_bf16 v[44:47], a[44:47], a[28:31], v[44:47]\n"
        "s_xor_b32 s66, s17, 0\n"
        "s_lshl_b32 s66, s66, 15\n"
        "v_mfma_f32_16x16x32_bf16 v[40:43], a[40:43], a[28:31], v[40:43]\n"
        "v_mfma_f32_16x16x32_bf16 v[40:43], a[32:35], a[24:27], v[40:43]\n"
        "v_mfma_f32_16x16x32_bf16 v[40:43], a[184:187], a[20:23], v[40:43]\n"
        "v_mfma_f32_16x16x32_bf16 v[40:43], a[176:179], a[16:19], v[40:43]\n"
        "v_mfma_f32_16x16x32_bf16 v[48:51], a[176:179], a[192:195], v[48:51]\n"
        "s_barrier\n"
        "v_mfma_f32_16x16x32_bf16 v[48:51], a[184:187], a[196:199], v[48:51]\n"
        "ds_read_b128 a[0:3], v245 offset:16384\n"
        "global_load_lds_dwordx4 v254, s[60:61] offset:0 sc1\n"
        "v_mfma_f32_16x16x32_bf16 v[48:51], a[32:35], a[200:203], v[48:51]\n"
        "ds_read_b128 a[4:7], v245 offset:20480\n"
        "v_mfma_f32_16x16x32_bf16 v[48:51], a[40:43], a[204:207], v[48:51]\n"
        "ds_read_b128 a[8:11], v245 offset:24576\n"
        "v_mfma_f32_16x16x32_bf16 v[52:55], a[44:47], a[204:207], v[52:55]\n"
        "ds_read_b128 a[12:15], v245 offset:28672\n"
        "v_mfma_f32_16x16x32_bf16 v[52:55], a[36:39], a[200:203], v[52:55]\n"
        "ds_read_b128 a[28:31], v245 offset:29696\n"
        "v_mfma_f32_16x16x32_bf16 v[52:55], a[188:191], a[196:199], v[52:55]\n"
        "ds_read_b128 a[24:27], v245 offset:25600\n"
        "v_mfma_f32_16x16x32_bf16 v[52:55], a[180:183], a[192:195], v[52:55]\n"
        "ds_read_b128 a[20:23], v245 offset:21504\n"
        "s_waitcnt lgkmcnt(7)\n"
        "v_mfma_f32_16x16x32_bf16 v[60:63], a[180:183], a[208:211], v[60:63]\n"
        "ds_read_b128 a[16:19], v245 offset:17408\n"
        "global_load_lds_dwordx4 v254, s[60:61] offset:1024 sc1\n"
        "v_mfma_f32_16x16x32_bf16 v[60:63], a[188:191], a[212:215], v[60:63]\n"
        "ds_read_b128 a[192:195], v245 offset:18432\n"
        "s_waitcnt lgkmcnt(8)\n"
        "v_mfma_f32_16x16x32_bf16 v[32:35], a[48:51], a[0:3], v[32:35]\n"
        "ds_read_b128 a[196:199], v245 offset:22528\n"
        "s_waitcnt lgkmcnt(8)\n"
        "v_mfma_f32_16x16x32_bf16 v[32:35], a[56:59], a[4:7], v[32:35]\n"
        "ds_read_b128 a[200:203], v245 offset:26624\n"
        "s_waitcnt lgkmcnt(8)\n"
        "v_mfma_f32_16x16x32_bf16 v[32:35], a[224:227], a[8:11], v[32:35]\n"
        "ds_read_b128 a[204:207], v245 offset:30720\n"
        "s_waitcnt lgkmcnt(8)\n"
        "v_mfma_f32_16x16x32_bf16 v[32:35], a[232:235], a[12:15], v[32:35]\n"
        "v_mfma_f32_16x16x32_bf16 v[36:39], a[236:239], a[12:15], v[36:39]\n"
        "v_mfma_f32_16x16x32_bf16 v[36:39], a[228:231], a[8:11], v[36:39]\n"
        "global_load_lds_dwordx4 v254, s[60:61] offset:2048 sc1\n"
        "v_mfma_f32_16x16x32_bf16 v[36:39], a[60:63], a[4:7], v[36:39]\n"
        "v_mfma_f32_16x16x32_bf16 v[36:39], a[52:55], a[0:3], v[36:39]\n"
        "s_waitcnt lgkmcnt(3)\n"
        "v_mfma_f32_16x16x32_bf16 v[60:63], a[36:39], a[216:219], v[60:63]\n"
        "v_mfma_f32_16x16x32_bf16 v[60:63], a[44:47], a[220:223], v[60:63]\n"
        "v_cvt_pk_bf16_f32 v32, v32, v33\n"
        "v_cvt_pk_bf16_f32 v33, v34, v35\n"
        "v_mfma_f32_16x16x32_bf16 v[40:43], a[232:235], a[28:31], v[40:43]\n"
        "v_mfma_f32_16x16x32_bf16 v[40:43], a[224:227], a[24:27], v[40:43]\n"
        "v_mfma_f32_16x16x32_bf16 v[40:43], a[56:59], a[20:23], v[40:43]\n"
        "v_cvt_pk_bf16_f32 v34, v36, v37\n"
        "v_cvt_pk_bf16_f32 v35, v38, v39\n"
        "v_mfma_f32_16x16x32_bf16 v[40:43], a[48:51], a[16:19], v[40:43]\n"
        "global_store_dwordx4 v254, v[32:35], s[28:29] offset:0 sc1\n"
        "s_barrier\n"
        "v_mfma_f32_16x16x32_bf16 v[44:47], a[52:55], a[16:19], v[44:47]\n"
        "ds_read_b128 a[0:3], v254 offset:0\n"
        "v_mfma_f32_16x16x32_bf16 v[44:47], a[60:63], a[20:23], v[44:47]\n"
        "ds_read_b128 a[4:7], v254 offset:4096\n"
        "v_mfma_f32_16x16x32_bf16 v[44:47], a[228:231], a[24:27], v[44:47]\n"
        "ds_read_b128 a[8:11], v254 offset:8192\n"
        "v_cvt_pk_bf16_f32 v40, v40, v41\n"
        "v_mfma_f32_16x16x32_bf16 v[44:47], a[236:239], a[28:31], v[44:47]\n"
        "ds_read_b128 a[12:15], v254 offset:12288\n"
        "v_cvt_pk_bf16_f32 v41, v42, v43\n"
        "v_mfma_f32_16x16x32_bf16 v[56:59], a[40:43], a[220:223], v[56:59]\n"
        "ds_read_b128 a[220:223], v245 offset:31744\n"
        "v_mfma_f32_16x16x32_bf16 v[56:59], a[32:35], a[216:219], v[56:59]\n"
        "ds_read_b128 a[216:219], v245 offset:27648\n"
        "global_load_lds_dwordx4 v254, s[60:61] offset:3072 sc1\n"
        "s_add_u32 m0, s73, 0x20000\n"
        "v_mfma_f32_16x16x32_bf16 v[56:59], a[184:187], a[212:215], v[56:59]\n"
        "ds_read_b128 a[212:215], v245 offset:23552\n"
        "v_cvt_pk_bf16_f32 v42, v44, v45\n"
        "v_mfma_f32_16x16x32_bf16 v[56:59], a[176:179], a[208:211], v[56:59]\n"
        "ds_read_b128 a[208:211], v245 offset:19456\n"
        "v_cvt_pk_bf16_f32 v43, v46, v47\n"
        "v_mfma_f32_16x16x32_bf16 v[48:51], a[48:51], a[192:195], v[48:51]\n"
        "ds_read_b128 a[16:19], v254 offset:1024\n"
        "s_waitcnt lgkmcnt(8)\n"
        "v_mfma_f32_16x16x32_bf16 v[48:51], a[56:59], a[196:199], v[48:51]\n"
        "global_store_dwordx4 v254, v[40:43], s[28:29] offset:1024 sc1\n"
        "ds_read_b128 a[20:23], v254 offset:5120\n"
        "s_add_u32 s60, s68, s66\n"
        "s_addc_u32 s61, s69, 0\n"
        "ds_read_b128 a[24:27], v254 offset:9216\n"
        "s_xor_b32 s66, s17, 1\n"
        "v_mfma_f32_16x16x32_bf16 v[48:51], a[224:227], a[200:203], v[48:51]\n"
        "ds_read_b128 a[28:31], v254 offset:13312\n"
        "s_lshl_b32 s66, s66, 15\n"
        "v_mfma_f32_16x16x32_bf16 v[48:51], a[232:235], a[204:207], v[48:51]\n"
        "v_mfma_f32_16x16x32_bf16 v[52:55], a[236:239], a[204:207], v[52:55]\n"
        "ds_read_b128 a[204:207], v254 offset:14336\n"
        "s_waitcnt lgkmcnt(7)\n"
        "v_mfma_f32_16x16x32_bf16 v[52:55], a[228:231], a[200:203], v[52:55]\n"
        "ds_read_b128 a[200:203], v254 offset:10240\n"
        "v_mfma_f32_16x16x32_bf16 v[52:55], a[60:63], a[196:199], v[52:55]\n"
        "ds_read_b128 a[196:199], v254 offset:6144\n"
        "s_waitcnt lgkmcnt(7)\n"
        "v_mfma_f32_16x16x32_bf16 v[52:55], a[52:55], a[192:195], v[52:55]\n"
        "ds_read_b128 a[192:195], v254 offset:2048\n"
        "v_cvt_pk_bf16_f32 v48, v48, v49\n"
        "v_mfma_f32_16x16x32_bf16 v[56:59], a[232:235], a[220:223], v[56:59]\n"
        "global_load_lds_dwordx4 v254, s[60:61] offset:0 sc1\n"
        "v_cvt_pk_bf16_f32 v49, v50, v51\n"
        "v_mfma_f32_16x16x32_bf16 v[56:59], a[224:227], a[216:219], v[56:59]\n"
        "v_mfma_f32_16x16x32_bf16 v[56:59], a[56:59], a[212:215], v[56:59]\n"
        "v_mfma_f32_16x16x32_bf16 v[56:59], a[48:51], a[208:211], v[56:59]\n"
        "v_cvt_pk_bf16_f32 v50, v52, v53\n"
        "v_cvt_pk_bf16_f32 v51, v54, v55\n"
        "v_mfma_f32_16x16x32_bf16 v[60:63], a[52:55], a[208:211], v[60:63]\n"
        "global_store_dwordx4 v254, v[48:51], s[28:29] offset:2048 sc1\n"
        "ds_read_b128 a[208:211], v254 offset:3072\n"
        "v_mfma_f32_16x16x32_bf16 v[60:63], a[60:63], a[212:215], v[60:63]\n"
        "ds_read_b128 a[212:215], v254 offset:7168\n"
        "v_mfma_f32_16x16x32_bf16 v[60:63], a[228:231], a[216:219], v[60:63]\n"
        "ds_read_b128 a[216:219], v254 offset:11264\n"
        "v_cvt_pk_bf16_f32 v56, v56, v57\n"
        "v_mfma_f32_16x16x32_bf16 v[60:63], a[236:239], a[220:223], v[60:63]\n"
        "ds_read_b128 a[220:223], v254 offset:15360\n"
        "v_cvt_pk_bf16_f32 v57, v58, v59\n"
        "v_mfma_f32_16x16x32_bf16 v[0:3], a[96:99], a[0:3], 0\n"
        "v_mfma_f32_16x16x32_bf16 v[0:3], a[104:107], a[4:7], v[0:3]\n"
        "v_mfma_f32_16x16x32_bf16 v[0:3], a[112:115], a[8:11], v[0:3]\n"
        "global_load_dwordx2 v[236:237], v234, s[48:49] sc1\n"
        "v_mfma_f32_16x16x32_bf16 v[0:3], a[120:123], a[12:15], v[0:3]\n"
        "global_load_lds_dwordx4 v254, s[60:61] offset:1024 sc1\n"
        "v_cvt_pk_bf16_f32 v58, v60, v61\n"
        "v_mfma_f32_16x16x32_bf16 v[4:7], a[124:127], a[12:15], 0\n"
        "s_waitcnt lgkmcnt(3)\n"
        "v_cvt_pk_bf16_f32 v59, v62, v63\n"
        "v_mfma_f32_16x16x32_bf16 v[4:7], a[116:119], a[8:11], v[4:7]\n"
        "v_mfma_f32_16x16x32_bf16 v[4:7], a[108:111], a[4:7], v[4:7]\n"
        "v_mfma_f32_16x16x32_bf16 v[4:7], a[100:103], a[0:3], v[4:7]\n"
        "global_store_dwordx4 v254, v[56:59], s[28:29] offset:3072 sc1\n"
        "v_mfma_f32_16x16x32_bf16 v[12:15], a[100:103], a[16:19], 0\n"
        "v_mfma_f32_16x16x32_bf16 v[12:15], a[108:111], a[20:23], v[12:15]\n"
        "v_mfma_f32_16x16x32_bf16 v[12:15], a[116:119], a[24:27], v[12:15]\n"
        "v_mfma_f32_16x16x32_bf16 v[12:15], a[124:127], a[28:31], v[12:15]\n"
        "v_mfma_f32_16x16x32_bf16 v[8:11], a[120:123], a[28:31], 0\n"
        "v_mfma_f32_16x16x32_bf16 v[8:11], a[112:115], a[24:27], v[8:11]\n"
        "global_load_lds_dwordx4 v254, s[60:61] offset:2048 sc1\n"
        "v_mfma_f32_16x16x32_bf16 v[8:11], a[104:107], a[20:23], v[8:11]\n"
        "v_mfma_f32_16x16x32_bf16 v[8:11], a[96:99], a[16:19], v[8:11]\n"
        "v_mfma_f32_16x16x32_bf16 v[16:19], a[96:99], a[192:195], 0\n"
        "s_barrier\n"
        "v_mfma_f32_16x16x32_bf16 v[16:19], a[104:107], a[196:199], v[16:19]\n"
        "ds_read_b128 a[0:3], v254 offset:16384\n"
        "v_mfma_f32_16x16x32_bf16 v[16:19], a[112:115], a[200:203], v[16:19]\n"
        "ds_read_b128 a[4:7], v254 offset:20480\n"
        "v_mfma_f32_16x16x32_bf16 v[16:19], a[120:123], a[204:207], v[16:19]\n"
        "ds_read_b128 a[8:11], v254 offset:24576\n"
        "v_mfma_f32_16x16x32_bf16 v[20:23], a[124:127], a[204:207], 0\n"
        "ds_read_b128 a[12:15], v254 offset:28672\n"
        "global_load_lds_dwordx4 v254, s[60:61] offset:3072 sc1\n"
        "s_add_u32 m0, s73, 0x24000\n"
        "v_mfma_f32_16x16x32_bf16 v[20:23], a[116:119], a[200:203], v[20:23]\n"
        "ds_read_b128 a[16:19], v254 offset:17408\n"
        "s_add_u32 s60, s68, s66\n"
        "s_addc_u32 s61, s69, 0\n"
        "v_mfma_f32_16x16x32_bf16 v[20:23], a[108:111], a[196:199], v[20:23]\n"
        "ds_read_b128 a[20:23], v254 offset:21504\n"
        "s_xor_b32 s66, s17, 2\n"
        "s_lshl_b32 s66, s66, 15\n"
        "v_mfma_f32_16x16x32_bf16 v[20:23], a[100:103], a[192:195], v[20:23]\n"
        "ds_read_b128 a[24:27], v254 offset:25600\n"
        "s_lshl2_add_u32 s15, s76, s10\n"
        "s_mul_i32 s16, s13, 263168\n"
        "v_mfma_f32_16x16x32_bf16 v[28:31], a[100:103], a[208:211], 0\n"
        "ds_read_b128 a[28:31], v254 offset:29696\n"
        "s_lshl_b32 s15, s15, 14\n"
        "s_waitcnt lgkmcnt(8)\n"
        "v_mfma_f32_16x16x32_bf16 v[28:31], a[108:111], a[212:215], v[28:31]\n"
        "ds_read_b128 a[192:195], v254 offset:18432\n"
        "s_add_u32 s16, s16, s15\n"
        "s_add_u32 s16, s16, s73\n"
        "v_mfma_f32_16x16x32_bf16 v[28:31], a[116:119], a[216:219], v[28:31]\n"
        "ds_read_b128 a[196:199], v254 offset:22528\n"
        "s_add_u32 s16, s16, 0x4000000\n"
        "v_mfma_f32_16x16x32_bf16 v[28:31], a[124:127], a[220:223], v[28:31]\n"
        "ds_read_b128 a[200:203], v254 offset:26624\n"
        "global_load_lds_dwordx4 v254, s[60:61] offset:0 sc1\n"
        "v_mfma_f32_16x16x32_bf16 v[24:27], a[120:123], a[220:223], 0\n"
        "ds_read_b128 a[204:207], v254 offset:30720\n"
        "s_add_u32 s28, s38, s16\n"
        "s_addc_u32 s29, s39, 0\n"
        "v_mfma_f32_16x16x32_bf16 v[24:27], a[112:115], a[216:219], v[24:27]\n"
        "ds_read_b128 a[216:219], v254 offset:27648\n"
        "s_waitcnt lgkmcnt(8)\n"
        "v_mfma_f32_16x16x32_bf16 v[24:27], a[104:107], a[212:215], v[24:27]\n"
        "ds_read_b128 a[212:215], v254 offset:23552\n"
        "v_mfma_f32_16x16x32_bf16 v[24:27], a[96:99], a[208:211], v[24:27]\n"
        "ds_read_b128 a[208:211], v254 offset:19456\n"
        "v_mfma_f32_16x16x32_bf16 v[0:3], a[128:131], a[0:3], v[0:3]\n"
        "ds_read_b128 a[220:223], v254 offset:31744\n"
        "v_mfma_f32_16x16x32_bf16 v[0:3], a[136:139], a[4:7], v[0:3]\n"
        "v_mfma_f32_16x16x32_bf16 v[0:3], a[144:147], a[8:11], v[0:3]\n"
        "global_load_lds_dwordx4 v254, s[60:61] offset:1024 sc1\n"
        "v_mfma_f32_16x16x32_bf16 v[0:3], a[152:155], a[12:15], v[0:3]\n"
        "v_mfma_f32_16x16x32_bf16 v[4:7], a[156:159], a[12:15], v[4:7]\n"
        "v_mfma_f32_16x16x32_bf16 v[4:7], a[148:151], a[8:11], v[4:7]\n"
        "s_waitcnt lgkmcnt(3)\n"
        "v_mfma_f32_16x16x32_bf16 v[4:7], a[140:143], a[4:7], v[4:7]\n"
        "v_mfma_f32_16x16x32_bf16 v[4:7], a[132:135], a[0:3], v[4:7]\n"
        "v_mfma_f32_16x16x32_bf16 v[12:15], a[132:135], a[16:19], v[12:15]\n"
        "v_mfma_f32_16x16x32_bf16 v[12:15], a[140:143], a[20:23], v[12:15]\n"
        "global_load_lds_dwordx4 v254, s[60:61] offset:2048 sc1\n"
        "v_mfma_f32_16x16x32_bf16 v[12:15], a[148:151], a[24:27], v[12:15]\n"
        "v_mfma_f32_16x16x32_bf16 v[12:15], a[156:159], a[28:31], v[12:15]\n"
        "v_mfma_f32_16x16x32_bf16 v[8:11], a[152:155], a[28:31], v[8:11]\n"
        "v_mfma_f32_16x16x32_bf16 v[8:11], a[144:147], a[24:27], v[8:11]\n"
        "v_mfma_f32_16x16x32_bf16 v[8:11], a[136:139], a[20:23], v[8:11]\n"
        "v_mfma_f32_16x16x32_bf16 v[8:11], a[128:131], a[16:19], v[8:11]\n"
        "s_waitcnt vmcnt(36)\n"
        "v_mfma_f32_16x16x32_bf16 v[16:19], a[128:131], a[192:195], v[16:19]\n"
        "s_barrier\n"
        "v_mfma_f32_16x16x32_bf16 v[16:19], a[136:139], a[196:199], v[16:19]\n"
        "ds_write_b32 v225, v224\n"
        "v_mfma_f32_16x16x32_bf16 v[16:19], a[144:147], a[200:203], v[16:19]\n"
        "ds_read_b128 a[0:3], v244 offset:0\n"
        "global_load_lds_dwordx4 v254, s[60:61] offset:3072 sc1\n"
        "s_add_u32 m0, s73, 0x4000\n"
        "v_mfma_f32_16x16x32_bf16 v[16:19], a[152:155], a[204:207], v[16:19]\n"
        "ds_read_b128 a[4:7], v244 offset:4096\n"
        "s_add_u32 s60, s68, s66\n"
        "s_addc_u32 s61, s69, 0\n"
        "v_mfma_f32_16x16x32_bf16 v[20:23], a[156:159], a[204:207], v[20:23]\n"
        "ds_read_b128 a[8:11], v244 offset:8192\n"
        "s_xor_b32 s66, s17, 3\n"
        "s_lshl_b32 s66, s66, 15\n"
        "v_mfma_f32_16x16x32_bf16 v[20:23], a[148:151], a[200:203], v[20:23]\n"
        "ds_read_b128 a[12:15], v244 offset:12288\n"
        "s_waitcnt lgkmcnt(5)\n"
        "v_mfma_f32_16x16x32_bf16 v[20:23], a[140:143], a[196:199], v[20:23]\n"
        "ds_read_b128 a[16:19], v244 offset:1024\n"
        "v_mfma_f32_16x16x32_bf16 v[20:23], a[132:135], a[192:195], v[20:23]\n"
        "ds_read_b128 a[20:23], v244 offset:5120\n"
        "v_mfma_f32_16x16x32_bf16 v[28:31], a[132:135], a[208:211], v[28:31]\n"
        "ds_read_b128 a[24:27], v244 offset:9216\n"
        "v_mfma_f32_16x16x32_bf16 v[28:31], a[140:143], a[212:215], v[28:31]\n"
        "ds_read_b128 a[28:31], v244 offset:13312\n"
        "v_mfma_f32_16x16x32_bf16 v[28:31], a[148:151], a[216:219], v[28:31]\n"
        "ds_read_b128 a[192:195], v244 offset:2048\n"
        "v_mfma_f32_16x16x32_bf16 v[28:31], a[156:159], a[220:223], v[28:31]\n"
        "ds_read_b128 a[196:199], v244 offset:6144\n"
        "v_mfma_f32_16x16x32_bf16 v[24:27], a[152:155], a[220:223], v[24:27]\n"
        "ds_read_b128 a[200:203], v244 offset:10240\n"
        "s_waitcnt lgkmcnt(8)\n"
        "v_mfma_f32_16x16x32_bf16 v[24:27], a[144:147], a[216:219], v[24:27]\n"
        "ds_read_b128 a[204:207], v244 offset:14336\n"
        "v_mfma_f32_16x16x32_bf16 v[24:27], a[136:139], a[212:215], v[24:27]\n"
        "ds_read_b128 a[212:215], v244 offset:7168\n"
        "v_mfma_f32_16x16x32_bf16 v[24:27], a[128:131], a[208:211], v[24:27]\n"
        "ds_read_b128 a[208:211], v244 offset:3072\n"
        "s_waitcnt lgkmcnt(8)\n"
        "v_mfma_f32_16x16x32_bf16 v[0:3], v[160:163], a[0:3], v[0:3]\n"
        "ds_read_b128 a[216:219], v244 offset:11264\n"
        "v_mfma_f32_16x16x32_bf16 v[0:3], v[168:171], a[4:7], v[0:3]\n"
        "ds_read_b128 a[220:223], v244 offset:15360\n"
        "v_mfma_f32_16x16x32_bf16 v[0:3], v[176:179], a[8:11], v[0:3]\n"
        "v_mfma_f32_16x16x32_bf16 v[0:3], v[184:187], a[12:15], v[0:3]\n"
        "v_mfma_f32_16x16x32_bf16 v[4:7], v[188:191], a[12:15], v[4:7]\n"
        "v_mfma_f32_16x16x32_bf16 v[4:7], v[180:183], a[8:11], v[4:7]\n"
        "v_mfma_f32_16x16x32_bf16 v[4:7], v[172:175], a[4:7], v[4:7]\n"
        "s_waitcnt lgkmcnt(3)\n"
        "v_mfma_f32_16x16x32_bf16 v[4:7], v[164:167], a[0:3], v[4:7]\n"
        "v_mfma_f32_16x16x32_bf16 v[12:15], v[164:167], a[16:19], v[12:15]\n"
        "v_mfma_f32_16x16x32_bf16 v[12:15], v[172:175], a[20:23], v[12:15]\n"
        "v_mfma_f32_16x16x32_bf16 v[12:15], v[180:183], a[24:27], v[12:15]\n"
        "v_mfma_f32_16x16x32_bf16 v[12:15], v[188:191], a[28:31], v[12:15]\n"
        "v_mfma_f32_16x16x32_bf16 v[8:11], v[184:187], a[28:31], v[8:11]\n"
        "v_mfma_f32_16x16x32_bf16 v[8:11], v[176:179], a[24:27], v[8:11]\n"
        "v_mfma_f32_16x16x32_bf16 v[8:11], v[168:171], a[20:23], v[8:11]\n"
        "v_mfma_f32_16x16x32_bf16 v[8:11], v[160:163], a[16:19], v[8:11]\n"
        "s_waitcnt vmcnt(33)\n"
        "v_mfma_f32_16x16x32_bf16 v[16:19], v[160:163], a[192:195], v[16:19]\n"
        "s_barrier\n"
        "v_mfma_f32_16x16x32_bf16 v[16:19], v[168:171], a[196:199], v[16:19]\n"
        "ds_read_b128 a[0:3], v244 offset:16384\n"
        "v_mfma_f32_16x16x32_bf16 v[16:19], v[176:179], a[200:203], v[16:19]\n"
        "ds_read_b128 a[4:7], v244 offset:20480\n"
        "v_mfma_f32_16x16x32_bf16 v[16:19], v[184:187], a[204:207], v[16:19]\n"
        "ds_read_b128 a[8:11], v244 offset:24576\n"
        "v_mfma_f32_16x16x32_bf16 v[20:23], v[188:191], a[204:207], v[20:23]\n"
        "ds_read_b128 a[12:15], v244 offset:28672\n"
        "s_waitcnt lgkmcnt(4)\n"
        "v_mfma_f32_16x16x32_bf16 v[20:23], v[180:183], a[200:203], v[20:23]\n"
        "ds_read_b128 a[16:19], v244 offset:17408\n"
        "v_mfma_f32_16x16x32_bf16 v[20:23], v[172:175], a[196:199], v[20:23]\n"
        "ds_read_b128 a[20:23], v244 offset:21504\n"
        "v_mfma_f32_16x16x32_bf16 v[20:23], v[164:167], a[192:195], v[20:23]\n"
        "ds_read_b128 a[24:27], v244 offset:25600\n"
        "v_mfma_f32_16x16x32_bf16 v[28:31], v[164:167], a[208:211], v[28:31]\n"
        "ds_read_b128 a[28:31], v244 offset:29696\n"
        "v_mfma_f32_16x16x32_bf16 v[28:31], v[172:175], a[212:215], v[28:31]\n"
        "ds_read_b128 a[192:195], v244 offset:18432\n"
        "v_mfma_f32_16x16x32_bf16 v[28:31], v[180:183], a[216:219], v[28:31]\n"
        "ds_read_b128 a[196:199], v244 offset:22528\n"
        "v_mfma_f32_16x16x32_bf16 v[28:31], v[188:191], a[220:223], v[28:31]\n"
        "ds_read_b128 a[200:203], v244 offset:26624\n"
        "v_mfma_f32_16x16x32_bf16 v[24:27], v[184:187], a[220:223], v[24:27]\n"
        "ds_read_b128 a[204:207], v244 offset:30720\n"
        "s_waitcnt lgkmcnt(8)\n"
        "v_mfma_f32_16x16x32_bf16 v[24:27], v[176:179], a[216:219], v[24:27]\n"
        "ds_read_b128 a[216:219], v244 offset:27648\n"
        "v_mfma_f32_16x16x32_bf16 v[24:27], v[168:171], a[212:215], v[24:27]\n"
        "ds_read_b128 a[212:215], v244 offset:23552\n"
        "v_mfma_f32_16x16x32_bf16 v[24:27], v[160:163], a[208:211], v[24:27]\n"
        "ds_read_b128 a[208:211], v244 offset:19456\n"
        "s_waitcnt vmcnt(8) lgkmcnt(8)\n"
        "v_mfma_f32_16x16x32_bf16 v[0:3], v[192:195], a[0:3], v[0:3]\n"
        "ds_read_b128 a[220:223], v244 offset:31744\n"
        "v_cmp_ne_u64 vcc, v[236:237], v[250:251]\n"
        "v_mfma_f32_16x16x32_bf16 v[0:3], v[200:203], a[4:7], v[0:3]\n"
        "s_cbranch_vccnz LPZZ_%=\n"
        "LBZZ_%=:\n"
        "global_load_lds_dwordx4 v148, s[58:59] offset:0 sc1\n"
        "s_add_u32 m0, s73, 0x10000\n"
        "v_mfma_f32_16x16x32_bf16 v[0:3], v[208:211], a[8:11], v[0:3]\n"
        "s_add_u32 s58, s58, 0x8000\n"
        "s_addc_u32 s59, s59, 0\n"
        "v_mfma_f32_16x16x32_bf16 v[0:3], v[216:219], a[12:15], v[0:3]\n"
        "v_mfma_f32_16x16x32_bf16 v[4:7], v[220:223], a[12:15], v[4:7]\n"
        "v_mfma_f32_16x16x32_bf16 v[4:7], v[212:215], a[8:11], v[4:7]\n"
        "v_mfma_f32_16x16x32_bf16 v[4:7], v[204:207], a[4:7], v[4:7]\n"
        "v_mfma_f32_16x16x32_bf16 v[4:7], v[196:199], a[0:3], v[4:7]\n"
        "s_waitcnt lgkmcnt(1)\n"
        "v_mfma_f32_16x16x32_bf16 v[12:15], v[196:199], a[16:19], v[12:15]\n"
        "global_load_dwordx4 v[96:99], v151, s[64:65]\n"
        "s_lshl2_add_u32 s14, s13, s10\n"
        "v_mfma_f32_16x16x32_bf16 v[12:15], v[204:207], a[20:23], v[12:15]\n"
        "s_add_u32 s64, s64, 0x8000\n"
        "s_addc_u32 s65, s65, 0\n"
        "s_lshl2_add_u32 s14, s14, s40\n"
        "v_mfma_f32_16x16x32_bf16 v[12:15], v[212:215], a[24:27], v[12:15]\n"
        "s_add_u32 s14, s14, 3072\n"
        "v_lshrrev_b32 v154, 1, v254\n"
        "s_lshl_b32 s14, s14, 7\n"
        "v_mfma_f32_16x16x32_bf16 v[12:15], v[220:223], a[28:31], v[12:15]\n"
        "v_add_u32 v154, s14, v154\n"
        "v_mfma_f32_16x16x32_bf16 v[8:11], v[216:219], a[28:31], v[8:11]\n"
        "v_mfma_f32_16x16x32_bf16 v[8:11], v[208:211], a[24:27], v[8:11]\n"
        "v_mfma_f32_16x16x32_bf16 v[8:11], v[200:203], a[20:23], v[8:11]\n"
        "v_mfma_f32_16x16x32_bf16 v[8:11], v[192:195], a[16:19], v[8:11]\n"
        "v_mfma_f32_16x16x32_bf16 v[16:19], v[192:195], a[192:195], v[16:19]\n"
        "s_barrier\n"
        "v_mfma_f32_16x16x32_bf16 v[16:19], v[200:203], a[196:199], v[16:19]\n"
        "ds_read_b128 a[0:3], v254 offset:32768\n"
        "global_load_lds_dwordx4 v254, s[60:61] offset:0 sc1\n"
        "v_mfma_f32_16x16x32_bf16 v[16:19], v[208:211], a[200:203], v[16:19]\n"
        "ds_read_b128 a[4:7], v254 offset:36864\n"
        "v_mfma_f32_16x16x32_bf16 v[16:19], v[216:219], a[204:207], v[16:19]\n"
        "ds_read_b128 a[8:11], v254 offset:40960\n"
        "v_mfma_f32_16x16x32_bf16 v[20:23], v[220:223], a[204:207], v[20:23]\n"
        "ds_read_b128 a[12:15], v254 offset:45056\n"
        "v_mfma_f32_16x16x32_bf16 v[20:23], v[212:215], a[200:203], v[20:23]\n"
        "ds_read_b128 a[16:19], v254 offset:33792\n"
        "v_mfma_f32_16x16x32_bf16 v[20:23], v[204:207], a[196:199], v[20:23]\n"
        "ds_read_b128 a[20:23], v254 offset:37888\n"
        "v_mfma_f32_16x16x32_bf16 v[20:23], v[196:199], a[192:195], v[20:23]\n"
        "ds_read_b128 a[24:27], v254 offset:41984\n"
        "s_waitcnt lgkmcnt(7)\n"
        "v_mfma_f32_16x16x32_bf16 v[28:31], v[196:199], a[208:211], v[28:31]\n"
        "ds_read_b128 a[28:31], v254 offset:46080\n"
        "global_load_lds_dwordx4 v254, s[60:61] offset:1024 sc1\n"
        "v_mfma_f32_16x16x32_bf16 v[28:31], v[204:207], a[212:215], v[28:31]\n"
        "ds_read_b128 a[192:195], v254 offset:34816\n"
        "v_mfma_f32_16x16x32_bf16 v[28:31], v[212:215], a[216:219], v[28:31]\n"
        "ds_read_b128 a[196:199], v254 offset:38912\n"
        "v_mfma_f32_16x16x32_bf16 v[28:31], v[220:223], a[220:223], v[28:31]\n"
        "ds_read_b128 a[200:203], v254 offset:43008\n"
        "v_mfma_f32_16x16x32_bf16 v[24:27], v[216:219], a[220:223], v[24:27]\n"
        "ds_read_b128 a[204:207], v254 offset:47104\n"
        "s_waitcnt lgkmcnt(8)\n"
        "v_mfma_f32_16x16x32_bf16 v[24:27], v[208:211], a[216:219], v[24:27]\n"
        "ds_read_b128 a[216:219], v254 offset:44032\n"
        "v_mfma_f32_16x16x32_bf16 v[24:27], v[200:203], a[212:215], v[24:27]\n"
        "ds_read_b128 a[212:215], v254 offset:39936\n"
        "v_mfma_f32_16x16x32_bf16 v[24:27], v[192:195], a[208:211], v[24:27]\n"
        "ds_read_b128 a[208:211], v254 offset:35840\n"
        "global_load_lds_dwordx4 v254, s[60:61] offset:2048 sc1\n"
        "s_and_b32 s14, s14, 0xfffff9ff\n"
        "v_mfma_f32_16x16x32_bf16 v[0:3], v[112:115], a[0:3], v[0:3]\n"
        "ds_read_b128 a[220:223], v254 offset:48128\n"
        "v_bfe_u32 v155, v254, 4, 2\n"
        "v_mfma_f32_16x16x32_bf16 v[0:3], v[120:123], a[4:7], v[0:3]\n"
        "ds_read_b32 v224, v159 offset:0\n"
        "v_lshl_add_u32 v155, v155, 9, s14\n"
        "v_mfma_f32_16x16x32_bf16 v[0:3], a[64:67], a[8:11], v[0:3]\n"
        "ds_read_b32 v225, v159 offset:64\n"
        "v_mfma_f32_16x16x32_bf16 v[0:3], a[72:75], a[12:15], v[0:3]\n"
        "ds_read_b32 v226, v159 offset:128\n"
        "v_mfma_f32_16x16x32_bf16 v[4:7], a[76:79], a[12:15], v[4:7]\n"
        "ds_read_b32 v227, v159 offset:192\n"
        "s_waitcnt lgkmcnt(8)\n"
        "v_mfma_f32_16x16x32_bf16 v[4:7], a[68:71], a[8:11], v[4:7]\n"
        "v_mfma_f32_16x16x32_bf16 v[4:7], v[124:127], a[4:7], v[4:7]\n"
        "global_load_lds_dwordx4 v254, s[60:61] offset:3072 sc1\n"
        "s_add_u32 m0, s73, 0x4400\n"
        "v_mfma_f32_16x16x32_bf16 v[4:7], v[116:119], a[0:3], v[4:7]\n"
        "s_add_u32 s60, s68, s66\n"
        "s_addc_u32 s61, s69, 0\n"
        "s_xor_b32 s66, s17, 4\n"
        "v_mfma_f32_16x16x32_bf16 v[12:15], v[116:119], a[16:19], v[12:15]\n"
        "s_lshl_b32 s66, s66, 15\n"
        "v_mfma_f32_16x16x32_bf16 v[12:15], v[124:127], a[20:23], v[12:15]\n"
        "s_waitcnt lgkmcnt(3)\n"
        "v_sub_f32 v228, 1.0, v224\n"
        "v_cvt_pk_bf16_f32 v228, 0, v228\n"
        "v_mfma_f32_16x16x32_bf16 v[12:15], a[68:71], a[24:27], v[12:15]\n"
        "s_waitcnt lgkmcnt(2)\n"
        "v_sub_f32 v229, 1.0, v225\n"
        "v_cvt_pk_bf16_f32 v229, 0, v229\n"
        "v_mfma_f32_16x16x32_bf16 v[12:15], a[76:79], a[28:31], v[12:15]\n"
        "s_waitcnt lgkmcnt(1)\n"
        "v_sub_f32 v230, 1.0, v226\n"
        "v_cvt_pk_bf16_f32 v230, 0, v230\n"
        "v_mfma_f32_16x16x32_bf16 v[8:11], a[72:75], a[28:31], v[8:11]\n"
        "s_waitcnt lgkmcnt(0)\n"
        "v_sub_f32 v231, 1.0, v227\n"
        "v_cvt_pk_bf16_f32 v231, 0, v231\n"
        "v_mfma_f32_16x16x32_bf16 v[8:11], a[64:67], a[24:27], v[8:11]\n"
        "global_load_lds_dwordx4 v148, s[58:59] offset:0 sc1\n"
        "s_add_u32 m0, s73, 0x14000\n"
        "v_mfma_f32_16x16x32_bf16 v[8:11], v[120:123], a[20:23], v[8:11]\n"
        "s_add_u32 s58, s58, 0x8000\n"
        "s_addc_u32 s59, s59, 0\n"
        "v_mfma_f32_16x16x32_bf16 v[8:11], v[112:115], a[16:19], v[8:11]\n"
        "v_mfma_f32_16x16x32_bf16 v[16:19], v[112:115], a[192:195], v[16:19]\n"
        "s_barrier\n"
        "v_mfma_f32_16x16x32_bf16 v[16:19], v[120:123], a[196:199], v[16:19]\n"
        "ds_read_b128 a[0:3], v254 offset:49152\n"
        "v_mfma_f32_16x16x32_bf16 v[16:19], a[64:67], a[200:203], v[16:19]\n"
        "ds_read_b128 a[4:7], v254 offset:53248\n"
        "v_mfma_f32_16x16x32_bf16 v[16:19], a[72:75], a[204:207], v[16:19]\n"
        "ds_read_b128 a[8:11], v254 offset:57344\n"
        "v_mfma_f32_16x16x32_bf16 v[20:23], a[76:79], a[204:207], v[20:23]\n"
        "ds_read_b128 a[12:15], v254 offset:61440\n"
        "global_load_lds_dwordx4 v254, s[60:61] offset:0 sc1\n"
        "v_mfma_f32_16x16x32_bf16 v[20:23], a[68:71], a[200:203], v[20:23]\n"
        "ds_read_b128 a[16:19], v254 offset:50176\n"
        "v_mfma_f32_16x16x32_bf16 v[20:23], v[124:127], a[196:199], v[20:23]\n"
        "ds_read_b128 a[20:23], v254 offset:54272\n"
        "v_mfma_f32_16x16x32_bf16 v[20:23], v[116:119], a[192:195], v[20:23]\n"
        "ds_read_b128 a[24:27], v254 offset:58368\n"
        "v_mfma_f32_16x16x32_bf16 v[28:31], v[116:119], a[208:211], v[28:31]\n"
        "ds_read_b128 a[28:31], v254 offset:62464\n"
        "v_mfma_f32_16x16x32_bf16 v[28:31], v[124:127], a[212:215], v[28:31]\n"
        "ds_read_b128 a[192:195], v254 offset:51200\n"
        "v_mfma_f32_16x16x32_bf16 v[28:31], a[68:71], a[216:219], v[28:31]\n"
        "ds_read_b128 a[196:199], v254 offset:55296\n"
        "v_mfma_f32_16x16x32_bf16 v[28:31], a[76:79], a[220:223], v[28:31]\n"
        "ds_read_b128 a[200:203], v254 offset:59392\n"
        "global_load_dwordx4 v[100:103], v151, s[64:65]\n"
        "v_mfma_f32_16x16x32_bf16 v[24:27], a[72:75], a[220:223], v[24:27]\n"
        "ds_read_b128 a[204:207], v254 offset:63488\n"
        "s_add_u32 s64, s64, 0x8000\n"
        "s_addc_u32 s65, s65, 0\n"
        "v_mfma_f32_16x16x32_bf16 v[24:27], a[64:67], a[216:219], v[24:27]\n"
        "ds_read_b128 a[216:219], v254 offset:60416\n"
        "s_waitcnt lgkmcnt(8)\n"
        "v_mfma_f32_16x16x32_bf16 v[24:27], v[120:123], a[212:215], v[24:27]\n"
        "ds_read_b128 a[212:215], v254 offset:56320\n"
        "v_mfma_f32_16x16x32_bf16 v[24:27], v[112:115], a[208:211], v[24:27]\n"
        "ds_read_b128 a[208:211], v254 offset:52224\n"
        "v_mfma_f32_16x16x32_bf16 v[0:3], a[80:83], a[0:3], v[0:3]\n"
        "ds_read_b128 a[220:223], v254 offset:64512\n"
        "v_mfma_f32_16x16x32_bf16 v[0:3], a[88:91], a[4:7], v[0:3]\n"
        "v_mfma_f32_16x16x32_bf16 v[0:3], a[160:163], a[8:11], v[0:3]\n"
        "global_load_lds_dwordx4 v254, s[60:61] offset:1024 sc1\n"
        "v_mfma_f32_16x16x32_bf16 v[0:3], a[168:171], a[12:15], v[0:3]\n"
        "v_mfma_f32_16x16x32_bf16 v[4:7], a[172:175], a[12:15], v[4:7]\n"
        "v_mfma_f32_16x16x32_bf16 v[4:7], a[164:167], a[8:11], v[4:7]\n"
        "s_waitcnt lgkmcnt(3)\n"
        "v_mfma_f32_16x16x32_bf16 v[4:7], a[92:95], a[4:7], v[4:7]\n"
        "v_mfma_f32_16x16x32_bf16 v[4:7], a[84:87], a[0:3], v[4:7]\n"
        "v_mfma_f32_16x16x32_bf16 v[12:15], a[84:87], a[16:19], v[12:15]\n"
        "v_mfma_f32_16x16x32_bf16 v[12:15], a[92:95], a[20:23], v[12:15]\n"
        "global_load_lds_dwordx4 v254, s[60:61] offset:2048 sc1\n"
        "v_mfma_f32_16x16x32_bf16 v[12:15], a[164:167], a[24:27], v[12:15]\n"
        "v_mfma_f32_16x16x32_bf16 v[12:15], a[172:175], a[28:31], v[12:15]\n"
        "v_mfma_f32_16x16x32_bf16 v[8:11], a[168:171], a[28:31], v[8:11]\n"
        "v_mfma_f32_16x16x32_bf16 v[8:11], a[160:163], a[24:27], v[8:11]\n"
        "v_mfma_f32_16x16x32_bf16 v[8:11], a[88:91], a[20:23], v[8:11]\n"
        "v_mfma_f32_16x16x32_bf16 v[8:11], a[80:83], a[16:19], v[8:11]\n"
        "v_mfma_f32_16x16x32_bf16 v[16:19], a[80:83], a[192:195], v[16:19]\n"
        "s_barrier\n"
        "v_mfma_f32_16x16x32_bf16 v[16:19], a[88:91], a[196:199], v[16:19]\n"
        "ds_read_b128 a[0:3], v244 offset:32768\n"
        "global_load_lds_dwordx4 v254, s[60:61] offset:3072 sc1\n"
        "s_add_u32 m0, s73, 0x4800\n"
        "v_mfma_f32_16x16x32_bf16 v[16:19], a[160:163], a[200:203], v[16:19]\n"
        "ds_read_b128 a[4:7], v244 offset:36864\n"
        "s_add_u32 s60, s68, s66\n"
        "s_addc_u32 s61, s69, 0\n"
        "v_mfma_f32_16x16x32_bf16 v[16:19], a[168:171], a[204:207], v[16:19]\n"
        "ds_read_b128 a[8:11], v244 offset:40960\n"
        "s_xor_b32 s66, s17, 5\n"
        "s_lshl_b32 s66, s66, 15\n"
        "v_mfma_f32_16x16x32_bf16 v[20:23], a[172:175], a[204:207], v[20:23]\n"
        "ds_read_b128 a[12:15], v244 offset:45056\n"
        "s_waitcnt lgkmcnt(4)\n"
        "v_mfma_f32_16x16x32_bf16 v[20:23], a[164:167], a[200:203], v[20:23]\n"
        "ds_read_b128 a[16:19], v244 offset:33792\n"
        "v_mfma_f32_16x16x32_bf16 v[20:23], a[92:95], a[196:199], v[20:23]\n"
        "LHB2_%=:\n"
        "s_cbranch_execz LHRB_%=\n"
        "ds_read_b128 a[20:23], v244 offset:37888\n"
        "v_mfma_f32_16x16x32_bf16 v[20:23], a[84:87], a[192:195], v[20:23]\n"
        "ds_read_b128 a[24:27], v244 offset:41984\n"
        "v_mfma_f32_16x16x32_bf16 v[28:31], a[84:87], a[208:211], v[28:31]\n"
        "ds_read_b128 a[28:31], v244 offset:46080\n"
        "global_load_lds_dwordx4 v148, s[58:59] offset:0 sc1\n"
        "s_add_u32 m0, s73, 0x8000\n"
        "v_mfma_f32_16x16x32_bf16 v[28:31], a[92:95], a[212:215], v[28:31]\n"
        "ds_read_b128 a[192:195], v244 offset:34816\n"
        "s_add_u32 s58, s58, 0x8000\n"
        "s_addc_u32 s59, s59, 0\n"
        "v_mfma_f32_16x16x32_bf16 v[28:31], a[164:167], a[216:219], v[28:31]\n"
        "ds_read_b128 a[196:199], v244 offset:38912\n"
        "v_mfma_f32_16x16x32_bf16 v[28:31], a[172:175], a[220:223], v[28:31]\n"
        "ds_read_b128 a[200:203], v244 offset:43008\n"
        "v_mfma_f32_16x16x32_bf16 v[24:27], a[168:171], a[220:223], v[24:27]\n"
        "ds_read_b128 a[204:207], v244 offset:47104\n"
        "s_waitcnt lgkmcnt(8)\n"
        "v_mfma_f32_16x16x32_bf16 v[24:27], a[160:163], a[216:219], v[24:27]\n"
        "ds_read_b128 a[216:219], v244 offset:44032\n"
        "v_mfma_f32_16x16x32_bf16 v[24:27], a[88:91], a[212:215], v[24:27]\n"
        "ds_read_b128 a[212:215], v244 offset:39936\n"
        "v_mfma_f32_16x16x32_bf16 v[24:27], a[80:83], a[208:211], v[24:27]\n"
        "ds_read_b128 a[208:211], v244 offset:35840\n"
        "global_load_lds_dwordx4 v254, s[60:61] offset:0 sc1\n"
        "s_mul_i32 s16, s13, 263168\n"
        "v_mfma_f32_16x16x32_bf16 v[0:3], a[176:179], a[0:3], v[0:3]\n"
        "ds_read_b128 a[220:223], v244 offset:48128\n"
        "s_lshl2_add_u32 s15, s10, s74\n"
        "s_lshl_b32 s15, s15, 14\n"
        "v_mfma_f32_16x16x32_bf16 v[0:3], a[184:187], a[4:7], v[0:3]\n"
        "s_add_u32 s16, s16, s15\n"
        "s_add_u32 s16, s16, s73\n"
        "s_add_u32 s16, s16, 0x4000000\n"
        "v_mfma_f32_16x16x32_bf16 v[0:3], a[32:35], a[8:11], v[0:3]\n"
        "s_add_u32 s30, s38, s16\n"
        "s_addc_u32 s31, s39, 0\n"
        "s_mul_i32 s16, s13, 263168\n"
        "v_mfma_f32_16x16x32_bf16 v[0:3], a[40:43], a[12:15], v[0:3]\n"
        "s_lshl2_add_u32 s15, s10, s75\n"
        "s_lshl_b32 s15, s15, 14\n"
        "s_add_u32 s16, s16, s15\n"
        "v_mfma_f32_16x16x32_bf16 v[4:7], a[44:47], a[12:15], v[4:7]\n"
        "s_add_u32 s16, s16, s73\n"
        "s_add_u32 s16, s16, 0x4000000\n"
        "s_lshl2_add_u32 s15, s10, s76\n"
        "v_mfma_f32_16x16x32_bf16 v[4:7], a[36:39], a[8:11], v[4:7]\n"
        "s_lshl_b32 s15, s15, 14\n"
        "s_waitcnt lgkmcnt(3)\n"
        "v_mfma_f32_16x16x32_bf16 v[4:7], a[188:191], a[4:7], v[4:7]\n"
        "global_load_dwordx4 v[104:107], v151, s[64:65]\n"
        "v_mfma_f32_16x16x32_bf16 v[4:7], a[180:183], a[0:3], v[4:7]\n"
        "s_add_u32 s64, s64, 0x8000\n"
        "s_addc_u32 s65, s65, 0\n"
        "s_waitcnt vmcnt(13) lgkmcnt(1)\n"
        "v_mfma_f32_16x16x32_bf16 v[12:15], a[180:183], a[16:19], v[12:15]\n"
        "v_permlane16_swap_b32 v96, v98\n"
        "v_mfma_f32_16x16x32_bf16 v[12:15], a[188:191], a[20:23], v[12:15]\n"
        "v_permlane16_swap_b32 v97, v99\n"
        "v_mfma_f32_16x16x32_bf16 v[12:15], a[36:39], a[24:27], v[12:15]\n"
        "v_lshlrev_b32 v64, 16, v96\n"
        "v_and_b32 v65, s35, v96\n"
        "v_mfma_f32_16x16x32_bf16 v[12:15], a[44:47], a[28:31], v[12:15]\n"
        "v_lshlrev_b32 v66, 16, v97\n"
        "v_and_b32 v67, s35, v97\n"
        "v_mfma_f32_16x16x32_bf16 v[8:11], a[40:43], a[28:31], v[8:11]\n"
        "v_lshlrev_b32 v68, 16, v98\n"
        "v_and_b32 v69, s35, v98\n"
        "v_mfma_f32_16x16x32_bf16 v[8:11], a[32:35], a[24:27], v[8:11]\n"
        "global_load_lds_dwordx4 v254, s[60:61] offset:1024 sc1\n"
        "v_mfma_f32_16x16x32_bf16 v[8:11], a[184:187], a[20:23], v[8:11]\n"
        "v_lshlrev_b32 v70, 16, v99\n"
        "v_and_b32 v71, s35, v99\n"
        "v_mfma_f32_16x16x32_bf16 v[8:11], a[176:179], a[16:19], v[8:11]\n"
        "v_mfma_f32_16x16x32_bf16 v[16:19], a[176:179], a[192:195], v[16:19]\n"
        "s_barrier\n"
        "v_mfma_f32_16x16x32_bf16 v[16:19], a[184:187], a[196:199], v[16:19]\n"
        "ds_read_b128 a[0:3], v244 offset:49152\n"
        "v_mfma_f32_16x16x32_bf16 v[16:19], a[32:35], a[200:203], v[16:19]\n"
        "ds_read_b128 a[4:7], v244 offset:53248\n"
        "v_mfma_f32_16x16x32_bf16 v[16:19], a[40:43], a[204:207], v[16:19]\n"
        "ds_read_b128 a[8:11], v244 offset:57344\n"
        "v_mfma_f32_16x16x32_bf16 v[20:23], a[44:47], a[204:207], v[20:23]\n"
        "ds_read_b128 a[12:15], v244 offset:61440\n"
        "global_load_lds_dwordx4 v254, s[60:61] offset:2048 sc1\n"
        "v_mfma_f32_16x16x32_bf16 v[20:23], a[36:39], a[200:203], v[20:23]\n"
        "ds_read_b128 a[28:31], v244 offset:62464\n"
        "v_mfma_f32_16x16x32_bf16 v[20:23], a[188:191], a[196:199], v[20:23]\n"
        "ds_read_b128 a[24:27], v244 offset:58368\n"
        "v_mfma_f32_16x16x32_bf16 v[20:23], a[180:183], a[192:195], v[20:23]\n"
        "ds_read_b128 a[20:23], v244 offset:54272\n"
        "v_mfma_f32_16x16x32_bf16 v[28:31], a[180:183], a[208:211], v[28:31]\n"
        "ds_read_b128 a[16:19], v244 offset:50176\n"
        "v_mfma_f32_16x16x32_bf16 v[28:31], a[188:191], a[212:215], v[28:31]\n"
        "ds_read_b128 a[192:195], v244 offset:51200\n"
        "s_waitcnt lgkmcnt(8)\n"
        "v_mfma_f32_16x16x32_bf16 v[0:3], a[48:51], a[0:3], v[0:3]\n"
        "ds_read_b128 a[196:199], v244 offset:55296\n"
        "s_waitcnt lgkmcnt(8)\n"
        "v_mfma_f32_16x16x32_bf16 v[0:3], a[56:59], a[4:7], v[0:3]\n"
        "ds_read_b128 a[200:203], v244 offset:59392\n"
        "s_waitcnt lgkmcnt(8)\n"
        "v_mfma_f32_16x16x32_bf16 v[0:3], a[224:227], a[8:11], v[0:3]\n"
        "ds_read_b128 a[204:207], v244 offset:63488\n"
        "s_waitcnt lgkmcnt(8)\n"
        "v_mfma_f32_16x16x32_bf16 v[0:3], a[232:235], a[12:15], v[0:3]\n"
        "global_load_lds_dwordx4 v254, s[60:61] offset:3072 sc1\n"
        "s_add_u32 m0, s73, 0x4c00\n"
        "v_mfma_f32_16x16x32_bf16 v[4:7], a[236:239], a[12:15], v[4:7]\n"
        "s_add_u32 s60, s68, s66\n"
        "s_addc_u32 s61, s69, 0\n"
        "s_xor_b32 s66, s17, 6\n"
        "v_mfma_f32_16x16x32_bf16 v[4:7], a[228:231], a[8:11], v[4:7]\n"
        "s_lshl_b32 s66, s66, 15\n"
        "v_mfma_f32_16x16x32_bf16 v[4:7], a[60:63], a[4:7], v[4:7]\n"
        "v_cvt_pk_bf16_f32 v0, v0, v1\n"
        "v_cvt_pk_bf16_f32 v1, v2, v3\n"
        "v_mfma_f32_16x16x32_bf16 v[4:7], a[52:55], a[0:3], v[4:7]\n"
        "s_waitcnt lgkmcnt(3)\n"
        "v_mfma_f32_16x16x32_bf16 v[28:31], a[36:39], a[216:219], v[28:31]\n"
        "v_mfma_f32_16x16x32_bf16 v[28:31], a[44:47], a[220:223], v[28:31]\n"
        "v_mfma_f32_16x16x32_bf16 v[8:11], a[232:235], a[28:31], v[8:11]\n"
        "global_load_lds_dwordx4 v148, s[58:59] offset:0 sc1\n"
        "s_add_u32 m0, s73, 0xc000\n"
        "v_mfma_f32_16x16x32_bf16 v[8:11], a[224:227], a[24:27], v[8:11]\n"
        "v_mfma_f32_16x16x32_bf16 v[8:11], a[56:59], a[20:23], v[8:11]\n"
        "v_cvt_pk_bf16_f32 v2, v4, v5\n"
        "v_cvt_pk_bf16_f32 v3, v6, v7\n"
        "v_mfma_f32_16x16x32_bf16 v[8:11], a[48:51], a[16:19], v[8:11]\n"
        "v_mfma_f32_16x16x32_bf16 v[12:15], a[52:55], a[16:19], v[12:15]\n"
        "global_store_dwordx4 v254, v[0:3], s[28:29] offset:0 sc1\n"
        "s_barrier\n"
        "ds_read_b128 a[0:3], v245 offset:0\n"
        "v_mfma_f32_16x16x32_bf16 v[12:15], a[60:63], a[20:23], v[12:15]\n"
        "ds_read_b128 a[4:7], v245 offset:4096\n"
        "v_mfma_f32_16x16x32_bf16 v[12:15], a[228:231], a[24:27], v[12:15]\n"
        "ds_read_b128 a[8:11], v245 offset:8192\n"
        "v_cvt_pk_bf16_f32 v8, v8, v9\n"
        "v_mfma_f32_16x16x32_bf16 v[12:15], a[236:239], a[28:31], v[12:15]\n"
        "ds_read_b128 a[12:15], v245 offset:12288\n"
        "v_cvt_pk_bf16_f32 v9, v10, v11\n"
        "v_mfma_f32_16x16x32_bf16 v[24:27], a[40:43], a[220:223], v[24:27]\n"
        "ds_read_b128 a[220:223], v244 offset:64512\n"
        "v_mfma_f32_16x16x32_bf16 v[24:27], a[32:35], a[216:219], v[24:27]\n"
        "ds_read_b128 a[216:219], v244 offset:60416\n"
        "s_waitcnt lgkmcnt(6)\n"
        "v_mfma_f32_16x16x32_bf16 v[24:27], a[184:187], a[212:215], v[24:27]\n"
        "ds_read_b128 a[212:215], v244 offset:56320\n"
        "global_load_lds_dwordx4 v254, s[60:61] offset:0 sc1\n"
        "v_mfma_f32_16x16x32_bf16 v[24:27], a[176:179], a[208:211], v[24:27]\n"
        "ds_read_b128 a[208:211], v244 offset:52224\n"
        "v_cvt_pk_bf16_f32 v10, v12, v13\n"
        "v_mfma_f32_16x16x32_bf16 v[16:19], a[48:51], a[192:195], v[16:19]\n"
        "ds_read_b128 a[16:19], v245 offset:1024\n"
        "v_cvt_pk_bf16_f32 v11, v14, v15\n"
        "v_mfma_f32_16x16x32_bf16 v[16:19], a[56:59], a[196:199], v[16:19]\n"
        "ds_read_b128 a[20:23], v245 offset:5120\n"
        "v_mfma_f32_16x16x32_bf16 v[16:19], a[224:227], a[200:203], v[16:19]\n"
        "global_store_dwordx4 v254, v[8:11], s[28:29] offset:1024 sc1\n"
        "ds_read_b128 a[24:27], v245 offset:9216\n"
        "ds_read_b128 a[28:31], v245 offset:13312\n"
        "v_mfma_f32_16x16x32_bf16 v[16:19], a[232:235], a[204:207], v[16:19]\n"
        "v_mfma_f32_16x16x32_bf16 v[20:23], a[236:239], a[204:207], v[20:23]\n"
        "ds_read_b128 a[204:207], v245 offset:14336\n"
        "s_waitcnt lgkmcnt(7)\n"
        "v_mfma_f32_16x16x32_bf16 v[20:23], a[228:231], a[200:203], v[20:23]\n"
        "ds_read_b128 a[200:203], v245 offset:10240\n"
        "v_mfma_f32_16x16x32_bf16 v[20:23], a[60:63], a[196:199], v[20:23]\n"
        "ds_read_b128 a[196:199], v245 offset:6144\n"
        "s_waitcnt lgkmcnt(7)\n"
        "v_mfma_f32_16x16x32_bf16 v[20:23], a[52:55], a[192:195], v[20:23]\n"
        "ds_read_b128 a[192:195], v245 offset:2048\n"
        "v_cvt_pk_bf16_f32 v16, v16, v17\n"
        "v_mfma_f32_16x16x32_bf16 v[24:27], a[232:235], a[220:223], v[24:27]\n"
        "global_load_dwordx4 v[108:111], v151, s[64:65]\n"
        "v_mfma_f32_16x16x32_bf16 v[24:27], a[224:227], a[216:219], v[24:27]\n"
        "s_waitcnt vmcnt(14) lgkmcnt(6)\n"
        "v_permlane16_swap_b32 v100, v102\n"
        "v_mfma_f32_16x16x32_bf16 v[28:31], a[52:55], a[208:211], v[28:31]\n"
        "v_permlane16_swap_b32 v101, v103\n"
        "v_mfma_f32_16x16x32_bf16 v[24:27], a[56:59], a[212:215], v[24:27]\n"
        "v_cvt_pk_bf16_f32 v17, v18, v19\n"
        "v_cvt_pk_bf16_f32 v18, v20, v21\n"
        "v_mfma_f32_16x16x32_bf16 v[28:31], a[60:63], a[212:215], v[28:31]\n"
        "ds_read_b128 a[212:215], v245 offset:7168\n"
        "v_cvt_pk_bf16_f32 v19, v22, v23\n"
        "v_mfma_f32_16x16x32_bf16 v[24:27], a[48:51], a[208:211], v[24:27]\n"
        "global_store_dwordx4 v254, v[16:19], s[28:29] offset:2048 sc1\n"
        "ds_read_b128 a[208:211], v245 offset:3072\n"
        "v_lshlrev_b32 v72, 16, v100\n"
        "v_and_b32 v73, s35, v100\n"
        "v_lshlrev_b32 v74, 16, v101\n"
        "v_mfma_f32_16x16x32_bf16 v[28:31], a[228:231], a[216:219], v[28:31]\n"
        "ds_read_b128 a[216:219], v245 offset:11264\n"
        "v_and_b32 v75, s35, v101\n"
        "v_mfma_f32_16x16x32_bf16 v[28:31], a[236:239], a[220:223], v[28:31]\n"
        "ds_read_b128 a[220:223], v245 offset:15360\n"
        "v_cvt_pk_bf16_f32 v24, v24, v25\n"
        "v_mfma_f32_16x16x32_bf16 v[32:35], a[96:99], a[0:3], 0\n"
        "v_cvt_pk_bf16_f32 v25, v26, v27\n"
        "v_lshlrev_b32 v76, 16, v102\n"
        "v_mfma_f32_16x16x32_bf16 v[32:35], a[104:107], a[4:7], v[32:35]\n"
        "v_and_b32 v77, s35, v102\n"
        "v_lshlrev_b32 v78, 16, v103\n"
        "v_mfma_f32_16x16x32_bf16 v[32:35], a[112:115], a[8:11], v[32:35]\n"
        "v_cvt_pk_bf16_f32 v26, v28, v29\n"
        "v_cvt_pk_bf16_f32 v27, v30, v31\n"
        "v_mfma_f32_16x16x32_bf16 v[32:35], a[120:123], a[12:15], v[32:35]\n"
        "global_store_dwordx4 v254, v[24:27], s[28:29] offset:3072 sc1\n"
        "v_and_b32 v79, s35, v103\n"
        "v_mfma_f32_16x16x32_bf16 v[36:39], a[124:127], a[12:15], 0\n"
        "v_mfma_f32_16x16x32_bf16 v[36:39], a[116:119], a[8:11], v[36:39]\n"
        "v_mfma_f32_16x16x32_bf16 v[36:39], a[108:111], a[4:7], v[36:39]\n"
        "s_waitcnt lgkmcnt(1)\n"
        "v_mfma_f32_16x16x32_bf16 v[36:39], a[100:103], a[0:3], v[36:39]\n"
        "v_mfma_f32_16x16x32_bf16 v[44:47], a[100:103], a[16:19], 0\n"
        "v_mfma_f32_16x16x32_bf16 v[44:47], a[108:111], a[20:23], v[44:47]\n"
        "global_load_lds_dwordx4 v254, s[60:61] offset:1024 sc1\n"
        "v_mfma_f32_16x16x32_bf16 v[44:47], a[116:119], a[24:27], v[44:47]\n"
        "v_mfma_f32_16x16x32_bf16 v[44:47], a[124:127], a[28:31], v[44:47]\n"
        "v_mfma_f32_16x16x32_bf16 v[40:43], a[120:123], a[28:31], 0\n"
        "v_mfma_f32_16x16x32_bf16 v[40:43], a[112:115], a[24:27], v[40:43]\n"
        "v_mfma_f32_16x16x32_bf16 v[40:43], a[104:107], a[20:23], v[40:43]\n"
        "v_mfma_f32_16x16x32_bf16 v[40:43], a[96:99], a[16:19], v[40:43]\n"
        "v_mfma_f32_16x16x32_bf16 v[48:51], a[96:99], a[192:195], 0\n"
        "s_barrier\n"
        "v_mfma_f32_16x16x32_bf16 v[48:51], a[104:107], a[196:199], v[48:51]\n"
        "ds_read_b128 a[0:3], v245 offset:16384\n"
        "global_load_lds_dwordx4 v254, s[60:61] offset:2048 sc1\n"
        "v_mfma_f32_16x16x32_bf16 v[48:51], a[112:115], a[200:203], v[48:51]\n"
        "ds_read_b128 a[4:7], v245 offset:20480\n"
        "v_mfma_f32_16x16x32_bf16 v[48:51], a[120:123], a[204:207], v[48:51]\n"
        "ds_read_b128 a[8:11], v245 offset:24576\n"
        "v_mfma_f32_16x16x32_bf16 v[52:55], a[124:127], a[204:207], 0\n"
        "ds_read_b128 a[12:15], v245 offset:28672\n"
        "v_mfma_f32_16x16x32_bf16 v[52:55], a[116:119], a[200:203], v[52:55]\n"
        "ds_read_b128 a[16:19], v245 offset:17408\n"
        "v_mfma_f32_16x16x32_bf16 v[52:55], a[108:111], a[196:199], v[52:55]\n"
        "ds_read_b128 a[20:23], v245 offset:21504\n"
        "v_mfma_f32_16x16x32_bf16 v[52:55], a[100:103], a[192:195], v[52:55]\n"
        "ds_read_b128 a[24:27], v245 offset:25600\n"
        "s_waitcnt lgkmcnt(7)\n"
        "v_mfma_f32_16x16x32_bf16 v[60:63], a[100:103], a[208:211], 0\n"
        "ds_read_b128 a[28:31], v245 offset:29696\n"
        "global_load_lds_dwordx4 v254, s[60:61] offset:3072 sc1\n"
        "s_add_u32 m0, s73, 0x18000\n"
        "v_mfma_f32_16x16x32_bf16 v[60:63], a[108:111], a[212:215], v[60:63]\n"
        "ds_read_b128 a[192:195], v245 offset:18432\n"
        "s_add_u32 s60, s68, s66\n"
        "s_addc_u32 s61, s69, 0\n"
        "v_mfma_f32_16x16x32_bf16 v[60:63], a[116:119], a[216:219], v[60:63]\n"
        "ds_read_b128 a[196:199], v245 offset:22528\n"
        "s_xor_b32 s66, s17, 7\n"
        "s_lshl_b32 s66, s66, 15\n"
        "v_mfma_f32_16x16x32_bf16 v[60:63], a[124:127], a[220:223], v[60:63]\n"
        "ds_read_b128 a[200:203], v245 offset:26624\n"
        "v_mfma_f32_16x16x32_bf16 v[56:59], a[120:123], a[220:223], 0\n"
        "ds_read_b128 a[204:207], v245 offset:30720\n"
        "s_waitcnt lgkmcnt(8)\n"
        "v_mfma_f32_16x16x32_bf16 v[56:59], a[112:115], a[216:219], v[56:59]\n"
        "ds_read_b128 a[216:219], v245 offset:27648\n"
        "v_mfma_f32_16x16x32_bf16 v[56:59], a[104:107], a[212:215], v[56:59]\n"
        "ds_read_b128 a[212:215], v245 offset:23552\n"
        "v_mfma_f32_16x16x32_bf16 v[56:59], a[96:99], a[208:211], v[56:59]\n"
        "ds_read_b128 a[208:211], v245 offset:19456\n"
        "global_load_lds_dwordx4 v254, s[60:61] offset:0 sc1\n"
        "v_mfma_f32_16x16x32_bf16 v[32:35], a[128:131], a[0:3], v[32:35]\n"
        "ds_read_b128 a[220:223], v245 offset:31744\n"
        "s_waitcnt vmcnt(14) lgkmcnt(8)\n"
        "v_mfma_f32_16x16x32_bf16 v[32:35], a[136:139], a[4:7], v[32:35]\n"
        "v_permlane16_swap_b32 v104, v106\n"
        "v_mfma_f32_16x16x32_bf16 v[32:35], a[144:147], a[8:11], v[32:35]\n"
        "v_permlane16_swap_b32 v105, v107\n"
        "v_mfma_f32_16x16x32_bf16 v[32:35], a[152:155], a[12:15], v[32:35]\n"
        "v_lshlrev_b32 v80, 16, v104\n"
        "v_and_b32 v81, s35, v104\n"
        "v_mfma_f32_16x16x32_bf16 v[36:39], a[156:159], a[12:15], v[36:39]\n"
        "v_lshlrev_b32 v82, 16, v105\n"
        "v_and_b32 v83, s35, v105\n"
        "v_mfma_f32_16x16x32_bf16 v[36:39], a[148:151], a[8:11], v[36:39]\n"
        "v_lshlrev_b32 v84, 16, v106\n"
        "v_and_b32 v85, s35, v106\n"
        "v_mfma_f32_16x16x32_bf16 v[36:39], a[140:143], a[4:7], v[36:39]\n"
        "global_load_lds_dwordx4 v254, s[60:61] offset:1024 sc1\n"
        "v_mfma_f32_16x16x32_bf16 v[36:39], a[132:135], a[0:3], v[36:39]\n"
        "v_lshlrev_b32 v86, 16, v107\n"
        "v_and_b32 v87, s35, v107\n"
        "v_mfma_f32_16x16x32_bf16 v[44:47], a[132:135], a[16:19], v[44:47]\n"
        "v_mfma_f32_16x16x32_bf16 v[44:47], a[140:143], a[20:23], v[44:47]\n"
        "v_mfma_f32_16x16x32_bf16 v[44:47], a[148:151], a[24:27], v[44:47]\n"
        "v_mfma_f32_16x16x32_bf16 v[44:47], a[156:159], a[28:31], v[44:47]\n"
        "v_mfma_f32_16x16x32_bf16 v[40:43], a[152:155], a[28:31], v[40:43]\n"
        "s_waitcnt lgkmcnt(0)\n"
        "v_mfma_f32_16x16x32_bf16 v[40:43], a[144:147], a[24:27], v[40:43]\n"
        "global_load_lds_dwordx4 v254, s[60:61] offset:2048 sc1\n"
        "v_mfma_f32_16x16x32_bf16 v[40:43], a[136:139], a[20:23], v[40:43]\n"
        "v_mfma_f32_16x16x32_bf16 v[40:43], a[128:131], a[16:19], v[40:43]\n"
        "v_mfma_f32_16x16x32_bf16 v[48:51], a[128:131], a[192:195], v[48:51]\n"
        "s_barrier\n"
        "v_mfma_f32_16x16x32_bf16 v[48:51], a[136:139], a[196:199], v[48:51]\n"
        "ds_read_b128 a[0:3], v244 offset:0\n"
        "v_mfma_f32_16x16x32_bf16 v[48:51], a[144:147], a[200:203], v[48:51]\n"
        "ds_read_b128 a[4:7], v244 offset:4096\n"
        "v_mfma_f32_16x16x32_bf16 v[48:51], a[152:155], a[204:207], v[48:51]\n"
        "ds_read_b128 a[8:11], v244 offset:8192\n"
        "v_mfma_f32_16x16x32_bf16 v[52:55], a[156:159], a[204:207], v[52:55]\n"
        "ds_read_b128 a[12:15], v244 offset:12288\n"
        "global_load_lds_dwordx4 v254, s[60:61] offset:3072 sc1\n"
        "s_add_u32 m0, s73, 0x1c000\n"
        "v_mfma_f32_16x16x32_bf16 v[52:55], a[148:151], a[200:203], v[52:55]\n"
        "ds_read_b128 a[16:19], v244 offset:1024\n"
        "s_add_u32 s60, s68, s66\n"
        "s_addc_u32 s61, s69, 0\n"
        "v_mfma_f32_16x16x32_bf16 v[52:55], a[140:143], a[196:199], v[52:55]\n"
        "ds_read_b128 a[20:23], v244 offset:5120\n"
        "v_mfma_f32_16x16x32_bf16 v[52:55], a[132:135], a[192:195], v[52:55]\n"
        "ds_read_b128 a[24:27], v244 offset:9216\n"
        "v_mfma_f32_16x16x32_bf16 v[60:63], a[132:135], a[208:211], v[60:63]\n"
        "ds_read_b128 a[28:31], v244 offset:13312\n"
        "v_mfma_f32_16x16x32_bf16 v[60:63], a[140:143], a[212:215], v[60:63]\n"
        "ds_read_b128 a[192:195], v244 offset:2048\n"
        "v_mfma_f32_16x16x32_bf16 v[60:63], a[148:151], a[216:219], v[60:63]\n"
        "ds_read_b128 a[196:199], v244 offset:6144\n"
        "s_waitcnt vmcnt(7)\n"
        "v_mfma_f32_16x16x32_bf16 v[60:63], a[156:159], a[220:223], v[60:63]\n"
        "s_mov_b64 exec, 0xffff\n"
        "global_store_dwordx2 v154, v[250:251], s[48:49] sc1\n"
        "s_mov_b64 exec, -1\n"
        "v_mfma_f32_16x16x32_bf16 v[56:59], a[152:155], a[220:223], v[56:59]\n"
        "ds_read_b128 a[200:203], v244 offset:10240\n"
        "global_load_lds_dwordx4 v254, s[60:61] offset:0 sc1\n"
        "v_mfma_f32_16x16x32_bf16 v[56:59], a[144:147], a[216:219], v[56:59]\n"
        "ds_read_b128 a[204:207], v244 offset:14336\n"
        "s_waitcnt lgkmcnt(7)\n"
        "v_mfma_f32_16x16x32_bf16 v[56:59], a[136:139], a[212:215], v[56:59]\n"
        "ds_read_b128 a[212:215], v244 offset:7168\n"
        "v_mfma_f32_16x16x32_bf16 v[56:59], a[128:131], a[208:211], v[56:59]\n"
        "ds_read_b128 a[208:211], v244 offset:3072\n"
        "v_mfma_f32_16x16x32_bf16 v[32:35], v[160:163], a[0:3], v[32:35]\n"
        "ds_read_b128 a[216:219], v244 offset:11264\n"
        "v_mfma_f32_16x16x32_bf16 v[32:35], v[168:171], a[4:7], v[32:35]\n"
        "ds_read_b128 a[220:223], v244 offset:15360\n"
        "v_mfma_f32_16x16x32_bf16 v[32:35], v[176:179], a[8:11], v[32:35]\n"
        "ds_read_b64_tr_b16 v[128:129], v149 offset:0\n"
        "v_mfma_f32_16x16x32_bf16 v[32:35], v[184:187], a[12:15], v[32:35]\n"
        "ds_read_b64_tr_b16 v[130:131], v149 offset:2048\n"
        "global_load_lds_dwordx4 v254, s[60:61] offset:1024 sc1\n"
        "v_mfma_f32_16x16x32_bf16 v[36:39], v[188:191], a[12:15], v[36:39]\n"
        "ds_read_b64_tr_b16 v[132:133], v149 offset:32\n"
        "v_mfma_f32_16x16x32_bf16 v[36:39], v[180:183], a[8:11], v[36:39]\n"
        "ds_read_b64_tr_b16 v[134:135], v149 offset:2080\n"
        "s_waitcnt lgkmcnt(8)\n"
        "v_mfma_f32_16x16x32_bf16 v[36:39], v[172:175], a[4:7], v[36:39]\n"
        "ds_read_b64_tr_b16 v[136:137], v149 offset:64\n"
        "v_mfma_f32_16x16x32_bf16 v[36:39], v[164:167], a[0:3], v[36:39]\n"
        "ds_read_b64_tr_b16 v[138:139], v149 offset:2112\n"
        "v_mfma_f32_16x16x32_bf16 v[44:47], v[164:167], a[16:19], v[44:47]\n"
        "ds_read_b64_tr_b16 v[140:141], v149 offset:96\n"
        "v_mfma_f32_16x16x32_bf16 v[44:47], v[172:175], a[20:23], v[44:47]\n"
        "ds_read_b64_tr_b16 v[142:143], v149 offset:2144\n"
        "v_mfma_f32_16x16x32_bf16 v[40:43], v[184:187], a[28:31], v[40:43]\n"
        "global_load_lds_dwordx4 v254, s[60:61] offset:2048 sc1\n"
        "v_mfma_f32_16x16x32_bf16 v[40:43], v[176:179], a[24:27], v[40:43]\n"
        "s_waitcnt lgkmcnt(6)\n"
        "v_lshlrev_b32 v246, 16, v128\n"
        "v_mul_f32 v64, v64, v246\n"
        "v_mfma_f32_16x16x32_bf16 v[48:51], v[160:163], a[192:195], v[48:51]\n"
        "s_waitcnt lgkmcnt(5)\n"
        "v_cvt_pk_bf16_f32 v64, 0, v64\n"
        "v_mul_f32 v64, v64, v228\n"
        "v_mfma_f32_16x16x32_bf16 v[40:43], v[168:171], a[20:23], v[40:43]\n"
        "v_cvt_pk_bf16_f32 v64, 0, v64\n"
        "v_and_b32 v247, s35, v128\n"
        "v_mfma_f32_16x16x32_bf16 v[48:51], v[168:171], a[196:199], v[48:51]\n"
        "v_mul_f32 v65, v65, v247\n"
        "v_cvt_pk_bf16_f32 v65, 0, v65\n"
        "v_mfma_f32_16x16x32_bf16 v[40:43], v[160:163], a[16:19], v[40:43]\n"
        "v_mul_f32 v65, v65, v228\n"
        "v_cvt_pk_bf16_f32 v65, 0, v65\n"
        "v_mfma_f32_16x16x32_bf16 v[48:51], v[176:179], a[200:203], v[48:51]\n"
        "s_barrier\n"
        "v_mfma_f32_16x16x32_bf16 v[52:55], v[188:191], a[204:207], v[52:55]\n"
        "ds_read_b128 a[0:3], v244 offset:16384\n"
        "global_load_lds_dwordx4 v254, s[60:61] offset:3072 sc1\n"
        "v_mfma_f32_16x16x32_bf16 v[48:51], v[184:187], a[204:207], v[48:51]\n"
        "ds_read_b128 a[204:207], v244 offset:30720\n"
        "v_lshlrev_b32 v248, 16, v129\n"
        "v_mfma_f32_16x16x32_bf16 v[52:55], v[180:183], a[200:203], v[52:55]\n"
        "ds_read_b128 a[200:203], v244 offset:26624\n"
        "v_mul_f32 v66, v66, v248\n"
        "v_mfma_f32_16x16x32_bf16 v[52:55], v[172:175], a[196:199], v[52:55]\n"
        "ds_read_b128 a[196:199], v244 offset:22528\n"
        "v_cvt_pk_bf16_f32 v66, 0, v66\n"
        "v_mfma_f32_16x16x32_bf16 v[52:55], v[164:167], a[192:195], v[52:55]\n"
        "ds_read_b128 a[192:195], v244 offset:18432\n"
        "v_mul_f32 v66, v66, v228\n"
        "v_mfma_f32_16x16x32_bf16 v[44:47], v[180:183], a[24:27], v[44:47]\n"
        "ds_read_b128 a[4:7], v244 offset:20480\n"
        "v_cvt_pk_bf16_f32 v66, 0, v66\n"
        "v_mfma_f32_16x16x32_bf16 v[44:47], v[188:191], a[28:31], v[44:47]\n"
        "ds_read_b128 a[8:11], v244 offset:24576\n"
        "v_and_b32 v249, s35, v129\n"
        "v_mfma_f32_16x16x32_bf16 v[60:63], v[164:167], a[208:211], v[60:63]\n"
        "ds_read_b128 a[12:15], v244 offset:28672\n"
        "v_mul_f32 v67, v67, v249\n"
        "v_mfma_f32_16x16x32_bf16 v[60:63], v[172:175], a[212:215], v[60:63]\n"
        "ds_read_b128 a[16:19], v244 offset:17408\n"
        "v_cvt_pk_bf16_f32 v67, 0, v67\n"
        "v_mfma_f32_16x16x32_bf16 v[60:63], v[180:183], a[216:219], v[60:63]\n"
        "s_waitcnt lgkmcnt(7)\n"
        "ds_read_b128 a[20:23], v244 offset:21504\n"
        "v_mfma_f32_16x16x32_bf16 v[52:55], v[220:223], a[204:207], v[52:55]\n"
        "s_waitcnt lgkmcnt(7)\n"
        "ds_read_b128 a[24:27], v244 offset:25600\n"
        "v_mfma_f32_16x16x32_bf16 v[52:55], v[212:215], a[200:203], v[52:55]\n"
        "s_waitcnt lgkmcnt(7)\n"
        "ds_read_b128 a[28:31], v244 offset:29696\n"
        "v_mfma_f32_16x16x32_bf16 v[52:55], v[204:207], a[196:199], v[52:55]\n"
        "s_waitcnt lgkmcnt(7)\n"
        "v_permlane16_swap_b32 v108, v110\n"
        "v_mfma_f32_16x16x32_bf16 v[52:55], v[196:199], a[192:195], v[52:55]\n"
        "v_permlane16_swap_b32 v109, v111\n"
        "v_mfma_f32_16x16x32_bf16 v[60:63], v[188:191], a[220:223], v[60:63]\n"
        "global_load_dwordx2 v[156:157], v155, s[48:49] sc1\n"
        "v_mul_f32 v67, v67, v228\n"
        "v_mfma_f32_16x16x32_bf16 v[56:59], v[184:187], a[220:223], v[56:59]\n"
        "ds_read_b128 a[220:223], v244 offset:31744\n"
        "v_cvt_pk_bf16_f32 v67, 0, v67\n"
        "v_mfma_f32_16x16x32_bf16 v[56:59], v[176:179], a[216:219], v[56:59]\n"
        "ds_read_b128 a[216:219], v244 offset:27648\n"
        "v_lshlrev_b32 v144, 16, v130\n"
        "v_mfma_f32_16x16x32_bf16 v[56:59], v[168:171], a[212:215], v[56:59]\n"
        "ds_read_b128 a[212:215], v244 offset:23552\n"
        "v_mul_f32 v68, v68, v144\n"
        "v_mfma_f32_16x16x32_bf16 v[56:59], v[160:163], a[208:211], v[56:59]\n"
        "ds_read_b128 a[208:211], v244 offset:19456\n"
        "v_cvt_pk_bf16_f32 v68, 0, v68\n"
        "v_mfma_f32_16x16x32_bf16 v[32:35], v[192:195], a[0:3], v[32:35]\n"
        "s_waitcnt lgkmcnt(5)\n"
        "v_mul_f32 v68, v68, v228\n"
        "v_cvt_pk_bf16_f32 v68, 0, v68\n"
        "v_mfma_f32_16x16x32_bf16 v[32:35], v[200:203], a[4:7], v[32:35]\n"
        "v_and_b32 v145, s35, v130\n"
        "v_mul_f32 v69, v69, v145\n"
        "v_mfma_f32_16x16x32_bf16 v[32:35], v[208:211], a[8:11], v[32:35]\n"
        "v_cvt_pk_bf16_f32 v69, 0, v69\n"
        "v_mul_f32 v69, v69, v228\n"
        "v_mfma_f32_16x16x32_bf16 v[32:35], v[216:219], a[12:15], v[32:35]\n"
        "v_cvt_pk_bf16_f32 v69, 0, v69\n"
        "v_lshlrev_b32 v146, 16, v131\n"
        "v_mfma_f32_16x16x32_bf16 v[36:39], v[220:223], a[12:15], v[36:39]\n"
        "v_mul_f32 v70, v70, v146\n"
        "v_cvt_pk_bf16_f32 v70, 0, v70\n"
        "v_mfma_f32_16x16x32_bf16 v[36:39], v[212:215], a[8:11], v[36:39]\n"
        "v_mul_f32 v70, v70, v228\n"
        "v_cvt_pk_bf16_f32 v70, 0, v70\n"
        "v_mfma_f32_16x16x32_bf16 v[36:39], v[204:207], a[4:7], v[36:39]\n"
        "v_and_b32 v147, s35, v131\n"
        "v_mul_f32 v71, v71, v147\n"
        "v_mfma_f32_16x16x32_bf16 v[36:39], v[196:199], a[0:3], v[36:39]\n"
        "s_waitcnt lgkmcnt(0)\n"
        "v_cvt_pk_bf16_f32 v71, 0, v71\n"
        "v_mul_f32 v71, v71, v228\n"
        "v_mfma_f32_16x16x32_bf16 v[44:47], v[196:199], a[16:19], v[44:47]\n"
        "v_cvt_pk_bf16_f32 v71, 0, v71\n"
        "v_lshlrev_b32 v246, 16, v132\n"
        "v_mfma_f32_16x16x32_bf16 v[44:47], v[204:207], a[20:23], v[44:47]\n"
        "v_and_b32 v247, s35, v132\n"
        "v_lshlrev_b32 v248, 16, v133\n"
        "v_mfma_f32_16x16x32_bf16 v[44:47], v[212:215], a[24:27], v[44:47]\n"
        "v_and_b32 v249, s35, v133\n"
        "v_mul_f32 v72, v72, v246\n"
        "v_mfma_f32_16x16x32_bf16 v[44:47], v[220:223], a[28:31], v[44:47]\n"
        "v_mul_f32 v73, v73, v247\n"
        "v_mul_f32 v74, v74, v248\n"
        "v_mfma_f32_16x16x32_bf16 v[40:43], v[216:219], a[28:31], v[40:43]\n"
        "v_mul_f32 v75, v75, v249\n"
        "v_cvt_pk_bf16_f32 v72, 0, v72\n"
        "v_mfma_f32_16x16x32_bf16 v[40:43], v[208:211], a[24:27], v[40:43]\n"
        "v_cvt_pk_bf16_f32 v73, 0, v73\n"
        "v_cvt_pk_bf16_f32 v74, 0, v74\n"
        "v_mfma_f32_16x16x32_bf16 v[40:43], v[200:203], a[20:23], v[40:43]\n"
        "v_cvt_pk_bf16_f32 v75, 0, v75\n"
        "v_mul_f32 v72, v72, v229\n"
        "v_mfma_f32_16x16x32_bf16 v[40:43], v[192:195], a[16:19], v[40:43]\n"
        "v_mul_f32 v73, v73, v229\n"
        "v_mul_f32 v74, v74, v229\n"
        "v_mfma_f32_16x16x32_bf16 v[48:51], v[192:195], a[192:195], v[48:51]\n"
        "s_barrier\n"
        "v_mfma_f32_16x16x32_bf16 v[48:51], v[200:203], a[196:199], v[48:51]\n"
        "ds_read_b128 a[0:3], v254 offset:32768\n"
        "v_mul_f32 v75, v75, v229\n"
        "v_mfma_f32_16x16x32_bf16 v[48:51], v[208:211], a[200:203], v[48:51]\n"
        "ds_read_b128 a[4:7], v254 offset:36864\n"
        "v_cvt_pk_bf16_f32 v72, 0, v72\n"
        "v_mfma_f32_16x16x32_bf16 v[48:51], v[216:219], a[204:207], v[48:51]\n"
        "ds_read_b128 a[8:11], v254 offset:40960\n"
        "v_cvt_pk_bf16_f32 v73, 0, v73\n"
        "v_mfma_f32_16x16x32_bf16 v[60:63], v[196:199], a[208:211], v[60:63]\n"
        "ds_read_b128 a[12:15], v254 offset:45056\n"
        "v_cvt_pk_bf16_f32 v74, 0, v74\n"
        "v_mfma_f32_16x16x32_bf16 v[60:63], v[204:207], a[212:215], v[60:63]\n"
        "ds_read_b128 a[16:19], v254 offset:33792\n"
        "v_cvt_pk_bf16_f32 v75, 0, v75\n"
        "v_mfma_f32_16x16x32_bf16 v[60:63], v[212:215], a[216:219], v[60:63]\n"
        "ds_read_b128 a[20:23], v254 offset:37888\n"
        "v_lshlrev_b32 v144, 16, v134\n"
        "v_mfma_f32_16x16x32_bf16 v[60:63], v[220:223], a[220:223], v[60:63]\n"
        "ds_read_b128 a[24:27], v254 offset:41984\n"
        "v_and_b32 v145, s35, v134\n"
        "v_mfma_f32_16x16x32_bf16 v[56:59], v[216:219], a[220:223], v[56:59]\n"
        "ds_read_b128 a[28:31], v254 offset:46080\n"
        "v_lshlrev_b32 v146, 16, v135\n"
        "v_mfma_f32_16x16x32_bf16 v[56:59], v[208:211], a[216:219], v[56:59]\n"
        "ds_read_b128 a[192:195], v254 offset:34816\n"
        "v_and_b32 v147, s35, v135\n"
        "v_mfma_f32_16x16x32_bf16 v[56:59], v[200:203], a[212:215], v[56:59]\n"
        "ds_read_b128 a[196:199], v254 offset:38912\n"
        "v_mul_f32 v76, v76, v144\n"
        "v_mfma_f32_16x16x32_bf16 v[56:59], v[192:195], a[208:211], v[56:59]\n"
        "ds_read_b128 a[200:203], v254 offset:43008\n"
        "s_waitcnt lgkmcnt(8)\n"
        "v_mfma_f32_16x16x32_bf16 v[32:35], v[112:115], a[0:3], v[32:35]\n"
        "ds_read_b128 a[204:207], v254 offset:47104\n"
        "v_mul_f32 v77, v77, v145\n"
        "v_mfma_f32_16x16x32_bf16 v[32:35], v[120:123], a[4:7], v[32:35]\n"
        "ds_read_b128 a[208:211], v254 offset:35840\n"
        "v_mul_f32 v78, v78, v146\n"
        "v_mfma_f32_16x16x32_bf16 v[32:35], a[64:67], a[8:11], v[32:35]\n"
        "ds_read_b128 a[212:215], v254 offset:39936\n"
        "s_waitcnt lgkmcnt(8)\n"
        "v_mfma_f32_16x16x32_bf16 v[32:35], a[72:75], a[12:15], v[32:35]\n"
        "ds_read_b128 a[216:219], v254 offset:44032\n"
        "v_mul_f32 v79, v79, v147\n"
        "v_mfma_f32_16x16x32_bf16 v[36:39], a[76:79], a[12:15], v[36:39]\n"
        "ds_read_b128 a[220:223], v254 offset:48128\n"
        "v_cvt_pk_bf16_f32 v76, 0, v76\n"
        "v_mfma_f32_16x16x32_bf16 v[36:39], a[68:71], a[8:11], v[36:39]\n"
        "s_waitcnt vmcnt(0) lgkmcnt(7)\n"
        "v_cmp_ne_u64 vcc, v[156:157], v[250:251]\n"
        "v_cvt_pk_bf16_f32 v77, 0, v77\n"
        "v_mfma_f32_16x16x32_bf16 v[36:39], v[124:127], a[4:7], v[36:39]\n"
        "s_cbranch_vccnz LPPR_%=\n"
        "LBPR_%=:\n"
        "global_load_dwordx4 a[240:243], v254, s[30:31] offset:0 sc1\n"
        "v_mfma_f32_16x16x32_bf16 v[36:39], v[116:119], a[0:3], v[36:39]\n"
        "v_cvt_pk_bf16_f32 v78, 0, v78\n"
        "v_cvt_pk_bf16_f32 v79, 0, v79\n"
        "v_mfma_f32_16x16x32_bf16 v[44:47], v[116:119], a[16:19], v[44:47]\n"
        "global_load_dwordx4 a[244:247], v254, s[30:31] offset:1024 sc1\n"
        "v_mfma_f32_16x16x32_bf16 v[44:47], v[124:127], a[20:23], v[44:47]\n"
        "v_mul_f32 v76, v76, v229\n"
        "v_mul_f32 v77, v77, v229\n"
        "v_mfma_f32_16x16x32_bf16 v[44:47], a[68:71], a[24:27], v[44:47]\n"
        "global_load_dwordx4 a[248:251], v254, s[30:31] offset:2048 sc1\n"
        "v_mfma_f32_16x16x32_bf16 v[44:47], a[76:79], a[28:31], v[44:47]\n"
        "v_mul_f32 v78, v78, v229\n"
        "v_mul_f32 v79, v79, v229\n"
        "v_mfma_f32_16x16x32_bf16 v[40:43], a[72:75], a[28:31], v[40:43]\n"
        "global_load_dwordx4 a[252:255], v254, s[30:31] offset:3072 sc1\n"
        "v_mfma_f32_16x16x32_bf16 v[40:43], a[64:67], a[24:27], v[40:43]\n"
        "s_add_u32 s30, s38, s16\n"
        "s_addc_u32 s31, s39, 0\n"
        "s_mul_i32 s16, s13, 263168\n"
        "v_mfma_f32_16x16x32_bf16 v[40:43], v[120:123], a[20:23], v[40:43]\n"
        "global_load_dwordx4 v[0:3], v254, s[30:31] offset:0 sc1\n"
        "s_add_u32 s16, s16, s15\n"
        "v_mfma_f32_16x16x32_bf16 v[40:43], v[112:115], a[16:19], v[40:43]\n"
        "s_add_u32 s16, s16, s73\n"
        "s_add_u32 s16, s16, 0x4000000\n"
        "v_mfma_f32_16x16x32_bf16 v[48:51], v[112:115], a[192:195], v[48:51]\n"
        "s_waitcnt lgkmcnt(0)\n"
        "global_load_dwordx4 v[4:7], v254, s[30:31] offset:1024 sc1\n"
        "v_mfma_f32_16x16x32_bf16 v[48:51], v[120:123], a[196:199], v[48:51]\n"
        "s_barrier\n"
        "v_mfma_f32_16x16x32_bf16 v[48:51], a[64:67], a[200:203], v[48:51]\n"
        "ds_read_b128 a[0:3], v254 offset:49152\n"
        "global_load_dwordx4 v[8:11], v254, s[30:31] offset:2048 sc1\n"
        "v_mfma_f32_16x16x32_bf16 v[48:51], a[72:75], a[204:207], v[48:51]\n"
        "ds_read_b128 a[4:7], v254 offset:53248\n"
        "v_cvt_pk_bf16_f32 v76, 0, v76\n"
        "v_mfma_f32_16x16x32_bf16 v[52:55], a[76:79], a[204:207], v[52:55]\n"
        "ds_read_b128 a[8:11], v254 offset:57344\n"
        "global_load_dwordx4 v[12:15], v254, s[30:31] offset:3072 sc1\n"
        "v_mfma_f32_16x16x32_bf16 v[52:55], a[68:71], a[200:203], v[52:55]\n"
        "ds_read_b128 a[12:15], v254 offset:61440\n"
        "LHB3_%=:\n"
        "s_cbranch_execz LHRB_%=\n"
        "s_add_u32 s30, s38, s16\n"
        "s_addc_u32 s31, s39, 0\n"
        "v_mfma_f32_16x16x32_bf16 v[52:55], v[124:127], a[196:199], v[52:55]\n"
        "ds_read_b128 a[16:19], v254 offset:50176\n"
        "global_load_dwordx4 v[16:19], v254, s[30:31] offset:0 sc1\n"
        "v_mfma_f32_16x16x32_bf16 v[52:55], v[116:119], a[192:195], v[52:55]\n"
        "ds_read_b128 a[20:23], v254 offset:54272\n"
        "v_cvt_pk_bf16_f32 v77, 0, v77\n"
        "v_mfma_f32_16x16x32_bf16 v[60:63], v[116:119], a[208:211], v[60:63]\n"
        "ds_read_b128 a[24:27], v254 offset:58368\n"
        "global_load_dwordx4 v[20:23], v254, s[30:31] offset:1024 sc1\n"
        "v_mfma_f32_16x16x32_bf16 v[60:63], v[124:127], a[212:215], v[60:63]\n"
        "ds_read_b128 a[28:31], v254 offset:62464\n"
        "v_cvt_pk_bf16_f32 v78, 0, v78\n"
        "v_mfma_f32_16x16x32_bf16 v[60:63], a[68:71], a[216:219], v[60:63]\n"
        "ds_read_b128 a[192:195], v254 offset:51200\n"
        "global_load_dwordx4 v[24:27], v254, s[30:31] offset:2048 sc1\n"
        "v_mfma_f32_16x16x32_bf16 v[60:63], a[76:79], a[220:223], v[60:63]\n"
        "ds_read_b128 a[196:199], v254 offset:55296\n"
        "v_cvt_pk_bf16_f32 v79, 0, v79\n"
        "v_mfma_f32_16x16x32_bf16 v[56:59], a[72:75], a[220:223], v[56:59]\n"
        "ds_read_b128 a[200:203], v254 offset:59392\n"
        "global_load_dwordx4 v[28:31], v254, s[30:31] offset:3072 sc1\n"
        "v_mfma_f32_16x16x32_bf16 v[56:59], a[64:67], a[216:219], v[56:59]\n"
        "ds_read_b128 a[204:207], v254 offset:63488\n"
        "v_lshlrev_b32 v246, 16, v136\n"
        "v_mfma_f32_16x16x32_bf16 v[56:59], v[120:123], a[212:215], v[56:59]\n"
        "ds_read_b128 a[212:215], v254 offset:56320\n"
        "v_and_b32 v247, s35, v136\n"
        "v_mfma_f32_16x16x32_bf16 v[56:59], v[112:115], a[208:211], v[56:59]\n"
        "ds_read_b128 a[208:211], v254 offset:52224\n"
        "s_waitcnt lgkmcnt(8)\n"
        "v_mfma_f32_16x16x32_bf16 v[32:35], a[80:83], a[0:3], v[32:35]\n"
        "ds_read_b128 a[216:219], v254 offset:60416\n"
        "v_lshlrev_b32 v248, 16, v137\n"
        "v_mfma_f32_16x16x32_bf16 v[32:35], a[88:91], a[4:7], v[32:35]\n"
        "ds_read_b128 a[220:223], v254 offset:64512\n"
        "v_and_b32 v249, s35, v137\n"
        "v_mfma_f32_16x16x32_bf16 v[32:35], a[160:163], a[8:11], v[32:35]\n"
        "v_mul_f32 v80, v80, v246\n"
        "v_mul_f32 v81, v81, v247\n"
        "v_mfma_f32_16x16x32_bf16 v[32:35], a[168:171], a[12:15], v[32:35]\n"
        "v_mul_f32 v82, v82, v248\n"
        "v_mul_f32 v83, v83, v249\n"
        "v_mfma_f32_16x16x32_bf16 v[36:39], a[172:175], a[12:15], v[36:39]\n"
        "v_cvt_pk_bf16_f32 v80, 0, v80\n"
        "v_cvt_pk_bf16_f32 v81, 0, v81\n"
        "v_mfma_f32_16x16x32_bf16 v[36:39], a[164:167], a[8:11], v[36:39]\n"
        "v_cvt_pk_bf16_f32 v82, 0, v82\n"
        "v_cvt_pk_bf16_f32 v83, 0, v83\n"
        "v_mfma_f32_16x16x32_bf16 v[36:39], a[92:95], a[4:7], v[36:39]\n"
        "s_waitcnt lgkmcnt(3)\n"
        "v_mul_f32 v80, v80, v230\n"
        "v_mul_f32 v81, v81, v230\n"
        "v_mfma_f32_16x16x32_bf16 v[36:39], a[84:87], a[0:3], v[36:39]\n"
        "v_mul_f32 v82, v82, v230\n"
        "v_mul_f32 v83, v83, v230\n"
        "v_mfma_f32_16x16x32_bf16 v[44:47], a[84:87], a[16:19], v[44:47]\n"
        "v_cvt_pk_bf16_f32 v80, 0, v80\n"
        "v_cvt_pk_bf16_f32 v81, 0, v81\n"
        "v_mfma_f32_16x16x32_bf16 v[44:47], a[92:95], a[20:23], v[44:47]\n"
        "v_cvt_pk_bf16_f32 v82, 0, v82\n"
        "v_cvt_pk_bf16_f32 v83, 0, v83\n"
        "v_mfma_f32_16x16x32_bf16 v[44:47], a[164:167], a[24:27], v[44:47]\n"
        "v_lshlrev_b32 v144, 16, v138\n"
        "v_and_b32 v145, s35, v138\n"
        "v_mfma_f32_16x16x32_bf16 v[44:47], a[172:175], a[28:31], v[44:47]\n"
        "v_lshlrev_b32 v146, 16, v139\n"
        "v_and_b32 v147, s35, v139\n"
        "v_mfma_f32_16x16x32_bf16 v[40:43], a[168:171], a[28:31], v[40:43]\n"
        "v_mul_f32 v84, v84, v144\n"
        "v_mul_f32 v85, v85, v145\n"
        "v_mfma_f32_16x16x32_bf16 v[40:43], a[160:163], a[24:27], v[40:43]\n"
        "v_mul_f32 v86, v86, v146\n"
        "v_mul_f32 v87, v87, v147\n"
        "v_mfma_f32_16x16x32_bf16 v[40:43], a[88:91], a[20:23], v[40:43]\n"
        "v_cvt_pk_bf16_f32 v84, 0, v84\n"
        "v_cvt_pk_bf16_f32 v85, 0, v85\n"
        "v_mfma_f32_16x16x32_bf16 v[40:43], a[80:83], a[16:19], v[40:43]\n"
        "v_cvt_pk_bf16_f32 v86, 0, v86\n"
        "v_cvt_pk_bf16_f32 v87, 0, v87\n"
        "v_mfma_f32_16x16x32_bf16 v[48:51], a[80:83], a[192:195], v[48:51]\n"
        "s_barrier\n"
        "v_mfma_f32_16x16x32_bf16 v[48:51], a[88:91], a[196:199], v[48:51]\n"
        "ds_read_b128 a[0:3], v244 offset:32768\n"
        "v_mul_f32 v84, v84, v230\n"
        "v_mfma_f32_16x16x32_bf16 v[48:51], a[160:163], a[200:203], v[48:51]\n"
        "ds_read_b128 a[4:7], v244 offset:36864\n"
        "v_mul_f32 v85, v85, v230\n"
        "v_mfma_f32_16x16x32_bf16 v[48:51], a[168:171], a[204:207], v[48:51]\n"
        "ds_read_b128 a[8:11], v244 offset:40960\n"
        "v_mul_f32 v86, v86, v230\n"
        "v_mfma_f32_16x16x32_bf16 v[52:55], a[172:175], a[204:207], v[52:55]\n"
        "ds_read_b128 a[12:15], v244 offset:45056\n"
        "v_mul_f32 v87, v87, v230\n"
        "v_mfma_f32_16x16x32_bf16 v[52:55], a[164:167], a[200:203], v[52:55]\n"
        "ds_read_b128 a[200:203], v244 offset:41984\n"
        "v_cvt_pk_bf16_f32 v84, 0, v84\n"
        "v_mfma_f32_16x16x32_bf16 v[52:55], a[92:95], a[196:199], v[52:55]\n"
        "ds_read_b128 a[196:199], v244 offset:37888\n"
        "v_cvt_pk_bf16_f32 v85, 0, v85\n"
        "v_mfma_f32_16x16x32_bf16 v[52:55], a[84:87], a[192:195], v[52:55]\n"
        "ds_read_b128 a[192:195], v244 offset:33792\n"
        "s_waitcnt lgkmcnt(7)\n"
        "v_mfma_f32_16x16x32_bf16 v[60:63], a[84:87], a[208:211], v[60:63]\n"
        "ds_read_b128 a[204:207], v244 offset:46080\n"
        "v_mfma_f32_16x16x32_bf16 v[60:63], a[92:95], a[212:215], v[60:63]\n"
        "s_barrier\n"
        "v_mfma_f32_16x16x32_bf16 v[60:63], a[164:167], a[216:219], v[60:63]\n"
        "ds_read_b128 a[16:19], v244 offset:49152\n"
        "v_cvt_pk_bf16_f32 v86, 0, v86\n"
        "v_mfma_f32_16x16x32_bf16 v[60:63], a[172:175], a[220:223], v[60:63]\n"
        "ds_read_b128 a[20:23], v244 offset:53248\n"
        "v_cvt_pk_bf16_f32 v87, 0, v87\n"
        "v_mfma_f32_16x16x32_bf16 v[56:59], a[168:171], a[220:223], v[56:59]\n"
        "ds_read_b128 a[24:27], v244 offset:57344\n"
        "v_lshlrev_b32 v88, 16, v108\n"
        "v_mfma_f32_16x16x32_bf16 v[56:59], a[160:163], a[216:219], v[56:59]\n"
        "ds_read_b128 a[28:31], v244 offset:61440\n"
        "v_and_b32 v89, s35, v108\n"
        "v_mfma_f32_16x16x32_bf16 v[56:59], a[88:91], a[212:215], v[56:59]\n"
        "ds_read_b128 a[212:215], v244 offset:54272\n"
        "v_lshlrev_b32 v90, 16, v109\n"
        "v_mfma_f32_16x16x32_bf16 v[56:59], a[80:83], a[208:211], v[56:59]\n"
        "ds_read_b128 a[208:211], v244 offset:50176\n"
        "s_waitcnt lgkmcnt(7)\n"
        "v_mfma_f32_16x16x32_bf16 v[32:35], a[176:179], a[0:3], v[32:35]\n"
        "ds_read_b128 a[216:219], v244 offset:58368\n"
        "v_and_b32 v91, s35, v109\n"
        "v_mfma_f32_16x16x32_bf16 v[32:35], a[184:187], a[4:7], v[32:35]\n"
        "ds_read_b128 a[220:223], v244 offset:62464\n"
        "v_lshlrev_b32 v246, 16, v140\n"
        "v_mfma_f32_16x16x32_bf16 v[32:35], a[32:35], a[8:11], v[32:35]\n"
        "v_and_b32 v247, s35, v140\n"
        "v_lshlrev_b32 v248, 16, v141\n"
        "v_mfma_f32_16x16x32_bf16 v[32:35], a[40:43], a[12:15], v[32:35]\n"
        "v_and_b32 v249, s35, v141\n"
        "v_mul_f32 v88, v88, v246\n"
        "v_mfma_f32_16x16x32_bf16 v[36:39], a[44:47], a[12:15], v[36:39]\n"
        "ds_read_b128 a[12:15], v244 offset:47104\n"
        "v_mul_f32 v89, v89, v247\n"
        "v_mfma_f32_16x16x32_bf16 v[36:39], a[36:39], a[8:11], v[36:39]\n"
        "ds_read_b128 a[8:11], v244 offset:43008\n"
        "v_mul_f32 v90, v90, v248\n"
        "v_mfma_f32_16x16x32_bf16 v[36:39], a[188:191], a[4:7], v[36:39]\n"
        "ds_read_b128 a[4:7], v244 offset:38912\n"
        "v_mul_f32 v91, v91, v249\n"
        "v_mfma_f32_16x16x32_bf16 v[36:39], a[180:183], a[0:3], v[36:39]\n"
        "ds_read_b128 a[0:3], v244 offset:34816\n"
        "s_waitcnt lgkmcnt(6)\n"
        "v_mfma_f32_16x16x32_bf16 v[32:35], a[48:51], a[16:19], v[32:35]\n"
        "v_cvt_pk_bf16_f32 v88, 0, v88\n"
        "v_cvt_pk_bf16_f32 v89, 0, v89\n"
        "v_mfma_f32_16x16x32_bf16 v[32:35], a[56:59], a[20:23], v[32:35]\n"
        "v_cvt_pk_bf16_f32 v90, 0, v90\n"
        "v_cvt_pk_bf16_f32 v91, 0, v91\n"
        "v_mfma_f32_16x16x32_bf16 v[32:35], a[224:227], a[24:27], v[32:35]\n"
        "v_mul_f32 v88, v88, v231\n"
        "v_mul_f32 v89, v89, v231\n"
        "v_mfma_f32_16x16x32_bf16 v[32:35], a[232:235], a[28:31], v[32:35]\n"
        "v_mul_f32 v90, v90, v231\n"
        "v_mul_f32 v91, v91, v231\n"
        "v_mfma_f32_16x16x32_bf16 v[36:39], a[236:239], a[28:31], v[36:39]\n"
        "ds_read_b128 a[28:31], v244 offset:63488\n"
        "v_cvt_pk_bf16_f32 v88, 0, v88\n"
        "v_mfma_f32_16x16x32_bf16 v[36:39], a[228:231], a[24:27], v[36:39]\n"
        "ds_read_b128 a[24:27], v244 offset:59392\n"
        "v_cvt_pk_bf16_f32 v89, 0, v89\n"
        "v_mfma_f32_16x16x32_bf16 v[36:39], a[60:63], a[20:23], v[36:39]\n"
        "ds_read_b128 a[20:23], v244 offset:55296\n"
        "v_cvt_pk_bf16_f32 v90, 0, v90\n"
        "v_mfma_f32_16x16x32_bf16 v[36:39], a[52:55], a[16:19], v[36:39]\n"
        "ds_read_b128 a[16:19], v244 offset:51200\n"
        "s_waitcnt vmcnt(11) lgkmcnt(4)\n"
        "v_mfma_f32_16x16x32_bf16 v[32:35], v[240:243], a[240:243], v[32:35]\n"
        "v_cvt_pk_bf16_f32 v91, 0, v91\n"
        "v_lshlrev_b32 v92, 16, v110\n"
        "v_mfma_f32_16x16x32_bf16 v[36:39], v[238:241], a[240:243], v[36:39]\n"
        "s_waitcnt vmcnt(7)\n"
        "v_and_b32 v93, s35, v110\n"
        "v_lshlrev_b32 v94, 16, v111\n"
        "v_mfma_f32_16x16x32_bf16 v[32:35], v[240:243], v[0:3], v[32:35]\n"
        "v_and_b32 v95, s35, v111\n"
        "v_lshlrev_b32 v144, 16, v142\n"
        "v_mfma_f32_16x16x32_bf16 v[36:39], v[238:241], v[0:3], v[36:39]\n"
        "v_and_b32 v145, s35, v142\n"
        "v_lshlrev_b32 v146, 16, v143\n"
        "v_mfma_f32_16x16x32_bf16 v[40:43], a[176:179], a[192:195], v[40:43]\n"
        "v_and_b32 v147, s35, v143\n"
        "s_waitcnt vmcnt(3) lgkmcnt(3)\n"
        "v_mfma_f32_16x16x32_bf16 v[32:35], v[240:243], v[16:19], v[32:35]\n"
        "v_mul_f32 v92, v92, v144\n"
        "v_mul_f32 v93, v93, v145\n"
        "v_mfma_f32_16x16x32_bf16 v[36:39], v[238:241], v[16:19], v[36:39]\n"
        "v_mul_f32 v94, v94, v146\n"
        "v_mul_f32 v95, v95, v147\n"
        "v_mfma_f32_16x16x32_bf16 v[40:43], a[184:187], a[196:199], v[40:43]\n"
        "v_cvt_pk_bf16_f32 v92, 0, v92\n"
        "v_cvt_pk_bf16_f32 v93, 0, v93\n"
        "v_mfma_f32_16x16x32_bf16 v[40:43], a[32:35], a[200:203], v[40:43]\n"
        "v_fmac_f32 v64, v32, v224\n"
        "v_fmac_f32 v65, v33, v224\n"
        "v_mfma_f32_16x16x32_bf16 v[40:43], a[40:43], a[204:207], v[40:43]\n"
        "v_fmac_f32 v66, v34, v224\n"
        "v_fmac_f32 v67, v35, v224\n"
        "v_mfma_f32_16x16x32_bf16 v[44:47], a[44:47], a[204:207], v[44:47]\n"
        "ds_read_b128 a[204:207], v244 offset:48128\n"
        "v_fmac_f32 v68, v36, v224\n"
        "v_mfma_f32_16x16x32_bf16 v[44:47], a[36:39], a[200:203], v[44:47]\n"
        "ds_read_b128 a[200:203], v244 offset:44032\n"
        "v_fmac_f32 v69, v37, v224\n"
        "v_mfma_f32_16x16x32_bf16 v[44:47], a[188:191], a[196:199], v[44:47]\n"
        "ds_read_b128 a[196:199], v244 offset:39936\n"
        "v_fmac_f32 v70, v38, v224\n"
        "v_mfma_f32_16x16x32_bf16 v[44:47], a[180:183], a[192:195], v[44:47]\n"
        "ds_read_b128 a[192:195], v244 offset:35840\n"
        "v_fmac_f32 v71, v39, v224\n"
        "v_mfma_f32_16x16x32_bf16 v[40:43], a[48:51], a[208:211], v[40:43]\n"
        "v_cvt_pk_bf16_f32 v64, v64, v65\n"
        "v_cvt_pk_bf16_f32 v65, v66, v67\n"
        "v_mfma_f32_16x16x32_bf16 v[40:43], a[56:59], a[212:215], v[40:43]\n"
        "v_cvt_pk_bf16_f32 v66, v68, v69\n"
        "v_cvt_pk_bf16_f32 v67, v70, v71\n"
        "v_mfma_f32_16x16x32_bf16 v[40:43], a[224:227], a[216:219], v[40:43]\n"
        "v_permlane16_swap_b32 v64, v66\n"
        "v_mfma_f32_16x16x32_bf16 v[40:43], a[232:235], a[220:223], v[40:43]\n"
        "v_permlane16_swap_b32 v65, v67\n"
        "v_mfma_f32_16x16x32_bf16 v[44:47], a[236:239], a[220:223], v[44:47]\n"
        "global_store_dwordx4 v151, v[64:67], s[56:57] sc0 sc1\n"
        "ds_read_b128 a[220:223], v244 offset:64512\n"
        "s_add_u32 s56, s56, 0x8000\n"
        "s_addc_u32 s57, s57, 0\n"
        "v_cvt_pk_bf16_f32 v94, 0, v94\n"
        "v_cvt_pk_bf16_f32 v95, 0, v95\n"
        "v_mfma_f32_16x16x32_bf16 v[44:47], a[228:231], a[216:219], v[44:47]\n"
        "ds_read_b128 a[216:219], v244 offset:60416\n"
        "v_mul_f32 v92, v92, v231\n"
        "v_mfma_f32_16x16x32_bf16 v[44:47], a[60:63], a[212:215], v[44:47]\n"
        "ds_read_b128 a[212:215], v244 offset:56320\n"
        "v_mul_f32 v93, v93, v231\n"
        "v_mfma_f32_16x16x32_bf16 v[44:47], a[52:55], a[208:211], v[44:47]\n"
        "ds_read_b128 a[208:211], v244 offset:52224\n"
        "v_mul_f32 v94, v94, v231\n"
        "v_mfma_f32_16x16x32_bf16 v[40:43], v[240:243], a[244:247], v[40:43]\n"
        "v_mul_f32 v95, v95, v231\n"
        "v_cvt_pk_bf16_f32 v92, 0, v92\n"
        "v_mfma_f32_16x16x32_bf16 v[44:47], v[238:241], a[244:247], v[44:47]\n"
        "v_cvt_pk_bf16_f32 v93, 0, v93\n"
        "v_cvt_pk_bf16_f32 v94, 0, v94\n"
        "v_mfma_f32_16x16x32_bf16 v[40:43], v[240:243], v[4:7], v[40:43]\n"
        "v_cvt_pk_bf16_f32 v95, 0, v95\n"
        "v_mov_b32 v144, 0\n"
        "v_mfma_f32_16x16x32_bf16 v[44:47], v[238:241], v[4:7], v[44:47]\n"
        "s_waitcnt vmcnt(3) lgkmcnt(4)\n"
        "v_mfma_f32_16x16x32_bf16 v[40:43], v[240:243], v[20:23], v[40:43]\n"
        "v_mfma_f32_16x16x32_bf16 v[44:47], v[238:241], v[20:23], v[44:47]\n"
        "v_mfma_f32_16x16x32_bf16 v[48:51], a[176:179], a[0:3], v[48:51]\n"
        "v_mfma_f32_16x16x32_bf16 v[48:51], a[184:187], a[4:7], v[48:51]\n"
        "v_mfma_f32_16x16x32_bf16 v[48:51], a[32:35], a[8:11], v[48:51]\n"
        "v_mfma_f32_16x16x32_bf16 v[48:51], a[40:43], a[12:15], v[48:51]\n"
        "v_mfma_f32_16x16x32_bf16 v[52:55], a[44:47], a[12:15], v[52:55]\n"
        "v_mfma_f32_16x16x32_bf16 v[52:55], a[36:39], a[8:11], v[52:55]\n"
        "v_mfma_f32_16x16x32_bf16 v[52:55], a[188:191], a[4:7], v[52:55]\n"
        "v_fmac_f32 v72, v40, v225\n"
        "v_fmac_f32 v73, v41, v225\n"
        "v_mfma_f32_16x16x32_bf16 v[52:55], a[180:183], a[0:3], v[52:55]\n"
        "v_fmac_f32 v74, v42, v225\n"
        "v_fmac_f32 v75, v43, v225\n"
        "v_mfma_f32_16x16x32_bf16 v[48:51], a[48:51], a[16:19], v[48:51]\n"
        "v_fmac_f32 v76, v44, v225\n"
        "v_fmac_f32 v77, v45, v225\n"
        "v_mfma_f32_16x16x32_bf16 v[48:51], a[56:59], a[20:23], v[48:51]\n"
        "v_fmac_f32 v78, v46, v225\n"
        "v_fmac_f32 v79, v47, v225\n"
        "v_mfma_f32_16x16x32_bf16 v[48:51], a[224:227], a[24:27], v[48:51]\n"
        "v_cvt_pk_bf16_f32 v72, v72, v73\n"
        "v_cvt_pk_bf16_f32 v73, v74, v75\n"
        "v_mfma_f32_16x16x32_bf16 v[48:51], a[232:235], a[28:31], v[48:51]\n"
        "v_cvt_pk_bf16_f32 v74, v76, v77\n"
        "v_cvt_pk_bf16_f32 v75, v78, v79\n"
        "v_mfma_f32_16x16x32_bf16 v[52:55], a[236:239], a[28:31], v[52:55]\n"
        "v_permlane16_swap_b32 v72, v74\n"
        "v_mfma_f32_16x16x32_bf16 v[52:55], a[228:231], a[24:27], v[52:55]\n"
        "v_permlane16_swap_b32 v73, v75\n"
        "v_mfma_f32_16x16x32_bf16 v[52:55], a[60:63], a[20:23], v[52:55]\n"
        "global_store_dwordx4 v151, v[72:75], s[56:57] sc0 sc1\n"
        "s_add_u32 s56, s56, 0x8000\n"
        "s_addc_u32 s57, s57, 0\n"
        "v_mfma_f32_16x16x32_bf16 v[52:55], a[52:55], a[16:19], v[52:55]\n"
        "v_mfma_f32_16x16x32_bf16 v[48:51], v[240:243], a[248:251], v[48:51]\n"
        "v_mfma_f32_16x16x32_bf16 v[52:55], v[238:241], a[248:251], v[52:55]\n"
        "v_mfma_f32_16x16x32_bf16 v[48:51], v[240:243], v[8:11], v[48:51]\n"
        "v_mfma_f32_16x16x32_bf16 v[52:55], v[238:241], v[8:11], v[52:55]\n"
        "s_waitcnt vmcnt(3) lgkmcnt(0)\n"
        "v_mfma_f32_16x16x32_bf16 v[48:51], v[240:243], v[24:27], v[48:51]\n"
        "v_mfma_f32_16x16x32_bf16 v[52:55], v[238:241], v[24:27], v[52:55]\n"
        "v_mfma_f32_16x16x32_bf16 v[56:59], a[176:179], a[192:195], v[56:59]\n"
        "v_mfma_f32_16x16x32_bf16 v[56:59], a[184:187], a[196:199], v[56:59]\n"
        "v_mfma_f32_16x16x32_bf16 v[56:59], a[32:35], a[200:203], v[56:59]\n"
        "v_mfma_f32_16x16x32_bf16 v[56:59], a[40:43], a[204:207], v[56:59]\n"
        "v_mfma_f32_16x16x32_bf16 v[60:63], a[44:47], a[204:207], v[60:63]\n"
        "v_mfma_f32_16x16x32_bf16 v[60:63], a[36:39], a[200:203], v[60:63]\n"
        "v_mfma_f32_16x16x32_bf16 v[60:63], a[188:191], a[196:199], v[60:63]\n"
        "v_fmac_f32 v80, v48, v226\n"
        "v_fmac_f32 v81, v49, v226\n"
        "v_mfma_f32_16x16x32_bf16 v[60:63], a[180:183], a[192:195], v[60:63]\n"
        "v_fmac_f32 v82, v50, v226\n"
        "v_fmac_f32 v83, v51, v226\n"
        "v_mfma_f32_16x16x32_bf16 v[56:59], a[48:51], a[208:211], v[56:59]\n"
        "v_fmac_f32 v84, v52, v226\n"
        "v_fmac_f32 v85, v53, v226\n"
        "v_mfma_f32_16x16x32_bf16 v[56:59], a[56:59], a[212:215], v[56:59]\n"
        "v_fmac_f32 v86, v54, v226\n"
        "v_fmac_f32 v87, v55, v226\n"
        "v_mfma_f32_16x16x32_bf16 v[56:59], a[224:227], a[216:219], v[56:59]\n"
        "v_cvt_pk_bf16_f32 v80, v80, v81\n"
        "v_cvt_pk_bf16_f32 v81, v82, v83\n"
        "v_mfma_f32_16x16x32_bf16 v[56:59], a[232:235], a[220:223], v[56:59]\n"
        "v_cvt_pk_bf16_f32 v82, v84, v85\n"
        "v_cvt_pk_bf16_f32 v83, v86, v87\n"
        "v_mfma_f32_16x16x32_bf16 v[60:63], a[236:239], a[220:223], v[60:63]\n"
        "v_permlane16_swap_b32 v80, v82\n"
        "v_mfma_f32_16x16x32_bf16 v[60:63], a[228:231], a[216:219], v[60:63]\n"
        "v_permlane16_swap_b32 v81, v83\n"
        "v_mfma_f32_16x16x32_bf16 v[60:63], a[60:63], a[212:215], v[60:63]\n"
        "global_store_dwordx4 v151, v[80:83], s[56:57] sc0 sc1\n"
        "s_add_u32 s56, s56, 0x8000\n"
        "s_addc_u32 s57, s57, 0\n"
        "v_mfma_f32_16x16x32_bf16 v[60:63], a[52:55], a[208:211], v[60:63]\n"
        "v_mfma_f32_16x16x32_bf16 v[56:59], v[240:243], a[252:255], v[56:59]\n"
        "v_mfma_f32_16x16x32_bf16 v[60:63], v[238:241], a[252:255], v[60:63]\n"
        "v_mfma_f32_16x16x32_bf16 v[56:59], v[240:243], v[12:15], v[56:59]\n"
        "v_mfma_f32_16x16x32_bf16 v[60:63], v[238:241], v[12:15], v[60:63]\n"
        "s_waitcnt vmcnt(3)\n"
        "v_mfma_f32_16x16x32_bf16 v[56:59], v[240:243], v[28:31], v[56:59]\n"
        "v_mfma_f32_16x16x32_bf16 v[60:63], v[238:241], v[28:31], v[60:63]\n"
        "s_nop 6\n"
        "v_fmac_f32 v88, v56, v227\n"
        "v_fmac_f32 v89, v57, v227\n"
        "v_fmac_f32 v90, v58, v227\n"
        "v_fmac_f32 v91, v59, v227\n"
        "v_fmac_f32 v92, v60, v227\n"
        "v_fmac_f32 v93, v61, v227\n"
        "v_fmac_f32 v94, v62, v227\n"
        "v_fmac_f32 v95, v63, v227\n"
        "v_cvt_pk_bf16_f32 v88, v88, v89\n"
        "v_cvt_pk_bf16_f32 v89, v90, v91\n"
        "v_cvt_pk_bf16_f32 v90, v92, v93\n"
        "v_cvt_pk_bf16_f32 v91, v94, v95\n"
        "s_nop 0\n"
        "v_permlane16_swap_b32 v88, v90\n"
        "v_permlane16_swap_b32 v89, v91\n"
        "global_store_dwordx4 v151, v[88:91], s[56:57] sc0 sc1\n"
        "s_or_b32 s97, s55, s40\n"
        "s_cbranch_scc1 LEB_%=\n"
        "s_mov_b64 exec, 1\n"
        "global_store_dwordx2 v144, v[250:251], s[48:49] sc1\n"
        "s_mov_b64 exec, -1\n"
        "LEB_%=:\n"
        "; ---- END\n"
        "s_endpgm\n"
        "LPKV_%=:\n"
        "s_mov_b32 s97, 0x200000\n"
        "LQKV_%=:\n"
        "global_load_dwordx2 v[252:253], v245, s[48:49] sc1\n"
        "s_waitcnt vmcnt(0)\n"
        "v_cmp_ne_u64 vcc, v[252:253], v[250:251]\n"
        "s_nop 1\n"
        "s_cbranch_vccz LBKV_%=\n"
        "s_sleep 2\n"
        "s_sub_u32 s97, s97, 1\n"
        "s_cmp_lg_u32 s97, 0\n"
        "s_cbranch_scc1 LQKV_%=\n"
        "s_branch LBKV_%=\n"
        "LE4D_%=:\n"
        "s_mov_b32 s97, 0x200000\n"
        "LE5D_%=:\n"
        "s_sleep 2\n"
        "global_load_dwordx2 v[252:253], v155, s[48:49] sc1\n"
        "s_waitcnt vmcnt(0)\n"
        "v_cmp_ne_u64 vcc, v[252:253], v[250:251]\n"
        "s_nop 1\n"
        "s_cbranch_vccz LEWD_%=\n"
        "s_sub_u32 s97, s97, 1\n"
        "s_cmp_lg_u32 s97, 0\n"
        "s_cbranch_scc1 LE5D_%=\n"
        "s_branch LEWD_%=\n"
        "LRL_%=:\n"
        "s_waitcnt vmcnt(0) lgkmcnt(0)\n"
        "s_nop 7\n"
        "v_accvgpr_write_b32 a176, v96\n"
        "v_accvgpr_write_b32 a177, v97\n"
        "v_accvgpr_write_b32 a178, v98\n"
        "v_accvgpr_write_b32 a179, v99\n"
        "v_accvgpr_write_b32 a180, v100\n"
        "v_accvgpr_write_b32 a181, v101\n"
        "v_accvgpr_write_b32 a182, v102\n"
        "v_accvgpr_write_b32 a183, v103\n"
        "v_accvgpr_write_b32 a184, v104\n"
        "v_accvgpr_write_b32 a185, v105\n"
        "v_accvgpr_write_b32 a186, v106\n"
        "v_accvgpr_write_b32 a187, v107\n"
        "v_accvgpr_write_b32 a188, v108\n"
        "v_accvgpr_write_b32 a189, v109\n"
        "v_accvgpr_write_b32 a190, v110\n"
        "v_accvgpr_write_b32 a191, v111\n"
        "s_mov_b32 s53, s44\n"
        "s_mov_b32 s96, 64\n"
        "s_mov_b32 s52, 0\n"
        "s_branch LREDO_%=\n"
        "LRET0_%=:\n"
        "v_accvgpr_write_b32 a176, v128\n"
        "v_accvgpr_write_b32 a177, v129\n"
        "v_accvgpr_write_b32 a178, v130\n"
        "v_accvgpr_write_b32 a179, v131\n"
        "v_accvgpr_write_b32 a180, v132\n"
        "v_accvgpr_write_b32 a181, v133\n"
        "v_accvgpr_write_b32 a182, v134\n"
        "v_accvgpr_write_b32 a183, v135\n"
        "v_accvgpr_write_b32 a184, v136\n"
        "v_accvgpr_write_b32 a185, v137\n"
        "v_accvgpr_write_b32 a186, v138\n"
        "v_accvgpr_write_b32 a187, v139\n"
        "v_accvgpr_write_b32 a188, v140\n"
        "v_accvgpr_write_b32 a189, v141\n"
        "v_accvgpr_write_b32 a190, v142\n"
        "v_accvgpr_write_b32 a191, v143\n"
        "s_mov_b32 s53, s44\n"
        "s_mov_b32 s96, 0\n"
        "s_mov_b32 s52, 1\n"
        "s_branch LREDO_%=\n"
        "LRET1_%=:\n"
        "s_branch LSL_%=\n"
        "LE4S_%=:\n"
        "s_mov_b32 s97, 0x200000\n"
        "LE5S_%=:\n"
        "s_sleep 2\n"
        "global_load_dwordx2 v[252:253], v155, s[48:49] sc1\n"
        "s_waitcnt vmcnt(0)\n"
        "v_cmp_ne_u64 vcc, v[252:253], v[250:251]\n"
        "s_nop 1\n"
        "s_cbranch_vccz LEWS_%=\n"
        "s_sub_u32 s97, s97, 1\n"
        "s_cmp_lg_u32 s97, 0\n"
        "s_cbranch_scc1 LE5S_%=\n"
        "s_branch LEWS_%=\n"
        "LRH_%=:\n"
        "s_waitcnt vmcnt(0) lgkmcnt(0)\n"
        "s_nop 7\n"
        "v_accvgpr_write_b32 a176, v0\n"
        "v_accvgpr_write_b32 a177, v1\n"
        "v_accvgpr_write_b32 a178, v2\n"
        "v_accvgpr_write_b32 a179, v3\n"
        "v_accvgpr_write_b32 a180, v4\n"
        "v_accvgpr_write_b32 a181, v5\n"
        "v_accvgpr_write_b32 a182, v6\n"
        "v_accvgpr_write_b32 a183, v7\n"
        "v_accvgpr_write_b32 a184, v8\n"
        "v_accvgpr_write_b32 a185, v9\n"
        "v_accvgpr_write_b32 a186, v10\n"
        "v_accvgpr_write_b32 a187, v11\n"
        "v_accvgpr_write_b32 a188, v12\n"
        "v_accvgpr_write_b32 a189, v13\n"
        "v_accvgpr_write_b32 a190, v14\n"
        "v_accvgpr_write_b32 a191, v15\n"
        "s_mov_b32 s53, s45\n"
        "s_mov_b32 s96, 64\n"
        "s_mov_b32 s52, 2\n"
        "s_branch LREDO_%=\n"
        "LRET2_%=:\n"
        "v_accvgpr_write_b32 a176, v64\n"
        "v_accvgpr_write_b32 a177, v65\n"
        "v_accvgpr_write_b32 a178, v66\n"
        "v_accvgpr_write_b32 a179, v67\n"
        "v_accvgpr_write_b32 a180, v68\n"
        "v_accvgpr_write_b32 a181, v69\n"
        "v_accvgpr_write_b32 a182, v70\n"
        "v_accvgpr_write_b32 a183, v71\n"
        "v_accvgpr_write_b32 a184, v72\n"
        "v_accvgpr_write_b32 a185, v73\n"
        "v_accvgpr_write_b32 a186, v74\n"
        "v_accvgpr_write_b32 a187, v75\n"
        "v_accvgpr_write_b32 a188, v76\n"
        "v_accvgpr_write_b32 a189, v77\n"
        "v_accvgpr_write_b32 a190, v78\n"
        "v_accvgpr_write_b32 a191, v79\n"
        "s_mov_b32 s53, s45\n"
        "s_mov_b32 s96, 0\n"
        "s_mov_b32 s52, 3\n"
        "s_branch LREDO_%=\n"
        "LRET3_%=:\n"
        "s_branch LSH_%=\n"
        "LPCX_%=:\n"
        "s_mov_b32 s97, 0x200000\n"
        "LQCX_%=:\n"
        "global_load_dwordx2 v[236:237], v159, s[48:49] sc1\n"
        "s_waitcnt vmcnt(0)\n"
        "v_cmp_ne_u64 vcc, v[236:237], v[250:251]\n"
        "s_nop 1\n"
        "s_cbranch_vccz LBCX_%=\n"
        "s_sleep 2\n"
        "s_sub_u32 s97, s97, 1\n"
        "s_cmp_lg_u32 s97, 0\n"
        "s_cbranch_scc1 LQCX_%=\n"
        "s_branch LBCX_%=\n"
        "LPZZ_%=:\n"
        "s_mov_b32 s97, 0x200000\n"
        "LQZZ_%=:\n"
        "global_load_dwordx2 v[236:237], v234, s[48:49] sc1\n"
        "s_waitcnt vmcnt(0)\n"
        "v_cmp_ne_u64 vcc, v[236:237], v[250:251]\n"
        "s_nop 1\n"
        "s_cbranch_vccz LBZZ_%=\n"
        "s_sleep 2\n"
        "s_sub_u32 s97, s97, 1\n"
        "s_cmp_lg_u32 s97, 0\n"
        "s_cbranch_scc1 LQZZ_%=\n"
        "s_branch LBZZ_%=\n"
        "LPPR_%=:\n"
        "s_mov_b32 s97, 0x200000\n"
        "LQPR_%=:\n"
        "global_load_dwordx2 v[156:157], v155, s[48:49] sc1\n"
        "s_waitcnt vmcnt(0)\n"
        "v_cmp_ne_u64 vcc, v[156:157], v[250:251]\n"
        "s_nop 1\n"
        "s_cbranch_vccz LBPR_%=\n"
        "s_sleep 2\n"
        "s_sub_u32 s97, s97, 1\n"
        "s_cmp_lg_u32 s97, 0\n"
        "s_cbranch_scc1 LQPR_%=\n"
        "s_branch LBPR_%=\n"
        "LREDO_%=:\n"
        "s_lshl2_add_u32 s97, s55, s40\n"
        "s_lshl_b32 s97, s97, 12\n"
        "s_add_u32 s97, s97, 0x5100000\n"
        "s_add_u32 s88, s38, s97\n"
        "s_addc_u32 s89, s39, 0\n"
        "global_store_dwordx4 v254, v[144:147], s[88:89] offset:0\n"
        "global_store_dwordx4 v254, v[148:151], s[88:89] offset:1024\n"
        "global_store_dwordx4 v254, v[152:155], s[88:89] offset:2048\n"
        "global_store_dwordx4 v254, v[156:159], s[88:89] offset:3072\n"
        "s_waitcnt vmcnt(0)\n"
        "v_mov_b32 v112, 0\n"
        "v_mov_b32 v113, 0\n"
        "v_mov_b32 v114, 0\n"
        "v_mov_b32 v115, 0\n"
        "v_mov_b32 v116, 0\n"
        "v_mov_b32 v117, 0\n"
        "v_mov_b32 v118, 0\n"
        "v_mov_b32 v119, 0\n"
        "v_mov_b32 v120, 0\n"
        "v_mov_b32 v121, 0\n"
        "v_mov_b32 v122, 0\n"
        "v_mov_b32 v123, 0\n"
        "v_mov_b32 v124, 0\n"
        "v_mov_b32 v125, 0\n"
        "v_mov_b32 v126, 0\n"
        "v_mov_b32 v127, 0\n"
        "v_mov_b32 v144, 0\n"
        "v_mov_b32 v145, 0\n"
        "v_mov_b32 v146, 0\n"
        "v_mov_b32 v147, 0\n"
        "v_mov_b32 v148, 0\n"
        "v_mov_b32 v149, 0\n"
        "v_mov_b32 v150, 0\n"
        "v_mov_b32 v151, 0\n"
        "v_mov_b32 v152, 0\n"
        "v_mov_b32 v153, 0\n"
        "v_mov_b32 v154, 0\n"
        "v_mov_b32 v155, 0\n"
        "v_mov_b32 v156, 0\n"
        "v_mov_b32 v157, 0\n"
        "v_mov_b32 v158, 0\n"
        "v_mov_b32 v159, 0\n"
        "v_mov_b32 v60, 0\n"
        "v_mov_b32 v61, 0\n"
        "v_mov_b32 v62, 0\n"
        "v_mov_b32 v63, 0\n"
        "v_mov_b32 v24, 0xf149f2ca\n"
        "s_lshl_b32 s97, s53, 7\n"
        "s_add_u32 s97, s97, s96\n"
        "v_add_u32 v27, s97, v243\n"
        "s_mul_i32 s97, s43, 529408\n"
        "s_add_u32 s84, s38, s97\n"
        "s_addc_u32 s85, s39, 0\n"
        "s_add_u32 s84, s84, 0x1000\n"
        "s_addc_u32 s85, s85, 0\n"
        "s_add_u32 s86, s84, 0x1100000\n"
        "s_addc_u32 s87, s85, 0\n"
        "s_lshl_b32 s97, s53, 2\n"
        "s_add_u32 s97, s97, 4\n"
        "LRLOOP_%=:\n"
        "global_load_dwordx4 a[160:163], v254, s[84:85] offset:-4096 sc1\n"
        "global_load_dwordx4 a[164:167], v254, s[84:85] offset:-3072 sc1\n"
        "global_load_dwordx4 a[168:171], v254, s[84:85] offset:-2048 sc1\n"
        "global_load_dwordx4 a[172:175], v254, s[84:85] offset:-1024 sc1\n"
        "s_waitcnt vmcnt(0)\n"
        "v_mfma_f32_16x16x32_bf16 v[40:43], a[160:163], a[176:179], 0\n"
        "v_mfma_f32_16x16x32_bf16 v[40:43], a[164:167], a[180:183], v[40:43]\n"
        "v_mfma_f32_16x16x32_bf16 v[40:43], a[168:171], a[184:187], v[40:43]\n"
        "v_mfma_f32_16x16x32_bf16 v[40:43], a[172:175], a[188:191], v[40:43]\n"
        "s_nop 7\n"
        "global_load_dwordx4 a[160:163], v254, s[84:85] offset:0 sc1\n"
        "global_load_dwordx4 a[164:167], v254, s[84:85] offset:1024 sc1\n"
        "global_load_dwordx4 a[168:171], v254, s[84:85] offset:2048 sc1\n"
        "global_load_dwordx4 a[172:175], v254, s[84:85] offset:3072 sc1\n"
        "s_waitcnt vmcnt(0)\n"
        "v_mfma_f32_16x16x32_bf16 v[44:47], a[160:163], a[176:179], 0\n"
        "v_mfma_f32_16x16x32_bf16 v[44:47], a[164:167], a[180:183], v[44:47]\n"
        "v_mfma_f32_16x16x32_bf16 v[44:47], a[168:171], a[184:187], v[44:47]\n"
        "v_mfma_f32_16x16x32_bf16 v[44:47], a[172:175], a[188:191], v[44:47]\n"
        "s_nop 7\n"
        "v_cmp_gt_i32 vcc, 0, v27\n"
        "s_nop 1\n"
        "v_cndmask_b32 v40, v40, v242, vcc\n"
        "v_cmp_gt_i32 vcc, 1, v27\n"
        "s_nop 1\n"
        "v_cndmask_b32 v41, v41, v242, vcc\n"
        "v_cmp_gt_i32 vcc, 2, v27\n"
        "s_nop 1\n"
        "v_cndmask_b32 v42, v42, v242, vcc\n"
        "v_cmp_gt_i32 vcc, 3, v27\n"
        "s_nop 1\n"
        "v_cndmask_b32 v43, v43, v242, vcc\n"
        "v_cmp_gt_i32 vcc, 16, v27\n"
        "s_nop 1\n"
        "v_cndmask_b32 v44, v44, v242, vcc\n"
        "v_cmp_gt_i32 vcc, 17, v27\n"
        "s_nop 1\n"
        "v_cndmask_b32 v45, v45, v242, vcc\n"
        "v_cmp_gt_i32 vcc, 18, v27\n"
        "s_nop 1\n"
        "v_cndmask_b32 v46, v46, v242, vcc\n"
        "v_cmp_gt_i32 vcc, 19, v27\n"
        "s_nop 1\n"
        "v_cndmask_b32 v47, v47, v242, vcc\n"
        "v_max3_f32 v25, v40, v41, v24\n"
        "v_max3_f32 v25, v42, v43, v25\n"
        "v_max3_f32 v25, v44, v45, v25\n"
        "v_max3_f32 v25, v46, v47, v25\n"
        "v_mov_b32 v28, v25\n"
        "s_nop 1\n"
        "v_permlane16_swap_b32 v25, v28\n"
        "v_max_f32 v25, v25, v28\n"
        "v_mov_b32 v28, v25\n"
        "s_nop 1\n"
        "v_permlane32_swap_b32 v25, v28\n"
        "v_max_f32 v25, v25, v28\n"
        "v_sub_f32 v26, v24, v25\n"
        "v_exp_f32 v26, v26\n"
        "v_mov_b32 v24, v25\n"
        "v_sub_f32 v40, v40, v25\n"
        "v_exp_f32 v40, v40\n"
        "v_sub_f32 v41, v41, v25\n"
        "v_exp_f32 v41, v41\n"
        "v_sub_f32 v42, v42, v25\n"
        "v_exp_f32 v42, v42\n"
        "v_sub_f32 v43, v43, v25\n"
        "v_exp_f32 v43, v43\n"
        "v_sub_f32 v44, v44, v25\n"
        "v_exp_f32 v44, v44\n"
        "v_sub_f32 v45, v45, v25\n"
        "v_exp_f32 v45, v45\n"
        "v_sub_f32 v46, v46, v25\n"
        "v_exp_f32 v46, v46\n"
        "v_sub_f32 v47, v47, v25\n"
        "v_exp_f32 v47, v47\n"
        "v_cvt_pk_bf16_f32 v56, v40, v41\n"
        "v_cvt_pk_bf16_f32 v57, v42, v43\n"
        "v_cvt_pk_bf16_f32 v58, v44, v45\n"
        "v_cvt_pk_bf16_f32 v59, v46, v47\n"
        "v_mul_f32 v112, v112, v26\n"
        "v_mul_f32 v113, v113, v26\n"
        "v_mul_f32 v114, v114, v26\n"
        "v_mul_f32 v115, v115, v26\n"
        "v_mul_f32 v116, v116, v26\n"
        "v_mul_f32 v117, v117, v26\n"
        "v_mul_f32 v118, v118, v26\n"
        "v_mul_f32 v119, v119, v26\n"
        "v_mul_f32 v120, v120, v26\n"
        "v_mul_f32 v121, v121, v26\n"
        "v_mul_f32 v122, v122, v26\n"
        "v_mul_f32 v123, v123, v26\n"
        "v_mul_f32 v124, v124, v26\n"
        "v_mul_f32 v125, v125, v26\n"
        "v_mul_f32 v126, v126, v26\n"
        "v_mul_f32 v127, v127, v26\n"
        "v_mul_f32 v144, v144, v26\n"
        "v_mul_f32 v145, v145, v26\n"
        "v_mul_f32 v146, v146, v26\n"
        "v_mul_f32 v147, v147, v26\n"
        "v_mul_f32 v148, v148, v26\n"
        "v_mul_f32 v149, v149, v26\n"
        "v_mul_f32 v150, v150, v26\n"
        "v_mul_f32 v151, v151, v26\n"
        "v_mul_f32 v152, v152, v26\n"
        "v_mul_f32 v153, v153, v26\n"
        "v_mul_f32 v154, v154, v26\n"
        "v_mul_f32 v155, v155, v26\n"
        "v_mul_f32 v156, v156, v26\n"
        "v_mul_f32 v157, v157, v26\n"
        "v_mul_f32 v158, v158, v26\n"
        "v_mul_f32 v159, v159, v26\n"
        "v_mul_f32 v60, v60, v26\n"
        "v_mul_f32 v61, v61, v26\n"
        "v_mul_f32 v62, v62, v26\n"
        "v_mul_f32 v63, v63, v26\n"
        "s_nop 1\n"
        "v_mfma_f32_16x16x32_bf16 v[60:63], a[80:83], v[56:59], v[60:63]\n"
        "global_load_dwordx4 a[160:163], v254, s[86:87] offset:-4096 sc1\n"
        "global_load_dwordx4 a[164:167], v254, s[86:87] offset:-3072 sc1\n"
        "global_load_dwordx4 a[168:171], v254, s[86:87] offset:-2048 sc1\n"
        "global_load_dwordx4 a[172:175], v254, s[86:87] offset:-1024 sc1\n"
        "s_waitcnt vmcnt(0)\n"
        "v_mfma_f32_16x16x32_bf16 v[112:115], a[160:163], v[56:59], v[112:115]\n"
        "v_mfma_f32_16x16x32_bf16 v[116:119], a[164:167], v[56:59], v[116:119]\n"
        "v_mfma_f32_16x16x32_bf16 v[120:123], a[168:171], v[56:59], v[120:123]\n"
        "v_mfma_f32_16x16x32_bf16 v[124:127], a[172:175], v[56:59], v[124:127]\n"
        "s_nop 7\n"
        "global_load_dwordx4 a[160:163], v254, s[86:87] offset:0 sc1\n"
        "global_load_dwordx4 a[164:167], v254, s[86:87] offset:1024 sc1\n"
        "global_load_dwordx4 a[168:171], v254, s[86:87] offset:2048 sc1\n"
        "global_load_dwordx4 a[172:175], v254, s[86:87] offset:3072 sc1\n"
        "s_waitcnt vmcnt(0)\n"
        "v_mfma_f32_16x16x32_bf16 v[144:147], a[160:163], v[56:59], v[144:147]\n"
        "v_mfma_f32_16x16x32_bf16 v[148:151], a[164:167], v[56:59], v[148:151]\n"
        "v_mfma_f32_16x16x32_bf16 v[152:155], a[168:171], v[56:59], v[152:155]\n"
        "v_mfma_f32_16x16x32_bf16 v[156:159], a[172:175], v[56:59], v[156:159]\n"
        "s_nop 7\n"
        "s_add_u32 s84, s84, 0x2000\n"
        "s_addc_u32 s85, s85, 0\n"
        "s_add_u32 s86, s86, 0x2000\n"
        "s_addc_u32 s87, s87, 0\n"
        "v_subrev_u32 v27, 32, v27\n"
        "s_sub_u32 s97, s97, 1\n"
        "s_cmp_lg_u32 s97, 0\n"
        "s_cbranch_scc1 LRLOOP_%=\n"
        "s_mul_i32 s97, s53, 1052672\n"
        "s_lshl_b32 s86, s43, 15\n"
        "s_add_u32 s97, s97, s86\n"
        "s_lshl_b32 s86, s40, 10\n"
        "s_add_u32 s97, s97, s86\n"
        "s_lshl_b32 s86, s96, 6\n"
        "s_add_u32 s97, s97, s86\n"
        "s_add_u32 s97, s97, 0x2200000\n"
        "s_add_u32 s84, s38, s97\n"
        "s_addc_u32 s85, s39, 0\n"
        "v_rcp_f32 v26, v60\n"
        "s_nop 0\n"
        "v_mul_f32 v112, v112, v26\n"
        "v_mul_f32 v113, v113, v26\n"
        "v_mul_f32 v114, v114, v26\n"
        "v_mul_f32 v115, v115, v26\n"
        "v_mul_f32 v116, v116, v26\n"
        "v_mul_f32 v117, v117, v26\n"
        "v_mul_f32 v118, v118, v26\n"
        "v_mul_f32 v119, v119, v26\n"
        "v_cvt_pk_bf16_f32 v112, v112, v113\n"
        "v_cvt_pk_bf16_f32 v113, v114, v115\n"
        "v_cvt_pk_bf16_f32 v114, v116, v117\n"
        "v_cvt_pk_bf16_f32 v115, v118, v119\n"
        "global_store_dwordx4 v254, v[112:115], s[84:85] sc1\n"
        "s_add_u32 s84, s84, 0x2000\n"
        "s_addc_u32 s85, s85, 0\n"
        "v_mul_f32 v120, v120, v26\n"
        "v_mul_f32 v121, v121, v26\n"
        "v_mul_f32 v122, v122, v26\n"
        "v_mul_f32 v123, v123, v26\n"
        "v_mul_f32 v124, v124, v26\n"
        "v_mul_f32 v125, v125, v26\n"
        "v_mul_f32 v126, v126, v26\n"
        "v_mul_f32 v127, v127, v26\n"
        "v_cvt_pk_bf16_f32 v120, v120, v121\n"
        "v_cvt_pk_bf16_f32 v121, v122, v123\n"
        "v_cvt_pk_bf16_f32 v122, v124, v125\n"
        "v_cvt_pk_bf16_f32 v123, v126, v127\n"
        "global_store_dwordx4 v254, v[120:123], s[84:85] sc1\n"
        "s_add_u32 s84, s84, 0x2000\n"
        "s_addc_u32 s85, s85, 0\n"
        "v_mul_f32 v144, v144, v26\n"
        "v_mul_f32 v145, v145, v26\n"
        "v_mul_f32 v146, v146, v26\n"
        "v_mul_f32 v147, v147, v26\n"
        "v_mul_f32 v148, v148, v26\n"
        "v_mul_f32 v149, v149, v26\n"
        "v_mul_f32 v150, v150, v26\n"
        "v_mul_f32 v151, v151, v26\n"
        "v_cvt_pk_bf16_f32 v144, v144, v145\n"
        "v_cvt_pk_bf16_f32 v145, v146, v147\n"
        "v_cvt_pk_bf16_f32 v146, v148, v149\n"
        "v_cvt_pk_bf16_f32 v147, v150, v151\n"
        "global_store_dwordx4 v254, v[144:147], s[84:85] sc1\n"
        "s_add_u32 s84, s84, 0x2000\n"
        "s_addc_u32 s85, s85, 0\n"
        "v_mul_f32 v152, v152, v26\n"
        "v_mul_f32 v153, v153, v26\n"
        "v_mul_f32 v154, v154, v26\n"
        "v_mul_f32 v155, v155, v26\n"
        "v_mul_f32 v156, v156, v26\n"
        "v_mul_f32 v157, v157, v26\n"
        "v_mul_f32 v158, v158, v26\n"
        "v_mul_f32 v159, v159, v26\n"
        "v_cvt_pk_bf16_f32 v152, v152, v153\n"
        "v_cvt_pk_bf16_f32 v153, v154, v155\n"
        "v_cvt_pk_bf16_f32 v154, v156, v157\n"
        "v_cvt_pk_bf16_f32 v155, v158, v159\n"
        "global_store_dwordx4 v254, v[152:155], s[84:85] sc1\n"
        "s_add_u32 s84, s84, 0x2000\n"
        "s_addc_u32 s85, s85, 0\n"
        "s_waitcnt vmcnt(0)\n"
        "global_load_dwordx4 v[144:147], v254, s[88:89] offset:0 sc1\n"
        "global_load_dwordx4 v[148:151], v254, s[88:89] offset:1024 sc1\n"
        "global_load_dwordx4 v[152:155], v254, s[88:89] offset:2048 sc1\n"
        "global_load_dwordx4 v[156:159], v254, s[88:89] offset:3072 sc1\n"
        "s_waitcnt vmcnt(0)\n"
        "s_cmp_eq_u32 s52, 0\n"
        "s_cbranch_scc1 LRET0_%=\n"
        "s_cmp_eq_u32 s52, 1\n"
        "s_cbranch_scc1 LRET1_%=\n"
        "s_cmp_eq_u32 s52, 2\n"
        "s_cbranch_scc1 LRET2_%=\n"
        "s_branch LRET3_%=\n"
        "LEND_%=:\n"
        ".p2align 12\n"
        :
        : "s"(__builtin_amdgcn_kernarg_segment_ptr()), "s"(blockIdx.x), "s"(__builtin_amdgcn_readfirstlane(threadIdx.x >> 6))
        : "v0", "v1", "v2", "v3", "v4", "v5", "v6", "v7", "v8", "v9", "v10", "v11", "v12", "v13", "v14", "v15", "v16", "v17", "v18", "v19", "v20", "v21", "v22", "v23", "v24", "v25", "v26", "v27", "v28", "v29", "v30", "v31",
        "v32", "v33", "v34", "v35", "v36", "v37", "v38", "v39", "v40", "v41", "v42", "v43", "v44", "v45", "v46", "v47", "v48", "v49", "v50", "v51", "v52", "v53", "v54", "v55", "v56", "v57", "v58", "v59", "v60", "v61", "v62", "v63",
        "v64", "v65", "v66", "v67", "v68", "v69", "v70", "v71", "v72", "v73", "v74", "v75", "v76", "v77", "v78", "v79", "v80", "v81", "v82", "v83", "v84", "v85", "v86", "v87", "v88", "v89", "v90", "v91", "v92", "v93", "v94", "v95",
        "v96", "v97", "v98", "v99", "v100", "v101", "v102", "v103", "v104", "v105", "v106", "v107", "v108", "v109", "v110", "v111", "v112", "v113", "v114", "v115", "v116", "v117", "v118", "v119", "v120", "v121", "v122", "v123", "v124", "v125", "v126", "v127",
        "v128", "v129", "v130", "v131", "v132", "v133", "v134", "v135", "v136", "v137", "v138", "v139", "v140", "v141", "v142", "v143", "v144", "v145", "v146", "v147", "v148", "v149", "v150", "v151", "v152", "v153", "v154", "v155", "v156", "v157", "v158", "v159",
        "v160", "v161", "v162", "v163", "v164", "v165", "v166", "v167", "v168", "v169", "v170", "v171", "v172", "v173", "v174", "v175", "v176", "v177", "v178", "v179", "v180", "v181", "v182", "v183", "v184", "v185", "v186", "v187", "v188", "v189", "v190", "v191",
        "v192", "v193", "v194", "v195", "v196", "v197", "v198", "v199", "v200", "v201", "v202", "v203", "v204", "v205", "v206", "v207", "v208", "v209", "v210", "v211", "v212", "v213", "v214", "v215", "v216", "v217", "v218", "v219", "v220", "v221", "v222", "v223",
        "v224", "v225", "v226", "v227", "v228", "v229", "v230", "v231", "v232", "v233", "v234", "v235", "v236", "v237", "v238", "v239", "v240", "v241", "v242", "v243", "v244", "v245", "v246", "v247", "v248", "v249", "v250", "v251", "v252", "v253", "v254", "v255",
        "a0", "a1", "a2", "a3", "a4", "a5", "a6", "a7", "a8", "a9", "a10", "a11", "a12", "a13", "a14", "a15", "a16", "a17", "a18", "a19", "a20", "a21", "a22", "a23", "a24", "a25", "a26", "a27", "a28", "a29", "a30", "a31",
        "a32", "a33", "a34", "a35", "a36", "a37", "a38", "a39", "a40", "a41", "a42", "a43", "a44", "a45", "a46", "a47", "a48", "a49", "a50", "a51", "a52", "a53", "a54", "a55", "a56", "a57", "a58", "a59", "a60", "a61", "a62", "a63",
        "a64", "a65", "a66", "a67", "a68", "a69", "a70", "a71", "a72", "a73", "a74", "a75", "a76", "a77", "a78", "a79", "a80", "a81", "a82", "a83", "a84", "a85", "a86", "a87", "a88", "a89", "a90", "a91", "a92", "a93", "a94", "a95",
        "a96", "a97", "a98", "a99", "a100", "a101", "a102", "a103", "a104", "a105", "a106", "a107", "a108", "a109", "a110", "a111", "a112", "a113", "a114", "a115", "a116", "a117", "a118", "a119", "a120", "a121", "a122", "a123", "a124", "a125", "a126", "a127",
        "a128", "a129", "a130", "a131", "a132", "a133", "a134", "a135", "a136", "a137", "a138", "a139", "a140", "a141", "a142", "a143", "a144", "a145", "a146", "a147", "a148", "a149", "a150", "a151", "a152", "a153", "a154", "a155", "a156", "a157", "a158", "a159",
        "a160", "a161", "a162", "a163", "a164", "a165", "a166", "a167", "a168", "a169", "a170", "a171", "a172", "a173", "a174", "a175", "a176", "a177", "a178", "a179", "a180", "a181", "a182", "a183", "a184", "a185", "a186", "a187", "a188", "a189", "a190", "a191",
        "a192", "a193", "a194", "a195", "a196", "a197", "a198", "a199", "a200", "a201", "a202", "a203", "a204", "a205", "a206", "a207", "a208", "a209", "a210", "a211", "a212", "a213", "a214", "a215", "a216", "a217", "a218", "a219", "a220", "a221", "a222", "a223",
        "a224", "a225", "a226", "a227", "a228", "a229", "a230", "a231", "a232", "a233", "a234", "a235", "a236", "a237", "a238", "a239", "a240", "a241", "a242", "a243", "a244", "a245", "a246", "a247", "a248", "a249", "a250", "a251", "a252", "a253", "a254", "a255",
        "s8", "s9", "s10", "s11", "s12", "s13", "s14", "s15", "s16", "s17", "s18", "s19", "s20", "s21", "s22", "s23", "s24", "s25", "s26", "s27", "s28", "s29", "s30", "s31", "s34", "s35", "s36", "s37", "s38", "s39", "s40", "s41",
        "s42", "s43", "s44", "s45", "s46", "s47", "s48", "s49", "s50", "s51", "s52", "s53", "s54", "s55", "s56", "s57", "s58", "s59", "s60", "s61", "s62", "s63", "s64", "s65", "s66", "s67", "s68", "s69", "s70", "s71", "s72", "s73",
        "s74", "s75", "s76", "s77", "s78", "s79", "s80", "s81", "s82", "s83", "s84", "s85", "s86", "s87", "s88", "s89", "s90", "s91", "s92", "s93", "s94", "s95", "s96", "s97", "s98", "s99", "vcc", "scc", "memory");
}

extern "C" void kernel_launch(void* const* d_in, const int* in_sizes, int n_in, void* d_out, int out_size, void* d_ws, size_t ws_size, hipStream_t stream) {
    (void)in_sizes; (void)n_in; (void)out_size;
    constexpr size_t WS_USED = (size_t)85 << 20;
    Params P;
    P.x = d_in[0]; P.ckv = d_in[1]; P.wq = d_in[2]; P.wuk = d_in[3]; P.wuv = d_in[4]; P.wo = d_in[5];
    P.bk = d_in[7]; P.bv = d_in[8]; P.shk = d_in[9]; P.ssk = d_in[10]; P.ssd = d_in[11]; P.gw = d_in[12];
    P.out = d_out; P.ws = (char*)d_ws;
    if (ws_size > WS_USED) P.ws += (ws_size - WS_USED) & ~(size_t)0xfffff;
    constexpr int LDS_MAX = 163840;
    (void)hipFuncSetAttribute((const void*)k_main, hipFuncAttributeMaxDynamicSharedMemorySize, LDS_MAX);
    k_main<<<256, 256, LDS_MAX, stream>>>(P);
}
```
